```python
import torch

def _softmax(x, axis=-1):
    return torch.softmax(x, dim=axis)
def _gelu(x):
    return torch.nn.functional.gelu(x, approximate="tanh")
def _silu(x):
    return torch.nn.functional.silu(x)
def _sigmoid(x):
    return torch.sigmoid(x)
def _layernorm(x, axis=-1, eps=1e-5):
    m = x.mean(dim=axis, keepdim=True)
    v = x.var(dim=axis, keepdim=True, unbiased=False)
    return (x - m) / torch.sqrt(v + eps)
def _scatter_add(tgt, idx, src, axis=0):
    return tgt.clone().index_add_(axis, idx, src)
def _segment_sum(src, seg, n):
    out = torch.zeros((n,) + src.shape[1:], dtype=src.dtype)
    return out.index_add_(0, seg, src)
def _pad(x, pw):
    flat = tuple(v for pair in reversed(pw) for v in pair)
    return torch.nn.functional.pad(x, flat)

kernel_name = 'concat_heads__compressed_kv_cla__sliding_local_dilated__prompt'

def setup_inputs():

    _w = (lambda a: (lambda t: t.to(torch.bfloat16) if t.is_floating_point() else t)(torch.as_tensor(a)))
    def _A_setup():
        B, H, Sq, Sk, D, M = 1, 32, 2048, 2048, 128, 1024
        Dc = 4 * D
        return {
            "x": _w((lambda *s: torch.randn(*s, dtype=torch.bfloat16) * 0.2)(B, Sq, M)),
            "c_kv": _w((lambda *s: torch.randn(*s, dtype=torch.bfloat16) * 1.6)(B, Sk, Dc)),
            "w_q": _w((lambda *s: torch.randn(*s, dtype=torch.bfloat16) * 3.2)(M, H, D) * (M ** -0.5)),
            "w_uk": _w((lambda *s: torch.randn(*s, dtype=torch.bfloat16) * 3.2)(Dc, H, D) * (Dc ** -0.5)),
            "w_uv": _w((lambda *s: torch.randn(*s, dtype=torch.bfloat16) * 1.6)(Dc, H, D) * (Dc ** -0.5)),
            "w_o": _w((lambda *s: torch.randn(*s, dtype=torch.bfloat16) * 1.6)(H, D, M) * ((H * D) ** -0.5)),
        }
    _A_in = _A_setup()

    def _B_setup():
        B, S, D = 1, 2048, 1024
        return {
            "x": _w((lambda *s: torch.randn(*s, dtype=torch.bfloat16) * 0.2)(B, S, D)),
            "wqkv": _w((lambda *s: torch.randn(*s, dtype=torch.bfloat16) * 0.2)(D, 3 * D)),
            "wo": _w((lambda *s: torch.randn(*s, dtype=torch.bfloat16) * 0.05)(D, D)),
        }
    _B_in = _B_setup()

    return {
        "A_x": _A_in["x"],
        "A_c_kv": _A_in["c_kv"],
        "A_w_q": _A_in["w_q"],
        "A_w_uk": _A_in["w_uk"],
        "A_w_uv": _A_in["w_uv"],
        "A_w_o": _A_in["w_o"],
        "B_x": _B_in["x"],
        "B_wqkv": _B_in["wqkv"],
        "B_wo": _B_in["wo"],
    }

def reference(A_x, A_c_kv, A_w_q, A_w_uk, A_w_uv, A_w_o, B_x, B_wqkv, B_wo):
    def _A_ref(x, c_kv, w_q, w_uk, w_uv, w_o):
        B, Sq, M = x.shape[0], x.shape[1], x.shape[2]
        Sk = c_kv.shape[1]
        H, D = w_q.shape[1], w_q.shape[2]
        q = torch.einsum("bqm,mhd->bhqd", x, w_q)
        k = torch.einsum("bkc,chd->bhkd", c_kv, w_uk)
        v = torch.einsum("bkc,chd->bhkd", c_kv, w_uv)
        scores = torch.einsum("bhqd,bhkd->bhqk", q, k) * (D ** -0.5)
        off = Sk - Sq
        qi = torch.arange(Sq)[:, None]
        kj = torch.arange(Sk)[None, :]
        cmask = kj <= (qi + off)
        scores = torch.where(cmask[None, None], scores, -1e9)
        p = _softmax(scores, axis=-1)
        ctx = torch.einsum("bhqk,bhkd->bhqd", p, v)
        return torch.einsum("bhqd,hdm->bqm", ctx, w_o)

    def _B_ref(x, wqkv, wo):
        B, S, D = x.shape
        H = 32; Dh = D // H
        W, dil = 8, 2
        qkv = (lambda x, *d: x.permute(*d))((x @ wqkv).reshape(B, S, 3, H, Dh), 2, 0, 3, 1, 4)
        q, k, v = qkv[0], qkv[1], qkv[2]
        scale = Dh ** -0.5
        pos = torch.arange(S)
        off = torch.arange(W) * dil
        idx = pos[:, None] - off[None, :]
        mask = idx >= 0
        idx_c = torch.where(mask, idx, 0).reshape(-1)
        kg = k[:, :, idx_c, :].reshape(B, H, S, W, Dh)
        vg = v[:, :, idx_c, :].reshape(B, H, S, W, Dh)
        logits = torch.einsum("bhsd,bhswd->bhsw", q, kg) * scale
        logits = torch.where(mask[None, None, :, :], logits, -1e9)
        attn = _softmax(logits, axis=-1)
        o = torch.einsum("bhsw,bhswd->bhsd", attn, vg)
        return (lambda x, *d: x.permute(*d))(o, 0, 2, 1, 3).reshape(B, S, D) @ wo

    a = _A_ref(A_x, A_c_kv, A_w_q, A_w_uk, A_w_uv, A_w_o)
    b = _B_ref(B_x, B_wqkv, B_wo)
    return torch.concatenate([a, b], axis=-1)
```

```cpp
#include <hip/hip_runtime.h>
#include <stdint.h>
#define LAS __attribute__((address_space(3)))
constexpr size_t LAY_TS = 17408, LAY_HS = 557824, LAY_CRS = 266240, LAY_ORS = 69632, WS_K = 0, WS_V = 17850368, WS_CTX = 35700736, WS_OB = 52740096, WS_FL = 65585152, WS_END = 65634304;
#define MK_ASM_BODY \
"s_load_dwordx16 s[8:23], %0, 0x0\n" \
"s_load_dwordx4 s[24:27], %0, 0x40\n" \
"s_load_dwordx2 s[28:29], %0, 0x50\n" \
"v_readfirstlane_b32 s61, %2\n" \
"s_lshr_b32 s61, s61, 6\n" \
"v_mbcnt_lo_u32_b32 v46, -1, 0\n" \
"v_mbcnt_hi_u32_b32 v46, -1, v46\n" \
"v_lshlrev_b32 v234, 4, v46\n" \
"s_getpc_b64 s[64:65]\n" \
"Lpc_%=:\n" \
"s_lshr_b32 s70, %1, 3\n" \
"s_lshl_b32 s70, s70, 2\n" \
"s_add_u32 s70, s70, s61\n" \
"s_and_b32 s71, s70, 31\n" \
"s_lshl_b32 s71, s71, 12\n" \
"v_lshl_add_u32 v237, v234, 2, s71\n" \
"s_mov_b32 s71, Lcode_end_%=-Lpc_%=-4\n" \
"v_min_u32 v237, s71, v237\n" \
"global_load_dword v238, v237, s[64:65]\n" \
"s_and_b32 s62, %1, 7\n" \
"s_lshl_b32 s62, s62, 2\n" \
"s_lshr_b32 s70, %1, 6\n" \
"s_add_u32 s62, s62, s70\n" \
"s_bfe_u32 s63, %1, 0x30003\n" \
"s_lshl_b32 s74, s61, 13\n" \
"s_add_u32 s74, s74, %3\n" \
"v_bfe_u32 v47, v46, 4, 1\n" \
"v_bfe_u32 v48, v46, 2, 2\n" \
"v_lshl_add_u32 v48, v47, 3, v48\n" \
"s_lshl_b32 s71, s61, 4\n" \
"v_add_u32 v48, s71, v48\n" \
"v_lshlrev_b32 v48, 13, v48\n" \
"v_lshlrev_b32 v47, 1, v47\n" \
"v_and_b32 v49, 3, v46\n" \
"v_xor_b32 v47, v47, v49\n" \
"v_lshl_add_u32 v48, v47, 4, v48\n" \
"v_lshrrev_b32 v58, 5, v46\n" \
"v_lshl_add_u32 v52, v58, 6, v48\n" \
"v_add_u32 v36, 4096, v52\n" \
"v_add_u32 v37, 3072, v52\n" \
"v_add_u32 v38, 2176, v52\n" \
"v_add_u32 v39, 1152, v52\n" \
"s_lshl_b32 s83, s62, 8\n" \
"s_add_u32 s84, s83, 32768\n" \
"v_lshrrev_b32 v53, 3, v46\n" \
"v_and_b32 v54, 7, v46\n" \
"v_xor_b32 v54, v54, v53\n" \
"v_lshlrev_b32 v54, 4, v54\n" \
"v_lshl_add_u32 v32, v53, 10, v54\n" \
"v_add_u32 v33, 7168, v32\n" \
"v_add_u32 v34, 14336, v32\n" \
"v_add_u32 v35, 21504, v32\n" \
"s_bitcmp1_b32 s61, 0\n" \
"s_cselect_b32 s70, 0, 15\n" \
"s_xor_b32 s81, s63, s70\n" \
"s_lshl_b32 s81, s81, 17\n" \
"s_lshr_b32 s70, s61, 1\n" \
"s_lshl_b32 s71, s70, 16\n" \
"s_add_u32 s81, s81, s71\n" \
"s_add_u32 s82, s81, 32768\n" \
"s_mov_b32 s42, 4198400\n" \
"s_mov_b32 s43, 0x20000\n" \
"s_mov_b32 s46, 4198400\n" \
"s_mov_b32 s47, 0x20000\n" \
"s_mov_b32 s38, 2097152\n" \
"s_mov_b32 s39, 0x20000\n" \
"s_waitcnt lgkmcnt(0)\n" \
"s_mov_b64 s[40:41], s[14:15]\n" \
"s_sub_u32 s40, s40, 4096\n" \
"s_subb_u32 s41, s41, 0\n" \
"s_and_b32 s41, s41, 0xffff\n" \
"s_mov_b64 s[44:45], s[16:17]\n" \
"s_sub_u32 s44, s44, 4096\n" \
"s_subb_u32 s45, s45, 0\n" \
"s_and_b32 s45, s45, 0xffff\n" \
"s_mov_b64 s[36:37], s[10:11]\n" \
"s_and_b32 s37, s37, 0xffff\n" \
"s_add_u32 m0, s74, 32768\n" \
"s_nop 0\n" \
"buffer_load_dwordx4 v36, s[40:43], s83 offen offset:0 lds\n" \
"buffer_load_dwordx4 v37, s[44:47], s83 offen offset:1024 lds\n" \
"buffer_load_dwordx4 v38, s[40:43], s83 offen offset:2048 lds\n" \
"buffer_load_dwordx4 v39, s[44:47], s83 offen offset:3072 lds\n" \
"s_xor_b32 s83, s83, 0x80000\n" \
"s_add_u32 m0, s74, 36864\n" \
"s_nop 0\n" \
"buffer_load_dwordx4 v36, s[40:43], s84 offen offset:0 lds\n" \
"buffer_load_dwordx4 v37, s[44:47], s84 offen offset:1024 lds\n" \
"buffer_load_dwordx4 v38, s[40:43], s84 offen offset:2048 lds\n" \
"buffer_load_dwordx4 v39, s[44:47], s84 offen offset:3072 lds\n" \
"s_xor_b32 s84, s84, 0x80000\n" \
"s_add_u32 m0, s74, 0\n" \
"s_nop 0\n" \
"buffer_load_dwordx4 v32, s[36:39], s81 offen offset:0 lds\n" \
"buffer_load_dwordx4 v33, s[36:39], s81 offen offset:1024 lds\n" \
"buffer_load_dwordx4 v34, s[36:39], s81 offen offset:2048 lds\n" \
"buffer_load_dwordx4 v35, s[36:39], s81 offen offset:3072 lds\n" \
"s_xor_b32 s81, s81, 0x80\n" \
"s_add_u32 m0, s74, 4096\n" \
"s_nop 0\n" \
"buffer_load_dwordx4 v32, s[36:39], s82 offen offset:0 lds\n" \
"buffer_load_dwordx4 v33, s[36:39], s82 offen offset:1024 lds\n" \
"buffer_load_dwordx4 v34, s[36:39], s82 offen offset:2048 lds\n" \
"buffer_load_dwordx4 v35, s[36:39], s82 offen offset:3072 lds\n" \
"s_xor_b32 s82, s82, 0x80\n" \
"v_lshrrev_b32 v55, 4, v46\n" \
"v_and_b32 v47, 15, v46\n" \
"v_and_b32 v48, 7, v46\n" \
"v_xor_b32 v48, v48, v55\n" \
"v_lshlrev_b32 v47, 7, v47\n" \
"s_lshl_b32 s70, s70, 14\n" \
"s_add_u32 s70, s70, %3\n" \
"v_lshl_add_u32 v56, v48, 4, v47\n" \
"v_add_u32 v40, s70, v56\n" \
"v_xor_b32 v41, 64, v40\n" \
"v_lshlrev_b32 v47, 3, v49\n" \
"v_bfe_u32 v48, v46, 2, 2\n" \
"v_lshl_add_u32 v47, v48, 6, v47\n" \
"v_bfe_u32 v48, v46, 4, 1\n" \
"v_lshl_add_u32 v47, v48, 8, v47\n" \
"v_lshl_add_u32 v57, v48, 5, v47\n" \
"s_and_b32 s70, s61, 1\n" \
"s_lshl_b32 s70, s70, 11\n" \
"s_add_u32 s70, s70, %3\n" \
"s_add_u32 s70, s70, 32768\n" \
"v_lshl_add_u32 v50, v58, 13, v57\n" \
"v_add_u32 v42, s70, v50\n" \
"v_xor_b32 v43, 32, v42\n" \
"s_mov_b64 s[56:57], s[28:29]\n" \
"s_and_b32 s57, s57, 0xffff\n" \
"s_mov_b32 s58, 65634304\n" \
"s_mov_b32 s59, 0x20000\n" \
"v_mov_b32 v241, 0\n" \
"s_lshl_b32 s66, %1, 2\n" \
"s_add_u32 s66, s66, s61\n" \
"s_lshl_b32 s66, s66, 10\n" \
"s_lshr_b32 s67, %1, 3\n" \
"s_lshl_b32 s67, s67, 2\n" \
"s_add_u32 s67, s67, s61\n" \
"s_lshl_b32 s67, s67, 13\n" \
"s_and_b32 s64, %1, 7\n" \
"s_lshl_b32 s64, s64, 10\n" \
"s_add_u32 s67, s67, s64\n" \
"s_mov_b64 exec, 0\n" \
"s_cmp_eq_u32 s61, 0\n" \
"s_cbranch_scc1 Lst0x0_%=\n" \
"s_cmp_eq_u32 s61, 1\n" \
"s_cbranch_scc1 Lst0x1_%=\n" \
"s_cmp_eq_u32 s61, 2\n" \
"s_cbranch_scc1 Lst0x2_%=\n" \
"s_branch Lst0x3_%=\n" \
"Lcb0_%=:\n" \
"s_mov_b64 exec, -1\n" \
"s_add_u32 m0, s74, 131072\n" \
"s_nop 0\n" \
"buffer_load_dwordx4 v36, s[40:43], s83 offen offset:0 lds\n" \
"buffer_load_dwordx4 v37, s[44:47], s83 offen offset:1024 lds\n" \
"buffer_load_dwordx4 v38, s[40:43], s83 offen offset:2048 lds\n" \
"buffer_load_dwordx4 v39, s[44:47], s83 offen offset:3072 lds\n" \
"s_xor_b32 s83, s83, 0x180000\n" \
"s_add_u32 m0, s74, 135168\n" \
"s_nop 0\n" \
"buffer_load_dwordx4 v36, s[40:43], s84 offen offset:0 lds\n" \
"buffer_load_dwordx4 v37, s[44:47], s84 offen offset:1024 lds\n" \
"buffer_load_dwordx4 v38, s[40:43], s84 offen offset:2048 lds\n" \
"buffer_load_dwordx4 v39, s[44:47], s84 offen offset:3072 lds\n" \
"s_xor_b32 s84, s84, 0x180000\n" \
"s_mov_b32 s93, 65593344\n" \
"buffer_load_dword v240, v241, s[56:59], s93 offen sc1\n" \
"s_mov_b64 s[64:65], s[10:11]\n" \
"s_add_u32 s68, s66, 0x0\n" \
"s_add_u32 s64, s64, s68\n" \
"s_addc_u32 s65, s65, 0\n" \
"global_load_dword v238, v234, s[64:65]\n" \
"s_mov_b64 s[64:65], s[10:11]\n" \
"s_add_u32 s68, s66, 0x100000\n" \
"s_add_u32 s64, s64, s68\n" \
"s_addc_u32 s65, s65, 0\n" \
"global_load_dword v238, v234, s[64:65]\n" \
"s_mov_b64 s[64:65], s[14:15]\n" \
"s_add_u32 s68, s67, 0x100000\n" \
"s_add_u32 s64, s64, s68\n" \
"s_addc_u32 s65, s65, 0\n" \
"global_load_dword v238, v234, s[64:65]\n" \
"s_mov_b64 s[64:65], s[16:17]\n" \
"s_add_u32 s68, s67, 0x100000\n" \
"s_add_u32 s64, s64, s68\n" \
"s_addc_u32 s65, s65, 0\n" \
"global_load_dword v238, v234, s[64:65]\n" \
"s_waitcnt vmcnt(13)\n" \
"s_barrier\n" \
"ds_read_b64_tr_b16 v[128:129], v42 offset:0\n" \
"ds_read_b64_tr_b16 v[130:131], v42 offset:4096\n" \
"ds_read_b64_tr_b16 v[132:133], v42 offset:16384\n" \
"ds_read_b64_tr_b16 v[134:135], v42 offset:20480\n" \
"ds_read_b128 v[64:67], v40 offset:0\n" \
"ds_read_b128 v[68:71], v41 offset:0\n" \
"ds_read_b64_tr_b16 v[136:137], v43 offset:0\n" \
"ds_read_b64_tr_b16 v[138:139], v43 offset:4096\n" \
"ds_read_b64_tr_b16 v[140:141], v43 offset:16384\n" \
"ds_read_b64_tr_b16 v[142:143], v43 offset:20480\n" \
"ds_read_b64_tr_b16 v[144:145], v42 offset:512\n" \
"ds_read_b64_tr_b16 v[146:147], v42 offset:4608\n" \
"ds_read_b64_tr_b16 v[148:149], v42 offset:16896\n" \
"ds_read_b64_tr_b16 v[150:151], v42 offset:20992\n" \
"ds_read_b64_tr_b16 v[152:153], v43 offset:512\n" \
"ds_read_b64_tr_b16 v[154:155], v43 offset:4608\n" \
"ds_read_b64_tr_b16 v[156:157], v43 offset:16896\n" \
"ds_read_b64_tr_b16 v[158:159], v43 offset:20992\n" \
"ds_read_b128 v[72:75], v40 offset:2048\n" \
"ds_read_b128 v[76:79], v41 offset:2048\n" \
"ds_read_b128 v[80:83], v40 offset:4096\n" \
"ds_read_b128 v[84:87], v41 offset:4096\n" \
"ds_read_b128 v[88:91], v40 offset:6144\n" \
"ds_read_b128 v[92:95], v41 offset:6144\n" \
"ds_read_b128 v[96:99], v40 offset:8192\n" \
"ds_read_b128 v[100:103], v41 offset:8192\n" \
"ds_read_b128 v[104:107], v40 offset:10240\n" \
"ds_read_b128 v[108:111], v41 offset:10240\n" \
"ds_read_b128 v[112:115], v40 offset:12288\n" \
"ds_read_b128 v[116:119], v41 offset:12288\n" \
"s_waitcnt lgkmcnt(15)\n" \
"v_mfma_f32_16x16x32_bf16 a[0:3], v[128:131], v[64:67], 0\n" \
"s_add_u32 m0, s74, 65536\n" \
"ds_read_b128 v[120:123], v40 offset:14336\n" \
"v_mfma_f32_16x16x32_bf16 a[0:3], v[132:135], v[68:71], a[0:3]\n" \
"buffer_load_dwordx4 v32, s[36:39], s81 offen offset:0 lds\n" \
"v_mfma_f32_16x16x32_bf16 a[4:7], v[136:139], v[64:67], 0\n" \
"ds_read_b128 v[124:127], v41 offset:14336\n" \
"v_mfma_f32_16x16x32_bf16 a[4:7], v[140:143], v[68:71], a[4:7]\n" \
"v_mfma_f32_16x16x32_bf16 a[8:11], v[144:147], v[64:67], 0\n" \
"s_waitcnt lgkmcnt(2)\n" \
"ds_read_b64_tr_b16 v[160:161], v42 offset:1024\n" \
"v_mfma_f32_16x16x32_bf16 a[8:11], v[148:151], v[68:71], a[8:11]\n" \
"buffer_load_dwordx4 v33, s[36:39], s81 offen offset:1024 lds\n" \
"v_mfma_f32_16x16x32_bf16 a[12:15], v[152:155], v[64:67], 0\n" \
"ds_read_b64_tr_b16 v[162:163], v42 offset:5120\n" \
"v_mfma_f32_16x16x32_bf16 a[12:15], v[156:159], v[68:71], a[12:15]\n" \
"v_mfma_f32_16x16x32_bf16 a[44:47], v[152:155], v[72:75], 0\n" \
"ds_read_b64_tr_b16 v[164:165], v42 offset:17408\n" \
"v_mfma_f32_16x16x32_bf16 a[44:47], v[156:159], v[76:79], a[44:47]\n" \
"buffer_load_dwordx4 v34, s[36:39], s81 offen offset:2048 lds\n" \
"v_mfma_f32_16x16x32_bf16 a[40:43], v[144:147], v[72:75], 0\n" \
"ds_read_b64_tr_b16 v[166:167], v42 offset:21504\n" \
"v_mfma_f32_16x16x32_bf16 a[40:43], v[148:151], v[76:79], a[40:43]\n" \
"v_mfma_f32_16x16x32_bf16 a[36:39], v[136:139], v[72:75], 0\n" \
"ds_read_b64_tr_b16 v[168:169], v43 offset:1024\n" \
"v_mfma_f32_16x16x32_bf16 a[36:39], v[140:143], v[76:79], a[36:39]\n" \
"buffer_load_dwordx4 v35, s[36:39], s81 offen offset:3072 lds\n" \
"v_mfma_f32_16x16x32_bf16 a[32:35], v[128:131], v[72:75], 0\n" \
"ds_read_b64_tr_b16 v[170:171], v43 offset:5120\n" \
"v_mfma_f32_16x16x32_bf16 a[32:35], v[132:135], v[76:79], a[32:35]\n" \
"s_xor_b32 s81, s81, 0x180\n" \
"v_mfma_f32_16x16x32_bf16 a[64:67], v[128:131], v[80:83], 0\n" \
"s_add_u32 m0, s74, 69632\n" \
"ds_read_b64_tr_b16 v[172:173], v43 offset:17408\n" \
"v_mfma_f32_16x16x32_bf16 a[64:67], v[132:135], v[84:87], a[64:67]\n" \
"buffer_load_dwordx4 v32, s[36:39], s82 offen offset:0 lds\n" \
"v_mfma_f32_16x16x32_bf16 a[68:71], v[136:139], v[80:83], 0\n" \
"ds_read_b64_tr_b16 v[174:175], v43 offset:21504\n" \
"v_mfma_f32_16x16x32_bf16 a[68:71], v[140:143], v[84:87], a[68:71]\n" \
"v_mfma_f32_16x16x32_bf16 a[72:75], v[144:147], v[80:83], 0\n" \
"ds_read_b64_tr_b16 v[176:177], v42 offset:1536\n" \
"v_mfma_f32_16x16x32_bf16 a[72:75], v[148:151], v[84:87], a[72:75]\n" \
"buffer_load_dwordx4 v33, s[36:39], s82 offen offset:1024 lds\n" \
"v_mfma_f32_16x16x32_bf16 a[76:79], v[152:155], v[80:83], 0\n" \
"ds_read_b64_tr_b16 v[178:179], v42 offset:5632\n" \
"v_mfma_f32_16x16x32_bf16 a[76:79], v[156:159], v[84:87], a[76:79]\n" \
"v_mfma_f32_16x16x32_bf16 a[108:111], v[152:155], v[88:91], 0\n" \
"ds_read_b64_tr_b16 v[180:181], v42 offset:17920\n" \
"v_mfma_f32_16x16x32_bf16 a[108:111], v[156:159], v[92:95], a[108:111]\n" \
"buffer_load_dwordx4 v34, s[36:39], s82 offen offset:2048 lds\n" \
"v_mfma_f32_16x16x32_bf16 a[104:107], v[144:147], v[88:91], 0\n" \
"ds_read_b64_tr_b16 v[182:183], v42 offset:22016\n" \
"v_mfma_f32_16x16x32_bf16 a[104:107], v[148:151], v[92:95], a[104:107]\n" \
"v_mfma_f32_16x16x32_bf16 a[100:103], v[136:139], v[88:91], 0\n" \
"ds_read_b64_tr_b16 v[184:185], v43 offset:1536\n" \
"v_mfma_f32_16x16x32_bf16 a[100:103], v[140:143], v[92:95], a[100:103]\n" \
"buffer_load_dwordx4 v35, s[36:39], s82 offen offset:3072 lds\n" \
"v_mfma_f32_16x16x32_bf16 a[96:99], v[128:131], v[88:91], 0\n" \
"ds_read_b64_tr_b16 v[186:187], v43 offset:5632\n" \
"v_mfma_f32_16x16x32_bf16 a[96:99], v[132:135], v[92:95], a[96:99]\n" \
"v_mfma_f32_16x16x32_bf16 a[128:131], v[128:131], v[96:99], 0\n" \
"s_xor_b32 s82, s82, 0x180\n" \
"ds_read_b64_tr_b16 v[188:189], v43 offset:17920\n" \
"v_mfma_f32_16x16x32_bf16 a[128:131], v[132:135], v[100:103], a[128:131]\n" \
"v_mfma_f32_16x16x32_bf16 a[132:135], v[136:139], v[96:99], 0\n" \
"ds_read_b64_tr_b16 v[190:191], v43 offset:22016\n" \
"v_mfma_f32_16x16x32_bf16 a[132:135], v[140:143], v[100:103], a[132:135]\n" \
"v_mfma_f32_16x16x32_bf16 a[136:139], v[144:147], v[96:99], 0\n" \
"v_mfma_f32_16x16x32_bf16 a[136:139], v[148:151], v[100:103], a[136:139]\n" \
"v_mfma_f32_16x16x32_bf16 a[140:143], v[152:155], v[96:99], 0\n" \
"v_mfma_f32_16x16x32_bf16 a[140:143], v[156:159], v[100:103], a[140:143]\n" \
"v_mfma_f32_16x16x32_bf16 a[172:175], v[152:155], v[104:107], 0\n" \
"v_mfma_f32_16x16x32_bf16 a[172:175], v[156:159], v[108:111], a[172:175]\n" \
"v_mfma_f32_16x16x32_bf16 a[168:171], v[144:147], v[104:107], 0\n" \
"v_mfma_f32_16x16x32_bf16 a[168:171], v[148:151], v[108:111], a[168:171]\n" \
"v_mfma_f32_16x16x32_bf16 a[164:167], v[136:139], v[104:107], 0\n" \
"v_mfma_f32_16x16x32_bf16 a[164:167], v[140:143], v[108:111], a[164:167]\n" \
"v_mfma_f32_16x16x32_bf16 a[160:163], v[128:131], v[104:107], 0\n" \
"v_mfma_f32_16x16x32_bf16 a[160:163], v[132:135], v[108:111], a[160:163]\n" \
"v_mfma_f32_16x16x32_bf16 a[192:195], v[128:131], v[112:115], 0\n" \
"s_add_u32 m0, s74, 98304\n" \
"v_mfma_f32_16x16x32_bf16 a[192:195], v[132:135], v[116:119], a[192:195]\n" \
"buffer_load_dwordx4 v36, s[40:43], s83 offen offset:0 lds\n" \
"v_mfma_f32_16x16x32_bf16 a[196:199], v[136:139], v[112:115], 0\n" \
"v_add_u32 v40, 0x10000, v40\n" \
"v_mfma_f32_16x16x32_bf16 a[196:199], v[140:143], v[116:119], a[196:199]\n" \
"v_mfma_f32_16x16x32_bf16 a[200:203], v[144:147], v[112:115], 0\n" \
"v_add_u32 v41, 0x10000, v41\n" \
"v_mfma_f32_16x16x32_bf16 a[200:203], v[148:151], v[116:119], a[200:203]\n" \
"buffer_load_dwordx4 v37, s[44:47], s83 offen offset:1024 lds\n" \
"v_mfma_f32_16x16x32_bf16 a[204:207], v[152:155], v[112:115], 0\n" \
"v_add_u32 v42, 0x18000, v42\n" \
"v_mfma_f32_16x16x32_bf16 a[204:207], v[156:159], v[116:119], a[204:207]\n" \
"s_waitcnt lgkmcnt(0)\n" \
"v_mfma_f32_16x16x32_bf16 a[236:239], v[152:155], v[120:123], 0\n" \
"v_add_u32 v43, 0x18000, v43\n" \
"v_mfma_f32_16x16x32_bf16 a[236:239], v[156:159], v[124:127], a[236:239]\n" \
"buffer_load_dwordx4 v38, s[40:43], s83 offen offset:2048 lds\n" \
"v_mfma_f32_16x16x32_bf16 a[232:235], v[144:147], v[120:123], 0\n" \
"v_mfma_f32_16x16x32_bf16 a[232:235], v[148:151], v[124:127], a[232:235]\n" \
"v_mfma_f32_16x16x32_bf16 a[228:231], v[136:139], v[120:123], 0\n" \
"v_mfma_f32_16x16x32_bf16 a[228:231], v[140:143], v[124:127], a[228:231]\n" \
"buffer_load_dwordx4 v39, s[44:47], s83 offen offset:3072 lds\n" \
"v_mfma_f32_16x16x32_bf16 a[224:227], v[128:131], v[120:123], 0\n" \
"v_mfma_f32_16x16x32_bf16 a[224:227], v[132:135], v[124:127], a[224:227]\n" \
"s_xor_b32 s83, s83, 0x80000\n" \
"s_waitcnt lgkmcnt(0)\n" \
"s_barrier\n" \
"v_mfma_f32_16x16x32_bf16 a[16:19], v[64:67], v[160:163], 0\n" \
"s_add_u32 m0, s74, 102400\n" \
"v_mfma_f32_16x16x32_bf16 a[16:19], v[68:71], v[164:167], a[16:19]\n" \
"buffer_load_dwordx4 v36, s[40:43], s84 offen offset:0 lds\n" \
"v_mfma_f32_16x16x32_bf16 a[20:23], v[64:67], v[168:171], 0\n" \
"v_mfma_f32_16x16x32_bf16 a[20:23], v[68:71], v[172:175], a[20:23]\n" \
"v_mfma_f32_16x16x32_bf16 a[24:27], v[64:67], v[176:179], 0\n" \
"v_mfma_f32_16x16x32_bf16 a[24:27], v[68:71], v[180:183], a[24:27]\n" \
"buffer_load_dwordx4 v37, s[44:47], s84 offen offset:1024 lds\n" \
"v_mfma_f32_16x16x32_bf16 a[28:31], v[64:67], v[184:187], 0\n" \
"v_mfma_f32_16x16x32_bf16 a[28:31], v[68:71], v[188:191], a[28:31]\n" \
"v_mfma_f32_16x16x32_bf16 a[60:63], v[72:75], v[184:187], 0\n" \
"v_mfma_f32_16x16x32_bf16 a[60:63], v[76:79], v[188:191], a[60:63]\n" \
"buffer_load_dwordx4 v38, s[40:43], s84 offen offset:2048 lds\n" \
"v_mfma_f32_16x16x32_bf16 a[56:59], v[72:75], v[176:179], 0\n" \
"v_mfma_f32_16x16x32_bf16 a[56:59], v[76:79], v[180:183], a[56:59]\n" \
"v_mfma_f32_16x16x32_bf16 a[52:55], v[72:75], v[168:171], 0\n" \
"v_mfma_f32_16x16x32_bf16 a[52:55], v[76:79], v[172:175], a[52:55]\n" \
"buffer_load_dwordx4 v39, s[44:47], s84 offen offset:3072 lds\n" \
"v_mfma_f32_16x16x32_bf16 a[48:51], v[72:75], v[160:163], 0\n" \
"v_mfma_f32_16x16x32_bf16 a[48:51], v[76:79], v[164:167], a[48:51]\n" \
"v_mfma_f32_16x16x32_bf16 a[80:83], v[80:83], v[160:163], 0\n" \
"s_xor_b32 s84, s84, 0x80000\n" \
"v_mfma_f32_16x16x32_bf16 a[80:83], v[84:87], v[164:167], a[80:83]\n" \
"v_mfma_f32_16x16x32_bf16 a[84:87], v[80:83], v[168:171], 0\n" \
"v_mfma_f32_16x16x32_bf16 a[84:87], v[84:87], v[172:175], a[84:87]\n" \
"v_mfma_f32_16x16x32_bf16 a[88:91], v[80:83], v[176:179], 0\n" \
"s_add_u32 m0, s74, 32768\n" \
"v_mfma_f32_16x16x32_bf16 a[88:91], v[84:87], v[180:183], a[88:91]\n" \
"buffer_load_dwordx4 v32, s[36:39], s81 offen offset:0 lds\n" \
"v_mfma_f32_16x16x32_bf16 a[92:95], v[80:83], v[184:187], 0\n" \
"v_mfma_f32_16x16x32_bf16 a[92:95], v[84:87], v[188:191], a[92:95]\n" \
"v_mfma_f32_16x16x32_bf16 a[124:127], v[88:91], v[184:187], 0\n" \
"v_mfma_f32_16x16x32_bf16 a[124:127], v[92:95], v[188:191], a[124:127]\n" \
"buffer_load_dwordx4 v33, s[36:39], s81 offen offset:1024 lds\n" \
"v_mfma_f32_16x16x32_bf16 a[120:123], v[88:91], v[176:179], 0\n" \
"v_mfma_f32_16x16x32_bf16 a[120:123], v[92:95], v[180:183], a[120:123]\n" \
"v_mfma_f32_16x16x32_bf16 a[116:119], v[88:91], v[168:171], 0\n" \
"v_mfma_f32_16x16x32_bf16 a[116:119], v[92:95], v[172:175], a[116:119]\n" \
"buffer_load_dwordx4 v34, s[36:39], s81 offen offset:2048 lds\n" \
"v_mfma_f32_16x16x32_bf16 a[112:115], v[88:91], v[160:163], 0\n" \
"v_mfma_f32_16x16x32_bf16 a[112:115], v[92:95], v[164:167], a[112:115]\n" \
"v_mfma_f32_16x16x32_bf16 a[144:147], v[96:99], v[160:163], 0\n" \
"v_mfma_f32_16x16x32_bf16 a[144:147], v[100:103], v[164:167], a[144:147]\n" \
"buffer_load_dwordx4 v35, s[36:39], s81 offen offset:3072 lds\n" \
"v_mfma_f32_16x16x32_bf16 a[148:151], v[96:99], v[168:171], 0\n" \
"v_mfma_f32_16x16x32_bf16 a[148:151], v[100:103], v[172:175], a[148:151]\n" \
"s_xor_b32 s81, s81, 0x80\n" \
"v_mfma_f32_16x16x32_bf16 a[152:155], v[96:99], v[176:179], 0\n" \
"s_add_u32 m0, s74, 36864\n" \
"v_mfma_f32_16x16x32_bf16 a[152:155], v[100:103], v[180:183], a[152:155]\n" \
"buffer_load_dwordx4 v32, s[36:39], s82 offen offset:0 lds\n" \
"v_mfma_f32_16x16x32_bf16 a[156:159], v[96:99], v[184:187], 0\n" \
"v_mfma_f32_16x16x32_bf16 a[156:159], v[100:103], v[188:191], a[156:159]\n" \
"s_waitcnt vmcnt(13) lgkmcnt(0)\n" \
"s_barrier\n" \
"v_mfma_f32_16x16x32_bf16 a[188:191], v[104:107], v[184:187], 0\n" \
"ds_read_b64_tr_b16 v[128:129], v42 offset:0\n" \
"ds_read_b64_tr_b16 v[130:131], v42 offset:4096\n" \
"v_mfma_f32_16x16x32_bf16 a[188:191], v[108:111], v[188:191], a[188:191]\n" \
"buffer_load_dwordx4 v33, s[36:39], s82 offen offset:1024 lds\n" \
"v_mfma_f32_16x16x32_bf16 a[184:187], v[104:107], v[176:179], 0\n" \
"ds_read_b64_tr_b16 v[132:133], v42 offset:16384\n" \
"ds_read_b64_tr_b16 v[134:135], v42 offset:20480\n" \
"v_mfma_f32_16x16x32_bf16 a[184:187], v[108:111], v[180:183], a[184:187]\n" \
"ds_read_b128 v[64:67], v40 offset:0\n" \
"ds_read_b128 v[68:71], v41 offset:0\n" \
"v_mfma_f32_16x16x32_bf16 a[180:183], v[104:107], v[168:171], 0\n" \
"ds_read_b64_tr_b16 v[136:137], v43 offset:0\n" \
"ds_read_b64_tr_b16 v[138:139], v43 offset:4096\n" \
"v_mfma_f32_16x16x32_bf16 a[180:183], v[108:111], v[172:175], a[180:183]\n" \
"buffer_load_dwordx4 v34, s[36:39], s82 offen offset:2048 lds\n" \
"v_mfma_f32_16x16x32_bf16 a[176:179], v[104:107], v[160:163], 0\n" \
"ds_read_b64_tr_b16 v[140:141], v43 offset:16384\n" \
"ds_read_b64_tr_b16 v[142:143], v43 offset:20480\n" \
"v_mfma_f32_16x16x32_bf16 a[176:179], v[108:111], v[164:167], a[176:179]\n" \
"ds_read_b64_tr_b16 v[144:145], v42 offset:512\n" \
"ds_read_b64_tr_b16 v[146:147], v42 offset:4608\n" \
"v_mfma_f32_16x16x32_bf16 a[208:211], v[112:115], v[160:163], 0\n" \
"ds_read_b64_tr_b16 v[148:149], v42 offset:16896\n" \
"ds_read_b64_tr_b16 v[150:151], v42 offset:20992\n" \
"v_mfma_f32_16x16x32_bf16 a[208:211], v[116:119], v[164:167], a[208:211]\n" \
"buffer_load_dwordx4 v35, s[36:39], s82 offen offset:3072 lds\n" \
"v_mfma_f32_16x16x32_bf16 a[212:215], v[112:115], v[168:171], 0\n" \
"ds_read_b64_tr_b16 v[152:153], v43 offset:512\n" \
"ds_read_b64_tr_b16 v[154:155], v43 offset:4608\n" \
"v_mfma_f32_16x16x32_bf16 a[212:215], v[116:119], v[172:175], a[212:215]\n" \
"ds_read_b64_tr_b16 v[156:157], v43 offset:16896\n" \
"ds_read_b64_tr_b16 v[158:159], v43 offset:20992\n" \
"v_mfma_f32_16x16x32_bf16 a[216:219], v[112:115], v[176:179], 0\n" \
"s_xor_b32 s82, s82, 0x80\n" \
"ds_read_b128 v[72:75], v40 offset:2048\n" \
"ds_read_b128 v[76:79], v41 offset:2048\n" \
"v_mfma_f32_16x16x32_bf16 a[216:219], v[116:119], v[180:183], a[216:219]\n" \
"v_mfma_f32_16x16x32_bf16 a[220:223], v[112:115], v[184:187], 0\n" \
"ds_read_b128 v[80:83], v40 offset:4096\n" \
"ds_read_b128 v[84:87], v41 offset:4096\n" \
"v_mfma_f32_16x16x32_bf16 a[220:223], v[116:119], v[188:191], a[220:223]\n" \
"ds_read_b128 v[88:91], v40 offset:6144\n" \
"ds_read_b128 v[92:95], v41 offset:6144\n" \
"v_mfma_f32_16x16x32_bf16 a[252:255], v[120:123], v[184:187], 0\n" \
"ds_read_b128 v[96:99], v40 offset:8192\n" \
"ds_read_b128 v[100:103], v41 offset:8192\n" \
"v_mfma_f32_16x16x32_bf16 a[252:255], v[124:127], v[188:191], a[252:255]\n" \
"v_mfma_f32_16x16x32_bf16 a[248:251], v[120:123], v[176:179], 0\n" \
"ds_read_b128 v[104:107], v40 offset:10240\n" \
"ds_read_b128 v[108:111], v41 offset:10240\n" \
"v_mfma_f32_16x16x32_bf16 a[248:251], v[124:127], v[180:183], a[248:251]\n" \
"ds_read_b128 v[112:115], v40 offset:12288\n" \
"ds_read_b128 v[116:119], v41 offset:12288\n" \
"v_mfma_f32_16x16x32_bf16 a[244:247], v[120:123], v[168:171], 0\n" \
"v_mfma_f32_16x16x32_bf16 a[244:247], v[124:127], v[172:175], a[244:247]\n" \
"v_mfma_f32_16x16x32_bf16 a[240:243], v[120:123], v[160:163], 0\n" \
"v_mfma_f32_16x16x32_bf16 a[240:243], v[124:127], v[164:167], a[240:243]\n" \
"s_waitcnt lgkmcnt(2)\n" \
"v_mfma_f32_16x16x32_bf16 a[0:3], v[128:131], v[64:67], a[0:3]\n" \
"s_lshl_b32 s79, s61, 12\n" \
"ds_read_b128 v[120:123], v40 offset:14336\n" \
"v_mfma_f32_16x16x32_bf16 a[0:3], v[132:135], v[68:71], a[0:3]\n" \
"s_add_u32 s79, s79, %3\n" \
"v_add_u32 v228, 4096, v52\n" \
"v_add_u32 v229, 3200, v52\n" \
"v_mfma_f32_16x16x32_bf16 a[4:7], v[136:139], v[64:67], a[4:7]\n" \
"s_add_u32 m0, s74, 0\n" \
"ds_read_b128 v[124:127], v41 offset:14336\n" \
"v_mfma_f32_16x16x32_bf16 a[4:7], v[140:143], v[68:71], a[4:7]\n" \
"buffer_load_dwordx4 v36, s[40:43], s83 offen offset:0 lds\n" \
"v_mfma_f32_16x16x32_bf16 a[8:11], v[144:147], v[64:67], a[8:11]\n" \
"s_lshl_b32 s78, s62, 8\n" \
"ds_read_b64_tr_b16 v[160:161], v42 offset:1024\n" \
"v_mfma_f32_16x16x32_bf16 a[8:11], v[148:151], v[68:71], a[8:11]\n" \
"s_add_u32 s89, s78, 30720\n" \
"v_mfma_f32_16x16x32_bf16 a[12:15], v[152:155], v[64:67], a[12:15]\n" \
"s_mov_b64 s[48:49], s[8:9]\n" \
"ds_read_b64_tr_b16 v[162:163], v42 offset:5120\n" \
"v_mfma_f32_16x16x32_bf16 a[12:15], v[156:159], v[68:71], a[12:15]\n" \
"buffer_load_dwordx4 v37, s[44:47], s83 offen offset:1024 lds\n" \
"v_mfma_f32_16x16x32_bf16 a[44:47], v[152:155], v[72:75], a[44:47]\n" \
"s_and_b32 s49, s49, 0xffff\n" \
"ds_read_b64_tr_b16 v[164:165], v42 offset:17408\n" \
"v_mfma_f32_16x16x32_bf16 a[44:47], v[156:159], v[76:79], a[44:47]\n" \
"s_mov_b32 s50, 4194304\n" \
"v_mfma_f32_16x16x32_bf16 a[40:43], v[144:147], v[72:75], a[40:43]\n" \
"s_mov_b32 s51, 0x20000\n" \
"ds_read_b64_tr_b16 v[166:167], v42 offset:21504\n" \
"v_mfma_f32_16x16x32_bf16 a[40:43], v[148:151], v[76:79], a[40:43]\n" \
"buffer_load_dwordx4 v38, s[40:43], s83 offen offset:2048 lds\n" \
"v_mfma_f32_16x16x32_bf16 a[36:39], v[136:139], v[72:75], a[36:39]\n" \
"s_mov_b64 s[52:53], s[12:13]\n" \
"ds_read_b64_tr_b16 v[168:169], v43 offset:1024\n" \
"v_mfma_f32_16x16x32_bf16 a[36:39], v[140:143], v[76:79], a[36:39]\n" \
"s_sub_u32 s52, s52, 4096\n" \
"s_subb_u32 s53, s53, 0\n" \
"v_mfma_f32_16x16x32_bf16 a[32:35], v[128:131], v[72:75], a[32:35]\n" \
"s_and_b32 s53, s53, 0xffff\n" \
"ds_read_b64_tr_b16 v[170:171], v43 offset:5120\n" \
"v_mfma_f32_16x16x32_bf16 a[32:35], v[132:135], v[76:79], a[32:35]\n" \
"buffer_load_dwordx4 v39, s[44:47], s83 offen offset:3072 lds\n" \
"v_mfma_f32_16x16x32_bf16 a[64:67], v[128:131], v[80:83], a[64:67]\n" \
"s_mov_b32 s54, 8392704\n" \
"s_mov_b32 s55, 0x20000\n" \
"ds_read_b64_tr_b16 v[172:173], v43 offset:17408\n" \
"v_mfma_f32_16x16x32_bf16 a[64:67], v[132:135], v[84:87], a[64:67]\n" \
"s_xor_b32 s83, s83, 0x380000\n" \
"v_lshl_add_u32 v224, v53, 11, v54\n" \
"v_add_u32 v225, 15360, v224\n" \
"v_mfma_f32_16x16x32_bf16 a[68:71], v[136:139], v[80:83], a[68:71]\n" \
"s_add_u32 m0, s74, 4096\n" \
"ds_read_b64_tr_b16 v[174:175], v43 offset:21504\n" \
"v_add_u32 v226, 129024, v224\n" \
"v_mfma_f32_16x16x32_bf16 a[68:71], v[140:143], v[84:87], a[68:71]\n" \
"buffer_load_dwordx4 v36, s[40:43], s84 offen offset:0 lds\n" \
"v_mfma_f32_16x16x32_bf16 a[72:75], v[144:147], v[80:83], a[72:75]\n" \
"ds_read_b64_tr_b16 v[176:177], v42 offset:1536\n" \
"v_add_u32 v227, 144384, v224\n" \
"v_mfma_f32_16x16x32_bf16 a[72:75], v[148:151], v[84:87], a[72:75]\n" \
"s_lshl_b32 s70, s61, 15\n" \
"v_mfma_f32_16x16x32_bf16 a[76:79], v[152:155], v[80:83], a[76:79]\n" \
"s_lshl_b32 s75, s63, 18\n" \
"ds_read_b64_tr_b16 v[178:179], v42 offset:5632\n" \
"v_mfma_f32_16x16x32_bf16 a[76:79], v[156:159], v[84:87], a[76:79]\n" \
"buffer_load_dwordx4 v37, s[44:47], s84 offen offset:1024 lds\n" \
"v_mfma_f32_16x16x32_bf16 a[108:111], v[152:155], v[88:91], a[108:111]\n" \
"s_sub_u32 s77, 3932160, s75\n" \
"ds_read_b64_tr_b16 v[180:181], v42 offset:17920\n" \
"v_mfma_f32_16x16x32_bf16 a[108:111], v[156:159], v[92:95], a[108:111]\n" \
"s_lshr_b32 s71, %1, 6\n" \
"v_mfma_f32_16x16x32_bf16 a[104:107], v[144:147], v[88:91], a[104:107]\n" \
"s_xor_b32 s71, s71, s63\n" \
"ds_read_b64_tr_b16 v[182:183], v42 offset:22016\n" \
"v_mfma_f32_16x16x32_bf16 a[104:107], v[148:151], v[92:95], a[104:107]\n" \
"buffer_load_dwordx4 v38, s[40:43], s84 offen offset:2048 lds\n" \
"v_mfma_f32_16x16x32_bf16 a[100:103], v[136:139], v[88:91], a[100:103]\n" \
"s_and_b32 s71, s71, 0\n" \
"ds_read_b64_tr_b16 v[184:185], v43 offset:1536\n" \
"v_mfma_f32_16x16x32_bf16 a[100:103], v[140:143], v[92:95], a[100:103]\n" \
"s_lshl_b32 s71, s71, 7\n" \
"v_mfma_f32_16x16x32_bf16 a[96:99], v[128:131], v[88:91], a[96:99]\n" \
"s_add_u32 s70, s70, s71\n" \
"ds_read_b64_tr_b16 v[186:187], v43 offset:5632\n" \
"v_mfma_f32_16x16x32_bf16 a[96:99], v[132:135], v[92:95], a[96:99]\n" \
"buffer_load_dwordx4 v39, s[44:47], s84 offen offset:3072 lds\n" \
"v_mfma_f32_16x16x32_bf16 a[128:131], v[128:131], v[96:99], a[128:131]\n" \
"s_add_u32 s75, s75, s70\n" \
"ds_read_b64_tr_b16 v[188:189], v43 offset:17920\n" \
"v_mfma_f32_16x16x32_bf16 a[128:131], v[132:135], v[100:103], a[128:131]\n" \
"s_add_u32 s77, s77, s70\n" \
"v_mfma_f32_16x16x32_bf16 a[132:135], v[136:139], v[96:99], a[132:135]\n" \
"s_xor_b32 s84, s84, 0x380000\n" \
"ds_read_b64_tr_b16 v[190:191], v43 offset:22016\n" \
"v_mfma_f32_16x16x32_bf16 a[132:135], v[140:143], v[100:103], a[132:135]\n" \
"s_lshl_b32 s71, s71, 12\n" \
"v_mfma_f32_16x16x32_bf16 a[136:139], v[144:147], v[96:99], a[136:139]\n" \
"s_add_u32 s78, s78, s71\n" \
"v_mfma_f32_16x16x32_bf16 a[136:139], v[148:151], v[100:103], a[136:139]\n" \
"s_add_u32 s89, s89, s71\n" \
"v_lshlrev_b32 v242, 2, v46\n" \
"v_and_b32 v236, 0xf0, v234\n" \
"v_mfma_f32_16x16x32_bf16 a[140:143], v[152:155], v[96:99], a[140:143]\n" \
"v_lshl_add_u32 v236, v55, 9, v236\n" \
"v_mfma_f32_16x16x32_bf16 a[140:143], v[156:159], v[100:103], a[140:143]\n" \
"s_add_u32 s71, s74, 131072\n" \
"v_add_u32 v230, s71, v56\n" \
"v_xor_b32 v231, 64, v230\n" \
"v_mfma_f32_16x16x32_bf16 a[172:175], v[152:155], v[104:107], a[172:175]\n" \
"v_lshl_add_u32 v50, v58, 12, v57\n" \
"v_add_u32 v232, %3, v50\n" \
"v_mfma_f32_16x16x32_bf16 a[172:175], v[156:159], v[108:111], a[172:175]\n" \
"v_xor_b32 v233, 32, v232\n" \
"v_mfma_f32_16x16x32_bf16 a[168:171], v[144:147], v[104:107], a[168:171]\n" \
"s_mul_i32 s87, s62, 557824\n" \
"v_mfma_f32_16x16x32_bf16 a[168:171], v[148:151], v[108:111], a[168:171]\n" \
"s_lshr_b32 s70, s61, 1\n" \
"v_mfma_f32_16x16x32_bf16 a[164:167], v[136:139], v[104:107], a[164:167]\n" \
"s_mul_i32 s70, s70, 17408\n" \
"v_mfma_f32_16x16x32_bf16 a[164:167], v[140:143], v[108:111], a[164:167]\n" \
"s_add_u32 s87, s87, s70\n" \
"v_mfma_f32_16x16x32_bf16 a[160:163], v[128:131], v[104:107], a[160:163]\n" \
"s_and_b32 s70, s61, 1\n" \
"v_mfma_f32_16x16x32_bf16 a[160:163], v[132:135], v[108:111], a[160:163]\n" \
"s_waitcnt lgkmcnt(0)\n" \
"s_lshl_b32 s70, s70, 12\n" \
"v_mfma_f32_16x16x32_bf16 a[192:195], v[128:131], v[112:115], a[192:195]\n" \
"s_add_u32 s87, s87, s70\n" \
"v_mfma_f32_16x16x32_bf16 a[192:195], v[132:135], v[116:119], a[192:195]\n" \
"s_mul_i32 s70, s63, 34816\n" \
"v_mfma_f32_16x16x32_bf16 a[196:199], v[136:139], v[112:115], a[196:199]\n" \
"s_add_u32 s94, s87, 522240\n" \
"v_add_u32 v40, 0xffff8000, v40\n" \
"v_mfma_f32_16x16x32_bf16 a[196:199], v[140:143], v[116:119], a[196:199]\n" \
"s_sub_u32 s94, s94, s70\n" \
"v_mfma_f32_16x16x32_bf16 a[200:203], v[144:147], v[112:115], a[200:203]\n" \
"s_add_u32 s87, s87, s70\n" \
"v_add_u32 v41, 0xffff8000, v41\n" \
"Lpg584_%=:\n" \
"Lst0x0_%=:\n" \
"s_cbranch_execz Lpg2723_%=\n" \
"v_mfma_f32_16x16x32_bf16 a[200:203], v[148:151], v[116:119], a[200:203]\n" \
"s_lshl_b32 s90, s62, 7\n" \
"v_mfma_f32_16x16x32_bf16 a[204:207], v[152:155], v[112:115], a[204:207]\n" \
"s_add_u32 s95, s90, 65597440\n" \
"v_add_u32 v42, 0xffff8000, v42\n" \
"v_mfma_f32_16x16x32_bf16 a[204:207], v[156:159], v[116:119], a[204:207]\n" \
"s_add_u32 s90, s90, 65585152\n" \
"v_mfma_f32_16x16x32_bf16 a[236:239], v[152:155], v[120:123], a[236:239]\n" \
"s_lshl_b32 s92, 0xff, s63\n" \
"v_add_u32 v43, 0xffff8000, v43\n" \
"v_mfma_f32_16x16x32_bf16 a[236:239], v[156:159], v[124:127], a[236:239]\n" \
"s_and_b32 s92, s92, 0xff\n" \
"v_mfma_f32_16x16x32_bf16 a[232:235], v[144:147], v[120:123], a[232:235]\n" \
"s_mov_b32 s76, 0x3e0293ee\n" \
"v_mfma_f32_16x16x32_bf16 a[232:235], v[148:151], v[124:127], a[232:235]\n" \
"v_mfma_f32_16x16x32_bf16 a[228:231], v[136:139], v[120:123], a[228:231]\n" \
"v_mfma_f32_16x16x32_bf16 a[228:231], v[140:143], v[124:127], a[228:231]\n" \
"v_mfma_f32_16x16x32_bf16 a[224:227], v[128:131], v[120:123], a[224:227]\n" \
"v_mfma_f32_16x16x32_bf16 a[224:227], v[132:135], v[124:127], a[224:227]\n" \
"s_waitcnt vmcnt(8) lgkmcnt(0)\n" \
"s_barrier\n" \
"v_readfirstlane_b32 s60, v240\n" \
"s_add_u32 s60, s60, 0x9e3779b9\n" \
"v_mfma_f32_16x16x32_bf16 a[16:19], v[64:67], v[160:163], a[16:19]\n" \
"s_add_u32 m0, s74, 131072\n" \
"ds_read_b64_tr_b16 v[128:129], v42 offset:0\n" \
"v_mfma_f32_16x16x32_bf16 a[16:19], v[68:71], v[164:167], a[16:19]\n" \
"buffer_load_dwordx4 v32, s[36:39], s81 offen offset:0 lds\n" \
"v_mfma_f32_16x16x32_bf16 a[20:23], v[64:67], v[168:171], a[20:23]\n" \
"ds_read_b64_tr_b16 v[130:131], v42 offset:4096\n" \
"v_mfma_f32_16x16x32_bf16 a[20:23], v[68:71], v[172:175], a[20:23]\n" \
"v_mfma_f32_16x16x32_bf16 a[24:27], v[64:67], v[176:179], a[24:27]\n" \
"ds_read_b64_tr_b16 v[132:133], v42 offset:16384\n" \
"v_mfma_f32_16x16x32_bf16 a[24:27], v[68:71], v[180:183], a[24:27]\n" \
"buffer_load_dwordx4 v33, s[36:39], s81 offen offset:1024 lds\n" \
"v_mfma_f32_16x16x32_bf16 a[28:31], v[64:67], v[184:187], a[28:31]\n" \
"ds_read_b64_tr_b16 v[134:135], v42 offset:20480\n" \
"v_mfma_f32_16x16x32_bf16 a[28:31], v[68:71], v[188:191], a[28:31]\n" \
"v_mfma_f32_16x16x32_bf16 a[60:63], v[72:75], v[184:187], a[60:63]\n" \
"ds_read_b128 v[64:67], v40 offset:0\n" \
"v_mfma_f32_16x16x32_bf16 a[60:63], v[76:79], v[188:191], a[60:63]\n" \
"buffer_load_dwordx4 v34, s[36:39], s81 offen offset:2048 lds\n" \
"v_mfma_f32_16x16x32_bf16 a[56:59], v[72:75], v[176:179], a[56:59]\n" \
"ds_read_b64_tr_b16 v[136:137], v43 offset:0\n" \
"v_mfma_f32_16x16x32_bf16 a[56:59], v[76:79], v[180:183], a[56:59]\n" \
"v_mfma_f32_16x16x32_bf16 a[52:55], v[72:75], v[168:171], a[52:55]\n" \
"ds_read_b128 v[68:71], v41 offset:0\n" \
"v_mfma_f32_16x16x32_bf16 a[52:55], v[76:79], v[172:175], a[52:55]\n" \
"buffer_load_dwordx4 v35, s[36:39], s81 offen offset:3072 lds\n" \
"v_mfma_f32_16x16x32_bf16 a[48:51], v[72:75], v[160:163], a[48:51]\n" \
"ds_read_b64_tr_b16 v[138:139], v43 offset:4096\n" \
"v_mfma_f32_16x16x32_bf16 a[48:51], v[76:79], v[164:167], a[48:51]\n" \
"s_xor_b32 s81, s81, 0x380\n" \
"v_mfma_f32_16x16x32_bf16 a[80:83], v[80:83], v[160:163], a[80:83]\n" \
"s_add_u32 m0, s74, 135168\n" \
"ds_read_b128 v[72:75], v40 offset:2048\n" \
"v_mfma_f32_16x16x32_bf16 a[80:83], v[84:87], v[164:167], a[80:83]\n" \
"buffer_load_dwordx4 v32, s[36:39], s82 offen offset:0 lds\n" \
"v_mfma_f32_16x16x32_bf16 a[84:87], v[80:83], v[168:171], a[84:87]\n" \
"ds_read_b64_tr_b16 v[140:141], v43 offset:16384\n" \
"v_mfma_f32_16x16x32_bf16 a[84:87], v[84:87], v[172:175], a[84:87]\n" \
"v_mfma_f32_16x16x32_bf16 a[88:91], v[80:83], v[176:179], a[88:91]\n" \
"ds_read_b128 v[76:79], v41 offset:2048\n" \
"v_mfma_f32_16x16x32_bf16 a[88:91], v[84:87], v[180:183], a[88:91]\n" \
"buffer_load_dwordx4 v33, s[36:39], s82 offen offset:1024 lds\n" \
"v_mfma_f32_16x16x32_bf16 a[92:95], v[80:83], v[184:187], a[92:95]\n" \
"ds_read_b64_tr_b16 v[142:143], v43 offset:20480\n" \
"v_mfma_f32_16x16x32_bf16 a[92:95], v[84:87], v[188:191], a[92:95]\n" \
"v_mfma_f32_16x16x32_bf16 a[124:127], v[88:91], v[184:187], a[124:127]\n" \
"ds_read_b128 v[80:83], v40 offset:4096\n" \
"v_mfma_f32_16x16x32_bf16 a[124:127], v[92:95], v[188:191], a[124:127]\n" \
"buffer_load_dwordx4 v34, s[36:39], s82 offen offset:2048 lds\n" \
"v_mfma_f32_16x16x32_bf16 a[120:123], v[88:91], v[176:179], a[120:123]\n" \
"ds_read_b64_tr_b16 v[144:145], v42 offset:512\n" \
"v_mfma_f32_16x16x32_bf16 a[120:123], v[92:95], v[180:183], a[120:123]\n" \
"v_mfma_f32_16x16x32_bf16 a[116:119], v[88:91], v[168:171], a[116:119]\n" \
"ds_read_b128 v[84:87], v41 offset:4096\n" \
"v_mfma_f32_16x16x32_bf16 a[116:119], v[92:95], v[172:175], a[116:119]\n" \
"buffer_load_dwordx4 v35, s[36:39], s82 offen offset:3072 lds\n" \
"v_mfma_f32_16x16x32_bf16 a[112:115], v[88:91], v[160:163], a[112:115]\n" \
"s_mov_b64 s[64:65], s[14:15]\n" \
"s_add_u32 s68, s67, 0x200000\n" \
"ds_read_b64_tr_b16 v[146:147], v42 offset:4608\n" \
"v_mfma_f32_16x16x32_bf16 a[112:115], v[92:95], v[164:167], a[112:115]\n" \
"s_add_u32 s64, s64, s68\n" \
"s_addc_u32 s65, s65, 0\n" \
"v_mfma_f32_16x16x32_bf16 a[144:147], v[96:99], v[160:163], a[144:147]\n" \
"s_xor_b32 s82, s82, 0x380\n" \
"ds_read_b128 v[88:91], v40 offset:6144\n" \
"v_mfma_f32_16x16x32_bf16 a[144:147], v[100:103], v[164:167], a[144:147]\n" \
"global_load_dword v238, v234, s[64:65]\n" \
"v_mfma_f32_16x16x32_bf16 a[148:151], v[96:99], v[168:171], a[148:151]\n" \
"s_mov_b64 s[64:65], s[16:17]\n" \
"s_add_u32 s68, s67, 0x200000\n" \
"ds_read_b64_tr_b16 v[148:149], v42 offset:16896\n" \
"v_mfma_f32_16x16x32_bf16 a[148:151], v[100:103], v[172:175], a[148:151]\n" \
"s_add_u32 s64, s64, s68\n" \
"s_addc_u32 s65, s65, 0\n" \
"v_mfma_f32_16x16x32_bf16 a[152:155], v[96:99], v[176:179], a[152:155]\n" \
"ds_read_b128 v[92:95], v41 offset:6144\n" \
"v_mfma_f32_16x16x32_bf16 a[152:155], v[100:103], v[180:183], a[152:155]\n" \
"global_load_dword v238, v234, s[64:65]\n" \
"v_mfma_f32_16x16x32_bf16 a[156:159], v[96:99], v[184:187], a[156:159]\n" \
"ds_read_b64_tr_b16 v[150:151], v42 offset:20992\n" \
"v_mfma_f32_16x16x32_bf16 a[156:159], v[100:103], v[188:191], a[156:159]\n" \
"v_mfma_f32_16x16x32_bf16 a[188:191], v[104:107], v[184:187], a[188:191]\n" \
"ds_read_b128 v[96:99], v40 offset:8192\n" \
"v_mfma_f32_16x16x32_bf16 a[188:191], v[108:111], v[188:191], a[188:191]\n" \
"v_mfma_f32_16x16x32_bf16 a[184:187], v[104:107], v[176:179], a[184:187]\n" \
"ds_read_b64_tr_b16 v[152:153], v43 offset:512\n" \
"v_mfma_f32_16x16x32_bf16 a[184:187], v[108:111], v[180:183], a[184:187]\n" \
"v_mfma_f32_16x16x32_bf16 a[180:183], v[104:107], v[168:171], a[180:183]\n" \
"ds_read_b128 v[100:103], v41 offset:8192\n" \
"v_mfma_f32_16x16x32_bf16 a[180:183], v[108:111], v[172:175], a[180:183]\n" \
"v_mfma_f32_16x16x32_bf16 a[176:179], v[104:107], v[160:163], a[176:179]\n" \
"ds_read_b64_tr_b16 v[154:155], v43 offset:4608\n" \
"v_mfma_f32_16x16x32_bf16 a[176:179], v[108:111], v[164:167], a[176:179]\n" \
"v_mfma_f32_16x16x32_bf16 a[208:211], v[112:115], v[160:163], a[208:211]\n" \
"ds_read_b128 v[104:107], v40 offset:10240\n" \
"v_mfma_f32_16x16x32_bf16 a[208:211], v[116:119], v[164:167], a[208:211]\n" \
"v_mfma_f32_16x16x32_bf16 a[212:215], v[112:115], v[168:171], a[212:215]\n" \
"ds_read_b64_tr_b16 v[156:157], v43 offset:16896\n" \
"v_mfma_f32_16x16x32_bf16 a[212:215], v[116:119], v[172:175], a[212:215]\n" \
"v_mfma_f32_16x16x32_bf16 a[216:219], v[112:115], v[176:179], a[216:219]\n" \
"ds_read_b128 v[108:111], v41 offset:10240\n" \
"v_mfma_f32_16x16x32_bf16 a[216:219], v[116:119], v[180:183], a[216:219]\n" \
"v_mfma_f32_16x16x32_bf16 a[220:223], v[112:115], v[184:187], a[220:223]\n" \
"ds_read_b64_tr_b16 v[158:159], v43 offset:20992\n" \
"v_mfma_f32_16x16x32_bf16 a[220:223], v[116:119], v[188:191], a[220:223]\n" \
"v_mfma_f32_16x16x32_bf16 a[252:255], v[120:123], v[184:187], a[252:255]\n" \
"ds_read_b128 v[112:115], v40 offset:12288\n" \
"v_mfma_f32_16x16x32_bf16 a[252:255], v[124:127], v[188:191], a[252:255]\n" \
"v_mfma_f32_16x16x32_bf16 a[248:251], v[120:123], v[176:179], a[248:251]\n" \
"v_mfma_f32_16x16x32_bf16 a[248:251], v[124:127], v[180:183], a[248:251]\n" \
"v_mfma_f32_16x16x32_bf16 a[244:247], v[120:123], v[168:171], a[244:247]\n" \
"ds_read_b128 v[116:119], v41 offset:12288\n" \
"v_mfma_f32_16x16x32_bf16 a[244:247], v[124:127], v[172:175], a[244:247]\n" \
"v_mfma_f32_16x16x32_bf16 a[240:243], v[120:123], v[160:163], a[240:243]\n" \
"v_mfma_f32_16x16x32_bf16 a[240:243], v[124:127], v[164:167], a[240:243]\n" \
"s_waitcnt lgkmcnt(1)\n" \
"v_mfma_f32_16x16x32_bf16 a[0:3], v[128:131], v[64:67], a[0:3]\n" \
"ds_read_b128 v[120:123], v40 offset:14336\n" \
"v_mfma_f32_16x16x32_bf16 a[0:3], v[132:135], v[68:71], a[0:3]\n" \
"v_mfma_f32_16x16x32_bf16 a[4:7], v[136:139], v[64:67], a[4:7]\n" \
"s_add_u32 m0, s74, 65536\n" \
"ds_read_b128 v[124:127], v41 offset:14336\n" \
"v_mfma_f32_16x16x32_bf16 a[4:7], v[140:143], v[68:71], a[4:7]\n" \
"buffer_load_dwordx4 v36, s[40:43], s83 offen offset:0 lds\n" \
"v_mfma_f32_16x16x32_bf16 a[8:11], v[144:147], v[64:67], a[8:11]\n" \
"ds_read_b64_tr_b16 v[160:161], v42 offset:1024\n" \
"v_mfma_f32_16x16x32_bf16 a[8:11], v[148:151], v[68:71], a[8:11]\n" \
"v_mfma_f32_16x16x32_bf16 a[12:15], v[152:155], v[64:67], a[12:15]\n" \
"ds_read_b64_tr_b16 v[162:163], v42 offset:5120\n" \
"v_mfma_f32_16x16x32_bf16 a[12:15], v[156:159], v[68:71], a[12:15]\n" \
"buffer_load_dwordx4 v37, s[44:47], s83 offen offset:1024 lds\n" \
"v_mfma_f32_16x16x32_bf16 a[44:47], v[152:155], v[72:75], a[44:47]\n" \
"ds_read_b64_tr_b16 v[164:165], v42 offset:17408\n" \
"v_mfma_f32_16x16x32_bf16 a[44:47], v[156:159], v[76:79], a[44:47]\n" \
"v_mfma_f32_16x16x32_bf16 a[40:43], v[144:147], v[72:75], a[40:43]\n" \
"ds_read_b64_tr_b16 v[166:167], v42 offset:21504\n" \
"v_mfma_f32_16x16x32_bf16 a[40:43], v[148:151], v[76:79], a[40:43]\n" \
"buffer_load_dwordx4 v38, s[40:43], s83 offen offset:2048 lds\n" \
"v_mfma_f32_16x16x32_bf16 a[36:39], v[136:139], v[72:75], a[36:39]\n" \
"ds_read_b64_tr_b16 v[168:169], v43 offset:1024\n" \
"v_mfma_f32_16x16x32_bf16 a[36:39], v[140:143], v[76:79], a[36:39]\n" \
"v_mfma_f32_16x16x32_bf16 a[32:35], v[128:131], v[72:75], a[32:35]\n" \
"ds_read_b64_tr_b16 v[170:171], v43 offset:5120\n" \
"v_mfma_f32_16x16x32_bf16 a[32:35], v[132:135], v[76:79], a[32:35]\n" \
"buffer_load_dwordx4 v39, s[44:47], s83 offen offset:3072 lds\n" \
"v_mfma_f32_16x16x32_bf16 a[64:67], v[128:131], v[80:83], a[64:67]\n" \
"ds_read_b64_tr_b16 v[172:173], v43 offset:17408\n" \
"v_mfma_f32_16x16x32_bf16 a[64:67], v[132:135], v[84:87], a[64:67]\n" \
"s_xor_b32 s83, s83, 0x80000\n" \
"v_mfma_f32_16x16x32_bf16 a[68:71], v[136:139], v[80:83], a[68:71]\n" \
"s_add_u32 m0, s74, 69632\n" \
"ds_read_b64_tr_b16 v[174:175], v43 offset:21504\n" \
"v_mfma_f32_16x16x32_bf16 a[68:71], v[140:143], v[84:87], a[68:71]\n" \
"buffer_load_dwordx4 v36, s[40:43], s84 offen offset:0 lds\n" \
"v_mfma_f32_16x16x32_bf16 a[72:75], v[144:147], v[80:83], a[72:75]\n" \
"ds_read_b64_tr_b16 v[176:177], v42 offset:1536\n" \
"v_mfma_f32_16x16x32_bf16 a[72:75], v[148:151], v[84:87], a[72:75]\n" \
"v_mfma_f32_16x16x32_bf16 a[76:79], v[152:155], v[80:83], a[76:79]\n" \
"ds_read_b64_tr_b16 v[178:179], v42 offset:5632\n" \
"v_mfma_f32_16x16x32_bf16 a[76:79], v[156:159], v[84:87], a[76:79]\n" \
"buffer_load_dwordx4 v37, s[44:47], s84 offen offset:1024 lds\n" \
"v_mfma_f32_16x16x32_bf16 a[108:111], v[152:155], v[88:91], a[108:111]\n" \
"ds_read_b64_tr_b16 v[180:181], v42 offset:17920\n" \
"v_mfma_f32_16x16x32_bf16 a[108:111], v[156:159], v[92:95], a[108:111]\n" \
"v_mfma_f32_16x16x32_bf16 a[104:107], v[144:147], v[88:91], a[104:107]\n" \
"ds_read_b64_tr_b16 v[182:183], v42 offset:22016\n" \
"v_mfma_f32_16x16x32_bf16 a[104:107], v[148:151], v[92:95], a[104:107]\n" \
"buffer_load_dwordx4 v38, s[40:43], s84 offen offset:2048 lds\n" \
"v_mfma_f32_16x16x32_bf16 a[100:103], v[136:139], v[88:91], a[100:103]\n" \
"ds_read_b64_tr_b16 v[184:185], v43 offset:1536\n" \
"v_mfma_f32_16x16x32_bf16 a[100:103], v[140:143], v[92:95], a[100:103]\n" \
"v_mfma_f32_16x16x32_bf16 a[96:99], v[128:131], v[88:91], a[96:99]\n" \
"ds_read_b64_tr_b16 v[186:187], v43 offset:5632\n" \
"v_mfma_f32_16x16x32_bf16 a[96:99], v[132:135], v[92:95], a[96:99]\n" \
"buffer_load_dwordx4 v39, s[44:47], s84 offen offset:3072 lds\n" \
"v_mfma_f32_16x16x32_bf16 a[128:131], v[128:131], v[96:99], a[128:131]\n" \
"ds_read_b64_tr_b16 v[188:189], v43 offset:17920\n" \
"v_mfma_f32_16x16x32_bf16 a[128:131], v[132:135], v[100:103], a[128:131]\n" \
"v_mfma_f32_16x16x32_bf16 a[132:135], v[136:139], v[96:99], a[132:135]\n" \
"s_xor_b32 s84, s84, 0x80000\n" \
"ds_read_b64_tr_b16 v[190:191], v43 offset:22016\n" \
"v_mfma_f32_16x16x32_bf16 a[132:135], v[140:143], v[100:103], a[132:135]\n" \
"v_mfma_f32_16x16x32_bf16 a[136:139], v[144:147], v[96:99], a[136:139]\n" \
"v_mfma_f32_16x16x32_bf16 a[136:139], v[148:151], v[100:103], a[136:139]\n" \
"v_mfma_f32_16x16x32_bf16 a[140:143], v[152:155], v[96:99], a[140:143]\n" \
"v_mfma_f32_16x16x32_bf16 a[140:143], v[156:159], v[100:103], a[140:143]\n" \
"v_mfma_f32_16x16x32_bf16 a[172:175], v[152:155], v[104:107], a[172:175]\n" \
"v_mfma_f32_16x16x32_bf16 a[172:175], v[156:159], v[108:111], a[172:175]\n" \
"v_mfma_f32_16x16x32_bf16 a[168:171], v[144:147], v[104:107], a[168:171]\n" \
"v_mfma_f32_16x16x32_bf16 a[168:171], v[148:151], v[108:111], a[168:171]\n" \
"v_mfma_f32_16x16x32_bf16 a[164:167], v[136:139], v[104:107], a[164:167]\n" \
"v_mfma_f32_16x16x32_bf16 a[164:167], v[140:143], v[108:111], a[164:167]\n" \
"v_mfma_f32_16x16x32_bf16 a[160:163], v[128:131], v[104:107], a[160:163]\n" \
"v_mfma_f32_16x16x32_bf16 a[160:163], v[132:135], v[108:111], a[160:163]\n" \
"s_waitcnt lgkmcnt(0)\n" \
"v_mfma_f32_16x16x32_bf16 a[192:195], v[128:131], v[112:115], a[192:195]\n" \
"v_mfma_f32_16x16x32_bf16 a[192:195], v[132:135], v[116:119], a[192:195]\n" \
"v_mfma_f32_16x16x32_bf16 a[196:199], v[136:139], v[112:115], a[196:199]\n" \
"v_add_u32 v40, 0x18000, v40\n" \
"v_mfma_f32_16x16x32_bf16 a[196:199], v[140:143], v[116:119], a[196:199]\n" \
"v_mfma_f32_16x16x32_bf16 a[200:203], v[144:147], v[112:115], a[200:203]\n" \
"v_add_u32 v41, 0x18000, v41\n" \
"v_mfma_f32_16x16x32_bf16 a[200:203], v[148:151], v[116:119], a[200:203]\n" \
"v_mfma_f32_16x16x32_bf16 a[204:207], v[152:155], v[112:115], a[204:207]\n" \
"v_add_u32 v42, 0xfffe8000, v42\n" \
"v_mfma_f32_16x16x32_bf16 a[204:207], v[156:159], v[116:119], a[204:207]\n" \
"v_mfma_f32_16x16x32_bf16 a[236:239], v[152:155], v[120:123], a[236:239]\n" \
"v_add_u32 v43, 0xfffe8000, v43\n" \
"v_mfma_f32_16x16x32_bf16 a[236:239], v[156:159], v[124:127], a[236:239]\n" \
"v_mfma_f32_16x16x32_bf16 a[232:235], v[144:147], v[120:123], a[232:235]\n" \
"v_mfma_f32_16x16x32_bf16 a[232:235], v[148:151], v[124:127], a[232:235]\n" \
"v_mfma_f32_16x16x32_bf16 a[228:231], v[136:139], v[120:123], a[228:231]\n" \
"v_mfma_f32_16x16x32_bf16 a[228:231], v[140:143], v[124:127], a[228:231]\n" \
"v_mfma_f32_16x16x32_bf16 a[224:227], v[128:131], v[120:123], a[224:227]\n" \
"v_mfma_f32_16x16x32_bf16 a[224:227], v[132:135], v[124:127], a[224:227]\n" \
"s_waitcnt vmcnt(10) lgkmcnt(0)\n" \
"s_barrier\n" \
"v_mfma_f32_16x16x32_bf16 a[16:19], v[64:67], v[160:163], a[16:19]\n" \
"s_add_u32 m0, s74, 98304\n" \
"ds_read_b64_tr_b16 v[128:129], v42 offset:0\n" \
"v_mfma_f32_16x16x32_bf16 a[16:19], v[68:71], v[164:167], a[16:19]\n" \
"buffer_load_dwordx4 v32, s[36:39], s81 offen offset:0 lds\n" \
"v_mfma_f32_16x16x32_bf16 a[20:23], v[64:67], v[168:171], a[20:23]\n" \
"ds_read_b64_tr_b16 v[130:131], v42 offset:4096\n" \
"v_mfma_f32_16x16x32_bf16 a[20:23], v[68:71], v[172:175], a[20:23]\n" \
"v_mfma_f32_16x16x32_bf16 a[24:27], v[64:67], v[176:179], a[24:27]\n" \
"ds_read_b64_tr_b16 v[132:133], v42 offset:16384\n" \
"v_mfma_f32_16x16x32_bf16 a[24:27], v[68:71], v[180:183], a[24:27]\n" \
"buffer_load_dwordx4 v33, s[36:39], s81 offen offset:1024 lds\n" \
"v_mfma_f32_16x16x32_bf16 a[28:31], v[64:67], v[184:187], a[28:31]\n" \
"ds_read_b64_tr_b16 v[134:135], v42 offset:20480\n" \
"v_mfma_f32_16x16x32_bf16 a[28:31], v[68:71], v[188:191], a[28:31]\n" \
"v_mfma_f32_16x16x32_bf16 a[60:63], v[72:75], v[184:187], a[60:63]\n" \
"ds_read_b128 v[64:67], v40 offset:0\n" \
"v_mfma_f32_16x16x32_bf16 a[60:63], v[76:79], v[188:191], a[60:63]\n" \
"buffer_load_dwordx4 v34, s[36:39], s81 offen offset:2048 lds\n" \
"v_mfma_f32_16x16x32_bf16 a[56:59], v[72:75], v[176:179], a[56:59]\n" \
"ds_read_b64_tr_b16 v[136:137], v43 offset:0\n" \
"v_mfma_f32_16x16x32_bf16 a[56:59], v[76:79], v[180:183], a[56:59]\n" \
"v_mfma_f32_16x16x32_bf16 a[52:55], v[72:75], v[168:171], a[52:55]\n" \
"ds_read_b128 v[68:71], v41 offset:0\n" \
"v_mfma_f32_16x16x32_bf16 a[52:55], v[76:79], v[172:175], a[52:55]\n" \
"buffer_load_dwordx4 v35, s[36:39], s81 offen offset:3072 lds\n" \
"v_mfma_f32_16x16x32_bf16 a[48:51], v[72:75], v[160:163], a[48:51]\n" \
"ds_read_b64_tr_b16 v[138:139], v43 offset:4096\n" \
"v_mfma_f32_16x16x32_bf16 a[48:51], v[76:79], v[164:167], a[48:51]\n" \
"s_xor_b32 s81, s81, 0x80\n" \
"v_mfma_f32_16x16x32_bf16 a[80:83], v[80:83], v[160:163], a[80:83]\n" \
"s_add_u32 m0, s74, 102400\n" \
"ds_read_b128 v[72:75], v40 offset:2048\n" \
"v_mfma_f32_16x16x32_bf16 a[80:83], v[84:87], v[164:167], a[80:83]\n" \
"buffer_load_dwordx4 v32, s[36:39], s82 offen offset:0 lds\n" \
"v_mfma_f32_16x16x32_bf16 a[84:87], v[80:83], v[168:171], a[84:87]\n" \
"ds_read_b64_tr_b16 v[140:141], v43 offset:16384\n" \
"v_mfma_f32_16x16x32_bf16 a[84:87], v[84:87], v[172:175], a[84:87]\n" \
"v_mfma_f32_16x16x32_bf16 a[88:91], v[80:83], v[176:179], a[88:91]\n" \
"ds_read_b128 v[76:79], v41 offset:2048\n" \
"v_mfma_f32_16x16x32_bf16 a[88:91], v[84:87], v[180:183], a[88:91]\n" \
"buffer_load_dwordx4 v33, s[36:39], s82 offen offset:1024 lds\n" \
"v_mfma_f32_16x16x32_bf16 a[92:95], v[80:83], v[184:187], a[92:95]\n" \
"ds_read_b64_tr_b16 v[142:143], v43 offset:20480\n" \
"v_mfma_f32_16x16x32_bf16 a[92:95], v[84:87], v[188:191], a[92:95]\n" \
"v_mfma_f32_16x16x32_bf16 a[124:127], v[88:91], v[184:187], a[124:127]\n" \
"ds_read_b128 v[80:83], v40 offset:4096\n" \
"v_mfma_f32_16x16x32_bf16 a[124:127], v[92:95], v[188:191], a[124:127]\n" \
"buffer_load_dwordx4 v34, s[36:39], s82 offen offset:2048 lds\n" \
"v_mfma_f32_16x16x32_bf16 a[120:123], v[88:91], v[176:179], a[120:123]\n" \
"ds_read_b64_tr_b16 v[144:145], v42 offset:512\n" \
"v_mfma_f32_16x16x32_bf16 a[120:123], v[92:95], v[180:183], a[120:123]\n" \
"v_mfma_f32_16x16x32_bf16 a[116:119], v[88:91], v[168:171], a[116:119]\n" \
"ds_read_b128 v[84:87], v41 offset:4096\n" \
"v_mfma_f32_16x16x32_bf16 a[116:119], v[92:95], v[172:175], a[116:119]\n" \
"buffer_load_dwordx4 v35, s[36:39], s82 offen offset:3072 lds\n" \
"v_mfma_f32_16x16x32_bf16 a[112:115], v[88:91], v[160:163], a[112:115]\n" \
"s_mov_b64 s[64:65], s[14:15]\n" \
"s_add_u32 s68, s67, 0x300000\n" \
"ds_read_b64_tr_b16 v[146:147], v42 offset:4608\n" \
"v_mfma_f32_16x16x32_bf16 a[112:115], v[92:95], v[164:167], a[112:115]\n" \
"s_add_u32 s64, s64, s68\n" \
"s_addc_u32 s65, s65, 0\n" \
"v_mfma_f32_16x16x32_bf16 a[144:147], v[96:99], v[160:163], a[144:147]\n" \
"s_xor_b32 s82, s82, 0x80\n" \
"ds_read_b128 v[88:91], v40 offset:6144\n" \
"v_mfma_f32_16x16x32_bf16 a[144:147], v[100:103], v[164:167], a[144:147]\n" \
"global_load_dword v238, v234, s[64:65]\n" \
"v_mfma_f32_16x16x32_bf16 a[148:151], v[96:99], v[168:171], a[148:151]\n" \
"s_mov_b64 s[64:65], s[16:17]\n" \
"s_add_u32 s68, s67, 0x300000\n" \
"ds_read_b64_tr_b16 v[148:149], v42 offset:16896\n" \
"v_mfma_f32_16x16x32_bf16 a[148:151], v[100:103], v[172:175], a[148:151]\n" \
"s_add_u32 s64, s64, s68\n" \
"s_addc_u32 s65, s65, 0\n" \
"v_mfma_f32_16x16x32_bf16 a[152:155], v[96:99], v[176:179], a[152:155]\n" \
"ds_read_b128 v[92:95], v41 offset:6144\n" \
"v_mfma_f32_16x16x32_bf16 a[152:155], v[100:103], v[180:183], a[152:155]\n" \
"global_load_dword v238, v234, s[64:65]\n" \
"v_mfma_f32_16x16x32_bf16 a[156:159], v[96:99], v[184:187], a[156:159]\n" \
"s_mov_b64 s[64:65], s[8:9]\n" \
"s_add_u32 s68, s66, 0x0\n" \
"ds_read_b64_tr_b16 v[150:151], v42 offset:20992\n" \
"v_mfma_f32_16x16x32_bf16 a[156:159], v[100:103], v[188:191], a[156:159]\n" \
"s_add_u32 s64, s64, s68\n" \
"s_addc_u32 s65, s65, 0\n" \
"v_mfma_f32_16x16x32_bf16 a[188:191], v[104:107], v[184:187], a[188:191]\n" \
"ds_read_b128 v[96:99], v40 offset:8192\n" \
"v_mfma_f32_16x16x32_bf16 a[188:191], v[108:111], v[188:191], a[188:191]\n" \
"global_load_dword v238, v234, s[64:65]\n" \
"v_mfma_f32_16x16x32_bf16 a[184:187], v[104:107], v[176:179], a[184:187]\n" \
"s_mov_b64 s[64:65], s[8:9]\n" \
"s_add_u32 s68, s66, 0x100000\n" \
"ds_read_b64_tr_b16 v[152:153], v43 offset:512\n" \
"v_mfma_f32_16x16x32_bf16 a[184:187], v[108:111], v[180:183], a[184:187]\n" \
"s_add_u32 s64, s64, s68\n" \
"s_addc_u32 s65, s65, 0\n" \
"v_mfma_f32_16x16x32_bf16 a[180:183], v[104:107], v[168:171], a[180:183]\n" \
"ds_read_b128 v[100:103], v41 offset:8192\n" \
"v_mfma_f32_16x16x32_bf16 a[180:183], v[108:111], v[172:175], a[180:183]\n" \
"global_load_dword v238, v234, s[64:65]\n" \
"v_mfma_f32_16x16x32_bf16 a[176:179], v[104:107], v[160:163], a[176:179]\n" \
"ds_read_b64_tr_b16 v[154:155], v43 offset:4608\n" \
"v_mfma_f32_16x16x32_bf16 a[176:179], v[108:111], v[164:167], a[176:179]\n" \
"v_mfma_f32_16x16x32_bf16 a[208:211], v[112:115], v[160:163], a[208:211]\n" \
"ds_read_b128 v[104:107], v40 offset:10240\n" \
"v_mfma_f32_16x16x32_bf16 a[208:211], v[116:119], v[164:167], a[208:211]\n" \
"v_mfma_f32_16x16x32_bf16 a[212:215], v[112:115], v[168:171], a[212:215]\n" \
"ds_read_b64_tr_b16 v[156:157], v43 offset:16896\n" \
"v_mfma_f32_16x16x32_bf16 a[212:215], v[116:119], v[172:175], a[212:215]\n" \
"v_mfma_f32_16x16x32_bf16 a[216:219], v[112:115], v[176:179], a[216:219]\n" \
"ds_read_b128 v[108:111], v41 offset:10240\n" \
"v_mfma_f32_16x16x32_bf16 a[216:219], v[116:119], v[180:183], a[216:219]\n" \
"v_mfma_f32_16x16x32_bf16 a[220:223], v[112:115], v[184:187], a[220:223]\n" \
"ds_read_b64_tr_b16 v[158:159], v43 offset:20992\n" \
"v_mfma_f32_16x16x32_bf16 a[220:223], v[116:119], v[188:191], a[220:223]\n" \
"v_mfma_f32_16x16x32_bf16 a[252:255], v[120:123], v[184:187], a[252:255]\n" \
"ds_read_b128 v[112:115], v40 offset:12288\n" \
"v_mfma_f32_16x16x32_bf16 a[252:255], v[124:127], v[188:191], a[252:255]\n" \
"v_mfma_f32_16x16x32_bf16 a[248:251], v[120:123], v[176:179], a[248:251]\n" \
"v_mfma_f32_16x16x32_bf16 a[248:251], v[124:127], v[180:183], a[248:251]\n" \
"v_mfma_f32_16x16x32_bf16 a[244:247], v[120:123], v[168:171], a[244:247]\n" \
"ds_read_b128 v[116:119], v41 offset:12288\n" \
"v_mfma_f32_16x16x32_bf16 a[244:247], v[124:127], v[172:175], a[244:247]\n" \
"v_mfma_f32_16x16x32_bf16 a[240:243], v[120:123], v[160:163], a[240:243]\n" \
"v_mfma_f32_16x16x32_bf16 a[240:243], v[124:127], v[164:167], a[240:243]\n" \
"s_waitcnt lgkmcnt(1)\n" \
"v_mfma_f32_16x16x32_bf16 a[0:3], v[128:131], v[64:67], a[0:3]\n" \
"ds_read_b128 v[120:123], v40 offset:14336\n" \
"v_mfma_f32_16x16x32_bf16 a[0:3], v[132:135], v[68:71], a[0:3]\n" \
"v_mfma_f32_16x16x32_bf16 a[4:7], v[136:139], v[64:67], a[4:7]\n" \
"s_add_u32 m0, s74, 32768\n" \
"ds_read_b128 v[124:127], v41 offset:14336\n" \
"v_mfma_f32_16x16x32_bf16 a[4:7], v[140:143], v[68:71], a[4:7]\n" \
"buffer_load_dwordx4 v36, s[40:43], s83 offen offset:0 lds\n" \
"v_mfma_f32_16x16x32_bf16 a[8:11], v[144:147], v[64:67], a[8:11]\n" \
"ds_read_b64_tr_b16 v[160:161], v42 offset:1024\n" \
"v_mfma_f32_16x16x32_bf16 a[8:11], v[148:151], v[68:71], a[8:11]\n" \
"v_mfma_f32_16x16x32_bf16 a[12:15], v[152:155], v[64:67], a[12:15]\n" \
"ds_read_b64_tr_b16 v[162:163], v42 offset:5120\n" \
"v_mfma_f32_16x16x32_bf16 a[12:15], v[156:159], v[68:71], a[12:15]\n" \
"buffer_load_dwordx4 v37, s[44:47], s83 offen offset:1024 lds\n" \
"v_mfma_f32_16x16x32_bf16 a[44:47], v[152:155], v[72:75], a[44:47]\n" \
"ds_read_b64_tr_b16 v[164:165], v42 offset:17408\n" \
"v_mfma_f32_16x16x32_bf16 a[44:47], v[156:159], v[76:79], a[44:47]\n" \
"v_mfma_f32_16x16x32_bf16 a[40:43], v[144:147], v[72:75], a[40:43]\n" \
"ds_read_b64_tr_b16 v[166:167], v42 offset:21504\n" \
"v_mfma_f32_16x16x32_bf16 a[40:43], v[148:151], v[76:79], a[40:43]\n" \
"buffer_load_dwordx4 v38, s[40:43], s83 offen offset:2048 lds\n" \
"v_mfma_f32_16x16x32_bf16 a[36:39], v[136:139], v[72:75], a[36:39]\n" \
"ds_read_b64_tr_b16 v[168:169], v43 offset:1024\n" \
"v_mfma_f32_16x16x32_bf16 a[36:39], v[140:143], v[76:79], a[36:39]\n" \
"v_mfma_f32_16x16x32_bf16 a[32:35], v[128:131], v[72:75], a[32:35]\n" \
"ds_read_b64_tr_b16 v[170:171], v43 offset:5120\n" \
"v_mfma_f32_16x16x32_bf16 a[32:35], v[132:135], v[76:79], a[32:35]\n" \
"buffer_load_dwordx4 v39, s[44:47], s83 offen offset:3072 lds\n" \
"v_mfma_f32_16x16x32_bf16 a[64:67], v[128:131], v[80:83], a[64:67]\n" \
"ds_read_b64_tr_b16 v[172:173], v43 offset:17408\n" \
"v_mfma_f32_16x16x32_bf16 a[64:67], v[132:135], v[84:87], a[64:67]\n" \
"s_xor_b32 s83, s83, 0x180000\n" \
"v_mfma_f32_16x16x32_bf16 a[68:71], v[136:139], v[80:83], a[68:71]\n" \
"s_add_u32 m0, s74, 36864\n" \
"ds_read_b64_tr_b16 v[174:175], v43 offset:21504\n" \
"v_mfma_f32_16x16x32_bf16 a[68:71], v[140:143], v[84:87], a[68:71]\n" \
"buffer_load_dwordx4 v36, s[40:43], s84 offen offset:0 lds\n" \
"v_mfma_f32_16x16x32_bf16 a[72:75], v[144:147], v[80:83], a[72:75]\n" \
"ds_read_b64_tr_b16 v[176:177], v42 offset:1536\n" \
"v_mfma_f32_16x16x32_bf16 a[72:75], v[148:151], v[84:87], a[72:75]\n" \
"v_mfma_f32_16x16x32_bf16 a[76:79], v[152:155], v[80:83], a[76:79]\n" \
"ds_read_b64_tr_b16 v[178:179], v42 offset:5632\n" \
"v_mfma_f32_16x16x32_bf16 a[76:79], v[156:159], v[84:87], a[76:79]\n" \
"buffer_load_dwordx4 v37, s[44:47], s84 offen offset:1024 lds\n" \
"v_mfma_f32_16x16x32_bf16 a[108:111], v[152:155], v[88:91], a[108:111]\n" \
"ds_read_b64_tr_b16 v[180:181], v42 offset:17920\n" \
"v_mfma_f32_16x16x32_bf16 a[108:111], v[156:159], v[92:95], a[108:111]\n" \
"v_mfma_f32_16x16x32_bf16 a[104:107], v[144:147], v[88:91], a[104:107]\n" \
"ds_read_b64_tr_b16 v[182:183], v42 offset:22016\n" \
"v_mfma_f32_16x16x32_bf16 a[104:107], v[148:151], v[92:95], a[104:107]\n" \
"buffer_load_dwordx4 v38, s[40:43], s84 offen offset:2048 lds\n" \
"v_mfma_f32_16x16x32_bf16 a[100:103], v[136:139], v[88:91], a[100:103]\n" \
"ds_read_b64_tr_b16 v[184:185], v43 offset:1536\n" \
"v_mfma_f32_16x16x32_bf16 a[100:103], v[140:143], v[92:95], a[100:103]\n" \
"v_mfma_f32_16x16x32_bf16 a[96:99], v[128:131], v[88:91], a[96:99]\n" \
"ds_read_b64_tr_b16 v[186:187], v43 offset:5632\n" \
"v_mfma_f32_16x16x32_bf16 a[96:99], v[132:135], v[92:95], a[96:99]\n" \
"buffer_load_dwordx4 v39, s[44:47], s84 offen offset:3072 lds\n" \
"v_mfma_f32_16x16x32_bf16 a[128:131], v[128:131], v[96:99], a[128:131]\n" \
"ds_read_b64_tr_b16 v[188:189], v43 offset:17920\n" \
"v_mfma_f32_16x16x32_bf16 a[128:131], v[132:135], v[100:103], a[128:131]\n" \
"v_mfma_f32_16x16x32_bf16 a[132:135], v[136:139], v[96:99], a[132:135]\n" \
"s_xor_b32 s84, s84, 0x180000\n" \
"ds_read_b64_tr_b16 v[190:191], v43 offset:22016\n" \
"v_mfma_f32_16x16x32_bf16 a[132:135], v[140:143], v[100:103], a[132:135]\n" \
"v_mfma_f32_16x16x32_bf16 a[136:139], v[144:147], v[96:99], a[136:139]\n" \
"v_mfma_f32_16x16x32_bf16 a[136:139], v[148:151], v[100:103], a[136:139]\n" \
"v_mfma_f32_16x16x32_bf16 a[140:143], v[152:155], v[96:99], a[140:143]\n" \
"v_mfma_f32_16x16x32_bf16 a[140:143], v[156:159], v[100:103], a[140:143]\n" \
"v_mfma_f32_16x16x32_bf16 a[172:175], v[152:155], v[104:107], a[172:175]\n" \
"v_mfma_f32_16x16x32_bf16 a[172:175], v[156:159], v[108:111], a[172:175]\n" \
"v_mfma_f32_16x16x32_bf16 a[168:171], v[144:147], v[104:107], a[168:171]\n" \
"v_mfma_f32_16x16x32_bf16 a[168:171], v[148:151], v[108:111], a[168:171]\n" \
"v_mfma_f32_16x16x32_bf16 a[164:167], v[136:139], v[104:107], a[164:167]\n" \
"v_mfma_f32_16x16x32_bf16 a[164:167], v[140:143], v[108:111], a[164:167]\n" \
"v_mfma_f32_16x16x32_bf16 a[160:163], v[128:131], v[104:107], a[160:163]\n" \
"v_mfma_f32_16x16x32_bf16 a[160:163], v[132:135], v[108:111], a[160:163]\n" \
"s_waitcnt lgkmcnt(0)\n" \
"v_mfma_f32_16x16x32_bf16 a[192:195], v[128:131], v[112:115], a[192:195]\n" \
"v_mfma_f32_16x16x32_bf16 a[192:195], v[132:135], v[116:119], a[192:195]\n" \
"v_mfma_f32_16x16x32_bf16 a[196:199], v[136:139], v[112:115], a[196:199]\n" \
"v_add_u32 v40, 0xffff8000, v40\n" \
"v_mfma_f32_16x16x32_bf16 a[196:199], v[140:143], v[116:119], a[196:199]\n" \
"v_mfma_f32_16x16x32_bf16 a[200:203], v[144:147], v[112:115], a[200:203]\n" \
"v_add_u32 v41, 0xffff8000, v41\n" \
"v_mfma_f32_16x16x32_bf16 a[200:203], v[148:151], v[116:119], a[200:203]\n" \
"v_mfma_f32_16x16x32_bf16 a[204:207], v[152:155], v[112:115], a[204:207]\n" \
"v_add_u32 v42, 0x10000, v42\n" \
"v_mfma_f32_16x16x32_bf16 a[204:207], v[156:159], v[116:119], a[204:207]\n" \
"v_mfma_f32_16x16x32_bf16 a[236:239], v[152:155], v[120:123], a[236:239]\n" \
"v_add_u32 v43, 0x10000, v43\n" \
"v_mfma_f32_16x16x32_bf16 a[236:239], v[156:159], v[124:127], a[236:239]\n" \
"v_mfma_f32_16x16x32_bf16 a[232:235], v[144:147], v[120:123], a[232:235]\n" \
"v_mfma_f32_16x16x32_bf16 a[232:235], v[148:151], v[124:127], a[232:235]\n" \
"v_mfma_f32_16x16x32_bf16 a[228:231], v[136:139], v[120:123], a[228:231]\n" \
"v_mfma_f32_16x16x32_bf16 a[228:231], v[140:143], v[124:127], a[228:231]\n" \
"v_mfma_f32_16x16x32_bf16 a[224:227], v[128:131], v[120:123], a[224:227]\n" \
"v_mfma_f32_16x16x32_bf16 a[224:227], v[132:135], v[124:127], a[224:227]\n" \
"s_waitcnt vmcnt(12) lgkmcnt(0)\n" \
"s_barrier\n" \
"v_mfma_f32_16x16x32_bf16 a[16:19], v[64:67], v[160:163], a[16:19]\n" \
"s_add_u32 m0, s74, 0\n" \
"ds_read_b64_tr_b16 v[128:129], v42 offset:0\n" \
"v_mfma_f32_16x16x32_bf16 a[16:19], v[68:71], v[164:167], a[16:19]\n" \
"buffer_load_dwordx4 v32, s[36:39], s81 offen offset:0 lds\n" \
"v_mfma_f32_16x16x32_bf16 a[20:23], v[64:67], v[168:171], a[20:23]\n" \
"ds_read_b64_tr_b16 v[130:131], v42 offset:4096\n" \
"v_mfma_f32_16x16x32_bf16 a[20:23], v[68:71], v[172:175], a[20:23]\n" \
"v_mfma_f32_16x16x32_bf16 a[24:27], v[64:67], v[176:179], a[24:27]\n" \
"ds_read_b64_tr_b16 v[132:133], v42 offset:16384\n" \
"v_mfma_f32_16x16x32_bf16 a[24:27], v[68:71], v[180:183], a[24:27]\n" \
"buffer_load_dwordx4 v33, s[36:39], s81 offen offset:1024 lds\n" \
"v_mfma_f32_16x16x32_bf16 a[28:31], v[64:67], v[184:187], a[28:31]\n" \
"ds_read_b64_tr_b16 v[134:135], v42 offset:20480\n" \
"v_mfma_f32_16x16x32_bf16 a[28:31], v[68:71], v[188:191], a[28:31]\n" \
"v_mfma_f32_16x16x32_bf16 a[60:63], v[72:75], v[184:187], a[60:63]\n" \
"ds_read_b128 v[64:67], v40 offset:0\n" \
"v_mfma_f32_16x16x32_bf16 a[60:63], v[76:79], v[188:191], a[60:63]\n" \
"buffer_load_dwordx4 v34, s[36:39], s81 offen offset:2048 lds\n" \
"v_mfma_f32_16x16x32_bf16 a[56:59], v[72:75], v[176:179], a[56:59]\n" \
"ds_read_b64_tr_b16 v[136:137], v43 offset:0\n" \
"v_mfma_f32_16x16x32_bf16 a[56:59], v[76:79], v[180:183], a[56:59]\n" \
"v_mfma_f32_16x16x32_bf16 a[52:55], v[72:75], v[168:171], a[52:55]\n" \
"ds_read_b128 v[68:71], v41 offset:0\n" \
"v_mfma_f32_16x16x32_bf16 a[52:55], v[76:79], v[172:175], a[52:55]\n" \
"buffer_load_dwordx4 v35, s[36:39], s81 offen offset:3072 lds\n" \
"v_mfma_f32_16x16x32_bf16 a[48:51], v[72:75], v[160:163], a[48:51]\n" \
"ds_read_b64_tr_b16 v[138:139], v43 offset:4096\n" \
"v_mfma_f32_16x16x32_bf16 a[48:51], v[76:79], v[164:167], a[48:51]\n" \
"s_xor_b32 s81, s81, 0x180\n" \
"v_mfma_f32_16x16x32_bf16 a[80:83], v[80:83], v[160:163], a[80:83]\n" \
"s_add_u32 m0, s74, 4096\n" \
"ds_read_b128 v[72:75], v40 offset:2048\n" \
"v_mfma_f32_16x16x32_bf16 a[80:83], v[84:87], v[164:167], a[80:83]\n" \
"buffer_load_dwordx4 v32, s[36:39], s82 offen offset:0 lds\n" \
"v_mfma_f32_16x16x32_bf16 a[84:87], v[80:83], v[168:171], a[84:87]\n" \
"ds_read_b64_tr_b16 v[140:141], v43 offset:16384\n" \
"v_mfma_f32_16x16x32_bf16 a[84:87], v[84:87], v[172:175], a[84:87]\n" \
"v_mfma_f32_16x16x32_bf16 a[88:91], v[80:83], v[176:179], a[88:91]\n" \
"ds_read_b128 v[76:79], v41 offset:2048\n" \
"v_mfma_f32_16x16x32_bf16 a[88:91], v[84:87], v[180:183], a[88:91]\n" \
"buffer_load_dwordx4 v33, s[36:39], s82 offen offset:1024 lds\n" \
"v_mfma_f32_16x16x32_bf16 a[92:95], v[80:83], v[184:187], a[92:95]\n" \
"ds_read_b64_tr_b16 v[142:143], v43 offset:20480\n" \
"v_mfma_f32_16x16x32_bf16 a[92:95], v[84:87], v[188:191], a[92:95]\n" \
"v_mfma_f32_16x16x32_bf16 a[124:127], v[88:91], v[184:187], a[124:127]\n" \
"ds_read_b128 v[80:83], v40 offset:4096\n" \
"v_mfma_f32_16x16x32_bf16 a[124:127], v[92:95], v[188:191], a[124:127]\n" \
"buffer_load_dwordx4 v34, s[36:39], s82 offen offset:2048 lds\n" \
"v_mfma_f32_16x16x32_bf16 a[120:123], v[88:91], v[176:179], a[120:123]\n" \
"ds_read_b64_tr_b16 v[144:145], v42 offset:512\n" \
"v_mfma_f32_16x16x32_bf16 a[120:123], v[92:95], v[180:183], a[120:123]\n" \
"v_mfma_f32_16x16x32_bf16 a[116:119], v[88:91], v[168:171], a[116:119]\n" \
"ds_read_b128 v[84:87], v41 offset:4096\n" \
"v_mfma_f32_16x16x32_bf16 a[116:119], v[92:95], v[172:175], a[116:119]\n" \
"buffer_load_dwordx4 v35, s[36:39], s82 offen offset:3072 lds\n" \
"v_mfma_f32_16x16x32_bf16 a[112:115], v[88:91], v[160:163], a[112:115]\n" \
"s_mov_b64 s[64:65], s[8:9]\n" \
"s_add_u32 s68, s66, 0x200000\n" \
"Lpg1113_%=:\n" \
"Lst0x1_%=:\n" \
"s_cbranch_execz Lpg3260_%=\n" \
"ds_read_b64_tr_b16 v[146:147], v42 offset:4608\n" \
"v_mfma_f32_16x16x32_bf16 a[112:115], v[92:95], v[164:167], a[112:115]\n" \
"s_add_u32 s64, s64, s68\n" \
"s_addc_u32 s65, s65, 0\n" \
"v_mfma_f32_16x16x32_bf16 a[144:147], v[96:99], v[160:163], a[144:147]\n" \
"s_xor_b32 s82, s82, 0x180\n" \
"ds_read_b128 v[88:91], v40 offset:6144\n" \
"v_mfma_f32_16x16x32_bf16 a[144:147], v[100:103], v[164:167], a[144:147]\n" \
"global_load_dword v238, v234, s[64:65]\n" \
"v_mfma_f32_16x16x32_bf16 a[148:151], v[96:99], v[168:171], a[148:151]\n" \
"s_mov_b64 s[64:65], s[8:9]\n" \
"s_add_u32 s68, s66, 0x300000\n" \
"ds_read_b64_tr_b16 v[148:149], v42 offset:16896\n" \
"v_mfma_f32_16x16x32_bf16 a[148:151], v[100:103], v[172:175], a[148:151]\n" \
"s_add_u32 s64, s64, s68\n" \
"s_addc_u32 s65, s65, 0\n" \
"v_mfma_f32_16x16x32_bf16 a[152:155], v[96:99], v[176:179], a[152:155]\n" \
"ds_read_b128 v[92:95], v41 offset:6144\n" \
"v_mfma_f32_16x16x32_bf16 a[152:155], v[100:103], v[180:183], a[152:155]\n" \
"global_load_dword v238, v234, s[64:65]\n" \
"v_mfma_f32_16x16x32_bf16 a[156:159], v[96:99], v[184:187], a[156:159]\n" \
"s_mov_b64 s[64:65], s[12:13]\n" \
"s_add_u32 s68, s67, 0x0\n" \
"ds_read_b64_tr_b16 v[150:151], v42 offset:20992\n" \
"v_mfma_f32_16x16x32_bf16 a[156:159], v[100:103], v[188:191], a[156:159]\n" \
"s_add_u32 s64, s64, s68\n" \
"s_addc_u32 s65, s65, 0\n" \
"v_mfma_f32_16x16x32_bf16 a[188:191], v[104:107], v[184:187], a[188:191]\n" \
"ds_read_b128 v[96:99], v40 offset:8192\n" \
"v_mfma_f32_16x16x32_bf16 a[188:191], v[108:111], v[188:191], a[188:191]\n" \
"global_load_dword v238, v234, s[64:65]\n" \
"v_mfma_f32_16x16x32_bf16 a[184:187], v[104:107], v[176:179], a[184:187]\n" \
"ds_read_b64_tr_b16 v[152:153], v43 offset:512\n" \
"v_mfma_f32_16x16x32_bf16 a[184:187], v[108:111], v[180:183], a[184:187]\n" \
"v_mfma_f32_16x16x32_bf16 a[180:183], v[104:107], v[168:171], a[180:183]\n" \
"ds_read_b128 v[100:103], v41 offset:8192\n" \
"v_mfma_f32_16x16x32_bf16 a[180:183], v[108:111], v[172:175], a[180:183]\n" \
"v_mfma_f32_16x16x32_bf16 a[176:179], v[104:107], v[160:163], a[176:179]\n" \
"ds_read_b64_tr_b16 v[154:155], v43 offset:4608\n" \
"v_mfma_f32_16x16x32_bf16 a[176:179], v[108:111], v[164:167], a[176:179]\n" \
"v_mfma_f32_16x16x32_bf16 a[208:211], v[112:115], v[160:163], a[208:211]\n" \
"ds_read_b128 v[104:107], v40 offset:10240\n" \
"v_mfma_f32_16x16x32_bf16 a[208:211], v[116:119], v[164:167], a[208:211]\n" \
"v_mfma_f32_16x16x32_bf16 a[212:215], v[112:115], v[168:171], a[212:215]\n" \
"ds_read_b64_tr_b16 v[156:157], v43 offset:16896\n" \
"v_mfma_f32_16x16x32_bf16 a[212:215], v[116:119], v[172:175], a[212:215]\n" \
"v_mfma_f32_16x16x32_bf16 a[216:219], v[112:115], v[176:179], a[216:219]\n" \
"ds_read_b128 v[108:111], v41 offset:10240\n" \
"v_mfma_f32_16x16x32_bf16 a[216:219], v[116:119], v[180:183], a[216:219]\n" \
"v_mfma_f32_16x16x32_bf16 a[220:223], v[112:115], v[184:187], a[220:223]\n" \
"ds_read_b64_tr_b16 v[158:159], v43 offset:20992\n" \
"v_mfma_f32_16x16x32_bf16 a[220:223], v[116:119], v[188:191], a[220:223]\n" \
"v_mfma_f32_16x16x32_bf16 a[252:255], v[120:123], v[184:187], a[252:255]\n" \
"ds_read_b128 v[112:115], v40 offset:12288\n" \
"v_mfma_f32_16x16x32_bf16 a[252:255], v[124:127], v[188:191], a[252:255]\n" \
"v_mfma_f32_16x16x32_bf16 a[248:251], v[120:123], v[176:179], a[248:251]\n" \
"v_mfma_f32_16x16x32_bf16 a[248:251], v[124:127], v[180:183], a[248:251]\n" \
"v_mfma_f32_16x16x32_bf16 a[244:247], v[120:123], v[168:171], a[244:247]\n" \
"ds_read_b128 v[116:119], v41 offset:12288\n" \
"v_mfma_f32_16x16x32_bf16 a[244:247], v[124:127], v[172:175], a[244:247]\n" \
"v_mfma_f32_16x16x32_bf16 a[240:243], v[120:123], v[160:163], a[240:243]\n" \
"v_mfma_f32_16x16x32_bf16 a[240:243], v[124:127], v[164:167], a[240:243]\n" \
"s_waitcnt lgkmcnt(1)\n" \
"v_mfma_f32_16x16x32_bf16 a[0:3], v[128:131], v[64:67], a[0:3]\n" \
"ds_read_b128 v[120:123], v40 offset:14336\n" \
"v_mfma_f32_16x16x32_bf16 a[0:3], v[132:135], v[68:71], a[0:3]\n" \
"v_mfma_f32_16x16x32_bf16 a[4:7], v[136:139], v[64:67], a[4:7]\n" \
"s_add_u32 m0, s74, 131072\n" \
"ds_read_b128 v[124:127], v41 offset:14336\n" \
"v_mfma_f32_16x16x32_bf16 a[4:7], v[140:143], v[68:71], a[4:7]\n" \
"buffer_load_dwordx4 v36, s[40:43], s83 offen offset:0 lds\n" \
"v_mfma_f32_16x16x32_bf16 a[8:11], v[144:147], v[64:67], a[8:11]\n" \
"ds_read_b64_tr_b16 v[160:161], v42 offset:1024\n" \
"v_mfma_f32_16x16x32_bf16 a[8:11], v[148:151], v[68:71], a[8:11]\n" \
"v_mfma_f32_16x16x32_bf16 a[12:15], v[152:155], v[64:67], a[12:15]\n" \
"ds_read_b64_tr_b16 v[162:163], v42 offset:5120\n" \
"v_mfma_f32_16x16x32_bf16 a[12:15], v[156:159], v[68:71], a[12:15]\n" \
"buffer_load_dwordx4 v37, s[44:47], s83 offen offset:1024 lds\n" \
"v_mfma_f32_16x16x32_bf16 a[44:47], v[152:155], v[72:75], a[44:47]\n" \
"ds_read_b64_tr_b16 v[164:165], v42 offset:17408\n" \
"v_mfma_f32_16x16x32_bf16 a[44:47], v[156:159], v[76:79], a[44:47]\n" \
"v_mfma_f32_16x16x32_bf16 a[40:43], v[144:147], v[72:75], a[40:43]\n" \
"ds_read_b64_tr_b16 v[166:167], v42 offset:21504\n" \
"v_mfma_f32_16x16x32_bf16 a[40:43], v[148:151], v[76:79], a[40:43]\n" \
"buffer_load_dwordx4 v38, s[40:43], s83 offen offset:2048 lds\n" \
"v_mfma_f32_16x16x32_bf16 a[36:39], v[136:139], v[72:75], a[36:39]\n" \
"ds_read_b64_tr_b16 v[168:169], v43 offset:1024\n" \
"v_mfma_f32_16x16x32_bf16 a[36:39], v[140:143], v[76:79], a[36:39]\n" \
"v_mfma_f32_16x16x32_bf16 a[32:35], v[128:131], v[72:75], a[32:35]\n" \
"ds_read_b64_tr_b16 v[170:171], v43 offset:5120\n" \
"v_mfma_f32_16x16x32_bf16 a[32:35], v[132:135], v[76:79], a[32:35]\n" \
"buffer_load_dwordx4 v39, s[44:47], s83 offen offset:3072 lds\n" \
"v_mfma_f32_16x16x32_bf16 a[64:67], v[128:131], v[80:83], a[64:67]\n" \
"ds_read_b64_tr_b16 v[172:173], v43 offset:17408\n" \
"v_mfma_f32_16x16x32_bf16 a[64:67], v[132:135], v[84:87], a[64:67]\n" \
"s_xor_b32 s83, s83, 0x80000\n" \
"v_mfma_f32_16x16x32_bf16 a[68:71], v[136:139], v[80:83], a[68:71]\n" \
"s_add_u32 m0, s74, 135168\n" \
"ds_read_b64_tr_b16 v[174:175], v43 offset:21504\n" \
"v_mfma_f32_16x16x32_bf16 a[68:71], v[140:143], v[84:87], a[68:71]\n" \
"buffer_load_dwordx4 v36, s[40:43], s84 offen offset:0 lds\n" \
"v_mfma_f32_16x16x32_bf16 a[72:75], v[144:147], v[80:83], a[72:75]\n" \
"ds_read_b64_tr_b16 v[176:177], v42 offset:1536\n" \
"v_mfma_f32_16x16x32_bf16 a[72:75], v[148:151], v[84:87], a[72:75]\n" \
"v_mfma_f32_16x16x32_bf16 a[76:79], v[152:155], v[80:83], a[76:79]\n" \
"ds_read_b64_tr_b16 v[178:179], v42 offset:5632\n" \
"v_mfma_f32_16x16x32_bf16 a[76:79], v[156:159], v[84:87], a[76:79]\n" \
"buffer_load_dwordx4 v37, s[44:47], s84 offen offset:1024 lds\n" \
"v_mfma_f32_16x16x32_bf16 a[108:111], v[152:155], v[88:91], a[108:111]\n" \
"ds_read_b64_tr_b16 v[180:181], v42 offset:17920\n" \
"v_mfma_f32_16x16x32_bf16 a[108:111], v[156:159], v[92:95], a[108:111]\n" \
"v_mfma_f32_16x16x32_bf16 a[104:107], v[144:147], v[88:91], a[104:107]\n" \
"ds_read_b64_tr_b16 v[182:183], v42 offset:22016\n" \
"v_mfma_f32_16x16x32_bf16 a[104:107], v[148:151], v[92:95], a[104:107]\n" \
"buffer_load_dwordx4 v38, s[40:43], s84 offen offset:2048 lds\n" \
"v_mfma_f32_16x16x32_bf16 a[100:103], v[136:139], v[88:91], a[100:103]\n" \
"ds_read_b64_tr_b16 v[184:185], v43 offset:1536\n" \
"v_mfma_f32_16x16x32_bf16 a[100:103], v[140:143], v[92:95], a[100:103]\n" \
"v_mfma_f32_16x16x32_bf16 a[96:99], v[128:131], v[88:91], a[96:99]\n" \
"ds_read_b64_tr_b16 v[186:187], v43 offset:5632\n" \
"v_mfma_f32_16x16x32_bf16 a[96:99], v[132:135], v[92:95], a[96:99]\n" \
"buffer_load_dwordx4 v39, s[44:47], s84 offen offset:3072 lds\n" \
"v_mfma_f32_16x16x32_bf16 a[128:131], v[128:131], v[96:99], a[128:131]\n" \
"ds_read_b64_tr_b16 v[188:189], v43 offset:17920\n" \
"v_mfma_f32_16x16x32_bf16 a[128:131], v[132:135], v[100:103], a[128:131]\n" \
"v_mfma_f32_16x16x32_bf16 a[132:135], v[136:139], v[96:99], a[132:135]\n" \
"s_xor_b32 s84, s84, 0x80000\n" \
"ds_read_b64_tr_b16 v[190:191], v43 offset:22016\n" \
"v_mfma_f32_16x16x32_bf16 a[132:135], v[140:143], v[100:103], a[132:135]\n" \
"v_mfma_f32_16x16x32_bf16 a[136:139], v[144:147], v[96:99], a[136:139]\n" \
"v_mfma_f32_16x16x32_bf16 a[136:139], v[148:151], v[100:103], a[136:139]\n" \
"v_mfma_f32_16x16x32_bf16 a[140:143], v[152:155], v[96:99], a[140:143]\n" \
"v_mfma_f32_16x16x32_bf16 a[140:143], v[156:159], v[100:103], a[140:143]\n" \
"v_mfma_f32_16x16x32_bf16 a[172:175], v[152:155], v[104:107], a[172:175]\n" \
"v_mfma_f32_16x16x32_bf16 a[172:175], v[156:159], v[108:111], a[172:175]\n" \
"v_mfma_f32_16x16x32_bf16 a[168:171], v[144:147], v[104:107], a[168:171]\n" \
"v_mfma_f32_16x16x32_bf16 a[168:171], v[148:151], v[108:111], a[168:171]\n" \
"v_mfma_f32_16x16x32_bf16 a[164:167], v[136:139], v[104:107], a[164:167]\n" \
"v_mfma_f32_16x16x32_bf16 a[164:167], v[140:143], v[108:111], a[164:167]\n" \
"v_mfma_f32_16x16x32_bf16 a[160:163], v[128:131], v[104:107], a[160:163]\n" \
"v_mfma_f32_16x16x32_bf16 a[160:163], v[132:135], v[108:111], a[160:163]\n" \
"s_waitcnt lgkmcnt(0)\n" \
"v_mfma_f32_16x16x32_bf16 a[192:195], v[128:131], v[112:115], a[192:195]\n" \
"v_mfma_f32_16x16x32_bf16 a[192:195], v[132:135], v[116:119], a[192:195]\n" \
"v_mfma_f32_16x16x32_bf16 a[196:199], v[136:139], v[112:115], a[196:199]\n" \
"v_add_u32 v40, 0xfffe8000, v40\n" \
"v_mfma_f32_16x16x32_bf16 a[196:199], v[140:143], v[116:119], a[196:199]\n" \
"v_mfma_f32_16x16x32_bf16 a[200:203], v[144:147], v[112:115], a[200:203]\n" \
"v_add_u32 v41, 0xfffe8000, v41\n" \
"v_mfma_f32_16x16x32_bf16 a[200:203], v[148:151], v[116:119], a[200:203]\n" \
"v_mfma_f32_16x16x32_bf16 a[204:207], v[152:155], v[112:115], a[204:207]\n" \
"v_add_u32 v42, 0xffff8000, v42\n" \
"v_mfma_f32_16x16x32_bf16 a[204:207], v[156:159], v[116:119], a[204:207]\n" \
"v_mfma_f32_16x16x32_bf16 a[236:239], v[152:155], v[120:123], a[236:239]\n" \
"v_add_u32 v43, 0xffff8000, v43\n" \
"v_mfma_f32_16x16x32_bf16 a[236:239], v[156:159], v[124:127], a[236:239]\n" \
"v_mfma_f32_16x16x32_bf16 a[232:235], v[144:147], v[120:123], a[232:235]\n" \
"v_mfma_f32_16x16x32_bf16 a[232:235], v[148:151], v[124:127], a[232:235]\n" \
"v_mfma_f32_16x16x32_bf16 a[228:231], v[136:139], v[120:123], a[228:231]\n" \
"v_mfma_f32_16x16x32_bf16 a[228:231], v[140:143], v[124:127], a[228:231]\n" \
"v_mfma_f32_16x16x32_bf16 a[224:227], v[128:131], v[120:123], a[224:227]\n" \
"v_mfma_f32_16x16x32_bf16 a[224:227], v[132:135], v[124:127], a[224:227]\n" \
"s_waitcnt vmcnt(11) lgkmcnt(0)\n" \
"s_barrier\n" \
"v_mfma_f32_16x16x32_bf16 a[16:19], v[64:67], v[160:163], a[16:19]\n" \
"s_add_u32 m0, s74, 65536\n" \
"ds_read_b64_tr_b16 v[128:129], v42 offset:0\n" \
"v_mfma_f32_16x16x32_bf16 a[16:19], v[68:71], v[164:167], a[16:19]\n" \
"buffer_load_dwordx4 v32, s[36:39], s81 offen offset:0 lds\n" \
"v_mfma_f32_16x16x32_bf16 a[20:23], v[64:67], v[168:171], a[20:23]\n" \
"ds_read_b64_tr_b16 v[130:131], v42 offset:4096\n" \
"v_mfma_f32_16x16x32_bf16 a[20:23], v[68:71], v[172:175], a[20:23]\n" \
"v_mfma_f32_16x16x32_bf16 a[24:27], v[64:67], v[176:179], a[24:27]\n" \
"ds_read_b64_tr_b16 v[132:133], v42 offset:16384\n" \
"v_mfma_f32_16x16x32_bf16 a[24:27], v[68:71], v[180:183], a[24:27]\n" \
"buffer_load_dwordx4 v33, s[36:39], s81 offen offset:1024 lds\n" \
"v_mfma_f32_16x16x32_bf16 a[28:31], v[64:67], v[184:187], a[28:31]\n" \
"ds_read_b64_tr_b16 v[134:135], v42 offset:20480\n" \
"v_mfma_f32_16x16x32_bf16 a[28:31], v[68:71], v[188:191], a[28:31]\n" \
"v_mfma_f32_16x16x32_bf16 a[60:63], v[72:75], v[184:187], a[60:63]\n" \
"ds_read_b128 v[64:67], v40 offset:0\n" \
"v_mfma_f32_16x16x32_bf16 a[60:63], v[76:79], v[188:191], a[60:63]\n" \
"buffer_load_dwordx4 v34, s[36:39], s81 offen offset:2048 lds\n" \
"v_mfma_f32_16x16x32_bf16 a[56:59], v[72:75], v[176:179], a[56:59]\n" \
"ds_read_b64_tr_b16 v[136:137], v43 offset:0\n" \
"v_mfma_f32_16x16x32_bf16 a[56:59], v[76:79], v[180:183], a[56:59]\n" \
"v_mfma_f32_16x16x32_bf16 a[52:55], v[72:75], v[168:171], a[52:55]\n" \
"ds_read_b128 v[68:71], v41 offset:0\n" \
"v_mfma_f32_16x16x32_bf16 a[52:55], v[76:79], v[172:175], a[52:55]\n" \
"buffer_load_dwordx4 v35, s[36:39], s81 offen offset:3072 lds\n" \
"v_mfma_f32_16x16x32_bf16 a[48:51], v[72:75], v[160:163], a[48:51]\n" \
"ds_read_b64_tr_b16 v[138:139], v43 offset:4096\n" \
"v_mfma_f32_16x16x32_bf16 a[48:51], v[76:79], v[164:167], a[48:51]\n" \
"s_xor_b32 s81, s81, 0x80\n" \
"v_mfma_f32_16x16x32_bf16 a[80:83], v[80:83], v[160:163], a[80:83]\n" \
"s_add_u32 m0, s74, 69632\n" \
"ds_read_b128 v[72:75], v40 offset:2048\n" \
"v_mfma_f32_16x16x32_bf16 a[80:83], v[84:87], v[164:167], a[80:83]\n" \
"buffer_load_dwordx4 v32, s[36:39], s82 offen offset:0 lds\n" \
"v_mfma_f32_16x16x32_bf16 a[84:87], v[80:83], v[168:171], a[84:87]\n" \
"ds_read_b64_tr_b16 v[140:141], v43 offset:16384\n" \
"v_mfma_f32_16x16x32_bf16 a[84:87], v[84:87], v[172:175], a[84:87]\n" \
"v_mfma_f32_16x16x32_bf16 a[88:91], v[80:83], v[176:179], a[88:91]\n" \
"ds_read_b128 v[76:79], v41 offset:2048\n" \
"v_mfma_f32_16x16x32_bf16 a[88:91], v[84:87], v[180:183], a[88:91]\n" \
"buffer_load_dwordx4 v33, s[36:39], s82 offen offset:1024 lds\n" \
"v_mfma_f32_16x16x32_bf16 a[92:95], v[80:83], v[184:187], a[92:95]\n" \
"ds_read_b64_tr_b16 v[142:143], v43 offset:20480\n" \
"v_mfma_f32_16x16x32_bf16 a[92:95], v[84:87], v[188:191], a[92:95]\n" \
"v_mfma_f32_16x16x32_bf16 a[124:127], v[88:91], v[184:187], a[124:127]\n" \
"ds_read_b128 v[80:83], v40 offset:4096\n" \
"v_mfma_f32_16x16x32_bf16 a[124:127], v[92:95], v[188:191], a[124:127]\n" \
"buffer_load_dwordx4 v34, s[36:39], s82 offen offset:2048 lds\n" \
"v_mfma_f32_16x16x32_bf16 a[120:123], v[88:91], v[176:179], a[120:123]\n" \
"ds_read_b64_tr_b16 v[144:145], v42 offset:512\n" \
"v_mfma_f32_16x16x32_bf16 a[120:123], v[92:95], v[180:183], a[120:123]\n" \
"v_mfma_f32_16x16x32_bf16 a[116:119], v[88:91], v[168:171], a[116:119]\n" \
"ds_read_b128 v[84:87], v41 offset:4096\n" \
"v_mfma_f32_16x16x32_bf16 a[116:119], v[92:95], v[172:175], a[116:119]\n" \
"buffer_load_dwordx4 v35, s[36:39], s82 offen offset:3072 lds\n" \
"v_mfma_f32_16x16x32_bf16 a[112:115], v[88:91], v[160:163], a[112:115]\n" \
"s_mov_b64 s[64:65], s[12:13]\n" \
"s_add_u32 s68, s67, 0x100000\n" \
"ds_read_b64_tr_b16 v[146:147], v42 offset:4608\n" \
"v_mfma_f32_16x16x32_bf16 a[112:115], v[92:95], v[164:167], a[112:115]\n" \
"s_add_u32 s64, s64, s68\n" \
"s_addc_u32 s65, s65, 0\n" \
"v_mfma_f32_16x16x32_bf16 a[144:147], v[96:99], v[160:163], a[144:147]\n" \
"s_xor_b32 s82, s82, 0x80\n" \
"ds_read_b128 v[88:91], v40 offset:6144\n" \
"v_mfma_f32_16x16x32_bf16 a[144:147], v[100:103], v[164:167], a[144:147]\n" \
"global_load_dword v238, v234, s[64:65]\n" \
"v_mfma_f32_16x16x32_bf16 a[148:151], v[96:99], v[168:171], a[148:151]\n" \
"s_mov_b64 s[64:65], s[12:13]\n" \
"s_add_u32 s68, s67, 0x500000\n" \
"ds_read_b64_tr_b16 v[148:149], v42 offset:16896\n" \
"v_mfma_f32_16x16x32_bf16 a[148:151], v[100:103], v[172:175], a[148:151]\n" \
"s_add_u32 s64, s64, s68\n" \
"s_addc_u32 s65, s65, 0\n" \
"v_mfma_f32_16x16x32_bf16 a[152:155], v[96:99], v[176:179], a[152:155]\n" \
"ds_read_b128 v[92:95], v41 offset:6144\n" \
"v_mfma_f32_16x16x32_bf16 a[152:155], v[100:103], v[180:183], a[152:155]\n" \
"global_load_dword v238, v234, s[64:65]\n" \
"v_mfma_f32_16x16x32_bf16 a[156:159], v[96:99], v[184:187], a[156:159]\n" \
"s_mov_b64 s[64:65], s[12:13]\n" \
"s_add_u32 s68, s67, 0x200000\n" \
"ds_read_b64_tr_b16 v[150:151], v42 offset:20992\n" \
"v_mfma_f32_16x16x32_bf16 a[156:159], v[100:103], v[188:191], a[156:159]\n" \
"s_add_u32 s64, s64, s68\n" \
"s_addc_u32 s65, s65, 0\n" \
"v_mfma_f32_16x16x32_bf16 a[188:191], v[104:107], v[184:187], a[188:191]\n" \
"ds_read_b128 v[96:99], v40 offset:8192\n" \
"v_mfma_f32_16x16x32_bf16 a[188:191], v[108:111], v[188:191], a[188:191]\n" \
"global_load_dword v238, v234, s[64:65]\n" \
"v_mfma_f32_16x16x32_bf16 a[184:187], v[104:107], v[176:179], a[184:187]\n" \
"ds_read_b64_tr_b16 v[152:153], v43 offset:512\n" \
"v_mfma_f32_16x16x32_bf16 a[184:187], v[108:111], v[180:183], a[184:187]\n" \
"v_mfma_f32_16x16x32_bf16 a[180:183], v[104:107], v[168:171], a[180:183]\n" \
"ds_read_b128 v[100:103], v41 offset:8192\n" \
"v_mfma_f32_16x16x32_bf16 a[180:183], v[108:111], v[172:175], a[180:183]\n" \
"v_mfma_f32_16x16x32_bf16 a[176:179], v[104:107], v[160:163], a[176:179]\n" \
"ds_read_b64_tr_b16 v[154:155], v43 offset:4608\n" \
"v_mfma_f32_16x16x32_bf16 a[176:179], v[108:111], v[164:167], a[176:179]\n" \
"v_mfma_f32_16x16x32_bf16 a[208:211], v[112:115], v[160:163], a[208:211]\n" \
"ds_read_b128 v[104:107], v40 offset:10240\n" \
"v_mfma_f32_16x16x32_bf16 a[208:211], v[116:119], v[164:167], a[208:211]\n" \
"v_mfma_f32_16x16x32_bf16 a[212:215], v[112:115], v[168:171], a[212:215]\n" \
"ds_read_b64_tr_b16 v[156:157], v43 offset:16896\n" \
"v_mfma_f32_16x16x32_bf16 a[212:215], v[116:119], v[172:175], a[212:215]\n" \
"v_mfma_f32_16x16x32_bf16 a[216:219], v[112:115], v[176:179], a[216:219]\n" \
"ds_read_b128 v[108:111], v41 offset:10240\n" \
"v_mfma_f32_16x16x32_bf16 a[216:219], v[116:119], v[180:183], a[216:219]\n" \
"v_mfma_f32_16x16x32_bf16 a[220:223], v[112:115], v[184:187], a[220:223]\n" \
"ds_read_b64_tr_b16 v[158:159], v43 offset:20992\n" \
"v_mfma_f32_16x16x32_bf16 a[220:223], v[116:119], v[188:191], a[220:223]\n" \
"v_mfma_f32_16x16x32_bf16 a[252:255], v[120:123], v[184:187], a[252:255]\n" \
"ds_read_b128 v[112:115], v40 offset:12288\n" \
"v_mfma_f32_16x16x32_bf16 a[252:255], v[124:127], v[188:191], a[252:255]\n" \
"v_mfma_f32_16x16x32_bf16 a[248:251], v[120:123], v[176:179], a[248:251]\n" \
"v_mfma_f32_16x16x32_bf16 a[248:251], v[124:127], v[180:183], a[248:251]\n" \
"v_mfma_f32_16x16x32_bf16 a[244:247], v[120:123], v[168:171], a[244:247]\n" \
"ds_read_b128 v[116:119], v41 offset:12288\n" \
"v_mfma_f32_16x16x32_bf16 a[244:247], v[124:127], v[172:175], a[244:247]\n" \
"v_mfma_f32_16x16x32_bf16 a[240:243], v[120:123], v[160:163], a[240:243]\n" \
"v_mfma_f32_16x16x32_bf16 a[240:243], v[124:127], v[164:167], a[240:243]\n" \
"s_waitcnt lgkmcnt(1)\n" \
"v_mfma_f32_16x16x32_bf16 a[0:3], v[128:131], v[64:67], a[0:3]\n" \
"ds_read_b128 v[120:123], v40 offset:14336\n" \
"v_mfma_f32_16x16x32_bf16 a[0:3], v[132:135], v[68:71], a[0:3]\n" \
"v_mfma_f32_16x16x32_bf16 a[4:7], v[136:139], v[64:67], a[4:7]\n" \
"s_add_u32 m0, s74, 98304\n" \
"ds_read_b128 v[124:127], v41 offset:14336\n" \
"v_mfma_f32_16x16x32_bf16 a[4:7], v[140:143], v[68:71], a[4:7]\n" \
"buffer_load_dwordx4 v36, s[40:43], s83 offen offset:0 lds\n" \
"v_mfma_f32_16x16x32_bf16 a[8:11], v[144:147], v[64:67], a[8:11]\n" \
"ds_read_b64_tr_b16 v[160:161], v42 offset:1024\n" \
"v_mfma_f32_16x16x32_bf16 a[8:11], v[148:151], v[68:71], a[8:11]\n" \
"v_mfma_f32_16x16x32_bf16 a[12:15], v[152:155], v[64:67], a[12:15]\n" \
"ds_read_b64_tr_b16 v[162:163], v42 offset:5120\n" \
"v_mfma_f32_16x16x32_bf16 a[12:15], v[156:159], v[68:71], a[12:15]\n" \
"buffer_load_dwordx4 v37, s[44:47], s83 offen offset:1024 lds\n" \
"v_mfma_f32_16x16x32_bf16 a[44:47], v[152:155], v[72:75], a[44:47]\n" \
"ds_read_b64_tr_b16 v[164:165], v42 offset:17408\n" \
"v_mfma_f32_16x16x32_bf16 a[44:47], v[156:159], v[76:79], a[44:47]\n" \
"v_mfma_f32_16x16x32_bf16 a[40:43], v[144:147], v[72:75], a[40:43]\n" \
"ds_read_b64_tr_b16 v[166:167], v42 offset:21504\n" \
"v_mfma_f32_16x16x32_bf16 a[40:43], v[148:151], v[76:79], a[40:43]\n" \
"buffer_load_dwordx4 v38, s[40:43], s83 offen offset:2048 lds\n" \
"v_mfma_f32_16x16x32_bf16 a[36:39], v[136:139], v[72:75], a[36:39]\n" \
"ds_read_b64_tr_b16 v[168:169], v43 offset:1024\n" \
"v_mfma_f32_16x16x32_bf16 a[36:39], v[140:143], v[76:79], a[36:39]\n" \
"v_mfma_f32_16x16x32_bf16 a[32:35], v[128:131], v[72:75], a[32:35]\n" \
"ds_read_b64_tr_b16 v[170:171], v43 offset:5120\n" \
"v_mfma_f32_16x16x32_bf16 a[32:35], v[132:135], v[76:79], a[32:35]\n" \
"buffer_load_dwordx4 v39, s[44:47], s83 offen offset:3072 lds\n" \
"v_mfma_f32_16x16x32_bf16 a[64:67], v[128:131], v[80:83], a[64:67]\n" \
"ds_read_b64_tr_b16 v[172:173], v43 offset:17408\n" \
"v_mfma_f32_16x16x32_bf16 a[64:67], v[132:135], v[84:87], a[64:67]\n" \
"v_mfma_f32_16x16x32_bf16 a[68:71], v[136:139], v[80:83], a[68:71]\n" \
"s_add_u32 m0, s74, 102400\n" \
"ds_read_b64_tr_b16 v[174:175], v43 offset:21504\n" \
"v_mfma_f32_16x16x32_bf16 a[68:71], v[140:143], v[84:87], a[68:71]\n" \
"buffer_load_dwordx4 v36, s[40:43], s84 offen offset:0 lds\n" \
"v_mfma_f32_16x16x32_bf16 a[72:75], v[144:147], v[80:83], a[72:75]\n" \
"ds_read_b64_tr_b16 v[176:177], v42 offset:1536\n" \
"v_mfma_f32_16x16x32_bf16 a[72:75], v[148:151], v[84:87], a[72:75]\n" \
"v_mfma_f32_16x16x32_bf16 a[76:79], v[152:155], v[80:83], a[76:79]\n" \
"ds_read_b64_tr_b16 v[178:179], v42 offset:5632\n" \
"v_mfma_f32_16x16x32_bf16 a[76:79], v[156:159], v[84:87], a[76:79]\n" \
"buffer_load_dwordx4 v37, s[44:47], s84 offen offset:1024 lds\n" \
"v_mfma_f32_16x16x32_bf16 a[108:111], v[152:155], v[88:91], a[108:111]\n" \
"ds_read_b64_tr_b16 v[180:181], v42 offset:17920\n" \
"v_mfma_f32_16x16x32_bf16 a[108:111], v[156:159], v[92:95], a[108:111]\n" \
"v_mfma_f32_16x16x32_bf16 a[104:107], v[144:147], v[88:91], a[104:107]\n" \
"ds_read_b64_tr_b16 v[182:183], v42 offset:22016\n" \
"v_mfma_f32_16x16x32_bf16 a[104:107], v[148:151], v[92:95], a[104:107]\n" \
"buffer_load_dwordx4 v38, s[40:43], s84 offen offset:2048 lds\n" \
"v_mfma_f32_16x16x32_bf16 a[100:103], v[136:139], v[88:91], a[100:103]\n" \
"ds_read_b64_tr_b16 v[184:185], v43 offset:1536\n" \
"v_mfma_f32_16x16x32_bf16 a[100:103], v[140:143], v[92:95], a[100:103]\n" \
"v_mfma_f32_16x16x32_bf16 a[96:99], v[128:131], v[88:91], a[96:99]\n" \
"ds_read_b64_tr_b16 v[186:187], v43 offset:5632\n" \
"v_mfma_f32_16x16x32_bf16 a[96:99], v[132:135], v[92:95], a[96:99]\n" \
"buffer_load_dwordx4 v39, s[44:47], s84 offen offset:3072 lds\n" \
"v_mfma_f32_16x16x32_bf16 a[128:131], v[128:131], v[96:99], a[128:131]\n" \
"ds_read_b64_tr_b16 v[188:189], v43 offset:17920\n" \
"v_mfma_f32_16x16x32_bf16 a[128:131], v[132:135], v[100:103], a[128:131]\n" \
"v_mfma_f32_16x16x32_bf16 a[132:135], v[136:139], v[96:99], a[132:135]\n" \
"ds_read_b64_tr_b16 v[190:191], v43 offset:22016\n" \
"v_mfma_f32_16x16x32_bf16 a[132:135], v[140:143], v[100:103], a[132:135]\n" \
"v_mfma_f32_16x16x32_bf16 a[136:139], v[144:147], v[96:99], a[136:139]\n" \
"v_mfma_f32_16x16x32_bf16 a[136:139], v[148:151], v[100:103], a[136:139]\n" \
"v_mfma_f32_16x16x32_bf16 a[140:143], v[152:155], v[96:99], a[140:143]\n" \
"v_mfma_f32_16x16x32_bf16 a[140:143], v[156:159], v[100:103], a[140:143]\n" \
"v_mfma_f32_16x16x32_bf16 a[172:175], v[152:155], v[104:107], a[172:175]\n" \
"v_mfma_f32_16x16x32_bf16 a[172:175], v[156:159], v[108:111], a[172:175]\n" \
"v_mfma_f32_16x16x32_bf16 a[168:171], v[144:147], v[104:107], a[168:171]\n" \
"v_mfma_f32_16x16x32_bf16 a[168:171], v[148:151], v[108:111], a[168:171]\n" \
"v_mfma_f32_16x16x32_bf16 a[164:167], v[136:139], v[104:107], a[164:167]\n" \
"v_mfma_f32_16x16x32_bf16 a[164:167], v[140:143], v[108:111], a[164:167]\n" \
"v_mfma_f32_16x16x32_bf16 a[160:163], v[128:131], v[104:107], a[160:163]\n" \
"v_mfma_f32_16x16x32_bf16 a[160:163], v[132:135], v[108:111], a[160:163]\n" \
"s_waitcnt lgkmcnt(0)\n" \
"v_mfma_f32_16x16x32_bf16 a[192:195], v[128:131], v[112:115], a[192:195]\n" \
"v_mfma_f32_16x16x32_bf16 a[192:195], v[132:135], v[116:119], a[192:195]\n" \
"v_mfma_f32_16x16x32_bf16 a[196:199], v[136:139], v[112:115], a[196:199]\n" \
"v_add_u32 v40, 0x10000, v40\n" \
"v_mfma_f32_16x16x32_bf16 a[196:199], v[140:143], v[116:119], a[196:199]\n" \
"v_mfma_f32_16x16x32_bf16 a[200:203], v[144:147], v[112:115], a[200:203]\n" \
"v_add_u32 v41, 0x10000, v41\n" \
"v_mfma_f32_16x16x32_bf16 a[200:203], v[148:151], v[116:119], a[200:203]\n" \
"v_mfma_f32_16x16x32_bf16 a[204:207], v[152:155], v[112:115], a[204:207]\n" \
"v_add_u32 v42, 0x18000, v42\n" \
"v_mfma_f32_16x16x32_bf16 a[204:207], v[156:159], v[116:119], a[204:207]\n" \
"v_mfma_f32_16x16x32_bf16 a[236:239], v[152:155], v[120:123], a[236:239]\n" \
"v_add_u32 v43, 0x18000, v43\n" \
"v_mfma_f32_16x16x32_bf16 a[236:239], v[156:159], v[124:127], a[236:239]\n" \
"v_mfma_f32_16x16x32_bf16 a[232:235], v[144:147], v[120:123], a[232:235]\n" \
"v_mfma_f32_16x16x32_bf16 a[232:235], v[148:151], v[124:127], a[232:235]\n" \
"v_mfma_f32_16x16x32_bf16 a[228:231], v[136:139], v[120:123], a[228:231]\n" \
"v_mfma_f32_16x16x32_bf16 a[228:231], v[140:143], v[124:127], a[228:231]\n" \
"v_mfma_f32_16x16x32_bf16 a[224:227], v[128:131], v[120:123], a[224:227]\n" \
"v_mfma_f32_16x16x32_bf16 a[224:227], v[132:135], v[124:127], a[224:227]\n" \
"s_waitcnt vmcnt(11) lgkmcnt(0)\n" \
"s_barrier\n" \
"v_mfma_f32_16x16x32_bf16 a[16:19], v[64:67], v[160:163], a[16:19]\n" \
"s_add_u32 m0, s74, 32768\n" \
"ds_read_b64_tr_b16 v[128:129], v42 offset:0\n" \
"v_mfma_f32_16x16x32_bf16 a[16:19], v[68:71], v[164:167], a[16:19]\n" \
"buffer_load_dwordx4 v32, s[36:39], s81 offen offset:0 lds\n" \
"v_mfma_f32_16x16x32_bf16 a[20:23], v[64:67], v[168:171], a[20:23]\n" \
"ds_read_b64_tr_b16 v[130:131], v42 offset:4096\n" \
"v_mfma_f32_16x16x32_bf16 a[20:23], v[68:71], v[172:175], a[20:23]\n" \
"v_mfma_f32_16x16x32_bf16 a[24:27], v[64:67], v[176:179], a[24:27]\n" \
"ds_read_b64_tr_b16 v[132:133], v42 offset:16384\n" \
"v_mfma_f32_16x16x32_bf16 a[24:27], v[68:71], v[180:183], a[24:27]\n" \
"buffer_load_dwordx4 v33, s[36:39], s81 offen offset:1024 lds\n" \
"v_mfma_f32_16x16x32_bf16 a[28:31], v[64:67], v[184:187], a[28:31]\n" \
"ds_read_b64_tr_b16 v[134:135], v42 offset:20480\n" \
"v_mfma_f32_16x16x32_bf16 a[28:31], v[68:71], v[188:191], a[28:31]\n" \
"v_mfma_f32_16x16x32_bf16 a[60:63], v[72:75], v[184:187], a[60:63]\n" \
"ds_read_b128 v[64:67], v40 offset:0\n" \
"v_mfma_f32_16x16x32_bf16 a[60:63], v[76:79], v[188:191], a[60:63]\n" \
"buffer_load_dwordx4 v34, s[36:39], s81 offen offset:2048 lds\n" \
"v_mfma_f32_16x16x32_bf16 a[56:59], v[72:75], v[176:179], a[56:59]\n" \
"ds_read_b64_tr_b16 v[136:137], v43 offset:0\n" \
"v_mfma_f32_16x16x32_bf16 a[56:59], v[76:79], v[180:183], a[56:59]\n" \
"v_mfma_f32_16x16x32_bf16 a[52:55], v[72:75], v[168:171], a[52:55]\n" \
"ds_read_b128 v[68:71], v41 offset:0\n" \
"v_mfma_f32_16x16x32_bf16 a[52:55], v[76:79], v[172:175], a[52:55]\n" \
"buffer_load_dwordx4 v35, s[36:39], s81 offen offset:3072 lds\n" \
"v_mfma_f32_16x16x32_bf16 a[48:51], v[72:75], v[160:163], a[48:51]\n" \
"ds_read_b64_tr_b16 v[138:139], v43 offset:4096\n" \
"v_mfma_f32_16x16x32_bf16 a[48:51], v[76:79], v[164:167], a[48:51]\n" \
"v_mfma_f32_16x16x32_bf16 a[80:83], v[80:83], v[160:163], a[80:83]\n" \
"s_add_u32 m0, s74, 36864\n" \
"ds_read_b128 v[72:75], v40 offset:2048\n" \
"v_mfma_f32_16x16x32_bf16 a[80:83], v[84:87], v[164:167], a[80:83]\n" \
"buffer_load_dwordx4 v32, s[36:39], s82 offen offset:0 lds\n" \
"v_mfma_f32_16x16x32_bf16 a[84:87], v[80:83], v[168:171], a[84:87]\n" \
"ds_read_b64_tr_b16 v[140:141], v43 offset:16384\n" \
"v_mfma_f32_16x16x32_bf16 a[84:87], v[84:87], v[172:175], a[84:87]\n" \
"v_mfma_f32_16x16x32_bf16 a[88:91], v[80:83], v[176:179], a[88:91]\n" \
"ds_read_b128 v[76:79], v41 offset:2048\n" \
"v_mfma_f32_16x16x32_bf16 a[88:91], v[84:87], v[180:183], a[88:91]\n" \
"buffer_load_dwordx4 v33, s[36:39], s82 offen offset:1024 lds\n" \
"v_mfma_f32_16x16x32_bf16 a[92:95], v[80:83], v[184:187], a[92:95]\n" \
"ds_read_b64_tr_b16 v[142:143], v43 offset:20480\n" \
"v_mfma_f32_16x16x32_bf16 a[92:95], v[84:87], v[188:191], a[92:95]\n" \
"v_mfma_f32_16x16x32_bf16 a[124:127], v[88:91], v[184:187], a[124:127]\n" \
"ds_read_b128 v[80:83], v40 offset:4096\n" \
"v_mfma_f32_16x16x32_bf16 a[124:127], v[92:95], v[188:191], a[124:127]\n" \
"buffer_load_dwordx4 v34, s[36:39], s82 offen offset:2048 lds\n" \
"v_mfma_f32_16x16x32_bf16 a[120:123], v[88:91], v[176:179], a[120:123]\n" \
"ds_read_b64_tr_b16 v[144:145], v42 offset:512\n" \
"v_mfma_f32_16x16x32_bf16 a[120:123], v[92:95], v[180:183], a[120:123]\n" \
"v_mfma_f32_16x16x32_bf16 a[116:119], v[88:91], v[168:171], a[116:119]\n" \
"ds_read_b128 v[84:87], v41 offset:4096\n" \
"v_mfma_f32_16x16x32_bf16 a[116:119], v[92:95], v[172:175], a[116:119]\n" \
"buffer_load_dwordx4 v35, s[36:39], s82 offen offset:3072 lds\n" \
"v_mfma_f32_16x16x32_bf16 a[112:115], v[88:91], v[160:163], a[112:115]\n" \
"s_mov_b64 s[64:65], s[12:13]\n" \
"s_add_u32 s68, s67, 0x300000\n" \
"ds_read_b64_tr_b16 v[146:147], v42 offset:4608\n" \
"v_mfma_f32_16x16x32_bf16 a[112:115], v[92:95], v[164:167], a[112:115]\n" \
"s_add_u32 s64, s64, s68\n" \
"s_addc_u32 s65, s65, 0\n" \
"v_mfma_f32_16x16x32_bf16 a[144:147], v[96:99], v[160:163], a[144:147]\n" \
"ds_read_b128 v[88:91], v40 offset:6144\n" \
"v_mfma_f32_16x16x32_bf16 a[144:147], v[100:103], v[164:167], a[144:147]\n" \
"global_load_dword v238, v234, s[64:65]\n" \
"v_mfma_f32_16x16x32_bf16 a[148:151], v[96:99], v[168:171], a[148:151]\n" \
"s_mov_b64 s[64:65], s[12:13]\n" \
"s_add_u32 s68, s67, 0x400000\n" \
"ds_read_b64_tr_b16 v[148:149], v42 offset:16896\n" \
"v_mfma_f32_16x16x32_bf16 a[148:151], v[100:103], v[172:175], a[148:151]\n" \
"s_add_u32 s64, s64, s68\n" \
"s_addc_u32 s65, s65, 0\n" \
"v_mfma_f32_16x16x32_bf16 a[152:155], v[96:99], v[176:179], a[152:155]\n" \
"ds_read_b128 v[92:95], v41 offset:6144\n" \
"v_mfma_f32_16x16x32_bf16 a[152:155], v[100:103], v[180:183], a[152:155]\n" \
"global_load_dword v238, v234, s[64:65]\n" \
"v_mfma_f32_16x16x32_bf16 a[156:159], v[96:99], v[184:187], a[156:159]\n" \
"s_mov_b64 s[64:65], s[12:13]\n" \
"s_add_u32 s68, s67, 0x600000\n" \
"ds_read_b64_tr_b16 v[150:151], v42 offset:20992\n" \
"v_mfma_f32_16x16x32_bf16 a[156:159], v[100:103], v[188:191], a[156:159]\n" \
"s_add_u32 s64, s64, s68\n" \
"s_addc_u32 s65, s65, 0\n" \
"v_mfma_f32_16x16x32_bf16 a[188:191], v[104:107], v[184:187], a[188:191]\n" \
"ds_read_b128 v[96:99], v40 offset:8192\n" \
"v_mfma_f32_16x16x32_bf16 a[188:191], v[108:111], v[188:191], a[188:191]\n" \
"global_load_dword v238, v234, s[64:65]\n" \
"v_mfma_f32_16x16x32_bf16 a[184:187], v[104:107], v[176:179], a[184:187]\n" \
"ds_read_b64_tr_b16 v[152:153], v43 offset:512\n" \
"v_mfma_f32_16x16x32_bf16 a[184:187], v[108:111], v[180:183], a[184:187]\n" \
"v_mfma_f32_16x16x32_bf16 a[180:183], v[104:107], v[168:171], a[180:183]\n" \
"ds_read_b128 v[100:103], v41 offset:8192\n" \
"v_mfma_f32_16x16x32_bf16 a[180:183], v[108:111], v[172:175], a[180:183]\n" \
"v_mfma_f32_16x16x32_bf16 a[176:179], v[104:107], v[160:163], a[176:179]\n" \
"ds_read_b64_tr_b16 v[154:155], v43 offset:4608\n" \
"v_mfma_f32_16x16x32_bf16 a[176:179], v[108:111], v[164:167], a[176:179]\n" \
"v_mfma_f32_16x16x32_bf16 a[208:211], v[112:115], v[160:163], a[208:211]\n" \
"ds_read_b128 v[104:107], v40 offset:10240\n" \
"v_mfma_f32_16x16x32_bf16 a[208:211], v[116:119], v[164:167], a[208:211]\n" \
"v_mfma_f32_16x16x32_bf16 a[212:215], v[112:115], v[168:171], a[212:215]\n" \
"ds_read_b64_tr_b16 v[156:157], v43 offset:16896\n" \
"v_mfma_f32_16x16x32_bf16 a[212:215], v[116:119], v[172:175], a[212:215]\n" \
"v_mfma_f32_16x16x32_bf16 a[216:219], v[112:115], v[176:179], a[216:219]\n" \
"ds_read_b128 v[108:111], v41 offset:10240\n" \
"v_mfma_f32_16x16x32_bf16 a[216:219], v[116:119], v[180:183], a[216:219]\n" \
"v_mfma_f32_16x16x32_bf16 a[220:223], v[112:115], v[184:187], a[220:223]\n" \
"ds_read_b64_tr_b16 v[158:159], v43 offset:20992\n" \
"v_mfma_f32_16x16x32_bf16 a[220:223], v[116:119], v[188:191], a[220:223]\n" \
"v_mfma_f32_16x16x32_bf16 a[252:255], v[120:123], v[184:187], a[252:255]\n" \
"ds_read_b128 v[112:115], v40 offset:12288\n" \
"v_mfma_f32_16x16x32_bf16 a[252:255], v[124:127], v[188:191], a[252:255]\n" \
"v_mfma_f32_16x16x32_bf16 a[248:251], v[120:123], v[176:179], a[248:251]\n" \
"v_mfma_f32_16x16x32_bf16 a[248:251], v[124:127], v[180:183], a[248:251]\n" \
"v_mfma_f32_16x16x32_bf16 a[244:247], v[120:123], v[168:171], a[244:247]\n" \
"ds_read_b128 v[116:119], v41 offset:12288\n" \
"v_mfma_f32_16x16x32_bf16 a[244:247], v[124:127], v[172:175], a[244:247]\n" \
"v_mfma_f32_16x16x32_bf16 a[240:243], v[120:123], v[160:163], a[240:243]\n" \
"v_mfma_f32_16x16x32_bf16 a[240:243], v[124:127], v[164:167], a[240:243]\n" \
"s_waitcnt lgkmcnt(1)\n" \
"v_mfma_f32_16x16x32_bf16 a[0:3], v[128:131], v[64:67], a[0:3]\n" \
"ds_read_b128 v[120:123], v40 offset:14336\n" \
"v_mfma_f32_16x16x32_bf16 a[0:3], v[132:135], v[68:71], a[0:3]\n" \
"v_mfma_f32_16x16x32_bf16 a[4:7], v[136:139], v[64:67], a[4:7]\n" \
"s_add_u32 m0, s79, 0\n" \
"ds_read_b128 v[124:127], v41 offset:14336\n" \
"v_mfma_f32_16x16x32_bf16 a[4:7], v[140:143], v[68:71], a[4:7]\n" \
"buffer_load_dwordx4 v228, s[52:55], s78 offen offset:0 lds\n" \
"v_mfma_f32_16x16x32_bf16 a[8:11], v[144:147], v[64:67], a[8:11]\n" \
"ds_read_b64_tr_b16 v[160:161], v42 offset:1024\n" \
"v_mfma_f32_16x16x32_bf16 a[8:11], v[148:151], v[68:71], a[8:11]\n" \
"v_mfma_f32_16x16x32_bf16 a[12:15], v[152:155], v[64:67], a[12:15]\n" \
"ds_read_b64_tr_b16 v[162:163], v42 offset:5120\n" \
"v_mfma_f32_16x16x32_bf16 a[12:15], v[156:159], v[68:71], a[12:15]\n" \
"buffer_load_dwordx4 v229, s[52:55], s78 offen offset:1024 lds\n" \
"v_mfma_f32_16x16x32_bf16 a[44:47], v[152:155], v[72:75], a[44:47]\n" \
"ds_read_b64_tr_b16 v[164:165], v42 offset:17408\n" \
"v_mfma_f32_16x16x32_bf16 a[44:47], v[156:159], v[76:79], a[44:47]\n" \
"v_mfma_f32_16x16x32_bf16 a[40:43], v[144:147], v[72:75], a[40:43]\n" \
"s_xor_b32 s78, s78, 0x180000\n" \
"ds_read_b64_tr_b16 v[166:167], v42 offset:21504\n" \
"v_mfma_f32_16x16x32_bf16 a[40:43], v[148:151], v[76:79], a[40:43]\n" \
"buffer_load_dwordx4 v228, s[52:55], s89 offen offset:2048 lds\n" \
"v_mfma_f32_16x16x32_bf16 a[36:39], v[136:139], v[72:75], a[36:39]\n" \
"ds_read_b64_tr_b16 v[168:169], v43 offset:1024\n" \
"Lpg1644_%=:\n" \
"Lst0x2_%=:\n" \
"s_cbranch_execz Lpg3811_%=\n" \
"v_mfma_f32_16x16x32_bf16 a[36:39], v[140:143], v[76:79], a[36:39]\n" \
"v_mfma_f32_16x16x32_bf16 a[32:35], v[128:131], v[72:75], a[32:35]\n" \
"ds_read_b64_tr_b16 v[170:171], v43 offset:5120\n" \
"v_mfma_f32_16x16x32_bf16 a[32:35], v[132:135], v[76:79], a[32:35]\n" \
"buffer_load_dwordx4 v229, s[52:55], s89 offen offset:3072 lds\n" \
"v_mfma_f32_16x16x32_bf16 a[64:67], v[128:131], v[80:83], a[64:67]\n" \
"ds_read_b64_tr_b16 v[172:173], v43 offset:17408\n" \
"v_mfma_f32_16x16x32_bf16 a[64:67], v[132:135], v[84:87], a[64:67]\n" \
"s_xor_b32 s89, s89, 0x180000\n" \
"v_mfma_f32_16x16x32_bf16 a[68:71], v[136:139], v[80:83], a[68:71]\n" \
"s_add_u32 m0, s79, 16384\n" \
"ds_read_b64_tr_b16 v[174:175], v43 offset:21504\n" \
"v_mfma_f32_16x16x32_bf16 a[68:71], v[140:143], v[84:87], a[68:71]\n" \
"buffer_load_dwordx4 v228, s[52:55], s78 offen offset:0 lds\n" \
"v_mfma_f32_16x16x32_bf16 a[72:75], v[144:147], v[80:83], a[72:75]\n" \
"ds_read_b64_tr_b16 v[176:177], v42 offset:1536\n" \
"v_mfma_f32_16x16x32_bf16 a[72:75], v[148:151], v[84:87], a[72:75]\n" \
"v_mfma_f32_16x16x32_bf16 a[76:79], v[152:155], v[80:83], a[76:79]\n" \
"ds_read_b64_tr_b16 v[178:179], v42 offset:5632\n" \
"v_mfma_f32_16x16x32_bf16 a[76:79], v[156:159], v[84:87], a[76:79]\n" \
"buffer_load_dwordx4 v229, s[52:55], s78 offen offset:1024 lds\n" \
"v_mfma_f32_16x16x32_bf16 a[108:111], v[152:155], v[88:91], a[108:111]\n" \
"ds_read_b64_tr_b16 v[180:181], v42 offset:17920\n" \
"v_mfma_f32_16x16x32_bf16 a[108:111], v[156:159], v[92:95], a[108:111]\n" \
"v_mfma_f32_16x16x32_bf16 a[104:107], v[144:147], v[88:91], a[104:107]\n" \
"s_xor_b32 s78, s78, 0x400000\n" \
"ds_read_b64_tr_b16 v[182:183], v42 offset:22016\n" \
"v_mfma_f32_16x16x32_bf16 a[104:107], v[148:151], v[92:95], a[104:107]\n" \
"buffer_load_dwordx4 v228, s[52:55], s89 offen offset:2048 lds\n" \
"v_mfma_f32_16x16x32_bf16 a[100:103], v[136:139], v[88:91], a[100:103]\n" \
"ds_read_b64_tr_b16 v[184:185], v43 offset:1536\n" \
"v_mfma_f32_16x16x32_bf16 a[100:103], v[140:143], v[92:95], a[100:103]\n" \
"v_mfma_f32_16x16x32_bf16 a[96:99], v[128:131], v[88:91], a[96:99]\n" \
"ds_read_b64_tr_b16 v[186:187], v43 offset:5632\n" \
"v_mfma_f32_16x16x32_bf16 a[96:99], v[132:135], v[92:95], a[96:99]\n" \
"buffer_load_dwordx4 v229, s[52:55], s89 offen offset:3072 lds\n" \
"v_mfma_f32_16x16x32_bf16 a[128:131], v[128:131], v[96:99], a[128:131]\n" \
"ds_read_b64_tr_b16 v[188:189], v43 offset:17920\n" \
"v_mfma_f32_16x16x32_bf16 a[128:131], v[132:135], v[100:103], a[128:131]\n" \
"v_mfma_f32_16x16x32_bf16 a[132:135], v[136:139], v[96:99], a[132:135]\n" \
"s_xor_b32 s89, s89, 0x400000\n" \
"ds_read_b64_tr_b16 v[190:191], v43 offset:22016\n" \
"v_mfma_f32_16x16x32_bf16 a[132:135], v[140:143], v[100:103], a[132:135]\n" \
"v_mfma_f32_16x16x32_bf16 a[136:139], v[144:147], v[96:99], a[136:139]\n" \
"v_mfma_f32_16x16x32_bf16 a[136:139], v[148:151], v[100:103], a[136:139]\n" \
"v_mfma_f32_16x16x32_bf16 a[140:143], v[152:155], v[96:99], a[140:143]\n" \
"v_mfma_f32_16x16x32_bf16 a[140:143], v[156:159], v[100:103], a[140:143]\n" \
"v_mfma_f32_16x16x32_bf16 a[172:175], v[152:155], v[104:107], a[172:175]\n" \
"v_mfma_f32_16x16x32_bf16 a[172:175], v[156:159], v[108:111], a[172:175]\n" \
"v_mfma_f32_16x16x32_bf16 a[168:171], v[144:147], v[104:107], a[168:171]\n" \
"v_mfma_f32_16x16x32_bf16 a[168:171], v[148:151], v[108:111], a[168:171]\n" \
"v_mfma_f32_16x16x32_bf16 a[164:167], v[136:139], v[104:107], a[164:167]\n" \
"v_mfma_f32_16x16x32_bf16 a[164:167], v[140:143], v[108:111], a[164:167]\n" \
"v_mfma_f32_16x16x32_bf16 a[160:163], v[128:131], v[104:107], a[160:163]\n" \
"v_mfma_f32_16x16x32_bf16 a[160:163], v[132:135], v[108:111], a[160:163]\n" \
"s_waitcnt lgkmcnt(0)\n" \
"v_mfma_f32_16x16x32_bf16 a[192:195], v[128:131], v[112:115], a[192:195]\n" \
"v_mfma_f32_16x16x32_bf16 a[192:195], v[132:135], v[116:119], a[192:195]\n" \
"v_mfma_f32_16x16x32_bf16 a[196:199], v[136:139], v[112:115], a[196:199]\n" \
"v_add_u32 v40, 0xffff8000, v40\n" \
"v_mfma_f32_16x16x32_bf16 a[196:199], v[140:143], v[116:119], a[196:199]\n" \
"v_mfma_f32_16x16x32_bf16 a[200:203], v[144:147], v[112:115], a[200:203]\n" \
"v_add_u32 v41, 0xffff8000, v41\n" \
"v_mfma_f32_16x16x32_bf16 a[200:203], v[148:151], v[116:119], a[200:203]\n" \
"v_mfma_f32_16x16x32_bf16 a[204:207], v[152:155], v[112:115], a[204:207]\n" \
"v_add_u32 v42, 0xffff8000, v42\n" \
"v_mfma_f32_16x16x32_bf16 a[204:207], v[156:159], v[116:119], a[204:207]\n" \
"v_mfma_f32_16x16x32_bf16 a[236:239], v[152:155], v[120:123], a[236:239]\n" \
"v_add_u32 v43, 0xffff8000, v43\n" \
"v_mfma_f32_16x16x32_bf16 a[236:239], v[156:159], v[124:127], a[236:239]\n" \
"v_mfma_f32_16x16x32_bf16 a[232:235], v[144:147], v[120:123], a[232:235]\n" \
"v_mfma_f32_16x16x32_bf16 a[232:235], v[148:151], v[124:127], a[232:235]\n" \
"v_mfma_f32_16x16x32_bf16 a[228:231], v[136:139], v[120:123], a[228:231]\n" \
"v_mfma_f32_16x16x32_bf16 a[228:231], v[140:143], v[124:127], a[228:231]\n" \
"v_mfma_f32_16x16x32_bf16 a[224:227], v[128:131], v[120:123], a[224:227]\n" \
"v_mfma_f32_16x16x32_bf16 a[224:227], v[132:135], v[124:127], a[224:227]\n" \
"s_waitcnt vmcnt(11) lgkmcnt(0)\n" \
"s_barrier\n" \
"v_mfma_f32_16x16x32_bf16 a[16:19], v[64:67], v[160:163], a[16:19]\n" \
"s_add_u32 m0, s74, 131072\n" \
"ds_read_b64_tr_b16 v[128:129], v42 offset:0\n" \
"v_mfma_f32_16x16x32_bf16 a[16:19], v[68:71], v[164:167], a[16:19]\n" \
"buffer_load_dwordx4 v224, s[48:51], s75 offen offset:0 lds\n" \
"v_mfma_f32_16x16x32_bf16 a[20:23], v[64:67], v[168:171], a[20:23]\n" \
"ds_read_b64_tr_b16 v[130:131], v42 offset:4096\n" \
"v_mfma_f32_16x16x32_bf16 a[20:23], v[68:71], v[172:175], a[20:23]\n" \
"v_mfma_f32_16x16x32_bf16 a[24:27], v[64:67], v[176:179], a[24:27]\n" \
"ds_read_b64_tr_b16 v[132:133], v42 offset:16384\n" \
"v_mfma_f32_16x16x32_bf16 a[24:27], v[68:71], v[180:183], a[24:27]\n" \
"buffer_load_dwordx4 v225, s[48:51], s75 offen offset:1024 lds\n" \
"v_mfma_f32_16x16x32_bf16 a[28:31], v[64:67], v[184:187], a[28:31]\n" \
"ds_read_b64_tr_b16 v[134:135], v42 offset:20480\n" \
"v_mfma_f32_16x16x32_bf16 a[28:31], v[68:71], v[188:191], a[28:31]\n" \
"v_mfma_f32_16x16x32_bf16 a[60:63], v[72:75], v[184:187], a[60:63]\n" \
"ds_read_b128 v[64:67], v40 offset:0\n" \
"v_mfma_f32_16x16x32_bf16 a[60:63], v[76:79], v[188:191], a[60:63]\n" \
"buffer_load_dwordx4 v226, s[48:51], s75 offen offset:2048 lds\n" \
"v_mfma_f32_16x16x32_bf16 a[56:59], v[72:75], v[176:179], a[56:59]\n" \
"ds_read_b64_tr_b16 v[136:137], v43 offset:0\n" \
"v_mfma_f32_16x16x32_bf16 a[56:59], v[76:79], v[180:183], a[56:59]\n" \
"v_mfma_f32_16x16x32_bf16 a[52:55], v[72:75], v[168:171], a[52:55]\n" \
"ds_read_b128 v[68:71], v41 offset:0\n" \
"v_mfma_f32_16x16x32_bf16 a[52:55], v[76:79], v[172:175], a[52:55]\n" \
"buffer_load_dwordx4 v227, s[48:51], s75 offen offset:3072 lds\n" \
"v_mfma_f32_16x16x32_bf16 a[48:51], v[72:75], v[160:163], a[48:51]\n" \
"ds_read_b64_tr_b16 v[138:139], v43 offset:4096\n" \
"v_mfma_f32_16x16x32_bf16 a[48:51], v[76:79], v[164:167], a[48:51]\n" \
"s_xor_b32 s75, s75, 0x180\n" \
"v_mfma_f32_16x16x32_bf16 a[80:83], v[80:83], v[160:163], a[80:83]\n" \
"s_add_u32 m0, s74, 135168\n" \
"ds_read_b128 v[72:75], v40 offset:2048\n" \
"v_mfma_f32_16x16x32_bf16 a[80:83], v[84:87], v[164:167], a[80:83]\n" \
"buffer_load_dwordx4 v224, s[48:51], s77 offen offset:0 lds\n" \
"v_mfma_f32_16x16x32_bf16 a[84:87], v[80:83], v[168:171], a[84:87]\n" \
"ds_read_b64_tr_b16 v[140:141], v43 offset:16384\n" \
"v_mfma_f32_16x16x32_bf16 a[84:87], v[84:87], v[172:175], a[84:87]\n" \
"v_mfma_f32_16x16x32_bf16 a[88:91], v[80:83], v[176:179], a[88:91]\n" \
"ds_read_b128 v[76:79], v41 offset:2048\n" \
"v_mfma_f32_16x16x32_bf16 a[88:91], v[84:87], v[180:183], a[88:91]\n" \
"buffer_load_dwordx4 v225, s[48:51], s77 offen offset:1024 lds\n" \
"v_mfma_f32_16x16x32_bf16 a[92:95], v[80:83], v[184:187], a[92:95]\n" \
"ds_read_b64_tr_b16 v[142:143], v43 offset:20480\n" \
"v_mfma_f32_16x16x32_bf16 a[92:95], v[84:87], v[188:191], a[92:95]\n" \
"v_mfma_f32_16x16x32_bf16 a[124:127], v[88:91], v[184:187], a[124:127]\n" \
"ds_read_b128 v[80:83], v40 offset:4096\n" \
"v_mfma_f32_16x16x32_bf16 a[124:127], v[92:95], v[188:191], a[124:127]\n" \
"buffer_load_dwordx4 v226, s[48:51], s77 offen offset:2048 lds\n" \
"v_mfma_f32_16x16x32_bf16 a[120:123], v[88:91], v[176:179], a[120:123]\n" \
"ds_read_b64_tr_b16 v[144:145], v42 offset:512\n" \
"v_mfma_f32_16x16x32_bf16 a[120:123], v[92:95], v[180:183], a[120:123]\n" \
"v_mfma_f32_16x16x32_bf16 a[116:119], v[88:91], v[168:171], a[116:119]\n" \
"ds_read_b128 v[84:87], v41 offset:4096\n" \
"v_mfma_f32_16x16x32_bf16 a[116:119], v[92:95], v[172:175], a[116:119]\n" \
"buffer_load_dwordx4 v227, s[48:51], s77 offen offset:3072 lds\n" \
"v_mfma_f32_16x16x32_bf16 a[112:115], v[88:91], v[160:163], a[112:115]\n" \
"s_mov_b64 s[64:65], s[12:13]\n" \
"s_add_u32 s68, s67, 0x700000\n" \
"ds_read_b64_tr_b16 v[146:147], v42 offset:4608\n" \
"v_mfma_f32_16x16x32_bf16 a[112:115], v[92:95], v[164:167], a[112:115]\n" \
"s_add_u32 s64, s64, s68\n" \
"s_addc_u32 s65, s65, 0\n" \
"v_mfma_f32_16x16x32_bf16 a[144:147], v[96:99], v[160:163], a[144:147]\n" \
"s_xor_b32 s77, s77, 0x180\n" \
"ds_read_b128 v[88:91], v40 offset:6144\n" \
"v_mfma_f32_16x16x32_bf16 a[144:147], v[100:103], v[164:167], a[144:147]\n" \
"global_load_dword v238, v234, s[64:65]\n" \
"v_mfma_f32_16x16x32_bf16 a[148:151], v[96:99], v[168:171], a[148:151]\n" \
"ds_read_b64_tr_b16 v[148:149], v42 offset:16896\n" \
"v_mfma_f32_16x16x32_bf16 a[148:151], v[100:103], v[172:175], a[148:151]\n" \
"v_mfma_f32_16x16x32_bf16 a[152:155], v[96:99], v[176:179], a[152:155]\n" \
"s_lshr_b32 s68, s66, 2\n" \
"s_and_b32 s68, s68, 0xffff\n" \
"s_add_u32 s68, s68, 65585152\n" \
"ds_read_b128 v[92:95], v41 offset:6144\n" \
"v_mfma_f32_16x16x32_bf16 a[152:155], v[100:103], v[180:183], a[152:155]\n" \
"buffer_load_dword v238, v242, s[56:59], s68 offen sc1\n" \
"v_mfma_f32_16x16x32_bf16 a[156:159], v[96:99], v[184:187], a[156:159]\n" \
"ds_read_b64_tr_b16 v[150:151], v42 offset:20992\n" \
"v_mfma_f32_16x16x32_bf16 a[156:159], v[100:103], v[188:191], a[156:159]\n" \
"v_mfma_f32_16x16x32_bf16 a[188:191], v[104:107], v[184:187], a[188:191]\n" \
"ds_read_b128 v[96:99], v40 offset:8192\n" \
"v_mfma_f32_16x16x32_bf16 a[188:191], v[108:111], v[188:191], a[188:191]\n" \
"v_mfma_f32_16x16x32_bf16 a[184:187], v[104:107], v[176:179], a[184:187]\n" \
"ds_read_b64_tr_b16 v[152:153], v43 offset:512\n" \
"v_mfma_f32_16x16x32_bf16 a[184:187], v[108:111], v[180:183], a[184:187]\n" \
"v_mfma_f32_16x16x32_bf16 a[180:183], v[104:107], v[168:171], a[180:183]\n" \
"ds_read_b128 v[100:103], v41 offset:8192\n" \
"v_mfma_f32_16x16x32_bf16 a[180:183], v[108:111], v[172:175], a[180:183]\n" \
"v_mfma_f32_16x16x32_bf16 a[176:179], v[104:107], v[160:163], a[176:179]\n" \
"ds_read_b64_tr_b16 v[154:155], v43 offset:4608\n" \
"v_mfma_f32_16x16x32_bf16 a[176:179], v[108:111], v[164:167], a[176:179]\n" \
"v_mfma_f32_16x16x32_bf16 a[208:211], v[112:115], v[160:163], a[208:211]\n" \
"ds_read_b128 v[104:107], v40 offset:10240\n" \
"v_mfma_f32_16x16x32_bf16 a[208:211], v[116:119], v[164:167], a[208:211]\n" \
"v_mfma_f32_16x16x32_bf16 a[212:215], v[112:115], v[168:171], a[212:215]\n" \
"ds_read_b64_tr_b16 v[156:157], v43 offset:16896\n" \
"v_mfma_f32_16x16x32_bf16 a[212:215], v[116:119], v[172:175], a[212:215]\n" \
"v_mfma_f32_16x16x32_bf16 a[216:219], v[112:115], v[176:179], a[216:219]\n" \
"ds_read_b128 v[108:111], v41 offset:10240\n" \
"v_mfma_f32_16x16x32_bf16 a[216:219], v[116:119], v[180:183], a[216:219]\n" \
"v_mfma_f32_16x16x32_bf16 a[220:223], v[112:115], v[184:187], a[220:223]\n" \
"ds_read_b64_tr_b16 v[158:159], v43 offset:20992\n" \
"v_mfma_f32_16x16x32_bf16 a[220:223], v[116:119], v[188:191], a[220:223]\n" \
"v_mfma_f32_16x16x32_bf16 a[252:255], v[120:123], v[184:187], a[252:255]\n" \
"ds_read_b128 v[112:115], v40 offset:12288\n" \
"v_mfma_f32_16x16x32_bf16 a[252:255], v[124:127], v[188:191], a[252:255]\n" \
"v_mfma_f32_16x16x32_bf16 a[248:251], v[120:123], v[176:179], a[248:251]\n" \
"v_mfma_f32_16x16x32_bf16 a[248:251], v[124:127], v[180:183], a[248:251]\n" \
"v_mfma_f32_16x16x32_bf16 a[244:247], v[120:123], v[168:171], a[244:247]\n" \
"ds_read_b128 v[116:119], v41 offset:12288\n" \
"v_mfma_f32_16x16x32_bf16 a[244:247], v[124:127], v[172:175], a[244:247]\n" \
"v_mfma_f32_16x16x32_bf16 a[240:243], v[120:123], v[160:163], a[240:243]\n" \
"v_mfma_f32_16x16x32_bf16 a[240:243], v[124:127], v[164:167], a[240:243]\n" \
"s_waitcnt lgkmcnt(1)\n" \
"v_mfma_f32_16x16x32_bf16 a[0:3], v[128:131], v[64:67], a[0:3]\n" \
"ds_read_b128 v[120:123], v40 offset:14336\n" \
"v_mfma_f32_16x16x32_bf16 a[0:3], v[132:135], v[68:71], a[0:3]\n" \
"v_mfma_f32_16x16x32_bf16 a[4:7], v[136:139], v[64:67], a[4:7]\n" \
"s_add_u32 m0, s74, 65536\n" \
"ds_read_b128 v[124:127], v41 offset:14336\n" \
"v_mfma_f32_16x16x32_bf16 a[4:7], v[140:143], v[68:71], a[4:7]\n" \
"buffer_load_dwordx4 v224, s[48:51], s75 offen offset:0 lds\n" \
"v_mfma_f32_16x16x32_bf16 a[8:11], v[144:147], v[64:67], a[8:11]\n" \
"ds_read_b64_tr_b16 v[160:161], v42 offset:1024\n" \
"v_mfma_f32_16x16x32_bf16 a[8:11], v[148:151], v[68:71], a[8:11]\n" \
"v_mfma_f32_16x16x32_bf16 a[12:15], v[152:155], v[64:67], a[12:15]\n" \
"ds_read_b64_tr_b16 v[162:163], v42 offset:5120\n" \
"v_mfma_f32_16x16x32_bf16 a[12:15], v[156:159], v[68:71], a[12:15]\n" \
"buffer_load_dwordx4 v225, s[48:51], s75 offen offset:1024 lds\n" \
"v_mfma_f32_16x16x32_bf16 a[44:47], v[152:155], v[72:75], a[44:47]\n" \
"ds_read_b64_tr_b16 v[164:165], v42 offset:17408\n" \
"v_mfma_f32_16x16x32_bf16 a[44:47], v[156:159], v[76:79], a[44:47]\n" \
"v_mfma_f32_16x16x32_bf16 a[40:43], v[144:147], v[72:75], a[40:43]\n" \
"ds_read_b64_tr_b16 v[166:167], v42 offset:21504\n" \
"v_mfma_f32_16x16x32_bf16 a[40:43], v[148:151], v[76:79], a[40:43]\n" \
"buffer_load_dwordx4 v226, s[48:51], s75 offen offset:2048 lds\n" \
"v_mfma_f32_16x16x32_bf16 a[36:39], v[136:139], v[72:75], a[36:39]\n" \
"ds_read_b64_tr_b16 v[168:169], v43 offset:1024\n" \
"v_mfma_f32_16x16x32_bf16 a[36:39], v[140:143], v[76:79], a[36:39]\n" \
"v_mfma_f32_16x16x32_bf16 a[32:35], v[128:131], v[72:75], a[32:35]\n" \
"ds_read_b64_tr_b16 v[170:171], v43 offset:5120\n" \
"v_mfma_f32_16x16x32_bf16 a[32:35], v[132:135], v[76:79], a[32:35]\n" \
"buffer_load_dwordx4 v227, s[48:51], s75 offen offset:3072 lds\n" \
"v_mfma_f32_16x16x32_bf16 a[64:67], v[128:131], v[80:83], a[64:67]\n" \
"ds_read_b64_tr_b16 v[172:173], v43 offset:17408\n" \
"v_mfma_f32_16x16x32_bf16 a[64:67], v[132:135], v[84:87], a[64:67]\n" \
"s_xor_b32 s75, s75, 0x400\n" \
"v_mfma_f32_16x16x32_bf16 a[68:71], v[136:139], v[80:83], a[68:71]\n" \
"s_add_u32 m0, s74, 69632\n" \
"ds_read_b64_tr_b16 v[174:175], v43 offset:21504\n" \
"v_mfma_f32_16x16x32_bf16 a[68:71], v[140:143], v[84:87], a[68:71]\n" \
"buffer_load_dwordx4 v224, s[48:51], s77 offen offset:0 lds\n" \
"v_mfma_f32_16x16x32_bf16 a[72:75], v[144:147], v[80:83], a[72:75]\n" \
"ds_read_b64_tr_b16 v[176:177], v42 offset:1536\n" \
"v_mfma_f32_16x16x32_bf16 a[72:75], v[148:151], v[84:87], a[72:75]\n" \
"v_mfma_f32_16x16x32_bf16 a[76:79], v[152:155], v[80:83], a[76:79]\n" \
"ds_read_b64_tr_b16 v[178:179], v42 offset:5632\n" \
"v_mfma_f32_16x16x32_bf16 a[76:79], v[156:159], v[84:87], a[76:79]\n" \
"buffer_load_dwordx4 v225, s[48:51], s77 offen offset:1024 lds\n" \
"v_mfma_f32_16x16x32_bf16 a[108:111], v[152:155], v[88:91], a[108:111]\n" \
"ds_read_b64_tr_b16 v[180:181], v42 offset:17920\n" \
"v_mfma_f32_16x16x32_bf16 a[108:111], v[156:159], v[92:95], a[108:111]\n" \
"v_mfma_f32_16x16x32_bf16 a[104:107], v[144:147], v[88:91], a[104:107]\n" \
"ds_read_b64_tr_b16 v[182:183], v42 offset:22016\n" \
"v_mfma_f32_16x16x32_bf16 a[104:107], v[148:151], v[92:95], a[104:107]\n" \
"buffer_load_dwordx4 v226, s[48:51], s77 offen offset:2048 lds\n" \
"v_mfma_f32_16x16x32_bf16 a[100:103], v[136:139], v[88:91], a[100:103]\n" \
"ds_read_b64_tr_b16 v[184:185], v43 offset:1536\n" \
"v_mfma_f32_16x16x32_bf16 a[100:103], v[140:143], v[92:95], a[100:103]\n" \
"v_mfma_f32_16x16x32_bf16 a[96:99], v[128:131], v[88:91], a[96:99]\n" \
"ds_read_b64_tr_b16 v[186:187], v43 offset:5632\n" \
"v_mfma_f32_16x16x32_bf16 a[96:99], v[132:135], v[92:95], a[96:99]\n" \
"buffer_load_dwordx4 v227, s[48:51], s77 offen offset:3072 lds\n" \
"v_mfma_f32_16x16x32_bf16 a[128:131], v[128:131], v[96:99], a[128:131]\n" \
"ds_read_b64_tr_b16 v[188:189], v43 offset:17920\n" \
"v_mfma_f32_16x16x32_bf16 a[128:131], v[132:135], v[100:103], a[128:131]\n" \
"v_mfma_f32_16x16x32_bf16 a[132:135], v[136:139], v[96:99], a[132:135]\n" \
"s_xor_b32 s77, s77, 0x400\n" \
"ds_read_b64_tr_b16 v[190:191], v43 offset:22016\n" \
"v_mfma_f32_16x16x32_bf16 a[132:135], v[140:143], v[100:103], a[132:135]\n" \
"v_mfma_f32_16x16x32_bf16 a[136:139], v[144:147], v[96:99], a[136:139]\n" \
"v_mfma_f32_16x16x32_bf16 a[136:139], v[148:151], v[100:103], a[136:139]\n" \
"v_mfma_f32_16x16x32_bf16 a[140:143], v[152:155], v[96:99], a[140:143]\n" \
"v_mfma_f32_16x16x32_bf16 a[140:143], v[156:159], v[100:103], a[140:143]\n" \
"v_mfma_f32_16x16x32_bf16 a[172:175], v[152:155], v[104:107], a[172:175]\n" \
"v_mfma_f32_16x16x32_bf16 a[172:175], v[156:159], v[108:111], a[172:175]\n" \
"v_mfma_f32_16x16x32_bf16 a[168:171], v[144:147], v[104:107], a[168:171]\n" \
"v_mfma_f32_16x16x32_bf16 a[168:171], v[148:151], v[108:111], a[168:171]\n" \
"v_mfma_f32_16x16x32_bf16 a[164:167], v[136:139], v[104:107], a[164:167]\n" \
"v_mfma_f32_16x16x32_bf16 a[164:167], v[140:143], v[108:111], a[164:167]\n" \
"v_mfma_f32_16x16x32_bf16 a[160:163], v[128:131], v[104:107], a[160:163]\n" \
"v_mfma_f32_16x16x32_bf16 a[160:163], v[132:135], v[108:111], a[160:163]\n" \
"s_waitcnt lgkmcnt(0)\n" \
"v_mfma_f32_16x16x32_bf16 a[192:195], v[128:131], v[112:115], a[192:195]\n" \
"v_mfma_f32_16x16x32_bf16 a[192:195], v[132:135], v[116:119], a[192:195]\n" \
"v_mfma_f32_16x16x32_bf16 a[196:199], v[136:139], v[112:115], a[196:199]\n" \
"v_mfma_f32_16x16x32_bf16 a[196:199], v[140:143], v[116:119], a[196:199]\n" \
"v_mfma_f32_16x16x32_bf16 a[200:203], v[144:147], v[112:115], a[200:203]\n" \
"v_mfma_f32_16x16x32_bf16 a[200:203], v[148:151], v[116:119], a[200:203]\n" \
"v_mfma_f32_16x16x32_bf16 a[204:207], v[152:155], v[112:115], a[204:207]\n" \
"v_mfma_f32_16x16x32_bf16 a[204:207], v[156:159], v[116:119], a[204:207]\n" \
"v_mfma_f32_16x16x32_bf16 a[236:239], v[152:155], v[120:123], a[236:239]\n" \
"v_mfma_f32_16x16x32_bf16 a[236:239], v[156:159], v[124:127], a[236:239]\n" \
"v_mfma_f32_16x16x32_bf16 a[232:235], v[144:147], v[120:123], a[232:235]\n" \
"v_mfma_f32_16x16x32_bf16 a[232:235], v[148:151], v[124:127], a[232:235]\n" \
"v_mfma_f32_16x16x32_bf16 a[228:231], v[136:139], v[120:123], a[228:231]\n" \
"v_mfma_f32_16x16x32_bf16 a[228:231], v[140:143], v[124:127], a[228:231]\n" \
"v_mfma_f32_16x16x32_bf16 a[224:227], v[128:131], v[120:123], a[224:227]\n" \
"v_mfma_f32_16x16x32_bf16 a[224:227], v[132:135], v[124:127], a[224:227]\n" \
"s_waitcnt vmcnt(10) lgkmcnt(0)\n" \
"s_barrier\n" \
"v_mfma_f32_16x16x32_bf16 a[16:19], v[64:67], v[160:163], a[16:19]\n" \
"s_add_u32 m0, s74, 98304\n" \
"v_mfma_f32_16x16x32_bf16 a[16:19], v[68:71], v[164:167], a[16:19]\n" \
"buffer_load_dwordx4 v224, s[48:51], s75 offen offset:0 lds\n" \
"v_mfma_f32_16x16x32_bf16 a[20:23], v[64:67], v[168:171], a[20:23]\n" \
"ds_read_b64_tr_b16 v[0:1], v232 offset:0\n" \
"v_mfma_f32_16x16x32_bf16 a[20:23], v[68:71], v[172:175], a[20:23]\n" \
"v_mfma_f32_16x16x32_bf16 a[24:27], v[64:67], v[176:179], a[24:27]\n" \
"ds_read_b64_tr_b16 v[2:3], v232 offset:2048\n" \
"v_mfma_f32_16x16x32_bf16 a[24:27], v[68:71], v[180:183], a[24:27]\n" \
"buffer_load_dwordx4 v225, s[48:51], s75 offen offset:1024 lds\n" \
"v_mfma_f32_16x16x32_bf16 a[28:31], v[64:67], v[184:187], a[28:31]\n" \
"ds_read_b64_tr_b16 v[4:5], v232 offset:8192\n" \
"v_mfma_f32_16x16x32_bf16 a[28:31], v[68:71], v[188:191], a[28:31]\n" \
"v_mfma_f32_16x16x32_bf16 a[60:63], v[72:75], v[184:187], a[60:63]\n" \
"ds_read_b64_tr_b16 v[6:7], v232 offset:10240\n" \
"v_accvgpr_read_b32 v243, a192\n" \
"v_mfma_f32_16x16x32_bf16 a[60:63], v[76:79], v[188:191], a[60:63]\n" \
"buffer_load_dwordx4 v226, s[48:51], s75 offen offset:2048 lds\n" \
"v_mfma_f32_16x16x32_bf16 a[56:59], v[72:75], v[176:179], a[56:59]\n" \
"ds_read_b64_tr_b16 v[8:9], v233 offset:0\n" \
"v_accvgpr_read_b32 v252, a193\n" \
"v_mfma_f32_16x16x32_bf16 a[56:59], v[76:79], v[180:183], a[56:59]\n" \
"v_accvgpr_read_b32 v253, a194\n" \
"v_accvgpr_read_b32 v254, a195\n" \
"v_mfma_f32_16x16x32_bf16 a[52:55], v[72:75], v[168:171], a[52:55]\n" \
"ds_read_b64_tr_b16 v[10:11], v233 offset:2048\n" \
"v_mul_f32 v243, s76, v243\n" \
"v_mfma_f32_16x16x32_bf16 a[52:55], v[76:79], v[172:175], a[52:55]\n" \
"buffer_load_dwordx4 v227, s[48:51], s75 offen offset:3072 lds\n" \
"v_mfma_f32_16x16x32_bf16 a[48:51], v[72:75], v[160:163], a[48:51]\n" \
"ds_read_b64_tr_b16 v[12:13], v233 offset:8192\n" \
"v_mul_f32 v252, s76, v252\n" \
"v_mfma_f32_16x16x32_bf16 a[48:51], v[76:79], v[164:167], a[48:51]\n" \
"s_xor_b32 s75, s75, 0x500\n" \
"v_mul_f32 v253, s76, v253\n" \
"v_mul_f32 v254, s76, v254\n" \
"v_mfma_f32_16x16x32_bf16 a[80:83], v[80:83], v[160:163], a[80:83]\n" \
"s_add_u32 m0, s74, 102400\n" \
"ds_read_b64_tr_b16 v[14:15], v233 offset:10240\n" \
"v_cvt_pk_bf16_f32 v243, v243, v252\n" \
"v_mfma_f32_16x16x32_bf16 a[80:83], v[84:87], v[164:167], a[80:83]\n" \
"buffer_load_dwordx4 v224, s[48:51], s77 offen offset:0 lds\n" \
"v_mfma_f32_16x16x32_bf16 a[84:87], v[80:83], v[168:171], a[84:87]\n" \
"ds_read_b64_tr_b16 v[16:17], v232 offset:512\n" \
"v_cvt_pk_bf16_f32 v253, v253, v254\n" \
"v_mfma_f32_16x16x32_bf16 a[84:87], v[84:87], v[172:175], a[84:87]\n" \
"v_accvgpr_write_b32 a192, v243\n" \
"v_accvgpr_write_b32 a193, v253\n" \
"v_mfma_f32_16x16x32_bf16 a[88:91], v[80:83], v[176:179], a[88:91]\n" \
"ds_read_b64_tr_b16 v[18:19], v232 offset:2560\n" \
"v_accvgpr_read_b32 v243, a196\n" \
"v_mfma_f32_16x16x32_bf16 a[88:91], v[84:87], v[180:183], a[88:91]\n" \
"buffer_load_dwordx4 v225, s[48:51], s77 offen offset:1024 lds\n" \
"v_mfma_f32_16x16x32_bf16 a[92:95], v[80:83], v[184:187], a[92:95]\n" \
"ds_read_b64_tr_b16 v[20:21], v232 offset:8704\n" \
"v_accvgpr_read_b32 v252, a197\n" \
"v_mfma_f32_16x16x32_bf16 a[92:95], v[84:87], v[188:191], a[92:95]\n" \
"v_accvgpr_read_b32 v253, a198\n" \
"v_accvgpr_read_b32 v254, a199\n" \
"v_mfma_f32_16x16x32_bf16 a[124:127], v[88:91], v[184:187], a[124:127]\n" \
"ds_read_b64_tr_b16 v[22:23], v232 offset:10752\n" \
"v_mul_f32 v243, s76, v243\n" \
"v_mfma_f32_16x16x32_bf16 a[124:127], v[92:95], v[188:191], a[124:127]\n" \
"buffer_load_dwordx4 v226, s[48:51], s77 offen offset:2048 lds\n" \
"v_mfma_f32_16x16x32_bf16 a[120:123], v[88:91], v[176:179], a[120:123]\n" \
"ds_read_b64_tr_b16 v[24:25], v233 offset:512\n" \
"v_mul_f32 v252, s76, v252\n" \
"v_mfma_f32_16x16x32_bf16 a[120:123], v[92:95], v[180:183], a[120:123]\n" \
"v_mul_f32 v253, s76, v253\n" \
"v_mul_f32 v254, s76, v254\n" \
"v_mfma_f32_16x16x32_bf16 a[116:119], v[88:91], v[168:171], a[116:119]\n" \
"ds_read_b64_tr_b16 v[26:27], v233 offset:2560\n" \
"v_cvt_pk_bf16_f32 v243, v243, v252\n" \
"v_mfma_f32_16x16x32_bf16 a[116:119], v[92:95], v[172:175], a[116:119]\n" \
"buffer_load_dwordx4 v227, s[48:51], s77 offen offset:3072 lds\n" \
"v_mfma_f32_16x16x32_bf16 a[112:115], v[88:91], v[160:163], a[112:115]\n" \
"ds_read_b64_tr_b16 v[28:29], v233 offset:8704\n" \
"v_cvt_pk_bf16_f32 v253, v253, v254\n" \
"v_mfma_f32_16x16x32_bf16 a[112:115], v[92:95], v[164:167], a[112:115]\n" \
"s_xor_b32 s77, s77, 0x500\n" \
"v_accvgpr_write_b32 a194, v243\n" \
"v_accvgpr_write_b32 a195, v253\n" \
"v_mfma_f32_16x16x32_bf16 a[144:147], v[96:99], v[160:163], a[144:147]\n" \
"s_add_u32 m0, s79, 32768\n" \
"ds_read_b64_tr_b16 v[30:31], v233 offset:10752\n" \
"v_accvgpr_read_b32 v243, a224\n" \
"v_mfma_f32_16x16x32_bf16 a[144:147], v[100:103], v[164:167], a[144:147]\n" \
"buffer_load_dwordx4 v228, s[52:55], s78 offen offset:0 lds\n" \
"v_mfma_f32_16x16x32_bf16 a[148:151], v[96:99], v[168:171], a[148:151]\n" \
"ds_read_b128 v[192:195], v230 offset:0\n" \
"v_accvgpr_read_b32 v252, a225\n" \
"v_mfma_f32_16x16x32_bf16 a[148:151], v[100:103], v[172:175], a[148:151]\n" \
"v_accvgpr_read_b32 v253, a226\n" \
"v_accvgpr_read_b32 v254, a227\n" \
"v_mfma_f32_16x16x32_bf16 a[152:155], v[96:99], v[176:179], a[152:155]\n" \
"ds_read_b128 v[196:199], v231 offset:0\n" \
"v_mul_f32 v243, s76, v243\n" \
"v_mfma_f32_16x16x32_bf16 a[152:155], v[100:103], v[180:183], a[152:155]\n" \
"buffer_load_dwordx4 v229, s[52:55], s78 offen offset:1024 lds\n" \
"v_mfma_f32_16x16x32_bf16 a[156:159], v[96:99], v[184:187], a[156:159]\n" \
"ds_read_b128 v[200:203], v230 offset:2048\n" \
"v_mul_f32 v252, s76, v252\n" \
"v_mfma_f32_16x16x32_bf16 a[156:159], v[100:103], v[188:191], a[156:159]\n" \
"v_mul_f32 v253, s76, v253\n" \
"v_mul_f32 v254, s76, v254\n" \
"v_mfma_f32_16x16x32_bf16 a[188:191], v[104:107], v[184:187], a[188:191]\n" \
"s_xor_b32 s78, s78, 0x500000\n" \
"ds_read_b128 v[204:207], v231 offset:2048\n" \
"v_cvt_pk_bf16_f32 v243, v243, v252\n" \
"v_mfma_f32_16x16x32_bf16 a[188:191], v[108:111], v[188:191], a[188:191]\n" \
"buffer_load_dwordx4 v228, s[52:55], s89 offen offset:2048 lds\n" \
"v_mfma_f32_16x16x32_bf16 a[184:187], v[104:107], v[176:179], a[184:187]\n" \
"ds_read_b128 v[208:211], v230 offset:4096\n" \
"v_cvt_pk_bf16_f32 v253, v253, v254\n" \
"v_mfma_f32_16x16x32_bf16 a[184:187], v[108:111], v[180:183], a[184:187]\n" \
"v_accvgpr_write_b32 a224, v243\n" \
"v_accvgpr_write_b32 a225, v253\n" \
"v_mfma_f32_16x16x32_bf16 a[180:183], v[104:107], v[168:171], a[180:183]\n" \
"ds_read_b128 v[212:215], v231 offset:4096\n" \
"v_accvgpr_read_b32 v243, a228\n" \
"v_mfma_f32_16x16x32_bf16 a[180:183], v[108:111], v[172:175], a[180:183]\n" \
"buffer_load_dwordx4 v229, s[52:55], s89 offen offset:3072 lds\n" \
"v_mfma_f32_16x16x32_bf16 a[176:179], v[104:107], v[160:163], a[176:179]\n" \
"v_accvgpr_read_b32 v252, a229\n" \
"v_accvgpr_read_b32 v253, a230\n" \
"v_mfma_f32_16x16x32_bf16 a[176:179], v[108:111], v[164:167], a[176:179]\n" \
"s_xor_b32 s89, s89, 0x500000\n" \
"v_accvgpr_read_b32 v254, a231\n" \
"v_mul_f32 v243, s76, v243\n" \
"v_mfma_f32_16x16x32_bf16 a[208:211], v[112:115], v[160:163], a[208:211]\n" \
"s_add_u32 m0, s79, 49152\n" \
"v_mul_f32 v252, s76, v252\n" \
"v_mul_f32 v253, s76, v253\n" \
"v_mfma_f32_16x16x32_bf16 a[208:211], v[116:119], v[164:167], a[208:211]\n" \
"buffer_load_dwordx4 v228, s[52:55], s78 offen offset:0 lds\n" \
"v_mfma_f32_16x16x32_bf16 a[212:215], v[112:115], v[168:171], a[212:215]\n" \
"v_mul_f32 v254, s76, v254\n" \
"v_cvt_pk_bf16_f32 v243, v243, v252\n" \
"v_mfma_f32_16x16x32_bf16 a[212:215], v[116:119], v[172:175], a[212:215]\n" \
"v_cvt_pk_bf16_f32 v253, v253, v254\n" \
"v_accvgpr_write_b32 a226, v243\n" \
"v_mfma_f32_16x16x32_bf16 a[216:219], v[112:115], v[176:179], a[216:219]\n" \
"v_accvgpr_write_b32 a227, v253\n" \
"v_accvgpr_read_b32 v243, a200\n" \
"v_mfma_f32_16x16x32_bf16 a[216:219], v[116:119], v[180:183], a[216:219]\n" \
"buffer_load_dwordx4 v229, s[52:55], s78 offen offset:1024 lds\n" \
"v_mfma_f32_16x16x32_bf16 a[220:223], v[112:115], v[184:187], a[220:223]\n" \
"v_accvgpr_read_b32 v252, a201\n" \
"v_accvgpr_read_b32 v253, a202\n" \
"v_mfma_f32_16x16x32_bf16 a[220:223], v[116:119], v[188:191], a[220:223]\n" \
"v_accvgpr_read_b32 v254, a203\n" \
"v_mul_f32 v243, s76, v243\n" \
"v_mfma_f32_16x16x32_bf16 a[252:255], v[120:123], v[184:187], a[252:255]\n" \
"s_xor_b32 s78, s78, 0x180000\n" \
"v_mul_f32 v252, s76, v252\n" \
"v_mul_f32 v253, s76, v253\n" \
"v_mfma_f32_16x16x32_bf16 a[252:255], v[124:127], v[188:191], a[252:255]\n" \
"buffer_load_dwordx4 v228, s[52:55], s89 offen offset:2048 lds\n" \
"v_mfma_f32_16x16x32_bf16 a[248:251], v[120:123], v[176:179], a[248:251]\n" \
"v_mul_f32 v254, s76, v254\n" \
"v_cvt_pk_bf16_f32 v243, v243, v252\n" \
"v_mfma_f32_16x16x32_bf16 a[248:251], v[124:127], v[180:183], a[248:251]\n" \
"v_cvt_pk_bf16_f32 v253, v253, v254\n" \
"v_accvgpr_write_b32 a200, v243\n" \
"v_mfma_f32_16x16x32_bf16 a[244:247], v[120:123], v[168:171], a[244:247]\n" \
"v_accvgpr_write_b32 a201, v253\n" \
"v_accvgpr_read_b32 v243, a204\n" \
"v_mfma_f32_16x16x32_bf16 a[244:247], v[124:127], v[172:175], a[244:247]\n" \
"buffer_load_dwordx4 v229, s[52:55], s89 offen offset:3072 lds\n" \
"v_mfma_f32_16x16x32_bf16 a[240:243], v[120:123], v[160:163], a[240:243]\n" \
"s_waitcnt lgkmcnt(0)\n" \
"v_accvgpr_read_b32 v252, a205\n" \
"v_accvgpr_read_b32 v253, a206\n" \
"v_mfma_f32_16x16x32_bf16 a[240:243], v[124:127], v[164:167], a[240:243]\n" \
"s_xor_b32 s89, s89, 0x180000\n" \
"v_accvgpr_read_b32 v254, a207\n" \
"v_mul_f32 v243, s76, v243\n" \
"v_mfma_f32_16x16x32_bf16 v[64:67], v[0:3], v[192:195], 0\n" \
"ds_read_b128 v[216:219], v230 offset:6144\n" \
"v_mul_f32 v252, s76, v252\n" \
"v_mfma_f32_16x16x32_bf16 v[64:67], v[4:7], v[196:199], v[64:67]\n" \
"s_add_u32 m0, s74, 131072\n" \
"ds_read_b128 v[220:223], v231 offset:6144\n" \
"v_mul_f32 v253, s76, v253\n" \
"v_mfma_f32_16x16x32_bf16 v[68:71], v[8:11], v[192:195], 0\n" \
"ds_read_b64_tr_b16 v[32:33], v232 offset:1024\n" \
"v_mul_f32 v254, s76, v254\n" \
"v_mfma_f32_16x16x32_bf16 v[68:71], v[12:15], v[196:199], v[68:71]\n" \
"buffer_load_dwordx4 v224, s[48:51], s75 offen offset:0 lds\n" \
"v_mfma_f32_16x16x32_bf16 v[72:75], v[16:19], v[192:195], 0\n" \
"ds_read_b64_tr_b16 v[34:35], v232 offset:3072\n" \
"v_cvt_pk_bf16_f32 v243, v243, v252\n" \
"v_mfma_f32_16x16x32_bf16 v[72:75], v[20:23], v[196:199], v[72:75]\n" \
"ds_read_b64_tr_b16 v[36:37], v232 offset:9216\n" \
"v_cvt_pk_bf16_f32 v253, v253, v254\n" \
"v_mfma_f32_16x16x32_bf16 v[76:79], v[24:27], v[192:195], 0\n" \
"ds_read_b64_tr_b16 v[38:39], v232 offset:11264\n" \
"v_accvgpr_write_b32 a202, v243\n" \
"v_mfma_f32_16x16x32_bf16 v[76:79], v[28:31], v[196:199], v[76:79]\n" \
"buffer_load_dwordx4 v225, s[48:51], s75 offen offset:1024 lds\n" \
"v_mfma_f32_16x16x32_bf16 v[108:111], v[24:27], v[200:203], 0\n" \
"ds_read_b64_tr_b16 v[40:41], v233 offset:1024\n" \
"v_accvgpr_write_b32 a203, v253\n" \
"v_mfma_f32_16x16x32_bf16 v[108:111], v[28:31], v[204:207], v[108:111]\n" \
"ds_read_b64_tr_b16 v[42:43], v233 offset:3072\n" \
"v_accvgpr_read_b32 v243, a232\n" \
"v_mfma_f32_16x16x32_bf16 v[104:107], v[16:19], v[200:203], 0\n" \
"ds_read_b64_tr_b16 v[44:45], v233 offset:9216\n" \
"v_accvgpr_read_b32 v252, a233\n" \
"v_mfma_f32_16x16x32_bf16 v[104:107], v[20:23], v[204:207], v[104:107]\n" \
"buffer_load_dwordx4 v226, s[48:51], s75 offen offset:2048 lds\n" \
"v_mfma_f32_16x16x32_bf16 v[100:103], v[8:11], v[200:203], 0\n" \
"ds_read_b64_tr_b16 v[46:47], v233 offset:11264\n" \
"v_accvgpr_read_b32 v253, a234\n" \
"v_mfma_f32_16x16x32_bf16 v[100:103], v[12:15], v[204:207], v[100:103]\n" \
"ds_read_b64_tr_b16 v[48:49], v232 offset:1536\n" \
"v_accvgpr_read_b32 v254, a235\n" \
"v_mfma_f32_16x16x32_bf16 v[96:99], v[0:3], v[200:203], 0\n" \
"ds_read_b64_tr_b16 v[50:51], v232 offset:3584\n" \
"v_mul_f32 v243, s76, v243\n" \
"v_mfma_f32_16x16x32_bf16 v[96:99], v[4:7], v[204:207], v[96:99]\n" \
"buffer_load_dwordx4 v227, s[48:51], s75 offen offset:3072 lds\n" \
"v_mfma_f32_16x16x32_bf16 v[128:131], v[0:3], v[208:211], 0\n" \
"s_xor_b32 s75, s75, 0x180\n" \
"ds_read_b64_tr_b16 v[52:53], v232 offset:9728\n" \
"v_mul_f32 v252, s76, v252\n" \
"v_mfma_f32_16x16x32_bf16 v[128:131], v[4:7], v[212:215], v[128:131]\n" \
"s_add_u32 m0, s74, 135168\n" \
"ds_read_b64_tr_b16 v[54:55], v232 offset:11776\n" \
"v_mul_f32 v253, s76, v253\n" \
"v_mfma_f32_16x16x32_bf16 v[132:135], v[8:11], v[208:211], 0\n" \
"ds_read_b64_tr_b16 v[56:57], v233 offset:1536\n" \
"v_mul_f32 v254, s76, v254\n" \
"v_mfma_f32_16x16x32_bf16 v[132:135], v[12:15], v[212:215], v[132:135]\n" \
"buffer_load_dwordx4 v224, s[48:51], s77 offen offset:0 lds\n" \
"v_mfma_f32_16x16x32_bf16 v[136:139], v[16:19], v[208:211], 0\n" \
"ds_read_b64_tr_b16 v[58:59], v233 offset:3584\n" \
"v_cvt_pk_bf16_f32 v243, v243, v252\n" \
"v_mfma_f32_16x16x32_bf16 v[136:139], v[20:23], v[212:215], v[136:139]\n" \
"ds_read_b64_tr_b16 v[60:61], v233 offset:9728\n" \
"Lpg2177_%=:\n" \
"Lst0x3_%=:\n" \
"s_cbranch_execz Lpg4351_%=\n" \
"v_cvt_pk_bf16_f32 v253, v253, v254\n" \
"v_mfma_f32_16x16x32_bf16 v[140:143], v[24:27], v[208:211], 0\n" \
"s_waitcnt lgkmcnt(3)\n" \
"ds_read_b64_tr_b16 v[62:63], v233 offset:11776\n" \
"v_accvgpr_write_b32 a232, v243\n" \
"v_mfma_f32_16x16x32_bf16 v[140:143], v[28:31], v[212:215], v[140:143]\n" \
"buffer_load_dwordx4 v225, s[48:51], s77 offen offset:1024 lds\n" \
"v_mfma_f32_16x16x32_bf16 v[172:175], v[24:27], v[216:219], 0\n" \
"v_add_u32 v230, 0xffff0000, v230\n" \
"v_accvgpr_write_b32 a233, v253\n" \
"v_mfma_f32_16x16x32_bf16 v[172:175], v[28:31], v[220:223], v[172:175]\n" \
"v_add_u32 v231, 0xffff0000, v231\n" \
"v_accvgpr_read_b32 v243, a236\n" \
"v_mfma_f32_16x16x32_bf16 v[168:171], v[16:19], v[216:219], 0\n" \
"v_add_u32 v232, 0x4000, v232\n" \
"v_accvgpr_read_b32 v252, a237\n" \
"v_mfma_f32_16x16x32_bf16 v[168:171], v[20:23], v[220:223], v[168:171]\n" \
"buffer_load_dwordx4 v226, s[48:51], s77 offen offset:2048 lds\n" \
"v_mfma_f32_16x16x32_bf16 v[164:167], v[8:11], v[216:219], 0\n" \
"v_add_u32 v233, 0x4000, v233\n" \
"v_accvgpr_read_b32 v253, a238\n" \
"v_mfma_f32_16x16x32_bf16 v[164:167], v[12:15], v[220:223], v[164:167]\n" \
"buffer_load_dwordx4 v227, s[48:51], s77 offen offset:3072 lds\n" \
"v_mfma_f32_16x16x32_bf16 v[160:163], v[0:3], v[216:219], 0\n" \
"v_accvgpr_read_b32 v254, a239\n" \
"v_mul_f32 v243, s76, v243\n" \
"v_mfma_f32_16x16x32_bf16 v[160:163], v[4:7], v[220:223], v[160:163]\n" \
"s_waitcnt vmcnt(42) lgkmcnt(0)\n" \
"s_barrier\n" \
"v_mfma_f32_16x16x32_bf16 v[80:83], v[32:35], v[192:195], 0\n" \
"s_xor_b32 s77, s77, 0x180\n" \
"ds_read_b64_tr_b16 v[0:1], v232 offset:0\n" \
"v_mul_f32 v252, s76, v252\n" \
"v_mfma_f32_16x16x32_bf16 v[80:83], v[36:39], v[196:199], v[80:83]\n" \
"ds_read_b64_tr_b16 v[2:3], v232 offset:2048\n" \
"v_mul_f32 v253, s76, v253\n" \
"v_mfma_f32_16x16x32_bf16 v[84:87], v[40:43], v[192:195], 0\n" \
"s_add_u32 m0, s79, 0\n" \
"ds_read_b64_tr_b16 v[4:5], v232 offset:8192\n" \
"v_mul_f32 v254, s76, v254\n" \
"v_mfma_f32_16x16x32_bf16 v[84:87], v[44:47], v[196:199], v[84:87]\n" \
"buffer_load_dwordx4 v228, s[52:55], s78 offen offset:0 lds\n" \
"v_mfma_f32_16x16x32_bf16 v[88:91], v[48:51], v[192:195], 0\n" \
"ds_read_b64_tr_b16 v[6:7], v232 offset:10240\n" \
"v_cvt_pk_bf16_f32 v243, v243, v252\n" \
"v_mfma_f32_16x16x32_bf16 v[88:91], v[52:55], v[196:199], v[88:91]\n" \
"ds_read_b64_tr_b16 v[8:9], v233 offset:0\n" \
"v_cvt_pk_bf16_f32 v253, v253, v254\n" \
"v_mfma_f32_16x16x32_bf16 v[92:95], v[56:59], v[192:195], 0\n" \
"ds_read_b64_tr_b16 v[10:11], v233 offset:2048\n" \
"v_accvgpr_write_b32 a234, v243\n" \
"v_mfma_f32_16x16x32_bf16 v[92:95], v[60:63], v[196:199], v[92:95]\n" \
"buffer_load_dwordx4 v229, s[52:55], s78 offen offset:1024 lds\n" \
"v_mfma_f32_16x16x32_bf16 v[124:127], v[56:59], v[200:203], 0\n" \
"s_waitcnt vmcnt(32)\n" \
"ds_read_b128 v[192:195], v230 offset:0\n" \
"v_accvgpr_write_b32 a235, v253\n" \
"v_mfma_f32_16x16x32_bf16 v[124:127], v[60:63], v[204:207], v[124:127]\n" \
"ds_read_b128 v[196:199], v231 offset:0\n" \
"v_accvgpr_read_b32 v243, a128\n" \
"v_mfma_f32_16x16x32_bf16 v[120:123], v[48:51], v[200:203], 0\n" \
"s_xor_b32 s78, s78, 0x300000\n" \
"ds_read_b64_tr_b16 v[12:13], v233 offset:8192\n" \
"v_accvgpr_read_b32 v252, a129\n" \
"v_mfma_f32_16x16x32_bf16 v[120:123], v[52:55], v[204:207], v[120:123]\n" \
"buffer_load_dwordx4 v228, s[52:55], s89 offen offset:2048 lds\n" \
"v_mfma_f32_16x16x32_bf16 v[116:119], v[40:43], v[200:203], 0\n" \
"ds_read_b64_tr_b16 v[14:15], v233 offset:10240\n" \
"v_accvgpr_read_b32 v253, a130\n" \
"v_mfma_f32_16x16x32_bf16 v[116:119], v[44:47], v[204:207], v[116:119]\n" \
"ds_read_b64_tr_b16 v[16:17], v232 offset:512\n" \
"v_accvgpr_read_b32 v254, a131\n" \
"v_mfma_f32_16x16x32_bf16 v[112:115], v[32:35], v[200:203], 0\n" \
"ds_read_b64_tr_b16 v[18:19], v232 offset:2560\n" \
"v_mul_f32 v243, s76, v243\n" \
"v_mfma_f32_16x16x32_bf16 v[112:115], v[36:39], v[204:207], v[112:115]\n" \
"buffer_load_dwordx4 v229, s[52:55], s89 offen offset:3072 lds\n" \
"v_mfma_f32_16x16x32_bf16 v[144:147], v[32:35], v[208:211], 0\n" \
"s_waitcnt vmcnt(32)\n" \
"s_xor_b32 s89, s89, 0x300000\n" \
"ds_read_b128 v[200:203], v230 offset:2048\n" \
"v_mfma_f32_16x16x32_bf16 v[144:147], v[36:39], v[212:215], v[144:147]\n" \
"s_add_u32 m0, s74, 65536\n" \
"ds_read_b128 v[204:207], v231 offset:2048\n" \
"v_mul_f32 v252, s76, v252\n" \
"v_mfma_f32_16x16x32_bf16 v[148:151], v[40:43], v[208:211], 0\n" \
"s_waitcnt lgkmcnt(6)\n" \
"ds_read_b64_tr_b16 v[20:21], v232 offset:8704\n" \
"v_mul_f32 v253, s76, v253\n" \
"v_mfma_f32_16x16x32_bf16 v[148:151], v[44:47], v[212:215], v[148:151]\n" \
"buffer_load_dwordx4 v224, s[48:51], s75 offen offset:0 lds\n" \
"v_mfma_f32_16x16x32_bf16 v[152:155], v[48:51], v[208:211], 0\n" \
"ds_read_b64_tr_b16 v[22:23], v232 offset:10752\n" \
"v_mul_f32 v254, s76, v254\n" \
"v_mfma_f32_16x16x32_bf16 v[152:155], v[52:55], v[212:215], v[152:155]\n" \
"ds_read_b64_tr_b16 v[24:25], v233 offset:512\n" \
"v_cvt_pk_bf16_f32 v243, v243, v252\n" \
"v_mfma_f32_16x16x32_bf16 v[156:159], v[56:59], v[208:211], 0\n" \
"ds_read_b64_tr_b16 v[26:27], v233 offset:2560\n" \
"v_cvt_pk_bf16_f32 v253, v253, v254\n" \
"v_mfma_f32_16x16x32_bf16 v[156:159], v[60:63], v[212:215], v[156:159]\n" \
"buffer_load_dwordx4 v225, s[48:51], s75 offen offset:1024 lds\n" \
"v_mfma_f32_16x16x32_bf16 v[188:191], v[56:59], v[216:219], 0\n" \
"s_waitcnt vmcnt(32)\n" \
"ds_read_b128 v[208:211], v230 offset:4096\n" \
"v_accvgpr_write_b32 a128, v243\n" \
"v_mfma_f32_16x16x32_bf16 v[188:191], v[60:63], v[220:223], v[188:191]\n" \
"ds_read_b128 v[212:215], v231 offset:4096\n" \
"v_accvgpr_write_b32 a129, v253\n" \
"v_mfma_f32_16x16x32_bf16 v[184:187], v[48:51], v[216:219], 0\n" \
"s_waitcnt lgkmcnt(6)\n" \
"ds_read_b64_tr_b16 v[28:29], v233 offset:8704\n" \
"v_accvgpr_read_b32 v243, a132\n" \
"v_mfma_f32_16x16x32_bf16 v[184:187], v[52:55], v[220:223], v[184:187]\n" \
"buffer_load_dwordx4 v226, s[48:51], s75 offen offset:2048 lds\n" \
"v_mfma_f32_16x16x32_bf16 v[180:183], v[40:43], v[216:219], 0\n" \
"ds_read_b64_tr_b16 v[30:31], v233 offset:10752\n" \
"v_accvgpr_read_b32 v252, a133\n" \
"v_mfma_f32_16x16x32_bf16 v[180:183], v[44:47], v[220:223], v[180:183]\n" \
"v_accvgpr_read_b32 v253, a134\n" \
"v_accvgpr_read_b32 v254, a135\n" \
"v_mfma_f32_16x16x32_bf16 v[176:179], v[32:35], v[216:219], 0\n" \
"v_mul_f32 v243, s76, v243\n" \
"v_mul_f32 v252, s76, v252\n" \
"v_mfma_f32_16x16x32_bf16 v[176:179], v[36:39], v[220:223], v[176:179]\n" \
"buffer_load_dwordx4 v227, s[48:51], s75 offen offset:3072 lds\n" \
"v_mfma_f32_16x16x32_bf16 v[64:67], v[0:3], v[192:195], v[64:67]\n" \
"s_waitcnt vmcnt(32)\n" \
"s_xor_b32 s75, s75, 0x300\n" \
"ds_read_b128 v[216:219], v230 offset:6144\n" \
"v_mfma_f32_16x16x32_bf16 v[64:67], v[4:7], v[196:199], v[64:67]\n" \
"s_add_u32 m0, s74, 69632\n" \
"ds_read_b128 v[220:223], v231 offset:6144\n" \
"v_mul_f32 v253, s76, v253\n" \
"v_mfma_f32_16x16x32_bf16 v[68:71], v[8:11], v[192:195], v[68:71]\n" \
"s_waitcnt lgkmcnt(4)\n" \
"ds_read_b64_tr_b16 v[32:33], v232 offset:1024\n" \
"v_mul_f32 v254, s76, v254\n" \
"v_mfma_f32_16x16x32_bf16 v[68:71], v[12:15], v[196:199], v[68:71]\n" \
"buffer_load_dwordx4 v224, s[48:51], s77 offen offset:0 lds\n" \
"v_mfma_f32_16x16x32_bf16 v[72:75], v[16:19], v[192:195], v[72:75]\n" \
"ds_read_b64_tr_b16 v[34:35], v232 offset:3072\n" \
"v_cvt_pk_bf16_f32 v243, v243, v252\n" \
"v_mfma_f32_16x16x32_bf16 v[72:75], v[20:23], v[196:199], v[72:75]\n" \
"s_waitcnt lgkmcnt(3)\n" \
"ds_read_b64_tr_b16 v[36:37], v232 offset:9216\n" \
"v_cvt_pk_bf16_f32 v253, v253, v254\n" \
"v_mfma_f32_16x16x32_bf16 v[76:79], v[24:27], v[192:195], v[76:79]\n" \
"ds_read_b64_tr_b16 v[38:39], v232 offset:11264\n" \
"v_accvgpr_write_b32 a130, v243\n" \
"v_mfma_f32_16x16x32_bf16 v[76:79], v[28:31], v[196:199], v[76:79]\n" \
"buffer_load_dwordx4 v225, s[48:51], s77 offen offset:1024 lds\n" \
"v_mfma_f32_16x16x32_bf16 v[108:111], v[24:27], v[200:203], v[108:111]\n" \
"ds_read_b64_tr_b16 v[40:41], v233 offset:1024\n" \
"v_accvgpr_write_b32 a131, v253\n" \
"v_mfma_f32_16x16x32_bf16 v[108:111], v[28:31], v[204:207], v[108:111]\n" \
"ds_read_b64_tr_b16 v[42:43], v233 offset:3072\n" \
"v_accvgpr_read_b32 v243, a160\n" \
"v_mfma_f32_16x16x32_bf16 v[104:107], v[16:19], v[200:203], v[104:107]\n" \
"s_waitcnt lgkmcnt(6)\n" \
"ds_read_b64_tr_b16 v[44:45], v233 offset:9216\n" \
"v_accvgpr_read_b32 v252, a161\n" \
"v_mfma_f32_16x16x32_bf16 v[104:107], v[20:23], v[204:207], v[104:107]\n" \
"buffer_load_dwordx4 v226, s[48:51], s77 offen offset:2048 lds\n" \
"v_mfma_f32_16x16x32_bf16 v[100:103], v[8:11], v[200:203], v[100:103]\n" \
"ds_read_b64_tr_b16 v[46:47], v233 offset:11264\n" \
"v_accvgpr_read_b32 v253, a162\n" \
"v_mfma_f32_16x16x32_bf16 v[100:103], v[12:15], v[204:207], v[100:103]\n" \
"ds_read_b64_tr_b16 v[48:49], v232 offset:1536\n" \
"v_accvgpr_read_b32 v254, a163\n" \
"v_mfma_f32_16x16x32_bf16 v[96:99], v[0:3], v[200:203], v[96:99]\n" \
"ds_read_b64_tr_b16 v[50:51], v232 offset:3584\n" \
"v_mul_f32 v243, s76, v243\n" \
"v_mfma_f32_16x16x32_bf16 v[96:99], v[4:7], v[204:207], v[96:99]\n" \
"buffer_load_dwordx4 v227, s[48:51], s77 offen offset:3072 lds\n" \
"v_mfma_f32_16x16x32_bf16 v[128:131], v[0:3], v[208:211], v[128:131]\n" \
"ds_read_b64_tr_b16 v[52:53], v232 offset:9728\n" \
"v_mul_f32 v252, s76, v252\n" \
"v_mfma_f32_16x16x32_bf16 v[128:131], v[4:7], v[212:215], v[128:131]\n" \
"ds_read_b64_tr_b16 v[54:55], v232 offset:11776\n" \
"v_mul_f32 v253, s76, v253\n" \
"v_mfma_f32_16x16x32_bf16 v[132:135], v[8:11], v[208:211], v[132:135]\n" \
"s_xor_b32 s77, s77, 0x300\n" \
"s_add_u32 s93, s87, 8192\n" \
"ds_read_b64_tr_b16 v[56:57], v233 offset:1536\n" \
"v_mfma_f32_16x16x32_bf16 v[132:135], v[12:15], v[212:215], v[132:135]\n" \
"buffer_store_dwordx4 a[192:195], v236, s[56:59], s93 offen offset:0 sc1\n" \
"v_mfma_f32_16x16x32_bf16 v[136:139], v[16:19], v[208:211], v[136:139]\n" \
"ds_read_b64_tr_b16 v[58:59], v233 offset:3584\n" \
"v_mul_f32 v254, s76, v254\n" \
"v_mfma_f32_16x16x32_bf16 v[136:139], v[20:23], v[212:215], v[136:139]\n" \
"ds_read_b64_tr_b16 v[60:61], v233 offset:9728\n" \
"v_cvt_pk_bf16_f32 v243, v243, v252\n" \
"v_mfma_f32_16x16x32_bf16 v[140:143], v[24:27], v[208:211], v[140:143]\n" \
"ds_read_b64_tr_b16 v[62:63], v233 offset:11776\n" \
"v_cvt_pk_bf16_f32 v253, v253, v254\n" \
"v_mfma_f32_16x16x32_bf16 v[140:143], v[28:31], v[212:215], v[140:143]\n" \
"v_accvgpr_write_b32 a160, v243\n" \
"v_accvgpr_write_b32 a161, v253\n" \
"v_mfma_f32_16x16x32_bf16 v[172:175], v[24:27], v[216:219], v[172:175]\n" \
"v_add_u32 v230, 0x8000, v230\n" \
"v_accvgpr_read_b32 v243, a164\n" \
"v_mfma_f32_16x16x32_bf16 v[172:175], v[28:31], v[220:223], v[172:175]\n" \
"v_add_u32 v231, 0x8000, v231\n" \
"v_accvgpr_read_b32 v252, a165\n" \
"v_mfma_f32_16x16x32_bf16 v[168:171], v[16:19], v[216:219], v[168:171]\n" \
"v_add_u32 v232, 0x4000, v232\n" \
"v_accvgpr_read_b32 v253, a166\n" \
"v_mfma_f32_16x16x32_bf16 v[168:171], v[20:23], v[220:223], v[168:171]\n" \
"buffer_store_dwordx4 a[224:227], v236, s[56:59], s93 offen offset:256 sc1\n" \
"v_mfma_f32_16x16x32_bf16 v[164:167], v[8:11], v[216:219], v[164:167]\n" \
"v_add_u32 v233, 0x4000, v233\n" \
"v_accvgpr_read_b32 v254, a167\n" \
"v_mfma_f32_16x16x32_bf16 v[164:167], v[12:15], v[220:223], v[164:167]\n" \
"v_mul_f32 v243, s76, v243\n" \
"v_mul_f32 v252, s76, v252\n" \
"v_mfma_f32_16x16x32_bf16 v[160:163], v[0:3], v[216:219], v[160:163]\n" \
"v_mul_f32 v253, s76, v253\n" \
"v_mul_f32 v254, s76, v254\n" \
"v_mfma_f32_16x16x32_bf16 v[160:163], v[4:7], v[220:223], v[160:163]\n" \
"s_waitcnt vmcnt(26) lgkmcnt(0)\n" \
"s_barrier\n" \
"v_mfma_f32_16x16x32_bf16 v[80:83], v[32:35], v[192:195], v[80:83]\n" \
"ds_read_b64_tr_b16 v[0:1], v232 offset:0\n" \
"v_cvt_pk_bf16_f32 v243, v243, v252\n" \
"v_mfma_f32_16x16x32_bf16 v[80:83], v[36:39], v[196:199], v[80:83]\n" \
"ds_read_b64_tr_b16 v[2:3], v232 offset:2048\n" \
"v_cvt_pk_bf16_f32 v253, v253, v254\n" \
"v_mfma_f32_16x16x32_bf16 v[84:87], v[40:43], v[192:195], v[84:87]\n" \
"s_add_u32 m0, s79, 16384\n" \
"ds_read_b64_tr_b16 v[4:5], v232 offset:8192\n" \
"v_accvgpr_write_b32 a162, v243\n" \
"v_mfma_f32_16x16x32_bf16 v[84:87], v[44:47], v[196:199], v[84:87]\n" \
"buffer_load_dwordx4 v228, s[52:55], s78 offen offset:0 lds\n" \
"v_mfma_f32_16x16x32_bf16 v[88:91], v[48:51], v[192:195], v[88:91]\n" \
"ds_read_b64_tr_b16 v[6:7], v232 offset:10240\n" \
"v_accvgpr_write_b32 a163, v253\n" \
"v_mfma_f32_16x16x32_bf16 v[88:91], v[52:55], v[196:199], v[88:91]\n" \
"ds_read_b64_tr_b16 v[8:9], v233 offset:0\n" \
"v_accvgpr_read_b32 v243, a136\n" \
"v_mfma_f32_16x16x32_bf16 v[92:95], v[56:59], v[192:195], v[92:95]\n" \
"ds_read_b64_tr_b16 v[10:11], v233 offset:2048\n" \
"v_accvgpr_read_b32 v252, a137\n" \
"v_mfma_f32_16x16x32_bf16 v[92:95], v[60:63], v[196:199], v[92:95]\n" \
"buffer_load_dwordx4 v229, s[52:55], s78 offen offset:1024 lds\n" \
"v_mfma_f32_16x16x32_bf16 v[124:127], v[56:59], v[200:203], v[124:127]\n" \
"ds_read_b128 v[192:195], v230 offset:0\n" \
"v_accvgpr_read_b32 v253, a138\n" \
"v_mfma_f32_16x16x32_bf16 v[124:127], v[60:63], v[204:207], v[124:127]\n" \
"ds_read_b128 v[196:199], v231 offset:0\n" \
"v_accvgpr_read_b32 v254, a139\n" \
"v_mfma_f32_16x16x32_bf16 v[120:123], v[48:51], v[200:203], v[120:123]\n" \
"s_xor_b32 s78, s78, 0x80000\n" \
"ds_read_b64_tr_b16 v[12:13], v233 offset:8192\n" \
"v_mul_f32 v243, s76, v243\n" \
"v_mfma_f32_16x16x32_bf16 v[120:123], v[52:55], v[204:207], v[120:123]\n" \
"buffer_load_dwordx4 v228, s[52:55], s89 offen offset:2048 lds\n" \
"v_mfma_f32_16x16x32_bf16 v[116:119], v[40:43], v[200:203], v[116:119]\n" \
"ds_read_b64_tr_b16 v[14:15], v233 offset:10240\n" \
"v_mul_f32 v252, s76, v252\n" \
"v_mfma_f32_16x16x32_bf16 v[116:119], v[44:47], v[204:207], v[116:119]\n" \
"ds_read_b64_tr_b16 v[16:17], v232 offset:512\n" \
"v_mul_f32 v253, s76, v253\n" \
"v_mfma_f32_16x16x32_bf16 v[112:115], v[32:35], v[200:203], v[112:115]\n" \
"ds_read_b64_tr_b16 v[18:19], v232 offset:2560\n" \
"v_mul_f32 v254, s76, v254\n" \
"v_mfma_f32_16x16x32_bf16 v[112:115], v[36:39], v[204:207], v[112:115]\n" \
"buffer_load_dwordx4 v229, s[52:55], s89 offen offset:3072 lds\n" \
"v_mfma_f32_16x16x32_bf16 v[144:147], v[32:35], v[208:211], v[144:147]\n" \
"s_xor_b32 s89, s89, 0x80000\n" \
"ds_read_b128 v[200:203], v230 offset:2048\n" \
"v_mfma_f32_16x16x32_bf16 v[144:147], v[36:39], v[212:215], v[144:147]\n" \
"s_add_u32 m0, s74, 98304\n" \
"ds_read_b128 v[204:207], v231 offset:2048\n" \
"v_cvt_pk_bf16_f32 v243, v243, v252\n" \
"v_mfma_f32_16x16x32_bf16 v[148:151], v[40:43], v[208:211], v[148:151]\n" \
"s_waitcnt lgkmcnt(6)\n" \
"ds_read_b64_tr_b16 v[20:21], v232 offset:8704\n" \
"v_cvt_pk_bf16_f32 v253, v253, v254\n" \
"v_mfma_f32_16x16x32_bf16 v[148:151], v[44:47], v[212:215], v[148:151]\n" \
"buffer_load_dwordx4 v224, s[48:51], s75 offen offset:0 lds\n" \
"v_mfma_f32_16x16x32_bf16 v[152:155], v[48:51], v[208:211], v[152:155]\n" \
"ds_read_b64_tr_b16 v[22:23], v232 offset:10752\n" \
"v_accvgpr_write_b32 a136, v243\n" \
"v_mfma_f32_16x16x32_bf16 v[152:155], v[52:55], v[212:215], v[152:155]\n" \
"ds_read_b64_tr_b16 v[24:25], v233 offset:512\n" \
"v_accvgpr_write_b32 a137, v253\n" \
"v_mfma_f32_16x16x32_bf16 v[156:159], v[56:59], v[208:211], v[156:159]\n" \
"ds_read_b64_tr_b16 v[26:27], v233 offset:2560\n" \
"v_accvgpr_read_b32 v243, a140\n" \
"v_mfma_f32_16x16x32_bf16 v[156:159], v[60:63], v[212:215], v[156:159]\n" \
"buffer_load_dwordx4 v225, s[48:51], s75 offen offset:1024 lds\n" \
"v_mfma_f32_16x16x32_bf16 v[188:191], v[56:59], v[216:219], v[188:191]\n" \
"ds_read_b128 v[208:211], v230 offset:4096\n" \
"v_accvgpr_read_b32 v252, a141\n" \
"v_mfma_f32_16x16x32_bf16 v[188:191], v[60:63], v[220:223], v[188:191]\n" \
"ds_read_b128 v[212:215], v231 offset:4096\n" \
"v_accvgpr_read_b32 v253, a142\n" \
"v_mfma_f32_16x16x32_bf16 v[184:187], v[48:51], v[216:219], v[184:187]\n" \
"s_waitcnt lgkmcnt(6)\n" \
"ds_read_b64_tr_b16 v[28:29], v233 offset:8704\n" \
"v_accvgpr_read_b32 v254, a143\n" \
"v_mfma_f32_16x16x32_bf16 v[184:187], v[52:55], v[220:223], v[184:187]\n" \
"buffer_load_dwordx4 v226, s[48:51], s75 offen offset:2048 lds\n" \
"v_mfma_f32_16x16x32_bf16 v[180:183], v[40:43], v[216:219], v[180:183]\n" \
"ds_read_b64_tr_b16 v[30:31], v233 offset:10752\n" \
"v_mul_f32 v243, s76, v243\n" \
"v_mfma_f32_16x16x32_bf16 v[180:183], v[44:47], v[220:223], v[180:183]\n" \
"v_mul_f32 v252, s76, v252\n" \
"v_mul_f32 v253, s76, v253\n" \
"v_mfma_f32_16x16x32_bf16 v[176:179], v[32:35], v[216:219], v[176:179]\n" \
"v_mul_f32 v254, s76, v254\n" \
"v_cvt_pk_bf16_f32 v243, v243, v252\n" \
"v_mfma_f32_16x16x32_bf16 v[176:179], v[36:39], v[220:223], v[176:179]\n" \
"buffer_load_dwordx4 v227, s[48:51], s75 offen offset:3072 lds\n" \
"v_mfma_f32_16x16x32_bf16 v[64:67], v[0:3], v[192:195], v[64:67]\n" \
"s_xor_b32 s75, s75, 0x80\n" \
"ds_read_b128 v[216:219], v230 offset:6144\n" \
"v_mfma_f32_16x16x32_bf16 v[64:67], v[4:7], v[196:199], v[64:67]\n" \
"s_add_u32 m0, s74, 102400\n" \
"ds_read_b128 v[220:223], v231 offset:6144\n" \
"v_cvt_pk_bf16_f32 v253, v253, v254\n" \
"v_mfma_f32_16x16x32_bf16 v[68:71], v[8:11], v[192:195], v[68:71]\n" \
"s_waitcnt lgkmcnt(4)\n" \
"ds_read_b64_tr_b16 v[32:33], v232 offset:1024\n" \
"v_accvgpr_write_b32 a138, v243\n" \
"v_mfma_f32_16x16x32_bf16 v[68:71], v[12:15], v[196:199], v[68:71]\n" \
"buffer_load_dwordx4 v224, s[48:51], s77 offen offset:0 lds\n" \
"v_mfma_f32_16x16x32_bf16 v[72:75], v[16:19], v[192:195], v[72:75]\n" \
"ds_read_b64_tr_b16 v[34:35], v232 offset:3072\n" \
"v_accvgpr_write_b32 a139, v253\n" \
"v_mfma_f32_16x16x32_bf16 v[72:75], v[20:23], v[196:199], v[72:75]\n" \
"s_waitcnt lgkmcnt(3)\n" \
"ds_read_b64_tr_b16 v[36:37], v232 offset:9216\n" \
"v_accvgpr_read_b32 v243, a168\n" \
"v_mfma_f32_16x16x32_bf16 v[76:79], v[24:27], v[192:195], v[76:79]\n" \
"ds_read_b64_tr_b16 v[38:39], v232 offset:11264\n" \
"v_accvgpr_read_b32 v252, a169\n" \
"v_mfma_f32_16x16x32_bf16 v[76:79], v[28:31], v[196:199], v[76:79]\n" \
"buffer_load_dwordx4 v225, s[48:51], s77 offen offset:1024 lds\n" \
"v_mfma_f32_16x16x32_bf16 v[108:111], v[24:27], v[200:203], v[108:111]\n" \
"ds_read_b64_tr_b16 v[40:41], v233 offset:1024\n" \
"v_accvgpr_read_b32 v253, a170\n" \
"v_mfma_f32_16x16x32_bf16 v[108:111], v[28:31], v[204:207], v[108:111]\n" \
"ds_read_b64_tr_b16 v[42:43], v233 offset:3072\n" \
"v_accvgpr_read_b32 v254, a171\n" \
"v_mfma_f32_16x16x32_bf16 v[104:107], v[16:19], v[200:203], v[104:107]\n" \
"s_waitcnt lgkmcnt(6)\n" \
"ds_read_b64_tr_b16 v[44:45], v233 offset:9216\n" \
"v_mul_f32 v243, s76, v243\n" \
"v_mfma_f32_16x16x32_bf16 v[104:107], v[20:23], v[204:207], v[104:107]\n" \
"buffer_load_dwordx4 v226, s[48:51], s77 offen offset:2048 lds\n" \
"v_mfma_f32_16x16x32_bf16 v[100:103], v[8:11], v[200:203], v[100:103]\n" \
"ds_read_b64_tr_b16 v[46:47], v233 offset:11264\n" \
"v_mul_f32 v252, s76, v252\n" \
"v_mfma_f32_16x16x32_bf16 v[100:103], v[12:15], v[204:207], v[100:103]\n" \
"ds_read_b64_tr_b16 v[48:49], v232 offset:1536\n" \
"v_mul_f32 v253, s76, v253\n" \
"v_mfma_f32_16x16x32_bf16 v[96:99], v[0:3], v[200:203], v[96:99]\n" \
"ds_read_b64_tr_b16 v[50:51], v232 offset:3584\n" \
"v_mul_f32 v254, s76, v254\n" \
"v_mfma_f32_16x16x32_bf16 v[96:99], v[4:7], v[204:207], v[96:99]\n" \
"buffer_load_dwordx4 v227, s[48:51], s77 offen offset:3072 lds\n" \
"v_mfma_f32_16x16x32_bf16 v[128:131], v[0:3], v[208:211], v[128:131]\n" \
"ds_read_b64_tr_b16 v[52:53], v232 offset:9728\n" \
"v_cvt_pk_bf16_f32 v243, v243, v252\n" \
"v_mfma_f32_16x16x32_bf16 v[128:131], v[4:7], v[212:215], v[128:131]\n" \
"ds_read_b64_tr_b16 v[54:55], v232 offset:11776\n" \
"v_cvt_pk_bf16_f32 v253, v253, v254\n" \
"v_mfma_f32_16x16x32_bf16 v[132:135], v[8:11], v[208:211], v[132:135]\n" \
"s_xor_b32 s77, s77, 0x80\n" \
"ds_read_b64_tr_b16 v[56:57], v233 offset:1536\n" \
"v_accvgpr_write_b32 a168, v243\n" \
"v_mfma_f32_16x16x32_bf16 v[132:135], v[12:15], v[212:215], v[132:135]\n" \
"v_accvgpr_write_b32 a169, v253\n" \
"v_accvgpr_read_b32 v243, a172\n" \
"v_mfma_f32_16x16x32_bf16 v[136:139], v[16:19], v[208:211], v[136:139]\n" \
"ds_read_b64_tr_b16 v[58:59], v233 offset:3584\n" \
"v_accvgpr_read_b32 v252, a173\n" \
"v_mfma_f32_16x16x32_bf16 v[136:139], v[20:23], v[212:215], v[136:139]\n" \
"ds_read_b64_tr_b16 v[60:61], v233 offset:9728\n" \
"v_accvgpr_read_b32 v253, a174\n" \
"v_mfma_f32_16x16x32_bf16 v[140:143], v[24:27], v[208:211], v[140:143]\n" \
"ds_read_b64_tr_b16 v[62:63], v233 offset:11776\n" \
"v_accvgpr_read_b32 v254, a175\n" \
"v_mfma_f32_16x16x32_bf16 v[140:143], v[28:31], v[212:215], v[140:143]\n" \
"buffer_store_dwordx4 a[200:203], v236, s[56:59], s93 offen offset:2048 sc1\n" \
"v_mfma_f32_16x16x32_bf16 v[172:175], v[24:27], v[216:219], v[172:175]\n" \
"v_add_u32 v230, 0x8000, v230\n" \
"v_mul_f32 v243, s76, v243\n" \
"v_mfma_f32_16x16x32_bf16 v[172:175], v[28:31], v[220:223], v[172:175]\n" \
"v_add_u32 v231, 0x8000, v231\n" \
"v_mul_f32 v252, s76, v252\n" \
"v_mfma_f32_16x16x32_bf16 v[168:171], v[16:19], v[216:219], v[168:171]\n" \
"v_add_u32 v232, 0x4000, v232\n" \
"v_mul_f32 v253, s76, v253\n" \
"v_mfma_f32_16x16x32_bf16 v[168:171], v[20:23], v[220:223], v[168:171]\n" \
"v_mul_f32 v254, s76, v254\n" \
"v_cvt_pk_bf16_f32 v243, v243, v252\n" \
"v_mfma_f32_16x16x32_bf16 v[164:167], v[8:11], v[216:219], v[164:167]\n" \
"v_add_u32 v233, 0x4000, v233\n" \
"v_cvt_pk_bf16_f32 v253, v253, v254\n" \
"v_mfma_f32_16x16x32_bf16 v[164:167], v[12:15], v[220:223], v[164:167]\n" \
"v_accvgpr_write_b32 a170, v243\n" \
"v_accvgpr_write_b32 a171, v253\n" \
"v_mfma_f32_16x16x32_bf16 v[160:163], v[0:3], v[216:219], v[160:163]\n" \
"v_accvgpr_read_b32 v243, a208\n" \
"v_accvgpr_read_b32 v252, a209\n" \
"v_mfma_f32_16x16x32_bf16 v[160:163], v[4:7], v[220:223], v[160:163]\n" \
"s_waitcnt vmcnt(35) lgkmcnt(0)\n" \
"s_barrier\n" \
"v_mfma_f32_16x16x32_bf16 v[80:83], v[32:35], v[192:195], v[80:83]\n" \
"ds_read_b64_tr_b16 v[0:1], v232 offset:0\n" \
"v_accvgpr_read_b32 v253, a210\n" \
"v_mfma_f32_16x16x32_bf16 v[80:83], v[36:39], v[196:199], v[80:83]\n" \
"ds_read_b64_tr_b16 v[2:3], v232 offset:2048\n" \
"v_accvgpr_read_b32 v254, a211\n" \
"v_mfma_f32_16x16x32_bf16 v[84:87], v[40:43], v[192:195], v[84:87]\n" \
"s_add_u32 m0, s79, 32768\n" \
"ds_read_b64_tr_b16 v[4:5], v232 offset:8192\n" \
"v_cvt_pk_bf16_f32 v243, v243, v252\n" \
"v_mfma_f32_16x16x32_bf16 v[84:87], v[44:47], v[196:199], v[84:87]\n" \
"buffer_load_dwordx4 v228, s[52:55], s78 offen offset:0 lds\n" \
"v_mfma_f32_16x16x32_bf16 v[88:91], v[48:51], v[192:195], v[88:91]\n" \
"ds_read_b64_tr_b16 v[6:7], v232 offset:10240\n" \
"v_cvt_pk_bf16_f32 v253, v253, v254\n" \
"v_mfma_f32_16x16x32_bf16 v[88:91], v[52:55], v[196:199], v[88:91]\n" \
"ds_read_b64_tr_b16 v[8:9], v233 offset:0\n" \
"v_accvgpr_write_b32 a208, v243\n" \
"v_mfma_f32_16x16x32_bf16 v[92:95], v[56:59], v[192:195], v[92:95]\n" \
"ds_read_b64_tr_b16 v[10:11], v233 offset:2048\n" \
"v_accvgpr_write_b32 a209, v253\n" \
"v_mfma_f32_16x16x32_bf16 v[92:95], v[60:63], v[196:199], v[92:95]\n" \
"buffer_load_dwordx4 v229, s[52:55], s78 offen offset:1024 lds\n" \
"v_mfma_f32_16x16x32_bf16 v[124:127], v[56:59], v[200:203], v[124:127]\n" \
"s_waitcnt vmcnt(35)\n" \
"ds_read_b128 v[192:195], v230 offset:0\n" \
"v_accvgpr_read_b32 v243, a240\n" \
"v_mfma_f32_16x16x32_bf16 v[124:127], v[60:63], v[204:207], v[124:127]\n" \
"ds_read_b128 v[196:199], v231 offset:0\n" \
"v_accvgpr_read_b32 v252, a241\n" \
"v_mfma_f32_16x16x32_bf16 v[120:123], v[48:51], v[200:203], v[120:123]\n" \
"s_xor_b32 s78, s78, 0x180000\n" \
"ds_read_b64_tr_b16 v[12:13], v233 offset:8192\n" \
"v_accvgpr_read_b32 v253, a242\n" \
"v_mfma_f32_16x16x32_bf16 v[120:123], v[52:55], v[204:207], v[120:123]\n" \
"buffer_load_dwordx4 v228, s[52:55], s89 offen offset:2048 lds\n" \
"v_mfma_f32_16x16x32_bf16 v[116:119], v[40:43], v[200:203], v[116:119]\n" \
"ds_read_b64_tr_b16 v[14:15], v233 offset:10240\n" \
"v_accvgpr_read_b32 v254, a243\n" \
"v_mfma_f32_16x16x32_bf16 v[116:119], v[44:47], v[204:207], v[116:119]\n" \
"ds_read_b64_tr_b16 v[16:17], v232 offset:512\n" \
"v_cvt_pk_bf16_f32 v243, v243, v252\n" \
"v_mfma_f32_16x16x32_bf16 v[112:115], v[32:35], v[200:203], v[112:115]\n" \
"ds_read_b64_tr_b16 v[18:19], v232 offset:2560\n" \
"v_cvt_pk_bf16_f32 v253, v253, v254\n" \
"v_mfma_f32_16x16x32_bf16 v[112:115], v[36:39], v[204:207], v[112:115]\n" \
"buffer_load_dwordx4 v229, s[52:55], s89 offen offset:3072 lds\n" \
"v_mfma_f32_16x16x32_bf16 v[144:147], v[32:35], v[208:211], v[144:147]\n" \
"s_waitcnt vmcnt(35)\n" \
"s_xor_b32 s89, s89, 0x180000\n" \
"ds_read_b128 v[200:203], v230 offset:2048\n" \
"v_mfma_f32_16x16x32_bf16 v[144:147], v[36:39], v[212:215], v[144:147]\n" \
"s_add_u32 m0, s74, 131072\n" \
"ds_read_b128 v[204:207], v231 offset:2048\n" \
"v_accvgpr_write_b32 a210, v243\n" \
"v_mfma_f32_16x16x32_bf16 v[148:151], v[40:43], v[208:211], v[148:151]\n" \
"s_waitcnt lgkmcnt(6)\n" \
"ds_read_b64_tr_b16 v[20:21], v232 offset:8704\n" \
"v_accvgpr_write_b32 a211, v253\n" \
"v_mfma_f32_16x16x32_bf16 v[148:151], v[44:47], v[212:215], v[148:151]\n" \
"buffer_load_dwordx4 v224, s[48:51], s75 offen offset:0 lds\n" \
"v_mfma_f32_16x16x32_bf16 v[152:155], v[48:51], v[208:211], v[152:155]\n" \
"ds_read_b64_tr_b16 v[22:23], v232 offset:10752\n" \
"v_accvgpr_read_b32 v243, a212\n" \
"v_mfma_f32_16x16x32_bf16 v[152:155], v[52:55], v[212:215], v[152:155]\n" \
"ds_read_b64_tr_b16 v[24:25], v233 offset:512\n" \
"v_accvgpr_read_b32 v252, a213\n" \
"v_mfma_f32_16x16x32_bf16 v[156:159], v[56:59], v[208:211], v[156:159]\n" \
"ds_read_b64_tr_b16 v[26:27], v233 offset:2560\n" \
"v_accvgpr_read_b32 v253, a214\n" \
"v_mfma_f32_16x16x32_bf16 v[156:159], v[60:63], v[212:215], v[156:159]\n" \
"buffer_load_dwordx4 v225, s[48:51], s75 offen offset:1024 lds\n" \
"v_mfma_f32_16x16x32_bf16 v[188:191], v[56:59], v[216:219], v[188:191]\n" \
"s_waitcnt vmcnt(35)\n" \
"ds_read_b128 v[208:211], v230 offset:4096\n" \
"v_accvgpr_read_b32 v254, a215\n" \
"v_mfma_f32_16x16x32_bf16 v[188:191], v[60:63], v[220:223], v[188:191]\n" \
"ds_read_b128 v[212:215], v231 offset:4096\n" \
"v_cvt_pk_bf16_f32 v243, v243, v252\n" \
"v_mfma_f32_16x16x32_bf16 v[184:187], v[48:51], v[216:219], v[184:187]\n" \
"s_waitcnt lgkmcnt(6)\n" \
"ds_read_b64_tr_b16 v[28:29], v233 offset:8704\n" \
"v_cvt_pk_bf16_f32 v253, v253, v254\n" \
"v_mfma_f32_16x16x32_bf16 v[184:187], v[52:55], v[220:223], v[184:187]\n" \
"buffer_load_dwordx4 v226, s[48:51], s75 offen offset:2048 lds\n" \
"v_mfma_f32_16x16x32_bf16 v[180:183], v[40:43], v[216:219], v[180:183]\n" \
"ds_read_b64_tr_b16 v[30:31], v233 offset:10752\n" \
"v_accvgpr_write_b32 a212, v243\n" \
"v_mfma_f32_16x16x32_bf16 v[180:183], v[44:47], v[220:223], v[180:183]\n" \
"v_accvgpr_write_b32 a213, v253\n" \
"v_accvgpr_read_b32 v243, a244\n" \
"v_mfma_f32_16x16x32_bf16 v[176:179], v[32:35], v[216:219], v[176:179]\n" \
"v_accvgpr_read_b32 v252, a245\n" \
"v_accvgpr_read_b32 v253, a246\n" \
"v_mfma_f32_16x16x32_bf16 v[176:179], v[36:39], v[220:223], v[176:179]\n" \
"buffer_load_dwordx4 v227, s[48:51], s75 offen offset:3072 lds\n" \
"v_mfma_f32_16x16x32_bf16 v[64:67], v[0:3], v[192:195], v[64:67]\n" \
"s_waitcnt vmcnt(35)\n" \
"s_xor_b32 s75, s75, 0x180\n" \
"ds_read_b128 v[216:219], v230 offset:6144\n" \
"v_mfma_f32_16x16x32_bf16 v[64:67], v[4:7], v[196:199], v[64:67]\n" \
"s_add_u32 m0, s74, 135168\n" \
"ds_read_b128 v[220:223], v231 offset:6144\n" \
"v_accvgpr_read_b32 v254, a247\n" \
"v_mfma_f32_16x16x32_bf16 v[68:71], v[8:11], v[192:195], v[68:71]\n" \
"s_waitcnt lgkmcnt(4)\n" \
"ds_read_b64_tr_b16 v[32:33], v232 offset:1024\n" \
"v_cvt_pk_bf16_f32 v243, v243, v252\n" \
"v_mfma_f32_16x16x32_bf16 v[68:71], v[12:15], v[196:199], v[68:71]\n" \
"buffer_load_dwordx4 v224, s[48:51], s77 offen offset:0 lds\n" \
"v_mfma_f32_16x16x32_bf16 v[72:75], v[16:19], v[192:195], v[72:75]\n" \
"ds_read_b64_tr_b16 v[34:35], v232 offset:3072\n" \
"v_cvt_pk_bf16_f32 v253, v253, v254\n" \
"v_mfma_f32_16x16x32_bf16 v[72:75], v[20:23], v[196:199], v[72:75]\n" \
"s_waitcnt lgkmcnt(3)\n" \
"ds_read_b64_tr_b16 v[36:37], v232 offset:9216\n" \
"v_accvgpr_write_b32 a214, v243\n" \
"v_mfma_f32_16x16x32_bf16 v[76:79], v[24:27], v[192:195], v[76:79]\n" \
"ds_read_b64_tr_b16 v[38:39], v232 offset:11264\n" \
"v_accvgpr_write_b32 a215, v253\n" \
"v_mfma_f32_16x16x32_bf16 v[76:79], v[28:31], v[196:199], v[76:79]\n" \
"buffer_load_dwordx4 v225, s[48:51], s77 offen offset:1024 lds\n" \
"v_mfma_f32_16x16x32_bf16 v[108:111], v[24:27], v[200:203], v[108:111]\n" \
"ds_read_b64_tr_b16 v[40:41], v233 offset:1024\n" \
"v_accvgpr_read_b32 v243, a216\n" \
"v_mfma_f32_16x16x32_bf16 v[108:111], v[28:31], v[204:207], v[108:111]\n" \
"ds_read_b64_tr_b16 v[42:43], v233 offset:3072\n" \
"v_accvgpr_read_b32 v252, a217\n" \
"v_mfma_f32_16x16x32_bf16 v[104:107], v[16:19], v[200:203], v[104:107]\n" \
"s_waitcnt lgkmcnt(6)\n" \
"ds_read_b64_tr_b16 v[44:45], v233 offset:9216\n" \
"v_accvgpr_read_b32 v253, a218\n" \
"v_mfma_f32_16x16x32_bf16 v[104:107], v[20:23], v[204:207], v[104:107]\n" \
"buffer_load_dwordx4 v226, s[48:51], s77 offen offset:2048 lds\n" \
"Lpg2723_%=:\n" \
"s_cbranch_execz Lpg4941_%=\n" \
"v_mfma_f32_16x16x32_bf16 v[100:103], v[8:11], v[200:203], v[100:103]\n" \
"ds_read_b64_tr_b16 v[46:47], v233 offset:11264\n" \
"v_accvgpr_read_b32 v254, a219\n" \
"v_mfma_f32_16x16x32_bf16 v[100:103], v[12:15], v[204:207], v[100:103]\n" \
"ds_read_b64_tr_b16 v[48:49], v232 offset:1536\n" \
"v_cvt_pk_bf16_f32 v243, v243, v252\n" \
"v_mfma_f32_16x16x32_bf16 v[96:99], v[0:3], v[200:203], v[96:99]\n" \
"ds_read_b64_tr_b16 v[50:51], v232 offset:3584\n" \
"v_cvt_pk_bf16_f32 v253, v253, v254\n" \
"v_mfma_f32_16x16x32_bf16 v[96:99], v[4:7], v[204:207], v[96:99]\n" \
"buffer_load_dwordx4 v227, s[48:51], s77 offen offset:3072 lds\n" \
"v_mfma_f32_16x16x32_bf16 v[128:131], v[0:3], v[208:211], v[128:131]\n" \
"ds_read_b64_tr_b16 v[52:53], v232 offset:9728\n" \
"v_accvgpr_write_b32 a216, v243\n" \
"v_mfma_f32_16x16x32_bf16 v[128:131], v[4:7], v[212:215], v[128:131]\n" \
"ds_read_b64_tr_b16 v[54:55], v232 offset:11776\n" \
"v_accvgpr_write_b32 a217, v253\n" \
"v_mfma_f32_16x16x32_bf16 v[132:135], v[8:11], v[208:211], v[132:135]\n" \
"s_xor_b32 s77, s77, 0x180\n" \
"ds_read_b64_tr_b16 v[56:57], v233 offset:1536\n" \
"v_accvgpr_read_b32 v243, a248\n" \
"v_mfma_f32_16x16x32_bf16 v[132:135], v[12:15], v[212:215], v[132:135]\n" \
"buffer_store_dwordx4 a[232:235], v236, s[56:59], s93 offen offset:2304 sc1\n" \
"v_mfma_f32_16x16x32_bf16 v[136:139], v[16:19], v[208:211], v[136:139]\n" \
"ds_read_b64_tr_b16 v[58:59], v233 offset:3584\n" \
"v_accvgpr_read_b32 v252, a249\n" \
"v_mfma_f32_16x16x32_bf16 v[136:139], v[20:23], v[212:215], v[136:139]\n" \
"ds_read_b64_tr_b16 v[60:61], v233 offset:9728\n" \
"v_accvgpr_read_b32 v253, a250\n" \
"v_mfma_f32_16x16x32_bf16 v[140:143], v[24:27], v[208:211], v[140:143]\n" \
"ds_read_b64_tr_b16 v[62:63], v233 offset:11776\n" \
"v_accvgpr_read_b32 v254, a251\n" \
"v_mfma_f32_16x16x32_bf16 v[140:143], v[28:31], v[212:215], v[140:143]\n" \
"v_cvt_pk_bf16_f32 v243, v243, v252\n" \
"v_cvt_pk_bf16_f32 v253, v253, v254\n" \
"v_mfma_f32_16x16x32_bf16 v[172:175], v[24:27], v[216:219], v[172:175]\n" \
"v_add_u32 v230, 0xffff0000, v230\n" \
"v_accvgpr_write_b32 a218, v243\n" \
"v_mfma_f32_16x16x32_bf16 v[172:175], v[28:31], v[220:223], v[172:175]\n" \
"v_add_u32 v231, 0xffff0000, v231\n" \
"v_accvgpr_write_b32 a219, v253\n" \
"v_mfma_f32_16x16x32_bf16 v[168:171], v[16:19], v[216:219], v[168:171]\n" \
"s_add_u32 s93, s87, 0\n" \
"v_add_u32 v232, 0xffff4000, v232\n" \
"v_accvgpr_read_b32 v243, a220\n" \
"v_mfma_f32_16x16x32_bf16 v[168:171], v[20:23], v[220:223], v[168:171]\n" \
"buffer_store_dwordx4 a[128:131], v236, s[56:59], s93 offen offset:0 sc1\n" \
"v_mfma_f32_16x16x32_bf16 v[164:167], v[8:11], v[216:219], v[164:167]\n" \
"v_add_u32 v233, 0xffff4000, v233\n" \
"v_accvgpr_read_b32 v252, a221\n" \
"v_mfma_f32_16x16x32_bf16 v[164:167], v[12:15], v[220:223], v[164:167]\n" \
"v_accvgpr_read_b32 v253, a222\n" \
"v_accvgpr_read_b32 v254, a223\n" \
"v_mfma_f32_16x16x32_bf16 v[160:163], v[0:3], v[216:219], v[160:163]\n" \
"v_cvt_pk_bf16_f32 v243, v243, v252\n" \
"v_cvt_pk_bf16_f32 v253, v253, v254\n" \
"v_mfma_f32_16x16x32_bf16 v[160:163], v[4:7], v[220:223], v[160:163]\n" \
"s_waitcnt vmcnt(37) lgkmcnt(0)\n" \
"s_barrier\n" \
"v_mfma_f32_16x16x32_bf16 v[80:83], v[32:35], v[192:195], v[80:83]\n" \
"ds_read_b64_tr_b16 v[0:1], v232 offset:0\n" \
"v_accvgpr_write_b32 a220, v243\n" \
"v_mfma_f32_16x16x32_bf16 v[80:83], v[36:39], v[196:199], v[80:83]\n" \
"ds_read_b64_tr_b16 v[2:3], v232 offset:2048\n" \
"v_accvgpr_write_b32 a221, v253\n" \
"v_mfma_f32_16x16x32_bf16 v[84:87], v[40:43], v[192:195], v[84:87]\n" \
"s_add_u32 m0, s79, 49152\n" \
"ds_read_b64_tr_b16 v[4:5], v232 offset:8192\n" \
"v_accvgpr_read_b32 v243, a252\n" \
"v_mfma_f32_16x16x32_bf16 v[84:87], v[44:47], v[196:199], v[84:87]\n" \
"buffer_load_dwordx4 v228, s[52:55], s78 offen offset:0 lds\n" \
"v_mfma_f32_16x16x32_bf16 v[88:91], v[48:51], v[192:195], v[88:91]\n" \
"ds_read_b64_tr_b16 v[6:7], v232 offset:10240\n" \
"v_accvgpr_read_b32 v252, a253\n" \
"v_mfma_f32_16x16x32_bf16 v[88:91], v[52:55], v[196:199], v[88:91]\n" \
"ds_read_b64_tr_b16 v[8:9], v233 offset:0\n" \
"v_accvgpr_read_b32 v253, a254\n" \
"v_mfma_f32_16x16x32_bf16 v[92:95], v[56:59], v[192:195], v[92:95]\n" \
"ds_read_b64_tr_b16 v[10:11], v233 offset:2048\n" \
"v_accvgpr_read_b32 v254, a255\n" \
"v_mfma_f32_16x16x32_bf16 v[92:95], v[60:63], v[196:199], v[92:95]\n" \
"buffer_load_dwordx4 v229, s[52:55], s78 offen offset:1024 lds\n" \
"v_mfma_f32_16x16x32_bf16 v[124:127], v[56:59], v[200:203], v[124:127]\n" \
"s_waitcnt vmcnt(37)\n" \
"ds_read_b128 v[192:195], v230 offset:0\n" \
"v_cvt_pk_bf16_f32 v243, v243, v252\n" \
"v_mfma_f32_16x16x32_bf16 v[124:127], v[60:63], v[204:207], v[124:127]\n" \
"ds_read_b128 v[196:199], v231 offset:0\n" \
"v_cvt_pk_bf16_f32 v253, v253, v254\n" \
"v_mfma_f32_16x16x32_bf16 v[120:123], v[48:51], v[200:203], v[120:123]\n" \
"s_xor_b32 s78, s78, 0x80000\n" \
"ds_read_b64_tr_b16 v[12:13], v233 offset:8192\n" \
"v_accvgpr_write_b32 a222, v243\n" \
"v_mfma_f32_16x16x32_bf16 v[120:123], v[52:55], v[204:207], v[120:123]\n" \
"buffer_load_dwordx4 v228, s[52:55], s89 offen offset:2048 lds\n" \
"v_mfma_f32_16x16x32_bf16 v[116:119], v[40:43], v[200:203], v[116:119]\n" \
"ds_read_b64_tr_b16 v[14:15], v233 offset:10240\n" \
"v_accvgpr_write_b32 a223, v253\n" \
"v_mfma_f32_16x16x32_bf16 v[116:119], v[44:47], v[204:207], v[116:119]\n" \
"ds_read_b64_tr_b16 v[16:17], v232 offset:512\n" \
"v_accvgpr_read_b32 v243, a144\n" \
"v_mfma_f32_16x16x32_bf16 v[112:115], v[32:35], v[200:203], v[112:115]\n" \
"ds_read_b64_tr_b16 v[18:19], v232 offset:2560\n" \
"v_accvgpr_read_b32 v252, a145\n" \
"v_mfma_f32_16x16x32_bf16 v[112:115], v[36:39], v[204:207], v[112:115]\n" \
"buffer_load_dwordx4 v229, s[52:55], s89 offen offset:3072 lds\n" \
"v_mfma_f32_16x16x32_bf16 v[144:147], v[32:35], v[208:211], v[144:147]\n" \
"s_waitcnt vmcnt(37)\n" \
"s_xor_b32 s89, s89, 0x80000\n" \
"ds_read_b128 v[200:203], v230 offset:2048\n" \
"v_mfma_f32_16x16x32_bf16 v[144:147], v[36:39], v[212:215], v[144:147]\n" \
"s_add_u32 m0, s74, 65536\n" \
"ds_read_b128 v[204:207], v231 offset:2048\n" \
"v_accvgpr_read_b32 v253, a146\n" \
"v_mfma_f32_16x16x32_bf16 v[148:151], v[40:43], v[208:211], v[148:151]\n" \
"s_waitcnt lgkmcnt(6)\n" \
"ds_read_b64_tr_b16 v[20:21], v232 offset:8704\n" \
"v_accvgpr_read_b32 v254, a147\n" \
"v_mfma_f32_16x16x32_bf16 v[148:151], v[44:47], v[212:215], v[148:151]\n" \
"buffer_load_dwordx4 v224, s[48:51], s75 offen offset:0 lds\n" \
"v_mfma_f32_16x16x32_bf16 v[152:155], v[48:51], v[208:211], v[152:155]\n" \
"ds_read_b64_tr_b16 v[22:23], v232 offset:10752\n" \
"v_cvt_pk_bf16_f32 v243, v243, v252\n" \
"v_mfma_f32_16x16x32_bf16 v[152:155], v[52:55], v[212:215], v[152:155]\n" \
"ds_read_b64_tr_b16 v[24:25], v233 offset:512\n" \
"v_cvt_pk_bf16_f32 v253, v253, v254\n" \
"v_mfma_f32_16x16x32_bf16 v[156:159], v[56:59], v[208:211], v[156:159]\n" \
"ds_read_b64_tr_b16 v[26:27], v233 offset:2560\n" \
"v_accvgpr_write_b32 a144, v243\n" \
"v_mfma_f32_16x16x32_bf16 v[156:159], v[60:63], v[212:215], v[156:159]\n" \
"buffer_load_dwordx4 v225, s[48:51], s75 offen offset:1024 lds\n" \
"v_mfma_f32_16x16x32_bf16 v[188:191], v[56:59], v[216:219], v[188:191]\n" \
"s_waitcnt vmcnt(37)\n" \
"ds_read_b128 v[208:211], v230 offset:4096\n" \
"v_accvgpr_write_b32 a145, v253\n" \
"v_mfma_f32_16x16x32_bf16 v[188:191], v[60:63], v[220:223], v[188:191]\n" \
"ds_read_b128 v[212:215], v231 offset:4096\n" \
"v_accvgpr_read_b32 v243, a176\n" \
"v_mfma_f32_16x16x32_bf16 v[184:187], v[48:51], v[216:219], v[184:187]\n" \
"s_waitcnt lgkmcnt(6)\n" \
"ds_read_b64_tr_b16 v[28:29], v233 offset:8704\n" \
"v_accvgpr_read_b32 v252, a177\n" \
"v_mfma_f32_16x16x32_bf16 v[184:187], v[52:55], v[220:223], v[184:187]\n" \
"buffer_load_dwordx4 v226, s[48:51], s75 offen offset:2048 lds\n" \
"v_mfma_f32_16x16x32_bf16 v[180:183], v[40:43], v[216:219], v[180:183]\n" \
"ds_read_b64_tr_b16 v[30:31], v233 offset:10752\n" \
"v_accvgpr_read_b32 v253, a178\n" \
"v_mfma_f32_16x16x32_bf16 v[180:183], v[44:47], v[220:223], v[180:183]\n" \
"v_accvgpr_read_b32 v254, a179\n" \
"v_cvt_pk_bf16_f32 v243, v243, v252\n" \
"v_mfma_f32_16x16x32_bf16 v[176:179], v[32:35], v[216:219], v[176:179]\n" \
"v_cvt_pk_bf16_f32 v253, v253, v254\n" \
"v_accvgpr_write_b32 a146, v243\n" \
"v_mfma_f32_16x16x32_bf16 v[176:179], v[36:39], v[220:223], v[176:179]\n" \
"buffer_load_dwordx4 v227, s[48:51], s75 offen offset:3072 lds\n" \
"v_mfma_f32_16x16x32_bf16 v[64:67], v[0:3], v[192:195], v[64:67]\n" \
"s_waitcnt vmcnt(37)\n" \
"s_xor_b32 s75, s75, 0x80\n" \
"ds_read_b128 v[216:219], v230 offset:6144\n" \
"v_mfma_f32_16x16x32_bf16 v[64:67], v[4:7], v[196:199], v[64:67]\n" \
"s_add_u32 m0, s74, 69632\n" \
"ds_read_b128 v[220:223], v231 offset:6144\n" \
"v_accvgpr_write_b32 a147, v253\n" \
"v_mfma_f32_16x16x32_bf16 v[68:71], v[8:11], v[192:195], v[68:71]\n" \
"s_waitcnt lgkmcnt(4)\n" \
"ds_read_b64_tr_b16 v[32:33], v232 offset:1024\n" \
"v_accvgpr_read_b32 v243, a148\n" \
"v_mfma_f32_16x16x32_bf16 v[68:71], v[12:15], v[196:199], v[68:71]\n" \
"buffer_load_dwordx4 v224, s[48:51], s77 offen offset:0 lds\n" \
"v_mfma_f32_16x16x32_bf16 v[72:75], v[16:19], v[192:195], v[72:75]\n" \
"ds_read_b64_tr_b16 v[34:35], v232 offset:3072\n" \
"v_accvgpr_read_b32 v252, a149\n" \
"v_mfma_f32_16x16x32_bf16 v[72:75], v[20:23], v[196:199], v[72:75]\n" \
"s_waitcnt lgkmcnt(3)\n" \
"ds_read_b64_tr_b16 v[36:37], v232 offset:9216\n" \
"v_accvgpr_read_b32 v253, a150\n" \
"v_mfma_f32_16x16x32_bf16 v[76:79], v[24:27], v[192:195], v[76:79]\n" \
"ds_read_b64_tr_b16 v[38:39], v232 offset:11264\n" \
"v_accvgpr_read_b32 v254, a151\n" \
"v_mfma_f32_16x16x32_bf16 v[76:79], v[28:31], v[196:199], v[76:79]\n" \
"buffer_load_dwordx4 v225, s[48:51], s77 offen offset:1024 lds\n" \
"v_mfma_f32_16x16x32_bf16 v[108:111], v[24:27], v[200:203], v[108:111]\n" \
"ds_read_b64_tr_b16 v[40:41], v233 offset:1024\n" \
"v_cvt_pk_bf16_f32 v243, v243, v252\n" \
"v_mfma_f32_16x16x32_bf16 v[108:111], v[28:31], v[204:207], v[108:111]\n" \
"ds_read_b64_tr_b16 v[42:43], v233 offset:3072\n" \
"v_cvt_pk_bf16_f32 v253, v253, v254\n" \
"v_mfma_f32_16x16x32_bf16 v[104:107], v[16:19], v[200:203], v[104:107]\n" \
"s_waitcnt lgkmcnt(6)\n" \
"ds_read_b64_tr_b16 v[44:45], v233 offset:9216\n" \
"v_accvgpr_write_b32 a148, v243\n" \
"v_mfma_f32_16x16x32_bf16 v[104:107], v[20:23], v[204:207], v[104:107]\n" \
"buffer_load_dwordx4 v226, s[48:51], s77 offen offset:2048 lds\n" \
"v_mfma_f32_16x16x32_bf16 v[100:103], v[8:11], v[200:203], v[100:103]\n" \
"ds_read_b64_tr_b16 v[46:47], v233 offset:11264\n" \
"v_accvgpr_write_b32 a149, v253\n" \
"v_mfma_f32_16x16x32_bf16 v[100:103], v[12:15], v[204:207], v[100:103]\n" \
"ds_read_b64_tr_b16 v[48:49], v232 offset:1536\n" \
"v_accvgpr_read_b32 v243, a180\n" \
"v_mfma_f32_16x16x32_bf16 v[96:99], v[0:3], v[200:203], v[96:99]\n" \
"ds_read_b64_tr_b16 v[50:51], v232 offset:3584\n" \
"v_accvgpr_read_b32 v252, a181\n" \
"v_mfma_f32_16x16x32_bf16 v[96:99], v[4:7], v[204:207], v[96:99]\n" \
"buffer_load_dwordx4 v227, s[48:51], s77 offen offset:3072 lds\n" \
"v_mfma_f32_16x16x32_bf16 v[128:131], v[0:3], v[208:211], v[128:131]\n" \
"ds_read_b64_tr_b16 v[52:53], v232 offset:9728\n" \
"v_accvgpr_read_b32 v253, a182\n" \
"v_mfma_f32_16x16x32_bf16 v[128:131], v[4:7], v[212:215], v[128:131]\n" \
"ds_read_b64_tr_b16 v[54:55], v232 offset:11776\n" \
"v_accvgpr_read_b32 v254, a183\n" \
"v_mfma_f32_16x16x32_bf16 v[132:135], v[8:11], v[208:211], v[132:135]\n" \
"s_xor_b32 s77, s77, 0x80\n" \
"ds_read_b64_tr_b16 v[56:57], v233 offset:1536\n" \
"v_cvt_pk_bf16_f32 v243, v243, v252\n" \
"v_mfma_f32_16x16x32_bf16 v[132:135], v[12:15], v[212:215], v[132:135]\n" \
"v_cvt_pk_bf16_f32 v253, v253, v254\n" \
"v_accvgpr_write_b32 a150, v243\n" \
"v_mfma_f32_16x16x32_bf16 v[136:139], v[16:19], v[208:211], v[136:139]\n" \
"ds_read_b64_tr_b16 v[58:59], v233 offset:3584\n" \
"v_accvgpr_write_b32 a151, v253\n" \
"v_mfma_f32_16x16x32_bf16 v[136:139], v[20:23], v[212:215], v[136:139]\n" \
"ds_read_b64_tr_b16 v[60:61], v233 offset:9728\n" \
"v_accvgpr_read_b32 v243, a152\n" \
"v_mfma_f32_16x16x32_bf16 v[140:143], v[24:27], v[208:211], v[140:143]\n" \
"ds_read_b64_tr_b16 v[62:63], v233 offset:11776\n" \
"v_accvgpr_read_b32 v252, a153\n" \
"v_mfma_f32_16x16x32_bf16 v[140:143], v[28:31], v[212:215], v[140:143]\n" \
"buffer_store_dwordx4 a[160:163], v236, s[56:59], s93 offen offset:256 sc1\n" \
"v_mfma_f32_16x16x32_bf16 v[172:175], v[24:27], v[216:219], v[172:175]\n" \
"v_add_u32 v230, 0x8000, v230\n" \
"v_accvgpr_read_b32 v253, a154\n" \
"v_mfma_f32_16x16x32_bf16 v[172:175], v[28:31], v[220:223], v[172:175]\n" \
"v_add_u32 v231, 0x8000, v231\n" \
"v_accvgpr_read_b32 v254, a155\n" \
"v_mfma_f32_16x16x32_bf16 v[168:171], v[16:19], v[216:219], v[168:171]\n" \
"v_add_u32 v232, 0x4000, v232\n" \
"v_cvt_pk_bf16_f32 v243, v243, v252\n" \
"v_mfma_f32_16x16x32_bf16 v[168:171], v[20:23], v[220:223], v[168:171]\n" \
"v_cvt_pk_bf16_f32 v253, v253, v254\n" \
"v_accvgpr_write_b32 a152, v243\n" \
"v_mfma_f32_16x16x32_bf16 v[164:167], v[8:11], v[216:219], v[164:167]\n" \
"v_add_u32 v233, 0x4000, v233\n" \
"v_accvgpr_write_b32 a153, v253\n" \
"v_mfma_f32_16x16x32_bf16 v[164:167], v[12:15], v[220:223], v[164:167]\n" \
"v_accvgpr_read_b32 v243, a184\n" \
"v_accvgpr_read_b32 v252, a185\n" \
"v_mfma_f32_16x16x32_bf16 v[160:163], v[0:3], v[216:219], v[160:163]\n" \
"v_accvgpr_read_b32 v253, a186\n" \
"v_accvgpr_read_b32 v254, a187\n" \
"v_mfma_f32_16x16x32_bf16 v[160:163], v[4:7], v[220:223], v[160:163]\n" \
"s_waitcnt vmcnt(36) lgkmcnt(0)\n" \
"s_barrier\n" \
"v_mfma_f32_16x16x32_bf16 v[80:83], v[32:35], v[192:195], v[80:83]\n" \
"ds_read_b64_tr_b16 v[0:1], v232 offset:0\n" \
"v_cvt_pk_bf16_f32 v243, v243, v252\n" \
"v_mfma_f32_16x16x32_bf16 v[80:83], v[36:39], v[196:199], v[80:83]\n" \
"ds_read_b64_tr_b16 v[2:3], v232 offset:2048\n" \
"v_cvt_pk_bf16_f32 v253, v253, v254\n" \
"v_mfma_f32_16x16x32_bf16 v[84:87], v[40:43], v[192:195], v[84:87]\n" \
"s_add_u32 m0, s79, 0\n" \
"ds_read_b64_tr_b16 v[4:5], v232 offset:8192\n" \
"v_accvgpr_write_b32 a154, v243\n" \
"v_mfma_f32_16x16x32_bf16 v[84:87], v[44:47], v[196:199], v[84:87]\n" \
"buffer_load_dwordx4 v228, s[52:55], s78 offen offset:0 lds\n" \
"v_mfma_f32_16x16x32_bf16 v[88:91], v[48:51], v[192:195], v[88:91]\n" \
"ds_read_b64_tr_b16 v[6:7], v232 offset:10240\n" \
"v_accvgpr_write_b32 a155, v253\n" \
"v_mfma_f32_16x16x32_bf16 v[88:91], v[52:55], v[196:199], v[88:91]\n" \
"ds_read_b64_tr_b16 v[8:9], v233 offset:0\n" \
"v_accvgpr_read_b32 v243, a156\n" \
"v_mfma_f32_16x16x32_bf16 v[92:95], v[56:59], v[192:195], v[92:95]\n" \
"ds_read_b64_tr_b16 v[10:11], v233 offset:2048\n" \
"v_accvgpr_read_b32 v252, a157\n" \
"v_mfma_f32_16x16x32_bf16 v[92:95], v[60:63], v[196:199], v[92:95]\n" \
"buffer_load_dwordx4 v229, s[52:55], s78 offen offset:1024 lds\n" \
"v_mfma_f32_16x16x32_bf16 v[124:127], v[56:59], v[200:203], v[124:127]\n" \
"s_waitcnt vmcnt(36)\n" \
"ds_read_b128 v[192:195], v230 offset:0\n" \
"v_accvgpr_read_b32 v253, a158\n" \
"v_mfma_f32_16x16x32_bf16 v[124:127], v[60:63], v[204:207], v[124:127]\n" \
"ds_read_b128 v[196:199], v231 offset:0\n" \
"v_accvgpr_read_b32 v254, a159\n" \
"v_mfma_f32_16x16x32_bf16 v[120:123], v[48:51], v[200:203], v[120:123]\n" \
"s_xor_b32 s78, s78, 0x780000\n" \
"ds_read_b64_tr_b16 v[12:13], v233 offset:8192\n" \
"v_cvt_pk_bf16_f32 v243, v243, v252\n" \
"v_mfma_f32_16x16x32_bf16 v[120:123], v[52:55], v[204:207], v[120:123]\n" \
"buffer_load_dwordx4 v228, s[52:55], s89 offen offset:2048 lds\n" \
"v_mfma_f32_16x16x32_bf16 v[116:119], v[40:43], v[200:203], v[116:119]\n" \
"ds_read_b64_tr_b16 v[14:15], v233 offset:10240\n" \
"v_cvt_pk_bf16_f32 v253, v253, v254\n" \
"v_mfma_f32_16x16x32_bf16 v[116:119], v[44:47], v[204:207], v[116:119]\n" \
"ds_read_b64_tr_b16 v[16:17], v232 offset:512\n" \
"v_accvgpr_write_b32 a156, v243\n" \
"v_mfma_f32_16x16x32_bf16 v[112:115], v[32:35], v[200:203], v[112:115]\n" \
"ds_read_b64_tr_b16 v[18:19], v232 offset:2560\n" \
"v_accvgpr_write_b32 a157, v253\n" \
"v_mfma_f32_16x16x32_bf16 v[112:115], v[36:39], v[204:207], v[112:115]\n" \
"buffer_load_dwordx4 v229, s[52:55], s89 offen offset:3072 lds\n" \
"v_mfma_f32_16x16x32_bf16 v[144:147], v[32:35], v[208:211], v[144:147]\n" \
"s_waitcnt vmcnt(36)\n" \
"s_xor_b32 s89, s89, 0x780000\n" \
"ds_read_b128 v[200:203], v230 offset:2048\n" \
"v_mfma_f32_16x16x32_bf16 v[144:147], v[36:39], v[212:215], v[144:147]\n" \
"s_add_u32 m0, s74, 98304\n" \
"ds_read_b128 v[204:207], v231 offset:2048\n" \
"v_accvgpr_read_b32 v243, a188\n" \
"v_mfma_f32_16x16x32_bf16 v[148:151], v[40:43], v[208:211], v[148:151]\n" \
"s_waitcnt lgkmcnt(6)\n" \
"ds_read_b64_tr_b16 v[20:21], v232 offset:8704\n" \
"v_accvgpr_read_b32 v252, a189\n" \
"v_mfma_f32_16x16x32_bf16 v[148:151], v[44:47], v[212:215], v[148:151]\n" \
"buffer_load_dwordx4 v224, s[48:51], s75 offen offset:0 lds\n" \
"v_mfma_f32_16x16x32_bf16 v[152:155], v[48:51], v[208:211], v[152:155]\n" \
"ds_read_b64_tr_b16 v[22:23], v232 offset:10752\n" \
"v_accvgpr_read_b32 v253, a190\n" \
"v_mfma_f32_16x16x32_bf16 v[152:155], v[52:55], v[212:215], v[152:155]\n" \
"ds_read_b64_tr_b16 v[24:25], v233 offset:512\n" \
"v_accvgpr_read_b32 v254, a191\n" \
"v_mfma_f32_16x16x32_bf16 v[156:159], v[56:59], v[208:211], v[156:159]\n" \
"ds_read_b64_tr_b16 v[26:27], v233 offset:2560\n" \
"v_cvt_pk_bf16_f32 v243, v243, v252\n" \
"v_mfma_f32_16x16x32_bf16 v[156:159], v[60:63], v[212:215], v[156:159]\n" \
"buffer_load_dwordx4 v225, s[48:51], s75 offen offset:1024 lds\n" \
"v_mfma_f32_16x16x32_bf16 v[188:191], v[56:59], v[216:219], v[188:191]\n" \
"s_waitcnt vmcnt(36)\n" \
"ds_read_b128 v[208:211], v230 offset:4096\n" \
"v_cvt_pk_bf16_f32 v253, v253, v254\n" \
"v_mfma_f32_16x16x32_bf16 v[188:191], v[60:63], v[220:223], v[188:191]\n" \
"ds_read_b128 v[212:215], v231 offset:4096\n" \
"v_accvgpr_write_b32 a158, v243\n" \
"v_mfma_f32_16x16x32_bf16 v[184:187], v[48:51], v[216:219], v[184:187]\n" \
"s_waitcnt lgkmcnt(6)\n" \
"ds_read_b64_tr_b16 v[28:29], v233 offset:8704\n" \
"v_accvgpr_write_b32 a159, v253\n" \
"v_mfma_f32_16x16x32_bf16 v[184:187], v[52:55], v[220:223], v[184:187]\n" \
"buffer_load_dwordx4 v226, s[48:51], s75 offen offset:2048 lds\n" \
"v_mfma_f32_16x16x32_bf16 v[180:183], v[40:43], v[216:219], v[180:183]\n" \
"ds_read_b64_tr_b16 v[30:31], v233 offset:10752\n" \
"v_accvgpr_read_b32 v243, a64\n" \
"v_mfma_f32_16x16x32_bf16 v[180:183], v[44:47], v[220:223], v[180:183]\n" \
"v_accvgpr_read_b32 v252, a65\n" \
"v_accvgpr_read_b32 v253, a66\n" \
"v_mfma_f32_16x16x32_bf16 v[176:179], v[32:35], v[216:219], v[176:179]\n" \
"v_accvgpr_read_b32 v254, a67\n" \
"v_mul_f32 v243, s76, v243\n" \
"v_mfma_f32_16x16x32_bf16 v[176:179], v[36:39], v[220:223], v[176:179]\n" \
"buffer_load_dwordx4 v227, s[48:51], s75 offen offset:3072 lds\n" \
"v_mfma_f32_16x16x32_bf16 v[64:67], v[0:3], v[192:195], v[64:67]\n" \
"s_waitcnt vmcnt(36)\n" \
"s_xor_b32 s75, s75, 0x780\n" \
"ds_read_b128 v[216:219], v230 offset:6144\n" \
"v_mfma_f32_16x16x32_bf16 v[64:67], v[4:7], v[196:199], v[64:67]\n" \
"s_add_u32 m0, s74, 102400\n" \
"ds_read_b128 v[220:223], v231 offset:6144\n" \
"v_mul_f32 v252, s76, v252\n" \
"v_mfma_f32_16x16x32_bf16 v[68:71], v[8:11], v[192:195], v[68:71]\n" \
"s_waitcnt lgkmcnt(4)\n" \
"ds_read_b64_tr_b16 v[32:33], v232 offset:1024\n" \
"v_mul_f32 v253, s76, v253\n" \
"v_mfma_f32_16x16x32_bf16 v[68:71], v[12:15], v[196:199], v[68:71]\n" \
"buffer_load_dwordx4 v224, s[48:51], s77 offen offset:0 lds\n" \
"v_mfma_f32_16x16x32_bf16 v[72:75], v[16:19], v[192:195], v[72:75]\n" \
"ds_read_b64_tr_b16 v[34:35], v232 offset:3072\n" \
"v_mul_f32 v254, s76, v254\n" \
"v_mfma_f32_16x16x32_bf16 v[72:75], v[20:23], v[196:199], v[72:75]\n" \
"s_waitcnt lgkmcnt(3)\n" \
"ds_read_b64_tr_b16 v[36:37], v232 offset:9216\n" \
"v_cvt_pk_bf16_f32 v243, v243, v252\n" \
"v_mfma_f32_16x16x32_bf16 v[76:79], v[24:27], v[192:195], v[76:79]\n" \
"ds_read_b64_tr_b16 v[38:39], v232 offset:11264\n" \
"v_cvt_pk_bf16_f32 v253, v253, v254\n" \
"v_mfma_f32_16x16x32_bf16 v[76:79], v[28:31], v[196:199], v[76:79]\n" \
"buffer_load_dwordx4 v225, s[48:51], s77 offen offset:1024 lds\n" \
"v_mfma_f32_16x16x32_bf16 v[108:111], v[24:27], v[200:203], v[108:111]\n" \
"ds_read_b64_tr_b16 v[40:41], v233 offset:1024\n" \
"v_accvgpr_write_b32 a64, v243\n" \
"v_mfma_f32_16x16x32_bf16 v[108:111], v[28:31], v[204:207], v[108:111]\n" \
"ds_read_b64_tr_b16 v[42:43], v233 offset:3072\n" \
"v_accvgpr_write_b32 a65, v253\n" \
"v_mfma_f32_16x16x32_bf16 v[104:107], v[16:19], v[200:203], v[104:107]\n" \
"s_waitcnt lgkmcnt(6)\n" \
"ds_read_b64_tr_b16 v[44:45], v233 offset:9216\n" \
"v_accvgpr_read_b32 v243, a68\n" \
"v_mfma_f32_16x16x32_bf16 v[104:107], v[20:23], v[204:207], v[104:107]\n" \
"buffer_load_dwordx4 v226, s[48:51], s77 offen offset:2048 lds\n" \
"v_mfma_f32_16x16x32_bf16 v[100:103], v[8:11], v[200:203], v[100:103]\n" \
"ds_read_b64_tr_b16 v[46:47], v233 offset:11264\n" \
"v_accvgpr_read_b32 v252, a69\n" \
"v_mfma_f32_16x16x32_bf16 v[100:103], v[12:15], v[204:207], v[100:103]\n" \
"ds_read_b64_tr_b16 v[48:49], v232 offset:1536\n" \
"v_accvgpr_read_b32 v253, a70\n" \
"v_mfma_f32_16x16x32_bf16 v[96:99], v[0:3], v[200:203], v[96:99]\n" \
"ds_read_b64_tr_b16 v[50:51], v232 offset:3584\n" \
"v_accvgpr_read_b32 v254, a71\n" \
"v_mfma_f32_16x16x32_bf16 v[96:99], v[4:7], v[204:207], v[96:99]\n" \
"buffer_load_dwordx4 v227, s[48:51], s77 offen offset:3072 lds\n" \
"v_mfma_f32_16x16x32_bf16 v[128:131], v[0:3], v[208:211], v[128:131]\n" \
"ds_read_b64_tr_b16 v[52:53], v232 offset:9728\n" \
"v_mul_f32 v243, s76, v243\n" \
"v_mfma_f32_16x16x32_bf16 v[128:131], v[4:7], v[212:215], v[128:131]\n" \
"ds_read_b64_tr_b16 v[54:55], v232 offset:11776\n" \
"v_mul_f32 v252, s76, v252\n" \
"v_mfma_f32_16x16x32_bf16 v[132:135], v[8:11], v[208:211], v[132:135]\n" \
"s_xor_b32 s77, s77, 0x780\n" \
"ds_read_b64_tr_b16 v[56:57], v233 offset:1536\n" \
"v_mul_f32 v253, s76, v253\n" \
"v_mfma_f32_16x16x32_bf16 v[132:135], v[12:15], v[212:215], v[132:135]\n" \
"buffer_store_dwordx4 a[136:139], v236, s[56:59], s93 offen offset:2048 sc1\n" \
"v_mfma_f32_16x16x32_bf16 v[136:139], v[16:19], v[208:211], v[136:139]\n" \
"ds_read_b64_tr_b16 v[58:59], v233 offset:3584\n" \
"v_mul_f32 v254, s76, v254\n" \
"v_mfma_f32_16x16x32_bf16 v[136:139], v[20:23], v[212:215], v[136:139]\n" \
"ds_read_b64_tr_b16 v[60:61], v233 offset:9728\n" \
"v_cvt_pk_bf16_f32 v243, v243, v252\n" \
"v_mfma_f32_16x16x32_bf16 v[140:143], v[24:27], v[208:211], v[140:143]\n" \
"ds_read_b64_tr_b16 v[62:63], v233 offset:11776\n" \
"v_cvt_pk_bf16_f32 v253, v253, v254\n" \
"v_mfma_f32_16x16x32_bf16 v[140:143], v[28:31], v[212:215], v[140:143]\n" \
"v_accvgpr_write_b32 a66, v243\n" \
"v_accvgpr_write_b32 a67, v253\n" \
"v_mfma_f32_16x16x32_bf16 v[172:175], v[24:27], v[216:219], v[172:175]\n" \
"v_add_u32 v230, 0x8000, v230\n" \
"v_accvgpr_read_b32 v243, a96\n" \
"v_mfma_f32_16x16x32_bf16 v[172:175], v[28:31], v[220:223], v[172:175]\n" \
"v_add_u32 v231, 0x8000, v231\n" \
"v_accvgpr_read_b32 v252, a97\n" \
"v_mfma_f32_16x16x32_bf16 v[168:171], v[16:19], v[216:219], v[168:171]\n" \
"v_add_u32 v232, 0x4000, v232\n" \
"v_accvgpr_read_b32 v253, a98\n" \
"v_mfma_f32_16x16x32_bf16 v[168:171], v[20:23], v[220:223], v[168:171]\n" \
"buffer_store_dwordx4 a[168:171], v236, s[56:59], s93 offen offset:2304 sc1\n" \
"v_mfma_f32_16x16x32_bf16 v[164:167], v[8:11], v[216:219], v[164:167]\n" \
"v_add_u32 v233, 0x4000, v233\n" \
"v_accvgpr_read_b32 v254, a99\n" \
"v_mfma_f32_16x16x32_bf16 v[164:167], v[12:15], v[220:223], v[164:167]\n" \
"v_mul_f32 v243, s76, v243\n" \
"v_mul_f32 v252, s76, v252\n" \
"v_mfma_f32_16x16x32_bf16 v[160:163], v[0:3], v[216:219], v[160:163]\n" \
"v_mul_f32 v253, s76, v253\n" \
"v_mul_f32 v254, s76, v254\n" \
"v_mfma_f32_16x16x32_bf16 v[160:163], v[4:7], v[220:223], v[160:163]\n" \
"s_waitcnt vmcnt(37) lgkmcnt(0)\n" \
"s_barrier\n" \
"v_mfma_f32_16x16x32_bf16 v[80:83], v[32:35], v[192:195], v[80:83]\n" \
"ds_read_b64_tr_b16 v[0:1], v232 offset:0\n" \
"v_cvt_pk_bf16_f32 v243, v243, v252\n" \
"v_mfma_f32_16x16x32_bf16 v[80:83], v[36:39], v[196:199], v[80:83]\n" \
"ds_read_b64_tr_b16 v[2:3], v232 offset:2048\n" \
"v_cvt_pk_bf16_f32 v253, v253, v254\n" \
"v_mfma_f32_16x16x32_bf16 v[84:87], v[40:43], v[192:195], v[84:87]\n" \
"s_add_u32 m0, s79, 16384\n" \
"ds_read_b64_tr_b16 v[4:5], v232 offset:8192\n" \
"v_accvgpr_write_b32 a96, v243\n" \
"v_mfma_f32_16x16x32_bf16 v[84:87], v[44:47], v[196:199], v[84:87]\n" \
"buffer_load_dwordx4 v228, s[52:55], s78 offen offset:0 lds\n" \
"v_mfma_f32_16x16x32_bf16 v[88:91], v[48:51], v[192:195], v[88:91]\n" \
"ds_read_b64_tr_b16 v[6:7], v232 offset:10240\n" \
"v_accvgpr_write_b32 a97, v253\n" \
"v_mfma_f32_16x16x32_bf16 v[88:91], v[52:55], v[196:199], v[88:91]\n" \
"ds_read_b64_tr_b16 v[8:9], v233 offset:0\n" \
"v_accvgpr_read_b32 v243, a100\n" \
"v_mfma_f32_16x16x32_bf16 v[92:95], v[56:59], v[192:195], v[92:95]\n" \
"ds_read_b64_tr_b16 v[10:11], v233 offset:2048\n" \
"v_accvgpr_read_b32 v252, a101\n" \
"v_mfma_f32_16x16x32_bf16 v[92:95], v[60:63], v[196:199], v[92:95]\n" \
"buffer_load_dwordx4 v229, s[52:55], s78 offen offset:1024 lds\n" \
"v_mfma_f32_16x16x32_bf16 v[124:127], v[56:59], v[200:203], v[124:127]\n" \
"s_waitcnt vmcnt(37)\n" \
"ds_read_b128 v[192:195], v230 offset:0\n" \
"v_accvgpr_read_b32 v253, a102\n" \
"v_mfma_f32_16x16x32_bf16 v[124:127], v[60:63], v[204:207], v[124:127]\n" \
"ds_read_b128 v[196:199], v231 offset:0\n" \
"v_accvgpr_read_b32 v254, a103\n" \
"v_mfma_f32_16x16x32_bf16 v[120:123], v[48:51], v[200:203], v[120:123]\n" \
"s_xor_b32 s78, s78, 0x80000\n" \
"ds_read_b64_tr_b16 v[12:13], v233 offset:8192\n" \
"v_mul_f32 v243, s76, v243\n" \
"v_mfma_f32_16x16x32_bf16 v[120:123], v[52:55], v[204:207], v[120:123]\n" \
"buffer_load_dwordx4 v228, s[52:55], s89 offen offset:2048 lds\n" \
"v_mfma_f32_16x16x32_bf16 v[116:119], v[40:43], v[200:203], v[116:119]\n" \
"ds_read_b64_tr_b16 v[14:15], v233 offset:10240\n" \
"v_mul_f32 v252, s76, v252\n" \
"v_mfma_f32_16x16x32_bf16 v[116:119], v[44:47], v[204:207], v[116:119]\n" \
"ds_read_b64_tr_b16 v[16:17], v232 offset:512\n" \
"v_mul_f32 v253, s76, v253\n" \
"v_mfma_f32_16x16x32_bf16 v[112:115], v[32:35], v[200:203], v[112:115]\n" \
"ds_read_b64_tr_b16 v[18:19], v232 offset:2560\n" \
"v_mul_f32 v254, s76, v254\n" \
"v_mfma_f32_16x16x32_bf16 v[112:115], v[36:39], v[204:207], v[112:115]\n" \
"buffer_load_dwordx4 v229, s[52:55], s89 offen offset:3072 lds\n" \
"v_mfma_f32_16x16x32_bf16 v[144:147], v[32:35], v[208:211], v[144:147]\n" \
"s_waitcnt vmcnt(37)\n" \
"s_xor_b32 s89, s89, 0x80000\n" \
"ds_read_b128 v[200:203], v230 offset:2048\n" \
"v_mfma_f32_16x16x32_bf16 v[144:147], v[36:39], v[212:215], v[144:147]\n" \
"s_add_u32 m0, s74, 131072\n" \
"ds_read_b128 v[204:207], v231 offset:2048\n" \
"v_cvt_pk_bf16_f32 v243, v243, v252\n" \
"v_mfma_f32_16x16x32_bf16 v[148:151], v[40:43], v[208:211], v[148:151]\n" \
"s_waitcnt lgkmcnt(6)\n" \
"ds_read_b64_tr_b16 v[20:21], v232 offset:8704\n" \
"v_cvt_pk_bf16_f32 v253, v253, v254\n" \
"v_mfma_f32_16x16x32_bf16 v[148:151], v[44:47], v[212:215], v[148:151]\n" \
"buffer_load_dwordx4 v224, s[48:51], s75 offen offset:0 lds\n" \
"v_mfma_f32_16x16x32_bf16 v[152:155], v[48:51], v[208:211], v[152:155]\n" \
"ds_read_b64_tr_b16 v[22:23], v232 offset:10752\n" \
"v_accvgpr_write_b32 a98, v243\n" \
"v_mfma_f32_16x16x32_bf16 v[152:155], v[52:55], v[212:215], v[152:155]\n" \
"ds_read_b64_tr_b16 v[24:25], v233 offset:512\n" \
"v_accvgpr_write_b32 a99, v253\n" \
"v_mfma_f32_16x16x32_bf16 v[156:159], v[56:59], v[208:211], v[156:159]\n" \
"ds_read_b64_tr_b16 v[26:27], v233 offset:2560\n" \
"v_accvgpr_read_b32 v243, a72\n" \
"v_mfma_f32_16x16x32_bf16 v[156:159], v[60:63], v[212:215], v[156:159]\n" \
"buffer_load_dwordx4 v225, s[48:51], s75 offen offset:1024 lds\n" \
"v_mfma_f32_16x16x32_bf16 v[188:191], v[56:59], v[216:219], v[188:191]\n" \
"s_waitcnt vmcnt(37)\n" \
"ds_read_b128 v[208:211], v230 offset:4096\n" \
"v_accvgpr_read_b32 v252, a73\n" \
"v_mfma_f32_16x16x32_bf16 v[188:191], v[60:63], v[220:223], v[188:191]\n" \
"ds_read_b128 v[212:215], v231 offset:4096\n" \
"v_accvgpr_read_b32 v253, a74\n" \
"v_mfma_f32_16x16x32_bf16 v[184:187], v[48:51], v[216:219], v[184:187]\n" \
"s_waitcnt lgkmcnt(6)\n" \
"ds_read_b64_tr_b16 v[28:29], v233 offset:8704\n" \
"v_accvgpr_read_b32 v254, a75\n" \
"v_mfma_f32_16x16x32_bf16 v[184:187], v[52:55], v[220:223], v[184:187]\n" \
"buffer_load_dwordx4 v226, s[48:51], s75 offen offset:2048 lds\n" \
"v_mfma_f32_16x16x32_bf16 v[180:183], v[40:43], v[216:219], v[180:183]\n" \
"ds_read_b64_tr_b16 v[30:31], v233 offset:10752\n" \
"v_mul_f32 v243, s76, v243\n" \
"v_mfma_f32_16x16x32_bf16 v[180:183], v[44:47], v[220:223], v[180:183]\n" \
"v_mul_f32 v252, s76, v252\n" \
"v_mul_f32 v253, s76, v253\n" \
"v_mfma_f32_16x16x32_bf16 v[176:179], v[32:35], v[216:219], v[176:179]\n" \
"Lpg3260_%=:\n" \
"s_cbranch_execz Lpg5501_%=\n" \
"v_mul_f32 v254, s76, v254\n" \
"v_cvt_pk_bf16_f32 v243, v243, v252\n" \
"v_mfma_f32_16x16x32_bf16 v[176:179], v[36:39], v[220:223], v[176:179]\n" \
"buffer_load_dwordx4 v227, s[48:51], s75 offen offset:3072 lds\n" \
"v_mfma_f32_16x16x32_bf16 v[64:67], v[0:3], v[192:195], v[64:67]\n" \
"s_waitcnt vmcnt(37)\n" \
"s_xor_b32 s75, s75, 0x80\n" \
"ds_read_b128 v[216:219], v230 offset:6144\n" \
"v_mfma_f32_16x16x32_bf16 v[64:67], v[4:7], v[196:199], v[64:67]\n" \
"s_add_u32 m0, s74, 135168\n" \
"ds_read_b128 v[220:223], v231 offset:6144\n" \
"v_cvt_pk_bf16_f32 v253, v253, v254\n" \
"v_mfma_f32_16x16x32_bf16 v[68:71], v[8:11], v[192:195], v[68:71]\n" \
"s_waitcnt lgkmcnt(4)\n" \
"ds_read_b64_tr_b16 v[32:33], v232 offset:1024\n" \
"v_accvgpr_write_b32 a72, v243\n" \
"v_mfma_f32_16x16x32_bf16 v[68:71], v[12:15], v[196:199], v[68:71]\n" \
"buffer_load_dwordx4 v224, s[48:51], s77 offen offset:0 lds\n" \
"v_mfma_f32_16x16x32_bf16 v[72:75], v[16:19], v[192:195], v[72:75]\n" \
"ds_read_b64_tr_b16 v[34:35], v232 offset:3072\n" \
"v_accvgpr_write_b32 a73, v253\n" \
"v_mfma_f32_16x16x32_bf16 v[72:75], v[20:23], v[196:199], v[72:75]\n" \
"s_waitcnt lgkmcnt(3)\n" \
"ds_read_b64_tr_b16 v[36:37], v232 offset:9216\n" \
"v_accvgpr_read_b32 v243, a76\n" \
"v_mfma_f32_16x16x32_bf16 v[76:79], v[24:27], v[192:195], v[76:79]\n" \
"ds_read_b64_tr_b16 v[38:39], v232 offset:11264\n" \
"v_accvgpr_read_b32 v252, a77\n" \
"v_mfma_f32_16x16x32_bf16 v[76:79], v[28:31], v[196:199], v[76:79]\n" \
"buffer_load_dwordx4 v225, s[48:51], s77 offen offset:1024 lds\n" \
"v_mfma_f32_16x16x32_bf16 v[108:111], v[24:27], v[200:203], v[108:111]\n" \
"ds_read_b64_tr_b16 v[40:41], v233 offset:1024\n" \
"v_accvgpr_read_b32 v253, a78\n" \
"v_mfma_f32_16x16x32_bf16 v[108:111], v[28:31], v[204:207], v[108:111]\n" \
"ds_read_b64_tr_b16 v[42:43], v233 offset:3072\n" \
"v_accvgpr_read_b32 v254, a79\n" \
"v_mfma_f32_16x16x32_bf16 v[104:107], v[16:19], v[200:203], v[104:107]\n" \
"s_waitcnt lgkmcnt(6)\n" \
"ds_read_b64_tr_b16 v[44:45], v233 offset:9216\n" \
"v_mul_f32 v243, s76, v243\n" \
"v_mfma_f32_16x16x32_bf16 v[104:107], v[20:23], v[204:207], v[104:107]\n" \
"buffer_load_dwordx4 v226, s[48:51], s77 offen offset:2048 lds\n" \
"v_mfma_f32_16x16x32_bf16 v[100:103], v[8:11], v[200:203], v[100:103]\n" \
"ds_read_b64_tr_b16 v[46:47], v233 offset:11264\n" \
"v_mul_f32 v252, s76, v252\n" \
"v_mfma_f32_16x16x32_bf16 v[100:103], v[12:15], v[204:207], v[100:103]\n" \
"ds_read_b64_tr_b16 v[48:49], v232 offset:1536\n" \
"v_mul_f32 v253, s76, v253\n" \
"v_mfma_f32_16x16x32_bf16 v[96:99], v[0:3], v[200:203], v[96:99]\n" \
"ds_read_b64_tr_b16 v[50:51], v232 offset:3584\n" \
"v_mul_f32 v254, s76, v254\n" \
"v_mfma_f32_16x16x32_bf16 v[96:99], v[4:7], v[204:207], v[96:99]\n" \
"buffer_load_dwordx4 v227, s[48:51], s77 offen offset:3072 lds\n" \
"v_mfma_f32_16x16x32_bf16 v[128:131], v[0:3], v[208:211], v[128:131]\n" \
"ds_read_b64_tr_b16 v[52:53], v232 offset:9728\n" \
"v_cvt_pk_bf16_f32 v243, v243, v252\n" \
"v_mfma_f32_16x16x32_bf16 v[128:131], v[4:7], v[212:215], v[128:131]\n" \
"ds_read_b64_tr_b16 v[54:55], v232 offset:11776\n" \
"v_cvt_pk_bf16_f32 v253, v253, v254\n" \
"v_mfma_f32_16x16x32_bf16 v[132:135], v[8:11], v[208:211], v[132:135]\n" \
"s_xor_b32 s77, s77, 0x80\n" \
"ds_read_b64_tr_b16 v[56:57], v233 offset:1536\n" \
"v_accvgpr_write_b32 a74, v243\n" \
"v_mfma_f32_16x16x32_bf16 v[132:135], v[12:15], v[212:215], v[132:135]\n" \
"v_accvgpr_write_b32 a75, v253\n" \
"v_accvgpr_read_b32 v243, a104\n" \
"v_mfma_f32_16x16x32_bf16 v[136:139], v[16:19], v[208:211], v[136:139]\n" \
"ds_read_b64_tr_b16 v[58:59], v233 offset:3584\n" \
"v_accvgpr_read_b32 v252, a105\n" \
"v_mfma_f32_16x16x32_bf16 v[136:139], v[20:23], v[212:215], v[136:139]\n" \
"ds_read_b64_tr_b16 v[60:61], v233 offset:9728\n" \
"v_accvgpr_read_b32 v253, a106\n" \
"v_mfma_f32_16x16x32_bf16 v[140:143], v[24:27], v[208:211], v[140:143]\n" \
"s_add_u32 s93, s87, 17858560\n" \
"ds_read_b64_tr_b16 v[62:63], v233 offset:11776\n" \
"v_accvgpr_read_b32 v254, a107\n" \
"v_mfma_f32_16x16x32_bf16 v[140:143], v[28:31], v[212:215], v[140:143]\n" \
"buffer_store_dwordx4 a[208:211], v234, s[56:59], s93 offen offset:0 sc1\n" \
"v_mfma_f32_16x16x32_bf16 v[172:175], v[24:27], v[216:219], v[172:175]\n" \
"v_add_u32 v230, 0xffff0000, v230\n" \
"v_mul_f32 v243, s76, v243\n" \
"v_mfma_f32_16x16x32_bf16 v[172:175], v[28:31], v[220:223], v[172:175]\n" \
"v_add_u32 v231, 0xffff0000, v231\n" \
"v_mul_f32 v252, s76, v252\n" \
"v_mfma_f32_16x16x32_bf16 v[168:171], v[16:19], v[216:219], v[168:171]\n" \
"v_add_u32 v232, 0x4000, v232\n" \
"v_mul_f32 v253, s76, v253\n" \
"v_mfma_f32_16x16x32_bf16 v[168:171], v[20:23], v[220:223], v[168:171]\n" \
"v_mul_f32 v254, s76, v254\n" \
"v_cvt_pk_bf16_f32 v243, v243, v252\n" \
"v_mfma_f32_16x16x32_bf16 v[164:167], v[8:11], v[216:219], v[164:167]\n" \
"v_add_u32 v233, 0x4000, v233\n" \
"v_cvt_pk_bf16_f32 v253, v253, v254\n" \
"v_mfma_f32_16x16x32_bf16 v[164:167], v[12:15], v[220:223], v[164:167]\n" \
"v_accvgpr_write_b32 a104, v243\n" \
"v_accvgpr_write_b32 a105, v253\n" \
"v_mfma_f32_16x16x32_bf16 v[160:163], v[0:3], v[216:219], v[160:163]\n" \
"v_accvgpr_read_b32 v243, a108\n" \
"v_accvgpr_read_b32 v252, a109\n" \
"v_mfma_f32_16x16x32_bf16 v[160:163], v[4:7], v[220:223], v[160:163]\n" \
"s_waitcnt vmcnt(36) lgkmcnt(0)\n" \
"s_barrier\n" \
"v_mfma_f32_16x16x32_bf16 v[80:83], v[32:35], v[192:195], v[80:83]\n" \
"ds_read_b64_tr_b16 v[0:1], v232 offset:0\n" \
"v_accvgpr_read_b32 v253, a110\n" \
"v_mfma_f32_16x16x32_bf16 v[80:83], v[36:39], v[196:199], v[80:83]\n" \
"ds_read_b64_tr_b16 v[2:3], v232 offset:2048\n" \
"v_accvgpr_read_b32 v254, a111\n" \
"v_mfma_f32_16x16x32_bf16 v[84:87], v[40:43], v[192:195], v[84:87]\n" \
"s_add_u32 m0, s79, 32768\n" \
"ds_read_b64_tr_b16 v[4:5], v232 offset:8192\n" \
"v_mul_f32 v243, s76, v243\n" \
"v_mfma_f32_16x16x32_bf16 v[84:87], v[44:47], v[196:199], v[84:87]\n" \
"buffer_load_dwordx4 v228, s[52:55], s78 offen offset:0 lds\n" \
"v_mfma_f32_16x16x32_bf16 v[88:91], v[48:51], v[192:195], v[88:91]\n" \
"ds_read_b64_tr_b16 v[6:7], v232 offset:10240\n" \
"v_mul_f32 v252, s76, v252\n" \
"v_mfma_f32_16x16x32_bf16 v[88:91], v[52:55], v[196:199], v[88:91]\n" \
"ds_read_b64_tr_b16 v[8:9], v233 offset:0\n" \
"v_mul_f32 v253, s76, v253\n" \
"v_mfma_f32_16x16x32_bf16 v[92:95], v[56:59], v[192:195], v[92:95]\n" \
"ds_read_b64_tr_b16 v[10:11], v233 offset:2048\n" \
"v_mul_f32 v254, s76, v254\n" \
"v_mfma_f32_16x16x32_bf16 v[92:95], v[60:63], v[196:199], v[92:95]\n" \
"buffer_load_dwordx4 v229, s[52:55], s78 offen offset:1024 lds\n" \
"v_mfma_f32_16x16x32_bf16 v[124:127], v[56:59], v[200:203], v[124:127]\n" \
"s_waitcnt vmcnt(36)\n" \
"ds_read_b128 v[192:195], v230 offset:0\n" \
"v_cvt_pk_bf16_f32 v243, v243, v252\n" \
"v_mfma_f32_16x16x32_bf16 v[124:127], v[60:63], v[204:207], v[124:127]\n" \
"ds_read_b128 v[196:199], v231 offset:0\n" \
"v_cvt_pk_bf16_f32 v253, v253, v254\n" \
"v_mfma_f32_16x16x32_bf16 v[120:123], v[48:51], v[200:203], v[120:123]\n" \
"s_xor_b32 s78, s78, 0x180000\n" \
"ds_read_b64_tr_b16 v[12:13], v233 offset:8192\n" \
"v_accvgpr_write_b32 a106, v243\n" \
"v_mfma_f32_16x16x32_bf16 v[120:123], v[52:55], v[204:207], v[120:123]\n" \
"buffer_load_dwordx4 v228, s[52:55], s89 offen offset:2048 lds\n" \
"v_mfma_f32_16x16x32_bf16 v[116:119], v[40:43], v[200:203], v[116:119]\n" \
"ds_read_b64_tr_b16 v[14:15], v233 offset:10240\n" \
"v_accvgpr_write_b32 a107, v253\n" \
"v_mfma_f32_16x16x32_bf16 v[116:119], v[44:47], v[204:207], v[116:119]\n" \
"ds_read_b64_tr_b16 v[16:17], v232 offset:512\n" \
"v_accvgpr_read_b32 v243, a0\n" \
"v_mfma_f32_16x16x32_bf16 v[112:115], v[32:35], v[200:203], v[112:115]\n" \
"ds_read_b64_tr_b16 v[18:19], v232 offset:2560\n" \
"v_accvgpr_read_b32 v252, a1\n" \
"v_mfma_f32_16x16x32_bf16 v[112:115], v[36:39], v[204:207], v[112:115]\n" \
"buffer_load_dwordx4 v229, s[52:55], s89 offen offset:3072 lds\n" \
"v_mfma_f32_16x16x32_bf16 v[144:147], v[32:35], v[208:211], v[144:147]\n" \
"s_waitcnt vmcnt(36)\n" \
"s_xor_b32 s89, s89, 0x180000\n" \
"ds_read_b128 v[200:203], v230 offset:2048\n" \
"v_mfma_f32_16x16x32_bf16 v[144:147], v[36:39], v[212:215], v[144:147]\n" \
"s_add_u32 m0, s74, 65536\n" \
"ds_read_b128 v[204:207], v231 offset:2048\n" \
"v_accvgpr_read_b32 v253, a2\n" \
"v_mfma_f32_16x16x32_bf16 v[148:151], v[40:43], v[208:211], v[148:151]\n" \
"s_waitcnt lgkmcnt(6)\n" \
"ds_read_b64_tr_b16 v[20:21], v232 offset:8704\n" \
"v_accvgpr_read_b32 v254, a3\n" \
"v_mfma_f32_16x16x32_bf16 v[148:151], v[44:47], v[212:215], v[148:151]\n" \
"buffer_load_dwordx4 v224, s[48:51], s75 offen offset:0 lds\n" \
"v_mfma_f32_16x16x32_bf16 v[152:155], v[48:51], v[208:211], v[152:155]\n" \
"ds_read_b64_tr_b16 v[22:23], v232 offset:10752\n" \
"v_mul_f32 v243, s76, v243\n" \
"v_mfma_f32_16x16x32_bf16 v[152:155], v[52:55], v[212:215], v[152:155]\n" \
"ds_read_b64_tr_b16 v[24:25], v233 offset:512\n" \
"v_mul_f32 v252, s76, v252\n" \
"v_mfma_f32_16x16x32_bf16 v[156:159], v[56:59], v[208:211], v[156:159]\n" \
"ds_read_b64_tr_b16 v[26:27], v233 offset:2560\n" \
"v_mul_f32 v253, s76, v253\n" \
"v_mfma_f32_16x16x32_bf16 v[156:159], v[60:63], v[212:215], v[156:159]\n" \
"buffer_load_dwordx4 v225, s[48:51], s75 offen offset:1024 lds\n" \
"v_mfma_f32_16x16x32_bf16 v[188:191], v[56:59], v[216:219], v[188:191]\n" \
"s_waitcnt vmcnt(36)\n" \
"ds_read_b128 v[208:211], v230 offset:4096\n" \
"v_mul_f32 v254, s76, v254\n" \
"v_mfma_f32_16x16x32_bf16 v[188:191], v[60:63], v[220:223], v[188:191]\n" \
"ds_read_b128 v[212:215], v231 offset:4096\n" \
"v_cvt_pk_bf16_f32 v243, v243, v252\n" \
"v_mfma_f32_16x16x32_bf16 v[184:187], v[48:51], v[216:219], v[184:187]\n" \
"s_waitcnt lgkmcnt(6)\n" \
"ds_read_b64_tr_b16 v[28:29], v233 offset:8704\n" \
"v_cvt_pk_bf16_f32 v253, v253, v254\n" \
"v_mfma_f32_16x16x32_bf16 v[184:187], v[52:55], v[220:223], v[184:187]\n" \
"buffer_load_dwordx4 v226, s[48:51], s75 offen offset:2048 lds\n" \
"v_mfma_f32_16x16x32_bf16 v[180:183], v[40:43], v[216:219], v[180:183]\n" \
"ds_read_b64_tr_b16 v[30:31], v233 offset:10752\n" \
"v_accvgpr_write_b32 a0, v243\n" \
"v_mfma_f32_16x16x32_bf16 v[180:183], v[44:47], v[220:223], v[180:183]\n" \
"v_accvgpr_write_b32 a1, v253\n" \
"v_accvgpr_read_b32 v243, a4\n" \
"v_mfma_f32_16x16x32_bf16 v[176:179], v[32:35], v[216:219], v[176:179]\n" \
"v_accvgpr_read_b32 v252, a5\n" \
"v_accvgpr_read_b32 v253, a6\n" \
"v_mfma_f32_16x16x32_bf16 v[176:179], v[36:39], v[220:223], v[176:179]\n" \
"buffer_load_dwordx4 v227, s[48:51], s75 offen offset:3072 lds\n" \
"v_mfma_f32_16x16x32_bf16 v[64:67], v[0:3], v[192:195], v[64:67]\n" \
"s_waitcnt vmcnt(36)\n" \
"s_xor_b32 s75, s75, 0x180\n" \
"ds_read_b128 v[216:219], v230 offset:6144\n" \
"v_mfma_f32_16x16x32_bf16 v[64:67], v[4:7], v[196:199], v[64:67]\n" \
"s_add_u32 m0, s74, 69632\n" \
"ds_read_b128 v[220:223], v231 offset:6144\n" \
"v_accvgpr_read_b32 v254, a7\n" \
"v_mfma_f32_16x16x32_bf16 v[68:71], v[8:11], v[192:195], v[68:71]\n" \
"s_waitcnt lgkmcnt(4)\n" \
"ds_read_b64_tr_b16 v[32:33], v232 offset:1024\n" \
"v_mul_f32 v243, s76, v243\n" \
"v_mfma_f32_16x16x32_bf16 v[68:71], v[12:15], v[196:199], v[68:71]\n" \
"buffer_load_dwordx4 v224, s[48:51], s77 offen offset:0 lds\n" \
"v_mfma_f32_16x16x32_bf16 v[72:75], v[16:19], v[192:195], v[72:75]\n" \
"ds_read_b64_tr_b16 v[34:35], v232 offset:3072\n" \
"v_mul_f32 v252, s76, v252\n" \
"v_mfma_f32_16x16x32_bf16 v[72:75], v[20:23], v[196:199], v[72:75]\n" \
"s_waitcnt lgkmcnt(3)\n" \
"ds_read_b64_tr_b16 v[36:37], v232 offset:9216\n" \
"v_mul_f32 v253, s76, v253\n" \
"v_mfma_f32_16x16x32_bf16 v[76:79], v[24:27], v[192:195], v[76:79]\n" \
"ds_read_b64_tr_b16 v[38:39], v232 offset:11264\n" \
"v_mul_f32 v254, s76, v254\n" \
"v_mfma_f32_16x16x32_bf16 v[76:79], v[28:31], v[196:199], v[76:79]\n" \
"buffer_load_dwordx4 v225, s[48:51], s77 offen offset:1024 lds\n" \
"v_mfma_f32_16x16x32_bf16 v[108:111], v[24:27], v[200:203], v[108:111]\n" \
"ds_read_b64_tr_b16 v[40:41], v233 offset:1024\n" \
"v_cvt_pk_bf16_f32 v243, v243, v252\n" \
"v_mfma_f32_16x16x32_bf16 v[108:111], v[28:31], v[204:207], v[108:111]\n" \
"ds_read_b64_tr_b16 v[42:43], v233 offset:3072\n" \
"v_cvt_pk_bf16_f32 v253, v253, v254\n" \
"v_mfma_f32_16x16x32_bf16 v[104:107], v[16:19], v[200:203], v[104:107]\n" \
"s_waitcnt lgkmcnt(6)\n" \
"ds_read_b64_tr_b16 v[44:45], v233 offset:9216\n" \
"v_accvgpr_write_b32 a2, v243\n" \
"v_mfma_f32_16x16x32_bf16 v[104:107], v[20:23], v[204:207], v[104:107]\n" \
"buffer_load_dwordx4 v226, s[48:51], s77 offen offset:2048 lds\n" \
"v_mfma_f32_16x16x32_bf16 v[100:103], v[8:11], v[200:203], v[100:103]\n" \
"ds_read_b64_tr_b16 v[46:47], v233 offset:11264\n" \
"v_accvgpr_write_b32 a3, v253\n" \
"v_mfma_f32_16x16x32_bf16 v[100:103], v[12:15], v[204:207], v[100:103]\n" \
"ds_read_b64_tr_b16 v[48:49], v232 offset:1536\n" \
"v_accvgpr_read_b32 v243, a32\n" \
"v_mfma_f32_16x16x32_bf16 v[96:99], v[0:3], v[200:203], v[96:99]\n" \
"ds_read_b64_tr_b16 v[50:51], v232 offset:3584\n" \
"v_accvgpr_read_b32 v252, a33\n" \
"v_mfma_f32_16x16x32_bf16 v[96:99], v[4:7], v[204:207], v[96:99]\n" \
"buffer_load_dwordx4 v227, s[48:51], s77 offen offset:3072 lds\n" \
"v_mfma_f32_16x16x32_bf16 v[128:131], v[0:3], v[208:211], v[128:131]\n" \
"ds_read_b64_tr_b16 v[52:53], v232 offset:9728\n" \
"v_accvgpr_read_b32 v253, a34\n" \
"v_mfma_f32_16x16x32_bf16 v[128:131], v[4:7], v[212:215], v[128:131]\n" \
"ds_read_b64_tr_b16 v[54:55], v232 offset:11776\n" \
"v_accvgpr_read_b32 v254, a35\n" \
"v_mfma_f32_16x16x32_bf16 v[132:135], v[8:11], v[208:211], v[132:135]\n" \
"s_xor_b32 s77, s77, 0x180\n" \
"ds_read_b64_tr_b16 v[56:57], v233 offset:1536\n" \
"v_mul_f32 v243, s76, v243\n" \
"v_mfma_f32_16x16x32_bf16 v[132:135], v[12:15], v[212:215], v[132:135]\n" \
"buffer_store_dwordx4 a[212:215], v234, s[56:59], s93 offen offset:1024 sc1\n" \
"v_mfma_f32_16x16x32_bf16 v[136:139], v[16:19], v[208:211], v[136:139]\n" \
"ds_read_b64_tr_b16 v[58:59], v233 offset:3584\n" \
"v_mul_f32 v252, s76, v252\n" \
"v_mfma_f32_16x16x32_bf16 v[136:139], v[20:23], v[212:215], v[136:139]\n" \
"ds_read_b64_tr_b16 v[60:61], v233 offset:9728\n" \
"v_mul_f32 v253, s76, v253\n" \
"v_mfma_f32_16x16x32_bf16 v[140:143], v[24:27], v[208:211], v[140:143]\n" \
"ds_read_b64_tr_b16 v[62:63], v233 offset:11776\n" \
"v_mul_f32 v254, s76, v254\n" \
"v_mfma_f32_16x16x32_bf16 v[140:143], v[28:31], v[212:215], v[140:143]\n" \
"v_cvt_pk_bf16_f32 v243, v243, v252\n" \
"v_cvt_pk_bf16_f32 v253, v253, v254\n" \
"v_mfma_f32_16x16x32_bf16 v[172:175], v[24:27], v[216:219], v[172:175]\n" \
"v_add_u32 v230, 0x8000, v230\n" \
"v_accvgpr_write_b32 a32, v243\n" \
"v_mfma_f32_16x16x32_bf16 v[172:175], v[28:31], v[220:223], v[172:175]\n" \
"v_add_u32 v231, 0x8000, v231\n" \
"v_accvgpr_write_b32 a33, v253\n" \
"v_mfma_f32_16x16x32_bf16 v[168:171], v[16:19], v[216:219], v[168:171]\n" \
"v_add_u32 v232, 0xffff4000, v232\n" \
"v_accvgpr_read_b32 v243, a36\n" \
"v_mfma_f32_16x16x32_bf16 v[168:171], v[20:23], v[220:223], v[168:171]\n" \
"buffer_store_dwordx4 a[216:219], v234, s[56:59], s93 offen offset:2048 sc1\n" \
"v_mfma_f32_16x16x32_bf16 v[164:167], v[8:11], v[216:219], v[164:167]\n" \
"v_add_u32 v233, 0xffff4000, v233\n" \
"v_accvgpr_read_b32 v252, a37\n" \
"v_mfma_f32_16x16x32_bf16 v[164:167], v[12:15], v[220:223], v[164:167]\n" \
"v_accvgpr_read_b32 v253, a38\n" \
"v_accvgpr_read_b32 v254, a39\n" \
"v_mfma_f32_16x16x32_bf16 v[160:163], v[0:3], v[216:219], v[160:163]\n" \
"v_mul_f32 v243, s76, v243\n" \
"v_mul_f32 v252, s76, v252\n" \
"v_mfma_f32_16x16x32_bf16 v[160:163], v[4:7], v[220:223], v[160:163]\n" \
"s_waitcnt vmcnt(37) lgkmcnt(0)\n" \
"s_barrier\n" \
"v_mfma_f32_16x16x32_bf16 v[80:83], v[32:35], v[192:195], v[80:83]\n" \
"ds_read_b64_tr_b16 v[0:1], v232 offset:0\n" \
"v_mul_f32 v253, s76, v253\n" \
"v_mfma_f32_16x16x32_bf16 v[80:83], v[36:39], v[196:199], v[80:83]\n" \
"ds_read_b64_tr_b16 v[2:3], v232 offset:2048\n" \
"v_mul_f32 v254, s76, v254\n" \
"v_mfma_f32_16x16x32_bf16 v[84:87], v[40:43], v[192:195], v[84:87]\n" \
"s_add_u32 m0, s79, 49152\n" \
"ds_read_b64_tr_b16 v[4:5], v232 offset:8192\n" \
"v_cvt_pk_bf16_f32 v243, v243, v252\n" \
"v_mfma_f32_16x16x32_bf16 v[84:87], v[44:47], v[196:199], v[84:87]\n" \
"buffer_load_dwordx4 v228, s[52:55], s78 offen offset:0 lds\n" \
"v_mfma_f32_16x16x32_bf16 v[88:91], v[48:51], v[192:195], v[88:91]\n" \
"ds_read_b64_tr_b16 v[6:7], v232 offset:10240\n" \
"v_cvt_pk_bf16_f32 v253, v253, v254\n" \
"v_mfma_f32_16x16x32_bf16 v[88:91], v[52:55], v[196:199], v[88:91]\n" \
"ds_read_b64_tr_b16 v[8:9], v233 offset:0\n" \
"v_accvgpr_write_b32 a34, v243\n" \
"v_mfma_f32_16x16x32_bf16 v[92:95], v[56:59], v[192:195], v[92:95]\n" \
"ds_read_b64_tr_b16 v[10:11], v233 offset:2048\n" \
"v_accvgpr_write_b32 a35, v253\n" \
"v_mfma_f32_16x16x32_bf16 v[92:95], v[60:63], v[196:199], v[92:95]\n" \
"buffer_load_dwordx4 v229, s[52:55], s78 offen offset:1024 lds\n" \
"v_mfma_f32_16x16x32_bf16 v[124:127], v[56:59], v[200:203], v[124:127]\n" \
"s_waitcnt vmcnt(37)\n" \
"ds_read_b128 v[192:195], v230 offset:0\n" \
"v_accvgpr_read_b32 v243, a8\n" \
"v_mfma_f32_16x16x32_bf16 v[124:127], v[60:63], v[204:207], v[124:127]\n" \
"ds_read_b128 v[196:199], v231 offset:0\n" \
"v_accvgpr_read_b32 v252, a9\n" \
"v_mfma_f32_16x16x32_bf16 v[120:123], v[48:51], v[200:203], v[120:123]\n" \
"s_xor_b32 s78, s78, 0x300000\n" \
"ds_read_b64_tr_b16 v[12:13], v233 offset:8192\n" \
"v_accvgpr_read_b32 v253, a10\n" \
"v_mfma_f32_16x16x32_bf16 v[120:123], v[52:55], v[204:207], v[120:123]\n" \
"buffer_load_dwordx4 v228, s[52:55], s89 offen offset:2048 lds\n" \
"v_mfma_f32_16x16x32_bf16 v[116:119], v[40:43], v[200:203], v[116:119]\n" \
"ds_read_b64_tr_b16 v[14:15], v233 offset:10240\n" \
"v_accvgpr_read_b32 v254, a11\n" \
"v_mfma_f32_16x16x32_bf16 v[116:119], v[44:47], v[204:207], v[116:119]\n" \
"ds_read_b64_tr_b16 v[16:17], v232 offset:512\n" \
"v_mul_f32 v243, s76, v243\n" \
"v_mfma_f32_16x16x32_bf16 v[112:115], v[32:35], v[200:203], v[112:115]\n" \
"ds_read_b64_tr_b16 v[18:19], v232 offset:2560\n" \
"v_mul_f32 v252, s76, v252\n" \
"v_mfma_f32_16x16x32_bf16 v[112:115], v[36:39], v[204:207], v[112:115]\n" \
"buffer_load_dwordx4 v229, s[52:55], s89 offen offset:3072 lds\n" \
"v_mfma_f32_16x16x32_bf16 v[144:147], v[32:35], v[208:211], v[144:147]\n" \
"s_waitcnt vmcnt(37)\n" \
"s_xor_b32 s89, s89, 0x300000\n" \
"ds_read_b128 v[200:203], v230 offset:2048\n" \
"v_mfma_f32_16x16x32_bf16 v[144:147], v[36:39], v[212:215], v[144:147]\n" \
"s_add_u32 m0, s74, 98304\n" \
"ds_read_b128 v[204:207], v231 offset:2048\n" \
"v_mul_f32 v253, s76, v253\n" \
"v_mfma_f32_16x16x32_bf16 v[148:151], v[40:43], v[208:211], v[148:151]\n" \
"s_waitcnt lgkmcnt(6)\n" \
"ds_read_b64_tr_b16 v[20:21], v232 offset:8704\n" \
"v_mul_f32 v254, s76, v254\n" \
"v_mfma_f32_16x16x32_bf16 v[148:151], v[44:47], v[212:215], v[148:151]\n" \
"buffer_load_dwordx4 v224, s[48:51], s75 offen offset:0 lds\n" \
"v_mfma_f32_16x16x32_bf16 v[152:155], v[48:51], v[208:211], v[152:155]\n" \
"ds_read_b64_tr_b16 v[22:23], v232 offset:10752\n" \
"v_cvt_pk_bf16_f32 v243, v243, v252\n" \
"v_mfma_f32_16x16x32_bf16 v[152:155], v[52:55], v[212:215], v[152:155]\n" \
"ds_read_b64_tr_b16 v[24:25], v233 offset:512\n" \
"v_cvt_pk_bf16_f32 v253, v253, v254\n" \
"v_mfma_f32_16x16x32_bf16 v[156:159], v[56:59], v[208:211], v[156:159]\n" \
"ds_read_b64_tr_b16 v[26:27], v233 offset:2560\n" \
"v_accvgpr_write_b32 a8, v243\n" \
"v_mfma_f32_16x16x32_bf16 v[156:159], v[60:63], v[212:215], v[156:159]\n" \
"buffer_load_dwordx4 v225, s[48:51], s75 offen offset:1024 lds\n" \
"v_mfma_f32_16x16x32_bf16 v[188:191], v[56:59], v[216:219], v[188:191]\n" \
"s_waitcnt vmcnt(37)\n" \
"ds_read_b128 v[208:211], v230 offset:4096\n" \
"v_accvgpr_write_b32 a9, v253\n" \
"v_mfma_f32_16x16x32_bf16 v[188:191], v[60:63], v[220:223], v[188:191]\n" \
"ds_read_b128 v[212:215], v231 offset:4096\n" \
"v_accvgpr_read_b32 v243, a12\n" \
"v_mfma_f32_16x16x32_bf16 v[184:187], v[48:51], v[216:219], v[184:187]\n" \
"s_waitcnt lgkmcnt(6)\n" \
"ds_read_b64_tr_b16 v[28:29], v233 offset:8704\n" \
"v_accvgpr_read_b32 v252, a13\n" \
"v_mfma_f32_16x16x32_bf16 v[184:187], v[52:55], v[220:223], v[184:187]\n" \
"buffer_load_dwordx4 v226, s[48:51], s75 offen offset:2048 lds\n" \
"v_mfma_f32_16x16x32_bf16 v[180:183], v[40:43], v[216:219], v[180:183]\n" \
"ds_read_b64_tr_b16 v[30:31], v233 offset:10752\n" \
"v_accvgpr_read_b32 v253, a14\n" \
"v_mfma_f32_16x16x32_bf16 v[180:183], v[44:47], v[220:223], v[180:183]\n" \
"v_accvgpr_read_b32 v254, a15\n" \
"v_mul_f32 v243, s76, v243\n" \
"v_mfma_f32_16x16x32_bf16 v[176:179], v[32:35], v[216:219], v[176:179]\n" \
"v_mul_f32 v252, s76, v252\n" \
"v_mul_f32 v253, s76, v253\n" \
"v_mfma_f32_16x16x32_bf16 v[176:179], v[36:39], v[220:223], v[176:179]\n" \
"buffer_load_dwordx4 v227, s[48:51], s75 offen offset:3072 lds\n" \
"v_mfma_f32_16x16x32_bf16 v[64:67], v[0:3], v[192:195], v[64:67]\n" \
"s_waitcnt vmcnt(37)\n" \
"s_xor_b32 s75, s75, 0x300\n" \
"ds_read_b128 v[216:219], v230 offset:6144\n" \
"v_mfma_f32_16x16x32_bf16 v[64:67], v[4:7], v[196:199], v[64:67]\n" \
"s_add_u32 m0, s74, 102400\n" \
"ds_read_b128 v[220:223], v231 offset:6144\n" \
"v_mul_f32 v254, s76, v254\n" \
"v_mfma_f32_16x16x32_bf16 v[68:71], v[8:11], v[192:195], v[68:71]\n" \
"s_waitcnt lgkmcnt(4)\n" \
"ds_read_b64_tr_b16 v[32:33], v232 offset:1024\n" \
"v_cvt_pk_bf16_f32 v243, v243, v252\n" \
"v_mfma_f32_16x16x32_bf16 v[68:71], v[12:15], v[196:199], v[68:71]\n" \
"buffer_load_dwordx4 v224, s[48:51], s77 offen offset:0 lds\n" \
"v_mfma_f32_16x16x32_bf16 v[72:75], v[16:19], v[192:195], v[72:75]\n" \
"ds_read_b64_tr_b16 v[34:35], v232 offset:3072\n" \
"v_cvt_pk_bf16_f32 v253, v253, v254\n" \
"v_mfma_f32_16x16x32_bf16 v[72:75], v[20:23], v[196:199], v[72:75]\n" \
"s_waitcnt lgkmcnt(3)\n" \
"ds_read_b64_tr_b16 v[36:37], v232 offset:9216\n" \
"v_accvgpr_write_b32 a10, v243\n" \
"v_mfma_f32_16x16x32_bf16 v[76:79], v[24:27], v[192:195], v[76:79]\n" \
"ds_read_b64_tr_b16 v[38:39], v232 offset:11264\n" \
"v_accvgpr_write_b32 a11, v253\n" \
"v_mfma_f32_16x16x32_bf16 v[76:79], v[28:31], v[196:199], v[76:79]\n" \
"buffer_load_dwordx4 v225, s[48:51], s77 offen offset:1024 lds\n" \
"v_mfma_f32_16x16x32_bf16 v[108:111], v[24:27], v[200:203], v[108:111]\n" \
"ds_read_b64_tr_b16 v[40:41], v233 offset:1024\n" \
"v_accvgpr_read_b32 v243, a40\n" \
"v_mfma_f32_16x16x32_bf16 v[108:111], v[28:31], v[204:207], v[108:111]\n" \
"ds_read_b64_tr_b16 v[42:43], v233 offset:3072\n" \
"v_accvgpr_read_b32 v252, a41\n" \
"v_mfma_f32_16x16x32_bf16 v[104:107], v[16:19], v[200:203], v[104:107]\n" \
"s_waitcnt lgkmcnt(6)\n" \
"ds_read_b64_tr_b16 v[44:45], v233 offset:9216\n" \
"v_accvgpr_read_b32 v253, a42\n" \
"v_mfma_f32_16x16x32_bf16 v[104:107], v[20:23], v[204:207], v[104:107]\n" \
"buffer_load_dwordx4 v226, s[48:51], s77 offen offset:2048 lds\n" \
"v_mfma_f32_16x16x32_bf16 v[100:103], v[8:11], v[200:203], v[100:103]\n" \
"ds_read_b64_tr_b16 v[46:47], v233 offset:11264\n" \
"v_accvgpr_read_b32 v254, a43\n" \
"v_mfma_f32_16x16x32_bf16 v[100:103], v[12:15], v[204:207], v[100:103]\n" \
"ds_read_b64_tr_b16 v[48:49], v232 offset:1536\n" \
"v_mul_f32 v243, s76, v243\n" \
"v_mfma_f32_16x16x32_bf16 v[96:99], v[0:3], v[200:203], v[96:99]\n" \
"ds_read_b64_tr_b16 v[50:51], v232 offset:3584\n" \
"v_mul_f32 v252, s76, v252\n" \
"v_mfma_f32_16x16x32_bf16 v[96:99], v[4:7], v[204:207], v[96:99]\n" \
"buffer_load_dwordx4 v227, s[48:51], s77 offen offset:3072 lds\n" \
"v_mfma_f32_16x16x32_bf16 v[128:131], v[0:3], v[208:211], v[128:131]\n" \
"ds_read_b64_tr_b16 v[52:53], v232 offset:9728\n" \
"v_mul_f32 v253, s76, v253\n" \
"v_mfma_f32_16x16x32_bf16 v[128:131], v[4:7], v[212:215], v[128:131]\n" \
"ds_read_b64_tr_b16 v[54:55], v232 offset:11776\n" \
"v_mul_f32 v254, s76, v254\n" \
"v_mfma_f32_16x16x32_bf16 v[132:135], v[8:11], v[208:211], v[132:135]\n" \
"s_xor_b32 s77, s77, 0x300\n" \
"ds_read_b64_tr_b16 v[56:57], v233 offset:1536\n" \
"v_cvt_pk_bf16_f32 v243, v243, v252\n" \
"v_mfma_f32_16x16x32_bf16 v[132:135], v[12:15], v[212:215], v[132:135]\n" \
"v_cvt_pk_bf16_f32 v253, v253, v254\n" \
"v_accvgpr_write_b32 a40, v243\n" \
"v_mfma_f32_16x16x32_bf16 v[136:139], v[16:19], v[208:211], v[136:139]\n" \
"ds_read_b64_tr_b16 v[58:59], v233 offset:3584\n" \
"v_accvgpr_write_b32 a41, v253\n" \
"v_mfma_f32_16x16x32_bf16 v[136:139], v[20:23], v[212:215], v[136:139]\n" \
"ds_read_b64_tr_b16 v[60:61], v233 offset:9728\n" \
"v_accvgpr_read_b32 v243, a44\n" \
"v_mfma_f32_16x16x32_bf16 v[140:143], v[24:27], v[208:211], v[140:143]\n" \
"ds_read_b64_tr_b16 v[62:63], v233 offset:11776\n" \
"v_accvgpr_read_b32 v252, a45\n" \
"v_mfma_f32_16x16x32_bf16 v[140:143], v[28:31], v[212:215], v[140:143]\n" \
"buffer_store_dwordx4 a[220:223], v234, s[56:59], s93 offen offset:3072 sc1\n" \
"v_mfma_f32_16x16x32_bf16 v[172:175], v[24:27], v[216:219], v[172:175]\n" \
"v_add_u32 v230, 0x8000, v230\n" \
"v_accvgpr_read_b32 v253, a46\n" \
"v_mfma_f32_16x16x32_bf16 v[172:175], v[28:31], v[220:223], v[172:175]\n" \
"v_add_u32 v231, 0x8000, v231\n" \
"v_accvgpr_read_b32 v254, a47\n" \
"v_mfma_f32_16x16x32_bf16 v[168:171], v[16:19], v[216:219], v[168:171]\n" \
"v_add_u32 v232, 0x4000, v232\n" \
"v_mul_f32 v243, s76, v243\n" \
"v_mfma_f32_16x16x32_bf16 v[168:171], v[20:23], v[220:223], v[168:171]\n" \
"v_mul_f32 v252, s76, v252\n" \
"v_mul_f32 v253, s76, v253\n" \
"v_mfma_f32_16x16x32_bf16 v[164:167], v[8:11], v[216:219], v[164:167]\n" \
"v_add_u32 v233, 0x4000, v233\n" \
"v_mul_f32 v254, s76, v254\n" \
"v_mfma_f32_16x16x32_bf16 v[164:167], v[12:15], v[220:223], v[164:167]\n" \
"v_cvt_pk_bf16_f32 v243, v243, v252\n" \
"v_cvt_pk_bf16_f32 v253, v253, v254\n" \
"v_mfma_f32_16x16x32_bf16 v[160:163], v[0:3], v[216:219], v[160:163]\n" \
"v_accvgpr_write_b32 a42, v243\n" \
"v_accvgpr_write_b32 a43, v253\n" \
"v_mfma_f32_16x16x32_bf16 v[160:163], v[4:7], v[220:223], v[160:163]\n" \
"s_waitcnt vmcnt(36) lgkmcnt(0)\n" \
"s_barrier\n" \
"v_mfma_f32_16x16x32_bf16 v[80:83], v[32:35], v[192:195], v[80:83]\n" \
"ds_read_b64_tr_b16 v[0:1], v232 offset:0\n" \
"v_accvgpr_read_b32 v243, a80\n" \
"v_mfma_f32_16x16x32_bf16 v[80:83], v[36:39], v[196:199], v[80:83]\n" \
"ds_read_b64_tr_b16 v[2:3], v232 offset:2048\n" \
"v_accvgpr_read_b32 v252, a81\n" \
"v_mfma_f32_16x16x32_bf16 v[84:87], v[40:43], v[192:195], v[84:87]\n" \
"s_add_u32 m0, s79, 0\n" \
"ds_read_b64_tr_b16 v[4:5], v232 offset:8192\n" \
"v_accvgpr_read_b32 v253, a82\n" \
"v_mfma_f32_16x16x32_bf16 v[84:87], v[44:47], v[196:199], v[84:87]\n" \
"buffer_load_dwordx4 v228, s[52:55], s78 offen offset:0 lds\n" \
"v_mfma_f32_16x16x32_bf16 v[88:91], v[48:51], v[192:195], v[88:91]\n" \
"ds_read_b64_tr_b16 v[6:7], v232 offset:10240\n" \
"v_accvgpr_read_b32 v254, a83\n" \
"v_mfma_f32_16x16x32_bf16 v[88:91], v[52:55], v[196:199], v[88:91]\n" \
"ds_read_b64_tr_b16 v[8:9], v233 offset:0\n" \
"v_cvt_pk_bf16_f32 v243, v243, v252\n" \
"v_mfma_f32_16x16x32_bf16 v[92:95], v[56:59], v[192:195], v[92:95]\n" \
"ds_read_b64_tr_b16 v[10:11], v233 offset:2048\n" \
"v_cvt_pk_bf16_f32 v253, v253, v254\n" \
"v_mfma_f32_16x16x32_bf16 v[92:95], v[60:63], v[196:199], v[92:95]\n" \
"buffer_load_dwordx4 v229, s[52:55], s78 offen offset:1024 lds\n" \
"v_mfma_f32_16x16x32_bf16 v[124:127], v[56:59], v[200:203], v[124:127]\n" \
"s_waitcnt vmcnt(36)\n" \
"ds_read_b128 v[192:195], v230 offset:0\n" \
"v_accvgpr_write_b32 a80, v243\n" \
"v_mfma_f32_16x16x32_bf16 v[124:127], v[60:63], v[204:207], v[124:127]\n" \
"ds_read_b128 v[196:199], v231 offset:0\n" \
"v_accvgpr_write_b32 a81, v253\n" \
"v_mfma_f32_16x16x32_bf16 v[120:123], v[48:51], v[200:203], v[120:123]\n" \
"s_xor_b32 s78, s78, 0x80000\n" \
"ds_read_b64_tr_b16 v[12:13], v233 offset:8192\n" \
"v_accvgpr_read_b32 v243, a112\n" \
"v_mfma_f32_16x16x32_bf16 v[120:123], v[52:55], v[204:207], v[120:123]\n" \
"buffer_load_dwordx4 v228, s[52:55], s89 offen offset:2048 lds\n" \
"v_mfma_f32_16x16x32_bf16 v[116:119], v[40:43], v[200:203], v[116:119]\n" \
"ds_read_b64_tr_b16 v[14:15], v233 offset:10240\n" \
"v_accvgpr_read_b32 v252, a113\n" \
"v_mfma_f32_16x16x32_bf16 v[116:119], v[44:47], v[204:207], v[116:119]\n" \
"ds_read_b64_tr_b16 v[16:17], v232 offset:512\n" \
"v_accvgpr_read_b32 v253, a114\n" \
"v_mfma_f32_16x16x32_bf16 v[112:115], v[32:35], v[200:203], v[112:115]\n" \
"ds_read_b64_tr_b16 v[18:19], v232 offset:2560\n" \
"v_accvgpr_read_b32 v254, a115\n" \
"v_mfma_f32_16x16x32_bf16 v[112:115], v[36:39], v[204:207], v[112:115]\n" \
"buffer_load_dwordx4 v229, s[52:55], s89 offen offset:3072 lds\n" \
"v_mfma_f32_16x16x32_bf16 v[144:147], v[32:35], v[208:211], v[144:147]\n" \
"s_waitcnt vmcnt(36)\n" \
"s_xor_b32 s89, s89, 0x80000\n" \
"ds_read_b128 v[200:203], v230 offset:2048\n" \
"v_mfma_f32_16x16x32_bf16 v[144:147], v[36:39], v[212:215], v[144:147]\n" \
"s_add_u32 m0, s74, 131072\n" \
"ds_read_b128 v[204:207], v231 offset:2048\n" \
"v_cvt_pk_bf16_f32 v243, v243, v252\n" \
"v_mfma_f32_16x16x32_bf16 v[148:151], v[40:43], v[208:211], v[148:151]\n" \
"s_waitcnt lgkmcnt(6)\n" \
"ds_read_b64_tr_b16 v[20:21], v232 offset:8704\n" \
"v_cvt_pk_bf16_f32 v253, v253, v254\n" \
"v_mfma_f32_16x16x32_bf16 v[148:151], v[44:47], v[212:215], v[148:151]\n" \
"buffer_load_dwordx4 v224, s[48:51], s75 offen offset:0 lds\n" \
"v_mfma_f32_16x16x32_bf16 v[152:155], v[48:51], v[208:211], v[152:155]\n" \
"ds_read_b64_tr_b16 v[22:23], v232 offset:10752\n" \
"Lpg3811_%=:\n" \
"s_cbranch_execz Lpg6093_%=\n" \
"v_accvgpr_write_b32 a82, v243\n" \
"v_mfma_f32_16x16x32_bf16 v[152:155], v[52:55], v[212:215], v[152:155]\n" \
"ds_read_b64_tr_b16 v[24:25], v233 offset:512\n" \
"v_accvgpr_write_b32 a83, v253\n" \
"v_mfma_f32_16x16x32_bf16 v[156:159], v[56:59], v[208:211], v[156:159]\n" \
"ds_read_b64_tr_b16 v[26:27], v233 offset:2560\n" \
"v_accvgpr_read_b32 v243, a84\n" \
"v_mfma_f32_16x16x32_bf16 v[156:159], v[60:63], v[212:215], v[156:159]\n" \
"buffer_load_dwordx4 v225, s[48:51], s75 offen offset:1024 lds\n" \
"v_mfma_f32_16x16x32_bf16 v[188:191], v[56:59], v[216:219], v[188:191]\n" \
"s_waitcnt vmcnt(36)\n" \
"ds_read_b128 v[208:211], v230 offset:4096\n" \
"v_accvgpr_read_b32 v252, a85\n" \
"v_mfma_f32_16x16x32_bf16 v[188:191], v[60:63], v[220:223], v[188:191]\n" \
"ds_read_b128 v[212:215], v231 offset:4096\n" \
"v_accvgpr_read_b32 v253, a86\n" \
"v_mfma_f32_16x16x32_bf16 v[184:187], v[48:51], v[216:219], v[184:187]\n" \
"s_waitcnt lgkmcnt(6)\n" \
"ds_read_b64_tr_b16 v[28:29], v233 offset:8704\n" \
"v_accvgpr_read_b32 v254, a87\n" \
"v_mfma_f32_16x16x32_bf16 v[184:187], v[52:55], v[220:223], v[184:187]\n" \
"buffer_load_dwordx4 v226, s[48:51], s75 offen offset:2048 lds\n" \
"v_mfma_f32_16x16x32_bf16 v[180:183], v[40:43], v[216:219], v[180:183]\n" \
"ds_read_b64_tr_b16 v[30:31], v233 offset:10752\n" \
"v_cvt_pk_bf16_f32 v243, v243, v252\n" \
"v_mfma_f32_16x16x32_bf16 v[180:183], v[44:47], v[220:223], v[180:183]\n" \
"v_cvt_pk_bf16_f32 v253, v253, v254\n" \
"v_accvgpr_write_b32 a84, v243\n" \
"v_mfma_f32_16x16x32_bf16 v[176:179], v[32:35], v[216:219], v[176:179]\n" \
"v_accvgpr_write_b32 a85, v253\n" \
"v_accvgpr_read_b32 v243, a116\n" \
"v_mfma_f32_16x16x32_bf16 v[176:179], v[36:39], v[220:223], v[176:179]\n" \
"buffer_load_dwordx4 v227, s[48:51], s75 offen offset:3072 lds\n" \
"v_mfma_f32_16x16x32_bf16 v[64:67], v[0:3], v[192:195], v[64:67]\n" \
"s_waitcnt vmcnt(36)\n" \
"s_xor_b32 s75, s75, 0x80\n" \
"ds_read_b128 v[216:219], v230 offset:6144\n" \
"v_mfma_f32_16x16x32_bf16 v[64:67], v[4:7], v[196:199], v[64:67]\n" \
"s_add_u32 m0, s74, 135168\n" \
"ds_read_b128 v[220:223], v231 offset:6144\n" \
"v_accvgpr_read_b32 v252, a117\n" \
"v_mfma_f32_16x16x32_bf16 v[68:71], v[8:11], v[192:195], v[68:71]\n" \
"s_waitcnt lgkmcnt(4)\n" \
"ds_read_b64_tr_b16 v[32:33], v232 offset:1024\n" \
"v_accvgpr_read_b32 v253, a118\n" \
"v_mfma_f32_16x16x32_bf16 v[68:71], v[12:15], v[196:199], v[68:71]\n" \
"buffer_load_dwordx4 v224, s[48:51], s77 offen offset:0 lds\n" \
"v_mfma_f32_16x16x32_bf16 v[72:75], v[16:19], v[192:195], v[72:75]\n" \
"ds_read_b64_tr_b16 v[34:35], v232 offset:3072\n" \
"v_accvgpr_read_b32 v254, a119\n" \
"v_mfma_f32_16x16x32_bf16 v[72:75], v[20:23], v[196:199], v[72:75]\n" \
"s_waitcnt lgkmcnt(3)\n" \
"ds_read_b64_tr_b16 v[36:37], v232 offset:9216\n" \
"v_cvt_pk_bf16_f32 v243, v243, v252\n" \
"v_mfma_f32_16x16x32_bf16 v[76:79], v[24:27], v[192:195], v[76:79]\n" \
"ds_read_b64_tr_b16 v[38:39], v232 offset:11264\n" \
"v_cvt_pk_bf16_f32 v253, v253, v254\n" \
"v_mfma_f32_16x16x32_bf16 v[76:79], v[28:31], v[196:199], v[76:79]\n" \
"buffer_load_dwordx4 v225, s[48:51], s77 offen offset:1024 lds\n" \
"v_mfma_f32_16x16x32_bf16 v[108:111], v[24:27], v[200:203], v[108:111]\n" \
"ds_read_b64_tr_b16 v[40:41], v233 offset:1024\n" \
"v_accvgpr_write_b32 a86, v243\n" \
"v_mfma_f32_16x16x32_bf16 v[108:111], v[28:31], v[204:207], v[108:111]\n" \
"ds_read_b64_tr_b16 v[42:43], v233 offset:3072\n" \
"v_accvgpr_write_b32 a87, v253\n" \
"v_mfma_f32_16x16x32_bf16 v[104:107], v[16:19], v[200:203], v[104:107]\n" \
"s_waitcnt lgkmcnt(6)\n" \
"ds_read_b64_tr_b16 v[44:45], v233 offset:9216\n" \
"v_accvgpr_read_b32 v243, a88\n" \
"v_mfma_f32_16x16x32_bf16 v[104:107], v[20:23], v[204:207], v[104:107]\n" \
"buffer_load_dwordx4 v226, s[48:51], s77 offen offset:2048 lds\n" \
"v_mfma_f32_16x16x32_bf16 v[100:103], v[8:11], v[200:203], v[100:103]\n" \
"ds_read_b64_tr_b16 v[46:47], v233 offset:11264\n" \
"v_accvgpr_read_b32 v252, a89\n" \
"v_mfma_f32_16x16x32_bf16 v[100:103], v[12:15], v[204:207], v[100:103]\n" \
"ds_read_b64_tr_b16 v[48:49], v232 offset:1536\n" \
"v_accvgpr_read_b32 v253, a90\n" \
"v_mfma_f32_16x16x32_bf16 v[96:99], v[0:3], v[200:203], v[96:99]\n" \
"ds_read_b64_tr_b16 v[50:51], v232 offset:3584\n" \
"v_accvgpr_read_b32 v254, a91\n" \
"v_mfma_f32_16x16x32_bf16 v[96:99], v[4:7], v[204:207], v[96:99]\n" \
"buffer_load_dwordx4 v227, s[48:51], s77 offen offset:3072 lds\n" \
"v_mfma_f32_16x16x32_bf16 v[128:131], v[0:3], v[208:211], v[128:131]\n" \
"ds_read_b64_tr_b16 v[52:53], v232 offset:9728\n" \
"v_cvt_pk_bf16_f32 v243, v243, v252\n" \
"v_mfma_f32_16x16x32_bf16 v[128:131], v[4:7], v[212:215], v[128:131]\n" \
"ds_read_b64_tr_b16 v[54:55], v232 offset:11776\n" \
"v_cvt_pk_bf16_f32 v253, v253, v254\n" \
"v_mfma_f32_16x16x32_bf16 v[132:135], v[8:11], v[208:211], v[132:135]\n" \
"s_xor_b32 s77, s77, 0x80\n" \
"s_add_u32 s93, s87, 17850368\n" \
"ds_read_b64_tr_b16 v[56:57], v233 offset:1536\n" \
"v_mfma_f32_16x16x32_bf16 v[132:135], v[12:15], v[212:215], v[132:135]\n" \
"buffer_store_dwordx4 a[144:147], v234, s[56:59], s93 offen offset:0 sc1\n" \
"v_mfma_f32_16x16x32_bf16 v[136:139], v[16:19], v[208:211], v[136:139]\n" \
"ds_read_b64_tr_b16 v[58:59], v233 offset:3584\n" \
"v_accvgpr_write_b32 a88, v243\n" \
"v_mfma_f32_16x16x32_bf16 v[136:139], v[20:23], v[212:215], v[136:139]\n" \
"ds_read_b64_tr_b16 v[60:61], v233 offset:9728\n" \
"v_accvgpr_write_b32 a89, v253\n" \
"v_mfma_f32_16x16x32_bf16 v[140:143], v[24:27], v[208:211], v[140:143]\n" \
"ds_read_b64_tr_b16 v[62:63], v233 offset:11776\n" \
"v_accvgpr_read_b32 v243, a120\n" \
"v_mfma_f32_16x16x32_bf16 v[140:143], v[28:31], v[212:215], v[140:143]\n" \
"v_accvgpr_read_b32 v252, a121\n" \
"v_accvgpr_read_b32 v253, a122\n" \
"v_mfma_f32_16x16x32_bf16 v[172:175], v[24:27], v[216:219], v[172:175]\n" \
"v_add_u32 v230, 0xffff0000, v230\n" \
"v_accvgpr_read_b32 v254, a123\n" \
"v_mfma_f32_16x16x32_bf16 v[172:175], v[28:31], v[220:223], v[172:175]\n" \
"v_add_u32 v231, 0xffff0000, v231\n" \
"v_cvt_pk_bf16_f32 v243, v243, v252\n" \
"v_mfma_f32_16x16x32_bf16 v[168:171], v[16:19], v[216:219], v[168:171]\n" \
"v_add_u32 v232, 0x4000, v232\n" \
"v_cvt_pk_bf16_f32 v253, v253, v254\n" \
"v_mfma_f32_16x16x32_bf16 v[168:171], v[20:23], v[220:223], v[168:171]\n" \
"buffer_store_dwordx4 a[148:151], v234, s[56:59], s93 offen offset:1024 sc1\n" \
"v_mfma_f32_16x16x32_bf16 v[164:167], v[8:11], v[216:219], v[164:167]\n" \
"v_add_u32 v233, 0x4000, v233\n" \
"v_accvgpr_write_b32 a90, v243\n" \
"v_mfma_f32_16x16x32_bf16 v[164:167], v[12:15], v[220:223], v[164:167]\n" \
"v_accvgpr_write_b32 a91, v253\n" \
"v_accvgpr_read_b32 v243, a92\n" \
"v_mfma_f32_16x16x32_bf16 v[160:163], v[0:3], v[216:219], v[160:163]\n" \
"v_accvgpr_read_b32 v252, a93\n" \
"v_accvgpr_read_b32 v253, a94\n" \
"v_mfma_f32_16x16x32_bf16 v[160:163], v[4:7], v[220:223], v[160:163]\n" \
"s_waitcnt vmcnt(37) lgkmcnt(0)\n" \
"s_barrier\n" \
"v_mfma_f32_16x16x32_bf16 v[80:83], v[32:35], v[192:195], v[80:83]\n" \
"ds_read_b64_tr_b16 v[0:1], v232 offset:0\n" \
"v_accvgpr_read_b32 v254, a95\n" \
"v_mfma_f32_16x16x32_bf16 v[80:83], v[36:39], v[196:199], v[80:83]\n" \
"ds_read_b64_tr_b16 v[2:3], v232 offset:2048\n" \
"v_cvt_pk_bf16_f32 v243, v243, v252\n" \
"v_mfma_f32_16x16x32_bf16 v[84:87], v[40:43], v[192:195], v[84:87]\n" \
"s_add_u32 m0, s79, 16384\n" \
"ds_read_b64_tr_b16 v[4:5], v232 offset:8192\n" \
"v_cvt_pk_bf16_f32 v253, v253, v254\n" \
"v_mfma_f32_16x16x32_bf16 v[84:87], v[44:47], v[196:199], v[84:87]\n" \
"buffer_load_dwordx4 v228, s[52:55], s78 offen offset:0 lds\n" \
"v_mfma_f32_16x16x32_bf16 v[88:91], v[48:51], v[192:195], v[88:91]\n" \
"ds_read_b64_tr_b16 v[6:7], v232 offset:10240\n" \
"v_accvgpr_write_b32 a92, v243\n" \
"v_mfma_f32_16x16x32_bf16 v[88:91], v[52:55], v[196:199], v[88:91]\n" \
"ds_read_b64_tr_b16 v[8:9], v233 offset:0\n" \
"v_accvgpr_write_b32 a93, v253\n" \
"v_mfma_f32_16x16x32_bf16 v[92:95], v[56:59], v[192:195], v[92:95]\n" \
"ds_read_b64_tr_b16 v[10:11], v233 offset:2048\n" \
"v_accvgpr_read_b32 v243, a124\n" \
"v_mfma_f32_16x16x32_bf16 v[92:95], v[60:63], v[196:199], v[92:95]\n" \
"buffer_load_dwordx4 v229, s[52:55], s78 offen offset:1024 lds\n" \
"v_mfma_f32_16x16x32_bf16 v[124:127], v[56:59], v[200:203], v[124:127]\n" \
"s_waitcnt vmcnt(37)\n" \
"ds_read_b128 v[192:195], v230 offset:0\n" \
"v_accvgpr_read_b32 v252, a125\n" \
"v_mfma_f32_16x16x32_bf16 v[124:127], v[60:63], v[204:207], v[124:127]\n" \
"ds_read_b128 v[196:199], v231 offset:0\n" \
"v_accvgpr_read_b32 v253, a126\n" \
"v_mfma_f32_16x16x32_bf16 v[120:123], v[48:51], v[200:203], v[120:123]\n" \
"s_xor_b32 s78, s78, 0x180000\n" \
"ds_read_b64_tr_b16 v[12:13], v233 offset:8192\n" \
"v_accvgpr_read_b32 v254, a127\n" \
"v_mfma_f32_16x16x32_bf16 v[120:123], v[52:55], v[204:207], v[120:123]\n" \
"buffer_load_dwordx4 v228, s[52:55], s89 offen offset:2048 lds\n" \
"v_mfma_f32_16x16x32_bf16 v[116:119], v[40:43], v[200:203], v[116:119]\n" \
"ds_read_b64_tr_b16 v[14:15], v233 offset:10240\n" \
"v_cvt_pk_bf16_f32 v243, v243, v252\n" \
"v_mfma_f32_16x16x32_bf16 v[116:119], v[44:47], v[204:207], v[116:119]\n" \
"ds_read_b64_tr_b16 v[16:17], v232 offset:512\n" \
"v_cvt_pk_bf16_f32 v253, v253, v254\n" \
"v_mfma_f32_16x16x32_bf16 v[112:115], v[32:35], v[200:203], v[112:115]\n" \
"ds_read_b64_tr_b16 v[18:19], v232 offset:2560\n" \
"v_accvgpr_write_b32 a94, v243\n" \
"v_mfma_f32_16x16x32_bf16 v[112:115], v[36:39], v[204:207], v[112:115]\n" \
"buffer_load_dwordx4 v229, s[52:55], s89 offen offset:3072 lds\n" \
"v_mfma_f32_16x16x32_bf16 v[144:147], v[32:35], v[208:211], v[144:147]\n" \
"s_waitcnt vmcnt(37)\n" \
"s_xor_b32 s89, s89, 0x180000\n" \
"ds_read_b128 v[200:203], v230 offset:2048\n" \
"v_mfma_f32_16x16x32_bf16 v[144:147], v[36:39], v[212:215], v[144:147]\n" \
"s_add_u32 m0, s74, 65536\n" \
"ds_read_b128 v[204:207], v231 offset:2048\n" \
"v_accvgpr_write_b32 a95, v253\n" \
"v_mfma_f32_16x16x32_bf16 v[148:151], v[40:43], v[208:211], v[148:151]\n" \
"s_waitcnt lgkmcnt(6)\n" \
"ds_read_b64_tr_b16 v[20:21], v232 offset:8704\n" \
"v_accvgpr_read_b32 v243, a16\n" \
"v_mfma_f32_16x16x32_bf16 v[148:151], v[44:47], v[212:215], v[148:151]\n" \
"buffer_load_dwordx4 v224, s[48:51], s75 offen offset:0 lds\n" \
"v_mfma_f32_16x16x32_bf16 v[152:155], v[48:51], v[208:211], v[152:155]\n" \
"ds_read_b64_tr_b16 v[22:23], v232 offset:10752\n" \
"v_accvgpr_read_b32 v252, a17\n" \
"v_mfma_f32_16x16x32_bf16 v[152:155], v[52:55], v[212:215], v[152:155]\n" \
"ds_read_b64_tr_b16 v[24:25], v233 offset:512\n" \
"v_accvgpr_read_b32 v253, a18\n" \
"v_mfma_f32_16x16x32_bf16 v[156:159], v[56:59], v[208:211], v[156:159]\n" \
"ds_read_b64_tr_b16 v[26:27], v233 offset:2560\n" \
"v_accvgpr_read_b32 v254, a19\n" \
"v_mfma_f32_16x16x32_bf16 v[156:159], v[60:63], v[212:215], v[156:159]\n" \
"buffer_load_dwordx4 v225, s[48:51], s75 offen offset:1024 lds\n" \
"v_mfma_f32_16x16x32_bf16 v[188:191], v[56:59], v[216:219], v[188:191]\n" \
"s_waitcnt vmcnt(37)\n" \
"ds_read_b128 v[208:211], v230 offset:4096\n" \
"v_cvt_pk_bf16_f32 v243, v243, v252\n" \
"v_mfma_f32_16x16x32_bf16 v[188:191], v[60:63], v[220:223], v[188:191]\n" \
"ds_read_b128 v[212:215], v231 offset:4096\n" \
"v_cvt_pk_bf16_f32 v253, v253, v254\n" \
"v_mfma_f32_16x16x32_bf16 v[184:187], v[48:51], v[216:219], v[184:187]\n" \
"s_waitcnt lgkmcnt(6)\n" \
"ds_read_b64_tr_b16 v[28:29], v233 offset:8704\n" \
"v_accvgpr_write_b32 a16, v243\n" \
"v_mfma_f32_16x16x32_bf16 v[184:187], v[52:55], v[220:223], v[184:187]\n" \
"buffer_load_dwordx4 v226, s[48:51], s75 offen offset:2048 lds\n" \
"v_mfma_f32_16x16x32_bf16 v[180:183], v[40:43], v[216:219], v[180:183]\n" \
"ds_read_b64_tr_b16 v[30:31], v233 offset:10752\n" \
"v_accvgpr_write_b32 a17, v253\n" \
"v_mfma_f32_16x16x32_bf16 v[180:183], v[44:47], v[220:223], v[180:183]\n" \
"v_accvgpr_read_b32 v243, a48\n" \
"v_accvgpr_read_b32 v252, a49\n" \
"v_mfma_f32_16x16x32_bf16 v[176:179], v[32:35], v[216:219], v[176:179]\n" \
"v_accvgpr_read_b32 v253, a50\n" \
"v_accvgpr_read_b32 v254, a51\n" \
"v_mfma_f32_16x16x32_bf16 v[176:179], v[36:39], v[220:223], v[176:179]\n" \
"buffer_load_dwordx4 v227, s[48:51], s75 offen offset:3072 lds\n" \
"v_mfma_f32_16x16x32_bf16 v[64:67], v[0:3], v[192:195], v[64:67]\n" \
"s_waitcnt vmcnt(37)\n" \
"s_xor_b32 s75, s75, 0x180\n" \
"ds_read_b128 v[216:219], v230 offset:6144\n" \
"v_mfma_f32_16x16x32_bf16 v[64:67], v[4:7], v[196:199], v[64:67]\n" \
"s_add_u32 m0, s74, 69632\n" \
"ds_read_b128 v[220:223], v231 offset:6144\n" \
"v_cvt_pk_bf16_f32 v243, v243, v252\n" \
"v_mfma_f32_16x16x32_bf16 v[68:71], v[8:11], v[192:195], v[68:71]\n" \
"s_waitcnt lgkmcnt(4)\n" \
"ds_read_b64_tr_b16 v[32:33], v232 offset:1024\n" \
"v_cvt_pk_bf16_f32 v253, v253, v254\n" \
"v_mfma_f32_16x16x32_bf16 v[68:71], v[12:15], v[196:199], v[68:71]\n" \
"buffer_load_dwordx4 v224, s[48:51], s77 offen offset:0 lds\n" \
"v_mfma_f32_16x16x32_bf16 v[72:75], v[16:19], v[192:195], v[72:75]\n" \
"ds_read_b64_tr_b16 v[34:35], v232 offset:3072\n" \
"v_accvgpr_write_b32 a18, v243\n" \
"v_mfma_f32_16x16x32_bf16 v[72:75], v[20:23], v[196:199], v[72:75]\n" \
"s_waitcnt lgkmcnt(3)\n" \
"ds_read_b64_tr_b16 v[36:37], v232 offset:9216\n" \
"v_accvgpr_write_b32 a19, v253\n" \
"v_mfma_f32_16x16x32_bf16 v[76:79], v[24:27], v[192:195], v[76:79]\n" \
"ds_read_b64_tr_b16 v[38:39], v232 offset:11264\n" \
"v_accvgpr_read_b32 v243, a20\n" \
"v_mfma_f32_16x16x32_bf16 v[76:79], v[28:31], v[196:199], v[76:79]\n" \
"buffer_load_dwordx4 v225, s[48:51], s77 offen offset:1024 lds\n" \
"v_mfma_f32_16x16x32_bf16 v[108:111], v[24:27], v[200:203], v[108:111]\n" \
"ds_read_b64_tr_b16 v[40:41], v233 offset:1024\n" \
"v_accvgpr_read_b32 v252, a21\n" \
"v_mfma_f32_16x16x32_bf16 v[108:111], v[28:31], v[204:207], v[108:111]\n" \
"ds_read_b64_tr_b16 v[42:43], v233 offset:3072\n" \
"v_accvgpr_read_b32 v253, a22\n" \
"v_mfma_f32_16x16x32_bf16 v[104:107], v[16:19], v[200:203], v[104:107]\n" \
"s_waitcnt lgkmcnt(6)\n" \
"ds_read_b64_tr_b16 v[44:45], v233 offset:9216\n" \
"v_accvgpr_read_b32 v254, a23\n" \
"v_mfma_f32_16x16x32_bf16 v[104:107], v[20:23], v[204:207], v[104:107]\n" \
"buffer_load_dwordx4 v226, s[48:51], s77 offen offset:2048 lds\n" \
"v_mfma_f32_16x16x32_bf16 v[100:103], v[8:11], v[200:203], v[100:103]\n" \
"ds_read_b64_tr_b16 v[46:47], v233 offset:11264\n" \
"v_cvt_pk_bf16_f32 v243, v243, v252\n" \
"v_mfma_f32_16x16x32_bf16 v[100:103], v[12:15], v[204:207], v[100:103]\n" \
"ds_read_b64_tr_b16 v[48:49], v232 offset:1536\n" \
"v_cvt_pk_bf16_f32 v253, v253, v254\n" \
"v_mfma_f32_16x16x32_bf16 v[96:99], v[0:3], v[200:203], v[96:99]\n" \
"ds_read_b64_tr_b16 v[50:51], v232 offset:3584\n" \
"v_accvgpr_write_b32 a20, v243\n" \
"v_mfma_f32_16x16x32_bf16 v[96:99], v[4:7], v[204:207], v[96:99]\n" \
"buffer_load_dwordx4 v227, s[48:51], s77 offen offset:3072 lds\n" \
"v_mfma_f32_16x16x32_bf16 v[128:131], v[0:3], v[208:211], v[128:131]\n" \
"ds_read_b64_tr_b16 v[52:53], v232 offset:9728\n" \
"v_accvgpr_write_b32 a21, v253\n" \
"v_mfma_f32_16x16x32_bf16 v[128:131], v[4:7], v[212:215], v[128:131]\n" \
"ds_read_b64_tr_b16 v[54:55], v232 offset:11776\n" \
"v_accvgpr_read_b32 v243, a52\n" \
"v_mfma_f32_16x16x32_bf16 v[132:135], v[8:11], v[208:211], v[132:135]\n" \
"s_xor_b32 s77, s77, 0x180\n" \
"ds_read_b64_tr_b16 v[56:57], v233 offset:1536\n" \
"v_accvgpr_read_b32 v252, a53\n" \
"v_mfma_f32_16x16x32_bf16 v[132:135], v[12:15], v[212:215], v[132:135]\n" \
"v_accvgpr_read_b32 v253, a54\n" \
"v_accvgpr_read_b32 v254, a55\n" \
"v_mfma_f32_16x16x32_bf16 v[136:139], v[16:19], v[208:211], v[136:139]\n" \
"ds_read_b64_tr_b16 v[58:59], v233 offset:3584\n" \
"v_cvt_pk_bf16_f32 v243, v243, v252\n" \
"v_mfma_f32_16x16x32_bf16 v[136:139], v[20:23], v[212:215], v[136:139]\n" \
"ds_read_b64_tr_b16 v[60:61], v233 offset:9728\n" \
"v_cvt_pk_bf16_f32 v253, v253, v254\n" \
"v_mfma_f32_16x16x32_bf16 v[140:143], v[24:27], v[208:211], v[140:143]\n" \
"ds_read_b64_tr_b16 v[62:63], v233 offset:11776\n" \
"v_accvgpr_write_b32 a22, v243\n" \
"v_mfma_f32_16x16x32_bf16 v[140:143], v[28:31], v[212:215], v[140:143]\n" \
"buffer_store_dwordx4 a[152:155], v234, s[56:59], s93 offen offset:2048 sc1\n" \
"v_mfma_f32_16x16x32_bf16 v[172:175], v[24:27], v[216:219], v[172:175]\n" \
"v_add_u32 v230, 0x8000, v230\n" \
"v_accvgpr_write_b32 a23, v253\n" \
"v_mfma_f32_16x16x32_bf16 v[172:175], v[28:31], v[220:223], v[172:175]\n" \
"v_add_u32 v231, 0x8000, v231\n" \
"v_accvgpr_read_b32 v243, a24\n" \
"v_mfma_f32_16x16x32_bf16 v[168:171], v[16:19], v[216:219], v[168:171]\n" \
"v_add_u32 v232, 0x4000, v232\n" \
"v_accvgpr_read_b32 v252, a25\n" \
"v_mfma_f32_16x16x32_bf16 v[168:171], v[20:23], v[220:223], v[168:171]\n" \
"v_accvgpr_read_b32 v253, a26\n" \
"v_accvgpr_read_b32 v254, a27\n" \
"v_mfma_f32_16x16x32_bf16 v[164:167], v[8:11], v[216:219], v[164:167]\n" \
"v_add_u32 v233, 0x4000, v233\n" \
"v_cvt_pk_bf16_f32 v243, v243, v252\n" \
"v_mfma_f32_16x16x32_bf16 v[164:167], v[12:15], v[220:223], v[164:167]\n" \
"v_cvt_pk_bf16_f32 v253, v253, v254\n" \
"v_accvgpr_write_b32 a24, v243\n" \
"v_mfma_f32_16x16x32_bf16 v[160:163], v[0:3], v[216:219], v[160:163]\n" \
"v_accvgpr_write_b32 a25, v253\n" \
"v_accvgpr_read_b32 v243, a56\n" \
"v_mfma_f32_16x16x32_bf16 v[160:163], v[4:7], v[220:223], v[160:163]\n" \
"s_waitcnt vmcnt(36) lgkmcnt(0)\n" \
"s_barrier\n" \
"v_mfma_f32_16x16x32_bf16 v[80:83], v[32:35], v[192:195], v[80:83]\n" \
"ds_read_b64_tr_b16 v[0:1], v232 offset:0\n" \
"v_accvgpr_read_b32 v252, a57\n" \
"v_mfma_f32_16x16x32_bf16 v[80:83], v[36:39], v[196:199], v[80:83]\n" \
"ds_read_b64_tr_b16 v[2:3], v232 offset:2048\n" \
"v_accvgpr_read_b32 v253, a58\n" \
"v_mfma_f32_16x16x32_bf16 v[84:87], v[40:43], v[192:195], v[84:87]\n" \
"s_add_u32 m0, s79, 32768\n" \
"ds_read_b64_tr_b16 v[4:5], v232 offset:8192\n" \
"v_accvgpr_read_b32 v254, a59\n" \
"v_mfma_f32_16x16x32_bf16 v[84:87], v[44:47], v[196:199], v[84:87]\n" \
"buffer_load_dwordx4 v228, s[52:55], s78 offen offset:0 lds\n" \
"v_mfma_f32_16x16x32_bf16 v[88:91], v[48:51], v[192:195], v[88:91]\n" \
"ds_read_b64_tr_b16 v[6:7], v232 offset:10240\n" \
"v_cvt_pk_bf16_f32 v243, v243, v252\n" \
"v_mfma_f32_16x16x32_bf16 v[88:91], v[52:55], v[196:199], v[88:91]\n" \
"ds_read_b64_tr_b16 v[8:9], v233 offset:0\n" \
"v_cvt_pk_bf16_f32 v253, v253, v254\n" \
"v_mfma_f32_16x16x32_bf16 v[92:95], v[56:59], v[192:195], v[92:95]\n" \
"ds_read_b64_tr_b16 v[10:11], v233 offset:2048\n" \
"v_accvgpr_write_b32 a26, v243\n" \
"v_mfma_f32_16x16x32_bf16 v[92:95], v[60:63], v[196:199], v[92:95]\n" \
"buffer_load_dwordx4 v229, s[52:55], s78 offen offset:1024 lds\n" \
"v_mfma_f32_16x16x32_bf16 v[124:127], v[56:59], v[200:203], v[124:127]\n" \
"s_waitcnt vmcnt(36)\n" \
"ds_read_b128 v[192:195], v230 offset:0\n" \
"v_accvgpr_write_b32 a27, v253\n" \
"v_mfma_f32_16x16x32_bf16 v[124:127], v[60:63], v[204:207], v[124:127]\n" \
"ds_read_b128 v[196:199], v231 offset:0\n" \
"v_accvgpr_read_b32 v243, a28\n" \
"v_mfma_f32_16x16x32_bf16 v[120:123], v[48:51], v[200:203], v[120:123]\n" \
"s_xor_b32 s78, s78, 0x80000\n" \
"ds_read_b64_tr_b16 v[12:13], v233 offset:8192\n" \
"v_accvgpr_read_b32 v252, a29\n" \
"v_mfma_f32_16x16x32_bf16 v[120:123], v[52:55], v[204:207], v[120:123]\n" \
"buffer_load_dwordx4 v228, s[52:55], s89 offen offset:2048 lds\n" \
"v_mfma_f32_16x16x32_bf16 v[116:119], v[40:43], v[200:203], v[116:119]\n" \
"ds_read_b64_tr_b16 v[14:15], v233 offset:10240\n" \
"v_accvgpr_read_b32 v253, a30\n" \
"v_mfma_f32_16x16x32_bf16 v[116:119], v[44:47], v[204:207], v[116:119]\n" \
"ds_read_b64_tr_b16 v[16:17], v232 offset:512\n" \
"v_accvgpr_read_b32 v254, a31\n" \
"v_mfma_f32_16x16x32_bf16 v[112:115], v[32:35], v[200:203], v[112:115]\n" \
"ds_read_b64_tr_b16 v[18:19], v232 offset:2560\n" \
"v_cvt_pk_bf16_f32 v243, v243, v252\n" \
"v_mfma_f32_16x16x32_bf16 v[112:115], v[36:39], v[204:207], v[112:115]\n" \
"buffer_load_dwordx4 v229, s[52:55], s89 offen offset:3072 lds\n" \
"v_mfma_f32_16x16x32_bf16 v[144:147], v[32:35], v[208:211], v[144:147]\n" \
"s_waitcnt vmcnt(36)\n" \
"s_xor_b32 s89, s89, 0x80000\n" \
"ds_read_b128 v[200:203], v230 offset:2048\n" \
"v_mfma_f32_16x16x32_bf16 v[144:147], v[36:39], v[212:215], v[144:147]\n" \
"s_add_u32 m0, s74, 98304\n" \
"ds_read_b128 v[204:207], v231 offset:2048\n" \
"v_cvt_pk_bf16_f32 v253, v253, v254\n" \
"v_mfma_f32_16x16x32_bf16 v[148:151], v[40:43], v[208:211], v[148:151]\n" \
"s_waitcnt lgkmcnt(6)\n" \
"ds_read_b64_tr_b16 v[20:21], v232 offset:8704\n" \
"v_accvgpr_write_b32 a28, v243\n" \
"v_mfma_f32_16x16x32_bf16 v[148:151], v[44:47], v[212:215], v[148:151]\n" \
"buffer_load_dwordx4 v224, s[48:51], s75 offen offset:0 lds\n" \
"v_mfma_f32_16x16x32_bf16 v[152:155], v[48:51], v[208:211], v[152:155]\n" \
"ds_read_b64_tr_b16 v[22:23], v232 offset:10752\n" \
"v_accvgpr_write_b32 a29, v253\n" \
"v_mfma_f32_16x16x32_bf16 v[152:155], v[52:55], v[212:215], v[152:155]\n" \
"ds_read_b64_tr_b16 v[24:25], v233 offset:512\n" \
"v_accvgpr_read_b32 v243, a60\n" \
"v_mfma_f32_16x16x32_bf16 v[156:159], v[56:59], v[208:211], v[156:159]\n" \
"ds_read_b64_tr_b16 v[26:27], v233 offset:2560\n" \
"v_accvgpr_read_b32 v252, a61\n" \
"v_mfma_f32_16x16x32_bf16 v[156:159], v[60:63], v[212:215], v[156:159]\n" \
"buffer_load_dwordx4 v225, s[48:51], s75 offen offset:1024 lds\n" \
"v_mfma_f32_16x16x32_bf16 v[188:191], v[56:59], v[216:219], v[188:191]\n" \
"s_waitcnt vmcnt(36)\n" \
"ds_read_b128 v[208:211], v230 offset:4096\n" \
"v_accvgpr_read_b32 v253, a62\n" \
"v_mfma_f32_16x16x32_bf16 v[188:191], v[60:63], v[220:223], v[188:191]\n" \
"ds_read_b128 v[212:215], v231 offset:4096\n" \
"v_accvgpr_read_b32 v254, a63\n" \
"v_mfma_f32_16x16x32_bf16 v[184:187], v[48:51], v[216:219], v[184:187]\n" \
"s_waitcnt lgkmcnt(6)\n" \
"ds_read_b64_tr_b16 v[28:29], v233 offset:8704\n" \
"v_cvt_pk_bf16_f32 v243, v243, v252\n" \
"v_mfma_f32_16x16x32_bf16 v[184:187], v[52:55], v[220:223], v[184:187]\n" \
"buffer_load_dwordx4 v226, s[48:51], s75 offen offset:2048 lds\n" \
"v_mfma_f32_16x16x32_bf16 v[180:183], v[40:43], v[216:219], v[180:183]\n" \
"ds_read_b64_tr_b16 v[30:31], v233 offset:10752\n" \
"v_cvt_pk_bf16_f32 v253, v253, v254\n" \
"v_mfma_f32_16x16x32_bf16 v[180:183], v[44:47], v[220:223], v[180:183]\n" \
"v_accvgpr_write_b32 a30, v243\n" \
"v_accvgpr_write_b32 a31, v253\n" \
"v_mfma_f32_16x16x32_bf16 v[176:179], v[32:35], v[216:219], v[176:179]\n" \
"v_lshrrev_b32 v244, 4, v234\n" \
"v_bfe_u32 v245, v244, 4, 1\n" \
"v_mfma_f32_16x16x32_bf16 v[176:179], v[36:39], v[220:223], v[176:179]\n" \
"buffer_load_dwordx4 v227, s[48:51], s75 offen offset:3072 lds\n" \
"v_mfma_f32_16x16x32_bf16 v[64:67], v[0:3], v[192:195], v[64:67]\n" \
"s_waitcnt vmcnt(36)\n" \
"s_xor_b32 s75, s75, 0x80\n" \
"ds_read_b128 v[216:219], v230 offset:6144\n" \
"v_mfma_f32_16x16x32_bf16 v[64:67], v[4:7], v[196:199], v[64:67]\n" \
"s_add_u32 m0, s74, 102400\n" \
"ds_read_b128 v[220:223], v231 offset:6144\n" \
"v_bfe_u32 v246, v244, 2, 2\n" \
"v_mfma_f32_16x16x32_bf16 v[68:71], v[8:11], v[192:195], v[68:71]\n" \
"s_waitcnt lgkmcnt(4)\n" \
"ds_read_b64_tr_b16 v[32:33], v232 offset:1024\n" \
"v_lshl_add_u32 v246, v245, 3, v246\n" \
"v_mfma_f32_16x16x32_bf16 v[68:71], v[12:15], v[196:199], v[68:71]\n" \
"buffer_load_dwordx4 v224, s[48:51], s77 offen offset:0 lds\n" \
"v_mfma_f32_16x16x32_bf16 v[72:75], v[16:19], v[192:195], v[72:75]\n" \
"s_lshl_b32 s71, s61, 4\n" \
"ds_read_b64_tr_b16 v[34:35], v232 offset:3072\n" \
"v_add_u32 v246, s71, v246\n" \
"v_mfma_f32_16x16x32_bf16 v[72:75], v[20:23], v[196:199], v[72:75]\n" \
"s_waitcnt lgkmcnt(3)\n" \
"ds_read_b64_tr_b16 v[36:37], v232 offset:9216\n" \
"v_mul_u32_u24 v246, 6144, v246\n" \
"v_mfma_f32_16x16x32_bf16 v[76:79], v[24:27], v[192:195], v[76:79]\n" \
"ds_read_b64_tr_b16 v[38:39], v232 offset:11264\n" \
"v_lshlrev_b32 v245, 1, v245\n" \
"v_mfma_f32_16x16x32_bf16 v[76:79], v[28:31], v[196:199], v[76:79]\n" \
"buffer_load_dwordx4 v225, s[48:51], s77 offen offset:1024 lds\n" \
"v_mfma_f32_16x16x32_bf16 v[108:111], v[24:27], v[200:203], v[108:111]\n" \
"ds_read_b64_tr_b16 v[40:41], v233 offset:1024\n" \
"v_and_b32 v247, 3, v244\n" \
"v_mfma_f32_16x16x32_bf16 v[108:111], v[28:31], v[204:207], v[108:111]\n" \
"ds_read_b64_tr_b16 v[42:43], v233 offset:3072\n" \
"v_xor_b32 v245, v245, v247\n" \
"v_mfma_f32_16x16x32_bf16 v[104:107], v[16:19], v[200:203], v[104:107]\n" \
"s_waitcnt lgkmcnt(6)\n" \
"ds_read_b64_tr_b16 v[44:45], v233 offset:9216\n" \
"v_lshl_add_u32 v246, v245, 4, v246\n" \
"v_mfma_f32_16x16x32_bf16 v[104:107], v[20:23], v[204:207], v[104:107]\n" \
"buffer_load_dwordx4 v226, s[48:51], s77 offen offset:2048 lds\n" \
"v_mfma_f32_16x16x32_bf16 v[100:103], v[8:11], v[200:203], v[100:103]\n" \
"ds_read_b64_tr_b16 v[46:47], v233 offset:11264\n" \
"v_lshrrev_b32 v245, 5, v244\n" \
"v_mfma_f32_16x16x32_bf16 v[100:103], v[12:15], v[204:207], v[100:103]\n" \
"ds_read_b64_tr_b16 v[48:49], v232 offset:1536\n" \
"v_lshl_add_u32 v250, v245, 6, v246\n" \
"v_mfma_f32_16x16x32_bf16 v[96:99], v[0:3], v[200:203], v[96:99]\n" \
"ds_read_b64_tr_b16 v[50:51], v232 offset:3584\n" \
"v_accvgpr_write_b32 a56, v250\n" \
"v_mfma_f32_16x16x32_bf16 v[96:99], v[4:7], v[204:207], v[96:99]\n" \
"buffer_load_dwordx4 v227, s[48:51], s77 offen offset:3072 lds\n" \
"v_mfma_f32_16x16x32_bf16 v[128:131], v[0:3], v[208:211], v[128:131]\n" \
"ds_read_b64_tr_b16 v[52:53], v232 offset:9728\n" \
"v_lshrrev_b32 v247, 3, v244\n" \
"v_mfma_f32_16x16x32_bf16 v[128:131], v[4:7], v[212:215], v[128:131]\n" \
"ds_read_b64_tr_b16 v[54:55], v232 offset:11776\n" \
"v_and_b32 v248, 7, v244\n" \
"v_mfma_f32_16x16x32_bf16 v[132:135], v[8:11], v[208:211], v[132:135]\n" \
"s_xor_b32 s77, s77, 0x80\n" \
"ds_read_b64_tr_b16 v[56:57], v233 offset:1536\n" \
"v_xor_b32 v249, v248, v247\n" \
"v_mfma_f32_16x16x32_bf16 v[132:135], v[12:15], v[212:215], v[132:135]\n" \
"v_lshlrev_b32 v249, 4, v249\n" \
"v_lshl_add_u32 v250, v247, 12, v249\n" \
"v_mfma_f32_16x16x32_bf16 v[136:139], v[16:19], v[208:211], v[136:139]\n" \
"ds_read_b64_tr_b16 v[58:59], v233 offset:3584\n" \
"v_accvgpr_write_b32 a57, v250\n" \
"v_mfma_f32_16x16x32_bf16 v[136:139], v[20:23], v[212:215], v[136:139]\n" \
"s_and_b32 s70, s61, 1\n" \
"s_lshl_b32 s71, s70, 2\n" \
"ds_read_b64_tr_b16 v[60:61], v233 offset:9728\n" \
"v_mfma_f32_16x16x32_bf16 v[140:143], v[24:27], v[208:211], v[140:143]\n" \
"ds_read_b64_tr_b16 v[62:63], v233 offset:11776\n" \
"v_add_u32 v247, s71, v247\n" \
"v_mfma_f32_16x16x32_bf16 v[140:143], v[28:31], v[212:215], v[140:143]\n" \
"buffer_store_dwordx4 a[156:159], v234, s[56:59], s93 offen offset:3072 sc1\n" \
"v_mfma_f32_16x16x32_bf16 v[172:175], v[24:27], v[216:219], v[172:175]\n" \
"v_add_u32 v230, 0x8000, v230\n" \
"v_xor_b32 v249, v248, v247\n" \
"v_mfma_f32_16x16x32_bf16 v[172:175], v[28:31], v[220:223], v[172:175]\n" \
"v_add_u32 v231, 0x8000, v231\n" \
"v_lshlrev_b32 v249, 4, v249\n" \
"v_mfma_f32_16x16x32_bf16 v[168:171], v[16:19], v[216:219], v[168:171]\n" \
"v_add_u32 v232, 0xffff4000, v232\n" \
"v_lshl_add_u32 v250, v247, 12, v249\n" \
"v_mfma_f32_16x16x32_bf16 v[168:171], v[20:23], v[220:223], v[168:171]\n" \
"s_lshr_b32 s71, s61, 1\n" \
"s_lshl_b32 s70, s71, 11\n" \
"v_lshl_add_u32 v250, v245, 30, v250\n" \
"v_mfma_f32_16x16x32_bf16 v[164:167], v[8:11], v[216:219], v[164:167]\n" \
"v_add_u32 v233, 0xffff4000, v233\n" \
"v_add_u32 v250, s70, v250\n" \
"v_mfma_f32_16x16x32_bf16 v[164:167], v[12:15], v[220:223], v[164:167]\n" \
"v_accvgpr_write_b32 a58, v250\n" \
"v_lshrrev_b32 v247, 4, v244\n" \
"v_mfma_f32_16x16x32_bf16 v[160:163], v[0:3], v[216:219], v[160:163]\n" \
"v_xor_b32 v247, v247, v248\n" \
"v_and_b32 v249, 15, v244\n" \
"v_mfma_f32_16x16x32_bf16 v[160:163], v[4:7], v[220:223], v[160:163]\n" \
"s_waitcnt vmcnt(36) lgkmcnt(0)\n" \
"s_barrier\n" \
"v_mfma_f32_16x16x32_bf16 v[80:83], v[32:35], v[192:195], v[80:83]\n" \
"ds_read_b64_tr_b16 v[0:1], v232 offset:0\n" \
"v_lshlrev_b32 v246, 7, v249\n" \
"v_mfma_f32_16x16x32_bf16 v[80:83], v[36:39], v[196:199], v[80:83]\n" \
"ds_read_b64_tr_b16 v[2:3], v232 offset:2048\n" \
"v_lshl_add_u32 v246, v247, 4, v246\n" \
"v_mfma_f32_16x16x32_bf16 v[84:87], v[40:43], v[192:195], v[84:87]\n" \
"s_add_u32 m0, s79, 49152\n" \
"s_add_u32 s70, s70, %3\n" \
"ds_read_b64_tr_b16 v[4:5], v232 offset:8192\n" \
"v_mfma_f32_16x16x32_bf16 v[84:87], v[44:47], v[196:199], v[84:87]\n" \
"buffer_load_dwordx4 v228, s[52:55], s78 offen offset:0 lds\n" \
"v_mfma_f32_16x16x32_bf16 v[88:91], v[48:51], v[192:195], v[88:91]\n" \
"s_mul_i32 s73, s62, 557824\n" \
"s_add_u32 s85, s73, 17850368\n" \
"ds_read_b64_tr_b16 v[6:7], v232 offset:10240\n" \
"v_mfma_f32_16x16x32_bf16 v[88:91], v[52:55], v[196:199], v[88:91]\n" \
"s_mul_i32 s83, s63, 34816\n" \
"s_sub_u32 s86, 557056, s83\n" \
"ds_read_b64_tr_b16 v[8:9], v233 offset:0\n" \
"v_mfma_f32_16x16x32_bf16 v[92:95], v[56:59], v[192:195], v[92:95]\n" \
"Lpg4351_%=:\n" \
"s_cbranch_execz Lpg6217_%=\n" \
"ds_read_b64_tr_b16 v[10:11], v233 offset:2048\n" \
"v_add_u32 v237, %3, v236\n" \
"v_mfma_f32_16x16x32_bf16 v[92:95], v[60:63], v[196:199], v[92:95]\n" \
"buffer_load_dwordx4 v229, s[52:55], s78 offen offset:1024 lds\n" \
"v_mfma_f32_16x16x32_bf16 v[124:127], v[56:59], v[200:203], v[124:127]\n" \
"s_waitcnt vmcnt(36)\n" \
"ds_read_b128 v[192:195], v230 offset:0\n" \
"v_add_u32 v250, s70, v246\n" \
"v_mfma_f32_16x16x32_bf16 v[124:127], v[60:63], v[204:207], v[124:127]\n" \
"s_add_u32 s36, s56, s73\n" \
"s_addc_u32 s37, s57, 0\n" \
"ds_read_b128 v[196:199], v231 offset:0\n" \
"v_mfma_f32_16x16x32_bf16 v[120:123], v[48:51], v[200:203], v[120:123]\n" \
"s_mov_b32 s38, s86\n" \
"s_mov_b32 s39, 0x20000\n" \
"ds_read_b64_tr_b16 v[12:13], v233 offset:8192\n" \
"v_mfma_f32_16x16x32_bf16 v[120:123], v[52:55], v[204:207], v[120:123]\n" \
"buffer_load_dwordx4 v228, s[52:55], s89 offen offset:2048 lds\n" \
"v_mfma_f32_16x16x32_bf16 v[116:119], v[40:43], v[200:203], v[116:119]\n" \
"ds_read_b64_tr_b16 v[14:15], v233 offset:10240\n" \
"v_add_u32 v235, %3, v234\n" \
"v_mfma_f32_16x16x32_bf16 v[116:119], v[44:47], v[204:207], v[116:119]\n" \
"s_add_u32 s40, s56, s85\n" \
"s_addc_u32 s41, s57, 0\n" \
"ds_read_b64_tr_b16 v[16:17], v232 offset:512\n" \
"v_mfma_f32_16x16x32_bf16 v[112:115], v[32:35], v[200:203], v[112:115]\n" \
"s_mov_b32 s42, s86\n" \
"s_mov_b32 s43, 0x20000\n" \
"ds_read_b64_tr_b16 v[18:19], v232 offset:2560\n" \
"v_mfma_f32_16x16x32_bf16 v[112:115], v[36:39], v[204:207], v[112:115]\n" \
"buffer_load_dwordx4 v229, s[52:55], s89 offen offset:3072 lds\n" \
"v_mfma_f32_16x16x32_bf16 v[144:147], v[32:35], v[208:211], v[144:147]\n" \
"s_waitcnt vmcnt(36)\n" \
"s_add_u32 s85, s74, 131072\n" \
"ds_read_b128 v[200:203], v230 offset:2048\n" \
"v_mfma_f32_16x16x32_bf16 v[144:147], v[36:39], v[212:215], v[144:147]\n" \
"s_add_u32 m0, s74, 131072\n" \
"ds_read_b128 v[204:207], v231 offset:2048\n" \
"v_add_u32 v239, s85, v234\n" \
"v_mfma_f32_16x16x32_bf16 v[148:151], v[40:43], v[208:211], v[148:151]\n" \
"s_waitcnt lgkmcnt(6)\n" \
"s_lshl_b32 s73, s61, 12\n" \
"ds_read_b64_tr_b16 v[20:21], v232 offset:8704\n" \
"v_mfma_f32_16x16x32_bf16 v[148:151], v[44:47], v[212:215], v[148:151]\n" \
"buffer_load_dwordx4 v224, s[48:51], s75 offen offset:0 lds\n" \
"v_mfma_f32_16x16x32_bf16 v[152:155], v[48:51], v[208:211], v[152:155]\n" \
"s_add_u32 s80, s79, 0x10000\n" \
"s_add_u32 s83, s83, s73\n" \
"ds_read_b64_tr_b16 v[22:23], v232 offset:10752\n" \
"v_mfma_f32_16x16x32_bf16 v[152:155], v[52:55], v[212:215], v[152:155]\n" \
"s_cmp_eq_u32 s63, 0\n" \
"s_cselect_b32 s85, 34816, 0\n" \
"ds_read_b64_tr_b16 v[24:25], v233 offset:512\n" \
"v_mfma_f32_16x16x32_bf16 v[156:159], v[56:59], v[208:211], v[156:159]\n" \
"s_add_u32 s73, s73, s85\n" \
"ds_read_b64_tr_b16 v[26:27], v233 offset:2560\n" \
"v_accvgpr_write_b32 a59, v250\n" \
"v_mfma_f32_16x16x32_bf16 v[156:159], v[60:63], v[212:215], v[156:159]\n" \
"buffer_load_dwordx4 v225, s[48:51], s75 offen offset:1024 lds\n" \
"v_mfma_f32_16x16x32_bf16 v[188:191], v[56:59], v[216:219], v[188:191]\n" \
"s_waitcnt vmcnt(36)\n" \
"ds_read_b128 v[208:211], v230 offset:4096\n" \
"v_lshrrev_b32 v249, 2, v249\n" \
"v_mfma_f32_16x16x32_bf16 v[188:191], v[60:63], v[220:223], v[188:191]\n" \
"ds_read_b128 v[212:215], v231 offset:4096\n" \
"v_lshl_add_u32 v249, v249, 9, v246\n" \
"v_mfma_f32_16x16x32_bf16 v[184:187], v[48:51], v[216:219], v[184:187]\n" \
"s_waitcnt lgkmcnt(6)\n" \
"s_add_u32 s70, %3, 16384\n" \
"ds_read_b64_tr_b16 v[28:29], v233 offset:8704\n" \
"v_mfma_f32_16x16x32_bf16 v[184:187], v[52:55], v[220:223], v[184:187]\n" \
"buffer_load_dwordx4 v226, s[48:51], s75 offen offset:2048 lds\n" \
"v_mfma_f32_16x16x32_bf16 v[180:183], v[40:43], v[216:219], v[180:183]\n" \
"ds_read_b64_tr_b16 v[30:31], v233 offset:10752\n" \
"v_add_u32 v250, s70, v249\n" \
"v_mfma_f32_16x16x32_bf16 v[180:183], v[44:47], v[220:223], v[180:183]\n" \
"v_accvgpr_write_b32 a60, v250\n" \
"v_and_b32 v246, 3, v244\n" \
"v_mfma_f32_16x16x32_bf16 v[176:179], v[32:35], v[216:219], v[176:179]\n" \
"v_lshlrev_b32 v246, 3, v246\n" \
"v_bfe_u32 v247, v244, 2, 2\n" \
"v_mfma_f32_16x16x32_bf16 v[176:179], v[36:39], v[220:223], v[176:179]\n" \
"buffer_load_dwordx4 v227, s[48:51], s75 offen offset:3072 lds\n" \
"v_mfma_f32_16x16x32_bf16 v[64:67], v[0:3], v[192:195], v[64:67]\n" \
"s_waitcnt vmcnt(36)\n" \
"ds_read_b128 v[216:219], v230 offset:6144\n" \
"v_lshl_add_u32 v246, v247, 6, v246\n" \
"v_mfma_f32_16x16x32_bf16 v[64:67], v[4:7], v[196:199], v[64:67]\n" \
"s_add_u32 m0, s74, 135168\n" \
"ds_read_b128 v[220:223], v231 offset:6144\n" \
"v_bfe_u32 v247, v244, 4, 1\n" \
"v_mfma_f32_16x16x32_bf16 v[68:71], v[8:11], v[192:195], v[68:71]\n" \
"s_waitcnt lgkmcnt(4)\n" \
"ds_read_b64_tr_b16 v[32:33], v232 offset:1024\n" \
"v_mul_u32_u24 v247, 288, v247\n" \
"v_mfma_f32_16x16x32_bf16 v[68:71], v[12:15], v[196:199], v[68:71]\n" \
"buffer_load_dwordx4 v224, s[48:51], s77 offen offset:0 lds\n" \
"v_mfma_f32_16x16x32_bf16 v[72:75], v[16:19], v[192:195], v[72:75]\n" \
"ds_read_b64_tr_b16 v[34:35], v232 offset:3072\n" \
"v_mul_u32_u24 v248, 6144, v245\n" \
"v_mfma_f32_16x16x32_bf16 v[72:75], v[20:23], v[196:199], v[72:75]\n" \
"s_waitcnt lgkmcnt(3)\n" \
"ds_read_b64_tr_b16 v[36:37], v232 offset:9216\n" \
"v_add3_u32 v246, v246, v247, v248\n" \
"v_mfma_f32_16x16x32_bf16 v[76:79], v[24:27], v[192:195], v[76:79]\n" \
"s_and_b32 s70, s61, 1\n" \
"s_lshl_b32 s70, s70, 9\n" \
"ds_read_b64_tr_b16 v[38:39], v232 offset:11264\n" \
"v_mfma_f32_16x16x32_bf16 v[76:79], v[28:31], v[196:199], v[76:79]\n" \
"buffer_load_dwordx4 v225, s[48:51], s77 offen offset:1024 lds\n" \
"v_mfma_f32_16x16x32_bf16 v[108:111], v[24:27], v[200:203], v[108:111]\n" \
"s_add_u32 s70, s70, %3\n" \
"ds_read_b64_tr_b16 v[40:41], v233 offset:1024\n" \
"v_add_u32 v250, s70, v246\n" \
"v_mfma_f32_16x16x32_bf16 v[108:111], v[28:31], v[204:207], v[108:111]\n" \
"s_lshl_b32 s70, s71, 5\n" \
"ds_read_b64_tr_b16 v[42:43], v233 offset:3072\n" \
"v_xor_b32 v250, s70, v250\n" \
"v_mfma_f32_16x16x32_bf16 v[104:107], v[16:19], v[200:203], v[104:107]\n" \
"s_waitcnt lgkmcnt(6)\n" \
"ds_read_b64_tr_b16 v[44:45], v233 offset:9216\n" \
"v_accvgpr_write_b32 a61, v250\n" \
"v_mfma_f32_16x16x32_bf16 v[104:107], v[20:23], v[204:207], v[104:107]\n" \
"buffer_load_dwordx4 v226, s[48:51], s77 offen offset:2048 lds\n" \
"v_mfma_f32_16x16x32_bf16 v[100:103], v[8:11], v[200:203], v[100:103]\n" \
"ds_read_b64_tr_b16 v[46:47], v233 offset:11264\n" \
"v_accvgpr_write_b32 a48, 0\n" \
"v_mfma_f32_16x16x32_bf16 v[100:103], v[12:15], v[204:207], v[100:103]\n" \
"ds_read_b64_tr_b16 v[48:49], v232 offset:1536\n" \
"v_accvgpr_write_b32 a49, 0\n" \
"v_mfma_f32_16x16x32_bf16 v[96:99], v[0:3], v[200:203], v[96:99]\n" \
"ds_read_b64_tr_b16 v[50:51], v232 offset:3584\n" \
"v_accvgpr_write_b32 a50, 0\n" \
"v_mfma_f32_16x16x32_bf16 v[96:99], v[4:7], v[204:207], v[96:99]\n" \
"buffer_load_dwordx4 v227, s[48:51], s77 offen offset:3072 lds\n" \
"v_mfma_f32_16x16x32_bf16 v[128:131], v[0:3], v[208:211], v[128:131]\n" \
"ds_read_b64_tr_b16 v[52:53], v232 offset:9728\n" \
"v_accvgpr_write_b32 a51, 0\n" \
"v_mfma_f32_16x16x32_bf16 v[128:131], v[4:7], v[212:215], v[128:131]\n" \
"ds_read_b64_tr_b16 v[54:55], v232 offset:11776\n" \
"v_accvgpr_write_b32 a52, 0\n" \
"v_mfma_f32_16x16x32_bf16 v[132:135], v[8:11], v[208:211], v[132:135]\n" \
"s_add_u32 s93, s94, 8192\n" \
"ds_read_b64_tr_b16 v[56:57], v233 offset:1536\n" \
"v_accvgpr_write_b32 a53, 0\n" \
"v_mfma_f32_16x16x32_bf16 v[132:135], v[12:15], v[212:215], v[132:135]\n" \
"buffer_store_dwordx4 a[64:67], v236, s[56:59], s93 offen offset:0 sc1\n" \
"v_mfma_f32_16x16x32_bf16 v[136:139], v[16:19], v[208:211], v[136:139]\n" \
"ds_read_b64_tr_b16 v[58:59], v233 offset:3584\n" \
"v_mfma_f32_16x16x32_bf16 v[136:139], v[20:23], v[212:215], v[136:139]\n" \
"ds_read_b64_tr_b16 v[60:61], v233 offset:9728\n" \
"v_mfma_f32_16x16x32_bf16 v[140:143], v[24:27], v[208:211], v[140:143]\n" \
"ds_read_b64_tr_b16 v[62:63], v233 offset:11776\n" \
"v_mfma_f32_16x16x32_bf16 v[140:143], v[28:31], v[212:215], v[140:143]\n" \
"v_mfma_f32_16x16x32_bf16 v[172:175], v[24:27], v[216:219], v[172:175]\n" \
"v_add_u32 v230, 0xffff0000, v230\n" \
"v_mfma_f32_16x16x32_bf16 v[172:175], v[28:31], v[220:223], v[172:175]\n" \
"v_add_u32 v231, 0xffff0000, v231\n" \
"v_mfma_f32_16x16x32_bf16 v[168:171], v[16:19], v[216:219], v[168:171]\n" \
"v_add_u32 v232, 0x4000, v232\n" \
"v_mfma_f32_16x16x32_bf16 v[168:171], v[20:23], v[220:223], v[168:171]\n" \
"v_mfma_f32_16x16x32_bf16 v[164:167], v[8:11], v[216:219], v[164:167]\n" \
"v_add_u32 v233, 0x4000, v233\n" \
"v_mfma_f32_16x16x32_bf16 v[164:167], v[12:15], v[220:223], v[164:167]\n" \
"v_mfma_f32_16x16x32_bf16 v[160:163], v[0:3], v[216:219], v[160:163]\n" \
"v_mfma_f32_16x16x32_bf16 v[160:163], v[4:7], v[220:223], v[160:163]\n" \
"s_waitcnt vmcnt(35) lgkmcnt(0)\n" \
"s_barrier\n" \
"v_mfma_f32_16x16x32_bf16 v[80:83], v[32:35], v[192:195], v[80:83]\n" \
"s_lshr_b32 s71, s61, 1\n" \
"s_lshl_b32 s71, s71, 14\n" \
"ds_read_b64_tr_b16 v[0:1], v232 offset:0\n" \
"v_mfma_f32_16x16x32_bf16 v[80:83], v[36:39], v[196:199], v[80:83]\n" \
"s_xor_b32 s71, s71, 0x4000\n" \
"s_and_b32 s70, s61, 1\n" \
"ds_read_b64_tr_b16 v[2:3], v232 offset:2048\n" \
"v_mfma_f32_16x16x32_bf16 v[84:87], v[40:43], v[192:195], v[84:87]\n" \
"s_lshl_b32 s70, s70, 12\n" \
"s_add_u32 s71, s71, s70\n" \
"ds_read_b64_tr_b16 v[4:5], v232 offset:8192\n" \
"v_mfma_f32_16x16x32_bf16 v[84:87], v[44:47], v[196:199], v[84:87]\n" \
"buffer_store_dwordx4 a[96:99], v236, s[56:59], s93 offen offset:256 sc1\n" \
"v_mfma_f32_16x16x32_bf16 v[88:91], v[48:51], v[192:195], v[88:91]\n" \
"ds_read_b64_tr_b16 v[6:7], v232 offset:10240\n" \
"v_add_u32 v224, s71, v237\n" \
"v_mfma_f32_16x16x32_bf16 v[88:91], v[52:55], v[196:199], v[88:91]\n" \
"s_and_b32 s70, s63, 1\n" \
"s_lshl_b32 s70, s70, 15\n" \
"ds_read_b64_tr_b16 v[8:9], v233 offset:0\n" \
"v_mfma_f32_16x16x32_bf16 v[92:95], v[56:59], v[192:195], v[92:95]\n" \
"s_xor_b32 s70, s70, 0x18000\n" \
"s_add_u32 s71, s71, s70\n" \
"ds_read_b64_tr_b16 v[10:11], v233 offset:2048\n" \
"v_mfma_f32_16x16x32_bf16 v[92:95], v[60:63], v[196:199], v[92:95]\n" \
"v_add_u32 v225, s71, v235\n" \
"s_lshr_b32 s64, s22, 7\n" \
"v_mfma_f32_16x16x32_bf16 v[124:127], v[56:59], v[200:203], v[124:127]\n" \
"s_waitcnt vmcnt(34)\n" \
"s_sub_u32 s64, 3, s64\n" \
"ds_read_b128 v[192:195], v230 offset:0\n" \
"v_mfma_f32_16x16x32_bf16 v[124:127], v[60:63], v[204:207], v[124:127]\n" \
"s_and_b32 s64, s64, 7\n" \
"s_and_b32 s65, %1, 1\n" \
"ds_read_b128 v[196:199], v231 offset:0\n" \
"v_mfma_f32_16x16x32_bf16 v[120:123], v[48:51], v[200:203], v[120:123]\n" \
"s_lshl_b32 s65, s65, 3\n" \
"s_add_u32 s64, s64, s65\n" \
"ds_read_b64_tr_b16 v[12:13], v233 offset:8192\n" \
"v_mfma_f32_16x16x32_bf16 v[120:123], v[52:55], v[204:207], v[120:123]\n" \
"s_lshl_b32 s64, s64, 7\n" \
"s_lshr_b32 s65, %1, 3\n" \
"s_lshl_b32 s65, s65, 2\n" \
"v_mfma_f32_16x16x32_bf16 v[116:119], v[40:43], v[200:203], v[116:119]\n" \
"s_add_u32 s65, s65, s61\n" \
"s_mul_i32 s65, s65, 24576\n" \
"ds_read_b64_tr_b16 v[14:15], v233 offset:10240\n" \
"v_mfma_f32_16x16x32_bf16 v[116:119], v[44:47], v[204:207], v[116:119]\n" \
"s_add_u32 s64, s64, s65\n" \
"ds_read_b64_tr_b16 v[16:17], v232 offset:512\n" \
"v_bfe_u32 v254, v234, 7, 2\n" \
"v_mfma_f32_16x16x32_bf16 v[112:115], v[32:35], v[200:203], v[112:115]\n" \
"ds_read_b64_tr_b16 v[18:19], v232 offset:2560\n" \
"v_mul_u32_u24 v254, 3, v254\n" \
"v_mfma_f32_16x16x32_bf16 v[112:115], v[36:39], v[204:207], v[112:115]\n" \
"buffer_store_dwordx4 a[72:75], v236, s[56:59], s93 offen offset:2048 sc1\n" \
"v_mfma_f32_16x16x32_bf16 v[144:147], v[32:35], v[208:211], v[144:147]\n" \
"s_waitcnt vmcnt(33)\n" \
"ds_read_b128 v[200:203], v230 offset:2048\n" \
"v_bfe_u32 v243, v234, 5, 2\n" \
"v_mfma_f32_16x16x32_bf16 v[144:147], v[36:39], v[212:215], v[144:147]\n" \
"ds_read_b128 v[204:207], v231 offset:2048\n" \
"v_min_u32 v243, 2, v243\n" \
"v_mfma_f32_16x16x32_bf16 v[148:151], v[40:43], v[208:211], v[148:151]\n" \
"ds_read_b64_tr_b16 v[20:21], v232 offset:8704\n" \
"v_add_u32 v254, v254, v243\n" \
"v_mfma_f32_16x16x32_bf16 v[148:151], v[44:47], v[212:215], v[148:151]\n" \
"v_and_b32 v243, 16, v234\n" \
"v_lshl_add_u32 v243, v243, 2, s64\n" \
"v_mfma_f32_16x16x32_bf16 v[152:155], v[48:51], v[208:211], v[152:155]\n" \
"ds_read_b64_tr_b16 v[22:23], v232 offset:10752\n" \
"v_lshl_add_u32 v254, v254, 11, v243\n" \
"v_mfma_f32_16x16x32_bf16 v[152:155], v[52:55], v[212:215], v[152:155]\n" \
"ds_read_b64_tr_b16 v[24:25], v233 offset:512\n" \
"v_mfma_f32_16x16x32_bf16 v[156:159], v[56:59], v[208:211], v[156:159]\n" \
"ds_read_b64_tr_b16 v[26:27], v233 offset:2560\n" \
"v_mfma_f32_16x16x32_bf16 v[156:159], v[60:63], v[212:215], v[156:159]\n" \
"v_mfma_f32_16x16x32_bf16 v[188:191], v[56:59], v[216:219], v[188:191]\n" \
"s_waitcnt vmcnt(31)\n" \
"ds_read_b128 v[208:211], v230 offset:4096\n" \
"v_mfma_f32_16x16x32_bf16 v[188:191], v[60:63], v[220:223], v[188:191]\n" \
"ds_read_b128 v[212:215], v231 offset:4096\n" \
"v_mfma_f32_16x16x32_bf16 v[184:187], v[48:51], v[216:219], v[184:187]\n" \
"ds_read_b64_tr_b16 v[28:29], v233 offset:8704\n" \
"v_mfma_f32_16x16x32_bf16 v[184:187], v[52:55], v[220:223], v[184:187]\n" \
"v_mfma_f32_16x16x32_bf16 v[180:183], v[40:43], v[216:219], v[180:183]\n" \
"ds_read_b64_tr_b16 v[30:31], v233 offset:10752\n" \
"v_mfma_f32_16x16x32_bf16 v[180:183], v[44:47], v[220:223], v[180:183]\n" \
"v_mfma_f32_16x16x32_bf16 v[176:179], v[32:35], v[216:219], v[176:179]\n" \
"buffer_store_dwordx4 a[104:107], v236, s[56:59], s93 offen offset:2304 sc1\n" \
"v_mfma_f32_16x16x32_bf16 v[176:179], v[36:39], v[220:223], v[176:179]\n" \
"s_waitcnt lgkmcnt(1)\n" \
"v_mfma_f32_16x16x32_bf16 v[64:67], v[0:3], v[192:195], v[64:67]\n" \
"s_waitcnt vmcnt(30)\n" \
"s_lshl_b32 s68, s63, 2\n" \
"ds_read_b128 v[216:219], v230 offset:6144\n" \
"v_mfma_f32_16x16x32_bf16 v[64:67], v[4:7], v[196:199], v[64:67]\n" \
"s_add_u32 s68, s68, s90\n" \
"ds_read_b128 v[220:223], v231 offset:6144\n" \
"v_mfma_f32_16x16x32_bf16 v[68:71], v[8:11], v[192:195], v[68:71]\n" \
"ds_read_b64_tr_b16 v[32:33], v232 offset:1024\n" \
"v_mfma_f32_16x16x32_bf16 v[68:71], v[12:15], v[196:199], v[68:71]\n" \
"v_mov_b32 v228, s60\n" \
"v_lshlrev_b32 v229, 28, s61\n" \
"v_mfma_f32_16x16x32_bf16 v[72:75], v[16:19], v[192:195], v[72:75]\n" \
"ds_read_b64_tr_b16 v[34:35], v232 offset:3072\n" \
"v_mfma_f32_16x16x32_bf16 v[72:75], v[20:23], v[196:199], v[72:75]\n" \
"s_waitcnt lgkmcnt(3)\n" \
"ds_read_b64_tr_b16 v[36:37], v232 offset:9216\n" \
"v_mfma_f32_16x16x32_bf16 v[76:79], v[24:27], v[192:195], v[76:79]\n" \
"ds_read_b64_tr_b16 v[38:39], v232 offset:11264\n" \
"v_mfma_f32_16x16x32_bf16 v[76:79], v[28:31], v[196:199], v[76:79]\n" \
"v_mfma_f32_16x16x32_bf16 v[108:111], v[24:27], v[200:203], v[108:111]\n" \
"ds_read_b64_tr_b16 v[40:41], v233 offset:1024\n" \
"v_mfma_f32_16x16x32_bf16 v[108:111], v[28:31], v[204:207], v[108:111]\n" \
"ds_read_b64_tr_b16 v[42:43], v233 offset:3072\n" \
"v_mfma_f32_16x16x32_bf16 v[104:107], v[16:19], v[200:203], v[104:107]\n" \
"ds_read_b64_tr_b16 v[44:45], v233 offset:9216\n" \
"v_mfma_f32_16x16x32_bf16 v[104:107], v[20:23], v[204:207], v[104:107]\n" \
"v_mfma_f32_16x16x32_bf16 v[100:103], v[8:11], v[200:203], v[100:103]\n" \
"ds_read_b64_tr_b16 v[46:47], v233 offset:11264\n" \
"v_mfma_f32_16x16x32_bf16 v[100:103], v[12:15], v[204:207], v[100:103]\n" \
"ds_read_b64_tr_b16 v[48:49], v232 offset:1536\n" \
"v_mfma_f32_16x16x32_bf16 v[96:99], v[0:3], v[200:203], v[96:99]\n" \
"s_add_u32 s93, s94, 0\n" \
"ds_read_b64_tr_b16 v[50:51], v232 offset:3584\n" \
"v_mfma_f32_16x16x32_bf16 v[96:99], v[4:7], v[204:207], v[96:99]\n" \
"buffer_store_dwordx4 a[0:3], v236, s[56:59], s93 offen offset:0 sc1\n" \
"v_mfma_f32_16x16x32_bf16 v[128:131], v[0:3], v[208:211], v[128:131]\n" \
"ds_read_b64_tr_b16 v[52:53], v232 offset:9728\n" \
"v_mfma_f32_16x16x32_bf16 v[128:131], v[4:7], v[212:215], v[128:131]\n" \
"ds_read_b64_tr_b16 v[54:55], v232 offset:11776\n" \
"v_mfma_f32_16x16x32_bf16 v[132:135], v[8:11], v[208:211], v[132:135]\n" \
"ds_read_b64_tr_b16 v[56:57], v233 offset:1536\n" \
"v_mfma_f32_16x16x32_bf16 v[132:135], v[12:15], v[212:215], v[132:135]\n" \
"v_mfma_f32_16x16x32_bf16 v[136:139], v[16:19], v[208:211], v[136:139]\n" \
"ds_read_b64_tr_b16 v[58:59], v233 offset:3584\n" \
"v_mfma_f32_16x16x32_bf16 v[136:139], v[20:23], v[212:215], v[136:139]\n" \
"ds_read_b64_tr_b16 v[60:61], v233 offset:9728\n" \
"v_mfma_f32_16x16x32_bf16 v[140:143], v[24:27], v[208:211], v[140:143]\n" \
"ds_read_b64_tr_b16 v[62:63], v233 offset:11776\n" \
"v_mfma_f32_16x16x32_bf16 v[140:143], v[28:31], v[212:215], v[140:143]\n" \
"s_waitcnt lgkmcnt(3)\n" \
"v_mfma_f32_16x16x32_bf16 v[172:175], v[24:27], v[216:219], v[172:175]\n" \
"v_add_u32 v230, 0x8000, v230\n" \
"v_mfma_f32_16x16x32_bf16 v[172:175], v[28:31], v[220:223], v[172:175]\n" \
"v_add_u32 v231, 0x8000, v231\n" \
"v_mfma_f32_16x16x32_bf16 v[168:171], v[16:19], v[216:219], v[168:171]\n" \
"v_add_u32 v232, 0x4000, v232\n" \
"v_mfma_f32_16x16x32_bf16 v[168:171], v[20:23], v[220:223], v[168:171]\n" \
"v_mfma_f32_16x16x32_bf16 v[164:167], v[8:11], v[216:219], v[164:167]\n" \
"v_add_u32 v233, 0x4000, v233\n" \
"v_mfma_f32_16x16x32_bf16 v[164:167], v[12:15], v[220:223], v[164:167]\n" \
"buffer_store_dwordx4 a[32:35], v236, s[56:59], s93 offen offset:256 sc1\n" \
"v_mfma_f32_16x16x32_bf16 v[160:163], v[0:3], v[216:219], v[160:163]\n" \
"v_mfma_f32_16x16x32_bf16 v[160:163], v[4:7], v[220:223], v[160:163]\n" \
"s_waitcnt vmcnt(18) lgkmcnt(0)\n" \
"s_barrier\n" \
"v_mfma_f32_16x16x32_bf16 v[80:83], v[32:35], v[192:195], v[80:83]\n" \
"ds_read_b64_tr_b16 v[0:1], v232 offset:0\n" \
"v_mfma_f32_16x16x32_bf16 v[80:83], v[36:39], v[196:199], v[80:83]\n" \
"ds_read_b64_tr_b16 v[2:3], v232 offset:2048\n" \
"buffer_store_dword v228, v229, s[56:59], s68 offen sc1\n" \
"v_mfma_f32_16x16x32_bf16 v[84:87], v[40:43], v[192:195], v[84:87]\n" \
"ds_read_b64_tr_b16 v[4:5], v232 offset:8192\n" \
"v_lshrrev_b32 v238, 4, v234\n" \
"v_mfma_f32_16x16x32_bf16 v[84:87], v[44:47], v[196:199], v[84:87]\n" \
"ds_write_b128 v224, a[192:195] offset:8192\n" \
"v_mfma_f32_16x16x32_bf16 v[88:91], v[48:51], v[192:195], v[88:91]\n" \
"ds_read_b64_tr_b16 v[6:7], v232 offset:10240\n" \
"v_lshrrev_b32 v240, 4, v238\n" \
"v_mfma_f32_16x16x32_bf16 v[88:91], v[52:55], v[196:199], v[88:91]\n" \
"ds_read_b64_tr_b16 v[8:9], v233 offset:0\n" \
"v_lshlrev_b32 v240, 2, v240\n" \
"v_mfma_f32_16x16x32_bf16 v[92:95], v[56:59], v[192:195], v[92:95]\n" \
"ds_read_b64_tr_b16 v[10:11], v233 offset:2048\n" \
"v_and_b32 v238, 15, v238\n" \
"v_mfma_f32_16x16x32_bf16 v[92:95], v[60:63], v[196:199], v[92:95]\n" \
"v_sub_u32 v238, v238, v240\n" \
"v_subrev_u32 v248, 0, v238\n" \
"v_mfma_f32_16x16x32_bf16 v[124:127], v[56:59], v[200:203], v[124:127]\n" \
"ds_read_b128 v[192:195], v230 offset:0\n" \
"v_subrev_u32 v249, 1, v238\n" \
"v_mfma_f32_16x16x32_bf16 v[124:127], v[60:63], v[204:207], v[124:127]\n" \
"ds_read_b128 v[196:199], v231 offset:0\n" \
"v_subrev_u32 v250, 2, v238\n" \
"v_mfma_f32_16x16x32_bf16 v[120:123], v[48:51], v[200:203], v[120:123]\n" \
"ds_read_b64_tr_b16 v[12:13], v233 offset:8192\n" \
"v_subrev_u32 v251, 3, v238\n" \
"v_mfma_f32_16x16x32_bf16 v[120:123], v[52:55], v[204:207], v[120:123]\n" \
"ds_write_b128 v224, a[224:227] offset:8448\n" \
"v_mfma_f32_16x16x32_bf16 v[116:119], v[40:43], v[200:203], v[116:119]\n" \
"ds_read_b64_tr_b16 v[14:15], v233 offset:10240\n" \
"v_ashrrev_i32 v248, 31, v248\n" \
"v_mfma_f32_16x16x32_bf16 v[116:119], v[44:47], v[204:207], v[116:119]\n" \
"ds_read_b64_tr_b16 v[16:17], v232 offset:512\n" \
"v_ashrrev_i32 v249, 31, v249\n" \
"v_mfma_f32_16x16x32_bf16 v[112:115], v[32:35], v[200:203], v[112:115]\n" \
"ds_read_b64_tr_b16 v[18:19], v232 offset:2560\n" \
"v_ashrrev_i32 v250, 31, v250\n" \
"v_mfma_f32_16x16x32_bf16 v[112:115], v[36:39], v[204:207], v[112:115]\n" \
"buffer_store_dwordx4 a[8:11], v236, s[56:59], s93 offen offset:2048 sc1\n" \
"v_mfma_f32_16x16x32_bf16 v[144:147], v[32:35], v[208:211], v[144:147]\n" \
"ds_read_b128 v[200:203], v230 offset:2048\n" \
"v_ashrrev_i32 v251, 31, v251\n" \
"v_mfma_f32_16x16x32_bf16 v[144:147], v[36:39], v[212:215], v[144:147]\n" \
"ds_read_b128 v[204:207], v231 offset:2048\n" \
"v_lshlrev_b32 v248, 23, v248\n" \
"v_mfma_f32_16x16x32_bf16 v[148:151], v[40:43], v[208:211], v[148:151]\n" \
"ds_read_b64_tr_b16 v[20:21], v232 offset:8704\n" \
"v_lshlrev_b32 v249, 23, v249\n" \
"v_mfma_f32_16x16x32_bf16 v[148:151], v[44:47], v[212:215], v[148:151]\n" \
"ds_write_b128 v224, a[200:203] offset:10240\n" \
"v_mfma_f32_16x16x32_bf16 v[152:155], v[48:51], v[208:211], v[152:155]\n" \
"ds_read_b64_tr_b16 v[22:23], v232 offset:10752\n" \
"v_lshlrev_b32 v250, 23, v250\n" \
"v_mfma_f32_16x16x32_bf16 v[152:155], v[52:55], v[212:215], v[152:155]\n" \
"ds_read_b64_tr_b16 v[24:25], v233 offset:512\n" \
"v_lshlrev_b32 v251, 23, v251\n" \
"v_mfma_f32_16x16x32_bf16 v[156:159], v[56:59], v[208:211], v[156:159]\n" \
"ds_read_b64_tr_b16 v[26:27], v233 offset:2560\n" \
"v_mov_b32 v240, 0xff800000\n" \
"v_mfma_f32_16x16x32_bf16 v[156:159], v[60:63], v[212:215], v[156:159]\n" \
"ds_write_b128 v224, a[232:235] offset:10496\n" \
"v_mfma_f32_16x16x32_bf16 v[188:191], v[56:59], v[216:219], v[188:191]\n" \
"ds_read_b128 v[208:211], v230 offset:4096\n" \
"v_mfma_f32_16x16x32_bf16 v[188:191], v[60:63], v[220:223], v[188:191]\n" \
"s_cmp_gt_u32 s61, 0\n" \
"s_cselect_b32 s70, 0, 0xff800000\n" \
"ds_read_b128 v[212:215], v231 offset:4096\n" \
"v_mfma_f32_16x16x32_bf16 v[184:187], v[48:51], v[216:219], v[184:187]\n" \
"ds_read_b64_tr_b16 v[28:29], v233 offset:8704\n" \
"v_mov_b32 v252, s70\n" \
"v_mfma_f32_16x16x32_bf16 v[184:187], v[52:55], v[220:223], v[184:187]\n" \
"s_cmp_gt_u32 s61, 1\n" \
"s_cselect_b32 s70, 0, 0xff800000\n" \
"v_mov_b32 v253, s70\n" \
"v_mfma_f32_16x16x32_bf16 v[180:183], v[40:43], v[216:219], v[180:183]\n" \
"s_cmp_gt_u32 s61, 2\n" \
"s_cselect_b32 s70, 0, 0xff800000\n" \
"ds_read_b64_tr_b16 v[30:31], v233 offset:10752\n" \
"v_mfma_f32_16x16x32_bf16 v[180:183], v[44:47], v[220:223], v[180:183]\n" \
"buffer_store_dwordx4 a[40:43], v236, s[56:59], s93 offen offset:2304 sc1\n" \
"v_mfma_f32_16x16x32_bf16 v[176:179], v[32:35], v[216:219], v[176:179]\n" \
"ds_write_b128 v224, a[128:131] offset:0\n" \
"v_mfma_f32_16x16x32_bf16 v[176:179], v[36:39], v[220:223], v[176:179]\n" \
"s_waitcnt lgkmcnt(2)\n" \
"v_mov_b32 v226, s70\n" \
"v_bfe_i32 v243, v234, 4, 1\n" \
"v_mfma_f32_16x16x32_bf16 v[64:67], v[0:3], v[192:195], v[64:67]\n" \
"ds_read_b128 v[216:219], v230 offset:6144\n" \
"v_and_b32 v243, 0xaaaa, v243\n" \
"v_mfma_f32_16x16x32_bf16 v[64:67], v[4:7], v[196:199], v[64:67]\n" \
"ds_read_b128 v[220:223], v231 offset:6144\n" \
"v_or_b32 v243, 0x4444, v243\n" \
"v_mfma_f32_16x16x32_bf16 v[68:71], v[8:11], v[192:195], v[68:71]\n" \
"ds_read_b64_tr_b16 v[32:33], v232 offset:1024\n" \
"v_bfe_i32 v244, v234, 5, 1\n" \
"v_mfma_f32_16x16x32_bf16 v[68:71], v[12:15], v[196:199], v[68:71]\n" \
"v_not_b32 v244, v244\n" \
"v_and_b32 v244, 0x3f803f80, v244\n" \
"v_mfma_f32_16x16x32_bf16 v[72:75], v[16:19], v[192:195], v[72:75]\n" \
"ds_read_b64_tr_b16 v[34:35], v232 offset:3072\n" \
"v_mov_b32 v245, v244\n" \
"v_mfma_f32_16x16x32_bf16 v[72:75], v[20:23], v[196:199], v[72:75]\n" \
"s_waitcnt lgkmcnt(3)\n" \
"ds_read_b64_tr_b16 v[36:37], v232 offset:9216\n" \
"v_mov_b32 v246, v244\n" \
"v_mfma_f32_16x16x32_bf16 v[76:79], v[24:27], v[192:195], v[76:79]\n" \
"ds_read_b64_tr_b16 v[38:39], v232 offset:11264\n" \
"v_mov_b32 v247, v244\n" \
"v_mfma_f32_16x16x32_bf16 v[76:79], v[28:31], v[196:199], v[76:79]\n" \
"ds_write_b128 v224, a[160:163] offset:256\n" \
"v_mfma_f32_16x16x32_bf16 v[108:111], v[24:27], v[200:203], v[108:111]\n" \
"s_mov_b32 s76, s73\n" \
"s_and_b32 s77, s63, 1\n" \
"ds_read_b64_tr_b16 v[40:41], v233 offset:1024\n" \
"v_mfma_f32_16x16x32_bf16 v[108:111], v[28:31], v[204:207], v[108:111]\n" \
"s_lshl_b32 s77, s77, 15\n" \
"s_xor_b32 s89, s77, 0x8000\n" \
"ds_read_b64_tr_b16 v[42:43], v233 offset:3072\n" \
"v_mfma_f32_16x16x32_bf16 v[104:107], v[16:19], v[200:203], v[104:107]\n" \
"s_add_u32 s82, s89, 0x10000\n" \
"s_mov_b32 s74, 0\n" \
"ds_read_b64_tr_b16 v[44:45], v233 offset:9216\n" \
"v_mfma_f32_16x16x32_bf16 v[104:107], v[20:23], v[204:207], v[104:107]\n" \
"s_add_u32 s73, s73, 34816\n" \
"s_cmp_eq_u32 s73, s83\n" \
"s_cselect_b32 s70, 34816, 0\n" \
"v_mfma_f32_16x16x32_bf16 v[100:103], v[8:11], v[200:203], v[100:103]\n" \
"s_add_u32 s73, s73, s70\n" \
"s_lshr_b32 s70, s61, 1\n" \
"ds_read_b64_tr_b16 v[46:47], v233 offset:11264\n" \
"v_mfma_f32_16x16x32_bf16 v[100:103], v[12:15], v[204:207], v[100:103]\n" \
"s_lshl_b32 s85, s63, 2\n" \
"s_sub_u32 s86, 60, s85\n" \
"ds_read_b64_tr_b16 v[48:49], v232 offset:1536\n" \
"v_mfma_f32_16x16x32_bf16 v[96:99], v[0:3], v[200:203], v[96:99]\n" \
"s_add_u32 s93, s94, 17858560\n" \
"s_add_u32 s85, s85, s70\n" \
"ds_read_b64_tr_b16 v[50:51], v232 offset:3584\n" \
"v_mfma_f32_16x16x32_bf16 v[96:99], v[4:7], v[204:207], v[96:99]\n" \
"buffer_store_dwordx4 a[80:83], v234, s[56:59], s93 offen offset:0 sc1\n" \
"v_mfma_f32_16x16x32_bf16 v[128:131], v[0:3], v[208:211], v[128:131]\n" \
"s_add_u32 s86, s86, s70\n" \
"s_mul_i32 s87, s85, 266240\n" \
"ds_read_b64_tr_b16 v[52:53], v232 offset:9728\n" \
"v_mfma_f32_16x16x32_bf16 v[128:131], v[4:7], v[212:215], v[128:131]\n" \
"s_mul_i32 s88, s86, 266240\n" \
"s_and_b32 s75, s61, 1\n" \
"ds_read_b64_tr_b16 v[54:55], v232 offset:11776\n" \
"v_mfma_f32_16x16x32_bf16 v[132:135], v[8:11], v[208:211], v[132:135]\n" \
"s_lshl_b32 s70, s75, 8\n" \
"s_lshl_b32 s91, s62, 13\n" \
"ds_read_b64_tr_b16 v[56:57], v233 offset:1536\n" \
"v_mfma_f32_16x16x32_bf16 v[132:135], v[12:15], v[212:215], v[132:135]\n" \
"ds_write_b128 v224, a[136:139] offset:2048\n" \
"v_mfma_f32_16x16x32_bf16 v[136:139], v[16:19], v[208:211], v[136:139]\n" \
"s_add_u32 s91, s91, s70\n" \
"s_add_u32 s91, s91, 35700736\n" \
"ds_read_b64_tr_b16 v[58:59], v233 offset:3584\n" \
"v_mfma_f32_16x16x32_bf16 v[136:139], v[20:23], v[212:215], v[136:139]\n" \
"s_add_u32 s87, s87, s91\n" \
"s_add_u32 s88, s88, s91\n" \
"ds_read_b64_tr_b16 v[60:61], v233 offset:9728\n" \
"v_mfma_f32_16x16x32_bf16 v[140:143], v[24:27], v[208:211], v[140:143]\n" \
"s_waitcnt lgkmcnt(4)\n" \
"s_lshl_b32 s75, s75, 14\n" \
"ds_read_b64_tr_b16 v[62:63], v233 offset:11776\n" \
"v_mfma_f32_16x16x32_bf16 v[140:143], v[28:31], v[212:215], v[140:143]\n" \
"ds_write_b128 v224, a[168:171] offset:2304\n" \
"v_mfma_f32_16x16x32_bf16 v[172:175], v[24:27], v[216:219], v[172:175]\n" \
"s_cmp_eq_u32 s63, 7\n" \
"s_cselect_b32 s75, 0, s75\n" \
"v_add_u32 v230, 0x8000, v230\n" \
"v_mfma_f32_16x16x32_bf16 v[172:175], v[28:31], v[220:223], v[172:175]\n" \
"s_xor_b32 s78, s75, 0xc000\n" \
"s_mov_b32 s81, s78\n" \
"v_add_u32 v231, 0x8000, v231\n" \
"v_mfma_f32_16x16x32_bf16 v[168:171], v[16:19], v[216:219], v[168:171]\n" \
"s_cmp_eq_u32 s63, 0\n" \
"s_cselect_b32 s91, s75, 0\n" \
"v_add_u32 v232, 0x4000, v232\n" \
"v_mfma_f32_16x16x32_bf16 v[168:171], v[20:23], v[220:223], v[168:171]\n" \
"buffer_store_dwordx4 a[84:87], v234, s[56:59], s93 offen offset:1024 sc1\n" \
"v_mfma_f32_16x16x32_bf16 v[164:167], v[8:11], v[216:219], v[164:167]\n" \
"s_add_u32 s91, s91, 0x8000\n" \
"s_xor_b32 s75, s75, 0x8000\n" \
"v_add_u32 v233, 0x4000, v233\n" \
"v_mfma_f32_16x16x32_bf16 v[164:167], v[12:15], v[220:223], v[164:167]\n" \
"s_sub_u32 s84, 7, s63\n" \
"v_mfma_f32_16x16x32_bf16 v[160:163], v[0:3], v[216:219], v[160:163]\n" \
"v_mfma_f32_16x16x32_bf16 v[160:163], v[4:7], v[220:223], v[160:163]\n" \
"s_waitcnt vmcnt(19) lgkmcnt(0)\n" \
"s_barrier\n" \
"v_mfma_f32_16x16x32_bf16 v[144:147], v[32:35], v[208:211], v[144:147]\n" \
"s_mov_b64 s[44:45], s[14:15]\n" \
"s_mov_b32 s46, 0\n" \
"ds_read_b64_tr_b16 v[0:1], v232 offset:0\n" \
"v_mfma_f32_16x16x32_bf16 v[144:147], v[36:39], v[212:215], v[144:147]\n" \
"s_mov_b32 s47, 0\n" \
"s_cmp_eq_u32 s63, 7\n" \
"s_cselect_b64 s[44:45], s[18:19], s[44:45]\n" \
"s_cselect_b32 s46, 5, s46\n" \
"s_cmp_eq_u32 s63, 3\n" \
"s_cselect_b64 s[44:45], s[18:19], s[44:45]\n" \
"s_cselect_b32 s46, 3, s46\n" \
"ds_read_b64_tr_b16 v[2:3], v232 offset:2048\n" \
"v_mfma_f32_16x16x32_bf16 v[148:151], v[40:43], v[208:211], v[148:151]\n" \
"s_cmp_eq_u32 s63, 3\n" \
"s_cselect_b32 s47, 640, s47\n" \
"ds_read_b64_tr_b16 v[4:5], v232 offset:8192\n" \
"v_mfma_f32_16x16x32_bf16 v[148:151], v[44:47], v[212:215], v[148:151]\n" \
"ds_write_b128 v225, a[144:147] offset:0\n" \
"v_mfma_f32_16x16x32_bf16 v[152:155], v[48:51], v[208:211], v[152:155]\n" \
"ds_read_b64_tr_b16 v[6:7], v232 offset:10240\n" \
"v_mfma_f32_16x16x32_bf16 v[152:155], v[52:55], v[212:215], v[152:155]\n" \
"s_cmp_eq_u32 s63, 6\n" \
"s_cselect_b64 s[44:45], s[22:23], s[44:45]\n" \
"s_cselect_b32 s46, 6, s46\n" \
"ds_read_b64_tr_b16 v[8:9], v233 offset:0\n" \
"v_mfma_f32_16x16x32_bf16 v[156:159], v[56:59], v[208:211], v[156:159]\n" \
"ds_read_b64_tr_b16 v[10:11], v233 offset:2048\n" \
"v_mfma_f32_16x16x32_bf16 v[156:159], v[60:63], v[212:215], v[156:159]\n" \
"s_cmp_eq_u32 s63, 4\n" \
"s_cselect_b64 s[44:45], s[20:21], s[44:45]\n" \
"s_cselect_b32 s46, 4, s46\n" \
"v_mfma_f32_16x16x32_bf16 v[188:191], v[56:59], v[216:219], v[188:191]\n" \
"s_waitcnt vmcnt(17)\n" \
"ds_read_b128 v[208:211], v230 offset:0\n" \
"v_mfma_f32_16x16x32_bf16 v[188:191], v[60:63], v[220:223], v[188:191]\n" \
"s_cmp_eq_u32 s63, 2\n" \
"s_cselect_b64 s[44:45], s[24:25], s[44:45]\n" \
"s_cselect_b32 s46, 2, s46\n" \
"ds_read_b128 v[212:215], v231 offset:0\n" \
"v_mfma_f32_16x16x32_bf16 v[184:187], v[48:51], v[216:219], v[184:187]\n" \
"s_lshl_b32 s48, s62, 2\n" \
"s_add_u32 s48, s48, s61\n" \
"ds_read_b64_tr_b16 v[12:13], v233 offset:8192\n" \
"v_mfma_f32_16x16x32_bf16 v[184:187], v[52:55], v[220:223], v[184:187]\n" \
"buffer_store_dwordx4 a[88:91], v234, s[56:59], s93 offen offset:2048 sc1\n" \
"v_mfma_f32_16x16x32_bf16 v[180:183], v[40:43], v[216:219], v[180:183]\n" \
"s_add_u32 s48, s48, s47\n" \
"s_lshl_b32 s48, s48, 13\n" \
"ds_read_b64_tr_b16 v[14:15], v233 offset:10240\n" \
"v_mfma_f32_16x16x32_bf16 v[180:183], v[44:47], v[220:223], v[180:183]\n" \
"s_add_u32 s44, s44, s48\n" \
"s_addc_u32 s45, s45, 0\n" \
"ds_read_b64_tr_b16 v[16:17], v232 offset:512\n" \
"v_mfma_f32_16x16x32_bf16 v[176:179], v[32:35], v[216:219], v[176:179]\n" \
"ds_read_b64_tr_b16 v[18:19], v232 offset:2560\n" \
"v_mfma_f32_16x16x32_bf16 v[176:179], v[36:39], v[220:223], v[176:179]\n" \
"ds_write_b128 v225, a[148:151] offset:1024\n" \
"v_mfma_f32_16x16x32_bf16 v[80:83], v[32:35], v[192:195], v[80:83]\n" \
"s_waitcnt vmcnt(16)\n" \
"ds_read_b128 v[216:219], v230 offset:2048\n" \
"v_mfma_f32_16x16x32_bf16 v[80:83], v[36:39], v[196:199], v[80:83]\n" \
"ds_read_b128 v[220:223], v231 offset:2048\n" \
"v_mfma_f32_16x16x32_bf16 v[84:87], v[40:43], v[192:195], v[84:87]\n" \
"ds_read_b64_tr_b16 v[20:21], v232 offset:8704\n" \
"Lpg4941_%=:\n" \
"s_cbranch_execz Lpg6346_%=\n" \
"v_mfma_f32_16x16x32_bf16 v[84:87], v[44:47], v[196:199], v[84:87]\n" \
"ds_write_b128 v225, a[152:155] offset:2048\n" \
"v_mfma_f32_16x16x32_bf16 v[88:91], v[48:51], v[192:195], v[88:91]\n" \
"ds_read_b64_tr_b16 v[22:23], v232 offset:10752\n" \
"v_mfma_f32_16x16x32_bf16 v[88:91], v[52:55], v[196:199], v[88:91]\n" \
"ds_read_b64_tr_b16 v[24:25], v233 offset:512\n" \
"v_mfma_f32_16x16x32_bf16 v[92:95], v[56:59], v[192:195], v[92:95]\n" \
"ds_read_b64_tr_b16 v[26:27], v233 offset:2560\n" \
"v_mfma_f32_16x16x32_bf16 v[92:95], v[60:63], v[196:199], v[92:95]\n" \
"ds_write_b128 v225, a[156:159] offset:3072\n" \
"v_mfma_f32_16x16x32_bf16 v[124:127], v[56:59], v[200:203], v[124:127]\n" \
"s_waitcnt vmcnt(14)\n" \
"ds_read_b128 v[192:195], v230 offset:4096\n" \
"v_mfma_f32_16x16x32_bf16 v[124:127], v[60:63], v[204:207], v[124:127]\n" \
"ds_read_b128 v[196:199], v231 offset:4096\n" \
"v_mfma_f32_16x16x32_bf16 v[120:123], v[48:51], v[200:203], v[120:123]\n" \
"ds_read_b64_tr_b16 v[28:29], v233 offset:8704\n" \
"v_mfma_f32_16x16x32_bf16 v[120:123], v[52:55], v[204:207], v[120:123]\n" \
"buffer_store_dwordx4 a[92:95], v234, s[56:59], s93 offen offset:3072 sc1\n" \
"v_mfma_f32_16x16x32_bf16 v[116:119], v[40:43], v[200:203], v[116:119]\n" \
"ds_read_b64_tr_b16 v[30:31], v233 offset:10752\n" \
"v_mfma_f32_16x16x32_bf16 v[116:119], v[44:47], v[204:207], v[116:119]\n" \
"v_mfma_f32_16x16x32_bf16 v[112:115], v[32:35], v[200:203], v[112:115]\n" \
"ds_write_b128 v225, a[208:211] offset:8192\n" \
"v_mfma_f32_16x16x32_bf16 v[112:115], v[36:39], v[204:207], v[112:115]\n" \
"s_waitcnt lgkmcnt(2)\n" \
"v_mfma_f32_16x16x32_bf16 v[64:67], v[0:3], v[208:211], v[64:67]\n" \
"s_waitcnt vmcnt(13)\n" \
"ds_read_b128 v[200:203], v230 offset:6144\n" \
"ds_read_b128 v[204:207], v231 offset:6144\n" \
"v_mfma_f32_16x16x32_bf16 v[64:67], v[4:7], v[212:215], v[64:67]\n" \
"ds_read_b64_tr_b16 v[32:33], v232 offset:1024\n" \
"ds_read_b64_tr_b16 v[34:35], v232 offset:3072\n" \
"v_mfma_f32_16x16x32_bf16 v[68:71], v[8:11], v[208:211], v[68:71]\n" \
"s_waitcnt lgkmcnt(5)\n" \
"ds_read_b64_tr_b16 v[36:37], v232 offset:9216\n" \
"ds_read_b64_tr_b16 v[38:39], v232 offset:11264\n" \
"v_mfma_f32_16x16x32_bf16 v[68:71], v[12:15], v[212:215], v[68:71]\n" \
"s_getpc_b64 s[64:65]\n" \
"Lpd1_%=:\n" \
"s_lshl_b32 s66, s62, 2\n" \
"ds_read_b64_tr_b16 v[40:41], v233 offset:1024\n" \
"ds_read_b64_tr_b16 v[42:43], v233 offset:3072\n" \
"v_mfma_f32_16x16x32_bf16 v[72:75], v[16:19], v[208:211], v[72:75]\n" \
"s_add_u32 s66, s66, s61\n" \
"ds_read_b64_tr_b16 v[44:45], v233 offset:9216\n" \
"ds_read_b64_tr_b16 v[46:47], v233 offset:11264\n" \
"v_mfma_f32_16x16x32_bf16 v[72:75], v[20:23], v[212:215], v[72:75]\n" \
"s_and_b32 s66, s66, 15\n" \
"ds_read_b64_tr_b16 v[48:49], v232 offset:1536\n" \
"ds_read_b64_tr_b16 v[50:51], v232 offset:3584\n" \
"v_mfma_f32_16x16x32_bf16 v[76:79], v[24:27], v[208:211], v[76:79]\n" \
"s_lshl_b32 s66, s66, 12\n" \
"ds_read_b64_tr_b16 v[52:53], v232 offset:9728\n" \
"ds_read_b64_tr_b16 v[54:55], v232 offset:11776\n" \
"v_mfma_f32_16x16x32_bf16 v[76:79], v[28:31], v[212:215], v[76:79]\n" \
"ds_read_b64_tr_b16 v[56:57], v233 offset:1536\n" \
"ds_read_b64_tr_b16 v[58:59], v233 offset:3584\n" \
"v_mfma_f32_16x16x32_bf16 v[96:99], v[0:3], v[216:219], v[96:99]\n" \
"ds_read_b64_tr_b16 v[60:61], v233 offset:9728\n" \
"ds_read_b64_tr_b16 v[62:63], v233 offset:11776\n" \
"v_mfma_f32_16x16x32_bf16 v[96:99], v[4:7], v[220:223], v[96:99]\n" \
"s_add_u32 s93, s94, 17850368\n" \
"s_add_u32 s64, s64, Lloop2_%=-Lpd1_%=\n" \
"s_addc_u32 s65, s65, 0\n" \
"s_mov_b32 s67, Lcode_end_%=-Lloop2_%=-4\n" \
"v_mfma_f32_16x16x32_bf16 v[100:103], v[8:11], v[216:219], v[100:103]\n" \
"buffer_store_dwordx4 a[16:19], v234, s[56:59], s93 offen offset:0 sc1\n" \
"v_mfma_f32_16x16x32_bf16 v[100:103], v[12:15], v[220:223], v[100:103]\n" \
"ds_write_b128 v225, a[212:215] offset:9216\n" \
"v_mfma_f32_16x16x32_bf16 v[104:107], v[16:19], v[216:219], v[104:107]\n" \
"buffer_load_dword v227, v242, s[56:59], s90 offen sc1\n" \
"v_mfma_f32_16x16x32_bf16 v[104:107], v[20:23], v[220:223], v[104:107]\n" \
"s_waitcnt lgkmcnt(1)\n" \
"v_lshl_add_u32 v230, v234, 2, s66\n" \
"v_min_u32 v230, s67, v230\n" \
"v_mfma_f32_16x16x32_bf16 v[108:111], v[24:27], v[216:219], v[108:111]\n" \
"ds_write_b128 v225, a[216:219] offset:10240\n" \
"v_mfma_f32_16x16x32_bf16 v[108:111], v[28:31], v[220:223], v[108:111]\n" \
"global_load_dword v230, v230, s[64:65]\n" \
"v_mfma_f32_16x16x32_bf16 v[80:83], v[32:35], v[208:211], v[80:83]\n" \
"v_cvt_pk_bf16_f32 v64, v64, v65\n" \
"v_cvt_pk_bf16_f32 v65, v66, v67\n" \
"v_mfma_f32_16x16x32_bf16 v[80:83], v[36:39], v[212:215], v[80:83]\n" \
"v_cvt_pk_bf16_f32 v66, v68, v69\n" \
"v_cvt_pk_bf16_f32 v67, v70, v71\n" \
"v_mfma_f32_16x16x32_bf16 v[84:87], v[40:43], v[208:211], v[84:87]\n" \
"v_accvgpr_write_b32 a128, v64\n" \
"v_accvgpr_write_b32 a129, v65\n" \
"v_mfma_f32_16x16x32_bf16 v[84:87], v[44:47], v[212:215], v[84:87]\n" \
"ds_write_b128 v225, a[220:223] offset:11264\n" \
"v_mfma_f32_16x16x32_bf16 v[88:91], v[48:51], v[208:211], v[88:91]\n" \
"v_accvgpr_write_b32 a130, v66\n" \
"v_accvgpr_write_b32 a131, v67\n" \
"v_mfma_f32_16x16x32_bf16 v[88:91], v[52:55], v[212:215], v[88:91]\n" \
"v_cvt_pk_bf16_f32 v72, v72, v73\n" \
"v_cvt_pk_bf16_f32 v73, v74, v75\n" \
"v_cvt_pk_bf16_f32 v74, v76, v77\n" \
"v_mfma_f32_16x16x32_bf16 v[92:95], v[56:59], v[208:211], v[92:95]\n" \
"v_cvt_pk_bf16_f32 v75, v78, v79\n" \
"v_accvgpr_write_b32 a132, v72\n" \
"v_mfma_f32_16x16x32_bf16 v[92:95], v[60:63], v[212:215], v[92:95]\n" \
"v_accvgpr_write_b32 a133, v73\n" \
"v_accvgpr_write_b32 a134, v74\n" \
"v_mfma_f32_16x16x32_bf16 v[112:115], v[32:35], v[216:219], v[112:115]\n" \
"v_accvgpr_write_b32 a135, v75\n" \
"v_cvt_pk_bf16_f32 v96, v96, v97\n" \
"v_mfma_f32_16x16x32_bf16 v[112:115], v[36:39], v[220:223], v[112:115]\n" \
"buffer_store_dwordx4 a[20:23], v234, s[56:59], s93 offen offset:1024 sc1\n" \
"v_mfma_f32_16x16x32_bf16 v[116:119], v[40:43], v[216:219], v[116:119]\n" \
"v_cvt_pk_bf16_f32 v97, v98, v99\n" \
"v_cvt_pk_bf16_f32 v98, v100, v101\n" \
"v_mfma_f32_16x16x32_bf16 v[116:119], v[44:47], v[220:223], v[116:119]\n" \
"v_cvt_pk_bf16_f32 v99, v102, v103\n" \
"v_cvt_pk_bf16_f32 v104, v104, v105\n" \
"v_mfma_f32_16x16x32_bf16 v[120:123], v[48:51], v[216:219], v[120:123]\n" \
"v_cvt_pk_bf16_f32 v105, v106, v107\n" \
"v_cvt_pk_bf16_f32 v106, v108, v109\n" \
"v_mfma_f32_16x16x32_bf16 v[120:123], v[52:55], v[220:223], v[120:123]\n" \
"v_cvt_pk_bf16_f32 v107, v110, v111\n" \
"v_cvt_pk_bf16_f32 v80, v80, v81\n" \
"v_mfma_f32_16x16x32_bf16 v[124:127], v[56:59], v[216:219], v[124:127]\n" \
"v_cvt_pk_bf16_f32 v81, v82, v83\n" \
"v_cvt_pk_bf16_f32 v82, v84, v85\n" \
"v_mfma_f32_16x16x32_bf16 v[124:127], v[60:63], v[220:223], v[124:127]\n" \
"v_cvt_pk_bf16_f32 v83, v86, v87\n" \
"s_waitcnt lgkmcnt(0)\n" \
"s_barrier\n" \
"v_mfma_f32_16x16x32_bf16 v[128:131], v[0:3], v[192:195], v[128:131]\n" \
"v_accvgpr_write_b32 a136, v80\n" \
"v_accvgpr_write_b32 a137, v81\n" \
"v_mfma_f32_16x16x32_bf16 v[128:131], v[4:7], v[196:199], v[128:131]\n" \
"ds_read_b128 a[192:195], v237 offset:0\n" \
"v_accvgpr_write_b32 a138, v82\n" \
"v_mfma_f32_16x16x32_bf16 v[132:135], v[8:11], v[192:195], v[132:135]\n" \
"v_accvgpr_write_b32 a139, v83\n" \
"v_cvt_pk_bf16_f32 v88, v88, v89\n" \
"v_mfma_f32_16x16x32_bf16 v[132:135], v[12:15], v[196:199], v[132:135]\n" \
"ds_read_b128 a[196:199], v237 offset:2048\n" \
"v_cvt_pk_bf16_f32 v89, v90, v91\n" \
"v_mfma_f32_16x16x32_bf16 v[136:139], v[16:19], v[192:195], v[136:139]\n" \
"v_cvt_pk_bf16_f32 v90, v92, v93\n" \
"v_cvt_pk_bf16_f32 v91, v94, v95\n" \
"v_mfma_f32_16x16x32_bf16 v[136:139], v[20:23], v[196:199], v[136:139]\n" \
"ds_read_b128 a[200:203], v237 offset:4096\n" \
"v_med3_f32 v208, v248, v252, 0\n" \
"v_mfma_f32_16x16x32_bf16 v[140:143], v[24:27], v[192:195], v[140:143]\n" \
"v_med3_f32 v209, v249, v252, 0\n" \
"v_med3_f32 v210, v250, v252, 0\n" \
"v_mfma_f32_16x16x32_bf16 v[140:143], v[28:31], v[196:199], v[140:143]\n" \
"ds_read_b128 a[204:207], v237 offset:6144\n" \
"v_med3_f32 v211, v251, v252, 0\n" \
"v_mfma_f32_16x16x32_bf16 v[144:147], v[32:35], v[192:195], v[144:147]\n" \
"v_cvt_pk_bf16_f32 v112, v112, v113\n" \
"v_cvt_pk_bf16_f32 v113, v114, v115\n" \
"v_mfma_f32_16x16x32_bf16 v[144:147], v[36:39], v[196:199], v[144:147]\n" \
"ds_read_b128 a[208:211], v237 offset:256\n" \
"v_cvt_pk_bf16_f32 v114, v116, v117\n" \
"v_cvt_pk_bf16_f32 v115, v118, v119\n" \
"v_mfma_f32_16x16x32_bf16 v[148:151], v[40:43], v[192:195], v[148:151]\n" \
"buffer_store_dwordx4 a[24:27], v234, s[56:59], s93 offen offset:2048 sc1\n" \
"v_mfma_f32_16x16x32_bf16 v[148:151], v[44:47], v[196:199], v[148:151]\n" \
"ds_read_b128 a[212:215], v237 offset:2304\n" \
"v_cvt_pk_bf16_f32 v120, v120, v121\n" \
"v_mfma_f32_16x16x32_bf16 v[152:155], v[48:51], v[192:195], v[152:155]\n" \
"v_cvt_pk_bf16_f32 v121, v122, v123\n" \
"v_cvt_pk_bf16_f32 v122, v124, v125\n" \
"v_mfma_f32_16x16x32_bf16 v[152:155], v[52:55], v[196:199], v[152:155]\n" \
"ds_read_b128 a[216:219], v237 offset:4352\n" \
"v_cvt_pk_bf16_f32 v123, v126, v127\n" \
"v_mfma_f32_16x16x32_bf16 v[156:159], v[56:59], v[192:195], v[156:159]\n" \
"v_cvt_pk_bf16_f32 v128, v128, v129\n" \
"v_cvt_pk_bf16_f32 v129, v130, v131\n" \
"v_mfma_f32_16x16x32_bf16 v[156:159], v[60:63], v[196:199], v[156:159]\n" \
"ds_read_b128 a[220:223], v237 offset:6400\n" \
"v_cvt_pk_bf16_f32 v130, v132, v133\n" \
"v_mfma_f32_16x16x32_bf16 v[160:163], v[0:3], v[200:203], v[160:163]\n" \
"v_cvt_pk_bf16_f32 v131, v134, v135\n" \
"v_cvt_pk_bf16_f32 v136, v136, v137\n" \
"v_mfma_f32_16x16x32_bf16 v[160:163], v[4:7], v[204:207], v[160:163]\n" \
"ds_read_b128 a[224:227], v237 offset:8192\n" \
"v_cvt_pk_bf16_f32 v137, v138, v139\n" \
"v_mfma_f32_16x16x32_bf16 v[164:167], v[8:11], v[200:203], v[164:167]\n" \
"v_cvt_pk_bf16_f32 v138, v140, v141\n" \
"v_cvt_pk_bf16_f32 v139, v142, v143\n" \
"v_mfma_f32_16x16x32_bf16 v[164:167], v[12:15], v[204:207], v[164:167]\n" \
"ds_read_b128 a[228:231], v237 offset:10240\n" \
"v_mfma_f32_16x16x32_bf16 v[168:171], v[16:19], v[200:203], v[168:171]\n" \
"v_mfma_f32_16x16x32_bf16 v[168:171], v[20:23], v[204:207], v[168:171]\n" \
"ds_read_b128 a[232:235], v237 offset:12288\n" \
"v_cvt_pk_bf16_f32 v144, v144, v145\n" \
"v_cvt_pk_bf16_f32 v145, v146, v147\n" \
"v_mfma_f32_16x16x32_bf16 v[172:175], v[24:27], v[200:203], v[172:175]\n" \
"v_cvt_pk_bf16_f32 v146, v148, v149\n" \
"v_cvt_pk_bf16_f32 v147, v150, v151\n" \
"v_mfma_f32_16x16x32_bf16 v[172:175], v[28:31], v[204:207], v[172:175]\n" \
"ds_read_b128 a[236:239], v237 offset:14336\n" \
"v_cvt_pk_bf16_f32 v152, v152, v153\n" \
"v_mfma_f32_16x16x32_bf16 v[176:179], v[32:35], v[200:203], v[176:179]\n" \
"v_cvt_pk_bf16_f32 v153, v154, v155\n" \
"v_cvt_pk_bf16_f32 v154, v156, v157\n" \
"v_mfma_f32_16x16x32_bf16 v[176:179], v[36:39], v[204:207], v[176:179]\n" \
"ds_read_b128 a[240:243], v237 offset:8448\n" \
"v_cvt_pk_bf16_f32 v155, v158, v159\n" \
"v_mfma_f32_16x16x32_bf16 v[180:183], v[40:43], v[200:203], v[180:183]\n" \
"buffer_store_dwordx4 a[28:31], v234, s[56:59], s93 offen offset:3072 sc1\n" \
"v_mfma_f32_16x16x32_bf16 v[180:183], v[44:47], v[204:207], v[180:183]\n" \
"ds_read_b128 a[244:247], v237 offset:10496\n" \
"ds_write_b128 v239, v[128:131] offset:0\n" \
"v_mfma_f32_16x16x32_bf16 v[184:187], v[48:51], v[200:203], v[184:187]\n" \
"v_cvt_pk_bf16_f32 v160, v160, v161\n" \
"v_cvt_pk_bf16_f32 v161, v162, v163\n" \
"v_mfma_f32_16x16x32_bf16 v[184:187], v[52:55], v[204:207], v[184:187]\n" \
"ds_read_b128 a[248:251], v237 offset:12544\n" \
"v_cvt_pk_bf16_f32 v162, v164, v165\n" \
"v_cvt_pk_bf16_f32 v163, v166, v167\n" \
"v_mfma_f32_16x16x32_bf16 v[188:191], v[56:59], v[200:203], v[188:191]\n" \
"v_mfma_f32_16x16x32_bf16 v[188:191], v[60:63], v[204:207], v[188:191]\n" \
"s_waitcnt lgkmcnt(3)\n" \
"ds_read_b128 a[252:255], v237 offset:14592\n" \
"v_mfma_f32_16x16x32_bf16 v[16:19], a[192:195], v[96:99], v[208:211]\n" \
"v_cvt_pk_bf16_f32 v168, v168, v169\n" \
"v_cvt_pk_bf16_f32 v169, v170, v171\n" \
"v_mfma_f32_16x16x32_bf16 v[16:19], a[196:199], v[104:107], v[16:19]\n" \
"v_cvt_pk_bf16_f32 v170, v172, v173\n" \
"v_cvt_pk_bf16_f32 v171, v174, v175\n" \
"v_mfma_f32_16x16x32_bf16 v[16:19], a[200:203], v[112:115], v[16:19]\n" \
"v_cvt_pk_bf16_f32 v176, v176, v177\n" \
"v_cvt_pk_bf16_f32 v177, v178, v179\n" \
"v_mfma_f32_16x16x32_bf16 v[16:19], a[204:207], v[120:123], v[16:19]\n" \
"v_cvt_pk_bf16_f32 v178, v180, v181\n" \
"v_cvt_pk_bf16_f32 v179, v182, v183\n" \
"v_mfma_f32_16x16x32_bf16 v[32:35], a[204:207], v[152:155], 0\n" \
"v_cvt_pk_bf16_f32 v184, v184, v185\n" \
"v_cvt_pk_bf16_f32 v185, v186, v187\n" \
"v_mfma_f32_16x16x32_bf16 v[32:35], a[200:203], v[144:147], v[32:35]\n" \
"v_cvt_pk_bf16_f32 v186, v188, v189\n" \
"v_cvt_pk_bf16_f32 v187, v190, v191\n" \
"v_mfma_f32_16x16x32_bf16 v[32:35], a[196:199], v[136:139], v[32:35]\n" \
"v_med3_f32 v212, v248, v253, v252\n" \
"v_med3_f32 v213, v249, v253, v252\n" \
"v_mfma_f32_16x16x32_bf16 v[32:35], a[192:195], v[128:131], v[32:35]\n" \
"v_med3_f32 v214, v250, v253, v252\n" \
"v_med3_f32 v215, v251, v253, v252\n" \
"v_mfma_f32_16x16x32_bf16 v[48:51], a[192:195], v[160:163], 0\n" \
"v_med3_f32 v216, v248, v226, v253\n" \
"v_med3_f32 v217, v249, v226, v253\n" \
"v_mfma_f32_16x16x32_bf16 v[48:51], a[196:199], v[168:171], v[48:51]\n" \
"ds_read_b128 a[192:195], v237 offset:16384\n" \
"v_med3_f32 v218, v250, v226, v253\n" \
"v_mfma_f32_16x16x32_bf16 v[48:51], a[200:203], v[176:179], v[48:51]\n" \
"ds_read_b128 a[196:199], v237 offset:18432\n" \
"v_med3_f32 v219, v251, v226, v253\n" \
"v_mfma_f32_16x16x32_bf16 v[48:51], a[204:207], v[184:187], v[48:51]\n" \
"ds_read_b128 a[200:203], v237 offset:20480\n" \
"v_med3_f32 v220, v248, v240, v226\n" \
"v_mfma_f32_16x16x32_bf16 v[52:55], a[220:223], v[184:187], 0\n" \
"ds_read_b128 a[204:207], v237 offset:22528\n" \
"v_med3_f32 v221, v249, v240, v226\n" \
"v_mfma_f32_16x16x32_bf16 v[52:55], a[216:219], v[176:179], v[52:55]\n" \
"ds_write_b128 v239, v[136:139] offset:1024\n" \
"v_mfma_f32_16x16x32_bf16 v[52:55], a[212:215], v[168:171], v[52:55]\n" \
"v_med3_f32 v222, v250, v240, v226\n" \
"v_med3_f32 v223, v251, v240, v226\n" \
"v_mfma_f32_16x16x32_bf16 v[52:55], a[208:211], v[160:163], v[52:55]\n" \
"v_accvgpr_write_b32 a144, v96\n" \
"v_accvgpr_write_b32 a145, v97\n" \
"v_mfma_f32_16x16x32_bf16 v[36:39], a[208:211], v[128:131], 0\n" \
"ds_write_b128 v239, v[144:147] offset:2048\n" \
"v_mfma_f32_16x16x32_bf16 v[36:39], a[212:215], v[136:139], v[36:39]\n" \
"v_accvgpr_write_b32 a146, v98\n" \
"v_accvgpr_write_b32 a147, v99\n" \
"v_mfma_f32_16x16x32_bf16 v[36:39], a[216:219], v[144:147], v[36:39]\n" \
"v_accvgpr_write_b32 a148, v104\n" \
"v_accvgpr_write_b32 a149, v105\n" \
"v_mfma_f32_16x16x32_bf16 v[36:39], a[220:223], v[152:155], v[36:39]\n" \
"ds_write_b128 v239, v[152:155] offset:3072\n" \
"v_mfma_f32_16x16x32_bf16 v[20:23], a[220:223], v[120:123], v[212:215]\n" \
"v_accvgpr_write_b32 a150, v106\n" \
"v_accvgpr_write_b32 a151, v107\n" \
"v_mfma_f32_16x16x32_bf16 v[20:23], a[216:219], v[112:115], v[20:23]\n" \
"ds_read_b128 a[220:223], v237 offset:22784\n" \
"v_accvgpr_write_b32 a152, v112\n" \
"v_mfma_f32_16x16x32_bf16 v[20:23], a[212:215], v[104:107], v[20:23]\n" \
"ds_read_b128 a[216:219], v237 offset:20736\n" \
"v_accvgpr_write_b32 a153, v113\n" \
"v_mfma_f32_16x16x32_bf16 v[20:23], a[208:211], v[96:99], v[20:23]\n" \
"ds_read_b128 a[212:215], v237 offset:18688\n" \
"v_accvgpr_write_b32 a154, v114\n" \
"v_mfma_f32_16x16x32_bf16 v[24:27], a[224:227], v[96:99], v[216:219]\n" \
"ds_read_b128 a[208:211], v237 offset:16640\n" \
"v_accvgpr_write_b32 a155, v115\n" \
"v_mfma_f32_16x16x32_bf16 v[24:27], a[228:231], v[104:107], v[24:27]\n" \
"ds_write_b128 v239, v[160:163] offset:4096\n" \
"v_mfma_f32_16x16x32_bf16 v[24:27], a[232:235], v[112:115], v[24:27]\n" \
"v_accvgpr_write_b32 a156, v120\n" \
"v_accvgpr_write_b32 a157, v121\n" \
"v_mfma_f32_16x16x32_bf16 v[24:27], a[236:239], v[120:123], v[24:27]\n" \
"v_accvgpr_write_b32 a158, v122\n" \
"v_accvgpr_write_b32 a159, v123\n" \
"v_mfma_f32_16x16x32_bf16 v[40:43], a[236:239], v[152:155], 0\n" \
"ds_write_b128 v239, v[168:171] offset:5120\n" \
"v_mfma_f32_16x16x32_bf16 v[40:43], a[232:235], v[144:147], v[40:43]\n" \
"v_accvgpr_write_b32 a140, v88\n" \
"v_accvgpr_write_b32 a141, v89\n" \
"v_mfma_f32_16x16x32_bf16 v[40:43], a[228:231], v[136:139], v[40:43]\n" \
"v_accvgpr_write_b32 a142, v90\n" \
"v_accvgpr_write_b32 a143, v91\n" \
"v_mfma_f32_16x16x32_bf16 v[40:43], a[224:227], v[128:131], v[40:43]\n" \
"ds_write_b128 v239, v[176:179] offset:6144\n" \
"v_mfma_f32_16x16x32_bf16 v[56:59], a[224:227], v[160:163], 0\n" \
"v_add_u32 v233, s82, v235\n" \
"v_mfma_f32_16x16x32_bf16 v[56:59], a[228:231], v[168:171], v[56:59]\n" \
"ds_read_b128 a[224:227], v237 offset:24576\n" \
"v_mfma_f32_16x16x32_bf16 v[56:59], a[232:235], v[176:179], v[56:59]\n" \
"ds_read_b128 a[228:231], v237 offset:26624\n" \
"ds_write_b128 v239, v[184:187] offset:7168\n" \
"v_exp_f32 v16, v16\n" \
"v_mfma_f32_16x16x32_bf16 v[56:59], a[236:239], v[184:187], v[56:59]\n" \
"s_waitcnt lgkmcnt(4)\n" \
"ds_read_b128 a[232:235], v237 offset:28672\n" \
"v_exp_f32 v17, v17\n" \
"v_mfma_f32_16x16x32_bf16 v[60:63], a[252:255], v[184:187], 0\n" \
"ds_read_b128 a[236:239], v237 offset:30720\n" \
"v_mfma_f32_16x16x32_bf16 v[60:63], a[248:251], v[176:179], v[60:63]\n" \
"s_waitcnt vmcnt(4)\n" \
"v_cmp_eq_u32 vcc, s60, v227\n" \
"v_mfma_f32_16x16x32_bf16 v[60:63], a[244:247], v[168:171], v[60:63]\n" \
"v_exp_f32 v18, v18\n" \
"v_mfma_f32_16x16x32_bf16 v[60:63], a[240:243], v[160:163], v[60:63]\n" \
"s_andn2_b32 s70, 0xff, vcc_lo\n" \
"s_cbranch_scc1 Lpl1_%=\n" \
"Lpr1_%=:\n" \
"v_cvt_pk_bf16_f32 v16, v16, v17\n" \
"v_exp_f32 v19, v19\n" \
"v_exp_f32 v20, v20\n" \
"v_mfma_f32_16x16x32_bf16 v[44:47], a[240:243], v[128:131], 0\n" \
"v_exp_f32 v21, v21\n" \
"v_mfma_f32_16x16x32_bf16 v[44:47], a[244:247], v[136:139], v[44:47]\n" \
"s_add_u32 m0, s79, 32768\n" \
"v_exp_f32 v22, v22\n" \
"v_mfma_f32_16x16x32_bf16 v[44:47], a[248:251], v[144:147], v[44:47]\n" \
"s_add_u32 s71, s76, 0\n" \
"v_cvt_pk_bf16_f32 v17, v18, v19\n" \
"v_cvt_pk_bf16_f32 v18, v20, v21\n" \
"v_mfma_f32_16x16x32_bf16 v[44:47], a[252:255], v[152:155], v[44:47]\n" \
"buffer_load_dwordx4 v234, s[36:39], s71 offen offset:0 sc1 lds\n" \
"v_exp_f32 v23, v23\n" \
"v_exp_f32 v32, v32\n" \
"v_mfma_f32_16x16x32_bf16 v[28:31], a[252:255], v[120:123], v[220:223]\n" \
"v_exp_f32 v33, v33\n" \
"v_mfma_f32_16x16x32_bf16 v[28:31], a[248:251], v[112:115], v[28:31]\n" \
"ds_read_b128 a[252:255], v237 offset:30976\n" \
"v_cvt_pk_bf16_f32 v19, v22, v23\n" \
"v_mfma_f32_16x16x32_bf16 v[28:31], a[244:247], v[104:107], v[28:31]\n" \
"buffer_load_dwordx4 v234, s[36:39], s71 offen offset:1024 sc1 lds\n" \
"v_mfma_f32_16x16x32_bf16 v[28:31], a[240:243], v[96:99], v[28:31]\n" \
"ds_read_b128 a[244:247], v237 offset:26880\n" \
"ds_read_b128 a[248:251], v237 offset:28928\n" \
"v_exp_f32 v34, v34\n" \
"v_exp_f32 v35, v35\n" \
"v_mfma_f32_16x16x32_bf16 v[80:83], a[192:195], a[144:147], 0\n" \
"ds_read_b128 a[240:243], v237 offset:24832\n" \
"v_cvt_pk_bf16_f32 v32, v32, v33\n" \
"v_mfma_f32_16x16x32_bf16 v[80:83], a[196:199], a[148:151], v[80:83]\n" \
"buffer_load_dwordx4 v234, s[36:39], s71 offen offset:2048 sc1 lds\n" \
"v_mfma_f32_16x16x32_bf16 v[80:83], a[200:203], a[152:155], v[80:83]\n" \
"s_lshl_b32 s68, s63, 2\n" \
"s_add_u32 s68, s68, s95\n" \
"s_mov_b32 s69, 0\n" \
"v_mfma_f32_16x16x32_bf16 v[80:83], a[204:207], a[156:159], v[80:83]\n" \
"s_mov_b32 s89, s63\n" \
"ds_read_b128 a[44:47], v233 offset:15360\n" \
"v_mov_b32 v192, s60\n" \
"v_lshlrev_b32 v193, 28, s61\n" \
"v_exp_f32 v36, v36\n" \
"v_exp_f32 v37, v37\n" \
"v_mfma_f32_16x16x32_bf16 v[96:99], a[204:207], v[152:155], 0\n" \
"buffer_load_dwordx4 v234, s[36:39], s71 offen offset:3072 sc1 lds\n" \
"v_mfma_f32_16x16x32_bf16 v[96:99], a[200:203], v[144:147], v[96:99]\n" \
"s_add_u32 m0, s79, 49152\n" \
"s_mov_b32 s90, 0\n" \
"ds_read_b128 a[40:43], v233 offset:7168\n" \
"v_mfma_f32_16x16x32_bf16 v[96:99], a[196:199], v[136:139], v[96:99]\n" \
"s_add_u32 s71, s76, 17408\n" \
"v_exp_f32 v38, v38\n" \
"v_mfma_f32_16x16x32_bf16 v[96:99], a[192:195], v[128:131], v[96:99]\n" \
"buffer_load_dwordx4 v234, s[36:39], s71 offen offset:0 sc1 lds\n" \
"v_cvt_pk_bf16_f32 v33, v34, v35\n" \
"v_exp_f32 v39, v39\n" \
"v_mfma_f32_16x16x32_bf16 v[112:115], a[192:195], v[160:163], 0\n" \
"ds_read_b128 a[36:39], v233 offset:14336\n" \
"v_cvt_pk_bf16_f32 v34, v36, v37\n" \
"v_mfma_f32_16x16x32_bf16 v[112:115], a[196:199], v[168:171], v[112:115]\n" \
"v_exp_f32 v48, v48\n" \
"v_mfma_f32_16x16x32_bf16 v[112:115], a[200:203], v[176:179], v[112:115]\n" \
"buffer_load_dwordx4 v234, s[36:39], s71 offen offset:1024 sc1 lds\n" \
"v_mfma_f32_16x16x32_bf16 v[112:115], a[204:207], v[184:187], v[112:115]\n" \
"ds_read_b128 a[32:35], v233 offset:6144\n" \
"v_cvt_pk_bf16_f32 v35, v38, v39\n" \
"v_exp_f32 v49, v49\n" \
"v_exp_f32 v50, v50\n" \
"v_mfma_f32_16x16x32_bf16 v[64:67], a[204:207], a[140:143], v[208:211]\n" \
"v_exp_f32 v51, v51\n" \
"v_mfma_f32_16x16x32_bf16 v[64:67], a[200:203], a[136:139], v[64:67]\n" \
"buffer_load_dwordx4 v234, s[36:39], s71 offen offset:2048 sc1 lds\n" \
"v_mfma_f32_16x16x32_bf16 v[64:67], a[196:199], a[132:135], v[64:67]\n" \
"ds_read_b128 a[204:207], v233 offset:9216\n" \
"v_cvt_pk_bf16_f32 v48, v48, v49\n" \
"v_mfma_f32_16x16x32_bf16 v[64:67], a[192:195], a[128:131], v[64:67]\n" \
"v_exp_f32 v52, v52\n" \
"v_exp_f32 v53, v53\n" \
"v_exp_f32 v54, v54\n" \
"v_mfma_f32_16x16x32_bf16 v[68:71], a[208:211], a[128:131], v[212:215]\n" \
"buffer_load_dwordx4 v234, s[36:39], s71 offen offset:3072 sc1 lds\n" \
"v_mfma_f32_16x16x32_bf16 v[68:71], a[212:215], a[132:135], v[68:71]\n" \
"ds_read_b128 a[200:203], v233 offset:1024\n" \
"v_cvt_pk_bf16_f32 v49, v50, v51\n" \
"v_mfma_f32_16x16x32_bf16 v[68:71], a[216:219], a[136:139], v[68:71]\n" \
"s_add_u32 m0, s77, s80\n" \
"v_exp_f32 v55, v55\n" \
"v_cvt_pk_bf16_f32 v50, v52, v53\n" \
"v_mfma_f32_16x16x32_bf16 v[68:71], a[220:223], a[140:143], v[68:71]\n" \
"buffer_load_dwordx4 v234, s[40:43], s76 offen offset:0 sc1 lds\n" \
"v_cvt_pk_bf16_f32 v51, v54, v55\n" \
"v_mfma_f32_16x16x32_bf16 v[116:119], a[220:223], v[184:187], 0\n" \
"ds_read_b128 a[196:199], v233 offset:8192\n" \
"v_mfma_f32_16x16x32_bf16 v[116:119], a[216:219], v[176:179], v[116:119]\n" \
"ds_read_b128 a[160:163], v239 offset:0\n" \
"v_mfma_f32_16x16x32_bf16 v[116:119], a[212:215], v[168:171], v[116:119]\n" \
"s_add_u32 s71, s76, 17408\n" \
"ds_read_b128 a[192:195], v233 offset:0\n" \
"v_exp_f32 v24, v24\n" \
"v_mfma_f32_16x16x32_bf16 v[116:119], a[208:211], v[160:163], v[116:119]\n" \
"buffer_load_dwordx4 v234, s[40:43], s76 offen offset:1024 sc1 lds\n" \
"v_mfma_f32_16x16x32_bf16 v[100:103], a[208:211], v[128:131], 0\n" \
"ds_read_b128 a[164:167], v239 offset:1024\n" \
"v_mfma_f32_16x16x32_bf16 v[100:103], a[212:215], v[136:139], v[100:103]\n" \
"v_exp_f32 v25, v25\n" \
"v_mfma_f32_16x16x32_bf16 v[100:103], a[216:219], v[144:147], v[100:103]\n" \
"v_exp_f32 v26, v26\n" \
"v_mfma_f32_16x16x32_bf16 v[100:103], a[220:223], v[152:155], v[100:103]\n" \
"buffer_load_dwordx4 v234, s[40:43], s76 offen offset:2048 sc1 lds\n" \
"v_exp_f32 v27, v27\n" \
"v_exp_f32 v28, v28\n" \
"v_mfma_f32_16x16x32_bf16 v[84:87], a[220:223], a[156:159], 0\n" \
"ds_read_b128 a[168:171], v239 offset:2048\n" \
"v_cvt_pk_bf16_f32 v20, v24, v25\n" \
"v_mfma_f32_16x16x32_bf16 v[84:87], a[216:219], a[152:155], v[84:87]\n" \
"ds_read_b128 a[220:223], v233 offset:11264\n" \
"v_cvt_pk_bf16_f32 v21, v26, v27\n" \
"v_mfma_f32_16x16x32_bf16 v[84:87], a[212:215], a[148:151], v[84:87]\n" \
"s_waitcnt lgkmcnt(2)\n" \
"v_exp_f32 v29, v29\n" \
"v_mfma_f32_16x16x32_bf16 v[84:87], a[208:211], a[144:147], v[84:87]\n" \
"buffer_load_dwordx4 v234, s[40:43], s76 offen offset:3072 sc1 lds\n" \
"v_exp_f32 v30, v30\n" \
"v_exp_f32 v31, v31\n" \
"v_mfma_f32_16x16x32_bf16 v[88:91], a[224:227], a[144:147], 0\n" \
"ds_read_b128 a[216:219], v233 offset:3072\n" \
"v_cvt_pk_bf16_f32 v22, v28, v29\n" \
"v_mfma_f32_16x16x32_bf16 v[88:91], a[228:231], a[148:151], v[88:91]\n" \
"ds_read_b128 a[172:175], v239 offset:3072\n" \
"v_mfma_f32_16x16x32_bf16 v[88:91], a[232:235], a[152:155], v[88:91]\n" \
"s_add_u32 m0, m0, 0x4000\n" \
"ds_read_b128 a[212:215], v233 offset:10240\n" \
"v_xor_b32 v248, 0x2000, v239\n" \
"v_mfma_f32_16x16x32_bf16 v[88:91], a[236:239], a[156:159], v[88:91]\n" \
"buffer_load_dwordx4 v234, s[40:43], s71 offen offset:0 sc1 lds\n" \
"v_exp_f32 v40, v40\n" \
"v_exp_f32 v41, v41\n" \
"v_mfma_f32_16x16x32_bf16 v[104:107], a[236:239], v[152:155], 0\n" \
"ds_read_b128 a[208:211], v233 offset:2048\n" \
"v_lshlrev_b32 v252, 3, v234\n" \
"v_mfma_f32_16x16x32_bf16 v[104:107], a[232:235], v[144:147], v[104:107]\n" \
"ds_read_b128 a[176:179], v239 offset:4096\n" \
"v_mov_b32 v224, 0\n" \
"v_mfma_f32_16x16x32_bf16 v[104:107], a[228:231], v[136:139], v[104:107]\n" \
"v_mov_b32 v225, 0\n" \
"v_mov_b32 v228, 0\n" \
"v_mfma_f32_16x16x32_bf16 v[104:107], a[224:227], v[128:131], v[104:107]\n" \
"buffer_load_dwordx4 v234, s[40:43], s71 offen offset:1024 sc1 lds\n" \
"v_exp_f32 v42, v42\n" \
"v_cvt_pk_bf16_f32 v23, v30, v31\n" \
"v_mfma_f32_16x16x32_bf16 v[120:123], a[224:227], v[160:163], 0\n" \
"ds_read_b128 a[180:183], v239 offset:5120\n" \
"v_mov_b32 v229, 0\n" \
"v_mfma_f32_16x16x32_bf16 v[120:123], a[228:231], v[168:171], v[120:123]\n" \
"v_exp_f32 v43, v43\n" \
"v_mfma_f32_16x16x32_bf16 v[120:123], a[232:235], v[176:179], v[120:123]\n" \
"v_exp_f32 v44, v44\n" \
"v_cvt_pk_bf16_f32 v36, v40, v41\n" \
"v_mfma_f32_16x16x32_bf16 v[120:123], a[236:239], v[184:187], v[120:123]\n" \
"buffer_load_dwordx4 v234, s[40:43], s71 offen offset:2048 sc1 lds\n" \
"v_exp_f32 v45, v45\n" \
"v_mfma_f32_16x16x32_bf16 v[72:75], a[236:239], a[140:143], v[216:219]\n" \
"ds_read_b128 a[184:187], v239 offset:6144\n" \
"v_cvt_pk_bf16_f32 v37, v42, v43\n" \
"v_mfma_f32_16x16x32_bf16 v[72:75], a[232:235], a[136:139], v[72:75]\n" \
"ds_read_b128 a[236:239], v233 offset:13312\n" \
"v_mfma_f32_16x16x32_bf16 v[72:75], a[228:231], a[132:135], v[72:75]\n" \
"v_exp_f32 v46, v46\n" \
"v_mfma_f32_16x16x32_bf16 v[72:75], a[224:227], a[128:131], v[72:75]\n" \
"buffer_load_dwordx4 v234, s[40:43], s71 offen offset:3072 sc1 lds\n" \
"v_exp_f32 v47, v47\n" \
"v_mfma_f32_16x16x32_bf16 v[76:79], a[240:243], a[128:131], v[220:223]\n" \
"ds_read_b128 a[232:235], v233 offset:5120\n" \
"v_cvt_pk_bf16_f32 v38, v44, v45\n" \
"v_mfma_f32_16x16x32_bf16 v[76:79], a[244:247], a[132:135], v[76:79]\n" \
"ds_read_b128 a[188:191], v239 offset:7168\n" \
"v_exp_f32 v56, v56\n" \
"v_mfma_f32_16x16x32_bf16 v[76:79], a[248:251], a[136:139], v[76:79]\n" \
"ds_read_b128 a[228:231], v233 offset:12288\n" \
"v_cvt_pk_bf16_f32 v39, v46, v47\n" \
"v_mfma_f32_16x16x32_bf16 v[76:79], a[252:255], a[140:143], v[76:79]\n" \
"v_exp_f32 v57, v57\n" \
"v_exp_f32 v58, v58\n" \
"v_mfma_f32_16x16x32_bf16 v[124:127], a[252:255], v[184:187], 0\n" \
"ds_read_b128 a[224:227], v233 offset:4096\n" \
"v_cvt_pk_bf16_f32 v52, v56, v57\n" \
"v_mfma_f32_16x16x32_bf16 v[124:127], a[248:251], v[176:179], v[124:127]\n" \
"v_exp_f32 v59, v59\n" \
"v_mfma_f32_16x16x32_bf16 v[124:127], a[244:247], v[168:171], v[124:127]\n" \
"v_exp_f32 v60, v60\n" \
"v_mfma_f32_16x16x32_bf16 v[124:127], a[240:243], v[160:163], v[124:127]\n" \
"v_exp_f32 v61, v61\n" \
"v_exp_f32 v62, v62\n" \
"v_cvt_pk_bf16_f32 v53, v58, v59\n" \
"v_mfma_f32_16x16x32_bf16 v[108:111], a[240:243], v[128:131], 0\n" \
"v_exp_f32 v63, v63\n" \
"v_mfma_f32_16x16x32_bf16 v[108:111], a[244:247], v[136:139], v[108:111]\n" \
"v_cvt_pk_bf16_f32 v54, v60, v61\n" \
"v_mfma_f32_16x16x32_bf16 v[108:111], a[248:251], v[144:147], v[108:111]\n" \
"v_cvt_pk_bf16_f32 v55, v62, v63\n" \
"v_mfma_f32_16x16x32_bf16 v[108:111], a[252:255], v[152:155], v[108:111]\n" \
"v_exp_f32 v80, v80\n" \
"v_mfma_f32_16x16x32_bf16 v[92:95], a[252:255], a[156:159], 0\n" \
"v_add_u32 v232, s91, v237\n" \
"v_mfma_f32_16x16x32_bf16 v[92:95], a[248:251], a[152:155], v[92:95]\n" \
"v_exp_f32 v81, v81\n" \
"v_mfma_f32_16x16x32_bf16 v[92:95], a[244:247], a[148:151], v[92:95]\n" \
"s_waitcnt vmcnt(8) lgkmcnt(0)\n" \
"s_barrier\n" \
"v_mfma_f32_16x16x32_bf16 v[92:95], a[240:243], a[144:147], v[92:95]\n" \
"buffer_store_dword v192, v193, s[56:59], s68 offen sc1\n" \
"v_exp_f32 v82, v82\n" \
"v_mfma_f32_16x16x32_bf16 v[160:163], a[192:195], v[16:19], 0\n" \
"s_add_u32 m0, s74, s79\n" \
"ds_read_b128 a[240:243], v232 offset:8448\n" \
"v_xor_b32 v233, 0x4000, v233\n" \
"v_mfma_f32_16x16x32_bf16 v[160:163], a[196:199], v[20:23], v[160:163]\n" \
"buffer_load_dwordx4 v234, s[36:39], s73 offen offset:0 sc1 lds\n" \
"v_mfma_f32_16x16x32_bf16 a[64:67], a[192:195], v[32:35], 0\n" \
"ds_read_b128 a[244:247], v232 offset:10496\n" \
"v_cvt_pk_bf16_f32 v80, v80, v81\n" \
"v_mfma_f32_16x16x32_bf16 a[64:67], a[196:199], v[36:39], a[64:67]\n" \
"ds_read_b128 a[0:3], v248 offset:0\n" \
"v_exp_f32 v83, v83\n" \
"v_mfma_f32_16x16x32_bf16 a[96:99], a[192:195], v[48:51], 0\n" \
"Lpg5501_%=:\n" \
"s_cbranch_execz Lpg6691_%=\n" \
"v_exp_f32 v84, v84\n" \
"v_mfma_f32_16x16x32_bf16 a[96:99], a[196:199], v[52:55], a[96:99]\n" \
"s_add_u32 s71, s73, 17408\n" \
"ds_read_b128 a[192:195], v232 offset:0\n" \
"v_cvt_pk_bf16_f32 v81, v82, v83\n" \
"v_mfma_f32_16x16x32_bf16 a[100:103], a[200:203], v[48:51], 0\n" \
"buffer_load_dwordx4 v234, s[36:39], s73 offen offset:1024 sc1 lds\n" \
"v_mfma_f32_16x16x32_bf16 a[100:103], a[204:207], v[52:55], a[100:103]\n" \
"ds_read_b128 a[196:199], v232 offset:2048\n" \
"v_exp_f32 v85, v85\n" \
"v_mfma_f32_16x16x32_bf16 a[68:71], a[200:203], v[32:35], 0\n" \
"ds_read_b128 a[4:7], v248 offset:1024\n" \
"v_mfma_f32_16x16x32_bf16 a[68:71], a[204:207], v[36:39], a[68:71]\n" \
"v_exp_f32 v86, v86\n" \
"v_mfma_f32_16x16x32_bf16 v[164:167], a[200:203], v[16:19], 0\n" \
"ds_read_b128 a[8:11], v248 offset:2048\n" \
"v_cvt_pk_bf16_f32 v82, v84, v85\n" \
"v_mfma_f32_16x16x32_bf16 v[164:167], a[204:207], v[20:23], v[164:167]\n" \
"buffer_load_dwordx4 v234, s[36:39], s73 offen offset:2048 sc1 lds\n" \
"v_exp_f32 v87, v87\n" \
"v_mfma_f32_16x16x32_bf16 v[168:171], a[208:211], v[16:19], 0\n" \
"ds_read_b128 a[200:203], v232 offset:4096\n" \
"v_mfma_f32_16x16x32_bf16 v[168:171], a[212:215], v[20:23], v[168:171]\n" \
"ds_read_b128 a[204:207], v232 offset:6144\n" \
"v_exp_f32 v96, v96\n" \
"v_mfma_f32_16x16x32_bf16 a[72:75], a[208:211], v[32:35], 0\n" \
"ds_read_b128 a[12:15], v248 offset:3072\n" \
"v_cvt_pk_bf16_f32 v83, v86, v87\n" \
"v_mfma_f32_16x16x32_bf16 a[72:75], a[212:215], v[36:39], a[72:75]\n" \
"v_exp_f32 v97, v97\n" \
"v_mfma_f32_16x16x32_bf16 a[104:107], a[208:211], v[48:51], 0\n" \
"buffer_load_dwordx4 v234, s[36:39], s73 offen offset:3072 sc1 lds\n" \
"v_mfma_f32_16x16x32_bf16 a[104:107], a[212:215], v[52:55], a[104:107]\n" \
"ds_read_b128 a[208:211], v232 offset:256\n" \
"v_exp_f32 v98, v98\n" \
"v_smfmac_f32_16x16x64_bf16 a[52:55], v[244:247], v[16:23], v243\n" \
"ds_read_b128 a[212:215], v232 offset:2304\n" \
"v_cvt_pk_bf16_f32 v96, v96, v97\n" \
"v_mfma_f32_16x16x32_bf16 a[108:111], a[216:219], v[48:51], 0\n" \
"ds_read_b128 a[16:19], v248 offset:4096\n" \
"v_mfma_f32_16x16x32_bf16 a[108:111], a[220:223], v[52:55], a[108:111]\n" \
"s_add_u32 m0, m0, 0x4000\n" \
"v_exp_f32 v99, v99\n" \
"v_mfma_f32_16x16x32_bf16 a[76:79], a[216:219], v[32:35], 0\n" \
"buffer_load_dwordx4 v234, s[36:39], s71 offen offset:0 sc1 lds\n" \
"v_mfma_f32_16x16x32_bf16 a[76:79], a[220:223], v[36:39], a[76:79]\n" \
"ds_read_b128 a[20:23], v248 offset:5120\n" \
"v_exp_f32 v100, v100\n" \
"v_mfma_f32_16x16x32_bf16 v[172:175], a[216:219], v[16:19], 0\n" \
"v_exp_f32 v101, v101\n" \
"v_mfma_f32_16x16x32_bf16 v[172:175], a[220:223], v[20:23], v[172:175]\n" \
"ds_read_b128 a[216:219], v232 offset:4352\n" \
"v_cvt_pk_bf16_f32 v97, v98, v99\n" \
"v_mfma_f32_32x32x16_bf16 v[128:143], a[48:51], a[48:51], 0\n" \
"ds_read_b128 a[220:223], v232 offset:6400\n" \
"v_exp_f32 v102, v102\n" \
"v_mfma_f32_16x16x32_bf16 v[176:179], a[224:227], v[16:19], 0\n" \
"buffer_load_dwordx4 v234, s[36:39], s71 offen offset:1024 sc1 lds\n" \
"v_mfma_f32_16x16x32_bf16 v[176:179], a[228:231], v[20:23], v[176:179]\n" \
"ds_read_b128 a[24:27], v248 offset:6144\n" \
"v_cvt_pk_bf16_f32 v98, v100, v101\n" \
"v_mfma_f32_16x16x32_bf16 a[80:83], a[224:227], v[32:35], 0\n" \
"v_exp_f32 v103, v103\n" \
"v_mfma_f32_16x16x32_bf16 a[80:83], a[228:231], v[36:39], a[80:83]\n" \
"ds_read_b128 a[28:31], v248 offset:7168\n" \
"v_exp_f32 v112, v112\n" \
"v_mfma_f32_16x16x32_bf16 a[112:115], a[224:227], v[48:51], 0\n" \
"v_exp_f32 v113, v113\n" \
"v_mfma_f32_16x16x32_bf16 a[112:115], a[228:231], v[52:55], a[112:115]\n" \
"buffer_load_dwordx4 v234, s[36:39], s71 offen offset:2048 sc1 lds\n" \
"v_exp_f32 v114, v114\n" \
"v_smfmac_f32_16x16x64_bf16 v[224:227], v[244:247], v[32:39], v243\n" \
"ds_read_b128 a[224:227], v232 offset:8192\n" \
"v_cvt_pk_bf16_f32 v99, v102, v103\n" \
"v_mfma_f32_16x16x32_bf16 a[116:119], a[232:235], v[48:51], 0\n" \
"ds_read_b128 a[228:231], v232 offset:10240\n" \
"v_cvt_pk_bf16_f32 v112, v112, v113\n" \
"v_mfma_f32_16x16x32_bf16 a[116:119], a[236:239], v[52:55], a[116:119]\n" \
"v_exp_f32 v115, v115\n" \
"v_mfma_f32_16x16x32_bf16 a[84:87], a[232:235], v[32:35], 0\n" \
"v_exp_f32 v116, v116\n" \
"v_mfma_f32_16x16x32_bf16 a[84:87], a[236:239], v[36:39], a[84:87]\n" \
"buffer_load_dwordx4 v234, s[36:39], s71 offen offset:3072 sc1 lds\n" \
"v_exp_f32 v117, v117\n" \
"v_mfma_f32_16x16x32_bf16 v[180:183], a[232:235], v[16:19], 0\n" \
"v_exp_f32 v118, v118\n" \
"v_mfma_f32_16x16x32_bf16 v[180:183], a[236:239], v[20:23], v[180:183]\n" \
"s_mov_b32 s76, s73\n" \
"s_add_u32 s73, s73, 34816\n" \
"ds_read_b128 a[232:235], v232 offset:12288\n" \
"v_mfma_f32_32x32x16_bf16 v[144:159], a[48:51], a[48:51], 0\n" \
"s_cmp_eq_u32 s73, s83\n" \
"s_cselect_b32 s70, 34816, 0\n" \
"ds_read_b128 a[236:239], v232 offset:14336\n" \
"v_cvt_pk_bf16_f32 v113, v114, v115\n" \
"v_mfma_f32_16x16x32_bf16 v[184:187], a[32:35], v[16:19], 0\n" \
"s_add_u32 s73, s73, s70\n" \
"s_xor_b32 s74, s74, 0x8000\n" \
"ds_read_b128 a[248:251], v232 offset:12544\n" \
"v_mfma_f32_16x16x32_bf16 v[184:187], a[36:39], v[20:23], v[184:187]\n" \
"s_xor_b32 s77, s77, 0x8000\n" \
"ds_read_b128 a[252:255], v232 offset:14592\n" \
"v_cvt_pk_bf16_f32 v114, v116, v117\n" \
"v_mfma_f32_16x16x32_bf16 a[88:91], a[32:35], v[32:35], 0\n" \
"v_exp_f32 v119, v119\n" \
"v_mfma_f32_16x16x32_bf16 a[88:91], a[36:39], v[36:39], a[88:91]\n" \
"v_exp_f32 v64, v64\n" \
"v_mfma_f32_16x16x32_bf16 a[120:123], a[32:35], v[48:51], 0\n" \
"v_exp_f32 v65, v65\n" \
"v_mfma_f32_16x16x32_bf16 a[120:123], a[36:39], v[52:55], a[120:123]\n" \
"v_exp_f32 v66, v66\n" \
"v_cvt_pk_bf16_f32 v115, v118, v119\n" \
"v_smfmac_f32_16x16x64_bf16 v[228:231], v[244:247], v[48:55], v243\n" \
"v_cvt_pk_bf16_f32 v64, v64, v65\n" \
"v_mfma_f32_16x16x32_bf16 a[124:127], a[40:43], v[48:51], 0\n" \
"v_exp_f32 v67, v67\n" \
"v_mfma_f32_16x16x32_bf16 a[124:127], a[44:47], v[52:55], a[124:127]\n" \
"v_exp_f32 v68, v68\n" \
"v_exp_f32 v69, v69\n" \
"v_mfma_f32_16x16x32_bf16 a[92:95], a[40:43], v[32:35], 0\n" \
"v_exp_f32 v70, v70\n" \
"v_mfma_f32_16x16x32_bf16 a[92:95], a[44:47], v[36:39], a[92:95]\n" \
"v_cvt_pk_bf16_f32 v65, v66, v67\n" \
"v_cvt_pk_bf16_f32 v66, v68, v69\n" \
"v_mfma_f32_16x16x32_bf16 v[188:191], a[40:43], v[16:19], 0\n" \
"s_cmp_eq_u32 s63, 0\n" \
"v_exp_f32 v71, v71\n" \
"v_mfma_f32_16x16x32_bf16 v[188:191], a[44:47], v[20:23], v[188:191]\n" \
"s_waitcnt lgkmcnt(0)\n" \
"v_cvt_pk_bf16_f32 v67, v70, v71\n" \
"s_cbranch_scc1 Lloop2_%=\n" \
"Lloop_%=:\n" \
"v_mfma_f32_16x16x32_bf16 v[0:3], a[192:195], a[128:131], 0\n" \
"v_exp_f32 v72, v72\n" \
"v_mfma_f32_16x16x32_bf16 v[0:3], a[196:199], a[132:135], v[0:3]\n" \
"v_mfma_f32_16x16x32_bf16 v[0:3], a[200:203], a[136:139], v[0:3]\n" \
"v_exp_f32 v73, v73\n" \
"v_mfma_f32_16x16x32_bf16 v[0:3], a[204:207], a[140:143], v[0:3]\n" \
"ds_read_b128 a[32:35], v233 offset:6144\n" \
"v_cvt_pk_bf16_f32 v68, v72, v73\n" \
"v_mfma_f32_16x16x32_bf16 v[16:19], a[204:207], a[156:159], 0\n" \
"v_exp_f32 v74, v74\n" \
"v_mfma_f32_16x16x32_bf16 v[16:19], a[200:203], a[152:155], v[16:19]\n" \
"global_load_dword v253, v252, s[44:45]\n" \
"v_mfma_f32_16x16x32_bf16 v[16:19], a[196:199], a[148:151], v[16:19]\n" \
"s_sub_u32 s46, s46, 1\n" \
"v_exp_f32 v75, v75\n" \
"v_mfma_f32_16x16x32_bf16 v[16:19], a[192:195], a[144:147], v[16:19]\n" \
"ds_read_b128 a[36:39], v233 offset:14336\n" \
"v_cvt_pk_bf16_f32 v69, v74, v75\n" \
"v_mfma_f32_16x16x32_bf16 v[32:35], a[192:195], a[160:163], 0\n" \
"v_exp_f32 v76, v76\n" \
"v_mfma_f32_16x16x32_bf16 v[32:35], a[196:199], a[164:167], v[32:35]\n" \
"s_cmp_gt_i32 s46, 0\n" \
"s_cselect_b32 s47, 0x100000, 0\n" \
"v_mfma_f32_16x16x32_bf16 v[32:35], a[200:203], a[168:171], v[32:35]\n" \
"v_exp_f32 v77, v77\n" \
"v_mfma_f32_16x16x32_bf16 v[32:35], a[204:207], a[172:175], v[32:35]\n" \
"s_add_u32 s44, s44, s47\n" \
"s_addc_u32 s45, s45, 0\n" \
"ds_read_b128 a[40:43], v233 offset:7168\n" \
"v_cvt_pk_bf16_f32 v70, v76, v77\n" \
"v_mfma_f32_16x16x32_bf16 v[48:51], a[204:207], a[188:191], 0\n" \
"v_exp_f32 v78, v78\n" \
"v_mfma_f32_16x16x32_bf16 v[48:51], a[200:203], a[184:187], v[48:51]\n" \
"v_xor_b32 v232, 0x4000, v232\n" \
"v_mfma_f32_16x16x32_bf16 v[48:51], a[196:199], a[180:183], v[48:51]\n" \
"v_exp_f32 v79, v79\n" \
"v_mfma_f32_16x16x32_bf16 v[48:51], a[192:195], a[176:179], v[48:51]\n" \
"ds_read_b128 a[204:207], v233 offset:9216\n" \
"v_cvt_pk_bf16_f32 v71, v78, v79\n" \
"v_mfma_f32_16x16x32_bf16 v[52:55], a[208:211], a[176:179], 0\n" \
"v_exp_f32 v88, v88\n" \
"v_mfma_f32_16x16x32_bf16 v[52:55], a[212:215], a[180:183], v[52:55]\n" \
"v_mfma_f32_16x16x32_bf16 v[52:55], a[216:219], a[184:187], v[52:55]\n" \
"v_exp_f32 v89, v89\n" \
"v_mfma_f32_16x16x32_bf16 v[52:55], a[220:223], a[188:191], v[52:55]\n" \
"ds_read_b128 a[200:203], v233 offset:1024\n" \
"v_cvt_pk_bf16_f32 v84, v88, v89\n" \
"v_mfma_f32_16x16x32_bf16 v[36:39], a[220:223], a[172:175], 0\n" \
"v_exp_f32 v90, v90\n" \
"v_mfma_f32_16x16x32_bf16 v[36:39], a[216:219], a[168:171], v[36:39]\n" \
"v_mfma_f32_16x16x32_bf16 v[36:39], a[212:215], a[164:167], v[36:39]\n" \
"v_exp_f32 v91, v91\n" \
"v_mfma_f32_16x16x32_bf16 v[36:39], a[208:211], a[160:163], v[36:39]\n" \
"ds_read_b128 a[196:199], v233 offset:8192\n" \
"v_cvt_pk_bf16_f32 v85, v90, v91\n" \
"v_mfma_f32_16x16x32_bf16 v[20:23], a[208:211], a[144:147], 0\n" \
"v_exp_f32 v92, v92\n" \
"v_mfma_f32_16x16x32_bf16 v[20:23], a[212:215], a[148:151], v[20:23]\n" \
"v_mfma_f32_16x16x32_bf16 v[20:23], a[216:219], a[152:155], v[20:23]\n" \
"v_exp_f32 v93, v93\n" \
"v_mfma_f32_16x16x32_bf16 v[20:23], a[220:223], a[156:159], v[20:23]\n" \
"ds_read_b128 a[192:195], v233 offset:0\n" \
"v_cvt_pk_bf16_f32 v86, v92, v93\n" \
"v_mfma_f32_16x16x32_bf16 v[4:7], a[220:223], a[140:143], 0\n" \
"v_exp_f32 v94, v94\n" \
"v_mfma_f32_16x16x32_bf16 v[4:7], a[216:219], a[136:139], v[4:7]\n" \
"v_mfma_f32_16x16x32_bf16 v[4:7], a[212:215], a[132:135], v[4:7]\n" \
"s_waitcnt lgkmcnt(7)\n" \
"v_exp_f32 v95, v95\n" \
"v_mfma_f32_16x16x32_bf16 v[4:7], a[208:211], a[128:131], v[4:7]\n" \
"ds_read_b128 a[220:223], v233 offset:11264\n" \
"v_cvt_pk_bf16_f32 v87, v94, v95\n" \
"v_mfma_f32_16x16x32_bf16 v[8:11], a[224:227], a[128:131], 0\n" \
"v_exp_f32 v104, v104\n" \
"v_mfma_f32_16x16x32_bf16 v[8:11], a[228:231], a[132:135], v[8:11]\n" \
"v_mfma_f32_16x16x32_bf16 v[8:11], a[232:235], a[136:139], v[8:11]\n" \
"v_exp_f32 v105, v105\n" \
"v_mfma_f32_16x16x32_bf16 v[8:11], a[236:239], a[140:143], v[8:11]\n" \
"ds_read_b128 a[216:219], v233 offset:3072\n" \
"v_cvt_pk_bf16_f32 v100, v104, v105\n" \
"v_mfma_f32_16x16x32_bf16 v[24:27], a[236:239], a[156:159], 0\n" \
"v_exp_f32 v106, v106\n" \
"v_mfma_f32_16x16x32_bf16 v[24:27], a[232:235], a[152:155], v[24:27]\n" \
"v_mfma_f32_16x16x32_bf16 v[24:27], a[228:231], a[148:151], v[24:27]\n" \
"v_exp_f32 v107, v107\n" \
"v_mfma_f32_16x16x32_bf16 v[24:27], a[224:227], a[144:147], v[24:27]\n" \
"ds_read_b128 a[212:215], v233 offset:10240\n" \
"v_cvt_pk_bf16_f32 v101, v106, v107\n" \
"v_mfma_f32_16x16x32_bf16 v[40:43], a[224:227], a[160:163], 0\n" \
"v_exp_f32 v108, v108\n" \
"v_mfma_f32_16x16x32_bf16 v[40:43], a[228:231], a[164:167], v[40:43]\n" \
"v_mfma_f32_16x16x32_bf16 v[40:43], a[232:235], a[168:171], v[40:43]\n" \
"v_exp_f32 v109, v109\n" \
"v_mfma_f32_16x16x32_bf16 v[40:43], a[236:239], a[172:175], v[40:43]\n" \
"ds_read_b128 a[208:211], v233 offset:2048\n" \
"v_cvt_pk_bf16_f32 v102, v108, v109\n" \
"v_mfma_f32_16x16x32_bf16 v[56:59], a[236:239], a[188:191], 0\n" \
"v_exp_f32 v110, v110\n" \
"v_mfma_f32_16x16x32_bf16 v[56:59], a[232:235], a[184:187], v[56:59]\n" \
"v_mfma_f32_16x16x32_bf16 v[56:59], a[228:231], a[180:183], v[56:59]\n" \
"v_exp_f32 v111, v111\n" \
"v_mfma_f32_16x16x32_bf16 v[56:59], a[224:227], a[176:179], v[56:59]\n" \
"ds_read_b128 a[236:239], v233 offset:13312\n" \
"v_cvt_pk_bf16_f32 v103, v110, v111\n" \
"v_mfma_f32_16x16x32_bf16 v[60:63], a[240:243], a[176:179], 0\n" \
"v_exp_f32 v120, v120\n" \
"v_mfma_f32_16x16x32_bf16 v[60:63], a[244:247], a[180:183], v[60:63]\n" \
"v_mfma_f32_16x16x32_bf16 v[60:63], a[248:251], a[184:187], v[60:63]\n" \
"v_exp_f32 v121, v121\n" \
"v_mfma_f32_16x16x32_bf16 v[60:63], a[252:255], a[188:191], v[60:63]\n" \
"ds_read_b128 a[232:235], v233 offset:5120\n" \
"v_cvt_pk_bf16_f32 v116, v120, v121\n" \
"v_mfma_f32_16x16x32_bf16 v[44:47], a[252:255], a[172:175], 0\n" \
"v_exp_f32 v122, v122\n" \
"v_mfma_f32_16x16x32_bf16 v[44:47], a[248:251], a[168:171], v[44:47]\n" \
"v_mfma_f32_16x16x32_bf16 v[44:47], a[244:247], a[164:167], v[44:47]\n" \
"v_exp_f32 v123, v123\n" \
"v_mfma_f32_16x16x32_bf16 v[44:47], a[240:243], a[160:163], v[44:47]\n" \
"ds_read_b128 a[228:231], v233 offset:12288\n" \
"v_cvt_pk_bf16_f32 v117, v122, v123\n" \
"v_mfma_f32_16x16x32_bf16 v[28:31], a[240:243], a[144:147], 0\n" \
"v_exp_f32 v124, v124\n" \
"v_mfma_f32_16x16x32_bf16 v[28:31], a[244:247], a[148:151], v[28:31]\n" \
"v_mfma_f32_16x16x32_bf16 v[28:31], a[248:251], a[152:155], v[28:31]\n" \
"v_exp_f32 v125, v125\n" \
"v_mfma_f32_16x16x32_bf16 v[28:31], a[252:255], a[156:159], v[28:31]\n" \
"ds_read_b128 a[224:227], v233 offset:4096\n" \
"v_cvt_pk_bf16_f32 v118, v124, v125\n" \
"v_mfma_f32_16x16x32_bf16 v[12:15], a[252:255], a[140:143], 0\n" \
"v_exp_f32 v126, v126\n" \
"v_mfma_f32_16x16x32_bf16 v[12:15], a[248:251], a[136:139], v[12:15]\n" \
"v_mfma_f32_16x16x32_bf16 v[12:15], a[244:247], a[132:135], v[12:15]\n" \
"s_waitcnt lgkmcnt(2)\n" \
"v_exp_f32 v127, v127\n" \
"v_mfma_f32_16x16x32_bf16 v[12:15], a[240:243], a[128:131], v[12:15]\n" \
"ds_read_b128 a[44:47], v233 offset:15360\n" \
"v_cvt_pk_bf16_f32 v119, v126, v127\n" \
"v_mfma_f32_16x16x32_bf16 v[128:131], a[192:195], v[64:67], v[128:131]\n" \
"v_exp_f32 v0, v0\n" \
"v_mfma_f32_16x16x32_bf16 v[128:131], a[196:199], v[68:71], v[128:131]\n" \
"ds_read_b128 a[244:247], v232 offset:10496\n" \
"v_mfma_f32_16x16x32_bf16 v[160:163], a[192:195], v[80:83], v[160:163]\n" \
"v_exp_f32 v1, v1\n" \
"v_mfma_f32_16x16x32_bf16 v[160:163], a[196:199], v[84:87], v[160:163]\n" \
"ds_read_b128 a[240:243], v232 offset:8448\n" \
"v_cvt_pk_bf16_f32 v0, v0, v1\n" \
"v_mfma_f32_16x16x32_bf16 a[64:67], a[192:195], v[96:99], a[64:67]\n" \
"v_exp_f32 v2, v2\n" \
"v_mfma_f32_16x16x32_bf16 a[64:67], a[196:199], v[100:103], a[64:67]\n" \
"ds_read_b128 a[248:251], v232 offset:12544\n" \
"v_xor_b32 v233, 0xc000, v233\n" \
"v_mfma_f32_16x16x32_bf16 a[96:99], a[192:195], v[112:115], a[96:99]\n" \
"v_exp_f32 v3, v3\n" \
"v_mfma_f32_16x16x32_bf16 a[96:99], a[196:199], v[116:119], a[96:99]\n" \
"ds_read_b128 a[192:195], v232 offset:0\n" \
"v_cvt_pk_bf16_f32 v1, v2, v3\n" \
"v_mfma_f32_16x16x32_bf16 a[100:103], a[200:203], v[112:115], a[100:103]\n" \
"v_exp_f32 v4, v4\n" \
"v_mfma_f32_16x16x32_bf16 a[100:103], a[204:207], v[116:119], a[100:103]\n" \
"ds_read_b128 a[252:255], v232 offset:14592\n" \
"v_mfma_f32_16x16x32_bf16 a[68:71], a[200:203], v[96:99], a[68:71]\n" \
"v_exp_f32 v5, v5\n" \
"v_mfma_f32_16x16x32_bf16 a[68:71], a[204:207], v[100:103], a[68:71]\n" \
"ds_read_b128 a[196:199], v232 offset:2048\n" \
"v_cvt_pk_bf16_f32 v2, v4, v5\n" \
"v_smfmac_f32_16x16x64_bf16 a[48:51], v[244:247], v[64:71], v243\n" \
"v_mfma_f32_16x16x32_bf16 v[164:167], a[200:203], v[80:83], v[164:167]\n" \
"v_exp_f32 v6, v6\n" \
"v_mfma_f32_16x16x32_bf16 v[164:167], a[204:207], v[84:87], v[164:167]\n" \
"v_mfma_f32_16x16x32_bf16 v[132:135], a[200:203], v[64:67], v[132:135]\n" \
"v_exp_f32 v7, v7\n" \
"v_mfma_f32_16x16x32_bf16 v[132:135], a[204:207], v[68:71], v[132:135]\n" \
"ds_read_b128 a[200:203], v232 offset:4096\n" \
"v_cvt_pk_bf16_f32 v3, v6, v7\n" \
"v_mfma_f32_16x16x32_bf16 v[136:139], a[208:211], v[64:67], v[136:139]\n" \
"v_exp_f32 v16, v16\n" \
"v_mfma_f32_16x16x32_bf16 v[136:139], a[212:215], v[68:71], v[136:139]\n" \
"v_mfma_f32_16x16x32_bf16 v[168:171], a[208:211], v[80:83], v[168:171]\n" \
"v_exp_f32 v17, v17\n" \
"v_mfma_f32_16x16x32_bf16 v[168:171], a[212:215], v[84:87], v[168:171]\n" \
"ds_read_b128 a[204:207], v232 offset:6144\n" \
"v_cvt_pk_bf16_f32 v16, v16, v17\n" \
"v_mfma_f32_16x16x32_bf16 a[72:75], a[208:211], v[96:99], a[72:75]\n" \
"v_exp_f32 v18, v18\n" \
"v_mfma_f32_16x16x32_bf16 a[72:75], a[212:215], v[100:103], a[72:75]\n" \
"v_mfma_f32_16x16x32_bf16 a[104:107], a[208:211], v[112:115], a[104:107]\n" \
"v_exp_f32 v19, v19\n" \
"v_mfma_f32_16x16x32_bf16 a[104:107], a[212:215], v[116:119], a[104:107]\n" \
"ds_read_b128 a[208:211], v232 offset:256\n" \
"v_cvt_pk_bf16_f32 v17, v18, v19\n" \
"v_mfma_f32_16x16x32_bf16 a[108:111], a[216:219], v[112:115], a[108:111]\n" \
"v_exp_f32 v20, v20\n" \
"v_mfma_f32_16x16x32_bf16 a[108:111], a[220:223], v[116:119], a[108:111]\n" \
"v_mfma_f32_16x16x32_bf16 a[76:79], a[216:219], v[96:99], a[76:79]\n" \
"v_exp_f32 v21, v21\n" \
"v_mfma_f32_16x16x32_bf16 a[76:79], a[220:223], v[100:103], a[76:79]\n" \
"ds_read_b128 a[212:215], v232 offset:2304\n" \
"v_cvt_pk_bf16_f32 v18, v20, v21\n" \
"v_smfmac_f32_16x16x64_bf16 a[52:55], v[244:247], v[80:87], v243\n" \
"v_mfma_f32_16x16x32_bf16 v[172:175], a[216:219], v[80:83], v[172:175]\n" \
"v_exp_f32 v22, v22\n" \
"v_mfma_f32_16x16x32_bf16 v[172:175], a[220:223], v[84:87], v[172:175]\n" \
"v_mfma_f32_16x16x32_bf16 v[140:143], a[216:219], v[64:67], v[140:143]\n" \
"s_waitcnt lgkmcnt(10)\n" \
"v_exp_f32 v23, v23\n" \
"v_mfma_f32_16x16x32_bf16 v[140:143], a[220:223], v[68:71], v[140:143]\n" \
"ds_read_b128 a[216:219], v232 offset:4352\n" \
"v_cvt_pk_bf16_f32 v19, v22, v23\n" \
"v_mfma_f32_16x16x32_bf16 v[144:147], a[224:227], v[64:67], v[144:147]\n" \
"v_exp_f32 v32, v32\n" \
"v_mfma_f32_16x16x32_bf16 v[144:147], a[228:231], v[68:71], v[144:147]\n" \
"v_mfma_f32_16x16x32_bf16 v[176:179], a[224:227], v[80:83], v[176:179]\n" \
"v_exp_f32 v33, v33\n" \
"v_mfma_f32_16x16x32_bf16 v[176:179], a[228:231], v[84:87], v[176:179]\n" \
"ds_read_b128 a[220:223], v232 offset:6400\n" \
"v_cvt_pk_bf16_f32 v32, v32, v33\n" \
"v_mfma_f32_16x16x32_bf16 a[80:83], a[224:227], v[96:99], a[80:83]\n" \
"v_exp_f32 v34, v34\n" \
"v_mfma_f32_16x16x32_bf16 a[80:83], a[228:231], v[100:103], a[80:83]\n" \
"v_mfma_f32_16x16x32_bf16 a[112:115], a[224:227], v[112:115], a[112:115]\n" \
"v_exp_f32 v35, v35\n" \
"v_mfma_f32_16x16x32_bf16 a[112:115], a[228:231], v[116:119], a[112:115]\n" \
"ds_read_b128 a[224:227], v232 offset:8192\n" \
"v_cvt_pk_bf16_f32 v33, v34, v35\n" \
"v_mfma_f32_16x16x32_bf16 a[116:119], a[232:235], v[112:115], a[116:119]\n" \
"v_exp_f32 v36, v36\n" \
"v_mfma_f32_16x16x32_bf16 a[116:119], a[236:239], v[116:119], a[116:119]\n" \
"v_mfma_f32_16x16x32_bf16 a[84:87], a[232:235], v[96:99], a[84:87]\n" \
"v_exp_f32 v37, v37\n" \
"v_mfma_f32_16x16x32_bf16 a[84:87], a[236:239], v[100:103], a[84:87]\n" \
"ds_read_b128 a[228:231], v232 offset:10240\n" \
"v_cvt_pk_bf16_f32 v34, v36, v37\n" \
"v_smfmac_f32_16x16x64_bf16 v[224:227], v[244:247], v[96:103], v243\n" \
"v_mfma_f32_16x16x32_bf16 v[180:183], a[232:235], v[80:83], v[180:183]\n" \
"v_exp_f32 v38, v38\n" \
"v_mfma_f32_16x16x32_bf16 v[180:183], a[236:239], v[84:87], v[180:183]\n" \
"v_mfma_f32_16x16x32_bf16 v[148:151], a[232:235], v[64:67], v[148:151]\n" \
"v_exp_f32 v39, v39\n" \
"v_mfma_f32_16x16x32_bf16 v[148:151], a[236:239], v[68:71], v[148:151]\n" \
"ds_read_b128 a[232:235], v232 offset:12288\n" \
"v_cvt_pk_bf16_f32 v35, v38, v39\n" \
"v_mfma_f32_16x16x32_bf16 v[152:155], a[32:35], v[64:67], v[152:155]\n" \
"v_exp_f32 v48, v48\n" \
"v_mfma_f32_16x16x32_bf16 v[152:155], a[36:39], v[68:71], v[152:155]\n" \
"v_mfma_f32_16x16x32_bf16 v[184:187], a[32:35], v[80:83], v[184:187]\n" \
"v_exp_f32 v49, v49\n" \
"v_mfma_f32_16x16x32_bf16 v[184:187], a[36:39], v[84:87], v[184:187]\n" \
"ds_read_b128 a[236:239], v232 offset:14336\n" \
"v_cvt_pk_bf16_f32 v48, v48, v49\n" \
"v_mfma_f32_16x16x32_bf16 a[88:91], a[32:35], v[96:99], a[88:91]\n" \
"v_exp_f32 v50, v50\n" \
"v_mfma_f32_16x16x32_bf16 a[88:91], a[36:39], v[100:103], a[88:91]\n" \
"v_mfma_f32_16x16x32_bf16 a[120:123], a[32:35], v[112:115], a[120:123]\n" \
"v_exp_f32 v51, v51\n" \
"v_mfma_f32_16x16x32_bf16 a[120:123], a[36:39], v[116:119], a[120:123]\n" \
"v_cvt_pk_bf16_f32 v49, v50, v51\n" \
"v_mfma_f32_16x16x32_bf16 a[124:127], a[40:43], v[112:115], a[124:127]\n" \
"v_exp_f32 v52, v52\n" \
"v_mfma_f32_16x16x32_bf16 a[124:127], a[44:47], v[116:119], a[124:127]\n" \
"v_mfma_f32_16x16x32_bf16 a[92:95], a[40:43], v[96:99], a[92:95]\n" \
"v_exp_f32 v53, v53\n" \
"v_mfma_f32_16x16x32_bf16 a[92:95], a[44:47], v[100:103], a[92:95]\n" \
"v_cvt_pk_bf16_f32 v50, v52, v53\n" \
"v_smfmac_f32_16x16x64_bf16 v[228:231], v[244:247], v[112:119], v243\n" \
"v_mfma_f32_16x16x32_bf16 v[188:191], a[40:43], v[80:83], v[188:191]\n" \
"v_exp_f32 v54, v54\n" \
"v_mfma_f32_16x16x32_bf16 v[188:191], a[44:47], v[84:87], v[188:191]\n" \
"v_mfma_f32_16x16x32_bf16 v[156:159], a[40:43], v[64:67], v[156:159]\n" \
"s_waitcnt vmcnt(1) lgkmcnt(0)\n" \
"v_exp_f32 v55, v55\n" \
"v_mfma_f32_16x16x32_bf16 v[156:159], a[44:47], v[68:71], v[156:159]\n" \
"s_barrier\n" \
"v_cvt_pk_bf16_f32 v51, v54, v55\n" \
"v_mfma_f32_16x16x32_bf16 v[64:67], a[192:195], a[128:131], 0\n" \
"v_exp_f32 v8, v8\n" \
"v_mfma_f32_16x16x32_bf16 v[64:67], a[196:199], a[132:135], v[64:67]\n" \
"v_cmp_eq_u32 vcc, s60, v239\n" \
"v_mfma_f32_16x16x32_bf16 v[64:67], a[200:203], a[136:139], v[64:67]\n" \
"v_exp_f32 v9, v9\n" \
"v_mfma_f32_16x16x32_bf16 v[64:67], a[204:207], a[140:143], v[64:67]\n" \
"s_andn2_b32 s70, s69, vcc_lo\n" \
"s_cbranch_scc1 Lpl2_%=\n" \
"Lpr2_%=:\n" \
"ds_read_b128 a[32:35], v233 offset:6144\n" \
"v_cvt_pk_bf16_f32 v4, v8, v9\n" \
"v_mfma_f32_16x16x32_bf16 v[80:83], a[204:207], a[156:159], 0\n" \
"s_add_u32 m0, s77, s80\n" \
"v_exp_f32 v10, v10\n" \
"v_mfma_f32_16x16x32_bf16 v[80:83], a[200:203], a[152:155], v[80:83]\n" \
"buffer_load_dwordx4 v234, s[40:43], s76 offen offset:0 sc1 lds\n" \
"v_mfma_f32_16x16x32_bf16 v[80:83], a[196:199], a[148:151], v[80:83]\n" \
"s_sub_u32 s89, s89, 1\n" \
"v_exp_f32 v11, v11\n" \
"v_mfma_f32_16x16x32_bf16 v[80:83], a[192:195], a[144:147], v[80:83]\n" \
"s_cmp_eq_u32 s89, 0\n" \
"s_cselect_b32 s70, s78, 0xc000\n" \
"ds_read_b128 a[36:39], v233 offset:14336\n" \
"v_cvt_pk_bf16_f32 v5, v10, v11\n" \
"v_mfma_f32_16x16x32_bf16 v[96:99], a[192:195], a[160:163], 0\n" \
"s_add_u32 s71, s76, 17408\n" \
"v_exp_f32 v12, v12\n" \
"v_mfma_f32_16x16x32_bf16 v[96:99], a[196:199], a[164:167], v[96:99]\n" \
"buffer_load_dwordx4 v234, s[40:43], s76 offen offset:1024 sc1 lds\n" \
"v_mfma_f32_16x16x32_bf16 v[96:99], a[200:203], a[168:171], v[96:99]\n" \
"v_exp_f32 v13, v13\n" \
"v_mfma_f32_16x16x32_bf16 v[96:99], a[204:207], a[172:175], v[96:99]\n" \
"ds_read_b128 a[40:43], v233 offset:7168\n" \
"v_cvt_pk_bf16_f32 v6, v12, v13\n" \
"v_mfma_f32_16x16x32_bf16 v[112:115], a[204:207], a[188:191], 0\n" \
"v_exp_f32 v14, v14\n" \
"v_mfma_f32_16x16x32_bf16 v[112:115], a[200:203], a[184:187], v[112:115]\n" \
"v_xor_b32 v232, s70, v232\n" \
"v_mfma_f32_16x16x32_bf16 v[112:115], a[196:199], a[180:183], v[112:115]\n" \
"v_exp_f32 v15, v15\n" \
"v_mfma_f32_16x16x32_bf16 v[112:115], a[192:195], a[176:179], v[112:115]\n" \
"ds_read_b128 a[204:207], v233 offset:9216\n" \
"v_cvt_pk_bf16_f32 v7, v14, v15\n" \
"v_mfma_f32_16x16x32_bf16 v[116:119], a[208:211], a[176:179], 0\n" \
"v_exp_f32 v24, v24\n" \
"v_mfma_f32_16x16x32_bf16 v[116:119], a[212:215], a[180:183], v[116:119]\n" \
"buffer_load_dwordx4 v234, s[40:43], s76 offen offset:2048 sc1 lds\n" \
"v_mfma_f32_16x16x32_bf16 v[116:119], a[216:219], a[184:187], v[116:119]\n" \
"v_exp_f32 v25, v25\n" \
"v_mfma_f32_16x16x32_bf16 v[116:119], a[220:223], a[188:191], v[116:119]\n" \
"ds_read_b128 a[200:203], v233 offset:1024\n" \
"v_cvt_pk_bf16_f32 v20, v24, v25\n" \
"v_mfma_f32_16x16x32_bf16 v[100:103], a[220:223], a[172:175], 0\n" \
"v_exp_f32 v26, v26\n" \
"v_mfma_f32_16x16x32_bf16 v[100:103], a[216:219], a[168:171], v[100:103]\n" \
"buffer_load_dwordx4 v234, s[40:43], s76 offen offset:3072 sc1 lds\n" \
"v_mfma_f32_16x16x32_bf16 v[100:103], a[212:215], a[164:167], v[100:103]\n" \
"v_exp_f32 v27, v27\n" \
"v_mfma_f32_16x16x32_bf16 v[100:103], a[208:211], a[160:163], v[100:103]\n" \
"ds_read_b128 a[196:199], v233 offset:8192\n" \
"v_cvt_pk_bf16_f32 v21, v26, v27\n" \
"v_mfma_f32_16x16x32_bf16 v[84:87], a[208:211], a[144:147], 0\n" \
"v_exp_f32 v28, v28\n" \
"v_mfma_f32_16x16x32_bf16 v[84:87], a[212:215], a[148:151], v[84:87]\n" \
"v_mfma_f32_16x16x32_bf16 v[84:87], a[216:219], a[152:155], v[84:87]\n" \
"v_exp_f32 v29, v29\n" \
"v_mfma_f32_16x16x32_bf16 v[84:87], a[220:223], a[156:159], v[84:87]\n" \
"ds_read_b128 a[192:195], v233 offset:0\n" \
"v_cvt_pk_bf16_f32 v22, v28, v29\n" \
"v_mfma_f32_16x16x32_bf16 v[68:71], a[220:223], a[140:143], 0\n" \
"s_add_u32 m0, m0, 0x4000\n" \
"v_exp_f32 v30, v30\n" \
"v_mfma_f32_16x16x32_bf16 v[68:71], a[216:219], a[136:139], v[68:71]\n" \
"buffer_load_dwordx4 v234, s[40:43], s71 offen offset:0 sc1 lds\n" \
"v_mfma_f32_16x16x32_bf16 v[68:71], a[212:215], a[132:135], v[68:71]\n" \
"v_exp_f32 v31, v31\n" \
"v_mfma_f32_16x16x32_bf16 v[68:71], a[208:211], a[128:131], v[68:71]\n" \
"ds_read_b128 a[220:223], v233 offset:11264\n" \
"v_cvt_pk_bf16_f32 v23, v30, v31\n" \
"v_mfma_f32_16x16x32_bf16 v[72:75], a[224:227], a[128:131], 0\n" \
"v_exp_f32 v40, v40\n" \
"v_mfma_f32_16x16x32_bf16 v[72:75], a[228:231], a[132:135], v[72:75]\n" \
"buffer_load_dwordx4 v234, s[40:43], s71 offen offset:1024 sc1 lds\n" \
"v_mfma_f32_16x16x32_bf16 v[72:75], a[232:235], a[136:139], v[72:75]\n" \
"v_exp_f32 v41, v41\n" \
"v_mfma_f32_16x16x32_bf16 v[72:75], a[236:239], a[140:143], v[72:75]\n" \
"ds_read_b128 a[216:219], v233 offset:3072\n" \
"v_cvt_pk_bf16_f32 v36, v40, v41\n" \
"v_mfma_f32_16x16x32_bf16 v[88:91], a[236:239], a[156:159], 0\n" \
"v_exp_f32 v42, v42\n" \
"v_mfma_f32_16x16x32_bf16 v[88:91], a[232:235], a[152:155], v[88:91]\n" \
"v_mfma_f32_16x16x32_bf16 v[88:91], a[228:231], a[148:151], v[88:91]\n" \
"v_exp_f32 v43, v43\n" \
"v_mfma_f32_16x16x32_bf16 v[88:91], a[224:227], a[144:147], v[88:91]\n" \
"ds_read_b128 a[212:215], v233 offset:10240\n" \
"v_cvt_pk_bf16_f32 v37, v42, v43\n" \
"v_mfma_f32_16x16x32_bf16 v[104:107], a[224:227], a[160:163], 0\n" \
"v_exp_f32 v44, v44\n" \
"v_mfma_f32_16x16x32_bf16 v[104:107], a[228:231], a[164:167], v[104:107]\n" \
"buffer_load_dwordx4 v234, s[40:43], s71 offen offset:2048 sc1 lds\n" \
"v_mfma_f32_16x16x32_bf16 v[104:107], a[232:235], a[168:171], v[104:107]\n" \
"v_exp_f32 v45, v45\n" \
"v_mfma_f32_16x16x32_bf16 v[104:107], a[236:239], a[172:175], v[104:107]\n" \
"ds_read_b128 a[208:211], v233 offset:2048\n" \
"v_cvt_pk_bf16_f32 v38, v44, v45\n" \
"v_mfma_f32_16x16x32_bf16 v[120:123], a[236:239], a[188:191], 0\n" \
"v_exp_f32 v46, v46\n" \
"v_mfma_f32_16x16x32_bf16 v[120:123], a[232:235], a[184:187], v[120:123]\n" \
"buffer_load_dwordx4 v234, s[40:43], s71 offen offset:3072 sc1 lds\n" \
"v_mfma_f32_16x16x32_bf16 v[120:123], a[228:231], a[180:183], v[120:123]\n" \
"v_exp_f32 v47, v47\n" \
"v_mfma_f32_16x16x32_bf16 v[120:123], a[224:227], a[176:179], v[120:123]\n" \
"ds_read_b128 a[236:239], v233 offset:13312\n" \
"v_cvt_pk_bf16_f32 v39, v46, v47\n" \
"v_mfma_f32_16x16x32_bf16 v[124:127], a[240:243], a[176:179], 0\n" \
"v_exp_f32 v56, v56\n" \
"v_mfma_f32_16x16x32_bf16 v[124:127], a[244:247], a[180:183], v[124:127]\n" \
"v_mfma_f32_16x16x32_bf16 v[124:127], a[248:251], a[184:187], v[124:127]\n" \
"v_exp_f32 v57, v57\n" \
"v_mfma_f32_16x16x32_bf16 v[124:127], a[252:255], a[188:191], v[124:127]\n" \
"ds_read_b128 a[232:235], v233 offset:5120\n" \
"v_cvt_pk_bf16_f32 v52, v56, v57\n" \
"v_mfma_f32_16x16x32_bf16 v[108:111], a[252:255], a[172:175], 0\n" \
"v_exp_f32 v58, v58\n" \
"v_mfma_f32_16x16x32_bf16 v[108:111], a[248:251], a[168:171], v[108:111]\n" \
"v_mfma_f32_16x16x32_bf16 v[108:111], a[244:247], a[164:167], v[108:111]\n" \
"v_exp_f32 v59, v59\n" \
"v_mfma_f32_16x16x32_bf16 v[108:111], a[240:243], a[160:163], v[108:111]\n" \
"ds_read_b128 a[228:231], v233 offset:12288\n" \
"v_cvt_pk_bf16_f32 v53, v58, v59\n" \
"v_mfma_f32_16x16x32_bf16 v[92:95], a[240:243], a[144:147], 0\n" \
"v_exp_f32 v60, v60\n" \
"v_mfma_f32_16x16x32_bf16 v[92:95], a[244:247], a[148:151], v[92:95]\n" \
"v_mfma_f32_16x16x32_bf16 v[92:95], a[248:251], a[152:155], v[92:95]\n" \
"v_exp_f32 v61, v61\n" \
"v_mfma_f32_16x16x32_bf16 v[92:95], a[252:255], a[156:159], v[92:95]\n" \
"ds_read_b128 a[224:227], v233 offset:4096\n" \
"v_cvt_pk_bf16_f32 v54, v60, v61\n" \
"v_mfma_f32_16x16x32_bf16 v[76:79], a[252:255], a[140:143], 0\n" \
"v_exp_f32 v62, v62\n" \
"v_mfma_f32_16x16x32_bf16 v[76:79], a[248:251], a[136:139], v[76:79]\n" \
"v_mfma_f32_16x16x32_bf16 v[76:79], a[244:247], a[132:135], v[76:79]\n" \
"s_waitcnt lgkmcnt(2)\n" \
"v_exp_f32 v63, v63\n" \
"v_mfma_f32_16x16x32_bf16 v[76:79], a[240:243], a[128:131], v[76:79]\n" \
"ds_read_b128 a[44:47], v233 offset:15360\n" \
"v_cvt_pk_bf16_f32 v55, v62, v63\n" \
"v_mfma_f32_16x16x32_bf16 v[128:131], a[192:195], v[0:3], v[128:131]\n" \
"s_add_u32 m0, s74, s79\n" \
"v_exp_f32 v64, v64\n" \
"v_mfma_f32_16x16x32_bf16 v[128:131], a[196:199], v[4:7], v[128:131]\n" \
"buffer_load_dwordx4 v234, s[36:39], s73 offen offset:0 sc1 lds\n" \
"v_mfma_f32_16x16x32_bf16 v[160:163], a[192:195], v[16:19], v[160:163]\n" \
"v_exp_f32 v65, v65\n" \
"v_mfma_f32_16x16x32_bf16 v[160:163], a[196:199], v[20:23], v[160:163]\n" \
"ds_read_b128 a[240:243], v232 offset:8448\n" \
"v_cvt_pk_bf16_f32 v64, v64, v65\n" \
"v_mfma_f32_16x16x32_bf16 a[64:67], a[192:195], v[32:35], a[64:67]\n" \
"s_add_u32 s71, s73, 17408\n" \
"v_exp_f32 v66, v66\n" \
"v_mfma_f32_16x16x32_bf16 a[64:67], a[196:199], v[36:39], a[64:67]\n" \
"buffer_load_dwordx4 v234, s[36:39], s73 offen offset:1024 sc1 lds\n" \
"v_mfma_f32_16x16x32_bf16 a[96:99], a[192:195], v[48:51], a[96:99]\n" \
"v_exp_f32 v67, v67\n" \
"v_mfma_f32_16x16x32_bf16 a[96:99], a[196:199], v[52:55], a[96:99]\n" \
"ds_read_b128 a[192:195], v232 offset:0\n" \
"v_cvt_pk_bf16_f32 v65, v66, v67\n" \
"v_mfma_f32_16x16x32_bf16 a[100:103], a[200:203], v[48:51], a[100:103]\n" \
"v_exp_f32 v68, v68\n" \
"v_mfma_f32_16x16x32_bf16 a[100:103], a[204:207], v[52:55], a[100:103]\n" \
"v_xor_b32 v233, 0x4000, v233\n" \
"v_mfma_f32_16x16x32_bf16 a[68:71], a[200:203], v[32:35], a[68:71]\n" \
"v_exp_f32 v69, v69\n" \
"v_mfma_f32_16x16x32_bf16 a[68:71], a[204:207], v[36:39], a[68:71]\n" \
"ds_read_b128 a[196:199], v232 offset:2048\n" \
"v_cvt_pk_bf16_f32 v66, v68, v69\n" \
"v_smfmac_f32_16x16x64_bf16 a[48:51], v[244:247], v[0:7], v243\n" \
"buffer_load_dwordx4 v234, s[36:39], s73 offen offset:2048 sc1 lds\n" \
"v_mfma_f32_16x16x32_bf16 v[164:167], a[200:203], v[16:19], v[164:167]\n" \
"v_exp_f32 v70, v70\n" \
"v_mfma_f32_16x16x32_bf16 v[164:167], a[204:207], v[20:23], v[164:167]\n" \
"v_mfma_f32_16x16x32_bf16 v[132:135], a[200:203], v[0:3], v[132:135]\n" \
"v_exp_f32 v71, v71\n" \
"v_mfma_f32_16x16x32_bf16 v[132:135], a[204:207], v[4:7], v[132:135]\n" \
"ds_read_b128 a[200:203], v232 offset:4096\n" \
"Lpg6093_%=:\n" \
"s_cbranch_execz Lpg6953_%=\n" \
"v_cvt_pk_bf16_f32 v67, v70, v71\n" \
"v_mfma_f32_16x16x32_bf16 v[136:139], a[208:211], v[0:3], v[136:139]\n" \
"v_exp_f32 v80, v80\n" \
"v_mfma_f32_16x16x32_bf16 v[136:139], a[212:215], v[4:7], v[136:139]\n" \
"buffer_load_dwordx4 v234, s[36:39], s73 offen offset:3072 sc1 lds\n" \
"v_mfma_f32_16x16x32_bf16 v[168:171], a[208:211], v[16:19], v[168:171]\n" \
"v_exp_f32 v81, v81\n" \
"v_mfma_f32_16x16x32_bf16 v[168:171], a[212:215], v[20:23], v[168:171]\n" \
"ds_read_b128 a[204:207], v232 offset:6144\n" \
"v_cvt_pk_bf16_f32 v80, v80, v81\n" \
"v_mfma_f32_16x16x32_bf16 a[72:75], a[208:211], v[32:35], a[72:75]\n" \
"v_exp_f32 v82, v82\n" \
"v_mfma_f32_16x16x32_bf16 a[72:75], a[212:215], v[36:39], a[72:75]\n" \
"v_mfma_f32_16x16x32_bf16 a[104:107], a[208:211], v[48:51], a[104:107]\n" \
"v_exp_f32 v83, v83\n" \
"v_mfma_f32_16x16x32_bf16 a[104:107], a[212:215], v[52:55], a[104:107]\n" \
"ds_read_b128 a[208:211], v232 offset:256\n" \
"v_cvt_pk_bf16_f32 v81, v82, v83\n" \
"v_mfma_f32_16x16x32_bf16 a[108:111], a[216:219], v[48:51], a[108:111]\n" \
"s_add_u32 m0, m0, 0x4000\n" \
"v_exp_f32 v84, v84\n" \
"v_mfma_f32_16x16x32_bf16 a[108:111], a[220:223], v[52:55], a[108:111]\n" \
"buffer_load_dwordx4 v234, s[36:39], s71 offen offset:0 sc1 lds\n" \
"v_mfma_f32_16x16x32_bf16 a[76:79], a[216:219], v[32:35], a[76:79]\n" \
"v_exp_f32 v85, v85\n" \
"v_mfma_f32_16x16x32_bf16 a[76:79], a[220:223], v[36:39], a[76:79]\n" \
"ds_read_b128 a[212:215], v232 offset:2304\n" \
"v_cvt_pk_bf16_f32 v82, v84, v85\n" \
"v_smfmac_f32_16x16x64_bf16 a[52:55], v[244:247], v[16:23], v243\n" \
"v_mfma_f32_16x16x32_bf16 v[172:175], a[216:219], v[16:19], v[172:175]\n" \
"v_exp_f32 v86, v86\n" \
"v_mfma_f32_16x16x32_bf16 v[172:175], a[220:223], v[20:23], v[172:175]\n" \
"buffer_load_dwordx4 v234, s[36:39], s71 offen offset:1024 sc1 lds\n" \
"v_mfma_f32_16x16x32_bf16 v[140:143], a[216:219], v[0:3], v[140:143]\n" \
"s_waitcnt lgkmcnt(7)\n" \
"v_exp_f32 v87, v87\n" \
"v_mfma_f32_16x16x32_bf16 v[140:143], a[220:223], v[4:7], v[140:143]\n" \
"ds_read_b128 a[216:219], v232 offset:4352\n" \
"v_cvt_pk_bf16_f32 v83, v86, v87\n" \
"v_mfma_f32_16x16x32_bf16 v[144:147], a[224:227], v[0:3], v[144:147]\n" \
"v_exp_f32 v96, v96\n" \
"v_mfma_f32_16x16x32_bf16 v[144:147], a[228:231], v[4:7], v[144:147]\n" \
"buffer_load_dwordx4 v234, s[36:39], s71 offen offset:2048 sc1 lds\n" \
"v_mfma_f32_16x16x32_bf16 v[176:179], a[224:227], v[16:19], v[176:179]\n" \
"v_exp_f32 v97, v97\n" \
"v_mfma_f32_16x16x32_bf16 v[176:179], a[228:231], v[20:23], v[176:179]\n" \
"ds_read_b128 a[220:223], v232 offset:6400\n" \
"v_cvt_pk_bf16_f32 v96, v96, v97\n" \
"v_mfma_f32_16x16x32_bf16 a[80:83], a[224:227], v[32:35], a[80:83]\n" \
"v_exp_f32 v98, v98\n" \
"v_mfma_f32_16x16x32_bf16 a[80:83], a[228:231], v[36:39], a[80:83]\n" \
"v_mfma_f32_16x16x32_bf16 a[112:115], a[224:227], v[48:51], a[112:115]\n" \
"v_exp_f32 v99, v99\n" \
"v_mfma_f32_16x16x32_bf16 a[112:115], a[228:231], v[52:55], a[112:115]\n" \
"ds_read_b128 a[224:227], v232 offset:8192\n" \
"v_cvt_pk_bf16_f32 v97, v98, v99\n" \
"v_mfma_f32_16x16x32_bf16 a[116:119], a[232:235], v[48:51], a[116:119]\n" \
"v_exp_f32 v100, v100\n" \
"v_mfma_f32_16x16x32_bf16 a[116:119], a[236:239], v[52:55], a[116:119]\n" \
"buffer_load_dwordx4 v234, s[36:39], s71 offen offset:3072 sc1 lds\n" \
"v_mfma_f32_16x16x32_bf16 a[84:87], a[232:235], v[32:35], a[84:87]\n" \
"v_exp_f32 v101, v101\n" \
"v_mfma_f32_16x16x32_bf16 a[84:87], a[236:239], v[36:39], a[84:87]\n" \
"ds_read_b128 a[228:231], v232 offset:10240\n" \
"v_cvt_pk_bf16_f32 v98, v100, v101\n" \
"v_smfmac_f32_16x16x64_bf16 v[224:227], v[244:247], v[32:39], v243\n" \
"v_mfma_f32_16x16x32_bf16 v[180:183], a[232:235], v[16:19], v[180:183]\n" \
"v_exp_f32 v102, v102\n" \
"v_mfma_f32_16x16x32_bf16 v[180:183], a[236:239], v[20:23], v[180:183]\n" \
"buffer_load_dword v239, v242, s[56:59], s95 offen sc1\n" \
"v_mfma_f32_16x16x32_bf16 v[148:151], a[232:235], v[0:3], v[148:151]\n" \
"s_mov_b32 s69, s92\n" \
"v_exp_f32 v103, v103\n" \
"v_mfma_f32_16x16x32_bf16 v[148:151], a[236:239], v[4:7], v[148:151]\n" \
"s_mov_b32 s76, s73\n" \
"ds_read_b128 a[232:235], v232 offset:12288\n" \
"v_cvt_pk_bf16_f32 v99, v102, v103\n" \
"v_mfma_f32_16x16x32_bf16 v[152:155], a[32:35], v[0:3], v[152:155]\n" \
"s_add_u32 s73, s73, 34816\n" \
"v_exp_f32 v112, v112\n" \
"v_mfma_f32_16x16x32_bf16 v[152:155], a[36:39], v[4:7], v[152:155]\n" \
"s_cmp_eq_u32 s73, s83\n" \
"s_cselect_b32 s70, 34816, 0\n" \
"s_add_u32 s73, s73, s70\n" \
"v_mfma_f32_16x16x32_bf16 v[184:187], a[32:35], v[16:19], v[184:187]\n" \
"s_xor_b32 s74, s74, 0x8000\n" \
"v_exp_f32 v113, v113\n" \
"v_mfma_f32_16x16x32_bf16 v[184:187], a[36:39], v[20:23], v[184:187]\n" \
"s_xor_b32 s77, s77, 0x8000\n" \
"ds_read_b128 a[236:239], v232 offset:14336\n" \
"v_cvt_pk_bf16_f32 v112, v112, v113\n" \
"v_mfma_f32_16x16x32_bf16 a[88:91], a[32:35], v[32:35], a[88:91]\n" \
"v_exp_f32 v114, v114\n" \
"v_mfma_f32_16x16x32_bf16 a[88:91], a[36:39], v[36:39], a[88:91]\n" \
"v_mfma_f32_16x16x32_bf16 a[120:123], a[32:35], v[48:51], a[120:123]\n" \
"v_exp_f32 v115, v115\n" \
"v_mfma_f32_16x16x32_bf16 a[120:123], a[36:39], v[52:55], a[120:123]\n" \
"ds_read_b128 a[244:247], v232 offset:10496\n" \
"v_cvt_pk_bf16_f32 v113, v114, v115\n" \
"v_mfma_f32_16x16x32_bf16 a[124:127], a[40:43], v[48:51], a[124:127]\n" \
"v_exp_f32 v116, v116\n" \
"v_mfma_f32_16x16x32_bf16 a[124:127], a[44:47], v[52:55], a[124:127]\n" \
"v_mfma_f32_16x16x32_bf16 a[92:95], a[40:43], v[32:35], a[92:95]\n" \
"v_exp_f32 v117, v117\n" \
"v_mfma_f32_16x16x32_bf16 a[92:95], a[44:47], v[36:39], a[92:95]\n" \
"ds_read_b128 a[248:251], v232 offset:12544\n" \
"v_cvt_pk_bf16_f32 v114, v116, v117\n" \
"v_smfmac_f32_16x16x64_bf16 v[228:231], v[244:247], v[48:55], v243\n" \
"v_mfma_f32_16x16x32_bf16 v[188:191], a[40:43], v[16:19], v[188:191]\n" \
"v_exp_f32 v118, v118\n" \
"v_mfma_f32_16x16x32_bf16 v[188:191], a[44:47], v[20:23], v[188:191]\n" \
"s_cmp_lg_u32 s89, 0\n" \
"v_exp_f32 v119, v119\n" \
"v_mfma_f32_16x16x32_bf16 v[156:159], a[40:43], v[0:3], v[156:159]\n" \
"s_waitcnt lgkmcnt(2)\n" \
"ds_read_b128 a[252:255], v232 offset:14592\n" \
"v_cvt_pk_bf16_f32 v115, v118, v119\n" \
"v_mfma_f32_16x16x32_bf16 v[156:159], a[44:47], v[4:7], v[156:159]\n" \
"s_cbranch_scc1 Lloop_%=\n" \
"s_cmp_eq_u32 s63, 7\n" \
"s_cbranch_scc0 Lloop2_%=\n" \
"s_branch Lg_%=\n" \
"Lep_%=:\n" \
"Lpg6217_%=:\n" \
"s_cbranch_execz Lpg7293_%=\n" \
"s_nop 4\n" \
"v_accvgpr_read_b32 v92, a48\n" \
"v_accvgpr_read_b32 v93, a49\n" \
"v_accvgpr_read_b32 v94, a52\n" \
"v_accvgpr_read_b32 v95, a53\n" \
"v_accvgpr_write_b32 a48, 0\n" \
"v_accvgpr_write_b32 a49, 0\n" \
"v_accvgpr_write_b32 a52, 0\n" \
"v_accvgpr_write_b32 a53, 0\n" \
"v_add_f32 v86, v92, v93\n" \
"v_add_f32 v87, v94, v95\n" \
"v_rcp_f32 v84, v86\n" \
"v_rcp_f32 v85, v87\n" \
"v_add_u32 v240, 0x1000, v236\n" \
"v_add_f32 v80, v86, v87\n" \
"v_mul_f32 v80, 0x43800000, v80\n" \
"v_add_f32 v86, v84, v85\n" \
"v_fmac_f32 v80, 0x5f800000, v86\n" \
"v_mov_b32 v81, 0\n" \
"v_mov_b32 v82, 0\n" \
"v_mov_b32 v83, 0\n" \
"v_mov_b32 v88, 0\n" \
"v_mov_b32 v89, 0\n" \
"v_mov_b32 v90, 0\n" \
"v_mov_b32 v91, 0\n" \
"v_pk_mul_f32 v[128:129], v[128:129], v[84:85] op_sel:[0,0] op_sel_hi:[1,0]\n" \
"v_pk_mul_f32 v[130:131], v[130:131], v[84:85] op_sel:[0,0] op_sel_hi:[1,0]\n" \
"v_pk_mul_f32 v[132:133], v[132:133], v[84:85] op_sel:[0,0] op_sel_hi:[1,0]\n" \
"v_pk_mul_f32 v[134:135], v[134:135], v[84:85] op_sel:[0,0] op_sel_hi:[1,0]\n" \
"v_cvt_pk_bf16_f32 v64, v128, v129\n" \
"v_cvt_pk_bf16_f32 v65, v130, v131\n" \
"v_cvt_pk_bf16_f32 v66, v132, v133\n" \
"v_cvt_pk_bf16_f32 v67, v134, v135\n" \
"buffer_store_dwordx4 v[64:67], v236, s[56:59], s87 offen offset:0 sc1\n" \
"v_pk_mul_f32 v[136:137], v[136:137], v[84:85] op_sel:[0,0] op_sel_hi:[1,0]\n" \
"v_pk_mul_f32 v[138:139], v[138:139], v[84:85] op_sel:[0,0] op_sel_hi:[1,0]\n" \
"v_pk_mul_f32 v[140:141], v[140:141], v[84:85] op_sel:[0,0] op_sel_hi:[1,0]\n" \
"v_pk_mul_f32 v[142:143], v[142:143], v[84:85] op_sel:[0,0] op_sel_hi:[1,0]\n" \
"v_cvt_pk_bf16_f32 v68, v136, v137\n" \
"v_cvt_pk_bf16_f32 v69, v138, v139\n" \
"v_cvt_pk_bf16_f32 v70, v140, v141\n" \
"v_cvt_pk_bf16_f32 v71, v142, v143\n" \
"buffer_store_dwordx4 v[68:71], v236, s[56:59], s87 offen offset:2048 sc1\n" \
"v_pk_mul_f32 v[144:145], v[144:145], v[84:85] op_sel:[0,0] op_sel_hi:[1,0]\n" \
"v_pk_mul_f32 v[146:147], v[146:147], v[84:85] op_sel:[0,0] op_sel_hi:[1,0]\n" \
"v_pk_mul_f32 v[148:149], v[148:149], v[84:85] op_sel:[0,0] op_sel_hi:[1,0]\n" \
"v_pk_mul_f32 v[150:151], v[150:151], v[84:85] op_sel:[0,0] op_sel_hi:[1,0]\n" \
"v_cvt_pk_bf16_f32 v72, v144, v145\n" \
"v_cvt_pk_bf16_f32 v73, v146, v147\n" \
"v_cvt_pk_bf16_f32 v74, v148, v149\n" \
"v_cvt_pk_bf16_f32 v75, v150, v151\n" \
"buffer_store_dwordx4 v[72:75], v240, s[56:59], s87 offen offset:0 sc1\n" \
"v_pk_mul_f32 v[152:153], v[152:153], v[84:85] op_sel:[0,0] op_sel_hi:[1,0]\n" \
"v_pk_mul_f32 v[154:155], v[154:155], v[84:85] op_sel:[0,0] op_sel_hi:[1,0]\n" \
"v_pk_mul_f32 v[156:157], v[156:157], v[84:85] op_sel:[0,0] op_sel_hi:[1,0]\n" \
"v_pk_mul_f32 v[158:159], v[158:159], v[84:85] op_sel:[0,0] op_sel_hi:[1,0]\n" \
"v_cvt_pk_bf16_f32 v76, v152, v153\n" \
"v_cvt_pk_bf16_f32 v77, v154, v155\n" \
"v_cvt_pk_bf16_f32 v78, v156, v157\n" \
"v_cvt_pk_bf16_f32 v79, v158, v159\n" \
"buffer_store_dwordx4 v[76:79], v240, s[56:59], s87 offen offset:2048 sc1\n" \
"v_smfmac_f32_16x16x64_bf16 v[80:83], v[244:247], v[64:71], v243\n" \
"v_smfmac_f32_16x16x64_bf16 v[80:83], v[244:247], v[72:79], v243\n" \
"v_mfma_f32_32x32x16_bf16 v[128:143], v[88:91], v[88:91], 0\n" \
"v_mfma_f32_32x32x16_bf16 v[144:159], v[88:91], v[88:91], 0\n" \
"s_mov_b64 exec, 0\n" \
"s_cmp_eq_u32 s61, 0\n" \
"s_cbranch_scc1 Lst1x0_%=\n" \
"s_cmp_eq_u32 s61, 1\n" \
"s_cbranch_scc1 Lst1x1_%=\n" \
"s_cmp_eq_u32 s61, 2\n" \
"s_cbranch_scc1 Lst1x2_%=\n" \
"s_branch Lst1x3_%=\n" \
"Lcb1_%=:\n" \
"s_mov_b64 exec, -1\n" \
"s_add_u32 s72, s87, 532480\n" \
"v_pk_mul_f32 v[160:161], v[160:161], v[84:85] op_sel:[0,1] op_sel_hi:[1,1]\n" \
"v_pk_mul_f32 v[162:163], v[162:163], v[84:85] op_sel:[0,1] op_sel_hi:[1,1]\n" \
"v_pk_mul_f32 v[164:165], v[164:165], v[84:85] op_sel:[0,1] op_sel_hi:[1,1]\n" \
"v_pk_mul_f32 v[166:167], v[166:167], v[84:85] op_sel:[0,1] op_sel_hi:[1,1]\n" \
"v_cvt_pk_bf16_f32 v64, v160, v161\n" \
"v_cvt_pk_bf16_f32 v65, v162, v163\n" \
"v_cvt_pk_bf16_f32 v66, v164, v165\n" \
"v_cvt_pk_bf16_f32 v67, v166, v167\n" \
"buffer_store_dwordx4 v[64:67], v236, s[56:59], s72 offen offset:0 sc1\n" \
"v_pk_mul_f32 v[168:169], v[168:169], v[84:85] op_sel:[0,1] op_sel_hi:[1,1]\n" \
"v_pk_mul_f32 v[170:171], v[170:171], v[84:85] op_sel:[0,1] op_sel_hi:[1,1]\n" \
"v_pk_mul_f32 v[172:173], v[172:173], v[84:85] op_sel:[0,1] op_sel_hi:[1,1]\n" \
"v_pk_mul_f32 v[174:175], v[174:175], v[84:85] op_sel:[0,1] op_sel_hi:[1,1]\n" \
"v_cvt_pk_bf16_f32 v68, v168, v169\n" \
"v_cvt_pk_bf16_f32 v69, v170, v171\n" \
"v_cvt_pk_bf16_f32 v70, v172, v173\n" \
"v_cvt_pk_bf16_f32 v71, v174, v175\n" \
"buffer_store_dwordx4 v[68:71], v236, s[56:59], s72 offen offset:2048 sc1\n" \
"v_pk_mul_f32 v[176:177], v[176:177], v[84:85] op_sel:[0,1] op_sel_hi:[1,1]\n" \
"v_pk_mul_f32 v[178:179], v[178:179], v[84:85] op_sel:[0,1] op_sel_hi:[1,1]\n" \
"v_pk_mul_f32 v[180:181], v[180:181], v[84:85] op_sel:[0,1] op_sel_hi:[1,1]\n" \
"v_pk_mul_f32 v[182:183], v[182:183], v[84:85] op_sel:[0,1] op_sel_hi:[1,1]\n" \
"v_cvt_pk_bf16_f32 v72, v176, v177\n" \
"v_cvt_pk_bf16_f32 v73, v178, v179\n" \
"v_cvt_pk_bf16_f32 v74, v180, v181\n" \
"v_cvt_pk_bf16_f32 v75, v182, v183\n" \
"buffer_store_dwordx4 v[72:75], v240, s[56:59], s72 offen offset:0 sc1\n" \
"v_pk_mul_f32 v[184:185], v[184:185], v[84:85] op_sel:[0,1] op_sel_hi:[1,1]\n" \
"v_pk_mul_f32 v[186:187], v[186:187], v[84:85] op_sel:[0,1] op_sel_hi:[1,1]\n" \
"v_pk_mul_f32 v[188:189], v[188:189], v[84:85] op_sel:[0,1] op_sel_hi:[1,1]\n" \
"v_pk_mul_f32 v[190:191], v[190:191], v[84:85] op_sel:[0,1] op_sel_hi:[1,1]\n" \
"v_cvt_pk_bf16_f32 v76, v184, v185\n" \
"v_cvt_pk_bf16_f32 v77, v186, v187\n" \
"v_cvt_pk_bf16_f32 v78, v188, v189\n" \
"v_cvt_pk_bf16_f32 v79, v190, v191\n" \
"buffer_store_dwordx4 v[76:79], v240, s[56:59], s72 offen offset:2048 sc1\n" \
"v_smfmac_f32_16x16x64_bf16 v[80:83], v[244:247], v[64:71], v243\n" \
"v_smfmac_f32_16x16x64_bf16 v[80:83], v[244:247], v[72:79], v243\n" \
"v_mfma_f32_32x32x16_bf16 v[160:175], v[88:91], v[88:91], 0\n" \
"v_mfma_f32_32x32x16_bf16 v[176:191], v[88:91], v[88:91], 0\n" \
"s_nop 5\n" \
"v_add_f32 v80, v80, v81\n" \
"s_movk_i32 s64, 0x207\n" \
"v_cmp_class_f32 vcc, v80, s64\n" \
"s_nop 1\n" \
"s_cmp_lg_u64 vcc, 0\n" \
"s_addc_u32 s90, s90, 0\n" \
"s_waitcnt vmcnt(8)\n" \
"s_cmp_eq_u32 s63, 7\n" \
"s_cbranch_scc0 Lepr_%=\n" \
"s_branch Lepg_%=\n" \
"Lloop2_%=:\n" \
"Lpg6346_%=:\n" \
"s_cbranch_execz Lcb0_%=\n" \
"v_mfma_f32_16x16x32_bf16 v[0:3], a[192:195], a[0:3], 0\n" \
"v_exp_f32 v72, v72\n" \
"v_mfma_f32_16x16x32_bf16 v[0:3], a[196:199], a[4:7], v[0:3]\n" \
"ds_read_b128 a[44:47], v233 offset:15360\n" \
"v_mfma_f32_16x16x32_bf16 v[0:3], a[200:203], a[8:11], v[0:3]\n" \
"v_exp_f32 v73, v73\n" \
"v_mfma_f32_16x16x32_bf16 v[0:3], a[204:207], a[12:15], v[0:3]\n" \
"ds_read_b128 a[32:35], v233 offset:6144\n" \
"v_cvt_pk_bf16_f32 v68, v72, v73\n" \
"v_mfma_f32_16x16x32_bf16 v[16:19], a[204:207], a[28:31], 0\n" \
"v_exp_f32 v74, v74\n" \
"v_mfma_f32_16x16x32_bf16 v[16:19], a[200:203], a[24:27], v[16:19]\n" \
"v_xor_b32 v232, 0x8000, v232\n" \
"v_mfma_f32_16x16x32_bf16 v[16:19], a[196:199], a[20:23], v[16:19]\n" \
"v_exp_f32 v75, v75\n" \
"v_mfma_f32_16x16x32_bf16 v[16:19], a[192:195], a[16:19], v[16:19]\n" \
"ds_read_b128 a[36:39], v233 offset:14336\n" \
"v_cvt_pk_bf16_f32 v69, v74, v75\n" \
"v_mfma_f32_16x16x32_bf16 v[32:35], a[192:195], a[160:163], 0\n" \
"v_exp_f32 v76, v76\n" \
"v_mfma_f32_16x16x32_bf16 v[32:35], a[196:199], a[164:167], v[32:35]\n" \
"v_mfma_f32_16x16x32_bf16 v[32:35], a[200:203], a[168:171], v[32:35]\n" \
"v_exp_f32 v77, v77\n" \
"v_mfma_f32_16x16x32_bf16 v[32:35], a[204:207], a[172:175], v[32:35]\n" \
"ds_read_b128 a[40:43], v233 offset:7168\n" \
"v_cvt_pk_bf16_f32 v70, v76, v77\n" \
"v_mfma_f32_16x16x32_bf16 v[48:51], a[204:207], a[188:191], 0\n" \
"v_exp_f32 v78, v78\n" \
"v_mfma_f32_16x16x32_bf16 v[48:51], a[200:203], a[184:187], v[48:51]\n" \
"v_mfma_f32_16x16x32_bf16 v[48:51], a[196:199], a[180:183], v[48:51]\n" \
"v_exp_f32 v79, v79\n" \
"v_mfma_f32_16x16x32_bf16 v[48:51], a[192:195], a[176:179], v[48:51]\n" \
"ds_read_b128 a[204:207], v233 offset:9216\n" \
"v_cvt_pk_bf16_f32 v71, v78, v79\n" \
"v_mfma_f32_16x16x32_bf16 v[52:55], a[208:211], a[176:179], 0\n" \
"v_exp_f32 v88, v88\n" \
"v_mfma_f32_16x16x32_bf16 v[52:55], a[212:215], a[180:183], v[52:55]\n" \
"v_mfma_f32_16x16x32_bf16 v[52:55], a[216:219], a[184:187], v[52:55]\n" \
"v_exp_f32 v89, v89\n" \
"v_mfma_f32_16x16x32_bf16 v[52:55], a[220:223], a[188:191], v[52:55]\n" \
"ds_read_b128 a[200:203], v233 offset:1024\n" \
"v_cvt_pk_bf16_f32 v84, v88, v89\n" \
"v_mfma_f32_16x16x32_bf16 v[36:39], a[220:223], a[172:175], 0\n" \
"v_exp_f32 v90, v90\n" \
"v_mfma_f32_16x16x32_bf16 v[36:39], a[216:219], a[168:171], v[36:39]\n" \
"v_mfma_f32_16x16x32_bf16 v[36:39], a[212:215], a[164:167], v[36:39]\n" \
"v_exp_f32 v91, v91\n" \
"v_mfma_f32_16x16x32_bf16 v[36:39], a[208:211], a[160:163], v[36:39]\n" \
"ds_read_b128 a[196:199], v233 offset:8192\n" \
"v_cvt_pk_bf16_f32 v85, v90, v91\n" \
"v_mfma_f32_16x16x32_bf16 v[20:23], a[208:211], a[16:19], 0\n" \
"v_exp_f32 v92, v92\n" \
"v_mfma_f32_16x16x32_bf16 v[20:23], a[212:215], a[20:23], v[20:23]\n" \
"v_mfma_f32_16x16x32_bf16 v[20:23], a[216:219], a[24:27], v[20:23]\n" \
"v_exp_f32 v93, v93\n" \
"v_mfma_f32_16x16x32_bf16 v[20:23], a[220:223], a[28:31], v[20:23]\n" \
"ds_read_b128 a[192:195], v233 offset:0\n" \
"v_cvt_pk_bf16_f32 v86, v92, v93\n" \
"v_mfma_f32_16x16x32_bf16 v[4:7], a[220:223], a[12:15], 0\n" \
"v_exp_f32 v94, v94\n" \
"v_mfma_f32_16x16x32_bf16 v[4:7], a[216:219], a[8:11], v[4:7]\n" \
"v_mfma_f32_16x16x32_bf16 v[4:7], a[212:215], a[4:7], v[4:7]\n" \
"s_waitcnt lgkmcnt(8)\n" \
"v_exp_f32 v95, v95\n" \
"v_mfma_f32_16x16x32_bf16 v[4:7], a[208:211], a[0:3], v[4:7]\n" \
"ds_read_b128 a[220:223], v233 offset:11264\n" \
"v_cvt_pk_bf16_f32 v87, v94, v95\n" \
"v_mfma_f32_16x16x32_bf16 v[8:11], a[224:227], a[0:3], 0\n" \
"v_exp_f32 v104, v104\n" \
"v_mfma_f32_16x16x32_bf16 v[8:11], a[228:231], a[4:7], v[8:11]\n" \
"v_mfma_f32_16x16x32_bf16 v[8:11], a[232:235], a[8:11], v[8:11]\n" \
"v_exp_f32 v105, v105\n" \
"v_mfma_f32_16x16x32_bf16 v[8:11], a[236:239], a[12:15], v[8:11]\n" \
"ds_read_b128 a[216:219], v233 offset:3072\n" \
"v_cvt_pk_bf16_f32 v100, v104, v105\n" \
"v_mfma_f32_16x16x32_bf16 v[24:27], a[236:239], a[28:31], 0\n" \
"v_exp_f32 v106, v106\n" \
"v_mfma_f32_16x16x32_bf16 v[24:27], a[232:235], a[24:27], v[24:27]\n" \
"v_mfma_f32_16x16x32_bf16 v[24:27], a[228:231], a[20:23], v[24:27]\n" \
"v_exp_f32 v107, v107\n" \
"v_mfma_f32_16x16x32_bf16 v[24:27], a[224:227], a[16:19], v[24:27]\n" \
"ds_read_b128 a[212:215], v233 offset:10240\n" \
"v_cvt_pk_bf16_f32 v101, v106, v107\n" \
"v_mfma_f32_16x16x32_bf16 v[40:43], a[224:227], a[160:163], 0\n" \
"v_exp_f32 v108, v108\n" \
"v_mfma_f32_16x16x32_bf16 v[40:43], a[228:231], a[164:167], v[40:43]\n" \
"v_mfma_f32_16x16x32_bf16 v[40:43], a[232:235], a[168:171], v[40:43]\n" \
"v_exp_f32 v109, v109\n" \
"v_mfma_f32_16x16x32_bf16 v[40:43], a[236:239], a[172:175], v[40:43]\n" \
"ds_read_b128 a[208:211], v233 offset:2048\n" \
"v_cvt_pk_bf16_f32 v102, v108, v109\n" \
"v_mfma_f32_16x16x32_bf16 v[56:59], a[236:239], a[188:191], 0\n" \
"v_exp_f32 v110, v110\n" \
"v_mfma_f32_16x16x32_bf16 v[56:59], a[232:235], a[184:187], v[56:59]\n" \
"v_mfma_f32_16x16x32_bf16 v[56:59], a[228:231], a[180:183], v[56:59]\n" \
"v_exp_f32 v111, v111\n" \
"v_mfma_f32_16x16x32_bf16 v[56:59], a[224:227], a[176:179], v[56:59]\n" \
"ds_read_b128 a[236:239], v233 offset:13312\n" \
"v_cvt_pk_bf16_f32 v103, v110, v111\n" \
"v_mfma_f32_16x16x32_bf16 v[60:63], a[240:243], a[176:179], 0\n" \
"v_exp_f32 v120, v120\n" \
"v_mfma_f32_16x16x32_bf16 v[60:63], a[244:247], a[180:183], v[60:63]\n" \
"ds_read_b128 a[232:235], v233 offset:5120\n" \
"v_mfma_f32_16x16x32_bf16 v[60:63], a[248:251], a[184:187], v[60:63]\n" \
"v_exp_f32 v121, v121\n" \
"v_mfma_f32_16x16x32_bf16 v[60:63], a[252:255], a[188:191], v[60:63]\n" \
"ds_read_b128 a[228:231], v233 offset:12288\n" \
"v_cvt_pk_bf16_f32 v116, v120, v121\n" \
"v_mfma_f32_16x16x32_bf16 v[44:47], a[252:255], a[172:175], 0\n" \
"v_exp_f32 v122, v122\n" \
"v_mfma_f32_16x16x32_bf16 v[44:47], a[248:251], a[168:171], v[44:47]\n" \
"ds_read_b128 a[224:227], v233 offset:4096\n" \
"v_mfma_f32_16x16x32_bf16 v[44:47], a[244:247], a[164:167], v[44:47]\n" \
"v_exp_f32 v123, v123\n" \
"v_mfma_f32_16x16x32_bf16 v[44:47], a[240:243], a[160:163], v[44:47]\n" \
"v_cvt_pk_bf16_f32 v117, v122, v123\n" \
"v_mfma_f32_16x16x32_bf16 v[28:31], a[240:243], a[16:19], 0\n" \
"v_exp_f32 v124, v124\n" \
"v_mfma_f32_16x16x32_bf16 v[28:31], a[244:247], a[20:23], v[28:31]\n" \
"v_mfma_f32_16x16x32_bf16 v[28:31], a[248:251], a[24:27], v[28:31]\n" \
"v_exp_f32 v125, v125\n" \
"v_mfma_f32_16x16x32_bf16 v[28:31], a[252:255], a[28:31], v[28:31]\n" \
"v_cvt_pk_bf16_f32 v118, v124, v125\n" \
"v_mfma_f32_16x16x32_bf16 v[12:15], a[252:255], a[12:15], 0\n" \
"v_exp_f32 v126, v126\n" \
"v_mfma_f32_16x16x32_bf16 v[12:15], a[248:251], a[8:11], v[12:15]\n" \
"v_mfma_f32_16x16x32_bf16 v[12:15], a[244:247], a[4:7], v[12:15]\n" \
"s_waitcnt vmcnt(0) lgkmcnt(0)\n" \
"v_exp_f32 v127, v127\n" \
"v_mfma_f32_16x16x32_bf16 v[12:15], a[240:243], a[0:3], v[12:15]\n" \
"s_barrier\n" \
"v_cvt_pk_bf16_f32 v119, v126, v127\n" \
"v_mfma_f32_16x16x32_bf16 v[128:131], a[192:195], v[64:67], v[128:131]\n" \
"v_exp_f32 v0, v0\n" \
"v_mfma_f32_16x16x32_bf16 v[128:131], a[196:199], v[68:71], v[128:131]\n" \
"v_cmp_eq_u32 vcc, s60, v239\n" \
"v_mfma_f32_16x16x32_bf16 v[160:163], a[192:195], v[80:83], v[160:163]\n" \
"v_exp_f32 v1, v1\n" \
"v_mfma_f32_16x16x32_bf16 v[160:163], a[196:199], v[84:87], v[160:163]\n" \
"s_andn2_b32 s70, s69, vcc_lo\n" \
"s_cbranch_scc1 Lpl3_%=\n" \
"Lpr3_%=:\n" \
"ds_read_b128 a[240:243], v232 offset:8448\n" \
"v_cvt_pk_bf16_f32 v0, v0, v1\n" \
"v_mfma_f32_16x16x32_bf16 a[64:67], a[192:195], v[96:99], a[64:67]\n" \
"s_add_u32 m0, s74, s79\n" \
"v_exp_f32 v2, v2\n" \
"v_mfma_f32_16x16x32_bf16 a[64:67], a[196:199], v[100:103], a[64:67]\n" \
"buffer_load_dwordx4 v234, s[36:39], s73 offen offset:0 sc1 lds\n" \
"v_mfma_f32_16x16x32_bf16 a[96:99], a[192:195], v[112:115], a[96:99]\n" \
"v_exp_f32 v3, v3\n" \
"v_mfma_f32_16x16x32_bf16 a[96:99], a[196:199], v[116:119], a[96:99]\n" \
"ds_read_b128 a[192:195], v232 offset:0\n" \
"v_cvt_pk_bf16_f32 v1, v2, v3\n" \
"v_mfma_f32_16x16x32_bf16 a[100:103], a[200:203], v[112:115], a[100:103]\n" \
"s_add_u32 s71, s73, 17408\n" \
"v_exp_f32 v4, v4\n" \
"v_mfma_f32_16x16x32_bf16 a[100:103], a[204:207], v[116:119], a[100:103]\n" \
"buffer_load_dwordx4 v234, s[36:39], s73 offen offset:1024 sc1 lds\n" \
"v_mfma_f32_16x16x32_bf16 a[68:71], a[200:203], v[96:99], a[68:71]\n" \
"v_exp_f32 v5, v5\n" \
"v_mfma_f32_16x16x32_bf16 a[68:71], a[204:207], v[100:103], a[68:71]\n" \
"ds_read_b128 a[196:199], v232 offset:2048\n" \
"v_cvt_pk_bf16_f32 v2, v4, v5\n" \
"v_smfmac_f32_16x16x64_bf16 a[48:51], v[244:247], v[64:71], v243\n" \
"buffer_load_dwordx4 v234, s[36:39], s73 offen offset:2048 sc1 lds\n" \
"v_mfma_f32_16x16x32_bf16 v[164:167], a[200:203], v[80:83], v[164:167]\n" \
"v_exp_f32 v6, v6\n" \
"v_mfma_f32_16x16x32_bf16 v[164:167], a[204:207], v[84:87], v[164:167]\n" \
"buffer_load_dwordx4 v234, s[36:39], s73 offen offset:3072 sc1 lds\n" \
"v_mfma_f32_16x16x32_bf16 v[132:135], a[200:203], v[64:67], v[132:135]\n" \
"v_exp_f32 v7, v7\n" \
"v_mfma_f32_16x16x32_bf16 v[132:135], a[204:207], v[68:71], v[132:135]\n" \
"ds_read_b128 a[200:203], v232 offset:4096\n" \
"v_cvt_pk_bf16_f32 v3, v6, v7\n" \
"v_mfma_f32_16x16x32_bf16 v[136:139], a[208:211], v[64:67], v[136:139]\n" \
"s_add_u32 m0, m0, 0x4000\n" \
"v_exp_f32 v16, v16\n" \
"v_mfma_f32_16x16x32_bf16 v[136:139], a[212:215], v[68:71], v[136:139]\n" \
"buffer_load_dwordx4 v234, s[36:39], s71 offen offset:0 sc1 lds\n" \
"v_mfma_f32_16x16x32_bf16 v[168:171], a[208:211], v[80:83], v[168:171]\n" \
"v_exp_f32 v17, v17\n" \
"v_mfma_f32_16x16x32_bf16 v[168:171], a[212:215], v[84:87], v[168:171]\n" \
"ds_read_b128 a[204:207], v232 offset:6144\n" \
"v_cvt_pk_bf16_f32 v16, v16, v17\n" \
"v_mfma_f32_16x16x32_bf16 a[72:75], a[208:211], v[96:99], a[72:75]\n" \
"v_exp_f32 v18, v18\n" \
"v_mfma_f32_16x16x32_bf16 a[72:75], a[212:215], v[100:103], a[72:75]\n" \
"buffer_load_dwordx4 v234, s[36:39], s71 offen offset:1024 sc1 lds\n" \
"v_mfma_f32_16x16x32_bf16 a[104:107], a[208:211], v[112:115], a[104:107]\n" \
"v_exp_f32 v19, v19\n" \
"v_mfma_f32_16x16x32_bf16 a[104:107], a[212:215], v[116:119], a[104:107]\n" \
"ds_read_b128 a[208:211], v232 offset:256\n" \
"v_cvt_pk_bf16_f32 v17, v18, v19\n" \
"v_mfma_f32_16x16x32_bf16 a[108:111], a[216:219], v[112:115], a[108:111]\n" \
"v_exp_f32 v20, v20\n" \
"v_mfma_f32_16x16x32_bf16 a[108:111], a[220:223], v[116:119], a[108:111]\n" \
"buffer_load_dwordx4 v234, s[36:39], s71 offen offset:2048 sc1 lds\n" \
"v_mfma_f32_16x16x32_bf16 a[76:79], a[216:219], v[96:99], a[76:79]\n" \
"v_exp_f32 v21, v21\n" \
"v_mfma_f32_16x16x32_bf16 a[76:79], a[220:223], v[100:103], a[76:79]\n" \
"ds_read_b128 a[212:215], v232 offset:2304\n" \
"v_cvt_pk_bf16_f32 v18, v20, v21\n" \
"v_smfmac_f32_16x16x64_bf16 a[52:55], v[244:247], v[80:87], v243\n" \
"buffer_load_dwordx4 v234, s[36:39], s71 offen offset:3072 sc1 lds\n" \
"v_mfma_f32_16x16x32_bf16 v[172:175], a[216:219], v[80:83], v[172:175]\n" \
"s_add_u32 m0, s77, s80\n" \
"v_exp_f32 v22, v22\n" \
"v_mfma_f32_16x16x32_bf16 v[172:175], a[220:223], v[84:87], v[172:175]\n" \
"buffer_load_dwordx4 v234, s[40:43], s76 offen offset:0 sc1 lds\n" \
"v_mfma_f32_16x16x32_bf16 v[140:143], a[216:219], v[64:67], v[140:143]\n" \
"v_exp_f32 v23, v23\n" \
"v_mfma_f32_16x16x32_bf16 v[140:143], a[220:223], v[68:71], v[140:143]\n" \
"ds_read_b128 a[216:219], v232 offset:4352\n" \
"v_cvt_pk_bf16_f32 v19, v22, v23\n" \
"v_mfma_f32_16x16x32_bf16 v[144:147], a[224:227], v[64:67], v[144:147]\n" \
"s_add_u32 s71, s76, 17408\n" \
"v_exp_f32 v32, v32\n" \
"v_mfma_f32_16x16x32_bf16 v[144:147], a[228:231], v[68:71], v[144:147]\n" \
"buffer_load_dwordx4 v234, s[40:43], s76 offen offset:1024 sc1 lds\n" \
"v_mfma_f32_16x16x32_bf16 v[176:179], a[224:227], v[80:83], v[176:179]\n" \
"v_exp_f32 v33, v33\n" \
"v_mfma_f32_16x16x32_bf16 v[176:179], a[228:231], v[84:87], v[176:179]\n" \
"ds_read_b128 a[220:223], v232 offset:6400\n" \
"v_cvt_pk_bf16_f32 v32, v32, v33\n" \
"v_mfma_f32_16x16x32_bf16 a[80:83], a[224:227], v[96:99], a[80:83]\n" \
"v_exp_f32 v34, v34\n" \
"v_mfma_f32_16x16x32_bf16 a[80:83], a[228:231], v[100:103], a[80:83]\n" \
"buffer_load_dwordx4 v234, s[40:43], s76 offen offset:2048 sc1 lds\n" \
"v_mfma_f32_16x16x32_bf16 a[112:115], a[224:227], v[112:115], a[112:115]\n" \
"v_exp_f32 v35, v35\n" \
"v_mfma_f32_16x16x32_bf16 a[112:115], a[228:231], v[116:119], a[112:115]\n" \
"ds_read_b128 a[224:227], v232 offset:8192\n" \
"v_cvt_pk_bf16_f32 v33, v34, v35\n" \
"v_mfma_f32_16x16x32_bf16 a[116:119], a[232:235], v[112:115], a[116:119]\n" \
"v_exp_f32 v36, v36\n" \
"v_mfma_f32_16x16x32_bf16 a[116:119], a[236:239], v[116:119], a[116:119]\n" \
"buffer_load_dwordx4 v234, s[40:43], s76 offen offset:3072 sc1 lds\n" \
"v_mfma_f32_16x16x32_bf16 a[84:87], a[232:235], v[96:99], a[84:87]\n" \
"v_exp_f32 v37, v37\n" \
"v_mfma_f32_16x16x32_bf16 a[84:87], a[236:239], v[100:103], a[84:87]\n" \
"s_add_u32 m0, m0, 0x4000\n" \
"ds_read_b128 a[228:231], v232 offset:10240\n" \
"v_cvt_pk_bf16_f32 v34, v36, v37\n" \
"v_smfmac_f32_16x16x64_bf16 v[224:227], v[244:247], v[96:103], v243\n" \
"buffer_load_dwordx4 v234, s[40:43], s71 offen offset:0 sc1 lds\n" \
"v_mfma_f32_16x16x32_bf16 v[180:183], a[232:235], v[80:83], v[180:183]\n" \
"v_exp_f32 v38, v38\n" \
"v_mfma_f32_16x16x32_bf16 v[180:183], a[236:239], v[84:87], v[180:183]\n" \
"buffer_load_dwordx4 v234, s[40:43], s71 offen offset:1024 sc1 lds\n" \
"v_mfma_f32_16x16x32_bf16 v[148:151], a[232:235], v[64:67], v[148:151]\n" \
"v_exp_f32 v39, v39\n" \
"v_mfma_f32_16x16x32_bf16 v[148:151], a[236:239], v[68:71], v[148:151]\n" \
"ds_read_b128 a[232:235], v232 offset:12288\n" \
"v_cvt_pk_bf16_f32 v35, v38, v39\n" \
"v_mfma_f32_16x16x32_bf16 v[152:155], a[32:35], v[64:67], v[152:155]\n" \
"v_exp_f32 v48, v48\n" \
"v_mfma_f32_16x16x32_bf16 v[152:155], a[36:39], v[68:71], v[152:155]\n" \
"buffer_load_dwordx4 v234, s[40:43], s71 offen offset:2048 sc1 lds\n" \
"v_mfma_f32_16x16x32_bf16 v[184:187], a[32:35], v[80:83], v[184:187]\n" \
"v_exp_f32 v49, v49\n" \
"v_mfma_f32_16x16x32_bf16 v[184:187], a[36:39], v[84:87], v[184:187]\n" \
"ds_read_b128 a[236:239], v232 offset:14336\n" \
"v_cvt_pk_bf16_f32 v48, v48, v49\n" \
"v_mfma_f32_16x16x32_bf16 a[88:91], a[32:35], v[96:99], a[88:91]\n" \
"v_exp_f32 v50, v50\n" \
"v_mfma_f32_16x16x32_bf16 a[88:91], a[36:39], v[100:103], a[88:91]\n" \
"buffer_load_dwordx4 v234, s[40:43], s71 offen offset:3072 sc1 lds\n" \
"v_mfma_f32_16x16x32_bf16 a[120:123], a[32:35], v[112:115], a[120:123]\n" \
"s_mov_b32 s76, s73\n" \
"v_exp_f32 v51, v51\n" \
"v_mfma_f32_16x16x32_bf16 a[120:123], a[36:39], v[116:119], a[120:123]\n" \
"s_add_u32 s73, s73, 34816\n" \
"ds_read_b128 a[244:247], v232 offset:10496\n" \
"v_cvt_pk_bf16_f32 v49, v50, v51\n" \
"v_mfma_f32_16x16x32_bf16 a[124:127], a[40:43], v[112:115], a[124:127]\n" \
"s_xor_b32 s74, s74, 0x8000\n" \
"v_exp_f32 v52, v52\n" \
"v_mfma_f32_16x16x32_bf16 a[124:127], a[44:47], v[116:119], a[124:127]\n" \
"s_xor_b32 s77, s77, 0x8000\n" \
"v_xor_b32 v233, s81, v233\n" \
"s_mov_b32 s81, 0x8000\n" \
"v_mfma_f32_16x16x32_bf16 a[92:95], a[40:43], v[96:99], a[92:95]\n" \
"v_exp_f32 v53, v53\n" \
"v_mfma_f32_16x16x32_bf16 a[92:95], a[44:47], v[100:103], a[92:95]\n" \
"ds_read_b128 a[248:251], v232 offset:12544\n" \
"v_cvt_pk_bf16_f32 v50, v52, v53\n" \
"v_smfmac_f32_16x16x64_bf16 v[228:231], v[244:247], v[112:119], v243\n" \
"v_mfma_f32_16x16x32_bf16 v[188:191], a[40:43], v[80:83], v[188:191]\n" \
"v_exp_f32 v54, v54\n" \
"v_mfma_f32_16x16x32_bf16 v[188:191], a[44:47], v[84:87], v[188:191]\n" \
"s_cmp_lg_u32 s82, 0\n" \
"v_exp_f32 v55, v55\n" \
"v_mfma_f32_16x16x32_bf16 v[156:159], a[40:43], v[64:67], v[156:159]\n" \
"s_waitcnt lgkmcnt(2)\n" \
"ds_read_b128 a[252:255], v232 offset:14592\n" \
"v_cvt_pk_bf16_f32 v51, v54, v55\n" \
"v_mfma_f32_16x16x32_bf16 v[156:159], a[44:47], v[68:71], v[156:159]\n" \
"s_cbranch_scc1 Lep_%=\n" \
"Lepr_%=:\n" \
"v_mfma_f32_16x16x32_bf16 v[64:67], a[192:195], a[0:3], 0\n" \
"v_exp_f32 v8, v8\n" \
"v_mfma_f32_16x16x32_bf16 v[64:67], a[196:199], a[4:7], v[64:67]\n" \
"ds_read_b128 a[44:47], v233 offset:15360\n" \
"v_mfma_f32_16x16x32_bf16 v[64:67], a[200:203], a[8:11], v[64:67]\n" \
"v_exp_f32 v9, v9\n" \
"v_mfma_f32_16x16x32_bf16 v[64:67], a[204:207], a[12:15], v[64:67]\n" \
"ds_read_b128 a[32:35], v233 offset:6144\n" \
"v_cvt_pk_bf16_f32 v4, v8, v9\n" \
"v_mfma_f32_16x16x32_bf16 v[80:83], a[204:207], a[28:31], 0\n" \
"s_sub_u32 s84, s84, 1\n" \
"v_exp_f32 v10, v10\n" \
"v_mfma_f32_16x16x32_bf16 v[80:83], a[200:203], a[24:27], v[80:83]\n" \
"s_cmp_eq_u32 s84, 0\n" \
"s_cselect_b32 s70, s75, 0x8000\n" \
"v_xor_b32 v232, s70, v232\n" \
"v_mfma_f32_16x16x32_bf16 v[80:83], a[196:199], a[20:23], v[80:83]\n" \
"v_exp_f32 v11, v11\n" \
"v_mfma_f32_16x16x32_bf16 v[80:83], a[192:195], a[16:19], v[80:83]\n" \
"ds_read_b128 a[36:39], v233 offset:14336\n" \
"v_cvt_pk_bf16_f32 v5, v10, v11\n" \
"v_mfma_f32_16x16x32_bf16 v[96:99], a[192:195], a[160:163], 0\n" \
"v_exp_f32 v12, v12\n" \
"v_mfma_f32_16x16x32_bf16 v[96:99], a[196:199], a[164:167], v[96:99]\n" \
"v_mfma_f32_16x16x32_bf16 v[96:99], a[200:203], a[168:171], v[96:99]\n" \
"v_exp_f32 v13, v13\n" \
"v_mfma_f32_16x16x32_bf16 v[96:99], a[204:207], a[172:175], v[96:99]\n" \
"ds_read_b128 a[40:43], v233 offset:7168\n" \
"v_cvt_pk_bf16_f32 v6, v12, v13\n" \
"v_mfma_f32_16x16x32_bf16 v[112:115], a[204:207], a[188:191], 0\n" \
"v_exp_f32 v14, v14\n" \
"v_mfma_f32_16x16x32_bf16 v[112:115], a[200:203], a[184:187], v[112:115]\n" \
"v_mfma_f32_16x16x32_bf16 v[112:115], a[196:199], a[180:183], v[112:115]\n" \
"v_exp_f32 v15, v15\n" \
"v_mfma_f32_16x16x32_bf16 v[112:115], a[192:195], a[176:179], v[112:115]\n" \
"ds_read_b128 a[204:207], v233 offset:9216\n" \
"v_cvt_pk_bf16_f32 v7, v14, v15\n" \
"v_mfma_f32_16x16x32_bf16 v[116:119], a[208:211], a[176:179], 0\n" \
"v_exp_f32 v24, v24\n" \
"v_mfma_f32_16x16x32_bf16 v[116:119], a[212:215], a[180:183], v[116:119]\n" \
"v_mfma_f32_16x16x32_bf16 v[116:119], a[216:219], a[184:187], v[116:119]\n" \
"v_exp_f32 v25, v25\n" \
"v_mfma_f32_16x16x32_bf16 v[116:119], a[220:223], a[188:191], v[116:119]\n" \
"ds_read_b128 a[200:203], v233 offset:1024\n" \
"Lpg6691_%=:\n" \
"s_cbranch_execz Lcb0_%=\n" \
"v_cvt_pk_bf16_f32 v20, v24, v25\n" \
"v_mfma_f32_16x16x32_bf16 v[100:103], a[220:223], a[172:175], 0\n" \
"v_exp_f32 v26, v26\n" \
"v_mfma_f32_16x16x32_bf16 v[100:103], a[216:219], a[168:171], v[100:103]\n" \
"v_mfma_f32_16x16x32_bf16 v[100:103], a[212:215], a[164:167], v[100:103]\n" \
"v_exp_f32 v27, v27\n" \
"v_mfma_f32_16x16x32_bf16 v[100:103], a[208:211], a[160:163], v[100:103]\n" \
"ds_read_b128 a[196:199], v233 offset:8192\n" \
"v_cvt_pk_bf16_f32 v21, v26, v27\n" \
"v_mfma_f32_16x16x32_bf16 v[84:87], a[208:211], a[16:19], 0\n" \
"v_exp_f32 v28, v28\n" \
"v_mfma_f32_16x16x32_bf16 v[84:87], a[212:215], a[20:23], v[84:87]\n" \
"v_mfma_f32_16x16x32_bf16 v[84:87], a[216:219], a[24:27], v[84:87]\n" \
"v_exp_f32 v29, v29\n" \
"v_mfma_f32_16x16x32_bf16 v[84:87], a[220:223], a[28:31], v[84:87]\n" \
"ds_read_b128 a[192:195], v233 offset:0\n" \
"v_cvt_pk_bf16_f32 v22, v28, v29\n" \
"v_mfma_f32_16x16x32_bf16 v[68:71], a[220:223], a[12:15], 0\n" \
"v_exp_f32 v30, v30\n" \
"v_mfma_f32_16x16x32_bf16 v[68:71], a[216:219], a[8:11], v[68:71]\n" \
"v_mfma_f32_16x16x32_bf16 v[68:71], a[212:215], a[4:7], v[68:71]\n" \
"s_waitcnt lgkmcnt(8)\n" \
"v_exp_f32 v31, v31\n" \
"v_mfma_f32_16x16x32_bf16 v[68:71], a[208:211], a[0:3], v[68:71]\n" \
"ds_read_b128 a[220:223], v233 offset:11264\n" \
"v_cvt_pk_bf16_f32 v23, v30, v31\n" \
"v_mfma_f32_16x16x32_bf16 v[72:75], a[224:227], a[0:3], 0\n" \
"v_exp_f32 v40, v40\n" \
"v_mfma_f32_16x16x32_bf16 v[72:75], a[228:231], a[4:7], v[72:75]\n" \
"v_mfma_f32_16x16x32_bf16 v[72:75], a[232:235], a[8:11], v[72:75]\n" \
"v_exp_f32 v41, v41\n" \
"v_mfma_f32_16x16x32_bf16 v[72:75], a[236:239], a[12:15], v[72:75]\n" \
"ds_read_b128 a[216:219], v233 offset:3072\n" \
"v_cvt_pk_bf16_f32 v36, v40, v41\n" \
"v_mfma_f32_16x16x32_bf16 v[88:91], a[236:239], a[28:31], 0\n" \
"v_exp_f32 v42, v42\n" \
"v_mfma_f32_16x16x32_bf16 v[88:91], a[232:235], a[24:27], v[88:91]\n" \
"v_mfma_f32_16x16x32_bf16 v[88:91], a[228:231], a[20:23], v[88:91]\n" \
"v_exp_f32 v43, v43\n" \
"v_mfma_f32_16x16x32_bf16 v[88:91], a[224:227], a[16:19], v[88:91]\n" \
"ds_read_b128 a[212:215], v233 offset:10240\n" \
"v_cvt_pk_bf16_f32 v37, v42, v43\n" \
"v_mfma_f32_16x16x32_bf16 v[104:107], a[224:227], a[160:163], 0\n" \
"v_exp_f32 v44, v44\n" \
"v_mfma_f32_16x16x32_bf16 v[104:107], a[228:231], a[164:167], v[104:107]\n" \
"v_mfma_f32_16x16x32_bf16 v[104:107], a[232:235], a[168:171], v[104:107]\n" \
"v_exp_f32 v45, v45\n" \
"v_mfma_f32_16x16x32_bf16 v[104:107], a[236:239], a[172:175], v[104:107]\n" \
"ds_read_b128 a[208:211], v233 offset:2048\n" \
"v_cvt_pk_bf16_f32 v38, v44, v45\n" \
"v_mfma_f32_16x16x32_bf16 v[120:123], a[236:239], a[188:191], 0\n" \
"v_exp_f32 v46, v46\n" \
"v_mfma_f32_16x16x32_bf16 v[120:123], a[232:235], a[184:187], v[120:123]\n" \
"v_mfma_f32_16x16x32_bf16 v[120:123], a[228:231], a[180:183], v[120:123]\n" \
"v_exp_f32 v47, v47\n" \
"v_mfma_f32_16x16x32_bf16 v[120:123], a[224:227], a[176:179], v[120:123]\n" \
"ds_read_b128 a[236:239], v233 offset:13312\n" \
"v_cvt_pk_bf16_f32 v39, v46, v47\n" \
"v_mfma_f32_16x16x32_bf16 v[124:127], a[240:243], a[176:179], 0\n" \
"v_exp_f32 v56, v56\n" \
"v_mfma_f32_16x16x32_bf16 v[124:127], a[244:247], a[180:183], v[124:127]\n" \
"ds_read_b128 a[232:235], v233 offset:5120\n" \
"v_mfma_f32_16x16x32_bf16 v[124:127], a[248:251], a[184:187], v[124:127]\n" \
"v_exp_f32 v57, v57\n" \
"v_mfma_f32_16x16x32_bf16 v[124:127], a[252:255], a[188:191], v[124:127]\n" \
"ds_read_b128 a[228:231], v233 offset:12288\n" \
"v_cvt_pk_bf16_f32 v52, v56, v57\n" \
"v_mfma_f32_16x16x32_bf16 v[108:111], a[252:255], a[172:175], 0\n" \
"v_exp_f32 v58, v58\n" \
"v_mfma_f32_16x16x32_bf16 v[108:111], a[248:251], a[168:171], v[108:111]\n" \
"ds_read_b128 a[224:227], v233 offset:4096\n" \
"v_mfma_f32_16x16x32_bf16 v[108:111], a[244:247], a[164:167], v[108:111]\n" \
"v_exp_f32 v59, v59\n" \
"v_mfma_f32_16x16x32_bf16 v[108:111], a[240:243], a[160:163], v[108:111]\n" \
"v_cvt_pk_bf16_f32 v53, v58, v59\n" \
"v_mfma_f32_16x16x32_bf16 v[92:95], a[240:243], a[16:19], 0\n" \
"v_exp_f32 v60, v60\n" \
"v_mfma_f32_16x16x32_bf16 v[92:95], a[244:247], a[20:23], v[92:95]\n" \
"v_mfma_f32_16x16x32_bf16 v[92:95], a[248:251], a[24:27], v[92:95]\n" \
"v_exp_f32 v61, v61\n" \
"v_mfma_f32_16x16x32_bf16 v[92:95], a[252:255], a[28:31], v[92:95]\n" \
"v_cvt_pk_bf16_f32 v54, v60, v61\n" \
"v_mfma_f32_16x16x32_bf16 v[76:79], a[252:255], a[12:15], 0\n" \
"v_exp_f32 v62, v62\n" \
"v_mfma_f32_16x16x32_bf16 v[76:79], a[248:251], a[8:11], v[76:79]\n" \
"s_cmp_lg_u32 s82, 0\n" \
"s_cbranch_scc1 Lnw_%=\n" \
"s_waitcnt vmcnt(0)\n" \
"Lnw_%=:\n" \
"v_mfma_f32_16x16x32_bf16 v[76:79], a[244:247], a[4:7], v[76:79]\n" \
"s_waitcnt lgkmcnt(0)\n" \
"v_exp_f32 v63, v63\n" \
"v_mfma_f32_16x16x32_bf16 v[76:79], a[240:243], a[0:3], v[76:79]\n" \
"s_barrier\n" \
"v_cvt_pk_bf16_f32 v55, v62, v63\n" \
"v_mfma_f32_16x16x32_bf16 v[128:131], a[192:195], v[0:3], v[128:131]\n" \
"s_add_u32 m0, s74, s79\n" \
"v_exp_f32 v64, v64\n" \
"v_mfma_f32_16x16x32_bf16 v[128:131], a[196:199], v[4:7], v[128:131]\n" \
"buffer_load_dwordx4 v234, s[36:39], s73 offen offset:0 sc1 lds\n" \
"v_mfma_f32_16x16x32_bf16 v[160:163], a[192:195], v[16:19], v[160:163]\n" \
"v_exp_f32 v65, v65\n" \
"v_mfma_f32_16x16x32_bf16 v[160:163], a[196:199], v[20:23], v[160:163]\n" \
"ds_read_b128 a[240:243], v232 offset:8448\n" \
"v_cvt_pk_bf16_f32 v64, v64, v65\n" \
"v_mfma_f32_16x16x32_bf16 a[64:67], a[192:195], v[32:35], a[64:67]\n" \
"s_add_u32 s71, s73, 17408\n" \
"v_exp_f32 v66, v66\n" \
"v_mfma_f32_16x16x32_bf16 a[64:67], a[196:199], v[36:39], a[64:67]\n" \
"buffer_load_dwordx4 v234, s[36:39], s73 offen offset:1024 sc1 lds\n" \
"v_mfma_f32_16x16x32_bf16 a[96:99], a[192:195], v[48:51], a[96:99]\n" \
"v_exp_f32 v67, v67\n" \
"v_mfma_f32_16x16x32_bf16 a[96:99], a[196:199], v[52:55], a[96:99]\n" \
"ds_read_b128 a[192:195], v232 offset:0\n" \
"v_cvt_pk_bf16_f32 v65, v66, v67\n" \
"v_mfma_f32_16x16x32_bf16 a[100:103], a[200:203], v[48:51], a[100:103]\n" \
"v_exp_f32 v68, v68\n" \
"v_mfma_f32_16x16x32_bf16 a[100:103], a[204:207], v[52:55], a[100:103]\n" \
"buffer_load_dwordx4 v234, s[36:39], s73 offen offset:2048 sc1 lds\n" \
"v_mfma_f32_16x16x32_bf16 a[68:71], a[200:203], v[32:35], a[68:71]\n" \
"v_exp_f32 v69, v69\n" \
"v_mfma_f32_16x16x32_bf16 a[68:71], a[204:207], v[36:39], a[68:71]\n" \
"ds_read_b128 a[196:199], v232 offset:2048\n" \
"v_cvt_pk_bf16_f32 v66, v68, v69\n" \
"v_smfmac_f32_16x16x64_bf16 a[48:51], v[244:247], v[0:7], v243\n" \
"buffer_load_dwordx4 v234, s[36:39], s73 offen offset:3072 sc1 lds\n" \
"v_mfma_f32_16x16x32_bf16 v[164:167], a[200:203], v[16:19], v[164:167]\n" \
"s_add_u32 m0, m0, 0x4000\n" \
"v_exp_f32 v70, v70\n" \
"v_mfma_f32_16x16x32_bf16 v[164:167], a[204:207], v[20:23], v[164:167]\n" \
"buffer_load_dwordx4 v234, s[36:39], s71 offen offset:0 sc1 lds\n" \
"v_mfma_f32_16x16x32_bf16 v[132:135], a[200:203], v[0:3], v[132:135]\n" \
"v_exp_f32 v71, v71\n" \
"v_mfma_f32_16x16x32_bf16 v[132:135], a[204:207], v[4:7], v[132:135]\n" \
"ds_read_b128 a[200:203], v232 offset:4096\n" \
"v_cvt_pk_bf16_f32 v67, v70, v71\n" \
"v_mfma_f32_16x16x32_bf16 v[136:139], a[208:211], v[0:3], v[136:139]\n" \
"v_exp_f32 v80, v80\n" \
"v_mfma_f32_16x16x32_bf16 v[136:139], a[212:215], v[4:7], v[136:139]\n" \
"buffer_load_dwordx4 v234, s[36:39], s71 offen offset:1024 sc1 lds\n" \
"v_mfma_f32_16x16x32_bf16 v[168:171], a[208:211], v[16:19], v[168:171]\n" \
"v_exp_f32 v81, v81\n" \
"v_mfma_f32_16x16x32_bf16 v[168:171], a[212:215], v[20:23], v[168:171]\n" \
"ds_read_b128 a[204:207], v232 offset:6144\n" \
"v_cvt_pk_bf16_f32 v80, v80, v81\n" \
"v_mfma_f32_16x16x32_bf16 a[72:75], a[208:211], v[32:35], a[72:75]\n" \
"v_exp_f32 v82, v82\n" \
"v_mfma_f32_16x16x32_bf16 a[72:75], a[212:215], v[36:39], a[72:75]\n" \
"buffer_load_dwordx4 v234, s[36:39], s71 offen offset:2048 sc1 lds\n" \
"v_mfma_f32_16x16x32_bf16 a[104:107], a[208:211], v[48:51], a[104:107]\n" \
"v_exp_f32 v83, v83\n" \
"v_mfma_f32_16x16x32_bf16 a[104:107], a[212:215], v[52:55], a[104:107]\n" \
"ds_read_b128 a[208:211], v232 offset:256\n" \
"v_cvt_pk_bf16_f32 v81, v82, v83\n" \
"v_mfma_f32_16x16x32_bf16 a[108:111], a[216:219], v[48:51], a[108:111]\n" \
"v_exp_f32 v84, v84\n" \
"v_mfma_f32_16x16x32_bf16 a[108:111], a[220:223], v[52:55], a[108:111]\n" \
"buffer_load_dwordx4 v234, s[36:39], s71 offen offset:3072 sc1 lds\n" \
"v_mfma_f32_16x16x32_bf16 a[76:79], a[216:219], v[32:35], a[76:79]\n" \
"v_exp_f32 v85, v85\n" \
"v_mfma_f32_16x16x32_bf16 a[76:79], a[220:223], v[36:39], a[76:79]\n" \
"s_add_u32 m0, s77, s80\n" \
"ds_read_b128 a[212:215], v232 offset:2304\n" \
"v_cvt_pk_bf16_f32 v82, v84, v85\n" \
"v_smfmac_f32_16x16x64_bf16 a[52:55], v[244:247], v[16:23], v243\n" \
"buffer_load_dwordx4 v234, s[40:43], s76 offen offset:0 sc1 lds\n" \
"v_mfma_f32_16x16x32_bf16 v[172:175], a[216:219], v[16:19], v[172:175]\n" \
"s_add_u32 s71, s76, 17408\n" \
"v_exp_f32 v86, v86\n" \
"v_mfma_f32_16x16x32_bf16 v[172:175], a[220:223], v[20:23], v[172:175]\n" \
"buffer_load_dwordx4 v234, s[40:43], s76 offen offset:1024 sc1 lds\n" \
"v_mfma_f32_16x16x32_bf16 v[140:143], a[216:219], v[0:3], v[140:143]\n" \
"v_exp_f32 v87, v87\n" \
"v_mfma_f32_16x16x32_bf16 v[140:143], a[220:223], v[4:7], v[140:143]\n" \
"ds_read_b128 a[216:219], v232 offset:4352\n" \
"v_cvt_pk_bf16_f32 v83, v86, v87\n" \
"v_mfma_f32_16x16x32_bf16 v[144:147], a[224:227], v[0:3], v[144:147]\n" \
"v_exp_f32 v96, v96\n" \
"v_mfma_f32_16x16x32_bf16 v[144:147], a[228:231], v[4:7], v[144:147]\n" \
"buffer_load_dwordx4 v234, s[40:43], s76 offen offset:2048 sc1 lds\n" \
"v_mfma_f32_16x16x32_bf16 v[176:179], a[224:227], v[16:19], v[176:179]\n" \
"v_exp_f32 v97, v97\n" \
"v_mfma_f32_16x16x32_bf16 v[176:179], a[228:231], v[20:23], v[176:179]\n" \
"ds_read_b128 a[220:223], v232 offset:6400\n" \
"v_cvt_pk_bf16_f32 v96, v96, v97\n" \
"v_mfma_f32_16x16x32_bf16 a[80:83], a[224:227], v[32:35], a[80:83]\n" \
"v_exp_f32 v98, v98\n" \
"v_mfma_f32_16x16x32_bf16 a[80:83], a[228:231], v[36:39], a[80:83]\n" \
"buffer_load_dwordx4 v234, s[40:43], s76 offen offset:3072 sc1 lds\n" \
"v_mfma_f32_16x16x32_bf16 a[112:115], a[224:227], v[48:51], a[112:115]\n" \
"v_exp_f32 v99, v99\n" \
"v_mfma_f32_16x16x32_bf16 a[112:115], a[228:231], v[52:55], a[112:115]\n" \
"ds_read_b128 a[224:227], v232 offset:8192\n" \
"v_cvt_pk_bf16_f32 v97, v98, v99\n" \
"v_mfma_f32_16x16x32_bf16 a[116:119], a[232:235], v[48:51], a[116:119]\n" \
"s_add_u32 m0, m0, 0x4000\n" \
"v_exp_f32 v100, v100\n" \
"v_mfma_f32_16x16x32_bf16 a[116:119], a[236:239], v[52:55], a[116:119]\n" \
"buffer_load_dwordx4 v234, s[40:43], s71 offen offset:0 sc1 lds\n" \
"v_mfma_f32_16x16x32_bf16 a[84:87], a[232:235], v[32:35], a[84:87]\n" \
"v_exp_f32 v101, v101\n" \
"v_mfma_f32_16x16x32_bf16 a[84:87], a[236:239], v[36:39], a[84:87]\n" \
"ds_read_b128 a[228:231], v232 offset:10240\n" \
"v_cvt_pk_bf16_f32 v98, v100, v101\n" \
"v_smfmac_f32_16x16x64_bf16 v[224:227], v[244:247], v[32:39], v243\n" \
"buffer_load_dwordx4 v234, s[40:43], s71 offen offset:1024 sc1 lds\n" \
"v_mfma_f32_16x16x32_bf16 v[180:183], a[232:235], v[16:19], v[180:183]\n" \
"v_exp_f32 v102, v102\n" \
"v_mfma_f32_16x16x32_bf16 v[180:183], a[236:239], v[20:23], v[180:183]\n" \
"buffer_load_dwordx4 v234, s[40:43], s71 offen offset:2048 sc1 lds\n" \
"v_mfma_f32_16x16x32_bf16 v[148:151], a[232:235], v[0:3], v[148:151]\n" \
"v_exp_f32 v103, v103\n" \
"v_mfma_f32_16x16x32_bf16 v[148:151], a[236:239], v[4:7], v[148:151]\n" \
"ds_read_b128 a[232:235], v232 offset:12288\n" \
"v_cvt_pk_bf16_f32 v99, v102, v103\n" \
"v_mfma_f32_16x16x32_bf16 v[152:155], a[32:35], v[0:3], v[152:155]\n" \
"v_exp_f32 v112, v112\n" \
"v_mfma_f32_16x16x32_bf16 v[152:155], a[36:39], v[4:7], v[152:155]\n" \
"buffer_load_dwordx4 v234, s[40:43], s71 offen offset:3072 sc1 lds\n" \
"v_mfma_f32_16x16x32_bf16 v[184:187], a[32:35], v[16:19], v[184:187]\n" \
"v_exp_f32 v113, v113\n" \
"v_mfma_f32_16x16x32_bf16 v[184:187], a[36:39], v[20:23], v[184:187]\n" \
"ds_read_b128 a[236:239], v232 offset:14336\n" \
"v_cvt_pk_bf16_f32 v112, v112, v113\n" \
"v_mfma_f32_16x16x32_bf16 a[88:91], a[32:35], v[32:35], a[88:91]\n" \
"v_exp_f32 v114, v114\n" \
"v_mfma_f32_16x16x32_bf16 a[88:91], a[36:39], v[36:39], a[88:91]\n" \
"buffer_load_dword v239, v242, s[56:59], s95 offen sc1\n" \
"v_mfma_f32_16x16x32_bf16 a[120:123], a[32:35], v[48:51], a[120:123]\n" \
"s_mov_b32 s69, s92\n" \
"v_exp_f32 v115, v115\n" \
"v_mfma_f32_16x16x32_bf16 a[120:123], a[36:39], v[52:55], a[120:123]\n" \
"s_mov_b32 s76, s73\n" \
"ds_read_b128 a[244:247], v232 offset:10496\n" \
"v_cvt_pk_bf16_f32 v113, v114, v115\n" \
"v_mfma_f32_16x16x32_bf16 a[124:127], a[40:43], v[48:51], a[124:127]\n" \
"s_add_u32 s73, s73, 34816\n" \
"v_exp_f32 v116, v116\n" \
"v_mfma_f32_16x16x32_bf16 a[124:127], a[44:47], v[52:55], a[124:127]\n" \
"s_xor_b32 s74, s74, 0x8000\n" \
"s_xor_b32 s77, s77, 0x8000\n" \
"v_xor_b32 v233, 0x8000, v233\n" \
"v_mfma_f32_16x16x32_bf16 a[92:95], a[40:43], v[32:35], a[92:95]\n" \
"s_mov_b32 s82, 0\n" \
"v_exp_f32 v117, v117\n" \
"v_mfma_f32_16x16x32_bf16 a[92:95], a[44:47], v[36:39], a[92:95]\n" \
"ds_read_b128 a[248:251], v232 offset:12544\n" \
"v_cvt_pk_bf16_f32 v114, v116, v117\n" \
"v_smfmac_f32_16x16x64_bf16 v[228:231], v[244:247], v[48:55], v243\n" \
"v_mfma_f32_16x16x32_bf16 v[188:191], a[40:43], v[16:19], v[188:191]\n" \
"v_exp_f32 v118, v118\n" \
"v_mfma_f32_16x16x32_bf16 v[188:191], a[44:47], v[20:23], v[188:191]\n" \
"s_cmp_lg_u32 s84, 0\n" \
"v_exp_f32 v119, v119\n" \
"v_mfma_f32_16x16x32_bf16 v[156:159], a[40:43], v[0:3], v[156:159]\n" \
"s_waitcnt lgkmcnt(2)\n" \
"ds_read_b128 a[252:255], v232 offset:14592\n" \
"v_cvt_pk_bf16_f32 v115, v118, v119\n" \
"v_mfma_f32_16x16x32_bf16 v[156:159], a[44:47], v[4:7], v[156:159]\n" \
"s_cbranch_scc1 Lloop2_%=\n" \
"Lg_%=:\n" \
"Lpg6953_%=:\n" \
"s_cbranch_execz Lcb0_%=\n" \
"v_mfma_f32_16x16x32_bf16 v[32:35], a[192:195], a[160:163], v[208:211]\n" \
"s_mov_b64 s[44:45], s[20:21]\n" \
"s_memrealtime s[14:15]\n" \
"v_exp_f32 v72, v72\n" \
"v_mfma_f32_16x16x32_bf16 v[32:35], a[196:199], a[164:167], v[32:35]\n" \
"s_and_b32 s45, s45, 0xffff\n" \
"ds_read_b128 a[32:35], v233 offset:6144\n" \
"ds_read_b128 a[36:39], v233 offset:14336\n" \
"v_mfma_f32_16x16x32_bf16 v[32:35], a[200:203], a[168:171], v[32:35]\n" \
"s_mov_b32 s46, 4194304\n" \
"v_exp_f32 v73, v73\n" \
"v_mfma_f32_16x16x32_bf16 v[32:35], a[204:207], a[172:175], v[32:35]\n" \
"ds_read_b128 a[40:43], v233 offset:7168\n" \
"ds_read_b128 a[44:47], v233 offset:15360\n" \
"global_load_dword v253, v254, s[22:23]\n" \
"v_mfma_f32_16x16x32_bf16 v[48:51], a[204:207], a[188:191], 0\n" \
"s_mov_b32 s47, 0x20000\n" \
"v_exp_f32 v74, v74\n" \
"v_mfma_f32_16x16x32_bf16 v[48:51], a[200:203], a[184:187], v[48:51]\n" \
"s_mov_b64 s[48:49], s[22:23]\n" \
"v_exp_f32 v75, v75\n" \
"v_mfma_f32_16x16x32_bf16 v[48:51], a[196:199], a[180:183], v[48:51]\n" \
"s_and_b32 s49, s49, 0xffff\n" \
"v_exp_f32 v76, v76\n" \
"v_mfma_f32_16x16x32_bf16 v[48:51], a[192:195], a[176:179], v[48:51]\n" \
"s_mov_b32 s50, 6291456\n" \
"ds_read_b128 a[200:203], v233 offset:1024\n" \
"ds_read_b128 a[204:207], v233 offset:9216\n" \
"v_mfma_f32_16x16x32_bf16 v[52:55], a[208:211], a[176:179], 0\n" \
"s_mov_b32 s51, 0x20000\n" \
"v_cvt_pk_bf16_f32 v68, v72, v73\n" \
"v_cvt_pk_bf16_f32 v69, v74, v75\n" \
"v_mfma_f32_16x16x32_bf16 v[52:55], a[212:215], a[180:183], v[52:55]\n" \
"s_bfe_u32 s94, %1, 0x20001\n" \
"v_exp_f32 v77, v77\n" \
"v_mfma_f32_16x16x32_bf16 v[52:55], a[216:219], a[184:187], v[52:55]\n" \
"s_lshl_b32 s94, s94, 2\n" \
"v_exp_f32 v78, v78\n" \
"v_mfma_f32_16x16x32_bf16 v[52:55], a[220:223], a[188:191], v[52:55]\n" \
"s_lshr_b32 s70, %1, 6\n" \
"ds_read_b128 a[192:195], v233 offset:0\n" \
"ds_read_b128 a[196:199], v233 offset:8192\n" \
"v_mfma_f32_16x16x32_bf16 v[36:39], a[220:223], a[172:175], v[212:215]\n" \
"s_add_u32 s94, s94, s70\n" \
"v_exp_f32 v79, v79\n" \
"v_mfma_f32_16x16x32_bf16 v[36:39], a[216:219], a[168:171], v[36:39]\n" \
"s_lshr_b32 s71, s22, 7\n" \
"v_exp_f32 v88, v88\n" \
"v_mfma_f32_16x16x32_bf16 v[36:39], a[212:215], a[164:167], v[36:39]\n" \
"s_sub_u32 s71, s63, s71\n" \
"v_cvt_pk_bf16_f32 v70, v76, v77\n" \
"v_cvt_pk_bf16_f32 v71, v78, v79\n" \
"v_mfma_f32_16x16x32_bf16 v[36:39], a[208:211], a[160:163], v[36:39]\n" \
"s_add_u32 s71, s71, 6\n" \
"ds_read_b128 a[216:219], v233 offset:3072\n" \
"ds_read_b128 a[220:223], v233 offset:11264\n" \
"v_mfma_f32_16x16x32_bf16 v[40:43], a[224:227], a[160:163], v[216:219]\n" \
"s_and_b32 s71, s71, 7\n" \
"v_exp_f32 v89, v89\n" \
"v_mfma_f32_16x16x32_bf16 v[40:43], a[228:231], a[164:167], v[40:43]\n" \
"s_and_b32 s70, %1, 1\n" \
"v_exp_f32 v90, v90\n" \
"v_mfma_f32_16x16x32_bf16 v[40:43], a[232:235], a[168:171], v[40:43]\n" \
"s_lshl_b32 s70, s70, 3\n" \
"v_exp_f32 v91, v91\n" \
"v_mfma_f32_16x16x32_bf16 v[40:43], a[236:239], a[172:175], v[40:43]\n" \
"s_add_u32 s70, s70, s71\n" \
"ds_read_b128 a[208:211], v233 offset:2048\n" \
"ds_read_b128 a[212:215], v233 offset:10240\n" \
"v_mfma_f32_16x16x32_bf16 v[56:59], a[236:239], a[188:191], 0\n" \
"s_lshl_b32 s78, s70, 7\n" \
"v_exp_f32 v92, v92\n" \
"v_mfma_f32_16x16x32_bf16 v[56:59], a[232:235], a[184:187], v[56:59]\n" \
"s_lshr_b32 s80, %1, 6\n" \
"v_cvt_pk_bf16_f32 v84, v88, v89\n" \
"v_cvt_pk_bf16_f32 v85, v90, v91\n" \
"v_mfma_f32_16x16x32_bf16 v[56:59], a[228:231], a[180:183], v[56:59]\n" \
"s_xor_b32 s80, s80, s63\n" \
"v_exp_f32 v93, v93\n" \
"v_mfma_f32_16x16x32_bf16 v[56:59], a[224:227], a[176:179], v[56:59]\n" \
"s_and_b32 s80, s80, 0\n" \
"ds_read_b128 a[232:235], v233 offset:5120\n" \
"ds_read_b128 a[236:239], v233 offset:13312\n" \
"v_mfma_f32_16x16x32_bf16 v[60:63], a[240:243], a[176:179], 0\n" \
"s_waitcnt lgkmcnt(2)\n" \
"s_lshl_b32 s93, s70, 12\n" \
"v_exp_f32 v94, v94\n" \
"v_mfma_f32_16x16x32_bf16 v[60:63], a[244:247], a[180:183], v[60:63]\n" \
"s_mul_i32 s91, s61, 6144\n" \
"v_exp_f32 v95, v95\n" \
"v_mfma_f32_16x16x32_bf16 v[60:63], a[248:251], a[184:187], v[60:63]\n" \
"s_add_u32 s91, s91, %3\n" \
"v_cvt_pk_bf16_f32 v86, v92, v93\n" \
"v_accvgpr_read_b32 v197, a56\n" \
"v_mfma_f32_16x16x32_bf16 v[60:63], a[252:255], a[188:191], v[60:63]\n" \
"s_lshl_b32 s71, s80, 7\n" \
"ds_read_b128 a[224:227], v233 offset:4096\n" \
"ds_read_b128 a[228:231], v233 offset:12288\n" \
"v_mfma_f32_16x16x32_bf16 v[44:47], a[252:255], a[172:175], v[220:223]\n" \
"s_lshl_b32 s75, s94, 18\n" \
"v_cvt_pk_bf16_f32 v87, v94, v95\n" \
"v_accvgpr_read_b32 v192, a57\n" \
"v_mfma_f32_16x16x32_bf16 v[44:47], a[248:251], a[168:171], v[44:47]\n" \
"s_lshl_b32 s70, s61, 16\n" \
"v_accvgpr_read_b32 v196, a58\n" \
"v_add_u32 v198, 1024, v197\n" \
"v_mfma_f32_16x16x32_bf16 v[44:47], a[244:247], a[164:167], v[44:47]\n" \
"s_add_u32 s73, s75, s70\n" \
"v_add_u32 v199, 2048, v197\n" \
"v_add_u32 v193, 31744, v192\n" \
"v_mfma_f32_16x16x32_bf16 v[44:47], a[240:243], a[160:163], v[44:47]\n" \
"s_add_u32 s73, s73, s71\n" \
"v_exp_f32 v104, v104\n" \
"v_mfma_f32_16x16x32_bf16 v[128:131], a[192:195], v[64:67], v[128:131]\n" \
"s_max_u32 s75, s75, 32768\n" \
"v_exp_f32 v105, v105\n" \
"v_mfma_f32_16x16x32_bf16 v[128:131], a[196:199], v[68:71], v[128:131]\n" \
"s_sub_u32 s75, s75, 32768\n" \
"ds_read_b128 a[0:3], v232 offset:16384\n" \
"ds_read_b128 a[4:7], v232 offset:18432\n" \
"v_mfma_f32_16x16x32_bf16 v[160:163], a[192:195], v[80:83], v[160:163]\n" \
"s_add_u32 s75, s75, s71\n" \
"s_lshl_b32 s89, s61, 10\n" \
"v_mfma_f32_16x16x32_bf16 v[160:163], a[196:199], v[84:87], v[160:163]\n" \
"s_mul_i32 s76, s80, 393216\n" \
"s_add_u32 s89, s89, %3\n" \
"v_exp_f32 v106, v106\n" \
"v_mfma_f32_16x16x32_bf16 v[164:167], a[200:203], v[80:83], v[164:167]\n" \
"s_add_u32 s76, s76, s78\n" \
"ds_read_b128 a[8:11], v232 offset:20480\n" \
"ds_read_b128 a[12:15], v232 offset:22528\n" \
"v_mfma_f32_16x16x32_bf16 v[164:167], a[204:207], v[84:87], v[164:167]\n" \
"s_add_u32 m0, s91, 139264\n" \
"s_add_u32 s89, s89, 16384\n" \
"v_cvt_pk_bf16_f32 v100, v104, v105\n" \
"v_mfma_f32_16x16x32_bf16 v[132:135], a[200:203], v[64:67], v[132:135]\n" \
"buffer_load_dwordx4 v197, s[48:51], s76 offen offset:0 lds\n" \
"v_mfma_f32_16x16x32_bf16 v[132:135], a[204:207], v[68:71], v[132:135]\n" \
"s_and_b32 s70, s61, 1\n" \
"ds_read_b128 a[16:19], v232 offset:16640\n" \
"ds_read_b128 a[20:23], v232 offset:18688\n" \
"v_smfmac_f32_16x16x64_bf16 a[48:51], v[244:247], v[64:71], v243\n" \
"s_add_u32 s77, s76, 24576\n" \
"v_mfma_f32_16x16x32_bf16 v[136:139], a[208:211], v[64:67], v[136:139]\n" \
"buffer_load_dwordx4 v198, s[48:51], s76 offen offset:1024 lds\n" \
"v_mfma_f32_16x16x32_bf16 v[136:139], a[212:215], v[68:71], v[136:139]\n" \
"s_lshl_b32 s70, s70, 11\n" \
"ds_read_b128 a[24:27], v232 offset:20736\n" \
"ds_read_b128 a[28:31], v232 offset:22784\n" \
"v_mfma_f32_16x16x32_bf16 v[168:171], a[208:211], v[80:83], v[168:171]\n" \
"s_xor_b32 s80, s80, 0x3\n" \
"v_exp_f32 v107, v107\n" \
"v_mfma_f32_16x16x32_bf16 v[168:171], a[212:215], v[84:87], v[168:171]\n" \
"buffer_load_dwordx4 v199, s[48:51], s76 offen offset:2048 lds\n" \
"v_mfma_f32_16x16x32_bf16 v[172:175], a[216:219], v[80:83], v[172:175]\n" \
"s_add_u32 s93, s93, s70\n" \
"ds_read_b128 v[0:3], v232 offset:24576\n" \
"ds_read_b128 v[4:7], v232 offset:26624\n" \
"v_mfma_f32_16x16x32_bf16 v[172:175], a[220:223], v[84:87], v[172:175]\n" \
"s_add_u32 m0, m0, 3072\n" \
"v_exp_f32 v108, v108\n" \
"v_mfma_f32_16x16x32_bf16 v[140:143], a[216:219], v[64:67], v[140:143]\n" \
"buffer_load_dwordx4 v197, s[48:51], s77 offen offset:0 lds\n" \
"v_mfma_f32_16x16x32_bf16 v[140:143], a[220:223], v[68:71], v[140:143]\n" \
"s_add_u32 s93, s93, 52740096\n" \
"ds_read_b128 v[8:11], v232 offset:28672\n" \
"ds_read_b128 v[12:15], v232 offset:30720\n" \
"v_exp_f32 v109, v109\n" \
"v_smfmac_f32_16x16x64_bf16 a[52:55], v[244:247], v[80:87], v243\n" \
"s_waitcnt lgkmcnt(4)\n" \
"s_cmp_eq_u32 s94, 0\n" \
"s_cselect_b32 s92, -1, 0\n" \
"v_cvt_pk_bf16_f32 v101, v106, v107\n" \
"v_mfma_f32_16x16x32_bf16 v[144:147], a[224:227], v[64:67], v[144:147]\n" \
"buffer_load_dwordx4 v198, s[48:51], s77 offen offset:1024 lds\n" \
"v_mfma_f32_16x16x32_bf16 v[144:147], a[228:231], v[68:71], v[144:147]\n" \
"s_mul_i32 s70, s94, 278528\n" \
"ds_read_b128 v[200:203], v232 offset:24832\n" \
"ds_read_b128 v[204:207], v232 offset:26880\n" \
"v_mfma_f32_16x16x32_bf16 v[176:179], a[224:227], v[80:83], v[176:179]\n" \
"s_add_u32 s93, s93, s70\n" \
"v_exp_f32 v110, v110\n" \
"v_mfma_f32_16x16x32_bf16 v[176:179], a[228:231], v[84:87], v[176:179]\n" \
"buffer_load_dwordx4 v199, s[48:51], s77 offen offset:2048 lds\n" \
"v_mfma_f32_16x16x32_bf16 v[180:183], a[232:235], v[80:83], v[180:183]\n" \
"s_lshr_b32 s70, s61, 1\n" \
"ds_read_b128 a[240:243], v232 offset:28928\n" \
"ds_read_b128 a[244:247], v232 offset:30976\n" \
"v_mfma_f32_16x16x32_bf16 v[180:183], a[236:239], v[84:87], v[180:183]\n" \
"s_lshl_b32 s70, s70, 8\n" \
"v_exp_f32 v111, v111\n" \
"v_mfma_f32_16x16x32_bf16 v[148:151], a[232:235], v[64:67], v[148:151]\n" \
"s_add_u32 s93, s93, s70\n" \
"v_exp_f32 v120, v120\n" \
"v_mfma_f32_16x16x32_bf16 v[148:151], a[236:239], v[68:71], v[148:151]\n" \
"v_cvt_pk_bf16_f32 v102, v108, v109\n" \
"v_cvt_pk_bf16_f32 v103, v110, v111\n" \
"v_mfma_f32_16x16x32_bf16 v[152:155], a[32:35], v[64:67], v[152:155]\n" \
"v_add_u32 v233, 0x10000, v235\n" \
"s_lshl_b32 s72, s61, 14\n" \
"v_mfma_f32_16x16x32_bf16 v[152:155], a[36:39], v[68:71], v[152:155]\n" \
"v_add_u32 v232, s72, v235\n" \
"v_exp_f32 v121, v121\n" \
"v_mfma_f32_16x16x32_bf16 v[184:187], a[32:35], v[80:83], v[184:187]\n" \
"v_exp_f32 v122, v122\n" \
"v_mfma_f32_16x16x32_bf16 v[184:187], a[36:39], v[84:87], v[184:187]\n" \
"v_exp_f32 v123, v123\n" \
"v_mfma_f32_16x16x32_bf16 v[188:191], a[40:43], v[80:83], v[188:191]\n" \
"v_exp_f32 v124, v124\n" \
"v_mfma_f32_16x16x32_bf16 v[188:191], a[44:47], v[84:87], v[188:191]\n" \
"v_cvt_pk_bf16_f32 v116, v120, v121\n" \
"v_cvt_pk_bf16_f32 v117, v122, v123\n" \
"v_mfma_f32_16x16x32_bf16 v[156:159], a[40:43], v[64:67], v[156:159]\n" \
"v_exp_f32 v125, v125\n" \
"v_mfma_f32_16x16x32_bf16 v[156:159], a[44:47], v[68:71], v[156:159]\n" \
"s_cmp_eq_u32 s63, 7\n" \
"s_waitcnt vmcnt(7) lgkmcnt(0)\n" \
"s_barrier\n" \
"s_cbranch_scc1 Lep_%=\n" \
"Lepg_%=:\n" \
"v_mfma_f32_16x16x32_bf16 v[16:19], a[0:3], a[176:179], v[208:211]\n" \
"s_add_u32 m0, s79, 110592\n" \
"ds_read_b128 v[64:67], v233 offset:16384\n" \
"ds_read_b128 v[68:71], v233 offset:24576\n" \
"v_mfma_f32_16x16x32_bf16 v[16:19], a[4:7], a[180:183], v[16:19]\n" \
"buffer_load_dwordx4 v192, s[44:47], s73 offen offset:0 lds\n" \
"v_mfma_f32_16x16x32_bf16 v[16:19], a[8:11], a[184:187], v[16:19]\n" \
"ds_read_b128 v[72:75], v233 offset:17408\n" \
"ds_read_b128 v[76:79], v233 offset:25600\n" \
"v_mfma_f32_16x16x32_bf16 v[16:19], a[12:15], a[188:191], v[16:19]\n" \
"ds_write_b128 v232, v[128:131] offset:0\n" \
"v_exp_f32 v126, v126\n" \
"v_mfma_f32_16x16x32_bf16 v[20:23], a[28:31], a[188:191], v[212:215]\n" \
"ds_read_b128 v[80:83], v233 offset:18432\n" \
"ds_read_b128 v[84:87], v233 offset:26624\n" \
"v_mfma_f32_16x16x32_bf16 v[20:23], a[24:27], a[184:187], v[20:23]\n" \
"buffer_load_dwordx4 v193, s[44:47], s73 offen offset:1024 lds\n" \
"v_mfma_f32_16x16x32_bf16 v[20:23], a[20:23], a[180:183], v[20:23]\n" \
"ds_write_b128 v232, v[132:135] offset:1024\n" \
"v_xor_b32 v248, 0x4000, v232\n" \
"v_mfma_f32_16x16x32_bf16 v[20:23], a[16:19], a[176:179], v[20:23]\n" \
"ds_read_b128 v[88:91], v233 offset:19456\n" \
"ds_read_b128 v[92:95], v233 offset:27648\n" \
"v_mfma_f32_16x16x32_bf16 v[24:27], v[0:3], a[176:179], v[216:219]\n" \
"ds_read_b128 a[248:251], v233 offset:7168\n" \
"ds_read_b128 a[252:255], v233 offset:15360\n" \
"v_mfma_f32_16x16x32_bf16 v[24:27], v[4:7], a[180:183], v[24:27]\n" \
"buffer_load_dwordx4 v192, s[44:47], s73 offen offset:2048 lds\n" \
"v_mfma_f32_16x16x32_bf16 v[24:27], v[8:11], a[184:187], v[24:27]\n" \
"ds_write_b128 v232, v[136:139] offset:2048\n" \
"v_mfma_f32_16x16x32_bf16 v[24:27], v[12:15], a[188:191], v[24:27]\n" \
"v_exp_f32 v127, v127\n" \
"v_cvt_pk_bf16_f32 v118, v124, v125\n" \
"v_mfma_f32_16x16x32_bf16 v[28:31], a[244:247], a[188:191], v[220:223]\n" \
"v_cvt_pk_bf16_f32 v119, v126, v127\n" \
"v_lshrrev_b32 v249, 1, v234\n" \
"v_mfma_f32_16x16x32_bf16 v[28:31], a[240:243], a[184:187], v[28:31]\n" \
"buffer_load_dwordx4 v193, s[44:47], s73 offen offset:3072 lds\n" \
"v_mfma_f32_16x16x32_bf16 v[28:31], v[204:207], a[180:183], v[28:31]\n" \
"ds_write_b128 v232, v[140:143] offset:3072\n" \
"s_lshl_b32 s72, s61, 9\n" \
"s_add_u32 s72, s72, 0x20000\n" \
"v_exp_f32 v32, v32\n" \
"v_exp_f32 v33, v33\n" \
"v_mfma_f32_16x16x32_bf16 v[28:31], v[200:203], a[176:179], v[28:31]\n" \
"s_add_u32 m0, s89, 110592\n" \
"v_exp_f32 v34, v34\n" \
"v_exp_f32 v35, v35\n" \
"v_mfma_f32_16x16x32_bf16 a[64:67], a[192:195], v[96:99], a[64:67]\n" \
"s_xor_b32 s73, s73, 0x180\n" \
"v_exp_f32 v36, v36\n" \
"v_mfma_f32_16x16x32_bf16 a[64:67], a[196:199], v[100:103], a[64:67]\n" \
"buffer_load_dwordx4 v196, s[44:47], s75 offen lds\n" \
"s_add_u32 s72, s72, %3\n" \
"v_cvt_pk_bf16_f32 v32, v32, v33\n" \
"v_exp_f32 v37, v37\n" \
"v_add_u32 v249, s72, v249\n" \
"v_mfma_f32_16x16x32_bf16 a[96:99], a[192:195], v[112:115], a[96:99]\n" \
"ds_write_b128 v232, v[144:147] offset:4096\n" \
"v_mfma_f32_16x16x32_bf16 a[96:99], a[196:199], v[116:119], a[96:99]\n" \
"ds_read_b128 a[192:195], v233 offset:0\n" \
"ds_read_b128 a[196:199], v233 offset:8192\n" \
"v_exp_f32 v38, v38\n" \
"v_cvt_pk_bf16_f32 v33, v34, v35\n" \
"v_accvgpr_read_b32 v250, a48\n" \
"v_mfma_f32_16x16x32_bf16 a[100:103], a[200:203], v[112:115], a[100:103]\n" \
"s_xor_b32 s75, s75, 0x180\n" \
"v_exp_f32 v39, v39\n" \
"v_mfma_f32_16x16x32_bf16 a[100:103], a[204:207], v[116:119], a[100:103]\n" \
"v_exp_f32 v48, v48\n" \
"v_cvt_pk_bf16_f32 v34, v36, v37\n" \
"v_exp_f32 v49, v49\n" \
"v_mfma_f32_16x16x32_bf16 a[68:71], a[200:203], v[96:99], a[68:71]\n" \
"ds_write_b128 v232, v[148:151] offset:5120\n" \
"v_mfma_f32_16x16x32_bf16 a[68:71], a[204:207], v[100:103], a[68:71]\n" \
"ds_read_b128 a[200:203], v233 offset:1024\n" \
"ds_read_b128 a[204:207], v233 offset:9216\n" \
"v_exp_f32 v50, v50\n" \
"v_cvt_pk_bf16_f32 v35, v38, v39\n" \
"v_smfmac_f32_16x16x64_bf16 v[224:227], v[244:247], v[96:103], v243\n" \
"v_cvt_pk_bf16_f32 v48, v48, v49\n" \
"v_mfma_f32_16x16x32_bf16 a[72:75], a[208:211], v[96:99], a[72:75]\n" \
"v_exp_f32 v51, v51\n" \
"v_mfma_f32_16x16x32_bf16 a[72:75], a[212:215], v[100:103], a[72:75]\n" \
"ds_write_b128 v232, v[152:155] offset:6144\n" \
"v_exp_f32 v52, v52\n" \
"v_exp_f32 v53, v53\n" \
"v_accvgpr_read_b32 v240, a49\n" \
"v_mfma_f32_16x16x32_bf16 a[104:107], a[208:211], v[112:115], a[104:107]\n" \
"v_exp_f32 v54, v54\n" \
"v_mfma_f32_16x16x32_bf16 a[104:107], a[212:215], v[116:119], a[104:107]\n" \
"ds_read_b128 a[208:211], v233 offset:2048\n" \
"ds_read_b128 a[212:215], v233 offset:10240\n" \
"v_cvt_pk_bf16_f32 v49, v50, v51\n" \
"v_exp_f32 v55, v55\n" \
"v_mfma_f32_16x16x32_bf16 a[108:111], a[216:219], v[112:115], a[108:111]\n" \
"v_exp_f32 v40, v40\n" \
"v_mfma_f32_16x16x32_bf16 a[108:111], a[220:223], v[116:119], a[108:111]\n" \
"ds_write_b128 v232, v[156:159] offset:7168\n" \
"v_cvt_pk_bf16_f32 v50, v52, v53\n" \
"v_exp_f32 v41, v41\n" \
"v_mfma_f32_16x16x32_bf16 a[76:79], a[216:219], v[96:99], a[76:79]\n" \
"v_exp_f32 v42, v42\n" \
"v_mfma_f32_16x16x32_bf16 a[76:79], a[220:223], v[100:103], a[76:79]\n" \
"ds_read_b128 a[216:219], v233 offset:3072\n" \
"ds_read_b128 a[220:223], v233 offset:11264\n" \
"v_cvt_pk_bf16_f32 v51, v54, v55\n" \
"v_exp_f32 v43, v43\n" \
"v_smfmac_f32_16x16x64_bf16 v[228:231], v[244:247], v[112:119], v243\n" \
"ds_write_b128 v232, v[160:163] offset:8192\n" \
"v_exp_f32 v44, v44\n" \
"v_add_f32 v250, v250, v240\n" \
"v_mfma_f32_16x16x32_bf16 a[80:83], a[224:227], v[96:99], a[80:83]\n" \
"v_cvt_pk_bf16_f32 v36, v40, v41\n" \
"v_cvt_pk_bf16_f32 v37, v42, v43\n" \
"v_mfma_f32_16x16x32_bf16 a[80:83], a[228:231], v[100:103], a[80:83]\n" \
"v_exp_f32 v45, v45\n" \
"v_exp_f32 v46, v46\n" \
"v_mfma_f32_16x16x32_bf16 a[112:115], a[224:227], v[112:115], a[112:115]\n" \
"Lpg7293_%=:\n" \
"s_cbranch_execz Lcb0_%=\n" \
"v_exp_f32 v47, v47\n" \
"v_mfma_f32_16x16x32_bf16 a[112:115], a[228:231], v[116:119], a[112:115]\n" \
"ds_write_b128 v232, v[164:167] offset:9216\n" \
"v_exp_f32 v56, v56\n" \
"v_cvt_pk_bf16_f32 v38, v44, v45\n" \
"v_mfma_f32_16x16x32_bf16 a[116:119], a[232:235], v[112:115], a[116:119]\n" \
"ds_read_b128 a[224:227], v233 offset:4096\n" \
"ds_read_b128 a[228:231], v233 offset:12288\n" \
"v_mfma_f32_16x16x32_bf16 a[116:119], a[236:239], v[116:119], a[116:119]\n" \
"v_exp_f32 v57, v57\n" \
"v_exp_f32 v58, v58\n" \
"v_cvt_pk_bf16_f32 v39, v46, v47\n" \
"v_accvgpr_read_b32 v251, a52\n" \
"v_mfma_f32_16x16x32_bf16 a[84:87], a[232:235], v[96:99], a[84:87]\n" \
"v_exp_f32 v59, v59\n" \
"v_mfma_f32_16x16x32_bf16 a[84:87], a[236:239], v[100:103], a[84:87]\n" \
"ds_write_b128 v232, v[168:171] offset:10240\n" \
"v_exp_f32 v60, v60\n" \
"v_cvt_pk_bf16_f32 v52, v56, v57\n" \
"v_mfma_f32_16x16x32_bf16 a[88:91], a[32:35], v[96:99], a[88:91]\n" \
"ds_read_b128 a[232:235], v233 offset:5120\n" \
"ds_read_b128 a[236:239], v233 offset:13312\n" \
"v_mfma_f32_16x16x32_bf16 a[88:91], a[36:39], v[100:103], a[88:91]\n" \
"ds_read_b128 a[240:243], v233 offset:6144\n" \
"ds_read_b128 a[244:247], v233 offset:14336\n" \
"v_exp_f32 v61, v61\n" \
"v_exp_f32 v62, v62\n" \
"v_mfma_f32_16x16x32_bf16 a[120:123], a[32:35], v[112:115], a[120:123]\n" \
"ds_read_b128 v[0:3], v233 offset:20480\n" \
"ds_read_b128 v[4:7], v233 offset:28672\n" \
"v_mfma_f32_16x16x32_bf16 a[120:123], a[36:39], v[116:119], a[120:123]\n" \
"ds_write_b128 v232, v[172:175] offset:11264\n" \
"v_cvt_pk_bf16_f32 v53, v58, v59\n" \
"v_exp_f32 v63, v63\n" \
"v_mfma_f32_16x16x32_bf16 a[124:127], a[40:43], v[112:115], a[124:127]\n" \
"ds_read_b128 v[8:11], v233 offset:21504\n" \
"ds_read_b128 v[12:15], v233 offset:29696\n" \
"v_mfma_f32_16x16x32_bf16 a[124:127], a[44:47], v[116:119], a[124:127]\n" \
"v_cvt_pk_bf16_f32 v54, v60, v61\n" \
"v_accvgpr_read_b32 v240, a53\n" \
"v_add_f32 v251, v251, v240\n" \
"v_mfma_f32_16x16x32_bf16 a[92:95], a[40:43], v[96:99], a[92:95]\n" \
"s_waitcnt lgkmcnt(5)\n" \
"v_cvt_pk_bf16_f32 v55, v62, v63\n" \
"v_mfma_f32_16x16x32_bf16 a[92:95], a[44:47], v[100:103], a[92:95]\n" \
"ds_write_b128 v232, v[176:179] offset:12288\n" \
"ds_write_b64 v249, v[250:251]\n" \
"v_mfma_f32_16x16x32_bf16 a[64:67], a[192:195], v[32:35], a[64:67]\n" \
"ds_read_b128 v[96:99], v233 offset:22528\n" \
"ds_read_b128 v[100:103], v233 offset:30720\n" \
"v_mfma_f32_16x16x32_bf16 a[64:67], a[196:199], v[36:39], a[64:67]\n" \
"ds_read_b128 v[104:107], v233 offset:23552\n" \
"ds_read_b128 v[108:111], v233 offset:31744\n" \
"v_exp_f32 v16, v16\n" \
"v_mfma_f32_16x16x32_bf16 a[96:99], a[192:195], v[48:51], a[96:99]\n" \
"v_exp_f32 v17, v17\n" \
"v_mfma_f32_16x16x32_bf16 a[96:99], a[196:199], v[52:55], a[96:99]\n" \
"ds_write_b128 v232, v[180:183] offset:13312\n" \
"v_exp_f32 v18, v18\n" \
"v_xor_b32 v249, 0x200, v249\n" \
"v_accvgpr_read_b32 v200, a59\n" \
"v_mfma_f32_16x16x32_bf16 a[100:103], a[200:203], v[48:51], a[100:103]\n" \
"v_exp_f32 v19, v19\n" \
"v_mfma_f32_16x16x32_bf16 a[100:103], a[204:207], v[52:55], a[100:103]\n" \
"v_exp_f32 v20, v20\n" \
"v_cvt_pk_bf16_f32 v16, v16, v17\n" \
"v_accvgpr_read_b32 v202, a60\n" \
"v_accvgpr_read_b32 v204, a61\n" \
"v_mfma_f32_16x16x32_bf16 a[68:71], a[200:203], v[32:35], a[68:71]\n" \
"v_exp_f32 v21, v21\n" \
"v_mfma_f32_16x16x32_bf16 a[68:71], a[204:207], v[36:39], a[68:71]\n" \
"ds_write_b128 v232, v[184:187] offset:14336\n" \
"v_exp_f32 v22, v22\n" \
"v_xor_b32 v201, 64, v200\n" \
"v_xor_b32 v203, 64, v202\n" \
"v_add_u32 v205, 110592, v200\n" \
"v_smfmac_f32_16x16x64_bf16 v[224:227], v[244:247], v[32:39], v243\n" \
"v_cvt_pk_bf16_f32 v17, v18, v19\n" \
"v_add_u32 v206, 110592, v201\n" \
"v_mfma_f32_16x16x32_bf16 a[72:75], a[208:211], v[32:35], a[72:75]\n" \
"v_exp_f32 v23, v23\n" \
"v_mfma_f32_16x16x32_bf16 a[72:75], a[212:215], v[36:39], a[72:75]\n" \
"v_exp_f32 v24, v24\n" \
"v_cvt_pk_bf16_f32 v18, v20, v21\n" \
"v_add_u32 v207, 110592, v202\n" \
"v_add_u32 v208, 110592, v203\n" \
"v_add_u32 v209, 139264, v204\n" \
"v_mfma_f32_16x16x32_bf16 a[104:107], a[208:211], v[48:51], a[104:107]\n" \
"ds_write_b128 v232, v[188:191] offset:15360\n" \
"v_exp_f32 v25, v25\n" \
"v_xor_b32 v210, 32, v209\n" \
"v_mfma_f32_16x16x32_bf16 a[104:107], a[212:215], v[52:55], a[104:107]\n" \
"v_exp_f32 v26, v26\n" \
"v_cvt_pk_bf16_f32 v19, v22, v23\n" \
"v_mfma_f32_16x16x32_bf16 a[108:111], a[216:219], v[48:51], a[108:111]\n" \
"s_mul_i32 s76, s80, 393216\n" \
"s_add_u32 s76, s76, s78\n" \
"v_cvt_pk_bf16_f32 v20, v24, v25\n" \
"v_mfma_f32_16x16x32_bf16 a[108:111], a[220:223], v[52:55], a[108:111]\n" \
"s_waitcnt vmcnt(0) lgkmcnt(0)\n" \
"s_barrier\n" \
"v_mfma_f32_16x16x32_bf16 a[76:79], a[216:219], v[32:35], a[76:79]\n" \
"s_add_u32 m0, s91, 86016\n" \
"ds_read_b64_tr_b16 a[32:33], v209 offset:1024\n" \
"ds_read_b64_tr_b16 a[34:35], v209 offset:4096\n" \
"v_mfma_f32_16x16x32_bf16 a[76:79], a[220:223], v[36:39], a[76:79]\n" \
"buffer_load_dwordx4 v197, s[48:51], s76 offen offset:0 lds\n" \
"v_exp_f32 v27, v27\n" \
"v_smfmac_f32_16x16x64_bf16 v[228:231], v[244:247], v[48:55], v243\n" \
"s_and_b32 s15, %1, 7\n" \
"ds_read_b64_tr_b16 a[36:37], v209 offset:13312\n" \
"ds_read_b64_tr_b16 a[38:39], v209 offset:16384\n" \
"v_exp_f32 v28, v28\n" \
"v_mfma_f32_16x16x32_bf16 a[80:83], a[224:227], v[32:35], a[80:83]\n" \
"s_add_u32 s77, s76, 24576\n" \
"ds_read_b64_tr_b16 a[40:41], v209 offset:0\n" \
"ds_read_b64_tr_b16 a[42:43], v209 offset:3072\n" \
"v_mfma_f32_16x16x32_bf16 a[80:83], a[228:231], v[36:39], a[80:83]\n" \
"buffer_load_dwordx4 v198, s[48:51], s76 offen offset:1024 lds\n" \
"v_mfma_f32_16x16x32_bf16 a[112:115], a[224:227], v[48:51], a[112:115]\n" \
"s_lshl3_add_u32 s14, s14, s15\n" \
"ds_read_b64_tr_b16 a[44:45], v209 offset:12288\n" \
"ds_read_b64_tr_b16 a[46:47], v209 offset:15360\n" \
"v_mfma_f32_16x16x32_bf16 a[112:115], a[228:231], v[52:55], a[112:115]\n" \
"s_xor_b32 s80, s80, 0x8\n" \
"ds_read_b64_tr_b16 a[48:49], v210 offset:0\n" \
"ds_read_b64_tr_b16 a[50:51], v210 offset:3072\n" \
"v_exp_f32 v29, v29\n" \
"v_mfma_f32_16x16x32_bf16 a[116:119], a[232:235], v[48:51], a[116:119]\n" \
"buffer_load_dwordx4 v199, s[48:51], s76 offen offset:2048 lds\n" \
"v_mfma_f32_16x16x32_bf16 a[116:119], a[236:239], v[52:55], a[116:119]\n" \
"ds_read_b64_tr_b16 a[52:53], v210 offset:12288\n" \
"ds_read_b64_tr_b16 a[54:55], v210 offset:15360\n" \
"v_exp_f32 v30, v30\n" \
"v_mfma_f32_16x16x32_bf16 a[84:87], a[232:235], v[32:35], a[84:87]\n" \
"s_add_u32 m0, m0, 3072\n" \
"ds_read_b128 a[0:3], v207 offset:0\n" \
"ds_read_b128 a[4:7], v208 offset:0\n" \
"v_mfma_f32_16x16x32_bf16 a[84:87], a[236:239], v[36:39], a[84:87]\n" \
"buffer_load_dwordx4 v197, s[48:51], s77 offen offset:0 lds\n" \
"v_cvt_pk_bf16_f32 v21, v26, v27\n" \
"v_mfma_f32_16x16x32_bf16 a[88:91], a[240:243], v[32:35], a[88:91]\n" \
"ds_read_b128 a[8:11], v205 offset:0\n" \
"ds_read_b128 a[12:15], v206 offset:0\n" \
"v_mfma_f32_16x16x32_bf16 a[88:91], a[244:247], v[36:39], a[88:91]\n" \
"ds_read_b128 a[16:19], v205 offset:4096\n" \
"ds_read_b128 a[20:23], v206 offset:4096\n" \
"v_exp_f32 v31, v31\n" \
"v_mfma_f32_16x16x32_bf16 a[120:123], a[240:243], v[48:51], a[120:123]\n" \
"buffer_load_dwordx4 v198, s[48:51], s77 offen offset:1024 lds\n" \
"v_mfma_f32_16x16x32_bf16 a[120:123], a[244:247], v[52:55], a[120:123]\n" \
"ds_read_b128 a[24:27], v205 offset:8192\n" \
"ds_read_b128 a[28:31], v206 offset:8192\n" \
"v_cvt_pk_bf16_f32 v22, v28, v29\n" \
"v_mfma_f32_16x16x32_bf16 a[124:127], a[248:251], v[48:51], a[124:127]\n" \
"ds_read_b64 v[250:251], v249\n" \
"ds_read_b128 v[128:131], v248 offset:0\n" \
"v_mfma_f32_16x16x32_bf16 a[124:127], a[252:255], v[52:55], a[124:127]\n" \
"buffer_load_dwordx4 v199, s[48:51], s77 offen offset:2048 lds\n" \
"v_cvt_pk_bf16_f32 v23, v30, v31\n" \
"v_mfma_f32_16x16x32_bf16 a[92:95], a[248:251], v[32:35], a[92:95]\n" \
"ds_read_b128 v[132:135], v248 offset:1024\n" \
"ds_read_b128 v[136:139], v248 offset:2048\n" \
"v_mfma_f32_16x16x32_bf16 a[92:95], a[252:255], v[36:39], a[92:95]\n" \
"s_add_u32 m0, s79, 65536\n" \
"ds_read_b128 v[140:143], v248 offset:3072\n" \
"ds_read_b128 v[144:147], v248 offset:4096\n" \
"v_mfma_f32_16x16x32_bf16 a[96:99], v[64:67], v[16:19], a[96:99]\n" \
"buffer_load_dwordx4 v192, s[44:47], s73 offen offset:0 lds\n" \
"v_mfma_f32_16x16x32_bf16 a[96:99], v[68:71], v[20:23], a[96:99]\n" \
"ds_read_b128 v[148:151], v248 offset:5120\n" \
"ds_read_b128 v[152:155], v248 offset:6144\n" \
"v_mfma_f32_16x16x32_bf16 a[100:103], v[72:75], v[16:19], a[100:103]\n" \
"ds_read_b128 v[156:159], v248 offset:7168\n" \
"ds_read_b128 v[160:163], v248 offset:8192\n" \
"v_mfma_f32_16x16x32_bf16 a[100:103], v[76:79], v[20:23], a[100:103]\n" \
"buffer_load_dwordx4 v193, s[44:47], s73 offen offset:1024 lds\n" \
"v_smfmac_f32_16x16x64_bf16 v[228:231], v[244:247], v[16:23], v243\n" \
"ds_read_b128 v[164:167], v248 offset:9216\n" \
"ds_read_b128 v[168:171], v248 offset:10240\n" \
"v_mfma_f32_16x16x32_bf16 a[104:107], v[80:83], v[16:19], a[104:107]\n" \
"ds_read_b128 v[172:175], v248 offset:11264\n" \
"ds_read_b128 v[176:179], v248 offset:12288\n" \
"v_mfma_f32_16x16x32_bf16 a[104:107], v[84:87], v[20:23], a[104:107]\n" \
"buffer_load_dwordx4 v192, s[44:47], s73 offen offset:2048 lds\n" \
"v_mfma_f32_16x16x32_bf16 a[108:111], v[88:91], v[16:19], a[108:111]\n" \
"ds_read_b128 v[180:183], v248 offset:13312\n" \
"ds_read_b128 v[184:187], v248 offset:14336\n" \
"v_mfma_f32_16x16x32_bf16 a[108:111], v[92:95], v[20:23], a[108:111]\n" \
"ds_read_b128 v[188:191], v248 offset:15360\n" \
"v_mov_b32 v218, s60\n" \
"v_mfma_f32_16x16x32_bf16 a[112:115], v[0:3], v[16:19], a[112:115]\n" \
"buffer_load_dwordx4 v193, s[44:47], s73 offen offset:3072 lds\n" \
"v_mfma_f32_16x16x32_bf16 a[112:115], v[4:7], v[20:23], a[112:115]\n" \
"s_add_u32 m0, s89, 65536\n" \
"v_mov_b32 v219, s14\n" \
"v_lshlrev_b32 v220, 28, s61\n" \
"v_mfma_f32_16x16x32_bf16 a[116:119], v[8:11], v[16:19], a[116:119]\n" \
"s_xor_b32 s73, s73, 0x400\n" \
"v_mfma_f32_16x16x32_bf16 a[116:119], v[12:15], v[20:23], a[116:119]\n" \
"buffer_load_dwordx4 v196, s[44:47], s75 offen lds\n" \
"v_mfma_f32_16x16x32_bf16 a[120:123], v[96:99], v[16:19], a[120:123]\n" \
"s_lshl3_add_u32 s15, s15, 65593856\n" \
"v_mfma_f32_16x16x32_bf16 a[120:123], v[100:103], v[20:23], a[120:123]\n" \
"s_lshr_b32 s14, %1, 3\n" \
"s_cmp_eq_u32 s14, 3\n" \
"s_cselect_b32 s15, s15, 0x20000000\n" \
"buffer_store_dwordx2 v[218:219], v220, s[56:59], s15 offen sc1\n" \
"v_mfma_f32_16x16x32_bf16 a[124:127], v[104:107], v[16:19], a[124:127]\n" \
"s_xor_b32 s75, s75, 0x400\n" \
"v_mfma_f32_16x16x32_bf16 a[124:127], v[108:111], v[20:23], a[124:127]\n" \
"s_waitcnt lgkmcnt(0)\n" \
"s_barrier\n" \
"v_mfma_f32_16x16x32_bf16 v[32:35], a[32:35], a[0:3], 0\n" \
"s_add_u32 m0, s79, 0\n" \
"ds_read_b128 a[192:195], v205 offset:12288\n" \
"v_mfma_f32_16x16x32_bf16 v[32:35], a[36:39], a[4:7], v[32:35]\n" \
"buffer_load_dwordx4 v192, s[44:47], s73 offen offset:0 lds\n" \
"v_mfma_f32_16x16x32_bf16 v[40:43], a[32:35], a[8:11], 0\n" \
"ds_read_b128 a[196:199], v206 offset:12288\n" \
"v_add_f32 v248, v224, v225\n" \
"v_mfma_f32_16x16x32_bf16 v[40:43], a[36:39], a[12:15], v[40:43]\n" \
"buffer_load_dwordx4 v193, s[44:47], s73 offen offset:1024 lds\n" \
"v_mfma_f32_16x16x32_bf16 v[0:3], a[40:43], a[8:11], 0\n" \
"ds_read_b64_tr_b16 a[200:201], v210 offset:1024\n" \
"ds_read_b64_tr_b16 a[202:203], v210 offset:4096\n" \
"v_mfma_f32_16x16x32_bf16 v[0:3], a[44:47], a[12:15], v[0:3]\n" \
"buffer_load_dwordx4 v192, s[44:47], s73 offen offset:2048 lds\n" \
"v_add_f32 v249, v228, v229\n" \
"v_mfma_f32_16x16x32_bf16 v[4:7], a[48:51], a[8:11], 0\n" \
"ds_read_b64_tr_b16 a[204:205], v210 offset:13312\n" \
"ds_read_b64_tr_b16 a[206:207], v210 offset:16384\n" \
"v_mfma_f32_16x16x32_bf16 v[4:7], a[52:55], a[12:15], v[4:7]\n" \
"buffer_load_dwordx4 v193, s[44:47], s73 offen offset:3072 lds\n" \
"v_mfma_f32_16x16x32_bf16 v[12:15], a[48:51], a[16:19], 0\n" \
"s_add_u32 m0, s89, 0\n" \
"s_xor_b32 s73, s73, 0x500\n" \
"ds_read_b64_tr_b16 a[208:209], v209 offset:2048\n" \
"ds_read_b64_tr_b16 a[210:211], v209 offset:5120\n" \
"v_add_f32 v248, v248, v250\n" \
"v_mfma_f32_16x16x32_bf16 v[12:15], a[52:55], a[20:23], v[12:15]\n" \
"buffer_load_dwordx4 v196, s[44:47], s75 offen lds\n" \
"v_mfma_f32_16x16x32_bf16 v[8:11], a[40:43], a[16:19], 0\n" \
"s_xor_b32 s75, s75, 0x500\n" \
"s_mul_i32 s76, s80, 393216\n" \
"s_add_u32 s76, s76, s78\n" \
"s_add_u32 m0, s91, 20480\n" \
"ds_read_b64_tr_b16 a[212:213], v209 offset:14336\n" \
"ds_read_b64_tr_b16 a[214:215], v209 offset:17408\n" \
"v_mfma_f32_16x16x32_bf16 v[8:11], a[44:47], a[20:23], v[8:11]\n" \
"buffer_load_dwordx4 v197, s[48:51], s76 offen offset:0 lds\n" \
"v_add_f32 v249, v249, v251\n" \
"v_mfma_f32_16x16x32_bf16 v[48:51], a[32:35], a[16:19], 0\n" \
"s_add_u32 s77, s76, 24576\n" \
"ds_read_b64_tr_b16 a[216:217], v210 offset:2048\n" \
"v_mfma_f32_16x16x32_bf16 v[48:51], a[36:39], a[20:23], v[48:51]\n" \
"buffer_load_dwordx4 v198, s[48:51], s76 offen offset:1024 lds\n" \
"v_mfma_f32_16x16x32_bf16 v[56:59], a[32:35], a[24:27], 0\n" \
"s_xor_b32 s80, s80, 0xa\n" \
"ds_read_b64_tr_b16 a[218:219], v210 offset:5120\n" \
"v_mfma_f32_16x16x32_bf16 v[56:59], a[36:39], a[28:31], v[56:59]\n" \
"buffer_load_dwordx4 v199, s[48:51], s76 offen offset:2048 lds\n" \
"v_mfma_f32_16x16x32_bf16 v[16:19], a[40:43], a[24:27], 0\n" \
"s_add_u32 m0, m0, 3072\n" \
"ds_read_b64_tr_b16 a[220:221], v210 offset:14336\n" \
"v_mfma_f32_16x16x32_bf16 v[16:19], a[44:47], a[28:31], v[16:19]\n" \
"buffer_load_dwordx4 v197, s[48:51], s77 offen offset:0 lds\n" \
"v_mfma_f32_16x16x32_bf16 v[20:23], a[48:51], a[24:27], 0\n" \
"s_waitcnt lgkmcnt(2)\n" \
"ds_read_b64_tr_b16 a[222:223], v210 offset:17408\n" \
"v_mfma_f32_16x16x32_bf16 v[20:23], a[52:55], a[28:31], v[20:23]\n" \
"buffer_load_dwordx4 v198, s[48:51], s77 offen offset:1024 lds\n" \
"v_rcp_f32 v232, v248\n" \
"v_mfma_f32_16x16x32_bf16 v[28:31], a[48:51], a[192:195], 0\n" \
"v_rcp_f32 v233, v249\n" \
"v_mfma_f32_16x16x32_bf16 v[28:31], a[52:55], a[196:199], v[28:31]\n" \
"buffer_load_dwordx4 v199, s[48:51], s77 offen offset:2048 lds\n" \
"v_add_u32 v240, 0x1000, v236\n" \
"v_mfma_f32_16x16x32_bf16 v[24:27], a[40:43], a[192:195], 0\n" \
"v_add_u32 v205, 65536, v200\n" \
"v_add_u32 v206, 65536, v201\n" \
"v_mfma_f32_16x16x32_bf16 v[24:27], a[44:47], a[196:199], v[24:27]\n" \
"v_add_u32 v207, 65536, v202\n" \
"v_add_u32 v208, 65536, v203\n" \
"v_mfma_f32_16x16x32_bf16 v[64:67], a[32:35], a[192:195], 0\n" \
"v_add_u32 v209, 86016, v204\n" \
"v_xor_b32 v210, 32, v209\n" \
"v_mfma_f32_16x16x32_bf16 v[64:67], a[36:39], a[196:199], v[64:67]\n" \
"v_add_f32 v224, v248, v249\n" \
"s_waitcnt vmcnt(12) lgkmcnt(0)\n" \
"s_barrier\n" \
"v_mfma_f32_16x16x32_bf16 v[72:75], a[0:3], a[208:211], 0\n" \
"ds_read_b64_tr_b16 a[32:33], v209 offset:1024\n" \
"ds_read_b64_tr_b16 a[34:35], v209 offset:4096\n" \
"v_mfma_f32_16x16x32_bf16 v[72:75], a[4:7], a[212:215], v[72:75]\n" \
"ds_read_b64_tr_b16 a[36:37], v209 offset:13312\n" \
"ds_read_b64_tr_b16 a[38:39], v209 offset:16384\n" \
"v_mfma_f32_16x16x32_bf16 v[80:83], a[8:11], a[208:211], 0\n" \
"s_add_u32 m0, s79, 110592\n" \
"ds_read_b128 a[0:3], v207 offset:0\n" \
"v_mul_f32 v224, 0x43800000, v224\n" \
"v_mfma_f32_16x16x32_bf16 v[80:83], a[12:15], a[212:215], v[80:83]\n" \
"buffer_load_dwordx4 v192, s[44:47], s73 offen offset:0 lds\n" \
"v_add_f32 v248, v232, v233\n" \
"v_mfma_f32_16x16x32_bf16 v[84:87], a[8:11], a[216:219], 0\n" \
"ds_read_b128 a[4:7], v208 offset:0\n" \
"v_fmac_f32 v224, 0x5f800000, v248\n" \
"v_mfma_f32_16x16x32_bf16 v[84:87], a[12:15], a[220:223], v[84:87]\n" \
"ds_read_b64_tr_b16 a[40:41], v209 offset:0\n" \
"v_mov_b32 v225, 0\n" \
"v_mov_b32 v226, 0\n" \
"v_mfma_f32_16x16x32_bf16 v[44:47], a[200:203], a[8:11], 0\n" \
"ds_read_b64_tr_b16 a[42:43], v209 offset:3072\n" \
"v_mov_b32 v227, 0\n" \
"v_mfma_f32_16x16x32_bf16 v[44:47], a[204:207], a[12:15], v[44:47]\n" \
"buffer_load_dwordx4 v193, s[44:47], s73 offen offset:1024 lds\n" \
"v_mfma_f32_16x16x32_bf16 v[52:55], a[200:203], a[16:19], 0\n" \
"ds_read_b128 a[8:11], v205 offset:0\n" \
"v_accvgpr_read_b32 v112, a64\n" \
"v_mfma_f32_16x16x32_bf16 v[52:55], a[204:207], a[20:23], v[52:55]\n" \
"ds_read_b128 a[12:15], v206 offset:0\n" \
"v_accvgpr_read_b32 v113, a65\n" \
"v_accvgpr_read_b32 v114, a66\n" \
"v_mfma_f32_16x16x32_bf16 v[92:95], a[16:19], a[216:219], 0\n" \
"ds_read_b64_tr_b16 a[44:45], v209 offset:12288\n" \
"v_accvgpr_read_b32 v115, a67\n" \
"v_mfma_f32_16x16x32_bf16 v[92:95], a[20:23], a[220:223], v[92:95]\n" \
"buffer_load_dwordx4 v192, s[44:47], s73 offen offset:2048 lds\n" \
"v_mfma_f32_16x16x32_bf16 v[88:91], a[16:19], a[208:211], 0\n" \
"ds_read_b64_tr_b16 a[46:47], v209 offset:15360\n" \
"v_accvgpr_read_b32 v116, a68\n" \
"v_mfma_f32_16x16x32_bf16 v[88:91], a[20:23], a[212:215], v[88:91]\n" \
"ds_read_b64_tr_b16 a[48:49], v210 offset:0\n" \
"v_accvgpr_read_b32 v117, a69\n" \
"v_accvgpr_read_b32 v118, a70\n" \
"v_mfma_f32_16x16x32_bf16 v[96:99], a[24:27], a[208:211], 0\n" \
"ds_read_b128 a[16:19], v205 offset:4096\n" \
"v_accvgpr_read_b32 v119, a71\n" \
"v_mfma_f32_16x16x32_bf16 v[96:99], a[28:31], a[212:215], v[96:99]\n" \
"buffer_load_dwordx4 v193, s[44:47], s73 offen offset:3072 lds\n" \
"v_add_f32 v112, v112, v128\n" \
"v_mfma_f32_16x16x32_bf16 v[100:103], a[24:27], a[216:219], 0\n" \
"ds_read_b128 a[20:23], v206 offset:4096\n" \
"v_add_f32 v113, v113, v129\n" \
"v_mfma_f32_16x16x32_bf16 v[100:103], a[28:31], a[220:223], v[100:103]\n" \
"s_add_u32 m0, s89, 110592\n" \
"ds_read_b64_tr_b16 a[50:51], v210 offset:3072\n" \
"v_add_f32 v114, v114, v130\n" \
"v_mfma_f32_16x16x32_bf16 v[60:63], a[200:203], a[24:27], 0\n" \
"s_xor_b32 s73, s73, 0x180\n" \
"ds_read_b64_tr_b16 a[52:53], v210 offset:12288\n" \
"v_add_f32 v115, v115, v131\n" \
"v_mfma_f32_16x16x32_bf16 v[60:63], a[204:207], a[28:31], v[60:63]\n" \
"buffer_load_dwordx4 v196, s[44:47], s75 offen lds\n" \
"v_add_f32 v116, v116, v132\n" \
"v_mfma_f32_16x16x32_bf16 v[68:71], a[200:203], a[192:195], 0\n" \
"s_xor_b32 s75, s75, 0x180\n" \
"s_mul_i32 s76, s80, 393216\n" \
"ds_read_b128 a[24:27], v205 offset:8192\n" \
"v_mfma_f32_16x16x32_bf16 v[68:71], a[204:207], a[196:199], v[68:71]\n" \
"s_add_u32 s76, s76, s78\n" \
"ds_read_b128 a[28:31], v206 offset:8192\n" \
"v_add_f32 v117, v117, v133\n" \
"v_mfma_f32_16x16x32_bf16 v[108:111], a[192:195], a[216:219], 0\n" \
"s_add_u32 m0, s91, 139264\n" \
"ds_read_b64_tr_b16 a[54:55], v210 offset:15360\n" \
"v_add_f32 v118, v118, v134\n" \
"v_mfma_f32_16x16x32_bf16 v[108:111], a[196:199], a[220:223], v[108:111]\n" \
"buffer_load_dwordx4 v197, s[48:51], s76 offen offset:0 lds\n" \
"v_add_f32 v119, v119, v135\n" \
"v_mfma_f32_16x16x32_bf16 v[104:107], a[192:195], a[208:211], 0\n" \
"v_mul_f32 v112, v112, v232\n" \
"v_mul_f32 v113, v113, v232\n" \
"v_mfma_f32_16x16x32_bf16 v[104:107], a[196:199], a[212:215], v[104:107]\n" \
"s_waitcnt lgkmcnt(2)\n" \
"v_mul_f32 v114, v114, v232\n" \
"v_mul_f32 v115, v115, v232\n" \
"v_mul_f32 v116, v116, v232\n" \
"v_mfma_f32_16x16x32_bf16 v[32:35], a[32:35], a[0:3], v[32:35]\n" \
"s_add_u32 s77, s76, 24576\n" \
"ds_read_b128 a[192:195], v205 offset:12288\n" \
"v_mul_f32 v117, v117, v232\n" \
"v_mfma_f32_16x16x32_bf16 v[32:35], a[36:39], a[4:7], v[32:35]\n" \
"buffer_load_dwordx4 v198, s[48:51], s76 offen offset:1024 lds\n" \
"v_mfma_f32_16x16x32_bf16 v[40:43], a[32:35], a[8:11], v[40:43]\n" \
"ds_read_b128 a[196:199], v206 offset:12288\n" \
"v_mul_f32 v118, v118, v232\n" \
"v_mfma_f32_16x16x32_bf16 v[40:43], a[36:39], a[12:15], v[40:43]\n" \
"s_waitcnt lgkmcnt(2)\n" \
"ds_read_b64_tr_b16 a[200:201], v210 offset:1024\n" \
"v_mul_f32 v119, v119, v232\n" \
"v_cvt_pk_bf16_f32 v120, v112, v113\n" \
"v_mfma_f32_16x16x32_bf16 v[0:3], a[40:43], a[8:11], v[0:3]\n" \
"s_xor_b32 s80, s80, 0x3\n" \
"ds_read_b64_tr_b16 a[202:203], v210 offset:4096\n" \
"v_cvt_pk_bf16_f32 v121, v114, v115\n" \
"v_mfma_f32_16x16x32_bf16 v[0:3], a[44:47], a[12:15], v[0:3]\n" \
"buffer_load_dwordx4 v199, s[48:51], s76 offen offset:2048 lds\n" \
"v_mfma_f32_16x16x32_bf16 v[4:7], a[48:51], a[8:11], v[4:7]\n" \
"ds_read_b64_tr_b16 a[204:205], v210 offset:13312\n" \
"v_cvt_pk_bf16_f32 v122, v116, v117\n" \
"v_mfma_f32_16x16x32_bf16 v[4:7], a[52:55], a[12:15], v[4:7]\n" \
"ds_read_b64_tr_b16 a[206:207], v210 offset:16384\n" \
"v_cvt_pk_bf16_f32 v123, v118, v119\n" \
"v_mfma_f32_16x16x32_bf16 v[12:15], a[48:51], a[16:19], v[12:15]\n" \
"s_add_u32 m0, m0, 3072\n" \
"ds_read_b64_tr_b16 a[208:209], v209 offset:2048\n" \
"v_mfma_f32_16x16x32_bf16 v[12:15], a[52:55], a[20:23], v[12:15]\n" \
"buffer_load_dwordx4 v197, s[48:51], s77 offen offset:0 lds\n" \
"v_mfma_f32_16x16x32_bf16 v[8:11], a[40:43], a[16:19], v[8:11]\n" \
"ds_read_b64_tr_b16 a[210:211], v209 offset:5120\n" \
"v_mfma_f32_16x16x32_bf16 v[8:11], a[44:47], a[20:23], v[8:11]\n" \
"ds_read_b64_tr_b16 a[212:213], v209 offset:14336\n" \
"buffer_store_dwordx4 v[120:123], v236, s[56:59], s88 offen offset:0 sc1\n" \
"v_mfma_f32_16x16x32_bf16 v[48:51], a[32:35], a[16:19], v[48:51]\n" \
"ds_read_b64_tr_b16 a[214:215], v209 offset:17408\n" \
"v_mfma_f32_16x16x32_bf16 v[224:227], v[244:247], v[120:123], v[224:227]\n" \
"v_accvgpr_read_b32 v112, a72\n" \
"v_accvgpr_read_b32 v113, a73\n" \
"v_mfma_f32_16x16x32_bf16 v[48:51], a[36:39], a[20:23], v[48:51]\n" \
"buffer_load_dwordx4 v198, s[48:51], s77 offen offset:1024 lds\n" \
"v_accvgpr_read_b32 v114, a74\n" \
"v_mfma_f32_16x16x32_bf16 v[56:59], a[32:35], a[24:27], v[56:59]\n" \
"ds_read_b64_tr_b16 a[216:217], v210 offset:2048\n" \
"v_accvgpr_read_b32 v115, a75\n" \
"v_mfma_f32_16x16x32_bf16 v[56:59], a[36:39], a[28:31], v[56:59]\n" \
"ds_read_b64_tr_b16 a[218:219], v210 offset:5120\n" \
"v_accvgpr_read_b32 v116, a76\n" \
"v_mfma_f32_16x16x32_bf16 v[16:19], a[40:43], a[24:27], v[16:19]\n" \
"ds_read_b64_tr_b16 a[220:221], v210 offset:14336\n" \
"v_accvgpr_read_b32 v117, a77\n" \
"v_mfma_f32_16x16x32_bf16 v[16:19], a[44:47], a[28:31], v[16:19]\n" \
"buffer_load_dwordx4 v199, s[48:51], s77 offen offset:2048 lds\n" \
"v_accvgpr_read_b32 v118, a78\n" \
"v_mfma_f32_16x16x32_bf16 v[20:23], a[48:51], a[24:27], v[20:23]\n" \
"ds_read_b64_tr_b16 a[222:223], v210 offset:17408\n" \
"v_accvgpr_read_b32 v119, a79\n" \
"v_mfma_f32_16x16x32_bf16 v[20:23], a[52:55], a[28:31], v[20:23]\n" \
"s_waitcnt lgkmcnt(3)\n" \
"v_add_f32 v112, v112, v136\n" \
"v_add_f32 v113, v113, v137\n" \
"v_add_f32 v114, v114, v138\n" \
"v_mfma_f32_16x16x32_bf16 v[28:31], a[48:51], a[192:195], v[28:31]\n" \
"v_add_f32 v115, v115, v139\n" \
"v_add_f32 v116, v116, v140\n" \
"v_mfma_f32_16x16x32_bf16 v[28:31], a[52:55], a[196:199], v[28:31]\n" \
"v_add_f32 v117, v117, v141\n" \
"v_add_f32 v118, v118, v142\n" \
"v_mfma_f32_16x16x32_bf16 v[24:27], a[40:43], a[192:195], v[24:27]\n" \
"v_add_u32 v205, 0, v200\n" \
"v_add_u32 v206, 0, v201\n" \
"v_mfma_f32_16x16x32_bf16 v[24:27], a[44:47], a[196:199], v[24:27]\n" \
"v_add_u32 v207, 0, v202\n" \
"v_add_u32 v208, 0, v203\n" \
"v_add_f32 v119, v119, v143\n" \
"v_mfma_f32_16x16x32_bf16 v[64:67], a[32:35], a[192:195], v[64:67]\n" \
"v_add_u32 v209, 20480, v204\n" \
"v_xor_b32 v210, 32, v209\n" \
"v_mfma_f32_16x16x32_bf16 v[64:67], a[36:39], a[196:199], v[64:67]\n" \
"s_waitcnt vmcnt(12) lgkmcnt(0)\n" \
"s_barrier\n" \
"v_mfma_f32_16x16x32_bf16 v[72:75], a[0:3], a[208:211], v[72:75]\n" \
"ds_read_b64_tr_b16 a[32:33], v209 offset:1024\n" \
"ds_read_b64_tr_b16 a[34:35], v209 offset:4096\n" \
"v_mfma_f32_16x16x32_bf16 v[72:75], a[4:7], a[212:215], v[72:75]\n" \
"ds_read_b64_tr_b16 a[36:37], v209 offset:13312\n" \
"ds_read_b64_tr_b16 a[38:39], v209 offset:16384\n" \
"v_mul_f32 v112, v112, v232\n" \
"v_mfma_f32_16x16x32_bf16 v[80:83], a[8:11], a[208:211], v[80:83]\n" \
"s_add_u32 m0, s79, 65536\n" \
"ds_read_b128 a[0:3], v207 offset:0\n" \
"v_mul_f32 v113, v113, v232\n" \
"v_mfma_f32_16x16x32_bf16 v[80:83], a[12:15], a[212:215], v[80:83]\n" \
"buffer_load_dwordx4 v192, s[44:47], s73 offen offset:0 lds\n" \
"v_mul_f32 v114, v114, v232\n" \
"v_mfma_f32_16x16x32_bf16 v[84:87], a[8:11], a[216:219], v[84:87]\n" \
"ds_read_b128 a[4:7], v208 offset:0\n" \
"v_mul_f32 v115, v115, v232\n" \
"v_mfma_f32_16x16x32_bf16 v[84:87], a[12:15], a[220:223], v[84:87]\n" \
"ds_read_b64_tr_b16 a[40:41], v209 offset:0\n" \
"v_mul_f32 v116, v116, v232\n" \
"v_mfma_f32_16x16x32_bf16 v[44:47], a[200:203], a[8:11], v[44:47]\n" \
"ds_read_b64_tr_b16 a[42:43], v209 offset:3072\n" \
"v_mul_f32 v117, v117, v232\n" \
"v_mfma_f32_16x16x32_bf16 v[44:47], a[204:207], a[12:15], v[44:47]\n" \
"buffer_load_dwordx4 v193, s[44:47], s73 offen offset:1024 lds\n" \
"v_mul_f32 v118, v118, v232\n" \
"v_mfma_f32_16x16x32_bf16 v[52:55], a[200:203], a[16:19], v[52:55]\n" \
"ds_read_b128 a[8:11], v205 offset:0\n" \
"v_mul_f32 v119, v119, v232\n" \
"v_mfma_f32_16x16x32_bf16 v[52:55], a[204:207], a[20:23], v[52:55]\n" \
"ds_read_b128 a[12:15], v206 offset:0\n" \
"v_cvt_pk_bf16_f32 v124, v112, v113\n" \
"v_mfma_f32_16x16x32_bf16 v[92:95], a[16:19], a[216:219], v[92:95]\n" \
"ds_read_b64_tr_b16 a[44:45], v209 offset:12288\n" \
"v_cvt_pk_bf16_f32 v125, v114, v115\n" \
"v_mfma_f32_16x16x32_bf16 v[92:95], a[20:23], a[220:223], v[92:95]\n" \
"buffer_load_dwordx4 v192, s[44:47], s73 offen offset:2048 lds\n" \
"v_cvt_pk_bf16_f32 v126, v116, v117\n" \
"v_mfma_f32_16x16x32_bf16 v[88:91], a[16:19], a[208:211], v[88:91]\n" \
"ds_read_b64_tr_b16 a[46:47], v209 offset:15360\n" \
"v_cvt_pk_bf16_f32 v127, v118, v119\n" \
"v_mfma_f32_16x16x32_bf16 v[88:91], a[20:23], a[212:215], v[88:91]\n" \
"ds_read_b64_tr_b16 a[48:49], v210 offset:0\n" \
"buffer_store_dwordx4 v[124:127], v236, s[56:59], s88 offen offset:2048 sc1\n" \
"v_mfma_f32_16x16x32_bf16 v[96:99], a[24:27], a[208:211], v[96:99]\n" \
"ds_read_b128 a[16:19], v205 offset:4096\n" \
"v_mfma_f32_16x16x32_bf16 v[96:99], a[28:31], a[212:215], v[96:99]\n" \
"buffer_load_dwordx4 v193, s[44:47], s73 offen offset:3072 lds\n" \
"v_mfma_f32_16x16x32_bf16 v[224:227], v[244:247], v[124:127], v[224:227]\n" \
"v_accvgpr_read_b32 v112, a80\n" \
"v_accvgpr_read_b32 v113, a81\n" \
"v_accvgpr_read_b32 v114, a82\n" \
"v_mfma_f32_16x16x32_bf16 v[100:103], a[24:27], a[216:219], v[100:103]\n" \
"ds_read_b128 a[20:23], v206 offset:4096\n" \
"v_accvgpr_read_b32 v115, a83\n" \
"v_mfma_f32_16x16x32_bf16 v[100:103], a[28:31], a[220:223], v[100:103]\n" \
"s_add_u32 m0, s89, 65536\n" \
"ds_read_b64_tr_b16 a[50:51], v210 offset:3072\n" \
"v_accvgpr_read_b32 v116, a84\n" \
"v_accvgpr_read_b32 v117, a85\n" \
"v_mfma_f32_16x16x32_bf16 v[60:63], a[200:203], a[24:27], v[60:63]\n" \
"s_xor_b32 s73, s73, 0x300\n" \
"ds_read_b64_tr_b16 a[52:53], v210 offset:12288\n" \
"v_accvgpr_read_b32 v118, a86\n" \
"v_mfma_f32_16x16x32_bf16 v[60:63], a[204:207], a[28:31], v[60:63]\n" \
"buffer_load_dwordx4 v196, s[44:47], s75 offen lds\n" \
"v_mfma_f32_16x16x32_bf16 v[68:71], a[200:203], a[192:195], v[68:71]\n" \
"s_xor_b32 s75, s75, 0x300\n" \
"s_mul_i32 s76, s80, 393216\n" \
"ds_read_b128 a[24:27], v205 offset:8192\n" \
"v_mfma_f32_16x16x32_bf16 v[68:71], a[204:207], a[196:199], v[68:71]\n" \
"s_add_u32 s76, s76, s78\n" \
"ds_read_b128 a[28:31], v206 offset:8192\n" \
"v_accvgpr_read_b32 v119, a87\n" \
"v_add_f32 v112, v112, v144\n" \
"v_mfma_f32_16x16x32_bf16 v[108:111], a[192:195], a[216:219], v[108:111]\n" \
"s_add_u32 m0, s91, 86016\n" \
"ds_read_b64_tr_b16 a[54:55], v210 offset:15360\n" \
"v_add_f32 v113, v113, v145\n" \
"v_mfma_f32_16x16x32_bf16 v[108:111], a[196:199], a[220:223], v[108:111]\n" \
"buffer_load_dwordx4 v197, s[48:51], s76 offen offset:0 lds\n" \
"v_add_f32 v114, v114, v146\n" \
"v_mfma_f32_16x16x32_bf16 v[104:107], a[192:195], a[208:211], v[104:107]\n" \
"v_add_f32 v115, v115, v147\n" \
"v_add_f32 v116, v116, v148\n" \
"v_mfma_f32_16x16x32_bf16 v[104:107], a[196:199], a[212:215], v[104:107]\n" \
"s_waitcnt lgkmcnt(2)\n" \
"v_add_f32 v117, v117, v149\n" \
"v_add_f32 v118, v118, v150\n" \
"v_mfma_f32_16x16x32_bf16 v[32:35], a[32:35], a[0:3], v[32:35]\n" \
"s_add_u32 s77, s76, 24576\n" \
"ds_read_b128 a[192:195], v205 offset:12288\n" \
"v_add_f32 v119, v119, v151\n" \
"v_mfma_f32_16x16x32_bf16 v[32:35], a[36:39], a[4:7], v[32:35]\n" \
"buffer_load_dwordx4 v198, s[48:51], s76 offen offset:1024 lds\n" \
"v_mul_f32 v112, v112, v232\n" \
"v_mfma_f32_16x16x32_bf16 v[40:43], a[32:35], a[8:11], v[40:43]\n" \
"ds_read_b128 a[196:199], v206 offset:12288\n" \
"v_mul_f32 v113, v113, v232\n" \
"v_mfma_f32_16x16x32_bf16 v[40:43], a[36:39], a[12:15], v[40:43]\n" \
"s_waitcnt lgkmcnt(2)\n" \
"ds_read_b64_tr_b16 a[200:201], v210 offset:1024\n" \
"v_mul_f32 v114, v114, v232\n" \
"v_mfma_f32_16x16x32_bf16 v[0:3], a[40:43], a[8:11], v[0:3]\n" \
"s_xor_b32 s80, s80, 0x6\n" \
"ds_read_b64_tr_b16 a[202:203], v210 offset:4096\n" \
"v_mul_f32 v115, v115, v232\n" \
"v_mfma_f32_16x16x32_bf16 v[0:3], a[44:47], a[12:15], v[0:3]\n" \
"buffer_load_dwordx4 v199, s[48:51], s76 offen offset:2048 lds\n" \
"v_mul_f32 v116, v116, v232\n" \
"v_mfma_f32_16x16x32_bf16 v[4:7], a[48:51], a[8:11], v[4:7]\n" \
"ds_read_b64_tr_b16 a[204:205], v210 offset:13312\n" \
"Lpg7867_%=:\n" \
"Lst1x0_%=:\n" \
"s_cbranch_execz Lpg10165_%=\n" \
"v_mul_f32 v117, v117, v232\n" \
"v_mfma_f32_16x16x32_bf16 v[4:7], a[52:55], a[12:15], v[4:7]\n" \
"ds_read_b64_tr_b16 a[206:207], v210 offset:16384\n" \
"v_mul_f32 v118, v118, v232\n" \
"v_mul_f32 v119, v119, v232\n" \
"v_mfma_f32_16x16x32_bf16 v[12:15], a[48:51], a[16:19], v[12:15]\n" \
"s_add_u32 m0, m0, 3072\n" \
"ds_read_b64_tr_b16 a[208:209], v209 offset:2048\n" \
"v_cvt_pk_bf16_f32 v120, v112, v113\n" \
"v_mfma_f32_16x16x32_bf16 v[12:15], a[52:55], a[20:23], v[12:15]\n" \
"buffer_load_dwordx4 v197, s[48:51], s77 offen offset:0 lds\n" \
"v_mfma_f32_16x16x32_bf16 v[8:11], a[40:43], a[16:19], v[8:11]\n" \
"ds_read_b64_tr_b16 a[210:211], v209 offset:5120\n" \
"v_cvt_pk_bf16_f32 v121, v114, v115\n" \
"v_mfma_f32_16x16x32_bf16 v[8:11], a[44:47], a[20:23], v[8:11]\n" \
"ds_read_b64_tr_b16 a[212:213], v209 offset:14336\n" \
"v_cvt_pk_bf16_f32 v122, v116, v117\n" \
"v_cvt_pk_bf16_f32 v123, v118, v119\n" \
"v_mfma_f32_16x16x32_bf16 v[48:51], a[32:35], a[16:19], v[48:51]\n" \
"ds_read_b64_tr_b16 a[214:215], v209 offset:17408\n" \
"v_mfma_f32_16x16x32_bf16 v[48:51], a[36:39], a[20:23], v[48:51]\n" \
"buffer_load_dwordx4 v198, s[48:51], s77 offen offset:1024 lds\n" \
"v_mfma_f32_16x16x32_bf16 v[56:59], a[32:35], a[24:27], v[56:59]\n" \
"ds_read_b64_tr_b16 a[216:217], v210 offset:2048\n" \
"v_mfma_f32_16x16x32_bf16 v[56:59], a[36:39], a[28:31], v[56:59]\n" \
"ds_read_b64_tr_b16 a[218:219], v210 offset:5120\n" \
"buffer_store_dwordx4 v[120:123], v240, s[56:59], s88 offen offset:0 sc1\n" \
"v_mfma_f32_16x16x32_bf16 v[16:19], a[40:43], a[24:27], v[16:19]\n" \
"ds_read_b64_tr_b16 a[220:221], v210 offset:14336\n" \
"v_mfma_f32_16x16x32_bf16 v[16:19], a[44:47], a[28:31], v[16:19]\n" \
"buffer_load_dwordx4 v199, s[48:51], s77 offen offset:2048 lds\n" \
"v_mfma_f32_16x16x32_bf16 v[224:227], v[244:247], v[120:123], v[224:227]\n" \
"v_accvgpr_read_b32 v112, a88\n" \
"v_accvgpr_read_b32 v113, a89\n" \
"v_accvgpr_read_b32 v114, a90\n" \
"v_accvgpr_read_b32 v115, a91\n" \
"v_mfma_f32_16x16x32_bf16 v[20:23], a[48:51], a[24:27], v[20:23]\n" \
"ds_read_b64_tr_b16 a[222:223], v210 offset:17408\n" \
"v_accvgpr_read_b32 v116, a92\n" \
"v_mfma_f32_16x16x32_bf16 v[20:23], a[52:55], a[28:31], v[20:23]\n" \
"s_waitcnt lgkmcnt(3)\n" \
"v_accvgpr_read_b32 v117, a93\n" \
"v_accvgpr_read_b32 v118, a94\n" \
"v_mfma_f32_16x16x32_bf16 v[28:31], a[48:51], a[192:195], v[28:31]\n" \
"v_accvgpr_read_b32 v119, a95\n" \
"v_add_f32 v112, v112, v152\n" \
"v_mfma_f32_16x16x32_bf16 v[28:31], a[52:55], a[196:199], v[28:31]\n" \
"v_add_f32 v113, v113, v153\n" \
"v_add_f32 v114, v114, v154\n" \
"v_add_f32 v115, v115, v155\n" \
"v_mfma_f32_16x16x32_bf16 v[24:27], a[40:43], a[192:195], v[24:27]\n" \
"v_add_u32 v205, 110592, v200\n" \
"v_add_u32 v206, 110592, v201\n" \
"v_mfma_f32_16x16x32_bf16 v[24:27], a[44:47], a[196:199], v[24:27]\n" \
"v_add_u32 v207, 110592, v202\n" \
"v_add_u32 v208, 110592, v203\n" \
"v_mfma_f32_16x16x32_bf16 v[64:67], a[32:35], a[192:195], v[64:67]\n" \
"v_add_u32 v209, 139264, v204\n" \
"v_xor_b32 v210, 32, v209\n" \
"v_mfma_f32_16x16x32_bf16 v[64:67], a[36:39], a[196:199], v[64:67]\n" \
"v_add_f32 v116, v116, v156\n" \
"s_waitcnt vmcnt(13) lgkmcnt(0)\n" \
"s_barrier\n" \
"v_mfma_f32_16x16x32_bf16 v[72:75], a[0:3], a[208:211], v[72:75]\n" \
"ds_read_b64_tr_b16 a[32:33], v209 offset:1024\n" \
"ds_read_b64_tr_b16 a[34:35], v209 offset:4096\n" \
"v_mfma_f32_16x16x32_bf16 v[72:75], a[4:7], a[212:215], v[72:75]\n" \
"ds_read_b64_tr_b16 a[36:37], v209 offset:13312\n" \
"ds_read_b64_tr_b16 a[38:39], v209 offset:16384\n" \
"v_add_f32 v117, v117, v157\n" \
"v_mfma_f32_16x16x32_bf16 v[80:83], a[8:11], a[208:211], v[80:83]\n" \
"s_add_u32 m0, s79, 0\n" \
"ds_read_b128 a[0:3], v207 offset:0\n" \
"v_add_f32 v118, v118, v158\n" \
"v_mfma_f32_16x16x32_bf16 v[80:83], a[12:15], a[212:215], v[80:83]\n" \
"buffer_load_dwordx4 v192, s[44:47], s73 offen offset:0 lds\n" \
"v_mfma_f32_16x16x32_bf16 v[84:87], a[8:11], a[216:219], v[84:87]\n" \
"ds_read_b128 a[4:7], v208 offset:0\n" \
"v_add_f32 v119, v119, v159\n" \
"v_mfma_f32_16x16x32_bf16 v[84:87], a[12:15], a[220:223], v[84:87]\n" \
"ds_read_b64_tr_b16 a[40:41], v209 offset:0\n" \
"v_mul_f32 v112, v112, v232\n" \
"v_mul_f32 v113, v113, v232\n" \
"v_mfma_f32_16x16x32_bf16 v[44:47], a[200:203], a[8:11], v[44:47]\n" \
"ds_read_b64_tr_b16 a[42:43], v209 offset:3072\n" \
"v_mul_f32 v114, v114, v232\n" \
"v_mfma_f32_16x16x32_bf16 v[44:47], a[204:207], a[12:15], v[44:47]\n" \
"buffer_load_dwordx4 v193, s[44:47], s73 offen offset:1024 lds\n" \
"v_mfma_f32_16x16x32_bf16 v[52:55], a[200:203], a[16:19], v[52:55]\n" \
"ds_read_b128 a[8:11], v205 offset:0\n" \
"v_mul_f32 v115, v115, v232\n" \
"v_mfma_f32_16x16x32_bf16 v[52:55], a[204:207], a[20:23], v[52:55]\n" \
"ds_read_b128 a[12:15], v206 offset:0\n" \
"v_mul_f32 v116, v116, v232\n" \
"v_mul_f32 v117, v117, v232\n" \
"v_mfma_f32_16x16x32_bf16 v[92:95], a[16:19], a[216:219], v[92:95]\n" \
"ds_read_b64_tr_b16 a[44:45], v209 offset:12288\n" \
"v_mul_f32 v118, v118, v232\n" \
"v_mfma_f32_16x16x32_bf16 v[92:95], a[20:23], a[220:223], v[92:95]\n" \
"buffer_load_dwordx4 v192, s[44:47], s73 offen offset:2048 lds\n" \
"v_mul_f32 v119, v119, v232\n" \
"v_mfma_f32_16x16x32_bf16 v[88:91], a[16:19], a[208:211], v[88:91]\n" \
"ds_read_b64_tr_b16 a[46:47], v209 offset:15360\n" \
"v_cvt_pk_bf16_f32 v124, v112, v113\n" \
"v_mfma_f32_16x16x32_bf16 v[88:91], a[20:23], a[212:215], v[88:91]\n" \
"ds_read_b64_tr_b16 a[48:49], v210 offset:0\n" \
"v_cvt_pk_bf16_f32 v125, v114, v115\n" \
"v_mfma_f32_16x16x32_bf16 v[96:99], a[24:27], a[208:211], v[96:99]\n" \
"ds_read_b128 a[16:19], v205 offset:4096\n" \
"v_cvt_pk_bf16_f32 v126, v116, v117\n" \
"v_mfma_f32_16x16x32_bf16 v[96:99], a[28:31], a[212:215], v[96:99]\n" \
"buffer_load_dwordx4 v193, s[44:47], s73 offen offset:3072 lds\n" \
"v_cvt_pk_bf16_f32 v127, v118, v119\n" \
"v_mfma_f32_16x16x32_bf16 v[100:103], a[24:27], a[216:219], v[100:103]\n" \
"ds_read_b128 a[20:23], v206 offset:4096\n" \
"v_mfma_f32_16x16x32_bf16 v[100:103], a[28:31], a[220:223], v[100:103]\n" \
"s_add_u32 m0, s89, 0\n" \
"ds_read_b64_tr_b16 a[50:51], v210 offset:3072\n" \
"buffer_store_dwordx4 v[124:127], v240, s[56:59], s88 offen offset:2048 sc1\n" \
"v_mfma_f32_16x16x32_bf16 v[60:63], a[200:203], a[24:27], v[60:63]\n" \
"s_xor_b32 s73, s73, 0x80\n" \
"ds_read_b64_tr_b16 a[52:53], v210 offset:12288\n" \
"v_mfma_f32_16x16x32_bf16 v[60:63], a[204:207], a[28:31], v[60:63]\n" \
"buffer_load_dwordx4 v196, s[44:47], s75 offen lds\n" \
"v_mfma_f32_16x16x32_bf16 v[224:227], v[244:247], v[124:127], v[224:227]\n" \
"s_add_u32 s82, s88, 532480\n" \
"v_accvgpr_read_b32 v112, a96\n" \
"v_accvgpr_read_b32 v113, a97\n" \
"v_accvgpr_read_b32 v114, a98\n" \
"v_mfma_f32_16x16x32_bf16 v[68:71], a[200:203], a[192:195], v[68:71]\n" \
"s_xor_b32 s75, s75, 0x80\n" \
"s_mul_i32 s76, s80, 393216\n" \
"ds_read_b128 a[24:27], v205 offset:8192\n" \
"v_mfma_f32_16x16x32_bf16 v[68:71], a[204:207], a[196:199], v[68:71]\n" \
"s_add_u32 s76, s76, s78\n" \
"ds_read_b128 a[28:31], v206 offset:8192\n" \
"v_accvgpr_read_b32 v115, a99\n" \
"v_accvgpr_read_b32 v116, a100\n" \
"v_mfma_f32_16x16x32_bf16 v[108:111], a[192:195], a[216:219], v[108:111]\n" \
"s_add_u32 m0, s91, 20480\n" \
"ds_read_b64_tr_b16 a[54:55], v210 offset:15360\n" \
"v_accvgpr_read_b32 v117, a101\n" \
"v_mfma_f32_16x16x32_bf16 v[108:111], a[196:199], a[220:223], v[108:111]\n" \
"buffer_load_dwordx4 v197, s[48:51], s76 offen offset:0 lds\n" \
"v_mfma_f32_16x16x32_bf16 v[104:107], a[192:195], a[208:211], v[104:107]\n" \
"v_accvgpr_read_b32 v118, a102\n" \
"v_accvgpr_read_b32 v119, a103\n" \
"v_mfma_f32_16x16x32_bf16 v[104:107], a[196:199], a[212:215], v[104:107]\n" \
"s_waitcnt lgkmcnt(2)\n" \
"v_add_f32 v112, v112, v160\n" \
"v_add_f32 v113, v113, v161\n" \
"v_add_f32 v114, v114, v162\n" \
"v_mfma_f32_16x16x32_bf16 v[32:35], a[32:35], a[0:3], v[32:35]\n" \
"s_add_u32 s77, s76, 24576\n" \
"ds_read_b128 a[192:195], v205 offset:12288\n" \
"v_add_f32 v115, v115, v163\n" \
"v_mfma_f32_16x16x32_bf16 v[32:35], a[36:39], a[4:7], v[32:35]\n" \
"buffer_load_dwordx4 v198, s[48:51], s76 offen offset:1024 lds\n" \
"v_mfma_f32_16x16x32_bf16 v[40:43], a[32:35], a[8:11], v[40:43]\n" \
"ds_read_b128 a[196:199], v206 offset:12288\n" \
"v_add_f32 v116, v116, v164\n" \
"v_mfma_f32_16x16x32_bf16 v[40:43], a[36:39], a[12:15], v[40:43]\n" \
"s_waitcnt lgkmcnt(2)\n" \
"ds_read_b64_tr_b16 a[200:201], v210 offset:1024\n" \
"v_add_f32 v117, v117, v165\n" \
"v_add_f32 v118, v118, v166\n" \
"v_mfma_f32_16x16x32_bf16 v[0:3], a[40:43], a[8:11], v[0:3]\n" \
"s_xor_b32 s80, s80, 0x1\n" \
"ds_read_b64_tr_b16 a[202:203], v210 offset:4096\n" \
"v_add_f32 v119, v119, v167\n" \
"v_mfma_f32_16x16x32_bf16 v[0:3], a[44:47], a[12:15], v[0:3]\n" \
"buffer_load_dwordx4 v199, s[48:51], s76 offen offset:2048 lds\n" \
"v_mul_f32 v112, v112, v233\n" \
"v_mfma_f32_16x16x32_bf16 v[4:7], a[48:51], a[8:11], v[4:7]\n" \
"ds_read_b64_tr_b16 a[204:205], v210 offset:13312\n" \
"v_mul_f32 v113, v113, v233\n" \
"v_mfma_f32_16x16x32_bf16 v[4:7], a[52:55], a[12:15], v[4:7]\n" \
"ds_read_b64_tr_b16 a[206:207], v210 offset:16384\n" \
"v_mul_f32 v114, v114, v233\n" \
"v_mfma_f32_16x16x32_bf16 v[12:15], a[48:51], a[16:19], v[12:15]\n" \
"s_add_u32 m0, m0, 3072\n" \
"ds_read_b64_tr_b16 a[208:209], v209 offset:2048\n" \
"v_mul_f32 v115, v115, v233\n" \
"v_mfma_f32_16x16x32_bf16 v[12:15], a[52:55], a[20:23], v[12:15]\n" \
"buffer_load_dwordx4 v197, s[48:51], s77 offen offset:0 lds\n" \
"v_mul_f32 v116, v116, v233\n" \
"v_mfma_f32_16x16x32_bf16 v[8:11], a[40:43], a[16:19], v[8:11]\n" \
"ds_read_b64_tr_b16 a[210:211], v209 offset:5120\n" \
"v_mul_f32 v117, v117, v233\n" \
"v_mfma_f32_16x16x32_bf16 v[8:11], a[44:47], a[20:23], v[8:11]\n" \
"ds_read_b64_tr_b16 a[212:213], v209 offset:14336\n" \
"v_mul_f32 v118, v118, v233\n" \
"v_mfma_f32_16x16x32_bf16 v[48:51], a[32:35], a[16:19], v[48:51]\n" \
"ds_read_b64_tr_b16 a[214:215], v209 offset:17408\n" \
"v_mul_f32 v119, v119, v233\n" \
"v_mfma_f32_16x16x32_bf16 v[48:51], a[36:39], a[20:23], v[48:51]\n" \
"buffer_load_dwordx4 v198, s[48:51], s77 offen offset:1024 lds\n" \
"v_cvt_pk_bf16_f32 v120, v112, v113\n" \
"v_mfma_f32_16x16x32_bf16 v[56:59], a[32:35], a[24:27], v[56:59]\n" \
"ds_read_b64_tr_b16 a[216:217], v210 offset:2048\n" \
"v_cvt_pk_bf16_f32 v121, v114, v115\n" \
"v_mfma_f32_16x16x32_bf16 v[56:59], a[36:39], a[28:31], v[56:59]\n" \
"ds_read_b64_tr_b16 a[218:219], v210 offset:5120\n" \
"v_cvt_pk_bf16_f32 v122, v116, v117\n" \
"v_cvt_pk_bf16_f32 v123, v118, v119\n" \
"v_mfma_f32_16x16x32_bf16 v[16:19], a[40:43], a[24:27], v[16:19]\n" \
"ds_read_b64_tr_b16 a[220:221], v210 offset:14336\n" \
"v_mfma_f32_16x16x32_bf16 v[16:19], a[44:47], a[28:31], v[16:19]\n" \
"buffer_load_dwordx4 v199, s[48:51], s77 offen offset:2048 lds\n" \
"v_mfma_f32_16x16x32_bf16 v[20:23], a[48:51], a[24:27], v[20:23]\n" \
"s_waitcnt lgkmcnt(3)\n" \
"ds_read_b64_tr_b16 a[222:223], v210 offset:17408\n" \
"v_mfma_f32_16x16x32_bf16 v[20:23], a[52:55], a[28:31], v[20:23]\n" \
"buffer_store_dwordx4 v[120:123], v236, s[56:59], s82 offen offset:0 sc1\n" \
"v_mfma_f32_16x16x32_bf16 v[28:31], a[48:51], a[192:195], v[28:31]\n" \
"v_mfma_f32_16x16x32_bf16 v[28:31], a[52:55], a[196:199], v[28:31]\n" \
"v_mfma_f32_16x16x32_bf16 v[224:227], v[244:247], v[120:123], v[224:227]\n" \
"v_accvgpr_read_b32 v112, a104\n" \
"v_accvgpr_read_b32 v113, a105\n" \
"v_accvgpr_read_b32 v114, a106\n" \
"v_mfma_f32_16x16x32_bf16 v[24:27], a[40:43], a[192:195], v[24:27]\n" \
"v_add_u32 v205, 65536, v200\n" \
"v_add_u32 v206, 65536, v201\n" \
"v_mfma_f32_16x16x32_bf16 v[24:27], a[44:47], a[196:199], v[24:27]\n" \
"v_add_u32 v207, 65536, v202\n" \
"v_add_u32 v208, 65536, v203\n" \
"v_accvgpr_read_b32 v115, a107\n" \
"v_mfma_f32_16x16x32_bf16 v[64:67], a[32:35], a[192:195], v[64:67]\n" \
"v_add_u32 v209, 86016, v204\n" \
"v_xor_b32 v210, 32, v209\n" \
"v_mfma_f32_16x16x32_bf16 v[64:67], a[36:39], a[196:199], v[64:67]\n" \
"v_accvgpr_read_b32 v116, a108\n" \
"s_waitcnt vmcnt(13) lgkmcnt(0)\n" \
"s_barrier\n" \
"v_mfma_f32_16x16x32_bf16 v[72:75], a[0:3], a[208:211], v[72:75]\n" \
"ds_read_b64_tr_b16 a[32:33], v209 offset:1024\n" \
"ds_read_b64_tr_b16 a[34:35], v209 offset:4096\n" \
"v_mfma_f32_16x16x32_bf16 v[72:75], a[4:7], a[212:215], v[72:75]\n" \
"ds_read_b64_tr_b16 a[36:37], v209 offset:13312\n" \
"ds_read_b64_tr_b16 a[38:39], v209 offset:16384\n" \
"v_mfma_f32_16x16x32_bf16 v[80:83], a[8:11], a[208:211], v[80:83]\n" \
"s_add_u32 m0, s79, 110592\n" \
"ds_read_b128 a[0:3], v207 offset:0\n" \
"v_accvgpr_read_b32 v117, a109\n" \
"v_mfma_f32_16x16x32_bf16 v[80:83], a[12:15], a[212:215], v[80:83]\n" \
"buffer_load_dwordx4 v192, s[44:47], s73 offen offset:0 lds\n" \
"v_accvgpr_read_b32 v118, a110\n" \
"v_mfma_f32_16x16x32_bf16 v[84:87], a[8:11], a[216:219], v[84:87]\n" \
"ds_read_b128 a[4:7], v208 offset:0\n" \
"v_accvgpr_read_b32 v119, a111\n" \
"v_mfma_f32_16x16x32_bf16 v[84:87], a[12:15], a[220:223], v[84:87]\n" \
"ds_read_b64_tr_b16 a[40:41], v209 offset:0\n" \
"v_add_f32 v112, v112, v168\n" \
"v_mfma_f32_16x16x32_bf16 v[44:47], a[200:203], a[8:11], v[44:47]\n" \
"ds_read_b64_tr_b16 a[42:43], v209 offset:3072\n" \
"v_add_f32 v113, v113, v169\n" \
"v_mfma_f32_16x16x32_bf16 v[44:47], a[204:207], a[12:15], v[44:47]\n" \
"buffer_load_dwordx4 v193, s[44:47], s73 offen offset:1024 lds\n" \
"v_add_f32 v114, v114, v170\n" \
"v_mfma_f32_16x16x32_bf16 v[52:55], a[200:203], a[16:19], v[52:55]\n" \
"ds_read_b128 a[8:11], v205 offset:0\n" \
"v_add_f32 v115, v115, v171\n" \
"v_mfma_f32_16x16x32_bf16 v[52:55], a[204:207], a[20:23], v[52:55]\n" \
"ds_read_b128 a[12:15], v206 offset:0\n" \
"v_add_f32 v116, v116, v172\n" \
"v_add_f32 v117, v117, v173\n" \
"v_mfma_f32_16x16x32_bf16 v[92:95], a[16:19], a[216:219], v[92:95]\n" \
"ds_read_b64_tr_b16 a[44:45], v209 offset:12288\n" \
"v_add_f32 v118, v118, v174\n" \
"v_mfma_f32_16x16x32_bf16 v[92:95], a[20:23], a[220:223], v[92:95]\n" \
"buffer_load_dwordx4 v192, s[44:47], s73 offen offset:2048 lds\n" \
"v_mfma_f32_16x16x32_bf16 v[88:91], a[16:19], a[208:211], v[88:91]\n" \
"ds_read_b64_tr_b16 a[46:47], v209 offset:15360\n" \
"v_add_f32 v119, v119, v175\n" \
"v_mfma_f32_16x16x32_bf16 v[88:91], a[20:23], a[212:215], v[88:91]\n" \
"ds_read_b64_tr_b16 a[48:49], v210 offset:0\n" \
"v_mul_f32 v112, v112, v233\n" \
"v_mul_f32 v113, v113, v233\n" \
"v_mfma_f32_16x16x32_bf16 v[96:99], a[24:27], a[208:211], v[96:99]\n" \
"ds_read_b128 a[16:19], v205 offset:4096\n" \
"v_mul_f32 v114, v114, v233\n" \
"v_mfma_f32_16x16x32_bf16 v[96:99], a[28:31], a[212:215], v[96:99]\n" \
"buffer_load_dwordx4 v193, s[44:47], s73 offen offset:3072 lds\n" \
"v_mfma_f32_16x16x32_bf16 v[100:103], a[24:27], a[216:219], v[100:103]\n" \
"ds_read_b128 a[20:23], v206 offset:4096\n" \
"v_mul_f32 v115, v115, v233\n" \
"v_mfma_f32_16x16x32_bf16 v[100:103], a[28:31], a[220:223], v[100:103]\n" \
"s_add_u32 m0, s89, 110592\n" \
"ds_read_b64_tr_b16 a[50:51], v210 offset:3072\n" \
"v_mul_f32 v116, v116, v233\n" \
"v_mul_f32 v117, v117, v233\n" \
"v_mfma_f32_16x16x32_bf16 v[60:63], a[200:203], a[24:27], v[60:63]\n" \
"s_xor_b32 s73, s73, 0x180\n" \
"ds_read_b64_tr_b16 a[52:53], v210 offset:12288\n" \
"v_mul_f32 v118, v118, v233\n" \
"v_mfma_f32_16x16x32_bf16 v[60:63], a[204:207], a[28:31], v[60:63]\n" \
"buffer_load_dwordx4 v196, s[44:47], s75 offen lds\n" \
"v_mul_f32 v119, v119, v233\n" \
"v_mfma_f32_16x16x32_bf16 v[68:71], a[200:203], a[192:195], v[68:71]\n" \
"s_xor_b32 s75, s75, 0x180\n" \
"s_mul_i32 s76, s80, 393216\n" \
"ds_read_b128 a[24:27], v205 offset:8192\n" \
"v_mfma_f32_16x16x32_bf16 v[68:71], a[204:207], a[196:199], v[68:71]\n" \
"s_add_u32 s76, s76, s78\n" \
"ds_read_b128 a[28:31], v206 offset:8192\n" \
"v_cvt_pk_bf16_f32 v124, v112, v113\n" \
"v_mfma_f32_16x16x32_bf16 v[108:111], a[192:195], a[216:219], v[108:111]\n" \
"s_add_u32 m0, s91, 139264\n" \
"ds_read_b64_tr_b16 a[54:55], v210 offset:15360\n" \
"v_cvt_pk_bf16_f32 v125, v114, v115\n" \
"v_mfma_f32_16x16x32_bf16 v[108:111], a[196:199], a[220:223], v[108:111]\n" \
"buffer_load_dwordx4 v197, s[48:51], s76 offen offset:0 lds\n" \
"v_cvt_pk_bf16_f32 v126, v116, v117\n" \
"v_mfma_f32_16x16x32_bf16 v[104:107], a[192:195], a[208:211], v[104:107]\n" \
"s_waitcnt lgkmcnt(3)\n" \
"v_cvt_pk_bf16_f32 v127, v118, v119\n" \
"v_mfma_f32_16x16x32_bf16 v[104:107], a[196:199], a[212:215], v[104:107]\n" \
"buffer_store_dwordx4 v[124:127], v236, s[56:59], s82 offen offset:2048 sc1\n" \
"v_mfma_f32_16x16x32_bf16 v[32:35], a[32:35], a[0:3], v[32:35]\n" \
"s_add_u32 s77, s76, 24576\n" \
"ds_read_b128 a[192:195], v205 offset:12288\n" \
"v_mfma_f32_16x16x32_bf16 v[32:35], a[36:39], a[4:7], v[32:35]\n" \
"buffer_load_dwordx4 v198, s[48:51], s76 offen offset:1024 lds\n" \
"v_mfma_f32_16x16x32_bf16 v[224:227], v[244:247], v[124:127], v[224:227]\n" \
"v_accvgpr_read_b32 v112, a112\n" \
"v_accvgpr_read_b32 v113, a113\n" \
"v_accvgpr_read_b32 v114, a114\n" \
"v_mfma_f32_16x16x32_bf16 v[40:43], a[32:35], a[8:11], v[40:43]\n" \
"ds_read_b128 a[196:199], v206 offset:12288\n" \
"v_accvgpr_read_b32 v115, a115\n" \
"v_mfma_f32_16x16x32_bf16 v[40:43], a[36:39], a[12:15], v[40:43]\n" \
"s_waitcnt lgkmcnt(2)\n" \
"ds_read_b64_tr_b16 a[200:201], v210 offset:1024\n" \
"v_accvgpr_read_b32 v116, a116\n" \
"v_accvgpr_read_b32 v117, a117\n" \
"v_mfma_f32_16x16x32_bf16 v[0:3], a[40:43], a[8:11], v[0:3]\n" \
"s_xor_b32 s80, s80, 0x3\n" \
"ds_read_b64_tr_b16 a[202:203], v210 offset:4096\n" \
"v_accvgpr_read_b32 v118, a118\n" \
"v_mfma_f32_16x16x32_bf16 v[0:3], a[44:47], a[12:15], v[0:3]\n" \
"buffer_load_dwordx4 v199, s[48:51], s76 offen offset:2048 lds\n" \
"v_mfma_f32_16x16x32_bf16 v[4:7], a[48:51], a[8:11], v[4:7]\n" \
"ds_read_b64_tr_b16 a[204:205], v210 offset:13312\n" \
"v_accvgpr_read_b32 v119, a119\n" \
"v_mfma_f32_16x16x32_bf16 v[4:7], a[52:55], a[12:15], v[4:7]\n" \
"ds_read_b64_tr_b16 a[206:207], v210 offset:16384\n" \
"v_add_f32 v112, v112, v176\n" \
"v_add_f32 v113, v113, v177\n" \
"v_mfma_f32_16x16x32_bf16 v[12:15], a[48:51], a[16:19], v[12:15]\n" \
"s_add_u32 m0, m0, 3072\n" \
"ds_read_b64_tr_b16 a[208:209], v209 offset:2048\n" \
"v_add_f32 v114, v114, v178\n" \
"v_mfma_f32_16x16x32_bf16 v[12:15], a[52:55], a[20:23], v[12:15]\n" \
"buffer_load_dwordx4 v197, s[48:51], s77 offen offset:0 lds\n" \
"v_mfma_f32_16x16x32_bf16 v[8:11], a[40:43], a[16:19], v[8:11]\n" \
"ds_read_b64_tr_b16 a[210:211], v209 offset:5120\n" \
"v_add_f32 v115, v115, v179\n" \
"v_mfma_f32_16x16x32_bf16 v[8:11], a[44:47], a[20:23], v[8:11]\n" \
"ds_read_b64_tr_b16 a[212:213], v209 offset:14336\n" \
"v_add_f32 v116, v116, v180\n" \
"v_add_f32 v117, v117, v181\n" \
"v_mfma_f32_16x16x32_bf16 v[48:51], a[32:35], a[16:19], v[48:51]\n" \
"ds_read_b64_tr_b16 a[214:215], v209 offset:17408\n" \
"v_add_f32 v118, v118, v182\n" \
"v_mfma_f32_16x16x32_bf16 v[48:51], a[36:39], a[20:23], v[48:51]\n" \
"buffer_load_dwordx4 v198, s[48:51], s77 offen offset:1024 lds\n" \
"v_add_f32 v119, v119, v183\n" \
"v_mfma_f32_16x16x32_bf16 v[56:59], a[32:35], a[24:27], v[56:59]\n" \
"ds_read_b64_tr_b16 a[216:217], v210 offset:2048\n" \
"v_mul_f32 v112, v112, v233\n" \
"v_mfma_f32_16x16x32_bf16 v[56:59], a[36:39], a[28:31], v[56:59]\n" \
"ds_read_b64_tr_b16 a[218:219], v210 offset:5120\n" \
"v_mul_f32 v113, v113, v233\n" \
"v_mfma_f32_16x16x32_bf16 v[16:19], a[40:43], a[24:27], v[16:19]\n" \
"ds_read_b64_tr_b16 a[220:221], v210 offset:14336\n" \
"v_mul_f32 v114, v114, v233\n" \
"v_mfma_f32_16x16x32_bf16 v[16:19], a[44:47], a[28:31], v[16:19]\n" \
"buffer_load_dwordx4 v199, s[48:51], s77 offen offset:2048 lds\n" \
"v_mul_f32 v115, v115, v233\n" \
"v_mfma_f32_16x16x32_bf16 v[20:23], a[48:51], a[24:27], v[20:23]\n" \
"ds_read_b64_tr_b16 a[222:223], v210 offset:17408\n" \
"v_mul_f32 v116, v116, v233\n" \
"v_mfma_f32_16x16x32_bf16 v[20:23], a[52:55], a[28:31], v[20:23]\n" \
"s_waitcnt lgkmcnt(3)\n" \
"v_mul_f32 v117, v117, v233\n" \
"v_mul_f32 v118, v118, v233\n" \
"v_mfma_f32_16x16x32_bf16 v[28:31], a[48:51], a[192:195], v[28:31]\n" \
"v_mul_f32 v119, v119, v233\n" \
"v_cvt_pk_bf16_f32 v120, v112, v113\n" \
"v_mfma_f32_16x16x32_bf16 v[28:31], a[52:55], a[196:199], v[28:31]\n" \
"v_cvt_pk_bf16_f32 v121, v114, v115\n" \
"v_cvt_pk_bf16_f32 v122, v116, v117\n" \
"v_cvt_pk_bf16_f32 v123, v118, v119\n" \
"v_mfma_f32_16x16x32_bf16 v[24:27], a[40:43], a[192:195], v[24:27]\n" \
"v_add_u32 v205, 0, v200\n" \
"v_add_u32 v206, 0, v201\n" \
"v_mfma_f32_16x16x32_bf16 v[24:27], a[44:47], a[196:199], v[24:27]\n" \
"buffer_store_dwordx4 v[120:123], v240, s[56:59], s82 offen offset:0 sc1\n" \
"v_add_u32 v207, 0, v202\n" \
"v_add_u32 v208, 0, v203\n" \
"v_mfma_f32_16x16x32_bf16 v[64:67], a[32:35], a[192:195], v[64:67]\n" \
"v_add_u32 v209, 20480, v204\n" \
"v_xor_b32 v210, 32, v209\n" \
"v_mfma_f32_16x16x32_bf16 v[64:67], a[36:39], a[196:199], v[64:67]\n" \
"v_mfma_f32_16x16x32_bf16 v[224:227], v[244:247], v[120:123], v[224:227]\n" \
"v_accvgpr_read_b32 v112, a120\n" \
"s_waitcnt vmcnt(14) lgkmcnt(0)\n" \
"s_barrier\n" \
"v_mfma_f32_16x16x32_bf16 v[72:75], a[0:3], a[208:211], v[72:75]\n" \
"ds_read_b64_tr_b16 a[32:33], v209 offset:1024\n" \
"ds_read_b64_tr_b16 a[34:35], v209 offset:4096\n" \
"v_mfma_f32_16x16x32_bf16 v[72:75], a[4:7], a[212:215], v[72:75]\n" \
"ds_read_b64_tr_b16 a[36:37], v209 offset:13312\n" \
"ds_read_b64_tr_b16 a[38:39], v209 offset:16384\n" \
"v_accvgpr_read_b32 v113, a121\n" \
"v_mfma_f32_16x16x32_bf16 v[80:83], a[8:11], a[208:211], v[80:83]\n" \
"s_add_u32 m0, s79, 65536\n" \
"ds_read_b128 a[0:3], v207 offset:0\n" \
"v_accvgpr_read_b32 v114, a122\n" \
"v_mfma_f32_16x16x32_bf16 v[80:83], a[12:15], a[212:215], v[80:83]\n" \
"buffer_load_dwordx4 v192, s[44:47], s73 offen offset:0 lds\n" \
"v_mfma_f32_16x16x32_bf16 v[84:87], a[8:11], a[216:219], v[84:87]\n" \
"ds_read_b128 a[4:7], v208 offset:0\n" \
"v_accvgpr_read_b32 v115, a123\n" \
"v_mfma_f32_16x16x32_bf16 v[84:87], a[12:15], a[220:223], v[84:87]\n" \
"ds_read_b64_tr_b16 a[40:41], v209 offset:0\n" \
"v_accvgpr_read_b32 v116, a124\n" \
"v_accvgpr_read_b32 v117, a125\n" \
"v_mfma_f32_16x16x32_bf16 v[44:47], a[200:203], a[8:11], v[44:47]\n" \
"ds_read_b64_tr_b16 a[42:43], v209 offset:3072\n" \
"v_accvgpr_read_b32 v118, a126\n" \
"v_mfma_f32_16x16x32_bf16 v[44:47], a[204:207], a[12:15], v[44:47]\n" \
"buffer_load_dwordx4 v193, s[44:47], s73 offen offset:1024 lds\n" \
"v_accvgpr_read_b32 v119, a127\n" \
"v_mfma_f32_16x16x32_bf16 v[52:55], a[200:203], a[16:19], v[52:55]\n" \
"ds_read_b128 a[8:11], v205 offset:0\n" \
"v_add_f32 v112, v112, v184\n" \
"v_mfma_f32_16x16x32_bf16 v[52:55], a[204:207], a[20:23], v[52:55]\n" \
"ds_read_b128 a[12:15], v206 offset:0\n" \
"v_add_f32 v113, v113, v185\n" \
"v_mfma_f32_16x16x32_bf16 v[92:95], a[16:19], a[216:219], v[92:95]\n" \
"ds_read_b64_tr_b16 a[44:45], v209 offset:12288\n" \
"v_add_f32 v114, v114, v186\n" \
"v_mfma_f32_16x16x32_bf16 v[92:95], a[20:23], a[220:223], v[92:95]\n" \
"buffer_load_dwordx4 v192, s[44:47], s73 offen offset:2048 lds\n" \
"v_add_f32 v115, v115, v187\n" \
"v_mfma_f32_16x16x32_bf16 v[88:91], a[16:19], a[208:211], v[88:91]\n" \
"ds_read_b64_tr_b16 a[46:47], v209 offset:15360\n" \
"v_add_f32 v116, v116, v188\n" \
"v_mfma_f32_16x16x32_bf16 v[88:91], a[20:23], a[212:215], v[88:91]\n" \
"ds_read_b64_tr_b16 a[48:49], v210 offset:0\n" \
"v_add_f32 v117, v117, v189\n" \
"v_mfma_f32_16x16x32_bf16 v[96:99], a[24:27], a[208:211], v[96:99]\n" \
"ds_read_b128 a[16:19], v205 offset:4096\n" \
"v_add_f32 v118, v118, v190\n" \
"v_mfma_f32_16x16x32_bf16 v[96:99], a[28:31], a[212:215], v[96:99]\n" \
"buffer_load_dwordx4 v193, s[44:47], s73 offen offset:3072 lds\n" \
"v_add_f32 v119, v119, v191\n" \
"v_mfma_f32_16x16x32_bf16 v[100:103], a[24:27], a[216:219], v[100:103]\n" \
"ds_read_b128 a[20:23], v206 offset:4096\n" \
"v_mul_f32 v112, v112, v233\n" \
"v_mfma_f32_16x16x32_bf16 v[100:103], a[28:31], a[220:223], v[100:103]\n" \
"s_add_u32 m0, s89, 65536\n" \
"ds_read_b64_tr_b16 a[50:51], v210 offset:3072\n" \
"v_mul_f32 v113, v113, v233\n" \
"v_mul_f32 v114, v114, v233\n" \
"v_mfma_f32_16x16x32_bf16 v[60:63], a[200:203], a[24:27], v[60:63]\n" \
"s_xor_b32 s73, s73, 0x80\n" \
"ds_read_b64_tr_b16 a[52:53], v210 offset:12288\n" \
"v_mul_f32 v115, v115, v233\n" \
"v_mfma_f32_16x16x32_bf16 v[60:63], a[204:207], a[28:31], v[60:63]\n" \
"buffer_load_dwordx4 v196, s[44:47], s75 offen lds\n" \
"v_mfma_f32_16x16x32_bf16 v[68:71], a[200:203], a[192:195], v[68:71]\n" \
"s_xor_b32 s75, s75, 0x80\n" \
"s_mul_i32 s76, s80, 393216\n" \
"ds_read_b128 a[24:27], v205 offset:8192\n" \
"v_mfma_f32_16x16x32_bf16 v[68:71], a[204:207], a[196:199], v[68:71]\n" \
"s_add_u32 s76, s76, s78\n" \
"ds_read_b128 a[28:31], v206 offset:8192\n" \
"v_mul_f32 v116, v116, v233\n" \
"v_mul_f32 v117, v117, v233\n" \
"v_mfma_f32_16x16x32_bf16 v[108:111], a[192:195], a[216:219], v[108:111]\n" \
"s_add_u32 m0, s91, 86016\n" \
"ds_read_b64_tr_b16 a[54:55], v210 offset:15360\n" \
"v_mul_f32 v118, v118, v233\n" \
"v_mfma_f32_16x16x32_bf16 v[108:111], a[196:199], a[220:223], v[108:111]\n" \
"buffer_load_dwordx4 v197, s[48:51], s76 offen offset:0 lds\n" \
"v_mfma_f32_16x16x32_bf16 v[104:107], a[192:195], a[208:211], v[104:107]\n" \
"v_mul_f32 v119, v119, v233\n" \
"v_cvt_pk_bf16_f32 v124, v112, v113\n" \
"v_mfma_f32_16x16x32_bf16 v[104:107], a[196:199], a[212:215], v[104:107]\n" \
"s_waitcnt lgkmcnt(2)\n" \
"v_cvt_pk_bf16_f32 v125, v114, v115\n" \
"v_cvt_pk_bf16_f32 v126, v116, v117\n" \
"v_cvt_pk_bf16_f32 v127, v118, v119\n" \
"v_mfma_f32_16x16x32_bf16 v[32:35], a[32:35], a[0:3], v[32:35]\n" \
"s_add_u32 s77, s76, 24576\n" \
"ds_read_b128 a[192:195], v205 offset:12288\n" \
"v_mfma_f32_16x16x32_bf16 v[32:35], a[36:39], a[4:7], v[32:35]\n" \
"buffer_load_dwordx4 v198, s[48:51], s76 offen offset:1024 lds\n" \
"v_mfma_f32_16x16x32_bf16 v[40:43], a[32:35], a[8:11], v[40:43]\n" \
"ds_read_b128 a[196:199], v206 offset:12288\n" \
"v_mfma_f32_16x16x32_bf16 v[40:43], a[36:39], a[12:15], v[40:43]\n" \
"ds_read_b64_tr_b16 a[200:201], v210 offset:1024\n" \
"buffer_store_dwordx4 v[124:127], v240, s[56:59], s82 offen offset:2048 sc1\n" \
"v_mfma_f32_16x16x32_bf16 v[0:3], a[40:43], a[8:11], v[0:3]\n" \
"s_waitcnt lgkmcnt(3)\n" \
"s_xor_b32 s80, s80, 0x1\n" \
"ds_read_b64_tr_b16 a[202:203], v210 offset:4096\n" \
"v_mfma_f32_16x16x32_bf16 v[0:3], a[44:47], a[12:15], v[0:3]\n" \
"buffer_load_dwordx4 v199, s[48:51], s76 offen offset:2048 lds\n" \
"v_mfma_f32_16x16x32_bf16 v[224:227], v[244:247], v[124:127], v[224:227]\n" \
"v_mfma_f32_16x16x32_bf16 v[4:7], a[48:51], a[8:11], v[4:7]\n" \
"ds_read_b64_tr_b16 a[204:205], v210 offset:13312\n" \
"v_mfma_f32_16x16x32_bf16 v[4:7], a[52:55], a[12:15], v[4:7]\n" \
"ds_read_b64_tr_b16 a[206:207], v210 offset:16384\n" \
"v_mfma_f32_16x16x32_bf16 v[12:15], a[48:51], a[16:19], v[12:15]\n" \
"s_add_u32 m0, m0, 3072\n" \
"ds_read_b64_tr_b16 a[208:209], v209 offset:2048\n" \
"v_mfma_f32_16x16x32_bf16 v[12:15], a[52:55], a[20:23], v[12:15]\n" \
"buffer_load_dwordx4 v197, s[48:51], s77 offen offset:0 lds\n" \
"v_mfma_f32_16x16x32_bf16 v[8:11], a[40:43], a[16:19], v[8:11]\n" \
"ds_read_b64_tr_b16 a[210:211], v209 offset:5120\n" \
"v_mfma_f32_16x16x32_bf16 v[8:11], a[44:47], a[20:23], v[8:11]\n" \
"ds_read_b64_tr_b16 a[212:213], v209 offset:14336\n" \
"v_mfma_f32_16x16x32_bf16 v[48:51], a[32:35], a[16:19], v[48:51]\n" \
"ds_read_b64_tr_b16 a[214:215], v209 offset:17408\n" \
"v_mfma_f32_16x16x32_bf16 v[48:51], a[36:39], a[20:23], v[48:51]\n" \
"buffer_load_dwordx4 v198, s[48:51], s77 offen offset:1024 lds\n" \
"v_mfma_f32_16x16x32_bf16 v[56:59], a[32:35], a[24:27], v[56:59]\n" \
"ds_read_b64_tr_b16 a[216:217], v210 offset:2048\n" \
"v_mfma_f32_16x16x32_bf16 v[56:59], a[36:39], a[28:31], v[56:59]\n" \
"ds_read_b64_tr_b16 a[218:219], v210 offset:5120\n" \
"v_mfma_f32_16x16x32_bf16 v[16:19], a[40:43], a[24:27], v[16:19]\n" \
"ds_read_b64_tr_b16 a[220:221], v210 offset:14336\n" \
"v_mfma_f32_16x16x32_bf16 v[16:19], a[44:47], a[28:31], v[16:19]\n" \
"buffer_load_dwordx4 v199, s[48:51], s77 offen offset:2048 lds\n" \
"v_mfma_f32_16x16x32_bf16 v[20:23], a[48:51], a[24:27], v[20:23]\n" \
"ds_read_b64_tr_b16 a[222:223], v210 offset:17408\n" \
"v_mfma_f32_16x16x32_bf16 v[20:23], a[52:55], a[28:31], v[20:23]\n" \
"s_waitcnt lgkmcnt(3)\n" \
"v_mfma_f32_16x16x32_bf16 v[28:31], a[48:51], a[192:195], v[28:31]\n" \
"v_mfma_f32_16x16x32_bf16 v[28:31], a[52:55], a[196:199], v[28:31]\n" \
"v_mfma_f32_16x16x32_bf16 v[24:27], a[40:43], a[192:195], v[24:27]\n" \
"v_add_u32 v205, 110592, v200\n" \
"v_add_u32 v206, 110592, v201\n" \
"v_mfma_f32_16x16x32_bf16 v[24:27], a[44:47], a[196:199], v[24:27]\n" \
"v_add_u32 v207, 110592, v202\n" \
"v_add_u32 v208, 110592, v203\n" \
"v_mfma_f32_16x16x32_bf16 v[64:67], a[32:35], a[192:195], v[64:67]\n" \
"v_add_u32 v209, 139264, v204\n" \
"v_xor_b32 v210, 32, v209\n" \
"v_mfma_f32_16x16x32_bf16 v[64:67], a[36:39], a[196:199], v[64:67]\n" \
"s_waitcnt vmcnt(13) lgkmcnt(0)\n" \
"s_barrier\n" \
"s_movk_i32 s64, 0x207\n" \
"v_cmp_class_f32 vcc, v224, s64\n" \
"s_nop 1\n" \
"s_cmp_lg_u64 vcc, 0\n" \
"s_addc_u32 s90, s90, 0\n" \
"s_cmp_lg_u32 s90, 0\n" \
"s_cbranch_scc1 Lexact_%=\n" \
"Lexr_%=:\n" \
"v_mfma_f32_16x16x32_bf16 v[72:75], a[0:3], a[208:211], v[72:75]\n" \
"s_lshl_b32 s15, s63, 7\n" \
"ds_read_b64_tr_b16 a[32:33], v209 offset:1024\n" \
"ds_read_b64_tr_b16 a[34:35], v209 offset:4096\n" \
"v_mfma_f32_16x16x32_bf16 v[72:75], a[4:7], a[212:215], v[72:75]\n" \
"s_lshl2_add_u32 s16, s62, 65589248\n" \
"ds_read_b64_tr_b16 a[36:37], v209 offset:13312\n" \
"ds_read_b64_tr_b16 a[38:39], v209 offset:16384\n" \
"v_mfma_f32_16x16x32_bf16 v[80:83], a[8:11], a[208:211], v[80:83]\n" \
"s_add_u32 m0, s79, 0\n" \
"s_sub_u32 s17, s16, s15\n" \
"ds_read_b128 a[0:3], v207 offset:0\n" \
"Lpg8443_%=:\n" \
"Lst1x1_%=:\n" \
"s_cbranch_execz Lpg10770_%=\n" \
"v_mfma_f32_16x16x32_bf16 v[80:83], a[12:15], a[212:215], v[80:83]\n" \
"buffer_load_dwordx4 v192, s[44:47], s73 offen offset:0 lds\n" \
"v_mfma_f32_16x16x32_bf16 v[84:87], a[8:11], a[216:219], v[84:87]\n" \
"s_add_u32 s15, s15, s16\n" \
"s_add_u32 s17, s17, 1920\n" \
"ds_read_b128 a[4:7], v208 offset:0\n" \
"v_mfma_f32_16x16x32_bf16 v[84:87], a[12:15], a[220:223], v[84:87]\n" \
"ds_read_b64_tr_b16 a[40:41], v209 offset:0\n" \
"v_mov_b32 v146, s60\n" \
"v_mfma_f32_16x16x32_bf16 v[44:47], a[200:203], a[8:11], v[44:47]\n" \
"ds_read_b64_tr_b16 a[42:43], v209 offset:3072\n" \
"v_lshlrev_b32 v147, 28, s61\n" \
"v_mfma_f32_16x16x32_bf16 v[44:47], a[204:207], a[12:15], v[44:47]\n" \
"buffer_load_dwordx4 v193, s[44:47], s73 offen offset:1024 lds\n" \
"v_mfma_f32_16x16x32_bf16 v[52:55], a[200:203], a[16:19], v[52:55]\n" \
"ds_read_b128 a[8:11], v205 offset:0\n" \
"v_mfma_f32_16x16x32_bf16 v[52:55], a[204:207], a[20:23], v[52:55]\n" \
"ds_read_b128 a[12:15], v206 offset:0\n" \
"v_mfma_f32_16x16x32_bf16 v[92:95], a[16:19], a[216:219], v[92:95]\n" \
"ds_read_b64_tr_b16 a[44:45], v209 offset:12288\n" \
"v_mfma_f32_16x16x32_bf16 v[92:95], a[20:23], a[220:223], v[92:95]\n" \
"buffer_load_dwordx4 v192, s[44:47], s73 offen offset:2048 lds\n" \
"v_mfma_f32_16x16x32_bf16 v[88:91], a[16:19], a[208:211], v[88:91]\n" \
"ds_read_b64_tr_b16 a[46:47], v209 offset:15360\n" \
"v_mfma_f32_16x16x32_bf16 v[88:91], a[20:23], a[212:215], v[88:91]\n" \
"ds_read_b64_tr_b16 a[48:49], v210 offset:0\n" \
"v_mfma_f32_16x16x32_bf16 v[96:99], a[24:27], a[208:211], v[96:99]\n" \
"ds_read_b128 a[16:19], v205 offset:4096\n" \
"v_mfma_f32_16x16x32_bf16 v[96:99], a[28:31], a[212:215], v[96:99]\n" \
"buffer_load_dwordx4 v193, s[44:47], s73 offen offset:3072 lds\n" \
"v_mfma_f32_16x16x32_bf16 v[100:103], a[24:27], a[216:219], v[100:103]\n" \
"ds_read_b128 a[20:23], v206 offset:4096\n" \
"v_mfma_f32_16x16x32_bf16 v[100:103], a[28:31], a[220:223], v[100:103]\n" \
"s_add_u32 m0, s89, 0\n" \
"ds_read_b64_tr_b16 a[50:51], v210 offset:3072\n" \
"v_mfma_f32_16x16x32_bf16 v[60:63], a[200:203], a[24:27], v[60:63]\n" \
"s_xor_b32 s73, s73, 0x780\n" \
"ds_read_b64_tr_b16 a[52:53], v210 offset:12288\n" \
"v_mfma_f32_16x16x32_bf16 v[60:63], a[204:207], a[28:31], v[60:63]\n" \
"buffer_load_dwordx4 v196, s[44:47], s75 offen lds\n" \
"v_mfma_f32_16x16x32_bf16 v[68:71], a[200:203], a[192:195], v[68:71]\n" \
"s_xor_b32 s75, s75, 0x780\n" \
"s_mul_i32 s76, s80, 393216\n" \
"ds_read_b128 a[24:27], v205 offset:8192\n" \
"v_mfma_f32_16x16x32_bf16 v[68:71], a[204:207], a[196:199], v[68:71]\n" \
"s_add_u32 s76, s76, s78\n" \
"ds_read_b128 a[28:31], v206 offset:8192\n" \
"v_mfma_f32_16x16x32_bf16 v[108:111], a[192:195], a[216:219], v[108:111]\n" \
"s_add_u32 m0, s91, 20480\n" \
"ds_read_b64_tr_b16 a[54:55], v210 offset:15360\n" \
"v_mfma_f32_16x16x32_bf16 v[108:111], a[196:199], a[220:223], v[108:111]\n" \
"buffer_load_dwordx4 v197, s[48:51], s76 offen offset:0 lds\n" \
"v_mfma_f32_16x16x32_bf16 v[104:107], a[192:195], a[208:211], v[104:107]\n" \
"v_mfma_f32_16x16x32_bf16 v[104:107], a[196:199], a[212:215], v[104:107]\n" \
"s_waitcnt lgkmcnt(2)\n" \
"v_mfma_f32_16x16x32_bf16 v[32:35], a[32:35], a[0:3], v[32:35]\n" \
"s_add_u32 s77, s76, 24576\n" \
"ds_read_b128 a[192:195], v205 offset:12288\n" \
"v_mfma_f32_16x16x32_bf16 v[32:35], a[36:39], a[4:7], v[32:35]\n" \
"buffer_load_dwordx4 v198, s[48:51], s76 offen offset:1024 lds\n" \
"v_mfma_f32_16x16x32_bf16 v[40:43], a[32:35], a[8:11], v[40:43]\n" \
"ds_read_b128 a[196:199], v206 offset:12288\n" \
"v_mfma_f32_16x16x32_bf16 v[40:43], a[36:39], a[12:15], v[40:43]\n" \
"ds_read_b64_tr_b16 a[200:201], v210 offset:1024\n" \
"v_mfma_f32_16x16x32_bf16 v[0:3], a[40:43], a[8:11], v[0:3]\n" \
"s_waitcnt lgkmcnt(3)\n" \
"s_xor_b32 s80, s80, 0xf\n" \
"ds_read_b64_tr_b16 a[202:203], v210 offset:4096\n" \
"v_mfma_f32_16x16x32_bf16 v[0:3], a[44:47], a[12:15], v[0:3]\n" \
"buffer_load_dwordx4 v199, s[48:51], s76 offen offset:2048 lds\n" \
"v_mfma_f32_16x16x32_bf16 v[4:7], a[48:51], a[8:11], v[4:7]\n" \
"ds_read_b64_tr_b16 a[204:205], v210 offset:13312\n" \
"v_mfma_f32_16x16x32_bf16 v[4:7], a[52:55], a[12:15], v[4:7]\n" \
"ds_read_b64_tr_b16 a[206:207], v210 offset:16384\n" \
"v_mfma_f32_16x16x32_bf16 v[12:15], a[48:51], a[16:19], v[12:15]\n" \
"s_add_u32 m0, m0, 3072\n" \
"ds_read_b64_tr_b16 a[208:209], v209 offset:2048\n" \
"v_mfma_f32_16x16x32_bf16 v[12:15], a[52:55], a[20:23], v[12:15]\n" \
"buffer_load_dwordx4 v197, s[48:51], s77 offen offset:0 lds\n" \
"v_mfma_f32_16x16x32_bf16 v[8:11], a[40:43], a[16:19], v[8:11]\n" \
"ds_read_b64_tr_b16 a[210:211], v209 offset:5120\n" \
"v_mfma_f32_16x16x32_bf16 v[8:11], a[44:47], a[20:23], v[8:11]\n" \
"ds_read_b64_tr_b16 a[212:213], v209 offset:14336\n" \
"v_mfma_f32_16x16x32_bf16 v[48:51], a[32:35], a[16:19], v[48:51]\n" \
"ds_read_b64_tr_b16 a[214:215], v209 offset:17408\n" \
"v_mfma_f32_16x16x32_bf16 v[48:51], a[36:39], a[20:23], v[48:51]\n" \
"buffer_load_dwordx4 v198, s[48:51], s77 offen offset:1024 lds\n" \
"v_mfma_f32_16x16x32_bf16 v[56:59], a[32:35], a[24:27], v[56:59]\n" \
"ds_read_b64_tr_b16 a[216:217], v210 offset:2048\n" \
"v_mfma_f32_16x16x32_bf16 v[56:59], a[36:39], a[28:31], v[56:59]\n" \
"ds_read_b64_tr_b16 a[218:219], v210 offset:5120\n" \
"v_mfma_f32_16x16x32_bf16 v[16:19], a[40:43], a[24:27], v[16:19]\n" \
"ds_read_b64_tr_b16 a[220:221], v210 offset:14336\n" \
"v_mfma_f32_16x16x32_bf16 v[16:19], a[44:47], a[28:31], v[16:19]\n" \
"buffer_load_dwordx4 v199, s[48:51], s77 offen offset:2048 lds\n" \
"v_mfma_f32_16x16x32_bf16 v[20:23], a[48:51], a[24:27], v[20:23]\n" \
"ds_read_b64_tr_b16 a[222:223], v210 offset:17408\n" \
"v_mfma_f32_16x16x32_bf16 v[20:23], a[52:55], a[28:31], v[20:23]\n" \
"s_waitcnt lgkmcnt(3)\n" \
"v_mfma_f32_16x16x32_bf16 v[28:31], a[48:51], a[192:195], v[28:31]\n" \
"v_add_u32 v254, 3145728, v254\n" \
"v_mfma_f32_16x16x32_bf16 v[28:31], a[52:55], a[196:199], v[28:31]\n" \
"global_load_dword v253, v254, s[22:23]\n" \
"v_mfma_f32_16x16x32_bf16 v[24:27], a[40:43], a[192:195], v[24:27]\n" \
"v_add_u32 v205, 65536, v200\n" \
"v_add_u32 v206, 65536, v201\n" \
"v_mfma_f32_16x16x32_bf16 v[24:27], a[44:47], a[196:199], v[24:27]\n" \
"v_add_u32 v207, 65536, v202\n" \
"v_add_u32 v208, 65536, v203\n" \
"v_mfma_f32_16x16x32_bf16 v[64:67], a[32:35], a[192:195], v[64:67]\n" \
"v_add_u32 v209, 86016, v204\n" \
"v_xor_b32 v210, 32, v209\n" \
"v_mfma_f32_16x16x32_bf16 v[64:67], a[36:39], a[196:199], v[64:67]\n" \
"s_waitcnt vmcnt(12) lgkmcnt(0)\n" \
"s_barrier\n" \
"v_mfma_f32_16x16x32_bf16 v[72:75], a[0:3], a[208:211], v[72:75]\n" \
"ds_read_b64_tr_b16 a[32:33], v209 offset:1024\n" \
"ds_read_b64_tr_b16 a[34:35], v209 offset:4096\n" \
"v_mfma_f32_16x16x32_bf16 v[72:75], a[4:7], a[212:215], v[72:75]\n" \
"ds_read_b64_tr_b16 a[36:37], v209 offset:13312\n" \
"ds_read_b64_tr_b16 a[38:39], v209 offset:16384\n" \
"v_mfma_f32_16x16x32_bf16 v[80:83], a[8:11], a[208:211], v[80:83]\n" \
"s_add_u32 m0, s79, 110592\n" \
"ds_read_b128 a[0:3], v207 offset:0\n" \
"buffer_store_dword v146, v147, s[56:59], s15 offen sc1\n" \
"v_mfma_f32_16x16x32_bf16 v[80:83], a[12:15], a[212:215], v[80:83]\n" \
"buffer_load_dwordx4 v192, s[44:47], s73 offen offset:0 lds\n" \
"v_mfma_f32_16x16x32_bf16 v[84:87], a[8:11], a[216:219], v[84:87]\n" \
"ds_read_b128 a[4:7], v208 offset:0\n" \
"v_mfma_f32_16x16x32_bf16 v[84:87], a[12:15], a[220:223], v[84:87]\n" \
"ds_read_b64_tr_b16 a[40:41], v209 offset:0\n" \
"buffer_store_dword v146, v147, s[56:59], s17 offen sc1\n" \
"v_mfma_f32_16x16x32_bf16 v[44:47], a[200:203], a[8:11], v[44:47]\n" \
"s_mov_b64 s[36:37], s[18:19]\n" \
"ds_read_b64_tr_b16 a[42:43], v209 offset:3072\n" \
"v_mfma_f32_16x16x32_bf16 v[44:47], a[204:207], a[12:15], v[44:47]\n" \
"buffer_load_dwordx4 v193, s[44:47], s73 offen offset:1024 lds\n" \
"v_mfma_f32_16x16x32_bf16 v[52:55], a[200:203], a[16:19], v[52:55]\n" \
"s_sub_u32 s36, s36, 4096\n" \
"s_subb_u32 s37, s37, 0\n" \
"ds_read_b128 a[8:11], v205 offset:0\n" \
"v_mfma_f32_16x16x32_bf16 v[52:55], a[204:207], a[20:23], v[52:55]\n" \
"s_and_b32 s37, s37, 0xffff\n" \
"s_mov_b32 s38, 8392704\n" \
"ds_read_b128 a[12:15], v206 offset:0\n" \
"v_mfma_f32_16x16x32_bf16 v[92:95], a[16:19], a[216:219], v[92:95]\n" \
"s_mov_b32 s39, 0x20000\n" \
"s_mov_b64 s[40:41], s[24:25]\n" \
"ds_read_b64_tr_b16 a[44:45], v209 offset:12288\n" \
"v_mfma_f32_16x16x32_bf16 v[92:95], a[20:23], a[220:223], v[92:95]\n" \
"buffer_load_dwordx4 v192, s[44:47], s73 offen offset:2048 lds\n" \
"v_mfma_f32_16x16x32_bf16 v[88:91], a[16:19], a[208:211], v[88:91]\n" \
"s_sub_u32 s40, s40, 4096\n" \
"s_subb_u32 s41, s41, 0\n" \
"ds_read_b64_tr_b16 a[46:47], v209 offset:15360\n" \
"v_mfma_f32_16x16x32_bf16 v[88:91], a[20:23], a[212:215], v[88:91]\n" \
"s_and_b32 s41, s41, 0xffff\n" \
"s_mov_b32 s42, 2101248\n" \
"ds_read_b64_tr_b16 a[48:49], v210 offset:0\n" \
"v_mfma_f32_16x16x32_bf16 v[96:99], a[24:27], a[208:211], v[96:99]\n" \
"s_mov_b32 s43, 0x20000\n" \
"s_mov_b64 s[52:53], s[26:27]\n" \
"ds_read_b128 a[16:19], v205 offset:4096\n" \
"v_mfma_f32_16x16x32_bf16 v[96:99], a[28:31], a[212:215], v[96:99]\n" \
"buffer_load_dwordx4 v193, s[44:47], s73 offen offset:3072 lds\n" \
"v_mfma_f32_16x16x32_bf16 v[100:103], a[24:27], a[216:219], v[100:103]\n" \
"s_and_b32 s53, s53, 0xffff\n" \
"s_mov_b32 s54, 8388608\n" \
"ds_read_b128 a[20:23], v206 offset:4096\n" \
"v_mfma_f32_16x16x32_bf16 v[100:103], a[28:31], a[220:223], v[100:103]\n" \
"s_add_u32 m0, s89, 110592\n" \
"s_mov_b32 s55, 0x20000\n" \
"ds_read_b64_tr_b16 a[50:51], v210 offset:3072\n" \
"v_mfma_f32_16x16x32_bf16 v[60:63], a[200:203], a[24:27], v[60:63]\n" \
"s_xor_b32 s73, s73, 0x80\n" \
"ds_read_b64_tr_b16 a[52:53], v210 offset:12288\n" \
"v_lshrrev_b32 v221, 2, v234\n" \
"v_mfma_f32_16x16x32_bf16 v[60:63], a[204:207], a[28:31], v[60:63]\n" \
"buffer_load_dwordx4 v196, s[44:47], s75 offen lds\n" \
"v_mfma_f32_16x16x32_bf16 v[68:71], a[200:203], a[192:195], v[68:71]\n" \
"s_xor_b32 s75, s75, 0x80\n" \
"s_mul_i32 s76, s80, 393216\n" \
"ds_read_b128 a[24:27], v205 offset:8192\n" \
"v_mfma_f32_16x16x32_bf16 v[68:71], a[204:207], a[196:199], v[68:71]\n" \
"s_add_u32 s76, s76, s78\n" \
"ds_read_b128 a[28:31], v206 offset:8192\n" \
"v_lshrrev_b32 v244, 4, v234\n" \
"v_mfma_f32_16x16x32_bf16 v[108:111], a[192:195], a[216:219], v[108:111]\n" \
"s_add_u32 m0, s91, 139264\n" \
"s_lshl_b32 s12, s94, 7\n" \
"ds_read_b64_tr_b16 a[54:55], v210 offset:15360\n" \
"v_mfma_f32_16x16x32_bf16 v[108:111], a[196:199], a[220:223], v[108:111]\n" \
"buffer_load_dwordx4 v197, s[48:51], s76 offen offset:0 lds\n" \
"v_mfma_f32_16x16x32_bf16 v[104:107], a[192:195], a[208:211], v[104:107]\n" \
"s_add_u32 s12, s12, 65591296\n" \
"s_and_b32 s70, %1, 1\n" \
"s_lshl3_add_u32 s70, s70, s63\n" \
"v_mfma_f32_16x16x32_bf16 v[104:107], a[196:199], a[212:215], v[104:107]\n" \
"s_waitcnt lgkmcnt(2)\n" \
"s_lshl2_add_u32 s12, s70, s12\n" \
"s_lshr_b32 s68, s63, 1\n" \
"s_add_u32 s68, s68, 2\n" \
"v_mfma_f32_16x16x32_bf16 v[32:35], a[32:35], a[0:3], v[32:35]\n" \
"s_add_u32 s77, s76, 24576\n" \
"s_mov_b32 s13, 65593856\n" \
"ds_read_b128 a[192:195], v205 offset:12288\n" \
"v_mfma_f32_16x16x32_bf16 v[32:35], a[36:39], a[4:7], v[32:35]\n" \
"buffer_load_dwordx4 v198, s[48:51], s76 offen offset:1024 lds\n" \
"v_mfma_f32_16x16x32_bf16 v[40:43], a[32:35], a[8:11], v[40:43]\n" \
"s_lshr_b32 s70, s18, 8\n" \
"ds_read_b128 a[196:199], v206 offset:12288\n" \
"v_lshrrev_b32 v228, 1, v234\n" \
"v_mfma_f32_16x16x32_bf16 v[40:43], a[36:39], a[12:15], v[40:43]\n" \
"s_sub_u32 s68, s68, s70\n" \
"s_and_b32 s68, s68, 3\n" \
"ds_read_b64_tr_b16 a[200:201], v210 offset:1024\n" \
"v_mfma_f32_16x16x32_bf16 v[0:3], a[40:43], a[8:11], v[0:3]\n" \
"s_xor_b32 s80, s80, 0x1\n" \
"s_and_b32 s70, s63, 1\n" \
"ds_read_b64_tr_b16 a[202:203], v210 offset:4096\n" \
"v_mfma_f32_16x16x32_bf16 v[0:3], a[44:47], a[12:15], v[0:3]\n" \
"s_waitcnt lgkmcnt(3)\n" \
"buffer_load_dwordx4 v199, s[48:51], s76 offen offset:2048 lds\n" \
"v_mfma_f32_16x16x32_bf16 v[4:7], a[48:51], a[8:11], v[4:7]\n" \
"s_lshl_b32 s70, s70, 2\n" \
"s_or_b32 s68, s68, s70\n" \
"ds_read_b64_tr_b16 a[204:205], v210 offset:13312\n" \
"v_mfma_f32_16x16x32_bf16 v[4:7], a[52:55], a[12:15], v[4:7]\n" \
"s_lshr_b32 s84, s61, 1\n" \
"s_lshl_b32 s84, s84, 16\n" \
"ds_read_b64_tr_b16 a[206:207], v210 offset:16384\n" \
"v_mfma_f32_16x16x32_bf16 v[12:15], a[48:51], a[16:19], v[12:15]\n" \
"s_add_u32 m0, m0, 3072\n" \
"s_and_b32 s70, s61, 1\n" \
"ds_read_b64_tr_b16 a[208:209], v209 offset:2048\n" \
"v_mfma_f32_16x16x32_bf16 v[12:15], a[52:55], a[20:23], v[12:15]\n" \
"buffer_load_dwordx4 v197, s[48:51], s77 offen offset:0 lds\n" \
"v_mfma_f32_16x16x32_bf16 v[8:11], a[40:43], a[16:19], v[8:11]\n" \
"s_lshl_b32 s70, s70, 14\n" \
"s_add_u32 s84, s84, s70\n" \
"ds_read_b64_tr_b16 a[210:211], v209 offset:5120\n" \
"v_mfma_f32_16x16x32_bf16 v[8:11], a[44:47], a[20:23], v[8:11]\n" \
"s_lshl_b32 s88, s68, 8\n" \
"s_add_u32 s84, s84, s88\n" \
"ds_read_b64_tr_b16 a[212:213], v209 offset:14336\n" \
"v_mfma_f32_16x16x32_bf16 v[48:51], a[32:35], a[16:19], v[48:51]\n" \
"ds_read_b64_tr_b16 a[214:215], v209 offset:17408\n" \
"v_bfe_u32 v245, v244, 2, 3\n" \
"v_mfma_f32_16x16x32_bf16 v[48:51], a[36:39], a[20:23], v[48:51]\n" \
"buffer_load_dwordx4 v198, s[48:51], s77 offen offset:1024 lds\n" \
"v_mfma_f32_16x16x32_bf16 v[56:59], a[32:35], a[24:27], v[56:59]\n" \
"ds_read_b64_tr_b16 a[216:217], v210 offset:2048\n" \
"v_lshlrev_b32 v245, 11, v245\n" \
"v_mfma_f32_16x16x32_bf16 v[56:59], a[36:39], a[28:31], v[56:59]\n" \
"ds_read_b64_tr_b16 a[218:219], v210 offset:5120\n" \
"v_lshrrev_b32 v246, 5, v244\n" \
"v_mfma_f32_16x16x32_bf16 v[16:19], a[40:43], a[24:27], v[16:19]\n" \
"ds_read_b64_tr_b16 a[220:221], v210 offset:14336\n" \
"v_lshl_add_u32 v245, v246, 6, v245\n" \
"v_mfma_f32_16x16x32_bf16 v[16:19], a[44:47], a[28:31], v[16:19]\n" \
"buffer_load_dwordx4 v199, s[48:51], s77 offen offset:2048 lds\n" \
"v_mfma_f32_16x16x32_bf16 v[20:23], a[48:51], a[24:27], v[20:23]\n" \
"ds_read_b64_tr_b16 a[222:223], v210 offset:17408\n" \
"v_and_b32 v246, 3, v244\n" \
"v_mfma_f32_16x16x32_bf16 v[20:23], a[52:55], a[28:31], v[20:23]\n" \
"s_waitcnt lgkmcnt(3)\n" \
"v_bfe_u32 v247, v244, 4, 1\n" \
"v_lshlrev_b32 v247, 1, v247\n" \
"v_mfma_f32_16x16x32_bf16 v[28:31], a[48:51], a[192:195], v[28:31]\n" \
"v_xor_b32 v246, v246, v247\n" \
"v_lshl_add_u32 v245, v246, 4, v245\n" \
"v_mfma_f32_16x16x32_bf16 v[28:31], a[52:55], a[196:199], v[28:31]\n" \
"buffer_load_dwordx2 v[226:227], v228, s[56:59], s13 offen sc1\n" \
"v_mfma_f32_16x16x32_bf16 v[24:27], a[40:43], a[192:195], v[24:27]\n" \
"v_add_u32 v205, 0, v200\n" \
"v_add_u32 v206, 0, v201\n" \
"v_mfma_f32_16x16x32_bf16 v[24:27], a[44:47], a[196:199], v[24:27]\n" \
"v_add_u32 v207, 0, v202\n" \
"v_add_u32 v208, 0, v203\n" \
"v_mfma_f32_16x16x32_bf16 v[64:67], a[32:35], a[192:195], v[64:67]\n" \
"v_add_u32 v209, 20480, v204\n" \
"v_xor_b32 v210, 32, v209\n" \
"v_mfma_f32_16x16x32_bf16 v[64:67], a[36:39], a[196:199], v[64:67]\n" \
"s_waitcnt vmcnt(15) lgkmcnt(0)\n" \
"s_barrier\n" \
"v_mfma_f32_16x16x32_bf16 v[72:75], a[0:3], a[208:211], v[72:75]\n" \
"ds_read_b64_tr_b16 a[32:33], v209 offset:1024\n" \
"ds_read_b64_tr_b16 a[34:35], v209 offset:4096\n" \
"v_mfma_f32_16x16x32_bf16 v[72:75], a[4:7], a[212:215], v[72:75]\n" \
"ds_read_b64_tr_b16 a[36:37], v209 offset:13312\n" \
"ds_read_b64_tr_b16 a[38:39], v209 offset:16384\n" \
"v_mfma_f32_16x16x32_bf16 v[80:83], a[8:11], a[208:211], v[80:83]\n" \
"s_add_u32 m0, s79, 65536\n" \
"ds_read_b128 a[0:3], v207 offset:0\n" \
"v_add_u32 v216, 4096, v245\n" \
"v_mfma_f32_16x16x32_bf16 v[80:83], a[12:15], a[212:215], v[80:83]\n" \
"buffer_load_dwordx4 v192, s[44:47], s73 offen offset:0 lds\n" \
"v_mfma_f32_16x16x32_bf16 v[84:87], a[8:11], a[216:219], v[84:87]\n" \
"ds_read_b128 a[4:7], v208 offset:0\n" \
"v_add_u32 v217, 3200, v245\n" \
"v_mfma_f32_16x16x32_bf16 v[84:87], a[12:15], a[220:223], v[84:87]\n" \
"ds_read_b64_tr_b16 a[40:41], v209 offset:0\n" \
"v_add_u32 v218, 34816, v245\n" \
"v_mfma_f32_16x16x32_bf16 v[44:47], a[200:203], a[8:11], v[44:47]\n" \
"ds_read_b64_tr_b16 a[42:43], v209 offset:3072\n" \
"v_add_u32 v219, 33920, v245\n" \
"v_mfma_f32_16x16x32_bf16 v[44:47], a[204:207], a[12:15], v[44:47]\n" \
"buffer_load_dwordx4 v193, s[44:47], s73 offen offset:1024 lds\n" \
"v_mfma_f32_16x16x32_bf16 v[52:55], a[200:203], a[16:19], v[52:55]\n" \
"s_mov_b32 s86, 65536\n" \
"s_lshr_b32 s70, s61, 1\n" \
"ds_read_b128 a[8:11], v205 offset:0\n" \
"v_mfma_f32_16x16x32_bf16 v[52:55], a[204:207], a[20:23], v[52:55]\n" \
"s_lshl_b32 s71, s70, 13\n" \
"s_add_u32 s71, s71, %3\n" \
"ds_read_b128 a[12:15], v206 offset:0\n" \
"v_mfma_f32_16x16x32_bf16 v[92:95], a[16:19], a[216:219], v[92:95]\n" \
"ds_read_b64_tr_b16 a[44:45], v209 offset:12288\n" \
"v_add_u32 v211, s71, v236\n" \
"v_mfma_f32_16x16x32_bf16 v[92:95], a[20:23], a[220:223], v[92:95]\n" \
"buffer_load_dwordx4 v192, s[44:47], s73 offen offset:2048 lds\n" \
"v_mfma_f32_16x16x32_bf16 v[88:91], a[16:19], a[208:211], v[88:91]\n" \
"s_lshl_b32 s70, s70, 18\n" \
"s_add_u32 s88, s88, s70\n" \
"ds_read_b64_tr_b16 a[46:47], v209 offset:15360\n" \
"v_mfma_f32_16x16x32_bf16 v[88:91], a[20:23], a[212:215], v[88:91]\n" \
"s_and_b32 s70, s61, 1\n" \
"s_lshl_b32 s71, s70, 7\n" \
"ds_read_b64_tr_b16 a[48:49], v210 offset:0\n" \
"v_mfma_f32_16x16x32_bf16 v[96:99], a[24:27], a[208:211], v[96:99]\n" \
"s_add_u32 s88, s88, s71\n" \
"ds_read_b128 a[16:19], v205 offset:4096\n" \
"v_and_b32 v245, 3, v244\n" \
"v_mfma_f32_16x16x32_bf16 v[96:99], a[28:31], a[212:215], v[96:99]\n" \
"buffer_load_dwordx4 v193, s[44:47], s73 offen offset:3072 lds\n" \
"v_mfma_f32_16x16x32_bf16 v[100:103], a[24:27], a[216:219], v[100:103]\n" \
"ds_read_b128 a[20:23], v206 offset:4096\n" \
"v_lshlrev_b32 v245, 3, v245\n" \
"v_mfma_f32_16x16x32_bf16 v[100:103], a[28:31], a[220:223], v[100:103]\n" \
"s_add_u32 m0, s89, 65536\n" \
"ds_read_b64_tr_b16 a[50:51], v210 offset:3072\n" \
"v_bfe_u32 v246, v244, 2, 2\n" \
"v_mfma_f32_16x16x32_bf16 v[60:63], a[200:203], a[24:27], v[60:63]\n" \
"s_xor_b32 s73, s73, 0x180\n" \
"ds_read_b64_tr_b16 a[52:53], v210 offset:12288\n" \
"v_lshl_add_u32 v245, v246, 6, v245\n" \
"v_mfma_f32_16x16x32_bf16 v[60:63], a[204:207], a[28:31], v[60:63]\n" \
"buffer_load_dwordx4 v196, s[44:47], s75 offen lds\n" \
"v_mfma_f32_16x16x32_bf16 v[68:71], a[200:203], a[192:195], v[68:71]\n" \
"s_xor_b32 s75, s75, 0x180\n" \
"s_mul_i32 s76, s80, 393216\n" \
"ds_read_b128 a[24:27], v205 offset:8192\n" \
"v_mfma_f32_16x16x32_bf16 v[68:71], a[204:207], a[196:199], v[68:71]\n" \
"s_add_u32 s76, s76, s78\n" \
"ds_read_b128 a[28:31], v206 offset:8192\n" \
"v_bfe_u32 v246, v244, 4, 1\n" \
"v_mfma_f32_16x16x32_bf16 v[108:111], a[192:195], a[216:219], v[108:111]\n" \
"s_add_u32 m0, s91, 86016\n" \
"ds_read_b64_tr_b16 a[54:55], v210 offset:15360\n" \
"v_lshl_add_u32 v245, v246, 5, v245\n" \
"v_mfma_f32_16x16x32_bf16 v[108:111], a[196:199], a[220:223], v[108:111]\n" \
"buffer_load_dwordx4 v197, s[48:51], s76 offen offset:0 lds\n" \
"v_mfma_f32_16x16x32_bf16 v[104:107], a[192:195], a[208:211], v[104:107]\n" \
"v_lshl_add_u32 v245, v246, 8, v245\n" \
"v_lshrrev_b32 v246, 5, v244\n" \
"v_mfma_f32_16x16x32_bf16 v[104:107], a[196:199], a[212:215], v[104:107]\n" \
"s_waitcnt lgkmcnt(2)\n" \
"v_lshl_add_u32 v245, v246, 12, v245\n" \
"s_lshl_b32 s71, s70, 10\n" \
"v_mfma_f32_16x16x32_bf16 v[32:35], a[32:35], a[0:3], v[32:35]\n" \
"s_add_u32 s77, s76, 24576\n" \
"s_add_u32 s71, s71, %3\n" \
"ds_read_b128 a[192:195], v205 offset:12288\n" \
"v_mfma_f32_16x16x32_bf16 v[32:35], a[36:39], a[4:7], v[32:35]\n" \
"buffer_load_dwordx4 v198, s[48:51], s76 offen offset:1024 lds\n" \
"v_mfma_f32_16x16x32_bf16 v[40:43], a[32:35], a[8:11], v[40:43]\n" \
"ds_read_b128 a[196:199], v206 offset:12288\n" \
"v_add_u32 v212, s71, v245\n" \
"v_mfma_f32_16x16x32_bf16 v[40:43], a[36:39], a[12:15], v[40:43]\n" \
"s_mov_b32 s69, 1\n" \
"s_mov_b32 s85, 98304\n" \
"ds_read_b64_tr_b16 a[200:201], v210 offset:1024\n" \
"v_mfma_f32_16x16x32_bf16 v[0:3], a[40:43], a[8:11], v[0:3]\n" \
"s_waitcnt lgkmcnt(3)\n" \
"s_xor_b32 s80, s80, 0x3\n" \
"ds_read_b64_tr_b16 a[202:203], v210 offset:4096\n" \
"v_mfma_f32_16x16x32_bf16 v[0:3], a[44:47], a[12:15], v[0:3]\n" \
"buffer_load_dwordx4 v199, s[48:51], s76 offen offset:2048 lds\n" \
"v_mfma_f32_16x16x32_bf16 v[4:7], a[48:51], a[8:11], v[4:7]\n" \
"s_sub_u32 s85, s85, 32768\n" \
"s_cselect_b32 s85, 131072, s85\n" \
"ds_read_b64_tr_b16 a[204:205], v210 offset:13312\n" \
"v_mfma_f32_16x16x32_bf16 v[4:7], a[52:55], a[12:15], v[4:7]\n" \
"ds_read_b64_tr_b16 a[206:207], v210 offset:16384\n" \
"v_add_u32 v213, s85, v211\n" \
"v_mfma_f32_16x16x32_bf16 v[12:15], a[48:51], a[16:19], v[12:15]\n" \
"s_add_u32 m0, m0, 3072\n" \
"ds_read_b64_tr_b16 a[208:209], v209 offset:2048\n" \
"v_add_u32 v214, s85, v212\n" \
"v_mfma_f32_16x16x32_bf16 v[12:15], a[52:55], a[20:23], v[12:15]\n" \
"buffer_load_dwordx4 v197, s[48:51], s77 offen offset:0 lds\n" \
"v_mfma_f32_16x16x32_bf16 v[8:11], a[40:43], a[16:19], v[8:11]\n" \
"ds_read_b64_tr_b16 a[210:211], v209 offset:5120\n" \
"v_xor_b32 v215, 32, v214\n" \
"v_mfma_f32_16x16x32_bf16 v[8:11], a[44:47], a[20:23], v[8:11]\n" \
"ds_read_b64_tr_b16 a[212:213], v209 offset:14336\n" \
"v_mov_b32 v230, s60\n" \
"v_mfma_f32_16x16x32_bf16 v[48:51], a[32:35], a[16:19], v[48:51]\n" \
"ds_read_b64_tr_b16 a[214:215], v209 offset:17408\n" \
"v_lshlrev_b32 v231, 28, s61\n" \
"v_mfma_f32_16x16x32_bf16 v[48:51], a[36:39], a[20:23], v[48:51]\n" \
"buffer_load_dwordx4 v198, s[48:51], s77 offen offset:1024 lds\n" \
"v_mfma_f32_16x16x32_bf16 v[56:59], a[32:35], a[24:27], v[56:59]\n" \
"ds_read_b64_tr_b16 a[216:217], v210 offset:2048\n" \
"v_mfma_f32_16x16x32_bf16 v[56:59], a[36:39], a[28:31], v[56:59]\n" \
"ds_read_b64_tr_b16 a[218:219], v210 offset:5120\n" \
"v_mfma_f32_16x16x32_bf16 v[16:19], a[40:43], a[24:27], v[16:19]\n" \
"ds_read_b64_tr_b16 a[220:221], v210 offset:14336\n" \
"v_mfma_f32_16x16x32_bf16 v[16:19], a[44:47], a[28:31], v[16:19]\n" \
"buffer_load_dwordx4 v199, s[48:51], s77 offen offset:2048 lds\n" \
"v_mfma_f32_16x16x32_bf16 v[20:23], a[48:51], a[24:27], v[20:23]\n" \
"ds_read_b64_tr_b16 a[222:223], v210 offset:17408\n" \
"v_mfma_f32_16x16x32_bf16 v[20:23], a[52:55], a[28:31], v[20:23]\n" \
"s_waitcnt lgkmcnt(3)\n" \
"v_mfma_f32_16x16x32_bf16 v[28:31], a[48:51], a[192:195], v[28:31]\n" \
"v_mfma_f32_16x16x32_bf16 v[28:31], a[52:55], a[196:199], v[28:31]\n" \
"v_mfma_f32_16x16x32_bf16 v[24:27], a[40:43], a[192:195], v[24:27]\n" \
"v_add_u32 v205, 110592, v200\n" \
"v_add_u32 v206, 110592, v201\n" \
"v_mfma_f32_16x16x32_bf16 v[24:27], a[44:47], a[196:199], v[24:27]\n" \
"v_add_u32 v207, 110592, v202\n" \
"v_add_u32 v208, 110592, v203\n" \
"v_mfma_f32_16x16x32_bf16 v[64:67], a[32:35], a[192:195], v[64:67]\n" \
"v_add_u32 v209, 139264, v204\n" \
"v_xor_b32 v210, 32, v209\n" \
"v_mfma_f32_16x16x32_bf16 v[64:67], a[36:39], a[196:199], v[64:67]\n" \
"s_waitcnt vmcnt(12) lgkmcnt(0)\n" \
"s_barrier\n" \
"v_mfma_f32_16x16x32_bf16 v[72:75], a[0:3], a[208:211], v[72:75]\n" \
"ds_read_b64_tr_b16 a[32:33], v209 offset:1024\n" \
"ds_read_b64_tr_b16 a[34:35], v209 offset:4096\n" \
"v_mfma_f32_16x16x32_bf16 v[72:75], a[4:7], a[212:215], v[72:75]\n" \
"ds_read_b64_tr_b16 a[36:37], v209 offset:13312\n" \
"ds_read_b64_tr_b16 a[38:39], v209 offset:16384\n" \
"v_mfma_f32_16x16x32_bf16 v[80:83], a[8:11], a[208:211], v[80:83]\n" \
"s_add_u32 m0, s79, 0\n" \
"ds_read_b128 a[0:3], v207 offset:0\n" \
"v_mfma_f32_16x16x32_bf16 v[80:83], a[12:15], a[212:215], v[80:83]\n" \
"buffer_load_dwordx4 v192, s[44:47], s73 offen offset:0 lds\n" \
"v_mfma_f32_16x16x32_bf16 v[84:87], a[8:11], a[216:219], v[84:87]\n" \
"ds_read_b128 a[4:7], v208 offset:0\n" \
"v_mfma_f32_16x16x32_bf16 v[84:87], a[12:15], a[220:223], v[84:87]\n" \
"ds_read_b64_tr_b16 a[40:41], v209 offset:0\n" \
"v_mfma_f32_16x16x32_bf16 v[44:47], a[200:203], a[8:11], v[44:47]\n" \
"ds_read_b64_tr_b16 a[42:43], v209 offset:3072\n" \
"v_mfma_f32_16x16x32_bf16 v[44:47], a[204:207], a[12:15], v[44:47]\n" \
"buffer_load_dwordx4 v193, s[44:47], s73 offen offset:1024 lds\n" \
"v_mfma_f32_16x16x32_bf16 v[52:55], a[200:203], a[16:19], v[52:55]\n" \
"ds_read_b128 a[8:11], v205 offset:0\n" \
"v_mfma_f32_16x16x32_bf16 v[52:55], a[204:207], a[20:23], v[52:55]\n" \
"ds_read_b128 a[12:15], v206 offset:0\n" \
"v_mfma_f32_16x16x32_bf16 v[92:95], a[16:19], a[216:219], v[92:95]\n" \
"ds_read_b64_tr_b16 a[44:45], v209 offset:12288\n" \
"v_mfma_f32_16x16x32_bf16 v[92:95], a[20:23], a[220:223], v[92:95]\n" \
"buffer_load_dwordx4 v192, s[44:47], s73 offen offset:2048 lds\n" \
"v_mfma_f32_16x16x32_bf16 v[88:91], a[16:19], a[208:211], v[88:91]\n" \
"ds_read_b64_tr_b16 a[46:47], v209 offset:15360\n" \
"v_mfma_f32_16x16x32_bf16 v[88:91], a[20:23], a[212:215], v[88:91]\n" \
"ds_read_b64_tr_b16 a[48:49], v210 offset:0\n" \
"v_mfma_f32_16x16x32_bf16 v[96:99], a[24:27], a[208:211], v[96:99]\n" \
"ds_read_b128 a[16:19], v205 offset:4096\n" \
"v_mfma_f32_16x16x32_bf16 v[96:99], a[28:31], a[212:215], v[96:99]\n" \
"buffer_load_dwordx4 v193, s[44:47], s73 offen offset:3072 lds\n" \
"v_mfma_f32_16x16x32_bf16 v[100:103], a[24:27], a[216:219], v[100:103]\n" \
"ds_read_b128 a[20:23], v206 offset:4096\n" \
"v_mfma_f32_16x16x32_bf16 v[100:103], a[28:31], a[220:223], v[100:103]\n" \
"s_add_u32 m0, s89, 0\n" \
"ds_read_b64_tr_b16 a[50:51], v210 offset:3072\n" \
"v_mfma_f32_16x16x32_bf16 v[60:63], a[200:203], a[24:27], v[60:63]\n" \
"s_xor_b32 s73, s73, 0x300\n" \
"ds_read_b64_tr_b16 a[52:53], v210 offset:12288\n" \
"v_mfma_f32_16x16x32_bf16 v[60:63], a[204:207], a[28:31], v[60:63]\n" \
"buffer_load_dwordx4 v196, s[44:47], s75 offen lds\n" \
"v_mfma_f32_16x16x32_bf16 v[68:71], a[200:203], a[192:195], v[68:71]\n" \
"s_xor_b32 s75, s75, 0x300\n" \
"s_mul_i32 s76, s80, 393216\n" \
"ds_read_b128 a[24:27], v205 offset:8192\n" \
"v_mfma_f32_16x16x32_bf16 v[68:71], a[204:207], a[196:199], v[68:71]\n" \
"s_add_u32 s76, s76, s78\n" \
"ds_read_b128 a[28:31], v206 offset:8192\n" \
"v_mfma_f32_16x16x32_bf16 v[108:111], a[192:195], a[216:219], v[108:111]\n" \
"s_add_u32 m0, s91, 20480\n" \
"ds_read_b64_tr_b16 a[54:55], v210 offset:15360\n" \
"v_mfma_f32_16x16x32_bf16 v[108:111], a[196:199], a[220:223], v[108:111]\n" \
"buffer_load_dwordx4 v197, s[48:51], s76 offen offset:0 lds\n" \
"v_mfma_f32_16x16x32_bf16 v[104:107], a[192:195], a[208:211], v[104:107]\n" \
"v_mfma_f32_16x16x32_bf16 v[104:107], a[196:199], a[212:215], v[104:107]\n" \
"s_waitcnt lgkmcnt(2)\n" \
"v_mfma_f32_16x16x32_bf16 v[32:35], a[32:35], a[0:3], v[32:35]\n" \
"s_add_u32 s77, s76, 24576\n" \
"ds_read_b128 a[192:195], v205 offset:12288\n" \
"v_mfma_f32_16x16x32_bf16 v[32:35], a[36:39], a[4:7], v[32:35]\n" \
"buffer_load_dwordx4 v198, s[48:51], s76 offen offset:1024 lds\n" \
"v_mfma_f32_16x16x32_bf16 v[40:43], a[32:35], a[8:11], v[40:43]\n" \
"ds_read_b128 a[196:199], v206 offset:12288\n" \
"v_mfma_f32_16x16x32_bf16 v[40:43], a[36:39], a[12:15], v[40:43]\n" \
"ds_read_b64_tr_b16 a[200:201], v210 offset:1024\n" \
"v_mfma_f32_16x16x32_bf16 v[0:3], a[40:43], a[8:11], v[0:3]\n" \
"s_waitcnt lgkmcnt(3)\n" \
"s_xor_b32 s80, s80, 0x6\n" \
"ds_read_b64_tr_b16 a[202:203], v210 offset:4096\n" \
"v_mfma_f32_16x16x32_bf16 v[0:3], a[44:47], a[12:15], v[0:3]\n" \
"buffer_load_dwordx4 v199, s[48:51], s76 offen offset:2048 lds\n" \
"v_mfma_f32_16x16x32_bf16 v[4:7], a[48:51], a[8:11], v[4:7]\n" \
"ds_read_b64_tr_b16 a[204:205], v210 offset:13312\n" \
"v_mfma_f32_16x16x32_bf16 v[4:7], a[52:55], a[12:15], v[4:7]\n" \
"ds_read_b64_tr_b16 a[206:207], v210 offset:16384\n" \
"v_mfma_f32_16x16x32_bf16 v[12:15], a[48:51], a[16:19], v[12:15]\n" \
"s_add_u32 m0, m0, 3072\n" \
"ds_read_b64_tr_b16 a[208:209], v209 offset:2048\n" \
"v_mfma_f32_16x16x32_bf16 v[12:15], a[52:55], a[20:23], v[12:15]\n" \
"buffer_load_dwordx4 v197, s[48:51], s77 offen offset:0 lds\n" \
"v_mfma_f32_16x16x32_bf16 v[8:11], a[40:43], a[16:19], v[8:11]\n" \
"ds_read_b64_tr_b16 a[210:211], v209 offset:5120\n" \
"v_mfma_f32_16x16x32_bf16 v[8:11], a[44:47], a[20:23], v[8:11]\n" \
"ds_read_b64_tr_b16 a[212:213], v209 offset:14336\n" \
"v_mfma_f32_16x16x32_bf16 v[48:51], a[32:35], a[16:19], v[48:51]\n" \
"ds_read_b64_tr_b16 a[214:215], v209 offset:17408\n" \
"v_mfma_f32_16x16x32_bf16 v[48:51], a[36:39], a[20:23], v[48:51]\n" \
"buffer_load_dwordx4 v198, s[48:51], s77 offen offset:1024 lds\n" \
"v_mfma_f32_16x16x32_bf16 v[56:59], a[32:35], a[24:27], v[56:59]\n" \
"ds_read_b64_tr_b16 a[216:217], v210 offset:2048\n" \
"v_mfma_f32_16x16x32_bf16 v[56:59], a[36:39], a[28:31], v[56:59]\n" \
"ds_read_b64_tr_b16 a[218:219], v210 offset:5120\n" \
"v_mfma_f32_16x16x32_bf16 v[16:19], a[40:43], a[24:27], v[16:19]\n" \
"ds_read_b64_tr_b16 a[220:221], v210 offset:14336\n" \
"v_mfma_f32_16x16x32_bf16 v[16:19], a[44:47], a[28:31], v[16:19]\n" \
"buffer_load_dwordx4 v199, s[48:51], s77 offen offset:2048 lds\n" \
"v_mfma_f32_16x16x32_bf16 v[20:23], a[48:51], a[24:27], v[20:23]\n" \
"ds_read_b64_tr_b16 a[222:223], v210 offset:17408\n" \
"v_mfma_f32_16x16x32_bf16 v[20:23], a[52:55], a[28:31], v[20:23]\n" \
"s_waitcnt lgkmcnt(3)\n" \
"v_mfma_f32_16x16x32_bf16 v[28:31], a[48:51], a[192:195], v[28:31]\n" \
"v_mfma_f32_16x16x32_bf16 v[28:31], a[52:55], a[196:199], v[28:31]\n" \
"v_mfma_f32_16x16x32_bf16 v[24:27], a[40:43], a[192:195], v[24:27]\n" \
"v_add_u32 v205, 65536, v200\n" \
"v_add_u32 v206, 65536, v201\n" \
"v_mfma_f32_16x16x32_bf16 v[24:27], a[44:47], a[196:199], v[24:27]\n" \
"v_add_u32 v207, 65536, v202\n" \
"v_add_u32 v208, 65536, v203\n" \
"v_mfma_f32_16x16x32_bf16 v[64:67], a[32:35], a[192:195], v[64:67]\n" \
"v_add_u32 v209, 86016, v204\n" \
"v_xor_b32 v210, 32, v209\n" \
"v_mfma_f32_16x16x32_bf16 v[64:67], a[36:39], a[196:199], v[64:67]\n" \
"s_waitcnt vmcnt(11) lgkmcnt(0)\n" \
"s_barrier\n" \
"v_mfma_f32_16x16x32_bf16 v[72:75], a[0:3], a[208:211], v[72:75]\n" \
"ds_read_b64_tr_b16 a[32:33], v209 offset:1024\n" \
"Lpg9002_%=:\n" \
"Lst1x2_%=:\n" \
"s_cbranch_execz Lpg11159_%=\n" \
"ds_read_b64_tr_b16 a[34:35], v209 offset:4096\n" \
"v_mfma_f32_16x16x32_bf16 v[72:75], a[4:7], a[212:215], v[72:75]\n" \
"ds_read_b64_tr_b16 a[36:37], v209 offset:13312\n" \
"ds_read_b64_tr_b16 a[38:39], v209 offset:16384\n" \
"v_mfma_f32_16x16x32_bf16 v[80:83], a[8:11], a[208:211], v[80:83]\n" \
"s_add_u32 m0, s79, 110592\n" \
"ds_read_b128 a[0:3], v207 offset:0\n" \
"v_cmp_eq_u32 vcc, s60, v226\n" \
"v_mfma_f32_16x16x32_bf16 v[80:83], a[12:15], a[212:215], v[80:83]\n" \
"buffer_load_dwordx4 v192, s[44:47], s73 offen offset:0 lds\n" \
"v_mfma_f32_16x16x32_bf16 v[84:87], a[8:11], a[216:219], v[84:87]\n" \
"s_andn2_b32 s70, 0xff, vcc_lo\n" \
"s_cbranch_scc1 Lpl4_%=\n" \
"Lpr4_%=:\n" \
"ds_read_b128 a[4:7], v208 offset:0\n" \
"v_mfma_f32_16x16x32_bf16 v[84:87], a[12:15], a[220:223], v[84:87]\n" \
"s_and_b32 s70, %1, 7\n" \
"ds_read_b64_tr_b16 a[40:41], v209 offset:0\n" \
"v_readlane_b32 s70, v227, s70\n" \
"v_mfma_f32_16x16x32_bf16 v[44:47], a[200:203], a[8:11], v[44:47]\n" \
"ds_read_b64_tr_b16 a[42:43], v209 offset:3072\n" \
"v_mfma_f32_16x16x32_bf16 v[44:47], a[204:207], a[12:15], v[44:47]\n" \
"buffer_load_dwordx4 v193, s[44:47], s73 offen offset:1024 lds\n" \
"v_mfma_f32_16x16x32_bf16 v[52:55], a[200:203], a[16:19], v[52:55]\n" \
"ds_read_b128 a[8:11], v205 offset:0\n" \
"v_subrev_u32 v227, s70, v227\n" \
"v_mfma_f32_16x16x32_bf16 v[52:55], a[204:207], a[20:23], v[52:55]\n" \
"ds_read_b128 a[12:15], v206 offset:0\n" \
"v_cmp_gt_i32 vcc, 0, v227\n" \
"v_mfma_f32_16x16x32_bf16 v[92:95], a[16:19], a[216:219], v[92:95]\n" \
"ds_read_b64_tr_b16 a[44:45], v209 offset:12288\n" \
"v_mfma_f32_16x16x32_bf16 v[92:95], a[20:23], a[220:223], v[92:95]\n" \
"buffer_load_dwordx4 v192, s[44:47], s73 offen offset:2048 lds\n" \
"v_mfma_f32_16x16x32_bf16 v[88:91], a[16:19], a[208:211], v[88:91]\n" \
"s_and_b32 s13, vcc_lo, 0xff\n" \
"s_bcnt1_i32_b32 s13, s13\n" \
"ds_read_b64_tr_b16 a[46:47], v209 offset:15360\n" \
"v_mfma_f32_16x16x32_bf16 v[88:91], a[20:23], a[212:215], v[88:91]\n" \
"s_lshr_b32 s90, s13, 2\n" \
"s_mul_i32 s70, s90, 7\n" \
"ds_read_b64_tr_b16 a[48:49], v210 offset:0\n" \
"v_mfma_f32_16x16x32_bf16 v[96:99], a[24:27], a[208:211], v[96:99]\n" \
"s_xor_b32 s13, s13, s70\n" \
"s_lshl_b32 s70, s13, 3\n" \
"ds_read_b128 a[16:19], v205 offset:4096\n" \
"v_mfma_f32_16x16x32_bf16 v[96:99], a[28:31], a[212:215], v[96:99]\n" \
"buffer_load_dwordx4 v193, s[44:47], s73 offen offset:3072 lds\n" \
"v_mfma_f32_16x16x32_bf16 v[100:103], a[24:27], a[216:219], v[100:103]\n" \
"s_lshr_b32 s9, 0x2a2b2d2f, s70\n" \
"s_and_b32 s9, s9, 0xff\n" \
"ds_read_b128 a[20:23], v206 offset:4096\n" \
"v_mfma_f32_16x16x32_bf16 v[100:103], a[28:31], a[220:223], v[100:103]\n" \
"s_add_u32 m0, s89, 110592\n" \
"s_lshr_b32 s8, %1, 6\n" \
"ds_read_b64_tr_b16 a[50:51], v210 offset:3072\n" \
"v_mfma_f32_16x16x32_bf16 v[60:63], a[200:203], a[24:27], v[60:63]\n" \
"s_xor_b32 s73, s73, 0x80\n" \
"s_lshl2_add_u32 s8, s13, s8\n" \
"ds_read_b64_tr_b16 a[52:53], v210 offset:12288\n" \
"v_mfma_f32_16x16x32_bf16 v[60:63], a[204:207], a[28:31], v[60:63]\n" \
"buffer_load_dwordx4 v196, s[44:47], s75 offen lds\n" \
"v_mfma_f32_16x16x32_bf16 v[68:71], a[200:203], a[192:195], v[68:71]\n" \
"s_xor_b32 s75, s75, 0x80\n" \
"s_mul_i32 s76, s80, 393216\n" \
"ds_read_b128 a[24:27], v205 offset:8192\n" \
"v_mfma_f32_16x16x32_bf16 v[68:71], a[204:207], a[196:199], v[68:71]\n" \
"s_add_u32 s76, s76, s78\n" \
"ds_read_b128 a[28:31], v206 offset:8192\n" \
"v_max_i32_dpp v226, v227, v227 quad_perm:[1,0,3,2] row_mask:0xf bank_mask:0xf\n" \
"v_mfma_f32_16x16x32_bf16 v[108:111], a[192:195], a[216:219], v[108:111]\n" \
"s_add_u32 m0, s91, 139264\n" \
"s_mov_b32 s14, 0\n" \
"ds_read_b64_tr_b16 a[54:55], v210 offset:15360\n" \
"v_mfma_f32_16x16x32_bf16 v[108:111], a[196:199], a[220:223], v[108:111]\n" \
"buffer_load_dwordx4 v197, s[48:51], s76 offen offset:0 lds\n" \
"v_mfma_f32_16x16x32_bf16 v[104:107], a[192:195], a[208:211], v[104:107]\n" \
"v_readlane_b32 s15, v226, 2\n" \
"v_readlane_b32 s16, v226, 4\n" \
"v_mfma_f32_16x16x32_bf16 v[104:107], a[196:199], a[212:215], v[104:107]\n" \
"s_waitcnt lgkmcnt(2)\n" \
"v_readlane_b32 s17, v226, 6\n" \
"v_cmp_gt_i32 vcc, s15, v226\n" \
"v_mfma_f32_16x16x32_bf16 v[32:35], a[32:35], a[0:3], v[32:35]\n" \
"s_add_u32 s77, s76, 24576\n" \
"ds_read_b128 a[192:195], v205 offset:12288\n" \
"v_mfma_f32_16x16x32_bf16 v[32:35], a[36:39], a[4:7], v[32:35]\n" \
"buffer_load_dwordx4 v198, s[48:51], s76 offen offset:1024 lds\n" \
"v_mfma_f32_16x16x32_bf16 v[40:43], a[32:35], a[8:11], v[40:43]\n" \
"s_and_b32 s70, vcc_lo, 0x55\n" \
"s_bcnt1_i32_b32 s70, s70\n" \
"ds_read_b128 a[196:199], v206 offset:12288\n" \
"v_mfma_f32_16x16x32_bf16 v[40:43], a[36:39], a[12:15], v[40:43]\n" \
"s_cmp_eq_u32 s70, s13\n" \
"s_cselect_b32 s14, 4, s14\n" \
"ds_read_b64_tr_b16 a[200:201], v210 offset:1024\n" \
"v_mfma_f32_16x16x32_bf16 v[0:3], a[40:43], a[8:11], v[0:3]\n" \
"s_xor_b32 s80, s80, 0x1\n" \
"ds_read_b64_tr_b16 a[202:203], v210 offset:4096\n" \
"v_cmp_gt_i32 vcc, s16, v226\n" \
"v_mfma_f32_16x16x32_bf16 v[0:3], a[44:47], a[12:15], v[0:3]\n" \
"s_waitcnt lgkmcnt(3)\n" \
"buffer_load_dwordx4 v199, s[48:51], s76 offen offset:2048 lds\n" \
"v_mfma_f32_16x16x32_bf16 v[4:7], a[48:51], a[8:11], v[4:7]\n" \
"s_and_b32 s70, vcc_lo, 0x55\n" \
"s_bcnt1_i32_b32 s70, s70\n" \
"ds_read_b64_tr_b16 a[204:205], v210 offset:13312\n" \
"v_mfma_f32_16x16x32_bf16 v[4:7], a[52:55], a[12:15], v[4:7]\n" \
"s_cmp_eq_u32 s70, s13\n" \
"s_cselect_b32 s14, 8, s14\n" \
"ds_read_b64_tr_b16 a[206:207], v210 offset:16384\n" \
"v_mfma_f32_16x16x32_bf16 v[12:15], a[48:51], a[16:19], v[12:15]\n" \
"s_add_u32 m0, m0, 3072\n" \
"ds_read_b64_tr_b16 a[208:209], v209 offset:2048\n" \
"v_cmp_gt_i32 vcc, s17, v226\n" \
"v_mfma_f32_16x16x32_bf16 v[12:15], a[52:55], a[20:23], v[12:15]\n" \
"buffer_load_dwordx4 v197, s[48:51], s77 offen offset:0 lds\n" \
"v_mfma_f32_16x16x32_bf16 v[8:11], a[40:43], a[16:19], v[8:11]\n" \
"s_and_b32 s70, vcc_lo, 0x55\n" \
"s_bcnt1_i32_b32 s70, s70\n" \
"ds_read_b64_tr_b16 a[210:211], v209 offset:5120\n" \
"v_mfma_f32_16x16x32_bf16 v[8:11], a[44:47], a[20:23], v[8:11]\n" \
"s_cmp_eq_u32 s70, s13\n" \
"s_cselect_b32 s14, 12, s14\n" \
"ds_read_b64_tr_b16 a[212:213], v209 offset:14336\n" \
"v_mfma_f32_16x16x32_bf16 v[48:51], a[32:35], a[16:19], v[48:51]\n" \
"s_lshr_b32 s70, %1, 6\n" \
"s_add_u32 s14, s14, s70\n" \
"ds_read_b64_tr_b16 a[214:215], v209 offset:17408\n" \
"v_mfma_f32_16x16x32_bf16 v[48:51], a[36:39], a[20:23], v[48:51]\n" \
"buffer_load_dwordx4 v198, s[48:51], s77 offen offset:1024 lds\n" \
"v_mfma_f32_16x16x32_bf16 v[56:59], a[32:35], a[24:27], v[56:59]\n" \
"s_cmp_eq_u32 s90, 0\n" \
"s_cselect_b32 s14, s8, s14\n" \
"ds_read_b64_tr_b16 a[216:217], v210 offset:2048\n" \
"v_mfma_f32_16x16x32_bf16 v[56:59], a[36:39], a[28:31], v[56:59]\n" \
"s_sub_u32 s10, 49, s9\n" \
"s_lshl_b32 s11, s9, 1\n" \
"ds_read_b64_tr_b16 a[218:219], v210 offset:5120\n" \
"v_mfma_f32_16x16x32_bf16 v[16:19], a[40:43], a[24:27], v[16:19]\n" \
"s_sub_u32 s11, s11, 71\n" \
"s_lshl2_add_u32 s70, s8, s61\n" \
"ds_read_b64_tr_b16 a[220:221], v210 offset:14336\n" \
"v_mfma_f32_16x16x32_bf16 v[16:19], a[44:47], a[28:31], v[16:19]\n" \
"buffer_load_dwordx4 v199, s[48:51], s77 offen offset:2048 lds\n" \
"v_mfma_f32_16x16x32_bf16 v[20:23], a[48:51], a[24:27], v[20:23]\n" \
"s_mul_i32 s81, s70, 266240\n" \
"s_mul_i32 s82, s90, s9\n" \
"ds_read_b64_tr_b16 a[222:223], v210 offset:17408\n" \
"v_mfma_f32_16x16x32_bf16 v[20:23], a[52:55], a[28:31], v[20:23]\n" \
"s_waitcnt lgkmcnt(3)\n" \
"s_lshl_b32 s71, s82, 12\n" \
"s_add_u32 s81, s81, s71\n" \
"s_add_u32 s81, s81, 35700736\n" \
"v_mfma_f32_16x16x32_bf16 v[28:31], a[48:51], a[192:195], v[28:31]\n" \
"s_lshl2_add_u32 s70, s14, s61\n" \
"s_mul_i32 s83, s70, 69632\n" \
"s_add_u32 s83, s83, 52740096\n" \
"v_mfma_f32_16x16x32_bf16 v[28:31], a[52:55], a[196:199], v[28:31]\n" \
"s_lshl_b32 s82, s82, 17\n" \
"s_add_u32 s82, s82, s84\n" \
"s_lshl_b32 s70, s14, 19\n" \
"v_mfma_f32_16x16x32_bf16 v[24:27], a[40:43], a[192:195], v[24:27]\n" \
"s_add_u32 s88, s88, s70\n" \
"v_add_u32 v205, 0, v200\n" \
"v_add_u32 v206, 0, v201\n" \
"v_mfma_f32_16x16x32_bf16 v[24:27], a[44:47], a[196:199], v[24:27]\n" \
"s_lshl_b32 s71, s90, 11\n" \
"v_add_u32 v207, 0, v202\n" \
"v_add_u32 v208, 0, v203\n" \
"v_mfma_f32_16x16x32_bf16 v[64:67], a[32:35], a[192:195], v[64:67]\n" \
"s_add_u32 s88, s88, s71\n" \
"v_add_u32 v209, 20480, v204\n" \
"v_xor_b32 v210, 32, v209\n" \
"v_mfma_f32_16x16x32_bf16 v[64:67], a[36:39], a[196:199], v[64:67]\n" \
"s_lshl3_add_u32 s70, s8, s68\n" \
"s_waitcnt vmcnt(11) lgkmcnt(0)\n" \
"s_barrier\n" \
"v_mfma_f32_16x16x32_bf16 v[72:75], a[0:3], a[208:211], v[72:75]\n" \
"s_lshl2_add_u32 s87, s70, s61\n" \
"ds_read_b64_tr_b16 a[32:33], v209 offset:1024\n" \
"ds_read_b64_tr_b16 a[34:35], v209 offset:4096\n" \
"v_mfma_f32_16x16x32_bf16 v[72:75], a[4:7], a[212:215], v[72:75]\n" \
"s_lshl_b32 s87, s87, 14\n" \
"ds_read_b64_tr_b16 a[36:37], v209 offset:13312\n" \
"ds_read_b64_tr_b16 a[38:39], v209 offset:16384\n" \
"v_mfma_f32_16x16x32_bf16 v[80:83], a[8:11], a[208:211], v[80:83]\n" \
"s_add_u32 m0, s79, 65536\n" \
"s_add_u32 s87, s87, 57196544\n" \
"ds_read_b128 a[0:3], v207 offset:0\n" \
"v_mfma_f32_16x16x32_bf16 v[80:83], a[12:15], a[212:215], v[80:83]\n" \
"buffer_load_dwordx4 v192, s[44:47], s73 offen offset:0 lds\n" \
"v_mfma_f32_16x16x32_bf16 v[84:87], a[8:11], a[216:219], v[84:87]\n" \
"s_lshl_b32 s64, s70, 7\n" \
"s_add_u32 s64, s64, 65601536\n" \
"ds_read_b128 a[4:7], v208 offset:0\n" \
"v_mfma_f32_16x16x32_bf16 v[84:87], a[12:15], a[220:223], v[84:87]\n" \
"s_lshl_b32 s66, s8, 7\n" \
"s_lshl_b32 s67, s14, 7\n" \
"ds_read_b64_tr_b16 a[40:41], v209 offset:0\n" \
"v_mfma_f32_16x16x32_bf16 v[44:47], a[200:203], a[8:11], v[44:47]\n" \
"s_add_u32 s67, s67, 65591296\n" \
"s_add_u32 s66, s66, 65589248\n" \
"ds_read_b64_tr_b16 a[42:43], v209 offset:3072\n" \
"v_mfma_f32_16x16x32_bf16 v[44:47], a[204:207], a[12:15], v[44:47]\n" \
"buffer_load_dwordx4 v193, s[44:47], s73 offen offset:1024 lds\n" \
"v_mfma_f32_16x16x32_bf16 v[52:55], a[200:203], a[16:19], v[52:55]\n" \
"s_add_u32 s70, s9, 1\n" \
"s_sub_u32 s70, s70, s90\n" \
"ds_read_b128 a[8:11], v205 offset:0\n" \
"v_mfma_f32_16x16x32_bf16 v[52:55], a[204:207], a[20:23], v[52:55]\n" \
"s_lshr_b32 s70, s70, 1\n" \
"s_bfm_b32 s65, s70, 0\n" \
"ds_read_b128 a[12:15], v206 offset:0\n" \
"v_mfma_f32_16x16x32_bf16 v[92:95], a[16:19], a[216:219], v[92:95]\n" \
"s_sub_u32 s70, 0, s90\n" \
"s_xor_b32 s65, s65, s70\n" \
"ds_read_b64_tr_b16 a[44:45], v209 offset:12288\n" \
"v_mfma_f32_16x16x32_bf16 v[92:95], a[20:23], a[220:223], v[92:95]\n" \
"buffer_load_dwordx4 v192, s[44:47], s73 offen offset:2048 lds\n" \
"v_mfma_f32_16x16x32_bf16 v[88:91], a[16:19], a[208:211], v[88:91]\n" \
"s_and_b32 s74, s70, 0xffff\n" \
"s_xor_b32 s68, s68, s8\n" \
"ds_read_b64_tr_b16 a[46:47], v209 offset:15360\n" \
"v_mfma_f32_16x16x32_bf16 v[88:91], a[20:23], a[212:215], v[88:91]\n" \
"s_and_b32 s68, s68, 3\n" \
"s_lshl_b32 s68, s68, 12\n" \
"ds_read_b64_tr_b16 a[48:49], v210 offset:0\n" \
"v_mfma_f32_16x16x32_bf16 v[96:99], a[24:27], a[208:211], v[96:99]\n" \
"ds_read_b128 a[16:19], v205 offset:4096\n" \
"v_mfma_f32_16x16x32_bf16 v[96:99], a[28:31], a[212:215], v[96:99]\n" \
"buffer_load_dwordx4 v193, s[44:47], s73 offen offset:3072 lds\n" \
"v_mfma_f32_16x16x32_bf16 v[100:103], a[24:27], a[216:219], v[100:103]\n" \
"ds_read_b128 a[20:23], v206 offset:4096\n" \
"v_mfma_f32_16x16x32_bf16 v[100:103], a[28:31], a[220:223], v[100:103]\n" \
"s_add_u32 m0, s89, 65536\n" \
"ds_read_b64_tr_b16 a[50:51], v210 offset:3072\n" \
"v_mfma_f32_16x16x32_bf16 v[60:63], a[200:203], a[24:27], v[60:63]\n" \
"s_xor_b32 s73, s73, 0x180\n" \
"ds_read_b64_tr_b16 a[52:53], v210 offset:12288\n" \
"v_mfma_f32_16x16x32_bf16 v[60:63], a[204:207], a[28:31], v[60:63]\n" \
"buffer_load_dwordx4 v196, s[44:47], s75 offen lds\n" \
"v_mfma_f32_16x16x32_bf16 v[68:71], a[200:203], a[192:195], v[68:71]\n" \
"s_xor_b32 s75, s75, 0x180\n" \
"s_mul_i32 s76, s80, 393216\n" \
"ds_read_b128 a[24:27], v205 offset:8192\n" \
"v_mfma_f32_16x16x32_bf16 v[68:71], a[204:207], a[196:199], v[68:71]\n" \
"s_add_u32 s76, s76, s78\n" \
"ds_read_b128 a[28:31], v206 offset:8192\n" \
"v_mfma_f32_16x16x32_bf16 v[108:111], a[192:195], a[216:219], v[108:111]\n" \
"s_add_u32 m0, s91, 86016\n" \
"ds_read_b64_tr_b16 a[54:55], v210 offset:15360\n" \
"v_mfma_f32_16x16x32_bf16 v[108:111], a[196:199], a[220:223], v[108:111]\n" \
"buffer_load_dwordx4 v197, s[48:51], s76 offen offset:0 lds\n" \
"v_mfma_f32_16x16x32_bf16 v[104:107], a[192:195], a[208:211], v[104:107]\n" \
"v_mfma_f32_16x16x32_bf16 v[104:107], a[196:199], a[212:215], v[104:107]\n" \
"s_waitcnt lgkmcnt(2)\n" \
"v_mfma_f32_16x16x32_bf16 v[32:35], a[32:35], a[0:3], v[32:35]\n" \
"s_add_u32 s77, s76, 24576\n" \
"ds_read_b128 a[192:195], v205 offset:12288\n" \
"v_mfma_f32_16x16x32_bf16 v[32:35], a[36:39], a[4:7], v[32:35]\n" \
"buffer_load_dwordx4 v198, s[48:51], s76 offen offset:1024 lds\n" \
"v_mfma_f32_16x16x32_bf16 v[40:43], a[32:35], a[8:11], v[40:43]\n" \
"ds_read_b128 a[196:199], v206 offset:12288\n" \
"v_mfma_f32_16x16x32_bf16 v[40:43], a[36:39], a[12:15], v[40:43]\n" \
"ds_read_b64_tr_b16 a[200:201], v210 offset:1024\n" \
"v_mfma_f32_16x16x32_bf16 v[0:3], a[40:43], a[8:11], v[0:3]\n" \
"s_waitcnt lgkmcnt(3)\n" \
"s_xor_b32 s80, s80, 0x3\n" \
"ds_read_b64_tr_b16 a[202:203], v210 offset:4096\n" \
"v_mfma_f32_16x16x32_bf16 v[0:3], a[44:47], a[12:15], v[0:3]\n" \
"buffer_load_dwordx4 v199, s[48:51], s76 offen offset:2048 lds\n" \
"v_mfma_f32_16x16x32_bf16 v[4:7], a[48:51], a[8:11], v[4:7]\n" \
"ds_read_b64_tr_b16 a[204:205], v210 offset:13312\n" \
"v_mfma_f32_16x16x32_bf16 v[4:7], a[52:55], a[12:15], v[4:7]\n" \
"ds_read_b64_tr_b16 a[206:207], v210 offset:16384\n" \
"v_mfma_f32_16x16x32_bf16 v[12:15], a[48:51], a[16:19], v[12:15]\n" \
"s_add_u32 m0, m0, 3072\n" \
"ds_read_b64_tr_b16 a[208:209], v209 offset:2048\n" \
"v_mfma_f32_16x16x32_bf16 v[12:15], a[52:55], a[20:23], v[12:15]\n" \
"buffer_load_dwordx4 v197, s[48:51], s77 offen offset:0 lds\n" \
"v_mfma_f32_16x16x32_bf16 v[8:11], a[40:43], a[16:19], v[8:11]\n" \
"ds_read_b64_tr_b16 a[210:211], v209 offset:5120\n" \
"v_mfma_f32_16x16x32_bf16 v[8:11], a[44:47], a[20:23], v[8:11]\n" \
"ds_read_b64_tr_b16 a[212:213], v209 offset:14336\n" \
"v_mfma_f32_16x16x32_bf16 v[48:51], a[32:35], a[16:19], v[48:51]\n" \
"ds_read_b64_tr_b16 a[214:215], v209 offset:17408\n" \
"v_mfma_f32_16x16x32_bf16 v[48:51], a[36:39], a[20:23], v[48:51]\n" \
"buffer_load_dwordx4 v198, s[48:51], s77 offen offset:1024 lds\n" \
"v_mfma_f32_16x16x32_bf16 v[56:59], a[32:35], a[24:27], v[56:59]\n" \
"ds_read_b64_tr_b16 a[216:217], v210 offset:2048\n" \
"v_mfma_f32_16x16x32_bf16 v[56:59], a[36:39], a[28:31], v[56:59]\n" \
"ds_read_b64_tr_b16 a[218:219], v210 offset:5120\n" \
"v_mfma_f32_16x16x32_bf16 v[16:19], a[40:43], a[24:27], v[16:19]\n" \
"ds_read_b64_tr_b16 a[220:221], v210 offset:14336\n" \
"v_mfma_f32_16x16x32_bf16 v[16:19], a[44:47], a[28:31], v[16:19]\n" \
"buffer_load_dwordx4 v199, s[48:51], s77 offen offset:2048 lds\n" \
"v_mfma_f32_16x16x32_bf16 v[20:23], a[48:51], a[24:27], v[20:23]\n" \
"ds_read_b64_tr_b16 a[222:223], v210 offset:17408\n" \
"v_mfma_f32_16x16x32_bf16 v[20:23], a[52:55], a[28:31], v[20:23]\n" \
"s_waitcnt lgkmcnt(3)\n" \
"v_mfma_f32_16x16x32_bf16 v[28:31], a[48:51], a[192:195], v[28:31]\n" \
"v_mfma_f32_16x16x32_bf16 v[28:31], a[52:55], a[196:199], v[28:31]\n" \
"v_mfma_f32_16x16x32_bf16 v[24:27], a[40:43], a[192:195], v[24:27]\n" \
"v_add_u32 v205, 110592, v200\n" \
"v_add_u32 v206, 110592, v201\n" \
"v_mfma_f32_16x16x32_bf16 v[24:27], a[44:47], a[196:199], v[24:27]\n" \
"v_add_u32 v207, 110592, v202\n" \
"v_add_u32 v208, 110592, v203\n" \
"v_mfma_f32_16x16x32_bf16 v[64:67], a[32:35], a[192:195], v[64:67]\n" \
"v_add_u32 v209, 139264, v204\n" \
"v_xor_b32 v210, 32, v209\n" \
"v_mfma_f32_16x16x32_bf16 v[64:67], a[36:39], a[196:199], v[64:67]\n" \
"s_waitcnt vmcnt(11) lgkmcnt(0)\n" \
"s_barrier\n" \
"v_mfma_f32_16x16x32_bf16 v[72:75], a[0:3], a[208:211], v[72:75]\n" \
"ds_read_b64_tr_b16 a[32:33], v209 offset:1024\n" \
"ds_read_b64_tr_b16 a[34:35], v209 offset:4096\n" \
"v_mfma_f32_16x16x32_bf16 v[72:75], a[4:7], a[212:215], v[72:75]\n" \
"ds_read_b64_tr_b16 a[36:37], v209 offset:13312\n" \
"ds_read_b64_tr_b16 a[38:39], v209 offset:16384\n" \
"v_mfma_f32_16x16x32_bf16 v[80:83], a[8:11], a[208:211], v[80:83]\n" \
"s_add_u32 m0, s79, 0\n" \
"ds_read_b128 a[0:3], v207 offset:0\n" \
"v_mfma_f32_16x16x32_bf16 v[80:83], a[12:15], a[212:215], v[80:83]\n" \
"buffer_load_dwordx4 v192, s[44:47], s73 offen offset:0 lds\n" \
"v_mfma_f32_16x16x32_bf16 v[84:87], a[8:11], a[216:219], v[84:87]\n" \
"ds_read_b128 a[4:7], v208 offset:0\n" \
"v_mfma_f32_16x16x32_bf16 v[84:87], a[12:15], a[220:223], v[84:87]\n" \
"ds_read_b64_tr_b16 a[40:41], v209 offset:0\n" \
"v_mfma_f32_16x16x32_bf16 v[44:47], a[200:203], a[8:11], v[44:47]\n" \
"ds_read_b64_tr_b16 a[42:43], v209 offset:3072\n" \
"v_mfma_f32_16x16x32_bf16 v[44:47], a[204:207], a[12:15], v[44:47]\n" \
"buffer_load_dwordx4 v193, s[44:47], s73 offen offset:1024 lds\n" \
"v_mfma_f32_16x16x32_bf16 v[52:55], a[200:203], a[16:19], v[52:55]\n" \
"ds_read_b128 a[8:11], v205 offset:0\n" \
"v_mfma_f32_16x16x32_bf16 v[52:55], a[204:207], a[20:23], v[52:55]\n" \
"ds_read_b128 a[12:15], v206 offset:0\n" \
"v_mfma_f32_16x16x32_bf16 v[92:95], a[16:19], a[216:219], v[92:95]\n" \
"ds_read_b64_tr_b16 a[44:45], v209 offset:12288\n" \
"v_mfma_f32_16x16x32_bf16 v[92:95], a[20:23], a[220:223], v[92:95]\n" \
"buffer_load_dwordx4 v192, s[44:47], s73 offen offset:2048 lds\n" \
"v_mfma_f32_16x16x32_bf16 v[88:91], a[16:19], a[208:211], v[88:91]\n" \
"ds_read_b64_tr_b16 a[46:47], v209 offset:15360\n" \
"v_mfma_f32_16x16x32_bf16 v[88:91], a[20:23], a[212:215], v[88:91]\n" \
"ds_read_b64_tr_b16 a[48:49], v210 offset:0\n" \
"v_mfma_f32_16x16x32_bf16 v[96:99], a[24:27], a[208:211], v[96:99]\n" \
"ds_read_b128 a[16:19], v205 offset:4096\n" \
"v_mfma_f32_16x16x32_bf16 v[96:99], a[28:31], a[212:215], v[96:99]\n" \
"buffer_load_dwordx4 v193, s[44:47], s73 offen offset:3072 lds\n" \
"v_mfma_f32_16x16x32_bf16 v[100:103], a[24:27], a[216:219], v[100:103]\n" \
"ds_read_b128 a[20:23], v206 offset:4096\n" \
"v_mfma_f32_16x16x32_bf16 v[100:103], a[28:31], a[220:223], v[100:103]\n" \
"s_add_u32 m0, s89, 0\n" \
"ds_read_b64_tr_b16 a[50:51], v210 offset:3072\n" \
"v_mfma_f32_16x16x32_bf16 v[60:63], a[200:203], a[24:27], v[60:63]\n" \
"s_xor_b32 s73, s73, 0x80\n" \
"ds_read_b64_tr_b16 a[52:53], v210 offset:12288\n" \
"v_mfma_f32_16x16x32_bf16 v[60:63], a[204:207], a[28:31], v[60:63]\n" \
"buffer_load_dwordx4 v196, s[44:47], s75 offen lds\n" \
"v_mfma_f32_16x16x32_bf16 v[68:71], a[200:203], a[192:195], v[68:71]\n" \
"s_xor_b32 s75, s75, 0x80\n" \
"s_mul_i32 s76, s80, 393216\n" \
"ds_read_b128 a[24:27], v205 offset:8192\n" \
"v_mfma_f32_16x16x32_bf16 v[68:71], a[204:207], a[196:199], v[68:71]\n" \
"s_add_u32 s76, s76, s78\n" \
"ds_read_b128 a[28:31], v206 offset:8192\n" \
"v_mfma_f32_16x16x32_bf16 v[108:111], a[192:195], a[216:219], v[108:111]\n" \
"s_add_u32 m0, s91, 20480\n" \
"ds_read_b64_tr_b16 a[54:55], v210 offset:15360\n" \
"v_mfma_f32_16x16x32_bf16 v[108:111], a[196:199], a[220:223], v[108:111]\n" \
"buffer_load_dwordx4 v197, s[48:51], s76 offen offset:0 lds\n" \
"v_mfma_f32_16x16x32_bf16 v[104:107], a[192:195], a[208:211], v[104:107]\n" \
"v_mfma_f32_16x16x32_bf16 v[104:107], a[196:199], a[212:215], v[104:107]\n" \
"s_waitcnt lgkmcnt(2)\n" \
"v_mfma_f32_16x16x32_bf16 v[32:35], a[32:35], a[0:3], v[32:35]\n" \
"s_add_u32 s77, s76, 24576\n" \
"ds_read_b128 a[192:195], v205 offset:12288\n" \
"v_mfma_f32_16x16x32_bf16 v[32:35], a[36:39], a[4:7], v[32:35]\n" \
"buffer_load_dwordx4 v198, s[48:51], s76 offen offset:1024 lds\n" \
"v_mfma_f32_16x16x32_bf16 v[40:43], a[32:35], a[8:11], v[40:43]\n" \
"ds_read_b128 a[196:199], v206 offset:12288\n" \
"v_mfma_f32_16x16x32_bf16 v[40:43], a[36:39], a[12:15], v[40:43]\n" \
"ds_read_b64_tr_b16 a[200:201], v210 offset:1024\n" \
"v_mfma_f32_16x16x32_bf16 v[0:3], a[40:43], a[8:11], v[0:3]\n" \
"s_waitcnt lgkmcnt(3)\n" \
"s_xor_b32 s80, s80, 0x1\n" \
"ds_read_b64_tr_b16 a[202:203], v210 offset:4096\n" \
"v_mfma_f32_16x16x32_bf16 v[0:3], a[44:47], a[12:15], v[0:3]\n" \
"buffer_load_dwordx4 v199, s[48:51], s76 offen offset:2048 lds\n" \
"v_mfma_f32_16x16x32_bf16 v[4:7], a[48:51], a[8:11], v[4:7]\n" \
"ds_read_b64_tr_b16 a[204:205], v210 offset:13312\n" \
"v_mfma_f32_16x16x32_bf16 v[4:7], a[52:55], a[12:15], v[4:7]\n" \
"ds_read_b64_tr_b16 a[206:207], v210 offset:16384\n" \
"v_mfma_f32_16x16x32_bf16 v[12:15], a[48:51], a[16:19], v[12:15]\n" \
"s_add_u32 m0, m0, 3072\n" \
"ds_read_b64_tr_b16 a[208:209], v209 offset:2048\n" \
"v_mfma_f32_16x16x32_bf16 v[12:15], a[52:55], a[20:23], v[12:15]\n" \
"buffer_load_dwordx4 v197, s[48:51], s77 offen offset:0 lds\n" \
"v_mfma_f32_16x16x32_bf16 v[8:11], a[40:43], a[16:19], v[8:11]\n" \
"ds_read_b64_tr_b16 a[210:211], v209 offset:5120\n" \
"v_mfma_f32_16x16x32_bf16 v[8:11], a[44:47], a[20:23], v[8:11]\n" \
"ds_read_b64_tr_b16 a[212:213], v209 offset:14336\n" \
"v_mfma_f32_16x16x32_bf16 v[48:51], a[32:35], a[16:19], v[48:51]\n" \
"ds_read_b64_tr_b16 a[214:215], v209 offset:17408\n" \
"v_mfma_f32_16x16x32_bf16 v[48:51], a[36:39], a[20:23], v[48:51]\n" \
"buffer_load_dwordx4 v198, s[48:51], s77 offen offset:1024 lds\n" \
"v_mfma_f32_16x16x32_bf16 v[56:59], a[32:35], a[24:27], v[56:59]\n" \
"ds_read_b64_tr_b16 a[216:217], v210 offset:2048\n" \
"v_mfma_f32_16x16x32_bf16 v[56:59], a[36:39], a[28:31], v[56:59]\n" \
"ds_read_b64_tr_b16 a[218:219], v210 offset:5120\n" \
"v_mfma_f32_16x16x32_bf16 v[16:19], a[40:43], a[24:27], v[16:19]\n" \
"ds_read_b64_tr_b16 a[220:221], v210 offset:14336\n" \
"v_mfma_f32_16x16x32_bf16 v[16:19], a[44:47], a[28:31], v[16:19]\n" \
"buffer_load_dwordx4 v199, s[48:51], s77 offen offset:2048 lds\n" \
"v_mfma_f32_16x16x32_bf16 v[20:23], a[48:51], a[24:27], v[20:23]\n" \
"ds_read_b64_tr_b16 a[222:223], v210 offset:17408\n" \
"v_mfma_f32_16x16x32_bf16 v[20:23], a[52:55], a[28:31], v[20:23]\n" \
"s_waitcnt lgkmcnt(3)\n" \
"v_mfma_f32_16x16x32_bf16 v[28:31], a[48:51], a[192:195], v[28:31]\n" \
"v_mfma_f32_16x16x32_bf16 v[28:31], a[52:55], a[196:199], v[28:31]\n" \
"buffer_load_dword v220, v221, s[56:59], s66 offen sc1\n" \
"v_mfma_f32_16x16x32_bf16 v[24:27], a[40:43], a[192:195], v[24:27]\n" \
"v_add_u32 v205, 65536, v200\n" \
"v_add_u32 v206, 65536, v201\n" \
"v_mfma_f32_16x16x32_bf16 v[24:27], a[44:47], a[196:199], v[24:27]\n" \
"v_add_u32 v207, 65536, v202\n" \
"v_add_u32 v208, 65536, v203\n" \
"v_mfma_f32_16x16x32_bf16 v[64:67], a[32:35], a[192:195], v[64:67]\n" \
"v_add_u32 v209, 86016, v204\n" \
"v_xor_b32 v210, 32, v209\n" \
"v_mfma_f32_16x16x32_bf16 v[64:67], a[36:39], a[196:199], v[64:67]\n" \
"s_waitcnt vmcnt(12) lgkmcnt(0)\n" \
"s_barrier\n" \
"v_mfma_f32_16x16x32_bf16 v[72:75], a[0:3], a[208:211], v[72:75]\n" \
"ds_read_b64_tr_b16 a[32:33], v209 offset:1024\n" \
"ds_read_b64_tr_b16 a[34:35], v209 offset:4096\n" \
"v_mfma_f32_16x16x32_bf16 v[72:75], a[4:7], a[212:215], v[72:75]\n" \
"ds_read_b64_tr_b16 a[36:37], v209 offset:13312\n" \
"ds_read_b64_tr_b16 a[38:39], v209 offset:16384\n" \
"v_mfma_f32_16x16x32_bf16 v[80:83], a[8:11], a[208:211], v[80:83]\n" \
"s_add_u32 m0, s79, 110592\n" \
"ds_read_b128 a[0:3], v207 offset:0\n" \
"v_mfma_f32_16x16x32_bf16 v[80:83], a[12:15], a[212:215], v[80:83]\n" \
"buffer_load_dwordx4 v192, s[44:47], s73 offen offset:0 lds\n" \
"v_mfma_f32_16x16x32_bf16 v[84:87], a[8:11], a[216:219], v[84:87]\n" \
"ds_read_b128 a[4:7], v208 offset:0\n" \
"v_mfma_f32_16x16x32_bf16 v[84:87], a[12:15], a[220:223], v[84:87]\n" \
"ds_read_b64_tr_b16 a[40:41], v209 offset:0\n" \
"v_mfma_f32_16x16x32_bf16 v[44:47], a[200:203], a[8:11], v[44:47]\n" \
"ds_read_b64_tr_b16 a[42:43], v209 offset:3072\n" \
"v_mfma_f32_16x16x32_bf16 v[44:47], a[204:207], a[12:15], v[44:47]\n" \
"buffer_load_dwordx4 v193, s[44:47], s73 offen offset:1024 lds\n" \
"v_mfma_f32_16x16x32_bf16 v[52:55], a[200:203], a[16:19], v[52:55]\n" \
"ds_read_b128 a[8:11], v205 offset:0\n" \
"v_mfma_f32_16x16x32_bf16 v[52:55], a[204:207], a[20:23], v[52:55]\n" \
"ds_read_b128 a[12:15], v206 offset:0\n" \
"v_mfma_f32_16x16x32_bf16 v[92:95], a[16:19], a[216:219], v[92:95]\n" \
"ds_read_b64_tr_b16 a[44:45], v209 offset:12288\n" \
"v_mfma_f32_16x16x32_bf16 v[92:95], a[20:23], a[220:223], v[92:95]\n" \
"buffer_load_dwordx4 v192, s[44:47], s73 offen offset:2048 lds\n" \
"v_mfma_f32_16x16x32_bf16 v[88:91], a[16:19], a[208:211], v[88:91]\n" \
"ds_read_b64_tr_b16 a[46:47], v209 offset:15360\n" \
"v_mfma_f32_16x16x32_bf16 v[88:91], a[20:23], a[212:215], v[88:91]\n" \
"ds_read_b64_tr_b16 a[48:49], v210 offset:0\n" \
"v_mfma_f32_16x16x32_bf16 v[96:99], a[24:27], a[208:211], v[96:99]\n" \
"ds_read_b128 a[16:19], v205 offset:4096\n" \
"v_mfma_f32_16x16x32_bf16 v[96:99], a[28:31], a[212:215], v[96:99]\n" \
"buffer_load_dwordx4 v193, s[44:47], s73 offen offset:3072 lds\n" \
"v_mfma_f32_16x16x32_bf16 v[100:103], a[24:27], a[216:219], v[100:103]\n" \
"ds_read_b128 a[20:23], v206 offset:4096\n" \
"v_mfma_f32_16x16x32_bf16 v[100:103], a[28:31], a[220:223], v[100:103]\n" \
"s_add_u32 m0, s89, 110592\n" \
"ds_read_b64_tr_b16 a[50:51], v210 offset:3072\n" \
"v_mfma_f32_16x16x32_bf16 v[60:63], a[200:203], a[24:27], v[60:63]\n" \
"s_xor_b32 s73, s73, 0x80\n" \
"ds_read_b64_tr_b16 a[52:53], v210 offset:12288\n" \
"v_mfma_f32_16x16x32_bf16 v[60:63], a[204:207], a[28:31], v[60:63]\n" \
"buffer_load_dwordx4 v196, s[44:47], s75 offen lds\n" \
"v_mfma_f32_16x16x32_bf16 v[68:71], a[200:203], a[192:195], v[68:71]\n" \
"s_xor_b32 s75, s75, 0x80\n" \
"s_mul_i32 s76, s80, 393216\n" \
"ds_read_b128 a[24:27], v205 offset:8192\n" \
"v_mfma_f32_16x16x32_bf16 v[68:71], a[204:207], a[196:199], v[68:71]\n" \
"s_add_u32 s76, s76, s78\n" \
"ds_read_b128 a[28:31], v206 offset:8192\n" \
"v_mfma_f32_16x16x32_bf16 v[108:111], a[192:195], a[216:219], v[108:111]\n" \
"s_add_u32 m0, s91, 139264\n" \
"ds_read_b64_tr_b16 a[54:55], v210 offset:15360\n" \
"v_mfma_f32_16x16x32_bf16 v[108:111], a[196:199], a[220:223], v[108:111]\n" \
"buffer_load_dwordx4 v197, s[48:51], s76 offen offset:0 lds\n" \
"v_mfma_f32_16x16x32_bf16 v[104:107], a[192:195], a[208:211], v[104:107]\n" \
"v_mfma_f32_16x16x32_bf16 v[104:107], a[196:199], a[212:215], v[104:107]\n" \
"s_waitcnt lgkmcnt(2)\n" \
"v_mfma_f32_16x16x32_bf16 v[32:35], a[32:35], a[0:3], v[32:35]\n" \
"s_add_u32 s77, s76, 24576\n" \
"ds_read_b128 a[192:195], v205 offset:12288\n" \
"v_mfma_f32_16x16x32_bf16 v[32:35], a[36:39], a[4:7], v[32:35]\n" \
"buffer_load_dwordx4 v198, s[48:51], s76 offen offset:1024 lds\n" \
"v_mfma_f32_16x16x32_bf16 v[40:43], a[32:35], a[8:11], v[40:43]\n" \
"ds_read_b128 a[196:199], v206 offset:12288\n" \
"v_mfma_f32_16x16x32_bf16 v[40:43], a[36:39], a[12:15], v[40:43]\n" \
"ds_read_b64_tr_b16 a[200:201], v210 offset:1024\n" \
"v_mfma_f32_16x16x32_bf16 v[0:3], a[40:43], a[8:11], v[0:3]\n" \
"s_waitcnt lgkmcnt(3)\n" \
"s_xor_b32 s80, s80, 0x1\n" \
"ds_read_b64_tr_b16 a[202:203], v210 offset:4096\n" \
"v_mfma_f32_16x16x32_bf16 v[0:3], a[44:47], a[12:15], v[0:3]\n" \
"buffer_load_dwordx4 v199, s[48:51], s76 offen offset:2048 lds\n" \
"v_mfma_f32_16x16x32_bf16 v[4:7], a[48:51], a[8:11], v[4:7]\n" \
"ds_read_b64_tr_b16 a[204:205], v210 offset:13312\n" \
"v_mfma_f32_16x16x32_bf16 v[4:7], a[52:55], a[12:15], v[4:7]\n" \
"ds_read_b64_tr_b16 a[206:207], v210 offset:16384\n" \
"v_mfma_f32_16x16x32_bf16 v[12:15], a[48:51], a[16:19], v[12:15]\n" \
"s_add_u32 m0, m0, 3072\n" \
"ds_read_b64_tr_b16 a[208:209], v209 offset:2048\n" \
"v_mfma_f32_16x16x32_bf16 v[12:15], a[52:55], a[20:23], v[12:15]\n" \
"buffer_load_dwordx4 v197, s[48:51], s77 offen offset:0 lds\n" \
"v_mfma_f32_16x16x32_bf16 v[8:11], a[40:43], a[16:19], v[8:11]\n" \
"ds_read_b64_tr_b16 a[210:211], v209 offset:5120\n" \
"v_mfma_f32_16x16x32_bf16 v[8:11], a[44:47], a[20:23], v[8:11]\n" \
"ds_read_b64_tr_b16 a[212:213], v209 offset:14336\n" \
"v_mfma_f32_16x16x32_bf16 v[48:51], a[32:35], a[16:19], v[48:51]\n" \
"ds_read_b64_tr_b16 a[214:215], v209 offset:17408\n" \
"v_mfma_f32_16x16x32_bf16 v[48:51], a[36:39], a[20:23], v[48:51]\n" \
"buffer_load_dwordx4 v198, s[48:51], s77 offen offset:1024 lds\n" \
"v_mfma_f32_16x16x32_bf16 v[56:59], a[32:35], a[24:27], v[56:59]\n" \
"ds_read_b64_tr_b16 a[216:217], v210 offset:2048\n" \
"v_mfma_f32_16x16x32_bf16 v[56:59], a[36:39], a[28:31], v[56:59]\n" \
"ds_read_b64_tr_b16 a[218:219], v210 offset:5120\n" \
"v_mfma_f32_16x16x32_bf16 v[16:19], a[40:43], a[24:27], v[16:19]\n" \
"s_waitcnt lgkmcnt(3)\n" \
"ds_read_b64_tr_b16 a[220:221], v210 offset:14336\n" \
"v_mfma_f32_16x16x32_bf16 v[16:19], a[44:47], a[28:31], v[16:19]\n" \
"buffer_load_dwordx4 v199, s[48:51], s77 offen offset:2048 lds\n" \
"v_mfma_f32_16x16x32_bf16 v[20:23], a[48:51], a[24:27], v[20:23]\n" \
"s_mov_b32 s46, s81\n" \
"s_mov_b32 s47, s82\n" \
"ds_read_b64_tr_b16 a[222:223], v210 offset:17408\n" \
"v_mfma_f32_16x16x32_bf16 v[20:23], a[52:55], a[28:31], v[20:23]\n" \
"s_mov_b32 s48, 0\n" \
"s_mov_b32 s49, s68\n" \
"s_sub_u32 s50, 64, s9\n" \
"v_mfma_f32_16x16x32_bf16 v[28:31], a[48:51], a[192:195], v[28:31]\n" \
"s_cmp_eq_u32 s90, 0\n" \
"s_cselect_b32 s50, s9, s50\n" \
"s_andn2_b32 s50, s50, 3\n" \
"v_mfma_f32_16x16x32_bf16 v[28:31], a[52:55], a[196:199], v[28:31]\n" \
"s_lshl_b32 s50, s50, 12\n" \
"s_waitcnt vmcnt(11)\n" \
"v_cmp_eq_u32 vcc, s60, v220\n" \
"v_mfma_f32_16x16x32_bf16 v[24:27], a[40:43], a[192:195], v[24:27]\n" \
"v_add_u32 v205, 0, v200\n" \
"v_add_u32 v206, 0, v201\n" \
"v_mfma_f32_16x16x32_bf16 v[24:27], a[44:47], a[196:199], v[24:27]\n" \
"s_andn2_b32 s70, s65, vcc_lo\n" \
"s_cbranch_scc1 Lpl5_%=\n" \
"Lpr5_%=:\n" \
"v_add_u32 v207, 0, v202\n" \
"v_add_u32 v208, 0, v203\n" \
"v_mfma_f32_16x16x32_bf16 v[64:67], a[32:35], a[192:195], v[64:67]\n" \
"v_add_u32 v209, 20480, v204\n" \
"v_xor_b32 v210, 32, v209\n" \
"v_mfma_f32_16x16x32_bf16 v[64:67], a[36:39], a[196:199], v[64:67]\n" \
"s_xor_b32 s82, s48, s49\n" \
"s_cmp_lt_u32 s48, s50\n" \
"s_cselect_b32 s82, s82, s48\n" \
"s_waitcnt vmcnt(12) lgkmcnt(0)\n" \
"s_barrier\n" \
"v_mfma_f32_16x16x32_bf16 v[72:75], a[0:3], a[208:211], v[72:75]\n" \
"ds_read_b64_tr_b16 a[32:33], v209 offset:1024\n" \
"ds_read_b64_tr_b16 a[34:35], v209 offset:4096\n" \
"Lpg9574_%=:\n" \
"Lst1x3_%=:\n" \
"s_cbranch_execz Lpg11721_%=\n" \
"v_mfma_f32_16x16x32_bf16 v[72:75], a[4:7], a[212:215], v[72:75]\n" \
"s_add_u32 s81, s82, s46\n" \
"s_lshr_b32 s78, s61, 1\n" \
"s_lshl_b32 s80, s78, 9\n" \
"s_add_u32 s80, s80, 511\n" \
"ds_read_b64_tr_b16 a[36:37], v209 offset:13312\n" \
"ds_read_b64_tr_b16 a[38:39], v209 offset:16384\n" \
"v_mfma_f32_16x16x32_bf16 v[80:83], a[8:11], a[208:211], v[80:83]\n" \
"s_add_u32 m0, s86, s79\n" \
"ds_read_b128 a[0:3], v207 offset:0\n" \
"v_sub_u32 v145, s80, v234\n" \
"v_mfma_f32_16x16x32_bf16 v[80:83], a[12:15], a[212:215], v[80:83]\n" \
"buffer_load_dwordx4 v234, s[56:59], s81 offen offset:0 sc1 lds\n" \
"v_mfma_f32_16x16x32_bf16 v[84:87], a[8:11], a[216:219], v[84:87]\n" \
"s_lshl_b32 s80, s78, 3\n" \
"s_not_b32 s80, s80\n" \
"ds_read_b128 a[4:7], v208 offset:0\n" \
"v_mfma_f32_16x16x32_bf16 v[84:87], a[12:15], a[220:223], v[84:87]\n" \
"s_lshl_b32 s82, s82, 5\n" \
"ds_read_b64_tr_b16 a[40:41], v209 offset:0\n" \
"v_sub_u32 v146, s80, v238\n" \
"v_mfma_f32_16x16x32_bf16 v[44:47], a[200:203], a[8:11], v[44:47]\n" \
"s_add_u32 s82, s82, s47\n" \
"ds_read_b64_tr_b16 a[42:43], v209 offset:3072\n" \
"v_subrev_u32 v152, 0, v238\n" \
"v_mfma_f32_16x16x32_bf16 v[44:47], a[204:207], a[12:15], v[44:47]\n" \
"buffer_load_dwordx4 v234, s[56:59], s81 offen offset:1024 sc1 lds\n" \
"v_mfma_f32_16x16x32_bf16 v[52:55], a[200:203], a[16:19], v[52:55]\n" \
"ds_read_b128 a[8:11], v205 offset:0\n" \
"v_sub_u32 v144, 7, v238\n" \
"v_mfma_f32_16x16x32_bf16 v[52:55], a[204:207], a[20:23], v[52:55]\n" \
"s_add_u32 s48, s48, 0x1000\n" \
"ds_read_b128 a[12:15], v206 offset:0\n" \
"v_or_b32 v152, v152, v144\n" \
"v_mfma_f32_16x16x32_bf16 v[92:95], a[16:19], a[216:219], v[92:95]\n" \
"s_sub_u32 s86, s86, 32768\n" \
"s_cselect_b32 s86, 131072, s86\n" \
"ds_read_b64_tr_b16 a[44:45], v209 offset:12288\n" \
"v_mfma_f32_16x16x32_bf16 v[92:95], a[20:23], a[220:223], v[92:95]\n" \
"buffer_load_dwordx4 v234, s[56:59], s81 offen offset:2048 sc1 lds\n" \
"v_mfma_f32_16x16x32_bf16 v[88:91], a[16:19], a[208:211], v[88:91]\n" \
"ds_read_b64_tr_b16 a[46:47], v209 offset:15360\n" \
"v_ashrrev_i32 v152, 31, v152\n" \
"v_mfma_f32_16x16x32_bf16 v[88:91], a[20:23], a[212:215], v[88:91]\n" \
"ds_read_b64_tr_b16 a[48:49], v210 offset:0\n" \
"v_and_b32 v152, 0xff800000, v152\n" \
"v_mfma_f32_16x16x32_bf16 v[96:99], a[24:27], a[208:211], v[96:99]\n" \
"ds_read_b128 a[16:19], v205 offset:4096\n" \
"v_mov_b32 v160, v152\n" \
"v_mfma_f32_16x16x32_bf16 v[96:99], a[28:31], a[212:215], v[96:99]\n" \
"buffer_load_dwordx4 v234, s[56:59], s81 offen offset:3072 sc1 lds\n" \
"v_mfma_f32_16x16x32_bf16 v[100:103], a[24:27], a[216:219], v[100:103]\n" \
"ds_read_b128 a[20:23], v206 offset:4096\n" \
"v_mov_b32 v168, v152\n" \
"v_mfma_f32_16x16x32_bf16 v[100:103], a[28:31], a[220:223], v[100:103]\n" \
"ds_read_b64_tr_b16 a[50:51], v210 offset:3072\n" \
"v_mov_b32 v176, v152\n" \
"v_mfma_f32_16x16x32_bf16 v[60:63], a[200:203], a[24:27], v[60:63]\n" \
"s_add_u32 m0, m0, 0x4000\n" \
"ds_read_b64_tr_b16 a[52:53], v210 offset:12288\n" \
"v_sub_u32 v156, -9, v238\n" \
"v_mfma_f32_16x16x32_bf16 v[60:63], a[204:207], a[28:31], v[60:63]\n" \
"buffer_load_dwordx4 v216, s[36:39], s82 offen offset:0 lds\n" \
"v_mfma_f32_16x16x32_bf16 v[68:71], a[200:203], a[192:195], v[68:71]\n" \
"ds_read_b128 a[24:27], v205 offset:8192\n" \
"v_ashrrev_i32 v156, 31, v156\n" \
"v_mfma_f32_16x16x32_bf16 v[68:71], a[204:207], a[196:199], v[68:71]\n" \
"ds_read_b128 a[28:31], v206 offset:8192\n" \
"v_and_b32 v156, 0xff800000, v156\n" \
"v_mfma_f32_16x16x32_bf16 v[108:111], a[192:195], a[216:219], v[108:111]\n" \
"ds_read_b64_tr_b16 a[54:55], v210 offset:15360\n" \
"v_mov_b32 v164, v156\n" \
"v_mfma_f32_16x16x32_bf16 v[108:111], a[196:199], a[220:223], v[108:111]\n" \
"buffer_load_dwordx4 v217, s[36:39], s82 offen offset:1024 lds\n" \
"v_mfma_f32_16x16x32_bf16 v[104:107], a[192:195], a[208:211], v[104:107]\n" \
"v_mov_b32 v172, v156\n" \
"v_add_u32 v148, 0, v146\n" \
"v_mfma_f32_16x16x32_bf16 v[104:107], a[196:199], a[212:215], v[104:107]\n" \
"s_waitcnt lgkmcnt(2)\n" \
"v_or3_b32 v148, v148, v145, s92\n" \
"v_ashrrev_i32 v148, 31, v148\n" \
"v_mfma_f32_16x16x32_bf16 v[32:35], a[32:35], a[0:3], v[32:35]\n" \
"ds_read_b128 a[192:195], v205 offset:12288\n" \
"v_and_b32 v148, 0xff800000, v148\n" \
"v_mfma_f32_16x16x32_bf16 v[32:35], a[36:39], a[4:7], v[32:35]\n" \
"buffer_load_dwordx4 v218, s[36:39], s82 offen offset:2048 lds\n" \
"v_mfma_f32_16x16x32_bf16 v[40:43], a[32:35], a[8:11], v[40:43]\n" \
"ds_read_b128 a[196:199], v206 offset:12288\n" \
"v_subrev_u32 v153, 1, v238\n" \
"v_mfma_f32_16x16x32_bf16 v[40:43], a[36:39], a[12:15], v[40:43]\n" \
"ds_read_b64_tr_b16 a[200:201], v210 offset:1024\n" \
"v_sub_u32 v144, 8, v238\n" \
"v_mfma_f32_16x16x32_bf16 v[0:3], a[40:43], a[8:11], v[0:3]\n" \
"s_waitcnt lgkmcnt(3)\n" \
"ds_read_b64_tr_b16 a[202:203], v210 offset:4096\n" \
"v_or_b32 v153, v153, v144\n" \
"v_mfma_f32_16x16x32_bf16 v[0:3], a[44:47], a[12:15], v[0:3]\n" \
"buffer_load_dwordx4 v219, s[36:39], s82 offen offset:3072 lds\n" \
"v_mfma_f32_16x16x32_bf16 v[4:7], a[48:51], a[8:11], v[4:7]\n" \
"ds_read_b64_tr_b16 a[204:205], v210 offset:13312\n" \
"v_ashrrev_i32 v153, 31, v153\n" \
"v_mfma_f32_16x16x32_bf16 v[4:7], a[52:55], a[12:15], v[4:7]\n" \
"ds_read_b64_tr_b16 a[206:207], v210 offset:16384\n" \
"v_and_b32 v153, 0xff800000, v153\n" \
"v_mfma_f32_16x16x32_bf16 v[12:15], a[48:51], a[16:19], v[12:15]\n" \
"ds_read_b64_tr_b16 a[208:209], v209 offset:2048\n" \
"v_mov_b32 v161, v153\n" \
"v_mfma_f32_16x16x32_bf16 v[12:15], a[52:55], a[20:23], v[12:15]\n" \
"v_mov_b32 v169, v153\n" \
"v_mov_b32 v177, v153\n" \
"v_mfma_f32_16x16x32_bf16 v[8:11], a[40:43], a[16:19], v[8:11]\n" \
"ds_read_b64_tr_b16 a[210:211], v209 offset:5120\n" \
"v_sub_u32 v157, -8, v238\n" \
"v_mfma_f32_16x16x32_bf16 v[8:11], a[44:47], a[20:23], v[8:11]\n" \
"ds_read_b64_tr_b16 a[212:213], v209 offset:14336\n" \
"v_ashrrev_i32 v157, 31, v157\n" \
"v_mfma_f32_16x16x32_bf16 v[48:51], a[32:35], a[16:19], v[48:51]\n" \
"ds_read_b64_tr_b16 a[214:215], v209 offset:17408\n" \
"v_and_b32 v157, 0xff800000, v157\n" \
"v_mfma_f32_16x16x32_bf16 v[48:51], a[36:39], a[20:23], v[48:51]\n" \
"v_mov_b32 v165, v157\n" \
"v_mov_b32 v173, v157\n" \
"v_mfma_f32_16x16x32_bf16 v[56:59], a[32:35], a[24:27], v[56:59]\n" \
"ds_read_b64_tr_b16 a[216:217], v210 offset:2048\n" \
"v_add_u32 v149, 1, v146\n" \
"v_mfma_f32_16x16x32_bf16 v[56:59], a[36:39], a[28:31], v[56:59]\n" \
"ds_read_b64_tr_b16 a[218:219], v210 offset:5120\n" \
"v_or3_b32 v149, v149, v145, s92\n" \
"v_mfma_f32_16x16x32_bf16 v[16:19], a[40:43], a[24:27], v[16:19]\n" \
"ds_read_b64_tr_b16 a[220:221], v210 offset:14336\n" \
"v_ashrrev_i32 v149, 31, v149\n" \
"v_mfma_f32_16x16x32_bf16 v[16:19], a[44:47], a[28:31], v[16:19]\n" \
"v_and_b32 v149, 0xff800000, v149\n" \
"v_subrev_u32 v154, 2, v238\n" \
"v_mfma_f32_16x16x32_bf16 v[20:23], a[48:51], a[24:27], v[20:23]\n" \
"ds_read_b64_tr_b16 a[222:223], v210 offset:17408\n" \
"v_sub_u32 v144, 9, v238\n" \
"v_mfma_f32_16x16x32_bf16 v[20:23], a[52:55], a[28:31], v[20:23]\n" \
"s_waitcnt lgkmcnt(3)\n" \
"v_or_b32 v154, v154, v144\n" \
"v_ashrrev_i32 v154, 31, v154\n" \
"v_mfma_f32_16x16x32_bf16 v[28:31], a[48:51], a[192:195], v[28:31]\n" \
"v_and_b32 v154, 0xff800000, v154\n" \
"v_mov_b32 v162, v154\n" \
"v_mfma_f32_16x16x32_bf16 v[28:31], a[52:55], a[196:199], v[28:31]\n" \
"v_mov_b32 v170, v154\n" \
"v_mov_b32 v178, v154\n" \
"v_mfma_f32_16x16x32_bf16 v[24:27], a[40:43], a[192:195], v[24:27]\n" \
"v_add_u32 v205, 110592, v200\n" \
"v_add_u32 v206, 110592, v201\n" \
"v_mfma_f32_16x16x32_bf16 v[24:27], a[44:47], a[196:199], v[24:27]\n" \
"v_add_u32 v207, 110592, v202\n" \
"v_add_u32 v208, 110592, v203\n" \
"v_mfma_f32_16x16x32_bf16 v[64:67], a[32:35], a[192:195], v[64:67]\n" \
"v_add_u32 v209, 139264, v204\n" \
"v_xor_b32 v210, 32, v209\n" \
"v_mfma_f32_16x16x32_bf16 v[64:67], a[36:39], a[196:199], v[64:67]\n" \
"s_xor_b32 s82, s48, s49\n" \
"s_cmp_lt_u32 s48, s50\n" \
"s_cselect_b32 s82, s82, s48\n" \
"s_waitcnt vmcnt(8) lgkmcnt(0)\n" \
"s_barrier\n" \
"v_mfma_f32_16x16x32_bf16 v[72:75], a[0:3], a[208:211], v[72:75]\n" \
"ds_read_b64_tr_b16 a[32:33], v209 offset:1024\n" \
"ds_read_b64_tr_b16 a[34:35], v209 offset:4096\n" \
"v_mfma_f32_16x16x32_bf16 v[72:75], a[4:7], a[212:215], v[72:75]\n" \
"s_add_u32 s81, s82, s46\n" \
"ds_read_b64_tr_b16 a[36:37], v209 offset:13312\n" \
"ds_read_b64_tr_b16 a[38:39], v209 offset:16384\n" \
"v_mfma_f32_16x16x32_bf16 v[80:83], a[8:11], a[208:211], v[80:83]\n" \
"s_add_u32 m0, s86, s79\n" \
"ds_read_b128 a[0:3], v207 offset:0\n" \
"v_sub_u32 v158, -7, v238\n" \
"v_mfma_f32_16x16x32_bf16 v[80:83], a[12:15], a[212:215], v[80:83]\n" \
"buffer_load_dwordx4 v234, s[56:59], s81 offen offset:0 sc1 lds\n" \
"v_mfma_f32_16x16x32_bf16 v[84:87], a[8:11], a[216:219], v[84:87]\n" \
"ds_read_b128 a[4:7], v208 offset:0\n" \
"v_ashrrev_i32 v158, 31, v158\n" \
"v_mfma_f32_16x16x32_bf16 v[84:87], a[12:15], a[220:223], v[84:87]\n" \
"s_lshl_b32 s82, s82, 5\n" \
"ds_read_b64_tr_b16 a[40:41], v209 offset:0\n" \
"v_and_b32 v158, 0xff800000, v158\n" \
"v_mfma_f32_16x16x32_bf16 v[44:47], a[200:203], a[8:11], v[44:47]\n" \
"s_add_u32 s82, s82, s47\n" \
"ds_read_b64_tr_b16 a[42:43], v209 offset:3072\n" \
"v_mov_b32 v166, v158\n" \
"v_mfma_f32_16x16x32_bf16 v[44:47], a[204:207], a[12:15], v[44:47]\n" \
"buffer_load_dwordx4 v234, s[56:59], s81 offen offset:1024 sc1 lds\n" \
"v_mfma_f32_16x16x32_bf16 v[52:55], a[200:203], a[16:19], v[52:55]\n" \
"ds_read_b128 a[8:11], v205 offset:0\n" \
"v_mov_b32 v174, v158\n" \
"v_mfma_f32_16x16x32_bf16 v[52:55], a[204:207], a[20:23], v[52:55]\n" \
"s_add_u32 s48, s48, 0x1000\n" \
"ds_read_b128 a[12:15], v206 offset:0\n" \
"v_add_u32 v150, 2, v146\n" \
"v_mfma_f32_16x16x32_bf16 v[92:95], a[16:19], a[216:219], v[92:95]\n" \
"s_sub_u32 s86, s86, 32768\n" \
"s_cselect_b32 s86, 131072, s86\n" \
"ds_read_b64_tr_b16 a[44:45], v209 offset:12288\n" \
"v_mfma_f32_16x16x32_bf16 v[92:95], a[20:23], a[220:223], v[92:95]\n" \
"buffer_load_dwordx4 v234, s[56:59], s81 offen offset:2048 sc1 lds\n" \
"v_mfma_f32_16x16x32_bf16 v[88:91], a[16:19], a[208:211], v[88:91]\n" \
"ds_read_b64_tr_b16 a[46:47], v209 offset:15360\n" \
"v_or3_b32 v150, v150, v145, s92\n" \
"v_mfma_f32_16x16x32_bf16 v[88:91], a[20:23], a[212:215], v[88:91]\n" \
"ds_read_b64_tr_b16 a[48:49], v210 offset:0\n" \
"v_ashrrev_i32 v150, 31, v150\n" \
"v_mfma_f32_16x16x32_bf16 v[96:99], a[24:27], a[208:211], v[96:99]\n" \
"ds_read_b128 a[16:19], v205 offset:4096\n" \
"v_and_b32 v150, 0xff800000, v150\n" \
"v_mfma_f32_16x16x32_bf16 v[96:99], a[28:31], a[212:215], v[96:99]\n" \
"buffer_load_dwordx4 v234, s[56:59], s81 offen offset:3072 sc1 lds\n" \
"v_mfma_f32_16x16x32_bf16 v[100:103], a[24:27], a[216:219], v[100:103]\n" \
"ds_read_b128 a[20:23], v206 offset:4096\n" \
"v_subrev_u32 v155, 3, v238\n" \
"v_mfma_f32_16x16x32_bf16 v[100:103], a[28:31], a[220:223], v[100:103]\n" \
"ds_read_b64_tr_b16 a[50:51], v210 offset:3072\n" \
"v_sub_u32 v144, 10, v238\n" \
"v_mfma_f32_16x16x32_bf16 v[60:63], a[200:203], a[24:27], v[60:63]\n" \
"s_add_u32 m0, m0, 0x4000\n" \
"ds_read_b64_tr_b16 a[52:53], v210 offset:12288\n" \
"v_or_b32 v155, v155, v144\n" \
"v_mfma_f32_16x16x32_bf16 v[60:63], a[204:207], a[28:31], v[60:63]\n" \
"buffer_load_dwordx4 v216, s[36:39], s82 offen offset:0 lds\n" \
"v_mfma_f32_16x16x32_bf16 v[68:71], a[200:203], a[192:195], v[68:71]\n" \
"ds_read_b128 a[24:27], v205 offset:8192\n" \
"v_ashrrev_i32 v155, 31, v155\n" \
"v_mfma_f32_16x16x32_bf16 v[68:71], a[204:207], a[196:199], v[68:71]\n" \
"ds_read_b128 a[28:31], v206 offset:8192\n" \
"v_and_b32 v155, 0xff800000, v155\n" \
"v_mfma_f32_16x16x32_bf16 v[108:111], a[192:195], a[216:219], v[108:111]\n" \
"ds_read_b64_tr_b16 a[54:55], v210 offset:15360\n" \
"v_mov_b32 v163, v155\n" \
"v_mfma_f32_16x16x32_bf16 v[108:111], a[196:199], a[220:223], v[108:111]\n" \
"buffer_load_dwordx4 v217, s[36:39], s82 offen offset:1024 lds\n" \
"v_mfma_f32_16x16x32_bf16 v[104:107], a[192:195], a[208:211], v[104:107]\n" \
"v_mov_b32 v171, v155\n" \
"v_mov_b32 v179, v155\n" \
"v_mfma_f32_16x16x32_bf16 v[104:107], a[196:199], a[212:215], v[104:107]\n" \
"s_waitcnt lgkmcnt(2)\n" \
"v_sub_u32 v159, -6, v238\n" \
"v_ashrrev_i32 v159, 31, v159\n" \
"v_mfma_f32_16x16x32_bf16 v[32:35], a[32:35], a[0:3], v[32:35]\n" \
"ds_read_b128 a[192:195], v205 offset:12288\n" \
"v_and_b32 v159, 0xff800000, v159\n" \
"v_mfma_f32_16x16x32_bf16 v[32:35], a[36:39], a[4:7], v[32:35]\n" \
"buffer_load_dwordx4 v218, s[36:39], s82 offen offset:2048 lds\n" \
"v_mfma_f32_16x16x32_bf16 v[40:43], a[32:35], a[8:11], v[40:43]\n" \
"ds_read_b128 a[196:199], v206 offset:12288\n" \
"v_mov_b32 v167, v159\n" \
"v_mfma_f32_16x16x32_bf16 v[40:43], a[36:39], a[12:15], v[40:43]\n" \
"ds_read_b64_tr_b16 a[200:201], v210 offset:1024\n" \
"v_mov_b32 v175, v159\n" \
"v_mfma_f32_16x16x32_bf16 v[0:3], a[40:43], a[8:11], v[0:3]\n" \
"s_waitcnt lgkmcnt(3)\n" \
"ds_read_b64_tr_b16 a[202:203], v210 offset:4096\n" \
"v_add_u32 v151, 3, v146\n" \
"v_mfma_f32_16x16x32_bf16 v[0:3], a[44:47], a[12:15], v[0:3]\n" \
"buffer_load_dwordx4 v219, s[36:39], s82 offen offset:3072 lds\n" \
"v_mfma_f32_16x16x32_bf16 v[4:7], a[48:51], a[8:11], v[4:7]\n" \
"s_xor_b32 s82, s48, s49\n" \
"s_cmp_lt_u32 s48, s50\n" \
"s_cselect_b32 s82, s82, s48\n" \
"ds_read_b64_tr_b16 a[204:205], v210 offset:13312\n" \
"v_mfma_f32_16x16x32_bf16 v[4:7], a[52:55], a[12:15], v[4:7]\n" \
"s_add_u32 s81, s82, s46\n" \
"ds_read_b64_tr_b16 a[206:207], v210 offset:16384\n" \
"v_or3_b32 v151, v151, v145, s92\n" \
"v_mfma_f32_16x16x32_bf16 v[12:15], a[48:51], a[16:19], v[12:15]\n" \
"s_add_u32 m0, s86, s79\n" \
"ds_read_b64_tr_b16 a[208:209], v209 offset:2048\n" \
"v_ashrrev_i32 v151, 31, v151\n" \
"v_mfma_f32_16x16x32_bf16 v[12:15], a[52:55], a[20:23], v[12:15]\n" \
"buffer_load_dwordx4 v234, s[56:59], s81 offen offset:0 sc1 lds\n" \
"v_mfma_f32_16x16x32_bf16 v[8:11], a[40:43], a[16:19], v[8:11]\n" \
"ds_read_b64_tr_b16 a[210:211], v209 offset:5120\n" \
"v_and_b32 v151, 0xff800000, v151\n" \
"v_mfma_f32_16x16x32_bf16 v[8:11], a[44:47], a[20:23], v[8:11]\n" \
"s_lshl_b32 s82, s82, 5\n" \
"ds_read_b64_tr_b16 a[212:213], v209 offset:14336\n" \
"v_mov_b32 v186, 0xffff0000\n" \
"v_mfma_f32_16x16x32_bf16 v[48:51], a[32:35], a[16:19], v[48:51]\n" \
"s_add_u32 s82, s82, s47\n" \
"ds_read_b64_tr_b16 a[214:215], v209 offset:17408\n" \
"v_subrev_u32 v144, 0, v238\n" \
"v_mfma_f32_16x16x32_bf16 v[48:51], a[36:39], a[20:23], v[48:51]\n" \
"buffer_load_dwordx4 v234, s[56:59], s81 offen offset:1024 sc1 lds\n" \
"v_mfma_f32_16x16x32_bf16 v[56:59], a[32:35], a[24:27], v[56:59]\n" \
"ds_read_b64_tr_b16 a[216:217], v210 offset:2048\n" \
"v_subrev_u32 v184, 1, v238\n" \
"v_mfma_f32_16x16x32_bf16 v[56:59], a[36:39], a[28:31], v[56:59]\n" \
"s_add_u32 s48, s48, 0x1000\n" \
"ds_read_b64_tr_b16 a[218:219], v210 offset:5120\n" \
"v_lshrrev_b32 v144, 16, v144\n" \
"v_mfma_f32_16x16x32_bf16 v[16:19], a[40:43], a[24:27], v[16:19]\n" \
"s_sub_u32 s86, s86, 32768\n" \
"s_cselect_b32 s86, 131072, s86\n" \
"ds_read_b64_tr_b16 a[220:221], v210 offset:14336\n" \
"v_mfma_f32_16x16x32_bf16 v[16:19], a[44:47], a[28:31], v[16:19]\n" \
"buffer_load_dwordx4 v234, s[56:59], s81 offen offset:2048 sc1 lds\n" \
"v_mfma_f32_16x16x32_bf16 v[20:23], a[48:51], a[24:27], v[20:23]\n" \
"ds_read_b64_tr_b16 a[222:223], v210 offset:17408\n" \
"v_and_or_b32 v184, v184, v186, v144\n" \
"v_mfma_f32_16x16x32_bf16 v[20:23], a[52:55], a[28:31], v[20:23]\n" \
"s_waitcnt lgkmcnt(3)\n" \
"v_subrev_u32 v144, 2, v238\n" \
"v_subrev_u32 v185, 3, v238\n" \
"v_mfma_f32_16x16x32_bf16 v[28:31], a[48:51], a[192:195], v[28:31]\n" \
"v_lshrrev_b32 v144, 16, v144\n" \
"v_and_or_b32 v185, v185, v186, v144\n" \
"v_mfma_f32_16x16x32_bf16 v[28:31], a[52:55], a[196:199], v[28:31]\n" \
"buffer_load_dwordx4 v234, s[56:59], s81 offen offset:3072 sc1 lds\n" \
"v_mfma_f32_16x16x32_bf16 v[24:27], a[40:43], a[192:195], v[24:27]\n" \
"v_mov_b32 v144, 0x3e8293ee\n" \
"s_add_u32 s80, s79, 117760\n" \
"v_mfma_f32_16x16x32_bf16 v[24:27], a[44:47], a[196:199], v[24:27]\n" \
"v_add_u32 v223, s80, v234\n" \
"v_mfma_f32_16x16x32_bf16 v[64:67], a[32:35], a[192:195], v[64:67]\n" \
"s_add_u32 m0, m0, 0x4000\n" \
"s_cmp_lg_u32 s78, 0\n" \
"s_cselect_b64 s[76:77], -1, 0\n" \
"v_mfma_f32_16x16x32_bf16 v[64:67], a[36:39], a[196:199], v[64:67]\n" \
"buffer_load_dwordx4 v216, s[36:39], s82 offen offset:0 lds\n" \
"s_waitcnt vmcnt(13) lgkmcnt(0)\n" \
"s_barrier\n" \
"v_mfma_f32_16x16x32_bf16 v[72:75], a[0:3], a[208:211], v[72:75]\n" \
"ds_read_b64_tr_b16 a[96:97], v214 offset:16384\n" \
"v_mfma_f32_16x16x32_bf16 v[72:75], a[4:7], a[212:215], v[72:75]\n" \
"ds_read_b64_tr_b16 a[98:99], v214 offset:18432\n" \
"v_mfma_f32_16x16x32_bf16 v[44:47], a[200:203], a[8:11], v[44:47]\n" \
"ds_read_b64_tr_b16 a[100:101], v214 offset:24576\n" \
"v_mfma_f32_16x16x32_bf16 v[44:47], a[204:207], a[12:15], v[44:47]\n" \
"buffer_load_dwordx4 v217, s[36:39], s82 offen offset:1024 lds\n" \
"v_mfma_f32_16x16x32_bf16 v[80:83], a[8:11], a[208:211], v[80:83]\n" \
"ds_read_b64_tr_b16 a[102:103], v214 offset:26624\n" \
"v_cvt_pk_bf16_f32 v112, v0, v1\n" \
"v_mfma_f32_16x16x32_bf16 v[80:83], a[12:15], a[212:215], v[80:83]\n" \
"ds_read_b128 a[64:67], v213 offset:0\n" \
"v_cvt_pk_bf16_f32 v113, v2, v3\n" \
"v_mfma_f32_16x16x32_bf16 v[84:87], a[8:11], a[216:219], v[84:87]\n" \
"ds_read_b128 a[68:71], v213 offset:2048\n" \
"v_cvt_pk_bf16_f32 v114, v4, v5\n" \
"v_mfma_f32_16x16x32_bf16 v[84:87], a[12:15], a[220:223], v[84:87]\n" \
"buffer_load_dwordx4 v218, s[36:39], s82 offen offset:2048 lds\n" \
"v_mfma_f32_16x16x32_bf16 v[52:55], a[200:203], a[16:19], v[52:55]\n" \
"ds_read_b64_tr_b16 a[104:105], v215 offset:16384\n" \
"v_cvt_pk_bf16_f32 v115, v6, v7\n" \
"v_mfma_f32_16x16x32_bf16 v[52:55], a[204:207], a[20:23], v[52:55]\n" \
"ds_read_b64_tr_b16 a[106:107], v215 offset:18432\n" \
"v_cvt_pk_bf16_f32 v116, v8, v9\n" \
"v_mfma_f32_16x16x32_bf16 v[88:91], a[16:19], a[208:211], v[88:91]\n" \
"ds_read_b64_tr_b16 a[108:109], v215 offset:24576\n" \
"v_cvt_pk_bf16_f32 v117, v10, v11\n" \
"v_mfma_f32_16x16x32_bf16 v[88:91], a[20:23], a[212:215], v[88:91]\n" \
"buffer_load_dwordx4 v219, s[36:39], s82 offen offset:3072 lds\n" \
"v_mfma_f32_16x16x32_bf16 v[92:95], a[16:19], a[216:219], v[92:95]\n" \
"s_xor_b32 s82, s48, s49\n" \
"s_cmp_lt_u32 s48, s50\n" \
"s_cselect_b32 s82, s82, s48\n" \
"ds_read_b64_tr_b16 a[110:111], v215 offset:26624\n" \
"v_mfma_f32_16x16x32_bf16 v[92:95], a[20:23], a[220:223], v[92:95]\n" \
"s_add_u32 s81, s82, s46\n" \
"ds_read_b128 a[72:75], v213 offset:256\n" \
"v_cvt_pk_bf16_f32 v118, v12, v13\n" \
"v_mfma_f32_16x16x32_bf16 v[60:63], a[200:203], a[24:27], v[60:63]\n" \
"s_add_u32 m0, s86, s79\n" \
"ds_read_b128 a[76:79], v213 offset:2304\n" \
"v_cvt_pk_bf16_f32 v119, v14, v15\n" \
"v_mfma_f32_16x16x32_bf16 v[60:63], a[204:207], a[28:31], v[60:63]\n" \
"buffer_load_dwordx4 v234, s[56:59], s81 offen offset:0 sc1 lds\n" \
"v_mfma_f32_16x16x32_bf16 v[96:99], a[24:27], a[208:211], v[96:99]\n" \
"ds_read_b128 a[80:83], v213 offset:4096\n" \
"v_cvt_pk_bf16_f32 v120, v16, v17\n" \
"v_mfma_f32_16x16x32_bf16 v[96:99], a[28:31], a[212:215], v[96:99]\n" \
"s_lshl_b32 s82, s82, 5\n" \
"ds_read_b128 a[84:87], v213 offset:6144\n" \
"v_cvt_pk_bf16_f32 v121, v18, v19\n" \
"v_mfma_f32_16x16x32_bf16 v[100:103], a[24:27], a[216:219], v[100:103]\n" \
"s_add_u32 s82, s82, s47\n" \
"v_cvt_pk_bf16_f32 v122, v20, v21\n" \
"v_cvt_pk_bf16_f32 v123, v22, v23\n" \
"v_mfma_f32_16x16x32_bf16 v[100:103], a[28:31], a[220:223], v[100:103]\n" \
"buffer_load_dwordx4 v234, s[56:59], s81 offen offset:1024 sc1 lds\n" \
"v_mfma_f32_16x16x32_bf16 v[68:71], a[200:203], a[192:195], v[68:71]\n" \
"v_cvt_pk_bf16_f32 v124, v24, v25\n" \
"v_cvt_pk_bf16_f32 v125, v26, v27\n" \
"v_mfma_f32_16x16x32_bf16 v[68:71], a[204:207], a[196:199], v[68:71]\n" \
"s_add_u32 s48, s48, 0x1000\n" \
"v_cvt_pk_bf16_f32 v126, v28, v29\n" \
"v_cvt_pk_bf16_f32 v127, v30, v31\n" \
"v_mfma_f32_16x16x32_bf16 v[104:107], a[192:195], a[208:211], v[104:107]\n" \
"s_sub_u32 s86, s86, 32768\n" \
"s_cselect_b32 s86, 131072, s86\n" \
"v_cvt_pk_bf16_f32 v128, v32, v33\n" \
"v_mfma_f32_16x16x32_bf16 v[104:107], a[196:199], a[212:215], v[104:107]\n" \
"buffer_load_dwordx4 v234, s[56:59], s81 offen offset:2048 sc1 lds\n" \
"v_mfma_f32_16x16x32_bf16 v[108:111], a[192:195], a[216:219], v[108:111]\n" \
"v_cvt_pk_bf16_f32 v129, v34, v35\n" \
"v_xor_b32 v145, 0x2000, v223\n" \
"v_mfma_f32_16x16x32_bf16 v[108:111], a[196:199], a[220:223], v[108:111]\n" \
"s_waitcnt lgkmcnt(0)\n" \
"v_cvt_pk_bf16_f32 v192, v72, v73\n" \
"v_cvt_pk_bf16_f32 v193, v74, v75\n" \
"ds_write_b64 v145, v[128:129]\n" \
"s_nop 0\n" \
"ds_write_b64 v145, v[192:193] offset:8\n" \
"v_cvt_pk_bf16_f32 v132, v40, v41\n" \
"v_cvt_pk_bf16_f32 v133, v42, v43\n" \
"v_cvt_pk_bf16_f32 v134, v44, v45\n" \
"v_cvt_pk_bf16_f32 v135, v46, v47\n" \
"v_cvt_pk_bf16_f32 v136, v48, v49\n" \
"v_cvt_pk_bf16_f32 v137, v50, v51\n" \
"v_cvt_pk_bf16_f32 v138, v52, v53\n" \
"v_cvt_pk_bf16_f32 v139, v54, v55\n" \
"v_cvt_pk_bf16_f32 v140, v56, v57\n" \
"v_cvt_pk_bf16_f32 v141, v58, v59\n" \
"v_cvt_pk_bf16_f32 v142, v60, v61\n" \
"v_cvt_pk_bf16_f32 v143, v62, v63\n" \
"v_mfma_f32_16x16x32_bf16 v[0:3], a[96:99], a[64:67], 0\n" \
"ds_read_b128 a[88:91], v213 offset:4352\n" \
"v_mfma_f32_16x16x32_bf16 v[0:3], a[100:103], a[68:71], v[0:3]\n" \
"ds_read_b128 a[92:95], v213 offset:6400\n" \
"v_mfma_f32_16x16x32_bf16 v[4:7], a[104:107], a[64:67], 0\n" \
"ds_read_b64_tr_b16 a[112:113], v214 offset:16896\n" \
"v_mfma_f32_16x16x32_bf16 v[4:7], a[108:111], a[68:71], v[4:7]\n" \
"ds_read_b64_tr_b16 a[114:115], v214 offset:18944\n" \
"v_mfma_f32_16x16x32_bf16 v[20:23], a[104:107], a[72:75], 0\n" \
"buffer_load_dwordx4 v234, s[56:59], s81 offen offset:3072 sc1 lds\n" \
"v_mfma_f32_16x16x32_bf16 v[20:23], a[108:111], a[76:79], v[20:23]\n" \
"ds_read_b64_tr_b16 a[116:117], v214 offset:25088\n" \
"v_cvt_pk_bf16_f32 v64, v64, v65\n" \
"v_cvt_pk_bf16_f32 v65, v66, v67\n" \
"v_cvt_pk_bf16_f32 v66, v68, v69\n" \
"v_mfma_f32_16x16x32_bf16 v[16:19], a[96:99], a[72:75], 0\n" \
"ds_read_b64_tr_b16 a[118:119], v214 offset:27136\n" \
"v_cvt_pk_bf16_f32 v67, v70, v71\n" \
"v_mfma_f32_16x16x32_bf16 v[16:19], a[100:103], a[76:79], v[16:19]\n" \
"s_add_u32 m0, m0, 0x4000\n" \
"ds_read_b64_tr_b16 a[120:121], v215 offset:16896\n" \
"v_cvt_pk_bf16_f32 v194, v80, v81\n" \
"v_cvt_pk_bf16_f32 v195, v82, v83\n" \
"v_cvt_pk_bf16_f32 v74, v84, v85\n" \
"v_cvt_pk_bf16_f32 v75, v86, v87\n" \
"v_mfma_f32_16x16x32_bf16 v[32:35], a[96:99], a[80:83], 0\n" \
"buffer_load_dwordx4 v216, s[36:39], s82 offen offset:0 lds\n" \
"v_mfma_f32_16x16x32_bf16 v[32:35], a[100:103], a[84:87], v[32:35]\n" \
"s_sub_u32 s85, s85, 32768\n" \
"s_cselect_b32 s85, 131072, s85\n" \
"ds_read_b64_tr_b16 a[122:123], v215 offset:18944\n" \
"v_cvt_pk_bf16_f32 v196, v88, v89\n" \
"v_cvt_pk_bf16_f32 v197, v90, v91\n" \
"v_mfma_f32_16x16x32_bf16 v[176:179], v[64:67], v[124:127], v[176:179]\n" \
"v_mfma_f32_16x16x32_bf16 v[172:175], v[140:143], v[124:127], v[172:175]\n" \
"v_mfma_f32_16x16x32_bf16 v[168:171], v[140:143], v[120:123], v[168:171]\n" \
"v_mfma_f32_16x16x32_bf16 v[164:167], v[136:139], v[120:123], v[164:167]\n" \
"v_mfma_f32_16x16x32_bf16 v[160:163], v[136:139], v[116:119], v[160:163]\n" \
"v_mfma_f32_16x16x32_bf16 v[156:159], v[132:135], v[116:119], v[156:159]\n" \
"v_mfma_f32_16x16x32_bf16 v[152:155], v[132:135], v[112:115], v[152:155]\n" \
"v_mfma_f32_16x16x32_bf16 v[36:39], a[104:107], a[80:83], 0\n" \
"ds_read_b64_tr_b16 a[124:125], v215 offset:25088\n" \
"v_add_u32 v213, s85, v211\n" \
"v_mfma_f32_16x16x32_bf16 v[36:39], a[108:111], a[84:87], v[36:39]\n" \
"s_waitcnt lgkmcnt(3)\n" \
"ds_read_b64_tr_b16 a[126:127], v215 offset:27136\n" \
"v_mfma_f32_16x16x32_bf16 v[52:55], a[104:107], a[88:91], 0\n" \
"buffer_load_dwordx4 v217, s[36:39], s82 offen offset:1024 lds\n" \
"v_mfma_f32_16x16x32_bf16 v[52:55], a[108:111], a[92:95], v[52:55]\n" \
"v_add_u32 v214, s85, v212\n" \
"v_xor_b32 v215, 32, v214\n" \
"v_mfma_f32_16x16x32_bf16 v[48:51], a[96:99], a[88:91], 0\n" \
"buffer_load_dwordx4 v218, s[36:39], s82 offen offset:2048 lds\n" \
"v_mfma_f32_16x16x32_bf16 v[48:51], a[100:103], a[92:95], v[48:51]\n" \
"s_waitcnt vmcnt(15) lgkmcnt(0)\n" \
"s_barrier\n" \
"v_mfma_f32_16x16x32_bf16 v[8:11], a[112:115], a[64:67], 0\n" \
"ds_read_b64 v[130:131], v223\n" \
"ds_read_b64_tr_b16 a[96:97], v214 offset:16384\n" \
"ds_read_b64_tr_b16 a[98:99], v214 offset:18432\n" \
"v_mfma_f32_16x16x32_bf16 v[8:11], a[116:119], a[68:71], v[8:11]\n" \
"ds_read_b64 v[72:73], v223 offset:8\n" \
"ds_read_b64_tr_b16 a[100:101], v214 offset:24576\n" \
"ds_read_b64_tr_b16 a[102:103], v214 offset:26624\n" \
"v_mfma_f32_16x16x32_bf16 v[12:15], a[120:123], a[64:67], 0\n" \
"buffer_load_dwordx4 v219, s[36:39], s82 offen offset:3072 lds\n" \
"v_mfma_f32_16x16x32_bf16 v[12:15], a[124:127], a[68:71], v[12:15]\n" \
"s_xor_b32 s82, s48, s49\n" \
"s_cmp_lt_u32 s48, s50\n" \
"s_cselect_b32 s82, s82, s48\n" \
"ds_read_b128 a[64:67], v213 offset:0\n" \
"v_mfma_f32_16x16x32_bf16 v[28:31], a[120:123], a[72:75], 0\n" \
"s_add_u32 s81, s82, s46\n" \
"ds_read_b128 a[68:71], v213 offset:2048\n" \
"v_mfma_f32_16x16x32_bf16 v[28:31], a[124:127], a[76:79], v[28:31]\n" \
"s_add_u32 m0, s86, s79\n" \
"ds_read_b64_tr_b16 a[104:105], v215 offset:16384\n" \
"ds_read_b64_tr_b16 a[106:107], v215 offset:18432\n" \
"v_mfma_f32_16x16x32_bf16 v[24:27], a[112:115], a[72:75], 0\n" \
"buffer_load_dwordx4 v234, s[56:59], s81 offen offset:0 sc1 lds\n" \
"v_mfma_f32_16x16x32_bf16 v[24:27], a[116:119], a[76:79], v[24:27]\n" \
"s_waitcnt lgkmcnt(4)\n" \
"ds_read_b128 a[72:75], v213 offset:256\n" \
"v_mfma_f32_16x16x32_bf16 v[40:43], a[112:115], a[80:83], 0\n" \
"s_lshl_b32 s82, s82, 5\n" \
"ds_read_b128 a[76:79], v213 offset:2304\n" \
"v_mfma_f32_16x16x32_bf16 v[148:151], v[128:131], v[112:115], v[148:151]\n" \
"v_max_f32 v172, v172, v176\n" \
"v_max_f32 v173, v173, v177\n" \
"v_max_f32 v174, v174, v178\n" \
"v_max_f32 v175, v175, v179\n" \
"v_max_f32 v164, v164, v168\n" \
"v_max_f32 v165, v165, v169\n" \
"v_max_f32 v166, v166, v170\n" \
"v_max_f32 v167, v167, v171\n" \
"v_max_f32 v156, v156, v160\n" \
"v_max_f32 v157, v157, v161\n" \
"v_max_f32 v158, v158, v162\n" \
"v_max_f32 v159, v159, v163\n" \
"v_max_f32 v148, v148, v152\n" \
"v_max_f32 v149, v149, v153\n" \
"v_max_f32 v150, v150, v154\n" \
"v_max_f32 v151, v151, v155\n" \
"v_max3_f32 v183, v172, v173, v174\n" \
"v_max3_f32 v182, v164, v165, v166\n" \
"v_max3_f32 v181, v156, v157, v158\n" \
"v_mfma_f32_16x16x32_bf16 v[40:43], a[116:119], a[84:87], v[40:43]\n" \
"s_add_u32 s82, s82, s47\n" \
"ds_read_b64_tr_b16 a[108:109], v215 offset:24576\n" \
"ds_read_b64_tr_b16 a[110:111], v215 offset:26624\n" \
"v_max3_f32 v180, v148, v149, v150\n" \
"v_mfma_f32_16x16x32_bf16 v[44:47], a[120:123], a[80:83], 0\n" \
"buffer_load_dwordx4 v234, s[56:59], s81 offen offset:1024 sc1 lds\n" \
"v_mfma_f32_16x16x32_bf16 v[44:47], a[124:127], a[84:87], v[44:47]\n" \
"s_add_u32 s48, s48, 0x1000\n" \
"s_sub_u32 s86, s86, 32768\n" \
"s_cselect_b32 s86, 131072, s86\n" \
"ds_read_b128 a[80:83], v213 offset:4096\n" \
"v_max_f32 v183, v183, v175\n" \
"v_max_f32 v182, v182, v167\n" \
"v_max_f32 v181, v181, v159\n" \
"v_mfma_f32_16x16x32_bf16 v[60:63], a[120:123], a[88:91], 0\n" \
"s_waitcnt lgkmcnt(4)\n" \
"buffer_load_dwordx4 v234, s[56:59], s81 offen offset:2048 sc1 lds\n" \
"v_max_f32 v180, v180, v151\n" \
"v_mfma_f32_16x16x32_bf16 v[60:63], a[124:127], a[92:95], v[60:63]\n" \
"ds_read_b128 a[84:87], v213 offset:6144\n" \
"v_cvt_pk_bf16_f32 v76, v92, v93\n" \
"v_cvt_pk_bf16_f32 v77, v94, v95\n" \
"v_mfma_f32_16x16x32_bf16 v[56:59], a[112:115], a[88:91], 0\n" \
"buffer_load_dwordx4 v234, s[56:59], s81 offen offset:3072 sc1 lds\n" \
"v_mfma_f32_16x16x32_bf16 v[56:59], a[116:119], a[92:95], v[56:59]\n" \
"s_getpc_b64 s[44:45]\n" \
"Lop_%=:\n" \
"s_add_u32 s44, s44, Lor_%=-Lop_%=\n" \
"s_addc_u32 s45, s45, 0\n" \
"v_permlane32_swap_b32 v180, v181\n" \
"v_permlane32_swap_b32 v182, v183\n" \
"v_max_f32 v180, v180, v181\n" \
"v_mfma_f32_16x16x32_bf16 v[0:3], a[96:99], a[64:67], v[0:3]\n" \
"ds_read_b128 a[88:91], v213 offset:4352\n" \
"v_max_f32 v182, v182, v183\n" \
"v_mfma_f32_16x16x32_bf16 v[0:3], a[100:103], a[68:71], v[0:3]\n" \
"s_waitcnt lgkmcnt(2)\n" \
"ds_read_b128 a[92:95], v213 offset:6400\n" \
"v_cvt_pk_bf16_f32 v198, v96, v97\n" \
"v_cvt_pk_bf16_f32 v199, v98, v99\n" \
"v_permlane16_swap_b32 v180, v182\n" \
"v_mfma_f32_16x16x32_bf16 v[4:7], a[104:107], a[64:67], v[4:7]\n" \
"ds_read_b64_tr_b16 a[112:113], v214 offset:16896\n" \
"v_max_f32 v180, v180, v182\n" \
"v_mfma_f32_16x16x32_bf16 v[4:7], a[108:111], a[68:71], v[4:7]\n" \
"s_add_u32 m0, m0, 0x4000\n" \
"ds_read_b64_tr_b16 a[114:115], v214 offset:18944\n" \
"v_mul_f32 v180, v180, v144\n" \
"v_cvt_pk_bf16_f32 v78, v100, v101\n" \
"v_cvt_pk_bf16_f32 v79, v102, v103\n" \
"v_mov_b32 v182, v180\n" \
"v_mfma_f32_16x16x32_bf16 v[20:23], a[104:107], a[72:75], v[20:23]\n" \
"buffer_load_dwordx4 v216, s[36:39], s82 offen offset:0 lds\n" \
"v_mfma_f32_16x16x32_bf16 v[20:23], a[108:111], a[76:79], v[20:23]\n" \
"ds_read_b64_tr_b16 a[116:117], v214 offset:25088\n" \
"v_cvt_pk_bf16_f32 v200, v104, v105\n" \
"v_cvt_pk_bf16_f32 v201, v106, v107\n" \
"v_permlane16_swap_b32 v180, v182\n" \
"v_mfma_f32_16x16x32_bf16 v[16:19], a[96:99], a[72:75], v[16:19]\n" \
"ds_read_b64_tr_b16 a[118:119], v214 offset:27136\n" \
"v_mov_b32 v181, v180\n" \
"v_mfma_f32_16x16x32_bf16 v[16:19], a[100:103], a[76:79], v[16:19]\n" \
"s_waitcnt lgkmcnt(3)\n" \
"ds_read_b64_tr_b16 a[120:121], v215 offset:16896\n" \
"v_mov_b32 v183, v182\n" \
"Lpg10165_%=:\n" \
"s_cbranch_execz Lcb1_%=\n" \
"v_cvt_pk_bf16_f32 v80, v108, v109\n" \
"v_cvt_pk_bf16_f32 v81, v110, v111\n" \
"v_permlane32_swap_b32 v180, v181\n" \
"v_mfma_f32_16x16x32_bf16 v[32:35], a[96:99], a[80:83], v[32:35]\n" \
"buffer_load_dwordx4 v217, s[36:39], s82 offen offset:1024 lds\n" \
"v_mfma_f32_16x16x32_bf16 v[32:35], a[100:103], a[84:87], v[32:35]\n" \
"s_sub_u32 s85, s85, 32768\n" \
"s_cselect_b32 s85, 131072, s85\n" \
"ds_read_b64_tr_b16 a[122:123], v215 offset:18944\n" \
"v_permlane32_swap_b32 v182, v183\n" \
"v_fma_f32 v172, v172, v144, -v183\n" \
"v_mfma_f32_16x16x32_bf16 v[36:39], a[104:107], a[80:83], v[36:39]\n" \
"ds_read_b64_tr_b16 a[124:125], v215 offset:25088\n" \
"v_add_u32 v213, s85, v211\n" \
"v_mfma_f32_16x16x32_bf16 v[36:39], a[108:111], a[84:87], v[36:39]\n" \
"ds_read_b64_tr_b16 a[126:127], v215 offset:27136\n" \
"v_fma_f32 v164, v164, v144, -v182\n" \
"v_fma_f32 v156, v156, v144, -v181\n" \
"v_fma_f32 v148, v148, v144, -v180\n" \
"v_fma_f32 v173, v173, v144, -v183\n" \
"v_mfma_f32_16x16x32_bf16 v[52:55], a[104:107], a[88:91], v[52:55]\n" \
"buffer_load_dwordx4 v218, s[36:39], s82 offen offset:2048 lds\n" \
"v_mfma_f32_16x16x32_bf16 v[52:55], a[108:111], a[92:95], v[52:55]\n" \
"v_add_u32 v214, s85, v212\n" \
"v_xor_b32 v215, 32, v214\n" \
"v_fma_f32 v165, v165, v144, -v182\n" \
"v_fma_f32 v157, v157, v144, -v181\n" \
"v_mfma_f32_16x16x32_bf16 v[48:51], a[96:99], a[88:91], v[48:51]\n" \
"buffer_load_dwordx4 v219, s[36:39], s82 offen offset:3072 lds\n" \
"v_mfma_f32_16x16x32_bf16 v[48:51], a[100:103], a[92:95], v[48:51]\n" \
"s_xor_b32 s82, s48, s49\n" \
"s_cmp_lt_u32 s48, s50\n" \
"s_cselect_b32 s82, s82, s48\n" \
"v_fma_f32 v149, v149, v144, -v180\n" \
"v_fma_f32 v174, v174, v144, -v183\n" \
"v_fma_f32 v166, v166, v144, -v182\n" \
"s_waitcnt vmcnt(16) lgkmcnt(0)\n" \
"s_barrier\n" \
"v_mfma_f32_16x16x32_bf16 v[8:11], a[112:115], a[64:67], v[8:11]\n" \
"s_add_u32 s81, s82, s46\n" \
"ds_read_b64_tr_b16 a[96:97], v214 offset:16384\n" \
"ds_read_b64_tr_b16 a[98:99], v214 offset:18432\n" \
"v_mfma_f32_16x16x32_bf16 v[8:11], a[116:119], a[68:71], v[8:11]\n" \
"s_add_u32 m0, s86, s79\n" \
"ds_read_b64_tr_b16 a[100:101], v214 offset:24576\n" \
"ds_read_b64_tr_b16 a[102:103], v214 offset:26624\n" \
"v_fma_f32 v158, v158, v144, -v181\n" \
"v_fma_f32 v150, v150, v144, -v180\n" \
"v_mfma_f32_16x16x32_bf16 v[12:15], a[120:123], a[64:67], v[12:15]\n" \
"buffer_load_dwordx4 v234, s[56:59], s81 offen offset:0 sc1 lds\n" \
"v_mfma_f32_16x16x32_bf16 v[12:15], a[124:127], a[68:71], v[12:15]\n" \
"ds_read_b128 a[64:67], v213 offset:0\n" \
"v_fma_f32 v175, v175, v144, -v183\n" \
"v_fma_f32 v167, v167, v144, -v182\n" \
"v_fma_f32 v159, v159, v144, -v181\n" \
"v_fma_f32 v151, v151, v144, -v180\n" \
"v_mfma_f32_16x16x32_bf16 v[28:31], a[120:123], a[72:75], v[28:31]\n" \
"s_lshl_b32 s82, s82, 5\n" \
"ds_read_b128 a[68:71], v213 offset:2048\n" \
"v_mfma_f32_16x16x32_bf16 v[28:31], a[124:127], a[76:79], v[28:31]\n" \
"s_add_u32 s82, s82, s47\n" \
"ds_read_b64_tr_b16 a[104:105], v215 offset:16384\n" \
"ds_read_b64_tr_b16 a[106:107], v215 offset:18432\n" \
"v_exp_f32 v172, v172\n" \
"v_mfma_f32_16x16x32_bf16 v[24:27], a[112:115], a[72:75], v[24:27]\n" \
"buffer_load_dwordx4 v234, s[56:59], s81 offen offset:1024 sc1 lds\n" \
"v_mfma_f32_16x16x32_bf16 v[24:27], a[116:119], a[76:79], v[24:27]\n" \
"ds_read_b128 a[72:75], v213 offset:256\n" \
"v_exp_f32 v164, v164\n" \
"v_exp_f32 v156, v156\n" \
"v_mfma_f32_16x16x32_bf16 v[40:43], a[112:115], a[80:83], v[40:43]\n" \
"s_add_u32 s48, s48, 0x1000\n" \
"ds_read_b128 a[76:79], v213 offset:2304\n" \
"v_mfma_f32_16x16x32_bf16 v[40:43], a[116:119], a[84:87], v[40:43]\n" \
"s_sub_u32 s86, s86, 32768\n" \
"s_cselect_b32 s86, 131072, s86\n" \
"ds_read_b64_tr_b16 a[108:109], v215 offset:24576\n" \
"ds_read_b64_tr_b16 a[110:111], v215 offset:26624\n" \
"v_exp_f32 v148, v148\n" \
"v_mfma_f32_16x16x32_bf16 v[44:47], a[120:123], a[80:83], v[44:47]\n" \
"buffer_load_dwordx4 v234, s[56:59], s81 offen offset:2048 sc1 lds\n" \
"v_mfma_f32_16x16x32_bf16 v[44:47], a[124:127], a[84:87], v[44:47]\n" \
"ds_read_b128 a[80:83], v213 offset:4096\n" \
"v_exp_f32 v173, v173\n" \
"v_exp_f32 v165, v165\n" \
"v_mfma_f32_16x16x32_bf16 v[60:63], a[120:123], a[88:91], v[60:63]\n" \
"s_waitcnt lgkmcnt(4)\n" \
"buffer_load_dwordx4 v234, s[56:59], s81 offen offset:3072 sc1 lds\n" \
"v_mfma_f32_16x16x32_bf16 v[60:63], a[124:127], a[92:95], v[60:63]\n" \
"s_add_u32 m0, m0, 0x4000\n" \
"ds_read_b128 a[84:87], v213 offset:6144\n" \
"v_exp_f32 v157, v157\n" \
"v_mfma_f32_16x16x32_bf16 v[56:59], a[112:115], a[88:91], v[56:59]\n" \
"buffer_load_dwordx4 v216, s[36:39], s82 offen offset:0 lds\n" \
"v_mfma_f32_16x16x32_bf16 v[56:59], a[116:119], a[92:95], v[56:59]\n" \
"v_exp_f32 v149, v149\n" \
"v_exp_f32 v174, v174\n" \
"v_exp_f32 v166, v166\n" \
"v_mfma_f32_16x16x32_bf16 v[0:3], a[96:99], a[64:67], v[0:3]\n" \
"ds_read_b128 a[88:91], v213 offset:4352\n" \
"v_mfma_f32_16x16x32_bf16 v[0:3], a[100:103], a[68:71], v[0:3]\n" \
"s_waitcnt lgkmcnt(2)\n" \
"ds_read_b128 a[92:95], v213 offset:6400\n" \
"v_exp_f32 v158, v158\n" \
"v_mfma_f32_16x16x32_bf16 v[4:7], a[104:107], a[64:67], v[4:7]\n" \
"ds_read_b64_tr_b16 a[112:113], v214 offset:16896\n" \
"v_mfma_f32_16x16x32_bf16 v[4:7], a[108:111], a[68:71], v[4:7]\n" \
"ds_read_b64_tr_b16 a[114:115], v214 offset:18944\n" \
"v_exp_f32 v150, v150\n" \
"v_exp_f32 v175, v175\n" \
"v_mfma_f32_16x16x32_bf16 v[20:23], a[104:107], a[72:75], v[20:23]\n" \
"buffer_load_dwordx4 v217, s[36:39], s82 offen offset:1024 lds\n" \
"v_mfma_f32_16x16x32_bf16 v[20:23], a[108:111], a[76:79], v[20:23]\n" \
"ds_read_b64_tr_b16 a[116:117], v214 offset:25088\n" \
"v_exp_f32 v167, v167\n" \
"v_exp_f32 v159, v159\n" \
"v_mfma_f32_16x16x32_bf16 v[16:19], a[96:99], a[72:75], v[16:19]\n" \
"ds_read_b64_tr_b16 a[118:119], v214 offset:27136\n" \
"v_mfma_f32_16x16x32_bf16 v[16:19], a[100:103], a[76:79], v[16:19]\n" \
"s_waitcnt lgkmcnt(3)\n" \
"ds_read_b64_tr_b16 a[120:121], v215 offset:16896\n" \
"v_exp_f32 v151, v151\n" \
"v_add_f32 v191, v172, v173\n" \
"v_add_f32 v190, v164, v165\n" \
"v_mfma_f32_16x16x32_bf16 v[32:35], a[96:99], a[80:83], v[32:35]\n" \
"buffer_load_dwordx4 v218, s[36:39], s82 offen offset:2048 lds\n" \
"v_mfma_f32_16x16x32_bf16 v[32:35], a[100:103], a[84:87], v[32:35]\n" \
"s_sub_u32 s85, s85, 32768\n" \
"s_cselect_b32 s85, 131072, s85\n" \
"ds_read_b64_tr_b16 a[122:123], v215 offset:18944\n" \
"v_add_f32 v189, v156, v157\n" \
"v_add_f32 v188, v148, v149\n" \
"v_mfma_f32_16x16x32_bf16 v[36:39], a[104:107], a[80:83], v[36:39]\n" \
"ds_read_b64_tr_b16 a[124:125], v215 offset:25088\n" \
"v_add_u32 v213, s85, v211\n" \
"v_mfma_f32_16x16x32_bf16 v[36:39], a[108:111], a[84:87], v[36:39]\n" \
"ds_read_b64_tr_b16 a[126:127], v215 offset:27136\n" \
"v_add_f32 v191, v191, v174\n" \
"v_add_f32 v190, v190, v166\n" \
"v_add_f32 v189, v189, v158\n" \
"v_add_f32 v188, v188, v150\n" \
"v_mfma_f32_16x16x32_bf16 v[52:55], a[104:107], a[88:91], v[52:55]\n" \
"buffer_load_dwordx4 v219, s[36:39], s82 offen offset:3072 lds\n" \
"v_mfma_f32_16x16x32_bf16 v[52:55], a[108:111], a[92:95], v[52:55]\n" \
"s_xor_b32 s82, s48, s49\n" \
"s_cmp_lt_u32 s48, s50\n" \
"s_cselect_b32 s82, s82, s48\n" \
"s_add_u32 s81, s82, s46\n" \
"s_add_u32 m0, s86, s79\n" \
"v_add_u32 v214, s85, v212\n" \
"v_xor_b32 v215, 32, v214\n" \
"v_add_f32 v191, v191, v175\n" \
"v_add_f32 v190, v190, v167\n" \
"v_mfma_f32_16x16x32_bf16 v[48:51], a[96:99], a[88:91], v[48:51]\n" \
"buffer_load_dwordx4 v234, s[56:59], s81 offen offset:0 sc1 lds\n" \
"v_mfma_f32_16x16x32_bf16 v[48:51], a[100:103], a[92:95], v[48:51]\n" \
"v_add_f32 v189, v189, v159\n" \
"v_add_f32 v188, v188, v151\n" \
"s_waitcnt vmcnt(17) lgkmcnt(0)\n" \
"s_barrier\n" \
"v_mfma_f32_16x16x32_bf16 v[8:11], a[112:115], a[64:67], v[8:11]\n" \
"s_lshl_b32 s82, s82, 5\n" \
"ds_read_b64_tr_b16 a[96:97], v214 offset:16384\n" \
"ds_read_b64_tr_b16 a[98:99], v214 offset:18432\n" \
"v_mfma_f32_16x16x32_bf16 v[8:11], a[116:119], a[68:71], v[8:11]\n" \
"s_add_u32 s82, s82, s47\n" \
"ds_read_b64_tr_b16 a[100:101], v214 offset:24576\n" \
"ds_read_b64_tr_b16 a[102:103], v214 offset:26624\n" \
"v_permlane32_swap_b32 v188, v189\n" \
"v_permlane32_swap_b32 v190, v191\n" \
"v_add_f32 v188, v188, v189\n" \
"v_mfma_f32_16x16x32_bf16 v[12:15], a[120:123], a[64:67], v[12:15]\n" \
"buffer_load_dwordx4 v234, s[56:59], s81 offen offset:1024 sc1 lds\n" \
"v_mfma_f32_16x16x32_bf16 v[12:15], a[124:127], a[68:71], v[12:15]\n" \
"ds_read_b128 a[64:67], v213 offset:0\n" \
"v_add_f32 v190, v190, v191\n" \
"v_mfma_f32_16x16x32_bf16 v[28:31], a[120:123], a[72:75], v[28:31]\n" \
"s_add_u32 s48, s48, 0x1000\n" \
"ds_read_b128 a[68:71], v213 offset:2048\n" \
"v_mfma_f32_16x16x32_bf16 v[28:31], a[124:127], a[76:79], v[28:31]\n" \
"s_sub_u32 s86, s86, 32768\n" \
"s_cselect_b32 s86, 131072, s86\n" \
"ds_read_b64_tr_b16 a[104:105], v215 offset:16384\n" \
"ds_read_b64_tr_b16 a[106:107], v215 offset:18432\n" \
"v_permlane16_swap_b32 v188, v190\n" \
"v_add_f32 v188, v188, v190\n" \
"v_rcp_f32 v188, v188\n" \
"v_mfma_f32_16x16x32_bf16 v[24:27], a[112:115], a[72:75], v[24:27]\n" \
"buffer_load_dwordx4 v234, s[56:59], s81 offen offset:2048 sc1 lds\n" \
"v_mfma_f32_16x16x32_bf16 v[24:27], a[116:119], a[76:79], v[24:27]\n" \
"ds_read_b128 a[72:75], v213 offset:256\n" \
"v_mov_b32 v190, v188\n" \
"v_mfma_f32_16x16x32_bf16 v[40:43], a[112:115], a[80:83], v[40:43]\n" \
"ds_read_b128 a[76:79], v213 offset:2304\n" \
"v_mfma_f32_16x16x32_bf16 v[40:43], a[116:119], a[84:87], v[40:43]\n" \
"ds_read_b64_tr_b16 a[108:109], v215 offset:24576\n" \
"ds_read_b64_tr_b16 a[110:111], v215 offset:26624\n" \
"v_permlane16_swap_b32 v188, v190\n" \
"v_mov_b32 v189, v188\n" \
"v_mov_b32 v191, v190\n" \
"v_mfma_f32_16x16x32_bf16 v[44:47], a[120:123], a[80:83], v[44:47]\n" \
"buffer_load_dwordx4 v234, s[56:59], s81 offen offset:3072 sc1 lds\n" \
"v_mfma_f32_16x16x32_bf16 v[44:47], a[124:127], a[84:87], v[44:47]\n" \
"s_add_u32 m0, m0, 0x4000\n" \
"ds_read_b128 a[80:83], v213 offset:4096\n" \
"v_permlane32_swap_b32 v188, v189\n" \
"v_permlane32_swap_b32 v190, v191\n" \
"v_mul_f32 v172, v172, v191\n" \
"v_mul_f32 v164, v164, v190\n" \
"v_mul_f32 v156, v156, v189\n" \
"v_mul_f32 v148, v148, v188\n" \
"v_mul_f32 v173, v173, v191\n" \
"v_mfma_f32_16x16x32_bf16 v[60:63], a[120:123], a[88:91], v[60:63]\n" \
"s_waitcnt lgkmcnt(4)\n" \
"buffer_load_dwordx4 v216, s[36:39], s82 offen offset:0 lds\n" \
"v_mul_f32 v165, v165, v190\n" \
"v_mfma_f32_16x16x32_bf16 v[60:63], a[124:127], a[92:95], v[60:63]\n" \
"ds_read_b128 a[84:87], v213 offset:6144\n" \
"v_mul_f32 v157, v157, v189\n" \
"v_mul_f32 v149, v149, v188\n" \
"v_mfma_f32_16x16x32_bf16 v[56:59], a[112:115], a[88:91], v[56:59]\n" \
"buffer_load_dwordx4 v217, s[36:39], s82 offen offset:1024 lds\n" \
"v_mfma_f32_16x16x32_bf16 v[56:59], a[116:119], a[92:95], v[56:59]\n" \
"v_mul_f32 v174, v174, v191\n" \
"v_mul_f32 v166, v166, v190\n" \
"v_mul_f32 v158, v158, v189\n" \
"v_mul_f32 v150, v150, v188\n" \
"v_mul_f32 v175, v175, v191\n" \
"v_mfma_f32_16x16x32_bf16 v[0:3], a[96:99], a[64:67], v[0:3]\n" \
"ds_read_b128 a[88:91], v213 offset:4352\n" \
"v_mul_f32 v167, v167, v190\n" \
"v_mfma_f32_16x16x32_bf16 v[0:3], a[100:103], a[68:71], v[0:3]\n" \
"s_waitcnt lgkmcnt(2)\n" \
"ds_read_b128 a[92:95], v213 offset:6400\n" \
"v_mul_f32 v159, v159, v189\n" \
"v_mul_f32 v151, v151, v188\n" \
"v_cvt_pk_bf16_f32 v178, v172, v173\n" \
"v_mfma_f32_16x16x32_bf16 v[4:7], a[104:107], a[64:67], v[4:7]\n" \
"ds_read_b64_tr_b16 a[112:113], v214 offset:16896\n" \
"v_cvt_pk_bf16_f32 v179, v174, v175\n" \
"v_mfma_f32_16x16x32_bf16 v[4:7], a[108:111], a[68:71], v[4:7]\n" \
"ds_read_b64_tr_b16 a[114:115], v214 offset:18944\n" \
"v_cvt_pk_bf16_f32 v170, v164, v165\n" \
"v_cvt_pk_bf16_f32 v171, v166, v167\n" \
"v_cvt_pk_bf16_f32 v162, v156, v157\n" \
"v_cvt_pk_bf16_f32 v163, v158, v159\n" \
"v_mfma_f32_16x16x32_bf16 v[20:23], a[104:107], a[72:75], v[20:23]\n" \
"buffer_load_dwordx4 v218, s[36:39], s82 offen offset:2048 lds\n" \
"v_mfma_f32_16x16x32_bf16 v[20:23], a[108:111], a[76:79], v[20:23]\n" \
"ds_read_b64_tr_b16 a[116:117], v214 offset:25088\n" \
"v_cvt_pk_bf16_f32 v154, v148, v149\n" \
"v_cvt_pk_bf16_f32 v155, v150, v151\n" \
"v_and_b32 v176, v178, v184\n" \
"v_mfma_f32_16x16x32_bf16 v[16:19], a[96:99], a[72:75], v[16:19]\n" \
"ds_read_b64_tr_b16 a[118:119], v214 offset:27136\n" \
"v_and_b32 v177, v179, v185\n" \
"v_mfma_f32_16x16x32_bf16 v[16:19], a[100:103], a[76:79], v[16:19]\n" \
"s_waitcnt lgkmcnt(3)\n" \
"ds_read_b64_tr_b16 a[120:121], v215 offset:16896\n" \
"v_xor_b32 v178, v178, v176\n" \
"v_xor_b32 v179, v179, v177\n" \
"v_and_b32 v168, v170, v184\n" \
"v_and_b32 v169, v171, v185\n" \
"v_mfma_f32_16x16x32_bf16 v[32:35], a[96:99], a[80:83], v[32:35]\n" \
"buffer_load_dwordx4 v219, s[36:39], s82 offen offset:3072 lds\n" \
"v_mfma_f32_16x16x32_bf16 v[32:35], a[100:103], a[84:87], v[32:35]\n" \
"s_sub_u32 s85, s85, 32768\n" \
"s_cselect_b32 s85, 131072, s85\n" \
"s_xor_b32 s82, s48, s49\n" \
"s_cmp_lt_u32 s48, s50\n" \
"s_cselect_b32 s82, s82, s48\n" \
"ds_read_b64_tr_b16 a[122:123], v215 offset:18944\n" \
"v_xor_b32 v170, v170, v168\n" \
"v_xor_b32 v171, v171, v169\n" \
"v_mfma_f32_16x16x32_bf16 v[36:39], a[104:107], a[80:83], v[36:39]\n" \
"s_add_u32 s81, s82, s46\n" \
"ds_read_b64_tr_b16 a[124:125], v215 offset:25088\n" \
"v_add_u32 v213, s85, v211\n" \
"v_mfma_f32_16x16x32_bf16 v[36:39], a[108:111], a[84:87], v[36:39]\n" \
"s_add_u32 m0, s86, s79\n" \
"ds_read_b64_tr_b16 a[126:127], v215 offset:27136\n" \
"v_and_b32 v160, v162, v184\n" \
"v_and_b32 v161, v163, v185\n" \
"v_xor_b32 v162, v162, v160\n" \
"v_xor_b32 v163, v163, v161\n" \
"v_mfma_f32_16x16x32_bf16 v[52:55], a[104:107], a[88:91], v[52:55]\n" \
"buffer_load_dwordx4 v234, s[56:59], s81 offen offset:0 sc1 lds\n" \
"v_mfma_f32_16x16x32_bf16 v[52:55], a[108:111], a[92:95], v[52:55]\n" \
"s_lshl_b32 s82, s82, 5\n" \
"s_add_u32 s82, s82, s47\n" \
"v_add_u32 v214, s85, v212\n" \
"v_xor_b32 v215, 32, v214\n" \
"v_and_b32 v152, v154, v184\n" \
"v_and_b32 v153, v155, v185\n" \
"v_mfma_f32_16x16x32_bf16 v[48:51], a[96:99], a[88:91], v[48:51]\n" \
"buffer_load_dwordx4 v234, s[56:59], s81 offen offset:1024 sc1 lds\n" \
"v_mfma_f32_16x16x32_bf16 v[48:51], a[100:103], a[92:95], v[48:51]\n" \
"v_xor_b32 v154, v154, v152\n" \
"v_xor_b32 v155, v155, v153\n" \
"s_waitcnt vmcnt(18) lgkmcnt(0)\n" \
"s_barrier\n" \
"v_mfma_f32_16x16x32_bf16 v[8:11], a[112:115], a[64:67], v[8:11]\n" \
"s_add_u32 s48, s48, 0x1000\n" \
"ds_read_b64_tr_b16 a[96:97], v214 offset:16384\n" \
"ds_read_b64_tr_b16 a[98:99], v214 offset:18432\n" \
"v_mfma_f32_16x16x32_bf16 v[136:139], v[198:201], v[176:179], 0\n" \
"v_mfma_f32_16x16x32_bf16 v[140:143], v[78:81], v[176:179], 0\n" \
"v_mfma_f32_16x16x32_bf16 v[128:131], v[196:199], v[168:171], 0\n" \
"v_mfma_f32_16x16x32_bf16 v[132:135], v[76:79], v[168:171], 0\n" \
"v_mfma_f32_16x16x32_bf16 v[120:123], v[194:197], v[160:163], 0\n" \
"v_mfma_f32_16x16x32_bf16 v[124:127], v[74:77], v[160:163], 0\n" \
"v_mfma_f32_16x16x32_bf16 v[112:115], v[192:195], v[152:155], 0\n" \
"v_mfma_f32_16x16x32_bf16 v[116:119], v[72:75], v[152:155], 0\n" \
"v_mfma_f32_16x16x32_bf16 v[8:11], a[116:119], a[68:71], v[8:11]\n" \
"s_sub_u32 s86, s86, 32768\n" \
"s_cselect_b32 s86, 131072, s86\n" \
"ds_read_b64_tr_b16 a[100:101], v214 offset:24576\n" \
"ds_read_b64_tr_b16 a[102:103], v214 offset:26624\n" \
"v_mfma_f32_16x16x32_bf16 v[12:15], a[120:123], a[64:67], v[12:15]\n" \
"buffer_load_dwordx4 v234, s[56:59], s81 offen offset:2048 sc1 lds\n" \
"v_mfma_f32_16x16x32_bf16 v[12:15], a[124:127], a[68:71], v[12:15]\n" \
"ds_read_b128 a[64:67], v213 offset:0\n" \
"v_cvt_pk_bf16_f32 v136, v136, v137\n" \
"v_cvt_pk_bf16_f32 v137, v138, v139\n" \
"v_cvt_pk_bf16_f32 v138, v140, v141\n" \
"v_cvt_pk_bf16_f32 v139, v142, v143\n" \
"v_cndmask_b32 v140, v136, v138, s[76:77]\n" \
"v_mfma_f32_16x16x32_bf16 v[28:31], a[120:123], a[72:75], v[28:31]\n" \
"ds_read_b128 a[68:71], v213 offset:2048\n" \
"v_cndmask_b32 v141, v137, v139, s[76:77]\n" \
"v_mfma_f32_16x16x32_bf16 v[28:31], a[124:127], a[76:79], v[28:31]\n" \
"ds_read_b64_tr_b16 a[104:105], v215 offset:16384\n" \
"ds_read_b64_tr_b16 a[106:107], v215 offset:18432\n" \
"v_cndmask_b32 v142, v138, v136, s[76:77]\n" \
"v_cndmask_b32 v143, v139, v137, s[76:77]\n" \
"v_mfma_f32_16x16x32_bf16 v[24:27], a[112:115], a[72:75], v[24:27]\n" \
"buffer_load_dwordx4 v234, s[56:59], s81 offen offset:3072 sc1 lds\n" \
"v_mfma_f32_16x16x32_bf16 v[24:27], a[116:119], a[76:79], v[24:27]\n" \
"ds_read_b128 a[72:75], v213 offset:256\n" \
"v_cvt_pk_bf16_f32 v128, v128, v129\n" \
"v_cvt_pk_bf16_f32 v129, v130, v131\n" \
"v_cvt_pk_bf16_f32 v130, v132, v133\n" \
"v_cvt_pk_bf16_f32 v131, v134, v135\n" \
"v_mfma_f32_16x16x32_bf16 v[40:43], a[112:115], a[80:83], v[40:43]\n" \
"ds_read_b128 a[76:79], v213 offset:2304\n" \
"v_cndmask_b32 v132, v128, v130, s[76:77]\n" \
"v_mfma_f32_16x16x32_bf16 v[40:43], a[116:119], a[84:87], v[40:43]\n" \
"s_add_u32 m0, m0, 0x4000\n" \
"ds_read_b64_tr_b16 a[108:109], v215 offset:24576\n" \
"ds_read_b64_tr_b16 a[110:111], v215 offset:26624\n" \
"v_cndmask_b32 v133, v129, v131, s[76:77]\n" \
"v_cndmask_b32 v134, v130, v128, s[76:77]\n" \
"v_mfma_f32_16x16x32_bf16 v[44:47], a[120:123], a[80:83], v[44:47]\n" \
"buffer_load_dwordx4 v216, s[36:39], s82 offen offset:0 lds\n" \
"v_mfma_f32_16x16x32_bf16 v[44:47], a[124:127], a[84:87], v[44:47]\n" \
"ds_read_b128 a[80:83], v213 offset:4096\n" \
"v_cndmask_b32 v135, v131, v129, s[76:77]\n" \
"v_cvt_pk_bf16_f32 v120, v120, v121\n" \
"v_cvt_pk_bf16_f32 v121, v122, v123\n" \
"v_cvt_pk_bf16_f32 v122, v124, v125\n" \
"v_mfma_f32_16x16x32_bf16 v[60:63], a[120:123], a[88:91], v[60:63]\n" \
"s_waitcnt lgkmcnt(4)\n" \
"buffer_load_dwordx4 v217, s[36:39], s82 offen offset:1024 lds\n" \
"v_cvt_pk_bf16_f32 v123, v126, v127\n" \
"v_mfma_f32_16x16x32_bf16 v[60:63], a[124:127], a[92:95], v[60:63]\n" \
"ds_read_b128 a[84:87], v213 offset:6144\n" \
"v_cndmask_b32 v124, v120, v122, s[76:77]\n" \
"v_cndmask_b32 v125, v121, v123, s[76:77]\n" \
"v_mfma_f32_16x16x32_bf16 v[56:59], a[112:115], a[88:91], v[56:59]\n" \
"buffer_load_dwordx4 v218, s[36:39], s82 offen offset:2048 lds\n" \
"v_mfma_f32_16x16x32_bf16 v[56:59], a[116:119], a[92:95], v[56:59]\n" \
"v_cndmask_b32 v126, v122, v120, s[76:77]\n" \
"v_cndmask_b32 v127, v123, v121, s[76:77]\n" \
"v_cvt_pk_bf16_f32 v112, v112, v113\n" \
"v_cvt_pk_bf16_f32 v113, v114, v115\n" \
"v_cvt_pk_bf16_f32 v114, v116, v117\n" \
"v_mfma_f32_16x16x32_bf16 v[0:3], a[96:99], a[64:67], v[0:3]\n" \
"ds_read_b128 a[88:91], v213 offset:4352\n" \
"v_cvt_pk_bf16_f32 v115, v118, v119\n" \
"v_mfma_f32_16x16x32_bf16 v[0:3], a[100:103], a[68:71], v[0:3]\n" \
"s_waitcnt lgkmcnt(2)\n" \
"ds_read_b128 a[92:95], v213 offset:6400\n" \
"v_cndmask_b32 v116, v112, v114, s[76:77]\n" \
"v_cndmask_b32 v117, v113, v115, s[76:77]\n" \
"v_cndmask_b32 v118, v114, v112, s[76:77]\n" \
"v_mfma_f32_16x16x32_bf16 v[4:7], a[104:107], a[64:67], v[4:7]\n" \
"ds_read_b64_tr_b16 a[112:113], v214 offset:16896\n" \
"v_cndmask_b32 v119, v115, v113, s[76:77]\n" \
"v_mfma_f32_16x16x32_bf16 v[4:7], a[108:111], a[68:71], v[4:7]\n" \
"s_add_u32 s71, s93, 208896\n" \
"ds_read_b64_tr_b16 a[114:115], v214 offset:18944\n" \
"buffer_store_dwordx4 v[140:143], v236, s[56:59], s71 offen sc1\n" \
"s_add_u32 s71, s93, 139264\n" \
"v_mfma_f32_16x16x32_bf16 v[20:23], a[104:107], a[72:75], v[20:23]\n" \
"buffer_load_dwordx4 v219, s[36:39], s82 offen offset:3072 lds\n" \
"v_mfma_f32_16x16x32_bf16 v[20:23], a[108:111], a[76:79], v[20:23]\n" \
"s_xor_b32 s82, s48, s49\n" \
"s_cmp_lt_u32 s48, s50\n" \
"s_cselect_b32 s82, s82, s48\n" \
"ds_read_b64_tr_b16 a[116:117], v214 offset:25088\n" \
"buffer_store_dwordx4 v[132:135], v236, s[56:59], s71 offen sc1\n" \
"s_add_u32 s71, s93, 69632\n" \
"v_mfma_f32_16x16x32_bf16 v[16:19], a[96:99], a[72:75], v[16:19]\n" \
"s_waitcnt lgkmcnt(3)\n" \
"s_add_u32 s81, s82, s46\n" \
"ds_read_b64_tr_b16 a[118:119], v214 offset:27136\n" \
"v_mfma_f32_16x16x32_bf16 v[16:19], a[100:103], a[76:79], v[16:19]\n" \
"s_add_u32 m0, s86, s79\n" \
"ds_read_b64_tr_b16 a[120:121], v215 offset:16896\n" \
"buffer_store_dwordx4 v[124:127], v236, s[56:59], s71 offen sc1\n" \
"s_add_u32 s71, s93, 0\n" \
"v_mfma_f32_16x16x32_bf16 v[32:35], a[96:99], a[80:83], v[32:35]\n" \
"buffer_load_dwordx4 v234, s[56:59], s81 offen offset:0 sc1 lds\n" \
"v_mfma_f32_16x16x32_bf16 v[32:35], a[100:103], a[84:87], v[32:35]\n" \
"s_sub_u32 s85, s85, 32768\n" \
"s_cselect_b32 s85, 131072, s85\n" \
"ds_read_b64_tr_b16 a[122:123], v215 offset:18944\n" \
"buffer_store_dwordx4 v[116:119], v236, s[56:59], s71 offen sc1\n" \
"v_mfma_f32_16x16x32_bf16 v[36:39], a[104:107], a[80:83], v[36:39]\n" \
"s_lshl_b32 s82, s82, 5\n" \
"ds_read_b64_tr_b16 a[124:125], v215 offset:25088\n" \
"v_add_u32 v213, s85, v211\n" \
"v_mfma_f32_16x16x32_bf16 v[36:39], a[108:111], a[84:87], v[36:39]\n" \
"s_add_u32 s82, s82, s47\n" \
"ds_read_b64_tr_b16 a[126:127], v215 offset:27136\n" \
"v_mfma_f32_16x16x32_bf16 v[52:55], a[104:107], a[88:91], v[52:55]\n" \
"buffer_load_dwordx4 v234, s[56:59], s81 offen offset:1024 sc1 lds\n" \
"v_mfma_f32_16x16x32_bf16 v[52:55], a[108:111], a[92:95], v[52:55]\n" \
"s_add_u32 s48, s48, 0x1000\n" \
"s_sub_u32 s86, s86, 32768\n" \
"s_cselect_b32 s86, 131072, s86\n" \
"v_add_u32 v214, s85, v212\n" \
"v_xor_b32 v215, 32, v214\n" \
"v_mfma_f32_16x16x32_bf16 v[48:51], a[96:99], a[88:91], v[48:51]\n" \
"buffer_load_dwordx4 v234, s[56:59], s81 offen offset:2048 sc1 lds\n" \
"v_mfma_f32_16x16x32_bf16 v[48:51], a[100:103], a[92:95], v[48:51]\n" \
"s_waitcnt vmcnt(23) lgkmcnt(0)\n" \
"s_barrier\n" \
"v_mfma_f32_16x16x32_bf16 v[8:11], a[112:115], a[64:67], v[8:11]\n" \
"ds_read_b64_tr_b16 a[96:97], v214 offset:16384\n" \
"ds_read_b64_tr_b16 a[98:99], v214 offset:18432\n" \
"v_mfma_f32_16x16x32_bf16 v[8:11], a[116:119], a[68:71], v[8:11]\n" \
"ds_read_b64_tr_b16 a[100:101], v214 offset:24576\n" \
"ds_read_b64_tr_b16 a[102:103], v214 offset:26624\n" \
"v_mfma_f32_16x16x32_bf16 v[12:15], a[120:123], a[64:67], v[12:15]\n" \
"buffer_load_dwordx4 v234, s[56:59], s81 offen offset:3072 sc1 lds\n" \
"v_mfma_f32_16x16x32_bf16 v[12:15], a[124:127], a[68:71], v[12:15]\n" \
"ds_read_b128 a[64:67], v213 offset:0\n" \
"v_mfma_f32_16x16x32_bf16 v[28:31], a[120:123], a[72:75], v[28:31]\n" \
"ds_read_b128 a[68:71], v213 offset:2048\n" \
"v_mfma_f32_16x16x32_bf16 v[28:31], a[124:127], a[76:79], v[28:31]\n" \
"s_add_u32 m0, m0, 0x4000\n" \
"ds_read_b64_tr_b16 a[104:105], v215 offset:16384\n" \
"ds_read_b64_tr_b16 a[106:107], v215 offset:18432\n" \
"v_mfma_f32_16x16x32_bf16 v[24:27], a[112:115], a[72:75], v[24:27]\n" \
"buffer_load_dwordx4 v216, s[36:39], s82 offen offset:0 lds\n" \
"v_mfma_f32_16x16x32_bf16 v[24:27], a[116:119], a[76:79], v[24:27]\n" \
"ds_read_b128 a[72:75], v213 offset:256\n" \
"v_mfma_f32_16x16x32_bf16 v[40:43], a[112:115], a[80:83], v[40:43]\n" \
"ds_read_b128 a[76:79], v213 offset:2304\n" \
"v_mfma_f32_16x16x32_bf16 v[40:43], a[116:119], a[84:87], v[40:43]\n" \
"ds_read_b64_tr_b16 a[108:109], v215 offset:24576\n" \
"ds_read_b64_tr_b16 a[110:111], v215 offset:26624\n" \
"v_mfma_f32_16x16x32_bf16 v[44:47], a[120:123], a[80:83], v[44:47]\n" \
"buffer_load_dwordx4 v217, s[36:39], s82 offen offset:1024 lds\n" \
"v_mfma_f32_16x16x32_bf16 v[44:47], a[124:127], a[84:87], v[44:47]\n" \
"ds_read_b128 a[80:83], v213 offset:4096\n" \
"v_mfma_f32_16x16x32_bf16 v[60:63], a[120:123], a[88:91], v[60:63]\n" \
"s_waitcnt lgkmcnt(4)\n" \
"buffer_load_dwordx4 v218, s[36:39], s82 offen offset:2048 lds\n" \
"v_mfma_f32_16x16x32_bf16 v[60:63], a[124:127], a[92:95], v[60:63]\n" \
"ds_read_b128 a[84:87], v213 offset:6144\n" \
"v_mfma_f32_16x16x32_bf16 v[56:59], a[112:115], a[88:91], v[56:59]\n" \
"buffer_load_dwordx4 v219, s[36:39], s82 offen offset:3072 lds\n" \
"v_mfma_f32_16x16x32_bf16 v[56:59], a[116:119], a[92:95], v[56:59]\n" \
"s_mov_b32 s72, 2\n" \
"Lo0_%=:\n" \
"v_mfma_f32_16x16x32_bf16 v[0:3], a[96:99], a[64:67], v[0:3]\n" \
"s_waitcnt lgkmcnt(1)\n" \
"s_xor_b32 s82, s48, s49\n" \
"ds_read_b128 a[88:91], v213 offset:4352\n" \
"v_mfma_f32_16x16x32_bf16 v[0:3], a[100:103], a[68:71], v[0:3]\n" \
"s_cmp_lt_u32 s48, s50\n" \
"s_cselect_b32 s82, s82, s48\n" \
"ds_read_b128 a[92:95], v213 offset:6400\n" \
"v_mfma_f32_16x16x32_bf16 v[4:7], a[104:107], a[64:67], v[4:7]\n" \
"s_add_u32 s81, s82, s46\n" \
"ds_read_b64_tr_b16 a[112:113], v214 offset:16896\n" \
"v_mfma_f32_16x16x32_bf16 v[4:7], a[108:111], a[68:71], v[4:7]\n" \
"s_add_u32 m0, s86, s79\n" \
"ds_read_b64_tr_b16 a[114:115], v214 offset:18944\n" \
"v_mfma_f32_16x16x32_bf16 v[20:23], a[104:107], a[72:75], v[20:23]\n" \
"buffer_load_dwordx4 v234, s[56:59], s81 offen offset:0 sc1 lds\n" \
"v_mfma_f32_16x16x32_bf16 v[20:23], a[108:111], a[76:79], v[20:23]\n" \
"ds_read_b64_tr_b16 a[116:117], v214 offset:25088\n" \
"v_mfma_f32_16x16x32_bf16 v[16:19], a[96:99], a[72:75], v[16:19]\n" \
"s_lshl_b32 s82, s82, 5\n" \
"ds_read_b64_tr_b16 a[118:119], v214 offset:27136\n" \
"v_mfma_f32_16x16x32_bf16 v[16:19], a[100:103], a[76:79], v[16:19]\n" \
"s_waitcnt lgkmcnt(3)\n" \
"s_add_u32 s82, s82, s47\n" \
"ds_read_b64_tr_b16 a[120:121], v215 offset:16896\n" \
"v_mfma_f32_16x16x32_bf16 v[32:35], a[96:99], a[80:83], v[32:35]\n" \
"buffer_load_dwordx4 v234, s[56:59], s81 offen offset:1024 sc1 lds\n" \
"v_mfma_f32_16x16x32_bf16 v[32:35], a[100:103], a[84:87], v[32:35]\n" \
"s_sub_u32 s85, s85, 32768\n" \
"s_cselect_b32 s85, 131072, s85\n" \
"ds_read_b64_tr_b16 a[122:123], v215 offset:18944\n" \
"v_mfma_f32_16x16x32_bf16 v[36:39], a[104:107], a[80:83], v[36:39]\n" \
"s_add_u32 s48, s48, 0x1000\n" \
"ds_read_b64_tr_b16 a[124:125], v215 offset:25088\n" \
"v_add_u32 v213, s85, v211\n" \
"v_mfma_f32_16x16x32_bf16 v[36:39], a[108:111], a[84:87], v[36:39]\n" \
"s_sub_u32 s86, s86, 32768\n" \
"s_cselect_b32 s86, 131072, s86\n" \
"ds_read_b64_tr_b16 a[126:127], v215 offset:27136\n" \
"v_mfma_f32_16x16x32_bf16 v[52:55], a[104:107], a[88:91], v[52:55]\n" \
"buffer_load_dwordx4 v234, s[56:59], s81 offen offset:2048 sc1 lds\n" \
"v_mfma_f32_16x16x32_bf16 v[52:55], a[108:111], a[92:95], v[52:55]\n" \
"v_add_u32 v214, s85, v212\n" \
"v_xor_b32 v215, 32, v214\n" \
"v_mfma_f32_16x16x32_bf16 v[48:51], a[96:99], a[88:91], v[48:51]\n" \
"buffer_load_dwordx4 v234, s[56:59], s81 offen offset:3072 sc1 lds\n" \
"v_mfma_f32_16x16x32_bf16 v[48:51], a[100:103], a[92:95], v[48:51]\n" \
"s_waitcnt vmcnt(20) lgkmcnt(0)\n" \
"s_barrier\n" \
"v_mfma_f32_16x16x32_bf16 v[8:11], a[112:115], a[64:67], v[8:11]\n" \
"ds_read_b64_tr_b16 a[96:97], v214 offset:16384\n" \
"ds_read_b64_tr_b16 a[98:99], v214 offset:18432\n" \
"v_mfma_f32_16x16x32_bf16 v[8:11], a[116:119], a[68:71], v[8:11]\n" \
"s_add_u32 m0, m0, 0x4000\n" \
"ds_read_b64_tr_b16 a[100:101], v214 offset:24576\n" \
"ds_read_b64_tr_b16 a[102:103], v214 offset:26624\n" \
"v_mfma_f32_16x16x32_bf16 v[12:15], a[120:123], a[64:67], v[12:15]\n" \
"buffer_load_dwordx4 v216, s[36:39], s82 offen offset:0 lds\n" \
"v_mfma_f32_16x16x32_bf16 v[12:15], a[124:127], a[68:71], v[12:15]\n" \
"ds_read_b128 a[64:67], v213 offset:0\n" \
"v_mfma_f32_16x16x32_bf16 v[28:31], a[120:123], a[72:75], v[28:31]\n" \
"ds_read_b128 a[68:71], v213 offset:2048\n" \
"v_mfma_f32_16x16x32_bf16 v[28:31], a[124:127], a[76:79], v[28:31]\n" \
"ds_read_b64_tr_b16 a[104:105], v215 offset:16384\n" \
"ds_read_b64_tr_b16 a[106:107], v215 offset:18432\n" \
"v_mfma_f32_16x16x32_bf16 v[24:27], a[112:115], a[72:75], v[24:27]\n" \
"buffer_load_dwordx4 v217, s[36:39], s82 offen offset:1024 lds\n" \
"v_mfma_f32_16x16x32_bf16 v[24:27], a[116:119], a[76:79], v[24:27]\n" \
"ds_read_b128 a[72:75], v213 offset:256\n" \
"v_mfma_f32_16x16x32_bf16 v[40:43], a[112:115], a[80:83], v[40:43]\n" \
"ds_read_b128 a[76:79], v213 offset:2304\n" \
"v_mfma_f32_16x16x32_bf16 v[40:43], a[116:119], a[84:87], v[40:43]\n" \
"ds_read_b64_tr_b16 a[108:109], v215 offset:24576\n" \
"ds_read_b64_tr_b16 a[110:111], v215 offset:26624\n" \
"v_mfma_f32_16x16x32_bf16 v[44:47], a[120:123], a[80:83], v[44:47]\n" \
"buffer_load_dwordx4 v218, s[36:39], s82 offen offset:2048 lds\n" \
"v_mfma_f32_16x16x32_bf16 v[44:47], a[124:127], a[84:87], v[44:47]\n" \
"ds_read_b128 a[80:83], v213 offset:4096\n" \
"v_mfma_f32_16x16x32_bf16 v[60:63], a[120:123], a[88:91], v[60:63]\n" \
"s_waitcnt lgkmcnt(4)\n" \
"v_mfma_f32_16x16x32_bf16 v[60:63], a[124:127], a[92:95], v[60:63]\n" \
"s_sub_u32 s72, s72, 1\n" \
"ds_read_b128 a[84:87], v213 offset:6144\n" \
"v_mfma_f32_16x16x32_bf16 v[56:59], a[112:115], a[88:91], v[56:59]\n" \
"buffer_load_dwordx4 v219, s[36:39], s82 offen offset:3072 lds\n" \
"v_mfma_f32_16x16x32_bf16 v[56:59], a[116:119], a[92:95], v[56:59]\n" \
"s_cbranch_scc0 Lo0_%=\n" \
"s_setpc_b64 s[44:45]\n" \
"Lor_%=:\n" \
"buffer_store_dword v230, v231, s[56:59], s12 offen sc1\n" \
"s_mov_b32 s72, s10\n" \
"s_call_b64 s[44:45], Lo0_%=\n" \
"buffer_load_dword v220, v221, s[56:59], s67 offen sc1\n" \
"s_mov_b32 s72, 1\n" \
"s_call_b64 s[44:45], Lo0_%=\n" \
"s_waitcnt vmcnt(16)\n" \
"v_cmp_eq_u32 vcc, s60, v220\n" \
"s_nop 1\n" \
"s_andn2_b32 s70, s74, vcc_lo\n" \
"s_cbranch_scc1 Lpl6_%=\n" \
"Lpr6_%=:\n" \
"s_cmp_eq_u32 s90, 0\n" \
"s_cselect_b32 s46, s46, s83\n" \
"s_cselect_b32 s47, s47, s84\n" \
"s_cselect_b32 s48, s48, 0\n" \
"s_cselect_b64 s[36:37], s[36:37], s[40:41]\n" \
"s_cselect_b32 s38, s38, s42\n" \
"s_mov_b32 s72, 3\n" \
"s_call_b64 s[44:45], Lo0_%=\n" \
"s_cmp_eq_u32 s90, 0\n" \
"s_cbranch_scc0 Lrole1_%=\n" \
"s_mov_b32 s72, s11\n" \
"s_call_b64 s[44:45], Lo0_%=\n" \
"buffer_load_dword v220, v241, s[56:59], s64 offen sc1\n" \
"s_mov_b32 s72, 1\n" \
"s_call_b64 s[44:45], Lo0_%=\n" \
"v_mfma_f32_16x16x32_bf16 v[0:3], a[96:99], a[64:67], v[0:3]\n" \
"s_waitcnt lgkmcnt(1)\n" \
"ds_read_b128 a[88:91], v213 offset:4352\n" \
"v_mov_b32 v74, 0x3f80\n" \
"v_mfma_f32_16x16x32_bf16 v[0:3], a[100:103], a[68:71], v[0:3]\n" \
"s_add_u32 s48, s86, 32768\n" \
"s_cmp_eq_u32 s48, 163840\n" \
"s_cselect_b32 s48, 0, s48\n" \
"ds_read_b128 a[92:95], v213 offset:6400\n" \
"v_mfma_f32_16x16x32_bf16 v[4:7], a[104:107], a[64:67], v[4:7]\n" \
"ds_read_b64_tr_b16 a[112:113], v214 offset:16896\n" \
"Lpg10770_%=:\n" \
"s_cbranch_execz Lcb1_%=\n" \
"v_mov_b32 v64, 0\n" \
"v_mfma_f32_16x16x32_bf16 v[4:7], a[108:111], a[68:71], v[4:7]\n" \
"ds_read_b64_tr_b16 a[114:115], v214 offset:18944\n" \
"v_mov_b32 v65, 0\n" \
"v_mfma_f32_16x16x32_bf16 v[20:23], a[104:107], a[72:75], v[20:23]\n" \
"s_add_u32 s49, s48, 32768\n" \
"s_cmp_eq_u32 s49, 163840\n" \
"s_cselect_b32 s49, 0, s49\n" \
"v_mfma_f32_16x16x32_bf16 v[20:23], a[108:111], a[76:79], v[20:23]\n" \
"ds_read_b64_tr_b16 a[116:117], v214 offset:25088\n" \
"v_add_u32 v224, s49, v211\n" \
"v_mfma_f32_16x16x32_bf16 v[16:19], a[96:99], a[72:75], v[16:19]\n" \
"ds_read_b64_tr_b16 a[118:119], v214 offset:27136\n" \
"v_add_u32 v225, s49, v212\n" \
"v_mfma_f32_16x16x32_bf16 v[16:19], a[100:103], a[76:79], v[16:19]\n" \
"s_waitcnt lgkmcnt(3)\n" \
"ds_read_b64_tr_b16 a[120:121], v215 offset:16896\n" \
"v_xor_b32 v226, 32, v225\n" \
"v_mfma_f32_16x16x32_bf16 v[32:35], a[96:99], a[80:83], v[32:35]\n" \
"v_add_u32 v227, s48, v211\n" \
"v_add_u32 v228, s48, v212\n" \
"v_mfma_f32_16x16x32_bf16 v[32:35], a[100:103], a[84:87], v[32:35]\n" \
"s_sub_u32 s85, s85, 32768\n" \
"s_cselect_b32 s85, 131072, s85\n" \
"ds_read_b64_tr_b16 a[122:123], v215 offset:18944\n" \
"v_mfma_f32_16x16x32_bf16 v[36:39], a[104:107], a[80:83], v[36:39]\n" \
"ds_read_b64_tr_b16 a[124:125], v215 offset:25088\n" \
"v_add_u32 v213, s85, v211\n" \
"v_mfma_f32_16x16x32_bf16 v[36:39], a[108:111], a[84:87], v[36:39]\n" \
"ds_read_b64_tr_b16 a[126:127], v215 offset:27136\n" \
"v_xor_b32 v229, 32, v228\n" \
"v_mfma_f32_16x16x32_bf16 v[52:55], a[104:107], a[88:91], v[52:55]\n" \
"v_mov_b32 v68, 0\n" \
"v_mov_b32 v69, 0\n" \
"v_mfma_f32_16x16x32_bf16 v[52:55], a[108:111], a[92:95], v[52:55]\n" \
"v_add_u32 v214, s85, v212\n" \
"v_xor_b32 v215, 32, v214\n" \
"v_mfma_f32_16x16x32_bf16 v[48:51], a[96:99], a[88:91], v[48:51]\n" \
"v_lshrrev_b32 v72, 8, v234\n" \
"v_bfe_u32 v73, v234, 6, 2\n" \
"v_mfma_f32_16x16x32_bf16 v[48:51], a[100:103], a[92:95], v[48:51]\n" \
"s_waitcnt vmcnt(8) lgkmcnt(0)\n" \
"s_barrier\n" \
"s_waitcnt vmcnt(16)\n" \
"v_cmp_eq_u32 vcc, s60, v220\n" \
"s_nop 1\n" \
"s_andn2_b32 s70, s69, vcc_lo\n" \
"s_cbranch_scc1 Lpl7_%=\n" \
"Lpr7_%=:\n" \
"v_mfma_f32_16x16x32_bf16 v[8:11], a[112:115], a[64:67], v[8:11]\n" \
"s_add_u32 s71, s87, 0\n" \
"ds_read_b128 a[0:3], v213 offset:0\n" \
"ds_read_b128 a[4:7], v213 offset:2048\n" \
"v_mfma_f32_16x16x32_bf16 v[8:11], a[116:119], a[68:71], v[8:11]\n" \
"buffer_load_dwordx4 v[176:179], v234, s[56:59], s71 offen offset:0 sc1\n" \
"v_mfma_f32_16x16x32_bf16 v[12:15], a[120:123], a[64:67], v[12:15]\n" \
"ds_read_b64_tr_b16 a[128:129], v214 offset:16384\n" \
"ds_read_b64_tr_b16 a[130:131], v214 offset:18432\n" \
"v_mfma_f32_16x16x32_bf16 v[12:15], a[124:127], a[68:71], v[12:15]\n" \
"buffer_load_dwordx4 v[180:183], v234, s[56:59], s71 offen offset:1024 sc1\n" \
"v_mfma_f32_16x16x32_bf16 v[28:31], a[120:123], a[72:75], v[28:31]\n" \
"ds_read_b64_tr_b16 a[132:133], v214 offset:24576\n" \
"ds_read_b64_tr_b16 a[134:135], v214 offset:26624\n" \
"v_mfma_f32_16x16x32_bf16 v[28:31], a[124:127], a[76:79], v[28:31]\n" \
"buffer_load_dwordx4 v[184:187], v234, s[56:59], s71 offen offset:2048 sc1\n" \
"v_mfma_f32_16x16x32_bf16 v[24:27], a[112:115], a[72:75], v[24:27]\n" \
"ds_read_b64_tr_b16 a[136:137], v215 offset:16384\n" \
"ds_read_b64_tr_b16 a[138:139], v215 offset:18432\n" \
"v_mfma_f32_16x16x32_bf16 v[24:27], a[116:119], a[76:79], v[24:27]\n" \
"buffer_load_dwordx4 v[188:191], v234, s[56:59], s71 offen offset:3072 sc1\n" \
"v_mfma_f32_16x16x32_bf16 v[40:43], a[112:115], a[80:83], v[40:43]\n" \
"ds_read_b64_tr_b16 a[140:141], v215 offset:24576\n" \
"ds_read_b64_tr_b16 a[142:143], v215 offset:26624\n" \
"v_mfma_f32_16x16x32_bf16 v[40:43], a[116:119], a[84:87], v[40:43]\n" \
"ds_read_b64_tr_b16 a[144:145], v214 offset:16896\n" \
"ds_read_b64_tr_b16 a[146:147], v214 offset:18944\n" \
"v_mfma_f32_16x16x32_bf16 v[44:47], a[120:123], a[80:83], v[44:47]\n" \
"ds_read_b64_tr_b16 a[148:149], v214 offset:25088\n" \
"ds_read_b64_tr_b16 a[150:151], v214 offset:27136\n" \
"v_mfma_f32_16x16x32_bf16 v[44:47], a[124:127], a[84:87], v[44:47]\n" \
"ds_read_b64_tr_b16 a[152:153], v215 offset:16896\n" \
"ds_read_b64_tr_b16 a[154:155], v215 offset:18944\n" \
"v_mfma_f32_16x16x32_bf16 v[60:63], a[120:123], a[88:91], v[60:63]\n" \
"ds_read_b64_tr_b16 a[156:157], v215 offset:25088\n" \
"ds_read_b64_tr_b16 a[158:159], v215 offset:27136\n" \
"v_mfma_f32_16x16x32_bf16 v[60:63], a[124:127], a[92:95], v[60:63]\n" \
"ds_read_b128 a[24:27], v213 offset:256\n" \
"ds_read_b128 a[28:31], v213 offset:2304\n" \
"v_mfma_f32_16x16x32_bf16 v[56:59], a[112:115], a[88:91], v[56:59]\n" \
"ds_read_b128 a[8:11], v224 offset:0\n" \
"ds_read_b128 a[12:15], v224 offset:2048\n" \
"v_mfma_f32_16x16x32_bf16 v[56:59], a[116:119], a[92:95], v[56:59]\n" \
"s_waitcnt lgkmcnt(8)\n" \
"ds_read_b64_tr_b16 a[160:161], v225 offset:16384\n" \
"ds_read_b64_tr_b16 a[162:163], v225 offset:18432\n" \
"v_mfma_f32_16x16x32_bf16 v[0:3], a[128:131], a[0:3], v[0:3]\n" \
"ds_read_b64_tr_b16 a[164:165], v225 offset:24576\n" \
"ds_read_b64_tr_b16 a[166:167], v225 offset:26624\n" \
"v_mfma_f32_16x16x32_bf16 v[0:3], a[132:135], a[4:7], v[0:3]\n" \
"ds_read_b64_tr_b16 a[168:169], v226 offset:16384\n" \
"ds_read_b64_tr_b16 a[170:171], v226 offset:18432\n" \
"v_mfma_f32_16x16x32_bf16 v[4:7], a[136:139], a[0:3], v[4:7]\n" \
"s_mov_b32 s70, 0x20000000\n" \
"ds_read_b64_tr_b16 a[172:173], v226 offset:24576\n" \
"ds_read_b64_tr_b16 a[174:175], v226 offset:26624\n" \
"v_mfma_f32_16x16x32_bf16 v[4:7], a[140:143], a[4:7], v[4:7]\n" \
"s_cmp_eq_u32 s64, 65616896\n" \
"s_cselect_b32 s70, 65593344, s70\n" \
"ds_read_b64_tr_b16 a[176:177], v225 offset:16896\n" \
"ds_read_b64_tr_b16 a[178:179], v225 offset:18944\n" \
"buffer_store_dword v230, v231, s[56:59], s70 offen sc1\n" \
"v_mfma_f32_16x16x32_bf16 v[8:11], a[144:147], a[0:3], v[8:11]\n" \
"s_waitcnt lgkmcnt(8)\n" \
"ds_read_b64_tr_b16 a[180:181], v225 offset:25088\n" \
"ds_read_b64_tr_b16 a[182:183], v225 offset:27136\n" \
"v_mfma_f32_16x16x32_bf16 v[8:11], a[148:151], a[4:7], v[8:11]\n" \
"s_sub_u32 s68, s86, 32768\n" \
"s_cselect_b32 s68, 131072, s68\n" \
"ds_read_b64_tr_b16 a[184:185], v226 offset:16896\n" \
"ds_read_b64_tr_b16 a[186:187], v226 offset:18944\n" \
"v_mfma_f32_16x16x32_bf16 v[12:15], a[152:155], a[0:3], v[12:15]\n" \
"ds_read_b64_tr_b16 a[188:189], v226 offset:25088\n" \
"ds_read_b64_tr_b16 a[190:191], v226 offset:27136\n" \
"v_mfma_f32_16x16x32_bf16 v[12:15], a[156:159], a[4:7], v[12:15]\n" \
"s_cmp_lt_u32 s61, 2\n" \
"s_cselect_b32 s68, s86, s68\n" \
"v_cmp_eq_u32 s[16:17], v72, v73\n" \
"v_and_b32 v72, 16, v234\n" \
"v_mfma_f32_16x16x32_bf16 v[16:19], a[128:131], a[24:27], v[16:19]\n" \
"s_and_b32 s50, s61, 1\n" \
"v_lshlrev_b32 v72, v72, v74\n" \
"v_cndmask_b32 v72, 0, v72, s[16:17]\n" \
"v_mfma_f32_16x16x32_bf16 v[16:19], a[132:135], a[28:31], v[16:19]\n" \
"s_mul_i32 s50, s50, 9216\n" \
"v_bfe_i32 v73, v234, 5, 1\n" \
"v_and_b32 v67, v72, v73\n" \
"v_mfma_f32_16x16x32_bf16 v[20:23], a[136:139], a[24:27], v[20:23]\n" \
"s_add_u32 s68, s68, s50\n" \
"s_add_u32 s68, s68, %3\n" \
"v_xor_b32 v66, v72, v67\n" \
"v_mfma_f32_16x16x32_bf16 v[20:23], a[140:143], a[28:31], v[20:23]\n" \
"v_lshrrev_b32 v132, 4, v234\n" \
"v_and_b32 v133, 15, v132\n" \
"v_mul_u32_u24 v128, 144, v133\n" \
"v_mfma_f32_16x16x32_bf16 v[24:27], a[144:147], a[24:27], v[24:27]\n" \
"s_waitcnt vmcnt(5)\n" \
"s_barrier\n" \
"v_mfma_f32_16x16x32_bf16 v[24:27], a[148:151], a[28:31], v[24:27]\n" \
"s_add_u32 s71, s87, 4096\n" \
"ds_read_b128 a[32:35], v224 offset:256\n" \
"ds_read_b128 a[36:39], v224 offset:2304\n" \
"v_mfma_f32_16x16x32_bf16 v[28:31], a[152:155], a[24:27], v[28:31]\n" \
"buffer_load_dwordx4 v[192:195], v234, s[56:59], s71 offen offset:0 sc1\n" \
"v_mfma_f32_16x16x32_bf16 v[28:31], a[156:159], a[28:31], v[28:31]\n" \
"s_waitcnt lgkmcnt(2)\n" \
"ds_read_b128 a[16:19], v227 offset:0\n" \
"ds_read_b128 a[20:23], v227 offset:2048\n" \
"v_mfma_f32_16x16x32_bf16 v[0:3], a[160:163], a[8:11], v[0:3]\n" \
"ds_read_b64_tr_b16 a[192:193], v228 offset:16384\n" \
"ds_read_b64_tr_b16 a[194:195], v228 offset:18432\n" \
"v_mfma_f32_16x16x32_bf16 v[0:3], a[164:167], a[12:15], v[0:3]\n" \
"buffer_load_dwordx4 v[196:199], v234, s[56:59], s71 offen offset:1024 sc1\n" \
"v_mfma_f32_16x16x32_bf16 v[4:7], a[168:171], a[8:11], v[4:7]\n" \
"ds_read_b64_tr_b16 a[196:197], v228 offset:24576\n" \
"ds_read_b64_tr_b16 a[198:199], v228 offset:26624\n" \
"v_mfma_f32_16x16x32_bf16 v[4:7], a[172:175], a[12:15], v[4:7]\n" \
"ds_read_b64_tr_b16 a[200:201], v229 offset:16384\n" \
"ds_read_b64_tr_b16 a[202:203], v229 offset:18432\n" \
"v_mfma_f32_16x16x32_bf16 v[8:11], a[176:179], a[8:11], v[8:11]\n" \
"buffer_load_dwordx4 v[200:203], v234, s[56:59], s71 offen offset:2048 sc1\n" \
"v_mfma_f32_16x16x32_bf16 v[8:11], a[180:183], a[12:15], v[8:11]\n" \
"ds_read_b64_tr_b16 a[204:205], v229 offset:24576\n" \
"ds_read_b64_tr_b16 a[206:207], v229 offset:26624\n" \
"v_lshrrev_b32 v133, 4, v132\n" \
"v_mfma_f32_16x16x32_bf16 v[12:15], a[184:187], a[8:11], v[12:15]\n" \
"s_waitcnt lgkmcnt(6)\n" \
"ds_read_b64_tr_b16 a[208:209], v228 offset:16896\n" \
"ds_read_b64_tr_b16 a[210:211], v228 offset:18944\n" \
"v_mfma_f32_16x16x32_bf16 v[12:15], a[188:191], a[12:15], v[12:15]\n" \
"buffer_load_dwordx4 v[204:207], v234, s[56:59], s71 offen offset:3072 sc1\n" \
"v_mfma_f32_16x16x32_bf16 v[16:19], a[160:163], a[32:35], v[16:19]\n" \
"ds_read_b64_tr_b16 a[212:213], v228 offset:25088\n" \
"ds_read_b64_tr_b16 a[214:215], v228 offset:27136\n" \
"v_mfma_f32_16x16x32_bf16 v[16:19], a[164:167], a[36:39], v[16:19]\n" \
"ds_read_b64_tr_b16 a[216:217], v229 offset:16896\n" \
"ds_read_b64_tr_b16 a[218:219], v229 offset:18944\n" \
"v_mfma_f32_16x16x32_bf16 v[20:23], a[168:171], a[32:35], v[20:23]\n" \
"ds_read_b64_tr_b16 a[220:221], v229 offset:25088\n" \
"ds_read_b64_tr_b16 a[222:223], v229 offset:27136\n" \
"v_mfma_f32_16x16x32_bf16 v[20:23], a[172:175], a[36:39], v[20:23]\n" \
"ds_read_b128 a[40:43], v227 offset:256\n" \
"ds_read_b128 a[44:47], v227 offset:2304\n" \
"v_mfma_f32_16x16x32_bf16 v[24:27], a[176:179], a[32:35], v[24:27]\n" \
"ds_read_b128 a[248:251], v213 offset:4096\n" \
"ds_read_b128 a[252:255], v213 offset:6144\n" \
"v_mfma_f32_16x16x32_bf16 v[24:27], a[180:183], a[36:39], v[24:27]\n" \
"ds_read_b128 a[48:51], v224 offset:4096\n" \
"ds_read_b128 a[52:55], v224 offset:6144\n" \
"v_mfma_f32_16x16x32_bf16 v[28:31], a[184:187], a[32:35], v[28:31]\n" \
"ds_read_b128 a[56:59], v227 offset:4096\n" \
"ds_read_b128 a[60:63], v227 offset:6144\n" \
"v_mfma_f32_16x16x32_bf16 v[28:31], a[188:191], a[36:39], v[28:31]\n" \
"s_waitcnt lgkmcnt(8)\n" \
"ds_read_b128 a[224:227], v213 offset:4352\n" \
"ds_read_b128 a[228:231], v213 offset:6400\n" \
"v_lshl_add_u32 v128, v133, 3, v128\n" \
"v_mfma_f32_16x16x32_bf16 v[0:3], a[192:195], a[16:19], v[0:3]\n" \
"s_waitcnt vmcnt(7)\n" \
"ds_read_b128 a[232:235], v224 offset:4352\n" \
"ds_read_b128 a[236:239], v224 offset:6400\n" \
"v_mfma_f32_16x16x32_bf16 v[0:3], a[196:199], a[20:23], v[0:3]\n" \
"ds_read_b128 a[240:243], v227 offset:4352\n" \
"ds_read_b128 a[244:247], v227 offset:6400\n" \
"v_mfma_f32_16x16x32_bf16 v[0:3], v[66:69], v[176:179], v[0:3]\n" \
"v_add_u32 v128, s68, v128\n" \
"v_lshrrev_b32 v133, 3, v132\n" \
"v_mfma_f32_16x16x32_bf16 v[4:7], a[200:203], a[16:19], v[4:7]\n" \
"v_and_b32 v134, 7, v132\n" \
"v_mul_u32_u24 v129, 144, v133\n" \
"v_mfma_f32_16x16x32_bf16 v[4:7], a[204:207], a[20:23], v[4:7]\n" \
"v_lshl_add_u32 v129, v134, 4, v129\n" \
"v_add_u32 v129, s68, v129\n" \
"v_mfma_f32_16x16x32_bf16 v[4:7], v[64:67], v[176:179], v[4:7]\n" \
"v_lshlrev_b32 v134, 4, v134\n" \
"v_lshl_add_u32 v130, v133, 12, v134\n" \
"v_mfma_f32_16x16x32_bf16 v[8:11], a[208:211], a[16:19], v[8:11]\n" \
"v_mfma_f32_16x16x32_bf16 v[8:11], a[212:215], a[20:23], v[8:11]\n" \
"v_mfma_f32_16x16x32_bf16 v[8:11], v[66:69], v[180:183], v[8:11]\n" \
"v_mfma_f32_16x16x32_bf16 v[12:15], a[216:219], a[16:19], v[12:15]\n" \
"v_mfma_f32_16x16x32_bf16 v[12:15], a[220:223], a[20:23], v[12:15]\n" \
"v_mfma_f32_16x16x32_bf16 v[12:15], v[64:67], v[180:183], v[12:15]\n" \
"s_waitcnt lgkmcnt(0)\n" \
"v_mfma_f32_16x16x32_bf16 v[16:19], a[192:195], a[40:43], v[16:19]\n" \
"s_waitcnt vmcnt(5)\n" \
"v_mfma_f32_16x16x32_bf16 v[16:19], a[196:199], a[44:47], v[16:19]\n" \
"v_mfma_f32_16x16x32_bf16 v[16:19], v[66:69], v[184:187], v[16:19]\n" \
"v_cvt_pk_bf16_f32 v136, v0, v1\n" \
"v_cvt_pk_bf16_f32 v137, v2, v3\n" \
"v_mfma_f32_16x16x32_bf16 v[20:23], a[200:203], a[40:43], v[20:23]\n" \
"v_mfma_f32_16x16x32_bf16 v[20:23], a[204:207], a[44:47], v[20:23]\n" \
"v_mfma_f32_16x16x32_bf16 v[20:23], v[64:67], v[184:187], v[20:23]\n" \
"ds_write_b64 v128, v[136:137] offset:0\n" \
"v_mfma_f32_16x16x32_bf16 v[24:27], a[208:211], a[40:43], v[24:27]\n" \
"v_cvt_pk_bf16_f32 v138, v4, v5\n" \
"v_cvt_pk_bf16_f32 v139, v6, v7\n" \
"v_mfma_f32_16x16x32_bf16 v[24:27], a[212:215], a[44:47], v[24:27]\n" \
"v_mfma_f32_16x16x32_bf16 v[24:27], v[66:69], v[188:191], v[24:27]\n" \
"ds_write_b64 v128, v[138:139] offset:32\n" \
"v_mfma_f32_16x16x32_bf16 v[28:31], a[216:219], a[40:43], v[28:31]\n" \
"v_cvt_pk_bf16_f32 v136, v8, v9\n" \
"v_cvt_pk_bf16_f32 v137, v10, v11\n" \
"v_mfma_f32_16x16x32_bf16 v[28:31], a[220:223], a[44:47], v[28:31]\n" \
"v_mfma_f32_16x16x32_bf16 v[28:31], v[64:67], v[188:191], v[28:31]\n" \
"ds_write_b64 v128, v[136:137] offset:64\n" \
"v_mfma_f32_16x16x32_bf16 v[32:35], a[128:131], a[248:251], v[32:35]\n" \
"s_waitcnt vmcnt(2)\n" \
"v_cvt_pk_bf16_f32 v138, v12, v13\n" \
"v_cvt_pk_bf16_f32 v139, v14, v15\n" \
"v_mfma_f32_16x16x32_bf16 v[32:35], a[132:135], a[252:255], v[32:35]\n" \
"v_mfma_f32_16x16x32_bf16 v[32:35], v[66:69], v[192:195], v[32:35]\n" \
"ds_write_b64 v128, v[138:139] offset:96\n" \
"v_mfma_f32_16x16x32_bf16 v[36:39], a[136:139], a[248:251], v[36:39]\n" \
"ds_read_b128 v[140:143], v129 offset:0\n" \
"ds_read_b128 v[144:147], v129 offset:1152\n" \
"v_mfma_f32_16x16x32_bf16 v[36:39], a[140:143], a[252:255], v[36:39]\n" \
"v_cvt_pk_bf16_f32 v136, v16, v17\n" \
"v_cvt_pk_bf16_f32 v137, v18, v19\n" \
"v_mfma_f32_16x16x32_bf16 v[36:39], v[64:67], v[192:195], v[36:39]\n" \
"ds_write_b64 v128, v[136:137] offset:2304\n" \
"v_mfma_f32_16x16x32_bf16 v[40:43], a[144:147], a[248:251], v[40:43]\n" \
"s_add_u32 s70, s88, 0\n" \
"s_waitcnt lgkmcnt(2)\n" \
"v_cvt_pk_bf16_f32 v138, v20, v21\n" \
"v_mfma_f32_16x16x32_bf16 v[40:43], a[148:151], a[252:255], v[40:43]\n" \
"buffer_store_dwordx4 v[140:143], v130, s[52:55], s70 offen sc1\n" \
"v_mfma_f32_16x16x32_bf16 v[40:43], v[66:69], v[196:199], v[40:43]\n" \
"v_cvt_pk_bf16_f32 v139, v22, v23\n" \
"v_mfma_f32_16x16x32_bf16 v[44:47], a[152:155], a[248:251], v[44:47]\n" \
"s_add_u32 s70, s88, 32768\n" \
"s_waitcnt lgkmcnt(1)\n" \
"v_mfma_f32_16x16x32_bf16 v[44:47], a[156:159], a[252:255], v[44:47]\n" \
"buffer_store_dwordx4 v[144:147], v130, s[52:55], s70 offen sc1\n" \
"v_mfma_f32_16x16x32_bf16 v[44:47], v[64:67], v[196:199], v[44:47]\n" \
"ds_write_b64 v128, v[138:139] offset:2336\n" \
"v_mfma_f32_16x16x32_bf16 v[32:35], a[160:163], a[48:51], v[32:35]\n" \
"v_cvt_pk_bf16_f32 v136, v24, v25\n" \
"v_cvt_pk_bf16_f32 v137, v26, v27\n" \
"v_mfma_f32_16x16x32_bf16 v[32:35], a[164:167], a[52:55], v[32:35]\n" \
"ds_write_b64 v128, v[136:137] offset:2368\n" \
"v_mfma_f32_16x16x32_bf16 v[36:39], a[168:171], a[48:51], v[36:39]\n" \
"v_cvt_pk_bf16_f32 v138, v28, v29\n" \
"v_cvt_pk_bf16_f32 v139, v30, v31\n" \
"v_mfma_f32_16x16x32_bf16 v[36:39], a[172:175], a[52:55], v[36:39]\n" \
"ds_write_b64 v128, v[138:139] offset:2400\n" \
"v_mfma_f32_16x16x32_bf16 v[40:43], a[176:179], a[48:51], v[40:43]\n" \
"ds_read_b128 v[148:151], v129 offset:2304\n" \
"ds_read_b128 v[152:155], v129 offset:3456\n" \
"v_mfma_f32_16x16x32_bf16 v[40:43], a[180:183], a[52:55], v[40:43]\n" \
"v_mfma_f32_16x16x32_bf16 v[44:47], a[184:187], a[48:51], v[44:47]\n" \
"v_mfma_f32_16x16x32_bf16 v[44:47], a[188:191], a[52:55], v[44:47]\n" \
"s_add_u32 s70, s88, 65536\n" \
"s_waitcnt lgkmcnt(1)\n" \
"v_mfma_f32_16x16x32_bf16 v[32:35], a[192:195], a[56:59], v[32:35]\n" \
"buffer_store_dwordx4 v[148:151], v130, s[52:55], s70 offen sc1\n" \
"v_mfma_f32_16x16x32_bf16 v[32:35], a[196:199], a[60:63], v[32:35]\n" \
"v_mfma_f32_16x16x32_bf16 v[36:39], a[200:203], a[56:59], v[36:39]\n" \
"s_add_u32 s70, s88, 98304\n" \
"s_waitcnt lgkmcnt(0)\n" \
"v_mfma_f32_16x16x32_bf16 v[36:39], a[204:207], a[60:63], v[36:39]\n" \
"buffer_store_dwordx4 v[152:155], v130, s[52:55], s70 offen sc1\n" \
"v_mfma_f32_16x16x32_bf16 v[40:43], a[208:211], a[56:59], v[40:43]\n" \
"v_mfma_f32_16x16x32_bf16 v[40:43], a[212:215], a[60:63], v[40:43]\n" \
"v_mfma_f32_16x16x32_bf16 v[44:47], a[216:219], a[56:59], v[44:47]\n" \
"v_mfma_f32_16x16x32_bf16 v[44:47], a[220:223], a[60:63], v[44:47]\n" \
"v_mfma_f32_16x16x32_bf16 v[48:51], a[128:131], a[224:227], v[48:51]\n" \
"s_waitcnt vmcnt(4)\n" \
"v_mfma_f32_16x16x32_bf16 v[48:51], a[132:135], a[228:231], v[48:51]\n" \
"v_mfma_f32_16x16x32_bf16 v[48:51], v[66:69], v[200:203], v[48:51]\n" \
"v_cvt_pk_bf16_f32 v136, v32, v33\n" \
"v_cvt_pk_bf16_f32 v137, v34, v35\n" \
"v_mfma_f32_16x16x32_bf16 v[52:55], a[136:139], a[224:227], v[52:55]\n" \
"v_mfma_f32_16x16x32_bf16 v[52:55], a[140:143], a[228:231], v[52:55]\n" \
"v_mfma_f32_16x16x32_bf16 v[52:55], v[64:67], v[200:203], v[52:55]\n" \
"ds_write_b64 v128, v[136:137] offset:4608\n" \
"v_mfma_f32_16x16x32_bf16 v[56:59], a[144:147], a[224:227], v[56:59]\n" \
"v_cvt_pk_bf16_f32 v138, v36, v37\n" \
"v_cvt_pk_bf16_f32 v139, v38, v39\n" \
"v_mfma_f32_16x16x32_bf16 v[56:59], a[148:151], a[228:231], v[56:59]\n" \
"v_mfma_f32_16x16x32_bf16 v[56:59], v[66:69], v[204:207], v[56:59]\n" \
"ds_write_b64 v128, v[138:139] offset:4640\n" \
"v_mfma_f32_16x16x32_bf16 v[60:63], a[152:155], a[224:227], v[60:63]\n" \
"v_cvt_pk_bf16_f32 v136, v40, v41\n" \
"v_cvt_pk_bf16_f32 v137, v42, v43\n" \
"v_mfma_f32_16x16x32_bf16 v[60:63], a[156:159], a[228:231], v[60:63]\n" \
"v_mfma_f32_16x16x32_bf16 v[60:63], v[64:67], v[204:207], v[60:63]\n" \
"ds_write_b64 v128, v[136:137] offset:4672\n" \
"v_mfma_f32_16x16x32_bf16 v[48:51], a[160:163], a[232:235], v[48:51]\n" \
"v_cvt_pk_bf16_f32 v138, v44, v45\n" \
"v_cvt_pk_bf16_f32 v139, v46, v47\n" \
"v_mfma_f32_16x16x32_bf16 v[48:51], a[164:167], a[236:239], v[48:51]\n" \
"ds_write_b64 v128, v[138:139] offset:4704\n" \
"v_mfma_f32_16x16x32_bf16 v[52:55], a[168:171], a[232:235], v[52:55]\n" \
"ds_read_b128 v[156:159], v129 offset:4608\n" \
"ds_read_b128 v[160:163], v129 offset:5760\n" \
"v_mfma_f32_16x16x32_bf16 v[52:55], a[172:175], a[236:239], v[52:55]\n" \
"v_mfma_f32_16x16x32_bf16 v[56:59], a[176:179], a[232:235], v[56:59]\n" \
"v_mfma_f32_16x16x32_bf16 v[56:59], a[180:183], a[236:239], v[56:59]\n" \
"s_add_u32 s70, s88, 131072\n" \
"s_waitcnt lgkmcnt(1)\n" \
"v_mfma_f32_16x16x32_bf16 v[60:63], a[184:187], a[232:235], v[60:63]\n" \
"buffer_store_dwordx4 v[156:159], v130, s[52:55], s70 offen sc1\n" \
"v_mfma_f32_16x16x32_bf16 v[60:63], a[188:191], a[236:239], v[60:63]\n" \
"v_mfma_f32_16x16x32_bf16 v[48:51], a[192:195], a[240:243], v[48:51]\n" \
"s_add_u32 s70, s88, 163840\n" \
"s_waitcnt lgkmcnt(0)\n" \
"v_mfma_f32_16x16x32_bf16 v[48:51], a[196:199], a[244:247], v[48:51]\n" \
"buffer_store_dwordx4 v[160:163], v130, s[52:55], s70 offen sc1\n" \
"v_mfma_f32_16x16x32_bf16 v[52:55], a[200:203], a[240:243], v[52:55]\n" \
"v_mfma_f32_16x16x32_bf16 v[52:55], a[204:207], a[244:247], v[52:55]\n" \
"v_mfma_f32_16x16x32_bf16 v[56:59], a[208:211], a[240:243], v[56:59]\n" \
"v_mfma_f32_16x16x32_bf16 v[56:59], a[212:215], a[244:247], v[56:59]\n" \
"v_mfma_f32_16x16x32_bf16 v[60:63], a[216:219], a[240:243], v[60:63]\n" \
"v_mfma_f32_16x16x32_bf16 v[60:63], a[220:223], a[244:247], v[60:63]\n" \
"s_nop 1\n" \
"v_cvt_pk_bf16_f32 v136, v48, v49\n" \
"v_cvt_pk_bf16_f32 v137, v50, v51\n" \
"ds_write_b64 v128, v[136:137] offset:6912\n" \
"v_cvt_pk_bf16_f32 v138, v52, v53\n" \
"v_cvt_pk_bf16_f32 v139, v54, v55\n" \
"ds_write_b64 v128, v[138:139] offset:6944\n" \
"v_cvt_pk_bf16_f32 v136, v56, v57\n" \
"v_cvt_pk_bf16_f32 v137, v58, v59\n" \
"ds_write_b64 v128, v[136:137] offset:6976\n" \
"v_cvt_pk_bf16_f32 v138, v60, v61\n" \
"v_cvt_pk_bf16_f32 v139, v62, v63\n" \
"ds_write_b64 v128, v[138:139] offset:7008\n" \
"ds_read_b128 v[164:167], v129 offset:6912\n" \
"ds_read_b128 v[168:171], v129 offset:8064\n" \
"s_add_u32 s70, s88, 196608\n" \
"s_waitcnt lgkmcnt(1)\n" \
"buffer_store_dwordx4 v[164:167], v130, s[52:55], s70 offen sc1\n" \
"s_add_u32 s70, s88, 229376\n" \
"s_waitcnt lgkmcnt(0)\n" \
"buffer_store_dwordx4 v[168:171], v130, s[52:55], s70 offen sc1\n" \
"s_endpgm\n" \
".p2align 12\n" \
"Lrole1_%=:\n" \
"v_mfma_f32_16x16x32_bf16 v[64:67], a[96:99], a[64:67], 0\n" \
"Lpg11159_%=:\n" \
"s_cbranch_execz Lcb1_%=\n" \
"s_waitcnt lgkmcnt(1)\n" \
"s_xor_b32 s82, s48, s49\n" \
"ds_read_b128 a[88:91], v213 offset:4352\n" \
"v_mfma_f32_16x16x32_bf16 v[64:67], a[100:103], a[68:71], v[64:67]\n" \
"s_cmp_lt_u32 s48, s50\n" \
"s_cselect_b32 s82, s82, s48\n" \
"ds_read_b128 a[92:95], v213 offset:6400\n" \
"v_mfma_f32_16x16x32_bf16 v[68:71], a[104:107], a[64:67], 0\n" \
"s_add_u32 s81, s82, s46\n" \
"ds_read_b64_tr_b16 a[112:113], v214 offset:16896\n" \
"v_mfma_f32_16x16x32_bf16 v[68:71], a[108:111], a[68:71], v[68:71]\n" \
"s_add_u32 m0, s86, s79\n" \
"ds_read_b64_tr_b16 a[114:115], v214 offset:18944\n" \
"v_mfma_f32_16x16x32_bf16 v[84:87], a[104:107], a[72:75], 0\n" \
"buffer_load_dwordx4 v234, s[56:59], s81 offen offset:0 sc1 lds\n" \
"v_mfma_f32_16x16x32_bf16 v[84:87], a[108:111], a[76:79], v[84:87]\n" \
"s_waitcnt lgkmcnt(3)\n" \
"ds_read_b64_tr_b16 a[116:117], v214 offset:25088\n" \
"v_mfma_f32_16x16x32_bf16 v[80:83], a[96:99], a[72:75], 0\n" \
"s_lshl_b32 s82, s82, 5\n" \
"ds_read_b64_tr_b16 a[118:119], v214 offset:27136\n" \
"v_cvt_pk_bf16_f32 v0, v0, v1\n" \
"v_mfma_f32_16x16x32_bf16 v[80:83], a[100:103], a[76:79], v[80:83]\n" \
"s_add_u32 s82, s82, s47\n" \
"ds_read_b64_tr_b16 a[120:121], v215 offset:16896\n" \
"v_cvt_pk_bf16_f32 v1, v2, v3\n" \
"v_mfma_f32_16x16x32_bf16 v[96:99], a[96:99], a[80:83], 0\n" \
"buffer_load_dwordx4 v234, s[56:59], s81 offen offset:1024 sc1 lds\n" \
"v_mfma_f32_16x16x32_bf16 v[96:99], a[100:103], a[84:87], v[96:99]\n" \
"s_sub_u32 s85, s85, 32768\n" \
"s_cselect_b32 s85, 131072, s85\n" \
"ds_read_b64_tr_b16 a[122:123], v215 offset:18944\n" \
"v_cvt_pk_bf16_f32 v2, v4, v5\n" \
"v_mfma_f32_16x16x32_bf16 v[100:103], a[104:107], a[80:83], 0\n" \
"s_add_u32 s48, s48, 0x1000\n" \
"ds_read_b64_tr_b16 a[124:125], v215 offset:25088\n" \
"v_add_u32 v213, s85, v211\n" \
"v_mfma_f32_16x16x32_bf16 v[100:103], a[108:111], a[84:87], v[100:103]\n" \
"s_waitcnt lgkmcnt(3)\n" \
"s_sub_u32 s86, s86, 32768\n" \
"s_cselect_b32 s86, 131072, s86\n" \
"ds_read_b64_tr_b16 a[126:127], v215 offset:27136\n" \
"v_mfma_f32_16x16x32_bf16 v[116:119], a[104:107], a[88:91], 0\n" \
"buffer_load_dwordx4 v234, s[56:59], s81 offen offset:2048 sc1 lds\n" \
"v_mfma_f32_16x16x32_bf16 v[116:119], a[108:111], a[92:95], v[116:119]\n" \
"v_add_u32 v214, s85, v212\n" \
"v_xor_b32 v215, 32, v214\n" \
"v_cvt_pk_bf16_f32 v3, v6, v7\n" \
"v_mfma_f32_16x16x32_bf16 v[112:115], a[96:99], a[88:91], 0\n" \
"buffer_load_dwordx4 v234, s[56:59], s81 offen offset:3072 sc1 lds\n" \
"v_mfma_f32_16x16x32_bf16 v[112:115], a[100:103], a[92:95], v[112:115]\n" \
"s_waitcnt vmcnt(20) lgkmcnt(0)\n" \
"s_barrier\n" \
"v_mfma_f32_16x16x32_bf16 v[72:75], a[112:115], a[64:67], 0\n" \
"ds_read_b64_tr_b16 a[96:97], v214 offset:16384\n" \
"ds_read_b64_tr_b16 a[98:99], v214 offset:18432\n" \
"v_mfma_f32_16x16x32_bf16 v[72:75], a[116:119], a[68:71], v[72:75]\n" \
"s_add_u32 m0, m0, 0x4000\n" \
"ds_read_b64_tr_b16 a[100:101], v214 offset:24576\n" \
"ds_read_b64_tr_b16 a[102:103], v214 offset:26624\n" \
"v_cvt_pk_bf16_f32 v4, v8, v9\n" \
"v_mfma_f32_16x16x32_bf16 v[76:79], a[120:123], a[64:67], 0\n" \
"buffer_load_dwordx4 v216, s[36:39], s82 offen offset:0 lds\n" \
"v_mfma_f32_16x16x32_bf16 v[76:79], a[124:127], a[68:71], v[76:79]\n" \
"ds_read_b128 a[64:67], v213 offset:0\n" \
"v_cvt_pk_bf16_f32 v5, v10, v11\n" \
"v_mfma_f32_16x16x32_bf16 v[92:95], a[120:123], a[72:75], 0\n" \
"ds_read_b128 a[68:71], v213 offset:2048\n" \
"v_cvt_pk_bf16_f32 v6, v12, v13\n" \
"v_mfma_f32_16x16x32_bf16 v[92:95], a[124:127], a[76:79], v[92:95]\n" \
"ds_read_b64_tr_b16 a[104:105], v215 offset:16384\n" \
"ds_read_b64_tr_b16 a[106:107], v215 offset:18432\n" \
"v_cvt_pk_bf16_f32 v7, v14, v15\n" \
"v_mfma_f32_16x16x32_bf16 v[88:91], a[112:115], a[72:75], 0\n" \
"buffer_load_dwordx4 v217, s[36:39], s82 offen offset:1024 lds\n" \
"v_mfma_f32_16x16x32_bf16 v[88:91], a[116:119], a[76:79], v[88:91]\n" \
"ds_read_b128 a[72:75], v213 offset:256\n" \
"v_cvt_pk_bf16_f32 v8, v16, v17\n" \
"v_mfma_f32_16x16x32_bf16 v[104:107], a[112:115], a[80:83], 0\n" \
"ds_read_b128 a[76:79], v213 offset:2304\n" \
"v_cvt_pk_bf16_f32 v9, v18, v19\n" \
"v_mfma_f32_16x16x32_bf16 v[104:107], a[116:119], a[84:87], v[104:107]\n" \
"ds_read_b64_tr_b16 a[108:109], v215 offset:24576\n" \
"ds_read_b64_tr_b16 a[110:111], v215 offset:26624\n" \
"v_cvt_pk_bf16_f32 v10, v20, v21\n" \
"v_mfma_f32_16x16x32_bf16 v[108:111], a[120:123], a[80:83], 0\n" \
"buffer_load_dwordx4 v218, s[36:39], s82 offen offset:2048 lds\n" \
"v_mfma_f32_16x16x32_bf16 v[108:111], a[124:127], a[84:87], v[108:111]\n" \
"s_add_u32 s71, s87, 0\n" \
"ds_read_b128 a[80:83], v213 offset:4096\n" \
"v_cvt_pk_bf16_f32 v11, v22, v23\n" \
"v_mfma_f32_16x16x32_bf16 v[124:127], a[120:123], a[88:91], 0\n" \
"s_waitcnt lgkmcnt(4)\n" \
"buffer_store_dwordx4 v[0:3], v234, s[56:59], s71 offen offset:0 sc1\n" \
"v_mfma_f32_16x16x32_bf16 v[124:127], a[124:127], a[92:95], v[124:127]\n" \
"ds_read_b128 a[84:87], v213 offset:6144\n" \
"v_cvt_pk_bf16_f32 v12, v24, v25\n" \
"v_mfma_f32_16x16x32_bf16 v[120:123], a[112:115], a[88:91], 0\n" \
"buffer_load_dwordx4 v219, s[36:39], s82 offen offset:3072 lds\n" \
"v_mfma_f32_16x16x32_bf16 v[120:123], a[116:119], a[92:95], v[120:123]\n" \
"buffer_store_dwordx4 v[4:7], v234, s[56:59], s71 offen offset:1024 sc1\n" \
"buffer_store_dwordx4 v[8:11], v234, s[56:59], s71 offen offset:2048 sc1\n" \
"v_mfma_f32_16x16x32_bf16 v[64:67], a[96:99], a[64:67], v[64:67]\n" \
"s_xor_b32 s82, s48, s49\n" \
"ds_read_b128 a[88:91], v213 offset:4352\n" \
"v_cvt_pk_bf16_f32 v13, v26, v27\n" \
"v_mfma_f32_16x16x32_bf16 v[64:67], a[100:103], a[68:71], v[64:67]\n" \
"s_waitcnt lgkmcnt(2)\n" \
"s_cmp_lt_u32 s48, s50\n" \
"s_cselect_b32 s82, s82, s48\n" \
"ds_read_b128 a[92:95], v213 offset:6400\n" \
"v_mfma_f32_16x16x32_bf16 v[68:71], a[104:107], a[64:67], v[68:71]\n" \
"s_add_u32 s81, s82, s46\n" \
"ds_read_b64_tr_b16 a[112:113], v214 offset:16896\n" \
"v_cvt_pk_bf16_f32 v14, v28, v29\n" \
"v_mfma_f32_16x16x32_bf16 v[68:71], a[108:111], a[68:71], v[68:71]\n" \
"s_add_u32 m0, s86, s79\n" \
"ds_read_b64_tr_b16 a[114:115], v214 offset:18944\n" \
"v_cvt_pk_bf16_f32 v15, v30, v31\n" \
"v_mfma_f32_16x16x32_bf16 v[84:87], a[104:107], a[72:75], v[84:87]\n" \
"buffer_load_dwordx4 v234, s[56:59], s81 offen offset:0 sc1 lds\n" \
"v_mfma_f32_16x16x32_bf16 v[84:87], a[108:111], a[76:79], v[84:87]\n" \
"s_waitcnt lgkmcnt(3)\n" \
"ds_read_b64_tr_b16 a[116:117], v214 offset:25088\n" \
"v_cvt_pk_bf16_f32 v16, v32, v33\n" \
"v_mfma_f32_16x16x32_bf16 v[80:83], a[96:99], a[72:75], v[80:83]\n" \
"s_lshl_b32 s82, s82, 5\n" \
"ds_read_b64_tr_b16 a[118:119], v214 offset:27136\n" \
"v_cvt_pk_bf16_f32 v17, v34, v35\n" \
"v_mfma_f32_16x16x32_bf16 v[80:83], a[100:103], a[76:79], v[80:83]\n" \
"s_add_u32 s82, s82, s47\n" \
"ds_read_b64_tr_b16 a[120:121], v215 offset:16896\n" \
"v_cvt_pk_bf16_f32 v18, v36, v37\n" \
"v_mfma_f32_16x16x32_bf16 v[96:99], a[96:99], a[80:83], v[96:99]\n" \
"buffer_load_dwordx4 v234, s[56:59], s81 offen offset:1024 sc1 lds\n" \
"v_mfma_f32_16x16x32_bf16 v[96:99], a[100:103], a[84:87], v[96:99]\n" \
"s_sub_u32 s85, s85, 32768\n" \
"s_cselect_b32 s85, 131072, s85\n" \
"ds_read_b64_tr_b16 a[122:123], v215 offset:18944\n" \
"v_mfma_f32_16x16x32_bf16 v[100:103], a[104:107], a[80:83], v[100:103]\n" \
"s_add_u32 s48, s48, 0x1000\n" \
"ds_read_b64_tr_b16 a[124:125], v215 offset:25088\n" \
"v_add_u32 v213, s85, v211\n" \
"v_mfma_f32_16x16x32_bf16 v[100:103], a[108:111], a[84:87], v[100:103]\n" \
"s_waitcnt lgkmcnt(3)\n" \
"s_sub_u32 s86, s86, 32768\n" \
"s_cselect_b32 s86, 131072, s86\n" \
"ds_read_b64_tr_b16 a[126:127], v215 offset:27136\n" \
"v_mfma_f32_16x16x32_bf16 v[116:119], a[104:107], a[88:91], v[116:119]\n" \
"buffer_load_dwordx4 v234, s[56:59], s81 offen offset:2048 sc1 lds\n" \
"v_mfma_f32_16x16x32_bf16 v[116:119], a[108:111], a[92:95], v[116:119]\n" \
"v_add_u32 v214, s85, v212\n" \
"v_xor_b32 v215, 32, v214\n" \
"v_mfma_f32_16x16x32_bf16 v[112:115], a[96:99], a[88:91], v[112:115]\n" \
"buffer_load_dwordx4 v234, s[56:59], s81 offen offset:3072 sc1 lds\n" \
"v_mfma_f32_16x16x32_bf16 v[112:115], a[100:103], a[92:95], v[112:115]\n" \
"s_waitcnt vmcnt(23) lgkmcnt(0)\n" \
"s_barrier\n" \
"v_mfma_f32_16x16x32_bf16 v[72:75], a[112:115], a[64:67], v[72:75]\n" \
"ds_read_b64_tr_b16 a[96:97], v214 offset:16384\n" \
"ds_read_b64_tr_b16 a[98:99], v214 offset:18432\n" \
"v_mfma_f32_16x16x32_bf16 v[72:75], a[116:119], a[68:71], v[72:75]\n" \
"s_add_u32 m0, m0, 0x4000\n" \
"ds_read_b64_tr_b16 a[100:101], v214 offset:24576\n" \
"ds_read_b64_tr_b16 a[102:103], v214 offset:26624\n" \
"v_mfma_f32_16x16x32_bf16 v[76:79], a[120:123], a[64:67], v[76:79]\n" \
"buffer_load_dwordx4 v216, s[36:39], s82 offen offset:0 lds\n" \
"v_mfma_f32_16x16x32_bf16 v[76:79], a[124:127], a[68:71], v[76:79]\n" \
"ds_read_b128 a[64:67], v213 offset:0\n" \
"v_cvt_pk_bf16_f32 v19, v38, v39\n" \
"v_mfma_f32_16x16x32_bf16 v[92:95], a[120:123], a[72:75], v[92:95]\n" \
"ds_read_b128 a[68:71], v213 offset:2048\n" \
"v_cvt_pk_bf16_f32 v20, v40, v41\n" \
"v_mfma_f32_16x16x32_bf16 v[92:95], a[124:127], a[76:79], v[92:95]\n" \
"ds_read_b64_tr_b16 a[104:105], v215 offset:16384\n" \
"ds_read_b64_tr_b16 a[106:107], v215 offset:18432\n" \
"v_mfma_f32_16x16x32_bf16 v[88:91], a[112:115], a[72:75], v[88:91]\n" \
"buffer_load_dwordx4 v217, s[36:39], s82 offen offset:1024 lds\n" \
"v_mfma_f32_16x16x32_bf16 v[88:91], a[116:119], a[76:79], v[88:91]\n" \
"ds_read_b128 a[72:75], v213 offset:256\n" \
"v_cvt_pk_bf16_f32 v21, v42, v43\n" \
"v_mfma_f32_16x16x32_bf16 v[104:107], a[112:115], a[80:83], v[104:107]\n" \
"ds_read_b128 a[76:79], v213 offset:2304\n" \
"v_cvt_pk_bf16_f32 v22, v44, v45\n" \
"v_mfma_f32_16x16x32_bf16 v[104:107], a[116:119], a[84:87], v[104:107]\n" \
"ds_read_b64_tr_b16 a[108:109], v215 offset:24576\n" \
"ds_read_b64_tr_b16 a[110:111], v215 offset:26624\n" \
"v_mfma_f32_16x16x32_bf16 v[108:111], a[120:123], a[80:83], v[108:111]\n" \
"buffer_load_dwordx4 v218, s[36:39], s82 offen offset:2048 lds\n" \
"v_mfma_f32_16x16x32_bf16 v[108:111], a[124:127], a[84:87], v[108:111]\n" \
"ds_read_b128 a[80:83], v213 offset:4096\n" \
"v_cvt_pk_bf16_f32 v23, v46, v47\n" \
"v_mfma_f32_16x16x32_bf16 v[124:127], a[120:123], a[88:91], v[124:127]\n" \
"s_waitcnt lgkmcnt(4)\n" \
"buffer_store_dwordx4 v[12:15], v234, s[56:59], s71 offen offset:3072 sc1\n" \
"v_mfma_f32_16x16x32_bf16 v[124:127], a[124:127], a[92:95], v[124:127]\n" \
"s_add_u32 s71, s87, 4096\n" \
"ds_read_b128 a[84:87], v213 offset:6144\n" \
"v_cvt_pk_bf16_f32 v24, v48, v49\n" \
"v_mfma_f32_16x16x32_bf16 v[120:123], a[112:115], a[88:91], v[120:123]\n" \
"buffer_load_dwordx4 v219, s[36:39], s82 offen offset:3072 lds\n" \
"v_mfma_f32_16x16x32_bf16 v[120:123], a[116:119], a[92:95], v[120:123]\n" \
"buffer_store_dwordx4 v[16:19], v234, s[56:59], s71 offen offset:0 sc1\n" \
"buffer_store_dwordx4 v[20:23], v234, s[56:59], s71 offen offset:1024 sc1\n" \
"v_mfma_f32_16x16x32_bf16 v[64:67], a[96:99], a[64:67], v[64:67]\n" \
"s_xor_b32 s82, s48, s49\n" \
"ds_read_b128 a[88:91], v213 offset:4352\n" \
"v_cvt_pk_bf16_f32 v25, v50, v51\n" \
"v_mfma_f32_16x16x32_bf16 v[64:67], a[100:103], a[68:71], v[64:67]\n" \
"s_waitcnt lgkmcnt(2)\n" \
"s_cmp_lt_u32 s48, s50\n" \
"s_cselect_b32 s82, s82, s48\n" \
"ds_read_b128 a[92:95], v213 offset:6400\n" \
"v_mfma_f32_16x16x32_bf16 v[68:71], a[104:107], a[64:67], v[68:71]\n" \
"s_add_u32 s81, s82, s46\n" \
"ds_read_b64_tr_b16 a[112:113], v214 offset:16896\n" \
"v_cvt_pk_bf16_f32 v26, v52, v53\n" \
"v_mfma_f32_16x16x32_bf16 v[68:71], a[108:111], a[68:71], v[68:71]\n" \
"s_add_u32 m0, s86, s79\n" \
"ds_read_b64_tr_b16 a[114:115], v214 offset:18944\n" \
"v_cvt_pk_bf16_f32 v27, v54, v55\n" \
"v_mfma_f32_16x16x32_bf16 v[84:87], a[104:107], a[72:75], v[84:87]\n" \
"buffer_load_dwordx4 v234, s[56:59], s81 offen offset:0 sc1 lds\n" \
"v_mfma_f32_16x16x32_bf16 v[84:87], a[108:111], a[76:79], v[84:87]\n" \
"s_waitcnt lgkmcnt(3)\n" \
"ds_read_b64_tr_b16 a[116:117], v214 offset:25088\n" \
"v_cvt_pk_bf16_f32 v28, v56, v57\n" \
"v_mfma_f32_16x16x32_bf16 v[80:83], a[96:99], a[72:75], v[80:83]\n" \
"s_lshl_b32 s82, s82, 5\n" \
"ds_read_b64_tr_b16 a[118:119], v214 offset:27136\n" \
"v_cvt_pk_bf16_f32 v29, v58, v59\n" \
"v_mfma_f32_16x16x32_bf16 v[80:83], a[100:103], a[76:79], v[80:83]\n" \
"s_add_u32 s82, s82, s47\n" \
"ds_read_b64_tr_b16 a[120:121], v215 offset:16896\n" \
"v_cvt_pk_bf16_f32 v30, v60, v61\n" \
"v_mfma_f32_16x16x32_bf16 v[96:99], a[96:99], a[80:83], v[96:99]\n" \
"buffer_load_dwordx4 v234, s[56:59], s81 offen offset:1024 sc1 lds\n" \
"v_mfma_f32_16x16x32_bf16 v[96:99], a[100:103], a[84:87], v[96:99]\n" \
"s_sub_u32 s85, s85, 32768\n" \
"s_cselect_b32 s85, 131072, s85\n" \
"ds_read_b64_tr_b16 a[122:123], v215 offset:18944\n" \
"v_mfma_f32_16x16x32_bf16 v[100:103], a[104:107], a[80:83], v[100:103]\n" \
"s_add_u32 s48, s48, 0x1000\n" \
"ds_read_b64_tr_b16 a[124:125], v215 offset:25088\n" \
"v_add_u32 v213, s85, v211\n" \
"v_mfma_f32_16x16x32_bf16 v[100:103], a[108:111], a[84:87], v[100:103]\n" \
"s_waitcnt lgkmcnt(3)\n" \
"s_sub_u32 s86, s86, 32768\n" \
"s_cselect_b32 s86, 131072, s86\n" \
"ds_read_b64_tr_b16 a[126:127], v215 offset:27136\n" \
"v_mfma_f32_16x16x32_bf16 v[116:119], a[104:107], a[88:91], v[116:119]\n" \
"buffer_load_dwordx4 v234, s[56:59], s81 offen offset:2048 sc1 lds\n" \
"v_mfma_f32_16x16x32_bf16 v[116:119], a[108:111], a[92:95], v[116:119]\n" \
"v_add_u32 v214, s85, v212\n" \
"v_xor_b32 v215, 32, v214\n" \
"v_mfma_f32_16x16x32_bf16 v[112:115], a[96:99], a[88:91], v[112:115]\n" \
"buffer_load_dwordx4 v234, s[56:59], s81 offen offset:3072 sc1 lds\n" \
"v_mfma_f32_16x16x32_bf16 v[112:115], a[100:103], a[92:95], v[112:115]\n" \
"s_waitcnt vmcnt(26) lgkmcnt(0)\n" \
"s_barrier\n" \
"v_mfma_f32_16x16x32_bf16 v[72:75], a[112:115], a[64:67], v[72:75]\n" \
"ds_read_b64_tr_b16 a[96:97], v214 offset:16384\n" \
"ds_read_b64_tr_b16 a[98:99], v214 offset:18432\n" \
"v_mfma_f32_16x16x32_bf16 v[72:75], a[116:119], a[68:71], v[72:75]\n" \
"s_add_u32 m0, m0, 0x4000\n" \
"ds_read_b64_tr_b16 a[100:101], v214 offset:24576\n" \
"ds_read_b64_tr_b16 a[102:103], v214 offset:26624\n" \
"v_mfma_f32_16x16x32_bf16 v[76:79], a[120:123], a[64:67], v[76:79]\n" \
"buffer_load_dwordx4 v216, s[36:39], s82 offen offset:0 lds\n" \
"v_mfma_f32_16x16x32_bf16 v[76:79], a[124:127], a[68:71], v[76:79]\n" \
"ds_read_b128 a[64:67], v213 offset:0\n" \
"v_cvt_pk_bf16_f32 v31, v62, v63\n" \
"v_mfma_f32_16x16x32_bf16 v[92:95], a[120:123], a[72:75], v[92:95]\n" \
"ds_read_b128 a[68:71], v213 offset:2048\n" \
"v_mfma_f32_16x16x32_bf16 v[92:95], a[124:127], a[76:79], v[92:95]\n" \
"ds_read_b64_tr_b16 a[104:105], v215 offset:16384\n" \
"ds_read_b64_tr_b16 a[106:107], v215 offset:18432\n" \
"v_mfma_f32_16x16x32_bf16 v[88:91], a[112:115], a[72:75], v[88:91]\n" \
"buffer_load_dwordx4 v217, s[36:39], s82 offen offset:1024 lds\n" \
"v_mfma_f32_16x16x32_bf16 v[88:91], a[116:119], a[76:79], v[88:91]\n" \
"ds_read_b128 a[72:75], v213 offset:256\n" \
"v_mfma_f32_16x16x32_bf16 v[104:107], a[112:115], a[80:83], v[104:107]\n" \
"ds_read_b128 a[76:79], v213 offset:2304\n" \
"v_mfma_f32_16x16x32_bf16 v[104:107], a[116:119], a[84:87], v[104:107]\n" \
"ds_read_b64_tr_b16 a[108:109], v215 offset:24576\n" \
"ds_read_b64_tr_b16 a[110:111], v215 offset:26624\n" \
"v_mfma_f32_16x16x32_bf16 v[108:111], a[120:123], a[80:83], v[108:111]\n" \
"buffer_load_dwordx4 v218, s[36:39], s82 offen offset:2048 lds\n" \
"v_mfma_f32_16x16x32_bf16 v[108:111], a[124:127], a[84:87], v[108:111]\n" \
"ds_read_b128 a[80:83], v213 offset:4096\n" \
"v_mfma_f32_16x16x32_bf16 v[124:127], a[120:123], a[88:91], v[124:127]\n" \
"s_waitcnt lgkmcnt(4)\n" \
"buffer_store_dwordx4 v[24:27], v234, s[56:59], s71 offen offset:2048 sc1\n" \
"v_mfma_f32_16x16x32_bf16 v[124:127], a[124:127], a[92:95], v[124:127]\n" \
"ds_read_b128 a[84:87], v213 offset:6144\n" \
"v_mfma_f32_16x16x32_bf16 v[120:123], a[112:115], a[88:91], v[120:123]\n" \
"buffer_load_dwordx4 v219, s[36:39], s82 offen offset:3072 lds\n" \
"v_mfma_f32_16x16x32_bf16 v[120:123], a[116:119], a[92:95], v[120:123]\n" \
"buffer_store_dwordx4 v[28:31], v234, s[56:59], s71 offen offset:3072 sc1\n" \
"v_mfma_f32_16x16x32_bf16 v[64:67], a[96:99], a[64:67], v[64:67]\n" \
"s_waitcnt lgkmcnt(1)\n" \
"s_xor_b32 s82, s48, s49\n" \
"ds_read_b128 a[88:91], v213 offset:4352\n" \
"v_mfma_f32_16x16x32_bf16 v[64:67], a[100:103], a[68:71], v[64:67]\n" \
"s_cmp_lt_u32 s48, s50\n" \
"s_cselect_b32 s82, s82, s48\n" \
"ds_read_b128 a[92:95], v213 offset:6400\n" \
"v_mfma_f32_16x16x32_bf16 v[68:71], a[104:107], a[64:67], v[68:71]\n" \
"s_add_u32 s81, s82, s46\n" \
"ds_read_b64_tr_b16 a[112:113], v214 offset:16896\n" \
"v_mfma_f32_16x16x32_bf16 v[68:71], a[108:111], a[68:71], v[68:71]\n" \
"s_add_u32 m0, s86, s79\n" \
"ds_read_b64_tr_b16 a[114:115], v214 offset:18944\n" \
"v_mfma_f32_16x16x32_bf16 v[84:87], a[104:107], a[72:75], v[84:87]\n" \
"buffer_load_dwordx4 v234, s[56:59], s81 offen offset:0 sc1 lds\n" \
"v_mfma_f32_16x16x32_bf16 v[84:87], a[108:111], a[76:79], v[84:87]\n" \
"ds_read_b64_tr_b16 a[116:117], v214 offset:25088\n" \
"v_mfma_f32_16x16x32_bf16 v[80:83], a[96:99], a[72:75], v[80:83]\n" \
"s_lshl_b32 s82, s82, 5\n" \
"ds_read_b64_tr_b16 a[118:119], v214 offset:27136\n" \
"v_mfma_f32_16x16x32_bf16 v[80:83], a[100:103], a[76:79], v[80:83]\n" \
"s_waitcnt lgkmcnt(3)\n" \
"s_add_u32 s82, s82, s47\n" \
"ds_read_b64_tr_b16 a[120:121], v215 offset:16896\n" \
"v_mfma_f32_16x16x32_bf16 v[96:99], a[96:99], a[80:83], v[96:99]\n" \
"buffer_load_dwordx4 v234, s[56:59], s81 offen offset:1024 sc1 lds\n" \
"v_mfma_f32_16x16x32_bf16 v[96:99], a[100:103], a[84:87], v[96:99]\n" \
"s_sub_u32 s85, s85, 32768\n" \
"s_cselect_b32 s85, 131072, s85\n" \
"ds_read_b64_tr_b16 a[122:123], v215 offset:18944\n" \
"v_mfma_f32_16x16x32_bf16 v[100:103], a[104:107], a[80:83], v[100:103]\n" \
"s_add_u32 s48, s48, 0x1000\n" \
"ds_read_b64_tr_b16 a[124:125], v215 offset:25088\n" \
"v_add_u32 v213, s85, v211\n" \
"v_mfma_f32_16x16x32_bf16 v[100:103], a[108:111], a[84:87], v[100:103]\n" \
"s_sub_u32 s86, s86, 32768\n" \
"s_cselect_b32 s86, 131072, s86\n" \
"ds_read_b64_tr_b16 a[126:127], v215 offset:27136\n" \
"v_mfma_f32_16x16x32_bf16 v[116:119], a[104:107], a[88:91], v[116:119]\n" \
"buffer_load_dwordx4 v234, s[56:59], s81 offen offset:2048 sc1 lds\n" \
"v_mfma_f32_16x16x32_bf16 v[116:119], a[108:111], a[92:95], v[116:119]\n" \
"v_add_u32 v214, s85, v212\n" \
"v_xor_b32 v215, 32, v214\n" \
"v_mfma_f32_16x16x32_bf16 v[112:115], a[96:99], a[88:91], v[112:115]\n" \
"buffer_load_dwordx4 v234, s[56:59], s81 offen offset:3072 sc1 lds\n" \
"v_mfma_f32_16x16x32_bf16 v[112:115], a[100:103], a[92:95], v[112:115]\n" \
"s_waitcnt vmcnt(27) lgkmcnt(0)\n" \
"s_barrier\n" \
"v_mfma_f32_16x16x32_bf16 v[72:75], a[112:115], a[64:67], v[72:75]\n" \
"ds_read_b64_tr_b16 a[96:97], v214 offset:16384\n" \
"ds_read_b64_tr_b16 a[98:99], v214 offset:18432\n" \
"v_mfma_f32_16x16x32_bf16 v[72:75], a[116:119], a[68:71], v[72:75]\n" \
"s_add_u32 m0, m0, 0x4000\n" \
"ds_read_b64_tr_b16 a[100:101], v214 offset:24576\n" \
"ds_read_b64_tr_b16 a[102:103], v214 offset:26624\n" \
"v_mfma_f32_16x16x32_bf16 v[76:79], a[120:123], a[64:67], v[76:79]\n" \
"buffer_load_dwordx4 v216, s[36:39], s82 offen offset:0 lds\n" \
"v_mfma_f32_16x16x32_bf16 v[76:79], a[124:127], a[68:71], v[76:79]\n" \
"ds_read_b128 a[64:67], v213 offset:0\n" \
"v_mfma_f32_16x16x32_bf16 v[92:95], a[120:123], a[72:75], v[92:95]\n" \
"ds_read_b128 a[68:71], v213 offset:2048\n" \
"v_mfma_f32_16x16x32_bf16 v[92:95], a[124:127], a[76:79], v[92:95]\n" \
"ds_read_b64_tr_b16 a[104:105], v215 offset:16384\n" \
"ds_read_b64_tr_b16 a[106:107], v215 offset:18432\n" \
"v_mfma_f32_16x16x32_bf16 v[88:91], a[112:115], a[72:75], v[88:91]\n" \
"buffer_load_dwordx4 v217, s[36:39], s82 offen offset:1024 lds\n" \
"v_mfma_f32_16x16x32_bf16 v[88:91], a[116:119], a[76:79], v[88:91]\n" \
"ds_read_b128 a[72:75], v213 offset:256\n" \
"v_mfma_f32_16x16x32_bf16 v[104:107], a[112:115], a[80:83], v[104:107]\n" \
"ds_read_b128 a[76:79], v213 offset:2304\n" \
"v_mfma_f32_16x16x32_bf16 v[104:107], a[116:119], a[84:87], v[104:107]\n" \
"ds_read_b64_tr_b16 a[108:109], v215 offset:24576\n" \
"ds_read_b64_tr_b16 a[110:111], v215 offset:26624\n" \
"v_mfma_f32_16x16x32_bf16 v[108:111], a[120:123], a[80:83], v[108:111]\n" \
"buffer_load_dwordx4 v218, s[36:39], s82 offen offset:2048 lds\n" \
"v_mfma_f32_16x16x32_bf16 v[108:111], a[124:127], a[84:87], v[108:111]\n" \
"ds_read_b128 a[80:83], v213 offset:4096\n" \
"v_mfma_f32_16x16x32_bf16 v[124:127], a[120:123], a[88:91], v[124:127]\n" \
"s_waitcnt lgkmcnt(4)\n" \
"v_mfma_f32_16x16x32_bf16 v[124:127], a[124:127], a[92:95], v[124:127]\n" \
"ds_read_b128 a[84:87], v213 offset:6144\n" \
"v_mfma_f32_16x16x32_bf16 v[120:123], a[112:115], a[88:91], v[120:123]\n" \
"buffer_load_dwordx4 v219, s[36:39], s82 offen offset:3072 lds\n" \
"v_mfma_f32_16x16x32_bf16 v[120:123], a[116:119], a[92:95], v[120:123]\n" \
"v_mfma_f32_16x16x32_bf16 v[64:67], a[96:99], a[64:67], v[64:67]\n" \
"s_waitcnt lgkmcnt(1)\n" \
"s_xor_b32 s82, s48, s49\n" \
"ds_read_b128 a[88:91], v213 offset:4352\n" \
"v_mfma_f32_16x16x32_bf16 v[64:67], a[100:103], a[68:71], v[64:67]\n" \
"s_cmp_lt_u32 s48, s50\n" \
"s_cselect_b32 s82, s82, s48\n" \
"ds_read_b128 a[92:95], v213 offset:6400\n" \
"v_mfma_f32_16x16x32_bf16 v[68:71], a[104:107], a[64:67], v[68:71]\n" \
"s_add_u32 s81, s82, s46\n" \
"ds_read_b64_tr_b16 a[112:113], v214 offset:16896\n" \
"v_mfma_f32_16x16x32_bf16 v[68:71], a[108:111], a[68:71], v[68:71]\n" \
"s_add_u32 m0, s86, s79\n" \
"ds_read_b64_tr_b16 a[114:115], v214 offset:18944\n" \
"v_mfma_f32_16x16x32_bf16 v[84:87], a[104:107], a[72:75], v[84:87]\n" \
"buffer_load_dwordx4 v234, s[56:59], s81 offen offset:0 sc1 lds\n" \
"v_mfma_f32_16x16x32_bf16 v[84:87], a[108:111], a[76:79], v[84:87]\n" \
"ds_read_b64_tr_b16 a[116:117], v214 offset:25088\n" \
"v_mfma_f32_16x16x32_bf16 v[80:83], a[96:99], a[72:75], v[80:83]\n" \
"s_lshl_b32 s82, s82, 5\n" \
"ds_read_b64_tr_b16 a[118:119], v214 offset:27136\n" \
"v_mfma_f32_16x16x32_bf16 v[80:83], a[100:103], a[76:79], v[80:83]\n" \
"s_waitcnt lgkmcnt(3)\n" \
"s_add_u32 s82, s82, s47\n" \
"ds_read_b64_tr_b16 a[120:121], v215 offset:16896\n" \
"v_mfma_f32_16x16x32_bf16 v[96:99], a[96:99], a[80:83], v[96:99]\n" \
"buffer_load_dwordx4 v234, s[56:59], s81 offen offset:1024 sc1 lds\n" \
"v_mfma_f32_16x16x32_bf16 v[96:99], a[100:103], a[84:87], v[96:99]\n" \
"s_sub_u32 s85, s85, 32768\n" \
"s_cselect_b32 s85, 131072, s85\n" \
"ds_read_b64_tr_b16 a[122:123], v215 offset:18944\n" \
"v_mfma_f32_16x16x32_bf16 v[100:103], a[104:107], a[80:83], v[100:103]\n" \
"s_add_u32 s48, s48, 0x1000\n" \
"ds_read_b64_tr_b16 a[124:125], v215 offset:25088\n" \
"v_add_u32 v213, s85, v211\n" \
"v_mfma_f32_16x16x32_bf16 v[100:103], a[108:111], a[84:87], v[100:103]\n" \
"s_sub_u32 s86, s86, 32768\n" \
"s_cselect_b32 s86, 131072, s86\n" \
"ds_read_b64_tr_b16 a[126:127], v215 offset:27136\n" \
"v_mfma_f32_16x16x32_bf16 v[116:119], a[104:107], a[88:91], v[116:119]\n" \
"buffer_load_dwordx4 v234, s[56:59], s81 offen offset:2048 sc1 lds\n" \
"v_mfma_f32_16x16x32_bf16 v[116:119], a[108:111], a[92:95], v[116:119]\n" \
"v_add_u32 v214, s85, v212\n" \
"v_xor_b32 v215, 32, v214\n" \
"v_mfma_f32_16x16x32_bf16 v[112:115], a[96:99], a[88:91], v[112:115]\n" \
"buffer_load_dwordx4 v234, s[56:59], s81 offen offset:3072 sc1 lds\n" \
"v_mfma_f32_16x16x32_bf16 v[112:115], a[100:103], a[92:95], v[112:115]\n" \
"s_waitcnt vmcnt(24) lgkmcnt(0)\n" \
"s_barrier\n" \
"v_mfma_f32_16x16x32_bf16 v[72:75], a[112:115], a[64:67], v[72:75]\n" \
"ds_read_b64_tr_b16 a[96:97], v214 offset:16384\n" \
"ds_read_b64_tr_b16 a[98:99], v214 offset:18432\n" \
"v_mfma_f32_16x16x32_bf16 v[72:75], a[116:119], a[68:71], v[72:75]\n" \
"s_add_u32 m0, m0, 0x4000\n" \
"ds_read_b64_tr_b16 a[100:101], v214 offset:24576\n" \
"ds_read_b64_tr_b16 a[102:103], v214 offset:26624\n" \
"v_mfma_f32_16x16x32_bf16 v[76:79], a[120:123], a[64:67], v[76:79]\n" \
"buffer_load_dwordx4 v216, s[36:39], s82 offen offset:0 lds\n" \
"v_mfma_f32_16x16x32_bf16 v[76:79], a[124:127], a[68:71], v[76:79]\n" \
"ds_read_b128 a[64:67], v213 offset:0\n" \
"v_mfma_f32_16x16x32_bf16 v[92:95], a[120:123], a[72:75], v[92:95]\n" \
"ds_read_b128 a[68:71], v213 offset:2048\n" \
"v_mfma_f32_16x16x32_bf16 v[92:95], a[124:127], a[76:79], v[92:95]\n" \
"ds_read_b64_tr_b16 a[104:105], v215 offset:16384\n" \
"ds_read_b64_tr_b16 a[106:107], v215 offset:18432\n" \
"v_mfma_f32_16x16x32_bf16 v[88:91], a[112:115], a[72:75], v[88:91]\n" \
"buffer_load_dwordx4 v217, s[36:39], s82 offen offset:1024 lds\n" \
"v_mfma_f32_16x16x32_bf16 v[88:91], a[116:119], a[76:79], v[88:91]\n" \
"ds_read_b128 a[72:75], v213 offset:256\n" \
"v_mfma_f32_16x16x32_bf16 v[104:107], a[112:115], a[80:83], v[104:107]\n" \
"ds_read_b128 a[76:79], v213 offset:2304\n" \
"v_mfma_f32_16x16x32_bf16 v[104:107], a[116:119], a[84:87], v[104:107]\n" \
"ds_read_b64_tr_b16 a[108:109], v215 offset:24576\n" \
"ds_read_b64_tr_b16 a[110:111], v215 offset:26624\n" \
"v_mfma_f32_16x16x32_bf16 v[108:111], a[120:123], a[80:83], v[108:111]\n" \
"buffer_load_dwordx4 v218, s[36:39], s82 offen offset:2048 lds\n" \
"v_mfma_f32_16x16x32_bf16 v[108:111], a[124:127], a[84:87], v[108:111]\n" \
"ds_read_b128 a[80:83], v213 offset:4096\n" \
"v_mfma_f32_16x16x32_bf16 v[124:127], a[120:123], a[88:91], v[124:127]\n" \
"s_waitcnt lgkmcnt(4)\n" \
"v_mfma_f32_16x16x32_bf16 v[124:127], a[124:127], a[92:95], v[124:127]\n" \
"ds_read_b128 a[84:87], v213 offset:6144\n" \
"v_mfma_f32_16x16x32_bf16 v[120:123], a[112:115], a[88:91], v[120:123]\n" \
"buffer_load_dwordx4 v219, s[36:39], s82 offen offset:3072 lds\n" \
"v_mfma_f32_16x16x32_bf16 v[120:123], a[116:119], a[92:95], v[120:123]\n" \
"v_mfma_f32_16x16x32_bf16 v[64:67], a[96:99], a[64:67], v[64:67]\n" \
"s_waitcnt lgkmcnt(1)\n" \
"s_xor_b32 s82, s48, s49\n" \
"ds_read_b128 a[88:91], v213 offset:4352\n" \
"v_mfma_f32_16x16x32_bf16 v[64:67], a[100:103], a[68:71], v[64:67]\n" \
"s_cmp_lt_u32 s48, s50\n" \
"s_cselect_b32 s82, s82, s48\n" \
"ds_read_b128 a[92:95], v213 offset:6400\n" \
"v_mfma_f32_16x16x32_bf16 v[68:71], a[104:107], a[64:67], v[68:71]\n" \
"s_add_u32 s81, s82, s46\n" \
"ds_read_b64_tr_b16 a[112:113], v214 offset:16896\n" \
"v_mfma_f32_16x16x32_bf16 v[68:71], a[108:111], a[68:71], v[68:71]\n" \
"s_add_u32 m0, s86, s79\n" \
"ds_read_b64_tr_b16 a[114:115], v214 offset:18944\n" \
"v_mfma_f32_16x16x32_bf16 v[84:87], a[104:107], a[72:75], v[84:87]\n" \
"buffer_load_dwordx4 v234, s[56:59], s81 offen offset:0 sc1 lds\n" \
"v_mfma_f32_16x16x32_bf16 v[84:87], a[108:111], a[76:79], v[84:87]\n" \
"ds_read_b64_tr_b16 a[116:117], v214 offset:25088\n" \
"v_mfma_f32_16x16x32_bf16 v[80:83], a[96:99], a[72:75], v[80:83]\n" \
"s_lshl_b32 s82, s82, 5\n" \
"ds_read_b64_tr_b16 a[118:119], v214 offset:27136\n" \
"v_mfma_f32_16x16x32_bf16 v[80:83], a[100:103], a[76:79], v[80:83]\n" \
"s_waitcnt lgkmcnt(3)\n" \
"s_add_u32 s82, s82, s47\n" \
"ds_read_b64_tr_b16 a[120:121], v215 offset:16896\n" \
"v_mfma_f32_16x16x32_bf16 v[96:99], a[96:99], a[80:83], v[96:99]\n" \
"buffer_load_dwordx4 v234, s[56:59], s81 offen offset:1024 sc1 lds\n" \
"v_mfma_f32_16x16x32_bf16 v[96:99], a[100:103], a[84:87], v[96:99]\n" \
"s_sub_u32 s85, s85, 32768\n" \
"s_cselect_b32 s85, 131072, s85\n" \
"ds_read_b64_tr_b16 a[122:123], v215 offset:18944\n" \
"v_mfma_f32_16x16x32_bf16 v[100:103], a[104:107], a[80:83], v[100:103]\n" \
"s_add_u32 s48, s48, 0x1000\n" \
"ds_read_b64_tr_b16 a[124:125], v215 offset:25088\n" \
"v_add_u32 v213, s85, v211\n" \
"v_mfma_f32_16x16x32_bf16 v[100:103], a[108:111], a[84:87], v[100:103]\n" \
"s_sub_u32 s86, s86, 32768\n" \
"s_cselect_b32 s86, 131072, s86\n" \
"ds_read_b64_tr_b16 a[126:127], v215 offset:27136\n" \
"v_mfma_f32_16x16x32_bf16 v[116:119], a[104:107], a[88:91], v[116:119]\n" \
"buffer_load_dwordx4 v234, s[56:59], s81 offen offset:2048 sc1 lds\n" \
"v_mfma_f32_16x16x32_bf16 v[116:119], a[108:111], a[92:95], v[116:119]\n" \
"v_add_u32 v214, s85, v212\n" \
"v_xor_b32 v215, 32, v214\n" \
"v_mfma_f32_16x16x32_bf16 v[112:115], a[96:99], a[88:91], v[112:115]\n" \
"buffer_load_dwordx4 v234, s[56:59], s81 offen offset:3072 sc1 lds\n" \
"v_mfma_f32_16x16x32_bf16 v[112:115], a[100:103], a[92:95], v[112:115]\n" \
"s_waitcnt vmcnt(20) lgkmcnt(0)\n" \
"s_barrier\n" \
"buffer_store_dword v230, v231, s[56:59], s64 offen sc1\n" \
"v_mfma_f32_16x16x32_bf16 v[72:75], a[112:115], a[64:67], v[72:75]\n" \
"ds_read_b64_tr_b16 a[96:97], v214 offset:16384\n" \
"ds_read_b64_tr_b16 a[98:99], v214 offset:18432\n" \
"v_mfma_f32_16x16x32_bf16 v[72:75], a[116:119], a[68:71], v[72:75]\n" \
"s_add_u32 m0, m0, 0x4000\n" \
"ds_read_b64_tr_b16 a[100:101], v214 offset:24576\n" \
"ds_read_b64_tr_b16 a[102:103], v214 offset:26624\n" \
"v_mfma_f32_16x16x32_bf16 v[76:79], a[120:123], a[64:67], v[76:79]\n" \
"buffer_load_dwordx4 v216, s[36:39], s82 offen offset:0 lds\n" \
"v_mfma_f32_16x16x32_bf16 v[76:79], a[124:127], a[68:71], v[76:79]\n" \
"ds_read_b128 a[64:67], v213 offset:0\n" \
"v_mfma_f32_16x16x32_bf16 v[92:95], a[120:123], a[72:75], v[92:95]\n" \
"ds_read_b128 a[68:71], v213 offset:2048\n" \
"v_mfma_f32_16x16x32_bf16 v[92:95], a[124:127], a[76:79], v[92:95]\n" \
"ds_read_b64_tr_b16 a[104:105], v215 offset:16384\n" \
"ds_read_b64_tr_b16 a[106:107], v215 offset:18432\n" \
"v_mfma_f32_16x16x32_bf16 v[88:91], a[112:115], a[72:75], v[88:91]\n" \
"buffer_load_dwordx4 v217, s[36:39], s82 offen offset:1024 lds\n" \
"v_mfma_f32_16x16x32_bf16 v[88:91], a[116:119], a[76:79], v[88:91]\n" \
"ds_read_b128 a[72:75], v213 offset:256\n" \
"v_mfma_f32_16x16x32_bf16 v[104:107], a[112:115], a[80:83], v[104:107]\n" \
"ds_read_b128 a[76:79], v213 offset:2304\n" \
"v_mfma_f32_16x16x32_bf16 v[104:107], a[116:119], a[84:87], v[104:107]\n" \
"ds_read_b64_tr_b16 a[108:109], v215 offset:24576\n" \
"ds_read_b64_tr_b16 a[110:111], v215 offset:26624\n" \
"v_mfma_f32_16x16x32_bf16 v[108:111], a[120:123], a[80:83], v[108:111]\n" \
"buffer_load_dwordx4 v218, s[36:39], s82 offen offset:2048 lds\n" \
"v_mfma_f32_16x16x32_bf16 v[108:111], a[124:127], a[84:87], v[108:111]\n" \
"ds_read_b128 a[80:83], v213 offset:4096\n" \
"v_mfma_f32_16x16x32_bf16 v[124:127], a[120:123], a[88:91], v[124:127]\n" \
"s_waitcnt lgkmcnt(4)\n" \
"v_mfma_f32_16x16x32_bf16 v[124:127], a[124:127], a[92:95], v[124:127]\n" \
"ds_read_b128 a[84:87], v213 offset:6144\n" \
"v_mfma_f32_16x16x32_bf16 v[120:123], a[112:115], a[88:91], v[120:123]\n" \
"buffer_load_dwordx4 v219, s[36:39], s82 offen offset:3072 lds\n" \
"v_mfma_f32_16x16x32_bf16 v[120:123], a[116:119], a[92:95], v[120:123]\n" \
"s_mov_b32 s72, 5\n" \
"Lo64_%=:\n" \
"v_mfma_f32_16x16x32_bf16 v[64:67], a[96:99], a[64:67], v[64:67]\n" \
"s_waitcnt lgkmcnt(1)\n" \
"s_xor_b32 s82, s48, s49\n" \
"ds_read_b128 a[88:91], v213 offset:4352\n" \
"Lpg11721_%=:\n" \
"s_cbranch_execz Lcb1_%=\n" \
"v_mfma_f32_16x16x32_bf16 v[64:67], a[100:103], a[68:71], v[64:67]\n" \
"s_cmp_lt_u32 s48, s50\n" \
"s_cselect_b32 s82, s82, s48\n" \
"ds_read_b128 a[92:95], v213 offset:6400\n" \
"v_mfma_f32_16x16x32_bf16 v[68:71], a[104:107], a[64:67], v[68:71]\n" \
"s_add_u32 s81, s82, s46\n" \
"ds_read_b64_tr_b16 a[112:113], v214 offset:16896\n" \
"v_mfma_f32_16x16x32_bf16 v[68:71], a[108:111], a[68:71], v[68:71]\n" \
"s_add_u32 m0, s86, s79\n" \
"ds_read_b64_tr_b16 a[114:115], v214 offset:18944\n" \
"v_mfma_f32_16x16x32_bf16 v[84:87], a[104:107], a[72:75], v[84:87]\n" \
"buffer_load_dwordx4 v234, s[56:59], s81 offen offset:0 sc1 lds\n" \
"v_mfma_f32_16x16x32_bf16 v[84:87], a[108:111], a[76:79], v[84:87]\n" \
"ds_read_b64_tr_b16 a[116:117], v214 offset:25088\n" \
"v_mfma_f32_16x16x32_bf16 v[80:83], a[96:99], a[72:75], v[80:83]\n" \
"s_lshl_b32 s82, s82, 5\n" \
"ds_read_b64_tr_b16 a[118:119], v214 offset:27136\n" \
"v_mfma_f32_16x16x32_bf16 v[80:83], a[100:103], a[76:79], v[80:83]\n" \
"s_waitcnt lgkmcnt(3)\n" \
"s_add_u32 s82, s82, s47\n" \
"ds_read_b64_tr_b16 a[120:121], v215 offset:16896\n" \
"v_mfma_f32_16x16x32_bf16 v[96:99], a[96:99], a[80:83], v[96:99]\n" \
"buffer_load_dwordx4 v234, s[56:59], s81 offen offset:1024 sc1 lds\n" \
"v_mfma_f32_16x16x32_bf16 v[96:99], a[100:103], a[84:87], v[96:99]\n" \
"s_sub_u32 s85, s85, 32768\n" \
"s_cselect_b32 s85, 131072, s85\n" \
"ds_read_b64_tr_b16 a[122:123], v215 offset:18944\n" \
"v_mfma_f32_16x16x32_bf16 v[100:103], a[104:107], a[80:83], v[100:103]\n" \
"s_add_u32 s48, s48, 0x1000\n" \
"ds_read_b64_tr_b16 a[124:125], v215 offset:25088\n" \
"v_add_u32 v213, s85, v211\n" \
"v_mfma_f32_16x16x32_bf16 v[100:103], a[108:111], a[84:87], v[100:103]\n" \
"s_sub_u32 s86, s86, 32768\n" \
"s_cselect_b32 s86, 131072, s86\n" \
"ds_read_b64_tr_b16 a[126:127], v215 offset:27136\n" \
"v_mfma_f32_16x16x32_bf16 v[116:119], a[104:107], a[88:91], v[116:119]\n" \
"buffer_load_dwordx4 v234, s[56:59], s81 offen offset:2048 sc1 lds\n" \
"v_mfma_f32_16x16x32_bf16 v[116:119], a[108:111], a[92:95], v[116:119]\n" \
"v_add_u32 v214, s85, v212\n" \
"v_xor_b32 v215, 32, v214\n" \
"v_mfma_f32_16x16x32_bf16 v[112:115], a[96:99], a[88:91], v[112:115]\n" \
"buffer_load_dwordx4 v234, s[56:59], s81 offen offset:3072 sc1 lds\n" \
"v_mfma_f32_16x16x32_bf16 v[112:115], a[100:103], a[92:95], v[112:115]\n" \
"s_waitcnt vmcnt(20) lgkmcnt(0)\n" \
"s_barrier\n" \
"v_mfma_f32_16x16x32_bf16 v[72:75], a[112:115], a[64:67], v[72:75]\n" \
"ds_read_b64_tr_b16 a[96:97], v214 offset:16384\n" \
"ds_read_b64_tr_b16 a[98:99], v214 offset:18432\n" \
"v_mfma_f32_16x16x32_bf16 v[72:75], a[116:119], a[68:71], v[72:75]\n" \
"s_add_u32 m0, m0, 0x4000\n" \
"ds_read_b64_tr_b16 a[100:101], v214 offset:24576\n" \
"ds_read_b64_tr_b16 a[102:103], v214 offset:26624\n" \
"v_mfma_f32_16x16x32_bf16 v[76:79], a[120:123], a[64:67], v[76:79]\n" \
"buffer_load_dwordx4 v216, s[36:39], s82 offen offset:0 lds\n" \
"v_mfma_f32_16x16x32_bf16 v[76:79], a[124:127], a[68:71], v[76:79]\n" \
"ds_read_b128 a[64:67], v213 offset:0\n" \
"v_mfma_f32_16x16x32_bf16 v[92:95], a[120:123], a[72:75], v[92:95]\n" \
"ds_read_b128 a[68:71], v213 offset:2048\n" \
"v_mfma_f32_16x16x32_bf16 v[92:95], a[124:127], a[76:79], v[92:95]\n" \
"ds_read_b64_tr_b16 a[104:105], v215 offset:16384\n" \
"ds_read_b64_tr_b16 a[106:107], v215 offset:18432\n" \
"v_mfma_f32_16x16x32_bf16 v[88:91], a[112:115], a[72:75], v[88:91]\n" \
"buffer_load_dwordx4 v217, s[36:39], s82 offen offset:1024 lds\n" \
"v_mfma_f32_16x16x32_bf16 v[88:91], a[116:119], a[76:79], v[88:91]\n" \
"ds_read_b128 a[72:75], v213 offset:256\n" \
"v_mfma_f32_16x16x32_bf16 v[104:107], a[112:115], a[80:83], v[104:107]\n" \
"ds_read_b128 a[76:79], v213 offset:2304\n" \
"v_mfma_f32_16x16x32_bf16 v[104:107], a[116:119], a[84:87], v[104:107]\n" \
"ds_read_b64_tr_b16 a[108:109], v215 offset:24576\n" \
"ds_read_b64_tr_b16 a[110:111], v215 offset:26624\n" \
"v_mfma_f32_16x16x32_bf16 v[108:111], a[120:123], a[80:83], v[108:111]\n" \
"buffer_load_dwordx4 v218, s[36:39], s82 offen offset:2048 lds\n" \
"v_mfma_f32_16x16x32_bf16 v[108:111], a[124:127], a[84:87], v[108:111]\n" \
"ds_read_b128 a[80:83], v213 offset:4096\n" \
"v_mfma_f32_16x16x32_bf16 v[124:127], a[120:123], a[88:91], v[124:127]\n" \
"s_waitcnt lgkmcnt(4)\n" \
"v_mfma_f32_16x16x32_bf16 v[124:127], a[124:127], a[92:95], v[124:127]\n" \
"s_sub_u32 s72, s72, 1\n" \
"ds_read_b128 a[84:87], v213 offset:6144\n" \
"v_mfma_f32_16x16x32_bf16 v[120:123], a[112:115], a[88:91], v[120:123]\n" \
"buffer_load_dwordx4 v219, s[36:39], s82 offen offset:3072 lds\n" \
"v_mfma_f32_16x16x32_bf16 v[120:123], a[116:119], a[92:95], v[120:123]\n" \
"s_cbranch_scc0 Lo64_%=\n" \
"v_mfma_f32_16x16x32_bf16 v[64:67], a[96:99], a[64:67], v[64:67]\n" \
"s_waitcnt lgkmcnt(1)\n" \
"ds_read_b128 a[88:91], v213 offset:4352\n" \
"v_mfma_f32_16x16x32_bf16 v[64:67], a[100:103], a[68:71], v[64:67]\n" \
"s_add_u32 s48, s86, 32768\n" \
"s_cmp_eq_u32 s48, 163840\n" \
"s_cselect_b32 s48, 0, s48\n" \
"ds_read_b128 a[92:95], v213 offset:6400\n" \
"v_mfma_f32_16x16x32_bf16 v[68:71], a[104:107], a[64:67], v[68:71]\n" \
"ds_read_b64_tr_b16 a[112:113], v214 offset:16896\n" \
"v_mfma_f32_16x16x32_bf16 v[68:71], a[108:111], a[68:71], v[68:71]\n" \
"ds_read_b64_tr_b16 a[114:115], v214 offset:18944\n" \
"v_mfma_f32_16x16x32_bf16 v[84:87], a[104:107], a[72:75], v[84:87]\n" \
"s_add_u32 s49, s48, 32768\n" \
"s_cmp_eq_u32 s49, 163840\n" \
"s_cselect_b32 s49, 0, s49\n" \
"v_mfma_f32_16x16x32_bf16 v[84:87], a[108:111], a[76:79], v[84:87]\n" \
"ds_read_b64_tr_b16 a[116:117], v214 offset:25088\n" \
"v_add_u32 v224, s49, v211\n" \
"v_mfma_f32_16x16x32_bf16 v[80:83], a[96:99], a[72:75], v[80:83]\n" \
"ds_read_b64_tr_b16 a[118:119], v214 offset:27136\n" \
"v_add_u32 v225, s49, v212\n" \
"v_mfma_f32_16x16x32_bf16 v[80:83], a[100:103], a[76:79], v[80:83]\n" \
"s_waitcnt lgkmcnt(3)\n" \
"ds_read_b64_tr_b16 a[120:121], v215 offset:16896\n" \
"v_xor_b32 v226, 32, v225\n" \
"v_mfma_f32_16x16x32_bf16 v[96:99], a[96:99], a[80:83], v[96:99]\n" \
"v_add_u32 v227, s48, v211\n" \
"v_add_u32 v228, s48, v212\n" \
"v_mfma_f32_16x16x32_bf16 v[96:99], a[100:103], a[84:87], v[96:99]\n" \
"s_sub_u32 s85, s85, 32768\n" \
"s_cselect_b32 s85, 131072, s85\n" \
"ds_read_b64_tr_b16 a[122:123], v215 offset:18944\n" \
"v_mfma_f32_16x16x32_bf16 v[100:103], a[104:107], a[80:83], v[100:103]\n" \
"ds_read_b64_tr_b16 a[124:125], v215 offset:25088\n" \
"v_add_u32 v213, s85, v211\n" \
"v_mfma_f32_16x16x32_bf16 v[100:103], a[108:111], a[84:87], v[100:103]\n" \
"ds_read_b64_tr_b16 a[126:127], v215 offset:27136\n" \
"v_xor_b32 v229, 32, v228\n" \
"v_mfma_f32_16x16x32_bf16 v[116:119], a[104:107], a[88:91], v[116:119]\n" \
"v_mfma_f32_16x16x32_bf16 v[116:119], a[108:111], a[92:95], v[116:119]\n" \
"v_add_u32 v214, s85, v212\n" \
"v_xor_b32 v215, 32, v214\n" \
"v_mfma_f32_16x16x32_bf16 v[112:115], a[96:99], a[88:91], v[112:115]\n" \
"v_mfma_f32_16x16x32_bf16 v[112:115], a[100:103], a[92:95], v[112:115]\n" \
"s_waitcnt vmcnt(8) lgkmcnt(0)\n" \
"s_barrier\n" \
"v_mfma_f32_16x16x32_bf16 v[72:75], a[112:115], a[64:67], v[72:75]\n" \
"ds_read_b128 a[0:3], v213 offset:0\n" \
"ds_read_b128 a[4:7], v213 offset:2048\n" \
"v_mfma_f32_16x16x32_bf16 v[72:75], a[116:119], a[68:71], v[72:75]\n" \
"s_sub_u32 s68, s86, 32768\n" \
"s_cselect_b32 s68, 131072, s68\n" \
"ds_read_b64_tr_b16 a[128:129], v214 offset:16384\n" \
"ds_read_b64_tr_b16 a[130:131], v214 offset:18432\n" \
"v_mfma_f32_16x16x32_bf16 v[76:79], a[120:123], a[64:67], v[76:79]\n" \
"ds_read_b64_tr_b16 a[132:133], v214 offset:24576\n" \
"ds_read_b64_tr_b16 a[134:135], v214 offset:26624\n" \
"v_mfma_f32_16x16x32_bf16 v[76:79], a[124:127], a[68:71], v[76:79]\n" \
"s_cmp_lt_u32 s61, 2\n" \
"s_cselect_b32 s68, s86, s68\n" \
"ds_read_b64_tr_b16 a[136:137], v215 offset:16384\n" \
"ds_read_b64_tr_b16 a[138:139], v215 offset:18432\n" \
"v_mfma_f32_16x16x32_bf16 v[92:95], a[120:123], a[72:75], v[92:95]\n" \
"s_and_b32 s50, s61, 1\n" \
"ds_read_b64_tr_b16 a[140:141], v215 offset:24576\n" \
"ds_read_b64_tr_b16 a[142:143], v215 offset:26624\n" \
"v_mfma_f32_16x16x32_bf16 v[92:95], a[124:127], a[76:79], v[92:95]\n" \
"s_mul_i32 s50, s50, 9216\n" \
"ds_read_b64_tr_b16 a[144:145], v214 offset:16896\n" \
"ds_read_b64_tr_b16 a[146:147], v214 offset:18944\n" \
"v_mfma_f32_16x16x32_bf16 v[88:91], a[112:115], a[72:75], v[88:91]\n" \
"s_add_u32 s68, s68, s50\n" \
"ds_read_b64_tr_b16 a[148:149], v214 offset:25088\n" \
"ds_read_b64_tr_b16 a[150:151], v214 offset:27136\n" \
"v_mfma_f32_16x16x32_bf16 v[88:91], a[116:119], a[76:79], v[88:91]\n" \
"s_add_u32 s68, s68, %3\n" \
"ds_read_b64_tr_b16 a[152:153], v215 offset:16896\n" \
"ds_read_b64_tr_b16 a[154:155], v215 offset:18944\n" \
"v_mfma_f32_16x16x32_bf16 v[104:107], a[112:115], a[80:83], v[104:107]\n" \
"ds_read_b64_tr_b16 a[156:157], v215 offset:25088\n" \
"ds_read_b64_tr_b16 a[158:159], v215 offset:27136\n" \
"v_mfma_f32_16x16x32_bf16 v[104:107], a[116:119], a[84:87], v[104:107]\n" \
"ds_read_b128 a[24:27], v213 offset:256\n" \
"ds_read_b128 a[28:31], v213 offset:2304\n" \
"v_mfma_f32_16x16x32_bf16 v[108:111], a[120:123], a[80:83], v[108:111]\n" \
"ds_read_b128 a[8:11], v224 offset:0\n" \
"ds_read_b128 a[12:15], v224 offset:2048\n" \
"v_mfma_f32_16x16x32_bf16 v[108:111], a[124:127], a[84:87], v[108:111]\n" \
"ds_read_b64_tr_b16 a[160:161], v225 offset:16384\n" \
"ds_read_b64_tr_b16 a[162:163], v225 offset:18432\n" \
"v_mfma_f32_16x16x32_bf16 v[124:127], a[120:123], a[88:91], v[124:127]\n" \
"ds_read_b64_tr_b16 a[164:165], v225 offset:24576\n" \
"ds_read_b64_tr_b16 a[166:167], v225 offset:26624\n" \
"v_mfma_f32_16x16x32_bf16 v[124:127], a[124:127], a[92:95], v[124:127]\n" \
"ds_read_b64_tr_b16 a[168:169], v226 offset:16384\n" \
"ds_read_b64_tr_b16 a[170:171], v226 offset:18432\n" \
"v_mfma_f32_16x16x32_bf16 v[120:123], a[112:115], a[88:91], v[120:123]\n" \
"ds_read_b64_tr_b16 a[172:173], v226 offset:24576\n" \
"ds_read_b64_tr_b16 a[174:175], v226 offset:26624\n" \
"v_mfma_f32_16x16x32_bf16 v[120:123], a[116:119], a[92:95], v[120:123]\n" \
"s_waitcnt lgkmcnt(8)\n" \
"ds_read_b64_tr_b16 a[176:177], v225 offset:16896\n" \
"ds_read_b64_tr_b16 a[178:179], v225 offset:18944\n" \
"v_mfma_f32_16x16x32_bf16 v[64:67], a[128:131], a[0:3], v[64:67]\n" \
"ds_read_b64_tr_b16 a[180:181], v225 offset:25088\n" \
"ds_read_b64_tr_b16 a[182:183], v225 offset:27136\n" \
"v_mfma_f32_16x16x32_bf16 v[64:67], a[132:135], a[4:7], v[64:67]\n" \
"ds_read_b64_tr_b16 a[184:185], v226 offset:16896\n" \
"ds_read_b64_tr_b16 a[186:187], v226 offset:18944\n" \
"v_mfma_f32_16x16x32_bf16 v[68:71], a[136:139], a[0:3], v[68:71]\n" \
"ds_read_b64_tr_b16 a[188:189], v226 offset:25088\n" \
"ds_read_b64_tr_b16 a[190:191], v226 offset:27136\n" \
"v_mfma_f32_16x16x32_bf16 v[68:71], a[140:143], a[4:7], v[68:71]\n" \
"v_lshrrev_b32 v132, 4, v234\n" \
"v_and_b32 v133, 15, v132\n" \
"v_mfma_f32_16x16x32_bf16 v[72:75], a[144:147], a[0:3], v[72:75]\n" \
"v_mul_u32_u24 v128, 144, v133\n" \
"v_lshrrev_b32 v133, 4, v132\n" \
"v_mfma_f32_16x16x32_bf16 v[72:75], a[148:151], a[4:7], v[72:75]\n" \
"v_lshl_add_u32 v128, v133, 3, v128\n" \
"v_add_u32 v128, s68, v128\n" \
"v_mfma_f32_16x16x32_bf16 v[76:79], a[152:155], a[0:3], v[76:79]\n" \
"v_lshrrev_b32 v133, 3, v132\n" \
"v_and_b32 v134, 7, v132\n" \
"v_mfma_f32_16x16x32_bf16 v[76:79], a[156:159], a[4:7], v[76:79]\n" \
"v_mul_u32_u24 v129, 144, v133\n" \
"v_lshl_add_u32 v129, v134, 4, v129\n" \
"v_mfma_f32_16x16x32_bf16 v[80:83], a[128:131], a[24:27], v[80:83]\n" \
"v_add_u32 v129, s68, v129\n" \
"v_lshlrev_b32 v134, 4, v134\n" \
"v_mfma_f32_16x16x32_bf16 v[80:83], a[132:135], a[28:31], v[80:83]\n" \
"v_lshl_add_u32 v130, v133, 13, v134\n" \
"v_mfma_f32_16x16x32_bf16 v[84:87], a[136:139], a[24:27], v[84:87]\n" \
"v_mfma_f32_16x16x32_bf16 v[84:87], a[140:143], a[28:31], v[84:87]\n" \
"v_mfma_f32_16x16x32_bf16 v[88:91], a[144:147], a[24:27], v[88:91]\n" \
"s_waitcnt vmcnt(0)\n" \
"s_barrier\n" \
"v_mfma_f32_16x16x32_bf16 v[88:91], a[148:151], a[28:31], v[88:91]\n" \
"ds_read_b128 a[32:35], v224 offset:256\n" \
"ds_read_b128 a[36:39], v224 offset:2304\n" \
"v_mfma_f32_16x16x32_bf16 v[92:95], a[152:155], a[24:27], v[92:95]\n" \
"ds_read_b128 a[16:19], v227 offset:0\n" \
"ds_read_b128 a[20:23], v227 offset:2048\n" \
"v_mfma_f32_16x16x32_bf16 v[92:95], a[156:159], a[28:31], v[92:95]\n" \
"s_waitcnt lgkmcnt(4)\n" \
"ds_read_b64_tr_b16 a[192:193], v228 offset:16384\n" \
"ds_read_b64_tr_b16 a[194:195], v228 offset:18432\n" \
"v_mfma_f32_16x16x32_bf16 v[64:67], a[160:163], a[8:11], v[64:67]\n" \
"ds_read_b64_tr_b16 a[196:197], v228 offset:24576\n" \
"ds_read_b64_tr_b16 a[198:199], v228 offset:26624\n" \
"v_mfma_f32_16x16x32_bf16 v[64:67], a[164:167], a[12:15], v[64:67]\n" \
"ds_read_b64_tr_b16 a[200:201], v229 offset:16384\n" \
"ds_read_b64_tr_b16 a[202:203], v229 offset:18432\n" \
"v_mfma_f32_16x16x32_bf16 v[68:71], a[168:171], a[8:11], v[68:71]\n" \
"ds_read_b64_tr_b16 a[204:205], v229 offset:24576\n" \
"ds_read_b64_tr_b16 a[206:207], v229 offset:26624\n" \
"v_mfma_f32_16x16x32_bf16 v[68:71], a[172:175], a[12:15], v[68:71]\n" \
"ds_read_b64_tr_b16 a[208:209], v228 offset:16896\n" \
"ds_read_b64_tr_b16 a[210:211], v228 offset:18944\n" \
"v_mfma_f32_16x16x32_bf16 v[72:75], a[176:179], a[8:11], v[72:75]\n" \
"ds_read_b64_tr_b16 a[212:213], v228 offset:25088\n" \
"ds_read_b64_tr_b16 a[214:215], v228 offset:27136\n" \
"v_mfma_f32_16x16x32_bf16 v[72:75], a[180:183], a[12:15], v[72:75]\n" \
"ds_read_b64_tr_b16 a[216:217], v229 offset:16896\n" \
"ds_read_b64_tr_b16 a[218:219], v229 offset:18944\n" \
"v_mfma_f32_16x16x32_bf16 v[76:79], a[184:187], a[8:11], v[76:79]\n" \
"ds_read_b64_tr_b16 a[220:221], v229 offset:25088\n" \
"ds_read_b64_tr_b16 a[222:223], v229 offset:27136\n" \
"v_mfma_f32_16x16x32_bf16 v[76:79], a[188:191], a[12:15], v[76:79]\n" \
"s_waitcnt lgkmcnt(8)\n" \
"ds_read_b128 a[40:43], v227 offset:256\n" \
"ds_read_b128 a[44:47], v227 offset:2304\n" \
"v_mfma_f32_16x16x32_bf16 v[80:83], a[160:163], a[32:35], v[80:83]\n" \
"ds_read_b128 a[248:251], v213 offset:4096\n" \
"ds_read_b128 a[252:255], v213 offset:6144\n" \
"v_mfma_f32_16x16x32_bf16 v[80:83], a[164:167], a[36:39], v[80:83]\n" \
"ds_read_b128 a[48:51], v224 offset:4096\n" \
"ds_read_b128 a[52:55], v224 offset:6144\n" \
"v_mfma_f32_16x16x32_bf16 v[84:87], a[168:171], a[32:35], v[84:87]\n" \
"ds_read_b128 a[56:59], v227 offset:4096\n" \
"ds_read_b128 a[60:63], v227 offset:6144\n" \
"v_mfma_f32_16x16x32_bf16 v[84:87], a[172:175], a[36:39], v[84:87]\n" \
"ds_read_b128 a[224:227], v213 offset:4352\n" \
"ds_read_b128 a[228:231], v213 offset:6400\n" \
"v_mfma_f32_16x16x32_bf16 v[88:91], a[176:179], a[32:35], v[88:91]\n" \
"ds_read_b128 a[232:235], v224 offset:4352\n" \
"ds_read_b128 a[236:239], v224 offset:6400\n" \
"v_mfma_f32_16x16x32_bf16 v[88:91], a[180:183], a[36:39], v[88:91]\n" \
"ds_read_b128 a[240:243], v227 offset:4352\n" \
"ds_read_b128 a[244:247], v227 offset:6400\n" \
"v_mfma_f32_16x16x32_bf16 v[92:95], a[184:187], a[32:35], v[92:95]\n" \
"v_mfma_f32_16x16x32_bf16 v[92:95], a[188:191], a[36:39], v[92:95]\n" \
"v_mfma_f32_16x16x32_bf16 v[64:67], a[192:195], a[16:19], v[64:67]\n" \
"v_mfma_f32_16x16x32_bf16 v[64:67], a[196:199], a[20:23], v[64:67]\n" \
"v_mfma_f32_16x16x32_bf16 v[68:71], a[200:203], a[16:19], v[68:71]\n" \
"v_mfma_f32_16x16x32_bf16 v[68:71], a[204:207], a[20:23], v[68:71]\n" \
"s_waitcnt lgkmcnt(0)\n" \
"v_mfma_f32_16x16x32_bf16 v[72:75], a[208:211], a[16:19], v[72:75]\n" \
"v_mfma_f32_16x16x32_bf16 v[72:75], a[212:215], a[20:23], v[72:75]\n" \
"v_mfma_f32_16x16x32_bf16 v[76:79], a[216:219], a[16:19], v[76:79]\n" \
"v_mfma_f32_16x16x32_bf16 v[76:79], a[220:223], a[20:23], v[76:79]\n" \
"v_mfma_f32_16x16x32_bf16 v[80:83], a[192:195], a[40:43], v[80:83]\n" \
"v_mfma_f32_16x16x32_bf16 v[80:83], a[196:199], a[44:47], v[80:83]\n" \
"v_mfma_f32_16x16x32_bf16 v[84:87], a[200:203], a[40:43], v[84:87]\n" \
"v_cvt_pk_bf16_f32 v136, v64, v65\n" \
"v_cvt_pk_bf16_f32 v137, v66, v67\n" \
"v_mfma_f32_16x16x32_bf16 v[84:87], a[204:207], a[44:47], v[84:87]\n" \
"ds_write_b64 v128, v[136:137] offset:0\n" \
"v_mfma_f32_16x16x32_bf16 v[88:91], a[208:211], a[40:43], v[88:91]\n" \
"v_cvt_pk_bf16_f32 v138, v68, v69\n" \
"v_cvt_pk_bf16_f32 v139, v70, v71\n" \
"v_mfma_f32_16x16x32_bf16 v[88:91], a[212:215], a[44:47], v[88:91]\n" \
"ds_write_b64 v128, v[138:139] offset:32\n" \
"v_mfma_f32_16x16x32_bf16 v[92:95], a[216:219], a[40:43], v[92:95]\n" \
"v_cvt_pk_bf16_f32 v136, v72, v73\n" \
"v_cvt_pk_bf16_f32 v137, v74, v75\n" \
"v_mfma_f32_16x16x32_bf16 v[92:95], a[220:223], a[44:47], v[92:95]\n" \
"ds_write_b64 v128, v[136:137] offset:64\n" \
"v_mfma_f32_16x16x32_bf16 v[96:99], a[128:131], a[248:251], v[96:99]\n" \
"v_cvt_pk_bf16_f32 v138, v76, v77\n" \
"v_cvt_pk_bf16_f32 v139, v78, v79\n" \
"v_mfma_f32_16x16x32_bf16 v[96:99], a[132:135], a[252:255], v[96:99]\n" \
"ds_write_b64 v128, v[138:139] offset:96\n" \
"v_mfma_f32_16x16x32_bf16 v[100:103], a[136:139], a[248:251], v[100:103]\n" \
"ds_read_b128 v[140:143], v129 offset:0\n" \
"ds_read_b128 v[144:147], v129 offset:1152\n" \
"v_mfma_f32_16x16x32_bf16 v[100:103], a[140:143], a[252:255], v[100:103]\n" \
"v_cvt_pk_bf16_f32 v136, v80, v81\n" \
"v_cvt_pk_bf16_f32 v137, v82, v83\n" \
"v_mfma_f32_16x16x32_bf16 v[104:107], a[144:147], a[248:251], v[104:107]\n" \
"v_mfma_f32_16x16x32_bf16 v[104:107], a[148:151], a[252:255], v[104:107]\n" \
"s_add_u32 s70, s88, 0\n" \
"s_waitcnt lgkmcnt(1)\n" \
"v_mfma_f32_16x16x32_bf16 v[108:111], a[152:155], a[248:251], v[108:111]\n" \
"buffer_store_dwordx4 v[140:143], v130, s[52:55], s70 offen sc1\n" \
"v_mfma_f32_16x16x32_bf16 v[108:111], a[156:159], a[252:255], v[108:111]\n" \
"ds_write_b64 v128, v[136:137] offset:2304\n" \
"v_mfma_f32_16x16x32_bf16 v[96:99], a[160:163], a[48:51], v[96:99]\n" \
"s_add_u32 s70, s88, 65536\n" \
"s_waitcnt lgkmcnt(1)\n" \
"v_cvt_pk_bf16_f32 v138, v84, v85\n" \
"v_mfma_f32_16x16x32_bf16 v[96:99], a[164:167], a[52:55], v[96:99]\n" \
"buffer_store_dwordx4 v[144:147], v130, s[52:55], s70 offen sc1\n" \
"v_mfma_f32_16x16x32_bf16 v[100:103], a[168:171], a[48:51], v[100:103]\n" \
"v_cvt_pk_bf16_f32 v139, v86, v87\n" \
"v_mfma_f32_16x16x32_bf16 v[100:103], a[172:175], a[52:55], v[100:103]\n" \
"ds_write_b64 v128, v[138:139] offset:2336\n" \
"v_mfma_f32_16x16x32_bf16 v[104:107], a[176:179], a[48:51], v[104:107]\n" \
"v_cvt_pk_bf16_f32 v136, v88, v89\n" \
"v_cvt_pk_bf16_f32 v137, v90, v91\n" \
"v_mfma_f32_16x16x32_bf16 v[104:107], a[180:183], a[52:55], v[104:107]\n" \
"ds_write_b64 v128, v[136:137] offset:2368\n" \
"v_mfma_f32_16x16x32_bf16 v[108:111], a[184:187], a[48:51], v[108:111]\n" \
"v_cvt_pk_bf16_f32 v138, v92, v93\n" \
"v_cvt_pk_bf16_f32 v139, v94, v95\n" \
"v_mfma_f32_16x16x32_bf16 v[108:111], a[188:191], a[52:55], v[108:111]\n" \
"ds_write_b64 v128, v[138:139] offset:2400\n" \
"v_mfma_f32_16x16x32_bf16 v[96:99], a[192:195], a[56:59], v[96:99]\n" \
"ds_read_b128 v[148:151], v129 offset:2304\n" \
"ds_read_b128 v[152:155], v129 offset:3456\n" \
"v_mfma_f32_16x16x32_bf16 v[96:99], a[196:199], a[60:63], v[96:99]\n" \
"v_mfma_f32_16x16x32_bf16 v[100:103], a[200:203], a[56:59], v[100:103]\n" \
"v_mfma_f32_16x16x32_bf16 v[100:103], a[204:207], a[60:63], v[100:103]\n" \
"s_add_u32 s70, s88, 4096\n" \
"s_waitcnt lgkmcnt(1)\n" \
"v_mfma_f32_16x16x32_bf16 v[104:107], a[208:211], a[56:59], v[104:107]\n" \
"buffer_store_dwordx4 v[148:151], v130, s[52:55], s70 offen sc1\n" \
"v_mfma_f32_16x16x32_bf16 v[104:107], a[212:215], a[60:63], v[104:107]\n" \
"v_mfma_f32_16x16x32_bf16 v[108:111], a[216:219], a[56:59], v[108:111]\n" \
"s_add_u32 s70, s88, 69632\n" \
"s_waitcnt lgkmcnt(0)\n" \
"v_mfma_f32_16x16x32_bf16 v[108:111], a[220:223], a[60:63], v[108:111]\n" \
"buffer_store_dwordx4 v[152:155], v130, s[52:55], s70 offen sc1\n" \
"v_mfma_f32_16x16x32_bf16 v[112:115], a[128:131], a[224:227], v[112:115]\n" \
"v_mfma_f32_16x16x32_bf16 v[112:115], a[132:135], a[228:231], v[112:115]\n" \
"v_mfma_f32_16x16x32_bf16 v[116:119], a[136:139], a[224:227], v[116:119]\n" \
"v_cvt_pk_bf16_f32 v136, v96, v97\n" \
"v_cvt_pk_bf16_f32 v137, v98, v99\n" \
"v_mfma_f32_16x16x32_bf16 v[116:119], a[140:143], a[228:231], v[116:119]\n" \
"ds_write_b64 v128, v[136:137] offset:4608\n" \
"v_mfma_f32_16x16x32_bf16 v[120:123], a[144:147], a[224:227], v[120:123]\n" \
"v_cvt_pk_bf16_f32 v138, v100, v101\n" \
"v_cvt_pk_bf16_f32 v139, v102, v103\n" \
"v_mfma_f32_16x16x32_bf16 v[120:123], a[148:151], a[228:231], v[120:123]\n" \
"ds_write_b64 v128, v[138:139] offset:4640\n" \
"v_mfma_f32_16x16x32_bf16 v[124:127], a[152:155], a[224:227], v[124:127]\n" \
"v_cvt_pk_bf16_f32 v136, v104, v105\n" \
"v_cvt_pk_bf16_f32 v137, v106, v107\n" \
"v_mfma_f32_16x16x32_bf16 v[124:127], a[156:159], a[228:231], v[124:127]\n" \
"ds_write_b64 v128, v[136:137] offset:4672\n" \
"v_mfma_f32_16x16x32_bf16 v[112:115], a[160:163], a[232:235], v[112:115]\n" \
"v_cvt_pk_bf16_f32 v138, v108, v109\n" \
"v_cvt_pk_bf16_f32 v139, v110, v111\n" \
"v_mfma_f32_16x16x32_bf16 v[112:115], a[164:167], a[236:239], v[112:115]\n" \
"ds_write_b64 v128, v[138:139] offset:4704\n" \
"v_mfma_f32_16x16x32_bf16 v[116:119], a[168:171], a[232:235], v[116:119]\n" \
"ds_read_b128 v[156:159], v129 offset:4608\n" \
"ds_read_b128 v[160:163], v129 offset:5760\n" \
"v_mfma_f32_16x16x32_bf16 v[116:119], a[172:175], a[236:239], v[116:119]\n" \
"v_mfma_f32_16x16x32_bf16 v[120:123], a[176:179], a[232:235], v[120:123]\n" \
"v_mfma_f32_16x16x32_bf16 v[120:123], a[180:183], a[236:239], v[120:123]\n" \
"s_add_u32 s70, s88, 131072\n" \
"s_waitcnt lgkmcnt(1)\n" \
"v_mfma_f32_16x16x32_bf16 v[124:127], a[184:187], a[232:235], v[124:127]\n" \
"buffer_store_dwordx4 v[156:159], v130, s[52:55], s70 offen sc1\n" \
"v_mfma_f32_16x16x32_bf16 v[124:127], a[188:191], a[236:239], v[124:127]\n" \
"v_mfma_f32_16x16x32_bf16 v[112:115], a[192:195], a[240:243], v[112:115]\n" \
"s_add_u32 s70, s88, 196608\n" \
"s_waitcnt lgkmcnt(0)\n" \
"v_mfma_f32_16x16x32_bf16 v[112:115], a[196:199], a[244:247], v[112:115]\n" \
"buffer_store_dwordx4 v[160:163], v130, s[52:55], s70 offen sc1\n" \
"v_mfma_f32_16x16x32_bf16 v[116:119], a[200:203], a[240:243], v[116:119]\n" \
"v_mfma_f32_16x16x32_bf16 v[116:119], a[204:207], a[244:247], v[116:119]\n" \
"v_mfma_f32_16x16x32_bf16 v[120:123], a[208:211], a[240:243], v[120:123]\n" \
"v_mfma_f32_16x16x32_bf16 v[120:123], a[212:215], a[244:247], v[120:123]\n" \
"v_mfma_f32_16x16x32_bf16 v[124:127], a[216:219], a[240:243], v[124:127]\n" \
"v_mfma_f32_16x16x32_bf16 v[124:127], a[220:223], a[244:247], v[124:127]\n" \
"s_nop 1\n" \
"v_cvt_pk_bf16_f32 v136, v112, v113\n" \
"v_cvt_pk_bf16_f32 v137, v114, v115\n" \
"ds_write_b64 v128, v[136:137] offset:6912\n" \
"v_cvt_pk_bf16_f32 v138, v116, v117\n" \
"v_cvt_pk_bf16_f32 v139, v118, v119\n" \
"ds_write_b64 v128, v[138:139] offset:6944\n" \
"v_cvt_pk_bf16_f32 v136, v120, v121\n" \
"v_cvt_pk_bf16_f32 v137, v122, v123\n" \
"ds_write_b64 v128, v[136:137] offset:6976\n" \
"v_cvt_pk_bf16_f32 v138, v124, v125\n" \
"v_cvt_pk_bf16_f32 v139, v126, v127\n" \
"ds_write_b64 v128, v[138:139] offset:7008\n" \
"ds_read_b128 v[164:167], v129 offset:6912\n" \
"ds_read_b128 v[168:171], v129 offset:8064\n" \
"s_add_u32 s70, s88, 135168\n" \
"s_waitcnt lgkmcnt(1)\n" \
"buffer_store_dwordx4 v[164:167], v130, s[52:55], s70 offen sc1\n" \
"s_add_u32 s70, s88, 200704\n" \
"s_waitcnt lgkmcnt(0)\n" \
"buffer_store_dwordx4 v[168:171], v130, s[52:55], s70 offen sc1\n" \
"s_endpgm\n" \
"Lhot_end_%=:\n" \
"Lpl1_%=:\n" \
"s_mov_b32 s72, 0\n" \
"Lpk1_%=:\n" \
"s_sleep 2\n" \
"buffer_load_dword v227, v242, s[56:59], s90 offen sc1\n" \
"s_waitcnt vmcnt(0)\n" \
"v_cmp_eq_u32 vcc, s60, v227\n" \
"s_nop 1\n" \
"s_andn2_b32 s70, 0xff, vcc_lo\n" \
"s_cbranch_scc0 Lpr1_%=\n" \
"s_add_u32 s72, s72, 1\n" \
"s_cmp_lt_u32 s72, 0x40000\n" \
"s_cbranch_scc1 Lpk1_%=\n" \
"s_branch Lpr1_%=\n" \
"Lpl2_%=:\n" \
"s_mov_b32 s72, 0\n" \
"Lpk2_%=:\n" \
"s_sleep 2\n" \
"buffer_load_dword v239, v242, s[56:59], s95 offen sc1\n" \
"s_waitcnt vmcnt(0)\n" \
"v_cmp_eq_u32 vcc, s60, v239\n" \
"s_nop 1\n" \
"s_andn2_b32 s70, s69, vcc_lo\n" \
"s_cbranch_scc0 Lpr2_%=\n" \
"s_add_u32 s72, s72, 1\n" \
"s_cmp_lt_u32 s72, 0x40000\n" \
"s_cbranch_scc1 Lpk2_%=\n" \
"s_branch Lpr2_%=\n" \
"Lpl3_%=:\n" \
"s_mov_b32 s72, 0\n" \
"Lpk3_%=:\n" \
"s_sleep 2\n" \
"buffer_load_dword v239, v242, s[56:59], s95 offen sc1\n" \
"s_waitcnt vmcnt(0)\n" \
"v_cmp_eq_u32 vcc, s60, v239\n" \
"s_nop 1\n" \
"s_andn2_b32 s70, s69, vcc_lo\n" \
"s_cbranch_scc0 Lpr3_%=\n" \
"s_add_u32 s72, s72, 1\n" \
"s_cmp_lt_u32 s72, 0x40000\n" \
"s_cbranch_scc1 Lpk3_%=\n" \
"s_branch Lpr3_%=\n" \
"Lexact_%=:\n" \
"v_mov_b32 v239, 0xff800000\n" \
"s_and_b32 s70, s61, 1\n" \
"s_lshl_b32 s70, s70, 4\n" \
"v_add_u32 v186, s70, v238\n" \
"v_accvgpr_read_b32 v160, a128\n" \
"v_accvgpr_read_b32 v161, a129\n" \
"v_accvgpr_read_b32 v162, a130\n" \
"v_accvgpr_read_b32 v163, a131\n" \
"v_accvgpr_read_b32 v164, a132\n" \
"v_accvgpr_read_b32 v165, a133\n" \
"v_accvgpr_read_b32 v166, a134\n" \
"v_accvgpr_read_b32 v167, a135\n" \
"v_accvgpr_read_b32 v168, a136\n" \
"v_accvgpr_read_b32 v169, a137\n" \
"v_accvgpr_read_b32 v170, a138\n" \
"v_accvgpr_read_b32 v171, a139\n" \
"v_accvgpr_read_b32 v172, a140\n" \
"v_accvgpr_read_b32 v173, a141\n" \
"v_accvgpr_read_b32 v174, a142\n" \
"v_accvgpr_read_b32 v175, a143\n" \
"s_add_u32 s83, s85, 0\n" \
"s_add_u32 s84, s87, 0\n" \
"s_call_b64 s[64:65], Lex1_%=\n" \
"v_accvgpr_read_b32 v160, a144\n" \
"v_accvgpr_read_b32 v161, a145\n" \
"v_accvgpr_read_b32 v162, a146\n" \
"v_accvgpr_read_b32 v163, a147\n" \
"v_accvgpr_read_b32 v164, a148\n" \
"v_accvgpr_read_b32 v165, a149\n" \
"v_accvgpr_read_b32 v166, a150\n" \
"v_accvgpr_read_b32 v167, a151\n" \
"v_accvgpr_read_b32 v168, a152\n" \
"v_accvgpr_read_b32 v169, a153\n" \
"v_accvgpr_read_b32 v170, a154\n" \
"v_accvgpr_read_b32 v171, a155\n" \
"v_accvgpr_read_b32 v172, a156\n" \
"v_accvgpr_read_b32 v173, a157\n" \
"v_accvgpr_read_b32 v174, a158\n" \
"v_accvgpr_read_b32 v175, a159\n" \
"s_add_u32 s83, s85, 2\n" \
"s_add_u32 s84, s87, 532480\n" \
"s_call_b64 s[64:65], Lex1_%=\n" \
"v_accvgpr_read_b32 v160, a160\n" \
"v_accvgpr_read_b32 v161, a161\n" \
"v_accvgpr_read_b32 v162, a162\n" \
"v_accvgpr_read_b32 v163, a163\n" \
"v_accvgpr_read_b32 v164, a164\n" \
"v_accvgpr_read_b32 v165, a165\n" \
"v_accvgpr_read_b32 v166, a166\n" \
"v_accvgpr_read_b32 v167, a167\n" \
"v_accvgpr_read_b32 v168, a168\n" \
"v_accvgpr_read_b32 v169, a169\n" \
"v_accvgpr_read_b32 v170, a170\n" \
"v_accvgpr_read_b32 v171, a171\n" \
"v_accvgpr_read_b32 v172, a172\n" \
"v_accvgpr_read_b32 v173, a173\n" \
"v_accvgpr_read_b32 v174, a174\n" \
"v_accvgpr_read_b32 v175, a175\n" \
"s_add_u32 s83, s86, 0\n" \
"s_add_u32 s84, s88, 0\n" \
"s_call_b64 s[64:65], Lex1_%=\n" \
"v_accvgpr_read_b32 v160, a176\n" \
"v_accvgpr_read_b32 v161, a177\n" \
"v_accvgpr_read_b32 v162, a178\n" \
"v_accvgpr_read_b32 v163, a179\n" \
"v_accvgpr_read_b32 v164, a180\n" \
"v_accvgpr_read_b32 v165, a181\n" \
"v_accvgpr_read_b32 v166, a182\n" \
"v_accvgpr_read_b32 v167, a183\n" \
"v_accvgpr_read_b32 v168, a184\n" \
"v_accvgpr_read_b32 v169, a185\n" \
"v_accvgpr_read_b32 v170, a186\n" \
"v_accvgpr_read_b32 v171, a187\n" \
"v_accvgpr_read_b32 v172, a188\n" \
"v_accvgpr_read_b32 v173, a189\n" \
"v_accvgpr_read_b32 v174, a190\n" \
"v_accvgpr_read_b32 v175, a191\n" \
"s_add_u32 s83, s86, 2\n" \
"s_add_u32 s84, s88, 532480\n" \
"s_call_b64 s[64:65], Lex1_%=\n" \
"s_waitcnt vmcnt(0)\n" \
"s_branch Lexr_%=\n" \
"Lex1_%=:\n" \
"v_mov_b32 v128, 0\n" \
"v_mov_b32 v129, 0\n" \
"v_mov_b32 v130, 0\n" \
"v_mov_b32 v131, 0\n" \
"v_mov_b32 v132, 0\n" \
"v_mov_b32 v133, 0\n" \
"v_mov_b32 v134, 0\n" \
"v_mov_b32 v135, 0\n" \
"v_mov_b32 v136, 0\n" \
"v_mov_b32 v137, 0\n" \
"v_mov_b32 v138, 0\n" \
"v_mov_b32 v139, 0\n" \
"v_mov_b32 v140, 0\n" \
"v_mov_b32 v141, 0\n" \
"v_mov_b32 v142, 0\n" \
"v_mov_b32 v143, 0\n" \
"v_mov_b32 v144, 0\n" \
"v_mov_b32 v145, 0\n" \
"v_mov_b32 v146, 0\n" \
"v_mov_b32 v147, 0\n" \
"v_mov_b32 v148, 0\n" \
"v_mov_b32 v149, 0\n" \
"v_mov_b32 v150, 0\n" \
"v_mov_b32 v151, 0\n" \
"v_mov_b32 v152, 0\n" \
"v_mov_b32 v153, 0\n" \
"v_mov_b32 v154, 0\n" \
"v_mov_b32 v155, 0\n" \
"v_mov_b32 v156, 0\n" \
"v_mov_b32 v157, 0\n" \
"v_mov_b32 v158, 0\n" \
"v_mov_b32 v159, 0\n" \
"v_mov_b32 v184, 0xf149f2ca\n" \
"v_mov_b32 v185, 0\n" \
"s_mov_b32 s81, 0\n" \
"s_mul_i32 s82, s62, 557824\n" \
"Lex2_%=:\n" \
"s_add_u32 s70, s82, 4096\n" \
"buffer_load_dwordx4 a[64:67], v236, s[56:59], s82 offen offset:0 sc1\n" \
"buffer_load_dwordx4 a[68:71], v236, s[56:59], s82 offen offset:2048 sc1\n" \
"buffer_load_dwordx4 a[72:75], v236, s[56:59], s70 offen offset:0 sc1\n" \
"buffer_load_dwordx4 a[76:79], v236, s[56:59], s70 offen offset:2048 sc1\n" \
"buffer_load_dwordx4 a[80:83], v236, s[56:59], s82 offen offset:256 sc1\n" \
"buffer_load_dwordx4 a[84:87], v236, s[56:59], s82 offen offset:2304 sc1\n" \
"buffer_load_dwordx4 a[88:91], v236, s[56:59], s70 offen offset:256 sc1\n" \
"buffer_load_dwordx4 a[92:95], v236, s[56:59], s70 offen offset:2304 sc1\n" \
"s_add_u32 s70, s82, 17850368\n" \
"s_add_u32 s71, s70, 4096\n" \
"buffer_load_dwordx4 a[96:99], v234, s[56:59], s70 offen offset:0 sc1\n" \
"buffer_load_dwordx4 a[100:103], v234, s[56:59], s70 offen offset:1024 sc1\n" \
"buffer_load_dwordx4 a[104:107], v234, s[56:59], s70 offen offset:2048 sc1\n" \
"buffer_load_dwordx4 a[108:111], v234, s[56:59], s70 offen offset:3072 sc1\n" \
"buffer_load_dwordx4 a[112:115], v234, s[56:59], s71 offen offset:0 sc1\n" \
"buffer_load_dwordx4 a[116:119], v234, s[56:59], s71 offen offset:1024 sc1\n" \
"buffer_load_dwordx4 a[120:123], v234, s[56:59], s71 offen offset:2048 sc1\n" \
"buffer_load_dwordx4 a[124:127], v234, s[56:59], s71 offen offset:3072 sc1\n" \
"s_waitcnt vmcnt(0)\n" \
"v_mfma_f32_16x16x32_bf16 v[176:179], a[64:67], v[160:163], 0\n" \
"v_mfma_f32_16x16x32_bf16 v[176:179], a[68:71], v[164:167], v[176:179]\n" \
"v_mfma_f32_16x16x32_bf16 v[176:179], a[72:75], v[168:171], v[176:179]\n" \
"v_mfma_f32_16x16x32_bf16 v[176:179], a[76:79], v[172:175], v[176:179]\n" \
"v_mfma_f32_16x16x32_bf16 v[180:183], a[80:83], v[160:163], 0\n" \
"v_mfma_f32_16x16x32_bf16 v[180:183], a[84:87], v[164:167], v[180:183]\n" \
"v_mfma_f32_16x16x32_bf16 v[180:183], a[88:91], v[168:171], v[180:183]\n" \
"v_mfma_f32_16x16x32_bf16 v[180:183], a[92:95], v[172:175], v[180:183]\n" \
"s_nop 7\n" \
"s_nop 1\n" \
"s_cmp_eq_u32 s81, s83\n" \
"s_cbranch_scc0 Lex3_%=\n" \
"v_cmp_gt_i32 vcc, 0, v186\n" \
"s_nop 1\n" \
"v_cndmask_b32 v176, v176, v239, vcc\n" \
"v_cmp_gt_i32 vcc, 1, v186\n" \
"s_nop 1\n" \
"v_cndmask_b32 v177, v177, v239, vcc\n" \
"v_cmp_gt_i32 vcc, 2, v186\n" \
"s_nop 1\n" \
"v_cndmask_b32 v178, v178, v239, vcc\n" \
"v_cmp_gt_i32 vcc, 3, v186\n" \
"s_nop 1\n" \
"v_cndmask_b32 v179, v179, v239, vcc\n" \
"v_cmp_gt_i32 vcc, 16, v186\n" \
"s_nop 1\n" \
"v_cndmask_b32 v180, v180, v239, vcc\n" \
"v_cmp_gt_i32 vcc, 17, v186\n" \
"s_nop 1\n" \
"v_cndmask_b32 v181, v181, v239, vcc\n" \
"v_cmp_gt_i32 vcc, 18, v186\n" \
"s_nop 1\n" \
"v_cndmask_b32 v182, v182, v239, vcc\n" \
"v_cmp_gt_i32 vcc, 19, v186\n" \
"s_nop 1\n" \
"v_cndmask_b32 v183, v183, v239, vcc\n" \
"Lex3_%=:\n" \
"v_max3_f32 v187, v176, v177, v178\n" \
"v_max3_f32 v187, v187, v179, v180\n" \
"v_max3_f32 v187, v187, v181, v182\n" \
"v_max3_f32 v187, v187, v183, v184\n" \
"v_mov_b32 v188, v187\n" \
"s_nop 1\n" \
"v_permlane16_swap_b32 v188, v187\n" \
"v_max_f32 v187, v187, v188\n" \
"v_mov_b32 v188, v187\n" \
"s_nop 1\n" \
"v_permlane32_swap_b32 v188, v187\n" \
"v_max_f32 v187, v187, v188\n" \
"v_sub_f32 v189, v184, v187\n" \
"v_exp_f32 v189, v189\n" \
"v_mov_b32 v184, v187\n" \
"v_sub_f32 v176, v176, v187\n" \
"v_sub_f32 v177, v177, v187\n" \
"v_sub_f32 v178, v178, v187\n" \
"v_sub_f32 v179, v179, v187\n" \
"v_sub_f32 v180, v180, v187\n" \
"v_sub_f32 v181, v181, v187\n" \
"v_sub_f32 v182, v182, v187\n" \
"v_sub_f32 v183, v183, v187\n" \
"v_exp_f32 v176, v176\n" \
"v_exp_f32 v177, v177\n" \
"v_exp_f32 v178, v178\n" \
"v_exp_f32 v179, v179\n" \
"v_exp_f32 v180, v180\n" \
"v_exp_f32 v181, v181\n" \
"v_exp_f32 v182, v182\n" \
"v_exp_f32 v183, v183\n" \
"v_mul_f32 v185, v185, v189\n" \
"v_add_f32 v185, v185, v176\n" \
"v_add_f32 v185, v185, v177\n" \
"v_add_f32 v185, v185, v178\n" \
"v_add_f32 v185, v185, v179\n" \
"v_add_f32 v185, v185, v180\n" \
"v_add_f32 v185, v185, v181\n" \
"v_add_f32 v185, v185, v182\n" \
"v_add_f32 v185, v185, v183\n" \
"v_cvt_pk_bf16_f32 v112, v176, v177\n" \
"v_cvt_pk_bf16_f32 v113, v178, v179\n" \
"v_cvt_pk_bf16_f32 v114, v180, v181\n" \
"v_cvt_pk_bf16_f32 v115, v182, v183\n" \
"v_mul_f32 v128, v128, v189\n" \
"v_mul_f32 v129, v129, v189\n" \
"v_mul_f32 v130, v130, v189\n" \
"v_mul_f32 v131, v131, v189\n" \
"v_mul_f32 v132, v132, v189\n" \
"v_mul_f32 v133, v133, v189\n" \
"v_mul_f32 v134, v134, v189\n" \
"v_mul_f32 v135, v135, v189\n" \
"v_mul_f32 v136, v136, v189\n" \
"v_mul_f32 v137, v137, v189\n" \
"v_mul_f32 v138, v138, v189\n" \
"v_mul_f32 v139, v139, v189\n" \
"v_mul_f32 v140, v140, v189\n" \
"v_mul_f32 v141, v141, v189\n" \
"v_mul_f32 v142, v142, v189\n" \
"v_mul_f32 v143, v143, v189\n" \
"v_mul_f32 v144, v144, v189\n" \
"v_mul_f32 v145, v145, v189\n" \
"v_mul_f32 v146, v146, v189\n" \
"v_mul_f32 v147, v147, v189\n" \
"v_mul_f32 v148, v148, v189\n" \
"v_mul_f32 v149, v149, v189\n" \
"v_mul_f32 v150, v150, v189\n" \
"v_mul_f32 v151, v151, v189\n" \
"v_mul_f32 v152, v152, v189\n" \
"v_mul_f32 v153, v153, v189\n" \
"v_mul_f32 v154, v154, v189\n" \
"v_mul_f32 v155, v155, v189\n" \
"v_mul_f32 v156, v156, v189\n" \
"v_mul_f32 v157, v157, v189\n" \
"v_mul_f32 v158, v158, v189\n" \
"v_mul_f32 v159, v159, v189\n" \
"s_nop 1\n" \
"v_mfma_f32_16x16x32_bf16 v[128:131], a[96:99], v[112:115], v[128:131]\n" \
"v_mfma_f32_16x16x32_bf16 v[132:135], a[100:103], v[112:115], v[132:135]\n" \
"v_mfma_f32_16x16x32_bf16 v[136:139], a[104:107], v[112:115], v[136:139]\n" \
"v_mfma_f32_16x16x32_bf16 v[140:143], a[108:111], v[112:115], v[140:143]\n" \
"v_mfma_f32_16x16x32_bf16 v[144:147], a[112:115], v[112:115], v[144:147]\n" \
"v_mfma_f32_16x16x32_bf16 v[148:151], a[116:119], v[112:115], v[148:151]\n" \
"v_mfma_f32_16x16x32_bf16 v[152:155], a[120:123], v[112:115], v[152:155]\n" \
"v_mfma_f32_16x16x32_bf16 v[156:159], a[124:127], v[112:115], v[156:159]\n" \
"s_nop 7\n" \
"s_nop 1\n" \
"s_and_b32 s70, s81, 1\n" \
"s_mul_i32 s70, s70, 1024\n" \
"s_add_u32 s82, s82, s70\n" \
"s_add_u32 s82, s82, 8192\n" \
"s_add_u32 s81, s81, 1\n" \
"s_cmp_le_u32 s81, s83\n" \
"s_cbranch_scc1 Lex2_%=\n" \
"v_mov_b32 v187, v185\n" \
"s_nop 1\n" \
"v_permlane16_swap_b32 v187, v185\n" \
"v_add_f32 v185, v185, v187\n" \
"v_mov_b32 v187, v185\n" \
"s_nop 1\n" \
"v_permlane32_swap_b32 v187, v185\n" \
"v_add_f32 v185, v185, v187\n" \
"v_rcp_f32 v185, v185\n" \
"s_nop 0\n" \
"v_mul_f32 v128, v128, v185\n" \
"v_mul_f32 v129, v129, v185\n" \
"v_mul_f32 v130, v130, v185\n" \
"v_mul_f32 v131, v131, v185\n" \
"v_mul_f32 v132, v132, v185\n" \
"v_mul_f32 v133, v133, v185\n" \
"v_mul_f32 v134, v134, v185\n" \
"v_mul_f32 v135, v135, v185\n" \
"v_mul_f32 v136, v136, v185\n" \
"v_mul_f32 v137, v137, v185\n" \
"v_mul_f32 v138, v138, v185\n" \
"v_mul_f32 v139, v139, v185\n" \
"v_mul_f32 v140, v140, v185\n" \
"v_mul_f32 v141, v141, v185\n" \
"v_mul_f32 v142, v142, v185\n" \
"v_mul_f32 v143, v143, v185\n" \
"v_mul_f32 v144, v144, v185\n" \
"v_mul_f32 v145, v145, v185\n" \
"v_mul_f32 v146, v146, v185\n" \
"v_mul_f32 v147, v147, v185\n" \
"v_mul_f32 v148, v148, v185\n" \
"v_mul_f32 v149, v149, v185\n" \
"v_mul_f32 v150, v150, v185\n" \
"v_mul_f32 v151, v151, v185\n" \
"v_mul_f32 v152, v152, v185\n" \
"v_mul_f32 v153, v153, v185\n" \
"v_mul_f32 v154, v154, v185\n" \
"v_mul_f32 v155, v155, v185\n" \
"v_mul_f32 v156, v156, v185\n" \
"v_mul_f32 v157, v157, v185\n" \
"v_mul_f32 v158, v158, v185\n" \
"v_mul_f32 v159, v159, v185\n" \
"s_add_u32 s70, s84, 4096\n" \
"v_cvt_pk_bf16_f32 v112, v128, v129\n" \
"v_cvt_pk_bf16_f32 v113, v130, v131\n" \
"v_cvt_pk_bf16_f32 v114, v132, v133\n" \
"v_cvt_pk_bf16_f32 v115, v134, v135\n" \
"buffer_store_dwordx4 v[112:115], v236, s[56:59], s84 offen offset:0 sc1\n" \
"v_cvt_pk_bf16_f32 v116, v136, v137\n" \
"v_cvt_pk_bf16_f32 v117, v138, v139\n" \
"v_cvt_pk_bf16_f32 v118, v140, v141\n" \
"v_cvt_pk_bf16_f32 v119, v142, v143\n" \
"buffer_store_dwordx4 v[116:119], v236, s[56:59], s84 offen offset:2048 sc1\n" \
"v_cvt_pk_bf16_f32 v120, v144, v145\n" \
"v_cvt_pk_bf16_f32 v121, v146, v147\n" \
"v_cvt_pk_bf16_f32 v122, v148, v149\n" \
"v_cvt_pk_bf16_f32 v123, v150, v151\n" \
"buffer_store_dwordx4 v[120:123], v236, s[56:59], s70 offen offset:0 sc1\n" \
"v_cvt_pk_bf16_f32 v124, v152, v153\n" \
"v_cvt_pk_bf16_f32 v125, v154, v155\n" \
"v_cvt_pk_bf16_f32 v126, v156, v157\n" \
"v_cvt_pk_bf16_f32 v127, v158, v159\n" \
"buffer_store_dwordx4 v[124:127], v236, s[56:59], s70 offen offset:2048 sc1\n" \
"s_waitcnt vmcnt(0)\n" \
"s_setpc_b64 s[64:65]\n" \
"Lpl4_%=:\n" \
"s_mov_b32 s72, 0\n" \
"Lpk4_%=:\n" \
"s_sleep 2\n" \
"buffer_load_dwordx2 v[226:227], v228, s[56:59], s13 offen sc1\n" \
"s_waitcnt vmcnt(0)\n" \
"v_cmp_eq_u32 vcc, s60, v226\n" \
"s_nop 1\n" \
"s_andn2_b32 s70, 0xff, vcc_lo\n" \
"s_cbranch_scc0 Lpr4_%=\n" \
"s_add_u32 s72, s72, 1\n" \
"s_cmp_lt_u32 s72, 0x40000\n" \
"s_cbranch_scc1 Lpk4_%=\n" \
"s_branch Lpr4_%=\n" \
"Lpl5_%=:\n" \
"s_mov_b32 s72, 0\n" \
"Lpk5_%=:\n" \
"s_sleep 2\n" \
"buffer_load_dword v220, v221, s[56:59], s66 offen sc1\n" \
"s_waitcnt vmcnt(0)\n" \
"v_cmp_eq_u32 vcc, s60, v220\n" \
"s_nop 1\n" \
"s_andn2_b32 s70, s65, vcc_lo\n" \
"s_cbranch_scc0 Lpr5_%=\n" \
"s_add_u32 s72, s72, 1\n" \
"s_cmp_lt_u32 s72, 0x40000\n" \
"s_cbranch_scc1 Lpk5_%=\n" \
"s_branch Lpr5_%=\n" \
"Lpl6_%=:\n" \
"s_mov_b32 s72, 0\n" \
"Lpk6_%=:\n" \
"s_sleep 2\n" \
"buffer_load_dword v220, v221, s[56:59], s67 offen sc1\n" \
"s_waitcnt vmcnt(0)\n" \
"v_cmp_eq_u32 vcc, s60, v220\n" \
"s_nop 1\n" \
"s_andn2_b32 s70, s74, vcc_lo\n" \
"s_cbranch_scc0 Lpr6_%=\n" \
"s_add_u32 s72, s72, 1\n" \
"s_cmp_lt_u32 s72, 0x40000\n" \
"s_cbranch_scc1 Lpk6_%=\n" \
"s_branch Lpr6_%=\n" \
"Lpl7_%=:\n" \
"s_mov_b32 s72, 0\n" \
"Lpk7_%=:\n" \
"s_sleep 2\n" \
"buffer_load_dword v220, v241, s[56:59], s64 offen sc1\n" \
"s_waitcnt vmcnt(0)\n" \
"v_cmp_eq_u32 vcc, s60, v220\n" \
"s_nop 1\n" \
"s_andn2_b32 s70, s69, vcc_lo\n" \
"s_cbranch_scc0 Lpr7_%=\n" \
"s_add_u32 s72, s72, 1\n" \
"s_cmp_lt_u32 s72, 0x40000\n" \
"s_cbranch_scc1 Lpk7_%=\n" \
"s_branch Lpr7_%=\n" \
"Lcode_end_%=:\n" \
".p2align 12\n" \
""
#define MK_ASM_CLOBBERS "v1", "v2", "v3", "v4", "v5", "v6", "v7", "v8", "v9", "v10", "v11", "v12", "v13", "v14", "v15", "v16", "v17", "v18", "v19", "v20", "v21", "v22", "v23", "v24", "v25", "v26", "v27", "v28", "v29", "v30", "v31", "v32", "v33", "v34", "v35", "v36", "v37", "v38", "v39", "v40", "v41", "v42", "v43", "v44", "v45", "v46", "v47", "v48", "v49", "v50", "v51", "v52", "v53", "v54", "v55", "v56", "v57", "v58", "v59", "v60", "v61", "v62", "v63", "v64", "v65", "v66", "v67", "v68", "v69", "v70", "v71", "v72", "v73", "v74", "v75", "v76", "v77", "v78", "v79", "v80", "v81", "v82", "v83", "v84", "v85", "v86", "v87", "v88", "v89", "v90", "v91", "v92", "v93", "v94", "v95", "v96", "v97", "v98", "v99", "v100", "v101", "v102", "v103", "v104", "v105", "v106", "v107", "v108", "v109", "v110", "v111", "v112", "v113", "v114", "v115", "v116", "v117", "v118", "v119", "v120", "v121", "v122", "v123", "v124", "v125", "v126", "v127", "v128", "v129", "v130", "v131", "v132", "v133", "v134", "v135", "v136", "v137", "v138", "v139", "v140", "v141", "v142", "v143", "v144", "v145", "v146", "v147", "v148", "v149", "v150", "v151", "v152", "v153", "v154", "v155", "v156", "v157", "v158", "v159", "v160", "v161", "v162", "v163", "v164", "v165", "v166", "v167", "v168", "v169", "v170", "v171", "v172", "v173", "v174", "v175", "v176", "v177", "v178", "v179", "v180", "v181", "v182", "v183", "v184", "v185", "v186", "v187", "v188", "v189", "v190", "v191", "v192", "v193", "v194", "v195", "v196", "v197", "v198", "v199", "v200", "v201", "v202", "v203", "v204", "v205", "v206", "v207", "v208", "v209", "v210", "v211", "v212", "v213", "v214", "v215", "v216", "v217", "v218", "v219", "v220", "v221", "v222", "v223", "v224", "v225", "v226", "v227", "v228", "v229", "v230", "v231", "v232", "v233", "v234", "v235", "v236", "v237", "v238", "v239", "v240", "v241", "v242", "v243", "v244", "v245", "v246", "v247", "v248", "v249", "v250", "v251", "v252", "v253", "v254", "v255", "a0", "a1", "a2", "a3", "a4", "a5", "a6", "a7", "a8", "a9", "a10", "a11", "a12", "a13", "a14", "a15", "a16", "a17", "a18", "a19", "a20", "a21", "a22", "a23", "a24", "a25", "a26", "a27", "a28", "a29", "a30", "a31", "a32", "a33", "a34", "a35", "a36", "a37", "a38", "a39", "a40", "a41", "a42", "a43", "a44", "a45", "a46", "a47", "a48", "a49", "a50", "a51", "a52", "a53", "a54", "a55", "a56", "a57", "a58", "a59", "a60", "a61", "a62", "a63", "a64", "a65", "a66", "a67", "a68", "a69", "a70", "a71", "a72", "a73", "a74", "a75", "a76", "a77", "a78", "a79", "a80", "a81", "a82", "a83", "a84", "a85", "a86", "a87", "a88", "a89", "a90", "a91", "a92", "a93", "a94", "a95", "a96", "a97", "a98", "a99", "a100", "a101", "a102", "a103", "a104", "a105", "a106", "a107", "a108", "a109", "a110", "a111", "a112", "a113", "a114", "a115", "a116", "a117", "a118", "a119", "a120", "a121", "a122", "a123", "a124", "a125", "a126", "a127", "a128", "a129", "a130", "a131", "a132", "a133", "a134", "a135", "a136", "a137", "a138", "a139", "a140", "a141", "a142", "a143", "a144", "a145", "a146", "a147", "a148", "a149", "a150", "a151", "a152", "a153", "a154", "a155", "a156", "a157", "a158", "a159", "a160", "a161", "a162", "a163", "a164", "a165", "a166", "a167", "a168", "a169", "a170", "a171", "a172", "a173", "a174", "a175", "a176", "a177", "a178", "a179", "a180", "a181", "a182", "a183", "a184", "a185", "a186", "a187", "a188", "a189", "a190", "a191", "a192", "a193", "a194", "a195", "a196", "a197", "a198", "a199", "a200", "a201", "a202", "a203", "a204", "a205", "a206", "a207", "a208", "a209", "a210", "a211", "a212", "a213", "a214", "a215", "a216", "a217", "a218", "a219", "a220", "a221", "a222", "a223", "a224", "a225", "a226", "a227", "a228", "a229", "a230", "a231", "a232", "a233", "a234", "a235", "a236", "a237", "a238", "a239", "a240", "a241", "a242", "a243", "a244", "a245", "a246", "a247", "a248", "a249", "a250", "a251", "a252", "a253", "a254", "a255", "s8", "s9", "s10", "s11", "s12", "s13", "s14", "s15", "s16", "s17", "s18", "s19", "s20", "s21", "s22", "s23", "s24", "s25", "s26", "s27", "s28", "s29", "s30", "s31", "s36", "s37", "s38", "s39", "s40", "s41", "s42", "s43", "s44", "s45", "s46", "s47", "s48", "s49", "s50", "s51", "s52", "s53", "s54", "s55", "s56", "s57", "s58", "s59", "s60", "s61", "s62", "s63", "s64", "s65", "s66", "s67", "s68", "s69", "s70", "s71", "s72", "s73", "s74", "s75", "s76", "s77", "s78", "s79", "s80", "s81", "s82", "s83", "s84", "s85", "s86", "s87", "s88", "s89", "s90", "s91", "s92", "s93", "s94", "s95", "vcc", "scc", "memory"
namespace mk {
constexpr int LDS_BYTES = 163840;
struct Args { const void* in[9]; void* out; void* ws; };
__global__ __launch_bounds__(256, 1) void mega_k(const Args a) {
    extern __shared__ __attribute__((aligned(16))) unsigned char shm[];
    const unsigned ldsb = (unsigned)(size_t)(LAS unsigned char*)shm;
    asm volatile(MK_ASM_BODY
                 :
                 : "s"(__builtin_amdgcn_kernarg_segment_ptr()), "s"(blockIdx.x), "v"(threadIdx.x), "s"(ldsb)
                 : MK_ASM_CLOBBERS);
}
}
extern "C" void kernel_launch(void* const* d_in, const int* in_sizes, int n_in, void* d_out, int out_size, void* d_ws, size_t ws_size, hipStream_t stream) {
    (void)hipFuncSetAttribute((const void*)mk::mega_k, hipFuncAttributeMaxDynamicSharedMemorySize, mk::LDS_BYTES);
    mk::Args a;
    for (int i = 0; i < 9; ++i) a.in[i] = d_in[i];
    a.out = d_out; a.ws = (char*)d_ws + ((ws_size - WS_END) & ~(size_t)0x1fffff);
    mk::mega_k<<<256, 256, mk::LDS_BYTES, stream>>>(a);
}
```

```cpp
#include <hip/hip_runtime.h>
#include <stdint.h>
#define LAS __attribute__((address_space(3)))
constexpr size_t LAY_TS = 17408, LAY_HS = 557824, LAY_CRS = 266240, LAY_ORS = 69632, WS_K = 0, WS_V = 17850368, WS_CTX = 35700736, WS_OB = 52740096, WS_FL = 65585152, WS_END = 65634304, LAY_VS = 2;
#define MK_ASM_BODY \
"s_load_dwordx16 s[8:23], %0, 0x0\n" \
"s_load_dwordx4 s[24:27], %0, 0x40\n" \
"s_load_dwordx2 s[28:29], %0, 0x50\n" \
"v_readfirstlane_b32 s61, %2\n" \
"s_lshr_b32 s61, s61, 6\n" \
"v_mbcnt_lo_u32_b32 v46, -1, 0\n" \
"v_mbcnt_hi_u32_b32 v46, -1, v46\n" \
"v_lshlrev_b32 v234, 4, v46\n" \
"s_getpc_b64 s[64:65]\n" \
"Lpc_%=:\n" \
"s_lshr_b32 s70, %1, 3\n" \
"s_lshl_b32 s70, s70, 2\n" \
"s_add_u32 s70, s70, s61\n" \
"s_and_b32 s71, s70, 31\n" \
"s_lshl_b32 s71, s71, 12\n" \
"v_lshl_add_u32 v237, v234, 2, s71\n" \
"s_mov_b32 s71, Lcode_end_%=-Lpc_%=-4\n" \
"v_min_u32 v237, s71, v237\n" \
"global_load_dword v238, v237, s[64:65]\n" \
"s_and_b32 s62, %1, 7\n" \
"s_lshl_b32 s62, s62, 2\n" \
"s_lshr_b32 s70, %1, 6\n" \
"s_add_u32 s62, s62, s70\n" \
"s_bfe_u32 s63, %1, 0x30003\n" \
"s_lshl_b32 s74, s61, 13\n" \
"s_add_u32 s74, s74, %3\n" \
"v_bfe_u32 v47, v46, 4, 1\n" \
"v_bfe_u32 v48, v46, 2, 2\n" \
"v_lshl_add_u32 v48, v47, 3, v48\n" \
"s_lshl_b32 s71, s61, 4\n" \
"v_add_u32 v48, s71, v48\n" \
"v_lshlrev_b32 v48, 13, v48\n" \
"v_lshlrev_b32 v47, 1, v47\n" \
"v_and_b32 v49, 3, v46\n" \
"v_xor_b32 v47, v47, v49\n" \
"v_lshl_add_u32 v48, v47, 4, v48\n" \
"v_lshrrev_b32 v58, 5, v46\n" \
"v_lshl_add_u32 v52, v58, 6, v48\n" \
"v_add_u32 v36, 4096, v52\n" \
"v_add_u32 v37, 3072, v52\n" \
"v_add_u32 v38, 2176, v52\n" \
"v_add_u32 v39, 1152, v52\n" \
"s_lshl_b32 s83, s62, 8\n" \
"s_add_u32 s84, s83, 32768\n" \
"v_lshrrev_b32 v53, 3, v46\n" \
"v_and_b32 v54, 7, v46\n" \
"v_xor_b32 v54, v54, v53\n" \
"v_lshlrev_b32 v54, 4, v54\n" \
"v_lshl_add_u32 v32, v53, 10, v54\n" \
"v_add_u32 v33, 7168, v32\n" \
"v_add_u32 v34, 14336, v32\n" \
"v_add_u32 v35, 21504, v32\n" \
"s_bitcmp1_b32 s61, 0\n" \
"s_cselect_b32 s70, 0, 15\n" \
"s_xor_b32 s81, s63, s70\n" \
"s_lshl_b32 s81, s81, 17\n" \
"s_lshr_b32 s70, s61, 1\n" \
"s_lshl_b32 s71, s70, 16\n" \
"s_add_u32 s81, s81, s71\n" \
"s_add_u32 s82, s81, 32768\n" \
"s_mov_b32 s42, 4198400\n" \
"s_mov_b32 s43, 0x20000\n" \
"s_mov_b32 s46, 4198400\n" \
"s_mov_b32 s47, 0x20000\n" \
"s_mov_b32 s38, 2097152\n" \
"s_mov_b32 s39, 0x20000\n" \
"s_waitcnt lgkmcnt(0)\n" \
"s_mov_b64 s[40:41], s[14:15]\n" \
"s_sub_u32 s40, s40, 4096\n" \
"s_subb_u32 s41, s41, 0\n" \
"s_and_b32 s41, s41, 0xffff\n" \
"s_mov_b64 s[44:45], s[16:17]\n" \
"s_sub_u32 s44, s44, 4096\n" \
"s_subb_u32 s45, s45, 0\n" \
"s_and_b32 s45, s45, 0xffff\n" \
"s_mov_b64 s[36:37], s[10:11]\n" \
"s_and_b32 s37, s37, 0xffff\n" \
"s_add_u32 m0, s74, 32768\n" \
"s_nop 0\n" \
"buffer_load_dwordx4 v36, s[40:43], s83 offen offset:0 lds\n" \
"buffer_load_dwordx4 v37, s[44:47], s83 offen offset:1024 lds\n" \
"buffer_load_dwordx4 v38, s[40:43], s83 offen offset:2048 lds\n" \
"buffer_load_dwordx4 v39, s[44:47], s83 offen offset:3072 lds\n" \
"s_xor_b32 s83, s83, 0x80000\n" \
"s_add_u32 m0, s74, 36864\n" \
"s_nop 0\n" \
"buffer_load_dwordx4 v36, s[40:43], s84 offen offset:0 lds\n" \
"buffer_load_dwordx4 v37, s[44:47], s84 offen offset:1024 lds\n" \
"buffer_load_dwordx4 v38, s[40:43], s84 offen offset:2048 lds\n" \
"buffer_load_dwordx4 v39, s[44:47], s84 offen offset:3072 lds\n" \
"s_xor_b32 s84, s84, 0x80000\n" \
"s_add_u32 m0, s74, 0\n" \
"s_nop 0\n" \
"buffer_load_dwordx4 v32, s[36:39], s81 offen offset:0 lds\n" \
"buffer_load_dwordx4 v33, s[36:39], s81 offen offset:1024 lds\n" \
"buffer_load_dwordx4 v34, s[36:39], s81 offen offset:2048 lds\n" \
"buffer_load_dwordx4 v35, s[36:39], s81 offen offset:3072 lds\n" \
"s_xor_b32 s81, s81, 0x80\n" \
"s_add_u32 m0, s74, 4096\n" \
"s_nop 0\n" \
"buffer_load_dwordx4 v32, s[36:39], s82 offen offset:0 lds\n" \
"buffer_load_dwordx4 v33, s[36:39], s82 offen offset:1024 lds\n" \
"buffer_load_dwordx4 v34, s[36:39], s82 offen offset:2048 lds\n" \
"buffer_load_dwordx4 v35, s[36:39], s82 offen offset:3072 lds\n" \
"s_xor_b32 s82, s82, 0x80\n" \
"v_lshrrev_b32 v55, 4, v46\n" \
"v_and_b32 v47, 15, v46\n" \
"v_and_b32 v48, 7, v46\n" \
"v_xor_b32 v48, v48, v55\n" \
"v_lshlrev_b32 v47, 7, v47\n" \
"s_lshl_b32 s70, s70, 14\n" \
"s_add_u32 s70, s70, %3\n" \
"v_lshl_add_u32 v56, v48, 4, v47\n" \
"v_add_u32 v40, s70, v56\n" \
"v_xor_b32 v41, 64, v40\n" \
"v_lshlrev_b32 v47, 3, v49\n" \
"v_bfe_u32 v48, v46, 2, 2\n" \
"v_lshl_add_u32 v47, v48, 6, v47\n" \
"v_bfe_u32 v48, v46, 4, 1\n" \
"v_lshl_add_u32 v47, v48, 8, v47\n" \
"v_lshl_add_u32 v57, v48, 5, v47\n" \
"s_and_b32 s70, s61, 1\n" \
"s_lshl_b32 s70, s70, 11\n" \
"s_add_u32 s70, s70, %3\n" \
"v_lshl_add_u32 v50, v58, 13, v57\n" \
"v_add_u32 v42, s70, v50\n" \
"v_xor_b32 v43, 32, v42\n" \
"s_mov_b64 s[56:57], s[28:29]\n" \
"s_and_b32 s57, s57, 0xffff\n" \
"s_mov_b32 s58, 65634304\n" \
"s_mov_b32 s59, 0x20000\n" \
"v_mov_b32 v241, 0\n" \
"s_lshl_b32 s66, %1, 2\n" \
"s_add_u32 s66, s66, s61\n" \
"s_lshl_b32 s66, s66, 10\n" \
"s_lshr_b32 s67, %1, 3\n" \
"s_lshl_b32 s67, s67, 2\n" \
"s_add_u32 s67, s67, s61\n" \
"s_lshl_b32 s67, s67, 13\n" \
"s_and_b32 s64, %1, 7\n" \
"s_lshl_b32 s64, s64, 10\n" \
"s_add_u32 s67, s67, s64\n" \
"s_mov_b64 exec, 0\n" \
"s_cmp_eq_u32 s61, 0\n" \
"s_cbranch_scc1 Lst0x0_%=\n" \
"s_cmp_eq_u32 s61, 1\n" \
"s_cbranch_scc1 Lst0x1_%=\n" \
"s_cmp_eq_u32 s61, 2\n" \
"s_cbranch_scc1 Lst0x2_%=\n" \
"s_branch Lst0x3_%=\n" \
"Lcb0_%=:\n" \
"s_mov_b64 exec, -1\n" \
"s_add_u32 m0, s74, 131072\n" \
"s_nop 0\n" \
"buffer_load_dwordx4 v36, s[40:43], s83 offen offset:0 lds\n" \
"buffer_load_dwordx4 v37, s[44:47], s83 offen offset:1024 lds\n" \
"buffer_load_dwordx4 v38, s[40:43], s83 offen offset:2048 lds\n" \
"buffer_load_dwordx4 v39, s[44:47], s83 offen offset:3072 lds\n" \
"s_xor_b32 s83, s83, 0x180000\n" \
"s_add_u32 m0, s74, 135168\n" \
"s_nop 0\n" \
"buffer_load_dwordx4 v36, s[40:43], s84 offen offset:0 lds\n" \
"buffer_load_dwordx4 v37, s[44:47], s84 offen offset:1024 lds\n" \
"buffer_load_dwordx4 v38, s[40:43], s84 offen offset:2048 lds\n" \
"buffer_load_dwordx4 v39, s[44:47], s84 offen offset:3072 lds\n" \
"s_xor_b32 s84, s84, 0x180000\n" \
"s_mov_b32 s93, 65593344\n" \
"buffer_load_dword v240, v241, s[56:59], s93 offen sc1\n" \
"s_mov_b64 s[64:65], s[10:11]\n" \
"s_add_u32 s68, s66, 0x0\n" \
"s_add_u32 s64, s64, s68\n" \
"s_addc_u32 s65, s65, 0\n" \
"global_load_dword v238, v234, s[64:65]\n" \
"s_mov_b64 s[64:65], s[10:11]\n" \
"s_add_u32 s68, s66, 0x100000\n" \
"s_add_u32 s64, s64, s68\n" \
"s_addc_u32 s65, s65, 0\n" \
"global_load_dword v238, v234, s[64:65]\n" \
"s_mov_b64 s[64:65], s[14:15]\n" \
"s_add_u32 s68, s67, 0x100000\n" \
"s_add_u32 s64, s64, s68\n" \
"s_addc_u32 s65, s65, 0\n" \
"global_load_dword v238, v234, s[64:65]\n" \
"s_mov_b64 s[64:65], s[16:17]\n" \
"s_add_u32 s68, s67, 0x100000\n" \
"s_add_u32 s64, s64, s68\n" \
"s_addc_u32 s65, s65, 0\n" \
"global_load_dword v238, v234, s[64:65]\n" \
"s_waitcnt vmcnt(13)\n" \
"s_barrier\n" \
"ds_read_b64_tr_b16 v[128:129], v42 offset:32768\n" \
"ds_read_b64_tr_b16 v[130:131], v42 offset:36864\n" \
"ds_read_b64_tr_b16 v[132:133], v42 offset:49152\n" \
"ds_read_b64_tr_b16 v[134:135], v42 offset:53248\n" \
"ds_read_b128 v[64:67], v40 offset:0\n" \
"ds_read_b128 v[68:71], v41 offset:0\n" \
"ds_read_b64_tr_b16 v[136:137], v43 offset:32768\n" \
"ds_read_b64_tr_b16 v[138:139], v43 offset:36864\n" \
"ds_read_b64_tr_b16 v[140:141], v43 offset:49152\n" \
"ds_read_b64_tr_b16 v[142:143], v43 offset:53248\n" \
"ds_read_b64_tr_b16 v[144:145], v42 offset:33280\n" \
"ds_read_b64_tr_b16 v[146:147], v42 offset:37376\n" \
"ds_read_b64_tr_b16 v[148:149], v42 offset:49664\n" \
"ds_read_b64_tr_b16 v[150:151], v42 offset:53760\n" \
"ds_read_b64_tr_b16 v[152:153], v43 offset:33280\n" \
"ds_read_b64_tr_b16 v[154:155], v43 offset:37376\n" \
"ds_read_b64_tr_b16 v[156:157], v43 offset:49664\n" \
"ds_read_b64_tr_b16 v[158:159], v43 offset:53760\n" \
"ds_read_b128 v[72:75], v40 offset:2048\n" \
"ds_read_b128 v[76:79], v41 offset:2048\n" \
"ds_read_b128 v[80:83], v40 offset:4096\n" \
"ds_read_b128 v[84:87], v41 offset:4096\n" \
"ds_read_b128 v[88:91], v40 offset:6144\n" \
"ds_read_b128 v[92:95], v41 offset:6144\n" \
"ds_read_b128 v[96:99], v40 offset:8192\n" \
"ds_read_b128 v[100:103], v41 offset:8192\n" \
"ds_read_b128 v[104:107], v40 offset:10240\n" \
"ds_read_b128 v[108:111], v41 offset:10240\n" \
"ds_read_b128 v[112:115], v40 offset:12288\n" \
"ds_read_b128 v[116:119], v41 offset:12288\n" \
"s_waitcnt lgkmcnt(15)\n" \
"v_mfma_f32_16x16x32_bf16 a[0:3], v[128:131], v[64:67], 0\n" \
"s_add_u32 m0, s74, 65536\n" \
"ds_read_b128 v[120:123], v40 offset:14336\n" \
"v_mfma_f32_16x16x32_bf16 a[0:3], v[132:135], v[68:71], a[0:3]\n" \
"buffer_load_dwordx4 v32, s[36:39], s81 offen offset:0 lds\n" \
"v_mfma_f32_16x16x32_bf16 a[4:7], v[136:139], v[64:67], 0\n" \
"ds_read_b128 v[124:127], v41 offset:14336\n" \
"v_mfma_f32_16x16x32_bf16 a[4:7], v[140:143], v[68:71], a[4:7]\n" \
"v_mfma_f32_16x16x32_bf16 a[8:11], v[144:147], v[64:67], 0\n" \
"s_waitcnt lgkmcnt(2)\n" \
"ds_read_b64_tr_b16 v[160:161], v42 offset:33792\n" \
"v_mfma_f32_16x16x32_bf16 a[8:11], v[148:151], v[68:71], a[8:11]\n" \
"buffer_load_dwordx4 v33, s[36:39], s81 offen offset:1024 lds\n" \
"v_mfma_f32_16x16x32_bf16 a[12:15], v[152:155], v[64:67], 0\n" \
"ds_read_b64_tr_b16 v[162:163], v42 offset:37888\n" \
"v_mfma_f32_16x16x32_bf16 a[12:15], v[156:159], v[68:71], a[12:15]\n" \
"v_mfma_f32_16x16x32_bf16 a[44:47], v[152:155], v[72:75], 0\n" \
"ds_read_b64_tr_b16 v[164:165], v42 offset:50176\n" \
"v_mfma_f32_16x16x32_bf16 a[44:47], v[156:159], v[76:79], a[44:47]\n" \
"buffer_load_dwordx4 v34, s[36:39], s81 offen offset:2048 lds\n" \
"v_mfma_f32_16x16x32_bf16 a[40:43], v[144:147], v[72:75], 0\n" \
"ds_read_b64_tr_b16 v[166:167], v42 offset:54272\n" \
"v_mfma_f32_16x16x32_bf16 a[40:43], v[148:151], v[76:79], a[40:43]\n" \
"v_mfma_f32_16x16x32_bf16 a[36:39], v[136:139], v[72:75], 0\n" \
"ds_read_b64_tr_b16 v[168:169], v43 offset:33792\n" \
"v_mfma_f32_16x16x32_bf16 a[36:39], v[140:143], v[76:79], a[36:39]\n" \
"buffer_load_dwordx4 v35, s[36:39], s81 offen offset:3072 lds\n" \
"v_mfma_f32_16x16x32_bf16 a[32:35], v[128:131], v[72:75], 0\n" \
"ds_read_b64_tr_b16 v[170:171], v43 offset:37888\n" \
"v_mfma_f32_16x16x32_bf16 a[32:35], v[132:135], v[76:79], a[32:35]\n" \
"s_xor_b32 s81, s81, 0x180\n" \
"v_mfma_f32_16x16x32_bf16 a[64:67], v[128:131], v[80:83], 0\n" \
"s_add_u32 m0, s74, 69632\n" \
"ds_read_b64_tr_b16 v[172:173], v43 offset:50176\n" \
"v_mfma_f32_16x16x32_bf16 a[64:67], v[132:135], v[84:87], a[64:67]\n" \
"buffer_load_dwordx4 v32, s[36:39], s82 offen offset:0 lds\n" \
"v_mfma_f32_16x16x32_bf16 a[68:71], v[136:139], v[80:83], 0\n" \
"ds_read_b64_tr_b16 v[174:175], v43 offset:54272\n" \
"v_mfma_f32_16x16x32_bf16 a[68:71], v[140:143], v[84:87], a[68:71]\n" \
"v_mfma_f32_16x16x32_bf16 a[72:75], v[144:147], v[80:83], 0\n" \
"ds_read_b64_tr_b16 v[176:177], v42 offset:34304\n" \
"v_mfma_f32_16x16x32_bf16 a[72:75], v[148:151], v[84:87], a[72:75]\n" \
"buffer_load_dwordx4 v33, s[36:39], s82 offen offset:1024 lds\n" \
"v_mfma_f32_16x16x32_bf16 a[76:79], v[152:155], v[80:83], 0\n" \
"ds_read_b64_tr_b16 v[178:179], v42 offset:38400\n" \
"v_mfma_f32_16x16x32_bf16 a[76:79], v[156:159], v[84:87], a[76:79]\n" \
"v_mfma_f32_16x16x32_bf16 a[108:111], v[152:155], v[88:91], 0\n" \
"ds_read_b64_tr_b16 v[180:181], v42 offset:50688\n" \
"v_mfma_f32_16x16x32_bf16 a[108:111], v[156:159], v[92:95], a[108:111]\n" \
"buffer_load_dwordx4 v34, s[36:39], s82 offen offset:2048 lds\n" \
"v_mfma_f32_16x16x32_bf16 a[104:107], v[144:147], v[88:91], 0\n" \
"ds_read_b64_tr_b16 v[182:183], v42 offset:54784\n" \
"v_mfma_f32_16x16x32_bf16 a[104:107], v[148:151], v[92:95], a[104:107]\n" \
"v_mfma_f32_16x16x32_bf16 a[100:103], v[136:139], v[88:91], 0\n" \
"ds_read_b64_tr_b16 v[184:185], v43 offset:34304\n" \
"v_mfma_f32_16x16x32_bf16 a[100:103], v[140:143], v[92:95], a[100:103]\n" \
"buffer_load_dwordx4 v35, s[36:39], s82 offen offset:3072 lds\n" \
"v_mfma_f32_16x16x32_bf16 a[96:99], v[128:131], v[88:91], 0\n" \
"ds_read_b64_tr_b16 v[186:187], v43 offset:38400\n" \
"v_mfma_f32_16x16x32_bf16 a[96:99], v[132:135], v[92:95], a[96:99]\n" \
"v_mfma_f32_16x16x32_bf16 a[128:131], v[128:131], v[96:99], 0\n" \
"s_xor_b32 s82, s82, 0x180\n" \
"ds_read_b64_tr_b16 v[188:189], v43 offset:50688\n" \
"v_mfma_f32_16x16x32_bf16 a[128:131], v[132:135], v[100:103], a[128:131]\n" \
"v_mfma_f32_16x16x32_bf16 a[132:135], v[136:139], v[96:99], 0\n" \
"ds_read_b64_tr_b16 v[190:191], v43 offset:54784\n" \
"v_mfma_f32_16x16x32_bf16 a[132:135], v[140:143], v[100:103], a[132:135]\n" \
"v_mfma_f32_16x16x32_bf16 a[136:139], v[144:147], v[96:99], 0\n" \
"v_mfma_f32_16x16x32_bf16 a[136:139], v[148:151], v[100:103], a[136:139]\n" \
"v_mfma_f32_16x16x32_bf16 a[140:143], v[152:155], v[96:99], 0\n" \
"v_mfma_f32_16x16x32_bf16 a[140:143], v[156:159], v[100:103], a[140:143]\n" \
"v_mfma_f32_16x16x32_bf16 a[172:175], v[152:155], v[104:107], 0\n" \
"v_add_u32 v44, 0x10000, v40\n" \
"v_mfma_f32_16x16x32_bf16 a[172:175], v[156:159], v[108:111], a[172:175]\n" \
"v_mfma_f32_16x16x32_bf16 a[168:171], v[144:147], v[104:107], 0\n" \
"v_add_u32 v45, 0x10000, v41\n" \
"v_mfma_f32_16x16x32_bf16 a[168:171], v[148:151], v[108:111], a[168:171]\n" \
"v_mfma_f32_16x16x32_bf16 a[164:167], v[136:139], v[104:107], 0\n" \
"v_add_u32 v59, 0x10000, v42\n" \
"v_mfma_f32_16x16x32_bf16 a[164:167], v[140:143], v[108:111], a[164:167]\n" \
"v_mfma_f32_16x16x32_bf16 a[160:163], v[128:131], v[104:107], 0\n" \
"v_add_u32 v60, 0x10000, v43\n" \
"v_mfma_f32_16x16x32_bf16 a[160:163], v[132:135], v[108:111], a[160:163]\n" \
"v_mfma_f32_16x16x32_bf16 a[192:195], v[128:131], v[112:115], 0\n" \
"s_add_u32 m0, s74, 98304\n" \
"v_add_u32 v61, 0x20000, v40\n" \
"v_mfma_f32_16x16x32_bf16 a[192:195], v[132:135], v[116:119], a[192:195]\n" \
"buffer_load_dwordx4 v36, s[40:43], s83 offen offset:0 lds\n" \
"v_mfma_f32_16x16x32_bf16 a[196:199], v[136:139], v[112:115], 0\n" \
"v_add_u32 v62, 0x20000, v41\n" \
"v_mfma_f32_16x16x32_bf16 a[196:199], v[140:143], v[116:119], a[196:199]\n" \
"v_mfma_f32_16x16x32_bf16 a[200:203], v[144:147], v[112:115], 0\n" \
"v_add_u32 v63, 0x20000, v42\n" \
"v_mfma_f32_16x16x32_bf16 a[200:203], v[148:151], v[116:119], a[200:203]\n" \
"buffer_load_dwordx4 v37, s[44:47], s83 offen offset:1024 lds\n" \
"v_mfma_f32_16x16x32_bf16 a[204:207], v[152:155], v[112:115], 0\n" \
"v_add_u32 v252, 0x20000, v43\n" \
"v_mfma_f32_16x16x32_bf16 a[204:207], v[156:159], v[116:119], a[204:207]\n" \
"s_waitcnt lgkmcnt(0)\n" \
"v_mfma_f32_16x16x32_bf16 a[236:239], v[152:155], v[120:123], 0\n" \
"v_mfma_f32_16x16x32_bf16 a[236:239], v[156:159], v[124:127], a[236:239]\n" \
"buffer_load_dwordx4 v38, s[40:43], s83 offen offset:2048 lds\n" \
"v_mfma_f32_16x16x32_bf16 a[232:235], v[144:147], v[120:123], 0\n" \
"v_mfma_f32_16x16x32_bf16 a[232:235], v[148:151], v[124:127], a[232:235]\n" \
"v_mfma_f32_16x16x32_bf16 a[228:231], v[136:139], v[120:123], 0\n" \
"v_mfma_f32_16x16x32_bf16 a[228:231], v[140:143], v[124:127], a[228:231]\n" \
"buffer_load_dwordx4 v39, s[44:47], s83 offen offset:3072 lds\n" \
"v_mfma_f32_16x16x32_bf16 a[224:227], v[128:131], v[120:123], 0\n" \
"v_mfma_f32_16x16x32_bf16 a[224:227], v[132:135], v[124:127], a[224:227]\n" \
"s_xor_b32 s83, s83, 0x80000\n" \
"s_waitcnt lgkmcnt(0)\n" \
"s_barrier\n" \
"v_mfma_f32_16x16x32_bf16 a[16:19], v[64:67], v[160:163], 0\n" \
"s_add_u32 m0, s74, 102400\n" \
"v_mfma_f32_16x16x32_bf16 a[16:19], v[68:71], v[164:167], a[16:19]\n" \
"buffer_load_dwordx4 v36, s[40:43], s84 offen offset:0 lds\n" \
"v_mfma_f32_16x16x32_bf16 a[20:23], v[64:67], v[168:171], 0\n" \
"v_mfma_f32_16x16x32_bf16 a[20:23], v[68:71], v[172:175], a[20:23]\n" \
"v_mfma_f32_16x16x32_bf16 a[24:27], v[64:67], v[176:179], 0\n" \
"v_mfma_f32_16x16x32_bf16 a[24:27], v[68:71], v[180:183], a[24:27]\n" \
"buffer_load_dwordx4 v37, s[44:47], s84 offen offset:1024 lds\n" \
"v_mfma_f32_16x16x32_bf16 a[28:31], v[64:67], v[184:187], 0\n" \
"v_mfma_f32_16x16x32_bf16 a[28:31], v[68:71], v[188:191], a[28:31]\n" \
"v_mfma_f32_16x16x32_bf16 a[60:63], v[72:75], v[184:187], 0\n" \
"v_mfma_f32_16x16x32_bf16 a[60:63], v[76:79], v[188:191], a[60:63]\n" \
"buffer_load_dwordx4 v38, s[40:43], s84 offen offset:2048 lds\n" \
"v_mfma_f32_16x16x32_bf16 a[56:59], v[72:75], v[176:179], 0\n" \
"v_mfma_f32_16x16x32_bf16 a[56:59], v[76:79], v[180:183], a[56:59]\n" \
"v_mfma_f32_16x16x32_bf16 a[52:55], v[72:75], v[168:171], 0\n" \
"v_mfma_f32_16x16x32_bf16 a[52:55], v[76:79], v[172:175], a[52:55]\n" \
"buffer_load_dwordx4 v39, s[44:47], s84 offen offset:3072 lds\n" \
"v_mfma_f32_16x16x32_bf16 a[48:51], v[72:75], v[160:163], 0\n" \
"v_mfma_f32_16x16x32_bf16 a[48:51], v[76:79], v[164:167], a[48:51]\n" \
"v_mfma_f32_16x16x32_bf16 a[80:83], v[80:83], v[160:163], 0\n" \
"s_xor_b32 s84, s84, 0x80000\n" \
"v_mfma_f32_16x16x32_bf16 a[80:83], v[84:87], v[164:167], a[80:83]\n" \
"v_mfma_f32_16x16x32_bf16 a[84:87], v[80:83], v[168:171], 0\n" \
"v_mfma_f32_16x16x32_bf16 a[84:87], v[84:87], v[172:175], a[84:87]\n" \
"v_mfma_f32_16x16x32_bf16 a[88:91], v[80:83], v[176:179], 0\n" \
"s_add_u32 m0, s74, 32768\n" \
"v_mfma_f32_16x16x32_bf16 a[88:91], v[84:87], v[180:183], a[88:91]\n" \
"buffer_load_dwordx4 v32, s[36:39], s81 offen offset:0 lds\n" \
"v_mfma_f32_16x16x32_bf16 a[92:95], v[80:83], v[184:187], 0\n" \
"v_mfma_f32_16x16x32_bf16 a[92:95], v[84:87], v[188:191], a[92:95]\n" \
"v_mfma_f32_16x16x32_bf16 a[124:127], v[88:91], v[184:187], 0\n" \
"v_mfma_f32_16x16x32_bf16 a[124:127], v[92:95], v[188:191], a[124:127]\n" \
"buffer_load_dwordx4 v33, s[36:39], s81 offen offset:1024 lds\n" \
"v_mfma_f32_16x16x32_bf16 a[120:123], v[88:91], v[176:179], 0\n" \
"v_mfma_f32_16x16x32_bf16 a[120:123], v[92:95], v[180:183], a[120:123]\n" \
"v_mfma_f32_16x16x32_bf16 a[116:119], v[88:91], v[168:171], 0\n" \
"v_mfma_f32_16x16x32_bf16 a[116:119], v[92:95], v[172:175], a[116:119]\n" \
"buffer_load_dwordx4 v34, s[36:39], s81 offen offset:2048 lds\n" \
"v_mfma_f32_16x16x32_bf16 a[112:115], v[88:91], v[160:163], 0\n" \
"v_mfma_f32_16x16x32_bf16 a[112:115], v[92:95], v[164:167], a[112:115]\n" \
"v_mfma_f32_16x16x32_bf16 a[144:147], v[96:99], v[160:163], 0\n" \
"v_mfma_f32_16x16x32_bf16 a[144:147], v[100:103], v[164:167], a[144:147]\n" \
"buffer_load_dwordx4 v35, s[36:39], s81 offen offset:3072 lds\n" \
"v_mfma_f32_16x16x32_bf16 a[148:151], v[96:99], v[168:171], 0\n" \
"v_mfma_f32_16x16x32_bf16 a[148:151], v[100:103], v[172:175], a[148:151]\n" \
"s_xor_b32 s81, s81, 0x80\n" \
"v_mfma_f32_16x16x32_bf16 a[152:155], v[96:99], v[176:179], 0\n" \
"s_add_u32 m0, s74, 36864\n" \
"v_mfma_f32_16x16x32_bf16 a[152:155], v[100:103], v[180:183], a[152:155]\n" \
"buffer_load_dwordx4 v32, s[36:39], s82 offen offset:0 lds\n" \
"v_mfma_f32_16x16x32_bf16 a[156:159], v[96:99], v[184:187], 0\n" \
"v_mfma_f32_16x16x32_bf16 a[156:159], v[100:103], v[188:191], a[156:159]\n" \
"s_waitcnt vmcnt(13) lgkmcnt(0)\n" \
"s_barrier\n" \
"v_mfma_f32_16x16x32_bf16 a[188:191], v[104:107], v[184:187], 0\n" \
"ds_read_b64_tr_b16 v[128:129], v63 offset:0\n" \
"ds_read_b64_tr_b16 v[130:131], v63 offset:4096\n" \
"v_mfma_f32_16x16x32_bf16 a[188:191], v[108:111], v[188:191], a[188:191]\n" \
"buffer_load_dwordx4 v33, s[36:39], s82 offen offset:1024 lds\n" \
"v_mfma_f32_16x16x32_bf16 a[184:187], v[104:107], v[176:179], 0\n" \
"ds_read_b64_tr_b16 v[132:133], v63 offset:16384\n" \
"ds_read_b64_tr_b16 v[134:135], v63 offset:20480\n" \
"v_mfma_f32_16x16x32_bf16 a[184:187], v[108:111], v[180:183], a[184:187]\n" \
"ds_read_b128 v[64:67], v44 offset:0\n" \
"ds_read_b128 v[68:71], v45 offset:0\n" \
"v_mfma_f32_16x16x32_bf16 a[180:183], v[104:107], v[168:171], 0\n" \
"ds_read_b64_tr_b16 v[136:137], v252 offset:0\n" \
"ds_read_b64_tr_b16 v[138:139], v252 offset:4096\n" \
"v_mfma_f32_16x16x32_bf16 a[180:183], v[108:111], v[172:175], a[180:183]\n" \
"buffer_load_dwordx4 v34, s[36:39], s82 offen offset:2048 lds\n" \
"v_mfma_f32_16x16x32_bf16 a[176:179], v[104:107], v[160:163], 0\n" \
"ds_read_b64_tr_b16 v[140:141], v252 offset:16384\n" \
"ds_read_b64_tr_b16 v[142:143], v252 offset:20480\n" \
"v_mfma_f32_16x16x32_bf16 a[176:179], v[108:111], v[164:167], a[176:179]\n" \
"ds_read_b64_tr_b16 v[144:145], v63 offset:512\n" \
"ds_read_b64_tr_b16 v[146:147], v63 offset:4608\n" \
"v_mfma_f32_16x16x32_bf16 a[208:211], v[112:115], v[160:163], 0\n" \
"ds_read_b64_tr_b16 v[148:149], v63 offset:16896\n" \
"ds_read_b64_tr_b16 v[150:151], v63 offset:20992\n" \
"v_mfma_f32_16x16x32_bf16 a[208:211], v[116:119], v[164:167], a[208:211]\n" \
"buffer_load_dwordx4 v35, s[36:39], s82 offen offset:3072 lds\n" \
"v_mfma_f32_16x16x32_bf16 a[212:215], v[112:115], v[168:171], 0\n" \
"ds_read_b64_tr_b16 v[152:153], v252 offset:512\n" \
"ds_read_b64_tr_b16 v[154:155], v252 offset:4608\n" \
"v_mfma_f32_16x16x32_bf16 a[212:215], v[116:119], v[172:175], a[212:215]\n" \
"ds_read_b64_tr_b16 v[156:157], v252 offset:16896\n" \
"ds_read_b64_tr_b16 v[158:159], v252 offset:20992\n" \
"v_mfma_f32_16x16x32_bf16 a[216:219], v[112:115], v[176:179], 0\n" \
"s_xor_b32 s82, s82, 0x80\n" \
"ds_read_b128 v[72:75], v44 offset:2048\n" \
"ds_read_b128 v[76:79], v45 offset:2048\n" \
"v_mfma_f32_16x16x32_bf16 a[216:219], v[116:119], v[180:183], a[216:219]\n" \
"v_mfma_f32_16x16x32_bf16 a[220:223], v[112:115], v[184:187], 0\n" \
"ds_read_b128 v[80:83], v44 offset:4096\n" \
"ds_read_b128 v[84:87], v45 offset:4096\n" \
"v_mfma_f32_16x16x32_bf16 a[220:223], v[116:119], v[188:191], a[220:223]\n" \
"ds_read_b128 v[88:91], v44 offset:6144\n" \
"ds_read_b128 v[92:95], v45 offset:6144\n" \
"v_mfma_f32_16x16x32_bf16 a[252:255], v[120:123], v[184:187], 0\n" \
"ds_read_b128 v[96:99], v44 offset:8192\n" \
"ds_read_b128 v[100:103], v45 offset:8192\n" \
"v_mfma_f32_16x16x32_bf16 a[252:255], v[124:127], v[188:191], a[252:255]\n" \
"v_mfma_f32_16x16x32_bf16 a[248:251], v[120:123], v[176:179], 0\n" \
"ds_read_b128 v[104:107], v44 offset:10240\n" \
"ds_read_b128 v[108:111], v45 offset:10240\n" \
"v_mfma_f32_16x16x32_bf16 a[248:251], v[124:127], v[180:183], a[248:251]\n" \
"ds_read_b128 v[112:115], v44 offset:12288\n" \
"ds_read_b128 v[116:119], v45 offset:12288\n" \
"v_mfma_f32_16x16x32_bf16 a[244:247], v[120:123], v[168:171], 0\n" \
"v_mfma_f32_16x16x32_bf16 a[244:247], v[124:127], v[172:175], a[244:247]\n" \
"v_mfma_f32_16x16x32_bf16 a[240:243], v[120:123], v[160:163], 0\n" \
"v_mfma_f32_16x16x32_bf16 a[240:243], v[124:127], v[164:167], a[240:243]\n" \
"s_waitcnt lgkmcnt(2)\n" \
"v_mfma_f32_16x16x32_bf16 a[0:3], v[128:131], v[64:67], a[0:3]\n" \
"s_lshl_b32 s79, s61, 12\n" \
"ds_read_b128 v[120:123], v44 offset:14336\n" \
"v_mfma_f32_16x16x32_bf16 a[0:3], v[132:135], v[68:71], a[0:3]\n" \
"s_add_u32 s79, s79, %3\n" \
"v_add_u32 v228, 4096, v52\n" \
"v_add_u32 v229, 3200, v52\n" \
"v_mfma_f32_16x16x32_bf16 a[4:7], v[136:139], v[64:67], a[4:7]\n" \
"s_add_u32 m0, s74, 0\n" \
"ds_read_b128 v[124:127], v45 offset:14336\n" \
"v_mfma_f32_16x16x32_bf16 a[4:7], v[140:143], v[68:71], a[4:7]\n" \
"buffer_load_dwordx4 v36, s[40:43], s83 offen offset:0 lds\n" \
"v_mfma_f32_16x16x32_bf16 a[8:11], v[144:147], v[64:67], a[8:11]\n" \
"s_lshl_b32 s78, s62, 8\n" \
"ds_read_b64_tr_b16 v[160:161], v63 offset:1024\n" \
"v_mfma_f32_16x16x32_bf16 a[8:11], v[148:151], v[68:71], a[8:11]\n" \
"s_add_u32 s89, s78, 30720\n" \
"v_mfma_f32_16x16x32_bf16 a[12:15], v[152:155], v[64:67], a[12:15]\n" \
"s_mov_b64 s[48:49], s[8:9]\n" \
"ds_read_b64_tr_b16 v[162:163], v63 offset:5120\n" \
"v_mfma_f32_16x16x32_bf16 a[12:15], v[156:159], v[68:71], a[12:15]\n" \
"buffer_load_dwordx4 v37, s[44:47], s83 offen offset:1024 lds\n" \
"v_mfma_f32_16x16x32_bf16 a[44:47], v[152:155], v[72:75], a[44:47]\n" \
"s_and_b32 s49, s49, 0xffff\n" \
"ds_read_b64_tr_b16 v[164:165], v63 offset:17408\n" \
"v_mfma_f32_16x16x32_bf16 a[44:47], v[156:159], v[76:79], a[44:47]\n" \
"s_mov_b32 s50, 4194304\n" \
"v_mfma_f32_16x16x32_bf16 a[40:43], v[144:147], v[72:75], a[40:43]\n" \
"s_mov_b32 s51, 0x20000\n" \
"ds_read_b64_tr_b16 v[166:167], v63 offset:21504\n" \
"v_mfma_f32_16x16x32_bf16 a[40:43], v[148:151], v[76:79], a[40:43]\n" \
"buffer_load_dwordx4 v38, s[40:43], s83 offen offset:2048 lds\n" \
"v_mfma_f32_16x16x32_bf16 a[36:39], v[136:139], v[72:75], a[36:39]\n" \
"s_mov_b64 s[52:53], s[12:13]\n" \
"ds_read_b64_tr_b16 v[168:169], v252 offset:1024\n" \
"v_mfma_f32_16x16x32_bf16 a[36:39], v[140:143], v[76:79], a[36:39]\n" \
"s_sub_u32 s52, s52, 4096\n" \
"s_subb_u32 s53, s53, 0\n" \
"v_mfma_f32_16x16x32_bf16 a[32:35], v[128:131], v[72:75], a[32:35]\n" \
"s_and_b32 s53, s53, 0xffff\n" \
"ds_read_b64_tr_b16 v[170:171], v252 offset:5120\n" \
"v_mfma_f32_16x16x32_bf16 a[32:35], v[132:135], v[76:79], a[32:35]\n" \
"buffer_load_dwordx4 v39, s[44:47], s83 offen offset:3072 lds\n" \
"v_mfma_f32_16x16x32_bf16 a[64:67], v[128:131], v[80:83], a[64:67]\n" \
"s_mov_b32 s54, 8392704\n" \
"s_mov_b32 s55, 0x20000\n" \
"ds_read_b64_tr_b16 v[172:173], v252 offset:17408\n" \
"v_mfma_f32_16x16x32_bf16 a[64:67], v[132:135], v[84:87], a[64:67]\n" \
"s_xor_b32 s83, s83, 0x380000\n" \
"v_lshl_add_u32 v224, v53, 11, v54\n" \
"v_add_u32 v225, 15360, v224\n" \
"v_mfma_f32_16x16x32_bf16 a[68:71], v[136:139], v[80:83], a[68:71]\n" \
"s_add_u32 m0, s74, 4096\n" \
"ds_read_b64_tr_b16 v[174:175], v252 offset:21504\n" \
"v_add_u32 v226, 129024, v224\n" \
"v_mfma_f32_16x16x32_bf16 a[68:71], v[140:143], v[84:87], a[68:71]\n" \
"buffer_load_dwordx4 v36, s[40:43], s84 offen offset:0 lds\n" \
"v_mfma_f32_16x16x32_bf16 a[72:75], v[144:147], v[80:83], a[72:75]\n" \
"ds_read_b64_tr_b16 v[176:177], v63 offset:1536\n" \
"v_add_u32 v227, 144384, v224\n" \
"v_mfma_f32_16x16x32_bf16 a[72:75], v[148:151], v[84:87], a[72:75]\n" \
"s_lshl_b32 s70, s61, 15\n" \
"v_mfma_f32_16x16x32_bf16 a[76:79], v[152:155], v[80:83], a[76:79]\n" \
"s_lshl_b32 s75, s63, 18\n" \
"ds_read_b64_tr_b16 v[178:179], v63 offset:5632\n" \
"v_mfma_f32_16x16x32_bf16 a[76:79], v[156:159], v[84:87], a[76:79]\n" \
"buffer_load_dwordx4 v37, s[44:47], s84 offen offset:1024 lds\n" \
"v_mfma_f32_16x16x32_bf16 a[108:111], v[152:155], v[88:91], a[108:111]\n" \
"s_sub_u32 s77, 3932160, s75\n" \
"ds_read_b64_tr_b16 v[180:181], v63 offset:17920\n" \
"v_mfma_f32_16x16x32_bf16 a[108:111], v[156:159], v[92:95], a[108:111]\n" \
"s_lshr_b32 s71, %1, 6\n" \
"v_mfma_f32_16x16x32_bf16 a[104:107], v[144:147], v[88:91], a[104:107]\n" \
"s_xor_b32 s71, s71, s63\n" \
"ds_read_b64_tr_b16 v[182:183], v63 offset:22016\n" \
"v_mfma_f32_16x16x32_bf16 a[104:107], v[148:151], v[92:95], a[104:107]\n" \
"buffer_load_dwordx4 v38, s[40:43], s84 offen offset:2048 lds\n" \
"v_mfma_f32_16x16x32_bf16 a[100:103], v[136:139], v[88:91], a[100:103]\n" \
"s_and_b32 s71, s71, 0\n" \
"ds_read_b64_tr_b16 v[184:185], v252 offset:1536\n" \
"v_mfma_f32_16x16x32_bf16 a[100:103], v[140:143], v[92:95], a[100:103]\n" \
"s_lshl_b32 s71, s71, 7\n" \
"v_mfma_f32_16x16x32_bf16 a[96:99], v[128:131], v[88:91], a[96:99]\n" \
"s_add_u32 s70, s70, s71\n" \
"ds_read_b64_tr_b16 v[186:187], v252 offset:5632\n" \
"v_mfma_f32_16x16x32_bf16 a[96:99], v[132:135], v[92:95], a[96:99]\n" \
"buffer_load_dwordx4 v39, s[44:47], s84 offen offset:3072 lds\n" \
"v_mfma_f32_16x16x32_bf16 a[128:131], v[128:131], v[96:99], a[128:131]\n" \
"s_add_u32 s75, s75, s70\n" \
"ds_read_b64_tr_b16 v[188:189], v252 offset:17920\n" \
"v_mfma_f32_16x16x32_bf16 a[128:131], v[132:135], v[100:103], a[128:131]\n" \
"s_add_u32 s77, s77, s70\n" \
"v_mfma_f32_16x16x32_bf16 a[132:135], v[136:139], v[96:99], a[132:135]\n" \
"s_xor_b32 s84, s84, 0x380000\n" \
"ds_read_b64_tr_b16 v[190:191], v252 offset:22016\n" \
"v_mfma_f32_16x16x32_bf16 a[132:135], v[140:143], v[100:103], a[132:135]\n" \
"s_lshl_b32 s71, s71, 12\n" \
"v_mfma_f32_16x16x32_bf16 a[136:139], v[144:147], v[96:99], a[136:139]\n" \
"s_add_u32 s78, s78, s71\n" \
"v_mfma_f32_16x16x32_bf16 a[136:139], v[148:151], v[100:103], a[136:139]\n" \
"s_add_u32 s89, s89, s71\n" \
"v_lshlrev_b32 v242, 2, v46\n" \
"v_and_b32 v236, 0xf0, v234\n" \
"v_mfma_f32_16x16x32_bf16 a[140:143], v[152:155], v[96:99], a[140:143]\n" \
"v_lshl_add_u32 v236, v55, 9, v236\n" \
"v_mfma_f32_16x16x32_bf16 a[140:143], v[156:159], v[100:103], a[140:143]\n" \
"s_add_u32 s71, s74, 0x10000\n" \
"v_add_u32 v230, s71, v56\n" \
"v_xor_b32 v231, 64, v230\n" \
"v_mfma_f32_16x16x32_bf16 a[172:175], v[152:155], v[104:107], a[172:175]\n" \
"v_add_u32 v250, 0x10000, v230\n" \
"v_add_u32 v251, 0x10000, v231\n" \
"v_mfma_f32_16x16x32_bf16 a[172:175], v[156:159], v[108:111], a[172:175]\n" \
"v_lshl_add_u32 v50, v58, 12, v57\n" \
"v_add_u32 v232, %3, v50\n" \
"v_mfma_f32_16x16x32_bf16 a[168:171], v[144:147], v[104:107], a[168:171]\n" \
"v_xor_b32 v233, 32, v232\n" \
"v_mfma_f32_16x16x32_bf16 a[168:171], v[148:151], v[108:111], a[168:171]\n" \
"s_mul_i32 s87, s62, 557824\n" \
"v_mfma_f32_16x16x32_bf16 a[164:167], v[136:139], v[104:107], a[164:167]\n" \
"s_lshr_b32 s70, s61, 1\n" \
"v_mfma_f32_16x16x32_bf16 a[164:167], v[140:143], v[108:111], a[164:167]\n" \
"s_mul_i32 s70, s70, 17408\n" \
"v_mfma_f32_16x16x32_bf16 a[160:163], v[128:131], v[104:107], a[160:163]\n" \
"s_add_u32 s87, s87, s70\n" \
"v_mfma_f32_16x16x32_bf16 a[160:163], v[132:135], v[108:111], a[160:163]\n" \
"s_waitcnt lgkmcnt(0)\n" \
"s_and_b32 s70, s61, 1\n" \
"v_mfma_f32_16x16x32_bf16 a[192:195], v[128:131], v[112:115], a[192:195]\n" \
"s_lshl_b32 s70, s70, 12\n" \
"v_mfma_f32_16x16x32_bf16 a[192:195], v[132:135], v[116:119], a[192:195]\n" \
"s_add_u32 s87, s87, s70\n" \
"v_mfma_f32_16x16x32_bf16 a[196:199], v[136:139], v[112:115], a[196:199]\n" \
"s_mul_i32 s70, s63, 34816\n" \
"v_mfma_f32_16x16x32_bf16 a[196:199], v[140:143], v[116:119], a[196:199]\n" \
"Lpg583_%=:\n" \
"Lst0x0_%=:\n" \
"s_cbranch_execz Lpg2748_%=\n" \
"s_add_u32 s94, s87, 522240\n" \
"v_mfma_f32_16x16x32_bf16 a[200:203], v[144:147], v[112:115], a[200:203]\n" \
"s_sub_u32 s94, s94, s70\n" \
"v_mfma_f32_16x16x32_bf16 a[200:203], v[148:151], v[116:119], a[200:203]\n" \
"s_add_u32 s87, s87, s70\n" \
"v_mfma_f32_16x16x32_bf16 a[204:207], v[152:155], v[112:115], a[204:207]\n" \
"s_lshl_b32 s90, s62, 7\n" \
"v_mfma_f32_16x16x32_bf16 a[204:207], v[156:159], v[116:119], a[204:207]\n" \
"s_add_u32 s95, s90, 65597440\n" \
"v_mfma_f32_16x16x32_bf16 a[236:239], v[152:155], v[120:123], a[236:239]\n" \
"s_add_u32 s90, s90, 65585152\n" \
"v_mfma_f32_16x16x32_bf16 a[236:239], v[156:159], v[124:127], a[236:239]\n" \
"s_lshl_b32 s92, 0xff, s63\n" \
"v_mfma_f32_16x16x32_bf16 a[232:235], v[144:147], v[120:123], a[232:235]\n" \
"s_and_b32 s92, s92, 0xff\n" \
"v_mfma_f32_16x16x32_bf16 a[232:235], v[148:151], v[124:127], a[232:235]\n" \
"s_mov_b32 s76, 0x3e0293ee\n" \
"v_mfma_f32_16x16x32_bf16 a[228:231], v[136:139], v[120:123], a[228:231]\n" \
"s_mov_b32 s31, 0x3e800000\n" \
"v_mfma_f32_16x16x32_bf16 a[228:231], v[140:143], v[124:127], a[228:231]\n" \
"v_mfma_f32_16x16x32_bf16 a[224:227], v[128:131], v[120:123], a[224:227]\n" \
"v_mfma_f32_16x16x32_bf16 a[224:227], v[132:135], v[124:127], a[224:227]\n" \
"s_waitcnt vmcnt(8) lgkmcnt(0)\n" \
"s_barrier\n" \
"v_readfirstlane_b32 s60, v240\n" \
"s_add_u32 s60, s60, 0x9e3779b9\n" \
"v_mfma_f32_16x16x32_bf16 a[16:19], v[64:67], v[160:163], a[16:19]\n" \
"s_add_u32 m0, s74, 131072\n" \
"ds_read_b64_tr_b16 v[128:129], v59 offset:32768\n" \
"v_mfma_f32_16x16x32_bf16 a[16:19], v[68:71], v[164:167], a[16:19]\n" \
"buffer_load_dwordx4 v32, s[36:39], s81 offen offset:0 lds\n" \
"v_mfma_f32_16x16x32_bf16 a[20:23], v[64:67], v[168:171], a[20:23]\n" \
"ds_read_b64_tr_b16 v[130:131], v59 offset:36864\n" \
"v_mfma_f32_16x16x32_bf16 a[20:23], v[68:71], v[172:175], a[20:23]\n" \
"v_mfma_f32_16x16x32_bf16 a[24:27], v[64:67], v[176:179], a[24:27]\n" \
"ds_read_b64_tr_b16 v[132:133], v59 offset:49152\n" \
"v_mfma_f32_16x16x32_bf16 a[24:27], v[68:71], v[180:183], a[24:27]\n" \
"buffer_load_dwordx4 v33, s[36:39], s81 offen offset:1024 lds\n" \
"v_mfma_f32_16x16x32_bf16 a[28:31], v[64:67], v[184:187], a[28:31]\n" \
"ds_read_b64_tr_b16 v[134:135], v59 offset:53248\n" \
"v_mfma_f32_16x16x32_bf16 a[28:31], v[68:71], v[188:191], a[28:31]\n" \
"v_mfma_f32_16x16x32_bf16 a[60:63], v[72:75], v[184:187], a[60:63]\n" \
"ds_read_b128 v[64:67], v40 offset:32768\n" \
"v_mfma_f32_16x16x32_bf16 a[60:63], v[76:79], v[188:191], a[60:63]\n" \
"buffer_load_dwordx4 v34, s[36:39], s81 offen offset:2048 lds\n" \
"v_mfma_f32_16x16x32_bf16 a[56:59], v[72:75], v[176:179], a[56:59]\n" \
"ds_read_b64_tr_b16 v[136:137], v60 offset:32768\n" \
"v_mfma_f32_16x16x32_bf16 a[56:59], v[76:79], v[180:183], a[56:59]\n" \
"v_mfma_f32_16x16x32_bf16 a[52:55], v[72:75], v[168:171], a[52:55]\n" \
"ds_read_b128 v[68:71], v41 offset:32768\n" \
"v_mfma_f32_16x16x32_bf16 a[52:55], v[76:79], v[172:175], a[52:55]\n" \
"buffer_load_dwordx4 v35, s[36:39], s81 offen offset:3072 lds\n" \
"v_mfma_f32_16x16x32_bf16 a[48:51], v[72:75], v[160:163], a[48:51]\n" \
"ds_read_b64_tr_b16 v[138:139], v60 offset:36864\n" \
"v_mfma_f32_16x16x32_bf16 a[48:51], v[76:79], v[164:167], a[48:51]\n" \
"s_xor_b32 s81, s81, 0x380\n" \
"v_mfma_f32_16x16x32_bf16 a[80:83], v[80:83], v[160:163], a[80:83]\n" \
"s_add_u32 m0, s74, 135168\n" \
"ds_read_b128 v[72:75], v40 offset:34816\n" \
"v_mfma_f32_16x16x32_bf16 a[80:83], v[84:87], v[164:167], a[80:83]\n" \
"buffer_load_dwordx4 v32, s[36:39], s82 offen offset:0 lds\n" \
"v_mfma_f32_16x16x32_bf16 a[84:87], v[80:83], v[168:171], a[84:87]\n" \
"ds_read_b64_tr_b16 v[140:141], v60 offset:49152\n" \
"v_mfma_f32_16x16x32_bf16 a[84:87], v[84:87], v[172:175], a[84:87]\n" \
"v_mfma_f32_16x16x32_bf16 a[88:91], v[80:83], v[176:179], a[88:91]\n" \
"ds_read_b128 v[76:79], v41 offset:34816\n" \
"v_mfma_f32_16x16x32_bf16 a[88:91], v[84:87], v[180:183], a[88:91]\n" \
"buffer_load_dwordx4 v33, s[36:39], s82 offen offset:1024 lds\n" \
"v_mfma_f32_16x16x32_bf16 a[92:95], v[80:83], v[184:187], a[92:95]\n" \
"ds_read_b64_tr_b16 v[142:143], v60 offset:53248\n" \
"v_mfma_f32_16x16x32_bf16 a[92:95], v[84:87], v[188:191], a[92:95]\n" \
"v_mfma_f32_16x16x32_bf16 a[124:127], v[88:91], v[184:187], a[124:127]\n" \
"ds_read_b128 v[80:83], v40 offset:36864\n" \
"v_mfma_f32_16x16x32_bf16 a[124:127], v[92:95], v[188:191], a[124:127]\n" \
"buffer_load_dwordx4 v34, s[36:39], s82 offen offset:2048 lds\n" \
"v_mfma_f32_16x16x32_bf16 a[120:123], v[88:91], v[176:179], a[120:123]\n" \
"ds_read_b64_tr_b16 v[144:145], v59 offset:33280\n" \
"v_mfma_f32_16x16x32_bf16 a[120:123], v[92:95], v[180:183], a[120:123]\n" \
"v_mfma_f32_16x16x32_bf16 a[116:119], v[88:91], v[168:171], a[116:119]\n" \
"ds_read_b128 v[84:87], v41 offset:36864\n" \
"v_mfma_f32_16x16x32_bf16 a[116:119], v[92:95], v[172:175], a[116:119]\n" \
"buffer_load_dwordx4 v35, s[36:39], s82 offen offset:3072 lds\n" \
"v_mfma_f32_16x16x32_bf16 a[112:115], v[88:91], v[160:163], a[112:115]\n" \
"s_mov_b64 s[64:65], s[14:15]\n" \
"s_add_u32 s68, s67, 0x200000\n" \
"ds_read_b64_tr_b16 v[146:147], v59 offset:37376\n" \
"v_mfma_f32_16x16x32_bf16 a[112:115], v[92:95], v[164:167], a[112:115]\n" \
"s_add_u32 s64, s64, s68\n" \
"s_addc_u32 s65, s65, 0\n" \
"v_mfma_f32_16x16x32_bf16 a[144:147], v[96:99], v[160:163], a[144:147]\n" \
"s_xor_b32 s82, s82, 0x380\n" \
"ds_read_b128 v[88:91], v40 offset:38912\n" \
"v_mfma_f32_16x16x32_bf16 a[144:147], v[100:103], v[164:167], a[144:147]\n" \
"global_load_dword v238, v234, s[64:65]\n" \
"v_mfma_f32_16x16x32_bf16 a[148:151], v[96:99], v[168:171], a[148:151]\n" \
"s_mov_b64 s[64:65], s[16:17]\n" \
"s_add_u32 s68, s67, 0x200000\n" \
"ds_read_b64_tr_b16 v[148:149], v59 offset:49664\n" \
"v_mfma_f32_16x16x32_bf16 a[148:151], v[100:103], v[172:175], a[148:151]\n" \
"s_add_u32 s64, s64, s68\n" \
"s_addc_u32 s65, s65, 0\n" \
"v_mfma_f32_16x16x32_bf16 a[152:155], v[96:99], v[176:179], a[152:155]\n" \
"ds_read_b128 v[92:95], v41 offset:38912\n" \
"v_mfma_f32_16x16x32_bf16 a[152:155], v[100:103], v[180:183], a[152:155]\n" \
"global_load_dword v238, v234, s[64:65]\n" \
"v_mfma_f32_16x16x32_bf16 a[156:159], v[96:99], v[184:187], a[156:159]\n" \
"ds_read_b64_tr_b16 v[150:151], v59 offset:53760\n" \
"v_mfma_f32_16x16x32_bf16 a[156:159], v[100:103], v[188:191], a[156:159]\n" \
"v_mfma_f32_16x16x32_bf16 a[188:191], v[104:107], v[184:187], a[188:191]\n" \
"ds_read_b128 v[96:99], v40 offset:40960\n" \
"v_mfma_f32_16x16x32_bf16 a[188:191], v[108:111], v[188:191], a[188:191]\n" \
"v_mfma_f32_16x16x32_bf16 a[184:187], v[104:107], v[176:179], a[184:187]\n" \
"ds_read_b64_tr_b16 v[152:153], v60 offset:33280\n" \
"v_mfma_f32_16x16x32_bf16 a[184:187], v[108:111], v[180:183], a[184:187]\n" \
"v_mfma_f32_16x16x32_bf16 a[180:183], v[104:107], v[168:171], a[180:183]\n" \
"ds_read_b128 v[100:103], v41 offset:40960\n" \
"v_mfma_f32_16x16x32_bf16 a[180:183], v[108:111], v[172:175], a[180:183]\n" \
"v_mfma_f32_16x16x32_bf16 a[176:179], v[104:107], v[160:163], a[176:179]\n" \
"ds_read_b64_tr_b16 v[154:155], v60 offset:37376\n" \
"v_mfma_f32_16x16x32_bf16 a[176:179], v[108:111], v[164:167], a[176:179]\n" \
"v_mfma_f32_16x16x32_bf16 a[208:211], v[112:115], v[160:163], a[208:211]\n" \
"ds_read_b128 v[104:107], v40 offset:43008\n" \
"v_mfma_f32_16x16x32_bf16 a[208:211], v[116:119], v[164:167], a[208:211]\n" \
"v_mfma_f32_16x16x32_bf16 a[212:215], v[112:115], v[168:171], a[212:215]\n" \
"ds_read_b64_tr_b16 v[156:157], v60 offset:49664\n" \
"v_mfma_f32_16x16x32_bf16 a[212:215], v[116:119], v[172:175], a[212:215]\n" \
"v_mfma_f32_16x16x32_bf16 a[216:219], v[112:115], v[176:179], a[216:219]\n" \
"ds_read_b128 v[108:111], v41 offset:43008\n" \
"v_mfma_f32_16x16x32_bf16 a[216:219], v[116:119], v[180:183], a[216:219]\n" \
"v_mfma_f32_16x16x32_bf16 a[220:223], v[112:115], v[184:187], a[220:223]\n" \
"ds_read_b64_tr_b16 v[158:159], v60 offset:53760\n" \
"v_mfma_f32_16x16x32_bf16 a[220:223], v[116:119], v[188:191], a[220:223]\n" \
"v_mfma_f32_16x16x32_bf16 a[252:255], v[120:123], v[184:187], a[252:255]\n" \
"ds_read_b128 v[112:115], v40 offset:45056\n" \
"v_mfma_f32_16x16x32_bf16 a[252:255], v[124:127], v[188:191], a[252:255]\n" \
"v_mfma_f32_16x16x32_bf16 a[248:251], v[120:123], v[176:179], a[248:251]\n" \
"v_mfma_f32_16x16x32_bf16 a[248:251], v[124:127], v[180:183], a[248:251]\n" \
"v_mfma_f32_16x16x32_bf16 a[244:247], v[120:123], v[168:171], a[244:247]\n" \
"ds_read_b128 v[116:119], v41 offset:45056\n" \
"v_mfma_f32_16x16x32_bf16 a[244:247], v[124:127], v[172:175], a[244:247]\n" \
"v_mfma_f32_16x16x32_bf16 a[240:243], v[120:123], v[160:163], a[240:243]\n" \
"v_mfma_f32_16x16x32_bf16 a[240:243], v[124:127], v[164:167], a[240:243]\n" \
"s_waitcnt lgkmcnt(1)\n" \
"v_mfma_f32_16x16x32_bf16 a[0:3], v[128:131], v[64:67], a[0:3]\n" \
"ds_read_b128 v[120:123], v40 offset:47104\n" \
"v_lshrrev_b32 v0, 4, v234\n" \
"v_mfma_f32_16x16x32_bf16 a[0:3], v[132:135], v[68:71], a[0:3]\n" \
"v_bfe_u32 v1, v0, 4, 1\n" \
"v_bfe_u32 v2, v0, 2, 2\n" \
"v_mfma_f32_16x16x32_bf16 a[4:7], v[136:139], v[64:67], a[4:7]\n" \
"s_add_u32 m0, s74, 65536\n" \
"ds_read_b128 v[124:127], v41 offset:47104\n" \
"v_lshl_add_u32 v2, v1, 3, v2\n" \
"v_mfma_f32_16x16x32_bf16 a[4:7], v[140:143], v[68:71], a[4:7]\n" \
"buffer_load_dwordx4 v36, s[40:43], s83 offen offset:0 lds\n" \
"v_mfma_f32_16x16x32_bf16 a[8:11], v[144:147], v[64:67], a[8:11]\n" \
"s_lshl_b32 s71, s61, 4\n" \
"ds_read_b64_tr_b16 v[160:161], v59 offset:33792\n" \
"v_add_u32 v2, s71, v2\n" \
"v_mfma_f32_16x16x32_bf16 a[8:11], v[148:151], v[68:71], a[8:11]\n" \
"v_mul_u32_u24 v2, 6144, v2\n" \
"v_lshlrev_b32 v1, 1, v1\n" \
"v_mfma_f32_16x16x32_bf16 a[12:15], v[152:155], v[64:67], a[12:15]\n" \
"ds_read_b64_tr_b16 v[162:163], v59 offset:37888\n" \
"v_and_b32 v3, 3, v0\n" \
"v_mfma_f32_16x16x32_bf16 a[12:15], v[156:159], v[68:71], a[12:15]\n" \
"buffer_load_dwordx4 v37, s[44:47], s83 offen offset:1024 lds\n" \
"v_mfma_f32_16x16x32_bf16 a[44:47], v[152:155], v[72:75], a[44:47]\n" \
"ds_read_b64_tr_b16 v[164:165], v59 offset:50176\n" \
"v_xor_b32 v1, v1, v3\n" \
"v_mfma_f32_16x16x32_bf16 a[44:47], v[156:159], v[76:79], a[44:47]\n" \
"v_lshl_add_u32 v2, v1, 4, v2\n" \
"v_lshrrev_b32 v1, 5, v0\n" \
"v_mfma_f32_16x16x32_bf16 a[40:43], v[144:147], v[72:75], a[40:43]\n" \
"ds_read_b64_tr_b16 v[166:167], v59 offset:54272\n" \
"v_lshl_add_u32 v244, v1, 6, v2\n" \
"v_mfma_f32_16x16x32_bf16 a[40:43], v[148:151], v[76:79], a[40:43]\n" \
"buffer_load_dwordx4 v38, s[40:43], s83 offen offset:2048 lds\n" \
"v_mfma_f32_16x16x32_bf16 a[36:39], v[136:139], v[72:75], a[36:39]\n" \
"ds_read_b64_tr_b16 v[168:169], v60 offset:33792\n" \
"v_lshrrev_b32 v3, 3, v0\n" \
"v_mfma_f32_16x16x32_bf16 a[36:39], v[140:143], v[76:79], a[36:39]\n" \
"v_and_b32 v4, 7, v0\n" \
"v_xor_b32 v5, v4, v3\n" \
"v_mfma_f32_16x16x32_bf16 a[32:35], v[128:131], v[72:75], a[32:35]\n" \
"ds_read_b64_tr_b16 v[170:171], v60 offset:37888\n" \
"v_lshlrev_b32 v5, 4, v5\n" \
"v_mfma_f32_16x16x32_bf16 a[32:35], v[132:135], v[76:79], a[32:35]\n" \
"buffer_load_dwordx4 v39, s[44:47], s83 offen offset:3072 lds\n" \
"v_mfma_f32_16x16x32_bf16 a[64:67], v[128:131], v[80:83], a[64:67]\n" \
"ds_read_b64_tr_b16 v[172:173], v60 offset:50176\n" \
"v_lshl_add_u32 v245, v3, 12, v5\n" \
"v_mfma_f32_16x16x32_bf16 a[64:67], v[132:135], v[84:87], a[64:67]\n" \
"s_xor_b32 s83, s83, 0x80000\n" \
"v_mfma_f32_16x16x32_bf16 a[68:71], v[136:139], v[80:83], a[68:71]\n" \
"s_add_u32 m0, s74, 69632\n" \
"ds_read_b64_tr_b16 v[174:175], v60 offset:54272\n" \
"v_mfma_f32_16x16x32_bf16 a[68:71], v[140:143], v[84:87], a[68:71]\n" \
"buffer_load_dwordx4 v36, s[40:43], s84 offen offset:0 lds\n" \
"v_mfma_f32_16x16x32_bf16 a[72:75], v[144:147], v[80:83], a[72:75]\n" \
"s_and_b32 s70, s61, 1\n" \
"ds_read_b64_tr_b16 v[176:177], v59 offset:34304\n" \
"v_mfma_f32_16x16x32_bf16 a[72:75], v[148:151], v[84:87], a[72:75]\n" \
"s_lshl_b32 s71, s70, 2\n" \
"v_add_u32 v3, s71, v3\n" \
"v_xor_b32 v5, v4, v3\n" \
"v_mfma_f32_16x16x32_bf16 a[76:79], v[152:155], v[80:83], a[76:79]\n" \
"ds_read_b64_tr_b16 v[178:179], v59 offset:38400\n" \
"v_lshlrev_b32 v5, 4, v5\n" \
"v_mfma_f32_16x16x32_bf16 a[76:79], v[156:159], v[84:87], a[76:79]\n" \
"buffer_load_dwordx4 v37, s[44:47], s84 offen offset:1024 lds\n" \
"v_mfma_f32_16x16x32_bf16 a[108:111], v[152:155], v[88:91], a[108:111]\n" \
"ds_read_b64_tr_b16 v[180:181], v59 offset:50688\n" \
"v_lshl_add_u32 v246, v3, 12, v5\n" \
"v_mfma_f32_16x16x32_bf16 a[108:111], v[156:159], v[92:95], a[108:111]\n" \
"s_lshr_b32 s71, s61, 1\n" \
"v_mfma_f32_16x16x32_bf16 a[104:107], v[144:147], v[88:91], a[104:107]\n" \
"s_lshl_b32 s70, s71, 11\n" \
"ds_read_b64_tr_b16 v[182:183], v59 offset:54784\n" \
"v_lshl_add_u32 v246, v1, 30, v246\n" \
"v_mfma_f32_16x16x32_bf16 a[104:107], v[148:151], v[92:95], a[104:107]\n" \
"buffer_load_dwordx4 v38, s[40:43], s84 offen offset:2048 lds\n" \
"v_mfma_f32_16x16x32_bf16 a[100:103], v[136:139], v[88:91], a[100:103]\n" \
"ds_read_b64_tr_b16 v[184:185], v60 offset:34304\n" \
"v_add_u32 v246, s70, v246\n" \
"v_mfma_f32_16x16x32_bf16 a[100:103], v[140:143], v[92:95], a[100:103]\n" \
"v_lshrrev_b32 v3, 4, v0\n" \
"v_xor_b32 v3, v3, v4\n" \
"v_mfma_f32_16x16x32_bf16 a[96:99], v[128:131], v[88:91], a[96:99]\n" \
"ds_read_b64_tr_b16 v[186:187], v60 offset:38400\n" \
"v_and_b32 v5, 15, v0\n" \
"v_mfma_f32_16x16x32_bf16 a[96:99], v[132:135], v[92:95], a[96:99]\n" \
"buffer_load_dwordx4 v39, s[44:47], s84 offen offset:3072 lds\n" \
"v_mfma_f32_16x16x32_bf16 a[128:131], v[128:131], v[96:99], a[128:131]\n" \
"ds_read_b64_tr_b16 v[188:189], v60 offset:50688\n" \
"v_lshlrev_b32 v2, 7, v5\n" \
"v_mfma_f32_16x16x32_bf16 a[128:131], v[132:135], v[100:103], a[128:131]\n" \
"v_lshl_add_u32 v2, v3, 4, v2\n" \
"v_mfma_f32_16x16x32_bf16 a[132:135], v[136:139], v[96:99], a[132:135]\n" \
"s_xor_b32 s84, s84, 0x80000\n" \
"ds_read_b64_tr_b16 v[190:191], v60 offset:54784\n" \
"v_mfma_f32_16x16x32_bf16 a[132:135], v[140:143], v[100:103], a[132:135]\n" \
"s_add_u32 s70, s70, %3\n" \
"v_add_u32 v247, s70, v2\n" \
"v_lshrrev_b32 v5, 2, v5\n" \
"v_mfma_f32_16x16x32_bf16 a[136:139], v[144:147], v[96:99], a[136:139]\n" \
"v_lshl_add_u32 v5, v5, 9, v2\n" \
"v_mfma_f32_16x16x32_bf16 a[136:139], v[148:151], v[100:103], a[136:139]\n" \
"s_add_u32 s70, %3, 16384\n" \
"v_add_u32 v248, s70, v5\n" \
"v_and_b32 v2, 3, v0\n" \
"v_mfma_f32_16x16x32_bf16 a[140:143], v[152:155], v[96:99], a[140:143]\n" \
"v_lshlrev_b32 v2, 3, v2\n" \
"v_bfe_u32 v3, v0, 2, 2\n" \
"v_mfma_f32_16x16x32_bf16 a[140:143], v[156:159], v[100:103], a[140:143]\n" \
"v_lshl_add_u32 v2, v3, 6, v2\n" \
"v_bfe_u32 v3, v0, 4, 1\n" \
"v_mfma_f32_16x16x32_bf16 a[172:175], v[152:155], v[104:107], a[172:175]\n" \
"v_mul_u32_u24 v3, 288, v3\n" \
"v_mul_u32_u24 v4, 6144, v1\n" \
"v_mfma_f32_16x16x32_bf16 a[172:175], v[156:159], v[108:111], a[172:175]\n" \
"v_add3_u32 v2, v2, v3, v4\n" \
"v_mfma_f32_16x16x32_bf16 a[168:171], v[144:147], v[104:107], a[168:171]\n" \
"s_and_b32 s70, s61, 1\n" \
"v_mfma_f32_16x16x32_bf16 a[168:171], v[148:151], v[108:111], a[168:171]\n" \
"s_lshl_b32 s70, s70, 9\n" \
"v_mfma_f32_16x16x32_bf16 a[164:167], v[136:139], v[104:107], a[164:167]\n" \
"s_add_u32 s70, s70, %3\n" \
"v_add_u32 v249, s70, v2\n" \
"v_mfma_f32_16x16x32_bf16 a[164:167], v[140:143], v[108:111], a[164:167]\n" \
"s_lshl_b32 s70, s71, 5\n" \
"v_xor_b32 v249, s70, v249\n" \
"v_mfma_f32_16x16x32_bf16 a[160:163], v[128:131], v[104:107], a[160:163]\n" \
"v_mfma_f32_16x16x32_bf16 a[160:163], v[132:135], v[108:111], a[160:163]\n" \
"s_waitcnt lgkmcnt(0)\n" \
"v_mfma_f32_16x16x32_bf16 a[192:195], v[128:131], v[112:115], a[192:195]\n" \
"v_mfma_f32_16x16x32_bf16 a[192:195], v[132:135], v[116:119], a[192:195]\n" \
"v_mfma_f32_16x16x32_bf16 a[196:199], v[136:139], v[112:115], a[196:199]\n" \
"v_mfma_f32_16x16x32_bf16 a[196:199], v[140:143], v[116:119], a[196:199]\n" \
"v_mfma_f32_16x16x32_bf16 a[200:203], v[144:147], v[112:115], a[200:203]\n" \
"v_mfma_f32_16x16x32_bf16 a[200:203], v[148:151], v[116:119], a[200:203]\n" \
"v_mfma_f32_16x16x32_bf16 a[204:207], v[152:155], v[112:115], a[204:207]\n" \
"v_mfma_f32_16x16x32_bf16 a[204:207], v[156:159], v[116:119], a[204:207]\n" \
"v_mfma_f32_16x16x32_bf16 a[236:239], v[152:155], v[120:123], a[236:239]\n" \
"v_mfma_f32_16x16x32_bf16 a[236:239], v[156:159], v[124:127], a[236:239]\n" \
"v_mfma_f32_16x16x32_bf16 a[232:235], v[144:147], v[120:123], a[232:235]\n" \
"v_mfma_f32_16x16x32_bf16 a[232:235], v[148:151], v[124:127], a[232:235]\n" \
"v_mfma_f32_16x16x32_bf16 a[228:231], v[136:139], v[120:123], a[228:231]\n" \
"v_mfma_f32_16x16x32_bf16 a[228:231], v[140:143], v[124:127], a[228:231]\n" \
"v_mfma_f32_16x16x32_bf16 a[224:227], v[128:131], v[120:123], a[224:227]\n" \
"v_mfma_f32_16x16x32_bf16 a[224:227], v[132:135], v[124:127], a[224:227]\n" \
"s_waitcnt vmcnt(10) lgkmcnt(0)\n" \
"s_barrier\n" \
"v_mfma_f32_16x16x32_bf16 a[16:19], v[64:67], v[160:163], a[16:19]\n" \
"s_add_u32 m0, s74, 98304\n" \
"ds_read_b64_tr_b16 v[128:129], v42 offset:0\n" \
"v_mfma_f32_16x16x32_bf16 a[16:19], v[68:71], v[164:167], a[16:19]\n" \
"buffer_load_dwordx4 v32, s[36:39], s81 offen offset:0 lds\n" \
"v_mfma_f32_16x16x32_bf16 a[20:23], v[64:67], v[168:171], a[20:23]\n" \
"ds_read_b64_tr_b16 v[130:131], v42 offset:4096\n" \
"v_mfma_f32_16x16x32_bf16 a[20:23], v[68:71], v[172:175], a[20:23]\n" \
"v_mfma_f32_16x16x32_bf16 a[24:27], v[64:67], v[176:179], a[24:27]\n" \
"ds_read_b64_tr_b16 v[132:133], v42 offset:16384\n" \
"v_mfma_f32_16x16x32_bf16 a[24:27], v[68:71], v[180:183], a[24:27]\n" \
"buffer_load_dwordx4 v33, s[36:39], s81 offen offset:1024 lds\n" \
"v_mfma_f32_16x16x32_bf16 a[28:31], v[64:67], v[184:187], a[28:31]\n" \
"ds_read_b64_tr_b16 v[134:135], v42 offset:20480\n" \
"v_mfma_f32_16x16x32_bf16 a[28:31], v[68:71], v[188:191], a[28:31]\n" \
"v_mfma_f32_16x16x32_bf16 a[60:63], v[72:75], v[184:187], a[60:63]\n" \
"ds_read_b128 v[64:67], v61 offset:0\n" \
"v_mfma_f32_16x16x32_bf16 a[60:63], v[76:79], v[188:191], a[60:63]\n" \
"buffer_load_dwordx4 v34, s[36:39], s81 offen offset:2048 lds\n" \
"v_mfma_f32_16x16x32_bf16 a[56:59], v[72:75], v[176:179], a[56:59]\n" \
"ds_read_b64_tr_b16 v[136:137], v43 offset:0\n" \
"v_mfma_f32_16x16x32_bf16 a[56:59], v[76:79], v[180:183], a[56:59]\n" \
"v_mfma_f32_16x16x32_bf16 a[52:55], v[72:75], v[168:171], a[52:55]\n" \
"ds_read_b128 v[68:71], v62 offset:0\n" \
"v_mfma_f32_16x16x32_bf16 a[52:55], v[76:79], v[172:175], a[52:55]\n" \
"buffer_load_dwordx4 v35, s[36:39], s81 offen offset:3072 lds\n" \
"v_mfma_f32_16x16x32_bf16 a[48:51], v[72:75], v[160:163], a[48:51]\n" \
"ds_read_b64_tr_b16 v[138:139], v43 offset:4096\n" \
"v_mfma_f32_16x16x32_bf16 a[48:51], v[76:79], v[164:167], a[48:51]\n" \
"s_xor_b32 s81, s81, 0x80\n" \
"v_mfma_f32_16x16x32_bf16 a[80:83], v[80:83], v[160:163], a[80:83]\n" \
"s_add_u32 m0, s74, 102400\n" \
"ds_read_b128 v[72:75], v61 offset:2048\n" \
"v_mfma_f32_16x16x32_bf16 a[80:83], v[84:87], v[164:167], a[80:83]\n" \
"buffer_load_dwordx4 v32, s[36:39], s82 offen offset:0 lds\n" \
"v_mfma_f32_16x16x32_bf16 a[84:87], v[80:83], v[168:171], a[84:87]\n" \
"ds_read_b64_tr_b16 v[140:141], v43 offset:16384\n" \
"v_mfma_f32_16x16x32_bf16 a[84:87], v[84:87], v[172:175], a[84:87]\n" \
"v_mfma_f32_16x16x32_bf16 a[88:91], v[80:83], v[176:179], a[88:91]\n" \
"ds_read_b128 v[76:79], v62 offset:2048\n" \
"v_mfma_f32_16x16x32_bf16 a[88:91], v[84:87], v[180:183], a[88:91]\n" \
"buffer_load_dwordx4 v33, s[36:39], s82 offen offset:1024 lds\n" \
"v_mfma_f32_16x16x32_bf16 a[92:95], v[80:83], v[184:187], a[92:95]\n" \
"ds_read_b64_tr_b16 v[142:143], v43 offset:20480\n" \
"v_mfma_f32_16x16x32_bf16 a[92:95], v[84:87], v[188:191], a[92:95]\n" \
"v_mfma_f32_16x16x32_bf16 a[124:127], v[88:91], v[184:187], a[124:127]\n" \
"ds_read_b128 v[80:83], v61 offset:4096\n" \
"v_mfma_f32_16x16x32_bf16 a[124:127], v[92:95], v[188:191], a[124:127]\n" \
"buffer_load_dwordx4 v34, s[36:39], s82 offen offset:2048 lds\n" \
"v_mfma_f32_16x16x32_bf16 a[120:123], v[88:91], v[176:179], a[120:123]\n" \
"ds_read_b64_tr_b16 v[144:145], v42 offset:512\n" \
"v_mfma_f32_16x16x32_bf16 a[120:123], v[92:95], v[180:183], a[120:123]\n" \
"v_mfma_f32_16x16x32_bf16 a[116:119], v[88:91], v[168:171], a[116:119]\n" \
"ds_read_b128 v[84:87], v62 offset:4096\n" \
"v_mfma_f32_16x16x32_bf16 a[116:119], v[92:95], v[172:175], a[116:119]\n" \
"buffer_load_dwordx4 v35, s[36:39], s82 offen offset:3072 lds\n" \
"v_mfma_f32_16x16x32_bf16 a[112:115], v[88:91], v[160:163], a[112:115]\n" \
"s_mov_b64 s[64:65], s[14:15]\n" \
"s_add_u32 s68, s67, 0x300000\n" \
"ds_read_b64_tr_b16 v[146:147], v42 offset:4608\n" \
"v_mfma_f32_16x16x32_bf16 a[112:115], v[92:95], v[164:167], a[112:115]\n" \
"s_add_u32 s64, s64, s68\n" \
"s_addc_u32 s65, s65, 0\n" \
"v_mfma_f32_16x16x32_bf16 a[144:147], v[96:99], v[160:163], a[144:147]\n" \
"s_xor_b32 s82, s82, 0x80\n" \
"ds_read_b128 v[88:91], v61 offset:6144\n" \
"v_mfma_f32_16x16x32_bf16 a[144:147], v[100:103], v[164:167], a[144:147]\n" \
"global_load_dword v238, v234, s[64:65]\n" \
"v_mfma_f32_16x16x32_bf16 a[148:151], v[96:99], v[168:171], a[148:151]\n" \
"s_mov_b64 s[64:65], s[16:17]\n" \
"s_add_u32 s68, s67, 0x300000\n" \
"ds_read_b64_tr_b16 v[148:149], v42 offset:16896\n" \
"v_mfma_f32_16x16x32_bf16 a[148:151], v[100:103], v[172:175], a[148:151]\n" \
"s_add_u32 s64, s64, s68\n" \
"s_addc_u32 s65, s65, 0\n" \
"v_mfma_f32_16x16x32_bf16 a[152:155], v[96:99], v[176:179], a[152:155]\n" \
"ds_read_b128 v[92:95], v62 offset:6144\n" \
"v_mfma_f32_16x16x32_bf16 a[152:155], v[100:103], v[180:183], a[152:155]\n" \
"global_load_dword v238, v234, s[64:65]\n" \
"v_mfma_f32_16x16x32_bf16 a[156:159], v[96:99], v[184:187], a[156:159]\n" \
"s_mov_b64 s[64:65], s[8:9]\n" \
"s_add_u32 s68, s66, 0x0\n" \
"ds_read_b64_tr_b16 v[150:151], v42 offset:20992\n" \
"v_mfma_f32_16x16x32_bf16 a[156:159], v[100:103], v[188:191], a[156:159]\n" \
"s_add_u32 s64, s64, s68\n" \
"s_addc_u32 s65, s65, 0\n" \
"v_mfma_f32_16x16x32_bf16 a[188:191], v[104:107], v[184:187], a[188:191]\n" \
"ds_read_b128 v[96:99], v61 offset:8192\n" \
"v_mfma_f32_16x16x32_bf16 a[188:191], v[108:111], v[188:191], a[188:191]\n" \
"global_load_dword v238, v234, s[64:65]\n" \
"v_mfma_f32_16x16x32_bf16 a[184:187], v[104:107], v[176:179], a[184:187]\n" \
"s_mov_b64 s[64:65], s[8:9]\n" \
"s_add_u32 s68, s66, 0x100000\n" \
"ds_read_b64_tr_b16 v[152:153], v43 offset:512\n" \
"v_mfma_f32_16x16x32_bf16 a[184:187], v[108:111], v[180:183], a[184:187]\n" \
"s_add_u32 s64, s64, s68\n" \
"s_addc_u32 s65, s65, 0\n" \
"v_mfma_f32_16x16x32_bf16 a[180:183], v[104:107], v[168:171], a[180:183]\n" \
"ds_read_b128 v[100:103], v62 offset:8192\n" \
"v_mfma_f32_16x16x32_bf16 a[180:183], v[108:111], v[172:175], a[180:183]\n" \
"global_load_dword v238, v234, s[64:65]\n" \
"v_mfma_f32_16x16x32_bf16 a[176:179], v[104:107], v[160:163], a[176:179]\n" \
"ds_read_b64_tr_b16 v[154:155], v43 offset:4608\n" \
"v_mfma_f32_16x16x32_bf16 a[176:179], v[108:111], v[164:167], a[176:179]\n" \
"v_mfma_f32_16x16x32_bf16 a[208:211], v[112:115], v[160:163], a[208:211]\n" \
"ds_read_b128 v[104:107], v61 offset:10240\n" \
"v_mfma_f32_16x16x32_bf16 a[208:211], v[116:119], v[164:167], a[208:211]\n" \
"v_mfma_f32_16x16x32_bf16 a[212:215], v[112:115], v[168:171], a[212:215]\n" \
"ds_read_b64_tr_b16 v[156:157], v43 offset:16896\n" \
"v_mfma_f32_16x16x32_bf16 a[212:215], v[116:119], v[172:175], a[212:215]\n" \
"v_mfma_f32_16x16x32_bf16 a[216:219], v[112:115], v[176:179], a[216:219]\n" \
"ds_read_b128 v[108:111], v62 offset:10240\n" \
"v_mfma_f32_16x16x32_bf16 a[216:219], v[116:119], v[180:183], a[216:219]\n" \
"v_mfma_f32_16x16x32_bf16 a[220:223], v[112:115], v[184:187], a[220:223]\n" \
"ds_read_b64_tr_b16 v[158:159], v43 offset:20992\n" \
"v_mfma_f32_16x16x32_bf16 a[220:223], v[116:119], v[188:191], a[220:223]\n" \
"v_mfma_f32_16x16x32_bf16 a[252:255], v[120:123], v[184:187], a[252:255]\n" \
"ds_read_b128 v[112:115], v61 offset:12288\n" \
"v_mfma_f32_16x16x32_bf16 a[252:255], v[124:127], v[188:191], a[252:255]\n" \
"v_mfma_f32_16x16x32_bf16 a[248:251], v[120:123], v[176:179], a[248:251]\n" \
"v_mfma_f32_16x16x32_bf16 a[248:251], v[124:127], v[180:183], a[248:251]\n" \
"v_mfma_f32_16x16x32_bf16 a[244:247], v[120:123], v[168:171], a[244:247]\n" \
"ds_read_b128 v[116:119], v62 offset:12288\n" \
"v_mfma_f32_16x16x32_bf16 a[244:247], v[124:127], v[172:175], a[244:247]\n" \
"v_mfma_f32_16x16x32_bf16 a[240:243], v[120:123], v[160:163], a[240:243]\n" \
"v_mfma_f32_16x16x32_bf16 a[240:243], v[124:127], v[164:167], a[240:243]\n" \
"s_waitcnt lgkmcnt(1)\n" \
"v_mfma_f32_16x16x32_bf16 a[0:3], v[128:131], v[64:67], a[0:3]\n" \
"ds_read_b128 v[120:123], v61 offset:14336\n" \
"v_mfma_f32_16x16x32_bf16 a[0:3], v[132:135], v[68:71], a[0:3]\n" \
"v_mfma_f32_16x16x32_bf16 a[4:7], v[136:139], v[64:67], a[4:7]\n" \
"s_add_u32 m0, s74, 32768\n" \
"ds_read_b128 v[124:127], v62 offset:14336\n" \
"v_mfma_f32_16x16x32_bf16 a[4:7], v[140:143], v[68:71], a[4:7]\n" \
"buffer_load_dwordx4 v36, s[40:43], s83 offen offset:0 lds\n" \
"v_mfma_f32_16x16x32_bf16 a[8:11], v[144:147], v[64:67], a[8:11]\n" \
"ds_read_b64_tr_b16 v[160:161], v42 offset:1024\n" \
"v_mfma_f32_16x16x32_bf16 a[8:11], v[148:151], v[68:71], a[8:11]\n" \
"v_mfma_f32_16x16x32_bf16 a[12:15], v[152:155], v[64:67], a[12:15]\n" \
"ds_read_b64_tr_b16 v[162:163], v42 offset:5120\n" \
"v_mfma_f32_16x16x32_bf16 a[12:15], v[156:159], v[68:71], a[12:15]\n" \
"buffer_load_dwordx4 v37, s[44:47], s83 offen offset:1024 lds\n" \
"v_mfma_f32_16x16x32_bf16 a[44:47], v[152:155], v[72:75], a[44:47]\n" \
"ds_read_b64_tr_b16 v[164:165], v42 offset:17408\n" \
"v_mfma_f32_16x16x32_bf16 a[44:47], v[156:159], v[76:79], a[44:47]\n" \
"v_mfma_f32_16x16x32_bf16 a[40:43], v[144:147], v[72:75], a[40:43]\n" \
"ds_read_b64_tr_b16 v[166:167], v42 offset:21504\n" \
"v_mfma_f32_16x16x32_bf16 a[40:43], v[148:151], v[76:79], a[40:43]\n" \
"buffer_load_dwordx4 v38, s[40:43], s83 offen offset:2048 lds\n" \
"v_mfma_f32_16x16x32_bf16 a[36:39], v[136:139], v[72:75], a[36:39]\n" \
"ds_read_b64_tr_b16 v[168:169], v43 offset:1024\n" \
"v_mfma_f32_16x16x32_bf16 a[36:39], v[140:143], v[76:79], a[36:39]\n" \
"v_mfma_f32_16x16x32_bf16 a[32:35], v[128:131], v[72:75], a[32:35]\n" \
"ds_read_b64_tr_b16 v[170:171], v43 offset:5120\n" \
"v_mfma_f32_16x16x32_bf16 a[32:35], v[132:135], v[76:79], a[32:35]\n" \
"buffer_load_dwordx4 v39, s[44:47], s83 offen offset:3072 lds\n" \
"v_mfma_f32_16x16x32_bf16 a[64:67], v[128:131], v[80:83], a[64:67]\n" \
"ds_read_b64_tr_b16 v[172:173], v43 offset:17408\n" \
"v_mfma_f32_16x16x32_bf16 a[64:67], v[132:135], v[84:87], a[64:67]\n" \
"s_xor_b32 s83, s83, 0x180000\n" \
"v_mfma_f32_16x16x32_bf16 a[68:71], v[136:139], v[80:83], a[68:71]\n" \
"s_add_u32 m0, s74, 36864\n" \
"ds_read_b64_tr_b16 v[174:175], v43 offset:21504\n" \
"v_mfma_f32_16x16x32_bf16 a[68:71], v[140:143], v[84:87], a[68:71]\n" \
"buffer_load_dwordx4 v36, s[40:43], s84 offen offset:0 lds\n" \
"v_mfma_f32_16x16x32_bf16 a[72:75], v[144:147], v[80:83], a[72:75]\n" \
"ds_read_b64_tr_b16 v[176:177], v42 offset:1536\n" \
"v_mfma_f32_16x16x32_bf16 a[72:75], v[148:151], v[84:87], a[72:75]\n" \
"v_mfma_f32_16x16x32_bf16 a[76:79], v[152:155], v[80:83], a[76:79]\n" \
"ds_read_b64_tr_b16 v[178:179], v42 offset:5632\n" \
"v_mfma_f32_16x16x32_bf16 a[76:79], v[156:159], v[84:87], a[76:79]\n" \
"buffer_load_dwordx4 v37, s[44:47], s84 offen offset:1024 lds\n" \
"v_mfma_f32_16x16x32_bf16 a[108:111], v[152:155], v[88:91], a[108:111]\n" \
"ds_read_b64_tr_b16 v[180:181], v42 offset:17920\n" \
"v_mfma_f32_16x16x32_bf16 a[108:111], v[156:159], v[92:95], a[108:111]\n" \
"v_mfma_f32_16x16x32_bf16 a[104:107], v[144:147], v[88:91], a[104:107]\n" \
"ds_read_b64_tr_b16 v[182:183], v42 offset:22016\n" \
"v_mfma_f32_16x16x32_bf16 a[104:107], v[148:151], v[92:95], a[104:107]\n" \
"buffer_load_dwordx4 v38, s[40:43], s84 offen offset:2048 lds\n" \
"v_mfma_f32_16x16x32_bf16 a[100:103], v[136:139], v[88:91], a[100:103]\n" \
"ds_read_b64_tr_b16 v[184:185], v43 offset:1536\n" \
"v_mfma_f32_16x16x32_bf16 a[100:103], v[140:143], v[92:95], a[100:103]\n" \
"v_mfma_f32_16x16x32_bf16 a[96:99], v[128:131], v[88:91], a[96:99]\n" \
"ds_read_b64_tr_b16 v[186:187], v43 offset:5632\n" \
"v_mfma_f32_16x16x32_bf16 a[96:99], v[132:135], v[92:95], a[96:99]\n" \
"buffer_load_dwordx4 v39, s[44:47], s84 offen offset:3072 lds\n" \
"v_mfma_f32_16x16x32_bf16 a[128:131], v[128:131], v[96:99], a[128:131]\n" \
"ds_read_b64_tr_b16 v[188:189], v43 offset:17920\n" \
"v_mfma_f32_16x16x32_bf16 a[128:131], v[132:135], v[100:103], a[128:131]\n" \
"v_mfma_f32_16x16x32_bf16 a[132:135], v[136:139], v[96:99], a[132:135]\n" \
"s_xor_b32 s84, s84, 0x180000\n" \
"ds_read_b64_tr_b16 v[190:191], v43 offset:22016\n" \
"v_mfma_f32_16x16x32_bf16 a[132:135], v[140:143], v[100:103], a[132:135]\n" \
"v_mfma_f32_16x16x32_bf16 a[136:139], v[144:147], v[96:99], a[136:139]\n" \
"v_mfma_f32_16x16x32_bf16 a[136:139], v[148:151], v[100:103], a[136:139]\n" \
"v_mfma_f32_16x16x32_bf16 a[140:143], v[152:155], v[96:99], a[140:143]\n" \
"v_mfma_f32_16x16x32_bf16 a[140:143], v[156:159], v[100:103], a[140:143]\n" \
"v_mfma_f32_16x16x32_bf16 a[172:175], v[152:155], v[104:107], a[172:175]\n" \
"v_mfma_f32_16x16x32_bf16 a[172:175], v[156:159], v[108:111], a[172:175]\n" \
"v_mfma_f32_16x16x32_bf16 a[168:171], v[144:147], v[104:107], a[168:171]\n" \
"v_mfma_f32_16x16x32_bf16 a[168:171], v[148:151], v[108:111], a[168:171]\n" \
"v_mfma_f32_16x16x32_bf16 a[164:167], v[136:139], v[104:107], a[164:167]\n" \
"v_mfma_f32_16x16x32_bf16 a[164:167], v[140:143], v[108:111], a[164:167]\n" \
"v_mfma_f32_16x16x32_bf16 a[160:163], v[128:131], v[104:107], a[160:163]\n" \
"v_mfma_f32_16x16x32_bf16 a[160:163], v[132:135], v[108:111], a[160:163]\n" \
"s_waitcnt lgkmcnt(0)\n" \
"v_mfma_f32_16x16x32_bf16 a[192:195], v[128:131], v[112:115], a[192:195]\n" \
"v_mfma_f32_16x16x32_bf16 a[192:195], v[132:135], v[116:119], a[192:195]\n" \
"v_mfma_f32_16x16x32_bf16 a[196:199], v[136:139], v[112:115], a[196:199]\n" \
"v_mfma_f32_16x16x32_bf16 a[196:199], v[140:143], v[116:119], a[196:199]\n" \
"v_mfma_f32_16x16x32_bf16 a[200:203], v[144:147], v[112:115], a[200:203]\n" \
"v_mfma_f32_16x16x32_bf16 a[200:203], v[148:151], v[116:119], a[200:203]\n" \
"v_mfma_f32_16x16x32_bf16 a[204:207], v[152:155], v[112:115], a[204:207]\n" \
"v_mfma_f32_16x16x32_bf16 a[204:207], v[156:159], v[116:119], a[204:207]\n" \
"v_mfma_f32_16x16x32_bf16 a[236:239], v[152:155], v[120:123], a[236:239]\n" \
"v_mfma_f32_16x16x32_bf16 a[236:239], v[156:159], v[124:127], a[236:239]\n" \
"v_mfma_f32_16x16x32_bf16 a[232:235], v[144:147], v[120:123], a[232:235]\n" \
"v_mfma_f32_16x16x32_bf16 a[232:235], v[148:151], v[124:127], a[232:235]\n" \
"v_mfma_f32_16x16x32_bf16 a[228:231], v[136:139], v[120:123], a[228:231]\n" \
"v_mfma_f32_16x16x32_bf16 a[228:231], v[140:143], v[124:127], a[228:231]\n" \
"v_mfma_f32_16x16x32_bf16 a[224:227], v[128:131], v[120:123], a[224:227]\n" \
"v_mfma_f32_16x16x32_bf16 a[224:227], v[132:135], v[124:127], a[224:227]\n" \
"s_waitcnt vmcnt(12) lgkmcnt(0)\n" \
"s_barrier\n" \
"v_mfma_f32_16x16x32_bf16 a[16:19], v[64:67], v[160:163], a[16:19]\n" \
"s_add_u32 m0, s74, 0\n" \
"ds_read_b64_tr_b16 v[128:129], v59 offset:0\n" \
"v_mfma_f32_16x16x32_bf16 a[16:19], v[68:71], v[164:167], a[16:19]\n" \
"buffer_load_dwordx4 v32, s[36:39], s81 offen offset:0 lds\n" \
"v_mfma_f32_16x16x32_bf16 a[20:23], v[64:67], v[168:171], a[20:23]\n" \
"ds_read_b64_tr_b16 v[130:131], v59 offset:4096\n" \
"v_mfma_f32_16x16x32_bf16 a[20:23], v[68:71], v[172:175], a[20:23]\n" \
"v_mfma_f32_16x16x32_bf16 a[24:27], v[64:67], v[176:179], a[24:27]\n" \
"ds_read_b64_tr_b16 v[132:133], v59 offset:16384\n" \
"v_mfma_f32_16x16x32_bf16 a[24:27], v[68:71], v[180:183], a[24:27]\n" \
"buffer_load_dwordx4 v33, s[36:39], s81 offen offset:1024 lds\n" \
"v_mfma_f32_16x16x32_bf16 a[28:31], v[64:67], v[184:187], a[28:31]\n" \
"ds_read_b64_tr_b16 v[134:135], v59 offset:20480\n" \
"v_mfma_f32_16x16x32_bf16 a[28:31], v[68:71], v[188:191], a[28:31]\n" \
"v_mfma_f32_16x16x32_bf16 a[60:63], v[72:75], v[184:187], a[60:63]\n" \
"ds_read_b128 v[64:67], v44 offset:32768\n" \
"v_mfma_f32_16x16x32_bf16 a[60:63], v[76:79], v[188:191], a[60:63]\n" \
"buffer_load_dwordx4 v34, s[36:39], s81 offen offset:2048 lds\n" \
"v_mfma_f32_16x16x32_bf16 a[56:59], v[72:75], v[176:179], a[56:59]\n" \
"ds_read_b64_tr_b16 v[136:137], v60 offset:0\n" \
"v_mfma_f32_16x16x32_bf16 a[56:59], v[76:79], v[180:183], a[56:59]\n" \
"v_mfma_f32_16x16x32_bf16 a[52:55], v[72:75], v[168:171], a[52:55]\n" \
"ds_read_b128 v[68:71], v45 offset:32768\n" \
"v_mfma_f32_16x16x32_bf16 a[52:55], v[76:79], v[172:175], a[52:55]\n" \
"buffer_load_dwordx4 v35, s[36:39], s81 offen offset:3072 lds\n" \
"v_mfma_f32_16x16x32_bf16 a[48:51], v[72:75], v[160:163], a[48:51]\n" \
"ds_read_b64_tr_b16 v[138:139], v60 offset:4096\n" \
"v_mfma_f32_16x16x32_bf16 a[48:51], v[76:79], v[164:167], a[48:51]\n" \
"Lpg1130_%=:\n" \
"Lst0x1_%=:\n" \
"s_cbranch_execz Lpg3303_%=\n" \
"s_xor_b32 s81, s81, 0x180\n" \
"v_mfma_f32_16x16x32_bf16 a[80:83], v[80:83], v[160:163], a[80:83]\n" \
"s_add_u32 m0, s74, 4096\n" \
"ds_read_b128 v[72:75], v44 offset:34816\n" \
"v_mfma_f32_16x16x32_bf16 a[80:83], v[84:87], v[164:167], a[80:83]\n" \
"buffer_load_dwordx4 v32, s[36:39], s82 offen offset:0 lds\n" \
"v_mfma_f32_16x16x32_bf16 a[84:87], v[80:83], v[168:171], a[84:87]\n" \
"ds_read_b64_tr_b16 v[140:141], v60 offset:16384\n" \
"v_mfma_f32_16x16x32_bf16 a[84:87], v[84:87], v[172:175], a[84:87]\n" \
"v_mfma_f32_16x16x32_bf16 a[88:91], v[80:83], v[176:179], a[88:91]\n" \
"ds_read_b128 v[76:79], v45 offset:34816\n" \
"v_mfma_f32_16x16x32_bf16 a[88:91], v[84:87], v[180:183], a[88:91]\n" \
"buffer_load_dwordx4 v33, s[36:39], s82 offen offset:1024 lds\n" \
"v_mfma_f32_16x16x32_bf16 a[92:95], v[80:83], v[184:187], a[92:95]\n" \
"ds_read_b64_tr_b16 v[142:143], v60 offset:20480\n" \
"v_mfma_f32_16x16x32_bf16 a[92:95], v[84:87], v[188:191], a[92:95]\n" \
"v_mfma_f32_16x16x32_bf16 a[124:127], v[88:91], v[184:187], a[124:127]\n" \
"ds_read_b128 v[80:83], v44 offset:36864\n" \
"v_mfma_f32_16x16x32_bf16 a[124:127], v[92:95], v[188:191], a[124:127]\n" \
"buffer_load_dwordx4 v34, s[36:39], s82 offen offset:2048 lds\n" \
"v_mfma_f32_16x16x32_bf16 a[120:123], v[88:91], v[176:179], a[120:123]\n" \
"ds_read_b64_tr_b16 v[144:145], v59 offset:512\n" \
"v_mfma_f32_16x16x32_bf16 a[120:123], v[92:95], v[180:183], a[120:123]\n" \
"v_mfma_f32_16x16x32_bf16 a[116:119], v[88:91], v[168:171], a[116:119]\n" \
"ds_read_b128 v[84:87], v45 offset:36864\n" \
"v_mfma_f32_16x16x32_bf16 a[116:119], v[92:95], v[172:175], a[116:119]\n" \
"buffer_load_dwordx4 v35, s[36:39], s82 offen offset:3072 lds\n" \
"v_mfma_f32_16x16x32_bf16 a[112:115], v[88:91], v[160:163], a[112:115]\n" \
"s_mov_b64 s[64:65], s[8:9]\n" \
"s_add_u32 s68, s66, 0x200000\n" \
"ds_read_b64_tr_b16 v[146:147], v59 offset:4608\n" \
"v_mfma_f32_16x16x32_bf16 a[112:115], v[92:95], v[164:167], a[112:115]\n" \
"s_add_u32 s64, s64, s68\n" \
"s_addc_u32 s65, s65, 0\n" \
"v_mfma_f32_16x16x32_bf16 a[144:147], v[96:99], v[160:163], a[144:147]\n" \
"s_xor_b32 s82, s82, 0x180\n" \
"ds_read_b128 v[88:91], v44 offset:38912\n" \
"v_mfma_f32_16x16x32_bf16 a[144:147], v[100:103], v[164:167], a[144:147]\n" \
"global_load_dword v238, v234, s[64:65]\n" \
"v_mfma_f32_16x16x32_bf16 a[148:151], v[96:99], v[168:171], a[148:151]\n" \
"s_mov_b64 s[64:65], s[8:9]\n" \
"s_add_u32 s68, s66, 0x300000\n" \
"ds_read_b64_tr_b16 v[148:149], v59 offset:16896\n" \
"v_mfma_f32_16x16x32_bf16 a[148:151], v[100:103], v[172:175], a[148:151]\n" \
"s_add_u32 s64, s64, s68\n" \
"s_addc_u32 s65, s65, 0\n" \
"v_mfma_f32_16x16x32_bf16 a[152:155], v[96:99], v[176:179], a[152:155]\n" \
"ds_read_b128 v[92:95], v45 offset:38912\n" \
"v_mfma_f32_16x16x32_bf16 a[152:155], v[100:103], v[180:183], a[152:155]\n" \
"global_load_dword v238, v234, s[64:65]\n" \
"v_mfma_f32_16x16x32_bf16 a[156:159], v[96:99], v[184:187], a[156:159]\n" \
"s_mov_b64 s[64:65], s[12:13]\n" \
"s_add_u32 s68, s67, 0x0\n" \
"ds_read_b64_tr_b16 v[150:151], v59 offset:20992\n" \
"v_mfma_f32_16x16x32_bf16 a[156:159], v[100:103], v[188:191], a[156:159]\n" \
"s_add_u32 s64, s64, s68\n" \
"s_addc_u32 s65, s65, 0\n" \
"v_mfma_f32_16x16x32_bf16 a[188:191], v[104:107], v[184:187], a[188:191]\n" \
"ds_read_b128 v[96:99], v44 offset:40960\n" \
"v_mfma_f32_16x16x32_bf16 a[188:191], v[108:111], v[188:191], a[188:191]\n" \
"global_load_dword v238, v234, s[64:65]\n" \
"v_mfma_f32_16x16x32_bf16 a[184:187], v[104:107], v[176:179], a[184:187]\n" \
"ds_read_b64_tr_b16 v[152:153], v60 offset:512\n" \
"v_mfma_f32_16x16x32_bf16 a[184:187], v[108:111], v[180:183], a[184:187]\n" \
"v_mfma_f32_16x16x32_bf16 a[180:183], v[104:107], v[168:171], a[180:183]\n" \
"ds_read_b128 v[100:103], v45 offset:40960\n" \
"v_mfma_f32_16x16x32_bf16 a[180:183], v[108:111], v[172:175], a[180:183]\n" \
"v_mfma_f32_16x16x32_bf16 a[176:179], v[104:107], v[160:163], a[176:179]\n" \
"ds_read_b64_tr_b16 v[154:155], v60 offset:4608\n" \
"v_mfma_f32_16x16x32_bf16 a[176:179], v[108:111], v[164:167], a[176:179]\n" \
"v_mfma_f32_16x16x32_bf16 a[208:211], v[112:115], v[160:163], a[208:211]\n" \
"ds_read_b128 v[104:107], v44 offset:43008\n" \
"v_mfma_f32_16x16x32_bf16 a[208:211], v[116:119], v[164:167], a[208:211]\n" \
"v_mfma_f32_16x16x32_bf16 a[212:215], v[112:115], v[168:171], a[212:215]\n" \
"ds_read_b64_tr_b16 v[156:157], v60 offset:16896\n" \
"v_mfma_f32_16x16x32_bf16 a[212:215], v[116:119], v[172:175], a[212:215]\n" \
"v_mfma_f32_16x16x32_bf16 a[216:219], v[112:115], v[176:179], a[216:219]\n" \
"ds_read_b128 v[108:111], v45 offset:43008\n" \
"v_mfma_f32_16x16x32_bf16 a[216:219], v[116:119], v[180:183], a[216:219]\n" \
"v_mfma_f32_16x16x32_bf16 a[220:223], v[112:115], v[184:187], a[220:223]\n" \
"ds_read_b64_tr_b16 v[158:159], v60 offset:20992\n" \
"v_mfma_f32_16x16x32_bf16 a[220:223], v[116:119], v[188:191], a[220:223]\n" \
"v_mfma_f32_16x16x32_bf16 a[252:255], v[120:123], v[184:187], a[252:255]\n" \
"ds_read_b128 v[112:115], v44 offset:45056\n" \
"v_mfma_f32_16x16x32_bf16 a[252:255], v[124:127], v[188:191], a[252:255]\n" \
"v_mfma_f32_16x16x32_bf16 a[248:251], v[120:123], v[176:179], a[248:251]\n" \
"v_mfma_f32_16x16x32_bf16 a[248:251], v[124:127], v[180:183], a[248:251]\n" \
"v_mfma_f32_16x16x32_bf16 a[244:247], v[120:123], v[168:171], a[244:247]\n" \
"ds_read_b128 v[116:119], v45 offset:45056\n" \
"v_mfma_f32_16x16x32_bf16 a[244:247], v[124:127], v[172:175], a[244:247]\n" \
"v_mfma_f32_16x16x32_bf16 a[240:243], v[120:123], v[160:163], a[240:243]\n" \
"v_mfma_f32_16x16x32_bf16 a[240:243], v[124:127], v[164:167], a[240:243]\n" \
"s_waitcnt lgkmcnt(1)\n" \
"v_mfma_f32_16x16x32_bf16 a[0:3], v[128:131], v[64:67], a[0:3]\n" \
"ds_read_b128 v[120:123], v44 offset:47104\n" \
"v_mfma_f32_16x16x32_bf16 a[0:3], v[132:135], v[68:71], a[0:3]\n" \
"v_mfma_f32_16x16x32_bf16 a[4:7], v[136:139], v[64:67], a[4:7]\n" \
"s_add_u32 m0, s74, 131072\n" \
"ds_read_b128 v[124:127], v45 offset:47104\n" \
"v_mfma_f32_16x16x32_bf16 a[4:7], v[140:143], v[68:71], a[4:7]\n" \
"buffer_load_dwordx4 v36, s[40:43], s83 offen offset:0 lds\n" \
"v_mfma_f32_16x16x32_bf16 a[8:11], v[144:147], v[64:67], a[8:11]\n" \
"ds_read_b64_tr_b16 v[160:161], v59 offset:1024\n" \
"v_mfma_f32_16x16x32_bf16 a[8:11], v[148:151], v[68:71], a[8:11]\n" \
"v_mfma_f32_16x16x32_bf16 a[12:15], v[152:155], v[64:67], a[12:15]\n" \
"ds_read_b64_tr_b16 v[162:163], v59 offset:5120\n" \
"v_mfma_f32_16x16x32_bf16 a[12:15], v[156:159], v[68:71], a[12:15]\n" \
"buffer_load_dwordx4 v37, s[44:47], s83 offen offset:1024 lds\n" \
"v_mfma_f32_16x16x32_bf16 a[44:47], v[152:155], v[72:75], a[44:47]\n" \
"ds_read_b64_tr_b16 v[164:165], v59 offset:17408\n" \
"v_mfma_f32_16x16x32_bf16 a[44:47], v[156:159], v[76:79], a[44:47]\n" \
"v_mfma_f32_16x16x32_bf16 a[40:43], v[144:147], v[72:75], a[40:43]\n" \
"ds_read_b64_tr_b16 v[166:167], v59 offset:21504\n" \
"v_mfma_f32_16x16x32_bf16 a[40:43], v[148:151], v[76:79], a[40:43]\n" \
"buffer_load_dwordx4 v38, s[40:43], s83 offen offset:2048 lds\n" \
"v_mfma_f32_16x16x32_bf16 a[36:39], v[136:139], v[72:75], a[36:39]\n" \
"ds_read_b64_tr_b16 v[168:169], v60 offset:1024\n" \
"v_mfma_f32_16x16x32_bf16 a[36:39], v[140:143], v[76:79], a[36:39]\n" \
"v_mfma_f32_16x16x32_bf16 a[32:35], v[128:131], v[72:75], a[32:35]\n" \
"ds_read_b64_tr_b16 v[170:171], v60 offset:5120\n" \
"v_mfma_f32_16x16x32_bf16 a[32:35], v[132:135], v[76:79], a[32:35]\n" \
"buffer_load_dwordx4 v39, s[44:47], s83 offen offset:3072 lds\n" \
"v_mfma_f32_16x16x32_bf16 a[64:67], v[128:131], v[80:83], a[64:67]\n" \
"ds_read_b64_tr_b16 v[172:173], v60 offset:17408\n" \
"v_mfma_f32_16x16x32_bf16 a[64:67], v[132:135], v[84:87], a[64:67]\n" \
"s_xor_b32 s83, s83, 0x80000\n" \
"v_mfma_f32_16x16x32_bf16 a[68:71], v[136:139], v[80:83], a[68:71]\n" \
"s_add_u32 m0, s74, 135168\n" \
"ds_read_b64_tr_b16 v[174:175], v60 offset:21504\n" \
"v_mfma_f32_16x16x32_bf16 a[68:71], v[140:143], v[84:87], a[68:71]\n" \
"buffer_load_dwordx4 v36, s[40:43], s84 offen offset:0 lds\n" \
"v_mfma_f32_16x16x32_bf16 a[72:75], v[144:147], v[80:83], a[72:75]\n" \
"ds_read_b64_tr_b16 v[176:177], v59 offset:1536\n" \
"v_mfma_f32_16x16x32_bf16 a[72:75], v[148:151], v[84:87], a[72:75]\n" \
"v_mfma_f32_16x16x32_bf16 a[76:79], v[152:155], v[80:83], a[76:79]\n" \
"ds_read_b64_tr_b16 v[178:179], v59 offset:5632\n" \
"v_mfma_f32_16x16x32_bf16 a[76:79], v[156:159], v[84:87], a[76:79]\n" \
"buffer_load_dwordx4 v37, s[44:47], s84 offen offset:1024 lds\n" \
"v_mfma_f32_16x16x32_bf16 a[108:111], v[152:155], v[88:91], a[108:111]\n" \
"ds_read_b64_tr_b16 v[180:181], v59 offset:17920\n" \
"v_mfma_f32_16x16x32_bf16 a[108:111], v[156:159], v[92:95], a[108:111]\n" \
"v_mfma_f32_16x16x32_bf16 a[104:107], v[144:147], v[88:91], a[104:107]\n" \
"ds_read_b64_tr_b16 v[182:183], v59 offset:22016\n" \
"v_mfma_f32_16x16x32_bf16 a[104:107], v[148:151], v[92:95], a[104:107]\n" \
"buffer_load_dwordx4 v38, s[40:43], s84 offen offset:2048 lds\n" \
"v_mfma_f32_16x16x32_bf16 a[100:103], v[136:139], v[88:91], a[100:103]\n" \
"ds_read_b64_tr_b16 v[184:185], v60 offset:1536\n" \
"v_mfma_f32_16x16x32_bf16 a[100:103], v[140:143], v[92:95], a[100:103]\n" \
"v_mfma_f32_16x16x32_bf16 a[96:99], v[128:131], v[88:91], a[96:99]\n" \
"ds_read_b64_tr_b16 v[186:187], v60 offset:5632\n" \
"v_mfma_f32_16x16x32_bf16 a[96:99], v[132:135], v[92:95], a[96:99]\n" \
"buffer_load_dwordx4 v39, s[44:47], s84 offen offset:3072 lds\n" \
"v_mfma_f32_16x16x32_bf16 a[128:131], v[128:131], v[96:99], a[128:131]\n" \
"ds_read_b64_tr_b16 v[188:189], v60 offset:17920\n" \
"v_mfma_f32_16x16x32_bf16 a[128:131], v[132:135], v[100:103], a[128:131]\n" \
"v_mfma_f32_16x16x32_bf16 a[132:135], v[136:139], v[96:99], a[132:135]\n" \
"s_xor_b32 s84, s84, 0x80000\n" \
"ds_read_b64_tr_b16 v[190:191], v60 offset:22016\n" \
"v_mfma_f32_16x16x32_bf16 a[132:135], v[140:143], v[100:103], a[132:135]\n" \
"v_mfma_f32_16x16x32_bf16 a[136:139], v[144:147], v[96:99], a[136:139]\n" \
"v_mfma_f32_16x16x32_bf16 a[136:139], v[148:151], v[100:103], a[136:139]\n" \
"v_mfma_f32_16x16x32_bf16 a[140:143], v[152:155], v[96:99], a[140:143]\n" \
"v_mfma_f32_16x16x32_bf16 a[140:143], v[156:159], v[100:103], a[140:143]\n" \
"v_mfma_f32_16x16x32_bf16 a[172:175], v[152:155], v[104:107], a[172:175]\n" \
"v_mfma_f32_16x16x32_bf16 a[172:175], v[156:159], v[108:111], a[172:175]\n" \
"v_mfma_f32_16x16x32_bf16 a[168:171], v[144:147], v[104:107], a[168:171]\n" \
"v_mfma_f32_16x16x32_bf16 a[168:171], v[148:151], v[108:111], a[168:171]\n" \
"v_mfma_f32_16x16x32_bf16 a[164:167], v[136:139], v[104:107], a[164:167]\n" \
"v_mfma_f32_16x16x32_bf16 a[164:167], v[140:143], v[108:111], a[164:167]\n" \
"v_mfma_f32_16x16x32_bf16 a[160:163], v[128:131], v[104:107], a[160:163]\n" \
"v_mfma_f32_16x16x32_bf16 a[160:163], v[132:135], v[108:111], a[160:163]\n" \
"s_waitcnt lgkmcnt(0)\n" \
"v_mfma_f32_16x16x32_bf16 a[192:195], v[128:131], v[112:115], a[192:195]\n" \
"v_mfma_f32_16x16x32_bf16 a[192:195], v[132:135], v[116:119], a[192:195]\n" \
"v_mfma_f32_16x16x32_bf16 a[196:199], v[136:139], v[112:115], a[196:199]\n" \
"v_mfma_f32_16x16x32_bf16 a[196:199], v[140:143], v[116:119], a[196:199]\n" \
"v_mfma_f32_16x16x32_bf16 a[200:203], v[144:147], v[112:115], a[200:203]\n" \
"v_mfma_f32_16x16x32_bf16 a[200:203], v[148:151], v[116:119], a[200:203]\n" \
"v_mfma_f32_16x16x32_bf16 a[204:207], v[152:155], v[112:115], a[204:207]\n" \
"v_mfma_f32_16x16x32_bf16 a[204:207], v[156:159], v[116:119], a[204:207]\n" \
"v_mfma_f32_16x16x32_bf16 a[236:239], v[152:155], v[120:123], a[236:239]\n" \
"v_mfma_f32_16x16x32_bf16 a[236:239], v[156:159], v[124:127], a[236:239]\n" \
"v_mfma_f32_16x16x32_bf16 a[232:235], v[144:147], v[120:123], a[232:235]\n" \
"v_mfma_f32_16x16x32_bf16 a[232:235], v[148:151], v[124:127], a[232:235]\n" \
"v_mfma_f32_16x16x32_bf16 a[228:231], v[136:139], v[120:123], a[228:231]\n" \
"v_mfma_f32_16x16x32_bf16 a[228:231], v[140:143], v[124:127], a[228:231]\n" \
"v_mfma_f32_16x16x32_bf16 a[224:227], v[128:131], v[120:123], a[224:227]\n" \
"v_mfma_f32_16x16x32_bf16 a[224:227], v[132:135], v[124:127], a[224:227]\n" \
"s_waitcnt vmcnt(11) lgkmcnt(0)\n" \
"s_barrier\n" \
"v_mfma_f32_16x16x32_bf16 a[16:19], v[64:67], v[160:163], a[16:19]\n" \
"s_add_u32 m0, s74, 65536\n" \
"ds_read_b64_tr_b16 v[128:129], v42 offset:32768\n" \
"v_mfma_f32_16x16x32_bf16 a[16:19], v[68:71], v[164:167], a[16:19]\n" \
"buffer_load_dwordx4 v32, s[36:39], s81 offen offset:0 lds\n" \
"v_mfma_f32_16x16x32_bf16 a[20:23], v[64:67], v[168:171], a[20:23]\n" \
"ds_read_b64_tr_b16 v[130:131], v42 offset:36864\n" \
"v_mfma_f32_16x16x32_bf16 a[20:23], v[68:71], v[172:175], a[20:23]\n" \
"v_mfma_f32_16x16x32_bf16 a[24:27], v[64:67], v[176:179], a[24:27]\n" \
"ds_read_b64_tr_b16 v[132:133], v42 offset:49152\n" \
"v_mfma_f32_16x16x32_bf16 a[24:27], v[68:71], v[180:183], a[24:27]\n" \
"buffer_load_dwordx4 v33, s[36:39], s81 offen offset:1024 lds\n" \
"v_mfma_f32_16x16x32_bf16 a[28:31], v[64:67], v[184:187], a[28:31]\n" \
"ds_read_b64_tr_b16 v[134:135], v42 offset:53248\n" \
"v_mfma_f32_16x16x32_bf16 a[28:31], v[68:71], v[188:191], a[28:31]\n" \
"v_mfma_f32_16x16x32_bf16 a[60:63], v[72:75], v[184:187], a[60:63]\n" \
"ds_read_b128 v[64:67], v40 offset:0\n" \
"v_mfma_f32_16x16x32_bf16 a[60:63], v[76:79], v[188:191], a[60:63]\n" \
"buffer_load_dwordx4 v34, s[36:39], s81 offen offset:2048 lds\n" \
"v_mfma_f32_16x16x32_bf16 a[56:59], v[72:75], v[176:179], a[56:59]\n" \
"ds_read_b64_tr_b16 v[136:137], v43 offset:32768\n" \
"v_mfma_f32_16x16x32_bf16 a[56:59], v[76:79], v[180:183], a[56:59]\n" \
"v_mfma_f32_16x16x32_bf16 a[52:55], v[72:75], v[168:171], a[52:55]\n" \
"ds_read_b128 v[68:71], v41 offset:0\n" \
"v_mfma_f32_16x16x32_bf16 a[52:55], v[76:79], v[172:175], a[52:55]\n" \
"buffer_load_dwordx4 v35, s[36:39], s81 offen offset:3072 lds\n" \
"v_mfma_f32_16x16x32_bf16 a[48:51], v[72:75], v[160:163], a[48:51]\n" \
"ds_read_b64_tr_b16 v[138:139], v43 offset:36864\n" \
"v_mfma_f32_16x16x32_bf16 a[48:51], v[76:79], v[164:167], a[48:51]\n" \
"s_xor_b32 s81, s81, 0x80\n" \
"v_mfma_f32_16x16x32_bf16 a[80:83], v[80:83], v[160:163], a[80:83]\n" \
"s_add_u32 m0, s74, 69632\n" \
"ds_read_b128 v[72:75], v40 offset:2048\n" \
"v_mfma_f32_16x16x32_bf16 a[80:83], v[84:87], v[164:167], a[80:83]\n" \
"buffer_load_dwordx4 v32, s[36:39], s82 offen offset:0 lds\n" \
"v_mfma_f32_16x16x32_bf16 a[84:87], v[80:83], v[168:171], a[84:87]\n" \
"ds_read_b64_tr_b16 v[140:141], v43 offset:49152\n" \
"v_mfma_f32_16x16x32_bf16 a[84:87], v[84:87], v[172:175], a[84:87]\n" \
"v_mfma_f32_16x16x32_bf16 a[88:91], v[80:83], v[176:179], a[88:91]\n" \
"ds_read_b128 v[76:79], v41 offset:2048\n" \
"v_mfma_f32_16x16x32_bf16 a[88:91], v[84:87], v[180:183], a[88:91]\n" \
"buffer_load_dwordx4 v33, s[36:39], s82 offen offset:1024 lds\n" \
"v_mfma_f32_16x16x32_bf16 a[92:95], v[80:83], v[184:187], a[92:95]\n" \
"ds_read_b64_tr_b16 v[142:143], v43 offset:53248\n" \
"v_mfma_f32_16x16x32_bf16 a[92:95], v[84:87], v[188:191], a[92:95]\n" \
"v_mfma_f32_16x16x32_bf16 a[124:127], v[88:91], v[184:187], a[124:127]\n" \
"ds_read_b128 v[80:83], v40 offset:4096\n" \
"v_mfma_f32_16x16x32_bf16 a[124:127], v[92:95], v[188:191], a[124:127]\n" \
"buffer_load_dwordx4 v34, s[36:39], s82 offen offset:2048 lds\n" \
"v_mfma_f32_16x16x32_bf16 a[120:123], v[88:91], v[176:179], a[120:123]\n" \
"ds_read_b64_tr_b16 v[144:145], v42 offset:33280\n" \
"v_mfma_f32_16x16x32_bf16 a[120:123], v[92:95], v[180:183], a[120:123]\n" \
"v_mfma_f32_16x16x32_bf16 a[116:119], v[88:91], v[168:171], a[116:119]\n" \
"ds_read_b128 v[84:87], v41 offset:4096\n" \
"v_mfma_f32_16x16x32_bf16 a[116:119], v[92:95], v[172:175], a[116:119]\n" \
"buffer_load_dwordx4 v35, s[36:39], s82 offen offset:3072 lds\n" \
"v_mfma_f32_16x16x32_bf16 a[112:115], v[88:91], v[160:163], a[112:115]\n" \
"s_mov_b64 s[64:65], s[12:13]\n" \
"s_add_u32 s68, s67, 0x100000\n" \
"ds_read_b64_tr_b16 v[146:147], v42 offset:37376\n" \
"v_mfma_f32_16x16x32_bf16 a[112:115], v[92:95], v[164:167], a[112:115]\n" \
"s_add_u32 s64, s64, s68\n" \
"s_addc_u32 s65, s65, 0\n" \
"v_mfma_f32_16x16x32_bf16 a[144:147], v[96:99], v[160:163], a[144:147]\n" \
"s_xor_b32 s82, s82, 0x80\n" \
"ds_read_b128 v[88:91], v40 offset:6144\n" \
"v_mfma_f32_16x16x32_bf16 a[144:147], v[100:103], v[164:167], a[144:147]\n" \
"global_load_dword v238, v234, s[64:65]\n" \
"v_mfma_f32_16x16x32_bf16 a[148:151], v[96:99], v[168:171], a[148:151]\n" \
"s_mov_b64 s[64:65], s[12:13]\n" \
"s_add_u32 s68, s67, 0x500000\n" \
"ds_read_b64_tr_b16 v[148:149], v42 offset:49664\n" \
"v_mfma_f32_16x16x32_bf16 a[148:151], v[100:103], v[172:175], a[148:151]\n" \
"s_add_u32 s64, s64, s68\n" \
"s_addc_u32 s65, s65, 0\n" \
"v_mfma_f32_16x16x32_bf16 a[152:155], v[96:99], v[176:179], a[152:155]\n" \
"ds_read_b128 v[92:95], v41 offset:6144\n" \
"v_mfma_f32_16x16x32_bf16 a[152:155], v[100:103], v[180:183], a[152:155]\n" \
"global_load_dword v238, v234, s[64:65]\n" \
"v_mfma_f32_16x16x32_bf16 a[156:159], v[96:99], v[184:187], a[156:159]\n" \
"s_mov_b64 s[64:65], s[12:13]\n" \
"s_add_u32 s68, s67, 0x200000\n" \
"ds_read_b64_tr_b16 v[150:151], v42 offset:53760\n" \
"v_mfma_f32_16x16x32_bf16 a[156:159], v[100:103], v[188:191], a[156:159]\n" \
"s_add_u32 s64, s64, s68\n" \
"s_addc_u32 s65, s65, 0\n" \
"v_mfma_f32_16x16x32_bf16 a[188:191], v[104:107], v[184:187], a[188:191]\n" \
"ds_read_b128 v[96:99], v40 offset:8192\n" \
"v_mfma_f32_16x16x32_bf16 a[188:191], v[108:111], v[188:191], a[188:191]\n" \
"global_load_dword v238, v234, s[64:65]\n" \
"v_mfma_f32_16x16x32_bf16 a[184:187], v[104:107], v[176:179], a[184:187]\n" \
"ds_read_b64_tr_b16 v[152:153], v43 offset:33280\n" \
"v_mfma_f32_16x16x32_bf16 a[184:187], v[108:111], v[180:183], a[184:187]\n" \
"v_mfma_f32_16x16x32_bf16 a[180:183], v[104:107], v[168:171], a[180:183]\n" \
"ds_read_b128 v[100:103], v41 offset:8192\n" \
"v_mfma_f32_16x16x32_bf16 a[180:183], v[108:111], v[172:175], a[180:183]\n" \
"v_mfma_f32_16x16x32_bf16 a[176:179], v[104:107], v[160:163], a[176:179]\n" \
"ds_read_b64_tr_b16 v[154:155], v43 offset:37376\n" \
"v_mfma_f32_16x16x32_bf16 a[176:179], v[108:111], v[164:167], a[176:179]\n" \
"v_mfma_f32_16x16x32_bf16 a[208:211], v[112:115], v[160:163], a[208:211]\n" \
"ds_read_b128 v[104:107], v40 offset:10240\n" \
"v_mfma_f32_16x16x32_bf16 a[208:211], v[116:119], v[164:167], a[208:211]\n" \
"v_mfma_f32_16x16x32_bf16 a[212:215], v[112:115], v[168:171], a[212:215]\n" \
"ds_read_b64_tr_b16 v[156:157], v43 offset:49664\n" \
"v_mfma_f32_16x16x32_bf16 a[212:215], v[116:119], v[172:175], a[212:215]\n" \
"v_mfma_f32_16x16x32_bf16 a[216:219], v[112:115], v[176:179], a[216:219]\n" \
"ds_read_b128 v[108:111], v41 offset:10240\n" \
"v_mfma_f32_16x16x32_bf16 a[216:219], v[116:119], v[180:183], a[216:219]\n" \
"v_mfma_f32_16x16x32_bf16 a[220:223], v[112:115], v[184:187], a[220:223]\n" \
"ds_read_b64_tr_b16 v[158:159], v43 offset:53760\n" \
"v_mfma_f32_16x16x32_bf16 a[220:223], v[116:119], v[188:191], a[220:223]\n" \
"v_mfma_f32_16x16x32_bf16 a[252:255], v[120:123], v[184:187], a[252:255]\n" \
"ds_read_b128 v[112:115], v40 offset:12288\n" \
"v_mfma_f32_16x16x32_bf16 a[252:255], v[124:127], v[188:191], a[252:255]\n" \
"v_mfma_f32_16x16x32_bf16 a[248:251], v[120:123], v[176:179], a[248:251]\n" \
"v_mfma_f32_16x16x32_bf16 a[248:251], v[124:127], v[180:183], a[248:251]\n" \
"v_mfma_f32_16x16x32_bf16 a[244:247], v[120:123], v[168:171], a[244:247]\n" \
"ds_read_b128 v[116:119], v41 offset:12288\n" \
"v_mfma_f32_16x16x32_bf16 a[244:247], v[124:127], v[172:175], a[244:247]\n" \
"v_mfma_f32_16x16x32_bf16 a[240:243], v[120:123], v[160:163], a[240:243]\n" \
"v_mfma_f32_16x16x32_bf16 a[240:243], v[124:127], v[164:167], a[240:243]\n" \
"s_waitcnt lgkmcnt(1)\n" \
"v_mfma_f32_16x16x32_bf16 a[0:3], v[128:131], v[64:67], a[0:3]\n" \
"ds_read_b128 v[120:123], v40 offset:14336\n" \
"v_mfma_f32_16x16x32_bf16 a[0:3], v[132:135], v[68:71], a[0:3]\n" \
"v_mfma_f32_16x16x32_bf16 a[4:7], v[136:139], v[64:67], a[4:7]\n" \
"s_add_u32 m0, s74, 98304\n" \
"ds_read_b128 v[124:127], v41 offset:14336\n" \
"v_mfma_f32_16x16x32_bf16 a[4:7], v[140:143], v[68:71], a[4:7]\n" \
"buffer_load_dwordx4 v36, s[40:43], s83 offen offset:0 lds\n" \
"v_mfma_f32_16x16x32_bf16 a[8:11], v[144:147], v[64:67], a[8:11]\n" \
"ds_read_b64_tr_b16 v[160:161], v42 offset:33792\n" \
"v_mfma_f32_16x16x32_bf16 a[8:11], v[148:151], v[68:71], a[8:11]\n" \
"v_mfma_f32_16x16x32_bf16 a[12:15], v[152:155], v[64:67], a[12:15]\n" \
"ds_read_b64_tr_b16 v[162:163], v42 offset:37888\n" \
"v_mfma_f32_16x16x32_bf16 a[12:15], v[156:159], v[68:71], a[12:15]\n" \
"buffer_load_dwordx4 v37, s[44:47], s83 offen offset:1024 lds\n" \
"v_mfma_f32_16x16x32_bf16 a[44:47], v[152:155], v[72:75], a[44:47]\n" \
"ds_read_b64_tr_b16 v[164:165], v42 offset:50176\n" \
"v_mfma_f32_16x16x32_bf16 a[44:47], v[156:159], v[76:79], a[44:47]\n" \
"v_mfma_f32_16x16x32_bf16 a[40:43], v[144:147], v[72:75], a[40:43]\n" \
"ds_read_b64_tr_b16 v[166:167], v42 offset:54272\n" \
"v_mfma_f32_16x16x32_bf16 a[40:43], v[148:151], v[76:79], a[40:43]\n" \
"buffer_load_dwordx4 v38, s[40:43], s83 offen offset:2048 lds\n" \
"v_mfma_f32_16x16x32_bf16 a[36:39], v[136:139], v[72:75], a[36:39]\n" \
"ds_read_b64_tr_b16 v[168:169], v43 offset:33792\n" \
"v_mfma_f32_16x16x32_bf16 a[36:39], v[140:143], v[76:79], a[36:39]\n" \
"v_mfma_f32_16x16x32_bf16 a[32:35], v[128:131], v[72:75], a[32:35]\n" \
"ds_read_b64_tr_b16 v[170:171], v43 offset:37888\n" \
"v_mfma_f32_16x16x32_bf16 a[32:35], v[132:135], v[76:79], a[32:35]\n" \
"buffer_load_dwordx4 v39, s[44:47], s83 offen offset:3072 lds\n" \
"v_mfma_f32_16x16x32_bf16 a[64:67], v[128:131], v[80:83], a[64:67]\n" \
"ds_read_b64_tr_b16 v[172:173], v43 offset:50176\n" \
"v_mfma_f32_16x16x32_bf16 a[64:67], v[132:135], v[84:87], a[64:67]\n" \
"v_mfma_f32_16x16x32_bf16 a[68:71], v[136:139], v[80:83], a[68:71]\n" \
"s_add_u32 m0, s74, 102400\n" \
"ds_read_b64_tr_b16 v[174:175], v43 offset:54272\n" \
"v_mfma_f32_16x16x32_bf16 a[68:71], v[140:143], v[84:87], a[68:71]\n" \
"buffer_load_dwordx4 v36, s[40:43], s84 offen offset:0 lds\n" \
"v_mfma_f32_16x16x32_bf16 a[72:75], v[144:147], v[80:83], a[72:75]\n" \
"ds_read_b64_tr_b16 v[176:177], v42 offset:34304\n" \
"v_mfma_f32_16x16x32_bf16 a[72:75], v[148:151], v[84:87], a[72:75]\n" \
"v_mfma_f32_16x16x32_bf16 a[76:79], v[152:155], v[80:83], a[76:79]\n" \
"ds_read_b64_tr_b16 v[178:179], v42 offset:38400\n" \
"v_mfma_f32_16x16x32_bf16 a[76:79], v[156:159], v[84:87], a[76:79]\n" \
"buffer_load_dwordx4 v37, s[44:47], s84 offen offset:1024 lds\n" \
"v_mfma_f32_16x16x32_bf16 a[108:111], v[152:155], v[88:91], a[108:111]\n" \
"ds_read_b64_tr_b16 v[180:181], v42 offset:50688\n" \
"v_mfma_f32_16x16x32_bf16 a[108:111], v[156:159], v[92:95], a[108:111]\n" \
"v_mfma_f32_16x16x32_bf16 a[104:107], v[144:147], v[88:91], a[104:107]\n" \
"ds_read_b64_tr_b16 v[182:183], v42 offset:54784\n" \
"v_mfma_f32_16x16x32_bf16 a[104:107], v[148:151], v[92:95], a[104:107]\n" \
"buffer_load_dwordx4 v38, s[40:43], s84 offen offset:2048 lds\n" \
"v_mfma_f32_16x16x32_bf16 a[100:103], v[136:139], v[88:91], a[100:103]\n" \
"ds_read_b64_tr_b16 v[184:185], v43 offset:34304\n" \
"v_mfma_f32_16x16x32_bf16 a[100:103], v[140:143], v[92:95], a[100:103]\n" \
"v_mfma_f32_16x16x32_bf16 a[96:99], v[128:131], v[88:91], a[96:99]\n" \
"ds_read_b64_tr_b16 v[186:187], v43 offset:38400\n" \
"v_mfma_f32_16x16x32_bf16 a[96:99], v[132:135], v[92:95], a[96:99]\n" \
"buffer_load_dwordx4 v39, s[44:47], s84 offen offset:3072 lds\n" \
"v_mfma_f32_16x16x32_bf16 a[128:131], v[128:131], v[96:99], a[128:131]\n" \
"ds_read_b64_tr_b16 v[188:189], v43 offset:50688\n" \
"v_mfma_f32_16x16x32_bf16 a[128:131], v[132:135], v[100:103], a[128:131]\n" \
"v_mfma_f32_16x16x32_bf16 a[132:135], v[136:139], v[96:99], a[132:135]\n" \
"ds_read_b64_tr_b16 v[190:191], v43 offset:54784\n" \
"v_mfma_f32_16x16x32_bf16 a[132:135], v[140:143], v[100:103], a[132:135]\n" \
"v_mfma_f32_16x16x32_bf16 a[136:139], v[144:147], v[96:99], a[136:139]\n" \
"v_mfma_f32_16x16x32_bf16 a[136:139], v[148:151], v[100:103], a[136:139]\n" \
"v_mfma_f32_16x16x32_bf16 a[140:143], v[152:155], v[96:99], a[140:143]\n" \
"v_mfma_f32_16x16x32_bf16 a[140:143], v[156:159], v[100:103], a[140:143]\n" \
"v_mfma_f32_16x16x32_bf16 a[172:175], v[152:155], v[104:107], a[172:175]\n" \
"v_mfma_f32_16x16x32_bf16 a[172:175], v[156:159], v[108:111], a[172:175]\n" \
"v_mfma_f32_16x16x32_bf16 a[168:171], v[144:147], v[104:107], a[168:171]\n" \
"v_mfma_f32_16x16x32_bf16 a[168:171], v[148:151], v[108:111], a[168:171]\n" \
"v_mfma_f32_16x16x32_bf16 a[164:167], v[136:139], v[104:107], a[164:167]\n" \
"v_mfma_f32_16x16x32_bf16 a[164:167], v[140:143], v[108:111], a[164:167]\n" \
"v_mfma_f32_16x16x32_bf16 a[160:163], v[128:131], v[104:107], a[160:163]\n" \
"v_mfma_f32_16x16x32_bf16 a[160:163], v[132:135], v[108:111], a[160:163]\n" \
"s_waitcnt lgkmcnt(0)\n" \
"v_mfma_f32_16x16x32_bf16 a[192:195], v[128:131], v[112:115], a[192:195]\n" \
"v_mfma_f32_16x16x32_bf16 a[192:195], v[132:135], v[116:119], a[192:195]\n" \
"v_mfma_f32_16x16x32_bf16 a[196:199], v[136:139], v[112:115], a[196:199]\n" \
"v_mfma_f32_16x16x32_bf16 a[196:199], v[140:143], v[116:119], a[196:199]\n" \
"v_mfma_f32_16x16x32_bf16 a[200:203], v[144:147], v[112:115], a[200:203]\n" \
"v_mfma_f32_16x16x32_bf16 a[200:203], v[148:151], v[116:119], a[200:203]\n" \
"v_mfma_f32_16x16x32_bf16 a[204:207], v[152:155], v[112:115], a[204:207]\n" \
"v_mfma_f32_16x16x32_bf16 a[204:207], v[156:159], v[116:119], a[204:207]\n" \
"v_mfma_f32_16x16x32_bf16 a[236:239], v[152:155], v[120:123], a[236:239]\n" \
"v_mfma_f32_16x16x32_bf16 a[236:239], v[156:159], v[124:127], a[236:239]\n" \
"v_mfma_f32_16x16x32_bf16 a[232:235], v[144:147], v[120:123], a[232:235]\n" \
"v_mfma_f32_16x16x32_bf16 a[232:235], v[148:151], v[124:127], a[232:235]\n" \
"v_mfma_f32_16x16x32_bf16 a[228:231], v[136:139], v[120:123], a[228:231]\n" \
"v_mfma_f32_16x16x32_bf16 a[228:231], v[140:143], v[124:127], a[228:231]\n" \
"v_mfma_f32_16x16x32_bf16 a[224:227], v[128:131], v[120:123], a[224:227]\n" \
"v_mfma_f32_16x16x32_bf16 a[224:227], v[132:135], v[124:127], a[224:227]\n" \
"s_waitcnt vmcnt(11) lgkmcnt(0)\n" \
"s_barrier\n" \
"v_mfma_f32_16x16x32_bf16 a[16:19], v[64:67], v[160:163], a[16:19]\n" \
"s_add_u32 m0, s74, 32768\n" \
"ds_read_b64_tr_b16 v[128:129], v63 offset:0\n" \
"v_mfma_f32_16x16x32_bf16 a[16:19], v[68:71], v[164:167], a[16:19]\n" \
"buffer_load_dwordx4 v32, s[36:39], s81 offen offset:0 lds\n" \
"v_mfma_f32_16x16x32_bf16 a[20:23], v[64:67], v[168:171], a[20:23]\n" \
"ds_read_b64_tr_b16 v[130:131], v63 offset:4096\n" \
"v_mfma_f32_16x16x32_bf16 a[20:23], v[68:71], v[172:175], a[20:23]\n" \
"v_mfma_f32_16x16x32_bf16 a[24:27], v[64:67], v[176:179], a[24:27]\n" \
"ds_read_b64_tr_b16 v[132:133], v63 offset:16384\n" \
"v_mfma_f32_16x16x32_bf16 a[24:27], v[68:71], v[180:183], a[24:27]\n" \
"buffer_load_dwordx4 v33, s[36:39], s81 offen offset:1024 lds\n" \
"v_mfma_f32_16x16x32_bf16 a[28:31], v[64:67], v[184:187], a[28:31]\n" \
"ds_read_b64_tr_b16 v[134:135], v63 offset:20480\n" \
"v_mfma_f32_16x16x32_bf16 a[28:31], v[68:71], v[188:191], a[28:31]\n" \
"v_mfma_f32_16x16x32_bf16 a[60:63], v[72:75], v[184:187], a[60:63]\n" \
"ds_read_b128 v[64:67], v44 offset:0\n" \
"v_mfma_f32_16x16x32_bf16 a[60:63], v[76:79], v[188:191], a[60:63]\n" \
"buffer_load_dwordx4 v34, s[36:39], s81 offen offset:2048 lds\n" \
"v_mfma_f32_16x16x32_bf16 a[56:59], v[72:75], v[176:179], a[56:59]\n" \
"ds_read_b64_tr_b16 v[136:137], v252 offset:0\n" \
"v_mfma_f32_16x16x32_bf16 a[56:59], v[76:79], v[180:183], a[56:59]\n" \
"v_mfma_f32_16x16x32_bf16 a[52:55], v[72:75], v[168:171], a[52:55]\n" \
"ds_read_b128 v[68:71], v45 offset:0\n" \
"v_mfma_f32_16x16x32_bf16 a[52:55], v[76:79], v[172:175], a[52:55]\n" \
"buffer_load_dwordx4 v35, s[36:39], s81 offen offset:3072 lds\n" \
"v_mfma_f32_16x16x32_bf16 a[48:51], v[72:75], v[160:163], a[48:51]\n" \
"ds_read_b64_tr_b16 v[138:139], v252 offset:4096\n" \
"v_mfma_f32_16x16x32_bf16 a[48:51], v[76:79], v[164:167], a[48:51]\n" \
"v_mfma_f32_16x16x32_bf16 a[80:83], v[80:83], v[160:163], a[80:83]\n" \
"s_add_u32 m0, s74, 36864\n" \
"ds_read_b128 v[72:75], v44 offset:2048\n" \
"v_mfma_f32_16x16x32_bf16 a[80:83], v[84:87], v[164:167], a[80:83]\n" \
"buffer_load_dwordx4 v32, s[36:39], s82 offen offset:0 lds\n" \
"v_mfma_f32_16x16x32_bf16 a[84:87], v[80:83], v[168:171], a[84:87]\n" \
"ds_read_b64_tr_b16 v[140:141], v252 offset:16384\n" \
"v_mfma_f32_16x16x32_bf16 a[84:87], v[84:87], v[172:175], a[84:87]\n" \
"v_mfma_f32_16x16x32_bf16 a[88:91], v[80:83], v[176:179], a[88:91]\n" \
"ds_read_b128 v[76:79], v45 offset:2048\n" \
"v_mfma_f32_16x16x32_bf16 a[88:91], v[84:87], v[180:183], a[88:91]\n" \
"buffer_load_dwordx4 v33, s[36:39], s82 offen offset:1024 lds\n" \
"v_mfma_f32_16x16x32_bf16 a[92:95], v[80:83], v[184:187], a[92:95]\n" \
"ds_read_b64_tr_b16 v[142:143], v252 offset:20480\n" \
"v_mfma_f32_16x16x32_bf16 a[92:95], v[84:87], v[188:191], a[92:95]\n" \
"v_mfma_f32_16x16x32_bf16 a[124:127], v[88:91], v[184:187], a[124:127]\n" \
"ds_read_b128 v[80:83], v44 offset:4096\n" \
"v_mfma_f32_16x16x32_bf16 a[124:127], v[92:95], v[188:191], a[124:127]\n" \
"buffer_load_dwordx4 v34, s[36:39], s82 offen offset:2048 lds\n" \
"v_mfma_f32_16x16x32_bf16 a[120:123], v[88:91], v[176:179], a[120:123]\n" \
"ds_read_b64_tr_b16 v[144:145], v63 offset:512\n" \
"v_mfma_f32_16x16x32_bf16 a[120:123], v[92:95], v[180:183], a[120:123]\n" \
"v_mfma_f32_16x16x32_bf16 a[116:119], v[88:91], v[168:171], a[116:119]\n" \
"ds_read_b128 v[84:87], v45 offset:4096\n" \
"v_mfma_f32_16x16x32_bf16 a[116:119], v[92:95], v[172:175], a[116:119]\n" \
"buffer_load_dwordx4 v35, s[36:39], s82 offen offset:3072 lds\n" \
"v_mfma_f32_16x16x32_bf16 a[112:115], v[88:91], v[160:163], a[112:115]\n" \
"s_mov_b64 s[64:65], s[12:13]\n" \
"s_add_u32 s68, s67, 0x300000\n" \
"ds_read_b64_tr_b16 v[146:147], v63 offset:4608\n" \
"v_mfma_f32_16x16x32_bf16 a[112:115], v[92:95], v[164:167], a[112:115]\n" \
"s_add_u32 s64, s64, s68\n" \
"s_addc_u32 s65, s65, 0\n" \
"v_mfma_f32_16x16x32_bf16 a[144:147], v[96:99], v[160:163], a[144:147]\n" \
"ds_read_b128 v[88:91], v44 offset:6144\n" \
"v_mfma_f32_16x16x32_bf16 a[144:147], v[100:103], v[164:167], a[144:147]\n" \
"global_load_dword v238, v234, s[64:65]\n" \
"v_mfma_f32_16x16x32_bf16 a[148:151], v[96:99], v[168:171], a[148:151]\n" \
"s_mov_b64 s[64:65], s[12:13]\n" \
"s_add_u32 s68, s67, 0x400000\n" \
"ds_read_b64_tr_b16 v[148:149], v63 offset:16896\n" \
"v_mfma_f32_16x16x32_bf16 a[148:151], v[100:103], v[172:175], a[148:151]\n" \
"s_add_u32 s64, s64, s68\n" \
"s_addc_u32 s65, s65, 0\n" \
"v_mfma_f32_16x16x32_bf16 a[152:155], v[96:99], v[176:179], a[152:155]\n" \
"ds_read_b128 v[92:95], v45 offset:6144\n" \
"v_mfma_f32_16x16x32_bf16 a[152:155], v[100:103], v[180:183], a[152:155]\n" \
"global_load_dword v238, v234, s[64:65]\n" \
"v_mfma_f32_16x16x32_bf16 a[156:159], v[96:99], v[184:187], a[156:159]\n" \
"s_mov_b64 s[64:65], s[12:13]\n" \
"s_add_u32 s68, s67, 0x600000\n" \
"ds_read_b64_tr_b16 v[150:151], v63 offset:20992\n" \
"v_mfma_f32_16x16x32_bf16 a[156:159], v[100:103], v[188:191], a[156:159]\n" \
"s_add_u32 s64, s64, s68\n" \
"s_addc_u32 s65, s65, 0\n" \
"v_mfma_f32_16x16x32_bf16 a[188:191], v[104:107], v[184:187], a[188:191]\n" \
"ds_read_b128 v[96:99], v44 offset:8192\n" \
"v_mfma_f32_16x16x32_bf16 a[188:191], v[108:111], v[188:191], a[188:191]\n" \
"global_load_dword v238, v234, s[64:65]\n" \
"v_mfma_f32_16x16x32_bf16 a[184:187], v[104:107], v[176:179], a[184:187]\n" \
"ds_read_b64_tr_b16 v[152:153], v252 offset:512\n" \
"v_mfma_f32_16x16x32_bf16 a[184:187], v[108:111], v[180:183], a[184:187]\n" \
"v_mfma_f32_16x16x32_bf16 a[180:183], v[104:107], v[168:171], a[180:183]\n" \
"ds_read_b128 v[100:103], v45 offset:8192\n" \
"v_mfma_f32_16x16x32_bf16 a[180:183], v[108:111], v[172:175], a[180:183]\n" \
"v_mfma_f32_16x16x32_bf16 a[176:179], v[104:107], v[160:163], a[176:179]\n" \
"ds_read_b64_tr_b16 v[154:155], v252 offset:4608\n" \
"v_mfma_f32_16x16x32_bf16 a[176:179], v[108:111], v[164:167], a[176:179]\n" \
"v_mfma_f32_16x16x32_bf16 a[208:211], v[112:115], v[160:163], a[208:211]\n" \
"ds_read_b128 v[104:107], v44 offset:10240\n" \
"v_mfma_f32_16x16x32_bf16 a[208:211], v[116:119], v[164:167], a[208:211]\n" \
"v_mfma_f32_16x16x32_bf16 a[212:215], v[112:115], v[168:171], a[212:215]\n" \
"ds_read_b64_tr_b16 v[156:157], v252 offset:16896\n" \
"v_mfma_f32_16x16x32_bf16 a[212:215], v[116:119], v[172:175], a[212:215]\n" \
"v_mfma_f32_16x16x32_bf16 a[216:219], v[112:115], v[176:179], a[216:219]\n" \
"ds_read_b128 v[108:111], v45 offset:10240\n" \
"v_mfma_f32_16x16x32_bf16 a[216:219], v[116:119], v[180:183], a[216:219]\n" \
"v_mfma_f32_16x16x32_bf16 a[220:223], v[112:115], v[184:187], a[220:223]\n" \
"ds_read_b64_tr_b16 v[158:159], v252 offset:20992\n" \
"v_mfma_f32_16x16x32_bf16 a[220:223], v[116:119], v[188:191], a[220:223]\n" \
"v_mfma_f32_16x16x32_bf16 a[252:255], v[120:123], v[184:187], a[252:255]\n" \
"ds_read_b128 v[112:115], v44 offset:12288\n" \
"v_mfma_f32_16x16x32_bf16 a[252:255], v[124:127], v[188:191], a[252:255]\n" \
"v_mfma_f32_16x16x32_bf16 a[248:251], v[120:123], v[176:179], a[248:251]\n" \
"v_mfma_f32_16x16x32_bf16 a[248:251], v[124:127], v[180:183], a[248:251]\n" \
"v_mfma_f32_16x16x32_bf16 a[244:247], v[120:123], v[168:171], a[244:247]\n" \
"ds_read_b128 v[116:119], v45 offset:12288\n" \
"v_mfma_f32_16x16x32_bf16 a[244:247], v[124:127], v[172:175], a[244:247]\n" \
"v_mfma_f32_16x16x32_bf16 a[240:243], v[120:123], v[160:163], a[240:243]\n" \
"v_mfma_f32_16x16x32_bf16 a[240:243], v[124:127], v[164:167], a[240:243]\n" \
"s_waitcnt lgkmcnt(1)\n" \
"v_mfma_f32_16x16x32_bf16 a[0:3], v[128:131], v[64:67], a[0:3]\n" \
"ds_read_b128 v[120:123], v44 offset:14336\n" \
"v_mfma_f32_16x16x32_bf16 a[0:3], v[132:135], v[68:71], a[0:3]\n" \
"Lpg1661_%=:\n" \
"Lst0x2_%=:\n" \
"s_cbranch_execz Lpg3859_%=\n" \
"v_mfma_f32_16x16x32_bf16 a[4:7], v[136:139], v[64:67], a[4:7]\n" \
"s_add_u32 m0, s79, 0\n" \
"ds_read_b128 v[124:127], v45 offset:14336\n" \
"v_mfma_f32_16x16x32_bf16 a[4:7], v[140:143], v[68:71], a[4:7]\n" \
"buffer_load_dwordx4 v228, s[52:55], s78 offen offset:0 lds\n" \
"v_mfma_f32_16x16x32_bf16 a[8:11], v[144:147], v[64:67], a[8:11]\n" \
"ds_read_b64_tr_b16 v[160:161], v63 offset:1024\n" \
"v_mfma_f32_16x16x32_bf16 a[8:11], v[148:151], v[68:71], a[8:11]\n" \
"v_mfma_f32_16x16x32_bf16 a[12:15], v[152:155], v[64:67], a[12:15]\n" \
"ds_read_b64_tr_b16 v[162:163], v63 offset:5120\n" \
"v_mfma_f32_16x16x32_bf16 a[12:15], v[156:159], v[68:71], a[12:15]\n" \
"buffer_load_dwordx4 v229, s[52:55], s78 offen offset:1024 lds\n" \
"v_mfma_f32_16x16x32_bf16 a[44:47], v[152:155], v[72:75], a[44:47]\n" \
"ds_read_b64_tr_b16 v[164:165], v63 offset:17408\n" \
"v_mfma_f32_16x16x32_bf16 a[44:47], v[156:159], v[76:79], a[44:47]\n" \
"v_mfma_f32_16x16x32_bf16 a[40:43], v[144:147], v[72:75], a[40:43]\n" \
"s_xor_b32 s78, s78, 0x180000\n" \
"ds_read_b64_tr_b16 v[166:167], v63 offset:21504\n" \
"v_mfma_f32_16x16x32_bf16 a[40:43], v[148:151], v[76:79], a[40:43]\n" \
"buffer_load_dwordx4 v228, s[52:55], s89 offen offset:2048 lds\n" \
"v_mfma_f32_16x16x32_bf16 a[36:39], v[136:139], v[72:75], a[36:39]\n" \
"ds_read_b64_tr_b16 v[168:169], v252 offset:1024\n" \
"v_mfma_f32_16x16x32_bf16 a[36:39], v[140:143], v[76:79], a[36:39]\n" \
"v_mfma_f32_16x16x32_bf16 a[32:35], v[128:131], v[72:75], a[32:35]\n" \
"ds_read_b64_tr_b16 v[170:171], v252 offset:5120\n" \
"v_mfma_f32_16x16x32_bf16 a[32:35], v[132:135], v[76:79], a[32:35]\n" \
"buffer_load_dwordx4 v229, s[52:55], s89 offen offset:3072 lds\n" \
"v_mfma_f32_16x16x32_bf16 a[64:67], v[128:131], v[80:83], a[64:67]\n" \
"ds_read_b64_tr_b16 v[172:173], v252 offset:17408\n" \
"v_mfma_f32_16x16x32_bf16 a[64:67], v[132:135], v[84:87], a[64:67]\n" \
"s_xor_b32 s89, s89, 0x180000\n" \
"v_mfma_f32_16x16x32_bf16 a[68:71], v[136:139], v[80:83], a[68:71]\n" \
"s_add_u32 m0, s79, 16384\n" \
"ds_read_b64_tr_b16 v[174:175], v252 offset:21504\n" \
"v_mfma_f32_16x16x32_bf16 a[68:71], v[140:143], v[84:87], a[68:71]\n" \
"buffer_load_dwordx4 v228, s[52:55], s78 offen offset:0 lds\n" \
"v_mfma_f32_16x16x32_bf16 a[72:75], v[144:147], v[80:83], a[72:75]\n" \
"ds_read_b64_tr_b16 v[176:177], v63 offset:1536\n" \
"v_mfma_f32_16x16x32_bf16 a[72:75], v[148:151], v[84:87], a[72:75]\n" \
"v_mfma_f32_16x16x32_bf16 a[76:79], v[152:155], v[80:83], a[76:79]\n" \
"ds_read_b64_tr_b16 v[178:179], v63 offset:5632\n" \
"v_mfma_f32_16x16x32_bf16 a[76:79], v[156:159], v[84:87], a[76:79]\n" \
"buffer_load_dwordx4 v229, s[52:55], s78 offen offset:1024 lds\n" \
"v_mfma_f32_16x16x32_bf16 a[108:111], v[152:155], v[88:91], a[108:111]\n" \
"ds_read_b64_tr_b16 v[180:181], v63 offset:17920\n" \
"v_mfma_f32_16x16x32_bf16 a[108:111], v[156:159], v[92:95], a[108:111]\n" \
"v_mfma_f32_16x16x32_bf16 a[104:107], v[144:147], v[88:91], a[104:107]\n" \
"s_xor_b32 s78, s78, 0x400000\n" \
"ds_read_b64_tr_b16 v[182:183], v63 offset:22016\n" \
"v_mfma_f32_16x16x32_bf16 a[104:107], v[148:151], v[92:95], a[104:107]\n" \
"buffer_load_dwordx4 v228, s[52:55], s89 offen offset:2048 lds\n" \
"v_mfma_f32_16x16x32_bf16 a[100:103], v[136:139], v[88:91], a[100:103]\n" \
"ds_read_b64_tr_b16 v[184:185], v252 offset:1536\n" \
"v_mfma_f32_16x16x32_bf16 a[100:103], v[140:143], v[92:95], a[100:103]\n" \
"v_mfma_f32_16x16x32_bf16 a[96:99], v[128:131], v[88:91], a[96:99]\n" \
"ds_read_b64_tr_b16 v[186:187], v252 offset:5632\n" \
"v_mfma_f32_16x16x32_bf16 a[96:99], v[132:135], v[92:95], a[96:99]\n" \
"buffer_load_dwordx4 v229, s[52:55], s89 offen offset:3072 lds\n" \
"v_mfma_f32_16x16x32_bf16 a[128:131], v[128:131], v[96:99], a[128:131]\n" \
"ds_read_b64_tr_b16 v[188:189], v252 offset:17920\n" \
"v_mfma_f32_16x16x32_bf16 a[128:131], v[132:135], v[100:103], a[128:131]\n" \
"v_mfma_f32_16x16x32_bf16 a[132:135], v[136:139], v[96:99], a[132:135]\n" \
"s_xor_b32 s89, s89, 0x400000\n" \
"ds_read_b64_tr_b16 v[190:191], v252 offset:22016\n" \
"v_mfma_f32_16x16x32_bf16 a[132:135], v[140:143], v[100:103], a[132:135]\n" \
"v_mfma_f32_16x16x32_bf16 a[136:139], v[144:147], v[96:99], a[136:139]\n" \
"v_mfma_f32_16x16x32_bf16 a[136:139], v[148:151], v[100:103], a[136:139]\n" \
"v_mfma_f32_16x16x32_bf16 a[140:143], v[152:155], v[96:99], a[140:143]\n" \
"v_mfma_f32_16x16x32_bf16 a[140:143], v[156:159], v[100:103], a[140:143]\n" \
"v_mfma_f32_16x16x32_bf16 a[172:175], v[152:155], v[104:107], a[172:175]\n" \
"v_mfma_f32_16x16x32_bf16 a[172:175], v[156:159], v[108:111], a[172:175]\n" \
"v_mfma_f32_16x16x32_bf16 a[168:171], v[144:147], v[104:107], a[168:171]\n" \
"v_mfma_f32_16x16x32_bf16 a[168:171], v[148:151], v[108:111], a[168:171]\n" \
"v_mfma_f32_16x16x32_bf16 a[164:167], v[136:139], v[104:107], a[164:167]\n" \
"v_mfma_f32_16x16x32_bf16 a[164:167], v[140:143], v[108:111], a[164:167]\n" \
"v_mfma_f32_16x16x32_bf16 a[160:163], v[128:131], v[104:107], a[160:163]\n" \
"v_mfma_f32_16x16x32_bf16 a[160:163], v[132:135], v[108:111], a[160:163]\n" \
"s_waitcnt lgkmcnt(0)\n" \
"v_mfma_f32_16x16x32_bf16 a[192:195], v[128:131], v[112:115], a[192:195]\n" \
"v_mfma_f32_16x16x32_bf16 a[192:195], v[132:135], v[116:119], a[192:195]\n" \
"v_mfma_f32_16x16x32_bf16 a[196:199], v[136:139], v[112:115], a[196:199]\n" \
"v_mfma_f32_16x16x32_bf16 a[196:199], v[140:143], v[116:119], a[196:199]\n" \
"v_mfma_f32_16x16x32_bf16 a[200:203], v[144:147], v[112:115], a[200:203]\n" \
"v_mfma_f32_16x16x32_bf16 a[200:203], v[148:151], v[116:119], a[200:203]\n" \
"v_mfma_f32_16x16x32_bf16 a[204:207], v[152:155], v[112:115], a[204:207]\n" \
"v_mfma_f32_16x16x32_bf16 a[204:207], v[156:159], v[116:119], a[204:207]\n" \
"v_mfma_f32_16x16x32_bf16 a[236:239], v[152:155], v[120:123], a[236:239]\n" \
"v_mfma_f32_16x16x32_bf16 a[236:239], v[156:159], v[124:127], a[236:239]\n" \
"v_mfma_f32_16x16x32_bf16 a[232:235], v[144:147], v[120:123], a[232:235]\n" \
"v_mfma_f32_16x16x32_bf16 a[232:235], v[148:151], v[124:127], a[232:235]\n" \
"v_mfma_f32_16x16x32_bf16 a[228:231], v[136:139], v[120:123], a[228:231]\n" \
"v_mfma_f32_16x16x32_bf16 a[228:231], v[140:143], v[124:127], a[228:231]\n" \
"v_mfma_f32_16x16x32_bf16 a[224:227], v[128:131], v[120:123], a[224:227]\n" \
"v_mfma_f32_16x16x32_bf16 a[224:227], v[132:135], v[124:127], a[224:227]\n" \
"s_waitcnt vmcnt(11) lgkmcnt(0)\n" \
"s_barrier\n" \
"v_mfma_f32_16x16x32_bf16 a[16:19], v[64:67], v[160:163], a[16:19]\n" \
"s_add_u32 m0, s74, 131072\n" \
"ds_read_b64_tr_b16 v[128:129], v59 offset:32768\n" \
"v_mfma_f32_16x16x32_bf16 a[16:19], v[68:71], v[164:167], a[16:19]\n" \
"buffer_load_dwordx4 v224, s[48:51], s75 offen offset:0 lds\n" \
"v_mfma_f32_16x16x32_bf16 a[20:23], v[64:67], v[168:171], a[20:23]\n" \
"ds_read_b64_tr_b16 v[130:131], v59 offset:36864\n" \
"v_mfma_f32_16x16x32_bf16 a[20:23], v[68:71], v[172:175], a[20:23]\n" \
"v_mfma_f32_16x16x32_bf16 a[24:27], v[64:67], v[176:179], a[24:27]\n" \
"ds_read_b64_tr_b16 v[132:133], v59 offset:49152\n" \
"v_mfma_f32_16x16x32_bf16 a[24:27], v[68:71], v[180:183], a[24:27]\n" \
"buffer_load_dwordx4 v225, s[48:51], s75 offen offset:1024 lds\n" \
"v_mfma_f32_16x16x32_bf16 a[28:31], v[64:67], v[184:187], a[28:31]\n" \
"ds_read_b64_tr_b16 v[134:135], v59 offset:53248\n" \
"v_mfma_f32_16x16x32_bf16 a[28:31], v[68:71], v[188:191], a[28:31]\n" \
"v_mfma_f32_16x16x32_bf16 a[60:63], v[72:75], v[184:187], a[60:63]\n" \
"ds_read_b128 v[64:67], v40 offset:32768\n" \
"v_mfma_f32_16x16x32_bf16 a[60:63], v[76:79], v[188:191], a[60:63]\n" \
"buffer_load_dwordx4 v226, s[48:51], s75 offen offset:2048 lds\n" \
"v_mfma_f32_16x16x32_bf16 a[56:59], v[72:75], v[176:179], a[56:59]\n" \
"ds_read_b64_tr_b16 v[136:137], v60 offset:32768\n" \
"v_mfma_f32_16x16x32_bf16 a[56:59], v[76:79], v[180:183], a[56:59]\n" \
"v_mfma_f32_16x16x32_bf16 a[52:55], v[72:75], v[168:171], a[52:55]\n" \
"ds_read_b128 v[68:71], v41 offset:32768\n" \
"v_mfma_f32_16x16x32_bf16 a[52:55], v[76:79], v[172:175], a[52:55]\n" \
"buffer_load_dwordx4 v227, s[48:51], s75 offen offset:3072 lds\n" \
"v_mfma_f32_16x16x32_bf16 a[48:51], v[72:75], v[160:163], a[48:51]\n" \
"ds_read_b64_tr_b16 v[138:139], v60 offset:36864\n" \
"v_mfma_f32_16x16x32_bf16 a[48:51], v[76:79], v[164:167], a[48:51]\n" \
"s_xor_b32 s75, s75, 0x180\n" \
"v_mfma_f32_16x16x32_bf16 a[80:83], v[80:83], v[160:163], a[80:83]\n" \
"s_add_u32 m0, s74, 135168\n" \
"ds_read_b128 v[72:75], v40 offset:34816\n" \
"v_mfma_f32_16x16x32_bf16 a[80:83], v[84:87], v[164:167], a[80:83]\n" \
"buffer_load_dwordx4 v224, s[48:51], s77 offen offset:0 lds\n" \
"v_mfma_f32_16x16x32_bf16 a[84:87], v[80:83], v[168:171], a[84:87]\n" \
"ds_read_b64_tr_b16 v[140:141], v60 offset:49152\n" \
"v_mfma_f32_16x16x32_bf16 a[84:87], v[84:87], v[172:175], a[84:87]\n" \
"v_mfma_f32_16x16x32_bf16 a[88:91], v[80:83], v[176:179], a[88:91]\n" \
"ds_read_b128 v[76:79], v41 offset:34816\n" \
"v_mfma_f32_16x16x32_bf16 a[88:91], v[84:87], v[180:183], a[88:91]\n" \
"buffer_load_dwordx4 v225, s[48:51], s77 offen offset:1024 lds\n" \
"v_mfma_f32_16x16x32_bf16 a[92:95], v[80:83], v[184:187], a[92:95]\n" \
"ds_read_b64_tr_b16 v[142:143], v60 offset:53248\n" \
"v_mfma_f32_16x16x32_bf16 a[92:95], v[84:87], v[188:191], a[92:95]\n" \
"v_mfma_f32_16x16x32_bf16 a[124:127], v[88:91], v[184:187], a[124:127]\n" \
"ds_read_b128 v[80:83], v40 offset:36864\n" \
"v_mfma_f32_16x16x32_bf16 a[124:127], v[92:95], v[188:191], a[124:127]\n" \
"buffer_load_dwordx4 v226, s[48:51], s77 offen offset:2048 lds\n" \
"v_mfma_f32_16x16x32_bf16 a[120:123], v[88:91], v[176:179], a[120:123]\n" \
"ds_read_b64_tr_b16 v[144:145], v59 offset:33280\n" \
"v_mfma_f32_16x16x32_bf16 a[120:123], v[92:95], v[180:183], a[120:123]\n" \
"v_mfma_f32_16x16x32_bf16 a[116:119], v[88:91], v[168:171], a[116:119]\n" \
"ds_read_b128 v[84:87], v41 offset:36864\n" \
"v_mfma_f32_16x16x32_bf16 a[116:119], v[92:95], v[172:175], a[116:119]\n" \
"buffer_load_dwordx4 v227, s[48:51], s77 offen offset:3072 lds\n" \
"v_mfma_f32_16x16x32_bf16 a[112:115], v[88:91], v[160:163], a[112:115]\n" \
"s_mov_b64 s[64:65], s[12:13]\n" \
"s_add_u32 s68, s67, 0x700000\n" \
"ds_read_b64_tr_b16 v[146:147], v59 offset:37376\n" \
"v_mfma_f32_16x16x32_bf16 a[112:115], v[92:95], v[164:167], a[112:115]\n" \
"s_add_u32 s64, s64, s68\n" \
"s_addc_u32 s65, s65, 0\n" \
"v_mfma_f32_16x16x32_bf16 a[144:147], v[96:99], v[160:163], a[144:147]\n" \
"s_xor_b32 s77, s77, 0x180\n" \
"ds_read_b128 v[88:91], v40 offset:38912\n" \
"v_mfma_f32_16x16x32_bf16 a[144:147], v[100:103], v[164:167], a[144:147]\n" \
"global_load_dword v238, v234, s[64:65]\n" \
"v_mfma_f32_16x16x32_bf16 a[148:151], v[96:99], v[168:171], a[148:151]\n" \
"ds_read_b64_tr_b16 v[148:149], v59 offset:49664\n" \
"v_mfma_f32_16x16x32_bf16 a[148:151], v[100:103], v[172:175], a[148:151]\n" \
"v_mfma_f32_16x16x32_bf16 a[152:155], v[96:99], v[176:179], a[152:155]\n" \
"s_lshr_b32 s68, s66, 2\n" \
"s_and_b32 s68, s68, 0xffff\n" \
"s_add_u32 s68, s68, 65585152\n" \
"ds_read_b128 v[92:95], v41 offset:38912\n" \
"v_mfma_f32_16x16x32_bf16 a[152:155], v[100:103], v[180:183], a[152:155]\n" \
"buffer_load_dword v238, v242, s[56:59], s68 offen sc1\n" \
"v_mfma_f32_16x16x32_bf16 a[156:159], v[96:99], v[184:187], a[156:159]\n" \
"ds_read_b64_tr_b16 v[150:151], v59 offset:53760\n" \
"v_mfma_f32_16x16x32_bf16 a[156:159], v[100:103], v[188:191], a[156:159]\n" \
"v_mfma_f32_16x16x32_bf16 a[188:191], v[104:107], v[184:187], a[188:191]\n" \
"ds_read_b128 v[96:99], v40 offset:40960\n" \
"v_mfma_f32_16x16x32_bf16 a[188:191], v[108:111], v[188:191], a[188:191]\n" \
"v_mfma_f32_16x16x32_bf16 a[184:187], v[104:107], v[176:179], a[184:187]\n" \
"ds_read_b64_tr_b16 v[152:153], v60 offset:33280\n" \
"v_mfma_f32_16x16x32_bf16 a[184:187], v[108:111], v[180:183], a[184:187]\n" \
"v_mfma_f32_16x16x32_bf16 a[180:183], v[104:107], v[168:171], a[180:183]\n" \
"ds_read_b128 v[100:103], v41 offset:40960\n" \
"v_mfma_f32_16x16x32_bf16 a[180:183], v[108:111], v[172:175], a[180:183]\n" \
"v_mfma_f32_16x16x32_bf16 a[176:179], v[104:107], v[160:163], a[176:179]\n" \
"ds_read_b64_tr_b16 v[154:155], v60 offset:37376\n" \
"v_mfma_f32_16x16x32_bf16 a[176:179], v[108:111], v[164:167], a[176:179]\n" \
"v_mfma_f32_16x16x32_bf16 a[208:211], v[112:115], v[160:163], a[208:211]\n" \
"ds_read_b128 v[104:107], v40 offset:43008\n" \
"v_mfma_f32_16x16x32_bf16 a[208:211], v[116:119], v[164:167], a[208:211]\n" \
"v_mfma_f32_16x16x32_bf16 a[212:215], v[112:115], v[168:171], a[212:215]\n" \
"ds_read_b64_tr_b16 v[156:157], v60 offset:49664\n" \
"v_mfma_f32_16x16x32_bf16 a[212:215], v[116:119], v[172:175], a[212:215]\n" \
"v_mfma_f32_16x16x32_bf16 a[216:219], v[112:115], v[176:179], a[216:219]\n" \
"ds_read_b128 v[108:111], v41 offset:43008\n" \
"v_mfma_f32_16x16x32_bf16 a[216:219], v[116:119], v[180:183], a[216:219]\n" \
"v_mfma_f32_16x16x32_bf16 a[220:223], v[112:115], v[184:187], a[220:223]\n" \
"ds_read_b64_tr_b16 v[158:159], v60 offset:53760\n" \
"v_mfma_f32_16x16x32_bf16 a[220:223], v[116:119], v[188:191], a[220:223]\n" \
"v_mfma_f32_16x16x32_bf16 a[252:255], v[120:123], v[184:187], a[252:255]\n" \
"ds_read_b128 v[112:115], v40 offset:45056\n" \
"v_mfma_f32_16x16x32_bf16 a[252:255], v[124:127], v[188:191], a[252:255]\n" \
"v_mfma_f32_16x16x32_bf16 a[248:251], v[120:123], v[176:179], a[248:251]\n" \
"v_mfma_f32_16x16x32_bf16 a[248:251], v[124:127], v[180:183], a[248:251]\n" \
"v_mfma_f32_16x16x32_bf16 a[244:247], v[120:123], v[168:171], a[244:247]\n" \
"ds_read_b128 v[116:119], v41 offset:45056\n" \
"v_mfma_f32_16x16x32_bf16 a[244:247], v[124:127], v[172:175], a[244:247]\n" \
"v_mfma_f32_16x16x32_bf16 a[240:243], v[120:123], v[160:163], a[240:243]\n" \
"v_mfma_f32_16x16x32_bf16 a[240:243], v[124:127], v[164:167], a[240:243]\n" \
"s_waitcnt lgkmcnt(1)\n" \
"v_mfma_f32_16x16x32_bf16 a[0:3], v[128:131], v[64:67], a[0:3]\n" \
"ds_read_b128 v[120:123], v40 offset:47104\n" \
"v_mfma_f32_16x16x32_bf16 a[0:3], v[132:135], v[68:71], a[0:3]\n" \
"v_mfma_f32_16x16x32_bf16 a[4:7], v[136:139], v[64:67], a[4:7]\n" \
"s_add_u32 m0, s74, 65536\n" \
"ds_read_b128 v[124:127], v41 offset:47104\n" \
"v_mfma_f32_16x16x32_bf16 a[4:7], v[140:143], v[68:71], a[4:7]\n" \
"buffer_load_dwordx4 v224, s[48:51], s75 offen offset:0 lds\n" \
"v_mfma_f32_16x16x32_bf16 a[8:11], v[144:147], v[64:67], a[8:11]\n" \
"ds_read_b64_tr_b16 v[160:161], v59 offset:33792\n" \
"v_mfma_f32_16x16x32_bf16 a[8:11], v[148:151], v[68:71], a[8:11]\n" \
"v_mfma_f32_16x16x32_bf16 a[12:15], v[152:155], v[64:67], a[12:15]\n" \
"ds_read_b64_tr_b16 v[162:163], v59 offset:37888\n" \
"v_mfma_f32_16x16x32_bf16 a[12:15], v[156:159], v[68:71], a[12:15]\n" \
"buffer_load_dwordx4 v225, s[48:51], s75 offen offset:1024 lds\n" \
"v_mfma_f32_16x16x32_bf16 a[44:47], v[152:155], v[72:75], a[44:47]\n" \
"ds_read_b64_tr_b16 v[164:165], v59 offset:50176\n" \
"v_mfma_f32_16x16x32_bf16 a[44:47], v[156:159], v[76:79], a[44:47]\n" \
"v_mfma_f32_16x16x32_bf16 a[40:43], v[144:147], v[72:75], a[40:43]\n" \
"ds_read_b64_tr_b16 v[166:167], v59 offset:54272\n" \
"v_mfma_f32_16x16x32_bf16 a[40:43], v[148:151], v[76:79], a[40:43]\n" \
"buffer_load_dwordx4 v226, s[48:51], s75 offen offset:2048 lds\n" \
"v_mfma_f32_16x16x32_bf16 a[36:39], v[136:139], v[72:75], a[36:39]\n" \
"ds_read_b64_tr_b16 v[168:169], v60 offset:33792\n" \
"v_mfma_f32_16x16x32_bf16 a[36:39], v[140:143], v[76:79], a[36:39]\n" \
"v_mfma_f32_16x16x32_bf16 a[32:35], v[128:131], v[72:75], a[32:35]\n" \
"ds_read_b64_tr_b16 v[170:171], v60 offset:37888\n" \
"v_mfma_f32_16x16x32_bf16 a[32:35], v[132:135], v[76:79], a[32:35]\n" \
"buffer_load_dwordx4 v227, s[48:51], s75 offen offset:3072 lds\n" \
"v_mfma_f32_16x16x32_bf16 a[64:67], v[128:131], v[80:83], a[64:67]\n" \
"ds_read_b64_tr_b16 v[172:173], v60 offset:50176\n" \
"v_mfma_f32_16x16x32_bf16 a[64:67], v[132:135], v[84:87], a[64:67]\n" \
"s_xor_b32 s75, s75, 0x400\n" \
"v_mfma_f32_16x16x32_bf16 a[68:71], v[136:139], v[80:83], a[68:71]\n" \
"s_add_u32 m0, s74, 69632\n" \
"ds_read_b64_tr_b16 v[174:175], v60 offset:54272\n" \
"v_mfma_f32_16x16x32_bf16 a[68:71], v[140:143], v[84:87], a[68:71]\n" \
"buffer_load_dwordx4 v224, s[48:51], s77 offen offset:0 lds\n" \
"v_mfma_f32_16x16x32_bf16 a[72:75], v[144:147], v[80:83], a[72:75]\n" \
"ds_read_b64_tr_b16 v[176:177], v59 offset:34304\n" \
"v_mfma_f32_16x16x32_bf16 a[72:75], v[148:151], v[84:87], a[72:75]\n" \
"v_mfma_f32_16x16x32_bf16 a[76:79], v[152:155], v[80:83], a[76:79]\n" \
"ds_read_b64_tr_b16 v[178:179], v59 offset:38400\n" \
"v_mfma_f32_16x16x32_bf16 a[76:79], v[156:159], v[84:87], a[76:79]\n" \
"buffer_load_dwordx4 v225, s[48:51], s77 offen offset:1024 lds\n" \
"v_mfma_f32_16x16x32_bf16 a[108:111], v[152:155], v[88:91], a[108:111]\n" \
"ds_read_b64_tr_b16 v[180:181], v59 offset:50688\n" \
"v_mfma_f32_16x16x32_bf16 a[108:111], v[156:159], v[92:95], a[108:111]\n" \
"v_mfma_f32_16x16x32_bf16 a[104:107], v[144:147], v[88:91], a[104:107]\n" \
"ds_read_b64_tr_b16 v[182:183], v59 offset:54784\n" \
"v_mfma_f32_16x16x32_bf16 a[104:107], v[148:151], v[92:95], a[104:107]\n" \
"buffer_load_dwordx4 v226, s[48:51], s77 offen offset:2048 lds\n" \
"v_mfma_f32_16x16x32_bf16 a[100:103], v[136:139], v[88:91], a[100:103]\n" \
"ds_read_b64_tr_b16 v[184:185], v60 offset:34304\n" \
"v_mfma_f32_16x16x32_bf16 a[100:103], v[140:143], v[92:95], a[100:103]\n" \
"v_mfma_f32_16x16x32_bf16 a[96:99], v[128:131], v[88:91], a[96:99]\n" \
"ds_read_b64_tr_b16 v[186:187], v60 offset:38400\n" \
"v_mfma_f32_16x16x32_bf16 a[96:99], v[132:135], v[92:95], a[96:99]\n" \
"buffer_load_dwordx4 v227, s[48:51], s77 offen offset:3072 lds\n" \
"v_mfma_f32_16x16x32_bf16 a[128:131], v[128:131], v[96:99], a[128:131]\n" \
"ds_read_b64_tr_b16 v[188:189], v60 offset:50688\n" \
"v_mfma_f32_16x16x32_bf16 a[128:131], v[132:135], v[100:103], a[128:131]\n" \
"v_mfma_f32_16x16x32_bf16 a[132:135], v[136:139], v[96:99], a[132:135]\n" \
"s_xor_b32 s77, s77, 0x400\n" \
"ds_read_b64_tr_b16 v[190:191], v60 offset:54784\n" \
"v_mfma_f32_16x16x32_bf16 a[132:135], v[140:143], v[100:103], a[132:135]\n" \
"v_mfma_f32_16x16x32_bf16 a[136:139], v[144:147], v[96:99], a[136:139]\n" \
"v_mfma_f32_16x16x32_bf16 a[136:139], v[148:151], v[100:103], a[136:139]\n" \
"v_mfma_f32_16x16x32_bf16 a[140:143], v[152:155], v[96:99], a[140:143]\n" \
"v_mfma_f32_16x16x32_bf16 a[140:143], v[156:159], v[100:103], a[140:143]\n" \
"v_mfma_f32_16x16x32_bf16 a[172:175], v[152:155], v[104:107], a[172:175]\n" \
"v_mfma_f32_16x16x32_bf16 a[172:175], v[156:159], v[108:111], a[172:175]\n" \
"v_mfma_f32_16x16x32_bf16 a[168:171], v[144:147], v[104:107], a[168:171]\n" \
"v_mfma_f32_16x16x32_bf16 a[168:171], v[148:151], v[108:111], a[168:171]\n" \
"v_mfma_f32_16x16x32_bf16 a[164:167], v[136:139], v[104:107], a[164:167]\n" \
"v_mfma_f32_16x16x32_bf16 a[164:167], v[140:143], v[108:111], a[164:167]\n" \
"v_mfma_f32_16x16x32_bf16 a[160:163], v[128:131], v[104:107], a[160:163]\n" \
"v_mfma_f32_16x16x32_bf16 a[160:163], v[132:135], v[108:111], a[160:163]\n" \
"s_waitcnt lgkmcnt(0)\n" \
"v_mfma_f32_16x16x32_bf16 a[192:195], v[128:131], v[112:115], a[192:195]\n" \
"v_mfma_f32_16x16x32_bf16 a[192:195], v[132:135], v[116:119], a[192:195]\n" \
"v_mfma_f32_16x16x32_bf16 a[196:199], v[136:139], v[112:115], a[196:199]\n" \
"v_mfma_f32_16x16x32_bf16 a[196:199], v[140:143], v[116:119], a[196:199]\n" \
"v_mfma_f32_16x16x32_bf16 a[200:203], v[144:147], v[112:115], a[200:203]\n" \
"v_mfma_f32_16x16x32_bf16 a[200:203], v[148:151], v[116:119], a[200:203]\n" \
"v_mfma_f32_16x16x32_bf16 a[204:207], v[152:155], v[112:115], a[204:207]\n" \
"v_mfma_f32_16x16x32_bf16 a[204:207], v[156:159], v[116:119], a[204:207]\n" \
"v_mfma_f32_16x16x32_bf16 a[236:239], v[152:155], v[120:123], a[236:239]\n" \
"v_mfma_f32_16x16x32_bf16 a[236:239], v[156:159], v[124:127], a[236:239]\n" \
"v_mfma_f32_16x16x32_bf16 a[232:235], v[144:147], v[120:123], a[232:235]\n" \
"v_mfma_f32_16x16x32_bf16 a[232:235], v[148:151], v[124:127], a[232:235]\n" \
"v_mfma_f32_16x16x32_bf16 a[228:231], v[136:139], v[120:123], a[228:231]\n" \
"v_mfma_f32_16x16x32_bf16 a[228:231], v[140:143], v[124:127], a[228:231]\n" \
"v_mfma_f32_16x16x32_bf16 a[224:227], v[128:131], v[120:123], a[224:227]\n" \
"v_mfma_f32_16x16x32_bf16 a[224:227], v[132:135], v[124:127], a[224:227]\n" \
"s_waitcnt vmcnt(10) lgkmcnt(0)\n" \
"s_barrier\n" \
"v_mfma_f32_16x16x32_bf16 a[16:19], v[64:67], v[160:163], a[16:19]\n" \
"s_add_u32 m0, s74, 98304\n" \
"v_mfma_f32_16x16x32_bf16 a[16:19], v[68:71], v[164:167], a[16:19]\n" \
"buffer_load_dwordx4 v224, s[48:51], s75 offen offset:0 lds\n" \
"v_mfma_f32_16x16x32_bf16 a[20:23], v[64:67], v[168:171], a[20:23]\n" \
"ds_read_b64_tr_b16 v[0:1], v232 offset:0\n" \
"v_mfma_f32_16x16x32_bf16 a[20:23], v[68:71], v[172:175], a[20:23]\n" \
"v_mfma_f32_16x16x32_bf16 a[24:27], v[64:67], v[176:179], a[24:27]\n" \
"ds_read_b64_tr_b16 v[2:3], v232 offset:2048\n" \
"v_mfma_f32_16x16x32_bf16 a[24:27], v[68:71], v[180:183], a[24:27]\n" \
"buffer_load_dwordx4 v225, s[48:51], s75 offen offset:1024 lds\n" \
"v_mfma_f32_16x16x32_bf16 a[28:31], v[64:67], v[184:187], a[28:31]\n" \
"ds_read_b64_tr_b16 v[4:5], v232 offset:8192\n" \
"v_mfma_f32_16x16x32_bf16 a[28:31], v[68:71], v[188:191], a[28:31]\n" \
"v_mfma_f32_16x16x32_bf16 a[60:63], v[72:75], v[184:187], a[60:63]\n" \
"ds_read_b64_tr_b16 v[6:7], v232 offset:10240\n" \
"v_accvgpr_read_b32 v243, a192\n" \
"v_mfma_f32_16x16x32_bf16 a[60:63], v[76:79], v[188:191], a[60:63]\n" \
"buffer_load_dwordx4 v226, s[48:51], s75 offen offset:2048 lds\n" \
"v_mfma_f32_16x16x32_bf16 a[56:59], v[72:75], v[176:179], a[56:59]\n" \
"ds_read_b64_tr_b16 v[8:9], v233 offset:0\n" \
"v_accvgpr_read_b32 v252, a193\n" \
"v_mfma_f32_16x16x32_bf16 a[56:59], v[76:79], v[180:183], a[56:59]\n" \
"v_accvgpr_read_b32 v253, a194\n" \
"v_accvgpr_read_b32 v254, a195\n" \
"v_mfma_f32_16x16x32_bf16 a[52:55], v[72:75], v[168:171], a[52:55]\n" \
"ds_read_b64_tr_b16 v[10:11], v233 offset:2048\n" \
"v_mul_f32 v243, s76, v243\n" \
"v_mfma_f32_16x16x32_bf16 a[52:55], v[76:79], v[172:175], a[52:55]\n" \
"buffer_load_dwordx4 v227, s[48:51], s75 offen offset:3072 lds\n" \
"v_mfma_f32_16x16x32_bf16 a[48:51], v[72:75], v[160:163], a[48:51]\n" \
"ds_read_b64_tr_b16 v[12:13], v233 offset:8192\n" \
"v_mul_f32 v252, s76, v252\n" \
"v_mfma_f32_16x16x32_bf16 a[48:51], v[76:79], v[164:167], a[48:51]\n" \
"s_xor_b32 s75, s75, 0x500\n" \
"v_mul_f32 v253, s76, v253\n" \
"v_mul_f32 v254, s76, v254\n" \
"v_mfma_f32_16x16x32_bf16 a[80:83], v[80:83], v[160:163], a[80:83]\n" \
"s_add_u32 m0, s74, 102400\n" \
"ds_read_b64_tr_b16 v[14:15], v233 offset:10240\n" \
"v_cvt_pk_bf16_f32 v243, v243, v252\n" \
"v_mfma_f32_16x16x32_bf16 a[80:83], v[84:87], v[164:167], a[80:83]\n" \
"buffer_load_dwordx4 v224, s[48:51], s77 offen offset:0 lds\n" \
"v_mfma_f32_16x16x32_bf16 a[84:87], v[80:83], v[168:171], a[84:87]\n" \
"ds_read_b64_tr_b16 v[16:17], v232 offset:512\n" \
"v_cvt_pk_bf16_f32 v253, v253, v254\n" \
"v_mfma_f32_16x16x32_bf16 a[84:87], v[84:87], v[172:175], a[84:87]\n" \
"v_accvgpr_write_b32 a192, v243\n" \
"v_accvgpr_write_b32 a193, v253\n" \
"v_mfma_f32_16x16x32_bf16 a[88:91], v[80:83], v[176:179], a[88:91]\n" \
"ds_read_b64_tr_b16 v[18:19], v232 offset:2560\n" \
"v_accvgpr_read_b32 v243, a196\n" \
"v_mfma_f32_16x16x32_bf16 a[88:91], v[84:87], v[180:183], a[88:91]\n" \
"buffer_load_dwordx4 v225, s[48:51], s77 offen offset:1024 lds\n" \
"v_mfma_f32_16x16x32_bf16 a[92:95], v[80:83], v[184:187], a[92:95]\n" \
"ds_read_b64_tr_b16 v[20:21], v232 offset:8704\n" \
"v_accvgpr_read_b32 v252, a197\n" \
"v_mfma_f32_16x16x32_bf16 a[92:95], v[84:87], v[188:191], a[92:95]\n" \
"v_accvgpr_read_b32 v253, a198\n" \
"v_accvgpr_read_b32 v254, a199\n" \
"v_mfma_f32_16x16x32_bf16 a[124:127], v[88:91], v[184:187], a[124:127]\n" \
"ds_read_b64_tr_b16 v[22:23], v232 offset:10752\n" \
"v_mul_f32 v243, s76, v243\n" \
"v_mfma_f32_16x16x32_bf16 a[124:127], v[92:95], v[188:191], a[124:127]\n" \
"buffer_load_dwordx4 v226, s[48:51], s77 offen offset:2048 lds\n" \
"v_mfma_f32_16x16x32_bf16 a[120:123], v[88:91], v[176:179], a[120:123]\n" \
"ds_read_b64_tr_b16 v[24:25], v233 offset:512\n" \
"v_mul_f32 v252, s76, v252\n" \
"v_mfma_f32_16x16x32_bf16 a[120:123], v[92:95], v[180:183], a[120:123]\n" \
"v_mul_f32 v253, s76, v253\n" \
"v_mul_f32 v254, s76, v254\n" \
"v_mfma_f32_16x16x32_bf16 a[116:119], v[88:91], v[168:171], a[116:119]\n" \
"ds_read_b64_tr_b16 v[26:27], v233 offset:2560\n" \
"v_cvt_pk_bf16_f32 v243, v243, v252\n" \
"v_mfma_f32_16x16x32_bf16 a[116:119], v[92:95], v[172:175], a[116:119]\n" \
"buffer_load_dwordx4 v227, s[48:51], s77 offen offset:3072 lds\n" \
"v_mfma_f32_16x16x32_bf16 a[112:115], v[88:91], v[160:163], a[112:115]\n" \
"ds_read_b64_tr_b16 v[28:29], v233 offset:8704\n" \
"v_cvt_pk_bf16_f32 v253, v253, v254\n" \
"v_mfma_f32_16x16x32_bf16 a[112:115], v[92:95], v[164:167], a[112:115]\n" \
"s_xor_b32 s77, s77, 0x500\n" \
"v_accvgpr_write_b32 a194, v243\n" \
"v_accvgpr_write_b32 a195, v253\n" \
"v_mfma_f32_16x16x32_bf16 a[144:147], v[96:99], v[160:163], a[144:147]\n" \
"s_add_u32 m0, s79, 32768\n" \
"ds_read_b64_tr_b16 v[30:31], v233 offset:10752\n" \
"v_accvgpr_read_b32 v243, a224\n" \
"v_mfma_f32_16x16x32_bf16 a[144:147], v[100:103], v[164:167], a[144:147]\n" \
"buffer_load_dwordx4 v228, s[52:55], s78 offen offset:0 lds\n" \
"v_mfma_f32_16x16x32_bf16 a[148:151], v[96:99], v[168:171], a[148:151]\n" \
"ds_read_b128 v[192:195], v250 offset:0\n" \
"v_accvgpr_read_b32 v252, a225\n" \
"v_mfma_f32_16x16x32_bf16 a[148:151], v[100:103], v[172:175], a[148:151]\n" \
"v_accvgpr_read_b32 v253, a226\n" \
"v_accvgpr_read_b32 v254, a227\n" \
"v_mfma_f32_16x16x32_bf16 a[152:155], v[96:99], v[176:179], a[152:155]\n" \
"ds_read_b128 v[196:199], v251 offset:0\n" \
"v_mul_f32 v243, s76, v243\n" \
"v_mfma_f32_16x16x32_bf16 a[152:155], v[100:103], v[180:183], a[152:155]\n" \
"buffer_load_dwordx4 v229, s[52:55], s78 offen offset:1024 lds\n" \
"v_mfma_f32_16x16x32_bf16 a[156:159], v[96:99], v[184:187], a[156:159]\n" \
"ds_read_b128 v[200:203], v250 offset:2048\n" \
"v_mul_f32 v252, s76, v252\n" \
"v_mfma_f32_16x16x32_bf16 a[156:159], v[100:103], v[188:191], a[156:159]\n" \
"v_mul_f32 v253, s76, v253\n" \
"v_mul_f32 v254, s76, v254\n" \
"v_mfma_f32_16x16x32_bf16 a[188:191], v[104:107], v[184:187], a[188:191]\n" \
"s_xor_b32 s78, s78, 0x500000\n" \
"ds_read_b128 v[204:207], v251 offset:2048\n" \
"v_cvt_pk_bf16_f32 v243, v243, v252\n" \
"v_mfma_f32_16x16x32_bf16 a[188:191], v[108:111], v[188:191], a[188:191]\n" \
"buffer_load_dwordx4 v228, s[52:55], s89 offen offset:2048 lds\n" \
"v_mfma_f32_16x16x32_bf16 a[184:187], v[104:107], v[176:179], a[184:187]\n" \
"ds_read_b128 v[208:211], v250 offset:4096\n" \
"v_cvt_pk_bf16_f32 v253, v253, v254\n" \
"v_mfma_f32_16x16x32_bf16 a[184:187], v[108:111], v[180:183], a[184:187]\n" \
"v_accvgpr_write_b32 a224, v243\n" \
"v_accvgpr_write_b32 a225, v253\n" \
"v_mfma_f32_16x16x32_bf16 a[180:183], v[104:107], v[168:171], a[180:183]\n" \
"ds_read_b128 v[212:215], v251 offset:4096\n" \
"v_accvgpr_read_b32 v243, a228\n" \
"v_mfma_f32_16x16x32_bf16 a[180:183], v[108:111], v[172:175], a[180:183]\n" \
"buffer_load_dwordx4 v229, s[52:55], s89 offen offset:3072 lds\n" \
"v_mfma_f32_16x16x32_bf16 a[176:179], v[104:107], v[160:163], a[176:179]\n" \
"v_accvgpr_read_b32 v252, a229\n" \
"v_accvgpr_read_b32 v253, a230\n" \
"v_mfma_f32_16x16x32_bf16 a[176:179], v[108:111], v[164:167], a[176:179]\n" \
"s_xor_b32 s89, s89, 0x500000\n" \
"v_accvgpr_read_b32 v254, a231\n" \
"v_mul_f32 v243, s76, v243\n" \
"v_mfma_f32_16x16x32_bf16 a[208:211], v[112:115], v[160:163], a[208:211]\n" \
"s_add_u32 m0, s79, 49152\n" \
"v_mul_f32 v252, s76, v252\n" \
"v_mul_f32 v253, s76, v253\n" \
"v_mfma_f32_16x16x32_bf16 a[208:211], v[116:119], v[164:167], a[208:211]\n" \
"buffer_load_dwordx4 v228, s[52:55], s78 offen offset:0 lds\n" \
"v_mfma_f32_16x16x32_bf16 a[212:215], v[112:115], v[168:171], a[212:215]\n" \
"v_mul_f32 v254, s76, v254\n" \
"v_cvt_pk_bf16_f32 v243, v243, v252\n" \
"v_mfma_f32_16x16x32_bf16 a[212:215], v[116:119], v[172:175], a[212:215]\n" \
"v_cvt_pk_bf16_f32 v253, v253, v254\n" \
"v_accvgpr_write_b32 a226, v243\n" \
"v_mfma_f32_16x16x32_bf16 a[216:219], v[112:115], v[176:179], a[216:219]\n" \
"v_accvgpr_write_b32 a227, v253\n" \
"v_accvgpr_read_b32 v243, a200\n" \
"v_mfma_f32_16x16x32_bf16 a[216:219], v[116:119], v[180:183], a[216:219]\n" \
"buffer_load_dwordx4 v229, s[52:55], s78 offen offset:1024 lds\n" \
"v_mfma_f32_16x16x32_bf16 a[220:223], v[112:115], v[184:187], a[220:223]\n" \
"v_accvgpr_read_b32 v252, a201\n" \
"v_accvgpr_read_b32 v253, a202\n" \
"v_mfma_f32_16x16x32_bf16 a[220:223], v[116:119], v[188:191], a[220:223]\n" \
"v_accvgpr_read_b32 v254, a203\n" \
"v_mul_f32 v243, s76, v243\n" \
"v_mfma_f32_16x16x32_bf16 a[252:255], v[120:123], v[184:187], a[252:255]\n" \
"s_xor_b32 s78, s78, 0x180000\n" \
"v_mul_f32 v252, s76, v252\n" \
"v_mul_f32 v253, s76, v253\n" \
"v_mfma_f32_16x16x32_bf16 a[252:255], v[124:127], v[188:191], a[252:255]\n" \
"buffer_load_dwordx4 v228, s[52:55], s89 offen offset:2048 lds\n" \
"v_mfma_f32_16x16x32_bf16 a[248:251], v[120:123], v[176:179], a[248:251]\n" \
"v_mul_f32 v254, s76, v254\n" \
"v_cvt_pk_bf16_f32 v243, v243, v252\n" \
"v_mfma_f32_16x16x32_bf16 a[248:251], v[124:127], v[180:183], a[248:251]\n" \
"v_cvt_pk_bf16_f32 v253, v253, v254\n" \
"v_accvgpr_write_b32 a200, v243\n" \
"v_mfma_f32_16x16x32_bf16 a[244:247], v[120:123], v[168:171], a[244:247]\n" \
"v_accvgpr_write_b32 a201, v253\n" \
"v_accvgpr_read_b32 v243, a204\n" \
"v_mfma_f32_16x16x32_bf16 a[244:247], v[124:127], v[172:175], a[244:247]\n" \
"buffer_load_dwordx4 v229, s[52:55], s89 offen offset:3072 lds\n" \
"v_mfma_f32_16x16x32_bf16 a[240:243], v[120:123], v[160:163], a[240:243]\n" \
"s_waitcnt lgkmcnt(0)\n" \
"v_accvgpr_read_b32 v252, a205\n" \
"v_accvgpr_read_b32 v253, a206\n" \
"v_mfma_f32_16x16x32_bf16 a[240:243], v[124:127], v[164:167], a[240:243]\n" \
"s_xor_b32 s89, s89, 0x180000\n" \
"v_accvgpr_read_b32 v254, a207\n" \
"v_mul_f32 v243, s76, v243\n" \
"v_mfma_f32_16x16x32_bf16 v[64:67], v[0:3], v[192:195], 0\n" \
"ds_read_b128 v[216:219], v250 offset:6144\n" \
"v_mul_f32 v252, s76, v252\n" \
"v_mfma_f32_16x16x32_bf16 v[64:67], v[4:7], v[196:199], v[64:67]\n" \
"s_add_u32 m0, s74, 131072\n" \
"ds_read_b128 v[220:223], v251 offset:6144\n" \
"v_mul_f32 v253, s76, v253\n" \
"v_mfma_f32_16x16x32_bf16 v[68:71], v[8:11], v[192:195], 0\n" \
"ds_read_b64_tr_b16 v[32:33], v232 offset:1024\n" \
"v_mul_f32 v254, s76, v254\n" \
"v_mfma_f32_16x16x32_bf16 v[68:71], v[12:15], v[196:199], v[68:71]\n" \
"buffer_load_dwordx4 v224, s[48:51], s75 offen offset:0 lds\n" \
"v_mfma_f32_16x16x32_bf16 v[72:75], v[16:19], v[192:195], 0\n" \
"ds_read_b64_tr_b16 v[34:35], v232 offset:3072\n" \
"v_cvt_pk_bf16_f32 v243, v243, v252\n" \
"v_mfma_f32_16x16x32_bf16 v[72:75], v[20:23], v[196:199], v[72:75]\n" \
"ds_read_b64_tr_b16 v[36:37], v232 offset:9216\n" \
"v_cvt_pk_bf16_f32 v253, v253, v254\n" \
"v_mfma_f32_16x16x32_bf16 v[76:79], v[24:27], v[192:195], 0\n" \
"ds_read_b64_tr_b16 v[38:39], v232 offset:11264\n" \
"v_accvgpr_write_b32 a202, v243\n" \
"v_mfma_f32_16x16x32_bf16 v[76:79], v[28:31], v[196:199], v[76:79]\n" \
"buffer_load_dwordx4 v225, s[48:51], s75 offen offset:1024 lds\n" \
"v_mfma_f32_16x16x32_bf16 v[108:111], v[24:27], v[200:203], 0\n" \
"ds_read_b64_tr_b16 v[40:41], v233 offset:1024\n" \
"v_accvgpr_write_b32 a203, v253\n" \
"v_mfma_f32_16x16x32_bf16 v[108:111], v[28:31], v[204:207], v[108:111]\n" \
"ds_read_b64_tr_b16 v[42:43], v233 offset:3072\n" \
"v_accvgpr_read_b32 v243, a232\n" \
"v_mfma_f32_16x16x32_bf16 v[104:107], v[16:19], v[200:203], 0\n" \
"ds_read_b64_tr_b16 v[44:45], v233 offset:9216\n" \
"v_accvgpr_read_b32 v252, a233\n" \
"v_mfma_f32_16x16x32_bf16 v[104:107], v[20:23], v[204:207], v[104:107]\n" \
"buffer_load_dwordx4 v226, s[48:51], s75 offen offset:2048 lds\n" \
"v_mfma_f32_16x16x32_bf16 v[100:103], v[8:11], v[200:203], 0\n" \
"ds_read_b64_tr_b16 v[46:47], v233 offset:11264\n" \
"v_accvgpr_read_b32 v253, a234\n" \
"v_mfma_f32_16x16x32_bf16 v[100:103], v[12:15], v[204:207], v[100:103]\n" \
"ds_read_b64_tr_b16 v[48:49], v232 offset:1536\n" \
"v_accvgpr_read_b32 v254, a235\n" \
"v_mfma_f32_16x16x32_bf16 v[96:99], v[0:3], v[200:203], 0\n" \
"ds_read_b64_tr_b16 v[50:51], v232 offset:3584\n" \
"v_mul_f32 v243, s76, v243\n" \
"v_mfma_f32_16x16x32_bf16 v[96:99], v[4:7], v[204:207], v[96:99]\n" \
"Lpg2193_%=:\n" \
"Lst0x3_%=:\n" \
"s_cbranch_execz Lpg4417_%=\n" \
"buffer_load_dwordx4 v227, s[48:51], s75 offen offset:3072 lds\n" \
"v_mfma_f32_16x16x32_bf16 v[128:131], v[0:3], v[208:211], 0\n" \
"s_xor_b32 s75, s75, 0x180\n" \
"ds_read_b64_tr_b16 v[52:53], v232 offset:9728\n" \
"v_mul_f32 v252, s76, v252\n" \
"v_mfma_f32_16x16x32_bf16 v[128:131], v[4:7], v[212:215], v[128:131]\n" \
"s_add_u32 m0, s74, 135168\n" \
"ds_read_b64_tr_b16 v[54:55], v232 offset:11776\n" \
"v_mul_f32 v253, s76, v253\n" \
"v_mfma_f32_16x16x32_bf16 v[132:135], v[8:11], v[208:211], 0\n" \
"ds_read_b64_tr_b16 v[56:57], v233 offset:1536\n" \
"v_mul_f32 v254, s76, v254\n" \
"v_mfma_f32_16x16x32_bf16 v[132:135], v[12:15], v[212:215], v[132:135]\n" \
"buffer_load_dwordx4 v224, s[48:51], s77 offen offset:0 lds\n" \
"v_mfma_f32_16x16x32_bf16 v[136:139], v[16:19], v[208:211], 0\n" \
"ds_read_b64_tr_b16 v[58:59], v233 offset:3584\n" \
"v_cvt_pk_bf16_f32 v243, v243, v252\n" \
"v_mfma_f32_16x16x32_bf16 v[136:139], v[20:23], v[212:215], v[136:139]\n" \
"ds_read_b64_tr_b16 v[60:61], v233 offset:9728\n" \
"v_cvt_pk_bf16_f32 v253, v253, v254\n" \
"v_mfma_f32_16x16x32_bf16 v[140:143], v[24:27], v[208:211], 0\n" \
"s_waitcnt lgkmcnt(3)\n" \
"ds_read_b64_tr_b16 v[62:63], v233 offset:11776\n" \
"v_accvgpr_write_b32 a232, v243\n" \
"v_mfma_f32_16x16x32_bf16 v[140:143], v[28:31], v[212:215], v[140:143]\n" \
"buffer_load_dwordx4 v225, s[48:51], s77 offen offset:1024 lds\n" \
"v_mfma_f32_16x16x32_bf16 v[172:175], v[24:27], v[216:219], 0\n" \
"v_accvgpr_write_b32 a233, v253\n" \
"v_accvgpr_read_b32 v243, a236\n" \
"v_mfma_f32_16x16x32_bf16 v[172:175], v[28:31], v[220:223], v[172:175]\n" \
"v_accvgpr_read_b32 v252, a237\n" \
"v_accvgpr_read_b32 v253, a238\n" \
"v_mfma_f32_16x16x32_bf16 v[168:171], v[16:19], v[216:219], 0\n" \
"v_accvgpr_read_b32 v254, a239\n" \
"v_mul_f32 v243, s76, v243\n" \
"v_mfma_f32_16x16x32_bf16 v[168:171], v[20:23], v[220:223], v[168:171]\n" \
"buffer_load_dwordx4 v226, s[48:51], s77 offen offset:2048 lds\n" \
"v_mfma_f32_16x16x32_bf16 v[164:167], v[8:11], v[216:219], 0\n" \
"v_mul_f32 v252, s76, v252\n" \
"v_mul_f32 v253, s76, v253\n" \
"v_mfma_f32_16x16x32_bf16 v[164:167], v[12:15], v[220:223], v[164:167]\n" \
"buffer_load_dwordx4 v227, s[48:51], s77 offen offset:3072 lds\n" \
"v_mfma_f32_16x16x32_bf16 v[160:163], v[0:3], v[216:219], 0\n" \
"v_mul_f32 v254, s76, v254\n" \
"v_cvt_pk_bf16_f32 v243, v243, v252\n" \
"v_mfma_f32_16x16x32_bf16 v[160:163], v[4:7], v[220:223], v[160:163]\n" \
"s_waitcnt vmcnt(42) lgkmcnt(0)\n" \
"s_barrier\n" \
"v_mfma_f32_16x16x32_bf16 v[80:83], v[32:35], v[192:195], 0\n" \
"s_xor_b32 s77, s77, 0x180\n" \
"ds_read_b64_tr_b16 v[0:1], v232 offset:16384\n" \
"v_cvt_pk_bf16_f32 v253, v253, v254\n" \
"v_mfma_f32_16x16x32_bf16 v[80:83], v[36:39], v[196:199], v[80:83]\n" \
"ds_read_b64_tr_b16 v[2:3], v232 offset:18432\n" \
"v_accvgpr_write_b32 a234, v243\n" \
"v_mfma_f32_16x16x32_bf16 v[84:87], v[40:43], v[192:195], 0\n" \
"s_add_u32 m0, s79, 0\n" \
"ds_read_b64_tr_b16 v[4:5], v232 offset:24576\n" \
"v_accvgpr_write_b32 a235, v253\n" \
"v_mfma_f32_16x16x32_bf16 v[84:87], v[44:47], v[196:199], v[84:87]\n" \
"buffer_load_dwordx4 v228, s[52:55], s78 offen offset:0 lds\n" \
"v_mfma_f32_16x16x32_bf16 v[88:91], v[48:51], v[192:195], 0\n" \
"ds_read_b64_tr_b16 v[6:7], v232 offset:26624\n" \
"v_accvgpr_read_b32 v243, a128\n" \
"v_mfma_f32_16x16x32_bf16 v[88:91], v[52:55], v[196:199], v[88:91]\n" \
"ds_read_b64_tr_b16 v[8:9], v233 offset:16384\n" \
"v_accvgpr_read_b32 v252, a129\n" \
"v_mfma_f32_16x16x32_bf16 v[92:95], v[56:59], v[192:195], 0\n" \
"ds_read_b64_tr_b16 v[10:11], v233 offset:18432\n" \
"v_accvgpr_read_b32 v253, a130\n" \
"v_mfma_f32_16x16x32_bf16 v[92:95], v[60:63], v[196:199], v[92:95]\n" \
"buffer_load_dwordx4 v229, s[52:55], s78 offen offset:1024 lds\n" \
"v_mfma_f32_16x16x32_bf16 v[124:127], v[56:59], v[200:203], 0\n" \
"s_waitcnt vmcnt(32)\n" \
"ds_read_b128 v[192:195], v230 offset:0\n" \
"v_accvgpr_read_b32 v254, a131\n" \
"v_mfma_f32_16x16x32_bf16 v[124:127], v[60:63], v[204:207], v[124:127]\n" \
"ds_read_b128 v[196:199], v231 offset:0\n" \
"v_mul_f32 v243, s76, v243\n" \
"v_mfma_f32_16x16x32_bf16 v[120:123], v[48:51], v[200:203], 0\n" \
"s_xor_b32 s78, s78, 0x300000\n" \
"ds_read_b64_tr_b16 v[12:13], v233 offset:24576\n" \
"v_mul_f32 v252, s76, v252\n" \
"v_mfma_f32_16x16x32_bf16 v[120:123], v[52:55], v[204:207], v[120:123]\n" \
"buffer_load_dwordx4 v228, s[52:55], s89 offen offset:2048 lds\n" \
"v_mfma_f32_16x16x32_bf16 v[116:119], v[40:43], v[200:203], 0\n" \
"ds_read_b64_tr_b16 v[14:15], v233 offset:26624\n" \
"v_mul_f32 v253, s76, v253\n" \
"v_mfma_f32_16x16x32_bf16 v[116:119], v[44:47], v[204:207], v[116:119]\n" \
"ds_read_b64_tr_b16 v[16:17], v232 offset:16896\n" \
"v_mul_f32 v254, s76, v254\n" \
"v_mfma_f32_16x16x32_bf16 v[112:115], v[32:35], v[200:203], 0\n" \
"ds_read_b64_tr_b16 v[18:19], v232 offset:18944\n" \
"v_cvt_pk_bf16_f32 v243, v243, v252\n" \
"v_mfma_f32_16x16x32_bf16 v[112:115], v[36:39], v[204:207], v[112:115]\n" \
"buffer_load_dwordx4 v229, s[52:55], s89 offen offset:3072 lds\n" \
"v_mfma_f32_16x16x32_bf16 v[144:147], v[32:35], v[208:211], 0\n" \
"s_waitcnt vmcnt(32)\n" \
"s_xor_b32 s89, s89, 0x300000\n" \
"ds_read_b128 v[200:203], v230 offset:2048\n" \
"v_mfma_f32_16x16x32_bf16 v[144:147], v[36:39], v[212:215], v[144:147]\n" \
"s_add_u32 m0, s74, 65536\n" \
"ds_read_b128 v[204:207], v231 offset:2048\n" \
"v_cvt_pk_bf16_f32 v253, v253, v254\n" \
"v_mfma_f32_16x16x32_bf16 v[148:151], v[40:43], v[208:211], 0\n" \
"s_waitcnt lgkmcnt(6)\n" \
"ds_read_b64_tr_b16 v[20:21], v232 offset:25088\n" \
"v_accvgpr_write_b32 a128, v243\n" \
"v_mfma_f32_16x16x32_bf16 v[148:151], v[44:47], v[212:215], v[148:151]\n" \
"buffer_load_dwordx4 v224, s[48:51], s75 offen offset:0 lds\n" \
"v_mfma_f32_16x16x32_bf16 v[152:155], v[48:51], v[208:211], 0\n" \
"ds_read_b64_tr_b16 v[22:23], v232 offset:27136\n" \
"v_accvgpr_write_b32 a129, v253\n" \
"v_mfma_f32_16x16x32_bf16 v[152:155], v[52:55], v[212:215], v[152:155]\n" \
"ds_read_b64_tr_b16 v[24:25], v233 offset:16896\n" \
"v_accvgpr_read_b32 v243, a132\n" \
"v_mfma_f32_16x16x32_bf16 v[156:159], v[56:59], v[208:211], 0\n" \
"ds_read_b64_tr_b16 v[26:27], v233 offset:18944\n" \
"v_accvgpr_read_b32 v252, a133\n" \
"v_mfma_f32_16x16x32_bf16 v[156:159], v[60:63], v[212:215], v[156:159]\n" \
"buffer_load_dwordx4 v225, s[48:51], s75 offen offset:1024 lds\n" \
"v_mfma_f32_16x16x32_bf16 v[188:191], v[56:59], v[216:219], 0\n" \
"s_waitcnt vmcnt(32)\n" \
"ds_read_b128 v[208:211], v230 offset:4096\n" \
"v_accvgpr_read_b32 v253, a134\n" \
"v_mfma_f32_16x16x32_bf16 v[188:191], v[60:63], v[220:223], v[188:191]\n" \
"ds_read_b128 v[212:215], v231 offset:4096\n" \
"v_accvgpr_read_b32 v254, a135\n" \
"v_mfma_f32_16x16x32_bf16 v[184:187], v[48:51], v[216:219], 0\n" \
"s_waitcnt lgkmcnt(6)\n" \
"ds_read_b64_tr_b16 v[28:29], v233 offset:25088\n" \
"v_mul_f32 v243, s76, v243\n" \
"v_mfma_f32_16x16x32_bf16 v[184:187], v[52:55], v[220:223], v[184:187]\n" \
"buffer_load_dwordx4 v226, s[48:51], s75 offen offset:2048 lds\n" \
"v_mfma_f32_16x16x32_bf16 v[180:183], v[40:43], v[216:219], 0\n" \
"ds_read_b64_tr_b16 v[30:31], v233 offset:27136\n" \
"v_mul_f32 v252, s76, v252\n" \
"v_mfma_f32_16x16x32_bf16 v[180:183], v[44:47], v[220:223], v[180:183]\n" \
"v_mul_f32 v253, s76, v253\n" \
"v_mul_f32 v254, s76, v254\n" \
"v_mfma_f32_16x16x32_bf16 v[176:179], v[32:35], v[216:219], 0\n" \
"v_cvt_pk_bf16_f32 v243, v243, v252\n" \
"v_cvt_pk_bf16_f32 v253, v253, v254\n" \
"v_mfma_f32_16x16x32_bf16 v[176:179], v[36:39], v[220:223], v[176:179]\n" \
"buffer_load_dwordx4 v227, s[48:51], s75 offen offset:3072 lds\n" \
"v_mfma_f32_16x16x32_bf16 v[64:67], v[0:3], v[192:195], v[64:67]\n" \
"s_waitcnt vmcnt(32)\n" \
"s_xor_b32 s75, s75, 0x300\n" \
"ds_read_b128 v[216:219], v230 offset:6144\n" \
"v_mfma_f32_16x16x32_bf16 v[64:67], v[4:7], v[196:199], v[64:67]\n" \
"s_add_u32 m0, s74, 69632\n" \
"ds_read_b128 v[220:223], v231 offset:6144\n" \
"v_accvgpr_write_b32 a130, v243\n" \
"v_mfma_f32_16x16x32_bf16 v[68:71], v[8:11], v[192:195], v[68:71]\n" \
"s_waitcnt lgkmcnt(4)\n" \
"ds_read_b64_tr_b16 v[32:33], v232 offset:17408\n" \
"v_accvgpr_write_b32 a131, v253\n" \
"v_mfma_f32_16x16x32_bf16 v[68:71], v[12:15], v[196:199], v[68:71]\n" \
"buffer_load_dwordx4 v224, s[48:51], s77 offen offset:0 lds\n" \
"v_mfma_f32_16x16x32_bf16 v[72:75], v[16:19], v[192:195], v[72:75]\n" \
"ds_read_b64_tr_b16 v[34:35], v232 offset:19456\n" \
"v_accvgpr_read_b32 v243, a160\n" \
"v_mfma_f32_16x16x32_bf16 v[72:75], v[20:23], v[196:199], v[72:75]\n" \
"s_waitcnt lgkmcnt(3)\n" \
"ds_read_b64_tr_b16 v[36:37], v232 offset:25600\n" \
"v_accvgpr_read_b32 v252, a161\n" \
"v_mfma_f32_16x16x32_bf16 v[76:79], v[24:27], v[192:195], v[76:79]\n" \
"ds_read_b64_tr_b16 v[38:39], v232 offset:27648\n" \
"v_accvgpr_read_b32 v253, a162\n" \
"v_mfma_f32_16x16x32_bf16 v[76:79], v[28:31], v[196:199], v[76:79]\n" \
"buffer_load_dwordx4 v225, s[48:51], s77 offen offset:1024 lds\n" \
"v_mfma_f32_16x16x32_bf16 v[108:111], v[24:27], v[200:203], v[108:111]\n" \
"ds_read_b64_tr_b16 v[40:41], v233 offset:17408\n" \
"v_accvgpr_read_b32 v254, a163\n" \
"v_mfma_f32_16x16x32_bf16 v[108:111], v[28:31], v[204:207], v[108:111]\n" \
"ds_read_b64_tr_b16 v[42:43], v233 offset:19456\n" \
"v_mul_f32 v243, s76, v243\n" \
"v_mfma_f32_16x16x32_bf16 v[104:107], v[16:19], v[200:203], v[104:107]\n" \
"s_waitcnt lgkmcnt(6)\n" \
"ds_read_b64_tr_b16 v[44:45], v233 offset:25600\n" \
"v_mul_f32 v252, s76, v252\n" \
"v_mfma_f32_16x16x32_bf16 v[104:107], v[20:23], v[204:207], v[104:107]\n" \
"buffer_load_dwordx4 v226, s[48:51], s77 offen offset:2048 lds\n" \
"v_mfma_f32_16x16x32_bf16 v[100:103], v[8:11], v[200:203], v[100:103]\n" \
"ds_read_b64_tr_b16 v[46:47], v233 offset:27648\n" \
"v_mul_f32 v253, s76, v253\n" \
"v_mfma_f32_16x16x32_bf16 v[100:103], v[12:15], v[204:207], v[100:103]\n" \
"ds_read_b64_tr_b16 v[48:49], v232 offset:17920\n" \
"v_mul_f32 v254, s76, v254\n" \
"v_mfma_f32_16x16x32_bf16 v[96:99], v[0:3], v[200:203], v[96:99]\n" \
"ds_read_b64_tr_b16 v[50:51], v232 offset:19968\n" \
"v_cvt_pk_bf16_f32 v243, v243, v252\n" \
"v_mfma_f32_16x16x32_bf16 v[96:99], v[4:7], v[204:207], v[96:99]\n" \
"buffer_load_dwordx4 v227, s[48:51], s77 offen offset:3072 lds\n" \
"v_mfma_f32_16x16x32_bf16 v[128:131], v[0:3], v[208:211], v[128:131]\n" \
"ds_read_b64_tr_b16 v[52:53], v232 offset:26112\n" \
"v_cvt_pk_bf16_f32 v253, v253, v254\n" \
"v_mfma_f32_16x16x32_bf16 v[128:131], v[4:7], v[212:215], v[128:131]\n" \
"ds_read_b64_tr_b16 v[54:55], v232 offset:28160\n" \
"v_accvgpr_write_b32 a160, v243\n" \
"v_mfma_f32_16x16x32_bf16 v[132:135], v[8:11], v[208:211], v[132:135]\n" \
"s_xor_b32 s77, s77, 0x300\n" \
"s_add_u32 s93, s87, 8192\n" \
"ds_read_b64_tr_b16 v[56:57], v233 offset:17920\n" \
"v_mfma_f32_16x16x32_bf16 v[132:135], v[12:15], v[212:215], v[132:135]\n" \
"buffer_store_dwordx4 a[192:195], v236, s[56:59], s93 offen offset:0 sc1\n" \
"v_mfma_f32_16x16x32_bf16 v[136:139], v[16:19], v[208:211], v[136:139]\n" \
"ds_read_b64_tr_b16 v[58:59], v233 offset:19968\n" \
"v_accvgpr_write_b32 a161, v253\n" \
"v_mfma_f32_16x16x32_bf16 v[136:139], v[20:23], v[212:215], v[136:139]\n" \
"ds_read_b64_tr_b16 v[60:61], v233 offset:26112\n" \
"v_accvgpr_read_b32 v243, a164\n" \
"v_mfma_f32_16x16x32_bf16 v[140:143], v[24:27], v[208:211], v[140:143]\n" \
"ds_read_b64_tr_b16 v[62:63], v233 offset:28160\n" \
"v_accvgpr_read_b32 v252, a165\n" \
"v_mfma_f32_16x16x32_bf16 v[140:143], v[28:31], v[212:215], v[140:143]\n" \
"v_accvgpr_read_b32 v253, a166\n" \
"v_accvgpr_read_b32 v254, a167\n" \
"v_mfma_f32_16x16x32_bf16 v[172:175], v[24:27], v[216:219], v[172:175]\n" \
"v_mul_f32 v243, s76, v243\n" \
"v_mul_f32 v252, s76, v252\n" \
"v_mfma_f32_16x16x32_bf16 v[172:175], v[28:31], v[220:223], v[172:175]\n" \
"v_mul_f32 v253, s76, v253\n" \
"v_mul_f32 v254, s76, v254\n" \
"v_mfma_f32_16x16x32_bf16 v[168:171], v[16:19], v[216:219], v[168:171]\n" \
"v_cvt_pk_bf16_f32 v243, v243, v252\n" \
"v_cvt_pk_bf16_f32 v253, v253, v254\n" \
"v_mfma_f32_16x16x32_bf16 v[168:171], v[20:23], v[220:223], v[168:171]\n" \
"buffer_store_dwordx4 a[224:227], v236, s[56:59], s93 offen offset:256 sc1\n" \
"v_mfma_f32_16x16x32_bf16 v[164:167], v[8:11], v[216:219], v[164:167]\n" \
"v_accvgpr_write_b32 a162, v243\n" \
"v_accvgpr_write_b32 a163, v253\n" \
"v_mfma_f32_16x16x32_bf16 v[164:167], v[12:15], v[220:223], v[164:167]\n" \
"v_accvgpr_read_b32 v243, a136\n" \
"v_accvgpr_read_b32 v252, a137\n" \
"v_mfma_f32_16x16x32_bf16 v[160:163], v[0:3], v[216:219], v[160:163]\n" \
"v_accvgpr_read_b32 v253, a138\n" \
"v_accvgpr_read_b32 v254, a139\n" \
"v_mfma_f32_16x16x32_bf16 v[160:163], v[4:7], v[220:223], v[160:163]\n" \
"s_waitcnt vmcnt(26) lgkmcnt(0)\n" \
"s_barrier\n" \
"v_mfma_f32_16x16x32_bf16 v[80:83], v[32:35], v[192:195], v[80:83]\n" \
"ds_read_b64_tr_b16 v[0:1], v232 offset:32768\n" \
"v_mul_f32 v243, s76, v243\n" \
"v_mfma_f32_16x16x32_bf16 v[80:83], v[36:39], v[196:199], v[80:83]\n" \
"ds_read_b64_tr_b16 v[2:3], v232 offset:34816\n" \
"v_mul_f32 v252, s76, v252\n" \
"v_mfma_f32_16x16x32_bf16 v[84:87], v[40:43], v[192:195], v[84:87]\n" \
"s_add_u32 m0, s79, 16384\n" \
"ds_read_b64_tr_b16 v[4:5], v232 offset:40960\n" \
"v_mul_f32 v253, s76, v253\n" \
"v_mfma_f32_16x16x32_bf16 v[84:87], v[44:47], v[196:199], v[84:87]\n" \
"buffer_load_dwordx4 v228, s[52:55], s78 offen offset:0 lds\n" \
"v_mfma_f32_16x16x32_bf16 v[88:91], v[48:51], v[192:195], v[88:91]\n" \
"ds_read_b64_tr_b16 v[6:7], v232 offset:43008\n" \
"v_mul_f32 v254, s76, v254\n" \
"v_mfma_f32_16x16x32_bf16 v[88:91], v[52:55], v[196:199], v[88:91]\n" \
"ds_read_b64_tr_b16 v[8:9], v233 offset:32768\n" \
"v_cvt_pk_bf16_f32 v243, v243, v252\n" \
"v_mfma_f32_16x16x32_bf16 v[92:95], v[56:59], v[192:195], v[92:95]\n" \
"ds_read_b64_tr_b16 v[10:11], v233 offset:34816\n" \
"v_cvt_pk_bf16_f32 v253, v253, v254\n" \
"v_mfma_f32_16x16x32_bf16 v[92:95], v[60:63], v[196:199], v[92:95]\n" \
"buffer_load_dwordx4 v229, s[52:55], s78 offen offset:1024 lds\n" \
"v_mfma_f32_16x16x32_bf16 v[124:127], v[56:59], v[200:203], v[124:127]\n" \
"ds_read_b128 v[192:195], v230 offset:32768\n" \
"v_accvgpr_write_b32 a136, v243\n" \
"v_mfma_f32_16x16x32_bf16 v[124:127], v[60:63], v[204:207], v[124:127]\n" \
"ds_read_b128 v[196:199], v231 offset:32768\n" \
"v_accvgpr_write_b32 a137, v253\n" \
"v_mfma_f32_16x16x32_bf16 v[120:123], v[48:51], v[200:203], v[120:123]\n" \
"s_xor_b32 s78, s78, 0x80000\n" \
"ds_read_b64_tr_b16 v[12:13], v233 offset:40960\n" \
"v_accvgpr_read_b32 v243, a140\n" \
"v_mfma_f32_16x16x32_bf16 v[120:123], v[52:55], v[204:207], v[120:123]\n" \
"buffer_load_dwordx4 v228, s[52:55], s89 offen offset:2048 lds\n" \
"v_mfma_f32_16x16x32_bf16 v[116:119], v[40:43], v[200:203], v[116:119]\n" \
"ds_read_b64_tr_b16 v[14:15], v233 offset:43008\n" \
"v_accvgpr_read_b32 v252, a141\n" \
"v_mfma_f32_16x16x32_bf16 v[116:119], v[44:47], v[204:207], v[116:119]\n" \
"ds_read_b64_tr_b16 v[16:17], v232 offset:33280\n" \
"v_accvgpr_read_b32 v253, a142\n" \
"v_mfma_f32_16x16x32_bf16 v[112:115], v[32:35], v[200:203], v[112:115]\n" \
"ds_read_b64_tr_b16 v[18:19], v232 offset:35328\n" \
"v_accvgpr_read_b32 v254, a143\n" \
"v_mfma_f32_16x16x32_bf16 v[112:115], v[36:39], v[204:207], v[112:115]\n" \
"buffer_load_dwordx4 v229, s[52:55], s89 offen offset:3072 lds\n" \
"v_mfma_f32_16x16x32_bf16 v[144:147], v[32:35], v[208:211], v[144:147]\n" \
"s_xor_b32 s89, s89, 0x80000\n" \
"ds_read_b128 v[200:203], v230 offset:34816\n" \
"v_mfma_f32_16x16x32_bf16 v[144:147], v[36:39], v[212:215], v[144:147]\n" \
"s_add_u32 m0, s74, 98304\n" \
"ds_read_b128 v[204:207], v231 offset:34816\n" \
"v_mul_f32 v243, s76, v243\n" \
"v_mfma_f32_16x16x32_bf16 v[148:151], v[40:43], v[208:211], v[148:151]\n" \
"s_waitcnt lgkmcnt(6)\n" \
"ds_read_b64_tr_b16 v[20:21], v232 offset:41472\n" \
"v_mul_f32 v252, s76, v252\n" \
"v_mfma_f32_16x16x32_bf16 v[148:151], v[44:47], v[212:215], v[148:151]\n" \
"buffer_load_dwordx4 v224, s[48:51], s75 offen offset:0 lds\n" \
"v_mfma_f32_16x16x32_bf16 v[152:155], v[48:51], v[208:211], v[152:155]\n" \
"ds_read_b64_tr_b16 v[22:23], v232 offset:43520\n" \
"v_mul_f32 v253, s76, v253\n" \
"v_mfma_f32_16x16x32_bf16 v[152:155], v[52:55], v[212:215], v[152:155]\n" \
"ds_read_b64_tr_b16 v[24:25], v233 offset:33280\n" \
"v_mul_f32 v254, s76, v254\n" \
"v_mfma_f32_16x16x32_bf16 v[156:159], v[56:59], v[208:211], v[156:159]\n" \
"ds_read_b64_tr_b16 v[26:27], v233 offset:35328\n" \
"v_cvt_pk_bf16_f32 v243, v243, v252\n" \
"v_mfma_f32_16x16x32_bf16 v[156:159], v[60:63], v[212:215], v[156:159]\n" \
"buffer_load_dwordx4 v225, s[48:51], s75 offen offset:1024 lds\n" \
"v_mfma_f32_16x16x32_bf16 v[188:191], v[56:59], v[216:219], v[188:191]\n" \
"ds_read_b128 v[208:211], v230 offset:36864\n" \
"v_cvt_pk_bf16_f32 v253, v253, v254\n" \
"v_mfma_f32_16x16x32_bf16 v[188:191], v[60:63], v[220:223], v[188:191]\n" \
"ds_read_b128 v[212:215], v231 offset:36864\n" \
"v_accvgpr_write_b32 a138, v243\n" \
"v_mfma_f32_16x16x32_bf16 v[184:187], v[48:51], v[216:219], v[184:187]\n" \
"s_waitcnt lgkmcnt(6)\n" \
"ds_read_b64_tr_b16 v[28:29], v233 offset:41472\n" \
"v_accvgpr_write_b32 a139, v253\n" \
"v_mfma_f32_16x16x32_bf16 v[184:187], v[52:55], v[220:223], v[184:187]\n" \
"buffer_load_dwordx4 v226, s[48:51], s75 offen offset:2048 lds\n" \
"v_mfma_f32_16x16x32_bf16 v[180:183], v[40:43], v[216:219], v[180:183]\n" \
"ds_read_b64_tr_b16 v[30:31], v233 offset:43520\n" \
"v_accvgpr_read_b32 v243, a168\n" \
"v_mfma_f32_16x16x32_bf16 v[180:183], v[44:47], v[220:223], v[180:183]\n" \
"v_accvgpr_read_b32 v252, a169\n" \
"v_accvgpr_read_b32 v253, a170\n" \
"v_mfma_f32_16x16x32_bf16 v[176:179], v[32:35], v[216:219], v[176:179]\n" \
"v_accvgpr_read_b32 v254, a171\n" \
"v_mul_f32 v243, s76, v243\n" \
"v_mfma_f32_16x16x32_bf16 v[176:179], v[36:39], v[220:223], v[176:179]\n" \
"buffer_load_dwordx4 v227, s[48:51], s75 offen offset:3072 lds\n" \
"v_mfma_f32_16x16x32_bf16 v[64:67], v[0:3], v[192:195], v[64:67]\n" \
"s_xor_b32 s75, s75, 0x80\n" \
"ds_read_b128 v[216:219], v230 offset:38912\n" \
"v_mfma_f32_16x16x32_bf16 v[64:67], v[4:7], v[196:199], v[64:67]\n" \
"s_add_u32 m0, s74, 102400\n" \
"ds_read_b128 v[220:223], v231 offset:38912\n" \
"v_mul_f32 v252, s76, v252\n" \
"v_mfma_f32_16x16x32_bf16 v[68:71], v[8:11], v[192:195], v[68:71]\n" \
"s_waitcnt lgkmcnt(4)\n" \
"ds_read_b64_tr_b16 v[32:33], v232 offset:33792\n" \
"v_mul_f32 v253, s76, v253\n" \
"v_mfma_f32_16x16x32_bf16 v[68:71], v[12:15], v[196:199], v[68:71]\n" \
"buffer_load_dwordx4 v224, s[48:51], s77 offen offset:0 lds\n" \
"v_mfma_f32_16x16x32_bf16 v[72:75], v[16:19], v[192:195], v[72:75]\n" \
"ds_read_b64_tr_b16 v[34:35], v232 offset:35840\n" \
"v_mul_f32 v254, s76, v254\n" \
"v_mfma_f32_16x16x32_bf16 v[72:75], v[20:23], v[196:199], v[72:75]\n" \
"s_waitcnt lgkmcnt(3)\n" \
"ds_read_b64_tr_b16 v[36:37], v232 offset:41984\n" \
"v_cvt_pk_bf16_f32 v243, v243, v252\n" \
"v_mfma_f32_16x16x32_bf16 v[76:79], v[24:27], v[192:195], v[76:79]\n" \
"ds_read_b64_tr_b16 v[38:39], v232 offset:44032\n" \
"v_cvt_pk_bf16_f32 v253, v253, v254\n" \
"v_mfma_f32_16x16x32_bf16 v[76:79], v[28:31], v[196:199], v[76:79]\n" \
"buffer_load_dwordx4 v225, s[48:51], s77 offen offset:1024 lds\n" \
"v_mfma_f32_16x16x32_bf16 v[108:111], v[24:27], v[200:203], v[108:111]\n" \
"ds_read_b64_tr_b16 v[40:41], v233 offset:33792\n" \
"v_accvgpr_write_b32 a168, v243\n" \
"v_mfma_f32_16x16x32_bf16 v[108:111], v[28:31], v[204:207], v[108:111]\n" \
"ds_read_b64_tr_b16 v[42:43], v233 offset:35840\n" \
"v_accvgpr_write_b32 a169, v253\n" \
"v_mfma_f32_16x16x32_bf16 v[104:107], v[16:19], v[200:203], v[104:107]\n" \
"s_waitcnt lgkmcnt(6)\n" \
"ds_read_b64_tr_b16 v[44:45], v233 offset:41984\n" \
"v_accvgpr_read_b32 v243, a172\n" \
"v_mfma_f32_16x16x32_bf16 v[104:107], v[20:23], v[204:207], v[104:107]\n" \
"buffer_load_dwordx4 v226, s[48:51], s77 offen offset:2048 lds\n" \
"v_mfma_f32_16x16x32_bf16 v[100:103], v[8:11], v[200:203], v[100:103]\n" \
"ds_read_b64_tr_b16 v[46:47], v233 offset:44032\n" \
"v_accvgpr_read_b32 v252, a173\n" \
"v_mfma_f32_16x16x32_bf16 v[100:103], v[12:15], v[204:207], v[100:103]\n" \
"ds_read_b64_tr_b16 v[48:49], v232 offset:34304\n" \
"v_accvgpr_read_b32 v253, a174\n" \
"v_mfma_f32_16x16x32_bf16 v[96:99], v[0:3], v[200:203], v[96:99]\n" \
"ds_read_b64_tr_b16 v[50:51], v232 offset:36352\n" \
"v_accvgpr_read_b32 v254, a175\n" \
"v_mfma_f32_16x16x32_bf16 v[96:99], v[4:7], v[204:207], v[96:99]\n" \
"buffer_load_dwordx4 v227, s[48:51], s77 offen offset:3072 lds\n" \
"v_mfma_f32_16x16x32_bf16 v[128:131], v[0:3], v[208:211], v[128:131]\n" \
"ds_read_b64_tr_b16 v[52:53], v232 offset:42496\n" \
"v_mul_f32 v243, s76, v243\n" \
"v_mfma_f32_16x16x32_bf16 v[128:131], v[4:7], v[212:215], v[128:131]\n" \
"ds_read_b64_tr_b16 v[54:55], v232 offset:44544\n" \
"v_mul_f32 v252, s76, v252\n" \
"v_mfma_f32_16x16x32_bf16 v[132:135], v[8:11], v[208:211], v[132:135]\n" \
"s_xor_b32 s77, s77, 0x80\n" \
"ds_read_b64_tr_b16 v[56:57], v233 offset:34304\n" \
"v_mul_f32 v253, s76, v253\n" \
"v_mfma_f32_16x16x32_bf16 v[132:135], v[12:15], v[212:215], v[132:135]\n" \
"v_mul_f32 v254, s76, v254\n" \
"v_cvt_pk_bf16_f32 v243, v243, v252\n" \
"v_mfma_f32_16x16x32_bf16 v[136:139], v[16:19], v[208:211], v[136:139]\n" \
"ds_read_b64_tr_b16 v[58:59], v233 offset:36352\n" \
"v_cvt_pk_bf16_f32 v253, v253, v254\n" \
"v_mfma_f32_16x16x32_bf16 v[136:139], v[20:23], v[212:215], v[136:139]\n" \
"ds_read_b64_tr_b16 v[60:61], v233 offset:42496\n" \
"v_accvgpr_write_b32 a170, v243\n" \
"v_mfma_f32_16x16x32_bf16 v[140:143], v[24:27], v[208:211], v[140:143]\n" \
"ds_read_b64_tr_b16 v[62:63], v233 offset:44544\n" \
"v_accvgpr_write_b32 a171, v253\n" \
"v_mfma_f32_16x16x32_bf16 v[140:143], v[28:31], v[212:215], v[140:143]\n" \
"buffer_store_dwordx4 a[200:203], v236, s[56:59], s93 offen offset:2048 sc1\n" \
"v_mfma_f32_16x16x32_bf16 v[172:175], v[24:27], v[216:219], v[172:175]\n" \
"v_accvgpr_read_b32 v243, a208\n" \
"v_accvgpr_read_b32 v252, a209\n" \
"v_mfma_f32_16x16x32_bf16 v[172:175], v[28:31], v[220:223], v[172:175]\n" \
"v_accvgpr_read_b32 v253, a210\n" \
"v_accvgpr_read_b32 v254, a211\n" \
"v_mfma_f32_16x16x32_bf16 v[168:171], v[16:19], v[216:219], v[168:171]\n" \
"v_mul_f32 v243, s31, v243\n" \
"v_mul_f32 v252, s31, v252\n" \
"v_mfma_f32_16x16x32_bf16 v[168:171], v[20:23], v[220:223], v[168:171]\n" \
"v_mul_f32 v253, s31, v253\n" \
"v_mul_f32 v254, s31, v254\n" \
"v_mfma_f32_16x16x32_bf16 v[164:167], v[8:11], v[216:219], v[164:167]\n" \
"v_cvt_pk_bf16_f32 v243, v243, v252\n" \
"v_cvt_pk_bf16_f32 v253, v253, v254\n" \
"v_mfma_f32_16x16x32_bf16 v[164:167], v[12:15], v[220:223], v[164:167]\n" \
"v_accvgpr_write_b32 a208, v243\n" \
"v_accvgpr_write_b32 a209, v253\n" \
"v_mfma_f32_16x16x32_bf16 v[160:163], v[0:3], v[216:219], v[160:163]\n" \
"v_accvgpr_read_b32 v243, a240\n" \
"v_accvgpr_read_b32 v252, a241\n" \
"v_mfma_f32_16x16x32_bf16 v[160:163], v[4:7], v[220:223], v[160:163]\n" \
"s_waitcnt vmcnt(35) lgkmcnt(0)\n" \
"s_barrier\n" \
"v_mfma_f32_16x16x32_bf16 v[80:83], v[32:35], v[192:195], v[80:83]\n" \
"ds_read_b64_tr_b16 v[0:1], v232 offset:49152\n" \
"v_accvgpr_read_b32 v253, a242\n" \
"v_mfma_f32_16x16x32_bf16 v[80:83], v[36:39], v[196:199], v[80:83]\n" \
"ds_read_b64_tr_b16 v[2:3], v232 offset:51200\n" \
"v_accvgpr_read_b32 v254, a243\n" \
"v_mfma_f32_16x16x32_bf16 v[84:87], v[40:43], v[192:195], v[84:87]\n" \
"s_add_u32 m0, s79, 32768\n" \
"ds_read_b64_tr_b16 v[4:5], v232 offset:57344\n" \
"v_mul_f32 v243, s31, v243\n" \
"v_mfma_f32_16x16x32_bf16 v[84:87], v[44:47], v[196:199], v[84:87]\n" \
"buffer_load_dwordx4 v228, s[52:55], s78 offen offset:0 lds\n" \
"v_mfma_f32_16x16x32_bf16 v[88:91], v[48:51], v[192:195], v[88:91]\n" \
"ds_read_b64_tr_b16 v[6:7], v232 offset:59392\n" \
"v_mul_f32 v252, s31, v252\n" \
"v_mfma_f32_16x16x32_bf16 v[88:91], v[52:55], v[196:199], v[88:91]\n" \
"ds_read_b64_tr_b16 v[8:9], v233 offset:49152\n" \
"v_mul_f32 v253, s31, v253\n" \
"v_mfma_f32_16x16x32_bf16 v[92:95], v[56:59], v[192:195], v[92:95]\n" \
"ds_read_b64_tr_b16 v[10:11], v233 offset:51200\n" \
"v_mul_f32 v254, s31, v254\n" \
"v_mfma_f32_16x16x32_bf16 v[92:95], v[60:63], v[196:199], v[92:95]\n" \
"buffer_load_dwordx4 v229, s[52:55], s78 offen offset:1024 lds\n" \
"v_mfma_f32_16x16x32_bf16 v[124:127], v[56:59], v[200:203], v[124:127]\n" \
"s_waitcnt vmcnt(35)\n" \
"ds_read_b128 v[192:195], v250 offset:0\n" \
"v_cvt_pk_bf16_f32 v243, v243, v252\n" \
"v_mfma_f32_16x16x32_bf16 v[124:127], v[60:63], v[204:207], v[124:127]\n" \
"ds_read_b128 v[196:199], v251 offset:0\n" \
"v_cvt_pk_bf16_f32 v253, v253, v254\n" \
"v_mfma_f32_16x16x32_bf16 v[120:123], v[48:51], v[200:203], v[120:123]\n" \
"s_xor_b32 s78, s78, 0x180000\n" \
"ds_read_b64_tr_b16 v[12:13], v233 offset:57344\n" \
"v_accvgpr_write_b32 a210, v243\n" \
"v_mfma_f32_16x16x32_bf16 v[120:123], v[52:55], v[204:207], v[120:123]\n" \
"buffer_load_dwordx4 v228, s[52:55], s89 offen offset:2048 lds\n" \
"v_mfma_f32_16x16x32_bf16 v[116:119], v[40:43], v[200:203], v[116:119]\n" \
"ds_read_b64_tr_b16 v[14:15], v233 offset:59392\n" \
"v_accvgpr_write_b32 a211, v253\n" \
"v_mfma_f32_16x16x32_bf16 v[116:119], v[44:47], v[204:207], v[116:119]\n" \
"ds_read_b64_tr_b16 v[16:17], v232 offset:49664\n" \
"v_accvgpr_read_b32 v243, a212\n" \
"v_mfma_f32_16x16x32_bf16 v[112:115], v[32:35], v[200:203], v[112:115]\n" \
"ds_read_b64_tr_b16 v[18:19], v232 offset:51712\n" \
"v_accvgpr_read_b32 v252, a213\n" \
"v_mfma_f32_16x16x32_bf16 v[112:115], v[36:39], v[204:207], v[112:115]\n" \
"buffer_load_dwordx4 v229, s[52:55], s89 offen offset:3072 lds\n" \
"v_mfma_f32_16x16x32_bf16 v[144:147], v[32:35], v[208:211], v[144:147]\n" \
"s_waitcnt vmcnt(35)\n" \
"s_xor_b32 s89, s89, 0x180000\n" \
"ds_read_b128 v[200:203], v250 offset:2048\n" \
"v_mfma_f32_16x16x32_bf16 v[144:147], v[36:39], v[212:215], v[144:147]\n" \
"s_add_u32 m0, s74, 131072\n" \
"ds_read_b128 v[204:207], v251 offset:2048\n" \
"v_accvgpr_read_b32 v253, a214\n" \
"v_mfma_f32_16x16x32_bf16 v[148:151], v[40:43], v[208:211], v[148:151]\n" \
"s_waitcnt lgkmcnt(6)\n" \
"ds_read_b64_tr_b16 v[20:21], v232 offset:57856\n" \
"v_accvgpr_read_b32 v254, a215\n" \
"v_mfma_f32_16x16x32_bf16 v[148:151], v[44:47], v[212:215], v[148:151]\n" \
"buffer_load_dwordx4 v224, s[48:51], s75 offen offset:0 lds\n" \
"v_mfma_f32_16x16x32_bf16 v[152:155], v[48:51], v[208:211], v[152:155]\n" \
"ds_read_b64_tr_b16 v[22:23], v232 offset:59904\n" \
"v_mul_f32 v243, s31, v243\n" \
"v_mfma_f32_16x16x32_bf16 v[152:155], v[52:55], v[212:215], v[152:155]\n" \
"ds_read_b64_tr_b16 v[24:25], v233 offset:49664\n" \
"v_mul_f32 v252, s31, v252\n" \
"v_mfma_f32_16x16x32_bf16 v[156:159], v[56:59], v[208:211], v[156:159]\n" \
"ds_read_b64_tr_b16 v[26:27], v233 offset:51712\n" \
"v_mul_f32 v253, s31, v253\n" \
"v_mfma_f32_16x16x32_bf16 v[156:159], v[60:63], v[212:215], v[156:159]\n" \
"buffer_load_dwordx4 v225, s[48:51], s75 offen offset:1024 lds\n" \
"v_mfma_f32_16x16x32_bf16 v[188:191], v[56:59], v[216:219], v[188:191]\n" \
"s_waitcnt vmcnt(35)\n" \
"ds_read_b128 v[208:211], v250 offset:4096\n" \
"v_mul_f32 v254, s31, v254\n" \
"v_mfma_f32_16x16x32_bf16 v[188:191], v[60:63], v[220:223], v[188:191]\n" \
"ds_read_b128 v[212:215], v251 offset:4096\n" \
"v_cvt_pk_bf16_f32 v243, v243, v252\n" \
"v_mfma_f32_16x16x32_bf16 v[184:187], v[48:51], v[216:219], v[184:187]\n" \
"s_waitcnt lgkmcnt(6)\n" \
"ds_read_b64_tr_b16 v[28:29], v233 offset:57856\n" \
"v_cvt_pk_bf16_f32 v253, v253, v254\n" \
"v_mfma_f32_16x16x32_bf16 v[184:187], v[52:55], v[220:223], v[184:187]\n" \
"buffer_load_dwordx4 v226, s[48:51], s75 offen offset:2048 lds\n" \
"v_mfma_f32_16x16x32_bf16 v[180:183], v[40:43], v[216:219], v[180:183]\n" \
"ds_read_b64_tr_b16 v[30:31], v233 offset:59904\n" \
"v_accvgpr_write_b32 a212, v243\n" \
"v_mfma_f32_16x16x32_bf16 v[180:183], v[44:47], v[220:223], v[180:183]\n" \
"v_accvgpr_write_b32 a213, v253\n" \
"v_accvgpr_read_b32 v243, a244\n" \
"v_mfma_f32_16x16x32_bf16 v[176:179], v[32:35], v[216:219], v[176:179]\n" \
"v_accvgpr_read_b32 v252, a245\n" \
"v_accvgpr_read_b32 v253, a246\n" \
"v_mfma_f32_16x16x32_bf16 v[176:179], v[36:39], v[220:223], v[176:179]\n" \
"buffer_load_dwordx4 v227, s[48:51], s75 offen offset:3072 lds\n" \
"v_mfma_f32_16x16x32_bf16 v[64:67], v[0:3], v[192:195], v[64:67]\n" \
"s_waitcnt vmcnt(35)\n" \
"s_xor_b32 s75, s75, 0x180\n" \
"ds_read_b128 v[216:219], v250 offset:6144\n" \
"v_mfma_f32_16x16x32_bf16 v[64:67], v[4:7], v[196:199], v[64:67]\n" \
"s_add_u32 m0, s74, 135168\n" \
"ds_read_b128 v[220:223], v251 offset:6144\n" \
"v_accvgpr_read_b32 v254, a247\n" \
"v_mfma_f32_16x16x32_bf16 v[68:71], v[8:11], v[192:195], v[68:71]\n" \
"s_waitcnt lgkmcnt(4)\n" \
"ds_read_b64_tr_b16 v[32:33], v232 offset:50176\n" \
"v_mul_f32 v243, s31, v243\n" \
"v_mfma_f32_16x16x32_bf16 v[68:71], v[12:15], v[196:199], v[68:71]\n" \
"buffer_load_dwordx4 v224, s[48:51], s77 offen offset:0 lds\n" \
"v_mfma_f32_16x16x32_bf16 v[72:75], v[16:19], v[192:195], v[72:75]\n" \
"ds_read_b64_tr_b16 v[34:35], v232 offset:52224\n" \
"v_mul_f32 v252, s31, v252\n" \
"v_mfma_f32_16x16x32_bf16 v[72:75], v[20:23], v[196:199], v[72:75]\n" \
"s_waitcnt lgkmcnt(3)\n" \
"ds_read_b64_tr_b16 v[36:37], v232 offset:58368\n" \
"v_mul_f32 v253, s31, v253\n" \
"v_mfma_f32_16x16x32_bf16 v[76:79], v[24:27], v[192:195], v[76:79]\n" \
"ds_read_b64_tr_b16 v[38:39], v232 offset:60416\n" \
"v_mul_f32 v254, s31, v254\n" \
"v_mfma_f32_16x16x32_bf16 v[76:79], v[28:31], v[196:199], v[76:79]\n" \
"buffer_load_dwordx4 v225, s[48:51], s77 offen offset:1024 lds\n" \
"v_mfma_f32_16x16x32_bf16 v[108:111], v[24:27], v[200:203], v[108:111]\n" \
"ds_read_b64_tr_b16 v[40:41], v233 offset:50176\n" \
"Lpg2748_%=:\n" \
"s_cbranch_execz Lpg5005_%=\n" \
"v_cvt_pk_bf16_f32 v243, v243, v252\n" \
"v_mfma_f32_16x16x32_bf16 v[108:111], v[28:31], v[204:207], v[108:111]\n" \
"ds_read_b64_tr_b16 v[42:43], v233 offset:52224\n" \
"v_cvt_pk_bf16_f32 v253, v253, v254\n" \
"v_mfma_f32_16x16x32_bf16 v[104:107], v[16:19], v[200:203], v[104:107]\n" \
"s_waitcnt lgkmcnt(6)\n" \
"ds_read_b64_tr_b16 v[44:45], v233 offset:58368\n" \
"v_accvgpr_write_b32 a214, v243\n" \
"v_mfma_f32_16x16x32_bf16 v[104:107], v[20:23], v[204:207], v[104:107]\n" \
"buffer_load_dwordx4 v226, s[48:51], s77 offen offset:2048 lds\n" \
"v_mfma_f32_16x16x32_bf16 v[100:103], v[8:11], v[200:203], v[100:103]\n" \
"ds_read_b64_tr_b16 v[46:47], v233 offset:60416\n" \
"v_accvgpr_write_b32 a215, v253\n" \
"v_mfma_f32_16x16x32_bf16 v[100:103], v[12:15], v[204:207], v[100:103]\n" \
"ds_read_b64_tr_b16 v[48:49], v232 offset:50688\n" \
"v_accvgpr_read_b32 v243, a216\n" \
"v_mfma_f32_16x16x32_bf16 v[96:99], v[0:3], v[200:203], v[96:99]\n" \
"ds_read_b64_tr_b16 v[50:51], v232 offset:52736\n" \
"v_accvgpr_read_b32 v252, a217\n" \
"v_mfma_f32_16x16x32_bf16 v[96:99], v[4:7], v[204:207], v[96:99]\n" \
"buffer_load_dwordx4 v227, s[48:51], s77 offen offset:3072 lds\n" \
"v_mfma_f32_16x16x32_bf16 v[128:131], v[0:3], v[208:211], v[128:131]\n" \
"ds_read_b64_tr_b16 v[52:53], v232 offset:58880\n" \
"v_accvgpr_read_b32 v253, a218\n" \
"v_mfma_f32_16x16x32_bf16 v[128:131], v[4:7], v[212:215], v[128:131]\n" \
"ds_read_b64_tr_b16 v[54:55], v232 offset:60928\n" \
"v_accvgpr_read_b32 v254, a219\n" \
"v_mfma_f32_16x16x32_bf16 v[132:135], v[8:11], v[208:211], v[132:135]\n" \
"s_xor_b32 s77, s77, 0x180\n" \
"ds_read_b64_tr_b16 v[56:57], v233 offset:50688\n" \
"v_mul_f32 v243, s31, v243\n" \
"v_mfma_f32_16x16x32_bf16 v[132:135], v[12:15], v[212:215], v[132:135]\n" \
"buffer_store_dwordx4 a[232:235], v236, s[56:59], s93 offen offset:2304 sc1\n" \
"v_mfma_f32_16x16x32_bf16 v[136:139], v[16:19], v[208:211], v[136:139]\n" \
"ds_read_b64_tr_b16 v[58:59], v233 offset:52736\n" \
"v_mul_f32 v252, s31, v252\n" \
"v_mfma_f32_16x16x32_bf16 v[136:139], v[20:23], v[212:215], v[136:139]\n" \
"ds_read_b64_tr_b16 v[60:61], v233 offset:58880\n" \
"v_mul_f32 v253, s31, v253\n" \
"v_mfma_f32_16x16x32_bf16 v[140:143], v[24:27], v[208:211], v[140:143]\n" \
"ds_read_b64_tr_b16 v[62:63], v233 offset:60928\n" \
"v_mul_f32 v254, s31, v254\n" \
"v_mfma_f32_16x16x32_bf16 v[140:143], v[28:31], v[212:215], v[140:143]\n" \
"v_cvt_pk_bf16_f32 v243, v243, v252\n" \
"v_cvt_pk_bf16_f32 v253, v253, v254\n" \
"v_mfma_f32_16x16x32_bf16 v[172:175], v[24:27], v[216:219], v[172:175]\n" \
"v_accvgpr_write_b32 a216, v243\n" \
"v_accvgpr_write_b32 a217, v253\n" \
"v_mfma_f32_16x16x32_bf16 v[172:175], v[28:31], v[220:223], v[172:175]\n" \
"v_accvgpr_read_b32 v243, a248\n" \
"v_accvgpr_read_b32 v252, a249\n" \
"v_mfma_f32_16x16x32_bf16 v[168:171], v[16:19], v[216:219], v[168:171]\n" \
"s_add_u32 s93, s87, 0\n" \
"v_accvgpr_read_b32 v253, a250\n" \
"v_accvgpr_read_b32 v254, a251\n" \
"v_mfma_f32_16x16x32_bf16 v[168:171], v[20:23], v[220:223], v[168:171]\n" \
"buffer_store_dwordx4 a[128:131], v236, s[56:59], s93 offen offset:0 sc1\n" \
"v_mfma_f32_16x16x32_bf16 v[164:167], v[8:11], v[216:219], v[164:167]\n" \
"v_mul_f32 v243, s31, v243\n" \
"v_mul_f32 v252, s31, v252\n" \
"v_mfma_f32_16x16x32_bf16 v[164:167], v[12:15], v[220:223], v[164:167]\n" \
"v_mul_f32 v253, s31, v253\n" \
"v_mul_f32 v254, s31, v254\n" \
"v_mfma_f32_16x16x32_bf16 v[160:163], v[0:3], v[216:219], v[160:163]\n" \
"v_cvt_pk_bf16_f32 v243, v243, v252\n" \
"v_cvt_pk_bf16_f32 v253, v253, v254\n" \
"v_mfma_f32_16x16x32_bf16 v[160:163], v[4:7], v[220:223], v[160:163]\n" \
"s_waitcnt vmcnt(37) lgkmcnt(0)\n" \
"s_barrier\n" \
"v_mfma_f32_16x16x32_bf16 v[80:83], v[32:35], v[192:195], v[80:83]\n" \
"ds_read_b64_tr_b16 v[0:1], v232 offset:0\n" \
"v_accvgpr_write_b32 a218, v243\n" \
"v_mfma_f32_16x16x32_bf16 v[80:83], v[36:39], v[196:199], v[80:83]\n" \
"ds_read_b64_tr_b16 v[2:3], v232 offset:2048\n" \
"v_accvgpr_write_b32 a219, v253\n" \
"v_mfma_f32_16x16x32_bf16 v[84:87], v[40:43], v[192:195], v[84:87]\n" \
"s_add_u32 m0, s79, 49152\n" \
"ds_read_b64_tr_b16 v[4:5], v232 offset:8192\n" \
"v_accvgpr_read_b32 v243, a220\n" \
"v_mfma_f32_16x16x32_bf16 v[84:87], v[44:47], v[196:199], v[84:87]\n" \
"buffer_load_dwordx4 v228, s[52:55], s78 offen offset:0 lds\n" \
"v_mfma_f32_16x16x32_bf16 v[88:91], v[48:51], v[192:195], v[88:91]\n" \
"ds_read_b64_tr_b16 v[6:7], v232 offset:10240\n" \
"v_accvgpr_read_b32 v252, a221\n" \
"v_mfma_f32_16x16x32_bf16 v[88:91], v[52:55], v[196:199], v[88:91]\n" \
"ds_read_b64_tr_b16 v[8:9], v233 offset:0\n" \
"v_accvgpr_read_b32 v253, a222\n" \
"v_mfma_f32_16x16x32_bf16 v[92:95], v[56:59], v[192:195], v[92:95]\n" \
"ds_read_b64_tr_b16 v[10:11], v233 offset:2048\n" \
"v_accvgpr_read_b32 v254, a223\n" \
"v_mfma_f32_16x16x32_bf16 v[92:95], v[60:63], v[196:199], v[92:95]\n" \
"buffer_load_dwordx4 v229, s[52:55], s78 offen offset:1024 lds\n" \
"v_mfma_f32_16x16x32_bf16 v[124:127], v[56:59], v[200:203], v[124:127]\n" \
"s_waitcnt vmcnt(37)\n" \
"ds_read_b128 v[192:195], v230 offset:0\n" \
"v_mul_f32 v243, s31, v243\n" \
"v_mfma_f32_16x16x32_bf16 v[124:127], v[60:63], v[204:207], v[124:127]\n" \
"ds_read_b128 v[196:199], v231 offset:0\n" \
"v_mul_f32 v252, s31, v252\n" \
"v_mfma_f32_16x16x32_bf16 v[120:123], v[48:51], v[200:203], v[120:123]\n" \
"s_xor_b32 s78, s78, 0x80000\n" \
"ds_read_b64_tr_b16 v[12:13], v233 offset:8192\n" \
"v_mul_f32 v253, s31, v253\n" \
"v_mfma_f32_16x16x32_bf16 v[120:123], v[52:55], v[204:207], v[120:123]\n" \
"buffer_load_dwordx4 v228, s[52:55], s89 offen offset:2048 lds\n" \
"v_mfma_f32_16x16x32_bf16 v[116:119], v[40:43], v[200:203], v[116:119]\n" \
"ds_read_b64_tr_b16 v[14:15], v233 offset:10240\n" \
"v_mul_f32 v254, s31, v254\n" \
"v_mfma_f32_16x16x32_bf16 v[116:119], v[44:47], v[204:207], v[116:119]\n" \
"ds_read_b64_tr_b16 v[16:17], v232 offset:512\n" \
"v_cvt_pk_bf16_f32 v243, v243, v252\n" \
"v_mfma_f32_16x16x32_bf16 v[112:115], v[32:35], v[200:203], v[112:115]\n" \
"ds_read_b64_tr_b16 v[18:19], v232 offset:2560\n" \
"v_cvt_pk_bf16_f32 v253, v253, v254\n" \
"v_mfma_f32_16x16x32_bf16 v[112:115], v[36:39], v[204:207], v[112:115]\n" \
"buffer_load_dwordx4 v229, s[52:55], s89 offen offset:3072 lds\n" \
"v_mfma_f32_16x16x32_bf16 v[144:147], v[32:35], v[208:211], v[144:147]\n" \
"s_waitcnt vmcnt(37)\n" \
"s_xor_b32 s89, s89, 0x80000\n" \
"ds_read_b128 v[200:203], v230 offset:2048\n" \
"v_mfma_f32_16x16x32_bf16 v[144:147], v[36:39], v[212:215], v[144:147]\n" \
"s_add_u32 m0, s74, 65536\n" \
"ds_read_b128 v[204:207], v231 offset:2048\n" \
"v_accvgpr_write_b32 a220, v243\n" \
"v_mfma_f32_16x16x32_bf16 v[148:151], v[40:43], v[208:211], v[148:151]\n" \
"s_waitcnt lgkmcnt(6)\n" \
"ds_read_b64_tr_b16 v[20:21], v232 offset:8704\n" \
"v_accvgpr_write_b32 a221, v253\n" \
"v_mfma_f32_16x16x32_bf16 v[148:151], v[44:47], v[212:215], v[148:151]\n" \
"buffer_load_dwordx4 v224, s[48:51], s75 offen offset:0 lds\n" \
"v_mfma_f32_16x16x32_bf16 v[152:155], v[48:51], v[208:211], v[152:155]\n" \
"ds_read_b64_tr_b16 v[22:23], v232 offset:10752\n" \
"v_accvgpr_read_b32 v243, a252\n" \
"v_mfma_f32_16x16x32_bf16 v[152:155], v[52:55], v[212:215], v[152:155]\n" \
"ds_read_b64_tr_b16 v[24:25], v233 offset:512\n" \
"v_accvgpr_read_b32 v252, a253\n" \
"v_mfma_f32_16x16x32_bf16 v[156:159], v[56:59], v[208:211], v[156:159]\n" \
"ds_read_b64_tr_b16 v[26:27], v233 offset:2560\n" \
"v_accvgpr_read_b32 v253, a254\n" \
"v_mfma_f32_16x16x32_bf16 v[156:159], v[60:63], v[212:215], v[156:159]\n" \
"buffer_load_dwordx4 v225, s[48:51], s75 offen offset:1024 lds\n" \
"v_mfma_f32_16x16x32_bf16 v[188:191], v[56:59], v[216:219], v[188:191]\n" \
"s_waitcnt vmcnt(37)\n" \
"ds_read_b128 v[208:211], v230 offset:4096\n" \
"v_accvgpr_read_b32 v254, a255\n" \
"v_mfma_f32_16x16x32_bf16 v[188:191], v[60:63], v[220:223], v[188:191]\n" \
"ds_read_b128 v[212:215], v231 offset:4096\n" \
"v_mul_f32 v243, s31, v243\n" \
"v_mfma_f32_16x16x32_bf16 v[184:187], v[48:51], v[216:219], v[184:187]\n" \
"s_waitcnt lgkmcnt(6)\n" \
"ds_read_b64_tr_b16 v[28:29], v233 offset:8704\n" \
"v_mul_f32 v252, s31, v252\n" \
"v_mfma_f32_16x16x32_bf16 v[184:187], v[52:55], v[220:223], v[184:187]\n" \
"buffer_load_dwordx4 v226, s[48:51], s75 offen offset:2048 lds\n" \
"v_mfma_f32_16x16x32_bf16 v[180:183], v[40:43], v[216:219], v[180:183]\n" \
"ds_read_b64_tr_b16 v[30:31], v233 offset:10752\n" \
"v_mul_f32 v253, s31, v253\n" \
"v_mfma_f32_16x16x32_bf16 v[180:183], v[44:47], v[220:223], v[180:183]\n" \
"v_mul_f32 v254, s31, v254\n" \
"v_cvt_pk_bf16_f32 v243, v243, v252\n" \
"v_mfma_f32_16x16x32_bf16 v[176:179], v[32:35], v[216:219], v[176:179]\n" \
"v_cvt_pk_bf16_f32 v253, v253, v254\n" \
"v_accvgpr_write_b32 a222, v243\n" \
"v_mfma_f32_16x16x32_bf16 v[176:179], v[36:39], v[220:223], v[176:179]\n" \
"buffer_load_dwordx4 v227, s[48:51], s75 offen offset:3072 lds\n" \
"v_mfma_f32_16x16x32_bf16 v[64:67], v[0:3], v[192:195], v[64:67]\n" \
"s_waitcnt vmcnt(37)\n" \
"s_xor_b32 s75, s75, 0x80\n" \
"ds_read_b128 v[216:219], v230 offset:6144\n" \
"v_mfma_f32_16x16x32_bf16 v[64:67], v[4:7], v[196:199], v[64:67]\n" \
"s_add_u32 m0, s74, 69632\n" \
"ds_read_b128 v[220:223], v231 offset:6144\n" \
"v_accvgpr_write_b32 a223, v253\n" \
"v_mfma_f32_16x16x32_bf16 v[68:71], v[8:11], v[192:195], v[68:71]\n" \
"s_waitcnt lgkmcnt(4)\n" \
"ds_read_b64_tr_b16 v[32:33], v232 offset:1024\n" \
"v_accvgpr_read_b32 v243, a144\n" \
"v_mfma_f32_16x16x32_bf16 v[68:71], v[12:15], v[196:199], v[68:71]\n" \
"buffer_load_dwordx4 v224, s[48:51], s77 offen offset:0 lds\n" \
"v_mfma_f32_16x16x32_bf16 v[72:75], v[16:19], v[192:195], v[72:75]\n" \
"ds_read_b64_tr_b16 v[34:35], v232 offset:3072\n" \
"v_accvgpr_read_b32 v252, a145\n" \
"v_mfma_f32_16x16x32_bf16 v[72:75], v[20:23], v[196:199], v[72:75]\n" \
"s_waitcnt lgkmcnt(3)\n" \
"ds_read_b64_tr_b16 v[36:37], v232 offset:9216\n" \
"v_accvgpr_read_b32 v253, a146\n" \
"v_mfma_f32_16x16x32_bf16 v[76:79], v[24:27], v[192:195], v[76:79]\n" \
"ds_read_b64_tr_b16 v[38:39], v232 offset:11264\n" \
"v_accvgpr_read_b32 v254, a147\n" \
"v_mfma_f32_16x16x32_bf16 v[76:79], v[28:31], v[196:199], v[76:79]\n" \
"buffer_load_dwordx4 v225, s[48:51], s77 offen offset:1024 lds\n" \
"v_mfma_f32_16x16x32_bf16 v[108:111], v[24:27], v[200:203], v[108:111]\n" \
"ds_read_b64_tr_b16 v[40:41], v233 offset:1024\n" \
"v_mul_f32 v243, s31, v243\n" \
"v_mfma_f32_16x16x32_bf16 v[108:111], v[28:31], v[204:207], v[108:111]\n" \
"ds_read_b64_tr_b16 v[42:43], v233 offset:3072\n" \
"v_mul_f32 v252, s31, v252\n" \
"v_mfma_f32_16x16x32_bf16 v[104:107], v[16:19], v[200:203], v[104:107]\n" \
"s_waitcnt lgkmcnt(6)\n" \
"ds_read_b64_tr_b16 v[44:45], v233 offset:9216\n" \
"v_mul_f32 v253, s31, v253\n" \
"v_mfma_f32_16x16x32_bf16 v[104:107], v[20:23], v[204:207], v[104:107]\n" \
"buffer_load_dwordx4 v226, s[48:51], s77 offen offset:2048 lds\n" \
"v_mfma_f32_16x16x32_bf16 v[100:103], v[8:11], v[200:203], v[100:103]\n" \
"ds_read_b64_tr_b16 v[46:47], v233 offset:11264\n" \
"v_mul_f32 v254, s31, v254\n" \
"v_mfma_f32_16x16x32_bf16 v[100:103], v[12:15], v[204:207], v[100:103]\n" \
"ds_read_b64_tr_b16 v[48:49], v232 offset:1536\n" \
"v_cvt_pk_bf16_f32 v243, v243, v252\n" \
"v_mfma_f32_16x16x32_bf16 v[96:99], v[0:3], v[200:203], v[96:99]\n" \
"ds_read_b64_tr_b16 v[50:51], v232 offset:3584\n" \
"v_cvt_pk_bf16_f32 v253, v253, v254\n" \
"v_mfma_f32_16x16x32_bf16 v[96:99], v[4:7], v[204:207], v[96:99]\n" \
"buffer_load_dwordx4 v227, s[48:51], s77 offen offset:3072 lds\n" \
"v_mfma_f32_16x16x32_bf16 v[128:131], v[0:3], v[208:211], v[128:131]\n" \
"ds_read_b64_tr_b16 v[52:53], v232 offset:9728\n" \
"v_accvgpr_write_b32 a144, v243\n" \
"v_mfma_f32_16x16x32_bf16 v[128:131], v[4:7], v[212:215], v[128:131]\n" \
"ds_read_b64_tr_b16 v[54:55], v232 offset:11776\n" \
"v_accvgpr_write_b32 a145, v253\n" \
"v_mfma_f32_16x16x32_bf16 v[132:135], v[8:11], v[208:211], v[132:135]\n" \
"s_xor_b32 s77, s77, 0x80\n" \
"ds_read_b64_tr_b16 v[56:57], v233 offset:1536\n" \
"v_accvgpr_read_b32 v243, a176\n" \
"v_mfma_f32_16x16x32_bf16 v[132:135], v[12:15], v[212:215], v[132:135]\n" \
"v_accvgpr_read_b32 v252, a177\n" \
"v_accvgpr_read_b32 v253, a178\n" \
"v_mfma_f32_16x16x32_bf16 v[136:139], v[16:19], v[208:211], v[136:139]\n" \
"ds_read_b64_tr_b16 v[58:59], v233 offset:3584\n" \
"v_accvgpr_read_b32 v254, a179\n" \
"v_mfma_f32_16x16x32_bf16 v[136:139], v[20:23], v[212:215], v[136:139]\n" \
"ds_read_b64_tr_b16 v[60:61], v233 offset:9728\n" \
"v_mul_f32 v243, s31, v243\n" \
"v_mfma_f32_16x16x32_bf16 v[140:143], v[24:27], v[208:211], v[140:143]\n" \
"ds_read_b64_tr_b16 v[62:63], v233 offset:11776\n" \
"v_mul_f32 v252, s31, v252\n" \
"v_mfma_f32_16x16x32_bf16 v[140:143], v[28:31], v[212:215], v[140:143]\n" \
"buffer_store_dwordx4 a[160:163], v236, s[56:59], s93 offen offset:256 sc1\n" \
"v_mfma_f32_16x16x32_bf16 v[172:175], v[24:27], v[216:219], v[172:175]\n" \
"v_mul_f32 v253, s31, v253\n" \
"v_mul_f32 v254, s31, v254\n" \
"v_mfma_f32_16x16x32_bf16 v[172:175], v[28:31], v[220:223], v[172:175]\n" \
"v_cvt_pk_bf16_f32 v243, v243, v252\n" \
"v_cvt_pk_bf16_f32 v253, v253, v254\n" \
"v_mfma_f32_16x16x32_bf16 v[168:171], v[16:19], v[216:219], v[168:171]\n" \
"v_accvgpr_write_b32 a146, v243\n" \
"v_accvgpr_write_b32 a147, v253\n" \
"v_mfma_f32_16x16x32_bf16 v[168:171], v[20:23], v[220:223], v[168:171]\n" \
"v_accvgpr_read_b32 v243, a148\n" \
"v_accvgpr_read_b32 v252, a149\n" \
"v_mfma_f32_16x16x32_bf16 v[164:167], v[8:11], v[216:219], v[164:167]\n" \
"v_accvgpr_read_b32 v253, a150\n" \
"v_accvgpr_read_b32 v254, a151\n" \
"v_mfma_f32_16x16x32_bf16 v[164:167], v[12:15], v[220:223], v[164:167]\n" \
"v_mul_f32 v243, s31, v243\n" \
"v_mul_f32 v252, s31, v252\n" \
"v_mfma_f32_16x16x32_bf16 v[160:163], v[0:3], v[216:219], v[160:163]\n" \
"v_mul_f32 v253, s31, v253\n" \
"v_mul_f32 v254, s31, v254\n" \
"v_mfma_f32_16x16x32_bf16 v[160:163], v[4:7], v[220:223], v[160:163]\n" \
"s_waitcnt vmcnt(36) lgkmcnt(0)\n" \
"s_barrier\n" \
"v_mfma_f32_16x16x32_bf16 v[80:83], v[32:35], v[192:195], v[80:83]\n" \
"ds_read_b64_tr_b16 v[0:1], v232 offset:16384\n" \
"v_cvt_pk_bf16_f32 v243, v243, v252\n" \
"v_mfma_f32_16x16x32_bf16 v[80:83], v[36:39], v[196:199], v[80:83]\n" \
"ds_read_b64_tr_b16 v[2:3], v232 offset:18432\n" \
"v_cvt_pk_bf16_f32 v253, v253, v254\n" \
"v_mfma_f32_16x16x32_bf16 v[84:87], v[40:43], v[192:195], v[84:87]\n" \
"s_add_u32 m0, s79, 0\n" \
"ds_read_b64_tr_b16 v[4:5], v232 offset:24576\n" \
"v_accvgpr_write_b32 a148, v243\n" \
"v_mfma_f32_16x16x32_bf16 v[84:87], v[44:47], v[196:199], v[84:87]\n" \
"buffer_load_dwordx4 v228, s[52:55], s78 offen offset:0 lds\n" \
"v_mfma_f32_16x16x32_bf16 v[88:91], v[48:51], v[192:195], v[88:91]\n" \
"ds_read_b64_tr_b16 v[6:7], v232 offset:26624\n" \
"v_accvgpr_write_b32 a149, v253\n" \
"v_mfma_f32_16x16x32_bf16 v[88:91], v[52:55], v[196:199], v[88:91]\n" \
"ds_read_b64_tr_b16 v[8:9], v233 offset:16384\n" \
"v_accvgpr_read_b32 v243, a180\n" \
"v_mfma_f32_16x16x32_bf16 v[92:95], v[56:59], v[192:195], v[92:95]\n" \
"ds_read_b64_tr_b16 v[10:11], v233 offset:18432\n" \
"v_accvgpr_read_b32 v252, a181\n" \
"v_mfma_f32_16x16x32_bf16 v[92:95], v[60:63], v[196:199], v[92:95]\n" \
"buffer_load_dwordx4 v229, s[52:55], s78 offen offset:1024 lds\n" \
"v_mfma_f32_16x16x32_bf16 v[124:127], v[56:59], v[200:203], v[124:127]\n" \
"s_waitcnt vmcnt(36)\n" \
"ds_read_b128 v[192:195], v230 offset:32768\n" \
"v_accvgpr_read_b32 v253, a182\n" \
"v_mfma_f32_16x16x32_bf16 v[124:127], v[60:63], v[204:207], v[124:127]\n" \
"ds_read_b128 v[196:199], v231 offset:32768\n" \
"v_accvgpr_read_b32 v254, a183\n" \
"v_mfma_f32_16x16x32_bf16 v[120:123], v[48:51], v[200:203], v[120:123]\n" \
"s_xor_b32 s78, s78, 0x780000\n" \
"ds_read_b64_tr_b16 v[12:13], v233 offset:24576\n" \
"v_mul_f32 v243, s31, v243\n" \
"v_mfma_f32_16x16x32_bf16 v[120:123], v[52:55], v[204:207], v[120:123]\n" \
"buffer_load_dwordx4 v228, s[52:55], s89 offen offset:2048 lds\n" \
"v_mfma_f32_16x16x32_bf16 v[116:119], v[40:43], v[200:203], v[116:119]\n" \
"ds_read_b64_tr_b16 v[14:15], v233 offset:26624\n" \
"v_mul_f32 v252, s31, v252\n" \
"v_mfma_f32_16x16x32_bf16 v[116:119], v[44:47], v[204:207], v[116:119]\n" \
"ds_read_b64_tr_b16 v[16:17], v232 offset:16896\n" \
"v_mul_f32 v253, s31, v253\n" \
"v_mfma_f32_16x16x32_bf16 v[112:115], v[32:35], v[200:203], v[112:115]\n" \
"ds_read_b64_tr_b16 v[18:19], v232 offset:18944\n" \
"v_mul_f32 v254, s31, v254\n" \
"v_mfma_f32_16x16x32_bf16 v[112:115], v[36:39], v[204:207], v[112:115]\n" \
"buffer_load_dwordx4 v229, s[52:55], s89 offen offset:3072 lds\n" \
"v_mfma_f32_16x16x32_bf16 v[144:147], v[32:35], v[208:211], v[144:147]\n" \
"s_waitcnt vmcnt(36)\n" \
"s_xor_b32 s89, s89, 0x780000\n" \
"ds_read_b128 v[200:203], v230 offset:34816\n" \
"v_mfma_f32_16x16x32_bf16 v[144:147], v[36:39], v[212:215], v[144:147]\n" \
"s_add_u32 m0, s74, 98304\n" \
"ds_read_b128 v[204:207], v231 offset:34816\n" \
"v_cvt_pk_bf16_f32 v243, v243, v252\n" \
"v_mfma_f32_16x16x32_bf16 v[148:151], v[40:43], v[208:211], v[148:151]\n" \
"s_waitcnt lgkmcnt(6)\n" \
"ds_read_b64_tr_b16 v[20:21], v232 offset:25088\n" \
"v_cvt_pk_bf16_f32 v253, v253, v254\n" \
"v_mfma_f32_16x16x32_bf16 v[148:151], v[44:47], v[212:215], v[148:151]\n" \
"buffer_load_dwordx4 v224, s[48:51], s75 offen offset:0 lds\n" \
"v_mfma_f32_16x16x32_bf16 v[152:155], v[48:51], v[208:211], v[152:155]\n" \
"ds_read_b64_tr_b16 v[22:23], v232 offset:27136\n" \
"v_accvgpr_write_b32 a150, v243\n" \
"v_mfma_f32_16x16x32_bf16 v[152:155], v[52:55], v[212:215], v[152:155]\n" \
"ds_read_b64_tr_b16 v[24:25], v233 offset:16896\n" \
"v_accvgpr_write_b32 a151, v253\n" \
"v_mfma_f32_16x16x32_bf16 v[156:159], v[56:59], v[208:211], v[156:159]\n" \
"ds_read_b64_tr_b16 v[26:27], v233 offset:18944\n" \
"v_accvgpr_read_b32 v243, a152\n" \
"v_mfma_f32_16x16x32_bf16 v[156:159], v[60:63], v[212:215], v[156:159]\n" \
"buffer_load_dwordx4 v225, s[48:51], s75 offen offset:1024 lds\n" \
"v_mfma_f32_16x16x32_bf16 v[188:191], v[56:59], v[216:219], v[188:191]\n" \
"s_waitcnt vmcnt(36)\n" \
"ds_read_b128 v[208:211], v230 offset:36864\n" \
"v_accvgpr_read_b32 v252, a153\n" \
"v_mfma_f32_16x16x32_bf16 v[188:191], v[60:63], v[220:223], v[188:191]\n" \
"ds_read_b128 v[212:215], v231 offset:36864\n" \
"v_accvgpr_read_b32 v253, a154\n" \
"v_mfma_f32_16x16x32_bf16 v[184:187], v[48:51], v[216:219], v[184:187]\n" \
"s_waitcnt lgkmcnt(6)\n" \
"ds_read_b64_tr_b16 v[28:29], v233 offset:25088\n" \
"v_accvgpr_read_b32 v254, a155\n" \
"v_mfma_f32_16x16x32_bf16 v[184:187], v[52:55], v[220:223], v[184:187]\n" \
"buffer_load_dwordx4 v226, s[48:51], s75 offen offset:2048 lds\n" \
"v_mfma_f32_16x16x32_bf16 v[180:183], v[40:43], v[216:219], v[180:183]\n" \
"ds_read_b64_tr_b16 v[30:31], v233 offset:27136\n" \
"v_mul_f32 v243, s31, v243\n" \
"v_mfma_f32_16x16x32_bf16 v[180:183], v[44:47], v[220:223], v[180:183]\n" \
"v_mul_f32 v252, s31, v252\n" \
"v_mul_f32 v253, s31, v253\n" \
"v_mfma_f32_16x16x32_bf16 v[176:179], v[32:35], v[216:219], v[176:179]\n" \
"v_mul_f32 v254, s31, v254\n" \
"v_cvt_pk_bf16_f32 v243, v243, v252\n" \
"v_mfma_f32_16x16x32_bf16 v[176:179], v[36:39], v[220:223], v[176:179]\n" \
"buffer_load_dwordx4 v227, s[48:51], s75 offen offset:3072 lds\n" \
"v_mfma_f32_16x16x32_bf16 v[64:67], v[0:3], v[192:195], v[64:67]\n" \
"s_waitcnt vmcnt(36)\n" \
"s_xor_b32 s75, s75, 0x780\n" \
"ds_read_b128 v[216:219], v230 offset:38912\n" \
"v_mfma_f32_16x16x32_bf16 v[64:67], v[4:7], v[196:199], v[64:67]\n" \
"s_add_u32 m0, s74, 102400\n" \
"ds_read_b128 v[220:223], v231 offset:38912\n" \
"v_cvt_pk_bf16_f32 v253, v253, v254\n" \
"v_mfma_f32_16x16x32_bf16 v[68:71], v[8:11], v[192:195], v[68:71]\n" \
"s_waitcnt lgkmcnt(4)\n" \
"ds_read_b64_tr_b16 v[32:33], v232 offset:17408\n" \
"v_accvgpr_write_b32 a152, v243\n" \
"v_mfma_f32_16x16x32_bf16 v[68:71], v[12:15], v[196:199], v[68:71]\n" \
"buffer_load_dwordx4 v224, s[48:51], s77 offen offset:0 lds\n" \
"v_mfma_f32_16x16x32_bf16 v[72:75], v[16:19], v[192:195], v[72:75]\n" \
"ds_read_b64_tr_b16 v[34:35], v232 offset:19456\n" \
"v_accvgpr_write_b32 a153, v253\n" \
"v_mfma_f32_16x16x32_bf16 v[72:75], v[20:23], v[196:199], v[72:75]\n" \
"s_waitcnt lgkmcnt(3)\n" \
"ds_read_b64_tr_b16 v[36:37], v232 offset:25600\n" \
"v_accvgpr_read_b32 v243, a184\n" \
"v_mfma_f32_16x16x32_bf16 v[76:79], v[24:27], v[192:195], v[76:79]\n" \
"ds_read_b64_tr_b16 v[38:39], v232 offset:27648\n" \
"v_accvgpr_read_b32 v252, a185\n" \
"v_mfma_f32_16x16x32_bf16 v[76:79], v[28:31], v[196:199], v[76:79]\n" \
"buffer_load_dwordx4 v225, s[48:51], s77 offen offset:1024 lds\n" \
"v_mfma_f32_16x16x32_bf16 v[108:111], v[24:27], v[200:203], v[108:111]\n" \
"ds_read_b64_tr_b16 v[40:41], v233 offset:17408\n" \
"v_accvgpr_read_b32 v253, a186\n" \
"v_mfma_f32_16x16x32_bf16 v[108:111], v[28:31], v[204:207], v[108:111]\n" \
"ds_read_b64_tr_b16 v[42:43], v233 offset:19456\n" \
"v_accvgpr_read_b32 v254, a187\n" \
"v_mfma_f32_16x16x32_bf16 v[104:107], v[16:19], v[200:203], v[104:107]\n" \
"s_waitcnt lgkmcnt(6)\n" \
"ds_read_b64_tr_b16 v[44:45], v233 offset:25600\n" \
"v_mul_f32 v243, s31, v243\n" \
"v_mfma_f32_16x16x32_bf16 v[104:107], v[20:23], v[204:207], v[104:107]\n" \
"buffer_load_dwordx4 v226, s[48:51], s77 offen offset:2048 lds\n" \
"v_mfma_f32_16x16x32_bf16 v[100:103], v[8:11], v[200:203], v[100:103]\n" \
"ds_read_b64_tr_b16 v[46:47], v233 offset:27648\n" \
"v_mul_f32 v252, s31, v252\n" \
"v_mfma_f32_16x16x32_bf16 v[100:103], v[12:15], v[204:207], v[100:103]\n" \
"ds_read_b64_tr_b16 v[48:49], v232 offset:17920\n" \
"v_mul_f32 v253, s31, v253\n" \
"v_mfma_f32_16x16x32_bf16 v[96:99], v[0:3], v[200:203], v[96:99]\n" \
"ds_read_b64_tr_b16 v[50:51], v232 offset:19968\n" \
"v_mul_f32 v254, s31, v254\n" \
"v_mfma_f32_16x16x32_bf16 v[96:99], v[4:7], v[204:207], v[96:99]\n" \
"buffer_load_dwordx4 v227, s[48:51], s77 offen offset:3072 lds\n" \
"v_mfma_f32_16x16x32_bf16 v[128:131], v[0:3], v[208:211], v[128:131]\n" \
"ds_read_b64_tr_b16 v[52:53], v232 offset:26112\n" \
"v_cvt_pk_bf16_f32 v243, v243, v252\n" \
"v_mfma_f32_16x16x32_bf16 v[128:131], v[4:7], v[212:215], v[128:131]\n" \
"ds_read_b64_tr_b16 v[54:55], v232 offset:28160\n" \
"v_cvt_pk_bf16_f32 v253, v253, v254\n" \
"v_mfma_f32_16x16x32_bf16 v[132:135], v[8:11], v[208:211], v[132:135]\n" \
"s_xor_b32 s77, s77, 0x780\n" \
"ds_read_b64_tr_b16 v[56:57], v233 offset:17920\n" \
"v_accvgpr_write_b32 a154, v243\n" \
"v_mfma_f32_16x16x32_bf16 v[132:135], v[12:15], v[212:215], v[132:135]\n" \
"buffer_store_dwordx4 a[136:139], v236, s[56:59], s93 offen offset:2048 sc1\n" \
"v_mfma_f32_16x16x32_bf16 v[136:139], v[16:19], v[208:211], v[136:139]\n" \
"ds_read_b64_tr_b16 v[58:59], v233 offset:19968\n" \
"v_accvgpr_write_b32 a155, v253\n" \
"v_mfma_f32_16x16x32_bf16 v[136:139], v[20:23], v[212:215], v[136:139]\n" \
"ds_read_b64_tr_b16 v[60:61], v233 offset:26112\n" \
"v_accvgpr_read_b32 v243, a156\n" \
"v_mfma_f32_16x16x32_bf16 v[140:143], v[24:27], v[208:211], v[140:143]\n" \
"ds_read_b64_tr_b16 v[62:63], v233 offset:28160\n" \
"v_accvgpr_read_b32 v252, a157\n" \
"v_mfma_f32_16x16x32_bf16 v[140:143], v[28:31], v[212:215], v[140:143]\n" \
"v_accvgpr_read_b32 v253, a158\n" \
"v_accvgpr_read_b32 v254, a159\n" \
"v_mfma_f32_16x16x32_bf16 v[172:175], v[24:27], v[216:219], v[172:175]\n" \
"v_mul_f32 v243, s31, v243\n" \
"v_mul_f32 v252, s31, v252\n" \
"v_mfma_f32_16x16x32_bf16 v[172:175], v[28:31], v[220:223], v[172:175]\n" \
"v_mul_f32 v253, s31, v253\n" \
"v_mul_f32 v254, s31, v254\n" \
"v_mfma_f32_16x16x32_bf16 v[168:171], v[16:19], v[216:219], v[168:171]\n" \
"v_cvt_pk_bf16_f32 v243, v243, v252\n" \
"v_cvt_pk_bf16_f32 v253, v253, v254\n" \
"v_mfma_f32_16x16x32_bf16 v[168:171], v[20:23], v[220:223], v[168:171]\n" \
"buffer_store_dwordx4 a[168:171], v236, s[56:59], s93 offen offset:2304 sc1\n" \
"v_mfma_f32_16x16x32_bf16 v[164:167], v[8:11], v[216:219], v[164:167]\n" \
"v_accvgpr_write_b32 a156, v243\n" \
"v_accvgpr_write_b32 a157, v253\n" \
"v_mfma_f32_16x16x32_bf16 v[164:167], v[12:15], v[220:223], v[164:167]\n" \
"v_accvgpr_read_b32 v243, a188\n" \
"v_accvgpr_read_b32 v252, a189\n" \
"v_mfma_f32_16x16x32_bf16 v[160:163], v[0:3], v[216:219], v[160:163]\n" \
"v_accvgpr_read_b32 v253, a190\n" \
"v_accvgpr_read_b32 v254, a191\n" \
"v_mfma_f32_16x16x32_bf16 v[160:163], v[4:7], v[220:223], v[160:163]\n" \
"s_waitcnt vmcnt(37) lgkmcnt(0)\n" \
"s_barrier\n" \
"v_mfma_f32_16x16x32_bf16 v[80:83], v[32:35], v[192:195], v[80:83]\n" \
"ds_read_b64_tr_b16 v[0:1], v232 offset:32768\n" \
"v_mul_f32 v243, s31, v243\n" \
"v_mfma_f32_16x16x32_bf16 v[80:83], v[36:39], v[196:199], v[80:83]\n" \
"ds_read_b64_tr_b16 v[2:3], v232 offset:34816\n" \
"v_mul_f32 v252, s31, v252\n" \
"v_mfma_f32_16x16x32_bf16 v[84:87], v[40:43], v[192:195], v[84:87]\n" \
"s_add_u32 m0, s79, 16384\n" \
"ds_read_b64_tr_b16 v[4:5], v232 offset:40960\n" \
"v_mul_f32 v253, s31, v253\n" \
"v_mfma_f32_16x16x32_bf16 v[84:87], v[44:47], v[196:199], v[84:87]\n" \
"buffer_load_dwordx4 v228, s[52:55], s78 offen offset:0 lds\n" \
"v_mfma_f32_16x16x32_bf16 v[88:91], v[48:51], v[192:195], v[88:91]\n" \
"ds_read_b64_tr_b16 v[6:7], v232 offset:43008\n" \
"v_mul_f32 v254, s31, v254\n" \
"v_mfma_f32_16x16x32_bf16 v[88:91], v[52:55], v[196:199], v[88:91]\n" \
"ds_read_b64_tr_b16 v[8:9], v233 offset:32768\n" \
"v_cvt_pk_bf16_f32 v243, v243, v252\n" \
"v_mfma_f32_16x16x32_bf16 v[92:95], v[56:59], v[192:195], v[92:95]\n" \
"ds_read_b64_tr_b16 v[10:11], v233 offset:34816\n" \
"v_cvt_pk_bf16_f32 v253, v253, v254\n" \
"v_mfma_f32_16x16x32_bf16 v[92:95], v[60:63], v[196:199], v[92:95]\n" \
"buffer_load_dwordx4 v229, s[52:55], s78 offen offset:1024 lds\n" \
"v_mfma_f32_16x16x32_bf16 v[124:127], v[56:59], v[200:203], v[124:127]\n" \
"s_waitcnt vmcnt(37)\n" \
"ds_read_b128 v[192:195], v250 offset:0\n" \
"v_accvgpr_write_b32 a158, v243\n" \
"v_mfma_f32_16x16x32_bf16 v[124:127], v[60:63], v[204:207], v[124:127]\n" \
"ds_read_b128 v[196:199], v251 offset:0\n" \
"v_accvgpr_write_b32 a159, v253\n" \
"v_mfma_f32_16x16x32_bf16 v[120:123], v[48:51], v[200:203], v[120:123]\n" \
"s_xor_b32 s78, s78, 0x80000\n" \
"ds_read_b64_tr_b16 v[12:13], v233 offset:40960\n" \
"v_accvgpr_read_b32 v243, a64\n" \
"v_mfma_f32_16x16x32_bf16 v[120:123], v[52:55], v[204:207], v[120:123]\n" \
"buffer_load_dwordx4 v228, s[52:55], s89 offen offset:2048 lds\n" \
"v_mfma_f32_16x16x32_bf16 v[116:119], v[40:43], v[200:203], v[116:119]\n" \
"ds_read_b64_tr_b16 v[14:15], v233 offset:43008\n" \
"v_accvgpr_read_b32 v252, a65\n" \
"v_mfma_f32_16x16x32_bf16 v[116:119], v[44:47], v[204:207], v[116:119]\n" \
"ds_read_b64_tr_b16 v[16:17], v232 offset:33280\n" \
"v_accvgpr_read_b32 v253, a66\n" \
"v_mfma_f32_16x16x32_bf16 v[112:115], v[32:35], v[200:203], v[112:115]\n" \
"ds_read_b64_tr_b16 v[18:19], v232 offset:35328\n" \
"v_accvgpr_read_b32 v254, a67\n" \
"v_mfma_f32_16x16x32_bf16 v[112:115], v[36:39], v[204:207], v[112:115]\n" \
"buffer_load_dwordx4 v229, s[52:55], s89 offen offset:3072 lds\n" \
"v_mfma_f32_16x16x32_bf16 v[144:147], v[32:35], v[208:211], v[144:147]\n" \
"s_waitcnt vmcnt(37)\n" \
"s_xor_b32 s89, s89, 0x80000\n" \
"ds_read_b128 v[200:203], v250 offset:2048\n" \
"v_mfma_f32_16x16x32_bf16 v[144:147], v[36:39], v[212:215], v[144:147]\n" \
"s_add_u32 m0, s74, 131072\n" \
"ds_read_b128 v[204:207], v251 offset:2048\n" \
"v_mul_f32 v243, s76, v243\n" \
"v_mfma_f32_16x16x32_bf16 v[148:151], v[40:43], v[208:211], v[148:151]\n" \
"s_waitcnt lgkmcnt(6)\n" \
"ds_read_b64_tr_b16 v[20:21], v232 offset:41472\n" \
"v_mul_f32 v252, s76, v252\n" \
"v_mfma_f32_16x16x32_bf16 v[148:151], v[44:47], v[212:215], v[148:151]\n" \
"buffer_load_dwordx4 v224, s[48:51], s75 offen offset:0 lds\n" \
"v_mfma_f32_16x16x32_bf16 v[152:155], v[48:51], v[208:211], v[152:155]\n" \
"ds_read_b64_tr_b16 v[22:23], v232 offset:43520\n" \
"v_mul_f32 v253, s76, v253\n" \
"v_mfma_f32_16x16x32_bf16 v[152:155], v[52:55], v[212:215], v[152:155]\n" \
"ds_read_b64_tr_b16 v[24:25], v233 offset:33280\n" \
"v_mul_f32 v254, s76, v254\n" \
"v_mfma_f32_16x16x32_bf16 v[156:159], v[56:59], v[208:211], v[156:159]\n" \
"ds_read_b64_tr_b16 v[26:27], v233 offset:35328\n" \
"v_cvt_pk_bf16_f32 v243, v243, v252\n" \
"v_mfma_f32_16x16x32_bf16 v[156:159], v[60:63], v[212:215], v[156:159]\n" \
"buffer_load_dwordx4 v225, s[48:51], s75 offen offset:1024 lds\n" \
"v_mfma_f32_16x16x32_bf16 v[188:191], v[56:59], v[216:219], v[188:191]\n" \
"s_waitcnt vmcnt(37)\n" \
"ds_read_b128 v[208:211], v250 offset:4096\n" \
"v_cvt_pk_bf16_f32 v253, v253, v254\n" \
"v_mfma_f32_16x16x32_bf16 v[188:191], v[60:63], v[220:223], v[188:191]\n" \
"ds_read_b128 v[212:215], v251 offset:4096\n" \
"v_accvgpr_write_b32 a64, v243\n" \
"v_mfma_f32_16x16x32_bf16 v[184:187], v[48:51], v[216:219], v[184:187]\n" \
"s_waitcnt lgkmcnt(6)\n" \
"ds_read_b64_tr_b16 v[28:29], v233 offset:41472\n" \
"v_accvgpr_write_b32 a65, v253\n" \
"v_mfma_f32_16x16x32_bf16 v[184:187], v[52:55], v[220:223], v[184:187]\n" \
"buffer_load_dwordx4 v226, s[48:51], s75 offen offset:2048 lds\n" \
"v_mfma_f32_16x16x32_bf16 v[180:183], v[40:43], v[216:219], v[180:183]\n" \
"ds_read_b64_tr_b16 v[30:31], v233 offset:43520\n" \
"v_accvgpr_read_b32 v243, a68\n" \
"v_mfma_f32_16x16x32_bf16 v[180:183], v[44:47], v[220:223], v[180:183]\n" \
"v_accvgpr_read_b32 v252, a69\n" \
"v_accvgpr_read_b32 v253, a70\n" \
"v_mfma_f32_16x16x32_bf16 v[176:179], v[32:35], v[216:219], v[176:179]\n" \
"v_accvgpr_read_b32 v254, a71\n" \
"v_mul_f32 v243, s76, v243\n" \
"v_mfma_f32_16x16x32_bf16 v[176:179], v[36:39], v[220:223], v[176:179]\n" \
"buffer_load_dwordx4 v227, s[48:51], s75 offen offset:3072 lds\n" \
"v_mfma_f32_16x16x32_bf16 v[64:67], v[0:3], v[192:195], v[64:67]\n" \
"s_waitcnt vmcnt(37)\n" \
"s_xor_b32 s75, s75, 0x80\n" \
"ds_read_b128 v[216:219], v250 offset:6144\n" \
"Lpg3303_%=:\n" \
"s_cbranch_execz Lpg5565_%=\n" \
"v_mfma_f32_16x16x32_bf16 v[64:67], v[4:7], v[196:199], v[64:67]\n" \
"s_add_u32 m0, s74, 135168\n" \
"ds_read_b128 v[220:223], v251 offset:6144\n" \
"v_mul_f32 v252, s76, v252\n" \
"v_mfma_f32_16x16x32_bf16 v[68:71], v[8:11], v[192:195], v[68:71]\n" \
"s_waitcnt lgkmcnt(4)\n" \
"ds_read_b64_tr_b16 v[32:33], v232 offset:33792\n" \
"v_mul_f32 v253, s76, v253\n" \
"v_mfma_f32_16x16x32_bf16 v[68:71], v[12:15], v[196:199], v[68:71]\n" \
"buffer_load_dwordx4 v224, s[48:51], s77 offen offset:0 lds\n" \
"v_mfma_f32_16x16x32_bf16 v[72:75], v[16:19], v[192:195], v[72:75]\n" \
"ds_read_b64_tr_b16 v[34:35], v232 offset:35840\n" \
"v_mul_f32 v254, s76, v254\n" \
"v_mfma_f32_16x16x32_bf16 v[72:75], v[20:23], v[196:199], v[72:75]\n" \
"s_waitcnt lgkmcnt(3)\n" \
"ds_read_b64_tr_b16 v[36:37], v232 offset:41984\n" \
"v_cvt_pk_bf16_f32 v243, v243, v252\n" \
"v_mfma_f32_16x16x32_bf16 v[76:79], v[24:27], v[192:195], v[76:79]\n" \
"ds_read_b64_tr_b16 v[38:39], v232 offset:44032\n" \
"v_cvt_pk_bf16_f32 v253, v253, v254\n" \
"v_mfma_f32_16x16x32_bf16 v[76:79], v[28:31], v[196:199], v[76:79]\n" \
"buffer_load_dwordx4 v225, s[48:51], s77 offen offset:1024 lds\n" \
"v_mfma_f32_16x16x32_bf16 v[108:111], v[24:27], v[200:203], v[108:111]\n" \
"ds_read_b64_tr_b16 v[40:41], v233 offset:33792\n" \
"v_accvgpr_write_b32 a66, v243\n" \
"v_mfma_f32_16x16x32_bf16 v[108:111], v[28:31], v[204:207], v[108:111]\n" \
"ds_read_b64_tr_b16 v[42:43], v233 offset:35840\n" \
"v_accvgpr_write_b32 a67, v253\n" \
"v_mfma_f32_16x16x32_bf16 v[104:107], v[16:19], v[200:203], v[104:107]\n" \
"s_waitcnt lgkmcnt(6)\n" \
"ds_read_b64_tr_b16 v[44:45], v233 offset:41984\n" \
"v_accvgpr_read_b32 v243, a96\n" \
"v_mfma_f32_16x16x32_bf16 v[104:107], v[20:23], v[204:207], v[104:107]\n" \
"buffer_load_dwordx4 v226, s[48:51], s77 offen offset:2048 lds\n" \
"v_mfma_f32_16x16x32_bf16 v[100:103], v[8:11], v[200:203], v[100:103]\n" \
"ds_read_b64_tr_b16 v[46:47], v233 offset:44032\n" \
"v_accvgpr_read_b32 v252, a97\n" \
"v_mfma_f32_16x16x32_bf16 v[100:103], v[12:15], v[204:207], v[100:103]\n" \
"ds_read_b64_tr_b16 v[48:49], v232 offset:34304\n" \
"v_accvgpr_read_b32 v253, a98\n" \
"v_mfma_f32_16x16x32_bf16 v[96:99], v[0:3], v[200:203], v[96:99]\n" \
"ds_read_b64_tr_b16 v[50:51], v232 offset:36352\n" \
"v_accvgpr_read_b32 v254, a99\n" \
"v_mfma_f32_16x16x32_bf16 v[96:99], v[4:7], v[204:207], v[96:99]\n" \
"buffer_load_dwordx4 v227, s[48:51], s77 offen offset:3072 lds\n" \
"v_mfma_f32_16x16x32_bf16 v[128:131], v[0:3], v[208:211], v[128:131]\n" \
"ds_read_b64_tr_b16 v[52:53], v232 offset:42496\n" \
"v_mul_f32 v243, s76, v243\n" \
"v_mfma_f32_16x16x32_bf16 v[128:131], v[4:7], v[212:215], v[128:131]\n" \
"ds_read_b64_tr_b16 v[54:55], v232 offset:44544\n" \
"v_mul_f32 v252, s76, v252\n" \
"v_mfma_f32_16x16x32_bf16 v[132:135], v[8:11], v[208:211], v[132:135]\n" \
"s_xor_b32 s77, s77, 0x80\n" \
"ds_read_b64_tr_b16 v[56:57], v233 offset:34304\n" \
"v_mul_f32 v253, s76, v253\n" \
"v_mfma_f32_16x16x32_bf16 v[132:135], v[12:15], v[212:215], v[132:135]\n" \
"v_mul_f32 v254, s76, v254\n" \
"v_cvt_pk_bf16_f32 v243, v243, v252\n" \
"v_mfma_f32_16x16x32_bf16 v[136:139], v[16:19], v[208:211], v[136:139]\n" \
"ds_read_b64_tr_b16 v[58:59], v233 offset:36352\n" \
"v_cvt_pk_bf16_f32 v253, v253, v254\n" \
"v_mfma_f32_16x16x32_bf16 v[136:139], v[20:23], v[212:215], v[136:139]\n" \
"ds_read_b64_tr_b16 v[60:61], v233 offset:42496\n" \
"v_accvgpr_write_b32 a96, v243\n" \
"v_mfma_f32_16x16x32_bf16 v[140:143], v[24:27], v[208:211], v[140:143]\n" \
"s_add_u32 s93, s87, 17858560\n" \
"ds_read_b64_tr_b16 v[62:63], v233 offset:44544\n" \
"v_accvgpr_write_b32 a97, v253\n" \
"v_mfma_f32_16x16x32_bf16 v[140:143], v[28:31], v[212:215], v[140:143]\n" \
"buffer_store_dwordx4 a[208:211], v234, s[56:59], s93 offen offset:0 sc1\n" \
"v_mfma_f32_16x16x32_bf16 v[172:175], v[24:27], v[216:219], v[172:175]\n" \
"v_accvgpr_read_b32 v243, a100\n" \
"v_accvgpr_read_b32 v252, a101\n" \
"v_mfma_f32_16x16x32_bf16 v[172:175], v[28:31], v[220:223], v[172:175]\n" \
"v_accvgpr_read_b32 v253, a102\n" \
"v_accvgpr_read_b32 v254, a103\n" \
"v_mfma_f32_16x16x32_bf16 v[168:171], v[16:19], v[216:219], v[168:171]\n" \
"v_mul_f32 v243, s76, v243\n" \
"v_mul_f32 v252, s76, v252\n" \
"v_mfma_f32_16x16x32_bf16 v[168:171], v[20:23], v[220:223], v[168:171]\n" \
"v_mul_f32 v253, s76, v253\n" \
"v_mul_f32 v254, s76, v254\n" \
"v_mfma_f32_16x16x32_bf16 v[164:167], v[8:11], v[216:219], v[164:167]\n" \
"v_cvt_pk_bf16_f32 v243, v243, v252\n" \
"v_cvt_pk_bf16_f32 v253, v253, v254\n" \
"v_mfma_f32_16x16x32_bf16 v[164:167], v[12:15], v[220:223], v[164:167]\n" \
"v_accvgpr_write_b32 a98, v243\n" \
"v_accvgpr_write_b32 a99, v253\n" \
"v_mfma_f32_16x16x32_bf16 v[160:163], v[0:3], v[216:219], v[160:163]\n" \
"v_accvgpr_read_b32 v243, a72\n" \
"v_accvgpr_read_b32 v252, a73\n" \
"v_mfma_f32_16x16x32_bf16 v[160:163], v[4:7], v[220:223], v[160:163]\n" \
"s_waitcnt vmcnt(36) lgkmcnt(0)\n" \
"s_barrier\n" \
"v_mfma_f32_16x16x32_bf16 v[80:83], v[32:35], v[192:195], v[80:83]\n" \
"ds_read_b64_tr_b16 v[0:1], v232 offset:49152\n" \
"v_accvgpr_read_b32 v253, a74\n" \
"v_mfma_f32_16x16x32_bf16 v[80:83], v[36:39], v[196:199], v[80:83]\n" \
"ds_read_b64_tr_b16 v[2:3], v232 offset:51200\n" \
"v_accvgpr_read_b32 v254, a75\n" \
"v_mfma_f32_16x16x32_bf16 v[84:87], v[40:43], v[192:195], v[84:87]\n" \
"s_add_u32 m0, s79, 32768\n" \
"ds_read_b64_tr_b16 v[4:5], v232 offset:57344\n" \
"v_mul_f32 v243, s76, v243\n" \
"v_mfma_f32_16x16x32_bf16 v[84:87], v[44:47], v[196:199], v[84:87]\n" \
"buffer_load_dwordx4 v228, s[52:55], s78 offen offset:0 lds\n" \
"v_mfma_f32_16x16x32_bf16 v[88:91], v[48:51], v[192:195], v[88:91]\n" \
"ds_read_b64_tr_b16 v[6:7], v232 offset:59392\n" \
"v_mul_f32 v252, s76, v252\n" \
"v_mfma_f32_16x16x32_bf16 v[88:91], v[52:55], v[196:199], v[88:91]\n" \
"ds_read_b64_tr_b16 v[8:9], v233 offset:49152\n" \
"v_mul_f32 v253, s76, v253\n" \
"v_mfma_f32_16x16x32_bf16 v[92:95], v[56:59], v[192:195], v[92:95]\n" \
"ds_read_b64_tr_b16 v[10:11], v233 offset:51200\n" \
"v_mul_f32 v254, s76, v254\n" \
"v_mfma_f32_16x16x32_bf16 v[92:95], v[60:63], v[196:199], v[92:95]\n" \
"buffer_load_dwordx4 v229, s[52:55], s78 offen offset:1024 lds\n" \
"v_mfma_f32_16x16x32_bf16 v[124:127], v[56:59], v[200:203], v[124:127]\n" \
"s_waitcnt vmcnt(36)\n" \
"ds_read_b128 v[192:195], v230 offset:0\n" \
"v_cvt_pk_bf16_f32 v243, v243, v252\n" \
"v_mfma_f32_16x16x32_bf16 v[124:127], v[60:63], v[204:207], v[124:127]\n" \
"ds_read_b128 v[196:199], v231 offset:0\n" \
"v_cvt_pk_bf16_f32 v253, v253, v254\n" \
"v_mfma_f32_16x16x32_bf16 v[120:123], v[48:51], v[200:203], v[120:123]\n" \
"s_xor_b32 s78, s78, 0x180000\n" \
"ds_read_b64_tr_b16 v[12:13], v233 offset:57344\n" \
"v_accvgpr_write_b32 a72, v243\n" \
"v_mfma_f32_16x16x32_bf16 v[120:123], v[52:55], v[204:207], v[120:123]\n" \
"buffer_load_dwordx4 v228, s[52:55], s89 offen offset:2048 lds\n" \
"v_mfma_f32_16x16x32_bf16 v[116:119], v[40:43], v[200:203], v[116:119]\n" \
"ds_read_b64_tr_b16 v[14:15], v233 offset:59392\n" \
"v_accvgpr_write_b32 a73, v253\n" \
"v_mfma_f32_16x16x32_bf16 v[116:119], v[44:47], v[204:207], v[116:119]\n" \
"ds_read_b64_tr_b16 v[16:17], v232 offset:49664\n" \
"v_accvgpr_read_b32 v243, a76\n" \
"v_mfma_f32_16x16x32_bf16 v[112:115], v[32:35], v[200:203], v[112:115]\n" \
"ds_read_b64_tr_b16 v[18:19], v232 offset:51712\n" \
"v_accvgpr_read_b32 v252, a77\n" \
"v_mfma_f32_16x16x32_bf16 v[112:115], v[36:39], v[204:207], v[112:115]\n" \
"buffer_load_dwordx4 v229, s[52:55], s89 offen offset:3072 lds\n" \
"v_mfma_f32_16x16x32_bf16 v[144:147], v[32:35], v[208:211], v[144:147]\n" \
"s_waitcnt vmcnt(36)\n" \
"s_xor_b32 s89, s89, 0x180000\n" \
"ds_read_b128 v[200:203], v230 offset:2048\n" \
"v_mfma_f32_16x16x32_bf16 v[144:147], v[36:39], v[212:215], v[144:147]\n" \
"s_add_u32 m0, s74, 65536\n" \
"ds_read_b128 v[204:207], v231 offset:2048\n" \
"v_accvgpr_read_b32 v253, a78\n" \
"v_mfma_f32_16x16x32_bf16 v[148:151], v[40:43], v[208:211], v[148:151]\n" \
"s_waitcnt lgkmcnt(6)\n" \
"ds_read_b64_tr_b16 v[20:21], v232 offset:57856\n" \
"v_accvgpr_read_b32 v254, a79\n" \
"v_mfma_f32_16x16x32_bf16 v[148:151], v[44:47], v[212:215], v[148:151]\n" \
"buffer_load_dwordx4 v224, s[48:51], s75 offen offset:0 lds\n" \
"v_mfma_f32_16x16x32_bf16 v[152:155], v[48:51], v[208:211], v[152:155]\n" \
"ds_read_b64_tr_b16 v[22:23], v232 offset:59904\n" \
"v_mul_f32 v243, s76, v243\n" \
"v_mfma_f32_16x16x32_bf16 v[152:155], v[52:55], v[212:215], v[152:155]\n" \
"ds_read_b64_tr_b16 v[24:25], v233 offset:49664\n" \
"v_mul_f32 v252, s76, v252\n" \
"v_mfma_f32_16x16x32_bf16 v[156:159], v[56:59], v[208:211], v[156:159]\n" \
"ds_read_b64_tr_b16 v[26:27], v233 offset:51712\n" \
"v_mul_f32 v253, s76, v253\n" \
"v_mfma_f32_16x16x32_bf16 v[156:159], v[60:63], v[212:215], v[156:159]\n" \
"buffer_load_dwordx4 v225, s[48:51], s75 offen offset:1024 lds\n" \
"v_mfma_f32_16x16x32_bf16 v[188:191], v[56:59], v[216:219], v[188:191]\n" \
"s_waitcnt vmcnt(36)\n" \
"ds_read_b128 v[208:211], v230 offset:4096\n" \
"v_mul_f32 v254, s76, v254\n" \
"v_mfma_f32_16x16x32_bf16 v[188:191], v[60:63], v[220:223], v[188:191]\n" \
"ds_read_b128 v[212:215], v231 offset:4096\n" \
"v_cvt_pk_bf16_f32 v243, v243, v252\n" \
"v_mfma_f32_16x16x32_bf16 v[184:187], v[48:51], v[216:219], v[184:187]\n" \
"s_waitcnt lgkmcnt(6)\n" \
"ds_read_b64_tr_b16 v[28:29], v233 offset:57856\n" \
"v_cvt_pk_bf16_f32 v253, v253, v254\n" \
"v_mfma_f32_16x16x32_bf16 v[184:187], v[52:55], v[220:223], v[184:187]\n" \
"buffer_load_dwordx4 v226, s[48:51], s75 offen offset:2048 lds\n" \
"v_mfma_f32_16x16x32_bf16 v[180:183], v[40:43], v[216:219], v[180:183]\n" \
"ds_read_b64_tr_b16 v[30:31], v233 offset:59904\n" \
"v_accvgpr_write_b32 a74, v243\n" \
"v_mfma_f32_16x16x32_bf16 v[180:183], v[44:47], v[220:223], v[180:183]\n" \
"v_accvgpr_write_b32 a75, v253\n" \
"v_accvgpr_read_b32 v243, a104\n" \
"v_mfma_f32_16x16x32_bf16 v[176:179], v[32:35], v[216:219], v[176:179]\n" \
"v_accvgpr_read_b32 v252, a105\n" \
"v_accvgpr_read_b32 v253, a106\n" \
"v_mfma_f32_16x16x32_bf16 v[176:179], v[36:39], v[220:223], v[176:179]\n" \
"buffer_load_dwordx4 v227, s[48:51], s75 offen offset:3072 lds\n" \
"v_mfma_f32_16x16x32_bf16 v[64:67], v[0:3], v[192:195], v[64:67]\n" \
"s_waitcnt vmcnt(36)\n" \
"s_xor_b32 s75, s75, 0x180\n" \
"ds_read_b128 v[216:219], v230 offset:6144\n" \
"v_mfma_f32_16x16x32_bf16 v[64:67], v[4:7], v[196:199], v[64:67]\n" \
"s_add_u32 m0, s74, 69632\n" \
"ds_read_b128 v[220:223], v231 offset:6144\n" \
"v_accvgpr_read_b32 v254, a107\n" \
"v_mfma_f32_16x16x32_bf16 v[68:71], v[8:11], v[192:195], v[68:71]\n" \
"s_waitcnt lgkmcnt(4)\n" \
"ds_read_b64_tr_b16 v[32:33], v232 offset:50176\n" \
"v_mul_f32 v243, s76, v243\n" \
"v_mfma_f32_16x16x32_bf16 v[68:71], v[12:15], v[196:199], v[68:71]\n" \
"buffer_load_dwordx4 v224, s[48:51], s77 offen offset:0 lds\n" \
"v_mfma_f32_16x16x32_bf16 v[72:75], v[16:19], v[192:195], v[72:75]\n" \
"ds_read_b64_tr_b16 v[34:35], v232 offset:52224\n" \
"v_mul_f32 v252, s76, v252\n" \
"v_mfma_f32_16x16x32_bf16 v[72:75], v[20:23], v[196:199], v[72:75]\n" \
"s_waitcnt lgkmcnt(3)\n" \
"ds_read_b64_tr_b16 v[36:37], v232 offset:58368\n" \
"v_mul_f32 v253, s76, v253\n" \
"v_mfma_f32_16x16x32_bf16 v[76:79], v[24:27], v[192:195], v[76:79]\n" \
"ds_read_b64_tr_b16 v[38:39], v232 offset:60416\n" \
"v_mul_f32 v254, s76, v254\n" \
"v_mfma_f32_16x16x32_bf16 v[76:79], v[28:31], v[196:199], v[76:79]\n" \
"buffer_load_dwordx4 v225, s[48:51], s77 offen offset:1024 lds\n" \
"v_mfma_f32_16x16x32_bf16 v[108:111], v[24:27], v[200:203], v[108:111]\n" \
"ds_read_b64_tr_b16 v[40:41], v233 offset:50176\n" \
"v_cvt_pk_bf16_f32 v243, v243, v252\n" \
"v_mfma_f32_16x16x32_bf16 v[108:111], v[28:31], v[204:207], v[108:111]\n" \
"ds_read_b64_tr_b16 v[42:43], v233 offset:52224\n" \
"v_cvt_pk_bf16_f32 v253, v253, v254\n" \
"v_mfma_f32_16x16x32_bf16 v[104:107], v[16:19], v[200:203], v[104:107]\n" \
"s_waitcnt lgkmcnt(6)\n" \
"ds_read_b64_tr_b16 v[44:45], v233 offset:58368\n" \
"v_accvgpr_write_b32 a104, v243\n" \
"v_mfma_f32_16x16x32_bf16 v[104:107], v[20:23], v[204:207], v[104:107]\n" \
"buffer_load_dwordx4 v226, s[48:51], s77 offen offset:2048 lds\n" \
"v_mfma_f32_16x16x32_bf16 v[100:103], v[8:11], v[200:203], v[100:103]\n" \
"ds_read_b64_tr_b16 v[46:47], v233 offset:60416\n" \
"v_accvgpr_write_b32 a105, v253\n" \
"v_mfma_f32_16x16x32_bf16 v[100:103], v[12:15], v[204:207], v[100:103]\n" \
"ds_read_b64_tr_b16 v[48:49], v232 offset:50688\n" \
"v_accvgpr_read_b32 v243, a108\n" \
"v_mfma_f32_16x16x32_bf16 v[96:99], v[0:3], v[200:203], v[96:99]\n" \
"ds_read_b64_tr_b16 v[50:51], v232 offset:52736\n" \
"v_accvgpr_read_b32 v252, a109\n" \
"v_mfma_f32_16x16x32_bf16 v[96:99], v[4:7], v[204:207], v[96:99]\n" \
"buffer_load_dwordx4 v227, s[48:51], s77 offen offset:3072 lds\n" \
"v_mfma_f32_16x16x32_bf16 v[128:131], v[0:3], v[208:211], v[128:131]\n" \
"ds_read_b64_tr_b16 v[52:53], v232 offset:58880\n" \
"v_accvgpr_read_b32 v253, a110\n" \
"v_mfma_f32_16x16x32_bf16 v[128:131], v[4:7], v[212:215], v[128:131]\n" \
"ds_read_b64_tr_b16 v[54:55], v232 offset:60928\n" \
"v_accvgpr_read_b32 v254, a111\n" \
"v_mfma_f32_16x16x32_bf16 v[132:135], v[8:11], v[208:211], v[132:135]\n" \
"s_xor_b32 s77, s77, 0x180\n" \
"ds_read_b64_tr_b16 v[56:57], v233 offset:50688\n" \
"v_mul_f32 v243, s76, v243\n" \
"v_mfma_f32_16x16x32_bf16 v[132:135], v[12:15], v[212:215], v[132:135]\n" \
"buffer_store_dwordx4 a[212:215], v234, s[56:59], s93 offen offset:1024 sc1\n" \
"v_mfma_f32_16x16x32_bf16 v[136:139], v[16:19], v[208:211], v[136:139]\n" \
"ds_read_b64_tr_b16 v[58:59], v233 offset:52736\n" \
"v_mul_f32 v252, s76, v252\n" \
"v_mfma_f32_16x16x32_bf16 v[136:139], v[20:23], v[212:215], v[136:139]\n" \
"ds_read_b64_tr_b16 v[60:61], v233 offset:58880\n" \
"v_mul_f32 v253, s76, v253\n" \
"v_mfma_f32_16x16x32_bf16 v[140:143], v[24:27], v[208:211], v[140:143]\n" \
"ds_read_b64_tr_b16 v[62:63], v233 offset:60928\n" \
"v_mul_f32 v254, s76, v254\n" \
"v_mfma_f32_16x16x32_bf16 v[140:143], v[28:31], v[212:215], v[140:143]\n" \
"v_cvt_pk_bf16_f32 v243, v243, v252\n" \
"v_cvt_pk_bf16_f32 v253, v253, v254\n" \
"v_mfma_f32_16x16x32_bf16 v[172:175], v[24:27], v[216:219], v[172:175]\n" \
"v_accvgpr_write_b32 a106, v243\n" \
"v_accvgpr_write_b32 a107, v253\n" \
"v_mfma_f32_16x16x32_bf16 v[172:175], v[28:31], v[220:223], v[172:175]\n" \
"v_accvgpr_read_b32 v243, a0\n" \
"v_accvgpr_read_b32 v252, a1\n" \
"v_mfma_f32_16x16x32_bf16 v[168:171], v[16:19], v[216:219], v[168:171]\n" \
"v_accvgpr_read_b32 v253, a2\n" \
"v_accvgpr_read_b32 v254, a3\n" \
"v_mfma_f32_16x16x32_bf16 v[168:171], v[20:23], v[220:223], v[168:171]\n" \
"buffer_store_dwordx4 a[216:219], v234, s[56:59], s93 offen offset:2048 sc1\n" \
"v_mfma_f32_16x16x32_bf16 v[164:167], v[8:11], v[216:219], v[164:167]\n" \
"v_mul_f32 v243, s76, v243\n" \
"v_mul_f32 v252, s76, v252\n" \
"v_mfma_f32_16x16x32_bf16 v[164:167], v[12:15], v[220:223], v[164:167]\n" \
"v_mul_f32 v253, s76, v253\n" \
"v_mul_f32 v254, s76, v254\n" \
"v_mfma_f32_16x16x32_bf16 v[160:163], v[0:3], v[216:219], v[160:163]\n" \
"v_cvt_pk_bf16_f32 v243, v243, v252\n" \
"v_cvt_pk_bf16_f32 v253, v253, v254\n" \
"v_mfma_f32_16x16x32_bf16 v[160:163], v[4:7], v[220:223], v[160:163]\n" \
"s_waitcnt vmcnt(37) lgkmcnt(0)\n" \
"s_barrier\n" \
"v_mfma_f32_16x16x32_bf16 v[80:83], v[32:35], v[192:195], v[80:83]\n" \
"ds_read_b64_tr_b16 v[0:1], v232 offset:0\n" \
"v_accvgpr_write_b32 a0, v243\n" \
"v_mfma_f32_16x16x32_bf16 v[80:83], v[36:39], v[196:199], v[80:83]\n" \
"ds_read_b64_tr_b16 v[2:3], v232 offset:2048\n" \
"v_accvgpr_write_b32 a1, v253\n" \
"v_mfma_f32_16x16x32_bf16 v[84:87], v[40:43], v[192:195], v[84:87]\n" \
"s_add_u32 m0, s79, 49152\n" \
"ds_read_b64_tr_b16 v[4:5], v232 offset:8192\n" \
"v_accvgpr_read_b32 v243, a4\n" \
"v_mfma_f32_16x16x32_bf16 v[84:87], v[44:47], v[196:199], v[84:87]\n" \
"buffer_load_dwordx4 v228, s[52:55], s78 offen offset:0 lds\n" \
"v_mfma_f32_16x16x32_bf16 v[88:91], v[48:51], v[192:195], v[88:91]\n" \
"ds_read_b64_tr_b16 v[6:7], v232 offset:10240\n" \
"v_accvgpr_read_b32 v252, a5\n" \
"v_mfma_f32_16x16x32_bf16 v[88:91], v[52:55], v[196:199], v[88:91]\n" \
"ds_read_b64_tr_b16 v[8:9], v233 offset:0\n" \
"v_accvgpr_read_b32 v253, a6\n" \
"v_mfma_f32_16x16x32_bf16 v[92:95], v[56:59], v[192:195], v[92:95]\n" \
"ds_read_b64_tr_b16 v[10:11], v233 offset:2048\n" \
"v_accvgpr_read_b32 v254, a7\n" \
"v_mfma_f32_16x16x32_bf16 v[92:95], v[60:63], v[196:199], v[92:95]\n" \
"buffer_load_dwordx4 v229, s[52:55], s78 offen offset:1024 lds\n" \
"v_mfma_f32_16x16x32_bf16 v[124:127], v[56:59], v[200:203], v[124:127]\n" \
"s_waitcnt vmcnt(37)\n" \
"ds_read_b128 v[192:195], v230 offset:32768\n" \
"v_mul_f32 v243, s76, v243\n" \
"v_mfma_f32_16x16x32_bf16 v[124:127], v[60:63], v[204:207], v[124:127]\n" \
"ds_read_b128 v[196:199], v231 offset:32768\n" \
"v_mul_f32 v252, s76, v252\n" \
"v_mfma_f32_16x16x32_bf16 v[120:123], v[48:51], v[200:203], v[120:123]\n" \
"s_xor_b32 s78, s78, 0x300000\n" \
"ds_read_b64_tr_b16 v[12:13], v233 offset:8192\n" \
"v_mul_f32 v253, s76, v253\n" \
"v_mfma_f32_16x16x32_bf16 v[120:123], v[52:55], v[204:207], v[120:123]\n" \
"buffer_load_dwordx4 v228, s[52:55], s89 offen offset:2048 lds\n" \
"v_mfma_f32_16x16x32_bf16 v[116:119], v[40:43], v[200:203], v[116:119]\n" \
"ds_read_b64_tr_b16 v[14:15], v233 offset:10240\n" \
"v_mul_f32 v254, s76, v254\n" \
"v_mfma_f32_16x16x32_bf16 v[116:119], v[44:47], v[204:207], v[116:119]\n" \
"ds_read_b64_tr_b16 v[16:17], v232 offset:512\n" \
"v_cvt_pk_bf16_f32 v243, v243, v252\n" \
"v_mfma_f32_16x16x32_bf16 v[112:115], v[32:35], v[200:203], v[112:115]\n" \
"ds_read_b64_tr_b16 v[18:19], v232 offset:2560\n" \
"v_cvt_pk_bf16_f32 v253, v253, v254\n" \
"v_mfma_f32_16x16x32_bf16 v[112:115], v[36:39], v[204:207], v[112:115]\n" \
"buffer_load_dwordx4 v229, s[52:55], s89 offen offset:3072 lds\n" \
"v_mfma_f32_16x16x32_bf16 v[144:147], v[32:35], v[208:211], v[144:147]\n" \
"s_waitcnt vmcnt(37)\n" \
"s_xor_b32 s89, s89, 0x300000\n" \
"ds_read_b128 v[200:203], v230 offset:34816\n" \
"v_mfma_f32_16x16x32_bf16 v[144:147], v[36:39], v[212:215], v[144:147]\n" \
"s_add_u32 m0, s74, 98304\n" \
"ds_read_b128 v[204:207], v231 offset:34816\n" \
"v_accvgpr_write_b32 a2, v243\n" \
"v_mfma_f32_16x16x32_bf16 v[148:151], v[40:43], v[208:211], v[148:151]\n" \
"s_waitcnt lgkmcnt(6)\n" \
"ds_read_b64_tr_b16 v[20:21], v232 offset:8704\n" \
"v_accvgpr_write_b32 a3, v253\n" \
"v_mfma_f32_16x16x32_bf16 v[148:151], v[44:47], v[212:215], v[148:151]\n" \
"buffer_load_dwordx4 v224, s[48:51], s75 offen offset:0 lds\n" \
"v_mfma_f32_16x16x32_bf16 v[152:155], v[48:51], v[208:211], v[152:155]\n" \
"ds_read_b64_tr_b16 v[22:23], v232 offset:10752\n" \
"v_accvgpr_read_b32 v243, a32\n" \
"v_mfma_f32_16x16x32_bf16 v[152:155], v[52:55], v[212:215], v[152:155]\n" \
"ds_read_b64_tr_b16 v[24:25], v233 offset:512\n" \
"v_accvgpr_read_b32 v252, a33\n" \
"v_mfma_f32_16x16x32_bf16 v[156:159], v[56:59], v[208:211], v[156:159]\n" \
"ds_read_b64_tr_b16 v[26:27], v233 offset:2560\n" \
"v_accvgpr_read_b32 v253, a34\n" \
"v_mfma_f32_16x16x32_bf16 v[156:159], v[60:63], v[212:215], v[156:159]\n" \
"buffer_load_dwordx4 v225, s[48:51], s75 offen offset:1024 lds\n" \
"v_mfma_f32_16x16x32_bf16 v[188:191], v[56:59], v[216:219], v[188:191]\n" \
"s_waitcnt vmcnt(37)\n" \
"ds_read_b128 v[208:211], v230 offset:36864\n" \
"v_accvgpr_read_b32 v254, a35\n" \
"v_mfma_f32_16x16x32_bf16 v[188:191], v[60:63], v[220:223], v[188:191]\n" \
"ds_read_b128 v[212:215], v231 offset:36864\n" \
"v_mul_f32 v243, s76, v243\n" \
"v_mfma_f32_16x16x32_bf16 v[184:187], v[48:51], v[216:219], v[184:187]\n" \
"s_waitcnt lgkmcnt(6)\n" \
"ds_read_b64_tr_b16 v[28:29], v233 offset:8704\n" \
"v_mul_f32 v252, s76, v252\n" \
"v_mfma_f32_16x16x32_bf16 v[184:187], v[52:55], v[220:223], v[184:187]\n" \
"buffer_load_dwordx4 v226, s[48:51], s75 offen offset:2048 lds\n" \
"v_mfma_f32_16x16x32_bf16 v[180:183], v[40:43], v[216:219], v[180:183]\n" \
"ds_read_b64_tr_b16 v[30:31], v233 offset:10752\n" \
"v_mul_f32 v253, s76, v253\n" \
"v_mfma_f32_16x16x32_bf16 v[180:183], v[44:47], v[220:223], v[180:183]\n" \
"v_mul_f32 v254, s76, v254\n" \
"v_cvt_pk_bf16_f32 v243, v243, v252\n" \
"v_mfma_f32_16x16x32_bf16 v[176:179], v[32:35], v[216:219], v[176:179]\n" \
"v_cvt_pk_bf16_f32 v253, v253, v254\n" \
"v_accvgpr_write_b32 a32, v243\n" \
"v_mfma_f32_16x16x32_bf16 v[176:179], v[36:39], v[220:223], v[176:179]\n" \
"buffer_load_dwordx4 v227, s[48:51], s75 offen offset:3072 lds\n" \
"v_mfma_f32_16x16x32_bf16 v[64:67], v[0:3], v[192:195], v[64:67]\n" \
"s_waitcnt vmcnt(37)\n" \
"s_xor_b32 s75, s75, 0x300\n" \
"ds_read_b128 v[216:219], v230 offset:38912\n" \
"v_mfma_f32_16x16x32_bf16 v[64:67], v[4:7], v[196:199], v[64:67]\n" \
"s_add_u32 m0, s74, 102400\n" \
"ds_read_b128 v[220:223], v231 offset:38912\n" \
"v_accvgpr_write_b32 a33, v253\n" \
"v_mfma_f32_16x16x32_bf16 v[68:71], v[8:11], v[192:195], v[68:71]\n" \
"s_waitcnt lgkmcnt(4)\n" \
"ds_read_b64_tr_b16 v[32:33], v232 offset:1024\n" \
"v_accvgpr_read_b32 v243, a36\n" \
"v_mfma_f32_16x16x32_bf16 v[68:71], v[12:15], v[196:199], v[68:71]\n" \
"buffer_load_dwordx4 v224, s[48:51], s77 offen offset:0 lds\n" \
"v_mfma_f32_16x16x32_bf16 v[72:75], v[16:19], v[192:195], v[72:75]\n" \
"ds_read_b64_tr_b16 v[34:35], v232 offset:3072\n" \
"v_accvgpr_read_b32 v252, a37\n" \
"v_mfma_f32_16x16x32_bf16 v[72:75], v[20:23], v[196:199], v[72:75]\n" \
"s_waitcnt lgkmcnt(3)\n" \
"ds_read_b64_tr_b16 v[36:37], v232 offset:9216\n" \
"v_accvgpr_read_b32 v253, a38\n" \
"v_mfma_f32_16x16x32_bf16 v[76:79], v[24:27], v[192:195], v[76:79]\n" \
"ds_read_b64_tr_b16 v[38:39], v232 offset:11264\n" \
"v_accvgpr_read_b32 v254, a39\n" \
"v_mfma_f32_16x16x32_bf16 v[76:79], v[28:31], v[196:199], v[76:79]\n" \
"buffer_load_dwordx4 v225, s[48:51], s77 offen offset:1024 lds\n" \
"v_mfma_f32_16x16x32_bf16 v[108:111], v[24:27], v[200:203], v[108:111]\n" \
"ds_read_b64_tr_b16 v[40:41], v233 offset:1024\n" \
"v_mul_f32 v243, s76, v243\n" \
"v_mfma_f32_16x16x32_bf16 v[108:111], v[28:31], v[204:207], v[108:111]\n" \
"ds_read_b64_tr_b16 v[42:43], v233 offset:3072\n" \
"v_mul_f32 v252, s76, v252\n" \
"v_mfma_f32_16x16x32_bf16 v[104:107], v[16:19], v[200:203], v[104:107]\n" \
"s_waitcnt lgkmcnt(6)\n" \
"ds_read_b64_tr_b16 v[44:45], v233 offset:9216\n" \
"v_mul_f32 v253, s76, v253\n" \
"v_mfma_f32_16x16x32_bf16 v[104:107], v[20:23], v[204:207], v[104:107]\n" \
"buffer_load_dwordx4 v226, s[48:51], s77 offen offset:2048 lds\n" \
"v_mfma_f32_16x16x32_bf16 v[100:103], v[8:11], v[200:203], v[100:103]\n" \
"ds_read_b64_tr_b16 v[46:47], v233 offset:11264\n" \
"v_mul_f32 v254, s76, v254\n" \
"v_mfma_f32_16x16x32_bf16 v[100:103], v[12:15], v[204:207], v[100:103]\n" \
"ds_read_b64_tr_b16 v[48:49], v232 offset:1536\n" \
"v_cvt_pk_bf16_f32 v243, v243, v252\n" \
"v_mfma_f32_16x16x32_bf16 v[96:99], v[0:3], v[200:203], v[96:99]\n" \
"ds_read_b64_tr_b16 v[50:51], v232 offset:3584\n" \
"v_cvt_pk_bf16_f32 v253, v253, v254\n" \
"v_mfma_f32_16x16x32_bf16 v[96:99], v[4:7], v[204:207], v[96:99]\n" \
"buffer_load_dwordx4 v227, s[48:51], s77 offen offset:3072 lds\n" \
"v_mfma_f32_16x16x32_bf16 v[128:131], v[0:3], v[208:211], v[128:131]\n" \
"ds_read_b64_tr_b16 v[52:53], v232 offset:9728\n" \
"v_accvgpr_write_b32 a34, v243\n" \
"v_mfma_f32_16x16x32_bf16 v[128:131], v[4:7], v[212:215], v[128:131]\n" \
"ds_read_b64_tr_b16 v[54:55], v232 offset:11776\n" \
"v_accvgpr_write_b32 a35, v253\n" \
"v_mfma_f32_16x16x32_bf16 v[132:135], v[8:11], v[208:211], v[132:135]\n" \
"s_xor_b32 s77, s77, 0x300\n" \
"ds_read_b64_tr_b16 v[56:57], v233 offset:1536\n" \
"v_accvgpr_read_b32 v243, a8\n" \
"v_mfma_f32_16x16x32_bf16 v[132:135], v[12:15], v[212:215], v[132:135]\n" \
"v_accvgpr_read_b32 v252, a9\n" \
"v_accvgpr_read_b32 v253, a10\n" \
"v_mfma_f32_16x16x32_bf16 v[136:139], v[16:19], v[208:211], v[136:139]\n" \
"ds_read_b64_tr_b16 v[58:59], v233 offset:3584\n" \
"v_accvgpr_read_b32 v254, a11\n" \
"v_mfma_f32_16x16x32_bf16 v[136:139], v[20:23], v[212:215], v[136:139]\n" \
"ds_read_b64_tr_b16 v[60:61], v233 offset:9728\n" \
"v_mul_f32 v243, s76, v243\n" \
"v_mfma_f32_16x16x32_bf16 v[140:143], v[24:27], v[208:211], v[140:143]\n" \
"ds_read_b64_tr_b16 v[62:63], v233 offset:11776\n" \
"v_mul_f32 v252, s76, v252\n" \
"v_mfma_f32_16x16x32_bf16 v[140:143], v[28:31], v[212:215], v[140:143]\n" \
"buffer_store_dwordx4 a[220:223], v234, s[56:59], s93 offen offset:3072 sc1\n" \
"v_mfma_f32_16x16x32_bf16 v[172:175], v[24:27], v[216:219], v[172:175]\n" \
"v_mul_f32 v253, s76, v253\n" \
"v_mul_f32 v254, s76, v254\n" \
"v_mfma_f32_16x16x32_bf16 v[172:175], v[28:31], v[220:223], v[172:175]\n" \
"v_cvt_pk_bf16_f32 v243, v243, v252\n" \
"v_cvt_pk_bf16_f32 v253, v253, v254\n" \
"v_mfma_f32_16x16x32_bf16 v[168:171], v[16:19], v[216:219], v[168:171]\n" \
"v_accvgpr_write_b32 a8, v243\n" \
"v_accvgpr_write_b32 a9, v253\n" \
"v_mfma_f32_16x16x32_bf16 v[168:171], v[20:23], v[220:223], v[168:171]\n" \
"v_accvgpr_read_b32 v243, a12\n" \
"v_accvgpr_read_b32 v252, a13\n" \
"v_mfma_f32_16x16x32_bf16 v[164:167], v[8:11], v[216:219], v[164:167]\n" \
"v_accvgpr_read_b32 v253, a14\n" \
"v_accvgpr_read_b32 v254, a15\n" \
"v_mfma_f32_16x16x32_bf16 v[164:167], v[12:15], v[220:223], v[164:167]\n" \
"v_mul_f32 v243, s76, v243\n" \
"v_mul_f32 v252, s76, v252\n" \
"v_mfma_f32_16x16x32_bf16 v[160:163], v[0:3], v[216:219], v[160:163]\n" \
"v_mul_f32 v253, s76, v253\n" \
"v_mul_f32 v254, s76, v254\n" \
"v_mfma_f32_16x16x32_bf16 v[160:163], v[4:7], v[220:223], v[160:163]\n" \
"s_waitcnt vmcnt(36) lgkmcnt(0)\n" \
"s_barrier\n" \
"v_mfma_f32_16x16x32_bf16 v[80:83], v[32:35], v[192:195], v[80:83]\n" \
"ds_read_b64_tr_b16 v[0:1], v232 offset:16384\n" \
"v_cvt_pk_bf16_f32 v243, v243, v252\n" \
"v_mfma_f32_16x16x32_bf16 v[80:83], v[36:39], v[196:199], v[80:83]\n" \
"ds_read_b64_tr_b16 v[2:3], v232 offset:18432\n" \
"v_cvt_pk_bf16_f32 v253, v253, v254\n" \
"v_mfma_f32_16x16x32_bf16 v[84:87], v[40:43], v[192:195], v[84:87]\n" \
"s_add_u32 m0, s79, 0\n" \
"ds_read_b64_tr_b16 v[4:5], v232 offset:24576\n" \
"v_accvgpr_write_b32 a10, v243\n" \
"v_mfma_f32_16x16x32_bf16 v[84:87], v[44:47], v[196:199], v[84:87]\n" \
"buffer_load_dwordx4 v228, s[52:55], s78 offen offset:0 lds\n" \
"v_mfma_f32_16x16x32_bf16 v[88:91], v[48:51], v[192:195], v[88:91]\n" \
"ds_read_b64_tr_b16 v[6:7], v232 offset:26624\n" \
"v_accvgpr_write_b32 a11, v253\n" \
"v_mfma_f32_16x16x32_bf16 v[88:91], v[52:55], v[196:199], v[88:91]\n" \
"ds_read_b64_tr_b16 v[8:9], v233 offset:16384\n" \
"v_accvgpr_read_b32 v243, a40\n" \
"v_mfma_f32_16x16x32_bf16 v[92:95], v[56:59], v[192:195], v[92:95]\n" \
"ds_read_b64_tr_b16 v[10:11], v233 offset:18432\n" \
"v_accvgpr_read_b32 v252, a41\n" \
"v_mfma_f32_16x16x32_bf16 v[92:95], v[60:63], v[196:199], v[92:95]\n" \
"buffer_load_dwordx4 v229, s[52:55], s78 offen offset:1024 lds\n" \
"v_mfma_f32_16x16x32_bf16 v[124:127], v[56:59], v[200:203], v[124:127]\n" \
"s_waitcnt vmcnt(36)\n" \
"ds_read_b128 v[192:195], v250 offset:0\n" \
"v_accvgpr_read_b32 v253, a42\n" \
"v_mfma_f32_16x16x32_bf16 v[124:127], v[60:63], v[204:207], v[124:127]\n" \
"ds_read_b128 v[196:199], v251 offset:0\n" \
"v_accvgpr_read_b32 v254, a43\n" \
"v_mfma_f32_16x16x32_bf16 v[120:123], v[48:51], v[200:203], v[120:123]\n" \
"s_xor_b32 s78, s78, 0x80000\n" \
"ds_read_b64_tr_b16 v[12:13], v233 offset:24576\n" \
"v_mul_f32 v243, s76, v243\n" \
"v_mfma_f32_16x16x32_bf16 v[120:123], v[52:55], v[204:207], v[120:123]\n" \
"buffer_load_dwordx4 v228, s[52:55], s89 offen offset:2048 lds\n" \
"v_mfma_f32_16x16x32_bf16 v[116:119], v[40:43], v[200:203], v[116:119]\n" \
"ds_read_b64_tr_b16 v[14:15], v233 offset:26624\n" \
"v_mul_f32 v252, s76, v252\n" \
"v_mfma_f32_16x16x32_bf16 v[116:119], v[44:47], v[204:207], v[116:119]\n" \
"ds_read_b64_tr_b16 v[16:17], v232 offset:16896\n" \
"v_mul_f32 v253, s76, v253\n" \
"v_mfma_f32_16x16x32_bf16 v[112:115], v[32:35], v[200:203], v[112:115]\n" \
"ds_read_b64_tr_b16 v[18:19], v232 offset:18944\n" \
"v_mul_f32 v254, s76, v254\n" \
"v_mfma_f32_16x16x32_bf16 v[112:115], v[36:39], v[204:207], v[112:115]\n" \
"buffer_load_dwordx4 v229, s[52:55], s89 offen offset:3072 lds\n" \
"v_mfma_f32_16x16x32_bf16 v[144:147], v[32:35], v[208:211], v[144:147]\n" \
"s_waitcnt vmcnt(36)\n" \
"s_xor_b32 s89, s89, 0x80000\n" \
"ds_read_b128 v[200:203], v250 offset:2048\n" \
"v_mfma_f32_16x16x32_bf16 v[144:147], v[36:39], v[212:215], v[144:147]\n" \
"s_add_u32 m0, s74, 131072\n" \
"ds_read_b128 v[204:207], v251 offset:2048\n" \
"v_cvt_pk_bf16_f32 v243, v243, v252\n" \
"v_mfma_f32_16x16x32_bf16 v[148:151], v[40:43], v[208:211], v[148:151]\n" \
"s_waitcnt lgkmcnt(6)\n" \
"ds_read_b64_tr_b16 v[20:21], v232 offset:25088\n" \
"v_cvt_pk_bf16_f32 v253, v253, v254\n" \
"v_mfma_f32_16x16x32_bf16 v[148:151], v[44:47], v[212:215], v[148:151]\n" \
"buffer_load_dwordx4 v224, s[48:51], s75 offen offset:0 lds\n" \
"v_mfma_f32_16x16x32_bf16 v[152:155], v[48:51], v[208:211], v[152:155]\n" \
"ds_read_b64_tr_b16 v[22:23], v232 offset:27136\n" \
"v_accvgpr_write_b32 a40, v243\n" \
"v_mfma_f32_16x16x32_bf16 v[152:155], v[52:55], v[212:215], v[152:155]\n" \
"ds_read_b64_tr_b16 v[24:25], v233 offset:16896\n" \
"v_accvgpr_write_b32 a41, v253\n" \
"v_mfma_f32_16x16x32_bf16 v[156:159], v[56:59], v[208:211], v[156:159]\n" \
"ds_read_b64_tr_b16 v[26:27], v233 offset:18944\n" \
"v_accvgpr_read_b32 v243, a44\n" \
"v_mfma_f32_16x16x32_bf16 v[156:159], v[60:63], v[212:215], v[156:159]\n" \
"buffer_load_dwordx4 v225, s[48:51], s75 offen offset:1024 lds\n" \
"v_mfma_f32_16x16x32_bf16 v[188:191], v[56:59], v[216:219], v[188:191]\n" \
"s_waitcnt vmcnt(36)\n" \
"ds_read_b128 v[208:211], v250 offset:4096\n" \
"v_accvgpr_read_b32 v252, a45\n" \
"Lpg3859_%=:\n" \
"s_cbranch_execz Lpg6160_%=\n" \
"v_mfma_f32_16x16x32_bf16 v[188:191], v[60:63], v[220:223], v[188:191]\n" \
"ds_read_b128 v[212:215], v251 offset:4096\n" \
"v_accvgpr_read_b32 v253, a46\n" \
"v_mfma_f32_16x16x32_bf16 v[184:187], v[48:51], v[216:219], v[184:187]\n" \
"s_waitcnt lgkmcnt(6)\n" \
"ds_read_b64_tr_b16 v[28:29], v233 offset:25088\n" \
"v_accvgpr_read_b32 v254, a47\n" \
"v_mfma_f32_16x16x32_bf16 v[184:187], v[52:55], v[220:223], v[184:187]\n" \
"buffer_load_dwordx4 v226, s[48:51], s75 offen offset:2048 lds\n" \
"v_mfma_f32_16x16x32_bf16 v[180:183], v[40:43], v[216:219], v[180:183]\n" \
"ds_read_b64_tr_b16 v[30:31], v233 offset:27136\n" \
"v_mul_f32 v243, s76, v243\n" \
"v_mfma_f32_16x16x32_bf16 v[180:183], v[44:47], v[220:223], v[180:183]\n" \
"v_mul_f32 v252, s76, v252\n" \
"v_mul_f32 v253, s76, v253\n" \
"v_mfma_f32_16x16x32_bf16 v[176:179], v[32:35], v[216:219], v[176:179]\n" \
"v_mul_f32 v254, s76, v254\n" \
"v_cvt_pk_bf16_f32 v243, v243, v252\n" \
"v_mfma_f32_16x16x32_bf16 v[176:179], v[36:39], v[220:223], v[176:179]\n" \
"buffer_load_dwordx4 v227, s[48:51], s75 offen offset:3072 lds\n" \
"v_mfma_f32_16x16x32_bf16 v[64:67], v[0:3], v[192:195], v[64:67]\n" \
"s_waitcnt vmcnt(36)\n" \
"s_xor_b32 s75, s75, 0x80\n" \
"ds_read_b128 v[216:219], v250 offset:6144\n" \
"v_mfma_f32_16x16x32_bf16 v[64:67], v[4:7], v[196:199], v[64:67]\n" \
"s_add_u32 m0, s74, 135168\n" \
"ds_read_b128 v[220:223], v251 offset:6144\n" \
"v_cvt_pk_bf16_f32 v253, v253, v254\n" \
"v_mfma_f32_16x16x32_bf16 v[68:71], v[8:11], v[192:195], v[68:71]\n" \
"s_waitcnt lgkmcnt(4)\n" \
"ds_read_b64_tr_b16 v[32:33], v232 offset:17408\n" \
"v_accvgpr_write_b32 a42, v243\n" \
"v_mfma_f32_16x16x32_bf16 v[68:71], v[12:15], v[196:199], v[68:71]\n" \
"buffer_load_dwordx4 v224, s[48:51], s77 offen offset:0 lds\n" \
"v_mfma_f32_16x16x32_bf16 v[72:75], v[16:19], v[192:195], v[72:75]\n" \
"ds_read_b64_tr_b16 v[34:35], v232 offset:19456\n" \
"v_accvgpr_write_b32 a43, v253\n" \
"v_mfma_f32_16x16x32_bf16 v[72:75], v[20:23], v[196:199], v[72:75]\n" \
"s_waitcnt lgkmcnt(3)\n" \
"ds_read_b64_tr_b16 v[36:37], v232 offset:25600\n" \
"v_accvgpr_read_b32 v243, a80\n" \
"v_mfma_f32_16x16x32_bf16 v[76:79], v[24:27], v[192:195], v[76:79]\n" \
"ds_read_b64_tr_b16 v[38:39], v232 offset:27648\n" \
"v_accvgpr_read_b32 v252, a81\n" \
"v_mfma_f32_16x16x32_bf16 v[76:79], v[28:31], v[196:199], v[76:79]\n" \
"buffer_load_dwordx4 v225, s[48:51], s77 offen offset:1024 lds\n" \
"v_mfma_f32_16x16x32_bf16 v[108:111], v[24:27], v[200:203], v[108:111]\n" \
"ds_read_b64_tr_b16 v[40:41], v233 offset:17408\n" \
"v_accvgpr_read_b32 v253, a82\n" \
"v_mfma_f32_16x16x32_bf16 v[108:111], v[28:31], v[204:207], v[108:111]\n" \
"ds_read_b64_tr_b16 v[42:43], v233 offset:19456\n" \
"v_accvgpr_read_b32 v254, a83\n" \
"v_mfma_f32_16x16x32_bf16 v[104:107], v[16:19], v[200:203], v[104:107]\n" \
"s_waitcnt lgkmcnt(6)\n" \
"ds_read_b64_tr_b16 v[44:45], v233 offset:25600\n" \
"v_mul_f32 v243, s31, v243\n" \
"v_mfma_f32_16x16x32_bf16 v[104:107], v[20:23], v[204:207], v[104:107]\n" \
"buffer_load_dwordx4 v226, s[48:51], s77 offen offset:2048 lds\n" \
"v_mfma_f32_16x16x32_bf16 v[100:103], v[8:11], v[200:203], v[100:103]\n" \
"ds_read_b64_tr_b16 v[46:47], v233 offset:27648\n" \
"v_mul_f32 v252, s31, v252\n" \
"v_mfma_f32_16x16x32_bf16 v[100:103], v[12:15], v[204:207], v[100:103]\n" \
"ds_read_b64_tr_b16 v[48:49], v232 offset:17920\n" \
"v_mul_f32 v253, s31, v253\n" \
"v_mfma_f32_16x16x32_bf16 v[96:99], v[0:3], v[200:203], v[96:99]\n" \
"ds_read_b64_tr_b16 v[50:51], v232 offset:19968\n" \
"v_mul_f32 v254, s31, v254\n" \
"v_mfma_f32_16x16x32_bf16 v[96:99], v[4:7], v[204:207], v[96:99]\n" \
"buffer_load_dwordx4 v227, s[48:51], s77 offen offset:3072 lds\n" \
"v_mfma_f32_16x16x32_bf16 v[128:131], v[0:3], v[208:211], v[128:131]\n" \
"ds_read_b64_tr_b16 v[52:53], v232 offset:26112\n" \
"v_cvt_pk_bf16_f32 v243, v243, v252\n" \
"v_mfma_f32_16x16x32_bf16 v[128:131], v[4:7], v[212:215], v[128:131]\n" \
"ds_read_b64_tr_b16 v[54:55], v232 offset:28160\n" \
"v_cvt_pk_bf16_f32 v253, v253, v254\n" \
"v_mfma_f32_16x16x32_bf16 v[132:135], v[8:11], v[208:211], v[132:135]\n" \
"s_xor_b32 s77, s77, 0x80\n" \
"s_add_u32 s93, s87, 17850368\n" \
"ds_read_b64_tr_b16 v[56:57], v233 offset:17920\n" \
"v_mfma_f32_16x16x32_bf16 v[132:135], v[12:15], v[212:215], v[132:135]\n" \
"buffer_store_dwordx4 a[144:147], v234, s[56:59], s93 offen offset:0 sc1\n" \
"v_mfma_f32_16x16x32_bf16 v[136:139], v[16:19], v[208:211], v[136:139]\n" \
"ds_read_b64_tr_b16 v[58:59], v233 offset:19968\n" \
"v_accvgpr_write_b32 a80, v243\n" \
"v_mfma_f32_16x16x32_bf16 v[136:139], v[20:23], v[212:215], v[136:139]\n" \
"ds_read_b64_tr_b16 v[60:61], v233 offset:26112\n" \
"v_accvgpr_write_b32 a81, v253\n" \
"v_mfma_f32_16x16x32_bf16 v[140:143], v[24:27], v[208:211], v[140:143]\n" \
"ds_read_b64_tr_b16 v[62:63], v233 offset:28160\n" \
"v_accvgpr_read_b32 v243, a112\n" \
"v_mfma_f32_16x16x32_bf16 v[140:143], v[28:31], v[212:215], v[140:143]\n" \
"v_accvgpr_read_b32 v252, a113\n" \
"v_accvgpr_read_b32 v253, a114\n" \
"v_mfma_f32_16x16x32_bf16 v[172:175], v[24:27], v[216:219], v[172:175]\n" \
"v_accvgpr_read_b32 v254, a115\n" \
"v_mul_f32 v243, s31, v243\n" \
"v_mfma_f32_16x16x32_bf16 v[172:175], v[28:31], v[220:223], v[172:175]\n" \
"v_mul_f32 v252, s31, v252\n" \
"v_mul_f32 v253, s31, v253\n" \
"v_mfma_f32_16x16x32_bf16 v[168:171], v[16:19], v[216:219], v[168:171]\n" \
"v_mul_f32 v254, s31, v254\n" \
"v_cvt_pk_bf16_f32 v243, v243, v252\n" \
"v_mfma_f32_16x16x32_bf16 v[168:171], v[20:23], v[220:223], v[168:171]\n" \
"buffer_store_dwordx4 a[148:151], v234, s[56:59], s93 offen offset:1024 sc1\n" \
"v_mfma_f32_16x16x32_bf16 v[164:167], v[8:11], v[216:219], v[164:167]\n" \
"v_cvt_pk_bf16_f32 v253, v253, v254\n" \
"v_accvgpr_write_b32 a82, v243\n" \
"v_mfma_f32_16x16x32_bf16 v[164:167], v[12:15], v[220:223], v[164:167]\n" \
"v_accvgpr_write_b32 a83, v253\n" \
"v_accvgpr_read_b32 v243, a84\n" \
"v_mfma_f32_16x16x32_bf16 v[160:163], v[0:3], v[216:219], v[160:163]\n" \
"v_accvgpr_read_b32 v252, a85\n" \
"v_accvgpr_read_b32 v253, a86\n" \
"v_mfma_f32_16x16x32_bf16 v[160:163], v[4:7], v[220:223], v[160:163]\n" \
"s_waitcnt vmcnt(37) lgkmcnt(0)\n" \
"s_barrier\n" \
"v_mfma_f32_16x16x32_bf16 v[80:83], v[32:35], v[192:195], v[80:83]\n" \
"ds_read_b64_tr_b16 v[0:1], v232 offset:32768\n" \
"v_accvgpr_read_b32 v254, a87\n" \
"v_mfma_f32_16x16x32_bf16 v[80:83], v[36:39], v[196:199], v[80:83]\n" \
"ds_read_b64_tr_b16 v[2:3], v232 offset:34816\n" \
"v_mul_f32 v243, s31, v243\n" \
"v_mfma_f32_16x16x32_bf16 v[84:87], v[40:43], v[192:195], v[84:87]\n" \
"s_add_u32 m0, s79, 16384\n" \
"ds_read_b64_tr_b16 v[4:5], v232 offset:40960\n" \
"v_mul_f32 v252, s31, v252\n" \
"v_mfma_f32_16x16x32_bf16 v[84:87], v[44:47], v[196:199], v[84:87]\n" \
"buffer_load_dwordx4 v228, s[52:55], s78 offen offset:0 lds\n" \
"v_mfma_f32_16x16x32_bf16 v[88:91], v[48:51], v[192:195], v[88:91]\n" \
"ds_read_b64_tr_b16 v[6:7], v232 offset:43008\n" \
"v_mul_f32 v253, s31, v253\n" \
"v_mfma_f32_16x16x32_bf16 v[88:91], v[52:55], v[196:199], v[88:91]\n" \
"ds_read_b64_tr_b16 v[8:9], v233 offset:32768\n" \
"v_mul_f32 v254, s31, v254\n" \
"v_mfma_f32_16x16x32_bf16 v[92:95], v[56:59], v[192:195], v[92:95]\n" \
"ds_read_b64_tr_b16 v[10:11], v233 offset:34816\n" \
"v_cvt_pk_bf16_f32 v243, v243, v252\n" \
"v_mfma_f32_16x16x32_bf16 v[92:95], v[60:63], v[196:199], v[92:95]\n" \
"buffer_load_dwordx4 v229, s[52:55], s78 offen offset:1024 lds\n" \
"v_mfma_f32_16x16x32_bf16 v[124:127], v[56:59], v[200:203], v[124:127]\n" \
"s_waitcnt vmcnt(37)\n" \
"ds_read_b128 v[192:195], v230 offset:0\n" \
"v_cvt_pk_bf16_f32 v253, v253, v254\n" \
"v_mfma_f32_16x16x32_bf16 v[124:127], v[60:63], v[204:207], v[124:127]\n" \
"ds_read_b128 v[196:199], v231 offset:0\n" \
"v_accvgpr_write_b32 a84, v243\n" \
"v_mfma_f32_16x16x32_bf16 v[120:123], v[48:51], v[200:203], v[120:123]\n" \
"s_xor_b32 s78, s78, 0x180000\n" \
"ds_read_b64_tr_b16 v[12:13], v233 offset:40960\n" \
"v_accvgpr_write_b32 a85, v253\n" \
"v_mfma_f32_16x16x32_bf16 v[120:123], v[52:55], v[204:207], v[120:123]\n" \
"buffer_load_dwordx4 v228, s[52:55], s89 offen offset:2048 lds\n" \
"v_mfma_f32_16x16x32_bf16 v[116:119], v[40:43], v[200:203], v[116:119]\n" \
"ds_read_b64_tr_b16 v[14:15], v233 offset:43008\n" \
"v_accvgpr_read_b32 v243, a116\n" \
"v_mfma_f32_16x16x32_bf16 v[116:119], v[44:47], v[204:207], v[116:119]\n" \
"ds_read_b64_tr_b16 v[16:17], v232 offset:33280\n" \
"v_accvgpr_read_b32 v252, a117\n" \
"v_mfma_f32_16x16x32_bf16 v[112:115], v[32:35], v[200:203], v[112:115]\n" \
"ds_read_b64_tr_b16 v[18:19], v232 offset:35328\n" \
"v_accvgpr_read_b32 v253, a118\n" \
"v_mfma_f32_16x16x32_bf16 v[112:115], v[36:39], v[204:207], v[112:115]\n" \
"buffer_load_dwordx4 v229, s[52:55], s89 offen offset:3072 lds\n" \
"v_mfma_f32_16x16x32_bf16 v[144:147], v[32:35], v[208:211], v[144:147]\n" \
"s_waitcnt vmcnt(37)\n" \
"s_xor_b32 s89, s89, 0x180000\n" \
"ds_read_b128 v[200:203], v230 offset:2048\n" \
"v_mfma_f32_16x16x32_bf16 v[144:147], v[36:39], v[212:215], v[144:147]\n" \
"s_add_u32 m0, s74, 65536\n" \
"ds_read_b128 v[204:207], v231 offset:2048\n" \
"v_accvgpr_read_b32 v254, a119\n" \
"v_mfma_f32_16x16x32_bf16 v[148:151], v[40:43], v[208:211], v[148:151]\n" \
"s_waitcnt lgkmcnt(6)\n" \
"ds_read_b64_tr_b16 v[20:21], v232 offset:41472\n" \
"v_mul_f32 v243, s31, v243\n" \
"v_mfma_f32_16x16x32_bf16 v[148:151], v[44:47], v[212:215], v[148:151]\n" \
"buffer_load_dwordx4 v224, s[48:51], s75 offen offset:0 lds\n" \
"v_mfma_f32_16x16x32_bf16 v[152:155], v[48:51], v[208:211], v[152:155]\n" \
"ds_read_b64_tr_b16 v[22:23], v232 offset:43520\n" \
"v_mul_f32 v252, s31, v252\n" \
"v_mfma_f32_16x16x32_bf16 v[152:155], v[52:55], v[212:215], v[152:155]\n" \
"ds_read_b64_tr_b16 v[24:25], v233 offset:33280\n" \
"v_mul_f32 v253, s31, v253\n" \
"v_mfma_f32_16x16x32_bf16 v[156:159], v[56:59], v[208:211], v[156:159]\n" \
"ds_read_b64_tr_b16 v[26:27], v233 offset:35328\n" \
"v_mul_f32 v254, s31, v254\n" \
"v_mfma_f32_16x16x32_bf16 v[156:159], v[60:63], v[212:215], v[156:159]\n" \
"buffer_load_dwordx4 v225, s[48:51], s75 offen offset:1024 lds\n" \
"v_mfma_f32_16x16x32_bf16 v[188:191], v[56:59], v[216:219], v[188:191]\n" \
"s_waitcnt vmcnt(37)\n" \
"ds_read_b128 v[208:211], v230 offset:4096\n" \
"v_cvt_pk_bf16_f32 v243, v243, v252\n" \
"v_mfma_f32_16x16x32_bf16 v[188:191], v[60:63], v[220:223], v[188:191]\n" \
"ds_read_b128 v[212:215], v231 offset:4096\n" \
"v_cvt_pk_bf16_f32 v253, v253, v254\n" \
"v_mfma_f32_16x16x32_bf16 v[184:187], v[48:51], v[216:219], v[184:187]\n" \
"s_waitcnt lgkmcnt(6)\n" \
"ds_read_b64_tr_b16 v[28:29], v233 offset:41472\n" \
"v_accvgpr_write_b32 a86, v243\n" \
"v_mfma_f32_16x16x32_bf16 v[184:187], v[52:55], v[220:223], v[184:187]\n" \
"buffer_load_dwordx4 v226, s[48:51], s75 offen offset:2048 lds\n" \
"v_mfma_f32_16x16x32_bf16 v[180:183], v[40:43], v[216:219], v[180:183]\n" \
"ds_read_b64_tr_b16 v[30:31], v233 offset:43520\n" \
"v_accvgpr_write_b32 a87, v253\n" \
"v_mfma_f32_16x16x32_bf16 v[180:183], v[44:47], v[220:223], v[180:183]\n" \
"v_accvgpr_read_b32 v243, a88\n" \
"v_accvgpr_read_b32 v252, a89\n" \
"v_mfma_f32_16x16x32_bf16 v[176:179], v[32:35], v[216:219], v[176:179]\n" \
"v_accvgpr_read_b32 v253, a90\n" \
"v_accvgpr_read_b32 v254, a91\n" \
"v_mfma_f32_16x16x32_bf16 v[176:179], v[36:39], v[220:223], v[176:179]\n" \
"buffer_load_dwordx4 v227, s[48:51], s75 offen offset:3072 lds\n" \
"v_mfma_f32_16x16x32_bf16 v[64:67], v[0:3], v[192:195], v[64:67]\n" \
"s_waitcnt vmcnt(37)\n" \
"s_xor_b32 s75, s75, 0x180\n" \
"ds_read_b128 v[216:219], v230 offset:6144\n" \
"v_mfma_f32_16x16x32_bf16 v[64:67], v[4:7], v[196:199], v[64:67]\n" \
"s_add_u32 m0, s74, 69632\n" \
"ds_read_b128 v[220:223], v231 offset:6144\n" \
"v_mul_f32 v243, s31, v243\n" \
"v_mfma_f32_16x16x32_bf16 v[68:71], v[8:11], v[192:195], v[68:71]\n" \
"s_waitcnt lgkmcnt(4)\n" \
"ds_read_b64_tr_b16 v[32:33], v232 offset:33792\n" \
"v_mul_f32 v252, s31, v252\n" \
"v_mfma_f32_16x16x32_bf16 v[68:71], v[12:15], v[196:199], v[68:71]\n" \
"buffer_load_dwordx4 v224, s[48:51], s77 offen offset:0 lds\n" \
"v_mfma_f32_16x16x32_bf16 v[72:75], v[16:19], v[192:195], v[72:75]\n" \
"ds_read_b64_tr_b16 v[34:35], v232 offset:35840\n" \
"v_mul_f32 v253, s31, v253\n" \
"v_mfma_f32_16x16x32_bf16 v[72:75], v[20:23], v[196:199], v[72:75]\n" \
"s_waitcnt lgkmcnt(3)\n" \
"ds_read_b64_tr_b16 v[36:37], v232 offset:41984\n" \
"v_mul_f32 v254, s31, v254\n" \
"v_mfma_f32_16x16x32_bf16 v[76:79], v[24:27], v[192:195], v[76:79]\n" \
"ds_read_b64_tr_b16 v[38:39], v232 offset:44032\n" \
"v_cvt_pk_bf16_f32 v243, v243, v252\n" \
"v_mfma_f32_16x16x32_bf16 v[76:79], v[28:31], v[196:199], v[76:79]\n" \
"buffer_load_dwordx4 v225, s[48:51], s77 offen offset:1024 lds\n" \
"v_mfma_f32_16x16x32_bf16 v[108:111], v[24:27], v[200:203], v[108:111]\n" \
"ds_read_b64_tr_b16 v[40:41], v233 offset:33792\n" \
"v_cvt_pk_bf16_f32 v253, v253, v254\n" \
"v_mfma_f32_16x16x32_bf16 v[108:111], v[28:31], v[204:207], v[108:111]\n" \
"ds_read_b64_tr_b16 v[42:43], v233 offset:35840\n" \
"v_accvgpr_write_b32 a88, v243\n" \
"v_mfma_f32_16x16x32_bf16 v[104:107], v[16:19], v[200:203], v[104:107]\n" \
"s_waitcnt lgkmcnt(6)\n" \
"ds_read_b64_tr_b16 v[44:45], v233 offset:41984\n" \
"v_accvgpr_write_b32 a89, v253\n" \
"v_mfma_f32_16x16x32_bf16 v[104:107], v[20:23], v[204:207], v[104:107]\n" \
"buffer_load_dwordx4 v226, s[48:51], s77 offen offset:2048 lds\n" \
"v_mfma_f32_16x16x32_bf16 v[100:103], v[8:11], v[200:203], v[100:103]\n" \
"ds_read_b64_tr_b16 v[46:47], v233 offset:44032\n" \
"v_accvgpr_read_b32 v243, a120\n" \
"v_mfma_f32_16x16x32_bf16 v[100:103], v[12:15], v[204:207], v[100:103]\n" \
"ds_read_b64_tr_b16 v[48:49], v232 offset:34304\n" \
"v_accvgpr_read_b32 v252, a121\n" \
"v_mfma_f32_16x16x32_bf16 v[96:99], v[0:3], v[200:203], v[96:99]\n" \
"ds_read_b64_tr_b16 v[50:51], v232 offset:36352\n" \
"v_accvgpr_read_b32 v253, a122\n" \
"v_mfma_f32_16x16x32_bf16 v[96:99], v[4:7], v[204:207], v[96:99]\n" \
"buffer_load_dwordx4 v227, s[48:51], s77 offen offset:3072 lds\n" \
"v_mfma_f32_16x16x32_bf16 v[128:131], v[0:3], v[208:211], v[128:131]\n" \
"ds_read_b64_tr_b16 v[52:53], v232 offset:42496\n" \
"v_accvgpr_read_b32 v254, a123\n" \
"v_mfma_f32_16x16x32_bf16 v[128:131], v[4:7], v[212:215], v[128:131]\n" \
"ds_read_b64_tr_b16 v[54:55], v232 offset:44544\n" \
"v_mul_f32 v243, s31, v243\n" \
"v_mfma_f32_16x16x32_bf16 v[132:135], v[8:11], v[208:211], v[132:135]\n" \
"s_xor_b32 s77, s77, 0x180\n" \
"ds_read_b64_tr_b16 v[56:57], v233 offset:34304\n" \
"v_mul_f32 v252, s31, v252\n" \
"v_mfma_f32_16x16x32_bf16 v[132:135], v[12:15], v[212:215], v[132:135]\n" \
"v_mul_f32 v253, s31, v253\n" \
"v_mul_f32 v254, s31, v254\n" \
"v_mfma_f32_16x16x32_bf16 v[136:139], v[16:19], v[208:211], v[136:139]\n" \
"ds_read_b64_tr_b16 v[58:59], v233 offset:36352\n" \
"v_cvt_pk_bf16_f32 v243, v243, v252\n" \
"v_mfma_f32_16x16x32_bf16 v[136:139], v[20:23], v[212:215], v[136:139]\n" \
"ds_read_b64_tr_b16 v[60:61], v233 offset:42496\n" \
"v_cvt_pk_bf16_f32 v253, v253, v254\n" \
"v_mfma_f32_16x16x32_bf16 v[140:143], v[24:27], v[208:211], v[140:143]\n" \
"ds_read_b64_tr_b16 v[62:63], v233 offset:44544\n" \
"v_accvgpr_write_b32 a90, v243\n" \
"v_mfma_f32_16x16x32_bf16 v[140:143], v[28:31], v[212:215], v[140:143]\n" \
"buffer_store_dwordx4 a[152:155], v234, s[56:59], s93 offen offset:2048 sc1\n" \
"v_mfma_f32_16x16x32_bf16 v[172:175], v[24:27], v[216:219], v[172:175]\n" \
"v_accvgpr_write_b32 a91, v253\n" \
"v_accvgpr_read_b32 v243, a92\n" \
"v_mfma_f32_16x16x32_bf16 v[172:175], v[28:31], v[220:223], v[172:175]\n" \
"v_accvgpr_read_b32 v252, a93\n" \
"v_accvgpr_read_b32 v253, a94\n" \
"v_mfma_f32_16x16x32_bf16 v[168:171], v[16:19], v[216:219], v[168:171]\n" \
"v_accvgpr_read_b32 v254, a95\n" \
"v_mul_f32 v243, s31, v243\n" \
"v_mfma_f32_16x16x32_bf16 v[168:171], v[20:23], v[220:223], v[168:171]\n" \
"v_mul_f32 v252, s31, v252\n" \
"v_mul_f32 v253, s31, v253\n" \
"v_mfma_f32_16x16x32_bf16 v[164:167], v[8:11], v[216:219], v[164:167]\n" \
"v_mul_f32 v254, s31, v254\n" \
"v_cvt_pk_bf16_f32 v243, v243, v252\n" \
"v_mfma_f32_16x16x32_bf16 v[164:167], v[12:15], v[220:223], v[164:167]\n" \
"v_cvt_pk_bf16_f32 v253, v253, v254\n" \
"v_accvgpr_write_b32 a92, v243\n" \
"v_mfma_f32_16x16x32_bf16 v[160:163], v[0:3], v[216:219], v[160:163]\n" \
"v_accvgpr_write_b32 a93, v253\n" \
"v_accvgpr_read_b32 v243, a124\n" \
"v_mfma_f32_16x16x32_bf16 v[160:163], v[4:7], v[220:223], v[160:163]\n" \
"s_waitcnt vmcnt(36) lgkmcnt(0)\n" \
"s_barrier\n" \
"v_mfma_f32_16x16x32_bf16 v[80:83], v[32:35], v[192:195], v[80:83]\n" \
"ds_read_b64_tr_b16 v[0:1], v232 offset:49152\n" \
"v_accvgpr_read_b32 v252, a125\n" \
"v_mfma_f32_16x16x32_bf16 v[80:83], v[36:39], v[196:199], v[80:83]\n" \
"ds_read_b64_tr_b16 v[2:3], v232 offset:51200\n" \
"v_accvgpr_read_b32 v253, a126\n" \
"v_mfma_f32_16x16x32_bf16 v[84:87], v[40:43], v[192:195], v[84:87]\n" \
"s_add_u32 m0, s79, 32768\n" \
"ds_read_b64_tr_b16 v[4:5], v232 offset:57344\n" \
"v_accvgpr_read_b32 v254, a127\n" \
"v_mfma_f32_16x16x32_bf16 v[84:87], v[44:47], v[196:199], v[84:87]\n" \
"buffer_load_dwordx4 v228, s[52:55], s78 offen offset:0 lds\n" \
"v_mfma_f32_16x16x32_bf16 v[88:91], v[48:51], v[192:195], v[88:91]\n" \
"ds_read_b64_tr_b16 v[6:7], v232 offset:59392\n" \
"v_mul_f32 v243, s31, v243\n" \
"v_mfma_f32_16x16x32_bf16 v[88:91], v[52:55], v[196:199], v[88:91]\n" \
"ds_read_b64_tr_b16 v[8:9], v233 offset:49152\n" \
"v_mul_f32 v252, s31, v252\n" \
"v_mfma_f32_16x16x32_bf16 v[92:95], v[56:59], v[192:195], v[92:95]\n" \
"ds_read_b64_tr_b16 v[10:11], v233 offset:51200\n" \
"v_mul_f32 v253, s31, v253\n" \
"v_mfma_f32_16x16x32_bf16 v[92:95], v[60:63], v[196:199], v[92:95]\n" \
"buffer_load_dwordx4 v229, s[52:55], s78 offen offset:1024 lds\n" \
"v_mfma_f32_16x16x32_bf16 v[124:127], v[56:59], v[200:203], v[124:127]\n" \
"s_waitcnt vmcnt(36)\n" \
"ds_read_b128 v[192:195], v230 offset:32768\n" \
"v_mul_f32 v254, s31, v254\n" \
"v_mfma_f32_16x16x32_bf16 v[124:127], v[60:63], v[204:207], v[124:127]\n" \
"ds_read_b128 v[196:199], v231 offset:32768\n" \
"v_cvt_pk_bf16_f32 v243, v243, v252\n" \
"v_mfma_f32_16x16x32_bf16 v[120:123], v[48:51], v[200:203], v[120:123]\n" \
"s_xor_b32 s78, s78, 0x80000\n" \
"ds_read_b64_tr_b16 v[12:13], v233 offset:57344\n" \
"v_cvt_pk_bf16_f32 v253, v253, v254\n" \
"v_mfma_f32_16x16x32_bf16 v[120:123], v[52:55], v[204:207], v[120:123]\n" \
"buffer_load_dwordx4 v228, s[52:55], s89 offen offset:2048 lds\n" \
"v_mfma_f32_16x16x32_bf16 v[116:119], v[40:43], v[200:203], v[116:119]\n" \
"ds_read_b64_tr_b16 v[14:15], v233 offset:59392\n" \
"v_accvgpr_write_b32 a94, v243\n" \
"v_mfma_f32_16x16x32_bf16 v[116:119], v[44:47], v[204:207], v[116:119]\n" \
"ds_read_b64_tr_b16 v[16:17], v232 offset:49664\n" \
"v_accvgpr_write_b32 a95, v253\n" \
"v_mfma_f32_16x16x32_bf16 v[112:115], v[32:35], v[200:203], v[112:115]\n" \
"ds_read_b64_tr_b16 v[18:19], v232 offset:51712\n" \
"v_accvgpr_read_b32 v243, a16\n" \
"v_mfma_f32_16x16x32_bf16 v[112:115], v[36:39], v[204:207], v[112:115]\n" \
"buffer_load_dwordx4 v229, s[52:55], s89 offen offset:3072 lds\n" \
"v_mfma_f32_16x16x32_bf16 v[144:147], v[32:35], v[208:211], v[144:147]\n" \
"s_waitcnt vmcnt(36)\n" \
"s_xor_b32 s89, s89, 0x80000\n" \
"ds_read_b128 v[200:203], v230 offset:34816\n" \
"v_mfma_f32_16x16x32_bf16 v[144:147], v[36:39], v[212:215], v[144:147]\n" \
"s_add_u32 m0, s74, 98304\n" \
"ds_read_b128 v[204:207], v231 offset:34816\n" \
"v_accvgpr_read_b32 v252, a17\n" \
"v_mfma_f32_16x16x32_bf16 v[148:151], v[40:43], v[208:211], v[148:151]\n" \
"s_waitcnt lgkmcnt(6)\n" \
"ds_read_b64_tr_b16 v[20:21], v232 offset:57856\n" \
"v_accvgpr_read_b32 v253, a18\n" \
"v_mfma_f32_16x16x32_bf16 v[148:151], v[44:47], v[212:215], v[148:151]\n" \
"buffer_load_dwordx4 v224, s[48:51], s75 offen offset:0 lds\n" \
"v_mfma_f32_16x16x32_bf16 v[152:155], v[48:51], v[208:211], v[152:155]\n" \
"ds_read_b64_tr_b16 v[22:23], v232 offset:59904\n" \
"v_accvgpr_read_b32 v254, a19\n" \
"v_mfma_f32_16x16x32_bf16 v[152:155], v[52:55], v[212:215], v[152:155]\n" \
"ds_read_b64_tr_b16 v[24:25], v233 offset:49664\n" \
"v_mul_f32 v243, s31, v243\n" \
"v_mfma_f32_16x16x32_bf16 v[156:159], v[56:59], v[208:211], v[156:159]\n" \
"ds_read_b64_tr_b16 v[26:27], v233 offset:51712\n" \
"v_mul_f32 v252, s31, v252\n" \
"v_mfma_f32_16x16x32_bf16 v[156:159], v[60:63], v[212:215], v[156:159]\n" \
"buffer_load_dwordx4 v225, s[48:51], s75 offen offset:1024 lds\n" \
"v_mfma_f32_16x16x32_bf16 v[188:191], v[56:59], v[216:219], v[188:191]\n" \
"s_waitcnt vmcnt(36)\n" \
"ds_read_b128 v[208:211], v230 offset:36864\n" \
"v_mul_f32 v253, s31, v253\n" \
"v_mfma_f32_16x16x32_bf16 v[188:191], v[60:63], v[220:223], v[188:191]\n" \
"ds_read_b128 v[212:215], v231 offset:36864\n" \
"v_mul_f32 v254, s31, v254\n" \
"v_mfma_f32_16x16x32_bf16 v[184:187], v[48:51], v[216:219], v[184:187]\n" \
"s_waitcnt lgkmcnt(6)\n" \
"ds_read_b64_tr_b16 v[28:29], v233 offset:57856\n" \
"v_cvt_pk_bf16_f32 v243, v243, v252\n" \
"v_mfma_f32_16x16x32_bf16 v[184:187], v[52:55], v[220:223], v[184:187]\n" \
"buffer_load_dwordx4 v226, s[48:51], s75 offen offset:2048 lds\n" \
"v_mfma_f32_16x16x32_bf16 v[180:183], v[40:43], v[216:219], v[180:183]\n" \
"ds_read_b64_tr_b16 v[30:31], v233 offset:59904\n" \
"v_cvt_pk_bf16_f32 v253, v253, v254\n" \
"v_mfma_f32_16x16x32_bf16 v[180:183], v[44:47], v[220:223], v[180:183]\n" \
"v_accvgpr_write_b32 a16, v243\n" \
"v_accvgpr_write_b32 a17, v253\n" \
"v_mfma_f32_16x16x32_bf16 v[176:179], v[32:35], v[216:219], v[176:179]\n" \
"v_accvgpr_read_b32 v243, a48\n" \
"v_accvgpr_read_b32 v252, a49\n" \
"v_mfma_f32_16x16x32_bf16 v[176:179], v[36:39], v[220:223], v[176:179]\n" \
"buffer_load_dwordx4 v227, s[48:51], s75 offen offset:3072 lds\n" \
"v_mfma_f32_16x16x32_bf16 v[64:67], v[0:3], v[192:195], v[64:67]\n" \
"s_waitcnt vmcnt(36)\n" \
"s_xor_b32 s75, s75, 0x80\n" \
"ds_read_b128 v[216:219], v230 offset:38912\n" \
"v_mfma_f32_16x16x32_bf16 v[64:67], v[4:7], v[196:199], v[64:67]\n" \
"s_add_u32 m0, s74, 102400\n" \
"ds_read_b128 v[220:223], v231 offset:38912\n" \
"v_accvgpr_read_b32 v253, a50\n" \
"v_mfma_f32_16x16x32_bf16 v[68:71], v[8:11], v[192:195], v[68:71]\n" \
"s_waitcnt lgkmcnt(4)\n" \
"ds_read_b64_tr_b16 v[32:33], v232 offset:50176\n" \
"v_accvgpr_read_b32 v254, a51\n" \
"v_mfma_f32_16x16x32_bf16 v[68:71], v[12:15], v[196:199], v[68:71]\n" \
"buffer_load_dwordx4 v224, s[48:51], s77 offen offset:0 lds\n" \
"v_mfma_f32_16x16x32_bf16 v[72:75], v[16:19], v[192:195], v[72:75]\n" \
"ds_read_b64_tr_b16 v[34:35], v232 offset:52224\n" \
"v_mul_f32 v243, s31, v243\n" \
"v_mfma_f32_16x16x32_bf16 v[72:75], v[20:23], v[196:199], v[72:75]\n" \
"s_waitcnt lgkmcnt(3)\n" \
"ds_read_b64_tr_b16 v[36:37], v232 offset:58368\n" \
"v_mul_f32 v252, s31, v252\n" \
"v_mfma_f32_16x16x32_bf16 v[76:79], v[24:27], v[192:195], v[76:79]\n" \
"ds_read_b64_tr_b16 v[38:39], v232 offset:60416\n" \
"v_mul_f32 v253, s31, v253\n" \
"v_mfma_f32_16x16x32_bf16 v[76:79], v[28:31], v[196:199], v[76:79]\n" \
"buffer_load_dwordx4 v225, s[48:51], s77 offen offset:1024 lds\n" \
"v_mfma_f32_16x16x32_bf16 v[108:111], v[24:27], v[200:203], v[108:111]\n" \
"ds_read_b64_tr_b16 v[40:41], v233 offset:50176\n" \
"v_mul_f32 v254, s31, v254\n" \
"v_mfma_f32_16x16x32_bf16 v[108:111], v[28:31], v[204:207], v[108:111]\n" \
"ds_read_b64_tr_b16 v[42:43], v233 offset:52224\n" \
"v_cvt_pk_bf16_f32 v243, v243, v252\n" \
"v_mfma_f32_16x16x32_bf16 v[104:107], v[16:19], v[200:203], v[104:107]\n" \
"s_waitcnt lgkmcnt(6)\n" \
"ds_read_b64_tr_b16 v[44:45], v233 offset:58368\n" \
"v_cvt_pk_bf16_f32 v253, v253, v254\n" \
"v_mfma_f32_16x16x32_bf16 v[104:107], v[20:23], v[204:207], v[104:107]\n" \
"buffer_load_dwordx4 v226, s[48:51], s77 offen offset:2048 lds\n" \
"v_mfma_f32_16x16x32_bf16 v[100:103], v[8:11], v[200:203], v[100:103]\n" \
"ds_read_b64_tr_b16 v[46:47], v233 offset:60416\n" \
"v_accvgpr_write_b32 a18, v243\n" \
"v_mfma_f32_16x16x32_bf16 v[100:103], v[12:15], v[204:207], v[100:103]\n" \
"ds_read_b64_tr_b16 v[48:49], v232 offset:50688\n" \
"v_accvgpr_write_b32 a19, v253\n" \
"v_mfma_f32_16x16x32_bf16 v[96:99], v[0:3], v[200:203], v[96:99]\n" \
"ds_read_b64_tr_b16 v[50:51], v232 offset:52736\n" \
"v_accvgpr_read_b32 v243, a20\n" \
"v_mfma_f32_16x16x32_bf16 v[96:99], v[4:7], v[204:207], v[96:99]\n" \
"buffer_load_dwordx4 v227, s[48:51], s77 offen offset:3072 lds\n" \
"v_mfma_f32_16x16x32_bf16 v[128:131], v[0:3], v[208:211], v[128:131]\n" \
"ds_read_b64_tr_b16 v[52:53], v232 offset:58880\n" \
"v_accvgpr_read_b32 v252, a21\n" \
"v_mfma_f32_16x16x32_bf16 v[128:131], v[4:7], v[212:215], v[128:131]\n" \
"ds_read_b64_tr_b16 v[54:55], v232 offset:60928\n" \
"v_accvgpr_read_b32 v253, a22\n" \
"v_mfma_f32_16x16x32_bf16 v[132:135], v[8:11], v[208:211], v[132:135]\n" \
"s_xor_b32 s77, s77, 0x80\n" \
"ds_read_b64_tr_b16 v[56:57], v233 offset:50688\n" \
"v_accvgpr_read_b32 v254, a23\n" \
"v_mfma_f32_16x16x32_bf16 v[132:135], v[12:15], v[212:215], v[132:135]\n" \
"v_mul_f32 v243, s31, v243\n" \
"v_mul_f32 v252, s31, v252\n" \
"v_mfma_f32_16x16x32_bf16 v[136:139], v[16:19], v[208:211], v[136:139]\n" \
"ds_read_b64_tr_b16 v[58:59], v233 offset:52736\n" \
"v_mul_f32 v253, s31, v253\n" \
"v_mfma_f32_16x16x32_bf16 v[136:139], v[20:23], v[212:215], v[136:139]\n" \
"ds_read_b64_tr_b16 v[60:61], v233 offset:58880\n" \
"v_mul_f32 v254, s31, v254\n" \
"v_mfma_f32_16x16x32_bf16 v[140:143], v[24:27], v[208:211], v[140:143]\n" \
"ds_read_b64_tr_b16 v[62:63], v233 offset:60928\n" \
"v_cvt_pk_bf16_f32 v243, v243, v252\n" \
"v_mfma_f32_16x16x32_bf16 v[140:143], v[28:31], v[212:215], v[140:143]\n" \
"buffer_store_dwordx4 a[156:159], v234, s[56:59], s93 offen offset:3072 sc1\n" \
"v_mfma_f32_16x16x32_bf16 v[172:175], v[24:27], v[216:219], v[172:175]\n" \
"v_cvt_pk_bf16_f32 v253, v253, v254\n" \
"v_accvgpr_write_b32 a20, v243\n" \
"v_mfma_f32_16x16x32_bf16 v[172:175], v[28:31], v[220:223], v[172:175]\n" \
"v_accvgpr_write_b32 a21, v253\n" \
"v_accvgpr_read_b32 v243, a52\n" \
"v_mfma_f32_16x16x32_bf16 v[168:171], v[16:19], v[216:219], v[168:171]\n" \
"v_accvgpr_read_b32 v252, a53\n" \
"v_accvgpr_read_b32 v253, a54\n" \
"v_mfma_f32_16x16x32_bf16 v[168:171], v[20:23], v[220:223], v[168:171]\n" \
"v_accvgpr_read_b32 v254, a55\n" \
"v_mul_f32 v243, s31, v243\n" \
"v_mfma_f32_16x16x32_bf16 v[164:167], v[8:11], v[216:219], v[164:167]\n" \
"v_mul_f32 v252, s31, v252\n" \
"v_mul_f32 v253, s31, v253\n" \
"v_mfma_f32_16x16x32_bf16 v[164:167], v[12:15], v[220:223], v[164:167]\n" \
"v_mul_f32 v254, s31, v254\n" \
"v_cvt_pk_bf16_f32 v243, v243, v252\n" \
"v_mfma_f32_16x16x32_bf16 v[160:163], v[0:3], v[216:219], v[160:163]\n" \
"v_cvt_pk_bf16_f32 v253, v253, v254\n" \
"v_accvgpr_write_b32 a22, v243\n" \
"v_mfma_f32_16x16x32_bf16 v[160:163], v[4:7], v[220:223], v[160:163]\n" \
"s_waitcnt vmcnt(36) lgkmcnt(0)\n" \
"s_barrier\n" \
"v_mfma_f32_16x16x32_bf16 v[80:83], v[32:35], v[192:195], v[80:83]\n" \
"ds_read_b64_tr_b16 v[0:1], v232 offset:0\n" \
"v_accvgpr_write_b32 a23, v253\n" \
"v_mfma_f32_16x16x32_bf16 v[80:83], v[36:39], v[196:199], v[80:83]\n" \
"ds_read_b64_tr_b16 v[2:3], v232 offset:2048\n" \
"v_accvgpr_read_b32 v243, a24\n" \
"v_mfma_f32_16x16x32_bf16 v[84:87], v[40:43], v[192:195], v[84:87]\n" \
"s_add_u32 m0, s79, 49152\n" \
"ds_read_b64_tr_b16 v[4:5], v232 offset:8192\n" \
"v_accvgpr_read_b32 v252, a25\n" \
"v_mfma_f32_16x16x32_bf16 v[84:87], v[44:47], v[196:199], v[84:87]\n" \
"buffer_load_dwordx4 v228, s[52:55], s78 offen offset:0 lds\n" \
"v_mfma_f32_16x16x32_bf16 v[88:91], v[48:51], v[192:195], v[88:91]\n" \
"s_mul_i32 s73, s62, 557824\n" \
"s_add_u32 s85, s73, 17850368\n" \
"ds_read_b64_tr_b16 v[6:7], v232 offset:10240\n" \
"v_mfma_f32_16x16x32_bf16 v[88:91], v[52:55], v[196:199], v[88:91]\n" \
"s_mul_i32 s83, s63, 34816\n" \
"s_sub_u32 s86, 557056, s83\n" \
"ds_read_b64_tr_b16 v[8:9], v233 offset:0\n" \
"v_mfma_f32_16x16x32_bf16 v[92:95], v[56:59], v[192:195], v[92:95]\n" \
"ds_read_b64_tr_b16 v[10:11], v233 offset:2048\n" \
"v_add_u32 v237, %3, v236\n" \
"v_mfma_f32_16x16x32_bf16 v[92:95], v[60:63], v[196:199], v[92:95]\n" \
"buffer_load_dwordx4 v229, s[52:55], s78 offen offset:1024 lds\n" \
"v_mfma_f32_16x16x32_bf16 v[124:127], v[56:59], v[200:203], v[124:127]\n" \
"s_waitcnt vmcnt(36)\n" \
"ds_read_b128 v[192:195], v250 offset:0\n" \
"v_accvgpr_read_b32 v253, a26\n" \
"v_mfma_f32_16x16x32_bf16 v[124:127], v[60:63], v[204:207], v[124:127]\n" \
"s_add_u32 s36, s56, s73\n" \
"s_addc_u32 s37, s57, 0\n" \
"ds_read_b128 v[196:199], v251 offset:0\n" \
"v_mfma_f32_16x16x32_bf16 v[120:123], v[48:51], v[200:203], v[120:123]\n" \
"s_mov_b32 s38, s86\n" \
"s_mov_b32 s39, 0x20000\n" \
"ds_read_b64_tr_b16 v[12:13], v233 offset:8192\n" \
"v_mfma_f32_16x16x32_bf16 v[120:123], v[52:55], v[204:207], v[120:123]\n" \
"buffer_load_dwordx4 v228, s[52:55], s89 offen offset:2048 lds\n" \
"v_mfma_f32_16x16x32_bf16 v[116:119], v[40:43], v[200:203], v[116:119]\n" \
"ds_read_b64_tr_b16 v[14:15], v233 offset:10240\n" \
"v_add_u32 v235, %3, v234\n" \
"v_mfma_f32_16x16x32_bf16 v[116:119], v[44:47], v[204:207], v[116:119]\n" \
"s_add_u32 s40, s56, s85\n" \
"s_addc_u32 s41, s57, 0\n" \
"ds_read_b64_tr_b16 v[16:17], v232 offset:512\n" \
"v_mfma_f32_16x16x32_bf16 v[112:115], v[32:35], v[200:203], v[112:115]\n" \
"s_mov_b32 s42, s86\n" \
"s_mov_b32 s43, 0x20000\n" \
"ds_read_b64_tr_b16 v[18:19], v232 offset:2560\n" \
"v_mfma_f32_16x16x32_bf16 v[112:115], v[36:39], v[204:207], v[112:115]\n" \
"buffer_load_dwordx4 v229, s[52:55], s89 offen offset:3072 lds\n" \
"v_mfma_f32_16x16x32_bf16 v[144:147], v[32:35], v[208:211], v[144:147]\n" \
"s_waitcnt vmcnt(36)\n" \
"s_add_u32 s85, s74, 131072\n" \
"Lpg4417_%=:\n" \
"s_cbranch_execz Lpg6284_%=\n" \
"ds_read_b128 v[200:203], v250 offset:2048\n" \
"v_mfma_f32_16x16x32_bf16 v[144:147], v[36:39], v[212:215], v[144:147]\n" \
"s_add_u32 m0, s74, 131072\n" \
"ds_read_b128 v[204:207], v251 offset:2048\n" \
"v_add_u32 v239, s85, v234\n" \
"v_mfma_f32_16x16x32_bf16 v[148:151], v[40:43], v[208:211], v[148:151]\n" \
"s_waitcnt lgkmcnt(6)\n" \
"s_lshl_b32 s73, s61, 12\n" \
"ds_read_b64_tr_b16 v[20:21], v232 offset:8704\n" \
"v_mfma_f32_16x16x32_bf16 v[148:151], v[44:47], v[212:215], v[148:151]\n" \
"buffer_load_dwordx4 v224, s[48:51], s75 offen offset:0 lds\n" \
"v_mfma_f32_16x16x32_bf16 v[152:155], v[48:51], v[208:211], v[152:155]\n" \
"s_add_u32 s80, s79, 0x10000\n" \
"s_add_u32 s83, s83, s73\n" \
"ds_read_b64_tr_b16 v[22:23], v232 offset:10752\n" \
"v_mfma_f32_16x16x32_bf16 v[152:155], v[52:55], v[212:215], v[152:155]\n" \
"s_cmp_eq_u32 s63, 0\n" \
"s_cselect_b32 s85, 34816, 0\n" \
"ds_read_b64_tr_b16 v[24:25], v233 offset:512\n" \
"v_mfma_f32_16x16x32_bf16 v[156:159], v[56:59], v[208:211], v[156:159]\n" \
"s_add_u32 s73, s73, s85\n" \
"ds_read_b64_tr_b16 v[26:27], v233 offset:2560\n" \
"v_accvgpr_read_b32 v254, a27\n" \
"v_mfma_f32_16x16x32_bf16 v[156:159], v[60:63], v[212:215], v[156:159]\n" \
"buffer_load_dwordx4 v225, s[48:51], s75 offen offset:1024 lds\n" \
"v_mfma_f32_16x16x32_bf16 v[188:191], v[56:59], v[216:219], v[188:191]\n" \
"s_waitcnt vmcnt(36)\n" \
"ds_read_b128 v[208:211], v250 offset:4096\n" \
"v_mul_f32 v243, s31, v243\n" \
"v_mfma_f32_16x16x32_bf16 v[188:191], v[60:63], v[220:223], v[188:191]\n" \
"ds_read_b128 v[212:215], v251 offset:4096\n" \
"v_mul_f32 v252, s31, v252\n" \
"v_mfma_f32_16x16x32_bf16 v[184:187], v[48:51], v[216:219], v[184:187]\n" \
"s_waitcnt lgkmcnt(6)\n" \
"ds_read_b64_tr_b16 v[28:29], v233 offset:8704\n" \
"v_mul_f32 v253, s31, v253\n" \
"v_mfma_f32_16x16x32_bf16 v[184:187], v[52:55], v[220:223], v[184:187]\n" \
"buffer_load_dwordx4 v226, s[48:51], s75 offen offset:2048 lds\n" \
"v_mfma_f32_16x16x32_bf16 v[180:183], v[40:43], v[216:219], v[180:183]\n" \
"ds_read_b64_tr_b16 v[30:31], v233 offset:10752\n" \
"v_mul_f32 v254, s31, v254\n" \
"v_mfma_f32_16x16x32_bf16 v[180:183], v[44:47], v[220:223], v[180:183]\n" \
"v_cvt_pk_bf16_f32 v243, v243, v252\n" \
"v_cvt_pk_bf16_f32 v253, v253, v254\n" \
"v_mfma_f32_16x16x32_bf16 v[176:179], v[32:35], v[216:219], v[176:179]\n" \
"v_accvgpr_write_b32 a24, v243\n" \
"v_accvgpr_write_b32 a25, v253\n" \
"v_mfma_f32_16x16x32_bf16 v[176:179], v[36:39], v[220:223], v[176:179]\n" \
"buffer_load_dwordx4 v227, s[48:51], s75 offen offset:3072 lds\n" \
"v_mfma_f32_16x16x32_bf16 v[64:67], v[0:3], v[192:195], v[64:67]\n" \
"s_waitcnt vmcnt(36)\n" \
"ds_read_b128 v[216:219], v250 offset:6144\n" \
"v_accvgpr_read_b32 v243, a56\n" \
"v_mfma_f32_16x16x32_bf16 v[64:67], v[4:7], v[196:199], v[64:67]\n" \
"s_add_u32 m0, s74, 135168\n" \
"ds_read_b128 v[220:223], v251 offset:6144\n" \
"v_accvgpr_read_b32 v252, a57\n" \
"v_mfma_f32_16x16x32_bf16 v[68:71], v[8:11], v[192:195], v[68:71]\n" \
"s_waitcnt lgkmcnt(4)\n" \
"ds_read_b64_tr_b16 v[32:33], v232 offset:1024\n" \
"v_accvgpr_read_b32 v253, a58\n" \
"v_mfma_f32_16x16x32_bf16 v[68:71], v[12:15], v[196:199], v[68:71]\n" \
"buffer_load_dwordx4 v224, s[48:51], s77 offen offset:0 lds\n" \
"v_mfma_f32_16x16x32_bf16 v[72:75], v[16:19], v[192:195], v[72:75]\n" \
"ds_read_b64_tr_b16 v[34:35], v232 offset:3072\n" \
"v_accvgpr_read_b32 v254, a59\n" \
"v_mfma_f32_16x16x32_bf16 v[72:75], v[20:23], v[196:199], v[72:75]\n" \
"s_waitcnt lgkmcnt(3)\n" \
"ds_read_b64_tr_b16 v[36:37], v232 offset:9216\n" \
"v_mul_f32 v243, s31, v243\n" \
"v_mfma_f32_16x16x32_bf16 v[76:79], v[24:27], v[192:195], v[76:79]\n" \
"ds_read_b64_tr_b16 v[38:39], v232 offset:11264\n" \
"v_mul_f32 v252, s31, v252\n" \
"v_mfma_f32_16x16x32_bf16 v[76:79], v[28:31], v[196:199], v[76:79]\n" \
"buffer_load_dwordx4 v225, s[48:51], s77 offen offset:1024 lds\n" \
"v_mfma_f32_16x16x32_bf16 v[108:111], v[24:27], v[200:203], v[108:111]\n" \
"ds_read_b64_tr_b16 v[40:41], v233 offset:1024\n" \
"v_mul_f32 v253, s31, v253\n" \
"v_mfma_f32_16x16x32_bf16 v[108:111], v[28:31], v[204:207], v[108:111]\n" \
"ds_read_b64_tr_b16 v[42:43], v233 offset:3072\n" \
"v_mul_f32 v254, s31, v254\n" \
"v_mfma_f32_16x16x32_bf16 v[104:107], v[16:19], v[200:203], v[104:107]\n" \
"s_waitcnt lgkmcnt(6)\n" \
"ds_read_b64_tr_b16 v[44:45], v233 offset:9216\n" \
"v_cvt_pk_bf16_f32 v243, v243, v252\n" \
"v_mfma_f32_16x16x32_bf16 v[104:107], v[20:23], v[204:207], v[104:107]\n" \
"buffer_load_dwordx4 v226, s[48:51], s77 offen offset:2048 lds\n" \
"v_mfma_f32_16x16x32_bf16 v[100:103], v[8:11], v[200:203], v[100:103]\n" \
"ds_read_b64_tr_b16 v[46:47], v233 offset:11264\n" \
"v_cvt_pk_bf16_f32 v253, v253, v254\n" \
"v_mfma_f32_16x16x32_bf16 v[100:103], v[12:15], v[204:207], v[100:103]\n" \
"ds_read_b64_tr_b16 v[48:49], v232 offset:1536\n" \
"v_accvgpr_write_b32 a26, v243\n" \
"v_mfma_f32_16x16x32_bf16 v[96:99], v[0:3], v[200:203], v[96:99]\n" \
"ds_read_b64_tr_b16 v[50:51], v232 offset:3584\n" \
"v_accvgpr_write_b32 a27, v253\n" \
"v_mfma_f32_16x16x32_bf16 v[96:99], v[4:7], v[204:207], v[96:99]\n" \
"buffer_load_dwordx4 v227, s[48:51], s77 offen offset:3072 lds\n" \
"v_mfma_f32_16x16x32_bf16 v[128:131], v[0:3], v[208:211], v[128:131]\n" \
"ds_read_b64_tr_b16 v[52:53], v232 offset:9728\n" \
"v_accvgpr_read_b32 v243, a28\n" \
"v_mfma_f32_16x16x32_bf16 v[128:131], v[4:7], v[212:215], v[128:131]\n" \
"ds_read_b64_tr_b16 v[54:55], v232 offset:11776\n" \
"v_accvgpr_read_b32 v252, a29\n" \
"v_mfma_f32_16x16x32_bf16 v[132:135], v[8:11], v[208:211], v[132:135]\n" \
"s_add_u32 s93, s94, 8192\n" \
"ds_read_b64_tr_b16 v[56:57], v233 offset:1536\n" \
"v_accvgpr_read_b32 v253, a30\n" \
"v_mfma_f32_16x16x32_bf16 v[132:135], v[12:15], v[212:215], v[132:135]\n" \
"buffer_store_dwordx4 a[64:67], v236, s[56:59], s93 offen offset:0 sc1\n" \
"v_mfma_f32_16x16x32_bf16 v[136:139], v[16:19], v[208:211], v[136:139]\n" \
"ds_read_b64_tr_b16 v[58:59], v233 offset:3584\n" \
"v_accvgpr_read_b32 v254, a31\n" \
"v_mfma_f32_16x16x32_bf16 v[136:139], v[20:23], v[212:215], v[136:139]\n" \
"ds_read_b64_tr_b16 v[60:61], v233 offset:9728\n" \
"v_mul_f32 v243, s31, v243\n" \
"v_mfma_f32_16x16x32_bf16 v[140:143], v[24:27], v[208:211], v[140:143]\n" \
"ds_read_b64_tr_b16 v[62:63], v233 offset:11776\n" \
"v_mul_f32 v252, s31, v252\n" \
"v_mfma_f32_16x16x32_bf16 v[140:143], v[28:31], v[212:215], v[140:143]\n" \
"v_mul_f32 v253, s31, v253\n" \
"v_mul_f32 v254, s31, v254\n" \
"v_mfma_f32_16x16x32_bf16 v[172:175], v[24:27], v[216:219], v[172:175]\n" \
"v_cvt_pk_bf16_f32 v243, v243, v252\n" \
"v_cvt_pk_bf16_f32 v253, v253, v254\n" \
"v_mfma_f32_16x16x32_bf16 v[172:175], v[28:31], v[220:223], v[172:175]\n" \
"v_accvgpr_write_b32 a28, v243\n" \
"v_accvgpr_write_b32 a29, v253\n" \
"v_mfma_f32_16x16x32_bf16 v[168:171], v[16:19], v[216:219], v[168:171]\n" \
"v_accvgpr_read_b32 v243, a60\n" \
"v_accvgpr_read_b32 v252, a61\n" \
"v_mfma_f32_16x16x32_bf16 v[168:171], v[20:23], v[220:223], v[168:171]\n" \
"v_accvgpr_read_b32 v253, a62\n" \
"v_accvgpr_read_b32 v254, a63\n" \
"v_mfma_f32_16x16x32_bf16 v[164:167], v[8:11], v[216:219], v[164:167]\n" \
"v_mul_f32 v243, s31, v243\n" \
"v_mul_f32 v252, s31, v252\n" \
"v_mfma_f32_16x16x32_bf16 v[164:167], v[12:15], v[220:223], v[164:167]\n" \
"v_mul_f32 v253, s31, v253\n" \
"v_mul_f32 v254, s31, v254\n" \
"v_mfma_f32_16x16x32_bf16 v[160:163], v[0:3], v[216:219], v[160:163]\n" \
"v_cvt_pk_bf16_f32 v243, v243, v252\n" \
"v_cvt_pk_bf16_f32 v253, v253, v254\n" \
"v_mfma_f32_16x16x32_bf16 v[160:163], v[4:7], v[220:223], v[160:163]\n" \
"s_waitcnt vmcnt(35) lgkmcnt(0)\n" \
"s_barrier\n" \
"v_mfma_f32_16x16x32_bf16 v[80:83], v[32:35], v[192:195], v[80:83]\n" \
"ds_read_b64_tr_b16 v[0:1], v232 offset:16384\n" \
"v_accvgpr_write_b32 a30, v243\n" \
"v_mfma_f32_16x16x32_bf16 v[80:83], v[36:39], v[196:199], v[80:83]\n" \
"ds_read_b64_tr_b16 v[2:3], v232 offset:18432\n" \
"v_accvgpr_write_b32 a31, v253\n" \
"v_mfma_f32_16x16x32_bf16 v[84:87], v[40:43], v[192:195], v[84:87]\n" \
"ds_read_b64_tr_b16 v[4:5], v232 offset:24576\n" \
"v_accvgpr_write_b32 a56, v244\n" \
"v_mfma_f32_16x16x32_bf16 v[84:87], v[44:47], v[196:199], v[84:87]\n" \
"buffer_store_dwordx4 a[96:99], v236, s[56:59], s93 offen offset:256 sc1\n" \
"v_mfma_f32_16x16x32_bf16 v[88:91], v[48:51], v[192:195], v[88:91]\n" \
"ds_read_b64_tr_b16 v[6:7], v232 offset:26624\n" \
"v_accvgpr_write_b32 a57, v245\n" \
"v_mfma_f32_16x16x32_bf16 v[88:91], v[52:55], v[196:199], v[88:91]\n" \
"ds_read_b64_tr_b16 v[8:9], v233 offset:16384\n" \
"v_accvgpr_write_b32 a58, v246\n" \
"v_mfma_f32_16x16x32_bf16 v[92:95], v[56:59], v[192:195], v[92:95]\n" \
"ds_read_b64_tr_b16 v[10:11], v233 offset:18432\n" \
"v_accvgpr_write_b32 a59, v247\n" \
"v_mfma_f32_16x16x32_bf16 v[92:95], v[60:63], v[196:199], v[92:95]\n" \
"v_accvgpr_write_b32 a60, v248\n" \
"v_accvgpr_write_b32 a61, v249\n" \
"v_mfma_f32_16x16x32_bf16 v[124:127], v[56:59], v[200:203], v[124:127]\n" \
"s_waitcnt vmcnt(34)\n" \
"ds_read_b128 v[192:195], v230 offset:0\n" \
"v_accvgpr_write_b32 a48, 0\n" \
"v_mfma_f32_16x16x32_bf16 v[124:127], v[60:63], v[204:207], v[124:127]\n" \
"ds_read_b128 v[196:199], v231 offset:0\n" \
"v_accvgpr_write_b32 a49, 0\n" \
"v_mfma_f32_16x16x32_bf16 v[120:123], v[48:51], v[200:203], v[120:123]\n" \
"ds_read_b64_tr_b16 v[12:13], v233 offset:24576\n" \
"v_accvgpr_write_b32 a50, 0\n" \
"v_mfma_f32_16x16x32_bf16 v[120:123], v[52:55], v[204:207], v[120:123]\n" \
"v_accvgpr_write_b32 a51, 0\n" \
"v_accvgpr_write_b32 a52, 0\n" \
"v_mfma_f32_16x16x32_bf16 v[116:119], v[40:43], v[200:203], v[116:119]\n" \
"ds_read_b64_tr_b16 v[14:15], v233 offset:26624\n" \
"v_accvgpr_write_b32 a53, 0\n" \
"v_mfma_f32_16x16x32_bf16 v[116:119], v[44:47], v[204:207], v[116:119]\n" \
"s_lshr_b32 s71, s61, 1\n" \
"s_lshl_b32 s71, s71, 14\n" \
"ds_read_b64_tr_b16 v[16:17], v232 offset:16896\n" \
"v_mfma_f32_16x16x32_bf16 v[112:115], v[32:35], v[200:203], v[112:115]\n" \
"s_xor_b32 s71, s71, 0x4000\n" \
"s_and_b32 s70, s61, 1\n" \
"ds_read_b64_tr_b16 v[18:19], v232 offset:18944\n" \
"v_mfma_f32_16x16x32_bf16 v[112:115], v[36:39], v[204:207], v[112:115]\n" \
"buffer_store_dwordx4 a[72:75], v236, s[56:59], s93 offen offset:2048 sc1\n" \
"v_mfma_f32_16x16x32_bf16 v[144:147], v[32:35], v[208:211], v[144:147]\n" \
"s_waitcnt vmcnt(33)\n" \
"s_lshl_b32 s70, s70, 12\n" \
"ds_read_b128 v[200:203], v230 offset:2048\n" \
"v_mfma_f32_16x16x32_bf16 v[144:147], v[36:39], v[212:215], v[144:147]\n" \
"s_add_u32 s71, s71, s70\n" \
"ds_read_b128 v[204:207], v231 offset:2048\n" \
"v_add_u32 v224, s71, v237\n" \
"v_mfma_f32_16x16x32_bf16 v[148:151], v[40:43], v[208:211], v[148:151]\n" \
"s_and_b32 s70, s63, 1\n" \
"s_lshl_b32 s70, s70, 15\n" \
"ds_read_b64_tr_b16 v[20:21], v232 offset:25088\n" \
"v_mfma_f32_16x16x32_bf16 v[148:151], v[44:47], v[212:215], v[148:151]\n" \
"s_xor_b32 s70, s70, 0x18000\n" \
"s_add_u32 s71, s71, s70\n" \
"v_add_u32 v225, s71, v235\n" \
"v_mfma_f32_16x16x32_bf16 v[152:155], v[48:51], v[208:211], v[152:155]\n" \
"s_lshr_b32 s64, s22, 7\n" \
"s_sub_u32 s64, 3, s64\n" \
"ds_read_b64_tr_b16 v[22:23], v232 offset:27136\n" \
"v_mfma_f32_16x16x32_bf16 v[152:155], v[52:55], v[212:215], v[152:155]\n" \
"s_and_b32 s64, s64, 7\n" \
"s_and_b32 s65, %1, 1\n" \
"ds_read_b64_tr_b16 v[24:25], v233 offset:16896\n" \
"v_mfma_f32_16x16x32_bf16 v[156:159], v[56:59], v[208:211], v[156:159]\n" \
"s_lshl_b32 s65, s65, 3\n" \
"s_add_u32 s64, s64, s65\n" \
"ds_read_b64_tr_b16 v[26:27], v233 offset:18944\n" \
"v_mfma_f32_16x16x32_bf16 v[156:159], v[60:63], v[212:215], v[156:159]\n" \
"s_lshl_b32 s64, s64, 7\n" \
"s_lshr_b32 s65, %1, 3\n" \
"s_lshl_b32 s65, s65, 2\n" \
"v_mfma_f32_16x16x32_bf16 v[188:191], v[56:59], v[216:219], v[188:191]\n" \
"s_waitcnt vmcnt(31)\n" \
"s_add_u32 s65, s65, s61\n" \
"ds_read_b128 v[208:211], v230 offset:4096\n" \
"v_mfma_f32_16x16x32_bf16 v[188:191], v[60:63], v[220:223], v[188:191]\n" \
"s_mul_i32 s65, s65, 24576\n" \
"s_add_u32 s64, s64, s65\n" \
"ds_read_b128 v[212:215], v231 offset:4096\n" \
"v_mfma_f32_16x16x32_bf16 v[184:187], v[48:51], v[216:219], v[184:187]\n" \
"ds_read_b64_tr_b16 v[28:29], v233 offset:25088\n" \
"v_bfe_u32 v254, v234, 7, 2\n" \
"v_mfma_f32_16x16x32_bf16 v[184:187], v[52:55], v[220:223], v[184:187]\n" \
"v_mul_u32_u24 v254, 3, v254\n" \
"v_bfe_u32 v243, v234, 5, 2\n" \
"v_mfma_f32_16x16x32_bf16 v[180:183], v[40:43], v[216:219], v[180:183]\n" \
"ds_read_b64_tr_b16 v[30:31], v233 offset:27136\n" \
"v_min_u32 v243, 2, v243\n" \
"v_mfma_f32_16x16x32_bf16 v[180:183], v[44:47], v[220:223], v[180:183]\n" \
"v_add_u32 v254, v254, v243\n" \
"v_and_b32 v243, 16, v234\n" \
"v_mfma_f32_16x16x32_bf16 v[176:179], v[32:35], v[216:219], v[176:179]\n" \
"buffer_store_dwordx4 a[104:107], v236, s[56:59], s93 offen offset:2304 sc1\n" \
"v_mfma_f32_16x16x32_bf16 v[176:179], v[36:39], v[220:223], v[176:179]\n" \
"s_waitcnt lgkmcnt(1)\n" \
"v_lshl_add_u32 v243, v243, 2, s64\n" \
"v_lshl_add_u32 v254, v254, 11, v243\n" \
"v_mfma_f32_16x16x32_bf16 v[64:67], v[0:3], v[192:195], v[64:67]\n" \
"s_waitcnt vmcnt(30)\n" \
"s_lshl_b32 s68, s63, 2\n" \
"ds_read_b128 v[216:219], v230 offset:6144\n" \
"v_mfma_f32_16x16x32_bf16 v[64:67], v[4:7], v[196:199], v[64:67]\n" \
"s_add_u32 s68, s68, s90\n" \
"ds_read_b128 v[220:223], v231 offset:6144\n" \
"v_mfma_f32_16x16x32_bf16 v[68:71], v[8:11], v[192:195], v[68:71]\n" \
"ds_read_b64_tr_b16 v[32:33], v232 offset:17408\n" \
"v_mfma_f32_16x16x32_bf16 v[68:71], v[12:15], v[196:199], v[68:71]\n" \
"v_mov_b32 v228, s60\n" \
"v_lshlrev_b32 v229, 28, s61\n" \
"v_mfma_f32_16x16x32_bf16 v[72:75], v[16:19], v[192:195], v[72:75]\n" \
"ds_read_b64_tr_b16 v[34:35], v232 offset:19456\n" \
"v_mfma_f32_16x16x32_bf16 v[72:75], v[20:23], v[196:199], v[72:75]\n" \
"s_waitcnt lgkmcnt(3)\n" \
"ds_read_b64_tr_b16 v[36:37], v232 offset:25600\n" \
"v_mfma_f32_16x16x32_bf16 v[76:79], v[24:27], v[192:195], v[76:79]\n" \
"ds_read_b64_tr_b16 v[38:39], v232 offset:27648\n" \
"v_mfma_f32_16x16x32_bf16 v[76:79], v[28:31], v[196:199], v[76:79]\n" \
"v_mfma_f32_16x16x32_bf16 v[108:111], v[24:27], v[200:203], v[108:111]\n" \
"ds_read_b64_tr_b16 v[40:41], v233 offset:17408\n" \
"v_mfma_f32_16x16x32_bf16 v[108:111], v[28:31], v[204:207], v[108:111]\n" \
"ds_read_b64_tr_b16 v[42:43], v233 offset:19456\n" \
"v_mfma_f32_16x16x32_bf16 v[104:107], v[16:19], v[200:203], v[104:107]\n" \
"ds_read_b64_tr_b16 v[44:45], v233 offset:25600\n" \
"v_mfma_f32_16x16x32_bf16 v[104:107], v[20:23], v[204:207], v[104:107]\n" \
"v_mfma_f32_16x16x32_bf16 v[100:103], v[8:11], v[200:203], v[100:103]\n" \
"ds_read_b64_tr_b16 v[46:47], v233 offset:27648\n" \
"v_mfma_f32_16x16x32_bf16 v[100:103], v[12:15], v[204:207], v[100:103]\n" \
"ds_read_b64_tr_b16 v[48:49], v232 offset:17920\n" \
"v_mfma_f32_16x16x32_bf16 v[96:99], v[0:3], v[200:203], v[96:99]\n" \
"s_add_u32 s93, s94, 0\n" \
"ds_read_b64_tr_b16 v[50:51], v232 offset:19968\n" \
"v_mfma_f32_16x16x32_bf16 v[96:99], v[4:7], v[204:207], v[96:99]\n" \
"buffer_store_dwordx4 a[0:3], v236, s[56:59], s93 offen offset:0 sc1\n" \
"v_mfma_f32_16x16x32_bf16 v[128:131], v[0:3], v[208:211], v[128:131]\n" \
"ds_read_b64_tr_b16 v[52:53], v232 offset:26112\n" \
"v_mfma_f32_16x16x32_bf16 v[128:131], v[4:7], v[212:215], v[128:131]\n" \
"ds_read_b64_tr_b16 v[54:55], v232 offset:28160\n" \
"v_mfma_f32_16x16x32_bf16 v[132:135], v[8:11], v[208:211], v[132:135]\n" \
"ds_read_b64_tr_b16 v[56:57], v233 offset:17920\n" \
"v_mfma_f32_16x16x32_bf16 v[132:135], v[12:15], v[212:215], v[132:135]\n" \
"v_mfma_f32_16x16x32_bf16 v[136:139], v[16:19], v[208:211], v[136:139]\n" \
"ds_read_b64_tr_b16 v[58:59], v233 offset:19968\n" \
"v_mfma_f32_16x16x32_bf16 v[136:139], v[20:23], v[212:215], v[136:139]\n" \
"ds_read_b64_tr_b16 v[60:61], v233 offset:26112\n" \
"v_mfma_f32_16x16x32_bf16 v[140:143], v[24:27], v[208:211], v[140:143]\n" \
"ds_read_b64_tr_b16 v[62:63], v233 offset:28160\n" \
"v_mfma_f32_16x16x32_bf16 v[140:143], v[28:31], v[212:215], v[140:143]\n" \
"s_waitcnt lgkmcnt(3)\n" \
"v_mfma_f32_16x16x32_bf16 v[172:175], v[24:27], v[216:219], v[172:175]\n" \
"v_mfma_f32_16x16x32_bf16 v[172:175], v[28:31], v[220:223], v[172:175]\n" \
"v_mfma_f32_16x16x32_bf16 v[168:171], v[16:19], v[216:219], v[168:171]\n" \
"v_mfma_f32_16x16x32_bf16 v[168:171], v[20:23], v[220:223], v[168:171]\n" \
"v_mfma_f32_16x16x32_bf16 v[164:167], v[8:11], v[216:219], v[164:167]\n" \
"v_mfma_f32_16x16x32_bf16 v[164:167], v[12:15], v[220:223], v[164:167]\n" \
"buffer_store_dwordx4 a[32:35], v236, s[56:59], s93 offen offset:256 sc1\n" \
"v_mfma_f32_16x16x32_bf16 v[160:163], v[0:3], v[216:219], v[160:163]\n" \
"v_mfma_f32_16x16x32_bf16 v[160:163], v[4:7], v[220:223], v[160:163]\n" \
"s_waitcnt vmcnt(18) lgkmcnt(0)\n" \
"s_barrier\n" \
"v_mfma_f32_16x16x32_bf16 v[80:83], v[32:35], v[192:195], v[80:83]\n" \
"ds_read_b64_tr_b16 v[0:1], v232 offset:32768\n" \
"v_mfma_f32_16x16x32_bf16 v[80:83], v[36:39], v[196:199], v[80:83]\n" \
"ds_read_b64_tr_b16 v[2:3], v232 offset:34816\n" \
"buffer_store_dword v228, v229, s[56:59], s68 offen sc1\n" \
"v_mfma_f32_16x16x32_bf16 v[84:87], v[40:43], v[192:195], v[84:87]\n" \
"ds_read_b64_tr_b16 v[4:5], v232 offset:40960\n" \
"v_lshrrev_b32 v238, 4, v234\n" \
"v_mfma_f32_16x16x32_bf16 v[84:87], v[44:47], v[196:199], v[84:87]\n" \
"ds_write_b128 v224, a[192:195] offset:8192\n" \
"v_mfma_f32_16x16x32_bf16 v[88:91], v[48:51], v[192:195], v[88:91]\n" \
"ds_read_b64_tr_b16 v[6:7], v232 offset:43008\n" \
"v_lshrrev_b32 v240, 4, v238\n" \
"v_mfma_f32_16x16x32_bf16 v[88:91], v[52:55], v[196:199], v[88:91]\n" \
"ds_read_b64_tr_b16 v[8:9], v233 offset:32768\n" \
"v_lshlrev_b32 v240, 2, v240\n" \
"v_mfma_f32_16x16x32_bf16 v[92:95], v[56:59], v[192:195], v[92:95]\n" \
"ds_read_b64_tr_b16 v[10:11], v233 offset:34816\n" \
"v_and_b32 v238, 15, v238\n" \
"v_mfma_f32_16x16x32_bf16 v[92:95], v[60:63], v[196:199], v[92:95]\n" \
"v_sub_u32 v238, v238, v240\n" \
"v_subrev_u32 v248, 0, v238\n" \
"v_mfma_f32_16x16x32_bf16 v[124:127], v[56:59], v[200:203], v[124:127]\n" \
"ds_read_b128 v[192:195], v230 offset:32768\n" \
"v_subrev_u32 v249, 1, v238\n" \
"v_mfma_f32_16x16x32_bf16 v[124:127], v[60:63], v[204:207], v[124:127]\n" \
"ds_read_b128 v[196:199], v231 offset:32768\n" \
"v_subrev_u32 v250, 2, v238\n" \
"v_mfma_f32_16x16x32_bf16 v[120:123], v[48:51], v[200:203], v[120:123]\n" \
"ds_read_b64_tr_b16 v[12:13], v233 offset:40960\n" \
"v_subrev_u32 v251, 3, v238\n" \
"v_mfma_f32_16x16x32_bf16 v[120:123], v[52:55], v[204:207], v[120:123]\n" \
"ds_write_b128 v224, a[224:227] offset:8448\n" \
"v_mfma_f32_16x16x32_bf16 v[116:119], v[40:43], v[200:203], v[116:119]\n" \
"ds_read_b64_tr_b16 v[14:15], v233 offset:43008\n" \
"v_ashrrev_i32 v248, 31, v248\n" \
"v_mfma_f32_16x16x32_bf16 v[116:119], v[44:47], v[204:207], v[116:119]\n" \
"ds_read_b64_tr_b16 v[16:17], v232 offset:33280\n" \
"v_ashrrev_i32 v249, 31, v249\n" \
"v_mfma_f32_16x16x32_bf16 v[112:115], v[32:35], v[200:203], v[112:115]\n" \
"ds_read_b64_tr_b16 v[18:19], v232 offset:35328\n" \
"v_ashrrev_i32 v250, 31, v250\n" \
"v_mfma_f32_16x16x32_bf16 v[112:115], v[36:39], v[204:207], v[112:115]\n" \
"buffer_store_dwordx4 a[8:11], v236, s[56:59], s93 offen offset:2048 sc1\n" \
"v_mfma_f32_16x16x32_bf16 v[144:147], v[32:35], v[208:211], v[144:147]\n" \
"ds_read_b128 v[200:203], v230 offset:34816\n" \
"v_ashrrev_i32 v251, 31, v251\n" \
"v_mfma_f32_16x16x32_bf16 v[144:147], v[36:39], v[212:215], v[144:147]\n" \
"ds_read_b128 v[204:207], v231 offset:34816\n" \
"v_lshlrev_b32 v248, 23, v248\n" \
"v_mfma_f32_16x16x32_bf16 v[148:151], v[40:43], v[208:211], v[148:151]\n" \
"ds_read_b64_tr_b16 v[20:21], v232 offset:41472\n" \
"v_lshlrev_b32 v249, 23, v249\n" \
"v_mfma_f32_16x16x32_bf16 v[148:151], v[44:47], v[212:215], v[148:151]\n" \
"ds_write_b128 v224, a[200:203] offset:10240\n" \
"v_mfma_f32_16x16x32_bf16 v[152:155], v[48:51], v[208:211], v[152:155]\n" \
"ds_read_b64_tr_b16 v[22:23], v232 offset:43520\n" \
"v_lshlrev_b32 v250, 23, v250\n" \
"v_mfma_f32_16x16x32_bf16 v[152:155], v[52:55], v[212:215], v[152:155]\n" \
"ds_read_b64_tr_b16 v[24:25], v233 offset:33280\n" \
"v_lshlrev_b32 v251, 23, v251\n" \
"v_mfma_f32_16x16x32_bf16 v[156:159], v[56:59], v[208:211], v[156:159]\n" \
"ds_read_b64_tr_b16 v[26:27], v233 offset:35328\n" \
"v_mov_b32 v240, 0xff800000\n" \
"v_mfma_f32_16x16x32_bf16 v[156:159], v[60:63], v[212:215], v[156:159]\n" \
"ds_write_b128 v224, a[232:235] offset:10496\n" \
"v_mfma_f32_16x16x32_bf16 v[188:191], v[56:59], v[216:219], v[188:191]\n" \
"ds_read_b128 v[208:211], v230 offset:36864\n" \
"v_add_f32 v248, 0x42400000, v248\n" \
"v_mfma_f32_16x16x32_bf16 v[188:191], v[60:63], v[220:223], v[188:191]\n" \
"ds_read_b128 v[212:215], v231 offset:36864\n" \
"v_add_f32 v249, 0x42400000, v249\n" \
"v_mfma_f32_16x16x32_bf16 v[184:187], v[48:51], v[216:219], v[184:187]\n" \
"ds_read_b64_tr_b16 v[28:29], v233 offset:41472\n" \
"v_add_f32 v250, 0x42400000, v250\n" \
"v_mfma_f32_16x16x32_bf16 v[184:187], v[52:55], v[220:223], v[184:187]\n" \
"v_add_f32 v251, 0x42400000, v251\n" \
"s_mov_b32 s72, 0x42400000\n" \
"v_mfma_f32_16x16x32_bf16 v[180:183], v[40:43], v[216:219], v[180:183]\n" \
"s_cmp_gt_u32 s61, 0\n" \
"s_cselect_b32 s70, s72, 0xff800000\n" \
"ds_read_b64_tr_b16 v[30:31], v233 offset:43520\n" \
"v_mfma_f32_16x16x32_bf16 v[180:183], v[44:47], v[220:223], v[180:183]\n" \
"buffer_store_dwordx4 a[40:43], v236, s[56:59], s93 offen offset:2304 sc1\n" \
"v_mfma_f32_16x16x32_bf16 v[176:179], v[32:35], v[216:219], v[176:179]\n" \
"ds_write_b128 v224, a[128:131] offset:0\n" \
"v_mfma_f32_16x16x32_bf16 v[176:179], v[36:39], v[220:223], v[176:179]\n" \
"s_waitcnt lgkmcnt(2)\n" \
"v_mov_b32 v252, s70\n" \
"v_mfma_f32_16x16x32_bf16 v[64:67], v[0:3], v[192:195], v[64:67]\n" \
"ds_read_b128 v[216:219], v230 offset:38912\n" \
"v_mfma_f32_16x16x32_bf16 v[64:67], v[4:7], v[196:199], v[64:67]\n" \
"s_cmp_gt_u32 s61, 1\n" \
"s_cselect_b32 s70, s72, 0xff800000\n" \
"ds_read_b128 v[220:223], v231 offset:38912\n" \
"v_mfma_f32_16x16x32_bf16 v[68:71], v[8:11], v[192:195], v[68:71]\n" \
"ds_read_b64_tr_b16 v[32:33], v232 offset:33792\n" \
"v_mov_b32 v253, s70\n" \
"v_mfma_f32_16x16x32_bf16 v[68:71], v[12:15], v[196:199], v[68:71]\n" \
"s_cmp_gt_u32 s61, 2\n" \
"s_cselect_b32 s70, s72, 0xff800000\n" \
"v_mov_b32 v226, s70\n" \
"v_mfma_f32_16x16x32_bf16 v[72:75], v[16:19], v[192:195], v[72:75]\n" \
"ds_read_b64_tr_b16 v[34:35], v232 offset:35840\n" \
"v_bfe_i32 v243, v234, 4, 1\n" \
"v_mfma_f32_16x16x32_bf16 v[72:75], v[20:23], v[196:199], v[72:75]\n" \
"s_waitcnt lgkmcnt(3)\n" \
"ds_read_b64_tr_b16 v[36:37], v232 offset:41984\n" \
"v_and_b32 v243, 0xaaaa, v243\n" \
"v_mfma_f32_16x16x32_bf16 v[76:79], v[24:27], v[192:195], v[76:79]\n" \
"ds_read_b64_tr_b16 v[38:39], v232 offset:44032\n" \
"v_or_b32 v243, 0x4444, v243\n" \
"v_mfma_f32_16x16x32_bf16 v[76:79], v[28:31], v[196:199], v[76:79]\n" \
"ds_write_b128 v224, a[160:163] offset:256\n" \
"v_mfma_f32_16x16x32_bf16 v[108:111], v[24:27], v[200:203], v[108:111]\n" \
"ds_read_b64_tr_b16 v[40:41], v233 offset:33792\n" \
"v_bfe_i32 v244, v234, 5, 1\n" \
"v_mfma_f32_16x16x32_bf16 v[108:111], v[28:31], v[204:207], v[108:111]\n" \
"ds_read_b64_tr_b16 v[42:43], v233 offset:35840\n" \
"v_not_b32 v244, v244\n" \
"v_mfma_f32_16x16x32_bf16 v[104:107], v[16:19], v[200:203], v[104:107]\n" \
"ds_read_b64_tr_b16 v[44:45], v233 offset:41984\n" \
"v_and_b32 v244, 0x3f803f80, v244\n" \
"v_mfma_f32_16x16x32_bf16 v[104:107], v[20:23], v[204:207], v[104:107]\n" \
"v_mov_b32 v245, v244\n" \
"v_mov_b32 v246, v244\n" \
"v_mfma_f32_16x16x32_bf16 v[100:103], v[8:11], v[200:203], v[100:103]\n" \
"ds_read_b64_tr_b16 v[46:47], v233 offset:44032\n" \
"v_mov_b32 v247, v244\n" \
"v_mfma_f32_16x16x32_bf16 v[100:103], v[12:15], v[204:207], v[100:103]\n" \
"s_mov_b32 s76, s73\n" \
"s_and_b32 s77, s63, 1\n" \
"ds_read_b64_tr_b16 v[48:49], v232 offset:34304\n" \
"v_mfma_f32_16x16x32_bf16 v[96:99], v[0:3], v[200:203], v[96:99]\n" \
"s_add_u32 s93, s94, 17858560\n" \
"s_lshl_b32 s77, s77, 15\n" \
"ds_read_b64_tr_b16 v[50:51], v232 offset:36352\n" \
"v_mfma_f32_16x16x32_bf16 v[96:99], v[4:7], v[204:207], v[96:99]\n" \
"buffer_store_dwordx4 a[80:83], v234, s[56:59], s93 offen offset:0 sc1\n" \
"v_mfma_f32_16x16x32_bf16 v[128:131], v[0:3], v[208:211], v[128:131]\n" \
"s_xor_b32 s89, s77, 0x8000\n" \
"s_add_u32 s82, s89, 0x10000\n" \
"ds_read_b64_tr_b16 v[52:53], v232 offset:42496\n" \
"v_mfma_f32_16x16x32_bf16 v[128:131], v[4:7], v[212:215], v[128:131]\n" \
"s_mov_b32 s74, 0\n" \
"s_add_u32 s73, s73, 34816\n" \
"ds_read_b64_tr_b16 v[54:55], v232 offset:44544\n" \
"v_mfma_f32_16x16x32_bf16 v[132:135], v[8:11], v[208:211], v[132:135]\n" \
"s_cmp_eq_u32 s73, s83\n" \
"s_cselect_b32 s70, 34816, 0\n" \
"ds_read_b64_tr_b16 v[56:57], v233 offset:34304\n" \
"v_mfma_f32_16x16x32_bf16 v[132:135], v[12:15], v[212:215], v[132:135]\n" \
"ds_write_b128 v224, a[136:139] offset:2048\n" \
"v_mfma_f32_16x16x32_bf16 v[136:139], v[16:19], v[208:211], v[136:139]\n" \
"s_add_u32 s73, s73, s70\n" \
"s_lshr_b32 s70, s61, 1\n" \
"ds_read_b64_tr_b16 v[58:59], v233 offset:36352\n" \
"v_mfma_f32_16x16x32_bf16 v[136:139], v[20:23], v[212:215], v[136:139]\n" \
"s_lshl_b32 s85, s63, 2\n" \
"s_sub_u32 s86, 60, s85\n" \
"ds_read_b64_tr_b16 v[60:61], v233 offset:42496\n" \
"v_mfma_f32_16x16x32_bf16 v[140:143], v[24:27], v[208:211], v[140:143]\n" \
"s_add_u32 s85, s85, s70\n" \
"s_add_u32 s86, s86, s70\n" \
"ds_read_b64_tr_b16 v[62:63], v233 offset:44544\n" \
"v_mfma_f32_16x16x32_bf16 v[140:143], v[28:31], v[212:215], v[140:143]\n" \
"s_waitcnt lgkmcnt(4)\n" \
"ds_write_b128 v224, a[168:171] offset:2304\n" \
"v_mfma_f32_16x16x32_bf16 v[172:175], v[24:27], v[216:219], v[172:175]\n" \
"s_mul_i32 s87, s85, 266240\n" \
"s_mul_i32 s88, s86, 266240\n" \
"v_add_u32 v230, 0x10000, v230\n" \
"v_mfma_f32_16x16x32_bf16 v[172:175], v[28:31], v[220:223], v[172:175]\n" \
"s_and_b32 s75, s61, 1\n" \
"s_lshl_b32 s70, s75, 8\n" \
"v_add_u32 v231, 0x10000, v231\n" \
"v_mfma_f32_16x16x32_bf16 v[168:171], v[16:19], v[216:219], v[168:171]\n" \
"s_lshl_b32 s91, s62, 13\n" \
"s_add_u32 s91, s91, s70\n" \
"s_add_u32 s91, s91, 35700736\n" \
"v_mfma_f32_16x16x32_bf16 v[168:171], v[20:23], v[220:223], v[168:171]\n" \
"buffer_store_dwordx4 a[84:87], v234, s[56:59], s93 offen offset:1024 sc1\n" \
"v_mfma_f32_16x16x32_bf16 v[164:167], v[8:11], v[216:219], v[164:167]\n" \
"s_add_u32 s87, s87, s91\n" \
"s_add_u32 s88, s88, s91\n" \
"s_lshl_b32 s75, s75, 14\n" \
"v_mfma_f32_16x16x32_bf16 v[164:167], v[12:15], v[220:223], v[164:167]\n" \
"s_cmp_eq_u32 s63, 7\n" \
"s_cselect_b32 s75, 0, s75\n" \
"s_xor_b32 s78, s75, 0xc000\n" \
"s_mov_b32 s81, s78\n" \
"s_cmp_eq_u32 s63, 0\n" \
"s_cselect_b32 s91, s75, 0\n" \
"s_add_u32 s91, s91, 0x8000\n" \
"s_xor_b32 s75, s75, 0x8000\n" \
"s_sub_u32 s84, 7, s63\n" \
"v_mfma_f32_16x16x32_bf16 v[160:163], v[0:3], v[216:219], v[160:163]\n" \
"v_mfma_f32_16x16x32_bf16 v[160:163], v[4:7], v[220:223], v[160:163]\n" \
"s_waitcnt vmcnt(19) lgkmcnt(0)\n" \
"s_barrier\n" \
"v_mfma_f32_16x16x32_bf16 v[144:147], v[32:35], v[208:211], v[144:147]\n" \
"s_mov_b64 s[44:45], s[14:15]\n" \
"s_mov_b32 s46, 0\n" \
"ds_read_b64_tr_b16 v[0:1], v232 offset:49152\n" \
"v_mfma_f32_16x16x32_bf16 v[144:147], v[36:39], v[212:215], v[144:147]\n" \
"s_mov_b32 s47, 0\n" \
"s_cmp_eq_u32 s63, 7\n" \
"s_cselect_b64 s[44:45], s[18:19], s[44:45]\n" \
"s_cselect_b32 s46, 5, s46\n" \
"s_cmp_eq_u32 s63, 3\n" \
"s_cselect_b64 s[44:45], s[18:19], s[44:45]\n" \
"s_cselect_b32 s46, 3, s46\n" \
"ds_read_b64_tr_b16 v[2:3], v232 offset:51200\n" \
"v_mfma_f32_16x16x32_bf16 v[148:151], v[40:43], v[208:211], v[148:151]\n" \
"s_cmp_eq_u32 s63, 3\n" \
"s_cselect_b32 s47, 640, s47\n" \
"ds_read_b64_tr_b16 v[4:5], v232 offset:57344\n" \
"v_mfma_f32_16x16x32_bf16 v[148:151], v[44:47], v[212:215], v[148:151]\n" \
"ds_write_b128 v225, a[144:147] offset:0\n" \
"v_mfma_f32_16x16x32_bf16 v[152:155], v[48:51], v[208:211], v[152:155]\n" \
"ds_read_b64_tr_b16 v[6:7], v232 offset:59392\n" \
"v_mfma_f32_16x16x32_bf16 v[152:155], v[52:55], v[212:215], v[152:155]\n" \
"s_cmp_eq_u32 s63, 6\n" \
"s_cselect_b64 s[44:45], s[22:23], s[44:45]\n" \
"s_cselect_b32 s46, 6, s46\n" \
"ds_read_b64_tr_b16 v[8:9], v233 offset:49152\n" \
"v_mfma_f32_16x16x32_bf16 v[156:159], v[56:59], v[208:211], v[156:159]\n" \
"ds_read_b64_tr_b16 v[10:11], v233 offset:51200\n" \
"v_mfma_f32_16x16x32_bf16 v[156:159], v[60:63], v[212:215], v[156:159]\n" \
"s_cmp_eq_u32 s63, 4\n" \
"s_cselect_b64 s[44:45], s[20:21], s[44:45]\n" \
"s_cselect_b32 s46, 4, s46\n" \
"v_mfma_f32_16x16x32_bf16 v[188:191], v[56:59], v[216:219], v[188:191]\n" \
"s_waitcnt vmcnt(17)\n" \
"ds_read_b128 v[208:211], v230 offset:0\n" \
"v_mfma_f32_16x16x32_bf16 v[188:191], v[60:63], v[220:223], v[188:191]\n" \
"s_cmp_eq_u32 s63, 2\n" \
"s_cselect_b64 s[44:45], s[24:25], s[44:45]\n" \
"s_cselect_b32 s46, 2, s46\n" \
"ds_read_b128 v[212:215], v231 offset:0\n" \
"v_mfma_f32_16x16x32_bf16 v[184:187], v[48:51], v[216:219], v[184:187]\n" \
"s_lshl_b32 s48, s62, 2\n" \
"s_add_u32 s48, s48, s61\n" \
"ds_read_b64_tr_b16 v[12:13], v233 offset:57344\n" \
"v_mfma_f32_16x16x32_bf16 v[184:187], v[52:55], v[220:223], v[184:187]\n" \
"buffer_store_dwordx4 a[88:91], v234, s[56:59], s93 offen offset:2048 sc1\n" \
"v_mfma_f32_16x16x32_bf16 v[180:183], v[40:43], v[216:219], v[180:183]\n" \
"s_add_u32 s48, s48, s47\n" \
"s_lshl_b32 s48, s48, 13\n" \
"ds_read_b64_tr_b16 v[14:15], v233 offset:59392\n" \
"v_mfma_f32_16x16x32_bf16 v[180:183], v[44:47], v[220:223], v[180:183]\n" \
"s_add_u32 s44, s44, s48\n" \
"s_addc_u32 s45, s45, 0\n" \
"ds_read_b64_tr_b16 v[16:17], v232 offset:49664\n" \
"v_mfma_f32_16x16x32_bf16 v[176:179], v[32:35], v[216:219], v[176:179]\n" \
"ds_read_b64_tr_b16 v[18:19], v232 offset:51712\n" \
"v_mfma_f32_16x16x32_bf16 v[176:179], v[36:39], v[220:223], v[176:179]\n" \
"ds_write_b128 v225, a[148:151] offset:1024\n" \
"v_mfma_f32_16x16x32_bf16 v[80:83], v[32:35], v[192:195], v[80:83]\n" \
"s_waitcnt vmcnt(16)\n" \
"ds_read_b128 v[216:219], v230 offset:2048\n" \
"v_mfma_f32_16x16x32_bf16 v[80:83], v[36:39], v[196:199], v[80:83]\n" \
"ds_read_b128 v[220:223], v231 offset:2048\n" \
"v_mfma_f32_16x16x32_bf16 v[84:87], v[40:43], v[192:195], v[84:87]\n" \
"ds_read_b64_tr_b16 v[20:21], v232 offset:57856\n" \
"v_mfma_f32_16x16x32_bf16 v[84:87], v[44:47], v[196:199], v[84:87]\n" \
"ds_write_b128 v225, a[152:155] offset:2048\n" \
"v_mfma_f32_16x16x32_bf16 v[88:91], v[48:51], v[192:195], v[88:91]\n" \
"ds_read_b64_tr_b16 v[22:23], v232 offset:59904\n" \
"v_mfma_f32_16x16x32_bf16 v[88:91], v[52:55], v[196:199], v[88:91]\n" \
"ds_read_b64_tr_b16 v[24:25], v233 offset:49664\n" \
"v_mfma_f32_16x16x32_bf16 v[92:95], v[56:59], v[192:195], v[92:95]\n" \
"Lpg5005_%=:\n" \
"s_cbranch_execz Lpg6413_%=\n" \
"ds_read_b64_tr_b16 v[26:27], v233 offset:51712\n" \
"v_mfma_f32_16x16x32_bf16 v[92:95], v[60:63], v[196:199], v[92:95]\n" \
"ds_write_b128 v225, a[156:159] offset:3072\n" \
"v_mfma_f32_16x16x32_bf16 v[124:127], v[56:59], v[200:203], v[124:127]\n" \
"s_waitcnt vmcnt(14)\n" \
"ds_read_b128 v[192:195], v230 offset:4096\n" \
"v_mfma_f32_16x16x32_bf16 v[124:127], v[60:63], v[204:207], v[124:127]\n" \
"ds_read_b128 v[196:199], v231 offset:4096\n" \
"v_mfma_f32_16x16x32_bf16 v[120:123], v[48:51], v[200:203], v[120:123]\n" \
"ds_read_b64_tr_b16 v[28:29], v233 offset:57856\n" \
"v_mfma_f32_16x16x32_bf16 v[120:123], v[52:55], v[204:207], v[120:123]\n" \
"buffer_store_dwordx4 a[92:95], v234, s[56:59], s93 offen offset:3072 sc1\n" \
"v_mfma_f32_16x16x32_bf16 v[116:119], v[40:43], v[200:203], v[116:119]\n" \
"ds_read_b64_tr_b16 v[30:31], v233 offset:59904\n" \
"v_mfma_f32_16x16x32_bf16 v[116:119], v[44:47], v[204:207], v[116:119]\n" \
"v_mfma_f32_16x16x32_bf16 v[112:115], v[32:35], v[200:203], v[112:115]\n" \
"ds_write_b128 v225, a[208:211] offset:8192\n" \
"v_mfma_f32_16x16x32_bf16 v[112:115], v[36:39], v[204:207], v[112:115]\n" \
"s_waitcnt lgkmcnt(2)\n" \
"v_mfma_f32_16x16x32_bf16 v[64:67], v[0:3], v[208:211], v[64:67]\n" \
"s_waitcnt vmcnt(13)\n" \
"ds_read_b128 v[200:203], v230 offset:6144\n" \
"ds_read_b128 v[204:207], v231 offset:6144\n" \
"v_mfma_f32_16x16x32_bf16 v[64:67], v[4:7], v[212:215], v[64:67]\n" \
"ds_read_b64_tr_b16 v[32:33], v232 offset:50176\n" \
"ds_read_b64_tr_b16 v[34:35], v232 offset:52224\n" \
"v_mfma_f32_16x16x32_bf16 v[68:71], v[8:11], v[208:211], v[68:71]\n" \
"s_waitcnt lgkmcnt(5)\n" \
"ds_read_b64_tr_b16 v[36:37], v232 offset:58368\n" \
"ds_read_b64_tr_b16 v[38:39], v232 offset:60416\n" \
"v_mfma_f32_16x16x32_bf16 v[68:71], v[12:15], v[212:215], v[68:71]\n" \
"s_getpc_b64 s[64:65]\n" \
"Lpd1_%=:\n" \
"s_lshl_b32 s66, s62, 2\n" \
"ds_read_b64_tr_b16 v[40:41], v233 offset:50176\n" \
"ds_read_b64_tr_b16 v[42:43], v233 offset:52224\n" \
"v_mfma_f32_16x16x32_bf16 v[72:75], v[16:19], v[208:211], v[72:75]\n" \
"s_add_u32 s66, s66, s61\n" \
"ds_read_b64_tr_b16 v[44:45], v233 offset:58368\n" \
"ds_read_b64_tr_b16 v[46:47], v233 offset:60416\n" \
"v_mfma_f32_16x16x32_bf16 v[72:75], v[20:23], v[212:215], v[72:75]\n" \
"s_and_b32 s66, s66, 15\n" \
"ds_read_b64_tr_b16 v[48:49], v232 offset:50688\n" \
"ds_read_b64_tr_b16 v[50:51], v232 offset:52736\n" \
"v_mfma_f32_16x16x32_bf16 v[76:79], v[24:27], v[208:211], v[76:79]\n" \
"s_lshl_b32 s66, s66, 12\n" \
"ds_read_b64_tr_b16 v[52:53], v232 offset:58880\n" \
"ds_read_b64_tr_b16 v[54:55], v232 offset:60928\n" \
"v_mfma_f32_16x16x32_bf16 v[76:79], v[28:31], v[212:215], v[76:79]\n" \
"ds_read_b64_tr_b16 v[56:57], v233 offset:50688\n" \
"ds_read_b64_tr_b16 v[58:59], v233 offset:52736\n" \
"v_mfma_f32_16x16x32_bf16 v[96:99], v[0:3], v[216:219], v[96:99]\n" \
"ds_read_b64_tr_b16 v[60:61], v233 offset:58880\n" \
"ds_read_b64_tr_b16 v[62:63], v233 offset:60928\n" \
"v_mfma_f32_16x16x32_bf16 v[96:99], v[4:7], v[220:223], v[96:99]\n" \
"s_add_u32 s93, s94, 17850368\n" \
"s_add_u32 s64, s64, Lloop2_%=-Lpd1_%=\n" \
"s_addc_u32 s65, s65, 0\n" \
"s_mov_b32 s67, Lcode_end_%=-Lloop2_%=-4\n" \
"v_mfma_f32_16x16x32_bf16 v[100:103], v[8:11], v[216:219], v[100:103]\n" \
"buffer_store_dwordx4 a[16:19], v234, s[56:59], s93 offen offset:0 sc1\n" \
"v_mfma_f32_16x16x32_bf16 v[100:103], v[12:15], v[220:223], v[100:103]\n" \
"ds_write_b128 v225, a[212:215] offset:9216\n" \
"v_mfma_f32_16x16x32_bf16 v[104:107], v[16:19], v[216:219], v[104:107]\n" \
"buffer_load_dword v227, v242, s[56:59], s90 offen sc1\n" \
"v_mfma_f32_16x16x32_bf16 v[104:107], v[20:23], v[220:223], v[104:107]\n" \
"s_waitcnt lgkmcnt(1)\n" \
"v_lshl_add_u32 v230, v234, 2, s66\n" \
"v_min_u32 v230, s67, v230\n" \
"v_mfma_f32_16x16x32_bf16 v[108:111], v[24:27], v[216:219], v[108:111]\n" \
"ds_write_b128 v225, a[216:219] offset:10240\n" \
"v_mfma_f32_16x16x32_bf16 v[108:111], v[28:31], v[220:223], v[108:111]\n" \
"global_load_dword v230, v230, s[64:65]\n" \
"v_mfma_f32_16x16x32_bf16 v[80:83], v[32:35], v[208:211], v[80:83]\n" \
"v_cvt_pk_bf16_f32 v64, v64, v65\n" \
"v_cvt_pk_bf16_f32 v65, v66, v67\n" \
"v_mfma_f32_16x16x32_bf16 v[80:83], v[36:39], v[212:215], v[80:83]\n" \
"v_cvt_pk_bf16_f32 v66, v68, v69\n" \
"v_cvt_pk_bf16_f32 v67, v70, v71\n" \
"v_accvgpr_write_b32 a128, v64\n" \
"v_mfma_f32_16x16x32_bf16 v[84:87], v[40:43], v[208:211], v[84:87]\n" \
"v_accvgpr_write_b32 a129, v65\n" \
"v_accvgpr_write_b32 a130, v66\n" \
"v_mfma_f32_16x16x32_bf16 v[84:87], v[44:47], v[212:215], v[84:87]\n" \
"ds_write_b128 v225, a[220:223] offset:11264\n" \
"v_mfma_f32_16x16x32_bf16 v[88:91], v[48:51], v[208:211], v[88:91]\n" \
"v_accvgpr_write_b32 a131, v67\n" \
"v_cvt_pk_bf16_f32 v72, v72, v73\n" \
"v_mfma_f32_16x16x32_bf16 v[88:91], v[52:55], v[212:215], v[88:91]\n" \
"v_cvt_pk_bf16_f32 v73, v74, v75\n" \
"v_cvt_pk_bf16_f32 v74, v76, v77\n" \
"v_cvt_pk_bf16_f32 v75, v78, v79\n" \
"v_mfma_f32_16x16x32_bf16 v[92:95], v[56:59], v[208:211], v[92:95]\n" \
"v_accvgpr_write_b32 a132, v72\n" \
"v_accvgpr_write_b32 a133, v73\n" \
"v_mfma_f32_16x16x32_bf16 v[92:95], v[60:63], v[212:215], v[92:95]\n" \
"v_accvgpr_write_b32 a134, v74\n" \
"v_accvgpr_write_b32 a135, v75\n" \
"v_mfma_f32_16x16x32_bf16 v[112:115], v[32:35], v[216:219], v[112:115]\n" \
"v_cvt_pk_bf16_f32 v96, v96, v97\n" \
"v_cvt_pk_bf16_f32 v97, v98, v99\n" \
"v_mfma_f32_16x16x32_bf16 v[112:115], v[36:39], v[220:223], v[112:115]\n" \
"buffer_store_dwordx4 a[20:23], v234, s[56:59], s93 offen offset:1024 sc1\n" \
"v_mfma_f32_16x16x32_bf16 v[116:119], v[40:43], v[216:219], v[116:119]\n" \
"v_cvt_pk_bf16_f32 v98, v100, v101\n" \
"v_cvt_pk_bf16_f32 v99, v102, v103\n" \
"v_mfma_f32_16x16x32_bf16 v[116:119], v[44:47], v[220:223], v[116:119]\n" \
"v_cvt_pk_bf16_f32 v104, v104, v105\n" \
"v_cvt_pk_bf16_f32 v105, v106, v107\n" \
"v_cvt_pk_bf16_f32 v106, v108, v109\n" \
"v_mfma_f32_16x16x32_bf16 v[120:123], v[48:51], v[216:219], v[120:123]\n" \
"v_cvt_pk_bf16_f32 v107, v110, v111\n" \
"v_mfma_f32_16x16x32_bf16 v[120:123], v[52:55], v[220:223], v[120:123]\n" \
"v_cvt_pk_bf16_f32 v80, v80, v81\n" \
"v_cvt_pk_bf16_f32 v81, v82, v83\n" \
"v_mfma_f32_16x16x32_bf16 v[124:127], v[56:59], v[216:219], v[124:127]\n" \
"v_cvt_pk_bf16_f32 v82, v84, v85\n" \
"v_cvt_pk_bf16_f32 v83, v86, v87\n" \
"v_mfma_f32_16x16x32_bf16 v[124:127], v[60:63], v[220:223], v[124:127]\n" \
"v_accvgpr_write_b32 a136, v80\n" \
"s_waitcnt lgkmcnt(0)\n" \
"s_barrier\n" \
"v_mfma_f32_16x16x32_bf16 v[128:131], v[0:3], v[192:195], v[128:131]\n" \
"v_accvgpr_write_b32 a137, v81\n" \
"v_accvgpr_write_b32 a138, v82\n" \
"v_mfma_f32_16x16x32_bf16 v[128:131], v[4:7], v[196:199], v[128:131]\n" \
"ds_read_b128 a[192:195], v237 offset:0\n" \
"v_accvgpr_write_b32 a139, v83\n" \
"v_mfma_f32_16x16x32_bf16 v[132:135], v[8:11], v[192:195], v[132:135]\n" \
"v_cvt_pk_bf16_f32 v88, v88, v89\n" \
"v_cvt_pk_bf16_f32 v89, v90, v91\n" \
"v_mfma_f32_16x16x32_bf16 v[132:135], v[12:15], v[196:199], v[132:135]\n" \
"ds_read_b128 a[196:199], v237 offset:2048\n" \
"v_cvt_pk_bf16_f32 v90, v92, v93\n" \
"v_mfma_f32_16x16x32_bf16 v[136:139], v[16:19], v[192:195], v[136:139]\n" \
"v_cvt_pk_bf16_f32 v91, v94, v95\n" \
"v_med3_f32 v208, v248, v252, s72\n" \
"v_mfma_f32_16x16x32_bf16 v[136:139], v[20:23], v[196:199], v[136:139]\n" \
"ds_read_b128 a[200:203], v237 offset:4096\n" \
"v_med3_f32 v209, v249, v252, s72\n" \
"v_med3_f32 v210, v250, v252, s72\n" \
"v_mfma_f32_16x16x32_bf16 v[140:143], v[24:27], v[192:195], v[140:143]\n" \
"v_med3_f32 v211, v251, v252, s72\n" \
"v_cvt_pk_bf16_f32 v112, v112, v113\n" \
"v_mfma_f32_16x16x32_bf16 v[140:143], v[28:31], v[196:199], v[140:143]\n" \
"ds_read_b128 a[204:207], v237 offset:6144\n" \
"v_cvt_pk_bf16_f32 v113, v114, v115\n" \
"v_mfma_f32_16x16x32_bf16 v[144:147], v[32:35], v[192:195], v[144:147]\n" \
"v_cvt_pk_bf16_f32 v114, v116, v117\n" \
"v_cvt_pk_bf16_f32 v115, v118, v119\n" \
"v_mfma_f32_16x16x32_bf16 v[144:147], v[36:39], v[196:199], v[144:147]\n" \
"ds_read_b128 a[208:211], v237 offset:256\n" \
"v_cvt_pk_bf16_f32 v120, v120, v121\n" \
"v_cvt_pk_bf16_f32 v121, v122, v123\n" \
"v_mfma_f32_16x16x32_bf16 v[148:151], v[40:43], v[192:195], v[148:151]\n" \
"buffer_store_dwordx4 a[24:27], v234, s[56:59], s93 offen offset:2048 sc1\n" \
"v_mfma_f32_16x16x32_bf16 v[148:151], v[44:47], v[196:199], v[148:151]\n" \
"ds_read_b128 a[212:215], v237 offset:2304\n" \
"v_cvt_pk_bf16_f32 v122, v124, v125\n" \
"v_mfma_f32_16x16x32_bf16 v[152:155], v[48:51], v[192:195], v[152:155]\n" \
"v_cvt_pk_bf16_f32 v123, v126, v127\n" \
"v_mfma_f32_16x16x32_bf16 v[152:155], v[52:55], v[196:199], v[152:155]\n" \
"ds_read_b128 a[216:219], v237 offset:4352\n" \
"v_cvt_pk_bf16_f32 v128, v128, v129\n" \
"v_mfma_f32_16x16x32_bf16 v[156:159], v[56:59], v[192:195], v[156:159]\n" \
"v_cvt_pk_bf16_f32 v129, v130, v131\n" \
"v_cvt_pk_bf16_f32 v130, v132, v133\n" \
"v_mfma_f32_16x16x32_bf16 v[156:159], v[60:63], v[196:199], v[156:159]\n" \
"ds_read_b128 a[220:223], v237 offset:6400\n" \
"v_cvt_pk_bf16_f32 v131, v134, v135\n" \
"v_cvt_pk_bf16_f32 v136, v136, v137\n" \
"v_mfma_f32_16x16x32_bf16 v[160:163], v[0:3], v[200:203], v[160:163]\n" \
"v_cvt_pk_bf16_f32 v137, v138, v139\n" \
"v_cvt_pk_bf16_f32 v138, v140, v141\n" \
"v_mfma_f32_16x16x32_bf16 v[160:163], v[4:7], v[204:207], v[160:163]\n" \
"ds_read_b128 a[224:227], v237 offset:8192\n" \
"v_cvt_pk_bf16_f32 v139, v142, v143\n" \
"v_mfma_f32_16x16x32_bf16 v[164:167], v[8:11], v[200:203], v[164:167]\n" \
"v_mfma_f32_16x16x32_bf16 v[164:167], v[12:15], v[204:207], v[164:167]\n" \
"ds_read_b128 a[228:231], v237 offset:10240\n" \
"v_mfma_f32_16x16x32_bf16 v[168:171], v[16:19], v[200:203], v[168:171]\n" \
"v_mfma_f32_16x16x32_bf16 v[168:171], v[20:23], v[204:207], v[168:171]\n" \
"ds_read_b128 a[232:235], v237 offset:12288\n" \
"v_cvt_pk_bf16_f32 v144, v144, v145\n" \
"v_cvt_pk_bf16_f32 v145, v146, v147\n" \
"v_mfma_f32_16x16x32_bf16 v[172:175], v[24:27], v[200:203], v[172:175]\n" \
"v_cvt_pk_bf16_f32 v146, v148, v149\n" \
"v_cvt_pk_bf16_f32 v147, v150, v151\n" \
"v_mfma_f32_16x16x32_bf16 v[172:175], v[28:31], v[204:207], v[172:175]\n" \
"ds_read_b128 a[236:239], v237 offset:14336\n" \
"v_cvt_pk_bf16_f32 v152, v152, v153\n" \
"v_mfma_f32_16x16x32_bf16 v[176:179], v[32:35], v[200:203], v[176:179]\n" \
"v_cvt_pk_bf16_f32 v153, v154, v155\n" \
"v_cvt_pk_bf16_f32 v154, v156, v157\n" \
"v_mfma_f32_16x16x32_bf16 v[176:179], v[36:39], v[204:207], v[176:179]\n" \
"ds_read_b128 a[240:243], v237 offset:8448\n" \
"v_cvt_pk_bf16_f32 v155, v158, v159\n" \
"v_mfma_f32_16x16x32_bf16 v[180:183], v[40:43], v[200:203], v[180:183]\n" \
"buffer_store_dwordx4 a[28:31], v234, s[56:59], s93 offen offset:3072 sc1\n" \
"v_mfma_f32_16x16x32_bf16 v[180:183], v[44:47], v[204:207], v[180:183]\n" \
"ds_read_b128 a[244:247], v237 offset:10496\n" \
"ds_write_b128 v239, v[128:131] offset:0\n" \
"v_mov_b32 v196, 0x42400000\n" \
"v_mfma_f32_16x16x32_bf16 v[184:187], v[48:51], v[200:203], v[184:187]\n" \
"v_mov_b32 v197, 0x42400000\n" \
"v_mov_b32 v198, 0x42400000\n" \
"v_mfma_f32_16x16x32_bf16 v[184:187], v[52:55], v[204:207], v[184:187]\n" \
"ds_read_b128 a[248:251], v237 offset:12544\n" \
"v_mov_b32 v199, 0x42400000\n" \
"v_cvt_pk_bf16_f32 v160, v160, v161\n" \
"v_mfma_f32_16x16x32_bf16 v[188:191], v[56:59], v[200:203], v[188:191]\n" \
"v_cvt_pk_bf16_f32 v161, v162, v163\n" \
"v_cvt_pk_bf16_f32 v162, v164, v165\n" \
"v_mfma_f32_16x16x32_bf16 v[188:191], v[60:63], v[204:207], v[188:191]\n" \
"s_waitcnt lgkmcnt(3)\n" \
"ds_read_b128 a[252:255], v237 offset:14592\n" \
"v_cvt_pk_bf16_f32 v163, v166, v167\n" \
"v_mfma_f32_16x16x32_bf16 v[16:19], a[192:195], v[96:99], v[208:211]\n" \
"v_cvt_pk_bf16_f32 v168, v168, v169\n" \
"v_cvt_pk_bf16_f32 v169, v170, v171\n" \
"v_mfma_f32_16x16x32_bf16 v[16:19], a[196:199], v[104:107], v[16:19]\n" \
"v_cvt_pk_bf16_f32 v170, v172, v173\n" \
"v_cvt_pk_bf16_f32 v171, v174, v175\n" \
"v_mfma_f32_16x16x32_bf16 v[16:19], a[200:203], v[112:115], v[16:19]\n" \
"v_cvt_pk_bf16_f32 v176, v176, v177\n" \
"v_cvt_pk_bf16_f32 v177, v178, v179\n" \
"v_mfma_f32_16x16x32_bf16 v[16:19], a[204:207], v[120:123], v[16:19]\n" \
"v_cvt_pk_bf16_f32 v178, v180, v181\n" \
"v_cvt_pk_bf16_f32 v179, v182, v183\n" \
"v_mfma_f32_16x16x32_bf16 v[32:35], a[204:207], v[152:155], v[196:199]\n" \
"v_cvt_pk_bf16_f32 v184, v184, v185\n" \
"v_cvt_pk_bf16_f32 v185, v186, v187\n" \
"v_mfma_f32_16x16x32_bf16 v[32:35], a[200:203], v[144:147], v[32:35]\n" \
"v_cvt_pk_bf16_f32 v186, v188, v189\n" \
"v_cvt_pk_bf16_f32 v187, v190, v191\n" \
"v_mfma_f32_16x16x32_bf16 v[32:35], a[196:199], v[136:139], v[32:35]\n" \
"v_med3_f32 v212, v248, v253, v252\n" \
"v_med3_f32 v213, v249, v253, v252\n" \
"v_mfma_f32_16x16x32_bf16 v[32:35], a[192:195], v[128:131], v[32:35]\n" \
"v_med3_f32 v214, v250, v253, v252\n" \
"v_med3_f32 v215, v251, v253, v252\n" \
"v_mfma_f32_16x16x32_bf16 v[48:51], a[192:195], v[160:163], v[196:199]\n" \
"v_med3_f32 v216, v248, v226, v253\n" \
"v_med3_f32 v217, v249, v226, v253\n" \
"v_mfma_f32_16x16x32_bf16 v[48:51], a[196:199], v[168:171], v[48:51]\n" \
"ds_read_b128 a[192:195], v237 offset:16384\n" \
"v_med3_f32 v218, v250, v226, v253\n" \
"v_mfma_f32_16x16x32_bf16 v[48:51], a[200:203], v[176:179], v[48:51]\n" \
"ds_read_b128 a[196:199], v237 offset:18432\n" \
"v_med3_f32 v219, v251, v226, v253\n" \
"v_mfma_f32_16x16x32_bf16 v[48:51], a[204:207], v[184:187], v[48:51]\n" \
"ds_read_b128 a[200:203], v237 offset:20480\n" \
"v_med3_f32 v220, v248, v240, v226\n" \
"v_mfma_f32_16x16x32_bf16 v[52:55], a[220:223], v[184:187], v[196:199]\n" \
"ds_read_b128 a[204:207], v237 offset:22528\n" \
"v_med3_f32 v221, v249, v240, v226\n" \
"v_mfma_f32_16x16x32_bf16 v[52:55], a[216:219], v[176:179], v[52:55]\n" \
"ds_write_b128 v239, v[136:139] offset:1024\n" \
"v_mfma_f32_16x16x32_bf16 v[52:55], a[212:215], v[168:171], v[52:55]\n" \
"v_med3_f32 v222, v250, v240, v226\n" \
"v_med3_f32 v223, v251, v240, v226\n" \
"v_mfma_f32_16x16x32_bf16 v[52:55], a[208:211], v[160:163], v[52:55]\n" \
"v_accvgpr_write_b32 a144, v96\n" \
"v_accvgpr_write_b32 a145, v97\n" \
"v_mfma_f32_16x16x32_bf16 v[36:39], a[208:211], v[128:131], v[196:199]\n" \
"ds_write_b128 v239, v[144:147] offset:2048\n" \
"v_mfma_f32_16x16x32_bf16 v[36:39], a[212:215], v[136:139], v[36:39]\n" \
"v_accvgpr_write_b32 a146, v98\n" \
"v_accvgpr_write_b32 a147, v99\n" \
"v_mfma_f32_16x16x32_bf16 v[36:39], a[216:219], v[144:147], v[36:39]\n" \
"v_accvgpr_write_b32 a148, v104\n" \
"v_accvgpr_write_b32 a149, v105\n" \
"v_mfma_f32_16x16x32_bf16 v[36:39], a[220:223], v[152:155], v[36:39]\n" \
"ds_write_b128 v239, v[152:155] offset:3072\n" \
"v_mfma_f32_16x16x32_bf16 v[20:23], a[220:223], v[120:123], v[212:215]\n" \
"v_accvgpr_write_b32 a150, v106\n" \
"v_accvgpr_write_b32 a151, v107\n" \
"v_mfma_f32_16x16x32_bf16 v[20:23], a[216:219], v[112:115], v[20:23]\n" \
"ds_read_b128 a[220:223], v237 offset:22784\n" \
"v_accvgpr_write_b32 a152, v112\n" \
"v_mfma_f32_16x16x32_bf16 v[20:23], a[212:215], v[104:107], v[20:23]\n" \
"ds_read_b128 a[216:219], v237 offset:20736\n" \
"v_accvgpr_write_b32 a153, v113\n" \
"v_mfma_f32_16x16x32_bf16 v[20:23], a[208:211], v[96:99], v[20:23]\n" \
"ds_read_b128 a[212:215], v237 offset:18688\n" \
"v_accvgpr_write_b32 a154, v114\n" \
"v_mfma_f32_16x16x32_bf16 v[24:27], a[224:227], v[96:99], v[216:219]\n" \
"ds_read_b128 a[208:211], v237 offset:16640\n" \
"v_accvgpr_write_b32 a155, v115\n" \
"v_mfma_f32_16x16x32_bf16 v[24:27], a[228:231], v[104:107], v[24:27]\n" \
"ds_write_b128 v239, v[160:163] offset:4096\n" \
"v_mfma_f32_16x16x32_bf16 v[24:27], a[232:235], v[112:115], v[24:27]\n" \
"v_accvgpr_write_b32 a156, v120\n" \
"v_accvgpr_write_b32 a157, v121\n" \
"v_mfma_f32_16x16x32_bf16 v[24:27], a[236:239], v[120:123], v[24:27]\n" \
"v_accvgpr_write_b32 a158, v122\n" \
"v_accvgpr_write_b32 a159, v123\n" \
"v_mfma_f32_16x16x32_bf16 v[40:43], a[236:239], v[152:155], v[196:199]\n" \
"ds_write_b128 v239, v[168:171] offset:5120\n" \
"v_mfma_f32_16x16x32_bf16 v[40:43], a[232:235], v[144:147], v[40:43]\n" \
"v_accvgpr_write_b32 a140, v88\n" \
"v_accvgpr_write_b32 a141, v89\n" \
"v_mfma_f32_16x16x32_bf16 v[40:43], a[228:231], v[136:139], v[40:43]\n" \
"v_accvgpr_write_b32 a142, v90\n" \
"v_accvgpr_write_b32 a143, v91\n" \
"v_mfma_f32_16x16x32_bf16 v[40:43], a[224:227], v[128:131], v[40:43]\n" \
"ds_write_b128 v239, v[176:179] offset:6144\n" \
"v_mfma_f32_16x16x32_bf16 v[56:59], a[224:227], v[160:163], v[196:199]\n" \
"v_add_u32 v233, s82, v235\n" \
"v_mfma_f32_16x16x32_bf16 v[56:59], a[228:231], v[168:171], v[56:59]\n" \
"ds_read_b128 a[224:227], v237 offset:24576\n" \
"v_mfma_f32_16x16x32_bf16 v[56:59], a[232:235], v[176:179], v[56:59]\n" \
"ds_read_b128 a[228:231], v237 offset:26624\n" \
"ds_write_b128 v239, v[184:187] offset:7168\n" \
"v_exp_f32 v16, v16\n" \
"v_mfma_f32_16x16x32_bf16 v[56:59], a[236:239], v[184:187], v[56:59]\n" \
"s_waitcnt lgkmcnt(4)\n" \
"ds_read_b128 a[232:235], v237 offset:28672\n" \
"v_exp_f32 v17, v17\n" \
"v_mfma_f32_16x16x32_bf16 v[60:63], a[252:255], v[184:187], v[196:199]\n" \
"ds_read_b128 a[236:239], v237 offset:30720\n" \
"v_mfma_f32_16x16x32_bf16 v[60:63], a[248:251], v[176:179], v[60:63]\n" \
"s_waitcnt vmcnt(4)\n" \
"v_cmp_eq_u32 vcc, s60, v227\n" \
"v_mfma_f32_16x16x32_bf16 v[60:63], a[244:247], v[168:171], v[60:63]\n" \
"v_exp_f32 v18, v18\n" \
"v_mfma_f32_16x16x32_bf16 v[60:63], a[240:243], v[160:163], v[60:63]\n" \
"s_andn2_b32 s70, 0xff, vcc_lo\n" \
"s_cbranch_scc1 Lpl1_%=\n" \
"Lpr1_%=:\n" \
"v_cvt_pk_bf16_f32 v16, v16, v17\n" \
"v_exp_f32 v19, v19\n" \
"v_exp_f32 v20, v20\n" \
"v_mfma_f32_16x16x32_bf16 v[44:47], a[240:243], v[128:131], v[196:199]\n" \
"v_exp_f32 v21, v21\n" \
"v_mfma_f32_16x16x32_bf16 v[44:47], a[244:247], v[136:139], v[44:47]\n" \
"s_add_u32 m0, s79, 32768\n" \
"v_exp_f32 v22, v22\n" \
"v_mfma_f32_16x16x32_bf16 v[44:47], a[248:251], v[144:147], v[44:47]\n" \
"s_add_u32 s71, s76, 0\n" \
"v_cvt_pk_bf16_f32 v17, v18, v19\n" \
"v_cvt_pk_bf16_f32 v18, v20, v21\n" \
"v_mfma_f32_16x16x32_bf16 v[44:47], a[252:255], v[152:155], v[44:47]\n" \
"buffer_load_dwordx4 v234, s[36:39], s71 offen offset:0 sc1 lds\n" \
"v_exp_f32 v23, v23\n" \
"v_exp_f32 v32, v32\n" \
"v_mfma_f32_16x16x32_bf16 v[28:31], a[252:255], v[120:123], v[220:223]\n" \
"v_exp_f32 v33, v33\n" \
"v_mfma_f32_16x16x32_bf16 v[28:31], a[248:251], v[112:115], v[28:31]\n" \
"ds_read_b128 a[252:255], v237 offset:30976\n" \
"v_cvt_pk_bf16_f32 v19, v22, v23\n" \
"v_mfma_f32_16x16x32_bf16 v[28:31], a[244:247], v[104:107], v[28:31]\n" \
"buffer_load_dwordx4 v234, s[36:39], s71 offen offset:1024 sc1 lds\n" \
"v_mfma_f32_16x16x32_bf16 v[28:31], a[240:243], v[96:99], v[28:31]\n" \
"ds_read_b128 a[244:247], v237 offset:26880\n" \
"ds_read_b128 a[248:251], v237 offset:28928\n" \
"v_exp_f32 v34, v34\n" \
"v_exp_f32 v35, v35\n" \
"v_mfma_f32_16x16x32_bf16 v[80:83], a[192:195], a[144:147], v[196:199]\n" \
"ds_read_b128 a[240:243], v237 offset:24832\n" \
"v_cvt_pk_bf16_f32 v32, v32, v33\n" \
"v_mfma_f32_16x16x32_bf16 v[80:83], a[196:199], a[148:151], v[80:83]\n" \
"buffer_load_dwordx4 v234, s[36:39], s71 offen offset:2048 sc1 lds\n" \
"v_mfma_f32_16x16x32_bf16 v[80:83], a[200:203], a[152:155], v[80:83]\n" \
"s_lshl_b32 s68, s63, 2\n" \
"s_add_u32 s68, s68, s95\n" \
"s_mov_b32 s69, 0\n" \
"v_mfma_f32_16x16x32_bf16 v[80:83], a[204:207], a[156:159], v[80:83]\n" \
"s_mov_b32 s89, s63\n" \
"ds_read_b128 a[44:47], v233 offset:15360\n" \
"v_mov_b32 v192, s60\n" \
"v_lshlrev_b32 v193, 28, s61\n" \
"v_exp_f32 v36, v36\n" \
"v_exp_f32 v37, v37\n" \
"v_mfma_f32_16x16x32_bf16 v[96:99], a[204:207], v[152:155], v[196:199]\n" \
"buffer_load_dwordx4 v234, s[36:39], s71 offen offset:3072 sc1 lds\n" \
"v_mfma_f32_16x16x32_bf16 v[96:99], a[200:203], v[144:147], v[96:99]\n" \
"s_add_u32 m0, s79, 49152\n" \
"s_mov_b32 s90, 0\n" \
"ds_read_b128 a[40:43], v233 offset:7168\n" \
"v_mfma_f32_16x16x32_bf16 v[96:99], a[196:199], v[136:139], v[96:99]\n" \
"s_add_u32 s71, s76, 17408\n" \
"v_exp_f32 v38, v38\n" \
"v_mfma_f32_16x16x32_bf16 v[96:99], a[192:195], v[128:131], v[96:99]\n" \
"buffer_load_dwordx4 v234, s[36:39], s71 offen offset:0 sc1 lds\n" \
"v_cvt_pk_bf16_f32 v33, v34, v35\n" \
"v_exp_f32 v39, v39\n" \
"v_mfma_f32_16x16x32_bf16 v[112:115], a[192:195], v[160:163], v[196:199]\n" \
"ds_read_b128 a[36:39], v233 offset:14336\n" \
"v_cvt_pk_bf16_f32 v34, v36, v37\n" \
"v_mfma_f32_16x16x32_bf16 v[112:115], a[196:199], v[168:171], v[112:115]\n" \
"v_exp_f32 v48, v48\n" \
"v_mfma_f32_16x16x32_bf16 v[112:115], a[200:203], v[176:179], v[112:115]\n" \
"buffer_load_dwordx4 v234, s[36:39], s71 offen offset:1024 sc1 lds\n" \
"v_mfma_f32_16x16x32_bf16 v[112:115], a[204:207], v[184:187], v[112:115]\n" \
"ds_read_b128 a[32:35], v233 offset:6144\n" \
"v_cvt_pk_bf16_f32 v35, v38, v39\n" \
"v_exp_f32 v49, v49\n" \
"v_exp_f32 v50, v50\n" \
"v_mfma_f32_16x16x32_bf16 v[64:67], a[204:207], a[140:143], v[208:211]\n" \
"v_exp_f32 v51, v51\n" \
"v_mfma_f32_16x16x32_bf16 v[64:67], a[200:203], a[136:139], v[64:67]\n" \
"buffer_load_dwordx4 v234, s[36:39], s71 offen offset:2048 sc1 lds\n" \
"v_mfma_f32_16x16x32_bf16 v[64:67], a[196:199], a[132:135], v[64:67]\n" \
"ds_read_b128 a[204:207], v233 offset:9216\n" \
"v_cvt_pk_bf16_f32 v48, v48, v49\n" \
"v_mfma_f32_16x16x32_bf16 v[64:67], a[192:195], a[128:131], v[64:67]\n" \
"v_exp_f32 v52, v52\n" \
"v_exp_f32 v53, v53\n" \
"v_exp_f32 v54, v54\n" \
"v_mfma_f32_16x16x32_bf16 v[68:71], a[208:211], a[128:131], v[212:215]\n" \
"buffer_load_dwordx4 v234, s[36:39], s71 offen offset:3072 sc1 lds\n" \
"v_mfma_f32_16x16x32_bf16 v[68:71], a[212:215], a[132:135], v[68:71]\n" \
"ds_read_b128 a[200:203], v233 offset:1024\n" \
"v_cvt_pk_bf16_f32 v49, v50, v51\n" \
"v_mfma_f32_16x16x32_bf16 v[68:71], a[216:219], a[136:139], v[68:71]\n" \
"s_add_u32 m0, s77, s80\n" \
"v_exp_f32 v55, v55\n" \
"v_cvt_pk_bf16_f32 v50, v52, v53\n" \
"v_mfma_f32_16x16x32_bf16 v[68:71], a[220:223], a[140:143], v[68:71]\n" \
"buffer_load_dwordx4 v234, s[40:43], s76 offen offset:0 sc1 lds\n" \
"v_cvt_pk_bf16_f32 v51, v54, v55\n" \
"v_mfma_f32_16x16x32_bf16 v[116:119], a[220:223], v[184:187], v[196:199]\n" \
"ds_read_b128 a[196:199], v233 offset:8192\n" \
"v_mfma_f32_16x16x32_bf16 v[116:119], a[216:219], v[176:179], v[116:119]\n" \
"ds_read_b128 a[160:163], v239 offset:0\n" \
"v_mfma_f32_16x16x32_bf16 v[116:119], a[212:215], v[168:171], v[116:119]\n" \
"s_add_u32 s71, s76, 17408\n" \
"ds_read_b128 a[192:195], v233 offset:0\n" \
"v_exp_f32 v24, v24\n" \
"v_mfma_f32_16x16x32_bf16 v[116:119], a[208:211], v[160:163], v[116:119]\n" \
"buffer_load_dwordx4 v234, s[40:43], s76 offen offset:1024 sc1 lds\n" \
"v_mfma_f32_16x16x32_bf16 v[100:103], a[208:211], v[128:131], v[196:199]\n" \
"ds_read_b128 a[164:167], v239 offset:1024\n" \
"v_mfma_f32_16x16x32_bf16 v[100:103], a[212:215], v[136:139], v[100:103]\n" \
"v_exp_f32 v25, v25\n" \
"v_mfma_f32_16x16x32_bf16 v[100:103], a[216:219], v[144:147], v[100:103]\n" \
"v_exp_f32 v26, v26\n" \
"v_mfma_f32_16x16x32_bf16 v[100:103], a[220:223], v[152:155], v[100:103]\n" \
"buffer_load_dwordx4 v234, s[40:43], s76 offen offset:2048 sc1 lds\n" \
"v_exp_f32 v27, v27\n" \
"v_exp_f32 v28, v28\n" \
"v_mfma_f32_16x16x32_bf16 v[84:87], a[220:223], a[156:159], v[196:199]\n" \
"ds_read_b128 a[168:171], v239 offset:2048\n" \
"v_cvt_pk_bf16_f32 v20, v24, v25\n" \
"v_mfma_f32_16x16x32_bf16 v[84:87], a[216:219], a[152:155], v[84:87]\n" \
"ds_read_b128 a[220:223], v233 offset:11264\n" \
"v_cvt_pk_bf16_f32 v21, v26, v27\n" \
"v_mfma_f32_16x16x32_bf16 v[84:87], a[212:215], a[148:151], v[84:87]\n" \
"s_waitcnt lgkmcnt(2)\n" \
"v_exp_f32 v29, v29\n" \
"v_mfma_f32_16x16x32_bf16 v[84:87], a[208:211], a[144:147], v[84:87]\n" \
"buffer_load_dwordx4 v234, s[40:43], s76 offen offset:3072 sc1 lds\n" \
"v_exp_f32 v30, v30\n" \
"v_exp_f32 v31, v31\n" \
"v_mfma_f32_16x16x32_bf16 v[88:91], a[224:227], a[144:147], v[196:199]\n" \
"ds_read_b128 a[216:219], v233 offset:3072\n" \
"v_cvt_pk_bf16_f32 v22, v28, v29\n" \
"v_mfma_f32_16x16x32_bf16 v[88:91], a[228:231], a[148:151], v[88:91]\n" \
"ds_read_b128 a[172:175], v239 offset:3072\n" \
"v_mfma_f32_16x16x32_bf16 v[88:91], a[232:235], a[152:155], v[88:91]\n" \
"s_add_u32 m0, m0, 0x4000\n" \
"ds_read_b128 a[212:215], v233 offset:10240\n" \
"v_xor_b32 v248, 0x2000, v239\n" \
"v_mfma_f32_16x16x32_bf16 v[88:91], a[236:239], a[156:159], v[88:91]\n" \
"buffer_load_dwordx4 v234, s[40:43], s71 offen offset:0 sc1 lds\n" \
"v_exp_f32 v40, v40\n" \
"v_exp_f32 v41, v41\n" \
"v_mfma_f32_16x16x32_bf16 v[104:107], a[236:239], v[152:155], v[196:199]\n" \
"ds_read_b128 a[208:211], v233 offset:2048\n" \
"v_lshlrev_b32 v252, 3, v234\n" \
"v_mfma_f32_16x16x32_bf16 v[104:107], a[232:235], v[144:147], v[104:107]\n" \
"ds_read_b128 a[176:179], v239 offset:4096\n" \
"v_mov_b32 v224, 0\n" \
"v_mfma_f32_16x16x32_bf16 v[104:107], a[228:231], v[136:139], v[104:107]\n" \
"v_mov_b32 v225, 0\n" \
"v_mov_b32 v228, 0\n" \
"v_mfma_f32_16x16x32_bf16 v[104:107], a[224:227], v[128:131], v[104:107]\n" \
"buffer_load_dwordx4 v234, s[40:43], s71 offen offset:1024 sc1 lds\n" \
"v_exp_f32 v42, v42\n" \
"v_cvt_pk_bf16_f32 v23, v30, v31\n" \
"v_mfma_f32_16x16x32_bf16 v[120:123], a[224:227], v[160:163], v[196:199]\n" \
"ds_read_b128 a[180:183], v239 offset:5120\n" \
"v_mov_b32 v229, 0\n" \
"v_mfma_f32_16x16x32_bf16 v[120:123], a[228:231], v[168:171], v[120:123]\n" \
"v_exp_f32 v43, v43\n" \
"v_mfma_f32_16x16x32_bf16 v[120:123], a[232:235], v[176:179], v[120:123]\n" \
"v_exp_f32 v44, v44\n" \
"v_cvt_pk_bf16_f32 v36, v40, v41\n" \
"v_mfma_f32_16x16x32_bf16 v[120:123], a[236:239], v[184:187], v[120:123]\n" \
"buffer_load_dwordx4 v234, s[40:43], s71 offen offset:2048 sc1 lds\n" \
"v_exp_f32 v45, v45\n" \
"v_mfma_f32_16x16x32_bf16 v[72:75], a[236:239], a[140:143], v[216:219]\n" \
"ds_read_b128 a[184:187], v239 offset:6144\n" \
"v_cvt_pk_bf16_f32 v37, v42, v43\n" \
"v_mfma_f32_16x16x32_bf16 v[72:75], a[232:235], a[136:139], v[72:75]\n" \
"ds_read_b128 a[236:239], v233 offset:13312\n" \
"v_mfma_f32_16x16x32_bf16 v[72:75], a[228:231], a[132:135], v[72:75]\n" \
"v_exp_f32 v46, v46\n" \
"v_mfma_f32_16x16x32_bf16 v[72:75], a[224:227], a[128:131], v[72:75]\n" \
"buffer_load_dwordx4 v234, s[40:43], s71 offen offset:3072 sc1 lds\n" \
"v_exp_f32 v47, v47\n" \
"v_mfma_f32_16x16x32_bf16 v[76:79], a[240:243], a[128:131], v[220:223]\n" \
"ds_read_b128 a[232:235], v233 offset:5120\n" \
"v_cvt_pk_bf16_f32 v38, v44, v45\n" \
"v_mfma_f32_16x16x32_bf16 v[76:79], a[244:247], a[132:135], v[76:79]\n" \
"ds_read_b128 a[188:191], v239 offset:7168\n" \
"v_exp_f32 v56, v56\n" \
"v_mfma_f32_16x16x32_bf16 v[76:79], a[248:251], a[136:139], v[76:79]\n" \
"ds_read_b128 a[228:231], v233 offset:12288\n" \
"v_cvt_pk_bf16_f32 v39, v46, v47\n" \
"v_mfma_f32_16x16x32_bf16 v[76:79], a[252:255], a[140:143], v[76:79]\n" \
"v_exp_f32 v57, v57\n" \
"v_exp_f32 v58, v58\n" \
"v_mfma_f32_16x16x32_bf16 v[124:127], a[252:255], v[184:187], v[196:199]\n" \
"ds_read_b128 a[224:227], v233 offset:4096\n" \
"v_cvt_pk_bf16_f32 v52, v56, v57\n" \
"v_mfma_f32_16x16x32_bf16 v[124:127], a[248:251], v[176:179], v[124:127]\n" \
"v_exp_f32 v59, v59\n" \
"v_mfma_f32_16x16x32_bf16 v[124:127], a[244:247], v[168:171], v[124:127]\n" \
"v_exp_f32 v60, v60\n" \
"v_mfma_f32_16x16x32_bf16 v[124:127], a[240:243], v[160:163], v[124:127]\n" \
"v_exp_f32 v61, v61\n" \
"v_exp_f32 v62, v62\n" \
"v_cvt_pk_bf16_f32 v53, v58, v59\n" \
"v_mfma_f32_16x16x32_bf16 v[108:111], a[240:243], v[128:131], v[196:199]\n" \
"v_exp_f32 v63, v63\n" \
"v_mfma_f32_16x16x32_bf16 v[108:111], a[244:247], v[136:139], v[108:111]\n" \
"v_cvt_pk_bf16_f32 v54, v60, v61\n" \
"v_mfma_f32_16x16x32_bf16 v[108:111], a[248:251], v[144:147], v[108:111]\n" \
"v_cvt_pk_bf16_f32 v55, v62, v63\n" \
"v_mfma_f32_16x16x32_bf16 v[108:111], a[252:255], v[152:155], v[108:111]\n" \
"v_exp_f32 v80, v80\n" \
"v_mfma_f32_16x16x32_bf16 v[92:95], a[252:255], a[156:159], v[196:199]\n" \
"v_add_u32 v232, s91, v237\n" \
"v_mfma_f32_16x16x32_bf16 v[92:95], a[248:251], a[152:155], v[92:95]\n" \
"v_exp_f32 v81, v81\n" \
"v_mfma_f32_16x16x32_bf16 v[92:95], a[244:247], a[148:151], v[92:95]\n" \
"s_waitcnt vmcnt(8) lgkmcnt(0)\n" \
"s_barrier\n" \
"v_mfma_f32_16x16x32_bf16 v[92:95], a[240:243], a[144:147], v[92:95]\n" \
"buffer_store_dword v192, v193, s[56:59], s68 offen sc1\n" \
"v_exp_f32 v82, v82\n" \
"v_mfma_f32_16x16x32_bf16 v[160:163], a[192:195], v[16:19], 0\n" \
"s_add_u32 m0, s74, s79\n" \
"ds_read_b128 a[240:243], v232 offset:8448\n" \
"v_xor_b32 v233, 0x4000, v233\n" \
"v_mfma_f32_16x16x32_bf16 v[160:163], a[196:199], v[20:23], v[160:163]\n" \
"buffer_load_dwordx4 v234, s[36:39], s73 offen offset:0 sc1 lds\n" \
"v_mfma_f32_16x16x32_bf16 a[64:67], a[192:195], v[32:35], 0\n" \
"ds_read_b128 a[244:247], v232 offset:10496\n" \
"v_cvt_pk_bf16_f32 v80, v80, v81\n" \
"v_mfma_f32_16x16x32_bf16 a[64:67], a[196:199], v[36:39], a[64:67]\n" \
"ds_read_b128 a[0:3], v248 offset:0\n" \
"v_exp_f32 v83, v83\n" \
"v_mfma_f32_16x16x32_bf16 a[96:99], a[192:195], v[48:51], 0\n" \
"v_exp_f32 v84, v84\n" \
"v_mfma_f32_16x16x32_bf16 a[96:99], a[196:199], v[52:55], a[96:99]\n" \
"s_add_u32 s71, s73, 17408\n" \
"Lpg5565_%=:\n" \
"s_cbranch_execz Lpg6758_%=\n" \
"ds_read_b128 a[192:195], v232 offset:0\n" \
"v_cvt_pk_bf16_f32 v81, v82, v83\n" \
"v_mfma_f32_16x16x32_bf16 a[100:103], a[200:203], v[48:51], 0\n" \
"buffer_load_dwordx4 v234, s[36:39], s73 offen offset:1024 sc1 lds\n" \
"v_mfma_f32_16x16x32_bf16 a[100:103], a[204:207], v[52:55], a[100:103]\n" \
"ds_read_b128 a[196:199], v232 offset:2048\n" \
"v_exp_f32 v85, v85\n" \
"v_mfma_f32_16x16x32_bf16 a[68:71], a[200:203], v[32:35], 0\n" \
"ds_read_b128 a[4:7], v248 offset:1024\n" \
"v_mfma_f32_16x16x32_bf16 a[68:71], a[204:207], v[36:39], a[68:71]\n" \
"v_exp_f32 v86, v86\n" \
"v_mfma_f32_16x16x32_bf16 v[164:167], a[200:203], v[16:19], 0\n" \
"ds_read_b128 a[8:11], v248 offset:2048\n" \
"v_cvt_pk_bf16_f32 v82, v84, v85\n" \
"v_mfma_f32_16x16x32_bf16 v[164:167], a[204:207], v[20:23], v[164:167]\n" \
"buffer_load_dwordx4 v234, s[36:39], s73 offen offset:2048 sc1 lds\n" \
"v_exp_f32 v87, v87\n" \
"v_mfma_f32_16x16x32_bf16 v[168:171], a[208:211], v[16:19], 0\n" \
"ds_read_b128 a[200:203], v232 offset:4096\n" \
"v_mfma_f32_16x16x32_bf16 v[168:171], a[212:215], v[20:23], v[168:171]\n" \
"ds_read_b128 a[204:207], v232 offset:6144\n" \
"v_exp_f32 v96, v96\n" \
"v_mfma_f32_16x16x32_bf16 a[72:75], a[208:211], v[32:35], 0\n" \
"ds_read_b128 a[12:15], v248 offset:3072\n" \
"v_cvt_pk_bf16_f32 v83, v86, v87\n" \
"v_mfma_f32_16x16x32_bf16 a[72:75], a[212:215], v[36:39], a[72:75]\n" \
"v_exp_f32 v97, v97\n" \
"v_mfma_f32_16x16x32_bf16 a[104:107], a[208:211], v[48:51], 0\n" \
"buffer_load_dwordx4 v234, s[36:39], s73 offen offset:3072 sc1 lds\n" \
"v_mfma_f32_16x16x32_bf16 a[104:107], a[212:215], v[52:55], a[104:107]\n" \
"ds_read_b128 a[208:211], v232 offset:256\n" \
"v_exp_f32 v98, v98\n" \
"v_smfmac_f32_16x16x64_bf16 a[52:55], v[244:247], v[16:23], v243\n" \
"ds_read_b128 a[212:215], v232 offset:2304\n" \
"v_cvt_pk_bf16_f32 v96, v96, v97\n" \
"v_mfma_f32_16x16x32_bf16 a[108:111], a[216:219], v[48:51], 0\n" \
"ds_read_b128 a[16:19], v248 offset:4096\n" \
"v_mfma_f32_16x16x32_bf16 a[108:111], a[220:223], v[52:55], a[108:111]\n" \
"s_add_u32 m0, m0, 0x4000\n" \
"v_exp_f32 v99, v99\n" \
"v_mfma_f32_16x16x32_bf16 a[76:79], a[216:219], v[32:35], 0\n" \
"buffer_load_dwordx4 v234, s[36:39], s71 offen offset:0 sc1 lds\n" \
"v_mfma_f32_16x16x32_bf16 a[76:79], a[220:223], v[36:39], a[76:79]\n" \
"ds_read_b128 a[20:23], v248 offset:5120\n" \
"v_exp_f32 v100, v100\n" \
"v_mfma_f32_16x16x32_bf16 v[172:175], a[216:219], v[16:19], 0\n" \
"v_exp_f32 v101, v101\n" \
"v_mfma_f32_16x16x32_bf16 v[172:175], a[220:223], v[20:23], v[172:175]\n" \
"ds_read_b128 a[216:219], v232 offset:4352\n" \
"v_cvt_pk_bf16_f32 v97, v98, v99\n" \
"v_mfma_f32_32x32x16_bf16 v[128:143], a[48:51], a[48:51], 0\n" \
"ds_read_b128 a[220:223], v232 offset:6400\n" \
"v_exp_f32 v102, v102\n" \
"v_mfma_f32_16x16x32_bf16 v[176:179], a[224:227], v[16:19], 0\n" \
"buffer_load_dwordx4 v234, s[36:39], s71 offen offset:1024 sc1 lds\n" \
"v_mfma_f32_16x16x32_bf16 v[176:179], a[228:231], v[20:23], v[176:179]\n" \
"ds_read_b128 a[24:27], v248 offset:6144\n" \
"v_cvt_pk_bf16_f32 v98, v100, v101\n" \
"v_mfma_f32_16x16x32_bf16 a[80:83], a[224:227], v[32:35], 0\n" \
"v_exp_f32 v103, v103\n" \
"v_mfma_f32_16x16x32_bf16 a[80:83], a[228:231], v[36:39], a[80:83]\n" \
"ds_read_b128 a[28:31], v248 offset:7168\n" \
"v_exp_f32 v112, v112\n" \
"v_mfma_f32_16x16x32_bf16 a[112:115], a[224:227], v[48:51], 0\n" \
"v_exp_f32 v113, v113\n" \
"v_mfma_f32_16x16x32_bf16 a[112:115], a[228:231], v[52:55], a[112:115]\n" \
"buffer_load_dwordx4 v234, s[36:39], s71 offen offset:2048 sc1 lds\n" \
"v_exp_f32 v114, v114\n" \
"v_smfmac_f32_16x16x64_bf16 v[224:227], v[244:247], v[32:39], v243\n" \
"ds_read_b128 a[224:227], v232 offset:8192\n" \
"v_cvt_pk_bf16_f32 v99, v102, v103\n" \
"v_mfma_f32_16x16x32_bf16 a[116:119], a[232:235], v[48:51], 0\n" \
"ds_read_b128 a[228:231], v232 offset:10240\n" \
"v_cvt_pk_bf16_f32 v112, v112, v113\n" \
"v_mfma_f32_16x16x32_bf16 a[116:119], a[236:239], v[52:55], a[116:119]\n" \
"v_exp_f32 v115, v115\n" \
"v_mfma_f32_16x16x32_bf16 a[84:87], a[232:235], v[32:35], 0\n" \
"v_exp_f32 v116, v116\n" \
"v_mfma_f32_16x16x32_bf16 a[84:87], a[236:239], v[36:39], a[84:87]\n" \
"buffer_load_dwordx4 v234, s[36:39], s71 offen offset:3072 sc1 lds\n" \
"v_exp_f32 v117, v117\n" \
"v_mfma_f32_16x16x32_bf16 v[180:183], a[232:235], v[16:19], 0\n" \
"v_exp_f32 v118, v118\n" \
"v_mfma_f32_16x16x32_bf16 v[180:183], a[236:239], v[20:23], v[180:183]\n" \
"s_mov_b32 s76, s73\n" \
"s_add_u32 s73, s73, 34816\n" \
"ds_read_b128 a[232:235], v232 offset:12288\n" \
"v_mfma_f32_32x32x16_bf16 v[144:159], a[48:51], a[48:51], 0\n" \
"s_cmp_eq_u32 s73, s83\n" \
"s_cselect_b32 s70, 34816, 0\n" \
"ds_read_b128 a[236:239], v232 offset:14336\n" \
"v_cvt_pk_bf16_f32 v113, v114, v115\n" \
"v_mfma_f32_16x16x32_bf16 v[184:187], a[32:35], v[16:19], 0\n" \
"s_add_u32 s73, s73, s70\n" \
"s_xor_b32 s74, s74, 0x8000\n" \
"ds_read_b128 a[248:251], v232 offset:12544\n" \
"v_mfma_f32_16x16x32_bf16 v[184:187], a[36:39], v[20:23], v[184:187]\n" \
"s_xor_b32 s77, s77, 0x8000\n" \
"ds_read_b128 a[252:255], v232 offset:14592\n" \
"v_cvt_pk_bf16_f32 v114, v116, v117\n" \
"v_mfma_f32_16x16x32_bf16 a[88:91], a[32:35], v[32:35], 0\n" \
"v_exp_f32 v119, v119\n" \
"v_mfma_f32_16x16x32_bf16 a[88:91], a[36:39], v[36:39], a[88:91]\n" \
"v_exp_f32 v64, v64\n" \
"v_mfma_f32_16x16x32_bf16 a[120:123], a[32:35], v[48:51], 0\n" \
"v_exp_f32 v65, v65\n" \
"v_mfma_f32_16x16x32_bf16 a[120:123], a[36:39], v[52:55], a[120:123]\n" \
"v_exp_f32 v66, v66\n" \
"v_cvt_pk_bf16_f32 v115, v118, v119\n" \
"v_smfmac_f32_16x16x64_bf16 v[228:231], v[244:247], v[48:55], v243\n" \
"v_cvt_pk_bf16_f32 v64, v64, v65\n" \
"v_mfma_f32_16x16x32_bf16 a[124:127], a[40:43], v[48:51], 0\n" \
"v_exp_f32 v67, v67\n" \
"v_mfma_f32_16x16x32_bf16 a[124:127], a[44:47], v[52:55], a[124:127]\n" \
"v_exp_f32 v68, v68\n" \
"v_exp_f32 v69, v69\n" \
"v_mfma_f32_16x16x32_bf16 a[92:95], a[40:43], v[32:35], 0\n" \
"v_exp_f32 v70, v70\n" \
"v_mfma_f32_16x16x32_bf16 a[92:95], a[44:47], v[36:39], a[92:95]\n" \
"v_cvt_pk_bf16_f32 v65, v66, v67\n" \
"v_cvt_pk_bf16_f32 v66, v68, v69\n" \
"v_mfma_f32_16x16x32_bf16 v[188:191], a[40:43], v[16:19], 0\n" \
"s_cmp_eq_u32 s63, 0\n" \
"v_exp_f32 v71, v71\n" \
"v_mfma_f32_16x16x32_bf16 v[188:191], a[44:47], v[20:23], v[188:191]\n" \
"s_waitcnt lgkmcnt(0)\n" \
"v_cvt_pk_bf16_f32 v67, v70, v71\n" \
"s_cbranch_scc1 Lloop2_%=\n" \
"s_nop 0\n" \
"s_nop 0\n" \
"s_nop 0\n" \
"s_nop 0\n" \
"s_nop 0\n" \
"s_nop 0\n" \
"Lloop_%=:\n" \
"v_mfma_f32_16x16x32_bf16 v[0:3], a[192:195], a[128:131], v[196:199]\n" \
"v_exp_f32 v72, v72\n" \
"v_mfma_f32_16x16x32_bf16 v[0:3], a[196:199], a[132:135], v[0:3]\n" \
"v_mfma_f32_16x16x32_bf16 v[0:3], a[200:203], a[136:139], v[0:3]\n" \
"v_exp_f32 v73, v73\n" \
"v_mfma_f32_16x16x32_bf16 v[0:3], a[204:207], a[140:143], v[0:3]\n" \
"ds_read_b128 a[32:35], v233 offset:6144\n" \
"v_cvt_pk_bf16_f32 v68, v72, v73\n" \
"v_mfma_f32_16x16x32_bf16 v[16:19], a[204:207], a[156:159], v[196:199]\n" \
"v_exp_f32 v74, v74\n" \
"v_mfma_f32_16x16x32_bf16 v[16:19], a[200:203], a[152:155], v[16:19]\n" \
"global_load_dword v253, v252, s[44:45]\n" \
"v_mfma_f32_16x16x32_bf16 v[16:19], a[196:199], a[148:151], v[16:19]\n" \
"s_sub_u32 s46, s46, 1\n" \
"v_exp_f32 v75, v75\n" \
"v_mfma_f32_16x16x32_bf16 v[16:19], a[192:195], a[144:147], v[16:19]\n" \
"ds_read_b128 a[36:39], v233 offset:14336\n" \
"v_cvt_pk_bf16_f32 v69, v74, v75\n" \
"v_mfma_f32_16x16x32_bf16 v[32:35], a[192:195], a[160:163], v[196:199]\n" \
"v_exp_f32 v76, v76\n" \
"v_mfma_f32_16x16x32_bf16 v[32:35], a[196:199], a[164:167], v[32:35]\n" \
"s_cmp_gt_i32 s46, 0\n" \
"s_cselect_b32 s47, 0x100000, 0\n" \
"v_mfma_f32_16x16x32_bf16 v[32:35], a[200:203], a[168:171], v[32:35]\n" \
"v_exp_f32 v77, v77\n" \
"v_mfma_f32_16x16x32_bf16 v[32:35], a[204:207], a[172:175], v[32:35]\n" \
"s_add_u32 s44, s44, s47\n" \
"s_addc_u32 s45, s45, 0\n" \
"ds_read_b128 a[40:43], v233 offset:7168\n" \
"v_cvt_pk_bf16_f32 v70, v76, v77\n" \
"v_mfma_f32_16x16x32_bf16 v[48:51], a[204:207], a[188:191], v[196:199]\n" \
"v_exp_f32 v78, v78\n" \
"v_mfma_f32_16x16x32_bf16 v[48:51], a[200:203], a[184:187], v[48:51]\n" \
"v_xor_b32 v232, 0x4000, v232\n" \
"v_mfma_f32_16x16x32_bf16 v[48:51], a[196:199], a[180:183], v[48:51]\n" \
"v_exp_f32 v79, v79\n" \
"v_mfma_f32_16x16x32_bf16 v[48:51], a[192:195], a[176:179], v[48:51]\n" \
"ds_read_b128 a[204:207], v233 offset:9216\n" \
"v_cvt_pk_bf16_f32 v71, v78, v79\n" \
"v_mfma_f32_16x16x32_bf16 v[52:55], a[208:211], a[176:179], v[196:199]\n" \
"v_exp_f32 v88, v88\n" \
"v_mfma_f32_16x16x32_bf16 v[52:55], a[212:215], a[180:183], v[52:55]\n" \
"v_mfma_f32_16x16x32_bf16 v[52:55], a[216:219], a[184:187], v[52:55]\n" \
"v_exp_f32 v89, v89\n" \
"v_mfma_f32_16x16x32_bf16 v[52:55], a[220:223], a[188:191], v[52:55]\n" \
"ds_read_b128 a[200:203], v233 offset:1024\n" \
"v_cvt_pk_bf16_f32 v84, v88, v89\n" \
"v_mfma_f32_16x16x32_bf16 v[36:39], a[220:223], a[172:175], v[196:199]\n" \
"v_exp_f32 v90, v90\n" \
"v_mfma_f32_16x16x32_bf16 v[36:39], a[216:219], a[168:171], v[36:39]\n" \
"v_mfma_f32_16x16x32_bf16 v[36:39], a[212:215], a[164:167], v[36:39]\n" \
"v_exp_f32 v91, v91\n" \
"v_mfma_f32_16x16x32_bf16 v[36:39], a[208:211], a[160:163], v[36:39]\n" \
"ds_read_b128 a[196:199], v233 offset:8192\n" \
"v_cvt_pk_bf16_f32 v85, v90, v91\n" \
"v_mfma_f32_16x16x32_bf16 v[20:23], a[208:211], a[144:147], v[196:199]\n" \
"v_exp_f32 v92, v92\n" \
"v_mfma_f32_16x16x32_bf16 v[20:23], a[212:215], a[148:151], v[20:23]\n" \
"v_mfma_f32_16x16x32_bf16 v[20:23], a[216:219], a[152:155], v[20:23]\n" \
"v_exp_f32 v93, v93\n" \
"v_mfma_f32_16x16x32_bf16 v[20:23], a[220:223], a[156:159], v[20:23]\n" \
"ds_read_b128 a[192:195], v233 offset:0\n" \
"v_cvt_pk_bf16_f32 v86, v92, v93\n" \
"v_mfma_f32_16x16x32_bf16 v[4:7], a[220:223], a[140:143], v[196:199]\n" \
"v_exp_f32 v94, v94\n" \
"v_mfma_f32_16x16x32_bf16 v[4:7], a[216:219], a[136:139], v[4:7]\n" \
"v_mfma_f32_16x16x32_bf16 v[4:7], a[212:215], a[132:135], v[4:7]\n" \
"s_waitcnt lgkmcnt(7)\n" \
"v_exp_f32 v95, v95\n" \
"v_mfma_f32_16x16x32_bf16 v[4:7], a[208:211], a[128:131], v[4:7]\n" \
"ds_read_b128 a[220:223], v233 offset:11264\n" \
"v_cvt_pk_bf16_f32 v87, v94, v95\n" \
"v_mfma_f32_16x16x32_bf16 v[8:11], a[224:227], a[128:131], v[196:199]\n" \
"v_exp_f32 v104, v104\n" \
"v_mfma_f32_16x16x32_bf16 v[8:11], a[228:231], a[132:135], v[8:11]\n" \
"v_mfma_f32_16x16x32_bf16 v[8:11], a[232:235], a[136:139], v[8:11]\n" \
"v_exp_f32 v105, v105\n" \
"v_mfma_f32_16x16x32_bf16 v[8:11], a[236:239], a[140:143], v[8:11]\n" \
"ds_read_b128 a[216:219], v233 offset:3072\n" \
"v_cvt_pk_bf16_f32 v100, v104, v105\n" \
"v_mfma_f32_16x16x32_bf16 v[24:27], a[236:239], a[156:159], v[196:199]\n" \
"v_exp_f32 v106, v106\n" \
"v_mfma_f32_16x16x32_bf16 v[24:27], a[232:235], a[152:155], v[24:27]\n" \
"v_mfma_f32_16x16x32_bf16 v[24:27], a[228:231], a[148:151], v[24:27]\n" \
"v_exp_f32 v107, v107\n" \
"v_mfma_f32_16x16x32_bf16 v[24:27], a[224:227], a[144:147], v[24:27]\n" \
"ds_read_b128 a[212:215], v233 offset:10240\n" \
"v_cvt_pk_bf16_f32 v101, v106, v107\n" \
"v_mfma_f32_16x16x32_bf16 v[40:43], a[224:227], a[160:163], v[196:199]\n" \
"v_exp_f32 v108, v108\n" \
"v_mfma_f32_16x16x32_bf16 v[40:43], a[228:231], a[164:167], v[40:43]\n" \
"v_mfma_f32_16x16x32_bf16 v[40:43], a[232:235], a[168:171], v[40:43]\n" \
"v_exp_f32 v109, v109\n" \
"v_mfma_f32_16x16x32_bf16 v[40:43], a[236:239], a[172:175], v[40:43]\n" \
"ds_read_b128 a[208:211], v233 offset:2048\n" \
"v_cvt_pk_bf16_f32 v102, v108, v109\n" \
"v_mfma_f32_16x16x32_bf16 v[56:59], a[236:239], a[188:191], v[196:199]\n" \
"v_exp_f32 v110, v110\n" \
"v_mfma_f32_16x16x32_bf16 v[56:59], a[232:235], a[184:187], v[56:59]\n" \
"v_mfma_f32_16x16x32_bf16 v[56:59], a[228:231], a[180:183], v[56:59]\n" \
"v_exp_f32 v111, v111\n" \
"v_mfma_f32_16x16x32_bf16 v[56:59], a[224:227], a[176:179], v[56:59]\n" \
"ds_read_b128 a[236:239], v233 offset:13312\n" \
"v_cvt_pk_bf16_f32 v103, v110, v111\n" \
"v_mfma_f32_16x16x32_bf16 v[60:63], a[240:243], a[176:179], v[196:199]\n" \
"v_exp_f32 v120, v120\n" \
"v_mfma_f32_16x16x32_bf16 v[60:63], a[244:247], a[180:183], v[60:63]\n" \
"v_mfma_f32_16x16x32_bf16 v[60:63], a[248:251], a[184:187], v[60:63]\n" \
"v_exp_f32 v121, v121\n" \
"v_mfma_f32_16x16x32_bf16 v[60:63], a[252:255], a[188:191], v[60:63]\n" \
"ds_read_b128 a[232:235], v233 offset:5120\n" \
"v_cvt_pk_bf16_f32 v116, v120, v121\n" \
"v_mfma_f32_16x16x32_bf16 v[44:47], a[252:255], a[172:175], v[196:199]\n" \
"v_exp_f32 v122, v122\n" \
"v_mfma_f32_16x16x32_bf16 v[44:47], a[248:251], a[168:171], v[44:47]\n" \
"v_mfma_f32_16x16x32_bf16 v[44:47], a[244:247], a[164:167], v[44:47]\n" \
"v_exp_f32 v123, v123\n" \
"v_mfma_f32_16x16x32_bf16 v[44:47], a[240:243], a[160:163], v[44:47]\n" \
"ds_read_b128 a[228:231], v233 offset:12288\n" \
"v_cvt_pk_bf16_f32 v117, v122, v123\n" \
"v_mfma_f32_16x16x32_bf16 v[28:31], a[240:243], a[144:147], v[196:199]\n" \
"v_exp_f32 v124, v124\n" \
"v_mfma_f32_16x16x32_bf16 v[28:31], a[244:247], a[148:151], v[28:31]\n" \
"v_mfma_f32_16x16x32_bf16 v[28:31], a[248:251], a[152:155], v[28:31]\n" \
"v_exp_f32 v125, v125\n" \
"v_mfma_f32_16x16x32_bf16 v[28:31], a[252:255], a[156:159], v[28:31]\n" \
"ds_read_b128 a[224:227], v233 offset:4096\n" \
"v_cvt_pk_bf16_f32 v118, v124, v125\n" \
"v_mfma_f32_16x16x32_bf16 v[12:15], a[252:255], a[140:143], v[196:199]\n" \
"v_exp_f32 v126, v126\n" \
"v_mfma_f32_16x16x32_bf16 v[12:15], a[248:251], a[136:139], v[12:15]\n" \
"v_mfma_f32_16x16x32_bf16 v[12:15], a[244:247], a[132:135], v[12:15]\n" \
"s_waitcnt lgkmcnt(2)\n" \
"v_exp_f32 v127, v127\n" \
"v_mfma_f32_16x16x32_bf16 v[12:15], a[240:243], a[128:131], v[12:15]\n" \
"ds_read_b128 a[44:47], v233 offset:15360\n" \
"v_cvt_pk_bf16_f32 v119, v126, v127\n" \
"v_mfma_f32_16x16x32_bf16 v[128:131], a[192:195], v[64:67], v[128:131]\n" \
"v_exp_f32 v0, v0\n" \
"v_mfma_f32_16x16x32_bf16 v[128:131], a[196:199], v[68:71], v[128:131]\n" \
"ds_read_b128 a[244:247], v232 offset:10496\n" \
"v_mfma_f32_16x16x32_bf16 v[160:163], a[192:195], v[80:83], v[160:163]\n" \
"v_exp_f32 v1, v1\n" \
"v_mfma_f32_16x16x32_bf16 v[160:163], a[196:199], v[84:87], v[160:163]\n" \
"ds_read_b128 a[240:243], v232 offset:8448\n" \
"v_cvt_pk_bf16_f32 v0, v0, v1\n" \
"v_mfma_f32_16x16x32_bf16 a[64:67], a[192:195], v[96:99], a[64:67]\n" \
"v_exp_f32 v2, v2\n" \
"v_mfma_f32_16x16x32_bf16 a[64:67], a[196:199], v[100:103], a[64:67]\n" \
"ds_read_b128 a[248:251], v232 offset:12544\n" \
"v_xor_b32 v233, 0xc000, v233\n" \
"v_mfma_f32_16x16x32_bf16 a[96:99], a[192:195], v[112:115], a[96:99]\n" \
"v_exp_f32 v3, v3\n" \
"v_mfma_f32_16x16x32_bf16 a[96:99], a[196:199], v[116:119], a[96:99]\n" \
"ds_read_b128 a[192:195], v232 offset:0\n" \
"v_cvt_pk_bf16_f32 v1, v2, v3\n" \
"v_mfma_f32_16x16x32_bf16 a[100:103], a[200:203], v[112:115], a[100:103]\n" \
"v_exp_f32 v4, v4\n" \
"v_mfma_f32_16x16x32_bf16 a[100:103], a[204:207], v[116:119], a[100:103]\n" \
"ds_read_b128 a[252:255], v232 offset:14592\n" \
"v_mfma_f32_16x16x32_bf16 a[68:71], a[200:203], v[96:99], a[68:71]\n" \
"v_exp_f32 v5, v5\n" \
"v_mfma_f32_16x16x32_bf16 a[68:71], a[204:207], v[100:103], a[68:71]\n" \
"ds_read_b128 a[196:199], v232 offset:2048\n" \
"v_cvt_pk_bf16_f32 v2, v4, v5\n" \
"v_smfmac_f32_16x16x64_bf16 a[48:51], v[244:247], v[64:71], v243\n" \
"v_mfma_f32_16x16x32_bf16 v[164:167], a[200:203], v[80:83], v[164:167]\n" \
"v_exp_f32 v6, v6\n" \
"v_mfma_f32_16x16x32_bf16 v[164:167], a[204:207], v[84:87], v[164:167]\n" \
"v_mfma_f32_16x16x32_bf16 v[132:135], a[200:203], v[64:67], v[132:135]\n" \
"v_exp_f32 v7, v7\n" \
"v_mfma_f32_16x16x32_bf16 v[132:135], a[204:207], v[68:71], v[132:135]\n" \
"ds_read_b128 a[200:203], v232 offset:4096\n" \
"v_cvt_pk_bf16_f32 v3, v6, v7\n" \
"v_mfma_f32_16x16x32_bf16 v[136:139], a[208:211], v[64:67], v[136:139]\n" \
"v_exp_f32 v16, v16\n" \
"v_mfma_f32_16x16x32_bf16 v[136:139], a[212:215], v[68:71], v[136:139]\n" \
"v_mfma_f32_16x16x32_bf16 v[168:171], a[208:211], v[80:83], v[168:171]\n" \
"v_exp_f32 v17, v17\n" \
"v_mfma_f32_16x16x32_bf16 v[168:171], a[212:215], v[84:87], v[168:171]\n" \
"ds_read_b128 a[204:207], v232 offset:6144\n" \
"v_cvt_pk_bf16_f32 v16, v16, v17\n" \
"v_mfma_f32_16x16x32_bf16 a[72:75], a[208:211], v[96:99], a[72:75]\n" \
"v_exp_f32 v18, v18\n" \
"v_mfma_f32_16x16x32_bf16 a[72:75], a[212:215], v[100:103], a[72:75]\n" \
"v_mfma_f32_16x16x32_bf16 a[104:107], a[208:211], v[112:115], a[104:107]\n" \
"v_exp_f32 v19, v19\n" \
"v_mfma_f32_16x16x32_bf16 a[104:107], a[212:215], v[116:119], a[104:107]\n" \
"ds_read_b128 a[208:211], v232 offset:256\n" \
"v_cvt_pk_bf16_f32 v17, v18, v19\n" \
"v_mfma_f32_16x16x32_bf16 a[108:111], a[216:219], v[112:115], a[108:111]\n" \
"v_exp_f32 v20, v20\n" \
"v_mfma_f32_16x16x32_bf16 a[108:111], a[220:223], v[116:119], a[108:111]\n" \
"v_mfma_f32_16x16x32_bf16 a[76:79], a[216:219], v[96:99], a[76:79]\n" \
"v_exp_f32 v21, v21\n" \
"v_mfma_f32_16x16x32_bf16 a[76:79], a[220:223], v[100:103], a[76:79]\n" \
"ds_read_b128 a[212:215], v232 offset:2304\n" \
"v_cvt_pk_bf16_f32 v18, v20, v21\n" \
"v_smfmac_f32_16x16x64_bf16 a[52:55], v[244:247], v[80:87], v243\n" \
"v_mfma_f32_16x16x32_bf16 v[172:175], a[216:219], v[80:83], v[172:175]\n" \
"v_exp_f32 v22, v22\n" \
"v_mfma_f32_16x16x32_bf16 v[172:175], a[220:223], v[84:87], v[172:175]\n" \
"v_mfma_f32_16x16x32_bf16 v[140:143], a[216:219], v[64:67], v[140:143]\n" \
"s_waitcnt lgkmcnt(10)\n" \
"v_exp_f32 v23, v23\n" \
"v_mfma_f32_16x16x32_bf16 v[140:143], a[220:223], v[68:71], v[140:143]\n" \
"ds_read_b128 a[216:219], v232 offset:4352\n" \
"v_cvt_pk_bf16_f32 v19, v22, v23\n" \
"v_mfma_f32_16x16x32_bf16 v[144:147], a[224:227], v[64:67], v[144:147]\n" \
"v_exp_f32 v32, v32\n" \
"v_mfma_f32_16x16x32_bf16 v[144:147], a[228:231], v[68:71], v[144:147]\n" \
"v_mfma_f32_16x16x32_bf16 v[176:179], a[224:227], v[80:83], v[176:179]\n" \
"v_exp_f32 v33, v33\n" \
"v_mfma_f32_16x16x32_bf16 v[176:179], a[228:231], v[84:87], v[176:179]\n" \
"ds_read_b128 a[220:223], v232 offset:6400\n" \
"v_cvt_pk_bf16_f32 v32, v32, v33\n" \
"v_mfma_f32_16x16x32_bf16 a[80:83], a[224:227], v[96:99], a[80:83]\n" \
"v_exp_f32 v34, v34\n" \
"v_mfma_f32_16x16x32_bf16 a[80:83], a[228:231], v[100:103], a[80:83]\n" \
"v_mfma_f32_16x16x32_bf16 a[112:115], a[224:227], v[112:115], a[112:115]\n" \
"v_exp_f32 v35, v35\n" \
"v_mfma_f32_16x16x32_bf16 a[112:115], a[228:231], v[116:119], a[112:115]\n" \
"ds_read_b128 a[224:227], v232 offset:8192\n" \
"v_cvt_pk_bf16_f32 v33, v34, v35\n" \
"v_mfma_f32_16x16x32_bf16 a[116:119], a[232:235], v[112:115], a[116:119]\n" \
"v_exp_f32 v36, v36\n" \
"v_mfma_f32_16x16x32_bf16 a[116:119], a[236:239], v[116:119], a[116:119]\n" \
"v_mfma_f32_16x16x32_bf16 a[84:87], a[232:235], v[96:99], a[84:87]\n" \
"v_exp_f32 v37, v37\n" \
"v_mfma_f32_16x16x32_bf16 a[84:87], a[236:239], v[100:103], a[84:87]\n" \
"ds_read_b128 a[228:231], v232 offset:10240\n" \
"v_cvt_pk_bf16_f32 v34, v36, v37\n" \
"v_smfmac_f32_16x16x64_bf16 v[224:227], v[244:247], v[96:103], v243\n" \
"v_mfma_f32_16x16x32_bf16 v[180:183], a[232:235], v[80:83], v[180:183]\n" \
"v_exp_f32 v38, v38\n" \
"v_mfma_f32_16x16x32_bf16 v[180:183], a[236:239], v[84:87], v[180:183]\n" \
"v_mfma_f32_16x16x32_bf16 v[148:151], a[232:235], v[64:67], v[148:151]\n" \
"v_exp_f32 v39, v39\n" \
"v_mfma_f32_16x16x32_bf16 v[148:151], a[236:239], v[68:71], v[148:151]\n" \
"ds_read_b128 a[232:235], v232 offset:12288\n" \
"v_cvt_pk_bf16_f32 v35, v38, v39\n" \
"v_mfma_f32_16x16x32_bf16 v[152:155], a[32:35], v[64:67], v[152:155]\n" \
"v_exp_f32 v48, v48\n" \
"v_mfma_f32_16x16x32_bf16 v[152:155], a[36:39], v[68:71], v[152:155]\n" \
"v_mfma_f32_16x16x32_bf16 v[184:187], a[32:35], v[80:83], v[184:187]\n" \
"v_exp_f32 v49, v49\n" \
"v_mfma_f32_16x16x32_bf16 v[184:187], a[36:39], v[84:87], v[184:187]\n" \
"ds_read_b128 a[236:239], v232 offset:14336\n" \
"v_cvt_pk_bf16_f32 v48, v48, v49\n" \
"v_mfma_f32_16x16x32_bf16 a[88:91], a[32:35], v[96:99], a[88:91]\n" \
"v_exp_f32 v50, v50\n" \
"v_mfma_f32_16x16x32_bf16 a[88:91], a[36:39], v[100:103], a[88:91]\n" \
"v_mfma_f32_16x16x32_bf16 a[120:123], a[32:35], v[112:115], a[120:123]\n" \
"v_exp_f32 v51, v51\n" \
"v_mfma_f32_16x16x32_bf16 a[120:123], a[36:39], v[116:119], a[120:123]\n" \
"v_cvt_pk_bf16_f32 v49, v50, v51\n" \
"v_mfma_f32_16x16x32_bf16 a[124:127], a[40:43], v[112:115], a[124:127]\n" \
"v_exp_f32 v52, v52\n" \
"v_mfma_f32_16x16x32_bf16 a[124:127], a[44:47], v[116:119], a[124:127]\n" \
"v_mfma_f32_16x16x32_bf16 a[92:95], a[40:43], v[96:99], a[92:95]\n" \
"v_exp_f32 v53, v53\n" \
"v_mfma_f32_16x16x32_bf16 a[92:95], a[44:47], v[100:103], a[92:95]\n" \
"v_cvt_pk_bf16_f32 v50, v52, v53\n" \
"v_smfmac_f32_16x16x64_bf16 v[228:231], v[244:247], v[112:119], v243\n" \
"v_mfma_f32_16x16x32_bf16 v[188:191], a[40:43], v[80:83], v[188:191]\n" \
"v_exp_f32 v54, v54\n" \
"v_mfma_f32_16x16x32_bf16 v[188:191], a[44:47], v[84:87], v[188:191]\n" \
"v_mfma_f32_16x16x32_bf16 v[156:159], a[40:43], v[64:67], v[156:159]\n" \
"s_waitcnt vmcnt(1) lgkmcnt(0)\n" \
"v_exp_f32 v55, v55\n" \
"v_mfma_f32_16x16x32_bf16 v[156:159], a[44:47], v[68:71], v[156:159]\n" \
"s_barrier\n" \
"v_cvt_pk_bf16_f32 v51, v54, v55\n" \
"v_mfma_f32_16x16x32_bf16 v[64:67], a[192:195], a[128:131], v[196:199]\n" \
"v_exp_f32 v8, v8\n" \
"v_mfma_f32_16x16x32_bf16 v[64:67], a[196:199], a[132:135], v[64:67]\n" \
"v_cmp_eq_u32 vcc, s60, v239\n" \
"v_mfma_f32_16x16x32_bf16 v[64:67], a[200:203], a[136:139], v[64:67]\n" \
"v_exp_f32 v9, v9\n" \
"v_mfma_f32_16x16x32_bf16 v[64:67], a[204:207], a[140:143], v[64:67]\n" \
"s_andn2_b32 s70, s69, vcc_lo\n" \
"s_cbranch_scc1 Lpl2_%=\n" \
"Lpr2_%=:\n" \
"ds_read_b128 a[32:35], v233 offset:6144\n" \
"v_cvt_pk_bf16_f32 v4, v8, v9\n" \
"v_mfma_f32_16x16x32_bf16 v[80:83], a[204:207], a[156:159], v[196:199]\n" \
"s_add_u32 m0, s77, s80\n" \
"v_exp_f32 v10, v10\n" \
"v_mfma_f32_16x16x32_bf16 v[80:83], a[200:203], a[152:155], v[80:83]\n" \
"buffer_load_dwordx4 v234, s[40:43], s76 offen offset:0 sc1 lds\n" \
"v_mfma_f32_16x16x32_bf16 v[80:83], a[196:199], a[148:151], v[80:83]\n" \
"s_sub_u32 s89, s89, 1\n" \
"v_exp_f32 v11, v11\n" \
"v_mfma_f32_16x16x32_bf16 v[80:83], a[192:195], a[144:147], v[80:83]\n" \
"s_cmp_eq_u32 s89, 0\n" \
"s_cselect_b32 s70, s78, 0xc000\n" \
"ds_read_b128 a[36:39], v233 offset:14336\n" \
"v_cvt_pk_bf16_f32 v5, v10, v11\n" \
"v_mfma_f32_16x16x32_bf16 v[96:99], a[192:195], a[160:163], v[196:199]\n" \
"s_add_u32 s71, s76, 17408\n" \
"v_exp_f32 v12, v12\n" \
"v_mfma_f32_16x16x32_bf16 v[96:99], a[196:199], a[164:167], v[96:99]\n" \
"buffer_load_dwordx4 v234, s[40:43], s76 offen offset:1024 sc1 lds\n" \
"v_mfma_f32_16x16x32_bf16 v[96:99], a[200:203], a[168:171], v[96:99]\n" \
"v_exp_f32 v13, v13\n" \
"v_mfma_f32_16x16x32_bf16 v[96:99], a[204:207], a[172:175], v[96:99]\n" \
"ds_read_b128 a[40:43], v233 offset:7168\n" \
"v_cvt_pk_bf16_f32 v6, v12, v13\n" \
"v_mfma_f32_16x16x32_bf16 v[112:115], a[204:207], a[188:191], v[196:199]\n" \
"v_exp_f32 v14, v14\n" \
"v_mfma_f32_16x16x32_bf16 v[112:115], a[200:203], a[184:187], v[112:115]\n" \
"v_xor_b32 v232, s70, v232\n" \
"v_mfma_f32_16x16x32_bf16 v[112:115], a[196:199], a[180:183], v[112:115]\n" \
"v_exp_f32 v15, v15\n" \
"v_mfma_f32_16x16x32_bf16 v[112:115], a[192:195], a[176:179], v[112:115]\n" \
"ds_read_b128 a[204:207], v233 offset:9216\n" \
"v_cvt_pk_bf16_f32 v7, v14, v15\n" \
"v_mfma_f32_16x16x32_bf16 v[116:119], a[208:211], a[176:179], v[196:199]\n" \
"v_exp_f32 v24, v24\n" \
"v_mfma_f32_16x16x32_bf16 v[116:119], a[212:215], a[180:183], v[116:119]\n" \
"buffer_load_dwordx4 v234, s[40:43], s76 offen offset:2048 sc1 lds\n" \
"v_mfma_f32_16x16x32_bf16 v[116:119], a[216:219], a[184:187], v[116:119]\n" \
"v_exp_f32 v25, v25\n" \
"v_mfma_f32_16x16x32_bf16 v[116:119], a[220:223], a[188:191], v[116:119]\n" \
"ds_read_b128 a[200:203], v233 offset:1024\n" \
"v_cvt_pk_bf16_f32 v20, v24, v25\n" \
"v_mfma_f32_16x16x32_bf16 v[100:103], a[220:223], a[172:175], v[196:199]\n" \
"v_exp_f32 v26, v26\n" \
"v_mfma_f32_16x16x32_bf16 v[100:103], a[216:219], a[168:171], v[100:103]\n" \
"buffer_load_dwordx4 v234, s[40:43], s76 offen offset:3072 sc1 lds\n" \
"v_mfma_f32_16x16x32_bf16 v[100:103], a[212:215], a[164:167], v[100:103]\n" \
"v_exp_f32 v27, v27\n" \
"v_mfma_f32_16x16x32_bf16 v[100:103], a[208:211], a[160:163], v[100:103]\n" \
"ds_read_b128 a[196:199], v233 offset:8192\n" \
"v_cvt_pk_bf16_f32 v21, v26, v27\n" \
"v_mfma_f32_16x16x32_bf16 v[84:87], a[208:211], a[144:147], v[196:199]\n" \
"v_exp_f32 v28, v28\n" \
"v_mfma_f32_16x16x32_bf16 v[84:87], a[212:215], a[148:151], v[84:87]\n" \
"v_mfma_f32_16x16x32_bf16 v[84:87], a[216:219], a[152:155], v[84:87]\n" \
"v_exp_f32 v29, v29\n" \
"v_mfma_f32_16x16x32_bf16 v[84:87], a[220:223], a[156:159], v[84:87]\n" \
"ds_read_b128 a[192:195], v233 offset:0\n" \
"v_cvt_pk_bf16_f32 v22, v28, v29\n" \
"v_mfma_f32_16x16x32_bf16 v[68:71], a[220:223], a[140:143], v[196:199]\n" \
"s_add_u32 m0, m0, 0x4000\n" \
"v_exp_f32 v30, v30\n" \
"v_mfma_f32_16x16x32_bf16 v[68:71], a[216:219], a[136:139], v[68:71]\n" \
"buffer_load_dwordx4 v234, s[40:43], s71 offen offset:0 sc1 lds\n" \
"v_mfma_f32_16x16x32_bf16 v[68:71], a[212:215], a[132:135], v[68:71]\n" \
"v_exp_f32 v31, v31\n" \
"v_mfma_f32_16x16x32_bf16 v[68:71], a[208:211], a[128:131], v[68:71]\n" \
"ds_read_b128 a[220:223], v233 offset:11264\n" \
"v_cvt_pk_bf16_f32 v23, v30, v31\n" \
"v_mfma_f32_16x16x32_bf16 v[72:75], a[224:227], a[128:131], v[196:199]\n" \
"v_exp_f32 v40, v40\n" \
"v_mfma_f32_16x16x32_bf16 v[72:75], a[228:231], a[132:135], v[72:75]\n" \
"buffer_load_dwordx4 v234, s[40:43], s71 offen offset:1024 sc1 lds\n" \
"v_mfma_f32_16x16x32_bf16 v[72:75], a[232:235], a[136:139], v[72:75]\n" \
"v_exp_f32 v41, v41\n" \
"v_mfma_f32_16x16x32_bf16 v[72:75], a[236:239], a[140:143], v[72:75]\n" \
"ds_read_b128 a[216:219], v233 offset:3072\n" \
"v_cvt_pk_bf16_f32 v36, v40, v41\n" \
"v_mfma_f32_16x16x32_bf16 v[88:91], a[236:239], a[156:159], v[196:199]\n" \
"v_exp_f32 v42, v42\n" \
"v_mfma_f32_16x16x32_bf16 v[88:91], a[232:235], a[152:155], v[88:91]\n" \
"v_mfma_f32_16x16x32_bf16 v[88:91], a[228:231], a[148:151], v[88:91]\n" \
"v_exp_f32 v43, v43\n" \
"v_mfma_f32_16x16x32_bf16 v[88:91], a[224:227], a[144:147], v[88:91]\n" \
"ds_read_b128 a[212:215], v233 offset:10240\n" \
"v_cvt_pk_bf16_f32 v37, v42, v43\n" \
"v_mfma_f32_16x16x32_bf16 v[104:107], a[224:227], a[160:163], v[196:199]\n" \
"v_exp_f32 v44, v44\n" \
"v_mfma_f32_16x16x32_bf16 v[104:107], a[228:231], a[164:167], v[104:107]\n" \
"buffer_load_dwordx4 v234, s[40:43], s71 offen offset:2048 sc1 lds\n" \
"v_mfma_f32_16x16x32_bf16 v[104:107], a[232:235], a[168:171], v[104:107]\n" \
"v_exp_f32 v45, v45\n" \
"v_mfma_f32_16x16x32_bf16 v[104:107], a[236:239], a[172:175], v[104:107]\n" \
"ds_read_b128 a[208:211], v233 offset:2048\n" \
"v_cvt_pk_bf16_f32 v38, v44, v45\n" \
"v_mfma_f32_16x16x32_bf16 v[120:123], a[236:239], a[188:191], v[196:199]\n" \
"v_exp_f32 v46, v46\n" \
"v_mfma_f32_16x16x32_bf16 v[120:123], a[232:235], a[184:187], v[120:123]\n" \
"buffer_load_dwordx4 v234, s[40:43], s71 offen offset:3072 sc1 lds\n" \
"v_mfma_f32_16x16x32_bf16 v[120:123], a[228:231], a[180:183], v[120:123]\n" \
"v_exp_f32 v47, v47\n" \
"v_mfma_f32_16x16x32_bf16 v[120:123], a[224:227], a[176:179], v[120:123]\n" \
"ds_read_b128 a[236:239], v233 offset:13312\n" \
"v_cvt_pk_bf16_f32 v39, v46, v47\n" \
"v_mfma_f32_16x16x32_bf16 v[124:127], a[240:243], a[176:179], v[196:199]\n" \
"v_exp_f32 v56, v56\n" \
"v_mfma_f32_16x16x32_bf16 v[124:127], a[244:247], a[180:183], v[124:127]\n" \
"v_mfma_f32_16x16x32_bf16 v[124:127], a[248:251], a[184:187], v[124:127]\n" \
"v_exp_f32 v57, v57\n" \
"v_mfma_f32_16x16x32_bf16 v[124:127], a[252:255], a[188:191], v[124:127]\n" \
"ds_read_b128 a[232:235], v233 offset:5120\n" \
"v_cvt_pk_bf16_f32 v52, v56, v57\n" \
"v_mfma_f32_16x16x32_bf16 v[108:111], a[252:255], a[172:175], v[196:199]\n" \
"v_exp_f32 v58, v58\n" \
"v_mfma_f32_16x16x32_bf16 v[108:111], a[248:251], a[168:171], v[108:111]\n" \
"v_mfma_f32_16x16x32_bf16 v[108:111], a[244:247], a[164:167], v[108:111]\n" \
"v_exp_f32 v59, v59\n" \
"v_mfma_f32_16x16x32_bf16 v[108:111], a[240:243], a[160:163], v[108:111]\n" \
"ds_read_b128 a[228:231], v233 offset:12288\n" \
"v_cvt_pk_bf16_f32 v53, v58, v59\n" \
"v_mfma_f32_16x16x32_bf16 v[92:95], a[240:243], a[144:147], v[196:199]\n" \
"v_exp_f32 v60, v60\n" \
"v_mfma_f32_16x16x32_bf16 v[92:95], a[244:247], a[148:151], v[92:95]\n" \
"v_mfma_f32_16x16x32_bf16 v[92:95], a[248:251], a[152:155], v[92:95]\n" \
"v_exp_f32 v61, v61\n" \
"v_mfma_f32_16x16x32_bf16 v[92:95], a[252:255], a[156:159], v[92:95]\n" \
"ds_read_b128 a[224:227], v233 offset:4096\n" \
"v_cvt_pk_bf16_f32 v54, v60, v61\n" \
"v_mfma_f32_16x16x32_bf16 v[76:79], a[252:255], a[140:143], v[196:199]\n" \
"v_exp_f32 v62, v62\n" \
"v_mfma_f32_16x16x32_bf16 v[76:79], a[248:251], a[136:139], v[76:79]\n" \
"v_mfma_f32_16x16x32_bf16 v[76:79], a[244:247], a[132:135], v[76:79]\n" \
"s_waitcnt lgkmcnt(2)\n" \
"v_exp_f32 v63, v63\n" \
"v_mfma_f32_16x16x32_bf16 v[76:79], a[240:243], a[128:131], v[76:79]\n" \
"ds_read_b128 a[44:47], v233 offset:15360\n" \
"v_cvt_pk_bf16_f32 v55, v62, v63\n" \
"v_mfma_f32_16x16x32_bf16 v[128:131], a[192:195], v[0:3], v[128:131]\n" \
"s_add_u32 m0, s74, s79\n" \
"v_exp_f32 v64, v64\n" \
"v_mfma_f32_16x16x32_bf16 v[128:131], a[196:199], v[4:7], v[128:131]\n" \
"buffer_load_dwordx4 v234, s[36:39], s73 offen offset:0 sc1 lds\n" \
"v_mfma_f32_16x16x32_bf16 v[160:163], a[192:195], v[16:19], v[160:163]\n" \
"v_exp_f32 v65, v65\n" \
"v_mfma_f32_16x16x32_bf16 v[160:163], a[196:199], v[20:23], v[160:163]\n" \
"ds_read_b128 a[240:243], v232 offset:8448\n" \
"v_cvt_pk_bf16_f32 v64, v64, v65\n" \
"v_mfma_f32_16x16x32_bf16 a[64:67], a[192:195], v[32:35], a[64:67]\n" \
"s_add_u32 s71, s73, 17408\n" \
"v_exp_f32 v66, v66\n" \
"v_mfma_f32_16x16x32_bf16 a[64:67], a[196:199], v[36:39], a[64:67]\n" \
"buffer_load_dwordx4 v234, s[36:39], s73 offen offset:1024 sc1 lds\n" \
"v_mfma_f32_16x16x32_bf16 a[96:99], a[192:195], v[48:51], a[96:99]\n" \
"v_exp_f32 v67, v67\n" \
"v_mfma_f32_16x16x32_bf16 a[96:99], a[196:199], v[52:55], a[96:99]\n" \
"ds_read_b128 a[192:195], v232 offset:0\n" \
"v_cvt_pk_bf16_f32 v65, v66, v67\n" \
"v_mfma_f32_16x16x32_bf16 a[100:103], a[200:203], v[48:51], a[100:103]\n" \
"v_exp_f32 v68, v68\n" \
"v_mfma_f32_16x16x32_bf16 a[100:103], a[204:207], v[52:55], a[100:103]\n" \
"v_xor_b32 v233, 0x4000, v233\n" \
"v_mfma_f32_16x16x32_bf16 a[68:71], a[200:203], v[32:35], a[68:71]\n" \
"v_exp_f32 v69, v69\n" \
"v_mfma_f32_16x16x32_bf16 a[68:71], a[204:207], v[36:39], a[68:71]\n" \
"ds_read_b128 a[196:199], v232 offset:2048\n" \
"v_cvt_pk_bf16_f32 v66, v68, v69\n" \
"v_smfmac_f32_16x16x64_bf16 a[48:51], v[244:247], v[0:7], v243\n" \
"buffer_load_dwordx4 v234, s[36:39], s73 offen offset:2048 sc1 lds\n" \
"v_mfma_f32_16x16x32_bf16 v[164:167], a[200:203], v[16:19], v[164:167]\n" \
"v_exp_f32 v70, v70\n" \
"v_mfma_f32_16x16x32_bf16 v[164:167], a[204:207], v[20:23], v[164:167]\n" \
"v_mfma_f32_16x16x32_bf16 v[132:135], a[200:203], v[0:3], v[132:135]\n" \
"v_exp_f32 v71, v71\n" \
"v_mfma_f32_16x16x32_bf16 v[132:135], a[204:207], v[4:7], v[132:135]\n" \
"ds_read_b128 a[200:203], v232 offset:4096\n" \
"Lpg6160_%=:\n" \
"s_cbranch_execz Lpg7020_%=\n" \
"v_cvt_pk_bf16_f32 v67, v70, v71\n" \
"v_mfma_f32_16x16x32_bf16 v[136:139], a[208:211], v[0:3], v[136:139]\n" \
"v_exp_f32 v80, v80\n" \
"v_mfma_f32_16x16x32_bf16 v[136:139], a[212:215], v[4:7], v[136:139]\n" \
"buffer_load_dwordx4 v234, s[36:39], s73 offen offset:3072 sc1 lds\n" \
"v_mfma_f32_16x16x32_bf16 v[168:171], a[208:211], v[16:19], v[168:171]\n" \
"v_exp_f32 v81, v81\n" \
"v_mfma_f32_16x16x32_bf16 v[168:171], a[212:215], v[20:23], v[168:171]\n" \
"ds_read_b128 a[204:207], v232 offset:6144\n" \
"v_cvt_pk_bf16_f32 v80, v80, v81\n" \
"v_mfma_f32_16x16x32_bf16 a[72:75], a[208:211], v[32:35], a[72:75]\n" \
"v_exp_f32 v82, v82\n" \
"v_mfma_f32_16x16x32_bf16 a[72:75], a[212:215], v[36:39], a[72:75]\n" \
"v_mfma_f32_16x16x32_bf16 a[104:107], a[208:211], v[48:51], a[104:107]\n" \
"v_exp_f32 v83, v83\n" \
"v_mfma_f32_16x16x32_bf16 a[104:107], a[212:215], v[52:55], a[104:107]\n" \
"ds_read_b128 a[208:211], v232 offset:256\n" \
"v_cvt_pk_bf16_f32 v81, v82, v83\n" \
"v_mfma_f32_16x16x32_bf16 a[108:111], a[216:219], v[48:51], a[108:111]\n" \
"s_add_u32 m0, m0, 0x4000\n" \
"v_exp_f32 v84, v84\n" \
"v_mfma_f32_16x16x32_bf16 a[108:111], a[220:223], v[52:55], a[108:111]\n" \
"buffer_load_dwordx4 v234, s[36:39], s71 offen offset:0 sc1 lds\n" \
"v_mfma_f32_16x16x32_bf16 a[76:79], a[216:219], v[32:35], a[76:79]\n" \
"v_exp_f32 v85, v85\n" \
"v_mfma_f32_16x16x32_bf16 a[76:79], a[220:223], v[36:39], a[76:79]\n" \
"ds_read_b128 a[212:215], v232 offset:2304\n" \
"v_cvt_pk_bf16_f32 v82, v84, v85\n" \
"v_smfmac_f32_16x16x64_bf16 a[52:55], v[244:247], v[16:23], v243\n" \
"v_mfma_f32_16x16x32_bf16 v[172:175], a[216:219], v[16:19], v[172:175]\n" \
"v_exp_f32 v86, v86\n" \
"v_mfma_f32_16x16x32_bf16 v[172:175], a[220:223], v[20:23], v[172:175]\n" \
"buffer_load_dwordx4 v234, s[36:39], s71 offen offset:1024 sc1 lds\n" \
"v_mfma_f32_16x16x32_bf16 v[140:143], a[216:219], v[0:3], v[140:143]\n" \
"s_waitcnt lgkmcnt(7)\n" \
"v_exp_f32 v87, v87\n" \
"v_mfma_f32_16x16x32_bf16 v[140:143], a[220:223], v[4:7], v[140:143]\n" \
"ds_read_b128 a[216:219], v232 offset:4352\n" \
"v_cvt_pk_bf16_f32 v83, v86, v87\n" \
"v_mfma_f32_16x16x32_bf16 v[144:147], a[224:227], v[0:3], v[144:147]\n" \
"v_exp_f32 v96, v96\n" \
"v_mfma_f32_16x16x32_bf16 v[144:147], a[228:231], v[4:7], v[144:147]\n" \
"buffer_load_dwordx4 v234, s[36:39], s71 offen offset:2048 sc1 lds\n" \
"v_mfma_f32_16x16x32_bf16 v[176:179], a[224:227], v[16:19], v[176:179]\n" \
"v_exp_f32 v97, v97\n" \
"v_mfma_f32_16x16x32_bf16 v[176:179], a[228:231], v[20:23], v[176:179]\n" \
"ds_read_b128 a[220:223], v232 offset:6400\n" \
"v_cvt_pk_bf16_f32 v96, v96, v97\n" \
"v_mfma_f32_16x16x32_bf16 a[80:83], a[224:227], v[32:35], a[80:83]\n" \
"v_exp_f32 v98, v98\n" \
"v_mfma_f32_16x16x32_bf16 a[80:83], a[228:231], v[36:39], a[80:83]\n" \
"v_mfma_f32_16x16x32_bf16 a[112:115], a[224:227], v[48:51], a[112:115]\n" \
"v_exp_f32 v99, v99\n" \
"v_mfma_f32_16x16x32_bf16 a[112:115], a[228:231], v[52:55], a[112:115]\n" \
"ds_read_b128 a[224:227], v232 offset:8192\n" \
"v_cvt_pk_bf16_f32 v97, v98, v99\n" \
"v_mfma_f32_16x16x32_bf16 a[116:119], a[232:235], v[48:51], a[116:119]\n" \
"v_exp_f32 v100, v100\n" \
"v_mfma_f32_16x16x32_bf16 a[116:119], a[236:239], v[52:55], a[116:119]\n" \
"buffer_load_dwordx4 v234, s[36:39], s71 offen offset:3072 sc1 lds\n" \
"v_mfma_f32_16x16x32_bf16 a[84:87], a[232:235], v[32:35], a[84:87]\n" \
"v_exp_f32 v101, v101\n" \
"v_mfma_f32_16x16x32_bf16 a[84:87], a[236:239], v[36:39], a[84:87]\n" \
"ds_read_b128 a[228:231], v232 offset:10240\n" \
"v_cvt_pk_bf16_f32 v98, v100, v101\n" \
"v_smfmac_f32_16x16x64_bf16 v[224:227], v[244:247], v[32:39], v243\n" \
"v_mfma_f32_16x16x32_bf16 v[180:183], a[232:235], v[16:19], v[180:183]\n" \
"v_exp_f32 v102, v102\n" \
"v_mfma_f32_16x16x32_bf16 v[180:183], a[236:239], v[20:23], v[180:183]\n" \
"buffer_load_dword v239, v242, s[56:59], s95 offen sc1\n" \
"v_mfma_f32_16x16x32_bf16 v[148:151], a[232:235], v[0:3], v[148:151]\n" \
"s_mov_b32 s69, s92\n" \
"v_exp_f32 v103, v103\n" \
"v_mfma_f32_16x16x32_bf16 v[148:151], a[236:239], v[4:7], v[148:151]\n" \
"s_mov_b32 s76, s73\n" \
"ds_read_b128 a[232:235], v232 offset:12288\n" \
"v_cvt_pk_bf16_f32 v99, v102, v103\n" \
"v_mfma_f32_16x16x32_bf16 v[152:155], a[32:35], v[0:3], v[152:155]\n" \
"s_add_u32 s73, s73, 34816\n" \
"v_exp_f32 v112, v112\n" \
"v_mfma_f32_16x16x32_bf16 v[152:155], a[36:39], v[4:7], v[152:155]\n" \
"s_cmp_eq_u32 s73, s83\n" \
"s_cselect_b32 s70, 34816, 0\n" \
"s_add_u32 s73, s73, s70\n" \
"v_mfma_f32_16x16x32_bf16 v[184:187], a[32:35], v[16:19], v[184:187]\n" \
"s_xor_b32 s74, s74, 0x8000\n" \
"v_exp_f32 v113, v113\n" \
"v_mfma_f32_16x16x32_bf16 v[184:187], a[36:39], v[20:23], v[184:187]\n" \
"s_xor_b32 s77, s77, 0x8000\n" \
"ds_read_b128 a[236:239], v232 offset:14336\n" \
"v_cvt_pk_bf16_f32 v112, v112, v113\n" \
"v_mfma_f32_16x16x32_bf16 a[88:91], a[32:35], v[32:35], a[88:91]\n" \
"v_exp_f32 v114, v114\n" \
"v_mfma_f32_16x16x32_bf16 a[88:91], a[36:39], v[36:39], a[88:91]\n" \
"v_mfma_f32_16x16x32_bf16 a[120:123], a[32:35], v[48:51], a[120:123]\n" \
"v_exp_f32 v115, v115\n" \
"v_mfma_f32_16x16x32_bf16 a[120:123], a[36:39], v[52:55], a[120:123]\n" \
"ds_read_b128 a[244:247], v232 offset:10496\n" \
"v_cvt_pk_bf16_f32 v113, v114, v115\n" \
"v_mfma_f32_16x16x32_bf16 a[124:127], a[40:43], v[48:51], a[124:127]\n" \
"v_exp_f32 v116, v116\n" \
"v_mfma_f32_16x16x32_bf16 a[124:127], a[44:47], v[52:55], a[124:127]\n" \
"v_mfma_f32_16x16x32_bf16 a[92:95], a[40:43], v[32:35], a[92:95]\n" \
"v_exp_f32 v117, v117\n" \
"v_mfma_f32_16x16x32_bf16 a[92:95], a[44:47], v[36:39], a[92:95]\n" \
"ds_read_b128 a[248:251], v232 offset:12544\n" \
"v_cvt_pk_bf16_f32 v114, v116, v117\n" \
"v_smfmac_f32_16x16x64_bf16 v[228:231], v[244:247], v[48:55], v243\n" \
"v_mfma_f32_16x16x32_bf16 v[188:191], a[40:43], v[16:19], v[188:191]\n" \
"v_exp_f32 v118, v118\n" \
"v_mfma_f32_16x16x32_bf16 v[188:191], a[44:47], v[20:23], v[188:191]\n" \
"s_cmp_lg_u32 s89, 0\n" \
"v_exp_f32 v119, v119\n" \
"v_mfma_f32_16x16x32_bf16 v[156:159], a[40:43], v[0:3], v[156:159]\n" \
"s_waitcnt lgkmcnt(2)\n" \
"ds_read_b128 a[252:255], v232 offset:14592\n" \
"v_cvt_pk_bf16_f32 v115, v118, v119\n" \
"v_mfma_f32_16x16x32_bf16 v[156:159], a[44:47], v[4:7], v[156:159]\n" \
"s_cbranch_scc1 Lloop_%=\n" \
"s_cmp_eq_u32 s63, 7\n" \
"s_cbranch_scc0 Lloop2_%=\n" \
"s_branch Lg_%=\n" \
"Lep_%=:\n" \
"Lpg6284_%=:\n" \
"s_cbranch_execz Lpg7360_%=\n" \
"s_nop 4\n" \
"v_accvgpr_read_b32 v92, a48\n" \
"v_accvgpr_read_b32 v93, a49\n" \
"v_accvgpr_read_b32 v94, a52\n" \
"v_accvgpr_read_b32 v95, a53\n" \
"v_accvgpr_write_b32 a48, 0\n" \
"v_accvgpr_write_b32 a49, 0\n" \
"v_accvgpr_write_b32 a52, 0\n" \
"v_accvgpr_write_b32 a53, 0\n" \
"v_add_f32 v86, v92, v93\n" \
"v_add_f32 v87, v94, v95\n" \
"v_rcp_f32 v84, v86\n" \
"v_rcp_f32 v85, v87\n" \
"v_add_u32 v240, 0x1000, v236\n" \
"v_add_f32 v80, v86, v87\n" \
"v_mul_f32 v80, 0x43800000, v80\n" \
"v_add_f32 v86, v84, v85\n" \
"v_fmac_f32 v80, 0x5f800000, v86\n" \
"v_mov_b32 v81, 0\n" \
"v_mov_b32 v82, 0\n" \
"v_mov_b32 v83, 0\n" \
"v_mov_b32 v88, 0\n" \
"v_mov_b32 v89, 0\n" \
"v_mov_b32 v90, 0\n" \
"v_mov_b32 v91, 0\n" \
"v_pk_mul_f32 v[128:129], v[128:129], v[84:85] op_sel:[0,0] op_sel_hi:[1,0]\n" \
"v_pk_mul_f32 v[130:131], v[130:131], v[84:85] op_sel:[0,0] op_sel_hi:[1,0]\n" \
"v_pk_mul_f32 v[132:133], v[132:133], v[84:85] op_sel:[0,0] op_sel_hi:[1,0]\n" \
"v_pk_mul_f32 v[134:135], v[134:135], v[84:85] op_sel:[0,0] op_sel_hi:[1,0]\n" \
"v_cvt_pk_bf16_f32 v64, v128, v129\n" \
"v_cvt_pk_bf16_f32 v65, v130, v131\n" \
"v_cvt_pk_bf16_f32 v66, v132, v133\n" \
"v_cvt_pk_bf16_f32 v67, v134, v135\n" \
"buffer_store_dwordx4 v[64:67], v236, s[56:59], s87 offen offset:0 sc1\n" \
"v_pk_mul_f32 v[136:137], v[136:137], v[84:85] op_sel:[0,0] op_sel_hi:[1,0]\n" \
"v_pk_mul_f32 v[138:139], v[138:139], v[84:85] op_sel:[0,0] op_sel_hi:[1,0]\n" \
"v_pk_mul_f32 v[140:141], v[140:141], v[84:85] op_sel:[0,0] op_sel_hi:[1,0]\n" \
"v_pk_mul_f32 v[142:143], v[142:143], v[84:85] op_sel:[0,0] op_sel_hi:[1,0]\n" \
"v_cvt_pk_bf16_f32 v68, v136, v137\n" \
"v_cvt_pk_bf16_f32 v69, v138, v139\n" \
"v_cvt_pk_bf16_f32 v70, v140, v141\n" \
"v_cvt_pk_bf16_f32 v71, v142, v143\n" \
"buffer_store_dwordx4 v[68:71], v236, s[56:59], s87 offen offset:2048 sc1\n" \
"v_pk_mul_f32 v[144:145], v[144:145], v[84:85] op_sel:[0,0] op_sel_hi:[1,0]\n" \
"v_pk_mul_f32 v[146:147], v[146:147], v[84:85] op_sel:[0,0] op_sel_hi:[1,0]\n" \
"v_pk_mul_f32 v[148:149], v[148:149], v[84:85] op_sel:[0,0] op_sel_hi:[1,0]\n" \
"v_pk_mul_f32 v[150:151], v[150:151], v[84:85] op_sel:[0,0] op_sel_hi:[1,0]\n" \
"v_cvt_pk_bf16_f32 v72, v144, v145\n" \
"v_cvt_pk_bf16_f32 v73, v146, v147\n" \
"v_cvt_pk_bf16_f32 v74, v148, v149\n" \
"v_cvt_pk_bf16_f32 v75, v150, v151\n" \
"buffer_store_dwordx4 v[72:75], v240, s[56:59], s87 offen offset:0 sc1\n" \
"v_pk_mul_f32 v[152:153], v[152:153], v[84:85] op_sel:[0,0] op_sel_hi:[1,0]\n" \
"v_pk_mul_f32 v[154:155], v[154:155], v[84:85] op_sel:[0,0] op_sel_hi:[1,0]\n" \
"v_pk_mul_f32 v[156:157], v[156:157], v[84:85] op_sel:[0,0] op_sel_hi:[1,0]\n" \
"v_pk_mul_f32 v[158:159], v[158:159], v[84:85] op_sel:[0,0] op_sel_hi:[1,0]\n" \
"v_cvt_pk_bf16_f32 v76, v152, v153\n" \
"v_cvt_pk_bf16_f32 v77, v154, v155\n" \
"v_cvt_pk_bf16_f32 v78, v156, v157\n" \
"v_cvt_pk_bf16_f32 v79, v158, v159\n" \
"buffer_store_dwordx4 v[76:79], v240, s[56:59], s87 offen offset:2048 sc1\n" \
"v_smfmac_f32_16x16x64_bf16 v[80:83], v[244:247], v[64:71], v243\n" \
"v_smfmac_f32_16x16x64_bf16 v[80:83], v[244:247], v[72:79], v243\n" \
"v_mfma_f32_32x32x16_bf16 v[128:143], v[88:91], v[88:91], 0\n" \
"v_mfma_f32_32x32x16_bf16 v[144:159], v[88:91], v[88:91], 0\n" \
"s_mov_b64 exec, 0\n" \
"s_cmp_eq_u32 s61, 0\n" \
"s_cbranch_scc1 Lst1x0_%=\n" \
"s_cmp_eq_u32 s61, 1\n" \
"s_cbranch_scc1 Lst1x1_%=\n" \
"s_cmp_eq_u32 s61, 2\n" \
"s_cbranch_scc1 Lst1x2_%=\n" \
"s_branch Lst1x3_%=\n" \
"Lcb1_%=:\n" \
"s_mov_b64 exec, -1\n" \
"s_add_u32 s72, s87, 532480\n" \
"v_pk_mul_f32 v[160:161], v[160:161], v[84:85] op_sel:[0,1] op_sel_hi:[1,1]\n" \
"v_pk_mul_f32 v[162:163], v[162:163], v[84:85] op_sel:[0,1] op_sel_hi:[1,1]\n" \
"v_pk_mul_f32 v[164:165], v[164:165], v[84:85] op_sel:[0,1] op_sel_hi:[1,1]\n" \
"v_pk_mul_f32 v[166:167], v[166:167], v[84:85] op_sel:[0,1] op_sel_hi:[1,1]\n" \
"v_cvt_pk_bf16_f32 v64, v160, v161\n" \
"v_cvt_pk_bf16_f32 v65, v162, v163\n" \
"v_cvt_pk_bf16_f32 v66, v164, v165\n" \
"v_cvt_pk_bf16_f32 v67, v166, v167\n" \
"buffer_store_dwordx4 v[64:67], v236, s[56:59], s72 offen offset:0 sc1\n" \
"v_pk_mul_f32 v[168:169], v[168:169], v[84:85] op_sel:[0,1] op_sel_hi:[1,1]\n" \
"v_pk_mul_f32 v[170:171], v[170:171], v[84:85] op_sel:[0,1] op_sel_hi:[1,1]\n" \
"v_pk_mul_f32 v[172:173], v[172:173], v[84:85] op_sel:[0,1] op_sel_hi:[1,1]\n" \
"v_pk_mul_f32 v[174:175], v[174:175], v[84:85] op_sel:[0,1] op_sel_hi:[1,1]\n" \
"v_cvt_pk_bf16_f32 v68, v168, v169\n" \
"v_cvt_pk_bf16_f32 v69, v170, v171\n" \
"v_cvt_pk_bf16_f32 v70, v172, v173\n" \
"v_cvt_pk_bf16_f32 v71, v174, v175\n" \
"buffer_store_dwordx4 v[68:71], v236, s[56:59], s72 offen offset:2048 sc1\n" \
"v_pk_mul_f32 v[176:177], v[176:177], v[84:85] op_sel:[0,1] op_sel_hi:[1,1]\n" \
"v_pk_mul_f32 v[178:179], v[178:179], v[84:85] op_sel:[0,1] op_sel_hi:[1,1]\n" \
"v_pk_mul_f32 v[180:181], v[180:181], v[84:85] op_sel:[0,1] op_sel_hi:[1,1]\n" \
"v_pk_mul_f32 v[182:183], v[182:183], v[84:85] op_sel:[0,1] op_sel_hi:[1,1]\n" \
"v_cvt_pk_bf16_f32 v72, v176, v177\n" \
"v_cvt_pk_bf16_f32 v73, v178, v179\n" \
"v_cvt_pk_bf16_f32 v74, v180, v181\n" \
"v_cvt_pk_bf16_f32 v75, v182, v183\n" \
"buffer_store_dwordx4 v[72:75], v240, s[56:59], s72 offen offset:0 sc1\n" \
"v_pk_mul_f32 v[184:185], v[184:185], v[84:85] op_sel:[0,1] op_sel_hi:[1,1]\n" \
"v_pk_mul_f32 v[186:187], v[186:187], v[84:85] op_sel:[0,1] op_sel_hi:[1,1]\n" \
"v_pk_mul_f32 v[188:189], v[188:189], v[84:85] op_sel:[0,1] op_sel_hi:[1,1]\n" \
"v_pk_mul_f32 v[190:191], v[190:191], v[84:85] op_sel:[0,1] op_sel_hi:[1,1]\n" \
"v_cvt_pk_bf16_f32 v76, v184, v185\n" \
"v_cvt_pk_bf16_f32 v77, v186, v187\n" \
"v_cvt_pk_bf16_f32 v78, v188, v189\n" \
"v_cvt_pk_bf16_f32 v79, v190, v191\n" \
"buffer_store_dwordx4 v[76:79], v240, s[56:59], s72 offen offset:2048 sc1\n" \
"v_smfmac_f32_16x16x64_bf16 v[80:83], v[244:247], v[64:71], v243\n" \
"v_smfmac_f32_16x16x64_bf16 v[80:83], v[244:247], v[72:79], v243\n" \
"v_mfma_f32_32x32x16_bf16 v[160:175], v[88:91], v[88:91], 0\n" \
"v_mfma_f32_32x32x16_bf16 v[176:191], v[88:91], v[88:91], 0\n" \
"s_nop 5\n" \
"v_add_f32 v80, v80, v81\n" \
"s_movk_i32 s64, 0x207\n" \
"v_cmp_class_f32 vcc, v80, s64\n" \
"s_nop 1\n" \
"s_cmp_lg_u64 vcc, 0\n" \
"s_addc_u32 s90, s90, 0\n" \
"s_waitcnt vmcnt(8)\n" \
"s_cmp_eq_u32 s63, 7\n" \
"s_cbranch_scc0 Lepr_%=\n" \
"s_branch Lepg_%=\n" \
"Lloop2_%=:\n" \
"Lpg6413_%=:\n" \
"s_cbranch_execz Lcb0_%=\n" \
"v_mfma_f32_16x16x32_bf16 v[0:3], a[192:195], a[0:3], v[196:199]\n" \
"v_exp_f32 v72, v72\n" \
"v_mfma_f32_16x16x32_bf16 v[0:3], a[196:199], a[4:7], v[0:3]\n" \
"ds_read_b128 a[44:47], v233 offset:15360\n" \
"v_mfma_f32_16x16x32_bf16 v[0:3], a[200:203], a[8:11], v[0:3]\n" \
"v_exp_f32 v73, v73\n" \
"v_mfma_f32_16x16x32_bf16 v[0:3], a[204:207], a[12:15], v[0:3]\n" \
"ds_read_b128 a[32:35], v233 offset:6144\n" \
"v_cvt_pk_bf16_f32 v68, v72, v73\n" \
"v_mfma_f32_16x16x32_bf16 v[16:19], a[204:207], a[28:31], v[196:199]\n" \
"v_exp_f32 v74, v74\n" \
"v_mfma_f32_16x16x32_bf16 v[16:19], a[200:203], a[24:27], v[16:19]\n" \
"v_xor_b32 v232, 0x8000, v232\n" \
"v_mfma_f32_16x16x32_bf16 v[16:19], a[196:199], a[20:23], v[16:19]\n" \
"v_exp_f32 v75, v75\n" \
"v_mfma_f32_16x16x32_bf16 v[16:19], a[192:195], a[16:19], v[16:19]\n" \
"ds_read_b128 a[36:39], v233 offset:14336\n" \
"v_cvt_pk_bf16_f32 v69, v74, v75\n" \
"v_mfma_f32_16x16x32_bf16 v[32:35], a[192:195], a[160:163], v[196:199]\n" \
"v_exp_f32 v76, v76\n" \
"v_mfma_f32_16x16x32_bf16 v[32:35], a[196:199], a[164:167], v[32:35]\n" \
"v_mfma_f32_16x16x32_bf16 v[32:35], a[200:203], a[168:171], v[32:35]\n" \
"v_exp_f32 v77, v77\n" \
"v_mfma_f32_16x16x32_bf16 v[32:35], a[204:207], a[172:175], v[32:35]\n" \
"ds_read_b128 a[40:43], v233 offset:7168\n" \
"v_cvt_pk_bf16_f32 v70, v76, v77\n" \
"v_mfma_f32_16x16x32_bf16 v[48:51], a[204:207], a[188:191], v[196:199]\n" \
"v_exp_f32 v78, v78\n" \
"v_mfma_f32_16x16x32_bf16 v[48:51], a[200:203], a[184:187], v[48:51]\n" \
"v_mfma_f32_16x16x32_bf16 v[48:51], a[196:199], a[180:183], v[48:51]\n" \
"v_exp_f32 v79, v79\n" \
"v_mfma_f32_16x16x32_bf16 v[48:51], a[192:195], a[176:179], v[48:51]\n" \
"ds_read_b128 a[204:207], v233 offset:9216\n" \
"v_cvt_pk_bf16_f32 v71, v78, v79\n" \
"v_mfma_f32_16x16x32_bf16 v[52:55], a[208:211], a[176:179], v[196:199]\n" \
"v_exp_f32 v88, v88\n" \
"v_mfma_f32_16x16x32_bf16 v[52:55], a[212:215], a[180:183], v[52:55]\n" \
"v_mfma_f32_16x16x32_bf16 v[52:55], a[216:219], a[184:187], v[52:55]\n" \
"v_exp_f32 v89, v89\n" \
"v_mfma_f32_16x16x32_bf16 v[52:55], a[220:223], a[188:191], v[52:55]\n" \
"ds_read_b128 a[200:203], v233 offset:1024\n" \
"v_cvt_pk_bf16_f32 v84, v88, v89\n" \
"v_mfma_f32_16x16x32_bf16 v[36:39], a[220:223], a[172:175], v[196:199]\n" \
"v_exp_f32 v90, v90\n" \
"v_mfma_f32_16x16x32_bf16 v[36:39], a[216:219], a[168:171], v[36:39]\n" \
"v_mfma_f32_16x16x32_bf16 v[36:39], a[212:215], a[164:167], v[36:39]\n" \
"v_exp_f32 v91, v91\n" \
"v_mfma_f32_16x16x32_bf16 v[36:39], a[208:211], a[160:163], v[36:39]\n" \
"ds_read_b128 a[196:199], v233 offset:8192\n" \
"v_cvt_pk_bf16_f32 v85, v90, v91\n" \
"v_mfma_f32_16x16x32_bf16 v[20:23], a[208:211], a[16:19], v[196:199]\n" \
"v_exp_f32 v92, v92\n" \
"v_mfma_f32_16x16x32_bf16 v[20:23], a[212:215], a[20:23], v[20:23]\n" \
"v_mfma_f32_16x16x32_bf16 v[20:23], a[216:219], a[24:27], v[20:23]\n" \
"v_exp_f32 v93, v93\n" \
"v_mfma_f32_16x16x32_bf16 v[20:23], a[220:223], a[28:31], v[20:23]\n" \
"ds_read_b128 a[192:195], v233 offset:0\n" \
"v_cvt_pk_bf16_f32 v86, v92, v93\n" \
"v_mfma_f32_16x16x32_bf16 v[4:7], a[220:223], a[12:15], v[196:199]\n" \
"v_exp_f32 v94, v94\n" \
"v_mfma_f32_16x16x32_bf16 v[4:7], a[216:219], a[8:11], v[4:7]\n" \
"v_mfma_f32_16x16x32_bf16 v[4:7], a[212:215], a[4:7], v[4:7]\n" \
"s_waitcnt lgkmcnt(8)\n" \
"v_exp_f32 v95, v95\n" \
"v_mfma_f32_16x16x32_bf16 v[4:7], a[208:211], a[0:3], v[4:7]\n" \
"ds_read_b128 a[220:223], v233 offset:11264\n" \
"v_cvt_pk_bf16_f32 v87, v94, v95\n" \
"v_mfma_f32_16x16x32_bf16 v[8:11], a[224:227], a[0:3], v[196:199]\n" \
"v_exp_f32 v104, v104\n" \
"v_mfma_f32_16x16x32_bf16 v[8:11], a[228:231], a[4:7], v[8:11]\n" \
"v_mfma_f32_16x16x32_bf16 v[8:11], a[232:235], a[8:11], v[8:11]\n" \
"v_exp_f32 v105, v105\n" \
"v_mfma_f32_16x16x32_bf16 v[8:11], a[236:239], a[12:15], v[8:11]\n" \
"ds_read_b128 a[216:219], v233 offset:3072\n" \
"v_cvt_pk_bf16_f32 v100, v104, v105\n" \
"v_mfma_f32_16x16x32_bf16 v[24:27], a[236:239], a[28:31], v[196:199]\n" \
"v_exp_f32 v106, v106\n" \
"v_mfma_f32_16x16x32_bf16 v[24:27], a[232:235], a[24:27], v[24:27]\n" \
"v_mfma_f32_16x16x32_bf16 v[24:27], a[228:231], a[20:23], v[24:27]\n" \
"v_exp_f32 v107, v107\n" \
"v_mfma_f32_16x16x32_bf16 v[24:27], a[224:227], a[16:19], v[24:27]\n" \
"ds_read_b128 a[212:215], v233 offset:10240\n" \
"v_cvt_pk_bf16_f32 v101, v106, v107\n" \
"v_mfma_f32_16x16x32_bf16 v[40:43], a[224:227], a[160:163], v[196:199]\n" \
"v_exp_f32 v108, v108\n" \
"v_mfma_f32_16x16x32_bf16 v[40:43], a[228:231], a[164:167], v[40:43]\n" \
"v_mfma_f32_16x16x32_bf16 v[40:43], a[232:235], a[168:171], v[40:43]\n" \
"v_exp_f32 v109, v109\n" \
"v_mfma_f32_16x16x32_bf16 v[40:43], a[236:239], a[172:175], v[40:43]\n" \
"ds_read_b128 a[208:211], v233 offset:2048\n" \
"v_cvt_pk_bf16_f32 v102, v108, v109\n" \
"v_mfma_f32_16x16x32_bf16 v[56:59], a[236:239], a[188:191], v[196:199]\n" \
"v_exp_f32 v110, v110\n" \
"v_mfma_f32_16x16x32_bf16 v[56:59], a[232:235], a[184:187], v[56:59]\n" \
"v_mfma_f32_16x16x32_bf16 v[56:59], a[228:231], a[180:183], v[56:59]\n" \
"v_exp_f32 v111, v111\n" \
"v_mfma_f32_16x16x32_bf16 v[56:59], a[224:227], a[176:179], v[56:59]\n" \
"ds_read_b128 a[236:239], v233 offset:13312\n" \
"v_cvt_pk_bf16_f32 v103, v110, v111\n" \
"v_mfma_f32_16x16x32_bf16 v[60:63], a[240:243], a[176:179], v[196:199]\n" \
"v_exp_f32 v120, v120\n" \
"v_mfma_f32_16x16x32_bf16 v[60:63], a[244:247], a[180:183], v[60:63]\n" \
"ds_read_b128 a[232:235], v233 offset:5120\n" \
"v_mfma_f32_16x16x32_bf16 v[60:63], a[248:251], a[184:187], v[60:63]\n" \
"v_exp_f32 v121, v121\n" \
"v_mfma_f32_16x16x32_bf16 v[60:63], a[252:255], a[188:191], v[60:63]\n" \
"ds_read_b128 a[228:231], v233 offset:12288\n" \
"v_cvt_pk_bf16_f32 v116, v120, v121\n" \
"v_mfma_f32_16x16x32_bf16 v[44:47], a[252:255], a[172:175], v[196:199]\n" \
"v_exp_f32 v122, v122\n" \
"v_mfma_f32_16x16x32_bf16 v[44:47], a[248:251], a[168:171], v[44:47]\n" \
"ds_read_b128 a[224:227], v233 offset:4096\n" \
"v_mfma_f32_16x16x32_bf16 v[44:47], a[244:247], a[164:167], v[44:47]\n" \
"v_exp_f32 v123, v123\n" \
"v_mfma_f32_16x16x32_bf16 v[44:47], a[240:243], a[160:163], v[44:47]\n" \
"v_cvt_pk_bf16_f32 v117, v122, v123\n" \
"v_mfma_f32_16x16x32_bf16 v[28:31], a[240:243], a[16:19], v[196:199]\n" \
"v_exp_f32 v124, v124\n" \
"v_mfma_f32_16x16x32_bf16 v[28:31], a[244:247], a[20:23], v[28:31]\n" \
"v_mfma_f32_16x16x32_bf16 v[28:31], a[248:251], a[24:27], v[28:31]\n" \
"v_exp_f32 v125, v125\n" \
"v_mfma_f32_16x16x32_bf16 v[28:31], a[252:255], a[28:31], v[28:31]\n" \
"v_cvt_pk_bf16_f32 v118, v124, v125\n" \
"v_mfma_f32_16x16x32_bf16 v[12:15], a[252:255], a[12:15], v[196:199]\n" \
"v_exp_f32 v126, v126\n" \
"v_mfma_f32_16x16x32_bf16 v[12:15], a[248:251], a[8:11], v[12:15]\n" \
"v_mfma_f32_16x16x32_bf16 v[12:15], a[244:247], a[4:7], v[12:15]\n" \
"s_waitcnt vmcnt(0) lgkmcnt(0)\n" \
"v_exp_f32 v127, v127\n" \
"v_mfma_f32_16x16x32_bf16 v[12:15], a[240:243], a[0:3], v[12:15]\n" \
"s_barrier\n" \
"v_cvt_pk_bf16_f32 v119, v126, v127\n" \
"v_mfma_f32_16x16x32_bf16 v[128:131], a[192:195], v[64:67], v[128:131]\n" \
"v_exp_f32 v0, v0\n" \
"v_mfma_f32_16x16x32_bf16 v[128:131], a[196:199], v[68:71], v[128:131]\n" \
"v_cmp_eq_u32 vcc, s60, v239\n" \
"v_mfma_f32_16x16x32_bf16 v[160:163], a[192:195], v[80:83], v[160:163]\n" \
"v_exp_f32 v1, v1\n" \
"v_mfma_f32_16x16x32_bf16 v[160:163], a[196:199], v[84:87], v[160:163]\n" \
"s_andn2_b32 s70, s69, vcc_lo\n" \
"s_cbranch_scc1 Lpl3_%=\n" \
"Lpr3_%=:\n" \
"ds_read_b128 a[240:243], v232 offset:8448\n" \
"v_cvt_pk_bf16_f32 v0, v0, v1\n" \
"v_mfma_f32_16x16x32_bf16 a[64:67], a[192:195], v[96:99], a[64:67]\n" \
"s_add_u32 m0, s74, s79\n" \
"v_exp_f32 v2, v2\n" \
"v_mfma_f32_16x16x32_bf16 a[64:67], a[196:199], v[100:103], a[64:67]\n" \
"buffer_load_dwordx4 v234, s[36:39], s73 offen offset:0 sc1 lds\n" \
"v_mfma_f32_16x16x32_bf16 a[96:99], a[192:195], v[112:115], a[96:99]\n" \
"v_exp_f32 v3, v3\n" \
"v_mfma_f32_16x16x32_bf16 a[96:99], a[196:199], v[116:119], a[96:99]\n" \
"ds_read_b128 a[192:195], v232 offset:0\n" \
"v_cvt_pk_bf16_f32 v1, v2, v3\n" \
"v_mfma_f32_16x16x32_bf16 a[100:103], a[200:203], v[112:115], a[100:103]\n" \
"s_add_u32 s71, s73, 17408\n" \
"v_exp_f32 v4, v4\n" \
"v_mfma_f32_16x16x32_bf16 a[100:103], a[204:207], v[116:119], a[100:103]\n" \
"buffer_load_dwordx4 v234, s[36:39], s73 offen offset:1024 sc1 lds\n" \
"v_mfma_f32_16x16x32_bf16 a[68:71], a[200:203], v[96:99], a[68:71]\n" \
"v_exp_f32 v5, v5\n" \
"v_mfma_f32_16x16x32_bf16 a[68:71], a[204:207], v[100:103], a[68:71]\n" \
"ds_read_b128 a[196:199], v232 offset:2048\n" \
"v_cvt_pk_bf16_f32 v2, v4, v5\n" \
"v_smfmac_f32_16x16x64_bf16 a[48:51], v[244:247], v[64:71], v243\n" \
"buffer_load_dwordx4 v234, s[36:39], s73 offen offset:2048 sc1 lds\n" \
"v_mfma_f32_16x16x32_bf16 v[164:167], a[200:203], v[80:83], v[164:167]\n" \
"v_exp_f32 v6, v6\n" \
"v_mfma_f32_16x16x32_bf16 v[164:167], a[204:207], v[84:87], v[164:167]\n" \
"buffer_load_dwordx4 v234, s[36:39], s73 offen offset:3072 sc1 lds\n" \
"v_mfma_f32_16x16x32_bf16 v[132:135], a[200:203], v[64:67], v[132:135]\n" \
"v_exp_f32 v7, v7\n" \
"v_mfma_f32_16x16x32_bf16 v[132:135], a[204:207], v[68:71], v[132:135]\n" \
"ds_read_b128 a[200:203], v232 offset:4096\n" \
"v_cvt_pk_bf16_f32 v3, v6, v7\n" \
"v_mfma_f32_16x16x32_bf16 v[136:139], a[208:211], v[64:67], v[136:139]\n" \
"s_add_u32 m0, m0, 0x4000\n" \
"v_exp_f32 v16, v16\n" \
"v_mfma_f32_16x16x32_bf16 v[136:139], a[212:215], v[68:71], v[136:139]\n" \
"buffer_load_dwordx4 v234, s[36:39], s71 offen offset:0 sc1 lds\n" \
"v_mfma_f32_16x16x32_bf16 v[168:171], a[208:211], v[80:83], v[168:171]\n" \
"v_exp_f32 v17, v17\n" \
"v_mfma_f32_16x16x32_bf16 v[168:171], a[212:215], v[84:87], v[168:171]\n" \
"ds_read_b128 a[204:207], v232 offset:6144\n" \
"v_cvt_pk_bf16_f32 v16, v16, v17\n" \
"v_mfma_f32_16x16x32_bf16 a[72:75], a[208:211], v[96:99], a[72:75]\n" \
"v_exp_f32 v18, v18\n" \
"v_mfma_f32_16x16x32_bf16 a[72:75], a[212:215], v[100:103], a[72:75]\n" \
"buffer_load_dwordx4 v234, s[36:39], s71 offen offset:1024 sc1 lds\n" \
"v_mfma_f32_16x16x32_bf16 a[104:107], a[208:211], v[112:115], a[104:107]\n" \
"v_exp_f32 v19, v19\n" \
"v_mfma_f32_16x16x32_bf16 a[104:107], a[212:215], v[116:119], a[104:107]\n" \
"ds_read_b128 a[208:211], v232 offset:256\n" \
"v_cvt_pk_bf16_f32 v17, v18, v19\n" \
"v_mfma_f32_16x16x32_bf16 a[108:111], a[216:219], v[112:115], a[108:111]\n" \
"v_exp_f32 v20, v20\n" \
"v_mfma_f32_16x16x32_bf16 a[108:111], a[220:223], v[116:119], a[108:111]\n" \
"buffer_load_dwordx4 v234, s[36:39], s71 offen offset:2048 sc1 lds\n" \
"v_mfma_f32_16x16x32_bf16 a[76:79], a[216:219], v[96:99], a[76:79]\n" \
"v_exp_f32 v21, v21\n" \
"v_mfma_f32_16x16x32_bf16 a[76:79], a[220:223], v[100:103], a[76:79]\n" \
"ds_read_b128 a[212:215], v232 offset:2304\n" \
"v_cvt_pk_bf16_f32 v18, v20, v21\n" \
"v_smfmac_f32_16x16x64_bf16 a[52:55], v[244:247], v[80:87], v243\n" \
"buffer_load_dwordx4 v234, s[36:39], s71 offen offset:3072 sc1 lds\n" \
"v_mfma_f32_16x16x32_bf16 v[172:175], a[216:219], v[80:83], v[172:175]\n" \
"s_add_u32 m0, s77, s80\n" \
"v_exp_f32 v22, v22\n" \
"v_mfma_f32_16x16x32_bf16 v[172:175], a[220:223], v[84:87], v[172:175]\n" \
"buffer_load_dwordx4 v234, s[40:43], s76 offen offset:0 sc1 lds\n" \
"v_mfma_f32_16x16x32_bf16 v[140:143], a[216:219], v[64:67], v[140:143]\n" \
"v_exp_f32 v23, v23\n" \
"v_mfma_f32_16x16x32_bf16 v[140:143], a[220:223], v[68:71], v[140:143]\n" \
"ds_read_b128 a[216:219], v232 offset:4352\n" \
"v_cvt_pk_bf16_f32 v19, v22, v23\n" \
"v_mfma_f32_16x16x32_bf16 v[144:147], a[224:227], v[64:67], v[144:147]\n" \
"s_add_u32 s71, s76, 17408\n" \
"v_exp_f32 v32, v32\n" \
"v_mfma_f32_16x16x32_bf16 v[144:147], a[228:231], v[68:71], v[144:147]\n" \
"buffer_load_dwordx4 v234, s[40:43], s76 offen offset:1024 sc1 lds\n" \
"v_mfma_f32_16x16x32_bf16 v[176:179], a[224:227], v[80:83], v[176:179]\n" \
"v_exp_f32 v33, v33\n" \
"v_mfma_f32_16x16x32_bf16 v[176:179], a[228:231], v[84:87], v[176:179]\n" \
"ds_read_b128 a[220:223], v232 offset:6400\n" \
"v_cvt_pk_bf16_f32 v32, v32, v33\n" \
"v_mfma_f32_16x16x32_bf16 a[80:83], a[224:227], v[96:99], a[80:83]\n" \
"v_exp_f32 v34, v34\n" \
"v_mfma_f32_16x16x32_bf16 a[80:83], a[228:231], v[100:103], a[80:83]\n" \
"buffer_load_dwordx4 v234, s[40:43], s76 offen offset:2048 sc1 lds\n" \
"v_mfma_f32_16x16x32_bf16 a[112:115], a[224:227], v[112:115], a[112:115]\n" \
"v_exp_f32 v35, v35\n" \
"v_mfma_f32_16x16x32_bf16 a[112:115], a[228:231], v[116:119], a[112:115]\n" \
"ds_read_b128 a[224:227], v232 offset:8192\n" \
"v_cvt_pk_bf16_f32 v33, v34, v35\n" \
"v_mfma_f32_16x16x32_bf16 a[116:119], a[232:235], v[112:115], a[116:119]\n" \
"v_exp_f32 v36, v36\n" \
"v_mfma_f32_16x16x32_bf16 a[116:119], a[236:239], v[116:119], a[116:119]\n" \
"buffer_load_dwordx4 v234, s[40:43], s76 offen offset:3072 sc1 lds\n" \
"v_mfma_f32_16x16x32_bf16 a[84:87], a[232:235], v[96:99], a[84:87]\n" \
"v_exp_f32 v37, v37\n" \
"v_mfma_f32_16x16x32_bf16 a[84:87], a[236:239], v[100:103], a[84:87]\n" \
"s_add_u32 m0, m0, 0x4000\n" \
"ds_read_b128 a[228:231], v232 offset:10240\n" \
"v_cvt_pk_bf16_f32 v34, v36, v37\n" \
"v_smfmac_f32_16x16x64_bf16 v[224:227], v[244:247], v[96:103], v243\n" \
"buffer_load_dwordx4 v234, s[40:43], s71 offen offset:0 sc1 lds\n" \
"v_mfma_f32_16x16x32_bf16 v[180:183], a[232:235], v[80:83], v[180:183]\n" \
"v_exp_f32 v38, v38\n" \
"v_mfma_f32_16x16x32_bf16 v[180:183], a[236:239], v[84:87], v[180:183]\n" \
"buffer_load_dwordx4 v234, s[40:43], s71 offen offset:1024 sc1 lds\n" \
"v_mfma_f32_16x16x32_bf16 v[148:151], a[232:235], v[64:67], v[148:151]\n" \
"v_exp_f32 v39, v39\n" \
"v_mfma_f32_16x16x32_bf16 v[148:151], a[236:239], v[68:71], v[148:151]\n" \
"ds_read_b128 a[232:235], v232 offset:12288\n" \
"v_cvt_pk_bf16_f32 v35, v38, v39\n" \
"v_mfma_f32_16x16x32_bf16 v[152:155], a[32:35], v[64:67], v[152:155]\n" \
"v_exp_f32 v48, v48\n" \
"v_mfma_f32_16x16x32_bf16 v[152:155], a[36:39], v[68:71], v[152:155]\n" \
"buffer_load_dwordx4 v234, s[40:43], s71 offen offset:2048 sc1 lds\n" \
"v_mfma_f32_16x16x32_bf16 v[184:187], a[32:35], v[80:83], v[184:187]\n" \
"v_exp_f32 v49, v49\n" \
"v_mfma_f32_16x16x32_bf16 v[184:187], a[36:39], v[84:87], v[184:187]\n" \
"ds_read_b128 a[236:239], v232 offset:14336\n" \
"v_cvt_pk_bf16_f32 v48, v48, v49\n" \
"v_mfma_f32_16x16x32_bf16 a[88:91], a[32:35], v[96:99], a[88:91]\n" \
"v_exp_f32 v50, v50\n" \
"v_mfma_f32_16x16x32_bf16 a[88:91], a[36:39], v[100:103], a[88:91]\n" \
"buffer_load_dwordx4 v234, s[40:43], s71 offen offset:3072 sc1 lds\n" \
"v_mfma_f32_16x16x32_bf16 a[120:123], a[32:35], v[112:115], a[120:123]\n" \
"s_mov_b32 s76, s73\n" \
"v_exp_f32 v51, v51\n" \
"v_mfma_f32_16x16x32_bf16 a[120:123], a[36:39], v[116:119], a[120:123]\n" \
"s_add_u32 s73, s73, 34816\n" \
"ds_read_b128 a[244:247], v232 offset:10496\n" \
"v_cvt_pk_bf16_f32 v49, v50, v51\n" \
"v_mfma_f32_16x16x32_bf16 a[124:127], a[40:43], v[112:115], a[124:127]\n" \
"s_xor_b32 s74, s74, 0x8000\n" \
"v_exp_f32 v52, v52\n" \
"v_mfma_f32_16x16x32_bf16 a[124:127], a[44:47], v[116:119], a[124:127]\n" \
"s_xor_b32 s77, s77, 0x8000\n" \
"v_xor_b32 v233, s81, v233\n" \
"s_mov_b32 s81, 0x8000\n" \
"v_mfma_f32_16x16x32_bf16 a[92:95], a[40:43], v[96:99], a[92:95]\n" \
"v_exp_f32 v53, v53\n" \
"v_mfma_f32_16x16x32_bf16 a[92:95], a[44:47], v[100:103], a[92:95]\n" \
"ds_read_b128 a[248:251], v232 offset:12544\n" \
"v_cvt_pk_bf16_f32 v50, v52, v53\n" \
"v_smfmac_f32_16x16x64_bf16 v[228:231], v[244:247], v[112:119], v243\n" \
"v_mfma_f32_16x16x32_bf16 v[188:191], a[40:43], v[80:83], v[188:191]\n" \
"v_exp_f32 v54, v54\n" \
"v_mfma_f32_16x16x32_bf16 v[188:191], a[44:47], v[84:87], v[188:191]\n" \
"s_cmp_lg_u32 s82, 0\n" \
"v_exp_f32 v55, v55\n" \
"v_mfma_f32_16x16x32_bf16 v[156:159], a[40:43], v[64:67], v[156:159]\n" \
"s_waitcnt lgkmcnt(2)\n" \
"ds_read_b128 a[252:255], v232 offset:14592\n" \
"v_cvt_pk_bf16_f32 v51, v54, v55\n" \
"v_mfma_f32_16x16x32_bf16 v[156:159], a[44:47], v[68:71], v[156:159]\n" \
"s_cbranch_scc1 Lep_%=\n" \
"Lepr_%=:\n" \
"v_mfma_f32_16x16x32_bf16 v[64:67], a[192:195], a[0:3], v[196:199]\n" \
"v_exp_f32 v8, v8\n" \
"v_mfma_f32_16x16x32_bf16 v[64:67], a[196:199], a[4:7], v[64:67]\n" \
"ds_read_b128 a[44:47], v233 offset:15360\n" \
"v_mfma_f32_16x16x32_bf16 v[64:67], a[200:203], a[8:11], v[64:67]\n" \
"v_exp_f32 v9, v9\n" \
"v_mfma_f32_16x16x32_bf16 v[64:67], a[204:207], a[12:15], v[64:67]\n" \
"ds_read_b128 a[32:35], v233 offset:6144\n" \
"v_cvt_pk_bf16_f32 v4, v8, v9\n" \
"v_mfma_f32_16x16x32_bf16 v[80:83], a[204:207], a[28:31], v[196:199]\n" \
"s_sub_u32 s84, s84, 1\n" \
"v_exp_f32 v10, v10\n" \
"v_mfma_f32_16x16x32_bf16 v[80:83], a[200:203], a[24:27], v[80:83]\n" \
"s_cmp_eq_u32 s84, 0\n" \
"s_cselect_b32 s70, s75, 0x8000\n" \
"v_xor_b32 v232, s70, v232\n" \
"v_mfma_f32_16x16x32_bf16 v[80:83], a[196:199], a[20:23], v[80:83]\n" \
"v_exp_f32 v11, v11\n" \
"v_mfma_f32_16x16x32_bf16 v[80:83], a[192:195], a[16:19], v[80:83]\n" \
"ds_read_b128 a[36:39], v233 offset:14336\n" \
"v_cvt_pk_bf16_f32 v5, v10, v11\n" \
"v_mfma_f32_16x16x32_bf16 v[96:99], a[192:195], a[160:163], v[196:199]\n" \
"v_exp_f32 v12, v12\n" \
"v_mfma_f32_16x16x32_bf16 v[96:99], a[196:199], a[164:167], v[96:99]\n" \
"v_mfma_f32_16x16x32_bf16 v[96:99], a[200:203], a[168:171], v[96:99]\n" \
"v_exp_f32 v13, v13\n" \
"v_mfma_f32_16x16x32_bf16 v[96:99], a[204:207], a[172:175], v[96:99]\n" \
"ds_read_b128 a[40:43], v233 offset:7168\n" \
"v_cvt_pk_bf16_f32 v6, v12, v13\n" \
"v_mfma_f32_16x16x32_bf16 v[112:115], a[204:207], a[188:191], v[196:199]\n" \
"v_exp_f32 v14, v14\n" \
"v_mfma_f32_16x16x32_bf16 v[112:115], a[200:203], a[184:187], v[112:115]\n" \
"v_mfma_f32_16x16x32_bf16 v[112:115], a[196:199], a[180:183], v[112:115]\n" \
"v_exp_f32 v15, v15\n" \
"v_mfma_f32_16x16x32_bf16 v[112:115], a[192:195], a[176:179], v[112:115]\n" \
"ds_read_b128 a[204:207], v233 offset:9216\n" \
"v_cvt_pk_bf16_f32 v7, v14, v15\n" \
"v_mfma_f32_16x16x32_bf16 v[116:119], a[208:211], a[176:179], v[196:199]\n" \
"v_exp_f32 v24, v24\n" \
"v_mfma_f32_16x16x32_bf16 v[116:119], a[212:215], a[180:183], v[116:119]\n" \
"v_mfma_f32_16x16x32_bf16 v[116:119], a[216:219], a[184:187], v[116:119]\n" \
"v_exp_f32 v25, v25\n" \
"v_mfma_f32_16x16x32_bf16 v[116:119], a[220:223], a[188:191], v[116:119]\n" \
"ds_read_b128 a[200:203], v233 offset:1024\n" \
"Lpg6758_%=:\n" \
"s_cbranch_execz Lcb0_%=\n" \
"v_cvt_pk_bf16_f32 v20, v24, v25\n" \
"v_mfma_f32_16x16x32_bf16 v[100:103], a[220:223], a[172:175], v[196:199]\n" \
"v_exp_f32 v26, v26\n" \
"v_mfma_f32_16x16x32_bf16 v[100:103], a[216:219], a[168:171], v[100:103]\n" \
"v_mfma_f32_16x16x32_bf16 v[100:103], a[212:215], a[164:167], v[100:103]\n" \
"v_exp_f32 v27, v27\n" \
"v_mfma_f32_16x16x32_bf16 v[100:103], a[208:211], a[160:163], v[100:103]\n" \
"ds_read_b128 a[196:199], v233 offset:8192\n" \
"v_cvt_pk_bf16_f32 v21, v26, v27\n" \
"v_mfma_f32_16x16x32_bf16 v[84:87], a[208:211], a[16:19], v[196:199]\n" \
"v_exp_f32 v28, v28\n" \
"v_mfma_f32_16x16x32_bf16 v[84:87], a[212:215], a[20:23], v[84:87]\n" \
"v_mfma_f32_16x16x32_bf16 v[84:87], a[216:219], a[24:27], v[84:87]\n" \
"v_exp_f32 v29, v29\n" \
"v_mfma_f32_16x16x32_bf16 v[84:87], a[220:223], a[28:31], v[84:87]\n" \
"ds_read_b128 a[192:195], v233 offset:0\n" \
"v_cvt_pk_bf16_f32 v22, v28, v29\n" \
"v_mfma_f32_16x16x32_bf16 v[68:71], a[220:223], a[12:15], v[196:199]\n" \
"v_exp_f32 v30, v30\n" \
"v_mfma_f32_16x16x32_bf16 v[68:71], a[216:219], a[8:11], v[68:71]\n" \
"v_mfma_f32_16x16x32_bf16 v[68:71], a[212:215], a[4:7], v[68:71]\n" \
"s_waitcnt lgkmcnt(8)\n" \
"v_exp_f32 v31, v31\n" \
"v_mfma_f32_16x16x32_bf16 v[68:71], a[208:211], a[0:3], v[68:71]\n" \
"ds_read_b128 a[220:223], v233 offset:11264\n" \
"v_cvt_pk_bf16_f32 v23, v30, v31\n" \
"v_mfma_f32_16x16x32_bf16 v[72:75], a[224:227], a[0:3], v[196:199]\n" \
"v_exp_f32 v40, v40\n" \
"v_mfma_f32_16x16x32_bf16 v[72:75], a[228:231], a[4:7], v[72:75]\n" \
"v_mfma_f32_16x16x32_bf16 v[72:75], a[232:235], a[8:11], v[72:75]\n" \
"v_exp_f32 v41, v41\n" \
"v_mfma_f32_16x16x32_bf16 v[72:75], a[236:239], a[12:15], v[72:75]\n" \
"ds_read_b128 a[216:219], v233 offset:3072\n" \
"v_cvt_pk_bf16_f32 v36, v40, v41\n" \
"v_mfma_f32_16x16x32_bf16 v[88:91], a[236:239], a[28:31], v[196:199]\n" \
"v_exp_f32 v42, v42\n" \
"v_mfma_f32_16x16x32_bf16 v[88:91], a[232:235], a[24:27], v[88:91]\n" \
"v_mfma_f32_16x16x32_bf16 v[88:91], a[228:231], a[20:23], v[88:91]\n" \
"v_exp_f32 v43, v43\n" \
"v_mfma_f32_16x16x32_bf16 v[88:91], a[224:227], a[16:19], v[88:91]\n" \
"ds_read_b128 a[212:215], v233 offset:10240\n" \
"v_cvt_pk_bf16_f32 v37, v42, v43\n" \
"v_mfma_f32_16x16x32_bf16 v[104:107], a[224:227], a[160:163], v[196:199]\n" \
"v_exp_f32 v44, v44\n" \
"v_mfma_f32_16x16x32_bf16 v[104:107], a[228:231], a[164:167], v[104:107]\n" \
"v_mfma_f32_16x16x32_bf16 v[104:107], a[232:235], a[168:171], v[104:107]\n" \
"v_exp_f32 v45, v45\n" \
"v_mfma_f32_16x16x32_bf16 v[104:107], a[236:239], a[172:175], v[104:107]\n" \
"ds_read_b128 a[208:211], v233 offset:2048\n" \
"v_cvt_pk_bf16_f32 v38, v44, v45\n" \
"v_mfma_f32_16x16x32_bf16 v[120:123], a[236:239], a[188:191], v[196:199]\n" \
"v_exp_f32 v46, v46\n" \
"v_mfma_f32_16x16x32_bf16 v[120:123], a[232:235], a[184:187], v[120:123]\n" \
"v_mfma_f32_16x16x32_bf16 v[120:123], a[228:231], a[180:183], v[120:123]\n" \
"v_exp_f32 v47, v47\n" \
"v_mfma_f32_16x16x32_bf16 v[120:123], a[224:227], a[176:179], v[120:123]\n" \
"ds_read_b128 a[236:239], v233 offset:13312\n" \
"v_cvt_pk_bf16_f32 v39, v46, v47\n" \
"v_mfma_f32_16x16x32_bf16 v[124:127], a[240:243], a[176:179], v[196:199]\n" \
"v_exp_f32 v56, v56\n" \
"v_mfma_f32_16x16x32_bf16 v[124:127], a[244:247], a[180:183], v[124:127]\n" \
"ds_read_b128 a[232:235], v233 offset:5120\n" \
"v_mfma_f32_16x16x32_bf16 v[124:127], a[248:251], a[184:187], v[124:127]\n" \
"v_exp_f32 v57, v57\n" \
"v_mfma_f32_16x16x32_bf16 v[124:127], a[252:255], a[188:191], v[124:127]\n" \
"ds_read_b128 a[228:231], v233 offset:12288\n" \
"v_cvt_pk_bf16_f32 v52, v56, v57\n" \
"v_mfma_f32_16x16x32_bf16 v[108:111], a[252:255], a[172:175], v[196:199]\n" \
"v_exp_f32 v58, v58\n" \
"v_mfma_f32_16x16x32_bf16 v[108:111], a[248:251], a[168:171], v[108:111]\n" \
"ds_read_b128 a[224:227], v233 offset:4096\n" \
"v_mfma_f32_16x16x32_bf16 v[108:111], a[244:247], a[164:167], v[108:111]\n" \
"v_exp_f32 v59, v59\n" \
"v_mfma_f32_16x16x32_bf16 v[108:111], a[240:243], a[160:163], v[108:111]\n" \
"v_cvt_pk_bf16_f32 v53, v58, v59\n" \
"v_mfma_f32_16x16x32_bf16 v[92:95], a[240:243], a[16:19], v[196:199]\n" \
"v_exp_f32 v60, v60\n" \
"v_mfma_f32_16x16x32_bf16 v[92:95], a[244:247], a[20:23], v[92:95]\n" \
"v_mfma_f32_16x16x32_bf16 v[92:95], a[248:251], a[24:27], v[92:95]\n" \
"v_exp_f32 v61, v61\n" \
"v_mfma_f32_16x16x32_bf16 v[92:95], a[252:255], a[28:31], v[92:95]\n" \
"v_cvt_pk_bf16_f32 v54, v60, v61\n" \
"v_mfma_f32_16x16x32_bf16 v[76:79], a[252:255], a[12:15], v[196:199]\n" \
"v_exp_f32 v62, v62\n" \
"v_mfma_f32_16x16x32_bf16 v[76:79], a[248:251], a[8:11], v[76:79]\n" \
"s_cmp_lg_u32 s82, 0\n" \
"s_cbranch_scc1 Lnw_%=\n" \
"s_waitcnt vmcnt(0)\n" \
"Lnw_%=:\n" \
"v_mfma_f32_16x16x32_bf16 v[76:79], a[244:247], a[4:7], v[76:79]\n" \
"s_waitcnt lgkmcnt(0)\n" \
"v_exp_f32 v63, v63\n" \
"v_mfma_f32_16x16x32_bf16 v[76:79], a[240:243], a[0:3], v[76:79]\n" \
"s_barrier\n" \
"v_cvt_pk_bf16_f32 v55, v62, v63\n" \
"v_mfma_f32_16x16x32_bf16 v[128:131], a[192:195], v[0:3], v[128:131]\n" \
"s_add_u32 m0, s74, s79\n" \
"v_exp_f32 v64, v64\n" \
"v_mfma_f32_16x16x32_bf16 v[128:131], a[196:199], v[4:7], v[128:131]\n" \
"buffer_load_dwordx4 v234, s[36:39], s73 offen offset:0 sc1 lds\n" \
"v_mfma_f32_16x16x32_bf16 v[160:163], a[192:195], v[16:19], v[160:163]\n" \
"v_exp_f32 v65, v65\n" \
"v_mfma_f32_16x16x32_bf16 v[160:163], a[196:199], v[20:23], v[160:163]\n" \
"ds_read_b128 a[240:243], v232 offset:8448\n" \
"v_cvt_pk_bf16_f32 v64, v64, v65\n" \
"v_mfma_f32_16x16x32_bf16 a[64:67], a[192:195], v[32:35], a[64:67]\n" \
"s_add_u32 s71, s73, 17408\n" \
"v_exp_f32 v66, v66\n" \
"v_mfma_f32_16x16x32_bf16 a[64:67], a[196:199], v[36:39], a[64:67]\n" \
"buffer_load_dwordx4 v234, s[36:39], s73 offen offset:1024 sc1 lds\n" \
"v_mfma_f32_16x16x32_bf16 a[96:99], a[192:195], v[48:51], a[96:99]\n" \
"v_exp_f32 v67, v67\n" \
"v_mfma_f32_16x16x32_bf16 a[96:99], a[196:199], v[52:55], a[96:99]\n" \
"ds_read_b128 a[192:195], v232 offset:0\n" \
"v_cvt_pk_bf16_f32 v65, v66, v67\n" \
"v_mfma_f32_16x16x32_bf16 a[100:103], a[200:203], v[48:51], a[100:103]\n" \
"v_exp_f32 v68, v68\n" \
"v_mfma_f32_16x16x32_bf16 a[100:103], a[204:207], v[52:55], a[100:103]\n" \
"buffer_load_dwordx4 v234, s[36:39], s73 offen offset:2048 sc1 lds\n" \
"v_mfma_f32_16x16x32_bf16 a[68:71], a[200:203], v[32:35], a[68:71]\n" \
"v_exp_f32 v69, v69\n" \
"v_mfma_f32_16x16x32_bf16 a[68:71], a[204:207], v[36:39], a[68:71]\n" \
"ds_read_b128 a[196:199], v232 offset:2048\n" \
"v_cvt_pk_bf16_f32 v66, v68, v69\n" \
"v_smfmac_f32_16x16x64_bf16 a[48:51], v[244:247], v[0:7], v243\n" \
"buffer_load_dwordx4 v234, s[36:39], s73 offen offset:3072 sc1 lds\n" \
"v_mfma_f32_16x16x32_bf16 v[164:167], a[200:203], v[16:19], v[164:167]\n" \
"s_add_u32 m0, m0, 0x4000\n" \
"v_exp_f32 v70, v70\n" \
"v_mfma_f32_16x16x32_bf16 v[164:167], a[204:207], v[20:23], v[164:167]\n" \
"buffer_load_dwordx4 v234, s[36:39], s71 offen offset:0 sc1 lds\n" \
"v_mfma_f32_16x16x32_bf16 v[132:135], a[200:203], v[0:3], v[132:135]\n" \
"v_exp_f32 v71, v71\n" \
"v_mfma_f32_16x16x32_bf16 v[132:135], a[204:207], v[4:7], v[132:135]\n" \
"ds_read_b128 a[200:203], v232 offset:4096\n" \
"v_cvt_pk_bf16_f32 v67, v70, v71\n" \
"v_mfma_f32_16x16x32_bf16 v[136:139], a[208:211], v[0:3], v[136:139]\n" \
"v_exp_f32 v80, v80\n" \
"v_mfma_f32_16x16x32_bf16 v[136:139], a[212:215], v[4:7], v[136:139]\n" \
"buffer_load_dwordx4 v234, s[36:39], s71 offen offset:1024 sc1 lds\n" \
"v_mfma_f32_16x16x32_bf16 v[168:171], a[208:211], v[16:19], v[168:171]\n" \
"v_exp_f32 v81, v81\n" \
"v_mfma_f32_16x16x32_bf16 v[168:171], a[212:215], v[20:23], v[168:171]\n" \
"ds_read_b128 a[204:207], v232 offset:6144\n" \
"v_cvt_pk_bf16_f32 v80, v80, v81\n" \
"v_mfma_f32_16x16x32_bf16 a[72:75], a[208:211], v[32:35], a[72:75]\n" \
"v_exp_f32 v82, v82\n" \
"v_mfma_f32_16x16x32_bf16 a[72:75], a[212:215], v[36:39], a[72:75]\n" \
"buffer_load_dwordx4 v234, s[36:39], s71 offen offset:2048 sc1 lds\n" \
"v_mfma_f32_16x16x32_bf16 a[104:107], a[208:211], v[48:51], a[104:107]\n" \
"v_exp_f32 v83, v83\n" \
"v_mfma_f32_16x16x32_bf16 a[104:107], a[212:215], v[52:55], a[104:107]\n" \
"ds_read_b128 a[208:211], v232 offset:256\n" \
"v_cvt_pk_bf16_f32 v81, v82, v83\n" \
"v_mfma_f32_16x16x32_bf16 a[108:111], a[216:219], v[48:51], a[108:111]\n" \
"v_exp_f32 v84, v84\n" \
"v_mfma_f32_16x16x32_bf16 a[108:111], a[220:223], v[52:55], a[108:111]\n" \
"buffer_load_dwordx4 v234, s[36:39], s71 offen offset:3072 sc1 lds\n" \
"v_mfma_f32_16x16x32_bf16 a[76:79], a[216:219], v[32:35], a[76:79]\n" \
"v_exp_f32 v85, v85\n" \
"v_mfma_f32_16x16x32_bf16 a[76:79], a[220:223], v[36:39], a[76:79]\n" \
"s_add_u32 m0, s77, s80\n" \
"ds_read_b128 a[212:215], v232 offset:2304\n" \
"v_cvt_pk_bf16_f32 v82, v84, v85\n" \
"v_smfmac_f32_16x16x64_bf16 a[52:55], v[244:247], v[16:23], v243\n" \
"buffer_load_dwordx4 v234, s[40:43], s76 offen offset:0 sc1 lds\n" \
"v_mfma_f32_16x16x32_bf16 v[172:175], a[216:219], v[16:19], v[172:175]\n" \
"s_add_u32 s71, s76, 17408\n" \
"v_exp_f32 v86, v86\n" \
"v_mfma_f32_16x16x32_bf16 v[172:175], a[220:223], v[20:23], v[172:175]\n" \
"buffer_load_dwordx4 v234, s[40:43], s76 offen offset:1024 sc1 lds\n" \
"v_mfma_f32_16x16x32_bf16 v[140:143], a[216:219], v[0:3], v[140:143]\n" \
"v_exp_f32 v87, v87\n" \
"v_mfma_f32_16x16x32_bf16 v[140:143], a[220:223], v[4:7], v[140:143]\n" \
"ds_read_b128 a[216:219], v232 offset:4352\n" \
"v_cvt_pk_bf16_f32 v83, v86, v87\n" \
"v_mfma_f32_16x16x32_bf16 v[144:147], a[224:227], v[0:3], v[144:147]\n" \
"v_exp_f32 v96, v96\n" \
"v_mfma_f32_16x16x32_bf16 v[144:147], a[228:231], v[4:7], v[144:147]\n" \
"buffer_load_dwordx4 v234, s[40:43], s76 offen offset:2048 sc1 lds\n" \
"v_mfma_f32_16x16x32_bf16 v[176:179], a[224:227], v[16:19], v[176:179]\n" \
"v_exp_f32 v97, v97\n" \
"v_mfma_f32_16x16x32_bf16 v[176:179], a[228:231], v[20:23], v[176:179]\n" \
"ds_read_b128 a[220:223], v232 offset:6400\n" \
"v_cvt_pk_bf16_f32 v96, v96, v97\n" \
"v_mfma_f32_16x16x32_bf16 a[80:83], a[224:227], v[32:35], a[80:83]\n" \
"v_exp_f32 v98, v98\n" \
"v_mfma_f32_16x16x32_bf16 a[80:83], a[228:231], v[36:39], a[80:83]\n" \
"buffer_load_dwordx4 v234, s[40:43], s76 offen offset:3072 sc1 lds\n" \
"v_mfma_f32_16x16x32_bf16 a[112:115], a[224:227], v[48:51], a[112:115]\n" \
"v_exp_f32 v99, v99\n" \
"v_mfma_f32_16x16x32_bf16 a[112:115], a[228:231], v[52:55], a[112:115]\n" \
"ds_read_b128 a[224:227], v232 offset:8192\n" \
"v_cvt_pk_bf16_f32 v97, v98, v99\n" \
"v_mfma_f32_16x16x32_bf16 a[116:119], a[232:235], v[48:51], a[116:119]\n" \
"s_add_u32 m0, m0, 0x4000\n" \
"v_exp_f32 v100, v100\n" \
"v_mfma_f32_16x16x32_bf16 a[116:119], a[236:239], v[52:55], a[116:119]\n" \
"buffer_load_dwordx4 v234, s[40:43], s71 offen offset:0 sc1 lds\n" \
"v_mfma_f32_16x16x32_bf16 a[84:87], a[232:235], v[32:35], a[84:87]\n" \
"v_exp_f32 v101, v101\n" \
"v_mfma_f32_16x16x32_bf16 a[84:87], a[236:239], v[36:39], a[84:87]\n" \
"ds_read_b128 a[228:231], v232 offset:10240\n" \
"v_cvt_pk_bf16_f32 v98, v100, v101\n" \
"v_smfmac_f32_16x16x64_bf16 v[224:227], v[244:247], v[32:39], v243\n" \
"buffer_load_dwordx4 v234, s[40:43], s71 offen offset:1024 sc1 lds\n" \
"v_mfma_f32_16x16x32_bf16 v[180:183], a[232:235], v[16:19], v[180:183]\n" \
"v_exp_f32 v102, v102\n" \
"v_mfma_f32_16x16x32_bf16 v[180:183], a[236:239], v[20:23], v[180:183]\n" \
"buffer_load_dwordx4 v234, s[40:43], s71 offen offset:2048 sc1 lds\n" \
"v_mfma_f32_16x16x32_bf16 v[148:151], a[232:235], v[0:3], v[148:151]\n" \
"v_exp_f32 v103, v103\n" \
"v_mfma_f32_16x16x32_bf16 v[148:151], a[236:239], v[4:7], v[148:151]\n" \
"ds_read_b128 a[232:235], v232 offset:12288\n" \
"v_cvt_pk_bf16_f32 v99, v102, v103\n" \
"v_mfma_f32_16x16x32_bf16 v[152:155], a[32:35], v[0:3], v[152:155]\n" \
"v_exp_f32 v112, v112\n" \
"v_mfma_f32_16x16x32_bf16 v[152:155], a[36:39], v[4:7], v[152:155]\n" \
"buffer_load_dwordx4 v234, s[40:43], s71 offen offset:3072 sc1 lds\n" \
"v_mfma_f32_16x16x32_bf16 v[184:187], a[32:35], v[16:19], v[184:187]\n" \
"v_exp_f32 v113, v113\n" \
"v_mfma_f32_16x16x32_bf16 v[184:187], a[36:39], v[20:23], v[184:187]\n" \
"ds_read_b128 a[236:239], v232 offset:14336\n" \
"v_cvt_pk_bf16_f32 v112, v112, v113\n" \
"v_mfma_f32_16x16x32_bf16 a[88:91], a[32:35], v[32:35], a[88:91]\n" \
"v_exp_f32 v114, v114\n" \
"v_mfma_f32_16x16x32_bf16 a[88:91], a[36:39], v[36:39], a[88:91]\n" \
"buffer_load_dword v239, v242, s[56:59], s95 offen sc1\n" \
"v_mfma_f32_16x16x32_bf16 a[120:123], a[32:35], v[48:51], a[120:123]\n" \
"s_mov_b32 s69, s92\n" \
"v_exp_f32 v115, v115\n" \
"v_mfma_f32_16x16x32_bf16 a[120:123], a[36:39], v[52:55], a[120:123]\n" \
"s_mov_b32 s76, s73\n" \
"ds_read_b128 a[244:247], v232 offset:10496\n" \
"v_cvt_pk_bf16_f32 v113, v114, v115\n" \
"v_mfma_f32_16x16x32_bf16 a[124:127], a[40:43], v[48:51], a[124:127]\n" \
"s_add_u32 s73, s73, 34816\n" \
"v_exp_f32 v116, v116\n" \
"v_mfma_f32_16x16x32_bf16 a[124:127], a[44:47], v[52:55], a[124:127]\n" \
"s_xor_b32 s74, s74, 0x8000\n" \
"s_xor_b32 s77, s77, 0x8000\n" \
"v_xor_b32 v233, 0x8000, v233\n" \
"v_mfma_f32_16x16x32_bf16 a[92:95], a[40:43], v[32:35], a[92:95]\n" \
"s_mov_b32 s82, 0\n" \
"v_exp_f32 v117, v117\n" \
"v_mfma_f32_16x16x32_bf16 a[92:95], a[44:47], v[36:39], a[92:95]\n" \
"ds_read_b128 a[248:251], v232 offset:12544\n" \
"v_cvt_pk_bf16_f32 v114, v116, v117\n" \
"v_smfmac_f32_16x16x64_bf16 v[228:231], v[244:247], v[48:55], v243\n" \
"v_mfma_f32_16x16x32_bf16 v[188:191], a[40:43], v[16:19], v[188:191]\n" \
"v_exp_f32 v118, v118\n" \
"v_mfma_f32_16x16x32_bf16 v[188:191], a[44:47], v[20:23], v[188:191]\n" \
"s_cmp_lg_u32 s84, 0\n" \
"v_exp_f32 v119, v119\n" \
"v_mfma_f32_16x16x32_bf16 v[156:159], a[40:43], v[0:3], v[156:159]\n" \
"s_waitcnt lgkmcnt(2)\n" \
"ds_read_b128 a[252:255], v232 offset:14592\n" \
"v_cvt_pk_bf16_f32 v115, v118, v119\n" \
"v_mfma_f32_16x16x32_bf16 v[156:159], a[44:47], v[4:7], v[156:159]\n" \
"s_cbranch_scc1 Lloop2_%=\n" \
"Lg_%=:\n" \
"Lpg7020_%=:\n" \
"s_cbranch_execz Lcb0_%=\n" \
"v_mfma_f32_16x16x32_bf16 v[32:35], a[192:195], a[160:163], v[208:211]\n" \
"s_mov_b64 s[44:45], s[20:21]\n" \
"s_memrealtime s[14:15]\n" \
"v_exp_f32 v72, v72\n" \
"v_mfma_f32_16x16x32_bf16 v[32:35], a[196:199], a[164:167], v[32:35]\n" \
"s_and_b32 s45, s45, 0xffff\n" \
"ds_read_b128 a[32:35], v233 offset:6144\n" \
"ds_read_b128 a[36:39], v233 offset:14336\n" \
"v_mfma_f32_16x16x32_bf16 v[32:35], a[200:203], a[168:171], v[32:35]\n" \
"s_mov_b32 s46, 4194304\n" \
"v_exp_f32 v73, v73\n" \
"v_mfma_f32_16x16x32_bf16 v[32:35], a[204:207], a[172:175], v[32:35]\n" \
"ds_read_b128 a[40:43], v233 offset:7168\n" \
"ds_read_b128 a[44:47], v233 offset:15360\n" \
"global_load_dword v253, v254, s[22:23]\n" \
"v_mfma_f32_16x16x32_bf16 v[48:51], a[204:207], a[188:191], v[196:199]\n" \
"s_mov_b32 s47, 0x20000\n" \
"v_exp_f32 v74, v74\n" \
"v_mfma_f32_16x16x32_bf16 v[48:51], a[200:203], a[184:187], v[48:51]\n" \
"s_mov_b64 s[48:49], s[22:23]\n" \
"v_exp_f32 v75, v75\n" \
"v_mfma_f32_16x16x32_bf16 v[48:51], a[196:199], a[180:183], v[48:51]\n" \
"s_and_b32 s49, s49, 0xffff\n" \
"v_exp_f32 v76, v76\n" \
"v_mfma_f32_16x16x32_bf16 v[48:51], a[192:195], a[176:179], v[48:51]\n" \
"s_mov_b32 s50, 6291456\n" \
"ds_read_b128 a[200:203], v233 offset:1024\n" \
"ds_read_b128 a[204:207], v233 offset:9216\n" \
"v_mfma_f32_16x16x32_bf16 v[52:55], a[208:211], a[176:179], v[196:199]\n" \
"s_mov_b32 s51, 0x20000\n" \
"v_cvt_pk_bf16_f32 v68, v72, v73\n" \
"v_cvt_pk_bf16_f32 v69, v74, v75\n" \
"v_mfma_f32_16x16x32_bf16 v[52:55], a[212:215], a[180:183], v[52:55]\n" \
"s_bfe_u32 s94, %1, 0x20001\n" \
"v_exp_f32 v77, v77\n" \
"v_mfma_f32_16x16x32_bf16 v[52:55], a[216:219], a[184:187], v[52:55]\n" \
"s_lshl_b32 s94, s94, 2\n" \
"v_exp_f32 v78, v78\n" \
"v_mfma_f32_16x16x32_bf16 v[52:55], a[220:223], a[188:191], v[52:55]\n" \
"s_lshr_b32 s70, %1, 6\n" \
"ds_read_b128 a[192:195], v233 offset:0\n" \
"ds_read_b128 a[196:199], v233 offset:8192\n" \
"v_mfma_f32_16x16x32_bf16 v[36:39], a[220:223], a[172:175], v[212:215]\n" \
"s_add_u32 s94, s94, s70\n" \
"v_exp_f32 v79, v79\n" \
"v_mfma_f32_16x16x32_bf16 v[36:39], a[216:219], a[168:171], v[36:39]\n" \
"s_lshr_b32 s71, s22, 7\n" \
"v_exp_f32 v88, v88\n" \
"v_mfma_f32_16x16x32_bf16 v[36:39], a[212:215], a[164:167], v[36:39]\n" \
"s_sub_u32 s71, s63, s71\n" \
"v_cvt_pk_bf16_f32 v70, v76, v77\n" \
"v_cvt_pk_bf16_f32 v71, v78, v79\n" \
"v_mfma_f32_16x16x32_bf16 v[36:39], a[208:211], a[160:163], v[36:39]\n" \
"s_add_u32 s71, s71, 6\n" \
"ds_read_b128 a[216:219], v233 offset:3072\n" \
"ds_read_b128 a[220:223], v233 offset:11264\n" \
"v_mfma_f32_16x16x32_bf16 v[40:43], a[224:227], a[160:163], v[216:219]\n" \
"s_and_b32 s71, s71, 7\n" \
"v_exp_f32 v89, v89\n" \
"v_mfma_f32_16x16x32_bf16 v[40:43], a[228:231], a[164:167], v[40:43]\n" \
"s_and_b32 s70, %1, 1\n" \
"v_exp_f32 v90, v90\n" \
"v_mfma_f32_16x16x32_bf16 v[40:43], a[232:235], a[168:171], v[40:43]\n" \
"s_lshl_b32 s70, s70, 3\n" \
"v_exp_f32 v91, v91\n" \
"v_mfma_f32_16x16x32_bf16 v[40:43], a[236:239], a[172:175], v[40:43]\n" \
"s_add_u32 s70, s70, s71\n" \
"ds_read_b128 a[208:211], v233 offset:2048\n" \
"ds_read_b128 a[212:215], v233 offset:10240\n" \
"v_mfma_f32_16x16x32_bf16 v[56:59], a[236:239], a[188:191], v[196:199]\n" \
"s_lshl_b32 s78, s70, 7\n" \
"v_exp_f32 v92, v92\n" \
"v_mfma_f32_16x16x32_bf16 v[56:59], a[232:235], a[184:187], v[56:59]\n" \
"s_lshr_b32 s80, %1, 6\n" \
"v_cvt_pk_bf16_f32 v84, v88, v89\n" \
"v_cvt_pk_bf16_f32 v85, v90, v91\n" \
"v_mfma_f32_16x16x32_bf16 v[56:59], a[228:231], a[180:183], v[56:59]\n" \
"s_xor_b32 s80, s80, s63\n" \
"v_exp_f32 v93, v93\n" \
"v_mfma_f32_16x16x32_bf16 v[56:59], a[224:227], a[176:179], v[56:59]\n" \
"s_and_b32 s80, s80, 0\n" \
"ds_read_b128 a[232:235], v233 offset:5120\n" \
"ds_read_b128 a[236:239], v233 offset:13312\n" \
"v_mfma_f32_16x16x32_bf16 v[60:63], a[240:243], a[176:179], v[196:199]\n" \
"s_waitcnt lgkmcnt(2)\n" \
"s_lshl_b32 s93, s70, 12\n" \
"v_exp_f32 v94, v94\n" \
"v_mfma_f32_16x16x32_bf16 v[60:63], a[244:247], a[180:183], v[60:63]\n" \
"s_mul_i32 s91, s61, 6144\n" \
"v_exp_f32 v95, v95\n" \
"v_mfma_f32_16x16x32_bf16 v[60:63], a[248:251], a[184:187], v[60:63]\n" \
"s_add_u32 s91, s91, %3\n" \
"v_cvt_pk_bf16_f32 v86, v92, v93\n" \
"v_mfma_f32_16x16x32_bf16 v[60:63], a[252:255], a[188:191], v[60:63]\n" \
"s_lshl_b32 s71, s80, 7\n" \
"ds_read_b128 a[224:227], v233 offset:4096\n" \
"ds_read_b128 a[228:231], v233 offset:12288\n" \
"v_mfma_f32_16x16x32_bf16 v[44:47], a[252:255], a[172:175], v[220:223]\n" \
"s_lshl_b32 s75, s94, 18\n" \
"v_cvt_pk_bf16_f32 v87, v94, v95\n" \
"v_mfma_f32_16x16x32_bf16 v[44:47], a[248:251], a[168:171], v[44:47]\n" \
"s_lshl_b32 s70, s61, 16\n" \
"v_exp_f32 v104, v104\n" \
"v_mfma_f32_16x16x32_bf16 v[44:47], a[244:247], a[164:167], v[44:47]\n" \
"s_add_u32 s73, s75, s70\n" \
"v_accvgpr_read_b32 v197, a56\n" \
"v_accvgpr_read_b32 v192, a57\n" \
"v_mfma_f32_16x16x32_bf16 v[44:47], a[240:243], a[160:163], v[44:47]\n" \
"s_add_u32 s73, s73, s71\n" \
"v_accvgpr_read_b32 v196, a58\n" \
"v_add_u32 v198, 1024, v197\n" \
"v_mfma_f32_16x16x32_bf16 v[128:131], a[192:195], v[64:67], v[128:131]\n" \
"s_max_u32 s75, s75, 32768\n" \
"v_add_u32 v199, 2048, v197\n" \
"v_add_u32 v193, 31744, v192\n" \
"v_mfma_f32_16x16x32_bf16 v[128:131], a[196:199], v[68:71], v[128:131]\n" \
"s_sub_u32 s75, s75, 32768\n" \
"ds_read_b128 a[0:3], v232 offset:16384\n" \
"ds_read_b128 a[4:7], v232 offset:18432\n" \
"v_mfma_f32_16x16x32_bf16 v[160:163], a[192:195], v[80:83], v[160:163]\n" \
"s_add_u32 s75, s75, s71\n" \
"s_lshl_b32 s89, s61, 10\n" \
"v_mfma_f32_16x16x32_bf16 v[160:163], a[196:199], v[84:87], v[160:163]\n" \
"s_mul_i32 s76, s80, 393216\n" \
"s_add_u32 s89, s89, %3\n" \
"v_exp_f32 v105, v105\n" \
"v_mfma_f32_16x16x32_bf16 v[164:167], a[200:203], v[80:83], v[164:167]\n" \
"s_add_u32 s76, s76, s78\n" \
"ds_read_b128 a[8:11], v232 offset:20480\n" \
"ds_read_b128 a[12:15], v232 offset:22528\n" \
"v_mfma_f32_16x16x32_bf16 v[164:167], a[204:207], v[84:87], v[164:167]\n" \
"s_add_u32 m0, s91, 139264\n" \
"s_add_u32 s89, s89, 16384\n" \
"v_exp_f32 v106, v106\n" \
"v_mfma_f32_16x16x32_bf16 v[132:135], a[200:203], v[64:67], v[132:135]\n" \
"buffer_load_dwordx4 v197, s[48:51], s76 offen offset:0 lds\n" \
"v_mfma_f32_16x16x32_bf16 v[132:135], a[204:207], v[68:71], v[132:135]\n" \
"s_and_b32 s70, s61, 1\n" \
"ds_read_b128 a[16:19], v232 offset:16640\n" \
"ds_read_b128 a[20:23], v232 offset:18688\n" \
"v_smfmac_f32_16x16x64_bf16 a[48:51], v[244:247], v[64:71], v243\n" \
"s_add_u32 s77, s76, 24576\n" \
"v_cvt_pk_bf16_f32 v100, v104, v105\n" \
"v_mfma_f32_16x16x32_bf16 v[136:139], a[208:211], v[64:67], v[136:139]\n" \
"buffer_load_dwordx4 v198, s[48:51], s76 offen offset:1024 lds\n" \
"v_mfma_f32_16x16x32_bf16 v[136:139], a[212:215], v[68:71], v[136:139]\n" \
"s_lshl_b32 s70, s70, 11\n" \
"ds_read_b128 a[24:27], v232 offset:20736\n" \
"ds_read_b128 a[28:31], v232 offset:22784\n" \
"v_mfma_f32_16x16x32_bf16 v[168:171], a[208:211], v[80:83], v[168:171]\n" \
"s_xor_b32 s80, s80, 0x3\n" \
"v_exp_f32 v107, v107\n" \
"v_mfma_f32_16x16x32_bf16 v[168:171], a[212:215], v[84:87], v[168:171]\n" \
"buffer_load_dwordx4 v199, s[48:51], s76 offen offset:2048 lds\n" \
"v_mfma_f32_16x16x32_bf16 v[172:175], a[216:219], v[80:83], v[172:175]\n" \
"s_add_u32 s93, s93, s70\n" \
"ds_read_b128 v[0:3], v232 offset:24576\n" \
"ds_read_b128 v[4:7], v232 offset:26624\n" \
"v_mfma_f32_16x16x32_bf16 v[172:175], a[220:223], v[84:87], v[172:175]\n" \
"s_add_u32 m0, m0, 3072\n" \
"v_exp_f32 v108, v108\n" \
"v_mfma_f32_16x16x32_bf16 v[140:143], a[216:219], v[64:67], v[140:143]\n" \
"buffer_load_dwordx4 v197, s[48:51], s77 offen offset:0 lds\n" \
"v_mfma_f32_16x16x32_bf16 v[140:143], a[220:223], v[68:71], v[140:143]\n" \
"s_add_u32 s93, s93, 52740096\n" \
"ds_read_b128 v[8:11], v232 offset:28672\n" \
"ds_read_b128 v[12:15], v232 offset:30720\n" \
"v_exp_f32 v109, v109\n" \
"v_smfmac_f32_16x16x64_bf16 a[52:55], v[244:247], v[80:87], v243\n" \
"s_waitcnt lgkmcnt(4)\n" \
"s_cmp_eq_u32 s94, 0\n" \
"s_cselect_b32 s92, -1, 0\n" \
"v_cvt_pk_bf16_f32 v101, v106, v107\n" \
"v_mfma_f32_16x16x32_bf16 v[144:147], a[224:227], v[64:67], v[144:147]\n" \
"buffer_load_dwordx4 v198, s[48:51], s77 offen offset:1024 lds\n" \
"v_mfma_f32_16x16x32_bf16 v[144:147], a[228:231], v[68:71], v[144:147]\n" \
"s_mul_i32 s70, s94, 278528\n" \
"ds_read_b128 v[200:203], v232 offset:24832\n" \
"ds_read_b128 v[204:207], v232 offset:26880\n" \
"v_mfma_f32_16x16x32_bf16 v[176:179], a[224:227], v[80:83], v[176:179]\n" \
"s_add_u32 s93, s93, s70\n" \
"v_exp_f32 v110, v110\n" \
"v_mfma_f32_16x16x32_bf16 v[176:179], a[228:231], v[84:87], v[176:179]\n" \
"buffer_load_dwordx4 v199, s[48:51], s77 offen offset:2048 lds\n" \
"v_mfma_f32_16x16x32_bf16 v[180:183], a[232:235], v[80:83], v[180:183]\n" \
"s_lshr_b32 s70, s61, 1\n" \
"ds_read_b128 a[240:243], v232 offset:28928\n" \
"ds_read_b128 a[244:247], v232 offset:30976\n" \
"v_mfma_f32_16x16x32_bf16 v[180:183], a[236:239], v[84:87], v[180:183]\n" \
"s_lshl_b32 s70, s70, 8\n" \
"v_exp_f32 v111, v111\n" \
"v_mfma_f32_16x16x32_bf16 v[148:151], a[232:235], v[64:67], v[148:151]\n" \
"s_add_u32 s93, s93, s70\n" \
"v_exp_f32 v120, v120\n" \
"v_mfma_f32_16x16x32_bf16 v[148:151], a[236:239], v[68:71], v[148:151]\n" \
"v_cvt_pk_bf16_f32 v102, v108, v109\n" \
"v_cvt_pk_bf16_f32 v103, v110, v111\n" \
"v_exp_f32 v121, v121\n" \
"v_mfma_f32_16x16x32_bf16 v[152:155], a[32:35], v[64:67], v[152:155]\n" \
"v_add_u32 v233, 0x10000, v235\n" \
"s_lshl_b32 s72, s61, 14\n" \
"v_mfma_f32_16x16x32_bf16 v[152:155], a[36:39], v[68:71], v[152:155]\n" \
"v_add_u32 v232, s72, v235\n" \
"v_mfma_f32_16x16x32_bf16 v[184:187], a[32:35], v[80:83], v[184:187]\n" \
"v_exp_f32 v122, v122\n" \
"v_mfma_f32_16x16x32_bf16 v[184:187], a[36:39], v[84:87], v[184:187]\n" \
"v_exp_f32 v123, v123\n" \
"v_mfma_f32_16x16x32_bf16 v[188:191], a[40:43], v[80:83], v[188:191]\n" \
"v_exp_f32 v124, v124\n" \
"v_mfma_f32_16x16x32_bf16 v[188:191], a[44:47], v[84:87], v[188:191]\n" \
"v_cvt_pk_bf16_f32 v116, v120, v121\n" \
"v_cvt_pk_bf16_f32 v117, v122, v123\n" \
"v_mfma_f32_16x16x32_bf16 v[156:159], a[40:43], v[64:67], v[156:159]\n" \
"v_exp_f32 v125, v125\n" \
"v_mfma_f32_16x16x32_bf16 v[156:159], a[44:47], v[68:71], v[156:159]\n" \
"s_cmp_eq_u32 s63, 7\n" \
"s_waitcnt vmcnt(7) lgkmcnt(0)\n" \
"s_barrier\n" \
"s_cbranch_scc1 Lep_%=\n" \
"Lepg_%=:\n" \
"v_mfma_f32_16x16x32_bf16 v[16:19], a[0:3], a[176:179], v[208:211]\n" \
"s_add_u32 m0, s79, 110592\n" \
"ds_read_b128 v[64:67], v233 offset:16384\n" \
"ds_read_b128 v[68:71], v233 offset:24576\n" \
"v_mfma_f32_16x16x32_bf16 v[16:19], a[4:7], a[180:183], v[16:19]\n" \
"buffer_load_dwordx4 v192, s[44:47], s73 offen offset:0 lds\n" \
"v_mfma_f32_16x16x32_bf16 v[16:19], a[8:11], a[184:187], v[16:19]\n" \
"ds_read_b128 v[72:75], v233 offset:17408\n" \
"ds_read_b128 v[76:79], v233 offset:25600\n" \
"v_mfma_f32_16x16x32_bf16 v[16:19], a[12:15], a[188:191], v[16:19]\n" \
"ds_write_b128 v232, v[128:131] offset:0\n" \
"v_exp_f32 v126, v126\n" \
"v_mfma_f32_16x16x32_bf16 v[20:23], a[28:31], a[188:191], v[212:215]\n" \
"ds_read_b128 v[80:83], v233 offset:18432\n" \
"ds_read_b128 v[84:87], v233 offset:26624\n" \
"v_mfma_f32_16x16x32_bf16 v[20:23], a[24:27], a[184:187], v[20:23]\n" \
"buffer_load_dwordx4 v193, s[44:47], s73 offen offset:1024 lds\n" \
"v_mfma_f32_16x16x32_bf16 v[20:23], a[20:23], a[180:183], v[20:23]\n" \
"ds_write_b128 v232, v[132:135] offset:1024\n" \
"v_xor_b32 v248, 0x4000, v232\n" \
"v_mfma_f32_16x16x32_bf16 v[20:23], a[16:19], a[176:179], v[20:23]\n" \
"ds_read_b128 v[88:91], v233 offset:19456\n" \
"ds_read_b128 v[92:95], v233 offset:27648\n" \
"v_mfma_f32_16x16x32_bf16 v[24:27], v[0:3], a[176:179], v[216:219]\n" \
"ds_read_b128 a[248:251], v233 offset:7168\n" \
"ds_read_b128 a[252:255], v233 offset:15360\n" \
"v_mfma_f32_16x16x32_bf16 v[24:27], v[4:7], a[180:183], v[24:27]\n" \
"buffer_load_dwordx4 v192, s[44:47], s73 offen offset:2048 lds\n" \
"v_mfma_f32_16x16x32_bf16 v[24:27], v[8:11], a[184:187], v[24:27]\n" \
"ds_write_b128 v232, v[136:139] offset:2048\n" \
"v_mfma_f32_16x16x32_bf16 v[24:27], v[12:15], a[188:191], v[24:27]\n" \
"v_exp_f32 v127, v127\n" \
"v_cvt_pk_bf16_f32 v118, v124, v125\n" \
"v_mfma_f32_16x16x32_bf16 v[28:31], a[244:247], a[188:191], v[220:223]\n" \
"v_cvt_pk_bf16_f32 v119, v126, v127\n" \
"v_lshrrev_b32 v249, 1, v234\n" \
"v_mfma_f32_16x16x32_bf16 v[28:31], a[240:243], a[184:187], v[28:31]\n" \
"buffer_load_dwordx4 v193, s[44:47], s73 offen offset:3072 lds\n" \
"v_mfma_f32_16x16x32_bf16 v[28:31], v[204:207], a[180:183], v[28:31]\n" \
"ds_write_b128 v232, v[140:143] offset:3072\n" \
"s_lshl_b32 s72, s61, 9\n" \
"s_add_u32 s72, s72, 0x20000\n" \
"v_exp_f32 v32, v32\n" \
"v_exp_f32 v33, v33\n" \
"v_mfma_f32_16x16x32_bf16 v[28:31], v[200:203], a[176:179], v[28:31]\n" \
"s_add_u32 m0, s89, 110592\n" \
"v_exp_f32 v34, v34\n" \
"v_exp_f32 v35, v35\n" \
"v_mfma_f32_16x16x32_bf16 a[64:67], a[192:195], v[96:99], a[64:67]\n" \
"s_xor_b32 s73, s73, 0x180\n" \
"v_exp_f32 v36, v36\n" \
"v_mfma_f32_16x16x32_bf16 a[64:67], a[196:199], v[100:103], a[64:67]\n" \
"buffer_load_dwordx4 v196, s[44:47], s75 offen lds\n" \
"s_add_u32 s72, s72, %3\n" \
"v_cvt_pk_bf16_f32 v32, v32, v33\n" \
"v_exp_f32 v37, v37\n" \
"v_add_u32 v249, s72, v249\n" \
"v_mfma_f32_16x16x32_bf16 a[96:99], a[192:195], v[112:115], a[96:99]\n" \
"ds_write_b128 v232, v[144:147] offset:4096\n" \
"v_mfma_f32_16x16x32_bf16 a[96:99], a[196:199], v[116:119], a[96:99]\n" \
"ds_read_b128 a[192:195], v233 offset:0\n" \
"ds_read_b128 a[196:199], v233 offset:8192\n" \
"v_exp_f32 v38, v38\n" \
"v_cvt_pk_bf16_f32 v33, v34, v35\n" \
"v_accvgpr_read_b32 v250, a48\n" \
"v_mfma_f32_16x16x32_bf16 a[100:103], a[200:203], v[112:115], a[100:103]\n" \
"s_xor_b32 s75, s75, 0x180\n" \
"v_exp_f32 v39, v39\n" \
"v_mfma_f32_16x16x32_bf16 a[100:103], a[204:207], v[116:119], a[100:103]\n" \
"v_exp_f32 v48, v48\n" \
"v_cvt_pk_bf16_f32 v34, v36, v37\n" \
"v_exp_f32 v49, v49\n" \
"v_mfma_f32_16x16x32_bf16 a[68:71], a[200:203], v[96:99], a[68:71]\n" \
"ds_write_b128 v232, v[148:151] offset:5120\n" \
"v_mfma_f32_16x16x32_bf16 a[68:71], a[204:207], v[100:103], a[68:71]\n" \
"ds_read_b128 a[200:203], v233 offset:1024\n" \
"ds_read_b128 a[204:207], v233 offset:9216\n" \
"v_exp_f32 v50, v50\n" \
"v_cvt_pk_bf16_f32 v35, v38, v39\n" \
"v_smfmac_f32_16x16x64_bf16 v[224:227], v[244:247], v[96:103], v243\n" \
"v_cvt_pk_bf16_f32 v48, v48, v49\n" \
"v_mfma_f32_16x16x32_bf16 a[72:75], a[208:211], v[96:99], a[72:75]\n" \
"v_exp_f32 v51, v51\n" \
"v_mfma_f32_16x16x32_bf16 a[72:75], a[212:215], v[100:103], a[72:75]\n" \
"ds_write_b128 v232, v[152:155] offset:6144\n" \
"v_exp_f32 v52, v52\n" \
"v_exp_f32 v53, v53\n" \
"v_accvgpr_read_b32 v240, a49\n" \
"v_mfma_f32_16x16x32_bf16 a[104:107], a[208:211], v[112:115], a[104:107]\n" \
"v_exp_f32 v54, v54\n" \
"v_mfma_f32_16x16x32_bf16 a[104:107], a[212:215], v[116:119], a[104:107]\n" \
"ds_read_b128 a[208:211], v233 offset:2048\n" \
"ds_read_b128 a[212:215], v233 offset:10240\n" \
"v_cvt_pk_bf16_f32 v49, v50, v51\n" \
"v_exp_f32 v55, v55\n" \
"v_mfma_f32_16x16x32_bf16 a[108:111], a[216:219], v[112:115], a[108:111]\n" \
"v_exp_f32 v40, v40\n" \
"v_mfma_f32_16x16x32_bf16 a[108:111], a[220:223], v[116:119], a[108:111]\n" \
"ds_write_b128 v232, v[156:159] offset:7168\n" \
"v_cvt_pk_bf16_f32 v50, v52, v53\n" \
"v_exp_f32 v41, v41\n" \
"v_mfma_f32_16x16x32_bf16 a[76:79], a[216:219], v[96:99], a[76:79]\n" \
"v_exp_f32 v42, v42\n" \
"v_mfma_f32_16x16x32_bf16 a[76:79], a[220:223], v[100:103], a[76:79]\n" \
"ds_read_b128 a[216:219], v233 offset:3072\n" \
"ds_read_b128 a[220:223], v233 offset:11264\n" \
"v_cvt_pk_bf16_f32 v51, v54, v55\n" \
"v_exp_f32 v43, v43\n" \
"v_smfmac_f32_16x16x64_bf16 v[228:231], v[244:247], v[112:119], v243\n" \
"ds_write_b128 v232, v[160:163] offset:8192\n" \
"v_exp_f32 v44, v44\n" \
"v_add_f32 v250, v250, v240\n" \
"v_mfma_f32_16x16x32_bf16 a[80:83], a[224:227], v[96:99], a[80:83]\n" \
"v_cvt_pk_bf16_f32 v36, v40, v41\n" \
"v_cvt_pk_bf16_f32 v37, v42, v43\n" \
"v_mfma_f32_16x16x32_bf16 a[80:83], a[228:231], v[100:103], a[80:83]\n" \
"v_exp_f32 v45, v45\n" \
"v_exp_f32 v46, v46\n" \
"v_mfma_f32_16x16x32_bf16 a[112:115], a[224:227], v[112:115], a[112:115]\n" \
"Lpg7360_%=:\n" \
"s_cbranch_execz Lcb0_%=\n" \
"v_exp_f32 v47, v47\n" \
"v_mfma_f32_16x16x32_bf16 a[112:115], a[228:231], v[116:119], a[112:115]\n" \
"ds_write_b128 v232, v[164:167] offset:9216\n" \
"v_exp_f32 v56, v56\n" \
"v_cvt_pk_bf16_f32 v38, v44, v45\n" \
"v_mfma_f32_16x16x32_bf16 a[116:119], a[232:235], v[112:115], a[116:119]\n" \
"ds_read_b128 a[224:227], v233 offset:4096\n" \
"ds_read_b128 a[228:231], v233 offset:12288\n" \
"v_mfma_f32_16x16x32_bf16 a[116:119], a[236:239], v[116:119], a[116:119]\n" \
"v_exp_f32 v57, v57\n" \
"v_exp_f32 v58, v58\n" \
"v_cvt_pk_bf16_f32 v39, v46, v47\n" \
"v_accvgpr_read_b32 v251, a52\n" \
"v_mfma_f32_16x16x32_bf16 a[84:87], a[232:235], v[96:99], a[84:87]\n" \
"v_exp_f32 v59, v59\n" \
"v_mfma_f32_16x16x32_bf16 a[84:87], a[236:239], v[100:103], a[84:87]\n" \
"ds_write_b128 v232, v[168:171] offset:10240\n" \
"v_exp_f32 v60, v60\n" \
"v_cvt_pk_bf16_f32 v52, v56, v57\n" \
"v_mfma_f32_16x16x32_bf16 a[88:91], a[32:35], v[96:99], a[88:91]\n" \
"ds_read_b128 a[232:235], v233 offset:5120\n" \
"ds_read_b128 a[236:239], v233 offset:13312\n" \
"v_mfma_f32_16x16x32_bf16 a[88:91], a[36:39], v[100:103], a[88:91]\n" \
"ds_read_b128 a[240:243], v233 offset:6144\n" \
"ds_read_b128 a[244:247], v233 offset:14336\n" \
"v_exp_f32 v61, v61\n" \
"v_exp_f32 v62, v62\n" \
"v_mfma_f32_16x16x32_bf16 a[120:123], a[32:35], v[112:115], a[120:123]\n" \
"ds_read_b128 v[0:3], v233 offset:20480\n" \
"ds_read_b128 v[4:7], v233 offset:28672\n" \
"v_mfma_f32_16x16x32_bf16 a[120:123], a[36:39], v[116:119], a[120:123]\n" \
"ds_write_b128 v232, v[172:175] offset:11264\n" \
"v_cvt_pk_bf16_f32 v53, v58, v59\n" \
"v_exp_f32 v63, v63\n" \
"v_mfma_f32_16x16x32_bf16 a[124:127], a[40:43], v[112:115], a[124:127]\n" \
"ds_read_b128 v[8:11], v233 offset:21504\n" \
"ds_read_b128 v[12:15], v233 offset:29696\n" \
"v_mfma_f32_16x16x32_bf16 a[124:127], a[44:47], v[116:119], a[124:127]\n" \
"v_cvt_pk_bf16_f32 v54, v60, v61\n" \
"v_accvgpr_read_b32 v240, a53\n" \
"v_add_f32 v251, v251, v240\n" \
"v_mfma_f32_16x16x32_bf16 a[92:95], a[40:43], v[96:99], a[92:95]\n" \
"s_waitcnt lgkmcnt(5)\n" \
"v_cvt_pk_bf16_f32 v55, v62, v63\n" \
"v_mfma_f32_16x16x32_bf16 a[92:95], a[44:47], v[100:103], a[92:95]\n" \
"ds_write_b128 v232, v[176:179] offset:12288\n" \
"ds_write_b64 v249, v[250:251]\n" \
"v_mfma_f32_16x16x32_bf16 a[64:67], a[192:195], v[32:35], a[64:67]\n" \
"ds_read_b128 v[96:99], v233 offset:22528\n" \
"ds_read_b128 v[100:103], v233 offset:30720\n" \
"v_mfma_f32_16x16x32_bf16 a[64:67], a[196:199], v[36:39], a[64:67]\n" \
"ds_read_b128 v[104:107], v233 offset:23552\n" \
"ds_read_b128 v[108:111], v233 offset:31744\n" \
"v_exp_f32 v16, v16\n" \
"v_mfma_f32_16x16x32_bf16 a[96:99], a[192:195], v[48:51], a[96:99]\n" \
"v_exp_f32 v17, v17\n" \
"v_mfma_f32_16x16x32_bf16 a[96:99], a[196:199], v[52:55], a[96:99]\n" \
"ds_write_b128 v232, v[180:183] offset:13312\n" \
"v_exp_f32 v18, v18\n" \
"v_xor_b32 v249, 0x200, v249\n" \
"v_accvgpr_read_b32 v200, a59\n" \
"v_mfma_f32_16x16x32_bf16 a[100:103], a[200:203], v[48:51], a[100:103]\n" \
"v_exp_f32 v19, v19\n" \
"v_mfma_f32_16x16x32_bf16 a[100:103], a[204:207], v[52:55], a[100:103]\n" \
"v_exp_f32 v20, v20\n" \
"v_cvt_pk_bf16_f32 v16, v16, v17\n" \
"v_accvgpr_read_b32 v202, a60\n" \
"v_accvgpr_read_b32 v204, a61\n" \
"v_mfma_f32_16x16x32_bf16 a[68:71], a[200:203], v[32:35], a[68:71]\n" \
"v_exp_f32 v21, v21\n" \
"v_mfma_f32_16x16x32_bf16 a[68:71], a[204:207], v[36:39], a[68:71]\n" \
"ds_write_b128 v232, v[184:187] offset:14336\n" \
"v_exp_f32 v22, v22\n" \
"v_xor_b32 v201, 64, v200\n" \
"v_xor_b32 v203, 64, v202\n" \
"v_xor_b32 v222, 32, v204\n" \
"v_smfmac_f32_16x16x64_bf16 v[224:227], v[244:247], v[32:39], v243\n" \
"v_cvt_pk_bf16_f32 v17, v18, v19\n" \
"v_add_u32 v205, 0x10000, v200\n" \
"v_mfma_f32_16x16x32_bf16 a[72:75], a[208:211], v[32:35], a[72:75]\n" \
"v_exp_f32 v23, v23\n" \
"v_mfma_f32_16x16x32_bf16 a[72:75], a[212:215], v[36:39], a[72:75]\n" \
"v_exp_f32 v24, v24\n" \
"v_cvt_pk_bf16_f32 v18, v20, v21\n" \
"v_add_u32 v207, 0x10000, v202\n" \
"v_add_u32 v194, 139264, v204\n" \
"v_add_u32 v206, 0x10000, v201\n" \
"v_mfma_f32_16x16x32_bf16 a[104:107], a[208:211], v[48:51], a[104:107]\n" \
"ds_write_b128 v232, v[188:191] offset:15360\n" \
"v_exp_f32 v25, v25\n" \
"v_add_u32 v208, 0x10000, v203\n" \
"v_mfma_f32_16x16x32_bf16 a[104:107], a[212:215], v[52:55], a[104:107]\n" \
"v_exp_f32 v26, v26\n" \
"v_cvt_pk_bf16_f32 v19, v22, v23\n" \
"v_add_u32 v195, 139264, v222\n" \
"v_mfma_f32_16x16x32_bf16 a[108:111], a[216:219], v[48:51], a[108:111]\n" \
"s_mul_i32 s76, s80, 393216\n" \
"s_add_u32 s76, s76, s78\n" \
"v_cvt_pk_bf16_f32 v20, v24, v25\n" \
"v_mfma_f32_16x16x32_bf16 a[108:111], a[220:223], v[52:55], a[108:111]\n" \
"s_waitcnt vmcnt(0) lgkmcnt(0)\n" \
"s_barrier\n" \
"v_mfma_f32_16x16x32_bf16 a[76:79], a[216:219], v[32:35], a[76:79]\n" \
"s_add_u32 m0, s91, 86016\n" \
"ds_read_b64_tr_b16 a[32:33], v194 offset:1024\n" \
"ds_read_b64_tr_b16 a[34:35], v194 offset:4096\n" \
"v_mfma_f32_16x16x32_bf16 a[76:79], a[220:223], v[36:39], a[76:79]\n" \
"buffer_load_dwordx4 v197, s[48:51], s76 offen offset:0 lds\n" \
"v_exp_f32 v27, v27\n" \
"v_smfmac_f32_16x16x64_bf16 v[228:231], v[244:247], v[48:55], v243\n" \
"s_and_b32 s15, %1, 7\n" \
"ds_read_b64_tr_b16 a[36:37], v194 offset:13312\n" \
"ds_read_b64_tr_b16 a[38:39], v194 offset:16384\n" \
"v_exp_f32 v28, v28\n" \
"v_mfma_f32_16x16x32_bf16 a[80:83], a[224:227], v[32:35], a[80:83]\n" \
"s_add_u32 s77, s76, 24576\n" \
"ds_read_b64_tr_b16 a[40:41], v194 offset:0\n" \
"ds_read_b64_tr_b16 a[42:43], v194 offset:3072\n" \
"v_mfma_f32_16x16x32_bf16 a[80:83], a[228:231], v[36:39], a[80:83]\n" \
"buffer_load_dwordx4 v198, s[48:51], s76 offen offset:1024 lds\n" \
"v_mfma_f32_16x16x32_bf16 a[112:115], a[224:227], v[48:51], a[112:115]\n" \
"s_lshl3_add_u32 s14, s14, s15\n" \
"ds_read_b64_tr_b16 a[44:45], v194 offset:12288\n" \
"ds_read_b64_tr_b16 a[46:47], v194 offset:15360\n" \
"v_mfma_f32_16x16x32_bf16 a[112:115], a[228:231], v[52:55], a[112:115]\n" \
"s_xor_b32 s80, s80, 0x8\n" \
"ds_read_b64_tr_b16 a[48:49], v195 offset:0\n" \
"ds_read_b64_tr_b16 a[50:51], v195 offset:3072\n" \
"v_exp_f32 v29, v29\n" \
"v_mfma_f32_16x16x32_bf16 a[116:119], a[232:235], v[48:51], a[116:119]\n" \
"buffer_load_dwordx4 v199, s[48:51], s76 offen offset:2048 lds\n" \
"v_mfma_f32_16x16x32_bf16 a[116:119], a[236:239], v[52:55], a[116:119]\n" \
"ds_read_b64_tr_b16 a[52:53], v195 offset:12288\n" \
"ds_read_b64_tr_b16 a[54:55], v195 offset:15360\n" \
"v_exp_f32 v30, v30\n" \
"v_mfma_f32_16x16x32_bf16 a[84:87], a[232:235], v[32:35], a[84:87]\n" \
"s_add_u32 m0, m0, 3072\n" \
"ds_read_b128 a[0:3], v207 offset:45056\n" \
"ds_read_b128 a[4:7], v208 offset:45056\n" \
"v_mfma_f32_16x16x32_bf16 a[84:87], a[236:239], v[36:39], a[84:87]\n" \
"buffer_load_dwordx4 v197, s[48:51], s77 offen offset:0 lds\n" \
"v_cvt_pk_bf16_f32 v21, v26, v27\n" \
"v_mfma_f32_16x16x32_bf16 a[88:91], a[240:243], v[32:35], a[88:91]\n" \
"ds_read_b128 a[8:11], v205 offset:45056\n" \
"ds_read_b128 a[12:15], v206 offset:45056\n" \
"v_mfma_f32_16x16x32_bf16 a[88:91], a[244:247], v[36:39], a[88:91]\n" \
"ds_read_b128 a[16:19], v205 offset:49152\n" \
"ds_read_b128 a[20:23], v206 offset:49152\n" \
"v_exp_f32 v31, v31\n" \
"v_mfma_f32_16x16x32_bf16 a[120:123], a[240:243], v[48:51], a[120:123]\n" \
"buffer_load_dwordx4 v198, s[48:51], s77 offen offset:1024 lds\n" \
"v_mfma_f32_16x16x32_bf16 a[120:123], a[244:247], v[52:55], a[120:123]\n" \
"ds_read_b128 a[24:27], v205 offset:53248\n" \
"ds_read_b128 a[28:31], v206 offset:53248\n" \
"v_cvt_pk_bf16_f32 v22, v28, v29\n" \
"v_mfma_f32_16x16x32_bf16 a[124:127], a[248:251], v[48:51], a[124:127]\n" \
"ds_read_b64 v[250:251], v249\n" \
"ds_read_b128 v[128:131], v248 offset:0\n" \
"v_mfma_f32_16x16x32_bf16 a[124:127], a[252:255], v[52:55], a[124:127]\n" \
"buffer_load_dwordx4 v199, s[48:51], s77 offen offset:2048 lds\n" \
"v_cvt_pk_bf16_f32 v23, v30, v31\n" \
"v_mfma_f32_16x16x32_bf16 a[92:95], a[248:251], v[32:35], a[92:95]\n" \
"ds_read_b128 v[132:135], v248 offset:1024\n" \
"ds_read_b128 v[136:139], v248 offset:2048\n" \
"v_mfma_f32_16x16x32_bf16 a[92:95], a[252:255], v[36:39], a[92:95]\n" \
"s_add_u32 m0, s79, 65536\n" \
"ds_read_b128 v[140:143], v248 offset:3072\n" \
"ds_read_b128 v[144:147], v248 offset:4096\n" \
"v_mfma_f32_16x16x32_bf16 a[96:99], v[64:67], v[16:19], a[96:99]\n" \
"buffer_load_dwordx4 v192, s[44:47], s73 offen offset:0 lds\n" \
"v_mfma_f32_16x16x32_bf16 a[96:99], v[68:71], v[20:23], a[96:99]\n" \
"ds_read_b128 v[148:151], v248 offset:5120\n" \
"ds_read_b128 v[152:155], v248 offset:6144\n" \
"v_mfma_f32_16x16x32_bf16 a[100:103], v[72:75], v[16:19], a[100:103]\n" \
"ds_read_b128 v[156:159], v248 offset:7168\n" \
"ds_read_b128 v[160:163], v248 offset:8192\n" \
"v_mfma_f32_16x16x32_bf16 a[100:103], v[76:79], v[20:23], a[100:103]\n" \
"buffer_load_dwordx4 v193, s[44:47], s73 offen offset:1024 lds\n" \
"v_smfmac_f32_16x16x64_bf16 v[228:231], v[244:247], v[16:23], v243\n" \
"ds_read_b128 v[164:167], v248 offset:9216\n" \
"ds_read_b128 v[168:171], v248 offset:10240\n" \
"v_mfma_f32_16x16x32_bf16 a[104:107], v[80:83], v[16:19], a[104:107]\n" \
"ds_read_b128 v[172:175], v248 offset:11264\n" \
"ds_read_b128 v[176:179], v248 offset:12288\n" \
"v_mfma_f32_16x16x32_bf16 a[104:107], v[84:87], v[20:23], a[104:107]\n" \
"buffer_load_dwordx4 v192, s[44:47], s73 offen offset:2048 lds\n" \
"v_mfma_f32_16x16x32_bf16 a[108:111], v[88:91], v[16:19], a[108:111]\n" \
"ds_read_b128 v[180:183], v248 offset:13312\n" \
"ds_read_b128 v[184:187], v248 offset:14336\n" \
"v_mfma_f32_16x16x32_bf16 a[108:111], v[92:95], v[20:23], a[108:111]\n" \
"ds_read_b128 v[188:191], v248 offset:15360\n" \
"v_mov_b32 v218, s60\n" \
"v_mfma_f32_16x16x32_bf16 a[112:115], v[0:3], v[16:19], a[112:115]\n" \
"buffer_load_dwordx4 v193, s[44:47], s73 offen offset:3072 lds\n" \
"v_mfma_f32_16x16x32_bf16 a[112:115], v[4:7], v[20:23], a[112:115]\n" \
"s_add_u32 m0, s89, 65536\n" \
"v_mov_b32 v219, s14\n" \
"v_lshlrev_b32 v220, 28, s61\n" \
"v_mfma_f32_16x16x32_bf16 a[116:119], v[8:11], v[16:19], a[116:119]\n" \
"s_xor_b32 s73, s73, 0x400\n" \
"v_mfma_f32_16x16x32_bf16 a[116:119], v[12:15], v[20:23], a[116:119]\n" \
"buffer_load_dwordx4 v196, s[44:47], s75 offen lds\n" \
"v_mfma_f32_16x16x32_bf16 a[120:123], v[96:99], v[16:19], a[120:123]\n" \
"s_lshl3_add_u32 s15, s15, 65593856\n" \
"v_mfma_f32_16x16x32_bf16 a[120:123], v[100:103], v[20:23], a[120:123]\n" \
"s_lshr_b32 s14, %1, 3\n" \
"s_cmp_eq_u32 s14, 3\n" \
"s_cselect_b32 s15, s15, 0x20000000\n" \
"buffer_store_dwordx2 v[218:219], v220, s[56:59], s15 offen sc1\n" \
"v_mfma_f32_16x16x32_bf16 a[124:127], v[104:107], v[16:19], a[124:127]\n" \
"s_xor_b32 s75, s75, 0x400\n" \
"v_mfma_f32_16x16x32_bf16 a[124:127], v[108:111], v[20:23], a[124:127]\n" \
"s_waitcnt lgkmcnt(0)\n" \
"s_barrier\n" \
"v_mfma_f32_16x16x32_bf16 v[32:35], a[32:35], a[0:3], 0\n" \
"s_add_u32 m0, s79, 0\n" \
"ds_read_b128 a[192:195], v205 offset:57344\n" \
"v_mfma_f32_16x16x32_bf16 v[32:35], a[36:39], a[4:7], v[32:35]\n" \
"buffer_load_dwordx4 v192, s[44:47], s73 offen offset:0 lds\n" \
"v_mfma_f32_16x16x32_bf16 v[40:43], a[32:35], a[8:11], 0\n" \
"ds_read_b128 a[196:199], v206 offset:57344\n" \
"v_add_u32 v209, 86016, v204\n" \
"v_mfma_f32_16x16x32_bf16 v[40:43], a[36:39], a[12:15], v[40:43]\n" \
"buffer_load_dwordx4 v193, s[44:47], s73 offen offset:1024 lds\n" \
"v_mfma_f32_16x16x32_bf16 v[0:3], a[40:43], a[8:11], 0\n" \
"ds_read_b64_tr_b16 a[200:201], v195 offset:1024\n" \
"ds_read_b64_tr_b16 a[202:203], v195 offset:4096\n" \
"v_mfma_f32_16x16x32_bf16 v[0:3], a[44:47], a[12:15], v[0:3]\n" \
"buffer_load_dwordx4 v192, s[44:47], s73 offen offset:2048 lds\n" \
"v_add_f32 v248, v224, v225\n" \
"v_mfma_f32_16x16x32_bf16 v[4:7], a[48:51], a[8:11], 0\n" \
"ds_read_b64_tr_b16 a[204:205], v195 offset:13312\n" \
"ds_read_b64_tr_b16 a[206:207], v195 offset:16384\n" \
"v_mfma_f32_16x16x32_bf16 v[4:7], a[52:55], a[12:15], v[4:7]\n" \
"buffer_load_dwordx4 v193, s[44:47], s73 offen offset:3072 lds\n" \
"v_mfma_f32_16x16x32_bf16 v[12:15], a[48:51], a[16:19], 0\n" \
"s_add_u32 m0, s89, 0\n" \
"s_xor_b32 s73, s73, 0x500\n" \
"ds_read_b64_tr_b16 a[208:209], v194 offset:2048\n" \
"ds_read_b64_tr_b16 a[210:211], v194 offset:5120\n" \
"v_add_f32 v249, v228, v229\n" \
"v_mfma_f32_16x16x32_bf16 v[12:15], a[52:55], a[20:23], v[12:15]\n" \
"buffer_load_dwordx4 v196, s[44:47], s75 offen lds\n" \
"v_mfma_f32_16x16x32_bf16 v[8:11], a[40:43], a[16:19], 0\n" \
"s_xor_b32 s75, s75, 0x500\n" \
"s_mul_i32 s76, s80, 393216\n" \
"s_add_u32 s76, s76, s78\n" \
"s_add_u32 m0, s91, 20480\n" \
"ds_read_b64_tr_b16 a[212:213], v194 offset:14336\n" \
"ds_read_b64_tr_b16 a[214:215], v194 offset:17408\n" \
"v_mfma_f32_16x16x32_bf16 v[8:11], a[44:47], a[20:23], v[8:11]\n" \
"buffer_load_dwordx4 v197, s[48:51], s76 offen offset:0 lds\n" \
"v_add_f32 v248, v248, v250\n" \
"v_mfma_f32_16x16x32_bf16 v[48:51], a[32:35], a[16:19], 0\n" \
"s_add_u32 s77, s76, 24576\n" \
"ds_read_b64_tr_b16 a[216:217], v195 offset:2048\n" \
"v_add_u32 v210, 86016, v222\n" \
"v_mfma_f32_16x16x32_bf16 v[48:51], a[36:39], a[20:23], v[48:51]\n" \
"buffer_load_dwordx4 v198, s[48:51], s76 offen offset:1024 lds\n" \
"v_mfma_f32_16x16x32_bf16 v[56:59], a[32:35], a[24:27], 0\n" \
"s_xor_b32 s80, s80, 0xa\n" \
"ds_read_b64_tr_b16 a[218:219], v195 offset:5120\n" \
"v_add_f32 v249, v249, v251\n" \
"v_mfma_f32_16x16x32_bf16 v[56:59], a[36:39], a[28:31], v[56:59]\n" \
"buffer_load_dwordx4 v199, s[48:51], s76 offen offset:2048 lds\n" \
"v_mfma_f32_16x16x32_bf16 v[16:19], a[40:43], a[24:27], 0\n" \
"s_add_u32 m0, m0, 3072\n" \
"ds_read_b64_tr_b16 a[220:221], v195 offset:14336\n" \
"v_mfma_f32_16x16x32_bf16 v[16:19], a[44:47], a[28:31], v[16:19]\n" \
"buffer_load_dwordx4 v197, s[48:51], s77 offen offset:0 lds\n" \
"v_mfma_f32_16x16x32_bf16 v[20:23], a[48:51], a[24:27], 0\n" \
"s_waitcnt lgkmcnt(2)\n" \
"ds_read_b64_tr_b16 a[222:223], v195 offset:17408\n" \
"v_mfma_f32_16x16x32_bf16 v[20:23], a[52:55], a[28:31], v[20:23]\n" \
"buffer_load_dwordx4 v198, s[48:51], s77 offen offset:1024 lds\n" \
"v_rcp_f32 v232, v248\n" \
"v_mfma_f32_16x16x32_bf16 v[28:31], a[48:51], a[192:195], 0\n" \
"v_rcp_f32 v233, v249\n" \
"v_mfma_f32_16x16x32_bf16 v[28:31], a[52:55], a[196:199], v[28:31]\n" \
"buffer_load_dwordx4 v199, s[48:51], s77 offen offset:2048 lds\n" \
"v_add_u32 v240, 0x1000, v236\n" \
"v_mfma_f32_16x16x32_bf16 v[24:27], a[40:43], a[192:195], 0\n" \
"v_add_f32 v224, v248, v249\n" \
"v_mul_f32 v224, 0x43800000, v224\n" \
"v_mfma_f32_16x16x32_bf16 v[24:27], a[44:47], a[196:199], v[24:27]\n" \
"v_add_f32 v248, v232, v233\n" \
"v_fmac_f32 v224, 0x5f800000, v248\n" \
"v_mfma_f32_16x16x32_bf16 v[64:67], a[32:35], a[192:195], 0\n" \
"v_mov_b32 v225, 0\n" \
"v_mov_b32 v226, 0\n" \
"v_mfma_f32_16x16x32_bf16 v[64:67], a[36:39], a[196:199], v[64:67]\n" \
"v_mov_b32 v227, 0\n" \
"s_waitcnt vmcnt(12) lgkmcnt(0)\n" \
"s_barrier\n" \
"v_mfma_f32_16x16x32_bf16 v[72:75], a[0:3], a[208:211], 0\n" \
"ds_read_b64_tr_b16 a[32:33], v209 offset:1024\n" \
"ds_read_b64_tr_b16 a[34:35], v209 offset:4096\n" \
"v_mfma_f32_16x16x32_bf16 v[72:75], a[4:7], a[212:215], v[72:75]\n" \
"ds_read_b64_tr_b16 a[36:37], v209 offset:13312\n" \
"ds_read_b64_tr_b16 a[38:39], v209 offset:16384\n" \
"v_mfma_f32_16x16x32_bf16 v[80:83], a[8:11], a[208:211], 0\n" \
"s_add_u32 m0, s79, 110592\n" \
"ds_read_b128 a[0:3], v207 offset:0\n" \
"v_accvgpr_read_b32 v112, a64\n" \
"v_mfma_f32_16x16x32_bf16 v[80:83], a[12:15], a[212:215], v[80:83]\n" \
"buffer_load_dwordx4 v192, s[44:47], s73 offen offset:0 lds\n" \
"v_accvgpr_read_b32 v113, a65\n" \
"v_mfma_f32_16x16x32_bf16 v[84:87], a[8:11], a[216:219], 0\n" \
"ds_read_b128 a[4:7], v208 offset:0\n" \
"v_accvgpr_read_b32 v114, a66\n" \
"v_mfma_f32_16x16x32_bf16 v[84:87], a[12:15], a[220:223], v[84:87]\n" \
"ds_read_b64_tr_b16 a[40:41], v209 offset:0\n" \
"v_accvgpr_read_b32 v115, a67\n" \
"v_accvgpr_read_b32 v116, a68\n" \
"v_mfma_f32_16x16x32_bf16 v[44:47], a[200:203], a[8:11], 0\n" \
"ds_read_b64_tr_b16 a[42:43], v209 offset:3072\n" \
"v_accvgpr_read_b32 v117, a69\n" \
"v_mfma_f32_16x16x32_bf16 v[44:47], a[204:207], a[12:15], v[44:47]\n" \
"buffer_load_dwordx4 v193, s[44:47], s73 offen offset:1024 lds\n" \
"v_mfma_f32_16x16x32_bf16 v[52:55], a[200:203], a[16:19], 0\n" \
"ds_read_b128 a[8:11], v205 offset:0\n" \
"v_accvgpr_read_b32 v118, a70\n" \
"v_mfma_f32_16x16x32_bf16 v[52:55], a[204:207], a[20:23], v[52:55]\n" \
"ds_read_b128 a[12:15], v206 offset:0\n" \
"v_accvgpr_read_b32 v119, a71\n" \
"v_add_f32 v112, v112, v128\n" \
"v_mfma_f32_16x16x32_bf16 v[92:95], a[16:19], a[216:219], 0\n" \
"ds_read_b64_tr_b16 a[44:45], v209 offset:12288\n" \
"v_add_f32 v113, v113, v129\n" \
"v_mfma_f32_16x16x32_bf16 v[92:95], a[20:23], a[220:223], v[92:95]\n" \
"buffer_load_dwordx4 v192, s[44:47], s73 offen offset:2048 lds\n" \
"v_mfma_f32_16x16x32_bf16 v[88:91], a[16:19], a[208:211], 0\n" \
"ds_read_b64_tr_b16 a[46:47], v209 offset:15360\n" \
"v_add_f32 v114, v114, v130\n" \
"v_mfma_f32_16x16x32_bf16 v[88:91], a[20:23], a[212:215], v[88:91]\n" \
"ds_read_b64_tr_b16 a[48:49], v210 offset:0\n" \
"v_add_f32 v115, v115, v131\n" \
"v_add_f32 v116, v116, v132\n" \
"v_mfma_f32_16x16x32_bf16 v[96:99], a[24:27], a[208:211], 0\n" \
"ds_read_b128 a[16:19], v205 offset:4096\n" \
"v_add_f32 v117, v117, v133\n" \
"v_mfma_f32_16x16x32_bf16 v[96:99], a[28:31], a[212:215], v[96:99]\n" \
"buffer_load_dwordx4 v193, s[44:47], s73 offen offset:3072 lds\n" \
"v_add_f32 v118, v118, v134\n" \
"v_mfma_f32_16x16x32_bf16 v[100:103], a[24:27], a[216:219], 0\n" \
"ds_read_b128 a[20:23], v206 offset:4096\n" \
"v_add_f32 v119, v119, v135\n" \
"v_mfma_f32_16x16x32_bf16 v[100:103], a[28:31], a[220:223], v[100:103]\n" \
"s_add_u32 m0, s89, 110592\n" \
"ds_read_b64_tr_b16 a[50:51], v210 offset:3072\n" \
"v_mul_f32 v112, v112, v232\n" \
"v_mfma_f32_16x16x32_bf16 v[60:63], a[200:203], a[24:27], 0\n" \
"s_xor_b32 s73, s73, 0x180\n" \
"ds_read_b64_tr_b16 a[52:53], v210 offset:12288\n" \
"v_mul_f32 v113, v113, v232\n" \
"v_mfma_f32_16x16x32_bf16 v[60:63], a[204:207], a[28:31], v[60:63]\n" \
"buffer_load_dwordx4 v196, s[44:47], s75 offen lds\n" \
"v_mul_f32 v114, v114, v232\n" \
"v_mfma_f32_16x16x32_bf16 v[68:71], a[200:203], a[192:195], 0\n" \
"s_xor_b32 s75, s75, 0x180\n" \
"s_mul_i32 s76, s80, 393216\n" \
"ds_read_b128 a[24:27], v205 offset:8192\n" \
"v_mfma_f32_16x16x32_bf16 v[68:71], a[204:207], a[196:199], v[68:71]\n" \
"s_add_u32 s76, s76, s78\n" \
"ds_read_b128 a[28:31], v206 offset:8192\n" \
"v_mul_f32 v115, v115, v232\n" \
"v_mfma_f32_16x16x32_bf16 v[108:111], a[192:195], a[216:219], 0\n" \
"s_add_u32 m0, s91, 139264\n" \
"ds_read_b64_tr_b16 a[54:55], v210 offset:15360\n" \
"v_mul_f32 v116, v116, v232\n" \
"v_mfma_f32_16x16x32_bf16 v[108:111], a[196:199], a[220:223], v[108:111]\n" \
"buffer_load_dwordx4 v197, s[48:51], s76 offen offset:0 lds\n" \
"v_mul_f32 v117, v117, v232\n" \
"v_mfma_f32_16x16x32_bf16 v[104:107], a[192:195], a[208:211], 0\n" \
"v_mul_f32 v118, v118, v232\n" \
"v_mul_f32 v119, v119, v232\n" \
"v_mfma_f32_16x16x32_bf16 v[104:107], a[196:199], a[212:215], v[104:107]\n" \
"s_waitcnt lgkmcnt(2)\n" \
"v_cvt_pk_bf16_f32 v120, v112, v113\n" \
"v_cvt_pk_bf16_f32 v121, v114, v115\n" \
"v_cvt_pk_bf16_f32 v122, v116, v117\n" \
"v_mfma_f32_16x16x32_bf16 v[32:35], a[32:35], a[0:3], v[32:35]\n" \
"s_add_u32 s77, s76, 24576\n" \
"ds_read_b128 a[192:195], v205 offset:12288\n" \
"v_cvt_pk_bf16_f32 v123, v118, v119\n" \
"v_mfma_f32_16x16x32_bf16 v[32:35], a[36:39], a[4:7], v[32:35]\n" \
"buffer_load_dwordx4 v198, s[48:51], s76 offen offset:1024 lds\n" \
"v_mfma_f32_16x16x32_bf16 v[40:43], a[32:35], a[8:11], v[40:43]\n" \
"ds_read_b128 a[196:199], v206 offset:12288\n" \
"v_mfma_f32_16x16x32_bf16 v[40:43], a[36:39], a[12:15], v[40:43]\n" \
"ds_read_b64_tr_b16 a[200:201], v210 offset:1024\n" \
"buffer_store_dwordx4 v[120:123], v236, s[56:59], s88 offen offset:0 sc1\n" \
"v_mfma_f32_16x16x32_bf16 v[0:3], a[40:43], a[8:11], v[0:3]\n" \
"s_waitcnt lgkmcnt(3)\n" \
"s_xor_b32 s80, s80, 0x3\n" \
"ds_read_b64_tr_b16 a[202:203], v210 offset:4096\n" \
"v_mfma_f32_16x16x32_bf16 v[0:3], a[44:47], a[12:15], v[0:3]\n" \
"buffer_load_dwordx4 v199, s[48:51], s76 offen offset:2048 lds\n" \
"v_mfma_f32_16x16x32_bf16 v[224:227], v[244:247], v[120:123], v[224:227]\n" \
"v_accvgpr_read_b32 v112, a72\n" \
"v_accvgpr_read_b32 v113, a73\n" \
"v_accvgpr_read_b32 v114, a74\n" \
"v_mfma_f32_16x16x32_bf16 v[4:7], a[48:51], a[8:11], v[4:7]\n" \
"ds_read_b64_tr_b16 a[204:205], v210 offset:13312\n" \
"v_accvgpr_read_b32 v115, a75\n" \
"v_mfma_f32_16x16x32_bf16 v[4:7], a[52:55], a[12:15], v[4:7]\n" \
"ds_read_b64_tr_b16 a[206:207], v210 offset:16384\n" \
"v_accvgpr_read_b32 v116, a76\n" \
"v_accvgpr_read_b32 v117, a77\n" \
"v_mfma_f32_16x16x32_bf16 v[12:15], a[48:51], a[16:19], v[12:15]\n" \
"s_add_u32 m0, m0, 3072\n" \
"ds_read_b64_tr_b16 a[208:209], v209 offset:2048\n" \
"v_accvgpr_read_b32 v118, a78\n" \
"v_mfma_f32_16x16x32_bf16 v[12:15], a[52:55], a[20:23], v[12:15]\n" \
"buffer_load_dwordx4 v197, s[48:51], s77 offen offset:0 lds\n" \
"v_accvgpr_read_b32 v119, a79\n" \
"v_mfma_f32_16x16x32_bf16 v[8:11], a[40:43], a[16:19], v[8:11]\n" \
"ds_read_b64_tr_b16 a[210:211], v209 offset:5120\n" \
"v_add_f32 v112, v112, v136\n" \
"v_mfma_f32_16x16x32_bf16 v[8:11], a[44:47], a[20:23], v[8:11]\n" \
"ds_read_b64_tr_b16 a[212:213], v209 offset:14336\n" \
"v_add_f32 v113, v113, v137\n" \
"v_mfma_f32_16x16x32_bf16 v[48:51], a[32:35], a[16:19], v[48:51]\n" \
"ds_read_b64_tr_b16 a[214:215], v209 offset:17408\n" \
"v_add_f32 v114, v114, v138\n" \
"v_mfma_f32_16x16x32_bf16 v[48:51], a[36:39], a[20:23], v[48:51]\n" \
"buffer_load_dwordx4 v198, s[48:51], s77 offen offset:1024 lds\n" \
"v_add_f32 v115, v115, v139\n" \
"v_mfma_f32_16x16x32_bf16 v[56:59], a[32:35], a[24:27], v[56:59]\n" \
"ds_read_b64_tr_b16 a[216:217], v210 offset:2048\n" \
"v_add_f32 v116, v116, v140\n" \
"v_mfma_f32_16x16x32_bf16 v[56:59], a[36:39], a[28:31], v[56:59]\n" \
"ds_read_b64_tr_b16 a[218:219], v210 offset:5120\n" \
"v_add_f32 v117, v117, v141\n" \
"v_mfma_f32_16x16x32_bf16 v[16:19], a[40:43], a[24:27], v[16:19]\n" \
"ds_read_b64_tr_b16 a[220:221], v210 offset:14336\n" \
"v_add_f32 v118, v118, v142\n" \
"v_mfma_f32_16x16x32_bf16 v[16:19], a[44:47], a[28:31], v[16:19]\n" \
"buffer_load_dwordx4 v199, s[48:51], s77 offen offset:2048 lds\n" \
"v_add_f32 v119, v119, v143\n" \
"v_mfma_f32_16x16x32_bf16 v[20:23], a[48:51], a[24:27], v[20:23]\n" \
"ds_read_b64_tr_b16 a[222:223], v210 offset:17408\n" \
"v_mul_f32 v112, v112, v232\n" \
"v_mfma_f32_16x16x32_bf16 v[20:23], a[52:55], a[28:31], v[20:23]\n" \
"s_waitcnt lgkmcnt(3)\n" \
"v_mul_f32 v113, v113, v232\n" \
"v_mul_f32 v114, v114, v232\n" \
"v_mul_f32 v115, v115, v232\n" \
"v_mfma_f32_16x16x32_bf16 v[28:31], a[48:51], a[192:195], v[28:31]\n" \
"v_mul_f32 v116, v116, v232\n" \
"v_mul_f32 v117, v117, v232\n" \
"v_mfma_f32_16x16x32_bf16 v[28:31], a[52:55], a[196:199], v[28:31]\n" \
"v_mul_f32 v118, v118, v232\n" \
"v_mul_f32 v119, v119, v232\n" \
"v_mfma_f32_16x16x32_bf16 v[24:27], a[40:43], a[192:195], v[24:27]\n" \
"v_cvt_pk_bf16_f32 v124, v112, v113\n" \
"v_cvt_pk_bf16_f32 v125, v114, v115\n" \
"v_mfma_f32_16x16x32_bf16 v[24:27], a[44:47], a[196:199], v[24:27]\n" \
"v_cvt_pk_bf16_f32 v126, v116, v117\n" \
"v_cvt_pk_bf16_f32 v127, v118, v119\n" \
"v_mfma_f32_16x16x32_bf16 v[64:67], a[32:35], a[192:195], v[64:67]\n" \
"buffer_store_dwordx4 v[124:127], v236, s[56:59], s88 offen offset:2048 sc1\n" \
"v_mfma_f32_16x16x32_bf16 v[64:67], a[36:39], a[196:199], v[64:67]\n" \
"v_mfma_f32_16x16x32_bf16 v[224:227], v[244:247], v[124:127], v[224:227]\n" \
"v_accvgpr_read_b32 v112, a80\n" \
"s_waitcnt vmcnt(13) lgkmcnt(0)\n" \
"s_barrier\n" \
"v_mfma_f32_16x16x32_bf16 v[72:75], a[0:3], a[208:211], v[72:75]\n" \
"ds_read_b64_tr_b16 a[32:33], v204 offset:21504\n" \
"ds_read_b64_tr_b16 a[34:35], v204 offset:24576\n" \
"v_mfma_f32_16x16x32_bf16 v[72:75], a[4:7], a[212:215], v[72:75]\n" \
"ds_read_b64_tr_b16 a[36:37], v204 offset:33792\n" \
"ds_read_b64_tr_b16 a[38:39], v204 offset:36864\n" \
"v_accvgpr_read_b32 v113, a81\n" \
"v_mfma_f32_16x16x32_bf16 v[80:83], a[8:11], a[208:211], v[80:83]\n" \
"s_add_u32 m0, s79, 65536\n" \
"ds_read_b128 a[0:3], v202 offset:0\n" \
"v_accvgpr_read_b32 v114, a82\n" \
"v_mfma_f32_16x16x32_bf16 v[80:83], a[12:15], a[212:215], v[80:83]\n" \
"buffer_load_dwordx4 v192, s[44:47], s73 offen offset:0 lds\n" \
"v_accvgpr_read_b32 v115, a83\n" \
"v_mfma_f32_16x16x32_bf16 v[84:87], a[8:11], a[216:219], v[84:87]\n" \
"ds_read_b128 a[4:7], v203 offset:0\n" \
"v_accvgpr_read_b32 v116, a84\n" \
"v_mfma_f32_16x16x32_bf16 v[84:87], a[12:15], a[220:223], v[84:87]\n" \
"ds_read_b64_tr_b16 a[40:41], v204 offset:20480\n" \
"v_accvgpr_read_b32 v117, a85\n" \
"v_mfma_f32_16x16x32_bf16 v[44:47], a[200:203], a[8:11], v[44:47]\n" \
"ds_read_b64_tr_b16 a[42:43], v204 offset:23552\n" \
"v_accvgpr_read_b32 v118, a86\n" \
"v_mfma_f32_16x16x32_bf16 v[44:47], a[204:207], a[12:15], v[44:47]\n" \
"buffer_load_dwordx4 v193, s[44:47], s73 offen offset:1024 lds\n" \
"v_accvgpr_read_b32 v119, a87\n" \
"v_mfma_f32_16x16x32_bf16 v[52:55], a[200:203], a[16:19], v[52:55]\n" \
"ds_read_b128 a[8:11], v200 offset:0\n" \
"v_add_f32 v112, v112, v144\n" \
"v_mfma_f32_16x16x32_bf16 v[52:55], a[204:207], a[20:23], v[52:55]\n" \
"ds_read_b128 a[12:15], v201 offset:0\n" \
"v_add_f32 v113, v113, v145\n" \
"v_mfma_f32_16x16x32_bf16 v[92:95], a[16:19], a[216:219], v[92:95]\n" \
"ds_read_b64_tr_b16 a[44:45], v204 offset:32768\n" \
"v_add_f32 v114, v114, v146\n" \
"v_mfma_f32_16x16x32_bf16 v[92:95], a[20:23], a[220:223], v[92:95]\n" \
"buffer_load_dwordx4 v192, s[44:47], s73 offen offset:2048 lds\n" \
"v_add_f32 v115, v115, v147\n" \
"v_mfma_f32_16x16x32_bf16 v[88:91], a[16:19], a[208:211], v[88:91]\n" \
"ds_read_b64_tr_b16 a[46:47], v204 offset:35840\n" \
"v_add_f32 v116, v116, v148\n" \
"v_mfma_f32_16x16x32_bf16 v[88:91], a[20:23], a[212:215], v[88:91]\n" \
"ds_read_b64_tr_b16 a[48:49], v222 offset:20480\n" \
"v_add_f32 v117, v117, v149\n" \
"v_add_f32 v118, v118, v150\n" \
"v_mfma_f32_16x16x32_bf16 v[96:99], a[24:27], a[208:211], v[96:99]\n" \
"ds_read_b128 a[16:19], v200 offset:4096\n" \
"v_add_f32 v119, v119, v151\n" \
"v_mfma_f32_16x16x32_bf16 v[96:99], a[28:31], a[212:215], v[96:99]\n" \
"buffer_load_dwordx4 v193, s[44:47], s73 offen offset:3072 lds\n" \
"v_mfma_f32_16x16x32_bf16 v[100:103], a[24:27], a[216:219], v[100:103]\n" \
"ds_read_b128 a[20:23], v201 offset:4096\n" \
"v_mul_f32 v112, v112, v232\n" \
"v_mfma_f32_16x16x32_bf16 v[100:103], a[28:31], a[220:223], v[100:103]\n" \
"s_add_u32 m0, s89, 65536\n" \
"ds_read_b64_tr_b16 a[50:51], v222 offset:23552\n" \
"v_mul_f32 v113, v113, v232\n" \
"v_mul_f32 v114, v114, v232\n" \
"v_mfma_f32_16x16x32_bf16 v[60:63], a[200:203], a[24:27], v[60:63]\n" \
"s_xor_b32 s73, s73, 0x300\n" \
"ds_read_b64_tr_b16 a[52:53], v222 offset:32768\n" \
"v_mul_f32 v115, v115, v232\n" \
"v_mfma_f32_16x16x32_bf16 v[60:63], a[204:207], a[28:31], v[60:63]\n" \
"buffer_load_dwordx4 v196, s[44:47], s75 offen lds\n" \
"v_mfma_f32_16x16x32_bf16 v[68:71], a[200:203], a[192:195], v[68:71]\n" \
"s_xor_b32 s75, s75, 0x300\n" \
"s_mul_i32 s76, s80, 393216\n" \
"ds_read_b128 a[24:27], v200 offset:8192\n" \
"v_mfma_f32_16x16x32_bf16 v[68:71], a[204:207], a[196:199], v[68:71]\n" \
"s_add_u32 s76, s76, s78\n" \
"ds_read_b128 a[28:31], v201 offset:8192\n" \
"v_mul_f32 v116, v116, v232\n" \
"v_mul_f32 v117, v117, v232\n" \
"v_mfma_f32_16x16x32_bf16 v[108:111], a[192:195], a[216:219], v[108:111]\n" \
"s_add_u32 m0, s91, 86016\n" \
"ds_read_b64_tr_b16 a[54:55], v222 offset:35840\n" \
"v_mul_f32 v118, v118, v232\n" \
"v_mfma_f32_16x16x32_bf16 v[108:111], a[196:199], a[220:223], v[108:111]\n" \
"buffer_load_dwordx4 v197, s[48:51], s76 offen offset:0 lds\n" \
"v_mul_f32 v119, v119, v232\n" \
"v_mfma_f32_16x16x32_bf16 v[104:107], a[192:195], a[208:211], v[104:107]\n" \
"v_cvt_pk_bf16_f32 v120, v112, v113\n" \
"v_cvt_pk_bf16_f32 v121, v114, v115\n" \
"v_mfma_f32_16x16x32_bf16 v[104:107], a[196:199], a[212:215], v[104:107]\n" \
"s_waitcnt lgkmcnt(2)\n" \
"v_cvt_pk_bf16_f32 v122, v116, v117\n" \
"v_cvt_pk_bf16_f32 v123, v118, v119\n" \
"v_mfma_f32_16x16x32_bf16 v[32:35], a[32:35], a[0:3], v[32:35]\n" \
"s_add_u32 s77, s76, 24576\n" \
"ds_read_b128 a[192:195], v200 offset:12288\n" \
"v_mfma_f32_16x16x32_bf16 v[32:35], a[36:39], a[4:7], v[32:35]\n" \
"buffer_load_dwordx4 v198, s[48:51], s76 offen offset:1024 lds\n" \
"v_mfma_f32_16x16x32_bf16 v[40:43], a[32:35], a[8:11], v[40:43]\n" \
"ds_read_b128 a[196:199], v201 offset:12288\n" \
"v_mfma_f32_16x16x32_bf16 v[40:43], a[36:39], a[12:15], v[40:43]\n" \
"ds_read_b64_tr_b16 a[200:201], v222 offset:21504\n" \
"buffer_store_dwordx4 v[120:123], v240, s[56:59], s88 offen offset:0 sc1\n" \
"v_mfma_f32_16x16x32_bf16 v[0:3], a[40:43], a[8:11], v[0:3]\n" \
"s_waitcnt lgkmcnt(3)\n" \
"s_xor_b32 s80, s80, 0x6\n" \
"ds_read_b64_tr_b16 a[202:203], v222 offset:24576\n" \
"v_mfma_f32_16x16x32_bf16 v[224:227], v[244:247], v[120:123], v[224:227]\n" \
"v_accvgpr_read_b32 v112, a88\n" \
"v_accvgpr_read_b32 v113, a89\n" \
"Lpg7932_%=:\n" \
"Lst1x0_%=:\n" \
"s_cbranch_execz Lpg10215_%=\n" \
"v_mfma_f32_16x16x32_bf16 v[0:3], a[44:47], a[12:15], v[0:3]\n" \
"buffer_load_dwordx4 v199, s[48:51], s76 offen offset:2048 lds\n" \
"v_mfma_f32_16x16x32_bf16 v[4:7], a[48:51], a[8:11], v[4:7]\n" \
"ds_read_b64_tr_b16 a[204:205], v222 offset:33792\n" \
"v_accvgpr_read_b32 v114, a90\n" \
"v_mfma_f32_16x16x32_bf16 v[4:7], a[52:55], a[12:15], v[4:7]\n" \
"ds_read_b64_tr_b16 a[206:207], v222 offset:36864\n" \
"v_accvgpr_read_b32 v115, a91\n" \
"v_accvgpr_read_b32 v116, a92\n" \
"v_mfma_f32_16x16x32_bf16 v[12:15], a[48:51], a[16:19], v[12:15]\n" \
"s_add_u32 m0, m0, 3072\n" \
"ds_read_b64_tr_b16 a[208:209], v204 offset:22528\n" \
"v_accvgpr_read_b32 v117, a93\n" \
"v_mfma_f32_16x16x32_bf16 v[12:15], a[52:55], a[20:23], v[12:15]\n" \
"buffer_load_dwordx4 v197, s[48:51], s77 offen offset:0 lds\n" \
"v_mfma_f32_16x16x32_bf16 v[8:11], a[40:43], a[16:19], v[8:11]\n" \
"ds_read_b64_tr_b16 a[210:211], v204 offset:25600\n" \
"v_accvgpr_read_b32 v118, a94\n" \
"v_mfma_f32_16x16x32_bf16 v[8:11], a[44:47], a[20:23], v[8:11]\n" \
"ds_read_b64_tr_b16 a[212:213], v204 offset:34816\n" \
"v_accvgpr_read_b32 v119, a95\n" \
"v_add_f32 v112, v112, v152\n" \
"v_mfma_f32_16x16x32_bf16 v[48:51], a[32:35], a[16:19], v[48:51]\n" \
"ds_read_b64_tr_b16 a[214:215], v204 offset:37888\n" \
"v_add_f32 v113, v113, v153\n" \
"v_mfma_f32_16x16x32_bf16 v[48:51], a[36:39], a[20:23], v[48:51]\n" \
"buffer_load_dwordx4 v198, s[48:51], s77 offen offset:1024 lds\n" \
"v_mfma_f32_16x16x32_bf16 v[56:59], a[32:35], a[24:27], v[56:59]\n" \
"ds_read_b64_tr_b16 a[216:217], v222 offset:22528\n" \
"v_add_f32 v114, v114, v154\n" \
"v_mfma_f32_16x16x32_bf16 v[56:59], a[36:39], a[28:31], v[56:59]\n" \
"ds_read_b64_tr_b16 a[218:219], v222 offset:25600\n" \
"v_add_f32 v115, v115, v155\n" \
"v_add_f32 v116, v116, v156\n" \
"v_mfma_f32_16x16x32_bf16 v[16:19], a[40:43], a[24:27], v[16:19]\n" \
"ds_read_b64_tr_b16 a[220:221], v222 offset:34816\n" \
"v_add_f32 v117, v117, v157\n" \
"v_mfma_f32_16x16x32_bf16 v[16:19], a[44:47], a[28:31], v[16:19]\n" \
"buffer_load_dwordx4 v199, s[48:51], s77 offen offset:2048 lds\n" \
"v_add_f32 v118, v118, v158\n" \
"v_mfma_f32_16x16x32_bf16 v[20:23], a[48:51], a[24:27], v[20:23]\n" \
"ds_read_b64_tr_b16 a[222:223], v222 offset:37888\n" \
"v_add_f32 v119, v119, v159\n" \
"v_mfma_f32_16x16x32_bf16 v[20:23], a[52:55], a[28:31], v[20:23]\n" \
"s_waitcnt lgkmcnt(3)\n" \
"v_mul_f32 v112, v112, v232\n" \
"v_mul_f32 v113, v113, v232\n" \
"v_mfma_f32_16x16x32_bf16 v[28:31], a[48:51], a[192:195], v[28:31]\n" \
"v_mul_f32 v114, v114, v232\n" \
"v_mul_f32 v115, v115, v232\n" \
"v_mfma_f32_16x16x32_bf16 v[28:31], a[52:55], a[196:199], v[28:31]\n" \
"v_mul_f32 v116, v116, v232\n" \
"v_mul_f32 v117, v117, v232\n" \
"v_mul_f32 v118, v118, v232\n" \
"v_mfma_f32_16x16x32_bf16 v[24:27], a[40:43], a[192:195], v[24:27]\n" \
"v_mul_f32 v119, v119, v232\n" \
"v_cvt_pk_bf16_f32 v124, v112, v113\n" \
"v_mfma_f32_16x16x32_bf16 v[24:27], a[44:47], a[196:199], v[24:27]\n" \
"v_cvt_pk_bf16_f32 v125, v114, v115\n" \
"v_cvt_pk_bf16_f32 v126, v116, v117\n" \
"v_mfma_f32_16x16x32_bf16 v[64:67], a[32:35], a[192:195], v[64:67]\n" \
"v_cvt_pk_bf16_f32 v127, v118, v119\n" \
"v_mfma_f32_16x16x32_bf16 v[64:67], a[36:39], a[196:199], v[64:67]\n" \
"buffer_store_dwordx4 v[124:127], v240, s[56:59], s88 offen offset:2048 sc1\n" \
"s_waitcnt vmcnt(14) lgkmcnt(0)\n" \
"s_barrier\n" \
"v_mfma_f32_16x16x32_bf16 v[72:75], a[0:3], a[208:211], v[72:75]\n" \
"ds_read_b64_tr_b16 a[32:33], v194 offset:1024\n" \
"ds_read_b64_tr_b16 a[34:35], v194 offset:4096\n" \
"v_mfma_f32_16x16x32_bf16 v[72:75], a[4:7], a[212:215], v[72:75]\n" \
"ds_read_b64_tr_b16 a[36:37], v194 offset:13312\n" \
"ds_read_b64_tr_b16 a[38:39], v194 offset:16384\n" \
"v_mfma_f32_16x16x32_bf16 v[224:227], v[244:247], v[124:127], v[224:227]\n" \
"s_add_u32 s82, s88, 532480\n" \
"v_accvgpr_read_b32 v112, a96\n" \
"v_accvgpr_read_b32 v113, a97\n" \
"v_accvgpr_read_b32 v114, a98\n" \
"v_accvgpr_read_b32 v115, a99\n" \
"v_mfma_f32_16x16x32_bf16 v[80:83], a[8:11], a[208:211], v[80:83]\n" \
"s_add_u32 m0, s79, 0\n" \
"ds_read_b128 a[0:3], v207 offset:45056\n" \
"v_accvgpr_read_b32 v116, a100\n" \
"v_mfma_f32_16x16x32_bf16 v[80:83], a[12:15], a[212:215], v[80:83]\n" \
"buffer_load_dwordx4 v192, s[44:47], s73 offen offset:0 lds\n" \
"v_mfma_f32_16x16x32_bf16 v[84:87], a[8:11], a[216:219], v[84:87]\n" \
"ds_read_b128 a[4:7], v208 offset:45056\n" \
"v_accvgpr_read_b32 v117, a101\n" \
"v_mfma_f32_16x16x32_bf16 v[84:87], a[12:15], a[220:223], v[84:87]\n" \
"ds_read_b64_tr_b16 a[40:41], v194 offset:0\n" \
"v_accvgpr_read_b32 v118, a102\n" \
"v_accvgpr_read_b32 v119, a103\n" \
"v_mfma_f32_16x16x32_bf16 v[44:47], a[200:203], a[8:11], v[44:47]\n" \
"ds_read_b64_tr_b16 a[42:43], v194 offset:3072\n" \
"v_add_f32 v112, v112, v160\n" \
"v_mfma_f32_16x16x32_bf16 v[44:47], a[204:207], a[12:15], v[44:47]\n" \
"buffer_load_dwordx4 v193, s[44:47], s73 offen offset:1024 lds\n" \
"v_mfma_f32_16x16x32_bf16 v[52:55], a[200:203], a[16:19], v[52:55]\n" \
"ds_read_b128 a[8:11], v205 offset:45056\n" \
"v_add_f32 v113, v113, v161\n" \
"v_mfma_f32_16x16x32_bf16 v[52:55], a[204:207], a[20:23], v[52:55]\n" \
"ds_read_b128 a[12:15], v206 offset:45056\n" \
"v_add_f32 v114, v114, v162\n" \
"v_add_f32 v115, v115, v163\n" \
"v_mfma_f32_16x16x32_bf16 v[92:95], a[16:19], a[216:219], v[92:95]\n" \
"ds_read_b64_tr_b16 a[44:45], v194 offset:12288\n" \
"v_add_f32 v116, v116, v164\n" \
"v_mfma_f32_16x16x32_bf16 v[92:95], a[20:23], a[220:223], v[92:95]\n" \
"buffer_load_dwordx4 v192, s[44:47], s73 offen offset:2048 lds\n" \
"v_add_f32 v117, v117, v165\n" \
"v_mfma_f32_16x16x32_bf16 v[88:91], a[16:19], a[208:211], v[88:91]\n" \
"ds_read_b64_tr_b16 a[46:47], v194 offset:15360\n" \
"v_add_f32 v118, v118, v166\n" \
"v_mfma_f32_16x16x32_bf16 v[88:91], a[20:23], a[212:215], v[88:91]\n" \
"ds_read_b64_tr_b16 a[48:49], v195 offset:0\n" \
"v_add_f32 v119, v119, v167\n" \
"v_mfma_f32_16x16x32_bf16 v[96:99], a[24:27], a[208:211], v[96:99]\n" \
"ds_read_b128 a[16:19], v205 offset:49152\n" \
"v_mul_f32 v112, v112, v233\n" \
"v_mfma_f32_16x16x32_bf16 v[96:99], a[28:31], a[212:215], v[96:99]\n" \
"buffer_load_dwordx4 v193, s[44:47], s73 offen offset:3072 lds\n" \
"v_mul_f32 v113, v113, v233\n" \
"v_mfma_f32_16x16x32_bf16 v[100:103], a[24:27], a[216:219], v[100:103]\n" \
"ds_read_b128 a[20:23], v206 offset:49152\n" \
"v_mul_f32 v114, v114, v233\n" \
"v_mfma_f32_16x16x32_bf16 v[100:103], a[28:31], a[220:223], v[100:103]\n" \
"s_add_u32 m0, s89, 0\n" \
"ds_read_b64_tr_b16 a[50:51], v195 offset:3072\n" \
"v_mul_f32 v115, v115, v233\n" \
"v_mfma_f32_16x16x32_bf16 v[60:63], a[200:203], a[24:27], v[60:63]\n" \
"s_xor_b32 s73, s73, 0x80\n" \
"ds_read_b64_tr_b16 a[52:53], v195 offset:12288\n" \
"v_mul_f32 v116, v116, v233\n" \
"v_mfma_f32_16x16x32_bf16 v[60:63], a[204:207], a[28:31], v[60:63]\n" \
"buffer_load_dwordx4 v196, s[44:47], s75 offen lds\n" \
"v_mul_f32 v117, v117, v233\n" \
"v_mfma_f32_16x16x32_bf16 v[68:71], a[200:203], a[192:195], v[68:71]\n" \
"s_xor_b32 s75, s75, 0x80\n" \
"s_mul_i32 s76, s80, 393216\n" \
"ds_read_b128 a[24:27], v205 offset:53248\n" \
"v_mfma_f32_16x16x32_bf16 v[68:71], a[204:207], a[196:199], v[68:71]\n" \
"s_add_u32 s76, s76, s78\n" \
"ds_read_b128 a[28:31], v206 offset:53248\n" \
"v_mul_f32 v118, v118, v233\n" \
"v_mul_f32 v119, v119, v233\n" \
"v_mfma_f32_16x16x32_bf16 v[108:111], a[192:195], a[216:219], v[108:111]\n" \
"s_add_u32 m0, s91, 20480\n" \
"ds_read_b64_tr_b16 a[54:55], v195 offset:15360\n" \
"v_cvt_pk_bf16_f32 v120, v112, v113\n" \
"v_mfma_f32_16x16x32_bf16 v[108:111], a[196:199], a[220:223], v[108:111]\n" \
"buffer_load_dwordx4 v197, s[48:51], s76 offen offset:0 lds\n" \
"v_mfma_f32_16x16x32_bf16 v[104:107], a[192:195], a[208:211], v[104:107]\n" \
"v_cvt_pk_bf16_f32 v121, v114, v115\n" \
"v_cvt_pk_bf16_f32 v122, v116, v117\n" \
"v_mfma_f32_16x16x32_bf16 v[104:107], a[196:199], a[212:215], v[104:107]\n" \
"s_waitcnt lgkmcnt(2)\n" \
"v_cvt_pk_bf16_f32 v123, v118, v119\n" \
"v_mfma_f32_16x16x32_bf16 v[32:35], a[32:35], a[0:3], v[32:35]\n" \
"s_add_u32 s77, s76, 24576\n" \
"ds_read_b128 a[192:195], v205 offset:57344\n" \
"v_mfma_f32_16x16x32_bf16 v[32:35], a[36:39], a[4:7], v[32:35]\n" \
"buffer_load_dwordx4 v198, s[48:51], s76 offen offset:1024 lds\n" \
"v_mfma_f32_16x16x32_bf16 v[40:43], a[32:35], a[8:11], v[40:43]\n" \
"ds_read_b128 a[196:199], v206 offset:57344\n" \
"v_mfma_f32_16x16x32_bf16 v[40:43], a[36:39], a[12:15], v[40:43]\n" \
"ds_read_b64_tr_b16 a[200:201], v195 offset:1024\n" \
"buffer_store_dwordx4 v[120:123], v236, s[56:59], s82 offen offset:0 sc1\n" \
"v_mfma_f32_16x16x32_bf16 v[0:3], a[40:43], a[8:11], v[0:3]\n" \
"s_waitcnt lgkmcnt(3)\n" \
"s_xor_b32 s80, s80, 0x1\n" \
"ds_read_b64_tr_b16 a[202:203], v195 offset:4096\n" \
"v_mfma_f32_16x16x32_bf16 v[224:227], v[244:247], v[120:123], v[224:227]\n" \
"v_accvgpr_read_b32 v112, a104\n" \
"v_accvgpr_read_b32 v113, a105\n" \
"v_mfma_f32_16x16x32_bf16 v[0:3], a[44:47], a[12:15], v[0:3]\n" \
"buffer_load_dwordx4 v199, s[48:51], s76 offen offset:2048 lds\n" \
"v_accvgpr_read_b32 v114, a106\n" \
"v_mfma_f32_16x16x32_bf16 v[4:7], a[48:51], a[8:11], v[4:7]\n" \
"ds_read_b64_tr_b16 a[204:205], v195 offset:13312\n" \
"v_accvgpr_read_b32 v115, a107\n" \
"v_mfma_f32_16x16x32_bf16 v[4:7], a[52:55], a[12:15], v[4:7]\n" \
"ds_read_b64_tr_b16 a[206:207], v195 offset:16384\n" \
"v_accvgpr_read_b32 v116, a108\n" \
"v_mfma_f32_16x16x32_bf16 v[12:15], a[48:51], a[16:19], v[12:15]\n" \
"s_add_u32 m0, m0, 3072\n" \
"ds_read_b64_tr_b16 a[208:209], v194 offset:2048\n" \
"v_accvgpr_read_b32 v117, a109\n" \
"v_mfma_f32_16x16x32_bf16 v[12:15], a[52:55], a[20:23], v[12:15]\n" \
"buffer_load_dwordx4 v197, s[48:51], s77 offen offset:0 lds\n" \
"v_accvgpr_read_b32 v118, a110\n" \
"v_mfma_f32_16x16x32_bf16 v[8:11], a[40:43], a[16:19], v[8:11]\n" \
"ds_read_b64_tr_b16 a[210:211], v194 offset:5120\n" \
"v_accvgpr_read_b32 v119, a111\n" \
"v_mfma_f32_16x16x32_bf16 v[8:11], a[44:47], a[20:23], v[8:11]\n" \
"ds_read_b64_tr_b16 a[212:213], v194 offset:14336\n" \
"v_add_f32 v112, v112, v168\n" \
"v_mfma_f32_16x16x32_bf16 v[48:51], a[32:35], a[16:19], v[48:51]\n" \
"ds_read_b64_tr_b16 a[214:215], v194 offset:17408\n" \
"v_add_f32 v113, v113, v169\n" \
"v_mfma_f32_16x16x32_bf16 v[48:51], a[36:39], a[20:23], v[48:51]\n" \
"buffer_load_dwordx4 v198, s[48:51], s77 offen offset:1024 lds\n" \
"v_add_f32 v114, v114, v170\n" \
"v_mfma_f32_16x16x32_bf16 v[56:59], a[32:35], a[24:27], v[56:59]\n" \
"ds_read_b64_tr_b16 a[216:217], v195 offset:2048\n" \
"v_add_f32 v115, v115, v171\n" \
"v_mfma_f32_16x16x32_bf16 v[56:59], a[36:39], a[28:31], v[56:59]\n" \
"ds_read_b64_tr_b16 a[218:219], v195 offset:5120\n" \
"v_add_f32 v116, v116, v172\n" \
"v_add_f32 v117, v117, v173\n" \
"v_mfma_f32_16x16x32_bf16 v[16:19], a[40:43], a[24:27], v[16:19]\n" \
"ds_read_b64_tr_b16 a[220:221], v195 offset:14336\n" \
"v_add_f32 v118, v118, v174\n" \
"v_mfma_f32_16x16x32_bf16 v[16:19], a[44:47], a[28:31], v[16:19]\n" \
"buffer_load_dwordx4 v199, s[48:51], s77 offen offset:2048 lds\n" \
"v_mfma_f32_16x16x32_bf16 v[20:23], a[48:51], a[24:27], v[20:23]\n" \
"ds_read_b64_tr_b16 a[222:223], v195 offset:17408\n" \
"v_add_f32 v119, v119, v175\n" \
"v_mfma_f32_16x16x32_bf16 v[20:23], a[52:55], a[28:31], v[20:23]\n" \
"s_waitcnt lgkmcnt(3)\n" \
"v_mul_f32 v112, v112, v233\n" \
"v_mul_f32 v113, v113, v233\n" \
"v_mul_f32 v114, v114, v233\n" \
"v_mfma_f32_16x16x32_bf16 v[28:31], a[48:51], a[192:195], v[28:31]\n" \
"v_mul_f32 v115, v115, v233\n" \
"v_mul_f32 v116, v116, v233\n" \
"v_mfma_f32_16x16x32_bf16 v[28:31], a[52:55], a[196:199], v[28:31]\n" \
"v_mul_f32 v117, v117, v233\n" \
"v_mul_f32 v118, v118, v233\n" \
"v_mfma_f32_16x16x32_bf16 v[24:27], a[40:43], a[192:195], v[24:27]\n" \
"v_mul_f32 v119, v119, v233\n" \
"v_cvt_pk_bf16_f32 v124, v112, v113\n" \
"v_mfma_f32_16x16x32_bf16 v[24:27], a[44:47], a[196:199], v[24:27]\n" \
"v_cvt_pk_bf16_f32 v125, v114, v115\n" \
"v_cvt_pk_bf16_f32 v126, v116, v117\n" \
"v_cvt_pk_bf16_f32 v127, v118, v119\n" \
"v_mfma_f32_16x16x32_bf16 v[64:67], a[32:35], a[192:195], v[64:67]\n" \
"buffer_store_dwordx4 v[124:127], v236, s[56:59], s82 offen offset:2048 sc1\n" \
"v_mfma_f32_16x16x32_bf16 v[64:67], a[36:39], a[196:199], v[64:67]\n" \
"v_mfma_f32_16x16x32_bf16 v[224:227], v[244:247], v[124:127], v[224:227]\n" \
"v_accvgpr_read_b32 v112, a112\n" \
"s_waitcnt vmcnt(14) lgkmcnt(0)\n" \
"s_barrier\n" \
"v_mfma_f32_16x16x32_bf16 v[72:75], a[0:3], a[208:211], v[72:75]\n" \
"ds_read_b64_tr_b16 a[32:33], v209 offset:1024\n" \
"ds_read_b64_tr_b16 a[34:35], v209 offset:4096\n" \
"v_mfma_f32_16x16x32_bf16 v[72:75], a[4:7], a[212:215], v[72:75]\n" \
"ds_read_b64_tr_b16 a[36:37], v209 offset:13312\n" \
"ds_read_b64_tr_b16 a[38:39], v209 offset:16384\n" \
"v_mfma_f32_16x16x32_bf16 v[80:83], a[8:11], a[208:211], v[80:83]\n" \
"s_add_u32 m0, s79, 110592\n" \
"ds_read_b128 a[0:3], v207 offset:0\n" \
"v_accvgpr_read_b32 v113, a113\n" \
"v_mfma_f32_16x16x32_bf16 v[80:83], a[12:15], a[212:215], v[80:83]\n" \
"buffer_load_dwordx4 v192, s[44:47], s73 offen offset:0 lds\n" \
"v_accvgpr_read_b32 v114, a114\n" \
"v_mfma_f32_16x16x32_bf16 v[84:87], a[8:11], a[216:219], v[84:87]\n" \
"ds_read_b128 a[4:7], v208 offset:0\n" \
"v_accvgpr_read_b32 v115, a115\n" \
"v_mfma_f32_16x16x32_bf16 v[84:87], a[12:15], a[220:223], v[84:87]\n" \
"ds_read_b64_tr_b16 a[40:41], v209 offset:0\n" \
"v_accvgpr_read_b32 v116, a116\n" \
"v_mfma_f32_16x16x32_bf16 v[44:47], a[200:203], a[8:11], v[44:47]\n" \
"ds_read_b64_tr_b16 a[42:43], v209 offset:3072\n" \
"v_accvgpr_read_b32 v117, a117\n" \
"v_mfma_f32_16x16x32_bf16 v[44:47], a[204:207], a[12:15], v[44:47]\n" \
"buffer_load_dwordx4 v193, s[44:47], s73 offen offset:1024 lds\n" \
"v_accvgpr_read_b32 v118, a118\n" \
"v_mfma_f32_16x16x32_bf16 v[52:55], a[200:203], a[16:19], v[52:55]\n" \
"ds_read_b128 a[8:11], v205 offset:0\n" \
"v_accvgpr_read_b32 v119, a119\n" \
"v_mfma_f32_16x16x32_bf16 v[52:55], a[204:207], a[20:23], v[52:55]\n" \
"ds_read_b128 a[12:15], v206 offset:0\n" \
"v_add_f32 v112, v112, v176\n" \
"v_add_f32 v113, v113, v177\n" \
"v_mfma_f32_16x16x32_bf16 v[92:95], a[16:19], a[216:219], v[92:95]\n" \
"ds_read_b64_tr_b16 a[44:45], v209 offset:12288\n" \
"v_add_f32 v114, v114, v178\n" \
"v_mfma_f32_16x16x32_bf16 v[92:95], a[20:23], a[220:223], v[92:95]\n" \
"buffer_load_dwordx4 v192, s[44:47], s73 offen offset:2048 lds\n" \
"v_mfma_f32_16x16x32_bf16 v[88:91], a[16:19], a[208:211], v[88:91]\n" \
"ds_read_b64_tr_b16 a[46:47], v209 offset:15360\n" \
"v_add_f32 v115, v115, v179\n" \
"v_mfma_f32_16x16x32_bf16 v[88:91], a[20:23], a[212:215], v[88:91]\n" \
"ds_read_b64_tr_b16 a[48:49], v210 offset:0\n" \
"v_add_f32 v116, v116, v180\n" \
"v_add_f32 v117, v117, v181\n" \
"v_mfma_f32_16x16x32_bf16 v[96:99], a[24:27], a[208:211], v[96:99]\n" \
"ds_read_b128 a[16:19], v205 offset:4096\n" \
"v_add_f32 v118, v118, v182\n" \
"v_mfma_f32_16x16x32_bf16 v[96:99], a[28:31], a[212:215], v[96:99]\n" \
"buffer_load_dwordx4 v193, s[44:47], s73 offen offset:3072 lds\n" \
"v_mfma_f32_16x16x32_bf16 v[100:103], a[24:27], a[216:219], v[100:103]\n" \
"ds_read_b128 a[20:23], v206 offset:4096\n" \
"v_add_f32 v119, v119, v183\n" \
"v_mfma_f32_16x16x32_bf16 v[100:103], a[28:31], a[220:223], v[100:103]\n" \
"s_add_u32 m0, s89, 110592\n" \
"ds_read_b64_tr_b16 a[50:51], v210 offset:3072\n" \
"v_mul_f32 v112, v112, v233\n" \
"v_mul_f32 v113, v113, v233\n" \
"v_mfma_f32_16x16x32_bf16 v[60:63], a[200:203], a[24:27], v[60:63]\n" \
"s_xor_b32 s73, s73, 0x180\n" \
"ds_read_b64_tr_b16 a[52:53], v210 offset:12288\n" \
"v_mul_f32 v114, v114, v233\n" \
"v_mfma_f32_16x16x32_bf16 v[60:63], a[204:207], a[28:31], v[60:63]\n" \
"buffer_load_dwordx4 v196, s[44:47], s75 offen lds\n" \
"v_mul_f32 v115, v115, v233\n" \
"v_mfma_f32_16x16x32_bf16 v[68:71], a[200:203], a[192:195], v[68:71]\n" \
"s_xor_b32 s75, s75, 0x180\n" \
"s_mul_i32 s76, s80, 393216\n" \
"ds_read_b128 a[24:27], v205 offset:8192\n" \
"v_mfma_f32_16x16x32_bf16 v[68:71], a[204:207], a[196:199], v[68:71]\n" \
"s_add_u32 s76, s76, s78\n" \
"ds_read_b128 a[28:31], v206 offset:8192\n" \
"v_mul_f32 v116, v116, v233\n" \
"v_mfma_f32_16x16x32_bf16 v[108:111], a[192:195], a[216:219], v[108:111]\n" \
"s_add_u32 m0, s91, 139264\n" \
"ds_read_b64_tr_b16 a[54:55], v210 offset:15360\n" \
"v_mul_f32 v117, v117, v233\n" \
"v_mfma_f32_16x16x32_bf16 v[108:111], a[196:199], a[220:223], v[108:111]\n" \
"buffer_load_dwordx4 v197, s[48:51], s76 offen offset:0 lds\n" \
"v_mul_f32 v118, v118, v233\n" \
"v_mfma_f32_16x16x32_bf16 v[104:107], a[192:195], a[208:211], v[104:107]\n" \
"v_mul_f32 v119, v119, v233\n" \
"v_cvt_pk_bf16_f32 v120, v112, v113\n" \
"v_mfma_f32_16x16x32_bf16 v[104:107], a[196:199], a[212:215], v[104:107]\n" \
"s_waitcnt lgkmcnt(2)\n" \
"v_cvt_pk_bf16_f32 v121, v114, v115\n" \
"v_cvt_pk_bf16_f32 v122, v116, v117\n" \
"v_mfma_f32_16x16x32_bf16 v[32:35], a[32:35], a[0:3], v[32:35]\n" \
"s_add_u32 s77, s76, 24576\n" \
"ds_read_b128 a[192:195], v205 offset:12288\n" \
"v_cvt_pk_bf16_f32 v123, v118, v119\n" \
"v_mfma_f32_16x16x32_bf16 v[32:35], a[36:39], a[4:7], v[32:35]\n" \
"buffer_load_dwordx4 v198, s[48:51], s76 offen offset:1024 lds\n" \
"v_mfma_f32_16x16x32_bf16 v[40:43], a[32:35], a[8:11], v[40:43]\n" \
"ds_read_b128 a[196:199], v206 offset:12288\n" \
"v_mfma_f32_16x16x32_bf16 v[40:43], a[36:39], a[12:15], v[40:43]\n" \
"ds_read_b64_tr_b16 a[200:201], v210 offset:1024\n" \
"buffer_store_dwordx4 v[120:123], v240, s[56:59], s82 offen offset:0 sc1\n" \
"v_mfma_f32_16x16x32_bf16 v[0:3], a[40:43], a[8:11], v[0:3]\n" \
"s_waitcnt lgkmcnt(3)\n" \
"s_xor_b32 s80, s80, 0x3\n" \
"ds_read_b64_tr_b16 a[202:203], v210 offset:4096\n" \
"v_mfma_f32_16x16x32_bf16 v[224:227], v[244:247], v[120:123], v[224:227]\n" \
"v_accvgpr_read_b32 v112, a120\n" \
"v_accvgpr_read_b32 v113, a121\n" \
"v_mfma_f32_16x16x32_bf16 v[0:3], a[44:47], a[12:15], v[0:3]\n" \
"buffer_load_dwordx4 v199, s[48:51], s76 offen offset:2048 lds\n" \
"v_mfma_f32_16x16x32_bf16 v[4:7], a[48:51], a[8:11], v[4:7]\n" \
"ds_read_b64_tr_b16 a[204:205], v210 offset:13312\n" \
"v_accvgpr_read_b32 v114, a122\n" \
"v_mfma_f32_16x16x32_bf16 v[4:7], a[52:55], a[12:15], v[4:7]\n" \
"ds_read_b64_tr_b16 a[206:207], v210 offset:16384\n" \
"v_accvgpr_read_b32 v115, a123\n" \
"v_accvgpr_read_b32 v116, a124\n" \
"v_mfma_f32_16x16x32_bf16 v[12:15], a[48:51], a[16:19], v[12:15]\n" \
"s_add_u32 m0, m0, 3072\n" \
"ds_read_b64_tr_b16 a[208:209], v209 offset:2048\n" \
"v_accvgpr_read_b32 v117, a125\n" \
"v_mfma_f32_16x16x32_bf16 v[12:15], a[52:55], a[20:23], v[12:15]\n" \
"buffer_load_dwordx4 v197, s[48:51], s77 offen offset:0 lds\n" \
"v_mfma_f32_16x16x32_bf16 v[8:11], a[40:43], a[16:19], v[8:11]\n" \
"ds_read_b64_tr_b16 a[210:211], v209 offset:5120\n" \
"v_accvgpr_read_b32 v118, a126\n" \
"v_mfma_f32_16x16x32_bf16 v[8:11], a[44:47], a[20:23], v[8:11]\n" \
"ds_read_b64_tr_b16 a[212:213], v209 offset:14336\n" \
"v_accvgpr_read_b32 v119, a127\n" \
"v_add_f32 v112, v112, v184\n" \
"v_mfma_f32_16x16x32_bf16 v[48:51], a[32:35], a[16:19], v[48:51]\n" \
"ds_read_b64_tr_b16 a[214:215], v209 offset:17408\n" \
"v_add_f32 v113, v113, v185\n" \
"v_mfma_f32_16x16x32_bf16 v[48:51], a[36:39], a[20:23], v[48:51]\n" \
"buffer_load_dwordx4 v198, s[48:51], s77 offen offset:1024 lds\n" \
"v_add_f32 v114, v114, v186\n" \
"v_mfma_f32_16x16x32_bf16 v[56:59], a[32:35], a[24:27], v[56:59]\n" \
"ds_read_b64_tr_b16 a[216:217], v210 offset:2048\n" \
"v_add_f32 v115, v115, v187\n" \
"v_mfma_f32_16x16x32_bf16 v[56:59], a[36:39], a[28:31], v[56:59]\n" \
"ds_read_b64_tr_b16 a[218:219], v210 offset:5120\n" \
"v_add_f32 v116, v116, v188\n" \
"v_mfma_f32_16x16x32_bf16 v[16:19], a[40:43], a[24:27], v[16:19]\n" \
"ds_read_b64_tr_b16 a[220:221], v210 offset:14336\n" \
"v_add_f32 v117, v117, v189\n" \
"v_mfma_f32_16x16x32_bf16 v[16:19], a[44:47], a[28:31], v[16:19]\n" \
"buffer_load_dwordx4 v199, s[48:51], s77 offen offset:2048 lds\n" \
"v_add_f32 v118, v118, v190\n" \
"v_mfma_f32_16x16x32_bf16 v[20:23], a[48:51], a[24:27], v[20:23]\n" \
"ds_read_b64_tr_b16 a[222:223], v210 offset:17408\n" \
"v_add_f32 v119, v119, v191\n" \
"v_mfma_f32_16x16x32_bf16 v[20:23], a[52:55], a[28:31], v[20:23]\n" \
"s_waitcnt lgkmcnt(3)\n" \
"v_mul_f32 v112, v112, v233\n" \
"v_mul_f32 v113, v113, v233\n" \
"v_mfma_f32_16x16x32_bf16 v[28:31], a[48:51], a[192:195], v[28:31]\n" \
"v_mul_f32 v114, v114, v233\n" \
"v_mul_f32 v115, v115, v233\n" \
"v_mfma_f32_16x16x32_bf16 v[28:31], a[52:55], a[196:199], v[28:31]\n" \
"v_mul_f32 v116, v116, v233\n" \
"v_mul_f32 v117, v117, v233\n" \
"v_mul_f32 v118, v118, v233\n" \
"v_mfma_f32_16x16x32_bf16 v[24:27], a[40:43], a[192:195], v[24:27]\n" \
"v_mul_f32 v119, v119, v233\n" \
"v_cvt_pk_bf16_f32 v124, v112, v113\n" \
"v_mfma_f32_16x16x32_bf16 v[24:27], a[44:47], a[196:199], v[24:27]\n" \
"v_cvt_pk_bf16_f32 v125, v114, v115\n" \
"v_cvt_pk_bf16_f32 v126, v116, v117\n" \
"v_cvt_pk_bf16_f32 v127, v118, v119\n" \
"v_mfma_f32_16x16x32_bf16 v[64:67], a[32:35], a[192:195], v[64:67]\n" \
"buffer_store_dwordx4 v[124:127], v240, s[56:59], s82 offen offset:2048 sc1\n" \
"v_mfma_f32_16x16x32_bf16 v[64:67], a[36:39], a[196:199], v[64:67]\n" \
"v_mfma_f32_16x16x32_bf16 v[224:227], v[244:247], v[124:127], v[224:227]\n" \
"s_waitcnt vmcnt(14) lgkmcnt(0)\n" \
"s_barrier\n" \
"v_mfma_f32_16x16x32_bf16 v[72:75], a[0:3], a[208:211], v[72:75]\n" \
"ds_read_b64_tr_b16 a[32:33], v204 offset:21504\n" \
"ds_read_b64_tr_b16 a[34:35], v204 offset:24576\n" \
"v_mfma_f32_16x16x32_bf16 v[72:75], a[4:7], a[212:215], v[72:75]\n" \
"ds_read_b64_tr_b16 a[36:37], v204 offset:33792\n" \
"ds_read_b64_tr_b16 a[38:39], v204 offset:36864\n" \
"v_mfma_f32_16x16x32_bf16 v[80:83], a[8:11], a[208:211], v[80:83]\n" \
"s_add_u32 m0, s79, 65536\n" \
"ds_read_b128 a[0:3], v202 offset:0\n" \
"v_mfma_f32_16x16x32_bf16 v[80:83], a[12:15], a[212:215], v[80:83]\n" \
"buffer_load_dwordx4 v192, s[44:47], s73 offen offset:0 lds\n" \
"v_mfma_f32_16x16x32_bf16 v[84:87], a[8:11], a[216:219], v[84:87]\n" \
"ds_read_b128 a[4:7], v203 offset:0\n" \
"v_mfma_f32_16x16x32_bf16 v[84:87], a[12:15], a[220:223], v[84:87]\n" \
"ds_read_b64_tr_b16 a[40:41], v204 offset:20480\n" \
"v_mfma_f32_16x16x32_bf16 v[44:47], a[200:203], a[8:11], v[44:47]\n" \
"ds_read_b64_tr_b16 a[42:43], v204 offset:23552\n" \
"v_mfma_f32_16x16x32_bf16 v[44:47], a[204:207], a[12:15], v[44:47]\n" \
"buffer_load_dwordx4 v193, s[44:47], s73 offen offset:1024 lds\n" \
"v_mfma_f32_16x16x32_bf16 v[52:55], a[200:203], a[16:19], v[52:55]\n" \
"ds_read_b128 a[8:11], v200 offset:0\n" \
"v_mfma_f32_16x16x32_bf16 v[52:55], a[204:207], a[20:23], v[52:55]\n" \
"ds_read_b128 a[12:15], v201 offset:0\n" \
"v_mfma_f32_16x16x32_bf16 v[92:95], a[16:19], a[216:219], v[92:95]\n" \
"ds_read_b64_tr_b16 a[44:45], v204 offset:32768\n" \
"v_mfma_f32_16x16x32_bf16 v[92:95], a[20:23], a[220:223], v[92:95]\n" \
"buffer_load_dwordx4 v192, s[44:47], s73 offen offset:2048 lds\n" \
"v_mfma_f32_16x16x32_bf16 v[88:91], a[16:19], a[208:211], v[88:91]\n" \
"ds_read_b64_tr_b16 a[46:47], v204 offset:35840\n" \
"v_mfma_f32_16x16x32_bf16 v[88:91], a[20:23], a[212:215], v[88:91]\n" \
"ds_read_b64_tr_b16 a[48:49], v222 offset:20480\n" \
"v_mfma_f32_16x16x32_bf16 v[96:99], a[24:27], a[208:211], v[96:99]\n" \
"ds_read_b128 a[16:19], v200 offset:4096\n" \
"v_mfma_f32_16x16x32_bf16 v[96:99], a[28:31], a[212:215], v[96:99]\n" \
"buffer_load_dwordx4 v193, s[44:47], s73 offen offset:3072 lds\n" \
"v_mfma_f32_16x16x32_bf16 v[100:103], a[24:27], a[216:219], v[100:103]\n" \
"ds_read_b128 a[20:23], v201 offset:4096\n" \
"v_mfma_f32_16x16x32_bf16 v[100:103], a[28:31], a[220:223], v[100:103]\n" \
"s_add_u32 m0, s89, 65536\n" \
"ds_read_b64_tr_b16 a[50:51], v222 offset:23552\n" \
"v_mfma_f32_16x16x32_bf16 v[60:63], a[200:203], a[24:27], v[60:63]\n" \
"s_xor_b32 s73, s73, 0x80\n" \
"ds_read_b64_tr_b16 a[52:53], v222 offset:32768\n" \
"v_mfma_f32_16x16x32_bf16 v[60:63], a[204:207], a[28:31], v[60:63]\n" \
"buffer_load_dwordx4 v196, s[44:47], s75 offen lds\n" \
"v_mfma_f32_16x16x32_bf16 v[68:71], a[200:203], a[192:195], v[68:71]\n" \
"s_xor_b32 s75, s75, 0x80\n" \
"s_mul_i32 s76, s80, 393216\n" \
"ds_read_b128 a[24:27], v200 offset:8192\n" \
"v_mfma_f32_16x16x32_bf16 v[68:71], a[204:207], a[196:199], v[68:71]\n" \
"s_add_u32 s76, s76, s78\n" \
"ds_read_b128 a[28:31], v201 offset:8192\n" \
"v_mfma_f32_16x16x32_bf16 v[108:111], a[192:195], a[216:219], v[108:111]\n" \
"s_add_u32 m0, s91, 86016\n" \
"ds_read_b64_tr_b16 a[54:55], v222 offset:35840\n" \
"v_mfma_f32_16x16x32_bf16 v[108:111], a[196:199], a[220:223], v[108:111]\n" \
"buffer_load_dwordx4 v197, s[48:51], s76 offen offset:0 lds\n" \
"v_mfma_f32_16x16x32_bf16 v[104:107], a[192:195], a[208:211], v[104:107]\n" \
"v_mfma_f32_16x16x32_bf16 v[104:107], a[196:199], a[212:215], v[104:107]\n" \
"s_waitcnt lgkmcnt(2)\n" \
"v_mfma_f32_16x16x32_bf16 v[32:35], a[32:35], a[0:3], v[32:35]\n" \
"s_add_u32 s77, s76, 24576\n" \
"ds_read_b128 a[192:195], v200 offset:12288\n" \
"v_mfma_f32_16x16x32_bf16 v[32:35], a[36:39], a[4:7], v[32:35]\n" \
"buffer_load_dwordx4 v198, s[48:51], s76 offen offset:1024 lds\n" \
"v_mfma_f32_16x16x32_bf16 v[40:43], a[32:35], a[8:11], v[40:43]\n" \
"ds_read_b128 a[196:199], v201 offset:12288\n" \
"v_mfma_f32_16x16x32_bf16 v[40:43], a[36:39], a[12:15], v[40:43]\n" \
"ds_read_b64_tr_b16 a[200:201], v222 offset:21504\n" \
"v_mfma_f32_16x16x32_bf16 v[0:3], a[40:43], a[8:11], v[0:3]\n" \
"s_waitcnt lgkmcnt(3)\n" \
"s_xor_b32 s80, s80, 0x1\n" \
"ds_read_b64_tr_b16 a[202:203], v222 offset:24576\n" \
"v_mfma_f32_16x16x32_bf16 v[0:3], a[44:47], a[12:15], v[0:3]\n" \
"buffer_load_dwordx4 v199, s[48:51], s76 offen offset:2048 lds\n" \
"v_mfma_f32_16x16x32_bf16 v[4:7], a[48:51], a[8:11], v[4:7]\n" \
"ds_read_b64_tr_b16 a[204:205], v222 offset:33792\n" \
"v_mfma_f32_16x16x32_bf16 v[4:7], a[52:55], a[12:15], v[4:7]\n" \
"ds_read_b64_tr_b16 a[206:207], v222 offset:36864\n" \
"v_mfma_f32_16x16x32_bf16 v[12:15], a[48:51], a[16:19], v[12:15]\n" \
"s_add_u32 m0, m0, 3072\n" \
"ds_read_b64_tr_b16 a[208:209], v204 offset:22528\n" \
"v_mfma_f32_16x16x32_bf16 v[12:15], a[52:55], a[20:23], v[12:15]\n" \
"buffer_load_dwordx4 v197, s[48:51], s77 offen offset:0 lds\n" \
"v_mfma_f32_16x16x32_bf16 v[8:11], a[40:43], a[16:19], v[8:11]\n" \
"ds_read_b64_tr_b16 a[210:211], v204 offset:25600\n" \
"v_mfma_f32_16x16x32_bf16 v[8:11], a[44:47], a[20:23], v[8:11]\n" \
"ds_read_b64_tr_b16 a[212:213], v204 offset:34816\n" \
"v_mfma_f32_16x16x32_bf16 v[48:51], a[32:35], a[16:19], v[48:51]\n" \
"ds_read_b64_tr_b16 a[214:215], v204 offset:37888\n" \
"v_mfma_f32_16x16x32_bf16 v[48:51], a[36:39], a[20:23], v[48:51]\n" \
"buffer_load_dwordx4 v198, s[48:51], s77 offen offset:1024 lds\n" \
"v_mfma_f32_16x16x32_bf16 v[56:59], a[32:35], a[24:27], v[56:59]\n" \
"ds_read_b64_tr_b16 a[216:217], v222 offset:22528\n" \
"v_mfma_f32_16x16x32_bf16 v[56:59], a[36:39], a[28:31], v[56:59]\n" \
"ds_read_b64_tr_b16 a[218:219], v222 offset:25600\n" \
"v_mfma_f32_16x16x32_bf16 v[16:19], a[40:43], a[24:27], v[16:19]\n" \
"ds_read_b64_tr_b16 a[220:221], v222 offset:34816\n" \
"v_mfma_f32_16x16x32_bf16 v[16:19], a[44:47], a[28:31], v[16:19]\n" \
"buffer_load_dwordx4 v199, s[48:51], s77 offen offset:2048 lds\n" \
"v_mfma_f32_16x16x32_bf16 v[20:23], a[48:51], a[24:27], v[20:23]\n" \
"ds_read_b64_tr_b16 a[222:223], v222 offset:37888\n" \
"v_mfma_f32_16x16x32_bf16 v[20:23], a[52:55], a[28:31], v[20:23]\n" \
"s_waitcnt lgkmcnt(3)\n" \
"v_mfma_f32_16x16x32_bf16 v[28:31], a[48:51], a[192:195], v[28:31]\n" \
"v_mfma_f32_16x16x32_bf16 v[28:31], a[52:55], a[196:199], v[28:31]\n" \
"v_mfma_f32_16x16x32_bf16 v[24:27], a[40:43], a[192:195], v[24:27]\n" \
"v_mfma_f32_16x16x32_bf16 v[24:27], a[44:47], a[196:199], v[24:27]\n" \
"v_mfma_f32_16x16x32_bf16 v[64:67], a[32:35], a[192:195], v[64:67]\n" \
"v_mfma_f32_16x16x32_bf16 v[64:67], a[36:39], a[196:199], v[64:67]\n" \
"s_waitcnt vmcnt(12) lgkmcnt(0)\n" \
"s_barrier\n" \
"s_movk_i32 s64, 0x207\n" \
"v_cmp_class_f32 vcc, v224, s64\n" \
"s_nop 1\n" \
"s_cmp_lg_u64 vcc, 0\n" \
"s_addc_u32 s90, s90, 0\n" \
"s_cmp_lg_u32 s90, 0\n" \
"s_cbranch_scc1 Lexact_%=\n" \
"Lexr_%=:\n" \
"v_mfma_f32_16x16x32_bf16 v[72:75], a[0:3], a[208:211], v[72:75]\n" \
"s_lshl_b32 s15, s63, 7\n" \
"ds_read_b64_tr_b16 a[32:33], v194 offset:1024\n" \
"ds_read_b64_tr_b16 a[34:35], v194 offset:4096\n" \
"v_mfma_f32_16x16x32_bf16 v[72:75], a[4:7], a[212:215], v[72:75]\n" \
"s_lshl2_add_u32 s16, s62, 65589248\n" \
"ds_read_b64_tr_b16 a[36:37], v194 offset:13312\n" \
"ds_read_b64_tr_b16 a[38:39], v194 offset:16384\n" \
"v_mfma_f32_16x16x32_bf16 v[80:83], a[8:11], a[208:211], v[80:83]\n" \
"s_add_u32 m0, s79, 0\n" \
"s_sub_u32 s17, s16, s15\n" \
"ds_read_b128 a[0:3], v207 offset:45056\n" \
"v_mfma_f32_16x16x32_bf16 v[80:83], a[12:15], a[212:215], v[80:83]\n" \
"buffer_load_dwordx4 v192, s[44:47], s73 offen offset:0 lds\n" \
"v_mfma_f32_16x16x32_bf16 v[84:87], a[8:11], a[216:219], v[84:87]\n" \
"s_add_u32 s15, s15, s16\n" \
"s_add_u32 s17, s17, 1920\n" \
"ds_read_b128 a[4:7], v208 offset:45056\n" \
"v_mfma_f32_16x16x32_bf16 v[84:87], a[12:15], a[220:223], v[84:87]\n" \
"ds_read_b64_tr_b16 a[40:41], v194 offset:0\n" \
"v_mov_b32 v146, s60\n" \
"v_mfma_f32_16x16x32_bf16 v[44:47], a[200:203], a[8:11], v[44:47]\n" \
"ds_read_b64_tr_b16 a[42:43], v194 offset:3072\n" \
"v_lshlrev_b32 v147, 28, s61\n" \
"v_mfma_f32_16x16x32_bf16 v[44:47], a[204:207], a[12:15], v[44:47]\n" \
"buffer_load_dwordx4 v193, s[44:47], s73 offen offset:1024 lds\n" \
"v_mfma_f32_16x16x32_bf16 v[52:55], a[200:203], a[16:19], v[52:55]\n" \
"ds_read_b128 a[8:11], v205 offset:45056\n" \
"v_mfma_f32_16x16x32_bf16 v[52:55], a[204:207], a[20:23], v[52:55]\n" \
"ds_read_b128 a[12:15], v206 offset:45056\n" \
"v_mfma_f32_16x16x32_bf16 v[92:95], a[16:19], a[216:219], v[92:95]\n" \
"ds_read_b64_tr_b16 a[44:45], v194 offset:12288\n" \
"v_mfma_f32_16x16x32_bf16 v[92:95], a[20:23], a[220:223], v[92:95]\n" \
"buffer_load_dwordx4 v192, s[44:47], s73 offen offset:2048 lds\n" \
"v_mfma_f32_16x16x32_bf16 v[88:91], a[16:19], a[208:211], v[88:91]\n" \
"ds_read_b64_tr_b16 a[46:47], v194 offset:15360\n" \
"v_mfma_f32_16x16x32_bf16 v[88:91], a[20:23], a[212:215], v[88:91]\n" \
"ds_read_b64_tr_b16 a[48:49], v195 offset:0\n" \
"v_mfma_f32_16x16x32_bf16 v[96:99], a[24:27], a[208:211], v[96:99]\n" \
"Lpg8504_%=:\n" \
"Lst1x1_%=:\n" \
"s_cbranch_execz Lpg10822_%=\n" \
"ds_read_b128 a[16:19], v205 offset:49152\n" \
"v_mfma_f32_16x16x32_bf16 v[96:99], a[28:31], a[212:215], v[96:99]\n" \
"buffer_load_dwordx4 v193, s[44:47], s73 offen offset:3072 lds\n" \
"v_mfma_f32_16x16x32_bf16 v[100:103], a[24:27], a[216:219], v[100:103]\n" \
"ds_read_b128 a[20:23], v206 offset:49152\n" \
"v_mfma_f32_16x16x32_bf16 v[100:103], a[28:31], a[220:223], v[100:103]\n" \
"s_add_u32 m0, s89, 0\n" \
"ds_read_b64_tr_b16 a[50:51], v195 offset:3072\n" \
"v_mfma_f32_16x16x32_bf16 v[60:63], a[200:203], a[24:27], v[60:63]\n" \
"s_xor_b32 s73, s73, 0x780\n" \
"ds_read_b64_tr_b16 a[52:53], v195 offset:12288\n" \
"v_mfma_f32_16x16x32_bf16 v[60:63], a[204:207], a[28:31], v[60:63]\n" \
"buffer_load_dwordx4 v196, s[44:47], s75 offen lds\n" \
"v_mfma_f32_16x16x32_bf16 v[68:71], a[200:203], a[192:195], v[68:71]\n" \
"s_xor_b32 s75, s75, 0x780\n" \
"s_mul_i32 s76, s80, 393216\n" \
"ds_read_b128 a[24:27], v205 offset:53248\n" \
"v_mfma_f32_16x16x32_bf16 v[68:71], a[204:207], a[196:199], v[68:71]\n" \
"s_add_u32 s76, s76, s78\n" \
"ds_read_b128 a[28:31], v206 offset:53248\n" \
"v_mfma_f32_16x16x32_bf16 v[108:111], a[192:195], a[216:219], v[108:111]\n" \
"s_add_u32 m0, s91, 20480\n" \
"ds_read_b64_tr_b16 a[54:55], v195 offset:15360\n" \
"v_mfma_f32_16x16x32_bf16 v[108:111], a[196:199], a[220:223], v[108:111]\n" \
"buffer_load_dwordx4 v197, s[48:51], s76 offen offset:0 lds\n" \
"v_mfma_f32_16x16x32_bf16 v[104:107], a[192:195], a[208:211], v[104:107]\n" \
"v_mfma_f32_16x16x32_bf16 v[104:107], a[196:199], a[212:215], v[104:107]\n" \
"s_waitcnt lgkmcnt(2)\n" \
"v_mfma_f32_16x16x32_bf16 v[32:35], a[32:35], a[0:3], v[32:35]\n" \
"s_add_u32 s77, s76, 24576\n" \
"ds_read_b128 a[192:195], v205 offset:57344\n" \
"v_mfma_f32_16x16x32_bf16 v[32:35], a[36:39], a[4:7], v[32:35]\n" \
"buffer_load_dwordx4 v198, s[48:51], s76 offen offset:1024 lds\n" \
"v_mfma_f32_16x16x32_bf16 v[40:43], a[32:35], a[8:11], v[40:43]\n" \
"ds_read_b128 a[196:199], v206 offset:57344\n" \
"v_mfma_f32_16x16x32_bf16 v[40:43], a[36:39], a[12:15], v[40:43]\n" \
"ds_read_b64_tr_b16 a[200:201], v195 offset:1024\n" \
"v_mfma_f32_16x16x32_bf16 v[0:3], a[40:43], a[8:11], v[0:3]\n" \
"s_waitcnt lgkmcnt(3)\n" \
"s_xor_b32 s80, s80, 0xf\n" \
"ds_read_b64_tr_b16 a[202:203], v195 offset:4096\n" \
"v_mfma_f32_16x16x32_bf16 v[0:3], a[44:47], a[12:15], v[0:3]\n" \
"buffer_load_dwordx4 v199, s[48:51], s76 offen offset:2048 lds\n" \
"v_mfma_f32_16x16x32_bf16 v[4:7], a[48:51], a[8:11], v[4:7]\n" \
"ds_read_b64_tr_b16 a[204:205], v195 offset:13312\n" \
"v_mfma_f32_16x16x32_bf16 v[4:7], a[52:55], a[12:15], v[4:7]\n" \
"ds_read_b64_tr_b16 a[206:207], v195 offset:16384\n" \
"v_mfma_f32_16x16x32_bf16 v[12:15], a[48:51], a[16:19], v[12:15]\n" \
"s_add_u32 m0, m0, 3072\n" \
"ds_read_b64_tr_b16 a[208:209], v194 offset:2048\n" \
"v_mfma_f32_16x16x32_bf16 v[12:15], a[52:55], a[20:23], v[12:15]\n" \
"buffer_load_dwordx4 v197, s[48:51], s77 offen offset:0 lds\n" \
"v_mfma_f32_16x16x32_bf16 v[8:11], a[40:43], a[16:19], v[8:11]\n" \
"ds_read_b64_tr_b16 a[210:211], v194 offset:5120\n" \
"v_mfma_f32_16x16x32_bf16 v[8:11], a[44:47], a[20:23], v[8:11]\n" \
"ds_read_b64_tr_b16 a[212:213], v194 offset:14336\n" \
"v_mfma_f32_16x16x32_bf16 v[48:51], a[32:35], a[16:19], v[48:51]\n" \
"ds_read_b64_tr_b16 a[214:215], v194 offset:17408\n" \
"v_mfma_f32_16x16x32_bf16 v[48:51], a[36:39], a[20:23], v[48:51]\n" \
"buffer_load_dwordx4 v198, s[48:51], s77 offen offset:1024 lds\n" \
"v_mfma_f32_16x16x32_bf16 v[56:59], a[32:35], a[24:27], v[56:59]\n" \
"ds_read_b64_tr_b16 a[216:217], v195 offset:2048\n" \
"v_mfma_f32_16x16x32_bf16 v[56:59], a[36:39], a[28:31], v[56:59]\n" \
"ds_read_b64_tr_b16 a[218:219], v195 offset:5120\n" \
"v_mfma_f32_16x16x32_bf16 v[16:19], a[40:43], a[24:27], v[16:19]\n" \
"ds_read_b64_tr_b16 a[220:221], v195 offset:14336\n" \
"v_mfma_f32_16x16x32_bf16 v[16:19], a[44:47], a[28:31], v[16:19]\n" \
"buffer_load_dwordx4 v199, s[48:51], s77 offen offset:2048 lds\n" \
"v_mfma_f32_16x16x32_bf16 v[20:23], a[48:51], a[24:27], v[20:23]\n" \
"ds_read_b64_tr_b16 a[222:223], v195 offset:17408\n" \
"v_mfma_f32_16x16x32_bf16 v[20:23], a[52:55], a[28:31], v[20:23]\n" \
"s_waitcnt lgkmcnt(3)\n" \
"v_mfma_f32_16x16x32_bf16 v[28:31], a[48:51], a[192:195], v[28:31]\n" \
"v_add_u32 v254, 3145728, v254\n" \
"v_mfma_f32_16x16x32_bf16 v[28:31], a[52:55], a[196:199], v[28:31]\n" \
"global_load_dword v253, v254, s[22:23]\n" \
"v_mfma_f32_16x16x32_bf16 v[24:27], a[40:43], a[192:195], v[24:27]\n" \
"v_mfma_f32_16x16x32_bf16 v[24:27], a[44:47], a[196:199], v[24:27]\n" \
"v_mfma_f32_16x16x32_bf16 v[64:67], a[32:35], a[192:195], v[64:67]\n" \
"v_mfma_f32_16x16x32_bf16 v[64:67], a[36:39], a[196:199], v[64:67]\n" \
"s_waitcnt vmcnt(12) lgkmcnt(0)\n" \
"s_barrier\n" \
"v_mfma_f32_16x16x32_bf16 v[72:75], a[0:3], a[208:211], v[72:75]\n" \
"ds_read_b64_tr_b16 a[32:33], v209 offset:1024\n" \
"ds_read_b64_tr_b16 a[34:35], v209 offset:4096\n" \
"v_mfma_f32_16x16x32_bf16 v[72:75], a[4:7], a[212:215], v[72:75]\n" \
"ds_read_b64_tr_b16 a[36:37], v209 offset:13312\n" \
"ds_read_b64_tr_b16 a[38:39], v209 offset:16384\n" \
"v_mfma_f32_16x16x32_bf16 v[80:83], a[8:11], a[208:211], v[80:83]\n" \
"s_add_u32 m0, s79, 110592\n" \
"ds_read_b128 a[0:3], v207 offset:0\n" \
"buffer_store_dword v146, v147, s[56:59], s15 offen sc1\n" \
"v_mfma_f32_16x16x32_bf16 v[80:83], a[12:15], a[212:215], v[80:83]\n" \
"buffer_load_dwordx4 v192, s[44:47], s73 offen offset:0 lds\n" \
"v_mfma_f32_16x16x32_bf16 v[84:87], a[8:11], a[216:219], v[84:87]\n" \
"ds_read_b128 a[4:7], v208 offset:0\n" \
"v_mfma_f32_16x16x32_bf16 v[84:87], a[12:15], a[220:223], v[84:87]\n" \
"ds_read_b64_tr_b16 a[40:41], v209 offset:0\n" \
"buffer_store_dword v146, v147, s[56:59], s17 offen sc1\n" \
"v_mfma_f32_16x16x32_bf16 v[44:47], a[200:203], a[8:11], v[44:47]\n" \
"s_mov_b64 s[36:37], s[18:19]\n" \
"ds_read_b64_tr_b16 a[42:43], v209 offset:3072\n" \
"v_mfma_f32_16x16x32_bf16 v[44:47], a[204:207], a[12:15], v[44:47]\n" \
"buffer_load_dwordx4 v193, s[44:47], s73 offen offset:1024 lds\n" \
"v_mfma_f32_16x16x32_bf16 v[52:55], a[200:203], a[16:19], v[52:55]\n" \
"s_sub_u32 s36, s36, 4096\n" \
"s_subb_u32 s37, s37, 0\n" \
"ds_read_b128 a[8:11], v205 offset:0\n" \
"v_mfma_f32_16x16x32_bf16 v[52:55], a[204:207], a[20:23], v[52:55]\n" \
"s_and_b32 s37, s37, 0xffff\n" \
"s_mov_b32 s38, 8392704\n" \
"ds_read_b128 a[12:15], v206 offset:0\n" \
"v_mfma_f32_16x16x32_bf16 v[92:95], a[16:19], a[216:219], v[92:95]\n" \
"s_mov_b32 s39, 0x20000\n" \
"s_mov_b64 s[40:41], s[24:25]\n" \
"ds_read_b64_tr_b16 a[44:45], v209 offset:12288\n" \
"v_mfma_f32_16x16x32_bf16 v[92:95], a[20:23], a[220:223], v[92:95]\n" \
"buffer_load_dwordx4 v192, s[44:47], s73 offen offset:2048 lds\n" \
"v_mfma_f32_16x16x32_bf16 v[88:91], a[16:19], a[208:211], v[88:91]\n" \
"s_sub_u32 s40, s40, 4096\n" \
"s_subb_u32 s41, s41, 0\n" \
"ds_read_b64_tr_b16 a[46:47], v209 offset:15360\n" \
"v_mfma_f32_16x16x32_bf16 v[88:91], a[20:23], a[212:215], v[88:91]\n" \
"s_and_b32 s41, s41, 0xffff\n" \
"s_mov_b32 s42, 2101248\n" \
"ds_read_b64_tr_b16 a[48:49], v210 offset:0\n" \
"v_mfma_f32_16x16x32_bf16 v[96:99], a[24:27], a[208:211], v[96:99]\n" \
"s_mov_b32 s43, 0x20000\n" \
"s_mov_b64 s[52:53], s[26:27]\n" \
"ds_read_b128 a[16:19], v205 offset:4096\n" \
"v_mfma_f32_16x16x32_bf16 v[96:99], a[28:31], a[212:215], v[96:99]\n" \
"buffer_load_dwordx4 v193, s[44:47], s73 offen offset:3072 lds\n" \
"v_mfma_f32_16x16x32_bf16 v[100:103], a[24:27], a[216:219], v[100:103]\n" \
"s_and_b32 s53, s53, 0xffff\n" \
"s_mov_b32 s54, 8388608\n" \
"ds_read_b128 a[20:23], v206 offset:4096\n" \
"v_mfma_f32_16x16x32_bf16 v[100:103], a[28:31], a[220:223], v[100:103]\n" \
"s_add_u32 m0, s89, 110592\n" \
"s_mov_b32 s55, 0x20000\n" \
"ds_read_b64_tr_b16 a[50:51], v210 offset:3072\n" \
"v_mfma_f32_16x16x32_bf16 v[60:63], a[200:203], a[24:27], v[60:63]\n" \
"s_xor_b32 s73, s73, 0x80\n" \
"ds_read_b64_tr_b16 a[52:53], v210 offset:12288\n" \
"v_lshrrev_b32 v221, 2, v234\n" \
"v_mfma_f32_16x16x32_bf16 v[60:63], a[204:207], a[28:31], v[60:63]\n" \
"buffer_load_dwordx4 v196, s[44:47], s75 offen lds\n" \
"v_mfma_f32_16x16x32_bf16 v[68:71], a[200:203], a[192:195], v[68:71]\n" \
"s_xor_b32 s75, s75, 0x80\n" \
"s_mul_i32 s76, s80, 393216\n" \
"ds_read_b128 a[24:27], v205 offset:8192\n" \
"v_mfma_f32_16x16x32_bf16 v[68:71], a[204:207], a[196:199], v[68:71]\n" \
"s_add_u32 s76, s76, s78\n" \
"ds_read_b128 a[28:31], v206 offset:8192\n" \
"v_lshrrev_b32 v244, 4, v234\n" \
"v_mfma_f32_16x16x32_bf16 v[108:111], a[192:195], a[216:219], v[108:111]\n" \
"s_add_u32 m0, s91, 139264\n" \
"s_lshl_b32 s12, s94, 7\n" \
"ds_read_b64_tr_b16 a[54:55], v210 offset:15360\n" \
"v_mfma_f32_16x16x32_bf16 v[108:111], a[196:199], a[220:223], v[108:111]\n" \
"buffer_load_dwordx4 v197, s[48:51], s76 offen offset:0 lds\n" \
"v_mfma_f32_16x16x32_bf16 v[104:107], a[192:195], a[208:211], v[104:107]\n" \
"s_add_u32 s12, s12, 65591296\n" \
"s_and_b32 s70, %1, 1\n" \
"s_lshl3_add_u32 s70, s70, s63\n" \
"v_mfma_f32_16x16x32_bf16 v[104:107], a[196:199], a[212:215], v[104:107]\n" \
"s_waitcnt lgkmcnt(2)\n" \
"s_lshl2_add_u32 s12, s70, s12\n" \
"s_lshr_b32 s68, s63, 1\n" \
"s_add_u32 s68, s68, 2\n" \
"v_mfma_f32_16x16x32_bf16 v[32:35], a[32:35], a[0:3], v[32:35]\n" \
"s_add_u32 s77, s76, 24576\n" \
"s_mov_b32 s13, 65593856\n" \
"ds_read_b128 a[192:195], v205 offset:12288\n" \
"v_mfma_f32_16x16x32_bf16 v[32:35], a[36:39], a[4:7], v[32:35]\n" \
"buffer_load_dwordx4 v198, s[48:51], s76 offen offset:1024 lds\n" \
"v_mfma_f32_16x16x32_bf16 v[40:43], a[32:35], a[8:11], v[40:43]\n" \
"s_lshr_b32 s70, s18, 8\n" \
"ds_read_b128 a[196:199], v206 offset:12288\n" \
"v_lshrrev_b32 v228, 1, v234\n" \
"v_mfma_f32_16x16x32_bf16 v[40:43], a[36:39], a[12:15], v[40:43]\n" \
"s_sub_u32 s68, s68, s70\n" \
"s_and_b32 s68, s68, 3\n" \
"ds_read_b64_tr_b16 a[200:201], v210 offset:1024\n" \
"v_mfma_f32_16x16x32_bf16 v[0:3], a[40:43], a[8:11], v[0:3]\n" \
"s_xor_b32 s80, s80, 0x1\n" \
"s_and_b32 s70, s63, 1\n" \
"ds_read_b64_tr_b16 a[202:203], v210 offset:4096\n" \
"v_mfma_f32_16x16x32_bf16 v[0:3], a[44:47], a[12:15], v[0:3]\n" \
"s_waitcnt lgkmcnt(3)\n" \
"buffer_load_dwordx4 v199, s[48:51], s76 offen offset:2048 lds\n" \
"v_mfma_f32_16x16x32_bf16 v[4:7], a[48:51], a[8:11], v[4:7]\n" \
"s_lshl_b32 s70, s70, 2\n" \
"s_or_b32 s68, s68, s70\n" \
"ds_read_b64_tr_b16 a[204:205], v210 offset:13312\n" \
"v_mfma_f32_16x16x32_bf16 v[4:7], a[52:55], a[12:15], v[4:7]\n" \
"s_lshr_b32 s84, s61, 1\n" \
"s_lshl_b32 s84, s84, 16\n" \
"ds_read_b64_tr_b16 a[206:207], v210 offset:16384\n" \
"v_mfma_f32_16x16x32_bf16 v[12:15], a[48:51], a[16:19], v[12:15]\n" \
"s_add_u32 m0, m0, 3072\n" \
"s_and_b32 s70, s61, 1\n" \
"ds_read_b64_tr_b16 a[208:209], v209 offset:2048\n" \
"v_mfma_f32_16x16x32_bf16 v[12:15], a[52:55], a[20:23], v[12:15]\n" \
"buffer_load_dwordx4 v197, s[48:51], s77 offen offset:0 lds\n" \
"v_mfma_f32_16x16x32_bf16 v[8:11], a[40:43], a[16:19], v[8:11]\n" \
"s_lshl_b32 s70, s70, 14\n" \
"s_add_u32 s84, s84, s70\n" \
"ds_read_b64_tr_b16 a[210:211], v209 offset:5120\n" \
"v_mfma_f32_16x16x32_bf16 v[8:11], a[44:47], a[20:23], v[8:11]\n" \
"s_lshl_b32 s88, s68, 8\n" \
"s_add_u32 s84, s84, s88\n" \
"ds_read_b64_tr_b16 a[212:213], v209 offset:14336\n" \
"v_mfma_f32_16x16x32_bf16 v[48:51], a[32:35], a[16:19], v[48:51]\n" \
"ds_read_b64_tr_b16 a[214:215], v209 offset:17408\n" \
"v_bfe_u32 v245, v244, 2, 3\n" \
"v_mfma_f32_16x16x32_bf16 v[48:51], a[36:39], a[20:23], v[48:51]\n" \
"buffer_load_dwordx4 v198, s[48:51], s77 offen offset:1024 lds\n" \
"v_mfma_f32_16x16x32_bf16 v[56:59], a[32:35], a[24:27], v[56:59]\n" \
"ds_read_b64_tr_b16 a[216:217], v210 offset:2048\n" \
"v_lshlrev_b32 v245, 11, v245\n" \
"v_mfma_f32_16x16x32_bf16 v[56:59], a[36:39], a[28:31], v[56:59]\n" \
"ds_read_b64_tr_b16 a[218:219], v210 offset:5120\n" \
"v_lshrrev_b32 v246, 5, v244\n" \
"v_mfma_f32_16x16x32_bf16 v[16:19], a[40:43], a[24:27], v[16:19]\n" \
"ds_read_b64_tr_b16 a[220:221], v210 offset:14336\n" \
"v_lshl_add_u32 v245, v246, 6, v245\n" \
"v_mfma_f32_16x16x32_bf16 v[16:19], a[44:47], a[28:31], v[16:19]\n" \
"buffer_load_dwordx4 v199, s[48:51], s77 offen offset:2048 lds\n" \
"v_mfma_f32_16x16x32_bf16 v[20:23], a[48:51], a[24:27], v[20:23]\n" \
"ds_read_b64_tr_b16 a[222:223], v210 offset:17408\n" \
"v_and_b32 v246, 3, v244\n" \
"v_mfma_f32_16x16x32_bf16 v[20:23], a[52:55], a[28:31], v[20:23]\n" \
"s_waitcnt lgkmcnt(3)\n" \
"v_bfe_u32 v247, v244, 4, 1\n" \
"v_lshlrev_b32 v247, 1, v247\n" \
"v_mfma_f32_16x16x32_bf16 v[28:31], a[48:51], a[192:195], v[28:31]\n" \
"v_xor_b32 v246, v246, v247\n" \
"v_lshl_add_u32 v245, v246, 4, v245\n" \
"v_mfma_f32_16x16x32_bf16 v[28:31], a[52:55], a[196:199], v[28:31]\n" \
"buffer_load_dwordx2 v[226:227], v228, s[56:59], s13 offen sc1\n" \
"v_mfma_f32_16x16x32_bf16 v[24:27], a[40:43], a[192:195], v[24:27]\n" \
"v_add_u32 v216, 4096, v245\n" \
"v_add_u32 v217, 3200, v245\n" \
"v_mfma_f32_16x16x32_bf16 v[24:27], a[44:47], a[196:199], v[24:27]\n" \
"v_add_u32 v218, 34816, v245\n" \
"v_add_u32 v219, 33920, v245\n" \
"v_mfma_f32_16x16x32_bf16 v[64:67], a[32:35], a[192:195], v[64:67]\n" \
"s_mov_b32 s86, 65536\n" \
"s_lshr_b32 s70, s61, 1\n" \
"s_lshl_b32 s71, s70, 13\n" \
"v_mfma_f32_16x16x32_bf16 v[64:67], a[36:39], a[196:199], v[64:67]\n" \
"s_add_u32 s71, s71, %3\n" \
"s_waitcnt vmcnt(15) lgkmcnt(0)\n" \
"s_barrier\n" \
"v_mfma_f32_16x16x32_bf16 v[72:75], a[0:3], a[208:211], v[72:75]\n" \
"ds_read_b64_tr_b16 a[32:33], v204 offset:21504\n" \
"ds_read_b64_tr_b16 a[34:35], v204 offset:24576\n" \
"v_mfma_f32_16x16x32_bf16 v[72:75], a[4:7], a[212:215], v[72:75]\n" \
"ds_read_b64_tr_b16 a[36:37], v204 offset:33792\n" \
"ds_read_b64_tr_b16 a[38:39], v204 offset:36864\n" \
"v_mfma_f32_16x16x32_bf16 v[80:83], a[8:11], a[208:211], v[80:83]\n" \
"s_add_u32 m0, s79, 65536\n" \
"ds_read_b128 a[0:3], v202 offset:0\n" \
"v_add_u32 v211, s71, v236\n" \
"v_mfma_f32_16x16x32_bf16 v[80:83], a[12:15], a[212:215], v[80:83]\n" \
"buffer_load_dwordx4 v192, s[44:47], s73 offen offset:0 lds\n" \
"v_mfma_f32_16x16x32_bf16 v[84:87], a[8:11], a[216:219], v[84:87]\n" \
"s_lshl_b32 s70, s70, 18\n" \
"s_add_u32 s88, s88, s70\n" \
"ds_read_b128 a[4:7], v203 offset:0\n" \
"v_mfma_f32_16x16x32_bf16 v[84:87], a[12:15], a[220:223], v[84:87]\n" \
"s_and_b32 s70, s61, 1\n" \
"s_lshl_b32 s71, s70, 7\n" \
"ds_read_b64_tr_b16 a[40:41], v204 offset:20480\n" \
"v_mfma_f32_16x16x32_bf16 v[44:47], a[200:203], a[8:11], v[44:47]\n" \
"s_add_u32 s88, s88, s71\n" \
"ds_read_b64_tr_b16 a[42:43], v204 offset:23552\n" \
"v_and_b32 v245, 3, v244\n" \
"v_mfma_f32_16x16x32_bf16 v[44:47], a[204:207], a[12:15], v[44:47]\n" \
"buffer_load_dwordx4 v193, s[44:47], s73 offen offset:1024 lds\n" \
"v_mfma_f32_16x16x32_bf16 v[52:55], a[200:203], a[16:19], v[52:55]\n" \
"ds_read_b128 a[8:11], v200 offset:0\n" \
"v_lshlrev_b32 v245, 3, v245\n" \
"v_mfma_f32_16x16x32_bf16 v[52:55], a[204:207], a[20:23], v[52:55]\n" \
"ds_read_b128 a[12:15], v201 offset:0\n" \
"v_bfe_u32 v246, v244, 2, 2\n" \
"v_mfma_f32_16x16x32_bf16 v[92:95], a[16:19], a[216:219], v[92:95]\n" \
"ds_read_b64_tr_b16 a[44:45], v204 offset:32768\n" \
"v_lshl_add_u32 v245, v246, 6, v245\n" \
"v_mfma_f32_16x16x32_bf16 v[92:95], a[20:23], a[220:223], v[92:95]\n" \
"buffer_load_dwordx4 v192, s[44:47], s73 offen offset:2048 lds\n" \
"v_mfma_f32_16x16x32_bf16 v[88:91], a[16:19], a[208:211], v[88:91]\n" \
"ds_read_b64_tr_b16 a[46:47], v204 offset:35840\n" \
"v_bfe_u32 v246, v244, 4, 1\n" \
"v_mfma_f32_16x16x32_bf16 v[88:91], a[20:23], a[212:215], v[88:91]\n" \
"ds_read_b64_tr_b16 a[48:49], v222 offset:20480\n" \
"v_lshl_add_u32 v245, v246, 5, v245\n" \
"v_mfma_f32_16x16x32_bf16 v[96:99], a[24:27], a[208:211], v[96:99]\n" \
"ds_read_b128 a[16:19], v200 offset:4096\n" \
"v_lshl_add_u32 v245, v246, 8, v245\n" \
"v_mfma_f32_16x16x32_bf16 v[96:99], a[28:31], a[212:215], v[96:99]\n" \
"buffer_load_dwordx4 v193, s[44:47], s73 offen offset:3072 lds\n" \
"v_mfma_f32_16x16x32_bf16 v[100:103], a[24:27], a[216:219], v[100:103]\n" \
"ds_read_b128 a[20:23], v201 offset:4096\n" \
"v_lshrrev_b32 v246, 5, v244\n" \
"v_mfma_f32_16x16x32_bf16 v[100:103], a[28:31], a[220:223], v[100:103]\n" \
"s_add_u32 m0, s89, 65536\n" \
"ds_read_b64_tr_b16 a[50:51], v222 offset:23552\n" \
"v_lshl_add_u32 v245, v246, 12, v245\n" \
"v_mfma_f32_16x16x32_bf16 v[60:63], a[200:203], a[24:27], v[60:63]\n" \
"s_xor_b32 s73, s73, 0x180\n" \
"s_lshl_b32 s71, s70, 10\n" \
"ds_read_b64_tr_b16 a[52:53], v222 offset:32768\n" \
"v_mfma_f32_16x16x32_bf16 v[60:63], a[204:207], a[28:31], v[60:63]\n" \
"buffer_load_dwordx4 v196, s[44:47], s75 offen lds\n" \
"v_mfma_f32_16x16x32_bf16 v[68:71], a[200:203], a[192:195], v[68:71]\n" \
"s_xor_b32 s75, s75, 0x180\n" \
"s_mul_i32 s76, s80, 393216\n" \
"ds_read_b128 a[24:27], v200 offset:8192\n" \
"v_mfma_f32_16x16x32_bf16 v[68:71], a[204:207], a[196:199], v[68:71]\n" \
"s_add_u32 s76, s76, s78\n" \
"s_add_u32 s71, s71, %3\n" \
"ds_read_b128 a[28:31], v201 offset:8192\n" \
"v_mfma_f32_16x16x32_bf16 v[108:111], a[192:195], a[216:219], v[108:111]\n" \
"s_add_u32 m0, s91, 86016\n" \
"ds_read_b64_tr_b16 a[54:55], v222 offset:35840\n" \
"v_add_u32 v212, s71, v245\n" \
"v_mfma_f32_16x16x32_bf16 v[108:111], a[196:199], a[220:223], v[108:111]\n" \
"buffer_load_dwordx4 v197, s[48:51], s76 offen offset:0 lds\n" \
"v_mfma_f32_16x16x32_bf16 v[104:107], a[192:195], a[208:211], v[104:107]\n" \
"s_waitcnt lgkmcnt(3)\n" \
"s_mov_b32 s69, 1\n" \
"s_mov_b32 s85, 98304\n" \
"v_mfma_f32_16x16x32_bf16 v[104:107], a[196:199], a[212:215], v[104:107]\n" \
"s_sub_u32 s85, s85, 32768\n" \
"s_cselect_b32 s85, 131072, s85\n" \
"v_add_u32 v213, s85, v211\n" \
"v_mfma_f32_16x16x32_bf16 v[32:35], a[32:35], a[0:3], v[32:35]\n" \
"s_add_u32 s77, s76, 24576\n" \
"ds_read_b128 a[192:195], v200 offset:12288\n" \
"v_add_u32 v214, s85, v212\n" \
"v_mfma_f32_16x16x32_bf16 v[32:35], a[36:39], a[4:7], v[32:35]\n" \
"buffer_load_dwordx4 v198, s[48:51], s76 offen offset:1024 lds\n" \
"v_mfma_f32_16x16x32_bf16 v[40:43], a[32:35], a[8:11], v[40:43]\n" \
"ds_read_b128 a[196:199], v201 offset:12288\n" \
"v_xor_b32 v215, 32, v214\n" \
"v_mfma_f32_16x16x32_bf16 v[40:43], a[36:39], a[12:15], v[40:43]\n" \
"s_waitcnt lgkmcnt(2)\n" \
"ds_read_b64_tr_b16 a[200:201], v222 offset:21504\n" \
"v_mov_b32 v230, s60\n" \
"v_mfma_f32_16x16x32_bf16 v[0:3], a[40:43], a[8:11], v[0:3]\n" \
"s_xor_b32 s80, s80, 0x3\n" \
"ds_read_b64_tr_b16 a[202:203], v222 offset:24576\n" \
"v_lshlrev_b32 v231, 28, s61\n" \
"v_mfma_f32_16x16x32_bf16 v[0:3], a[44:47], a[12:15], v[0:3]\n" \
"buffer_load_dwordx4 v199, s[48:51], s76 offen offset:2048 lds\n" \
"v_mfma_f32_16x16x32_bf16 v[4:7], a[48:51], a[8:11], v[4:7]\n" \
"ds_read_b64_tr_b16 a[204:205], v222 offset:33792\n" \
"v_mfma_f32_16x16x32_bf16 v[4:7], a[52:55], a[12:15], v[4:7]\n" \
"ds_read_b64_tr_b16 a[206:207], v222 offset:36864\n" \
"v_mfma_f32_16x16x32_bf16 v[12:15], a[48:51], a[16:19], v[12:15]\n" \
"s_add_u32 m0, m0, 3072\n" \
"ds_read_b64_tr_b16 a[208:209], v204 offset:22528\n" \
"v_mfma_f32_16x16x32_bf16 v[12:15], a[52:55], a[20:23], v[12:15]\n" \
"buffer_load_dwordx4 v197, s[48:51], s77 offen offset:0 lds\n" \
"v_mfma_f32_16x16x32_bf16 v[8:11], a[40:43], a[16:19], v[8:11]\n" \
"ds_read_b64_tr_b16 a[210:211], v204 offset:25600\n" \
"v_mfma_f32_16x16x32_bf16 v[8:11], a[44:47], a[20:23], v[8:11]\n" \
"ds_read_b64_tr_b16 a[212:213], v204 offset:34816\n" \
"v_mfma_f32_16x16x32_bf16 v[48:51], a[32:35], a[16:19], v[48:51]\n" \
"ds_read_b64_tr_b16 a[214:215], v204 offset:37888\n" \
"v_mfma_f32_16x16x32_bf16 v[48:51], a[36:39], a[20:23], v[48:51]\n" \
"buffer_load_dwordx4 v198, s[48:51], s77 offen offset:1024 lds\n" \
"v_mfma_f32_16x16x32_bf16 v[56:59], a[32:35], a[24:27], v[56:59]\n" \
"ds_read_b64_tr_b16 a[216:217], v222 offset:22528\n" \
"v_mfma_f32_16x16x32_bf16 v[56:59], a[36:39], a[28:31], v[56:59]\n" \
"ds_read_b64_tr_b16 a[218:219], v222 offset:25600\n" \
"v_mfma_f32_16x16x32_bf16 v[16:19], a[40:43], a[24:27], v[16:19]\n" \
"ds_read_b64_tr_b16 a[220:221], v222 offset:34816\n" \
"v_mfma_f32_16x16x32_bf16 v[16:19], a[44:47], a[28:31], v[16:19]\n" \
"buffer_load_dwordx4 v199, s[48:51], s77 offen offset:2048 lds\n" \
"v_mfma_f32_16x16x32_bf16 v[20:23], a[48:51], a[24:27], v[20:23]\n" \
"ds_read_b64_tr_b16 a[222:223], v222 offset:37888\n" \
"v_mfma_f32_16x16x32_bf16 v[20:23], a[52:55], a[28:31], v[20:23]\n" \
"s_waitcnt lgkmcnt(3)\n" \
"v_mfma_f32_16x16x32_bf16 v[28:31], a[48:51], a[192:195], v[28:31]\n" \
"v_mfma_f32_16x16x32_bf16 v[28:31], a[52:55], a[196:199], v[28:31]\n" \
"v_mfma_f32_16x16x32_bf16 v[24:27], a[40:43], a[192:195], v[24:27]\n" \
"v_mfma_f32_16x16x32_bf16 v[24:27], a[44:47], a[196:199], v[24:27]\n" \
"v_mfma_f32_16x16x32_bf16 v[64:67], a[32:35], a[192:195], v[64:67]\n" \
"v_mfma_f32_16x16x32_bf16 v[64:67], a[36:39], a[196:199], v[64:67]\n" \
"s_waitcnt vmcnt(12) lgkmcnt(0)\n" \
"s_barrier\n" \
"v_mfma_f32_16x16x32_bf16 v[72:75], a[0:3], a[208:211], v[72:75]\n" \
"ds_read_b64_tr_b16 a[32:33], v194 offset:1024\n" \
"ds_read_b64_tr_b16 a[34:35], v194 offset:4096\n" \
"v_mfma_f32_16x16x32_bf16 v[72:75], a[4:7], a[212:215], v[72:75]\n" \
"ds_read_b64_tr_b16 a[36:37], v194 offset:13312\n" \
"ds_read_b64_tr_b16 a[38:39], v194 offset:16384\n" \
"v_mfma_f32_16x16x32_bf16 v[80:83], a[8:11], a[208:211], v[80:83]\n" \
"s_add_u32 m0, s79, 0\n" \
"ds_read_b128 a[0:3], v207 offset:45056\n" \
"v_mfma_f32_16x16x32_bf16 v[80:83], a[12:15], a[212:215], v[80:83]\n" \
"buffer_load_dwordx4 v192, s[44:47], s73 offen offset:0 lds\n" \
"v_mfma_f32_16x16x32_bf16 v[84:87], a[8:11], a[216:219], v[84:87]\n" \
"ds_read_b128 a[4:7], v208 offset:45056\n" \
"v_mfma_f32_16x16x32_bf16 v[84:87], a[12:15], a[220:223], v[84:87]\n" \
"ds_read_b64_tr_b16 a[40:41], v194 offset:0\n" \
"v_mfma_f32_16x16x32_bf16 v[44:47], a[200:203], a[8:11], v[44:47]\n" \
"ds_read_b64_tr_b16 a[42:43], v194 offset:3072\n" \
"v_mfma_f32_16x16x32_bf16 v[44:47], a[204:207], a[12:15], v[44:47]\n" \
"buffer_load_dwordx4 v193, s[44:47], s73 offen offset:1024 lds\n" \
"v_mfma_f32_16x16x32_bf16 v[52:55], a[200:203], a[16:19], v[52:55]\n" \
"ds_read_b128 a[8:11], v205 offset:45056\n" \
"v_mfma_f32_16x16x32_bf16 v[52:55], a[204:207], a[20:23], v[52:55]\n" \
"ds_read_b128 a[12:15], v206 offset:45056\n" \
"v_mfma_f32_16x16x32_bf16 v[92:95], a[16:19], a[216:219], v[92:95]\n" \
"ds_read_b64_tr_b16 a[44:45], v194 offset:12288\n" \
"v_mfma_f32_16x16x32_bf16 v[92:95], a[20:23], a[220:223], v[92:95]\n" \
"buffer_load_dwordx4 v192, s[44:47], s73 offen offset:2048 lds\n" \
"v_mfma_f32_16x16x32_bf16 v[88:91], a[16:19], a[208:211], v[88:91]\n" \
"ds_read_b64_tr_b16 a[46:47], v194 offset:15360\n" \
"v_mfma_f32_16x16x32_bf16 v[88:91], a[20:23], a[212:215], v[88:91]\n" \
"ds_read_b64_tr_b16 a[48:49], v195 offset:0\n" \
"v_mfma_f32_16x16x32_bf16 v[96:99], a[24:27], a[208:211], v[96:99]\n" \
"ds_read_b128 a[16:19], v205 offset:49152\n" \
"v_mfma_f32_16x16x32_bf16 v[96:99], a[28:31], a[212:215], v[96:99]\n" \
"buffer_load_dwordx4 v193, s[44:47], s73 offen offset:3072 lds\n" \
"v_mfma_f32_16x16x32_bf16 v[100:103], a[24:27], a[216:219], v[100:103]\n" \
"ds_read_b128 a[20:23], v206 offset:49152\n" \
"v_mfma_f32_16x16x32_bf16 v[100:103], a[28:31], a[220:223], v[100:103]\n" \
"s_add_u32 m0, s89, 0\n" \
"ds_read_b64_tr_b16 a[50:51], v195 offset:3072\n" \
"v_mfma_f32_16x16x32_bf16 v[60:63], a[200:203], a[24:27], v[60:63]\n" \
"s_xor_b32 s73, s73, 0x300\n" \
"ds_read_b64_tr_b16 a[52:53], v195 offset:12288\n" \
"v_mfma_f32_16x16x32_bf16 v[60:63], a[204:207], a[28:31], v[60:63]\n" \
"buffer_load_dwordx4 v196, s[44:47], s75 offen lds\n" \
"v_mfma_f32_16x16x32_bf16 v[68:71], a[200:203], a[192:195], v[68:71]\n" \
"s_xor_b32 s75, s75, 0x300\n" \
"s_mul_i32 s76, s80, 393216\n" \
"ds_read_b128 a[24:27], v205 offset:53248\n" \
"v_mfma_f32_16x16x32_bf16 v[68:71], a[204:207], a[196:199], v[68:71]\n" \
"s_add_u32 s76, s76, s78\n" \
"ds_read_b128 a[28:31], v206 offset:53248\n" \
"v_mfma_f32_16x16x32_bf16 v[108:111], a[192:195], a[216:219], v[108:111]\n" \
"s_add_u32 m0, s91, 20480\n" \
"ds_read_b64_tr_b16 a[54:55], v195 offset:15360\n" \
"v_mfma_f32_16x16x32_bf16 v[108:111], a[196:199], a[220:223], v[108:111]\n" \
"buffer_load_dwordx4 v197, s[48:51], s76 offen offset:0 lds\n" \
"v_mfma_f32_16x16x32_bf16 v[104:107], a[192:195], a[208:211], v[104:107]\n" \
"v_mfma_f32_16x16x32_bf16 v[104:107], a[196:199], a[212:215], v[104:107]\n" \
"s_waitcnt lgkmcnt(2)\n" \
"v_mfma_f32_16x16x32_bf16 v[32:35], a[32:35], a[0:3], v[32:35]\n" \
"s_add_u32 s77, s76, 24576\n" \
"ds_read_b128 a[192:195], v205 offset:57344\n" \
"v_mfma_f32_16x16x32_bf16 v[32:35], a[36:39], a[4:7], v[32:35]\n" \
"buffer_load_dwordx4 v198, s[48:51], s76 offen offset:1024 lds\n" \
"v_mfma_f32_16x16x32_bf16 v[40:43], a[32:35], a[8:11], v[40:43]\n" \
"ds_read_b128 a[196:199], v206 offset:57344\n" \
"v_mfma_f32_16x16x32_bf16 v[40:43], a[36:39], a[12:15], v[40:43]\n" \
"ds_read_b64_tr_b16 a[200:201], v195 offset:1024\n" \
"v_mfma_f32_16x16x32_bf16 v[0:3], a[40:43], a[8:11], v[0:3]\n" \
"s_waitcnt lgkmcnt(3)\n" \
"s_xor_b32 s80, s80, 0x6\n" \
"ds_read_b64_tr_b16 a[202:203], v195 offset:4096\n" \
"v_mfma_f32_16x16x32_bf16 v[0:3], a[44:47], a[12:15], v[0:3]\n" \
"buffer_load_dwordx4 v199, s[48:51], s76 offen offset:2048 lds\n" \
"v_mfma_f32_16x16x32_bf16 v[4:7], a[48:51], a[8:11], v[4:7]\n" \
"ds_read_b64_tr_b16 a[204:205], v195 offset:13312\n" \
"v_mfma_f32_16x16x32_bf16 v[4:7], a[52:55], a[12:15], v[4:7]\n" \
"ds_read_b64_tr_b16 a[206:207], v195 offset:16384\n" \
"v_mfma_f32_16x16x32_bf16 v[12:15], a[48:51], a[16:19], v[12:15]\n" \
"s_add_u32 m0, m0, 3072\n" \
"ds_read_b64_tr_b16 a[208:209], v194 offset:2048\n" \
"v_mfma_f32_16x16x32_bf16 v[12:15], a[52:55], a[20:23], v[12:15]\n" \
"buffer_load_dwordx4 v197, s[48:51], s77 offen offset:0 lds\n" \
"v_mfma_f32_16x16x32_bf16 v[8:11], a[40:43], a[16:19], v[8:11]\n" \
"ds_read_b64_tr_b16 a[210:211], v194 offset:5120\n" \
"v_mfma_f32_16x16x32_bf16 v[8:11], a[44:47], a[20:23], v[8:11]\n" \
"ds_read_b64_tr_b16 a[212:213], v194 offset:14336\n" \
"v_mfma_f32_16x16x32_bf16 v[48:51], a[32:35], a[16:19], v[48:51]\n" \
"ds_read_b64_tr_b16 a[214:215], v194 offset:17408\n" \
"v_mfma_f32_16x16x32_bf16 v[48:51], a[36:39], a[20:23], v[48:51]\n" \
"buffer_load_dwordx4 v198, s[48:51], s77 offen offset:1024 lds\n" \
"v_mfma_f32_16x16x32_bf16 v[56:59], a[32:35], a[24:27], v[56:59]\n" \
"ds_read_b64_tr_b16 a[216:217], v195 offset:2048\n" \
"v_mfma_f32_16x16x32_bf16 v[56:59], a[36:39], a[28:31], v[56:59]\n" \
"ds_read_b64_tr_b16 a[218:219], v195 offset:5120\n" \
"v_mfma_f32_16x16x32_bf16 v[16:19], a[40:43], a[24:27], v[16:19]\n" \
"ds_read_b64_tr_b16 a[220:221], v195 offset:14336\n" \
"v_mfma_f32_16x16x32_bf16 v[16:19], a[44:47], a[28:31], v[16:19]\n" \
"buffer_load_dwordx4 v199, s[48:51], s77 offen offset:2048 lds\n" \
"v_mfma_f32_16x16x32_bf16 v[20:23], a[48:51], a[24:27], v[20:23]\n" \
"ds_read_b64_tr_b16 a[222:223], v195 offset:17408\n" \
"v_mfma_f32_16x16x32_bf16 v[20:23], a[52:55], a[28:31], v[20:23]\n" \
"s_waitcnt lgkmcnt(3)\n" \
"v_mfma_f32_16x16x32_bf16 v[28:31], a[48:51], a[192:195], v[28:31]\n" \
"v_mfma_f32_16x16x32_bf16 v[28:31], a[52:55], a[196:199], v[28:31]\n" \
"v_mfma_f32_16x16x32_bf16 v[24:27], a[40:43], a[192:195], v[24:27]\n" \
"v_mfma_f32_16x16x32_bf16 v[24:27], a[44:47], a[196:199], v[24:27]\n" \
"v_mfma_f32_16x16x32_bf16 v[64:67], a[32:35], a[192:195], v[64:67]\n" \
"v_mfma_f32_16x16x32_bf16 v[64:67], a[36:39], a[196:199], v[64:67]\n" \
"s_waitcnt vmcnt(11) lgkmcnt(0)\n" \
"s_barrier\n" \
"v_mfma_f32_16x16x32_bf16 v[72:75], a[0:3], a[208:211], v[72:75]\n" \
"ds_read_b64_tr_b16 a[32:33], v209 offset:1024\n" \
"ds_read_b64_tr_b16 a[34:35], v209 offset:4096\n" \
"v_mfma_f32_16x16x32_bf16 v[72:75], a[4:7], a[212:215], v[72:75]\n" \
"ds_read_b64_tr_b16 a[36:37], v209 offset:13312\n" \
"ds_read_b64_tr_b16 a[38:39], v209 offset:16384\n" \
"v_mfma_f32_16x16x32_bf16 v[80:83], a[8:11], a[208:211], v[80:83]\n" \
"s_add_u32 m0, s79, 110592\n" \
"ds_read_b128 a[0:3], v207 offset:0\n" \
"v_cmp_eq_u32 vcc, s60, v226\n" \
"v_mfma_f32_16x16x32_bf16 v[80:83], a[12:15], a[212:215], v[80:83]\n" \
"buffer_load_dwordx4 v192, s[44:47], s73 offen offset:0 lds\n" \
"v_mfma_f32_16x16x32_bf16 v[84:87], a[8:11], a[216:219], v[84:87]\n" \
"s_andn2_b32 s70, 0xff, vcc_lo\n" \
"s_cbranch_scc1 Lpl4_%=\n" \
"Lpr4_%=:\n" \
"ds_read_b128 a[4:7], v208 offset:0\n" \
"v_mfma_f32_16x16x32_bf16 v[84:87], a[12:15], a[220:223], v[84:87]\n" \
"s_and_b32 s70, %1, 7\n" \
"ds_read_b64_tr_b16 a[40:41], v209 offset:0\n" \
"v_readlane_b32 s70, v227, s70\n" \
"v_mfma_f32_16x16x32_bf16 v[44:47], a[200:203], a[8:11], v[44:47]\n" \
"ds_read_b64_tr_b16 a[42:43], v209 offset:3072\n" \
"v_mfma_f32_16x16x32_bf16 v[44:47], a[204:207], a[12:15], v[44:47]\n" \
"buffer_load_dwordx4 v193, s[44:47], s73 offen offset:1024 lds\n" \
"v_mfma_f32_16x16x32_bf16 v[52:55], a[200:203], a[16:19], v[52:55]\n" \
"ds_read_b128 a[8:11], v205 offset:0\n" \
"v_subrev_u32 v227, s70, v227\n" \
"v_mfma_f32_16x16x32_bf16 v[52:55], a[204:207], a[20:23], v[52:55]\n" \
"ds_read_b128 a[12:15], v206 offset:0\n" \
"v_cmp_gt_i32 vcc, 0, v227\n" \
"v_mfma_f32_16x16x32_bf16 v[92:95], a[16:19], a[216:219], v[92:95]\n" \
"ds_read_b64_tr_b16 a[44:45], v209 offset:12288\n" \
"v_mfma_f32_16x16x32_bf16 v[92:95], a[20:23], a[220:223], v[92:95]\n" \
"buffer_load_dwordx4 v192, s[44:47], s73 offen offset:2048 lds\n" \
"v_mfma_f32_16x16x32_bf16 v[88:91], a[16:19], a[208:211], v[88:91]\n" \
"s_and_b32 s13, vcc_lo, 0xff\n" \
"s_bcnt1_i32_b32 s13, s13\n" \
"ds_read_b64_tr_b16 a[46:47], v209 offset:15360\n" \
"v_mfma_f32_16x16x32_bf16 v[88:91], a[20:23], a[212:215], v[88:91]\n" \
"s_lshr_b32 s90, s13, 2\n" \
"s_mul_i32 s70, s90, 7\n" \
"ds_read_b64_tr_b16 a[48:49], v210 offset:0\n" \
"v_mfma_f32_16x16x32_bf16 v[96:99], a[24:27], a[208:211], v[96:99]\n" \
"s_xor_b32 s13, s13, s70\n" \
"s_lshl_b32 s70, s13, 3\n" \
"ds_read_b128 a[16:19], v205 offset:4096\n" \
"v_mfma_f32_16x16x32_bf16 v[96:99], a[28:31], a[212:215], v[96:99]\n" \
"buffer_load_dwordx4 v193, s[44:47], s73 offen offset:3072 lds\n" \
"v_mfma_f32_16x16x32_bf16 v[100:103], a[24:27], a[216:219], v[100:103]\n" \
"s_lshr_b32 s9, 0x2a2b2d2f, s70\n" \
"s_and_b32 s9, s9, 0xff\n" \
"ds_read_b128 a[20:23], v206 offset:4096\n" \
"v_mfma_f32_16x16x32_bf16 v[100:103], a[28:31], a[220:223], v[100:103]\n" \
"Lpg9064_%=:\n" \
"Lst1x2_%=:\n" \
"s_cbranch_execz Lpg11183_%=\n" \
"s_add_u32 m0, s89, 110592\n" \
"s_lshr_b32 s8, %1, 6\n" \
"ds_read_b64_tr_b16 a[50:51], v210 offset:3072\n" \
"v_mfma_f32_16x16x32_bf16 v[60:63], a[200:203], a[24:27], v[60:63]\n" \
"s_xor_b32 s73, s73, 0x80\n" \
"s_lshl2_add_u32 s8, s13, s8\n" \
"ds_read_b64_tr_b16 a[52:53], v210 offset:12288\n" \
"v_mfma_f32_16x16x32_bf16 v[60:63], a[204:207], a[28:31], v[60:63]\n" \
"buffer_load_dwordx4 v196, s[44:47], s75 offen lds\n" \
"v_mfma_f32_16x16x32_bf16 v[68:71], a[200:203], a[192:195], v[68:71]\n" \
"s_xor_b32 s75, s75, 0x80\n" \
"s_mul_i32 s76, s80, 393216\n" \
"ds_read_b128 a[24:27], v205 offset:8192\n" \
"v_mfma_f32_16x16x32_bf16 v[68:71], a[204:207], a[196:199], v[68:71]\n" \
"s_add_u32 s76, s76, s78\n" \
"ds_read_b128 a[28:31], v206 offset:8192\n" \
"v_max_i32_dpp v226, v227, v227 quad_perm:[1,0,3,2] row_mask:0xf bank_mask:0xf\n" \
"v_mfma_f32_16x16x32_bf16 v[108:111], a[192:195], a[216:219], v[108:111]\n" \
"s_add_u32 m0, s91, 139264\n" \
"s_mov_b32 s14, 0\n" \
"ds_read_b64_tr_b16 a[54:55], v210 offset:15360\n" \
"v_mfma_f32_16x16x32_bf16 v[108:111], a[196:199], a[220:223], v[108:111]\n" \
"buffer_load_dwordx4 v197, s[48:51], s76 offen offset:0 lds\n" \
"v_mfma_f32_16x16x32_bf16 v[104:107], a[192:195], a[208:211], v[104:107]\n" \
"v_readlane_b32 s15, v226, 2\n" \
"v_readlane_b32 s16, v226, 4\n" \
"v_mfma_f32_16x16x32_bf16 v[104:107], a[196:199], a[212:215], v[104:107]\n" \
"s_waitcnt lgkmcnt(2)\n" \
"v_readlane_b32 s17, v226, 6\n" \
"v_cmp_gt_i32 vcc, s15, v226\n" \
"v_mfma_f32_16x16x32_bf16 v[32:35], a[32:35], a[0:3], v[32:35]\n" \
"s_add_u32 s77, s76, 24576\n" \
"ds_read_b128 a[192:195], v205 offset:12288\n" \
"v_mfma_f32_16x16x32_bf16 v[32:35], a[36:39], a[4:7], v[32:35]\n" \
"buffer_load_dwordx4 v198, s[48:51], s76 offen offset:1024 lds\n" \
"v_mfma_f32_16x16x32_bf16 v[40:43], a[32:35], a[8:11], v[40:43]\n" \
"s_and_b32 s70, vcc_lo, 0x55\n" \
"s_bcnt1_i32_b32 s70, s70\n" \
"ds_read_b128 a[196:199], v206 offset:12288\n" \
"v_mfma_f32_16x16x32_bf16 v[40:43], a[36:39], a[12:15], v[40:43]\n" \
"s_cmp_eq_u32 s70, s13\n" \
"s_cselect_b32 s14, 4, s14\n" \
"ds_read_b64_tr_b16 a[200:201], v210 offset:1024\n" \
"v_mfma_f32_16x16x32_bf16 v[0:3], a[40:43], a[8:11], v[0:3]\n" \
"s_xor_b32 s80, s80, 0x1\n" \
"ds_read_b64_tr_b16 a[202:203], v210 offset:4096\n" \
"v_cmp_gt_i32 vcc, s16, v226\n" \
"v_mfma_f32_16x16x32_bf16 v[0:3], a[44:47], a[12:15], v[0:3]\n" \
"s_waitcnt lgkmcnt(3)\n" \
"buffer_load_dwordx4 v199, s[48:51], s76 offen offset:2048 lds\n" \
"v_mfma_f32_16x16x32_bf16 v[4:7], a[48:51], a[8:11], v[4:7]\n" \
"s_and_b32 s70, vcc_lo, 0x55\n" \
"s_bcnt1_i32_b32 s70, s70\n" \
"ds_read_b64_tr_b16 a[204:205], v210 offset:13312\n" \
"v_mfma_f32_16x16x32_bf16 v[4:7], a[52:55], a[12:15], v[4:7]\n" \
"s_cmp_eq_u32 s70, s13\n" \
"s_cselect_b32 s14, 8, s14\n" \
"ds_read_b64_tr_b16 a[206:207], v210 offset:16384\n" \
"v_mfma_f32_16x16x32_bf16 v[12:15], a[48:51], a[16:19], v[12:15]\n" \
"s_add_u32 m0, m0, 3072\n" \
"ds_read_b64_tr_b16 a[208:209], v209 offset:2048\n" \
"v_cmp_gt_i32 vcc, s17, v226\n" \
"v_mfma_f32_16x16x32_bf16 v[12:15], a[52:55], a[20:23], v[12:15]\n" \
"buffer_load_dwordx4 v197, s[48:51], s77 offen offset:0 lds\n" \
"v_mfma_f32_16x16x32_bf16 v[8:11], a[40:43], a[16:19], v[8:11]\n" \
"s_and_b32 s70, vcc_lo, 0x55\n" \
"s_bcnt1_i32_b32 s70, s70\n" \
"ds_read_b64_tr_b16 a[210:211], v209 offset:5120\n" \
"v_mfma_f32_16x16x32_bf16 v[8:11], a[44:47], a[20:23], v[8:11]\n" \
"s_cmp_eq_u32 s70, s13\n" \
"s_cselect_b32 s14, 12, s14\n" \
"ds_read_b64_tr_b16 a[212:213], v209 offset:14336\n" \
"v_mfma_f32_16x16x32_bf16 v[48:51], a[32:35], a[16:19], v[48:51]\n" \
"s_lshr_b32 s70, %1, 6\n" \
"s_add_u32 s14, s14, s70\n" \
"ds_read_b64_tr_b16 a[214:215], v209 offset:17408\n" \
"v_mfma_f32_16x16x32_bf16 v[48:51], a[36:39], a[20:23], v[48:51]\n" \
"buffer_load_dwordx4 v198, s[48:51], s77 offen offset:1024 lds\n" \
"v_mfma_f32_16x16x32_bf16 v[56:59], a[32:35], a[24:27], v[56:59]\n" \
"s_cmp_eq_u32 s90, 0\n" \
"s_cselect_b32 s14, s8, s14\n" \
"ds_read_b64_tr_b16 a[216:217], v210 offset:2048\n" \
"v_mfma_f32_16x16x32_bf16 v[56:59], a[36:39], a[28:31], v[56:59]\n" \
"s_sub_u32 s10, 49, s9\n" \
"s_lshl_b32 s11, s9, 1\n" \
"ds_read_b64_tr_b16 a[218:219], v210 offset:5120\n" \
"v_mfma_f32_16x16x32_bf16 v[16:19], a[40:43], a[24:27], v[16:19]\n" \
"s_sub_u32 s11, s11, 71\n" \
"s_lshl2_add_u32 s70, s8, s61\n" \
"ds_read_b64_tr_b16 a[220:221], v210 offset:14336\n" \
"v_mfma_f32_16x16x32_bf16 v[16:19], a[44:47], a[28:31], v[16:19]\n" \
"buffer_load_dwordx4 v199, s[48:51], s77 offen offset:2048 lds\n" \
"v_mfma_f32_16x16x32_bf16 v[20:23], a[48:51], a[24:27], v[20:23]\n" \
"s_mul_i32 s81, s70, 266240\n" \
"s_mul_i32 s82, s90, s9\n" \
"ds_read_b64_tr_b16 a[222:223], v210 offset:17408\n" \
"v_mfma_f32_16x16x32_bf16 v[20:23], a[52:55], a[28:31], v[20:23]\n" \
"s_waitcnt lgkmcnt(3)\n" \
"s_lshl_b32 s71, s82, 12\n" \
"s_add_u32 s81, s81, s71\n" \
"s_add_u32 s81, s81, 35700736\n" \
"v_mfma_f32_16x16x32_bf16 v[28:31], a[48:51], a[192:195], v[28:31]\n" \
"s_lshl2_add_u32 s70, s14, s61\n" \
"s_mul_i32 s83, s70, 69632\n" \
"s_add_u32 s83, s83, 52740096\n" \
"v_mfma_f32_16x16x32_bf16 v[28:31], a[52:55], a[196:199], v[28:31]\n" \
"s_lshl_b32 s82, s82, 17\n" \
"s_add_u32 s82, s82, s84\n" \
"s_lshl_b32 s70, s14, 19\n" \
"v_mfma_f32_16x16x32_bf16 v[24:27], a[40:43], a[192:195], v[24:27]\n" \
"s_add_u32 s88, s88, s70\n" \
"s_lshl_b32 s71, s90, 11\n" \
"s_add_u32 s88, s88, s71\n" \
"v_mfma_f32_16x16x32_bf16 v[24:27], a[44:47], a[196:199], v[24:27]\n" \
"s_lshl3_add_u32 s70, s8, s68\n" \
"s_lshl2_add_u32 s87, s70, s61\n" \
"s_lshl_b32 s87, s87, 14\n" \
"v_mfma_f32_16x16x32_bf16 v[64:67], a[32:35], a[192:195], v[64:67]\n" \
"s_add_u32 s87, s87, 57196544\n" \
"s_lshl_b32 s64, s70, 7\n" \
"s_add_u32 s64, s64, 65601536\n" \
"v_mfma_f32_16x16x32_bf16 v[64:67], a[36:39], a[196:199], v[64:67]\n" \
"s_lshl_b32 s66, s8, 7\n" \
"s_waitcnt vmcnt(11) lgkmcnt(0)\n" \
"s_barrier\n" \
"v_mfma_f32_16x16x32_bf16 v[72:75], a[0:3], a[208:211], v[72:75]\n" \
"s_lshl_b32 s67, s14, 7\n" \
"ds_read_b64_tr_b16 a[32:33], v204 offset:21504\n" \
"ds_read_b64_tr_b16 a[34:35], v204 offset:24576\n" \
"v_mfma_f32_16x16x32_bf16 v[72:75], a[4:7], a[212:215], v[72:75]\n" \
"s_add_u32 s67, s67, 65591296\n" \
"ds_read_b64_tr_b16 a[36:37], v204 offset:33792\n" \
"ds_read_b64_tr_b16 a[38:39], v204 offset:36864\n" \
"v_mfma_f32_16x16x32_bf16 v[80:83], a[8:11], a[208:211], v[80:83]\n" \
"s_add_u32 m0, s79, 65536\n" \
"s_add_u32 s66, s66, 65589248\n" \
"ds_read_b128 a[0:3], v202 offset:0\n" \
"v_mfma_f32_16x16x32_bf16 v[80:83], a[12:15], a[212:215], v[80:83]\n" \
"buffer_load_dwordx4 v192, s[44:47], s73 offen offset:0 lds\n" \
"v_mfma_f32_16x16x32_bf16 v[84:87], a[8:11], a[216:219], v[84:87]\n" \
"s_add_u32 s70, s9, 1\n" \
"s_sub_u32 s70, s70, s90\n" \
"ds_read_b128 a[4:7], v203 offset:0\n" \
"v_mfma_f32_16x16x32_bf16 v[84:87], a[12:15], a[220:223], v[84:87]\n" \
"s_lshr_b32 s70, s70, 1\n" \
"s_bfm_b32 s65, s70, 0\n" \
"ds_read_b64_tr_b16 a[40:41], v204 offset:20480\n" \
"v_mfma_f32_16x16x32_bf16 v[44:47], a[200:203], a[8:11], v[44:47]\n" \
"s_sub_u32 s70, 0, s90\n" \
"s_xor_b32 s65, s65, s70\n" \
"ds_read_b64_tr_b16 a[42:43], v204 offset:23552\n" \
"v_mfma_f32_16x16x32_bf16 v[44:47], a[204:207], a[12:15], v[44:47]\n" \
"buffer_load_dwordx4 v193, s[44:47], s73 offen offset:1024 lds\n" \
"v_mfma_f32_16x16x32_bf16 v[52:55], a[200:203], a[16:19], v[52:55]\n" \
"s_and_b32 s74, s70, 0xffff\n" \
"s_xor_b32 s68, s68, s8\n" \
"ds_read_b128 a[8:11], v200 offset:0\n" \
"v_mfma_f32_16x16x32_bf16 v[52:55], a[204:207], a[20:23], v[52:55]\n" \
"s_and_b32 s68, s68, 3\n" \
"s_lshl_b32 s68, s68, 12\n" \
"ds_read_b128 a[12:15], v201 offset:0\n" \
"v_mfma_f32_16x16x32_bf16 v[92:95], a[16:19], a[216:219], v[92:95]\n" \
"ds_read_b64_tr_b16 a[44:45], v204 offset:32768\n" \
"v_mfma_f32_16x16x32_bf16 v[92:95], a[20:23], a[220:223], v[92:95]\n" \
"buffer_load_dwordx4 v192, s[44:47], s73 offen offset:2048 lds\n" \
"v_mfma_f32_16x16x32_bf16 v[88:91], a[16:19], a[208:211], v[88:91]\n" \
"ds_read_b64_tr_b16 a[46:47], v204 offset:35840\n" \
"v_mfma_f32_16x16x32_bf16 v[88:91], a[20:23], a[212:215], v[88:91]\n" \
"ds_read_b64_tr_b16 a[48:49], v222 offset:20480\n" \
"v_mfma_f32_16x16x32_bf16 v[96:99], a[24:27], a[208:211], v[96:99]\n" \
"ds_read_b128 a[16:19], v200 offset:4096\n" \
"v_mfma_f32_16x16x32_bf16 v[96:99], a[28:31], a[212:215], v[96:99]\n" \
"buffer_load_dwordx4 v193, s[44:47], s73 offen offset:3072 lds\n" \
"v_mfma_f32_16x16x32_bf16 v[100:103], a[24:27], a[216:219], v[100:103]\n" \
"ds_read_b128 a[20:23], v201 offset:4096\n" \
"v_mfma_f32_16x16x32_bf16 v[100:103], a[28:31], a[220:223], v[100:103]\n" \
"s_add_u32 m0, s89, 65536\n" \
"ds_read_b64_tr_b16 a[50:51], v222 offset:23552\n" \
"v_mfma_f32_16x16x32_bf16 v[60:63], a[200:203], a[24:27], v[60:63]\n" \
"s_xor_b32 s73, s73, 0x180\n" \
"ds_read_b64_tr_b16 a[52:53], v222 offset:32768\n" \
"v_mfma_f32_16x16x32_bf16 v[60:63], a[204:207], a[28:31], v[60:63]\n" \
"buffer_load_dwordx4 v196, s[44:47], s75 offen lds\n" \
"v_mfma_f32_16x16x32_bf16 v[68:71], a[200:203], a[192:195], v[68:71]\n" \
"s_xor_b32 s75, s75, 0x180\n" \
"s_mul_i32 s76, s80, 393216\n" \
"ds_read_b128 a[24:27], v200 offset:8192\n" \
"v_mfma_f32_16x16x32_bf16 v[68:71], a[204:207], a[196:199], v[68:71]\n" \
"s_add_u32 s76, s76, s78\n" \
"ds_read_b128 a[28:31], v201 offset:8192\n" \
"v_mfma_f32_16x16x32_bf16 v[108:111], a[192:195], a[216:219], v[108:111]\n" \
"s_add_u32 m0, s91, 86016\n" \
"ds_read_b64_tr_b16 a[54:55], v222 offset:35840\n" \
"v_mfma_f32_16x16x32_bf16 v[108:111], a[196:199], a[220:223], v[108:111]\n" \
"buffer_load_dwordx4 v197, s[48:51], s76 offen offset:0 lds\n" \
"v_mfma_f32_16x16x32_bf16 v[104:107], a[192:195], a[208:211], v[104:107]\n" \
"v_mfma_f32_16x16x32_bf16 v[104:107], a[196:199], a[212:215], v[104:107]\n" \
"s_waitcnt lgkmcnt(2)\n" \
"v_mfma_f32_16x16x32_bf16 v[32:35], a[32:35], a[0:3], v[32:35]\n" \
"s_add_u32 s77, s76, 24576\n" \
"ds_read_b128 a[192:195], v200 offset:12288\n" \
"v_mfma_f32_16x16x32_bf16 v[32:35], a[36:39], a[4:7], v[32:35]\n" \
"buffer_load_dwordx4 v198, s[48:51], s76 offen offset:1024 lds\n" \
"v_mfma_f32_16x16x32_bf16 v[40:43], a[32:35], a[8:11], v[40:43]\n" \
"ds_read_b128 a[196:199], v201 offset:12288\n" \
"v_mfma_f32_16x16x32_bf16 v[40:43], a[36:39], a[12:15], v[40:43]\n" \
"ds_read_b64_tr_b16 a[200:201], v222 offset:21504\n" \
"v_mfma_f32_16x16x32_bf16 v[0:3], a[40:43], a[8:11], v[0:3]\n" \
"s_waitcnt lgkmcnt(3)\n" \
"s_xor_b32 s80, s80, 0x3\n" \
"ds_read_b64_tr_b16 a[202:203], v222 offset:24576\n" \
"v_mfma_f32_16x16x32_bf16 v[0:3], a[44:47], a[12:15], v[0:3]\n" \
"buffer_load_dwordx4 v199, s[48:51], s76 offen offset:2048 lds\n" \
"v_mfma_f32_16x16x32_bf16 v[4:7], a[48:51], a[8:11], v[4:7]\n" \
"ds_read_b64_tr_b16 a[204:205], v222 offset:33792\n" \
"v_mfma_f32_16x16x32_bf16 v[4:7], a[52:55], a[12:15], v[4:7]\n" \
"ds_read_b64_tr_b16 a[206:207], v222 offset:36864\n" \
"v_mfma_f32_16x16x32_bf16 v[12:15], a[48:51], a[16:19], v[12:15]\n" \
"s_add_u32 m0, m0, 3072\n" \
"ds_read_b64_tr_b16 a[208:209], v204 offset:22528\n" \
"v_mfma_f32_16x16x32_bf16 v[12:15], a[52:55], a[20:23], v[12:15]\n" \
"buffer_load_dwordx4 v197, s[48:51], s77 offen offset:0 lds\n" \
"v_mfma_f32_16x16x32_bf16 v[8:11], a[40:43], a[16:19], v[8:11]\n" \
"ds_read_b64_tr_b16 a[210:211], v204 offset:25600\n" \
"v_mfma_f32_16x16x32_bf16 v[8:11], a[44:47], a[20:23], v[8:11]\n" \
"ds_read_b64_tr_b16 a[212:213], v204 offset:34816\n" \
"v_mfma_f32_16x16x32_bf16 v[48:51], a[32:35], a[16:19], v[48:51]\n" \
"ds_read_b64_tr_b16 a[214:215], v204 offset:37888\n" \
"v_mfma_f32_16x16x32_bf16 v[48:51], a[36:39], a[20:23], v[48:51]\n" \
"buffer_load_dwordx4 v198, s[48:51], s77 offen offset:1024 lds\n" \
"v_mfma_f32_16x16x32_bf16 v[56:59], a[32:35], a[24:27], v[56:59]\n" \
"ds_read_b64_tr_b16 a[216:217], v222 offset:22528\n" \
"v_mfma_f32_16x16x32_bf16 v[56:59], a[36:39], a[28:31], v[56:59]\n" \
"ds_read_b64_tr_b16 a[218:219], v222 offset:25600\n" \
"v_mfma_f32_16x16x32_bf16 v[16:19], a[40:43], a[24:27], v[16:19]\n" \
"ds_read_b64_tr_b16 a[220:221], v222 offset:34816\n" \
"v_mfma_f32_16x16x32_bf16 v[16:19], a[44:47], a[28:31], v[16:19]\n" \
"buffer_load_dwordx4 v199, s[48:51], s77 offen offset:2048 lds\n" \
"v_mfma_f32_16x16x32_bf16 v[20:23], a[48:51], a[24:27], v[20:23]\n" \
"ds_read_b64_tr_b16 a[222:223], v222 offset:37888\n" \
"v_mfma_f32_16x16x32_bf16 v[20:23], a[52:55], a[28:31], v[20:23]\n" \
"s_waitcnt lgkmcnt(3)\n" \
"v_mfma_f32_16x16x32_bf16 v[28:31], a[48:51], a[192:195], v[28:31]\n" \
"v_mfma_f32_16x16x32_bf16 v[28:31], a[52:55], a[196:199], v[28:31]\n" \
"v_mfma_f32_16x16x32_bf16 v[24:27], a[40:43], a[192:195], v[24:27]\n" \
"v_mfma_f32_16x16x32_bf16 v[24:27], a[44:47], a[196:199], v[24:27]\n" \
"v_mfma_f32_16x16x32_bf16 v[64:67], a[32:35], a[192:195], v[64:67]\n" \
"v_mfma_f32_16x16x32_bf16 v[64:67], a[36:39], a[196:199], v[64:67]\n" \
"s_waitcnt vmcnt(11) lgkmcnt(0)\n" \
"s_barrier\n" \
"v_mfma_f32_16x16x32_bf16 v[72:75], a[0:3], a[208:211], v[72:75]\n" \
"ds_read_b64_tr_b16 a[32:33], v194 offset:1024\n" \
"ds_read_b64_tr_b16 a[34:35], v194 offset:4096\n" \
"v_mfma_f32_16x16x32_bf16 v[72:75], a[4:7], a[212:215], v[72:75]\n" \
"ds_read_b64_tr_b16 a[36:37], v194 offset:13312\n" \
"ds_read_b64_tr_b16 a[38:39], v194 offset:16384\n" \
"v_mfma_f32_16x16x32_bf16 v[80:83], a[8:11], a[208:211], v[80:83]\n" \
"s_add_u32 m0, s79, 0\n" \
"ds_read_b128 a[0:3], v207 offset:45056\n" \
"v_mfma_f32_16x16x32_bf16 v[80:83], a[12:15], a[212:215], v[80:83]\n" \
"buffer_load_dwordx4 v192, s[44:47], s73 offen offset:0 lds\n" \
"v_mfma_f32_16x16x32_bf16 v[84:87], a[8:11], a[216:219], v[84:87]\n" \
"ds_read_b128 a[4:7], v208 offset:45056\n" \
"v_mfma_f32_16x16x32_bf16 v[84:87], a[12:15], a[220:223], v[84:87]\n" \
"ds_read_b64_tr_b16 a[40:41], v194 offset:0\n" \
"v_mfma_f32_16x16x32_bf16 v[44:47], a[200:203], a[8:11], v[44:47]\n" \
"ds_read_b64_tr_b16 a[42:43], v194 offset:3072\n" \
"v_mfma_f32_16x16x32_bf16 v[44:47], a[204:207], a[12:15], v[44:47]\n" \
"buffer_load_dwordx4 v193, s[44:47], s73 offen offset:1024 lds\n" \
"v_mfma_f32_16x16x32_bf16 v[52:55], a[200:203], a[16:19], v[52:55]\n" \
"ds_read_b128 a[8:11], v205 offset:45056\n" \
"v_mfma_f32_16x16x32_bf16 v[52:55], a[204:207], a[20:23], v[52:55]\n" \
"ds_read_b128 a[12:15], v206 offset:45056\n" \
"v_mfma_f32_16x16x32_bf16 v[92:95], a[16:19], a[216:219], v[92:95]\n" \
"ds_read_b64_tr_b16 a[44:45], v194 offset:12288\n" \
"v_mfma_f32_16x16x32_bf16 v[92:95], a[20:23], a[220:223], v[92:95]\n" \
"buffer_load_dwordx4 v192, s[44:47], s73 offen offset:2048 lds\n" \
"v_mfma_f32_16x16x32_bf16 v[88:91], a[16:19], a[208:211], v[88:91]\n" \
"ds_read_b64_tr_b16 a[46:47], v194 offset:15360\n" \
"v_mfma_f32_16x16x32_bf16 v[88:91], a[20:23], a[212:215], v[88:91]\n" \
"ds_read_b64_tr_b16 a[48:49], v195 offset:0\n" \
"v_mfma_f32_16x16x32_bf16 v[96:99], a[24:27], a[208:211], v[96:99]\n" \
"ds_read_b128 a[16:19], v205 offset:49152\n" \
"v_mfma_f32_16x16x32_bf16 v[96:99], a[28:31], a[212:215], v[96:99]\n" \
"buffer_load_dwordx4 v193, s[44:47], s73 offen offset:3072 lds\n" \
"v_mfma_f32_16x16x32_bf16 v[100:103], a[24:27], a[216:219], v[100:103]\n" \
"ds_read_b128 a[20:23], v206 offset:49152\n" \
"v_mfma_f32_16x16x32_bf16 v[100:103], a[28:31], a[220:223], v[100:103]\n" \
"s_add_u32 m0, s89, 0\n" \
"ds_read_b64_tr_b16 a[50:51], v195 offset:3072\n" \
"v_mfma_f32_16x16x32_bf16 v[60:63], a[200:203], a[24:27], v[60:63]\n" \
"s_xor_b32 s73, s73, 0x80\n" \
"ds_read_b64_tr_b16 a[52:53], v195 offset:12288\n" \
"v_mfma_f32_16x16x32_bf16 v[60:63], a[204:207], a[28:31], v[60:63]\n" \
"buffer_load_dwordx4 v196, s[44:47], s75 offen lds\n" \
"v_mfma_f32_16x16x32_bf16 v[68:71], a[200:203], a[192:195], v[68:71]\n" \
"s_xor_b32 s75, s75, 0x80\n" \
"s_mul_i32 s76, s80, 393216\n" \
"ds_read_b128 a[24:27], v205 offset:53248\n" \
"v_mfma_f32_16x16x32_bf16 v[68:71], a[204:207], a[196:199], v[68:71]\n" \
"s_add_u32 s76, s76, s78\n" \
"ds_read_b128 a[28:31], v206 offset:53248\n" \
"v_mfma_f32_16x16x32_bf16 v[108:111], a[192:195], a[216:219], v[108:111]\n" \
"s_add_u32 m0, s91, 20480\n" \
"ds_read_b64_tr_b16 a[54:55], v195 offset:15360\n" \
"v_mfma_f32_16x16x32_bf16 v[108:111], a[196:199], a[220:223], v[108:111]\n" \
"buffer_load_dwordx4 v197, s[48:51], s76 offen offset:0 lds\n" \
"v_mfma_f32_16x16x32_bf16 v[104:107], a[192:195], a[208:211], v[104:107]\n" \
"v_mfma_f32_16x16x32_bf16 v[104:107], a[196:199], a[212:215], v[104:107]\n" \
"s_waitcnt lgkmcnt(2)\n" \
"v_mfma_f32_16x16x32_bf16 v[32:35], a[32:35], a[0:3], v[32:35]\n" \
"s_add_u32 s77, s76, 24576\n" \
"ds_read_b128 a[192:195], v205 offset:57344\n" \
"v_mfma_f32_16x16x32_bf16 v[32:35], a[36:39], a[4:7], v[32:35]\n" \
"buffer_load_dwordx4 v198, s[48:51], s76 offen offset:1024 lds\n" \
"v_mfma_f32_16x16x32_bf16 v[40:43], a[32:35], a[8:11], v[40:43]\n" \
"ds_read_b128 a[196:199], v206 offset:57344\n" \
"v_mfma_f32_16x16x32_bf16 v[40:43], a[36:39], a[12:15], v[40:43]\n" \
"ds_read_b64_tr_b16 a[200:201], v195 offset:1024\n" \
"v_mfma_f32_16x16x32_bf16 v[0:3], a[40:43], a[8:11], v[0:3]\n" \
"s_waitcnt lgkmcnt(3)\n" \
"s_xor_b32 s80, s80, 0x1\n" \
"ds_read_b64_tr_b16 a[202:203], v195 offset:4096\n" \
"v_mfma_f32_16x16x32_bf16 v[0:3], a[44:47], a[12:15], v[0:3]\n" \
"buffer_load_dwordx4 v199, s[48:51], s76 offen offset:2048 lds\n" \
"v_mfma_f32_16x16x32_bf16 v[4:7], a[48:51], a[8:11], v[4:7]\n" \
"ds_read_b64_tr_b16 a[204:205], v195 offset:13312\n" \
"v_mfma_f32_16x16x32_bf16 v[4:7], a[52:55], a[12:15], v[4:7]\n" \
"ds_read_b64_tr_b16 a[206:207], v195 offset:16384\n" \
"v_mfma_f32_16x16x32_bf16 v[12:15], a[48:51], a[16:19], v[12:15]\n" \
"s_add_u32 m0, m0, 3072\n" \
"ds_read_b64_tr_b16 a[208:209], v194 offset:2048\n" \
"v_mfma_f32_16x16x32_bf16 v[12:15], a[52:55], a[20:23], v[12:15]\n" \
"buffer_load_dwordx4 v197, s[48:51], s77 offen offset:0 lds\n" \
"v_mfma_f32_16x16x32_bf16 v[8:11], a[40:43], a[16:19], v[8:11]\n" \
"ds_read_b64_tr_b16 a[210:211], v194 offset:5120\n" \
"v_mfma_f32_16x16x32_bf16 v[8:11], a[44:47], a[20:23], v[8:11]\n" \
"ds_read_b64_tr_b16 a[212:213], v194 offset:14336\n" \
"v_mfma_f32_16x16x32_bf16 v[48:51], a[32:35], a[16:19], v[48:51]\n" \
"ds_read_b64_tr_b16 a[214:215], v194 offset:17408\n" \
"v_mfma_f32_16x16x32_bf16 v[48:51], a[36:39], a[20:23], v[48:51]\n" \
"buffer_load_dwordx4 v198, s[48:51], s77 offen offset:1024 lds\n" \
"v_mfma_f32_16x16x32_bf16 v[56:59], a[32:35], a[24:27], v[56:59]\n" \
"ds_read_b64_tr_b16 a[216:217], v195 offset:2048\n" \
"v_mfma_f32_16x16x32_bf16 v[56:59], a[36:39], a[28:31], v[56:59]\n" \
"ds_read_b64_tr_b16 a[218:219], v195 offset:5120\n" \
"v_mfma_f32_16x16x32_bf16 v[16:19], a[40:43], a[24:27], v[16:19]\n" \
"ds_read_b64_tr_b16 a[220:221], v195 offset:14336\n" \
"v_mfma_f32_16x16x32_bf16 v[16:19], a[44:47], a[28:31], v[16:19]\n" \
"buffer_load_dwordx4 v199, s[48:51], s77 offen offset:2048 lds\n" \
"v_mfma_f32_16x16x32_bf16 v[20:23], a[48:51], a[24:27], v[20:23]\n" \
"ds_read_b64_tr_b16 a[222:223], v195 offset:17408\n" \
"v_mfma_f32_16x16x32_bf16 v[20:23], a[52:55], a[28:31], v[20:23]\n" \
"s_waitcnt lgkmcnt(3)\n" \
"v_mfma_f32_16x16x32_bf16 v[28:31], a[48:51], a[192:195], v[28:31]\n" \
"v_mfma_f32_16x16x32_bf16 v[28:31], a[52:55], a[196:199], v[28:31]\n" \
"buffer_load_dword v220, v221, s[56:59], s66 offen sc1\n" \
"v_mfma_f32_16x16x32_bf16 v[24:27], a[40:43], a[192:195], v[24:27]\n" \
"v_mfma_f32_16x16x32_bf16 v[24:27], a[44:47], a[196:199], v[24:27]\n" \
"v_mfma_f32_16x16x32_bf16 v[64:67], a[32:35], a[192:195], v[64:67]\n" \
"v_mfma_f32_16x16x32_bf16 v[64:67], a[36:39], a[196:199], v[64:67]\n" \
"s_waitcnt vmcnt(12) lgkmcnt(0)\n" \
"s_barrier\n" \
"v_mfma_f32_16x16x32_bf16 v[72:75], a[0:3], a[208:211], v[72:75]\n" \
"ds_read_b64_tr_b16 a[32:33], v209 offset:1024\n" \
"ds_read_b64_tr_b16 a[34:35], v209 offset:4096\n" \
"v_mfma_f32_16x16x32_bf16 v[72:75], a[4:7], a[212:215], v[72:75]\n" \
"ds_read_b64_tr_b16 a[36:37], v209 offset:13312\n" \
"ds_read_b64_tr_b16 a[38:39], v209 offset:16384\n" \
"v_mfma_f32_16x16x32_bf16 v[80:83], a[8:11], a[208:211], v[80:83]\n" \
"s_add_u32 m0, s79, 110592\n" \
"ds_read_b128 a[0:3], v207 offset:0\n" \
"v_mfma_f32_16x16x32_bf16 v[80:83], a[12:15], a[212:215], v[80:83]\n" \
"buffer_load_dwordx4 v192, s[44:47], s73 offen offset:0 lds\n" \
"v_mfma_f32_16x16x32_bf16 v[84:87], a[8:11], a[216:219], v[84:87]\n" \
"ds_read_b128 a[4:7], v208 offset:0\n" \
"v_mfma_f32_16x16x32_bf16 v[84:87], a[12:15], a[220:223], v[84:87]\n" \
"ds_read_b64_tr_b16 a[40:41], v209 offset:0\n" \
"v_mfma_f32_16x16x32_bf16 v[44:47], a[200:203], a[8:11], v[44:47]\n" \
"ds_read_b64_tr_b16 a[42:43], v209 offset:3072\n" \
"v_mfma_f32_16x16x32_bf16 v[44:47], a[204:207], a[12:15], v[44:47]\n" \
"buffer_load_dwordx4 v193, s[44:47], s73 offen offset:1024 lds\n" \
"v_mfma_f32_16x16x32_bf16 v[52:55], a[200:203], a[16:19], v[52:55]\n" \
"ds_read_b128 a[8:11], v205 offset:0\n" \
"v_mfma_f32_16x16x32_bf16 v[52:55], a[204:207], a[20:23], v[52:55]\n" \
"ds_read_b128 a[12:15], v206 offset:0\n" \
"v_mfma_f32_16x16x32_bf16 v[92:95], a[16:19], a[216:219], v[92:95]\n" \
"ds_read_b64_tr_b16 a[44:45], v209 offset:12288\n" \
"v_mfma_f32_16x16x32_bf16 v[92:95], a[20:23], a[220:223], v[92:95]\n" \
"buffer_load_dwordx4 v192, s[44:47], s73 offen offset:2048 lds\n" \
"v_mfma_f32_16x16x32_bf16 v[88:91], a[16:19], a[208:211], v[88:91]\n" \
"ds_read_b64_tr_b16 a[46:47], v209 offset:15360\n" \
"v_mfma_f32_16x16x32_bf16 v[88:91], a[20:23], a[212:215], v[88:91]\n" \
"ds_read_b64_tr_b16 a[48:49], v210 offset:0\n" \
"v_mfma_f32_16x16x32_bf16 v[96:99], a[24:27], a[208:211], v[96:99]\n" \
"ds_read_b128 a[16:19], v205 offset:4096\n" \
"v_mfma_f32_16x16x32_bf16 v[96:99], a[28:31], a[212:215], v[96:99]\n" \
"buffer_load_dwordx4 v193, s[44:47], s73 offen offset:3072 lds\n" \
"v_mfma_f32_16x16x32_bf16 v[100:103], a[24:27], a[216:219], v[100:103]\n" \
"ds_read_b128 a[20:23], v206 offset:4096\n" \
"v_mfma_f32_16x16x32_bf16 v[100:103], a[28:31], a[220:223], v[100:103]\n" \
"s_add_u32 m0, s89, 110592\n" \
"ds_read_b64_tr_b16 a[50:51], v210 offset:3072\n" \
"v_mfma_f32_16x16x32_bf16 v[60:63], a[200:203], a[24:27], v[60:63]\n" \
"s_xor_b32 s73, s73, 0x80\n" \
"ds_read_b64_tr_b16 a[52:53], v210 offset:12288\n" \
"v_mfma_f32_16x16x32_bf16 v[60:63], a[204:207], a[28:31], v[60:63]\n" \
"buffer_load_dwordx4 v196, s[44:47], s75 offen lds\n" \
"v_mfma_f32_16x16x32_bf16 v[68:71], a[200:203], a[192:195], v[68:71]\n" \
"s_xor_b32 s75, s75, 0x80\n" \
"s_mul_i32 s76, s80, 393216\n" \
"ds_read_b128 a[24:27], v205 offset:8192\n" \
"v_mfma_f32_16x16x32_bf16 v[68:71], a[204:207], a[196:199], v[68:71]\n" \
"s_add_u32 s76, s76, s78\n" \
"ds_read_b128 a[28:31], v206 offset:8192\n" \
"v_mfma_f32_16x16x32_bf16 v[108:111], a[192:195], a[216:219], v[108:111]\n" \
"s_add_u32 m0, s91, 139264\n" \
"ds_read_b64_tr_b16 a[54:55], v210 offset:15360\n" \
"v_mfma_f32_16x16x32_bf16 v[108:111], a[196:199], a[220:223], v[108:111]\n" \
"buffer_load_dwordx4 v197, s[48:51], s76 offen offset:0 lds\n" \
"v_mfma_f32_16x16x32_bf16 v[104:107], a[192:195], a[208:211], v[104:107]\n" \
"v_mfma_f32_16x16x32_bf16 v[104:107], a[196:199], a[212:215], v[104:107]\n" \
"s_waitcnt lgkmcnt(2)\n" \
"v_mfma_f32_16x16x32_bf16 v[32:35], a[32:35], a[0:3], v[32:35]\n" \
"s_add_u32 s77, s76, 24576\n" \
"ds_read_b128 a[192:195], v205 offset:12288\n" \
"v_mfma_f32_16x16x32_bf16 v[32:35], a[36:39], a[4:7], v[32:35]\n" \
"buffer_load_dwordx4 v198, s[48:51], s76 offen offset:1024 lds\n" \
"v_mfma_f32_16x16x32_bf16 v[40:43], a[32:35], a[8:11], v[40:43]\n" \
"ds_read_b128 a[196:199], v206 offset:12288\n" \
"v_mfma_f32_16x16x32_bf16 v[40:43], a[36:39], a[12:15], v[40:43]\n" \
"ds_read_b64_tr_b16 a[200:201], v210 offset:1024\n" \
"v_mfma_f32_16x16x32_bf16 v[0:3], a[40:43], a[8:11], v[0:3]\n" \
"s_waitcnt lgkmcnt(3)\n" \
"s_xor_b32 s80, s80, 0x1\n" \
"ds_read_b64_tr_b16 a[202:203], v210 offset:4096\n" \
"v_mfma_f32_16x16x32_bf16 v[0:3], a[44:47], a[12:15], v[0:3]\n" \
"buffer_load_dwordx4 v199, s[48:51], s76 offen offset:2048 lds\n" \
"v_mfma_f32_16x16x32_bf16 v[4:7], a[48:51], a[8:11], v[4:7]\n" \
"ds_read_b64_tr_b16 a[204:205], v210 offset:13312\n" \
"v_mfma_f32_16x16x32_bf16 v[4:7], a[52:55], a[12:15], v[4:7]\n" \
"ds_read_b64_tr_b16 a[206:207], v210 offset:16384\n" \
"v_mfma_f32_16x16x32_bf16 v[12:15], a[48:51], a[16:19], v[12:15]\n" \
"s_add_u32 m0, m0, 3072\n" \
"ds_read_b64_tr_b16 a[208:209], v209 offset:2048\n" \
"v_mfma_f32_16x16x32_bf16 v[12:15], a[52:55], a[20:23], v[12:15]\n" \
"buffer_load_dwordx4 v197, s[48:51], s77 offen offset:0 lds\n" \
"v_mfma_f32_16x16x32_bf16 v[8:11], a[40:43], a[16:19], v[8:11]\n" \
"ds_read_b64_tr_b16 a[210:211], v209 offset:5120\n" \
"v_mfma_f32_16x16x32_bf16 v[8:11], a[44:47], a[20:23], v[8:11]\n" \
"ds_read_b64_tr_b16 a[212:213], v209 offset:14336\n" \
"v_mfma_f32_16x16x32_bf16 v[48:51], a[32:35], a[16:19], v[48:51]\n" \
"ds_read_b64_tr_b16 a[214:215], v209 offset:17408\n" \
"v_mfma_f32_16x16x32_bf16 v[48:51], a[36:39], a[20:23], v[48:51]\n" \
"buffer_load_dwordx4 v198, s[48:51], s77 offen offset:1024 lds\n" \
"v_mfma_f32_16x16x32_bf16 v[56:59], a[32:35], a[24:27], v[56:59]\n" \
"ds_read_b64_tr_b16 a[216:217], v210 offset:2048\n" \
"v_mfma_f32_16x16x32_bf16 v[56:59], a[36:39], a[28:31], v[56:59]\n" \
"ds_read_b64_tr_b16 a[218:219], v210 offset:5120\n" \
"v_mfma_f32_16x16x32_bf16 v[16:19], a[40:43], a[24:27], v[16:19]\n" \
"s_waitcnt lgkmcnt(3)\n" \
"ds_read_b64_tr_b16 a[220:221], v210 offset:14336\n" \
"v_mfma_f32_16x16x32_bf16 v[16:19], a[44:47], a[28:31], v[16:19]\n" \
"buffer_load_dwordx4 v199, s[48:51], s77 offen offset:2048 lds\n" \
"v_mfma_f32_16x16x32_bf16 v[20:23], a[48:51], a[24:27], v[20:23]\n" \
"s_mov_b32 s46, s81\n" \
"s_mov_b32 s47, s82\n" \
"ds_read_b64_tr_b16 a[222:223], v210 offset:17408\n" \
"v_mfma_f32_16x16x32_bf16 v[20:23], a[52:55], a[28:31], v[20:23]\n" \
"s_mov_b32 s48, 0\n" \
"s_mov_b32 s49, s68\n" \
"s_sub_u32 s50, 64, s9\n" \
"v_mfma_f32_16x16x32_bf16 v[28:31], a[48:51], a[192:195], v[28:31]\n" \
"s_cmp_eq_u32 s90, 0\n" \
"s_cselect_b32 s50, s9, s50\n" \
"s_andn2_b32 s50, s50, 3\n" \
"v_mfma_f32_16x16x32_bf16 v[28:31], a[52:55], a[196:199], v[28:31]\n" \
"s_lshl_b32 s50, s50, 12\n" \
"s_waitcnt vmcnt(11)\n" \
"v_cmp_eq_u32 vcc, s60, v220\n" \
"v_mfma_f32_16x16x32_bf16 v[24:27], a[40:43], a[192:195], v[24:27]\n" \
"v_mfma_f32_16x16x32_bf16 v[24:27], a[44:47], a[196:199], v[24:27]\n" \
"s_andn2_b32 s70, s65, vcc_lo\n" \
"s_cbranch_scc1 Lpl5_%=\n" \
"Lpr5_%=:\n" \
"v_mfma_f32_16x16x32_bf16 v[64:67], a[32:35], a[192:195], v[64:67]\n" \
"v_mfma_f32_16x16x32_bf16 v[64:67], a[36:39], a[196:199], v[64:67]\n" \
"s_xor_b32 s82, s48, s49\n" \
"s_cmp_lt_u32 s48, s50\n" \
"s_cselect_b32 s82, s82, s48\n" \
"s_waitcnt vmcnt(12) lgkmcnt(0)\n" \
"s_barrier\n" \
"v_mfma_f32_16x16x32_bf16 v[72:75], a[0:3], a[208:211], v[72:75]\n" \
"ds_read_b64_tr_b16 a[32:33], v204 offset:21504\n" \
"ds_read_b64_tr_b16 a[34:35], v204 offset:24576\n" \
"v_mfma_f32_16x16x32_bf16 v[72:75], a[4:7], a[212:215], v[72:75]\n" \
"s_add_u32 s81, s82, s46\n" \
"s_lshr_b32 s78, s61, 1\n" \
"s_lshl_b32 s80, s78, 9\n" \
"s_add_u32 s80, s80, 511\n" \
"ds_read_b64_tr_b16 a[36:37], v204 offset:33792\n" \
"ds_read_b64_tr_b16 a[38:39], v204 offset:36864\n" \
"v_mfma_f32_16x16x32_bf16 v[80:83], a[8:11], a[208:211], v[80:83]\n" \
"s_add_u32 m0, s86, s79\n" \
"ds_read_b128 a[0:3], v202 offset:0\n" \
"v_sub_u32 v145, s80, v234\n" \
"v_mfma_f32_16x16x32_bf16 v[80:83], a[12:15], a[212:215], v[80:83]\n" \
"buffer_load_dwordx4 v234, s[56:59], s81 offen offset:0 sc1 lds\n" \
"v_mfma_f32_16x16x32_bf16 v[84:87], a[8:11], a[216:219], v[84:87]\n" \
"s_lshl_b32 s80, s78, 3\n" \
"s_not_b32 s80, s80\n" \
"ds_read_b128 a[4:7], v203 offset:0\n" \
"v_mfma_f32_16x16x32_bf16 v[84:87], a[12:15], a[220:223], v[84:87]\n" \
"s_lshl_b32 s82, s82, 5\n" \
"ds_read_b64_tr_b16 a[40:41], v204 offset:20480\n" \
"v_sub_u32 v146, s80, v238\n" \
"v_mfma_f32_16x16x32_bf16 v[44:47], a[200:203], a[8:11], v[44:47]\n" \
"s_add_u32 s82, s82, s47\n" \
"ds_read_b64_tr_b16 a[42:43], v204 offset:23552\n" \
"v_subrev_u32 v152, 0, v238\n" \
"v_mfma_f32_16x16x32_bf16 v[44:47], a[204:207], a[12:15], v[44:47]\n" \
"buffer_load_dwordx4 v234, s[56:59], s81 offen offset:1024 sc1 lds\n" \
"v_mfma_f32_16x16x32_bf16 v[52:55], a[200:203], a[16:19], v[52:55]\n" \
"ds_read_b128 a[8:11], v200 offset:0\n" \
"v_sub_u32 v144, 7, v238\n" \
"v_mfma_f32_16x16x32_bf16 v[52:55], a[204:207], a[20:23], v[52:55]\n" \
"s_add_u32 s48, s48, 0x1000\n" \
"ds_read_b128 a[12:15], v201 offset:0\n" \
"v_or_b32 v152, v152, v144\n" \
"v_mfma_f32_16x16x32_bf16 v[92:95], a[16:19], a[216:219], v[92:95]\n" \
"s_sub_u32 s86, s86, 32768\n" \
"s_cselect_b32 s86, 131072, s86\n" \
"ds_read_b64_tr_b16 a[44:45], v204 offset:32768\n" \
"v_mfma_f32_16x16x32_bf16 v[92:95], a[20:23], a[220:223], v[92:95]\n" \
"buffer_load_dwordx4 v234, s[56:59], s81 offen offset:2048 sc1 lds\n" \
"v_mfma_f32_16x16x32_bf16 v[88:91], a[16:19], a[208:211], v[88:91]\n" \
"ds_read_b64_tr_b16 a[46:47], v204 offset:35840\n" \
"v_ashrrev_i32 v152, 31, v152\n" \
"v_mfma_f32_16x16x32_bf16 v[88:91], a[20:23], a[212:215], v[88:91]\n" \
"ds_read_b64_tr_b16 a[48:49], v222 offset:20480\n" \
"v_and_b32 v152, 0xff800000, v152\n" \
"v_mfma_f32_16x16x32_bf16 v[96:99], a[24:27], a[208:211], v[96:99]\n" \
"ds_read_b128 a[16:19], v200 offset:4096\n" \
"v_sub_u32 v156, -9, v238\n" \
"v_mfma_f32_16x16x32_bf16 v[96:99], a[28:31], a[212:215], v[96:99]\n" \
"buffer_load_dwordx4 v234, s[56:59], s81 offen offset:3072 sc1 lds\n" \
"v_mfma_f32_16x16x32_bf16 v[100:103], a[24:27], a[216:219], v[100:103]\n" \
"ds_read_b128 a[20:23], v201 offset:4096\n" \
"v_ashrrev_i32 v156, 31, v156\n" \
"v_mfma_f32_16x16x32_bf16 v[100:103], a[28:31], a[220:223], v[100:103]\n" \
"ds_read_b64_tr_b16 a[50:51], v222 offset:23552\n" \
"v_and_b32 v156, 0xff800000, v156\n" \
"v_mfma_f32_16x16x32_bf16 v[60:63], a[200:203], a[24:27], v[60:63]\n" \
"s_add_u32 m0, m0, 0x4000\n" \
"ds_read_b64_tr_b16 a[52:53], v222 offset:32768\n" \
"v_add_u32 v148, 0, v146\n" \
"v_mfma_f32_16x16x32_bf16 v[60:63], a[204:207], a[28:31], v[60:63]\n" \
"buffer_load_dwordx4 v216, s[36:39], s82 offen offset:0 lds\n" \
"v_mfma_f32_16x16x32_bf16 v[68:71], a[200:203], a[192:195], v[68:71]\n" \
"ds_read_b128 a[24:27], v200 offset:8192\n" \
"v_or3_b32 v148, v148, v145, s92\n" \
"v_mfma_f32_16x16x32_bf16 v[68:71], a[204:207], a[196:199], v[68:71]\n" \
"ds_read_b128 a[28:31], v201 offset:8192\n" \
"v_ashrrev_i32 v148, 31, v148\n" \
"v_mfma_f32_16x16x32_bf16 v[108:111], a[192:195], a[216:219], v[108:111]\n" \
"ds_read_b64_tr_b16 a[54:55], v222 offset:35840\n" \
"v_and_b32 v148, 0xff800000, v148\n" \
"v_mfma_f32_16x16x32_bf16 v[108:111], a[196:199], a[220:223], v[108:111]\n" \
"Lpg9633_%=:\n" \
"Lst1x3_%=:\n" \
"s_cbranch_execz Lpg11745_%=\n" \
"buffer_load_dwordx4 v217, s[36:39], s82 offen offset:1024 lds\n" \
"v_mfma_f32_16x16x32_bf16 v[104:107], a[192:195], a[208:211], v[104:107]\n" \
"v_subrev_u32 v153, 1, v238\n" \
"v_sub_u32 v144, 8, v238\n" \
"v_mfma_f32_16x16x32_bf16 v[104:107], a[196:199], a[212:215], v[104:107]\n" \
"s_waitcnt lgkmcnt(2)\n" \
"v_or_b32 v153, v153, v144\n" \
"v_ashrrev_i32 v153, 31, v153\n" \
"v_mfma_f32_16x16x32_bf16 v[32:35], a[32:35], a[0:3], v[32:35]\n" \
"ds_read_b128 a[192:195], v200 offset:12288\n" \
"v_and_b32 v153, 0xff800000, v153\n" \
"v_mfma_f32_16x16x32_bf16 v[32:35], a[36:39], a[4:7], v[32:35]\n" \
"buffer_load_dwordx4 v218, s[36:39], s82 offen offset:2048 lds\n" \
"v_mfma_f32_16x16x32_bf16 v[40:43], a[32:35], a[8:11], v[40:43]\n" \
"ds_read_b128 a[196:199], v201 offset:12288\n" \
"v_sub_u32 v157, -8, v238\n" \
"v_mfma_f32_16x16x32_bf16 v[40:43], a[36:39], a[12:15], v[40:43]\n" \
"ds_read_b64_tr_b16 a[200:201], v222 offset:21504\n" \
"v_ashrrev_i32 v157, 31, v157\n" \
"v_mfma_f32_16x16x32_bf16 v[0:3], a[40:43], a[8:11], v[0:3]\n" \
"s_waitcnt lgkmcnt(3)\n" \
"ds_read_b64_tr_b16 a[202:203], v222 offset:24576\n" \
"v_and_b32 v157, 0xff800000, v157\n" \
"v_mfma_f32_16x16x32_bf16 v[0:3], a[44:47], a[12:15], v[0:3]\n" \
"buffer_load_dwordx4 v219, s[36:39], s82 offen offset:3072 lds\n" \
"v_mfma_f32_16x16x32_bf16 v[4:7], a[48:51], a[8:11], v[4:7]\n" \
"ds_read_b64_tr_b16 a[204:205], v222 offset:33792\n" \
"v_add_u32 v149, 1, v146\n" \
"v_mfma_f32_16x16x32_bf16 v[4:7], a[52:55], a[12:15], v[4:7]\n" \
"ds_read_b64_tr_b16 a[206:207], v222 offset:36864\n" \
"v_or3_b32 v149, v149, v145, s92\n" \
"v_mfma_f32_16x16x32_bf16 v[12:15], a[48:51], a[16:19], v[12:15]\n" \
"ds_read_b64_tr_b16 a[208:209], v204 offset:22528\n" \
"v_ashrrev_i32 v149, 31, v149\n" \
"v_mfma_f32_16x16x32_bf16 v[12:15], a[52:55], a[20:23], v[12:15]\n" \
"v_and_b32 v149, 0xff800000, v149\n" \
"v_subrev_u32 v154, 2, v238\n" \
"v_mfma_f32_16x16x32_bf16 v[8:11], a[40:43], a[16:19], v[8:11]\n" \
"ds_read_b64_tr_b16 a[210:211], v204 offset:25600\n" \
"v_sub_u32 v144, 9, v238\n" \
"v_mfma_f32_16x16x32_bf16 v[8:11], a[44:47], a[20:23], v[8:11]\n" \
"ds_read_b64_tr_b16 a[212:213], v204 offset:34816\n" \
"v_or_b32 v154, v154, v144\n" \
"v_mfma_f32_16x16x32_bf16 v[48:51], a[32:35], a[16:19], v[48:51]\n" \
"ds_read_b64_tr_b16 a[214:215], v204 offset:37888\n" \
"v_ashrrev_i32 v154, 31, v154\n" \
"v_mfma_f32_16x16x32_bf16 v[48:51], a[36:39], a[20:23], v[48:51]\n" \
"v_and_b32 v154, 0xff800000, v154\n" \
"v_sub_u32 v158, -7, v238\n" \
"v_mfma_f32_16x16x32_bf16 v[56:59], a[32:35], a[24:27], v[56:59]\n" \
"ds_read_b64_tr_b16 a[216:217], v222 offset:22528\n" \
"v_ashrrev_i32 v158, 31, v158\n" \
"v_mfma_f32_16x16x32_bf16 v[56:59], a[36:39], a[28:31], v[56:59]\n" \
"ds_read_b64_tr_b16 a[218:219], v222 offset:25600\n" \
"v_and_b32 v158, 0xff800000, v158\n" \
"v_mfma_f32_16x16x32_bf16 v[16:19], a[40:43], a[24:27], v[16:19]\n" \
"ds_read_b64_tr_b16 a[220:221], v222 offset:34816\n" \
"v_add_u32 v150, 2, v146\n" \
"v_mfma_f32_16x16x32_bf16 v[16:19], a[44:47], a[28:31], v[16:19]\n" \
"v_or3_b32 v150, v150, v145, s92\n" \
"v_ashrrev_i32 v150, 31, v150\n" \
"v_mfma_f32_16x16x32_bf16 v[20:23], a[48:51], a[24:27], v[20:23]\n" \
"ds_read_b64_tr_b16 a[222:223], v222 offset:37888\n" \
"v_and_b32 v150, 0xff800000, v150\n" \
"v_mfma_f32_16x16x32_bf16 v[20:23], a[52:55], a[28:31], v[20:23]\n" \
"s_waitcnt lgkmcnt(3)\n" \
"v_subrev_u32 v155, 3, v238\n" \
"v_sub_u32 v144, 10, v238\n" \
"v_mfma_f32_16x16x32_bf16 v[28:31], a[48:51], a[192:195], v[28:31]\n" \
"v_or_b32 v155, v155, v144\n" \
"v_ashrrev_i32 v155, 31, v155\n" \
"v_mfma_f32_16x16x32_bf16 v[28:31], a[52:55], a[196:199], v[28:31]\n" \
"v_and_b32 v155, 0xff800000, v155\n" \
"v_sub_u32 v159, -6, v238\n" \
"v_mfma_f32_16x16x32_bf16 v[24:27], a[40:43], a[192:195], v[24:27]\n" \
"v_ashrrev_i32 v159, 31, v159\n" \
"v_and_b32 v159, 0xff800000, v159\n" \
"v_mfma_f32_16x16x32_bf16 v[24:27], a[44:47], a[196:199], v[24:27]\n" \
"v_add_u32 v151, 3, v146\n" \
"v_or3_b32 v151, v151, v145, s92\n" \
"v_mfma_f32_16x16x32_bf16 v[64:67], a[32:35], a[192:195], v[64:67]\n" \
"v_ashrrev_i32 v151, 31, v151\n" \
"v_and_b32 v151, 0xff800000, v151\n" \
"v_mfma_f32_16x16x32_bf16 v[64:67], a[36:39], a[196:199], v[64:67]\n" \
"s_xor_b32 s82, s48, s49\n" \
"s_cmp_lt_u32 s48, s50\n" \
"s_cselect_b32 s82, s82, s48\n" \
"s_waitcnt vmcnt(8) lgkmcnt(0)\n" \
"s_barrier\n" \
"v_mfma_f32_16x16x32_bf16 v[72:75], a[0:3], a[208:211], v[72:75]\n" \
"ds_read_b64_tr_b16 a[32:33], v194 offset:1024\n" \
"ds_read_b64_tr_b16 a[34:35], v194 offset:4096\n" \
"v_mfma_f32_16x16x32_bf16 v[72:75], a[4:7], a[212:215], v[72:75]\n" \
"s_add_u32 s81, s82, s46\n" \
"ds_read_b64_tr_b16 a[36:37], v194 offset:13312\n" \
"ds_read_b64_tr_b16 a[38:39], v194 offset:16384\n" \
"v_mfma_f32_16x16x32_bf16 v[80:83], a[8:11], a[208:211], v[80:83]\n" \
"s_add_u32 m0, s86, s79\n" \
"ds_read_b128 a[0:3], v207 offset:45056\n" \
"v_mov_b32 v186, 0xffff0000\n" \
"v_mfma_f32_16x16x32_bf16 v[80:83], a[12:15], a[212:215], v[80:83]\n" \
"buffer_load_dwordx4 v234, s[56:59], s81 offen offset:0 sc1 lds\n" \
"v_mfma_f32_16x16x32_bf16 v[84:87], a[8:11], a[216:219], v[84:87]\n" \
"ds_read_b128 a[4:7], v208 offset:45056\n" \
"v_subrev_u32 v144, 0, v238\n" \
"v_mfma_f32_16x16x32_bf16 v[84:87], a[12:15], a[220:223], v[84:87]\n" \
"s_lshl_b32 s82, s82, 5\n" \
"ds_read_b64_tr_b16 a[40:41], v194 offset:0\n" \
"v_subrev_u32 v184, 1, v238\n" \
"v_mfma_f32_16x16x32_bf16 v[44:47], a[200:203], a[8:11], v[44:47]\n" \
"s_add_u32 s82, s82, s47\n" \
"ds_read_b64_tr_b16 a[42:43], v194 offset:3072\n" \
"v_lshrrev_b32 v144, 16, v144\n" \
"v_mfma_f32_16x16x32_bf16 v[44:47], a[204:207], a[12:15], v[44:47]\n" \
"buffer_load_dwordx4 v234, s[56:59], s81 offen offset:1024 sc1 lds\n" \
"v_mfma_f32_16x16x32_bf16 v[52:55], a[200:203], a[16:19], v[52:55]\n" \
"ds_read_b128 a[8:11], v205 offset:45056\n" \
"v_and_or_b32 v184, v184, v186, v144\n" \
"v_mfma_f32_16x16x32_bf16 v[52:55], a[204:207], a[20:23], v[52:55]\n" \
"s_add_u32 s48, s48, 0x1000\n" \
"ds_read_b128 a[12:15], v206 offset:45056\n" \
"v_subrev_u32 v144, 2, v238\n" \
"v_mfma_f32_16x16x32_bf16 v[92:95], a[16:19], a[216:219], v[92:95]\n" \
"s_sub_u32 s86, s86, 32768\n" \
"s_cselect_b32 s86, 131072, s86\n" \
"ds_read_b64_tr_b16 a[44:45], v194 offset:12288\n" \
"v_mfma_f32_16x16x32_bf16 v[92:95], a[20:23], a[220:223], v[92:95]\n" \
"buffer_load_dwordx4 v234, s[56:59], s81 offen offset:2048 sc1 lds\n" \
"v_mfma_f32_16x16x32_bf16 v[88:91], a[16:19], a[208:211], v[88:91]\n" \
"ds_read_b64_tr_b16 a[46:47], v194 offset:15360\n" \
"v_subrev_u32 v185, 3, v238\n" \
"v_mfma_f32_16x16x32_bf16 v[88:91], a[20:23], a[212:215], v[88:91]\n" \
"ds_read_b64_tr_b16 a[48:49], v195 offset:0\n" \
"v_lshrrev_b32 v144, 16, v144\n" \
"v_mfma_f32_16x16x32_bf16 v[96:99], a[24:27], a[208:211], v[96:99]\n" \
"ds_read_b128 a[16:19], v205 offset:49152\n" \
"v_and_or_b32 v185, v185, v186, v144\n" \
"v_mfma_f32_16x16x32_bf16 v[96:99], a[28:31], a[212:215], v[96:99]\n" \
"buffer_load_dwordx4 v234, s[56:59], s81 offen offset:3072 sc1 lds\n" \
"v_mfma_f32_16x16x32_bf16 v[100:103], a[24:27], a[216:219], v[100:103]\n" \
"ds_read_b128 a[20:23], v206 offset:49152\n" \
"v_mov_b32 v144, 0x3e8293ee\n" \
"v_mfma_f32_16x16x32_bf16 v[100:103], a[28:31], a[220:223], v[100:103]\n" \
"s_add_u32 s80, s79, 117760\n" \
"ds_read_b64_tr_b16 a[50:51], v195 offset:3072\n" \
"v_add_u32 v223, s80, v234\n" \
"v_mfma_f32_16x16x32_bf16 v[60:63], a[200:203], a[24:27], v[60:63]\n" \
"s_add_u32 m0, m0, 0x4000\n" \
"ds_read_b64_tr_b16 a[52:53], v195 offset:12288\n" \
"v_mfma_f32_16x16x32_bf16 v[60:63], a[204:207], a[28:31], v[60:63]\n" \
"buffer_load_dwordx4 v216, s[36:39], s82 offen offset:0 lds\n" \
"v_mfma_f32_16x16x32_bf16 v[68:71], a[200:203], a[192:195], v[68:71]\n" \
"s_cmp_lg_u32 s78, 0\n" \
"s_cselect_b64 s[76:77], -1, 0\n" \
"ds_read_b128 a[24:27], v205 offset:53248\n" \
"v_mfma_f32_16x16x32_bf16 v[68:71], a[204:207], a[196:199], v[68:71]\n" \
"ds_read_b128 a[28:31], v206 offset:53248\n" \
"v_mfma_f32_16x16x32_bf16 v[108:111], a[192:195], a[216:219], v[108:111]\n" \
"ds_read_b64_tr_b16 a[54:55], v195 offset:15360\n" \
"v_mfma_f32_16x16x32_bf16 v[108:111], a[196:199], a[220:223], v[108:111]\n" \
"buffer_load_dwordx4 v217, s[36:39], s82 offen offset:1024 lds\n" \
"v_mfma_f32_16x16x32_bf16 v[104:107], a[192:195], a[208:211], v[104:107]\n" \
"v_mfma_f32_16x16x32_bf16 v[104:107], a[196:199], a[212:215], v[104:107]\n" \
"s_waitcnt lgkmcnt(2)\n" \
"v_mfma_f32_16x16x32_bf16 v[32:35], a[32:35], a[0:3], v[32:35]\n" \
"ds_read_b128 a[192:195], v205 offset:57344\n" \
"v_mfma_f32_16x16x32_bf16 v[32:35], a[36:39], a[4:7], v[32:35]\n" \
"buffer_load_dwordx4 v218, s[36:39], s82 offen offset:2048 lds\n" \
"v_mfma_f32_16x16x32_bf16 v[40:43], a[32:35], a[8:11], v[40:43]\n" \
"ds_read_b128 a[196:199], v206 offset:57344\n" \
"v_mfma_f32_16x16x32_bf16 v[40:43], a[36:39], a[12:15], v[40:43]\n" \
"ds_read_b64_tr_b16 a[200:201], v195 offset:1024\n" \
"v_mfma_f32_16x16x32_bf16 v[0:3], a[40:43], a[8:11], v[0:3]\n" \
"s_waitcnt lgkmcnt(3)\n" \
"ds_read_b64_tr_b16 a[202:203], v195 offset:4096\n" \
"v_mfma_f32_16x16x32_bf16 v[0:3], a[44:47], a[12:15], v[0:3]\n" \
"buffer_load_dwordx4 v219, s[36:39], s82 offen offset:3072 lds\n" \
"v_mfma_f32_16x16x32_bf16 v[4:7], a[48:51], a[8:11], v[4:7]\n" \
"s_xor_b32 s82, s48, s49\n" \
"s_cmp_lt_u32 s48, s50\n" \
"s_cselect_b32 s82, s82, s48\n" \
"ds_read_b64_tr_b16 a[204:205], v195 offset:13312\n" \
"v_mfma_f32_16x16x32_bf16 v[4:7], a[52:55], a[12:15], v[4:7]\n" \
"s_add_u32 s81, s82, s46\n" \
"ds_read_b64_tr_b16 a[206:207], v195 offset:16384\n" \
"v_mfma_f32_16x16x32_bf16 v[12:15], a[48:51], a[16:19], v[12:15]\n" \
"s_add_u32 m0, s86, s79\n" \
"ds_read_b64_tr_b16 a[208:209], v194 offset:2048\n" \
"v_mfma_f32_16x16x32_bf16 v[12:15], a[52:55], a[20:23], v[12:15]\n" \
"buffer_load_dwordx4 v234, s[56:59], s81 offen offset:0 sc1 lds\n" \
"v_mfma_f32_16x16x32_bf16 v[8:11], a[40:43], a[16:19], v[8:11]\n" \
"ds_read_b64_tr_b16 a[210:211], v194 offset:5120\n" \
"v_mfma_f32_16x16x32_bf16 v[8:11], a[44:47], a[20:23], v[8:11]\n" \
"s_lshl_b32 s82, s82, 5\n" \
"ds_read_b64_tr_b16 a[212:213], v194 offset:14336\n" \
"v_mfma_f32_16x16x32_bf16 v[48:51], a[32:35], a[16:19], v[48:51]\n" \
"s_add_u32 s82, s82, s47\n" \
"ds_read_b64_tr_b16 a[214:215], v194 offset:17408\n" \
"v_mfma_f32_16x16x32_bf16 v[48:51], a[36:39], a[20:23], v[48:51]\n" \
"buffer_load_dwordx4 v234, s[56:59], s81 offen offset:1024 sc1 lds\n" \
"v_mfma_f32_16x16x32_bf16 v[56:59], a[32:35], a[24:27], v[56:59]\n" \
"ds_read_b64_tr_b16 a[216:217], v195 offset:2048\n" \
"v_mfma_f32_16x16x32_bf16 v[56:59], a[36:39], a[28:31], v[56:59]\n" \
"s_add_u32 s48, s48, 0x1000\n" \
"ds_read_b64_tr_b16 a[218:219], v195 offset:5120\n" \
"v_mfma_f32_16x16x32_bf16 v[16:19], a[40:43], a[24:27], v[16:19]\n" \
"s_sub_u32 s86, s86, 32768\n" \
"s_cselect_b32 s86, 131072, s86\n" \
"ds_read_b64_tr_b16 a[220:221], v195 offset:14336\n" \
"v_mfma_f32_16x16x32_bf16 v[16:19], a[44:47], a[28:31], v[16:19]\n" \
"buffer_load_dwordx4 v234, s[56:59], s81 offen offset:2048 sc1 lds\n" \
"v_mfma_f32_16x16x32_bf16 v[20:23], a[48:51], a[24:27], v[20:23]\n" \
"ds_read_b64_tr_b16 a[222:223], v195 offset:17408\n" \
"v_mfma_f32_16x16x32_bf16 v[20:23], a[52:55], a[28:31], v[20:23]\n" \
"s_waitcnt lgkmcnt(3)\n" \
"v_mfma_f32_16x16x32_bf16 v[28:31], a[48:51], a[192:195], v[28:31]\n" \
"v_mfma_f32_16x16x32_bf16 v[28:31], a[52:55], a[196:199], v[28:31]\n" \
"buffer_load_dwordx4 v234, s[56:59], s81 offen offset:3072 sc1 lds\n" \
"v_mfma_f32_16x16x32_bf16 v[24:27], a[40:43], a[192:195], v[24:27]\n" \
"v_mfma_f32_16x16x32_bf16 v[24:27], a[44:47], a[196:199], v[24:27]\n" \
"v_mfma_f32_16x16x32_bf16 v[64:67], a[32:35], a[192:195], v[64:67]\n" \
"s_add_u32 m0, m0, 0x4000\n" \
"v_mfma_f32_16x16x32_bf16 v[64:67], a[36:39], a[196:199], v[64:67]\n" \
"buffer_load_dwordx4 v216, s[36:39], s82 offen offset:0 lds\n" \
"s_waitcnt vmcnt(13) lgkmcnt(0)\n" \
"s_barrier\n" \
"v_mfma_f32_16x16x32_bf16 v[72:75], a[0:3], a[208:211], v[72:75]\n" \
"ds_read_b64_tr_b16 a[96:97], v214 offset:16384\n" \
"v_mfma_f32_16x16x32_bf16 v[72:75], a[4:7], a[212:215], v[72:75]\n" \
"ds_read_b64_tr_b16 a[98:99], v214 offset:18432\n" \
"v_mfma_f32_16x16x32_bf16 v[44:47], a[200:203], a[8:11], v[44:47]\n" \
"ds_read_b64_tr_b16 a[100:101], v214 offset:24576\n" \
"v_mfma_f32_16x16x32_bf16 v[44:47], a[204:207], a[12:15], v[44:47]\n" \
"buffer_load_dwordx4 v217, s[36:39], s82 offen offset:1024 lds\n" \
"v_mfma_f32_16x16x32_bf16 v[80:83], a[8:11], a[208:211], v[80:83]\n" \
"ds_read_b64_tr_b16 a[102:103], v214 offset:26624\n" \
"v_cvt_pk_bf16_f32 v112, v0, v1\n" \
"v_mfma_f32_16x16x32_bf16 v[80:83], a[12:15], a[212:215], v[80:83]\n" \
"ds_read_b128 a[64:67], v213 offset:0\n" \
"v_cvt_pk_bf16_f32 v113, v2, v3\n" \
"v_mfma_f32_16x16x32_bf16 v[84:87], a[8:11], a[216:219], v[84:87]\n" \
"ds_read_b128 a[68:71], v213 offset:2048\n" \
"v_cvt_pk_bf16_f32 v114, v4, v5\n" \
"v_mfma_f32_16x16x32_bf16 v[84:87], a[12:15], a[220:223], v[84:87]\n" \
"buffer_load_dwordx4 v218, s[36:39], s82 offen offset:2048 lds\n" \
"v_mfma_f32_16x16x32_bf16 v[52:55], a[200:203], a[16:19], v[52:55]\n" \
"ds_read_b64_tr_b16 a[104:105], v215 offset:16384\n" \
"v_cvt_pk_bf16_f32 v115, v6, v7\n" \
"v_mfma_f32_16x16x32_bf16 v[52:55], a[204:207], a[20:23], v[52:55]\n" \
"ds_read_b64_tr_b16 a[106:107], v215 offset:18432\n" \
"v_cvt_pk_bf16_f32 v116, v8, v9\n" \
"v_mfma_f32_16x16x32_bf16 v[88:91], a[16:19], a[208:211], v[88:91]\n" \
"ds_read_b64_tr_b16 a[108:109], v215 offset:24576\n" \
"v_cvt_pk_bf16_f32 v117, v10, v11\n" \
"v_mfma_f32_16x16x32_bf16 v[88:91], a[20:23], a[212:215], v[88:91]\n" \
"buffer_load_dwordx4 v219, s[36:39], s82 offen offset:3072 lds\n" \
"v_mfma_f32_16x16x32_bf16 v[92:95], a[16:19], a[216:219], v[92:95]\n" \
"s_xor_b32 s82, s48, s49\n" \
"s_cmp_lt_u32 s48, s50\n" \
"s_cselect_b32 s82, s82, s48\n" \
"ds_read_b64_tr_b16 a[110:111], v215 offset:26624\n" \
"v_mfma_f32_16x16x32_bf16 v[92:95], a[20:23], a[220:223], v[92:95]\n" \
"s_add_u32 s81, s82, s46\n" \
"ds_read_b128 a[72:75], v213 offset:256\n" \
"v_cvt_pk_bf16_f32 v118, v12, v13\n" \
"v_mfma_f32_16x16x32_bf16 v[60:63], a[200:203], a[24:27], v[60:63]\n" \
"s_add_u32 m0, s86, s79\n" \
"ds_read_b128 a[76:79], v213 offset:2304\n" \
"v_cvt_pk_bf16_f32 v119, v14, v15\n" \
"v_mfma_f32_16x16x32_bf16 v[60:63], a[204:207], a[28:31], v[60:63]\n" \
"buffer_load_dwordx4 v234, s[56:59], s81 offen offset:0 sc1 lds\n" \
"v_mfma_f32_16x16x32_bf16 v[96:99], a[24:27], a[208:211], v[96:99]\n" \
"ds_read_b128 a[80:83], v213 offset:4096\n" \
"v_cvt_pk_bf16_f32 v120, v16, v17\n" \
"v_mfma_f32_16x16x32_bf16 v[96:99], a[28:31], a[212:215], v[96:99]\n" \
"s_lshl_b32 s82, s82, 5\n" \
"ds_read_b128 a[84:87], v213 offset:6144\n" \
"v_cvt_pk_bf16_f32 v121, v18, v19\n" \
"v_mfma_f32_16x16x32_bf16 v[100:103], a[24:27], a[216:219], v[100:103]\n" \
"s_add_u32 s82, s82, s47\n" \
"v_cvt_pk_bf16_f32 v122, v20, v21\n" \
"v_cvt_pk_bf16_f32 v123, v22, v23\n" \
"v_mfma_f32_16x16x32_bf16 v[100:103], a[28:31], a[220:223], v[100:103]\n" \
"buffer_load_dwordx4 v234, s[56:59], s81 offen offset:1024 sc1 lds\n" \
"v_mfma_f32_16x16x32_bf16 v[68:71], a[200:203], a[192:195], v[68:71]\n" \
"v_cvt_pk_bf16_f32 v124, v24, v25\n" \
"v_cvt_pk_bf16_f32 v125, v26, v27\n" \
"v_mfma_f32_16x16x32_bf16 v[68:71], a[204:207], a[196:199], v[68:71]\n" \
"s_add_u32 s48, s48, 0x1000\n" \
"v_cvt_pk_bf16_f32 v126, v28, v29\n" \
"v_cvt_pk_bf16_f32 v127, v30, v31\n" \
"v_mfma_f32_16x16x32_bf16 v[104:107], a[192:195], a[208:211], v[104:107]\n" \
"s_sub_u32 s86, s86, 32768\n" \
"s_cselect_b32 s86, 131072, s86\n" \
"v_cvt_pk_bf16_f32 v128, v32, v33\n" \
"v_mfma_f32_16x16x32_bf16 v[104:107], a[196:199], a[212:215], v[104:107]\n" \
"buffer_load_dwordx4 v234, s[56:59], s81 offen offset:2048 sc1 lds\n" \
"v_mfma_f32_16x16x32_bf16 v[108:111], a[192:195], a[216:219], v[108:111]\n" \
"v_cvt_pk_bf16_f32 v129, v34, v35\n" \
"v_xor_b32 v145, 0x2000, v223\n" \
"v_mfma_f32_16x16x32_bf16 v[108:111], a[196:199], a[220:223], v[108:111]\n" \
"s_waitcnt lgkmcnt(0)\n" \
"v_cvt_pk_bf16_f32 v192, v72, v73\n" \
"v_cvt_pk_bf16_f32 v193, v74, v75\n" \
"ds_write_b64 v145, v[128:129]\n" \
"s_nop 0\n" \
"ds_write_b64 v145, v[192:193] offset:8\n" \
"v_cvt_pk_bf16_f32 v132, v40, v41\n" \
"v_cvt_pk_bf16_f32 v133, v42, v43\n" \
"v_cvt_pk_bf16_f32 v134, v44, v45\n" \
"v_cvt_pk_bf16_f32 v135, v46, v47\n" \
"v_cvt_pk_bf16_f32 v136, v48, v49\n" \
"v_cvt_pk_bf16_f32 v137, v50, v51\n" \
"v_cvt_pk_bf16_f32 v138, v52, v53\n" \
"v_cvt_pk_bf16_f32 v139, v54, v55\n" \
"v_cvt_pk_bf16_f32 v140, v56, v57\n" \
"v_cvt_pk_bf16_f32 v141, v58, v59\n" \
"v_cvt_pk_bf16_f32 v142, v60, v61\n" \
"v_cvt_pk_bf16_f32 v143, v62, v63\n" \
"v_mfma_f32_16x16x32_bf16 v[0:3], a[96:99], a[64:67], 0\n" \
"ds_read_b128 a[88:91], v213 offset:4352\n" \
"v_mfma_f32_16x16x32_bf16 v[0:3], a[100:103], a[68:71], v[0:3]\n" \
"ds_read_b128 a[92:95], v213 offset:6400\n" \
"v_mfma_f32_16x16x32_bf16 v[4:7], a[104:107], a[64:67], 0\n" \
"ds_read_b64_tr_b16 a[112:113], v214 offset:16896\n" \
"v_mfma_f32_16x16x32_bf16 v[4:7], a[108:111], a[68:71], v[4:7]\n" \
"ds_read_b64_tr_b16 a[114:115], v214 offset:18944\n" \
"v_mfma_f32_16x16x32_bf16 v[20:23], a[104:107], a[72:75], 0\n" \
"buffer_load_dwordx4 v234, s[56:59], s81 offen offset:3072 sc1 lds\n" \
"v_mfma_f32_16x16x32_bf16 v[20:23], a[108:111], a[76:79], v[20:23]\n" \
"ds_read_b64_tr_b16 a[116:117], v214 offset:25088\n" \
"v_cvt_pk_bf16_f32 v64, v64, v65\n" \
"v_cvt_pk_bf16_f32 v65, v66, v67\n" \
"v_cvt_pk_bf16_f32 v66, v68, v69\n" \
"v_mfma_f32_16x16x32_bf16 v[16:19], a[96:99], a[72:75], 0\n" \
"ds_read_b64_tr_b16 a[118:119], v214 offset:27136\n" \
"v_cvt_pk_bf16_f32 v67, v70, v71\n" \
"v_mfma_f32_16x16x32_bf16 v[16:19], a[100:103], a[76:79], v[16:19]\n" \
"s_add_u32 m0, m0, 0x4000\n" \
"ds_read_b64_tr_b16 a[120:121], v215 offset:16896\n" \
"v_cvt_pk_bf16_f32 v194, v80, v81\n" \
"v_cvt_pk_bf16_f32 v195, v82, v83\n" \
"v_cvt_pk_bf16_f32 v74, v84, v85\n" \
"v_cvt_pk_bf16_f32 v75, v86, v87\n" \
"v_mfma_f32_16x16x32_bf16 v[32:35], a[96:99], a[80:83], 0\n" \
"buffer_load_dwordx4 v216, s[36:39], s82 offen offset:0 lds\n" \
"v_mfma_f32_16x16x32_bf16 v[32:35], a[100:103], a[84:87], v[32:35]\n" \
"s_sub_u32 s85, s85, 32768\n" \
"s_cselect_b32 s85, 131072, s85\n" \
"ds_read_b64_tr_b16 a[122:123], v215 offset:18944\n" \
"v_cvt_pk_bf16_f32 v196, v88, v89\n" \
"v_cvt_pk_bf16_f32 v197, v90, v91\n" \
"v_mfma_f32_16x16x32_bf16 v[176:179], v[64:67], v[124:127], v[152:155]\n" \
"v_mfma_f32_16x16x32_bf16 v[172:175], v[140:143], v[124:127], v[156:159]\n" \
"v_mfma_f32_16x16x32_bf16 v[168:171], v[140:143], v[120:123], v[152:155]\n" \
"v_mfma_f32_16x16x32_bf16 v[164:167], v[136:139], v[120:123], v[156:159]\n" \
"v_mfma_f32_16x16x32_bf16 v[160:163], v[136:139], v[116:119], v[152:155]\n" \
"v_mfma_f32_16x16x32_bf16 v[156:159], v[132:135], v[116:119], v[156:159]\n" \
"v_mfma_f32_16x16x32_bf16 v[152:155], v[132:135], v[112:115], v[152:155]\n" \
"v_mfma_f32_16x16x32_bf16 v[36:39], a[104:107], a[80:83], 0\n" \
"ds_read_b64_tr_b16 a[124:125], v215 offset:25088\n" \
"v_add_u32 v213, s85, v211\n" \
"v_mfma_f32_16x16x32_bf16 v[36:39], a[108:111], a[84:87], v[36:39]\n" \
"s_waitcnt lgkmcnt(3)\n" \
"ds_read_b64_tr_b16 a[126:127], v215 offset:27136\n" \
"v_mfma_f32_16x16x32_bf16 v[52:55], a[104:107], a[88:91], 0\n" \
"buffer_load_dwordx4 v217, s[36:39], s82 offen offset:1024 lds\n" \
"v_mfma_f32_16x16x32_bf16 v[52:55], a[108:111], a[92:95], v[52:55]\n" \
"v_add_u32 v214, s85, v212\n" \
"v_xor_b32 v215, 32, v214\n" \
"v_mfma_f32_16x16x32_bf16 v[48:51], a[96:99], a[88:91], 0\n" \
"buffer_load_dwordx4 v218, s[36:39], s82 offen offset:2048 lds\n" \
"v_mfma_f32_16x16x32_bf16 v[48:51], a[100:103], a[92:95], v[48:51]\n" \
"s_waitcnt vmcnt(15) lgkmcnt(0)\n" \
"s_barrier\n" \
"v_mfma_f32_16x16x32_bf16 v[8:11], a[112:115], a[64:67], 0\n" \
"ds_read_b64 v[130:131], v223\n" \
"ds_read_b64_tr_b16 a[96:97], v214 offset:16384\n" \
"ds_read_b64_tr_b16 a[98:99], v214 offset:18432\n" \
"v_mfma_f32_16x16x32_bf16 v[8:11], a[116:119], a[68:71], v[8:11]\n" \
"ds_read_b64 v[72:73], v223 offset:8\n" \
"ds_read_b64_tr_b16 a[100:101], v214 offset:24576\n" \
"ds_read_b64_tr_b16 a[102:103], v214 offset:26624\n" \
"v_mfma_f32_16x16x32_bf16 v[12:15], a[120:123], a[64:67], 0\n" \
"buffer_load_dwordx4 v219, s[36:39], s82 offen offset:3072 lds\n" \
"v_mfma_f32_16x16x32_bf16 v[12:15], a[124:127], a[68:71], v[12:15]\n" \
"s_xor_b32 s82, s48, s49\n" \
"s_cmp_lt_u32 s48, s50\n" \
"s_cselect_b32 s82, s82, s48\n" \
"ds_read_b128 a[64:67], v213 offset:0\n" \
"v_mfma_f32_16x16x32_bf16 v[28:31], a[120:123], a[72:75], 0\n" \
"s_add_u32 s81, s82, s46\n" \
"ds_read_b128 a[68:71], v213 offset:2048\n" \
"v_mfma_f32_16x16x32_bf16 v[28:31], a[124:127], a[76:79], v[28:31]\n" \
"s_add_u32 m0, s86, s79\n" \
"ds_read_b64_tr_b16 a[104:105], v215 offset:16384\n" \
"ds_read_b64_tr_b16 a[106:107], v215 offset:18432\n" \
"v_mfma_f32_16x16x32_bf16 v[24:27], a[112:115], a[72:75], 0\n" \
"buffer_load_dwordx4 v234, s[56:59], s81 offen offset:0 sc1 lds\n" \
"v_mfma_f32_16x16x32_bf16 v[24:27], a[116:119], a[76:79], v[24:27]\n" \
"s_waitcnt lgkmcnt(4)\n" \
"ds_read_b128 a[72:75], v213 offset:256\n" \
"v_mfma_f32_16x16x32_bf16 v[40:43], a[112:115], a[80:83], 0\n" \
"s_lshl_b32 s82, s82, 5\n" \
"ds_read_b128 a[76:79], v213 offset:2304\n" \
"v_mfma_f32_16x16x32_bf16 v[148:151], v[128:131], v[112:115], v[148:151]\n" \
"v_max_f32 v172, v172, v176\n" \
"v_max_f32 v173, v173, v177\n" \
"v_max_f32 v174, v174, v178\n" \
"v_max_f32 v175, v175, v179\n" \
"v_max_f32 v164, v164, v168\n" \
"v_max_f32 v165, v165, v169\n" \
"v_max_f32 v166, v166, v170\n" \
"v_max_f32 v167, v167, v171\n" \
"v_max_f32 v156, v156, v160\n" \
"v_max_f32 v157, v157, v161\n" \
"v_max_f32 v158, v158, v162\n" \
"v_max_f32 v159, v159, v163\n" \
"v_max_f32 v148, v148, v152\n" \
"v_max_f32 v149, v149, v153\n" \
"v_max_f32 v150, v150, v154\n" \
"v_max_f32 v151, v151, v155\n" \
"v_max3_f32 v183, v172, v173, v174\n" \
"v_max3_f32 v182, v164, v165, v166\n" \
"v_max3_f32 v181, v156, v157, v158\n" \
"v_mfma_f32_16x16x32_bf16 v[40:43], a[116:119], a[84:87], v[40:43]\n" \
"s_add_u32 s82, s82, s47\n" \
"ds_read_b64_tr_b16 a[108:109], v215 offset:24576\n" \
"ds_read_b64_tr_b16 a[110:111], v215 offset:26624\n" \
"v_max3_f32 v180, v148, v149, v150\n" \
"v_mfma_f32_16x16x32_bf16 v[44:47], a[120:123], a[80:83], 0\n" \
"buffer_load_dwordx4 v234, s[56:59], s81 offen offset:1024 sc1 lds\n" \
"v_mfma_f32_16x16x32_bf16 v[44:47], a[124:127], a[84:87], v[44:47]\n" \
"s_add_u32 s48, s48, 0x1000\n" \
"s_sub_u32 s86, s86, 32768\n" \
"s_cselect_b32 s86, 131072, s86\n" \
"ds_read_b128 a[80:83], v213 offset:4096\n" \
"v_max_f32 v183, v183, v175\n" \
"v_max_f32 v182, v182, v167\n" \
"v_max_f32 v181, v181, v159\n" \
"v_mfma_f32_16x16x32_bf16 v[60:63], a[120:123], a[88:91], 0\n" \
"s_waitcnt lgkmcnt(4)\n" \
"buffer_load_dwordx4 v234, s[56:59], s81 offen offset:2048 sc1 lds\n" \
"v_max_f32 v180, v180, v151\n" \
"v_mfma_f32_16x16x32_bf16 v[60:63], a[124:127], a[92:95], v[60:63]\n" \
"ds_read_b128 a[84:87], v213 offset:6144\n" \
"v_cvt_pk_bf16_f32 v76, v92, v93\n" \
"v_cvt_pk_bf16_f32 v77, v94, v95\n" \
"v_mfma_f32_16x16x32_bf16 v[56:59], a[112:115], a[88:91], 0\n" \
"buffer_load_dwordx4 v234, s[56:59], s81 offen offset:3072 sc1 lds\n" \
"v_mfma_f32_16x16x32_bf16 v[56:59], a[116:119], a[92:95], v[56:59]\n" \
"s_getpc_b64 s[44:45]\n" \
"Lop_%=:\n" \
"s_add_u32 s44, s44, Lor_%=-Lop_%=\n" \
"s_addc_u32 s45, s45, 0\n" \
"v_permlane32_swap_b32 v180, v181\n" \
"v_permlane32_swap_b32 v182, v183\n" \
"v_max_f32 v180, v180, v181\n" \
"v_mfma_f32_16x16x32_bf16 v[0:3], a[96:99], a[64:67], v[0:3]\n" \
"ds_read_b128 a[88:91], v213 offset:4352\n" \
"v_max_f32 v182, v182, v183\n" \
"v_mfma_f32_16x16x32_bf16 v[0:3], a[100:103], a[68:71], v[0:3]\n" \
"s_waitcnt lgkmcnt(2)\n" \
"ds_read_b128 a[92:95], v213 offset:6400\n" \
"v_cvt_pk_bf16_f32 v198, v96, v97\n" \
"v_cvt_pk_bf16_f32 v199, v98, v99\n" \
"v_permlane16_swap_b32 v180, v182\n" \
"v_mfma_f32_16x16x32_bf16 v[4:7], a[104:107], a[64:67], v[4:7]\n" \
"ds_read_b64_tr_b16 a[112:113], v214 offset:16896\n" \
"v_max_f32 v180, v180, v182\n" \
"v_mfma_f32_16x16x32_bf16 v[4:7], a[108:111], a[68:71], v[4:7]\n" \
"s_add_u32 m0, m0, 0x4000\n" \
"ds_read_b64_tr_b16 a[114:115], v214 offset:18944\n" \
"v_mul_f32 v180, v180, v144\n" \
"v_cvt_pk_bf16_f32 v78, v100, v101\n" \
"v_cvt_pk_bf16_f32 v79, v102, v103\n" \
"v_mov_b32 v182, v180\n" \
"v_mfma_f32_16x16x32_bf16 v[20:23], a[104:107], a[72:75], v[20:23]\n" \
"buffer_load_dwordx4 v216, s[36:39], s82 offen offset:0 lds\n" \
"v_mfma_f32_16x16x32_bf16 v[20:23], a[108:111], a[76:79], v[20:23]\n" \
"ds_read_b64_tr_b16 a[116:117], v214 offset:25088\n" \
"v_cvt_pk_bf16_f32 v200, v104, v105\n" \
"v_cvt_pk_bf16_f32 v201, v106, v107\n" \
"v_permlane16_swap_b32 v180, v182\n" \
"v_mfma_f32_16x16x32_bf16 v[16:19], a[96:99], a[72:75], v[16:19]\n" \
"ds_read_b64_tr_b16 a[118:119], v214 offset:27136\n" \
"v_mov_b32 v181, v180\n" \
"v_mfma_f32_16x16x32_bf16 v[16:19], a[100:103], a[76:79], v[16:19]\n" \
"s_waitcnt lgkmcnt(3)\n" \
"ds_read_b64_tr_b16 a[120:121], v215 offset:16896\n" \
"v_mov_b32 v183, v182\n" \
"v_cvt_pk_bf16_f32 v80, v108, v109\n" \
"v_cvt_pk_bf16_f32 v81, v110, v111\n" \
"v_permlane32_swap_b32 v180, v181\n" \
"v_mfma_f32_16x16x32_bf16 v[32:35], a[96:99], a[80:83], v[32:35]\n" \
"buffer_load_dwordx4 v217, s[36:39], s82 offen offset:1024 lds\n" \
"v_mfma_f32_16x16x32_bf16 v[32:35], a[100:103], a[84:87], v[32:35]\n" \
"s_sub_u32 s85, s85, 32768\n" \
"s_cselect_b32 s85, 131072, s85\n" \
"ds_read_b64_tr_b16 a[122:123], v215 offset:18944\n" \
"v_permlane32_swap_b32 v182, v183\n" \
"v_fma_f32 v172, v172, v144, -v183\n" \
"v_mfma_f32_16x16x32_bf16 v[36:39], a[104:107], a[80:83], v[36:39]\n" \
"ds_read_b64_tr_b16 a[124:125], v215 offset:25088\n" \
"v_add_u32 v213, s85, v211\n" \
"v_mfma_f32_16x16x32_bf16 v[36:39], a[108:111], a[84:87], v[36:39]\n" \
"ds_read_b64_tr_b16 a[126:127], v215 offset:27136\n" \
"v_fma_f32 v164, v164, v144, -v182\n" \
"v_fma_f32 v156, v156, v144, -v181\n" \
"v_fma_f32 v148, v148, v144, -v180\n" \
"v_fma_f32 v173, v173, v144, -v183\n" \
"v_mfma_f32_16x16x32_bf16 v[52:55], a[104:107], a[88:91], v[52:55]\n" \
"buffer_load_dwordx4 v218, s[36:39], s82 offen offset:2048 lds\n" \
"v_mfma_f32_16x16x32_bf16 v[52:55], a[108:111], a[92:95], v[52:55]\n" \
"v_add_u32 v214, s85, v212\n" \
"v_xor_b32 v215, 32, v214\n" \
"v_fma_f32 v165, v165, v144, -v182\n" \
"v_fma_f32 v157, v157, v144, -v181\n" \
"v_mfma_f32_16x16x32_bf16 v[48:51], a[96:99], a[88:91], v[48:51]\n" \
"buffer_load_dwordx4 v219, s[36:39], s82 offen offset:3072 lds\n" \
"v_mfma_f32_16x16x32_bf16 v[48:51], a[100:103], a[92:95], v[48:51]\n" \
"s_xor_b32 s82, s48, s49\n" \
"s_cmp_lt_u32 s48, s50\n" \
"s_cselect_b32 s82, s82, s48\n" \
"v_fma_f32 v149, v149, v144, -v180\n" \
"v_fma_f32 v174, v174, v144, -v183\n" \
"v_fma_f32 v166, v166, v144, -v182\n" \
"s_waitcnt vmcnt(16) lgkmcnt(0)\n" \
"s_barrier\n" \
"v_mfma_f32_16x16x32_bf16 v[8:11], a[112:115], a[64:67], v[8:11]\n" \
"s_add_u32 s81, s82, s46\n" \
"ds_read_b64_tr_b16 a[96:97], v214 offset:16384\n" \
"ds_read_b64_tr_b16 a[98:99], v214 offset:18432\n" \
"v_mfma_f32_16x16x32_bf16 v[8:11], a[116:119], a[68:71], v[8:11]\n" \
"s_add_u32 m0, s86, s79\n" \
"ds_read_b64_tr_b16 a[100:101], v214 offset:24576\n" \
"ds_read_b64_tr_b16 a[102:103], v214 offset:26624\n" \
"v_fma_f32 v158, v158, v144, -v181\n" \
"v_fma_f32 v150, v150, v144, -v180\n" \
"v_mfma_f32_16x16x32_bf16 v[12:15], a[120:123], a[64:67], v[12:15]\n" \
"buffer_load_dwordx4 v234, s[56:59], s81 offen offset:0 sc1 lds\n" \
"v_mfma_f32_16x16x32_bf16 v[12:15], a[124:127], a[68:71], v[12:15]\n" \
"ds_read_b128 a[64:67], v213 offset:0\n" \
"v_fma_f32 v175, v175, v144, -v183\n" \
"v_fma_f32 v167, v167, v144, -v182\n" \
"v_fma_f32 v159, v159, v144, -v181\n" \
"v_fma_f32 v151, v151, v144, -v180\n" \
"v_mfma_f32_16x16x32_bf16 v[28:31], a[120:123], a[72:75], v[28:31]\n" \
"s_lshl_b32 s82, s82, 5\n" \
"ds_read_b128 a[68:71], v213 offset:2048\n" \
"v_mfma_f32_16x16x32_bf16 v[28:31], a[124:127], a[76:79], v[28:31]\n" \
"s_add_u32 s82, s82, s47\n" \
"ds_read_b64_tr_b16 a[104:105], v215 offset:16384\n" \
"ds_read_b64_tr_b16 a[106:107], v215 offset:18432\n" \
"v_exp_f32 v172, v172\n" \
"v_mfma_f32_16x16x32_bf16 v[24:27], a[112:115], a[72:75], v[24:27]\n" \
"buffer_load_dwordx4 v234, s[56:59], s81 offen offset:1024 sc1 lds\n" \
"v_mfma_f32_16x16x32_bf16 v[24:27], a[116:119], a[76:79], v[24:27]\n" \
"ds_read_b128 a[72:75], v213 offset:256\n" \
"v_exp_f32 v164, v164\n" \
"v_exp_f32 v156, v156\n" \
"v_mfma_f32_16x16x32_bf16 v[40:43], a[112:115], a[80:83], v[40:43]\n" \
"s_add_u32 s48, s48, 0x1000\n" \
"ds_read_b128 a[76:79], v213 offset:2304\n" \
"v_mfma_f32_16x16x32_bf16 v[40:43], a[116:119], a[84:87], v[40:43]\n" \
"s_sub_u32 s86, s86, 32768\n" \
"s_cselect_b32 s86, 131072, s86\n" \
"ds_read_b64_tr_b16 a[108:109], v215 offset:24576\n" \
"ds_read_b64_tr_b16 a[110:111], v215 offset:26624\n" \
"v_exp_f32 v148, v148\n" \
"v_mfma_f32_16x16x32_bf16 v[44:47], a[120:123], a[80:83], v[44:47]\n" \
"buffer_load_dwordx4 v234, s[56:59], s81 offen offset:2048 sc1 lds\n" \
"v_mfma_f32_16x16x32_bf16 v[44:47], a[124:127], a[84:87], v[44:47]\n" \
"ds_read_b128 a[80:83], v213 offset:4096\n" \
"v_exp_f32 v173, v173\n" \
"v_exp_f32 v165, v165\n" \
"v_mfma_f32_16x16x32_bf16 v[60:63], a[120:123], a[88:91], v[60:63]\n" \
"s_waitcnt lgkmcnt(4)\n" \
"buffer_load_dwordx4 v234, s[56:59], s81 offen offset:3072 sc1 lds\n" \
"v_mfma_f32_16x16x32_bf16 v[60:63], a[124:127], a[92:95], v[60:63]\n" \
"s_add_u32 m0, m0, 0x4000\n" \
"Lpg10215_%=:\n" \
"s_cbranch_execz Lcb1_%=\n" \
"ds_read_b128 a[84:87], v213 offset:6144\n" \
"v_exp_f32 v157, v157\n" \
"v_mfma_f32_16x16x32_bf16 v[56:59], a[112:115], a[88:91], v[56:59]\n" \
"buffer_load_dwordx4 v216, s[36:39], s82 offen offset:0 lds\n" \
"v_mfma_f32_16x16x32_bf16 v[56:59], a[116:119], a[92:95], v[56:59]\n" \
"v_exp_f32 v149, v149\n" \
"v_exp_f32 v174, v174\n" \
"v_exp_f32 v166, v166\n" \
"v_mfma_f32_16x16x32_bf16 v[0:3], a[96:99], a[64:67], v[0:3]\n" \
"ds_read_b128 a[88:91], v213 offset:4352\n" \
"v_mfma_f32_16x16x32_bf16 v[0:3], a[100:103], a[68:71], v[0:3]\n" \
"s_waitcnt lgkmcnt(2)\n" \
"ds_read_b128 a[92:95], v213 offset:6400\n" \
"v_exp_f32 v158, v158\n" \
"v_mfma_f32_16x16x32_bf16 v[4:7], a[104:107], a[64:67], v[4:7]\n" \
"ds_read_b64_tr_b16 a[112:113], v214 offset:16896\n" \
"v_mfma_f32_16x16x32_bf16 v[4:7], a[108:111], a[68:71], v[4:7]\n" \
"ds_read_b64_tr_b16 a[114:115], v214 offset:18944\n" \
"v_exp_f32 v150, v150\n" \
"v_exp_f32 v175, v175\n" \
"v_mfma_f32_16x16x32_bf16 v[20:23], a[104:107], a[72:75], v[20:23]\n" \
"buffer_load_dwordx4 v217, s[36:39], s82 offen offset:1024 lds\n" \
"v_mfma_f32_16x16x32_bf16 v[20:23], a[108:111], a[76:79], v[20:23]\n" \
"ds_read_b64_tr_b16 a[116:117], v214 offset:25088\n" \
"v_exp_f32 v167, v167\n" \
"v_exp_f32 v159, v159\n" \
"v_mfma_f32_16x16x32_bf16 v[16:19], a[96:99], a[72:75], v[16:19]\n" \
"ds_read_b64_tr_b16 a[118:119], v214 offset:27136\n" \
"v_mfma_f32_16x16x32_bf16 v[16:19], a[100:103], a[76:79], v[16:19]\n" \
"s_waitcnt lgkmcnt(3)\n" \
"ds_read_b64_tr_b16 a[120:121], v215 offset:16896\n" \
"v_exp_f32 v151, v151\n" \
"v_add_f32 v191, v172, v173\n" \
"v_add_f32 v190, v164, v165\n" \
"v_mfma_f32_16x16x32_bf16 v[32:35], a[96:99], a[80:83], v[32:35]\n" \
"buffer_load_dwordx4 v218, s[36:39], s82 offen offset:2048 lds\n" \
"v_mfma_f32_16x16x32_bf16 v[32:35], a[100:103], a[84:87], v[32:35]\n" \
"s_sub_u32 s85, s85, 32768\n" \
"s_cselect_b32 s85, 131072, s85\n" \
"ds_read_b64_tr_b16 a[122:123], v215 offset:18944\n" \
"v_add_f32 v189, v156, v157\n" \
"v_add_f32 v188, v148, v149\n" \
"v_mfma_f32_16x16x32_bf16 v[36:39], a[104:107], a[80:83], v[36:39]\n" \
"ds_read_b64_tr_b16 a[124:125], v215 offset:25088\n" \
"v_add_u32 v213, s85, v211\n" \
"v_mfma_f32_16x16x32_bf16 v[36:39], a[108:111], a[84:87], v[36:39]\n" \
"ds_read_b64_tr_b16 a[126:127], v215 offset:27136\n" \
"v_add_f32 v191, v191, v174\n" \
"v_add_f32 v190, v190, v166\n" \
"v_add_f32 v189, v189, v158\n" \
"v_add_f32 v188, v188, v150\n" \
"v_mfma_f32_16x16x32_bf16 v[52:55], a[104:107], a[88:91], v[52:55]\n" \
"buffer_load_dwordx4 v219, s[36:39], s82 offen offset:3072 lds\n" \
"v_mfma_f32_16x16x32_bf16 v[52:55], a[108:111], a[92:95], v[52:55]\n" \
"s_xor_b32 s82, s48, s49\n" \
"s_cmp_lt_u32 s48, s50\n" \
"s_cselect_b32 s82, s82, s48\n" \
"s_add_u32 s81, s82, s46\n" \
"s_add_u32 m0, s86, s79\n" \
"v_add_u32 v214, s85, v212\n" \
"v_xor_b32 v215, 32, v214\n" \
"v_add_f32 v191, v191, v175\n" \
"v_add_f32 v190, v190, v167\n" \
"v_mfma_f32_16x16x32_bf16 v[48:51], a[96:99], a[88:91], v[48:51]\n" \
"buffer_load_dwordx4 v234, s[56:59], s81 offen offset:0 sc1 lds\n" \
"v_mfma_f32_16x16x32_bf16 v[48:51], a[100:103], a[92:95], v[48:51]\n" \
"v_add_f32 v189, v189, v159\n" \
"v_add_f32 v188, v188, v151\n" \
"s_waitcnt vmcnt(17) lgkmcnt(0)\n" \
"s_barrier\n" \
"v_mfma_f32_16x16x32_bf16 v[8:11], a[112:115], a[64:67], v[8:11]\n" \
"s_lshl_b32 s82, s82, 5\n" \
"ds_read_b64_tr_b16 a[96:97], v214 offset:16384\n" \
"ds_read_b64_tr_b16 a[98:99], v214 offset:18432\n" \
"v_mfma_f32_16x16x32_bf16 v[8:11], a[116:119], a[68:71], v[8:11]\n" \
"s_add_u32 s82, s82, s47\n" \
"ds_read_b64_tr_b16 a[100:101], v214 offset:24576\n" \
"ds_read_b64_tr_b16 a[102:103], v214 offset:26624\n" \
"v_permlane32_swap_b32 v188, v189\n" \
"v_permlane32_swap_b32 v190, v191\n" \
"v_add_f32 v188, v188, v189\n" \
"v_mfma_f32_16x16x32_bf16 v[12:15], a[120:123], a[64:67], v[12:15]\n" \
"buffer_load_dwordx4 v234, s[56:59], s81 offen offset:1024 sc1 lds\n" \
"v_mfma_f32_16x16x32_bf16 v[12:15], a[124:127], a[68:71], v[12:15]\n" \
"ds_read_b128 a[64:67], v213 offset:0\n" \
"v_add_f32 v190, v190, v191\n" \
"v_mfma_f32_16x16x32_bf16 v[28:31], a[120:123], a[72:75], v[28:31]\n" \
"s_add_u32 s48, s48, 0x1000\n" \
"ds_read_b128 a[68:71], v213 offset:2048\n" \
"v_mfma_f32_16x16x32_bf16 v[28:31], a[124:127], a[76:79], v[28:31]\n" \
"s_sub_u32 s86, s86, 32768\n" \
"s_cselect_b32 s86, 131072, s86\n" \
"ds_read_b64_tr_b16 a[104:105], v215 offset:16384\n" \
"ds_read_b64_tr_b16 a[106:107], v215 offset:18432\n" \
"v_permlane16_swap_b32 v188, v190\n" \
"v_add_f32 v188, v188, v190\n" \
"v_rcp_f32 v188, v188\n" \
"v_mfma_f32_16x16x32_bf16 v[24:27], a[112:115], a[72:75], v[24:27]\n" \
"buffer_load_dwordx4 v234, s[56:59], s81 offen offset:2048 sc1 lds\n" \
"v_mfma_f32_16x16x32_bf16 v[24:27], a[116:119], a[76:79], v[24:27]\n" \
"ds_read_b128 a[72:75], v213 offset:256\n" \
"v_mov_b32 v190, v188\n" \
"v_mfma_f32_16x16x32_bf16 v[40:43], a[112:115], a[80:83], v[40:43]\n" \
"ds_read_b128 a[76:79], v213 offset:2304\n" \
"v_mfma_f32_16x16x32_bf16 v[40:43], a[116:119], a[84:87], v[40:43]\n" \
"ds_read_b64_tr_b16 a[108:109], v215 offset:24576\n" \
"ds_read_b64_tr_b16 a[110:111], v215 offset:26624\n" \
"v_permlane16_swap_b32 v188, v190\n" \
"v_mov_b32 v189, v188\n" \
"v_mov_b32 v191, v190\n" \
"v_mfma_f32_16x16x32_bf16 v[44:47], a[120:123], a[80:83], v[44:47]\n" \
"buffer_load_dwordx4 v234, s[56:59], s81 offen offset:3072 sc1 lds\n" \
"v_mfma_f32_16x16x32_bf16 v[44:47], a[124:127], a[84:87], v[44:47]\n" \
"s_add_u32 m0, m0, 0x4000\n" \
"ds_read_b128 a[80:83], v213 offset:4096\n" \
"v_permlane32_swap_b32 v188, v189\n" \
"v_permlane32_swap_b32 v190, v191\n" \
"v_mul_f32 v172, v172, v191\n" \
"v_mul_f32 v164, v164, v190\n" \
"v_mul_f32 v156, v156, v189\n" \
"v_mul_f32 v148, v148, v188\n" \
"v_mul_f32 v173, v173, v191\n" \
"v_mfma_f32_16x16x32_bf16 v[60:63], a[120:123], a[88:91], v[60:63]\n" \
"s_waitcnt lgkmcnt(4)\n" \
"buffer_load_dwordx4 v216, s[36:39], s82 offen offset:0 lds\n" \
"v_mul_f32 v165, v165, v190\n" \
"v_mfma_f32_16x16x32_bf16 v[60:63], a[124:127], a[92:95], v[60:63]\n" \
"ds_read_b128 a[84:87], v213 offset:6144\n" \
"v_mul_f32 v157, v157, v189\n" \
"v_mul_f32 v149, v149, v188\n" \
"v_mfma_f32_16x16x32_bf16 v[56:59], a[112:115], a[88:91], v[56:59]\n" \
"buffer_load_dwordx4 v217, s[36:39], s82 offen offset:1024 lds\n" \
"v_mfma_f32_16x16x32_bf16 v[56:59], a[116:119], a[92:95], v[56:59]\n" \
"v_mul_f32 v174, v174, v191\n" \
"v_mul_f32 v166, v166, v190\n" \
"v_mul_f32 v158, v158, v189\n" \
"v_mul_f32 v150, v150, v188\n" \
"v_mul_f32 v175, v175, v191\n" \
"v_mfma_f32_16x16x32_bf16 v[0:3], a[96:99], a[64:67], v[0:3]\n" \
"ds_read_b128 a[88:91], v213 offset:4352\n" \
"v_mul_f32 v167, v167, v190\n" \
"v_mfma_f32_16x16x32_bf16 v[0:3], a[100:103], a[68:71], v[0:3]\n" \
"s_waitcnt lgkmcnt(2)\n" \
"ds_read_b128 a[92:95], v213 offset:6400\n" \
"v_mul_f32 v159, v159, v189\n" \
"v_mul_f32 v151, v151, v188\n" \
"v_cvt_pk_bf16_f32 v178, v172, v173\n" \
"v_mfma_f32_16x16x32_bf16 v[4:7], a[104:107], a[64:67], v[4:7]\n" \
"ds_read_b64_tr_b16 a[112:113], v214 offset:16896\n" \
"v_cvt_pk_bf16_f32 v179, v174, v175\n" \
"v_mfma_f32_16x16x32_bf16 v[4:7], a[108:111], a[68:71], v[4:7]\n" \
"ds_read_b64_tr_b16 a[114:115], v214 offset:18944\n" \
"v_cvt_pk_bf16_f32 v170, v164, v165\n" \
"v_cvt_pk_bf16_f32 v171, v166, v167\n" \
"v_cvt_pk_bf16_f32 v162, v156, v157\n" \
"v_cvt_pk_bf16_f32 v163, v158, v159\n" \
"v_mfma_f32_16x16x32_bf16 v[20:23], a[104:107], a[72:75], v[20:23]\n" \
"buffer_load_dwordx4 v218, s[36:39], s82 offen offset:2048 lds\n" \
"v_mfma_f32_16x16x32_bf16 v[20:23], a[108:111], a[76:79], v[20:23]\n" \
"ds_read_b64_tr_b16 a[116:117], v214 offset:25088\n" \
"v_cvt_pk_bf16_f32 v154, v148, v149\n" \
"v_cvt_pk_bf16_f32 v155, v150, v151\n" \
"v_and_b32 v176, v178, v184\n" \
"v_mfma_f32_16x16x32_bf16 v[16:19], a[96:99], a[72:75], v[16:19]\n" \
"ds_read_b64_tr_b16 a[118:119], v214 offset:27136\n" \
"v_and_b32 v177, v179, v185\n" \
"v_mfma_f32_16x16x32_bf16 v[16:19], a[100:103], a[76:79], v[16:19]\n" \
"s_waitcnt lgkmcnt(3)\n" \
"ds_read_b64_tr_b16 a[120:121], v215 offset:16896\n" \
"v_xor_b32 v178, v178, v176\n" \
"v_xor_b32 v179, v179, v177\n" \
"v_and_b32 v168, v170, v184\n" \
"v_and_b32 v169, v171, v185\n" \
"v_mfma_f32_16x16x32_bf16 v[32:35], a[96:99], a[80:83], v[32:35]\n" \
"buffer_load_dwordx4 v219, s[36:39], s82 offen offset:3072 lds\n" \
"v_mfma_f32_16x16x32_bf16 v[32:35], a[100:103], a[84:87], v[32:35]\n" \
"s_sub_u32 s85, s85, 32768\n" \
"s_cselect_b32 s85, 131072, s85\n" \
"s_xor_b32 s82, s48, s49\n" \
"s_cmp_lt_u32 s48, s50\n" \
"s_cselect_b32 s82, s82, s48\n" \
"ds_read_b64_tr_b16 a[122:123], v215 offset:18944\n" \
"v_xor_b32 v170, v170, v168\n" \
"v_xor_b32 v171, v171, v169\n" \
"v_mfma_f32_16x16x32_bf16 v[36:39], a[104:107], a[80:83], v[36:39]\n" \
"s_add_u32 s81, s82, s46\n" \
"ds_read_b64_tr_b16 a[124:125], v215 offset:25088\n" \
"v_add_u32 v213, s85, v211\n" \
"v_mfma_f32_16x16x32_bf16 v[36:39], a[108:111], a[84:87], v[36:39]\n" \
"s_add_u32 m0, s86, s79\n" \
"ds_read_b64_tr_b16 a[126:127], v215 offset:27136\n" \
"v_and_b32 v160, v162, v184\n" \
"v_and_b32 v161, v163, v185\n" \
"v_xor_b32 v162, v162, v160\n" \
"v_xor_b32 v163, v163, v161\n" \
"v_mfma_f32_16x16x32_bf16 v[52:55], a[104:107], a[88:91], v[52:55]\n" \
"buffer_load_dwordx4 v234, s[56:59], s81 offen offset:0 sc1 lds\n" \
"v_mfma_f32_16x16x32_bf16 v[52:55], a[108:111], a[92:95], v[52:55]\n" \
"s_lshl_b32 s82, s82, 5\n" \
"s_add_u32 s82, s82, s47\n" \
"v_add_u32 v214, s85, v212\n" \
"v_xor_b32 v215, 32, v214\n" \
"v_and_b32 v152, v154, v184\n" \
"v_and_b32 v153, v155, v185\n" \
"v_mfma_f32_16x16x32_bf16 v[48:51], a[96:99], a[88:91], v[48:51]\n" \
"buffer_load_dwordx4 v234, s[56:59], s81 offen offset:1024 sc1 lds\n" \
"v_mfma_f32_16x16x32_bf16 v[48:51], a[100:103], a[92:95], v[48:51]\n" \
"v_xor_b32 v154, v154, v152\n" \
"v_xor_b32 v155, v155, v153\n" \
"s_waitcnt vmcnt(18) lgkmcnt(0)\n" \
"s_barrier\n" \
"v_mfma_f32_16x16x32_bf16 v[8:11], a[112:115], a[64:67], v[8:11]\n" \
"s_add_u32 s48, s48, 0x1000\n" \
"ds_read_b64_tr_b16 a[96:97], v214 offset:16384\n" \
"ds_read_b64_tr_b16 a[98:99], v214 offset:18432\n" \
"v_mfma_f32_16x16x32_bf16 v[136:139], v[198:201], v[176:179], 0\n" \
"v_mfma_f32_16x16x32_bf16 v[140:143], v[78:81], v[176:179], 0\n" \
"v_mfma_f32_16x16x32_bf16 v[128:131], v[196:199], v[168:171], 0\n" \
"v_mfma_f32_16x16x32_bf16 v[132:135], v[76:79], v[168:171], 0\n" \
"v_mfma_f32_16x16x32_bf16 v[120:123], v[194:197], v[160:163], 0\n" \
"v_mfma_f32_16x16x32_bf16 v[124:127], v[74:77], v[160:163], 0\n" \
"v_mfma_f32_16x16x32_bf16 v[112:115], v[192:195], v[152:155], 0\n" \
"v_mfma_f32_16x16x32_bf16 v[116:119], v[72:75], v[152:155], 0\n" \
"v_mfma_f32_16x16x32_bf16 v[8:11], a[116:119], a[68:71], v[8:11]\n" \
"s_sub_u32 s86, s86, 32768\n" \
"s_cselect_b32 s86, 131072, s86\n" \
"ds_read_b64_tr_b16 a[100:101], v214 offset:24576\n" \
"ds_read_b64_tr_b16 a[102:103], v214 offset:26624\n" \
"v_mfma_f32_16x16x32_bf16 v[12:15], a[120:123], a[64:67], v[12:15]\n" \
"buffer_load_dwordx4 v234, s[56:59], s81 offen offset:2048 sc1 lds\n" \
"v_mfma_f32_16x16x32_bf16 v[12:15], a[124:127], a[68:71], v[12:15]\n" \
"ds_read_b128 a[64:67], v213 offset:0\n" \
"v_cvt_pk_bf16_f32 v136, v136, v137\n" \
"v_cvt_pk_bf16_f32 v137, v138, v139\n" \
"v_cvt_pk_bf16_f32 v138, v140, v141\n" \
"v_cvt_pk_bf16_f32 v139, v142, v143\n" \
"v_cndmask_b32 v140, v136, v138, s[76:77]\n" \
"v_mfma_f32_16x16x32_bf16 v[28:31], a[120:123], a[72:75], v[28:31]\n" \
"ds_read_b128 a[68:71], v213 offset:2048\n" \
"v_cndmask_b32 v141, v137, v139, s[76:77]\n" \
"v_mfma_f32_16x16x32_bf16 v[28:31], a[124:127], a[76:79], v[28:31]\n" \
"ds_read_b64_tr_b16 a[104:105], v215 offset:16384\n" \
"ds_read_b64_tr_b16 a[106:107], v215 offset:18432\n" \
"v_cndmask_b32 v142, v138, v136, s[76:77]\n" \
"v_cndmask_b32 v143, v139, v137, s[76:77]\n" \
"v_mfma_f32_16x16x32_bf16 v[24:27], a[112:115], a[72:75], v[24:27]\n" \
"buffer_load_dwordx4 v234, s[56:59], s81 offen offset:3072 sc1 lds\n" \
"v_mfma_f32_16x16x32_bf16 v[24:27], a[116:119], a[76:79], v[24:27]\n" \
"ds_read_b128 a[72:75], v213 offset:256\n" \
"v_cvt_pk_bf16_f32 v128, v128, v129\n" \
"v_cvt_pk_bf16_f32 v129, v130, v131\n" \
"v_cvt_pk_bf16_f32 v130, v132, v133\n" \
"v_cvt_pk_bf16_f32 v131, v134, v135\n" \
"v_mfma_f32_16x16x32_bf16 v[40:43], a[112:115], a[80:83], v[40:43]\n" \
"ds_read_b128 a[76:79], v213 offset:2304\n" \
"v_cndmask_b32 v132, v128, v130, s[76:77]\n" \
"v_mfma_f32_16x16x32_bf16 v[40:43], a[116:119], a[84:87], v[40:43]\n" \
"s_add_u32 m0, m0, 0x4000\n" \
"ds_read_b64_tr_b16 a[108:109], v215 offset:24576\n" \
"ds_read_b64_tr_b16 a[110:111], v215 offset:26624\n" \
"v_cndmask_b32 v133, v129, v131, s[76:77]\n" \
"v_cndmask_b32 v134, v130, v128, s[76:77]\n" \
"v_mfma_f32_16x16x32_bf16 v[44:47], a[120:123], a[80:83], v[44:47]\n" \
"buffer_load_dwordx4 v216, s[36:39], s82 offen offset:0 lds\n" \
"v_mfma_f32_16x16x32_bf16 v[44:47], a[124:127], a[84:87], v[44:47]\n" \
"ds_read_b128 a[80:83], v213 offset:4096\n" \
"v_cndmask_b32 v135, v131, v129, s[76:77]\n" \
"v_cvt_pk_bf16_f32 v120, v120, v121\n" \
"v_cvt_pk_bf16_f32 v121, v122, v123\n" \
"v_cvt_pk_bf16_f32 v122, v124, v125\n" \
"v_mfma_f32_16x16x32_bf16 v[60:63], a[120:123], a[88:91], v[60:63]\n" \
"s_waitcnt lgkmcnt(4)\n" \
"buffer_load_dwordx4 v217, s[36:39], s82 offen offset:1024 lds\n" \
"v_cvt_pk_bf16_f32 v123, v126, v127\n" \
"v_mfma_f32_16x16x32_bf16 v[60:63], a[124:127], a[92:95], v[60:63]\n" \
"ds_read_b128 a[84:87], v213 offset:6144\n" \
"v_cndmask_b32 v124, v120, v122, s[76:77]\n" \
"v_cndmask_b32 v125, v121, v123, s[76:77]\n" \
"v_mfma_f32_16x16x32_bf16 v[56:59], a[112:115], a[88:91], v[56:59]\n" \
"buffer_load_dwordx4 v218, s[36:39], s82 offen offset:2048 lds\n" \
"v_mfma_f32_16x16x32_bf16 v[56:59], a[116:119], a[92:95], v[56:59]\n" \
"v_cndmask_b32 v126, v122, v120, s[76:77]\n" \
"v_cndmask_b32 v127, v123, v121, s[76:77]\n" \
"v_cvt_pk_bf16_f32 v112, v112, v113\n" \
"v_cvt_pk_bf16_f32 v113, v114, v115\n" \
"v_cvt_pk_bf16_f32 v114, v116, v117\n" \
"v_mfma_f32_16x16x32_bf16 v[0:3], a[96:99], a[64:67], v[0:3]\n" \
"ds_read_b128 a[88:91], v213 offset:4352\n" \
"v_cvt_pk_bf16_f32 v115, v118, v119\n" \
"v_mfma_f32_16x16x32_bf16 v[0:3], a[100:103], a[68:71], v[0:3]\n" \
"s_waitcnt lgkmcnt(2)\n" \
"ds_read_b128 a[92:95], v213 offset:6400\n" \
"v_cndmask_b32 v116, v112, v114, s[76:77]\n" \
"v_cndmask_b32 v117, v113, v115, s[76:77]\n" \
"v_cndmask_b32 v118, v114, v112, s[76:77]\n" \
"v_mfma_f32_16x16x32_bf16 v[4:7], a[104:107], a[64:67], v[4:7]\n" \
"ds_read_b64_tr_b16 a[112:113], v214 offset:16896\n" \
"v_cndmask_b32 v119, v115, v113, s[76:77]\n" \
"v_mfma_f32_16x16x32_bf16 v[4:7], a[108:111], a[68:71], v[4:7]\n" \
"s_add_u32 s71, s93, 208896\n" \
"ds_read_b64_tr_b16 a[114:115], v214 offset:18944\n" \
"buffer_store_dwordx4 v[140:143], v236, s[56:59], s71 offen sc1\n" \
"s_add_u32 s71, s93, 139264\n" \
"v_mfma_f32_16x16x32_bf16 v[20:23], a[104:107], a[72:75], v[20:23]\n" \
"buffer_load_dwordx4 v219, s[36:39], s82 offen offset:3072 lds\n" \
"v_mfma_f32_16x16x32_bf16 v[20:23], a[108:111], a[76:79], v[20:23]\n" \
"s_xor_b32 s82, s48, s49\n" \
"s_cmp_lt_u32 s48, s50\n" \
"s_cselect_b32 s82, s82, s48\n" \
"ds_read_b64_tr_b16 a[116:117], v214 offset:25088\n" \
"buffer_store_dwordx4 v[132:135], v236, s[56:59], s71 offen sc1\n" \
"s_add_u32 s71, s93, 69632\n" \
"v_mfma_f32_16x16x32_bf16 v[16:19], a[96:99], a[72:75], v[16:19]\n" \
"s_waitcnt lgkmcnt(3)\n" \
"s_add_u32 s81, s82, s46\n" \
"ds_read_b64_tr_b16 a[118:119], v214 offset:27136\n" \
"v_mfma_f32_16x16x32_bf16 v[16:19], a[100:103], a[76:79], v[16:19]\n" \
"s_add_u32 m0, s86, s79\n" \
"ds_read_b64_tr_b16 a[120:121], v215 offset:16896\n" \
"buffer_store_dwordx4 v[124:127], v236, s[56:59], s71 offen sc1\n" \
"s_add_u32 s71, s93, 0\n" \
"v_mfma_f32_16x16x32_bf16 v[32:35], a[96:99], a[80:83], v[32:35]\n" \
"buffer_load_dwordx4 v234, s[56:59], s81 offen offset:0 sc1 lds\n" \
"v_mfma_f32_16x16x32_bf16 v[32:35], a[100:103], a[84:87], v[32:35]\n" \
"s_sub_u32 s85, s85, 32768\n" \
"s_cselect_b32 s85, 131072, s85\n" \
"ds_read_b64_tr_b16 a[122:123], v215 offset:18944\n" \
"buffer_store_dwordx4 v[116:119], v236, s[56:59], s71 offen sc1\n" \
"v_mfma_f32_16x16x32_bf16 v[36:39], a[104:107], a[80:83], v[36:39]\n" \
"s_lshl_b32 s82, s82, 5\n" \
"ds_read_b64_tr_b16 a[124:125], v215 offset:25088\n" \
"v_add_u32 v213, s85, v211\n" \
"v_mfma_f32_16x16x32_bf16 v[36:39], a[108:111], a[84:87], v[36:39]\n" \
"s_add_u32 s82, s82, s47\n" \
"ds_read_b64_tr_b16 a[126:127], v215 offset:27136\n" \
"v_mfma_f32_16x16x32_bf16 v[52:55], a[104:107], a[88:91], v[52:55]\n" \
"buffer_load_dwordx4 v234, s[56:59], s81 offen offset:1024 sc1 lds\n" \
"v_mfma_f32_16x16x32_bf16 v[52:55], a[108:111], a[92:95], v[52:55]\n" \
"s_add_u32 s48, s48, 0x1000\n" \
"s_sub_u32 s86, s86, 32768\n" \
"s_cselect_b32 s86, 131072, s86\n" \
"v_add_u32 v214, s85, v212\n" \
"v_xor_b32 v215, 32, v214\n" \
"v_mfma_f32_16x16x32_bf16 v[48:51], a[96:99], a[88:91], v[48:51]\n" \
"buffer_load_dwordx4 v234, s[56:59], s81 offen offset:2048 sc1 lds\n" \
"v_mfma_f32_16x16x32_bf16 v[48:51], a[100:103], a[92:95], v[48:51]\n" \
"s_waitcnt vmcnt(23) lgkmcnt(0)\n" \
"s_barrier\n" \
"v_mfma_f32_16x16x32_bf16 v[8:11], a[112:115], a[64:67], v[8:11]\n" \
"ds_read_b64_tr_b16 a[96:97], v214 offset:16384\n" \
"ds_read_b64_tr_b16 a[98:99], v214 offset:18432\n" \
"v_mfma_f32_16x16x32_bf16 v[8:11], a[116:119], a[68:71], v[8:11]\n" \
"ds_read_b64_tr_b16 a[100:101], v214 offset:24576\n" \
"ds_read_b64_tr_b16 a[102:103], v214 offset:26624\n" \
"v_mfma_f32_16x16x32_bf16 v[12:15], a[120:123], a[64:67], v[12:15]\n" \
"buffer_load_dwordx4 v234, s[56:59], s81 offen offset:3072 sc1 lds\n" \
"v_mfma_f32_16x16x32_bf16 v[12:15], a[124:127], a[68:71], v[12:15]\n" \
"ds_read_b128 a[64:67], v213 offset:0\n" \
"v_mfma_f32_16x16x32_bf16 v[28:31], a[120:123], a[72:75], v[28:31]\n" \
"ds_read_b128 a[68:71], v213 offset:2048\n" \
"v_mfma_f32_16x16x32_bf16 v[28:31], a[124:127], a[76:79], v[28:31]\n" \
"s_add_u32 m0, m0, 0x4000\n" \
"ds_read_b64_tr_b16 a[104:105], v215 offset:16384\n" \
"ds_read_b64_tr_b16 a[106:107], v215 offset:18432\n" \
"v_mfma_f32_16x16x32_bf16 v[24:27], a[112:115], a[72:75], v[24:27]\n" \
"buffer_load_dwordx4 v216, s[36:39], s82 offen offset:0 lds\n" \
"v_mfma_f32_16x16x32_bf16 v[24:27], a[116:119], a[76:79], v[24:27]\n" \
"ds_read_b128 a[72:75], v213 offset:256\n" \
"v_mfma_f32_16x16x32_bf16 v[40:43], a[112:115], a[80:83], v[40:43]\n" \
"ds_read_b128 a[76:79], v213 offset:2304\n" \
"v_mfma_f32_16x16x32_bf16 v[40:43], a[116:119], a[84:87], v[40:43]\n" \
"ds_read_b64_tr_b16 a[108:109], v215 offset:24576\n" \
"ds_read_b64_tr_b16 a[110:111], v215 offset:26624\n" \
"v_mfma_f32_16x16x32_bf16 v[44:47], a[120:123], a[80:83], v[44:47]\n" \
"buffer_load_dwordx4 v217, s[36:39], s82 offen offset:1024 lds\n" \
"v_mfma_f32_16x16x32_bf16 v[44:47], a[124:127], a[84:87], v[44:47]\n" \
"ds_read_b128 a[80:83], v213 offset:4096\n" \
"v_mfma_f32_16x16x32_bf16 v[60:63], a[120:123], a[88:91], v[60:63]\n" \
"s_waitcnt lgkmcnt(4)\n" \
"buffer_load_dwordx4 v218, s[36:39], s82 offen offset:2048 lds\n" \
"v_mfma_f32_16x16x32_bf16 v[60:63], a[124:127], a[92:95], v[60:63]\n" \
"ds_read_b128 a[84:87], v213 offset:6144\n" \
"v_mfma_f32_16x16x32_bf16 v[56:59], a[112:115], a[88:91], v[56:59]\n" \
"buffer_load_dwordx4 v219, s[36:39], s82 offen offset:3072 lds\n" \
"v_mfma_f32_16x16x32_bf16 v[56:59], a[116:119], a[92:95], v[56:59]\n" \
"s_mov_b32 s72, 2\n" \
"Lo0_%=:\n" \
"v_mfma_f32_16x16x32_bf16 v[0:3], a[96:99], a[64:67], v[0:3]\n" \
"s_waitcnt lgkmcnt(1)\n" \
"s_xor_b32 s82, s48, s49\n" \
"ds_read_b128 a[88:91], v213 offset:4352\n" \
"v_mfma_f32_16x16x32_bf16 v[0:3], a[100:103], a[68:71], v[0:3]\n" \
"s_cmp_lt_u32 s48, s50\n" \
"s_cselect_b32 s82, s82, s48\n" \
"ds_read_b128 a[92:95], v213 offset:6400\n" \
"v_mfma_f32_16x16x32_bf16 v[4:7], a[104:107], a[64:67], v[4:7]\n" \
"s_add_u32 s81, s82, s46\n" \
"ds_read_b64_tr_b16 a[112:113], v214 offset:16896\n" \
"v_mfma_f32_16x16x32_bf16 v[4:7], a[108:111], a[68:71], v[4:7]\n" \
"s_add_u32 m0, s86, s79\n" \
"ds_read_b64_tr_b16 a[114:115], v214 offset:18944\n" \
"v_mfma_f32_16x16x32_bf16 v[20:23], a[104:107], a[72:75], v[20:23]\n" \
"buffer_load_dwordx4 v234, s[56:59], s81 offen offset:0 sc1 lds\n" \
"v_mfma_f32_16x16x32_bf16 v[20:23], a[108:111], a[76:79], v[20:23]\n" \
"ds_read_b64_tr_b16 a[116:117], v214 offset:25088\n" \
"v_mfma_f32_16x16x32_bf16 v[16:19], a[96:99], a[72:75], v[16:19]\n" \
"s_lshl_b32 s82, s82, 5\n" \
"ds_read_b64_tr_b16 a[118:119], v214 offset:27136\n" \
"v_mfma_f32_16x16x32_bf16 v[16:19], a[100:103], a[76:79], v[16:19]\n" \
"s_waitcnt lgkmcnt(3)\n" \
"s_add_u32 s82, s82, s47\n" \
"ds_read_b64_tr_b16 a[120:121], v215 offset:16896\n" \
"v_mfma_f32_16x16x32_bf16 v[32:35], a[96:99], a[80:83], v[32:35]\n" \
"buffer_load_dwordx4 v234, s[56:59], s81 offen offset:1024 sc1 lds\n" \
"v_mfma_f32_16x16x32_bf16 v[32:35], a[100:103], a[84:87], v[32:35]\n" \
"s_sub_u32 s85, s85, 32768\n" \
"s_cselect_b32 s85, 131072, s85\n" \
"ds_read_b64_tr_b16 a[122:123], v215 offset:18944\n" \
"v_mfma_f32_16x16x32_bf16 v[36:39], a[104:107], a[80:83], v[36:39]\n" \
"s_add_u32 s48, s48, 0x1000\n" \
"ds_read_b64_tr_b16 a[124:125], v215 offset:25088\n" \
"v_add_u32 v213, s85, v211\n" \
"v_mfma_f32_16x16x32_bf16 v[36:39], a[108:111], a[84:87], v[36:39]\n" \
"s_sub_u32 s86, s86, 32768\n" \
"s_cselect_b32 s86, 131072, s86\n" \
"ds_read_b64_tr_b16 a[126:127], v215 offset:27136\n" \
"v_mfma_f32_16x16x32_bf16 v[52:55], a[104:107], a[88:91], v[52:55]\n" \
"buffer_load_dwordx4 v234, s[56:59], s81 offen offset:2048 sc1 lds\n" \
"v_mfma_f32_16x16x32_bf16 v[52:55], a[108:111], a[92:95], v[52:55]\n" \
"v_add_u32 v214, s85, v212\n" \
"v_xor_b32 v215, 32, v214\n" \
"v_mfma_f32_16x16x32_bf16 v[48:51], a[96:99], a[88:91], v[48:51]\n" \
"buffer_load_dwordx4 v234, s[56:59], s81 offen offset:3072 sc1 lds\n" \
"v_mfma_f32_16x16x32_bf16 v[48:51], a[100:103], a[92:95], v[48:51]\n" \
"s_waitcnt vmcnt(20) lgkmcnt(0)\n" \
"s_barrier\n" \
"v_mfma_f32_16x16x32_bf16 v[8:11], a[112:115], a[64:67], v[8:11]\n" \
"ds_read_b64_tr_b16 a[96:97], v214 offset:16384\n" \
"ds_read_b64_tr_b16 a[98:99], v214 offset:18432\n" \
"v_mfma_f32_16x16x32_bf16 v[8:11], a[116:119], a[68:71], v[8:11]\n" \
"s_add_u32 m0, m0, 0x4000\n" \
"ds_read_b64_tr_b16 a[100:101], v214 offset:24576\n" \
"ds_read_b64_tr_b16 a[102:103], v214 offset:26624\n" \
"v_mfma_f32_16x16x32_bf16 v[12:15], a[120:123], a[64:67], v[12:15]\n" \
"buffer_load_dwordx4 v216, s[36:39], s82 offen offset:0 lds\n" \
"v_mfma_f32_16x16x32_bf16 v[12:15], a[124:127], a[68:71], v[12:15]\n" \
"ds_read_b128 a[64:67], v213 offset:0\n" \
"v_mfma_f32_16x16x32_bf16 v[28:31], a[120:123], a[72:75], v[28:31]\n" \
"ds_read_b128 a[68:71], v213 offset:2048\n" \
"v_mfma_f32_16x16x32_bf16 v[28:31], a[124:127], a[76:79], v[28:31]\n" \
"ds_read_b64_tr_b16 a[104:105], v215 offset:16384\n" \
"ds_read_b64_tr_b16 a[106:107], v215 offset:18432\n" \
"v_mfma_f32_16x16x32_bf16 v[24:27], a[112:115], a[72:75], v[24:27]\n" \
"buffer_load_dwordx4 v217, s[36:39], s82 offen offset:1024 lds\n" \
"v_mfma_f32_16x16x32_bf16 v[24:27], a[116:119], a[76:79], v[24:27]\n" \
"ds_read_b128 a[72:75], v213 offset:256\n" \
"v_mfma_f32_16x16x32_bf16 v[40:43], a[112:115], a[80:83], v[40:43]\n" \
"ds_read_b128 a[76:79], v213 offset:2304\n" \
"v_mfma_f32_16x16x32_bf16 v[40:43], a[116:119], a[84:87], v[40:43]\n" \
"ds_read_b64_tr_b16 a[108:109], v215 offset:24576\n" \
"ds_read_b64_tr_b16 a[110:111], v215 offset:26624\n" \
"v_mfma_f32_16x16x32_bf16 v[44:47], a[120:123], a[80:83], v[44:47]\n" \
"buffer_load_dwordx4 v218, s[36:39], s82 offen offset:2048 lds\n" \
"v_mfma_f32_16x16x32_bf16 v[44:47], a[124:127], a[84:87], v[44:47]\n" \
"ds_read_b128 a[80:83], v213 offset:4096\n" \
"v_mfma_f32_16x16x32_bf16 v[60:63], a[120:123], a[88:91], v[60:63]\n" \
"s_waitcnt lgkmcnt(4)\n" \
"v_mfma_f32_16x16x32_bf16 v[60:63], a[124:127], a[92:95], v[60:63]\n" \
"s_sub_u32 s72, s72, 1\n" \
"ds_read_b128 a[84:87], v213 offset:6144\n" \
"v_mfma_f32_16x16x32_bf16 v[56:59], a[112:115], a[88:91], v[56:59]\n" \
"buffer_load_dwordx4 v219, s[36:39], s82 offen offset:3072 lds\n" \
"v_mfma_f32_16x16x32_bf16 v[56:59], a[116:119], a[92:95], v[56:59]\n" \
"s_cbranch_scc0 Lo0_%=\n" \
"s_setpc_b64 s[44:45]\n" \
"Lor_%=:\n" \
"buffer_store_dword v230, v231, s[56:59], s12 offen sc1\n" \
"s_mov_b32 s72, s10\n" \
"s_call_b64 s[44:45], Lo0_%=\n" \
"buffer_load_dword v220, v221, s[56:59], s67 offen sc1\n" \
"s_mov_b32 s72, 1\n" \
"s_call_b64 s[44:45], Lo0_%=\n" \
"s_waitcnt vmcnt(16)\n" \
"v_cmp_eq_u32 vcc, s60, v220\n" \
"s_nop 1\n" \
"s_andn2_b32 s70, s74, vcc_lo\n" \
"s_cbranch_scc1 Lpl6_%=\n" \
"Lpr6_%=:\n" \
"s_cmp_eq_u32 s90, 0\n" \
"s_cselect_b32 s46, s46, s83\n" \
"s_cselect_b32 s47, s47, s84\n" \
"s_cselect_b32 s48, s48, 0\n" \
"s_cselect_b64 s[36:37], s[36:37], s[40:41]\n" \
"s_cselect_b32 s38, s38, s42\n" \
"s_mov_b32 s72, 3\n" \
"s_call_b64 s[44:45], Lo0_%=\n" \
"s_cmp_eq_u32 s90, 0\n" \
"s_cbranch_scc0 Lrole1_%=\n" \
"s_mov_b32 s72, s11\n" \
"s_call_b64 s[44:45], Lo0_%=\n" \
"buffer_load_dword v220, v241, s[56:59], s64 offen sc1\n" \
"s_mov_b32 s72, 1\n" \
"s_call_b64 s[44:45], Lo0_%=\n" \
"v_mfma_f32_16x16x32_bf16 v[0:3], a[96:99], a[64:67], v[0:3]\n" \
"s_waitcnt lgkmcnt(1)\n" \
"ds_read_b128 a[88:91], v213 offset:4352\n" \
"v_mov_b32 v74, 0x3f80\n" \
"v_mfma_f32_16x16x32_bf16 v[0:3], a[100:103], a[68:71], v[0:3]\n" \
"s_add_u32 s48, s86, 32768\n" \
"s_cmp_eq_u32 s48, 163840\n" \
"s_cselect_b32 s48, 0, s48\n" \
"ds_read_b128 a[92:95], v213 offset:6400\n" \
"v_mfma_f32_16x16x32_bf16 v[4:7], a[104:107], a[64:67], v[4:7]\n" \
"ds_read_b64_tr_b16 a[112:113], v214 offset:16896\n" \
"v_mov_b32 v64, 0\n" \
"v_mfma_f32_16x16x32_bf16 v[4:7], a[108:111], a[68:71], v[4:7]\n" \
"ds_read_b64_tr_b16 a[114:115], v214 offset:18944\n" \
"v_mov_b32 v65, 0\n" \
"v_mfma_f32_16x16x32_bf16 v[20:23], a[104:107], a[72:75], v[20:23]\n" \
"s_add_u32 s49, s48, 32768\n" \
"s_cmp_eq_u32 s49, 163840\n" \
"s_cselect_b32 s49, 0, s49\n" \
"v_mfma_f32_16x16x32_bf16 v[20:23], a[108:111], a[76:79], v[20:23]\n" \
"ds_read_b64_tr_b16 a[116:117], v214 offset:25088\n" \
"v_add_u32 v224, s49, v211\n" \
"v_mfma_f32_16x16x32_bf16 v[16:19], a[96:99], a[72:75], v[16:19]\n" \
"ds_read_b64_tr_b16 a[118:119], v214 offset:27136\n" \
"v_add_u32 v225, s49, v212\n" \
"v_mfma_f32_16x16x32_bf16 v[16:19], a[100:103], a[76:79], v[16:19]\n" \
"s_waitcnt lgkmcnt(3)\n" \
"ds_read_b64_tr_b16 a[120:121], v215 offset:16896\n" \
"v_xor_b32 v226, 32, v225\n" \
"v_mfma_f32_16x16x32_bf16 v[32:35], a[96:99], a[80:83], v[32:35]\n" \
"v_add_u32 v227, s48, v211\n" \
"v_add_u32 v228, s48, v212\n" \
"v_mfma_f32_16x16x32_bf16 v[32:35], a[100:103], a[84:87], v[32:35]\n" \
"s_sub_u32 s85, s85, 32768\n" \
"s_cselect_b32 s85, 131072, s85\n" \
"ds_read_b64_tr_b16 a[122:123], v215 offset:18944\n" \
"v_mfma_f32_16x16x32_bf16 v[36:39], a[104:107], a[80:83], v[36:39]\n" \
"ds_read_b64_tr_b16 a[124:125], v215 offset:25088\n" \
"v_add_u32 v213, s85, v211\n" \
"v_mfma_f32_16x16x32_bf16 v[36:39], a[108:111], a[84:87], v[36:39]\n" \
"ds_read_b64_tr_b16 a[126:127], v215 offset:27136\n" \
"v_xor_b32 v229, 32, v228\n" \
"v_mfma_f32_16x16x32_bf16 v[52:55], a[104:107], a[88:91], v[52:55]\n" \
"v_mov_b32 v68, 0\n" \
"v_mov_b32 v69, 0\n" \
"v_mfma_f32_16x16x32_bf16 v[52:55], a[108:111], a[92:95], v[52:55]\n" \
"v_add_u32 v214, s85, v212\n" \
"v_xor_b32 v215, 32, v214\n" \
"v_mfma_f32_16x16x32_bf16 v[48:51], a[96:99], a[88:91], v[48:51]\n" \
"v_lshrrev_b32 v72, 8, v234\n" \
"v_bfe_u32 v73, v234, 6, 2\n" \
"v_mfma_f32_16x16x32_bf16 v[48:51], a[100:103], a[92:95], v[48:51]\n" \
"s_waitcnt vmcnt(8) lgkmcnt(0)\n" \
"s_barrier\n" \
"s_waitcnt vmcnt(16)\n" \
"v_cmp_eq_u32 vcc, s60, v220\n" \
"s_nop 1\n" \
"s_andn2_b32 s70, s69, vcc_lo\n" \
"s_cbranch_scc1 Lpl7_%=\n" \
"Lpr7_%=:\n" \
"v_mfma_f32_16x16x32_bf16 v[8:11], a[112:115], a[64:67], v[8:11]\n" \
"s_add_u32 s71, s87, 0\n" \
"ds_read_b128 a[0:3], v213 offset:0\n" \
"ds_read_b128 a[4:7], v213 offset:2048\n" \
"v_mfma_f32_16x16x32_bf16 v[8:11], a[116:119], a[68:71], v[8:11]\n" \
"buffer_load_dwordx4 v[176:179], v234, s[56:59], s71 offen offset:0 sc1\n" \
"v_mfma_f32_16x16x32_bf16 v[12:15], a[120:123], a[64:67], v[12:15]\n" \
"ds_read_b64_tr_b16 a[128:129], v214 offset:16384\n" \
"ds_read_b64_tr_b16 a[130:131], v214 offset:18432\n" \
"v_mfma_f32_16x16x32_bf16 v[12:15], a[124:127], a[68:71], v[12:15]\n" \
"buffer_load_dwordx4 v[180:183], v234, s[56:59], s71 offen offset:1024 sc1\n" \
"v_mfma_f32_16x16x32_bf16 v[28:31], a[120:123], a[72:75], v[28:31]\n" \
"ds_read_b64_tr_b16 a[132:133], v214 offset:24576\n" \
"ds_read_b64_tr_b16 a[134:135], v214 offset:26624\n" \
"v_mfma_f32_16x16x32_bf16 v[28:31], a[124:127], a[76:79], v[28:31]\n" \
"buffer_load_dwordx4 v[184:187], v234, s[56:59], s71 offen offset:2048 sc1\n" \
"v_mfma_f32_16x16x32_bf16 v[24:27], a[112:115], a[72:75], v[24:27]\n" \
"ds_read_b64_tr_b16 a[136:137], v215 offset:16384\n" \
"ds_read_b64_tr_b16 a[138:139], v215 offset:18432\n" \
"v_mfma_f32_16x16x32_bf16 v[24:27], a[116:119], a[76:79], v[24:27]\n" \
"buffer_load_dwordx4 v[188:191], v234, s[56:59], s71 offen offset:3072 sc1\n" \
"v_mfma_f32_16x16x32_bf16 v[40:43], a[112:115], a[80:83], v[40:43]\n" \
"ds_read_b64_tr_b16 a[140:141], v215 offset:24576\n" \
"ds_read_b64_tr_b16 a[142:143], v215 offset:26624\n" \
"v_mfma_f32_16x16x32_bf16 v[40:43], a[116:119], a[84:87], v[40:43]\n" \
"ds_read_b64_tr_b16 a[144:145], v214 offset:16896\n" \
"ds_read_b64_tr_b16 a[146:147], v214 offset:18944\n" \
"v_mfma_f32_16x16x32_bf16 v[44:47], a[120:123], a[80:83], v[44:47]\n" \
"ds_read_b64_tr_b16 a[148:149], v214 offset:25088\n" \
"ds_read_b64_tr_b16 a[150:151], v214 offset:27136\n" \
"v_mfma_f32_16x16x32_bf16 v[44:47], a[124:127], a[84:87], v[44:47]\n" \
"ds_read_b64_tr_b16 a[152:153], v215 offset:16896\n" \
"ds_read_b64_tr_b16 a[154:155], v215 offset:18944\n" \
"v_mfma_f32_16x16x32_bf16 v[60:63], a[120:123], a[88:91], v[60:63]\n" \
"ds_read_b64_tr_b16 a[156:157], v215 offset:25088\n" \
"ds_read_b64_tr_b16 a[158:159], v215 offset:27136\n" \
"v_mfma_f32_16x16x32_bf16 v[60:63], a[124:127], a[92:95], v[60:63]\n" \
"ds_read_b128 a[24:27], v213 offset:256\n" \
"ds_read_b128 a[28:31], v213 offset:2304\n" \
"v_mfma_f32_16x16x32_bf16 v[56:59], a[112:115], a[88:91], v[56:59]\n" \
"ds_read_b128 a[8:11], v224 offset:0\n" \
"ds_read_b128 a[12:15], v224 offset:2048\n" \
"v_mfma_f32_16x16x32_bf16 v[56:59], a[116:119], a[92:95], v[56:59]\n" \
"Lpg10822_%=:\n" \
"s_cbranch_execz Lcb1_%=\n" \
"s_waitcnt lgkmcnt(8)\n" \
"ds_read_b64_tr_b16 a[160:161], v225 offset:16384\n" \
"ds_read_b64_tr_b16 a[162:163], v225 offset:18432\n" \
"v_mfma_f32_16x16x32_bf16 v[0:3], a[128:131], a[0:3], v[0:3]\n" \
"ds_read_b64_tr_b16 a[164:165], v225 offset:24576\n" \
"ds_read_b64_tr_b16 a[166:167], v225 offset:26624\n" \
"v_mfma_f32_16x16x32_bf16 v[0:3], a[132:135], a[4:7], v[0:3]\n" \
"ds_read_b64_tr_b16 a[168:169], v226 offset:16384\n" \
"ds_read_b64_tr_b16 a[170:171], v226 offset:18432\n" \
"v_mfma_f32_16x16x32_bf16 v[4:7], a[136:139], a[0:3], v[4:7]\n" \
"s_mov_b32 s70, 0x20000000\n" \
"ds_read_b64_tr_b16 a[172:173], v226 offset:24576\n" \
"ds_read_b64_tr_b16 a[174:175], v226 offset:26624\n" \
"v_mfma_f32_16x16x32_bf16 v[4:7], a[140:143], a[4:7], v[4:7]\n" \
"s_cmp_eq_u32 s64, 65616896\n" \
"s_cselect_b32 s70, 65593344, s70\n" \
"ds_read_b64_tr_b16 a[176:177], v225 offset:16896\n" \
"ds_read_b64_tr_b16 a[178:179], v225 offset:18944\n" \
"buffer_store_dword v230, v231, s[56:59], s70 offen sc1\n" \
"v_mfma_f32_16x16x32_bf16 v[8:11], a[144:147], a[0:3], v[8:11]\n" \
"s_waitcnt lgkmcnt(8)\n" \
"ds_read_b64_tr_b16 a[180:181], v225 offset:25088\n" \
"ds_read_b64_tr_b16 a[182:183], v225 offset:27136\n" \
"v_mfma_f32_16x16x32_bf16 v[8:11], a[148:151], a[4:7], v[8:11]\n" \
"s_sub_u32 s68, s86, 32768\n" \
"s_cselect_b32 s68, 131072, s68\n" \
"ds_read_b64_tr_b16 a[184:185], v226 offset:16896\n" \
"ds_read_b64_tr_b16 a[186:187], v226 offset:18944\n" \
"v_mfma_f32_16x16x32_bf16 v[12:15], a[152:155], a[0:3], v[12:15]\n" \
"ds_read_b64_tr_b16 a[188:189], v226 offset:25088\n" \
"ds_read_b64_tr_b16 a[190:191], v226 offset:27136\n" \
"v_mfma_f32_16x16x32_bf16 v[12:15], a[156:159], a[4:7], v[12:15]\n" \
"s_cmp_lt_u32 s61, 2\n" \
"s_cselect_b32 s68, s86, s68\n" \
"v_cmp_eq_u32 s[16:17], v72, v73\n" \
"v_and_b32 v72, 16, v234\n" \
"v_mfma_f32_16x16x32_bf16 v[28:31], a[152:155], a[24:27], v[28:31]\n" \
"s_and_b32 s50, s61, 1\n" \
"v_lshlrev_b32 v72, v72, v74\n" \
"v_cndmask_b32 v72, 0, v72, s[16:17]\n" \
"v_mfma_f32_16x16x32_bf16 v[28:31], a[156:159], a[28:31], v[28:31]\n" \
"s_mul_i32 s50, s50, 9216\n" \
"v_bfe_i32 v73, v234, 5, 1\n" \
"v_and_b32 v67, v72, v73\n" \
"v_mfma_f32_16x16x32_bf16 v[24:27], a[144:147], a[24:27], v[24:27]\n" \
"s_add_u32 s68, s68, s50\n" \
"s_add_u32 s68, s68, %3\n" \
"v_xor_b32 v66, v72, v67\n" \
"v_mfma_f32_16x16x32_bf16 v[24:27], a[148:151], a[28:31], v[24:27]\n" \
"v_lshrrev_b32 v132, 4, v234\n" \
"v_and_b32 v133, 15, v132\n" \
"v_mul_u32_u24 v128, 144, v133\n" \
"v_mfma_f32_16x16x32_bf16 v[20:23], a[136:139], a[24:27], v[20:23]\n" \
"s_waitcnt vmcnt(5)\n" \
"s_barrier\n" \
"v_mfma_f32_16x16x32_bf16 v[20:23], a[140:143], a[28:31], v[20:23]\n" \
"s_add_u32 s71, s87, 4096\n" \
"ds_read_b128 a[32:35], v224 offset:256\n" \
"ds_read_b128 a[36:39], v224 offset:2304\n" \
"v_mfma_f32_16x16x32_bf16 v[16:19], a[128:131], a[24:27], v[16:19]\n" \
"buffer_load_dwordx4 v[192:195], v234, s[56:59], s71 offen offset:0 sc1\n" \
"v_mfma_f32_16x16x32_bf16 v[16:19], a[132:135], a[28:31], v[16:19]\n" \
"s_waitcnt lgkmcnt(2)\n" \
"ds_read_b128 a[16:19], v227 offset:0\n" \
"ds_read_b128 a[20:23], v227 offset:2048\n" \
"v_mfma_f32_16x16x32_bf16 v[0:3], a[160:163], a[8:11], v[0:3]\n" \
"ds_read_b64_tr_b16 a[192:193], v228 offset:16384\n" \
"ds_read_b64_tr_b16 a[194:195], v228 offset:18432\n" \
"v_mfma_f32_16x16x32_bf16 v[0:3], a[164:167], a[12:15], v[0:3]\n" \
"buffer_load_dwordx4 v[196:199], v234, s[56:59], s71 offen offset:1024 sc1\n" \
"v_mfma_f32_16x16x32_bf16 v[4:7], a[168:171], a[8:11], v[4:7]\n" \
"ds_read_b64_tr_b16 a[196:197], v228 offset:24576\n" \
"ds_read_b64_tr_b16 a[198:199], v228 offset:26624\n" \
"v_mfma_f32_16x16x32_bf16 v[4:7], a[172:175], a[12:15], v[4:7]\n" \
"ds_read_b64_tr_b16 a[200:201], v229 offset:16384\n" \
"ds_read_b64_tr_b16 a[202:203], v229 offset:18432\n" \
"v_mfma_f32_16x16x32_bf16 v[8:11], a[176:179], a[8:11], v[8:11]\n" \
"buffer_load_dwordx4 v[200:203], v234, s[56:59], s71 offen offset:2048 sc1\n" \
"v_mfma_f32_16x16x32_bf16 v[8:11], a[180:183], a[12:15], v[8:11]\n" \
"ds_read_b64_tr_b16 a[204:205], v229 offset:24576\n" \
"ds_read_b64_tr_b16 a[206:207], v229 offset:26624\n" \
"v_lshrrev_b32 v133, 4, v132\n" \
"v_mfma_f32_16x16x32_bf16 v[12:15], a[184:187], a[8:11], v[12:15]\n" \
"s_waitcnt lgkmcnt(6)\n" \
"ds_read_b64_tr_b16 a[208:209], v228 offset:16896\n" \
"ds_read_b64_tr_b16 a[210:211], v228 offset:18944\n" \
"v_mfma_f32_16x16x32_bf16 v[12:15], a[188:191], a[12:15], v[12:15]\n" \
"buffer_load_dwordx4 v[204:207], v234, s[56:59], s71 offen offset:3072 sc1\n" \
"v_mfma_f32_16x16x32_bf16 v[28:31], a[184:187], a[32:35], v[28:31]\n" \
"ds_read_b64_tr_b16 a[212:213], v228 offset:25088\n" \
"ds_read_b64_tr_b16 a[214:215], v228 offset:27136\n" \
"v_mfma_f32_16x16x32_bf16 v[28:31], a[188:191], a[36:39], v[28:31]\n" \
"ds_read_b64_tr_b16 a[216:217], v229 offset:16896\n" \
"ds_read_b64_tr_b16 a[218:219], v229 offset:18944\n" \
"v_mfma_f32_16x16x32_bf16 v[24:27], a[176:179], a[32:35], v[24:27]\n" \
"ds_read_b64_tr_b16 a[220:221], v229 offset:25088\n" \
"ds_read_b64_tr_b16 a[222:223], v229 offset:27136\n" \
"v_mfma_f32_16x16x32_bf16 v[24:27], a[180:183], a[36:39], v[24:27]\n" \
"ds_read_b128 a[40:43], v227 offset:256\n" \
"ds_read_b128 a[44:47], v227 offset:2304\n" \
"v_mfma_f32_16x16x32_bf16 v[20:23], a[168:171], a[32:35], v[20:23]\n" \
"ds_read_b128 a[248:251], v213 offset:4096\n" \
"ds_read_b128 a[252:255], v213 offset:6144\n" \
"v_mfma_f32_16x16x32_bf16 v[20:23], a[172:175], a[36:39], v[20:23]\n" \
"ds_read_b128 a[48:51], v224 offset:4096\n" \
"ds_read_b128 a[52:55], v224 offset:6144\n" \
"v_mfma_f32_16x16x32_bf16 v[16:19], a[160:163], a[32:35], v[16:19]\n" \
"ds_read_b128 a[56:59], v227 offset:4096\n" \
"ds_read_b128 a[60:63], v227 offset:6144\n" \
"v_mfma_f32_16x16x32_bf16 v[16:19], a[164:167], a[36:39], v[16:19]\n" \
"s_waitcnt lgkmcnt(8)\n" \
"ds_read_b128 a[224:227], v213 offset:4352\n" \
"ds_read_b128 a[228:231], v213 offset:6400\n" \
"v_lshl_add_u32 v128, v133, 3, v128\n" \
"v_mfma_f32_16x16x32_bf16 v[0:3], a[192:195], a[16:19], v[0:3]\n" \
"s_waitcnt vmcnt(7)\n" \
"ds_read_b128 a[232:235], v224 offset:4352\n" \
"ds_read_b128 a[236:239], v224 offset:6400\n" \
"v_mfma_f32_16x16x32_bf16 v[0:3], a[196:199], a[20:23], v[0:3]\n" \
"ds_read_b128 a[240:243], v227 offset:4352\n" \
"ds_read_b128 a[244:247], v227 offset:6400\n" \
"v_mfma_f32_16x16x32_bf16 v[0:3], v[66:69], v[176:179], v[0:3]\n" \
"v_add_u32 v128, s68, v128\n" \
"v_lshrrev_b32 v133, 3, v132\n" \
"v_mfma_f32_16x16x32_bf16 v[4:7], a[200:203], a[16:19], v[4:7]\n" \
"v_and_b32 v134, 7, v132\n" \
"v_mul_u32_u24 v129, 144, v133\n" \
"v_mfma_f32_16x16x32_bf16 v[4:7], a[204:207], a[20:23], v[4:7]\n" \
"v_lshl_add_u32 v129, v134, 4, v129\n" \
"v_add_u32 v129, s68, v129\n" \
"v_mfma_f32_16x16x32_bf16 v[4:7], v[64:67], v[176:179], v[4:7]\n" \
"v_lshlrev_b32 v134, 4, v134\n" \
"v_lshl_add_u32 v130, v133, 12, v134\n" \
"v_mfma_f32_16x16x32_bf16 v[8:11], a[208:211], a[16:19], v[8:11]\n" \
"v_mul_f32 v0, 4.0, v0\n" \
"v_mul_f32 v1, 4.0, v1\n" \
"v_mul_f32 v2, 4.0, v2\n" \
"v_mul_f32 v3, 4.0, v3\n" \
"v_cvt_pk_bf16_f32 v136, v0, v1\n" \
"v_cvt_pk_bf16_f32 v137, v2, v3\n" \
"v_mfma_f32_16x16x32_bf16 v[8:11], a[212:215], a[20:23], v[8:11]\n" \
"v_mfma_f32_16x16x32_bf16 v[8:11], v[66:69], v[180:183], v[8:11]\n" \
"ds_write_b64 v128, v[136:137] offset:0\n" \
"v_mfma_f32_16x16x32_bf16 v[12:15], a[216:219], a[16:19], v[12:15]\n" \
"v_mul_f32 v4, 4.0, v4\n" \
"v_mul_f32 v5, 4.0, v5\n" \
"v_mfma_f32_16x16x32_bf16 v[12:15], a[220:223], a[20:23], v[12:15]\n" \
"v_mul_f32 v6, 4.0, v6\n" \
"v_mul_f32 v7, 4.0, v7\n" \
"v_mfma_f32_16x16x32_bf16 v[12:15], v[64:67], v[180:183], v[12:15]\n" \
"s_waitcnt lgkmcnt(1)\n" \
"v_cvt_pk_bf16_f32 v138, v4, v5\n" \
"v_cvt_pk_bf16_f32 v139, v6, v7\n" \
"v_mfma_f32_16x16x32_bf16 v[16:19], a[192:195], a[40:43], v[16:19]\n" \
"s_waitcnt vmcnt(5)\n" \
"v_mfma_f32_16x16x32_bf16 v[16:19], a[196:199], a[44:47], v[16:19]\n" \
"v_mfma_f32_16x16x32_bf16 v[16:19], v[66:69], v[184:187], v[16:19]\n" \
"ds_write_b64 v128, v[138:139] offset:32\n" \
"v_mul_f32 v8, 4.0, v8\n" \
"v_mfma_f32_16x16x32_bf16 v[20:23], a[200:203], a[40:43], v[20:23]\n" \
"v_mul_f32 v9, 4.0, v9\n" \
"v_mul_f32 v10, 4.0, v10\n" \
"v_mfma_f32_16x16x32_bf16 v[20:23], a[204:207], a[44:47], v[20:23]\n" \
"v_mul_f32 v11, 4.0, v11\n" \
"v_cvt_pk_bf16_f32 v136, v8, v9\n" \
"v_mfma_f32_16x16x32_bf16 v[20:23], v[64:67], v[184:187], v[20:23]\n" \
"v_cvt_pk_bf16_f32 v137, v10, v11\n" \
"v_mfma_f32_16x16x32_bf16 v[24:27], a[208:211], a[40:43], v[24:27]\n" \
"v_mfma_f32_16x16x32_bf16 v[24:27], a[212:215], a[44:47], v[24:27]\n" \
"v_mfma_f32_16x16x32_bf16 v[24:27], v[66:69], v[188:191], v[24:27]\n" \
"ds_write_b64 v128, v[136:137] offset:64\n" \
"v_mul_f32 v12, 4.0, v12\n" \
"v_mfma_f32_16x16x32_bf16 v[28:31], a[216:219], a[40:43], v[28:31]\n" \
"v_mul_f32 v13, 4.0, v13\n" \
"v_mul_f32 v14, 4.0, v14\n" \
"v_mfma_f32_16x16x32_bf16 v[28:31], a[220:223], a[44:47], v[28:31]\n" \
"v_mul_f32 v15, 4.0, v15\n" \
"v_cvt_pk_bf16_f32 v138, v12, v13\n" \
"v_mfma_f32_16x16x32_bf16 v[28:31], v[64:67], v[188:191], v[28:31]\n" \
"v_cvt_pk_bf16_f32 v139, v14, v15\n" \
"v_mfma_f32_16x16x32_bf16 v[32:35], a[128:131], a[248:251], v[32:35]\n" \
"ds_write_b64 v128, v[138:139] offset:96\n" \
"v_mfma_f32_16x16x32_bf16 v[32:35], a[132:135], a[252:255], v[32:35]\n" \
"ds_read_b128 v[140:143], v129 offset:0\n" \
"ds_read_b128 v[144:147], v129 offset:1152\n" \
"v_mfma_f32_16x16x32_bf16 v[32:35], a[160:163], a[48:51], v[32:35]\n" \
"s_waitcnt vmcnt(2)\n" \
"v_mul_f32 v16, 4.0, v16\n" \
"v_mul_f32 v17, 4.0, v17\n" \
"v_mfma_f32_16x16x32_bf16 v[32:35], a[164:167], a[52:55], v[32:35]\n" \
"v_mul_f32 v18, 4.0, v18\n" \
"v_mul_f32 v19, 4.0, v19\n" \
"v_mfma_f32_16x16x32_bf16 v[32:35], v[66:69], v[192:195], v[32:35]\n" \
"s_add_u32 s70, s88, 0\n" \
"s_waitcnt lgkmcnt(1)\n" \
"v_cvt_pk_bf16_f32 v136, v16, v17\n" \
"v_mfma_f32_16x16x32_bf16 v[32:35], a[192:195], a[56:59], v[32:35]\n" \
"buffer_store_dwordx4 v[140:143], v130, s[52:55], s70 offen sc1\n" \
"v_cvt_pk_bf16_f32 v137, v18, v19\n" \
"v_mfma_f32_16x16x32_bf16 v[32:35], a[196:199], a[60:63], v[32:35]\n" \
"ds_write_b64 v128, v[136:137] offset:2304\n" \
"v_mfma_f32_16x16x32_bf16 v[36:39], a[204:207], a[60:63], v[36:39]\n" \
"s_add_u32 s70, s88, 32768\n" \
"s_waitcnt lgkmcnt(1)\n" \
"v_mul_f32 v20, 4.0, v20\n" \
"v_mfma_f32_16x16x32_bf16 v[36:39], a[200:203], a[56:59], v[36:39]\n" \
"buffer_store_dwordx4 v[144:147], v130, s[52:55], s70 offen sc1\n" \
"v_mfma_f32_16x16x32_bf16 v[36:39], a[172:175], a[52:55], v[36:39]\n" \
"v_mul_f32 v21, 4.0, v21\n" \
"v_mul_f32 v22, 4.0, v22\n" \
"v_mfma_f32_16x16x32_bf16 v[36:39], a[168:171], a[48:51], v[36:39]\n" \
"v_mul_f32 v23, 4.0, v23\n" \
"v_cvt_pk_bf16_f32 v138, v20, v21\n" \
"v_mfma_f32_16x16x32_bf16 v[36:39], v[64:67], v[192:195], v[36:39]\n" \
"v_cvt_pk_bf16_f32 v139, v22, v23\n" \
"v_mfma_f32_16x16x32_bf16 v[36:39], a[140:143], a[252:255], v[36:39]\n" \
"v_mfma_f32_16x16x32_bf16 v[36:39], a[136:139], a[248:251], v[36:39]\n" \
"ds_write_b64 v128, v[138:139] offset:2336\n" \
"v_mul_f32 v24, 4.0, v24\n" \
"v_mul_f32 v25, 4.0, v25\n" \
"v_mfma_f32_16x16x32_bf16 v[40:43], a[144:147], a[248:251], v[40:43]\n" \
"v_mul_f32 v26, 4.0, v26\n" \
"v_mul_f32 v27, 4.0, v27\n" \
"v_mfma_f32_16x16x32_bf16 v[40:43], a[148:151], a[252:255], v[40:43]\n" \
"v_cvt_pk_bf16_f32 v136, v24, v25\n" \
"v_cvt_pk_bf16_f32 v137, v26, v27\n" \
"v_mfma_f32_16x16x32_bf16 v[40:43], a[176:179], a[48:51], v[40:43]\n" \
"ds_write_b64 v128, v[136:137] offset:2368\n" \
"v_mfma_f32_16x16x32_bf16 v[40:43], a[180:183], a[52:55], v[40:43]\n" \
"v_mul_f32 v28, 4.0, v28\n" \
"v_mul_f32 v29, 4.0, v29\n" \
"v_mfma_f32_16x16x32_bf16 v[40:43], v[66:69], v[196:199], v[40:43]\n" \
"v_mul_f32 v30, 4.0, v30\n" \
"v_mul_f32 v31, 4.0, v31\n" \
"v_mfma_f32_16x16x32_bf16 v[40:43], a[208:211], a[56:59], v[40:43]\n" \
"v_cvt_pk_bf16_f32 v138, v28, v29\n" \
"v_cvt_pk_bf16_f32 v139, v30, v31\n" \
"v_mfma_f32_16x16x32_bf16 v[40:43], a[212:215], a[60:63], v[40:43]\n" \
"ds_write_b64 v128, v[138:139] offset:2400\n" \
"v_mfma_f32_16x16x32_bf16 v[44:47], a[220:223], a[60:63], v[44:47]\n" \
"ds_read_b128 v[148:151], v129 offset:2304\n" \
"ds_read_b128 v[152:155], v129 offset:3456\n" \
"v_mfma_f32_16x16x32_bf16 v[44:47], a[216:219], a[56:59], v[44:47]\n" \
"v_mul_f32 v32, 4.0, v32\n" \
"v_mul_f32 v33, 4.0, v33\n" \
"v_mfma_f32_16x16x32_bf16 v[44:47], a[188:191], a[52:55], v[44:47]\n" \
"v_mul_f32 v34, 4.0, v34\n" \
"v_mul_f32 v35, 4.0, v35\n" \
"v_mfma_f32_16x16x32_bf16 v[44:47], a[184:187], a[48:51], v[44:47]\n" \
"s_add_u32 s70, s88, 65536\n" \
"s_waitcnt lgkmcnt(1)\n" \
"v_cvt_pk_bf16_f32 v136, v32, v33\n" \
"v_mfma_f32_16x16x32_bf16 v[44:47], v[64:67], v[196:199], v[44:47]\n" \
"buffer_store_dwordx4 v[148:151], v130, s[52:55], s70 offen sc1\n" \
"v_cvt_pk_bf16_f32 v137, v34, v35\n" \
"v_mfma_f32_16x16x32_bf16 v[44:47], a[156:159], a[252:255], v[44:47]\n" \
"v_mfma_f32_16x16x32_bf16 v[44:47], a[152:155], a[248:251], v[44:47]\n" \
"s_add_u32 s70, s88, 98304\n" \
"s_waitcnt lgkmcnt(0)\n" \
"ds_write_b64 v128, v[136:137] offset:4608\n" \
"v_mul_f32 v36, 4.0, v36\n" \
"v_mfma_f32_16x16x32_bf16 v[48:51], a[128:131], a[224:227], v[48:51]\n" \
"buffer_store_dwordx4 v[152:155], v130, s[52:55], s70 offen sc1\n" \
"v_mul_f32 v37, 4.0, v37\n" \
"v_mfma_f32_16x16x32_bf16 v[48:51], a[132:135], a[228:231], v[48:51]\n" \
"v_mul_f32 v38, 4.0, v38\n" \
"v_mul_f32 v39, 4.0, v39\n" \
"v_mfma_f32_16x16x32_bf16 v[48:51], a[160:163], a[232:235], v[48:51]\n" \
"s_waitcnt vmcnt(4)\n" \
"v_cvt_pk_bf16_f32 v138, v36, v37\n" \
"v_cvt_pk_bf16_f32 v139, v38, v39\n" \
"v_mfma_f32_16x16x32_bf16 v[48:51], a[164:167], a[236:239], v[48:51]\n" \
"ds_write_b64 v128, v[138:139] offset:4640\n" \
"v_mul_f32 v40, 4.0, v40\n" \
"v_mfma_f32_16x16x32_bf16 v[48:51], v[66:69], v[200:203], v[48:51]\n" \
"v_mul_f32 v41, 4.0, v41\n" \
"v_mul_f32 v42, 4.0, v42\n" \
"v_mfma_f32_16x16x32_bf16 v[48:51], a[192:195], a[240:243], v[48:51]\n" \
"v_mul_f32 v43, 4.0, v43\n" \
"v_cvt_pk_bf16_f32 v136, v40, v41\n" \
"v_mfma_f32_16x16x32_bf16 v[48:51], a[196:199], a[244:247], v[48:51]\n" \
"v_cvt_pk_bf16_f32 v137, v42, v43\n" \
"v_mfma_f32_16x16x32_bf16 v[52:55], a[204:207], a[244:247], v[52:55]\n" \
"ds_write_b64 v128, v[136:137] offset:4672\n" \
"v_mul_f32 v44, 4.0, v44\n" \
"v_mul_f32 v45, 4.0, v45\n" \
"v_mfma_f32_16x16x32_bf16 v[52:55], a[200:203], a[240:243], v[52:55]\n" \
"v_mul_f32 v46, 4.0, v46\n" \
"v_mul_f32 v47, 4.0, v47\n" \
"v_mfma_f32_16x16x32_bf16 v[52:55], a[172:175], a[236:239], v[52:55]\n" \
"v_cvt_pk_bf16_f32 v138, v44, v45\n" \
"v_cvt_pk_bf16_f32 v139, v46, v47\n" \
"v_mfma_f32_16x16x32_bf16 v[52:55], a[168:171], a[232:235], v[52:55]\n" \
"ds_write_b64 v128, v[138:139] offset:4704\n" \
"v_mfma_f32_16x16x32_bf16 v[52:55], v[64:67], v[200:203], v[52:55]\n" \
"ds_read_b128 v[156:159], v129 offset:4608\n" \
"ds_read_b128 v[160:163], v129 offset:5760\n" \
"v_mfma_f32_16x16x32_bf16 v[52:55], a[140:143], a[228:231], v[52:55]\n" \
"v_mul_f32 v48, 4.0, v48\n" \
"v_mul_f32 v49, 4.0, v49\n" \
"v_mfma_f32_16x16x32_bf16 v[52:55], a[136:139], a[224:227], v[52:55]\n" \
"v_mul_f32 v50, 4.0, v50\n" \
"v_mul_f32 v51, 4.0, v51\n" \
"v_mfma_f32_16x16x32_bf16 v[56:59], a[144:147], a[224:227], v[56:59]\n" \
"s_add_u32 s70, s88, 131072\n" \
"s_waitcnt lgkmcnt(1)\n" \
"v_cvt_pk_bf16_f32 v136, v48, v49\n" \
"v_mfma_f32_16x16x32_bf16 v[56:59], a[148:151], a[228:231], v[56:59]\n" \
"buffer_store_dwordx4 v[156:159], v130, s[52:55], s70 offen sc1\n" \
"v_cvt_pk_bf16_f32 v137, v50, v51\n" \
"v_mfma_f32_16x16x32_bf16 v[56:59], a[176:179], a[232:235], v[56:59]\n" \
"ds_write_b64 v128, v[136:137] offset:6912\n" \
"v_mfma_f32_16x16x32_bf16 v[56:59], a[180:183], a[236:239], v[56:59]\n" \
"s_add_u32 s70, s88, 163840\n" \
"s_waitcnt lgkmcnt(1)\n" \
"v_mul_f32 v52, 4.0, v52\n" \
"v_mfma_f32_16x16x32_bf16 v[56:59], v[66:69], v[204:207], v[56:59]\n" \
"buffer_store_dwordx4 v[160:163], v130, s[52:55], s70 offen sc1\n" \
"v_mfma_f32_16x16x32_bf16 v[56:59], a[208:211], a[240:243], v[56:59]\n" \
"v_mul_f32 v53, 4.0, v53\n" \
"v_mul_f32 v54, 4.0, v54\n" \
"v_mfma_f32_16x16x32_bf16 v[56:59], a[212:215], a[244:247], v[56:59]\n" \
"v_mul_f32 v55, 4.0, v55\n" \
"v_cvt_pk_bf16_f32 v138, v52, v53\n" \
"v_cvt_pk_bf16_f32 v139, v54, v55\n" \
"v_mfma_f32_16x16x32_bf16 v[60:63], a[220:223], a[244:247], v[60:63]\n" \
"ds_write_b64 v128, v[138:139] offset:6944\n" \
"v_mfma_f32_16x16x32_bf16 v[60:63], a[216:219], a[240:243], v[60:63]\n" \
"v_mfma_f32_16x16x32_bf16 v[60:63], a[188:191], a[236:239], v[60:63]\n" \
"v_mfma_f32_16x16x32_bf16 v[60:63], a[184:187], a[232:235], v[60:63]\n" \
"v_mul_f32 v56, 4.0, v56\n" \
"v_mul_f32 v57, 4.0, v57\n" \
"v_mfma_f32_16x16x32_bf16 v[60:63], v[64:67], v[204:207], v[60:63]\n" \
"v_mul_f32 v58, 4.0, v58\n" \
"v_mul_f32 v59, 4.0, v59\n" \
"v_mfma_f32_16x16x32_bf16 v[60:63], a[156:159], a[228:231], v[60:63]\n" \
"v_cvt_pk_bf16_f32 v136, v56, v57\n" \
"v_cvt_pk_bf16_f32 v137, v58, v59\n" \
"v_mfma_f32_16x16x32_bf16 v[60:63], a[152:155], a[224:227], v[60:63]\n" \
"ds_write_b64 v128, v[136:137] offset:6976\n" \
"s_nop 7\n" \
"v_mul_f32 v60, 4.0, v60\n" \
"v_mul_f32 v61, 4.0, v61\n" \
"v_mul_f32 v62, 4.0, v62\n" \
"v_mul_f32 v63, 4.0, v63\n" \
"v_cvt_pk_bf16_f32 v138, v60, v61\n" \
"v_cvt_pk_bf16_f32 v139, v62, v63\n" \
"ds_write_b64 v128, v[138:139] offset:7008\n" \
"ds_read_b128 v[164:167], v129 offset:6912\n" \
"ds_read_b128 v[168:171], v129 offset:8064\n" \
"s_add_u32 s70, s88, 196608\n" \
"s_waitcnt lgkmcnt(1)\n" \
"buffer_store_dwordx4 v[164:167], v130, s[52:55], s70 offen sc1\n" \
"s_add_u32 s70, s88, 229376\n" \
"s_waitcnt lgkmcnt(0)\n" \
"buffer_store_dwordx4 v[168:171], v130, s[52:55], s70 offen sc1\n" \
"s_endpgm\n" \
".p2align 12\n" \
"Lrole1_%=:\n" \
"v_mfma_f32_16x16x32_bf16 v[64:67], a[96:99], a[64:67], 0\n" \
"Lpg11183_%=:\n" \
"s_cbranch_execz Lcb1_%=\n" \
"s_waitcnt lgkmcnt(1)\n" \
"s_xor_b32 s82, s48, s49\n" \
"ds_read_b128 a[88:91], v213 offset:4352\n" \
"v_mfma_f32_16x16x32_bf16 v[64:67], a[100:103], a[68:71], v[64:67]\n" \
"s_cmp_lt_u32 s48, s50\n" \
"s_cselect_b32 s82, s82, s48\n" \
"ds_read_b128 a[92:95], v213 offset:6400\n" \
"v_mfma_f32_16x16x32_bf16 v[68:71], a[104:107], a[64:67], 0\n" \
"s_add_u32 s81, s82, s46\n" \
"ds_read_b64_tr_b16 a[112:113], v214 offset:16896\n" \
"v_mfma_f32_16x16x32_bf16 v[68:71], a[108:111], a[68:71], v[68:71]\n" \
"s_add_u32 m0, s86, s79\n" \
"ds_read_b64_tr_b16 a[114:115], v214 offset:18944\n" \
"v_mfma_f32_16x16x32_bf16 v[84:87], a[104:107], a[72:75], 0\n" \
"buffer_load_dwordx4 v234, s[56:59], s81 offen offset:0 sc1 lds\n" \
"v_mfma_f32_16x16x32_bf16 v[84:87], a[108:111], a[76:79], v[84:87]\n" \
"s_waitcnt lgkmcnt(3)\n" \
"ds_read_b64_tr_b16 a[116:117], v214 offset:25088\n" \
"v_mfma_f32_16x16x32_bf16 v[80:83], a[96:99], a[72:75], 0\n" \
"s_lshl_b32 s82, s82, 5\n" \
"ds_read_b64_tr_b16 a[118:119], v214 offset:27136\n" \
"v_cvt_pk_bf16_f32 v0, v0, v1\n" \
"v_mfma_f32_16x16x32_bf16 v[80:83], a[100:103], a[76:79], v[80:83]\n" \
"s_add_u32 s82, s82, s47\n" \
"ds_read_b64_tr_b16 a[120:121], v215 offset:16896\n" \
"v_cvt_pk_bf16_f32 v1, v2, v3\n" \
"v_mfma_f32_16x16x32_bf16 v[96:99], a[96:99], a[80:83], 0\n" \
"buffer_load_dwordx4 v234, s[56:59], s81 offen offset:1024 sc1 lds\n" \
"v_mfma_f32_16x16x32_bf16 v[96:99], a[100:103], a[84:87], v[96:99]\n" \
"s_sub_u32 s85, s85, 32768\n" \
"s_cselect_b32 s85, 131072, s85\n" \
"ds_read_b64_tr_b16 a[122:123], v215 offset:18944\n" \
"v_cvt_pk_bf16_f32 v2, v4, v5\n" \
"v_mfma_f32_16x16x32_bf16 v[100:103], a[104:107], a[80:83], 0\n" \
"s_add_u32 s48, s48, 0x1000\n" \
"ds_read_b64_tr_b16 a[124:125], v215 offset:25088\n" \
"v_add_u32 v213, s85, v211\n" \
"v_mfma_f32_16x16x32_bf16 v[100:103], a[108:111], a[84:87], v[100:103]\n" \
"s_waitcnt lgkmcnt(3)\n" \
"s_sub_u32 s86, s86, 32768\n" \
"s_cselect_b32 s86, 131072, s86\n" \
"ds_read_b64_tr_b16 a[126:127], v215 offset:27136\n" \
"v_mfma_f32_16x16x32_bf16 v[116:119], a[104:107], a[88:91], 0\n" \
"buffer_load_dwordx4 v234, s[56:59], s81 offen offset:2048 sc1 lds\n" \
"v_mfma_f32_16x16x32_bf16 v[116:119], a[108:111], a[92:95], v[116:119]\n" \
"v_add_u32 v214, s85, v212\n" \
"v_xor_b32 v215, 32, v214\n" \
"v_cvt_pk_bf16_f32 v3, v6, v7\n" \
"v_mfma_f32_16x16x32_bf16 v[112:115], a[96:99], a[88:91], 0\n" \
"buffer_load_dwordx4 v234, s[56:59], s81 offen offset:3072 sc1 lds\n" \
"v_mfma_f32_16x16x32_bf16 v[112:115], a[100:103], a[92:95], v[112:115]\n" \
"s_waitcnt vmcnt(20) lgkmcnt(0)\n" \
"s_barrier\n" \
"v_mfma_f32_16x16x32_bf16 v[72:75], a[112:115], a[64:67], 0\n" \
"ds_read_b64_tr_b16 a[96:97], v214 offset:16384\n" \
"ds_read_b64_tr_b16 a[98:99], v214 offset:18432\n" \
"v_mfma_f32_16x16x32_bf16 v[72:75], a[116:119], a[68:71], v[72:75]\n" \
"s_add_u32 m0, m0, 0x4000\n" \
"ds_read_b64_tr_b16 a[100:101], v214 offset:24576\n" \
"ds_read_b64_tr_b16 a[102:103], v214 offset:26624\n" \
"v_cvt_pk_bf16_f32 v4, v8, v9\n" \
"v_mfma_f32_16x16x32_bf16 v[76:79], a[120:123], a[64:67], 0\n" \
"buffer_load_dwordx4 v216, s[36:39], s82 offen offset:0 lds\n" \
"v_mfma_f32_16x16x32_bf16 v[76:79], a[124:127], a[68:71], v[76:79]\n" \
"ds_read_b128 a[64:67], v213 offset:0\n" \
"v_cvt_pk_bf16_f32 v5, v10, v11\n" \
"v_mfma_f32_16x16x32_bf16 v[92:95], a[120:123], a[72:75], 0\n" \
"ds_read_b128 a[68:71], v213 offset:2048\n" \
"v_cvt_pk_bf16_f32 v6, v12, v13\n" \
"v_mfma_f32_16x16x32_bf16 v[92:95], a[124:127], a[76:79], v[92:95]\n" \
"ds_read_b64_tr_b16 a[104:105], v215 offset:16384\n" \
"ds_read_b64_tr_b16 a[106:107], v215 offset:18432\n" \
"v_cvt_pk_bf16_f32 v7, v14, v15\n" \
"v_mfma_f32_16x16x32_bf16 v[88:91], a[112:115], a[72:75], 0\n" \
"buffer_load_dwordx4 v217, s[36:39], s82 offen offset:1024 lds\n" \
"v_mfma_f32_16x16x32_bf16 v[88:91], a[116:119], a[76:79], v[88:91]\n" \
"ds_read_b128 a[72:75], v213 offset:256\n" \
"v_cvt_pk_bf16_f32 v8, v16, v17\n" \
"v_mfma_f32_16x16x32_bf16 v[104:107], a[112:115], a[80:83], 0\n" \
"ds_read_b128 a[76:79], v213 offset:2304\n" \
"v_cvt_pk_bf16_f32 v9, v18, v19\n" \
"v_mfma_f32_16x16x32_bf16 v[104:107], a[116:119], a[84:87], v[104:107]\n" \
"ds_read_b64_tr_b16 a[108:109], v215 offset:24576\n" \
"ds_read_b64_tr_b16 a[110:111], v215 offset:26624\n" \
"v_cvt_pk_bf16_f32 v10, v20, v21\n" \
"v_mfma_f32_16x16x32_bf16 v[108:111], a[120:123], a[80:83], 0\n" \
"buffer_load_dwordx4 v218, s[36:39], s82 offen offset:2048 lds\n" \
"v_mfma_f32_16x16x32_bf16 v[108:111], a[124:127], a[84:87], v[108:111]\n" \
"s_add_u32 s71, s87, 0\n" \
"ds_read_b128 a[80:83], v213 offset:4096\n" \
"v_cvt_pk_bf16_f32 v11, v22, v23\n" \
"v_mfma_f32_16x16x32_bf16 v[124:127], a[120:123], a[88:91], 0\n" \
"s_waitcnt lgkmcnt(4)\n" \
"buffer_store_dwordx4 v[0:3], v234, s[56:59], s71 offen offset:0 sc1\n" \
"v_mfma_f32_16x16x32_bf16 v[124:127], a[124:127], a[92:95], v[124:127]\n" \
"ds_read_b128 a[84:87], v213 offset:6144\n" \
"v_cvt_pk_bf16_f32 v12, v24, v25\n" \
"v_mfma_f32_16x16x32_bf16 v[120:123], a[112:115], a[88:91], 0\n" \
"buffer_load_dwordx4 v219, s[36:39], s82 offen offset:3072 lds\n" \
"v_mfma_f32_16x16x32_bf16 v[120:123], a[116:119], a[92:95], v[120:123]\n" \
"buffer_store_dwordx4 v[4:7], v234, s[56:59], s71 offen offset:1024 sc1\n" \
"buffer_store_dwordx4 v[8:11], v234, s[56:59], s71 offen offset:2048 sc1\n" \
"v_mfma_f32_16x16x32_bf16 v[64:67], a[96:99], a[64:67], v[64:67]\n" \
"s_xor_b32 s82, s48, s49\n" \
"ds_read_b128 a[88:91], v213 offset:4352\n" \
"v_cvt_pk_bf16_f32 v13, v26, v27\n" \
"v_mfma_f32_16x16x32_bf16 v[64:67], a[100:103], a[68:71], v[64:67]\n" \
"s_waitcnt lgkmcnt(2)\n" \
"s_cmp_lt_u32 s48, s50\n" \
"s_cselect_b32 s82, s82, s48\n" \
"ds_read_b128 a[92:95], v213 offset:6400\n" \
"v_mfma_f32_16x16x32_bf16 v[68:71], a[104:107], a[64:67], v[68:71]\n" \
"s_add_u32 s81, s82, s46\n" \
"ds_read_b64_tr_b16 a[112:113], v214 offset:16896\n" \
"v_cvt_pk_bf16_f32 v14, v28, v29\n" \
"v_mfma_f32_16x16x32_bf16 v[68:71], a[108:111], a[68:71], v[68:71]\n" \
"s_add_u32 m0, s86, s79\n" \
"ds_read_b64_tr_b16 a[114:115], v214 offset:18944\n" \
"v_cvt_pk_bf16_f32 v15, v30, v31\n" \
"v_mfma_f32_16x16x32_bf16 v[84:87], a[104:107], a[72:75], v[84:87]\n" \
"buffer_load_dwordx4 v234, s[56:59], s81 offen offset:0 sc1 lds\n" \
"v_mfma_f32_16x16x32_bf16 v[84:87], a[108:111], a[76:79], v[84:87]\n" \
"s_waitcnt lgkmcnt(3)\n" \
"ds_read_b64_tr_b16 a[116:117], v214 offset:25088\n" \
"v_cvt_pk_bf16_f32 v16, v32, v33\n" \
"v_mfma_f32_16x16x32_bf16 v[80:83], a[96:99], a[72:75], v[80:83]\n" \
"s_lshl_b32 s82, s82, 5\n" \
"ds_read_b64_tr_b16 a[118:119], v214 offset:27136\n" \
"v_cvt_pk_bf16_f32 v17, v34, v35\n" \
"v_mfma_f32_16x16x32_bf16 v[80:83], a[100:103], a[76:79], v[80:83]\n" \
"s_add_u32 s82, s82, s47\n" \
"ds_read_b64_tr_b16 a[120:121], v215 offset:16896\n" \
"v_cvt_pk_bf16_f32 v18, v36, v37\n" \
"v_mfma_f32_16x16x32_bf16 v[96:99], a[96:99], a[80:83], v[96:99]\n" \
"buffer_load_dwordx4 v234, s[56:59], s81 offen offset:1024 sc1 lds\n" \
"v_mfma_f32_16x16x32_bf16 v[96:99], a[100:103], a[84:87], v[96:99]\n" \
"s_sub_u32 s85, s85, 32768\n" \
"s_cselect_b32 s85, 131072, s85\n" \
"ds_read_b64_tr_b16 a[122:123], v215 offset:18944\n" \
"v_mfma_f32_16x16x32_bf16 v[100:103], a[104:107], a[80:83], v[100:103]\n" \
"s_add_u32 s48, s48, 0x1000\n" \
"ds_read_b64_tr_b16 a[124:125], v215 offset:25088\n" \
"v_add_u32 v213, s85, v211\n" \
"v_mfma_f32_16x16x32_bf16 v[100:103], a[108:111], a[84:87], v[100:103]\n" \
"s_waitcnt lgkmcnt(3)\n" \
"s_sub_u32 s86, s86, 32768\n" \
"s_cselect_b32 s86, 131072, s86\n" \
"ds_read_b64_tr_b16 a[126:127], v215 offset:27136\n" \
"v_mfma_f32_16x16x32_bf16 v[116:119], a[104:107], a[88:91], v[116:119]\n" \
"buffer_load_dwordx4 v234, s[56:59], s81 offen offset:2048 sc1 lds\n" \
"v_mfma_f32_16x16x32_bf16 v[116:119], a[108:111], a[92:95], v[116:119]\n" \
"v_add_u32 v214, s85, v212\n" \
"v_xor_b32 v215, 32, v214\n" \
"v_mfma_f32_16x16x32_bf16 v[112:115], a[96:99], a[88:91], v[112:115]\n" \
"buffer_load_dwordx4 v234, s[56:59], s81 offen offset:3072 sc1 lds\n" \
"v_mfma_f32_16x16x32_bf16 v[112:115], a[100:103], a[92:95], v[112:115]\n" \
"s_waitcnt vmcnt(23) lgkmcnt(0)\n" \
"s_barrier\n" \
"v_mfma_f32_16x16x32_bf16 v[72:75], a[112:115], a[64:67], v[72:75]\n" \
"ds_read_b64_tr_b16 a[96:97], v214 offset:16384\n" \
"ds_read_b64_tr_b16 a[98:99], v214 offset:18432\n" \
"v_mfma_f32_16x16x32_bf16 v[72:75], a[116:119], a[68:71], v[72:75]\n" \
"s_add_u32 m0, m0, 0x4000\n" \
"ds_read_b64_tr_b16 a[100:101], v214 offset:24576\n" \
"ds_read_b64_tr_b16 a[102:103], v214 offset:26624\n" \
"v_mfma_f32_16x16x32_bf16 v[76:79], a[120:123], a[64:67], v[76:79]\n" \
"buffer_load_dwordx4 v216, s[36:39], s82 offen offset:0 lds\n" \
"v_mfma_f32_16x16x32_bf16 v[76:79], a[124:127], a[68:71], v[76:79]\n" \
"ds_read_b128 a[64:67], v213 offset:0\n" \
"v_cvt_pk_bf16_f32 v19, v38, v39\n" \
"v_mfma_f32_16x16x32_bf16 v[92:95], a[120:123], a[72:75], v[92:95]\n" \
"ds_read_b128 a[68:71], v213 offset:2048\n" \
"v_cvt_pk_bf16_f32 v20, v40, v41\n" \
"v_mfma_f32_16x16x32_bf16 v[92:95], a[124:127], a[76:79], v[92:95]\n" \
"ds_read_b64_tr_b16 a[104:105], v215 offset:16384\n" \
"ds_read_b64_tr_b16 a[106:107], v215 offset:18432\n" \
"v_mfma_f32_16x16x32_bf16 v[88:91], a[112:115], a[72:75], v[88:91]\n" \
"buffer_load_dwordx4 v217, s[36:39], s82 offen offset:1024 lds\n" \
"v_mfma_f32_16x16x32_bf16 v[88:91], a[116:119], a[76:79], v[88:91]\n" \
"ds_read_b128 a[72:75], v213 offset:256\n" \
"v_cvt_pk_bf16_f32 v21, v42, v43\n" \
"v_mfma_f32_16x16x32_bf16 v[104:107], a[112:115], a[80:83], v[104:107]\n" \
"ds_read_b128 a[76:79], v213 offset:2304\n" \
"v_cvt_pk_bf16_f32 v22, v44, v45\n" \
"v_mfma_f32_16x16x32_bf16 v[104:107], a[116:119], a[84:87], v[104:107]\n" \
"ds_read_b64_tr_b16 a[108:109], v215 offset:24576\n" \
"ds_read_b64_tr_b16 a[110:111], v215 offset:26624\n" \
"v_mfma_f32_16x16x32_bf16 v[108:111], a[120:123], a[80:83], v[108:111]\n" \
"buffer_load_dwordx4 v218, s[36:39], s82 offen offset:2048 lds\n" \
"v_mfma_f32_16x16x32_bf16 v[108:111], a[124:127], a[84:87], v[108:111]\n" \
"ds_read_b128 a[80:83], v213 offset:4096\n" \
"v_cvt_pk_bf16_f32 v23, v46, v47\n" \
"v_mfma_f32_16x16x32_bf16 v[124:127], a[120:123], a[88:91], v[124:127]\n" \
"s_waitcnt lgkmcnt(4)\n" \
"buffer_store_dwordx4 v[12:15], v234, s[56:59], s71 offen offset:3072 sc1\n" \
"v_mfma_f32_16x16x32_bf16 v[124:127], a[124:127], a[92:95], v[124:127]\n" \
"s_add_u32 s71, s87, 4096\n" \
"ds_read_b128 a[84:87], v213 offset:6144\n" \
"v_cvt_pk_bf16_f32 v24, v48, v49\n" \
"v_mfma_f32_16x16x32_bf16 v[120:123], a[112:115], a[88:91], v[120:123]\n" \
"buffer_load_dwordx4 v219, s[36:39], s82 offen offset:3072 lds\n" \
"v_mfma_f32_16x16x32_bf16 v[120:123], a[116:119], a[92:95], v[120:123]\n" \
"buffer_store_dwordx4 v[16:19], v234, s[56:59], s71 offen offset:0 sc1\n" \
"buffer_store_dwordx4 v[20:23], v234, s[56:59], s71 offen offset:1024 sc1\n" \
"v_mfma_f32_16x16x32_bf16 v[64:67], a[96:99], a[64:67], v[64:67]\n" \
"s_xor_b32 s82, s48, s49\n" \
"ds_read_b128 a[88:91], v213 offset:4352\n" \
"v_cvt_pk_bf16_f32 v25, v50, v51\n" \
"v_mfma_f32_16x16x32_bf16 v[64:67], a[100:103], a[68:71], v[64:67]\n" \
"s_waitcnt lgkmcnt(2)\n" \
"s_cmp_lt_u32 s48, s50\n" \
"s_cselect_b32 s82, s82, s48\n" \
"ds_read_b128 a[92:95], v213 offset:6400\n" \
"v_mfma_f32_16x16x32_bf16 v[68:71], a[104:107], a[64:67], v[68:71]\n" \
"s_add_u32 s81, s82, s46\n" \
"ds_read_b64_tr_b16 a[112:113], v214 offset:16896\n" \
"v_cvt_pk_bf16_f32 v26, v52, v53\n" \
"v_mfma_f32_16x16x32_bf16 v[68:71], a[108:111], a[68:71], v[68:71]\n" \
"s_add_u32 m0, s86, s79\n" \
"ds_read_b64_tr_b16 a[114:115], v214 offset:18944\n" \
"v_cvt_pk_bf16_f32 v27, v54, v55\n" \
"v_mfma_f32_16x16x32_bf16 v[84:87], a[104:107], a[72:75], v[84:87]\n" \
"buffer_load_dwordx4 v234, s[56:59], s81 offen offset:0 sc1 lds\n" \
"v_mfma_f32_16x16x32_bf16 v[84:87], a[108:111], a[76:79], v[84:87]\n" \
"s_waitcnt lgkmcnt(3)\n" \
"ds_read_b64_tr_b16 a[116:117], v214 offset:25088\n" \
"v_cvt_pk_bf16_f32 v28, v56, v57\n" \
"v_mfma_f32_16x16x32_bf16 v[80:83], a[96:99], a[72:75], v[80:83]\n" \
"s_lshl_b32 s82, s82, 5\n" \
"ds_read_b64_tr_b16 a[118:119], v214 offset:27136\n" \
"v_cvt_pk_bf16_f32 v29, v58, v59\n" \
"v_mfma_f32_16x16x32_bf16 v[80:83], a[100:103], a[76:79], v[80:83]\n" \
"s_add_u32 s82, s82, s47\n" \
"ds_read_b64_tr_b16 a[120:121], v215 offset:16896\n" \
"v_cvt_pk_bf16_f32 v30, v60, v61\n" \
"v_mfma_f32_16x16x32_bf16 v[96:99], a[96:99], a[80:83], v[96:99]\n" \
"buffer_load_dwordx4 v234, s[56:59], s81 offen offset:1024 sc1 lds\n" \
"v_mfma_f32_16x16x32_bf16 v[96:99], a[100:103], a[84:87], v[96:99]\n" \
"s_sub_u32 s85, s85, 32768\n" \
"s_cselect_b32 s85, 131072, s85\n" \
"ds_read_b64_tr_b16 a[122:123], v215 offset:18944\n" \
"v_mfma_f32_16x16x32_bf16 v[100:103], a[104:107], a[80:83], v[100:103]\n" \
"s_add_u32 s48, s48, 0x1000\n" \
"ds_read_b64_tr_b16 a[124:125], v215 offset:25088\n" \
"v_add_u32 v213, s85, v211\n" \
"v_mfma_f32_16x16x32_bf16 v[100:103], a[108:111], a[84:87], v[100:103]\n" \
"s_waitcnt lgkmcnt(3)\n" \
"s_sub_u32 s86, s86, 32768\n" \
"s_cselect_b32 s86, 131072, s86\n" \
"ds_read_b64_tr_b16 a[126:127], v215 offset:27136\n" \
"v_mfma_f32_16x16x32_bf16 v[116:119], a[104:107], a[88:91], v[116:119]\n" \
"buffer_load_dwordx4 v234, s[56:59], s81 offen offset:2048 sc1 lds\n" \
"v_mfma_f32_16x16x32_bf16 v[116:119], a[108:111], a[92:95], v[116:119]\n" \
"v_add_u32 v214, s85, v212\n" \
"v_xor_b32 v215, 32, v214\n" \
"v_mfma_f32_16x16x32_bf16 v[112:115], a[96:99], a[88:91], v[112:115]\n" \
"buffer_load_dwordx4 v234, s[56:59], s81 offen offset:3072 sc1 lds\n" \
"v_mfma_f32_16x16x32_bf16 v[112:115], a[100:103], a[92:95], v[112:115]\n" \
"s_waitcnt vmcnt(26) lgkmcnt(0)\n" \
"s_barrier\n" \
"v_mfma_f32_16x16x32_bf16 v[72:75], a[112:115], a[64:67], v[72:75]\n" \
"ds_read_b64_tr_b16 a[96:97], v214 offset:16384\n" \
"ds_read_b64_tr_b16 a[98:99], v214 offset:18432\n" \
"v_mfma_f32_16x16x32_bf16 v[72:75], a[116:119], a[68:71], v[72:75]\n" \
"s_add_u32 m0, m0, 0x4000\n" \
"ds_read_b64_tr_b16 a[100:101], v214 offset:24576\n" \
"ds_read_b64_tr_b16 a[102:103], v214 offset:26624\n" \
"v_mfma_f32_16x16x32_bf16 v[76:79], a[120:123], a[64:67], v[76:79]\n" \
"buffer_load_dwordx4 v216, s[36:39], s82 offen offset:0 lds\n" \
"v_mfma_f32_16x16x32_bf16 v[76:79], a[124:127], a[68:71], v[76:79]\n" \
"ds_read_b128 a[64:67], v213 offset:0\n" \
"v_cvt_pk_bf16_f32 v31, v62, v63\n" \
"v_mfma_f32_16x16x32_bf16 v[92:95], a[120:123], a[72:75], v[92:95]\n" \
"ds_read_b128 a[68:71], v213 offset:2048\n" \
"v_mfma_f32_16x16x32_bf16 v[92:95], a[124:127], a[76:79], v[92:95]\n" \
"ds_read_b64_tr_b16 a[104:105], v215 offset:16384\n" \
"ds_read_b64_tr_b16 a[106:107], v215 offset:18432\n" \
"v_mfma_f32_16x16x32_bf16 v[88:91], a[112:115], a[72:75], v[88:91]\n" \
"buffer_load_dwordx4 v217, s[36:39], s82 offen offset:1024 lds\n" \
"v_mfma_f32_16x16x32_bf16 v[88:91], a[116:119], a[76:79], v[88:91]\n" \
"ds_read_b128 a[72:75], v213 offset:256\n" \
"v_mfma_f32_16x16x32_bf16 v[104:107], a[112:115], a[80:83], v[104:107]\n" \
"ds_read_b128 a[76:79], v213 offset:2304\n" \
"v_mfma_f32_16x16x32_bf16 v[104:107], a[116:119], a[84:87], v[104:107]\n" \
"ds_read_b64_tr_b16 a[108:109], v215 offset:24576\n" \
"ds_read_b64_tr_b16 a[110:111], v215 offset:26624\n" \
"v_mfma_f32_16x16x32_bf16 v[108:111], a[120:123], a[80:83], v[108:111]\n" \
"buffer_load_dwordx4 v218, s[36:39], s82 offen offset:2048 lds\n" \
"v_mfma_f32_16x16x32_bf16 v[108:111], a[124:127], a[84:87], v[108:111]\n" \
"ds_read_b128 a[80:83], v213 offset:4096\n" \
"v_mfma_f32_16x16x32_bf16 v[124:127], a[120:123], a[88:91], v[124:127]\n" \
"s_waitcnt lgkmcnt(4)\n" \
"buffer_store_dwordx4 v[24:27], v234, s[56:59], s71 offen offset:2048 sc1\n" \
"v_mfma_f32_16x16x32_bf16 v[124:127], a[124:127], a[92:95], v[124:127]\n" \
"ds_read_b128 a[84:87], v213 offset:6144\n" \
"v_mfma_f32_16x16x32_bf16 v[120:123], a[112:115], a[88:91], v[120:123]\n" \
"buffer_load_dwordx4 v219, s[36:39], s82 offen offset:3072 lds\n" \
"v_mfma_f32_16x16x32_bf16 v[120:123], a[116:119], a[92:95], v[120:123]\n" \
"buffer_store_dwordx4 v[28:31], v234, s[56:59], s71 offen offset:3072 sc1\n" \
"v_mfma_f32_16x16x32_bf16 v[64:67], a[96:99], a[64:67], v[64:67]\n" \
"s_waitcnt lgkmcnt(1)\n" \
"s_xor_b32 s82, s48, s49\n" \
"ds_read_b128 a[88:91], v213 offset:4352\n" \
"v_mfma_f32_16x16x32_bf16 v[64:67], a[100:103], a[68:71], v[64:67]\n" \
"s_cmp_lt_u32 s48, s50\n" \
"s_cselect_b32 s82, s82, s48\n" \
"ds_read_b128 a[92:95], v213 offset:6400\n" \
"v_mfma_f32_16x16x32_bf16 v[68:71], a[104:107], a[64:67], v[68:71]\n" \
"s_add_u32 s81, s82, s46\n" \
"ds_read_b64_tr_b16 a[112:113], v214 offset:16896\n" \
"v_mfma_f32_16x16x32_bf16 v[68:71], a[108:111], a[68:71], v[68:71]\n" \
"s_add_u32 m0, s86, s79\n" \
"ds_read_b64_tr_b16 a[114:115], v214 offset:18944\n" \
"v_mfma_f32_16x16x32_bf16 v[84:87], a[104:107], a[72:75], v[84:87]\n" \
"buffer_load_dwordx4 v234, s[56:59], s81 offen offset:0 sc1 lds\n" \
"v_mfma_f32_16x16x32_bf16 v[84:87], a[108:111], a[76:79], v[84:87]\n" \
"ds_read_b64_tr_b16 a[116:117], v214 offset:25088\n" \
"v_mfma_f32_16x16x32_bf16 v[80:83], a[96:99], a[72:75], v[80:83]\n" \
"s_lshl_b32 s82, s82, 5\n" \
"ds_read_b64_tr_b16 a[118:119], v214 offset:27136\n" \
"v_mfma_f32_16x16x32_bf16 v[80:83], a[100:103], a[76:79], v[80:83]\n" \
"s_waitcnt lgkmcnt(3)\n" \
"s_add_u32 s82, s82, s47\n" \
"ds_read_b64_tr_b16 a[120:121], v215 offset:16896\n" \
"v_mfma_f32_16x16x32_bf16 v[96:99], a[96:99], a[80:83], v[96:99]\n" \
"buffer_load_dwordx4 v234, s[56:59], s81 offen offset:1024 sc1 lds\n" \
"v_mfma_f32_16x16x32_bf16 v[96:99], a[100:103], a[84:87], v[96:99]\n" \
"s_sub_u32 s85, s85, 32768\n" \
"s_cselect_b32 s85, 131072, s85\n" \
"ds_read_b64_tr_b16 a[122:123], v215 offset:18944\n" \
"v_mfma_f32_16x16x32_bf16 v[100:103], a[104:107], a[80:83], v[100:103]\n" \
"s_add_u32 s48, s48, 0x1000\n" \
"ds_read_b64_tr_b16 a[124:125], v215 offset:25088\n" \
"v_add_u32 v213, s85, v211\n" \
"v_mfma_f32_16x16x32_bf16 v[100:103], a[108:111], a[84:87], v[100:103]\n" \
"s_sub_u32 s86, s86, 32768\n" \
"s_cselect_b32 s86, 131072, s86\n" \
"ds_read_b64_tr_b16 a[126:127], v215 offset:27136\n" \
"v_mfma_f32_16x16x32_bf16 v[116:119], a[104:107], a[88:91], v[116:119]\n" \
"buffer_load_dwordx4 v234, s[56:59], s81 offen offset:2048 sc1 lds\n" \
"v_mfma_f32_16x16x32_bf16 v[116:119], a[108:111], a[92:95], v[116:119]\n" \
"v_add_u32 v214, s85, v212\n" \
"v_xor_b32 v215, 32, v214\n" \
"v_mfma_f32_16x16x32_bf16 v[112:115], a[96:99], a[88:91], v[112:115]\n" \
"buffer_load_dwordx4 v234, s[56:59], s81 offen offset:3072 sc1 lds\n" \
"v_mfma_f32_16x16x32_bf16 v[112:115], a[100:103], a[92:95], v[112:115]\n" \
"s_waitcnt vmcnt(27) lgkmcnt(0)\n" \
"s_barrier\n" \
"v_mfma_f32_16x16x32_bf16 v[72:75], a[112:115], a[64:67], v[72:75]\n" \
"ds_read_b64_tr_b16 a[96:97], v214 offset:16384\n" \
"ds_read_b64_tr_b16 a[98:99], v214 offset:18432\n" \
"v_mfma_f32_16x16x32_bf16 v[72:75], a[116:119], a[68:71], v[72:75]\n" \
"s_add_u32 m0, m0, 0x4000\n" \
"ds_read_b64_tr_b16 a[100:101], v214 offset:24576\n" \
"ds_read_b64_tr_b16 a[102:103], v214 offset:26624\n" \
"v_mfma_f32_16x16x32_bf16 v[76:79], a[120:123], a[64:67], v[76:79]\n" \
"buffer_load_dwordx4 v216, s[36:39], s82 offen offset:0 lds\n" \
"v_mfma_f32_16x16x32_bf16 v[76:79], a[124:127], a[68:71], v[76:79]\n" \
"ds_read_b128 a[64:67], v213 offset:0\n" \
"v_mfma_f32_16x16x32_bf16 v[92:95], a[120:123], a[72:75], v[92:95]\n" \
"ds_read_b128 a[68:71], v213 offset:2048\n" \
"v_mfma_f32_16x16x32_bf16 v[92:95], a[124:127], a[76:79], v[92:95]\n" \
"ds_read_b64_tr_b16 a[104:105], v215 offset:16384\n" \
"ds_read_b64_tr_b16 a[106:107], v215 offset:18432\n" \
"v_mfma_f32_16x16x32_bf16 v[88:91], a[112:115], a[72:75], v[88:91]\n" \
"buffer_load_dwordx4 v217, s[36:39], s82 offen offset:1024 lds\n" \
"v_mfma_f32_16x16x32_bf16 v[88:91], a[116:119], a[76:79], v[88:91]\n" \
"ds_read_b128 a[72:75], v213 offset:256\n" \
"v_mfma_f32_16x16x32_bf16 v[104:107], a[112:115], a[80:83], v[104:107]\n" \
"ds_read_b128 a[76:79], v213 offset:2304\n" \
"v_mfma_f32_16x16x32_bf16 v[104:107], a[116:119], a[84:87], v[104:107]\n" \
"ds_read_b64_tr_b16 a[108:109], v215 offset:24576\n" \
"ds_read_b64_tr_b16 a[110:111], v215 offset:26624\n" \
"v_mfma_f32_16x16x32_bf16 v[108:111], a[120:123], a[80:83], v[108:111]\n" \
"buffer_load_dwordx4 v218, s[36:39], s82 offen offset:2048 lds\n" \
"v_mfma_f32_16x16x32_bf16 v[108:111], a[124:127], a[84:87], v[108:111]\n" \
"ds_read_b128 a[80:83], v213 offset:4096\n" \
"v_mfma_f32_16x16x32_bf16 v[124:127], a[120:123], a[88:91], v[124:127]\n" \
"s_waitcnt lgkmcnt(4)\n" \
"v_mfma_f32_16x16x32_bf16 v[124:127], a[124:127], a[92:95], v[124:127]\n" \
"ds_read_b128 a[84:87], v213 offset:6144\n" \
"v_mfma_f32_16x16x32_bf16 v[120:123], a[112:115], a[88:91], v[120:123]\n" \
"buffer_load_dwordx4 v219, s[36:39], s82 offen offset:3072 lds\n" \
"v_mfma_f32_16x16x32_bf16 v[120:123], a[116:119], a[92:95], v[120:123]\n" \
"v_mfma_f32_16x16x32_bf16 v[64:67], a[96:99], a[64:67], v[64:67]\n" \
"s_waitcnt lgkmcnt(1)\n" \
"s_xor_b32 s82, s48, s49\n" \
"ds_read_b128 a[88:91], v213 offset:4352\n" \
"v_mfma_f32_16x16x32_bf16 v[64:67], a[100:103], a[68:71], v[64:67]\n" \
"s_cmp_lt_u32 s48, s50\n" \
"s_cselect_b32 s82, s82, s48\n" \
"ds_read_b128 a[92:95], v213 offset:6400\n" \
"v_mfma_f32_16x16x32_bf16 v[68:71], a[104:107], a[64:67], v[68:71]\n" \
"s_add_u32 s81, s82, s46\n" \
"ds_read_b64_tr_b16 a[112:113], v214 offset:16896\n" \
"v_mfma_f32_16x16x32_bf16 v[68:71], a[108:111], a[68:71], v[68:71]\n" \
"s_add_u32 m0, s86, s79\n" \
"ds_read_b64_tr_b16 a[114:115], v214 offset:18944\n" \
"v_mfma_f32_16x16x32_bf16 v[84:87], a[104:107], a[72:75], v[84:87]\n" \
"buffer_load_dwordx4 v234, s[56:59], s81 offen offset:0 sc1 lds\n" \
"v_mfma_f32_16x16x32_bf16 v[84:87], a[108:111], a[76:79], v[84:87]\n" \
"ds_read_b64_tr_b16 a[116:117], v214 offset:25088\n" \
"v_mfma_f32_16x16x32_bf16 v[80:83], a[96:99], a[72:75], v[80:83]\n" \
"s_lshl_b32 s82, s82, 5\n" \
"ds_read_b64_tr_b16 a[118:119], v214 offset:27136\n" \
"v_mfma_f32_16x16x32_bf16 v[80:83], a[100:103], a[76:79], v[80:83]\n" \
"s_waitcnt lgkmcnt(3)\n" \
"s_add_u32 s82, s82, s47\n" \
"ds_read_b64_tr_b16 a[120:121], v215 offset:16896\n" \
"v_mfma_f32_16x16x32_bf16 v[96:99], a[96:99], a[80:83], v[96:99]\n" \
"buffer_load_dwordx4 v234, s[56:59], s81 offen offset:1024 sc1 lds\n" \
"v_mfma_f32_16x16x32_bf16 v[96:99], a[100:103], a[84:87], v[96:99]\n" \
"s_sub_u32 s85, s85, 32768\n" \
"s_cselect_b32 s85, 131072, s85\n" \
"ds_read_b64_tr_b16 a[122:123], v215 offset:18944\n" \
"v_mfma_f32_16x16x32_bf16 v[100:103], a[104:107], a[80:83], v[100:103]\n" \
"s_add_u32 s48, s48, 0x1000\n" \
"ds_read_b64_tr_b16 a[124:125], v215 offset:25088\n" \
"v_add_u32 v213, s85, v211\n" \
"v_mfma_f32_16x16x32_bf16 v[100:103], a[108:111], a[84:87], v[100:103]\n" \
"s_sub_u32 s86, s86, 32768\n" \
"s_cselect_b32 s86, 131072, s86\n" \
"ds_read_b64_tr_b16 a[126:127], v215 offset:27136\n" \
"v_mfma_f32_16x16x32_bf16 v[116:119], a[104:107], a[88:91], v[116:119]\n" \
"buffer_load_dwordx4 v234, s[56:59], s81 offen offset:2048 sc1 lds\n" \
"v_mfma_f32_16x16x32_bf16 v[116:119], a[108:111], a[92:95], v[116:119]\n" \
"v_add_u32 v214, s85, v212\n" \
"v_xor_b32 v215, 32, v214\n" \
"v_mfma_f32_16x16x32_bf16 v[112:115], a[96:99], a[88:91], v[112:115]\n" \
"buffer_load_dwordx4 v234, s[56:59], s81 offen offset:3072 sc1 lds\n" \
"v_mfma_f32_16x16x32_bf16 v[112:115], a[100:103], a[92:95], v[112:115]\n" \
"s_waitcnt vmcnt(24) lgkmcnt(0)\n" \
"s_barrier\n" \
"v_mfma_f32_16x16x32_bf16 v[72:75], a[112:115], a[64:67], v[72:75]\n" \
"ds_read_b64_tr_b16 a[96:97], v214 offset:16384\n" \
"ds_read_b64_tr_b16 a[98:99], v214 offset:18432\n" \
"v_mfma_f32_16x16x32_bf16 v[72:75], a[116:119], a[68:71], v[72:75]\n" \
"s_add_u32 m0, m0, 0x4000\n" \
"ds_read_b64_tr_b16 a[100:101], v214 offset:24576\n" \
"ds_read_b64_tr_b16 a[102:103], v214 offset:26624\n" \
"v_mfma_f32_16x16x32_bf16 v[76:79], a[120:123], a[64:67], v[76:79]\n" \
"buffer_load_dwordx4 v216, s[36:39], s82 offen offset:0 lds\n" \
"v_mfma_f32_16x16x32_bf16 v[76:79], a[124:127], a[68:71], v[76:79]\n" \
"ds_read_b128 a[64:67], v213 offset:0\n" \
"v_mfma_f32_16x16x32_bf16 v[92:95], a[120:123], a[72:75], v[92:95]\n" \
"ds_read_b128 a[68:71], v213 offset:2048\n" \
"v_mfma_f32_16x16x32_bf16 v[92:95], a[124:127], a[76:79], v[92:95]\n" \
"ds_read_b64_tr_b16 a[104:105], v215 offset:16384\n" \
"ds_read_b64_tr_b16 a[106:107], v215 offset:18432\n" \
"v_mfma_f32_16x16x32_bf16 v[88:91], a[112:115], a[72:75], v[88:91]\n" \
"buffer_load_dwordx4 v217, s[36:39], s82 offen offset:1024 lds\n" \
"v_mfma_f32_16x16x32_bf16 v[88:91], a[116:119], a[76:79], v[88:91]\n" \
"ds_read_b128 a[72:75], v213 offset:256\n" \
"v_mfma_f32_16x16x32_bf16 v[104:107], a[112:115], a[80:83], v[104:107]\n" \
"ds_read_b128 a[76:79], v213 offset:2304\n" \
"v_mfma_f32_16x16x32_bf16 v[104:107], a[116:119], a[84:87], v[104:107]\n" \
"ds_read_b64_tr_b16 a[108:109], v215 offset:24576\n" \
"ds_read_b64_tr_b16 a[110:111], v215 offset:26624\n" \
"v_mfma_f32_16x16x32_bf16 v[108:111], a[120:123], a[80:83], v[108:111]\n" \
"buffer_load_dwordx4 v218, s[36:39], s82 offen offset:2048 lds\n" \
"v_mfma_f32_16x16x32_bf16 v[108:111], a[124:127], a[84:87], v[108:111]\n" \
"ds_read_b128 a[80:83], v213 offset:4096\n" \
"v_mfma_f32_16x16x32_bf16 v[124:127], a[120:123], a[88:91], v[124:127]\n" \
"s_waitcnt lgkmcnt(4)\n" \
"v_mfma_f32_16x16x32_bf16 v[124:127], a[124:127], a[92:95], v[124:127]\n" \
"ds_read_b128 a[84:87], v213 offset:6144\n" \
"v_mfma_f32_16x16x32_bf16 v[120:123], a[112:115], a[88:91], v[120:123]\n" \
"buffer_load_dwordx4 v219, s[36:39], s82 offen offset:3072 lds\n" \
"v_mfma_f32_16x16x32_bf16 v[120:123], a[116:119], a[92:95], v[120:123]\n" \
"v_mfma_f32_16x16x32_bf16 v[64:67], a[96:99], a[64:67], v[64:67]\n" \
"s_waitcnt lgkmcnt(1)\n" \
"s_xor_b32 s82, s48, s49\n" \
"ds_read_b128 a[88:91], v213 offset:4352\n" \
"v_mfma_f32_16x16x32_bf16 v[64:67], a[100:103], a[68:71], v[64:67]\n" \
"s_cmp_lt_u32 s48, s50\n" \
"s_cselect_b32 s82, s82, s48\n" \
"ds_read_b128 a[92:95], v213 offset:6400\n" \
"v_mfma_f32_16x16x32_bf16 v[68:71], a[104:107], a[64:67], v[68:71]\n" \
"s_add_u32 s81, s82, s46\n" \
"ds_read_b64_tr_b16 a[112:113], v214 offset:16896\n" \
"v_mfma_f32_16x16x32_bf16 v[68:71], a[108:111], a[68:71], v[68:71]\n" \
"s_add_u32 m0, s86, s79\n" \
"ds_read_b64_tr_b16 a[114:115], v214 offset:18944\n" \
"v_mfma_f32_16x16x32_bf16 v[84:87], a[104:107], a[72:75], v[84:87]\n" \
"buffer_load_dwordx4 v234, s[56:59], s81 offen offset:0 sc1 lds\n" \
"v_mfma_f32_16x16x32_bf16 v[84:87], a[108:111], a[76:79], v[84:87]\n" \
"ds_read_b64_tr_b16 a[116:117], v214 offset:25088\n" \
"v_mfma_f32_16x16x32_bf16 v[80:83], a[96:99], a[72:75], v[80:83]\n" \
"s_lshl_b32 s82, s82, 5\n" \
"ds_read_b64_tr_b16 a[118:119], v214 offset:27136\n" \
"v_mfma_f32_16x16x32_bf16 v[80:83], a[100:103], a[76:79], v[80:83]\n" \
"s_waitcnt lgkmcnt(3)\n" \
"s_add_u32 s82, s82, s47\n" \
"ds_read_b64_tr_b16 a[120:121], v215 offset:16896\n" \
"v_mfma_f32_16x16x32_bf16 v[96:99], a[96:99], a[80:83], v[96:99]\n" \
"buffer_load_dwordx4 v234, s[56:59], s81 offen offset:1024 sc1 lds\n" \
"v_mfma_f32_16x16x32_bf16 v[96:99], a[100:103], a[84:87], v[96:99]\n" \
"s_sub_u32 s85, s85, 32768\n" \
"s_cselect_b32 s85, 131072, s85\n" \
"ds_read_b64_tr_b16 a[122:123], v215 offset:18944\n" \
"v_mfma_f32_16x16x32_bf16 v[100:103], a[104:107], a[80:83], v[100:103]\n" \
"s_add_u32 s48, s48, 0x1000\n" \
"ds_read_b64_tr_b16 a[124:125], v215 offset:25088\n" \
"v_add_u32 v213, s85, v211\n" \
"v_mfma_f32_16x16x32_bf16 v[100:103], a[108:111], a[84:87], v[100:103]\n" \
"s_sub_u32 s86, s86, 32768\n" \
"s_cselect_b32 s86, 131072, s86\n" \
"ds_read_b64_tr_b16 a[126:127], v215 offset:27136\n" \
"v_mfma_f32_16x16x32_bf16 v[116:119], a[104:107], a[88:91], v[116:119]\n" \
"buffer_load_dwordx4 v234, s[56:59], s81 offen offset:2048 sc1 lds\n" \
"v_mfma_f32_16x16x32_bf16 v[116:119], a[108:111], a[92:95], v[116:119]\n" \
"v_add_u32 v214, s85, v212\n" \
"v_xor_b32 v215, 32, v214\n" \
"v_mfma_f32_16x16x32_bf16 v[112:115], a[96:99], a[88:91], v[112:115]\n" \
"buffer_load_dwordx4 v234, s[56:59], s81 offen offset:3072 sc1 lds\n" \
"v_mfma_f32_16x16x32_bf16 v[112:115], a[100:103], a[92:95], v[112:115]\n" \
"s_waitcnt vmcnt(20) lgkmcnt(0)\n" \
"s_barrier\n" \
"buffer_store_dword v230, v231, s[56:59], s64 offen sc1\n" \
"v_mfma_f32_16x16x32_bf16 v[72:75], a[112:115], a[64:67], v[72:75]\n" \
"ds_read_b64_tr_b16 a[96:97], v214 offset:16384\n" \
"ds_read_b64_tr_b16 a[98:99], v214 offset:18432\n" \
"v_mfma_f32_16x16x32_bf16 v[72:75], a[116:119], a[68:71], v[72:75]\n" \
"s_add_u32 m0, m0, 0x4000\n" \
"ds_read_b64_tr_b16 a[100:101], v214 offset:24576\n" \
"ds_read_b64_tr_b16 a[102:103], v214 offset:26624\n" \
"v_mfma_f32_16x16x32_bf16 v[76:79], a[120:123], a[64:67], v[76:79]\n" \
"buffer_load_dwordx4 v216, s[36:39], s82 offen offset:0 lds\n" \
"v_mfma_f32_16x16x32_bf16 v[76:79], a[124:127], a[68:71], v[76:79]\n" \
"ds_read_b128 a[64:67], v213 offset:0\n" \
"v_mfma_f32_16x16x32_bf16 v[92:95], a[120:123], a[72:75], v[92:95]\n" \
"ds_read_b128 a[68:71], v213 offset:2048\n" \
"v_mfma_f32_16x16x32_bf16 v[92:95], a[124:127], a[76:79], v[92:95]\n" \
"ds_read_b64_tr_b16 a[104:105], v215 offset:16384\n" \
"ds_read_b64_tr_b16 a[106:107], v215 offset:18432\n" \
"v_mfma_f32_16x16x32_bf16 v[88:91], a[112:115], a[72:75], v[88:91]\n" \
"buffer_load_dwordx4 v217, s[36:39], s82 offen offset:1024 lds\n" \
"v_mfma_f32_16x16x32_bf16 v[88:91], a[116:119], a[76:79], v[88:91]\n" \
"ds_read_b128 a[72:75], v213 offset:256\n" \
"v_mfma_f32_16x16x32_bf16 v[104:107], a[112:115], a[80:83], v[104:107]\n" \
"ds_read_b128 a[76:79], v213 offset:2304\n" \
"v_mfma_f32_16x16x32_bf16 v[104:107], a[116:119], a[84:87], v[104:107]\n" \
"ds_read_b64_tr_b16 a[108:109], v215 offset:24576\n" \
"ds_read_b64_tr_b16 a[110:111], v215 offset:26624\n" \
"v_mfma_f32_16x16x32_bf16 v[108:111], a[120:123], a[80:83], v[108:111]\n" \
"buffer_load_dwordx4 v218, s[36:39], s82 offen offset:2048 lds\n" \
"v_mfma_f32_16x16x32_bf16 v[108:111], a[124:127], a[84:87], v[108:111]\n" \
"ds_read_b128 a[80:83], v213 offset:4096\n" \
"v_mfma_f32_16x16x32_bf16 v[124:127], a[120:123], a[88:91], v[124:127]\n" \
"s_waitcnt lgkmcnt(4)\n" \
"v_mfma_f32_16x16x32_bf16 v[124:127], a[124:127], a[92:95], v[124:127]\n" \
"ds_read_b128 a[84:87], v213 offset:6144\n" \
"v_mfma_f32_16x16x32_bf16 v[120:123], a[112:115], a[88:91], v[120:123]\n" \
"buffer_load_dwordx4 v219, s[36:39], s82 offen offset:3072 lds\n" \
"v_mfma_f32_16x16x32_bf16 v[120:123], a[116:119], a[92:95], v[120:123]\n" \
"s_mov_b32 s72, 5\n" \
"Lo64_%=:\n" \
"v_mfma_f32_16x16x32_bf16 v[64:67], a[96:99], a[64:67], v[64:67]\n" \
"s_waitcnt lgkmcnt(1)\n" \
"s_xor_b32 s82, s48, s49\n" \
"ds_read_b128 a[88:91], v213 offset:4352\n" \
"Lpg11745_%=:\n" \
"s_cbranch_execz Lcb1_%=\n" \
"v_mfma_f32_16x16x32_bf16 v[64:67], a[100:103], a[68:71], v[64:67]\n" \
"s_cmp_lt_u32 s48, s50\n" \
"s_cselect_b32 s82, s82, s48\n" \
"ds_read_b128 a[92:95], v213 offset:6400\n" \
"v_mfma_f32_16x16x32_bf16 v[68:71], a[104:107], a[64:67], v[68:71]\n" \
"s_add_u32 s81, s82, s46\n" \
"ds_read_b64_tr_b16 a[112:113], v214 offset:16896\n" \
"v_mfma_f32_16x16x32_bf16 v[68:71], a[108:111], a[68:71], v[68:71]\n" \
"s_add_u32 m0, s86, s79\n" \
"ds_read_b64_tr_b16 a[114:115], v214 offset:18944\n" \
"v_mfma_f32_16x16x32_bf16 v[84:87], a[104:107], a[72:75], v[84:87]\n" \
"buffer_load_dwordx4 v234, s[56:59], s81 offen offset:0 sc1 lds\n" \
"v_mfma_f32_16x16x32_bf16 v[84:87], a[108:111], a[76:79], v[84:87]\n" \
"ds_read_b64_tr_b16 a[116:117], v214 offset:25088\n" \
"v_mfma_f32_16x16x32_bf16 v[80:83], a[96:99], a[72:75], v[80:83]\n" \
"s_lshl_b32 s82, s82, 5\n" \
"ds_read_b64_tr_b16 a[118:119], v214 offset:27136\n" \
"v_mfma_f32_16x16x32_bf16 v[80:83], a[100:103], a[76:79], v[80:83]\n" \
"s_waitcnt lgkmcnt(3)\n" \
"s_add_u32 s82, s82, s47\n" \
"ds_read_b64_tr_b16 a[120:121], v215 offset:16896\n" \
"v_mfma_f32_16x16x32_bf16 v[96:99], a[96:99], a[80:83], v[96:99]\n" \
"buffer_load_dwordx4 v234, s[56:59], s81 offen offset:1024 sc1 lds\n" \
"v_mfma_f32_16x16x32_bf16 v[96:99], a[100:103], a[84:87], v[96:99]\n" \
"s_sub_u32 s85, s85, 32768\n" \
"s_cselect_b32 s85, 131072, s85\n" \
"ds_read_b64_tr_b16 a[122:123], v215 offset:18944\n" \
"v_mfma_f32_16x16x32_bf16 v[100:103], a[104:107], a[80:83], v[100:103]\n" \
"s_add_u32 s48, s48, 0x1000\n" \
"ds_read_b64_tr_b16 a[124:125], v215 offset:25088\n" \
"v_add_u32 v213, s85, v211\n" \
"v_mfma_f32_16x16x32_bf16 v[100:103], a[108:111], a[84:87], v[100:103]\n" \
"s_sub_u32 s86, s86, 32768\n" \
"s_cselect_b32 s86, 131072, s86\n" \
"ds_read_b64_tr_b16 a[126:127], v215 offset:27136\n" \
"v_mfma_f32_16x16x32_bf16 v[116:119], a[104:107], a[88:91], v[116:119]\n" \
"buffer_load_dwordx4 v234, s[56:59], s81 offen offset:2048 sc1 lds\n" \
"v_mfma_f32_16x16x32_bf16 v[116:119], a[108:111], a[92:95], v[116:119]\n" \
"v_add_u32 v214, s85, v212\n" \
"v_xor_b32 v215, 32, v214\n" \
"v_mfma_f32_16x16x32_bf16 v[112:115], a[96:99], a[88:91], v[112:115]\n" \
"buffer_load_dwordx4 v234, s[56:59], s81 offen offset:3072 sc1 lds\n" \
"v_mfma_f32_16x16x32_bf16 v[112:115], a[100:103], a[92:95], v[112:115]\n" \
"s_waitcnt vmcnt(20) lgkmcnt(0)\n" \
"s_barrier\n" \
"v_mfma_f32_16x16x32_bf16 v[72:75], a[112:115], a[64:67], v[72:75]\n" \
"ds_read_b64_tr_b16 a[96:97], v214 offset:16384\n" \
"ds_read_b64_tr_b16 a[98:99], v214 offset:18432\n" \
"v_mfma_f32_16x16x32_bf16 v[72:75], a[116:119], a[68:71], v[72:75]\n" \
"s_add_u32 m0, m0, 0x4000\n" \
"ds_read_b64_tr_b16 a[100:101], v214 offset:24576\n" \
"ds_read_b64_tr_b16 a[102:103], v214 offset:26624\n" \
"v_mfma_f32_16x16x32_bf16 v[76:79], a[120:123], a[64:67], v[76:79]\n" \
"buffer_load_dwordx4 v216, s[36:39], s82 offen offset:0 lds\n" \
"v_mfma_f32_16x16x32_bf16 v[76:79], a[124:127], a[68:71], v[76:79]\n" \
"ds_read_b128 a[64:67], v213 offset:0\n" \
"v_mfma_f32_16x16x32_bf16 v[92:95], a[120:123], a[72:75], v[92:95]\n" \
"ds_read_b128 a[68:71], v213 offset:2048\n" \
"v_mfma_f32_16x16x32_bf16 v[92:95], a[124:127], a[76:79], v[92:95]\n" \
"ds_read_b64_tr_b16 a[104:105], v215 offset:16384\n" \
"ds_read_b64_tr_b16 a[106:107], v215 offset:18432\n" \
"v_mfma_f32_16x16x32_bf16 v[88:91], a[112:115], a[72:75], v[88:91]\n" \
"buffer_load_dwordx4 v217, s[36:39], s82 offen offset:1024 lds\n" \
"v_mfma_f32_16x16x32_bf16 v[88:91], a[116:119], a[76:79], v[88:91]\n" \
"ds_read_b128 a[72:75], v213 offset:256\n" \
"v_mfma_f32_16x16x32_bf16 v[104:107], a[112:115], a[80:83], v[104:107]\n" \
"ds_read_b128 a[76:79], v213 offset:2304\n" \
"v_mfma_f32_16x16x32_bf16 v[104:107], a[116:119], a[84:87], v[104:107]\n" \
"ds_read_b64_tr_b16 a[108:109], v215 offset:24576\n" \
"ds_read_b64_tr_b16 a[110:111], v215 offset:26624\n" \
"v_mfma_f32_16x16x32_bf16 v[108:111], a[120:123], a[80:83], v[108:111]\n" \
"buffer_load_dwordx4 v218, s[36:39], s82 offen offset:2048 lds\n" \
"v_mfma_f32_16x16x32_bf16 v[108:111], a[124:127], a[84:87], v[108:111]\n" \
"ds_read_b128 a[80:83], v213 offset:4096\n" \
"v_mfma_f32_16x16x32_bf16 v[124:127], a[120:123], a[88:91], v[124:127]\n" \
"s_waitcnt lgkmcnt(4)\n" \
"v_mfma_f32_16x16x32_bf16 v[124:127], a[124:127], a[92:95], v[124:127]\n" \
"s_sub_u32 s72, s72, 1\n" \
"ds_read_b128 a[84:87], v213 offset:6144\n" \
"v_mfma_f32_16x16x32_bf16 v[120:123], a[112:115], a[88:91], v[120:123]\n" \
"buffer_load_dwordx4 v219, s[36:39], s82 offen offset:3072 lds\n" \
"v_mfma_f32_16x16x32_bf16 v[120:123], a[116:119], a[92:95], v[120:123]\n" \
"s_cbranch_scc0 Lo64_%=\n" \
"v_mfma_f32_16x16x32_bf16 v[64:67], a[96:99], a[64:67], v[64:67]\n" \
"s_waitcnt lgkmcnt(1)\n" \
"ds_read_b128 a[88:91], v213 offset:4352\n" \
"v_mfma_f32_16x16x32_bf16 v[64:67], a[100:103], a[68:71], v[64:67]\n" \
"s_add_u32 s48, s86, 32768\n" \
"s_cmp_eq_u32 s48, 163840\n" \
"s_cselect_b32 s48, 0, s48\n" \
"ds_read_b128 a[92:95], v213 offset:6400\n" \
"v_mfma_f32_16x16x32_bf16 v[68:71], a[104:107], a[64:67], v[68:71]\n" \
"ds_read_b64_tr_b16 a[112:113], v214 offset:16896\n" \
"v_mfma_f32_16x16x32_bf16 v[68:71], a[108:111], a[68:71], v[68:71]\n" \
"ds_read_b64_tr_b16 a[114:115], v214 offset:18944\n" \
"v_mfma_f32_16x16x32_bf16 v[84:87], a[104:107], a[72:75], v[84:87]\n" \
"s_add_u32 s49, s48, 32768\n" \
"s_cmp_eq_u32 s49, 163840\n" \
"s_cselect_b32 s49, 0, s49\n" \
"v_mfma_f32_16x16x32_bf16 v[84:87], a[108:111], a[76:79], v[84:87]\n" \
"ds_read_b64_tr_b16 a[116:117], v214 offset:25088\n" \
"v_add_u32 v224, s49, v211\n" \
"v_mfma_f32_16x16x32_bf16 v[80:83], a[96:99], a[72:75], v[80:83]\n" \
"ds_read_b64_tr_b16 a[118:119], v214 offset:27136\n" \
"v_add_u32 v225, s49, v212\n" \
"v_mfma_f32_16x16x32_bf16 v[80:83], a[100:103], a[76:79], v[80:83]\n" \
"s_waitcnt lgkmcnt(3)\n" \
"ds_read_b64_tr_b16 a[120:121], v215 offset:16896\n" \
"v_xor_b32 v226, 32, v225\n" \
"v_mfma_f32_16x16x32_bf16 v[96:99], a[96:99], a[80:83], v[96:99]\n" \
"v_add_u32 v227, s48, v211\n" \
"v_add_u32 v228, s48, v212\n" \
"v_mfma_f32_16x16x32_bf16 v[96:99], a[100:103], a[84:87], v[96:99]\n" \
"s_sub_u32 s85, s85, 32768\n" \
"s_cselect_b32 s85, 131072, s85\n" \
"ds_read_b64_tr_b16 a[122:123], v215 offset:18944\n" \
"v_mfma_f32_16x16x32_bf16 v[100:103], a[104:107], a[80:83], v[100:103]\n" \
"ds_read_b64_tr_b16 a[124:125], v215 offset:25088\n" \
"v_add_u32 v213, s85, v211\n" \
"v_mfma_f32_16x16x32_bf16 v[100:103], a[108:111], a[84:87], v[100:103]\n" \
"ds_read_b64_tr_b16 a[126:127], v215 offset:27136\n" \
"v_xor_b32 v229, 32, v228\n" \
"v_mfma_f32_16x16x32_bf16 v[116:119], a[104:107], a[88:91], v[116:119]\n" \
"v_mfma_f32_16x16x32_bf16 v[116:119], a[108:111], a[92:95], v[116:119]\n" \
"v_add_u32 v214, s85, v212\n" \
"v_xor_b32 v215, 32, v214\n" \
"v_mfma_f32_16x16x32_bf16 v[112:115], a[96:99], a[88:91], v[112:115]\n" \
"v_mfma_f32_16x16x32_bf16 v[112:115], a[100:103], a[92:95], v[112:115]\n" \
"s_waitcnt vmcnt(8) lgkmcnt(0)\n" \
"s_barrier\n" \
"v_mfma_f32_16x16x32_bf16 v[72:75], a[112:115], a[64:67], v[72:75]\n" \
"ds_read_b128 a[0:3], v213 offset:0\n" \
"ds_read_b128 a[4:7], v213 offset:2048\n" \
"v_mfma_f32_16x16x32_bf16 v[72:75], a[116:119], a[68:71], v[72:75]\n" \
"s_sub_u32 s68, s86, 32768\n" \
"s_cselect_b32 s68, 131072, s68\n" \
"ds_read_b64_tr_b16 a[128:129], v214 offset:16384\n" \
"ds_read_b64_tr_b16 a[130:131], v214 offset:18432\n" \
"v_mfma_f32_16x16x32_bf16 v[76:79], a[120:123], a[64:67], v[76:79]\n" \
"ds_read_b64_tr_b16 a[132:133], v214 offset:24576\n" \
"ds_read_b64_tr_b16 a[134:135], v214 offset:26624\n" \
"v_mfma_f32_16x16x32_bf16 v[76:79], a[124:127], a[68:71], v[76:79]\n" \
"s_cmp_lt_u32 s61, 2\n" \
"s_cselect_b32 s68, s86, s68\n" \
"ds_read_b64_tr_b16 a[136:137], v215 offset:16384\n" \
"ds_read_b64_tr_b16 a[138:139], v215 offset:18432\n" \
"v_mfma_f32_16x16x32_bf16 v[92:95], a[120:123], a[72:75], v[92:95]\n" \
"s_and_b32 s50, s61, 1\n" \
"ds_read_b64_tr_b16 a[140:141], v215 offset:24576\n" \
"ds_read_b64_tr_b16 a[142:143], v215 offset:26624\n" \
"v_mfma_f32_16x16x32_bf16 v[92:95], a[124:127], a[76:79], v[92:95]\n" \
"s_mul_i32 s50, s50, 9216\n" \
"ds_read_b64_tr_b16 a[144:145], v214 offset:16896\n" \
"ds_read_b64_tr_b16 a[146:147], v214 offset:18944\n" \
"v_mfma_f32_16x16x32_bf16 v[88:91], a[112:115], a[72:75], v[88:91]\n" \
"s_add_u32 s68, s68, s50\n" \
"ds_read_b64_tr_b16 a[148:149], v214 offset:25088\n" \
"ds_read_b64_tr_b16 a[150:151], v214 offset:27136\n" \
"v_mfma_f32_16x16x32_bf16 v[88:91], a[116:119], a[76:79], v[88:91]\n" \
"s_add_u32 s68, s68, %3\n" \
"ds_read_b64_tr_b16 a[152:153], v215 offset:16896\n" \
"ds_read_b64_tr_b16 a[154:155], v215 offset:18944\n" \
"v_mfma_f32_16x16x32_bf16 v[104:107], a[112:115], a[80:83], v[104:107]\n" \
"ds_read_b64_tr_b16 a[156:157], v215 offset:25088\n" \
"ds_read_b64_tr_b16 a[158:159], v215 offset:27136\n" \
"v_mfma_f32_16x16x32_bf16 v[104:107], a[116:119], a[84:87], v[104:107]\n" \
"ds_read_b128 a[24:27], v213 offset:256\n" \
"ds_read_b128 a[28:31], v213 offset:2304\n" \
"v_mfma_f32_16x16x32_bf16 v[108:111], a[120:123], a[80:83], v[108:111]\n" \
"ds_read_b128 a[8:11], v224 offset:0\n" \
"ds_read_b128 a[12:15], v224 offset:2048\n" \
"v_mfma_f32_16x16x32_bf16 v[108:111], a[124:127], a[84:87], v[108:111]\n" \
"ds_read_b64_tr_b16 a[160:161], v225 offset:16384\n" \
"ds_read_b64_tr_b16 a[162:163], v225 offset:18432\n" \
"v_mfma_f32_16x16x32_bf16 v[124:127], a[120:123], a[88:91], v[124:127]\n" \
"ds_read_b64_tr_b16 a[164:165], v225 offset:24576\n" \
"ds_read_b64_tr_b16 a[166:167], v225 offset:26624\n" \
"v_mfma_f32_16x16x32_bf16 v[124:127], a[124:127], a[92:95], v[124:127]\n" \
"ds_read_b64_tr_b16 a[168:169], v226 offset:16384\n" \
"ds_read_b64_tr_b16 a[170:171], v226 offset:18432\n" \
"v_mfma_f32_16x16x32_bf16 v[120:123], a[112:115], a[88:91], v[120:123]\n" \
"ds_read_b64_tr_b16 a[172:173], v226 offset:24576\n" \
"ds_read_b64_tr_b16 a[174:175], v226 offset:26624\n" \
"v_mfma_f32_16x16x32_bf16 v[120:123], a[116:119], a[92:95], v[120:123]\n" \
"s_waitcnt lgkmcnt(8)\n" \
"ds_read_b64_tr_b16 a[176:177], v225 offset:16896\n" \
"ds_read_b64_tr_b16 a[178:179], v225 offset:18944\n" \
"v_mfma_f32_16x16x32_bf16 v[64:67], a[128:131], a[0:3], v[64:67]\n" \
"ds_read_b64_tr_b16 a[180:181], v225 offset:25088\n" \
"ds_read_b64_tr_b16 a[182:183], v225 offset:27136\n" \
"v_mfma_f32_16x16x32_bf16 v[64:67], a[132:135], a[4:7], v[64:67]\n" \
"ds_read_b64_tr_b16 a[184:185], v226 offset:16896\n" \
"ds_read_b64_tr_b16 a[186:187], v226 offset:18944\n" \
"v_mfma_f32_16x16x32_bf16 v[68:71], a[136:139], a[0:3], v[68:71]\n" \
"ds_read_b64_tr_b16 a[188:189], v226 offset:25088\n" \
"ds_read_b64_tr_b16 a[190:191], v226 offset:27136\n" \
"v_mfma_f32_16x16x32_bf16 v[68:71], a[140:143], a[4:7], v[68:71]\n" \
"v_lshrrev_b32 v132, 4, v234\n" \
"v_and_b32 v133, 15, v132\n" \
"v_mfma_f32_16x16x32_bf16 v[72:75], a[144:147], a[0:3], v[72:75]\n" \
"v_mul_u32_u24 v128, 144, v133\n" \
"v_lshrrev_b32 v133, 4, v132\n" \
"v_mfma_f32_16x16x32_bf16 v[72:75], a[148:151], a[4:7], v[72:75]\n" \
"v_lshl_add_u32 v128, v133, 3, v128\n" \
"v_add_u32 v128, s68, v128\n" \
"v_mfma_f32_16x16x32_bf16 v[76:79], a[152:155], a[0:3], v[76:79]\n" \
"v_lshrrev_b32 v133, 3, v132\n" \
"v_and_b32 v134, 7, v132\n" \
"v_mfma_f32_16x16x32_bf16 v[76:79], a[156:159], a[4:7], v[76:79]\n" \
"v_mul_u32_u24 v129, 144, v133\n" \
"v_lshl_add_u32 v129, v134, 4, v129\n" \
"v_mfma_f32_16x16x32_bf16 v[92:95], a[152:155], a[24:27], v[92:95]\n" \
"v_add_u32 v129, s68, v129\n" \
"v_lshlrev_b32 v134, 4, v134\n" \
"v_mfma_f32_16x16x32_bf16 v[92:95], a[156:159], a[28:31], v[92:95]\n" \
"v_lshl_add_u32 v130, v133, 13, v134\n" \
"v_mfma_f32_16x16x32_bf16 v[88:91], a[144:147], a[24:27], v[88:91]\n" \
"v_mfma_f32_16x16x32_bf16 v[88:91], a[148:151], a[28:31], v[88:91]\n" \
"v_mfma_f32_16x16x32_bf16 v[84:87], a[136:139], a[24:27], v[84:87]\n" \
"s_waitcnt vmcnt(0)\n" \
"s_barrier\n" \
"v_mfma_f32_16x16x32_bf16 v[84:87], a[140:143], a[28:31], v[84:87]\n" \
"ds_read_b128 a[32:35], v224 offset:256\n" \
"ds_read_b128 a[36:39], v224 offset:2304\n" \
"v_mfma_f32_16x16x32_bf16 v[80:83], a[128:131], a[24:27], v[80:83]\n" \
"ds_read_b128 a[16:19], v227 offset:0\n" \
"ds_read_b128 a[20:23], v227 offset:2048\n" \
"v_mfma_f32_16x16x32_bf16 v[80:83], a[132:135], a[28:31], v[80:83]\n" \
"s_waitcnt lgkmcnt(4)\n" \
"ds_read_b64_tr_b16 a[192:193], v228 offset:16384\n" \
"ds_read_b64_tr_b16 a[194:195], v228 offset:18432\n" \
"v_mfma_f32_16x16x32_bf16 v[64:67], a[160:163], a[8:11], v[64:67]\n" \
"ds_read_b64_tr_b16 a[196:197], v228 offset:24576\n" \
"ds_read_b64_tr_b16 a[198:199], v228 offset:26624\n" \
"v_mfma_f32_16x16x32_bf16 v[64:67], a[164:167], a[12:15], v[64:67]\n" \
"ds_read_b64_tr_b16 a[200:201], v229 offset:16384\n" \
"ds_read_b64_tr_b16 a[202:203], v229 offset:18432\n" \
"v_mfma_f32_16x16x32_bf16 v[68:71], a[168:171], a[8:11], v[68:71]\n" \
"ds_read_b64_tr_b16 a[204:205], v229 offset:24576\n" \
"ds_read_b64_tr_b16 a[206:207], v229 offset:26624\n" \
"v_mfma_f32_16x16x32_bf16 v[68:71], a[172:175], a[12:15], v[68:71]\n" \
"ds_read_b64_tr_b16 a[208:209], v228 offset:16896\n" \
"ds_read_b64_tr_b16 a[210:211], v228 offset:18944\n" \
"v_mfma_f32_16x16x32_bf16 v[72:75], a[176:179], a[8:11], v[72:75]\n" \
"ds_read_b64_tr_b16 a[212:213], v228 offset:25088\n" \
"ds_read_b64_tr_b16 a[214:215], v228 offset:27136\n" \
"v_mfma_f32_16x16x32_bf16 v[72:75], a[180:183], a[12:15], v[72:75]\n" \
"ds_read_b64_tr_b16 a[216:217], v229 offset:16896\n" \
"ds_read_b64_tr_b16 a[218:219], v229 offset:18944\n" \
"v_mfma_f32_16x16x32_bf16 v[76:79], a[184:187], a[8:11], v[76:79]\n" \
"ds_read_b64_tr_b16 a[220:221], v229 offset:25088\n" \
"ds_read_b64_tr_b16 a[222:223], v229 offset:27136\n" \
"v_mfma_f32_16x16x32_bf16 v[76:79], a[188:191], a[12:15], v[76:79]\n" \
"s_waitcnt lgkmcnt(8)\n" \
"ds_read_b128 a[40:43], v227 offset:256\n" \
"ds_read_b128 a[44:47], v227 offset:2304\n" \
"v_mfma_f32_16x16x32_bf16 v[92:95], a[184:187], a[32:35], v[92:95]\n" \
"ds_read_b128 a[248:251], v213 offset:4096\n" \
"ds_read_b128 a[252:255], v213 offset:6144\n" \
"v_mfma_f32_16x16x32_bf16 v[92:95], a[188:191], a[36:39], v[92:95]\n" \
"ds_read_b128 a[48:51], v224 offset:4096\n" \
"ds_read_b128 a[52:55], v224 offset:6144\n" \
"v_mfma_f32_16x16x32_bf16 v[88:91], a[176:179], a[32:35], v[88:91]\n" \
"ds_read_b128 a[56:59], v227 offset:4096\n" \
"ds_read_b128 a[60:63], v227 offset:6144\n" \
"v_mfma_f32_16x16x32_bf16 v[88:91], a[180:183], a[36:39], v[88:91]\n" \
"ds_read_b128 a[224:227], v213 offset:4352\n" \
"ds_read_b128 a[228:231], v213 offset:6400\n" \
"v_mfma_f32_16x16x32_bf16 v[84:87], a[168:171], a[32:35], v[84:87]\n" \
"ds_read_b128 a[232:235], v224 offset:4352\n" \
"ds_read_b128 a[236:239], v224 offset:6400\n" \
"v_mfma_f32_16x16x32_bf16 v[84:87], a[172:175], a[36:39], v[84:87]\n" \
"ds_read_b128 a[240:243], v227 offset:4352\n" \
"ds_read_b128 a[244:247], v227 offset:6400\n" \
"v_mfma_f32_16x16x32_bf16 v[80:83], a[160:163], a[32:35], v[80:83]\n" \
"v_mfma_f32_16x16x32_bf16 v[80:83], a[164:167], a[36:39], v[80:83]\n" \
"v_mfma_f32_16x16x32_bf16 v[64:67], a[192:195], a[16:19], v[64:67]\n" \
"v_mfma_f32_16x16x32_bf16 v[64:67], a[196:199], a[20:23], v[64:67]\n" \
"v_mfma_f32_16x16x32_bf16 v[68:71], a[200:203], a[16:19], v[68:71]\n" \
"v_mfma_f32_16x16x32_bf16 v[68:71], a[204:207], a[20:23], v[68:71]\n" \
"s_waitcnt lgkmcnt(0)\n" \
"v_mfma_f32_16x16x32_bf16 v[72:75], a[208:211], a[16:19], v[72:75]\n" \
"v_mfma_f32_16x16x32_bf16 v[72:75], a[212:215], a[20:23], v[72:75]\n" \
"v_cvt_pk_bf16_f32 v136, v64, v65\n" \
"v_cvt_pk_bf16_f32 v137, v66, v67\n" \
"v_mfma_f32_16x16x32_bf16 v[76:79], a[216:219], a[16:19], v[76:79]\n" \
"v_mfma_f32_16x16x32_bf16 v[76:79], a[220:223], a[20:23], v[76:79]\n" \
"ds_write_b64 v128, v[136:137] offset:0\n" \
"v_mfma_f32_16x16x32_bf16 v[80:83], a[192:195], a[40:43], v[80:83]\n" \
"v_cvt_pk_bf16_f32 v138, v68, v69\n" \
"v_cvt_pk_bf16_f32 v139, v70, v71\n" \
"v_mfma_f32_16x16x32_bf16 v[80:83], a[196:199], a[44:47], v[80:83]\n" \
"ds_write_b64 v128, v[138:139] offset:32\n" \
"v_mfma_f32_16x16x32_bf16 v[84:87], a[200:203], a[40:43], v[84:87]\n" \
"v_cvt_pk_bf16_f32 v136, v72, v73\n" \
"v_cvt_pk_bf16_f32 v137, v74, v75\n" \
"v_mfma_f32_16x16x32_bf16 v[84:87], a[204:207], a[44:47], v[84:87]\n" \
"ds_write_b64 v128, v[136:137] offset:64\n" \
"v_mfma_f32_16x16x32_bf16 v[88:91], a[208:211], a[40:43], v[88:91]\n" \
"v_cvt_pk_bf16_f32 v138, v76, v77\n" \
"v_cvt_pk_bf16_f32 v139, v78, v79\n" \
"v_mfma_f32_16x16x32_bf16 v[88:91], a[212:215], a[44:47], v[88:91]\n" \
"ds_write_b64 v128, v[138:139] offset:96\n" \
"v_mfma_f32_16x16x32_bf16 v[92:95], a[216:219], a[40:43], v[92:95]\n" \
"ds_read_b128 v[140:143], v129 offset:0\n" \
"ds_read_b128 v[144:147], v129 offset:1152\n" \
"v_mfma_f32_16x16x32_bf16 v[92:95], a[220:223], a[44:47], v[92:95]\n" \
"v_cvt_pk_bf16_f32 v136, v80, v81\n" \
"v_cvt_pk_bf16_f32 v137, v82, v83\n" \
"v_mfma_f32_16x16x32_bf16 v[96:99], a[128:131], a[248:251], v[96:99]\n" \
"ds_write_b64 v128, v[136:137] offset:2304\n" \
"v_mfma_f32_16x16x32_bf16 v[96:99], a[132:135], a[252:255], v[96:99]\n" \
"s_add_u32 s70, s88, 0\n" \
"s_waitcnt lgkmcnt(2)\n" \
"v_cvt_pk_bf16_f32 v138, v84, v85\n" \
"v_mfma_f32_16x16x32_bf16 v[96:99], a[160:163], a[48:51], v[96:99]\n" \
"buffer_store_dwordx4 v[140:143], v130, s[52:55], s70 offen sc1\n" \
"v_mfma_f32_16x16x32_bf16 v[96:99], a[164:167], a[52:55], v[96:99]\n" \
"v_cvt_pk_bf16_f32 v139, v86, v87\n" \
"v_mfma_f32_16x16x32_bf16 v[96:99], a[192:195], a[56:59], v[96:99]\n" \
"s_add_u32 s70, s88, 65536\n" \
"s_waitcnt lgkmcnt(1)\n" \
"v_mfma_f32_16x16x32_bf16 v[96:99], a[196:199], a[60:63], v[96:99]\n" \
"buffer_store_dwordx4 v[144:147], v130, s[52:55], s70 offen sc1\n" \
"ds_write_b64 v128, v[138:139] offset:2336\n" \
"v_mfma_f32_16x16x32_bf16 v[100:103], a[204:207], a[60:63], v[100:103]\n" \
"v_cvt_pk_bf16_f32 v136, v88, v89\n" \
"v_cvt_pk_bf16_f32 v137, v90, v91\n" \
"v_mfma_f32_16x16x32_bf16 v[100:103], a[200:203], a[56:59], v[100:103]\n" \
"ds_write_b64 v128, v[136:137] offset:2368\n" \
"v_mfma_f32_16x16x32_bf16 v[100:103], a[172:175], a[52:55], v[100:103]\n" \
"v_cvt_pk_bf16_f32 v138, v92, v93\n" \
"v_cvt_pk_bf16_f32 v139, v94, v95\n" \
"v_mfma_f32_16x16x32_bf16 v[100:103], a[168:171], a[48:51], v[100:103]\n" \
"v_mfma_f32_16x16x32_bf16 v[100:103], a[140:143], a[252:255], v[100:103]\n" \
"v_mfma_f32_16x16x32_bf16 v[100:103], a[136:139], a[248:251], v[100:103]\n" \
"ds_write_b64 v128, v[138:139] offset:2400\n" \
"v_mfma_f32_16x16x32_bf16 v[104:107], a[144:147], a[248:251], v[104:107]\n" \
"ds_read_b128 v[148:151], v129 offset:2304\n" \
"ds_read_b128 v[152:155], v129 offset:3456\n" \
"v_mfma_f32_16x16x32_bf16 v[104:107], a[148:151], a[252:255], v[104:107]\n" \
"v_cvt_pk_bf16_f32 v136, v96, v97\n" \
"v_cvt_pk_bf16_f32 v137, v98, v99\n" \
"v_mfma_f32_16x16x32_bf16 v[104:107], a[176:179], a[48:51], v[104:107]\n" \
"ds_write_b64 v128, v[136:137] offset:4608\n" \
"v_mfma_f32_16x16x32_bf16 v[104:107], a[180:183], a[52:55], v[104:107]\n" \
"v_cvt_pk_bf16_f32 v138, v100, v101\n" \
"v_cvt_pk_bf16_f32 v139, v102, v103\n" \
"v_mfma_f32_16x16x32_bf16 v[104:107], a[208:211], a[56:59], v[104:107]\n" \
"s_add_u32 s70, s88, 4096\n" \
"s_waitcnt lgkmcnt(2)\n" \
"v_mfma_f32_16x16x32_bf16 v[104:107], a[212:215], a[60:63], v[104:107]\n" \
"buffer_store_dwordx4 v[148:151], v130, s[52:55], s70 offen sc1\n" \
"ds_write_b64 v128, v[138:139] offset:4640\n" \
"v_mfma_f32_16x16x32_bf16 v[108:111], a[220:223], a[60:63], v[108:111]\n" \
"s_add_u32 s70, s88, 69632\n" \
"s_waitcnt lgkmcnt(2)\n" \
"v_mfma_f32_16x16x32_bf16 v[108:111], a[216:219], a[56:59], v[108:111]\n" \
"buffer_store_dwordx4 v[152:155], v130, s[52:55], s70 offen sc1\n" \
"v_mfma_f32_16x16x32_bf16 v[108:111], a[188:191], a[52:55], v[108:111]\n" \
"v_mfma_f32_16x16x32_bf16 v[108:111], a[184:187], a[48:51], v[108:111]\n" \
"v_cvt_pk_bf16_f32 v136, v104, v105\n" \
"v_cvt_pk_bf16_f32 v137, v106, v107\n" \
"v_mfma_f32_16x16x32_bf16 v[108:111], a[156:159], a[252:255], v[108:111]\n" \
"v_mfma_f32_16x16x32_bf16 v[108:111], a[152:155], a[248:251], v[108:111]\n" \
"ds_write_b64 v128, v[136:137] offset:4672\n" \
"v_mfma_f32_16x16x32_bf16 v[112:115], a[128:131], a[224:227], v[112:115]\n" \
"v_mfma_f32_16x16x32_bf16 v[112:115], a[132:135], a[228:231], v[112:115]\n" \
"v_mfma_f32_16x16x32_bf16 v[112:115], a[160:163], a[232:235], v[112:115]\n" \
"v_mfma_f32_16x16x32_bf16 v[112:115], a[164:167], a[236:239], v[112:115]\n" \
"v_cvt_pk_bf16_f32 v138, v108, v109\n" \
"v_cvt_pk_bf16_f32 v139, v110, v111\n" \
"v_mfma_f32_16x16x32_bf16 v[112:115], a[192:195], a[240:243], v[112:115]\n" \
"v_mfma_f32_16x16x32_bf16 v[112:115], a[196:199], a[244:247], v[112:115]\n" \
"ds_write_b64 v128, v[138:139] offset:4704\n" \
"v_mfma_f32_16x16x32_bf16 v[116:119], a[204:207], a[244:247], v[116:119]\n" \
"ds_read_b128 v[156:159], v129 offset:4608\n" \
"ds_read_b128 v[160:163], v129 offset:5760\n" \
"v_mfma_f32_16x16x32_bf16 v[116:119], a[200:203], a[240:243], v[116:119]\n" \
"v_mfma_f32_16x16x32_bf16 v[116:119], a[172:175], a[236:239], v[116:119]\n" \
"v_mfma_f32_16x16x32_bf16 v[116:119], a[168:171], a[232:235], v[116:119]\n" \
"v_cvt_pk_bf16_f32 v136, v112, v113\n" \
"v_cvt_pk_bf16_f32 v137, v114, v115\n" \
"v_mfma_f32_16x16x32_bf16 v[116:119], a[140:143], a[228:231], v[116:119]\n" \
"s_add_u32 s70, s88, 131072\n" \
"s_waitcnt lgkmcnt(1)\n" \
"v_mfma_f32_16x16x32_bf16 v[116:119], a[136:139], a[224:227], v[116:119]\n" \
"buffer_store_dwordx4 v[156:159], v130, s[52:55], s70 offen sc1\n" \
"ds_write_b64 v128, v[136:137] offset:6912\n" \
"v_mfma_f32_16x16x32_bf16 v[120:123], a[144:147], a[224:227], v[120:123]\n" \
"s_add_u32 s70, s88, 196608\n" \
"s_waitcnt lgkmcnt(1)\n" \
"v_mfma_f32_16x16x32_bf16 v[120:123], a[148:151], a[228:231], v[120:123]\n" \
"buffer_store_dwordx4 v[160:163], v130, s[52:55], s70 offen sc1\n" \
"v_mfma_f32_16x16x32_bf16 v[120:123], a[176:179], a[232:235], v[120:123]\n" \
"v_mfma_f32_16x16x32_bf16 v[120:123], a[180:183], a[236:239], v[120:123]\n" \
"v_cvt_pk_bf16_f32 v138, v116, v117\n" \
"v_cvt_pk_bf16_f32 v139, v118, v119\n" \
"v_mfma_f32_16x16x32_bf16 v[120:123], a[208:211], a[240:243], v[120:123]\n" \
"v_mfma_f32_16x16x32_bf16 v[120:123], a[212:215], a[244:247], v[120:123]\n" \
"ds_write_b64 v128, v[138:139] offset:6944\n" \
"v_mfma_f32_16x16x32_bf16 v[124:127], a[220:223], a[244:247], v[124:127]\n" \
"v_mfma_f32_16x16x32_bf16 v[124:127], a[216:219], a[240:243], v[124:127]\n" \
"v_mfma_f32_16x16x32_bf16 v[124:127], a[188:191], a[236:239], v[124:127]\n" \
"v_mfma_f32_16x16x32_bf16 v[124:127], a[184:187], a[232:235], v[124:127]\n" \
"v_cvt_pk_bf16_f32 v136, v120, v121\n" \
"v_cvt_pk_bf16_f32 v137, v122, v123\n" \
"v_mfma_f32_16x16x32_bf16 v[124:127], a[156:159], a[228:231], v[124:127]\n" \
"v_mfma_f32_16x16x32_bf16 v[124:127], a[152:155], a[224:227], v[124:127]\n" \
"ds_write_b64 v128, v[136:137] offset:6976\n" \
"s_nop 7\n" \
"v_cvt_pk_bf16_f32 v138, v124, v125\n" \
"v_cvt_pk_bf16_f32 v139, v126, v127\n" \
"ds_write_b64 v128, v[138:139] offset:7008\n" \
"ds_read_b128 v[164:167], v129 offset:6912\n" \
"ds_read_b128 v[168:171], v129 offset:8064\n" \
"s_add_u32 s70, s88, 135168\n" \
"s_waitcnt lgkmcnt(1)\n" \
"buffer_store_dwordx4 v[164:167], v130, s[52:55], s70 offen sc1\n" \
"s_add_u32 s70, s88, 200704\n" \
"s_waitcnt lgkmcnt(0)\n" \
"buffer_store_dwordx4 v[168:171], v130, s[52:55], s70 offen sc1\n" \
"s_endpgm\n" \
"Lhot_end_%=:\n" \
"Lpl1_%=:\n" \
"s_mov_b32 s72, 0\n" \
"Lpk1_%=:\n" \
"s_sleep 2\n" \
"buffer_load_dword v227, v242, s[56:59], s90 offen sc1\n" \
"s_waitcnt vmcnt(0)\n" \
"v_cmp_eq_u32 vcc, s60, v227\n" \
"s_nop 1\n" \
"s_andn2_b32 s70, 0xff, vcc_lo\n" \
"s_cbranch_scc0 Lpr1_%=\n" \
"s_add_u32 s72, s72, 1\n" \
"s_cmp_lt_u32 s72, 0x40000\n" \
"s_cbranch_scc1 Lpk1_%=\n" \
"s_branch Lpr1_%=\n" \
"Lpl2_%=:\n" \
"s_mov_b32 s72, 0\n" \
"Lpk2_%=:\n" \
"s_sleep 2\n" \
"buffer_load_dword v239, v242, s[56:59], s95 offen sc1\n" \
"s_waitcnt vmcnt(0)\n" \
"v_cmp_eq_u32 vcc, s60, v239\n" \
"s_nop 1\n" \
"s_andn2_b32 s70, s69, vcc_lo\n" \
"s_cbranch_scc0 Lpr2_%=\n" \
"s_add_u32 s72, s72, 1\n" \
"s_cmp_lt_u32 s72, 0x40000\n" \
"s_cbranch_scc1 Lpk2_%=\n" \
"s_branch Lpr2_%=\n" \
"Lpl3_%=:\n" \
"s_mov_b32 s72, 0\n" \
"Lpk3_%=:\n" \
"s_sleep 2\n" \
"buffer_load_dword v239, v242, s[56:59], s95 offen sc1\n" \
"s_waitcnt vmcnt(0)\n" \
"v_cmp_eq_u32 vcc, s60, v239\n" \
"s_nop 1\n" \
"s_andn2_b32 s70, s69, vcc_lo\n" \
"s_cbranch_scc0 Lpr3_%=\n" \
"s_add_u32 s72, s72, 1\n" \
"s_cmp_lt_u32 s72, 0x40000\n" \
"s_cbranch_scc1 Lpk3_%=\n" \
"s_branch Lpr3_%=\n" \
"Lexact_%=:\n" \
"v_mov_b32 v239, 0xff800000\n" \
"s_and_b32 s70, s61, 1\n" \
"s_lshl_b32 s70, s70, 4\n" \
"v_add_u32 v186, s70, v238\n" \
"v_accvgpr_read_b32 v160, a128\n" \
"v_accvgpr_read_b32 v161, a129\n" \
"v_accvgpr_read_b32 v162, a130\n" \
"v_accvgpr_read_b32 v163, a131\n" \
"v_accvgpr_read_b32 v164, a132\n" \
"v_accvgpr_read_b32 v165, a133\n" \
"v_accvgpr_read_b32 v166, a134\n" \
"v_accvgpr_read_b32 v167, a135\n" \
"v_accvgpr_read_b32 v168, a136\n" \
"v_accvgpr_read_b32 v169, a137\n" \
"v_accvgpr_read_b32 v170, a138\n" \
"v_accvgpr_read_b32 v171, a139\n" \
"v_accvgpr_read_b32 v172, a140\n" \
"v_accvgpr_read_b32 v173, a141\n" \
"v_accvgpr_read_b32 v174, a142\n" \
"v_accvgpr_read_b32 v175, a143\n" \
"s_add_u32 s83, s85, 0\n" \
"s_add_u32 s84, s87, 0\n" \
"s_call_b64 s[64:65], Lex1_%=\n" \
"v_accvgpr_read_b32 v160, a144\n" \
"v_accvgpr_read_b32 v161, a145\n" \
"v_accvgpr_read_b32 v162, a146\n" \
"v_accvgpr_read_b32 v163, a147\n" \
"v_accvgpr_read_b32 v164, a148\n" \
"v_accvgpr_read_b32 v165, a149\n" \
"v_accvgpr_read_b32 v166, a150\n" \
"v_accvgpr_read_b32 v167, a151\n" \
"v_accvgpr_read_b32 v168, a152\n" \
"v_accvgpr_read_b32 v169, a153\n" \
"v_accvgpr_read_b32 v170, a154\n" \
"v_accvgpr_read_b32 v171, a155\n" \
"v_accvgpr_read_b32 v172, a156\n" \
"v_accvgpr_read_b32 v173, a157\n" \
"v_accvgpr_read_b32 v174, a158\n" \
"v_accvgpr_read_b32 v175, a159\n" \
"s_add_u32 s83, s85, 2\n" \
"s_add_u32 s84, s87, 532480\n" \
"s_call_b64 s[64:65], Lex1_%=\n" \
"v_accvgpr_read_b32 v160, a160\n" \
"v_accvgpr_read_b32 v161, a161\n" \
"v_accvgpr_read_b32 v162, a162\n" \
"v_accvgpr_read_b32 v163, a163\n" \
"v_accvgpr_read_b32 v164, a164\n" \
"v_accvgpr_read_b32 v165, a165\n" \
"v_accvgpr_read_b32 v166, a166\n" \
"v_accvgpr_read_b32 v167, a167\n" \
"v_accvgpr_read_b32 v168, a168\n" \
"v_accvgpr_read_b32 v169, a169\n" \
"v_accvgpr_read_b32 v170, a170\n" \
"v_accvgpr_read_b32 v171, a171\n" \
"v_accvgpr_read_b32 v172, a172\n" \
"v_accvgpr_read_b32 v173, a173\n" \
"v_accvgpr_read_b32 v174, a174\n" \
"v_accvgpr_read_b32 v175, a175\n" \
"s_add_u32 s83, s86, 0\n" \
"s_add_u32 s84, s88, 0\n" \
"s_call_b64 s[64:65], Lex1_%=\n" \
"v_accvgpr_read_b32 v160, a176\n" \
"v_accvgpr_read_b32 v161, a177\n" \
"v_accvgpr_read_b32 v162, a178\n" \
"v_accvgpr_read_b32 v163, a179\n" \
"v_accvgpr_read_b32 v164, a180\n" \
"v_accvgpr_read_b32 v165, a181\n" \
"v_accvgpr_read_b32 v166, a182\n" \
"v_accvgpr_read_b32 v167, a183\n" \
"v_accvgpr_read_b32 v168, a184\n" \
"v_accvgpr_read_b32 v169, a185\n" \
"v_accvgpr_read_b32 v170, a186\n" \
"v_accvgpr_read_b32 v171, a187\n" \
"v_accvgpr_read_b32 v172, a188\n" \
"v_accvgpr_read_b32 v173, a189\n" \
"v_accvgpr_read_b32 v174, a190\n" \
"v_accvgpr_read_b32 v175, a191\n" \
"s_add_u32 s83, s86, 2\n" \
"s_add_u32 s84, s88, 532480\n" \
"s_call_b64 s[64:65], Lex1_%=\n" \
"s_waitcnt vmcnt(0)\n" \
"s_branch Lexr_%=\n" \
"Lex1_%=:\n" \
"v_mov_b32 v128, 0\n" \
"v_mov_b32 v129, 0\n" \
"v_mov_b32 v130, 0\n" \
"v_mov_b32 v131, 0\n" \
"v_mov_b32 v132, 0\n" \
"v_mov_b32 v133, 0\n" \
"v_mov_b32 v134, 0\n" \
"v_mov_b32 v135, 0\n" \
"v_mov_b32 v136, 0\n" \
"v_mov_b32 v137, 0\n" \
"v_mov_b32 v138, 0\n" \
"v_mov_b32 v139, 0\n" \
"v_mov_b32 v140, 0\n" \
"v_mov_b32 v141, 0\n" \
"v_mov_b32 v142, 0\n" \
"v_mov_b32 v143, 0\n" \
"v_mov_b32 v144, 0\n" \
"v_mov_b32 v145, 0\n" \
"v_mov_b32 v146, 0\n" \
"v_mov_b32 v147, 0\n" \
"v_mov_b32 v148, 0\n" \
"v_mov_b32 v149, 0\n" \
"v_mov_b32 v150, 0\n" \
"v_mov_b32 v151, 0\n" \
"v_mov_b32 v152, 0\n" \
"v_mov_b32 v153, 0\n" \
"v_mov_b32 v154, 0\n" \
"v_mov_b32 v155, 0\n" \
"v_mov_b32 v156, 0\n" \
"v_mov_b32 v157, 0\n" \
"v_mov_b32 v158, 0\n" \
"v_mov_b32 v159, 0\n" \
"v_mov_b32 v184, 0xf149f2ca\n" \
"v_mov_b32 v185, 0\n" \
"s_mov_b32 s81, 0\n" \
"s_mul_i32 s82, s62, 557824\n" \
"Lex2_%=:\n" \
"s_add_u32 s70, s82, 4096\n" \
"buffer_load_dwordx4 a[64:67], v236, s[56:59], s82 offen offset:0 sc1\n" \
"buffer_load_dwordx4 a[68:71], v236, s[56:59], s82 offen offset:2048 sc1\n" \
"buffer_load_dwordx4 a[72:75], v236, s[56:59], s70 offen offset:0 sc1\n" \
"buffer_load_dwordx4 a[76:79], v236, s[56:59], s70 offen offset:2048 sc1\n" \
"buffer_load_dwordx4 a[80:83], v236, s[56:59], s82 offen offset:256 sc1\n" \
"buffer_load_dwordx4 a[84:87], v236, s[56:59], s82 offen offset:2304 sc1\n" \
"buffer_load_dwordx4 a[88:91], v236, s[56:59], s70 offen offset:256 sc1\n" \
"buffer_load_dwordx4 a[92:95], v236, s[56:59], s70 offen offset:2304 sc1\n" \
"s_add_u32 s70, s82, 17850368\n" \
"s_add_u32 s71, s70, 4096\n" \
"buffer_load_dwordx4 a[96:99], v234, s[56:59], s70 offen offset:0 sc1\n" \
"buffer_load_dwordx4 a[100:103], v234, s[56:59], s70 offen offset:1024 sc1\n" \
"buffer_load_dwordx4 a[104:107], v234, s[56:59], s70 offen offset:2048 sc1\n" \
"buffer_load_dwordx4 a[108:111], v234, s[56:59], s70 offen offset:3072 sc1\n" \
"buffer_load_dwordx4 a[112:115], v234, s[56:59], s71 offen offset:0 sc1\n" \
"buffer_load_dwordx4 a[116:119], v234, s[56:59], s71 offen offset:1024 sc1\n" \
"buffer_load_dwordx4 a[120:123], v234, s[56:59], s71 offen offset:2048 sc1\n" \
"buffer_load_dwordx4 a[124:127], v234, s[56:59], s71 offen offset:3072 sc1\n" \
"s_waitcnt vmcnt(0)\n" \
"v_mfma_f32_16x16x32_bf16 v[176:179], a[64:67], v[160:163], 0\n" \
"v_mfma_f32_16x16x32_bf16 v[176:179], a[68:71], v[164:167], v[176:179]\n" \
"v_mfma_f32_16x16x32_bf16 v[176:179], a[72:75], v[168:171], v[176:179]\n" \
"v_mfma_f32_16x16x32_bf16 v[176:179], a[76:79], v[172:175], v[176:179]\n" \
"v_mfma_f32_16x16x32_bf16 v[180:183], a[80:83], v[160:163], 0\n" \
"v_mfma_f32_16x16x32_bf16 v[180:183], a[84:87], v[164:167], v[180:183]\n" \
"v_mfma_f32_16x16x32_bf16 v[180:183], a[88:91], v[168:171], v[180:183]\n" \
"v_mfma_f32_16x16x32_bf16 v[180:183], a[92:95], v[172:175], v[180:183]\n" \
"s_nop 7\n" \
"s_nop 1\n" \
"s_cmp_eq_u32 s81, s83\n" \
"s_cbranch_scc0 Lex3_%=\n" \
"v_cmp_gt_i32 vcc, 0, v186\n" \
"s_nop 1\n" \
"v_cndmask_b32 v176, v176, v239, vcc\n" \
"v_cmp_gt_i32 vcc, 1, v186\n" \
"s_nop 1\n" \
"v_cndmask_b32 v177, v177, v239, vcc\n" \
"v_cmp_gt_i32 vcc, 2, v186\n" \
"s_nop 1\n" \
"v_cndmask_b32 v178, v178, v239, vcc\n" \
"v_cmp_gt_i32 vcc, 3, v186\n" \
"s_nop 1\n" \
"v_cndmask_b32 v179, v179, v239, vcc\n" \
"v_cmp_gt_i32 vcc, 16, v186\n" \
"s_nop 1\n" \
"v_cndmask_b32 v180, v180, v239, vcc\n" \
"v_cmp_gt_i32 vcc, 17, v186\n" \
"s_nop 1\n" \
"v_cndmask_b32 v181, v181, v239, vcc\n" \
"v_cmp_gt_i32 vcc, 18, v186\n" \
"s_nop 1\n" \
"v_cndmask_b32 v182, v182, v239, vcc\n" \
"v_cmp_gt_i32 vcc, 19, v186\n" \
"s_nop 1\n" \
"v_cndmask_b32 v183, v183, v239, vcc\n" \
"Lex3_%=:\n" \
"v_max3_f32 v187, v176, v177, v178\n" \
"v_max3_f32 v187, v187, v179, v180\n" \
"v_max3_f32 v187, v187, v181, v182\n" \
"v_max3_f32 v187, v187, v183, v184\n" \
"v_mov_b32 v188, v187\n" \
"s_nop 1\n" \
"v_permlane16_swap_b32 v188, v187\n" \
"v_max_f32 v187, v187, v188\n" \
"v_mov_b32 v188, v187\n" \
"s_nop 1\n" \
"v_permlane32_swap_b32 v188, v187\n" \
"v_max_f32 v187, v187, v188\n" \
"v_sub_f32 v189, v184, v187\n" \
"v_exp_f32 v189, v189\n" \
"v_mov_b32 v184, v187\n" \
"v_sub_f32 v176, v176, v187\n" \
"v_sub_f32 v177, v177, v187\n" \
"v_sub_f32 v178, v178, v187\n" \
"v_sub_f32 v179, v179, v187\n" \
"v_sub_f32 v180, v180, v187\n" \
"v_sub_f32 v181, v181, v187\n" \
"v_sub_f32 v182, v182, v187\n" \
"v_sub_f32 v183, v183, v187\n" \
"v_exp_f32 v176, v176\n" \
"v_exp_f32 v177, v177\n" \
"v_exp_f32 v178, v178\n" \
"v_exp_f32 v179, v179\n" \
"v_exp_f32 v180, v180\n" \
"v_exp_f32 v181, v181\n" \
"v_exp_f32 v182, v182\n" \
"v_exp_f32 v183, v183\n" \
"v_mul_f32 v185, v185, v189\n" \
"v_add_f32 v185, v185, v176\n" \
"v_add_f32 v185, v185, v177\n" \
"v_add_f32 v185, v185, v178\n" \
"v_add_f32 v185, v185, v179\n" \
"v_add_f32 v185, v185, v180\n" \
"v_add_f32 v185, v185, v181\n" \
"v_add_f32 v185, v185, v182\n" \
"v_add_f32 v185, v185, v183\n" \
"v_cvt_pk_bf16_f32 v112, v176, v177\n" \
"v_cvt_pk_bf16_f32 v113, v178, v179\n" \
"v_cvt_pk_bf16_f32 v114, v180, v181\n" \
"v_cvt_pk_bf16_f32 v115, v182, v183\n" \
"v_mul_f32 v128, v128, v189\n" \
"v_mul_f32 v129, v129, v189\n" \
"v_mul_f32 v130, v130, v189\n" \
"v_mul_f32 v131, v131, v189\n" \
"v_mul_f32 v132, v132, v189\n" \
"v_mul_f32 v133, v133, v189\n" \
"v_mul_f32 v134, v134, v189\n" \
"v_mul_f32 v135, v135, v189\n" \
"v_mul_f32 v136, v136, v189\n" \
"v_mul_f32 v137, v137, v189\n" \
"v_mul_f32 v138, v138, v189\n" \
"v_mul_f32 v139, v139, v189\n" \
"v_mul_f32 v140, v140, v189\n" \
"v_mul_f32 v141, v141, v189\n" \
"v_mul_f32 v142, v142, v189\n" \
"v_mul_f32 v143, v143, v189\n" \
"v_mul_f32 v144, v144, v189\n" \
"v_mul_f32 v145, v145, v189\n" \
"v_mul_f32 v146, v146, v189\n" \
"v_mul_f32 v147, v147, v189\n" \
"v_mul_f32 v148, v148, v189\n" \
"v_mul_f32 v149, v149, v189\n" \
"v_mul_f32 v150, v150, v189\n" \
"v_mul_f32 v151, v151, v189\n" \
"v_mul_f32 v152, v152, v189\n" \
"v_mul_f32 v153, v153, v189\n" \
"v_mul_f32 v154, v154, v189\n" \
"v_mul_f32 v155, v155, v189\n" \
"v_mul_f32 v156, v156, v189\n" \
"v_mul_f32 v157, v157, v189\n" \
"v_mul_f32 v158, v158, v189\n" \
"v_mul_f32 v159, v159, v189\n" \
"s_nop 1\n" \
"v_mfma_f32_16x16x32_bf16 v[128:131], a[96:99], v[112:115], v[128:131]\n" \
"v_mfma_f32_16x16x32_bf16 v[132:135], a[100:103], v[112:115], v[132:135]\n" \
"v_mfma_f32_16x16x32_bf16 v[136:139], a[104:107], v[112:115], v[136:139]\n" \
"v_mfma_f32_16x16x32_bf16 v[140:143], a[108:111], v[112:115], v[140:143]\n" \
"v_mfma_f32_16x16x32_bf16 v[144:147], a[112:115], v[112:115], v[144:147]\n" \
"v_mfma_f32_16x16x32_bf16 v[148:151], a[116:119], v[112:115], v[148:151]\n" \
"v_mfma_f32_16x16x32_bf16 v[152:155], a[120:123], v[112:115], v[152:155]\n" \
"v_mfma_f32_16x16x32_bf16 v[156:159], a[124:127], v[112:115], v[156:159]\n" \
"s_nop 7\n" \
"s_nop 1\n" \
"s_and_b32 s70, s81, 1\n" \
"s_mul_i32 s70, s70, 1024\n" \
"s_add_u32 s82, s82, s70\n" \
"s_add_u32 s82, s82, 8192\n" \
"s_add_u32 s81, s81, 1\n" \
"s_cmp_le_u32 s81, s83\n" \
"s_cbranch_scc1 Lex2_%=\n" \
"v_mov_b32 v187, v185\n" \
"s_nop 1\n" \
"v_permlane16_swap_b32 v187, v185\n" \
"v_add_f32 v185, v185, v187\n" \
"v_mov_b32 v187, v185\n" \
"s_nop 1\n" \
"v_permlane32_swap_b32 v187, v185\n" \
"v_add_f32 v185, v185, v187\n" \
"v_rcp_f32 v185, v185\n" \
"s_nop 0\n" \
"v_mul_f32 v128, v128, v185\n" \
"v_mul_f32 v129, v129, v185\n" \
"v_mul_f32 v130, v130, v185\n" \
"v_mul_f32 v131, v131, v185\n" \
"v_mul_f32 v132, v132, v185\n" \
"v_mul_f32 v133, v133, v185\n" \
"v_mul_f32 v134, v134, v185\n" \
"v_mul_f32 v135, v135, v185\n" \
"v_mul_f32 v136, v136, v185\n" \
"v_mul_f32 v137, v137, v185\n" \
"v_mul_f32 v138, v138, v185\n" \
"v_mul_f32 v139, v139, v185\n" \
"v_mul_f32 v140, v140, v185\n" \
"v_mul_f32 v141, v141, v185\n" \
"v_mul_f32 v142, v142, v185\n" \
"v_mul_f32 v143, v143, v185\n" \
"v_mul_f32 v144, v144, v185\n" \
"v_mul_f32 v145, v145, v185\n" \
"v_mul_f32 v146, v146, v185\n" \
"v_mul_f32 v147, v147, v185\n" \
"v_mul_f32 v148, v148, v185\n" \
"v_mul_f32 v149, v149, v185\n" \
"v_mul_f32 v150, v150, v185\n" \
"v_mul_f32 v151, v151, v185\n" \
"v_mul_f32 v152, v152, v185\n" \
"v_mul_f32 v153, v153, v185\n" \
"v_mul_f32 v154, v154, v185\n" \
"v_mul_f32 v155, v155, v185\n" \
"v_mul_f32 v156, v156, v185\n" \
"v_mul_f32 v157, v157, v185\n" \
"v_mul_f32 v158, v158, v185\n" \
"v_mul_f32 v159, v159, v185\n" \
"s_add_u32 s70, s84, 4096\n" \
"v_cvt_pk_bf16_f32 v112, v128, v129\n" \
"v_cvt_pk_bf16_f32 v113, v130, v131\n" \
"v_cvt_pk_bf16_f32 v114, v132, v133\n" \
"v_cvt_pk_bf16_f32 v115, v134, v135\n" \
"buffer_store_dwordx4 v[112:115], v236, s[56:59], s84 offen offset:0 sc1\n" \
"v_cvt_pk_bf16_f32 v116, v136, v137\n" \
"v_cvt_pk_bf16_f32 v117, v138, v139\n" \
"v_cvt_pk_bf16_f32 v118, v140, v141\n" \
"v_cvt_pk_bf16_f32 v119, v142, v143\n" \
"buffer_store_dwordx4 v[116:119], v236, s[56:59], s84 offen offset:2048 sc1\n" \
"v_cvt_pk_bf16_f32 v120, v144, v145\n" \
"v_cvt_pk_bf16_f32 v121, v146, v147\n" \
"v_cvt_pk_bf16_f32 v122, v148, v149\n" \
"v_cvt_pk_bf16_f32 v123, v150, v151\n" \
"buffer_store_dwordx4 v[120:123], v236, s[56:59], s70 offen offset:0 sc1\n" \
"v_cvt_pk_bf16_f32 v124, v152, v153\n" \
"v_cvt_pk_bf16_f32 v125, v154, v155\n" \
"v_cvt_pk_bf16_f32 v126, v156, v157\n" \
"v_cvt_pk_bf16_f32 v127, v158, v159\n" \
"buffer_store_dwordx4 v[124:127], v236, s[56:59], s70 offen offset:2048 sc1\n" \
"s_waitcnt vmcnt(0)\n" \
"s_setpc_b64 s[64:65]\n" \
"Lpl4_%=:\n" \
"s_mov_b32 s72, 0\n" \
"Lpk4_%=:\n" \
"s_sleep 2\n" \
"buffer_load_dwordx2 v[226:227], v228, s[56:59], s13 offen sc1\n" \
"s_waitcnt vmcnt(0)\n" \
"v_cmp_eq_u32 vcc, s60, v226\n" \
"s_nop 1\n" \
"s_andn2_b32 s70, 0xff, vcc_lo\n" \
"s_cbranch_scc0 Lpr4_%=\n" \
"s_add_u32 s72, s72, 1\n" \
"s_cmp_lt_u32 s72, 0x40000\n" \
"s_cbranch_scc1 Lpk4_%=\n" \
"s_branch Lpr4_%=\n" \
"Lpl5_%=:\n" \
"s_mov_b32 s72, 0\n" \
"Lpk5_%=:\n" \
"s_sleep 2\n" \
"buffer_load_dword v220, v221, s[56:59], s66 offen sc1\n" \
"s_waitcnt vmcnt(0)\n" \
"v_cmp_eq_u32 vcc, s60, v220\n" \
"s_nop 1\n" \
"s_andn2_b32 s70, s65, vcc_lo\n" \
"s_cbranch_scc0 Lpr5_%=\n" \
"s_add_u32 s72, s72, 1\n" \
"s_cmp_lt_u32 s72, 0x40000\n" \
"s_cbranch_scc1 Lpk5_%=\n" \
"s_branch Lpr5_%=\n" \
"Lpl6_%=:\n" \
"s_mov_b32 s72, 0\n" \
"Lpk6_%=:\n" \
"s_sleep 2\n" \
"buffer_load_dword v220, v221, s[56:59], s67 offen sc1\n" \
"s_waitcnt vmcnt(0)\n" \
"v_cmp_eq_u32 vcc, s60, v220\n" \
"s_nop 1\n" \
"s_andn2_b32 s70, s74, vcc_lo\n" \
"s_cbranch_scc0 Lpr6_%=\n" \
"s_add_u32 s72, s72, 1\n" \
"s_cmp_lt_u32 s72, 0x40000\n" \
"s_cbranch_scc1 Lpk6_%=\n" \
"s_branch Lpr6_%=\n" \
"Lpl7_%=:\n" \
"s_mov_b32 s72, 0\n" \
"Lpk7_%=:\n" \
"s_sleep 2\n" \
"buffer_load_dword v220, v241, s[56:59], s64 offen sc1\n" \
"s_waitcnt vmcnt(0)\n" \
"v_cmp_eq_u32 vcc, s60, v220\n" \
"s_nop 1\n" \
"s_andn2_b32 s70, s69, vcc_lo\n" \
"s_cbranch_scc0 Lpr7_%=\n" \
"s_add_u32 s72, s72, 1\n" \
"s_cmp_lt_u32 s72, 0x40000\n" \
"s_cbranch_scc1 Lpk7_%=\n" \
"s_branch Lpr7_%=\n" \
"Lcode_end_%=:\n" \
".p2align 12\n" \
""
#define MK_ASM_CLOBBERS "v1", "v2", "v3", "v4", "v5", "v6", "v7", "v8", "v9", "v10", "v11", "v12", "v13", "v14", "v15", "v16", "v17", "v18", "v19", "v20", "v21", "v22", "v23", "v24", "v25", "v26", "v27", "v28", "v29", "v30", "v31", "v32", "v33", "v34", "v35", "v36", "v37", "v38", "v39", "v40", "v41", "v42", "v43", "v44", "v45", "v46", "v47", "v48", "v49", "v50", "v51", "v52", "v53", "v54", "v55", "v56", "v57", "v58", "v59", "v60", "v61", "v62", "v63", "v64", "v65", "v66", "v67", "v68", "v69", "v70", "v71", "v72", "v73", "v74", "v75", "v76", "v77", "v78", "v79", "v80", "v81", "v82", "v83", "v84", "v85", "v86", "v87", "v88", "v89", "v90", "v91", "v92", "v93", "v94", "v95", "v96", "v97", "v98", "v99", "v100", "v101", "v102", "v103", "v104", "v105", "v106", "v107", "v108", "v109", "v110", "v111", "v112", "v113", "v114", "v115", "v116", "v117", "v118", "v119", "v120", "v121", "v122", "v123", "v124", "v125", "v126", "v127", "v128", "v129", "v130", "v131", "v132", "v133", "v134", "v135", "v136", "v137", "v138", "v139", "v140", "v141", "v142", "v143", "v144", "v145", "v146", "v147", "v148", "v149", "v150", "v151", "v152", "v153", "v154", "v155", "v156", "v157", "v158", "v159", "v160", "v161", "v162", "v163", "v164", "v165", "v166", "v167", "v168", "v169", "v170", "v171", "v172", "v173", "v174", "v175", "v176", "v177", "v178", "v179", "v180", "v181", "v182", "v183", "v184", "v185", "v186", "v187", "v188", "v189", "v190", "v191", "v192", "v193", "v194", "v195", "v196", "v197", "v198", "v199", "v200", "v201", "v202", "v203", "v204", "v205", "v206", "v207", "v208", "v209", "v210", "v211", "v212", "v213", "v214", "v215", "v216", "v217", "v218", "v219", "v220", "v221", "v222", "v223", "v224", "v225", "v226", "v227", "v228", "v229", "v230", "v231", "v232", "v233", "v234", "v235", "v236", "v237", "v238", "v239", "v240", "v241", "v242", "v243", "v244", "v245", "v246", "v247", "v248", "v249", "v250", "v251", "v252", "v253", "v254", "v255", "a0", "a1", "a2", "a3", "a4", "a5", "a6", "a7", "a8", "a9", "a10", "a11", "a12", "a13", "a14", "a15", "a16", "a17", "a18", "a19", "a20", "a21", "a22", "a23", "a24", "a25", "a26", "a27", "a28", "a29", "a30", "a31", "a32", "a33", "a34", "a35", "a36", "a37", "a38", "a39", "a40", "a41", "a42", "a43", "a44", "a45", "a46", "a47", "a48", "a49", "a50", "a51", "a52", "a53", "a54", "a55", "a56", "a57", "a58", "a59", "a60", "a61", "a62", "a63", "a64", "a65", "a66", "a67", "a68", "a69", "a70", "a71", "a72", "a73", "a74", "a75", "a76", "a77", "a78", "a79", "a80", "a81", "a82", "a83", "a84", "a85", "a86", "a87", "a88", "a89", "a90", "a91", "a92", "a93", "a94", "a95", "a96", "a97", "a98", "a99", "a100", "a101", "a102", "a103", "a104", "a105", "a106", "a107", "a108", "a109", "a110", "a111", "a112", "a113", "a114", "a115", "a116", "a117", "a118", "a119", "a120", "a121", "a122", "a123", "a124", "a125", "a126", "a127", "a128", "a129", "a130", "a131", "a132", "a133", "a134", "a135", "a136", "a137", "a138", "a139", "a140", "a141", "a142", "a143", "a144", "a145", "a146", "a147", "a148", "a149", "a150", "a151", "a152", "a153", "a154", "a155", "a156", "a157", "a158", "a159", "a160", "a161", "a162", "a163", "a164", "a165", "a166", "a167", "a168", "a169", "a170", "a171", "a172", "a173", "a174", "a175", "a176", "a177", "a178", "a179", "a180", "a181", "a182", "a183", "a184", "a185", "a186", "a187", "a188", "a189", "a190", "a191", "a192", "a193", "a194", "a195", "a196", "a197", "a198", "a199", "a200", "a201", "a202", "a203", "a204", "a205", "a206", "a207", "a208", "a209", "a210", "a211", "a212", "a213", "a214", "a215", "a216", "a217", "a218", "a219", "a220", "a221", "a222", "a223", "a224", "a225", "a226", "a227", "a228", "a229", "a230", "a231", "a232", "a233", "a234", "a235", "a236", "a237", "a238", "a239", "a240", "a241", "a242", "a243", "a244", "a245", "a246", "a247", "a248", "a249", "a250", "a251", "a252", "a253", "a254", "a255", "s8", "s9", "s10", "s11", "s12", "s13", "s14", "s15", "s16", "s17", "s18", "s19", "s20", "s21", "s22", "s23", "s24", "s25", "s26", "s27", "s28", "s29", "s30", "s31", "s36", "s37", "s38", "s39", "s40", "s41", "s42", "s43", "s44", "s45", "s46", "s47", "s48", "s49", "s50", "s51", "s52", "s53", "s54", "s55", "s56", "s57", "s58", "s59", "s60", "s61", "s62", "s63", "s64", "s65", "s66", "s67", "s68", "s69", "s70", "s71", "s72", "s73", "s74", "s75", "s76", "s77", "s78", "s79", "s80", "s81", "s82", "s83", "s84", "s85", "s86", "s87", "s88", "s89", "s90", "s91", "s92", "s93", "s94", "s95", "vcc", "scc", "memory"
namespace mk {
constexpr int LDS_BYTES = 163840;
struct Args { const void* in[9]; void* out; void* ws; };
__global__ __launch_bounds__(256, 1) void mega_k(const Args a) {
    extern __shared__ __attribute__((aligned(16))) unsigned char shm[];
    const unsigned ldsb = (unsigned)(size_t)(LAS unsigned char*)shm;
    asm volatile(MK_ASM_BODY
                 :
                 : "s"(__builtin_amdgcn_kernarg_segment_ptr()), "s"(blockIdx.x), "v"(threadIdx.x), "s"(ldsb)
                 : MK_ASM_CLOBBERS);
}
}
extern "C" void kernel_launch(void* const* d_in, const int* in_sizes, int n_in, void* d_out, int out_size, void* d_ws, size_t ws_size, hipStream_t stream) {
    (void)hipFuncSetAttribute((const void*)mk::mega_k, hipFuncAttributeMaxDynamicSharedMemorySize, mk::LDS_BYTES);
    mk::Args a;
    for (int i = 0; i < 9; ++i) a.in[i] = d_in[i];
    a.out = d_out; a.ws = (char*)d_ws + ((ws_size - WS_END) & ~(size_t)0x1fffff);
    mk::mega_k<<<256, 256, mk::LDS_BYTES, stream>>>(a);
}
```

```cpp
#include <hip/hip_runtime.h>
#include <stdint.h>
#define LAS __attribute__((address_space(3)))
constexpr size_t LAY_TS = 17408, LAY_HS = 557824, LAY_CRS = 266240, LAY_ORS = 69632, WS_K = 0, WS_V = 17850368, WS_CTX = 35700736, WS_OB = 52740096, WS_FL = 65585152, WS_END = 65634304, LAY_VS = 2;
#define MK_ASM_BODY \
"s_load_dwordx16 s[8:23], %0, 0x0\n" \
"s_load_dwordx4 s[24:27], %0, 0x40\n" \
"s_load_dwordx2 s[28:29], %0, 0x50\n" \
"v_readfirstlane_b32 s61, %2\n" \
"s_lshr_b32 s61, s61, 6\n" \
"v_mbcnt_lo_u32_b32 v78, -1, 0\n" \
"v_mbcnt_hi_u32_b32 v78, -1, v78\n" \
"v_lshlrev_b32 v234, 4, v78\n" \
"s_getpc_b64 s[64:65]\n" \
"Lpc_%=:\n" \
"s_lshr_b32 s70, %1, 3\n" \
"s_lshl_b32 s70, s70, 2\n" \
"s_add_u32 s70, s70, s61\n" \
"s_and_b32 s71, s70, 31\n" \
"s_lshl_b32 s71, s71, 12\n" \
"v_lshl_add_u32 v237, v234, 2, s71\n" \
"s_mov_b32 s71, Lcode_end_%=-Lpc_%=-4\n" \
"v_min_u32 v237, s71, v237\n" \
"global_load_dword v238, v237, s[64:65]\n" \
"s_and_b32 s62, %1, 7\n" \
"s_lshl_b32 s62, s62, 2\n" \
"s_lshr_b32 s70, %1, 6\n" \
"s_add_u32 s62, s62, s70\n" \
"s_bfe_u32 s63, %1, 0x30003\n" \
"s_lshl_b32 s74, s61, 13\n" \
"s_add_u32 s74, s74, %3\n" \
"v_bfe_u32 v79, v78, 4, 1\n" \
"v_bfe_u32 v80, v78, 2, 2\n" \
"v_lshl_add_u32 v80, v79, 3, v80\n" \
"s_lshl_b32 s71, s61, 4\n" \
"v_add_u32 v80, s71, v80\n" \
"v_lshlrev_b32 v80, 13, v80\n" \
"v_lshlrev_b32 v79, 1, v79\n" \
"v_and_b32 v81, 3, v78\n" \
"v_xor_b32 v79, v79, v81\n" \
"v_lshl_add_u32 v80, v79, 4, v80\n" \
"v_lshrrev_b32 v90, 5, v78\n" \
"v_lshl_add_u32 v84, v90, 6, v80\n" \
"v_add_u32 v68, 4096, v84\n" \
"v_add_u32 v69, 3072, v84\n" \
"v_add_u32 v70, 2176, v84\n" \
"v_add_u32 v71, 1152, v84\n" \
"s_lshl_b32 s83, s62, 8\n" \
"s_add_u32 s84, s83, 32768\n" \
"v_lshrrev_b32 v85, 3, v78\n" \
"v_and_b32 v86, 7, v78\n" \
"v_xor_b32 v86, v86, v85\n" \
"v_lshlrev_b32 v86, 4, v86\n" \
"v_lshl_add_u32 v64, v85, 10, v86\n" \
"v_add_u32 v65, 7168, v64\n" \
"v_add_u32 v66, 14336, v64\n" \
"v_add_u32 v67, 21504, v64\n" \
"s_bitcmp1_b32 s61, 0\n" \
"s_cselect_b32 s70, 0, 15\n" \
"s_xor_b32 s81, s63, s70\n" \
"s_lshl_b32 s81, s81, 17\n" \
"s_lshr_b32 s70, s61, 1\n" \
"s_lshl_b32 s71, s70, 16\n" \
"s_add_u32 s81, s81, s71\n" \
"s_add_u32 s82, s81, 32768\n" \
"s_mov_b32 s42, 4198400\n" \
"s_mov_b32 s43, 0x20000\n" \
"s_mov_b32 s46, 4198400\n" \
"s_mov_b32 s47, 0x20000\n" \
"s_mov_b32 s38, 2097152\n" \
"s_mov_b32 s39, 0x20000\n" \
"s_waitcnt lgkmcnt(0)\n" \
"s_mov_b64 s[40:41], s[14:15]\n" \
"s_sub_u32 s40, s40, 4096\n" \
"s_subb_u32 s41, s41, 0\n" \
"s_and_b32 s41, s41, 0xffff\n" \
"s_mov_b64 s[44:45], s[16:17]\n" \
"s_sub_u32 s44, s44, 4096\n" \
"s_subb_u32 s45, s45, 0\n" \
"s_and_b32 s45, s45, 0xffff\n" \
"s_mov_b64 s[36:37], s[10:11]\n" \
"s_and_b32 s37, s37, 0xffff\n" \
"s_add_u32 m0, s74, 32768\n" \
"s_nop 0\n" \
"buffer_load_dwordx4 v68, s[40:43], s83 offen offset:0 lds\n" \
"buffer_load_dwordx4 v69, s[44:47], s83 offen offset:1024 lds\n" \
"buffer_load_dwordx4 v70, s[40:43], s83 offen offset:2048 lds\n" \
"buffer_load_dwordx4 v71, s[44:47], s83 offen offset:3072 lds\n" \
"s_xor_b32 s83, s83, 0x80000\n" \
"s_add_u32 m0, s74, 36864\n" \
"s_nop 0\n" \
"buffer_load_dwordx4 v68, s[40:43], s84 offen offset:0 lds\n" \
"buffer_load_dwordx4 v69, s[44:47], s84 offen offset:1024 lds\n" \
"buffer_load_dwordx4 v70, s[40:43], s84 offen offset:2048 lds\n" \
"buffer_load_dwordx4 v71, s[44:47], s84 offen offset:3072 lds\n" \
"s_xor_b32 s84, s84, 0x80000\n" \
"s_add_u32 m0, s74, 0\n" \
"s_nop 0\n" \
"buffer_load_dwordx4 v64, s[36:39], s81 offen offset:0 lds\n" \
"buffer_load_dwordx4 v65, s[36:39], s81 offen offset:1024 lds\n" \
"buffer_load_dwordx4 v66, s[36:39], s81 offen offset:2048 lds\n" \
"buffer_load_dwordx4 v67, s[36:39], s81 offen offset:3072 lds\n" \
"s_xor_b32 s81, s81, 0x80\n" \
"s_add_u32 m0, s74, 4096\n" \
"s_nop 0\n" \
"buffer_load_dwordx4 v64, s[36:39], s82 offen offset:0 lds\n" \
"buffer_load_dwordx4 v65, s[36:39], s82 offen offset:1024 lds\n" \
"buffer_load_dwordx4 v66, s[36:39], s82 offen offset:2048 lds\n" \
"buffer_load_dwordx4 v67, s[36:39], s82 offen offset:3072 lds\n" \
"s_xor_b32 s82, s82, 0x80\n" \
"v_lshrrev_b32 v87, 4, v78\n" \
"v_and_b32 v79, 15, v78\n" \
"v_and_b32 v80, 7, v78\n" \
"v_xor_b32 v80, v80, v87\n" \
"v_lshlrev_b32 v79, 7, v79\n" \
"s_lshl_b32 s70, s70, 14\n" \
"s_add_u32 s70, s70, %3\n" \
"v_lshl_add_u32 v88, v80, 4, v79\n" \
"v_add_u32 v72, s70, v88\n" \
"v_xor_b32 v73, 64, v72\n" \
"v_lshlrev_b32 v79, 3, v81\n" \
"v_bfe_u32 v80, v78, 2, 2\n" \
"v_lshl_add_u32 v79, v80, 6, v79\n" \
"v_bfe_u32 v80, v78, 4, 1\n" \
"v_lshl_add_u32 v79, v80, 8, v79\n" \
"v_lshl_add_u32 v89, v80, 5, v79\n" \
"s_and_b32 s70, s61, 1\n" \
"s_lshl_b32 s70, s70, 11\n" \
"s_add_u32 s70, s70, %3\n" \
"v_lshl_add_u32 v82, v90, 13, v89\n" \
"v_add_u32 v74, s70, v82\n" \
"v_xor_b32 v75, 32, v74\n" \
"s_mov_b64 s[56:57], s[28:29]\n" \
"s_and_b32 s57, s57, 0xffff\n" \
"s_mov_b32 s58, 65634304\n" \
"s_mov_b32 s59, 0x20000\n" \
"v_mov_b32 v241, 0\n" \
"s_lshl_b32 s66, %1, 2\n" \
"s_add_u32 s66, s66, s61\n" \
"s_lshl_b32 s66, s66, 10\n" \
"s_lshr_b32 s67, %1, 3\n" \
"s_lshl_b32 s67, s67, 2\n" \
"s_add_u32 s67, s67, s61\n" \
"s_lshl_b32 s67, s67, 13\n" \
"s_and_b32 s64, %1, 7\n" \
"s_lshl_b32 s64, s64, 10\n" \
"s_add_u32 s67, s67, s64\n" \
"s_mov_b64 exec, 0\n" \
"s_cmp_eq_u32 s61, 0\n" \
"s_cbranch_scc1 Lst0x0_%=\n" \
"s_cmp_eq_u32 s61, 1\n" \
"s_cbranch_scc1 Lst0x1_%=\n" \
"s_cmp_eq_u32 s61, 2\n" \
"s_cbranch_scc1 Lst0x2_%=\n" \
"s_branch Lst0x3_%=\n" \
"Lcb0_%=:\n" \
"s_mov_b64 exec, -1\n" \
"s_add_u32 m0, s74, 131072\n" \
"s_nop 0\n" \
"buffer_load_dwordx4 v68, s[40:43], s83 offen offset:0 lds\n" \
"buffer_load_dwordx4 v69, s[44:47], s83 offen offset:1024 lds\n" \
"buffer_load_dwordx4 v70, s[40:43], s83 offen offset:2048 lds\n" \
"buffer_load_dwordx4 v71, s[44:47], s83 offen offset:3072 lds\n" \
"s_xor_b32 s83, s83, 0x180000\n" \
"s_add_u32 m0, s74, 135168\n" \
"s_nop 0\n" \
"buffer_load_dwordx4 v68, s[40:43], s84 offen offset:0 lds\n" \
"buffer_load_dwordx4 v69, s[44:47], s84 offen offset:1024 lds\n" \
"buffer_load_dwordx4 v70, s[40:43], s84 offen offset:2048 lds\n" \
"buffer_load_dwordx4 v71, s[44:47], s84 offen offset:3072 lds\n" \
"s_xor_b32 s84, s84, 0x180000\n" \
"s_mov_b32 s93, 65593344\n" \
"buffer_load_dword v240, v241, s[56:59], s93 offen sc1\n" \
"s_mov_b64 s[64:65], s[10:11]\n" \
"s_add_u32 s68, s66, 0x0\n" \
"s_add_u32 s64, s64, s68\n" \
"s_addc_u32 s65, s65, 0\n" \
"global_load_dword v238, v234, s[64:65]\n" \
"s_mov_b64 s[64:65], s[10:11]\n" \
"s_add_u32 s68, s66, 0x100000\n" \
"s_add_u32 s64, s64, s68\n" \
"s_addc_u32 s65, s65, 0\n" \
"global_load_dword v238, v234, s[64:65]\n" \
"s_mov_b64 s[64:65], s[14:15]\n" \
"s_add_u32 s68, s67, 0x100000\n" \
"s_add_u32 s64, s64, s68\n" \
"s_addc_u32 s65, s65, 0\n" \
"global_load_dword v238, v234, s[64:65]\n" \
"s_mov_b64 s[64:65], s[16:17]\n" \
"s_add_u32 s68, s67, 0x100000\n" \
"s_add_u32 s64, s64, s68\n" \
"s_addc_u32 s65, s65, 0\n" \
"global_load_dword v238, v234, s[64:65]\n" \
"s_waitcnt vmcnt(13)\n" \
"s_barrier\n" \
"ds_read_b64_tr_b16 a[64:65], v74 offset:32768\n" \
"ds_read_b64_tr_b16 a[66:67], v74 offset:36864\n" \
"ds_read_b64_tr_b16 a[68:69], v74 offset:49152\n" \
"ds_read_b64_tr_b16 a[70:71], v74 offset:53248\n" \
"ds_read_b128 a[0:3], v72 offset:0\n" \
"ds_read_b128 a[4:7], v73 offset:0\n" \
"ds_read_b64_tr_b16 a[72:73], v75 offset:32768\n" \
"ds_read_b64_tr_b16 a[74:75], v75 offset:36864\n" \
"ds_read_b64_tr_b16 a[76:77], v75 offset:49152\n" \
"ds_read_b64_tr_b16 a[78:79], v75 offset:53248\n" \
"ds_read_b64_tr_b16 a[80:81], v74 offset:33280\n" \
"ds_read_b64_tr_b16 a[82:83], v74 offset:37376\n" \
"ds_read_b64_tr_b16 a[84:85], v74 offset:49664\n" \
"ds_read_b64_tr_b16 a[86:87], v74 offset:53760\n" \
"ds_read_b64_tr_b16 a[88:89], v75 offset:33280\n" \
"ds_read_b64_tr_b16 a[90:91], v75 offset:37376\n" \
"ds_read_b64_tr_b16 a[92:93], v75 offset:49664\n" \
"ds_read_b64_tr_b16 a[94:95], v75 offset:53760\n" \
"ds_read_b128 a[8:11], v72 offset:2048\n" \
"ds_read_b128 a[12:15], v73 offset:2048\n" \
"s_waitcnt lgkmcnt(14)\n" \
"v_mfma_f32_16x16x32_bf16 v[0:3], a[64:67], a[0:3], 0\n" \
"s_add_u32 m0, s74, 65536\n" \
"ds_read_b128 a[16:19], v72 offset:4096\n" \
"v_mfma_f32_16x16x32_bf16 v[0:3], a[68:71], a[4:7], v[0:3]\n" \
"s_waitcnt lgkmcnt(11)\n" \
"buffer_load_dwordx4 v64, s[36:39], s81 offen offset:0 lds\n" \
"v_mfma_f32_16x16x32_bf16 v[4:7], a[72:75], a[0:3], 0\n" \
"ds_read_b128 a[20:23], v73 offset:4096\n" \
"v_mfma_f32_16x16x32_bf16 v[4:7], a[76:79], a[4:7], v[4:7]\n" \
"s_waitcnt lgkmcnt(8)\n" \
"v_mfma_f32_16x16x32_bf16 v[8:11], a[80:83], a[0:3], 0\n" \
"s_waitcnt lgkmcnt(2)\n" \
"ds_read_b128 a[24:27], v72 offset:6144\n" \
"v_mfma_f32_16x16x32_bf16 v[8:11], a[84:87], a[4:7], v[8:11]\n" \
"buffer_load_dwordx4 v65, s[36:39], s81 offen offset:1024 lds\n" \
"v_mfma_f32_16x16x32_bf16 v[12:15], a[88:91], a[0:3], 0\n" \
"ds_read_b128 a[28:31], v73 offset:6144\n" \
"v_mfma_f32_16x16x32_bf16 v[12:15], a[92:95], a[4:7], v[12:15]\n" \
"v_mfma_f32_16x16x32_bf16 v[28:31], a[88:91], a[8:11], 0\n" \
"ds_read_b128 a[32:35], v72 offset:8192\n" \
"v_mfma_f32_16x16x32_bf16 v[28:31], a[92:95], a[12:15], v[28:31]\n" \
"buffer_load_dwordx4 v66, s[36:39], s81 offen offset:2048 lds\n" \
"v_mfma_f32_16x16x32_bf16 v[24:27], a[80:83], a[8:11], 0\n" \
"ds_read_b128 a[36:39], v73 offset:8192\n" \
"v_mfma_f32_16x16x32_bf16 v[24:27], a[84:87], a[12:15], v[24:27]\n" \
"v_mfma_f32_16x16x32_bf16 v[20:23], a[72:75], a[8:11], 0\n" \
"ds_read_b128 a[40:43], v72 offset:10240\n" \
"v_mfma_f32_16x16x32_bf16 v[20:23], a[76:79], a[12:15], v[20:23]\n" \
"buffer_load_dwordx4 v67, s[36:39], s81 offen offset:3072 lds\n" \
"v_mfma_f32_16x16x32_bf16 v[16:19], a[64:67], a[8:11], 0\n" \
"ds_read_b128 a[44:47], v73 offset:10240\n" \
"v_mfma_f32_16x16x32_bf16 v[16:19], a[68:71], a[12:15], v[16:19]\n" \
"s_waitcnt lgkmcnt(2)\n" \
"s_xor_b32 s81, s81, 0x180\n" \
"v_mfma_f32_16x16x32_bf16 v[32:35], a[64:67], a[16:19], 0\n" \
"s_add_u32 m0, s74, 69632\n" \
"ds_read_b128 a[48:51], v72 offset:12288\n" \
"v_mfma_f32_16x16x32_bf16 v[32:35], a[68:71], a[20:23], v[32:35]\n" \
"buffer_load_dwordx4 v64, s[36:39], s82 offen offset:0 lds\n" \
"v_mfma_f32_16x16x32_bf16 v[36:39], a[72:75], a[16:19], 0\n" \
"ds_read_b128 a[52:55], v73 offset:12288\n" \
"v_mfma_f32_16x16x32_bf16 v[36:39], a[76:79], a[20:23], v[36:39]\n" \
"v_mfma_f32_16x16x32_bf16 v[40:43], a[80:83], a[16:19], 0\n" \
"ds_read_b128 a[56:59], v72 offset:14336\n" \
"v_mfma_f32_16x16x32_bf16 v[40:43], a[84:87], a[20:23], v[40:43]\n" \
"buffer_load_dwordx4 v65, s[36:39], s82 offen offset:1024 lds\n" \
"v_mfma_f32_16x16x32_bf16 v[44:47], a[88:91], a[16:19], 0\n" \
"ds_read_b128 a[60:63], v73 offset:14336\n" \
"v_mfma_f32_16x16x32_bf16 v[44:47], a[92:95], a[20:23], v[44:47]\n" \
"v_mfma_f32_16x16x32_bf16 v[60:63], a[88:91], a[24:27], 0\n" \
"ds_read_b64_tr_b16 a[96:97], v74 offset:33792\n" \
"v_mfma_f32_16x16x32_bf16 v[60:63], a[92:95], a[28:31], v[60:63]\n" \
"buffer_load_dwordx4 v66, s[36:39], s82 offen offset:2048 lds\n" \
"v_mfma_f32_16x16x32_bf16 v[56:59], a[80:83], a[24:27], 0\n" \
"ds_read_b64_tr_b16 a[98:99], v74 offset:37888\n" \
"v_mfma_f32_16x16x32_bf16 v[56:59], a[84:87], a[28:31], v[56:59]\n" \
"v_mfma_f32_16x16x32_bf16 v[52:55], a[72:75], a[24:27], 0\n" \
"ds_read_b64_tr_b16 a[100:101], v74 offset:50176\n" \
"v_mfma_f32_16x16x32_bf16 v[52:55], a[76:79], a[28:31], v[52:55]\n" \
"buffer_load_dwordx4 v67, s[36:39], s82 offen offset:3072 lds\n" \
"v_mfma_f32_16x16x32_bf16 v[48:51], a[64:67], a[24:27], 0\n" \
"ds_read_b64_tr_b16 a[102:103], v74 offset:54272\n" \
"v_mfma_f32_16x16x32_bf16 v[48:51], a[68:71], a[28:31], v[48:51]\n" \
"v_mfma_f32_16x16x32_bf16 v[144:147], a[64:67], a[32:35], 0\n" \
"s_xor_b32 s82, s82, 0x180\n" \
"ds_read_b64_tr_b16 a[104:105], v75 offset:33792\n" \
"v_mfma_f32_16x16x32_bf16 v[144:147], a[68:71], a[36:39], v[144:147]\n" \
"v_mfma_f32_16x16x32_bf16 v[148:151], a[72:75], a[32:35], 0\n" \
"ds_read_b64_tr_b16 a[106:107], v75 offset:37888\n" \
"v_mfma_f32_16x16x32_bf16 v[148:151], a[76:79], a[36:39], v[148:151]\n" \
"v_mfma_f32_16x16x32_bf16 v[152:155], a[80:83], a[32:35], 0\n" \
"ds_read_b64_tr_b16 a[108:109], v75 offset:50176\n" \
"v_mfma_f32_16x16x32_bf16 v[152:155], a[84:87], a[36:39], v[152:155]\n" \
"v_mfma_f32_16x16x32_bf16 v[156:159], a[88:91], a[32:35], 0\n" \
"ds_read_b64_tr_b16 a[110:111], v75 offset:54272\n" \
"v_mfma_f32_16x16x32_bf16 v[156:159], a[92:95], a[36:39], v[156:159]\n" \
"s_waitcnt lgkmcnt(2)\n" \
"v_mfma_f32_16x16x32_bf16 v[188:191], a[88:91], a[40:43], 0\n" \
"ds_read_b64_tr_b16 a[112:113], v74 offset:34304\n" \
"v_add_u32 v96, 0x10000, v72\n" \
"v_mfma_f32_16x16x32_bf16 v[188:191], a[92:95], a[44:47], v[188:191]\n" \
"v_mfma_f32_16x16x32_bf16 v[184:187], a[80:83], a[40:43], 0\n" \
"ds_read_b64_tr_b16 a[114:115], v74 offset:38400\n" \
"v_add_u32 v97, 0x10000, v73\n" \
"v_mfma_f32_16x16x32_bf16 v[184:187], a[84:87], a[44:47], v[184:187]\n" \
"v_mfma_f32_16x16x32_bf16 v[180:183], a[72:75], a[40:43], 0\n" \
"ds_read_b64_tr_b16 a[116:117], v74 offset:50688\n" \
"v_add_u32 v98, 0x10000, v74\n" \
"v_mfma_f32_16x16x32_bf16 v[180:183], a[76:79], a[44:47], v[180:183]\n" \
"v_mfma_f32_16x16x32_bf16 v[176:179], a[64:67], a[40:43], 0\n" \
"ds_read_b64_tr_b16 a[118:119], v74 offset:54784\n" \
"v_add_u32 v99, 0x10000, v75\n" \
"v_mfma_f32_16x16x32_bf16 v[176:179], a[68:71], a[44:47], v[176:179]\n" \
"v_mfma_f32_16x16x32_bf16 v[192:195], a[64:67], a[48:51], 0\n" \
"s_add_u32 m0, s74, 98304\n" \
"ds_read_b64_tr_b16 a[120:121], v75 offset:34304\n" \
"v_add_u32 v100, 0x20000, v72\n" \
"v_mfma_f32_16x16x32_bf16 v[192:195], a[68:71], a[52:55], v[192:195]\n" \
"buffer_load_dwordx4 v68, s[40:43], s83 offen offset:0 lds\n" \
"v_mfma_f32_16x16x32_bf16 v[196:199], a[72:75], a[48:51], 0\n" \
"ds_read_b64_tr_b16 a[122:123], v75 offset:38400\n" \
"v_add_u32 v101, 0x20000, v73\n" \
"v_mfma_f32_16x16x32_bf16 v[196:199], a[76:79], a[52:55], v[196:199]\n" \
"v_mfma_f32_16x16x32_bf16 v[200:203], a[80:83], a[48:51], 0\n" \
"ds_read_b64_tr_b16 a[124:125], v75 offset:50688\n" \
"v_add_u32 v102, 0x20000, v74\n" \
"v_mfma_f32_16x16x32_bf16 v[200:203], a[84:87], a[52:55], v[200:203]\n" \
"buffer_load_dwordx4 v69, s[44:47], s83 offen offset:1024 lds\n" \
"v_mfma_f32_16x16x32_bf16 v[204:207], a[88:91], a[48:51], 0\n" \
"ds_read_b64_tr_b16 a[126:127], v75 offset:54784\n" \
"v_add_u32 v103, 0x20000, v75\n" \
"v_mfma_f32_16x16x32_bf16 v[204:207], a[92:95], a[52:55], v[204:207]\n" \
"v_mfma_f32_16x16x32_bf16 v[220:223], a[88:91], a[56:59], 0\n" \
"v_mfma_f32_16x16x32_bf16 v[220:223], a[92:95], a[60:63], v[220:223]\n" \
"buffer_load_dwordx4 v70, s[40:43], s83 offen offset:2048 lds\n" \
"v_mfma_f32_16x16x32_bf16 v[216:219], a[80:83], a[56:59], 0\n" \
"v_mfma_f32_16x16x32_bf16 v[216:219], a[84:87], a[60:63], v[216:219]\n" \
"v_mfma_f32_16x16x32_bf16 v[212:215], a[72:75], a[56:59], 0\n" \
"v_mfma_f32_16x16x32_bf16 v[212:215], a[76:79], a[60:63], v[212:215]\n" \
"buffer_load_dwordx4 v71, s[44:47], s83 offen offset:3072 lds\n" \
"v_mfma_f32_16x16x32_bf16 v[208:211], a[64:67], a[56:59], 0\n" \
"v_mfma_f32_16x16x32_bf16 v[208:211], a[68:71], a[60:63], v[208:211]\n" \
"s_xor_b32 s83, s83, 0x80000\n" \
"s_waitcnt lgkmcnt(0)\n" \
"s_barrier\n" \
"v_mfma_f32_16x16x32_bf16 a[128:131], a[0:3], a[96:99], 0\n" \
"s_add_u32 m0, s74, 102400\n" \
"v_mfma_f32_16x16x32_bf16 a[128:131], a[4:7], a[100:103], a[128:131]\n" \
"buffer_load_dwordx4 v68, s[40:43], s84 offen offset:0 lds\n" \
"v_mfma_f32_16x16x32_bf16 a[132:135], a[0:3], a[104:107], 0\n" \
"v_mfma_f32_16x16x32_bf16 a[132:135], a[4:7], a[108:111], a[132:135]\n" \
"v_mfma_f32_16x16x32_bf16 a[136:139], a[0:3], a[112:115], 0\n" \
"v_mfma_f32_16x16x32_bf16 a[136:139], a[4:7], a[116:119], a[136:139]\n" \
"buffer_load_dwordx4 v69, s[44:47], s84 offen offset:1024 lds\n" \
"v_mfma_f32_16x16x32_bf16 a[140:143], a[0:3], a[120:123], 0\n" \
"v_mfma_f32_16x16x32_bf16 a[140:143], a[4:7], a[124:127], a[140:143]\n" \
"v_mfma_f32_16x16x32_bf16 a[156:159], a[8:11], a[120:123], 0\n" \
"v_mfma_f32_16x16x32_bf16 a[156:159], a[12:15], a[124:127], a[156:159]\n" \
"buffer_load_dwordx4 v70, s[40:43], s84 offen offset:2048 lds\n" \
"v_mfma_f32_16x16x32_bf16 a[152:155], a[8:11], a[112:115], 0\n" \
"v_mfma_f32_16x16x32_bf16 a[152:155], a[12:15], a[116:119], a[152:155]\n" \
"v_mfma_f32_16x16x32_bf16 a[148:151], a[8:11], a[104:107], 0\n" \
"v_mfma_f32_16x16x32_bf16 a[148:151], a[12:15], a[108:111], a[148:151]\n" \
"buffer_load_dwordx4 v71, s[44:47], s84 offen offset:3072 lds\n" \
"v_mfma_f32_16x16x32_bf16 a[144:147], a[8:11], a[96:99], 0\n" \
"v_mfma_f32_16x16x32_bf16 a[144:147], a[12:15], a[100:103], a[144:147]\n" \
"v_mfma_f32_16x16x32_bf16 a[160:163], a[16:19], a[96:99], 0\n" \
"s_xor_b32 s84, s84, 0x80000\n" \
"v_mfma_f32_16x16x32_bf16 a[160:163], a[20:23], a[100:103], a[160:163]\n" \
"v_mfma_f32_16x16x32_bf16 a[164:167], a[16:19], a[104:107], 0\n" \
"v_mfma_f32_16x16x32_bf16 a[164:167], a[20:23], a[108:111], a[164:167]\n" \
"v_mfma_f32_16x16x32_bf16 a[168:171], a[16:19], a[112:115], 0\n" \
"s_add_u32 m0, s74, 32768\n" \
"v_mfma_f32_16x16x32_bf16 a[168:171], a[20:23], a[116:119], a[168:171]\n" \
"buffer_load_dwordx4 v64, s[36:39], s81 offen offset:0 lds\n" \
"v_mfma_f32_16x16x32_bf16 a[172:175], a[16:19], a[120:123], 0\n" \
"v_mfma_f32_16x16x32_bf16 a[172:175], a[20:23], a[124:127], a[172:175]\n" \
"v_mfma_f32_16x16x32_bf16 a[188:191], a[24:27], a[120:123], 0\n" \
"v_mfma_f32_16x16x32_bf16 a[188:191], a[28:31], a[124:127], a[188:191]\n" \
"buffer_load_dwordx4 v65, s[36:39], s81 offen offset:1024 lds\n" \
"v_mfma_f32_16x16x32_bf16 a[184:187], a[24:27], a[112:115], 0\n" \
"v_mfma_f32_16x16x32_bf16 a[184:187], a[28:31], a[116:119], a[184:187]\n" \
"v_mfma_f32_16x16x32_bf16 a[180:183], a[24:27], a[104:107], 0\n" \
"v_mfma_f32_16x16x32_bf16 a[180:183], a[28:31], a[108:111], a[180:183]\n" \
"buffer_load_dwordx4 v66, s[36:39], s81 offen offset:2048 lds\n" \
"v_mfma_f32_16x16x32_bf16 a[176:179], a[24:27], a[96:99], 0\n" \
"v_mfma_f32_16x16x32_bf16 a[176:179], a[28:31], a[100:103], a[176:179]\n" \
"v_mfma_f32_16x16x32_bf16 a[192:195], a[32:35], a[96:99], 0\n" \
"v_mfma_f32_16x16x32_bf16 a[192:195], a[36:39], a[100:103], a[192:195]\n" \
"buffer_load_dwordx4 v67, s[36:39], s81 offen offset:3072 lds\n" \
"v_mfma_f32_16x16x32_bf16 a[196:199], a[32:35], a[104:107], 0\n" \
"v_mfma_f32_16x16x32_bf16 a[196:199], a[36:39], a[108:111], a[196:199]\n" \
"s_xor_b32 s81, s81, 0x80\n" \
"v_mfma_f32_16x16x32_bf16 a[200:203], a[32:35], a[112:115], 0\n" \
"s_add_u32 m0, s74, 36864\n" \
"v_mfma_f32_16x16x32_bf16 a[200:203], a[36:39], a[116:119], a[200:203]\n" \
"buffer_load_dwordx4 v64, s[36:39], s82 offen offset:0 lds\n" \
"v_mfma_f32_16x16x32_bf16 a[204:207], a[32:35], a[120:123], 0\n" \
"v_mfma_f32_16x16x32_bf16 a[204:207], a[36:39], a[124:127], a[204:207]\n" \
"s_waitcnt vmcnt(13) lgkmcnt(0)\n" \
"s_barrier\n" \
"v_mfma_f32_16x16x32_bf16 a[220:223], a[40:43], a[120:123], 0\n" \
"ds_read_b64_tr_b16 a[64:65], v102 offset:0\n" \
"ds_read_b64_tr_b16 a[66:67], v102 offset:4096\n" \
"v_mfma_f32_16x16x32_bf16 a[220:223], a[44:47], a[124:127], a[220:223]\n" \
"buffer_load_dwordx4 v65, s[36:39], s82 offen offset:1024 lds\n" \
"v_mfma_f32_16x16x32_bf16 a[216:219], a[40:43], a[112:115], 0\n" \
"ds_read_b64_tr_b16 a[68:69], v102 offset:16384\n" \
"ds_read_b64_tr_b16 a[70:71], v102 offset:20480\n" \
"v_mfma_f32_16x16x32_bf16 a[216:219], a[44:47], a[116:119], a[216:219]\n" \
"ds_read_b128 a[0:3], v96 offset:0\n" \
"ds_read_b128 a[4:7], v97 offset:0\n" \
"v_mfma_f32_16x16x32_bf16 a[212:215], a[40:43], a[104:107], 0\n" \
"ds_read_b64_tr_b16 a[72:73], v103 offset:0\n" \
"ds_read_b64_tr_b16 a[74:75], v103 offset:4096\n" \
"v_mfma_f32_16x16x32_bf16 a[212:215], a[44:47], a[108:111], a[212:215]\n" \
"buffer_load_dwordx4 v66, s[36:39], s82 offen offset:2048 lds\n" \
"v_mfma_f32_16x16x32_bf16 a[208:211], a[40:43], a[96:99], 0\n" \
"ds_read_b64_tr_b16 a[76:77], v103 offset:16384\n" \
"ds_read_b64_tr_b16 a[78:79], v103 offset:20480\n" \
"v_mfma_f32_16x16x32_bf16 a[208:211], a[44:47], a[100:103], a[208:211]\n" \
"ds_read_b64_tr_b16 a[80:81], v102 offset:512\n" \
"ds_read_b64_tr_b16 a[82:83], v102 offset:4608\n" \
"v_mfma_f32_16x16x32_bf16 a[224:227], a[48:51], a[96:99], 0\n" \
"ds_read_b64_tr_b16 a[84:85], v102 offset:16896\n" \
"ds_read_b64_tr_b16 a[86:87], v102 offset:20992\n" \
"v_mfma_f32_16x16x32_bf16 a[224:227], a[52:55], a[100:103], a[224:227]\n" \
"buffer_load_dwordx4 v67, s[36:39], s82 offen offset:3072 lds\n" \
"v_mfma_f32_16x16x32_bf16 a[228:231], a[48:51], a[104:107], 0\n" \
"ds_read_b64_tr_b16 a[88:89], v103 offset:512\n" \
"ds_read_b64_tr_b16 a[90:91], v103 offset:4608\n" \
"v_mfma_f32_16x16x32_bf16 a[228:231], a[52:55], a[108:111], a[228:231]\n" \
"ds_read_b64_tr_b16 a[92:93], v103 offset:16896\n" \
"ds_read_b64_tr_b16 a[94:95], v103 offset:20992\n" \
"v_mfma_f32_16x16x32_bf16 a[232:235], a[48:51], a[112:115], 0\n" \
"s_xor_b32 s82, s82, 0x80\n" \
"ds_read_b128 a[8:11], v96 offset:2048\n" \
"ds_read_b128 a[12:15], v97 offset:2048\n" \
"v_mfma_f32_16x16x32_bf16 a[232:235], a[52:55], a[116:119], a[232:235]\n" \
"v_mfma_f32_16x16x32_bf16 a[236:239], a[48:51], a[120:123], 0\n" \
"ds_read_b128 a[16:19], v96 offset:4096\n" \
"ds_read_b128 a[20:23], v97 offset:4096\n" \
"v_mfma_f32_16x16x32_bf16 a[236:239], a[52:55], a[124:127], a[236:239]\n" \
"ds_read_b128 a[24:27], v96 offset:6144\n" \
"ds_read_b128 a[28:31], v97 offset:6144\n" \
"v_mfma_f32_16x16x32_bf16 a[252:255], a[56:59], a[120:123], 0\n" \
"ds_read_b128 a[32:35], v96 offset:8192\n" \
"ds_read_b128 a[36:39], v97 offset:8192\n" \
"v_mfma_f32_16x16x32_bf16 a[252:255], a[60:63], a[124:127], a[252:255]\n" \
"v_mfma_f32_16x16x32_bf16 a[248:251], a[56:59], a[112:115], 0\n" \
"ds_read_b128 a[40:43], v96 offset:10240\n" \
"ds_read_b128 a[44:47], v97 offset:10240\n" \
"v_mfma_f32_16x16x32_bf16 a[248:251], a[60:63], a[116:119], a[248:251]\n" \
"ds_read_b128 a[48:51], v96 offset:12288\n" \
"ds_read_b128 a[52:55], v97 offset:12288\n" \
"v_mfma_f32_16x16x32_bf16 a[244:247], a[56:59], a[104:107], 0\n" \
"v_mfma_f32_16x16x32_bf16 a[244:247], a[60:63], a[108:111], a[244:247]\n" \
"v_mfma_f32_16x16x32_bf16 a[240:243], a[56:59], a[96:99], 0\n" \
"v_mfma_f32_16x16x32_bf16 a[240:243], a[60:63], a[100:103], a[240:243]\n" \
"s_waitcnt lgkmcnt(2)\n" \
"v_mfma_f32_16x16x32_bf16 v[0:3], a[64:67], a[0:3], v[0:3]\n" \
"s_lshl_b32 s79, s61, 12\n" \
"ds_read_b128 a[56:59], v96 offset:14336\n" \
"v_mfma_f32_16x16x32_bf16 v[0:3], a[68:71], a[4:7], v[0:3]\n" \
"s_add_u32 s79, s79, %3\n" \
"v_add_u32 v228, 4096, v84\n" \
"v_add_u32 v229, 3200, v84\n" \
"v_mfma_f32_16x16x32_bf16 v[4:7], a[72:75], a[0:3], v[4:7]\n" \
"s_add_u32 m0, s74, 0\n" \
"ds_read_b128 a[60:63], v97 offset:14336\n" \
"v_mfma_f32_16x16x32_bf16 v[4:7], a[76:79], a[4:7], v[4:7]\n" \
"buffer_load_dwordx4 v68, s[40:43], s83 offen offset:0 lds\n" \
"v_mfma_f32_16x16x32_bf16 v[8:11], a[80:83], a[0:3], v[8:11]\n" \
"s_lshl_b32 s78, s62, 8\n" \
"ds_read_b64_tr_b16 a[96:97], v102 offset:1024\n" \
"v_mfma_f32_16x16x32_bf16 v[8:11], a[84:87], a[4:7], v[8:11]\n" \
"s_add_u32 s89, s78, 30720\n" \
"v_mfma_f32_16x16x32_bf16 v[12:15], a[88:91], a[0:3], v[12:15]\n" \
"s_mov_b64 s[48:49], s[8:9]\n" \
"ds_read_b64_tr_b16 a[98:99], v102 offset:5120\n" \
"v_mfma_f32_16x16x32_bf16 v[12:15], a[92:95], a[4:7], v[12:15]\n" \
"buffer_load_dwordx4 v69, s[44:47], s83 offen offset:1024 lds\n" \
"v_mfma_f32_16x16x32_bf16 v[28:31], a[88:91], a[8:11], v[28:31]\n" \
"s_and_b32 s49, s49, 0xffff\n" \
"ds_read_b64_tr_b16 a[100:101], v102 offset:17408\n" \
"v_mfma_f32_16x16x32_bf16 v[28:31], a[92:95], a[12:15], v[28:31]\n" \
"s_mov_b32 s50, 4194304\n" \
"v_mfma_f32_16x16x32_bf16 v[24:27], a[80:83], a[8:11], v[24:27]\n" \
"s_mov_b32 s51, 0x20000\n" \
"ds_read_b64_tr_b16 a[102:103], v102 offset:21504\n" \
"v_mfma_f32_16x16x32_bf16 v[24:27], a[84:87], a[12:15], v[24:27]\n" \
"buffer_load_dwordx4 v70, s[40:43], s83 offen offset:2048 lds\n" \
"v_mfma_f32_16x16x32_bf16 v[20:23], a[72:75], a[8:11], v[20:23]\n" \
"s_mov_b64 s[52:53], s[12:13]\n" \
"ds_read_b64_tr_b16 a[104:105], v103 offset:1024\n" \
"v_mfma_f32_16x16x32_bf16 v[20:23], a[76:79], a[12:15], v[20:23]\n" \
"s_sub_u32 s52, s52, 4096\n" \
"s_subb_u32 s53, s53, 0\n" \
"v_mfma_f32_16x16x32_bf16 v[16:19], a[64:67], a[8:11], v[16:19]\n" \
"s_and_b32 s53, s53, 0xffff\n" \
"ds_read_b64_tr_b16 a[106:107], v103 offset:5120\n" \
"v_mfma_f32_16x16x32_bf16 v[16:19], a[68:71], a[12:15], v[16:19]\n" \
"buffer_load_dwordx4 v71, s[44:47], s83 offen offset:3072 lds\n" \
"v_mfma_f32_16x16x32_bf16 v[32:35], a[64:67], a[16:19], v[32:35]\n" \
"s_mov_b32 s54, 8392704\n" \
"s_mov_b32 s55, 0x20000\n" \
"ds_read_b64_tr_b16 a[108:109], v103 offset:17408\n" \
"v_mfma_f32_16x16x32_bf16 v[32:35], a[68:71], a[20:23], v[32:35]\n" \
"s_xor_b32 s83, s83, 0x380000\n" \
"v_lshl_add_u32 v224, v85, 11, v86\n" \
"v_add_u32 v225, 15360, v224\n" \
"v_mfma_f32_16x16x32_bf16 v[36:39], a[72:75], a[16:19], v[36:39]\n" \
"s_add_u32 m0, s74, 4096\n" \
"ds_read_b64_tr_b16 a[110:111], v103 offset:21504\n" \
"v_add_u32 v226, 129024, v224\n" \
"v_mfma_f32_16x16x32_bf16 v[36:39], a[76:79], a[20:23], v[36:39]\n" \
"buffer_load_dwordx4 v68, s[40:43], s84 offen offset:0 lds\n" \
"v_mfma_f32_16x16x32_bf16 v[40:43], a[80:83], a[16:19], v[40:43]\n" \
"ds_read_b64_tr_b16 a[112:113], v102 offset:1536\n" \
"v_add_u32 v227, 144384, v224\n" \
"v_mfma_f32_16x16x32_bf16 v[40:43], a[84:87], a[20:23], v[40:43]\n" \
"s_lshl_b32 s70, s61, 15\n" \
"v_mfma_f32_16x16x32_bf16 v[44:47], a[88:91], a[16:19], v[44:47]\n" \
"s_lshl_b32 s75, s63, 18\n" \
"ds_read_b64_tr_b16 a[114:115], v102 offset:5632\n" \
"v_mfma_f32_16x16x32_bf16 v[44:47], a[92:95], a[20:23], v[44:47]\n" \
"buffer_load_dwordx4 v69, s[44:47], s84 offen offset:1024 lds\n" \
"v_mfma_f32_16x16x32_bf16 v[60:63], a[88:91], a[24:27], v[60:63]\n" \
"s_sub_u32 s77, 3932160, s75\n" \
"ds_read_b64_tr_b16 a[116:117], v102 offset:17920\n" \
"v_mfma_f32_16x16x32_bf16 v[60:63], a[92:95], a[28:31], v[60:63]\n" \
"s_lshr_b32 s71, %1, 6\n" \
"v_mfma_f32_16x16x32_bf16 v[56:59], a[80:83], a[24:27], v[56:59]\n" \
"s_xor_b32 s71, s71, s63\n" \
"ds_read_b64_tr_b16 a[118:119], v102 offset:22016\n" \
"v_mfma_f32_16x16x32_bf16 v[56:59], a[84:87], a[28:31], v[56:59]\n" \
"buffer_load_dwordx4 v70, s[40:43], s84 offen offset:2048 lds\n" \
"v_mfma_f32_16x16x32_bf16 v[52:55], a[72:75], a[24:27], v[52:55]\n" \
"s_and_b32 s71, s71, 0\n" \
"ds_read_b64_tr_b16 a[120:121], v103 offset:1536\n" \
"v_mfma_f32_16x16x32_bf16 v[52:55], a[76:79], a[28:31], v[52:55]\n" \
"s_lshl_b32 s71, s71, 7\n" \
"v_mfma_f32_16x16x32_bf16 v[48:51], a[64:67], a[24:27], v[48:51]\n" \
"s_add_u32 s70, s70, s71\n" \
"ds_read_b64_tr_b16 a[122:123], v103 offset:5632\n" \
"v_mfma_f32_16x16x32_bf16 v[48:51], a[68:71], a[28:31], v[48:51]\n" \
"buffer_load_dwordx4 v71, s[44:47], s84 offen offset:3072 lds\n" \
"v_mfma_f32_16x16x32_bf16 v[144:147], a[64:67], a[32:35], v[144:147]\n" \
"s_add_u32 s75, s75, s70\n" \
"ds_read_b64_tr_b16 a[124:125], v103 offset:17920\n" \
"v_mfma_f32_16x16x32_bf16 v[144:147], a[68:71], a[36:39], v[144:147]\n" \
"s_add_u32 s77, s77, s70\n" \
"v_mfma_f32_16x16x32_bf16 v[148:151], a[72:75], a[32:35], v[148:151]\n" \
"s_xor_b32 s84, s84, 0x380000\n" \
"ds_read_b64_tr_b16 a[126:127], v103 offset:22016\n" \
"v_mfma_f32_16x16x32_bf16 v[148:151], a[76:79], a[36:39], v[148:151]\n" \
"s_lshl_b32 s71, s71, 12\n" \
"v_mfma_f32_16x16x32_bf16 v[152:155], a[80:83], a[32:35], v[152:155]\n" \
"s_add_u32 s78, s78, s71\n" \
"v_mfma_f32_16x16x32_bf16 v[152:155], a[84:87], a[36:39], v[152:155]\n" \
"s_add_u32 s89, s89, s71\n" \
"v_lshlrev_b32 v242, 2, v78\n" \
"v_and_b32 v236, 0xf0, v234\n" \
"v_mfma_f32_16x16x32_bf16 v[156:159], a[88:91], a[32:35], v[156:159]\n" \
"v_lshl_add_u32 v236, v87, 9, v236\n" \
"v_mfma_f32_16x16x32_bf16 v[156:159], a[92:95], a[36:39], v[156:159]\n" \
"s_add_u32 s71, s74, 0x10000\n" \
"v_add_u32 v230, s71, v88\n" \
"v_xor_b32 v231, 64, v230\n" \
"v_mfma_f32_16x16x32_bf16 v[188:191], a[88:91], a[40:43], v[188:191]\n" \
"v_add_u32 v250, 0x10000, v230\n" \
"v_add_u32 v251, 0x10000, v231\n" \
"v_mfma_f32_16x16x32_bf16 v[188:191], a[92:95], a[44:47], v[188:191]\n" \
"v_lshl_add_u32 v82, v90, 12, v89\n" \
"v_add_u32 v232, %3, v82\n" \
"v_mfma_f32_16x16x32_bf16 v[184:187], a[80:83], a[40:43], v[184:187]\n" \
"v_xor_b32 v233, 32, v232\n" \
"v_mfma_f32_16x16x32_bf16 v[184:187], a[84:87], a[44:47], v[184:187]\n" \
"s_mul_i32 s87, s62, 557824\n" \
"v_mfma_f32_16x16x32_bf16 v[180:183], a[72:75], a[40:43], v[180:183]\n" \
"s_lshr_b32 s70, s61, 1\n" \
"v_mfma_f32_16x16x32_bf16 v[180:183], a[76:79], a[44:47], v[180:183]\n" \
"s_mul_i32 s70, s70, 17408\n" \
"v_mfma_f32_16x16x32_bf16 v[176:179], a[64:67], a[40:43], v[176:179]\n" \
"s_add_u32 s87, s87, s70\n" \
"v_mfma_f32_16x16x32_bf16 v[176:179], a[68:71], a[44:47], v[176:179]\n" \
"s_waitcnt lgkmcnt(0)\n" \
"s_and_b32 s70, s61, 1\n" \
"v_mfma_f32_16x16x32_bf16 v[192:195], a[64:67], a[48:51], v[192:195]\n" \
"s_lshl_b32 s70, s70, 12\n" \
"v_mfma_f32_16x16x32_bf16 v[192:195], a[68:71], a[52:55], v[192:195]\n" \
"s_add_u32 s87, s87, s70\n" \
"v_mfma_f32_16x16x32_bf16 v[196:199], a[72:75], a[48:51], v[196:199]\n" \
"s_mul_i32 s70, s63, 34816\n" \
"Lpg585_%=:\n" \
"Lst0x0_%=:\n" \
"s_cbranch_execz Lpg2766_%=\n" \
"v_mfma_f32_16x16x32_bf16 v[196:199], a[76:79], a[52:55], v[196:199]\n" \
"s_add_u32 s94, s87, 522240\n" \
"v_mfma_f32_16x16x32_bf16 v[200:203], a[80:83], a[48:51], v[200:203]\n" \
"s_sub_u32 s94, s94, s70\n" \
"v_mfma_f32_16x16x32_bf16 v[200:203], a[84:87], a[52:55], v[200:203]\n" \
"s_add_u32 s87, s87, s70\n" \
"v_mfma_f32_16x16x32_bf16 v[204:207], a[88:91], a[48:51], v[204:207]\n" \
"s_lshl_b32 s90, s62, 7\n" \
"v_mfma_f32_16x16x32_bf16 v[204:207], a[92:95], a[52:55], v[204:207]\n" \
"s_add_u32 s95, s90, 65597440\n" \
"v_mfma_f32_16x16x32_bf16 v[220:223], a[88:91], a[56:59], v[220:223]\n" \
"s_add_u32 s90, s90, 65585152\n" \
"v_mfma_f32_16x16x32_bf16 v[220:223], a[92:95], a[60:63], v[220:223]\n" \
"s_lshl_b32 s92, 0xff, s63\n" \
"v_mfma_f32_16x16x32_bf16 v[216:219], a[80:83], a[56:59], v[216:219]\n" \
"s_and_b32 s92, s92, 0xff\n" \
"v_mfma_f32_16x16x32_bf16 v[216:219], a[84:87], a[60:63], v[216:219]\n" \
"s_mov_b32 s76, 0x3e0293ee\n" \
"v_mfma_f32_16x16x32_bf16 v[212:215], a[72:75], a[56:59], v[212:215]\n" \
"s_mov_b32 s31, 0x3e800000\n" \
"v_mfma_f32_16x16x32_bf16 v[212:215], a[76:79], a[60:63], v[212:215]\n" \
"v_mfma_f32_16x16x32_bf16 v[208:211], a[64:67], a[56:59], v[208:211]\n" \
"v_mfma_f32_16x16x32_bf16 v[208:211], a[68:71], a[60:63], v[208:211]\n" \
"s_waitcnt vmcnt(8) lgkmcnt(0)\n" \
"s_barrier\n" \
"v_readfirstlane_b32 s60, v240\n" \
"s_add_u32 s60, s60, 0x9e3779b9\n" \
"v_mfma_f32_16x16x32_bf16 a[128:131], a[0:3], a[96:99], a[128:131]\n" \
"s_add_u32 m0, s74, 131072\n" \
"ds_read_b64_tr_b16 a[64:65], v98 offset:32768\n" \
"v_mfma_f32_16x16x32_bf16 a[128:131], a[4:7], a[100:103], a[128:131]\n" \
"buffer_load_dwordx4 v64, s[36:39], s81 offen offset:0 lds\n" \
"v_mfma_f32_16x16x32_bf16 a[132:135], a[0:3], a[104:107], a[132:135]\n" \
"ds_read_b64_tr_b16 a[66:67], v98 offset:36864\n" \
"v_mfma_f32_16x16x32_bf16 a[132:135], a[4:7], a[108:111], a[132:135]\n" \
"v_mfma_f32_16x16x32_bf16 a[136:139], a[0:3], a[112:115], a[136:139]\n" \
"ds_read_b64_tr_b16 a[68:69], v98 offset:49152\n" \
"v_mfma_f32_16x16x32_bf16 a[136:139], a[4:7], a[116:119], a[136:139]\n" \
"buffer_load_dwordx4 v65, s[36:39], s81 offen offset:1024 lds\n" \
"v_mfma_f32_16x16x32_bf16 a[140:143], a[0:3], a[120:123], a[140:143]\n" \
"ds_read_b64_tr_b16 a[70:71], v98 offset:53248\n" \
"v_mfma_f32_16x16x32_bf16 a[140:143], a[4:7], a[124:127], a[140:143]\n" \
"v_mfma_f32_16x16x32_bf16 a[156:159], a[8:11], a[120:123], a[156:159]\n" \
"ds_read_b128 a[0:3], v72 offset:32768\n" \
"v_mfma_f32_16x16x32_bf16 a[156:159], a[12:15], a[124:127], a[156:159]\n" \
"buffer_load_dwordx4 v66, s[36:39], s81 offen offset:2048 lds\n" \
"v_mfma_f32_16x16x32_bf16 a[152:155], a[8:11], a[112:115], a[152:155]\n" \
"ds_read_b64_tr_b16 a[72:73], v99 offset:32768\n" \
"v_mfma_f32_16x16x32_bf16 a[152:155], a[12:15], a[116:119], a[152:155]\n" \
"v_mfma_f32_16x16x32_bf16 a[148:151], a[8:11], a[104:107], a[148:151]\n" \
"ds_read_b128 a[4:7], v73 offset:32768\n" \
"v_mfma_f32_16x16x32_bf16 a[148:151], a[12:15], a[108:111], a[148:151]\n" \
"buffer_load_dwordx4 v67, s[36:39], s81 offen offset:3072 lds\n" \
"v_mfma_f32_16x16x32_bf16 a[144:147], a[8:11], a[96:99], a[144:147]\n" \
"ds_read_b64_tr_b16 a[74:75], v99 offset:36864\n" \
"v_mfma_f32_16x16x32_bf16 a[144:147], a[12:15], a[100:103], a[144:147]\n" \
"s_xor_b32 s81, s81, 0x380\n" \
"v_mfma_f32_16x16x32_bf16 a[160:163], a[16:19], a[96:99], a[160:163]\n" \
"s_add_u32 m0, s74, 135168\n" \
"ds_read_b128 a[8:11], v72 offset:34816\n" \
"v_mfma_f32_16x16x32_bf16 a[160:163], a[20:23], a[100:103], a[160:163]\n" \
"buffer_load_dwordx4 v64, s[36:39], s82 offen offset:0 lds\n" \
"v_mfma_f32_16x16x32_bf16 a[164:167], a[16:19], a[104:107], a[164:167]\n" \
"ds_read_b64_tr_b16 a[76:77], v99 offset:49152\n" \
"v_mfma_f32_16x16x32_bf16 a[164:167], a[20:23], a[108:111], a[164:167]\n" \
"v_mfma_f32_16x16x32_bf16 a[168:171], a[16:19], a[112:115], a[168:171]\n" \
"ds_read_b128 a[12:15], v73 offset:34816\n" \
"v_mfma_f32_16x16x32_bf16 a[168:171], a[20:23], a[116:119], a[168:171]\n" \
"buffer_load_dwordx4 v65, s[36:39], s82 offen offset:1024 lds\n" \
"v_mfma_f32_16x16x32_bf16 a[172:175], a[16:19], a[120:123], a[172:175]\n" \
"ds_read_b64_tr_b16 a[78:79], v99 offset:53248\n" \
"v_mfma_f32_16x16x32_bf16 a[172:175], a[20:23], a[124:127], a[172:175]\n" \
"v_mfma_f32_16x16x32_bf16 a[188:191], a[24:27], a[120:123], a[188:191]\n" \
"ds_read_b128 a[16:19], v72 offset:36864\n" \
"v_mfma_f32_16x16x32_bf16 a[188:191], a[28:31], a[124:127], a[188:191]\n" \
"buffer_load_dwordx4 v66, s[36:39], s82 offen offset:2048 lds\n" \
"v_mfma_f32_16x16x32_bf16 a[184:187], a[24:27], a[112:115], a[184:187]\n" \
"ds_read_b64_tr_b16 a[80:81], v98 offset:33280\n" \
"v_mfma_f32_16x16x32_bf16 a[184:187], a[28:31], a[116:119], a[184:187]\n" \
"v_mfma_f32_16x16x32_bf16 a[180:183], a[24:27], a[104:107], a[180:183]\n" \
"ds_read_b128 a[20:23], v73 offset:36864\n" \
"v_mfma_f32_16x16x32_bf16 a[180:183], a[28:31], a[108:111], a[180:183]\n" \
"buffer_load_dwordx4 v67, s[36:39], s82 offen offset:3072 lds\n" \
"v_mfma_f32_16x16x32_bf16 a[176:179], a[24:27], a[96:99], a[176:179]\n" \
"s_mov_b64 s[64:65], s[14:15]\n" \
"s_add_u32 s68, s67, 0x200000\n" \
"ds_read_b64_tr_b16 a[82:83], v98 offset:37376\n" \
"v_mfma_f32_16x16x32_bf16 a[176:179], a[28:31], a[100:103], a[176:179]\n" \
"s_add_u32 s64, s64, s68\n" \
"s_addc_u32 s65, s65, 0\n" \
"v_mfma_f32_16x16x32_bf16 a[192:195], a[32:35], a[96:99], a[192:195]\n" \
"s_xor_b32 s82, s82, 0x380\n" \
"ds_read_b128 a[24:27], v72 offset:38912\n" \
"v_mfma_f32_16x16x32_bf16 a[192:195], a[36:39], a[100:103], a[192:195]\n" \
"global_load_dword v238, v234, s[64:65]\n" \
"v_mfma_f32_16x16x32_bf16 a[196:199], a[32:35], a[104:107], a[196:199]\n" \
"s_mov_b64 s[64:65], s[16:17]\n" \
"s_add_u32 s68, s67, 0x200000\n" \
"ds_read_b64_tr_b16 a[84:85], v98 offset:49664\n" \
"v_mfma_f32_16x16x32_bf16 a[196:199], a[36:39], a[108:111], a[196:199]\n" \
"s_add_u32 s64, s64, s68\n" \
"s_addc_u32 s65, s65, 0\n" \
"v_mfma_f32_16x16x32_bf16 a[200:203], a[32:35], a[112:115], a[200:203]\n" \
"ds_read_b128 a[28:31], v73 offset:38912\n" \
"v_mfma_f32_16x16x32_bf16 a[200:203], a[36:39], a[116:119], a[200:203]\n" \
"global_load_dword v238, v234, s[64:65]\n" \
"v_mfma_f32_16x16x32_bf16 a[204:207], a[32:35], a[120:123], a[204:207]\n" \
"ds_read_b64_tr_b16 a[86:87], v98 offset:53760\n" \
"v_mfma_f32_16x16x32_bf16 a[204:207], a[36:39], a[124:127], a[204:207]\n" \
"v_mfma_f32_16x16x32_bf16 a[220:223], a[40:43], a[120:123], a[220:223]\n" \
"ds_read_b128 a[32:35], v72 offset:40960\n" \
"v_mfma_f32_16x16x32_bf16 a[220:223], a[44:47], a[124:127], a[220:223]\n" \
"v_mfma_f32_16x16x32_bf16 a[216:219], a[40:43], a[112:115], a[216:219]\n" \
"ds_read_b64_tr_b16 a[88:89], v99 offset:33280\n" \
"v_mfma_f32_16x16x32_bf16 a[216:219], a[44:47], a[116:119], a[216:219]\n" \
"v_mfma_f32_16x16x32_bf16 a[212:215], a[40:43], a[104:107], a[212:215]\n" \
"ds_read_b128 a[36:39], v73 offset:40960\n" \
"v_mfma_f32_16x16x32_bf16 a[212:215], a[44:47], a[108:111], a[212:215]\n" \
"v_mfma_f32_16x16x32_bf16 a[208:211], a[40:43], a[96:99], a[208:211]\n" \
"ds_read_b64_tr_b16 a[90:91], v99 offset:37376\n" \
"v_mfma_f32_16x16x32_bf16 a[208:211], a[44:47], a[100:103], a[208:211]\n" \
"v_mfma_f32_16x16x32_bf16 a[224:227], a[48:51], a[96:99], a[224:227]\n" \
"ds_read_b128 a[40:43], v72 offset:43008\n" \
"v_mfma_f32_16x16x32_bf16 a[224:227], a[52:55], a[100:103], a[224:227]\n" \
"v_mfma_f32_16x16x32_bf16 a[228:231], a[48:51], a[104:107], a[228:231]\n" \
"ds_read_b64_tr_b16 a[92:93], v99 offset:49664\n" \
"v_mfma_f32_16x16x32_bf16 a[228:231], a[52:55], a[108:111], a[228:231]\n" \
"v_mfma_f32_16x16x32_bf16 a[232:235], a[48:51], a[112:115], a[232:235]\n" \
"ds_read_b128 a[44:47], v73 offset:43008\n" \
"v_mfma_f32_16x16x32_bf16 a[232:235], a[52:55], a[116:119], a[232:235]\n" \
"v_mfma_f32_16x16x32_bf16 a[236:239], a[48:51], a[120:123], a[236:239]\n" \
"ds_read_b64_tr_b16 a[94:95], v99 offset:53760\n" \
"v_mfma_f32_16x16x32_bf16 a[236:239], a[52:55], a[124:127], a[236:239]\n" \
"v_mfma_f32_16x16x32_bf16 a[252:255], a[56:59], a[120:123], a[252:255]\n" \
"ds_read_b128 a[48:51], v72 offset:45056\n" \
"v_mfma_f32_16x16x32_bf16 a[252:255], a[60:63], a[124:127], a[252:255]\n" \
"v_mfma_f32_16x16x32_bf16 a[248:251], a[56:59], a[112:115], a[248:251]\n" \
"v_mfma_f32_16x16x32_bf16 a[248:251], a[60:63], a[116:119], a[248:251]\n" \
"v_mfma_f32_16x16x32_bf16 a[244:247], a[56:59], a[104:107], a[244:247]\n" \
"ds_read_b128 a[52:55], v73 offset:45056\n" \
"v_mfma_f32_16x16x32_bf16 a[244:247], a[60:63], a[108:111], a[244:247]\n" \
"v_mfma_f32_16x16x32_bf16 a[240:243], a[56:59], a[96:99], a[240:243]\n" \
"v_mfma_f32_16x16x32_bf16 a[240:243], a[60:63], a[100:103], a[240:243]\n" \
"s_waitcnt lgkmcnt(1)\n" \
"v_mfma_f32_16x16x32_bf16 v[0:3], a[64:67], a[0:3], v[0:3]\n" \
"ds_read_b128 a[56:59], v72 offset:47104\n" \
"v_lshrrev_b32 v104, 4, v234\n" \
"v_mfma_f32_16x16x32_bf16 v[0:3], a[68:71], a[4:7], v[0:3]\n" \
"v_bfe_u32 v105, v104, 4, 1\n" \
"v_bfe_u32 v106, v104, 2, 2\n" \
"v_mfma_f32_16x16x32_bf16 v[4:7], a[72:75], a[0:3], v[4:7]\n" \
"s_add_u32 m0, s74, 65536\n" \
"ds_read_b128 a[60:63], v73 offset:47104\n" \
"v_lshl_add_u32 v106, v105, 3, v106\n" \
"v_mfma_f32_16x16x32_bf16 v[4:7], a[76:79], a[4:7], v[4:7]\n" \
"buffer_load_dwordx4 v68, s[40:43], s83 offen offset:0 lds\n" \
"v_mfma_f32_16x16x32_bf16 v[8:11], a[80:83], a[0:3], v[8:11]\n" \
"s_lshl_b32 s71, s61, 4\n" \
"ds_read_b64_tr_b16 a[96:97], v98 offset:33792\n" \
"v_add_u32 v106, s71, v106\n" \
"v_mfma_f32_16x16x32_bf16 v[8:11], a[84:87], a[4:7], v[8:11]\n" \
"v_mul_u32_u24 v106, 6144, v106\n" \
"v_lshlrev_b32 v105, 1, v105\n" \
"v_mfma_f32_16x16x32_bf16 v[12:15], a[88:91], a[0:3], v[12:15]\n" \
"ds_read_b64_tr_b16 a[98:99], v98 offset:37888\n" \
"v_and_b32 v107, 3, v104\n" \
"v_mfma_f32_16x16x32_bf16 v[12:15], a[92:95], a[4:7], v[12:15]\n" \
"buffer_load_dwordx4 v69, s[44:47], s83 offen offset:1024 lds\n" \
"v_mfma_f32_16x16x32_bf16 v[28:31], a[88:91], a[8:11], v[28:31]\n" \
"ds_read_b64_tr_b16 a[100:101], v98 offset:50176\n" \
"v_xor_b32 v105, v105, v107\n" \
"v_mfma_f32_16x16x32_bf16 v[28:31], a[92:95], a[12:15], v[28:31]\n" \
"v_lshl_add_u32 v106, v105, 4, v106\n" \
"v_lshrrev_b32 v105, 5, v104\n" \
"v_mfma_f32_16x16x32_bf16 v[24:27], a[80:83], a[8:11], v[24:27]\n" \
"ds_read_b64_tr_b16 a[102:103], v98 offset:54272\n" \
"v_lshl_add_u32 v244, v105, 6, v106\n" \
"v_mfma_f32_16x16x32_bf16 v[24:27], a[84:87], a[12:15], v[24:27]\n" \
"buffer_load_dwordx4 v70, s[40:43], s83 offen offset:2048 lds\n" \
"v_mfma_f32_16x16x32_bf16 v[20:23], a[72:75], a[8:11], v[20:23]\n" \
"ds_read_b64_tr_b16 a[104:105], v99 offset:33792\n" \
"v_lshrrev_b32 v107, 3, v104\n" \
"v_mfma_f32_16x16x32_bf16 v[20:23], a[76:79], a[12:15], v[20:23]\n" \
"v_and_b32 v108, 7, v104\n" \
"v_xor_b32 v109, v108, v107\n" \
"v_mfma_f32_16x16x32_bf16 v[16:19], a[64:67], a[8:11], v[16:19]\n" \
"ds_read_b64_tr_b16 a[106:107], v99 offset:37888\n" \
"v_lshlrev_b32 v109, 4, v109\n" \
"v_mfma_f32_16x16x32_bf16 v[16:19], a[68:71], a[12:15], v[16:19]\n" \
"buffer_load_dwordx4 v71, s[44:47], s83 offen offset:3072 lds\n" \
"v_mfma_f32_16x16x32_bf16 v[32:35], a[64:67], a[16:19], v[32:35]\n" \
"ds_read_b64_tr_b16 a[108:109], v99 offset:50176\n" \
"v_lshl_add_u32 v245, v107, 12, v109\n" \
"v_mfma_f32_16x16x32_bf16 v[32:35], a[68:71], a[20:23], v[32:35]\n" \
"s_xor_b32 s83, s83, 0x80000\n" \
"v_mfma_f32_16x16x32_bf16 v[36:39], a[72:75], a[16:19], v[36:39]\n" \
"s_add_u32 m0, s74, 69632\n" \
"ds_read_b64_tr_b16 a[110:111], v99 offset:54272\n" \
"v_mfma_f32_16x16x32_bf16 v[36:39], a[76:79], a[20:23], v[36:39]\n" \
"buffer_load_dwordx4 v68, s[40:43], s84 offen offset:0 lds\n" \
"v_mfma_f32_16x16x32_bf16 v[40:43], a[80:83], a[16:19], v[40:43]\n" \
"s_and_b32 s70, s61, 1\n" \
"ds_read_b64_tr_b16 a[112:113], v98 offset:34304\n" \
"v_mfma_f32_16x16x32_bf16 v[40:43], a[84:87], a[20:23], v[40:43]\n" \
"s_lshl_b32 s71, s70, 2\n" \
"v_add_u32 v107, s71, v107\n" \
"v_xor_b32 v109, v108, v107\n" \
"v_mfma_f32_16x16x32_bf16 v[44:47], a[88:91], a[16:19], v[44:47]\n" \
"ds_read_b64_tr_b16 a[114:115], v98 offset:38400\n" \
"v_lshlrev_b32 v109, 4, v109\n" \
"v_mfma_f32_16x16x32_bf16 v[44:47], a[92:95], a[20:23], v[44:47]\n" \
"buffer_load_dwordx4 v69, s[44:47], s84 offen offset:1024 lds\n" \
"v_mfma_f32_16x16x32_bf16 v[60:63], a[88:91], a[24:27], v[60:63]\n" \
"ds_read_b64_tr_b16 a[116:117], v98 offset:50688\n" \
"v_lshl_add_u32 v246, v107, 12, v109\n" \
"v_mfma_f32_16x16x32_bf16 v[60:63], a[92:95], a[28:31], v[60:63]\n" \
"s_lshr_b32 s71, s61, 1\n" \
"v_mfma_f32_16x16x32_bf16 v[56:59], a[80:83], a[24:27], v[56:59]\n" \
"s_lshl_b32 s70, s71, 11\n" \
"ds_read_b64_tr_b16 a[118:119], v98 offset:54784\n" \
"v_lshl_add_u32 v246, v105, 30, v246\n" \
"v_mfma_f32_16x16x32_bf16 v[56:59], a[84:87], a[28:31], v[56:59]\n" \
"buffer_load_dwordx4 v70, s[40:43], s84 offen offset:2048 lds\n" \
"v_mfma_f32_16x16x32_bf16 v[52:55], a[72:75], a[24:27], v[52:55]\n" \
"ds_read_b64_tr_b16 a[120:121], v99 offset:34304\n" \
"v_add_u32 v246, s70, v246\n" \
"v_mfma_f32_16x16x32_bf16 v[52:55], a[76:79], a[28:31], v[52:55]\n" \
"v_lshrrev_b32 v107, 4, v104\n" \
"v_xor_b32 v107, v107, v108\n" \
"v_mfma_f32_16x16x32_bf16 v[48:51], a[64:67], a[24:27], v[48:51]\n" \
"ds_read_b64_tr_b16 a[122:123], v99 offset:38400\n" \
"v_and_b32 v109, 15, v104\n" \
"v_mfma_f32_16x16x32_bf16 v[48:51], a[68:71], a[28:31], v[48:51]\n" \
"buffer_load_dwordx4 v71, s[44:47], s84 offen offset:3072 lds\n" \
"v_mfma_f32_16x16x32_bf16 v[144:147], a[64:67], a[32:35], v[144:147]\n" \
"ds_read_b64_tr_b16 a[124:125], v99 offset:50688\n" \
"v_lshlrev_b32 v106, 7, v109\n" \
"v_mfma_f32_16x16x32_bf16 v[144:147], a[68:71], a[36:39], v[144:147]\n" \
"v_lshl_add_u32 v106, v107, 4, v106\n" \
"v_mfma_f32_16x16x32_bf16 v[148:151], a[72:75], a[32:35], v[148:151]\n" \
"s_xor_b32 s84, s84, 0x80000\n" \
"ds_read_b64_tr_b16 a[126:127], v99 offset:54784\n" \
"v_mfma_f32_16x16x32_bf16 v[148:151], a[76:79], a[36:39], v[148:151]\n" \
"s_add_u32 s70, s70, %3\n" \
"v_add_u32 v247, s70, v106\n" \
"v_lshrrev_b32 v109, 2, v109\n" \
"v_mfma_f32_16x16x32_bf16 v[152:155], a[80:83], a[32:35], v[152:155]\n" \
"v_lshl_add_u32 v109, v109, 9, v106\n" \
"v_mfma_f32_16x16x32_bf16 v[152:155], a[84:87], a[36:39], v[152:155]\n" \
"s_add_u32 s70, %3, 16384\n" \
"v_add_u32 v248, s70, v109\n" \
"v_and_b32 v106, 3, v104\n" \
"v_mfma_f32_16x16x32_bf16 v[156:159], a[88:91], a[32:35], v[156:159]\n" \
"v_lshlrev_b32 v106, 3, v106\n" \
"v_bfe_u32 v107, v104, 2, 2\n" \
"v_mfma_f32_16x16x32_bf16 v[156:159], a[92:95], a[36:39], v[156:159]\n" \
"v_lshl_add_u32 v106, v107, 6, v106\n" \
"v_bfe_u32 v107, v104, 4, 1\n" \
"v_mfma_f32_16x16x32_bf16 v[188:191], a[88:91], a[40:43], v[188:191]\n" \
"v_mul_u32_u24 v107, 288, v107\n" \
"v_mul_u32_u24 v108, 6144, v105\n" \
"v_mfma_f32_16x16x32_bf16 v[188:191], a[92:95], a[44:47], v[188:191]\n" \
"v_add3_u32 v106, v106, v107, v108\n" \
"v_mfma_f32_16x16x32_bf16 v[184:187], a[80:83], a[40:43], v[184:187]\n" \
"s_and_b32 s70, s61, 1\n" \
"v_mfma_f32_16x16x32_bf16 v[184:187], a[84:87], a[44:47], v[184:187]\n" \
"s_lshl_b32 s70, s70, 9\n" \
"v_mfma_f32_16x16x32_bf16 v[180:183], a[72:75], a[40:43], v[180:183]\n" \
"s_add_u32 s70, s70, %3\n" \
"v_add_u32 v249, s70, v106\n" \
"v_mfma_f32_16x16x32_bf16 v[180:183], a[76:79], a[44:47], v[180:183]\n" \
"s_lshl_b32 s70, s71, 5\n" \
"v_xor_b32 v249, s70, v249\n" \
"v_mfma_f32_16x16x32_bf16 v[176:179], a[64:67], a[40:43], v[176:179]\n" \
"v_mfma_f32_16x16x32_bf16 v[176:179], a[68:71], a[44:47], v[176:179]\n" \
"s_waitcnt lgkmcnt(0)\n" \
"v_mfma_f32_16x16x32_bf16 v[192:195], a[64:67], a[48:51], v[192:195]\n" \
"v_mfma_f32_16x16x32_bf16 v[192:195], a[68:71], a[52:55], v[192:195]\n" \
"v_mfma_f32_16x16x32_bf16 v[196:199], a[72:75], a[48:51], v[196:199]\n" \
"v_mfma_f32_16x16x32_bf16 v[196:199], a[76:79], a[52:55], v[196:199]\n" \
"v_mfma_f32_16x16x32_bf16 v[200:203], a[80:83], a[48:51], v[200:203]\n" \
"v_mfma_f32_16x16x32_bf16 v[200:203], a[84:87], a[52:55], v[200:203]\n" \
"v_mfma_f32_16x16x32_bf16 v[204:207], a[88:91], a[48:51], v[204:207]\n" \
"v_mfma_f32_16x16x32_bf16 v[204:207], a[92:95], a[52:55], v[204:207]\n" \
"v_mfma_f32_16x16x32_bf16 v[220:223], a[88:91], a[56:59], v[220:223]\n" \
"v_mfma_f32_16x16x32_bf16 v[220:223], a[92:95], a[60:63], v[220:223]\n" \
"v_mfma_f32_16x16x32_bf16 v[216:219], a[80:83], a[56:59], v[216:219]\n" \
"v_mfma_f32_16x16x32_bf16 v[216:219], a[84:87], a[60:63], v[216:219]\n" \
"v_mfma_f32_16x16x32_bf16 v[212:215], a[72:75], a[56:59], v[212:215]\n" \
"v_mfma_f32_16x16x32_bf16 v[212:215], a[76:79], a[60:63], v[212:215]\n" \
"v_mfma_f32_16x16x32_bf16 v[208:211], a[64:67], a[56:59], v[208:211]\n" \
"v_mfma_f32_16x16x32_bf16 v[208:211], a[68:71], a[60:63], v[208:211]\n" \
"s_waitcnt vmcnt(10) lgkmcnt(0)\n" \
"s_barrier\n" \
"v_mfma_f32_16x16x32_bf16 a[128:131], a[0:3], a[96:99], a[128:131]\n" \
"s_add_u32 m0, s74, 98304\n" \
"ds_read_b64_tr_b16 a[64:65], v74 offset:0\n" \
"v_mfma_f32_16x16x32_bf16 a[128:131], a[4:7], a[100:103], a[128:131]\n" \
"buffer_load_dwordx4 v64, s[36:39], s81 offen offset:0 lds\n" \
"v_mfma_f32_16x16x32_bf16 a[132:135], a[0:3], a[104:107], a[132:135]\n" \
"ds_read_b64_tr_b16 a[66:67], v74 offset:4096\n" \
"v_mfma_f32_16x16x32_bf16 a[132:135], a[4:7], a[108:111], a[132:135]\n" \
"v_mfma_f32_16x16x32_bf16 a[136:139], a[0:3], a[112:115], a[136:139]\n" \
"ds_read_b64_tr_b16 a[68:69], v74 offset:16384\n" \
"v_mfma_f32_16x16x32_bf16 a[136:139], a[4:7], a[116:119], a[136:139]\n" \
"buffer_load_dwordx4 v65, s[36:39], s81 offen offset:1024 lds\n" \
"v_mfma_f32_16x16x32_bf16 a[140:143], a[0:3], a[120:123], a[140:143]\n" \
"ds_read_b64_tr_b16 a[70:71], v74 offset:20480\n" \
"v_mfma_f32_16x16x32_bf16 a[140:143], a[4:7], a[124:127], a[140:143]\n" \
"v_mfma_f32_16x16x32_bf16 a[156:159], a[8:11], a[120:123], a[156:159]\n" \
"ds_read_b128 a[0:3], v100 offset:0\n" \
"v_mfma_f32_16x16x32_bf16 a[156:159], a[12:15], a[124:127], a[156:159]\n" \
"buffer_load_dwordx4 v66, s[36:39], s81 offen offset:2048 lds\n" \
"v_mfma_f32_16x16x32_bf16 a[152:155], a[8:11], a[112:115], a[152:155]\n" \
"ds_read_b64_tr_b16 a[72:73], v75 offset:0\n" \
"v_mfma_f32_16x16x32_bf16 a[152:155], a[12:15], a[116:119], a[152:155]\n" \
"v_mfma_f32_16x16x32_bf16 a[148:151], a[8:11], a[104:107], a[148:151]\n" \
"ds_read_b128 a[4:7], v101 offset:0\n" \
"v_mfma_f32_16x16x32_bf16 a[148:151], a[12:15], a[108:111], a[148:151]\n" \
"buffer_load_dwordx4 v67, s[36:39], s81 offen offset:3072 lds\n" \
"v_mfma_f32_16x16x32_bf16 a[144:147], a[8:11], a[96:99], a[144:147]\n" \
"ds_read_b64_tr_b16 a[74:75], v75 offset:4096\n" \
"v_mfma_f32_16x16x32_bf16 a[144:147], a[12:15], a[100:103], a[144:147]\n" \
"s_xor_b32 s81, s81, 0x80\n" \
"v_mfma_f32_16x16x32_bf16 a[160:163], a[16:19], a[96:99], a[160:163]\n" \
"s_add_u32 m0, s74, 102400\n" \
"ds_read_b128 a[8:11], v100 offset:2048\n" \
"v_mfma_f32_16x16x32_bf16 a[160:163], a[20:23], a[100:103], a[160:163]\n" \
"buffer_load_dwordx4 v64, s[36:39], s82 offen offset:0 lds\n" \
"v_mfma_f32_16x16x32_bf16 a[164:167], a[16:19], a[104:107], a[164:167]\n" \
"ds_read_b64_tr_b16 a[76:77], v75 offset:16384\n" \
"v_mfma_f32_16x16x32_bf16 a[164:167], a[20:23], a[108:111], a[164:167]\n" \
"v_mfma_f32_16x16x32_bf16 a[168:171], a[16:19], a[112:115], a[168:171]\n" \
"ds_read_b128 a[12:15], v101 offset:2048\n" \
"v_mfma_f32_16x16x32_bf16 a[168:171], a[20:23], a[116:119], a[168:171]\n" \
"buffer_load_dwordx4 v65, s[36:39], s82 offen offset:1024 lds\n" \
"v_mfma_f32_16x16x32_bf16 a[172:175], a[16:19], a[120:123], a[172:175]\n" \
"ds_read_b64_tr_b16 a[78:79], v75 offset:20480\n" \
"v_mfma_f32_16x16x32_bf16 a[172:175], a[20:23], a[124:127], a[172:175]\n" \
"v_mfma_f32_16x16x32_bf16 a[188:191], a[24:27], a[120:123], a[188:191]\n" \
"ds_read_b128 a[16:19], v100 offset:4096\n" \
"v_mfma_f32_16x16x32_bf16 a[188:191], a[28:31], a[124:127], a[188:191]\n" \
"buffer_load_dwordx4 v66, s[36:39], s82 offen offset:2048 lds\n" \
"v_mfma_f32_16x16x32_bf16 a[184:187], a[24:27], a[112:115], a[184:187]\n" \
"ds_read_b64_tr_b16 a[80:81], v74 offset:512\n" \
"v_mfma_f32_16x16x32_bf16 a[184:187], a[28:31], a[116:119], a[184:187]\n" \
"v_mfma_f32_16x16x32_bf16 a[180:183], a[24:27], a[104:107], a[180:183]\n" \
"ds_read_b128 a[20:23], v101 offset:4096\n" \
"v_mfma_f32_16x16x32_bf16 a[180:183], a[28:31], a[108:111], a[180:183]\n" \
"buffer_load_dwordx4 v67, s[36:39], s82 offen offset:3072 lds\n" \
"v_mfma_f32_16x16x32_bf16 a[176:179], a[24:27], a[96:99], a[176:179]\n" \
"s_mov_b64 s[64:65], s[14:15]\n" \
"s_add_u32 s68, s67, 0x300000\n" \
"ds_read_b64_tr_b16 a[82:83], v74 offset:4608\n" \
"v_mfma_f32_16x16x32_bf16 a[176:179], a[28:31], a[100:103], a[176:179]\n" \
"s_add_u32 s64, s64, s68\n" \
"s_addc_u32 s65, s65, 0\n" \
"v_mfma_f32_16x16x32_bf16 a[192:195], a[32:35], a[96:99], a[192:195]\n" \
"s_xor_b32 s82, s82, 0x80\n" \
"ds_read_b128 a[24:27], v100 offset:6144\n" \
"v_mfma_f32_16x16x32_bf16 a[192:195], a[36:39], a[100:103], a[192:195]\n" \
"global_load_dword v238, v234, s[64:65]\n" \
"v_mfma_f32_16x16x32_bf16 a[196:199], a[32:35], a[104:107], a[196:199]\n" \
"s_mov_b64 s[64:65], s[16:17]\n" \
"s_add_u32 s68, s67, 0x300000\n" \
"ds_read_b64_tr_b16 a[84:85], v74 offset:16896\n" \
"v_mfma_f32_16x16x32_bf16 a[196:199], a[36:39], a[108:111], a[196:199]\n" \
"s_add_u32 s64, s64, s68\n" \
"s_addc_u32 s65, s65, 0\n" \
"v_mfma_f32_16x16x32_bf16 a[200:203], a[32:35], a[112:115], a[200:203]\n" \
"ds_read_b128 a[28:31], v101 offset:6144\n" \
"v_mfma_f32_16x16x32_bf16 a[200:203], a[36:39], a[116:119], a[200:203]\n" \
"global_load_dword v238, v234, s[64:65]\n" \
"v_mfma_f32_16x16x32_bf16 a[204:207], a[32:35], a[120:123], a[204:207]\n" \
"s_mov_b64 s[64:65], s[8:9]\n" \
"s_add_u32 s68, s66, 0x0\n" \
"ds_read_b64_tr_b16 a[86:87], v74 offset:20992\n" \
"v_mfma_f32_16x16x32_bf16 a[204:207], a[36:39], a[124:127], a[204:207]\n" \
"s_add_u32 s64, s64, s68\n" \
"s_addc_u32 s65, s65, 0\n" \
"v_mfma_f32_16x16x32_bf16 a[220:223], a[40:43], a[120:123], a[220:223]\n" \
"ds_read_b128 a[32:35], v100 offset:8192\n" \
"v_mfma_f32_16x16x32_bf16 a[220:223], a[44:47], a[124:127], a[220:223]\n" \
"global_load_dword v238, v234, s[64:65]\n" \
"v_mfma_f32_16x16x32_bf16 a[216:219], a[40:43], a[112:115], a[216:219]\n" \
"s_mov_b64 s[64:65], s[8:9]\n" \
"s_add_u32 s68, s66, 0x100000\n" \
"ds_read_b64_tr_b16 a[88:89], v75 offset:512\n" \
"v_mfma_f32_16x16x32_bf16 a[216:219], a[44:47], a[116:119], a[216:219]\n" \
"s_add_u32 s64, s64, s68\n" \
"s_addc_u32 s65, s65, 0\n" \
"v_mfma_f32_16x16x32_bf16 a[212:215], a[40:43], a[104:107], a[212:215]\n" \
"ds_read_b128 a[36:39], v101 offset:8192\n" \
"v_mfma_f32_16x16x32_bf16 a[212:215], a[44:47], a[108:111], a[212:215]\n" \
"global_load_dword v238, v234, s[64:65]\n" \
"v_mfma_f32_16x16x32_bf16 a[208:211], a[40:43], a[96:99], a[208:211]\n" \
"ds_read_b64_tr_b16 a[90:91], v75 offset:4608\n" \
"v_mfma_f32_16x16x32_bf16 a[208:211], a[44:47], a[100:103], a[208:211]\n" \
"v_mfma_f32_16x16x32_bf16 a[224:227], a[48:51], a[96:99], a[224:227]\n" \
"ds_read_b128 a[40:43], v100 offset:10240\n" \
"v_mfma_f32_16x16x32_bf16 a[224:227], a[52:55], a[100:103], a[224:227]\n" \
"v_mfma_f32_16x16x32_bf16 a[228:231], a[48:51], a[104:107], a[228:231]\n" \
"ds_read_b64_tr_b16 a[92:93], v75 offset:16896\n" \
"v_mfma_f32_16x16x32_bf16 a[228:231], a[52:55], a[108:111], a[228:231]\n" \
"v_mfma_f32_16x16x32_bf16 a[232:235], a[48:51], a[112:115], a[232:235]\n" \
"ds_read_b128 a[44:47], v101 offset:10240\n" \
"v_mfma_f32_16x16x32_bf16 a[232:235], a[52:55], a[116:119], a[232:235]\n" \
"v_mfma_f32_16x16x32_bf16 a[236:239], a[48:51], a[120:123], a[236:239]\n" \
"ds_read_b64_tr_b16 a[94:95], v75 offset:20992\n" \
"v_mfma_f32_16x16x32_bf16 a[236:239], a[52:55], a[124:127], a[236:239]\n" \
"v_mfma_f32_16x16x32_bf16 a[252:255], a[56:59], a[120:123], a[252:255]\n" \
"ds_read_b128 a[48:51], v100 offset:12288\n" \
"v_mfma_f32_16x16x32_bf16 a[252:255], a[60:63], a[124:127], a[252:255]\n" \
"v_mfma_f32_16x16x32_bf16 a[248:251], a[56:59], a[112:115], a[248:251]\n" \
"v_mfma_f32_16x16x32_bf16 a[248:251], a[60:63], a[116:119], a[248:251]\n" \
"v_mfma_f32_16x16x32_bf16 a[244:247], a[56:59], a[104:107], a[244:247]\n" \
"ds_read_b128 a[52:55], v101 offset:12288\n" \
"v_mfma_f32_16x16x32_bf16 a[244:247], a[60:63], a[108:111], a[244:247]\n" \
"v_mfma_f32_16x16x32_bf16 a[240:243], a[56:59], a[96:99], a[240:243]\n" \
"v_mfma_f32_16x16x32_bf16 a[240:243], a[60:63], a[100:103], a[240:243]\n" \
"s_waitcnt lgkmcnt(1)\n" \
"v_mfma_f32_16x16x32_bf16 v[0:3], a[64:67], a[0:3], v[0:3]\n" \
"ds_read_b128 a[56:59], v100 offset:14336\n" \
"v_mfma_f32_16x16x32_bf16 v[0:3], a[68:71], a[4:7], v[0:3]\n" \
"v_mfma_f32_16x16x32_bf16 v[4:7], a[72:75], a[0:3], v[4:7]\n" \
"s_add_u32 m0, s74, 32768\n" \
"ds_read_b128 a[60:63], v101 offset:14336\n" \
"v_mfma_f32_16x16x32_bf16 v[4:7], a[76:79], a[4:7], v[4:7]\n" \
"buffer_load_dwordx4 v68, s[40:43], s83 offen offset:0 lds\n" \
"v_mfma_f32_16x16x32_bf16 v[8:11], a[80:83], a[0:3], v[8:11]\n" \
"ds_read_b64_tr_b16 a[96:97], v74 offset:1024\n" \
"v_mfma_f32_16x16x32_bf16 v[8:11], a[84:87], a[4:7], v[8:11]\n" \
"v_mfma_f32_16x16x32_bf16 v[12:15], a[88:91], a[0:3], v[12:15]\n" \
"ds_read_b64_tr_b16 a[98:99], v74 offset:5120\n" \
"v_mfma_f32_16x16x32_bf16 v[12:15], a[92:95], a[4:7], v[12:15]\n" \
"buffer_load_dwordx4 v69, s[44:47], s83 offen offset:1024 lds\n" \
"v_mfma_f32_16x16x32_bf16 v[28:31], a[88:91], a[8:11], v[28:31]\n" \
"ds_read_b64_tr_b16 a[100:101], v74 offset:17408\n" \
"v_mfma_f32_16x16x32_bf16 v[28:31], a[92:95], a[12:15], v[28:31]\n" \
"v_mfma_f32_16x16x32_bf16 v[24:27], a[80:83], a[8:11], v[24:27]\n" \
"ds_read_b64_tr_b16 a[102:103], v74 offset:21504\n" \
"v_mfma_f32_16x16x32_bf16 v[24:27], a[84:87], a[12:15], v[24:27]\n" \
"buffer_load_dwordx4 v70, s[40:43], s83 offen offset:2048 lds\n" \
"v_mfma_f32_16x16x32_bf16 v[20:23], a[72:75], a[8:11], v[20:23]\n" \
"ds_read_b64_tr_b16 a[104:105], v75 offset:1024\n" \
"v_mfma_f32_16x16x32_bf16 v[20:23], a[76:79], a[12:15], v[20:23]\n" \
"v_mfma_f32_16x16x32_bf16 v[16:19], a[64:67], a[8:11], v[16:19]\n" \
"ds_read_b64_tr_b16 a[106:107], v75 offset:5120\n" \
"v_mfma_f32_16x16x32_bf16 v[16:19], a[68:71], a[12:15], v[16:19]\n" \
"buffer_load_dwordx4 v71, s[44:47], s83 offen offset:3072 lds\n" \
"v_mfma_f32_16x16x32_bf16 v[32:35], a[64:67], a[16:19], v[32:35]\n" \
"ds_read_b64_tr_b16 a[108:109], v75 offset:17408\n" \
"v_mfma_f32_16x16x32_bf16 v[32:35], a[68:71], a[20:23], v[32:35]\n" \
"s_xor_b32 s83, s83, 0x180000\n" \
"v_mfma_f32_16x16x32_bf16 v[36:39], a[72:75], a[16:19], v[36:39]\n" \
"s_add_u32 m0, s74, 36864\n" \
"ds_read_b64_tr_b16 a[110:111], v75 offset:21504\n" \
"v_mfma_f32_16x16x32_bf16 v[36:39], a[76:79], a[20:23], v[36:39]\n" \
"buffer_load_dwordx4 v68, s[40:43], s84 offen offset:0 lds\n" \
"v_mfma_f32_16x16x32_bf16 v[40:43], a[80:83], a[16:19], v[40:43]\n" \
"ds_read_b64_tr_b16 a[112:113], v74 offset:1536\n" \
"v_mfma_f32_16x16x32_bf16 v[40:43], a[84:87], a[20:23], v[40:43]\n" \
"v_mfma_f32_16x16x32_bf16 v[44:47], a[88:91], a[16:19], v[44:47]\n" \
"ds_read_b64_tr_b16 a[114:115], v74 offset:5632\n" \
"v_mfma_f32_16x16x32_bf16 v[44:47], a[92:95], a[20:23], v[44:47]\n" \
"buffer_load_dwordx4 v69, s[44:47], s84 offen offset:1024 lds\n" \
"v_mfma_f32_16x16x32_bf16 v[60:63], a[88:91], a[24:27], v[60:63]\n" \
"ds_read_b64_tr_b16 a[116:117], v74 offset:17920\n" \
"v_mfma_f32_16x16x32_bf16 v[60:63], a[92:95], a[28:31], v[60:63]\n" \
"v_mfma_f32_16x16x32_bf16 v[56:59], a[80:83], a[24:27], v[56:59]\n" \
"ds_read_b64_tr_b16 a[118:119], v74 offset:22016\n" \
"v_mfma_f32_16x16x32_bf16 v[56:59], a[84:87], a[28:31], v[56:59]\n" \
"buffer_load_dwordx4 v70, s[40:43], s84 offen offset:2048 lds\n" \
"v_mfma_f32_16x16x32_bf16 v[52:55], a[72:75], a[24:27], v[52:55]\n" \
"ds_read_b64_tr_b16 a[120:121], v75 offset:1536\n" \
"v_mfma_f32_16x16x32_bf16 v[52:55], a[76:79], a[28:31], v[52:55]\n" \
"v_mfma_f32_16x16x32_bf16 v[48:51], a[64:67], a[24:27], v[48:51]\n" \
"ds_read_b64_tr_b16 a[122:123], v75 offset:5632\n" \
"v_mfma_f32_16x16x32_bf16 v[48:51], a[68:71], a[28:31], v[48:51]\n" \
"buffer_load_dwordx4 v71, s[44:47], s84 offen offset:3072 lds\n" \
"v_mfma_f32_16x16x32_bf16 v[144:147], a[64:67], a[32:35], v[144:147]\n" \
"ds_read_b64_tr_b16 a[124:125], v75 offset:17920\n" \
"v_mfma_f32_16x16x32_bf16 v[144:147], a[68:71], a[36:39], v[144:147]\n" \
"v_mfma_f32_16x16x32_bf16 v[148:151], a[72:75], a[32:35], v[148:151]\n" \
"s_xor_b32 s84, s84, 0x180000\n" \
"ds_read_b64_tr_b16 a[126:127], v75 offset:22016\n" \
"v_mfma_f32_16x16x32_bf16 v[148:151], a[76:79], a[36:39], v[148:151]\n" \
"v_mfma_f32_16x16x32_bf16 v[152:155], a[80:83], a[32:35], v[152:155]\n" \
"v_mfma_f32_16x16x32_bf16 v[152:155], a[84:87], a[36:39], v[152:155]\n" \
"v_mfma_f32_16x16x32_bf16 v[156:159], a[88:91], a[32:35], v[156:159]\n" \
"v_mfma_f32_16x16x32_bf16 v[156:159], a[92:95], a[36:39], v[156:159]\n" \
"v_mfma_f32_16x16x32_bf16 v[188:191], a[88:91], a[40:43], v[188:191]\n" \
"v_mfma_f32_16x16x32_bf16 v[188:191], a[92:95], a[44:47], v[188:191]\n" \
"v_mfma_f32_16x16x32_bf16 v[184:187], a[80:83], a[40:43], v[184:187]\n" \
"v_mfma_f32_16x16x32_bf16 v[184:187], a[84:87], a[44:47], v[184:187]\n" \
"v_mfma_f32_16x16x32_bf16 v[180:183], a[72:75], a[40:43], v[180:183]\n" \
"v_mfma_f32_16x16x32_bf16 v[180:183], a[76:79], a[44:47], v[180:183]\n" \
"v_mfma_f32_16x16x32_bf16 v[176:179], a[64:67], a[40:43], v[176:179]\n" \
"v_mfma_f32_16x16x32_bf16 v[176:179], a[68:71], a[44:47], v[176:179]\n" \
"s_waitcnt lgkmcnt(0)\n" \
"v_mfma_f32_16x16x32_bf16 v[192:195], a[64:67], a[48:51], v[192:195]\n" \
"v_mfma_f32_16x16x32_bf16 v[192:195], a[68:71], a[52:55], v[192:195]\n" \
"v_mfma_f32_16x16x32_bf16 v[196:199], a[72:75], a[48:51], v[196:199]\n" \
"v_mfma_f32_16x16x32_bf16 v[196:199], a[76:79], a[52:55], v[196:199]\n" \
"v_mfma_f32_16x16x32_bf16 v[200:203], a[80:83], a[48:51], v[200:203]\n" \
"v_mfma_f32_16x16x32_bf16 v[200:203], a[84:87], a[52:55], v[200:203]\n" \
"v_mfma_f32_16x16x32_bf16 v[204:207], a[88:91], a[48:51], v[204:207]\n" \
"v_mfma_f32_16x16x32_bf16 v[204:207], a[92:95], a[52:55], v[204:207]\n" \
"v_mfma_f32_16x16x32_bf16 v[220:223], a[88:91], a[56:59], v[220:223]\n" \
"v_mfma_f32_16x16x32_bf16 v[220:223], a[92:95], a[60:63], v[220:223]\n" \
"v_mfma_f32_16x16x32_bf16 v[216:219], a[80:83], a[56:59], v[216:219]\n" \
"v_mfma_f32_16x16x32_bf16 v[216:219], a[84:87], a[60:63], v[216:219]\n" \
"v_mfma_f32_16x16x32_bf16 v[212:215], a[72:75], a[56:59], v[212:215]\n" \
"v_mfma_f32_16x16x32_bf16 v[212:215], a[76:79], a[60:63], v[212:215]\n" \
"v_mfma_f32_16x16x32_bf16 v[208:211], a[64:67], a[56:59], v[208:211]\n" \
"v_mfma_f32_16x16x32_bf16 v[208:211], a[68:71], a[60:63], v[208:211]\n" \
"s_waitcnt vmcnt(12) lgkmcnt(0)\n" \
"s_barrier\n" \
"v_mfma_f32_16x16x32_bf16 a[128:131], a[0:3], a[96:99], a[128:131]\n" \
"s_add_u32 m0, s74, 0\n" \
"ds_read_b64_tr_b16 a[64:65], v98 offset:0\n" \
"v_mfma_f32_16x16x32_bf16 a[128:131], a[4:7], a[100:103], a[128:131]\n" \
"buffer_load_dwordx4 v64, s[36:39], s81 offen offset:0 lds\n" \
"v_mfma_f32_16x16x32_bf16 a[132:135], a[0:3], a[104:107], a[132:135]\n" \
"ds_read_b64_tr_b16 a[66:67], v98 offset:4096\n" \
"v_mfma_f32_16x16x32_bf16 a[132:135], a[4:7], a[108:111], a[132:135]\n" \
"v_mfma_f32_16x16x32_bf16 a[136:139], a[0:3], a[112:115], a[136:139]\n" \
"ds_read_b64_tr_b16 a[68:69], v98 offset:16384\n" \
"v_mfma_f32_16x16x32_bf16 a[136:139], a[4:7], a[116:119], a[136:139]\n" \
"buffer_load_dwordx4 v65, s[36:39], s81 offen offset:1024 lds\n" \
"v_mfma_f32_16x16x32_bf16 a[140:143], a[0:3], a[120:123], a[140:143]\n" \
"ds_read_b64_tr_b16 a[70:71], v98 offset:20480\n" \
"v_mfma_f32_16x16x32_bf16 a[140:143], a[4:7], a[124:127], a[140:143]\n" \
"v_mfma_f32_16x16x32_bf16 a[156:159], a[8:11], a[120:123], a[156:159]\n" \
"ds_read_b128 a[0:3], v96 offset:32768\n" \
"v_mfma_f32_16x16x32_bf16 a[156:159], a[12:15], a[124:127], a[156:159]\n" \
"buffer_load_dwordx4 v66, s[36:39], s81 offen offset:2048 lds\n" \
"v_mfma_f32_16x16x32_bf16 a[152:155], a[8:11], a[112:115], a[152:155]\n" \
"ds_read_b64_tr_b16 a[72:73], v99 offset:0\n" \
"v_mfma_f32_16x16x32_bf16 a[152:155], a[12:15], a[116:119], a[152:155]\n" \
"v_mfma_f32_16x16x32_bf16 a[148:151], a[8:11], a[104:107], a[148:151]\n" \
"ds_read_b128 a[4:7], v97 offset:32768\n" \
"v_mfma_f32_16x16x32_bf16 a[148:151], a[12:15], a[108:111], a[148:151]\n" \
"buffer_load_dwordx4 v67, s[36:39], s81 offen offset:3072 lds\n" \
"v_mfma_f32_16x16x32_bf16 a[144:147], a[8:11], a[96:99], a[144:147]\n" \
"ds_read_b64_tr_b16 a[74:75], v99 offset:4096\n" \
"Lpg1132_%=:\n" \
"Lst0x1_%=:\n" \
"s_cbranch_execz Lpg3342_%=\n" \
"v_mfma_f32_16x16x32_bf16 a[144:147], a[12:15], a[100:103], a[144:147]\n" \
"s_xor_b32 s81, s81, 0x180\n" \
"v_mfma_f32_16x16x32_bf16 a[160:163], a[16:19], a[96:99], a[160:163]\n" \
"s_add_u32 m0, s74, 4096\n" \
"ds_read_b128 a[8:11], v96 offset:34816\n" \
"v_mfma_f32_16x16x32_bf16 a[160:163], a[20:23], a[100:103], a[160:163]\n" \
"buffer_load_dwordx4 v64, s[36:39], s82 offen offset:0 lds\n" \
"v_mfma_f32_16x16x32_bf16 a[164:167], a[16:19], a[104:107], a[164:167]\n" \
"ds_read_b64_tr_b16 a[76:77], v99 offset:16384\n" \
"v_mfma_f32_16x16x32_bf16 a[164:167], a[20:23], a[108:111], a[164:167]\n" \
"v_mfma_f32_16x16x32_bf16 a[168:171], a[16:19], a[112:115], a[168:171]\n" \
"ds_read_b128 a[12:15], v97 offset:34816\n" \
"v_mfma_f32_16x16x32_bf16 a[168:171], a[20:23], a[116:119], a[168:171]\n" \
"buffer_load_dwordx4 v65, s[36:39], s82 offen offset:1024 lds\n" \
"v_mfma_f32_16x16x32_bf16 a[172:175], a[16:19], a[120:123], a[172:175]\n" \
"ds_read_b64_tr_b16 a[78:79], v99 offset:20480\n" \
"v_mfma_f32_16x16x32_bf16 a[172:175], a[20:23], a[124:127], a[172:175]\n" \
"v_mfma_f32_16x16x32_bf16 a[188:191], a[24:27], a[120:123], a[188:191]\n" \
"ds_read_b128 a[16:19], v96 offset:36864\n" \
"v_mfma_f32_16x16x32_bf16 a[188:191], a[28:31], a[124:127], a[188:191]\n" \
"buffer_load_dwordx4 v66, s[36:39], s82 offen offset:2048 lds\n" \
"v_mfma_f32_16x16x32_bf16 a[184:187], a[24:27], a[112:115], a[184:187]\n" \
"ds_read_b64_tr_b16 a[80:81], v98 offset:512\n" \
"v_mfma_f32_16x16x32_bf16 a[184:187], a[28:31], a[116:119], a[184:187]\n" \
"v_mfma_f32_16x16x32_bf16 a[180:183], a[24:27], a[104:107], a[180:183]\n" \
"ds_read_b128 a[20:23], v97 offset:36864\n" \
"v_mfma_f32_16x16x32_bf16 a[180:183], a[28:31], a[108:111], a[180:183]\n" \
"buffer_load_dwordx4 v67, s[36:39], s82 offen offset:3072 lds\n" \
"v_mfma_f32_16x16x32_bf16 a[176:179], a[24:27], a[96:99], a[176:179]\n" \
"s_mov_b64 s[64:65], s[8:9]\n" \
"s_add_u32 s68, s66, 0x200000\n" \
"ds_read_b64_tr_b16 a[82:83], v98 offset:4608\n" \
"v_mfma_f32_16x16x32_bf16 a[176:179], a[28:31], a[100:103], a[176:179]\n" \
"s_add_u32 s64, s64, s68\n" \
"s_addc_u32 s65, s65, 0\n" \
"v_mfma_f32_16x16x32_bf16 a[192:195], a[32:35], a[96:99], a[192:195]\n" \
"s_xor_b32 s82, s82, 0x180\n" \
"ds_read_b128 a[24:27], v96 offset:38912\n" \
"v_mfma_f32_16x16x32_bf16 a[192:195], a[36:39], a[100:103], a[192:195]\n" \
"global_load_dword v238, v234, s[64:65]\n" \
"v_mfma_f32_16x16x32_bf16 a[196:199], a[32:35], a[104:107], a[196:199]\n" \
"s_mov_b64 s[64:65], s[8:9]\n" \
"s_add_u32 s68, s66, 0x300000\n" \
"ds_read_b64_tr_b16 a[84:85], v98 offset:16896\n" \
"v_mfma_f32_16x16x32_bf16 a[196:199], a[36:39], a[108:111], a[196:199]\n" \
"s_add_u32 s64, s64, s68\n" \
"s_addc_u32 s65, s65, 0\n" \
"v_mfma_f32_16x16x32_bf16 a[200:203], a[32:35], a[112:115], a[200:203]\n" \
"ds_read_b128 a[28:31], v97 offset:38912\n" \
"v_mfma_f32_16x16x32_bf16 a[200:203], a[36:39], a[116:119], a[200:203]\n" \
"global_load_dword v238, v234, s[64:65]\n" \
"v_mfma_f32_16x16x32_bf16 a[204:207], a[32:35], a[120:123], a[204:207]\n" \
"s_mov_b64 s[64:65], s[12:13]\n" \
"s_add_u32 s68, s67, 0x0\n" \
"ds_read_b64_tr_b16 a[86:87], v98 offset:20992\n" \
"v_mfma_f32_16x16x32_bf16 a[204:207], a[36:39], a[124:127], a[204:207]\n" \
"s_add_u32 s64, s64, s68\n" \
"s_addc_u32 s65, s65, 0\n" \
"v_mfma_f32_16x16x32_bf16 a[220:223], a[40:43], a[120:123], a[220:223]\n" \
"ds_read_b128 a[32:35], v96 offset:40960\n" \
"v_mfma_f32_16x16x32_bf16 a[220:223], a[44:47], a[124:127], a[220:223]\n" \
"global_load_dword v238, v234, s[64:65]\n" \
"v_mfma_f32_16x16x32_bf16 a[216:219], a[40:43], a[112:115], a[216:219]\n" \
"ds_read_b64_tr_b16 a[88:89], v99 offset:512\n" \
"v_mfma_f32_16x16x32_bf16 a[216:219], a[44:47], a[116:119], a[216:219]\n" \
"v_mfma_f32_16x16x32_bf16 a[212:215], a[40:43], a[104:107], a[212:215]\n" \
"ds_read_b128 a[36:39], v97 offset:40960\n" \
"v_mfma_f32_16x16x32_bf16 a[212:215], a[44:47], a[108:111], a[212:215]\n" \
"v_mfma_f32_16x16x32_bf16 a[208:211], a[40:43], a[96:99], a[208:211]\n" \
"ds_read_b64_tr_b16 a[90:91], v99 offset:4608\n" \
"v_mfma_f32_16x16x32_bf16 a[208:211], a[44:47], a[100:103], a[208:211]\n" \
"v_mfma_f32_16x16x32_bf16 a[224:227], a[48:51], a[96:99], a[224:227]\n" \
"ds_read_b128 a[40:43], v96 offset:43008\n" \
"v_mfma_f32_16x16x32_bf16 a[224:227], a[52:55], a[100:103], a[224:227]\n" \
"v_mfma_f32_16x16x32_bf16 a[228:231], a[48:51], a[104:107], a[228:231]\n" \
"ds_read_b64_tr_b16 a[92:93], v99 offset:16896\n" \
"v_mfma_f32_16x16x32_bf16 a[228:231], a[52:55], a[108:111], a[228:231]\n" \
"v_mfma_f32_16x16x32_bf16 a[232:235], a[48:51], a[112:115], a[232:235]\n" \
"ds_read_b128 a[44:47], v97 offset:43008\n" \
"v_mfma_f32_16x16x32_bf16 a[232:235], a[52:55], a[116:119], a[232:235]\n" \
"v_mfma_f32_16x16x32_bf16 a[236:239], a[48:51], a[120:123], a[236:239]\n" \
"ds_read_b64_tr_b16 a[94:95], v99 offset:20992\n" \
"v_mfma_f32_16x16x32_bf16 a[236:239], a[52:55], a[124:127], a[236:239]\n" \
"v_mfma_f32_16x16x32_bf16 a[252:255], a[56:59], a[120:123], a[252:255]\n" \
"ds_read_b128 a[48:51], v96 offset:45056\n" \
"v_mfma_f32_16x16x32_bf16 a[252:255], a[60:63], a[124:127], a[252:255]\n" \
"v_mfma_f32_16x16x32_bf16 a[248:251], a[56:59], a[112:115], a[248:251]\n" \
"v_mfma_f32_16x16x32_bf16 a[248:251], a[60:63], a[116:119], a[248:251]\n" \
"v_mfma_f32_16x16x32_bf16 a[244:247], a[56:59], a[104:107], a[244:247]\n" \
"ds_read_b128 a[52:55], v97 offset:45056\n" \
"v_mfma_f32_16x16x32_bf16 a[244:247], a[60:63], a[108:111], a[244:247]\n" \
"v_mfma_f32_16x16x32_bf16 a[240:243], a[56:59], a[96:99], a[240:243]\n" \
"v_mfma_f32_16x16x32_bf16 a[240:243], a[60:63], a[100:103], a[240:243]\n" \
"s_waitcnt lgkmcnt(1)\n" \
"v_mfma_f32_16x16x32_bf16 v[0:3], a[64:67], a[0:3], v[0:3]\n" \
"ds_read_b128 a[56:59], v96 offset:47104\n" \
"v_mfma_f32_16x16x32_bf16 v[0:3], a[68:71], a[4:7], v[0:3]\n" \
"v_mfma_f32_16x16x32_bf16 v[4:7], a[72:75], a[0:3], v[4:7]\n" \
"s_add_u32 m0, s74, 131072\n" \
"ds_read_b128 a[60:63], v97 offset:47104\n" \
"v_mfma_f32_16x16x32_bf16 v[4:7], a[76:79], a[4:7], v[4:7]\n" \
"buffer_load_dwordx4 v68, s[40:43], s83 offen offset:0 lds\n" \
"v_mfma_f32_16x16x32_bf16 v[8:11], a[80:83], a[0:3], v[8:11]\n" \
"ds_read_b64_tr_b16 a[96:97], v98 offset:1024\n" \
"v_mfma_f32_16x16x32_bf16 v[8:11], a[84:87], a[4:7], v[8:11]\n" \
"v_mfma_f32_16x16x32_bf16 v[12:15], a[88:91], a[0:3], v[12:15]\n" \
"ds_read_b64_tr_b16 a[98:99], v98 offset:5120\n" \
"v_mfma_f32_16x16x32_bf16 v[12:15], a[92:95], a[4:7], v[12:15]\n" \
"buffer_load_dwordx4 v69, s[44:47], s83 offen offset:1024 lds\n" \
"v_mfma_f32_16x16x32_bf16 v[28:31], a[88:91], a[8:11], v[28:31]\n" \
"ds_read_b64_tr_b16 a[100:101], v98 offset:17408\n" \
"v_mfma_f32_16x16x32_bf16 v[28:31], a[92:95], a[12:15], v[28:31]\n" \
"v_mfma_f32_16x16x32_bf16 v[24:27], a[80:83], a[8:11], v[24:27]\n" \
"ds_read_b64_tr_b16 a[102:103], v98 offset:21504\n" \
"v_mfma_f32_16x16x32_bf16 v[24:27], a[84:87], a[12:15], v[24:27]\n" \
"buffer_load_dwordx4 v70, s[40:43], s83 offen offset:2048 lds\n" \
"v_mfma_f32_16x16x32_bf16 v[20:23], a[72:75], a[8:11], v[20:23]\n" \
"ds_read_b64_tr_b16 a[104:105], v99 offset:1024\n" \
"v_mfma_f32_16x16x32_bf16 v[20:23], a[76:79], a[12:15], v[20:23]\n" \
"v_mfma_f32_16x16x32_bf16 v[16:19], a[64:67], a[8:11], v[16:19]\n" \
"ds_read_b64_tr_b16 a[106:107], v99 offset:5120\n" \
"v_mfma_f32_16x16x32_bf16 v[16:19], a[68:71], a[12:15], v[16:19]\n" \
"buffer_load_dwordx4 v71, s[44:47], s83 offen offset:3072 lds\n" \
"v_mfma_f32_16x16x32_bf16 v[32:35], a[64:67], a[16:19], v[32:35]\n" \
"ds_read_b64_tr_b16 a[108:109], v99 offset:17408\n" \
"v_mfma_f32_16x16x32_bf16 v[32:35], a[68:71], a[20:23], v[32:35]\n" \
"s_xor_b32 s83, s83, 0x80000\n" \
"v_mfma_f32_16x16x32_bf16 v[36:39], a[72:75], a[16:19], v[36:39]\n" \
"s_add_u32 m0, s74, 135168\n" \
"ds_read_b64_tr_b16 a[110:111], v99 offset:21504\n" \
"v_mfma_f32_16x16x32_bf16 v[36:39], a[76:79], a[20:23], v[36:39]\n" \
"buffer_load_dwordx4 v68, s[40:43], s84 offen offset:0 lds\n" \
"v_mfma_f32_16x16x32_bf16 v[40:43], a[80:83], a[16:19], v[40:43]\n" \
"ds_read_b64_tr_b16 a[112:113], v98 offset:1536\n" \
"v_mfma_f32_16x16x32_bf16 v[40:43], a[84:87], a[20:23], v[40:43]\n" \
"v_mfma_f32_16x16x32_bf16 v[44:47], a[88:91], a[16:19], v[44:47]\n" \
"ds_read_b64_tr_b16 a[114:115], v98 offset:5632\n" \
"v_mfma_f32_16x16x32_bf16 v[44:47], a[92:95], a[20:23], v[44:47]\n" \
"buffer_load_dwordx4 v69, s[44:47], s84 offen offset:1024 lds\n" \
"v_mfma_f32_16x16x32_bf16 v[60:63], a[88:91], a[24:27], v[60:63]\n" \
"ds_read_b64_tr_b16 a[116:117], v98 offset:17920\n" \
"v_mfma_f32_16x16x32_bf16 v[60:63], a[92:95], a[28:31], v[60:63]\n" \
"v_mfma_f32_16x16x32_bf16 v[56:59], a[80:83], a[24:27], v[56:59]\n" \
"ds_read_b64_tr_b16 a[118:119], v98 offset:22016\n" \
"v_mfma_f32_16x16x32_bf16 v[56:59], a[84:87], a[28:31], v[56:59]\n" \
"buffer_load_dwordx4 v70, s[40:43], s84 offen offset:2048 lds\n" \
"v_mfma_f32_16x16x32_bf16 v[52:55], a[72:75], a[24:27], v[52:55]\n" \
"ds_read_b64_tr_b16 a[120:121], v99 offset:1536\n" \
"v_mfma_f32_16x16x32_bf16 v[52:55], a[76:79], a[28:31], v[52:55]\n" \
"v_mfma_f32_16x16x32_bf16 v[48:51], a[64:67], a[24:27], v[48:51]\n" \
"ds_read_b64_tr_b16 a[122:123], v99 offset:5632\n" \
"v_mfma_f32_16x16x32_bf16 v[48:51], a[68:71], a[28:31], v[48:51]\n" \
"buffer_load_dwordx4 v71, s[44:47], s84 offen offset:3072 lds\n" \
"v_mfma_f32_16x16x32_bf16 v[144:147], a[64:67], a[32:35], v[144:147]\n" \
"ds_read_b64_tr_b16 a[124:125], v99 offset:17920\n" \
"v_mfma_f32_16x16x32_bf16 v[144:147], a[68:71], a[36:39], v[144:147]\n" \
"v_mfma_f32_16x16x32_bf16 v[148:151], a[72:75], a[32:35], v[148:151]\n" \
"s_xor_b32 s84, s84, 0x80000\n" \
"ds_read_b64_tr_b16 a[126:127], v99 offset:22016\n" \
"v_mfma_f32_16x16x32_bf16 v[148:151], a[76:79], a[36:39], v[148:151]\n" \
"v_mfma_f32_16x16x32_bf16 v[152:155], a[80:83], a[32:35], v[152:155]\n" \
"v_mfma_f32_16x16x32_bf16 v[152:155], a[84:87], a[36:39], v[152:155]\n" \
"v_mfma_f32_16x16x32_bf16 v[156:159], a[88:91], a[32:35], v[156:159]\n" \
"v_mfma_f32_16x16x32_bf16 v[156:159], a[92:95], a[36:39], v[156:159]\n" \
"v_mfma_f32_16x16x32_bf16 v[188:191], a[88:91], a[40:43], v[188:191]\n" \
"v_mfma_f32_16x16x32_bf16 v[188:191], a[92:95], a[44:47], v[188:191]\n" \
"v_mfma_f32_16x16x32_bf16 v[184:187], a[80:83], a[40:43], v[184:187]\n" \
"v_mfma_f32_16x16x32_bf16 v[184:187], a[84:87], a[44:47], v[184:187]\n" \
"v_mfma_f32_16x16x32_bf16 v[180:183], a[72:75], a[40:43], v[180:183]\n" \
"v_mfma_f32_16x16x32_bf16 v[180:183], a[76:79], a[44:47], v[180:183]\n" \
"v_mfma_f32_16x16x32_bf16 v[176:179], a[64:67], a[40:43], v[176:179]\n" \
"v_mfma_f32_16x16x32_bf16 v[176:179], a[68:71], a[44:47], v[176:179]\n" \
"s_waitcnt lgkmcnt(0)\n" \
"v_mfma_f32_16x16x32_bf16 v[192:195], a[64:67], a[48:51], v[192:195]\n" \
"v_mfma_f32_16x16x32_bf16 v[192:195], a[68:71], a[52:55], v[192:195]\n" \
"v_mfma_f32_16x16x32_bf16 v[196:199], a[72:75], a[48:51], v[196:199]\n" \
"v_mfma_f32_16x16x32_bf16 v[196:199], a[76:79], a[52:55], v[196:199]\n" \
"v_mfma_f32_16x16x32_bf16 v[200:203], a[80:83], a[48:51], v[200:203]\n" \
"v_mfma_f32_16x16x32_bf16 v[200:203], a[84:87], a[52:55], v[200:203]\n" \
"v_mfma_f32_16x16x32_bf16 v[204:207], a[88:91], a[48:51], v[204:207]\n" \
"v_mfma_f32_16x16x32_bf16 v[204:207], a[92:95], a[52:55], v[204:207]\n" \
"v_mfma_f32_16x16x32_bf16 v[220:223], a[88:91], a[56:59], v[220:223]\n" \
"v_mfma_f32_16x16x32_bf16 v[220:223], a[92:95], a[60:63], v[220:223]\n" \
"v_mfma_f32_16x16x32_bf16 v[216:219], a[80:83], a[56:59], v[216:219]\n" \
"v_mfma_f32_16x16x32_bf16 v[216:219], a[84:87], a[60:63], v[216:219]\n" \
"v_mfma_f32_16x16x32_bf16 v[212:215], a[72:75], a[56:59], v[212:215]\n" \
"v_mfma_f32_16x16x32_bf16 v[212:215], a[76:79], a[60:63], v[212:215]\n" \
"v_mfma_f32_16x16x32_bf16 v[208:211], a[64:67], a[56:59], v[208:211]\n" \
"v_mfma_f32_16x16x32_bf16 v[208:211], a[68:71], a[60:63], v[208:211]\n" \
"s_waitcnt vmcnt(11) lgkmcnt(0)\n" \
"s_barrier\n" \
"v_mfma_f32_16x16x32_bf16 a[128:131], a[0:3], a[96:99], a[128:131]\n" \
"s_add_u32 m0, s74, 65536\n" \
"ds_read_b64_tr_b16 a[64:65], v74 offset:32768\n" \
"v_mfma_f32_16x16x32_bf16 a[128:131], a[4:7], a[100:103], a[128:131]\n" \
"buffer_load_dwordx4 v64, s[36:39], s81 offen offset:0 lds\n" \
"v_mfma_f32_16x16x32_bf16 a[132:135], a[0:3], a[104:107], a[132:135]\n" \
"ds_read_b64_tr_b16 a[66:67], v74 offset:36864\n" \
"v_mfma_f32_16x16x32_bf16 a[132:135], a[4:7], a[108:111], a[132:135]\n" \
"v_mfma_f32_16x16x32_bf16 a[136:139], a[0:3], a[112:115], a[136:139]\n" \
"ds_read_b64_tr_b16 a[68:69], v74 offset:49152\n" \
"v_mfma_f32_16x16x32_bf16 a[136:139], a[4:7], a[116:119], a[136:139]\n" \
"buffer_load_dwordx4 v65, s[36:39], s81 offen offset:1024 lds\n" \
"v_mfma_f32_16x16x32_bf16 a[140:143], a[0:3], a[120:123], a[140:143]\n" \
"ds_read_b64_tr_b16 a[70:71], v74 offset:53248\n" \
"v_mfma_f32_16x16x32_bf16 a[140:143], a[4:7], a[124:127], a[140:143]\n" \
"v_mfma_f32_16x16x32_bf16 a[156:159], a[8:11], a[120:123], a[156:159]\n" \
"ds_read_b128 a[0:3], v72 offset:0\n" \
"v_mfma_f32_16x16x32_bf16 a[156:159], a[12:15], a[124:127], a[156:159]\n" \
"buffer_load_dwordx4 v66, s[36:39], s81 offen offset:2048 lds\n" \
"v_mfma_f32_16x16x32_bf16 a[152:155], a[8:11], a[112:115], a[152:155]\n" \
"ds_read_b64_tr_b16 a[72:73], v75 offset:32768\n" \
"v_mfma_f32_16x16x32_bf16 a[152:155], a[12:15], a[116:119], a[152:155]\n" \
"v_mfma_f32_16x16x32_bf16 a[148:151], a[8:11], a[104:107], a[148:151]\n" \
"ds_read_b128 a[4:7], v73 offset:0\n" \
"v_mfma_f32_16x16x32_bf16 a[148:151], a[12:15], a[108:111], a[148:151]\n" \
"buffer_load_dwordx4 v67, s[36:39], s81 offen offset:3072 lds\n" \
"v_mfma_f32_16x16x32_bf16 a[144:147], a[8:11], a[96:99], a[144:147]\n" \
"ds_read_b64_tr_b16 a[74:75], v75 offset:36864\n" \
"v_mfma_f32_16x16x32_bf16 a[144:147], a[12:15], a[100:103], a[144:147]\n" \
"s_xor_b32 s81, s81, 0x80\n" \
"v_mfma_f32_16x16x32_bf16 a[160:163], a[16:19], a[96:99], a[160:163]\n" \
"s_add_u32 m0, s74, 69632\n" \
"ds_read_b128 a[8:11], v72 offset:2048\n" \
"v_mfma_f32_16x16x32_bf16 a[160:163], a[20:23], a[100:103], a[160:163]\n" \
"buffer_load_dwordx4 v64, s[36:39], s82 offen offset:0 lds\n" \
"v_mfma_f32_16x16x32_bf16 a[164:167], a[16:19], a[104:107], a[164:167]\n" \
"ds_read_b64_tr_b16 a[76:77], v75 offset:49152\n" \
"v_mfma_f32_16x16x32_bf16 a[164:167], a[20:23], a[108:111], a[164:167]\n" \
"v_mfma_f32_16x16x32_bf16 a[168:171], a[16:19], a[112:115], a[168:171]\n" \
"ds_read_b128 a[12:15], v73 offset:2048\n" \
"v_mfma_f32_16x16x32_bf16 a[168:171], a[20:23], a[116:119], a[168:171]\n" \
"buffer_load_dwordx4 v65, s[36:39], s82 offen offset:1024 lds\n" \
"v_mfma_f32_16x16x32_bf16 a[172:175], a[16:19], a[120:123], a[172:175]\n" \
"ds_read_b64_tr_b16 a[78:79], v75 offset:53248\n" \
"v_mfma_f32_16x16x32_bf16 a[172:175], a[20:23], a[124:127], a[172:175]\n" \
"v_mfma_f32_16x16x32_bf16 a[188:191], a[24:27], a[120:123], a[188:191]\n" \
"ds_read_b128 a[16:19], v72 offset:4096\n" \
"v_mfma_f32_16x16x32_bf16 a[188:191], a[28:31], a[124:127], a[188:191]\n" \
"buffer_load_dwordx4 v66, s[36:39], s82 offen offset:2048 lds\n" \
"v_mfma_f32_16x16x32_bf16 a[184:187], a[24:27], a[112:115], a[184:187]\n" \
"ds_read_b64_tr_b16 a[80:81], v74 offset:33280\n" \
"v_mfma_f32_16x16x32_bf16 a[184:187], a[28:31], a[116:119], a[184:187]\n" \
"v_mfma_f32_16x16x32_bf16 a[180:183], a[24:27], a[104:107], a[180:183]\n" \
"ds_read_b128 a[20:23], v73 offset:4096\n" \
"v_mfma_f32_16x16x32_bf16 a[180:183], a[28:31], a[108:111], a[180:183]\n" \
"buffer_load_dwordx4 v67, s[36:39], s82 offen offset:3072 lds\n" \
"v_mfma_f32_16x16x32_bf16 a[176:179], a[24:27], a[96:99], a[176:179]\n" \
"s_mov_b64 s[64:65], s[12:13]\n" \
"s_add_u32 s68, s67, 0x100000\n" \
"ds_read_b64_tr_b16 a[82:83], v74 offset:37376\n" \
"v_mfma_f32_16x16x32_bf16 a[176:179], a[28:31], a[100:103], a[176:179]\n" \
"s_add_u32 s64, s64, s68\n" \
"s_addc_u32 s65, s65, 0\n" \
"v_mfma_f32_16x16x32_bf16 a[192:195], a[32:35], a[96:99], a[192:195]\n" \
"s_xor_b32 s82, s82, 0x80\n" \
"ds_read_b128 a[24:27], v72 offset:6144\n" \
"v_mfma_f32_16x16x32_bf16 a[192:195], a[36:39], a[100:103], a[192:195]\n" \
"global_load_dword v238, v234, s[64:65]\n" \
"v_mfma_f32_16x16x32_bf16 a[196:199], a[32:35], a[104:107], a[196:199]\n" \
"s_mov_b64 s[64:65], s[12:13]\n" \
"s_add_u32 s68, s67, 0x500000\n" \
"ds_read_b64_tr_b16 a[84:85], v74 offset:49664\n" \
"v_mfma_f32_16x16x32_bf16 a[196:199], a[36:39], a[108:111], a[196:199]\n" \
"s_add_u32 s64, s64, s68\n" \
"s_addc_u32 s65, s65, 0\n" \
"v_mfma_f32_16x16x32_bf16 a[200:203], a[32:35], a[112:115], a[200:203]\n" \
"ds_read_b128 a[28:31], v73 offset:6144\n" \
"v_mfma_f32_16x16x32_bf16 a[200:203], a[36:39], a[116:119], a[200:203]\n" \
"global_load_dword v238, v234, s[64:65]\n" \
"v_mfma_f32_16x16x32_bf16 a[204:207], a[32:35], a[120:123], a[204:207]\n" \
"s_mov_b64 s[64:65], s[12:13]\n" \
"s_add_u32 s68, s67, 0x200000\n" \
"ds_read_b64_tr_b16 a[86:87], v74 offset:53760\n" \
"v_mfma_f32_16x16x32_bf16 a[204:207], a[36:39], a[124:127], a[204:207]\n" \
"s_add_u32 s64, s64, s68\n" \
"s_addc_u32 s65, s65, 0\n" \
"v_mfma_f32_16x16x32_bf16 a[220:223], a[40:43], a[120:123], a[220:223]\n" \
"ds_read_b128 a[32:35], v72 offset:8192\n" \
"v_mfma_f32_16x16x32_bf16 a[220:223], a[44:47], a[124:127], a[220:223]\n" \
"global_load_dword v238, v234, s[64:65]\n" \
"v_mfma_f32_16x16x32_bf16 a[216:219], a[40:43], a[112:115], a[216:219]\n" \
"ds_read_b64_tr_b16 a[88:89], v75 offset:33280\n" \
"v_mfma_f32_16x16x32_bf16 a[216:219], a[44:47], a[116:119], a[216:219]\n" \
"v_mfma_f32_16x16x32_bf16 a[212:215], a[40:43], a[104:107], a[212:215]\n" \
"ds_read_b128 a[36:39], v73 offset:8192\n" \
"v_mfma_f32_16x16x32_bf16 a[212:215], a[44:47], a[108:111], a[212:215]\n" \
"v_mfma_f32_16x16x32_bf16 a[208:211], a[40:43], a[96:99], a[208:211]\n" \
"ds_read_b64_tr_b16 a[90:91], v75 offset:37376\n" \
"v_mfma_f32_16x16x32_bf16 a[208:211], a[44:47], a[100:103], a[208:211]\n" \
"v_mfma_f32_16x16x32_bf16 a[224:227], a[48:51], a[96:99], a[224:227]\n" \
"ds_read_b128 a[40:43], v72 offset:10240\n" \
"v_mfma_f32_16x16x32_bf16 a[224:227], a[52:55], a[100:103], a[224:227]\n" \
"v_mfma_f32_16x16x32_bf16 a[228:231], a[48:51], a[104:107], a[228:231]\n" \
"ds_read_b64_tr_b16 a[92:93], v75 offset:49664\n" \
"v_mfma_f32_16x16x32_bf16 a[228:231], a[52:55], a[108:111], a[228:231]\n" \
"v_mfma_f32_16x16x32_bf16 a[232:235], a[48:51], a[112:115], a[232:235]\n" \
"ds_read_b128 a[44:47], v73 offset:10240\n" \
"v_mfma_f32_16x16x32_bf16 a[232:235], a[52:55], a[116:119], a[232:235]\n" \
"v_mfma_f32_16x16x32_bf16 a[236:239], a[48:51], a[120:123], a[236:239]\n" \
"ds_read_b64_tr_b16 a[94:95], v75 offset:53760\n" \
"v_mfma_f32_16x16x32_bf16 a[236:239], a[52:55], a[124:127], a[236:239]\n" \
"v_mfma_f32_16x16x32_bf16 a[252:255], a[56:59], a[120:123], a[252:255]\n" \
"ds_read_b128 a[48:51], v72 offset:12288\n" \
"v_mfma_f32_16x16x32_bf16 a[252:255], a[60:63], a[124:127], a[252:255]\n" \
"v_mfma_f32_16x16x32_bf16 a[248:251], a[56:59], a[112:115], a[248:251]\n" \
"v_mfma_f32_16x16x32_bf16 a[248:251], a[60:63], a[116:119], a[248:251]\n" \
"v_mfma_f32_16x16x32_bf16 a[244:247], a[56:59], a[104:107], a[244:247]\n" \
"ds_read_b128 a[52:55], v73 offset:12288\n" \
"v_mfma_f32_16x16x32_bf16 a[244:247], a[60:63], a[108:111], a[244:247]\n" \
"v_mfma_f32_16x16x32_bf16 a[240:243], a[56:59], a[96:99], a[240:243]\n" \
"v_mfma_f32_16x16x32_bf16 a[240:243], a[60:63], a[100:103], a[240:243]\n" \
"s_waitcnt lgkmcnt(1)\n" \
"v_mfma_f32_16x16x32_bf16 v[0:3], a[64:67], a[0:3], v[0:3]\n" \
"ds_read_b128 a[56:59], v72 offset:14336\n" \
"v_mfma_f32_16x16x32_bf16 v[0:3], a[68:71], a[4:7], v[0:3]\n" \
"v_mfma_f32_16x16x32_bf16 v[4:7], a[72:75], a[0:3], v[4:7]\n" \
"s_add_u32 m0, s74, 98304\n" \
"ds_read_b128 a[60:63], v73 offset:14336\n" \
"v_mfma_f32_16x16x32_bf16 v[4:7], a[76:79], a[4:7], v[4:7]\n" \
"buffer_load_dwordx4 v68, s[40:43], s83 offen offset:0 lds\n" \
"v_mfma_f32_16x16x32_bf16 v[8:11], a[80:83], a[0:3], v[8:11]\n" \
"ds_read_b64_tr_b16 a[96:97], v74 offset:33792\n" \
"v_mfma_f32_16x16x32_bf16 v[8:11], a[84:87], a[4:7], v[8:11]\n" \
"v_mfma_f32_16x16x32_bf16 v[12:15], a[88:91], a[0:3], v[12:15]\n" \
"ds_read_b64_tr_b16 a[98:99], v74 offset:37888\n" \
"v_mfma_f32_16x16x32_bf16 v[12:15], a[92:95], a[4:7], v[12:15]\n" \
"buffer_load_dwordx4 v69, s[44:47], s83 offen offset:1024 lds\n" \
"v_mfma_f32_16x16x32_bf16 v[28:31], a[88:91], a[8:11], v[28:31]\n" \
"ds_read_b64_tr_b16 a[100:101], v74 offset:50176\n" \
"v_mfma_f32_16x16x32_bf16 v[28:31], a[92:95], a[12:15], v[28:31]\n" \
"v_mfma_f32_16x16x32_bf16 v[24:27], a[80:83], a[8:11], v[24:27]\n" \
"ds_read_b64_tr_b16 a[102:103], v74 offset:54272\n" \
"v_mfma_f32_16x16x32_bf16 v[24:27], a[84:87], a[12:15], v[24:27]\n" \
"buffer_load_dwordx4 v70, s[40:43], s83 offen offset:2048 lds\n" \
"v_mfma_f32_16x16x32_bf16 v[20:23], a[72:75], a[8:11], v[20:23]\n" \
"ds_read_b64_tr_b16 a[104:105], v75 offset:33792\n" \
"v_mfma_f32_16x16x32_bf16 v[20:23], a[76:79], a[12:15], v[20:23]\n" \
"v_mfma_f32_16x16x32_bf16 v[16:19], a[64:67], a[8:11], v[16:19]\n" \
"ds_read_b64_tr_b16 a[106:107], v75 offset:37888\n" \
"v_mfma_f32_16x16x32_bf16 v[16:19], a[68:71], a[12:15], v[16:19]\n" \
"buffer_load_dwordx4 v71, s[44:47], s83 offen offset:3072 lds\n" \
"v_mfma_f32_16x16x32_bf16 v[32:35], a[64:67], a[16:19], v[32:35]\n" \
"ds_read_b64_tr_b16 a[108:109], v75 offset:50176\n" \
"v_mfma_f32_16x16x32_bf16 v[32:35], a[68:71], a[20:23], v[32:35]\n" \
"v_mfma_f32_16x16x32_bf16 v[36:39], a[72:75], a[16:19], v[36:39]\n" \
"s_add_u32 m0, s74, 102400\n" \
"ds_read_b64_tr_b16 a[110:111], v75 offset:54272\n" \
"v_mfma_f32_16x16x32_bf16 v[36:39], a[76:79], a[20:23], v[36:39]\n" \
"buffer_load_dwordx4 v68, s[40:43], s84 offen offset:0 lds\n" \
"v_mfma_f32_16x16x32_bf16 v[40:43], a[80:83], a[16:19], v[40:43]\n" \
"ds_read_b64_tr_b16 a[112:113], v74 offset:34304\n" \
"v_mfma_f32_16x16x32_bf16 v[40:43], a[84:87], a[20:23], v[40:43]\n" \
"v_mfma_f32_16x16x32_bf16 v[44:47], a[88:91], a[16:19], v[44:47]\n" \
"ds_read_b64_tr_b16 a[114:115], v74 offset:38400\n" \
"v_mfma_f32_16x16x32_bf16 v[44:47], a[92:95], a[20:23], v[44:47]\n" \
"buffer_load_dwordx4 v69, s[44:47], s84 offen offset:1024 lds\n" \
"v_mfma_f32_16x16x32_bf16 v[60:63], a[88:91], a[24:27], v[60:63]\n" \
"ds_read_b64_tr_b16 a[116:117], v74 offset:50688\n" \
"v_mfma_f32_16x16x32_bf16 v[60:63], a[92:95], a[28:31], v[60:63]\n" \
"v_mfma_f32_16x16x32_bf16 v[56:59], a[80:83], a[24:27], v[56:59]\n" \
"ds_read_b64_tr_b16 a[118:119], v74 offset:54784\n" \
"v_mfma_f32_16x16x32_bf16 v[56:59], a[84:87], a[28:31], v[56:59]\n" \
"buffer_load_dwordx4 v70, s[40:43], s84 offen offset:2048 lds\n" \
"v_mfma_f32_16x16x32_bf16 v[52:55], a[72:75], a[24:27], v[52:55]\n" \
"ds_read_b64_tr_b16 a[120:121], v75 offset:34304\n" \
"v_mfma_f32_16x16x32_bf16 v[52:55], a[76:79], a[28:31], v[52:55]\n" \
"v_mfma_f32_16x16x32_bf16 v[48:51], a[64:67], a[24:27], v[48:51]\n" \
"ds_read_b64_tr_b16 a[122:123], v75 offset:38400\n" \
"v_mfma_f32_16x16x32_bf16 v[48:51], a[68:71], a[28:31], v[48:51]\n" \
"buffer_load_dwordx4 v71, s[44:47], s84 offen offset:3072 lds\n" \
"v_mfma_f32_16x16x32_bf16 v[144:147], a[64:67], a[32:35], v[144:147]\n" \
"ds_read_b64_tr_b16 a[124:125], v75 offset:50688\n" \
"v_mfma_f32_16x16x32_bf16 v[144:147], a[68:71], a[36:39], v[144:147]\n" \
"v_mfma_f32_16x16x32_bf16 v[148:151], a[72:75], a[32:35], v[148:151]\n" \
"ds_read_b64_tr_b16 a[126:127], v75 offset:54784\n" \
"v_mfma_f32_16x16x32_bf16 v[148:151], a[76:79], a[36:39], v[148:151]\n" \
"v_mfma_f32_16x16x32_bf16 v[152:155], a[80:83], a[32:35], v[152:155]\n" \
"v_mfma_f32_16x16x32_bf16 v[152:155], a[84:87], a[36:39], v[152:155]\n" \
"v_mfma_f32_16x16x32_bf16 v[156:159], a[88:91], a[32:35], v[156:159]\n" \
"v_mfma_f32_16x16x32_bf16 v[156:159], a[92:95], a[36:39], v[156:159]\n" \
"v_mfma_f32_16x16x32_bf16 v[188:191], a[88:91], a[40:43], v[188:191]\n" \
"v_mfma_f32_16x16x32_bf16 v[188:191], a[92:95], a[44:47], v[188:191]\n" \
"v_mfma_f32_16x16x32_bf16 v[184:187], a[80:83], a[40:43], v[184:187]\n" \
"v_mfma_f32_16x16x32_bf16 v[184:187], a[84:87], a[44:47], v[184:187]\n" \
"v_mfma_f32_16x16x32_bf16 v[180:183], a[72:75], a[40:43], v[180:183]\n" \
"v_mfma_f32_16x16x32_bf16 v[180:183], a[76:79], a[44:47], v[180:183]\n" \
"v_mfma_f32_16x16x32_bf16 v[176:179], a[64:67], a[40:43], v[176:179]\n" \
"v_mfma_f32_16x16x32_bf16 v[176:179], a[68:71], a[44:47], v[176:179]\n" \
"s_waitcnt lgkmcnt(0)\n" \
"v_mfma_f32_16x16x32_bf16 v[192:195], a[64:67], a[48:51], v[192:195]\n" \
"v_mfma_f32_16x16x32_bf16 v[192:195], a[68:71], a[52:55], v[192:195]\n" \
"v_mfma_f32_16x16x32_bf16 v[196:199], a[72:75], a[48:51], v[196:199]\n" \
"v_mfma_f32_16x16x32_bf16 v[196:199], a[76:79], a[52:55], v[196:199]\n" \
"v_mfma_f32_16x16x32_bf16 v[200:203], a[80:83], a[48:51], v[200:203]\n" \
"v_mfma_f32_16x16x32_bf16 v[200:203], a[84:87], a[52:55], v[200:203]\n" \
"v_mfma_f32_16x16x32_bf16 v[204:207], a[88:91], a[48:51], v[204:207]\n" \
"v_mfma_f32_16x16x32_bf16 v[204:207], a[92:95], a[52:55], v[204:207]\n" \
"v_mfma_f32_16x16x32_bf16 v[220:223], a[88:91], a[56:59], v[220:223]\n" \
"v_mfma_f32_16x16x32_bf16 v[220:223], a[92:95], a[60:63], v[220:223]\n" \
"v_mfma_f32_16x16x32_bf16 v[216:219], a[80:83], a[56:59], v[216:219]\n" \
"v_mfma_f32_16x16x32_bf16 v[216:219], a[84:87], a[60:63], v[216:219]\n" \
"v_mfma_f32_16x16x32_bf16 v[212:215], a[72:75], a[56:59], v[212:215]\n" \
"v_mfma_f32_16x16x32_bf16 v[212:215], a[76:79], a[60:63], v[212:215]\n" \
"v_mfma_f32_16x16x32_bf16 v[208:211], a[64:67], a[56:59], v[208:211]\n" \
"v_mfma_f32_16x16x32_bf16 v[208:211], a[68:71], a[60:63], v[208:211]\n" \
"s_waitcnt vmcnt(11) lgkmcnt(0)\n" \
"s_barrier\n" \
"v_mfma_f32_16x16x32_bf16 a[128:131], a[0:3], a[96:99], a[128:131]\n" \
"s_add_u32 m0, s74, 32768\n" \
"ds_read_b64_tr_b16 a[64:65], v102 offset:0\n" \
"v_mfma_f32_16x16x32_bf16 a[128:131], a[4:7], a[100:103], a[128:131]\n" \
"buffer_load_dwordx4 v64, s[36:39], s81 offen offset:0 lds\n" \
"v_mfma_f32_16x16x32_bf16 a[132:135], a[0:3], a[104:107], a[132:135]\n" \
"ds_read_b64_tr_b16 a[66:67], v102 offset:4096\n" \
"v_mfma_f32_16x16x32_bf16 a[132:135], a[4:7], a[108:111], a[132:135]\n" \
"v_mfma_f32_16x16x32_bf16 a[136:139], a[0:3], a[112:115], a[136:139]\n" \
"ds_read_b64_tr_b16 a[68:69], v102 offset:16384\n" \
"v_mfma_f32_16x16x32_bf16 a[136:139], a[4:7], a[116:119], a[136:139]\n" \
"buffer_load_dwordx4 v65, s[36:39], s81 offen offset:1024 lds\n" \
"v_mfma_f32_16x16x32_bf16 a[140:143], a[0:3], a[120:123], a[140:143]\n" \
"ds_read_b64_tr_b16 a[70:71], v102 offset:20480\n" \
"v_mfma_f32_16x16x32_bf16 a[140:143], a[4:7], a[124:127], a[140:143]\n" \
"v_mfma_f32_16x16x32_bf16 a[156:159], a[8:11], a[120:123], a[156:159]\n" \
"ds_read_b128 a[0:3], v96 offset:0\n" \
"v_mfma_f32_16x16x32_bf16 a[156:159], a[12:15], a[124:127], a[156:159]\n" \
"buffer_load_dwordx4 v66, s[36:39], s81 offen offset:2048 lds\n" \
"v_mfma_f32_16x16x32_bf16 a[152:155], a[8:11], a[112:115], a[152:155]\n" \
"ds_read_b64_tr_b16 a[72:73], v103 offset:0\n" \
"v_mfma_f32_16x16x32_bf16 a[152:155], a[12:15], a[116:119], a[152:155]\n" \
"v_mfma_f32_16x16x32_bf16 a[148:151], a[8:11], a[104:107], a[148:151]\n" \
"ds_read_b128 a[4:7], v97 offset:0\n" \
"v_mfma_f32_16x16x32_bf16 a[148:151], a[12:15], a[108:111], a[148:151]\n" \
"buffer_load_dwordx4 v67, s[36:39], s81 offen offset:3072 lds\n" \
"v_mfma_f32_16x16x32_bf16 a[144:147], a[8:11], a[96:99], a[144:147]\n" \
"ds_read_b64_tr_b16 a[74:75], v103 offset:4096\n" \
"v_mfma_f32_16x16x32_bf16 a[144:147], a[12:15], a[100:103], a[144:147]\n" \
"v_mfma_f32_16x16x32_bf16 a[160:163], a[16:19], a[96:99], a[160:163]\n" \
"s_add_u32 m0, s74, 36864\n" \
"ds_read_b128 a[8:11], v96 offset:2048\n" \
"v_mfma_f32_16x16x32_bf16 a[160:163], a[20:23], a[100:103], a[160:163]\n" \
"buffer_load_dwordx4 v64, s[36:39], s82 offen offset:0 lds\n" \
"v_mfma_f32_16x16x32_bf16 a[164:167], a[16:19], a[104:107], a[164:167]\n" \
"ds_read_b64_tr_b16 a[76:77], v103 offset:16384\n" \
"v_mfma_f32_16x16x32_bf16 a[164:167], a[20:23], a[108:111], a[164:167]\n" \
"v_mfma_f32_16x16x32_bf16 a[168:171], a[16:19], a[112:115], a[168:171]\n" \
"ds_read_b128 a[12:15], v97 offset:2048\n" \
"v_mfma_f32_16x16x32_bf16 a[168:171], a[20:23], a[116:119], a[168:171]\n" \
"buffer_load_dwordx4 v65, s[36:39], s82 offen offset:1024 lds\n" \
"v_mfma_f32_16x16x32_bf16 a[172:175], a[16:19], a[120:123], a[172:175]\n" \
"ds_read_b64_tr_b16 a[78:79], v103 offset:20480\n" \
"v_mfma_f32_16x16x32_bf16 a[172:175], a[20:23], a[124:127], a[172:175]\n" \
"v_mfma_f32_16x16x32_bf16 a[188:191], a[24:27], a[120:123], a[188:191]\n" \
"ds_read_b128 a[16:19], v96 offset:4096\n" \
"v_mfma_f32_16x16x32_bf16 a[188:191], a[28:31], a[124:127], a[188:191]\n" \
"buffer_load_dwordx4 v66, s[36:39], s82 offen offset:2048 lds\n" \
"v_mfma_f32_16x16x32_bf16 a[184:187], a[24:27], a[112:115], a[184:187]\n" \
"ds_read_b64_tr_b16 a[80:81], v102 offset:512\n" \
"v_mfma_f32_16x16x32_bf16 a[184:187], a[28:31], a[116:119], a[184:187]\n" \
"v_mfma_f32_16x16x32_bf16 a[180:183], a[24:27], a[104:107], a[180:183]\n" \
"ds_read_b128 a[20:23], v97 offset:4096\n" \
"v_mfma_f32_16x16x32_bf16 a[180:183], a[28:31], a[108:111], a[180:183]\n" \
"buffer_load_dwordx4 v67, s[36:39], s82 offen offset:3072 lds\n" \
"v_mfma_f32_16x16x32_bf16 a[176:179], a[24:27], a[96:99], a[176:179]\n" \
"s_mov_b64 s[64:65], s[12:13]\n" \
"s_add_u32 s68, s67, 0x300000\n" \
"ds_read_b64_tr_b16 a[82:83], v102 offset:4608\n" \
"v_mfma_f32_16x16x32_bf16 a[176:179], a[28:31], a[100:103], a[176:179]\n" \
"s_add_u32 s64, s64, s68\n" \
"s_addc_u32 s65, s65, 0\n" \
"v_mfma_f32_16x16x32_bf16 a[192:195], a[32:35], a[96:99], a[192:195]\n" \
"ds_read_b128 a[24:27], v96 offset:6144\n" \
"v_mfma_f32_16x16x32_bf16 a[192:195], a[36:39], a[100:103], a[192:195]\n" \
"global_load_dword v238, v234, s[64:65]\n" \
"v_mfma_f32_16x16x32_bf16 a[196:199], a[32:35], a[104:107], a[196:199]\n" \
"s_mov_b64 s[64:65], s[12:13]\n" \
"s_add_u32 s68, s67, 0x400000\n" \
"ds_read_b64_tr_b16 a[84:85], v102 offset:16896\n" \
"v_mfma_f32_16x16x32_bf16 a[196:199], a[36:39], a[108:111], a[196:199]\n" \
"s_add_u32 s64, s64, s68\n" \
"s_addc_u32 s65, s65, 0\n" \
"v_mfma_f32_16x16x32_bf16 a[200:203], a[32:35], a[112:115], a[200:203]\n" \
"ds_read_b128 a[28:31], v97 offset:6144\n" \
"v_mfma_f32_16x16x32_bf16 a[200:203], a[36:39], a[116:119], a[200:203]\n" \
"global_load_dword v238, v234, s[64:65]\n" \
"v_mfma_f32_16x16x32_bf16 a[204:207], a[32:35], a[120:123], a[204:207]\n" \
"s_mov_b64 s[64:65], s[12:13]\n" \
"s_add_u32 s68, s67, 0x600000\n" \
"ds_read_b64_tr_b16 a[86:87], v102 offset:20992\n" \
"v_mfma_f32_16x16x32_bf16 a[204:207], a[36:39], a[124:127], a[204:207]\n" \
"s_add_u32 s64, s64, s68\n" \
"s_addc_u32 s65, s65, 0\n" \
"v_mfma_f32_16x16x32_bf16 a[220:223], a[40:43], a[120:123], a[220:223]\n" \
"ds_read_b128 a[32:35], v96 offset:8192\n" \
"v_mfma_f32_16x16x32_bf16 a[220:223], a[44:47], a[124:127], a[220:223]\n" \
"global_load_dword v238, v234, s[64:65]\n" \
"v_mfma_f32_16x16x32_bf16 a[216:219], a[40:43], a[112:115], a[216:219]\n" \
"ds_read_b64_tr_b16 a[88:89], v103 offset:512\n" \
"v_mfma_f32_16x16x32_bf16 a[216:219], a[44:47], a[116:119], a[216:219]\n" \
"v_mfma_f32_16x16x32_bf16 a[212:215], a[40:43], a[104:107], a[212:215]\n" \
"ds_read_b128 a[36:39], v97 offset:8192\n" \
"v_mfma_f32_16x16x32_bf16 a[212:215], a[44:47], a[108:111], a[212:215]\n" \
"v_mfma_f32_16x16x32_bf16 a[208:211], a[40:43], a[96:99], a[208:211]\n" \
"ds_read_b64_tr_b16 a[90:91], v103 offset:4608\n" \
"v_mfma_f32_16x16x32_bf16 a[208:211], a[44:47], a[100:103], a[208:211]\n" \
"v_mfma_f32_16x16x32_bf16 a[224:227], a[48:51], a[96:99], a[224:227]\n" \
"ds_read_b128 a[40:43], v96 offset:10240\n" \
"v_mfma_f32_16x16x32_bf16 a[224:227], a[52:55], a[100:103], a[224:227]\n" \
"v_mfma_f32_16x16x32_bf16 a[228:231], a[48:51], a[104:107], a[228:231]\n" \
"ds_read_b64_tr_b16 a[92:93], v103 offset:16896\n" \
"v_mfma_f32_16x16x32_bf16 a[228:231], a[52:55], a[108:111], a[228:231]\n" \
"v_mfma_f32_16x16x32_bf16 a[232:235], a[48:51], a[112:115], a[232:235]\n" \
"ds_read_b128 a[44:47], v97 offset:10240\n" \
"v_mfma_f32_16x16x32_bf16 a[232:235], a[52:55], a[116:119], a[232:235]\n" \
"v_mfma_f32_16x16x32_bf16 a[236:239], a[48:51], a[120:123], a[236:239]\n" \
"ds_read_b64_tr_b16 a[94:95], v103 offset:20992\n" \
"v_mfma_f32_16x16x32_bf16 a[236:239], a[52:55], a[124:127], a[236:239]\n" \
"v_mfma_f32_16x16x32_bf16 a[252:255], a[56:59], a[120:123], a[252:255]\n" \
"ds_read_b128 a[48:51], v96 offset:12288\n" \
"v_mfma_f32_16x16x32_bf16 a[252:255], a[60:63], a[124:127], a[252:255]\n" \
"v_mfma_f32_16x16x32_bf16 a[248:251], a[56:59], a[112:115], a[248:251]\n" \
"v_mfma_f32_16x16x32_bf16 a[248:251], a[60:63], a[116:119], a[248:251]\n" \
"v_mfma_f32_16x16x32_bf16 a[244:247], a[56:59], a[104:107], a[244:247]\n" \
"ds_read_b128 a[52:55], v97 offset:12288\n" \
"v_mfma_f32_16x16x32_bf16 a[244:247], a[60:63], a[108:111], a[244:247]\n" \
"v_mfma_f32_16x16x32_bf16 a[240:243], a[56:59], a[96:99], a[240:243]\n" \
"v_mfma_f32_16x16x32_bf16 a[240:243], a[60:63], a[100:103], a[240:243]\n" \
"s_waitcnt lgkmcnt(1)\n" \
"v_mfma_f32_16x16x32_bf16 v[0:3], a[64:67], a[0:3], v[0:3]\n" \
"ds_read_b128 a[56:59], v96 offset:14336\n" \
"Lpg1663_%=:\n" \
"Lst0x2_%=:\n" \
"s_cbranch_execz Lpg3897_%=\n" \
"v_mfma_f32_16x16x32_bf16 v[0:3], a[68:71], a[4:7], v[0:3]\n" \
"v_mfma_f32_16x16x32_bf16 v[4:7], a[72:75], a[0:3], v[4:7]\n" \
"s_add_u32 m0, s79, 0\n" \
"ds_read_b128 a[60:63], v97 offset:14336\n" \
"v_mfma_f32_16x16x32_bf16 v[4:7], a[76:79], a[4:7], v[4:7]\n" \
"buffer_load_dwordx4 v228, s[52:55], s78 offen offset:0 lds\n" \
"v_mfma_f32_16x16x32_bf16 v[8:11], a[80:83], a[0:3], v[8:11]\n" \
"ds_read_b64_tr_b16 a[96:97], v102 offset:1024\n" \
"v_mfma_f32_16x16x32_bf16 v[8:11], a[84:87], a[4:7], v[8:11]\n" \
"v_mfma_f32_16x16x32_bf16 v[12:15], a[88:91], a[0:3], v[12:15]\n" \
"ds_read_b64_tr_b16 a[98:99], v102 offset:5120\n" \
"v_mfma_f32_16x16x32_bf16 v[12:15], a[92:95], a[4:7], v[12:15]\n" \
"buffer_load_dwordx4 v229, s[52:55], s78 offen offset:1024 lds\n" \
"v_mfma_f32_16x16x32_bf16 v[28:31], a[88:91], a[8:11], v[28:31]\n" \
"ds_read_b64_tr_b16 a[100:101], v102 offset:17408\n" \
"v_mfma_f32_16x16x32_bf16 v[28:31], a[92:95], a[12:15], v[28:31]\n" \
"v_mfma_f32_16x16x32_bf16 v[24:27], a[80:83], a[8:11], v[24:27]\n" \
"s_xor_b32 s78, s78, 0x180000\n" \
"ds_read_b64_tr_b16 a[102:103], v102 offset:21504\n" \
"v_mfma_f32_16x16x32_bf16 v[24:27], a[84:87], a[12:15], v[24:27]\n" \
"buffer_load_dwordx4 v228, s[52:55], s89 offen offset:2048 lds\n" \
"v_mfma_f32_16x16x32_bf16 v[20:23], a[72:75], a[8:11], v[20:23]\n" \
"ds_read_b64_tr_b16 a[104:105], v103 offset:1024\n" \
"v_mfma_f32_16x16x32_bf16 v[20:23], a[76:79], a[12:15], v[20:23]\n" \
"v_mfma_f32_16x16x32_bf16 v[16:19], a[64:67], a[8:11], v[16:19]\n" \
"ds_read_b64_tr_b16 a[106:107], v103 offset:5120\n" \
"v_mfma_f32_16x16x32_bf16 v[16:19], a[68:71], a[12:15], v[16:19]\n" \
"buffer_load_dwordx4 v229, s[52:55], s89 offen offset:3072 lds\n" \
"v_mfma_f32_16x16x32_bf16 v[32:35], a[64:67], a[16:19], v[32:35]\n" \
"ds_read_b64_tr_b16 a[108:109], v103 offset:17408\n" \
"v_mfma_f32_16x16x32_bf16 v[32:35], a[68:71], a[20:23], v[32:35]\n" \
"s_xor_b32 s89, s89, 0x180000\n" \
"v_mfma_f32_16x16x32_bf16 v[36:39], a[72:75], a[16:19], v[36:39]\n" \
"s_add_u32 m0, s79, 16384\n" \
"ds_read_b64_tr_b16 a[110:111], v103 offset:21504\n" \
"v_mfma_f32_16x16x32_bf16 v[36:39], a[76:79], a[20:23], v[36:39]\n" \
"buffer_load_dwordx4 v228, s[52:55], s78 offen offset:0 lds\n" \
"v_mfma_f32_16x16x32_bf16 v[40:43], a[80:83], a[16:19], v[40:43]\n" \
"ds_read_b64_tr_b16 a[112:113], v102 offset:1536\n" \
"v_mfma_f32_16x16x32_bf16 v[40:43], a[84:87], a[20:23], v[40:43]\n" \
"v_mfma_f32_16x16x32_bf16 v[44:47], a[88:91], a[16:19], v[44:47]\n" \
"ds_read_b64_tr_b16 a[114:115], v102 offset:5632\n" \
"v_mfma_f32_16x16x32_bf16 v[44:47], a[92:95], a[20:23], v[44:47]\n" \
"buffer_load_dwordx4 v229, s[52:55], s78 offen offset:1024 lds\n" \
"v_mfma_f32_16x16x32_bf16 v[60:63], a[88:91], a[24:27], v[60:63]\n" \
"ds_read_b64_tr_b16 a[116:117], v102 offset:17920\n" \
"v_mfma_f32_16x16x32_bf16 v[60:63], a[92:95], a[28:31], v[60:63]\n" \
"v_mfma_f32_16x16x32_bf16 v[56:59], a[80:83], a[24:27], v[56:59]\n" \
"s_xor_b32 s78, s78, 0x400000\n" \
"ds_read_b64_tr_b16 a[118:119], v102 offset:22016\n" \
"v_mfma_f32_16x16x32_bf16 v[56:59], a[84:87], a[28:31], v[56:59]\n" \
"buffer_load_dwordx4 v228, s[52:55], s89 offen offset:2048 lds\n" \
"v_mfma_f32_16x16x32_bf16 v[52:55], a[72:75], a[24:27], v[52:55]\n" \
"ds_read_b64_tr_b16 a[120:121], v103 offset:1536\n" \
"v_mfma_f32_16x16x32_bf16 v[52:55], a[76:79], a[28:31], v[52:55]\n" \
"v_mfma_f32_16x16x32_bf16 v[48:51], a[64:67], a[24:27], v[48:51]\n" \
"ds_read_b64_tr_b16 a[122:123], v103 offset:5632\n" \
"v_mfma_f32_16x16x32_bf16 v[48:51], a[68:71], a[28:31], v[48:51]\n" \
"buffer_load_dwordx4 v229, s[52:55], s89 offen offset:3072 lds\n" \
"v_mfma_f32_16x16x32_bf16 v[144:147], a[64:67], a[32:35], v[144:147]\n" \
"ds_read_b64_tr_b16 a[124:125], v103 offset:17920\n" \
"v_mfma_f32_16x16x32_bf16 v[144:147], a[68:71], a[36:39], v[144:147]\n" \
"v_mfma_f32_16x16x32_bf16 v[148:151], a[72:75], a[32:35], v[148:151]\n" \
"s_xor_b32 s89, s89, 0x400000\n" \
"ds_read_b64_tr_b16 a[126:127], v103 offset:22016\n" \
"v_mfma_f32_16x16x32_bf16 v[148:151], a[76:79], a[36:39], v[148:151]\n" \
"v_mfma_f32_16x16x32_bf16 v[152:155], a[80:83], a[32:35], v[152:155]\n" \
"v_mfma_f32_16x16x32_bf16 v[152:155], a[84:87], a[36:39], v[152:155]\n" \
"v_mfma_f32_16x16x32_bf16 v[156:159], a[88:91], a[32:35], v[156:159]\n" \
"v_mfma_f32_16x16x32_bf16 v[156:159], a[92:95], a[36:39], v[156:159]\n" \
"v_mfma_f32_16x16x32_bf16 v[188:191], a[88:91], a[40:43], v[188:191]\n" \
"v_mfma_f32_16x16x32_bf16 v[188:191], a[92:95], a[44:47], v[188:191]\n" \
"v_mfma_f32_16x16x32_bf16 v[184:187], a[80:83], a[40:43], v[184:187]\n" \
"v_mfma_f32_16x16x32_bf16 v[184:187], a[84:87], a[44:47], v[184:187]\n" \
"v_mfma_f32_16x16x32_bf16 v[180:183], a[72:75], a[40:43], v[180:183]\n" \
"v_mfma_f32_16x16x32_bf16 v[180:183], a[76:79], a[44:47], v[180:183]\n" \
"v_mfma_f32_16x16x32_bf16 v[176:179], a[64:67], a[40:43], v[176:179]\n" \
"v_mfma_f32_16x16x32_bf16 v[176:179], a[68:71], a[44:47], v[176:179]\n" \
"s_waitcnt lgkmcnt(0)\n" \
"v_mfma_f32_16x16x32_bf16 v[192:195], a[64:67], a[48:51], v[192:195]\n" \
"v_mfma_f32_16x16x32_bf16 v[192:195], a[68:71], a[52:55], v[192:195]\n" \
"v_mfma_f32_16x16x32_bf16 v[196:199], a[72:75], a[48:51], v[196:199]\n" \
"v_mfma_f32_16x16x32_bf16 v[196:199], a[76:79], a[52:55], v[196:199]\n" \
"v_mfma_f32_16x16x32_bf16 v[200:203], a[80:83], a[48:51], v[200:203]\n" \
"v_mfma_f32_16x16x32_bf16 v[200:203], a[84:87], a[52:55], v[200:203]\n" \
"v_mfma_f32_16x16x32_bf16 v[204:207], a[88:91], a[48:51], v[204:207]\n" \
"v_mfma_f32_16x16x32_bf16 v[204:207], a[92:95], a[52:55], v[204:207]\n" \
"v_mfma_f32_16x16x32_bf16 v[220:223], a[88:91], a[56:59], v[220:223]\n" \
"v_mfma_f32_16x16x32_bf16 v[220:223], a[92:95], a[60:63], v[220:223]\n" \
"v_mfma_f32_16x16x32_bf16 v[216:219], a[80:83], a[56:59], v[216:219]\n" \
"v_mfma_f32_16x16x32_bf16 v[216:219], a[84:87], a[60:63], v[216:219]\n" \
"v_mfma_f32_16x16x32_bf16 v[212:215], a[72:75], a[56:59], v[212:215]\n" \
"v_mfma_f32_16x16x32_bf16 v[212:215], a[76:79], a[60:63], v[212:215]\n" \
"v_mfma_f32_16x16x32_bf16 v[208:211], a[64:67], a[56:59], v[208:211]\n" \
"v_mfma_f32_16x16x32_bf16 v[208:211], a[68:71], a[60:63], v[208:211]\n" \
"s_waitcnt vmcnt(11) lgkmcnt(0)\n" \
"s_barrier\n" \
"v_mfma_f32_16x16x32_bf16 a[128:131], a[0:3], a[96:99], a[128:131]\n" \
"s_add_u32 m0, s74, 131072\n" \
"ds_read_b64_tr_b16 a[64:65], v98 offset:32768\n" \
"v_mfma_f32_16x16x32_bf16 a[128:131], a[4:7], a[100:103], a[128:131]\n" \
"buffer_load_dwordx4 v224, s[48:51], s75 offen offset:0 lds\n" \
"v_mfma_f32_16x16x32_bf16 a[132:135], a[0:3], a[104:107], a[132:135]\n" \
"ds_read_b64_tr_b16 a[66:67], v98 offset:36864\n" \
"v_mfma_f32_16x16x32_bf16 a[132:135], a[4:7], a[108:111], a[132:135]\n" \
"v_mfma_f32_16x16x32_bf16 a[136:139], a[0:3], a[112:115], a[136:139]\n" \
"ds_read_b64_tr_b16 a[68:69], v98 offset:49152\n" \
"v_mfma_f32_16x16x32_bf16 a[136:139], a[4:7], a[116:119], a[136:139]\n" \
"buffer_load_dwordx4 v225, s[48:51], s75 offen offset:1024 lds\n" \
"v_mfma_f32_16x16x32_bf16 a[140:143], a[0:3], a[120:123], a[140:143]\n" \
"ds_read_b64_tr_b16 a[70:71], v98 offset:53248\n" \
"v_mfma_f32_16x16x32_bf16 a[140:143], a[4:7], a[124:127], a[140:143]\n" \
"v_mfma_f32_16x16x32_bf16 a[156:159], a[8:11], a[120:123], a[156:159]\n" \
"ds_read_b128 a[0:3], v72 offset:32768\n" \
"v_mfma_f32_16x16x32_bf16 a[156:159], a[12:15], a[124:127], a[156:159]\n" \
"buffer_load_dwordx4 v226, s[48:51], s75 offen offset:2048 lds\n" \
"v_mfma_f32_16x16x32_bf16 a[152:155], a[8:11], a[112:115], a[152:155]\n" \
"ds_read_b64_tr_b16 a[72:73], v99 offset:32768\n" \
"v_mfma_f32_16x16x32_bf16 a[152:155], a[12:15], a[116:119], a[152:155]\n" \
"v_mfma_f32_16x16x32_bf16 a[148:151], a[8:11], a[104:107], a[148:151]\n" \
"ds_read_b128 a[4:7], v73 offset:32768\n" \
"v_mfma_f32_16x16x32_bf16 a[148:151], a[12:15], a[108:111], a[148:151]\n" \
"buffer_load_dwordx4 v227, s[48:51], s75 offen offset:3072 lds\n" \
"v_mfma_f32_16x16x32_bf16 a[144:147], a[8:11], a[96:99], a[144:147]\n" \
"ds_read_b64_tr_b16 a[74:75], v99 offset:36864\n" \
"v_mfma_f32_16x16x32_bf16 a[144:147], a[12:15], a[100:103], a[144:147]\n" \
"s_xor_b32 s75, s75, 0x180\n" \
"v_mfma_f32_16x16x32_bf16 a[160:163], a[16:19], a[96:99], a[160:163]\n" \
"s_add_u32 m0, s74, 135168\n" \
"ds_read_b128 a[8:11], v72 offset:34816\n" \
"v_mfma_f32_16x16x32_bf16 a[160:163], a[20:23], a[100:103], a[160:163]\n" \
"buffer_load_dwordx4 v224, s[48:51], s77 offen offset:0 lds\n" \
"v_mfma_f32_16x16x32_bf16 a[164:167], a[16:19], a[104:107], a[164:167]\n" \
"ds_read_b64_tr_b16 a[76:77], v99 offset:49152\n" \
"v_mfma_f32_16x16x32_bf16 a[164:167], a[20:23], a[108:111], a[164:167]\n" \
"v_mfma_f32_16x16x32_bf16 a[168:171], a[16:19], a[112:115], a[168:171]\n" \
"ds_read_b128 a[12:15], v73 offset:34816\n" \
"v_mfma_f32_16x16x32_bf16 a[168:171], a[20:23], a[116:119], a[168:171]\n" \
"buffer_load_dwordx4 v225, s[48:51], s77 offen offset:1024 lds\n" \
"v_mfma_f32_16x16x32_bf16 a[172:175], a[16:19], a[120:123], a[172:175]\n" \
"ds_read_b64_tr_b16 a[78:79], v99 offset:53248\n" \
"v_mfma_f32_16x16x32_bf16 a[172:175], a[20:23], a[124:127], a[172:175]\n" \
"v_mfma_f32_16x16x32_bf16 a[188:191], a[24:27], a[120:123], a[188:191]\n" \
"ds_read_b128 a[16:19], v72 offset:36864\n" \
"v_mfma_f32_16x16x32_bf16 a[188:191], a[28:31], a[124:127], a[188:191]\n" \
"buffer_load_dwordx4 v226, s[48:51], s77 offen offset:2048 lds\n" \
"v_mfma_f32_16x16x32_bf16 a[184:187], a[24:27], a[112:115], a[184:187]\n" \
"ds_read_b64_tr_b16 a[80:81], v98 offset:33280\n" \
"v_mfma_f32_16x16x32_bf16 a[184:187], a[28:31], a[116:119], a[184:187]\n" \
"v_mfma_f32_16x16x32_bf16 a[180:183], a[24:27], a[104:107], a[180:183]\n" \
"ds_read_b128 a[20:23], v73 offset:36864\n" \
"v_mfma_f32_16x16x32_bf16 a[180:183], a[28:31], a[108:111], a[180:183]\n" \
"buffer_load_dwordx4 v227, s[48:51], s77 offen offset:3072 lds\n" \
"v_mfma_f32_16x16x32_bf16 a[176:179], a[24:27], a[96:99], a[176:179]\n" \
"s_mov_b64 s[64:65], s[12:13]\n" \
"s_add_u32 s68, s67, 0x700000\n" \
"ds_read_b64_tr_b16 a[82:83], v98 offset:37376\n" \
"v_mfma_f32_16x16x32_bf16 a[176:179], a[28:31], a[100:103], a[176:179]\n" \
"s_add_u32 s64, s64, s68\n" \
"s_addc_u32 s65, s65, 0\n" \
"v_mfma_f32_16x16x32_bf16 a[192:195], a[32:35], a[96:99], a[192:195]\n" \
"s_xor_b32 s77, s77, 0x180\n" \
"ds_read_b128 a[24:27], v72 offset:38912\n" \
"v_mfma_f32_16x16x32_bf16 a[192:195], a[36:39], a[100:103], a[192:195]\n" \
"global_load_dword v238, v234, s[64:65]\n" \
"v_mfma_f32_16x16x32_bf16 a[196:199], a[32:35], a[104:107], a[196:199]\n" \
"ds_read_b64_tr_b16 a[84:85], v98 offset:49664\n" \
"v_mfma_f32_16x16x32_bf16 a[196:199], a[36:39], a[108:111], a[196:199]\n" \
"v_mfma_f32_16x16x32_bf16 a[200:203], a[32:35], a[112:115], a[200:203]\n" \
"s_lshr_b32 s68, s66, 2\n" \
"s_and_b32 s68, s68, 0xffff\n" \
"s_add_u32 s68, s68, 65585152\n" \
"ds_read_b128 a[28:31], v73 offset:38912\n" \
"v_mfma_f32_16x16x32_bf16 a[200:203], a[36:39], a[116:119], a[200:203]\n" \
"buffer_load_dword v238, v242, s[56:59], s68 offen sc1\n" \
"v_mfma_f32_16x16x32_bf16 a[204:207], a[32:35], a[120:123], a[204:207]\n" \
"ds_read_b64_tr_b16 a[86:87], v98 offset:53760\n" \
"v_mfma_f32_16x16x32_bf16 a[204:207], a[36:39], a[124:127], a[204:207]\n" \
"v_mfma_f32_16x16x32_bf16 a[220:223], a[40:43], a[120:123], a[220:223]\n" \
"ds_read_b128 a[32:35], v72 offset:40960\n" \
"v_mfma_f32_16x16x32_bf16 a[220:223], a[44:47], a[124:127], a[220:223]\n" \
"v_mfma_f32_16x16x32_bf16 a[216:219], a[40:43], a[112:115], a[216:219]\n" \
"ds_read_b64_tr_b16 a[88:89], v99 offset:33280\n" \
"v_mfma_f32_16x16x32_bf16 a[216:219], a[44:47], a[116:119], a[216:219]\n" \
"v_mfma_f32_16x16x32_bf16 a[212:215], a[40:43], a[104:107], a[212:215]\n" \
"ds_read_b128 a[36:39], v73 offset:40960\n" \
"v_mfma_f32_16x16x32_bf16 a[212:215], a[44:47], a[108:111], a[212:215]\n" \
"v_mfma_f32_16x16x32_bf16 a[208:211], a[40:43], a[96:99], a[208:211]\n" \
"ds_read_b64_tr_b16 a[90:91], v99 offset:37376\n" \
"v_mfma_f32_16x16x32_bf16 a[208:211], a[44:47], a[100:103], a[208:211]\n" \
"v_mfma_f32_16x16x32_bf16 a[224:227], a[48:51], a[96:99], a[224:227]\n" \
"ds_read_b128 a[40:43], v72 offset:43008\n" \
"v_mfma_f32_16x16x32_bf16 a[224:227], a[52:55], a[100:103], a[224:227]\n" \
"v_mfma_f32_16x16x32_bf16 a[228:231], a[48:51], a[104:107], a[228:231]\n" \
"ds_read_b64_tr_b16 a[92:93], v99 offset:49664\n" \
"v_mfma_f32_16x16x32_bf16 a[228:231], a[52:55], a[108:111], a[228:231]\n" \
"v_mfma_f32_16x16x32_bf16 a[232:235], a[48:51], a[112:115], a[232:235]\n" \
"ds_read_b128 a[44:47], v73 offset:43008\n" \
"v_mfma_f32_16x16x32_bf16 a[232:235], a[52:55], a[116:119], a[232:235]\n" \
"v_mfma_f32_16x16x32_bf16 a[236:239], a[48:51], a[120:123], a[236:239]\n" \
"ds_read_b64_tr_b16 a[94:95], v99 offset:53760\n" \
"v_mfma_f32_16x16x32_bf16 a[236:239], a[52:55], a[124:127], a[236:239]\n" \
"v_mfma_f32_16x16x32_bf16 a[252:255], a[56:59], a[120:123], a[252:255]\n" \
"ds_read_b128 a[48:51], v72 offset:45056\n" \
"v_mfma_f32_16x16x32_bf16 a[252:255], a[60:63], a[124:127], a[252:255]\n" \
"v_mfma_f32_16x16x32_bf16 a[248:251], a[56:59], a[112:115], a[248:251]\n" \
"v_mfma_f32_16x16x32_bf16 a[248:251], a[60:63], a[116:119], a[248:251]\n" \
"v_mfma_f32_16x16x32_bf16 a[244:247], a[56:59], a[104:107], a[244:247]\n" \
"ds_read_b128 a[52:55], v73 offset:45056\n" \
"v_mfma_f32_16x16x32_bf16 a[244:247], a[60:63], a[108:111], a[244:247]\n" \
"v_mfma_f32_16x16x32_bf16 a[240:243], a[56:59], a[96:99], a[240:243]\n" \
"v_mfma_f32_16x16x32_bf16 a[240:243], a[60:63], a[100:103], a[240:243]\n" \
"s_waitcnt lgkmcnt(1)\n" \
"v_mfma_f32_16x16x32_bf16 v[0:3], a[64:67], a[0:3], v[0:3]\n" \
"ds_read_b128 a[56:59], v72 offset:47104\n" \
"v_mfma_f32_16x16x32_bf16 v[0:3], a[68:71], a[4:7], v[0:3]\n" \
"v_mfma_f32_16x16x32_bf16 v[4:7], a[72:75], a[0:3], v[4:7]\n" \
"s_add_u32 m0, s74, 65536\n" \
"ds_read_b128 a[60:63], v73 offset:47104\n" \
"v_mfma_f32_16x16x32_bf16 v[4:7], a[76:79], a[4:7], v[4:7]\n" \
"buffer_load_dwordx4 v224, s[48:51], s75 offen offset:0 lds\n" \
"v_mfma_f32_16x16x32_bf16 v[8:11], a[80:83], a[0:3], v[8:11]\n" \
"ds_read_b64_tr_b16 a[96:97], v98 offset:33792\n" \
"v_mfma_f32_16x16x32_bf16 v[8:11], a[84:87], a[4:7], v[8:11]\n" \
"v_mfma_f32_16x16x32_bf16 v[12:15], a[88:91], a[0:3], v[12:15]\n" \
"ds_read_b64_tr_b16 a[98:99], v98 offset:37888\n" \
"v_mfma_f32_16x16x32_bf16 v[12:15], a[92:95], a[4:7], v[12:15]\n" \
"buffer_load_dwordx4 v225, s[48:51], s75 offen offset:1024 lds\n" \
"v_mfma_f32_16x16x32_bf16 v[28:31], a[88:91], a[8:11], v[28:31]\n" \
"ds_read_b64_tr_b16 a[100:101], v98 offset:50176\n" \
"v_mfma_f32_16x16x32_bf16 v[28:31], a[92:95], a[12:15], v[28:31]\n" \
"v_mfma_f32_16x16x32_bf16 v[24:27], a[80:83], a[8:11], v[24:27]\n" \
"ds_read_b64_tr_b16 a[102:103], v98 offset:54272\n" \
"v_mfma_f32_16x16x32_bf16 v[24:27], a[84:87], a[12:15], v[24:27]\n" \
"buffer_load_dwordx4 v226, s[48:51], s75 offen offset:2048 lds\n" \
"v_mfma_f32_16x16x32_bf16 v[20:23], a[72:75], a[8:11], v[20:23]\n" \
"ds_read_b64_tr_b16 a[104:105], v99 offset:33792\n" \
"v_mfma_f32_16x16x32_bf16 v[20:23], a[76:79], a[12:15], v[20:23]\n" \
"v_mfma_f32_16x16x32_bf16 v[16:19], a[64:67], a[8:11], v[16:19]\n" \
"ds_read_b64_tr_b16 a[106:107], v99 offset:37888\n" \
"v_mfma_f32_16x16x32_bf16 v[16:19], a[68:71], a[12:15], v[16:19]\n" \
"buffer_load_dwordx4 v227, s[48:51], s75 offen offset:3072 lds\n" \
"v_mfma_f32_16x16x32_bf16 v[32:35], a[64:67], a[16:19], v[32:35]\n" \
"ds_read_b64_tr_b16 a[108:109], v99 offset:50176\n" \
"v_mfma_f32_16x16x32_bf16 v[32:35], a[68:71], a[20:23], v[32:35]\n" \
"s_xor_b32 s75, s75, 0x400\n" \
"v_mfma_f32_16x16x32_bf16 v[36:39], a[72:75], a[16:19], v[36:39]\n" \
"s_add_u32 m0, s74, 69632\n" \
"ds_read_b64_tr_b16 a[110:111], v99 offset:54272\n" \
"v_mfma_f32_16x16x32_bf16 v[36:39], a[76:79], a[20:23], v[36:39]\n" \
"buffer_load_dwordx4 v224, s[48:51], s77 offen offset:0 lds\n" \
"v_mfma_f32_16x16x32_bf16 v[40:43], a[80:83], a[16:19], v[40:43]\n" \
"ds_read_b64_tr_b16 a[112:113], v98 offset:34304\n" \
"v_mfma_f32_16x16x32_bf16 v[40:43], a[84:87], a[20:23], v[40:43]\n" \
"v_mfma_f32_16x16x32_bf16 v[44:47], a[88:91], a[16:19], v[44:47]\n" \
"ds_read_b64_tr_b16 a[114:115], v98 offset:38400\n" \
"v_mfma_f32_16x16x32_bf16 v[44:47], a[92:95], a[20:23], v[44:47]\n" \
"buffer_load_dwordx4 v225, s[48:51], s77 offen offset:1024 lds\n" \
"v_mfma_f32_16x16x32_bf16 v[60:63], a[88:91], a[24:27], v[60:63]\n" \
"ds_read_b64_tr_b16 a[116:117], v98 offset:50688\n" \
"v_mfma_f32_16x16x32_bf16 v[60:63], a[92:95], a[28:31], v[60:63]\n" \
"v_mfma_f32_16x16x32_bf16 v[56:59], a[80:83], a[24:27], v[56:59]\n" \
"ds_read_b64_tr_b16 a[118:119], v98 offset:54784\n" \
"v_mfma_f32_16x16x32_bf16 v[56:59], a[84:87], a[28:31], v[56:59]\n" \
"buffer_load_dwordx4 v226, s[48:51], s77 offen offset:2048 lds\n" \
"v_mfma_f32_16x16x32_bf16 v[52:55], a[72:75], a[24:27], v[52:55]\n" \
"ds_read_b64_tr_b16 a[120:121], v99 offset:34304\n" \
"v_mfma_f32_16x16x32_bf16 v[52:55], a[76:79], a[28:31], v[52:55]\n" \
"v_mfma_f32_16x16x32_bf16 v[48:51], a[64:67], a[24:27], v[48:51]\n" \
"ds_read_b64_tr_b16 a[122:123], v99 offset:38400\n" \
"v_mfma_f32_16x16x32_bf16 v[48:51], a[68:71], a[28:31], v[48:51]\n" \
"buffer_load_dwordx4 v227, s[48:51], s77 offen offset:3072 lds\n" \
"v_mfma_f32_16x16x32_bf16 v[144:147], a[64:67], a[32:35], v[144:147]\n" \
"ds_read_b64_tr_b16 a[124:125], v99 offset:50688\n" \
"v_mfma_f32_16x16x32_bf16 v[144:147], a[68:71], a[36:39], v[144:147]\n" \
"v_mfma_f32_16x16x32_bf16 v[148:151], a[72:75], a[32:35], v[148:151]\n" \
"s_xor_b32 s77, s77, 0x400\n" \
"ds_read_b64_tr_b16 a[126:127], v99 offset:54784\n" \
"v_mfma_f32_16x16x32_bf16 v[148:151], a[76:79], a[36:39], v[148:151]\n" \
"v_mfma_f32_16x16x32_bf16 v[152:155], a[80:83], a[32:35], v[152:155]\n" \
"v_mfma_f32_16x16x32_bf16 v[152:155], a[84:87], a[36:39], v[152:155]\n" \
"v_mfma_f32_16x16x32_bf16 v[156:159], a[88:91], a[32:35], v[156:159]\n" \
"v_mfma_f32_16x16x32_bf16 v[156:159], a[92:95], a[36:39], v[156:159]\n" \
"v_mfma_f32_16x16x32_bf16 v[188:191], a[88:91], a[40:43], v[188:191]\n" \
"v_mfma_f32_16x16x32_bf16 v[188:191], a[92:95], a[44:47], v[188:191]\n" \
"v_mfma_f32_16x16x32_bf16 v[184:187], a[80:83], a[40:43], v[184:187]\n" \
"v_mfma_f32_16x16x32_bf16 v[184:187], a[84:87], a[44:47], v[184:187]\n" \
"v_mfma_f32_16x16x32_bf16 v[180:183], a[72:75], a[40:43], v[180:183]\n" \
"v_mfma_f32_16x16x32_bf16 v[180:183], a[76:79], a[44:47], v[180:183]\n" \
"v_mfma_f32_16x16x32_bf16 v[176:179], a[64:67], a[40:43], v[176:179]\n" \
"v_mfma_f32_16x16x32_bf16 v[176:179], a[68:71], a[44:47], v[176:179]\n" \
"s_waitcnt lgkmcnt(0)\n" \
"v_mfma_f32_16x16x32_bf16 v[192:195], a[64:67], a[48:51], v[192:195]\n" \
"v_mfma_f32_16x16x32_bf16 v[192:195], a[68:71], a[52:55], v[192:195]\n" \
"v_mfma_f32_16x16x32_bf16 v[196:199], a[72:75], a[48:51], v[196:199]\n" \
"v_mfma_f32_16x16x32_bf16 v[196:199], a[76:79], a[52:55], v[196:199]\n" \
"v_mfma_f32_16x16x32_bf16 v[200:203], a[80:83], a[48:51], v[200:203]\n" \
"v_mfma_f32_16x16x32_bf16 v[200:203], a[84:87], a[52:55], v[200:203]\n" \
"v_mfma_f32_16x16x32_bf16 v[204:207], a[88:91], a[48:51], v[204:207]\n" \
"v_mfma_f32_16x16x32_bf16 v[204:207], a[92:95], a[52:55], v[204:207]\n" \
"v_mfma_f32_16x16x32_bf16 v[220:223], a[88:91], a[56:59], v[220:223]\n" \
"v_mfma_f32_16x16x32_bf16 v[220:223], a[92:95], a[60:63], v[220:223]\n" \
"v_mfma_f32_16x16x32_bf16 v[216:219], a[80:83], a[56:59], v[216:219]\n" \
"v_mfma_f32_16x16x32_bf16 v[216:219], a[84:87], a[60:63], v[216:219]\n" \
"v_mfma_f32_16x16x32_bf16 v[212:215], a[72:75], a[56:59], v[212:215]\n" \
"v_mfma_f32_16x16x32_bf16 v[212:215], a[76:79], a[60:63], v[212:215]\n" \
"v_mfma_f32_16x16x32_bf16 v[208:211], a[64:67], a[56:59], v[208:211]\n" \
"v_mfma_f32_16x16x32_bf16 v[208:211], a[68:71], a[60:63], v[208:211]\n" \
"s_waitcnt vmcnt(10) lgkmcnt(0)\n" \
"s_barrier\n" \
"v_mfma_f32_16x16x32_bf16 a[128:131], a[0:3], a[96:99], a[128:131]\n" \
"s_add_u32 m0, s74, 98304\n" \
"v_mfma_f32_16x16x32_bf16 a[128:131], a[4:7], a[100:103], a[128:131]\n" \
"buffer_load_dwordx4 v224, s[48:51], s75 offen offset:0 lds\n" \
"v_mfma_f32_16x16x32_bf16 a[132:135], a[0:3], a[104:107], a[132:135]\n" \
"ds_read_b64_tr_b16 a[64:65], v232 offset:0\n" \
"ds_read_b64_tr_b16 a[66:67], v232 offset:2048\n" \
"v_mfma_f32_16x16x32_bf16 a[132:135], a[4:7], a[108:111], a[132:135]\n" \
"v_mfma_f32_16x16x32_bf16 a[136:139], a[0:3], a[112:115], a[136:139]\n" \
"v_mfma_f32_16x16x32_bf16 a[136:139], a[4:7], a[116:119], a[136:139]\n" \
"buffer_load_dwordx4 v225, s[48:51], s75 offen offset:1024 lds\n" \
"v_mfma_f32_16x16x32_bf16 a[140:143], a[0:3], a[120:123], a[140:143]\n" \
"ds_read_b64_tr_b16 a[68:69], v232 offset:8192\n" \
"ds_read_b64_tr_b16 a[70:71], v232 offset:10240\n" \
"v_mfma_f32_16x16x32_bf16 a[140:143], a[4:7], a[124:127], a[140:143]\n" \
"v_mfma_f32_16x16x32_bf16 a[156:159], a[8:11], a[120:123], a[156:159]\n" \
"v_mul_f32 v192, s76, v192\n" \
"v_mul_f32 v193, s76, v193\n" \
"v_mfma_f32_16x16x32_bf16 a[156:159], a[12:15], a[124:127], a[156:159]\n" \
"buffer_load_dwordx4 v226, s[48:51], s75 offen offset:2048 lds\n" \
"v_mfma_f32_16x16x32_bf16 a[152:155], a[8:11], a[112:115], a[152:155]\n" \
"ds_read_b64_tr_b16 a[72:73], v233 offset:0\n" \
"ds_read_b64_tr_b16 a[74:75], v233 offset:2048\n" \
"v_mfma_f32_16x16x32_bf16 a[152:155], a[12:15], a[116:119], a[152:155]\n" \
"v_mul_f32 v194, s76, v194\n" \
"v_mul_f32 v195, s76, v195\n" \
"v_mfma_f32_16x16x32_bf16 a[148:151], a[8:11], a[104:107], a[148:151]\n" \
"v_cvt_pk_bf16_f32 v192, v192, v193\n" \
"v_cvt_pk_bf16_f32 v193, v194, v195\n" \
"v_mfma_f32_16x16x32_bf16 a[148:151], a[12:15], a[108:111], a[148:151]\n" \
"buffer_load_dwordx4 v227, s[48:51], s75 offen offset:3072 lds\n" \
"v_mfma_f32_16x16x32_bf16 a[144:147], a[8:11], a[96:99], a[144:147]\n" \
"ds_read_b64_tr_b16 a[76:77], v233 offset:8192\n" \
"ds_read_b64_tr_b16 a[78:79], v233 offset:10240\n" \
"v_mfma_f32_16x16x32_bf16 a[144:147], a[12:15], a[100:103], a[144:147]\n" \
"s_xor_b32 s75, s75, 0x500\n" \
"v_mul_f32 v196, s76, v196\n" \
"v_mul_f32 v197, s76, v197\n" \
"v_mfma_f32_16x16x32_bf16 a[160:163], a[16:19], a[96:99], a[160:163]\n" \
"s_add_u32 m0, s74, 102400\n" \
"v_mul_f32 v198, s76, v198\n" \
"v_mul_f32 v199, s76, v199\n" \
"v_mfma_f32_16x16x32_bf16 a[160:163], a[20:23], a[100:103], a[160:163]\n" \
"buffer_load_dwordx4 v224, s[48:51], s77 offen offset:0 lds\n" \
"v_mfma_f32_16x16x32_bf16 a[164:167], a[16:19], a[104:107], a[164:167]\n" \
"ds_read_b64_tr_b16 a[80:81], v232 offset:512\n" \
"ds_read_b64_tr_b16 a[82:83], v232 offset:2560\n" \
"v_mfma_f32_16x16x32_bf16 a[164:167], a[20:23], a[108:111], a[164:167]\n" \
"v_cvt_pk_bf16_f32 v194, v196, v197\n" \
"v_cvt_pk_bf16_f32 v195, v198, v199\n" \
"v_mfma_f32_16x16x32_bf16 a[168:171], a[16:19], a[112:115], a[168:171]\n" \
"v_mul_f32 v208, s76, v208\n" \
"v_mul_f32 v209, s76, v209\n" \
"v_mfma_f32_16x16x32_bf16 a[168:171], a[20:23], a[116:119], a[168:171]\n" \
"buffer_load_dwordx4 v225, s[48:51], s77 offen offset:1024 lds\n" \
"v_mfma_f32_16x16x32_bf16 a[172:175], a[16:19], a[120:123], a[172:175]\n" \
"ds_read_b64_tr_b16 a[84:85], v232 offset:8704\n" \
"ds_read_b64_tr_b16 a[86:87], v232 offset:10752\n" \
"v_mfma_f32_16x16x32_bf16 a[172:175], a[20:23], a[124:127], a[172:175]\n" \
"v_mul_f32 v210, s76, v210\n" \
"v_mul_f32 v211, s76, v211\n" \
"v_mfma_f32_16x16x32_bf16 a[188:191], a[24:27], a[120:123], a[188:191]\n" \
"v_cvt_pk_bf16_f32 v208, v208, v209\n" \
"v_cvt_pk_bf16_f32 v209, v210, v211\n" \
"v_mfma_f32_16x16x32_bf16 a[188:191], a[28:31], a[124:127], a[188:191]\n" \
"buffer_load_dwordx4 v226, s[48:51], s77 offen offset:2048 lds\n" \
"v_mfma_f32_16x16x32_bf16 a[184:187], a[24:27], a[112:115], a[184:187]\n" \
"ds_read_b64_tr_b16 a[88:89], v233 offset:512\n" \
"ds_read_b64_tr_b16 a[90:91], v233 offset:2560\n" \
"v_mfma_f32_16x16x32_bf16 a[184:187], a[28:31], a[116:119], a[184:187]\n" \
"v_mul_f32 v212, s76, v212\n" \
"v_mul_f32 v213, s76, v213\n" \
"v_mfma_f32_16x16x32_bf16 a[180:183], a[24:27], a[104:107], a[180:183]\n" \
"v_mul_f32 v214, s76, v214\n" \
"v_mul_f32 v215, s76, v215\n" \
"v_mfma_f32_16x16x32_bf16 a[180:183], a[28:31], a[108:111], a[180:183]\n" \
"buffer_load_dwordx4 v227, s[48:51], s77 offen offset:3072 lds\n" \
"v_mfma_f32_16x16x32_bf16 a[176:179], a[24:27], a[96:99], a[176:179]\n" \
"ds_read_b64_tr_b16 a[92:93], v233 offset:8704\n" \
"ds_read_b64_tr_b16 a[94:95], v233 offset:10752\n" \
"v_mfma_f32_16x16x32_bf16 a[176:179], a[28:31], a[100:103], a[176:179]\n" \
"s_xor_b32 s77, s77, 0x500\n" \
"v_cvt_pk_bf16_f32 v210, v212, v213\n" \
"v_cvt_pk_bf16_f32 v211, v214, v215\n" \
"v_mfma_f32_16x16x32_bf16 a[192:195], a[32:35], a[96:99], a[192:195]\n" \
"s_add_u32 m0, s79, 32768\n" \
"v_mul_f32 v200, s76, v200\n" \
"v_mul_f32 v201, s76, v201\n" \
"v_mfma_f32_16x16x32_bf16 a[192:195], a[36:39], a[100:103], a[192:195]\n" \
"buffer_load_dwordx4 v228, s[52:55], s78 offen offset:0 lds\n" \
"v_mfma_f32_16x16x32_bf16 a[196:199], a[32:35], a[104:107], a[196:199]\n" \
"ds_read_b128 a[0:3], v250 offset:0\n" \
"ds_read_b128 a[4:7], v251 offset:0\n" \
"v_mfma_f32_16x16x32_bf16 a[196:199], a[36:39], a[108:111], a[196:199]\n" \
"v_mul_f32 v202, s76, v202\n" \
"v_mul_f32 v203, s76, v203\n" \
"v_mfma_f32_16x16x32_bf16 a[200:203], a[32:35], a[112:115], a[200:203]\n" \
"v_cvt_pk_bf16_f32 v200, v200, v201\n" \
"v_cvt_pk_bf16_f32 v201, v202, v203\n" \
"v_mfma_f32_16x16x32_bf16 a[200:203], a[36:39], a[116:119], a[200:203]\n" \
"buffer_load_dwordx4 v229, s[52:55], s78 offen offset:1024 lds\n" \
"v_mfma_f32_16x16x32_bf16 a[204:207], a[32:35], a[120:123], a[204:207]\n" \
"ds_read_b128 a[8:11], v250 offset:2048\n" \
"ds_read_b128 a[12:15], v251 offset:2048\n" \
"v_mfma_f32_16x16x32_bf16 a[204:207], a[36:39], a[124:127], a[204:207]\n" \
"v_mul_f32 v204, s76, v204\n" \
"v_mul_f32 v205, s76, v205\n" \
"v_mfma_f32_16x16x32_bf16 a[220:223], a[40:43], a[120:123], a[220:223]\n" \
"s_xor_b32 s78, s78, 0x500000\n" \
"v_mul_f32 v206, s76, v206\n" \
"v_mul_f32 v207, s76, v207\n" \
"v_mfma_f32_16x16x32_bf16 a[220:223], a[44:47], a[124:127], a[220:223]\n" \
"buffer_load_dwordx4 v228, s[52:55], s89 offen offset:2048 lds\n" \
"v_mfma_f32_16x16x32_bf16 a[216:219], a[40:43], a[112:115], a[216:219]\n" \
"ds_read_b128 a[16:19], v250 offset:4096\n" \
"ds_read_b128 a[20:23], v251 offset:4096\n" \
"v_mfma_f32_16x16x32_bf16 a[216:219], a[44:47], a[116:119], a[216:219]\n" \
"v_cvt_pk_bf16_f32 v202, v204, v205\n" \
"v_cvt_pk_bf16_f32 v203, v206, v207\n" \
"v_mfma_f32_16x16x32_bf16 a[212:215], a[40:43], a[104:107], a[212:215]\n" \
"v_mul_f32 v216, s76, v216\n" \
"v_mul_f32 v217, s76, v217\n" \
"v_mfma_f32_16x16x32_bf16 a[212:215], a[44:47], a[108:111], a[212:215]\n" \
"buffer_load_dwordx4 v229, s[52:55], s89 offen offset:3072 lds\n" \
"v_mfma_f32_16x16x32_bf16 a[208:211], a[40:43], a[96:99], a[208:211]\n" \
"v_mul_f32 v218, s76, v218\n" \
"v_mul_f32 v219, s76, v219\n" \
"v_mfma_f32_16x16x32_bf16 a[208:211], a[44:47], a[100:103], a[208:211]\n" \
"s_xor_b32 s89, s89, 0x500000\n" \
"v_cvt_pk_bf16_f32 v216, v216, v217\n" \
"v_cvt_pk_bf16_f32 v217, v218, v219\n" \
"v_mfma_f32_16x16x32_bf16 a[224:227], a[48:51], a[96:99], a[224:227]\n" \
"s_add_u32 m0, s79, 49152\n" \
"v_mul_f32 v220, s76, v220\n" \
"v_mul_f32 v221, s76, v221\n" \
"v_mfma_f32_16x16x32_bf16 a[224:227], a[52:55], a[100:103], a[224:227]\n" \
"buffer_load_dwordx4 v228, s[52:55], s78 offen offset:0 lds\n" \
"v_mfma_f32_16x16x32_bf16 a[228:231], a[48:51], a[104:107], a[228:231]\n" \
"v_mul_f32 v222, s76, v222\n" \
"v_mul_f32 v223, s76, v223\n" \
"v_mfma_f32_16x16x32_bf16 a[228:231], a[52:55], a[108:111], a[228:231]\n" \
"v_cvt_pk_bf16_f32 v218, v220, v221\n" \
"v_cvt_pk_bf16_f32 v219, v222, v223\n" \
"v_mfma_f32_16x16x32_bf16 a[232:235], a[48:51], a[112:115], a[232:235]\n" \
"v_mul_f32 v144, s76, v144\n" \
"v_mul_f32 v145, s76, v145\n" \
"v_mfma_f32_16x16x32_bf16 a[232:235], a[52:55], a[116:119], a[232:235]\n" \
"buffer_load_dwordx4 v229, s[52:55], s78 offen offset:1024 lds\n" \
"v_mfma_f32_16x16x32_bf16 a[236:239], a[48:51], a[120:123], a[236:239]\n" \
"v_mul_f32 v146, s76, v146\n" \
"v_mul_f32 v147, s76, v147\n" \
"v_mfma_f32_16x16x32_bf16 a[236:239], a[52:55], a[124:127], a[236:239]\n" \
"v_cvt_pk_bf16_f32 v196, v144, v145\n" \
"v_cvt_pk_bf16_f32 v197, v146, v147\n" \
"v_mfma_f32_16x16x32_bf16 a[252:255], a[56:59], a[120:123], a[252:255]\n" \
"s_xor_b32 s78, s78, 0x180000\n" \
"v_mul_f32 v148, s76, v148\n" \
"v_mul_f32 v149, s76, v149\n" \
"v_mfma_f32_16x16x32_bf16 a[252:255], a[60:63], a[124:127], a[252:255]\n" \
"buffer_load_dwordx4 v228, s[52:55], s89 offen offset:2048 lds\n" \
"v_mfma_f32_16x16x32_bf16 a[248:251], a[56:59], a[112:115], a[248:251]\n" \
"v_mul_f32 v150, s76, v150\n" \
"v_mul_f32 v151, s76, v151\n" \
"v_mfma_f32_16x16x32_bf16 a[248:251], a[60:63], a[116:119], a[248:251]\n" \
"v_cvt_pk_bf16_f32 v198, v148, v149\n" \
"v_cvt_pk_bf16_f32 v199, v150, v151\n" \
"v_mfma_f32_16x16x32_bf16 a[244:247], a[56:59], a[104:107], a[244:247]\n" \
"v_mul_f32 v176, s76, v176\n" \
"v_mul_f32 v177, s76, v177\n" \
"v_mfma_f32_16x16x32_bf16 a[244:247], a[60:63], a[108:111], a[244:247]\n" \
"buffer_load_dwordx4 v229, s[52:55], s89 offen offset:3072 lds\n" \
"v_mfma_f32_16x16x32_bf16 a[240:243], a[56:59], a[96:99], a[240:243]\n" \
"s_waitcnt lgkmcnt(0)\n" \
"v_mul_f32 v178, s76, v178\n" \
"v_mul_f32 v179, s76, v179\n" \
"v_mfma_f32_16x16x32_bf16 a[240:243], a[60:63], a[100:103], a[240:243]\n" \
"s_xor_b32 s89, s89, 0x180000\n" \
"v_cvt_pk_bf16_f32 v212, v176, v177\n" \
"v_cvt_pk_bf16_f32 v213, v178, v179\n" \
"v_mfma_f32_16x16x32_bf16 v[64:67], a[64:67], a[0:3], 0\n" \
"ds_read_b128 a[24:27], v250 offset:6144\n" \
"v_mul_f32 v180, s76, v180\n" \
"v_mfma_f32_16x16x32_bf16 v[64:67], a[68:71], a[4:7], v[64:67]\n" \
"s_add_u32 m0, s74, 131072\n" \
"ds_read_b128 a[28:31], v251 offset:6144\n" \
"v_mul_f32 v181, s76, v181\n" \
"v_mfma_f32_16x16x32_bf16 v[68:71], a[72:75], a[0:3], 0\n" \
"ds_read_b64_tr_b16 a[96:97], v232 offset:1024\n" \
"v_mul_f32 v182, s76, v182\n" \
"v_mfma_f32_16x16x32_bf16 v[68:71], a[76:79], a[4:7], v[68:71]\n" \
"buffer_load_dwordx4 v224, s[48:51], s75 offen offset:0 lds\n" \
"v_mfma_f32_16x16x32_bf16 v[72:75], a[80:83], a[0:3], 0\n" \
"ds_read_b64_tr_b16 a[98:99], v232 offset:3072\n" \
"v_mul_f32 v183, s76, v183\n" \
"v_mfma_f32_16x16x32_bf16 v[72:75], a[84:87], a[4:7], v[72:75]\n" \
"ds_read_b64_tr_b16 a[100:101], v232 offset:9216\n" \
"v_cvt_pk_bf16_f32 v214, v180, v181\n" \
"v_mfma_f32_16x16x32_bf16 v[76:79], a[88:91], a[0:3], 0\n" \
"ds_read_b64_tr_b16 a[102:103], v232 offset:11264\n" \
"v_cvt_pk_bf16_f32 v215, v182, v183\n" \
"v_mfma_f32_16x16x32_bf16 v[76:79], a[92:95], a[4:7], v[76:79]\n" \
"buffer_load_dwordx4 v225, s[48:51], s75 offen offset:1024 lds\n" \
"v_mfma_f32_16x16x32_bf16 v[108:111], a[88:91], a[8:11], 0\n" \
"ds_read_b64_tr_b16 a[104:105], v233 offset:1024\n" \
"v_mul_f32 v152, s76, v152\n" \
"v_mfma_f32_16x16x32_bf16 v[108:111], a[92:95], a[12:15], v[108:111]\n" \
"ds_read_b64_tr_b16 a[106:107], v233 offset:3072\n" \
"v_mul_f32 v153, s76, v153\n" \
"v_mfma_f32_16x16x32_bf16 v[104:107], a[80:83], a[8:11], 0\n" \
"ds_read_b64_tr_b16 a[108:109], v233 offset:9216\n" \
"v_mul_f32 v154, s76, v154\n" \
"v_mfma_f32_16x16x32_bf16 v[104:107], a[84:87], a[12:15], v[104:107]\n" \
"buffer_load_dwordx4 v226, s[48:51], s75 offen offset:2048 lds\n" \
"v_mfma_f32_16x16x32_bf16 v[100:103], a[72:75], a[8:11], 0\n" \
"ds_read_b64_tr_b16 a[110:111], v233 offset:11264\n" \
"v_mul_f32 v155, s76, v155\n" \
"v_mfma_f32_16x16x32_bf16 v[100:103], a[76:79], a[12:15], v[100:103]\n" \
"ds_read_b64_tr_b16 a[112:113], v232 offset:1536\n" \
"v_cvt_pk_bf16_f32 v204, v152, v153\n" \
"v_mfma_f32_16x16x32_bf16 v[96:99], a[64:67], a[8:11], 0\n" \
"ds_read_b64_tr_b16 a[114:115], v232 offset:3584\n" \
"v_cvt_pk_bf16_f32 v205, v154, v155\n" \
"v_mfma_f32_16x16x32_bf16 v[96:99], a[68:71], a[12:15], v[96:99]\n" \
"buffer_load_dwordx4 v227, s[48:51], s75 offen offset:3072 lds\n" \
"v_mfma_f32_16x16x32_bf16 v[128:131], a[64:67], a[16:19], 0\n" \
"s_xor_b32 s75, s75, 0x180\n" \
"ds_read_b64_tr_b16 a[116:117], v232 offset:9728\n" \
"v_mul_f32 v156, s76, v156\n" \
"v_mfma_f32_16x16x32_bf16 v[128:131], a[68:71], a[20:23], v[128:131]\n" \
"s_add_u32 m0, s74, 135168\n" \
"ds_read_b64_tr_b16 a[118:119], v232 offset:11776\n" \
"v_mul_f32 v157, s76, v157\n" \
"v_mfma_f32_16x16x32_bf16 v[132:135], a[72:75], a[16:19], 0\n" \
"ds_read_b64_tr_b16 a[120:121], v233 offset:1536\n" \
"v_mul_f32 v158, s76, v158\n" \
"Lpg2209_%=:\n" \
"Lst0x3_%=:\n" \
"s_cbranch_execz Lpg4446_%=\n" \
"v_mfma_f32_16x16x32_bf16 v[132:135], a[76:79], a[20:23], v[132:135]\n" \
"buffer_load_dwordx4 v224, s[48:51], s77 offen offset:0 lds\n" \
"v_mfma_f32_16x16x32_bf16 v[136:139], a[80:83], a[16:19], 0\n" \
"ds_read_b64_tr_b16 a[122:123], v233 offset:3584\n" \
"v_mul_f32 v159, s76, v159\n" \
"v_mfma_f32_16x16x32_bf16 v[136:139], a[84:87], a[20:23], v[136:139]\n" \
"ds_read_b64_tr_b16 a[124:125], v233 offset:9728\n" \
"v_cvt_pk_bf16_f32 v206, v156, v157\n" \
"v_mfma_f32_16x16x32_bf16 v[140:143], a[88:91], a[16:19], 0\n" \
"s_waitcnt lgkmcnt(3)\n" \
"ds_read_b64_tr_b16 a[126:127], v233 offset:11776\n" \
"v_cvt_pk_bf16_f32 v207, v158, v159\n" \
"v_mfma_f32_16x16x32_bf16 v[140:143], a[92:95], a[20:23], v[140:143]\n" \
"buffer_load_dwordx4 v225, s[48:51], s77 offen offset:1024 lds\n" \
"v_mfma_f32_16x16x32_bf16 v[172:175], a[88:91], a[24:27], 0\n" \
"s_add_u32 s93, s87, 8192\n" \
"v_mul_f32 v184, s76, v184\n" \
"v_mul_f32 v185, s76, v185\n" \
"v_mfma_f32_16x16x32_bf16 v[172:175], a[92:95], a[28:31], v[172:175]\n" \
"buffer_store_dwordx4 v[192:195], v236, s[56:59], s93 offen offset:0 sc1\n" \
"v_mfma_f32_16x16x32_bf16 v[168:171], a[80:83], a[24:27], 0\n" \
"v_mul_f32 v186, s76, v186\n" \
"v_mul_f32 v187, s76, v187\n" \
"v_mfma_f32_16x16x32_bf16 v[168:171], a[84:87], a[28:31], v[168:171]\n" \
"buffer_load_dwordx4 v226, s[48:51], s77 offen offset:2048 lds\n" \
"v_mfma_f32_16x16x32_bf16 v[164:167], a[72:75], a[24:27], 0\n" \
"v_cvt_pk_bf16_f32 v220, v184, v185\n" \
"v_cvt_pk_bf16_f32 v221, v186, v187\n" \
"v_mfma_f32_16x16x32_bf16 v[164:167], a[76:79], a[28:31], v[164:167]\n" \
"buffer_load_dwordx4 v227, s[48:51], s77 offen offset:3072 lds\n" \
"v_mfma_f32_16x16x32_bf16 v[160:163], a[64:67], a[24:27], 0\n" \
"v_mul_f32 v188, s76, v188\n" \
"v_mul_f32 v189, s76, v189\n" \
"v_mfma_f32_16x16x32_bf16 v[160:163], a[68:71], a[28:31], v[160:163]\n" \
"s_waitcnt vmcnt(43) lgkmcnt(0)\n" \
"s_barrier\n" \
"v_mfma_f32_16x16x32_bf16 v[80:83], a[96:99], a[0:3], 0\n" \
"s_xor_b32 s77, s77, 0x180\n" \
"ds_read_b64_tr_b16 a[64:65], v232 offset:16384\n" \
"v_mul_f32 v190, s76, v190\n" \
"v_mfma_f32_16x16x32_bf16 v[80:83], a[100:103], a[4:7], v[80:83]\n" \
"ds_read_b64_tr_b16 a[66:67], v232 offset:18432\n" \
"v_mul_f32 v191, s76, v191\n" \
"v_mfma_f32_16x16x32_bf16 v[84:87], a[104:107], a[0:3], 0\n" \
"s_add_u32 m0, s79, 0\n" \
"ds_read_b64_tr_b16 a[68:69], v232 offset:24576\n" \
"v_cvt_pk_bf16_f32 v222, v188, v189\n" \
"v_mfma_f32_16x16x32_bf16 v[84:87], a[108:111], a[4:7], v[84:87]\n" \
"buffer_load_dwordx4 v228, s[52:55], s78 offen offset:0 lds\n" \
"v_mfma_f32_16x16x32_bf16 v[88:91], a[112:115], a[0:3], 0\n" \
"ds_read_b64_tr_b16 a[70:71], v232 offset:26624\n" \
"v_cvt_pk_bf16_f32 v223, v190, v191\n" \
"v_mfma_f32_16x16x32_bf16 v[88:91], a[116:119], a[4:7], v[88:91]\n" \
"ds_read_b64_tr_b16 a[72:73], v233 offset:16384\n" \
"v_accvgpr_read_b32 v243, a224\n" \
"v_mfma_f32_16x16x32_bf16 v[92:95], a[120:123], a[0:3], 0\n" \
"ds_read_b64_tr_b16 a[74:75], v233 offset:18432\n" \
"v_accvgpr_read_b32 v252, a225\n" \
"v_mfma_f32_16x16x32_bf16 v[92:95], a[124:127], a[4:7], v[92:95]\n" \
"buffer_load_dwordx4 v229, s[52:55], s78 offen offset:1024 lds\n" \
"v_mfma_f32_16x16x32_bf16 v[124:127], a[120:123], a[8:11], 0\n" \
"s_waitcnt vmcnt(33)\n" \
"ds_read_b128 a[0:3], v230 offset:0\n" \
"v_accvgpr_read_b32 v253, a226\n" \
"v_mfma_f32_16x16x32_bf16 v[124:127], a[124:127], a[12:15], v[124:127]\n" \
"ds_read_b128 a[4:7], v231 offset:0\n" \
"v_accvgpr_read_b32 v254, a227\n" \
"v_mfma_f32_16x16x32_bf16 v[120:123], a[112:115], a[8:11], 0\n" \
"s_xor_b32 s78, s78, 0x300000\n" \
"ds_read_b64_tr_b16 a[76:77], v233 offset:24576\n" \
"v_mul_f32 v243, s31, v243\n" \
"v_mfma_f32_16x16x32_bf16 v[120:123], a[116:119], a[12:15], v[120:123]\n" \
"buffer_load_dwordx4 v228, s[52:55], s89 offen offset:2048 lds\n" \
"v_mfma_f32_16x16x32_bf16 v[116:119], a[104:107], a[8:11], 0\n" \
"ds_read_b64_tr_b16 a[78:79], v233 offset:26624\n" \
"v_mul_f32 v252, s31, v252\n" \
"v_mfma_f32_16x16x32_bf16 v[116:119], a[108:111], a[12:15], v[116:119]\n" \
"ds_read_b64_tr_b16 a[80:81], v232 offset:16896\n" \
"v_mul_f32 v253, s31, v253\n" \
"v_mfma_f32_16x16x32_bf16 v[112:115], a[96:99], a[8:11], 0\n" \
"ds_read_b64_tr_b16 a[82:83], v232 offset:18944\n" \
"v_mul_f32 v254, s31, v254\n" \
"v_mfma_f32_16x16x32_bf16 v[112:115], a[100:103], a[12:15], v[112:115]\n" \
"buffer_load_dwordx4 v229, s[52:55], s89 offen offset:3072 lds\n" \
"v_mfma_f32_16x16x32_bf16 v[144:147], a[96:99], a[16:19], 0\n" \
"s_waitcnt vmcnt(33)\n" \
"s_xor_b32 s89, s89, 0x300000\n" \
"ds_read_b128 a[8:11], v230 offset:2048\n" \
"v_mfma_f32_16x16x32_bf16 v[144:147], a[100:103], a[20:23], v[144:147]\n" \
"s_add_u32 m0, s74, 65536\n" \
"ds_read_b128 a[12:15], v231 offset:2048\n" \
"v_cvt_pk_bf16_f32 v243, v243, v252\n" \
"v_mfma_f32_16x16x32_bf16 v[148:151], a[104:107], a[16:19], 0\n" \
"s_waitcnt lgkmcnt(6)\n" \
"ds_read_b64_tr_b16 a[84:85], v232 offset:25088\n" \
"v_cvt_pk_bf16_f32 v253, v253, v254\n" \
"v_mfma_f32_16x16x32_bf16 v[148:151], a[108:111], a[20:23], v[148:151]\n" \
"buffer_load_dwordx4 v224, s[48:51], s75 offen offset:0 lds\n" \
"v_mfma_f32_16x16x32_bf16 v[152:155], a[112:115], a[16:19], 0\n" \
"ds_read_b64_tr_b16 a[86:87], v232 offset:27136\n" \
"v_accvgpr_write_b32 a224, v243\n" \
"v_mfma_f32_16x16x32_bf16 v[152:155], a[116:119], a[20:23], v[152:155]\n" \
"ds_read_b64_tr_b16 a[88:89], v233 offset:16896\n" \
"v_accvgpr_write_b32 a225, v253\n" \
"v_mfma_f32_16x16x32_bf16 v[156:159], a[120:123], a[16:19], 0\n" \
"ds_read_b64_tr_b16 a[90:91], v233 offset:18944\n" \
"v_accvgpr_read_b32 v243, a240\n" \
"v_mfma_f32_16x16x32_bf16 v[156:159], a[124:127], a[20:23], v[156:159]\n" \
"buffer_load_dwordx4 v225, s[48:51], s75 offen offset:1024 lds\n" \
"v_mfma_f32_16x16x32_bf16 v[188:191], a[120:123], a[24:27], 0\n" \
"s_waitcnt vmcnt(33)\n" \
"ds_read_b128 a[16:19], v230 offset:4096\n" \
"v_accvgpr_read_b32 v252, a241\n" \
"v_mfma_f32_16x16x32_bf16 v[188:191], a[124:127], a[28:31], v[188:191]\n" \
"ds_read_b128 a[20:23], v231 offset:4096\n" \
"v_accvgpr_read_b32 v253, a242\n" \
"v_mfma_f32_16x16x32_bf16 v[184:187], a[112:115], a[24:27], 0\n" \
"s_waitcnt lgkmcnt(6)\n" \
"ds_read_b64_tr_b16 a[92:93], v233 offset:25088\n" \
"v_accvgpr_read_b32 v254, a243\n" \
"v_mfma_f32_16x16x32_bf16 v[184:187], a[116:119], a[28:31], v[184:187]\n" \
"buffer_load_dwordx4 v226, s[48:51], s75 offen offset:2048 lds\n" \
"v_mfma_f32_16x16x32_bf16 v[180:183], a[104:107], a[24:27], 0\n" \
"ds_read_b64_tr_b16 a[94:95], v233 offset:27136\n" \
"v_mul_f32 v243, s31, v243\n" \
"v_mfma_f32_16x16x32_bf16 v[180:183], a[108:111], a[28:31], v[180:183]\n" \
"v_mul_f32 v252, s31, v252\n" \
"v_mul_f32 v253, s31, v253\n" \
"v_mfma_f32_16x16x32_bf16 v[176:179], a[96:99], a[24:27], 0\n" \
"v_mul_f32 v254, s31, v254\n" \
"v_cvt_pk_bf16_f32 v243, v243, v252\n" \
"v_mfma_f32_16x16x32_bf16 v[176:179], a[100:103], a[28:31], v[176:179]\n" \
"buffer_load_dwordx4 v227, s[48:51], s75 offen offset:3072 lds\n" \
"v_mfma_f32_16x16x32_bf16 v[64:67], a[64:67], a[0:3], v[64:67]\n" \
"s_waitcnt vmcnt(33)\n" \
"s_xor_b32 s75, s75, 0x300\n" \
"ds_read_b128 a[24:27], v230 offset:6144\n" \
"v_mfma_f32_16x16x32_bf16 v[64:67], a[68:71], a[4:7], v[64:67]\n" \
"s_add_u32 m0, s74, 69632\n" \
"ds_read_b128 a[28:31], v231 offset:6144\n" \
"v_cvt_pk_bf16_f32 v253, v253, v254\n" \
"v_mfma_f32_16x16x32_bf16 v[68:71], a[72:75], a[0:3], v[68:71]\n" \
"s_waitcnt lgkmcnt(4)\n" \
"ds_read_b64_tr_b16 a[96:97], v232 offset:17408\n" \
"v_accvgpr_write_b32 a226, v243\n" \
"v_mfma_f32_16x16x32_bf16 v[68:71], a[76:79], a[4:7], v[68:71]\n" \
"buffer_load_dwordx4 v224, s[48:51], s77 offen offset:0 lds\n" \
"v_mfma_f32_16x16x32_bf16 v[72:75], a[80:83], a[0:3], v[72:75]\n" \
"ds_read_b64_tr_b16 a[98:99], v232 offset:19456\n" \
"v_accvgpr_write_b32 a227, v253\n" \
"v_mfma_f32_16x16x32_bf16 v[72:75], a[84:87], a[4:7], v[72:75]\n" \
"s_waitcnt lgkmcnt(3)\n" \
"ds_read_b64_tr_b16 a[100:101], v232 offset:25600\n" \
"v_accvgpr_read_b32 v243, a228\n" \
"v_mfma_f32_16x16x32_bf16 v[76:79], a[88:91], a[0:3], v[76:79]\n" \
"ds_read_b64_tr_b16 a[102:103], v232 offset:27648\n" \
"v_accvgpr_read_b32 v252, a229\n" \
"v_mfma_f32_16x16x32_bf16 v[76:79], a[92:95], a[4:7], v[76:79]\n" \
"buffer_load_dwordx4 v225, s[48:51], s77 offen offset:1024 lds\n" \
"v_mfma_f32_16x16x32_bf16 v[108:111], a[88:91], a[8:11], v[108:111]\n" \
"ds_read_b64_tr_b16 a[104:105], v233 offset:17408\n" \
"v_accvgpr_read_b32 v253, a230\n" \
"v_mfma_f32_16x16x32_bf16 v[108:111], a[92:95], a[12:15], v[108:111]\n" \
"ds_read_b64_tr_b16 a[106:107], v233 offset:19456\n" \
"v_accvgpr_read_b32 v254, a231\n" \
"v_mfma_f32_16x16x32_bf16 v[104:107], a[80:83], a[8:11], v[104:107]\n" \
"s_waitcnt lgkmcnt(6)\n" \
"ds_read_b64_tr_b16 a[108:109], v233 offset:25600\n" \
"v_mul_f32 v243, s31, v243\n" \
"v_mfma_f32_16x16x32_bf16 v[104:107], a[84:87], a[12:15], v[104:107]\n" \
"buffer_load_dwordx4 v226, s[48:51], s77 offen offset:2048 lds\n" \
"v_mfma_f32_16x16x32_bf16 v[100:103], a[72:75], a[8:11], v[100:103]\n" \
"ds_read_b64_tr_b16 a[110:111], v233 offset:27648\n" \
"v_mul_f32 v252, s31, v252\n" \
"v_mfma_f32_16x16x32_bf16 v[100:103], a[76:79], a[12:15], v[100:103]\n" \
"ds_read_b64_tr_b16 a[112:113], v232 offset:17920\n" \
"v_mul_f32 v253, s31, v253\n" \
"v_mfma_f32_16x16x32_bf16 v[96:99], a[64:67], a[8:11], v[96:99]\n" \
"ds_read_b64_tr_b16 a[114:115], v232 offset:19968\n" \
"v_mul_f32 v254, s31, v254\n" \
"v_mfma_f32_16x16x32_bf16 v[96:99], a[68:71], a[12:15], v[96:99]\n" \
"buffer_load_dwordx4 v227, s[48:51], s77 offen offset:3072 lds\n" \
"v_mfma_f32_16x16x32_bf16 v[128:131], a[64:67], a[16:19], v[128:131]\n" \
"ds_read_b64_tr_b16 a[116:117], v232 offset:26112\n" \
"v_cvt_pk_bf16_f32 v243, v243, v252\n" \
"v_mfma_f32_16x16x32_bf16 v[128:131], a[68:71], a[20:23], v[128:131]\n" \
"ds_read_b64_tr_b16 a[118:119], v232 offset:28160\n" \
"v_cvt_pk_bf16_f32 v253, v253, v254\n" \
"v_mfma_f32_16x16x32_bf16 v[132:135], a[72:75], a[16:19], v[132:135]\n" \
"s_xor_b32 s77, s77, 0x300\n" \
"ds_read_b64_tr_b16 a[120:121], v233 offset:17920\n" \
"v_accvgpr_write_b32 a228, v243\n" \
"v_mfma_f32_16x16x32_bf16 v[132:135], a[76:79], a[20:23], v[132:135]\n" \
"v_accvgpr_write_b32 a229, v253\n" \
"v_accvgpr_read_b32 v243, a244\n" \
"v_mfma_f32_16x16x32_bf16 v[136:139], a[80:83], a[16:19], v[136:139]\n" \
"ds_read_b64_tr_b16 a[122:123], v233 offset:19968\n" \
"v_accvgpr_read_b32 v252, a245\n" \
"v_mfma_f32_16x16x32_bf16 v[136:139], a[84:87], a[20:23], v[136:139]\n" \
"ds_read_b64_tr_b16 a[124:125], v233 offset:26112\n" \
"v_accvgpr_read_b32 v253, a246\n" \
"v_mfma_f32_16x16x32_bf16 v[140:143], a[88:91], a[16:19], v[140:143]\n" \
"ds_read_b64_tr_b16 a[126:127], v233 offset:28160\n" \
"v_accvgpr_read_b32 v254, a247\n" \
"v_mfma_f32_16x16x32_bf16 v[140:143], a[92:95], a[20:23], v[140:143]\n" \
"buffer_store_dwordx4 v[208:211], v236, s[56:59], s93 offen offset:256 sc1\n" \
"v_mfma_f32_16x16x32_bf16 v[172:175], a[88:91], a[24:27], v[172:175]\n" \
"v_mul_f32 v243, s31, v243\n" \
"v_mul_f32 v252, s31, v252\n" \
"v_mfma_f32_16x16x32_bf16 v[172:175], a[92:95], a[28:31], v[172:175]\n" \
"v_mul_f32 v253, s31, v253\n" \
"v_mul_f32 v254, s31, v254\n" \
"v_mfma_f32_16x16x32_bf16 v[168:171], a[80:83], a[24:27], v[168:171]\n" \
"v_cvt_pk_bf16_f32 v243, v243, v252\n" \
"v_cvt_pk_bf16_f32 v253, v253, v254\n" \
"v_mfma_f32_16x16x32_bf16 v[168:171], a[84:87], a[28:31], v[168:171]\n" \
"v_accvgpr_write_b32 a230, v243\n" \
"v_accvgpr_write_b32 a231, v253\n" \
"v_mfma_f32_16x16x32_bf16 v[164:167], a[72:75], a[24:27], v[164:167]\n" \
"v_accvgpr_read_b32 v243, a232\n" \
"v_accvgpr_read_b32 v252, a233\n" \
"v_mfma_f32_16x16x32_bf16 v[164:167], a[76:79], a[28:31], v[164:167]\n" \
"v_accvgpr_read_b32 v253, a234\n" \
"v_accvgpr_read_b32 v254, a235\n" \
"v_mfma_f32_16x16x32_bf16 v[160:163], a[64:67], a[24:27], v[160:163]\n" \
"v_mul_f32 v243, s31, v243\n" \
"v_mul_f32 v252, s31, v252\n" \
"v_mfma_f32_16x16x32_bf16 v[160:163], a[68:71], a[28:31], v[160:163]\n" \
"s_waitcnt vmcnt(26) lgkmcnt(0)\n" \
"s_barrier\n" \
"v_mfma_f32_16x16x32_bf16 v[80:83], a[96:99], a[0:3], v[80:83]\n" \
"ds_read_b64_tr_b16 a[64:65], v232 offset:32768\n" \
"v_mul_f32 v253, s31, v253\n" \
"v_mfma_f32_16x16x32_bf16 v[80:83], a[100:103], a[4:7], v[80:83]\n" \
"ds_read_b64_tr_b16 a[66:67], v232 offset:34816\n" \
"v_mul_f32 v254, s31, v254\n" \
"v_mfma_f32_16x16x32_bf16 v[84:87], a[104:107], a[0:3], v[84:87]\n" \
"s_add_u32 m0, s79, 16384\n" \
"ds_read_b64_tr_b16 a[68:69], v232 offset:40960\n" \
"v_cvt_pk_bf16_f32 v243, v243, v252\n" \
"v_mfma_f32_16x16x32_bf16 v[84:87], a[108:111], a[4:7], v[84:87]\n" \
"buffer_load_dwordx4 v228, s[52:55], s78 offen offset:0 lds\n" \
"v_mfma_f32_16x16x32_bf16 v[88:91], a[112:115], a[0:3], v[88:91]\n" \
"ds_read_b64_tr_b16 a[70:71], v232 offset:43008\n" \
"v_cvt_pk_bf16_f32 v253, v253, v254\n" \
"v_mfma_f32_16x16x32_bf16 v[88:91], a[116:119], a[4:7], v[88:91]\n" \
"ds_read_b64_tr_b16 a[72:73], v233 offset:32768\n" \
"v_accvgpr_write_b32 a232, v243\n" \
"v_mfma_f32_16x16x32_bf16 v[92:95], a[120:123], a[0:3], v[92:95]\n" \
"ds_read_b64_tr_b16 a[74:75], v233 offset:34816\n" \
"v_accvgpr_write_b32 a233, v253\n" \
"v_mfma_f32_16x16x32_bf16 v[92:95], a[124:127], a[4:7], v[92:95]\n" \
"buffer_load_dwordx4 v229, s[52:55], s78 offen offset:1024 lds\n" \
"v_mfma_f32_16x16x32_bf16 v[124:127], a[120:123], a[8:11], v[124:127]\n" \
"ds_read_b128 a[0:3], v230 offset:32768\n" \
"v_accvgpr_read_b32 v243, a248\n" \
"v_mfma_f32_16x16x32_bf16 v[124:127], a[124:127], a[12:15], v[124:127]\n" \
"ds_read_b128 a[4:7], v231 offset:32768\n" \
"v_accvgpr_read_b32 v252, a249\n" \
"v_mfma_f32_16x16x32_bf16 v[120:123], a[112:115], a[8:11], v[120:123]\n" \
"s_xor_b32 s78, s78, 0x80000\n" \
"ds_read_b64_tr_b16 a[76:77], v233 offset:40960\n" \
"v_accvgpr_read_b32 v253, a250\n" \
"v_mfma_f32_16x16x32_bf16 v[120:123], a[116:119], a[12:15], v[120:123]\n" \
"buffer_load_dwordx4 v228, s[52:55], s89 offen offset:2048 lds\n" \
"v_mfma_f32_16x16x32_bf16 v[116:119], a[104:107], a[8:11], v[116:119]\n" \
"ds_read_b64_tr_b16 a[78:79], v233 offset:43008\n" \
"v_accvgpr_read_b32 v254, a251\n" \
"v_mfma_f32_16x16x32_bf16 v[116:119], a[108:111], a[12:15], v[116:119]\n" \
"ds_read_b64_tr_b16 a[80:81], v232 offset:33280\n" \
"v_mul_f32 v243, s31, v243\n" \
"v_mfma_f32_16x16x32_bf16 v[112:115], a[96:99], a[8:11], v[112:115]\n" \
"ds_read_b64_tr_b16 a[82:83], v232 offset:35328\n" \
"v_mul_f32 v252, s31, v252\n" \
"v_mfma_f32_16x16x32_bf16 v[112:115], a[100:103], a[12:15], v[112:115]\n" \
"buffer_load_dwordx4 v229, s[52:55], s89 offen offset:3072 lds\n" \
"v_mfma_f32_16x16x32_bf16 v[144:147], a[96:99], a[16:19], v[144:147]\n" \
"s_xor_b32 s89, s89, 0x80000\n" \
"ds_read_b128 a[8:11], v230 offset:34816\n" \
"v_mfma_f32_16x16x32_bf16 v[144:147], a[100:103], a[20:23], v[144:147]\n" \
"s_add_u32 m0, s74, 98304\n" \
"ds_read_b128 a[12:15], v231 offset:34816\n" \
"v_mul_f32 v253, s31, v253\n" \
"v_mfma_f32_16x16x32_bf16 v[148:151], a[104:107], a[16:19], v[148:151]\n" \
"s_waitcnt lgkmcnt(6)\n" \
"ds_read_b64_tr_b16 a[84:85], v232 offset:41472\n" \
"v_mul_f32 v254, s31, v254\n" \
"v_mfma_f32_16x16x32_bf16 v[148:151], a[108:111], a[20:23], v[148:151]\n" \
"buffer_load_dwordx4 v224, s[48:51], s75 offen offset:0 lds\n" \
"v_mfma_f32_16x16x32_bf16 v[152:155], a[112:115], a[16:19], v[152:155]\n" \
"ds_read_b64_tr_b16 a[86:87], v232 offset:43520\n" \
"v_cvt_pk_bf16_f32 v243, v243, v252\n" \
"v_mfma_f32_16x16x32_bf16 v[152:155], a[116:119], a[20:23], v[152:155]\n" \
"ds_read_b64_tr_b16 a[88:89], v233 offset:33280\n" \
"v_cvt_pk_bf16_f32 v253, v253, v254\n" \
"v_mfma_f32_16x16x32_bf16 v[156:159], a[120:123], a[16:19], v[156:159]\n" \
"ds_read_b64_tr_b16 a[90:91], v233 offset:35328\n" \
"v_accvgpr_write_b32 a234, v243\n" \
"v_mfma_f32_16x16x32_bf16 v[156:159], a[124:127], a[20:23], v[156:159]\n" \
"buffer_load_dwordx4 v225, s[48:51], s75 offen offset:1024 lds\n" \
"v_mfma_f32_16x16x32_bf16 v[188:191], a[120:123], a[24:27], v[188:191]\n" \
"ds_read_b128 a[16:19], v230 offset:36864\n" \
"v_accvgpr_write_b32 a235, v253\n" \
"v_mfma_f32_16x16x32_bf16 v[188:191], a[124:127], a[28:31], v[188:191]\n" \
"ds_read_b128 a[20:23], v231 offset:36864\n" \
"v_accvgpr_read_b32 v243, a236\n" \
"v_mfma_f32_16x16x32_bf16 v[184:187], a[112:115], a[24:27], v[184:187]\n" \
"s_waitcnt lgkmcnt(6)\n" \
"ds_read_b64_tr_b16 a[92:93], v233 offset:41472\n" \
"v_accvgpr_read_b32 v252, a237\n" \
"v_mfma_f32_16x16x32_bf16 v[184:187], a[116:119], a[28:31], v[184:187]\n" \
"buffer_load_dwordx4 v226, s[48:51], s75 offen offset:2048 lds\n" \
"v_mfma_f32_16x16x32_bf16 v[180:183], a[104:107], a[24:27], v[180:183]\n" \
"ds_read_b64_tr_b16 a[94:95], v233 offset:43520\n" \
"v_accvgpr_read_b32 v253, a238\n" \
"v_mfma_f32_16x16x32_bf16 v[180:183], a[108:111], a[28:31], v[180:183]\n" \
"v_accvgpr_read_b32 v254, a239\n" \
"v_mul_f32 v243, s31, v243\n" \
"v_mfma_f32_16x16x32_bf16 v[176:179], a[96:99], a[24:27], v[176:179]\n" \
"v_mul_f32 v252, s31, v252\n" \
"v_mul_f32 v253, s31, v253\n" \
"v_mfma_f32_16x16x32_bf16 v[176:179], a[100:103], a[28:31], v[176:179]\n" \
"buffer_load_dwordx4 v227, s[48:51], s75 offen offset:3072 lds\n" \
"v_mfma_f32_16x16x32_bf16 v[64:67], a[64:67], a[0:3], v[64:67]\n" \
"s_xor_b32 s75, s75, 0x80\n" \
"ds_read_b128 a[24:27], v230 offset:38912\n" \
"v_mfma_f32_16x16x32_bf16 v[64:67], a[68:71], a[4:7], v[64:67]\n" \
"s_add_u32 m0, s74, 102400\n" \
"ds_read_b128 a[28:31], v231 offset:38912\n" \
"v_mul_f32 v254, s31, v254\n" \
"v_mfma_f32_16x16x32_bf16 v[68:71], a[72:75], a[0:3], v[68:71]\n" \
"s_waitcnt lgkmcnt(4)\n" \
"ds_read_b64_tr_b16 a[96:97], v232 offset:33792\n" \
"v_cvt_pk_bf16_f32 v243, v243, v252\n" \
"v_mfma_f32_16x16x32_bf16 v[68:71], a[76:79], a[4:7], v[68:71]\n" \
"buffer_load_dwordx4 v224, s[48:51], s77 offen offset:0 lds\n" \
"v_mfma_f32_16x16x32_bf16 v[72:75], a[80:83], a[0:3], v[72:75]\n" \
"ds_read_b64_tr_b16 a[98:99], v232 offset:35840\n" \
"v_cvt_pk_bf16_f32 v253, v253, v254\n" \
"v_mfma_f32_16x16x32_bf16 v[72:75], a[84:87], a[4:7], v[72:75]\n" \
"s_waitcnt lgkmcnt(3)\n" \
"ds_read_b64_tr_b16 a[100:101], v232 offset:41984\n" \
"v_accvgpr_write_b32 a236, v243\n" \
"v_mfma_f32_16x16x32_bf16 v[76:79], a[88:91], a[0:3], v[76:79]\n" \
"ds_read_b64_tr_b16 a[102:103], v232 offset:44032\n" \
"v_accvgpr_write_b32 a237, v253\n" \
"v_mfma_f32_16x16x32_bf16 v[76:79], a[92:95], a[4:7], v[76:79]\n" \
"buffer_load_dwordx4 v225, s[48:51], s77 offen offset:1024 lds\n" \
"v_mfma_f32_16x16x32_bf16 v[108:111], a[88:91], a[8:11], v[108:111]\n" \
"ds_read_b64_tr_b16 a[104:105], v233 offset:33792\n" \
"v_accvgpr_read_b32 v243, a252\n" \
"v_mfma_f32_16x16x32_bf16 v[108:111], a[92:95], a[12:15], v[108:111]\n" \
"ds_read_b64_tr_b16 a[106:107], v233 offset:35840\n" \
"v_accvgpr_read_b32 v252, a253\n" \
"v_mfma_f32_16x16x32_bf16 v[104:107], a[80:83], a[8:11], v[104:107]\n" \
"s_waitcnt lgkmcnt(6)\n" \
"ds_read_b64_tr_b16 a[108:109], v233 offset:41984\n" \
"v_accvgpr_read_b32 v253, a254\n" \
"v_mfma_f32_16x16x32_bf16 v[104:107], a[84:87], a[12:15], v[104:107]\n" \
"buffer_load_dwordx4 v226, s[48:51], s77 offen offset:2048 lds\n" \
"v_mfma_f32_16x16x32_bf16 v[100:103], a[72:75], a[8:11], v[100:103]\n" \
"ds_read_b64_tr_b16 a[110:111], v233 offset:44032\n" \
"v_accvgpr_read_b32 v254, a255\n" \
"v_mfma_f32_16x16x32_bf16 v[100:103], a[76:79], a[12:15], v[100:103]\n" \
"ds_read_b64_tr_b16 a[112:113], v232 offset:34304\n" \
"v_mul_f32 v243, s31, v243\n" \
"v_mfma_f32_16x16x32_bf16 v[96:99], a[64:67], a[8:11], v[96:99]\n" \
"ds_read_b64_tr_b16 a[114:115], v232 offset:36352\n" \
"v_mul_f32 v252, s31, v252\n" \
"v_mfma_f32_16x16x32_bf16 v[96:99], a[68:71], a[12:15], v[96:99]\n" \
"buffer_load_dwordx4 v227, s[48:51], s77 offen offset:3072 lds\n" \
"v_mfma_f32_16x16x32_bf16 v[128:131], a[64:67], a[16:19], v[128:131]\n" \
"ds_read_b64_tr_b16 a[116:117], v232 offset:42496\n" \
"v_mul_f32 v253, s31, v253\n" \
"v_mfma_f32_16x16x32_bf16 v[128:131], a[68:71], a[20:23], v[128:131]\n" \
"ds_read_b64_tr_b16 a[118:119], v232 offset:44544\n" \
"v_mul_f32 v254, s31, v254\n" \
"v_mfma_f32_16x16x32_bf16 v[132:135], a[72:75], a[16:19], v[132:135]\n" \
"s_xor_b32 s77, s77, 0x80\n" \
"ds_read_b64_tr_b16 a[120:121], v233 offset:34304\n" \
"v_cvt_pk_bf16_f32 v243, v243, v252\n" \
"v_mfma_f32_16x16x32_bf16 v[132:135], a[76:79], a[20:23], v[132:135]\n" \
"v_cvt_pk_bf16_f32 v253, v253, v254\n" \
"v_accvgpr_write_b32 a238, v243\n" \
"v_mfma_f32_16x16x32_bf16 v[136:139], a[80:83], a[16:19], v[136:139]\n" \
"ds_read_b64_tr_b16 a[122:123], v233 offset:36352\n" \
"v_accvgpr_write_b32 a239, v253\n" \
"v_mfma_f32_16x16x32_bf16 v[136:139], a[84:87], a[20:23], v[136:139]\n" \
"ds_read_b64_tr_b16 a[124:125], v233 offset:42496\n" \
"v_accvgpr_read_b32 v243, a192\n" \
"v_mfma_f32_16x16x32_bf16 v[140:143], a[88:91], a[16:19], v[140:143]\n" \
"ds_read_b64_tr_b16 a[126:127], v233 offset:44544\n" \
"v_accvgpr_read_b32 v252, a193\n" \
"v_mfma_f32_16x16x32_bf16 v[140:143], a[92:95], a[20:23], v[140:143]\n" \
"buffer_store_dwordx4 v[200:203], v236, s[56:59], s93 offen offset:2048 sc1\n" \
"v_mfma_f32_16x16x32_bf16 v[172:175], a[88:91], a[24:27], v[172:175]\n" \
"v_accvgpr_read_b32 v253, a194\n" \
"v_accvgpr_read_b32 v254, a195\n" \
"v_mfma_f32_16x16x32_bf16 v[172:175], a[92:95], a[28:31], v[172:175]\n" \
"v_mul_f32 v243, s31, v243\n" \
"v_mul_f32 v252, s31, v252\n" \
"v_mfma_f32_16x16x32_bf16 v[168:171], a[80:83], a[24:27], v[168:171]\n" \
"v_mul_f32 v253, s31, v253\n" \
"v_mul_f32 v254, s31, v254\n" \
"v_mfma_f32_16x16x32_bf16 v[168:171], a[84:87], a[28:31], v[168:171]\n" \
"v_cvt_pk_bf16_f32 v243, v243, v252\n" \
"v_cvt_pk_bf16_f32 v253, v253, v254\n" \
"v_mfma_f32_16x16x32_bf16 v[164:167], a[72:75], a[24:27], v[164:167]\n" \
"v_accvgpr_write_b32 a192, v243\n" \
"v_accvgpr_write_b32 a193, v253\n" \
"v_mfma_f32_16x16x32_bf16 v[164:167], a[76:79], a[28:31], v[164:167]\n" \
"v_accvgpr_read_b32 v243, a208\n" \
"v_accvgpr_read_b32 v252, a209\n" \
"v_mfma_f32_16x16x32_bf16 v[160:163], a[64:67], a[24:27], v[160:163]\n" \
"v_accvgpr_read_b32 v253, a210\n" \
"v_accvgpr_read_b32 v254, a211\n" \
"v_mfma_f32_16x16x32_bf16 v[160:163], a[68:71], a[28:31], v[160:163]\n" \
"s_waitcnt vmcnt(35) lgkmcnt(0)\n" \
"s_barrier\n" \
"v_mfma_f32_16x16x32_bf16 v[80:83], a[96:99], a[0:3], v[80:83]\n" \
"ds_read_b64_tr_b16 a[64:65], v232 offset:49152\n" \
"v_mul_f32 v243, s31, v243\n" \
"v_mfma_f32_16x16x32_bf16 v[80:83], a[100:103], a[4:7], v[80:83]\n" \
"ds_read_b64_tr_b16 a[66:67], v232 offset:51200\n" \
"v_mul_f32 v252, s31, v252\n" \
"v_mfma_f32_16x16x32_bf16 v[84:87], a[104:107], a[0:3], v[84:87]\n" \
"s_add_u32 m0, s79, 32768\n" \
"ds_read_b64_tr_b16 a[68:69], v232 offset:57344\n" \
"v_mul_f32 v253, s31, v253\n" \
"v_mfma_f32_16x16x32_bf16 v[84:87], a[108:111], a[4:7], v[84:87]\n" \
"buffer_load_dwordx4 v228, s[52:55], s78 offen offset:0 lds\n" \
"v_mfma_f32_16x16x32_bf16 v[88:91], a[112:115], a[0:3], v[88:91]\n" \
"ds_read_b64_tr_b16 a[70:71], v232 offset:59392\n" \
"v_mul_f32 v254, s31, v254\n" \
"v_mfma_f32_16x16x32_bf16 v[88:91], a[116:119], a[4:7], v[88:91]\n" \
"ds_read_b64_tr_b16 a[72:73], v233 offset:49152\n" \
"v_cvt_pk_bf16_f32 v243, v243, v252\n" \
"v_mfma_f32_16x16x32_bf16 v[92:95], a[120:123], a[0:3], v[92:95]\n" \
"ds_read_b64_tr_b16 a[74:75], v233 offset:51200\n" \
"v_cvt_pk_bf16_f32 v253, v253, v254\n" \
"v_mfma_f32_16x16x32_bf16 v[92:95], a[124:127], a[4:7], v[92:95]\n" \
"buffer_load_dwordx4 v229, s[52:55], s78 offen offset:1024 lds\n" \
"v_mfma_f32_16x16x32_bf16 v[124:127], a[120:123], a[8:11], v[124:127]\n" \
"s_waitcnt vmcnt(35)\n" \
"ds_read_b128 a[0:3], v250 offset:0\n" \
"v_accvgpr_write_b32 a194, v243\n" \
"v_mfma_f32_16x16x32_bf16 v[124:127], a[124:127], a[12:15], v[124:127]\n" \
"ds_read_b128 a[4:7], v251 offset:0\n" \
"v_accvgpr_write_b32 a195, v253\n" \
"v_mfma_f32_16x16x32_bf16 v[120:123], a[112:115], a[8:11], v[120:123]\n" \
"s_xor_b32 s78, s78, 0x180000\n" \
"ds_read_b64_tr_b16 a[76:77], v233 offset:57344\n" \
"v_accvgpr_read_b32 v243, a196\n" \
"v_mfma_f32_16x16x32_bf16 v[120:123], a[116:119], a[12:15], v[120:123]\n" \
"buffer_load_dwordx4 v228, s[52:55], s89 offen offset:2048 lds\n" \
"v_mfma_f32_16x16x32_bf16 v[116:119], a[104:107], a[8:11], v[116:119]\n" \
"ds_read_b64_tr_b16 a[78:79], v233 offset:59392\n" \
"v_accvgpr_read_b32 v252, a197\n" \
"v_mfma_f32_16x16x32_bf16 v[116:119], a[108:111], a[12:15], v[116:119]\n" \
"ds_read_b64_tr_b16 a[80:81], v232 offset:49664\n" \
"v_accvgpr_read_b32 v253, a198\n" \
"v_mfma_f32_16x16x32_bf16 v[112:115], a[96:99], a[8:11], v[112:115]\n" \
"ds_read_b64_tr_b16 a[82:83], v232 offset:51712\n" \
"v_accvgpr_read_b32 v254, a199\n" \
"v_mfma_f32_16x16x32_bf16 v[112:115], a[100:103], a[12:15], v[112:115]\n" \
"buffer_load_dwordx4 v229, s[52:55], s89 offen offset:3072 lds\n" \
"v_mfma_f32_16x16x32_bf16 v[144:147], a[96:99], a[16:19], v[144:147]\n" \
"s_waitcnt vmcnt(35)\n" \
"s_xor_b32 s89, s89, 0x180000\n" \
"ds_read_b128 a[8:11], v250 offset:2048\n" \
"v_mfma_f32_16x16x32_bf16 v[144:147], a[100:103], a[20:23], v[144:147]\n" \
"s_add_u32 m0, s74, 131072\n" \
"ds_read_b128 a[12:15], v251 offset:2048\n" \
"v_mul_f32 v243, s31, v243\n" \
"v_mfma_f32_16x16x32_bf16 v[148:151], a[104:107], a[16:19], v[148:151]\n" \
"s_waitcnt lgkmcnt(6)\n" \
"ds_read_b64_tr_b16 a[84:85], v232 offset:57856\n" \
"v_mul_f32 v252, s31, v252\n" \
"v_mfma_f32_16x16x32_bf16 v[148:151], a[108:111], a[20:23], v[148:151]\n" \
"buffer_load_dwordx4 v224, s[48:51], s75 offen offset:0 lds\n" \
"v_mfma_f32_16x16x32_bf16 v[152:155], a[112:115], a[16:19], v[152:155]\n" \
"ds_read_b64_tr_b16 a[86:87], v232 offset:59904\n" \
"v_mul_f32 v253, s31, v253\n" \
"v_mfma_f32_16x16x32_bf16 v[152:155], a[116:119], a[20:23], v[152:155]\n" \
"ds_read_b64_tr_b16 a[88:89], v233 offset:49664\n" \
"v_mul_f32 v254, s31, v254\n" \
"v_mfma_f32_16x16x32_bf16 v[156:159], a[120:123], a[16:19], v[156:159]\n" \
"ds_read_b64_tr_b16 a[90:91], v233 offset:51712\n" \
"v_cvt_pk_bf16_f32 v243, v243, v252\n" \
"v_mfma_f32_16x16x32_bf16 v[156:159], a[124:127], a[20:23], v[156:159]\n" \
"buffer_load_dwordx4 v225, s[48:51], s75 offen offset:1024 lds\n" \
"v_mfma_f32_16x16x32_bf16 v[188:191], a[120:123], a[24:27], v[188:191]\n" \
"s_waitcnt vmcnt(35)\n" \
"ds_read_b128 a[16:19], v250 offset:4096\n" \
"v_cvt_pk_bf16_f32 v253, v253, v254\n" \
"v_mfma_f32_16x16x32_bf16 v[188:191], a[124:127], a[28:31], v[188:191]\n" \
"ds_read_b128 a[20:23], v251 offset:4096\n" \
"v_accvgpr_write_b32 a196, v243\n" \
"v_mfma_f32_16x16x32_bf16 v[184:187], a[112:115], a[24:27], v[184:187]\n" \
"s_waitcnt lgkmcnt(6)\n" \
"ds_read_b64_tr_b16 a[92:93], v233 offset:57856\n" \
"v_accvgpr_write_b32 a197, v253\n" \
"v_mfma_f32_16x16x32_bf16 v[184:187], a[116:119], a[28:31], v[184:187]\n" \
"buffer_load_dwordx4 v226, s[48:51], s75 offen offset:2048 lds\n" \
"v_mfma_f32_16x16x32_bf16 v[180:183], a[104:107], a[24:27], v[180:183]\n" \
"ds_read_b64_tr_b16 a[94:95], v233 offset:59904\n" \
"v_accvgpr_read_b32 v243, a212\n" \
"v_mfma_f32_16x16x32_bf16 v[180:183], a[108:111], a[28:31], v[180:183]\n" \
"v_accvgpr_read_b32 v252, a213\n" \
"v_accvgpr_read_b32 v253, a214\n" \
"v_mfma_f32_16x16x32_bf16 v[176:179], a[96:99], a[24:27], v[176:179]\n" \
"v_accvgpr_read_b32 v254, a215\n" \
"v_mul_f32 v243, s31, v243\n" \
"v_mfma_f32_16x16x32_bf16 v[176:179], a[100:103], a[28:31], v[176:179]\n" \
"buffer_load_dwordx4 v227, s[48:51], s75 offen offset:3072 lds\n" \
"v_mfma_f32_16x16x32_bf16 v[64:67], a[64:67], a[0:3], v[64:67]\n" \
"s_waitcnt vmcnt(34)\n" \
"s_xor_b32 s75, s75, 0x180\n" \
"ds_read_b128 a[24:27], v250 offset:6144\n" \
"v_mfma_f32_16x16x32_bf16 v[64:67], a[68:71], a[4:7], v[64:67]\n" \
"s_add_u32 m0, s74, 135168\n" \
"ds_read_b128 a[28:31], v251 offset:6144\n" \
"v_mul_f32 v252, s31, v252\n" \
"v_mfma_f32_16x16x32_bf16 v[68:71], a[72:75], a[0:3], v[68:71]\n" \
"s_waitcnt lgkmcnt(4)\n" \
"ds_read_b64_tr_b16 a[96:97], v232 offset:50176\n" \
"v_mul_f32 v253, s31, v253\n" \
"v_mfma_f32_16x16x32_bf16 v[68:71], a[76:79], a[4:7], v[68:71]\n" \
"buffer_load_dwordx4 v224, s[48:51], s77 offen offset:0 lds\n" \
"v_mfma_f32_16x16x32_bf16 v[72:75], a[80:83], a[0:3], v[72:75]\n" \
"ds_read_b64_tr_b16 a[98:99], v232 offset:52224\n" \
"v_mul_f32 v254, s31, v254\n" \
"v_mfma_f32_16x16x32_bf16 v[72:75], a[84:87], a[4:7], v[72:75]\n" \
"s_waitcnt lgkmcnt(3)\n" \
"ds_read_b64_tr_b16 a[100:101], v232 offset:58368\n" \
"v_cvt_pk_bf16_f32 v243, v243, v252\n" \
"v_mfma_f32_16x16x32_bf16 v[76:79], a[88:91], a[0:3], v[76:79]\n" \
"ds_read_b64_tr_b16 a[102:103], v232 offset:60416\n" \
"v_cvt_pk_bf16_f32 v253, v253, v254\n" \
"v_mfma_f32_16x16x32_bf16 v[76:79], a[92:95], a[4:7], v[76:79]\n" \
"buffer_load_dwordx4 v225, s[48:51], s77 offen offset:1024 lds\n" \
"v_mfma_f32_16x16x32_bf16 v[108:111], a[88:91], a[8:11], v[108:111]\n" \
"ds_read_b64_tr_b16 a[104:105], v233 offset:50176\n" \
"v_accvgpr_write_b32 a198, v243\n" \
"v_mfma_f32_16x16x32_bf16 v[108:111], a[92:95], a[12:15], v[108:111]\n" \
"ds_read_b64_tr_b16 a[106:107], v233 offset:52224\n" \
"v_accvgpr_write_b32 a199, v253\n" \
"v_mfma_f32_16x16x32_bf16 v[104:107], a[80:83], a[8:11], v[104:107]\n" \
"s_waitcnt lgkmcnt(6)\n" \
"ds_read_b64_tr_b16 a[108:109], v233 offset:58368\n" \
"v_accvgpr_read_b32 v243, a200\n" \
"v_mfma_f32_16x16x32_bf16 v[104:107], a[84:87], a[12:15], v[104:107]\n" \
"buffer_load_dwordx4 v226, s[48:51], s77 offen offset:2048 lds\n" \
"v_mfma_f32_16x16x32_bf16 v[100:103], a[72:75], a[8:11], v[100:103]\n" \
"ds_read_b64_tr_b16 a[110:111], v233 offset:60416\n" \
"v_accvgpr_read_b32 v252, a201\n" \
"Lpg2766_%=:\n" \
"s_cbranch_execz Lpg5013_%=\n" \
"v_mfma_f32_16x16x32_bf16 v[100:103], a[76:79], a[12:15], v[100:103]\n" \
"ds_read_b64_tr_b16 a[112:113], v232 offset:50688\n" \
"v_accvgpr_read_b32 v253, a202\n" \
"v_mfma_f32_16x16x32_bf16 v[96:99], a[64:67], a[8:11], v[96:99]\n" \
"ds_read_b64_tr_b16 a[114:115], v232 offset:52736\n" \
"v_accvgpr_read_b32 v254, a203\n" \
"v_mfma_f32_16x16x32_bf16 v[96:99], a[68:71], a[12:15], v[96:99]\n" \
"buffer_load_dwordx4 v227, s[48:51], s77 offen offset:3072 lds\n" \
"v_mfma_f32_16x16x32_bf16 v[128:131], a[64:67], a[16:19], v[128:131]\n" \
"ds_read_b64_tr_b16 a[116:117], v232 offset:58880\n" \
"v_mul_f32 v243, s31, v243\n" \
"v_mfma_f32_16x16x32_bf16 v[128:131], a[68:71], a[20:23], v[128:131]\n" \
"ds_read_b64_tr_b16 a[118:119], v232 offset:60928\n" \
"v_mul_f32 v252, s31, v252\n" \
"v_mfma_f32_16x16x32_bf16 v[132:135], a[72:75], a[16:19], v[132:135]\n" \
"s_xor_b32 s77, s77, 0x180\n" \
"ds_read_b64_tr_b16 a[120:121], v233 offset:50688\n" \
"v_mul_f32 v253, s31, v253\n" \
"v_mfma_f32_16x16x32_bf16 v[132:135], a[76:79], a[20:23], v[132:135]\n" \
"v_mul_f32 v254, s31, v254\n" \
"v_cvt_pk_bf16_f32 v243, v243, v252\n" \
"v_mfma_f32_16x16x32_bf16 v[136:139], a[80:83], a[16:19], v[136:139]\n" \
"ds_read_b64_tr_b16 a[122:123], v233 offset:52736\n" \
"v_cvt_pk_bf16_f32 v253, v253, v254\n" \
"v_mfma_f32_16x16x32_bf16 v[136:139], a[84:87], a[20:23], v[136:139]\n" \
"ds_read_b64_tr_b16 a[124:125], v233 offset:58880\n" \
"v_accvgpr_write_b32 a200, v243\n" \
"v_mfma_f32_16x16x32_bf16 v[140:143], a[88:91], a[16:19], v[140:143]\n" \
"ds_read_b64_tr_b16 a[126:127], v233 offset:60928\n" \
"v_accvgpr_write_b32 a201, v253\n" \
"v_mfma_f32_16x16x32_bf16 v[140:143], a[92:95], a[20:23], v[140:143]\n" \
"buffer_store_dwordx4 v[216:219], v236, s[56:59], s93 offen offset:2304 sc1\n" \
"v_mfma_f32_16x16x32_bf16 v[172:175], a[88:91], a[24:27], v[172:175]\n" \
"v_accvgpr_read_b32 v243, a216\n" \
"v_accvgpr_read_b32 v252, a217\n" \
"v_mfma_f32_16x16x32_bf16 v[172:175], a[92:95], a[28:31], v[172:175]\n" \
"v_accvgpr_read_b32 v253, a218\n" \
"v_accvgpr_read_b32 v254, a219\n" \
"v_mfma_f32_16x16x32_bf16 v[168:171], a[80:83], a[24:27], v[168:171]\n" \
"v_mul_f32 v243, s31, v243\n" \
"v_mul_f32 v252, s31, v252\n" \
"v_mfma_f32_16x16x32_bf16 v[168:171], a[84:87], a[28:31], v[168:171]\n" \
"v_mul_f32 v253, s31, v253\n" \
"v_mul_f32 v254, s31, v254\n" \
"v_mfma_f32_16x16x32_bf16 v[164:167], a[72:75], a[24:27], v[164:167]\n" \
"v_cvt_pk_bf16_f32 v243, v243, v252\n" \
"v_cvt_pk_bf16_f32 v253, v253, v254\n" \
"v_mfma_f32_16x16x32_bf16 v[164:167], a[76:79], a[28:31], v[164:167]\n" \
"v_accvgpr_write_b32 a202, v243\n" \
"v_accvgpr_write_b32 a203, v253\n" \
"v_mfma_f32_16x16x32_bf16 v[160:163], a[64:67], a[24:27], v[160:163]\n" \
"v_accvgpr_read_b32 v243, a204\n" \
"v_accvgpr_read_b32 v252, a205\n" \
"v_mfma_f32_16x16x32_bf16 v[160:163], a[68:71], a[28:31], v[160:163]\n" \
"s_waitcnt vmcnt(35) lgkmcnt(0)\n" \
"s_barrier\n" \
"v_mfma_f32_16x16x32_bf16 v[80:83], a[96:99], a[0:3], v[80:83]\n" \
"ds_read_b64_tr_b16 a[64:65], v232 offset:0\n" \
"v_accvgpr_read_b32 v253, a206\n" \
"v_mfma_f32_16x16x32_bf16 v[80:83], a[100:103], a[4:7], v[80:83]\n" \
"ds_read_b64_tr_b16 a[66:67], v232 offset:2048\n" \
"v_accvgpr_read_b32 v254, a207\n" \
"v_mfma_f32_16x16x32_bf16 v[84:87], a[104:107], a[0:3], v[84:87]\n" \
"s_add_u32 m0, s79, 49152\n" \
"ds_read_b64_tr_b16 a[68:69], v232 offset:8192\n" \
"v_mul_f32 v243, s31, v243\n" \
"v_mfma_f32_16x16x32_bf16 v[84:87], a[108:111], a[4:7], v[84:87]\n" \
"buffer_load_dwordx4 v228, s[52:55], s78 offen offset:0 lds\n" \
"v_mfma_f32_16x16x32_bf16 v[88:91], a[112:115], a[0:3], v[88:91]\n" \
"ds_read_b64_tr_b16 a[70:71], v232 offset:10240\n" \
"v_mul_f32 v252, s31, v252\n" \
"v_mfma_f32_16x16x32_bf16 v[88:91], a[116:119], a[4:7], v[88:91]\n" \
"ds_read_b64_tr_b16 a[72:73], v233 offset:0\n" \
"v_mul_f32 v253, s31, v253\n" \
"v_mfma_f32_16x16x32_bf16 v[92:95], a[120:123], a[0:3], v[92:95]\n" \
"ds_read_b64_tr_b16 a[74:75], v233 offset:2048\n" \
"v_mul_f32 v254, s31, v254\n" \
"v_mfma_f32_16x16x32_bf16 v[92:95], a[124:127], a[4:7], v[92:95]\n" \
"buffer_load_dwordx4 v229, s[52:55], s78 offen offset:1024 lds\n" \
"v_mfma_f32_16x16x32_bf16 v[124:127], a[120:123], a[8:11], v[124:127]\n" \
"s_waitcnt vmcnt(35)\n" \
"ds_read_b128 a[0:3], v230 offset:0\n" \
"v_cvt_pk_bf16_f32 v243, v243, v252\n" \
"v_mfma_f32_16x16x32_bf16 v[124:127], a[124:127], a[12:15], v[124:127]\n" \
"ds_read_b128 a[4:7], v231 offset:0\n" \
"v_cvt_pk_bf16_f32 v253, v253, v254\n" \
"v_mfma_f32_16x16x32_bf16 v[120:123], a[112:115], a[8:11], v[120:123]\n" \
"s_xor_b32 s78, s78, 0x80000\n" \
"ds_read_b64_tr_b16 a[76:77], v233 offset:8192\n" \
"v_accvgpr_write_b32 a204, v243\n" \
"v_mfma_f32_16x16x32_bf16 v[120:123], a[116:119], a[12:15], v[120:123]\n" \
"buffer_load_dwordx4 v228, s[52:55], s89 offen offset:2048 lds\n" \
"v_mfma_f32_16x16x32_bf16 v[116:119], a[104:107], a[8:11], v[116:119]\n" \
"ds_read_b64_tr_b16 a[78:79], v233 offset:10240\n" \
"v_accvgpr_write_b32 a205, v253\n" \
"v_mfma_f32_16x16x32_bf16 v[116:119], a[108:111], a[12:15], v[116:119]\n" \
"ds_read_b64_tr_b16 a[80:81], v232 offset:512\n" \
"v_accvgpr_read_b32 v243, a220\n" \
"v_mfma_f32_16x16x32_bf16 v[112:115], a[96:99], a[8:11], v[112:115]\n" \
"ds_read_b64_tr_b16 a[82:83], v232 offset:2560\n" \
"v_accvgpr_read_b32 v252, a221\n" \
"v_mfma_f32_16x16x32_bf16 v[112:115], a[100:103], a[12:15], v[112:115]\n" \
"buffer_load_dwordx4 v229, s[52:55], s89 offen offset:3072 lds\n" \
"v_mfma_f32_16x16x32_bf16 v[144:147], a[96:99], a[16:19], v[144:147]\n" \
"s_waitcnt vmcnt(35)\n" \
"s_xor_b32 s89, s89, 0x80000\n" \
"ds_read_b128 a[8:11], v230 offset:2048\n" \
"v_mfma_f32_16x16x32_bf16 v[144:147], a[100:103], a[20:23], v[144:147]\n" \
"s_add_u32 m0, s74, 65536\n" \
"ds_read_b128 a[12:15], v231 offset:2048\n" \
"v_accvgpr_read_b32 v253, a222\n" \
"v_mfma_f32_16x16x32_bf16 v[148:151], a[104:107], a[16:19], v[148:151]\n" \
"s_waitcnt lgkmcnt(6)\n" \
"ds_read_b64_tr_b16 a[84:85], v232 offset:8704\n" \
"v_accvgpr_read_b32 v254, a223\n" \
"v_mfma_f32_16x16x32_bf16 v[148:151], a[108:111], a[20:23], v[148:151]\n" \
"buffer_load_dwordx4 v224, s[48:51], s75 offen offset:0 lds\n" \
"v_mfma_f32_16x16x32_bf16 v[152:155], a[112:115], a[16:19], v[152:155]\n" \
"ds_read_b64_tr_b16 a[86:87], v232 offset:10752\n" \
"v_mul_f32 v243, s31, v243\n" \
"v_mfma_f32_16x16x32_bf16 v[152:155], a[116:119], a[20:23], v[152:155]\n" \
"ds_read_b64_tr_b16 a[88:89], v233 offset:512\n" \
"v_mul_f32 v252, s31, v252\n" \
"v_mfma_f32_16x16x32_bf16 v[156:159], a[120:123], a[16:19], v[156:159]\n" \
"ds_read_b64_tr_b16 a[90:91], v233 offset:2560\n" \
"v_mul_f32 v253, s31, v253\n" \
"v_mfma_f32_16x16x32_bf16 v[156:159], a[124:127], a[20:23], v[156:159]\n" \
"buffer_load_dwordx4 v225, s[48:51], s75 offen offset:1024 lds\n" \
"v_mfma_f32_16x16x32_bf16 v[188:191], a[120:123], a[24:27], v[188:191]\n" \
"s_waitcnt vmcnt(35)\n" \
"ds_read_b128 a[16:19], v230 offset:4096\n" \
"v_mul_f32 v254, s31, v254\n" \
"v_mfma_f32_16x16x32_bf16 v[188:191], a[124:127], a[28:31], v[188:191]\n" \
"ds_read_b128 a[20:23], v231 offset:4096\n" \
"v_cvt_pk_bf16_f32 v243, v243, v252\n" \
"v_mfma_f32_16x16x32_bf16 v[184:187], a[112:115], a[24:27], v[184:187]\n" \
"s_waitcnt lgkmcnt(6)\n" \
"ds_read_b64_tr_b16 a[92:93], v233 offset:8704\n" \
"v_cvt_pk_bf16_f32 v253, v253, v254\n" \
"v_mfma_f32_16x16x32_bf16 v[184:187], a[116:119], a[28:31], v[184:187]\n" \
"buffer_load_dwordx4 v226, s[48:51], s75 offen offset:2048 lds\n" \
"v_mfma_f32_16x16x32_bf16 v[180:183], a[104:107], a[24:27], v[180:183]\n" \
"ds_read_b64_tr_b16 a[94:95], v233 offset:10752\n" \
"v_accvgpr_write_b32 a206, v243\n" \
"v_mfma_f32_16x16x32_bf16 v[180:183], a[108:111], a[28:31], v[180:183]\n" \
"v_accvgpr_write_b32 a207, v253\n" \
"v_mul_f32 v32, s76, v32\n" \
"v_mfma_f32_16x16x32_bf16 v[176:179], a[96:99], a[24:27], v[176:179]\n" \
"v_mul_f32 v33, s76, v33\n" \
"v_mul_f32 v34, s76, v34\n" \
"v_mfma_f32_16x16x32_bf16 v[176:179], a[100:103], a[28:31], v[176:179]\n" \
"buffer_load_dwordx4 v227, s[48:51], s75 offen offset:3072 lds\n" \
"v_mfma_f32_16x16x32_bf16 v[64:67], a[64:67], a[0:3], v[64:67]\n" \
"s_waitcnt vmcnt(35)\n" \
"s_xor_b32 s75, s75, 0x80\n" \
"ds_read_b128 a[24:27], v230 offset:6144\n" \
"v_mfma_f32_16x16x32_bf16 v[64:67], a[68:71], a[4:7], v[64:67]\n" \
"s_add_u32 m0, s74, 69632\n" \
"ds_read_b128 a[28:31], v231 offset:6144\n" \
"v_mul_f32 v35, s76, v35\n" \
"v_mfma_f32_16x16x32_bf16 v[68:71], a[72:75], a[0:3], v[68:71]\n" \
"s_waitcnt lgkmcnt(4)\n" \
"ds_read_b64_tr_b16 a[96:97], v232 offset:1024\n" \
"v_cvt_pk_bf16_f32 v32, v32, v33\n" \
"v_mfma_f32_16x16x32_bf16 v[68:71], a[76:79], a[4:7], v[68:71]\n" \
"buffer_load_dwordx4 v224, s[48:51], s77 offen offset:0 lds\n" \
"v_mfma_f32_16x16x32_bf16 v[72:75], a[80:83], a[0:3], v[72:75]\n" \
"ds_read_b64_tr_b16 a[98:99], v232 offset:3072\n" \
"v_cvt_pk_bf16_f32 v33, v34, v35\n" \
"v_mfma_f32_16x16x32_bf16 v[72:75], a[84:87], a[4:7], v[72:75]\n" \
"s_waitcnt lgkmcnt(3)\n" \
"ds_read_b64_tr_b16 a[100:101], v232 offset:9216\n" \
"v_mul_f32 v36, s76, v36\n" \
"v_mfma_f32_16x16x32_bf16 v[76:79], a[88:91], a[0:3], v[76:79]\n" \
"ds_read_b64_tr_b16 a[102:103], v232 offset:11264\n" \
"v_mul_f32 v37, s76, v37\n" \
"v_mfma_f32_16x16x32_bf16 v[76:79], a[92:95], a[4:7], v[76:79]\n" \
"buffer_load_dwordx4 v225, s[48:51], s77 offen offset:1024 lds\n" \
"v_mfma_f32_16x16x32_bf16 v[108:111], a[88:91], a[8:11], v[108:111]\n" \
"ds_read_b64_tr_b16 a[104:105], v233 offset:1024\n" \
"v_mul_f32 v38, s76, v38\n" \
"v_mfma_f32_16x16x32_bf16 v[108:111], a[92:95], a[12:15], v[108:111]\n" \
"ds_read_b64_tr_b16 a[106:107], v233 offset:3072\n" \
"v_mul_f32 v39, s76, v39\n" \
"v_mfma_f32_16x16x32_bf16 v[104:107], a[80:83], a[8:11], v[104:107]\n" \
"s_waitcnt lgkmcnt(6)\n" \
"ds_read_b64_tr_b16 a[108:109], v233 offset:9216\n" \
"v_cvt_pk_bf16_f32 v34, v36, v37\n" \
"v_mfma_f32_16x16x32_bf16 v[104:107], a[84:87], a[12:15], v[104:107]\n" \
"buffer_load_dwordx4 v226, s[48:51], s77 offen offset:2048 lds\n" \
"v_mfma_f32_16x16x32_bf16 v[100:103], a[72:75], a[8:11], v[100:103]\n" \
"ds_read_b64_tr_b16 a[110:111], v233 offset:11264\n" \
"v_cvt_pk_bf16_f32 v35, v38, v39\n" \
"v_mfma_f32_16x16x32_bf16 v[100:103], a[76:79], a[12:15], v[100:103]\n" \
"ds_read_b64_tr_b16 a[112:113], v232 offset:1536\n" \
"v_mul_f32 v48, s76, v48\n" \
"v_mfma_f32_16x16x32_bf16 v[96:99], a[64:67], a[8:11], v[96:99]\n" \
"ds_read_b64_tr_b16 a[114:115], v232 offset:3584\n" \
"v_mul_f32 v49, s76, v49\n" \
"v_mfma_f32_16x16x32_bf16 v[96:99], a[68:71], a[12:15], v[96:99]\n" \
"buffer_load_dwordx4 v227, s[48:51], s77 offen offset:3072 lds\n" \
"v_mfma_f32_16x16x32_bf16 v[128:131], a[64:67], a[16:19], v[128:131]\n" \
"ds_read_b64_tr_b16 a[116:117], v232 offset:9728\n" \
"v_mul_f32 v50, s76, v50\n" \
"v_mfma_f32_16x16x32_bf16 v[128:131], a[68:71], a[20:23], v[128:131]\n" \
"ds_read_b64_tr_b16 a[118:119], v232 offset:11776\n" \
"v_mul_f32 v51, s76, v51\n" \
"v_mfma_f32_16x16x32_bf16 v[132:135], a[72:75], a[16:19], v[132:135]\n" \
"s_xor_b32 s77, s77, 0x80\n" \
"ds_read_b64_tr_b16 a[120:121], v233 offset:1536\n" \
"v_cvt_pk_bf16_f32 v48, v48, v49\n" \
"v_mfma_f32_16x16x32_bf16 v[132:135], a[76:79], a[20:23], v[132:135]\n" \
"v_cvt_pk_bf16_f32 v49, v50, v51\n" \
"v_mul_f32 v52, s76, v52\n" \
"v_mfma_f32_16x16x32_bf16 v[136:139], a[80:83], a[16:19], v[136:139]\n" \
"ds_read_b64_tr_b16 a[122:123], v233 offset:3584\n" \
"v_mul_f32 v53, s76, v53\n" \
"v_mfma_f32_16x16x32_bf16 v[136:139], a[84:87], a[20:23], v[136:139]\n" \
"ds_read_b64_tr_b16 a[124:125], v233 offset:9728\n" \
"v_mul_f32 v54, s76, v54\n" \
"v_mfma_f32_16x16x32_bf16 v[140:143], a[88:91], a[16:19], v[140:143]\n" \
"s_add_u32 s93, s87, 0\n" \
"ds_read_b64_tr_b16 a[126:127], v233 offset:11776\n" \
"v_mul_f32 v55, s76, v55\n" \
"v_mfma_f32_16x16x32_bf16 v[140:143], a[92:95], a[20:23], v[140:143]\n" \
"buffer_store_dwordx4 v[196:199], v236, s[56:59], s93 offen offset:0 sc1\n" \
"v_mfma_f32_16x16x32_bf16 v[172:175], a[88:91], a[24:27], v[172:175]\n" \
"v_cvt_pk_bf16_f32 v50, v52, v53\n" \
"v_cvt_pk_bf16_f32 v51, v54, v55\n" \
"v_mfma_f32_16x16x32_bf16 v[172:175], a[92:95], a[28:31], v[172:175]\n" \
"v_mul_f32 v40, s76, v40\n" \
"v_mul_f32 v41, s76, v41\n" \
"v_mfma_f32_16x16x32_bf16 v[168:171], a[80:83], a[24:27], v[168:171]\n" \
"v_mul_f32 v42, s76, v42\n" \
"v_mul_f32 v43, s76, v43\n" \
"v_mfma_f32_16x16x32_bf16 v[168:171], a[84:87], a[28:31], v[168:171]\n" \
"v_cvt_pk_bf16_f32 v40, v40, v41\n" \
"v_cvt_pk_bf16_f32 v41, v42, v43\n" \
"v_mfma_f32_16x16x32_bf16 v[164:167], a[72:75], a[24:27], v[164:167]\n" \
"v_mul_f32 v44, s76, v44\n" \
"v_mul_f32 v45, s76, v45\n" \
"v_mfma_f32_16x16x32_bf16 v[164:167], a[76:79], a[28:31], v[164:167]\n" \
"v_mul_f32 v46, s76, v46\n" \
"v_mul_f32 v47, s76, v47\n" \
"v_mfma_f32_16x16x32_bf16 v[160:163], a[64:67], a[24:27], v[160:163]\n" \
"v_cvt_pk_bf16_f32 v42, v44, v45\n" \
"v_cvt_pk_bf16_f32 v43, v46, v47\n" \
"v_mfma_f32_16x16x32_bf16 v[160:163], a[68:71], a[28:31], v[160:163]\n" \
"s_waitcnt vmcnt(35) lgkmcnt(0)\n" \
"s_barrier\n" \
"v_mfma_f32_16x16x32_bf16 v[80:83], a[96:99], a[0:3], v[80:83]\n" \
"ds_read_b64_tr_b16 a[64:65], v232 offset:16384\n" \
"v_mul_f32 v56, s76, v56\n" \
"v_mfma_f32_16x16x32_bf16 v[80:83], a[100:103], a[4:7], v[80:83]\n" \
"ds_read_b64_tr_b16 a[66:67], v232 offset:18432\n" \
"v_mul_f32 v57, s76, v57\n" \
"v_mfma_f32_16x16x32_bf16 v[84:87], a[104:107], a[0:3], v[84:87]\n" \
"s_add_u32 m0, s79, 0\n" \
"ds_read_b64_tr_b16 a[68:69], v232 offset:24576\n" \
"v_mul_f32 v58, s76, v58\n" \
"v_mfma_f32_16x16x32_bf16 v[84:87], a[108:111], a[4:7], v[84:87]\n" \
"buffer_load_dwordx4 v228, s[52:55], s78 offen offset:0 lds\n" \
"v_mfma_f32_16x16x32_bf16 v[88:91], a[112:115], a[0:3], v[88:91]\n" \
"ds_read_b64_tr_b16 a[70:71], v232 offset:26624\n" \
"v_mul_f32 v59, s76, v59\n" \
"v_mfma_f32_16x16x32_bf16 v[88:91], a[116:119], a[4:7], v[88:91]\n" \
"ds_read_b64_tr_b16 a[72:73], v233 offset:16384\n" \
"v_cvt_pk_bf16_f32 v56, v56, v57\n" \
"v_mfma_f32_16x16x32_bf16 v[92:95], a[120:123], a[0:3], v[92:95]\n" \
"ds_read_b64_tr_b16 a[74:75], v233 offset:18432\n" \
"v_cvt_pk_bf16_f32 v57, v58, v59\n" \
"v_mfma_f32_16x16x32_bf16 v[92:95], a[124:127], a[4:7], v[92:95]\n" \
"buffer_load_dwordx4 v229, s[52:55], s78 offen offset:1024 lds\n" \
"v_mfma_f32_16x16x32_bf16 v[124:127], a[120:123], a[8:11], v[124:127]\n" \
"s_waitcnt vmcnt(35)\n" \
"ds_read_b128 a[0:3], v230 offset:32768\n" \
"v_mul_f32 v60, s76, v60\n" \
"v_mfma_f32_16x16x32_bf16 v[124:127], a[124:127], a[12:15], v[124:127]\n" \
"ds_read_b128 a[4:7], v231 offset:32768\n" \
"v_mul_f32 v61, s76, v61\n" \
"v_mfma_f32_16x16x32_bf16 v[120:123], a[112:115], a[8:11], v[120:123]\n" \
"s_xor_b32 s78, s78, 0x780000\n" \
"ds_read_b64_tr_b16 a[76:77], v233 offset:24576\n" \
"v_mul_f32 v62, s76, v62\n" \
"v_mfma_f32_16x16x32_bf16 v[120:123], a[116:119], a[12:15], v[120:123]\n" \
"buffer_load_dwordx4 v228, s[52:55], s89 offen offset:2048 lds\n" \
"v_mfma_f32_16x16x32_bf16 v[116:119], a[104:107], a[8:11], v[116:119]\n" \
"ds_read_b64_tr_b16 a[78:79], v233 offset:26624\n" \
"v_mul_f32 v63, s76, v63\n" \
"v_mfma_f32_16x16x32_bf16 v[116:119], a[108:111], a[12:15], v[116:119]\n" \
"ds_read_b64_tr_b16 a[80:81], v232 offset:16896\n" \
"v_cvt_pk_bf16_f32 v58, v60, v61\n" \
"v_mfma_f32_16x16x32_bf16 v[112:115], a[96:99], a[8:11], v[112:115]\n" \
"ds_read_b64_tr_b16 a[82:83], v232 offset:18944\n" \
"v_cvt_pk_bf16_f32 v59, v62, v63\n" \
"v_mfma_f32_16x16x32_bf16 v[112:115], a[100:103], a[12:15], v[112:115]\n" \
"buffer_load_dwordx4 v229, s[52:55], s89 offen offset:3072 lds\n" \
"v_mfma_f32_16x16x32_bf16 v[144:147], a[96:99], a[16:19], v[144:147]\n" \
"s_waitcnt vmcnt(35)\n" \
"s_xor_b32 s89, s89, 0x780000\n" \
"ds_read_b128 a[8:11], v230 offset:34816\n" \
"v_mfma_f32_16x16x32_bf16 v[144:147], a[100:103], a[20:23], v[144:147]\n" \
"s_add_u32 m0, s74, 98304\n" \
"ds_read_b128 a[12:15], v231 offset:34816\n" \
"v_mul_f32 v0, s76, v0\n" \
"v_mfma_f32_16x16x32_bf16 v[148:151], a[104:107], a[16:19], v[148:151]\n" \
"s_waitcnt lgkmcnt(6)\n" \
"ds_read_b64_tr_b16 a[84:85], v232 offset:25088\n" \
"v_mul_f32 v1, s76, v1\n" \
"v_mfma_f32_16x16x32_bf16 v[148:151], a[108:111], a[20:23], v[148:151]\n" \
"buffer_load_dwordx4 v224, s[48:51], s75 offen offset:0 lds\n" \
"v_mfma_f32_16x16x32_bf16 v[152:155], a[112:115], a[16:19], v[152:155]\n" \
"ds_read_b64_tr_b16 a[86:87], v232 offset:27136\n" \
"v_mul_f32 v2, s76, v2\n" \
"v_mfma_f32_16x16x32_bf16 v[152:155], a[116:119], a[20:23], v[152:155]\n" \
"ds_read_b64_tr_b16 a[88:89], v233 offset:16896\n" \
"v_mul_f32 v3, s76, v3\n" \
"v_mfma_f32_16x16x32_bf16 v[156:159], a[120:123], a[16:19], v[156:159]\n" \
"ds_read_b64_tr_b16 a[90:91], v233 offset:18944\n" \
"v_cvt_pk_bf16_f32 v0, v0, v1\n" \
"v_mfma_f32_16x16x32_bf16 v[156:159], a[124:127], a[20:23], v[156:159]\n" \
"buffer_load_dwordx4 v225, s[48:51], s75 offen offset:1024 lds\n" \
"v_mfma_f32_16x16x32_bf16 v[188:191], a[120:123], a[24:27], v[188:191]\n" \
"s_waitcnt vmcnt(35)\n" \
"ds_read_b128 a[16:19], v230 offset:36864\n" \
"v_cvt_pk_bf16_f32 v1, v2, v3\n" \
"v_mfma_f32_16x16x32_bf16 v[188:191], a[124:127], a[28:31], v[188:191]\n" \
"ds_read_b128 a[20:23], v231 offset:36864\n" \
"v_mul_f32 v4, s76, v4\n" \
"v_mfma_f32_16x16x32_bf16 v[184:187], a[112:115], a[24:27], v[184:187]\n" \
"s_waitcnt lgkmcnt(6)\n" \
"ds_read_b64_tr_b16 a[92:93], v233 offset:25088\n" \
"v_mul_f32 v5, s76, v5\n" \
"v_mfma_f32_16x16x32_bf16 v[184:187], a[116:119], a[28:31], v[184:187]\n" \
"buffer_load_dwordx4 v226, s[48:51], s75 offen offset:2048 lds\n" \
"v_mfma_f32_16x16x32_bf16 v[180:183], a[104:107], a[24:27], v[180:183]\n" \
"ds_read_b64_tr_b16 a[94:95], v233 offset:27136\n" \
"v_mul_f32 v6, s76, v6\n" \
"v_mfma_f32_16x16x32_bf16 v[180:183], a[108:111], a[28:31], v[180:183]\n" \
"v_mul_f32 v7, s76, v7\n" \
"v_cvt_pk_bf16_f32 v2, v4, v5\n" \
"v_mfma_f32_16x16x32_bf16 v[176:179], a[96:99], a[24:27], v[176:179]\n" \
"v_cvt_pk_bf16_f32 v3, v6, v7\n" \
"v_mul_f32 v16, s76, v16\n" \
"v_mfma_f32_16x16x32_bf16 v[176:179], a[100:103], a[28:31], v[176:179]\n" \
"buffer_load_dwordx4 v227, s[48:51], s75 offen offset:3072 lds\n" \
"v_mfma_f32_16x16x32_bf16 v[64:67], a[64:67], a[0:3], v[64:67]\n" \
"s_waitcnt vmcnt(35)\n" \
"s_xor_b32 s75, s75, 0x780\n" \
"ds_read_b128 a[24:27], v230 offset:38912\n" \
"v_mfma_f32_16x16x32_bf16 v[64:67], a[68:71], a[4:7], v[64:67]\n" \
"s_add_u32 m0, s74, 102400\n" \
"ds_read_b128 a[28:31], v231 offset:38912\n" \
"v_mul_f32 v17, s76, v17\n" \
"v_mfma_f32_16x16x32_bf16 v[68:71], a[72:75], a[0:3], v[68:71]\n" \
"s_waitcnt lgkmcnt(4)\n" \
"ds_read_b64_tr_b16 a[96:97], v232 offset:17408\n" \
"v_mul_f32 v18, s76, v18\n" \
"v_mfma_f32_16x16x32_bf16 v[68:71], a[76:79], a[4:7], v[68:71]\n" \
"buffer_load_dwordx4 v224, s[48:51], s77 offen offset:0 lds\n" \
"v_mfma_f32_16x16x32_bf16 v[72:75], a[80:83], a[0:3], v[72:75]\n" \
"ds_read_b64_tr_b16 a[98:99], v232 offset:19456\n" \
"v_mul_f32 v19, s76, v19\n" \
"v_mfma_f32_16x16x32_bf16 v[72:75], a[84:87], a[4:7], v[72:75]\n" \
"s_waitcnt lgkmcnt(3)\n" \
"ds_read_b64_tr_b16 a[100:101], v232 offset:25600\n" \
"v_cvt_pk_bf16_f32 v16, v16, v17\n" \
"v_mfma_f32_16x16x32_bf16 v[76:79], a[88:91], a[0:3], v[76:79]\n" \
"ds_read_b64_tr_b16 a[102:103], v232 offset:27648\n" \
"v_cvt_pk_bf16_f32 v17, v18, v19\n" \
"v_mfma_f32_16x16x32_bf16 v[76:79], a[92:95], a[4:7], v[76:79]\n" \
"buffer_load_dwordx4 v225, s[48:51], s77 offen offset:1024 lds\n" \
"v_mfma_f32_16x16x32_bf16 v[108:111], a[88:91], a[8:11], v[108:111]\n" \
"ds_read_b64_tr_b16 a[104:105], v233 offset:17408\n" \
"v_mul_f32 v20, s76, v20\n" \
"v_mfma_f32_16x16x32_bf16 v[108:111], a[92:95], a[12:15], v[108:111]\n" \
"ds_read_b64_tr_b16 a[106:107], v233 offset:19456\n" \
"v_mul_f32 v21, s76, v21\n" \
"v_mfma_f32_16x16x32_bf16 v[104:107], a[80:83], a[8:11], v[104:107]\n" \
"s_waitcnt lgkmcnt(6)\n" \
"ds_read_b64_tr_b16 a[108:109], v233 offset:25600\n" \
"v_mul_f32 v22, s76, v22\n" \
"v_mfma_f32_16x16x32_bf16 v[104:107], a[84:87], a[12:15], v[104:107]\n" \
"buffer_load_dwordx4 v226, s[48:51], s77 offen offset:2048 lds\n" \
"v_mfma_f32_16x16x32_bf16 v[100:103], a[72:75], a[8:11], v[100:103]\n" \
"ds_read_b64_tr_b16 a[110:111], v233 offset:27648\n" \
"v_mul_f32 v23, s76, v23\n" \
"v_mfma_f32_16x16x32_bf16 v[100:103], a[76:79], a[12:15], v[100:103]\n" \
"ds_read_b64_tr_b16 a[112:113], v232 offset:17920\n" \
"v_cvt_pk_bf16_f32 v18, v20, v21\n" \
"v_mfma_f32_16x16x32_bf16 v[96:99], a[64:67], a[8:11], v[96:99]\n" \
"ds_read_b64_tr_b16 a[114:115], v232 offset:19968\n" \
"v_cvt_pk_bf16_f32 v19, v22, v23\n" \
"v_mfma_f32_16x16x32_bf16 v[96:99], a[68:71], a[12:15], v[96:99]\n" \
"buffer_load_dwordx4 v227, s[48:51], s77 offen offset:3072 lds\n" \
"v_mfma_f32_16x16x32_bf16 v[128:131], a[64:67], a[16:19], v[128:131]\n" \
"ds_read_b64_tr_b16 a[116:117], v232 offset:26112\n" \
"v_mul_f32 v8, s76, v8\n" \
"v_mfma_f32_16x16x32_bf16 v[128:131], a[68:71], a[20:23], v[128:131]\n" \
"ds_read_b64_tr_b16 a[118:119], v232 offset:28160\n" \
"v_mul_f32 v9, s76, v9\n" \
"v_mfma_f32_16x16x32_bf16 v[132:135], a[72:75], a[16:19], v[132:135]\n" \
"s_xor_b32 s77, s77, 0x780\n" \
"ds_read_b64_tr_b16 a[120:121], v233 offset:17920\n" \
"v_mul_f32 v10, s76, v10\n" \
"v_mfma_f32_16x16x32_bf16 v[132:135], a[76:79], a[20:23], v[132:135]\n" \
"v_mul_f32 v11, s76, v11\n" \
"v_cvt_pk_bf16_f32 v8, v8, v9\n" \
"v_mfma_f32_16x16x32_bf16 v[136:139], a[80:83], a[16:19], v[136:139]\n" \
"ds_read_b64_tr_b16 a[122:123], v233 offset:19968\n" \
"v_cvt_pk_bf16_f32 v9, v10, v11\n" \
"v_mfma_f32_16x16x32_bf16 v[136:139], a[84:87], a[20:23], v[136:139]\n" \
"ds_read_b64_tr_b16 a[124:125], v233 offset:26112\n" \
"v_mul_f32 v12, s76, v12\n" \
"v_mfma_f32_16x16x32_bf16 v[140:143], a[88:91], a[16:19], v[140:143]\n" \
"ds_read_b64_tr_b16 a[126:127], v233 offset:28160\n" \
"v_mul_f32 v13, s76, v13\n" \
"v_mfma_f32_16x16x32_bf16 v[140:143], a[92:95], a[20:23], v[140:143]\n" \
"buffer_store_dwordx4 v[212:215], v236, s[56:59], s93 offen offset:256 sc1\n" \
"v_mfma_f32_16x16x32_bf16 v[172:175], a[88:91], a[24:27], v[172:175]\n" \
"v_mul_f32 v14, s76, v14\n" \
"v_mul_f32 v15, s76, v15\n" \
"v_mfma_f32_16x16x32_bf16 v[172:175], a[92:95], a[28:31], v[172:175]\n" \
"v_cvt_pk_bf16_f32 v10, v12, v13\n" \
"v_cvt_pk_bf16_f32 v11, v14, v15\n" \
"v_mfma_f32_16x16x32_bf16 v[168:171], a[80:83], a[24:27], v[168:171]\n" \
"v_mul_f32 v24, s76, v24\n" \
"v_mul_f32 v25, s76, v25\n" \
"v_mfma_f32_16x16x32_bf16 v[168:171], a[84:87], a[28:31], v[168:171]\n" \
"v_mul_f32 v26, s76, v26\n" \
"v_mul_f32 v27, s76, v27\n" \
"v_mfma_f32_16x16x32_bf16 v[164:167], a[72:75], a[24:27], v[164:167]\n" \
"v_cvt_pk_bf16_f32 v24, v24, v25\n" \
"v_cvt_pk_bf16_f32 v25, v26, v27\n" \
"v_mfma_f32_16x16x32_bf16 v[164:167], a[76:79], a[28:31], v[164:167]\n" \
"v_mul_f32 v28, s76, v28\n" \
"v_mul_f32 v29, s76, v29\n" \
"v_mfma_f32_16x16x32_bf16 v[160:163], a[64:67], a[24:27], v[160:163]\n" \
"v_mul_f32 v30, s76, v30\n" \
"v_mul_f32 v31, s76, v31\n" \
"v_mfma_f32_16x16x32_bf16 v[160:163], a[68:71], a[28:31], v[160:163]\n" \
"s_waitcnt vmcnt(35) lgkmcnt(0)\n" \
"s_barrier\n" \
"v_mfma_f32_16x16x32_bf16 v[80:83], a[96:99], a[0:3], v[80:83]\n" \
"ds_read_b64_tr_b16 a[64:65], v232 offset:32768\n" \
"v_cvt_pk_bf16_f32 v26, v28, v29\n" \
"v_mfma_f32_16x16x32_bf16 v[80:83], a[100:103], a[4:7], v[80:83]\n" \
"ds_read_b64_tr_b16 a[66:67], v232 offset:34816\n" \
"v_cvt_pk_bf16_f32 v27, v30, v31\n" \
"v_mfma_f32_16x16x32_bf16 v[84:87], a[104:107], a[0:3], v[84:87]\n" \
"s_add_u32 m0, s79, 16384\n" \
"ds_read_b64_tr_b16 a[68:69], v232 offset:40960\n" \
"v_accvgpr_read_b32 v243, a160\n" \
"v_mfma_f32_16x16x32_bf16 v[84:87], a[108:111], a[4:7], v[84:87]\n" \
"buffer_load_dwordx4 v228, s[52:55], s78 offen offset:0 lds\n" \
"v_mfma_f32_16x16x32_bf16 v[88:91], a[112:115], a[0:3], v[88:91]\n" \
"ds_read_b64_tr_b16 a[70:71], v232 offset:43008\n" \
"v_accvgpr_read_b32 v252, a161\n" \
"v_mfma_f32_16x16x32_bf16 v[88:91], a[116:119], a[4:7], v[88:91]\n" \
"ds_read_b64_tr_b16 a[72:73], v233 offset:32768\n" \
"v_accvgpr_read_b32 v253, a162\n" \
"v_mfma_f32_16x16x32_bf16 v[92:95], a[120:123], a[0:3], v[92:95]\n" \
"ds_read_b64_tr_b16 a[74:75], v233 offset:34816\n" \
"v_accvgpr_read_b32 v254, a163\n" \
"v_mfma_f32_16x16x32_bf16 v[92:95], a[124:127], a[4:7], v[92:95]\n" \
"buffer_load_dwordx4 v229, s[52:55], s78 offen offset:1024 lds\n" \
"v_mfma_f32_16x16x32_bf16 v[124:127], a[120:123], a[8:11], v[124:127]\n" \
"s_waitcnt vmcnt(35)\n" \
"ds_read_b128 a[0:3], v250 offset:0\n" \
"v_mul_f32 v243, s31, v243\n" \
"v_mfma_f32_16x16x32_bf16 v[124:127], a[124:127], a[12:15], v[124:127]\n" \
"ds_read_b128 a[4:7], v251 offset:0\n" \
"v_mul_f32 v252, s31, v252\n" \
"v_mfma_f32_16x16x32_bf16 v[120:123], a[112:115], a[8:11], v[120:123]\n" \
"s_xor_b32 s78, s78, 0x80000\n" \
"ds_read_b64_tr_b16 a[76:77], v233 offset:40960\n" \
"v_mul_f32 v253, s31, v253\n" \
"v_mfma_f32_16x16x32_bf16 v[120:123], a[116:119], a[12:15], v[120:123]\n" \
"buffer_load_dwordx4 v228, s[52:55], s89 offen offset:2048 lds\n" \
"v_mfma_f32_16x16x32_bf16 v[116:119], a[104:107], a[8:11], v[116:119]\n" \
"ds_read_b64_tr_b16 a[78:79], v233 offset:43008\n" \
"v_mul_f32 v254, s31, v254\n" \
"v_mfma_f32_16x16x32_bf16 v[116:119], a[108:111], a[12:15], v[116:119]\n" \
"ds_read_b64_tr_b16 a[80:81], v232 offset:33280\n" \
"v_cvt_pk_bf16_f32 v36, v243, v252\n" \
"v_mfma_f32_16x16x32_bf16 v[112:115], a[96:99], a[8:11], v[112:115]\n" \
"ds_read_b64_tr_b16 a[82:83], v232 offset:35328\n" \
"v_cvt_pk_bf16_f32 v37, v253, v254\n" \
"v_mfma_f32_16x16x32_bf16 v[112:115], a[100:103], a[12:15], v[112:115]\n" \
"buffer_load_dwordx4 v229, s[52:55], s89 offen offset:3072 lds\n" \
"v_mfma_f32_16x16x32_bf16 v[144:147], a[96:99], a[16:19], v[144:147]\n" \
"s_waitcnt vmcnt(35)\n" \
"s_xor_b32 s89, s89, 0x80000\n" \
"ds_read_b128 a[8:11], v250 offset:2048\n" \
"v_mfma_f32_16x16x32_bf16 v[144:147], a[100:103], a[20:23], v[144:147]\n" \
"s_add_u32 m0, s74, 131072\n" \
"ds_read_b128 a[12:15], v251 offset:2048\n" \
"v_accvgpr_read_b32 v243, a176\n" \
"v_mfma_f32_16x16x32_bf16 v[148:151], a[104:107], a[16:19], v[148:151]\n" \
"s_waitcnt lgkmcnt(6)\n" \
"ds_read_b64_tr_b16 a[84:85], v232 offset:41472\n" \
"v_accvgpr_read_b32 v252, a177\n" \
"v_mfma_f32_16x16x32_bf16 v[148:151], a[108:111], a[20:23], v[148:151]\n" \
"buffer_load_dwordx4 v224, s[48:51], s75 offen offset:0 lds\n" \
"v_mfma_f32_16x16x32_bf16 v[152:155], a[112:115], a[16:19], v[152:155]\n" \
"ds_read_b64_tr_b16 a[86:87], v232 offset:43520\n" \
"v_accvgpr_read_b32 v253, a178\n" \
"v_mfma_f32_16x16x32_bf16 v[152:155], a[116:119], a[20:23], v[152:155]\n" \
"ds_read_b64_tr_b16 a[88:89], v233 offset:33280\n" \
"v_accvgpr_read_b32 v254, a179\n" \
"v_mfma_f32_16x16x32_bf16 v[156:159], a[120:123], a[16:19], v[156:159]\n" \
"ds_read_b64_tr_b16 a[90:91], v233 offset:35328\n" \
"v_mul_f32 v243, s31, v243\n" \
"v_mfma_f32_16x16x32_bf16 v[156:159], a[124:127], a[20:23], v[156:159]\n" \
"buffer_load_dwordx4 v225, s[48:51], s75 offen offset:1024 lds\n" \
"v_mfma_f32_16x16x32_bf16 v[188:191], a[120:123], a[24:27], v[188:191]\n" \
"s_waitcnt vmcnt(35)\n" \
"ds_read_b128 a[16:19], v250 offset:4096\n" \
"v_mul_f32 v252, s31, v252\n" \
"v_mfma_f32_16x16x32_bf16 v[188:191], a[124:127], a[28:31], v[188:191]\n" \
"ds_read_b128 a[20:23], v251 offset:4096\n" \
"v_mul_f32 v253, s31, v253\n" \
"v_mfma_f32_16x16x32_bf16 v[184:187], a[112:115], a[24:27], v[184:187]\n" \
"s_waitcnt lgkmcnt(6)\n" \
"ds_read_b64_tr_b16 a[92:93], v233 offset:41472\n" \
"v_mul_f32 v254, s31, v254\n" \
"v_mfma_f32_16x16x32_bf16 v[184:187], a[116:119], a[28:31], v[184:187]\n" \
"buffer_load_dwordx4 v226, s[48:51], s75 offen offset:2048 lds\n" \
"v_mfma_f32_16x16x32_bf16 v[180:183], a[104:107], a[24:27], v[180:183]\n" \
"ds_read_b64_tr_b16 a[94:95], v233 offset:43520\n" \
"v_cvt_pk_bf16_f32 v38, v243, v252\n" \
"v_mfma_f32_16x16x32_bf16 v[180:183], a[108:111], a[28:31], v[180:183]\n" \
"v_cvt_pk_bf16_f32 v39, v253, v254\n" \
"v_accvgpr_read_b32 v243, a164\n" \
"v_mfma_f32_16x16x32_bf16 v[176:179], a[96:99], a[24:27], v[176:179]\n" \
"v_accvgpr_read_b32 v252, a165\n" \
"v_accvgpr_read_b32 v253, a166\n" \
"v_mfma_f32_16x16x32_bf16 v[176:179], a[100:103], a[28:31], v[176:179]\n" \
"buffer_load_dwordx4 v227, s[48:51], s75 offen offset:3072 lds\n" \
"v_mfma_f32_16x16x32_bf16 v[64:67], a[64:67], a[0:3], v[64:67]\n" \
"s_waitcnt vmcnt(35)\n" \
"s_xor_b32 s75, s75, 0x80\n" \
"ds_read_b128 a[24:27], v250 offset:6144\n" \
"v_mfma_f32_16x16x32_bf16 v[64:67], a[68:71], a[4:7], v[64:67]\n" \
"s_add_u32 m0, s74, 135168\n" \
"ds_read_b128 a[28:31], v251 offset:6144\n" \
"v_accvgpr_read_b32 v254, a167\n" \
"v_mfma_f32_16x16x32_bf16 v[68:71], a[72:75], a[0:3], v[68:71]\n" \
"s_waitcnt lgkmcnt(4)\n" \
"ds_read_b64_tr_b16 a[96:97], v232 offset:33792\n" \
"v_mul_f32 v243, s31, v243\n" \
"v_mfma_f32_16x16x32_bf16 v[68:71], a[76:79], a[4:7], v[68:71]\n" \
"buffer_load_dwordx4 v224, s[48:51], s77 offen offset:0 lds\n" \
"v_mfma_f32_16x16x32_bf16 v[72:75], a[80:83], a[0:3], v[72:75]\n" \
"ds_read_b64_tr_b16 a[98:99], v232 offset:35840\n" \
"v_mul_f32 v252, s31, v252\n" \
"v_mfma_f32_16x16x32_bf16 v[72:75], a[84:87], a[4:7], v[72:75]\n" \
"s_waitcnt lgkmcnt(3)\n" \
"ds_read_b64_tr_b16 a[100:101], v232 offset:41984\n" \
"v_mul_f32 v253, s31, v253\n" \
"v_mfma_f32_16x16x32_bf16 v[76:79], a[88:91], a[0:3], v[76:79]\n" \
"ds_read_b64_tr_b16 a[102:103], v232 offset:44032\n" \
"v_mul_f32 v254, s31, v254\n" \
"v_mfma_f32_16x16x32_bf16 v[76:79], a[92:95], a[4:7], v[76:79]\n" \
"buffer_load_dwordx4 v225, s[48:51], s77 offen offset:1024 lds\n" \
"v_mfma_f32_16x16x32_bf16 v[108:111], a[88:91], a[8:11], v[108:111]\n" \
"ds_read_b64_tr_b16 a[104:105], v233 offset:33792\n" \
"v_cvt_pk_bf16_f32 v52, v243, v252\n" \
"v_mfma_f32_16x16x32_bf16 v[108:111], a[92:95], a[12:15], v[108:111]\n" \
"ds_read_b64_tr_b16 a[106:107], v233 offset:35840\n" \
"v_cvt_pk_bf16_f32 v53, v253, v254\n" \
"v_mfma_f32_16x16x32_bf16 v[104:107], a[80:83], a[8:11], v[104:107]\n" \
"s_waitcnt lgkmcnt(6)\n" \
"ds_read_b64_tr_b16 a[108:109], v233 offset:41984\n" \
"v_accvgpr_read_b32 v243, a180\n" \
"Lpg3342_%=:\n" \
"s_cbranch_execz Lpg5600_%=\n" \
"v_mfma_f32_16x16x32_bf16 v[104:107], a[84:87], a[12:15], v[104:107]\n" \
"buffer_load_dwordx4 v226, s[48:51], s77 offen offset:2048 lds\n" \
"v_mfma_f32_16x16x32_bf16 v[100:103], a[72:75], a[8:11], v[100:103]\n" \
"ds_read_b64_tr_b16 a[110:111], v233 offset:44032\n" \
"v_accvgpr_read_b32 v252, a181\n" \
"v_mfma_f32_16x16x32_bf16 v[100:103], a[76:79], a[12:15], v[100:103]\n" \
"ds_read_b64_tr_b16 a[112:113], v232 offset:34304\n" \
"v_accvgpr_read_b32 v253, a182\n" \
"v_mfma_f32_16x16x32_bf16 v[96:99], a[64:67], a[8:11], v[96:99]\n" \
"ds_read_b64_tr_b16 a[114:115], v232 offset:36352\n" \
"v_accvgpr_read_b32 v254, a183\n" \
"v_mfma_f32_16x16x32_bf16 v[96:99], a[68:71], a[12:15], v[96:99]\n" \
"buffer_load_dwordx4 v227, s[48:51], s77 offen offset:3072 lds\n" \
"v_mfma_f32_16x16x32_bf16 v[128:131], a[64:67], a[16:19], v[128:131]\n" \
"ds_read_b64_tr_b16 a[116:117], v232 offset:42496\n" \
"v_mul_f32 v243, s31, v243\n" \
"v_mfma_f32_16x16x32_bf16 v[128:131], a[68:71], a[20:23], v[128:131]\n" \
"ds_read_b64_tr_b16 a[118:119], v232 offset:44544\n" \
"v_mul_f32 v252, s31, v252\n" \
"v_mfma_f32_16x16x32_bf16 v[132:135], a[72:75], a[16:19], v[132:135]\n" \
"s_xor_b32 s77, s77, 0x80\n" \
"ds_read_b64_tr_b16 a[120:121], v233 offset:34304\n" \
"v_mul_f32 v253, s31, v253\n" \
"v_mfma_f32_16x16x32_bf16 v[132:135], a[76:79], a[20:23], v[132:135]\n" \
"v_mul_f32 v254, s31, v254\n" \
"v_cvt_pk_bf16_f32 v54, v243, v252\n" \
"v_mfma_f32_16x16x32_bf16 v[136:139], a[80:83], a[16:19], v[136:139]\n" \
"ds_read_b64_tr_b16 a[122:123], v233 offset:36352\n" \
"v_cvt_pk_bf16_f32 v55, v253, v254\n" \
"v_mfma_f32_16x16x32_bf16 v[136:139], a[84:87], a[20:23], v[136:139]\n" \
"ds_read_b64_tr_b16 a[124:125], v233 offset:42496\n" \
"v_accvgpr_read_b32 v243, a168\n" \
"v_mfma_f32_16x16x32_bf16 v[140:143], a[88:91], a[16:19], v[140:143]\n" \
"ds_read_b64_tr_b16 a[126:127], v233 offset:44544\n" \
"v_accvgpr_read_b32 v252, a169\n" \
"v_mfma_f32_16x16x32_bf16 v[140:143], a[92:95], a[20:23], v[140:143]\n" \
"buffer_store_dwordx4 v[204:207], v236, s[56:59], s93 offen offset:2048 sc1\n" \
"v_mfma_f32_16x16x32_bf16 v[172:175], a[88:91], a[24:27], v[172:175]\n" \
"v_accvgpr_read_b32 v253, a170\n" \
"v_accvgpr_read_b32 v254, a171\n" \
"v_mfma_f32_16x16x32_bf16 v[172:175], a[92:95], a[28:31], v[172:175]\n" \
"v_mul_f32 v243, s31, v243\n" \
"v_mul_f32 v252, s31, v252\n" \
"v_mfma_f32_16x16x32_bf16 v[168:171], a[80:83], a[24:27], v[168:171]\n" \
"v_mul_f32 v253, s31, v253\n" \
"v_mul_f32 v254, s31, v254\n" \
"v_mfma_f32_16x16x32_bf16 v[168:171], a[84:87], a[28:31], v[168:171]\n" \
"v_cvt_pk_bf16_f32 v44, v243, v252\n" \
"v_cvt_pk_bf16_f32 v45, v253, v254\n" \
"v_mfma_f32_16x16x32_bf16 v[164:167], a[72:75], a[24:27], v[164:167]\n" \
"v_accvgpr_read_b32 v243, a184\n" \
"v_accvgpr_read_b32 v252, a185\n" \
"v_mfma_f32_16x16x32_bf16 v[164:167], a[76:79], a[28:31], v[164:167]\n" \
"v_accvgpr_read_b32 v253, a186\n" \
"v_accvgpr_read_b32 v254, a187\n" \
"v_mfma_f32_16x16x32_bf16 v[160:163], a[64:67], a[24:27], v[160:163]\n" \
"v_mul_f32 v243, s31, v243\n" \
"v_mul_f32 v252, s31, v252\n" \
"v_mfma_f32_16x16x32_bf16 v[160:163], a[68:71], a[28:31], v[160:163]\n" \
"s_waitcnt vmcnt(35) lgkmcnt(0)\n" \
"s_barrier\n" \
"v_mfma_f32_16x16x32_bf16 v[80:83], a[96:99], a[0:3], v[80:83]\n" \
"ds_read_b64_tr_b16 a[64:65], v232 offset:49152\n" \
"v_mul_f32 v253, s31, v253\n" \
"v_mfma_f32_16x16x32_bf16 v[80:83], a[100:103], a[4:7], v[80:83]\n" \
"ds_read_b64_tr_b16 a[66:67], v232 offset:51200\n" \
"v_mul_f32 v254, s31, v254\n" \
"v_mfma_f32_16x16x32_bf16 v[84:87], a[104:107], a[0:3], v[84:87]\n" \
"s_add_u32 m0, s79, 32768\n" \
"ds_read_b64_tr_b16 a[68:69], v232 offset:57344\n" \
"v_cvt_pk_bf16_f32 v46, v243, v252\n" \
"v_mfma_f32_16x16x32_bf16 v[84:87], a[108:111], a[4:7], v[84:87]\n" \
"buffer_load_dwordx4 v228, s[52:55], s78 offen offset:0 lds\n" \
"v_mfma_f32_16x16x32_bf16 v[88:91], a[112:115], a[0:3], v[88:91]\n" \
"ds_read_b64_tr_b16 a[70:71], v232 offset:59392\n" \
"v_cvt_pk_bf16_f32 v47, v253, v254\n" \
"v_mfma_f32_16x16x32_bf16 v[88:91], a[116:119], a[4:7], v[88:91]\n" \
"ds_read_b64_tr_b16 a[72:73], v233 offset:49152\n" \
"v_accvgpr_read_b32 v243, a172\n" \
"v_mfma_f32_16x16x32_bf16 v[92:95], a[120:123], a[0:3], v[92:95]\n" \
"ds_read_b64_tr_b16 a[74:75], v233 offset:51200\n" \
"v_accvgpr_read_b32 v252, a173\n" \
"v_mfma_f32_16x16x32_bf16 v[92:95], a[124:127], a[4:7], v[92:95]\n" \
"buffer_load_dwordx4 v229, s[52:55], s78 offen offset:1024 lds\n" \
"v_mfma_f32_16x16x32_bf16 v[124:127], a[120:123], a[8:11], v[124:127]\n" \
"s_waitcnt vmcnt(35)\n" \
"ds_read_b128 a[0:3], v230 offset:0\n" \
"v_accvgpr_read_b32 v253, a174\n" \
"v_mfma_f32_16x16x32_bf16 v[124:127], a[124:127], a[12:15], v[124:127]\n" \
"ds_read_b128 a[4:7], v231 offset:0\n" \
"v_accvgpr_read_b32 v254, a175\n" \
"v_mfma_f32_16x16x32_bf16 v[120:123], a[112:115], a[8:11], v[120:123]\n" \
"s_xor_b32 s78, s78, 0x180000\n" \
"ds_read_b64_tr_b16 a[76:77], v233 offset:57344\n" \
"v_mul_f32 v243, s31, v243\n" \
"v_mfma_f32_16x16x32_bf16 v[120:123], a[116:119], a[12:15], v[120:123]\n" \
"buffer_load_dwordx4 v228, s[52:55], s89 offen offset:2048 lds\n" \
"v_mfma_f32_16x16x32_bf16 v[116:119], a[104:107], a[8:11], v[116:119]\n" \
"ds_read_b64_tr_b16 a[78:79], v233 offset:59392\n" \
"v_mul_f32 v252, s31, v252\n" \
"v_mfma_f32_16x16x32_bf16 v[116:119], a[108:111], a[12:15], v[116:119]\n" \
"ds_read_b64_tr_b16 a[80:81], v232 offset:49664\n" \
"v_mul_f32 v253, s31, v253\n" \
"v_mfma_f32_16x16x32_bf16 v[112:115], a[96:99], a[8:11], v[112:115]\n" \
"ds_read_b64_tr_b16 a[82:83], v232 offset:51712\n" \
"v_mul_f32 v254, s31, v254\n" \
"v_mfma_f32_16x16x32_bf16 v[112:115], a[100:103], a[12:15], v[112:115]\n" \
"buffer_load_dwordx4 v229, s[52:55], s89 offen offset:3072 lds\n" \
"v_mfma_f32_16x16x32_bf16 v[144:147], a[96:99], a[16:19], v[144:147]\n" \
"s_waitcnt vmcnt(35)\n" \
"s_xor_b32 s89, s89, 0x180000\n" \
"ds_read_b128 a[8:11], v230 offset:2048\n" \
"v_mfma_f32_16x16x32_bf16 v[144:147], a[100:103], a[20:23], v[144:147]\n" \
"s_add_u32 m0, s74, 65536\n" \
"ds_read_b128 a[12:15], v231 offset:2048\n" \
"v_cvt_pk_bf16_f32 v60, v243, v252\n" \
"v_mfma_f32_16x16x32_bf16 v[148:151], a[104:107], a[16:19], v[148:151]\n" \
"s_waitcnt lgkmcnt(6)\n" \
"ds_read_b64_tr_b16 a[84:85], v232 offset:57856\n" \
"v_cvt_pk_bf16_f32 v61, v253, v254\n" \
"v_mfma_f32_16x16x32_bf16 v[148:151], a[108:111], a[20:23], v[148:151]\n" \
"buffer_load_dwordx4 v224, s[48:51], s75 offen offset:0 lds\n" \
"v_mfma_f32_16x16x32_bf16 v[152:155], a[112:115], a[16:19], v[152:155]\n" \
"ds_read_b64_tr_b16 a[86:87], v232 offset:59904\n" \
"v_accvgpr_read_b32 v243, a188\n" \
"v_mfma_f32_16x16x32_bf16 v[152:155], a[116:119], a[20:23], v[152:155]\n" \
"ds_read_b64_tr_b16 a[88:89], v233 offset:49664\n" \
"v_accvgpr_read_b32 v252, a189\n" \
"v_mfma_f32_16x16x32_bf16 v[156:159], a[120:123], a[16:19], v[156:159]\n" \
"ds_read_b64_tr_b16 a[90:91], v233 offset:51712\n" \
"v_accvgpr_read_b32 v253, a190\n" \
"v_mfma_f32_16x16x32_bf16 v[156:159], a[124:127], a[20:23], v[156:159]\n" \
"buffer_load_dwordx4 v225, s[48:51], s75 offen offset:1024 lds\n" \
"v_mfma_f32_16x16x32_bf16 v[188:191], a[120:123], a[24:27], v[188:191]\n" \
"s_waitcnt vmcnt(35)\n" \
"ds_read_b128 a[16:19], v230 offset:4096\n" \
"v_accvgpr_read_b32 v254, a191\n" \
"v_mfma_f32_16x16x32_bf16 v[188:191], a[124:127], a[28:31], v[188:191]\n" \
"ds_read_b128 a[20:23], v231 offset:4096\n" \
"v_mul_f32 v243, s31, v243\n" \
"v_mfma_f32_16x16x32_bf16 v[184:187], a[112:115], a[24:27], v[184:187]\n" \
"s_waitcnt lgkmcnt(6)\n" \
"ds_read_b64_tr_b16 a[92:93], v233 offset:57856\n" \
"v_mul_f32 v252, s31, v252\n" \
"v_mfma_f32_16x16x32_bf16 v[184:187], a[116:119], a[28:31], v[184:187]\n" \
"buffer_load_dwordx4 v226, s[48:51], s75 offen offset:2048 lds\n" \
"v_mfma_f32_16x16x32_bf16 v[180:183], a[104:107], a[24:27], v[180:183]\n" \
"ds_read_b64_tr_b16 a[94:95], v233 offset:59904\n" \
"v_mul_f32 v253, s31, v253\n" \
"v_mfma_f32_16x16x32_bf16 v[180:183], a[108:111], a[28:31], v[180:183]\n" \
"v_mul_f32 v254, s31, v254\n" \
"v_cvt_pk_bf16_f32 v62, v243, v252\n" \
"v_mfma_f32_16x16x32_bf16 v[176:179], a[96:99], a[24:27], v[176:179]\n" \
"v_cvt_pk_bf16_f32 v63, v253, v254\n" \
"v_accvgpr_read_b32 v243, a128\n" \
"v_mfma_f32_16x16x32_bf16 v[176:179], a[100:103], a[28:31], v[176:179]\n" \
"buffer_load_dwordx4 v227, s[48:51], s75 offen offset:3072 lds\n" \
"v_mfma_f32_16x16x32_bf16 v[64:67], a[64:67], a[0:3], v[64:67]\n" \
"s_waitcnt vmcnt(35)\n" \
"s_xor_b32 s75, s75, 0x180\n" \
"ds_read_b128 a[24:27], v230 offset:6144\n" \
"v_mfma_f32_16x16x32_bf16 v[64:67], a[68:71], a[4:7], v[64:67]\n" \
"s_add_u32 m0, s74, 69632\n" \
"ds_read_b128 a[28:31], v231 offset:6144\n" \
"v_accvgpr_read_b32 v252, a129\n" \
"v_mfma_f32_16x16x32_bf16 v[68:71], a[72:75], a[0:3], v[68:71]\n" \
"s_waitcnt lgkmcnt(4)\n" \
"ds_read_b64_tr_b16 a[96:97], v232 offset:50176\n" \
"v_accvgpr_read_b32 v253, a130\n" \
"v_mfma_f32_16x16x32_bf16 v[68:71], a[76:79], a[4:7], v[68:71]\n" \
"buffer_load_dwordx4 v224, s[48:51], s77 offen offset:0 lds\n" \
"v_mfma_f32_16x16x32_bf16 v[72:75], a[80:83], a[0:3], v[72:75]\n" \
"ds_read_b64_tr_b16 a[98:99], v232 offset:52224\n" \
"v_accvgpr_read_b32 v254, a131\n" \
"v_mfma_f32_16x16x32_bf16 v[72:75], a[84:87], a[4:7], v[72:75]\n" \
"s_waitcnt lgkmcnt(3)\n" \
"ds_read_b64_tr_b16 a[100:101], v232 offset:58368\n" \
"v_mul_f32 v243, s31, v243\n" \
"v_mfma_f32_16x16x32_bf16 v[76:79], a[88:91], a[0:3], v[76:79]\n" \
"ds_read_b64_tr_b16 a[102:103], v232 offset:60416\n" \
"v_mul_f32 v252, s31, v252\n" \
"v_mfma_f32_16x16x32_bf16 v[76:79], a[92:95], a[4:7], v[76:79]\n" \
"buffer_load_dwordx4 v225, s[48:51], s77 offen offset:1024 lds\n" \
"v_mfma_f32_16x16x32_bf16 v[108:111], a[88:91], a[8:11], v[108:111]\n" \
"ds_read_b64_tr_b16 a[104:105], v233 offset:50176\n" \
"v_mul_f32 v253, s31, v253\n" \
"v_mfma_f32_16x16x32_bf16 v[108:111], a[92:95], a[12:15], v[108:111]\n" \
"ds_read_b64_tr_b16 a[106:107], v233 offset:52224\n" \
"v_mul_f32 v254, s31, v254\n" \
"v_mfma_f32_16x16x32_bf16 v[104:107], a[80:83], a[8:11], v[104:107]\n" \
"s_waitcnt lgkmcnt(6)\n" \
"ds_read_b64_tr_b16 a[108:109], v233 offset:58368\n" \
"v_cvt_pk_bf16_f32 v4, v243, v252\n" \
"v_mfma_f32_16x16x32_bf16 v[104:107], a[84:87], a[12:15], v[104:107]\n" \
"buffer_load_dwordx4 v226, s[48:51], s77 offen offset:2048 lds\n" \
"v_mfma_f32_16x16x32_bf16 v[100:103], a[72:75], a[8:11], v[100:103]\n" \
"ds_read_b64_tr_b16 a[110:111], v233 offset:60416\n" \
"v_cvt_pk_bf16_f32 v5, v253, v254\n" \
"v_mfma_f32_16x16x32_bf16 v[100:103], a[76:79], a[12:15], v[100:103]\n" \
"ds_read_b64_tr_b16 a[112:113], v232 offset:50688\n" \
"v_accvgpr_read_b32 v243, a144\n" \
"v_mfma_f32_16x16x32_bf16 v[96:99], a[64:67], a[8:11], v[96:99]\n" \
"ds_read_b64_tr_b16 a[114:115], v232 offset:52736\n" \
"v_accvgpr_read_b32 v252, a145\n" \
"v_mfma_f32_16x16x32_bf16 v[96:99], a[68:71], a[12:15], v[96:99]\n" \
"buffer_load_dwordx4 v227, s[48:51], s77 offen offset:3072 lds\n" \
"v_mfma_f32_16x16x32_bf16 v[128:131], a[64:67], a[16:19], v[128:131]\n" \
"ds_read_b64_tr_b16 a[116:117], v232 offset:58880\n" \
"v_accvgpr_read_b32 v253, a146\n" \
"v_mfma_f32_16x16x32_bf16 v[128:131], a[68:71], a[20:23], v[128:131]\n" \
"ds_read_b64_tr_b16 a[118:119], v232 offset:60928\n" \
"v_accvgpr_read_b32 v254, a147\n" \
"v_mfma_f32_16x16x32_bf16 v[132:135], a[72:75], a[16:19], v[132:135]\n" \
"s_xor_b32 s77, s77, 0x180\n" \
"ds_read_b64_tr_b16 a[120:121], v233 offset:50688\n" \
"v_mul_f32 v243, s31, v243\n" \
"v_mfma_f32_16x16x32_bf16 v[132:135], a[76:79], a[20:23], v[132:135]\n" \
"v_mul_f32 v252, s31, v252\n" \
"v_mul_f32 v253, s31, v253\n" \
"v_mfma_f32_16x16x32_bf16 v[136:139], a[80:83], a[16:19], v[136:139]\n" \
"ds_read_b64_tr_b16 a[122:123], v233 offset:52736\n" \
"v_mul_f32 v254, s31, v254\n" \
"v_mfma_f32_16x16x32_bf16 v[136:139], a[84:87], a[20:23], v[136:139]\n" \
"ds_read_b64_tr_b16 a[124:125], v233 offset:58880\n" \
"v_cvt_pk_bf16_f32 v6, v243, v252\n" \
"v_mfma_f32_16x16x32_bf16 v[140:143], a[88:91], a[16:19], v[140:143]\n" \
"ds_read_b64_tr_b16 a[126:127], v233 offset:60928\n" \
"v_cvt_pk_bf16_f32 v7, v253, v254\n" \
"v_mfma_f32_16x16x32_bf16 v[140:143], a[92:95], a[20:23], v[140:143]\n" \
"buffer_store_dwordx4 v[220:223], v236, s[56:59], s93 offen offset:2304 sc1\n" \
"v_mfma_f32_16x16x32_bf16 v[172:175], a[88:91], a[24:27], v[172:175]\n" \
"v_accvgpr_read_b32 v243, a132\n" \
"v_accvgpr_read_b32 v252, a133\n" \
"v_mfma_f32_16x16x32_bf16 v[172:175], a[92:95], a[28:31], v[172:175]\n" \
"v_accvgpr_read_b32 v253, a134\n" \
"v_accvgpr_read_b32 v254, a135\n" \
"v_mfma_f32_16x16x32_bf16 v[168:171], a[80:83], a[24:27], v[168:171]\n" \
"v_mul_f32 v243, s31, v243\n" \
"v_mul_f32 v252, s31, v252\n" \
"v_mfma_f32_16x16x32_bf16 v[168:171], a[84:87], a[28:31], v[168:171]\n" \
"v_mul_f32 v253, s31, v253\n" \
"v_mul_f32 v254, s31, v254\n" \
"v_mfma_f32_16x16x32_bf16 v[164:167], a[72:75], a[24:27], v[164:167]\n" \
"v_cvt_pk_bf16_f32 v20, v243, v252\n" \
"v_cvt_pk_bf16_f32 v21, v253, v254\n" \
"v_mfma_f32_16x16x32_bf16 v[164:167], a[76:79], a[28:31], v[164:167]\n" \
"v_accvgpr_read_b32 v243, a148\n" \
"v_accvgpr_read_b32 v252, a149\n" \
"v_mfma_f32_16x16x32_bf16 v[160:163], a[64:67], a[24:27], v[160:163]\n" \
"v_accvgpr_read_b32 v253, a150\n" \
"v_accvgpr_read_b32 v254, a151\n" \
"v_mfma_f32_16x16x32_bf16 v[160:163], a[68:71], a[28:31], v[160:163]\n" \
"s_waitcnt vmcnt(35) lgkmcnt(0)\n" \
"s_barrier\n" \
"v_mfma_f32_16x16x32_bf16 v[80:83], a[96:99], a[0:3], v[80:83]\n" \
"ds_read_b64_tr_b16 a[64:65], v232 offset:0\n" \
"v_mul_f32 v243, s31, v243\n" \
"v_mfma_f32_16x16x32_bf16 v[80:83], a[100:103], a[4:7], v[80:83]\n" \
"ds_read_b64_tr_b16 a[66:67], v232 offset:2048\n" \
"v_mul_f32 v252, s31, v252\n" \
"v_mfma_f32_16x16x32_bf16 v[84:87], a[104:107], a[0:3], v[84:87]\n" \
"s_add_u32 m0, s79, 49152\n" \
"ds_read_b64_tr_b16 a[68:69], v232 offset:8192\n" \
"v_mul_f32 v253, s31, v253\n" \
"v_mfma_f32_16x16x32_bf16 v[84:87], a[108:111], a[4:7], v[84:87]\n" \
"buffer_load_dwordx4 v228, s[52:55], s78 offen offset:0 lds\n" \
"v_mfma_f32_16x16x32_bf16 v[88:91], a[112:115], a[0:3], v[88:91]\n" \
"ds_read_b64_tr_b16 a[70:71], v232 offset:10240\n" \
"v_mul_f32 v254, s31, v254\n" \
"v_mfma_f32_16x16x32_bf16 v[88:91], a[116:119], a[4:7], v[88:91]\n" \
"ds_read_b64_tr_b16 a[72:73], v233 offset:0\n" \
"v_cvt_pk_bf16_f32 v22, v243, v252\n" \
"v_mfma_f32_16x16x32_bf16 v[92:95], a[120:123], a[0:3], v[92:95]\n" \
"ds_read_b64_tr_b16 a[74:75], v233 offset:2048\n" \
"v_cvt_pk_bf16_f32 v23, v253, v254\n" \
"v_mfma_f32_16x16x32_bf16 v[92:95], a[124:127], a[4:7], v[92:95]\n" \
"buffer_load_dwordx4 v229, s[52:55], s78 offen offset:1024 lds\n" \
"v_mfma_f32_16x16x32_bf16 v[124:127], a[120:123], a[8:11], v[124:127]\n" \
"s_waitcnt vmcnt(35)\n" \
"ds_read_b128 a[0:3], v230 offset:32768\n" \
"v_accvgpr_read_b32 v243, a136\n" \
"v_mfma_f32_16x16x32_bf16 v[124:127], a[124:127], a[12:15], v[124:127]\n" \
"ds_read_b128 a[4:7], v231 offset:32768\n" \
"v_accvgpr_read_b32 v252, a137\n" \
"v_mfma_f32_16x16x32_bf16 v[120:123], a[112:115], a[8:11], v[120:123]\n" \
"s_xor_b32 s78, s78, 0x300000\n" \
"ds_read_b64_tr_b16 a[76:77], v233 offset:8192\n" \
"v_accvgpr_read_b32 v253, a138\n" \
"v_mfma_f32_16x16x32_bf16 v[120:123], a[116:119], a[12:15], v[120:123]\n" \
"buffer_load_dwordx4 v228, s[52:55], s89 offen offset:2048 lds\n" \
"v_mfma_f32_16x16x32_bf16 v[116:119], a[104:107], a[8:11], v[116:119]\n" \
"ds_read_b64_tr_b16 a[78:79], v233 offset:10240\n" \
"v_accvgpr_read_b32 v254, a139\n" \
"v_mfma_f32_16x16x32_bf16 v[116:119], a[108:111], a[12:15], v[116:119]\n" \
"ds_read_b64_tr_b16 a[80:81], v232 offset:512\n" \
"v_mul_f32 v243, s31, v243\n" \
"v_mfma_f32_16x16x32_bf16 v[112:115], a[96:99], a[8:11], v[112:115]\n" \
"ds_read_b64_tr_b16 a[82:83], v232 offset:2560\n" \
"v_mul_f32 v252, s31, v252\n" \
"v_mfma_f32_16x16x32_bf16 v[112:115], a[100:103], a[12:15], v[112:115]\n" \
"buffer_load_dwordx4 v229, s[52:55], s89 offen offset:3072 lds\n" \
"v_mfma_f32_16x16x32_bf16 v[144:147], a[96:99], a[16:19], v[144:147]\n" \
"s_waitcnt vmcnt(35)\n" \
"s_xor_b32 s89, s89, 0x300000\n" \
"ds_read_b128 a[8:11], v230 offset:34816\n" \
"v_mfma_f32_16x16x32_bf16 v[144:147], a[100:103], a[20:23], v[144:147]\n" \
"s_add_u32 m0, s74, 98304\n" \
"ds_read_b128 a[12:15], v231 offset:34816\n" \
"v_mul_f32 v253, s31, v253\n" \
"v_mfma_f32_16x16x32_bf16 v[148:151], a[104:107], a[16:19], v[148:151]\n" \
"s_waitcnt lgkmcnt(6)\n" \
"ds_read_b64_tr_b16 a[84:85], v232 offset:8704\n" \
"v_mul_f32 v254, s31, v254\n" \
"v_mfma_f32_16x16x32_bf16 v[148:151], a[108:111], a[20:23], v[148:151]\n" \
"buffer_load_dwordx4 v224, s[48:51], s75 offen offset:0 lds\n" \
"v_mfma_f32_16x16x32_bf16 v[152:155], a[112:115], a[16:19], v[152:155]\n" \
"ds_read_b64_tr_b16 a[86:87], v232 offset:10752\n" \
"v_cvt_pk_bf16_f32 v12, v243, v252\n" \
"v_mfma_f32_16x16x32_bf16 v[152:155], a[116:119], a[20:23], v[152:155]\n" \
"ds_read_b64_tr_b16 a[88:89], v233 offset:512\n" \
"v_cvt_pk_bf16_f32 v13, v253, v254\n" \
"v_mfma_f32_16x16x32_bf16 v[156:159], a[120:123], a[16:19], v[156:159]\n" \
"ds_read_b64_tr_b16 a[90:91], v233 offset:2560\n" \
"v_accvgpr_read_b32 v243, a152\n" \
"v_mfma_f32_16x16x32_bf16 v[156:159], a[124:127], a[20:23], v[156:159]\n" \
"buffer_load_dwordx4 v225, s[48:51], s75 offen offset:1024 lds\n" \
"v_mfma_f32_16x16x32_bf16 v[188:191], a[120:123], a[24:27], v[188:191]\n" \
"s_waitcnt vmcnt(35)\n" \
"ds_read_b128 a[16:19], v230 offset:36864\n" \
"v_accvgpr_read_b32 v252, a153\n" \
"v_mfma_f32_16x16x32_bf16 v[188:191], a[124:127], a[28:31], v[188:191]\n" \
"ds_read_b128 a[20:23], v231 offset:36864\n" \
"v_accvgpr_read_b32 v253, a154\n" \
"v_mfma_f32_16x16x32_bf16 v[184:187], a[112:115], a[24:27], v[184:187]\n" \
"s_waitcnt lgkmcnt(6)\n" \
"ds_read_b64_tr_b16 a[92:93], v233 offset:8704\n" \
"v_accvgpr_read_b32 v254, a155\n" \
"v_mfma_f32_16x16x32_bf16 v[184:187], a[116:119], a[28:31], v[184:187]\n" \
"buffer_load_dwordx4 v226, s[48:51], s75 offen offset:2048 lds\n" \
"v_mfma_f32_16x16x32_bf16 v[180:183], a[104:107], a[24:27], v[180:183]\n" \
"ds_read_b64_tr_b16 a[94:95], v233 offset:10752\n" \
"v_mul_f32 v243, s31, v243\n" \
"v_mfma_f32_16x16x32_bf16 v[180:183], a[108:111], a[28:31], v[180:183]\n" \
"v_mul_f32 v252, s31, v252\n" \
"v_mul_f32 v253, s31, v253\n" \
"v_mfma_f32_16x16x32_bf16 v[176:179], a[96:99], a[24:27], v[176:179]\n" \
"v_mul_f32 v254, s31, v254\n" \
"v_cvt_pk_bf16_f32 v14, v243, v252\n" \
"v_mfma_f32_16x16x32_bf16 v[176:179], a[100:103], a[28:31], v[176:179]\n" \
"buffer_load_dwordx4 v227, s[48:51], s75 offen offset:3072 lds\n" \
"v_mfma_f32_16x16x32_bf16 v[64:67], a[64:67], a[0:3], v[64:67]\n" \
"s_waitcnt vmcnt(35)\n" \
"s_xor_b32 s75, s75, 0x300\n" \
"ds_read_b128 a[24:27], v230 offset:38912\n" \
"v_mfma_f32_16x16x32_bf16 v[64:67], a[68:71], a[4:7], v[64:67]\n" \
"s_add_u32 m0, s74, 102400\n" \
"ds_read_b128 a[28:31], v231 offset:38912\n" \
"v_cvt_pk_bf16_f32 v15, v253, v254\n" \
"v_mfma_f32_16x16x32_bf16 v[68:71], a[72:75], a[0:3], v[68:71]\n" \
"s_waitcnt lgkmcnt(4)\n" \
"ds_read_b64_tr_b16 a[96:97], v232 offset:1024\n" \
"v_accvgpr_read_b32 v243, a140\n" \
"v_mfma_f32_16x16x32_bf16 v[68:71], a[76:79], a[4:7], v[68:71]\n" \
"buffer_load_dwordx4 v224, s[48:51], s77 offen offset:0 lds\n" \
"v_mfma_f32_16x16x32_bf16 v[72:75], a[80:83], a[0:3], v[72:75]\n" \
"ds_read_b64_tr_b16 a[98:99], v232 offset:3072\n" \
"v_accvgpr_read_b32 v252, a141\n" \
"v_mfma_f32_16x16x32_bf16 v[72:75], a[84:87], a[4:7], v[72:75]\n" \
"s_waitcnt lgkmcnt(3)\n" \
"ds_read_b64_tr_b16 a[100:101], v232 offset:9216\n" \
"v_accvgpr_read_b32 v253, a142\n" \
"v_mfma_f32_16x16x32_bf16 v[76:79], a[88:91], a[0:3], v[76:79]\n" \
"ds_read_b64_tr_b16 a[102:103], v232 offset:11264\n" \
"v_accvgpr_read_b32 v254, a143\n" \
"v_mfma_f32_16x16x32_bf16 v[76:79], a[92:95], a[4:7], v[76:79]\n" \
"buffer_load_dwordx4 v225, s[48:51], s77 offen offset:1024 lds\n" \
"v_mfma_f32_16x16x32_bf16 v[108:111], a[88:91], a[8:11], v[108:111]\n" \
"ds_read_b64_tr_b16 a[104:105], v233 offset:1024\n" \
"v_mul_f32 v243, s31, v243\n" \
"v_mfma_f32_16x16x32_bf16 v[108:111], a[92:95], a[12:15], v[108:111]\n" \
"ds_read_b64_tr_b16 a[106:107], v233 offset:3072\n" \
"v_mul_f32 v252, s31, v252\n" \
"v_mfma_f32_16x16x32_bf16 v[104:107], a[80:83], a[8:11], v[104:107]\n" \
"s_waitcnt lgkmcnt(6)\n" \
"ds_read_b64_tr_b16 a[108:109], v233 offset:9216\n" \
"v_mul_f32 v253, s31, v253\n" \
"v_mfma_f32_16x16x32_bf16 v[104:107], a[84:87], a[12:15], v[104:107]\n" \
"buffer_load_dwordx4 v226, s[48:51], s77 offen offset:2048 lds\n" \
"v_mfma_f32_16x16x32_bf16 v[100:103], a[72:75], a[8:11], v[100:103]\n" \
"ds_read_b64_tr_b16 a[110:111], v233 offset:11264\n" \
"v_mul_f32 v254, s31, v254\n" \
"v_mfma_f32_16x16x32_bf16 v[100:103], a[76:79], a[12:15], v[100:103]\n" \
"ds_read_b64_tr_b16 a[112:113], v232 offset:1536\n" \
"v_cvt_pk_bf16_f32 v28, v243, v252\n" \
"v_mfma_f32_16x16x32_bf16 v[96:99], a[64:67], a[8:11], v[96:99]\n" \
"ds_read_b64_tr_b16 a[114:115], v232 offset:3584\n" \
"v_cvt_pk_bf16_f32 v29, v253, v254\n" \
"v_mfma_f32_16x16x32_bf16 v[96:99], a[68:71], a[12:15], v[96:99]\n" \
"buffer_load_dwordx4 v227, s[48:51], s77 offen offset:3072 lds\n" \
"v_mfma_f32_16x16x32_bf16 v[128:131], a[64:67], a[16:19], v[128:131]\n" \
"ds_read_b64_tr_b16 a[116:117], v232 offset:9728\n" \
"v_accvgpr_read_b32 v243, a156\n" \
"v_mfma_f32_16x16x32_bf16 v[128:131], a[68:71], a[20:23], v[128:131]\n" \
"ds_read_b64_tr_b16 a[118:119], v232 offset:11776\n" \
"v_accvgpr_read_b32 v252, a157\n" \
"v_mfma_f32_16x16x32_bf16 v[132:135], a[72:75], a[16:19], v[132:135]\n" \
"s_xor_b32 s77, s77, 0x300\n" \
"ds_read_b64_tr_b16 a[120:121], v233 offset:1536\n" \
"v_accvgpr_read_b32 v253, a158\n" \
"v_mfma_f32_16x16x32_bf16 v[132:135], a[76:79], a[20:23], v[132:135]\n" \
"v_accvgpr_read_b32 v254, a159\n" \
"v_mul_f32 v243, s31, v243\n" \
"v_mfma_f32_16x16x32_bf16 v[136:139], a[80:83], a[16:19], v[136:139]\n" \
"ds_read_b64_tr_b16 a[122:123], v233 offset:3584\n" \
"v_mul_f32 v252, s31, v252\n" \
"v_mfma_f32_16x16x32_bf16 v[136:139], a[84:87], a[20:23], v[136:139]\n" \
"ds_read_b64_tr_b16 a[124:125], v233 offset:9728\n" \
"v_mul_f32 v253, s31, v253\n" \
"v_mfma_f32_16x16x32_bf16 v[140:143], a[88:91], a[16:19], v[140:143]\n" \
"s_add_u32 s93, s87, 17858560\n" \
"ds_read_b64_tr_b16 a[126:127], v233 offset:11776\n" \
"v_mul_f32 v254, s31, v254\n" \
"v_mfma_f32_16x16x32_bf16 v[140:143], a[92:95], a[20:23], v[140:143]\n" \
"buffer_store_dwordx4 a[224:227], v234, s[56:59], s93 offen offset:0 sc1\n" \
"v_mfma_f32_16x16x32_bf16 v[172:175], a[88:91], a[24:27], v[172:175]\n" \
"v_cvt_pk_bf16_f32 v30, v243, v252\n" \
"v_cvt_pk_bf16_f32 v31, v253, v254\n" \
"v_mfma_f32_16x16x32_bf16 v[172:175], a[92:95], a[28:31], v[172:175]\n" \
"v_accvgpr_write_b32 a56, v244\n" \
"v_accvgpr_write_b32 a57, v245\n" \
"v_mfma_f32_16x16x32_bf16 v[168:171], a[80:83], a[24:27], v[168:171]\n" \
"v_accvgpr_write_b32 a58, v246\n" \
"v_accvgpr_write_b32 a59, v247\n" \
"v_mfma_f32_16x16x32_bf16 v[168:171], a[84:87], a[28:31], v[168:171]\n" \
"v_accvgpr_write_b32 a60, v248\n" \
"v_accvgpr_write_b32 a61, v249\n" \
"v_mfma_f32_16x16x32_bf16 v[164:167], a[72:75], a[24:27], v[164:167]\n" \
"v_accvgpr_write_b32 a48, 0\n" \
"v_accvgpr_write_b32 a49, 0\n" \
"v_mfma_f32_16x16x32_bf16 v[164:167], a[76:79], a[28:31], v[164:167]\n" \
"v_accvgpr_write_b32 a50, 0\n" \
"v_accvgpr_write_b32 a51, 0\n" \
"v_mfma_f32_16x16x32_bf16 v[160:163], a[64:67], a[24:27], v[160:163]\n" \
"v_accvgpr_write_b32 a52, 0\n" \
"v_accvgpr_write_b32 a53, 0\n" \
"v_mfma_f32_16x16x32_bf16 v[160:163], a[68:71], a[28:31], v[160:163]\n" \
"s_waitcnt vmcnt(35) lgkmcnt(0)\n" \
"s_barrier\n" \
"v_mfma_f32_16x16x32_bf16 v[80:83], a[96:99], a[0:3], v[80:83]\n" \
"ds_read_b64_tr_b16 a[64:65], v232 offset:16384\n" \
"v_mfma_f32_16x16x32_bf16 v[80:83], a[100:103], a[4:7], v[80:83]\n" \
"ds_read_b64_tr_b16 a[66:67], v232 offset:18432\n" \
"v_mfma_f32_16x16x32_bf16 v[84:87], a[104:107], a[0:3], v[84:87]\n" \
"s_add_u32 m0, s79, 0\n" \
"ds_read_b64_tr_b16 a[68:69], v232 offset:24576\n" \
"v_mfma_f32_16x16x32_bf16 v[84:87], a[108:111], a[4:7], v[84:87]\n" \
"buffer_load_dwordx4 v228, s[52:55], s78 offen offset:0 lds\n" \
"v_mfma_f32_16x16x32_bf16 v[88:91], a[112:115], a[0:3], v[88:91]\n" \
"ds_read_b64_tr_b16 a[70:71], v232 offset:26624\n" \
"v_mfma_f32_16x16x32_bf16 v[88:91], a[116:119], a[4:7], v[88:91]\n" \
"ds_read_b64_tr_b16 a[72:73], v233 offset:16384\n" \
"v_mfma_f32_16x16x32_bf16 v[92:95], a[120:123], a[0:3], v[92:95]\n" \
"ds_read_b64_tr_b16 a[74:75], v233 offset:18432\n" \
"v_mfma_f32_16x16x32_bf16 v[92:95], a[124:127], a[4:7], v[92:95]\n" \
"buffer_load_dwordx4 v229, s[52:55], s78 offen offset:1024 lds\n" \
"v_mfma_f32_16x16x32_bf16 v[124:127], a[120:123], a[8:11], v[124:127]\n" \
"s_waitcnt vmcnt(35)\n" \
"ds_read_b128 a[0:3], v250 offset:0\n" \
"v_mfma_f32_16x16x32_bf16 v[124:127], a[124:127], a[12:15], v[124:127]\n" \
"ds_read_b128 a[4:7], v251 offset:0\n" \
"v_mfma_f32_16x16x32_bf16 v[120:123], a[112:115], a[8:11], v[120:123]\n" \
"s_xor_b32 s78, s78, 0x80000\n" \
"ds_read_b64_tr_b16 a[76:77], v233 offset:24576\n" \
"v_mfma_f32_16x16x32_bf16 v[120:123], a[116:119], a[12:15], v[120:123]\n" \
"buffer_load_dwordx4 v228, s[52:55], s89 offen offset:2048 lds\n" \
"v_mfma_f32_16x16x32_bf16 v[116:119], a[104:107], a[8:11], v[116:119]\n" \
"ds_read_b64_tr_b16 a[78:79], v233 offset:26624\n" \
"v_mfma_f32_16x16x32_bf16 v[116:119], a[108:111], a[12:15], v[116:119]\n" \
"ds_read_b64_tr_b16 a[80:81], v232 offset:16896\n" \
"v_mfma_f32_16x16x32_bf16 v[112:115], a[96:99], a[8:11], v[112:115]\n" \
"ds_read_b64_tr_b16 a[82:83], v232 offset:18944\n" \
"v_mfma_f32_16x16x32_bf16 v[112:115], a[100:103], a[12:15], v[112:115]\n" \
"buffer_load_dwordx4 v229, s[52:55], s89 offen offset:3072 lds\n" \
"v_mfma_f32_16x16x32_bf16 v[144:147], a[96:99], a[16:19], v[144:147]\n" \
"s_waitcnt vmcnt(35)\n" \
"s_xor_b32 s89, s89, 0x80000\n" \
"ds_read_b128 a[8:11], v250 offset:2048\n" \
"v_mfma_f32_16x16x32_bf16 v[144:147], a[100:103], a[20:23], v[144:147]\n" \
"s_add_u32 m0, s74, 131072\n" \
"ds_read_b128 a[12:15], v251 offset:2048\n" \
"v_mfma_f32_16x16x32_bf16 v[148:151], a[104:107], a[16:19], v[148:151]\n" \
"s_waitcnt lgkmcnt(6)\n" \
"ds_read_b64_tr_b16 a[84:85], v232 offset:25088\n" \
"v_mfma_f32_16x16x32_bf16 v[148:151], a[108:111], a[20:23], v[148:151]\n" \
"buffer_load_dwordx4 v224, s[48:51], s75 offen offset:0 lds\n" \
"v_mfma_f32_16x16x32_bf16 v[152:155], a[112:115], a[16:19], v[152:155]\n" \
"ds_read_b64_tr_b16 a[86:87], v232 offset:27136\n" \
"v_mfma_f32_16x16x32_bf16 v[152:155], a[116:119], a[20:23], v[152:155]\n" \
"ds_read_b64_tr_b16 a[88:89], v233 offset:16896\n" \
"v_mfma_f32_16x16x32_bf16 v[156:159], a[120:123], a[16:19], v[156:159]\n" \
"ds_read_b64_tr_b16 a[90:91], v233 offset:18944\n" \
"v_mfma_f32_16x16x32_bf16 v[156:159], a[124:127], a[20:23], v[156:159]\n" \
"buffer_load_dwordx4 v225, s[48:51], s75 offen offset:1024 lds\n" \
"v_mfma_f32_16x16x32_bf16 v[188:191], a[120:123], a[24:27], v[188:191]\n" \
"s_waitcnt vmcnt(35)\n" \
"ds_read_b128 a[16:19], v250 offset:4096\n" \
"v_mfma_f32_16x16x32_bf16 v[188:191], a[124:127], a[28:31], v[188:191]\n" \
"ds_read_b128 a[20:23], v251 offset:4096\n" \
"v_mfma_f32_16x16x32_bf16 v[184:187], a[112:115], a[24:27], v[184:187]\n" \
"s_waitcnt lgkmcnt(6)\n" \
"ds_read_b64_tr_b16 a[92:93], v233 offset:25088\n" \
"v_mfma_f32_16x16x32_bf16 v[184:187], a[116:119], a[28:31], v[184:187]\n" \
"buffer_load_dwordx4 v226, s[48:51], s75 offen offset:2048 lds\n" \
"v_mfma_f32_16x16x32_bf16 v[180:183], a[104:107], a[24:27], v[180:183]\n" \
"ds_read_b64_tr_b16 a[94:95], v233 offset:27136\n" \
"v_mfma_f32_16x16x32_bf16 v[180:183], a[108:111], a[28:31], v[180:183]\n" \
"v_mfma_f32_16x16x32_bf16 v[176:179], a[96:99], a[24:27], v[176:179]\n" \
"v_mfma_f32_16x16x32_bf16 v[176:179], a[100:103], a[28:31], v[176:179]\n" \
"buffer_load_dwordx4 v227, s[48:51], s75 offen offset:3072 lds\n" \
"v_mfma_f32_16x16x32_bf16 v[64:67], a[64:67], a[0:3], v[64:67]\n" \
"s_waitcnt vmcnt(35)\n" \
"s_xor_b32 s75, s75, 0x80\n" \
"ds_read_b128 a[24:27], v250 offset:6144\n" \
"v_mfma_f32_16x16x32_bf16 v[64:67], a[68:71], a[4:7], v[64:67]\n" \
"s_add_u32 m0, s74, 135168\n" \
"ds_read_b128 a[28:31], v251 offset:6144\n" \
"v_mfma_f32_16x16x32_bf16 v[68:71], a[72:75], a[0:3], v[68:71]\n" \
"s_waitcnt lgkmcnt(2)\n" \
"ds_read_b64_tr_b16 a[96:97], v232 offset:17408\n" \
"v_mfma_f32_16x16x32_bf16 v[68:71], a[76:79], a[4:7], v[68:71]\n" \
"buffer_load_dwordx4 v224, s[48:51], s77 offen offset:0 lds\n" \
"v_mfma_f32_16x16x32_bf16 v[72:75], a[80:83], a[0:3], v[72:75]\n" \
"ds_read_b64_tr_b16 a[98:99], v232 offset:19456\n" \
"v_mfma_f32_16x16x32_bf16 v[72:75], a[84:87], a[4:7], v[72:75]\n" \
"ds_read_b64_tr_b16 a[100:101], v232 offset:25600\n" \
"v_mfma_f32_16x16x32_bf16 v[76:79], a[88:91], a[0:3], v[76:79]\n" \
"ds_read_b64_tr_b16 a[102:103], v232 offset:27648\n" \
"v_mfma_f32_16x16x32_bf16 v[76:79], a[92:95], a[4:7], v[76:79]\n" \
"buffer_load_dwordx4 v225, s[48:51], s77 offen offset:1024 lds\n" \
"v_mfma_f32_16x16x32_bf16 v[108:111], a[88:91], a[8:11], v[108:111]\n" \
"ds_read_b64_tr_b16 a[104:105], v233 offset:17408\n" \
"v_mfma_f32_16x16x32_bf16 v[108:111], a[92:95], a[12:15], v[108:111]\n" \
"ds_read_b64_tr_b16 a[106:107], v233 offset:19456\n" \
"v_mfma_f32_16x16x32_bf16 v[104:107], a[80:83], a[8:11], v[104:107]\n" \
"s_waitcnt lgkmcnt(6)\n" \
"ds_read_b64_tr_b16 a[108:109], v233 offset:25600\n" \
"v_mfma_f32_16x16x32_bf16 v[104:107], a[84:87], a[12:15], v[104:107]\n" \
"buffer_load_dwordx4 v226, s[48:51], s77 offen offset:2048 lds\n" \
"v_mfma_f32_16x16x32_bf16 v[100:103], a[72:75], a[8:11], v[100:103]\n" \
"ds_read_b64_tr_b16 a[110:111], v233 offset:27648\n" \
"v_mfma_f32_16x16x32_bf16 v[100:103], a[76:79], a[12:15], v[100:103]\n" \
"ds_read_b64_tr_b16 a[112:113], v232 offset:17920\n" \
"v_mfma_f32_16x16x32_bf16 v[96:99], a[64:67], a[8:11], v[96:99]\n" \
"ds_read_b64_tr_b16 a[114:115], v232 offset:19968\n" \
"Lpg3897_%=:\n" \
"s_cbranch_execz Lpg6052_%=\n" \
"v_mfma_f32_16x16x32_bf16 v[96:99], a[68:71], a[12:15], v[96:99]\n" \
"buffer_load_dwordx4 v227, s[48:51], s77 offen offset:3072 lds\n" \
"v_mfma_f32_16x16x32_bf16 v[128:131], a[64:67], a[16:19], v[128:131]\n" \
"ds_read_b64_tr_b16 a[116:117], v232 offset:26112\n" \
"v_mfma_f32_16x16x32_bf16 v[128:131], a[68:71], a[20:23], v[128:131]\n" \
"ds_read_b64_tr_b16 a[118:119], v232 offset:28160\n" \
"v_mfma_f32_16x16x32_bf16 v[132:135], a[72:75], a[16:19], v[132:135]\n" \
"s_xor_b32 s77, s77, 0x80\n" \
"ds_read_b64_tr_b16 a[120:121], v233 offset:17920\n" \
"v_mfma_f32_16x16x32_bf16 v[132:135], a[76:79], a[20:23], v[132:135]\n" \
"buffer_store_dwordx4 a[228:231], v234, s[56:59], s93 offen offset:1024 sc1\n" \
"v_mfma_f32_16x16x32_bf16 v[136:139], a[80:83], a[16:19], v[136:139]\n" \
"ds_read_b64_tr_b16 a[122:123], v233 offset:19968\n" \
"v_mfma_f32_16x16x32_bf16 v[136:139], a[84:87], a[20:23], v[136:139]\n" \
"ds_read_b64_tr_b16 a[124:125], v233 offset:26112\n" \
"v_mfma_f32_16x16x32_bf16 v[140:143], a[88:91], a[16:19], v[140:143]\n" \
"ds_read_b64_tr_b16 a[126:127], v233 offset:28160\n" \
"v_mfma_f32_16x16x32_bf16 v[140:143], a[92:95], a[20:23], v[140:143]\n" \
"v_mfma_f32_16x16x32_bf16 v[172:175], a[88:91], a[24:27], v[172:175]\n" \
"v_mfma_f32_16x16x32_bf16 v[172:175], a[92:95], a[28:31], v[172:175]\n" \
"v_mfma_f32_16x16x32_bf16 v[168:171], a[80:83], a[24:27], v[168:171]\n" \
"v_mfma_f32_16x16x32_bf16 v[168:171], a[84:87], a[28:31], v[168:171]\n" \
"buffer_store_dwordx4 a[232:235], v234, s[56:59], s93 offen offset:2048 sc1\n" \
"v_mfma_f32_16x16x32_bf16 v[164:167], a[72:75], a[24:27], v[164:167]\n" \
"v_mfma_f32_16x16x32_bf16 v[164:167], a[76:79], a[28:31], v[164:167]\n" \
"v_mfma_f32_16x16x32_bf16 v[160:163], a[64:67], a[24:27], v[160:163]\n" \
"v_mfma_f32_16x16x32_bf16 v[160:163], a[68:71], a[28:31], v[160:163]\n" \
"s_waitcnt vmcnt(36) lgkmcnt(0)\n" \
"s_barrier\n" \
"v_mfma_f32_16x16x32_bf16 v[80:83], a[96:99], a[0:3], v[80:83]\n" \
"ds_read_b64_tr_b16 a[64:65], v232 offset:32768\n" \
"v_mfma_f32_16x16x32_bf16 v[80:83], a[100:103], a[4:7], v[80:83]\n" \
"ds_read_b64_tr_b16 a[66:67], v232 offset:34816\n" \
"v_mfma_f32_16x16x32_bf16 v[84:87], a[104:107], a[0:3], v[84:87]\n" \
"s_add_u32 m0, s79, 16384\n" \
"ds_read_b64_tr_b16 a[68:69], v232 offset:40960\n" \
"v_mfma_f32_16x16x32_bf16 v[84:87], a[108:111], a[4:7], v[84:87]\n" \
"buffer_load_dwordx4 v228, s[52:55], s78 offen offset:0 lds\n" \
"v_mfma_f32_16x16x32_bf16 v[88:91], a[112:115], a[0:3], v[88:91]\n" \
"ds_read_b64_tr_b16 a[70:71], v232 offset:43008\n" \
"v_mfma_f32_16x16x32_bf16 v[88:91], a[116:119], a[4:7], v[88:91]\n" \
"ds_read_b64_tr_b16 a[72:73], v233 offset:32768\n" \
"v_mfma_f32_16x16x32_bf16 v[92:95], a[120:123], a[0:3], v[92:95]\n" \
"ds_read_b64_tr_b16 a[74:75], v233 offset:34816\n" \
"v_mfma_f32_16x16x32_bf16 v[92:95], a[124:127], a[4:7], v[92:95]\n" \
"buffer_load_dwordx4 v229, s[52:55], s78 offen offset:1024 lds\n" \
"v_mfma_f32_16x16x32_bf16 v[124:127], a[120:123], a[8:11], v[124:127]\n" \
"s_waitcnt vmcnt(36)\n" \
"ds_read_b128 a[0:3], v230 offset:0\n" \
"v_mfma_f32_16x16x32_bf16 v[124:127], a[124:127], a[12:15], v[124:127]\n" \
"ds_read_b128 a[4:7], v231 offset:0\n" \
"v_mfma_f32_16x16x32_bf16 v[120:123], a[112:115], a[8:11], v[120:123]\n" \
"s_xor_b32 s78, s78, 0x180000\n" \
"ds_read_b64_tr_b16 a[76:77], v233 offset:40960\n" \
"v_mfma_f32_16x16x32_bf16 v[120:123], a[116:119], a[12:15], v[120:123]\n" \
"buffer_load_dwordx4 v228, s[52:55], s89 offen offset:2048 lds\n" \
"v_mfma_f32_16x16x32_bf16 v[116:119], a[104:107], a[8:11], v[116:119]\n" \
"ds_read_b64_tr_b16 a[78:79], v233 offset:43008\n" \
"v_mfma_f32_16x16x32_bf16 v[116:119], a[108:111], a[12:15], v[116:119]\n" \
"ds_read_b64_tr_b16 a[80:81], v232 offset:33280\n" \
"v_mfma_f32_16x16x32_bf16 v[112:115], a[96:99], a[8:11], v[112:115]\n" \
"ds_read_b64_tr_b16 a[82:83], v232 offset:35328\n" \
"v_mfma_f32_16x16x32_bf16 v[112:115], a[100:103], a[12:15], v[112:115]\n" \
"buffer_load_dwordx4 v229, s[52:55], s89 offen offset:3072 lds\n" \
"v_mfma_f32_16x16x32_bf16 v[144:147], a[96:99], a[16:19], v[144:147]\n" \
"s_waitcnt vmcnt(36)\n" \
"s_xor_b32 s89, s89, 0x180000\n" \
"ds_read_b128 a[8:11], v230 offset:2048\n" \
"v_mfma_f32_16x16x32_bf16 v[144:147], a[100:103], a[20:23], v[144:147]\n" \
"s_add_u32 m0, s74, 65536\n" \
"ds_read_b128 a[12:15], v231 offset:2048\n" \
"v_mfma_f32_16x16x32_bf16 v[148:151], a[104:107], a[16:19], v[148:151]\n" \
"s_waitcnt lgkmcnt(6)\n" \
"ds_read_b64_tr_b16 a[84:85], v232 offset:41472\n" \
"v_mfma_f32_16x16x32_bf16 v[148:151], a[108:111], a[20:23], v[148:151]\n" \
"buffer_load_dwordx4 v224, s[48:51], s75 offen offset:0 lds\n" \
"v_mfma_f32_16x16x32_bf16 v[152:155], a[112:115], a[16:19], v[152:155]\n" \
"ds_read_b64_tr_b16 a[86:87], v232 offset:43520\n" \
"v_mfma_f32_16x16x32_bf16 v[152:155], a[116:119], a[20:23], v[152:155]\n" \
"ds_read_b64_tr_b16 a[88:89], v233 offset:33280\n" \
"v_mfma_f32_16x16x32_bf16 v[156:159], a[120:123], a[16:19], v[156:159]\n" \
"ds_read_b64_tr_b16 a[90:91], v233 offset:35328\n" \
"v_mfma_f32_16x16x32_bf16 v[156:159], a[124:127], a[20:23], v[156:159]\n" \
"buffer_load_dwordx4 v225, s[48:51], s75 offen offset:1024 lds\n" \
"v_mfma_f32_16x16x32_bf16 v[188:191], a[120:123], a[24:27], v[188:191]\n" \
"s_waitcnt vmcnt(36)\n" \
"ds_read_b128 a[16:19], v230 offset:4096\n" \
"v_mfma_f32_16x16x32_bf16 v[188:191], a[124:127], a[28:31], v[188:191]\n" \
"ds_read_b128 a[20:23], v231 offset:4096\n" \
"v_mfma_f32_16x16x32_bf16 v[184:187], a[112:115], a[24:27], v[184:187]\n" \
"s_waitcnt lgkmcnt(6)\n" \
"ds_read_b64_tr_b16 a[92:93], v233 offset:41472\n" \
"v_mfma_f32_16x16x32_bf16 v[184:187], a[116:119], a[28:31], v[184:187]\n" \
"buffer_load_dwordx4 v226, s[48:51], s75 offen offset:2048 lds\n" \
"v_mfma_f32_16x16x32_bf16 v[180:183], a[104:107], a[24:27], v[180:183]\n" \
"ds_read_b64_tr_b16 a[94:95], v233 offset:43520\n" \
"v_mfma_f32_16x16x32_bf16 v[180:183], a[108:111], a[28:31], v[180:183]\n" \
"v_mfma_f32_16x16x32_bf16 v[176:179], a[96:99], a[24:27], v[176:179]\n" \
"v_mfma_f32_16x16x32_bf16 v[176:179], a[100:103], a[28:31], v[176:179]\n" \
"buffer_load_dwordx4 v227, s[48:51], s75 offen offset:3072 lds\n" \
"v_mfma_f32_16x16x32_bf16 v[64:67], a[64:67], a[0:3], v[64:67]\n" \
"s_waitcnt vmcnt(36)\n" \
"s_xor_b32 s75, s75, 0x180\n" \
"ds_read_b128 a[24:27], v230 offset:6144\n" \
"v_mfma_f32_16x16x32_bf16 v[64:67], a[68:71], a[4:7], v[64:67]\n" \
"s_add_u32 m0, s74, 69632\n" \
"ds_read_b128 a[28:31], v231 offset:6144\n" \
"v_mfma_f32_16x16x32_bf16 v[68:71], a[72:75], a[0:3], v[68:71]\n" \
"s_waitcnt lgkmcnt(2)\n" \
"ds_read_b64_tr_b16 a[96:97], v232 offset:33792\n" \
"v_mfma_f32_16x16x32_bf16 v[68:71], a[76:79], a[4:7], v[68:71]\n" \
"buffer_load_dwordx4 v224, s[48:51], s77 offen offset:0 lds\n" \
"v_mfma_f32_16x16x32_bf16 v[72:75], a[80:83], a[0:3], v[72:75]\n" \
"ds_read_b64_tr_b16 a[98:99], v232 offset:35840\n" \
"v_mfma_f32_16x16x32_bf16 v[72:75], a[84:87], a[4:7], v[72:75]\n" \
"ds_read_b64_tr_b16 a[100:101], v232 offset:41984\n" \
"v_mfma_f32_16x16x32_bf16 v[76:79], a[88:91], a[0:3], v[76:79]\n" \
"ds_read_b64_tr_b16 a[102:103], v232 offset:44032\n" \
"v_mfma_f32_16x16x32_bf16 v[76:79], a[92:95], a[4:7], v[76:79]\n" \
"buffer_load_dwordx4 v225, s[48:51], s77 offen offset:1024 lds\n" \
"v_mfma_f32_16x16x32_bf16 v[108:111], a[88:91], a[8:11], v[108:111]\n" \
"ds_read_b64_tr_b16 a[104:105], v233 offset:33792\n" \
"v_mfma_f32_16x16x32_bf16 v[108:111], a[92:95], a[12:15], v[108:111]\n" \
"ds_read_b64_tr_b16 a[106:107], v233 offset:35840\n" \
"v_mfma_f32_16x16x32_bf16 v[104:107], a[80:83], a[8:11], v[104:107]\n" \
"s_waitcnt lgkmcnt(6)\n" \
"ds_read_b64_tr_b16 a[108:109], v233 offset:41984\n" \
"v_mfma_f32_16x16x32_bf16 v[104:107], a[84:87], a[12:15], v[104:107]\n" \
"buffer_load_dwordx4 v226, s[48:51], s77 offen offset:2048 lds\n" \
"v_mfma_f32_16x16x32_bf16 v[100:103], a[72:75], a[8:11], v[100:103]\n" \
"ds_read_b64_tr_b16 a[110:111], v233 offset:44032\n" \
"v_mfma_f32_16x16x32_bf16 v[100:103], a[76:79], a[12:15], v[100:103]\n" \
"ds_read_b64_tr_b16 a[112:113], v232 offset:34304\n" \
"v_mfma_f32_16x16x32_bf16 v[96:99], a[64:67], a[8:11], v[96:99]\n" \
"ds_read_b64_tr_b16 a[114:115], v232 offset:36352\n" \
"v_mfma_f32_16x16x32_bf16 v[96:99], a[68:71], a[12:15], v[96:99]\n" \
"buffer_load_dwordx4 v227, s[48:51], s77 offen offset:3072 lds\n" \
"v_mfma_f32_16x16x32_bf16 v[128:131], a[64:67], a[16:19], v[128:131]\n" \
"ds_read_b64_tr_b16 a[116:117], v232 offset:42496\n" \
"v_mfma_f32_16x16x32_bf16 v[128:131], a[68:71], a[20:23], v[128:131]\n" \
"ds_read_b64_tr_b16 a[118:119], v232 offset:44544\n" \
"v_mfma_f32_16x16x32_bf16 v[132:135], a[72:75], a[16:19], v[132:135]\n" \
"s_xor_b32 s77, s77, 0x180\n" \
"ds_read_b64_tr_b16 a[120:121], v233 offset:34304\n" \
"v_mfma_f32_16x16x32_bf16 v[132:135], a[76:79], a[20:23], v[132:135]\n" \
"buffer_store_dwordx4 a[236:239], v234, s[56:59], s93 offen offset:3072 sc1\n" \
"v_mfma_f32_16x16x32_bf16 v[136:139], a[80:83], a[16:19], v[136:139]\n" \
"ds_read_b64_tr_b16 a[122:123], v233 offset:36352\n" \
"v_mfma_f32_16x16x32_bf16 v[136:139], a[84:87], a[20:23], v[136:139]\n" \
"ds_read_b64_tr_b16 a[124:125], v233 offset:42496\n" \
"v_mfma_f32_16x16x32_bf16 v[140:143], a[88:91], a[16:19], v[140:143]\n" \
"ds_read_b64_tr_b16 a[126:127], v233 offset:44544\n" \
"v_mfma_f32_16x16x32_bf16 v[140:143], a[92:95], a[20:23], v[140:143]\n" \
"v_mfma_f32_16x16x32_bf16 v[172:175], a[88:91], a[24:27], v[172:175]\n" \
"v_mfma_f32_16x16x32_bf16 v[172:175], a[92:95], a[28:31], v[172:175]\n" \
"v_mfma_f32_16x16x32_bf16 v[168:171], a[80:83], a[24:27], v[168:171]\n" \
"s_add_u32 s93, s87, 17850368\n" \
"v_mfma_f32_16x16x32_bf16 v[168:171], a[84:87], a[28:31], v[168:171]\n" \
"buffer_store_dwordx4 a[192:195], v234, s[56:59], s93 offen offset:0 sc1\n" \
"v_mfma_f32_16x16x32_bf16 v[164:167], a[72:75], a[24:27], v[164:167]\n" \
"v_mfma_f32_16x16x32_bf16 v[164:167], a[76:79], a[28:31], v[164:167]\n" \
"v_mfma_f32_16x16x32_bf16 v[160:163], a[64:67], a[24:27], v[160:163]\n" \
"v_mfma_f32_16x16x32_bf16 v[160:163], a[68:71], a[28:31], v[160:163]\n" \
"s_waitcnt vmcnt(37) lgkmcnt(0)\n" \
"s_barrier\n" \
"v_mfma_f32_16x16x32_bf16 v[80:83], a[96:99], a[0:3], v[80:83]\n" \
"ds_read_b64_tr_b16 a[64:65], v232 offset:49152\n" \
"v_mfma_f32_16x16x32_bf16 v[80:83], a[100:103], a[4:7], v[80:83]\n" \
"ds_read_b64_tr_b16 a[66:67], v232 offset:51200\n" \
"v_mfma_f32_16x16x32_bf16 v[84:87], a[104:107], a[0:3], v[84:87]\n" \
"s_add_u32 m0, s79, 32768\n" \
"ds_read_b64_tr_b16 a[68:69], v232 offset:57344\n" \
"v_mfma_f32_16x16x32_bf16 v[84:87], a[108:111], a[4:7], v[84:87]\n" \
"buffer_load_dwordx4 v228, s[52:55], s78 offen offset:0 lds\n" \
"v_mfma_f32_16x16x32_bf16 v[88:91], a[112:115], a[0:3], v[88:91]\n" \
"ds_read_b64_tr_b16 a[70:71], v232 offset:59392\n" \
"v_mfma_f32_16x16x32_bf16 v[88:91], a[116:119], a[4:7], v[88:91]\n" \
"ds_read_b64_tr_b16 a[72:73], v233 offset:49152\n" \
"v_mfma_f32_16x16x32_bf16 v[92:95], a[120:123], a[0:3], v[92:95]\n" \
"ds_read_b64_tr_b16 a[74:75], v233 offset:51200\n" \
"v_mfma_f32_16x16x32_bf16 v[92:95], a[124:127], a[4:7], v[92:95]\n" \
"buffer_load_dwordx4 v229, s[52:55], s78 offen offset:1024 lds\n" \
"v_mfma_f32_16x16x32_bf16 v[124:127], a[120:123], a[8:11], v[124:127]\n" \
"s_waitcnt vmcnt(37)\n" \
"ds_read_b128 a[0:3], v230 offset:32768\n" \
"v_mfma_f32_16x16x32_bf16 v[124:127], a[124:127], a[12:15], v[124:127]\n" \
"ds_read_b128 a[4:7], v231 offset:32768\n" \
"v_mfma_f32_16x16x32_bf16 v[120:123], a[112:115], a[8:11], v[120:123]\n" \
"s_xor_b32 s78, s78, 0x80000\n" \
"ds_read_b64_tr_b16 a[76:77], v233 offset:57344\n" \
"v_mfma_f32_16x16x32_bf16 v[120:123], a[116:119], a[12:15], v[120:123]\n" \
"buffer_load_dwordx4 v228, s[52:55], s89 offen offset:2048 lds\n" \
"v_mfma_f32_16x16x32_bf16 v[116:119], a[104:107], a[8:11], v[116:119]\n" \
"ds_read_b64_tr_b16 a[78:79], v233 offset:59392\n" \
"v_mfma_f32_16x16x32_bf16 v[116:119], a[108:111], a[12:15], v[116:119]\n" \
"ds_read_b64_tr_b16 a[80:81], v232 offset:49664\n" \
"v_mfma_f32_16x16x32_bf16 v[112:115], a[96:99], a[8:11], v[112:115]\n" \
"ds_read_b64_tr_b16 a[82:83], v232 offset:51712\n" \
"v_mfma_f32_16x16x32_bf16 v[112:115], a[100:103], a[12:15], v[112:115]\n" \
"buffer_load_dwordx4 v229, s[52:55], s89 offen offset:3072 lds\n" \
"v_mfma_f32_16x16x32_bf16 v[144:147], a[96:99], a[16:19], v[144:147]\n" \
"s_waitcnt vmcnt(37)\n" \
"s_xor_b32 s89, s89, 0x80000\n" \
"ds_read_b128 a[8:11], v230 offset:34816\n" \
"v_mfma_f32_16x16x32_bf16 v[144:147], a[100:103], a[20:23], v[144:147]\n" \
"s_add_u32 m0, s74, 98304\n" \
"ds_read_b128 a[12:15], v231 offset:34816\n" \
"v_mfma_f32_16x16x32_bf16 v[148:151], a[104:107], a[16:19], v[148:151]\n" \
"s_waitcnt lgkmcnt(6)\n" \
"ds_read_b64_tr_b16 a[84:85], v232 offset:57856\n" \
"v_mfma_f32_16x16x32_bf16 v[148:151], a[108:111], a[20:23], v[148:151]\n" \
"buffer_load_dwordx4 v224, s[48:51], s75 offen offset:0 lds\n" \
"v_mfma_f32_16x16x32_bf16 v[152:155], a[112:115], a[16:19], v[152:155]\n" \
"ds_read_b64_tr_b16 a[86:87], v232 offset:59904\n" \
"v_mfma_f32_16x16x32_bf16 v[152:155], a[116:119], a[20:23], v[152:155]\n" \
"ds_read_b64_tr_b16 a[88:89], v233 offset:49664\n" \
"v_mfma_f32_16x16x32_bf16 v[156:159], a[120:123], a[16:19], v[156:159]\n" \
"ds_read_b64_tr_b16 a[90:91], v233 offset:51712\n" \
"v_mfma_f32_16x16x32_bf16 v[156:159], a[124:127], a[20:23], v[156:159]\n" \
"buffer_load_dwordx4 v225, s[48:51], s75 offen offset:1024 lds\n" \
"v_mfma_f32_16x16x32_bf16 v[188:191], a[120:123], a[24:27], v[188:191]\n" \
"s_waitcnt vmcnt(37)\n" \
"ds_read_b128 a[16:19], v230 offset:36864\n" \
"v_mfma_f32_16x16x32_bf16 v[188:191], a[124:127], a[28:31], v[188:191]\n" \
"ds_read_b128 a[20:23], v231 offset:36864\n" \
"v_mfma_f32_16x16x32_bf16 v[184:187], a[112:115], a[24:27], v[184:187]\n" \
"s_waitcnt lgkmcnt(6)\n" \
"ds_read_b64_tr_b16 a[92:93], v233 offset:57856\n" \
"v_mfma_f32_16x16x32_bf16 v[184:187], a[116:119], a[28:31], v[184:187]\n" \
"buffer_load_dwordx4 v226, s[48:51], s75 offen offset:2048 lds\n" \
"v_mfma_f32_16x16x32_bf16 v[180:183], a[104:107], a[24:27], v[180:183]\n" \
"ds_read_b64_tr_b16 a[94:95], v233 offset:59904\n" \
"v_mfma_f32_16x16x32_bf16 v[180:183], a[108:111], a[28:31], v[180:183]\n" \
"v_mfma_f32_16x16x32_bf16 v[176:179], a[96:99], a[24:27], v[176:179]\n" \
"v_mfma_f32_16x16x32_bf16 v[176:179], a[100:103], a[28:31], v[176:179]\n" \
"buffer_load_dwordx4 v227, s[48:51], s75 offen offset:3072 lds\n" \
"v_mfma_f32_16x16x32_bf16 v[64:67], a[64:67], a[0:3], v[64:67]\n" \
"s_waitcnt vmcnt(37)\n" \
"s_xor_b32 s75, s75, 0x80\n" \
"ds_read_b128 a[24:27], v230 offset:38912\n" \
"v_mfma_f32_16x16x32_bf16 v[64:67], a[68:71], a[4:7], v[64:67]\n" \
"s_add_u32 m0, s74, 102400\n" \
"ds_read_b128 a[28:31], v231 offset:38912\n" \
"v_mfma_f32_16x16x32_bf16 v[68:71], a[72:75], a[0:3], v[68:71]\n" \
"s_waitcnt lgkmcnt(2)\n" \
"ds_read_b64_tr_b16 a[96:97], v232 offset:50176\n" \
"v_mfma_f32_16x16x32_bf16 v[68:71], a[76:79], a[4:7], v[68:71]\n" \
"buffer_load_dwordx4 v224, s[48:51], s77 offen offset:0 lds\n" \
"v_mfma_f32_16x16x32_bf16 v[72:75], a[80:83], a[0:3], v[72:75]\n" \
"ds_read_b64_tr_b16 a[98:99], v232 offset:52224\n" \
"v_mfma_f32_16x16x32_bf16 v[72:75], a[84:87], a[4:7], v[72:75]\n" \
"ds_read_b64_tr_b16 a[100:101], v232 offset:58368\n" \
"v_mfma_f32_16x16x32_bf16 v[76:79], a[88:91], a[0:3], v[76:79]\n" \
"ds_read_b64_tr_b16 a[102:103], v232 offset:60416\n" \
"v_mfma_f32_16x16x32_bf16 v[76:79], a[92:95], a[4:7], v[76:79]\n" \
"buffer_load_dwordx4 v225, s[48:51], s77 offen offset:1024 lds\n" \
"v_mfma_f32_16x16x32_bf16 v[108:111], a[88:91], a[8:11], v[108:111]\n" \
"ds_read_b64_tr_b16 a[104:105], v233 offset:50176\n" \
"v_mfma_f32_16x16x32_bf16 v[108:111], a[92:95], a[12:15], v[108:111]\n" \
"ds_read_b64_tr_b16 a[106:107], v233 offset:52224\n" \
"v_mfma_f32_16x16x32_bf16 v[104:107], a[80:83], a[8:11], v[104:107]\n" \
"s_waitcnt lgkmcnt(6)\n" \
"ds_read_b64_tr_b16 a[108:109], v233 offset:58368\n" \
"v_mfma_f32_16x16x32_bf16 v[104:107], a[84:87], a[12:15], v[104:107]\n" \
"buffer_load_dwordx4 v226, s[48:51], s77 offen offset:2048 lds\n" \
"v_mfma_f32_16x16x32_bf16 v[100:103], a[72:75], a[8:11], v[100:103]\n" \
"ds_read_b64_tr_b16 a[110:111], v233 offset:60416\n" \
"v_mfma_f32_16x16x32_bf16 v[100:103], a[76:79], a[12:15], v[100:103]\n" \
"ds_read_b64_tr_b16 a[112:113], v232 offset:50688\n" \
"v_mfma_f32_16x16x32_bf16 v[96:99], a[64:67], a[8:11], v[96:99]\n" \
"ds_read_b64_tr_b16 a[114:115], v232 offset:52736\n" \
"v_mfma_f32_16x16x32_bf16 v[96:99], a[68:71], a[12:15], v[96:99]\n" \
"buffer_load_dwordx4 v227, s[48:51], s77 offen offset:3072 lds\n" \
"v_mfma_f32_16x16x32_bf16 v[128:131], a[64:67], a[16:19], v[128:131]\n" \
"ds_read_b64_tr_b16 a[116:117], v232 offset:58880\n" \
"v_mfma_f32_16x16x32_bf16 v[128:131], a[68:71], a[20:23], v[128:131]\n" \
"ds_read_b64_tr_b16 a[118:119], v232 offset:60928\n" \
"v_mfma_f32_16x16x32_bf16 v[132:135], a[72:75], a[16:19], v[132:135]\n" \
"s_xor_b32 s77, s77, 0x80\n" \
"ds_read_b64_tr_b16 a[120:121], v233 offset:50688\n" \
"v_mfma_f32_16x16x32_bf16 v[132:135], a[76:79], a[20:23], v[132:135]\n" \
"buffer_store_dwordx4 a[196:199], v234, s[56:59], s93 offen offset:1024 sc1\n" \
"v_mfma_f32_16x16x32_bf16 v[136:139], a[80:83], a[16:19], v[136:139]\n" \
"ds_read_b64_tr_b16 a[122:123], v233 offset:52736\n" \
"v_mfma_f32_16x16x32_bf16 v[136:139], a[84:87], a[20:23], v[136:139]\n" \
"ds_read_b64_tr_b16 a[124:125], v233 offset:58880\n" \
"v_mfma_f32_16x16x32_bf16 v[140:143], a[88:91], a[16:19], v[140:143]\n" \
"ds_read_b64_tr_b16 a[126:127], v233 offset:60928\n" \
"v_mfma_f32_16x16x32_bf16 v[140:143], a[92:95], a[20:23], v[140:143]\n" \
"buffer_store_dwordx4 a[200:203], v234, s[56:59], s93 offen offset:2048 sc1\n" \
"v_mfma_f32_16x16x32_bf16 v[172:175], a[88:91], a[24:27], v[172:175]\n" \
"v_mfma_f32_16x16x32_bf16 v[172:175], a[92:95], a[28:31], v[172:175]\n" \
"v_mfma_f32_16x16x32_bf16 v[168:171], a[80:83], a[24:27], v[168:171]\n" \
"v_mfma_f32_16x16x32_bf16 v[168:171], a[84:87], a[28:31], v[168:171]\n" \
"buffer_store_dwordx4 a[204:207], v234, s[56:59], s93 offen offset:3072 sc1\n" \
"v_mfma_f32_16x16x32_bf16 v[164:167], a[72:75], a[24:27], v[164:167]\n" \
"v_mfma_f32_16x16x32_bf16 v[164:167], a[76:79], a[28:31], v[164:167]\n" \
"v_mfma_f32_16x16x32_bf16 v[160:163], a[64:67], a[24:27], v[160:163]\n" \
"v_mfma_f32_16x16x32_bf16 v[160:163], a[68:71], a[28:31], v[160:163]\n" \
"s_waitcnt vmcnt(39) lgkmcnt(0)\n" \
"s_barrier\n" \
"v_mfma_f32_16x16x32_bf16 v[80:83], a[96:99], a[0:3], v[80:83]\n" \
"ds_read_b64_tr_b16 a[64:65], v232 offset:0\n" \
"v_mfma_f32_16x16x32_bf16 v[80:83], a[100:103], a[4:7], v[80:83]\n" \
"ds_read_b64_tr_b16 a[66:67], v232 offset:2048\n" \
"v_mfma_f32_16x16x32_bf16 v[84:87], a[104:107], a[0:3], v[84:87]\n" \
"s_add_u32 m0, s79, 49152\n" \
"ds_read_b64_tr_b16 a[68:69], v232 offset:8192\n" \
"v_mfma_f32_16x16x32_bf16 v[84:87], a[108:111], a[4:7], v[84:87]\n" \
"buffer_load_dwordx4 v228, s[52:55], s78 offen offset:0 lds\n" \
"v_mfma_f32_16x16x32_bf16 v[88:91], a[112:115], a[0:3], v[88:91]\n" \
"s_mul_i32 s73, s62, 557824\n" \
"s_add_u32 s85, s73, 17850368\n" \
"ds_read_b64_tr_b16 a[70:71], v232 offset:10240\n" \
"v_mfma_f32_16x16x32_bf16 v[88:91], a[116:119], a[4:7], v[88:91]\n" \
"s_mul_i32 s83, s63, 34816\n" \
"s_sub_u32 s86, 557056, s83\n" \
"ds_read_b64_tr_b16 a[72:73], v233 offset:0\n" \
"v_mfma_f32_16x16x32_bf16 v[92:95], a[120:123], a[0:3], v[92:95]\n" \
"ds_read_b64_tr_b16 a[74:75], v233 offset:2048\n" \
"v_add_u32 v237, %3, v236\n" \
"v_mfma_f32_16x16x32_bf16 v[92:95], a[124:127], a[4:7], v[92:95]\n" \
"buffer_load_dwordx4 v229, s[52:55], s78 offen offset:1024 lds\n" \
"v_mfma_f32_16x16x32_bf16 v[124:127], a[120:123], a[8:11], v[124:127]\n" \
"s_waitcnt vmcnt(39)\n" \
"ds_read_b128 a[0:3], v250 offset:0\n" \
"v_mfma_f32_16x16x32_bf16 v[124:127], a[124:127], a[12:15], v[124:127]\n" \
"s_add_u32 s36, s56, s73\n" \
"s_addc_u32 s37, s57, 0\n" \
"ds_read_b128 a[4:7], v251 offset:0\n" \
"v_mfma_f32_16x16x32_bf16 v[120:123], a[112:115], a[8:11], v[120:123]\n" \
"s_mov_b32 s38, s86\n" \
"s_mov_b32 s39, 0x20000\n" \
"ds_read_b64_tr_b16 a[76:77], v233 offset:8192\n" \
"v_mfma_f32_16x16x32_bf16 v[120:123], a[116:119], a[12:15], v[120:123]\n" \
"buffer_load_dwordx4 v228, s[52:55], s89 offen offset:2048 lds\n" \
"v_mfma_f32_16x16x32_bf16 v[116:119], a[104:107], a[8:11], v[116:119]\n" \
"ds_read_b64_tr_b16 a[78:79], v233 offset:10240\n" \
"v_add_u32 v235, %3, v234\n" \
"v_mfma_f32_16x16x32_bf16 v[116:119], a[108:111], a[12:15], v[116:119]\n" \
"s_add_u32 s40, s56, s85\n" \
"s_addc_u32 s41, s57, 0\n" \
"ds_read_b64_tr_b16 a[80:81], v232 offset:512\n" \
"v_mfma_f32_16x16x32_bf16 v[112:115], a[96:99], a[8:11], v[112:115]\n" \
"s_mov_b32 s42, s86\n" \
"s_mov_b32 s43, 0x20000\n" \
"ds_read_b64_tr_b16 a[82:83], v232 offset:2560\n" \
"v_mfma_f32_16x16x32_bf16 v[112:115], a[100:103], a[12:15], v[112:115]\n" \
"buffer_load_dwordx4 v229, s[52:55], s89 offen offset:3072 lds\n" \
"v_mfma_f32_16x16x32_bf16 v[144:147], a[96:99], a[16:19], v[144:147]\n" \
"s_waitcnt vmcnt(39)\n" \
"s_add_u32 s85, s74, 131072\n" \
"ds_read_b128 a[8:11], v250 offset:2048\n" \
"v_mfma_f32_16x16x32_bf16 v[144:147], a[100:103], a[20:23], v[144:147]\n" \
"s_add_u32 m0, s74, 131072\n" \
"ds_read_b128 a[12:15], v251 offset:2048\n" \
"v_add_u32 v239, s85, v234\n" \
"v_mfma_f32_16x16x32_bf16 v[148:151], a[104:107], a[16:19], v[148:151]\n" \
"s_waitcnt lgkmcnt(6)\n" \
"s_lshl_b32 s73, s61, 12\n" \
"ds_read_b64_tr_b16 a[84:85], v232 offset:8704\n" \
"v_mfma_f32_16x16x32_bf16 v[148:151], a[108:111], a[20:23], v[148:151]\n" \
"buffer_load_dwordx4 v224, s[48:51], s75 offen offset:0 lds\n" \
"v_mfma_f32_16x16x32_bf16 v[152:155], a[112:115], a[16:19], v[152:155]\n" \
"s_add_u32 s80, s79, 0x10000\n" \
"s_add_u32 s83, s83, s73\n" \
"ds_read_b64_tr_b16 a[86:87], v232 offset:10752\n" \
"v_mfma_f32_16x16x32_bf16 v[152:155], a[116:119], a[20:23], v[152:155]\n" \
"s_cmp_eq_u32 s63, 0\n" \
"s_cselect_b32 s85, 34816, 0\n" \
"ds_read_b64_tr_b16 a[88:89], v233 offset:512\n" \
"v_mfma_f32_16x16x32_bf16 v[156:159], a[120:123], a[16:19], v[156:159]\n" \
"s_add_u32 s73, s73, s85\n" \
"ds_read_b64_tr_b16 a[90:91], v233 offset:2560\n" \
"v_mfma_f32_16x16x32_bf16 v[156:159], a[124:127], a[20:23], v[156:159]\n" \
"buffer_load_dwordx4 v225, s[48:51], s75 offen offset:1024 lds\n" \
"v_mfma_f32_16x16x32_bf16 v[188:191], a[120:123], a[24:27], v[188:191]\n" \
"s_waitcnt vmcnt(39)\n" \
"ds_read_b128 a[16:19], v250 offset:4096\n" \
"v_mfma_f32_16x16x32_bf16 v[188:191], a[124:127], a[28:31], v[188:191]\n" \
"ds_read_b128 a[20:23], v251 offset:4096\n" \
"v_mfma_f32_16x16x32_bf16 v[184:187], a[112:115], a[24:27], v[184:187]\n" \
"s_waitcnt lgkmcnt(6)\n" \
"ds_read_b64_tr_b16 a[92:93], v233 offset:8704\n" \
"v_mfma_f32_16x16x32_bf16 v[184:187], a[116:119], a[28:31], v[184:187]\n" \
"buffer_load_dwordx4 v226, s[48:51], s75 offen offset:2048 lds\n" \
"v_mfma_f32_16x16x32_bf16 v[180:183], a[104:107], a[24:27], v[180:183]\n" \
"ds_read_b64_tr_b16 a[94:95], v233 offset:10752\n" \
"v_mfma_f32_16x16x32_bf16 v[180:183], a[108:111], a[28:31], v[180:183]\n" \
"v_mfma_f32_16x16x32_bf16 v[176:179], a[96:99], a[24:27], v[176:179]\n" \
"v_mfma_f32_16x16x32_bf16 v[176:179], a[100:103], a[28:31], v[176:179]\n" \
"buffer_load_dwordx4 v227, s[48:51], s75 offen offset:3072 lds\n" \
"v_mfma_f32_16x16x32_bf16 v[64:67], a[64:67], a[0:3], v[64:67]\n" \
"s_waitcnt vmcnt(39)\n" \
"ds_read_b128 a[24:27], v250 offset:6144\n" \
"v_mfma_f32_16x16x32_bf16 v[64:67], a[68:71], a[4:7], v[64:67]\n" \
"s_add_u32 m0, s74, 135168\n" \
"ds_read_b128 a[28:31], v251 offset:6144\n" \
"v_mfma_f32_16x16x32_bf16 v[68:71], a[72:75], a[0:3], v[68:71]\n" \
"s_waitcnt lgkmcnt(2)\n" \
"ds_read_b64_tr_b16 a[96:97], v232 offset:1024\n" \
"v_mfma_f32_16x16x32_bf16 v[68:71], a[76:79], a[4:7], v[68:71]\n" \
"buffer_load_dwordx4 v224, s[48:51], s77 offen offset:0 lds\n" \
"v_mfma_f32_16x16x32_bf16 v[72:75], a[80:83], a[0:3], v[72:75]\n" \
"ds_read_b64_tr_b16 a[98:99], v232 offset:3072\n" \
"v_mfma_f32_16x16x32_bf16 v[72:75], a[84:87], a[4:7], v[72:75]\n" \
"ds_read_b64_tr_b16 a[100:101], v232 offset:9216\n" \
"v_mfma_f32_16x16x32_bf16 v[76:79], a[88:91], a[0:3], v[76:79]\n" \
"ds_read_b64_tr_b16 a[102:103], v232 offset:11264\n" \
"v_mfma_f32_16x16x32_bf16 v[76:79], a[92:95], a[4:7], v[76:79]\n" \
"buffer_load_dwordx4 v225, s[48:51], s77 offen offset:1024 lds\n" \
"v_mfma_f32_16x16x32_bf16 v[108:111], a[88:91], a[8:11], v[108:111]\n" \
"ds_read_b64_tr_b16 a[104:105], v233 offset:1024\n" \
"v_mfma_f32_16x16x32_bf16 v[108:111], a[92:95], a[12:15], v[108:111]\n" \
"ds_read_b64_tr_b16 a[106:107], v233 offset:3072\n" \
"v_mfma_f32_16x16x32_bf16 v[104:107], a[80:83], a[8:11], v[104:107]\n" \
"s_waitcnt lgkmcnt(6)\n" \
"ds_read_b64_tr_b16 a[108:109], v233 offset:9216\n" \
"v_mfma_f32_16x16x32_bf16 v[104:107], a[84:87], a[12:15], v[104:107]\n" \
"buffer_load_dwordx4 v226, s[48:51], s77 offen offset:2048 lds\n" \
"v_mfma_f32_16x16x32_bf16 v[100:103], a[72:75], a[8:11], v[100:103]\n" \
"ds_read_b64_tr_b16 a[110:111], v233 offset:11264\n" \
"v_mfma_f32_16x16x32_bf16 v[100:103], a[76:79], a[12:15], v[100:103]\n" \
"ds_read_b64_tr_b16 a[112:113], v232 offset:1536\n" \
"v_mfma_f32_16x16x32_bf16 v[96:99], a[64:67], a[8:11], v[96:99]\n" \
"ds_read_b64_tr_b16 a[114:115], v232 offset:3584\n" \
"v_mfma_f32_16x16x32_bf16 v[96:99], a[68:71], a[12:15], v[96:99]\n" \
"buffer_load_dwordx4 v227, s[48:51], s77 offen offset:3072 lds\n" \
"v_mfma_f32_16x16x32_bf16 v[128:131], a[64:67], a[16:19], v[128:131]\n" \
"ds_read_b64_tr_b16 a[116:117], v232 offset:9728\n" \
"v_mfma_f32_16x16x32_bf16 v[128:131], a[68:71], a[20:23], v[128:131]\n" \
"ds_read_b64_tr_b16 a[118:119], v232 offset:11776\n" \
"v_mfma_f32_16x16x32_bf16 v[132:135], a[72:75], a[16:19], v[132:135]\n" \
"s_add_u32 s93, s94, 8192\n" \
"ds_read_b64_tr_b16 a[120:121], v233 offset:1536\n" \
"v_mfma_f32_16x16x32_bf16 v[132:135], a[76:79], a[20:23], v[132:135]\n" \
"buffer_store_dwordx4 v[32:35], v236, s[56:59], s93 offen offset:0 sc1\n" \
"v_mfma_f32_16x16x32_bf16 v[136:139], a[80:83], a[16:19], v[136:139]\n" \
"ds_read_b64_tr_b16 a[122:123], v233 offset:3584\n" \
"v_mfma_f32_16x16x32_bf16 v[136:139], a[84:87], a[20:23], v[136:139]\n" \
"ds_read_b64_tr_b16 a[124:125], v233 offset:9728\n" \
"v_mfma_f32_16x16x32_bf16 v[140:143], a[88:91], a[16:19], v[140:143]\n" \
"ds_read_b64_tr_b16 a[126:127], v233 offset:11776\n" \
"v_mfma_f32_16x16x32_bf16 v[140:143], a[92:95], a[20:23], v[140:143]\n" \
"v_mfma_f32_16x16x32_bf16 v[172:175], a[88:91], a[24:27], v[172:175]\n" \
"v_mfma_f32_16x16x32_bf16 v[172:175], a[92:95], a[28:31], v[172:175]\n" \
"v_mfma_f32_16x16x32_bf16 v[168:171], a[80:83], a[24:27], v[168:171]\n" \
"v_mfma_f32_16x16x32_bf16 v[168:171], a[84:87], a[28:31], v[168:171]\n" \
"v_mfma_f32_16x16x32_bf16 v[164:167], a[72:75], a[24:27], v[164:167]\n" \
"v_mfma_f32_16x16x32_bf16 v[164:167], a[76:79], a[28:31], v[164:167]\n" \
"v_mfma_f32_16x16x32_bf16 v[160:163], a[64:67], a[24:27], v[160:163]\n" \
"v_mfma_f32_16x16x32_bf16 v[160:163], a[68:71], a[28:31], v[160:163]\n" \
"s_waitcnt vmcnt(38) lgkmcnt(0)\n" \
"s_barrier\n" \
"v_mfma_f32_16x16x32_bf16 v[80:83], a[96:99], a[0:3], v[80:83]\n" \
"s_lshr_b32 s71, s61, 1\n" \
"s_lshl_b32 s71, s71, 14\n" \
"ds_read_b64_tr_b16 a[64:65], v232 offset:16384\n" \
"v_mfma_f32_16x16x32_bf16 v[80:83], a[100:103], a[4:7], v[80:83]\n" \
"s_xor_b32 s71, s71, 0x4000\n" \
"s_and_b32 s70, s61, 1\n" \
"ds_read_b64_tr_b16 a[66:67], v232 offset:18432\n" \
"v_mfma_f32_16x16x32_bf16 v[84:87], a[104:107], a[0:3], v[84:87]\n" \
"s_lshl_b32 s70, s70, 12\n" \
"s_add_u32 s71, s71, s70\n" \
"ds_read_b64_tr_b16 a[68:69], v232 offset:24576\n" \
"v_mfma_f32_16x16x32_bf16 v[84:87], a[108:111], a[4:7], v[84:87]\n" \
"buffer_store_dwordx4 v[48:51], v236, s[56:59], s93 offen offset:256 sc1\n" \
"v_mfma_f32_16x16x32_bf16 v[88:91], a[112:115], a[0:3], v[88:91]\n" \
"ds_read_b64_tr_b16 a[70:71], v232 offset:26624\n" \
"v_add_u32 v224, s71, v237\n" \
"v_mfma_f32_16x16x32_bf16 v[88:91], a[116:119], a[4:7], v[88:91]\n" \
"s_and_b32 s70, s63, 1\n" \
"s_lshl_b32 s70, s70, 15\n" \
"ds_read_b64_tr_b16 a[72:73], v233 offset:16384\n" \
"v_mfma_f32_16x16x32_bf16 v[92:95], a[120:123], a[0:3], v[92:95]\n" \
"s_xor_b32 s70, s70, 0x18000\n" \
"s_add_u32 s71, s71, s70\n" \
"ds_read_b64_tr_b16 a[74:75], v233 offset:18432\n" \
"v_mfma_f32_16x16x32_bf16 v[92:95], a[124:127], a[4:7], v[92:95]\n" \
"v_add_u32 v225, s71, v235\n" \
"s_lshr_b32 s64, s22, 7\n" \
"v_mfma_f32_16x16x32_bf16 v[124:127], a[120:123], a[8:11], v[124:127]\n" \
"s_waitcnt vmcnt(37)\n" \
"s_sub_u32 s64, 3, s64\n" \
"ds_read_b128 a[0:3], v230 offset:0\n" \
"v_mfma_f32_16x16x32_bf16 v[124:127], a[124:127], a[12:15], v[124:127]\n" \
"s_and_b32 s64, s64, 7\n" \
"s_and_b32 s65, %1, 1\n" \
"ds_read_b128 a[4:7], v231 offset:0\n" \
"v_mfma_f32_16x16x32_bf16 v[120:123], a[112:115], a[8:11], v[120:123]\n" \
"s_lshl_b32 s65, s65, 3\n" \
"s_add_u32 s64, s64, s65\n" \
"ds_read_b64_tr_b16 a[76:77], v233 offset:24576\n" \
"v_mfma_f32_16x16x32_bf16 v[120:123], a[116:119], a[12:15], v[120:123]\n" \
"s_lshl_b32 s64, s64, 7\n" \
"s_lshr_b32 s65, %1, 3\n" \
"s_lshl_b32 s65, s65, 2\n" \
"v_mfma_f32_16x16x32_bf16 v[116:119], a[104:107], a[8:11], v[116:119]\n" \
"s_add_u32 s65, s65, s61\n" \
"s_mul_i32 s65, s65, 24576\n" \
"ds_read_b64_tr_b16 a[78:79], v233 offset:26624\n" \
"v_mfma_f32_16x16x32_bf16 v[116:119], a[108:111], a[12:15], v[116:119]\n" \
"s_add_u32 s64, s64, s65\n" \
"ds_read_b64_tr_b16 a[80:81], v232 offset:16896\n" \
"v_bfe_u32 v254, v234, 7, 2\n" \
"v_mfma_f32_16x16x32_bf16 v[112:115], a[96:99], a[8:11], v[112:115]\n" \
"ds_read_b64_tr_b16 a[82:83], v232 offset:18944\n" \
"v_mul_u32_u24 v254, 3, v254\n" \
"v_mfma_f32_16x16x32_bf16 v[112:115], a[100:103], a[12:15], v[112:115]\n" \
"buffer_store_dwordx4 v[40:43], v236, s[56:59], s93 offen offset:2048 sc1\n" \
"v_mfma_f32_16x16x32_bf16 v[144:147], a[96:99], a[16:19], v[144:147]\n" \
"s_waitcnt vmcnt(36)\n" \
"ds_read_b128 a[8:11], v230 offset:2048\n" \
"v_bfe_u32 v243, v234, 5, 2\n" \
"v_mfma_f32_16x16x32_bf16 v[144:147], a[100:103], a[20:23], v[144:147]\n" \
"ds_read_b128 a[12:15], v231 offset:2048\n" \
"v_min_u32 v243, 2, v243\n" \
"v_mfma_f32_16x16x32_bf16 v[148:151], a[104:107], a[16:19], v[148:151]\n" \
"ds_read_b64_tr_b16 a[84:85], v232 offset:25088\n" \
"v_add_u32 v254, v254, v243\n" \
"v_mfma_f32_16x16x32_bf16 v[148:151], a[108:111], a[20:23], v[148:151]\n" \
"v_and_b32 v243, 16, v234\n" \
"v_lshl_add_u32 v243, v243, 2, s64\n" \
"v_mfma_f32_16x16x32_bf16 v[152:155], a[112:115], a[16:19], v[152:155]\n" \
"ds_read_b64_tr_b16 a[86:87], v232 offset:27136\n" \
"v_lshl_add_u32 v254, v254, 11, v243\n" \
"v_mfma_f32_16x16x32_bf16 v[152:155], a[116:119], a[20:23], v[152:155]\n" \
"ds_read_b64_tr_b16 a[88:89], v233 offset:16896\n" \
"v_mfma_f32_16x16x32_bf16 v[156:159], a[120:123], a[16:19], v[156:159]\n" \
"ds_read_b64_tr_b16 a[90:91], v233 offset:18944\n" \
"v_mfma_f32_16x16x32_bf16 v[156:159], a[124:127], a[20:23], v[156:159]\n" \
"v_mfma_f32_16x16x32_bf16 v[188:191], a[120:123], a[24:27], v[188:191]\n" \
"s_waitcnt vmcnt(34)\n" \
"ds_read_b128 a[16:19], v230 offset:4096\n" \
"v_mfma_f32_16x16x32_bf16 v[188:191], a[124:127], a[28:31], v[188:191]\n" \
"ds_read_b128 a[20:23], v231 offset:4096\n" \
"v_mfma_f32_16x16x32_bf16 v[184:187], a[112:115], a[24:27], v[184:187]\n" \
"ds_read_b64_tr_b16 a[92:93], v233 offset:25088\n" \
"v_mfma_f32_16x16x32_bf16 v[184:187], a[116:119], a[28:31], v[184:187]\n" \
"v_mfma_f32_16x16x32_bf16 v[180:183], a[104:107], a[24:27], v[180:183]\n" \
"ds_read_b64_tr_b16 a[94:95], v233 offset:27136\n" \
"v_mfma_f32_16x16x32_bf16 v[180:183], a[108:111], a[28:31], v[180:183]\n" \
"v_mfma_f32_16x16x32_bf16 v[176:179], a[96:99], a[24:27], v[176:179]\n" \
"buffer_store_dwordx4 v[56:59], v236, s[56:59], s93 offen offset:2304 sc1\n" \
"v_mfma_f32_16x16x32_bf16 v[176:179], a[100:103], a[28:31], v[176:179]\n" \
"s_waitcnt lgkmcnt(1)\n" \
"v_mfma_f32_16x16x32_bf16 v[64:67], a[64:67], a[0:3], v[64:67]\n" \
"Lpg4446_%=:\n" \
"s_cbranch_execz Lpg6181_%=\n" \
"s_waitcnt vmcnt(33)\n" \
"s_lshl_b32 s68, s63, 2\n" \
"ds_read_b128 a[24:27], v230 offset:6144\n" \
"v_mfma_f32_16x16x32_bf16 v[64:67], a[68:71], a[4:7], v[64:67]\n" \
"s_add_u32 s68, s68, s90\n" \
"ds_read_b128 a[28:31], v231 offset:6144\n" \
"v_mfma_f32_16x16x32_bf16 v[68:71], a[72:75], a[0:3], v[68:71]\n" \
"ds_read_b64_tr_b16 a[96:97], v232 offset:17408\n" \
"v_mfma_f32_16x16x32_bf16 v[68:71], a[76:79], a[4:7], v[68:71]\n" \
"v_mov_b32 v228, s60\n" \
"v_lshlrev_b32 v229, 28, s61\n" \
"v_mfma_f32_16x16x32_bf16 v[72:75], a[80:83], a[0:3], v[72:75]\n" \
"ds_read_b64_tr_b16 a[98:99], v232 offset:19456\n" \
"v_mfma_f32_16x16x32_bf16 v[72:75], a[84:87], a[4:7], v[72:75]\n" \
"s_waitcnt lgkmcnt(3)\n" \
"ds_read_b64_tr_b16 a[100:101], v232 offset:25600\n" \
"v_mfma_f32_16x16x32_bf16 v[76:79], a[88:91], a[0:3], v[76:79]\n" \
"ds_read_b64_tr_b16 a[102:103], v232 offset:27648\n" \
"v_mfma_f32_16x16x32_bf16 v[76:79], a[92:95], a[4:7], v[76:79]\n" \
"v_mfma_f32_16x16x32_bf16 v[108:111], a[88:91], a[8:11], v[108:111]\n" \
"ds_read_b64_tr_b16 a[104:105], v233 offset:17408\n" \
"v_mfma_f32_16x16x32_bf16 v[108:111], a[92:95], a[12:15], v[108:111]\n" \
"ds_read_b64_tr_b16 a[106:107], v233 offset:19456\n" \
"v_mfma_f32_16x16x32_bf16 v[104:107], a[80:83], a[8:11], v[104:107]\n" \
"ds_read_b64_tr_b16 a[108:109], v233 offset:25600\n" \
"v_mfma_f32_16x16x32_bf16 v[104:107], a[84:87], a[12:15], v[104:107]\n" \
"v_mfma_f32_16x16x32_bf16 v[100:103], a[72:75], a[8:11], v[100:103]\n" \
"ds_read_b64_tr_b16 a[110:111], v233 offset:27648\n" \
"v_mfma_f32_16x16x32_bf16 v[100:103], a[76:79], a[12:15], v[100:103]\n" \
"ds_read_b64_tr_b16 a[112:113], v232 offset:17920\n" \
"v_mfma_f32_16x16x32_bf16 v[96:99], a[64:67], a[8:11], v[96:99]\n" \
"s_add_u32 s93, s94, 0\n" \
"ds_read_b64_tr_b16 a[114:115], v232 offset:19968\n" \
"v_mfma_f32_16x16x32_bf16 v[96:99], a[68:71], a[12:15], v[96:99]\n" \
"buffer_store_dwordx4 v[0:3], v236, s[56:59], s93 offen offset:0 sc1\n" \
"v_mfma_f32_16x16x32_bf16 v[128:131], a[64:67], a[16:19], v[128:131]\n" \
"ds_read_b64_tr_b16 a[116:117], v232 offset:26112\n" \
"v_mfma_f32_16x16x32_bf16 v[128:131], a[68:71], a[20:23], v[128:131]\n" \
"ds_read_b64_tr_b16 a[118:119], v232 offset:28160\n" \
"v_mfma_f32_16x16x32_bf16 v[132:135], a[72:75], a[16:19], v[132:135]\n" \
"ds_read_b64_tr_b16 a[120:121], v233 offset:17920\n" \
"v_mfma_f32_16x16x32_bf16 v[132:135], a[76:79], a[20:23], v[132:135]\n" \
"v_mfma_f32_16x16x32_bf16 v[136:139], a[80:83], a[16:19], v[136:139]\n" \
"ds_read_b64_tr_b16 a[122:123], v233 offset:19968\n" \
"v_mfma_f32_16x16x32_bf16 v[136:139], a[84:87], a[20:23], v[136:139]\n" \
"ds_read_b64_tr_b16 a[124:125], v233 offset:26112\n" \
"v_mfma_f32_16x16x32_bf16 v[140:143], a[88:91], a[16:19], v[140:143]\n" \
"ds_read_b64_tr_b16 a[126:127], v233 offset:28160\n" \
"v_mfma_f32_16x16x32_bf16 v[140:143], a[92:95], a[20:23], v[140:143]\n" \
"s_waitcnt lgkmcnt(3)\n" \
"v_mfma_f32_16x16x32_bf16 v[172:175], a[88:91], a[24:27], v[172:175]\n" \
"v_mfma_f32_16x16x32_bf16 v[172:175], a[92:95], a[28:31], v[172:175]\n" \
"v_mfma_f32_16x16x32_bf16 v[168:171], a[80:83], a[24:27], v[168:171]\n" \
"v_mfma_f32_16x16x32_bf16 v[168:171], a[84:87], a[28:31], v[168:171]\n" \
"v_mfma_f32_16x16x32_bf16 v[164:167], a[72:75], a[24:27], v[164:167]\n" \
"v_mfma_f32_16x16x32_bf16 v[164:167], a[76:79], a[28:31], v[164:167]\n" \
"buffer_store_dwordx4 v[16:19], v236, s[56:59], s93 offen offset:256 sc1\n" \
"v_mfma_f32_16x16x32_bf16 v[160:163], a[64:67], a[24:27], v[160:163]\n" \
"v_mfma_f32_16x16x32_bf16 v[160:163], a[68:71], a[28:31], v[160:163]\n" \
"s_waitcnt vmcnt(18) lgkmcnt(0)\n" \
"s_barrier\n" \
"v_mfma_f32_16x16x32_bf16 v[80:83], a[96:99], a[0:3], v[80:83]\n" \
"ds_read_b64_tr_b16 a[64:65], v232 offset:32768\n" \
"v_mfma_f32_16x16x32_bf16 v[80:83], a[100:103], a[4:7], v[80:83]\n" \
"ds_read_b64_tr_b16 a[66:67], v232 offset:34816\n" \
"buffer_store_dword v228, v229, s[56:59], s68 offen sc1\n" \
"v_mfma_f32_16x16x32_bf16 v[84:87], a[104:107], a[0:3], v[84:87]\n" \
"ds_read_b64_tr_b16 a[68:69], v232 offset:40960\n" \
"v_lshrrev_b32 v238, 4, v234\n" \
"v_mfma_f32_16x16x32_bf16 v[84:87], a[108:111], a[4:7], v[84:87]\n" \
"ds_write_b128 v224, v[192:195] offset:8192\n" \
"v_mfma_f32_16x16x32_bf16 v[88:91], a[112:115], a[0:3], v[88:91]\n" \
"ds_read_b64_tr_b16 a[70:71], v232 offset:43008\n" \
"v_lshrrev_b32 v240, 4, v238\n" \
"v_mfma_f32_16x16x32_bf16 v[88:91], a[116:119], a[4:7], v[88:91]\n" \
"ds_read_b64_tr_b16 a[72:73], v233 offset:32768\n" \
"v_lshlrev_b32 v240, 2, v240\n" \
"v_mfma_f32_16x16x32_bf16 v[92:95], a[120:123], a[0:3], v[92:95]\n" \
"ds_read_b64_tr_b16 a[74:75], v233 offset:34816\n" \
"v_and_b32 v238, 15, v238\n" \
"v_mfma_f32_16x16x32_bf16 v[92:95], a[124:127], a[4:7], v[92:95]\n" \
"v_sub_u32 v238, v238, v240\n" \
"v_subrev_u32 v248, 0, v238\n" \
"v_mfma_f32_16x16x32_bf16 v[124:127], a[120:123], a[8:11], v[124:127]\n" \
"ds_read_b128 a[0:3], v230 offset:32768\n" \
"v_subrev_u32 v249, 1, v238\n" \
"v_mfma_f32_16x16x32_bf16 v[124:127], a[124:127], a[12:15], v[124:127]\n" \
"ds_read_b128 a[4:7], v231 offset:32768\n" \
"v_subrev_u32 v250, 2, v238\n" \
"v_mfma_f32_16x16x32_bf16 v[120:123], a[112:115], a[8:11], v[120:123]\n" \
"ds_read_b64_tr_b16 a[76:77], v233 offset:40960\n" \
"v_subrev_u32 v251, 3, v238\n" \
"v_mfma_f32_16x16x32_bf16 v[120:123], a[116:119], a[12:15], v[120:123]\n" \
"ds_write_b128 v224, v[208:211] offset:8448\n" \
"v_mfma_f32_16x16x32_bf16 v[116:119], a[104:107], a[8:11], v[116:119]\n" \
"ds_read_b64_tr_b16 a[78:79], v233 offset:43008\n" \
"v_ashrrev_i32 v248, 31, v248\n" \
"v_mfma_f32_16x16x32_bf16 v[116:119], a[108:111], a[12:15], v[116:119]\n" \
"ds_read_b64_tr_b16 a[80:81], v232 offset:33280\n" \
"v_ashrrev_i32 v249, 31, v249\n" \
"v_mfma_f32_16x16x32_bf16 v[112:115], a[96:99], a[8:11], v[112:115]\n" \
"ds_read_b64_tr_b16 a[82:83], v232 offset:35328\n" \
"v_ashrrev_i32 v250, 31, v250\n" \
"v_mfma_f32_16x16x32_bf16 v[112:115], a[100:103], a[12:15], v[112:115]\n" \
"buffer_store_dwordx4 v[8:11], v236, s[56:59], s93 offen offset:2048 sc1\n" \
"v_mfma_f32_16x16x32_bf16 v[144:147], a[96:99], a[16:19], v[144:147]\n" \
"ds_read_b128 a[8:11], v230 offset:34816\n" \
"v_ashrrev_i32 v251, 31, v251\n" \
"v_mfma_f32_16x16x32_bf16 v[144:147], a[100:103], a[20:23], v[144:147]\n" \
"ds_read_b128 a[12:15], v231 offset:34816\n" \
"v_lshlrev_b32 v248, 23, v248\n" \
"v_mfma_f32_16x16x32_bf16 v[148:151], a[104:107], a[16:19], v[148:151]\n" \
"ds_read_b64_tr_b16 a[84:85], v232 offset:41472\n" \
"v_lshlrev_b32 v249, 23, v249\n" \
"v_mfma_f32_16x16x32_bf16 v[148:151], a[108:111], a[20:23], v[148:151]\n" \
"ds_write_b128 v224, v[200:203] offset:10240\n" \
"v_mfma_f32_16x16x32_bf16 v[152:155], a[112:115], a[16:19], v[152:155]\n" \
"ds_read_b64_tr_b16 a[86:87], v232 offset:43520\n" \
"v_lshlrev_b32 v250, 23, v250\n" \
"v_mfma_f32_16x16x32_bf16 v[152:155], a[116:119], a[20:23], v[152:155]\n" \
"ds_read_b64_tr_b16 a[88:89], v233 offset:33280\n" \
"v_lshlrev_b32 v251, 23, v251\n" \
"v_mfma_f32_16x16x32_bf16 v[156:159], a[120:123], a[16:19], v[156:159]\n" \
"ds_read_b64_tr_b16 a[90:91], v233 offset:35328\n" \
"v_mov_b32 v240, 0xff800000\n" \
"v_mfma_f32_16x16x32_bf16 v[156:159], a[124:127], a[20:23], v[156:159]\n" \
"ds_write_b128 v224, v[216:219] offset:10496\n" \
"v_mfma_f32_16x16x32_bf16 v[188:191], a[120:123], a[24:27], v[188:191]\n" \
"ds_read_b128 a[16:19], v230 offset:36864\n" \
"v_add_f32 v248, 0x42400000, v248\n" \
"v_mfma_f32_16x16x32_bf16 v[188:191], a[124:127], a[28:31], v[188:191]\n" \
"ds_read_b128 a[20:23], v231 offset:36864\n" \
"v_add_f32 v249, 0x42400000, v249\n" \
"v_mfma_f32_16x16x32_bf16 v[184:187], a[112:115], a[24:27], v[184:187]\n" \
"ds_read_b64_tr_b16 a[92:93], v233 offset:41472\n" \
"v_add_f32 v250, 0x42400000, v250\n" \
"v_mfma_f32_16x16x32_bf16 v[184:187], a[116:119], a[28:31], v[184:187]\n" \
"v_add_f32 v251, 0x42400000, v251\n" \
"s_mov_b32 s72, 0x42400000\n" \
"v_mfma_f32_16x16x32_bf16 v[180:183], a[104:107], a[24:27], v[180:183]\n" \
"s_cmp_gt_u32 s61, 0\n" \
"s_cselect_b32 s70, s72, 0xff800000\n" \
"ds_read_b64_tr_b16 a[94:95], v233 offset:43520\n" \
"v_mfma_f32_16x16x32_bf16 v[180:183], a[108:111], a[28:31], v[180:183]\n" \
"buffer_store_dwordx4 v[24:27], v236, s[56:59], s93 offen offset:2304 sc1\n" \
"v_mfma_f32_16x16x32_bf16 v[176:179], a[96:99], a[24:27], v[176:179]\n" \
"ds_write_b128 v224, v[196:199] offset:0\n" \
"v_mfma_f32_16x16x32_bf16 v[176:179], a[100:103], a[28:31], v[176:179]\n" \
"s_waitcnt lgkmcnt(2)\n" \
"v_mov_b32 v252, s70\n" \
"v_mfma_f32_16x16x32_bf16 v[64:67], a[64:67], a[0:3], v[64:67]\n" \
"ds_read_b128 a[24:27], v230 offset:38912\n" \
"v_mfma_f32_16x16x32_bf16 v[64:67], a[68:71], a[4:7], v[64:67]\n" \
"s_cmp_gt_u32 s61, 1\n" \
"s_cselect_b32 s70, s72, 0xff800000\n" \
"ds_read_b128 a[28:31], v231 offset:38912\n" \
"v_mfma_f32_16x16x32_bf16 v[68:71], a[72:75], a[0:3], v[68:71]\n" \
"ds_read_b64_tr_b16 a[96:97], v232 offset:33792\n" \
"v_mov_b32 v253, s70\n" \
"v_mfma_f32_16x16x32_bf16 v[68:71], a[76:79], a[4:7], v[68:71]\n" \
"s_cmp_gt_u32 s61, 2\n" \
"s_cselect_b32 s70, s72, 0xff800000\n" \
"v_mov_b32 v226, s70\n" \
"v_mfma_f32_16x16x32_bf16 v[72:75], a[80:83], a[0:3], v[72:75]\n" \
"ds_read_b64_tr_b16 a[98:99], v232 offset:35840\n" \
"v_bfe_i32 v243, v234, 4, 1\n" \
"v_mfma_f32_16x16x32_bf16 v[72:75], a[84:87], a[4:7], v[72:75]\n" \
"s_waitcnt lgkmcnt(3)\n" \
"ds_read_b64_tr_b16 a[100:101], v232 offset:41984\n" \
"v_and_b32 v243, 0xaaaa, v243\n" \
"v_mfma_f32_16x16x32_bf16 v[76:79], a[88:91], a[0:3], v[76:79]\n" \
"ds_read_b64_tr_b16 a[102:103], v232 offset:44032\n" \
"v_or_b32 v243, 0x4444, v243\n" \
"v_mfma_f32_16x16x32_bf16 v[76:79], a[92:95], a[4:7], v[76:79]\n" \
"ds_write_b128 v224, v[212:215] offset:256\n" \
"v_mfma_f32_16x16x32_bf16 v[108:111], a[88:91], a[8:11], v[108:111]\n" \
"ds_read_b64_tr_b16 a[104:105], v233 offset:33792\n" \
"v_bfe_i32 v244, v234, 5, 1\n" \
"v_mfma_f32_16x16x32_bf16 v[108:111], a[92:95], a[12:15], v[108:111]\n" \
"ds_read_b64_tr_b16 a[106:107], v233 offset:35840\n" \
"v_not_b32 v244, v244\n" \
"v_mfma_f32_16x16x32_bf16 v[104:107], a[80:83], a[8:11], v[104:107]\n" \
"ds_read_b64_tr_b16 a[108:109], v233 offset:41984\n" \
"v_and_b32 v244, 0x3f803f80, v244\n" \
"v_mfma_f32_16x16x32_bf16 v[104:107], a[84:87], a[12:15], v[104:107]\n" \
"v_mov_b32 v245, v244\n" \
"v_mov_b32 v246, v244\n" \
"v_mfma_f32_16x16x32_bf16 v[100:103], a[72:75], a[8:11], v[100:103]\n" \
"ds_read_b64_tr_b16 a[110:111], v233 offset:44032\n" \
"v_mov_b32 v247, v244\n" \
"v_mfma_f32_16x16x32_bf16 v[100:103], a[76:79], a[12:15], v[100:103]\n" \
"s_mov_b32 s76, s73\n" \
"s_and_b32 s77, s63, 1\n" \
"ds_read_b64_tr_b16 a[112:113], v232 offset:34304\n" \
"v_mfma_f32_16x16x32_bf16 v[96:99], a[64:67], a[8:11], v[96:99]\n" \
"s_add_u32 s93, s94, 17858560\n" \
"s_lshl_b32 s77, s77, 15\n" \
"ds_read_b64_tr_b16 a[114:115], v232 offset:36352\n" \
"v_mfma_f32_16x16x32_bf16 v[96:99], a[68:71], a[12:15], v[96:99]\n" \
"buffer_store_dwordx4 v[36:39], v234, s[56:59], s93 offen offset:0 sc1\n" \
"v_mfma_f32_16x16x32_bf16 v[128:131], a[64:67], a[16:19], v[128:131]\n" \
"s_xor_b32 s89, s77, 0x8000\n" \
"s_add_u32 s82, s89, 0x10000\n" \
"ds_read_b64_tr_b16 a[116:117], v232 offset:42496\n" \
"v_mfma_f32_16x16x32_bf16 v[128:131], a[68:71], a[20:23], v[128:131]\n" \
"s_mov_b32 s74, 0\n" \
"s_add_u32 s73, s73, 34816\n" \
"ds_read_b64_tr_b16 a[118:119], v232 offset:44544\n" \
"v_mfma_f32_16x16x32_bf16 v[132:135], a[72:75], a[16:19], v[132:135]\n" \
"s_cmp_eq_u32 s73, s83\n" \
"s_cselect_b32 s70, 34816, 0\n" \
"ds_read_b64_tr_b16 a[120:121], v233 offset:34304\n" \
"v_mfma_f32_16x16x32_bf16 v[132:135], a[76:79], a[20:23], v[132:135]\n" \
"ds_write_b128 v224, v[204:207] offset:2048\n" \
"v_mfma_f32_16x16x32_bf16 v[136:139], a[80:83], a[16:19], v[136:139]\n" \
"s_add_u32 s73, s73, s70\n" \
"s_lshr_b32 s70, s61, 1\n" \
"ds_read_b64_tr_b16 a[122:123], v233 offset:36352\n" \
"v_mfma_f32_16x16x32_bf16 v[136:139], a[84:87], a[20:23], v[136:139]\n" \
"s_lshl_b32 s85, s63, 2\n" \
"s_sub_u32 s86, 60, s85\n" \
"ds_read_b64_tr_b16 a[124:125], v233 offset:42496\n" \
"v_mfma_f32_16x16x32_bf16 v[140:143], a[88:91], a[16:19], v[140:143]\n" \
"s_add_u32 s85, s85, s70\n" \
"s_add_u32 s86, s86, s70\n" \
"ds_read_b64_tr_b16 a[126:127], v233 offset:44544\n" \
"v_mfma_f32_16x16x32_bf16 v[140:143], a[92:95], a[20:23], v[140:143]\n" \
"s_waitcnt lgkmcnt(4)\n" \
"ds_write_b128 v224, v[220:223] offset:2304\n" \
"v_mfma_f32_16x16x32_bf16 v[172:175], a[88:91], a[24:27], v[172:175]\n" \
"s_mul_i32 s87, s85, 266240\n" \
"s_mul_i32 s88, s86, 266240\n" \
"v_add_u32 v230, 0x10000, v230\n" \
"v_mfma_f32_16x16x32_bf16 v[172:175], a[92:95], a[28:31], v[172:175]\n" \
"s_and_b32 s75, s61, 1\n" \
"s_lshl_b32 s70, s75, 8\n" \
"v_add_u32 v231, 0x10000, v231\n" \
"v_mfma_f32_16x16x32_bf16 v[168:171], a[80:83], a[24:27], v[168:171]\n" \
"s_lshl_b32 s91, s62, 13\n" \
"s_add_u32 s91, s91, s70\n" \
"s_add_u32 s91, s91, 35700736\n" \
"v_mfma_f32_16x16x32_bf16 v[168:171], a[84:87], a[28:31], v[168:171]\n" \
"buffer_store_dwordx4 v[52:55], v234, s[56:59], s93 offen offset:1024 sc1\n" \
"v_mfma_f32_16x16x32_bf16 v[164:167], a[72:75], a[24:27], v[164:167]\n" \
"s_add_u32 s87, s87, s91\n" \
"s_add_u32 s88, s88, s91\n" \
"s_lshl_b32 s75, s75, 14\n" \
"v_mfma_f32_16x16x32_bf16 v[164:167], a[76:79], a[28:31], v[164:167]\n" \
"s_cmp_eq_u32 s63, 7\n" \
"s_cselect_b32 s75, 0, s75\n" \
"s_xor_b32 s78, s75, 0xc000\n" \
"s_mov_b32 s81, s78\n" \
"s_cmp_eq_u32 s63, 0\n" \
"s_cselect_b32 s91, s75, 0\n" \
"s_add_u32 s91, s91, 0x8000\n" \
"s_xor_b32 s75, s75, 0x8000\n" \
"s_sub_u32 s84, 7, s63\n" \
"v_mfma_f32_16x16x32_bf16 v[160:163], a[64:67], a[24:27], v[160:163]\n" \
"v_mfma_f32_16x16x32_bf16 v[160:163], a[68:71], a[28:31], v[160:163]\n" \
"s_waitcnt vmcnt(19) lgkmcnt(0)\n" \
"s_barrier\n" \
"v_mfma_f32_16x16x32_bf16 v[144:147], a[96:99], a[16:19], v[144:147]\n" \
"s_mov_b64 s[44:45], s[14:15]\n" \
"s_mov_b32 s46, 0\n" \
"ds_read_b64_tr_b16 a[64:65], v232 offset:49152\n" \
"v_mfma_f32_16x16x32_bf16 v[144:147], a[100:103], a[20:23], v[144:147]\n" \
"s_mov_b32 s47, 0\n" \
"s_cmp_eq_u32 s63, 7\n" \
"s_cselect_b64 s[44:45], s[18:19], s[44:45]\n" \
"s_cselect_b32 s46, 5, s46\n" \
"s_cmp_eq_u32 s63, 3\n" \
"s_cselect_b64 s[44:45], s[18:19], s[44:45]\n" \
"s_cselect_b32 s46, 3, s46\n" \
"ds_read_b64_tr_b16 a[66:67], v232 offset:51200\n" \
"v_mfma_f32_16x16x32_bf16 v[148:151], a[104:107], a[16:19], v[148:151]\n" \
"s_cmp_eq_u32 s63, 3\n" \
"s_cselect_b32 s47, 640, s47\n" \
"ds_read_b64_tr_b16 a[68:69], v232 offset:57344\n" \
"v_mfma_f32_16x16x32_bf16 v[148:151], a[108:111], a[20:23], v[148:151]\n" \
"ds_write_b128 v225, a[192:195] offset:0\n" \
"v_mfma_f32_16x16x32_bf16 v[152:155], a[112:115], a[16:19], v[152:155]\n" \
"ds_read_b64_tr_b16 a[70:71], v232 offset:59392\n" \
"v_mfma_f32_16x16x32_bf16 v[152:155], a[116:119], a[20:23], v[152:155]\n" \
"s_cmp_eq_u32 s63, 6\n" \
"s_cselect_b64 s[44:45], s[22:23], s[44:45]\n" \
"s_cselect_b32 s46, 6, s46\n" \
"ds_read_b64_tr_b16 a[72:73], v233 offset:49152\n" \
"v_mfma_f32_16x16x32_bf16 v[156:159], a[120:123], a[16:19], v[156:159]\n" \
"ds_read_b64_tr_b16 a[74:75], v233 offset:51200\n" \
"v_mfma_f32_16x16x32_bf16 v[156:159], a[124:127], a[20:23], v[156:159]\n" \
"s_cmp_eq_u32 s63, 4\n" \
"s_cselect_b64 s[44:45], s[20:21], s[44:45]\n" \
"s_cselect_b32 s46, 4, s46\n" \
"v_mfma_f32_16x16x32_bf16 v[188:191], a[120:123], a[24:27], v[188:191]\n" \
"s_waitcnt vmcnt(17)\n" \
"ds_read_b128 a[16:19], v230 offset:0\n" \
"v_mfma_f32_16x16x32_bf16 v[188:191], a[124:127], a[28:31], v[188:191]\n" \
"s_cmp_eq_u32 s63, 2\n" \
"s_cselect_b64 s[44:45], s[24:25], s[44:45]\n" \
"s_cselect_b32 s46, 2, s46\n" \
"ds_read_b128 a[20:23], v231 offset:0\n" \
"v_mfma_f32_16x16x32_bf16 v[184:187], a[112:115], a[24:27], v[184:187]\n" \
"s_lshl_b32 s48, s62, 2\n" \
"s_add_u32 s48, s48, s61\n" \
"ds_read_b64_tr_b16 a[76:77], v233 offset:57344\n" \
"v_mfma_f32_16x16x32_bf16 v[184:187], a[116:119], a[28:31], v[184:187]\n" \
"buffer_store_dwordx4 v[44:47], v234, s[56:59], s93 offen offset:2048 sc1\n" \
"v_mfma_f32_16x16x32_bf16 v[180:183], a[104:107], a[24:27], v[180:183]\n" \
"s_add_u32 s48, s48, s47\n" \
"s_lshl_b32 s48, s48, 13\n" \
"ds_read_b64_tr_b16 a[78:79], v233 offset:59392\n" \
"v_mfma_f32_16x16x32_bf16 v[180:183], a[108:111], a[28:31], v[180:183]\n" \
"s_add_u32 s44, s44, s48\n" \
"s_addc_u32 s45, s45, 0\n" \
"ds_read_b64_tr_b16 a[80:81], v232 offset:49664\n" \
"v_mfma_f32_16x16x32_bf16 v[176:179], a[96:99], a[24:27], v[176:179]\n" \
"ds_read_b64_tr_b16 a[82:83], v232 offset:51712\n" \
"v_mfma_f32_16x16x32_bf16 v[176:179], a[100:103], a[28:31], v[176:179]\n" \
"ds_write_b128 v225, a[196:199] offset:1024\n" \
"v_mfma_f32_16x16x32_bf16 v[80:83], a[96:99], a[0:3], v[80:83]\n" \
"s_waitcnt vmcnt(16)\n" \
"ds_read_b128 a[24:27], v230 offset:2048\n" \
"v_mfma_f32_16x16x32_bf16 v[80:83], a[100:103], a[4:7], v[80:83]\n" \
"ds_read_b128 a[28:31], v231 offset:2048\n" \
"v_mfma_f32_16x16x32_bf16 v[84:87], a[104:107], a[0:3], v[84:87]\n" \
"ds_read_b64_tr_b16 a[84:85], v232 offset:57856\n" \
"v_mfma_f32_16x16x32_bf16 v[84:87], a[108:111], a[4:7], v[84:87]\n" \
"ds_write_b128 v225, a[200:203] offset:2048\n" \
"v_mfma_f32_16x16x32_bf16 v[88:91], a[112:115], a[0:3], v[88:91]\n" \
"ds_read_b64_tr_b16 a[86:87], v232 offset:59904\n" \
"v_mfma_f32_16x16x32_bf16 v[88:91], a[116:119], a[4:7], v[88:91]\n" \
"ds_read_b64_tr_b16 a[88:89], v233 offset:49664\n" \
"v_mfma_f32_16x16x32_bf16 v[92:95], a[120:123], a[0:3], v[92:95]\n" \
"ds_read_b64_tr_b16 a[90:91], v233 offset:51712\n" \
"v_mfma_f32_16x16x32_bf16 v[92:95], a[124:127], a[4:7], v[92:95]\n" \
"ds_write_b128 v225, a[204:207] offset:3072\n" \
"v_mfma_f32_16x16x32_bf16 v[124:127], a[120:123], a[8:11], v[124:127]\n" \
"s_waitcnt vmcnt(14)\n" \
"ds_read_b128 a[0:3], v230 offset:4096\n" \
"v_mfma_f32_16x16x32_bf16 v[124:127], a[124:127], a[12:15], v[124:127]\n" \
"ds_read_b128 a[4:7], v231 offset:4096\n" \
"v_mfma_f32_16x16x32_bf16 v[120:123], a[112:115], a[8:11], v[120:123]\n" \
"ds_read_b64_tr_b16 a[92:93], v233 offset:57856\n" \
"v_mfma_f32_16x16x32_bf16 v[120:123], a[116:119], a[12:15], v[120:123]\n" \
"buffer_store_dwordx4 v[60:63], v234, s[56:59], s93 offen offset:3072 sc1\n" \
"v_mfma_f32_16x16x32_bf16 v[116:119], a[104:107], a[8:11], v[116:119]\n" \
"ds_read_b64_tr_b16 a[94:95], v233 offset:59904\n" \
"v_mfma_f32_16x16x32_bf16 v[116:119], a[108:111], a[12:15], v[116:119]\n" \
"v_mfma_f32_16x16x32_bf16 v[112:115], a[96:99], a[8:11], v[112:115]\n" \
"ds_write_b128 v225, a[224:227] offset:8192\n" \
"v_mfma_f32_16x16x32_bf16 v[112:115], a[100:103], a[12:15], v[112:115]\n" \
"s_waitcnt lgkmcnt(2)\n" \
"v_mfma_f32_16x16x32_bf16 v[64:67], a[64:67], a[16:19], v[64:67]\n" \
"s_waitcnt vmcnt(13)\n" \
"ds_read_b128 a[8:11], v230 offset:6144\n" \
"ds_read_b128 a[12:15], v231 offset:6144\n" \
"v_mfma_f32_16x16x32_bf16 v[64:67], a[68:71], a[20:23], v[64:67]\n" \
"ds_read_b64_tr_b16 a[96:97], v232 offset:50176\n" \
"ds_read_b64_tr_b16 a[98:99], v232 offset:52224\n" \
"v_mfma_f32_16x16x32_bf16 v[68:71], a[72:75], a[16:19], v[68:71]\n" \
"s_waitcnt lgkmcnt(5)\n" \
"ds_read_b64_tr_b16 a[100:101], v232 offset:58368\n" \
"ds_read_b64_tr_b16 a[102:103], v232 offset:60416\n" \
"v_mfma_f32_16x16x32_bf16 v[68:71], a[76:79], a[20:23], v[68:71]\n" \
"s_getpc_b64 s[64:65]\n" \
"Lpd1_%=:\n" \
"s_lshl_b32 s66, s62, 2\n" \
"ds_read_b64_tr_b16 a[104:105], v233 offset:50176\n" \
"ds_read_b64_tr_b16 a[106:107], v233 offset:52224\n" \
"v_mfma_f32_16x16x32_bf16 v[72:75], a[80:83], a[16:19], v[72:75]\n" \
"s_add_u32 s66, s66, s61\n" \
"ds_read_b64_tr_b16 a[108:109], v233 offset:58368\n" \
"ds_read_b64_tr_b16 a[110:111], v233 offset:60416\n" \
"v_mfma_f32_16x16x32_bf16 v[72:75], a[84:87], a[20:23], v[72:75]\n" \
"s_and_b32 s66, s66, 15\n" \
"ds_read_b64_tr_b16 a[112:113], v232 offset:50688\n" \
"ds_read_b64_tr_b16 a[114:115], v232 offset:52736\n" \
"v_mfma_f32_16x16x32_bf16 v[76:79], a[88:91], a[16:19], v[76:79]\n" \
"s_lshl_b32 s66, s66, 12\n" \
"ds_read_b64_tr_b16 a[116:117], v232 offset:58880\n" \
"ds_read_b64_tr_b16 a[118:119], v232 offset:60928\n" \
"v_mfma_f32_16x16x32_bf16 v[76:79], a[92:95], a[20:23], v[76:79]\n" \
"ds_read_b64_tr_b16 a[120:121], v233 offset:50688\n" \
"ds_read_b64_tr_b16 a[122:123], v233 offset:52736\n" \
"v_mfma_f32_16x16x32_bf16 v[96:99], a[64:67], a[24:27], v[96:99]\n" \
"ds_read_b64_tr_b16 a[124:125], v233 offset:58880\n" \
"ds_read_b64_tr_b16 a[126:127], v233 offset:60928\n" \
"v_mfma_f32_16x16x32_bf16 v[96:99], a[68:71], a[28:31], v[96:99]\n" \
"s_add_u32 s93, s94, 17850368\n" \
"s_add_u32 s64, s64, Lloop2_%=-Lpd1_%=\n" \
"s_addc_u32 s65, s65, 0\n" \
"s_mov_b32 s67, Lcode_end_%=-Lloop2_%=-4\n" \
"v_mfma_f32_16x16x32_bf16 v[100:103], a[72:75], a[24:27], v[100:103]\n" \
"buffer_store_dwordx4 v[4:7], v234, s[56:59], s93 offen offset:0 sc1\n" \
"v_mfma_f32_16x16x32_bf16 v[100:103], a[76:79], a[28:31], v[100:103]\n" \
"ds_write_b128 v225, a[228:231] offset:9216\n" \
"v_mfma_f32_16x16x32_bf16 v[104:107], a[80:83], a[24:27], v[104:107]\n" \
"buffer_load_dword v227, v242, s[56:59], s90 offen sc1\n" \
"v_mfma_f32_16x16x32_bf16 v[104:107], a[84:87], a[28:31], v[104:107]\n" \
"s_waitcnt lgkmcnt(1)\n" \
"v_lshl_add_u32 v230, v234, 2, s66\n" \
"v_min_u32 v230, s67, v230\n" \
"v_mfma_f32_16x16x32_bf16 v[108:111], a[88:91], a[24:27], v[108:111]\n" \
"ds_write_b128 v225, a[232:235] offset:10240\n" \
"v_mfma_f32_16x16x32_bf16 v[108:111], a[92:95], a[28:31], v[108:111]\n" \
"global_load_dword v230, v230, s[64:65]\n" \
"v_mfma_f32_16x16x32_bf16 v[80:83], a[96:99], a[16:19], v[80:83]\n" \
"v_cvt_pk_bf16_f32 v64, v64, v65\n" \
"v_cvt_pk_bf16_f32 v65, v66, v67\n" \
"v_mfma_f32_16x16x32_bf16 v[80:83], a[100:103], a[20:23], v[80:83]\n" \
"v_cvt_pk_bf16_f32 v66, v68, v69\n" \
"v_cvt_pk_bf16_f32 v67, v70, v71\n" \
"v_accvgpr_write_b32 a128, v64\n" \
"v_mfma_f32_16x16x32_bf16 v[84:87], a[104:107], a[16:19], v[84:87]\n" \
"v_accvgpr_write_b32 a129, v65\n" \
"v_accvgpr_write_b32 a130, v66\n" \
"v_mfma_f32_16x16x32_bf16 v[84:87], a[108:111], a[20:23], v[84:87]\n" \
"ds_write_b128 v225, a[236:239] offset:11264\n" \
"v_mfma_f32_16x16x32_bf16 v[88:91], a[112:115], a[16:19], v[88:91]\n" \
"v_accvgpr_write_b32 a131, v67\n" \
"v_cvt_pk_bf16_f32 v72, v72, v73\n" \
"v_mfma_f32_16x16x32_bf16 v[88:91], a[116:119], a[20:23], v[88:91]\n" \
"v_cvt_pk_bf16_f32 v73, v74, v75\n" \
"v_cvt_pk_bf16_f32 v74, v76, v77\n" \
"v_cvt_pk_bf16_f32 v75, v78, v79\n" \
"v_mfma_f32_16x16x32_bf16 v[92:95], a[120:123], a[16:19], v[92:95]\n" \
"v_accvgpr_write_b32 a132, v72\n" \
"v_accvgpr_write_b32 a133, v73\n" \
"v_mfma_f32_16x16x32_bf16 v[92:95], a[124:127], a[20:23], v[92:95]\n" \
"v_accvgpr_write_b32 a134, v74\n" \
"v_accvgpr_write_b32 a135, v75\n" \
"v_mfma_f32_16x16x32_bf16 v[112:115], a[96:99], a[24:27], v[112:115]\n" \
"v_cvt_pk_bf16_f32 v96, v96, v97\n" \
"v_cvt_pk_bf16_f32 v97, v98, v99\n" \
"v_mfma_f32_16x16x32_bf16 v[112:115], a[100:103], a[28:31], v[112:115]\n" \
"buffer_store_dwordx4 v[20:23], v234, s[56:59], s93 offen offset:1024 sc1\n" \
"v_mfma_f32_16x16x32_bf16 v[116:119], a[104:107], a[24:27], v[116:119]\n" \
"v_cvt_pk_bf16_f32 v98, v100, v101\n" \
"v_cvt_pk_bf16_f32 v99, v102, v103\n" \
"v_mfma_f32_16x16x32_bf16 v[116:119], a[108:111], a[28:31], v[116:119]\n" \
"v_cvt_pk_bf16_f32 v104, v104, v105\n" \
"v_cvt_pk_bf16_f32 v105, v106, v107\n" \
"v_cvt_pk_bf16_f32 v106, v108, v109\n" \
"v_mfma_f32_16x16x32_bf16 v[120:123], a[112:115], a[24:27], v[120:123]\n" \
"v_cvt_pk_bf16_f32 v107, v110, v111\n" \
"v_mfma_f32_16x16x32_bf16 v[120:123], a[116:119], a[28:31], v[120:123]\n" \
"v_cvt_pk_bf16_f32 v80, v80, v81\n" \
"v_cvt_pk_bf16_f32 v81, v82, v83\n" \
"v_mfma_f32_16x16x32_bf16 v[124:127], a[120:123], a[24:27], v[124:127]\n" \
"v_cvt_pk_bf16_f32 v82, v84, v85\n" \
"v_cvt_pk_bf16_f32 v83, v86, v87\n" \
"v_mfma_f32_16x16x32_bf16 v[124:127], a[124:127], a[28:31], v[124:127]\n" \
"v_accvgpr_write_b32 a136, v80\n" \
"s_waitcnt lgkmcnt(0)\n" \
"s_barrier\n" \
"v_mfma_f32_16x16x32_bf16 v[128:131], a[64:67], a[0:3], v[128:131]\n" \
"v_accvgpr_write_b32 a137, v81\n" \
"v_accvgpr_write_b32 a138, v82\n" \
"v_mfma_f32_16x16x32_bf16 v[128:131], a[68:71], a[4:7], v[128:131]\n" \
"ds_read_b128 a[192:195], v237 offset:0\n" \
"v_accvgpr_write_b32 a139, v83\n" \
"v_mfma_f32_16x16x32_bf16 v[132:135], a[72:75], a[0:3], v[132:135]\n" \
"v_cvt_pk_bf16_f32 v88, v88, v89\n" \
"v_cvt_pk_bf16_f32 v89, v90, v91\n" \
"v_mfma_f32_16x16x32_bf16 v[132:135], a[76:79], a[4:7], v[132:135]\n" \
"ds_read_b128 a[196:199], v237 offset:2048\n" \
"v_cvt_pk_bf16_f32 v90, v92, v93\n" \
"v_mfma_f32_16x16x32_bf16 v[136:139], a[80:83], a[0:3], v[136:139]\n" \
"v_cvt_pk_bf16_f32 v91, v94, v95\n" \
"v_med3_f32 v208, v248, v252, s72\n" \
"v_mfma_f32_16x16x32_bf16 v[136:139], a[84:87], a[4:7], v[136:139]\n" \
"ds_read_b128 a[200:203], v237 offset:4096\n" \
"v_med3_f32 v209, v249, v252, s72\n" \
"v_med3_f32 v210, v250, v252, s72\n" \
"v_mfma_f32_16x16x32_bf16 v[140:143], a[88:91], a[0:3], v[140:143]\n" \
"v_med3_f32 v211, v251, v252, s72\n" \
"v_cvt_pk_bf16_f32 v112, v112, v113\n" \
"v_mfma_f32_16x16x32_bf16 v[140:143], a[92:95], a[4:7], v[140:143]\n" \
"ds_read_b128 a[204:207], v237 offset:6144\n" \
"v_cvt_pk_bf16_f32 v113, v114, v115\n" \
"v_mfma_f32_16x16x32_bf16 v[144:147], a[96:99], a[0:3], v[144:147]\n" \
"v_cvt_pk_bf16_f32 v114, v116, v117\n" \
"v_cvt_pk_bf16_f32 v115, v118, v119\n" \
"v_mfma_f32_16x16x32_bf16 v[144:147], a[100:103], a[4:7], v[144:147]\n" \
"ds_read_b128 a[208:211], v237 offset:256\n" \
"v_cvt_pk_bf16_f32 v120, v120, v121\n" \
"v_cvt_pk_bf16_f32 v121, v122, v123\n" \
"v_mfma_f32_16x16x32_bf16 v[148:151], a[104:107], a[0:3], v[148:151]\n" \
"buffer_store_dwordx4 v[12:15], v234, s[56:59], s93 offen offset:2048 sc1\n" \
"v_mfma_f32_16x16x32_bf16 v[148:151], a[108:111], a[4:7], v[148:151]\n" \
"ds_read_b128 a[212:215], v237 offset:2304\n" \
"v_cvt_pk_bf16_f32 v122, v124, v125\n" \
"v_mfma_f32_16x16x32_bf16 v[152:155], a[112:115], a[0:3], v[152:155]\n" \
"v_cvt_pk_bf16_f32 v123, v126, v127\n" \
"v_mfma_f32_16x16x32_bf16 v[152:155], a[116:119], a[4:7], v[152:155]\n" \
"ds_read_b128 a[216:219], v237 offset:4352\n" \
"v_cvt_pk_bf16_f32 v128, v128, v129\n" \
"v_mfma_f32_16x16x32_bf16 v[156:159], a[120:123], a[0:3], v[156:159]\n" \
"v_cvt_pk_bf16_f32 v129, v130, v131\n" \
"v_cvt_pk_bf16_f32 v130, v132, v133\n" \
"v_mfma_f32_16x16x32_bf16 v[156:159], a[124:127], a[4:7], v[156:159]\n" \
"ds_read_b128 a[220:223], v237 offset:6400\n" \
"v_cvt_pk_bf16_f32 v131, v134, v135\n" \
"v_cvt_pk_bf16_f32 v136, v136, v137\n" \
"v_mfma_f32_16x16x32_bf16 v[160:163], a[64:67], a[8:11], v[160:163]\n" \
"v_cvt_pk_bf16_f32 v137, v138, v139\n" \
"v_cvt_pk_bf16_f32 v138, v140, v141\n" \
"v_mfma_f32_16x16x32_bf16 v[160:163], a[68:71], a[12:15], v[160:163]\n" \
"ds_read_b128 a[224:227], v237 offset:8192\n" \
"v_cvt_pk_bf16_f32 v139, v142, v143\n" \
"v_mfma_f32_16x16x32_bf16 v[164:167], a[72:75], a[8:11], v[164:167]\n" \
"v_mfma_f32_16x16x32_bf16 v[164:167], a[76:79], a[12:15], v[164:167]\n" \
"ds_read_b128 a[228:231], v237 offset:10240\n" \
"v_mfma_f32_16x16x32_bf16 v[168:171], a[80:83], a[8:11], v[168:171]\n" \
"v_mfma_f32_16x16x32_bf16 v[168:171], a[84:87], a[12:15], v[168:171]\n" \
"ds_read_b128 a[232:235], v237 offset:12288\n" \
"v_cvt_pk_bf16_f32 v144, v144, v145\n" \
"v_cvt_pk_bf16_f32 v145, v146, v147\n" \
"v_mfma_f32_16x16x32_bf16 v[172:175], a[88:91], a[8:11], v[172:175]\n" \
"v_cvt_pk_bf16_f32 v146, v148, v149\n" \
"v_cvt_pk_bf16_f32 v147, v150, v151\n" \
"v_mfma_f32_16x16x32_bf16 v[172:175], a[92:95], a[12:15], v[172:175]\n" \
"ds_read_b128 a[236:239], v237 offset:14336\n" \
"v_cvt_pk_bf16_f32 v152, v152, v153\n" \
"v_mfma_f32_16x16x32_bf16 v[176:179], a[96:99], a[8:11], v[176:179]\n" \
"v_cvt_pk_bf16_f32 v153, v154, v155\n" \
"v_cvt_pk_bf16_f32 v154, v156, v157\n" \
"v_mfma_f32_16x16x32_bf16 v[176:179], a[100:103], a[12:15], v[176:179]\n" \
"ds_read_b128 a[240:243], v237 offset:8448\n" \
"v_cvt_pk_bf16_f32 v155, v158, v159\n" \
"v_mfma_f32_16x16x32_bf16 v[180:183], a[104:107], a[8:11], v[180:183]\n" \
"buffer_store_dwordx4 v[28:31], v234, s[56:59], s93 offen offset:3072 sc1\n" \
"v_mfma_f32_16x16x32_bf16 v[180:183], a[108:111], a[12:15], v[180:183]\n" \
"ds_read_b128 a[244:247], v237 offset:10496\n" \
"ds_write_b128 v239, v[128:131] offset:0\n" \
"v_mov_b32 v196, 0x42400000\n" \
"v_mfma_f32_16x16x32_bf16 v[184:187], a[112:115], a[8:11], v[184:187]\n" \
"v_mov_b32 v197, 0x42400000\n" \
"v_mov_b32 v198, 0x42400000\n" \
"v_mfma_f32_16x16x32_bf16 v[184:187], a[116:119], a[12:15], v[184:187]\n" \
"ds_read_b128 a[248:251], v237 offset:12544\n" \
"v_mov_b32 v199, 0x42400000\n" \
"v_cvt_pk_bf16_f32 v160, v160, v161\n" \
"v_mfma_f32_16x16x32_bf16 v[188:191], a[120:123], a[8:11], v[188:191]\n" \
"v_cvt_pk_bf16_f32 v161, v162, v163\n" \
"v_cvt_pk_bf16_f32 v162, v164, v165\n" \
"v_mfma_f32_16x16x32_bf16 v[188:191], a[124:127], a[12:15], v[188:191]\n" \
"s_waitcnt lgkmcnt(3)\n" \
"ds_read_b128 a[252:255], v237 offset:14592\n" \
"v_cvt_pk_bf16_f32 v163, v166, v167\n" \
"v_mfma_f32_16x16x32_bf16 v[16:19], a[192:195], v[96:99], v[208:211]\n" \
"v_cvt_pk_bf16_f32 v168, v168, v169\n" \
"v_cvt_pk_bf16_f32 v169, v170, v171\n" \
"v_mfma_f32_16x16x32_bf16 v[16:19], a[196:199], v[104:107], v[16:19]\n" \
"v_cvt_pk_bf16_f32 v170, v172, v173\n" \
"v_cvt_pk_bf16_f32 v171, v174, v175\n" \
"v_mfma_f32_16x16x32_bf16 v[16:19], a[200:203], v[112:115], v[16:19]\n" \
"v_cvt_pk_bf16_f32 v176, v176, v177\n" \
"v_cvt_pk_bf16_f32 v177, v178, v179\n" \
"v_mfma_f32_16x16x32_bf16 v[16:19], a[204:207], v[120:123], v[16:19]\n" \
"v_cvt_pk_bf16_f32 v178, v180, v181\n" \
"v_cvt_pk_bf16_f32 v179, v182, v183\n" \
"v_mfma_f32_16x16x32_bf16 v[32:35], a[204:207], v[152:155], v[196:199]\n" \
"v_cvt_pk_bf16_f32 v184, v184, v185\n" \
"v_cvt_pk_bf16_f32 v185, v186, v187\n" \
"v_mfma_f32_16x16x32_bf16 v[32:35], a[200:203], v[144:147], v[32:35]\n" \
"Lpg5013_%=:\n" \
"s_cbranch_execz Lpg6196_%=\n" \
"v_cvt_pk_bf16_f32 v186, v188, v189\n" \
"v_cvt_pk_bf16_f32 v187, v190, v191\n" \
"v_mfma_f32_16x16x32_bf16 v[32:35], a[196:199], v[136:139], v[32:35]\n" \
"v_med3_f32 v212, v248, v253, v252\n" \
"v_med3_f32 v213, v249, v253, v252\n" \
"v_mfma_f32_16x16x32_bf16 v[32:35], a[192:195], v[128:131], v[32:35]\n" \
"v_med3_f32 v214, v250, v253, v252\n" \
"v_med3_f32 v215, v251, v253, v252\n" \
"v_mfma_f32_16x16x32_bf16 v[48:51], a[192:195], v[160:163], v[196:199]\n" \
"v_med3_f32 v216, v248, v226, v253\n" \
"v_med3_f32 v217, v249, v226, v253\n" \
"v_mfma_f32_16x16x32_bf16 v[48:51], a[196:199], v[168:171], v[48:51]\n" \
"ds_read_b128 a[192:195], v237 offset:16384\n" \
"v_med3_f32 v218, v250, v226, v253\n" \
"v_mfma_f32_16x16x32_bf16 v[48:51], a[200:203], v[176:179], v[48:51]\n" \
"ds_read_b128 a[196:199], v237 offset:18432\n" \
"v_med3_f32 v219, v251, v226, v253\n" \
"v_mfma_f32_16x16x32_bf16 v[48:51], a[204:207], v[184:187], v[48:51]\n" \
"ds_read_b128 a[200:203], v237 offset:20480\n" \
"v_med3_f32 v220, v248, v240, v226\n" \
"v_mfma_f32_16x16x32_bf16 v[52:55], a[220:223], v[184:187], v[196:199]\n" \
"ds_read_b128 a[204:207], v237 offset:22528\n" \
"v_med3_f32 v221, v249, v240, v226\n" \
"v_mfma_f32_16x16x32_bf16 v[52:55], a[216:219], v[176:179], v[52:55]\n" \
"ds_write_b128 v239, v[136:139] offset:1024\n" \
"v_mfma_f32_16x16x32_bf16 v[52:55], a[212:215], v[168:171], v[52:55]\n" \
"v_med3_f32 v222, v250, v240, v226\n" \
"v_med3_f32 v223, v251, v240, v226\n" \
"v_mfma_f32_16x16x32_bf16 v[52:55], a[208:211], v[160:163], v[52:55]\n" \
"v_accvgpr_write_b32 a144, v96\n" \
"v_accvgpr_write_b32 a145, v97\n" \
"v_mfma_f32_16x16x32_bf16 v[36:39], a[208:211], v[128:131], v[196:199]\n" \
"ds_write_b128 v239, v[144:147] offset:2048\n" \
"v_mfma_f32_16x16x32_bf16 v[36:39], a[212:215], v[136:139], v[36:39]\n" \
"v_accvgpr_write_b32 a146, v98\n" \
"v_accvgpr_write_b32 a147, v99\n" \
"v_mfma_f32_16x16x32_bf16 v[36:39], a[216:219], v[144:147], v[36:39]\n" \
"v_accvgpr_write_b32 a148, v104\n" \
"v_accvgpr_write_b32 a149, v105\n" \
"v_mfma_f32_16x16x32_bf16 v[36:39], a[220:223], v[152:155], v[36:39]\n" \
"ds_write_b128 v239, v[152:155] offset:3072\n" \
"v_mfma_f32_16x16x32_bf16 v[20:23], a[220:223], v[120:123], v[212:215]\n" \
"v_accvgpr_write_b32 a150, v106\n" \
"v_accvgpr_write_b32 a151, v107\n" \
"v_mfma_f32_16x16x32_bf16 v[20:23], a[216:219], v[112:115], v[20:23]\n" \
"ds_read_b128 a[220:223], v237 offset:22784\n" \
"v_accvgpr_write_b32 a152, v112\n" \
"v_mfma_f32_16x16x32_bf16 v[20:23], a[212:215], v[104:107], v[20:23]\n" \
"ds_read_b128 a[216:219], v237 offset:20736\n" \
"v_accvgpr_write_b32 a153, v113\n" \
"v_mfma_f32_16x16x32_bf16 v[20:23], a[208:211], v[96:99], v[20:23]\n" \
"ds_read_b128 a[212:215], v237 offset:18688\n" \
"v_accvgpr_write_b32 a154, v114\n" \
"v_mfma_f32_16x16x32_bf16 v[24:27], a[224:227], v[96:99], v[216:219]\n" \
"ds_read_b128 a[208:211], v237 offset:16640\n" \
"v_accvgpr_write_b32 a155, v115\n" \
"v_mfma_f32_16x16x32_bf16 v[24:27], a[228:231], v[104:107], v[24:27]\n" \
"ds_write_b128 v239, v[160:163] offset:4096\n" \
"v_mfma_f32_16x16x32_bf16 v[24:27], a[232:235], v[112:115], v[24:27]\n" \
"v_accvgpr_write_b32 a156, v120\n" \
"v_accvgpr_write_b32 a157, v121\n" \
"v_mfma_f32_16x16x32_bf16 v[24:27], a[236:239], v[120:123], v[24:27]\n" \
"v_accvgpr_write_b32 a158, v122\n" \
"v_accvgpr_write_b32 a159, v123\n" \
"v_mfma_f32_16x16x32_bf16 v[40:43], a[236:239], v[152:155], v[196:199]\n" \
"ds_write_b128 v239, v[168:171] offset:5120\n" \
"v_mfma_f32_16x16x32_bf16 v[40:43], a[232:235], v[144:147], v[40:43]\n" \
"v_accvgpr_write_b32 a140, v88\n" \
"v_accvgpr_write_b32 a141, v89\n" \
"v_mfma_f32_16x16x32_bf16 v[40:43], a[228:231], v[136:139], v[40:43]\n" \
"v_accvgpr_write_b32 a142, v90\n" \
"v_accvgpr_write_b32 a143, v91\n" \
"v_mfma_f32_16x16x32_bf16 v[40:43], a[224:227], v[128:131], v[40:43]\n" \
"ds_write_b128 v239, v[176:179] offset:6144\n" \
"v_mfma_f32_16x16x32_bf16 v[56:59], a[224:227], v[160:163], v[196:199]\n" \
"v_add_u32 v233, s82, v235\n" \
"v_mfma_f32_16x16x32_bf16 v[56:59], a[228:231], v[168:171], v[56:59]\n" \
"ds_read_b128 a[224:227], v237 offset:24576\n" \
"v_mfma_f32_16x16x32_bf16 v[56:59], a[232:235], v[176:179], v[56:59]\n" \
"ds_read_b128 a[228:231], v237 offset:26624\n" \
"ds_write_b128 v239, v[184:187] offset:7168\n" \
"v_exp_f32 v16, v16\n" \
"v_mfma_f32_16x16x32_bf16 v[56:59], a[236:239], v[184:187], v[56:59]\n" \
"s_waitcnt lgkmcnt(4)\n" \
"ds_read_b128 a[232:235], v237 offset:28672\n" \
"v_exp_f32 v17, v17\n" \
"v_mfma_f32_16x16x32_bf16 v[60:63], a[252:255], v[184:187], v[196:199]\n" \
"ds_read_b128 a[236:239], v237 offset:30720\n" \
"v_mfma_f32_16x16x32_bf16 v[60:63], a[248:251], v[176:179], v[60:63]\n" \
"s_waitcnt vmcnt(4)\n" \
"v_cmp_eq_u32 vcc, s60, v227\n" \
"v_mfma_f32_16x16x32_bf16 v[60:63], a[244:247], v[168:171], v[60:63]\n" \
"v_exp_f32 v18, v18\n" \
"v_mfma_f32_16x16x32_bf16 v[60:63], a[240:243], v[160:163], v[60:63]\n" \
"s_andn2_b32 s70, 0xff, vcc_lo\n" \
"s_cbranch_scc1 Lpl1_%=\n" \
"Lpr1_%=:\n" \
"v_cvt_pk_bf16_f32 v16, v16, v17\n" \
"v_exp_f32 v19, v19\n" \
"v_exp_f32 v20, v20\n" \
"v_mfma_f32_16x16x32_bf16 v[44:47], a[240:243], v[128:131], v[196:199]\n" \
"v_exp_f32 v21, v21\n" \
"v_mfma_f32_16x16x32_bf16 v[44:47], a[244:247], v[136:139], v[44:47]\n" \
"s_add_u32 m0, s79, 32768\n" \
"v_exp_f32 v22, v22\n" \
"v_mfma_f32_16x16x32_bf16 v[44:47], a[248:251], v[144:147], v[44:47]\n" \
"s_add_u32 s71, s76, 0\n" \
"v_cvt_pk_bf16_f32 v17, v18, v19\n" \
"v_cvt_pk_bf16_f32 v18, v20, v21\n" \
"v_mfma_f32_16x16x32_bf16 v[44:47], a[252:255], v[152:155], v[44:47]\n" \
"buffer_load_dwordx4 v234, s[36:39], s71 offen offset:0 sc1 lds\n" \
"v_exp_f32 v23, v23\n" \
"v_exp_f32 v32, v32\n" \
"v_mfma_f32_16x16x32_bf16 v[28:31], a[252:255], v[120:123], v[220:223]\n" \
"v_exp_f32 v33, v33\n" \
"v_mfma_f32_16x16x32_bf16 v[28:31], a[248:251], v[112:115], v[28:31]\n" \
"ds_read_b128 a[252:255], v237 offset:30976\n" \
"v_cvt_pk_bf16_f32 v19, v22, v23\n" \
"v_mfma_f32_16x16x32_bf16 v[28:31], a[244:247], v[104:107], v[28:31]\n" \
"buffer_load_dwordx4 v234, s[36:39], s71 offen offset:1024 sc1 lds\n" \
"v_mfma_f32_16x16x32_bf16 v[28:31], a[240:243], v[96:99], v[28:31]\n" \
"ds_read_b128 a[244:247], v237 offset:26880\n" \
"ds_read_b128 a[248:251], v237 offset:28928\n" \
"v_exp_f32 v34, v34\n" \
"v_exp_f32 v35, v35\n" \
"v_mfma_f32_16x16x32_bf16 v[80:83], a[192:195], a[144:147], v[196:199]\n" \
"ds_read_b128 a[240:243], v237 offset:24832\n" \
"v_cvt_pk_bf16_f32 v32, v32, v33\n" \
"v_mfma_f32_16x16x32_bf16 v[80:83], a[196:199], a[148:151], v[80:83]\n" \
"buffer_load_dwordx4 v234, s[36:39], s71 offen offset:2048 sc1 lds\n" \
"v_mfma_f32_16x16x32_bf16 v[80:83], a[200:203], a[152:155], v[80:83]\n" \
"s_lshl_b32 s68, s63, 2\n" \
"s_add_u32 s68, s68, s95\n" \
"s_mov_b32 s69, 0\n" \
"v_mfma_f32_16x16x32_bf16 v[80:83], a[204:207], a[156:159], v[80:83]\n" \
"s_mov_b32 s89, s63\n" \
"ds_read_b128 a[44:47], v233 offset:15360\n" \
"v_mov_b32 v192, s60\n" \
"v_lshlrev_b32 v193, 28, s61\n" \
"v_exp_f32 v36, v36\n" \
"v_exp_f32 v37, v37\n" \
"v_mfma_f32_16x16x32_bf16 v[96:99], a[204:207], v[152:155], v[196:199]\n" \
"buffer_load_dwordx4 v234, s[36:39], s71 offen offset:3072 sc1 lds\n" \
"v_mfma_f32_16x16x32_bf16 v[96:99], a[200:203], v[144:147], v[96:99]\n" \
"s_add_u32 m0, s79, 49152\n" \
"s_mov_b32 s90, 0\n" \
"ds_read_b128 a[40:43], v233 offset:7168\n" \
"v_mfma_f32_16x16x32_bf16 v[96:99], a[196:199], v[136:139], v[96:99]\n" \
"s_add_u32 s71, s76, 17408\n" \
"v_exp_f32 v38, v38\n" \
"v_mfma_f32_16x16x32_bf16 v[96:99], a[192:195], v[128:131], v[96:99]\n" \
"buffer_load_dwordx4 v234, s[36:39], s71 offen offset:0 sc1 lds\n" \
"v_cvt_pk_bf16_f32 v33, v34, v35\n" \
"v_exp_f32 v39, v39\n" \
"v_mfma_f32_16x16x32_bf16 v[112:115], a[192:195], v[160:163], v[196:199]\n" \
"ds_read_b128 a[36:39], v233 offset:14336\n" \
"v_cvt_pk_bf16_f32 v34, v36, v37\n" \
"v_mfma_f32_16x16x32_bf16 v[112:115], a[196:199], v[168:171], v[112:115]\n" \
"v_exp_f32 v48, v48\n" \
"v_mfma_f32_16x16x32_bf16 v[112:115], a[200:203], v[176:179], v[112:115]\n" \
"buffer_load_dwordx4 v234, s[36:39], s71 offen offset:1024 sc1 lds\n" \
"v_mfma_f32_16x16x32_bf16 v[112:115], a[204:207], v[184:187], v[112:115]\n" \
"ds_read_b128 a[32:35], v233 offset:6144\n" \
"v_cvt_pk_bf16_f32 v35, v38, v39\n" \
"v_exp_f32 v49, v49\n" \
"v_exp_f32 v50, v50\n" \
"v_mfma_f32_16x16x32_bf16 v[64:67], a[204:207], a[140:143], v[208:211]\n" \
"v_exp_f32 v51, v51\n" \
"v_mfma_f32_16x16x32_bf16 v[64:67], a[200:203], a[136:139], v[64:67]\n" \
"buffer_load_dwordx4 v234, s[36:39], s71 offen offset:2048 sc1 lds\n" \
"v_mfma_f32_16x16x32_bf16 v[64:67], a[196:199], a[132:135], v[64:67]\n" \
"ds_read_b128 a[204:207], v233 offset:9216\n" \
"v_cvt_pk_bf16_f32 v48, v48, v49\n" \
"v_mfma_f32_16x16x32_bf16 v[64:67], a[192:195], a[128:131], v[64:67]\n" \
"v_exp_f32 v52, v52\n" \
"v_exp_f32 v53, v53\n" \
"v_exp_f32 v54, v54\n" \
"v_mfma_f32_16x16x32_bf16 v[68:71], a[208:211], a[128:131], v[212:215]\n" \
"buffer_load_dwordx4 v234, s[36:39], s71 offen offset:3072 sc1 lds\n" \
"v_mfma_f32_16x16x32_bf16 v[68:71], a[212:215], a[132:135], v[68:71]\n" \
"ds_read_b128 a[200:203], v233 offset:1024\n" \
"v_cvt_pk_bf16_f32 v49, v50, v51\n" \
"v_mfma_f32_16x16x32_bf16 v[68:71], a[216:219], a[136:139], v[68:71]\n" \
"s_add_u32 m0, s77, s80\n" \
"v_exp_f32 v55, v55\n" \
"v_cvt_pk_bf16_f32 v50, v52, v53\n" \
"v_mfma_f32_16x16x32_bf16 v[68:71], a[220:223], a[140:143], v[68:71]\n" \
"buffer_load_dwordx4 v234, s[40:43], s76 offen offset:0 sc1 lds\n" \
"v_cvt_pk_bf16_f32 v51, v54, v55\n" \
"v_mfma_f32_16x16x32_bf16 v[116:119], a[220:223], v[184:187], v[196:199]\n" \
"ds_read_b128 a[196:199], v233 offset:8192\n" \
"v_mfma_f32_16x16x32_bf16 v[116:119], a[216:219], v[176:179], v[116:119]\n" \
"ds_read_b128 a[160:163], v239 offset:0\n" \
"v_mfma_f32_16x16x32_bf16 v[116:119], a[212:215], v[168:171], v[116:119]\n" \
"s_add_u32 s71, s76, 17408\n" \
"ds_read_b128 a[192:195], v233 offset:0\n" \
"v_exp_f32 v24, v24\n" \
"v_mfma_f32_16x16x32_bf16 v[116:119], a[208:211], v[160:163], v[116:119]\n" \
"buffer_load_dwordx4 v234, s[40:43], s76 offen offset:1024 sc1 lds\n" \
"v_mfma_f32_16x16x32_bf16 v[100:103], a[208:211], v[128:131], v[196:199]\n" \
"ds_read_b128 a[164:167], v239 offset:1024\n" \
"v_mfma_f32_16x16x32_bf16 v[100:103], a[212:215], v[136:139], v[100:103]\n" \
"v_exp_f32 v25, v25\n" \
"v_mfma_f32_16x16x32_bf16 v[100:103], a[216:219], v[144:147], v[100:103]\n" \
"v_exp_f32 v26, v26\n" \
"v_mfma_f32_16x16x32_bf16 v[100:103], a[220:223], v[152:155], v[100:103]\n" \
"buffer_load_dwordx4 v234, s[40:43], s76 offen offset:2048 sc1 lds\n" \
"v_exp_f32 v27, v27\n" \
"v_exp_f32 v28, v28\n" \
"v_mfma_f32_16x16x32_bf16 v[84:87], a[220:223], a[156:159], v[196:199]\n" \
"ds_read_b128 a[168:171], v239 offset:2048\n" \
"v_cvt_pk_bf16_f32 v20, v24, v25\n" \
"v_mfma_f32_16x16x32_bf16 v[84:87], a[216:219], a[152:155], v[84:87]\n" \
"ds_read_b128 a[220:223], v233 offset:11264\n" \
"v_cvt_pk_bf16_f32 v21, v26, v27\n" \
"v_mfma_f32_16x16x32_bf16 v[84:87], a[212:215], a[148:151], v[84:87]\n" \
"s_waitcnt lgkmcnt(2)\n" \
"v_exp_f32 v29, v29\n" \
"v_mfma_f32_16x16x32_bf16 v[84:87], a[208:211], a[144:147], v[84:87]\n" \
"buffer_load_dwordx4 v234, s[40:43], s76 offen offset:3072 sc1 lds\n" \
"v_exp_f32 v30, v30\n" \
"v_exp_f32 v31, v31\n" \
"v_mfma_f32_16x16x32_bf16 v[88:91], a[224:227], a[144:147], v[196:199]\n" \
"ds_read_b128 a[216:219], v233 offset:3072\n" \
"v_cvt_pk_bf16_f32 v22, v28, v29\n" \
"v_mfma_f32_16x16x32_bf16 v[88:91], a[228:231], a[148:151], v[88:91]\n" \
"ds_read_b128 a[172:175], v239 offset:3072\n" \
"v_mfma_f32_16x16x32_bf16 v[88:91], a[232:235], a[152:155], v[88:91]\n" \
"s_add_u32 m0, m0, 0x4000\n" \
"ds_read_b128 a[212:215], v233 offset:10240\n" \
"v_xor_b32 v248, 0x2000, v239\n" \
"v_mfma_f32_16x16x32_bf16 v[88:91], a[236:239], a[156:159], v[88:91]\n" \
"buffer_load_dwordx4 v234, s[40:43], s71 offen offset:0 sc1 lds\n" \
"v_exp_f32 v40, v40\n" \
"v_exp_f32 v41, v41\n" \
"v_mfma_f32_16x16x32_bf16 v[104:107], a[236:239], v[152:155], v[196:199]\n" \
"ds_read_b128 a[208:211], v233 offset:2048\n" \
"v_lshlrev_b32 v252, 3, v234\n" \
"v_mfma_f32_16x16x32_bf16 v[104:107], a[232:235], v[144:147], v[104:107]\n" \
"ds_read_b128 a[176:179], v239 offset:4096\n" \
"v_mov_b32 v224, 0\n" \
"v_mfma_f32_16x16x32_bf16 v[104:107], a[228:231], v[136:139], v[104:107]\n" \
"v_mov_b32 v225, 0\n" \
"v_mov_b32 v228, 0\n" \
"v_mfma_f32_16x16x32_bf16 v[104:107], a[224:227], v[128:131], v[104:107]\n" \
"buffer_load_dwordx4 v234, s[40:43], s71 offen offset:1024 sc1 lds\n" \
"v_exp_f32 v42, v42\n" \
"v_cvt_pk_bf16_f32 v23, v30, v31\n" \
"v_mfma_f32_16x16x32_bf16 v[120:123], a[224:227], v[160:163], v[196:199]\n" \
"ds_read_b128 a[180:183], v239 offset:5120\n" \
"v_mov_b32 v229, 0\n" \
"v_mfma_f32_16x16x32_bf16 v[120:123], a[228:231], v[168:171], v[120:123]\n" \
"v_exp_f32 v43, v43\n" \
"v_mfma_f32_16x16x32_bf16 v[120:123], a[232:235], v[176:179], v[120:123]\n" \
"v_exp_f32 v44, v44\n" \
"v_cvt_pk_bf16_f32 v36, v40, v41\n" \
"v_mfma_f32_16x16x32_bf16 v[120:123], a[236:239], v[184:187], v[120:123]\n" \
"buffer_load_dwordx4 v234, s[40:43], s71 offen offset:2048 sc1 lds\n" \
"v_exp_f32 v45, v45\n" \
"v_mfma_f32_16x16x32_bf16 v[72:75], a[236:239], a[140:143], v[216:219]\n" \
"ds_read_b128 a[184:187], v239 offset:6144\n" \
"v_cvt_pk_bf16_f32 v37, v42, v43\n" \
"v_mfma_f32_16x16x32_bf16 v[72:75], a[232:235], a[136:139], v[72:75]\n" \
"ds_read_b128 a[236:239], v233 offset:13312\n" \
"v_mfma_f32_16x16x32_bf16 v[72:75], a[228:231], a[132:135], v[72:75]\n" \
"v_exp_f32 v46, v46\n" \
"v_mfma_f32_16x16x32_bf16 v[72:75], a[224:227], a[128:131], v[72:75]\n" \
"buffer_load_dwordx4 v234, s[40:43], s71 offen offset:3072 sc1 lds\n" \
"v_exp_f32 v47, v47\n" \
"v_mfma_f32_16x16x32_bf16 v[76:79], a[240:243], a[128:131], v[220:223]\n" \
"ds_read_b128 a[232:235], v233 offset:5120\n" \
"v_cvt_pk_bf16_f32 v38, v44, v45\n" \
"v_mfma_f32_16x16x32_bf16 v[76:79], a[244:247], a[132:135], v[76:79]\n" \
"ds_read_b128 a[188:191], v239 offset:7168\n" \
"v_exp_f32 v56, v56\n" \
"v_mfma_f32_16x16x32_bf16 v[76:79], a[248:251], a[136:139], v[76:79]\n" \
"ds_read_b128 a[228:231], v233 offset:12288\n" \
"v_cvt_pk_bf16_f32 v39, v46, v47\n" \
"v_mfma_f32_16x16x32_bf16 v[76:79], a[252:255], a[140:143], v[76:79]\n" \
"v_exp_f32 v57, v57\n" \
"v_exp_f32 v58, v58\n" \
"v_mfma_f32_16x16x32_bf16 v[124:127], a[252:255], v[184:187], v[196:199]\n" \
"ds_read_b128 a[224:227], v233 offset:4096\n" \
"v_cvt_pk_bf16_f32 v52, v56, v57\n" \
"v_mfma_f32_16x16x32_bf16 v[124:127], a[248:251], v[176:179], v[124:127]\n" \
"v_exp_f32 v59, v59\n" \
"v_mfma_f32_16x16x32_bf16 v[124:127], a[244:247], v[168:171], v[124:127]\n" \
"v_exp_f32 v60, v60\n" \
"v_mfma_f32_16x16x32_bf16 v[124:127], a[240:243], v[160:163], v[124:127]\n" \
"v_exp_f32 v61, v61\n" \
"v_exp_f32 v62, v62\n" \
"v_cvt_pk_bf16_f32 v53, v58, v59\n" \
"v_mfma_f32_16x16x32_bf16 v[108:111], a[240:243], v[128:131], v[196:199]\n" \
"v_exp_f32 v63, v63\n" \
"v_mfma_f32_16x16x32_bf16 v[108:111], a[244:247], v[136:139], v[108:111]\n" \
"v_cvt_pk_bf16_f32 v54, v60, v61\n" \
"v_mfma_f32_16x16x32_bf16 v[108:111], a[248:251], v[144:147], v[108:111]\n" \
"v_cvt_pk_bf16_f32 v55, v62, v63\n" \
"v_mfma_f32_16x16x32_bf16 v[108:111], a[252:255], v[152:155], v[108:111]\n" \
"v_exp_f32 v80, v80\n" \
"v_mfma_f32_16x16x32_bf16 v[92:95], a[252:255], a[156:159], v[196:199]\n" \
"v_add_u32 v232, s91, v237\n" \
"v_mfma_f32_16x16x32_bf16 v[92:95], a[248:251], a[152:155], v[92:95]\n" \
"v_exp_f32 v81, v81\n" \
"v_mfma_f32_16x16x32_bf16 v[92:95], a[244:247], a[148:151], v[92:95]\n" \
"s_waitcnt vmcnt(8) lgkmcnt(0)\n" \
"s_barrier\n" \
"v_mfma_f32_16x16x32_bf16 v[92:95], a[240:243], a[144:147], v[92:95]\n" \
"buffer_store_dword v192, v193, s[56:59], s68 offen sc1\n" \
"v_exp_f32 v82, v82\n" \
"v_mfma_f32_16x16x32_bf16 v[160:163], a[192:195], v[16:19], 0\n" \
"s_add_u32 m0, s74, s79\n" \
"ds_read_b128 a[240:243], v232 offset:8448\n" \
"v_xor_b32 v233, 0x4000, v233\n" \
"v_mfma_f32_16x16x32_bf16 v[160:163], a[196:199], v[20:23], v[160:163]\n" \
"buffer_load_dwordx4 v234, s[36:39], s73 offen offset:0 sc1 lds\n" \
"v_mfma_f32_16x16x32_bf16 a[64:67], a[192:195], v[32:35], 0\n" \
"ds_read_b128 a[244:247], v232 offset:10496\n" \
"v_cvt_pk_bf16_f32 v80, v80, v81\n" \
"v_mfma_f32_16x16x32_bf16 a[64:67], a[196:199], v[36:39], a[64:67]\n" \
"ds_read_b128 a[0:3], v248 offset:0\n" \
"v_exp_f32 v83, v83\n" \
"v_mfma_f32_16x16x32_bf16 a[96:99], a[192:195], v[48:51], 0\n" \
"v_exp_f32 v84, v84\n" \
"v_mfma_f32_16x16x32_bf16 a[96:99], a[196:199], v[52:55], a[96:99]\n" \
"s_add_u32 s71, s73, 17408\n" \
"ds_read_b128 a[192:195], v232 offset:0\n" \
"v_cvt_pk_bf16_f32 v81, v82, v83\n" \
"v_mfma_f32_16x16x32_bf16 a[100:103], a[200:203], v[48:51], 0\n" \
"buffer_load_dwordx4 v234, s[36:39], s73 offen offset:1024 sc1 lds\n" \
"v_mfma_f32_16x16x32_bf16 a[100:103], a[204:207], v[52:55], a[100:103]\n" \
"ds_read_b128 a[196:199], v232 offset:2048\n" \
"v_exp_f32 v85, v85\n" \
"v_mfma_f32_16x16x32_bf16 a[68:71], a[200:203], v[32:35], 0\n" \
"ds_read_b128 a[4:7], v248 offset:1024\n" \
"v_mfma_f32_16x16x32_bf16 a[68:71], a[204:207], v[36:39], a[68:71]\n" \
"v_exp_f32 v86, v86\n" \
"v_mfma_f32_16x16x32_bf16 v[164:167], a[200:203], v[16:19], 0\n" \
"ds_read_b128 a[8:11], v248 offset:2048\n" \
"v_cvt_pk_bf16_f32 v82, v84, v85\n" \
"v_mfma_f32_16x16x32_bf16 v[164:167], a[204:207], v[20:23], v[164:167]\n" \
"buffer_load_dwordx4 v234, s[36:39], s73 offen offset:2048 sc1 lds\n" \
"v_exp_f32 v87, v87\n" \
"v_mfma_f32_16x16x32_bf16 v[168:171], a[208:211], v[16:19], 0\n" \
"ds_read_b128 a[200:203], v232 offset:4096\n" \
"v_mfma_f32_16x16x32_bf16 v[168:171], a[212:215], v[20:23], v[168:171]\n" \
"ds_read_b128 a[204:207], v232 offset:6144\n" \
"v_exp_f32 v96, v96\n" \
"v_mfma_f32_16x16x32_bf16 a[72:75], a[208:211], v[32:35], 0\n" \
"ds_read_b128 a[12:15], v248 offset:3072\n" \
"v_cvt_pk_bf16_f32 v83, v86, v87\n" \
"v_mfma_f32_16x16x32_bf16 a[72:75], a[212:215], v[36:39], a[72:75]\n" \
"v_exp_f32 v97, v97\n" \
"v_mfma_f32_16x16x32_bf16 a[104:107], a[208:211], v[48:51], 0\n" \
"buffer_load_dwordx4 v234, s[36:39], s73 offen offset:3072 sc1 lds\n" \
"v_mfma_f32_16x16x32_bf16 a[104:107], a[212:215], v[52:55], a[104:107]\n" \
"ds_read_b128 a[208:211], v232 offset:256\n" \
"v_exp_f32 v98, v98\n" \
"v_smfmac_f32_16x16x64_bf16 a[52:55], v[244:247], v[16:23], v243\n" \
"ds_read_b128 a[212:215], v232 offset:2304\n" \
"v_cvt_pk_bf16_f32 v96, v96, v97\n" \
"v_mfma_f32_16x16x32_bf16 a[108:111], a[216:219], v[48:51], 0\n" \
"ds_read_b128 a[16:19], v248 offset:4096\n" \
"v_mfma_f32_16x16x32_bf16 a[108:111], a[220:223], v[52:55], a[108:111]\n" \
"s_add_u32 m0, m0, 0x4000\n" \
"v_exp_f32 v99, v99\n" \
"v_mfma_f32_16x16x32_bf16 a[76:79], a[216:219], v[32:35], 0\n" \
"buffer_load_dwordx4 v234, s[36:39], s71 offen offset:0 sc1 lds\n" \
"v_mfma_f32_16x16x32_bf16 a[76:79], a[220:223], v[36:39], a[76:79]\n" \
"ds_read_b128 a[20:23], v248 offset:5120\n" \
"v_exp_f32 v100, v100\n" \
"v_mfma_f32_16x16x32_bf16 v[172:175], a[216:219], v[16:19], 0\n" \
"v_exp_f32 v101, v101\n" \
"v_mfma_f32_16x16x32_bf16 v[172:175], a[220:223], v[20:23], v[172:175]\n" \
"ds_read_b128 a[216:219], v232 offset:4352\n" \
"v_cvt_pk_bf16_f32 v97, v98, v99\n" \
"v_mfma_f32_32x32x16_bf16 v[128:143], a[48:51], a[48:51], 0\n" \
"ds_read_b128 a[220:223], v232 offset:6400\n" \
"v_exp_f32 v102, v102\n" \
"v_mfma_f32_16x16x32_bf16 v[176:179], a[224:227], v[16:19], 0\n" \
"buffer_load_dwordx4 v234, s[36:39], s71 offen offset:1024 sc1 lds\n" \
"v_mfma_f32_16x16x32_bf16 v[176:179], a[228:231], v[20:23], v[176:179]\n" \
"ds_read_b128 a[24:27], v248 offset:6144\n" \
"v_cvt_pk_bf16_f32 v98, v100, v101\n" \
"v_mfma_f32_16x16x32_bf16 a[80:83], a[224:227], v[32:35], 0\n" \
"v_exp_f32 v103, v103\n" \
"v_mfma_f32_16x16x32_bf16 a[80:83], a[228:231], v[36:39], a[80:83]\n" \
"ds_read_b128 a[28:31], v248 offset:7168\n" \
"v_exp_f32 v112, v112\n" \
"v_mfma_f32_16x16x32_bf16 a[112:115], a[224:227], v[48:51], 0\n" \
"v_exp_f32 v113, v113\n" \
"v_mfma_f32_16x16x32_bf16 a[112:115], a[228:231], v[52:55], a[112:115]\n" \
"buffer_load_dwordx4 v234, s[36:39], s71 offen offset:2048 sc1 lds\n" \
"v_exp_f32 v114, v114\n" \
"v_smfmac_f32_16x16x64_bf16 v[224:227], v[244:247], v[32:39], v243\n" \
"ds_read_b128 a[224:227], v232 offset:8192\n" \
"v_cvt_pk_bf16_f32 v99, v102, v103\n" \
"v_mfma_f32_16x16x32_bf16 a[116:119], a[232:235], v[48:51], 0\n" \
"ds_read_b128 a[228:231], v232 offset:10240\n" \
"v_cvt_pk_bf16_f32 v112, v112, v113\n" \
"v_mfma_f32_16x16x32_bf16 a[116:119], a[236:239], v[52:55], a[116:119]\n" \
"v_exp_f32 v115, v115\n" \
"v_mfma_f32_16x16x32_bf16 a[84:87], a[232:235], v[32:35], 0\n" \
"v_exp_f32 v116, v116\n" \
"v_mfma_f32_16x16x32_bf16 a[84:87], a[236:239], v[36:39], a[84:87]\n" \
"buffer_load_dwordx4 v234, s[36:39], s71 offen offset:3072 sc1 lds\n" \
"v_exp_f32 v117, v117\n" \
"v_mfma_f32_16x16x32_bf16 v[180:183], a[232:235], v[16:19], 0\n" \
"v_exp_f32 v118, v118\n" \
"v_mfma_f32_16x16x32_bf16 v[180:183], a[236:239], v[20:23], v[180:183]\n" \
"s_mov_b32 s76, s73\n" \
"s_add_u32 s73, s73, 34816\n" \
"ds_read_b128 a[232:235], v232 offset:12288\n" \
"v_mfma_f32_32x32x16_bf16 v[144:159], a[48:51], a[48:51], 0\n" \
"s_cmp_eq_u32 s73, s83\n" \
"s_cselect_b32 s70, 34816, 0\n" \
"ds_read_b128 a[236:239], v232 offset:14336\n" \
"v_cvt_pk_bf16_f32 v113, v114, v115\n" \
"v_mfma_f32_16x16x32_bf16 v[184:187], a[32:35], v[16:19], 0\n" \
"s_add_u32 s73, s73, s70\n" \
"s_xor_b32 s74, s74, 0x8000\n" \
"ds_read_b128 a[248:251], v232 offset:12544\n" \
"v_mfma_f32_16x16x32_bf16 v[184:187], a[36:39], v[20:23], v[184:187]\n" \
"s_xor_b32 s77, s77, 0x8000\n" \
"ds_read_b128 a[252:255], v232 offset:14592\n" \
"v_cvt_pk_bf16_f32 v114, v116, v117\n" \
"v_mfma_f32_16x16x32_bf16 a[88:91], a[32:35], v[32:35], 0\n" \
"v_exp_f32 v119, v119\n" \
"v_mfma_f32_16x16x32_bf16 a[88:91], a[36:39], v[36:39], a[88:91]\n" \
"v_exp_f32 v64, v64\n" \
"v_mfma_f32_16x16x32_bf16 a[120:123], a[32:35], v[48:51], 0\n" \
"v_exp_f32 v65, v65\n" \
"v_mfma_f32_16x16x32_bf16 a[120:123], a[36:39], v[52:55], a[120:123]\n" \
"v_exp_f32 v66, v66\n" \
"v_cvt_pk_bf16_f32 v115, v118, v119\n" \
"v_smfmac_f32_16x16x64_bf16 v[228:231], v[244:247], v[48:55], v243\n" \
"v_cvt_pk_bf16_f32 v64, v64, v65\n" \
"v_mfma_f32_16x16x32_bf16 a[124:127], a[40:43], v[48:51], 0\n" \
"v_exp_f32 v67, v67\n" \
"v_mfma_f32_16x16x32_bf16 a[124:127], a[44:47], v[52:55], a[124:127]\n" \
"v_exp_f32 v68, v68\n" \
"v_exp_f32 v69, v69\n" \
"v_mfma_f32_16x16x32_bf16 a[92:95], a[40:43], v[32:35], 0\n" \
"v_exp_f32 v70, v70\n" \
"v_mfma_f32_16x16x32_bf16 a[92:95], a[44:47], v[36:39], a[92:95]\n" \
"v_cvt_pk_bf16_f32 v65, v66, v67\n" \
"v_cvt_pk_bf16_f32 v66, v68, v69\n" \
"v_mfma_f32_16x16x32_bf16 v[188:191], a[40:43], v[16:19], 0\n" \
"s_cmp_eq_u32 s63, 0\n" \
"v_exp_f32 v71, v71\n" \
"v_mfma_f32_16x16x32_bf16 v[188:191], a[44:47], v[20:23], v[188:191]\n" \
"s_waitcnt lgkmcnt(0)\n" \
"v_cvt_pk_bf16_f32 v67, v70, v71\n" \
"s_cbranch_scc1 Lloop2_%=\n" \
"Lloop_%=:\n" \
"v_mfma_f32_16x16x32_bf16 v[0:3], a[192:195], a[128:131], v[196:199]\n" \
"v_exp_f32 v72, v72\n" \
"v_mfma_f32_16x16x32_bf16 v[0:3], a[196:199], a[132:135], v[0:3]\n" \
"v_mfma_f32_16x16x32_bf16 v[0:3], a[200:203], a[136:139], v[0:3]\n" \
"v_exp_f32 v73, v73\n" \
"v_mfma_f32_16x16x32_bf16 v[0:3], a[204:207], a[140:143], v[0:3]\n" \
"ds_read_b128 a[32:35], v233 offset:6144\n" \
"v_cvt_pk_bf16_f32 v68, v72, v73\n" \
"v_mfma_f32_16x16x32_bf16 v[16:19], a[204:207], a[156:159], v[196:199]\n" \
"v_exp_f32 v74, v74\n" \
"v_mfma_f32_16x16x32_bf16 v[16:19], a[200:203], a[152:155], v[16:19]\n" \
"global_load_dword v253, v252, s[44:45]\n" \
"v_mfma_f32_16x16x32_bf16 v[16:19], a[196:199], a[148:151], v[16:19]\n" \
"s_sub_u32 s46, s46, 1\n" \
"v_exp_f32 v75, v75\n" \
"v_mfma_f32_16x16x32_bf16 v[16:19], a[192:195], a[144:147], v[16:19]\n" \
"ds_read_b128 a[36:39], v233 offset:14336\n" \
"v_cvt_pk_bf16_f32 v69, v74, v75\n" \
"v_mfma_f32_16x16x32_bf16 v[32:35], a[192:195], a[160:163], v[196:199]\n" \
"v_exp_f32 v76, v76\n" \
"v_mfma_f32_16x16x32_bf16 v[32:35], a[196:199], a[164:167], v[32:35]\n" \
"s_cmp_gt_i32 s46, 0\n" \
"s_cselect_b32 s47, 0x100000, 0\n" \
"v_mfma_f32_16x16x32_bf16 v[32:35], a[200:203], a[168:171], v[32:35]\n" \
"v_exp_f32 v77, v77\n" \
"v_mfma_f32_16x16x32_bf16 v[32:35], a[204:207], a[172:175], v[32:35]\n" \
"s_add_u32 s44, s44, s47\n" \
"s_addc_u32 s45, s45, 0\n" \
"ds_read_b128 a[40:43], v233 offset:7168\n" \
"v_cvt_pk_bf16_f32 v70, v76, v77\n" \
"v_mfma_f32_16x16x32_bf16 v[48:51], a[204:207], a[188:191], v[196:199]\n" \
"v_exp_f32 v78, v78\n" \
"v_mfma_f32_16x16x32_bf16 v[48:51], a[200:203], a[184:187], v[48:51]\n" \
"v_xor_b32 v232, 0x4000, v232\n" \
"v_mfma_f32_16x16x32_bf16 v[48:51], a[196:199], a[180:183], v[48:51]\n" \
"v_exp_f32 v79, v79\n" \
"v_mfma_f32_16x16x32_bf16 v[48:51], a[192:195], a[176:179], v[48:51]\n" \
"ds_read_b128 a[204:207], v233 offset:9216\n" \
"v_cvt_pk_bf16_f32 v71, v78, v79\n" \
"v_mfma_f32_16x16x32_bf16 v[52:55], a[208:211], a[176:179], v[196:199]\n" \
"v_exp_f32 v88, v88\n" \
"v_mfma_f32_16x16x32_bf16 v[52:55], a[212:215], a[180:183], v[52:55]\n" \
"v_mfma_f32_16x16x32_bf16 v[52:55], a[216:219], a[184:187], v[52:55]\n" \
"v_exp_f32 v89, v89\n" \
"v_mfma_f32_16x16x32_bf16 v[52:55], a[220:223], a[188:191], v[52:55]\n" \
"ds_read_b128 a[200:203], v233 offset:1024\n" \
"v_cvt_pk_bf16_f32 v84, v88, v89\n" \
"v_mfma_f32_16x16x32_bf16 v[36:39], a[220:223], a[172:175], v[196:199]\n" \
"v_exp_f32 v90, v90\n" \
"v_mfma_f32_16x16x32_bf16 v[36:39], a[216:219], a[168:171], v[36:39]\n" \
"v_mfma_f32_16x16x32_bf16 v[36:39], a[212:215], a[164:167], v[36:39]\n" \
"v_exp_f32 v91, v91\n" \
"v_mfma_f32_16x16x32_bf16 v[36:39], a[208:211], a[160:163], v[36:39]\n" \
"ds_read_b128 a[196:199], v233 offset:8192\n" \
"v_cvt_pk_bf16_f32 v85, v90, v91\n" \
"v_mfma_f32_16x16x32_bf16 v[20:23], a[208:211], a[144:147], v[196:199]\n" \
"v_exp_f32 v92, v92\n" \
"v_mfma_f32_16x16x32_bf16 v[20:23], a[212:215], a[148:151], v[20:23]\n" \
"v_mfma_f32_16x16x32_bf16 v[20:23], a[216:219], a[152:155], v[20:23]\n" \
"v_exp_f32 v93, v93\n" \
"v_mfma_f32_16x16x32_bf16 v[20:23], a[220:223], a[156:159], v[20:23]\n" \
"ds_read_b128 a[192:195], v233 offset:0\n" \
"v_cvt_pk_bf16_f32 v86, v92, v93\n" \
"v_mfma_f32_16x16x32_bf16 v[4:7], a[220:223], a[140:143], v[196:199]\n" \
"v_exp_f32 v94, v94\n" \
"v_mfma_f32_16x16x32_bf16 v[4:7], a[216:219], a[136:139], v[4:7]\n" \
"v_mfma_f32_16x16x32_bf16 v[4:7], a[212:215], a[132:135], v[4:7]\n" \
"s_waitcnt lgkmcnt(7)\n" \
"v_exp_f32 v95, v95\n" \
"v_mfma_f32_16x16x32_bf16 v[4:7], a[208:211], a[128:131], v[4:7]\n" \
"ds_read_b128 a[220:223], v233 offset:11264\n" \
"v_cvt_pk_bf16_f32 v87, v94, v95\n" \
"v_mfma_f32_16x16x32_bf16 v[8:11], a[224:227], a[128:131], v[196:199]\n" \
"v_exp_f32 v104, v104\n" \
"v_mfma_f32_16x16x32_bf16 v[8:11], a[228:231], a[132:135], v[8:11]\n" \
"v_mfma_f32_16x16x32_bf16 v[8:11], a[232:235], a[136:139], v[8:11]\n" \
"v_exp_f32 v105, v105\n" \
"v_mfma_f32_16x16x32_bf16 v[8:11], a[236:239], a[140:143], v[8:11]\n" \
"ds_read_b128 a[216:219], v233 offset:3072\n" \
"v_cvt_pk_bf16_f32 v100, v104, v105\n" \
"v_mfma_f32_16x16x32_bf16 v[24:27], a[236:239], a[156:159], v[196:199]\n" \
"v_exp_f32 v106, v106\n" \
"v_mfma_f32_16x16x32_bf16 v[24:27], a[232:235], a[152:155], v[24:27]\n" \
"v_mfma_f32_16x16x32_bf16 v[24:27], a[228:231], a[148:151], v[24:27]\n" \
"v_exp_f32 v107, v107\n" \
"v_mfma_f32_16x16x32_bf16 v[24:27], a[224:227], a[144:147], v[24:27]\n" \
"ds_read_b128 a[212:215], v233 offset:10240\n" \
"v_cvt_pk_bf16_f32 v101, v106, v107\n" \
"v_mfma_f32_16x16x32_bf16 v[40:43], a[224:227], a[160:163], v[196:199]\n" \
"v_exp_f32 v108, v108\n" \
"v_mfma_f32_16x16x32_bf16 v[40:43], a[228:231], a[164:167], v[40:43]\n" \
"v_mfma_f32_16x16x32_bf16 v[40:43], a[232:235], a[168:171], v[40:43]\n" \
"v_exp_f32 v109, v109\n" \
"v_mfma_f32_16x16x32_bf16 v[40:43], a[236:239], a[172:175], v[40:43]\n" \
"ds_read_b128 a[208:211], v233 offset:2048\n" \
"v_cvt_pk_bf16_f32 v102, v108, v109\n" \
"v_mfma_f32_16x16x32_bf16 v[56:59], a[236:239], a[188:191], v[196:199]\n" \
"v_exp_f32 v110, v110\n" \
"v_mfma_f32_16x16x32_bf16 v[56:59], a[232:235], a[184:187], v[56:59]\n" \
"v_mfma_f32_16x16x32_bf16 v[56:59], a[228:231], a[180:183], v[56:59]\n" \
"v_exp_f32 v111, v111\n" \
"v_mfma_f32_16x16x32_bf16 v[56:59], a[224:227], a[176:179], v[56:59]\n" \
"ds_read_b128 a[236:239], v233 offset:13312\n" \
"v_cvt_pk_bf16_f32 v103, v110, v111\n" \
"v_mfma_f32_16x16x32_bf16 v[60:63], a[240:243], a[176:179], v[196:199]\n" \
"v_exp_f32 v120, v120\n" \
"v_mfma_f32_16x16x32_bf16 v[60:63], a[244:247], a[180:183], v[60:63]\n" \
"v_mfma_f32_16x16x32_bf16 v[60:63], a[248:251], a[184:187], v[60:63]\n" \
"v_exp_f32 v121, v121\n" \
"v_mfma_f32_16x16x32_bf16 v[60:63], a[252:255], a[188:191], v[60:63]\n" \
"ds_read_b128 a[232:235], v233 offset:5120\n" \
"v_cvt_pk_bf16_f32 v116, v120, v121\n" \
"v_mfma_f32_16x16x32_bf16 v[44:47], a[252:255], a[172:175], v[196:199]\n" \
"v_exp_f32 v122, v122\n" \
"v_mfma_f32_16x16x32_bf16 v[44:47], a[248:251], a[168:171], v[44:47]\n" \
"v_mfma_f32_16x16x32_bf16 v[44:47], a[244:247], a[164:167], v[44:47]\n" \
"v_exp_f32 v123, v123\n" \
"v_mfma_f32_16x16x32_bf16 v[44:47], a[240:243], a[160:163], v[44:47]\n" \
"ds_read_b128 a[228:231], v233 offset:12288\n" \
"v_cvt_pk_bf16_f32 v117, v122, v123\n" \
"v_mfma_f32_16x16x32_bf16 v[28:31], a[240:243], a[144:147], v[196:199]\n" \
"v_exp_f32 v124, v124\n" \
"v_mfma_f32_16x16x32_bf16 v[28:31], a[244:247], a[148:151], v[28:31]\n" \
"v_mfma_f32_16x16x32_bf16 v[28:31], a[248:251], a[152:155], v[28:31]\n" \
"v_exp_f32 v125, v125\n" \
"v_mfma_f32_16x16x32_bf16 v[28:31], a[252:255], a[156:159], v[28:31]\n" \
"ds_read_b128 a[224:227], v233 offset:4096\n" \
"v_cvt_pk_bf16_f32 v118, v124, v125\n" \
"v_mfma_f32_16x16x32_bf16 v[12:15], a[252:255], a[140:143], v[196:199]\n" \
"v_exp_f32 v126, v126\n" \
"v_mfma_f32_16x16x32_bf16 v[12:15], a[248:251], a[136:139], v[12:15]\n" \
"Lpg5600_%=:\n" \
"s_cbranch_execz Lpg6788_%=\n" \
"v_mfma_f32_16x16x32_bf16 v[12:15], a[244:247], a[132:135], v[12:15]\n" \
"s_waitcnt lgkmcnt(2)\n" \
"v_exp_f32 v127, v127\n" \
"v_mfma_f32_16x16x32_bf16 v[12:15], a[240:243], a[128:131], v[12:15]\n" \
"ds_read_b128 a[44:47], v233 offset:15360\n" \
"v_cvt_pk_bf16_f32 v119, v126, v127\n" \
"v_mfma_f32_16x16x32_bf16 v[128:131], a[192:195], v[64:67], v[128:131]\n" \
"v_exp_f32 v0, v0\n" \
"v_mfma_f32_16x16x32_bf16 v[128:131], a[196:199], v[68:71], v[128:131]\n" \
"ds_read_b128 a[244:247], v232 offset:10496\n" \
"v_mfma_f32_16x16x32_bf16 v[160:163], a[192:195], v[80:83], v[160:163]\n" \
"v_exp_f32 v1, v1\n" \
"v_mfma_f32_16x16x32_bf16 v[160:163], a[196:199], v[84:87], v[160:163]\n" \
"ds_read_b128 a[240:243], v232 offset:8448\n" \
"v_cvt_pk_bf16_f32 v0, v0, v1\n" \
"v_mfma_f32_16x16x32_bf16 a[64:67], a[192:195], v[96:99], a[64:67]\n" \
"v_exp_f32 v2, v2\n" \
"v_mfma_f32_16x16x32_bf16 a[64:67], a[196:199], v[100:103], a[64:67]\n" \
"ds_read_b128 a[248:251], v232 offset:12544\n" \
"v_xor_b32 v233, 0xc000, v233\n" \
"v_mfma_f32_16x16x32_bf16 a[96:99], a[192:195], v[112:115], a[96:99]\n" \
"v_exp_f32 v3, v3\n" \
"v_mfma_f32_16x16x32_bf16 a[96:99], a[196:199], v[116:119], a[96:99]\n" \
"ds_read_b128 a[192:195], v232 offset:0\n" \
"v_cvt_pk_bf16_f32 v1, v2, v3\n" \
"v_mfma_f32_16x16x32_bf16 a[100:103], a[200:203], v[112:115], a[100:103]\n" \
"v_exp_f32 v4, v4\n" \
"v_mfma_f32_16x16x32_bf16 a[100:103], a[204:207], v[116:119], a[100:103]\n" \
"ds_read_b128 a[252:255], v232 offset:14592\n" \
"v_mfma_f32_16x16x32_bf16 a[68:71], a[200:203], v[96:99], a[68:71]\n" \
"v_exp_f32 v5, v5\n" \
"v_mfma_f32_16x16x32_bf16 a[68:71], a[204:207], v[100:103], a[68:71]\n" \
"ds_read_b128 a[196:199], v232 offset:2048\n" \
"v_cvt_pk_bf16_f32 v2, v4, v5\n" \
"v_smfmac_f32_16x16x64_bf16 a[48:51], v[244:247], v[64:71], v243\n" \
"v_mfma_f32_16x16x32_bf16 v[164:167], a[200:203], v[80:83], v[164:167]\n" \
"v_exp_f32 v6, v6\n" \
"v_mfma_f32_16x16x32_bf16 v[164:167], a[204:207], v[84:87], v[164:167]\n" \
"v_mfma_f32_16x16x32_bf16 v[132:135], a[200:203], v[64:67], v[132:135]\n" \
"v_exp_f32 v7, v7\n" \
"v_mfma_f32_16x16x32_bf16 v[132:135], a[204:207], v[68:71], v[132:135]\n" \
"ds_read_b128 a[200:203], v232 offset:4096\n" \
"v_cvt_pk_bf16_f32 v3, v6, v7\n" \
"v_mfma_f32_16x16x32_bf16 v[136:139], a[208:211], v[64:67], v[136:139]\n" \
"v_exp_f32 v16, v16\n" \
"v_mfma_f32_16x16x32_bf16 v[136:139], a[212:215], v[68:71], v[136:139]\n" \
"v_mfma_f32_16x16x32_bf16 v[168:171], a[208:211], v[80:83], v[168:171]\n" \
"v_exp_f32 v17, v17\n" \
"v_mfma_f32_16x16x32_bf16 v[168:171], a[212:215], v[84:87], v[168:171]\n" \
"ds_read_b128 a[204:207], v232 offset:6144\n" \
"v_cvt_pk_bf16_f32 v16, v16, v17\n" \
"v_mfma_f32_16x16x32_bf16 a[72:75], a[208:211], v[96:99], a[72:75]\n" \
"v_exp_f32 v18, v18\n" \
"v_mfma_f32_16x16x32_bf16 a[72:75], a[212:215], v[100:103], a[72:75]\n" \
"v_mfma_f32_16x16x32_bf16 a[104:107], a[208:211], v[112:115], a[104:107]\n" \
"v_exp_f32 v19, v19\n" \
"v_mfma_f32_16x16x32_bf16 a[104:107], a[212:215], v[116:119], a[104:107]\n" \
"ds_read_b128 a[208:211], v232 offset:256\n" \
"v_cvt_pk_bf16_f32 v17, v18, v19\n" \
"v_mfma_f32_16x16x32_bf16 a[108:111], a[216:219], v[112:115], a[108:111]\n" \
"v_exp_f32 v20, v20\n" \
"v_mfma_f32_16x16x32_bf16 a[108:111], a[220:223], v[116:119], a[108:111]\n" \
"v_mfma_f32_16x16x32_bf16 a[76:79], a[216:219], v[96:99], a[76:79]\n" \
"v_exp_f32 v21, v21\n" \
"v_mfma_f32_16x16x32_bf16 a[76:79], a[220:223], v[100:103], a[76:79]\n" \
"ds_read_b128 a[212:215], v232 offset:2304\n" \
"v_cvt_pk_bf16_f32 v18, v20, v21\n" \
"v_smfmac_f32_16x16x64_bf16 a[52:55], v[244:247], v[80:87], v243\n" \
"v_mfma_f32_16x16x32_bf16 v[172:175], a[216:219], v[80:83], v[172:175]\n" \
"v_exp_f32 v22, v22\n" \
"v_mfma_f32_16x16x32_bf16 v[172:175], a[220:223], v[84:87], v[172:175]\n" \
"v_mfma_f32_16x16x32_bf16 v[140:143], a[216:219], v[64:67], v[140:143]\n" \
"s_waitcnt lgkmcnt(10)\n" \
"v_exp_f32 v23, v23\n" \
"v_mfma_f32_16x16x32_bf16 v[140:143], a[220:223], v[68:71], v[140:143]\n" \
"ds_read_b128 a[216:219], v232 offset:4352\n" \
"v_cvt_pk_bf16_f32 v19, v22, v23\n" \
"v_mfma_f32_16x16x32_bf16 v[144:147], a[224:227], v[64:67], v[144:147]\n" \
"v_exp_f32 v32, v32\n" \
"v_mfma_f32_16x16x32_bf16 v[144:147], a[228:231], v[68:71], v[144:147]\n" \
"v_mfma_f32_16x16x32_bf16 v[176:179], a[224:227], v[80:83], v[176:179]\n" \
"v_exp_f32 v33, v33\n" \
"v_mfma_f32_16x16x32_bf16 v[176:179], a[228:231], v[84:87], v[176:179]\n" \
"ds_read_b128 a[220:223], v232 offset:6400\n" \
"v_cvt_pk_bf16_f32 v32, v32, v33\n" \
"v_mfma_f32_16x16x32_bf16 a[80:83], a[224:227], v[96:99], a[80:83]\n" \
"v_exp_f32 v34, v34\n" \
"v_mfma_f32_16x16x32_bf16 a[80:83], a[228:231], v[100:103], a[80:83]\n" \
"v_mfma_f32_16x16x32_bf16 a[112:115], a[224:227], v[112:115], a[112:115]\n" \
"v_exp_f32 v35, v35\n" \
"v_mfma_f32_16x16x32_bf16 a[112:115], a[228:231], v[116:119], a[112:115]\n" \
"ds_read_b128 a[224:227], v232 offset:8192\n" \
"v_cvt_pk_bf16_f32 v33, v34, v35\n" \
"v_mfma_f32_16x16x32_bf16 a[116:119], a[232:235], v[112:115], a[116:119]\n" \
"v_exp_f32 v36, v36\n" \
"v_mfma_f32_16x16x32_bf16 a[116:119], a[236:239], v[116:119], a[116:119]\n" \
"v_mfma_f32_16x16x32_bf16 a[84:87], a[232:235], v[96:99], a[84:87]\n" \
"v_exp_f32 v37, v37\n" \
"v_mfma_f32_16x16x32_bf16 a[84:87], a[236:239], v[100:103], a[84:87]\n" \
"ds_read_b128 a[228:231], v232 offset:10240\n" \
"v_cvt_pk_bf16_f32 v34, v36, v37\n" \
"v_smfmac_f32_16x16x64_bf16 v[224:227], v[244:247], v[96:103], v243\n" \
"v_mfma_f32_16x16x32_bf16 v[180:183], a[232:235], v[80:83], v[180:183]\n" \
"v_exp_f32 v38, v38\n" \
"v_mfma_f32_16x16x32_bf16 v[180:183], a[236:239], v[84:87], v[180:183]\n" \
"v_mfma_f32_16x16x32_bf16 v[148:151], a[232:235], v[64:67], v[148:151]\n" \
"v_exp_f32 v39, v39\n" \
"v_mfma_f32_16x16x32_bf16 v[148:151], a[236:239], v[68:71], v[148:151]\n" \
"ds_read_b128 a[232:235], v232 offset:12288\n" \
"v_cvt_pk_bf16_f32 v35, v38, v39\n" \
"v_mfma_f32_16x16x32_bf16 v[152:155], a[32:35], v[64:67], v[152:155]\n" \
"v_exp_f32 v48, v48\n" \
"v_mfma_f32_16x16x32_bf16 v[152:155], a[36:39], v[68:71], v[152:155]\n" \
"v_mfma_f32_16x16x32_bf16 v[184:187], a[32:35], v[80:83], v[184:187]\n" \
"v_exp_f32 v49, v49\n" \
"v_mfma_f32_16x16x32_bf16 v[184:187], a[36:39], v[84:87], v[184:187]\n" \
"ds_read_b128 a[236:239], v232 offset:14336\n" \
"v_cvt_pk_bf16_f32 v48, v48, v49\n" \
"v_mfma_f32_16x16x32_bf16 a[88:91], a[32:35], v[96:99], a[88:91]\n" \
"v_exp_f32 v50, v50\n" \
"v_mfma_f32_16x16x32_bf16 a[88:91], a[36:39], v[100:103], a[88:91]\n" \
"v_mfma_f32_16x16x32_bf16 a[120:123], a[32:35], v[112:115], a[120:123]\n" \
"v_exp_f32 v51, v51\n" \
"v_mfma_f32_16x16x32_bf16 a[120:123], a[36:39], v[116:119], a[120:123]\n" \
"v_cvt_pk_bf16_f32 v49, v50, v51\n" \
"v_mfma_f32_16x16x32_bf16 a[124:127], a[40:43], v[112:115], a[124:127]\n" \
"v_exp_f32 v52, v52\n" \
"v_mfma_f32_16x16x32_bf16 a[124:127], a[44:47], v[116:119], a[124:127]\n" \
"v_mfma_f32_16x16x32_bf16 a[92:95], a[40:43], v[96:99], a[92:95]\n" \
"v_exp_f32 v53, v53\n" \
"v_mfma_f32_16x16x32_bf16 a[92:95], a[44:47], v[100:103], a[92:95]\n" \
"v_cvt_pk_bf16_f32 v50, v52, v53\n" \
"v_smfmac_f32_16x16x64_bf16 v[228:231], v[244:247], v[112:119], v243\n" \
"v_mfma_f32_16x16x32_bf16 v[188:191], a[40:43], v[80:83], v[188:191]\n" \
"v_exp_f32 v54, v54\n" \
"v_mfma_f32_16x16x32_bf16 v[188:191], a[44:47], v[84:87], v[188:191]\n" \
"v_mfma_f32_16x16x32_bf16 v[156:159], a[40:43], v[64:67], v[156:159]\n" \
"s_waitcnt vmcnt(1) lgkmcnt(0)\n" \
"v_exp_f32 v55, v55\n" \
"v_mfma_f32_16x16x32_bf16 v[156:159], a[44:47], v[68:71], v[156:159]\n" \
"s_barrier\n" \
"v_cvt_pk_bf16_f32 v51, v54, v55\n" \
"v_mfma_f32_16x16x32_bf16 v[64:67], a[192:195], a[128:131], v[196:199]\n" \
"v_exp_f32 v8, v8\n" \
"v_mfma_f32_16x16x32_bf16 v[64:67], a[196:199], a[132:135], v[64:67]\n" \
"v_cmp_eq_u32 vcc, s60, v239\n" \
"v_mfma_f32_16x16x32_bf16 v[64:67], a[200:203], a[136:139], v[64:67]\n" \
"v_exp_f32 v9, v9\n" \
"v_mfma_f32_16x16x32_bf16 v[64:67], a[204:207], a[140:143], v[64:67]\n" \
"s_andn2_b32 s70, s69, vcc_lo\n" \
"s_cbranch_scc1 Lpl2_%=\n" \
"Lpr2_%=:\n" \
"ds_read_b128 a[32:35], v233 offset:6144\n" \
"v_cvt_pk_bf16_f32 v4, v8, v9\n" \
"v_mfma_f32_16x16x32_bf16 v[80:83], a[204:207], a[156:159], v[196:199]\n" \
"s_add_u32 m0, s77, s80\n" \
"v_exp_f32 v10, v10\n" \
"v_mfma_f32_16x16x32_bf16 v[80:83], a[200:203], a[152:155], v[80:83]\n" \
"buffer_load_dwordx4 v234, s[40:43], s76 offen offset:0 sc1 lds\n" \
"v_mfma_f32_16x16x32_bf16 v[80:83], a[196:199], a[148:151], v[80:83]\n" \
"s_sub_u32 s89, s89, 1\n" \
"v_exp_f32 v11, v11\n" \
"v_mfma_f32_16x16x32_bf16 v[80:83], a[192:195], a[144:147], v[80:83]\n" \
"s_cmp_eq_u32 s89, 0\n" \
"s_cselect_b32 s70, s78, 0xc000\n" \
"ds_read_b128 a[36:39], v233 offset:14336\n" \
"v_cvt_pk_bf16_f32 v5, v10, v11\n" \
"v_mfma_f32_16x16x32_bf16 v[96:99], a[192:195], a[160:163], v[196:199]\n" \
"s_add_u32 s71, s76, 17408\n" \
"v_exp_f32 v12, v12\n" \
"v_mfma_f32_16x16x32_bf16 v[96:99], a[196:199], a[164:167], v[96:99]\n" \
"buffer_load_dwordx4 v234, s[40:43], s76 offen offset:1024 sc1 lds\n" \
"v_mfma_f32_16x16x32_bf16 v[96:99], a[200:203], a[168:171], v[96:99]\n" \
"v_exp_f32 v13, v13\n" \
"v_mfma_f32_16x16x32_bf16 v[96:99], a[204:207], a[172:175], v[96:99]\n" \
"ds_read_b128 a[40:43], v233 offset:7168\n" \
"v_cvt_pk_bf16_f32 v6, v12, v13\n" \
"v_mfma_f32_16x16x32_bf16 v[112:115], a[204:207], a[188:191], v[196:199]\n" \
"v_exp_f32 v14, v14\n" \
"v_mfma_f32_16x16x32_bf16 v[112:115], a[200:203], a[184:187], v[112:115]\n" \
"v_xor_b32 v232, s70, v232\n" \
"v_mfma_f32_16x16x32_bf16 v[112:115], a[196:199], a[180:183], v[112:115]\n" \
"v_exp_f32 v15, v15\n" \
"v_mfma_f32_16x16x32_bf16 v[112:115], a[192:195], a[176:179], v[112:115]\n" \
"ds_read_b128 a[204:207], v233 offset:9216\n" \
"v_cvt_pk_bf16_f32 v7, v14, v15\n" \
"v_mfma_f32_16x16x32_bf16 v[116:119], a[208:211], a[176:179], v[196:199]\n" \
"v_exp_f32 v24, v24\n" \
"v_mfma_f32_16x16x32_bf16 v[116:119], a[212:215], a[180:183], v[116:119]\n" \
"buffer_load_dwordx4 v234, s[40:43], s76 offen offset:2048 sc1 lds\n" \
"v_mfma_f32_16x16x32_bf16 v[116:119], a[216:219], a[184:187], v[116:119]\n" \
"v_exp_f32 v25, v25\n" \
"v_mfma_f32_16x16x32_bf16 v[116:119], a[220:223], a[188:191], v[116:119]\n" \
"ds_read_b128 a[200:203], v233 offset:1024\n" \
"v_cvt_pk_bf16_f32 v20, v24, v25\n" \
"v_mfma_f32_16x16x32_bf16 v[100:103], a[220:223], a[172:175], v[196:199]\n" \
"v_exp_f32 v26, v26\n" \
"v_mfma_f32_16x16x32_bf16 v[100:103], a[216:219], a[168:171], v[100:103]\n" \
"buffer_load_dwordx4 v234, s[40:43], s76 offen offset:3072 sc1 lds\n" \
"v_mfma_f32_16x16x32_bf16 v[100:103], a[212:215], a[164:167], v[100:103]\n" \
"v_exp_f32 v27, v27\n" \
"v_mfma_f32_16x16x32_bf16 v[100:103], a[208:211], a[160:163], v[100:103]\n" \
"ds_read_b128 a[196:199], v233 offset:8192\n" \
"v_cvt_pk_bf16_f32 v21, v26, v27\n" \
"v_mfma_f32_16x16x32_bf16 v[84:87], a[208:211], a[144:147], v[196:199]\n" \
"v_exp_f32 v28, v28\n" \
"v_mfma_f32_16x16x32_bf16 v[84:87], a[212:215], a[148:151], v[84:87]\n" \
"v_mfma_f32_16x16x32_bf16 v[84:87], a[216:219], a[152:155], v[84:87]\n" \
"v_exp_f32 v29, v29\n" \
"v_mfma_f32_16x16x32_bf16 v[84:87], a[220:223], a[156:159], v[84:87]\n" \
"ds_read_b128 a[192:195], v233 offset:0\n" \
"v_cvt_pk_bf16_f32 v22, v28, v29\n" \
"v_mfma_f32_16x16x32_bf16 v[68:71], a[220:223], a[140:143], v[196:199]\n" \
"s_add_u32 m0, m0, 0x4000\n" \
"v_exp_f32 v30, v30\n" \
"v_mfma_f32_16x16x32_bf16 v[68:71], a[216:219], a[136:139], v[68:71]\n" \
"buffer_load_dwordx4 v234, s[40:43], s71 offen offset:0 sc1 lds\n" \
"v_mfma_f32_16x16x32_bf16 v[68:71], a[212:215], a[132:135], v[68:71]\n" \
"v_exp_f32 v31, v31\n" \
"v_mfma_f32_16x16x32_bf16 v[68:71], a[208:211], a[128:131], v[68:71]\n" \
"ds_read_b128 a[220:223], v233 offset:11264\n" \
"v_cvt_pk_bf16_f32 v23, v30, v31\n" \
"v_mfma_f32_16x16x32_bf16 v[72:75], a[224:227], a[128:131], v[196:199]\n" \
"v_exp_f32 v40, v40\n" \
"v_mfma_f32_16x16x32_bf16 v[72:75], a[228:231], a[132:135], v[72:75]\n" \
"buffer_load_dwordx4 v234, s[40:43], s71 offen offset:1024 sc1 lds\n" \
"v_mfma_f32_16x16x32_bf16 v[72:75], a[232:235], a[136:139], v[72:75]\n" \
"v_exp_f32 v41, v41\n" \
"v_mfma_f32_16x16x32_bf16 v[72:75], a[236:239], a[140:143], v[72:75]\n" \
"ds_read_b128 a[216:219], v233 offset:3072\n" \
"v_cvt_pk_bf16_f32 v36, v40, v41\n" \
"v_mfma_f32_16x16x32_bf16 v[88:91], a[236:239], a[156:159], v[196:199]\n" \
"v_exp_f32 v42, v42\n" \
"v_mfma_f32_16x16x32_bf16 v[88:91], a[232:235], a[152:155], v[88:91]\n" \
"v_mfma_f32_16x16x32_bf16 v[88:91], a[228:231], a[148:151], v[88:91]\n" \
"v_exp_f32 v43, v43\n" \
"v_mfma_f32_16x16x32_bf16 v[88:91], a[224:227], a[144:147], v[88:91]\n" \
"ds_read_b128 a[212:215], v233 offset:10240\n" \
"v_cvt_pk_bf16_f32 v37, v42, v43\n" \
"v_mfma_f32_16x16x32_bf16 v[104:107], a[224:227], a[160:163], v[196:199]\n" \
"v_exp_f32 v44, v44\n" \
"v_mfma_f32_16x16x32_bf16 v[104:107], a[228:231], a[164:167], v[104:107]\n" \
"buffer_load_dwordx4 v234, s[40:43], s71 offen offset:2048 sc1 lds\n" \
"v_mfma_f32_16x16x32_bf16 v[104:107], a[232:235], a[168:171], v[104:107]\n" \
"v_exp_f32 v45, v45\n" \
"v_mfma_f32_16x16x32_bf16 v[104:107], a[236:239], a[172:175], v[104:107]\n" \
"ds_read_b128 a[208:211], v233 offset:2048\n" \
"v_cvt_pk_bf16_f32 v38, v44, v45\n" \
"v_mfma_f32_16x16x32_bf16 v[120:123], a[236:239], a[188:191], v[196:199]\n" \
"v_exp_f32 v46, v46\n" \
"v_mfma_f32_16x16x32_bf16 v[120:123], a[232:235], a[184:187], v[120:123]\n" \
"buffer_load_dwordx4 v234, s[40:43], s71 offen offset:3072 sc1 lds\n" \
"v_mfma_f32_16x16x32_bf16 v[120:123], a[228:231], a[180:183], v[120:123]\n" \
"v_exp_f32 v47, v47\n" \
"v_mfma_f32_16x16x32_bf16 v[120:123], a[224:227], a[176:179], v[120:123]\n" \
"ds_read_b128 a[236:239], v233 offset:13312\n" \
"v_cvt_pk_bf16_f32 v39, v46, v47\n" \
"v_mfma_f32_16x16x32_bf16 v[124:127], a[240:243], a[176:179], v[196:199]\n" \
"v_exp_f32 v56, v56\n" \
"v_mfma_f32_16x16x32_bf16 v[124:127], a[244:247], a[180:183], v[124:127]\n" \
"v_mfma_f32_16x16x32_bf16 v[124:127], a[248:251], a[184:187], v[124:127]\n" \
"v_exp_f32 v57, v57\n" \
"v_mfma_f32_16x16x32_bf16 v[124:127], a[252:255], a[188:191], v[124:127]\n" \
"ds_read_b128 a[232:235], v233 offset:5120\n" \
"v_cvt_pk_bf16_f32 v52, v56, v57\n" \
"v_mfma_f32_16x16x32_bf16 v[108:111], a[252:255], a[172:175], v[196:199]\n" \
"v_exp_f32 v58, v58\n" \
"v_mfma_f32_16x16x32_bf16 v[108:111], a[248:251], a[168:171], v[108:111]\n" \
"v_mfma_f32_16x16x32_bf16 v[108:111], a[244:247], a[164:167], v[108:111]\n" \
"v_exp_f32 v59, v59\n" \
"v_mfma_f32_16x16x32_bf16 v[108:111], a[240:243], a[160:163], v[108:111]\n" \
"ds_read_b128 a[228:231], v233 offset:12288\n" \
"v_cvt_pk_bf16_f32 v53, v58, v59\n" \
"v_mfma_f32_16x16x32_bf16 v[92:95], a[240:243], a[144:147], v[196:199]\n" \
"v_exp_f32 v60, v60\n" \
"v_mfma_f32_16x16x32_bf16 v[92:95], a[244:247], a[148:151], v[92:95]\n" \
"v_mfma_f32_16x16x32_bf16 v[92:95], a[248:251], a[152:155], v[92:95]\n" \
"v_exp_f32 v61, v61\n" \
"v_mfma_f32_16x16x32_bf16 v[92:95], a[252:255], a[156:159], v[92:95]\n" \
"ds_read_b128 a[224:227], v233 offset:4096\n" \
"v_cvt_pk_bf16_f32 v54, v60, v61\n" \
"v_mfma_f32_16x16x32_bf16 v[76:79], a[252:255], a[140:143], v[196:199]\n" \
"v_exp_f32 v62, v62\n" \
"v_mfma_f32_16x16x32_bf16 v[76:79], a[248:251], a[136:139], v[76:79]\n" \
"v_mfma_f32_16x16x32_bf16 v[76:79], a[244:247], a[132:135], v[76:79]\n" \
"s_waitcnt lgkmcnt(2)\n" \
"v_exp_f32 v63, v63\n" \
"v_mfma_f32_16x16x32_bf16 v[76:79], a[240:243], a[128:131], v[76:79]\n" \
"ds_read_b128 a[44:47], v233 offset:15360\n" \
"v_cvt_pk_bf16_f32 v55, v62, v63\n" \
"v_mfma_f32_16x16x32_bf16 v[128:131], a[192:195], v[0:3], v[128:131]\n" \
"s_add_u32 m0, s74, s79\n" \
"v_exp_f32 v64, v64\n" \
"v_mfma_f32_16x16x32_bf16 v[128:131], a[196:199], v[4:7], v[128:131]\n" \
"buffer_load_dwordx4 v234, s[36:39], s73 offen offset:0 sc1 lds\n" \
"v_mfma_f32_16x16x32_bf16 v[160:163], a[192:195], v[16:19], v[160:163]\n" \
"v_exp_f32 v65, v65\n" \
"v_mfma_f32_16x16x32_bf16 v[160:163], a[196:199], v[20:23], v[160:163]\n" \
"ds_read_b128 a[240:243], v232 offset:8448\n" \
"v_cvt_pk_bf16_f32 v64, v64, v65\n" \
"v_mfma_f32_16x16x32_bf16 a[64:67], a[192:195], v[32:35], a[64:67]\n" \
"s_add_u32 s71, s73, 17408\n" \
"v_exp_f32 v66, v66\n" \
"v_mfma_f32_16x16x32_bf16 a[64:67], a[196:199], v[36:39], a[64:67]\n" \
"buffer_load_dwordx4 v234, s[36:39], s73 offen offset:1024 sc1 lds\n" \
"v_mfma_f32_16x16x32_bf16 a[96:99], a[192:195], v[48:51], a[96:99]\n" \
"v_exp_f32 v67, v67\n" \
"v_mfma_f32_16x16x32_bf16 a[96:99], a[196:199], v[52:55], a[96:99]\n" \
"ds_read_b128 a[192:195], v232 offset:0\n" \
"v_cvt_pk_bf16_f32 v65, v66, v67\n" \
"v_mfma_f32_16x16x32_bf16 a[100:103], a[200:203], v[48:51], a[100:103]\n" \
"v_exp_f32 v68, v68\n" \
"v_mfma_f32_16x16x32_bf16 a[100:103], a[204:207], v[52:55], a[100:103]\n" \
"v_xor_b32 v233, 0x4000, v233\n" \
"v_mfma_f32_16x16x32_bf16 a[68:71], a[200:203], v[32:35], a[68:71]\n" \
"v_exp_f32 v69, v69\n" \
"v_mfma_f32_16x16x32_bf16 a[68:71], a[204:207], v[36:39], a[68:71]\n" \
"ds_read_b128 a[196:199], v232 offset:2048\n" \
"v_cvt_pk_bf16_f32 v66, v68, v69\n" \
"v_smfmac_f32_16x16x64_bf16 a[48:51], v[244:247], v[0:7], v243\n" \
"buffer_load_dwordx4 v234, s[36:39], s73 offen offset:2048 sc1 lds\n" \
"v_mfma_f32_16x16x32_bf16 v[164:167], a[200:203], v[16:19], v[164:167]\n" \
"v_exp_f32 v70, v70\n" \
"v_mfma_f32_16x16x32_bf16 v[164:167], a[204:207], v[20:23], v[164:167]\n" \
"v_mfma_f32_16x16x32_bf16 v[132:135], a[200:203], v[0:3], v[132:135]\n" \
"v_exp_f32 v71, v71\n" \
"v_mfma_f32_16x16x32_bf16 v[132:135], a[204:207], v[4:7], v[132:135]\n" \
"ds_read_b128 a[200:203], v232 offset:4096\n" \
"v_cvt_pk_bf16_f32 v67, v70, v71\n" \
"v_mfma_f32_16x16x32_bf16 v[136:139], a[208:211], v[0:3], v[136:139]\n" \
"v_exp_f32 v80, v80\n" \
"v_mfma_f32_16x16x32_bf16 v[136:139], a[212:215], v[4:7], v[136:139]\n" \
"buffer_load_dwordx4 v234, s[36:39], s73 offen offset:3072 sc1 lds\n" \
"v_mfma_f32_16x16x32_bf16 v[168:171], a[208:211], v[16:19], v[168:171]\n" \
"v_exp_f32 v81, v81\n" \
"v_mfma_f32_16x16x32_bf16 v[168:171], a[212:215], v[20:23], v[168:171]\n" \
"ds_read_b128 a[204:207], v232 offset:6144\n" \
"v_cvt_pk_bf16_f32 v80, v80, v81\n" \
"v_mfma_f32_16x16x32_bf16 a[72:75], a[208:211], v[32:35], a[72:75]\n" \
"v_exp_f32 v82, v82\n" \
"v_mfma_f32_16x16x32_bf16 a[72:75], a[212:215], v[36:39], a[72:75]\n" \
"v_mfma_f32_16x16x32_bf16 a[104:107], a[208:211], v[48:51], a[104:107]\n" \
"v_exp_f32 v83, v83\n" \
"v_mfma_f32_16x16x32_bf16 a[104:107], a[212:215], v[52:55], a[104:107]\n" \
"ds_read_b128 a[208:211], v232 offset:256\n" \
"v_cvt_pk_bf16_f32 v81, v82, v83\n" \
"v_mfma_f32_16x16x32_bf16 a[108:111], a[216:219], v[48:51], a[108:111]\n" \
"s_add_u32 m0, m0, 0x4000\n" \
"v_exp_f32 v84, v84\n" \
"v_mfma_f32_16x16x32_bf16 a[108:111], a[220:223], v[52:55], a[108:111]\n" \
"buffer_load_dwordx4 v234, s[36:39], s71 offen offset:0 sc1 lds\n" \
"v_mfma_f32_16x16x32_bf16 a[76:79], a[216:219], v[32:35], a[76:79]\n" \
"v_exp_f32 v85, v85\n" \
"v_mfma_f32_16x16x32_bf16 a[76:79], a[220:223], v[36:39], a[76:79]\n" \
"ds_read_b128 a[212:215], v232 offset:2304\n" \
"v_cvt_pk_bf16_f32 v82, v84, v85\n" \
"v_smfmac_f32_16x16x64_bf16 a[52:55], v[244:247], v[16:23], v243\n" \
"v_mfma_f32_16x16x32_bf16 v[172:175], a[216:219], v[16:19], v[172:175]\n" \
"v_exp_f32 v86, v86\n" \
"v_mfma_f32_16x16x32_bf16 v[172:175], a[220:223], v[20:23], v[172:175]\n" \
"buffer_load_dwordx4 v234, s[36:39], s71 offen offset:1024 sc1 lds\n" \
"v_mfma_f32_16x16x32_bf16 v[140:143], a[216:219], v[0:3], v[140:143]\n" \
"s_waitcnt lgkmcnt(7)\n" \
"v_exp_f32 v87, v87\n" \
"v_mfma_f32_16x16x32_bf16 v[140:143], a[220:223], v[4:7], v[140:143]\n" \
"ds_read_b128 a[216:219], v232 offset:4352\n" \
"v_cvt_pk_bf16_f32 v83, v86, v87\n" \
"v_mfma_f32_16x16x32_bf16 v[144:147], a[224:227], v[0:3], v[144:147]\n" \
"v_exp_f32 v96, v96\n" \
"v_mfma_f32_16x16x32_bf16 v[144:147], a[228:231], v[4:7], v[144:147]\n" \
"buffer_load_dwordx4 v234, s[36:39], s71 offen offset:2048 sc1 lds\n" \
"v_mfma_f32_16x16x32_bf16 v[176:179], a[224:227], v[16:19], v[176:179]\n" \
"v_exp_f32 v97, v97\n" \
"v_mfma_f32_16x16x32_bf16 v[176:179], a[228:231], v[20:23], v[176:179]\n" \
"ds_read_b128 a[220:223], v232 offset:6400\n" \
"v_cvt_pk_bf16_f32 v96, v96, v97\n" \
"v_mfma_f32_16x16x32_bf16 a[80:83], a[224:227], v[32:35], a[80:83]\n" \
"v_exp_f32 v98, v98\n" \
"v_mfma_f32_16x16x32_bf16 a[80:83], a[228:231], v[36:39], a[80:83]\n" \
"v_mfma_f32_16x16x32_bf16 a[112:115], a[224:227], v[48:51], a[112:115]\n" \
"v_exp_f32 v99, v99\n" \
"v_mfma_f32_16x16x32_bf16 a[112:115], a[228:231], v[52:55], a[112:115]\n" \
"ds_read_b128 a[224:227], v232 offset:8192\n" \
"v_cvt_pk_bf16_f32 v97, v98, v99\n" \
"v_mfma_f32_16x16x32_bf16 a[116:119], a[232:235], v[48:51], a[116:119]\n" \
"v_exp_f32 v100, v100\n" \
"v_mfma_f32_16x16x32_bf16 a[116:119], a[236:239], v[52:55], a[116:119]\n" \
"buffer_load_dwordx4 v234, s[36:39], s71 offen offset:3072 sc1 lds\n" \
"v_mfma_f32_16x16x32_bf16 a[84:87], a[232:235], v[32:35], a[84:87]\n" \
"v_exp_f32 v101, v101\n" \
"v_mfma_f32_16x16x32_bf16 a[84:87], a[236:239], v[36:39], a[84:87]\n" \
"ds_read_b128 a[228:231], v232 offset:10240\n" \
"v_cvt_pk_bf16_f32 v98, v100, v101\n" \
"v_smfmac_f32_16x16x64_bf16 v[224:227], v[244:247], v[32:39], v243\n" \
"v_mfma_f32_16x16x32_bf16 v[180:183], a[232:235], v[16:19], v[180:183]\n" \
"v_exp_f32 v102, v102\n" \
"v_mfma_f32_16x16x32_bf16 v[180:183], a[236:239], v[20:23], v[180:183]\n" \
"buffer_load_dword v239, v242, s[56:59], s95 offen sc1\n" \
"v_mfma_f32_16x16x32_bf16 v[148:151], a[232:235], v[0:3], v[148:151]\n" \
"s_mov_b32 s69, s92\n" \
"v_exp_f32 v103, v103\n" \
"v_mfma_f32_16x16x32_bf16 v[148:151], a[236:239], v[4:7], v[148:151]\n" \
"s_mov_b32 s76, s73\n" \
"ds_read_b128 a[232:235], v232 offset:12288\n" \
"v_cvt_pk_bf16_f32 v99, v102, v103\n" \
"v_mfma_f32_16x16x32_bf16 v[152:155], a[32:35], v[0:3], v[152:155]\n" \
"s_add_u32 s73, s73, 34816\n" \
"v_exp_f32 v112, v112\n" \
"v_mfma_f32_16x16x32_bf16 v[152:155], a[36:39], v[4:7], v[152:155]\n" \
"s_cmp_eq_u32 s73, s83\n" \
"s_cselect_b32 s70, 34816, 0\n" \
"s_add_u32 s73, s73, s70\n" \
"v_mfma_f32_16x16x32_bf16 v[184:187], a[32:35], v[16:19], v[184:187]\n" \
"s_xor_b32 s74, s74, 0x8000\n" \
"v_exp_f32 v113, v113\n" \
"v_mfma_f32_16x16x32_bf16 v[184:187], a[36:39], v[20:23], v[184:187]\n" \
"s_xor_b32 s77, s77, 0x8000\n" \
"ds_read_b128 a[236:239], v232 offset:14336\n" \
"v_cvt_pk_bf16_f32 v112, v112, v113\n" \
"v_mfma_f32_16x16x32_bf16 a[88:91], a[32:35], v[32:35], a[88:91]\n" \
"v_exp_f32 v114, v114\n" \
"v_mfma_f32_16x16x32_bf16 a[88:91], a[36:39], v[36:39], a[88:91]\n" \
"v_mfma_f32_16x16x32_bf16 a[120:123], a[32:35], v[48:51], a[120:123]\n" \
"v_exp_f32 v115, v115\n" \
"v_mfma_f32_16x16x32_bf16 a[120:123], a[36:39], v[52:55], a[120:123]\n" \
"ds_read_b128 a[244:247], v232 offset:10496\n" \
"v_cvt_pk_bf16_f32 v113, v114, v115\n" \
"v_mfma_f32_16x16x32_bf16 a[124:127], a[40:43], v[48:51], a[124:127]\n" \
"v_exp_f32 v116, v116\n" \
"v_mfma_f32_16x16x32_bf16 a[124:127], a[44:47], v[52:55], a[124:127]\n" \
"v_mfma_f32_16x16x32_bf16 a[92:95], a[40:43], v[32:35], a[92:95]\n" \
"v_exp_f32 v117, v117\n" \
"v_mfma_f32_16x16x32_bf16 a[92:95], a[44:47], v[36:39], a[92:95]\n" \
"ds_read_b128 a[248:251], v232 offset:12544\n" \
"v_cvt_pk_bf16_f32 v114, v116, v117\n" \
"v_smfmac_f32_16x16x64_bf16 v[228:231], v[244:247], v[48:55], v243\n" \
"v_mfma_f32_16x16x32_bf16 v[188:191], a[40:43], v[16:19], v[188:191]\n" \
"v_exp_f32 v118, v118\n" \
"v_mfma_f32_16x16x32_bf16 v[188:191], a[44:47], v[20:23], v[188:191]\n" \
"s_cmp_lg_u32 s89, 0\n" \
"v_exp_f32 v119, v119\n" \
"v_mfma_f32_16x16x32_bf16 v[156:159], a[40:43], v[0:3], v[156:159]\n" \
"s_waitcnt lgkmcnt(2)\n" \
"ds_read_b128 a[252:255], v232 offset:14592\n" \
"v_cvt_pk_bf16_f32 v115, v118, v119\n" \
"v_mfma_f32_16x16x32_bf16 v[156:159], a[44:47], v[4:7], v[156:159]\n" \
"s_cbranch_scc1 Lloop_%=\n" \
"s_cmp_eq_u32 s63, 7\n" \
"s_cbranch_scc0 Lloop2_%=\n" \
"s_branch Lg_%=\n" \
"Lep_%=:\n" \
"Lpg6052_%=:\n" \
"s_cbranch_execz Lpg6791_%=\n" \
"s_nop 4\n" \
"v_accvgpr_read_b32 v92, a48\n" \
"v_accvgpr_read_b32 v93, a49\n" \
"v_accvgpr_read_b32 v94, a52\n" \
"v_accvgpr_read_b32 v95, a53\n" \
"v_accvgpr_write_b32 a48, 0\n" \
"v_accvgpr_write_b32 a49, 0\n" \
"v_accvgpr_write_b32 a52, 0\n" \
"v_accvgpr_write_b32 a53, 0\n" \
"v_add_f32 v86, v92, v93\n" \
"v_add_f32 v87, v94, v95\n" \
"v_rcp_f32 v84, v86\n" \
"v_rcp_f32 v85, v87\n" \
"v_add_u32 v240, 0x1000, v236\n" \
"v_add_f32 v80, v86, v87\n" \
"v_mul_f32 v80, 0x43800000, v80\n" \
"v_add_f32 v86, v84, v85\n" \
"v_fmac_f32 v80, 0x5f800000, v86\n" \
"v_mov_b32 v81, 0\n" \
"v_mov_b32 v82, 0\n" \
"v_mov_b32 v83, 0\n" \
"v_mov_b32 v88, 0\n" \
"v_mov_b32 v89, 0\n" \
"v_mov_b32 v90, 0\n" \
"v_mov_b32 v91, 0\n" \
"v_pk_mul_f32 v[128:129], v[128:129], v[84:85] op_sel:[0,0] op_sel_hi:[1,0]\n" \
"v_pk_mul_f32 v[130:131], v[130:131], v[84:85] op_sel:[0,0] op_sel_hi:[1,0]\n" \
"v_pk_mul_f32 v[132:133], v[132:133], v[84:85] op_sel:[0,0] op_sel_hi:[1,0]\n" \
"v_pk_mul_f32 v[134:135], v[134:135], v[84:85] op_sel:[0,0] op_sel_hi:[1,0]\n" \
"v_cvt_pk_bf16_f32 v64, v128, v129\n" \
"v_cvt_pk_bf16_f32 v65, v130, v131\n" \
"v_cvt_pk_bf16_f32 v66, v132, v133\n" \
"v_cvt_pk_bf16_f32 v67, v134, v135\n" \
"buffer_store_dwordx4 v[64:67], v236, s[56:59], s87 offen offset:0 sc1\n" \
"v_pk_mul_f32 v[136:137], v[136:137], v[84:85] op_sel:[0,0] op_sel_hi:[1,0]\n" \
"v_pk_mul_f32 v[138:139], v[138:139], v[84:85] op_sel:[0,0] op_sel_hi:[1,0]\n" \
"v_pk_mul_f32 v[140:141], v[140:141], v[84:85] op_sel:[0,0] op_sel_hi:[1,0]\n" \
"v_pk_mul_f32 v[142:143], v[142:143], v[84:85] op_sel:[0,0] op_sel_hi:[1,0]\n" \
"v_cvt_pk_bf16_f32 v68, v136, v137\n" \
"v_cvt_pk_bf16_f32 v69, v138, v139\n" \
"v_cvt_pk_bf16_f32 v70, v140, v141\n" \
"v_cvt_pk_bf16_f32 v71, v142, v143\n" \
"buffer_store_dwordx4 v[68:71], v236, s[56:59], s87 offen offset:2048 sc1\n" \
"v_pk_mul_f32 v[144:145], v[144:145], v[84:85] op_sel:[0,0] op_sel_hi:[1,0]\n" \
"v_pk_mul_f32 v[146:147], v[146:147], v[84:85] op_sel:[0,0] op_sel_hi:[1,0]\n" \
"v_pk_mul_f32 v[148:149], v[148:149], v[84:85] op_sel:[0,0] op_sel_hi:[1,0]\n" \
"v_pk_mul_f32 v[150:151], v[150:151], v[84:85] op_sel:[0,0] op_sel_hi:[1,0]\n" \
"v_cvt_pk_bf16_f32 v72, v144, v145\n" \
"v_cvt_pk_bf16_f32 v73, v146, v147\n" \
"v_cvt_pk_bf16_f32 v74, v148, v149\n" \
"v_cvt_pk_bf16_f32 v75, v150, v151\n" \
"buffer_store_dwordx4 v[72:75], v240, s[56:59], s87 offen offset:0 sc1\n" \
"v_pk_mul_f32 v[152:153], v[152:153], v[84:85] op_sel:[0,0] op_sel_hi:[1,0]\n" \
"v_pk_mul_f32 v[154:155], v[154:155], v[84:85] op_sel:[0,0] op_sel_hi:[1,0]\n" \
"v_pk_mul_f32 v[156:157], v[156:157], v[84:85] op_sel:[0,0] op_sel_hi:[1,0]\n" \
"v_pk_mul_f32 v[158:159], v[158:159], v[84:85] op_sel:[0,0] op_sel_hi:[1,0]\n" \
"v_cvt_pk_bf16_f32 v76, v152, v153\n" \
"v_cvt_pk_bf16_f32 v77, v154, v155\n" \
"v_cvt_pk_bf16_f32 v78, v156, v157\n" \
"v_cvt_pk_bf16_f32 v79, v158, v159\n" \
"buffer_store_dwordx4 v[76:79], v240, s[56:59], s87 offen offset:2048 sc1\n" \
"v_smfmac_f32_16x16x64_bf16 v[80:83], v[244:247], v[64:71], v243\n" \
"v_smfmac_f32_16x16x64_bf16 v[80:83], v[244:247], v[72:79], v243\n" \
"v_mfma_f32_32x32x16_bf16 v[128:143], v[88:91], v[88:91], 0\n" \
"v_mfma_f32_32x32x16_bf16 v[144:159], v[88:91], v[88:91], 0\n" \
"s_mov_b64 exec, 0\n" \
"s_cmp_eq_u32 s61, 0\n" \
"s_cbranch_scc1 Lst1x0_%=\n" \
"s_cmp_eq_u32 s61, 1\n" \
"s_cbranch_scc1 Lst1x1_%=\n" \
"s_cmp_eq_u32 s61, 2\n" \
"s_cbranch_scc1 Lst1x2_%=\n" \
"s_branch Lst1x3_%=\n" \
"Lcb1_%=:\n" \
"s_mov_b64 exec, -1\n" \
"s_add_u32 s72, s87, 532480\n" \
"v_pk_mul_f32 v[160:161], v[160:161], v[84:85] op_sel:[0,1] op_sel_hi:[1,1]\n" \
"v_pk_mul_f32 v[162:163], v[162:163], v[84:85] op_sel:[0,1] op_sel_hi:[1,1]\n" \
"v_pk_mul_f32 v[164:165], v[164:165], v[84:85] op_sel:[0,1] op_sel_hi:[1,1]\n" \
"v_pk_mul_f32 v[166:167], v[166:167], v[84:85] op_sel:[0,1] op_sel_hi:[1,1]\n" \
"v_cvt_pk_bf16_f32 v64, v160, v161\n" \
"v_cvt_pk_bf16_f32 v65, v162, v163\n" \
"v_cvt_pk_bf16_f32 v66, v164, v165\n" \
"v_cvt_pk_bf16_f32 v67, v166, v167\n" \
"buffer_store_dwordx4 v[64:67], v236, s[56:59], s72 offen offset:0 sc1\n" \
"v_pk_mul_f32 v[168:169], v[168:169], v[84:85] op_sel:[0,1] op_sel_hi:[1,1]\n" \
"v_pk_mul_f32 v[170:171], v[170:171], v[84:85] op_sel:[0,1] op_sel_hi:[1,1]\n" \
"v_pk_mul_f32 v[172:173], v[172:173], v[84:85] op_sel:[0,1] op_sel_hi:[1,1]\n" \
"v_pk_mul_f32 v[174:175], v[174:175], v[84:85] op_sel:[0,1] op_sel_hi:[1,1]\n" \
"v_cvt_pk_bf16_f32 v68, v168, v169\n" \
"v_cvt_pk_bf16_f32 v69, v170, v171\n" \
"v_cvt_pk_bf16_f32 v70, v172, v173\n" \
"v_cvt_pk_bf16_f32 v71, v174, v175\n" \
"buffer_store_dwordx4 v[68:71], v236, s[56:59], s72 offen offset:2048 sc1\n" \
"v_pk_mul_f32 v[176:177], v[176:177], v[84:85] op_sel:[0,1] op_sel_hi:[1,1]\n" \
"v_pk_mul_f32 v[178:179], v[178:179], v[84:85] op_sel:[0,1] op_sel_hi:[1,1]\n" \
"v_pk_mul_f32 v[180:181], v[180:181], v[84:85] op_sel:[0,1] op_sel_hi:[1,1]\n" \
"v_pk_mul_f32 v[182:183], v[182:183], v[84:85] op_sel:[0,1] op_sel_hi:[1,1]\n" \
"v_cvt_pk_bf16_f32 v72, v176, v177\n" \
"v_cvt_pk_bf16_f32 v73, v178, v179\n" \
"v_cvt_pk_bf16_f32 v74, v180, v181\n" \
"v_cvt_pk_bf16_f32 v75, v182, v183\n" \
"buffer_store_dwordx4 v[72:75], v240, s[56:59], s72 offen offset:0 sc1\n" \
"v_pk_mul_f32 v[184:185], v[184:185], v[84:85] op_sel:[0,1] op_sel_hi:[1,1]\n" \
"v_pk_mul_f32 v[186:187], v[186:187], v[84:85] op_sel:[0,1] op_sel_hi:[1,1]\n" \
"v_pk_mul_f32 v[188:189], v[188:189], v[84:85] op_sel:[0,1] op_sel_hi:[1,1]\n" \
"v_pk_mul_f32 v[190:191], v[190:191], v[84:85] op_sel:[0,1] op_sel_hi:[1,1]\n" \
"v_cvt_pk_bf16_f32 v76, v184, v185\n" \
"v_cvt_pk_bf16_f32 v77, v186, v187\n" \
"v_cvt_pk_bf16_f32 v78, v188, v189\n" \
"v_cvt_pk_bf16_f32 v79, v190, v191\n" \
"buffer_store_dwordx4 v[76:79], v240, s[56:59], s72 offen offset:2048 sc1\n" \
"v_smfmac_f32_16x16x64_bf16 v[80:83], v[244:247], v[64:71], v243\n" \
"v_smfmac_f32_16x16x64_bf16 v[80:83], v[244:247], v[72:79], v243\n" \
"v_mfma_f32_32x32x16_bf16 v[160:175], v[88:91], v[88:91], 0\n" \
"v_mfma_f32_32x32x16_bf16 v[176:191], v[88:91], v[88:91], 0\n" \
"s_nop 5\n" \
"v_add_f32 v80, v80, v81\n" \
"s_movk_i32 s64, 0x207\n" \
"v_cmp_class_f32 vcc, v80, s64\n" \
"s_nop 1\n" \
"s_cmp_lg_u64 vcc, 0\n" \
"s_addc_u32 s90, s90, 0\n" \
"s_waitcnt vmcnt(8)\n" \
"s_cmp_eq_u32 s63, 7\n" \
"s_cbranch_scc0 Lepr_%=\n" \
"s_branch Lepg_%=\n" \
"Lloop2_%=:\n" \
"Lpg6181_%=:\n" \
"s_cbranch_execz Lpg7380_%=\n" \
"v_mfma_f32_16x16x32_bf16 v[0:3], a[192:195], a[0:3], v[196:199]\n" \
"v_exp_f32 v72, v72\n" \
"v_mfma_f32_16x16x32_bf16 v[0:3], a[196:199], a[4:7], v[0:3]\n" \
"ds_read_b128 a[44:47], v233 offset:15360\n" \
"v_mfma_f32_16x16x32_bf16 v[0:3], a[200:203], a[8:11], v[0:3]\n" \
"v_exp_f32 v73, v73\n" \
"v_mfma_f32_16x16x32_bf16 v[0:3], a[204:207], a[12:15], v[0:3]\n" \
"ds_read_b128 a[32:35], v233 offset:6144\n" \
"v_cvt_pk_bf16_f32 v68, v72, v73\n" \
"v_mfma_f32_16x16x32_bf16 v[16:19], a[204:207], a[28:31], v[196:199]\n" \
"v_exp_f32 v74, v74\n" \
"v_mfma_f32_16x16x32_bf16 v[16:19], a[200:203], a[24:27], v[16:19]\n" \
"v_xor_b32 v232, 0x8000, v232\n" \
"v_mfma_f32_16x16x32_bf16 v[16:19], a[196:199], a[20:23], v[16:19]\n" \
"Lpg6196_%=:\n" \
"s_cbranch_execz Lcb0_%=\n" \
"v_exp_f32 v75, v75\n" \
"v_mfma_f32_16x16x32_bf16 v[16:19], a[192:195], a[16:19], v[16:19]\n" \
"ds_read_b128 a[36:39], v233 offset:14336\n" \
"v_cvt_pk_bf16_f32 v69, v74, v75\n" \
"v_mfma_f32_16x16x32_bf16 v[32:35], a[192:195], a[160:163], v[196:199]\n" \
"v_exp_f32 v76, v76\n" \
"v_mfma_f32_16x16x32_bf16 v[32:35], a[196:199], a[164:167], v[32:35]\n" \
"v_mfma_f32_16x16x32_bf16 v[32:35], a[200:203], a[168:171], v[32:35]\n" \
"v_exp_f32 v77, v77\n" \
"v_mfma_f32_16x16x32_bf16 v[32:35], a[204:207], a[172:175], v[32:35]\n" \
"ds_read_b128 a[40:43], v233 offset:7168\n" \
"v_cvt_pk_bf16_f32 v70, v76, v77\n" \
"v_mfma_f32_16x16x32_bf16 v[48:51], a[204:207], a[188:191], v[196:199]\n" \
"v_exp_f32 v78, v78\n" \
"v_mfma_f32_16x16x32_bf16 v[48:51], a[200:203], a[184:187], v[48:51]\n" \
"v_mfma_f32_16x16x32_bf16 v[48:51], a[196:199], a[180:183], v[48:51]\n" \
"v_exp_f32 v79, v79\n" \
"v_mfma_f32_16x16x32_bf16 v[48:51], a[192:195], a[176:179], v[48:51]\n" \
"ds_read_b128 a[204:207], v233 offset:9216\n" \
"v_cvt_pk_bf16_f32 v71, v78, v79\n" \
"v_mfma_f32_16x16x32_bf16 v[52:55], a[208:211], a[176:179], v[196:199]\n" \
"v_exp_f32 v88, v88\n" \
"v_mfma_f32_16x16x32_bf16 v[52:55], a[212:215], a[180:183], v[52:55]\n" \
"v_mfma_f32_16x16x32_bf16 v[52:55], a[216:219], a[184:187], v[52:55]\n" \
"v_exp_f32 v89, v89\n" \
"v_mfma_f32_16x16x32_bf16 v[52:55], a[220:223], a[188:191], v[52:55]\n" \
"ds_read_b128 a[200:203], v233 offset:1024\n" \
"v_cvt_pk_bf16_f32 v84, v88, v89\n" \
"v_mfma_f32_16x16x32_bf16 v[36:39], a[220:223], a[172:175], v[196:199]\n" \
"v_exp_f32 v90, v90\n" \
"v_mfma_f32_16x16x32_bf16 v[36:39], a[216:219], a[168:171], v[36:39]\n" \
"v_mfma_f32_16x16x32_bf16 v[36:39], a[212:215], a[164:167], v[36:39]\n" \
"v_exp_f32 v91, v91\n" \
"v_mfma_f32_16x16x32_bf16 v[36:39], a[208:211], a[160:163], v[36:39]\n" \
"ds_read_b128 a[196:199], v233 offset:8192\n" \
"v_cvt_pk_bf16_f32 v85, v90, v91\n" \
"v_mfma_f32_16x16x32_bf16 v[20:23], a[208:211], a[16:19], v[196:199]\n" \
"v_exp_f32 v92, v92\n" \
"v_mfma_f32_16x16x32_bf16 v[20:23], a[212:215], a[20:23], v[20:23]\n" \
"v_mfma_f32_16x16x32_bf16 v[20:23], a[216:219], a[24:27], v[20:23]\n" \
"v_exp_f32 v93, v93\n" \
"v_mfma_f32_16x16x32_bf16 v[20:23], a[220:223], a[28:31], v[20:23]\n" \
"ds_read_b128 a[192:195], v233 offset:0\n" \
"v_cvt_pk_bf16_f32 v86, v92, v93\n" \
"v_mfma_f32_16x16x32_bf16 v[4:7], a[220:223], a[12:15], v[196:199]\n" \
"v_exp_f32 v94, v94\n" \
"v_mfma_f32_16x16x32_bf16 v[4:7], a[216:219], a[8:11], v[4:7]\n" \
"v_mfma_f32_16x16x32_bf16 v[4:7], a[212:215], a[4:7], v[4:7]\n" \
"s_waitcnt lgkmcnt(8)\n" \
"v_exp_f32 v95, v95\n" \
"v_mfma_f32_16x16x32_bf16 v[4:7], a[208:211], a[0:3], v[4:7]\n" \
"ds_read_b128 a[220:223], v233 offset:11264\n" \
"v_cvt_pk_bf16_f32 v87, v94, v95\n" \
"v_mfma_f32_16x16x32_bf16 v[8:11], a[224:227], a[0:3], v[196:199]\n" \
"v_exp_f32 v104, v104\n" \
"v_mfma_f32_16x16x32_bf16 v[8:11], a[228:231], a[4:7], v[8:11]\n" \
"v_mfma_f32_16x16x32_bf16 v[8:11], a[232:235], a[8:11], v[8:11]\n" \
"v_exp_f32 v105, v105\n" \
"v_mfma_f32_16x16x32_bf16 v[8:11], a[236:239], a[12:15], v[8:11]\n" \
"ds_read_b128 a[216:219], v233 offset:3072\n" \
"v_cvt_pk_bf16_f32 v100, v104, v105\n" \
"v_mfma_f32_16x16x32_bf16 v[24:27], a[236:239], a[28:31], v[196:199]\n" \
"v_exp_f32 v106, v106\n" \
"v_mfma_f32_16x16x32_bf16 v[24:27], a[232:235], a[24:27], v[24:27]\n" \
"v_mfma_f32_16x16x32_bf16 v[24:27], a[228:231], a[20:23], v[24:27]\n" \
"v_exp_f32 v107, v107\n" \
"v_mfma_f32_16x16x32_bf16 v[24:27], a[224:227], a[16:19], v[24:27]\n" \
"ds_read_b128 a[212:215], v233 offset:10240\n" \
"v_cvt_pk_bf16_f32 v101, v106, v107\n" \
"v_mfma_f32_16x16x32_bf16 v[40:43], a[224:227], a[160:163], v[196:199]\n" \
"v_exp_f32 v108, v108\n" \
"v_mfma_f32_16x16x32_bf16 v[40:43], a[228:231], a[164:167], v[40:43]\n" \
"v_mfma_f32_16x16x32_bf16 v[40:43], a[232:235], a[168:171], v[40:43]\n" \
"v_exp_f32 v109, v109\n" \
"v_mfma_f32_16x16x32_bf16 v[40:43], a[236:239], a[172:175], v[40:43]\n" \
"ds_read_b128 a[208:211], v233 offset:2048\n" \
"v_cvt_pk_bf16_f32 v102, v108, v109\n" \
"v_mfma_f32_16x16x32_bf16 v[56:59], a[236:239], a[188:191], v[196:199]\n" \
"v_exp_f32 v110, v110\n" \
"v_mfma_f32_16x16x32_bf16 v[56:59], a[232:235], a[184:187], v[56:59]\n" \
"v_mfma_f32_16x16x32_bf16 v[56:59], a[228:231], a[180:183], v[56:59]\n" \
"v_exp_f32 v111, v111\n" \
"v_mfma_f32_16x16x32_bf16 v[56:59], a[224:227], a[176:179], v[56:59]\n" \
"ds_read_b128 a[236:239], v233 offset:13312\n" \
"v_cvt_pk_bf16_f32 v103, v110, v111\n" \
"v_mfma_f32_16x16x32_bf16 v[60:63], a[240:243], a[176:179], v[196:199]\n" \
"v_exp_f32 v120, v120\n" \
"v_mfma_f32_16x16x32_bf16 v[60:63], a[244:247], a[180:183], v[60:63]\n" \
"ds_read_b128 a[232:235], v233 offset:5120\n" \
"v_mfma_f32_16x16x32_bf16 v[60:63], a[248:251], a[184:187], v[60:63]\n" \
"v_exp_f32 v121, v121\n" \
"v_mfma_f32_16x16x32_bf16 v[60:63], a[252:255], a[188:191], v[60:63]\n" \
"ds_read_b128 a[228:231], v233 offset:12288\n" \
"v_cvt_pk_bf16_f32 v116, v120, v121\n" \
"v_mfma_f32_16x16x32_bf16 v[44:47], a[252:255], a[172:175], v[196:199]\n" \
"v_exp_f32 v122, v122\n" \
"v_mfma_f32_16x16x32_bf16 v[44:47], a[248:251], a[168:171], v[44:47]\n" \
"ds_read_b128 a[224:227], v233 offset:4096\n" \
"v_mfma_f32_16x16x32_bf16 v[44:47], a[244:247], a[164:167], v[44:47]\n" \
"v_exp_f32 v123, v123\n" \
"v_mfma_f32_16x16x32_bf16 v[44:47], a[240:243], a[160:163], v[44:47]\n" \
"v_cvt_pk_bf16_f32 v117, v122, v123\n" \
"v_mfma_f32_16x16x32_bf16 v[28:31], a[240:243], a[16:19], v[196:199]\n" \
"v_exp_f32 v124, v124\n" \
"v_mfma_f32_16x16x32_bf16 v[28:31], a[244:247], a[20:23], v[28:31]\n" \
"v_mfma_f32_16x16x32_bf16 v[28:31], a[248:251], a[24:27], v[28:31]\n" \
"v_exp_f32 v125, v125\n" \
"v_mfma_f32_16x16x32_bf16 v[28:31], a[252:255], a[28:31], v[28:31]\n" \
"v_cvt_pk_bf16_f32 v118, v124, v125\n" \
"v_mfma_f32_16x16x32_bf16 v[12:15], a[252:255], a[12:15], v[196:199]\n" \
"v_exp_f32 v126, v126\n" \
"v_mfma_f32_16x16x32_bf16 v[12:15], a[248:251], a[8:11], v[12:15]\n" \
"v_mfma_f32_16x16x32_bf16 v[12:15], a[244:247], a[4:7], v[12:15]\n" \
"s_waitcnt vmcnt(0) lgkmcnt(0)\n" \
"v_exp_f32 v127, v127\n" \
"v_mfma_f32_16x16x32_bf16 v[12:15], a[240:243], a[0:3], v[12:15]\n" \
"s_barrier\n" \
"v_cvt_pk_bf16_f32 v119, v126, v127\n" \
"v_mfma_f32_16x16x32_bf16 v[128:131], a[192:195], v[64:67], v[128:131]\n" \
"v_exp_f32 v0, v0\n" \
"v_mfma_f32_16x16x32_bf16 v[128:131], a[196:199], v[68:71], v[128:131]\n" \
"v_cmp_eq_u32 vcc, s60, v239\n" \
"v_mfma_f32_16x16x32_bf16 v[160:163], a[192:195], v[80:83], v[160:163]\n" \
"v_exp_f32 v1, v1\n" \
"v_mfma_f32_16x16x32_bf16 v[160:163], a[196:199], v[84:87], v[160:163]\n" \
"s_andn2_b32 s70, s69, vcc_lo\n" \
"s_cbranch_scc1 Lpl3_%=\n" \
"Lpr3_%=:\n" \
"ds_read_b128 a[240:243], v232 offset:8448\n" \
"v_cvt_pk_bf16_f32 v0, v0, v1\n" \
"v_mfma_f32_16x16x32_bf16 a[64:67], a[192:195], v[96:99], a[64:67]\n" \
"s_add_u32 m0, s74, s79\n" \
"v_exp_f32 v2, v2\n" \
"v_mfma_f32_16x16x32_bf16 a[64:67], a[196:199], v[100:103], a[64:67]\n" \
"buffer_load_dwordx4 v234, s[36:39], s73 offen offset:0 sc1 lds\n" \
"v_mfma_f32_16x16x32_bf16 a[96:99], a[192:195], v[112:115], a[96:99]\n" \
"v_exp_f32 v3, v3\n" \
"v_mfma_f32_16x16x32_bf16 a[96:99], a[196:199], v[116:119], a[96:99]\n" \
"ds_read_b128 a[192:195], v232 offset:0\n" \
"v_cvt_pk_bf16_f32 v1, v2, v3\n" \
"v_mfma_f32_16x16x32_bf16 a[100:103], a[200:203], v[112:115], a[100:103]\n" \
"s_add_u32 s71, s73, 17408\n" \
"v_exp_f32 v4, v4\n" \
"v_mfma_f32_16x16x32_bf16 a[100:103], a[204:207], v[116:119], a[100:103]\n" \
"buffer_load_dwordx4 v234, s[36:39], s73 offen offset:1024 sc1 lds\n" \
"v_mfma_f32_16x16x32_bf16 a[68:71], a[200:203], v[96:99], a[68:71]\n" \
"v_exp_f32 v5, v5\n" \
"v_mfma_f32_16x16x32_bf16 a[68:71], a[204:207], v[100:103], a[68:71]\n" \
"ds_read_b128 a[196:199], v232 offset:2048\n" \
"v_cvt_pk_bf16_f32 v2, v4, v5\n" \
"v_smfmac_f32_16x16x64_bf16 a[48:51], v[244:247], v[64:71], v243\n" \
"buffer_load_dwordx4 v234, s[36:39], s73 offen offset:2048 sc1 lds\n" \
"v_mfma_f32_16x16x32_bf16 v[164:167], a[200:203], v[80:83], v[164:167]\n" \
"v_exp_f32 v6, v6\n" \
"v_mfma_f32_16x16x32_bf16 v[164:167], a[204:207], v[84:87], v[164:167]\n" \
"buffer_load_dwordx4 v234, s[36:39], s73 offen offset:3072 sc1 lds\n" \
"v_mfma_f32_16x16x32_bf16 v[132:135], a[200:203], v[64:67], v[132:135]\n" \
"v_exp_f32 v7, v7\n" \
"v_mfma_f32_16x16x32_bf16 v[132:135], a[204:207], v[68:71], v[132:135]\n" \
"ds_read_b128 a[200:203], v232 offset:4096\n" \
"v_cvt_pk_bf16_f32 v3, v6, v7\n" \
"v_mfma_f32_16x16x32_bf16 v[136:139], a[208:211], v[64:67], v[136:139]\n" \
"s_add_u32 m0, m0, 0x4000\n" \
"v_exp_f32 v16, v16\n" \
"v_mfma_f32_16x16x32_bf16 v[136:139], a[212:215], v[68:71], v[136:139]\n" \
"buffer_load_dwordx4 v234, s[36:39], s71 offen offset:0 sc1 lds\n" \
"v_mfma_f32_16x16x32_bf16 v[168:171], a[208:211], v[80:83], v[168:171]\n" \
"v_exp_f32 v17, v17\n" \
"v_mfma_f32_16x16x32_bf16 v[168:171], a[212:215], v[84:87], v[168:171]\n" \
"ds_read_b128 a[204:207], v232 offset:6144\n" \
"v_cvt_pk_bf16_f32 v16, v16, v17\n" \
"v_mfma_f32_16x16x32_bf16 a[72:75], a[208:211], v[96:99], a[72:75]\n" \
"v_exp_f32 v18, v18\n" \
"v_mfma_f32_16x16x32_bf16 a[72:75], a[212:215], v[100:103], a[72:75]\n" \
"buffer_load_dwordx4 v234, s[36:39], s71 offen offset:1024 sc1 lds\n" \
"v_mfma_f32_16x16x32_bf16 a[104:107], a[208:211], v[112:115], a[104:107]\n" \
"v_exp_f32 v19, v19\n" \
"v_mfma_f32_16x16x32_bf16 a[104:107], a[212:215], v[116:119], a[104:107]\n" \
"ds_read_b128 a[208:211], v232 offset:256\n" \
"v_cvt_pk_bf16_f32 v17, v18, v19\n" \
"v_mfma_f32_16x16x32_bf16 a[108:111], a[216:219], v[112:115], a[108:111]\n" \
"v_exp_f32 v20, v20\n" \
"v_mfma_f32_16x16x32_bf16 a[108:111], a[220:223], v[116:119], a[108:111]\n" \
"buffer_load_dwordx4 v234, s[36:39], s71 offen offset:2048 sc1 lds\n" \
"v_mfma_f32_16x16x32_bf16 a[76:79], a[216:219], v[96:99], a[76:79]\n" \
"v_exp_f32 v21, v21\n" \
"v_mfma_f32_16x16x32_bf16 a[76:79], a[220:223], v[100:103], a[76:79]\n" \
"ds_read_b128 a[212:215], v232 offset:2304\n" \
"v_cvt_pk_bf16_f32 v18, v20, v21\n" \
"v_smfmac_f32_16x16x64_bf16 a[52:55], v[244:247], v[80:87], v243\n" \
"buffer_load_dwordx4 v234, s[36:39], s71 offen offset:3072 sc1 lds\n" \
"v_mfma_f32_16x16x32_bf16 v[172:175], a[216:219], v[80:83], v[172:175]\n" \
"s_add_u32 m0, s77, s80\n" \
"v_exp_f32 v22, v22\n" \
"v_mfma_f32_16x16x32_bf16 v[172:175], a[220:223], v[84:87], v[172:175]\n" \
"buffer_load_dwordx4 v234, s[40:43], s76 offen offset:0 sc1 lds\n" \
"v_mfma_f32_16x16x32_bf16 v[140:143], a[216:219], v[64:67], v[140:143]\n" \
"v_exp_f32 v23, v23\n" \
"v_mfma_f32_16x16x32_bf16 v[140:143], a[220:223], v[68:71], v[140:143]\n" \
"ds_read_b128 a[216:219], v232 offset:4352\n" \
"v_cvt_pk_bf16_f32 v19, v22, v23\n" \
"v_mfma_f32_16x16x32_bf16 v[144:147], a[224:227], v[64:67], v[144:147]\n" \
"s_add_u32 s71, s76, 17408\n" \
"v_exp_f32 v32, v32\n" \
"v_mfma_f32_16x16x32_bf16 v[144:147], a[228:231], v[68:71], v[144:147]\n" \
"buffer_load_dwordx4 v234, s[40:43], s76 offen offset:1024 sc1 lds\n" \
"v_mfma_f32_16x16x32_bf16 v[176:179], a[224:227], v[80:83], v[176:179]\n" \
"v_exp_f32 v33, v33\n" \
"v_mfma_f32_16x16x32_bf16 v[176:179], a[228:231], v[84:87], v[176:179]\n" \
"ds_read_b128 a[220:223], v232 offset:6400\n" \
"v_cvt_pk_bf16_f32 v32, v32, v33\n" \
"v_mfma_f32_16x16x32_bf16 a[80:83], a[224:227], v[96:99], a[80:83]\n" \
"v_exp_f32 v34, v34\n" \
"v_mfma_f32_16x16x32_bf16 a[80:83], a[228:231], v[100:103], a[80:83]\n" \
"buffer_load_dwordx4 v234, s[40:43], s76 offen offset:2048 sc1 lds\n" \
"v_mfma_f32_16x16x32_bf16 a[112:115], a[224:227], v[112:115], a[112:115]\n" \
"v_exp_f32 v35, v35\n" \
"v_mfma_f32_16x16x32_bf16 a[112:115], a[228:231], v[116:119], a[112:115]\n" \
"ds_read_b128 a[224:227], v232 offset:8192\n" \
"v_cvt_pk_bf16_f32 v33, v34, v35\n" \
"v_mfma_f32_16x16x32_bf16 a[116:119], a[232:235], v[112:115], a[116:119]\n" \
"v_exp_f32 v36, v36\n" \
"v_mfma_f32_16x16x32_bf16 a[116:119], a[236:239], v[116:119], a[116:119]\n" \
"buffer_load_dwordx4 v234, s[40:43], s76 offen offset:3072 sc1 lds\n" \
"v_mfma_f32_16x16x32_bf16 a[84:87], a[232:235], v[96:99], a[84:87]\n" \
"v_exp_f32 v37, v37\n" \
"v_mfma_f32_16x16x32_bf16 a[84:87], a[236:239], v[100:103], a[84:87]\n" \
"s_add_u32 m0, m0, 0x4000\n" \
"ds_read_b128 a[228:231], v232 offset:10240\n" \
"v_cvt_pk_bf16_f32 v34, v36, v37\n" \
"v_smfmac_f32_16x16x64_bf16 v[224:227], v[244:247], v[96:103], v243\n" \
"buffer_load_dwordx4 v234, s[40:43], s71 offen offset:0 sc1 lds\n" \
"v_mfma_f32_16x16x32_bf16 v[180:183], a[232:235], v[80:83], v[180:183]\n" \
"v_exp_f32 v38, v38\n" \
"v_mfma_f32_16x16x32_bf16 v[180:183], a[236:239], v[84:87], v[180:183]\n" \
"buffer_load_dwordx4 v234, s[40:43], s71 offen offset:1024 sc1 lds\n" \
"v_mfma_f32_16x16x32_bf16 v[148:151], a[232:235], v[64:67], v[148:151]\n" \
"v_exp_f32 v39, v39\n" \
"v_mfma_f32_16x16x32_bf16 v[148:151], a[236:239], v[68:71], v[148:151]\n" \
"ds_read_b128 a[232:235], v232 offset:12288\n" \
"v_cvt_pk_bf16_f32 v35, v38, v39\n" \
"v_mfma_f32_16x16x32_bf16 v[152:155], a[32:35], v[64:67], v[152:155]\n" \
"v_exp_f32 v48, v48\n" \
"v_mfma_f32_16x16x32_bf16 v[152:155], a[36:39], v[68:71], v[152:155]\n" \
"buffer_load_dwordx4 v234, s[40:43], s71 offen offset:2048 sc1 lds\n" \
"v_mfma_f32_16x16x32_bf16 v[184:187], a[32:35], v[80:83], v[184:187]\n" \
"v_exp_f32 v49, v49\n" \
"v_mfma_f32_16x16x32_bf16 v[184:187], a[36:39], v[84:87], v[184:187]\n" \
"ds_read_b128 a[236:239], v232 offset:14336\n" \
"v_cvt_pk_bf16_f32 v48, v48, v49\n" \
"v_mfma_f32_16x16x32_bf16 a[88:91], a[32:35], v[96:99], a[88:91]\n" \
"v_exp_f32 v50, v50\n" \
"v_mfma_f32_16x16x32_bf16 a[88:91], a[36:39], v[100:103], a[88:91]\n" \
"buffer_load_dwordx4 v234, s[40:43], s71 offen offset:3072 sc1 lds\n" \
"v_mfma_f32_16x16x32_bf16 a[120:123], a[32:35], v[112:115], a[120:123]\n" \
"s_mov_b32 s76, s73\n" \
"v_exp_f32 v51, v51\n" \
"v_mfma_f32_16x16x32_bf16 a[120:123], a[36:39], v[116:119], a[120:123]\n" \
"s_add_u32 s73, s73, 34816\n" \
"ds_read_b128 a[244:247], v232 offset:10496\n" \
"v_cvt_pk_bf16_f32 v49, v50, v51\n" \
"v_mfma_f32_16x16x32_bf16 a[124:127], a[40:43], v[112:115], a[124:127]\n" \
"s_xor_b32 s74, s74, 0x8000\n" \
"v_exp_f32 v52, v52\n" \
"v_mfma_f32_16x16x32_bf16 a[124:127], a[44:47], v[116:119], a[124:127]\n" \
"s_xor_b32 s77, s77, 0x8000\n" \
"v_xor_b32 v233, s81, v233\n" \
"s_mov_b32 s81, 0x8000\n" \
"v_mfma_f32_16x16x32_bf16 a[92:95], a[40:43], v[96:99], a[92:95]\n" \
"v_exp_f32 v53, v53\n" \
"v_mfma_f32_16x16x32_bf16 a[92:95], a[44:47], v[100:103], a[92:95]\n" \
"ds_read_b128 a[248:251], v232 offset:12544\n" \
"v_cvt_pk_bf16_f32 v50, v52, v53\n" \
"v_smfmac_f32_16x16x64_bf16 v[228:231], v[244:247], v[112:119], v243\n" \
"v_mfma_f32_16x16x32_bf16 v[188:191], a[40:43], v[80:83], v[188:191]\n" \
"v_exp_f32 v54, v54\n" \
"v_mfma_f32_16x16x32_bf16 v[188:191], a[44:47], v[84:87], v[188:191]\n" \
"s_cmp_lg_u32 s82, 0\n" \
"v_exp_f32 v55, v55\n" \
"v_mfma_f32_16x16x32_bf16 v[156:159], a[40:43], v[64:67], v[156:159]\n" \
"s_waitcnt lgkmcnt(2)\n" \
"ds_read_b128 a[252:255], v232 offset:14592\n" \
"v_cvt_pk_bf16_f32 v51, v54, v55\n" \
"v_mfma_f32_16x16x32_bf16 v[156:159], a[44:47], v[68:71], v[156:159]\n" \
"s_cbranch_scc1 Lep_%=\n" \
"Lepr_%=:\n" \
"v_mfma_f32_16x16x32_bf16 v[64:67], a[192:195], a[0:3], v[196:199]\n" \
"v_exp_f32 v8, v8\n" \
"v_mfma_f32_16x16x32_bf16 v[64:67], a[196:199], a[4:7], v[64:67]\n" \
"ds_read_b128 a[44:47], v233 offset:15360\n" \
"v_mfma_f32_16x16x32_bf16 v[64:67], a[200:203], a[8:11], v[64:67]\n" \
"v_exp_f32 v9, v9\n" \
"v_mfma_f32_16x16x32_bf16 v[64:67], a[204:207], a[12:15], v[64:67]\n" \
"ds_read_b128 a[32:35], v233 offset:6144\n" \
"v_cvt_pk_bf16_f32 v4, v8, v9\n" \
"v_mfma_f32_16x16x32_bf16 v[80:83], a[204:207], a[28:31], v[196:199]\n" \
"s_sub_u32 s84, s84, 1\n" \
"v_exp_f32 v10, v10\n" \
"v_mfma_f32_16x16x32_bf16 v[80:83], a[200:203], a[24:27], v[80:83]\n" \
"s_cmp_eq_u32 s84, 0\n" \
"s_cselect_b32 s70, s75, 0x8000\n" \
"v_xor_b32 v232, s70, v232\n" \
"v_mfma_f32_16x16x32_bf16 v[80:83], a[196:199], a[20:23], v[80:83]\n" \
"v_exp_f32 v11, v11\n" \
"v_mfma_f32_16x16x32_bf16 v[80:83], a[192:195], a[16:19], v[80:83]\n" \
"ds_read_b128 a[36:39], v233 offset:14336\n" \
"v_cvt_pk_bf16_f32 v5, v10, v11\n" \
"v_mfma_f32_16x16x32_bf16 v[96:99], a[192:195], a[160:163], v[196:199]\n" \
"v_exp_f32 v12, v12\n" \
"v_mfma_f32_16x16x32_bf16 v[96:99], a[196:199], a[164:167], v[96:99]\n" \
"v_mfma_f32_16x16x32_bf16 v[96:99], a[200:203], a[168:171], v[96:99]\n" \
"v_exp_f32 v13, v13\n" \
"v_mfma_f32_16x16x32_bf16 v[96:99], a[204:207], a[172:175], v[96:99]\n" \
"ds_read_b128 a[40:43], v233 offset:7168\n" \
"v_cvt_pk_bf16_f32 v6, v12, v13\n" \
"v_mfma_f32_16x16x32_bf16 v[112:115], a[204:207], a[188:191], v[196:199]\n" \
"v_exp_f32 v14, v14\n" \
"v_mfma_f32_16x16x32_bf16 v[112:115], a[200:203], a[184:187], v[112:115]\n" \
"v_mfma_f32_16x16x32_bf16 v[112:115], a[196:199], a[180:183], v[112:115]\n" \
"v_exp_f32 v15, v15\n" \
"v_mfma_f32_16x16x32_bf16 v[112:115], a[192:195], a[176:179], v[112:115]\n" \
"ds_read_b128 a[204:207], v233 offset:9216\n" \
"v_cvt_pk_bf16_f32 v7, v14, v15\n" \
"v_mfma_f32_16x16x32_bf16 v[116:119], a[208:211], a[176:179], v[196:199]\n" \
"v_exp_f32 v24, v24\n" \
"v_mfma_f32_16x16x32_bf16 v[116:119], a[212:215], a[180:183], v[116:119]\n" \
"v_mfma_f32_16x16x32_bf16 v[116:119], a[216:219], a[184:187], v[116:119]\n" \
"v_exp_f32 v25, v25\n" \
"v_mfma_f32_16x16x32_bf16 v[116:119], a[220:223], a[188:191], v[116:119]\n" \
"ds_read_b128 a[200:203], v233 offset:1024\n" \
"v_cvt_pk_bf16_f32 v20, v24, v25\n" \
"v_mfma_f32_16x16x32_bf16 v[100:103], a[220:223], a[172:175], v[196:199]\n" \
"v_exp_f32 v26, v26\n" \
"v_mfma_f32_16x16x32_bf16 v[100:103], a[216:219], a[168:171], v[100:103]\n" \
"v_mfma_f32_16x16x32_bf16 v[100:103], a[212:215], a[164:167], v[100:103]\n" \
"v_exp_f32 v27, v27\n" \
"v_mfma_f32_16x16x32_bf16 v[100:103], a[208:211], a[160:163], v[100:103]\n" \
"ds_read_b128 a[196:199], v233 offset:8192\n" \
"v_cvt_pk_bf16_f32 v21, v26, v27\n" \
"v_mfma_f32_16x16x32_bf16 v[84:87], a[208:211], a[16:19], v[196:199]\n" \
"v_exp_f32 v28, v28\n" \
"v_mfma_f32_16x16x32_bf16 v[84:87], a[212:215], a[20:23], v[84:87]\n" \
"v_mfma_f32_16x16x32_bf16 v[84:87], a[216:219], a[24:27], v[84:87]\n" \
"v_exp_f32 v29, v29\n" \
"v_mfma_f32_16x16x32_bf16 v[84:87], a[220:223], a[28:31], v[84:87]\n" \
"ds_read_b128 a[192:195], v233 offset:0\n" \
"v_cvt_pk_bf16_f32 v22, v28, v29\n" \
"v_mfma_f32_16x16x32_bf16 v[68:71], a[220:223], a[12:15], v[196:199]\n" \
"v_exp_f32 v30, v30\n" \
"v_mfma_f32_16x16x32_bf16 v[68:71], a[216:219], a[8:11], v[68:71]\n" \
"v_mfma_f32_16x16x32_bf16 v[68:71], a[212:215], a[4:7], v[68:71]\n" \
"s_waitcnt lgkmcnt(8)\n" \
"v_exp_f32 v31, v31\n" \
"v_mfma_f32_16x16x32_bf16 v[68:71], a[208:211], a[0:3], v[68:71]\n" \
"ds_read_b128 a[220:223], v233 offset:11264\n" \
"v_cvt_pk_bf16_f32 v23, v30, v31\n" \
"v_mfma_f32_16x16x32_bf16 v[72:75], a[224:227], a[0:3], v[196:199]\n" \
"v_exp_f32 v40, v40\n" \
"v_mfma_f32_16x16x32_bf16 v[72:75], a[228:231], a[4:7], v[72:75]\n" \
"v_mfma_f32_16x16x32_bf16 v[72:75], a[232:235], a[8:11], v[72:75]\n" \
"v_exp_f32 v41, v41\n" \
"v_mfma_f32_16x16x32_bf16 v[72:75], a[236:239], a[12:15], v[72:75]\n" \
"ds_read_b128 a[216:219], v233 offset:3072\n" \
"v_cvt_pk_bf16_f32 v36, v40, v41\n" \
"v_mfma_f32_16x16x32_bf16 v[88:91], a[236:239], a[28:31], v[196:199]\n" \
"v_exp_f32 v42, v42\n" \
"v_mfma_f32_16x16x32_bf16 v[88:91], a[232:235], a[24:27], v[88:91]\n" \
"v_mfma_f32_16x16x32_bf16 v[88:91], a[228:231], a[20:23], v[88:91]\n" \
"v_exp_f32 v43, v43\n" \
"v_mfma_f32_16x16x32_bf16 v[88:91], a[224:227], a[16:19], v[88:91]\n" \
"ds_read_b128 a[212:215], v233 offset:10240\n" \
"v_cvt_pk_bf16_f32 v37, v42, v43\n" \
"v_mfma_f32_16x16x32_bf16 v[104:107], a[224:227], a[160:163], v[196:199]\n" \
"v_exp_f32 v44, v44\n" \
"v_mfma_f32_16x16x32_bf16 v[104:107], a[228:231], a[164:167], v[104:107]\n" \
"v_mfma_f32_16x16x32_bf16 v[104:107], a[232:235], a[168:171], v[104:107]\n" \
"v_exp_f32 v45, v45\n" \
"v_mfma_f32_16x16x32_bf16 v[104:107], a[236:239], a[172:175], v[104:107]\n" \
"ds_read_b128 a[208:211], v233 offset:2048\n" \
"v_cvt_pk_bf16_f32 v38, v44, v45\n" \
"v_mfma_f32_16x16x32_bf16 v[120:123], a[236:239], a[188:191], v[196:199]\n" \
"v_exp_f32 v46, v46\n" \
"v_mfma_f32_16x16x32_bf16 v[120:123], a[232:235], a[184:187], v[120:123]\n" \
"v_mfma_f32_16x16x32_bf16 v[120:123], a[228:231], a[180:183], v[120:123]\n" \
"v_exp_f32 v47, v47\n" \
"v_mfma_f32_16x16x32_bf16 v[120:123], a[224:227], a[176:179], v[120:123]\n" \
"ds_read_b128 a[236:239], v233 offset:13312\n" \
"v_cvt_pk_bf16_f32 v39, v46, v47\n" \
"v_mfma_f32_16x16x32_bf16 v[124:127], a[240:243], a[176:179], v[196:199]\n" \
"v_exp_f32 v56, v56\n" \
"v_mfma_f32_16x16x32_bf16 v[124:127], a[244:247], a[180:183], v[124:127]\n" \
"ds_read_b128 a[232:235], v233 offset:5120\n" \
"v_mfma_f32_16x16x32_bf16 v[124:127], a[248:251], a[184:187], v[124:127]\n" \
"v_exp_f32 v57, v57\n" \
"v_mfma_f32_16x16x32_bf16 v[124:127], a[252:255], a[188:191], v[124:127]\n" \
"ds_read_b128 a[228:231], v233 offset:12288\n" \
"v_cvt_pk_bf16_f32 v52, v56, v57\n" \
"v_mfma_f32_16x16x32_bf16 v[108:111], a[252:255], a[172:175], v[196:199]\n" \
"v_exp_f32 v58, v58\n" \
"v_mfma_f32_16x16x32_bf16 v[108:111], a[248:251], a[168:171], v[108:111]\n" \
"ds_read_b128 a[224:227], v233 offset:4096\n" \
"v_mfma_f32_16x16x32_bf16 v[108:111], a[244:247], a[164:167], v[108:111]\n" \
"v_exp_f32 v59, v59\n" \
"v_mfma_f32_16x16x32_bf16 v[108:111], a[240:243], a[160:163], v[108:111]\n" \
"v_cvt_pk_bf16_f32 v53, v58, v59\n" \
"v_mfma_f32_16x16x32_bf16 v[92:95], a[240:243], a[16:19], v[196:199]\n" \
"v_exp_f32 v60, v60\n" \
"v_mfma_f32_16x16x32_bf16 v[92:95], a[244:247], a[20:23], v[92:95]\n" \
"v_mfma_f32_16x16x32_bf16 v[92:95], a[248:251], a[24:27], v[92:95]\n" \
"v_exp_f32 v61, v61\n" \
"v_mfma_f32_16x16x32_bf16 v[92:95], a[252:255], a[28:31], v[92:95]\n" \
"v_cvt_pk_bf16_f32 v54, v60, v61\n" \
"v_mfma_f32_16x16x32_bf16 v[76:79], a[252:255], a[12:15], v[196:199]\n" \
"v_exp_f32 v62, v62\n" \
"v_mfma_f32_16x16x32_bf16 v[76:79], a[248:251], a[8:11], v[76:79]\n" \
"s_cmp_lg_u32 s82, 0\n" \
"s_cbranch_scc1 Lnw_%=\n" \
"s_waitcnt vmcnt(0)\n" \
"Lnw_%=:\n" \
"v_mfma_f32_16x16x32_bf16 v[76:79], a[244:247], a[4:7], v[76:79]\n" \
"s_waitcnt lgkmcnt(0)\n" \
"v_exp_f32 v63, v63\n" \
"v_mfma_f32_16x16x32_bf16 v[76:79], a[240:243], a[0:3], v[76:79]\n" \
"s_barrier\n" \
"v_cvt_pk_bf16_f32 v55, v62, v63\n" \
"v_mfma_f32_16x16x32_bf16 v[128:131], a[192:195], v[0:3], v[128:131]\n" \
"s_add_u32 m0, s74, s79\n" \
"v_exp_f32 v64, v64\n" \
"v_mfma_f32_16x16x32_bf16 v[128:131], a[196:199], v[4:7], v[128:131]\n" \
"buffer_load_dwordx4 v234, s[36:39], s73 offen offset:0 sc1 lds\n" \
"v_mfma_f32_16x16x32_bf16 v[160:163], a[192:195], v[16:19], v[160:163]\n" \
"v_exp_f32 v65, v65\n" \
"v_mfma_f32_16x16x32_bf16 v[160:163], a[196:199], v[20:23], v[160:163]\n" \
"ds_read_b128 a[240:243], v232 offset:8448\n" \
"v_cvt_pk_bf16_f32 v64, v64, v65\n" \
"v_mfma_f32_16x16x32_bf16 a[64:67], a[192:195], v[32:35], a[64:67]\n" \
"s_add_u32 s71, s73, 17408\n" \
"v_exp_f32 v66, v66\n" \
"v_mfma_f32_16x16x32_bf16 a[64:67], a[196:199], v[36:39], a[64:67]\n" \
"buffer_load_dwordx4 v234, s[36:39], s73 offen offset:1024 sc1 lds\n" \
"v_mfma_f32_16x16x32_bf16 a[96:99], a[192:195], v[48:51], a[96:99]\n" \
"v_exp_f32 v67, v67\n" \
"v_mfma_f32_16x16x32_bf16 a[96:99], a[196:199], v[52:55], a[96:99]\n" \
"ds_read_b128 a[192:195], v232 offset:0\n" \
"v_cvt_pk_bf16_f32 v65, v66, v67\n" \
"v_mfma_f32_16x16x32_bf16 a[100:103], a[200:203], v[48:51], a[100:103]\n" \
"v_exp_f32 v68, v68\n" \
"v_mfma_f32_16x16x32_bf16 a[100:103], a[204:207], v[52:55], a[100:103]\n" \
"buffer_load_dwordx4 v234, s[36:39], s73 offen offset:2048 sc1 lds\n" \
"v_mfma_f32_16x16x32_bf16 a[68:71], a[200:203], v[32:35], a[68:71]\n" \
"v_exp_f32 v69, v69\n" \
"v_mfma_f32_16x16x32_bf16 a[68:71], a[204:207], v[36:39], a[68:71]\n" \
"ds_read_b128 a[196:199], v232 offset:2048\n" \
"v_cvt_pk_bf16_f32 v66, v68, v69\n" \
"v_smfmac_f32_16x16x64_bf16 a[48:51], v[244:247], v[0:7], v243\n" \
"buffer_load_dwordx4 v234, s[36:39], s73 offen offset:3072 sc1 lds\n" \
"v_mfma_f32_16x16x32_bf16 v[164:167], a[200:203], v[16:19], v[164:167]\n" \
"s_add_u32 m0, m0, 0x4000\n" \
"v_exp_f32 v70, v70\n" \
"v_mfma_f32_16x16x32_bf16 v[164:167], a[204:207], v[20:23], v[164:167]\n" \
"buffer_load_dwordx4 v234, s[36:39], s71 offen offset:0 sc1 lds\n" \
"v_mfma_f32_16x16x32_bf16 v[132:135], a[200:203], v[0:3], v[132:135]\n" \
"v_exp_f32 v71, v71\n" \
"v_mfma_f32_16x16x32_bf16 v[132:135], a[204:207], v[4:7], v[132:135]\n" \
"ds_read_b128 a[200:203], v232 offset:4096\n" \
"v_cvt_pk_bf16_f32 v67, v70, v71\n" \
"v_mfma_f32_16x16x32_bf16 v[136:139], a[208:211], v[0:3], v[136:139]\n" \
"v_exp_f32 v80, v80\n" \
"v_mfma_f32_16x16x32_bf16 v[136:139], a[212:215], v[4:7], v[136:139]\n" \
"buffer_load_dwordx4 v234, s[36:39], s71 offen offset:1024 sc1 lds\n" \
"v_mfma_f32_16x16x32_bf16 v[168:171], a[208:211], v[16:19], v[168:171]\n" \
"v_exp_f32 v81, v81\n" \
"v_mfma_f32_16x16x32_bf16 v[168:171], a[212:215], v[20:23], v[168:171]\n" \
"ds_read_b128 a[204:207], v232 offset:6144\n" \
"v_cvt_pk_bf16_f32 v80, v80, v81\n" \
"v_mfma_f32_16x16x32_bf16 a[72:75], a[208:211], v[32:35], a[72:75]\n" \
"v_exp_f32 v82, v82\n" \
"v_mfma_f32_16x16x32_bf16 a[72:75], a[212:215], v[36:39], a[72:75]\n" \
"buffer_load_dwordx4 v234, s[36:39], s71 offen offset:2048 sc1 lds\n" \
"v_mfma_f32_16x16x32_bf16 a[104:107], a[208:211], v[48:51], a[104:107]\n" \
"v_exp_f32 v83, v83\n" \
"v_mfma_f32_16x16x32_bf16 a[104:107], a[212:215], v[52:55], a[104:107]\n" \
"ds_read_b128 a[208:211], v232 offset:256\n" \
"v_cvt_pk_bf16_f32 v81, v82, v83\n" \
"v_mfma_f32_16x16x32_bf16 a[108:111], a[216:219], v[48:51], a[108:111]\n" \
"v_exp_f32 v84, v84\n" \
"v_mfma_f32_16x16x32_bf16 a[108:111], a[220:223], v[52:55], a[108:111]\n" \
"buffer_load_dwordx4 v234, s[36:39], s71 offen offset:3072 sc1 lds\n" \
"v_mfma_f32_16x16x32_bf16 a[76:79], a[216:219], v[32:35], a[76:79]\n" \
"v_exp_f32 v85, v85\n" \
"v_mfma_f32_16x16x32_bf16 a[76:79], a[220:223], v[36:39], a[76:79]\n" \
"s_add_u32 m0, s77, s80\n" \
"ds_read_b128 a[212:215], v232 offset:2304\n" \
"v_cvt_pk_bf16_f32 v82, v84, v85\n" \
"v_smfmac_f32_16x16x64_bf16 a[52:55], v[244:247], v[16:23], v243\n" \
"buffer_load_dwordx4 v234, s[40:43], s76 offen offset:0 sc1 lds\n" \
"v_mfma_f32_16x16x32_bf16 v[172:175], a[216:219], v[16:19], v[172:175]\n" \
"s_add_u32 s71, s76, 17408\n" \
"v_exp_f32 v86, v86\n" \
"v_mfma_f32_16x16x32_bf16 v[172:175], a[220:223], v[20:23], v[172:175]\n" \
"buffer_load_dwordx4 v234, s[40:43], s76 offen offset:1024 sc1 lds\n" \
"v_mfma_f32_16x16x32_bf16 v[140:143], a[216:219], v[0:3], v[140:143]\n" \
"v_exp_f32 v87, v87\n" \
"v_mfma_f32_16x16x32_bf16 v[140:143], a[220:223], v[4:7], v[140:143]\n" \
"ds_read_b128 a[216:219], v232 offset:4352\n" \
"v_cvt_pk_bf16_f32 v83, v86, v87\n" \
"v_mfma_f32_16x16x32_bf16 v[144:147], a[224:227], v[0:3], v[144:147]\n" \
"v_exp_f32 v96, v96\n" \
"v_mfma_f32_16x16x32_bf16 v[144:147], a[228:231], v[4:7], v[144:147]\n" \
"buffer_load_dwordx4 v234, s[40:43], s76 offen offset:2048 sc1 lds\n" \
"v_mfma_f32_16x16x32_bf16 v[176:179], a[224:227], v[16:19], v[176:179]\n" \
"v_exp_f32 v97, v97\n" \
"v_mfma_f32_16x16x32_bf16 v[176:179], a[228:231], v[20:23], v[176:179]\n" \
"ds_read_b128 a[220:223], v232 offset:6400\n" \
"v_cvt_pk_bf16_f32 v96, v96, v97\n" \
"v_mfma_f32_16x16x32_bf16 a[80:83], a[224:227], v[32:35], a[80:83]\n" \
"v_exp_f32 v98, v98\n" \
"v_mfma_f32_16x16x32_bf16 a[80:83], a[228:231], v[36:39], a[80:83]\n" \
"buffer_load_dwordx4 v234, s[40:43], s76 offen offset:3072 sc1 lds\n" \
"v_mfma_f32_16x16x32_bf16 a[112:115], a[224:227], v[48:51], a[112:115]\n" \
"v_exp_f32 v99, v99\n" \
"v_mfma_f32_16x16x32_bf16 a[112:115], a[228:231], v[52:55], a[112:115]\n" \
"ds_read_b128 a[224:227], v232 offset:8192\n" \
"v_cvt_pk_bf16_f32 v97, v98, v99\n" \
"v_mfma_f32_16x16x32_bf16 a[116:119], a[232:235], v[48:51], a[116:119]\n" \
"s_add_u32 m0, m0, 0x4000\n" \
"v_exp_f32 v100, v100\n" \
"v_mfma_f32_16x16x32_bf16 a[116:119], a[236:239], v[52:55], a[116:119]\n" \
"buffer_load_dwordx4 v234, s[40:43], s71 offen offset:0 sc1 lds\n" \
"v_mfma_f32_16x16x32_bf16 a[84:87], a[232:235], v[32:35], a[84:87]\n" \
"v_exp_f32 v101, v101\n" \
"v_mfma_f32_16x16x32_bf16 a[84:87], a[236:239], v[36:39], a[84:87]\n" \
"ds_read_b128 a[228:231], v232 offset:10240\n" \
"v_cvt_pk_bf16_f32 v98, v100, v101\n" \
"v_smfmac_f32_16x16x64_bf16 v[224:227], v[244:247], v[32:39], v243\n" \
"buffer_load_dwordx4 v234, s[40:43], s71 offen offset:1024 sc1 lds\n" \
"v_mfma_f32_16x16x32_bf16 v[180:183], a[232:235], v[16:19], v[180:183]\n" \
"v_exp_f32 v102, v102\n" \
"v_mfma_f32_16x16x32_bf16 v[180:183], a[236:239], v[20:23], v[180:183]\n" \
"buffer_load_dwordx4 v234, s[40:43], s71 offen offset:2048 sc1 lds\n" \
"v_mfma_f32_16x16x32_bf16 v[148:151], a[232:235], v[0:3], v[148:151]\n" \
"v_exp_f32 v103, v103\n" \
"v_mfma_f32_16x16x32_bf16 v[148:151], a[236:239], v[4:7], v[148:151]\n" \
"ds_read_b128 a[232:235], v232 offset:12288\n" \
"v_cvt_pk_bf16_f32 v99, v102, v103\n" \
"v_mfma_f32_16x16x32_bf16 v[152:155], a[32:35], v[0:3], v[152:155]\n" \
"v_exp_f32 v112, v112\n" \
"v_mfma_f32_16x16x32_bf16 v[152:155], a[36:39], v[4:7], v[152:155]\n" \
"buffer_load_dwordx4 v234, s[40:43], s71 offen offset:3072 sc1 lds\n" \
"v_mfma_f32_16x16x32_bf16 v[184:187], a[32:35], v[16:19], v[184:187]\n" \
"v_exp_f32 v113, v113\n" \
"v_mfma_f32_16x16x32_bf16 v[184:187], a[36:39], v[20:23], v[184:187]\n" \
"ds_read_b128 a[236:239], v232 offset:14336\n" \
"v_cvt_pk_bf16_f32 v112, v112, v113\n" \
"v_mfma_f32_16x16x32_bf16 a[88:91], a[32:35], v[32:35], a[88:91]\n" \
"v_exp_f32 v114, v114\n" \
"v_mfma_f32_16x16x32_bf16 a[88:91], a[36:39], v[36:39], a[88:91]\n" \
"buffer_load_dword v239, v242, s[56:59], s95 offen sc1\n" \
"v_mfma_f32_16x16x32_bf16 a[120:123], a[32:35], v[48:51], a[120:123]\n" \
"s_mov_b32 s69, s92\n" \
"v_exp_f32 v115, v115\n" \
"v_mfma_f32_16x16x32_bf16 a[120:123], a[36:39], v[52:55], a[120:123]\n" \
"s_mov_b32 s76, s73\n" \
"ds_read_b128 a[244:247], v232 offset:10496\n" \
"v_cvt_pk_bf16_f32 v113, v114, v115\n" \
"v_mfma_f32_16x16x32_bf16 a[124:127], a[40:43], v[48:51], a[124:127]\n" \
"s_add_u32 s73, s73, 34816\n" \
"v_exp_f32 v116, v116\n" \
"v_mfma_f32_16x16x32_bf16 a[124:127], a[44:47], v[52:55], a[124:127]\n" \
"s_xor_b32 s74, s74, 0x8000\n" \
"s_xor_b32 s77, s77, 0x8000\n" \
"v_xor_b32 v233, 0x8000, v233\n" \
"v_mfma_f32_16x16x32_bf16 a[92:95], a[40:43], v[32:35], a[92:95]\n" \
"s_mov_b32 s82, 0\n" \
"v_exp_f32 v117, v117\n" \
"v_mfma_f32_16x16x32_bf16 a[92:95], a[44:47], v[36:39], a[92:95]\n" \
"ds_read_b128 a[248:251], v232 offset:12544\n" \
"v_cvt_pk_bf16_f32 v114, v116, v117\n" \
"v_smfmac_f32_16x16x64_bf16 v[228:231], v[244:247], v[48:55], v243\n" \
"v_mfma_f32_16x16x32_bf16 v[188:191], a[40:43], v[16:19], v[188:191]\n" \
"v_exp_f32 v118, v118\n" \
"v_mfma_f32_16x16x32_bf16 v[188:191], a[44:47], v[20:23], v[188:191]\n" \
"s_cmp_lg_u32 s84, 0\n" \
"v_exp_f32 v119, v119\n" \
"v_mfma_f32_16x16x32_bf16 v[156:159], a[40:43], v[0:3], v[156:159]\n" \
"s_waitcnt lgkmcnt(2)\n" \
"ds_read_b128 a[252:255], v232 offset:14592\n" \
"v_cvt_pk_bf16_f32 v115, v118, v119\n" \
"v_mfma_f32_16x16x32_bf16 v[156:159], a[44:47], v[4:7], v[156:159]\n" \
"s_cbranch_scc1 Lloop2_%=\n" \
"Lg_%=:\n" \
"Lpg6788_%=:\n" \
"s_cbranch_execz Lcb0_%=\n" \
"v_mfma_f32_16x16x32_bf16 v[32:35], a[192:195], a[160:163], v[208:211]\n" \
"s_mov_b64 s[44:45], s[20:21]\n" \
"Lpg6791_%=:\n" \
"s_cbranch_execz Lcb0_%=\n" \
"s_memrealtime s[14:15]\n" \
"v_exp_f32 v72, v72\n" \
"v_mfma_f32_16x16x32_bf16 v[32:35], a[196:199], a[164:167], v[32:35]\n" \
"s_and_b32 s45, s45, 0xffff\n" \
"ds_read_b128 a[32:35], v233 offset:6144\n" \
"ds_read_b128 a[36:39], v233 offset:14336\n" \
"v_mfma_f32_16x16x32_bf16 v[32:35], a[200:203], a[168:171], v[32:35]\n" \
"s_mov_b32 s46, 4194304\n" \
"v_exp_f32 v73, v73\n" \
"v_mfma_f32_16x16x32_bf16 v[32:35], a[204:207], a[172:175], v[32:35]\n" \
"ds_read_b128 a[40:43], v233 offset:7168\n" \
"ds_read_b128 a[44:47], v233 offset:15360\n" \
"global_load_dword v253, v254, s[22:23]\n" \
"v_mfma_f32_16x16x32_bf16 v[48:51], a[204:207], a[188:191], v[196:199]\n" \
"s_mov_b32 s47, 0x20000\n" \
"v_exp_f32 v74, v74\n" \
"v_mfma_f32_16x16x32_bf16 v[48:51], a[200:203], a[184:187], v[48:51]\n" \
"s_mov_b64 s[48:49], s[22:23]\n" \
"v_exp_f32 v75, v75\n" \
"v_mfma_f32_16x16x32_bf16 v[48:51], a[196:199], a[180:183], v[48:51]\n" \
"s_and_b32 s49, s49, 0xffff\n" \
"v_exp_f32 v76, v76\n" \
"v_mfma_f32_16x16x32_bf16 v[48:51], a[192:195], a[176:179], v[48:51]\n" \
"s_mov_b32 s50, 6291456\n" \
"ds_read_b128 a[200:203], v233 offset:1024\n" \
"ds_read_b128 a[204:207], v233 offset:9216\n" \
"v_mfma_f32_16x16x32_bf16 v[52:55], a[208:211], a[176:179], v[196:199]\n" \
"s_mov_b32 s51, 0x20000\n" \
"v_cvt_pk_bf16_f32 v68, v72, v73\n" \
"v_cvt_pk_bf16_f32 v69, v74, v75\n" \
"v_mfma_f32_16x16x32_bf16 v[52:55], a[212:215], a[180:183], v[52:55]\n" \
"s_bfe_u32 s94, %1, 0x20001\n" \
"v_exp_f32 v77, v77\n" \
"v_mfma_f32_16x16x32_bf16 v[52:55], a[216:219], a[184:187], v[52:55]\n" \
"s_lshl_b32 s94, s94, 2\n" \
"v_exp_f32 v78, v78\n" \
"v_mfma_f32_16x16x32_bf16 v[52:55], a[220:223], a[188:191], v[52:55]\n" \
"s_lshr_b32 s70, %1, 6\n" \
"ds_read_b128 a[192:195], v233 offset:0\n" \
"ds_read_b128 a[196:199], v233 offset:8192\n" \
"v_mfma_f32_16x16x32_bf16 v[36:39], a[220:223], a[172:175], v[212:215]\n" \
"s_add_u32 s94, s94, s70\n" \
"v_exp_f32 v79, v79\n" \
"v_mfma_f32_16x16x32_bf16 v[36:39], a[216:219], a[168:171], v[36:39]\n" \
"s_lshr_b32 s71, s22, 7\n" \
"v_exp_f32 v88, v88\n" \
"v_mfma_f32_16x16x32_bf16 v[36:39], a[212:215], a[164:167], v[36:39]\n" \
"s_sub_u32 s71, s63, s71\n" \
"v_cvt_pk_bf16_f32 v70, v76, v77\n" \
"v_cvt_pk_bf16_f32 v71, v78, v79\n" \
"v_mfma_f32_16x16x32_bf16 v[36:39], a[208:211], a[160:163], v[36:39]\n" \
"s_add_u32 s71, s71, 6\n" \
"ds_read_b128 a[216:219], v233 offset:3072\n" \
"ds_read_b128 a[220:223], v233 offset:11264\n" \
"v_mfma_f32_16x16x32_bf16 v[40:43], a[224:227], a[160:163], v[216:219]\n" \
"s_and_b32 s71, s71, 7\n" \
"v_exp_f32 v89, v89\n" \
"v_mfma_f32_16x16x32_bf16 v[40:43], a[228:231], a[164:167], v[40:43]\n" \
"s_and_b32 s70, %1, 1\n" \
"v_exp_f32 v90, v90\n" \
"v_mfma_f32_16x16x32_bf16 v[40:43], a[232:235], a[168:171], v[40:43]\n" \
"s_lshl_b32 s70, s70, 3\n" \
"v_exp_f32 v91, v91\n" \
"v_mfma_f32_16x16x32_bf16 v[40:43], a[236:239], a[172:175], v[40:43]\n" \
"s_add_u32 s70, s70, s71\n" \
"ds_read_b128 a[208:211], v233 offset:2048\n" \
"ds_read_b128 a[212:215], v233 offset:10240\n" \
"v_mfma_f32_16x16x32_bf16 v[56:59], a[236:239], a[188:191], v[196:199]\n" \
"s_lshl_b32 s78, s70, 7\n" \
"v_exp_f32 v92, v92\n" \
"v_mfma_f32_16x16x32_bf16 v[56:59], a[232:235], a[184:187], v[56:59]\n" \
"s_lshr_b32 s80, %1, 6\n" \
"v_cvt_pk_bf16_f32 v84, v88, v89\n" \
"v_cvt_pk_bf16_f32 v85, v90, v91\n" \
"v_mfma_f32_16x16x32_bf16 v[56:59], a[228:231], a[180:183], v[56:59]\n" \
"s_xor_b32 s80, s80, s63\n" \
"v_exp_f32 v93, v93\n" \
"v_mfma_f32_16x16x32_bf16 v[56:59], a[224:227], a[176:179], v[56:59]\n" \
"s_and_b32 s80, s80, 0\n" \
"ds_read_b128 a[232:235], v233 offset:5120\n" \
"ds_read_b128 a[236:239], v233 offset:13312\n" \
"v_mfma_f32_16x16x32_bf16 v[60:63], a[240:243], a[176:179], v[196:199]\n" \
"s_waitcnt lgkmcnt(2)\n" \
"s_lshl_b32 s93, s70, 12\n" \
"v_exp_f32 v94, v94\n" \
"v_mfma_f32_16x16x32_bf16 v[60:63], a[244:247], a[180:183], v[60:63]\n" \
"s_mul_i32 s91, s61, 6144\n" \
"v_exp_f32 v95, v95\n" \
"v_mfma_f32_16x16x32_bf16 v[60:63], a[248:251], a[184:187], v[60:63]\n" \
"s_add_u32 s91, s91, %3\n" \
"v_cvt_pk_bf16_f32 v86, v92, v93\n" \
"v_mfma_f32_16x16x32_bf16 v[60:63], a[252:255], a[188:191], v[60:63]\n" \
"s_lshl_b32 s71, s80, 7\n" \
"ds_read_b128 a[224:227], v233 offset:4096\n" \
"ds_read_b128 a[228:231], v233 offset:12288\n" \
"v_mfma_f32_16x16x32_bf16 v[44:47], a[252:255], a[172:175], v[220:223]\n" \
"s_lshl_b32 s75, s94, 18\n" \
"v_cvt_pk_bf16_f32 v87, v94, v95\n" \
"v_mfma_f32_16x16x32_bf16 v[44:47], a[248:251], a[168:171], v[44:47]\n" \
"s_lshl_b32 s70, s61, 16\n" \
"v_exp_f32 v104, v104\n" \
"v_mfma_f32_16x16x32_bf16 v[44:47], a[244:247], a[164:167], v[44:47]\n" \
"s_add_u32 s73, s75, s70\n" \
"v_accvgpr_read_b32 v197, a56\n" \
"v_accvgpr_read_b32 v192, a57\n" \
"v_mfma_f32_16x16x32_bf16 v[44:47], a[240:243], a[160:163], v[44:47]\n" \
"s_add_u32 s73, s73, s71\n" \
"v_accvgpr_read_b32 v196, a58\n" \
"v_add_u32 v198, 1024, v197\n" \
"v_mfma_f32_16x16x32_bf16 v[128:131], a[192:195], v[64:67], v[128:131]\n" \
"s_max_u32 s75, s75, 32768\n" \
"v_add_u32 v199, 2048, v197\n" \
"v_add_u32 v193, 31744, v192\n" \
"v_mfma_f32_16x16x32_bf16 v[128:131], a[196:199], v[68:71], v[128:131]\n" \
"s_sub_u32 s75, s75, 32768\n" \
"ds_read_b128 a[0:3], v232 offset:16384\n" \
"ds_read_b128 a[4:7], v232 offset:18432\n" \
"v_mfma_f32_16x16x32_bf16 v[160:163], a[192:195], v[80:83], v[160:163]\n" \
"s_add_u32 s75, s75, s71\n" \
"s_lshl_b32 s89, s61, 10\n" \
"v_mfma_f32_16x16x32_bf16 v[160:163], a[196:199], v[84:87], v[160:163]\n" \
"s_mul_i32 s76, s80, 393216\n" \
"s_add_u32 s89, s89, %3\n" \
"v_exp_f32 v105, v105\n" \
"v_mfma_f32_16x16x32_bf16 v[164:167], a[200:203], v[80:83], v[164:167]\n" \
"s_add_u32 s76, s76, s78\n" \
"ds_read_b128 a[8:11], v232 offset:20480\n" \
"ds_read_b128 a[12:15], v232 offset:22528\n" \
"v_mfma_f32_16x16x32_bf16 v[164:167], a[204:207], v[84:87], v[164:167]\n" \
"s_add_u32 m0, s91, 139264\n" \
"s_add_u32 s89, s89, 16384\n" \
"v_exp_f32 v106, v106\n" \
"v_mfma_f32_16x16x32_bf16 v[132:135], a[200:203], v[64:67], v[132:135]\n" \
"buffer_load_dwordx4 v197, s[48:51], s76 offen offset:0 lds\n" \
"v_mfma_f32_16x16x32_bf16 v[132:135], a[204:207], v[68:71], v[132:135]\n" \
"s_and_b32 s70, s61, 1\n" \
"ds_read_b128 a[16:19], v232 offset:16640\n" \
"ds_read_b128 a[20:23], v232 offset:18688\n" \
"v_smfmac_f32_16x16x64_bf16 a[48:51], v[244:247], v[64:71], v243\n" \
"s_add_u32 s77, s76, 24576\n" \
"v_cvt_pk_bf16_f32 v100, v104, v105\n" \
"v_mfma_f32_16x16x32_bf16 v[136:139], a[208:211], v[64:67], v[136:139]\n" \
"buffer_load_dwordx4 v198, s[48:51], s76 offen offset:1024 lds\n" \
"v_mfma_f32_16x16x32_bf16 v[136:139], a[212:215], v[68:71], v[136:139]\n" \
"s_lshl_b32 s70, s70, 11\n" \
"ds_read_b128 a[24:27], v232 offset:20736\n" \
"ds_read_b128 a[28:31], v232 offset:22784\n" \
"v_mfma_f32_16x16x32_bf16 v[168:171], a[208:211], v[80:83], v[168:171]\n" \
"s_xor_b32 s80, s80, 0x3\n" \
"v_exp_f32 v107, v107\n" \
"v_mfma_f32_16x16x32_bf16 v[168:171], a[212:215], v[84:87], v[168:171]\n" \
"buffer_load_dwordx4 v199, s[48:51], s76 offen offset:2048 lds\n" \
"v_mfma_f32_16x16x32_bf16 v[172:175], a[216:219], v[80:83], v[172:175]\n" \
"s_add_u32 s93, s93, s70\n" \
"ds_read_b128 v[0:3], v232 offset:24576\n" \
"ds_read_b128 v[4:7], v232 offset:26624\n" \
"v_mfma_f32_16x16x32_bf16 v[172:175], a[220:223], v[84:87], v[172:175]\n" \
"s_add_u32 m0, m0, 3072\n" \
"v_exp_f32 v108, v108\n" \
"v_mfma_f32_16x16x32_bf16 v[140:143], a[216:219], v[64:67], v[140:143]\n" \
"buffer_load_dwordx4 v197, s[48:51], s77 offen offset:0 lds\n" \
"v_mfma_f32_16x16x32_bf16 v[140:143], a[220:223], v[68:71], v[140:143]\n" \
"s_add_u32 s93, s93, 52740096\n" \
"ds_read_b128 v[8:11], v232 offset:28672\n" \
"ds_read_b128 v[12:15], v232 offset:30720\n" \
"v_exp_f32 v109, v109\n" \
"v_smfmac_f32_16x16x64_bf16 a[52:55], v[244:247], v[80:87], v243\n" \
"s_waitcnt lgkmcnt(4)\n" \
"s_cmp_eq_u32 s94, 0\n" \
"s_cselect_b32 s92, -1, 0\n" \
"v_cvt_pk_bf16_f32 v101, v106, v107\n" \
"v_mfma_f32_16x16x32_bf16 v[144:147], a[224:227], v[64:67], v[144:147]\n" \
"buffer_load_dwordx4 v198, s[48:51], s77 offen offset:1024 lds\n" \
"v_mfma_f32_16x16x32_bf16 v[144:147], a[228:231], v[68:71], v[144:147]\n" \
"s_mul_i32 s70, s94, 278528\n" \
"ds_read_b128 v[200:203], v232 offset:24832\n" \
"ds_read_b128 v[204:207], v232 offset:26880\n" \
"v_mfma_f32_16x16x32_bf16 v[176:179], a[224:227], v[80:83], v[176:179]\n" \
"s_add_u32 s93, s93, s70\n" \
"v_exp_f32 v110, v110\n" \
"v_mfma_f32_16x16x32_bf16 v[176:179], a[228:231], v[84:87], v[176:179]\n" \
"buffer_load_dwordx4 v199, s[48:51], s77 offen offset:2048 lds\n" \
"v_mfma_f32_16x16x32_bf16 v[180:183], a[232:235], v[80:83], v[180:183]\n" \
"s_lshr_b32 s70, s61, 1\n" \
"ds_read_b128 a[240:243], v232 offset:28928\n" \
"ds_read_b128 a[244:247], v232 offset:30976\n" \
"v_mfma_f32_16x16x32_bf16 v[180:183], a[236:239], v[84:87], v[180:183]\n" \
"s_lshl_b32 s70, s70, 8\n" \
"v_exp_f32 v111, v111\n" \
"v_mfma_f32_16x16x32_bf16 v[148:151], a[232:235], v[64:67], v[148:151]\n" \
"s_add_u32 s93, s93, s70\n" \
"v_exp_f32 v120, v120\n" \
"v_mfma_f32_16x16x32_bf16 v[148:151], a[236:239], v[68:71], v[148:151]\n" \
"v_cvt_pk_bf16_f32 v102, v108, v109\n" \
"v_cvt_pk_bf16_f32 v103, v110, v111\n" \
"v_exp_f32 v121, v121\n" \
"v_mfma_f32_16x16x32_bf16 v[152:155], a[32:35], v[64:67], v[152:155]\n" \
"v_add_u32 v233, 0x10000, v235\n" \
"s_lshl_b32 s72, s61, 14\n" \
"v_mfma_f32_16x16x32_bf16 v[152:155], a[36:39], v[68:71], v[152:155]\n" \
"v_add_u32 v232, s72, v235\n" \
"v_mfma_f32_16x16x32_bf16 v[184:187], a[32:35], v[80:83], v[184:187]\n" \
"v_exp_f32 v122, v122\n" \
"v_mfma_f32_16x16x32_bf16 v[184:187], a[36:39], v[84:87], v[184:187]\n" \
"v_exp_f32 v123, v123\n" \
"v_mfma_f32_16x16x32_bf16 v[188:191], a[40:43], v[80:83], v[188:191]\n" \
"v_exp_f32 v124, v124\n" \
"v_mfma_f32_16x16x32_bf16 v[188:191], a[44:47], v[84:87], v[188:191]\n" \
"v_cvt_pk_bf16_f32 v116, v120, v121\n" \
"v_cvt_pk_bf16_f32 v117, v122, v123\n" \
"v_mfma_f32_16x16x32_bf16 v[156:159], a[40:43], v[64:67], v[156:159]\n" \
"v_exp_f32 v125, v125\n" \
"v_mfma_f32_16x16x32_bf16 v[156:159], a[44:47], v[68:71], v[156:159]\n" \
"s_cmp_eq_u32 s63, 7\n" \
"s_waitcnt vmcnt(7) lgkmcnt(0)\n" \
"s_barrier\n" \
"s_cbranch_scc1 Lep_%=\n" \
"Lepg_%=:\n" \
"v_mfma_f32_16x16x32_bf16 v[16:19], a[0:3], a[176:179], v[208:211]\n" \
"s_add_u32 m0, s79, 110592\n" \
"ds_read_b128 v[64:67], v233 offset:16384\n" \
"ds_read_b128 v[68:71], v233 offset:24576\n" \
"v_mfma_f32_16x16x32_bf16 v[16:19], a[4:7], a[180:183], v[16:19]\n" \
"buffer_load_dwordx4 v192, s[44:47], s73 offen offset:0 lds\n" \
"v_mfma_f32_16x16x32_bf16 v[16:19], a[8:11], a[184:187], v[16:19]\n" \
"ds_read_b128 v[72:75], v233 offset:17408\n" \
"ds_read_b128 v[76:79], v233 offset:25600\n" \
"v_mfma_f32_16x16x32_bf16 v[16:19], a[12:15], a[188:191], v[16:19]\n" \
"ds_write_b128 v232, v[128:131] offset:0\n" \
"v_exp_f32 v126, v126\n" \
"v_mfma_f32_16x16x32_bf16 v[20:23], a[28:31], a[188:191], v[212:215]\n" \
"ds_read_b128 v[80:83], v233 offset:18432\n" \
"ds_read_b128 v[84:87], v233 offset:26624\n" \
"v_mfma_f32_16x16x32_bf16 v[20:23], a[24:27], a[184:187], v[20:23]\n" \
"buffer_load_dwordx4 v193, s[44:47], s73 offen offset:1024 lds\n" \
"v_mfma_f32_16x16x32_bf16 v[20:23], a[20:23], a[180:183], v[20:23]\n" \
"ds_write_b128 v232, v[132:135] offset:1024\n" \
"v_xor_b32 v248, 0x4000, v232\n" \
"v_mfma_f32_16x16x32_bf16 v[20:23], a[16:19], a[176:179], v[20:23]\n" \
"ds_read_b128 v[88:91], v233 offset:19456\n" \
"ds_read_b128 v[92:95], v233 offset:27648\n" \
"v_mfma_f32_16x16x32_bf16 v[24:27], v[0:3], a[176:179], v[216:219]\n" \
"ds_read_b128 a[248:251], v233 offset:7168\n" \
"ds_read_b128 a[252:255], v233 offset:15360\n" \
"v_mfma_f32_16x16x32_bf16 v[24:27], v[4:7], a[180:183], v[24:27]\n" \
"buffer_load_dwordx4 v192, s[44:47], s73 offen offset:2048 lds\n" \
"v_mfma_f32_16x16x32_bf16 v[24:27], v[8:11], a[184:187], v[24:27]\n" \
"ds_write_b128 v232, v[136:139] offset:2048\n" \
"v_mfma_f32_16x16x32_bf16 v[24:27], v[12:15], a[188:191], v[24:27]\n" \
"v_exp_f32 v127, v127\n" \
"v_cvt_pk_bf16_f32 v118, v124, v125\n" \
"v_mfma_f32_16x16x32_bf16 v[28:31], a[244:247], a[188:191], v[220:223]\n" \
"v_cvt_pk_bf16_f32 v119, v126, v127\n" \
"v_lshrrev_b32 v249, 1, v234\n" \
"v_mfma_f32_16x16x32_bf16 v[28:31], a[240:243], a[184:187], v[28:31]\n" \
"buffer_load_dwordx4 v193, s[44:47], s73 offen offset:3072 lds\n" \
"v_mfma_f32_16x16x32_bf16 v[28:31], v[204:207], a[180:183], v[28:31]\n" \
"ds_write_b128 v232, v[140:143] offset:3072\n" \
"s_lshl_b32 s72, s61, 9\n" \
"s_add_u32 s72, s72, 0x20000\n" \
"v_exp_f32 v32, v32\n" \
"v_exp_f32 v33, v33\n" \
"v_mfma_f32_16x16x32_bf16 v[28:31], v[200:203], a[176:179], v[28:31]\n" \
"s_add_u32 m0, s89, 110592\n" \
"v_exp_f32 v34, v34\n" \
"v_exp_f32 v35, v35\n" \
"v_mfma_f32_16x16x32_bf16 a[64:67], a[192:195], v[96:99], a[64:67]\n" \
"s_xor_b32 s73, s73, 0x180\n" \
"v_exp_f32 v36, v36\n" \
"v_mfma_f32_16x16x32_bf16 a[64:67], a[196:199], v[100:103], a[64:67]\n" \
"buffer_load_dwordx4 v196, s[44:47], s75 offen lds\n" \
"s_add_u32 s72, s72, %3\n" \
"v_cvt_pk_bf16_f32 v32, v32, v33\n" \
"v_exp_f32 v37, v37\n" \
"v_add_u32 v249, s72, v249\n" \
"v_mfma_f32_16x16x32_bf16 a[96:99], a[192:195], v[112:115], a[96:99]\n" \
"ds_write_b128 v232, v[144:147] offset:4096\n" \
"v_mfma_f32_16x16x32_bf16 a[96:99], a[196:199], v[116:119], a[96:99]\n" \
"ds_read_b128 a[192:195], v233 offset:0\n" \
"ds_read_b128 a[196:199], v233 offset:8192\n" \
"v_exp_f32 v38, v38\n" \
"v_cvt_pk_bf16_f32 v33, v34, v35\n" \
"v_accvgpr_read_b32 v250, a48\n" \
"v_mfma_f32_16x16x32_bf16 a[100:103], a[200:203], v[112:115], a[100:103]\n" \
"s_xor_b32 s75, s75, 0x180\n" \
"v_exp_f32 v39, v39\n" \
"v_mfma_f32_16x16x32_bf16 a[100:103], a[204:207], v[116:119], a[100:103]\n" \
"v_exp_f32 v48, v48\n" \
"v_cvt_pk_bf16_f32 v34, v36, v37\n" \
"v_exp_f32 v49, v49\n" \
"v_mfma_f32_16x16x32_bf16 a[68:71], a[200:203], v[96:99], a[68:71]\n" \
"ds_write_b128 v232, v[148:151] offset:5120\n" \
"v_mfma_f32_16x16x32_bf16 a[68:71], a[204:207], v[100:103], a[68:71]\n" \
"ds_read_b128 a[200:203], v233 offset:1024\n" \
"ds_read_b128 a[204:207], v233 offset:9216\n" \
"v_exp_f32 v50, v50\n" \
"v_cvt_pk_bf16_f32 v35, v38, v39\n" \
"v_smfmac_f32_16x16x64_bf16 v[224:227], v[244:247], v[96:103], v243\n" \
"v_cvt_pk_bf16_f32 v48, v48, v49\n" \
"v_mfma_f32_16x16x32_bf16 a[72:75], a[208:211], v[96:99], a[72:75]\n" \
"v_exp_f32 v51, v51\n" \
"v_mfma_f32_16x16x32_bf16 a[72:75], a[212:215], v[100:103], a[72:75]\n" \
"ds_write_b128 v232, v[152:155] offset:6144\n" \
"v_exp_f32 v52, v52\n" \
"v_exp_f32 v53, v53\n" \
"v_accvgpr_read_b32 v240, a49\n" \
"v_mfma_f32_16x16x32_bf16 a[104:107], a[208:211], v[112:115], a[104:107]\n" \
"v_exp_f32 v54, v54\n" \
"v_mfma_f32_16x16x32_bf16 a[104:107], a[212:215], v[116:119], a[104:107]\n" \
"ds_read_b128 a[208:211], v233 offset:2048\n" \
"ds_read_b128 a[212:215], v233 offset:10240\n" \
"v_cvt_pk_bf16_f32 v49, v50, v51\n" \
"v_exp_f32 v55, v55\n" \
"v_mfma_f32_16x16x32_bf16 a[108:111], a[216:219], v[112:115], a[108:111]\n" \
"v_exp_f32 v40, v40\n" \
"v_mfma_f32_16x16x32_bf16 a[108:111], a[220:223], v[116:119], a[108:111]\n" \
"ds_write_b128 v232, v[156:159] offset:7168\n" \
"v_cvt_pk_bf16_f32 v50, v52, v53\n" \
"v_exp_f32 v41, v41\n" \
"v_mfma_f32_16x16x32_bf16 a[76:79], a[216:219], v[96:99], a[76:79]\n" \
"v_exp_f32 v42, v42\n" \
"v_mfma_f32_16x16x32_bf16 a[76:79], a[220:223], v[100:103], a[76:79]\n" \
"ds_read_b128 a[216:219], v233 offset:3072\n" \
"ds_read_b128 a[220:223], v233 offset:11264\n" \
"v_cvt_pk_bf16_f32 v51, v54, v55\n" \
"v_exp_f32 v43, v43\n" \
"v_smfmac_f32_16x16x64_bf16 v[228:231], v[244:247], v[112:119], v243\n" \
"ds_write_b128 v232, v[160:163] offset:8192\n" \
"v_exp_f32 v44, v44\n" \
"v_add_f32 v250, v250, v240\n" \
"v_mfma_f32_16x16x32_bf16 a[80:83], a[224:227], v[96:99], a[80:83]\n" \
"v_cvt_pk_bf16_f32 v36, v40, v41\n" \
"v_cvt_pk_bf16_f32 v37, v42, v43\n" \
"v_mfma_f32_16x16x32_bf16 a[80:83], a[228:231], v[100:103], a[80:83]\n" \
"v_exp_f32 v45, v45\n" \
"v_exp_f32 v46, v46\n" \
"v_mfma_f32_16x16x32_bf16 a[112:115], a[224:227], v[112:115], a[112:115]\n" \
"v_exp_f32 v47, v47\n" \
"v_mfma_f32_16x16x32_bf16 a[112:115], a[228:231], v[116:119], a[112:115]\n" \
"ds_write_b128 v232, v[164:167] offset:9216\n" \
"v_exp_f32 v56, v56\n" \
"v_cvt_pk_bf16_f32 v38, v44, v45\n" \
"v_mfma_f32_16x16x32_bf16 a[116:119], a[232:235], v[112:115], a[116:119]\n" \
"ds_read_b128 a[224:227], v233 offset:4096\n" \
"ds_read_b128 a[228:231], v233 offset:12288\n" \
"v_mfma_f32_16x16x32_bf16 a[116:119], a[236:239], v[116:119], a[116:119]\n" \
"v_exp_f32 v57, v57\n" \
"v_exp_f32 v58, v58\n" \
"v_cvt_pk_bf16_f32 v39, v46, v47\n" \
"v_accvgpr_read_b32 v251, a52\n" \
"v_mfma_f32_16x16x32_bf16 a[84:87], a[232:235], v[96:99], a[84:87]\n" \
"v_exp_f32 v59, v59\n" \
"v_mfma_f32_16x16x32_bf16 a[84:87], a[236:239], v[100:103], a[84:87]\n" \
"ds_write_b128 v232, v[168:171] offset:10240\n" \
"v_exp_f32 v60, v60\n" \
"v_cvt_pk_bf16_f32 v52, v56, v57\n" \
"v_mfma_f32_16x16x32_bf16 a[88:91], a[32:35], v[96:99], a[88:91]\n" \
"ds_read_b128 a[232:235], v233 offset:5120\n" \
"ds_read_b128 a[236:239], v233 offset:13312\n" \
"v_mfma_f32_16x16x32_bf16 a[88:91], a[36:39], v[100:103], a[88:91]\n" \
"ds_read_b128 a[240:243], v233 offset:6144\n" \
"ds_read_b128 a[244:247], v233 offset:14336\n" \
"v_exp_f32 v61, v61\n" \
"v_exp_f32 v62, v62\n" \
"v_mfma_f32_16x16x32_bf16 a[120:123], a[32:35], v[112:115], a[120:123]\n" \
"ds_read_b128 v[0:3], v233 offset:20480\n" \
"ds_read_b128 v[4:7], v233 offset:28672\n" \
"v_mfma_f32_16x16x32_bf16 a[120:123], a[36:39], v[116:119], a[120:123]\n" \
"ds_write_b128 v232, v[172:175] offset:11264\n" \
"v_cvt_pk_bf16_f32 v53, v58, v59\n" \
"v_exp_f32 v63, v63\n" \
"v_mfma_f32_16x16x32_bf16 a[124:127], a[40:43], v[112:115], a[124:127]\n" \
"ds_read_b128 v[8:11], v233 offset:21504\n" \
"ds_read_b128 v[12:15], v233 offset:29696\n" \
"v_mfma_f32_16x16x32_bf16 a[124:127], a[44:47], v[116:119], a[124:127]\n" \
"v_cvt_pk_bf16_f32 v54, v60, v61\n" \
"v_accvgpr_read_b32 v240, a53\n" \
"v_add_f32 v251, v251, v240\n" \
"v_mfma_f32_16x16x32_bf16 a[92:95], a[40:43], v[96:99], a[92:95]\n" \
"s_waitcnt lgkmcnt(5)\n" \
"v_cvt_pk_bf16_f32 v55, v62, v63\n" \
"v_mfma_f32_16x16x32_bf16 a[92:95], a[44:47], v[100:103], a[92:95]\n" \
"ds_write_b128 v232, v[176:179] offset:12288\n" \
"ds_write_b64 v249, v[250:251]\n" \
"v_mfma_f32_16x16x32_bf16 a[64:67], a[192:195], v[32:35], a[64:67]\n" \
"ds_read_b128 v[96:99], v233 offset:22528\n" \
"ds_read_b128 v[100:103], v233 offset:30720\n" \
"v_mfma_f32_16x16x32_bf16 a[64:67], a[196:199], v[36:39], a[64:67]\n" \
"ds_read_b128 v[104:107], v233 offset:23552\n" \
"ds_read_b128 v[108:111], v233 offset:31744\n" \
"v_exp_f32 v16, v16\n" \
"v_mfma_f32_16x16x32_bf16 a[96:99], a[192:195], v[48:51], a[96:99]\n" \
"v_exp_f32 v17, v17\n" \
"v_mfma_f32_16x16x32_bf16 a[96:99], a[196:199], v[52:55], a[96:99]\n" \
"ds_write_b128 v232, v[180:183] offset:13312\n" \
"v_exp_f32 v18, v18\n" \
"v_xor_b32 v249, 0x200, v249\n" \
"v_accvgpr_read_b32 v200, a59\n" \
"v_mfma_f32_16x16x32_bf16 a[100:103], a[200:203], v[48:51], a[100:103]\n" \
"v_exp_f32 v19, v19\n" \
"v_mfma_f32_16x16x32_bf16 a[100:103], a[204:207], v[52:55], a[100:103]\n" \
"v_exp_f32 v20, v20\n" \
"v_cvt_pk_bf16_f32 v16, v16, v17\n" \
"v_accvgpr_read_b32 v202, a60\n" \
"v_accvgpr_read_b32 v204, a61\n" \
"v_mfma_f32_16x16x32_bf16 a[68:71], a[200:203], v[32:35], a[68:71]\n" \
"v_exp_f32 v21, v21\n" \
"v_mfma_f32_16x16x32_bf16 a[68:71], a[204:207], v[36:39], a[68:71]\n" \
"ds_write_b128 v232, v[184:187] offset:14336\n" \
"v_exp_f32 v22, v22\n" \
"v_xor_b32 v201, 64, v200\n" \
"v_xor_b32 v203, 64, v202\n" \
"v_xor_b32 v222, 32, v204\n" \
"v_smfmac_f32_16x16x64_bf16 v[224:227], v[244:247], v[32:39], v243\n" \
"v_cvt_pk_bf16_f32 v17, v18, v19\n" \
"v_add_u32 v205, 0x10000, v200\n" \
"v_mfma_f32_16x16x32_bf16 a[72:75], a[208:211], v[32:35], a[72:75]\n" \
"v_exp_f32 v23, v23\n" \
"v_mfma_f32_16x16x32_bf16 a[72:75], a[212:215], v[36:39], a[72:75]\n" \
"v_exp_f32 v24, v24\n" \
"v_cvt_pk_bf16_f32 v18, v20, v21\n" \
"v_add_u32 v207, 0x10000, v202\n" \
"v_add_u32 v194, 139264, v204\n" \
"v_add_u32 v206, 0x10000, v201\n" \
"v_mfma_f32_16x16x32_bf16 a[104:107], a[208:211], v[48:51], a[104:107]\n" \
"ds_write_b128 v232, v[188:191] offset:15360\n" \
"v_exp_f32 v25, v25\n" \
"v_add_u32 v208, 0x10000, v203\n" \
"v_mfma_f32_16x16x32_bf16 a[104:107], a[212:215], v[52:55], a[104:107]\n" \
"v_exp_f32 v26, v26\n" \
"v_cvt_pk_bf16_f32 v19, v22, v23\n" \
"v_add_u32 v195, 139264, v222\n" \
"v_mfma_f32_16x16x32_bf16 a[108:111], a[216:219], v[48:51], a[108:111]\n" \
"s_mul_i32 s76, s80, 393216\n" \
"s_add_u32 s76, s76, s78\n" \
"v_cvt_pk_bf16_f32 v20, v24, v25\n" \
"v_mfma_f32_16x16x32_bf16 a[108:111], a[220:223], v[52:55], a[108:111]\n" \
"s_waitcnt vmcnt(0) lgkmcnt(0)\n" \
"s_barrier\n" \
"v_mfma_f32_16x16x32_bf16 a[76:79], a[216:219], v[32:35], a[76:79]\n" \
"s_add_u32 m0, s91, 86016\n" \
"ds_read_b64_tr_b16 a[32:33], v194 offset:1024\n" \
"ds_read_b64_tr_b16 a[34:35], v194 offset:4096\n" \
"v_mfma_f32_16x16x32_bf16 a[76:79], a[220:223], v[36:39], a[76:79]\n" \
"buffer_load_dwordx4 v197, s[48:51], s76 offen offset:0 lds\n" \
"v_exp_f32 v27, v27\n" \
"v_smfmac_f32_16x16x64_bf16 v[228:231], v[244:247], v[48:55], v243\n" \
"s_and_b32 s15, %1, 7\n" \
"ds_read_b64_tr_b16 a[36:37], v194 offset:13312\n" \
"ds_read_b64_tr_b16 a[38:39], v194 offset:16384\n" \
"v_exp_f32 v28, v28\n" \
"v_mfma_f32_16x16x32_bf16 a[80:83], a[224:227], v[32:35], a[80:83]\n" \
"s_add_u32 s77, s76, 24576\n" \
"ds_read_b64_tr_b16 a[40:41], v194 offset:0\n" \
"ds_read_b64_tr_b16 a[42:43], v194 offset:3072\n" \
"v_mfma_f32_16x16x32_bf16 a[80:83], a[228:231], v[36:39], a[80:83]\n" \
"buffer_load_dwordx4 v198, s[48:51], s76 offen offset:1024 lds\n" \
"v_mfma_f32_16x16x32_bf16 a[112:115], a[224:227], v[48:51], a[112:115]\n" \
"s_lshl3_add_u32 s14, s14, s15\n" \
"ds_read_b64_tr_b16 a[44:45], v194 offset:12288\n" \
"ds_read_b64_tr_b16 a[46:47], v194 offset:15360\n" \
"v_mfma_f32_16x16x32_bf16 a[112:115], a[228:231], v[52:55], a[112:115]\n" \
"s_xor_b32 s80, s80, 0x8\n" \
"ds_read_b64_tr_b16 a[48:49], v195 offset:0\n" \
"ds_read_b64_tr_b16 a[50:51], v195 offset:3072\n" \
"v_exp_f32 v29, v29\n" \
"v_mfma_f32_16x16x32_bf16 a[116:119], a[232:235], v[48:51], a[116:119]\n" \
"buffer_load_dwordx4 v199, s[48:51], s76 offen offset:2048 lds\n" \
"v_mfma_f32_16x16x32_bf16 a[116:119], a[236:239], v[52:55], a[116:119]\n" \
"ds_read_b64_tr_b16 a[52:53], v195 offset:12288\n" \
"ds_read_b64_tr_b16 a[54:55], v195 offset:15360\n" \
"v_exp_f32 v30, v30\n" \
"v_mfma_f32_16x16x32_bf16 a[84:87], a[232:235], v[32:35], a[84:87]\n" \
"s_add_u32 m0, m0, 3072\n" \
"ds_read_b128 a[0:3], v207 offset:45056\n" \
"ds_read_b128 a[4:7], v208 offset:45056\n" \
"v_mfma_f32_16x16x32_bf16 a[84:87], a[236:239], v[36:39], a[84:87]\n" \
"buffer_load_dwordx4 v197, s[48:51], s77 offen offset:0 lds\n" \
"v_cvt_pk_bf16_f32 v21, v26, v27\n" \
"v_mfma_f32_16x16x32_bf16 a[88:91], a[240:243], v[32:35], a[88:91]\n" \
"ds_read_b128 a[8:11], v205 offset:45056\n" \
"ds_read_b128 a[12:15], v206 offset:45056\n" \
"v_mfma_f32_16x16x32_bf16 a[88:91], a[244:247], v[36:39], a[88:91]\n" \
"ds_read_b128 a[16:19], v205 offset:49152\n" \
"ds_read_b128 a[20:23], v206 offset:49152\n" \
"v_exp_f32 v31, v31\n" \
"v_mfma_f32_16x16x32_bf16 a[120:123], a[240:243], v[48:51], a[120:123]\n" \
"buffer_load_dwordx4 v198, s[48:51], s77 offen offset:1024 lds\n" \
"v_mfma_f32_16x16x32_bf16 a[120:123], a[244:247], v[52:55], a[120:123]\n" \
"ds_read_b128 a[24:27], v205 offset:53248\n" \
"ds_read_b128 a[28:31], v206 offset:53248\n" \
"v_cvt_pk_bf16_f32 v22, v28, v29\n" \
"v_mfma_f32_16x16x32_bf16 a[124:127], a[248:251], v[48:51], a[124:127]\n" \
"ds_read_b64 v[250:251], v249\n" \
"ds_read_b128 v[128:131], v248 offset:0\n" \
"v_mfma_f32_16x16x32_bf16 a[124:127], a[252:255], v[52:55], a[124:127]\n" \
"buffer_load_dwordx4 v199, s[48:51], s77 offen offset:2048 lds\n" \
"v_cvt_pk_bf16_f32 v23, v30, v31\n" \
"v_mfma_f32_16x16x32_bf16 a[92:95], a[248:251], v[32:35], a[92:95]\n" \
"ds_read_b128 v[132:135], v248 offset:1024\n" \
"ds_read_b128 v[136:139], v248 offset:2048\n" \
"v_mfma_f32_16x16x32_bf16 a[92:95], a[252:255], v[36:39], a[92:95]\n" \
"s_add_u32 m0, s79, 65536\n" \
"ds_read_b128 v[140:143], v248 offset:3072\n" \
"ds_read_b128 v[144:147], v248 offset:4096\n" \
"v_mfma_f32_16x16x32_bf16 a[96:99], v[64:67], v[16:19], a[96:99]\n" \
"buffer_load_dwordx4 v192, s[44:47], s73 offen offset:0 lds\n" \
"v_mfma_f32_16x16x32_bf16 a[96:99], v[68:71], v[20:23], a[96:99]\n" \
"ds_read_b128 v[148:151], v248 offset:5120\n" \
"ds_read_b128 v[152:155], v248 offset:6144\n" \
"v_mfma_f32_16x16x32_bf16 a[100:103], v[72:75], v[16:19], a[100:103]\n" \
"ds_read_b128 v[156:159], v248 offset:7168\n" \
"ds_read_b128 v[160:163], v248 offset:8192\n" \
"v_mfma_f32_16x16x32_bf16 a[100:103], v[76:79], v[20:23], a[100:103]\n" \
"buffer_load_dwordx4 v193, s[44:47], s73 offen offset:1024 lds\n" \
"v_smfmac_f32_16x16x64_bf16 v[228:231], v[244:247], v[16:23], v243\n" \
"ds_read_b128 v[164:167], v248 offset:9216\n" \
"ds_read_b128 v[168:171], v248 offset:10240\n" \
"v_mfma_f32_16x16x32_bf16 a[104:107], v[80:83], v[16:19], a[104:107]\n" \
"ds_read_b128 v[172:175], v248 offset:11264\n" \
"ds_read_b128 v[176:179], v248 offset:12288\n" \
"v_mfma_f32_16x16x32_bf16 a[104:107], v[84:87], v[20:23], a[104:107]\n" \
"buffer_load_dwordx4 v192, s[44:47], s73 offen offset:2048 lds\n" \
"v_mfma_f32_16x16x32_bf16 a[108:111], v[88:91], v[16:19], a[108:111]\n" \
"ds_read_b128 v[180:183], v248 offset:13312\n" \
"ds_read_b128 v[184:187], v248 offset:14336\n" \
"v_mfma_f32_16x16x32_bf16 a[108:111], v[92:95], v[20:23], a[108:111]\n" \
"ds_read_b128 v[188:191], v248 offset:15360\n" \
"v_mov_b32 v218, s60\n" \
"v_mfma_f32_16x16x32_bf16 a[112:115], v[0:3], v[16:19], a[112:115]\n" \
"buffer_load_dwordx4 v193, s[44:47], s73 offen offset:3072 lds\n" \
"v_mfma_f32_16x16x32_bf16 a[112:115], v[4:7], v[20:23], a[112:115]\n" \
"s_add_u32 m0, s89, 65536\n" \
"v_mov_b32 v219, s14\n" \
"v_lshlrev_b32 v220, 28, s61\n" \
"v_mfma_f32_16x16x32_bf16 a[116:119], v[8:11], v[16:19], a[116:119]\n" \
"s_xor_b32 s73, s73, 0x400\n" \
"v_mfma_f32_16x16x32_bf16 a[116:119], v[12:15], v[20:23], a[116:119]\n" \
"buffer_load_dwordx4 v196, s[44:47], s75 offen lds\n" \
"v_mfma_f32_16x16x32_bf16 a[120:123], v[96:99], v[16:19], a[120:123]\n" \
"s_lshl3_add_u32 s15, s15, 65593856\n" \
"v_mfma_f32_16x16x32_bf16 a[120:123], v[100:103], v[20:23], a[120:123]\n" \
"s_lshr_b32 s14, %1, 3\n" \
"s_cmp_eq_u32 s14, 3\n" \
"s_cselect_b32 s15, s15, 0x20000000\n" \
"buffer_store_dwordx2 v[218:219], v220, s[56:59], s15 offen sc1\n" \
"v_mfma_f32_16x16x32_bf16 a[124:127], v[104:107], v[16:19], a[124:127]\n" \
"s_xor_b32 s75, s75, 0x400\n" \
"v_mfma_f32_16x16x32_bf16 a[124:127], v[108:111], v[20:23], a[124:127]\n" \
"s_waitcnt lgkmcnt(0)\n" \
"s_barrier\n" \
"v_mfma_f32_16x16x32_bf16 v[32:35], a[32:35], a[0:3], 0\n" \
"s_add_u32 m0, s79, 0\n" \
"ds_read_b128 a[192:195], v205 offset:57344\n" \
"v_mfma_f32_16x16x32_bf16 v[32:35], a[36:39], a[4:7], v[32:35]\n" \
"buffer_load_dwordx4 v192, s[44:47], s73 offen offset:0 lds\n" \
"v_mfma_f32_16x16x32_bf16 v[40:43], a[32:35], a[8:11], 0\n" \
"ds_read_b128 a[196:199], v206 offset:57344\n" \
"v_add_u32 v209, 86016, v204\n" \
"v_mfma_f32_16x16x32_bf16 v[40:43], a[36:39], a[12:15], v[40:43]\n" \
"buffer_load_dwordx4 v193, s[44:47], s73 offen offset:1024 lds\n" \
"v_mfma_f32_16x16x32_bf16 v[0:3], a[40:43], a[8:11], 0\n" \
"ds_read_b64_tr_b16 a[200:201], v195 offset:1024\n" \
"ds_read_b64_tr_b16 a[202:203], v195 offset:4096\n" \
"v_mfma_f32_16x16x32_bf16 v[0:3], a[44:47], a[12:15], v[0:3]\n" \
"buffer_load_dwordx4 v192, s[44:47], s73 offen offset:2048 lds\n" \
"v_add_f32 v248, v224, v225\n" \
"v_mfma_f32_16x16x32_bf16 v[4:7], a[48:51], a[8:11], 0\n" \
"ds_read_b64_tr_b16 a[204:205], v195 offset:13312\n" \
"ds_read_b64_tr_b16 a[206:207], v195 offset:16384\n" \
"v_mfma_f32_16x16x32_bf16 v[4:7], a[52:55], a[12:15], v[4:7]\n" \
"buffer_load_dwordx4 v193, s[44:47], s73 offen offset:3072 lds\n" \
"v_mfma_f32_16x16x32_bf16 v[12:15], a[48:51], a[16:19], 0\n" \
"s_add_u32 m0, s89, 0\n" \
"s_xor_b32 s73, s73, 0x500\n" \
"ds_read_b64_tr_b16 a[208:209], v194 offset:2048\n" \
"ds_read_b64_tr_b16 a[210:211], v194 offset:5120\n" \
"v_add_f32 v249, v228, v229\n" \
"v_mfma_f32_16x16x32_bf16 v[12:15], a[52:55], a[20:23], v[12:15]\n" \
"buffer_load_dwordx4 v196, s[44:47], s75 offen lds\n" \
"v_mfma_f32_16x16x32_bf16 v[8:11], a[40:43], a[16:19], 0\n" \
"s_xor_b32 s75, s75, 0x500\n" \
"s_mul_i32 s76, s80, 393216\n" \
"s_add_u32 s76, s76, s78\n" \
"s_add_u32 m0, s91, 20480\n" \
"ds_read_b64_tr_b16 a[212:213], v194 offset:14336\n" \
"ds_read_b64_tr_b16 a[214:215], v194 offset:17408\n" \
"v_mfma_f32_16x16x32_bf16 v[8:11], a[44:47], a[20:23], v[8:11]\n" \
"Lpg7380_%=:\n" \
"s_cbranch_execz Lcb0_%=\n" \
"buffer_load_dwordx4 v197, s[48:51], s76 offen offset:0 lds\n" \
"v_add_f32 v248, v248, v250\n" \
"v_mfma_f32_16x16x32_bf16 v[48:51], a[32:35], a[16:19], 0\n" \
"s_add_u32 s77, s76, 24576\n" \
"ds_read_b64_tr_b16 a[216:217], v195 offset:2048\n" \
"v_add_u32 v210, 86016, v222\n" \
"v_mfma_f32_16x16x32_bf16 v[48:51], a[36:39], a[20:23], v[48:51]\n" \
"buffer_load_dwordx4 v198, s[48:51], s76 offen offset:1024 lds\n" \
"v_mfma_f32_16x16x32_bf16 v[56:59], a[32:35], a[24:27], 0\n" \
"s_xor_b32 s80, s80, 0xa\n" \
"ds_read_b64_tr_b16 a[218:219], v195 offset:5120\n" \
"v_add_f32 v249, v249, v251\n" \
"v_mfma_f32_16x16x32_bf16 v[56:59], a[36:39], a[28:31], v[56:59]\n" \
"buffer_load_dwordx4 v199, s[48:51], s76 offen offset:2048 lds\n" \
"v_mfma_f32_16x16x32_bf16 v[16:19], a[40:43], a[24:27], 0\n" \
"s_add_u32 m0, m0, 3072\n" \
"ds_read_b64_tr_b16 a[220:221], v195 offset:14336\n" \
"v_mfma_f32_16x16x32_bf16 v[16:19], a[44:47], a[28:31], v[16:19]\n" \
"buffer_load_dwordx4 v197, s[48:51], s77 offen offset:0 lds\n" \
"v_mfma_f32_16x16x32_bf16 v[20:23], a[48:51], a[24:27], 0\n" \
"s_waitcnt lgkmcnt(2)\n" \
"ds_read_b64_tr_b16 a[222:223], v195 offset:17408\n" \
"v_mfma_f32_16x16x32_bf16 v[20:23], a[52:55], a[28:31], v[20:23]\n" \
"buffer_load_dwordx4 v198, s[48:51], s77 offen offset:1024 lds\n" \
"v_rcp_f32 v232, v248\n" \
"v_mfma_f32_16x16x32_bf16 v[28:31], a[48:51], a[192:195], 0\n" \
"v_rcp_f32 v233, v249\n" \
"v_mfma_f32_16x16x32_bf16 v[28:31], a[52:55], a[196:199], v[28:31]\n" \
"buffer_load_dwordx4 v199, s[48:51], s77 offen offset:2048 lds\n" \
"v_add_u32 v240, 0x1000, v236\n" \
"v_mfma_f32_16x16x32_bf16 v[24:27], a[40:43], a[192:195], 0\n" \
"v_add_f32 v224, v248, v249\n" \
"v_mul_f32 v224, 0x43800000, v224\n" \
"v_mfma_f32_16x16x32_bf16 v[24:27], a[44:47], a[196:199], v[24:27]\n" \
"v_add_f32 v248, v232, v233\n" \
"v_fmac_f32 v224, 0x5f800000, v248\n" \
"v_mfma_f32_16x16x32_bf16 v[64:67], a[32:35], a[192:195], 0\n" \
"v_mov_b32 v225, 0\n" \
"v_mov_b32 v226, 0\n" \
"v_mfma_f32_16x16x32_bf16 v[64:67], a[36:39], a[196:199], v[64:67]\n" \
"v_mov_b32 v227, 0\n" \
"s_waitcnt vmcnt(12) lgkmcnt(0)\n" \
"s_barrier\n" \
"v_mfma_f32_16x16x32_bf16 v[68:71], a[200:203], a[192:195], 0\n" \
"ds_read_b64_tr_b16 a[32:33], v209 offset:1024\n" \
"ds_read_b64_tr_b16 a[34:35], v209 offset:4096\n" \
"v_mfma_f32_16x16x32_bf16 v[68:71], a[204:207], a[196:199], v[68:71]\n" \
"ds_read_b64_tr_b16 a[36:37], v209 offset:13312\n" \
"ds_read_b64_tr_b16 a[38:39], v209 offset:16384\n" \
"v_mfma_f32_16x16x32_bf16 v[60:63], a[200:203], a[24:27], 0\n" \
"s_add_u32 m0, s79, 110592\n" \
"ds_read_b64_tr_b16 a[40:41], v209 offset:0\n" \
"v_accvgpr_read_b32 v112, a64\n" \
"v_mfma_f32_16x16x32_bf16 v[60:63], a[204:207], a[28:31], v[60:63]\n" \
"buffer_load_dwordx4 v192, s[44:47], s73 offen offset:0 lds\n" \
"v_accvgpr_read_b32 v113, a65\n" \
"v_mfma_f32_16x16x32_bf16 v[52:55], a[200:203], a[16:19], 0\n" \
"ds_read_b64_tr_b16 a[42:43], v209 offset:3072\n" \
"v_accvgpr_read_b32 v114, a66\n" \
"v_mfma_f32_16x16x32_bf16 v[52:55], a[204:207], a[20:23], v[52:55]\n" \
"ds_read_b64_tr_b16 a[44:45], v209 offset:12288\n" \
"v_accvgpr_read_b32 v115, a67\n" \
"v_accvgpr_read_b32 v116, a68\n" \
"v_mfma_f32_16x16x32_bf16 v[44:47], a[200:203], a[8:11], 0\n" \
"ds_read_b64_tr_b16 a[46:47], v209 offset:15360\n" \
"v_accvgpr_read_b32 v117, a69\n" \
"v_mfma_f32_16x16x32_bf16 v[44:47], a[204:207], a[12:15], v[44:47]\n" \
"buffer_load_dwordx4 v193, s[44:47], s73 offen offset:1024 lds\n" \
"v_mfma_f32_16x16x32_bf16 v[72:75], a[0:3], a[208:211], 0\n" \
"ds_read_b64_tr_b16 a[48:49], v210 offset:0\n" \
"v_accvgpr_read_b32 v118, a70\n" \
"v_mfma_f32_16x16x32_bf16 v[72:75], a[4:7], a[212:215], v[72:75]\n" \
"ds_read_b128 a[0:3], v207 offset:0\n" \
"v_accvgpr_read_b32 v119, a71\n" \
"v_add_f32 v112, v112, v128\n" \
"v_mfma_f32_16x16x32_bf16 v[80:83], a[8:11], a[208:211], 0\n" \
"ds_read_b128 a[4:7], v208 offset:0\n" \
"v_add_f32 v113, v113, v129\n" \
"v_mfma_f32_16x16x32_bf16 v[80:83], a[12:15], a[212:215], v[80:83]\n" \
"buffer_load_dwordx4 v192, s[44:47], s73 offen offset:2048 lds\n" \
"v_mfma_f32_16x16x32_bf16 v[84:87], a[8:11], a[216:219], 0\n" \
"ds_read_b64_tr_b16 a[50:51], v210 offset:3072\n" \
"v_add_f32 v114, v114, v130\n" \
"v_mfma_f32_16x16x32_bf16 v[84:87], a[12:15], a[220:223], v[84:87]\n" \
"ds_read_b128 a[8:11], v205 offset:0\n" \
"v_add_f32 v115, v115, v131\n" \
"v_add_f32 v116, v116, v132\n" \
"v_mfma_f32_16x16x32_bf16 v[92:95], a[16:19], a[216:219], 0\n" \
"ds_read_b128 a[12:15], v206 offset:0\n" \
"v_add_f32 v117, v117, v133\n" \
"v_mfma_f32_16x16x32_bf16 v[92:95], a[20:23], a[220:223], v[92:95]\n" \
"buffer_load_dwordx4 v193, s[44:47], s73 offen offset:3072 lds\n" \
"v_add_f32 v118, v118, v134\n" \
"v_mfma_f32_16x16x32_bf16 v[88:91], a[16:19], a[208:211], 0\n" \
"ds_read_b64_tr_b16 a[52:53], v210 offset:12288\n" \
"v_add_f32 v119, v119, v135\n" \
"v_mfma_f32_16x16x32_bf16 v[88:91], a[20:23], a[212:215], v[88:91]\n" \
"s_add_u32 m0, s89, 110592\n" \
"ds_read_b128 a[16:19], v205 offset:4096\n" \
"v_mul_f32 v112, v112, v232\n" \
"v_mfma_f32_16x16x32_bf16 v[96:99], a[24:27], a[208:211], 0\n" \
"s_xor_b32 s73, s73, 0x180\n" \
"ds_read_b128 a[20:23], v206 offset:4096\n" \
"v_mul_f32 v113, v113, v232\n" \
"v_mfma_f32_16x16x32_bf16 v[96:99], a[28:31], a[212:215], v[96:99]\n" \
"buffer_load_dwordx4 v196, s[44:47], s75 offen lds\n" \
"v_mul_f32 v114, v114, v232\n" \
"v_mfma_f32_16x16x32_bf16 v[100:103], a[24:27], a[216:219], 0\n" \
"s_xor_b32 s75, s75, 0x180\n" \
"s_mul_i32 s76, s80, 393216\n" \
"ds_read_b64_tr_b16 a[54:55], v210 offset:15360\n" \
"v_mfma_f32_16x16x32_bf16 v[100:103], a[28:31], a[220:223], v[100:103]\n" \
"s_add_u32 s76, s76, s78\n" \
"ds_read_b128 a[24:27], v205 offset:8192\n" \
"v_mul_f32 v115, v115, v232\n" \
"v_mfma_f32_16x16x32_bf16 v[108:111], a[192:195], a[216:219], 0\n" \
"s_add_u32 m0, s91, 139264\n" \
"ds_read_b128 a[28:31], v206 offset:8192\n" \
"v_mul_f32 v116, v116, v232\n" \
"v_mfma_f32_16x16x32_bf16 v[108:111], a[196:199], a[220:223], v[108:111]\n" \
"buffer_load_dwordx4 v197, s[48:51], s76 offen offset:0 lds\n" \
"v_mul_f32 v117, v117, v232\n" \
"v_mfma_f32_16x16x32_bf16 v[104:107], a[192:195], a[208:211], 0\n" \
"v_mul_f32 v118, v118, v232\n" \
"v_mul_f32 v119, v119, v232\n" \
"v_mfma_f32_16x16x32_bf16 v[104:107], a[196:199], a[212:215], v[104:107]\n" \
"s_waitcnt lgkmcnt(2)\n" \
"v_cvt_pk_bf16_f32 v120, v112, v113\n" \
"v_cvt_pk_bf16_f32 v121, v114, v115\n" \
"v_cvt_pk_bf16_f32 v122, v116, v117\n" \
"v_mfma_f32_16x16x32_bf16 v[32:35], a[32:35], a[0:3], v[32:35]\n" \
"s_add_u32 s77, s76, 24576\n" \
"ds_read_b128 a[192:195], v205 offset:12288\n" \
"v_cvt_pk_bf16_f32 v123, v118, v119\n" \
"v_mfma_f32_16x16x32_bf16 v[32:35], a[36:39], a[4:7], v[32:35]\n" \
"buffer_load_dwordx4 v198, s[48:51], s76 offen offset:1024 lds\n" \
"v_mfma_f32_16x16x32_bf16 v[40:43], a[32:35], a[8:11], v[40:43]\n" \
"ds_read_b128 a[196:199], v206 offset:12288\n" \
"v_mfma_f32_16x16x32_bf16 v[40:43], a[36:39], a[12:15], v[40:43]\n" \
"ds_read_b64_tr_b16 a[200:201], v210 offset:1024\n" \
"buffer_store_dwordx4 v[120:123], v236, s[56:59], s88 offen offset:0 sc1\n" \
"v_mfma_f32_16x16x32_bf16 v[0:3], a[40:43], a[8:11], v[0:3]\n" \
"s_xor_b32 s80, s80, 0x3\n" \
"ds_read_b64_tr_b16 a[202:203], v210 offset:4096\n" \
"v_mfma_f32_16x16x32_bf16 v[0:3], a[44:47], a[12:15], v[0:3]\n" \
"buffer_load_dwordx4 v199, s[48:51], s76 offen offset:2048 lds\n" \
"v_mfma_f32_16x16x32_bf16 v[224:227], v[244:247], v[120:123], v[224:227]\n" \
"v_accvgpr_read_b32 v112, a72\n" \
"v_accvgpr_read_b32 v113, a73\n" \
"v_accvgpr_read_b32 v114, a74\n" \
"v_mfma_f32_16x16x32_bf16 v[4:7], a[48:51], a[8:11], v[4:7]\n" \
"ds_read_b64_tr_b16 a[204:205], v210 offset:13312\n" \
"v_accvgpr_read_b32 v115, a75\n" \
"v_mfma_f32_16x16x32_bf16 v[4:7], a[52:55], a[12:15], v[4:7]\n" \
"ds_read_b64_tr_b16 a[206:207], v210 offset:16384\n" \
"v_accvgpr_read_b32 v116, a76\n" \
"v_accvgpr_read_b32 v117, a77\n" \
"v_mfma_f32_16x16x32_bf16 v[12:15], a[48:51], a[16:19], v[12:15]\n" \
"s_add_u32 m0, m0, 3072\n" \
"ds_read_b64_tr_b16 a[208:209], v209 offset:2048\n" \
"v_accvgpr_read_b32 v118, a78\n" \
"v_mfma_f32_16x16x32_bf16 v[12:15], a[52:55], a[20:23], v[12:15]\n" \
"buffer_load_dwordx4 v197, s[48:51], s77 offen offset:0 lds\n" \
"v_accvgpr_read_b32 v119, a79\n" \
"v_mfma_f32_16x16x32_bf16 v[8:11], a[40:43], a[16:19], v[8:11]\n" \
"ds_read_b64_tr_b16 a[210:211], v209 offset:5120\n" \
"v_add_f32 v112, v112, v136\n" \
"v_mfma_f32_16x16x32_bf16 v[8:11], a[44:47], a[20:23], v[8:11]\n" \
"ds_read_b64_tr_b16 a[212:213], v209 offset:14336\n" \
"v_add_f32 v113, v113, v137\n" \
"v_mfma_f32_16x16x32_bf16 v[48:51], a[32:35], a[16:19], v[48:51]\n" \
"s_waitcnt lgkmcnt(3)\n" \
"ds_read_b64_tr_b16 a[214:215], v209 offset:17408\n" \
"v_add_f32 v114, v114, v138\n" \
"v_mfma_f32_16x16x32_bf16 v[48:51], a[36:39], a[20:23], v[48:51]\n" \
"buffer_load_dwordx4 v198, s[48:51], s77 offen offset:1024 lds\n" \
"v_add_f32 v115, v115, v139\n" \
"v_mfma_f32_16x16x32_bf16 v[56:59], a[32:35], a[24:27], v[56:59]\n" \
"ds_read_b64_tr_b16 a[216:217], v210 offset:2048\n" \
"v_add_f32 v116, v116, v140\n" \
"v_mfma_f32_16x16x32_bf16 v[56:59], a[36:39], a[28:31], v[56:59]\n" \
"ds_read_b64_tr_b16 a[218:219], v210 offset:5120\n" \
"v_add_f32 v117, v117, v141\n" \
"v_mfma_f32_16x16x32_bf16 v[16:19], a[40:43], a[24:27], v[16:19]\n" \
"ds_read_b64_tr_b16 a[220:221], v210 offset:14336\n" \
"v_add_f32 v118, v118, v142\n" \
"v_mfma_f32_16x16x32_bf16 v[16:19], a[44:47], a[28:31], v[16:19]\n" \
"buffer_load_dwordx4 v199, s[48:51], s77 offen offset:2048 lds\n" \
"v_add_f32 v119, v119, v143\n" \
"v_mfma_f32_16x16x32_bf16 v[20:23], a[48:51], a[24:27], v[20:23]\n" \
"ds_read_b64_tr_b16 a[222:223], v210 offset:17408\n" \
"v_mul_f32 v112, v112, v232\n" \
"v_mfma_f32_16x16x32_bf16 v[20:23], a[52:55], a[28:31], v[20:23]\n" \
"v_mul_f32 v113, v113, v232\n" \
"v_mul_f32 v114, v114, v232\n" \
"v_mul_f32 v115, v115, v232\n" \
"v_mfma_f32_16x16x32_bf16 v[28:31], a[48:51], a[192:195], v[28:31]\n" \
"v_mul_f32 v116, v116, v232\n" \
"v_mul_f32 v117, v117, v232\n" \
"v_mfma_f32_16x16x32_bf16 v[28:31], a[52:55], a[196:199], v[28:31]\n" \
"v_mul_f32 v118, v118, v232\n" \
"v_mul_f32 v119, v119, v232\n" \
"v_mfma_f32_16x16x32_bf16 v[24:27], a[40:43], a[192:195], v[24:27]\n" \
"v_cvt_pk_bf16_f32 v124, v112, v113\n" \
"v_cvt_pk_bf16_f32 v125, v114, v115\n" \
"v_mfma_f32_16x16x32_bf16 v[24:27], a[44:47], a[196:199], v[24:27]\n" \
"v_cvt_pk_bf16_f32 v126, v116, v117\n" \
"v_cvt_pk_bf16_f32 v127, v118, v119\n" \
"v_mfma_f32_16x16x32_bf16 v[64:67], a[32:35], a[192:195], v[64:67]\n" \
"buffer_store_dwordx4 v[124:127], v236, s[56:59], s88 offen offset:2048 sc1\n" \
"v_mfma_f32_16x16x32_bf16 v[64:67], a[36:39], a[196:199], v[64:67]\n" \
"v_mfma_f32_16x16x32_bf16 v[224:227], v[244:247], v[124:127], v[224:227]\n" \
"v_accvgpr_read_b32 v112, a80\n" \
"s_waitcnt vmcnt(13) lgkmcnt(0)\n" \
"s_barrier\n" \
"v_mfma_f32_16x16x32_bf16 v[68:71], a[200:203], a[192:195], v[68:71]\n" \
"ds_read_b64_tr_b16 a[32:33], v204 offset:21504\n" \
"ds_read_b64_tr_b16 a[34:35], v204 offset:24576\n" \
"v_mfma_f32_16x16x32_bf16 v[68:71], a[204:207], a[196:199], v[68:71]\n" \
"ds_read_b64_tr_b16 a[36:37], v204 offset:33792\n" \
"ds_read_b64_tr_b16 a[38:39], v204 offset:36864\n" \
"v_accvgpr_read_b32 v113, a81\n" \
"v_mfma_f32_16x16x32_bf16 v[60:63], a[200:203], a[24:27], v[60:63]\n" \
"s_add_u32 m0, s79, 65536\n" \
"ds_read_b64_tr_b16 a[40:41], v204 offset:20480\n" \
"v_accvgpr_read_b32 v114, a82\n" \
"v_mfma_f32_16x16x32_bf16 v[60:63], a[204:207], a[28:31], v[60:63]\n" \
"buffer_load_dwordx4 v192, s[44:47], s73 offen offset:0 lds\n" \
"v_accvgpr_read_b32 v115, a83\n" \
"v_mfma_f32_16x16x32_bf16 v[52:55], a[200:203], a[16:19], v[52:55]\n" \
"ds_read_b64_tr_b16 a[42:43], v204 offset:23552\n" \
"v_accvgpr_read_b32 v116, a84\n" \
"v_mfma_f32_16x16x32_bf16 v[52:55], a[204:207], a[20:23], v[52:55]\n" \
"ds_read_b64_tr_b16 a[44:45], v204 offset:32768\n" \
"v_accvgpr_read_b32 v117, a85\n" \
"v_mfma_f32_16x16x32_bf16 v[44:47], a[200:203], a[8:11], v[44:47]\n" \
"ds_read_b64_tr_b16 a[46:47], v204 offset:35840\n" \
"v_accvgpr_read_b32 v118, a86\n" \
"v_mfma_f32_16x16x32_bf16 v[44:47], a[204:207], a[12:15], v[44:47]\n" \
"buffer_load_dwordx4 v193, s[44:47], s73 offen offset:1024 lds\n" \
"v_accvgpr_read_b32 v119, a87\n" \
"v_mfma_f32_16x16x32_bf16 v[72:75], a[0:3], a[208:211], v[72:75]\n" \
"ds_read_b64_tr_b16 a[48:49], v222 offset:20480\n" \
"v_add_f32 v112, v112, v144\n" \
"v_mfma_f32_16x16x32_bf16 v[72:75], a[4:7], a[212:215], v[72:75]\n" \
"ds_read_b128 a[0:3], v202 offset:0\n" \
"v_add_f32 v113, v113, v145\n" \
"v_mfma_f32_16x16x32_bf16 v[80:83], a[8:11], a[208:211], v[80:83]\n" \
"ds_read_b128 a[4:7], v203 offset:0\n" \
"v_add_f32 v114, v114, v146\n" \
"v_mfma_f32_16x16x32_bf16 v[80:83], a[12:15], a[212:215], v[80:83]\n" \
"buffer_load_dwordx4 v192, s[44:47], s73 offen offset:2048 lds\n" \
"v_add_f32 v115, v115, v147\n" \
"v_mfma_f32_16x16x32_bf16 v[84:87], a[8:11], a[216:219], v[84:87]\n" \
"ds_read_b64_tr_b16 a[50:51], v222 offset:23552\n" \
"v_add_f32 v116, v116, v148\n" \
"v_mfma_f32_16x16x32_bf16 v[84:87], a[12:15], a[220:223], v[84:87]\n" \
"ds_read_b128 a[8:11], v200 offset:0\n" \
"v_add_f32 v117, v117, v149\n" \
"v_add_f32 v118, v118, v150\n" \
"v_mfma_f32_16x16x32_bf16 v[92:95], a[16:19], a[216:219], v[92:95]\n" \
"ds_read_b128 a[12:15], v201 offset:0\n" \
"v_add_f32 v119, v119, v151\n" \
"v_mfma_f32_16x16x32_bf16 v[92:95], a[20:23], a[220:223], v[92:95]\n" \
"buffer_load_dwordx4 v193, s[44:47], s73 offen offset:3072 lds\n" \
"v_mfma_f32_16x16x32_bf16 v[88:91], a[16:19], a[208:211], v[88:91]\n" \
"ds_read_b64_tr_b16 a[52:53], v222 offset:32768\n" \
"v_mul_f32 v112, v112, v232\n" \
"v_mfma_f32_16x16x32_bf16 v[88:91], a[20:23], a[212:215], v[88:91]\n" \
"s_add_u32 m0, s89, 65536\n" \
"ds_read_b128 a[16:19], v200 offset:4096\n" \
"v_mul_f32 v113, v113, v232\n" \
"v_mul_f32 v114, v114, v232\n" \
"v_mfma_f32_16x16x32_bf16 v[96:99], a[24:27], a[208:211], v[96:99]\n" \
"s_xor_b32 s73, s73, 0x300\n" \
"ds_read_b128 a[20:23], v201 offset:4096\n" \
"v_mul_f32 v115, v115, v232\n" \
"v_mfma_f32_16x16x32_bf16 v[96:99], a[28:31], a[212:215], v[96:99]\n" \
"buffer_load_dwordx4 v196, s[44:47], s75 offen lds\n" \
"v_mfma_f32_16x16x32_bf16 v[100:103], a[24:27], a[216:219], v[100:103]\n" \
"s_xor_b32 s75, s75, 0x300\n" \
"s_mul_i32 s76, s80, 393216\n" \
"ds_read_b64_tr_b16 a[54:55], v222 offset:35840\n" \
"v_mfma_f32_16x16x32_bf16 v[100:103], a[28:31], a[220:223], v[100:103]\n" \
"s_add_u32 s76, s76, s78\n" \
"ds_read_b128 a[24:27], v200 offset:8192\n" \
"v_mul_f32 v116, v116, v232\n" \
"v_mul_f32 v117, v117, v232\n" \
"v_mfma_f32_16x16x32_bf16 v[108:111], a[192:195], a[216:219], v[108:111]\n" \
"s_add_u32 m0, s91, 86016\n" \
"ds_read_b128 a[28:31], v201 offset:8192\n" \
"v_mul_f32 v118, v118, v232\n" \
"v_mfma_f32_16x16x32_bf16 v[108:111], a[196:199], a[220:223], v[108:111]\n" \
"buffer_load_dwordx4 v197, s[48:51], s76 offen offset:0 lds\n" \
"v_mul_f32 v119, v119, v232\n" \
"v_mfma_f32_16x16x32_bf16 v[104:107], a[192:195], a[208:211], v[104:107]\n" \
"v_cvt_pk_bf16_f32 v120, v112, v113\n" \
"v_cvt_pk_bf16_f32 v121, v114, v115\n" \
"v_mfma_f32_16x16x32_bf16 v[104:107], a[196:199], a[212:215], v[104:107]\n" \
"s_waitcnt lgkmcnt(2)\n" \
"v_cvt_pk_bf16_f32 v122, v116, v117\n" \
"v_cvt_pk_bf16_f32 v123, v118, v119\n" \
"v_mfma_f32_16x16x32_bf16 v[32:35], a[32:35], a[0:3], v[32:35]\n" \
"s_add_u32 s77, s76, 24576\n" \
"ds_read_b128 a[192:195], v200 offset:12288\n" \
"v_mfma_f32_16x16x32_bf16 v[32:35], a[36:39], a[4:7], v[32:35]\n" \
"buffer_load_dwordx4 v198, s[48:51], s76 offen offset:1024 lds\n" \
"v_mfma_f32_16x16x32_bf16 v[40:43], a[32:35], a[8:11], v[40:43]\n" \
"ds_read_b128 a[196:199], v201 offset:12288\n" \
"v_mfma_f32_16x16x32_bf16 v[40:43], a[36:39], a[12:15], v[40:43]\n" \
"ds_read_b64_tr_b16 a[200:201], v222 offset:21504\n" \
"buffer_store_dwordx4 v[120:123], v240, s[56:59], s88 offen offset:0 sc1\n" \
"v_mfma_f32_16x16x32_bf16 v[0:3], a[40:43], a[8:11], v[0:3]\n" \
"s_xor_b32 s80, s80, 0x6\n" \
"ds_read_b64_tr_b16 a[202:203], v222 offset:24576\n" \
"v_mfma_f32_16x16x32_bf16 v[224:227], v[244:247], v[120:123], v[224:227]\n" \
"v_accvgpr_read_b32 v112, a88\n" \
"v_accvgpr_read_b32 v113, a89\n" \
"v_mfma_f32_16x16x32_bf16 v[0:3], a[44:47], a[12:15], v[0:3]\n" \
"buffer_load_dwordx4 v199, s[48:51], s76 offen offset:2048 lds\n" \
"v_mfma_f32_16x16x32_bf16 v[4:7], a[48:51], a[8:11], v[4:7]\n" \
"ds_read_b64_tr_b16 a[204:205], v222 offset:33792\n" \
"v_accvgpr_read_b32 v114, a90\n" \
"v_mfma_f32_16x16x32_bf16 v[4:7], a[52:55], a[12:15], v[4:7]\n" \
"ds_read_b64_tr_b16 a[206:207], v222 offset:36864\n" \
"v_accvgpr_read_b32 v115, a91\n" \
"v_accvgpr_read_b32 v116, a92\n" \
"v_mfma_f32_16x16x32_bf16 v[12:15], a[48:51], a[16:19], v[12:15]\n" \
"s_add_u32 m0, m0, 3072\n" \
"ds_read_b64_tr_b16 a[208:209], v204 offset:22528\n" \
"v_accvgpr_read_b32 v117, a93\n" \
"v_mfma_f32_16x16x32_bf16 v[12:15], a[52:55], a[20:23], v[12:15]\n" \
"buffer_load_dwordx4 v197, s[48:51], s77 offen offset:0 lds\n" \
"v_mfma_f32_16x16x32_bf16 v[8:11], a[40:43], a[16:19], v[8:11]\n" \
"ds_read_b64_tr_b16 a[210:211], v204 offset:25600\n" \
"v_accvgpr_read_b32 v118, a94\n" \
"v_mfma_f32_16x16x32_bf16 v[8:11], a[44:47], a[20:23], v[8:11]\n" \
"ds_read_b64_tr_b16 a[212:213], v204 offset:34816\n" \
"v_accvgpr_read_b32 v119, a95\n" \
"v_add_f32 v112, v112, v152\n" \
"v_mfma_f32_16x16x32_bf16 v[48:51], a[32:35], a[16:19], v[48:51]\n" \
"s_waitcnt lgkmcnt(3)\n" \
"ds_read_b64_tr_b16 a[214:215], v204 offset:37888\n" \
"v_add_f32 v113, v113, v153\n" \
"v_mfma_f32_16x16x32_bf16 v[48:51], a[36:39], a[20:23], v[48:51]\n" \
"buffer_load_dwordx4 v198, s[48:51], s77 offen offset:1024 lds\n" \
"v_mfma_f32_16x16x32_bf16 v[56:59], a[32:35], a[24:27], v[56:59]\n" \
"ds_read_b64_tr_b16 a[216:217], v222 offset:22528\n" \
"v_add_f32 v114, v114, v154\n" \
"v_mfma_f32_16x16x32_bf16 v[56:59], a[36:39], a[28:31], v[56:59]\n" \
"ds_read_b64_tr_b16 a[218:219], v222 offset:25600\n" \
"v_add_f32 v115, v115, v155\n" \
"v_add_f32 v116, v116, v156\n" \
"v_mfma_f32_16x16x32_bf16 v[16:19], a[40:43], a[24:27], v[16:19]\n" \
"ds_read_b64_tr_b16 a[220:221], v222 offset:34816\n" \
"v_add_f32 v117, v117, v157\n" \
"v_mfma_f32_16x16x32_bf16 v[16:19], a[44:47], a[28:31], v[16:19]\n" \
"buffer_load_dwordx4 v199, s[48:51], s77 offen offset:2048 lds\n" \
"v_add_f32 v118, v118, v158\n" \
"v_mfma_f32_16x16x32_bf16 v[20:23], a[48:51], a[24:27], v[20:23]\n" \
"ds_read_b64_tr_b16 a[222:223], v222 offset:37888\n" \
"v_add_f32 v119, v119, v159\n" \
"v_mfma_f32_16x16x32_bf16 v[20:23], a[52:55], a[28:31], v[20:23]\n" \
"v_mul_f32 v112, v112, v232\n" \
"v_mul_f32 v113, v113, v232\n" \
"v_mfma_f32_16x16x32_bf16 v[28:31], a[48:51], a[192:195], v[28:31]\n" \
"v_mul_f32 v114, v114, v232\n" \
"v_mul_f32 v115, v115, v232\n" \
"v_mfma_f32_16x16x32_bf16 v[28:31], a[52:55], a[196:199], v[28:31]\n" \
"v_mul_f32 v116, v116, v232\n" \
"v_mul_f32 v117, v117, v232\n" \
"v_mul_f32 v118, v118, v232\n" \
"v_mfma_f32_16x16x32_bf16 v[24:27], a[40:43], a[192:195], v[24:27]\n" \
"v_mul_f32 v119, v119, v232\n" \
"v_cvt_pk_bf16_f32 v124, v112, v113\n" \
"v_mfma_f32_16x16x32_bf16 v[24:27], a[44:47], a[196:199], v[24:27]\n" \
"v_cvt_pk_bf16_f32 v125, v114, v115\n" \
"v_cvt_pk_bf16_f32 v126, v116, v117\n" \
"v_mfma_f32_16x16x32_bf16 v[64:67], a[32:35], a[192:195], v[64:67]\n" \
"v_cvt_pk_bf16_f32 v127, v118, v119\n" \
"v_mfma_f32_16x16x32_bf16 v[64:67], a[36:39], a[196:199], v[64:67]\n" \
"buffer_store_dwordx4 v[124:127], v240, s[56:59], s88 offen offset:2048 sc1\n" \
"s_waitcnt vmcnt(14) lgkmcnt(0)\n" \
"s_barrier\n" \
"v_mfma_f32_16x16x32_bf16 v[68:71], a[200:203], a[192:195], v[68:71]\n" \
"ds_read_b64_tr_b16 a[32:33], v194 offset:1024\n" \
"ds_read_b64_tr_b16 a[34:35], v194 offset:4096\n" \
"v_mfma_f32_16x16x32_bf16 v[68:71], a[204:207], a[196:199], v[68:71]\n" \
"ds_read_b64_tr_b16 a[36:37], v194 offset:13312\n" \
"ds_read_b64_tr_b16 a[38:39], v194 offset:16384\n" \
"v_mfma_f32_16x16x32_bf16 v[224:227], v[244:247], v[124:127], v[224:227]\n" \
"s_add_u32 s82, s88, 532480\n" \
"v_accvgpr_read_b32 v112, a96\n" \
"v_accvgpr_read_b32 v113, a97\n" \
"v_accvgpr_read_b32 v114, a98\n" \
"v_accvgpr_read_b32 v115, a99\n" \
"v_mfma_f32_16x16x32_bf16 v[60:63], a[200:203], a[24:27], v[60:63]\n" \
"s_add_u32 m0, s79, 0\n" \
"ds_read_b64_tr_b16 a[40:41], v194 offset:0\n" \
"v_accvgpr_read_b32 v116, a100\n" \
"v_mfma_f32_16x16x32_bf16 v[60:63], a[204:207], a[28:31], v[60:63]\n" \
"buffer_load_dwordx4 v192, s[44:47], s73 offen offset:0 lds\n" \
"v_mfma_f32_16x16x32_bf16 v[52:55], a[200:203], a[16:19], v[52:55]\n" \
"ds_read_b64_tr_b16 a[42:43], v194 offset:3072\n" \
"v_accvgpr_read_b32 v117, a101\n" \
"v_mfma_f32_16x16x32_bf16 v[52:55], a[204:207], a[20:23], v[52:55]\n" \
"ds_read_b64_tr_b16 a[44:45], v194 offset:12288\n" \
"v_accvgpr_read_b32 v118, a102\n" \
"v_accvgpr_read_b32 v119, a103\n" \
"v_mfma_f32_16x16x32_bf16 v[44:47], a[200:203], a[8:11], v[44:47]\n" \
"ds_read_b64_tr_b16 a[46:47], v194 offset:15360\n" \
"v_add_f32 v112, v112, v160\n" \
"v_mfma_f32_16x16x32_bf16 v[44:47], a[204:207], a[12:15], v[44:47]\n" \
"buffer_load_dwordx4 v193, s[44:47], s73 offen offset:1024 lds\n" \
"v_mfma_f32_16x16x32_bf16 v[72:75], a[0:3], a[208:211], v[72:75]\n" \
"ds_read_b64_tr_b16 a[48:49], v195 offset:0\n" \
"v_add_f32 v113, v113, v161\n" \
"v_mfma_f32_16x16x32_bf16 v[72:75], a[4:7], a[212:215], v[72:75]\n" \
"ds_read_b128 a[0:3], v207 offset:45056\n" \
"v_add_f32 v114, v114, v162\n" \
"v_add_f32 v115, v115, v163\n" \
"v_mfma_f32_16x16x32_bf16 v[80:83], a[8:11], a[208:211], v[80:83]\n" \
"ds_read_b128 a[4:7], v208 offset:45056\n" \
"v_add_f32 v116, v116, v164\n" \
"v_mfma_f32_16x16x32_bf16 v[80:83], a[12:15], a[212:215], v[80:83]\n" \
"buffer_load_dwordx4 v192, s[44:47], s73 offen offset:2048 lds\n" \
"v_add_f32 v117, v117, v165\n" \
"v_mfma_f32_16x16x32_bf16 v[84:87], a[8:11], a[216:219], v[84:87]\n" \
"ds_read_b64_tr_b16 a[50:51], v195 offset:3072\n" \
"v_add_f32 v118, v118, v166\n" \
"v_mfma_f32_16x16x32_bf16 v[84:87], a[12:15], a[220:223], v[84:87]\n" \
"ds_read_b128 a[8:11], v205 offset:45056\n" \
"v_add_f32 v119, v119, v167\n" \
"v_mfma_f32_16x16x32_bf16 v[92:95], a[16:19], a[216:219], v[92:95]\n" \
"ds_read_b128 a[12:15], v206 offset:45056\n" \
"v_mul_f32 v112, v112, v233\n" \
"v_mfma_f32_16x16x32_bf16 v[92:95], a[20:23], a[220:223], v[92:95]\n" \
"buffer_load_dwordx4 v193, s[44:47], s73 offen offset:3072 lds\n" \
"v_mul_f32 v113, v113, v233\n" \
"v_mfma_f32_16x16x32_bf16 v[88:91], a[16:19], a[208:211], v[88:91]\n" \
"ds_read_b64_tr_b16 a[52:53], v195 offset:12288\n" \
"v_mul_f32 v114, v114, v233\n" \
"v_mfma_f32_16x16x32_bf16 v[88:91], a[20:23], a[212:215], v[88:91]\n" \
"s_add_u32 m0, s89, 0\n" \
"ds_read_b128 a[16:19], v205 offset:49152\n" \
"v_mul_f32 v115, v115, v233\n" \
"v_mfma_f32_16x16x32_bf16 v[96:99], a[24:27], a[208:211], v[96:99]\n" \
"s_xor_b32 s73, s73, 0x80\n" \
"ds_read_b128 a[20:23], v206 offset:49152\n" \
"v_mul_f32 v116, v116, v233\n" \
"v_mfma_f32_16x16x32_bf16 v[96:99], a[28:31], a[212:215], v[96:99]\n" \
"buffer_load_dwordx4 v196, s[44:47], s75 offen lds\n" \
"v_mul_f32 v117, v117, v233\n" \
"v_mfma_f32_16x16x32_bf16 v[100:103], a[24:27], a[216:219], v[100:103]\n" \
"s_xor_b32 s75, s75, 0x80\n" \
"s_mul_i32 s76, s80, 393216\n" \
"ds_read_b64_tr_b16 a[54:55], v195 offset:15360\n" \
"v_mfma_f32_16x16x32_bf16 v[100:103], a[28:31], a[220:223], v[100:103]\n" \
"s_add_u32 s76, s76, s78\n" \
"ds_read_b128 a[24:27], v205 offset:53248\n" \
"v_mul_f32 v118, v118, v233\n" \
"v_mul_f32 v119, v119, v233\n" \
"v_mfma_f32_16x16x32_bf16 v[108:111], a[192:195], a[216:219], v[108:111]\n" \
"s_add_u32 m0, s91, 20480\n" \
"ds_read_b128 a[28:31], v206 offset:53248\n" \
"v_cvt_pk_bf16_f32 v120, v112, v113\n" \
"v_mfma_f32_16x16x32_bf16 v[108:111], a[196:199], a[220:223], v[108:111]\n" \
"buffer_load_dwordx4 v197, s[48:51], s76 offen offset:0 lds\n" \
"v_mfma_f32_16x16x32_bf16 v[104:107], a[192:195], a[208:211], v[104:107]\n" \
"v_cvt_pk_bf16_f32 v121, v114, v115\n" \
"v_cvt_pk_bf16_f32 v122, v116, v117\n" \
"v_mfma_f32_16x16x32_bf16 v[104:107], a[196:199], a[212:215], v[104:107]\n" \
"s_waitcnt lgkmcnt(2)\n" \
"v_cvt_pk_bf16_f32 v123, v118, v119\n" \
"v_mfma_f32_16x16x32_bf16 v[32:35], a[32:35], a[0:3], v[32:35]\n" \
"s_add_u32 s77, s76, 24576\n" \
"ds_read_b128 a[192:195], v205 offset:57344\n" \
"v_mfma_f32_16x16x32_bf16 v[32:35], a[36:39], a[4:7], v[32:35]\n" \
"buffer_load_dwordx4 v198, s[48:51], s76 offen offset:1024 lds\n" \
"v_mfma_f32_16x16x32_bf16 v[40:43], a[32:35], a[8:11], v[40:43]\n" \
"ds_read_b128 a[196:199], v206 offset:57344\n" \
"v_mfma_f32_16x16x32_bf16 v[40:43], a[36:39], a[12:15], v[40:43]\n" \
"ds_read_b64_tr_b16 a[200:201], v195 offset:1024\n" \
"buffer_store_dwordx4 v[120:123], v236, s[56:59], s82 offen offset:0 sc1\n" \
"v_mfma_f32_16x16x32_bf16 v[0:3], a[40:43], a[8:11], v[0:3]\n" \
"s_xor_b32 s80, s80, 0x1\n" \
"ds_read_b64_tr_b16 a[202:203], v195 offset:4096\n" \
"v_mfma_f32_16x16x32_bf16 v[224:227], v[244:247], v[120:123], v[224:227]\n" \
"v_accvgpr_read_b32 v112, a104\n" \
"v_accvgpr_read_b32 v113, a105\n" \
"v_mfma_f32_16x16x32_bf16 v[0:3], a[44:47], a[12:15], v[0:3]\n" \
"buffer_load_dwordx4 v199, s[48:51], s76 offen offset:2048 lds\n" \
"v_accvgpr_read_b32 v114, a106\n" \
"v_mfma_f32_16x16x32_bf16 v[4:7], a[48:51], a[8:11], v[4:7]\n" \
"ds_read_b64_tr_b16 a[204:205], v195 offset:13312\n" \
"v_accvgpr_read_b32 v115, a107\n" \
"v_mfma_f32_16x16x32_bf16 v[4:7], a[52:55], a[12:15], v[4:7]\n" \
"ds_read_b64_tr_b16 a[206:207], v195 offset:16384\n" \
"v_accvgpr_read_b32 v116, a108\n" \
"v_mfma_f32_16x16x32_bf16 v[12:15], a[48:51], a[16:19], v[12:15]\n" \
"s_add_u32 m0, m0, 3072\n" \
"ds_read_b64_tr_b16 a[208:209], v194 offset:2048\n" \
"v_accvgpr_read_b32 v117, a109\n" \
"v_mfma_f32_16x16x32_bf16 v[12:15], a[52:55], a[20:23], v[12:15]\n" \
"buffer_load_dwordx4 v197, s[48:51], s77 offen offset:0 lds\n" \
"v_accvgpr_read_b32 v118, a110\n" \
"v_mfma_f32_16x16x32_bf16 v[8:11], a[40:43], a[16:19], v[8:11]\n" \
"ds_read_b64_tr_b16 a[210:211], v194 offset:5120\n" \
"v_accvgpr_read_b32 v119, a111\n" \
"v_mfma_f32_16x16x32_bf16 v[8:11], a[44:47], a[20:23], v[8:11]\n" \
"ds_read_b64_tr_b16 a[212:213], v194 offset:14336\n" \
"v_add_f32 v112, v112, v168\n" \
"v_mfma_f32_16x16x32_bf16 v[48:51], a[32:35], a[16:19], v[48:51]\n" \
"s_waitcnt lgkmcnt(3)\n" \
"ds_read_b64_tr_b16 a[214:215], v194 offset:17408\n" \
"v_add_f32 v113, v113, v169\n" \
"v_mfma_f32_16x16x32_bf16 v[48:51], a[36:39], a[20:23], v[48:51]\n" \
"buffer_load_dwordx4 v198, s[48:51], s77 offen offset:1024 lds\n" \
"v_add_f32 v114, v114, v170\n" \
"v_mfma_f32_16x16x32_bf16 v[56:59], a[32:35], a[24:27], v[56:59]\n" \
"ds_read_b64_tr_b16 a[216:217], v195 offset:2048\n" \
"v_add_f32 v115, v115, v171\n" \
"v_mfma_f32_16x16x32_bf16 v[56:59], a[36:39], a[28:31], v[56:59]\n" \
"ds_read_b64_tr_b16 a[218:219], v195 offset:5120\n" \
"v_add_f32 v116, v116, v172\n" \
"v_add_f32 v117, v117, v173\n" \
"v_mfma_f32_16x16x32_bf16 v[16:19], a[40:43], a[24:27], v[16:19]\n" \
"ds_read_b64_tr_b16 a[220:221], v195 offset:14336\n" \
"v_add_f32 v118, v118, v174\n" \
"v_mfma_f32_16x16x32_bf16 v[16:19], a[44:47], a[28:31], v[16:19]\n" \
"buffer_load_dwordx4 v199, s[48:51], s77 offen offset:2048 lds\n" \
"v_mfma_f32_16x16x32_bf16 v[20:23], a[48:51], a[24:27], v[20:23]\n" \
"ds_read_b64_tr_b16 a[222:223], v195 offset:17408\n" \
"v_add_f32 v119, v119, v175\n" \
"v_mfma_f32_16x16x32_bf16 v[20:23], a[52:55], a[28:31], v[20:23]\n" \
"v_mul_f32 v112, v112, v233\n" \
"v_mul_f32 v113, v113, v233\n" \
"v_mul_f32 v114, v114, v233\n" \
"v_mfma_f32_16x16x32_bf16 v[28:31], a[48:51], a[192:195], v[28:31]\n" \
"v_mul_f32 v115, v115, v233\n" \
"v_mul_f32 v116, v116, v233\n" \
"v_mfma_f32_16x16x32_bf16 v[28:31], a[52:55], a[196:199], v[28:31]\n" \
"v_mul_f32 v117, v117, v233\n" \
"v_mul_f32 v118, v118, v233\n" \
"v_mfma_f32_16x16x32_bf16 v[24:27], a[40:43], a[192:195], v[24:27]\n" \
"v_mul_f32 v119, v119, v233\n" \
"v_cvt_pk_bf16_f32 v124, v112, v113\n" \
"v_mfma_f32_16x16x32_bf16 v[24:27], a[44:47], a[196:199], v[24:27]\n" \
"v_cvt_pk_bf16_f32 v125, v114, v115\n" \
"v_cvt_pk_bf16_f32 v126, v116, v117\n" \
"v_cvt_pk_bf16_f32 v127, v118, v119\n" \
"v_mfma_f32_16x16x32_bf16 v[64:67], a[32:35], a[192:195], v[64:67]\n" \
"buffer_store_dwordx4 v[124:127], v236, s[56:59], s82 offen offset:2048 sc1\n" \
"v_mfma_f32_16x16x32_bf16 v[64:67], a[36:39], a[196:199], v[64:67]\n" \
"v_mfma_f32_16x16x32_bf16 v[224:227], v[244:247], v[124:127], v[224:227]\n" \
"v_accvgpr_read_b32 v112, a112\n" \
"s_waitcnt vmcnt(14) lgkmcnt(0)\n" \
"s_barrier\n" \
"v_mfma_f32_16x16x32_bf16 v[68:71], a[200:203], a[192:195], v[68:71]\n" \
"ds_read_b64_tr_b16 a[32:33], v209 offset:1024\n" \
"ds_read_b64_tr_b16 a[34:35], v209 offset:4096\n" \
"v_mfma_f32_16x16x32_bf16 v[68:71], a[204:207], a[196:199], v[68:71]\n" \
"ds_read_b64_tr_b16 a[36:37], v209 offset:13312\n" \
"ds_read_b64_tr_b16 a[38:39], v209 offset:16384\n" \
"v_mfma_f32_16x16x32_bf16 v[60:63], a[200:203], a[24:27], v[60:63]\n" \
"s_add_u32 m0, s79, 110592\n" \
"ds_read_b64_tr_b16 a[40:41], v209 offset:0\n" \
"v_accvgpr_read_b32 v113, a113\n" \
"v_mfma_f32_16x16x32_bf16 v[60:63], a[204:207], a[28:31], v[60:63]\n" \
"buffer_load_dwordx4 v192, s[44:47], s73 offen offset:0 lds\n" \
"v_accvgpr_read_b32 v114, a114\n" \
"v_mfma_f32_16x16x32_bf16 v[52:55], a[200:203], a[16:19], v[52:55]\n" \
"ds_read_b64_tr_b16 a[42:43], v209 offset:3072\n" \
"v_accvgpr_read_b32 v115, a115\n" \
"v_mfma_f32_16x16x32_bf16 v[52:55], a[204:207], a[20:23], v[52:55]\n" \
"ds_read_b64_tr_b16 a[44:45], v209 offset:12288\n" \
"Lpg7957_%=:\n" \
"Lst1x0_%=:\n" \
"s_cbranch_execz Lpg10253_%=\n" \
"v_accvgpr_read_b32 v116, a116\n" \
"v_mfma_f32_16x16x32_bf16 v[44:47], a[200:203], a[8:11], v[44:47]\n" \
"ds_read_b64_tr_b16 a[46:47], v209 offset:15360\n" \
"v_accvgpr_read_b32 v117, a117\n" \
"v_mfma_f32_16x16x32_bf16 v[44:47], a[204:207], a[12:15], v[44:47]\n" \
"buffer_load_dwordx4 v193, s[44:47], s73 offen offset:1024 lds\n" \
"v_accvgpr_read_b32 v118, a118\n" \
"v_mfma_f32_16x16x32_bf16 v[72:75], a[0:3], a[208:211], v[72:75]\n" \
"ds_read_b64_tr_b16 a[48:49], v210 offset:0\n" \
"v_accvgpr_read_b32 v119, a119\n" \
"v_mfma_f32_16x16x32_bf16 v[72:75], a[4:7], a[212:215], v[72:75]\n" \
"ds_read_b128 a[0:3], v207 offset:0\n" \
"v_add_f32 v112, v112, v176\n" \
"v_add_f32 v113, v113, v177\n" \
"v_mfma_f32_16x16x32_bf16 v[80:83], a[8:11], a[208:211], v[80:83]\n" \
"ds_read_b128 a[4:7], v208 offset:0\n" \
"v_add_f32 v114, v114, v178\n" \
"v_mfma_f32_16x16x32_bf16 v[80:83], a[12:15], a[212:215], v[80:83]\n" \
"buffer_load_dwordx4 v192, s[44:47], s73 offen offset:2048 lds\n" \
"v_mfma_f32_16x16x32_bf16 v[84:87], a[8:11], a[216:219], v[84:87]\n" \
"ds_read_b64_tr_b16 a[50:51], v210 offset:3072\n" \
"v_add_f32 v115, v115, v179\n" \
"v_mfma_f32_16x16x32_bf16 v[84:87], a[12:15], a[220:223], v[84:87]\n" \
"ds_read_b128 a[8:11], v205 offset:0\n" \
"v_add_f32 v116, v116, v180\n" \
"v_add_f32 v117, v117, v181\n" \
"v_mfma_f32_16x16x32_bf16 v[92:95], a[16:19], a[216:219], v[92:95]\n" \
"ds_read_b128 a[12:15], v206 offset:0\n" \
"v_add_f32 v118, v118, v182\n" \
"v_mfma_f32_16x16x32_bf16 v[92:95], a[20:23], a[220:223], v[92:95]\n" \
"buffer_load_dwordx4 v193, s[44:47], s73 offen offset:3072 lds\n" \
"v_mfma_f32_16x16x32_bf16 v[88:91], a[16:19], a[208:211], v[88:91]\n" \
"ds_read_b64_tr_b16 a[52:53], v210 offset:12288\n" \
"v_add_f32 v119, v119, v183\n" \
"v_mfma_f32_16x16x32_bf16 v[88:91], a[20:23], a[212:215], v[88:91]\n" \
"s_add_u32 m0, s89, 110592\n" \
"ds_read_b128 a[16:19], v205 offset:4096\n" \
"v_mul_f32 v112, v112, v233\n" \
"v_mul_f32 v113, v113, v233\n" \
"v_mfma_f32_16x16x32_bf16 v[96:99], a[24:27], a[208:211], v[96:99]\n" \
"s_xor_b32 s73, s73, 0x180\n" \
"ds_read_b128 a[20:23], v206 offset:4096\n" \
"v_mul_f32 v114, v114, v233\n" \
"v_mfma_f32_16x16x32_bf16 v[96:99], a[28:31], a[212:215], v[96:99]\n" \
"buffer_load_dwordx4 v196, s[44:47], s75 offen lds\n" \
"v_mul_f32 v115, v115, v233\n" \
"v_mfma_f32_16x16x32_bf16 v[100:103], a[24:27], a[216:219], v[100:103]\n" \
"s_xor_b32 s75, s75, 0x180\n" \
"s_mul_i32 s76, s80, 393216\n" \
"ds_read_b64_tr_b16 a[54:55], v210 offset:15360\n" \
"v_mfma_f32_16x16x32_bf16 v[100:103], a[28:31], a[220:223], v[100:103]\n" \
"s_add_u32 s76, s76, s78\n" \
"ds_read_b128 a[24:27], v205 offset:8192\n" \
"v_mul_f32 v116, v116, v233\n" \
"v_mfma_f32_16x16x32_bf16 v[108:111], a[192:195], a[216:219], v[108:111]\n" \
"s_add_u32 m0, s91, 139264\n" \
"ds_read_b128 a[28:31], v206 offset:8192\n" \
"v_mul_f32 v117, v117, v233\n" \
"v_mfma_f32_16x16x32_bf16 v[108:111], a[196:199], a[220:223], v[108:111]\n" \
"buffer_load_dwordx4 v197, s[48:51], s76 offen offset:0 lds\n" \
"v_mul_f32 v118, v118, v233\n" \
"v_mfma_f32_16x16x32_bf16 v[104:107], a[192:195], a[208:211], v[104:107]\n" \
"v_mul_f32 v119, v119, v233\n" \
"v_cvt_pk_bf16_f32 v120, v112, v113\n" \
"v_mfma_f32_16x16x32_bf16 v[104:107], a[196:199], a[212:215], v[104:107]\n" \
"s_waitcnt lgkmcnt(2)\n" \
"v_cvt_pk_bf16_f32 v121, v114, v115\n" \
"v_cvt_pk_bf16_f32 v122, v116, v117\n" \
"v_mfma_f32_16x16x32_bf16 v[32:35], a[32:35], a[0:3], v[32:35]\n" \
"s_add_u32 s77, s76, 24576\n" \
"ds_read_b128 a[192:195], v205 offset:12288\n" \
"v_cvt_pk_bf16_f32 v123, v118, v119\n" \
"v_mfma_f32_16x16x32_bf16 v[32:35], a[36:39], a[4:7], v[32:35]\n" \
"buffer_load_dwordx4 v198, s[48:51], s76 offen offset:1024 lds\n" \
"v_mfma_f32_16x16x32_bf16 v[40:43], a[32:35], a[8:11], v[40:43]\n" \
"ds_read_b128 a[196:199], v206 offset:12288\n" \
"v_mfma_f32_16x16x32_bf16 v[40:43], a[36:39], a[12:15], v[40:43]\n" \
"ds_read_b64_tr_b16 a[200:201], v210 offset:1024\n" \
"buffer_store_dwordx4 v[120:123], v240, s[56:59], s82 offen offset:0 sc1\n" \
"v_mfma_f32_16x16x32_bf16 v[0:3], a[40:43], a[8:11], v[0:3]\n" \
"s_xor_b32 s80, s80, 0x3\n" \
"ds_read_b64_tr_b16 a[202:203], v210 offset:4096\n" \
"v_mfma_f32_16x16x32_bf16 v[224:227], v[244:247], v[120:123], v[224:227]\n" \
"v_accvgpr_read_b32 v112, a120\n" \
"v_accvgpr_read_b32 v113, a121\n" \
"v_mfma_f32_16x16x32_bf16 v[0:3], a[44:47], a[12:15], v[0:3]\n" \
"buffer_load_dwordx4 v199, s[48:51], s76 offen offset:2048 lds\n" \
"v_mfma_f32_16x16x32_bf16 v[4:7], a[48:51], a[8:11], v[4:7]\n" \
"ds_read_b64_tr_b16 a[204:205], v210 offset:13312\n" \
"v_accvgpr_read_b32 v114, a122\n" \
"v_mfma_f32_16x16x32_bf16 v[4:7], a[52:55], a[12:15], v[4:7]\n" \
"ds_read_b64_tr_b16 a[206:207], v210 offset:16384\n" \
"v_accvgpr_read_b32 v115, a123\n" \
"v_accvgpr_read_b32 v116, a124\n" \
"v_mfma_f32_16x16x32_bf16 v[12:15], a[48:51], a[16:19], v[12:15]\n" \
"s_add_u32 m0, m0, 3072\n" \
"ds_read_b64_tr_b16 a[208:209], v209 offset:2048\n" \
"v_accvgpr_read_b32 v117, a125\n" \
"v_mfma_f32_16x16x32_bf16 v[12:15], a[52:55], a[20:23], v[12:15]\n" \
"buffer_load_dwordx4 v197, s[48:51], s77 offen offset:0 lds\n" \
"v_mfma_f32_16x16x32_bf16 v[8:11], a[40:43], a[16:19], v[8:11]\n" \
"ds_read_b64_tr_b16 a[210:211], v209 offset:5120\n" \
"v_accvgpr_read_b32 v118, a126\n" \
"v_mfma_f32_16x16x32_bf16 v[8:11], a[44:47], a[20:23], v[8:11]\n" \
"ds_read_b64_tr_b16 a[212:213], v209 offset:14336\n" \
"v_accvgpr_read_b32 v119, a127\n" \
"v_add_f32 v112, v112, v184\n" \
"v_mfma_f32_16x16x32_bf16 v[48:51], a[32:35], a[16:19], v[48:51]\n" \
"s_waitcnt lgkmcnt(3)\n" \
"ds_read_b64_tr_b16 a[214:215], v209 offset:17408\n" \
"v_add_f32 v113, v113, v185\n" \
"v_mfma_f32_16x16x32_bf16 v[48:51], a[36:39], a[20:23], v[48:51]\n" \
"buffer_load_dwordx4 v198, s[48:51], s77 offen offset:1024 lds\n" \
"v_add_f32 v114, v114, v186\n" \
"v_mfma_f32_16x16x32_bf16 v[56:59], a[32:35], a[24:27], v[56:59]\n" \
"ds_read_b64_tr_b16 a[216:217], v210 offset:2048\n" \
"v_add_f32 v115, v115, v187\n" \
"v_mfma_f32_16x16x32_bf16 v[56:59], a[36:39], a[28:31], v[56:59]\n" \
"ds_read_b64_tr_b16 a[218:219], v210 offset:5120\n" \
"v_add_f32 v116, v116, v188\n" \
"v_mfma_f32_16x16x32_bf16 v[16:19], a[40:43], a[24:27], v[16:19]\n" \
"ds_read_b64_tr_b16 a[220:221], v210 offset:14336\n" \
"v_add_f32 v117, v117, v189\n" \
"v_mfma_f32_16x16x32_bf16 v[16:19], a[44:47], a[28:31], v[16:19]\n" \
"buffer_load_dwordx4 v199, s[48:51], s77 offen offset:2048 lds\n" \
"v_add_f32 v118, v118, v190\n" \
"v_mfma_f32_16x16x32_bf16 v[20:23], a[48:51], a[24:27], v[20:23]\n" \
"ds_read_b64_tr_b16 a[222:223], v210 offset:17408\n" \
"v_add_f32 v119, v119, v191\n" \
"v_mfma_f32_16x16x32_bf16 v[20:23], a[52:55], a[28:31], v[20:23]\n" \
"v_mul_f32 v112, v112, v233\n" \
"v_mul_f32 v113, v113, v233\n" \
"v_mfma_f32_16x16x32_bf16 v[28:31], a[48:51], a[192:195], v[28:31]\n" \
"v_mul_f32 v114, v114, v233\n" \
"v_mul_f32 v115, v115, v233\n" \
"v_mfma_f32_16x16x32_bf16 v[28:31], a[52:55], a[196:199], v[28:31]\n" \
"v_mul_f32 v116, v116, v233\n" \
"v_mul_f32 v117, v117, v233\n" \
"v_mul_f32 v118, v118, v233\n" \
"v_mfma_f32_16x16x32_bf16 v[24:27], a[40:43], a[192:195], v[24:27]\n" \
"v_mul_f32 v119, v119, v233\n" \
"v_cvt_pk_bf16_f32 v124, v112, v113\n" \
"v_mfma_f32_16x16x32_bf16 v[24:27], a[44:47], a[196:199], v[24:27]\n" \
"v_cvt_pk_bf16_f32 v125, v114, v115\n" \
"v_cvt_pk_bf16_f32 v126, v116, v117\n" \
"v_cvt_pk_bf16_f32 v127, v118, v119\n" \
"v_mfma_f32_16x16x32_bf16 v[64:67], a[32:35], a[192:195], v[64:67]\n" \
"buffer_store_dwordx4 v[124:127], v240, s[56:59], s82 offen offset:2048 sc1\n" \
"v_mfma_f32_16x16x32_bf16 v[64:67], a[36:39], a[196:199], v[64:67]\n" \
"v_mfma_f32_16x16x32_bf16 v[224:227], v[244:247], v[124:127], v[224:227]\n" \
"s_waitcnt vmcnt(14) lgkmcnt(0)\n" \
"s_barrier\n" \
"v_mfma_f32_16x16x32_bf16 v[68:71], a[200:203], a[192:195], v[68:71]\n" \
"ds_read_b64_tr_b16 a[32:33], v204 offset:21504\n" \
"ds_read_b64_tr_b16 a[34:35], v204 offset:24576\n" \
"v_mfma_f32_16x16x32_bf16 v[68:71], a[204:207], a[196:199], v[68:71]\n" \
"ds_read_b64_tr_b16 a[36:37], v204 offset:33792\n" \
"ds_read_b64_tr_b16 a[38:39], v204 offset:36864\n" \
"v_mfma_f32_16x16x32_bf16 v[60:63], a[200:203], a[24:27], v[60:63]\n" \
"s_add_u32 m0, s79, 65536\n" \
"ds_read_b64_tr_b16 a[40:41], v204 offset:20480\n" \
"v_mfma_f32_16x16x32_bf16 v[60:63], a[204:207], a[28:31], v[60:63]\n" \
"buffer_load_dwordx4 v192, s[44:47], s73 offen offset:0 lds\n" \
"v_mfma_f32_16x16x32_bf16 v[52:55], a[200:203], a[16:19], v[52:55]\n" \
"ds_read_b64_tr_b16 a[42:43], v204 offset:23552\n" \
"v_mfma_f32_16x16x32_bf16 v[52:55], a[204:207], a[20:23], v[52:55]\n" \
"ds_read_b64_tr_b16 a[44:45], v204 offset:32768\n" \
"v_mfma_f32_16x16x32_bf16 v[44:47], a[200:203], a[8:11], v[44:47]\n" \
"ds_read_b64_tr_b16 a[46:47], v204 offset:35840\n" \
"v_mfma_f32_16x16x32_bf16 v[44:47], a[204:207], a[12:15], v[44:47]\n" \
"buffer_load_dwordx4 v193, s[44:47], s73 offen offset:1024 lds\n" \
"v_mfma_f32_16x16x32_bf16 v[72:75], a[0:3], a[208:211], v[72:75]\n" \
"ds_read_b64_tr_b16 a[48:49], v222 offset:20480\n" \
"v_mfma_f32_16x16x32_bf16 v[72:75], a[4:7], a[212:215], v[72:75]\n" \
"ds_read_b128 a[0:3], v202 offset:0\n" \
"v_mfma_f32_16x16x32_bf16 v[80:83], a[8:11], a[208:211], v[80:83]\n" \
"ds_read_b128 a[4:7], v203 offset:0\n" \
"v_mfma_f32_16x16x32_bf16 v[80:83], a[12:15], a[212:215], v[80:83]\n" \
"buffer_load_dwordx4 v192, s[44:47], s73 offen offset:2048 lds\n" \
"v_mfma_f32_16x16x32_bf16 v[84:87], a[8:11], a[216:219], v[84:87]\n" \
"ds_read_b64_tr_b16 a[50:51], v222 offset:23552\n" \
"v_mfma_f32_16x16x32_bf16 v[84:87], a[12:15], a[220:223], v[84:87]\n" \
"ds_read_b128 a[8:11], v200 offset:0\n" \
"v_mfma_f32_16x16x32_bf16 v[92:95], a[16:19], a[216:219], v[92:95]\n" \
"ds_read_b128 a[12:15], v201 offset:0\n" \
"v_mfma_f32_16x16x32_bf16 v[92:95], a[20:23], a[220:223], v[92:95]\n" \
"buffer_load_dwordx4 v193, s[44:47], s73 offen offset:3072 lds\n" \
"v_mfma_f32_16x16x32_bf16 v[88:91], a[16:19], a[208:211], v[88:91]\n" \
"ds_read_b64_tr_b16 a[52:53], v222 offset:32768\n" \
"v_mfma_f32_16x16x32_bf16 v[88:91], a[20:23], a[212:215], v[88:91]\n" \
"s_add_u32 m0, s89, 65536\n" \
"ds_read_b128 a[16:19], v200 offset:4096\n" \
"v_mfma_f32_16x16x32_bf16 v[96:99], a[24:27], a[208:211], v[96:99]\n" \
"s_xor_b32 s73, s73, 0x80\n" \
"ds_read_b128 a[20:23], v201 offset:4096\n" \
"v_mfma_f32_16x16x32_bf16 v[96:99], a[28:31], a[212:215], v[96:99]\n" \
"buffer_load_dwordx4 v196, s[44:47], s75 offen lds\n" \
"v_mfma_f32_16x16x32_bf16 v[100:103], a[24:27], a[216:219], v[100:103]\n" \
"s_xor_b32 s75, s75, 0x80\n" \
"s_mul_i32 s76, s80, 393216\n" \
"ds_read_b64_tr_b16 a[54:55], v222 offset:35840\n" \
"v_mfma_f32_16x16x32_bf16 v[100:103], a[28:31], a[220:223], v[100:103]\n" \
"s_add_u32 s76, s76, s78\n" \
"ds_read_b128 a[24:27], v200 offset:8192\n" \
"v_mfma_f32_16x16x32_bf16 v[108:111], a[192:195], a[216:219], v[108:111]\n" \
"s_add_u32 m0, s91, 86016\n" \
"ds_read_b128 a[28:31], v201 offset:8192\n" \
"v_mfma_f32_16x16x32_bf16 v[108:111], a[196:199], a[220:223], v[108:111]\n" \
"buffer_load_dwordx4 v197, s[48:51], s76 offen offset:0 lds\n" \
"v_mfma_f32_16x16x32_bf16 v[104:107], a[192:195], a[208:211], v[104:107]\n" \
"v_mfma_f32_16x16x32_bf16 v[104:107], a[196:199], a[212:215], v[104:107]\n" \
"s_waitcnt lgkmcnt(2)\n" \
"v_mfma_f32_16x16x32_bf16 v[32:35], a[32:35], a[0:3], v[32:35]\n" \
"s_add_u32 s77, s76, 24576\n" \
"ds_read_b128 a[192:195], v200 offset:12288\n" \
"v_mfma_f32_16x16x32_bf16 v[32:35], a[36:39], a[4:7], v[32:35]\n" \
"buffer_load_dwordx4 v198, s[48:51], s76 offen offset:1024 lds\n" \
"v_mfma_f32_16x16x32_bf16 v[40:43], a[32:35], a[8:11], v[40:43]\n" \
"ds_read_b128 a[196:199], v201 offset:12288\n" \
"v_mfma_f32_16x16x32_bf16 v[40:43], a[36:39], a[12:15], v[40:43]\n" \
"ds_read_b64_tr_b16 a[200:201], v222 offset:21504\n" \
"v_mfma_f32_16x16x32_bf16 v[0:3], a[40:43], a[8:11], v[0:3]\n" \
"s_xor_b32 s80, s80, 0x1\n" \
"ds_read_b64_tr_b16 a[202:203], v222 offset:24576\n" \
"v_mfma_f32_16x16x32_bf16 v[0:3], a[44:47], a[12:15], v[0:3]\n" \
"buffer_load_dwordx4 v199, s[48:51], s76 offen offset:2048 lds\n" \
"v_mfma_f32_16x16x32_bf16 v[4:7], a[48:51], a[8:11], v[4:7]\n" \
"ds_read_b64_tr_b16 a[204:205], v222 offset:33792\n" \
"v_mfma_f32_16x16x32_bf16 v[4:7], a[52:55], a[12:15], v[4:7]\n" \
"ds_read_b64_tr_b16 a[206:207], v222 offset:36864\n" \
"v_mfma_f32_16x16x32_bf16 v[12:15], a[48:51], a[16:19], v[12:15]\n" \
"s_add_u32 m0, m0, 3072\n" \
"ds_read_b64_tr_b16 a[208:209], v204 offset:22528\n" \
"v_mfma_f32_16x16x32_bf16 v[12:15], a[52:55], a[20:23], v[12:15]\n" \
"buffer_load_dwordx4 v197, s[48:51], s77 offen offset:0 lds\n" \
"v_mfma_f32_16x16x32_bf16 v[8:11], a[40:43], a[16:19], v[8:11]\n" \
"ds_read_b64_tr_b16 a[210:211], v204 offset:25600\n" \
"v_mfma_f32_16x16x32_bf16 v[8:11], a[44:47], a[20:23], v[8:11]\n" \
"ds_read_b64_tr_b16 a[212:213], v204 offset:34816\n" \
"v_mfma_f32_16x16x32_bf16 v[48:51], a[32:35], a[16:19], v[48:51]\n" \
"s_waitcnt lgkmcnt(3)\n" \
"ds_read_b64_tr_b16 a[214:215], v204 offset:37888\n" \
"v_mfma_f32_16x16x32_bf16 v[48:51], a[36:39], a[20:23], v[48:51]\n" \
"buffer_load_dwordx4 v198, s[48:51], s77 offen offset:1024 lds\n" \
"v_mfma_f32_16x16x32_bf16 v[56:59], a[32:35], a[24:27], v[56:59]\n" \
"ds_read_b64_tr_b16 a[216:217], v222 offset:22528\n" \
"v_mfma_f32_16x16x32_bf16 v[56:59], a[36:39], a[28:31], v[56:59]\n" \
"ds_read_b64_tr_b16 a[218:219], v222 offset:25600\n" \
"v_mfma_f32_16x16x32_bf16 v[16:19], a[40:43], a[24:27], v[16:19]\n" \
"ds_read_b64_tr_b16 a[220:221], v222 offset:34816\n" \
"v_mfma_f32_16x16x32_bf16 v[16:19], a[44:47], a[28:31], v[16:19]\n" \
"buffer_load_dwordx4 v199, s[48:51], s77 offen offset:2048 lds\n" \
"v_mfma_f32_16x16x32_bf16 v[20:23], a[48:51], a[24:27], v[20:23]\n" \
"ds_read_b64_tr_b16 a[222:223], v222 offset:37888\n" \
"v_mfma_f32_16x16x32_bf16 v[20:23], a[52:55], a[28:31], v[20:23]\n" \
"v_mfma_f32_16x16x32_bf16 v[28:31], a[48:51], a[192:195], v[28:31]\n" \
"v_mfma_f32_16x16x32_bf16 v[28:31], a[52:55], a[196:199], v[28:31]\n" \
"v_mfma_f32_16x16x32_bf16 v[24:27], a[40:43], a[192:195], v[24:27]\n" \
"v_mfma_f32_16x16x32_bf16 v[24:27], a[44:47], a[196:199], v[24:27]\n" \
"v_mfma_f32_16x16x32_bf16 v[64:67], a[32:35], a[192:195], v[64:67]\n" \
"v_mfma_f32_16x16x32_bf16 v[64:67], a[36:39], a[196:199], v[64:67]\n" \
"s_waitcnt vmcnt(12) lgkmcnt(0)\n" \
"s_barrier\n" \
"s_movk_i32 s64, 0x207\n" \
"v_cmp_class_f32 vcc, v224, s64\n" \
"s_nop 1\n" \
"s_cmp_lg_u64 vcc, 0\n" \
"s_addc_u32 s90, s90, 0\n" \
"s_cmp_lg_u32 s90, 0\n" \
"s_cbranch_scc1 Lexact_%=\n" \
"Lexr_%=:\n" \
"v_mfma_f32_16x16x32_bf16 v[68:71], a[200:203], a[192:195], v[68:71]\n" \
"s_lshl_b32 s15, s63, 7\n" \
"ds_read_b64_tr_b16 a[32:33], v194 offset:1024\n" \
"ds_read_b64_tr_b16 a[34:35], v194 offset:4096\n" \
"v_mfma_f32_16x16x32_bf16 v[68:71], a[204:207], a[196:199], v[68:71]\n" \
"s_lshl2_add_u32 s16, s62, 65589248\n" \
"ds_read_b64_tr_b16 a[36:37], v194 offset:13312\n" \
"ds_read_b64_tr_b16 a[38:39], v194 offset:16384\n" \
"v_mfma_f32_16x16x32_bf16 v[60:63], a[200:203], a[24:27], v[60:63]\n" \
"s_add_u32 m0, s79, 0\n" \
"s_sub_u32 s17, s16, s15\n" \
"ds_read_b64_tr_b16 a[40:41], v194 offset:0\n" \
"v_mfma_f32_16x16x32_bf16 v[60:63], a[204:207], a[28:31], v[60:63]\n" \
"buffer_load_dwordx4 v192, s[44:47], s73 offen offset:0 lds\n" \
"v_mfma_f32_16x16x32_bf16 v[52:55], a[200:203], a[16:19], v[52:55]\n" \
"s_add_u32 s15, s15, s16\n" \
"s_add_u32 s17, s17, 1920\n" \
"ds_read_b64_tr_b16 a[42:43], v194 offset:3072\n" \
"v_mfma_f32_16x16x32_bf16 v[52:55], a[204:207], a[20:23], v[52:55]\n" \
"ds_read_b64_tr_b16 a[44:45], v194 offset:12288\n" \
"v_mov_b32 v146, s60\n" \
"v_mfma_f32_16x16x32_bf16 v[44:47], a[200:203], a[8:11], v[44:47]\n" \
"ds_read_b64_tr_b16 a[46:47], v194 offset:15360\n" \
"v_lshlrev_b32 v147, 28, s61\n" \
"v_mfma_f32_16x16x32_bf16 v[44:47], a[204:207], a[12:15], v[44:47]\n" \
"buffer_load_dwordx4 v193, s[44:47], s73 offen offset:1024 lds\n" \
"v_mfma_f32_16x16x32_bf16 v[72:75], a[0:3], a[208:211], v[72:75]\n" \
"ds_read_b64_tr_b16 a[48:49], v195 offset:0\n" \
"v_mfma_f32_16x16x32_bf16 v[72:75], a[4:7], a[212:215], v[72:75]\n" \
"ds_read_b128 a[0:3], v207 offset:45056\n" \
"v_mfma_f32_16x16x32_bf16 v[80:83], a[8:11], a[208:211], v[80:83]\n" \
"ds_read_b128 a[4:7], v208 offset:45056\n" \
"v_mfma_f32_16x16x32_bf16 v[80:83], a[12:15], a[212:215], v[80:83]\n" \
"buffer_load_dwordx4 v192, s[44:47], s73 offen offset:2048 lds\n" \
"v_mfma_f32_16x16x32_bf16 v[84:87], a[8:11], a[216:219], v[84:87]\n" \
"ds_read_b64_tr_b16 a[50:51], v195 offset:3072\n" \
"v_mfma_f32_16x16x32_bf16 v[84:87], a[12:15], a[220:223], v[84:87]\n" \
"ds_read_b128 a[8:11], v205 offset:45056\n" \
"v_mfma_f32_16x16x32_bf16 v[92:95], a[16:19], a[216:219], v[92:95]\n" \
"ds_read_b128 a[12:15], v206 offset:45056\n" \
"v_mfma_f32_16x16x32_bf16 v[92:95], a[20:23], a[220:223], v[92:95]\n" \
"buffer_load_dwordx4 v193, s[44:47], s73 offen offset:3072 lds\n" \
"v_mfma_f32_16x16x32_bf16 v[88:91], a[16:19], a[208:211], v[88:91]\n" \
"ds_read_b64_tr_b16 a[52:53], v195 offset:12288\n" \
"v_mfma_f32_16x16x32_bf16 v[88:91], a[20:23], a[212:215], v[88:91]\n" \
"s_add_u32 m0, s89, 0\n" \
"ds_read_b128 a[16:19], v205 offset:49152\n" \
"v_mfma_f32_16x16x32_bf16 v[96:99], a[24:27], a[208:211], v[96:99]\n" \
"s_xor_b32 s73, s73, 0x780\n" \
"ds_read_b128 a[20:23], v206 offset:49152\n" \
"v_mfma_f32_16x16x32_bf16 v[96:99], a[28:31], a[212:215], v[96:99]\n" \
"buffer_load_dwordx4 v196, s[44:47], s75 offen lds\n" \
"v_mfma_f32_16x16x32_bf16 v[100:103], a[24:27], a[216:219], v[100:103]\n" \
"s_xor_b32 s75, s75, 0x780\n" \
"s_mul_i32 s76, s80, 393216\n" \
"ds_read_b64_tr_b16 a[54:55], v195 offset:15360\n" \
"v_mfma_f32_16x16x32_bf16 v[100:103], a[28:31], a[220:223], v[100:103]\n" \
"s_add_u32 s76, s76, s78\n" \
"ds_read_b128 a[24:27], v205 offset:53248\n" \
"v_mfma_f32_16x16x32_bf16 v[108:111], a[192:195], a[216:219], v[108:111]\n" \
"s_add_u32 m0, s91, 20480\n" \
"ds_read_b128 a[28:31], v206 offset:53248\n" \
"v_mfma_f32_16x16x32_bf16 v[108:111], a[196:199], a[220:223], v[108:111]\n" \
"buffer_load_dwordx4 v197, s[48:51], s76 offen offset:0 lds\n" \
"v_mfma_f32_16x16x32_bf16 v[104:107], a[192:195], a[208:211], v[104:107]\n" \
"v_mfma_f32_16x16x32_bf16 v[104:107], a[196:199], a[212:215], v[104:107]\n" \
"s_waitcnt lgkmcnt(2)\n" \
"v_mfma_f32_16x16x32_bf16 v[32:35], a[32:35], a[0:3], v[32:35]\n" \
"s_add_u32 s77, s76, 24576\n" \
"ds_read_b128 a[192:195], v205 offset:57344\n" \
"v_mfma_f32_16x16x32_bf16 v[32:35], a[36:39], a[4:7], v[32:35]\n" \
"buffer_load_dwordx4 v198, s[48:51], s76 offen offset:1024 lds\n" \
"v_mfma_f32_16x16x32_bf16 v[40:43], a[32:35], a[8:11], v[40:43]\n" \
"ds_read_b128 a[196:199], v206 offset:57344\n" \
"v_mfma_f32_16x16x32_bf16 v[40:43], a[36:39], a[12:15], v[40:43]\n" \
"ds_read_b64_tr_b16 a[200:201], v195 offset:1024\n" \
"v_mfma_f32_16x16x32_bf16 v[0:3], a[40:43], a[8:11], v[0:3]\n" \
"s_xor_b32 s80, s80, 0xf\n" \
"ds_read_b64_tr_b16 a[202:203], v195 offset:4096\n" \
"v_mfma_f32_16x16x32_bf16 v[0:3], a[44:47], a[12:15], v[0:3]\n" \
"buffer_load_dwordx4 v199, s[48:51], s76 offen offset:2048 lds\n" \
"v_mfma_f32_16x16x32_bf16 v[4:7], a[48:51], a[8:11], v[4:7]\n" \
"ds_read_b64_tr_b16 a[204:205], v195 offset:13312\n" \
"v_mfma_f32_16x16x32_bf16 v[4:7], a[52:55], a[12:15], v[4:7]\n" \
"ds_read_b64_tr_b16 a[206:207], v195 offset:16384\n" \
"v_mfma_f32_16x16x32_bf16 v[12:15], a[48:51], a[16:19], v[12:15]\n" \
"s_add_u32 m0, m0, 3072\n" \
"ds_read_b64_tr_b16 a[208:209], v194 offset:2048\n" \
"v_mfma_f32_16x16x32_bf16 v[12:15], a[52:55], a[20:23], v[12:15]\n" \
"buffer_load_dwordx4 v197, s[48:51], s77 offen offset:0 lds\n" \
"v_mfma_f32_16x16x32_bf16 v[8:11], a[40:43], a[16:19], v[8:11]\n" \
"ds_read_b64_tr_b16 a[210:211], v194 offset:5120\n" \
"v_mfma_f32_16x16x32_bf16 v[8:11], a[44:47], a[20:23], v[8:11]\n" \
"ds_read_b64_tr_b16 a[212:213], v194 offset:14336\n" \
"v_mfma_f32_16x16x32_bf16 v[48:51], a[32:35], a[16:19], v[48:51]\n" \
"s_waitcnt lgkmcnt(3)\n" \
"ds_read_b64_tr_b16 a[214:215], v194 offset:17408\n" \
"v_mfma_f32_16x16x32_bf16 v[48:51], a[36:39], a[20:23], v[48:51]\n" \
"buffer_load_dwordx4 v198, s[48:51], s77 offen offset:1024 lds\n" \
"v_mfma_f32_16x16x32_bf16 v[56:59], a[32:35], a[24:27], v[56:59]\n" \
"ds_read_b64_tr_b16 a[216:217], v195 offset:2048\n" \
"v_mfma_f32_16x16x32_bf16 v[56:59], a[36:39], a[28:31], v[56:59]\n" \
"ds_read_b64_tr_b16 a[218:219], v195 offset:5120\n" \
"v_mfma_f32_16x16x32_bf16 v[16:19], a[40:43], a[24:27], v[16:19]\n" \
"ds_read_b64_tr_b16 a[220:221], v195 offset:14336\n" \
"v_mfma_f32_16x16x32_bf16 v[16:19], a[44:47], a[28:31], v[16:19]\n" \
"buffer_load_dwordx4 v199, s[48:51], s77 offen offset:2048 lds\n" \
"v_mfma_f32_16x16x32_bf16 v[20:23], a[48:51], a[24:27], v[20:23]\n" \
"ds_read_b64_tr_b16 a[222:223], v195 offset:17408\n" \
"v_mfma_f32_16x16x32_bf16 v[20:23], a[52:55], a[28:31], v[20:23]\n" \
"v_mfma_f32_16x16x32_bf16 v[28:31], a[48:51], a[192:195], v[28:31]\n" \
"v_add_u32 v254, 3145728, v254\n" \
"v_mfma_f32_16x16x32_bf16 v[28:31], a[52:55], a[196:199], v[28:31]\n" \
"global_load_dword v253, v254, s[22:23]\n" \
"v_mfma_f32_16x16x32_bf16 v[24:27], a[40:43], a[192:195], v[24:27]\n" \
"v_mfma_f32_16x16x32_bf16 v[24:27], a[44:47], a[196:199], v[24:27]\n" \
"v_mfma_f32_16x16x32_bf16 v[64:67], a[32:35], a[192:195], v[64:67]\n" \
"v_mfma_f32_16x16x32_bf16 v[64:67], a[36:39], a[196:199], v[64:67]\n" \
"s_waitcnt vmcnt(12) lgkmcnt(0)\n" \
"s_barrier\n" \
"v_mfma_f32_16x16x32_bf16 v[68:71], a[200:203], a[192:195], v[68:71]\n" \
"ds_read_b64_tr_b16 a[32:33], v209 offset:1024\n" \
"ds_read_b64_tr_b16 a[34:35], v209 offset:4096\n" \
"v_mfma_f32_16x16x32_bf16 v[68:71], a[204:207], a[196:199], v[68:71]\n" \
"ds_read_b64_tr_b16 a[36:37], v209 offset:13312\n" \
"ds_read_b64_tr_b16 a[38:39], v209 offset:16384\n" \
"v_mfma_f32_16x16x32_bf16 v[60:63], a[200:203], a[24:27], v[60:63]\n" \
"s_add_u32 m0, s79, 110592\n" \
"ds_read_b64_tr_b16 a[40:41], v209 offset:0\n" \
"buffer_store_dword v146, v147, s[56:59], s15 offen sc1\n" \
"v_mfma_f32_16x16x32_bf16 v[60:63], a[204:207], a[28:31], v[60:63]\n" \
"buffer_load_dwordx4 v192, s[44:47], s73 offen offset:0 lds\n" \
"v_mfma_f32_16x16x32_bf16 v[52:55], a[200:203], a[16:19], v[52:55]\n" \
"ds_read_b64_tr_b16 a[42:43], v209 offset:3072\n" \
"v_mfma_f32_16x16x32_bf16 v[52:55], a[204:207], a[20:23], v[52:55]\n" \
"ds_read_b64_tr_b16 a[44:45], v209 offset:12288\n" \
"buffer_store_dword v146, v147, s[56:59], s17 offen sc1\n" \
"v_mfma_f32_16x16x32_bf16 v[44:47], a[200:203], a[8:11], v[44:47]\n" \
"s_mov_b64 s[36:37], s[18:19]\n" \
"ds_read_b64_tr_b16 a[46:47], v209 offset:15360\n" \
"v_mfma_f32_16x16x32_bf16 v[44:47], a[204:207], a[12:15], v[44:47]\n" \
"buffer_load_dwordx4 v193, s[44:47], s73 offen offset:1024 lds\n" \
"v_mfma_f32_16x16x32_bf16 v[72:75], a[0:3], a[208:211], v[72:75]\n" \
"s_sub_u32 s36, s36, 4096\n" \
"s_subb_u32 s37, s37, 0\n" \
"ds_read_b64_tr_b16 a[48:49], v210 offset:0\n" \
"v_mfma_f32_16x16x32_bf16 v[72:75], a[4:7], a[212:215], v[72:75]\n" \
"s_and_b32 s37, s37, 0xffff\n" \
"s_mov_b32 s38, 8392704\n" \
"ds_read_b128 a[0:3], v207 offset:0\n" \
"v_mfma_f32_16x16x32_bf16 v[80:83], a[8:11], a[208:211], v[80:83]\n" \
"s_mov_b32 s39, 0x20000\n" \
"s_mov_b64 s[40:41], s[24:25]\n" \
"ds_read_b128 a[4:7], v208 offset:0\n" \
"v_mfma_f32_16x16x32_bf16 v[80:83], a[12:15], a[212:215], v[80:83]\n" \
"buffer_load_dwordx4 v192, s[44:47], s73 offen offset:2048 lds\n" \
"v_mfma_f32_16x16x32_bf16 v[84:87], a[8:11], a[216:219], v[84:87]\n" \
"s_sub_u32 s40, s40, 4096\n" \
"s_subb_u32 s41, s41, 0\n" \
"ds_read_b64_tr_b16 a[50:51], v210 offset:3072\n" \
"v_mfma_f32_16x16x32_bf16 v[84:87], a[12:15], a[220:223], v[84:87]\n" \
"s_and_b32 s41, s41, 0xffff\n" \
"s_mov_b32 s42, 2101248\n" \
"ds_read_b128 a[8:11], v205 offset:0\n" \
"v_mfma_f32_16x16x32_bf16 v[92:95], a[16:19], a[216:219], v[92:95]\n" \
"s_mov_b32 s43, 0x20000\n" \
"s_mov_b64 s[52:53], s[26:27]\n" \
"ds_read_b128 a[12:15], v206 offset:0\n" \
"v_mfma_f32_16x16x32_bf16 v[92:95], a[20:23], a[220:223], v[92:95]\n" \
"buffer_load_dwordx4 v193, s[44:47], s73 offen offset:3072 lds\n" \
"v_mfma_f32_16x16x32_bf16 v[88:91], a[16:19], a[208:211], v[88:91]\n" \
"s_and_b32 s53, s53, 0xffff\n" \
"s_mov_b32 s54, 8388608\n" \
"ds_read_b64_tr_b16 a[52:53], v210 offset:12288\n" \
"v_mfma_f32_16x16x32_bf16 v[88:91], a[20:23], a[212:215], v[88:91]\n" \
"s_add_u32 m0, s89, 110592\n" \
"s_mov_b32 s55, 0x20000\n" \
"ds_read_b128 a[16:19], v205 offset:4096\n" \
"v_mfma_f32_16x16x32_bf16 v[96:99], a[24:27], a[208:211], v[96:99]\n" \
"s_xor_b32 s73, s73, 0x80\n" \
"ds_read_b128 a[20:23], v206 offset:4096\n" \
"v_lshrrev_b32 v221, 2, v234\n" \
"v_mfma_f32_16x16x32_bf16 v[96:99], a[28:31], a[212:215], v[96:99]\n" \
"buffer_load_dwordx4 v196, s[44:47], s75 offen lds\n" \
"v_mfma_f32_16x16x32_bf16 v[100:103], a[24:27], a[216:219], v[100:103]\n" \
"s_xor_b32 s75, s75, 0x80\n" \
"s_mul_i32 s76, s80, 393216\n" \
"ds_read_b64_tr_b16 a[54:55], v210 offset:15360\n" \
"v_mfma_f32_16x16x32_bf16 v[100:103], a[28:31], a[220:223], v[100:103]\n" \
"s_add_u32 s76, s76, s78\n" \
"ds_read_b128 a[24:27], v205 offset:8192\n" \
"v_lshrrev_b32 v244, 4, v234\n" \
"v_mfma_f32_16x16x32_bf16 v[108:111], a[192:195], a[216:219], v[108:111]\n" \
"s_add_u32 m0, s91, 139264\n" \
"s_lshl_b32 s12, s94, 7\n" \
"ds_read_b128 a[28:31], v206 offset:8192\n" \
"v_mfma_f32_16x16x32_bf16 v[108:111], a[196:199], a[220:223], v[108:111]\n" \
"buffer_load_dwordx4 v197, s[48:51], s76 offen offset:0 lds\n" \
"v_mfma_f32_16x16x32_bf16 v[104:107], a[192:195], a[208:211], v[104:107]\n" \
"s_add_u32 s12, s12, 65591296\n" \
"s_and_b32 s70, %1, 1\n" \
"s_lshl3_add_u32 s70, s70, s63\n" \
"v_mfma_f32_16x16x32_bf16 v[104:107], a[196:199], a[212:215], v[104:107]\n" \
"s_waitcnt lgkmcnt(2)\n" \
"s_lshl2_add_u32 s12, s70, s12\n" \
"s_lshr_b32 s68, s63, 1\n" \
"s_add_u32 s68, s68, 2\n" \
"v_mfma_f32_16x16x32_bf16 v[32:35], a[32:35], a[0:3], v[32:35]\n" \
"s_add_u32 s77, s76, 24576\n" \
"s_mov_b32 s13, 65593856\n" \
"ds_read_b128 a[192:195], v205 offset:12288\n" \
"v_mfma_f32_16x16x32_bf16 v[32:35], a[36:39], a[4:7], v[32:35]\n" \
"buffer_load_dwordx4 v198, s[48:51], s76 offen offset:1024 lds\n" \
"v_mfma_f32_16x16x32_bf16 v[40:43], a[32:35], a[8:11], v[40:43]\n" \
"s_lshr_b32 s70, s18, 8\n" \
"ds_read_b128 a[196:199], v206 offset:12288\n" \
"v_lshrrev_b32 v228, 1, v234\n" \
"v_mfma_f32_16x16x32_bf16 v[40:43], a[36:39], a[12:15], v[40:43]\n" \
"s_sub_u32 s68, s68, s70\n" \
"s_and_b32 s68, s68, 3\n" \
"ds_read_b64_tr_b16 a[200:201], v210 offset:1024\n" \
"v_mfma_f32_16x16x32_bf16 v[0:3], a[40:43], a[8:11], v[0:3]\n" \
"s_xor_b32 s80, s80, 0x1\n" \
"s_and_b32 s70, s63, 1\n" \
"ds_read_b64_tr_b16 a[202:203], v210 offset:4096\n" \
"v_mfma_f32_16x16x32_bf16 v[0:3], a[44:47], a[12:15], v[0:3]\n" \
"buffer_load_dwordx4 v199, s[48:51], s76 offen offset:2048 lds\n" \
"v_mfma_f32_16x16x32_bf16 v[4:7], a[48:51], a[8:11], v[4:7]\n" \
"s_lshl_b32 s70, s70, 2\n" \
"s_or_b32 s68, s68, s70\n" \
"ds_read_b64_tr_b16 a[204:205], v210 offset:13312\n" \
"v_mfma_f32_16x16x32_bf16 v[4:7], a[52:55], a[12:15], v[4:7]\n" \
"s_lshr_b32 s84, s61, 1\n" \
"s_lshl_b32 s84, s84, 16\n" \
"ds_read_b64_tr_b16 a[206:207], v210 offset:16384\n" \
"v_mfma_f32_16x16x32_bf16 v[12:15], a[48:51], a[16:19], v[12:15]\n" \
"s_add_u32 m0, m0, 3072\n" \
"s_and_b32 s70, s61, 1\n" \
"ds_read_b64_tr_b16 a[208:209], v209 offset:2048\n" \
"v_mfma_f32_16x16x32_bf16 v[12:15], a[52:55], a[20:23], v[12:15]\n" \
"buffer_load_dwordx4 v197, s[48:51], s77 offen offset:0 lds\n" \
"v_mfma_f32_16x16x32_bf16 v[8:11], a[40:43], a[16:19], v[8:11]\n" \
"s_lshl_b32 s70, s70, 14\n" \
"s_add_u32 s84, s84, s70\n" \
"ds_read_b64_tr_b16 a[210:211], v209 offset:5120\n" \
"v_mfma_f32_16x16x32_bf16 v[8:11], a[44:47], a[20:23], v[8:11]\n" \
"s_lshl_b32 s88, s68, 8\n" \
"s_add_u32 s84, s84, s88\n" \
"ds_read_b64_tr_b16 a[212:213], v209 offset:14336\n" \
"v_mfma_f32_16x16x32_bf16 v[48:51], a[32:35], a[16:19], v[48:51]\n" \
"s_waitcnt lgkmcnt(3)\n" \
"ds_read_b64_tr_b16 a[214:215], v209 offset:17408\n" \
"v_bfe_u32 v245, v244, 2, 3\n" \
"v_mfma_f32_16x16x32_bf16 v[48:51], a[36:39], a[20:23], v[48:51]\n" \
"buffer_load_dwordx4 v198, s[48:51], s77 offen offset:1024 lds\n" \
"v_mfma_f32_16x16x32_bf16 v[56:59], a[32:35], a[24:27], v[56:59]\n" \
"ds_read_b64_tr_b16 a[216:217], v210 offset:2048\n" \
"v_lshlrev_b32 v245, 11, v245\n" \
"v_mfma_f32_16x16x32_bf16 v[56:59], a[36:39], a[28:31], v[56:59]\n" \
"ds_read_b64_tr_b16 a[218:219], v210 offset:5120\n" \
"v_lshrrev_b32 v246, 5, v244\n" \
"v_mfma_f32_16x16x32_bf16 v[16:19], a[40:43], a[24:27], v[16:19]\n" \
"ds_read_b64_tr_b16 a[220:221], v210 offset:14336\n" \
"v_lshl_add_u32 v245, v246, 6, v245\n" \
"v_mfma_f32_16x16x32_bf16 v[16:19], a[44:47], a[28:31], v[16:19]\n" \
"buffer_load_dwordx4 v199, s[48:51], s77 offen offset:2048 lds\n" \
"v_mfma_f32_16x16x32_bf16 v[20:23], a[48:51], a[24:27], v[20:23]\n" \
"ds_read_b64_tr_b16 a[222:223], v210 offset:17408\n" \
"v_and_b32 v246, 3, v244\n" \
"v_mfma_f32_16x16x32_bf16 v[20:23], a[52:55], a[28:31], v[20:23]\n" \
"v_bfe_u32 v247, v244, 4, 1\n" \
"v_lshlrev_b32 v247, 1, v247\n" \
"v_mfma_f32_16x16x32_bf16 v[28:31], a[48:51], a[192:195], v[28:31]\n" \
"v_xor_b32 v246, v246, v247\n" \
"v_lshl_add_u32 v245, v246, 4, v245\n" \
"v_mfma_f32_16x16x32_bf16 v[28:31], a[52:55], a[196:199], v[28:31]\n" \
"buffer_load_dwordx2 v[226:227], v228, s[56:59], s13 offen sc1\n" \
"v_mfma_f32_16x16x32_bf16 v[24:27], a[40:43], a[192:195], v[24:27]\n" \
"v_add_u32 v216, 4096, v245\n" \
"v_add_u32 v217, 3200, v245\n" \
"v_mfma_f32_16x16x32_bf16 v[24:27], a[44:47], a[196:199], v[24:27]\n" \
"v_add_u32 v218, 34816, v245\n" \
"v_add_u32 v219, 33920, v245\n" \
"v_mfma_f32_16x16x32_bf16 v[64:67], a[32:35], a[192:195], v[64:67]\n" \
"s_mov_b32 s86, 65536\n" \
"s_lshr_b32 s70, s61, 1\n" \
"s_lshl_b32 s71, s70, 13\n" \
"v_mfma_f32_16x16x32_bf16 v[64:67], a[36:39], a[196:199], v[64:67]\n" \
"s_add_u32 s71, s71, %3\n" \
"s_waitcnt vmcnt(15) lgkmcnt(0)\n" \
"s_barrier\n" \
"v_mfma_f32_16x16x32_bf16 v[68:71], a[200:203], a[192:195], v[68:71]\n" \
"ds_read_b64_tr_b16 a[32:33], v204 offset:21504\n" \
"Lpg8522_%=:\n" \
"Lst1x1_%=:\n" \
"s_cbranch_execz Lpg10849_%=\n" \
"ds_read_b64_tr_b16 a[34:35], v204 offset:24576\n" \
"v_mfma_f32_16x16x32_bf16 v[68:71], a[204:207], a[196:199], v[68:71]\n" \
"ds_read_b64_tr_b16 a[36:37], v204 offset:33792\n" \
"ds_read_b64_tr_b16 a[38:39], v204 offset:36864\n" \
"v_mfma_f32_16x16x32_bf16 v[60:63], a[200:203], a[24:27], v[60:63]\n" \
"s_add_u32 m0, s79, 65536\n" \
"ds_read_b64_tr_b16 a[40:41], v204 offset:20480\n" \
"v_add_u32 v211, s71, v236\n" \
"v_mfma_f32_16x16x32_bf16 v[60:63], a[204:207], a[28:31], v[60:63]\n" \
"buffer_load_dwordx4 v192, s[44:47], s73 offen offset:0 lds\n" \
"v_mfma_f32_16x16x32_bf16 v[52:55], a[200:203], a[16:19], v[52:55]\n" \
"s_lshl_b32 s70, s70, 18\n" \
"s_add_u32 s88, s88, s70\n" \
"ds_read_b64_tr_b16 a[42:43], v204 offset:23552\n" \
"v_mfma_f32_16x16x32_bf16 v[52:55], a[204:207], a[20:23], v[52:55]\n" \
"s_and_b32 s70, s61, 1\n" \
"s_lshl_b32 s71, s70, 7\n" \
"ds_read_b64_tr_b16 a[44:45], v204 offset:32768\n" \
"v_mfma_f32_16x16x32_bf16 v[44:47], a[200:203], a[8:11], v[44:47]\n" \
"s_add_u32 s88, s88, s71\n" \
"ds_read_b64_tr_b16 a[46:47], v204 offset:35840\n" \
"v_and_b32 v245, 3, v244\n" \
"v_mfma_f32_16x16x32_bf16 v[44:47], a[204:207], a[12:15], v[44:47]\n" \
"buffer_load_dwordx4 v193, s[44:47], s73 offen offset:1024 lds\n" \
"v_mfma_f32_16x16x32_bf16 v[72:75], a[0:3], a[208:211], v[72:75]\n" \
"ds_read_b64_tr_b16 a[48:49], v222 offset:20480\n" \
"v_lshlrev_b32 v245, 3, v245\n" \
"v_mfma_f32_16x16x32_bf16 v[72:75], a[4:7], a[212:215], v[72:75]\n" \
"ds_read_b128 a[0:3], v202 offset:0\n" \
"v_bfe_u32 v246, v244, 2, 2\n" \
"v_mfma_f32_16x16x32_bf16 v[80:83], a[8:11], a[208:211], v[80:83]\n" \
"ds_read_b128 a[4:7], v203 offset:0\n" \
"v_lshl_add_u32 v245, v246, 6, v245\n" \
"v_mfma_f32_16x16x32_bf16 v[80:83], a[12:15], a[212:215], v[80:83]\n" \
"buffer_load_dwordx4 v192, s[44:47], s73 offen offset:2048 lds\n" \
"v_mfma_f32_16x16x32_bf16 v[84:87], a[8:11], a[216:219], v[84:87]\n" \
"ds_read_b64_tr_b16 a[50:51], v222 offset:23552\n" \
"v_bfe_u32 v246, v244, 4, 1\n" \
"v_mfma_f32_16x16x32_bf16 v[84:87], a[12:15], a[220:223], v[84:87]\n" \
"ds_read_b128 a[8:11], v200 offset:0\n" \
"v_lshl_add_u32 v245, v246, 5, v245\n" \
"v_mfma_f32_16x16x32_bf16 v[92:95], a[16:19], a[216:219], v[92:95]\n" \
"ds_read_b128 a[12:15], v201 offset:0\n" \
"v_lshl_add_u32 v245, v246, 8, v245\n" \
"v_mfma_f32_16x16x32_bf16 v[92:95], a[20:23], a[220:223], v[92:95]\n" \
"buffer_load_dwordx4 v193, s[44:47], s73 offen offset:3072 lds\n" \
"v_mfma_f32_16x16x32_bf16 v[88:91], a[16:19], a[208:211], v[88:91]\n" \
"ds_read_b64_tr_b16 a[52:53], v222 offset:32768\n" \
"v_lshrrev_b32 v246, 5, v244\n" \
"v_mfma_f32_16x16x32_bf16 v[88:91], a[20:23], a[212:215], v[88:91]\n" \
"s_add_u32 m0, s89, 65536\n" \
"ds_read_b128 a[16:19], v200 offset:4096\n" \
"v_lshl_add_u32 v245, v246, 12, v245\n" \
"v_mfma_f32_16x16x32_bf16 v[96:99], a[24:27], a[208:211], v[96:99]\n" \
"s_xor_b32 s73, s73, 0x180\n" \
"s_lshl_b32 s71, s70, 10\n" \
"ds_read_b128 a[20:23], v201 offset:4096\n" \
"v_mfma_f32_16x16x32_bf16 v[96:99], a[28:31], a[212:215], v[96:99]\n" \
"buffer_load_dwordx4 v196, s[44:47], s75 offen lds\n" \
"v_mfma_f32_16x16x32_bf16 v[100:103], a[24:27], a[216:219], v[100:103]\n" \
"s_xor_b32 s75, s75, 0x180\n" \
"s_mul_i32 s76, s80, 393216\n" \
"ds_read_b64_tr_b16 a[54:55], v222 offset:35840\n" \
"v_mfma_f32_16x16x32_bf16 v[100:103], a[28:31], a[220:223], v[100:103]\n" \
"s_add_u32 s76, s76, s78\n" \
"s_add_u32 s71, s71, %3\n" \
"ds_read_b128 a[24:27], v200 offset:8192\n" \
"v_mfma_f32_16x16x32_bf16 v[108:111], a[192:195], a[216:219], v[108:111]\n" \
"s_add_u32 m0, s91, 86016\n" \
"ds_read_b128 a[28:31], v201 offset:8192\n" \
"v_add_u32 v212, s71, v245\n" \
"v_mfma_f32_16x16x32_bf16 v[108:111], a[196:199], a[220:223], v[108:111]\n" \
"buffer_load_dwordx4 v197, s[48:51], s76 offen offset:0 lds\n" \
"v_mfma_f32_16x16x32_bf16 v[104:107], a[192:195], a[208:211], v[104:107]\n" \
"s_mov_b32 s69, 1\n" \
"s_mov_b32 s30, 0x0\n" \
"s_mov_b32 s85, 98304\n" \
"v_mfma_f32_16x16x32_bf16 v[104:107], a[196:199], a[212:215], v[104:107]\n" \
"s_waitcnt lgkmcnt(2)\n" \
"s_sub_u32 s85, s85, 32768\n" \
"s_cselect_b32 s85, 131072, s85\n" \
"v_add_u32 v213, s85, v211\n" \
"v_mfma_f32_16x16x32_bf16 v[32:35], a[32:35], a[0:3], v[32:35]\n" \
"s_add_u32 s77, s76, 24576\n" \
"ds_read_b128 a[192:195], v200 offset:12288\n" \
"v_add_u32 v214, s85, v212\n" \
"v_mfma_f32_16x16x32_bf16 v[32:35], a[36:39], a[4:7], v[32:35]\n" \
"buffer_load_dwordx4 v198, s[48:51], s76 offen offset:1024 lds\n" \
"v_mfma_f32_16x16x32_bf16 v[40:43], a[32:35], a[8:11], v[40:43]\n" \
"ds_read_b128 a[196:199], v201 offset:12288\n" \
"v_xor_b32 v215, 32, v214\n" \
"v_mfma_f32_16x16x32_bf16 v[40:43], a[36:39], a[12:15], v[40:43]\n" \
"ds_read_b64_tr_b16 a[200:201], v222 offset:21504\n" \
"v_mov_b32 v230, s60\n" \
"v_mfma_f32_16x16x32_bf16 v[0:3], a[40:43], a[8:11], v[0:3]\n" \
"s_xor_b32 s80, s80, 0x3\n" \
"ds_read_b64_tr_b16 a[202:203], v222 offset:24576\n" \
"v_lshlrev_b32 v231, 28, s61\n" \
"v_mfma_f32_16x16x32_bf16 v[0:3], a[44:47], a[12:15], v[0:3]\n" \
"buffer_load_dwordx4 v199, s[48:51], s76 offen offset:2048 lds\n" \
"v_mfma_f32_16x16x32_bf16 v[4:7], a[48:51], a[8:11], v[4:7]\n" \
"ds_read_b64_tr_b16 a[204:205], v222 offset:33792\n" \
"v_mfma_f32_16x16x32_bf16 v[4:7], a[52:55], a[12:15], v[4:7]\n" \
"ds_read_b64_tr_b16 a[206:207], v222 offset:36864\n" \
"v_mfma_f32_16x16x32_bf16 v[12:15], a[48:51], a[16:19], v[12:15]\n" \
"s_add_u32 m0, m0, 3072\n" \
"ds_read_b64_tr_b16 a[208:209], v204 offset:22528\n" \
"v_mfma_f32_16x16x32_bf16 v[12:15], a[52:55], a[20:23], v[12:15]\n" \
"buffer_load_dwordx4 v197, s[48:51], s77 offen offset:0 lds\n" \
"v_mfma_f32_16x16x32_bf16 v[8:11], a[40:43], a[16:19], v[8:11]\n" \
"ds_read_b64_tr_b16 a[210:211], v204 offset:25600\n" \
"v_mfma_f32_16x16x32_bf16 v[8:11], a[44:47], a[20:23], v[8:11]\n" \
"ds_read_b64_tr_b16 a[212:213], v204 offset:34816\n" \
"v_mfma_f32_16x16x32_bf16 v[48:51], a[32:35], a[16:19], v[48:51]\n" \
"s_waitcnt lgkmcnt(3)\n" \
"ds_read_b64_tr_b16 a[214:215], v204 offset:37888\n" \
"v_mfma_f32_16x16x32_bf16 v[48:51], a[36:39], a[20:23], v[48:51]\n" \
"buffer_load_dwordx4 v198, s[48:51], s77 offen offset:1024 lds\n" \
"v_mfma_f32_16x16x32_bf16 v[56:59], a[32:35], a[24:27], v[56:59]\n" \
"ds_read_b64_tr_b16 a[216:217], v222 offset:22528\n" \
"v_mfma_f32_16x16x32_bf16 v[56:59], a[36:39], a[28:31], v[56:59]\n" \
"ds_read_b64_tr_b16 a[218:219], v222 offset:25600\n" \
"v_mfma_f32_16x16x32_bf16 v[16:19], a[40:43], a[24:27], v[16:19]\n" \
"ds_read_b64_tr_b16 a[220:221], v222 offset:34816\n" \
"v_mfma_f32_16x16x32_bf16 v[16:19], a[44:47], a[28:31], v[16:19]\n" \
"buffer_load_dwordx4 v199, s[48:51], s77 offen offset:2048 lds\n" \
"v_mfma_f32_16x16x32_bf16 v[20:23], a[48:51], a[24:27], v[20:23]\n" \
"ds_read_b64_tr_b16 a[222:223], v222 offset:37888\n" \
"v_mfma_f32_16x16x32_bf16 v[20:23], a[52:55], a[28:31], v[20:23]\n" \
"v_mfma_f32_16x16x32_bf16 v[28:31], a[48:51], a[192:195], v[28:31]\n" \
"v_mfma_f32_16x16x32_bf16 v[28:31], a[52:55], a[196:199], v[28:31]\n" \
"v_mfma_f32_16x16x32_bf16 v[24:27], a[40:43], a[192:195], v[24:27]\n" \
"v_mfma_f32_16x16x32_bf16 v[24:27], a[44:47], a[196:199], v[24:27]\n" \
"v_mfma_f32_16x16x32_bf16 v[64:67], a[32:35], a[192:195], v[64:67]\n" \
"v_mfma_f32_16x16x32_bf16 v[64:67], a[36:39], a[196:199], v[64:67]\n" \
"s_waitcnt vmcnt(12) lgkmcnt(0)\n" \
"s_barrier\n" \
"v_mfma_f32_16x16x32_bf16 v[68:71], a[200:203], a[192:195], v[68:71]\n" \
"ds_read_b64_tr_b16 a[32:33], v194 offset:1024\n" \
"ds_read_b64_tr_b16 a[34:35], v194 offset:4096\n" \
"v_mfma_f32_16x16x32_bf16 v[68:71], a[204:207], a[196:199], v[68:71]\n" \
"ds_read_b64_tr_b16 a[36:37], v194 offset:13312\n" \
"ds_read_b64_tr_b16 a[38:39], v194 offset:16384\n" \
"v_mfma_f32_16x16x32_bf16 v[60:63], a[200:203], a[24:27], v[60:63]\n" \
"s_add_u32 m0, s79, 0\n" \
"ds_read_b64_tr_b16 a[40:41], v194 offset:0\n" \
"v_mfma_f32_16x16x32_bf16 v[60:63], a[204:207], a[28:31], v[60:63]\n" \
"buffer_load_dwordx4 v192, s[44:47], s73 offen offset:0 lds\n" \
"v_mfma_f32_16x16x32_bf16 v[52:55], a[200:203], a[16:19], v[52:55]\n" \
"ds_read_b64_tr_b16 a[42:43], v194 offset:3072\n" \
"v_mfma_f32_16x16x32_bf16 v[52:55], a[204:207], a[20:23], v[52:55]\n" \
"ds_read_b64_tr_b16 a[44:45], v194 offset:12288\n" \
"v_mfma_f32_16x16x32_bf16 v[44:47], a[200:203], a[8:11], v[44:47]\n" \
"ds_read_b64_tr_b16 a[46:47], v194 offset:15360\n" \
"v_mfma_f32_16x16x32_bf16 v[44:47], a[204:207], a[12:15], v[44:47]\n" \
"buffer_load_dwordx4 v193, s[44:47], s73 offen offset:1024 lds\n" \
"v_mfma_f32_16x16x32_bf16 v[72:75], a[0:3], a[208:211], v[72:75]\n" \
"ds_read_b64_tr_b16 a[48:49], v195 offset:0\n" \
"v_mfma_f32_16x16x32_bf16 v[72:75], a[4:7], a[212:215], v[72:75]\n" \
"ds_read_b128 a[0:3], v207 offset:45056\n" \
"v_mfma_f32_16x16x32_bf16 v[80:83], a[8:11], a[208:211], v[80:83]\n" \
"ds_read_b128 a[4:7], v208 offset:45056\n" \
"v_mfma_f32_16x16x32_bf16 v[80:83], a[12:15], a[212:215], v[80:83]\n" \
"buffer_load_dwordx4 v192, s[44:47], s73 offen offset:2048 lds\n" \
"v_mfma_f32_16x16x32_bf16 v[84:87], a[8:11], a[216:219], v[84:87]\n" \
"ds_read_b64_tr_b16 a[50:51], v195 offset:3072\n" \
"v_mfma_f32_16x16x32_bf16 v[84:87], a[12:15], a[220:223], v[84:87]\n" \
"ds_read_b128 a[8:11], v205 offset:45056\n" \
"v_mfma_f32_16x16x32_bf16 v[92:95], a[16:19], a[216:219], v[92:95]\n" \
"ds_read_b128 a[12:15], v206 offset:45056\n" \
"v_mfma_f32_16x16x32_bf16 v[92:95], a[20:23], a[220:223], v[92:95]\n" \
"buffer_load_dwordx4 v193, s[44:47], s73 offen offset:3072 lds\n" \
"v_mfma_f32_16x16x32_bf16 v[88:91], a[16:19], a[208:211], v[88:91]\n" \
"ds_read_b64_tr_b16 a[52:53], v195 offset:12288\n" \
"v_mfma_f32_16x16x32_bf16 v[88:91], a[20:23], a[212:215], v[88:91]\n" \
"s_add_u32 m0, s89, 0\n" \
"ds_read_b128 a[16:19], v205 offset:49152\n" \
"v_mfma_f32_16x16x32_bf16 v[96:99], a[24:27], a[208:211], v[96:99]\n" \
"s_xor_b32 s73, s73, 0x300\n" \
"ds_read_b128 a[20:23], v206 offset:49152\n" \
"v_mfma_f32_16x16x32_bf16 v[96:99], a[28:31], a[212:215], v[96:99]\n" \
"buffer_load_dwordx4 v196, s[44:47], s75 offen lds\n" \
"v_mfma_f32_16x16x32_bf16 v[100:103], a[24:27], a[216:219], v[100:103]\n" \
"s_xor_b32 s75, s75, 0x300\n" \
"s_mul_i32 s76, s80, 393216\n" \
"ds_read_b64_tr_b16 a[54:55], v195 offset:15360\n" \
"v_mfma_f32_16x16x32_bf16 v[100:103], a[28:31], a[220:223], v[100:103]\n" \
"s_add_u32 s76, s76, s78\n" \
"ds_read_b128 a[24:27], v205 offset:53248\n" \
"v_mfma_f32_16x16x32_bf16 v[108:111], a[192:195], a[216:219], v[108:111]\n" \
"s_add_u32 m0, s91, 20480\n" \
"ds_read_b128 a[28:31], v206 offset:53248\n" \
"v_mfma_f32_16x16x32_bf16 v[108:111], a[196:199], a[220:223], v[108:111]\n" \
"buffer_load_dwordx4 v197, s[48:51], s76 offen offset:0 lds\n" \
"v_mfma_f32_16x16x32_bf16 v[104:107], a[192:195], a[208:211], v[104:107]\n" \
"v_mfma_f32_16x16x32_bf16 v[104:107], a[196:199], a[212:215], v[104:107]\n" \
"s_waitcnt lgkmcnt(2)\n" \
"v_mfma_f32_16x16x32_bf16 v[32:35], a[32:35], a[0:3], v[32:35]\n" \
"s_add_u32 s77, s76, 24576\n" \
"ds_read_b128 a[192:195], v205 offset:57344\n" \
"v_mfma_f32_16x16x32_bf16 v[32:35], a[36:39], a[4:7], v[32:35]\n" \
"buffer_load_dwordx4 v198, s[48:51], s76 offen offset:1024 lds\n" \
"v_mfma_f32_16x16x32_bf16 v[40:43], a[32:35], a[8:11], v[40:43]\n" \
"ds_read_b128 a[196:199], v206 offset:57344\n" \
"v_mfma_f32_16x16x32_bf16 v[40:43], a[36:39], a[12:15], v[40:43]\n" \
"ds_read_b64_tr_b16 a[200:201], v195 offset:1024\n" \
"v_mfma_f32_16x16x32_bf16 v[0:3], a[40:43], a[8:11], v[0:3]\n" \
"s_xor_b32 s80, s80, 0x6\n" \
"ds_read_b64_tr_b16 a[202:203], v195 offset:4096\n" \
"v_mfma_f32_16x16x32_bf16 v[0:3], a[44:47], a[12:15], v[0:3]\n" \
"buffer_load_dwordx4 v199, s[48:51], s76 offen offset:2048 lds\n" \
"v_mfma_f32_16x16x32_bf16 v[4:7], a[48:51], a[8:11], v[4:7]\n" \
"ds_read_b64_tr_b16 a[204:205], v195 offset:13312\n" \
"v_mfma_f32_16x16x32_bf16 v[4:7], a[52:55], a[12:15], v[4:7]\n" \
"ds_read_b64_tr_b16 a[206:207], v195 offset:16384\n" \
"v_mfma_f32_16x16x32_bf16 v[12:15], a[48:51], a[16:19], v[12:15]\n" \
"s_add_u32 m0, m0, 3072\n" \
"ds_read_b64_tr_b16 a[208:209], v194 offset:2048\n" \
"v_mfma_f32_16x16x32_bf16 v[12:15], a[52:55], a[20:23], v[12:15]\n" \
"buffer_load_dwordx4 v197, s[48:51], s77 offen offset:0 lds\n" \
"v_mfma_f32_16x16x32_bf16 v[8:11], a[40:43], a[16:19], v[8:11]\n" \
"ds_read_b64_tr_b16 a[210:211], v194 offset:5120\n" \
"v_mfma_f32_16x16x32_bf16 v[8:11], a[44:47], a[20:23], v[8:11]\n" \
"ds_read_b64_tr_b16 a[212:213], v194 offset:14336\n" \
"v_mfma_f32_16x16x32_bf16 v[48:51], a[32:35], a[16:19], v[48:51]\n" \
"s_waitcnt lgkmcnt(3)\n" \
"ds_read_b64_tr_b16 a[214:215], v194 offset:17408\n" \
"v_mfma_f32_16x16x32_bf16 v[48:51], a[36:39], a[20:23], v[48:51]\n" \
"buffer_load_dwordx4 v198, s[48:51], s77 offen offset:1024 lds\n" \
"v_mfma_f32_16x16x32_bf16 v[56:59], a[32:35], a[24:27], v[56:59]\n" \
"ds_read_b64_tr_b16 a[216:217], v195 offset:2048\n" \
"v_mfma_f32_16x16x32_bf16 v[56:59], a[36:39], a[28:31], v[56:59]\n" \
"ds_read_b64_tr_b16 a[218:219], v195 offset:5120\n" \
"v_mfma_f32_16x16x32_bf16 v[16:19], a[40:43], a[24:27], v[16:19]\n" \
"ds_read_b64_tr_b16 a[220:221], v195 offset:14336\n" \
"v_mfma_f32_16x16x32_bf16 v[16:19], a[44:47], a[28:31], v[16:19]\n" \
"buffer_load_dwordx4 v199, s[48:51], s77 offen offset:2048 lds\n" \
"v_mfma_f32_16x16x32_bf16 v[20:23], a[48:51], a[24:27], v[20:23]\n" \
"ds_read_b64_tr_b16 a[222:223], v195 offset:17408\n" \
"v_mfma_f32_16x16x32_bf16 v[20:23], a[52:55], a[28:31], v[20:23]\n" \
"v_mfma_f32_16x16x32_bf16 v[28:31], a[48:51], a[192:195], v[28:31]\n" \
"v_mfma_f32_16x16x32_bf16 v[28:31], a[52:55], a[196:199], v[28:31]\n" \
"v_mfma_f32_16x16x32_bf16 v[24:27], a[40:43], a[192:195], v[24:27]\n" \
"v_mfma_f32_16x16x32_bf16 v[24:27], a[44:47], a[196:199], v[24:27]\n" \
"v_mfma_f32_16x16x32_bf16 v[64:67], a[32:35], a[192:195], v[64:67]\n" \
"v_mfma_f32_16x16x32_bf16 v[64:67], a[36:39], a[196:199], v[64:67]\n" \
"s_waitcnt vmcnt(11) lgkmcnt(0)\n" \
"s_barrier\n" \
"v_mfma_f32_16x16x32_bf16 v[68:71], a[200:203], a[192:195], v[68:71]\n" \
"ds_read_b64_tr_b16 a[32:33], v209 offset:1024\n" \
"ds_read_b64_tr_b16 a[34:35], v209 offset:4096\n" \
"v_mfma_f32_16x16x32_bf16 v[68:71], a[204:207], a[196:199], v[68:71]\n" \
"ds_read_b64_tr_b16 a[36:37], v209 offset:13312\n" \
"ds_read_b64_tr_b16 a[38:39], v209 offset:16384\n" \
"v_mfma_f32_16x16x32_bf16 v[60:63], a[200:203], a[24:27], v[60:63]\n" \
"s_add_u32 m0, s79, 110592\n" \
"ds_read_b64_tr_b16 a[40:41], v209 offset:0\n" \
"v_cmp_eq_u32 vcc, s60, v226\n" \
"v_mfma_f32_16x16x32_bf16 v[60:63], a[204:207], a[28:31], v[60:63]\n" \
"buffer_load_dwordx4 v192, s[44:47], s73 offen offset:0 lds\n" \
"v_mfma_f32_16x16x32_bf16 v[52:55], a[200:203], a[16:19], v[52:55]\n" \
"s_andn2_b32 s70, 0xff, vcc_lo\n" \
"s_cbranch_scc1 Lpl4_%=\n" \
"Lpr4_%=:\n" \
"ds_read_b64_tr_b16 a[42:43], v209 offset:3072\n" \
"v_mfma_f32_16x16x32_bf16 v[52:55], a[204:207], a[20:23], v[52:55]\n" \
"s_and_b32 s70, %1, 7\n" \
"ds_read_b64_tr_b16 a[44:45], v209 offset:12288\n" \
"v_readlane_b32 s70, v227, s70\n" \
"v_mfma_f32_16x16x32_bf16 v[44:47], a[200:203], a[8:11], v[44:47]\n" \
"ds_read_b64_tr_b16 a[46:47], v209 offset:15360\n" \
"v_mfma_f32_16x16x32_bf16 v[44:47], a[204:207], a[12:15], v[44:47]\n" \
"buffer_load_dwordx4 v193, s[44:47], s73 offen offset:1024 lds\n" \
"v_mfma_f32_16x16x32_bf16 v[72:75], a[0:3], a[208:211], v[72:75]\n" \
"ds_read_b64_tr_b16 a[48:49], v210 offset:0\n" \
"v_subrev_u32 v227, s70, v227\n" \
"v_mfma_f32_16x16x32_bf16 v[72:75], a[4:7], a[212:215], v[72:75]\n" \
"ds_read_b128 a[0:3], v207 offset:0\n" \
"v_cmp_gt_i32 vcc, 0, v227\n" \
"v_mfma_f32_16x16x32_bf16 v[80:83], a[8:11], a[208:211], v[80:83]\n" \
"ds_read_b128 a[4:7], v208 offset:0\n" \
"v_mfma_f32_16x16x32_bf16 v[80:83], a[12:15], a[212:215], v[80:83]\n" \
"buffer_load_dwordx4 v192, s[44:47], s73 offen offset:2048 lds\n" \
"v_mfma_f32_16x16x32_bf16 v[84:87], a[8:11], a[216:219], v[84:87]\n" \
"s_and_b32 s13, vcc_lo, 0xff\n" \
"s_bcnt1_i32_b32 s13, s13\n" \
"ds_read_b64_tr_b16 a[50:51], v210 offset:3072\n" \
"v_mfma_f32_16x16x32_bf16 v[84:87], a[12:15], a[220:223], v[84:87]\n" \
"s_lshr_b32 s90, s13, 2\n" \
"s_mul_i32 s70, s90, 7\n" \
"ds_read_b128 a[8:11], v205 offset:0\n" \
"v_mfma_f32_16x16x32_bf16 v[92:95], a[16:19], a[216:219], v[92:95]\n" \
"s_xor_b32 s13, s13, s70\n" \
"s_lshl_b32 s70, s13, 3\n" \
"ds_read_b128 a[12:15], v206 offset:0\n" \
"v_mfma_f32_16x16x32_bf16 v[92:95], a[20:23], a[220:223], v[92:95]\n" \
"buffer_load_dwordx4 v193, s[44:47], s73 offen offset:3072 lds\n" \
"v_mfma_f32_16x16x32_bf16 v[88:91], a[16:19], a[208:211], v[88:91]\n" \
"s_lshr_b32 s9, 0x2a2b2d2f, s70\n" \
"s_and_b32 s9, s9, 0xff\n" \
"ds_read_b64_tr_b16 a[52:53], v210 offset:12288\n" \
"v_mfma_f32_16x16x32_bf16 v[88:91], a[20:23], a[212:215], v[88:91]\n" \
"s_add_u32 m0, s89, 110592\n" \
"s_lshr_b32 s8, %1, 6\n" \
"ds_read_b128 a[16:19], v205 offset:4096\n" \
"v_mfma_f32_16x16x32_bf16 v[96:99], a[24:27], a[208:211], v[96:99]\n" \
"s_xor_b32 s73, s73, 0x80\n" \
"s_lshl2_add_u32 s8, s13, s8\n" \
"ds_read_b128 a[20:23], v206 offset:4096\n" \
"v_mfma_f32_16x16x32_bf16 v[96:99], a[28:31], a[212:215], v[96:99]\n" \
"buffer_load_dwordx4 v196, s[44:47], s75 offen lds\n" \
"v_mfma_f32_16x16x32_bf16 v[100:103], a[24:27], a[216:219], v[100:103]\n" \
"s_xor_b32 s75, s75, 0x80\n" \
"s_mul_i32 s76, s80, 393216\n" \
"ds_read_b64_tr_b16 a[54:55], v210 offset:15360\n" \
"v_mfma_f32_16x16x32_bf16 v[100:103], a[28:31], a[220:223], v[100:103]\n" \
"s_add_u32 s76, s76, s78\n" \
"ds_read_b128 a[24:27], v205 offset:8192\n" \
"v_max_i32_dpp v226, v227, v227 quad_perm:[1,0,3,2] row_mask:0xf bank_mask:0xf\n" \
"v_mfma_f32_16x16x32_bf16 v[108:111], a[192:195], a[216:219], v[108:111]\n" \
"s_add_u32 m0, s91, 139264\n" \
"s_mov_b32 s14, 0\n" \
"ds_read_b128 a[28:31], v206 offset:8192\n" \
"v_mfma_f32_16x16x32_bf16 v[108:111], a[196:199], a[220:223], v[108:111]\n" \
"buffer_load_dwordx4 v197, s[48:51], s76 offen offset:0 lds\n" \
"v_mfma_f32_16x16x32_bf16 v[104:107], a[192:195], a[208:211], v[104:107]\n" \
"v_readlane_b32 s15, v226, 2\n" \
"v_readlane_b32 s16, v226, 4\n" \
"v_mfma_f32_16x16x32_bf16 v[104:107], a[196:199], a[212:215], v[104:107]\n" \
"s_waitcnt lgkmcnt(2)\n" \
"v_readlane_b32 s17, v226, 6\n" \
"v_cmp_gt_i32 vcc, s15, v226\n" \
"v_mfma_f32_16x16x32_bf16 v[32:35], a[32:35], a[0:3], v[32:35]\n" \
"s_add_u32 s77, s76, 24576\n" \
"ds_read_b128 a[192:195], v205 offset:12288\n" \
"v_mfma_f32_16x16x32_bf16 v[32:35], a[36:39], a[4:7], v[32:35]\n" \
"buffer_load_dwordx4 v198, s[48:51], s76 offen offset:1024 lds\n" \
"v_mfma_f32_16x16x32_bf16 v[40:43], a[32:35], a[8:11], v[40:43]\n" \
"s_and_b32 s70, vcc_lo, 0x55\n" \
"s_bcnt1_i32_b32 s70, s70\n" \
"ds_read_b128 a[196:199], v206 offset:12288\n" \
"v_mfma_f32_16x16x32_bf16 v[40:43], a[36:39], a[12:15], v[40:43]\n" \
"s_cmp_eq_u32 s70, s13\n" \
"s_cselect_b32 s14, 4, s14\n" \
"ds_read_b64_tr_b16 a[200:201], v210 offset:1024\n" \
"v_mfma_f32_16x16x32_bf16 v[0:3], a[40:43], a[8:11], v[0:3]\n" \
"s_xor_b32 s80, s80, 0x1\n" \
"ds_read_b64_tr_b16 a[202:203], v210 offset:4096\n" \
"v_cmp_gt_i32 vcc, s16, v226\n" \
"v_mfma_f32_16x16x32_bf16 v[0:3], a[44:47], a[12:15], v[0:3]\n" \
"buffer_load_dwordx4 v199, s[48:51], s76 offen offset:2048 lds\n" \
"v_mfma_f32_16x16x32_bf16 v[4:7], a[48:51], a[8:11], v[4:7]\n" \
"s_and_b32 s70, vcc_lo, 0x55\n" \
"s_bcnt1_i32_b32 s70, s70\n" \
"ds_read_b64_tr_b16 a[204:205], v210 offset:13312\n" \
"v_mfma_f32_16x16x32_bf16 v[4:7], a[52:55], a[12:15], v[4:7]\n" \
"s_cmp_eq_u32 s70, s13\n" \
"s_cselect_b32 s14, 8, s14\n" \
"ds_read_b64_tr_b16 a[206:207], v210 offset:16384\n" \
"v_mfma_f32_16x16x32_bf16 v[12:15], a[48:51], a[16:19], v[12:15]\n" \
"s_add_u32 m0, m0, 3072\n" \
"ds_read_b64_tr_b16 a[208:209], v209 offset:2048\n" \
"v_cmp_gt_i32 vcc, s17, v226\n" \
"v_mfma_f32_16x16x32_bf16 v[12:15], a[52:55], a[20:23], v[12:15]\n" \
"buffer_load_dwordx4 v197, s[48:51], s77 offen offset:0 lds\n" \
"v_mfma_f32_16x16x32_bf16 v[8:11], a[40:43], a[16:19], v[8:11]\n" \
"s_and_b32 s70, vcc_lo, 0x55\n" \
"s_bcnt1_i32_b32 s70, s70\n" \
"ds_read_b64_tr_b16 a[210:211], v209 offset:5120\n" \
"v_mfma_f32_16x16x32_bf16 v[8:11], a[44:47], a[20:23], v[8:11]\n" \
"s_cmp_eq_u32 s70, s13\n" \
"s_cselect_b32 s14, 12, s14\n" \
"ds_read_b64_tr_b16 a[212:213], v209 offset:14336\n" \
"v_mfma_f32_16x16x32_bf16 v[48:51], a[32:35], a[16:19], v[48:51]\n" \
"s_lshr_b32 s70, %1, 6\n" \
"s_add_u32 s14, s14, s70\n" \
"ds_read_b64_tr_b16 a[214:215], v209 offset:17408\n" \
"v_mfma_f32_16x16x32_bf16 v[48:51], a[36:39], a[20:23], v[48:51]\n" \
"s_waitcnt lgkmcnt(3)\n" \
"buffer_load_dwordx4 v198, s[48:51], s77 offen offset:1024 lds\n" \
"v_mfma_f32_16x16x32_bf16 v[56:59], a[32:35], a[24:27], v[56:59]\n" \
"s_cmp_eq_u32 s90, 0\n" \
"s_cselect_b32 s14, s8, s14\n" \
"ds_read_b64_tr_b16 a[216:217], v210 offset:2048\n" \
"v_mfma_f32_16x16x32_bf16 v[56:59], a[36:39], a[28:31], v[56:59]\n" \
"s_sub_u32 s10, 49, s9\n" \
"s_lshl_b32 s11, s9, 1\n" \
"ds_read_b64_tr_b16 a[218:219], v210 offset:5120\n" \
"v_mfma_f32_16x16x32_bf16 v[16:19], a[40:43], a[24:27], v[16:19]\n" \
"s_sub_u32 s11, s11, 71\n" \
"s_lshl2_add_u32 s70, s8, s61\n" \
"ds_read_b64_tr_b16 a[220:221], v210 offset:14336\n" \
"v_mfma_f32_16x16x32_bf16 v[16:19], a[44:47], a[28:31], v[16:19]\n" \
"buffer_load_dwordx4 v199, s[48:51], s77 offen offset:2048 lds\n" \
"v_mfma_f32_16x16x32_bf16 v[20:23], a[48:51], a[24:27], v[20:23]\n" \
"s_mul_i32 s81, s70, 266240\n" \
"s_mul_i32 s82, s90, s9\n" \
"ds_read_b64_tr_b16 a[222:223], v210 offset:17408\n" \
"v_mfma_f32_16x16x32_bf16 v[20:23], a[52:55], a[28:31], v[20:23]\n" \
"s_lshl_b32 s71, s82, 12\n" \
"s_add_u32 s81, s81, s71\n" \
"s_add_u32 s81, s81, 35700736\n" \
"v_mfma_f32_16x16x32_bf16 v[28:31], a[48:51], a[192:195], v[28:31]\n" \
"s_lshl2_add_u32 s70, s14, s61\n" \
"s_mul_i32 s83, s70, 69632\n" \
"s_add_u32 s83, s83, 52740096\n" \
"v_mfma_f32_16x16x32_bf16 v[28:31], a[52:55], a[196:199], v[28:31]\n" \
"s_lshl_b32 s82, s82, 17\n" \
"s_add_u32 s82, s82, s84\n" \
"s_lshl_b32 s70, s14, 19\n" \
"v_mfma_f32_16x16x32_bf16 v[24:27], a[40:43], a[192:195], v[24:27]\n" \
"s_add_u32 s88, s88, s70\n" \
"s_lshl_b32 s71, s90, 11\n" \
"s_add_u32 s88, s88, s71\n" \
"v_mfma_f32_16x16x32_bf16 v[24:27], a[44:47], a[196:199], v[24:27]\n" \
"s_lshl3_add_u32 s70, s8, s68\n" \
"s_lshl2_add_u32 s87, s70, s61\n" \
"s_lshl_b32 s87, s87, 14\n" \
"v_mfma_f32_16x16x32_bf16 v[64:67], a[32:35], a[192:195], v[64:67]\n" \
"s_add_u32 s87, s87, 57196544\n" \
"s_lshl_b32 s64, s70, 7\n" \
"s_add_u32 s64, s64, 65601536\n" \
"v_mfma_f32_16x16x32_bf16 v[64:67], a[36:39], a[196:199], v[64:67]\n" \
"s_lshl_b32 s66, s8, 7\n" \
"s_waitcnt vmcnt(11) lgkmcnt(0)\n" \
"s_barrier\n" \
"v_mfma_f32_16x16x32_bf16 v[68:71], a[200:203], a[192:195], v[68:71]\n" \
"s_lshl_b32 s67, s14, 7\n" \
"ds_read_b64_tr_b16 a[32:33], v204 offset:21504\n" \
"ds_read_b64_tr_b16 a[34:35], v204 offset:24576\n" \
"v_mfma_f32_16x16x32_bf16 v[68:71], a[204:207], a[196:199], v[68:71]\n" \
"s_add_u32 s67, s67, 65591296\n" \
"ds_read_b64_tr_b16 a[36:37], v204 offset:33792\n" \
"ds_read_b64_tr_b16 a[38:39], v204 offset:36864\n" \
"v_mfma_f32_16x16x32_bf16 v[60:63], a[200:203], a[24:27], v[60:63]\n" \
"s_add_u32 m0, s79, 65536\n" \
"s_add_u32 s66, s66, 65589248\n" \
"ds_read_b64_tr_b16 a[40:41], v204 offset:20480\n" \
"v_mfma_f32_16x16x32_bf16 v[60:63], a[204:207], a[28:31], v[60:63]\n" \
"buffer_load_dwordx4 v192, s[44:47], s73 offen offset:0 lds\n" \
"v_mfma_f32_16x16x32_bf16 v[52:55], a[200:203], a[16:19], v[52:55]\n" \
"s_add_u32 s70, s9, 1\n" \
"s_sub_u32 s70, s70, s90\n" \
"ds_read_b64_tr_b16 a[42:43], v204 offset:23552\n" \
"v_mfma_f32_16x16x32_bf16 v[52:55], a[204:207], a[20:23], v[52:55]\n" \
"s_lshr_b32 s70, s70, 1\n" \
"s_bfm_b32 s65, s70, 0\n" \
"ds_read_b64_tr_b16 a[44:45], v204 offset:32768\n" \
"v_mfma_f32_16x16x32_bf16 v[44:47], a[200:203], a[8:11], v[44:47]\n" \
"s_sub_u32 s70, 0, s90\n" \
"s_xor_b32 s65, s65, s70\n" \
"ds_read_b64_tr_b16 a[46:47], v204 offset:35840\n" \
"v_mfma_f32_16x16x32_bf16 v[44:47], a[204:207], a[12:15], v[44:47]\n" \
"buffer_load_dwordx4 v193, s[44:47], s73 offen offset:1024 lds\n" \
"v_mfma_f32_16x16x32_bf16 v[72:75], a[0:3], a[208:211], v[72:75]\n" \
"s_and_b32 s74, s70, 0xffff\n" \
"s_xor_b32 s68, s68, s8\n" \
"ds_read_b64_tr_b16 a[48:49], v222 offset:20480\n" \
"v_mfma_f32_16x16x32_bf16 v[72:75], a[4:7], a[212:215], v[72:75]\n" \
"s_and_b32 s68, s68, 3\n" \
"s_lshl_b32 s68, s68, 12\n" \
"ds_read_b128 a[0:3], v202 offset:0\n" \
"v_mfma_f32_16x16x32_bf16 v[80:83], a[8:11], a[208:211], v[80:83]\n" \
"ds_read_b128 a[4:7], v203 offset:0\n" \
"v_mfma_f32_16x16x32_bf16 v[80:83], a[12:15], a[212:215], v[80:83]\n" \
"buffer_load_dwordx4 v192, s[44:47], s73 offen offset:2048 lds\n" \
"v_mfma_f32_16x16x32_bf16 v[84:87], a[8:11], a[216:219], v[84:87]\n" \
"ds_read_b64_tr_b16 a[50:51], v222 offset:23552\n" \
"v_mfma_f32_16x16x32_bf16 v[84:87], a[12:15], a[220:223], v[84:87]\n" \
"ds_read_b128 a[8:11], v200 offset:0\n" \
"v_mfma_f32_16x16x32_bf16 v[92:95], a[16:19], a[216:219], v[92:95]\n" \
"ds_read_b128 a[12:15], v201 offset:0\n" \
"v_mfma_f32_16x16x32_bf16 v[92:95], a[20:23], a[220:223], v[92:95]\n" \
"buffer_load_dwordx4 v193, s[44:47], s73 offen offset:3072 lds\n" \
"v_mfma_f32_16x16x32_bf16 v[88:91], a[16:19], a[208:211], v[88:91]\n" \
"ds_read_b64_tr_b16 a[52:53], v222 offset:32768\n" \
"v_mfma_f32_16x16x32_bf16 v[88:91], a[20:23], a[212:215], v[88:91]\n" \
"s_add_u32 m0, s89, 65536\n" \
"ds_read_b128 a[16:19], v200 offset:4096\n" \
"v_mfma_f32_16x16x32_bf16 v[96:99], a[24:27], a[208:211], v[96:99]\n" \
"s_xor_b32 s73, s73, 0x180\n" \
"ds_read_b128 a[20:23], v201 offset:4096\n" \
"v_mfma_f32_16x16x32_bf16 v[96:99], a[28:31], a[212:215], v[96:99]\n" \
"buffer_load_dwordx4 v196, s[44:47], s75 offen lds\n" \
"v_mfma_f32_16x16x32_bf16 v[100:103], a[24:27], a[216:219], v[100:103]\n" \
"s_xor_b32 s75, s75, 0x180\n" \
"s_mul_i32 s76, s80, 393216\n" \
"ds_read_b64_tr_b16 a[54:55], v222 offset:35840\n" \
"v_mfma_f32_16x16x32_bf16 v[100:103], a[28:31], a[220:223], v[100:103]\n" \
"s_add_u32 s76, s76, s78\n" \
"ds_read_b128 a[24:27], v200 offset:8192\n" \
"v_mfma_f32_16x16x32_bf16 v[108:111], a[192:195], a[216:219], v[108:111]\n" \
"s_add_u32 m0, s91, 86016\n" \
"ds_read_b128 a[28:31], v201 offset:8192\n" \
"v_mfma_f32_16x16x32_bf16 v[108:111], a[196:199], a[220:223], v[108:111]\n" \
"buffer_load_dwordx4 v197, s[48:51], s76 offen offset:0 lds\n" \
"v_mfma_f32_16x16x32_bf16 v[104:107], a[192:195], a[208:211], v[104:107]\n" \
"v_mfma_f32_16x16x32_bf16 v[104:107], a[196:199], a[212:215], v[104:107]\n" \
"s_waitcnt lgkmcnt(2)\n" \
"v_mfma_f32_16x16x32_bf16 v[32:35], a[32:35], a[0:3], v[32:35]\n" \
"s_add_u32 s77, s76, 24576\n" \
"ds_read_b128 a[192:195], v200 offset:12288\n" \
"v_mfma_f32_16x16x32_bf16 v[32:35], a[36:39], a[4:7], v[32:35]\n" \
"buffer_load_dwordx4 v198, s[48:51], s76 offen offset:1024 lds\n" \
"v_mfma_f32_16x16x32_bf16 v[40:43], a[32:35], a[8:11], v[40:43]\n" \
"ds_read_b128 a[196:199], v201 offset:12288\n" \
"v_mfma_f32_16x16x32_bf16 v[40:43], a[36:39], a[12:15], v[40:43]\n" \
"ds_read_b64_tr_b16 a[200:201], v222 offset:21504\n" \
"v_mfma_f32_16x16x32_bf16 v[0:3], a[40:43], a[8:11], v[0:3]\n" \
"s_xor_b32 s80, s80, 0x3\n" \
"ds_read_b64_tr_b16 a[202:203], v222 offset:24576\n" \
"v_mfma_f32_16x16x32_bf16 v[0:3], a[44:47], a[12:15], v[0:3]\n" \
"buffer_load_dwordx4 v199, s[48:51], s76 offen offset:2048 lds\n" \
"v_mfma_f32_16x16x32_bf16 v[4:7], a[48:51], a[8:11], v[4:7]\n" \
"ds_read_b64_tr_b16 a[204:205], v222 offset:33792\n" \
"v_mfma_f32_16x16x32_bf16 v[4:7], a[52:55], a[12:15], v[4:7]\n" \
"ds_read_b64_tr_b16 a[206:207], v222 offset:36864\n" \
"v_mfma_f32_16x16x32_bf16 v[12:15], a[48:51], a[16:19], v[12:15]\n" \
"s_add_u32 m0, m0, 3072\n" \
"ds_read_b64_tr_b16 a[208:209], v204 offset:22528\n" \
"v_mfma_f32_16x16x32_bf16 v[12:15], a[52:55], a[20:23], v[12:15]\n" \
"buffer_load_dwordx4 v197, s[48:51], s77 offen offset:0 lds\n" \
"v_mfma_f32_16x16x32_bf16 v[8:11], a[40:43], a[16:19], v[8:11]\n" \
"ds_read_b64_tr_b16 a[210:211], v204 offset:25600\n" \
"v_mfma_f32_16x16x32_bf16 v[8:11], a[44:47], a[20:23], v[8:11]\n" \
"ds_read_b64_tr_b16 a[212:213], v204 offset:34816\n" \
"v_mfma_f32_16x16x32_bf16 v[48:51], a[32:35], a[16:19], v[48:51]\n" \
"s_waitcnt lgkmcnt(3)\n" \
"ds_read_b64_tr_b16 a[214:215], v204 offset:37888\n" \
"v_mfma_f32_16x16x32_bf16 v[48:51], a[36:39], a[20:23], v[48:51]\n" \
"buffer_load_dwordx4 v198, s[48:51], s77 offen offset:1024 lds\n" \
"v_mfma_f32_16x16x32_bf16 v[56:59], a[32:35], a[24:27], v[56:59]\n" \
"ds_read_b64_tr_b16 a[216:217], v222 offset:22528\n" \
"v_mfma_f32_16x16x32_bf16 v[56:59], a[36:39], a[28:31], v[56:59]\n" \
"ds_read_b64_tr_b16 a[218:219], v222 offset:25600\n" \
"v_mfma_f32_16x16x32_bf16 v[16:19], a[40:43], a[24:27], v[16:19]\n" \
"ds_read_b64_tr_b16 a[220:221], v222 offset:34816\n" \
"v_mfma_f32_16x16x32_bf16 v[16:19], a[44:47], a[28:31], v[16:19]\n" \
"buffer_load_dwordx4 v199, s[48:51], s77 offen offset:2048 lds\n" \
"v_mfma_f32_16x16x32_bf16 v[20:23], a[48:51], a[24:27], v[20:23]\n" \
"ds_read_b64_tr_b16 a[222:223], v222 offset:37888\n" \
"v_mfma_f32_16x16x32_bf16 v[20:23], a[52:55], a[28:31], v[20:23]\n" \
"v_mfma_f32_16x16x32_bf16 v[28:31], a[48:51], a[192:195], v[28:31]\n" \
"v_mfma_f32_16x16x32_bf16 v[28:31], a[52:55], a[196:199], v[28:31]\n" \
"v_mfma_f32_16x16x32_bf16 v[24:27], a[40:43], a[192:195], v[24:27]\n" \
"v_mfma_f32_16x16x32_bf16 v[24:27], a[44:47], a[196:199], v[24:27]\n" \
"v_mfma_f32_16x16x32_bf16 v[64:67], a[32:35], a[192:195], v[64:67]\n" \
"v_mfma_f32_16x16x32_bf16 v[64:67], a[36:39], a[196:199], v[64:67]\n" \
"s_waitcnt vmcnt(11) lgkmcnt(0)\n" \
"s_barrier\n" \
"v_mfma_f32_16x16x32_bf16 v[68:71], a[200:203], a[192:195], v[68:71]\n" \
"ds_read_b64_tr_b16 a[32:33], v194 offset:1024\n" \
"ds_read_b64_tr_b16 a[34:35], v194 offset:4096\n" \
"v_mfma_f32_16x16x32_bf16 v[68:71], a[204:207], a[196:199], v[68:71]\n" \
"ds_read_b64_tr_b16 a[36:37], v194 offset:13312\n" \
"ds_read_b64_tr_b16 a[38:39], v194 offset:16384\n" \
"v_mfma_f32_16x16x32_bf16 v[60:63], a[200:203], a[24:27], v[60:63]\n" \
"s_add_u32 m0, s79, 0\n" \
"ds_read_b64_tr_b16 a[40:41], v194 offset:0\n" \
"v_mfma_f32_16x16x32_bf16 v[60:63], a[204:207], a[28:31], v[60:63]\n" \
"buffer_load_dwordx4 v192, s[44:47], s73 offen offset:0 lds\n" \
"v_mfma_f32_16x16x32_bf16 v[52:55], a[200:203], a[16:19], v[52:55]\n" \
"ds_read_b64_tr_b16 a[42:43], v194 offset:3072\n" \
"v_mfma_f32_16x16x32_bf16 v[52:55], a[204:207], a[20:23], v[52:55]\n" \
"Lpg9087_%=:\n" \
"Lst1x2_%=:\n" \
"s_cbranch_execz Lpg10938_%=\n" \
"ds_read_b64_tr_b16 a[44:45], v194 offset:12288\n" \
"v_mfma_f32_16x16x32_bf16 v[44:47], a[200:203], a[8:11], v[44:47]\n" \
"ds_read_b64_tr_b16 a[46:47], v194 offset:15360\n" \
"v_mfma_f32_16x16x32_bf16 v[44:47], a[204:207], a[12:15], v[44:47]\n" \
"buffer_load_dwordx4 v193, s[44:47], s73 offen offset:1024 lds\n" \
"v_mfma_f32_16x16x32_bf16 v[72:75], a[0:3], a[208:211], v[72:75]\n" \
"ds_read_b64_tr_b16 a[48:49], v195 offset:0\n" \
"v_mfma_f32_16x16x32_bf16 v[72:75], a[4:7], a[212:215], v[72:75]\n" \
"ds_read_b128 a[0:3], v207 offset:45056\n" \
"v_mfma_f32_16x16x32_bf16 v[80:83], a[8:11], a[208:211], v[80:83]\n" \
"ds_read_b128 a[4:7], v208 offset:45056\n" \
"v_mfma_f32_16x16x32_bf16 v[80:83], a[12:15], a[212:215], v[80:83]\n" \
"buffer_load_dwordx4 v192, s[44:47], s73 offen offset:2048 lds\n" \
"v_mfma_f32_16x16x32_bf16 v[84:87], a[8:11], a[216:219], v[84:87]\n" \
"ds_read_b64_tr_b16 a[50:51], v195 offset:3072\n" \
"v_mfma_f32_16x16x32_bf16 v[84:87], a[12:15], a[220:223], v[84:87]\n" \
"ds_read_b128 a[8:11], v205 offset:45056\n" \
"v_mfma_f32_16x16x32_bf16 v[92:95], a[16:19], a[216:219], v[92:95]\n" \
"ds_read_b128 a[12:15], v206 offset:45056\n" \
"v_mfma_f32_16x16x32_bf16 v[92:95], a[20:23], a[220:223], v[92:95]\n" \
"buffer_load_dwordx4 v193, s[44:47], s73 offen offset:3072 lds\n" \
"v_mfma_f32_16x16x32_bf16 v[88:91], a[16:19], a[208:211], v[88:91]\n" \
"ds_read_b64_tr_b16 a[52:53], v195 offset:12288\n" \
"v_mfma_f32_16x16x32_bf16 v[88:91], a[20:23], a[212:215], v[88:91]\n" \
"s_add_u32 m0, s89, 0\n" \
"ds_read_b128 a[16:19], v205 offset:49152\n" \
"v_mfma_f32_16x16x32_bf16 v[96:99], a[24:27], a[208:211], v[96:99]\n" \
"s_xor_b32 s73, s73, 0x80\n" \
"ds_read_b128 a[20:23], v206 offset:49152\n" \
"v_mfma_f32_16x16x32_bf16 v[96:99], a[28:31], a[212:215], v[96:99]\n" \
"buffer_load_dwordx4 v196, s[44:47], s75 offen lds\n" \
"v_mfma_f32_16x16x32_bf16 v[100:103], a[24:27], a[216:219], v[100:103]\n" \
"s_xor_b32 s75, s75, 0x80\n" \
"s_mul_i32 s76, s80, 393216\n" \
"ds_read_b64_tr_b16 a[54:55], v195 offset:15360\n" \
"v_mfma_f32_16x16x32_bf16 v[100:103], a[28:31], a[220:223], v[100:103]\n" \
"s_add_u32 s76, s76, s78\n" \
"ds_read_b128 a[24:27], v205 offset:53248\n" \
"v_mfma_f32_16x16x32_bf16 v[108:111], a[192:195], a[216:219], v[108:111]\n" \
"s_add_u32 m0, s91, 20480\n" \
"ds_read_b128 a[28:31], v206 offset:53248\n" \
"v_mfma_f32_16x16x32_bf16 v[108:111], a[196:199], a[220:223], v[108:111]\n" \
"buffer_load_dwordx4 v197, s[48:51], s76 offen offset:0 lds\n" \
"v_mfma_f32_16x16x32_bf16 v[104:107], a[192:195], a[208:211], v[104:107]\n" \
"v_mfma_f32_16x16x32_bf16 v[104:107], a[196:199], a[212:215], v[104:107]\n" \
"s_waitcnt lgkmcnt(2)\n" \
"v_mfma_f32_16x16x32_bf16 v[32:35], a[32:35], a[0:3], v[32:35]\n" \
"s_add_u32 s77, s76, 24576\n" \
"ds_read_b128 a[192:195], v205 offset:57344\n" \
"v_mfma_f32_16x16x32_bf16 v[32:35], a[36:39], a[4:7], v[32:35]\n" \
"buffer_load_dwordx4 v198, s[48:51], s76 offen offset:1024 lds\n" \
"v_mfma_f32_16x16x32_bf16 v[40:43], a[32:35], a[8:11], v[40:43]\n" \
"ds_read_b128 a[196:199], v206 offset:57344\n" \
"v_mfma_f32_16x16x32_bf16 v[40:43], a[36:39], a[12:15], v[40:43]\n" \
"ds_read_b64_tr_b16 a[200:201], v195 offset:1024\n" \
"v_mfma_f32_16x16x32_bf16 v[0:3], a[40:43], a[8:11], v[0:3]\n" \
"s_xor_b32 s80, s80, 0x1\n" \
"ds_read_b64_tr_b16 a[202:203], v195 offset:4096\n" \
"v_mfma_f32_16x16x32_bf16 v[0:3], a[44:47], a[12:15], v[0:3]\n" \
"buffer_load_dwordx4 v199, s[48:51], s76 offen offset:2048 lds\n" \
"v_mfma_f32_16x16x32_bf16 v[4:7], a[48:51], a[8:11], v[4:7]\n" \
"ds_read_b64_tr_b16 a[204:205], v195 offset:13312\n" \
"v_mfma_f32_16x16x32_bf16 v[4:7], a[52:55], a[12:15], v[4:7]\n" \
"ds_read_b64_tr_b16 a[206:207], v195 offset:16384\n" \
"v_mfma_f32_16x16x32_bf16 v[12:15], a[48:51], a[16:19], v[12:15]\n" \
"s_add_u32 m0, m0, 3072\n" \
"ds_read_b64_tr_b16 a[208:209], v194 offset:2048\n" \
"v_mfma_f32_16x16x32_bf16 v[12:15], a[52:55], a[20:23], v[12:15]\n" \
"buffer_load_dwordx4 v197, s[48:51], s77 offen offset:0 lds\n" \
"v_mfma_f32_16x16x32_bf16 v[8:11], a[40:43], a[16:19], v[8:11]\n" \
"ds_read_b64_tr_b16 a[210:211], v194 offset:5120\n" \
"v_mfma_f32_16x16x32_bf16 v[8:11], a[44:47], a[20:23], v[8:11]\n" \
"ds_read_b64_tr_b16 a[212:213], v194 offset:14336\n" \
"v_mfma_f32_16x16x32_bf16 v[48:51], a[32:35], a[16:19], v[48:51]\n" \
"s_waitcnt lgkmcnt(3)\n" \
"ds_read_b64_tr_b16 a[214:215], v194 offset:17408\n" \
"v_mfma_f32_16x16x32_bf16 v[48:51], a[36:39], a[20:23], v[48:51]\n" \
"buffer_load_dwordx4 v198, s[48:51], s77 offen offset:1024 lds\n" \
"v_mfma_f32_16x16x32_bf16 v[56:59], a[32:35], a[24:27], v[56:59]\n" \
"ds_read_b64_tr_b16 a[216:217], v195 offset:2048\n" \
"v_mfma_f32_16x16x32_bf16 v[56:59], a[36:39], a[28:31], v[56:59]\n" \
"ds_read_b64_tr_b16 a[218:219], v195 offset:5120\n" \
"v_mfma_f32_16x16x32_bf16 v[16:19], a[40:43], a[24:27], v[16:19]\n" \
"ds_read_b64_tr_b16 a[220:221], v195 offset:14336\n" \
"v_mfma_f32_16x16x32_bf16 v[16:19], a[44:47], a[28:31], v[16:19]\n" \
"buffer_load_dwordx4 v199, s[48:51], s77 offen offset:2048 lds\n" \
"v_mfma_f32_16x16x32_bf16 v[20:23], a[48:51], a[24:27], v[20:23]\n" \
"ds_read_b64_tr_b16 a[222:223], v195 offset:17408\n" \
"v_mfma_f32_16x16x32_bf16 v[20:23], a[52:55], a[28:31], v[20:23]\n" \
"v_mfma_f32_16x16x32_bf16 v[28:31], a[48:51], a[192:195], v[28:31]\n" \
"v_mfma_f32_16x16x32_bf16 v[28:31], a[52:55], a[196:199], v[28:31]\n" \
"buffer_load_dword v220, v221, s[56:59], s66 offen sc1\n" \
"v_mfma_f32_16x16x32_bf16 v[24:27], a[40:43], a[192:195], v[24:27]\n" \
"v_mfma_f32_16x16x32_bf16 v[24:27], a[44:47], a[196:199], v[24:27]\n" \
"v_mfma_f32_16x16x32_bf16 v[64:67], a[32:35], a[192:195], v[64:67]\n" \
"v_mfma_f32_16x16x32_bf16 v[64:67], a[36:39], a[196:199], v[64:67]\n" \
"s_waitcnt vmcnt(12) lgkmcnt(0)\n" \
"s_barrier\n" \
"v_mfma_f32_16x16x32_bf16 v[68:71], a[200:203], a[192:195], v[68:71]\n" \
"ds_read_b64_tr_b16 a[32:33], v209 offset:1024\n" \
"ds_read_b64_tr_b16 a[34:35], v209 offset:4096\n" \
"v_mfma_f32_16x16x32_bf16 v[68:71], a[204:207], a[196:199], v[68:71]\n" \
"ds_read_b64_tr_b16 a[36:37], v209 offset:13312\n" \
"ds_read_b64_tr_b16 a[38:39], v209 offset:16384\n" \
"v_mfma_f32_16x16x32_bf16 v[60:63], a[200:203], a[24:27], v[60:63]\n" \
"s_add_u32 m0, s79, 110592\n" \
"ds_read_b64_tr_b16 a[40:41], v209 offset:0\n" \
"v_mfma_f32_16x16x32_bf16 v[60:63], a[204:207], a[28:31], v[60:63]\n" \
"buffer_load_dwordx4 v192, s[44:47], s73 offen offset:0 lds\n" \
"v_mfma_f32_16x16x32_bf16 v[52:55], a[200:203], a[16:19], v[52:55]\n" \
"ds_read_b64_tr_b16 a[42:43], v209 offset:3072\n" \
"v_mfma_f32_16x16x32_bf16 v[52:55], a[204:207], a[20:23], v[52:55]\n" \
"ds_read_b64_tr_b16 a[44:45], v209 offset:12288\n" \
"v_mfma_f32_16x16x32_bf16 v[44:47], a[200:203], a[8:11], v[44:47]\n" \
"ds_read_b64_tr_b16 a[46:47], v209 offset:15360\n" \
"v_mfma_f32_16x16x32_bf16 v[44:47], a[204:207], a[12:15], v[44:47]\n" \
"buffer_load_dwordx4 v193, s[44:47], s73 offen offset:1024 lds\n" \
"v_mfma_f32_16x16x32_bf16 v[72:75], a[0:3], a[208:211], v[72:75]\n" \
"ds_read_b64_tr_b16 a[48:49], v210 offset:0\n" \
"v_mfma_f32_16x16x32_bf16 v[72:75], a[4:7], a[212:215], v[72:75]\n" \
"ds_read_b128 a[0:3], v207 offset:0\n" \
"v_mfma_f32_16x16x32_bf16 v[80:83], a[8:11], a[208:211], v[80:83]\n" \
"ds_read_b128 a[4:7], v208 offset:0\n" \
"v_mfma_f32_16x16x32_bf16 v[80:83], a[12:15], a[212:215], v[80:83]\n" \
"buffer_load_dwordx4 v192, s[44:47], s73 offen offset:2048 lds\n" \
"v_mfma_f32_16x16x32_bf16 v[84:87], a[8:11], a[216:219], v[84:87]\n" \
"ds_read_b64_tr_b16 a[50:51], v210 offset:3072\n" \
"v_mfma_f32_16x16x32_bf16 v[84:87], a[12:15], a[220:223], v[84:87]\n" \
"ds_read_b128 a[8:11], v205 offset:0\n" \
"v_mfma_f32_16x16x32_bf16 v[92:95], a[16:19], a[216:219], v[92:95]\n" \
"ds_read_b128 a[12:15], v206 offset:0\n" \
"v_mfma_f32_16x16x32_bf16 v[92:95], a[20:23], a[220:223], v[92:95]\n" \
"buffer_load_dwordx4 v193, s[44:47], s73 offen offset:3072 lds\n" \
"v_mfma_f32_16x16x32_bf16 v[88:91], a[16:19], a[208:211], v[88:91]\n" \
"ds_read_b64_tr_b16 a[52:53], v210 offset:12288\n" \
"v_mfma_f32_16x16x32_bf16 v[88:91], a[20:23], a[212:215], v[88:91]\n" \
"s_add_u32 m0, s89, 110592\n" \
"ds_read_b128 a[16:19], v205 offset:4096\n" \
"v_mfma_f32_16x16x32_bf16 v[96:99], a[24:27], a[208:211], v[96:99]\n" \
"s_xor_b32 s73, s73, 0x80\n" \
"ds_read_b128 a[20:23], v206 offset:4096\n" \
"v_mfma_f32_16x16x32_bf16 v[96:99], a[28:31], a[212:215], v[96:99]\n" \
"buffer_load_dwordx4 v196, s[44:47], s75 offen lds\n" \
"v_mfma_f32_16x16x32_bf16 v[100:103], a[24:27], a[216:219], v[100:103]\n" \
"s_xor_b32 s75, s75, 0x80\n" \
"s_mul_i32 s76, s80, 393216\n" \
"ds_read_b64_tr_b16 a[54:55], v210 offset:15360\n" \
"v_mfma_f32_16x16x32_bf16 v[100:103], a[28:31], a[220:223], v[100:103]\n" \
"s_add_u32 s76, s76, s78\n" \
"ds_read_b128 a[24:27], v205 offset:8192\n" \
"v_mfma_f32_16x16x32_bf16 v[108:111], a[192:195], a[216:219], v[108:111]\n" \
"s_add_u32 m0, s91, 139264\n" \
"ds_read_b128 a[28:31], v206 offset:8192\n" \
"v_mfma_f32_16x16x32_bf16 v[108:111], a[196:199], a[220:223], v[108:111]\n" \
"buffer_load_dwordx4 v197, s[48:51], s76 offen offset:0 lds\n" \
"v_mfma_f32_16x16x32_bf16 v[104:107], a[192:195], a[208:211], v[104:107]\n" \
"v_mfma_f32_16x16x32_bf16 v[104:107], a[196:199], a[212:215], v[104:107]\n" \
"s_waitcnt lgkmcnt(2)\n" \
"v_mfma_f32_16x16x32_bf16 v[32:35], a[32:35], a[0:3], v[32:35]\n" \
"s_add_u32 s77, s76, 24576\n" \
"ds_read_b128 a[192:195], v205 offset:12288\n" \
"v_mfma_f32_16x16x32_bf16 v[32:35], a[36:39], a[4:7], v[32:35]\n" \
"buffer_load_dwordx4 v198, s[48:51], s76 offen offset:1024 lds\n" \
"v_mfma_f32_16x16x32_bf16 v[40:43], a[32:35], a[8:11], v[40:43]\n" \
"ds_read_b128 a[196:199], v206 offset:12288\n" \
"v_mfma_f32_16x16x32_bf16 v[40:43], a[36:39], a[12:15], v[40:43]\n" \
"ds_read_b64_tr_b16 a[200:201], v210 offset:1024\n" \
"v_mfma_f32_16x16x32_bf16 v[0:3], a[40:43], a[8:11], v[0:3]\n" \
"s_xor_b32 s80, s80, 0x1\n" \
"ds_read_b64_tr_b16 a[202:203], v210 offset:4096\n" \
"v_mfma_f32_16x16x32_bf16 v[0:3], a[44:47], a[12:15], v[0:3]\n" \
"buffer_load_dwordx4 v199, s[48:51], s76 offen offset:2048 lds\n" \
"v_mfma_f32_16x16x32_bf16 v[4:7], a[48:51], a[8:11], v[4:7]\n" \
"ds_read_b64_tr_b16 a[204:205], v210 offset:13312\n" \
"v_mfma_f32_16x16x32_bf16 v[4:7], a[52:55], a[12:15], v[4:7]\n" \
"ds_read_b64_tr_b16 a[206:207], v210 offset:16384\n" \
"v_mfma_f32_16x16x32_bf16 v[12:15], a[48:51], a[16:19], v[12:15]\n" \
"s_add_u32 m0, m0, 3072\n" \
"ds_read_b64_tr_b16 a[208:209], v209 offset:2048\n" \
"v_mfma_f32_16x16x32_bf16 v[12:15], a[52:55], a[20:23], v[12:15]\n" \
"buffer_load_dwordx4 v197, s[48:51], s77 offen offset:0 lds\n" \
"v_mfma_f32_16x16x32_bf16 v[8:11], a[40:43], a[16:19], v[8:11]\n" \
"ds_read_b64_tr_b16 a[210:211], v209 offset:5120\n" \
"v_mfma_f32_16x16x32_bf16 v[8:11], a[44:47], a[20:23], v[8:11]\n" \
"ds_read_b64_tr_b16 a[212:213], v209 offset:14336\n" \
"v_mfma_f32_16x16x32_bf16 v[48:51], a[32:35], a[16:19], v[48:51]\n" \
"s_waitcnt lgkmcnt(3)\n" \
"ds_read_b64_tr_b16 a[214:215], v209 offset:17408\n" \
"v_mfma_f32_16x16x32_bf16 v[48:51], a[36:39], a[20:23], v[48:51]\n" \
"buffer_load_dwordx4 v198, s[48:51], s77 offen offset:1024 lds\n" \
"v_mfma_f32_16x16x32_bf16 v[56:59], a[32:35], a[24:27], v[56:59]\n" \
"ds_read_b64_tr_b16 a[216:217], v210 offset:2048\n" \
"v_mfma_f32_16x16x32_bf16 v[56:59], a[36:39], a[28:31], v[56:59]\n" \
"ds_read_b64_tr_b16 a[218:219], v210 offset:5120\n" \
"v_mfma_f32_16x16x32_bf16 v[16:19], a[40:43], a[24:27], v[16:19]\n" \
"ds_read_b64_tr_b16 a[220:221], v210 offset:14336\n" \
"v_mfma_f32_16x16x32_bf16 v[16:19], a[44:47], a[28:31], v[16:19]\n" \
"buffer_load_dwordx4 v199, s[48:51], s77 offen offset:2048 lds\n" \
"v_mfma_f32_16x16x32_bf16 v[20:23], a[48:51], a[24:27], v[20:23]\n" \
"s_mov_b32 s46, s81\n" \
"s_mov_b32 s47, s82\n" \
"ds_read_b64_tr_b16 a[222:223], v210 offset:17408\n" \
"v_mfma_f32_16x16x32_bf16 v[20:23], a[52:55], a[28:31], v[20:23]\n" \
"s_mov_b32 s48, 0\n" \
"s_mov_b32 s49, s68\n" \
"s_sub_u32 s50, 64, s9\n" \
"v_mfma_f32_16x16x32_bf16 v[28:31], a[48:51], a[192:195], v[28:31]\n" \
"s_cmp_eq_u32 s90, 0\n" \
"s_cselect_b32 s50, s9, s50\n" \
"s_andn2_b32 s50, s50, 3\n" \
"v_mfma_f32_16x16x32_bf16 v[28:31], a[52:55], a[196:199], v[28:31]\n" \
"s_lshl_b32 s50, s50, 12\n" \
"s_waitcnt vmcnt(11)\n" \
"v_cmp_eq_u32 vcc, s60, v220\n" \
"v_mfma_f32_16x16x32_bf16 v[24:27], a[40:43], a[192:195], v[24:27]\n" \
"v_mfma_f32_16x16x32_bf16 v[24:27], a[44:47], a[196:199], v[24:27]\n" \
"s_andn2_b32 s70, s65, vcc_lo\n" \
"s_cbranch_scc1 Lpl5_%=\n" \
"Lpr5_%=:\n" \
"v_mfma_f32_16x16x32_bf16 v[64:67], a[32:35], a[192:195], v[64:67]\n" \
"v_mfma_f32_16x16x32_bf16 v[64:67], a[36:39], a[196:199], v[64:67]\n" \
"s_xor_b32 s82, s48, s49\n" \
"s_cmp_lt_u32 s48, s50\n" \
"s_cselect_b32 s82, s82, s48\n" \
"s_waitcnt vmcnt(12) lgkmcnt(0)\n" \
"s_barrier\n" \
"v_mfma_f32_16x16x32_bf16 v[68:71], a[200:203], a[192:195], v[68:71]\n" \
"ds_read_b64_tr_b16 a[32:33], v204 offset:21504\n" \
"ds_read_b64_tr_b16 a[34:35], v204 offset:24576\n" \
"v_mfma_f32_16x16x32_bf16 v[68:71], a[204:207], a[196:199], v[68:71]\n" \
"s_add_u32 s81, s82, s46\n" \
"s_lshr_b32 s78, s61, 1\n" \
"s_lshl_b32 s80, s78, 9\n" \
"s_add_u32 s80, s80, 511\n" \
"ds_read_b64_tr_b16 a[36:37], v204 offset:33792\n" \
"ds_read_b64_tr_b16 a[38:39], v204 offset:36864\n" \
"v_mfma_f32_16x16x32_bf16 v[60:63], a[200:203], a[24:27], v[60:63]\n" \
"s_add_u32 m0, s86, s79\n" \
"ds_read_b64_tr_b16 a[40:41], v204 offset:20480\n" \
"v_sub_u32 v145, s80, v234\n" \
"v_mfma_f32_16x16x32_bf16 v[60:63], a[204:207], a[28:31], v[60:63]\n" \
"buffer_load_dwordx4 v234, s[56:59], s81 offen offset:0 sc1 lds\n" \
"v_mfma_f32_16x16x32_bf16 v[52:55], a[200:203], a[16:19], v[52:55]\n" \
"s_lshl_b32 s80, s78, 3\n" \
"s_not_b32 s80, s80\n" \
"ds_read_b64_tr_b16 a[42:43], v204 offset:23552\n" \
"v_mfma_f32_16x16x32_bf16 v[52:55], a[204:207], a[20:23], v[52:55]\n" \
"s_lshl_b32 s82, s82, 5\n" \
"ds_read_b64_tr_b16 a[44:45], v204 offset:32768\n" \
"v_sub_u32 v146, s80, v238\n" \
"v_mfma_f32_16x16x32_bf16 v[44:47], a[200:203], a[8:11], v[44:47]\n" \
"s_add_u32 s82, s82, s47\n" \
"ds_read_b64_tr_b16 a[46:47], v204 offset:35840\n" \
"v_subrev_u32 v152, 0, v238\n" \
"v_mfma_f32_16x16x32_bf16 v[44:47], a[204:207], a[12:15], v[44:47]\n" \
"buffer_load_dwordx4 v234, s[56:59], s81 offen offset:1024 sc1 lds\n" \
"v_mfma_f32_16x16x32_bf16 v[72:75], a[0:3], a[208:211], v[72:75]\n" \
"ds_read_b64_tr_b16 a[48:49], v222 offset:20480\n" \
"v_sub_u32 v144, 7, v238\n" \
"v_mfma_f32_16x16x32_bf16 v[72:75], a[4:7], a[212:215], v[72:75]\n" \
"s_add_u32 s48, s48, 0x1000\n" \
"ds_read_b128 a[0:3], v202 offset:0\n" \
"v_or_b32 v152, v152, v144\n" \
"v_mfma_f32_16x16x32_bf16 v[80:83], a[8:11], a[208:211], v[80:83]\n" \
"s_sub_u32 s86, s86, 32768\n" \
"s_cselect_b32 s86, 131072, s86\n" \
"ds_read_b128 a[4:7], v203 offset:0\n" \
"v_mfma_f32_16x16x32_bf16 v[80:83], a[12:15], a[212:215], v[80:83]\n" \
"buffer_load_dwordx4 v234, s[56:59], s81 offen offset:2048 sc1 lds\n" \
"v_mfma_f32_16x16x32_bf16 v[84:87], a[8:11], a[216:219], v[84:87]\n" \
"ds_read_b64_tr_b16 a[50:51], v222 offset:23552\n" \
"v_ashrrev_i32 v152, 31, v152\n" \
"v_mfma_f32_16x16x32_bf16 v[84:87], a[12:15], a[220:223], v[84:87]\n" \
"ds_read_b128 a[8:11], v200 offset:0\n" \
"v_and_b32 v152, 0xff800000, v152\n" \
"v_mfma_f32_16x16x32_bf16 v[92:95], a[16:19], a[216:219], v[92:95]\n" \
"ds_read_b128 a[12:15], v201 offset:0\n" \
"v_sub_u32 v156, -9, v238\n" \
"v_mfma_f32_16x16x32_bf16 v[92:95], a[20:23], a[220:223], v[92:95]\n" \
"buffer_load_dwordx4 v234, s[56:59], s81 offen offset:3072 sc1 lds\n" \
"v_mfma_f32_16x16x32_bf16 v[88:91], a[16:19], a[208:211], v[88:91]\n" \
"ds_read_b64_tr_b16 a[52:53], v222 offset:32768\n" \
"v_ashrrev_i32 v156, 31, v156\n" \
"v_mfma_f32_16x16x32_bf16 v[88:91], a[20:23], a[212:215], v[88:91]\n" \
"ds_read_b128 a[16:19], v200 offset:4096\n" \
"v_and_b32 v156, 0xff800000, v156\n" \
"v_mfma_f32_16x16x32_bf16 v[96:99], a[24:27], a[208:211], v[96:99]\n" \
"s_add_u32 m0, m0, 0x4000\n" \
"ds_read_b128 a[20:23], v201 offset:4096\n" \
"v_add_u32 v148, 0, v146\n" \
"v_mfma_f32_16x16x32_bf16 v[96:99], a[28:31], a[212:215], v[96:99]\n" \
"buffer_load_dwordx4 v216, s[36:39], s82 offen offset:0 lds\n" \
"v_mfma_f32_16x16x32_bf16 v[100:103], a[24:27], a[216:219], v[100:103]\n" \
"ds_read_b64_tr_b16 a[54:55], v222 offset:35840\n" \
"v_or3_b32 v148, v148, v145, s92\n" \
"v_mfma_f32_16x16x32_bf16 v[100:103], a[28:31], a[220:223], v[100:103]\n" \
"ds_read_b128 a[24:27], v200 offset:8192\n" \
"v_ashrrev_i32 v148, 31, v148\n" \
"v_mfma_f32_16x16x32_bf16 v[108:111], a[192:195], a[216:219], v[108:111]\n" \
"ds_read_b128 a[28:31], v201 offset:8192\n" \
"v_and_b32 v148, 0xff800000, v148\n" \
"v_mfma_f32_16x16x32_bf16 v[108:111], a[196:199], a[220:223], v[108:111]\n" \
"buffer_load_dwordx4 v217, s[36:39], s82 offen offset:1024 lds\n" \
"v_mfma_f32_16x16x32_bf16 v[104:107], a[192:195], a[208:211], v[104:107]\n" \
"v_subrev_u32 v153, 1, v238\n" \
"v_sub_u32 v144, 8, v238\n" \
"v_mfma_f32_16x16x32_bf16 v[104:107], a[196:199], a[212:215], v[104:107]\n" \
"s_waitcnt lgkmcnt(2)\n" \
"v_or_b32 v153, v153, v144\n" \
"v_ashrrev_i32 v153, 31, v153\n" \
"v_mfma_f32_16x16x32_bf16 v[32:35], a[32:35], a[0:3], v[32:35]\n" \
"ds_read_b128 a[192:195], v200 offset:12288\n" \
"v_and_b32 v153, 0xff800000, v153\n" \
"v_mfma_f32_16x16x32_bf16 v[32:35], a[36:39], a[4:7], v[32:35]\n" \
"buffer_load_dwordx4 v218, s[36:39], s82 offen offset:2048 lds\n" \
"v_mfma_f32_16x16x32_bf16 v[40:43], a[32:35], a[8:11], v[40:43]\n" \
"ds_read_b128 a[196:199], v201 offset:12288\n" \
"v_sub_u32 v157, -8, v238\n" \
"v_mfma_f32_16x16x32_bf16 v[40:43], a[36:39], a[12:15], v[40:43]\n" \
"ds_read_b64_tr_b16 a[200:201], v222 offset:21504\n" \
"v_ashrrev_i32 v157, 31, v157\n" \
"v_mfma_f32_16x16x32_bf16 v[0:3], a[40:43], a[8:11], v[0:3]\n" \
"ds_read_b64_tr_b16 a[202:203], v222 offset:24576\n" \
"v_and_b32 v157, 0xff800000, v157\n" \
"v_mfma_f32_16x16x32_bf16 v[0:3], a[44:47], a[12:15], v[0:3]\n" \
"buffer_load_dwordx4 v219, s[36:39], s82 offen offset:3072 lds\n" \
"v_mfma_f32_16x16x32_bf16 v[4:7], a[48:51], a[8:11], v[4:7]\n" \
"ds_read_b64_tr_b16 a[204:205], v222 offset:33792\n" \
"v_add_u32 v149, 1, v146\n" \
"v_mfma_f32_16x16x32_bf16 v[4:7], a[52:55], a[12:15], v[4:7]\n" \
"ds_read_b64_tr_b16 a[206:207], v222 offset:36864\n" \
"v_or3_b32 v149, v149, v145, s92\n" \
"v_mfma_f32_16x16x32_bf16 v[12:15], a[48:51], a[16:19], v[12:15]\n" \
"ds_read_b64_tr_b16 a[208:209], v204 offset:22528\n" \
"v_ashrrev_i32 v149, 31, v149\n" \
"v_mfma_f32_16x16x32_bf16 v[12:15], a[52:55], a[20:23], v[12:15]\n" \
"v_and_b32 v149, 0xff800000, v149\n" \
"v_subrev_u32 v154, 2, v238\n" \
"v_mfma_f32_16x16x32_bf16 v[8:11], a[40:43], a[16:19], v[8:11]\n" \
"ds_read_b64_tr_b16 a[210:211], v204 offset:25600\n" \
"v_sub_u32 v144, 9, v238\n" \
"v_mfma_f32_16x16x32_bf16 v[8:11], a[44:47], a[20:23], v[8:11]\n" \
"ds_read_b64_tr_b16 a[212:213], v204 offset:34816\n" \
"v_or_b32 v154, v154, v144\n" \
"v_mfma_f32_16x16x32_bf16 v[48:51], a[32:35], a[16:19], v[48:51]\n" \
"ds_read_b64_tr_b16 a[214:215], v204 offset:37888\n" \
"v_ashrrev_i32 v154, 31, v154\n" \
"v_mfma_f32_16x16x32_bf16 v[48:51], a[36:39], a[20:23], v[48:51]\n" \
"s_waitcnt lgkmcnt(3)\n" \
"v_and_b32 v154, 0xff800000, v154\n" \
"v_sub_u32 v158, -7, v238\n" \
"v_mfma_f32_16x16x32_bf16 v[56:59], a[32:35], a[24:27], v[56:59]\n" \
"ds_read_b64_tr_b16 a[216:217], v222 offset:22528\n" \
"v_ashrrev_i32 v158, 31, v158\n" \
"v_mfma_f32_16x16x32_bf16 v[56:59], a[36:39], a[28:31], v[56:59]\n" \
"ds_read_b64_tr_b16 a[218:219], v222 offset:25600\n" \
"v_and_b32 v158, 0xff800000, v158\n" \
"v_mfma_f32_16x16x32_bf16 v[16:19], a[40:43], a[24:27], v[16:19]\n" \
"ds_read_b64_tr_b16 a[220:221], v222 offset:34816\n" \
"v_add_u32 v150, 2, v146\n" \
"v_mfma_f32_16x16x32_bf16 v[16:19], a[44:47], a[28:31], v[16:19]\n" \
"v_or3_b32 v150, v150, v145, s92\n" \
"v_ashrrev_i32 v150, 31, v150\n" \
"v_mfma_f32_16x16x32_bf16 v[20:23], a[48:51], a[24:27], v[20:23]\n" \
"ds_read_b64_tr_b16 a[222:223], v222 offset:37888\n" \
"v_and_b32 v150, 0xff800000, v150\n" \
"v_mfma_f32_16x16x32_bf16 v[20:23], a[52:55], a[28:31], v[20:23]\n" \
"v_subrev_u32 v155, 3, v238\n" \
"v_sub_u32 v144, 10, v238\n" \
"v_mfma_f32_16x16x32_bf16 v[28:31], a[48:51], a[192:195], v[28:31]\n" \
"v_or_b32 v155, v155, v144\n" \
"v_ashrrev_i32 v155, 31, v155\n" \
"v_mfma_f32_16x16x32_bf16 v[28:31], a[52:55], a[196:199], v[28:31]\n" \
"v_and_b32 v155, 0xff800000, v155\n" \
"v_sub_u32 v159, -6, v238\n" \
"v_mfma_f32_16x16x32_bf16 v[24:27], a[40:43], a[192:195], v[24:27]\n" \
"v_ashrrev_i32 v159, 31, v159\n" \
"v_and_b32 v159, 0xff800000, v159\n" \
"v_mfma_f32_16x16x32_bf16 v[24:27], a[44:47], a[196:199], v[24:27]\n" \
"v_add_u32 v151, 3, v146\n" \
"v_or3_b32 v151, v151, v145, s92\n" \
"v_mfma_f32_16x16x32_bf16 v[64:67], a[32:35], a[192:195], v[64:67]\n" \
"v_ashrrev_i32 v151, 31, v151\n" \
"v_and_b32 v151, 0xff800000, v151\n" \
"v_mfma_f32_16x16x32_bf16 v[64:67], a[36:39], a[196:199], v[64:67]\n" \
"s_xor_b32 s82, s48, s49\n" \
"s_cmp_lt_u32 s48, s50\n" \
"s_cselect_b32 s82, s82, s48\n" \
"s_waitcnt vmcnt(8) lgkmcnt(0)\n" \
"s_barrier\n" \
"v_mfma_f32_16x16x32_bf16 v[68:71], a[200:203], a[192:195], v[68:71]\n" \
"ds_read_b64_tr_b16 a[32:33], v194 offset:1024\n" \
"ds_read_b64_tr_b16 a[34:35], v194 offset:4096\n" \
"v_mfma_f32_16x16x32_bf16 v[68:71], a[204:207], a[196:199], v[68:71]\n" \
"s_add_u32 s81, s82, s46\n" \
"ds_read_b64_tr_b16 a[36:37], v194 offset:13312\n" \
"ds_read_b64_tr_b16 a[38:39], v194 offset:16384\n" \
"v_mfma_f32_16x16x32_bf16 v[60:63], a[200:203], a[24:27], v[60:63]\n" \
"s_add_u32 m0, s86, s79\n" \
"ds_read_b64_tr_b16 a[40:41], v194 offset:0\n" \
"v_mov_b32 v186, 0xffff0000\n" \
"v_mfma_f32_16x16x32_bf16 v[60:63], a[204:207], a[28:31], v[60:63]\n" \
"buffer_load_dwordx4 v234, s[56:59], s81 offen offset:0 sc1 lds\n" \
"v_mfma_f32_16x16x32_bf16 v[52:55], a[200:203], a[16:19], v[52:55]\n" \
"ds_read_b64_tr_b16 a[42:43], v194 offset:3072\n" \
"v_subrev_u32 v144, 0, v238\n" \
"v_mfma_f32_16x16x32_bf16 v[52:55], a[204:207], a[20:23], v[52:55]\n" \
"s_lshl_b32 s82, s82, 5\n" \
"ds_read_b64_tr_b16 a[44:45], v194 offset:12288\n" \
"v_subrev_u32 v184, 1, v238\n" \
"v_mfma_f32_16x16x32_bf16 v[44:47], a[200:203], a[8:11], v[44:47]\n" \
"s_add_u32 s82, s82, s47\n" \
"ds_read_b64_tr_b16 a[46:47], v194 offset:15360\n" \
"v_lshrrev_b32 v144, 16, v144\n" \
"v_mfma_f32_16x16x32_bf16 v[44:47], a[204:207], a[12:15], v[44:47]\n" \
"buffer_load_dwordx4 v234, s[56:59], s81 offen offset:1024 sc1 lds\n" \
"v_mfma_f32_16x16x32_bf16 v[72:75], a[0:3], a[208:211], v[72:75]\n" \
"ds_read_b64_tr_b16 a[48:49], v195 offset:0\n" \
"v_and_or_b32 v184, v184, v186, v144\n" \
"v_mfma_f32_16x16x32_bf16 v[72:75], a[4:7], a[212:215], v[72:75]\n" \
"s_add_u32 s48, s48, 0x1000\n" \
"ds_read_b128 a[0:3], v207 offset:45056\n" \
"v_subrev_u32 v144, 2, v238\n" \
"v_mfma_f32_16x16x32_bf16 v[80:83], a[8:11], a[208:211], v[80:83]\n" \
"s_sub_u32 s86, s86, 32768\n" \
"s_cselect_b32 s86, 131072, s86\n" \
"ds_read_b128 a[4:7], v208 offset:45056\n" \
"v_mfma_f32_16x16x32_bf16 v[80:83], a[12:15], a[212:215], v[80:83]\n" \
"buffer_load_dwordx4 v234, s[56:59], s81 offen offset:2048 sc1 lds\n" \
"v_mfma_f32_16x16x32_bf16 v[84:87], a[8:11], a[216:219], v[84:87]\n" \
"ds_read_b64_tr_b16 a[50:51], v195 offset:3072\n" \
"v_subrev_u32 v185, 3, v238\n" \
"v_mfma_f32_16x16x32_bf16 v[84:87], a[12:15], a[220:223], v[84:87]\n" \
"ds_read_b128 a[8:11], v205 offset:45056\n" \
"v_lshrrev_b32 v144, 16, v144\n" \
"v_mfma_f32_16x16x32_bf16 v[92:95], a[16:19], a[216:219], v[92:95]\n" \
"ds_read_b128 a[12:15], v206 offset:45056\n" \
"v_and_or_b32 v185, v185, v186, v144\n" \
"v_mfma_f32_16x16x32_bf16 v[92:95], a[20:23], a[220:223], v[92:95]\n" \
"buffer_load_dwordx4 v234, s[56:59], s81 offen offset:3072 sc1 lds\n" \
"v_mfma_f32_16x16x32_bf16 v[88:91], a[16:19], a[208:211], v[88:91]\n" \
"ds_read_b64_tr_b16 a[52:53], v195 offset:12288\n" \
"v_mov_b32 v144, 0x3e8293ee\n" \
"v_mfma_f32_16x16x32_bf16 v[88:91], a[20:23], a[212:215], v[88:91]\n" \
"s_add_u32 s80, s79, 117760\n" \
"ds_read_b128 a[16:19], v205 offset:49152\n" \
"v_add_u32 v223, s80, v234\n" \
"v_mfma_f32_16x16x32_bf16 v[96:99], a[24:27], a[208:211], v[96:99]\n" \
"s_add_u32 m0, m0, 0x4000\n" \
"ds_read_b128 a[20:23], v206 offset:49152\n" \
"v_mfma_f32_16x16x32_bf16 v[96:99], a[28:31], a[212:215], v[96:99]\n" \
"buffer_load_dwordx4 v216, s[36:39], s82 offen offset:0 lds\n" \
"v_mfma_f32_16x16x32_bf16 v[100:103], a[24:27], a[216:219], v[100:103]\n" \
"s_cmp_lg_u32 s78, 0\n" \
"s_cselect_b64 s[76:77], -1, 0\n" \
"ds_read_b64_tr_b16 a[54:55], v195 offset:15360\n" \
"v_mfma_f32_16x16x32_bf16 v[100:103], a[28:31], a[220:223], v[100:103]\n" \
"ds_read_b128 a[24:27], v205 offset:53248\n" \
"v_mfma_f32_16x16x32_bf16 v[108:111], a[192:195], a[216:219], v[108:111]\n" \
"ds_read_b128 a[28:31], v206 offset:53248\n" \
"v_mfma_f32_16x16x32_bf16 v[108:111], a[196:199], a[220:223], v[108:111]\n" \
"buffer_load_dwordx4 v217, s[36:39], s82 offen offset:1024 lds\n" \
"v_mfma_f32_16x16x32_bf16 v[104:107], a[192:195], a[208:211], v[104:107]\n" \
"v_mfma_f32_16x16x32_bf16 v[104:107], a[196:199], a[212:215], v[104:107]\n" \
"s_waitcnt lgkmcnt(2)\n" \
"v_mfma_f32_16x16x32_bf16 v[32:35], a[32:35], a[0:3], v[32:35]\n" \
"ds_read_b128 a[192:195], v205 offset:57344\n" \
"v_mfma_f32_16x16x32_bf16 v[32:35], a[36:39], a[4:7], v[32:35]\n" \
"buffer_load_dwordx4 v218, s[36:39], s82 offen offset:2048 lds\n" \
"v_mfma_f32_16x16x32_bf16 v[40:43], a[32:35], a[8:11], v[40:43]\n" \
"ds_read_b128 a[196:199], v206 offset:57344\n" \
"v_mfma_f32_16x16x32_bf16 v[40:43], a[36:39], a[12:15], v[40:43]\n" \
"ds_read_b64_tr_b16 a[200:201], v195 offset:1024\n" \
"v_mfma_f32_16x16x32_bf16 v[0:3], a[40:43], a[8:11], v[0:3]\n" \
"ds_read_b64_tr_b16 a[202:203], v195 offset:4096\n" \
"v_mfma_f32_16x16x32_bf16 v[0:3], a[44:47], a[12:15], v[0:3]\n" \
"buffer_load_dwordx4 v219, s[36:39], s82 offen offset:3072 lds\n" \
"v_mfma_f32_16x16x32_bf16 v[4:7], a[48:51], a[8:11], v[4:7]\n" \
"s_xor_b32 s82, s48, s49\n" \
"s_cmp_lt_u32 s48, s50\n" \
"s_cselect_b32 s82, s82, s48\n" \
"ds_read_b64_tr_b16 a[204:205], v195 offset:13312\n" \
"v_mfma_f32_16x16x32_bf16 v[4:7], a[52:55], a[12:15], v[4:7]\n" \
"s_add_u32 s81, s82, s46\n" \
"ds_read_b64_tr_b16 a[206:207], v195 offset:16384\n" \
"v_mfma_f32_16x16x32_bf16 v[12:15], a[48:51], a[16:19], v[12:15]\n" \
"s_add_u32 m0, s86, s79\n" \
"ds_read_b64_tr_b16 a[208:209], v194 offset:2048\n" \
"v_mfma_f32_16x16x32_bf16 v[12:15], a[52:55], a[20:23], v[12:15]\n" \
"buffer_load_dwordx4 v234, s[56:59], s81 offen offset:0 sc1 lds\n" \
"v_mfma_f32_16x16x32_bf16 v[8:11], a[40:43], a[16:19], v[8:11]\n" \
"ds_read_b64_tr_b16 a[210:211], v194 offset:5120\n" \
"v_mfma_f32_16x16x32_bf16 v[8:11], a[44:47], a[20:23], v[8:11]\n" \
"s_lshl_b32 s82, s82, 5\n" \
"ds_read_b64_tr_b16 a[212:213], v194 offset:14336\n" \
"v_mfma_f32_16x16x32_bf16 v[48:51], a[32:35], a[16:19], v[48:51]\n" \
"s_waitcnt lgkmcnt(3)\n" \
"s_add_u32 s82, s82, s47\n" \
"ds_read_b64_tr_b16 a[214:215], v194 offset:17408\n" \
"v_mfma_f32_16x16x32_bf16 v[48:51], a[36:39], a[20:23], v[48:51]\n" \
"buffer_load_dwordx4 v234, s[56:59], s81 offen offset:1024 sc1 lds\n" \
"v_mfma_f32_16x16x32_bf16 v[56:59], a[32:35], a[24:27], v[56:59]\n" \
"ds_read_b64_tr_b16 a[216:217], v195 offset:2048\n" \
"v_mfma_f32_16x16x32_bf16 v[56:59], a[36:39], a[28:31], v[56:59]\n" \
"s_add_u32 s48, s48, 0x1000\n" \
"ds_read_b64_tr_b16 a[218:219], v195 offset:5120\n" \
"v_mfma_f32_16x16x32_bf16 v[16:19], a[40:43], a[24:27], v[16:19]\n" \
"s_sub_u32 s86, s86, 32768\n" \
"s_cselect_b32 s86, 131072, s86\n" \
"ds_read_b64_tr_b16 a[220:221], v195 offset:14336\n" \
"v_mfma_f32_16x16x32_bf16 v[16:19], a[44:47], a[28:31], v[16:19]\n" \
"buffer_load_dwordx4 v234, s[56:59], s81 offen offset:2048 sc1 lds\n" \
"v_mfma_f32_16x16x32_bf16 v[20:23], a[48:51], a[24:27], v[20:23]\n" \
"ds_read_b64_tr_b16 a[222:223], v195 offset:17408\n" \
"v_mfma_f32_16x16x32_bf16 v[20:23], a[52:55], a[28:31], v[20:23]\n" \
"v_mfma_f32_16x16x32_bf16 v[28:31], a[48:51], a[192:195], v[28:31]\n" \
"v_mfma_f32_16x16x32_bf16 v[28:31], a[52:55], a[196:199], v[28:31]\n" \
"buffer_load_dwordx4 v234, s[56:59], s81 offen offset:3072 sc1 lds\n" \
"v_mfma_f32_16x16x32_bf16 v[24:27], a[40:43], a[192:195], v[24:27]\n" \
"v_mfma_f32_16x16x32_bf16 v[24:27], a[44:47], a[196:199], v[24:27]\n" \
"v_mfma_f32_16x16x32_bf16 v[64:67], a[32:35], a[192:195], v[64:67]\n" \
"s_add_u32 m0, m0, 0x4000\n" \
"v_mfma_f32_16x16x32_bf16 v[64:67], a[36:39], a[196:199], v[64:67]\n" \
"buffer_load_dwordx4 v216, s[36:39], s82 offen offset:0 lds\n" \
"s_waitcnt vmcnt(13) lgkmcnt(0)\n" \
"s_barrier\n" \
"v_mfma_f32_16x16x32_bf16 v[44:47], a[200:203], a[8:11], v[44:47]\n" \
"ds_read_b64_tr_b16 a[96:97], v214 offset:16384\n" \
"v_mfma_f32_16x16x32_bf16 v[44:47], a[204:207], a[12:15], v[44:47]\n" \
"ds_read_b64_tr_b16 a[98:99], v214 offset:18432\n" \
"v_mfma_f32_16x16x32_bf16 v[52:55], a[200:203], a[16:19], v[52:55]\n" \
"ds_read_b64_tr_b16 a[100:101], v214 offset:24576\n" \
"v_mfma_f32_16x16x32_bf16 v[52:55], a[204:207], a[20:23], v[52:55]\n" \
"buffer_load_dwordx4 v217, s[36:39], s82 offen offset:1024 lds\n" \
"v_mfma_f32_16x16x32_bf16 v[60:63], a[200:203], a[24:27], v[60:63]\n" \
"ds_read_b64_tr_b16 a[102:103], v214 offset:26624\n" \
"v_cvt_pk_bf16_f32 v112, v0, v1\n" \
"v_mfma_f32_16x16x32_bf16 v[60:63], a[204:207], a[28:31], v[60:63]\n" \
"ds_read_b128 a[64:67], v213 offset:0\n" \
"v_cvt_pk_bf16_f32 v113, v2, v3\n" \
"v_mfma_f32_16x16x32_bf16 v[68:71], a[200:203], a[192:195], v[68:71]\n" \
"ds_read_b128 a[68:71], v213 offset:2048\n" \
"v_cvt_pk_bf16_f32 v114, v4, v5\n" \
"v_mfma_f32_16x16x32_bf16 v[68:71], a[204:207], a[196:199], v[68:71]\n" \
"buffer_load_dwordx4 v218, s[36:39], s82 offen offset:2048 lds\n" \
"v_mfma_f32_16x16x32_bf16 v[72:75], a[0:3], a[208:211], v[72:75]\n" \
"ds_read_b64_tr_b16 a[104:105], v215 offset:16384\n" \
"v_cvt_pk_bf16_f32 v115, v6, v7\n" \
"v_mfma_f32_16x16x32_bf16 v[72:75], a[4:7], a[212:215], v[72:75]\n" \
"ds_read_b64_tr_b16 a[106:107], v215 offset:18432\n" \
"v_cvt_pk_bf16_f32 v116, v8, v9\n" \
"v_mfma_f32_16x16x32_bf16 v[80:83], a[8:11], a[208:211], v[80:83]\n" \
"ds_read_b64_tr_b16 a[108:109], v215 offset:24576\n" \
"v_cvt_pk_bf16_f32 v117, v10, v11\n" \
"v_mfma_f32_16x16x32_bf16 v[80:83], a[12:15], a[212:215], v[80:83]\n" \
"buffer_load_dwordx4 v219, s[36:39], s82 offen offset:3072 lds\n" \
"v_mfma_f32_16x16x32_bf16 v[84:87], a[8:11], a[216:219], v[84:87]\n" \
"s_xor_b32 s82, s48, s49\n" \
"s_cmp_lt_u32 s48, s50\n" \
"s_cselect_b32 s82, s82, s48\n" \
"ds_read_b64_tr_b16 a[110:111], v215 offset:26624\n" \
"v_mfma_f32_16x16x32_bf16 v[84:87], a[12:15], a[220:223], v[84:87]\n" \
"s_add_u32 s81, s82, s46\n" \
"ds_read_b128 a[72:75], v213 offset:256\n" \
"v_cvt_pk_bf16_f32 v118, v12, v13\n" \
"Lpg9653_%=:\n" \
"Lst1x3_%=:\n" \
"s_cbranch_execz Lpg11500_%=\n" \
"v_mfma_f32_16x16x32_bf16 v[92:95], a[16:19], a[216:219], v[92:95]\n" \
"s_add_u32 m0, s86, s79\n" \
"ds_read_b128 a[76:79], v213 offset:2304\n" \
"v_cvt_pk_bf16_f32 v119, v14, v15\n" \
"v_mfma_f32_16x16x32_bf16 v[92:95], a[20:23], a[220:223], v[92:95]\n" \
"buffer_load_dwordx4 v234, s[56:59], s81 offen offset:0 sc1 lds\n" \
"v_mfma_f32_16x16x32_bf16 v[88:91], a[16:19], a[208:211], v[88:91]\n" \
"ds_read_b128 a[80:83], v213 offset:4096\n" \
"v_cvt_pk_bf16_f32 v120, v16, v17\n" \
"v_mfma_f32_16x16x32_bf16 v[88:91], a[20:23], a[212:215], v[88:91]\n" \
"s_lshl_b32 s82, s82, 5\n" \
"ds_read_b128 a[84:87], v213 offset:6144\n" \
"v_cvt_pk_bf16_f32 v121, v18, v19\n" \
"v_mfma_f32_16x16x32_bf16 v[96:99], a[24:27], a[208:211], v[96:99]\n" \
"s_add_u32 s82, s82, s47\n" \
"v_cvt_pk_bf16_f32 v122, v20, v21\n" \
"v_cvt_pk_bf16_f32 v123, v22, v23\n" \
"v_mfma_f32_16x16x32_bf16 v[96:99], a[28:31], a[212:215], v[96:99]\n" \
"buffer_load_dwordx4 v234, s[56:59], s81 offen offset:1024 sc1 lds\n" \
"v_mfma_f32_16x16x32_bf16 v[100:103], a[24:27], a[216:219], v[100:103]\n" \
"v_cvt_pk_bf16_f32 v124, v24, v25\n" \
"v_cvt_pk_bf16_f32 v125, v26, v27\n" \
"v_mfma_f32_16x16x32_bf16 v[100:103], a[28:31], a[220:223], v[100:103]\n" \
"s_add_u32 s48, s48, 0x1000\n" \
"v_cvt_pk_bf16_f32 v126, v28, v29\n" \
"v_cvt_pk_bf16_f32 v127, v30, v31\n" \
"v_mfma_f32_16x16x32_bf16 v[108:111], a[192:195], a[216:219], v[108:111]\n" \
"s_sub_u32 s86, s86, 32768\n" \
"s_cselect_b32 s86, 131072, s86\n" \
"v_cvt_pk_bf16_f32 v128, v32, v33\n" \
"v_mfma_f32_16x16x32_bf16 v[108:111], a[196:199], a[220:223], v[108:111]\n" \
"buffer_load_dwordx4 v234, s[56:59], s81 offen offset:2048 sc1 lds\n" \
"v_mfma_f32_16x16x32_bf16 v[104:107], a[192:195], a[208:211], v[104:107]\n" \
"v_cvt_pk_bf16_f32 v129, v34, v35\n" \
"v_xor_b32 v145, 0x2000, v223\n" \
"v_mfma_f32_16x16x32_bf16 v[104:107], a[196:199], a[212:215], v[104:107]\n" \
"s_waitcnt lgkmcnt(0)\n" \
"v_cvt_pk_bf16_f32 v192, v72, v73\n" \
"v_cvt_pk_bf16_f32 v193, v74, v75\n" \
"ds_write_b64 v145, v[128:129]\n" \
"s_nop 0\n" \
"ds_write_b64 v145, v[192:193] offset:8\n" \
"v_cvt_pk_bf16_f32 v132, v40, v41\n" \
"v_cvt_pk_bf16_f32 v133, v42, v43\n" \
"v_cvt_pk_bf16_f32 v134, v44, v45\n" \
"v_cvt_pk_bf16_f32 v135, v46, v47\n" \
"v_cvt_pk_bf16_f32 v136, v48, v49\n" \
"v_cvt_pk_bf16_f32 v137, v50, v51\n" \
"v_cvt_pk_bf16_f32 v138, v52, v53\n" \
"v_cvt_pk_bf16_f32 v139, v54, v55\n" \
"v_cvt_pk_bf16_f32 v140, v56, v57\n" \
"v_cvt_pk_bf16_f32 v141, v58, v59\n" \
"v_cvt_pk_bf16_f32 v142, v60, v61\n" \
"v_cvt_pk_bf16_f32 v143, v62, v63\n" \
"v_mfma_f32_16x16x32_bf16 v[0:3], a[96:99], a[64:67], 0\n" \
"ds_read_b128 a[88:91], v213 offset:4352\n" \
"v_mfma_f32_16x16x32_bf16 v[0:3], a[100:103], a[68:71], v[0:3]\n" \
"ds_read_b128 a[92:95], v213 offset:6400\n" \
"v_mfma_f32_16x16x32_bf16 v[4:7], a[104:107], a[64:67], 0\n" \
"ds_read_b64_tr_b16 a[112:113], v214 offset:16896\n" \
"v_mfma_f32_16x16x32_bf16 v[4:7], a[108:111], a[68:71], v[4:7]\n" \
"ds_read_b64_tr_b16 a[114:115], v214 offset:18944\n" \
"v_mfma_f32_16x16x32_bf16 v[20:23], a[104:107], a[72:75], 0\n" \
"buffer_load_dwordx4 v234, s[56:59], s81 offen offset:3072 sc1 lds\n" \
"v_mfma_f32_16x16x32_bf16 v[20:23], a[108:111], a[76:79], v[20:23]\n" \
"ds_read_b64_tr_b16 a[116:117], v214 offset:25088\n" \
"v_cvt_pk_bf16_f32 v64, v64, v65\n" \
"v_cvt_pk_bf16_f32 v65, v66, v67\n" \
"v_cvt_pk_bf16_f32 v66, v68, v69\n" \
"v_mfma_f32_16x16x32_bf16 v[16:19], a[96:99], a[72:75], 0\n" \
"ds_read_b64_tr_b16 a[118:119], v214 offset:27136\n" \
"v_cvt_pk_bf16_f32 v67, v70, v71\n" \
"v_mfma_f32_16x16x32_bf16 v[16:19], a[100:103], a[76:79], v[16:19]\n" \
"s_add_u32 m0, m0, 0x4000\n" \
"ds_read_b64_tr_b16 a[120:121], v215 offset:16896\n" \
"v_cvt_pk_bf16_f32 v194, v80, v81\n" \
"v_cvt_pk_bf16_f32 v195, v82, v83\n" \
"v_cvt_pk_bf16_f32 v74, v84, v85\n" \
"v_cvt_pk_bf16_f32 v75, v86, v87\n" \
"v_mfma_f32_16x16x32_bf16 v[32:35], a[96:99], a[80:83], 0\n" \
"buffer_load_dwordx4 v216, s[36:39], s82 offen offset:0 lds\n" \
"v_mfma_f32_16x16x32_bf16 v[32:35], a[100:103], a[84:87], v[32:35]\n" \
"s_sub_u32 s85, s85, 32768\n" \
"s_cselect_b32 s85, 131072, s85\n" \
"ds_read_b64_tr_b16 a[122:123], v215 offset:18944\n" \
"v_cvt_pk_bf16_f32 v196, v88, v89\n" \
"v_cvt_pk_bf16_f32 v197, v90, v91\n" \
"v_mfma_f32_16x16x32_bf16 v[176:179], v[64:67], v[124:127], v[152:155]\n" \
"v_mfma_f32_16x16x32_bf16 v[172:175], v[140:143], v[124:127], v[156:159]\n" \
"v_mfma_f32_16x16x32_bf16 v[168:171], v[140:143], v[120:123], v[152:155]\n" \
"v_mfma_f32_16x16x32_bf16 v[164:167], v[136:139], v[120:123], v[156:159]\n" \
"v_mfma_f32_16x16x32_bf16 v[160:163], v[136:139], v[116:119], v[152:155]\n" \
"v_mfma_f32_16x16x32_bf16 v[156:159], v[132:135], v[116:119], v[156:159]\n" \
"v_mfma_f32_16x16x32_bf16 v[152:155], v[132:135], v[112:115], v[152:155]\n" \
"v_mfma_f32_16x16x32_bf16 v[36:39], a[104:107], a[80:83], 0\n" \
"ds_read_b64_tr_b16 a[124:125], v215 offset:25088\n" \
"v_add_u32 v213, s85, v211\n" \
"v_mfma_f32_16x16x32_bf16 v[36:39], a[108:111], a[84:87], v[36:39]\n" \
"s_waitcnt lgkmcnt(3)\n" \
"ds_read_b64_tr_b16 a[126:127], v215 offset:27136\n" \
"v_mfma_f32_16x16x32_bf16 v[52:55], a[104:107], a[88:91], 0\n" \
"buffer_load_dwordx4 v217, s[36:39], s82 offen offset:1024 lds\n" \
"v_mfma_f32_16x16x32_bf16 v[52:55], a[108:111], a[92:95], v[52:55]\n" \
"v_add_u32 v214, s85, v212\n" \
"v_xor_b32 v215, 32, v214\n" \
"v_mfma_f32_16x16x32_bf16 v[48:51], a[96:99], a[88:91], 0\n" \
"buffer_load_dwordx4 v218, s[36:39], s82 offen offset:2048 lds\n" \
"v_mfma_f32_16x16x32_bf16 v[48:51], a[100:103], a[92:95], v[48:51]\n" \
"s_waitcnt vmcnt(15) lgkmcnt(0)\n" \
"s_barrier\n" \
"v_mfma_f32_16x16x32_bf16 v[8:11], a[112:115], a[64:67], 0\n" \
"ds_read_b64 v[130:131], v223\n" \
"ds_read_b64_tr_b16 a[96:97], v214 offset:16384\n" \
"ds_read_b64_tr_b16 a[98:99], v214 offset:18432\n" \
"v_mfma_f32_16x16x32_bf16 v[8:11], a[116:119], a[68:71], v[8:11]\n" \
"ds_read_b64 v[72:73], v223 offset:8\n" \
"ds_read_b64_tr_b16 a[100:101], v214 offset:24576\n" \
"ds_read_b64_tr_b16 a[102:103], v214 offset:26624\n" \
"v_mfma_f32_16x16x32_bf16 v[12:15], a[120:123], a[64:67], 0\n" \
"buffer_load_dwordx4 v219, s[36:39], s82 offen offset:3072 lds\n" \
"v_mfma_f32_16x16x32_bf16 v[12:15], a[124:127], a[68:71], v[12:15]\n" \
"s_xor_b32 s82, s48, s49\n" \
"s_cmp_lt_u32 s48, s50\n" \
"s_cselect_b32 s82, s82, s48\n" \
"ds_read_b128 a[64:67], v213 offset:0\n" \
"v_mfma_f32_16x16x32_bf16 v[28:31], a[120:123], a[72:75], 0\n" \
"s_add_u32 s81, s82, s46\n" \
"ds_read_b128 a[68:71], v213 offset:2048\n" \
"v_mfma_f32_16x16x32_bf16 v[28:31], a[124:127], a[76:79], v[28:31]\n" \
"s_add_u32 m0, s86, s79\n" \
"ds_read_b64_tr_b16 a[104:105], v215 offset:16384\n" \
"ds_read_b64_tr_b16 a[106:107], v215 offset:18432\n" \
"v_mfma_f32_16x16x32_bf16 v[24:27], a[112:115], a[72:75], 0\n" \
"buffer_load_dwordx4 v234, s[56:59], s81 offen offset:0 sc1 lds\n" \
"v_mfma_f32_16x16x32_bf16 v[24:27], a[116:119], a[76:79], v[24:27]\n" \
"s_waitcnt lgkmcnt(4)\n" \
"ds_read_b128 a[72:75], v213 offset:256\n" \
"v_mfma_f32_16x16x32_bf16 v[40:43], a[112:115], a[80:83], 0\n" \
"s_lshl_b32 s82, s82, 5\n" \
"ds_read_b128 a[76:79], v213 offset:2304\n" \
"v_mfma_f32_16x16x32_bf16 v[148:151], v[128:131], v[112:115], v[148:151]\n" \
"v_max_f32 v172, v172, v176\n" \
"v_max_f32 v173, v173, v177\n" \
"v_max_f32 v174, v174, v178\n" \
"v_max_f32 v175, v175, v179\n" \
"v_max_f32 v164, v164, v168\n" \
"v_max_f32 v165, v165, v169\n" \
"v_max_f32 v166, v166, v170\n" \
"v_max_f32 v167, v167, v171\n" \
"v_max_f32 v156, v156, v160\n" \
"v_max_f32 v157, v157, v161\n" \
"v_max_f32 v158, v158, v162\n" \
"v_max_f32 v159, v159, v163\n" \
"v_max_f32 v148, v148, v152\n" \
"v_max_f32 v149, v149, v153\n" \
"v_max_f32 v150, v150, v154\n" \
"v_max_f32 v151, v151, v155\n" \
"v_max3_f32 v183, v172, v173, v174\n" \
"v_max3_f32 v182, v164, v165, v166\n" \
"v_max3_f32 v181, v156, v157, v158\n" \
"v_mfma_f32_16x16x32_bf16 v[40:43], a[116:119], a[84:87], v[40:43]\n" \
"s_add_u32 s82, s82, s47\n" \
"ds_read_b64_tr_b16 a[108:109], v215 offset:24576\n" \
"ds_read_b64_tr_b16 a[110:111], v215 offset:26624\n" \
"v_max3_f32 v180, v148, v149, v150\n" \
"v_mfma_f32_16x16x32_bf16 v[44:47], a[120:123], a[80:83], 0\n" \
"buffer_load_dwordx4 v234, s[56:59], s81 offen offset:1024 sc1 lds\n" \
"v_mfma_f32_16x16x32_bf16 v[44:47], a[124:127], a[84:87], v[44:47]\n" \
"s_add_u32 s48, s48, 0x1000\n" \
"s_sub_u32 s86, s86, 32768\n" \
"s_cselect_b32 s86, 131072, s86\n" \
"ds_read_b128 a[80:83], v213 offset:4096\n" \
"v_max_f32 v183, v183, v175\n" \
"v_max_f32 v182, v182, v167\n" \
"v_max_f32 v181, v181, v159\n" \
"v_mfma_f32_16x16x32_bf16 v[60:63], a[120:123], a[88:91], 0\n" \
"s_waitcnt lgkmcnt(4)\n" \
"buffer_load_dwordx4 v234, s[56:59], s81 offen offset:2048 sc1 lds\n" \
"v_max_f32 v180, v180, v151\n" \
"v_mfma_f32_16x16x32_bf16 v[60:63], a[124:127], a[92:95], v[60:63]\n" \
"ds_read_b128 a[84:87], v213 offset:6144\n" \
"v_cvt_pk_bf16_f32 v76, v92, v93\n" \
"v_cvt_pk_bf16_f32 v77, v94, v95\n" \
"v_mfma_f32_16x16x32_bf16 v[56:59], a[112:115], a[88:91], 0\n" \
"buffer_load_dwordx4 v234, s[56:59], s81 offen offset:3072 sc1 lds\n" \
"v_mfma_f32_16x16x32_bf16 v[56:59], a[116:119], a[92:95], v[56:59]\n" \
"s_getpc_b64 s[44:45]\n" \
"Lop_%=:\n" \
"s_add_u32 s44, s44, Lor_%=-Lop_%=\n" \
"s_addc_u32 s45, s45, 0\n" \
"v_permlane32_swap_b32 v180, v181\n" \
"v_permlane32_swap_b32 v182, v183\n" \
"v_max_f32 v180, v180, v181\n" \
"v_mfma_f32_16x16x32_bf16 v[0:3], a[96:99], a[64:67], v[0:3]\n" \
"ds_read_b128 a[88:91], v213 offset:4352\n" \
"v_max_f32 v182, v182, v183\n" \
"v_mfma_f32_16x16x32_bf16 v[0:3], a[100:103], a[68:71], v[0:3]\n" \
"s_waitcnt lgkmcnt(2)\n" \
"ds_read_b128 a[92:95], v213 offset:6400\n" \
"v_cvt_pk_bf16_f32 v198, v96, v97\n" \
"v_cvt_pk_bf16_f32 v199, v98, v99\n" \
"v_permlane16_swap_b32 v180, v182\n" \
"v_mfma_f32_16x16x32_bf16 v[4:7], a[104:107], a[64:67], v[4:7]\n" \
"ds_read_b64_tr_b16 a[112:113], v214 offset:16896\n" \
"v_max_f32 v180, v180, v182\n" \
"v_mfma_f32_16x16x32_bf16 v[4:7], a[108:111], a[68:71], v[4:7]\n" \
"s_add_u32 m0, m0, 0x4000\n" \
"ds_read_b64_tr_b16 a[114:115], v214 offset:18944\n" \
"v_mul_f32 v180, v180, v144\n" \
"v_cvt_pk_bf16_f32 v78, v100, v101\n" \
"v_cvt_pk_bf16_f32 v79, v102, v103\n" \
"v_mov_b32 v182, v180\n" \
"v_mfma_f32_16x16x32_bf16 v[20:23], a[104:107], a[72:75], v[20:23]\n" \
"buffer_load_dwordx4 v216, s[36:39], s82 offen offset:0 lds\n" \
"v_mfma_f32_16x16x32_bf16 v[20:23], a[108:111], a[76:79], v[20:23]\n" \
"ds_read_b64_tr_b16 a[116:117], v214 offset:25088\n" \
"v_cvt_pk_bf16_f32 v200, v104, v105\n" \
"v_cvt_pk_bf16_f32 v201, v106, v107\n" \
"v_permlane16_swap_b32 v180, v182\n" \
"v_mfma_f32_16x16x32_bf16 v[16:19], a[96:99], a[72:75], v[16:19]\n" \
"ds_read_b64_tr_b16 a[118:119], v214 offset:27136\n" \
"v_mov_b32 v181, v180\n" \
"v_mfma_f32_16x16x32_bf16 v[16:19], a[100:103], a[76:79], v[16:19]\n" \
"s_waitcnt lgkmcnt(3)\n" \
"ds_read_b64_tr_b16 a[120:121], v215 offset:16896\n" \
"v_mov_b32 v183, v182\n" \
"v_cvt_pk_bf16_f32 v80, v108, v109\n" \
"v_cvt_pk_bf16_f32 v81, v110, v111\n" \
"v_permlane32_swap_b32 v180, v181\n" \
"v_mfma_f32_16x16x32_bf16 v[32:35], a[96:99], a[80:83], v[32:35]\n" \
"buffer_load_dwordx4 v217, s[36:39], s82 offen offset:1024 lds\n" \
"v_mfma_f32_16x16x32_bf16 v[32:35], a[100:103], a[84:87], v[32:35]\n" \
"s_sub_u32 s85, s85, 32768\n" \
"s_cselect_b32 s85, 131072, s85\n" \
"ds_read_b64_tr_b16 a[122:123], v215 offset:18944\n" \
"v_permlane32_swap_b32 v182, v183\n" \
"v_fma_f32 v172, v172, v144, -v183\n" \
"v_mfma_f32_16x16x32_bf16 v[36:39], a[104:107], a[80:83], v[36:39]\n" \
"ds_read_b64_tr_b16 a[124:125], v215 offset:25088\n" \
"v_add_u32 v213, s85, v211\n" \
"v_mfma_f32_16x16x32_bf16 v[36:39], a[108:111], a[84:87], v[36:39]\n" \
"ds_read_b64_tr_b16 a[126:127], v215 offset:27136\n" \
"v_fma_f32 v164, v164, v144, -v182\n" \
"v_fma_f32 v156, v156, v144, -v181\n" \
"v_fma_f32 v148, v148, v144, -v180\n" \
"v_fma_f32 v173, v173, v144, -v183\n" \
"v_mfma_f32_16x16x32_bf16 v[52:55], a[104:107], a[88:91], v[52:55]\n" \
"buffer_load_dwordx4 v218, s[36:39], s82 offen offset:2048 lds\n" \
"v_mfma_f32_16x16x32_bf16 v[52:55], a[108:111], a[92:95], v[52:55]\n" \
"v_add_u32 v214, s85, v212\n" \
"v_xor_b32 v215, 32, v214\n" \
"v_fma_f32 v165, v165, v144, -v182\n" \
"v_fma_f32 v157, v157, v144, -v181\n" \
"v_mfma_f32_16x16x32_bf16 v[48:51], a[96:99], a[88:91], v[48:51]\n" \
"buffer_load_dwordx4 v219, s[36:39], s82 offen offset:3072 lds\n" \
"v_mfma_f32_16x16x32_bf16 v[48:51], a[100:103], a[92:95], v[48:51]\n" \
"s_xor_b32 s82, s48, s49\n" \
"s_cmp_lt_u32 s48, s50\n" \
"s_cselect_b32 s82, s82, s48\n" \
"v_fma_f32 v149, v149, v144, -v180\n" \
"v_fma_f32 v174, v174, v144, -v183\n" \
"v_fma_f32 v166, v166, v144, -v182\n" \
"s_waitcnt vmcnt(16) lgkmcnt(0)\n" \
"s_barrier\n" \
"v_mfma_f32_16x16x32_bf16 v[8:11], a[112:115], a[64:67], v[8:11]\n" \
"s_add_u32 s81, s82, s46\n" \
"ds_read_b64_tr_b16 a[96:97], v214 offset:16384\n" \
"ds_read_b64_tr_b16 a[98:99], v214 offset:18432\n" \
"v_mfma_f32_16x16x32_bf16 v[8:11], a[116:119], a[68:71], v[8:11]\n" \
"s_add_u32 m0, s86, s79\n" \
"ds_read_b64_tr_b16 a[100:101], v214 offset:24576\n" \
"ds_read_b64_tr_b16 a[102:103], v214 offset:26624\n" \
"v_fma_f32 v158, v158, v144, -v181\n" \
"v_fma_f32 v150, v150, v144, -v180\n" \
"v_mfma_f32_16x16x32_bf16 v[12:15], a[120:123], a[64:67], v[12:15]\n" \
"buffer_load_dwordx4 v234, s[56:59], s81 offen offset:0 sc1 lds\n" \
"v_mfma_f32_16x16x32_bf16 v[12:15], a[124:127], a[68:71], v[12:15]\n" \
"ds_read_b128 a[64:67], v213 offset:0\n" \
"v_fma_f32 v175, v175, v144, -v183\n" \
"v_fma_f32 v167, v167, v144, -v182\n" \
"v_fma_f32 v159, v159, v144, -v181\n" \
"v_fma_f32 v151, v151, v144, -v180\n" \
"v_mfma_f32_16x16x32_bf16 v[28:31], a[120:123], a[72:75], v[28:31]\n" \
"s_lshl_b32 s82, s82, 5\n" \
"ds_read_b128 a[68:71], v213 offset:2048\n" \
"v_mfma_f32_16x16x32_bf16 v[28:31], a[124:127], a[76:79], v[28:31]\n" \
"s_add_u32 s82, s82, s47\n" \
"ds_read_b64_tr_b16 a[104:105], v215 offset:16384\n" \
"ds_read_b64_tr_b16 a[106:107], v215 offset:18432\n" \
"v_exp_f32 v172, v172\n" \
"v_mfma_f32_16x16x32_bf16 v[24:27], a[112:115], a[72:75], v[24:27]\n" \
"buffer_load_dwordx4 v234, s[56:59], s81 offen offset:1024 sc1 lds\n" \
"v_mfma_f32_16x16x32_bf16 v[24:27], a[116:119], a[76:79], v[24:27]\n" \
"ds_read_b128 a[72:75], v213 offset:256\n" \
"v_exp_f32 v164, v164\n" \
"v_exp_f32 v156, v156\n" \
"v_mfma_f32_16x16x32_bf16 v[40:43], a[112:115], a[80:83], v[40:43]\n" \
"s_add_u32 s48, s48, 0x1000\n" \
"ds_read_b128 a[76:79], v213 offset:2304\n" \
"v_mfma_f32_16x16x32_bf16 v[40:43], a[116:119], a[84:87], v[40:43]\n" \
"s_sub_u32 s86, s86, 32768\n" \
"s_cselect_b32 s86, 131072, s86\n" \
"ds_read_b64_tr_b16 a[108:109], v215 offset:24576\n" \
"ds_read_b64_tr_b16 a[110:111], v215 offset:26624\n" \
"v_exp_f32 v148, v148\n" \
"v_mfma_f32_16x16x32_bf16 v[44:47], a[120:123], a[80:83], v[44:47]\n" \
"buffer_load_dwordx4 v234, s[56:59], s81 offen offset:2048 sc1 lds\n" \
"v_mfma_f32_16x16x32_bf16 v[44:47], a[124:127], a[84:87], v[44:47]\n" \
"ds_read_b128 a[80:83], v213 offset:4096\n" \
"v_exp_f32 v173, v173\n" \
"v_exp_f32 v165, v165\n" \
"v_mfma_f32_16x16x32_bf16 v[60:63], a[120:123], a[88:91], v[60:63]\n" \
"s_waitcnt lgkmcnt(4)\n" \
"buffer_load_dwordx4 v234, s[56:59], s81 offen offset:3072 sc1 lds\n" \
"v_mfma_f32_16x16x32_bf16 v[60:63], a[124:127], a[92:95], v[60:63]\n" \
"s_add_u32 m0, m0, 0x4000\n" \
"ds_read_b128 a[84:87], v213 offset:6144\n" \
"v_exp_f32 v157, v157\n" \
"v_mfma_f32_16x16x32_bf16 v[56:59], a[112:115], a[88:91], v[56:59]\n" \
"buffer_load_dwordx4 v216, s[36:39], s82 offen offset:0 lds\n" \
"v_mfma_f32_16x16x32_bf16 v[56:59], a[116:119], a[92:95], v[56:59]\n" \
"v_exp_f32 v149, v149\n" \
"v_exp_f32 v174, v174\n" \
"v_exp_f32 v166, v166\n" \
"v_mfma_f32_16x16x32_bf16 v[0:3], a[96:99], a[64:67], v[0:3]\n" \
"ds_read_b128 a[88:91], v213 offset:4352\n" \
"v_mfma_f32_16x16x32_bf16 v[0:3], a[100:103], a[68:71], v[0:3]\n" \
"s_waitcnt lgkmcnt(2)\n" \
"ds_read_b128 a[92:95], v213 offset:6400\n" \
"v_exp_f32 v158, v158\n" \
"v_mfma_f32_16x16x32_bf16 v[4:7], a[104:107], a[64:67], v[4:7]\n" \
"ds_read_b64_tr_b16 a[112:113], v214 offset:16896\n" \
"v_mfma_f32_16x16x32_bf16 v[4:7], a[108:111], a[68:71], v[4:7]\n" \
"ds_read_b64_tr_b16 a[114:115], v214 offset:18944\n" \
"v_exp_f32 v150, v150\n" \
"v_exp_f32 v175, v175\n" \
"v_mfma_f32_16x16x32_bf16 v[20:23], a[104:107], a[72:75], v[20:23]\n" \
"buffer_load_dwordx4 v217, s[36:39], s82 offen offset:1024 lds\n" \
"v_mfma_f32_16x16x32_bf16 v[20:23], a[108:111], a[76:79], v[20:23]\n" \
"ds_read_b64_tr_b16 a[116:117], v214 offset:25088\n" \
"v_exp_f32 v167, v167\n" \
"v_exp_f32 v159, v159\n" \
"v_mfma_f32_16x16x32_bf16 v[16:19], a[96:99], a[72:75], v[16:19]\n" \
"ds_read_b64_tr_b16 a[118:119], v214 offset:27136\n" \
"v_mfma_f32_16x16x32_bf16 v[16:19], a[100:103], a[76:79], v[16:19]\n" \
"s_waitcnt lgkmcnt(3)\n" \
"ds_read_b64_tr_b16 a[120:121], v215 offset:16896\n" \
"v_exp_f32 v151, v151\n" \
"v_add_f32 v191, v172, v173\n" \
"v_add_f32 v190, v164, v165\n" \
"v_mfma_f32_16x16x32_bf16 v[32:35], a[96:99], a[80:83], v[32:35]\n" \
"buffer_load_dwordx4 v218, s[36:39], s82 offen offset:2048 lds\n" \
"v_mfma_f32_16x16x32_bf16 v[32:35], a[100:103], a[84:87], v[32:35]\n" \
"s_sub_u32 s85, s85, 32768\n" \
"s_cselect_b32 s85, 131072, s85\n" \
"ds_read_b64_tr_b16 a[122:123], v215 offset:18944\n" \
"v_add_f32 v189, v156, v157\n" \
"v_add_f32 v188, v148, v149\n" \
"v_mfma_f32_16x16x32_bf16 v[36:39], a[104:107], a[80:83], v[36:39]\n" \
"ds_read_b64_tr_b16 a[124:125], v215 offset:25088\n" \
"v_add_u32 v213, s85, v211\n" \
"v_mfma_f32_16x16x32_bf16 v[36:39], a[108:111], a[84:87], v[36:39]\n" \
"ds_read_b64_tr_b16 a[126:127], v215 offset:27136\n" \
"v_add_f32 v191, v191, v174\n" \
"v_add_f32 v190, v190, v166\n" \
"v_add_f32 v189, v189, v158\n" \
"v_add_f32 v188, v188, v150\n" \
"v_mfma_f32_16x16x32_bf16 v[52:55], a[104:107], a[88:91], v[52:55]\n" \
"buffer_load_dwordx4 v219, s[36:39], s82 offen offset:3072 lds\n" \
"v_mfma_f32_16x16x32_bf16 v[52:55], a[108:111], a[92:95], v[52:55]\n" \
"s_xor_b32 s82, s48, s49\n" \
"s_cmp_lt_u32 s48, s50\n" \
"s_cselect_b32 s82, s82, s48\n" \
"s_add_u32 s81, s82, s46\n" \
"s_add_u32 m0, s86, s79\n" \
"v_add_u32 v214, s85, v212\n" \
"v_xor_b32 v215, 32, v214\n" \
"v_add_f32 v191, v191, v175\n" \
"v_add_f32 v190, v190, v167\n" \
"v_mfma_f32_16x16x32_bf16 v[48:51], a[96:99], a[88:91], v[48:51]\n" \
"buffer_load_dwordx4 v234, s[56:59], s81 offen offset:0 sc1 lds\n" \
"v_mfma_f32_16x16x32_bf16 v[48:51], a[100:103], a[92:95], v[48:51]\n" \
"v_add_f32 v189, v189, v159\n" \
"v_add_f32 v188, v188, v151\n" \
"s_waitcnt vmcnt(17) lgkmcnt(0)\n" \
"s_barrier\n" \
"v_mfma_f32_16x16x32_bf16 v[8:11], a[112:115], a[64:67], v[8:11]\n" \
"s_lshl_b32 s82, s82, 5\n" \
"ds_read_b64_tr_b16 a[96:97], v214 offset:16384\n" \
"ds_read_b64_tr_b16 a[98:99], v214 offset:18432\n" \
"v_mfma_f32_16x16x32_bf16 v[8:11], a[116:119], a[68:71], v[8:11]\n" \
"s_add_u32 s82, s82, s47\n" \
"ds_read_b64_tr_b16 a[100:101], v214 offset:24576\n" \
"ds_read_b64_tr_b16 a[102:103], v214 offset:26624\n" \
"v_permlane32_swap_b32 v188, v189\n" \
"v_permlane32_swap_b32 v190, v191\n" \
"v_add_f32 v188, v188, v189\n" \
"v_mfma_f32_16x16x32_bf16 v[12:15], a[120:123], a[64:67], v[12:15]\n" \
"buffer_load_dwordx4 v234, s[56:59], s81 offen offset:1024 sc1 lds\n" \
"v_mfma_f32_16x16x32_bf16 v[12:15], a[124:127], a[68:71], v[12:15]\n" \
"ds_read_b128 a[64:67], v213 offset:0\n" \
"v_add_f32 v190, v190, v191\n" \
"v_mfma_f32_16x16x32_bf16 v[28:31], a[120:123], a[72:75], v[28:31]\n" \
"s_add_u32 s48, s48, 0x1000\n" \
"ds_read_b128 a[68:71], v213 offset:2048\n" \
"v_mfma_f32_16x16x32_bf16 v[28:31], a[124:127], a[76:79], v[28:31]\n" \
"s_sub_u32 s86, s86, 32768\n" \
"s_cselect_b32 s86, 131072, s86\n" \
"ds_read_b64_tr_b16 a[104:105], v215 offset:16384\n" \
"ds_read_b64_tr_b16 a[106:107], v215 offset:18432\n" \
"v_permlane16_swap_b32 v188, v190\n" \
"v_add_f32 v188, v188, v190\n" \
"v_rcp_f32 v188, v188\n" \
"v_mfma_f32_16x16x32_bf16 v[24:27], a[112:115], a[72:75], v[24:27]\n" \
"buffer_load_dwordx4 v234, s[56:59], s81 offen offset:2048 sc1 lds\n" \
"v_mfma_f32_16x16x32_bf16 v[24:27], a[116:119], a[76:79], v[24:27]\n" \
"ds_read_b128 a[72:75], v213 offset:256\n" \
"v_mov_b32 v190, v188\n" \
"v_mfma_f32_16x16x32_bf16 v[40:43], a[112:115], a[80:83], v[40:43]\n" \
"ds_read_b128 a[76:79], v213 offset:2304\n" \
"v_mfma_f32_16x16x32_bf16 v[40:43], a[116:119], a[84:87], v[40:43]\n" \
"ds_read_b64_tr_b16 a[108:109], v215 offset:24576\n" \
"ds_read_b64_tr_b16 a[110:111], v215 offset:26624\n" \
"v_permlane16_swap_b32 v188, v190\n" \
"v_mov_b32 v189, v188\n" \
"v_mov_b32 v191, v190\n" \
"v_mfma_f32_16x16x32_bf16 v[44:47], a[120:123], a[80:83], v[44:47]\n" \
"buffer_load_dwordx4 v234, s[56:59], s81 offen offset:3072 sc1 lds\n" \
"v_mfma_f32_16x16x32_bf16 v[44:47], a[124:127], a[84:87], v[44:47]\n" \
"s_add_u32 m0, m0, 0x4000\n" \
"ds_read_b128 a[80:83], v213 offset:4096\n" \
"v_permlane32_swap_b32 v188, v189\n" \
"v_permlane32_swap_b32 v190, v191\n" \
"v_mul_f32 v172, v172, v191\n" \
"v_mul_f32 v164, v164, v190\n" \
"v_mul_f32 v156, v156, v189\n" \
"v_mul_f32 v148, v148, v188\n" \
"v_mul_f32 v173, v173, v191\n" \
"v_mfma_f32_16x16x32_bf16 v[60:63], a[120:123], a[88:91], v[60:63]\n" \
"s_waitcnt lgkmcnt(4)\n" \
"buffer_load_dwordx4 v216, s[36:39], s82 offen offset:0 lds\n" \
"v_mul_f32 v165, v165, v190\n" \
"v_mfma_f32_16x16x32_bf16 v[60:63], a[124:127], a[92:95], v[60:63]\n" \
"ds_read_b128 a[84:87], v213 offset:6144\n" \
"v_mul_f32 v157, v157, v189\n" \
"v_mul_f32 v149, v149, v188\n" \
"v_mfma_f32_16x16x32_bf16 v[56:59], a[112:115], a[88:91], v[56:59]\n" \
"buffer_load_dwordx4 v217, s[36:39], s82 offen offset:1024 lds\n" \
"v_mfma_f32_16x16x32_bf16 v[56:59], a[116:119], a[92:95], v[56:59]\n" \
"v_mul_f32 v174, v174, v191\n" \
"v_mul_f32 v166, v166, v190\n" \
"v_mul_f32 v158, v158, v189\n" \
"v_mul_f32 v150, v150, v188\n" \
"v_mul_f32 v175, v175, v191\n" \
"v_mfma_f32_16x16x32_bf16 v[0:3], a[96:99], a[64:67], v[0:3]\n" \
"ds_read_b128 a[88:91], v213 offset:4352\n" \
"v_mul_f32 v167, v167, v190\n" \
"v_mfma_f32_16x16x32_bf16 v[0:3], a[100:103], a[68:71], v[0:3]\n" \
"s_waitcnt lgkmcnt(2)\n" \
"ds_read_b128 a[92:95], v213 offset:6400\n" \
"v_mul_f32 v159, v159, v189\n" \
"v_mul_f32 v151, v151, v188\n" \
"v_cvt_pk_bf16_f32 v178, v172, v173\n" \
"v_mfma_f32_16x16x32_bf16 v[4:7], a[104:107], a[64:67], v[4:7]\n" \
"ds_read_b64_tr_b16 a[112:113], v214 offset:16896\n" \
"v_cvt_pk_bf16_f32 v179, v174, v175\n" \
"v_mfma_f32_16x16x32_bf16 v[4:7], a[108:111], a[68:71], v[4:7]\n" \
"ds_read_b64_tr_b16 a[114:115], v214 offset:18944\n" \
"v_cvt_pk_bf16_f32 v170, v164, v165\n" \
"v_cvt_pk_bf16_f32 v171, v166, v167\n" \
"v_cvt_pk_bf16_f32 v162, v156, v157\n" \
"v_cvt_pk_bf16_f32 v163, v158, v159\n" \
"v_mfma_f32_16x16x32_bf16 v[20:23], a[104:107], a[72:75], v[20:23]\n" \
"buffer_load_dwordx4 v218, s[36:39], s82 offen offset:2048 lds\n" \
"v_mfma_f32_16x16x32_bf16 v[20:23], a[108:111], a[76:79], v[20:23]\n" \
"ds_read_b64_tr_b16 a[116:117], v214 offset:25088\n" \
"v_cvt_pk_bf16_f32 v154, v148, v149\n" \
"v_cvt_pk_bf16_f32 v155, v150, v151\n" \
"v_and_b32 v176, v178, v184\n" \
"v_mfma_f32_16x16x32_bf16 v[16:19], a[96:99], a[72:75], v[16:19]\n" \
"ds_read_b64_tr_b16 a[118:119], v214 offset:27136\n" \
"v_and_b32 v177, v179, v185\n" \
"v_mfma_f32_16x16x32_bf16 v[16:19], a[100:103], a[76:79], v[16:19]\n" \
"s_waitcnt lgkmcnt(3)\n" \
"ds_read_b64_tr_b16 a[120:121], v215 offset:16896\n" \
"v_xor_b32 v178, v178, v176\n" \
"v_xor_b32 v179, v179, v177\n" \
"v_and_b32 v168, v170, v184\n" \
"v_and_b32 v169, v171, v185\n" \
"v_mfma_f32_16x16x32_bf16 v[32:35], a[96:99], a[80:83], v[32:35]\n" \
"buffer_load_dwordx4 v219, s[36:39], s82 offen offset:3072 lds\n" \
"v_mfma_f32_16x16x32_bf16 v[32:35], a[100:103], a[84:87], v[32:35]\n" \
"s_sub_u32 s85, s85, 32768\n" \
"s_cselect_b32 s85, 131072, s85\n" \
"s_xor_b32 s82, s48, s49\n" \
"s_cmp_lt_u32 s48, s50\n" \
"s_cselect_b32 s82, s82, s48\n" \
"ds_read_b64_tr_b16 a[122:123], v215 offset:18944\n" \
"v_xor_b32 v170, v170, v168\n" \
"v_xor_b32 v171, v171, v169\n" \
"v_mfma_f32_16x16x32_bf16 v[36:39], a[104:107], a[80:83], v[36:39]\n" \
"s_add_u32 s81, s82, s46\n" \
"ds_read_b64_tr_b16 a[124:125], v215 offset:25088\n" \
"v_add_u32 v213, s85, v211\n" \
"v_mfma_f32_16x16x32_bf16 v[36:39], a[108:111], a[84:87], v[36:39]\n" \
"s_add_u32 m0, s86, s79\n" \
"ds_read_b64_tr_b16 a[126:127], v215 offset:27136\n" \
"v_and_b32 v160, v162, v184\n" \
"v_and_b32 v161, v163, v185\n" \
"v_xor_b32 v162, v162, v160\n" \
"v_xor_b32 v163, v163, v161\n" \
"v_mfma_f32_16x16x32_bf16 v[52:55], a[104:107], a[88:91], v[52:55]\n" \
"buffer_load_dwordx4 v234, s[56:59], s81 offen offset:0 sc1 lds\n" \
"v_mfma_f32_16x16x32_bf16 v[52:55], a[108:111], a[92:95], v[52:55]\n" \
"s_lshl_b32 s82, s82, 5\n" \
"s_add_u32 s82, s82, s47\n" \
"v_add_u32 v214, s85, v212\n" \
"v_xor_b32 v215, 32, v214\n" \
"v_and_b32 v152, v154, v184\n" \
"v_and_b32 v153, v155, v185\n" \
"v_mfma_f32_16x16x32_bf16 v[48:51], a[96:99], a[88:91], v[48:51]\n" \
"buffer_load_dwordx4 v234, s[56:59], s81 offen offset:1024 sc1 lds\n" \
"v_mfma_f32_16x16x32_bf16 v[48:51], a[100:103], a[92:95], v[48:51]\n" \
"v_xor_b32 v154, v154, v152\n" \
"v_xor_b32 v155, v155, v153\n" \
"s_waitcnt vmcnt(18) lgkmcnt(0)\n" \
"s_barrier\n" \
"v_mfma_f32_16x16x32_bf16 v[8:11], a[112:115], a[64:67], v[8:11]\n" \
"s_add_u32 s48, s48, 0x1000\n" \
"ds_read_b64_tr_b16 a[96:97], v214 offset:16384\n" \
"ds_read_b64_tr_b16 a[98:99], v214 offset:18432\n" \
"v_mfma_f32_16x16x32_bf16 v[136:139], v[198:201], v[176:179], 0\n" \
"v_mfma_f32_16x16x32_bf16 v[140:143], v[78:81], v[176:179], 0\n" \
"v_mfma_f32_16x16x32_bf16 v[128:131], v[196:199], v[168:171], 0\n" \
"v_mfma_f32_16x16x32_bf16 v[132:135], v[76:79], v[168:171], 0\n" \
"v_mfma_f32_16x16x32_bf16 v[120:123], v[194:197], v[160:163], 0\n" \
"v_mfma_f32_16x16x32_bf16 v[124:127], v[74:77], v[160:163], 0\n" \
"v_mfma_f32_16x16x32_bf16 v[112:115], v[192:195], v[152:155], 0\n" \
"v_mfma_f32_16x16x32_bf16 v[116:119], v[72:75], v[152:155], 0\n" \
"v_mfma_f32_16x16x32_bf16 v[8:11], a[116:119], a[68:71], v[8:11]\n" \
"s_sub_u32 s86, s86, 32768\n" \
"s_cselect_b32 s86, 131072, s86\n" \
"ds_read_b64_tr_b16 a[100:101], v214 offset:24576\n" \
"ds_read_b64_tr_b16 a[102:103], v214 offset:26624\n" \
"v_mfma_f32_16x16x32_bf16 v[12:15], a[120:123], a[64:67], v[12:15]\n" \
"buffer_load_dwordx4 v234, s[56:59], s81 offen offset:2048 sc1 lds\n" \
"v_mfma_f32_16x16x32_bf16 v[12:15], a[124:127], a[68:71], v[12:15]\n" \
"ds_read_b128 a[64:67], v213 offset:0\n" \
"v_cvt_pk_bf16_f32 v136, v136, v137\n" \
"v_cvt_pk_bf16_f32 v137, v138, v139\n" \
"v_cvt_pk_bf16_f32 v138, v140, v141\n" \
"v_cvt_pk_bf16_f32 v139, v142, v143\n" \
"v_cndmask_b32 v140, v136, v138, s[76:77]\n" \
"v_mfma_f32_16x16x32_bf16 v[28:31], a[120:123], a[72:75], v[28:31]\n" \
"ds_read_b128 a[68:71], v213 offset:2048\n" \
"v_cndmask_b32 v141, v137, v139, s[76:77]\n" \
"v_mfma_f32_16x16x32_bf16 v[28:31], a[124:127], a[76:79], v[28:31]\n" \
"ds_read_b64_tr_b16 a[104:105], v215 offset:16384\n" \
"ds_read_b64_tr_b16 a[106:107], v215 offset:18432\n" \
"v_cndmask_b32 v142, v138, v136, s[76:77]\n" \
"v_cndmask_b32 v143, v139, v137, s[76:77]\n" \
"v_mfma_f32_16x16x32_bf16 v[24:27], a[112:115], a[72:75], v[24:27]\n" \
"buffer_load_dwordx4 v234, s[56:59], s81 offen offset:3072 sc1 lds\n" \
"v_mfma_f32_16x16x32_bf16 v[24:27], a[116:119], a[76:79], v[24:27]\n" \
"ds_read_b128 a[72:75], v213 offset:256\n" \
"v_cvt_pk_bf16_f32 v128, v128, v129\n" \
"v_cvt_pk_bf16_f32 v129, v130, v131\n" \
"v_cvt_pk_bf16_f32 v130, v132, v133\n" \
"v_cvt_pk_bf16_f32 v131, v134, v135\n" \
"v_mfma_f32_16x16x32_bf16 v[40:43], a[112:115], a[80:83], v[40:43]\n" \
"ds_read_b128 a[76:79], v213 offset:2304\n" \
"v_cndmask_b32 v132, v128, v130, s[76:77]\n" \
"v_mfma_f32_16x16x32_bf16 v[40:43], a[116:119], a[84:87], v[40:43]\n" \
"s_add_u32 m0, m0, 0x4000\n" \
"ds_read_b64_tr_b16 a[108:109], v215 offset:24576\n" \
"ds_read_b64_tr_b16 a[110:111], v215 offset:26624\n" \
"v_cndmask_b32 v133, v129, v131, s[76:77]\n" \
"v_cndmask_b32 v134, v130, v128, s[76:77]\n" \
"v_mfma_f32_16x16x32_bf16 v[44:47], a[120:123], a[80:83], v[44:47]\n" \
"buffer_load_dwordx4 v216, s[36:39], s82 offen offset:0 lds\n" \
"v_mfma_f32_16x16x32_bf16 v[44:47], a[124:127], a[84:87], v[44:47]\n" \
"ds_read_b128 a[80:83], v213 offset:4096\n" \
"v_cndmask_b32 v135, v131, v129, s[76:77]\n" \
"v_cvt_pk_bf16_f32 v120, v120, v121\n" \
"v_cvt_pk_bf16_f32 v121, v122, v123\n" \
"v_cvt_pk_bf16_f32 v122, v124, v125\n" \
"v_mfma_f32_16x16x32_bf16 v[60:63], a[120:123], a[88:91], v[60:63]\n" \
"s_waitcnt lgkmcnt(4)\n" \
"buffer_load_dwordx4 v217, s[36:39], s82 offen offset:1024 lds\n" \
"v_cvt_pk_bf16_f32 v123, v126, v127\n" \
"v_mfma_f32_16x16x32_bf16 v[60:63], a[124:127], a[92:95], v[60:63]\n" \
"ds_read_b128 a[84:87], v213 offset:6144\n" \
"v_cndmask_b32 v124, v120, v122, s[76:77]\n" \
"v_cndmask_b32 v125, v121, v123, s[76:77]\n" \
"v_mfma_f32_16x16x32_bf16 v[56:59], a[112:115], a[88:91], v[56:59]\n" \
"buffer_load_dwordx4 v218, s[36:39], s82 offen offset:2048 lds\n" \
"v_mfma_f32_16x16x32_bf16 v[56:59], a[116:119], a[92:95], v[56:59]\n" \
"v_cndmask_b32 v126, v122, v120, s[76:77]\n" \
"v_cndmask_b32 v127, v123, v121, s[76:77]\n" \
"Lpg10253_%=:\n" \
"s_cbranch_execz Lcb1_%=\n" \
"v_cvt_pk_bf16_f32 v112, v112, v113\n" \
"v_cvt_pk_bf16_f32 v113, v114, v115\n" \
"v_cvt_pk_bf16_f32 v114, v116, v117\n" \
"v_mfma_f32_16x16x32_bf16 v[0:3], a[96:99], a[64:67], v[0:3]\n" \
"ds_read_b128 a[88:91], v213 offset:4352\n" \
"v_cvt_pk_bf16_f32 v115, v118, v119\n" \
"v_mfma_f32_16x16x32_bf16 v[0:3], a[100:103], a[68:71], v[0:3]\n" \
"s_waitcnt lgkmcnt(2)\n" \
"ds_read_b128 a[92:95], v213 offset:6400\n" \
"v_cndmask_b32 v116, v112, v114, s[76:77]\n" \
"v_cndmask_b32 v117, v113, v115, s[76:77]\n" \
"v_cndmask_b32 v118, v114, v112, s[76:77]\n" \
"v_mfma_f32_16x16x32_bf16 v[4:7], a[104:107], a[64:67], v[4:7]\n" \
"ds_read_b64_tr_b16 a[112:113], v214 offset:16896\n" \
"v_cndmask_b32 v119, v115, v113, s[76:77]\n" \
"v_mfma_f32_16x16x32_bf16 v[4:7], a[108:111], a[68:71], v[4:7]\n" \
"s_add_u32 s71, s93, 208896\n" \
"ds_read_b64_tr_b16 a[114:115], v214 offset:18944\n" \
"buffer_store_dwordx4 v[140:143], v236, s[56:59], s71 offen sc1\n" \
"s_add_u32 s71, s93, 139264\n" \
"v_mfma_f32_16x16x32_bf16 v[20:23], a[104:107], a[72:75], v[20:23]\n" \
"buffer_load_dwordx4 v219, s[36:39], s82 offen offset:3072 lds\n" \
"v_mfma_f32_16x16x32_bf16 v[20:23], a[108:111], a[76:79], v[20:23]\n" \
"s_xor_b32 s82, s48, s49\n" \
"s_cmp_lt_u32 s48, s50\n" \
"s_cselect_b32 s82, s82, s48\n" \
"ds_read_b64_tr_b16 a[116:117], v214 offset:25088\n" \
"buffer_store_dwordx4 v[132:135], v236, s[56:59], s71 offen sc1\n" \
"s_add_u32 s71, s93, 69632\n" \
"v_mfma_f32_16x16x32_bf16 v[16:19], a[96:99], a[72:75], v[16:19]\n" \
"s_waitcnt lgkmcnt(3)\n" \
"s_add_u32 s81, s82, s46\n" \
"ds_read_b64_tr_b16 a[118:119], v214 offset:27136\n" \
"v_mfma_f32_16x16x32_bf16 v[16:19], a[100:103], a[76:79], v[16:19]\n" \
"s_add_u32 m0, s86, s79\n" \
"ds_read_b64_tr_b16 a[120:121], v215 offset:16896\n" \
"buffer_store_dwordx4 v[124:127], v236, s[56:59], s71 offen sc1\n" \
"s_add_u32 s71, s93, 0\n" \
"v_mfma_f32_16x16x32_bf16 v[32:35], a[96:99], a[80:83], v[32:35]\n" \
"buffer_load_dwordx4 v234, s[56:59], s81 offen offset:0 sc1 lds\n" \
"v_mfma_f32_16x16x32_bf16 v[32:35], a[100:103], a[84:87], v[32:35]\n" \
"s_sub_u32 s85, s85, 32768\n" \
"s_cselect_b32 s85, 131072, s85\n" \
"ds_read_b64_tr_b16 a[122:123], v215 offset:18944\n" \
"buffer_store_dwordx4 v[116:119], v236, s[56:59], s71 offen sc1\n" \
"v_mfma_f32_16x16x32_bf16 v[36:39], a[104:107], a[80:83], v[36:39]\n" \
"s_lshl_b32 s82, s82, 5\n" \
"ds_read_b64_tr_b16 a[124:125], v215 offset:25088\n" \
"v_add_u32 v213, s85, v211\n" \
"v_mfma_f32_16x16x32_bf16 v[36:39], a[108:111], a[84:87], v[36:39]\n" \
"s_add_u32 s82, s82, s47\n" \
"ds_read_b64_tr_b16 a[126:127], v215 offset:27136\n" \
"v_mfma_f32_16x16x32_bf16 v[52:55], a[104:107], a[88:91], v[52:55]\n" \
"buffer_load_dwordx4 v234, s[56:59], s81 offen offset:1024 sc1 lds\n" \
"v_mfma_f32_16x16x32_bf16 v[52:55], a[108:111], a[92:95], v[52:55]\n" \
"s_add_u32 s48, s48, 0x1000\n" \
"s_sub_u32 s86, s86, 32768\n" \
"s_cselect_b32 s86, 131072, s86\n" \
"v_add_u32 v214, s85, v212\n" \
"v_xor_b32 v215, 32, v214\n" \
"v_mfma_f32_16x16x32_bf16 v[48:51], a[96:99], a[88:91], v[48:51]\n" \
"buffer_load_dwordx4 v234, s[56:59], s81 offen offset:2048 sc1 lds\n" \
"v_mfma_f32_16x16x32_bf16 v[48:51], a[100:103], a[92:95], v[48:51]\n" \
"s_waitcnt vmcnt(23) lgkmcnt(0)\n" \
"s_barrier\n" \
"v_mfma_f32_16x16x32_bf16 v[8:11], a[112:115], a[64:67], v[8:11]\n" \
"ds_read_b64_tr_b16 a[96:97], v214 offset:16384\n" \
"ds_read_b64_tr_b16 a[98:99], v214 offset:18432\n" \
"v_mfma_f32_16x16x32_bf16 v[8:11], a[116:119], a[68:71], v[8:11]\n" \
"ds_read_b64_tr_b16 a[100:101], v214 offset:24576\n" \
"ds_read_b64_tr_b16 a[102:103], v214 offset:26624\n" \
"v_mfma_f32_16x16x32_bf16 v[12:15], a[120:123], a[64:67], v[12:15]\n" \
"buffer_load_dwordx4 v234, s[56:59], s81 offen offset:3072 sc1 lds\n" \
"v_mfma_f32_16x16x32_bf16 v[12:15], a[124:127], a[68:71], v[12:15]\n" \
"ds_read_b128 a[64:67], v213 offset:0\n" \
"v_mfma_f32_16x16x32_bf16 v[28:31], a[120:123], a[72:75], v[28:31]\n" \
"ds_read_b128 a[68:71], v213 offset:2048\n" \
"v_mfma_f32_16x16x32_bf16 v[28:31], a[124:127], a[76:79], v[28:31]\n" \
"s_add_u32 m0, m0, 0x4000\n" \
"ds_read_b64_tr_b16 a[104:105], v215 offset:16384\n" \
"ds_read_b64_tr_b16 a[106:107], v215 offset:18432\n" \
"v_mfma_f32_16x16x32_bf16 v[24:27], a[112:115], a[72:75], v[24:27]\n" \
"buffer_load_dwordx4 v216, s[36:39], s82 offen offset:0 lds\n" \
"v_mfma_f32_16x16x32_bf16 v[24:27], a[116:119], a[76:79], v[24:27]\n" \
"ds_read_b128 a[72:75], v213 offset:256\n" \
"v_mfma_f32_16x16x32_bf16 v[40:43], a[112:115], a[80:83], v[40:43]\n" \
"ds_read_b128 a[76:79], v213 offset:2304\n" \
"v_mfma_f32_16x16x32_bf16 v[40:43], a[116:119], a[84:87], v[40:43]\n" \
"ds_read_b64_tr_b16 a[108:109], v215 offset:24576\n" \
"ds_read_b64_tr_b16 a[110:111], v215 offset:26624\n" \
"v_mfma_f32_16x16x32_bf16 v[44:47], a[120:123], a[80:83], v[44:47]\n" \
"buffer_load_dwordx4 v217, s[36:39], s82 offen offset:1024 lds\n" \
"v_mfma_f32_16x16x32_bf16 v[44:47], a[124:127], a[84:87], v[44:47]\n" \
"ds_read_b128 a[80:83], v213 offset:4096\n" \
"v_mfma_f32_16x16x32_bf16 v[60:63], a[120:123], a[88:91], v[60:63]\n" \
"s_waitcnt lgkmcnt(4)\n" \
"buffer_load_dwordx4 v218, s[36:39], s82 offen offset:2048 lds\n" \
"v_mfma_f32_16x16x32_bf16 v[60:63], a[124:127], a[92:95], v[60:63]\n" \
"ds_read_b128 a[84:87], v213 offset:6144\n" \
"v_mfma_f32_16x16x32_bf16 v[56:59], a[112:115], a[88:91], v[56:59]\n" \
"buffer_load_dwordx4 v219, s[36:39], s82 offen offset:3072 lds\n" \
"v_mfma_f32_16x16x32_bf16 v[56:59], a[116:119], a[92:95], v[56:59]\n" \
"s_mov_b32 s72, 2\n" \
"Lo0_%=:\n" \
"v_mfma_f32_16x16x32_bf16 v[0:3], a[96:99], a[64:67], v[0:3]\n" \
"s_waitcnt lgkmcnt(1)\n" \
"s_xor_b32 s82, s48, s49\n" \
"ds_read_b128 a[88:91], v213 offset:4352\n" \
"v_mfma_f32_16x16x32_bf16 v[0:3], a[100:103], a[68:71], v[0:3]\n" \
"s_cmp_lt_u32 s48, s50\n" \
"s_cselect_b32 s82, s82, s48\n" \
"ds_read_b128 a[92:95], v213 offset:6400\n" \
"v_mfma_f32_16x16x32_bf16 v[4:7], a[104:107], a[64:67], v[4:7]\n" \
"s_add_u32 s81, s82, s46\n" \
"ds_read_b64_tr_b16 a[112:113], v214 offset:16896\n" \
"v_mfma_f32_16x16x32_bf16 v[4:7], a[108:111], a[68:71], v[4:7]\n" \
"s_add_u32 m0, s86, s79\n" \
"ds_read_b64_tr_b16 a[114:115], v214 offset:18944\n" \
"v_mfma_f32_16x16x32_bf16 v[20:23], a[104:107], a[72:75], v[20:23]\n" \
"buffer_load_dwordx4 v234, s[56:59], s81 offen offset:0 sc1 lds\n" \
"v_mfma_f32_16x16x32_bf16 v[20:23], a[108:111], a[76:79], v[20:23]\n" \
"ds_read_b64_tr_b16 a[116:117], v214 offset:25088\n" \
"v_mfma_f32_16x16x32_bf16 v[16:19], a[96:99], a[72:75], v[16:19]\n" \
"s_lshl_b32 s82, s82, 5\n" \
"ds_read_b64_tr_b16 a[118:119], v214 offset:27136\n" \
"v_mfma_f32_16x16x32_bf16 v[16:19], a[100:103], a[76:79], v[16:19]\n" \
"s_waitcnt lgkmcnt(3)\n" \
"s_add_u32 s82, s82, s47\n" \
"ds_read_b64_tr_b16 a[120:121], v215 offset:16896\n" \
"v_mfma_f32_16x16x32_bf16 v[32:35], a[96:99], a[80:83], v[32:35]\n" \
"buffer_load_dwordx4 v234, s[56:59], s81 offen offset:1024 sc1 lds\n" \
"v_mfma_f32_16x16x32_bf16 v[32:35], a[100:103], a[84:87], v[32:35]\n" \
"s_sub_u32 s85, s85, 32768\n" \
"s_cselect_b32 s85, 131072, s85\n" \
"ds_read_b64_tr_b16 a[122:123], v215 offset:18944\n" \
"v_mfma_f32_16x16x32_bf16 v[36:39], a[104:107], a[80:83], v[36:39]\n" \
"s_add_u32 s48, s48, 0x1000\n" \
"ds_read_b64_tr_b16 a[124:125], v215 offset:25088\n" \
"v_add_u32 v213, s85, v211\n" \
"v_mfma_f32_16x16x32_bf16 v[36:39], a[108:111], a[84:87], v[36:39]\n" \
"s_sub_u32 s86, s86, 32768\n" \
"s_cselect_b32 s86, 131072, s86\n" \
"ds_read_b64_tr_b16 a[126:127], v215 offset:27136\n" \
"v_mfma_f32_16x16x32_bf16 v[52:55], a[104:107], a[88:91], v[52:55]\n" \
"buffer_load_dwordx4 v234, s[56:59], s81 offen offset:2048 sc1 lds\n" \
"v_mfma_f32_16x16x32_bf16 v[52:55], a[108:111], a[92:95], v[52:55]\n" \
"v_add_u32 v214, s85, v212\n" \
"v_xor_b32 v215, 32, v214\n" \
"v_mfma_f32_16x16x32_bf16 v[48:51], a[96:99], a[88:91], v[48:51]\n" \
"buffer_load_dwordx4 v234, s[56:59], s81 offen offset:3072 sc1 lds\n" \
"v_mfma_f32_16x16x32_bf16 v[48:51], a[100:103], a[92:95], v[48:51]\n" \
"s_waitcnt vmcnt(20) lgkmcnt(0)\n" \
"s_barrier\n" \
"v_mfma_f32_16x16x32_bf16 v[8:11], a[112:115], a[64:67], v[8:11]\n" \
"ds_read_b64_tr_b16 a[96:97], v214 offset:16384\n" \
"ds_read_b64_tr_b16 a[98:99], v214 offset:18432\n" \
"v_mfma_f32_16x16x32_bf16 v[8:11], a[116:119], a[68:71], v[8:11]\n" \
"s_add_u32 m0, m0, 0x4000\n" \
"ds_read_b64_tr_b16 a[100:101], v214 offset:24576\n" \
"ds_read_b64_tr_b16 a[102:103], v214 offset:26624\n" \
"v_mfma_f32_16x16x32_bf16 v[12:15], a[120:123], a[64:67], v[12:15]\n" \
"buffer_load_dwordx4 v216, s[36:39], s82 offen offset:0 lds\n" \
"v_mfma_f32_16x16x32_bf16 v[12:15], a[124:127], a[68:71], v[12:15]\n" \
"ds_read_b128 a[64:67], v213 offset:0\n" \
"v_mfma_f32_16x16x32_bf16 v[28:31], a[120:123], a[72:75], v[28:31]\n" \
"ds_read_b128 a[68:71], v213 offset:2048\n" \
"v_mfma_f32_16x16x32_bf16 v[28:31], a[124:127], a[76:79], v[28:31]\n" \
"ds_read_b64_tr_b16 a[104:105], v215 offset:16384\n" \
"ds_read_b64_tr_b16 a[106:107], v215 offset:18432\n" \
"v_mfma_f32_16x16x32_bf16 v[24:27], a[112:115], a[72:75], v[24:27]\n" \
"buffer_load_dwordx4 v217, s[36:39], s82 offen offset:1024 lds\n" \
"v_mfma_f32_16x16x32_bf16 v[24:27], a[116:119], a[76:79], v[24:27]\n" \
"ds_read_b128 a[72:75], v213 offset:256\n" \
"v_mfma_f32_16x16x32_bf16 v[40:43], a[112:115], a[80:83], v[40:43]\n" \
"ds_read_b128 a[76:79], v213 offset:2304\n" \
"v_mfma_f32_16x16x32_bf16 v[40:43], a[116:119], a[84:87], v[40:43]\n" \
"ds_read_b64_tr_b16 a[108:109], v215 offset:24576\n" \
"ds_read_b64_tr_b16 a[110:111], v215 offset:26624\n" \
"v_mfma_f32_16x16x32_bf16 v[44:47], a[120:123], a[80:83], v[44:47]\n" \
"buffer_load_dwordx4 v218, s[36:39], s82 offen offset:2048 lds\n" \
"v_mfma_f32_16x16x32_bf16 v[44:47], a[124:127], a[84:87], v[44:47]\n" \
"ds_read_b128 a[80:83], v213 offset:4096\n" \
"v_mfma_f32_16x16x32_bf16 v[60:63], a[120:123], a[88:91], v[60:63]\n" \
"s_waitcnt lgkmcnt(4)\n" \
"v_mfma_f32_16x16x32_bf16 v[60:63], a[124:127], a[92:95], v[60:63]\n" \
"s_sub_u32 s72, s72, 1\n" \
"ds_read_b128 a[84:87], v213 offset:6144\n" \
"v_mfma_f32_16x16x32_bf16 v[56:59], a[112:115], a[88:91], v[56:59]\n" \
"buffer_load_dwordx4 v219, s[36:39], s82 offen offset:3072 lds\n" \
"v_mfma_f32_16x16x32_bf16 v[56:59], a[116:119], a[92:95], v[56:59]\n" \
"s_cbranch_scc0 Lo0_%=\n" \
"s_setpc_b64 s[44:45]\n" \
"Lor_%=:\n" \
"buffer_store_dword v230, v231, s[56:59], s12 offen sc1\n" \
"s_mov_b32 s72, s10\n" \
"s_call_b64 s[44:45], Lo0_%=\n" \
"buffer_load_dword v220, v221, s[56:59], s67 offen sc1\n" \
"s_mov_b32 s72, 1\n" \
"s_call_b64 s[44:45], Lo0_%=\n" \
"s_waitcnt vmcnt(16)\n" \
"v_cmp_eq_u32 vcc, s60, v220\n" \
"s_nop 1\n" \
"s_andn2_b32 s70, s74, vcc_lo\n" \
"s_cbranch_scc1 Lpl6_%=\n" \
"Lpr6_%=:\n" \
"s_cmp_eq_u32 s90, 0\n" \
"s_cselect_b32 s46, s46, s83\n" \
"s_cselect_b32 s47, s47, s84\n" \
"s_cselect_b32 s48, s48, 0\n" \
"s_cselect_b64 s[36:37], s[36:37], s[40:41]\n" \
"s_cselect_b32 s38, s38, s42\n" \
"s_mov_b32 s72, 3\n" \
"s_call_b64 s[44:45], Lo0_%=\n" \
"s_cmp_eq_u32 s90, 0\n" \
"s_cbranch_scc0 Lrole1_%=\n" \
"s_mov_b32 s72, s11\n" \
"s_call_b64 s[44:45], Lo0_%=\n" \
"buffer_load_dword v220, v241, s[56:59], s64 offen sc1\n" \
"s_mov_b32 s72, 1\n" \
"s_call_b64 s[44:45], Lo0_%=\n" \
"v_mfma_f32_16x16x32_bf16 v[0:3], a[96:99], a[64:67], v[0:3]\n" \
"s_waitcnt lgkmcnt(1)\n" \
"ds_read_b128 a[88:91], v213 offset:4352\n" \
"v_mov_b32 v74, 0x3f80\n" \
"v_mfma_f32_16x16x32_bf16 v[0:3], a[100:103], a[68:71], v[0:3]\n" \
"s_add_u32 s48, s86, 32768\n" \
"s_cmp_eq_u32 s48, 163840\n" \
"s_cselect_b32 s48, 0, s48\n" \
"ds_read_b128 a[92:95], v213 offset:6400\n" \
"v_mfma_f32_16x16x32_bf16 v[4:7], a[104:107], a[64:67], v[4:7]\n" \
"ds_read_b64_tr_b16 a[112:113], v214 offset:16896\n" \
"v_mov_b32 v64, 0\n" \
"v_mfma_f32_16x16x32_bf16 v[4:7], a[108:111], a[68:71], v[4:7]\n" \
"ds_read_b64_tr_b16 a[114:115], v214 offset:18944\n" \
"v_mov_b32 v65, 0\n" \
"v_mfma_f32_16x16x32_bf16 v[20:23], a[104:107], a[72:75], v[20:23]\n" \
"s_add_u32 s49, s48, 32768\n" \
"s_cmp_eq_u32 s49, 163840\n" \
"s_cselect_b32 s49, 0, s49\n" \
"v_mfma_f32_16x16x32_bf16 v[20:23], a[108:111], a[76:79], v[20:23]\n" \
"ds_read_b64_tr_b16 a[116:117], v214 offset:25088\n" \
"v_add_u32 v224, s49, v211\n" \
"v_mfma_f32_16x16x32_bf16 v[16:19], a[96:99], a[72:75], v[16:19]\n" \
"ds_read_b64_tr_b16 a[118:119], v214 offset:27136\n" \
"v_add_u32 v225, s49, v212\n" \
"v_mfma_f32_16x16x32_bf16 v[16:19], a[100:103], a[76:79], v[16:19]\n" \
"s_waitcnt lgkmcnt(3)\n" \
"ds_read_b64_tr_b16 a[120:121], v215 offset:16896\n" \
"v_xor_b32 v226, 32, v225\n" \
"v_mfma_f32_16x16x32_bf16 v[32:35], a[96:99], a[80:83], v[32:35]\n" \
"v_add_u32 v227, s48, v211\n" \
"v_add_u32 v228, s48, v212\n" \
"v_mfma_f32_16x16x32_bf16 v[32:35], a[100:103], a[84:87], v[32:35]\n" \
"s_sub_u32 s85, s85, 32768\n" \
"s_cselect_b32 s85, 131072, s85\n" \
"ds_read_b64_tr_b16 a[122:123], v215 offset:18944\n" \
"v_mfma_f32_16x16x32_bf16 v[36:39], a[104:107], a[80:83], v[36:39]\n" \
"ds_read_b64_tr_b16 a[124:125], v215 offset:25088\n" \
"v_add_u32 v213, s85, v211\n" \
"v_mfma_f32_16x16x32_bf16 v[36:39], a[108:111], a[84:87], v[36:39]\n" \
"ds_read_b64_tr_b16 a[126:127], v215 offset:27136\n" \
"v_xor_b32 v229, 32, v228\n" \
"v_mfma_f32_16x16x32_bf16 v[52:55], a[104:107], a[88:91], v[52:55]\n" \
"v_mov_b32 v68, 0\n" \
"v_mov_b32 v69, 0\n" \
"v_mfma_f32_16x16x32_bf16 v[52:55], a[108:111], a[92:95], v[52:55]\n" \
"v_add_u32 v214, s85, v212\n" \
"v_xor_b32 v215, 32, v214\n" \
"v_mfma_f32_16x16x32_bf16 v[48:51], a[96:99], a[88:91], v[48:51]\n" \
"v_lshrrev_b32 v72, 8, v234\n" \
"v_bfe_u32 v73, v234, 6, 2\n" \
"v_mfma_f32_16x16x32_bf16 v[48:51], a[100:103], a[92:95], v[48:51]\n" \
"s_waitcnt vmcnt(8) lgkmcnt(0)\n" \
"s_barrier\n" \
"s_waitcnt vmcnt(16)\n" \
"v_cmp_eq_u32 vcc, s60, v220\n" \
"s_nop 1\n" \
"s_andn2_b32 s70, s69, vcc_lo\n" \
"s_cbranch_scc1 Lpl7_%=\n" \
"Lpr7_%=:\n" \
"v_mfma_f32_16x16x32_bf16 v[8:11], a[112:115], a[64:67], v[8:11]\n" \
"s_add_u32 s71, s87, 0\n" \
"ds_read_b128 a[0:3], v213 offset:0\n" \
"ds_read_b128 a[4:7], v213 offset:2048\n" \
"v_mfma_f32_16x16x32_bf16 v[8:11], a[116:119], a[68:71], v[8:11]\n" \
"buffer_load_dwordx4 v[176:179], v234, s[56:59], s71 offen offset:0 sc1\n" \
"v_mfma_f32_16x16x32_bf16 v[12:15], a[120:123], a[64:67], v[12:15]\n" \
"ds_read_b64_tr_b16 a[128:129], v214 offset:16384\n" \
"ds_read_b64_tr_b16 a[130:131], v214 offset:18432\n" \
"v_mfma_f32_16x16x32_bf16 v[12:15], a[124:127], a[68:71], v[12:15]\n" \
"buffer_load_dwordx4 v[180:183], v234, s[56:59], s71 offen offset:1024 sc1\n" \
"v_mfma_f32_16x16x32_bf16 v[28:31], a[120:123], a[72:75], v[28:31]\n" \
"ds_read_b64_tr_b16 a[132:133], v214 offset:24576\n" \
"ds_read_b64_tr_b16 a[134:135], v214 offset:26624\n" \
"v_mfma_f32_16x16x32_bf16 v[28:31], a[124:127], a[76:79], v[28:31]\n" \
"buffer_load_dwordx4 v[184:187], v234, s[56:59], s71 offen offset:2048 sc1\n" \
"v_mfma_f32_16x16x32_bf16 v[24:27], a[112:115], a[72:75], v[24:27]\n" \
"ds_read_b64_tr_b16 a[136:137], v215 offset:16384\n" \
"ds_read_b64_tr_b16 a[138:139], v215 offset:18432\n" \
"v_mfma_f32_16x16x32_bf16 v[24:27], a[116:119], a[76:79], v[24:27]\n" \
"buffer_load_dwordx4 v[188:191], v234, s[56:59], s71 offen offset:3072 sc1\n" \
"v_mfma_f32_16x16x32_bf16 v[40:43], a[112:115], a[80:83], v[40:43]\n" \
"ds_read_b64_tr_b16 a[140:141], v215 offset:24576\n" \
"ds_read_b64_tr_b16 a[142:143], v215 offset:26624\n" \
"v_mfma_f32_16x16x32_bf16 v[40:43], a[116:119], a[84:87], v[40:43]\n" \
"ds_read_b64_tr_b16 a[144:145], v214 offset:16896\n" \
"ds_read_b64_tr_b16 a[146:147], v214 offset:18944\n" \
"v_mfma_f32_16x16x32_bf16 v[44:47], a[120:123], a[80:83], v[44:47]\n" \
"ds_read_b64_tr_b16 a[148:149], v214 offset:25088\n" \
"ds_read_b64_tr_b16 a[150:151], v214 offset:27136\n" \
"v_mfma_f32_16x16x32_bf16 v[44:47], a[124:127], a[84:87], v[44:47]\n" \
"ds_read_b64_tr_b16 a[152:153], v215 offset:16896\n" \
"ds_read_b64_tr_b16 a[154:155], v215 offset:18944\n" \
"v_mfma_f32_16x16x32_bf16 v[60:63], a[120:123], a[88:91], v[60:63]\n" \
"ds_read_b64_tr_b16 a[156:157], v215 offset:25088\n" \
"ds_read_b64_tr_b16 a[158:159], v215 offset:27136\n" \
"v_mfma_f32_16x16x32_bf16 v[60:63], a[124:127], a[92:95], v[60:63]\n" \
"ds_read_b128 a[24:27], v213 offset:256\n" \
"ds_read_b128 a[28:31], v213 offset:2304\n" \
"v_mfma_f32_16x16x32_bf16 v[56:59], a[112:115], a[88:91], v[56:59]\n" \
"ds_read_b128 a[8:11], v224 offset:0\n" \
"ds_read_b128 a[12:15], v224 offset:2048\n" \
"v_mfma_f32_16x16x32_bf16 v[56:59], a[116:119], a[92:95], v[56:59]\n" \
"s_waitcnt lgkmcnt(8)\n" \
"ds_read_b64_tr_b16 a[160:161], v225 offset:16384\n" \
"ds_read_b64_tr_b16 a[162:163], v225 offset:18432\n" \
"v_mfma_f32_16x16x32_bf16 v[0:3], a[128:131], a[0:3], v[0:3]\n" \
"ds_read_b64_tr_b16 a[164:165], v225 offset:24576\n" \
"ds_read_b64_tr_b16 a[166:167], v225 offset:26624\n" \
"v_mfma_f32_16x16x32_bf16 v[0:3], a[132:135], a[4:7], v[0:3]\n" \
"ds_read_b64_tr_b16 a[168:169], v226 offset:16384\n" \
"ds_read_b64_tr_b16 a[170:171], v226 offset:18432\n" \
"v_mfma_f32_16x16x32_bf16 v[4:7], a[136:139], a[0:3], v[4:7]\n" \
"s_mov_b32 s70, 0x20000000\n" \
"ds_read_b64_tr_b16 a[172:173], v226 offset:24576\n" \
"ds_read_b64_tr_b16 a[174:175], v226 offset:26624\n" \
"v_mfma_f32_16x16x32_bf16 v[4:7], a[140:143], a[4:7], v[4:7]\n" \
"s_cmp_eq_u32 s64, 65616896\n" \
"s_cselect_b32 s70, 65593344, s70\n" \
"ds_read_b64_tr_b16 a[176:177], v225 offset:16896\n" \
"ds_read_b64_tr_b16 a[178:179], v225 offset:18944\n" \
"buffer_store_dword v230, v231, s[56:59], s70 offen sc1\n" \
"v_mfma_f32_16x16x32_bf16 v[8:11], a[144:147], a[0:3], v[8:11]\n" \
"s_waitcnt lgkmcnt(8)\n" \
"ds_read_b64_tr_b16 a[180:181], v225 offset:25088\n" \
"ds_read_b64_tr_b16 a[182:183], v225 offset:27136\n" \
"v_mfma_f32_16x16x32_bf16 v[8:11], a[148:151], a[4:7], v[8:11]\n" \
"s_sub_u32 s68, s86, 32768\n" \
"s_cselect_b32 s68, 131072, s68\n" \
"ds_read_b64_tr_b16 a[184:185], v226 offset:16896\n" \
"ds_read_b64_tr_b16 a[186:187], v226 offset:18944\n" \
"v_mfma_f32_16x16x32_bf16 v[12:15], a[152:155], a[0:3], v[12:15]\n" \
"ds_read_b64_tr_b16 a[188:189], v226 offset:25088\n" \
"ds_read_b64_tr_b16 a[190:191], v226 offset:27136\n" \
"v_mfma_f32_16x16x32_bf16 v[12:15], a[156:159], a[4:7], v[12:15]\n" \
"s_cmp_lt_u32 s61, 2\n" \
"s_cselect_b32 s68, s86, s68\n" \
"v_cmp_eq_u32 s[16:17], v72, v73\n" \
"v_and_b32 v72, 16, v234\n" \
"v_mfma_f32_16x16x32_bf16 v[28:31], a[152:155], a[24:27], v[28:31]\n" \
"s_and_b32 s50, s61, 1\n" \
"v_lshlrev_b32 v72, v72, v74\n" \
"v_cndmask_b32 v72, 0, v72, s[16:17]\n" \
"v_mfma_f32_16x16x32_bf16 v[28:31], a[156:159], a[28:31], v[28:31]\n" \
"s_mul_i32 s50, s50, 9216\n" \
"v_bfe_i32 v73, v234, 5, 1\n" \
"v_and_b32 v67, v72, v73\n" \
"v_mfma_f32_16x16x32_bf16 v[24:27], a[144:147], a[24:27], v[24:27]\n" \
"s_add_u32 s68, s68, s50\n" \
"s_add_u32 s68, s68, %3\n" \
"v_xor_b32 v66, v72, v67\n" \
"v_mfma_f32_16x16x32_bf16 v[24:27], a[148:151], a[28:31], v[24:27]\n" \
"v_lshrrev_b32 v132, 4, v234\n" \
"v_and_b32 v133, 15, v132\n" \
"v_mul_u32_u24 v128, 144, v133\n" \
"v_mfma_f32_16x16x32_bf16 v[20:23], a[136:139], a[24:27], v[20:23]\n" \
"s_waitcnt vmcnt(5)\n" \
"s_barrier\n" \
"v_mfma_f32_16x16x32_bf16 v[20:23], a[140:143], a[28:31], v[20:23]\n" \
"s_add_u32 s71, s87, 4096\n" \
"ds_read_b128 a[32:35], v224 offset:256\n" \
"ds_read_b128 a[36:39], v224 offset:2304\n" \
"v_mfma_f32_16x16x32_bf16 v[16:19], a[128:131], a[24:27], v[16:19]\n" \
"buffer_load_dwordx4 v[192:195], v234, s[56:59], s71 offen offset:0 sc1\n" \
"v_mfma_f32_16x16x32_bf16 v[16:19], a[132:135], a[28:31], v[16:19]\n" \
"s_waitcnt lgkmcnt(2)\n" \
"ds_read_b128 a[16:19], v227 offset:0\n" \
"ds_read_b128 a[20:23], v227 offset:2048\n" \
"v_mfma_f32_16x16x32_bf16 v[0:3], a[160:163], a[8:11], v[0:3]\n" \
"ds_read_b64_tr_b16 a[192:193], v228 offset:16384\n" \
"ds_read_b64_tr_b16 a[194:195], v228 offset:18432\n" \
"v_mfma_f32_16x16x32_bf16 v[0:3], a[164:167], a[12:15], v[0:3]\n" \
"buffer_load_dwordx4 v[196:199], v234, s[56:59], s71 offen offset:1024 sc1\n" \
"v_mfma_f32_16x16x32_bf16 v[4:7], a[168:171], a[8:11], v[4:7]\n" \
"ds_read_b64_tr_b16 a[196:197], v228 offset:24576\n" \
"ds_read_b64_tr_b16 a[198:199], v228 offset:26624\n" \
"v_mfma_f32_16x16x32_bf16 v[4:7], a[172:175], a[12:15], v[4:7]\n" \
"ds_read_b64_tr_b16 a[200:201], v229 offset:16384\n" \
"ds_read_b64_tr_b16 a[202:203], v229 offset:18432\n" \
"v_mfma_f32_16x16x32_bf16 v[8:11], a[176:179], a[8:11], v[8:11]\n" \
"buffer_load_dwordx4 v[200:203], v234, s[56:59], s71 offen offset:2048 sc1\n" \
"v_mfma_f32_16x16x32_bf16 v[8:11], a[180:183], a[12:15], v[8:11]\n" \
"ds_read_b64_tr_b16 a[204:205], v229 offset:24576\n" \
"ds_read_b64_tr_b16 a[206:207], v229 offset:26624\n" \
"v_lshrrev_b32 v133, 4, v132\n" \
"v_mfma_f32_16x16x32_bf16 v[12:15], a[184:187], a[8:11], v[12:15]\n" \
"s_waitcnt lgkmcnt(6)\n" \
"ds_read_b64_tr_b16 a[208:209], v228 offset:16896\n" \
"ds_read_b64_tr_b16 a[210:211], v228 offset:18944\n" \
"v_mfma_f32_16x16x32_bf16 v[12:15], a[188:191], a[12:15], v[12:15]\n" \
"buffer_load_dwordx4 v[204:207], v234, s[56:59], s71 offen offset:3072 sc1\n" \
"v_mfma_f32_16x16x32_bf16 v[28:31], a[184:187], a[32:35], v[28:31]\n" \
"ds_read_b64_tr_b16 a[212:213], v228 offset:25088\n" \
"ds_read_b64_tr_b16 a[214:215], v228 offset:27136\n" \
"v_mfma_f32_16x16x32_bf16 v[28:31], a[188:191], a[36:39], v[28:31]\n" \
"ds_read_b64_tr_b16 a[216:217], v229 offset:16896\n" \
"ds_read_b64_tr_b16 a[218:219], v229 offset:18944\n" \
"v_mfma_f32_16x16x32_bf16 v[24:27], a[176:179], a[32:35], v[24:27]\n" \
"ds_read_b64_tr_b16 a[220:221], v229 offset:25088\n" \
"ds_read_b64_tr_b16 a[222:223], v229 offset:27136\n" \
"v_mfma_f32_16x16x32_bf16 v[24:27], a[180:183], a[36:39], v[24:27]\n" \
"ds_read_b128 a[40:43], v227 offset:256\n" \
"ds_read_b128 a[44:47], v227 offset:2304\n" \
"v_mfma_f32_16x16x32_bf16 v[20:23], a[168:171], a[32:35], v[20:23]\n" \
"ds_read_b128 a[248:251], v213 offset:4096\n" \
"ds_read_b128 a[252:255], v213 offset:6144\n" \
"v_mfma_f32_16x16x32_bf16 v[20:23], a[172:175], a[36:39], v[20:23]\n" \
"ds_read_b128 a[48:51], v224 offset:4096\n" \
"ds_read_b128 a[52:55], v224 offset:6144\n" \
"v_mfma_f32_16x16x32_bf16 v[16:19], a[160:163], a[32:35], v[16:19]\n" \
"ds_read_b128 a[56:59], v227 offset:4096\n" \
"ds_read_b128 a[60:63], v227 offset:6144\n" \
"v_mfma_f32_16x16x32_bf16 v[16:19], a[164:167], a[36:39], v[16:19]\n" \
"s_waitcnt lgkmcnt(8)\n" \
"ds_read_b128 a[224:227], v213 offset:4352\n" \
"ds_read_b128 a[228:231], v213 offset:6400\n" \
"v_lshl_add_u32 v128, v133, 3, v128\n" \
"v_mfma_f32_16x16x32_bf16 v[0:3], a[192:195], a[16:19], v[0:3]\n" \
"s_waitcnt vmcnt(7)\n" \
"ds_read_b128 a[232:235], v224 offset:4352\n" \
"ds_read_b128 a[236:239], v224 offset:6400\n" \
"v_mfma_f32_16x16x32_bf16 v[0:3], a[196:199], a[20:23], v[0:3]\n" \
"ds_read_b128 a[240:243], v227 offset:4352\n" \
"ds_read_b128 a[244:247], v227 offset:6400\n" \
"v_mfma_f32_16x16x32_bf16 v[0:3], v[66:69], v[176:179], v[0:3]\n" \
"v_add_u32 v128, s68, v128\n" \
"v_lshrrev_b32 v133, 3, v132\n" \
"v_mfma_f32_16x16x32_bf16 v[4:7], a[200:203], a[16:19], v[4:7]\n" \
"v_and_b32 v134, 7, v132\n" \
"v_mul_u32_u24 v129, 144, v133\n" \
"v_mfma_f32_16x16x32_bf16 v[4:7], a[204:207], a[20:23], v[4:7]\n" \
"v_lshl_add_u32 v129, v134, 4, v129\n" \
"v_add_u32 v129, s68, v129\n" \
"v_mfma_f32_16x16x32_bf16 v[4:7], v[64:67], v[176:179], v[4:7]\n" \
"v_lshlrev_b32 v134, 4, v134\n" \
"v_lshl_add_u32 v130, v133, 12, v134\n" \
"v_mfma_f32_16x16x32_bf16 v[8:11], a[208:211], a[16:19], v[8:11]\n" \
"v_mul_f32 v0, 4.0, v0\n" \
"v_mul_f32 v1, 4.0, v1\n" \
"v_mul_f32 v2, 4.0, v2\n" \
"v_mul_f32 v3, 4.0, v3\n" \
"v_cvt_pk_bf16_f32 v136, v0, v1\n" \
"v_cvt_pk_bf16_f32 v137, v2, v3\n" \
"v_mfma_f32_16x16x32_bf16 v[8:11], a[212:215], a[20:23], v[8:11]\n" \
"v_mfma_f32_16x16x32_bf16 v[8:11], v[66:69], v[180:183], v[8:11]\n" \
"ds_write_b64 v128, v[136:137] offset:0\n" \
"v_mfma_f32_16x16x32_bf16 v[12:15], a[216:219], a[16:19], v[12:15]\n" \
"v_mul_f32 v4, 4.0, v4\n" \
"v_mul_f32 v5, 4.0, v5\n" \
"v_mfma_f32_16x16x32_bf16 v[12:15], a[220:223], a[20:23], v[12:15]\n" \
"v_mul_f32 v6, 4.0, v6\n" \
"v_mul_f32 v7, 4.0, v7\n" \
"v_mfma_f32_16x16x32_bf16 v[12:15], v[64:67], v[180:183], v[12:15]\n" \
"s_waitcnt lgkmcnt(1)\n" \
"v_cvt_pk_bf16_f32 v138, v4, v5\n" \
"v_cvt_pk_bf16_f32 v139, v6, v7\n" \
"v_mfma_f32_16x16x32_bf16 v[16:19], a[192:195], a[40:43], v[16:19]\n" \
"s_waitcnt vmcnt(5)\n" \
"v_mfma_f32_16x16x32_bf16 v[16:19], a[196:199], a[44:47], v[16:19]\n" \
"v_mfma_f32_16x16x32_bf16 v[16:19], v[66:69], v[184:187], v[16:19]\n" \
"ds_write_b64 v128, v[138:139] offset:32\n" \
"v_mul_f32 v8, 4.0, v8\n" \
"v_mfma_f32_16x16x32_bf16 v[20:23], a[200:203], a[40:43], v[20:23]\n" \
"v_mul_f32 v9, 4.0, v9\n" \
"v_mul_f32 v10, 4.0, v10\n" \
"v_mfma_f32_16x16x32_bf16 v[20:23], a[204:207], a[44:47], v[20:23]\n" \
"v_mul_f32 v11, 4.0, v11\n" \
"v_cvt_pk_bf16_f32 v136, v8, v9\n" \
"v_mfma_f32_16x16x32_bf16 v[20:23], v[64:67], v[184:187], v[20:23]\n" \
"v_cvt_pk_bf16_f32 v137, v10, v11\n" \
"v_mfma_f32_16x16x32_bf16 v[24:27], a[208:211], a[40:43], v[24:27]\n" \
"v_mfma_f32_16x16x32_bf16 v[24:27], a[212:215], a[44:47], v[24:27]\n" \
"v_mfma_f32_16x16x32_bf16 v[24:27], v[66:69], v[188:191], v[24:27]\n" \
"ds_write_b64 v128, v[136:137] offset:64\n" \
"v_mul_f32 v12, 4.0, v12\n" \
"v_mfma_f32_16x16x32_bf16 v[28:31], a[216:219], a[40:43], v[28:31]\n" \
"v_mul_f32 v13, 4.0, v13\n" \
"v_mul_f32 v14, 4.0, v14\n" \
"v_mfma_f32_16x16x32_bf16 v[28:31], a[220:223], a[44:47], v[28:31]\n" \
"v_mul_f32 v15, 4.0, v15\n" \
"v_cvt_pk_bf16_f32 v138, v12, v13\n" \
"v_mfma_f32_16x16x32_bf16 v[28:31], v[64:67], v[188:191], v[28:31]\n" \
"v_cvt_pk_bf16_f32 v139, v14, v15\n" \
"v_mfma_f32_16x16x32_bf16 v[32:35], a[128:131], a[248:251], v[32:35]\n" \
"ds_write_b64 v128, v[138:139] offset:96\n" \
"v_mfma_f32_16x16x32_bf16 v[32:35], a[132:135], a[252:255], v[32:35]\n" \
"ds_read_b128 v[140:143], v129 offset:0\n" \
"ds_read_b128 v[144:147], v129 offset:1152\n" \
"v_mfma_f32_16x16x32_bf16 v[32:35], a[160:163], a[48:51], v[32:35]\n" \
"s_waitcnt vmcnt(2)\n" \
"v_mul_f32 v16, 4.0, v16\n" \
"v_mul_f32 v17, 4.0, v17\n" \
"v_mfma_f32_16x16x32_bf16 v[32:35], a[164:167], a[52:55], v[32:35]\n" \
"v_mul_f32 v18, 4.0, v18\n" \
"v_mul_f32 v19, 4.0, v19\n" \
"v_mfma_f32_16x16x32_bf16 v[32:35], v[66:69], v[192:195], v[32:35]\n" \
"s_add_u32 s70, s88, 0\n" \
"s_waitcnt lgkmcnt(1)\n" \
"v_cvt_pk_bf16_f32 v136, v16, v17\n" \
"v_mfma_f32_16x16x32_bf16 v[32:35], a[192:195], a[56:59], v[32:35]\n" \
"buffer_store_dwordx4 v[140:143], v130, s[52:55], s70 offen sc1\n" \
"v_cvt_pk_bf16_f32 v137, v18, v19\n" \
"v_mfma_f32_16x16x32_bf16 v[32:35], a[196:199], a[60:63], v[32:35]\n" \
"ds_write_b64 v128, v[136:137] offset:2304\n" \
"v_mfma_f32_16x16x32_bf16 v[36:39], a[204:207], a[60:63], v[36:39]\n" \
"s_add_u32 s70, s88, 32768\n" \
"s_waitcnt lgkmcnt(1)\n" \
"v_mul_f32 v20, 4.0, v20\n" \
"v_mfma_f32_16x16x32_bf16 v[36:39], a[200:203], a[56:59], v[36:39]\n" \
"buffer_store_dwordx4 v[144:147], v130, s[52:55], s70 offen sc1\n" \
"v_mfma_f32_16x16x32_bf16 v[36:39], a[172:175], a[52:55], v[36:39]\n" \
"v_mul_f32 v21, 4.0, v21\n" \
"v_mul_f32 v22, 4.0, v22\n" \
"v_mfma_f32_16x16x32_bf16 v[36:39], a[168:171], a[48:51], v[36:39]\n" \
"v_mul_f32 v23, 4.0, v23\n" \
"v_cvt_pk_bf16_f32 v138, v20, v21\n" \
"v_mfma_f32_16x16x32_bf16 v[36:39], v[64:67], v[192:195], v[36:39]\n" \
"v_cvt_pk_bf16_f32 v139, v22, v23\n" \
"v_mfma_f32_16x16x32_bf16 v[36:39], a[140:143], a[252:255], v[36:39]\n" \
"v_mfma_f32_16x16x32_bf16 v[36:39], a[136:139], a[248:251], v[36:39]\n" \
"ds_write_b64 v128, v[138:139] offset:2336\n" \
"v_mul_f32 v24, 4.0, v24\n" \
"v_mul_f32 v25, 4.0, v25\n" \
"v_mfma_f32_16x16x32_bf16 v[40:43], a[144:147], a[248:251], v[40:43]\n" \
"v_mul_f32 v26, 4.0, v26\n" \
"v_mul_f32 v27, 4.0, v27\n" \
"v_mfma_f32_16x16x32_bf16 v[40:43], a[148:151], a[252:255], v[40:43]\n" \
"v_cvt_pk_bf16_f32 v136, v24, v25\n" \
"v_cvt_pk_bf16_f32 v137, v26, v27\n" \
"v_mfma_f32_16x16x32_bf16 v[40:43], a[176:179], a[48:51], v[40:43]\n" \
"ds_write_b64 v128, v[136:137] offset:2368\n" \
"v_mfma_f32_16x16x32_bf16 v[40:43], a[180:183], a[52:55], v[40:43]\n" \
"v_mul_f32 v28, 4.0, v28\n" \
"v_mul_f32 v29, 4.0, v29\n" \
"v_mfma_f32_16x16x32_bf16 v[40:43], v[66:69], v[196:199], v[40:43]\n" \
"v_mul_f32 v30, 4.0, v30\n" \
"v_mul_f32 v31, 4.0, v31\n" \
"v_mfma_f32_16x16x32_bf16 v[40:43], a[208:211], a[56:59], v[40:43]\n" \
"v_cvt_pk_bf16_f32 v138, v28, v29\n" \
"v_cvt_pk_bf16_f32 v139, v30, v31\n" \
"v_mfma_f32_16x16x32_bf16 v[40:43], a[212:215], a[60:63], v[40:43]\n" \
"ds_write_b64 v128, v[138:139] offset:2400\n" \
"v_mfma_f32_16x16x32_bf16 v[44:47], a[220:223], a[60:63], v[44:47]\n" \
"ds_read_b128 v[148:151], v129 offset:2304\n" \
"ds_read_b128 v[152:155], v129 offset:3456\n" \
"v_mfma_f32_16x16x32_bf16 v[44:47], a[216:219], a[56:59], v[44:47]\n" \
"v_mul_f32 v32, 4.0, v32\n" \
"v_mul_f32 v33, 4.0, v33\n" \
"v_mfma_f32_16x16x32_bf16 v[44:47], a[188:191], a[52:55], v[44:47]\n" \
"v_mul_f32 v34, 4.0, v34\n" \
"v_mul_f32 v35, 4.0, v35\n" \
"v_mfma_f32_16x16x32_bf16 v[44:47], a[184:187], a[48:51], v[44:47]\n" \
"s_add_u32 s70, s88, 65536\n" \
"s_waitcnt lgkmcnt(1)\n" \
"v_cvt_pk_bf16_f32 v136, v32, v33\n" \
"v_mfma_f32_16x16x32_bf16 v[44:47], v[64:67], v[196:199], v[44:47]\n" \
"buffer_store_dwordx4 v[148:151], v130, s[52:55], s70 offen sc1\n" \
"v_cvt_pk_bf16_f32 v137, v34, v35\n" \
"v_mfma_f32_16x16x32_bf16 v[44:47], a[156:159], a[252:255], v[44:47]\n" \
"v_mfma_f32_16x16x32_bf16 v[44:47], a[152:155], a[248:251], v[44:47]\n" \
"s_add_u32 s70, s88, 98304\n" \
"s_waitcnt lgkmcnt(0)\n" \
"ds_write_b64 v128, v[136:137] offset:4608\n" \
"v_mul_f32 v36, 4.0, v36\n" \
"v_mfma_f32_16x16x32_bf16 v[48:51], a[128:131], a[224:227], v[48:51]\n" \
"buffer_store_dwordx4 v[152:155], v130, s[52:55], s70 offen sc1\n" \
"v_mul_f32 v37, 4.0, v37\n" \
"v_mfma_f32_16x16x32_bf16 v[48:51], a[132:135], a[228:231], v[48:51]\n" \
"v_mul_f32 v38, 4.0, v38\n" \
"v_mul_f32 v39, 4.0, v39\n" \
"v_mfma_f32_16x16x32_bf16 v[48:51], a[160:163], a[232:235], v[48:51]\n" \
"s_waitcnt vmcnt(4)\n" \
"v_cvt_pk_bf16_f32 v138, v36, v37\n" \
"v_cvt_pk_bf16_f32 v139, v38, v39\n" \
"v_mfma_f32_16x16x32_bf16 v[48:51], a[164:167], a[236:239], v[48:51]\n" \
"Lpg10849_%=:\n" \
"s_cbranch_execz Lcb1_%=\n" \
"ds_write_b64 v128, v[138:139] offset:4640\n" \
"v_mul_f32 v40, 4.0, v40\n" \
"v_mfma_f32_16x16x32_bf16 v[48:51], v[66:69], v[200:203], v[48:51]\n" \
"v_mul_f32 v41, 4.0, v41\n" \
"v_mul_f32 v42, 4.0, v42\n" \
"v_mfma_f32_16x16x32_bf16 v[48:51], a[192:195], a[240:243], v[48:51]\n" \
"v_mul_f32 v43, 4.0, v43\n" \
"v_cvt_pk_bf16_f32 v136, v40, v41\n" \
"v_mfma_f32_16x16x32_bf16 v[48:51], a[196:199], a[244:247], v[48:51]\n" \
"v_cvt_pk_bf16_f32 v137, v42, v43\n" \
"v_mfma_f32_16x16x32_bf16 v[52:55], a[204:207], a[244:247], v[52:55]\n" \
"ds_write_b64 v128, v[136:137] offset:4672\n" \
"v_mul_f32 v44, 4.0, v44\n" \
"v_mul_f32 v45, 4.0, v45\n" \
"v_mfma_f32_16x16x32_bf16 v[52:55], a[200:203], a[240:243], v[52:55]\n" \
"v_mul_f32 v46, 4.0, v46\n" \
"v_mul_f32 v47, 4.0, v47\n" \
"v_mfma_f32_16x16x32_bf16 v[52:55], a[172:175], a[236:239], v[52:55]\n" \
"v_cvt_pk_bf16_f32 v138, v44, v45\n" \
"v_cvt_pk_bf16_f32 v139, v46, v47\n" \
"v_mfma_f32_16x16x32_bf16 v[52:55], a[168:171], a[232:235], v[52:55]\n" \
"ds_write_b64 v128, v[138:139] offset:4704\n" \
"v_mfma_f32_16x16x32_bf16 v[52:55], v[64:67], v[200:203], v[52:55]\n" \
"ds_read_b128 v[156:159], v129 offset:4608\n" \
"ds_read_b128 v[160:163], v129 offset:5760\n" \
"v_mfma_f32_16x16x32_bf16 v[52:55], a[140:143], a[228:231], v[52:55]\n" \
"v_mul_f32 v48, 4.0, v48\n" \
"v_mul_f32 v49, 4.0, v49\n" \
"v_mfma_f32_16x16x32_bf16 v[52:55], a[136:139], a[224:227], v[52:55]\n" \
"v_mul_f32 v50, 4.0, v50\n" \
"v_mul_f32 v51, 4.0, v51\n" \
"v_mfma_f32_16x16x32_bf16 v[56:59], a[144:147], a[224:227], v[56:59]\n" \
"s_add_u32 s70, s88, 131072\n" \
"s_waitcnt lgkmcnt(1)\n" \
"v_cvt_pk_bf16_f32 v136, v48, v49\n" \
"v_mfma_f32_16x16x32_bf16 v[56:59], a[148:151], a[228:231], v[56:59]\n" \
"buffer_store_dwordx4 v[156:159], v130, s[52:55], s70 offen sc1\n" \
"v_cvt_pk_bf16_f32 v137, v50, v51\n" \
"v_mfma_f32_16x16x32_bf16 v[56:59], a[176:179], a[232:235], v[56:59]\n" \
"ds_write_b64 v128, v[136:137] offset:6912\n" \
"v_mfma_f32_16x16x32_bf16 v[56:59], a[180:183], a[236:239], v[56:59]\n" \
"s_add_u32 s70, s88, 163840\n" \
"s_waitcnt lgkmcnt(1)\n" \
"v_mul_f32 v52, 4.0, v52\n" \
"v_mfma_f32_16x16x32_bf16 v[56:59], v[66:69], v[204:207], v[56:59]\n" \
"buffer_store_dwordx4 v[160:163], v130, s[52:55], s70 offen sc1\n" \
"v_mfma_f32_16x16x32_bf16 v[56:59], a[208:211], a[240:243], v[56:59]\n" \
"v_mul_f32 v53, 4.0, v53\n" \
"v_mul_f32 v54, 4.0, v54\n" \
"v_mfma_f32_16x16x32_bf16 v[56:59], a[212:215], a[244:247], v[56:59]\n" \
"v_mul_f32 v55, 4.0, v55\n" \
"v_cvt_pk_bf16_f32 v138, v52, v53\n" \
"v_cvt_pk_bf16_f32 v139, v54, v55\n" \
"v_mfma_f32_16x16x32_bf16 v[60:63], a[220:223], a[244:247], v[60:63]\n" \
"ds_write_b64 v128, v[138:139] offset:6944\n" \
"v_mfma_f32_16x16x32_bf16 v[60:63], a[216:219], a[240:243], v[60:63]\n" \
"v_mfma_f32_16x16x32_bf16 v[60:63], a[188:191], a[236:239], v[60:63]\n" \
"v_mfma_f32_16x16x32_bf16 v[60:63], a[184:187], a[232:235], v[60:63]\n" \
"v_mul_f32 v56, 4.0, v56\n" \
"v_mul_f32 v57, 4.0, v57\n" \
"v_mfma_f32_16x16x32_bf16 v[60:63], v[64:67], v[204:207], v[60:63]\n" \
"v_mul_f32 v58, 4.0, v58\n" \
"v_mul_f32 v59, 4.0, v59\n" \
"v_mfma_f32_16x16x32_bf16 v[60:63], a[156:159], a[228:231], v[60:63]\n" \
"v_cvt_pk_bf16_f32 v136, v56, v57\n" \
"v_cvt_pk_bf16_f32 v137, v58, v59\n" \
"v_mfma_f32_16x16x32_bf16 v[60:63], a[152:155], a[224:227], v[60:63]\n" \
"ds_write_b64 v128, v[136:137] offset:6976\n" \
"s_nop 7\n" \
"v_mul_f32 v60, 4.0, v60\n" \
"v_mul_f32 v61, 4.0, v61\n" \
"v_mul_f32 v62, 4.0, v62\n" \
"v_mul_f32 v63, 4.0, v63\n" \
"v_cvt_pk_bf16_f32 v138, v60, v61\n" \
"v_cvt_pk_bf16_f32 v139, v62, v63\n" \
"ds_write_b64 v128, v[138:139] offset:7008\n" \
"ds_read_b128 v[164:167], v129 offset:6912\n" \
"ds_read_b128 v[168:171], v129 offset:8064\n" \
"s_add_u32 s70, s88, 196608\n" \
"s_waitcnt lgkmcnt(1)\n" \
"buffer_store_dwordx4 v[164:167], v130, s[52:55], s70 offen sc1\n" \
"s_add_u32 s70, s88, 229376\n" \
"s_waitcnt lgkmcnt(0)\n" \
"buffer_store_dwordx4 v[168:171], v130, s[52:55], s70 offen sc1\n" \
"s_endpgm\n" \
".p2align 12\n" \
"Lrole1_%=:\n" \
"v_mfma_f32_16x16x32_bf16 v[64:67], a[96:99], a[64:67], 0\n" \
"Lpg10938_%=:\n" \
"s_cbranch_execz Lcb1_%=\n" \
"s_waitcnt lgkmcnt(1)\n" \
"s_xor_b32 s82, s48, s49\n" \
"ds_read_b128 a[88:91], v213 offset:4352\n" \
"v_mfma_f32_16x16x32_bf16 v[64:67], a[100:103], a[68:71], v[64:67]\n" \
"s_cmp_lt_u32 s48, s50\n" \
"s_cselect_b32 s82, s82, s48\n" \
"ds_read_b128 a[92:95], v213 offset:6400\n" \
"v_mfma_f32_16x16x32_bf16 v[68:71], a[104:107], a[64:67], 0\n" \
"s_add_u32 s81, s82, s46\n" \
"ds_read_b64_tr_b16 a[112:113], v214 offset:16896\n" \
"v_mfma_f32_16x16x32_bf16 v[68:71], a[108:111], a[68:71], v[68:71]\n" \
"s_add_u32 m0, s86, s79\n" \
"ds_read_b64_tr_b16 a[114:115], v214 offset:18944\n" \
"v_mfma_f32_16x16x32_bf16 v[84:87], a[104:107], a[72:75], 0\n" \
"buffer_load_dwordx4 v234, s[56:59], s81 offen offset:0 sc1 lds\n" \
"v_mfma_f32_16x16x32_bf16 v[84:87], a[108:111], a[76:79], v[84:87]\n" \
"s_waitcnt lgkmcnt(3)\n" \
"ds_read_b64_tr_b16 a[116:117], v214 offset:25088\n" \
"v_mfma_f32_16x16x32_bf16 v[80:83], a[96:99], a[72:75], 0\n" \
"s_lshl_b32 s82, s82, 5\n" \
"ds_read_b64_tr_b16 a[118:119], v214 offset:27136\n" \
"v_cvt_pk_bf16_f32 v0, v0, v1\n" \
"v_mfma_f32_16x16x32_bf16 v[80:83], a[100:103], a[76:79], v[80:83]\n" \
"s_add_u32 s82, s82, s47\n" \
"ds_read_b64_tr_b16 a[120:121], v215 offset:16896\n" \
"v_cvt_pk_bf16_f32 v1, v2, v3\n" \
"v_mfma_f32_16x16x32_bf16 v[96:99], a[96:99], a[80:83], 0\n" \
"buffer_load_dwordx4 v234, s[56:59], s81 offen offset:1024 sc1 lds\n" \
"v_mfma_f32_16x16x32_bf16 v[96:99], a[100:103], a[84:87], v[96:99]\n" \
"s_sub_u32 s85, s85, 32768\n" \
"s_cselect_b32 s85, 131072, s85\n" \
"ds_read_b64_tr_b16 a[122:123], v215 offset:18944\n" \
"v_cvt_pk_bf16_f32 v2, v4, v5\n" \
"v_mfma_f32_16x16x32_bf16 v[100:103], a[104:107], a[80:83], 0\n" \
"s_add_u32 s48, s48, 0x1000\n" \
"ds_read_b64_tr_b16 a[124:125], v215 offset:25088\n" \
"v_add_u32 v213, s85, v211\n" \
"v_mfma_f32_16x16x32_bf16 v[100:103], a[108:111], a[84:87], v[100:103]\n" \
"s_waitcnt lgkmcnt(3)\n" \
"s_sub_u32 s86, s86, 32768\n" \
"s_cselect_b32 s86, 131072, s86\n" \
"ds_read_b64_tr_b16 a[126:127], v215 offset:27136\n" \
"v_mfma_f32_16x16x32_bf16 v[116:119], a[104:107], a[88:91], 0\n" \
"buffer_load_dwordx4 v234, s[56:59], s81 offen offset:2048 sc1 lds\n" \
"v_mfma_f32_16x16x32_bf16 v[116:119], a[108:111], a[92:95], v[116:119]\n" \
"v_add_u32 v214, s85, v212\n" \
"v_xor_b32 v215, 32, v214\n" \
"v_cvt_pk_bf16_f32 v3, v6, v7\n" \
"v_mfma_f32_16x16x32_bf16 v[112:115], a[96:99], a[88:91], 0\n" \
"buffer_load_dwordx4 v234, s[56:59], s81 offen offset:3072 sc1 lds\n" \
"v_mfma_f32_16x16x32_bf16 v[112:115], a[100:103], a[92:95], v[112:115]\n" \
"s_waitcnt vmcnt(20) lgkmcnt(0)\n" \
"s_barrier\n" \
"v_mfma_f32_16x16x32_bf16 v[72:75], a[112:115], a[64:67], 0\n" \
"ds_read_b64_tr_b16 a[96:97], v214 offset:16384\n" \
"ds_read_b64_tr_b16 a[98:99], v214 offset:18432\n" \
"v_mfma_f32_16x16x32_bf16 v[72:75], a[116:119], a[68:71], v[72:75]\n" \
"s_add_u32 m0, m0, 0x4000\n" \
"ds_read_b64_tr_b16 a[100:101], v214 offset:24576\n" \
"ds_read_b64_tr_b16 a[102:103], v214 offset:26624\n" \
"v_cvt_pk_bf16_f32 v4, v8, v9\n" \
"v_mfma_f32_16x16x32_bf16 v[76:79], a[120:123], a[64:67], 0\n" \
"buffer_load_dwordx4 v216, s[36:39], s82 offen offset:0 lds\n" \
"v_mfma_f32_16x16x32_bf16 v[76:79], a[124:127], a[68:71], v[76:79]\n" \
"ds_read_b128 a[64:67], v213 offset:0\n" \
"v_cvt_pk_bf16_f32 v5, v10, v11\n" \
"v_mfma_f32_16x16x32_bf16 v[92:95], a[120:123], a[72:75], 0\n" \
"ds_read_b128 a[68:71], v213 offset:2048\n" \
"v_cvt_pk_bf16_f32 v6, v12, v13\n" \
"v_mfma_f32_16x16x32_bf16 v[92:95], a[124:127], a[76:79], v[92:95]\n" \
"ds_read_b64_tr_b16 a[104:105], v215 offset:16384\n" \
"ds_read_b64_tr_b16 a[106:107], v215 offset:18432\n" \
"v_cvt_pk_bf16_f32 v7, v14, v15\n" \
"v_mfma_f32_16x16x32_bf16 v[88:91], a[112:115], a[72:75], 0\n" \
"buffer_load_dwordx4 v217, s[36:39], s82 offen offset:1024 lds\n" \
"v_mfma_f32_16x16x32_bf16 v[88:91], a[116:119], a[76:79], v[88:91]\n" \
"ds_read_b128 a[72:75], v213 offset:256\n" \
"v_cvt_pk_bf16_f32 v8, v16, v17\n" \
"v_mfma_f32_16x16x32_bf16 v[104:107], a[112:115], a[80:83], 0\n" \
"ds_read_b128 a[76:79], v213 offset:2304\n" \
"v_cvt_pk_bf16_f32 v9, v18, v19\n" \
"v_mfma_f32_16x16x32_bf16 v[104:107], a[116:119], a[84:87], v[104:107]\n" \
"ds_read_b64_tr_b16 a[108:109], v215 offset:24576\n" \
"ds_read_b64_tr_b16 a[110:111], v215 offset:26624\n" \
"v_cvt_pk_bf16_f32 v10, v20, v21\n" \
"v_mfma_f32_16x16x32_bf16 v[108:111], a[120:123], a[80:83], 0\n" \
"buffer_load_dwordx4 v218, s[36:39], s82 offen offset:2048 lds\n" \
"v_mfma_f32_16x16x32_bf16 v[108:111], a[124:127], a[84:87], v[108:111]\n" \
"s_add_u32 s71, s87, 0\n" \
"ds_read_b128 a[80:83], v213 offset:4096\n" \
"v_cvt_pk_bf16_f32 v11, v22, v23\n" \
"v_mfma_f32_16x16x32_bf16 v[124:127], a[120:123], a[88:91], 0\n" \
"s_waitcnt lgkmcnt(4)\n" \
"buffer_store_dwordx4 v[0:3], v234, s[56:59], s71 offen offset:0 sc1\n" \
"v_mfma_f32_16x16x32_bf16 v[124:127], a[124:127], a[92:95], v[124:127]\n" \
"ds_read_b128 a[84:87], v213 offset:6144\n" \
"v_cvt_pk_bf16_f32 v12, v24, v25\n" \
"v_mfma_f32_16x16x32_bf16 v[120:123], a[112:115], a[88:91], 0\n" \
"buffer_load_dwordx4 v219, s[36:39], s82 offen offset:3072 lds\n" \
"v_mfma_f32_16x16x32_bf16 v[120:123], a[116:119], a[92:95], v[120:123]\n" \
"buffer_store_dwordx4 v[4:7], v234, s[56:59], s71 offen offset:1024 sc1\n" \
"buffer_store_dwordx4 v[8:11], v234, s[56:59], s71 offen offset:2048 sc1\n" \
"v_mfma_f32_16x16x32_bf16 v[64:67], a[96:99], a[64:67], v[64:67]\n" \
"s_xor_b32 s82, s48, s49\n" \
"ds_read_b128 a[88:91], v213 offset:4352\n" \
"v_cvt_pk_bf16_f32 v13, v26, v27\n" \
"v_mfma_f32_16x16x32_bf16 v[64:67], a[100:103], a[68:71], v[64:67]\n" \
"s_waitcnt lgkmcnt(2)\n" \
"s_cmp_lt_u32 s48, s50\n" \
"s_cselect_b32 s82, s82, s48\n" \
"ds_read_b128 a[92:95], v213 offset:6400\n" \
"v_mfma_f32_16x16x32_bf16 v[68:71], a[104:107], a[64:67], v[68:71]\n" \
"s_add_u32 s81, s82, s46\n" \
"ds_read_b64_tr_b16 a[112:113], v214 offset:16896\n" \
"v_cvt_pk_bf16_f32 v14, v28, v29\n" \
"v_mfma_f32_16x16x32_bf16 v[68:71], a[108:111], a[68:71], v[68:71]\n" \
"s_add_u32 m0, s86, s79\n" \
"ds_read_b64_tr_b16 a[114:115], v214 offset:18944\n" \
"v_cvt_pk_bf16_f32 v15, v30, v31\n" \
"v_mfma_f32_16x16x32_bf16 v[84:87], a[104:107], a[72:75], v[84:87]\n" \
"buffer_load_dwordx4 v234, s[56:59], s81 offen offset:0 sc1 lds\n" \
"v_mfma_f32_16x16x32_bf16 v[84:87], a[108:111], a[76:79], v[84:87]\n" \
"s_waitcnt lgkmcnt(3)\n" \
"ds_read_b64_tr_b16 a[116:117], v214 offset:25088\n" \
"v_cvt_pk_bf16_f32 v16, v32, v33\n" \
"v_mfma_f32_16x16x32_bf16 v[80:83], a[96:99], a[72:75], v[80:83]\n" \
"s_lshl_b32 s82, s82, 5\n" \
"ds_read_b64_tr_b16 a[118:119], v214 offset:27136\n" \
"v_cvt_pk_bf16_f32 v17, v34, v35\n" \
"v_mfma_f32_16x16x32_bf16 v[80:83], a[100:103], a[76:79], v[80:83]\n" \
"s_add_u32 s82, s82, s47\n" \
"ds_read_b64_tr_b16 a[120:121], v215 offset:16896\n" \
"v_cvt_pk_bf16_f32 v18, v36, v37\n" \
"v_mfma_f32_16x16x32_bf16 v[96:99], a[96:99], a[80:83], v[96:99]\n" \
"buffer_load_dwordx4 v234, s[56:59], s81 offen offset:1024 sc1 lds\n" \
"v_mfma_f32_16x16x32_bf16 v[96:99], a[100:103], a[84:87], v[96:99]\n" \
"s_sub_u32 s85, s85, 32768\n" \
"s_cselect_b32 s85, 131072, s85\n" \
"ds_read_b64_tr_b16 a[122:123], v215 offset:18944\n" \
"v_mfma_f32_16x16x32_bf16 v[100:103], a[104:107], a[80:83], v[100:103]\n" \
"s_add_u32 s48, s48, 0x1000\n" \
"ds_read_b64_tr_b16 a[124:125], v215 offset:25088\n" \
"v_add_u32 v213, s85, v211\n" \
"v_mfma_f32_16x16x32_bf16 v[100:103], a[108:111], a[84:87], v[100:103]\n" \
"s_waitcnt lgkmcnt(3)\n" \
"s_sub_u32 s86, s86, 32768\n" \
"s_cselect_b32 s86, 131072, s86\n" \
"ds_read_b64_tr_b16 a[126:127], v215 offset:27136\n" \
"v_mfma_f32_16x16x32_bf16 v[116:119], a[104:107], a[88:91], v[116:119]\n" \
"buffer_load_dwordx4 v234, s[56:59], s81 offen offset:2048 sc1 lds\n" \
"v_mfma_f32_16x16x32_bf16 v[116:119], a[108:111], a[92:95], v[116:119]\n" \
"v_add_u32 v214, s85, v212\n" \
"v_xor_b32 v215, 32, v214\n" \
"v_mfma_f32_16x16x32_bf16 v[112:115], a[96:99], a[88:91], v[112:115]\n" \
"buffer_load_dwordx4 v234, s[56:59], s81 offen offset:3072 sc1 lds\n" \
"v_mfma_f32_16x16x32_bf16 v[112:115], a[100:103], a[92:95], v[112:115]\n" \
"s_waitcnt vmcnt(23) lgkmcnt(0)\n" \
"s_barrier\n" \
"v_mfma_f32_16x16x32_bf16 v[72:75], a[112:115], a[64:67], v[72:75]\n" \
"ds_read_b64_tr_b16 a[96:97], v214 offset:16384\n" \
"ds_read_b64_tr_b16 a[98:99], v214 offset:18432\n" \
"v_mfma_f32_16x16x32_bf16 v[72:75], a[116:119], a[68:71], v[72:75]\n" \
"s_add_u32 m0, m0, 0x4000\n" \
"ds_read_b64_tr_b16 a[100:101], v214 offset:24576\n" \
"ds_read_b64_tr_b16 a[102:103], v214 offset:26624\n" \
"v_mfma_f32_16x16x32_bf16 v[76:79], a[120:123], a[64:67], v[76:79]\n" \
"buffer_load_dwordx4 v216, s[36:39], s82 offen offset:0 lds\n" \
"v_mfma_f32_16x16x32_bf16 v[76:79], a[124:127], a[68:71], v[76:79]\n" \
"ds_read_b128 a[64:67], v213 offset:0\n" \
"v_cvt_pk_bf16_f32 v19, v38, v39\n" \
"v_mfma_f32_16x16x32_bf16 v[92:95], a[120:123], a[72:75], v[92:95]\n" \
"ds_read_b128 a[68:71], v213 offset:2048\n" \
"v_cvt_pk_bf16_f32 v20, v40, v41\n" \
"v_mfma_f32_16x16x32_bf16 v[92:95], a[124:127], a[76:79], v[92:95]\n" \
"ds_read_b64_tr_b16 a[104:105], v215 offset:16384\n" \
"ds_read_b64_tr_b16 a[106:107], v215 offset:18432\n" \
"v_mfma_f32_16x16x32_bf16 v[88:91], a[112:115], a[72:75], v[88:91]\n" \
"buffer_load_dwordx4 v217, s[36:39], s82 offen offset:1024 lds\n" \
"v_mfma_f32_16x16x32_bf16 v[88:91], a[116:119], a[76:79], v[88:91]\n" \
"ds_read_b128 a[72:75], v213 offset:256\n" \
"v_cvt_pk_bf16_f32 v21, v42, v43\n" \
"v_mfma_f32_16x16x32_bf16 v[104:107], a[112:115], a[80:83], v[104:107]\n" \
"ds_read_b128 a[76:79], v213 offset:2304\n" \
"v_cvt_pk_bf16_f32 v22, v44, v45\n" \
"v_mfma_f32_16x16x32_bf16 v[104:107], a[116:119], a[84:87], v[104:107]\n" \
"ds_read_b64_tr_b16 a[108:109], v215 offset:24576\n" \
"ds_read_b64_tr_b16 a[110:111], v215 offset:26624\n" \
"v_mfma_f32_16x16x32_bf16 v[108:111], a[120:123], a[80:83], v[108:111]\n" \
"buffer_load_dwordx4 v218, s[36:39], s82 offen offset:2048 lds\n" \
"v_mfma_f32_16x16x32_bf16 v[108:111], a[124:127], a[84:87], v[108:111]\n" \
"ds_read_b128 a[80:83], v213 offset:4096\n" \
"v_cvt_pk_bf16_f32 v23, v46, v47\n" \
"v_mfma_f32_16x16x32_bf16 v[124:127], a[120:123], a[88:91], v[124:127]\n" \
"s_waitcnt lgkmcnt(4)\n" \
"buffer_store_dwordx4 v[12:15], v234, s[56:59], s71 offen offset:3072 sc1\n" \
"v_mfma_f32_16x16x32_bf16 v[124:127], a[124:127], a[92:95], v[124:127]\n" \
"s_add_u32 s71, s87, 4096\n" \
"ds_read_b128 a[84:87], v213 offset:6144\n" \
"v_cvt_pk_bf16_f32 v24, v48, v49\n" \
"v_mfma_f32_16x16x32_bf16 v[120:123], a[112:115], a[88:91], v[120:123]\n" \
"buffer_load_dwordx4 v219, s[36:39], s82 offen offset:3072 lds\n" \
"v_mfma_f32_16x16x32_bf16 v[120:123], a[116:119], a[92:95], v[120:123]\n" \
"buffer_store_dwordx4 v[16:19], v234, s[56:59], s71 offen offset:0 sc1\n" \
"buffer_store_dwordx4 v[20:23], v234, s[56:59], s71 offen offset:1024 sc1\n" \
"v_mfma_f32_16x16x32_bf16 v[64:67], a[96:99], a[64:67], v[64:67]\n" \
"s_xor_b32 s82, s48, s49\n" \
"ds_read_b128 a[88:91], v213 offset:4352\n" \
"v_cvt_pk_bf16_f32 v25, v50, v51\n" \
"v_mfma_f32_16x16x32_bf16 v[64:67], a[100:103], a[68:71], v[64:67]\n" \
"s_waitcnt lgkmcnt(2)\n" \
"s_cmp_lt_u32 s48, s50\n" \
"s_cselect_b32 s82, s82, s48\n" \
"ds_read_b128 a[92:95], v213 offset:6400\n" \
"v_mfma_f32_16x16x32_bf16 v[68:71], a[104:107], a[64:67], v[68:71]\n" \
"s_add_u32 s81, s82, s46\n" \
"ds_read_b64_tr_b16 a[112:113], v214 offset:16896\n" \
"v_cvt_pk_bf16_f32 v26, v52, v53\n" \
"v_mfma_f32_16x16x32_bf16 v[68:71], a[108:111], a[68:71], v[68:71]\n" \
"s_add_u32 m0, s86, s79\n" \
"ds_read_b64_tr_b16 a[114:115], v214 offset:18944\n" \
"v_cvt_pk_bf16_f32 v27, v54, v55\n" \
"v_mfma_f32_16x16x32_bf16 v[84:87], a[104:107], a[72:75], v[84:87]\n" \
"buffer_load_dwordx4 v234, s[56:59], s81 offen offset:0 sc1 lds\n" \
"v_mfma_f32_16x16x32_bf16 v[84:87], a[108:111], a[76:79], v[84:87]\n" \
"s_waitcnt lgkmcnt(3)\n" \
"ds_read_b64_tr_b16 a[116:117], v214 offset:25088\n" \
"v_cvt_pk_bf16_f32 v28, v56, v57\n" \
"v_mfma_f32_16x16x32_bf16 v[80:83], a[96:99], a[72:75], v[80:83]\n" \
"s_lshl_b32 s82, s82, 5\n" \
"ds_read_b64_tr_b16 a[118:119], v214 offset:27136\n" \
"v_cvt_pk_bf16_f32 v29, v58, v59\n" \
"v_mfma_f32_16x16x32_bf16 v[80:83], a[100:103], a[76:79], v[80:83]\n" \
"s_add_u32 s82, s82, s47\n" \
"ds_read_b64_tr_b16 a[120:121], v215 offset:16896\n" \
"v_cvt_pk_bf16_f32 v30, v60, v61\n" \
"v_mfma_f32_16x16x32_bf16 v[96:99], a[96:99], a[80:83], v[96:99]\n" \
"buffer_load_dwordx4 v234, s[56:59], s81 offen offset:1024 sc1 lds\n" \
"v_mfma_f32_16x16x32_bf16 v[96:99], a[100:103], a[84:87], v[96:99]\n" \
"s_sub_u32 s85, s85, 32768\n" \
"s_cselect_b32 s85, 131072, s85\n" \
"ds_read_b64_tr_b16 a[122:123], v215 offset:18944\n" \
"v_mfma_f32_16x16x32_bf16 v[100:103], a[104:107], a[80:83], v[100:103]\n" \
"s_add_u32 s48, s48, 0x1000\n" \
"ds_read_b64_tr_b16 a[124:125], v215 offset:25088\n" \
"v_add_u32 v213, s85, v211\n" \
"v_mfma_f32_16x16x32_bf16 v[100:103], a[108:111], a[84:87], v[100:103]\n" \
"s_waitcnt lgkmcnt(3)\n" \
"s_sub_u32 s86, s86, 32768\n" \
"s_cselect_b32 s86, 131072, s86\n" \
"ds_read_b64_tr_b16 a[126:127], v215 offset:27136\n" \
"v_mfma_f32_16x16x32_bf16 v[116:119], a[104:107], a[88:91], v[116:119]\n" \
"buffer_load_dwordx4 v234, s[56:59], s81 offen offset:2048 sc1 lds\n" \
"v_mfma_f32_16x16x32_bf16 v[116:119], a[108:111], a[92:95], v[116:119]\n" \
"v_add_u32 v214, s85, v212\n" \
"v_xor_b32 v215, 32, v214\n" \
"v_mfma_f32_16x16x32_bf16 v[112:115], a[96:99], a[88:91], v[112:115]\n" \
"buffer_load_dwordx4 v234, s[56:59], s81 offen offset:3072 sc1 lds\n" \
"v_mfma_f32_16x16x32_bf16 v[112:115], a[100:103], a[92:95], v[112:115]\n" \
"s_waitcnt vmcnt(26) lgkmcnt(0)\n" \
"s_barrier\n" \
"v_mfma_f32_16x16x32_bf16 v[72:75], a[112:115], a[64:67], v[72:75]\n" \
"ds_read_b64_tr_b16 a[96:97], v214 offset:16384\n" \
"ds_read_b64_tr_b16 a[98:99], v214 offset:18432\n" \
"v_mfma_f32_16x16x32_bf16 v[72:75], a[116:119], a[68:71], v[72:75]\n" \
"s_add_u32 m0, m0, 0x4000\n" \
"ds_read_b64_tr_b16 a[100:101], v214 offset:24576\n" \
"ds_read_b64_tr_b16 a[102:103], v214 offset:26624\n" \
"v_mfma_f32_16x16x32_bf16 v[76:79], a[120:123], a[64:67], v[76:79]\n" \
"buffer_load_dwordx4 v216, s[36:39], s82 offen offset:0 lds\n" \
"v_mfma_f32_16x16x32_bf16 v[76:79], a[124:127], a[68:71], v[76:79]\n" \
"ds_read_b128 a[64:67], v213 offset:0\n" \
"v_cvt_pk_bf16_f32 v31, v62, v63\n" \
"v_mfma_f32_16x16x32_bf16 v[92:95], a[120:123], a[72:75], v[92:95]\n" \
"ds_read_b128 a[68:71], v213 offset:2048\n" \
"v_mfma_f32_16x16x32_bf16 v[92:95], a[124:127], a[76:79], v[92:95]\n" \
"ds_read_b64_tr_b16 a[104:105], v215 offset:16384\n" \
"ds_read_b64_tr_b16 a[106:107], v215 offset:18432\n" \
"v_mfma_f32_16x16x32_bf16 v[88:91], a[112:115], a[72:75], v[88:91]\n" \
"buffer_load_dwordx4 v217, s[36:39], s82 offen offset:1024 lds\n" \
"v_mfma_f32_16x16x32_bf16 v[88:91], a[116:119], a[76:79], v[88:91]\n" \
"ds_read_b128 a[72:75], v213 offset:256\n" \
"v_mfma_f32_16x16x32_bf16 v[104:107], a[112:115], a[80:83], v[104:107]\n" \
"ds_read_b128 a[76:79], v213 offset:2304\n" \
"v_mfma_f32_16x16x32_bf16 v[104:107], a[116:119], a[84:87], v[104:107]\n" \
"ds_read_b64_tr_b16 a[108:109], v215 offset:24576\n" \
"ds_read_b64_tr_b16 a[110:111], v215 offset:26624\n" \
"v_mfma_f32_16x16x32_bf16 v[108:111], a[120:123], a[80:83], v[108:111]\n" \
"buffer_load_dwordx4 v218, s[36:39], s82 offen offset:2048 lds\n" \
"v_mfma_f32_16x16x32_bf16 v[108:111], a[124:127], a[84:87], v[108:111]\n" \
"ds_read_b128 a[80:83], v213 offset:4096\n" \
"v_mfma_f32_16x16x32_bf16 v[124:127], a[120:123], a[88:91], v[124:127]\n" \
"s_waitcnt lgkmcnt(4)\n" \
"buffer_store_dwordx4 v[24:27], v234, s[56:59], s71 offen offset:2048 sc1\n" \
"v_mfma_f32_16x16x32_bf16 v[124:127], a[124:127], a[92:95], v[124:127]\n" \
"ds_read_b128 a[84:87], v213 offset:6144\n" \
"v_mfma_f32_16x16x32_bf16 v[120:123], a[112:115], a[88:91], v[120:123]\n" \
"buffer_load_dwordx4 v219, s[36:39], s82 offen offset:3072 lds\n" \
"v_mfma_f32_16x16x32_bf16 v[120:123], a[116:119], a[92:95], v[120:123]\n" \
"buffer_store_dwordx4 v[28:31], v234, s[56:59], s71 offen offset:3072 sc1\n" \
"v_mfma_f32_16x16x32_bf16 v[64:67], a[96:99], a[64:67], v[64:67]\n" \
"s_waitcnt lgkmcnt(1)\n" \
"s_xor_b32 s82, s48, s49\n" \
"ds_read_b128 a[88:91], v213 offset:4352\n" \
"v_mfma_f32_16x16x32_bf16 v[64:67], a[100:103], a[68:71], v[64:67]\n" \
"s_cmp_lt_u32 s48, s50\n" \
"s_cselect_b32 s82, s82, s48\n" \
"ds_read_b128 a[92:95], v213 offset:6400\n" \
"v_mfma_f32_16x16x32_bf16 v[68:71], a[104:107], a[64:67], v[68:71]\n" \
"s_add_u32 s81, s82, s46\n" \
"ds_read_b64_tr_b16 a[112:113], v214 offset:16896\n" \
"v_mfma_f32_16x16x32_bf16 v[68:71], a[108:111], a[68:71], v[68:71]\n" \
"s_add_u32 m0, s86, s79\n" \
"ds_read_b64_tr_b16 a[114:115], v214 offset:18944\n" \
"v_mfma_f32_16x16x32_bf16 v[84:87], a[104:107], a[72:75], v[84:87]\n" \
"buffer_load_dwordx4 v234, s[56:59], s81 offen offset:0 sc1 lds\n" \
"v_mfma_f32_16x16x32_bf16 v[84:87], a[108:111], a[76:79], v[84:87]\n" \
"ds_read_b64_tr_b16 a[116:117], v214 offset:25088\n" \
"v_mfma_f32_16x16x32_bf16 v[80:83], a[96:99], a[72:75], v[80:83]\n" \
"s_lshl_b32 s82, s82, 5\n" \
"ds_read_b64_tr_b16 a[118:119], v214 offset:27136\n" \
"v_mfma_f32_16x16x32_bf16 v[80:83], a[100:103], a[76:79], v[80:83]\n" \
"s_waitcnt lgkmcnt(3)\n" \
"s_add_u32 s82, s82, s47\n" \
"ds_read_b64_tr_b16 a[120:121], v215 offset:16896\n" \
"v_mfma_f32_16x16x32_bf16 v[96:99], a[96:99], a[80:83], v[96:99]\n" \
"buffer_load_dwordx4 v234, s[56:59], s81 offen offset:1024 sc1 lds\n" \
"v_mfma_f32_16x16x32_bf16 v[96:99], a[100:103], a[84:87], v[96:99]\n" \
"s_sub_u32 s85, s85, 32768\n" \
"s_cselect_b32 s85, 131072, s85\n" \
"ds_read_b64_tr_b16 a[122:123], v215 offset:18944\n" \
"v_mfma_f32_16x16x32_bf16 v[100:103], a[104:107], a[80:83], v[100:103]\n" \
"s_add_u32 s48, s48, 0x1000\n" \
"ds_read_b64_tr_b16 a[124:125], v215 offset:25088\n" \
"v_add_u32 v213, s85, v211\n" \
"v_mfma_f32_16x16x32_bf16 v[100:103], a[108:111], a[84:87], v[100:103]\n" \
"s_sub_u32 s86, s86, 32768\n" \
"s_cselect_b32 s86, 131072, s86\n" \
"ds_read_b64_tr_b16 a[126:127], v215 offset:27136\n" \
"v_mfma_f32_16x16x32_bf16 v[116:119], a[104:107], a[88:91], v[116:119]\n" \
"buffer_load_dwordx4 v234, s[56:59], s81 offen offset:2048 sc1 lds\n" \
"v_mfma_f32_16x16x32_bf16 v[116:119], a[108:111], a[92:95], v[116:119]\n" \
"v_add_u32 v214, s85, v212\n" \
"v_xor_b32 v215, 32, v214\n" \
"v_mfma_f32_16x16x32_bf16 v[112:115], a[96:99], a[88:91], v[112:115]\n" \
"buffer_load_dwordx4 v234, s[56:59], s81 offen offset:3072 sc1 lds\n" \
"v_mfma_f32_16x16x32_bf16 v[112:115], a[100:103], a[92:95], v[112:115]\n" \
"s_waitcnt vmcnt(27) lgkmcnt(0)\n" \
"s_barrier\n" \
"v_mfma_f32_16x16x32_bf16 v[72:75], a[112:115], a[64:67], v[72:75]\n" \
"ds_read_b64_tr_b16 a[96:97], v214 offset:16384\n" \
"ds_read_b64_tr_b16 a[98:99], v214 offset:18432\n" \
"v_mfma_f32_16x16x32_bf16 v[72:75], a[116:119], a[68:71], v[72:75]\n" \
"s_add_u32 m0, m0, 0x4000\n" \
"ds_read_b64_tr_b16 a[100:101], v214 offset:24576\n" \
"ds_read_b64_tr_b16 a[102:103], v214 offset:26624\n" \
"v_mfma_f32_16x16x32_bf16 v[76:79], a[120:123], a[64:67], v[76:79]\n" \
"buffer_load_dwordx4 v216, s[36:39], s82 offen offset:0 lds\n" \
"v_mfma_f32_16x16x32_bf16 v[76:79], a[124:127], a[68:71], v[76:79]\n" \
"ds_read_b128 a[64:67], v213 offset:0\n" \
"v_mfma_f32_16x16x32_bf16 v[92:95], a[120:123], a[72:75], v[92:95]\n" \
"ds_read_b128 a[68:71], v213 offset:2048\n" \
"v_mfma_f32_16x16x32_bf16 v[92:95], a[124:127], a[76:79], v[92:95]\n" \
"ds_read_b64_tr_b16 a[104:105], v215 offset:16384\n" \
"ds_read_b64_tr_b16 a[106:107], v215 offset:18432\n" \
"v_mfma_f32_16x16x32_bf16 v[88:91], a[112:115], a[72:75], v[88:91]\n" \
"buffer_load_dwordx4 v217, s[36:39], s82 offen offset:1024 lds\n" \
"v_mfma_f32_16x16x32_bf16 v[88:91], a[116:119], a[76:79], v[88:91]\n" \
"ds_read_b128 a[72:75], v213 offset:256\n" \
"v_mfma_f32_16x16x32_bf16 v[104:107], a[112:115], a[80:83], v[104:107]\n" \
"ds_read_b128 a[76:79], v213 offset:2304\n" \
"v_mfma_f32_16x16x32_bf16 v[104:107], a[116:119], a[84:87], v[104:107]\n" \
"ds_read_b64_tr_b16 a[108:109], v215 offset:24576\n" \
"ds_read_b64_tr_b16 a[110:111], v215 offset:26624\n" \
"v_mfma_f32_16x16x32_bf16 v[108:111], a[120:123], a[80:83], v[108:111]\n" \
"buffer_load_dwordx4 v218, s[36:39], s82 offen offset:2048 lds\n" \
"v_mfma_f32_16x16x32_bf16 v[108:111], a[124:127], a[84:87], v[108:111]\n" \
"ds_read_b128 a[80:83], v213 offset:4096\n" \
"v_mfma_f32_16x16x32_bf16 v[124:127], a[120:123], a[88:91], v[124:127]\n" \
"s_waitcnt lgkmcnt(4)\n" \
"v_mfma_f32_16x16x32_bf16 v[124:127], a[124:127], a[92:95], v[124:127]\n" \
"ds_read_b128 a[84:87], v213 offset:6144\n" \
"v_mfma_f32_16x16x32_bf16 v[120:123], a[112:115], a[88:91], v[120:123]\n" \
"buffer_load_dwordx4 v219, s[36:39], s82 offen offset:3072 lds\n" \
"v_mfma_f32_16x16x32_bf16 v[120:123], a[116:119], a[92:95], v[120:123]\n" \
"v_mfma_f32_16x16x32_bf16 v[64:67], a[96:99], a[64:67], v[64:67]\n" \
"s_waitcnt lgkmcnt(1)\n" \
"s_xor_b32 s82, s48, s49\n" \
"ds_read_b128 a[88:91], v213 offset:4352\n" \
"v_mfma_f32_16x16x32_bf16 v[64:67], a[100:103], a[68:71], v[64:67]\n" \
"s_cmp_lt_u32 s48, s50\n" \
"s_cselect_b32 s82, s82, s48\n" \
"ds_read_b128 a[92:95], v213 offset:6400\n" \
"v_mfma_f32_16x16x32_bf16 v[68:71], a[104:107], a[64:67], v[68:71]\n" \
"s_add_u32 s81, s82, s46\n" \
"ds_read_b64_tr_b16 a[112:113], v214 offset:16896\n" \
"v_mfma_f32_16x16x32_bf16 v[68:71], a[108:111], a[68:71], v[68:71]\n" \
"s_add_u32 m0, s86, s79\n" \
"ds_read_b64_tr_b16 a[114:115], v214 offset:18944\n" \
"v_mfma_f32_16x16x32_bf16 v[84:87], a[104:107], a[72:75], v[84:87]\n" \
"buffer_load_dwordx4 v234, s[56:59], s81 offen offset:0 sc1 lds\n" \
"v_mfma_f32_16x16x32_bf16 v[84:87], a[108:111], a[76:79], v[84:87]\n" \
"ds_read_b64_tr_b16 a[116:117], v214 offset:25088\n" \
"v_mfma_f32_16x16x32_bf16 v[80:83], a[96:99], a[72:75], v[80:83]\n" \
"s_lshl_b32 s82, s82, 5\n" \
"ds_read_b64_tr_b16 a[118:119], v214 offset:27136\n" \
"v_mfma_f32_16x16x32_bf16 v[80:83], a[100:103], a[76:79], v[80:83]\n" \
"s_waitcnt lgkmcnt(3)\n" \
"s_add_u32 s82, s82, s47\n" \
"ds_read_b64_tr_b16 a[120:121], v215 offset:16896\n" \
"v_mfma_f32_16x16x32_bf16 v[96:99], a[96:99], a[80:83], v[96:99]\n" \
"buffer_load_dwordx4 v234, s[56:59], s81 offen offset:1024 sc1 lds\n" \
"v_mfma_f32_16x16x32_bf16 v[96:99], a[100:103], a[84:87], v[96:99]\n" \
"s_sub_u32 s85, s85, 32768\n" \
"s_cselect_b32 s85, 131072, s85\n" \
"ds_read_b64_tr_b16 a[122:123], v215 offset:18944\n" \
"v_mfma_f32_16x16x32_bf16 v[100:103], a[104:107], a[80:83], v[100:103]\n" \
"s_add_u32 s48, s48, 0x1000\n" \
"ds_read_b64_tr_b16 a[124:125], v215 offset:25088\n" \
"v_add_u32 v213, s85, v211\n" \
"v_mfma_f32_16x16x32_bf16 v[100:103], a[108:111], a[84:87], v[100:103]\n" \
"s_sub_u32 s86, s86, 32768\n" \
"s_cselect_b32 s86, 131072, s86\n" \
"ds_read_b64_tr_b16 a[126:127], v215 offset:27136\n" \
"v_mfma_f32_16x16x32_bf16 v[116:119], a[104:107], a[88:91], v[116:119]\n" \
"buffer_load_dwordx4 v234, s[56:59], s81 offen offset:2048 sc1 lds\n" \
"v_mfma_f32_16x16x32_bf16 v[116:119], a[108:111], a[92:95], v[116:119]\n" \
"v_add_u32 v214, s85, v212\n" \
"v_xor_b32 v215, 32, v214\n" \
"v_mfma_f32_16x16x32_bf16 v[112:115], a[96:99], a[88:91], v[112:115]\n" \
"buffer_load_dwordx4 v234, s[56:59], s81 offen offset:3072 sc1 lds\n" \
"v_mfma_f32_16x16x32_bf16 v[112:115], a[100:103], a[92:95], v[112:115]\n" \
"s_waitcnt vmcnt(24) lgkmcnt(0)\n" \
"s_barrier\n" \
"v_mfma_f32_16x16x32_bf16 v[72:75], a[112:115], a[64:67], v[72:75]\n" \
"ds_read_b64_tr_b16 a[96:97], v214 offset:16384\n" \
"ds_read_b64_tr_b16 a[98:99], v214 offset:18432\n" \
"v_mfma_f32_16x16x32_bf16 v[72:75], a[116:119], a[68:71], v[72:75]\n" \
"s_add_u32 m0, m0, 0x4000\n" \
"ds_read_b64_tr_b16 a[100:101], v214 offset:24576\n" \
"ds_read_b64_tr_b16 a[102:103], v214 offset:26624\n" \
"v_mfma_f32_16x16x32_bf16 v[76:79], a[120:123], a[64:67], v[76:79]\n" \
"buffer_load_dwordx4 v216, s[36:39], s82 offen offset:0 lds\n" \
"v_mfma_f32_16x16x32_bf16 v[76:79], a[124:127], a[68:71], v[76:79]\n" \
"ds_read_b128 a[64:67], v213 offset:0\n" \
"v_mfma_f32_16x16x32_bf16 v[92:95], a[120:123], a[72:75], v[92:95]\n" \
"ds_read_b128 a[68:71], v213 offset:2048\n" \
"v_mfma_f32_16x16x32_bf16 v[92:95], a[124:127], a[76:79], v[92:95]\n" \
"ds_read_b64_tr_b16 a[104:105], v215 offset:16384\n" \
"ds_read_b64_tr_b16 a[106:107], v215 offset:18432\n" \
"v_mfma_f32_16x16x32_bf16 v[88:91], a[112:115], a[72:75], v[88:91]\n" \
"buffer_load_dwordx4 v217, s[36:39], s82 offen offset:1024 lds\n" \
"v_mfma_f32_16x16x32_bf16 v[88:91], a[116:119], a[76:79], v[88:91]\n" \
"ds_read_b128 a[72:75], v213 offset:256\n" \
"v_mfma_f32_16x16x32_bf16 v[104:107], a[112:115], a[80:83], v[104:107]\n" \
"ds_read_b128 a[76:79], v213 offset:2304\n" \
"v_mfma_f32_16x16x32_bf16 v[104:107], a[116:119], a[84:87], v[104:107]\n" \
"ds_read_b64_tr_b16 a[108:109], v215 offset:24576\n" \
"ds_read_b64_tr_b16 a[110:111], v215 offset:26624\n" \
"v_mfma_f32_16x16x32_bf16 v[108:111], a[120:123], a[80:83], v[108:111]\n" \
"buffer_load_dwordx4 v218, s[36:39], s82 offen offset:2048 lds\n" \
"v_mfma_f32_16x16x32_bf16 v[108:111], a[124:127], a[84:87], v[108:111]\n" \
"ds_read_b128 a[80:83], v213 offset:4096\n" \
"v_mfma_f32_16x16x32_bf16 v[124:127], a[120:123], a[88:91], v[124:127]\n" \
"s_waitcnt lgkmcnt(4)\n" \
"v_mfma_f32_16x16x32_bf16 v[124:127], a[124:127], a[92:95], v[124:127]\n" \
"ds_read_b128 a[84:87], v213 offset:6144\n" \
"v_mfma_f32_16x16x32_bf16 v[120:123], a[112:115], a[88:91], v[120:123]\n" \
"buffer_load_dwordx4 v219, s[36:39], s82 offen offset:3072 lds\n" \
"v_mfma_f32_16x16x32_bf16 v[120:123], a[116:119], a[92:95], v[120:123]\n" \
"v_mfma_f32_16x16x32_bf16 v[64:67], a[96:99], a[64:67], v[64:67]\n" \
"s_waitcnt lgkmcnt(1)\n" \
"s_xor_b32 s82, s48, s49\n" \
"ds_read_b128 a[88:91], v213 offset:4352\n" \
"v_mfma_f32_16x16x32_bf16 v[64:67], a[100:103], a[68:71], v[64:67]\n" \
"s_cmp_lt_u32 s48, s50\n" \
"s_cselect_b32 s82, s82, s48\n" \
"ds_read_b128 a[92:95], v213 offset:6400\n" \
"v_mfma_f32_16x16x32_bf16 v[68:71], a[104:107], a[64:67], v[68:71]\n" \
"s_add_u32 s81, s82, s46\n" \
"ds_read_b64_tr_b16 a[112:113], v214 offset:16896\n" \
"v_mfma_f32_16x16x32_bf16 v[68:71], a[108:111], a[68:71], v[68:71]\n" \
"s_add_u32 m0, s86, s79\n" \
"ds_read_b64_tr_b16 a[114:115], v214 offset:18944\n" \
"v_mfma_f32_16x16x32_bf16 v[84:87], a[104:107], a[72:75], v[84:87]\n" \
"buffer_load_dwordx4 v234, s[56:59], s81 offen offset:0 sc1 lds\n" \
"v_mfma_f32_16x16x32_bf16 v[84:87], a[108:111], a[76:79], v[84:87]\n" \
"ds_read_b64_tr_b16 a[116:117], v214 offset:25088\n" \
"v_mfma_f32_16x16x32_bf16 v[80:83], a[96:99], a[72:75], v[80:83]\n" \
"s_lshl_b32 s82, s82, 5\n" \
"ds_read_b64_tr_b16 a[118:119], v214 offset:27136\n" \
"v_mfma_f32_16x16x32_bf16 v[80:83], a[100:103], a[76:79], v[80:83]\n" \
"s_waitcnt lgkmcnt(3)\n" \
"s_add_u32 s82, s82, s47\n" \
"ds_read_b64_tr_b16 a[120:121], v215 offset:16896\n" \
"v_mfma_f32_16x16x32_bf16 v[96:99], a[96:99], a[80:83], v[96:99]\n" \
"buffer_load_dwordx4 v234, s[56:59], s81 offen offset:1024 sc1 lds\n" \
"v_mfma_f32_16x16x32_bf16 v[96:99], a[100:103], a[84:87], v[96:99]\n" \
"s_sub_u32 s85, s85, 32768\n" \
"s_cselect_b32 s85, 131072, s85\n" \
"ds_read_b64_tr_b16 a[122:123], v215 offset:18944\n" \
"v_mfma_f32_16x16x32_bf16 v[100:103], a[104:107], a[80:83], v[100:103]\n" \
"s_add_u32 s48, s48, 0x1000\n" \
"ds_read_b64_tr_b16 a[124:125], v215 offset:25088\n" \
"v_add_u32 v213, s85, v211\n" \
"v_mfma_f32_16x16x32_bf16 v[100:103], a[108:111], a[84:87], v[100:103]\n" \
"s_sub_u32 s86, s86, 32768\n" \
"s_cselect_b32 s86, 131072, s86\n" \
"ds_read_b64_tr_b16 a[126:127], v215 offset:27136\n" \
"v_mfma_f32_16x16x32_bf16 v[116:119], a[104:107], a[88:91], v[116:119]\n" \
"buffer_load_dwordx4 v234, s[56:59], s81 offen offset:2048 sc1 lds\n" \
"v_mfma_f32_16x16x32_bf16 v[116:119], a[108:111], a[92:95], v[116:119]\n" \
"v_add_u32 v214, s85, v212\n" \
"v_xor_b32 v215, 32, v214\n" \
"v_mfma_f32_16x16x32_bf16 v[112:115], a[96:99], a[88:91], v[112:115]\n" \
"buffer_load_dwordx4 v234, s[56:59], s81 offen offset:3072 sc1 lds\n" \
"v_mfma_f32_16x16x32_bf16 v[112:115], a[100:103], a[92:95], v[112:115]\n" \
"s_waitcnt vmcnt(20) lgkmcnt(0)\n" \
"s_barrier\n" \
"buffer_store_dword v230, v231, s[56:59], s64 offen sc1\n" \
"v_mfma_f32_16x16x32_bf16 v[72:75], a[112:115], a[64:67], v[72:75]\n" \
"ds_read_b64_tr_b16 a[96:97], v214 offset:16384\n" \
"ds_read_b64_tr_b16 a[98:99], v214 offset:18432\n" \
"v_mfma_f32_16x16x32_bf16 v[72:75], a[116:119], a[68:71], v[72:75]\n" \
"s_add_u32 m0, m0, 0x4000\n" \
"ds_read_b64_tr_b16 a[100:101], v214 offset:24576\n" \
"ds_read_b64_tr_b16 a[102:103], v214 offset:26624\n" \
"v_mfma_f32_16x16x32_bf16 v[76:79], a[120:123], a[64:67], v[76:79]\n" \
"buffer_load_dwordx4 v216, s[36:39], s82 offen offset:0 lds\n" \
"v_mfma_f32_16x16x32_bf16 v[76:79], a[124:127], a[68:71], v[76:79]\n" \
"ds_read_b128 a[64:67], v213 offset:0\n" \
"v_mfma_f32_16x16x32_bf16 v[92:95], a[120:123], a[72:75], v[92:95]\n" \
"ds_read_b128 a[68:71], v213 offset:2048\n" \
"v_mfma_f32_16x16x32_bf16 v[92:95], a[124:127], a[76:79], v[92:95]\n" \
"ds_read_b64_tr_b16 a[104:105], v215 offset:16384\n" \
"ds_read_b64_tr_b16 a[106:107], v215 offset:18432\n" \
"v_mfma_f32_16x16x32_bf16 v[88:91], a[112:115], a[72:75], v[88:91]\n" \
"buffer_load_dwordx4 v217, s[36:39], s82 offen offset:1024 lds\n" \
"v_mfma_f32_16x16x32_bf16 v[88:91], a[116:119], a[76:79], v[88:91]\n" \
"ds_read_b128 a[72:75], v213 offset:256\n" \
"v_mfma_f32_16x16x32_bf16 v[104:107], a[112:115], a[80:83], v[104:107]\n" \
"ds_read_b128 a[76:79], v213 offset:2304\n" \
"v_mfma_f32_16x16x32_bf16 v[104:107], a[116:119], a[84:87], v[104:107]\n" \
"ds_read_b64_tr_b16 a[108:109], v215 offset:24576\n" \
"ds_read_b64_tr_b16 a[110:111], v215 offset:26624\n" \
"v_mfma_f32_16x16x32_bf16 v[108:111], a[120:123], a[80:83], v[108:111]\n" \
"buffer_load_dwordx4 v218, s[36:39], s82 offen offset:2048 lds\n" \
"v_mfma_f32_16x16x32_bf16 v[108:111], a[124:127], a[84:87], v[108:111]\n" \
"ds_read_b128 a[80:83], v213 offset:4096\n" \
"v_mfma_f32_16x16x32_bf16 v[124:127], a[120:123], a[88:91], v[124:127]\n" \
"s_waitcnt lgkmcnt(4)\n" \
"v_mfma_f32_16x16x32_bf16 v[124:127], a[124:127], a[92:95], v[124:127]\n" \
"ds_read_b128 a[84:87], v213 offset:6144\n" \
"v_mfma_f32_16x16x32_bf16 v[120:123], a[112:115], a[88:91], v[120:123]\n" \
"buffer_load_dwordx4 v219, s[36:39], s82 offen offset:3072 lds\n" \
"v_mfma_f32_16x16x32_bf16 v[120:123], a[116:119], a[92:95], v[120:123]\n" \
"s_mov_b32 s72, 5\n" \
"Lo64_%=:\n" \
"v_mfma_f32_16x16x32_bf16 v[64:67], a[96:99], a[64:67], v[64:67]\n" \
"s_waitcnt lgkmcnt(1)\n" \
"s_xor_b32 s82, s48, s49\n" \
"ds_read_b128 a[88:91], v213 offset:4352\n" \
"Lpg11500_%=:\n" \
"s_cbranch_execz Lcb1_%=\n" \
"v_mfma_f32_16x16x32_bf16 v[64:67], a[100:103], a[68:71], v[64:67]\n" \
"s_cmp_lt_u32 s48, s50\n" \
"s_cselect_b32 s82, s82, s48\n" \
"ds_read_b128 a[92:95], v213 offset:6400\n" \
"v_mfma_f32_16x16x32_bf16 v[68:71], a[104:107], a[64:67], v[68:71]\n" \
"s_add_u32 s81, s82, s46\n" \
"ds_read_b64_tr_b16 a[112:113], v214 offset:16896\n" \
"v_mfma_f32_16x16x32_bf16 v[68:71], a[108:111], a[68:71], v[68:71]\n" \
"s_add_u32 m0, s86, s79\n" \
"ds_read_b64_tr_b16 a[114:115], v214 offset:18944\n" \
"v_mfma_f32_16x16x32_bf16 v[84:87], a[104:107], a[72:75], v[84:87]\n" \
"buffer_load_dwordx4 v234, s[56:59], s81 offen offset:0 sc1 lds\n" \
"v_mfma_f32_16x16x32_bf16 v[84:87], a[108:111], a[76:79], v[84:87]\n" \
"ds_read_b64_tr_b16 a[116:117], v214 offset:25088\n" \
"v_mfma_f32_16x16x32_bf16 v[80:83], a[96:99], a[72:75], v[80:83]\n" \
"s_lshl_b32 s82, s82, 5\n" \
"ds_read_b64_tr_b16 a[118:119], v214 offset:27136\n" \
"v_mfma_f32_16x16x32_bf16 v[80:83], a[100:103], a[76:79], v[80:83]\n" \
"s_waitcnt lgkmcnt(3)\n" \
"s_add_u32 s82, s82, s47\n" \
"ds_read_b64_tr_b16 a[120:121], v215 offset:16896\n" \
"v_mfma_f32_16x16x32_bf16 v[96:99], a[96:99], a[80:83], v[96:99]\n" \
"buffer_load_dwordx4 v234, s[56:59], s81 offen offset:1024 sc1 lds\n" \
"v_mfma_f32_16x16x32_bf16 v[96:99], a[100:103], a[84:87], v[96:99]\n" \
"s_sub_u32 s85, s85, 32768\n" \
"s_cselect_b32 s85, 131072, s85\n" \
"ds_read_b64_tr_b16 a[122:123], v215 offset:18944\n" \
"v_mfma_f32_16x16x32_bf16 v[100:103], a[104:107], a[80:83], v[100:103]\n" \
"s_add_u32 s48, s48, 0x1000\n" \
"ds_read_b64_tr_b16 a[124:125], v215 offset:25088\n" \
"v_add_u32 v213, s85, v211\n" \
"v_mfma_f32_16x16x32_bf16 v[100:103], a[108:111], a[84:87], v[100:103]\n" \
"s_sub_u32 s86, s86, 32768\n" \
"s_cselect_b32 s86, 131072, s86\n" \
"ds_read_b64_tr_b16 a[126:127], v215 offset:27136\n" \
"v_mfma_f32_16x16x32_bf16 v[116:119], a[104:107], a[88:91], v[116:119]\n" \
"buffer_load_dwordx4 v234, s[56:59], s81 offen offset:2048 sc1 lds\n" \
"v_mfma_f32_16x16x32_bf16 v[116:119], a[108:111], a[92:95], v[116:119]\n" \
"v_add_u32 v214, s85, v212\n" \
"v_xor_b32 v215, 32, v214\n" \
"v_mfma_f32_16x16x32_bf16 v[112:115], a[96:99], a[88:91], v[112:115]\n" \
"buffer_load_dwordx4 v234, s[56:59], s81 offen offset:3072 sc1 lds\n" \
"v_mfma_f32_16x16x32_bf16 v[112:115], a[100:103], a[92:95], v[112:115]\n" \
"s_waitcnt vmcnt(20) lgkmcnt(0)\n" \
"s_barrier\n" \
"v_mfma_f32_16x16x32_bf16 v[72:75], a[112:115], a[64:67], v[72:75]\n" \
"ds_read_b64_tr_b16 a[96:97], v214 offset:16384\n" \
"ds_read_b64_tr_b16 a[98:99], v214 offset:18432\n" \
"v_mfma_f32_16x16x32_bf16 v[72:75], a[116:119], a[68:71], v[72:75]\n" \
"s_add_u32 m0, m0, 0x4000\n" \
"ds_read_b64_tr_b16 a[100:101], v214 offset:24576\n" \
"ds_read_b64_tr_b16 a[102:103], v214 offset:26624\n" \
"v_mfma_f32_16x16x32_bf16 v[76:79], a[120:123], a[64:67], v[76:79]\n" \
"buffer_load_dwordx4 v216, s[36:39], s82 offen offset:0 lds\n" \
"v_mfma_f32_16x16x32_bf16 v[76:79], a[124:127], a[68:71], v[76:79]\n" \
"ds_read_b128 a[64:67], v213 offset:0\n" \
"v_mfma_f32_16x16x32_bf16 v[92:95], a[120:123], a[72:75], v[92:95]\n" \
"ds_read_b128 a[68:71], v213 offset:2048\n" \
"v_mfma_f32_16x16x32_bf16 v[92:95], a[124:127], a[76:79], v[92:95]\n" \
"ds_read_b64_tr_b16 a[104:105], v215 offset:16384\n" \
"ds_read_b64_tr_b16 a[106:107], v215 offset:18432\n" \
"v_mfma_f32_16x16x32_bf16 v[88:91], a[112:115], a[72:75], v[88:91]\n" \
"buffer_load_dwordx4 v217, s[36:39], s82 offen offset:1024 lds\n" \
"v_mfma_f32_16x16x32_bf16 v[88:91], a[116:119], a[76:79], v[88:91]\n" \
"ds_read_b128 a[72:75], v213 offset:256\n" \
"v_mfma_f32_16x16x32_bf16 v[104:107], a[112:115], a[80:83], v[104:107]\n" \
"ds_read_b128 a[76:79], v213 offset:2304\n" \
"v_mfma_f32_16x16x32_bf16 v[104:107], a[116:119], a[84:87], v[104:107]\n" \
"ds_read_b64_tr_b16 a[108:109], v215 offset:24576\n" \
"ds_read_b64_tr_b16 a[110:111], v215 offset:26624\n" \
"v_mfma_f32_16x16x32_bf16 v[108:111], a[120:123], a[80:83], v[108:111]\n" \
"buffer_load_dwordx4 v218, s[36:39], s82 offen offset:2048 lds\n" \
"v_mfma_f32_16x16x32_bf16 v[108:111], a[124:127], a[84:87], v[108:111]\n" \
"ds_read_b128 a[80:83], v213 offset:4096\n" \
"v_mfma_f32_16x16x32_bf16 v[124:127], a[120:123], a[88:91], v[124:127]\n" \
"s_waitcnt lgkmcnt(4)\n" \
"v_mfma_f32_16x16x32_bf16 v[124:127], a[124:127], a[92:95], v[124:127]\n" \
"s_sub_u32 s72, s72, 1\n" \
"ds_read_b128 a[84:87], v213 offset:6144\n" \
"v_mfma_f32_16x16x32_bf16 v[120:123], a[112:115], a[88:91], v[120:123]\n" \
"buffer_load_dwordx4 v219, s[36:39], s82 offen offset:3072 lds\n" \
"v_mfma_f32_16x16x32_bf16 v[120:123], a[116:119], a[92:95], v[120:123]\n" \
"s_cbranch_scc0 Lo64_%=\n" \
"v_mfma_f32_16x16x32_bf16 v[64:67], a[96:99], a[64:67], v[64:67]\n" \
"s_waitcnt lgkmcnt(1)\n" \
"ds_read_b128 a[88:91], v213 offset:4352\n" \
"v_mfma_f32_16x16x32_bf16 v[64:67], a[100:103], a[68:71], v[64:67]\n" \
"s_add_u32 s48, s86, 32768\n" \
"s_cmp_eq_u32 s48, 163840\n" \
"s_cselect_b32 s48, 0, s48\n" \
"ds_read_b128 a[92:95], v213 offset:6400\n" \
"v_mfma_f32_16x16x32_bf16 v[68:71], a[104:107], a[64:67], v[68:71]\n" \
"ds_read_b64_tr_b16 a[112:113], v214 offset:16896\n" \
"v_mfma_f32_16x16x32_bf16 v[68:71], a[108:111], a[68:71], v[68:71]\n" \
"ds_read_b64_tr_b16 a[114:115], v214 offset:18944\n" \
"v_mfma_f32_16x16x32_bf16 v[84:87], a[104:107], a[72:75], v[84:87]\n" \
"s_add_u32 s49, s48, 32768\n" \
"s_cmp_eq_u32 s49, 163840\n" \
"s_cselect_b32 s49, 0, s49\n" \
"v_mfma_f32_16x16x32_bf16 v[84:87], a[108:111], a[76:79], v[84:87]\n" \
"ds_read_b64_tr_b16 a[116:117], v214 offset:25088\n" \
"v_add_u32 v224, s49, v211\n" \
"v_mfma_f32_16x16x32_bf16 v[80:83], a[96:99], a[72:75], v[80:83]\n" \
"ds_read_b64_tr_b16 a[118:119], v214 offset:27136\n" \
"v_add_u32 v225, s49, v212\n" \
"v_mfma_f32_16x16x32_bf16 v[80:83], a[100:103], a[76:79], v[80:83]\n" \
"s_waitcnt lgkmcnt(3)\n" \
"ds_read_b64_tr_b16 a[120:121], v215 offset:16896\n" \
"v_xor_b32 v226, 32, v225\n" \
"v_mfma_f32_16x16x32_bf16 v[96:99], a[96:99], a[80:83], v[96:99]\n" \
"v_add_u32 v227, s48, v211\n" \
"v_add_u32 v228, s48, v212\n" \
"v_mfma_f32_16x16x32_bf16 v[96:99], a[100:103], a[84:87], v[96:99]\n" \
"s_sub_u32 s85, s85, 32768\n" \
"s_cselect_b32 s85, 131072, s85\n" \
"ds_read_b64_tr_b16 a[122:123], v215 offset:18944\n" \
"v_mfma_f32_16x16x32_bf16 v[100:103], a[104:107], a[80:83], v[100:103]\n" \
"ds_read_b64_tr_b16 a[124:125], v215 offset:25088\n" \
"v_add_u32 v213, s85, v211\n" \
"v_mfma_f32_16x16x32_bf16 v[100:103], a[108:111], a[84:87], v[100:103]\n" \
"ds_read_b64_tr_b16 a[126:127], v215 offset:27136\n" \
"v_xor_b32 v229, 32, v228\n" \
"v_mfma_f32_16x16x32_bf16 v[116:119], a[104:107], a[88:91], v[116:119]\n" \
"v_mfma_f32_16x16x32_bf16 v[116:119], a[108:111], a[92:95], v[116:119]\n" \
"v_add_u32 v214, s85, v212\n" \
"v_xor_b32 v215, 32, v214\n" \
"v_mfma_f32_16x16x32_bf16 v[112:115], a[96:99], a[88:91], v[112:115]\n" \
"v_mfma_f32_16x16x32_bf16 v[112:115], a[100:103], a[92:95], v[112:115]\n" \
"s_waitcnt vmcnt(8) lgkmcnt(0)\n" \
"s_barrier\n" \
"v_mfma_f32_16x16x32_bf16 v[72:75], a[112:115], a[64:67], v[72:75]\n" \
"ds_read_b128 a[0:3], v213 offset:0\n" \
"ds_read_b128 a[4:7], v213 offset:2048\n" \
"v_mfma_f32_16x16x32_bf16 v[72:75], a[116:119], a[68:71], v[72:75]\n" \
"s_sub_u32 s68, s86, 32768\n" \
"s_cselect_b32 s68, 131072, s68\n" \
"ds_read_b64_tr_b16 a[128:129], v214 offset:16384\n" \
"ds_read_b64_tr_b16 a[130:131], v214 offset:18432\n" \
"v_mfma_f32_16x16x32_bf16 v[76:79], a[120:123], a[64:67], v[76:79]\n" \
"ds_read_b64_tr_b16 a[132:133], v214 offset:24576\n" \
"ds_read_b64_tr_b16 a[134:135], v214 offset:26624\n" \
"v_mfma_f32_16x16x32_bf16 v[76:79], a[124:127], a[68:71], v[76:79]\n" \
"s_cmp_lt_u32 s61, 2\n" \
"s_cselect_b32 s68, s86, s68\n" \
"ds_read_b64_tr_b16 a[136:137], v215 offset:16384\n" \
"ds_read_b64_tr_b16 a[138:139], v215 offset:18432\n" \
"v_mfma_f32_16x16x32_bf16 v[92:95], a[120:123], a[72:75], v[92:95]\n" \
"s_and_b32 s50, s61, 1\n" \
"ds_read_b64_tr_b16 a[140:141], v215 offset:24576\n" \
"ds_read_b64_tr_b16 a[142:143], v215 offset:26624\n" \
"v_mfma_f32_16x16x32_bf16 v[92:95], a[124:127], a[76:79], v[92:95]\n" \
"s_mul_i32 s50, s50, 9216\n" \
"ds_read_b64_tr_b16 a[144:145], v214 offset:16896\n" \
"ds_read_b64_tr_b16 a[146:147], v214 offset:18944\n" \
"v_mfma_f32_16x16x32_bf16 v[88:91], a[112:115], a[72:75], v[88:91]\n" \
"s_add_u32 s68, s68, s50\n" \
"ds_read_b64_tr_b16 a[148:149], v214 offset:25088\n" \
"ds_read_b64_tr_b16 a[150:151], v214 offset:27136\n" \
"v_mfma_f32_16x16x32_bf16 v[88:91], a[116:119], a[76:79], v[88:91]\n" \
"s_add_u32 s68, s68, %3\n" \
"ds_read_b64_tr_b16 a[152:153], v215 offset:16896\n" \
"ds_read_b64_tr_b16 a[154:155], v215 offset:18944\n" \
"v_mfma_f32_16x16x32_bf16 v[104:107], a[112:115], a[80:83], v[104:107]\n" \
"ds_read_b64_tr_b16 a[156:157], v215 offset:25088\n" \
"ds_read_b64_tr_b16 a[158:159], v215 offset:27136\n" \
"v_mfma_f32_16x16x32_bf16 v[104:107], a[116:119], a[84:87], v[104:107]\n" \
"ds_read_b128 a[24:27], v213 offset:256\n" \
"ds_read_b128 a[28:31], v213 offset:2304\n" \
"v_mfma_f32_16x16x32_bf16 v[108:111], a[120:123], a[80:83], v[108:111]\n" \
"ds_read_b128 a[8:11], v224 offset:0\n" \
"ds_read_b128 a[12:15], v224 offset:2048\n" \
"v_mfma_f32_16x16x32_bf16 v[108:111], a[124:127], a[84:87], v[108:111]\n" \
"ds_read_b64_tr_b16 a[160:161], v225 offset:16384\n" \
"ds_read_b64_tr_b16 a[162:163], v225 offset:18432\n" \
"v_mfma_f32_16x16x32_bf16 v[124:127], a[120:123], a[88:91], v[124:127]\n" \
"ds_read_b64_tr_b16 a[164:165], v225 offset:24576\n" \
"ds_read_b64_tr_b16 a[166:167], v225 offset:26624\n" \
"v_mfma_f32_16x16x32_bf16 v[124:127], a[124:127], a[92:95], v[124:127]\n" \
"ds_read_b64_tr_b16 a[168:169], v226 offset:16384\n" \
"ds_read_b64_tr_b16 a[170:171], v226 offset:18432\n" \
"v_mfma_f32_16x16x32_bf16 v[120:123], a[112:115], a[88:91], v[120:123]\n" \
"ds_read_b64_tr_b16 a[172:173], v226 offset:24576\n" \
"ds_read_b64_tr_b16 a[174:175], v226 offset:26624\n" \
"v_mfma_f32_16x16x32_bf16 v[120:123], a[116:119], a[92:95], v[120:123]\n" \
"s_waitcnt lgkmcnt(8)\n" \
"ds_read_b64_tr_b16 a[176:177], v225 offset:16896\n" \
"ds_read_b64_tr_b16 a[178:179], v225 offset:18944\n" \
"v_mfma_f32_16x16x32_bf16 v[64:67], a[128:131], a[0:3], v[64:67]\n" \
"ds_read_b64_tr_b16 a[180:181], v225 offset:25088\n" \
"ds_read_b64_tr_b16 a[182:183], v225 offset:27136\n" \
"v_mfma_f32_16x16x32_bf16 v[64:67], a[132:135], a[4:7], v[64:67]\n" \
"ds_read_b64_tr_b16 a[184:185], v226 offset:16896\n" \
"ds_read_b64_tr_b16 a[186:187], v226 offset:18944\n" \
"v_mfma_f32_16x16x32_bf16 v[68:71], a[136:139], a[0:3], v[68:71]\n" \
"ds_read_b64_tr_b16 a[188:189], v226 offset:25088\n" \
"ds_read_b64_tr_b16 a[190:191], v226 offset:27136\n" \
"v_mfma_f32_16x16x32_bf16 v[68:71], a[140:143], a[4:7], v[68:71]\n" \
"v_lshrrev_b32 v132, 4, v234\n" \
"v_and_b32 v133, 15, v132\n" \
"v_mfma_f32_16x16x32_bf16 v[72:75], a[144:147], a[0:3], v[72:75]\n" \
"v_mul_u32_u24 v128, 144, v133\n" \
"v_lshrrev_b32 v133, 4, v132\n" \
"v_mfma_f32_16x16x32_bf16 v[72:75], a[148:151], a[4:7], v[72:75]\n" \
"v_lshl_add_u32 v128, v133, 3, v128\n" \
"v_add_u32 v128, s68, v128\n" \
"v_mfma_f32_16x16x32_bf16 v[76:79], a[152:155], a[0:3], v[76:79]\n" \
"v_lshrrev_b32 v133, 3, v132\n" \
"v_and_b32 v134, 7, v132\n" \
"v_mfma_f32_16x16x32_bf16 v[76:79], a[156:159], a[4:7], v[76:79]\n" \
"v_mul_u32_u24 v129, 144, v133\n" \
"v_lshl_add_u32 v129, v134, 4, v129\n" \
"v_mfma_f32_16x16x32_bf16 v[92:95], a[152:155], a[24:27], v[92:95]\n" \
"v_add_u32 v129, s68, v129\n" \
"v_lshlrev_b32 v134, 4, v134\n" \
"v_mfma_f32_16x16x32_bf16 v[92:95], a[156:159], a[28:31], v[92:95]\n" \
"v_lshl_add_u32 v130, v133, 13, v134\n" \
"v_mfma_f32_16x16x32_bf16 v[88:91], a[144:147], a[24:27], v[88:91]\n" \
"v_mfma_f32_16x16x32_bf16 v[88:91], a[148:151], a[28:31], v[88:91]\n" \
"v_mfma_f32_16x16x32_bf16 v[84:87], a[136:139], a[24:27], v[84:87]\n" \
"s_waitcnt vmcnt(0)\n" \
"s_barrier\n" \
"v_mfma_f32_16x16x32_bf16 v[84:87], a[140:143], a[28:31], v[84:87]\n" \
"ds_read_b128 a[32:35], v224 offset:256\n" \
"ds_read_b128 a[36:39], v224 offset:2304\n" \
"v_mfma_f32_16x16x32_bf16 v[80:83], a[128:131], a[24:27], v[80:83]\n" \
"ds_read_b128 a[16:19], v227 offset:0\n" \
"ds_read_b128 a[20:23], v227 offset:2048\n" \
"v_mfma_f32_16x16x32_bf16 v[80:83], a[132:135], a[28:31], v[80:83]\n" \
"s_waitcnt lgkmcnt(4)\n" \
"ds_read_b64_tr_b16 a[192:193], v228 offset:16384\n" \
"ds_read_b64_tr_b16 a[194:195], v228 offset:18432\n" \
"v_mfma_f32_16x16x32_bf16 v[64:67], a[160:163], a[8:11], v[64:67]\n" \
"ds_read_b64_tr_b16 a[196:197], v228 offset:24576\n" \
"ds_read_b64_tr_b16 a[198:199], v228 offset:26624\n" \
"v_mfma_f32_16x16x32_bf16 v[64:67], a[164:167], a[12:15], v[64:67]\n" \
"ds_read_b64_tr_b16 a[200:201], v229 offset:16384\n" \
"ds_read_b64_tr_b16 a[202:203], v229 offset:18432\n" \
"v_mfma_f32_16x16x32_bf16 v[68:71], a[168:171], a[8:11], v[68:71]\n" \
"ds_read_b64_tr_b16 a[204:205], v229 offset:24576\n" \
"ds_read_b64_tr_b16 a[206:207], v229 offset:26624\n" \
"v_mfma_f32_16x16x32_bf16 v[68:71], a[172:175], a[12:15], v[68:71]\n" \
"ds_read_b64_tr_b16 a[208:209], v228 offset:16896\n" \
"ds_read_b64_tr_b16 a[210:211], v228 offset:18944\n" \
"v_mfma_f32_16x16x32_bf16 v[72:75], a[176:179], a[8:11], v[72:75]\n" \
"ds_read_b64_tr_b16 a[212:213], v228 offset:25088\n" \
"ds_read_b64_tr_b16 a[214:215], v228 offset:27136\n" \
"v_mfma_f32_16x16x32_bf16 v[72:75], a[180:183], a[12:15], v[72:75]\n" \
"ds_read_b64_tr_b16 a[216:217], v229 offset:16896\n" \
"ds_read_b64_tr_b16 a[218:219], v229 offset:18944\n" \
"v_mfma_f32_16x16x32_bf16 v[76:79], a[184:187], a[8:11], v[76:79]\n" \
"ds_read_b64_tr_b16 a[220:221], v229 offset:25088\n" \
"ds_read_b64_tr_b16 a[222:223], v229 offset:27136\n" \
"v_mfma_f32_16x16x32_bf16 v[76:79], a[188:191], a[12:15], v[76:79]\n" \
"s_waitcnt lgkmcnt(8)\n" \
"ds_read_b128 a[40:43], v227 offset:256\n" \
"ds_read_b128 a[44:47], v227 offset:2304\n" \
"v_mfma_f32_16x16x32_bf16 v[92:95], a[184:187], a[32:35], v[92:95]\n" \
"ds_read_b128 a[248:251], v213 offset:4096\n" \
"ds_read_b128 a[252:255], v213 offset:6144\n" \
"v_mfma_f32_16x16x32_bf16 v[92:95], a[188:191], a[36:39], v[92:95]\n" \
"ds_read_b128 a[48:51], v224 offset:4096\n" \
"ds_read_b128 a[52:55], v224 offset:6144\n" \
"v_mfma_f32_16x16x32_bf16 v[88:91], a[176:179], a[32:35], v[88:91]\n" \
"ds_read_b128 a[56:59], v227 offset:4096\n" \
"ds_read_b128 a[60:63], v227 offset:6144\n" \
"v_mfma_f32_16x16x32_bf16 v[88:91], a[180:183], a[36:39], v[88:91]\n" \
"ds_read_b128 a[224:227], v213 offset:4352\n" \
"ds_read_b128 a[228:231], v213 offset:6400\n" \
"v_mfma_f32_16x16x32_bf16 v[84:87], a[168:171], a[32:35], v[84:87]\n" \
"ds_read_b128 a[232:235], v224 offset:4352\n" \
"ds_read_b128 a[236:239], v224 offset:6400\n" \
"v_mfma_f32_16x16x32_bf16 v[84:87], a[172:175], a[36:39], v[84:87]\n" \
"ds_read_b128 a[240:243], v227 offset:4352\n" \
"ds_read_b128 a[244:247], v227 offset:6400\n" \
"v_mfma_f32_16x16x32_bf16 v[80:83], a[160:163], a[32:35], v[80:83]\n" \
"v_mfma_f32_16x16x32_bf16 v[80:83], a[164:167], a[36:39], v[80:83]\n" \
"v_mfma_f32_16x16x32_bf16 v[64:67], a[192:195], a[16:19], v[64:67]\n" \
"v_mfma_f32_16x16x32_bf16 v[64:67], a[196:199], a[20:23], v[64:67]\n" \
"v_mfma_f32_16x16x32_bf16 v[68:71], a[200:203], a[16:19], v[68:71]\n" \
"v_mfma_f32_16x16x32_bf16 v[68:71], a[204:207], a[20:23], v[68:71]\n" \
"s_waitcnt lgkmcnt(0)\n" \
"v_mfma_f32_16x16x32_bf16 v[72:75], a[208:211], a[16:19], v[72:75]\n" \
"v_mfma_f32_16x16x32_bf16 v[72:75], a[212:215], a[20:23], v[72:75]\n" \
"v_cvt_pk_bf16_f32 v136, v64, v65\n" \
"v_cvt_pk_bf16_f32 v137, v66, v67\n" \
"v_mfma_f32_16x16x32_bf16 v[76:79], a[216:219], a[16:19], v[76:79]\n" \
"v_mfma_f32_16x16x32_bf16 v[76:79], a[220:223], a[20:23], v[76:79]\n" \
"ds_write_b64 v128, v[136:137] offset:0\n" \
"v_mfma_f32_16x16x32_bf16 v[80:83], a[192:195], a[40:43], v[80:83]\n" \
"v_cvt_pk_bf16_f32 v138, v68, v69\n" \
"v_cvt_pk_bf16_f32 v139, v70, v71\n" \
"v_mfma_f32_16x16x32_bf16 v[80:83], a[196:199], a[44:47], v[80:83]\n" \
"ds_write_b64 v128, v[138:139] offset:32\n" \
"v_mfma_f32_16x16x32_bf16 v[84:87], a[200:203], a[40:43], v[84:87]\n" \
"v_cvt_pk_bf16_f32 v136, v72, v73\n" \
"v_cvt_pk_bf16_f32 v137, v74, v75\n" \
"v_mfma_f32_16x16x32_bf16 v[84:87], a[204:207], a[44:47], v[84:87]\n" \
"ds_write_b64 v128, v[136:137] offset:64\n" \
"v_mfma_f32_16x16x32_bf16 v[88:91], a[208:211], a[40:43], v[88:91]\n" \
"v_cvt_pk_bf16_f32 v138, v76, v77\n" \
"v_cvt_pk_bf16_f32 v139, v78, v79\n" \
"v_mfma_f32_16x16x32_bf16 v[88:91], a[212:215], a[44:47], v[88:91]\n" \
"ds_write_b64 v128, v[138:139] offset:96\n" \
"v_mfma_f32_16x16x32_bf16 v[92:95], a[216:219], a[40:43], v[92:95]\n" \
"ds_read_b128 v[140:143], v129 offset:0\n" \
"ds_read_b128 v[144:147], v129 offset:1152\n" \
"v_mfma_f32_16x16x32_bf16 v[92:95], a[220:223], a[44:47], v[92:95]\n" \
"v_cvt_pk_bf16_f32 v136, v80, v81\n" \
"v_cvt_pk_bf16_f32 v137, v82, v83\n" \
"v_mfma_f32_16x16x32_bf16 v[96:99], a[128:131], a[248:251], v[96:99]\n" \
"ds_write_b64 v128, v[136:137] offset:2304\n" \
"v_mfma_f32_16x16x32_bf16 v[96:99], a[132:135], a[252:255], v[96:99]\n" \
"s_add_u32 s70, s88, 0\n" \
"s_waitcnt lgkmcnt(2)\n" \
"v_cvt_pk_bf16_f32 v138, v84, v85\n" \
"v_mfma_f32_16x16x32_bf16 v[96:99], a[160:163], a[48:51], v[96:99]\n" \
"buffer_store_dwordx4 v[140:143], v130, s[52:55], s70 offen sc1\n" \
"v_mfma_f32_16x16x32_bf16 v[96:99], a[164:167], a[52:55], v[96:99]\n" \
"v_cvt_pk_bf16_f32 v139, v86, v87\n" \
"v_mfma_f32_16x16x32_bf16 v[96:99], a[192:195], a[56:59], v[96:99]\n" \
"s_add_u32 s70, s88, 65536\n" \
"s_waitcnt lgkmcnt(1)\n" \
"v_mfma_f32_16x16x32_bf16 v[96:99], a[196:199], a[60:63], v[96:99]\n" \
"buffer_store_dwordx4 v[144:147], v130, s[52:55], s70 offen sc1\n" \
"ds_write_b64 v128, v[138:139] offset:2336\n" \
"v_mfma_f32_16x16x32_bf16 v[100:103], a[204:207], a[60:63], v[100:103]\n" \
"v_cvt_pk_bf16_f32 v136, v88, v89\n" \
"v_cvt_pk_bf16_f32 v137, v90, v91\n" \
"v_mfma_f32_16x16x32_bf16 v[100:103], a[200:203], a[56:59], v[100:103]\n" \
"ds_write_b64 v128, v[136:137] offset:2368\n" \
"v_mfma_f32_16x16x32_bf16 v[100:103], a[172:175], a[52:55], v[100:103]\n" \
"v_cvt_pk_bf16_f32 v138, v92, v93\n" \
"v_cvt_pk_bf16_f32 v139, v94, v95\n" \
"v_mfma_f32_16x16x32_bf16 v[100:103], a[168:171], a[48:51], v[100:103]\n" \
"v_mfma_f32_16x16x32_bf16 v[100:103], a[140:143], a[252:255], v[100:103]\n" \
"v_mfma_f32_16x16x32_bf16 v[100:103], a[136:139], a[248:251], v[100:103]\n" \
"ds_write_b64 v128, v[138:139] offset:2400\n" \
"v_mfma_f32_16x16x32_bf16 v[104:107], a[144:147], a[248:251], v[104:107]\n" \
"ds_read_b128 v[148:151], v129 offset:2304\n" \
"ds_read_b128 v[152:155], v129 offset:3456\n" \
"v_mfma_f32_16x16x32_bf16 v[104:107], a[148:151], a[252:255], v[104:107]\n" \
"v_cvt_pk_bf16_f32 v136, v96, v97\n" \
"v_cvt_pk_bf16_f32 v137, v98, v99\n" \
"v_mfma_f32_16x16x32_bf16 v[104:107], a[176:179], a[48:51], v[104:107]\n" \
"ds_write_b64 v128, v[136:137] offset:4608\n" \
"v_mfma_f32_16x16x32_bf16 v[104:107], a[180:183], a[52:55], v[104:107]\n" \
"v_cvt_pk_bf16_f32 v138, v100, v101\n" \
"v_cvt_pk_bf16_f32 v139, v102, v103\n" \
"v_mfma_f32_16x16x32_bf16 v[104:107], a[208:211], a[56:59], v[104:107]\n" \
"s_add_u32 s70, s88, 4096\n" \
"s_waitcnt lgkmcnt(2)\n" \
"v_mfma_f32_16x16x32_bf16 v[104:107], a[212:215], a[60:63], v[104:107]\n" \
"buffer_store_dwordx4 v[148:151], v130, s[52:55], s70 offen sc1\n" \
"ds_write_b64 v128, v[138:139] offset:4640\n" \
"v_mfma_f32_16x16x32_bf16 v[108:111], a[220:223], a[60:63], v[108:111]\n" \
"s_add_u32 s70, s88, 69632\n" \
"s_waitcnt lgkmcnt(2)\n" \
"v_mfma_f32_16x16x32_bf16 v[108:111], a[216:219], a[56:59], v[108:111]\n" \
"buffer_store_dwordx4 v[152:155], v130, s[52:55], s70 offen sc1\n" \
"v_mfma_f32_16x16x32_bf16 v[108:111], a[188:191], a[52:55], v[108:111]\n" \
"v_mfma_f32_16x16x32_bf16 v[108:111], a[184:187], a[48:51], v[108:111]\n" \
"v_cvt_pk_bf16_f32 v136, v104, v105\n" \
"v_cvt_pk_bf16_f32 v137, v106, v107\n" \
"v_mfma_f32_16x16x32_bf16 v[108:111], a[156:159], a[252:255], v[108:111]\n" \
"v_mfma_f32_16x16x32_bf16 v[108:111], a[152:155], a[248:251], v[108:111]\n" \
"ds_write_b64 v128, v[136:137] offset:4672\n" \
"v_mfma_f32_16x16x32_bf16 v[112:115], a[128:131], a[224:227], v[112:115]\n" \
"v_mfma_f32_16x16x32_bf16 v[112:115], a[132:135], a[228:231], v[112:115]\n" \
"v_mfma_f32_16x16x32_bf16 v[112:115], a[160:163], a[232:235], v[112:115]\n" \
"v_mfma_f32_16x16x32_bf16 v[112:115], a[164:167], a[236:239], v[112:115]\n" \
"v_cvt_pk_bf16_f32 v138, v108, v109\n" \
"v_cvt_pk_bf16_f32 v139, v110, v111\n" \
"v_mfma_f32_16x16x32_bf16 v[112:115], a[192:195], a[240:243], v[112:115]\n" \
"v_mfma_f32_16x16x32_bf16 v[112:115], a[196:199], a[244:247], v[112:115]\n" \
"ds_write_b64 v128, v[138:139] offset:4704\n" \
"v_mfma_f32_16x16x32_bf16 v[116:119], a[204:207], a[244:247], v[116:119]\n" \
"ds_read_b128 v[156:159], v129 offset:4608\n" \
"ds_read_b128 v[160:163], v129 offset:5760\n" \
"v_mfma_f32_16x16x32_bf16 v[116:119], a[200:203], a[240:243], v[116:119]\n" \
"v_mfma_f32_16x16x32_bf16 v[116:119], a[172:175], a[236:239], v[116:119]\n" \
"v_mfma_f32_16x16x32_bf16 v[116:119], a[168:171], a[232:235], v[116:119]\n" \
"v_cvt_pk_bf16_f32 v136, v112, v113\n" \
"v_cvt_pk_bf16_f32 v137, v114, v115\n" \
"v_mfma_f32_16x16x32_bf16 v[116:119], a[140:143], a[228:231], v[116:119]\n" \
"s_add_u32 s70, s88, 131072\n" \
"s_waitcnt lgkmcnt(1)\n" \
"v_mfma_f32_16x16x32_bf16 v[116:119], a[136:139], a[224:227], v[116:119]\n" \
"buffer_store_dwordx4 v[156:159], v130, s[52:55], s70 offen sc1\n" \
"ds_write_b64 v128, v[136:137] offset:6912\n" \
"v_mfma_f32_16x16x32_bf16 v[120:123], a[144:147], a[224:227], v[120:123]\n" \
"s_add_u32 s70, s88, 196608\n" \
"s_waitcnt lgkmcnt(1)\n" \
"v_mfma_f32_16x16x32_bf16 v[120:123], a[148:151], a[228:231], v[120:123]\n" \
"buffer_store_dwordx4 v[160:163], v130, s[52:55], s70 offen sc1\n" \
"v_mfma_f32_16x16x32_bf16 v[120:123], a[176:179], a[232:235], v[120:123]\n" \
"v_mfma_f32_16x16x32_bf16 v[120:123], a[180:183], a[236:239], v[120:123]\n" \
"v_cvt_pk_bf16_f32 v138, v116, v117\n" \
"v_cvt_pk_bf16_f32 v139, v118, v119\n" \
"v_mfma_f32_16x16x32_bf16 v[120:123], a[208:211], a[240:243], v[120:123]\n" \
"v_mfma_f32_16x16x32_bf16 v[120:123], a[212:215], a[244:247], v[120:123]\n" \
"ds_write_b64 v128, v[138:139] offset:6944\n" \
"v_mfma_f32_16x16x32_bf16 v[124:127], a[220:223], a[244:247], v[124:127]\n" \
"v_mfma_f32_16x16x32_bf16 v[124:127], a[216:219], a[240:243], v[124:127]\n" \
"v_mfma_f32_16x16x32_bf16 v[124:127], a[188:191], a[236:239], v[124:127]\n" \
"v_mfma_f32_16x16x32_bf16 v[124:127], a[184:187], a[232:235], v[124:127]\n" \
"v_cvt_pk_bf16_f32 v136, v120, v121\n" \
"v_cvt_pk_bf16_f32 v137, v122, v123\n" \
"v_mfma_f32_16x16x32_bf16 v[124:127], a[156:159], a[228:231], v[124:127]\n" \
"v_mfma_f32_16x16x32_bf16 v[124:127], a[152:155], a[224:227], v[124:127]\n" \
"ds_write_b64 v128, v[136:137] offset:6976\n" \
"s_nop 7\n" \
"v_cvt_pk_bf16_f32 v138, v124, v125\n" \
"v_cvt_pk_bf16_f32 v139, v126, v127\n" \
"ds_write_b64 v128, v[138:139] offset:7008\n" \
"ds_read_b128 v[164:167], v129 offset:6912\n" \
"ds_read_b128 v[168:171], v129 offset:8064\n" \
"s_add_u32 s70, s88, 135168\n" \
"s_waitcnt lgkmcnt(1)\n" \
"buffer_store_dwordx4 v[164:167], v130, s[52:55], s70 offen sc1\n" \
"s_add_u32 s70, s88, 200704\n" \
"s_waitcnt lgkmcnt(0)\n" \
"buffer_store_dwordx4 v[168:171], v130, s[52:55], s70 offen sc1\n" \
"s_endpgm\n" \
"Lhot_end_%=:\n" \
"Lpl1_%=:\n" \
"s_mov_b32 s72, 0\n" \
"Lpk1_%=:\n" \
"s_sleep 2\n" \
"buffer_load_dword v227, v242, s[56:59], s90 offen sc1\n" \
"s_waitcnt vmcnt(0)\n" \
"v_cmp_eq_u32 vcc, s60, v227\n" \
"s_nop 1\n" \
"s_andn2_b32 s70, 0xff, vcc_lo\n" \
"s_cbranch_scc0 Lpr1_%=\n" \
"s_add_u32 s72, s72, 1\n" \
"s_cmp_lt_u32 s72, 0x40000\n" \
"s_cbranch_scc1 Lpk1_%=\n" \
"s_branch Lpr1_%=\n" \
"Lpl2_%=:\n" \
"s_mov_b32 s72, 0\n" \
"Lpk2_%=:\n" \
"s_sleep 2\n" \
"buffer_load_dword v239, v242, s[56:59], s95 offen sc1\n" \
"s_waitcnt vmcnt(0)\n" \
"v_cmp_eq_u32 vcc, s60, v239\n" \
"s_nop 1\n" \
"s_andn2_b32 s70, s69, vcc_lo\n" \
"s_cbranch_scc0 Lpr2_%=\n" \
"s_add_u32 s72, s72, 1\n" \
"s_cmp_lt_u32 s72, 0x40000\n" \
"s_cbranch_scc1 Lpk2_%=\n" \
"s_branch Lpr2_%=\n" \
"Lpl3_%=:\n" \
"s_mov_b32 s72, 0\n" \
"Lpk3_%=:\n" \
"s_sleep 2\n" \
"buffer_load_dword v239, v242, s[56:59], s95 offen sc1\n" \
"s_waitcnt vmcnt(0)\n" \
"v_cmp_eq_u32 vcc, s60, v239\n" \
"s_nop 1\n" \
"s_andn2_b32 s70, s69, vcc_lo\n" \
"s_cbranch_scc0 Lpr3_%=\n" \
"s_add_u32 s72, s72, 1\n" \
"s_cmp_lt_u32 s72, 0x40000\n" \
"s_cbranch_scc1 Lpk3_%=\n" \
"s_branch Lpr3_%=\n" \
"Lexact_%=:\n" \
"v_mov_b32 v239, 0xff800000\n" \
"s_and_b32 s70, s61, 1\n" \
"s_lshl_b32 s70, s70, 4\n" \
"v_add_u32 v186, s70, v238\n" \
"v_accvgpr_read_b32 v160, a128\n" \
"v_accvgpr_read_b32 v161, a129\n" \
"v_accvgpr_read_b32 v162, a130\n" \
"v_accvgpr_read_b32 v163, a131\n" \
"v_accvgpr_read_b32 v164, a132\n" \
"v_accvgpr_read_b32 v165, a133\n" \
"v_accvgpr_read_b32 v166, a134\n" \
"v_accvgpr_read_b32 v167, a135\n" \
"v_accvgpr_read_b32 v168, a136\n" \
"v_accvgpr_read_b32 v169, a137\n" \
"v_accvgpr_read_b32 v170, a138\n" \
"v_accvgpr_read_b32 v171, a139\n" \
"v_accvgpr_read_b32 v172, a140\n" \
"v_accvgpr_read_b32 v173, a141\n" \
"v_accvgpr_read_b32 v174, a142\n" \
"v_accvgpr_read_b32 v175, a143\n" \
"s_add_u32 s83, s85, 0\n" \
"s_add_u32 s84, s87, 0\n" \
"s_call_b64 s[64:65], Lex1_%=\n" \
"v_accvgpr_read_b32 v160, a144\n" \
"v_accvgpr_read_b32 v161, a145\n" \
"v_accvgpr_read_b32 v162, a146\n" \
"v_accvgpr_read_b32 v163, a147\n" \
"v_accvgpr_read_b32 v164, a148\n" \
"v_accvgpr_read_b32 v165, a149\n" \
"v_accvgpr_read_b32 v166, a150\n" \
"v_accvgpr_read_b32 v167, a151\n" \
"v_accvgpr_read_b32 v168, a152\n" \
"v_accvgpr_read_b32 v169, a153\n" \
"v_accvgpr_read_b32 v170, a154\n" \
"v_accvgpr_read_b32 v171, a155\n" \
"v_accvgpr_read_b32 v172, a156\n" \
"v_accvgpr_read_b32 v173, a157\n" \
"v_accvgpr_read_b32 v174, a158\n" \
"v_accvgpr_read_b32 v175, a159\n" \
"s_add_u32 s83, s85, 2\n" \
"s_add_u32 s84, s87, 532480\n" \
"s_call_b64 s[64:65], Lex1_%=\n" \
"v_accvgpr_read_b32 v160, a160\n" \
"v_accvgpr_read_b32 v161, a161\n" \
"v_accvgpr_read_b32 v162, a162\n" \
"v_accvgpr_read_b32 v163, a163\n" \
"v_accvgpr_read_b32 v164, a164\n" \
"v_accvgpr_read_b32 v165, a165\n" \
"v_accvgpr_read_b32 v166, a166\n" \
"v_accvgpr_read_b32 v167, a167\n" \
"v_accvgpr_read_b32 v168, a168\n" \
"v_accvgpr_read_b32 v169, a169\n" \
"v_accvgpr_read_b32 v170, a170\n" \
"v_accvgpr_read_b32 v171, a171\n" \
"v_accvgpr_read_b32 v172, a172\n" \
"v_accvgpr_read_b32 v173, a173\n" \
"v_accvgpr_read_b32 v174, a174\n" \
"v_accvgpr_read_b32 v175, a175\n" \
"s_add_u32 s83, s86, 0\n" \
"s_add_u32 s84, s88, 0\n" \
"s_call_b64 s[64:65], Lex1_%=\n" \
"v_accvgpr_read_b32 v160, a176\n" \
"v_accvgpr_read_b32 v161, a177\n" \
"v_accvgpr_read_b32 v162, a178\n" \
"v_accvgpr_read_b32 v163, a179\n" \
"v_accvgpr_read_b32 v164, a180\n" \
"v_accvgpr_read_b32 v165, a181\n" \
"v_accvgpr_read_b32 v166, a182\n" \
"v_accvgpr_read_b32 v167, a183\n" \
"v_accvgpr_read_b32 v168, a184\n" \
"v_accvgpr_read_b32 v169, a185\n" \
"v_accvgpr_read_b32 v170, a186\n" \
"v_accvgpr_read_b32 v171, a187\n" \
"v_accvgpr_read_b32 v172, a188\n" \
"v_accvgpr_read_b32 v173, a189\n" \
"v_accvgpr_read_b32 v174, a190\n" \
"v_accvgpr_read_b32 v175, a191\n" \
"s_add_u32 s83, s86, 2\n" \
"s_add_u32 s84, s88, 532480\n" \
"s_call_b64 s[64:65], Lex1_%=\n" \
"s_waitcnt vmcnt(0)\n" \
"s_branch Lexr_%=\n" \
"Lex1_%=:\n" \
"v_mov_b32 v128, 0\n" \
"v_mov_b32 v129, 0\n" \
"v_mov_b32 v130, 0\n" \
"v_mov_b32 v131, 0\n" \
"v_mov_b32 v132, 0\n" \
"v_mov_b32 v133, 0\n" \
"v_mov_b32 v134, 0\n" \
"v_mov_b32 v135, 0\n" \
"v_mov_b32 v136, 0\n" \
"v_mov_b32 v137, 0\n" \
"v_mov_b32 v138, 0\n" \
"v_mov_b32 v139, 0\n" \
"v_mov_b32 v140, 0\n" \
"v_mov_b32 v141, 0\n" \
"v_mov_b32 v142, 0\n" \
"v_mov_b32 v143, 0\n" \
"v_mov_b32 v144, 0\n" \
"v_mov_b32 v145, 0\n" \
"v_mov_b32 v146, 0\n" \
"v_mov_b32 v147, 0\n" \
"v_mov_b32 v148, 0\n" \
"v_mov_b32 v149, 0\n" \
"v_mov_b32 v150, 0\n" \
"v_mov_b32 v151, 0\n" \
"v_mov_b32 v152, 0\n" \
"v_mov_b32 v153, 0\n" \
"v_mov_b32 v154, 0\n" \
"v_mov_b32 v155, 0\n" \
"v_mov_b32 v156, 0\n" \
"v_mov_b32 v157, 0\n" \
"v_mov_b32 v158, 0\n" \
"v_mov_b32 v159, 0\n" \
"v_mov_b32 v184, 0xf149f2ca\n" \
"v_mov_b32 v185, 0\n" \
"s_mov_b32 s81, 0\n" \
"s_mul_i32 s82, s62, 557824\n" \
"Lex2_%=:\n" \
"s_add_u32 s70, s82, 4096\n" \
"buffer_load_dwordx4 a[64:67], v236, s[56:59], s82 offen offset:0 sc1\n" \
"buffer_load_dwordx4 a[68:71], v236, s[56:59], s82 offen offset:2048 sc1\n" \
"buffer_load_dwordx4 a[72:75], v236, s[56:59], s70 offen offset:0 sc1\n" \
"buffer_load_dwordx4 a[76:79], v236, s[56:59], s70 offen offset:2048 sc1\n" \
"buffer_load_dwordx4 a[80:83], v236, s[56:59], s82 offen offset:256 sc1\n" \
"buffer_load_dwordx4 a[84:87], v236, s[56:59], s82 offen offset:2304 sc1\n" \
"buffer_load_dwordx4 a[88:91], v236, s[56:59], s70 offen offset:256 sc1\n" \
"buffer_load_dwordx4 a[92:95], v236, s[56:59], s70 offen offset:2304 sc1\n" \
"s_add_u32 s70, s82, 17850368\n" \
"s_add_u32 s71, s70, 4096\n" \
"buffer_load_dwordx4 a[96:99], v234, s[56:59], s70 offen offset:0 sc1\n" \
"buffer_load_dwordx4 a[100:103], v234, s[56:59], s70 offen offset:1024 sc1\n" \
"buffer_load_dwordx4 a[104:107], v234, s[56:59], s70 offen offset:2048 sc1\n" \
"buffer_load_dwordx4 a[108:111], v234, s[56:59], s70 offen offset:3072 sc1\n" \
"buffer_load_dwordx4 a[112:115], v234, s[56:59], s71 offen offset:0 sc1\n" \
"buffer_load_dwordx4 a[116:119], v234, s[56:59], s71 offen offset:1024 sc1\n" \
"buffer_load_dwordx4 a[120:123], v234, s[56:59], s71 offen offset:2048 sc1\n" \
"buffer_load_dwordx4 a[124:127], v234, s[56:59], s71 offen offset:3072 sc1\n" \
"s_waitcnt vmcnt(0)\n" \
"v_mfma_f32_16x16x32_bf16 v[176:179], a[64:67], v[160:163], 0\n" \
"v_mfma_f32_16x16x32_bf16 v[176:179], a[68:71], v[164:167], v[176:179]\n" \
"v_mfma_f32_16x16x32_bf16 v[176:179], a[72:75], v[168:171], v[176:179]\n" \
"v_mfma_f32_16x16x32_bf16 v[176:179], a[76:79], v[172:175], v[176:179]\n" \
"v_mfma_f32_16x16x32_bf16 v[180:183], a[80:83], v[160:163], 0\n" \
"v_mfma_f32_16x16x32_bf16 v[180:183], a[84:87], v[164:167], v[180:183]\n" \
"v_mfma_f32_16x16x32_bf16 v[180:183], a[88:91], v[168:171], v[180:183]\n" \
"v_mfma_f32_16x16x32_bf16 v[180:183], a[92:95], v[172:175], v[180:183]\n" \
"s_nop 7\n" \
"s_nop 1\n" \
"s_cmp_eq_u32 s81, s83\n" \
"s_cbranch_scc0 Lex3_%=\n" \
"v_cmp_gt_i32 vcc, 0, v186\n" \
"s_nop 1\n" \
"v_cndmask_b32 v176, v176, v239, vcc\n" \
"v_cmp_gt_i32 vcc, 1, v186\n" \
"s_nop 1\n" \
"v_cndmask_b32 v177, v177, v239, vcc\n" \
"v_cmp_gt_i32 vcc, 2, v186\n" \
"s_nop 1\n" \
"v_cndmask_b32 v178, v178, v239, vcc\n" \
"v_cmp_gt_i32 vcc, 3, v186\n" \
"s_nop 1\n" \
"v_cndmask_b32 v179, v179, v239, vcc\n" \
"v_cmp_gt_i32 vcc, 16, v186\n" \
"s_nop 1\n" \
"v_cndmask_b32 v180, v180, v239, vcc\n" \
"v_cmp_gt_i32 vcc, 17, v186\n" \
"s_nop 1\n" \
"v_cndmask_b32 v181, v181, v239, vcc\n" \
"v_cmp_gt_i32 vcc, 18, v186\n" \
"s_nop 1\n" \
"v_cndmask_b32 v182, v182, v239, vcc\n" \
"v_cmp_gt_i32 vcc, 19, v186\n" \
"s_nop 1\n" \
"v_cndmask_b32 v183, v183, v239, vcc\n" \
"Lex3_%=:\n" \
"v_max3_f32 v187, v176, v177, v178\n" \
"v_max3_f32 v187, v187, v179, v180\n" \
"v_max3_f32 v187, v187, v181, v182\n" \
"v_max3_f32 v187, v187, v183, v184\n" \
"v_mov_b32 v188, v187\n" \
"s_nop 1\n" \
"v_permlane16_swap_b32 v188, v187\n" \
"v_max_f32 v187, v187, v188\n" \
"v_mov_b32 v188, v187\n" \
"s_nop 1\n" \
"v_permlane32_swap_b32 v188, v187\n" \
"v_max_f32 v187, v187, v188\n" \
"v_sub_f32 v189, v184, v187\n" \
"v_exp_f32 v189, v189\n" \
"v_mov_b32 v184, v187\n" \
"v_sub_f32 v176, v176, v187\n" \
"v_sub_f32 v177, v177, v187\n" \
"v_sub_f32 v178, v178, v187\n" \
"v_sub_f32 v179, v179, v187\n" \
"v_sub_f32 v180, v180, v187\n" \
"v_sub_f32 v181, v181, v187\n" \
"v_sub_f32 v182, v182, v187\n" \
"v_sub_f32 v183, v183, v187\n" \
"v_exp_f32 v176, v176\n" \
"v_exp_f32 v177, v177\n" \
"v_exp_f32 v178, v178\n" \
"v_exp_f32 v179, v179\n" \
"v_exp_f32 v180, v180\n" \
"v_exp_f32 v181, v181\n" \
"v_exp_f32 v182, v182\n" \
"v_exp_f32 v183, v183\n" \
"v_mul_f32 v185, v185, v189\n" \
"v_add_f32 v185, v185, v176\n" \
"v_add_f32 v185, v185, v177\n" \
"v_add_f32 v185, v185, v178\n" \
"v_add_f32 v185, v185, v179\n" \
"v_add_f32 v185, v185, v180\n" \
"v_add_f32 v185, v185, v181\n" \
"v_add_f32 v185, v185, v182\n" \
"v_add_f32 v185, v185, v183\n" \
"v_cvt_pk_bf16_f32 v112, v176, v177\n" \
"v_cvt_pk_bf16_f32 v113, v178, v179\n" \
"v_cvt_pk_bf16_f32 v114, v180, v181\n" \
"v_cvt_pk_bf16_f32 v115, v182, v183\n" \
"v_mul_f32 v128, v128, v189\n" \
"v_mul_f32 v129, v129, v189\n" \
"v_mul_f32 v130, v130, v189\n" \
"v_mul_f32 v131, v131, v189\n" \
"v_mul_f32 v132, v132, v189\n" \
"v_mul_f32 v133, v133, v189\n" \
"v_mul_f32 v134, v134, v189\n" \
"v_mul_f32 v135, v135, v189\n" \
"v_mul_f32 v136, v136, v189\n" \
"v_mul_f32 v137, v137, v189\n" \
"v_mul_f32 v138, v138, v189\n" \
"v_mul_f32 v139, v139, v189\n" \
"v_mul_f32 v140, v140, v189\n" \
"v_mul_f32 v141, v141, v189\n" \
"v_mul_f32 v142, v142, v189\n" \
"v_mul_f32 v143, v143, v189\n" \
"v_mul_f32 v144, v144, v189\n" \
"v_mul_f32 v145, v145, v189\n" \
"v_mul_f32 v146, v146, v189\n" \
"v_mul_f32 v147, v147, v189\n" \
"v_mul_f32 v148, v148, v189\n" \
"v_mul_f32 v149, v149, v189\n" \
"v_mul_f32 v150, v150, v189\n" \
"v_mul_f32 v151, v151, v189\n" \
"v_mul_f32 v152, v152, v189\n" \
"v_mul_f32 v153, v153, v189\n" \
"v_mul_f32 v154, v154, v189\n" \
"v_mul_f32 v155, v155, v189\n" \
"v_mul_f32 v156, v156, v189\n" \
"v_mul_f32 v157, v157, v189\n" \
"v_mul_f32 v158, v158, v189\n" \
"v_mul_f32 v159, v159, v189\n" \
"s_nop 1\n" \
"v_mfma_f32_16x16x32_bf16 v[128:131], a[96:99], v[112:115], v[128:131]\n" \
"v_mfma_f32_16x16x32_bf16 v[132:135], a[100:103], v[112:115], v[132:135]\n" \
"v_mfma_f32_16x16x32_bf16 v[136:139], a[104:107], v[112:115], v[136:139]\n" \
"v_mfma_f32_16x16x32_bf16 v[140:143], a[108:111], v[112:115], v[140:143]\n" \
"v_mfma_f32_16x16x32_bf16 v[144:147], a[112:115], v[112:115], v[144:147]\n" \
"v_mfma_f32_16x16x32_bf16 v[148:151], a[116:119], v[112:115], v[148:151]\n" \
"v_mfma_f32_16x16x32_bf16 v[152:155], a[120:123], v[112:115], v[152:155]\n" \
"v_mfma_f32_16x16x32_bf16 v[156:159], a[124:127], v[112:115], v[156:159]\n" \
"s_nop 7\n" \
"s_nop 1\n" \
"s_and_b32 s70, s81, 1\n" \
"s_mul_i32 s70, s70, 1024\n" \
"s_add_u32 s82, s82, s70\n" \
"s_add_u32 s82, s82, 8192\n" \
"s_add_u32 s81, s81, 1\n" \
"s_cmp_le_u32 s81, s83\n" \
"s_cbranch_scc1 Lex2_%=\n" \
"v_mov_b32 v187, v185\n" \
"s_nop 1\n" \
"v_permlane16_swap_b32 v187, v185\n" \
"v_add_f32 v185, v185, v187\n" \
"v_mov_b32 v187, v185\n" \
"s_nop 1\n" \
"v_permlane32_swap_b32 v187, v185\n" \
"v_add_f32 v185, v185, v187\n" \
"v_rcp_f32 v185, v185\n" \
"s_nop 0\n" \
"v_mul_f32 v128, v128, v185\n" \
"v_mul_f32 v129, v129, v185\n" \
"v_mul_f32 v130, v130, v185\n" \
"v_mul_f32 v131, v131, v185\n" \
"v_mul_f32 v132, v132, v185\n" \
"v_mul_f32 v133, v133, v185\n" \
"v_mul_f32 v134, v134, v185\n" \
"v_mul_f32 v135, v135, v185\n" \
"v_mul_f32 v136, v136, v185\n" \
"v_mul_f32 v137, v137, v185\n" \
"v_mul_f32 v138, v138, v185\n" \
"v_mul_f32 v139, v139, v185\n" \
"v_mul_f32 v140, v140, v185\n" \
"v_mul_f32 v141, v141, v185\n" \
"v_mul_f32 v142, v142, v185\n" \
"v_mul_f32 v143, v143, v185\n" \
"v_mul_f32 v144, v144, v185\n" \
"v_mul_f32 v145, v145, v185\n" \
"v_mul_f32 v146, v146, v185\n" \
"v_mul_f32 v147, v147, v185\n" \
"v_mul_f32 v148, v148, v185\n" \
"v_mul_f32 v149, v149, v185\n" \
"v_mul_f32 v150, v150, v185\n" \
"v_mul_f32 v151, v151, v185\n" \
"v_mul_f32 v152, v152, v185\n" \
"v_mul_f32 v153, v153, v185\n" \
"v_mul_f32 v154, v154, v185\n" \
"v_mul_f32 v155, v155, v185\n" \
"v_mul_f32 v156, v156, v185\n" \
"v_mul_f32 v157, v157, v185\n" \
"v_mul_f32 v158, v158, v185\n" \
"v_mul_f32 v159, v159, v185\n" \
"s_add_u32 s70, s84, 4096\n" \
"v_cvt_pk_bf16_f32 v112, v128, v129\n" \
"v_cvt_pk_bf16_f32 v113, v130, v131\n" \
"v_cvt_pk_bf16_f32 v114, v132, v133\n" \
"v_cvt_pk_bf16_f32 v115, v134, v135\n" \
"buffer_store_dwordx4 v[112:115], v236, s[56:59], s84 offen offset:0 sc1\n" \
"v_cvt_pk_bf16_f32 v116, v136, v137\n" \
"v_cvt_pk_bf16_f32 v117, v138, v139\n" \
"v_cvt_pk_bf16_f32 v118, v140, v141\n" \
"v_cvt_pk_bf16_f32 v119, v142, v143\n" \
"buffer_store_dwordx4 v[116:119], v236, s[56:59], s84 offen offset:2048 sc1\n" \
"v_cvt_pk_bf16_f32 v120, v144, v145\n" \
"v_cvt_pk_bf16_f32 v121, v146, v147\n" \
"v_cvt_pk_bf16_f32 v122, v148, v149\n" \
"v_cvt_pk_bf16_f32 v123, v150, v151\n" \
"buffer_store_dwordx4 v[120:123], v236, s[56:59], s70 offen offset:0 sc1\n" \
"v_cvt_pk_bf16_f32 v124, v152, v153\n" \
"v_cvt_pk_bf16_f32 v125, v154, v155\n" \
"v_cvt_pk_bf16_f32 v126, v156, v157\n" \
"v_cvt_pk_bf16_f32 v127, v158, v159\n" \
"buffer_store_dwordx4 v[124:127], v236, s[56:59], s70 offen offset:2048 sc1\n" \
"s_waitcnt vmcnt(0)\n" \
"s_setpc_b64 s[64:65]\n" \
"Lpl4_%=:\n" \
"s_mov_b32 s72, 0\n" \
"Lpk4_%=:\n" \
"s_sleep 2\n" \
"buffer_load_dwordx2 v[226:227], v228, s[56:59], s13 offen sc1\n" \
"s_waitcnt vmcnt(0)\n" \
"v_cmp_eq_u32 vcc, s60, v226\n" \
"s_nop 1\n" \
"s_andn2_b32 s70, 0xff, vcc_lo\n" \
"s_cbranch_scc0 Lpr4_%=\n" \
"s_add_u32 s72, s72, 1\n" \
"s_cmp_lt_u32 s72, 0x40000\n" \
"s_cbranch_scc1 Lpk4_%=\n" \
"s_branch Lpr4_%=\n" \
"Lpl5_%=:\n" \
"s_mov_b32 s72, 0\n" \
"Lpk5_%=:\n" \
"s_sleep 2\n" \
"buffer_load_dword v220, v221, s[56:59], s66 offen sc1\n" \
"s_waitcnt vmcnt(0)\n" \
"v_cmp_eq_u32 vcc, s60, v220\n" \
"s_nop 1\n" \
"s_andn2_b32 s70, s65, vcc_lo\n" \
"s_cbranch_scc0 Lpr5_%=\n" \
"s_add_u32 s72, s72, 1\n" \
"s_cmp_lt_u32 s72, 0x40000\n" \
"s_cbranch_scc1 Lpk5_%=\n" \
"s_branch Lpr5_%=\n" \
"Lpl6_%=:\n" \
"s_mov_b32 s72, 0\n" \
"Lpk6_%=:\n" \
"s_sleep 2\n" \
"buffer_load_dword v220, v221, s[56:59], s67 offen sc1\n" \
"s_waitcnt vmcnt(0)\n" \
"v_cmp_eq_u32 vcc, s60, v220\n" \
"s_nop 1\n" \
"s_andn2_b32 s70, s74, vcc_lo\n" \
"s_cbranch_scc0 Lpr6_%=\n" \
"s_add_u32 s72, s72, 1\n" \
"s_cmp_lt_u32 s72, 0x40000\n" \
"s_cbranch_scc1 Lpk6_%=\n" \
"s_branch Lpr6_%=\n" \
"Lpl7_%=:\n" \
"s_mov_b32 s72, 0\n" \
"Lpk7_%=:\n" \
"s_sleep 2\n" \
"buffer_load_dword v220, v241, s[56:59], s64 offen sc1\n" \
"s_waitcnt vmcnt(0)\n" \
"v_cmp_eq_u32 vcc, s60, v220\n" \
"s_nop 1\n" \
"s_andn2_b32 s70, s69, vcc_lo\n" \
"s_cbranch_scc0 Lpr7_%=\n" \
"s_add_u32 s72, s72, 1\n" \
"s_cmp_lt_u32 s72, 0x40000\n" \
"s_cbranch_scc1 Lpk7_%=\n" \
"s_branch Lpr7_%=\n" \
"Lcode_end_%=:\n" \
".p2align 12\n" \
""
#define MK_ASM_CLOBBERS "v1", "v2", "v3", "v4", "v5", "v6", "v7", "v8", "v9", "v10", "v11", "v12", "v13", "v14", "v15", "v16", "v17", "v18", "v19", "v20", "v21", "v22", "v23", "v24", "v25", "v26", "v27", "v28", "v29", "v30", "v31", "v32", "v33", "v34", "v35", "v36", "v37", "v38", "v39", "v40", "v41", "v42", "v43", "v44", "v45", "v46", "v47", "v48", "v49", "v50", "v51", "v52", "v53", "v54", "v55", "v56", "v57", "v58", "v59", "v60", "v61", "v62", "v63", "v64", "v65", "v66", "v67", "v68", "v69", "v70", "v71", "v72", "v73", "v74", "v75", "v76", "v77", "v78", "v79", "v80", "v81", "v82", "v83", "v84", "v85", "v86", "v87", "v88", "v89", "v90", "v91", "v92", "v93", "v94", "v95", "v96", "v97", "v98", "v99", "v100", "v101", "v102", "v103", "v104", "v105", "v106", "v107", "v108", "v109", "v110", "v111", "v112", "v113", "v114", "v115", "v116", "v117", "v118", "v119", "v120", "v121", "v122", "v123", "v124", "v125", "v126", "v127", "v128", "v129", "v130", "v131", "v132", "v133", "v134", "v135", "v136", "v137", "v138", "v139", "v140", "v141", "v142", "v143", "v144", "v145", "v146", "v147", "v148", "v149", "v150", "v151", "v152", "v153", "v154", "v155", "v156", "v157", "v158", "v159", "v160", "v161", "v162", "v163", "v164", "v165", "v166", "v167", "v168", "v169", "v170", "v171", "v172", "v173", "v174", "v175", "v176", "v177", "v178", "v179", "v180", "v181", "v182", "v183", "v184", "v185", "v186", "v187", "v188", "v189", "v190", "v191", "v192", "v193", "v194", "v195", "v196", "v197", "v198", "v199", "v200", "v201", "v202", "v203", "v204", "v205", "v206", "v207", "v208", "v209", "v210", "v211", "v212", "v213", "v214", "v215", "v216", "v217", "v218", "v219", "v220", "v221", "v222", "v223", "v224", "v225", "v226", "v227", "v228", "v229", "v230", "v231", "v232", "v233", "v234", "v235", "v236", "v237", "v238", "v239", "v240", "v241", "v242", "v243", "v244", "v245", "v246", "v247", "v248", "v249", "v250", "v251", "v252", "v253", "v254", "v255", "a0", "a1", "a2", "a3", "a4", "a5", "a6", "a7", "a8", "a9", "a10", "a11", "a12", "a13", "a14", "a15", "a16", "a17", "a18", "a19", "a20", "a21", "a22", "a23", "a24", "a25", "a26", "a27", "a28", "a29", "a30", "a31", "a32", "a33", "a34", "a35", "a36", "a37", "a38", "a39", "a40", "a41", "a42", "a43", "a44", "a45", "a46", "a47", "a48", "a49", "a50", "a51", "a52", "a53", "a54", "a55", "a56", "a57", "a58", "a59", "a60", "a61", "a62", "a63", "a64", "a65", "a66", "a67", "a68", "a69", "a70", "a71", "a72", "a73", "a74", "a75", "a76", "a77", "a78", "a79", "a80", "a81", "a82", "a83", "a84", "a85", "a86", "a87", "a88", "a89", "a90", "a91", "a92", "a93", "a94", "a95", "a96", "a97", "a98", "a99", "a100", "a101", "a102", "a103", "a104", "a105", "a106", "a107", "a108", "a109", "a110", "a111", "a112", "a113", "a114", "a115", "a116", "a117", "a118", "a119", "a120", "a121", "a122", "a123", "a124", "a125", "a126", "a127", "a128", "a129", "a130", "a131", "a132", "a133", "a134", "a135", "a136", "a137", "a138", "a139", "a140", "a141", "a142", "a143", "a144", "a145", "a146", "a147", "a148", "a149", "a150", "a151", "a152", "a153", "a154", "a155", "a156", "a157", "a158", "a159", "a160", "a161", "a162", "a163", "a164", "a165", "a166", "a167", "a168", "a169", "a170", "a171", "a172", "a173", "a174", "a175", "a176", "a177", "a178", "a179", "a180", "a181", "a182", "a183", "a184", "a185", "a186", "a187", "a188", "a189", "a190", "a191", "a192", "a193", "a194", "a195", "a196", "a197", "a198", "a199", "a200", "a201", "a202", "a203", "a204", "a205", "a206", "a207", "a208", "a209", "a210", "a211", "a212", "a213", "a214", "a215", "a216", "a217", "a218", "a219", "a220", "a221", "a222", "a223", "a224", "a225", "a226", "a227", "a228", "a229", "a230", "a231", "a232", "a233", "a234", "a235", "a236", "a237", "a238", "a239", "a240", "a241", "a242", "a243", "a244", "a245", "a246", "a247", "a248", "a249", "a250", "a251", "a252", "a253", "a254", "a255", "s8", "s9", "s10", "s11", "s12", "s13", "s14", "s15", "s16", "s17", "s18", "s19", "s20", "s21", "s22", "s23", "s24", "s25", "s26", "s27", "s28", "s29", "s30", "s31", "s36", "s37", "s38", "s39", "s40", "s41", "s42", "s43", "s44", "s45", "s46", "s47", "s48", "s49", "s50", "s51", "s52", "s53", "s54", "s55", "s56", "s57", "s58", "s59", "s60", "s61", "s62", "s63", "s64", "s65", "s66", "s67", "s68", "s69", "s70", "s71", "s72", "s73", "s74", "s75", "s76", "s77", "s78", "s79", "s80", "s81", "s82", "s83", "s84", "s85", "s86", "s87", "s88", "s89", "s90", "s91", "s92", "s93", "s94", "s95", "vcc", "scc", "memory"
namespace mk {
constexpr int LDS_BYTES = 163840;
struct Args { const void* in[9]; void* out; void* ws; };
__global__ __launch_bounds__(256, 1) void mega_k(const Args a) {
    extern __shared__ __attribute__((aligned(16))) unsigned char shm[];
    const unsigned ldsb = (unsigned)(size_t)(LAS unsigned char*)shm;
    asm volatile(MK_ASM_BODY
                 :
                 : "s"(__builtin_amdgcn_kernarg_segment_ptr()), "s"(blockIdx.x), "v"(threadIdx.x), "s"(ldsb)
                 : MK_ASM_CLOBBERS);
}
}
extern "C" void kernel_launch(void* const* d_in, const int* in_sizes, int n_in, void* d_out, int out_size, void* d_ws, size_t ws_size, hipStream_t stream) {
    (void)hipFuncSetAttribute((const void*)mk::mega_k, hipFuncAttributeMaxDynamicSharedMemorySize, mk::LDS_BYTES);
    mk::Args a;
    for (int i = 0; i < 9; ++i) a.in[i] = d_in[i];
    a.out = d_out; a.ws = (char*)d_ws + ((ws_size - WS_END) & ~(size_t)0x1fffff);
    mk::mega_k<<<256, 256, mk::LDS_BYTES, stream>>>(a);
}
```

```cpp
#include <hip/hip_runtime.h>
#include <stdint.h>
#define LAS __attribute__((address_space(3)))
constexpr size_t LAY_TS = 17408, LAY_HS = 557824, LAY_CRS = 266240, LAY_ORS = 69632, WS_K = 0, WS_V = 17850368, WS_CTX = 35700736, WS_OB = 52740096, WS_FL = 65585152, WS_END = 65634304, LAY_VS = 2;
#define MK_ASM_BODY \
"s_load_dwordx16 s[8:23], %0, 0x0\n" \
"s_load_dwordx4 s[24:27], %0, 0x40\n" \
"s_load_dwordx2 s[28:29], %0, 0x50\n" \
"v_readfirstlane_b32 s61, %2\n" \
"s_lshr_b32 s61, s61, 6\n" \
"v_mbcnt_lo_u32_b32 v78, -1, 0\n" \
"v_mbcnt_hi_u32_b32 v78, -1, v78\n" \
"v_lshlrev_b32 v234, 4, v78\n" \
"s_getpc_b64 s[64:65]\n" \
"Lpc_%=:\n" \
"s_lshr_b32 s70, %1, 3\n" \
"s_lshl_b32 s70, s70, 2\n" \
"s_add_u32 s70, s70, s61\n" \
"s_and_b32 s71, s70, 31\n" \
"s_lshl_b32 s71, s71, 12\n" \
"v_lshl_add_u32 v237, v234, 2, s71\n" \
"s_mov_b32 s71, Lcode_end_%=-Lpc_%=-4\n" \
"v_min_u32 v237, s71, v237\n" \
"global_load_dword v238, v237, s[64:65]\n" \
"s_and_b32 s62, %1, 7\n" \
"s_lshl_b32 s62, s62, 2\n" \
"s_lshr_b32 s70, %1, 6\n" \
"s_add_u32 s62, s62, s70\n" \
"s_bfe_u32 s63, %1, 0x30003\n" \
"s_lshl_b32 s74, s61, 13\n" \
"s_add_u32 s74, s74, %3\n" \
"v_bfe_u32 v79, v78, 4, 1\n" \
"v_bfe_u32 v80, v78, 2, 2\n" \
"v_lshl_add_u32 v80, v79, 3, v80\n" \
"s_lshl_b32 s71, s61, 4\n" \
"v_add_u32 v80, s71, v80\n" \
"v_lshlrev_b32 v80, 13, v80\n" \
"v_lshlrev_b32 v79, 1, v79\n" \
"v_and_b32 v81, 3, v78\n" \
"v_xor_b32 v79, v79, v81\n" \
"v_lshl_add_u32 v80, v79, 4, v80\n" \
"v_lshrrev_b32 v90, 5, v78\n" \
"v_lshl_add_u32 v84, v90, 6, v80\n" \
"v_add_u32 v68, 4096, v84\n" \
"v_add_u32 v69, 3072, v84\n" \
"v_add_u32 v70, 2176, v84\n" \
"v_add_u32 v71, 1152, v84\n" \
"s_lshl_b32 s83, s62, 8\n" \
"s_add_u32 s84, s83, 32768\n" \
"v_lshrrev_b32 v85, 3, v78\n" \
"v_and_b32 v86, 7, v78\n" \
"v_xor_b32 v86, v86, v85\n" \
"v_lshlrev_b32 v86, 4, v86\n" \
"v_lshl_add_u32 v64, v85, 10, v86\n" \
"v_add_u32 v65, 7168, v64\n" \
"v_add_u32 v66, 14336, v64\n" \
"v_add_u32 v67, 21504, v64\n" \
"s_bitcmp1_b32 s61, 0\n" \
"s_cselect_b32 s70, 0, 15\n" \
"s_xor_b32 s81, s63, s70\n" \
"s_lshl_b32 s81, s81, 17\n" \
"s_lshr_b32 s70, s61, 1\n" \
"s_lshl_b32 s71, s70, 16\n" \
"s_add_u32 s81, s81, s71\n" \
"s_add_u32 s82, s81, 32768\n" \
"s_mov_b32 s42, 4198400\n" \
"s_mov_b32 s43, 0x20000\n" \
"s_mov_b32 s46, 4198400\n" \
"s_mov_b32 s47, 0x20000\n" \
"s_mov_b32 s38, 2097152\n" \
"s_mov_b32 s39, 0x20000\n" \
"s_waitcnt lgkmcnt(0)\n" \
"s_mov_b64 s[40:41], s[14:15]\n" \
"s_sub_u32 s40, s40, 4096\n" \
"s_subb_u32 s41, s41, 0\n" \
"s_and_b32 s41, s41, 0xffff\n" \
"s_mov_b64 s[44:45], s[16:17]\n" \
"s_sub_u32 s44, s44, 4096\n" \
"s_subb_u32 s45, s45, 0\n" \
"s_and_b32 s45, s45, 0xffff\n" \
"s_mov_b64 s[36:37], s[10:11]\n" \
"s_and_b32 s37, s37, 0xffff\n" \
"s_add_u32 m0, s74, 32768\n" \
"s_nop 0\n" \
"buffer_load_dwordx4 v68, s[40:43], s83 offen offset:0 lds\n" \
"buffer_load_dwordx4 v69, s[44:47], s83 offen offset:1024 lds\n" \
"buffer_load_dwordx4 v70, s[40:43], s83 offen offset:2048 lds\n" \
"buffer_load_dwordx4 v71, s[44:47], s83 offen offset:3072 lds\n" \
"s_xor_b32 s83, s83, 0x80000\n" \
"s_add_u32 m0, s74, 36864\n" \
"s_nop 0\n" \
"buffer_load_dwordx4 v68, s[40:43], s84 offen offset:0 lds\n" \
"buffer_load_dwordx4 v69, s[44:47], s84 offen offset:1024 lds\n" \
"buffer_load_dwordx4 v70, s[40:43], s84 offen offset:2048 lds\n" \
"buffer_load_dwordx4 v71, s[44:47], s84 offen offset:3072 lds\n" \
"s_xor_b32 s84, s84, 0x80000\n" \
"s_add_u32 m0, s74, 0\n" \
"s_nop 0\n" \
"buffer_load_dwordx4 v64, s[36:39], s81 offen offset:0 lds\n" \
"buffer_load_dwordx4 v65, s[36:39], s81 offen offset:1024 lds\n" \
"buffer_load_dwordx4 v66, s[36:39], s81 offen offset:2048 lds\n" \
"buffer_load_dwordx4 v67, s[36:39], s81 offen offset:3072 lds\n" \
"s_xor_b32 s81, s81, 0x80\n" \
"s_add_u32 m0, s74, 4096\n" \
"s_nop 0\n" \
"buffer_load_dwordx4 v64, s[36:39], s82 offen offset:0 lds\n" \
"buffer_load_dwordx4 v65, s[36:39], s82 offen offset:1024 lds\n" \
"buffer_load_dwordx4 v66, s[36:39], s82 offen offset:2048 lds\n" \
"buffer_load_dwordx4 v67, s[36:39], s82 offen offset:3072 lds\n" \
"s_xor_b32 s82, s82, 0x80\n" \
"v_lshrrev_b32 v87, 4, v78\n" \
"v_and_b32 v79, 15, v78\n" \
"v_and_b32 v80, 7, v78\n" \
"v_xor_b32 v80, v80, v87\n" \
"v_lshlrev_b32 v79, 7, v79\n" \
"s_lshl_b32 s70, s70, 14\n" \
"s_add_u32 s70, s70, %3\n" \
"v_lshl_add_u32 v88, v80, 4, v79\n" \
"v_add_u32 v72, s70, v88\n" \
"v_xor_b32 v73, 64, v72\n" \
"v_lshlrev_b32 v79, 3, v81\n" \
"v_bfe_u32 v80, v78, 2, 2\n" \
"v_lshl_add_u32 v79, v80, 6, v79\n" \
"v_bfe_u32 v80, v78, 4, 1\n" \
"v_lshl_add_u32 v79, v80, 8, v79\n" \
"v_lshl_add_u32 v89, v80, 5, v79\n" \
"s_and_b32 s70, s61, 1\n" \
"s_lshl_b32 s70, s70, 11\n" \
"s_add_u32 s70, s70, %3\n" \
"v_lshl_add_u32 v82, v90, 13, v89\n" \
"v_add_u32 v74, s70, v82\n" \
"v_xor_b32 v75, 32, v74\n" \
"s_mov_b64 s[56:57], s[28:29]\n" \
"s_and_b32 s57, s57, 0xffff\n" \
"s_mov_b32 s58, 65634304\n" \
"s_mov_b32 s59, 0x20000\n" \
"v_mov_b32 v241, 0\n" \
"s_lshl_b32 s66, %1, 2\n" \
"s_add_u32 s66, s66, s61\n" \
"s_lshl_b32 s66, s66, 10\n" \
"s_lshr_b32 s67, %1, 3\n" \
"s_lshl_b32 s67, s67, 2\n" \
"s_add_u32 s67, s67, s61\n" \
"s_lshl_b32 s67, s67, 13\n" \
"s_and_b32 s64, %1, 7\n" \
"s_lshl_b32 s64, s64, 10\n" \
"s_add_u32 s67, s67, s64\n" \
"s_mov_b64 exec, 0\n" \
"s_cmp_eq_u32 s61, 0\n" \
"s_cbranch_scc1 Lst0x0_%=\n" \
"s_cmp_eq_u32 s61, 1\n" \
"s_cbranch_scc1 Lst0x1_%=\n" \
"s_cmp_eq_u32 s61, 2\n" \
"s_cbranch_scc1 Lst0x2_%=\n" \
"s_branch Lst0x3_%=\n" \
"Lcb0_%=:\n" \
"s_mov_b64 exec, -1\n" \
"s_add_u32 m0, s74, 131072\n" \
"s_nop 0\n" \
"buffer_load_dwordx4 v68, s[40:43], s83 offen offset:0 lds\n" \
"buffer_load_dwordx4 v69, s[44:47], s83 offen offset:1024 lds\n" \
"buffer_load_dwordx4 v70, s[40:43], s83 offen offset:2048 lds\n" \
"buffer_load_dwordx4 v71, s[44:47], s83 offen offset:3072 lds\n" \
"s_xor_b32 s83, s83, 0x180000\n" \
"s_add_u32 m0, s74, 135168\n" \
"s_nop 0\n" \
"buffer_load_dwordx4 v68, s[40:43], s84 offen offset:0 lds\n" \
"buffer_load_dwordx4 v69, s[44:47], s84 offen offset:1024 lds\n" \
"buffer_load_dwordx4 v70, s[40:43], s84 offen offset:2048 lds\n" \
"buffer_load_dwordx4 v71, s[44:47], s84 offen offset:3072 lds\n" \
"s_xor_b32 s84, s84, 0x180000\n" \
"s_mov_b32 s93, 65593344\n" \
"buffer_load_dword v240, v241, s[56:59], s93 offen sc1\n" \
"s_mov_b64 s[64:65], s[10:11]\n" \
"s_add_u32 s68, s66, 0x0\n" \
"s_add_u32 s64, s64, s68\n" \
"s_addc_u32 s65, s65, 0\n" \
"global_load_dword v238, v234, s[64:65]\n" \
"s_mov_b64 s[64:65], s[10:11]\n" \
"s_add_u32 s68, s66, 0x100000\n" \
"s_add_u32 s64, s64, s68\n" \
"s_addc_u32 s65, s65, 0\n" \
"global_load_dword v238, v234, s[64:65]\n" \
"s_mov_b64 s[64:65], s[14:15]\n" \
"s_add_u32 s68, s67, 0x100000\n" \
"s_add_u32 s64, s64, s68\n" \
"s_addc_u32 s65, s65, 0\n" \
"global_load_dword v238, v234, s[64:65]\n" \
"s_mov_b64 s[64:65], s[16:17]\n" \
"s_add_u32 s68, s67, 0x100000\n" \
"s_add_u32 s64, s64, s68\n" \
"s_addc_u32 s65, s65, 0\n" \
"global_load_dword v238, v234, s[64:65]\n" \
"s_waitcnt vmcnt(13)\n" \
"s_barrier\n" \
"ds_read_b64_tr_b16 a[64:65], v74 offset:32768\n" \
"ds_read_b64_tr_b16 a[66:67], v74 offset:36864\n" \
"ds_read_b64_tr_b16 a[68:69], v74 offset:49152\n" \
"ds_read_b64_tr_b16 a[70:71], v74 offset:53248\n" \
"ds_read_b128 a[0:3], v72 offset:0\n" \
"ds_read_b128 a[4:7], v73 offset:0\n" \
"ds_read_b64_tr_b16 a[72:73], v75 offset:32768\n" \
"ds_read_b64_tr_b16 a[74:75], v75 offset:36864\n" \
"ds_read_b64_tr_b16 a[76:77], v75 offset:49152\n" \
"ds_read_b64_tr_b16 a[78:79], v75 offset:53248\n" \
"ds_read_b64_tr_b16 a[80:81], v74 offset:33280\n" \
"ds_read_b64_tr_b16 a[82:83], v74 offset:37376\n" \
"ds_read_b64_tr_b16 a[84:85], v74 offset:49664\n" \
"ds_read_b64_tr_b16 a[86:87], v74 offset:53760\n" \
"ds_read_b64_tr_b16 a[88:89], v75 offset:33280\n" \
"ds_read_b64_tr_b16 a[90:91], v75 offset:37376\n" \
"ds_read_b64_tr_b16 a[92:93], v75 offset:49664\n" \
"ds_read_b64_tr_b16 a[94:95], v75 offset:53760\n" \
"ds_read_b128 a[8:11], v72 offset:2048\n" \
"ds_read_b128 a[12:15], v73 offset:2048\n" \
"s_waitcnt lgkmcnt(14)\n" \
"v_mfma_f32_16x16x32_bf16 v[0:3], a[64:67], a[0:3], 0\n" \
"s_add_u32 m0, s74, 65536\n" \
"ds_read_b128 a[16:19], v72 offset:4096\n" \
"v_mfma_f32_16x16x32_bf16 v[0:3], a[68:71], a[4:7], v[0:3]\n" \
"s_waitcnt lgkmcnt(11)\n" \
"buffer_load_dwordx4 v64, s[36:39], s81 offen offset:0 lds\n" \
"v_mfma_f32_16x16x32_bf16 v[4:7], a[72:75], a[0:3], 0\n" \
"ds_read_b128 a[20:23], v73 offset:4096\n" \
"v_mfma_f32_16x16x32_bf16 v[4:7], a[76:79], a[4:7], v[4:7]\n" \
"s_waitcnt lgkmcnt(8)\n" \
"v_mfma_f32_16x16x32_bf16 v[8:11], a[80:83], a[0:3], 0\n" \
"s_waitcnt lgkmcnt(2)\n" \
"ds_read_b128 a[24:27], v72 offset:6144\n" \
"v_mfma_f32_16x16x32_bf16 v[8:11], a[84:87], a[4:7], v[8:11]\n" \
"buffer_load_dwordx4 v65, s[36:39], s81 offen offset:1024 lds\n" \
"v_mfma_f32_16x16x32_bf16 v[12:15], a[88:91], a[0:3], 0\n" \
"ds_read_b128 a[28:31], v73 offset:6144\n" \
"v_mfma_f32_16x16x32_bf16 v[12:15], a[92:95], a[4:7], v[12:15]\n" \
"v_mfma_f32_16x16x32_bf16 v[28:31], a[88:91], a[8:11], 0\n" \
"ds_read_b128 a[32:35], v72 offset:8192\n" \
"v_mfma_f32_16x16x32_bf16 v[28:31], a[92:95], a[12:15], v[28:31]\n" \
"buffer_load_dwordx4 v66, s[36:39], s81 offen offset:2048 lds\n" \
"v_mfma_f32_16x16x32_bf16 v[24:27], a[80:83], a[8:11], 0\n" \
"ds_read_b128 a[36:39], v73 offset:8192\n" \
"v_mfma_f32_16x16x32_bf16 v[24:27], a[84:87], a[12:15], v[24:27]\n" \
"v_mfma_f32_16x16x32_bf16 v[20:23], a[72:75], a[8:11], 0\n" \
"ds_read_b128 a[40:43], v72 offset:10240\n" \
"v_mfma_f32_16x16x32_bf16 v[20:23], a[76:79], a[12:15], v[20:23]\n" \
"buffer_load_dwordx4 v67, s[36:39], s81 offen offset:3072 lds\n" \
"v_mfma_f32_16x16x32_bf16 v[16:19], a[64:67], a[8:11], 0\n" \
"ds_read_b128 a[44:47], v73 offset:10240\n" \
"v_mfma_f32_16x16x32_bf16 v[16:19], a[68:71], a[12:15], v[16:19]\n" \
"s_waitcnt lgkmcnt(2)\n" \
"s_xor_b32 s81, s81, 0x180\n" \
"v_mfma_f32_16x16x32_bf16 v[32:35], a[64:67], a[16:19], 0\n" \
"s_add_u32 m0, s74, 69632\n" \
"ds_read_b128 a[48:51], v72 offset:12288\n" \
"v_mfma_f32_16x16x32_bf16 v[32:35], a[68:71], a[20:23], v[32:35]\n" \
"buffer_load_dwordx4 v64, s[36:39], s82 offen offset:0 lds\n" \
"v_mfma_f32_16x16x32_bf16 v[36:39], a[72:75], a[16:19], 0\n" \
"ds_read_b128 a[52:55], v73 offset:12288\n" \
"v_mfma_f32_16x16x32_bf16 v[36:39], a[76:79], a[20:23], v[36:39]\n" \
"v_mfma_f32_16x16x32_bf16 v[40:43], a[80:83], a[16:19], 0\n" \
"ds_read_b128 a[56:59], v72 offset:14336\n" \
"v_mfma_f32_16x16x32_bf16 v[40:43], a[84:87], a[20:23], v[40:43]\n" \
"buffer_load_dwordx4 v65, s[36:39], s82 offen offset:1024 lds\n" \
"v_mfma_f32_16x16x32_bf16 v[44:47], a[88:91], a[16:19], 0\n" \
"ds_read_b128 a[60:63], v73 offset:14336\n" \
"v_mfma_f32_16x16x32_bf16 v[44:47], a[92:95], a[20:23], v[44:47]\n" \
"v_mfma_f32_16x16x32_bf16 v[60:63], a[88:91], a[24:27], 0\n" \
"ds_read_b64_tr_b16 a[96:97], v74 offset:33792\n" \
"v_mfma_f32_16x16x32_bf16 v[60:63], a[92:95], a[28:31], v[60:63]\n" \
"buffer_load_dwordx4 v66, s[36:39], s82 offen offset:2048 lds\n" \
"v_mfma_f32_16x16x32_bf16 v[56:59], a[80:83], a[24:27], 0\n" \
"ds_read_b64_tr_b16 a[98:99], v74 offset:37888\n" \
"v_mfma_f32_16x16x32_bf16 v[56:59], a[84:87], a[28:31], v[56:59]\n" \
"v_mfma_f32_16x16x32_bf16 v[52:55], a[72:75], a[24:27], 0\n" \
"ds_read_b64_tr_b16 a[100:101], v74 offset:50176\n" \
"v_mfma_f32_16x16x32_bf16 v[52:55], a[76:79], a[28:31], v[52:55]\n" \
"buffer_load_dwordx4 v67, s[36:39], s82 offen offset:3072 lds\n" \
"v_mfma_f32_16x16x32_bf16 v[48:51], a[64:67], a[24:27], 0\n" \
"ds_read_b64_tr_b16 a[102:103], v74 offset:54272\n" \
"v_mfma_f32_16x16x32_bf16 v[48:51], a[68:71], a[28:31], v[48:51]\n" \
"v_mfma_f32_16x16x32_bf16 v[144:147], a[64:67], a[32:35], 0\n" \
"s_xor_b32 s82, s82, 0x180\n" \
"ds_read_b64_tr_b16 a[104:105], v75 offset:33792\n" \
"v_mfma_f32_16x16x32_bf16 v[144:147], a[68:71], a[36:39], v[144:147]\n" \
"v_mfma_f32_16x16x32_bf16 v[148:151], a[72:75], a[32:35], 0\n" \
"ds_read_b64_tr_b16 a[106:107], v75 offset:37888\n" \
"v_mfma_f32_16x16x32_bf16 v[148:151], a[76:79], a[36:39], v[148:151]\n" \
"v_mfma_f32_16x16x32_bf16 v[152:155], a[80:83], a[32:35], 0\n" \
"ds_read_b64_tr_b16 a[108:109], v75 offset:50176\n" \
"v_mfma_f32_16x16x32_bf16 v[152:155], a[84:87], a[36:39], v[152:155]\n" \
"v_mfma_f32_16x16x32_bf16 v[156:159], a[88:91], a[32:35], 0\n" \
"ds_read_b64_tr_b16 a[110:111], v75 offset:54272\n" \
"v_mfma_f32_16x16x32_bf16 v[156:159], a[92:95], a[36:39], v[156:159]\n" \
"s_waitcnt lgkmcnt(2)\n" \
"v_mfma_f32_16x16x32_bf16 v[188:191], a[88:91], a[40:43], 0\n" \
"ds_read_b64_tr_b16 a[112:113], v74 offset:34304\n" \
"v_add_u32 v96, 0x10000, v72\n" \
"v_mfma_f32_16x16x32_bf16 v[188:191], a[92:95], a[44:47], v[188:191]\n" \
"v_mfma_f32_16x16x32_bf16 v[184:187], a[80:83], a[40:43], 0\n" \
"ds_read_b64_tr_b16 a[114:115], v74 offset:38400\n" \
"v_add_u32 v97, 0x10000, v73\n" \
"v_mfma_f32_16x16x32_bf16 v[184:187], a[84:87], a[44:47], v[184:187]\n" \
"v_mfma_f32_16x16x32_bf16 v[180:183], a[72:75], a[40:43], 0\n" \
"ds_read_b64_tr_b16 a[116:117], v74 offset:50688\n" \
"v_add_u32 v98, 0x10000, v74\n" \
"v_mfma_f32_16x16x32_bf16 v[180:183], a[76:79], a[44:47], v[180:183]\n" \
"v_mfma_f32_16x16x32_bf16 v[176:179], a[64:67], a[40:43], 0\n" \
"ds_read_b64_tr_b16 a[118:119], v74 offset:54784\n" \
"v_add_u32 v99, 0x10000, v75\n" \
"v_mfma_f32_16x16x32_bf16 v[176:179], a[68:71], a[44:47], v[176:179]\n" \
"v_mfma_f32_16x16x32_bf16 v[192:195], a[64:67], a[48:51], 0\n" \
"s_add_u32 m0, s74, 98304\n" \
"ds_read_b64_tr_b16 a[120:121], v75 offset:34304\n" \
"v_add_u32 v100, 0x20000, v72\n" \
"v_mfma_f32_16x16x32_bf16 v[192:195], a[68:71], a[52:55], v[192:195]\n" \
"buffer_load_dwordx4 v68, s[40:43], s83 offen offset:0 lds\n" \
"v_mfma_f32_16x16x32_bf16 v[196:199], a[72:75], a[48:51], 0\n" \
"ds_read_b64_tr_b16 a[122:123], v75 offset:38400\n" \
"v_add_u32 v101, 0x20000, v73\n" \
"v_mfma_f32_16x16x32_bf16 v[196:199], a[76:79], a[52:55], v[196:199]\n" \
"v_mfma_f32_16x16x32_bf16 v[200:203], a[80:83], a[48:51], 0\n" \
"ds_read_b64_tr_b16 a[124:125], v75 offset:50688\n" \
"v_add_u32 v102, 0x20000, v74\n" \
"v_mfma_f32_16x16x32_bf16 v[200:203], a[84:87], a[52:55], v[200:203]\n" \
"buffer_load_dwordx4 v69, s[44:47], s83 offen offset:1024 lds\n" \
"v_mfma_f32_16x16x32_bf16 v[204:207], a[88:91], a[48:51], 0\n" \
"ds_read_b64_tr_b16 a[126:127], v75 offset:54784\n" \
"v_add_u32 v103, 0x20000, v75\n" \
"v_mfma_f32_16x16x32_bf16 v[204:207], a[92:95], a[52:55], v[204:207]\n" \
"v_mfma_f32_16x16x32_bf16 v[220:223], a[88:91], a[56:59], 0\n" \
"v_mfma_f32_16x16x32_bf16 v[220:223], a[92:95], a[60:63], v[220:223]\n" \
"buffer_load_dwordx4 v70, s[40:43], s83 offen offset:2048 lds\n" \
"v_mfma_f32_16x16x32_bf16 v[216:219], a[80:83], a[56:59], 0\n" \
"v_mfma_f32_16x16x32_bf16 v[216:219], a[84:87], a[60:63], v[216:219]\n" \
"v_mfma_f32_16x16x32_bf16 v[212:215], a[72:75], a[56:59], 0\n" \
"v_mfma_f32_16x16x32_bf16 v[212:215], a[76:79], a[60:63], v[212:215]\n" \
"buffer_load_dwordx4 v71, s[44:47], s83 offen offset:3072 lds\n" \
"v_mfma_f32_16x16x32_bf16 v[208:211], a[64:67], a[56:59], 0\n" \
"v_mfma_f32_16x16x32_bf16 v[208:211], a[68:71], a[60:63], v[208:211]\n" \
"s_xor_b32 s83, s83, 0x80000\n" \
"s_waitcnt lgkmcnt(0)\n" \
"s_barrier\n" \
"v_mfma_f32_16x16x32_bf16 a[128:131], a[0:3], a[96:99], 0\n" \
"s_add_u32 m0, s74, 102400\n" \
"v_mfma_f32_16x16x32_bf16 a[128:131], a[4:7], a[100:103], a[128:131]\n" \
"buffer_load_dwordx4 v68, s[40:43], s84 offen offset:0 lds\n" \
"v_mfma_f32_16x16x32_bf16 a[132:135], a[0:3], a[104:107], 0\n" \
"v_mfma_f32_16x16x32_bf16 a[132:135], a[4:7], a[108:111], a[132:135]\n" \
"v_mfma_f32_16x16x32_bf16 a[136:139], a[0:3], a[112:115], 0\n" \
"v_mfma_f32_16x16x32_bf16 a[136:139], a[4:7], a[116:119], a[136:139]\n" \
"buffer_load_dwordx4 v69, s[44:47], s84 offen offset:1024 lds\n" \
"v_mfma_f32_16x16x32_bf16 a[140:143], a[0:3], a[120:123], 0\n" \
"v_mfma_f32_16x16x32_bf16 a[140:143], a[4:7], a[124:127], a[140:143]\n" \
"v_mfma_f32_16x16x32_bf16 a[156:159], a[8:11], a[120:123], 0\n" \
"v_mfma_f32_16x16x32_bf16 a[156:159], a[12:15], a[124:127], a[156:159]\n" \
"buffer_load_dwordx4 v70, s[40:43], s84 offen offset:2048 lds\n" \
"v_mfma_f32_16x16x32_bf16 a[152:155], a[8:11], a[112:115], 0\n" \
"v_mfma_f32_16x16x32_bf16 a[152:155], a[12:15], a[116:119], a[152:155]\n" \
"v_mfma_f32_16x16x32_bf16 a[148:151], a[8:11], a[104:107], 0\n" \
"v_mfma_f32_16x16x32_bf16 a[148:151], a[12:15], a[108:111], a[148:151]\n" \
"buffer_load_dwordx4 v71, s[44:47], s84 offen offset:3072 lds\n" \
"v_mfma_f32_16x16x32_bf16 a[144:147], a[8:11], a[96:99], 0\n" \
"v_mfma_f32_16x16x32_bf16 a[144:147], a[12:15], a[100:103], a[144:147]\n" \
"v_mfma_f32_16x16x32_bf16 a[160:163], a[16:19], a[96:99], 0\n" \
"s_xor_b32 s84, s84, 0x80000\n" \
"v_mfma_f32_16x16x32_bf16 a[160:163], a[20:23], a[100:103], a[160:163]\n" \
"v_mfma_f32_16x16x32_bf16 a[164:167], a[16:19], a[104:107], 0\n" \
"v_mfma_f32_16x16x32_bf16 a[164:167], a[20:23], a[108:111], a[164:167]\n" \
"v_mfma_f32_16x16x32_bf16 a[168:171], a[16:19], a[112:115], 0\n" \
"s_add_u32 m0, s74, 32768\n" \
"v_mfma_f32_16x16x32_bf16 a[168:171], a[20:23], a[116:119], a[168:171]\n" \
"buffer_load_dwordx4 v64, s[36:39], s81 offen offset:0 lds\n" \
"v_mfma_f32_16x16x32_bf16 a[172:175], a[16:19], a[120:123], 0\n" \
"v_mfma_f32_16x16x32_bf16 a[172:175], a[20:23], a[124:127], a[172:175]\n" \
"v_mfma_f32_16x16x32_bf16 a[188:191], a[24:27], a[120:123], 0\n" \
"v_mfma_f32_16x16x32_bf16 a[188:191], a[28:31], a[124:127], a[188:191]\n" \
"buffer_load_dwordx4 v65, s[36:39], s81 offen offset:1024 lds\n" \
"v_mfma_f32_16x16x32_bf16 a[184:187], a[24:27], a[112:115], 0\n" \
"v_mfma_f32_16x16x32_bf16 a[184:187], a[28:31], a[116:119], a[184:187]\n" \
"v_mfma_f32_16x16x32_bf16 a[180:183], a[24:27], a[104:107], 0\n" \
"v_mfma_f32_16x16x32_bf16 a[180:183], a[28:31], a[108:111], a[180:183]\n" \
"buffer_load_dwordx4 v66, s[36:39], s81 offen offset:2048 lds\n" \
"v_mfma_f32_16x16x32_bf16 a[176:179], a[24:27], a[96:99], 0\n" \
"v_mfma_f32_16x16x32_bf16 a[176:179], a[28:31], a[100:103], a[176:179]\n" \
"v_mfma_f32_16x16x32_bf16 a[192:195], a[32:35], a[96:99], 0\n" \
"v_mfma_f32_16x16x32_bf16 a[192:195], a[36:39], a[100:103], a[192:195]\n" \
"buffer_load_dwordx4 v67, s[36:39], s81 offen offset:3072 lds\n" \
"v_mfma_f32_16x16x32_bf16 a[196:199], a[32:35], a[104:107], 0\n" \
"v_mfma_f32_16x16x32_bf16 a[196:199], a[36:39], a[108:111], a[196:199]\n" \
"s_xor_b32 s81, s81, 0x80\n" \
"v_mfma_f32_16x16x32_bf16 a[200:203], a[32:35], a[112:115], 0\n" \
"s_add_u32 m0, s74, 36864\n" \
"v_mfma_f32_16x16x32_bf16 a[200:203], a[36:39], a[116:119], a[200:203]\n" \
"buffer_load_dwordx4 v64, s[36:39], s82 offen offset:0 lds\n" \
"v_mfma_f32_16x16x32_bf16 a[204:207], a[32:35], a[120:123], 0\n" \
"v_mfma_f32_16x16x32_bf16 a[204:207], a[36:39], a[124:127], a[204:207]\n" \
"s_waitcnt vmcnt(13) lgkmcnt(0)\n" \
"s_barrier\n" \
"v_mfma_f32_16x16x32_bf16 a[220:223], a[40:43], a[120:123], 0\n" \
"ds_read_b64_tr_b16 a[64:65], v102 offset:0\n" \
"ds_read_b64_tr_b16 a[66:67], v102 offset:4096\n" \
"v_mfma_f32_16x16x32_bf16 a[220:223], a[44:47], a[124:127], a[220:223]\n" \
"buffer_load_dwordx4 v65, s[36:39], s82 offen offset:1024 lds\n" \
"v_mfma_f32_16x16x32_bf16 a[216:219], a[40:43], a[112:115], 0\n" \
"ds_read_b64_tr_b16 a[68:69], v102 offset:16384\n" \
"ds_read_b64_tr_b16 a[70:71], v102 offset:20480\n" \
"v_mfma_f32_16x16x32_bf16 a[216:219], a[44:47], a[116:119], a[216:219]\n" \
"ds_read_b128 a[0:3], v96 offset:0\n" \
"ds_read_b128 a[4:7], v97 offset:0\n" \
"v_mfma_f32_16x16x32_bf16 a[212:215], a[40:43], a[104:107], 0\n" \
"ds_read_b64_tr_b16 a[72:73], v103 offset:0\n" \
"ds_read_b64_tr_b16 a[74:75], v103 offset:4096\n" \
"v_mfma_f32_16x16x32_bf16 a[212:215], a[44:47], a[108:111], a[212:215]\n" \
"buffer_load_dwordx4 v66, s[36:39], s82 offen offset:2048 lds\n" \
"v_mfma_f32_16x16x32_bf16 a[208:211], a[40:43], a[96:99], 0\n" \
"ds_read_b64_tr_b16 a[76:77], v103 offset:16384\n" \
"ds_read_b64_tr_b16 a[78:79], v103 offset:20480\n" \
"v_mfma_f32_16x16x32_bf16 a[208:211], a[44:47], a[100:103], a[208:211]\n" \
"ds_read_b64_tr_b16 a[80:81], v102 offset:512\n" \
"ds_read_b64_tr_b16 a[82:83], v102 offset:4608\n" \
"v_mfma_f32_16x16x32_bf16 a[224:227], a[48:51], a[96:99], 0\n" \
"ds_read_b64_tr_b16 a[84:85], v102 offset:16896\n" \
"ds_read_b64_tr_b16 a[86:87], v102 offset:20992\n" \
"v_mfma_f32_16x16x32_bf16 a[224:227], a[52:55], a[100:103], a[224:227]\n" \
"buffer_load_dwordx4 v67, s[36:39], s82 offen offset:3072 lds\n" \
"v_mfma_f32_16x16x32_bf16 a[228:231], a[48:51], a[104:107], 0\n" \
"s_mov_b64 s[64:65], s[14:15]\n" \
"s_add_u32 s68, s67, 0x200000\n" \
"ds_read_b64_tr_b16 a[88:89], v103 offset:512\n" \
"ds_read_b64_tr_b16 a[90:91], v103 offset:4608\n" \
"v_mfma_f32_16x16x32_bf16 a[228:231], a[52:55], a[108:111], a[228:231]\n" \
"s_add_u32 s64, s64, s68\n" \
"s_addc_u32 s65, s65, 0\n" \
"ds_read_b64_tr_b16 a[92:93], v103 offset:16896\n" \
"ds_read_b64_tr_b16 a[94:95], v103 offset:20992\n" \
"v_mfma_f32_16x16x32_bf16 a[232:235], a[48:51], a[112:115], 0\n" \
"s_xor_b32 s82, s82, 0x80\n" \
"ds_read_b128 a[8:11], v96 offset:2048\n" \
"ds_read_b128 a[12:15], v97 offset:2048\n" \
"v_mfma_f32_16x16x32_bf16 a[232:235], a[52:55], a[116:119], a[232:235]\n" \
"global_load_dword v238, v234, s[64:65]\n" \
"v_mfma_f32_16x16x32_bf16 a[236:239], a[48:51], a[120:123], 0\n" \
"s_mov_b64 s[64:65], s[16:17]\n" \
"s_add_u32 s68, s67, 0x200000\n" \
"ds_read_b128 a[16:19], v96 offset:4096\n" \
"ds_read_b128 a[20:23], v97 offset:4096\n" \
"v_mfma_f32_16x16x32_bf16 a[236:239], a[52:55], a[124:127], a[236:239]\n" \
"s_add_u32 s64, s64, s68\n" \
"s_addc_u32 s65, s65, 0\n" \
"ds_read_b128 a[24:27], v96 offset:6144\n" \
"ds_read_b128 a[28:31], v97 offset:6144\n" \
"v_mfma_f32_16x16x32_bf16 a[252:255], a[56:59], a[120:123], 0\n" \
"ds_read_b128 a[32:35], v96 offset:8192\n" \
"ds_read_b128 a[36:39], v97 offset:8192\n" \
"v_mfma_f32_16x16x32_bf16 a[252:255], a[60:63], a[124:127], a[252:255]\n" \
"global_load_dword v238, v234, s[64:65]\n" \
"v_mfma_f32_16x16x32_bf16 a[248:251], a[56:59], a[112:115], 0\n" \
"ds_read_b128 a[40:43], v96 offset:10240\n" \
"ds_read_b128 a[44:47], v97 offset:10240\n" \
"v_mfma_f32_16x16x32_bf16 a[248:251], a[60:63], a[116:119], a[248:251]\n" \
"ds_read_b128 a[48:51], v96 offset:12288\n" \
"ds_read_b128 a[52:55], v97 offset:12288\n" \
"v_mfma_f32_16x16x32_bf16 a[244:247], a[56:59], a[104:107], 0\n" \
"v_mfma_f32_16x16x32_bf16 a[244:247], a[60:63], a[108:111], a[244:247]\n" \
"v_mfma_f32_16x16x32_bf16 a[240:243], a[56:59], a[96:99], 0\n" \
"v_mfma_f32_16x16x32_bf16 a[240:243], a[60:63], a[100:103], a[240:243]\n" \
"s_waitcnt lgkmcnt(2)\n" \
"v_mfma_f32_16x16x32_bf16 v[0:3], a[64:67], a[0:3], v[0:3]\n" \
"s_lshl_b32 s79, s61, 12\n" \
"ds_read_b128 a[56:59], v96 offset:14336\n" \
"v_mfma_f32_16x16x32_bf16 v[0:3], a[68:71], a[4:7], v[0:3]\n" \
"s_add_u32 s79, s79, %3\n" \
"v_add_u32 v228, 4096, v84\n" \
"v_add_u32 v229, 3200, v84\n" \
"v_mfma_f32_16x16x32_bf16 v[4:7], a[72:75], a[0:3], v[4:7]\n" \
"s_add_u32 m0, s74, 0\n" \
"ds_read_b128 a[60:63], v97 offset:14336\n" \
"v_mfma_f32_16x16x32_bf16 v[4:7], a[76:79], a[4:7], v[4:7]\n" \
"buffer_load_dwordx4 v68, s[40:43], s83 offen offset:0 lds\n" \
"v_mfma_f32_16x16x32_bf16 v[8:11], a[80:83], a[0:3], v[8:11]\n" \
"s_lshl_b32 s78, s62, 8\n" \
"ds_read_b64_tr_b16 a[96:97], v102 offset:1024\n" \
"v_mfma_f32_16x16x32_bf16 v[8:11], a[84:87], a[4:7], v[8:11]\n" \
"s_add_u32 s89, s78, 30720\n" \
"v_mfma_f32_16x16x32_bf16 v[12:15], a[88:91], a[0:3], v[12:15]\n" \
"s_mov_b64 s[48:49], s[8:9]\n" \
"ds_read_b64_tr_b16 a[98:99], v102 offset:5120\n" \
"v_mfma_f32_16x16x32_bf16 v[12:15], a[92:95], a[4:7], v[12:15]\n" \
"buffer_load_dwordx4 v69, s[44:47], s83 offen offset:1024 lds\n" \
"v_mfma_f32_16x16x32_bf16 v[28:31], a[88:91], a[8:11], v[28:31]\n" \
"s_and_b32 s49, s49, 0xffff\n" \
"ds_read_b64_tr_b16 a[100:101], v102 offset:17408\n" \
"v_mfma_f32_16x16x32_bf16 v[28:31], a[92:95], a[12:15], v[28:31]\n" \
"s_mov_b32 s50, 4194304\n" \
"v_mfma_f32_16x16x32_bf16 v[24:27], a[80:83], a[8:11], v[24:27]\n" \
"s_mov_b32 s51, 0x20000\n" \
"ds_read_b64_tr_b16 a[102:103], v102 offset:21504\n" \
"v_mfma_f32_16x16x32_bf16 v[24:27], a[84:87], a[12:15], v[24:27]\n" \
"buffer_load_dwordx4 v70, s[40:43], s83 offen offset:2048 lds\n" \
"v_mfma_f32_16x16x32_bf16 v[20:23], a[72:75], a[8:11], v[20:23]\n" \
"s_mov_b64 s[52:53], s[12:13]\n" \
"ds_read_b64_tr_b16 a[104:105], v103 offset:1024\n" \
"v_mfma_f32_16x16x32_bf16 v[20:23], a[76:79], a[12:15], v[20:23]\n" \
"s_sub_u32 s52, s52, 4096\n" \
"s_subb_u32 s53, s53, 0\n" \
"v_mfma_f32_16x16x32_bf16 v[16:19], a[64:67], a[8:11], v[16:19]\n" \
"s_and_b32 s53, s53, 0xffff\n" \
"ds_read_b64_tr_b16 a[106:107], v103 offset:5120\n" \
"v_mfma_f32_16x16x32_bf16 v[16:19], a[68:71], a[12:15], v[16:19]\n" \
"buffer_load_dwordx4 v71, s[44:47], s83 offen offset:3072 lds\n" \
"v_mfma_f32_16x16x32_bf16 v[32:35], a[64:67], a[16:19], v[32:35]\n" \
"s_mov_b32 s54, 8392704\n" \
"s_mov_b32 s55, 0x20000\n" \
"ds_read_b64_tr_b16 a[108:109], v103 offset:17408\n" \
"v_mfma_f32_16x16x32_bf16 v[32:35], a[68:71], a[20:23], v[32:35]\n" \
"s_xor_b32 s83, s83, 0x380000\n" \
"v_lshl_add_u32 v224, v85, 11, v86\n" \
"v_add_u32 v225, 15360, v224\n" \
"v_mfma_f32_16x16x32_bf16 v[36:39], a[72:75], a[16:19], v[36:39]\n" \
"s_add_u32 m0, s74, 4096\n" \
"ds_read_b64_tr_b16 a[110:111], v103 offset:21504\n" \
"v_add_u32 v226, 129024, v224\n" \
"v_mfma_f32_16x16x32_bf16 v[36:39], a[76:79], a[20:23], v[36:39]\n" \
"buffer_load_dwordx4 v68, s[40:43], s84 offen offset:0 lds\n" \
"v_mfma_f32_16x16x32_bf16 v[40:43], a[80:83], a[16:19], v[40:43]\n" \
"ds_read_b64_tr_b16 a[112:113], v102 offset:1536\n" \
"v_add_u32 v227, 144384, v224\n" \
"v_mfma_f32_16x16x32_bf16 v[40:43], a[84:87], a[20:23], v[40:43]\n" \
"s_lshl_b32 s70, s61, 15\n" \
"v_mfma_f32_16x16x32_bf16 v[44:47], a[88:91], a[16:19], v[44:47]\n" \
"s_lshl_b32 s75, s63, 18\n" \
"ds_read_b64_tr_b16 a[114:115], v102 offset:5632\n" \
"v_mfma_f32_16x16x32_bf16 v[44:47], a[92:95], a[20:23], v[44:47]\n" \
"buffer_load_dwordx4 v69, s[44:47], s84 offen offset:1024 lds\n" \
"v_mfma_f32_16x16x32_bf16 v[60:63], a[88:91], a[24:27], v[60:63]\n" \
"s_sub_u32 s77, 3932160, s75\n" \
"ds_read_b64_tr_b16 a[116:117], v102 offset:17920\n" \
"v_mfma_f32_16x16x32_bf16 v[60:63], a[92:95], a[28:31], v[60:63]\n" \
"s_lshr_b32 s71, %1, 6\n" \
"v_mfma_f32_16x16x32_bf16 v[56:59], a[80:83], a[24:27], v[56:59]\n" \
"s_xor_b32 s71, s71, s63\n" \
"ds_read_b64_tr_b16 a[118:119], v102 offset:22016\n" \
"v_mfma_f32_16x16x32_bf16 v[56:59], a[84:87], a[28:31], v[56:59]\n" \
"buffer_load_dwordx4 v70, s[40:43], s84 offen offset:2048 lds\n" \
"v_mfma_f32_16x16x32_bf16 v[52:55], a[72:75], a[24:27], v[52:55]\n" \
"s_and_b32 s71, s71, 0\n" \
"ds_read_b64_tr_b16 a[120:121], v103 offset:1536\n" \
"v_mfma_f32_16x16x32_bf16 v[52:55], a[76:79], a[28:31], v[52:55]\n" \
"s_lshl_b32 s71, s71, 7\n" \
"v_mfma_f32_16x16x32_bf16 v[48:51], a[64:67], a[24:27], v[48:51]\n" \
"s_add_u32 s70, s70, s71\n" \
"ds_read_b64_tr_b16 a[122:123], v103 offset:5632\n" \
"v_mfma_f32_16x16x32_bf16 v[48:51], a[68:71], a[28:31], v[48:51]\n" \
"buffer_load_dwordx4 v71, s[44:47], s84 offen offset:3072 lds\n" \
"v_mfma_f32_16x16x32_bf16 v[144:147], a[64:67], a[32:35], v[144:147]\n" \
"s_add_u32 s75, s75, s70\n" \
"ds_read_b64_tr_b16 a[124:125], v103 offset:17920\n" \
"v_mfma_f32_16x16x32_bf16 v[144:147], a[68:71], a[36:39], v[144:147]\n" \
"s_add_u32 s77, s77, s70\n" \
"v_mfma_f32_16x16x32_bf16 v[148:151], a[72:75], a[32:35], v[148:151]\n" \
"s_xor_b32 s84, s84, 0x380000\n" \
"ds_read_b64_tr_b16 a[126:127], v103 offset:22016\n" \
"v_mfma_f32_16x16x32_bf16 v[148:151], a[76:79], a[36:39], v[148:151]\n" \
"s_lshl_b32 s71, s71, 12\n" \
"v_mfma_f32_16x16x32_bf16 v[152:155], a[80:83], a[32:35], v[152:155]\n" \
"s_add_u32 s78, s78, s71\n" \
"v_mfma_f32_16x16x32_bf16 v[152:155], a[84:87], a[36:39], v[152:155]\n" \
"s_add_u32 s89, s89, s71\n" \
"v_lshlrev_b32 v242, 2, v78\n" \
"v_and_b32 v236, 0xf0, v234\n" \
"v_mfma_f32_16x16x32_bf16 v[156:159], a[88:91], a[32:35], v[156:159]\n" \
"v_lshl_add_u32 v236, v87, 9, v236\n" \
"v_mfma_f32_16x16x32_bf16 v[156:159], a[92:95], a[36:39], v[156:159]\n" \
"s_add_u32 s71, s74, 0x10000\n" \
"v_add_u32 v230, s71, v88\n" \
"v_xor_b32 v231, 64, v230\n" \
"v_mfma_f32_16x16x32_bf16 v[188:191], a[88:91], a[40:43], v[188:191]\n" \
"v_add_u32 v250, 0x10000, v230\n" \
"v_add_u32 v251, 0x10000, v231\n" \
"v_mfma_f32_16x16x32_bf16 v[188:191], a[92:95], a[44:47], v[188:191]\n" \
"v_lshl_add_u32 v82, v90, 12, v89\n" \
"v_add_u32 v232, %3, v82\n" \
"v_mfma_f32_16x16x32_bf16 v[184:187], a[80:83], a[40:43], v[184:187]\n" \
"v_xor_b32 v233, 32, v232\n" \
"v_mfma_f32_16x16x32_bf16 v[184:187], a[84:87], a[44:47], v[184:187]\n" \
"s_mul_i32 s87, s62, 557824\n" \
"v_mfma_f32_16x16x32_bf16 v[180:183], a[72:75], a[40:43], v[180:183]\n" \
"s_lshr_b32 s70, s61, 1\n" \
"v_mfma_f32_16x16x32_bf16 v[180:183], a[76:79], a[44:47], v[180:183]\n" \
"s_mul_i32 s70, s70, 17408\n" \
"v_mfma_f32_16x16x32_bf16 v[176:179], a[64:67], a[40:43], v[176:179]\n" \
"Lpg585_%=:\n" \
"Lst0x0_%=:\n" \
"s_cbranch_execz Lpg2766_%=\n" \
"s_add_u32 s87, s87, s70\n" \
"v_mfma_f32_16x16x32_bf16 v[176:179], a[68:71], a[44:47], v[176:179]\n" \
"s_waitcnt lgkmcnt(0)\n" \
"s_and_b32 s70, s61, 1\n" \
"v_mfma_f32_16x16x32_bf16 v[192:195], a[64:67], a[48:51], v[192:195]\n" \
"s_lshl_b32 s70, s70, 12\n" \
"v_mfma_f32_16x16x32_bf16 v[192:195], a[68:71], a[52:55], v[192:195]\n" \
"s_add_u32 s87, s87, s70\n" \
"v_mfma_f32_16x16x32_bf16 v[196:199], a[72:75], a[48:51], v[196:199]\n" \
"s_mul_i32 s70, s63, 34816\n" \
"v_mfma_f32_16x16x32_bf16 v[196:199], a[76:79], a[52:55], v[196:199]\n" \
"s_add_u32 s94, s87, 522240\n" \
"v_mfma_f32_16x16x32_bf16 v[200:203], a[80:83], a[48:51], v[200:203]\n" \
"s_sub_u32 s94, s94, s70\n" \
"v_mfma_f32_16x16x32_bf16 v[200:203], a[84:87], a[52:55], v[200:203]\n" \
"s_add_u32 s87, s87, s70\n" \
"v_mfma_f32_16x16x32_bf16 v[204:207], a[88:91], a[48:51], v[204:207]\n" \
"s_lshl_b32 s90, s62, 7\n" \
"v_mfma_f32_16x16x32_bf16 v[204:207], a[92:95], a[52:55], v[204:207]\n" \
"s_add_u32 s95, s90, 65597440\n" \
"v_mfma_f32_16x16x32_bf16 v[220:223], a[88:91], a[56:59], v[220:223]\n" \
"s_add_u32 s90, s90, 65585152\n" \
"v_mfma_f32_16x16x32_bf16 v[220:223], a[92:95], a[60:63], v[220:223]\n" \
"s_lshl_b32 s92, 0xff, s63\n" \
"v_mfma_f32_16x16x32_bf16 v[216:219], a[80:83], a[56:59], v[216:219]\n" \
"s_and_b32 s92, s92, 0xff\n" \
"v_mfma_f32_16x16x32_bf16 v[216:219], a[84:87], a[60:63], v[216:219]\n" \
"s_mov_b32 s76, 0x3e0293ee\n" \
"v_mfma_f32_16x16x32_bf16 v[212:215], a[72:75], a[56:59], v[212:215]\n" \
"s_mov_b32 s31, 0x3e800000\n" \
"v_mfma_f32_16x16x32_bf16 v[212:215], a[76:79], a[60:63], v[212:215]\n" \
"v_mfma_f32_16x16x32_bf16 v[208:211], a[64:67], a[56:59], v[208:211]\n" \
"v_mfma_f32_16x16x32_bf16 v[208:211], a[68:71], a[60:63], v[208:211]\n" \
"s_waitcnt vmcnt(10) lgkmcnt(0)\n" \
"s_barrier\n" \
"v_readfirstlane_b32 s60, v240\n" \
"s_add_u32 s60, s60, 0x9e3779b9\n" \
"v_mfma_f32_16x16x32_bf16 a[128:131], a[0:3], a[96:99], a[128:131]\n" \
"s_add_u32 m0, s74, 131072\n" \
"ds_read_b64_tr_b16 a[64:65], v98 offset:32768\n" \
"v_mfma_f32_16x16x32_bf16 a[128:131], a[4:7], a[100:103], a[128:131]\n" \
"buffer_load_dwordx4 v64, s[36:39], s81 offen offset:0 lds\n" \
"v_mfma_f32_16x16x32_bf16 a[132:135], a[0:3], a[104:107], a[132:135]\n" \
"ds_read_b64_tr_b16 a[66:67], v98 offset:36864\n" \
"v_mfma_f32_16x16x32_bf16 a[132:135], a[4:7], a[108:111], a[132:135]\n" \
"v_mfma_f32_16x16x32_bf16 a[136:139], a[0:3], a[112:115], a[136:139]\n" \
"ds_read_b64_tr_b16 a[68:69], v98 offset:49152\n" \
"v_mfma_f32_16x16x32_bf16 a[136:139], a[4:7], a[116:119], a[136:139]\n" \
"buffer_load_dwordx4 v65, s[36:39], s81 offen offset:1024 lds\n" \
"v_mfma_f32_16x16x32_bf16 a[140:143], a[0:3], a[120:123], a[140:143]\n" \
"ds_read_b64_tr_b16 a[70:71], v98 offset:53248\n" \
"v_mfma_f32_16x16x32_bf16 a[140:143], a[4:7], a[124:127], a[140:143]\n" \
"v_mfma_f32_16x16x32_bf16 a[156:159], a[8:11], a[120:123], a[156:159]\n" \
"ds_read_b128 a[0:3], v72 offset:32768\n" \
"v_mfma_f32_16x16x32_bf16 a[156:159], a[12:15], a[124:127], a[156:159]\n" \
"buffer_load_dwordx4 v66, s[36:39], s81 offen offset:2048 lds\n" \
"v_mfma_f32_16x16x32_bf16 a[152:155], a[8:11], a[112:115], a[152:155]\n" \
"ds_read_b64_tr_b16 a[72:73], v99 offset:32768\n" \
"v_mfma_f32_16x16x32_bf16 a[152:155], a[12:15], a[116:119], a[152:155]\n" \
"v_mfma_f32_16x16x32_bf16 a[148:151], a[8:11], a[104:107], a[148:151]\n" \
"ds_read_b128 a[4:7], v73 offset:32768\n" \
"v_mfma_f32_16x16x32_bf16 a[148:151], a[12:15], a[108:111], a[148:151]\n" \
"buffer_load_dwordx4 v67, s[36:39], s81 offen offset:3072 lds\n" \
"v_mfma_f32_16x16x32_bf16 a[144:147], a[8:11], a[96:99], a[144:147]\n" \
"ds_read_b64_tr_b16 a[74:75], v99 offset:36864\n" \
"v_mfma_f32_16x16x32_bf16 a[144:147], a[12:15], a[100:103], a[144:147]\n" \
"s_xor_b32 s81, s81, 0x380\n" \
"v_mfma_f32_16x16x32_bf16 a[160:163], a[16:19], a[96:99], a[160:163]\n" \
"s_add_u32 m0, s74, 135168\n" \
"ds_read_b128 a[8:11], v72 offset:34816\n" \
"v_mfma_f32_16x16x32_bf16 a[160:163], a[20:23], a[100:103], a[160:163]\n" \
"buffer_load_dwordx4 v64, s[36:39], s82 offen offset:0 lds\n" \
"v_mfma_f32_16x16x32_bf16 a[164:167], a[16:19], a[104:107], a[164:167]\n" \
"ds_read_b64_tr_b16 a[76:77], v99 offset:49152\n" \
"v_mfma_f32_16x16x32_bf16 a[164:167], a[20:23], a[108:111], a[164:167]\n" \
"v_mfma_f32_16x16x32_bf16 a[168:171], a[16:19], a[112:115], a[168:171]\n" \
"ds_read_b128 a[12:15], v73 offset:34816\n" \
"v_mfma_f32_16x16x32_bf16 a[168:171], a[20:23], a[116:119], a[168:171]\n" \
"buffer_load_dwordx4 v65, s[36:39], s82 offen offset:1024 lds\n" \
"v_mfma_f32_16x16x32_bf16 a[172:175], a[16:19], a[120:123], a[172:175]\n" \
"ds_read_b64_tr_b16 a[78:79], v99 offset:53248\n" \
"v_mfma_f32_16x16x32_bf16 a[172:175], a[20:23], a[124:127], a[172:175]\n" \
"v_mfma_f32_16x16x32_bf16 a[188:191], a[24:27], a[120:123], a[188:191]\n" \
"ds_read_b128 a[16:19], v72 offset:36864\n" \
"v_mfma_f32_16x16x32_bf16 a[188:191], a[28:31], a[124:127], a[188:191]\n" \
"buffer_load_dwordx4 v66, s[36:39], s82 offen offset:2048 lds\n" \
"v_mfma_f32_16x16x32_bf16 a[184:187], a[24:27], a[112:115], a[184:187]\n" \
"ds_read_b64_tr_b16 a[80:81], v98 offset:33280\n" \
"v_mfma_f32_16x16x32_bf16 a[184:187], a[28:31], a[116:119], a[184:187]\n" \
"v_mfma_f32_16x16x32_bf16 a[180:183], a[24:27], a[104:107], a[180:183]\n" \
"ds_read_b128 a[20:23], v73 offset:36864\n" \
"v_mfma_f32_16x16x32_bf16 a[180:183], a[28:31], a[108:111], a[180:183]\n" \
"buffer_load_dwordx4 v67, s[36:39], s82 offen offset:3072 lds\n" \
"v_mfma_f32_16x16x32_bf16 a[176:179], a[24:27], a[96:99], a[176:179]\n" \
"s_mov_b64 s[64:65], s[14:15]\n" \
"s_add_u32 s68, s67, 0x300000\n" \
"ds_read_b64_tr_b16 a[82:83], v98 offset:37376\n" \
"v_mfma_f32_16x16x32_bf16 a[176:179], a[28:31], a[100:103], a[176:179]\n" \
"s_add_u32 s64, s64, s68\n" \
"s_addc_u32 s65, s65, 0\n" \
"v_mfma_f32_16x16x32_bf16 a[192:195], a[32:35], a[96:99], a[192:195]\n" \
"s_xor_b32 s82, s82, 0x380\n" \
"ds_read_b128 a[24:27], v72 offset:38912\n" \
"v_mfma_f32_16x16x32_bf16 a[192:195], a[36:39], a[100:103], a[192:195]\n" \
"global_load_dword v238, v234, s[64:65]\n" \
"v_mfma_f32_16x16x32_bf16 a[196:199], a[32:35], a[104:107], a[196:199]\n" \
"s_mov_b64 s[64:65], s[16:17]\n" \
"s_add_u32 s68, s67, 0x300000\n" \
"ds_read_b64_tr_b16 a[84:85], v98 offset:49664\n" \
"v_mfma_f32_16x16x32_bf16 a[196:199], a[36:39], a[108:111], a[196:199]\n" \
"s_add_u32 s64, s64, s68\n" \
"s_addc_u32 s65, s65, 0\n" \
"v_mfma_f32_16x16x32_bf16 a[200:203], a[32:35], a[112:115], a[200:203]\n" \
"ds_read_b128 a[28:31], v73 offset:38912\n" \
"v_mfma_f32_16x16x32_bf16 a[200:203], a[36:39], a[116:119], a[200:203]\n" \
"global_load_dword v238, v234, s[64:65]\n" \
"v_mfma_f32_16x16x32_bf16 a[204:207], a[32:35], a[120:123], a[204:207]\n" \
"ds_read_b64_tr_b16 a[86:87], v98 offset:53760\n" \
"v_mfma_f32_16x16x32_bf16 a[204:207], a[36:39], a[124:127], a[204:207]\n" \
"v_mfma_f32_16x16x32_bf16 a[220:223], a[40:43], a[120:123], a[220:223]\n" \
"ds_read_b128 a[32:35], v72 offset:40960\n" \
"v_mfma_f32_16x16x32_bf16 a[220:223], a[44:47], a[124:127], a[220:223]\n" \
"v_mfma_f32_16x16x32_bf16 a[216:219], a[40:43], a[112:115], a[216:219]\n" \
"ds_read_b64_tr_b16 a[88:89], v99 offset:33280\n" \
"v_mfma_f32_16x16x32_bf16 a[216:219], a[44:47], a[116:119], a[216:219]\n" \
"v_mfma_f32_16x16x32_bf16 a[212:215], a[40:43], a[104:107], a[212:215]\n" \
"ds_read_b128 a[36:39], v73 offset:40960\n" \
"v_mfma_f32_16x16x32_bf16 a[212:215], a[44:47], a[108:111], a[212:215]\n" \
"v_mfma_f32_16x16x32_bf16 a[208:211], a[40:43], a[96:99], a[208:211]\n" \
"ds_read_b64_tr_b16 a[90:91], v99 offset:37376\n" \
"v_mfma_f32_16x16x32_bf16 a[208:211], a[44:47], a[100:103], a[208:211]\n" \
"v_mfma_f32_16x16x32_bf16 a[224:227], a[48:51], a[96:99], a[224:227]\n" \
"ds_read_b128 a[40:43], v72 offset:43008\n" \
"v_mfma_f32_16x16x32_bf16 a[224:227], a[52:55], a[100:103], a[224:227]\n" \
"v_mfma_f32_16x16x32_bf16 a[228:231], a[48:51], a[104:107], a[228:231]\n" \
"ds_read_b64_tr_b16 a[92:93], v99 offset:49664\n" \
"v_mfma_f32_16x16x32_bf16 a[228:231], a[52:55], a[108:111], a[228:231]\n" \
"v_mfma_f32_16x16x32_bf16 a[232:235], a[48:51], a[112:115], a[232:235]\n" \
"ds_read_b128 a[44:47], v73 offset:43008\n" \
"v_mfma_f32_16x16x32_bf16 a[232:235], a[52:55], a[116:119], a[232:235]\n" \
"v_mfma_f32_16x16x32_bf16 a[236:239], a[48:51], a[120:123], a[236:239]\n" \
"ds_read_b64_tr_b16 a[94:95], v99 offset:53760\n" \
"v_mfma_f32_16x16x32_bf16 a[236:239], a[52:55], a[124:127], a[236:239]\n" \
"v_mfma_f32_16x16x32_bf16 a[252:255], a[56:59], a[120:123], a[252:255]\n" \
"ds_read_b128 a[48:51], v72 offset:45056\n" \
"v_mfma_f32_16x16x32_bf16 a[252:255], a[60:63], a[124:127], a[252:255]\n" \
"v_mfma_f32_16x16x32_bf16 a[248:251], a[56:59], a[112:115], a[248:251]\n" \
"v_mfma_f32_16x16x32_bf16 a[248:251], a[60:63], a[116:119], a[248:251]\n" \
"v_mfma_f32_16x16x32_bf16 a[244:247], a[56:59], a[104:107], a[244:247]\n" \
"ds_read_b128 a[52:55], v73 offset:45056\n" \
"v_mfma_f32_16x16x32_bf16 a[244:247], a[60:63], a[108:111], a[244:247]\n" \
"v_mfma_f32_16x16x32_bf16 a[240:243], a[56:59], a[96:99], a[240:243]\n" \
"v_mfma_f32_16x16x32_bf16 a[240:243], a[60:63], a[100:103], a[240:243]\n" \
"s_waitcnt lgkmcnt(1)\n" \
"v_mfma_f32_16x16x32_bf16 v[0:3], a[64:67], a[0:3], v[0:3]\n" \
"ds_read_b128 a[56:59], v72 offset:47104\n" \
"v_lshrrev_b32 v104, 4, v234\n" \
"v_mfma_f32_16x16x32_bf16 v[0:3], a[68:71], a[4:7], v[0:3]\n" \
"v_bfe_u32 v105, v104, 4, 1\n" \
"v_bfe_u32 v106, v104, 2, 2\n" \
"v_mfma_f32_16x16x32_bf16 v[4:7], a[72:75], a[0:3], v[4:7]\n" \
"s_add_u32 m0, s74, 65536\n" \
"ds_read_b128 a[60:63], v73 offset:47104\n" \
"v_lshl_add_u32 v106, v105, 3, v106\n" \
"v_mfma_f32_16x16x32_bf16 v[4:7], a[76:79], a[4:7], v[4:7]\n" \
"buffer_load_dwordx4 v68, s[40:43], s83 offen offset:0 lds\n" \
"v_mfma_f32_16x16x32_bf16 v[8:11], a[80:83], a[0:3], v[8:11]\n" \
"s_lshl_b32 s71, s61, 4\n" \
"ds_read_b64_tr_b16 a[96:97], v98 offset:33792\n" \
"v_add_u32 v106, s71, v106\n" \
"v_mfma_f32_16x16x32_bf16 v[8:11], a[84:87], a[4:7], v[8:11]\n" \
"v_mul_u32_u24 v106, 6144, v106\n" \
"v_lshlrev_b32 v105, 1, v105\n" \
"v_mfma_f32_16x16x32_bf16 v[12:15], a[88:91], a[0:3], v[12:15]\n" \
"ds_read_b64_tr_b16 a[98:99], v98 offset:37888\n" \
"v_and_b32 v107, 3, v104\n" \
"v_mfma_f32_16x16x32_bf16 v[12:15], a[92:95], a[4:7], v[12:15]\n" \
"buffer_load_dwordx4 v69, s[44:47], s83 offen offset:1024 lds\n" \
"v_mfma_f32_16x16x32_bf16 v[28:31], a[88:91], a[8:11], v[28:31]\n" \
"ds_read_b64_tr_b16 a[100:101], v98 offset:50176\n" \
"v_xor_b32 v105, v105, v107\n" \
"v_mfma_f32_16x16x32_bf16 v[28:31], a[92:95], a[12:15], v[28:31]\n" \
"v_lshl_add_u32 v106, v105, 4, v106\n" \
"v_lshrrev_b32 v105, 5, v104\n" \
"v_mfma_f32_16x16x32_bf16 v[24:27], a[80:83], a[8:11], v[24:27]\n" \
"ds_read_b64_tr_b16 a[102:103], v98 offset:54272\n" \
"v_lshl_add_u32 v244, v105, 6, v106\n" \
"v_mfma_f32_16x16x32_bf16 v[24:27], a[84:87], a[12:15], v[24:27]\n" \
"buffer_load_dwordx4 v70, s[40:43], s83 offen offset:2048 lds\n" \
"v_mfma_f32_16x16x32_bf16 v[20:23], a[72:75], a[8:11], v[20:23]\n" \
"ds_read_b64_tr_b16 a[104:105], v99 offset:33792\n" \
"v_lshrrev_b32 v107, 3, v104\n" \
"v_mfma_f32_16x16x32_bf16 v[20:23], a[76:79], a[12:15], v[20:23]\n" \
"v_and_b32 v108, 7, v104\n" \
"v_xor_b32 v109, v108, v107\n" \
"v_mfma_f32_16x16x32_bf16 v[16:19], a[64:67], a[8:11], v[16:19]\n" \
"ds_read_b64_tr_b16 a[106:107], v99 offset:37888\n" \
"v_lshlrev_b32 v109, 4, v109\n" \
"v_mfma_f32_16x16x32_bf16 v[16:19], a[68:71], a[12:15], v[16:19]\n" \
"buffer_load_dwordx4 v71, s[44:47], s83 offen offset:3072 lds\n" \
"v_mfma_f32_16x16x32_bf16 v[32:35], a[64:67], a[16:19], v[32:35]\n" \
"ds_read_b64_tr_b16 a[108:109], v99 offset:50176\n" \
"v_lshl_add_u32 v245, v107, 12, v109\n" \
"v_mfma_f32_16x16x32_bf16 v[32:35], a[68:71], a[20:23], v[32:35]\n" \
"s_xor_b32 s83, s83, 0x80000\n" \
"v_mfma_f32_16x16x32_bf16 v[36:39], a[72:75], a[16:19], v[36:39]\n" \
"s_add_u32 m0, s74, 69632\n" \
"ds_read_b64_tr_b16 a[110:111], v99 offset:54272\n" \
"v_mfma_f32_16x16x32_bf16 v[36:39], a[76:79], a[20:23], v[36:39]\n" \
"buffer_load_dwordx4 v68, s[40:43], s84 offen offset:0 lds\n" \
"v_mfma_f32_16x16x32_bf16 v[40:43], a[80:83], a[16:19], v[40:43]\n" \
"s_and_b32 s70, s61, 1\n" \
"ds_read_b64_tr_b16 a[112:113], v98 offset:34304\n" \
"v_mfma_f32_16x16x32_bf16 v[40:43], a[84:87], a[20:23], v[40:43]\n" \
"s_lshl_b32 s71, s70, 2\n" \
"v_add_u32 v107, s71, v107\n" \
"v_xor_b32 v109, v108, v107\n" \
"v_mfma_f32_16x16x32_bf16 v[44:47], a[88:91], a[16:19], v[44:47]\n" \
"ds_read_b64_tr_b16 a[114:115], v98 offset:38400\n" \
"v_lshlrev_b32 v109, 4, v109\n" \
"v_mfma_f32_16x16x32_bf16 v[44:47], a[92:95], a[20:23], v[44:47]\n" \
"buffer_load_dwordx4 v69, s[44:47], s84 offen offset:1024 lds\n" \
"v_mfma_f32_16x16x32_bf16 v[60:63], a[88:91], a[24:27], v[60:63]\n" \
"ds_read_b64_tr_b16 a[116:117], v98 offset:50688\n" \
"v_lshl_add_u32 v246, v107, 12, v109\n" \
"v_mfma_f32_16x16x32_bf16 v[60:63], a[92:95], a[28:31], v[60:63]\n" \
"s_lshr_b32 s71, s61, 1\n" \
"v_mfma_f32_16x16x32_bf16 v[56:59], a[80:83], a[24:27], v[56:59]\n" \
"s_lshl_b32 s70, s71, 11\n" \
"ds_read_b64_tr_b16 a[118:119], v98 offset:54784\n" \
"v_lshl_add_u32 v246, v105, 30, v246\n" \
"v_mfma_f32_16x16x32_bf16 v[56:59], a[84:87], a[28:31], v[56:59]\n" \
"buffer_load_dwordx4 v70, s[40:43], s84 offen offset:2048 lds\n" \
"v_mfma_f32_16x16x32_bf16 v[52:55], a[72:75], a[24:27], v[52:55]\n" \
"ds_read_b64_tr_b16 a[120:121], v99 offset:34304\n" \
"v_add_u32 v246, s70, v246\n" \
"v_mfma_f32_16x16x32_bf16 v[52:55], a[76:79], a[28:31], v[52:55]\n" \
"v_lshrrev_b32 v107, 4, v104\n" \
"v_xor_b32 v107, v107, v108\n" \
"v_mfma_f32_16x16x32_bf16 v[48:51], a[64:67], a[24:27], v[48:51]\n" \
"ds_read_b64_tr_b16 a[122:123], v99 offset:38400\n" \
"v_and_b32 v109, 15, v104\n" \
"v_mfma_f32_16x16x32_bf16 v[48:51], a[68:71], a[28:31], v[48:51]\n" \
"buffer_load_dwordx4 v71, s[44:47], s84 offen offset:3072 lds\n" \
"v_mfma_f32_16x16x32_bf16 v[144:147], a[64:67], a[32:35], v[144:147]\n" \
"ds_read_b64_tr_b16 a[124:125], v99 offset:50688\n" \
"v_lshlrev_b32 v106, 7, v109\n" \
"v_mfma_f32_16x16x32_bf16 v[144:147], a[68:71], a[36:39], v[144:147]\n" \
"v_lshl_add_u32 v106, v107, 4, v106\n" \
"v_mfma_f32_16x16x32_bf16 v[148:151], a[72:75], a[32:35], v[148:151]\n" \
"s_xor_b32 s84, s84, 0x80000\n" \
"ds_read_b64_tr_b16 a[126:127], v99 offset:54784\n" \
"v_mfma_f32_16x16x32_bf16 v[148:151], a[76:79], a[36:39], v[148:151]\n" \
"s_add_u32 s70, s70, %3\n" \
"v_add_u32 v247, s70, v106\n" \
"v_lshrrev_b32 v109, 2, v109\n" \
"v_mfma_f32_16x16x32_bf16 v[152:155], a[80:83], a[32:35], v[152:155]\n" \
"v_lshl_add_u32 v109, v109, 9, v106\n" \
"v_mfma_f32_16x16x32_bf16 v[152:155], a[84:87], a[36:39], v[152:155]\n" \
"s_add_u32 s70, %3, 16384\n" \
"v_add_u32 v248, s70, v109\n" \
"v_and_b32 v106, 3, v104\n" \
"v_mfma_f32_16x16x32_bf16 v[156:159], a[88:91], a[32:35], v[156:159]\n" \
"v_lshlrev_b32 v106, 3, v106\n" \
"v_bfe_u32 v107, v104, 2, 2\n" \
"v_mfma_f32_16x16x32_bf16 v[156:159], a[92:95], a[36:39], v[156:159]\n" \
"v_lshl_add_u32 v106, v107, 6, v106\n" \
"v_bfe_u32 v107, v104, 4, 1\n" \
"v_mfma_f32_16x16x32_bf16 v[188:191], a[88:91], a[40:43], v[188:191]\n" \
"v_mul_u32_u24 v107, 288, v107\n" \
"v_mul_u32_u24 v108, 6144, v105\n" \
"v_mfma_f32_16x16x32_bf16 v[188:191], a[92:95], a[44:47], v[188:191]\n" \
"v_add3_u32 v106, v106, v107, v108\n" \
"v_mfma_f32_16x16x32_bf16 v[184:187], a[80:83], a[40:43], v[184:187]\n" \
"s_and_b32 s70, s61, 1\n" \
"v_mfma_f32_16x16x32_bf16 v[184:187], a[84:87], a[44:47], v[184:187]\n" \
"s_lshl_b32 s70, s70, 9\n" \
"v_mfma_f32_16x16x32_bf16 v[180:183], a[72:75], a[40:43], v[180:183]\n" \
"s_add_u32 s70, s70, %3\n" \
"v_add_u32 v249, s70, v106\n" \
"v_mfma_f32_16x16x32_bf16 v[180:183], a[76:79], a[44:47], v[180:183]\n" \
"s_lshl_b32 s70, s71, 5\n" \
"v_xor_b32 v249, s70, v249\n" \
"v_mfma_f32_16x16x32_bf16 v[176:179], a[64:67], a[40:43], v[176:179]\n" \
"v_mfma_f32_16x16x32_bf16 v[176:179], a[68:71], a[44:47], v[176:179]\n" \
"s_waitcnt lgkmcnt(0)\n" \
"v_mfma_f32_16x16x32_bf16 v[192:195], a[64:67], a[48:51], v[192:195]\n" \
"v_mfma_f32_16x16x32_bf16 v[192:195], a[68:71], a[52:55], v[192:195]\n" \
"v_mfma_f32_16x16x32_bf16 v[196:199], a[72:75], a[48:51], v[196:199]\n" \
"v_mfma_f32_16x16x32_bf16 v[196:199], a[76:79], a[52:55], v[196:199]\n" \
"v_mfma_f32_16x16x32_bf16 v[200:203], a[80:83], a[48:51], v[200:203]\n" \
"v_mfma_f32_16x16x32_bf16 v[200:203], a[84:87], a[52:55], v[200:203]\n" \
"v_mfma_f32_16x16x32_bf16 v[204:207], a[88:91], a[48:51], v[204:207]\n" \
"v_mfma_f32_16x16x32_bf16 v[204:207], a[92:95], a[52:55], v[204:207]\n" \
"v_mfma_f32_16x16x32_bf16 v[220:223], a[88:91], a[56:59], v[220:223]\n" \
"v_mfma_f32_16x16x32_bf16 v[220:223], a[92:95], a[60:63], v[220:223]\n" \
"v_mfma_f32_16x16x32_bf16 v[216:219], a[80:83], a[56:59], v[216:219]\n" \
"v_mfma_f32_16x16x32_bf16 v[216:219], a[84:87], a[60:63], v[216:219]\n" \
"v_mfma_f32_16x16x32_bf16 v[212:215], a[72:75], a[56:59], v[212:215]\n" \
"v_mfma_f32_16x16x32_bf16 v[212:215], a[76:79], a[60:63], v[212:215]\n" \
"v_mfma_f32_16x16x32_bf16 v[208:211], a[64:67], a[56:59], v[208:211]\n" \
"v_mfma_f32_16x16x32_bf16 v[208:211], a[68:71], a[60:63], v[208:211]\n" \
"s_waitcnt vmcnt(10) lgkmcnt(0)\n" \
"s_barrier\n" \
"v_mfma_f32_16x16x32_bf16 a[128:131], a[0:3], a[96:99], a[128:131]\n" \
"s_add_u32 m0, s74, 98304\n" \
"ds_read_b64_tr_b16 a[64:65], v74 offset:0\n" \
"v_mfma_f32_16x16x32_bf16 a[128:131], a[4:7], a[100:103], a[128:131]\n" \
"buffer_load_dwordx4 v64, s[36:39], s81 offen offset:0 lds\n" \
"v_mfma_f32_16x16x32_bf16 a[132:135], a[0:3], a[104:107], a[132:135]\n" \
"ds_read_b64_tr_b16 a[66:67], v74 offset:4096\n" \
"v_mfma_f32_16x16x32_bf16 a[132:135], a[4:7], a[108:111], a[132:135]\n" \
"v_mfma_f32_16x16x32_bf16 a[136:139], a[0:3], a[112:115], a[136:139]\n" \
"ds_read_b64_tr_b16 a[68:69], v74 offset:16384\n" \
"v_mfma_f32_16x16x32_bf16 a[136:139], a[4:7], a[116:119], a[136:139]\n" \
"buffer_load_dwordx4 v65, s[36:39], s81 offen offset:1024 lds\n" \
"v_mfma_f32_16x16x32_bf16 a[140:143], a[0:3], a[120:123], a[140:143]\n" \
"ds_read_b64_tr_b16 a[70:71], v74 offset:20480\n" \
"v_mfma_f32_16x16x32_bf16 a[140:143], a[4:7], a[124:127], a[140:143]\n" \
"v_mfma_f32_16x16x32_bf16 a[156:159], a[8:11], a[120:123], a[156:159]\n" \
"ds_read_b128 a[0:3], v100 offset:0\n" \
"v_mfma_f32_16x16x32_bf16 a[156:159], a[12:15], a[124:127], a[156:159]\n" \
"buffer_load_dwordx4 v66, s[36:39], s81 offen offset:2048 lds\n" \
"v_mfma_f32_16x16x32_bf16 a[152:155], a[8:11], a[112:115], a[152:155]\n" \
"ds_read_b64_tr_b16 a[72:73], v75 offset:0\n" \
"v_mfma_f32_16x16x32_bf16 a[152:155], a[12:15], a[116:119], a[152:155]\n" \
"v_mfma_f32_16x16x32_bf16 a[148:151], a[8:11], a[104:107], a[148:151]\n" \
"ds_read_b128 a[4:7], v101 offset:0\n" \
"v_mfma_f32_16x16x32_bf16 a[148:151], a[12:15], a[108:111], a[148:151]\n" \
"buffer_load_dwordx4 v67, s[36:39], s81 offen offset:3072 lds\n" \
"v_mfma_f32_16x16x32_bf16 a[144:147], a[8:11], a[96:99], a[144:147]\n" \
"ds_read_b64_tr_b16 a[74:75], v75 offset:4096\n" \
"v_mfma_f32_16x16x32_bf16 a[144:147], a[12:15], a[100:103], a[144:147]\n" \
"s_xor_b32 s81, s81, 0x80\n" \
"v_mfma_f32_16x16x32_bf16 a[160:163], a[16:19], a[96:99], a[160:163]\n" \
"s_add_u32 m0, s74, 102400\n" \
"ds_read_b128 a[8:11], v100 offset:2048\n" \
"v_mfma_f32_16x16x32_bf16 a[160:163], a[20:23], a[100:103], a[160:163]\n" \
"buffer_load_dwordx4 v64, s[36:39], s82 offen offset:0 lds\n" \
"v_mfma_f32_16x16x32_bf16 a[164:167], a[16:19], a[104:107], a[164:167]\n" \
"ds_read_b64_tr_b16 a[76:77], v75 offset:16384\n" \
"v_mfma_f32_16x16x32_bf16 a[164:167], a[20:23], a[108:111], a[164:167]\n" \
"v_mfma_f32_16x16x32_bf16 a[168:171], a[16:19], a[112:115], a[168:171]\n" \
"ds_read_b128 a[12:15], v101 offset:2048\n" \
"v_mfma_f32_16x16x32_bf16 a[168:171], a[20:23], a[116:119], a[168:171]\n" \
"buffer_load_dwordx4 v65, s[36:39], s82 offen offset:1024 lds\n" \
"v_mfma_f32_16x16x32_bf16 a[172:175], a[16:19], a[120:123], a[172:175]\n" \
"ds_read_b64_tr_b16 a[78:79], v75 offset:20480\n" \
"v_mfma_f32_16x16x32_bf16 a[172:175], a[20:23], a[124:127], a[172:175]\n" \
"v_mfma_f32_16x16x32_bf16 a[188:191], a[24:27], a[120:123], a[188:191]\n" \
"ds_read_b128 a[16:19], v100 offset:4096\n" \
"v_mfma_f32_16x16x32_bf16 a[188:191], a[28:31], a[124:127], a[188:191]\n" \
"buffer_load_dwordx4 v66, s[36:39], s82 offen offset:2048 lds\n" \
"v_mfma_f32_16x16x32_bf16 a[184:187], a[24:27], a[112:115], a[184:187]\n" \
"ds_read_b64_tr_b16 a[80:81], v74 offset:512\n" \
"v_mfma_f32_16x16x32_bf16 a[184:187], a[28:31], a[116:119], a[184:187]\n" \
"v_mfma_f32_16x16x32_bf16 a[180:183], a[24:27], a[104:107], a[180:183]\n" \
"ds_read_b128 a[20:23], v101 offset:4096\n" \
"v_mfma_f32_16x16x32_bf16 a[180:183], a[28:31], a[108:111], a[180:183]\n" \
"buffer_load_dwordx4 v67, s[36:39], s82 offen offset:3072 lds\n" \
"v_mfma_f32_16x16x32_bf16 a[176:179], a[24:27], a[96:99], a[176:179]\n" \
"s_mov_b64 s[64:65], s[8:9]\n" \
"s_add_u32 s68, s66, 0x0\n" \
"ds_read_b64_tr_b16 a[82:83], v74 offset:4608\n" \
"v_mfma_f32_16x16x32_bf16 a[176:179], a[28:31], a[100:103], a[176:179]\n" \
"s_add_u32 s64, s64, s68\n" \
"s_addc_u32 s65, s65, 0\n" \
"v_mfma_f32_16x16x32_bf16 a[192:195], a[32:35], a[96:99], a[192:195]\n" \
"s_xor_b32 s82, s82, 0x80\n" \
"ds_read_b128 a[24:27], v100 offset:6144\n" \
"v_mfma_f32_16x16x32_bf16 a[192:195], a[36:39], a[100:103], a[192:195]\n" \
"global_load_dword v238, v234, s[64:65]\n" \
"v_mfma_f32_16x16x32_bf16 a[196:199], a[32:35], a[104:107], a[196:199]\n" \
"s_mov_b64 s[64:65], s[8:9]\n" \
"s_add_u32 s68, s66, 0x100000\n" \
"ds_read_b64_tr_b16 a[84:85], v74 offset:16896\n" \
"v_mfma_f32_16x16x32_bf16 a[196:199], a[36:39], a[108:111], a[196:199]\n" \
"s_add_u32 s64, s64, s68\n" \
"s_addc_u32 s65, s65, 0\n" \
"v_mfma_f32_16x16x32_bf16 a[200:203], a[32:35], a[112:115], a[200:203]\n" \
"ds_read_b128 a[28:31], v101 offset:6144\n" \
"v_mfma_f32_16x16x32_bf16 a[200:203], a[36:39], a[116:119], a[200:203]\n" \
"global_load_dword v238, v234, s[64:65]\n" \
"v_mfma_f32_16x16x32_bf16 a[204:207], a[32:35], a[120:123], a[204:207]\n" \
"ds_read_b64_tr_b16 a[86:87], v74 offset:20992\n" \
"v_mfma_f32_16x16x32_bf16 a[204:207], a[36:39], a[124:127], a[204:207]\n" \
"v_mfma_f32_16x16x32_bf16 a[220:223], a[40:43], a[120:123], a[220:223]\n" \
"ds_read_b128 a[32:35], v100 offset:8192\n" \
"v_mfma_f32_16x16x32_bf16 a[220:223], a[44:47], a[124:127], a[220:223]\n" \
"v_mfma_f32_16x16x32_bf16 a[216:219], a[40:43], a[112:115], a[216:219]\n" \
"ds_read_b64_tr_b16 a[88:89], v75 offset:512\n" \
"v_mfma_f32_16x16x32_bf16 a[216:219], a[44:47], a[116:119], a[216:219]\n" \
"v_mfma_f32_16x16x32_bf16 a[212:215], a[40:43], a[104:107], a[212:215]\n" \
"ds_read_b128 a[36:39], v101 offset:8192\n" \
"v_mfma_f32_16x16x32_bf16 a[212:215], a[44:47], a[108:111], a[212:215]\n" \
"v_mfma_f32_16x16x32_bf16 a[208:211], a[40:43], a[96:99], a[208:211]\n" \
"ds_read_b64_tr_b16 a[90:91], v75 offset:4608\n" \
"v_mfma_f32_16x16x32_bf16 a[208:211], a[44:47], a[100:103], a[208:211]\n" \
"v_mfma_f32_16x16x32_bf16 a[224:227], a[48:51], a[96:99], a[224:227]\n" \
"ds_read_b128 a[40:43], v100 offset:10240\n" \
"v_mfma_f32_16x16x32_bf16 a[224:227], a[52:55], a[100:103], a[224:227]\n" \
"v_mfma_f32_16x16x32_bf16 a[228:231], a[48:51], a[104:107], a[228:231]\n" \
"ds_read_b64_tr_b16 a[92:93], v75 offset:16896\n" \
"v_mfma_f32_16x16x32_bf16 a[228:231], a[52:55], a[108:111], a[228:231]\n" \
"v_mfma_f32_16x16x32_bf16 a[232:235], a[48:51], a[112:115], a[232:235]\n" \
"ds_read_b128 a[44:47], v101 offset:10240\n" \
"v_mfma_f32_16x16x32_bf16 a[232:235], a[52:55], a[116:119], a[232:235]\n" \
"v_mfma_f32_16x16x32_bf16 a[236:239], a[48:51], a[120:123], a[236:239]\n" \
"ds_read_b64_tr_b16 a[94:95], v75 offset:20992\n" \
"v_mfma_f32_16x16x32_bf16 a[236:239], a[52:55], a[124:127], a[236:239]\n" \
"v_mfma_f32_16x16x32_bf16 a[252:255], a[56:59], a[120:123], a[252:255]\n" \
"ds_read_b128 a[48:51], v100 offset:12288\n" \
"v_mfma_f32_16x16x32_bf16 a[252:255], a[60:63], a[124:127], a[252:255]\n" \
"v_mfma_f32_16x16x32_bf16 a[248:251], a[56:59], a[112:115], a[248:251]\n" \
"v_mfma_f32_16x16x32_bf16 a[248:251], a[60:63], a[116:119], a[248:251]\n" \
"v_mfma_f32_16x16x32_bf16 a[244:247], a[56:59], a[104:107], a[244:247]\n" \
"ds_read_b128 a[52:55], v101 offset:12288\n" \
"v_mfma_f32_16x16x32_bf16 a[244:247], a[60:63], a[108:111], a[244:247]\n" \
"v_mfma_f32_16x16x32_bf16 a[240:243], a[56:59], a[96:99], a[240:243]\n" \
"v_mfma_f32_16x16x32_bf16 a[240:243], a[60:63], a[100:103], a[240:243]\n" \
"s_waitcnt lgkmcnt(1)\n" \
"v_mfma_f32_16x16x32_bf16 v[0:3], a[64:67], a[0:3], v[0:3]\n" \
"ds_read_b128 a[56:59], v100 offset:14336\n" \
"v_mfma_f32_16x16x32_bf16 v[0:3], a[68:71], a[4:7], v[0:3]\n" \
"v_mfma_f32_16x16x32_bf16 v[4:7], a[72:75], a[0:3], v[4:7]\n" \
"s_add_u32 m0, s74, 32768\n" \
"ds_read_b128 a[60:63], v101 offset:14336\n" \
"v_mfma_f32_16x16x32_bf16 v[4:7], a[76:79], a[4:7], v[4:7]\n" \
"buffer_load_dwordx4 v68, s[40:43], s83 offen offset:0 lds\n" \
"v_mfma_f32_16x16x32_bf16 v[8:11], a[80:83], a[0:3], v[8:11]\n" \
"ds_read_b64_tr_b16 a[96:97], v74 offset:1024\n" \
"v_mfma_f32_16x16x32_bf16 v[8:11], a[84:87], a[4:7], v[8:11]\n" \
"v_mfma_f32_16x16x32_bf16 v[12:15], a[88:91], a[0:3], v[12:15]\n" \
"ds_read_b64_tr_b16 a[98:99], v74 offset:5120\n" \
"v_mfma_f32_16x16x32_bf16 v[12:15], a[92:95], a[4:7], v[12:15]\n" \
"buffer_load_dwordx4 v69, s[44:47], s83 offen offset:1024 lds\n" \
"v_mfma_f32_16x16x32_bf16 v[28:31], a[88:91], a[8:11], v[28:31]\n" \
"ds_read_b64_tr_b16 a[100:101], v74 offset:17408\n" \
"v_mfma_f32_16x16x32_bf16 v[28:31], a[92:95], a[12:15], v[28:31]\n" \
"v_mfma_f32_16x16x32_bf16 v[24:27], a[80:83], a[8:11], v[24:27]\n" \
"ds_read_b64_tr_b16 a[102:103], v74 offset:21504\n" \
"v_mfma_f32_16x16x32_bf16 v[24:27], a[84:87], a[12:15], v[24:27]\n" \
"buffer_load_dwordx4 v70, s[40:43], s83 offen offset:2048 lds\n" \
"v_mfma_f32_16x16x32_bf16 v[20:23], a[72:75], a[8:11], v[20:23]\n" \
"ds_read_b64_tr_b16 a[104:105], v75 offset:1024\n" \
"v_mfma_f32_16x16x32_bf16 v[20:23], a[76:79], a[12:15], v[20:23]\n" \
"v_mfma_f32_16x16x32_bf16 v[16:19], a[64:67], a[8:11], v[16:19]\n" \
"ds_read_b64_tr_b16 a[106:107], v75 offset:5120\n" \
"v_mfma_f32_16x16x32_bf16 v[16:19], a[68:71], a[12:15], v[16:19]\n" \
"buffer_load_dwordx4 v71, s[44:47], s83 offen offset:3072 lds\n" \
"v_mfma_f32_16x16x32_bf16 v[32:35], a[64:67], a[16:19], v[32:35]\n" \
"ds_read_b64_tr_b16 a[108:109], v75 offset:17408\n" \
"v_mfma_f32_16x16x32_bf16 v[32:35], a[68:71], a[20:23], v[32:35]\n" \
"s_xor_b32 s83, s83, 0x180000\n" \
"v_mfma_f32_16x16x32_bf16 v[36:39], a[72:75], a[16:19], v[36:39]\n" \
"s_add_u32 m0, s74, 36864\n" \
"ds_read_b64_tr_b16 a[110:111], v75 offset:21504\n" \
"v_mfma_f32_16x16x32_bf16 v[36:39], a[76:79], a[20:23], v[36:39]\n" \
"buffer_load_dwordx4 v68, s[40:43], s84 offen offset:0 lds\n" \
"v_mfma_f32_16x16x32_bf16 v[40:43], a[80:83], a[16:19], v[40:43]\n" \
"ds_read_b64_tr_b16 a[112:113], v74 offset:1536\n" \
"v_mfma_f32_16x16x32_bf16 v[40:43], a[84:87], a[20:23], v[40:43]\n" \
"v_mfma_f32_16x16x32_bf16 v[44:47], a[88:91], a[16:19], v[44:47]\n" \
"ds_read_b64_tr_b16 a[114:115], v74 offset:5632\n" \
"v_mfma_f32_16x16x32_bf16 v[44:47], a[92:95], a[20:23], v[44:47]\n" \
"buffer_load_dwordx4 v69, s[44:47], s84 offen offset:1024 lds\n" \
"v_mfma_f32_16x16x32_bf16 v[60:63], a[88:91], a[24:27], v[60:63]\n" \
"ds_read_b64_tr_b16 a[116:117], v74 offset:17920\n" \
"v_mfma_f32_16x16x32_bf16 v[60:63], a[92:95], a[28:31], v[60:63]\n" \
"v_mfma_f32_16x16x32_bf16 v[56:59], a[80:83], a[24:27], v[56:59]\n" \
"ds_read_b64_tr_b16 a[118:119], v74 offset:22016\n" \
"v_mfma_f32_16x16x32_bf16 v[56:59], a[84:87], a[28:31], v[56:59]\n" \
"buffer_load_dwordx4 v70, s[40:43], s84 offen offset:2048 lds\n" \
"v_mfma_f32_16x16x32_bf16 v[52:55], a[72:75], a[24:27], v[52:55]\n" \
"ds_read_b64_tr_b16 a[120:121], v75 offset:1536\n" \
"v_mfma_f32_16x16x32_bf16 v[52:55], a[76:79], a[28:31], v[52:55]\n" \
"v_mfma_f32_16x16x32_bf16 v[48:51], a[64:67], a[24:27], v[48:51]\n" \
"ds_read_b64_tr_b16 a[122:123], v75 offset:5632\n" \
"v_mfma_f32_16x16x32_bf16 v[48:51], a[68:71], a[28:31], v[48:51]\n" \
"buffer_load_dwordx4 v71, s[44:47], s84 offen offset:3072 lds\n" \
"v_mfma_f32_16x16x32_bf16 v[144:147], a[64:67], a[32:35], v[144:147]\n" \
"ds_read_b64_tr_b16 a[124:125], v75 offset:17920\n" \
"v_mfma_f32_16x16x32_bf16 v[144:147], a[68:71], a[36:39], v[144:147]\n" \
"v_mfma_f32_16x16x32_bf16 v[148:151], a[72:75], a[32:35], v[148:151]\n" \
"s_xor_b32 s84, s84, 0x180000\n" \
"ds_read_b64_tr_b16 a[126:127], v75 offset:22016\n" \
"v_mfma_f32_16x16x32_bf16 v[148:151], a[76:79], a[36:39], v[148:151]\n" \
"v_mfma_f32_16x16x32_bf16 v[152:155], a[80:83], a[32:35], v[152:155]\n" \
"v_mfma_f32_16x16x32_bf16 v[152:155], a[84:87], a[36:39], v[152:155]\n" \
"v_mfma_f32_16x16x32_bf16 v[156:159], a[88:91], a[32:35], v[156:159]\n" \
"v_mfma_f32_16x16x32_bf16 v[156:159], a[92:95], a[36:39], v[156:159]\n" \
"v_mfma_f32_16x16x32_bf16 v[188:191], a[88:91], a[40:43], v[188:191]\n" \
"v_mfma_f32_16x16x32_bf16 v[188:191], a[92:95], a[44:47], v[188:191]\n" \
"v_mfma_f32_16x16x32_bf16 v[184:187], a[80:83], a[40:43], v[184:187]\n" \
"v_mfma_f32_16x16x32_bf16 v[184:187], a[84:87], a[44:47], v[184:187]\n" \
"v_mfma_f32_16x16x32_bf16 v[180:183], a[72:75], a[40:43], v[180:183]\n" \
"v_mfma_f32_16x16x32_bf16 v[180:183], a[76:79], a[44:47], v[180:183]\n" \
"v_mfma_f32_16x16x32_bf16 v[176:179], a[64:67], a[40:43], v[176:179]\n" \
"v_mfma_f32_16x16x32_bf16 v[176:179], a[68:71], a[44:47], v[176:179]\n" \
"s_waitcnt lgkmcnt(0)\n" \
"v_mfma_f32_16x16x32_bf16 v[192:195], a[64:67], a[48:51], v[192:195]\n" \
"v_mfma_f32_16x16x32_bf16 v[192:195], a[68:71], a[52:55], v[192:195]\n" \
"v_mfma_f32_16x16x32_bf16 v[196:199], a[72:75], a[48:51], v[196:199]\n" \
"v_mfma_f32_16x16x32_bf16 v[196:199], a[76:79], a[52:55], v[196:199]\n" \
"v_mfma_f32_16x16x32_bf16 v[200:203], a[80:83], a[48:51], v[200:203]\n" \
"v_mfma_f32_16x16x32_bf16 v[200:203], a[84:87], a[52:55], v[200:203]\n" \
"v_mfma_f32_16x16x32_bf16 v[204:207], a[88:91], a[48:51], v[204:207]\n" \
"v_mfma_f32_16x16x32_bf16 v[204:207], a[92:95], a[52:55], v[204:207]\n" \
"v_mfma_f32_16x16x32_bf16 v[220:223], a[88:91], a[56:59], v[220:223]\n" \
"v_mfma_f32_16x16x32_bf16 v[220:223], a[92:95], a[60:63], v[220:223]\n" \
"v_mfma_f32_16x16x32_bf16 v[216:219], a[80:83], a[56:59], v[216:219]\n" \
"v_mfma_f32_16x16x32_bf16 v[216:219], a[84:87], a[60:63], v[216:219]\n" \
"v_mfma_f32_16x16x32_bf16 v[212:215], a[72:75], a[56:59], v[212:215]\n" \
"v_mfma_f32_16x16x32_bf16 v[212:215], a[76:79], a[60:63], v[212:215]\n" \
"v_mfma_f32_16x16x32_bf16 v[208:211], a[64:67], a[56:59], v[208:211]\n" \
"v_mfma_f32_16x16x32_bf16 v[208:211], a[68:71], a[60:63], v[208:211]\n" \
"s_waitcnt vmcnt(10) lgkmcnt(0)\n" \
"s_barrier\n" \
"v_mfma_f32_16x16x32_bf16 a[128:131], a[0:3], a[96:99], a[128:131]\n" \
"s_add_u32 m0, s74, 0\n" \
"ds_read_b64_tr_b16 a[64:65], v98 offset:0\n" \
"v_mfma_f32_16x16x32_bf16 a[128:131], a[4:7], a[100:103], a[128:131]\n" \
"buffer_load_dwordx4 v64, s[36:39], s81 offen offset:0 lds\n" \
"v_mfma_f32_16x16x32_bf16 a[132:135], a[0:3], a[104:107], a[132:135]\n" \
"ds_read_b64_tr_b16 a[66:67], v98 offset:4096\n" \
"v_mfma_f32_16x16x32_bf16 a[132:135], a[4:7], a[108:111], a[132:135]\n" \
"v_mfma_f32_16x16x32_bf16 a[136:139], a[0:3], a[112:115], a[136:139]\n" \
"ds_read_b64_tr_b16 a[68:69], v98 offset:16384\n" \
"v_mfma_f32_16x16x32_bf16 a[136:139], a[4:7], a[116:119], a[136:139]\n" \
"buffer_load_dwordx4 v65, s[36:39], s81 offen offset:1024 lds\n" \
"v_mfma_f32_16x16x32_bf16 a[140:143], a[0:3], a[120:123], a[140:143]\n" \
"ds_read_b64_tr_b16 a[70:71], v98 offset:20480\n" \
"v_mfma_f32_16x16x32_bf16 a[140:143], a[4:7], a[124:127], a[140:143]\n" \
"v_mfma_f32_16x16x32_bf16 a[156:159], a[8:11], a[120:123], a[156:159]\n" \
"ds_read_b128 a[0:3], v96 offset:32768\n" \
"v_mfma_f32_16x16x32_bf16 a[156:159], a[12:15], a[124:127], a[156:159]\n" \
"buffer_load_dwordx4 v66, s[36:39], s81 offen offset:2048 lds\n" \
"v_mfma_f32_16x16x32_bf16 a[152:155], a[8:11], a[112:115], a[152:155]\n" \
"ds_read_b64_tr_b16 a[72:73], v99 offset:0\n" \
"v_mfma_f32_16x16x32_bf16 a[152:155], a[12:15], a[116:119], a[152:155]\n" \
"v_mfma_f32_16x16x32_bf16 a[148:151], a[8:11], a[104:107], a[148:151]\n" \
"ds_read_b128 a[4:7], v97 offset:32768\n" \
"v_mfma_f32_16x16x32_bf16 a[148:151], a[12:15], a[108:111], a[148:151]\n" \
"buffer_load_dwordx4 v67, s[36:39], s81 offen offset:3072 lds\n" \
"v_mfma_f32_16x16x32_bf16 a[144:147], a[8:11], a[96:99], a[144:147]\n" \
"ds_read_b64_tr_b16 a[74:75], v99 offset:4096\n" \
"Lpg1132_%=:\n" \
"Lst0x1_%=:\n" \
"s_cbranch_execz Lpg3342_%=\n" \
"v_mfma_f32_16x16x32_bf16 a[144:147], a[12:15], a[100:103], a[144:147]\n" \
"s_xor_b32 s81, s81, 0x180\n" \
"v_mfma_f32_16x16x32_bf16 a[160:163], a[16:19], a[96:99], a[160:163]\n" \
"s_add_u32 m0, s74, 4096\n" \
"ds_read_b128 a[8:11], v96 offset:34816\n" \
"v_mfma_f32_16x16x32_bf16 a[160:163], a[20:23], a[100:103], a[160:163]\n" \
"buffer_load_dwordx4 v64, s[36:39], s82 offen offset:0 lds\n" \
"v_mfma_f32_16x16x32_bf16 a[164:167], a[16:19], a[104:107], a[164:167]\n" \
"ds_read_b64_tr_b16 a[76:77], v99 offset:16384\n" \
"v_mfma_f32_16x16x32_bf16 a[164:167], a[20:23], a[108:111], a[164:167]\n" \
"v_mfma_f32_16x16x32_bf16 a[168:171], a[16:19], a[112:115], a[168:171]\n" \
"ds_read_b128 a[12:15], v97 offset:34816\n" \
"v_mfma_f32_16x16x32_bf16 a[168:171], a[20:23], a[116:119], a[168:171]\n" \
"buffer_load_dwordx4 v65, s[36:39], s82 offen offset:1024 lds\n" \
"v_mfma_f32_16x16x32_bf16 a[172:175], a[16:19], a[120:123], a[172:175]\n" \
"ds_read_b64_tr_b16 a[78:79], v99 offset:20480\n" \
"v_mfma_f32_16x16x32_bf16 a[172:175], a[20:23], a[124:127], a[172:175]\n" \
"v_mfma_f32_16x16x32_bf16 a[188:191], a[24:27], a[120:123], a[188:191]\n" \
"ds_read_b128 a[16:19], v96 offset:36864\n" \
"v_mfma_f32_16x16x32_bf16 a[188:191], a[28:31], a[124:127], a[188:191]\n" \
"buffer_load_dwordx4 v66, s[36:39], s82 offen offset:2048 lds\n" \
"v_mfma_f32_16x16x32_bf16 a[184:187], a[24:27], a[112:115], a[184:187]\n" \
"ds_read_b64_tr_b16 a[80:81], v98 offset:512\n" \
"v_mfma_f32_16x16x32_bf16 a[184:187], a[28:31], a[116:119], a[184:187]\n" \
"v_mfma_f32_16x16x32_bf16 a[180:183], a[24:27], a[104:107], a[180:183]\n" \
"ds_read_b128 a[20:23], v97 offset:36864\n" \
"v_mfma_f32_16x16x32_bf16 a[180:183], a[28:31], a[108:111], a[180:183]\n" \
"buffer_load_dwordx4 v67, s[36:39], s82 offen offset:3072 lds\n" \
"v_mfma_f32_16x16x32_bf16 a[176:179], a[24:27], a[96:99], a[176:179]\n" \
"s_mov_b64 s[64:65], s[8:9]\n" \
"s_add_u32 s68, s66, 0x200000\n" \
"ds_read_b64_tr_b16 a[82:83], v98 offset:4608\n" \
"v_mfma_f32_16x16x32_bf16 a[176:179], a[28:31], a[100:103], a[176:179]\n" \
"s_add_u32 s64, s64, s68\n" \
"s_addc_u32 s65, s65, 0\n" \
"v_mfma_f32_16x16x32_bf16 a[192:195], a[32:35], a[96:99], a[192:195]\n" \
"s_xor_b32 s82, s82, 0x180\n" \
"ds_read_b128 a[24:27], v96 offset:38912\n" \
"v_mfma_f32_16x16x32_bf16 a[192:195], a[36:39], a[100:103], a[192:195]\n" \
"global_load_dword v238, v234, s[64:65]\n" \
"v_mfma_f32_16x16x32_bf16 a[196:199], a[32:35], a[104:107], a[196:199]\n" \
"s_mov_b64 s[64:65], s[8:9]\n" \
"s_add_u32 s68, s66, 0x300000\n" \
"ds_read_b64_tr_b16 a[84:85], v98 offset:16896\n" \
"v_mfma_f32_16x16x32_bf16 a[196:199], a[36:39], a[108:111], a[196:199]\n" \
"s_add_u32 s64, s64, s68\n" \
"s_addc_u32 s65, s65, 0\n" \
"v_mfma_f32_16x16x32_bf16 a[200:203], a[32:35], a[112:115], a[200:203]\n" \
"ds_read_b128 a[28:31], v97 offset:38912\n" \
"v_mfma_f32_16x16x32_bf16 a[200:203], a[36:39], a[116:119], a[200:203]\n" \
"global_load_dword v238, v234, s[64:65]\n" \
"v_mfma_f32_16x16x32_bf16 a[204:207], a[32:35], a[120:123], a[204:207]\n" \
"s_mov_b64 s[64:65], s[12:13]\n" \
"s_add_u32 s68, s67, 0x0\n" \
"ds_read_b64_tr_b16 a[86:87], v98 offset:20992\n" \
"v_mfma_f32_16x16x32_bf16 a[204:207], a[36:39], a[124:127], a[204:207]\n" \
"s_add_u32 s64, s64, s68\n" \
"s_addc_u32 s65, s65, 0\n" \
"v_mfma_f32_16x16x32_bf16 a[220:223], a[40:43], a[120:123], a[220:223]\n" \
"ds_read_b128 a[32:35], v96 offset:40960\n" \
"v_mfma_f32_16x16x32_bf16 a[220:223], a[44:47], a[124:127], a[220:223]\n" \
"global_load_dword v238, v234, s[64:65]\n" \
"v_mfma_f32_16x16x32_bf16 a[216:219], a[40:43], a[112:115], a[216:219]\n" \
"ds_read_b64_tr_b16 a[88:89], v99 offset:512\n" \
"v_mfma_f32_16x16x32_bf16 a[216:219], a[44:47], a[116:119], a[216:219]\n" \
"v_mfma_f32_16x16x32_bf16 a[212:215], a[40:43], a[104:107], a[212:215]\n" \
"ds_read_b128 a[36:39], v97 offset:40960\n" \
"v_mfma_f32_16x16x32_bf16 a[212:215], a[44:47], a[108:111], a[212:215]\n" \
"v_mfma_f32_16x16x32_bf16 a[208:211], a[40:43], a[96:99], a[208:211]\n" \
"ds_read_b64_tr_b16 a[90:91], v99 offset:4608\n" \
"v_mfma_f32_16x16x32_bf16 a[208:211], a[44:47], a[100:103], a[208:211]\n" \
"v_mfma_f32_16x16x32_bf16 a[224:227], a[48:51], a[96:99], a[224:227]\n" \
"ds_read_b128 a[40:43], v96 offset:43008\n" \
"v_mfma_f32_16x16x32_bf16 a[224:227], a[52:55], a[100:103], a[224:227]\n" \
"v_mfma_f32_16x16x32_bf16 a[228:231], a[48:51], a[104:107], a[228:231]\n" \
"ds_read_b64_tr_b16 a[92:93], v99 offset:16896\n" \
"v_mfma_f32_16x16x32_bf16 a[228:231], a[52:55], a[108:111], a[228:231]\n" \
"v_mfma_f32_16x16x32_bf16 a[232:235], a[48:51], a[112:115], a[232:235]\n" \
"ds_read_b128 a[44:47], v97 offset:43008\n" \
"v_mfma_f32_16x16x32_bf16 a[232:235], a[52:55], a[116:119], a[232:235]\n" \
"v_mfma_f32_16x16x32_bf16 a[236:239], a[48:51], a[120:123], a[236:239]\n" \
"ds_read_b64_tr_b16 a[94:95], v99 offset:20992\n" \
"v_mfma_f32_16x16x32_bf16 a[236:239], a[52:55], a[124:127], a[236:239]\n" \
"v_mfma_f32_16x16x32_bf16 a[252:255], a[56:59], a[120:123], a[252:255]\n" \
"ds_read_b128 a[48:51], v96 offset:45056\n" \
"v_mfma_f32_16x16x32_bf16 a[252:255], a[60:63], a[124:127], a[252:255]\n" \
"v_mfma_f32_16x16x32_bf16 a[248:251], a[56:59], a[112:115], a[248:251]\n" \
"v_mfma_f32_16x16x32_bf16 a[248:251], a[60:63], a[116:119], a[248:251]\n" \
"v_mfma_f32_16x16x32_bf16 a[244:247], a[56:59], a[104:107], a[244:247]\n" \
"ds_read_b128 a[52:55], v97 offset:45056\n" \
"v_mfma_f32_16x16x32_bf16 a[244:247], a[60:63], a[108:111], a[244:247]\n" \
"v_mfma_f32_16x16x32_bf16 a[240:243], a[56:59], a[96:99], a[240:243]\n" \
"v_mfma_f32_16x16x32_bf16 a[240:243], a[60:63], a[100:103], a[240:243]\n" \
"s_waitcnt lgkmcnt(1)\n" \
"v_mfma_f32_16x16x32_bf16 v[0:3], a[64:67], a[0:3], v[0:3]\n" \
"ds_read_b128 a[56:59], v96 offset:47104\n" \
"v_mfma_f32_16x16x32_bf16 v[0:3], a[68:71], a[4:7], v[0:3]\n" \
"v_mfma_f32_16x16x32_bf16 v[4:7], a[72:75], a[0:3], v[4:7]\n" \
"s_add_u32 m0, s74, 131072\n" \
"ds_read_b128 a[60:63], v97 offset:47104\n" \
"v_mfma_f32_16x16x32_bf16 v[4:7], a[76:79], a[4:7], v[4:7]\n" \
"buffer_load_dwordx4 v68, s[40:43], s83 offen offset:0 lds\n" \
"v_mfma_f32_16x16x32_bf16 v[8:11], a[80:83], a[0:3], v[8:11]\n" \
"ds_read_b64_tr_b16 a[96:97], v98 offset:1024\n" \
"v_mfma_f32_16x16x32_bf16 v[8:11], a[84:87], a[4:7], v[8:11]\n" \
"v_mfma_f32_16x16x32_bf16 v[12:15], a[88:91], a[0:3], v[12:15]\n" \
"ds_read_b64_tr_b16 a[98:99], v98 offset:5120\n" \
"v_mfma_f32_16x16x32_bf16 v[12:15], a[92:95], a[4:7], v[12:15]\n" \
"buffer_load_dwordx4 v69, s[44:47], s83 offen offset:1024 lds\n" \
"v_mfma_f32_16x16x32_bf16 v[28:31], a[88:91], a[8:11], v[28:31]\n" \
"ds_read_b64_tr_b16 a[100:101], v98 offset:17408\n" \
"v_mfma_f32_16x16x32_bf16 v[28:31], a[92:95], a[12:15], v[28:31]\n" \
"v_mfma_f32_16x16x32_bf16 v[24:27], a[80:83], a[8:11], v[24:27]\n" \
"ds_read_b64_tr_b16 a[102:103], v98 offset:21504\n" \
"v_mfma_f32_16x16x32_bf16 v[24:27], a[84:87], a[12:15], v[24:27]\n" \
"buffer_load_dwordx4 v70, s[40:43], s83 offen offset:2048 lds\n" \
"v_mfma_f32_16x16x32_bf16 v[20:23], a[72:75], a[8:11], v[20:23]\n" \
"ds_read_b64_tr_b16 a[104:105], v99 offset:1024\n" \
"v_mfma_f32_16x16x32_bf16 v[20:23], a[76:79], a[12:15], v[20:23]\n" \
"v_mfma_f32_16x16x32_bf16 v[16:19], a[64:67], a[8:11], v[16:19]\n" \
"ds_read_b64_tr_b16 a[106:107], v99 offset:5120\n" \
"v_mfma_f32_16x16x32_bf16 v[16:19], a[68:71], a[12:15], v[16:19]\n" \
"buffer_load_dwordx4 v71, s[44:47], s83 offen offset:3072 lds\n" \
"v_mfma_f32_16x16x32_bf16 v[32:35], a[64:67], a[16:19], v[32:35]\n" \
"ds_read_b64_tr_b16 a[108:109], v99 offset:17408\n" \
"v_mfma_f32_16x16x32_bf16 v[32:35], a[68:71], a[20:23], v[32:35]\n" \
"s_xor_b32 s83, s83, 0x80000\n" \
"v_mfma_f32_16x16x32_bf16 v[36:39], a[72:75], a[16:19], v[36:39]\n" \
"s_add_u32 m0, s74, 135168\n" \
"ds_read_b64_tr_b16 a[110:111], v99 offset:21504\n" \
"v_mfma_f32_16x16x32_bf16 v[36:39], a[76:79], a[20:23], v[36:39]\n" \
"buffer_load_dwordx4 v68, s[40:43], s84 offen offset:0 lds\n" \
"v_mfma_f32_16x16x32_bf16 v[40:43], a[80:83], a[16:19], v[40:43]\n" \
"ds_read_b64_tr_b16 a[112:113], v98 offset:1536\n" \
"v_mfma_f32_16x16x32_bf16 v[40:43], a[84:87], a[20:23], v[40:43]\n" \
"v_mfma_f32_16x16x32_bf16 v[44:47], a[88:91], a[16:19], v[44:47]\n" \
"ds_read_b64_tr_b16 a[114:115], v98 offset:5632\n" \
"v_mfma_f32_16x16x32_bf16 v[44:47], a[92:95], a[20:23], v[44:47]\n" \
"buffer_load_dwordx4 v69, s[44:47], s84 offen offset:1024 lds\n" \
"v_mfma_f32_16x16x32_bf16 v[60:63], a[88:91], a[24:27], v[60:63]\n" \
"ds_read_b64_tr_b16 a[116:117], v98 offset:17920\n" \
"v_mfma_f32_16x16x32_bf16 v[60:63], a[92:95], a[28:31], v[60:63]\n" \
"v_mfma_f32_16x16x32_bf16 v[56:59], a[80:83], a[24:27], v[56:59]\n" \
"ds_read_b64_tr_b16 a[118:119], v98 offset:22016\n" \
"v_mfma_f32_16x16x32_bf16 v[56:59], a[84:87], a[28:31], v[56:59]\n" \
"buffer_load_dwordx4 v70, s[40:43], s84 offen offset:2048 lds\n" \
"v_mfma_f32_16x16x32_bf16 v[52:55], a[72:75], a[24:27], v[52:55]\n" \
"ds_read_b64_tr_b16 a[120:121], v99 offset:1536\n" \
"v_mfma_f32_16x16x32_bf16 v[52:55], a[76:79], a[28:31], v[52:55]\n" \
"v_mfma_f32_16x16x32_bf16 v[48:51], a[64:67], a[24:27], v[48:51]\n" \
"ds_read_b64_tr_b16 a[122:123], v99 offset:5632\n" \
"v_mfma_f32_16x16x32_bf16 v[48:51], a[68:71], a[28:31], v[48:51]\n" \
"buffer_load_dwordx4 v71, s[44:47], s84 offen offset:3072 lds\n" \
"v_mfma_f32_16x16x32_bf16 v[144:147], a[64:67], a[32:35], v[144:147]\n" \
"ds_read_b64_tr_b16 a[124:125], v99 offset:17920\n" \
"v_mfma_f32_16x16x32_bf16 v[144:147], a[68:71], a[36:39], v[144:147]\n" \
"v_mfma_f32_16x16x32_bf16 v[148:151], a[72:75], a[32:35], v[148:151]\n" \
"s_xor_b32 s84, s84, 0x80000\n" \
"ds_read_b64_tr_b16 a[126:127], v99 offset:22016\n" \
"v_mfma_f32_16x16x32_bf16 v[148:151], a[76:79], a[36:39], v[148:151]\n" \
"v_mfma_f32_16x16x32_bf16 v[152:155], a[80:83], a[32:35], v[152:155]\n" \
"v_mfma_f32_16x16x32_bf16 v[152:155], a[84:87], a[36:39], v[152:155]\n" \
"v_mfma_f32_16x16x32_bf16 v[156:159], a[88:91], a[32:35], v[156:159]\n" \
"v_mfma_f32_16x16x32_bf16 v[156:159], a[92:95], a[36:39], v[156:159]\n" \
"v_mfma_f32_16x16x32_bf16 v[188:191], a[88:91], a[40:43], v[188:191]\n" \
"v_mfma_f32_16x16x32_bf16 v[188:191], a[92:95], a[44:47], v[188:191]\n" \
"v_mfma_f32_16x16x32_bf16 v[184:187], a[80:83], a[40:43], v[184:187]\n" \
"v_mfma_f32_16x16x32_bf16 v[184:187], a[84:87], a[44:47], v[184:187]\n" \
"v_mfma_f32_16x16x32_bf16 v[180:183], a[72:75], a[40:43], v[180:183]\n" \
"v_mfma_f32_16x16x32_bf16 v[180:183], a[76:79], a[44:47], v[180:183]\n" \
"v_mfma_f32_16x16x32_bf16 v[176:179], a[64:67], a[40:43], v[176:179]\n" \
"v_mfma_f32_16x16x32_bf16 v[176:179], a[68:71], a[44:47], v[176:179]\n" \
"s_waitcnt lgkmcnt(0)\n" \
"v_mfma_f32_16x16x32_bf16 v[192:195], a[64:67], a[48:51], v[192:195]\n" \
"v_mfma_f32_16x16x32_bf16 v[192:195], a[68:71], a[52:55], v[192:195]\n" \
"v_mfma_f32_16x16x32_bf16 v[196:199], a[72:75], a[48:51], v[196:199]\n" \
"v_mfma_f32_16x16x32_bf16 v[196:199], a[76:79], a[52:55], v[196:199]\n" \
"v_mfma_f32_16x16x32_bf16 v[200:203], a[80:83], a[48:51], v[200:203]\n" \
"v_mfma_f32_16x16x32_bf16 v[200:203], a[84:87], a[52:55], v[200:203]\n" \
"v_mfma_f32_16x16x32_bf16 v[204:207], a[88:91], a[48:51], v[204:207]\n" \
"v_mfma_f32_16x16x32_bf16 v[204:207], a[92:95], a[52:55], v[204:207]\n" \
"v_mfma_f32_16x16x32_bf16 v[220:223], a[88:91], a[56:59], v[220:223]\n" \
"v_mfma_f32_16x16x32_bf16 v[220:223], a[92:95], a[60:63], v[220:223]\n" \
"v_mfma_f32_16x16x32_bf16 v[216:219], a[80:83], a[56:59], v[216:219]\n" \
"v_mfma_f32_16x16x32_bf16 v[216:219], a[84:87], a[60:63], v[216:219]\n" \
"v_mfma_f32_16x16x32_bf16 v[212:215], a[72:75], a[56:59], v[212:215]\n" \
"v_mfma_f32_16x16x32_bf16 v[212:215], a[76:79], a[60:63], v[212:215]\n" \
"v_mfma_f32_16x16x32_bf16 v[208:211], a[64:67], a[56:59], v[208:211]\n" \
"v_mfma_f32_16x16x32_bf16 v[208:211], a[68:71], a[60:63], v[208:211]\n" \
"s_waitcnt vmcnt(11) lgkmcnt(0)\n" \
"s_barrier\n" \
"v_mfma_f32_16x16x32_bf16 a[128:131], a[0:3], a[96:99], a[128:131]\n" \
"s_add_u32 m0, s74, 65536\n" \
"ds_read_b64_tr_b16 a[64:65], v74 offset:32768\n" \
"v_mfma_f32_16x16x32_bf16 a[128:131], a[4:7], a[100:103], a[128:131]\n" \
"buffer_load_dwordx4 v64, s[36:39], s81 offen offset:0 lds\n" \
"v_mfma_f32_16x16x32_bf16 a[132:135], a[0:3], a[104:107], a[132:135]\n" \
"ds_read_b64_tr_b16 a[66:67], v74 offset:36864\n" \
"v_mfma_f32_16x16x32_bf16 a[132:135], a[4:7], a[108:111], a[132:135]\n" \
"v_mfma_f32_16x16x32_bf16 a[136:139], a[0:3], a[112:115], a[136:139]\n" \
"ds_read_b64_tr_b16 a[68:69], v74 offset:49152\n" \
"v_mfma_f32_16x16x32_bf16 a[136:139], a[4:7], a[116:119], a[136:139]\n" \
"buffer_load_dwordx4 v65, s[36:39], s81 offen offset:1024 lds\n" \
"v_mfma_f32_16x16x32_bf16 a[140:143], a[0:3], a[120:123], a[140:143]\n" \
"ds_read_b64_tr_b16 a[70:71], v74 offset:53248\n" \
"v_mfma_f32_16x16x32_bf16 a[140:143], a[4:7], a[124:127], a[140:143]\n" \
"v_mfma_f32_16x16x32_bf16 a[156:159], a[8:11], a[120:123], a[156:159]\n" \
"ds_read_b128 a[0:3], v72 offset:0\n" \
"v_mfma_f32_16x16x32_bf16 a[156:159], a[12:15], a[124:127], a[156:159]\n" \
"buffer_load_dwordx4 v66, s[36:39], s81 offen offset:2048 lds\n" \
"v_mfma_f32_16x16x32_bf16 a[152:155], a[8:11], a[112:115], a[152:155]\n" \
"ds_read_b64_tr_b16 a[72:73], v75 offset:32768\n" \
"v_mfma_f32_16x16x32_bf16 a[152:155], a[12:15], a[116:119], a[152:155]\n" \
"v_mfma_f32_16x16x32_bf16 a[148:151], a[8:11], a[104:107], a[148:151]\n" \
"ds_read_b128 a[4:7], v73 offset:0\n" \
"v_mfma_f32_16x16x32_bf16 a[148:151], a[12:15], a[108:111], a[148:151]\n" \
"buffer_load_dwordx4 v67, s[36:39], s81 offen offset:3072 lds\n" \
"v_mfma_f32_16x16x32_bf16 a[144:147], a[8:11], a[96:99], a[144:147]\n" \
"ds_read_b64_tr_b16 a[74:75], v75 offset:36864\n" \
"v_mfma_f32_16x16x32_bf16 a[144:147], a[12:15], a[100:103], a[144:147]\n" \
"s_xor_b32 s81, s81, 0x80\n" \
"v_mfma_f32_16x16x32_bf16 a[160:163], a[16:19], a[96:99], a[160:163]\n" \
"s_add_u32 m0, s74, 69632\n" \
"ds_read_b128 a[8:11], v72 offset:2048\n" \
"v_mfma_f32_16x16x32_bf16 a[160:163], a[20:23], a[100:103], a[160:163]\n" \
"buffer_load_dwordx4 v64, s[36:39], s82 offen offset:0 lds\n" \
"v_mfma_f32_16x16x32_bf16 a[164:167], a[16:19], a[104:107], a[164:167]\n" \
"ds_read_b64_tr_b16 a[76:77], v75 offset:49152\n" \
"v_mfma_f32_16x16x32_bf16 a[164:167], a[20:23], a[108:111], a[164:167]\n" \
"v_mfma_f32_16x16x32_bf16 a[168:171], a[16:19], a[112:115], a[168:171]\n" \
"ds_read_b128 a[12:15], v73 offset:2048\n" \
"v_mfma_f32_16x16x32_bf16 a[168:171], a[20:23], a[116:119], a[168:171]\n" \
"buffer_load_dwordx4 v65, s[36:39], s82 offen offset:1024 lds\n" \
"v_mfma_f32_16x16x32_bf16 a[172:175], a[16:19], a[120:123], a[172:175]\n" \
"ds_read_b64_tr_b16 a[78:79], v75 offset:53248\n" \
"v_mfma_f32_16x16x32_bf16 a[172:175], a[20:23], a[124:127], a[172:175]\n" \
"v_mfma_f32_16x16x32_bf16 a[188:191], a[24:27], a[120:123], a[188:191]\n" \
"ds_read_b128 a[16:19], v72 offset:4096\n" \
"v_mfma_f32_16x16x32_bf16 a[188:191], a[28:31], a[124:127], a[188:191]\n" \
"buffer_load_dwordx4 v66, s[36:39], s82 offen offset:2048 lds\n" \
"v_mfma_f32_16x16x32_bf16 a[184:187], a[24:27], a[112:115], a[184:187]\n" \
"ds_read_b64_tr_b16 a[80:81], v74 offset:33280\n" \
"v_mfma_f32_16x16x32_bf16 a[184:187], a[28:31], a[116:119], a[184:187]\n" \
"v_mfma_f32_16x16x32_bf16 a[180:183], a[24:27], a[104:107], a[180:183]\n" \
"ds_read_b128 a[20:23], v73 offset:4096\n" \
"v_mfma_f32_16x16x32_bf16 a[180:183], a[28:31], a[108:111], a[180:183]\n" \
"buffer_load_dwordx4 v67, s[36:39], s82 offen offset:3072 lds\n" \
"v_mfma_f32_16x16x32_bf16 a[176:179], a[24:27], a[96:99], a[176:179]\n" \
"s_mov_b64 s[64:65], s[12:13]\n" \
"s_add_u32 s68, s67, 0x100000\n" \
"ds_read_b64_tr_b16 a[82:83], v74 offset:37376\n" \
"v_mfma_f32_16x16x32_bf16 a[176:179], a[28:31], a[100:103], a[176:179]\n" \
"s_add_u32 s64, s64, s68\n" \
"s_addc_u32 s65, s65, 0\n" \
"v_mfma_f32_16x16x32_bf16 a[192:195], a[32:35], a[96:99], a[192:195]\n" \
"s_xor_b32 s82, s82, 0x80\n" \
"ds_read_b128 a[24:27], v72 offset:6144\n" \
"v_mfma_f32_16x16x32_bf16 a[192:195], a[36:39], a[100:103], a[192:195]\n" \
"global_load_dword v238, v234, s[64:65]\n" \
"v_mfma_f32_16x16x32_bf16 a[196:199], a[32:35], a[104:107], a[196:199]\n" \
"s_mov_b64 s[64:65], s[12:13]\n" \
"s_add_u32 s68, s67, 0x500000\n" \
"ds_read_b64_tr_b16 a[84:85], v74 offset:49664\n" \
"v_mfma_f32_16x16x32_bf16 a[196:199], a[36:39], a[108:111], a[196:199]\n" \
"s_add_u32 s64, s64, s68\n" \
"s_addc_u32 s65, s65, 0\n" \
"v_mfma_f32_16x16x32_bf16 a[200:203], a[32:35], a[112:115], a[200:203]\n" \
"ds_read_b128 a[28:31], v73 offset:6144\n" \
"v_mfma_f32_16x16x32_bf16 a[200:203], a[36:39], a[116:119], a[200:203]\n" \
"global_load_dword v238, v234, s[64:65]\n" \
"v_mfma_f32_16x16x32_bf16 a[204:207], a[32:35], a[120:123], a[204:207]\n" \
"s_mov_b64 s[64:65], s[12:13]\n" \
"s_add_u32 s68, s67, 0x200000\n" \
"ds_read_b64_tr_b16 a[86:87], v74 offset:53760\n" \
"v_mfma_f32_16x16x32_bf16 a[204:207], a[36:39], a[124:127], a[204:207]\n" \
"s_add_u32 s64, s64, s68\n" \
"s_addc_u32 s65, s65, 0\n" \
"v_mfma_f32_16x16x32_bf16 a[220:223], a[40:43], a[120:123], a[220:223]\n" \
"ds_read_b128 a[32:35], v72 offset:8192\n" \
"v_mfma_f32_16x16x32_bf16 a[220:223], a[44:47], a[124:127], a[220:223]\n" \
"global_load_dword v238, v234, s[64:65]\n" \
"v_mfma_f32_16x16x32_bf16 a[216:219], a[40:43], a[112:115], a[216:219]\n" \
"ds_read_b64_tr_b16 a[88:89], v75 offset:33280\n" \
"v_mfma_f32_16x16x32_bf16 a[216:219], a[44:47], a[116:119], a[216:219]\n" \
"v_mfma_f32_16x16x32_bf16 a[212:215], a[40:43], a[104:107], a[212:215]\n" \
"ds_read_b128 a[36:39], v73 offset:8192\n" \
"v_mfma_f32_16x16x32_bf16 a[212:215], a[44:47], a[108:111], a[212:215]\n" \
"v_mfma_f32_16x16x32_bf16 a[208:211], a[40:43], a[96:99], a[208:211]\n" \
"ds_read_b64_tr_b16 a[90:91], v75 offset:37376\n" \
"v_mfma_f32_16x16x32_bf16 a[208:211], a[44:47], a[100:103], a[208:211]\n" \
"v_mfma_f32_16x16x32_bf16 a[224:227], a[48:51], a[96:99], a[224:227]\n" \
"ds_read_b128 a[40:43], v72 offset:10240\n" \
"v_mfma_f32_16x16x32_bf16 a[224:227], a[52:55], a[100:103], a[224:227]\n" \
"v_mfma_f32_16x16x32_bf16 a[228:231], a[48:51], a[104:107], a[228:231]\n" \
"ds_read_b64_tr_b16 a[92:93], v75 offset:49664\n" \
"v_mfma_f32_16x16x32_bf16 a[228:231], a[52:55], a[108:111], a[228:231]\n" \
"v_mfma_f32_16x16x32_bf16 a[232:235], a[48:51], a[112:115], a[232:235]\n" \
"ds_read_b128 a[44:47], v73 offset:10240\n" \
"v_mfma_f32_16x16x32_bf16 a[232:235], a[52:55], a[116:119], a[232:235]\n" \
"v_mfma_f32_16x16x32_bf16 a[236:239], a[48:51], a[120:123], a[236:239]\n" \
"ds_read_b64_tr_b16 a[94:95], v75 offset:53760\n" \
"v_mfma_f32_16x16x32_bf16 a[236:239], a[52:55], a[124:127], a[236:239]\n" \
"v_mfma_f32_16x16x32_bf16 a[252:255], a[56:59], a[120:123], a[252:255]\n" \
"ds_read_b128 a[48:51], v72 offset:12288\n" \
"v_mfma_f32_16x16x32_bf16 a[252:255], a[60:63], a[124:127], a[252:255]\n" \
"v_mfma_f32_16x16x32_bf16 a[248:251], a[56:59], a[112:115], a[248:251]\n" \
"v_mfma_f32_16x16x32_bf16 a[248:251], a[60:63], a[116:119], a[248:251]\n" \
"v_mfma_f32_16x16x32_bf16 a[244:247], a[56:59], a[104:107], a[244:247]\n" \
"ds_read_b128 a[52:55], v73 offset:12288\n" \
"v_mfma_f32_16x16x32_bf16 a[244:247], a[60:63], a[108:111], a[244:247]\n" \
"v_mfma_f32_16x16x32_bf16 a[240:243], a[56:59], a[96:99], a[240:243]\n" \
"v_mfma_f32_16x16x32_bf16 a[240:243], a[60:63], a[100:103], a[240:243]\n" \
"s_waitcnt lgkmcnt(1)\n" \
"v_mfma_f32_16x16x32_bf16 v[0:3], a[64:67], a[0:3], v[0:3]\n" \
"ds_read_b128 a[56:59], v72 offset:14336\n" \
"v_mfma_f32_16x16x32_bf16 v[0:3], a[68:71], a[4:7], v[0:3]\n" \
"v_mfma_f32_16x16x32_bf16 v[4:7], a[72:75], a[0:3], v[4:7]\n" \
"s_add_u32 m0, s74, 98304\n" \
"ds_read_b128 a[60:63], v73 offset:14336\n" \
"v_mfma_f32_16x16x32_bf16 v[4:7], a[76:79], a[4:7], v[4:7]\n" \
"buffer_load_dwordx4 v68, s[40:43], s83 offen offset:0 lds\n" \
"v_mfma_f32_16x16x32_bf16 v[8:11], a[80:83], a[0:3], v[8:11]\n" \
"ds_read_b64_tr_b16 a[96:97], v74 offset:33792\n" \
"v_mfma_f32_16x16x32_bf16 v[8:11], a[84:87], a[4:7], v[8:11]\n" \
"v_mfma_f32_16x16x32_bf16 v[12:15], a[88:91], a[0:3], v[12:15]\n" \
"ds_read_b64_tr_b16 a[98:99], v74 offset:37888\n" \
"v_mfma_f32_16x16x32_bf16 v[12:15], a[92:95], a[4:7], v[12:15]\n" \
"buffer_load_dwordx4 v69, s[44:47], s83 offen offset:1024 lds\n" \
"v_mfma_f32_16x16x32_bf16 v[28:31], a[88:91], a[8:11], v[28:31]\n" \
"ds_read_b64_tr_b16 a[100:101], v74 offset:50176\n" \
"v_mfma_f32_16x16x32_bf16 v[28:31], a[92:95], a[12:15], v[28:31]\n" \
"v_mfma_f32_16x16x32_bf16 v[24:27], a[80:83], a[8:11], v[24:27]\n" \
"ds_read_b64_tr_b16 a[102:103], v74 offset:54272\n" \
"v_mfma_f32_16x16x32_bf16 v[24:27], a[84:87], a[12:15], v[24:27]\n" \
"buffer_load_dwordx4 v70, s[40:43], s83 offen offset:2048 lds\n" \
"v_mfma_f32_16x16x32_bf16 v[20:23], a[72:75], a[8:11], v[20:23]\n" \
"ds_read_b64_tr_b16 a[104:105], v75 offset:33792\n" \
"v_mfma_f32_16x16x32_bf16 v[20:23], a[76:79], a[12:15], v[20:23]\n" \
"v_mfma_f32_16x16x32_bf16 v[16:19], a[64:67], a[8:11], v[16:19]\n" \
"ds_read_b64_tr_b16 a[106:107], v75 offset:37888\n" \
"v_mfma_f32_16x16x32_bf16 v[16:19], a[68:71], a[12:15], v[16:19]\n" \
"buffer_load_dwordx4 v71, s[44:47], s83 offen offset:3072 lds\n" \
"v_mfma_f32_16x16x32_bf16 v[32:35], a[64:67], a[16:19], v[32:35]\n" \
"ds_read_b64_tr_b16 a[108:109], v75 offset:50176\n" \
"v_mfma_f32_16x16x32_bf16 v[32:35], a[68:71], a[20:23], v[32:35]\n" \
"v_mfma_f32_16x16x32_bf16 v[36:39], a[72:75], a[16:19], v[36:39]\n" \
"s_add_u32 m0, s74, 102400\n" \
"ds_read_b64_tr_b16 a[110:111], v75 offset:54272\n" \
"v_mfma_f32_16x16x32_bf16 v[36:39], a[76:79], a[20:23], v[36:39]\n" \
"buffer_load_dwordx4 v68, s[40:43], s84 offen offset:0 lds\n" \
"v_mfma_f32_16x16x32_bf16 v[40:43], a[80:83], a[16:19], v[40:43]\n" \
"ds_read_b64_tr_b16 a[112:113], v74 offset:34304\n" \
"v_mfma_f32_16x16x32_bf16 v[40:43], a[84:87], a[20:23], v[40:43]\n" \
"v_mfma_f32_16x16x32_bf16 v[44:47], a[88:91], a[16:19], v[44:47]\n" \
"ds_read_b64_tr_b16 a[114:115], v74 offset:38400\n" \
"v_mfma_f32_16x16x32_bf16 v[44:47], a[92:95], a[20:23], v[44:47]\n" \
"buffer_load_dwordx4 v69, s[44:47], s84 offen offset:1024 lds\n" \
"v_mfma_f32_16x16x32_bf16 v[60:63], a[88:91], a[24:27], v[60:63]\n" \
"ds_read_b64_tr_b16 a[116:117], v74 offset:50688\n" \
"v_mfma_f32_16x16x32_bf16 v[60:63], a[92:95], a[28:31], v[60:63]\n" \
"v_mfma_f32_16x16x32_bf16 v[56:59], a[80:83], a[24:27], v[56:59]\n" \
"ds_read_b64_tr_b16 a[118:119], v74 offset:54784\n" \
"v_mfma_f32_16x16x32_bf16 v[56:59], a[84:87], a[28:31], v[56:59]\n" \
"buffer_load_dwordx4 v70, s[40:43], s84 offen offset:2048 lds\n" \
"v_mfma_f32_16x16x32_bf16 v[52:55], a[72:75], a[24:27], v[52:55]\n" \
"ds_read_b64_tr_b16 a[120:121], v75 offset:34304\n" \
"v_mfma_f32_16x16x32_bf16 v[52:55], a[76:79], a[28:31], v[52:55]\n" \
"v_mfma_f32_16x16x32_bf16 v[48:51], a[64:67], a[24:27], v[48:51]\n" \
"ds_read_b64_tr_b16 a[122:123], v75 offset:38400\n" \
"v_mfma_f32_16x16x32_bf16 v[48:51], a[68:71], a[28:31], v[48:51]\n" \
"buffer_load_dwordx4 v71, s[44:47], s84 offen offset:3072 lds\n" \
"v_mfma_f32_16x16x32_bf16 v[144:147], a[64:67], a[32:35], v[144:147]\n" \
"ds_read_b64_tr_b16 a[124:125], v75 offset:50688\n" \
"v_mfma_f32_16x16x32_bf16 v[144:147], a[68:71], a[36:39], v[144:147]\n" \
"v_mfma_f32_16x16x32_bf16 v[148:151], a[72:75], a[32:35], v[148:151]\n" \
"ds_read_b64_tr_b16 a[126:127], v75 offset:54784\n" \
"v_mfma_f32_16x16x32_bf16 v[148:151], a[76:79], a[36:39], v[148:151]\n" \
"v_mfma_f32_16x16x32_bf16 v[152:155], a[80:83], a[32:35], v[152:155]\n" \
"v_mfma_f32_16x16x32_bf16 v[152:155], a[84:87], a[36:39], v[152:155]\n" \
"v_mfma_f32_16x16x32_bf16 v[156:159], a[88:91], a[32:35], v[156:159]\n" \
"v_mfma_f32_16x16x32_bf16 v[156:159], a[92:95], a[36:39], v[156:159]\n" \
"v_mfma_f32_16x16x32_bf16 v[188:191], a[88:91], a[40:43], v[188:191]\n" \
"v_mfma_f32_16x16x32_bf16 v[188:191], a[92:95], a[44:47], v[188:191]\n" \
"v_mfma_f32_16x16x32_bf16 v[184:187], a[80:83], a[40:43], v[184:187]\n" \
"v_mfma_f32_16x16x32_bf16 v[184:187], a[84:87], a[44:47], v[184:187]\n" \
"v_mfma_f32_16x16x32_bf16 v[180:183], a[72:75], a[40:43], v[180:183]\n" \
"v_mfma_f32_16x16x32_bf16 v[180:183], a[76:79], a[44:47], v[180:183]\n" \
"v_mfma_f32_16x16x32_bf16 v[176:179], a[64:67], a[40:43], v[176:179]\n" \
"v_mfma_f32_16x16x32_bf16 v[176:179], a[68:71], a[44:47], v[176:179]\n" \
"s_waitcnt lgkmcnt(0)\n" \
"v_mfma_f32_16x16x32_bf16 v[192:195], a[64:67], a[48:51], v[192:195]\n" \
"v_mfma_f32_16x16x32_bf16 v[192:195], a[68:71], a[52:55], v[192:195]\n" \
"v_mfma_f32_16x16x32_bf16 v[196:199], a[72:75], a[48:51], v[196:199]\n" \
"v_mfma_f32_16x16x32_bf16 v[196:199], a[76:79], a[52:55], v[196:199]\n" \
"v_mfma_f32_16x16x32_bf16 v[200:203], a[80:83], a[48:51], v[200:203]\n" \
"v_mfma_f32_16x16x32_bf16 v[200:203], a[84:87], a[52:55], v[200:203]\n" \
"v_mfma_f32_16x16x32_bf16 v[204:207], a[88:91], a[48:51], v[204:207]\n" \
"v_mfma_f32_16x16x32_bf16 v[204:207], a[92:95], a[52:55], v[204:207]\n" \
"v_mfma_f32_16x16x32_bf16 v[220:223], a[88:91], a[56:59], v[220:223]\n" \
"v_mfma_f32_16x16x32_bf16 v[220:223], a[92:95], a[60:63], v[220:223]\n" \
"v_mfma_f32_16x16x32_bf16 v[216:219], a[80:83], a[56:59], v[216:219]\n" \
"v_mfma_f32_16x16x32_bf16 v[216:219], a[84:87], a[60:63], v[216:219]\n" \
"v_mfma_f32_16x16x32_bf16 v[212:215], a[72:75], a[56:59], v[212:215]\n" \
"v_mfma_f32_16x16x32_bf16 v[212:215], a[76:79], a[60:63], v[212:215]\n" \
"v_mfma_f32_16x16x32_bf16 v[208:211], a[64:67], a[56:59], v[208:211]\n" \
"v_mfma_f32_16x16x32_bf16 v[208:211], a[68:71], a[60:63], v[208:211]\n" \
"s_waitcnt vmcnt(11) lgkmcnt(0)\n" \
"s_barrier\n" \
"v_mfma_f32_16x16x32_bf16 a[128:131], a[0:3], a[96:99], a[128:131]\n" \
"s_add_u32 m0, s74, 32768\n" \
"ds_read_b64_tr_b16 a[64:65], v102 offset:0\n" \
"v_mfma_f32_16x16x32_bf16 a[128:131], a[4:7], a[100:103], a[128:131]\n" \
"buffer_load_dwordx4 v64, s[36:39], s81 offen offset:0 lds\n" \
"v_mfma_f32_16x16x32_bf16 a[132:135], a[0:3], a[104:107], a[132:135]\n" \
"ds_read_b64_tr_b16 a[66:67], v102 offset:4096\n" \
"v_mfma_f32_16x16x32_bf16 a[132:135], a[4:7], a[108:111], a[132:135]\n" \
"v_mfma_f32_16x16x32_bf16 a[136:139], a[0:3], a[112:115], a[136:139]\n" \
"ds_read_b64_tr_b16 a[68:69], v102 offset:16384\n" \
"v_mfma_f32_16x16x32_bf16 a[136:139], a[4:7], a[116:119], a[136:139]\n" \
"buffer_load_dwordx4 v65, s[36:39], s81 offen offset:1024 lds\n" \
"v_mfma_f32_16x16x32_bf16 a[140:143], a[0:3], a[120:123], a[140:143]\n" \
"ds_read_b64_tr_b16 a[70:71], v102 offset:20480\n" \
"v_mfma_f32_16x16x32_bf16 a[140:143], a[4:7], a[124:127], a[140:143]\n" \
"v_mfma_f32_16x16x32_bf16 a[156:159], a[8:11], a[120:123], a[156:159]\n" \
"ds_read_b128 a[0:3], v96 offset:0\n" \
"v_mfma_f32_16x16x32_bf16 a[156:159], a[12:15], a[124:127], a[156:159]\n" \
"buffer_load_dwordx4 v66, s[36:39], s81 offen offset:2048 lds\n" \
"v_mfma_f32_16x16x32_bf16 a[152:155], a[8:11], a[112:115], a[152:155]\n" \
"ds_read_b64_tr_b16 a[72:73], v103 offset:0\n" \
"v_mfma_f32_16x16x32_bf16 a[152:155], a[12:15], a[116:119], a[152:155]\n" \
"v_mfma_f32_16x16x32_bf16 a[148:151], a[8:11], a[104:107], a[148:151]\n" \
"ds_read_b128 a[4:7], v97 offset:0\n" \
"v_mfma_f32_16x16x32_bf16 a[148:151], a[12:15], a[108:111], a[148:151]\n" \
"buffer_load_dwordx4 v67, s[36:39], s81 offen offset:3072 lds\n" \
"v_mfma_f32_16x16x32_bf16 a[144:147], a[8:11], a[96:99], a[144:147]\n" \
"ds_read_b64_tr_b16 a[74:75], v103 offset:4096\n" \
"v_mfma_f32_16x16x32_bf16 a[144:147], a[12:15], a[100:103], a[144:147]\n" \
"v_mfma_f32_16x16x32_bf16 a[160:163], a[16:19], a[96:99], a[160:163]\n" \
"s_add_u32 m0, s74, 36864\n" \
"ds_read_b128 a[8:11], v96 offset:2048\n" \
"v_mfma_f32_16x16x32_bf16 a[160:163], a[20:23], a[100:103], a[160:163]\n" \
"buffer_load_dwordx4 v64, s[36:39], s82 offen offset:0 lds\n" \
"v_mfma_f32_16x16x32_bf16 a[164:167], a[16:19], a[104:107], a[164:167]\n" \
"ds_read_b64_tr_b16 a[76:77], v103 offset:16384\n" \
"v_mfma_f32_16x16x32_bf16 a[164:167], a[20:23], a[108:111], a[164:167]\n" \
"v_mfma_f32_16x16x32_bf16 a[168:171], a[16:19], a[112:115], a[168:171]\n" \
"ds_read_b128 a[12:15], v97 offset:2048\n" \
"v_mfma_f32_16x16x32_bf16 a[168:171], a[20:23], a[116:119], a[168:171]\n" \
"buffer_load_dwordx4 v65, s[36:39], s82 offen offset:1024 lds\n" \
"v_mfma_f32_16x16x32_bf16 a[172:175], a[16:19], a[120:123], a[172:175]\n" \
"ds_read_b64_tr_b16 a[78:79], v103 offset:20480\n" \
"v_mfma_f32_16x16x32_bf16 a[172:175], a[20:23], a[124:127], a[172:175]\n" \
"v_mfma_f32_16x16x32_bf16 a[188:191], a[24:27], a[120:123], a[188:191]\n" \
"ds_read_b128 a[16:19], v96 offset:4096\n" \
"v_mfma_f32_16x16x32_bf16 a[188:191], a[28:31], a[124:127], a[188:191]\n" \
"buffer_load_dwordx4 v66, s[36:39], s82 offen offset:2048 lds\n" \
"v_mfma_f32_16x16x32_bf16 a[184:187], a[24:27], a[112:115], a[184:187]\n" \
"ds_read_b64_tr_b16 a[80:81], v102 offset:512\n" \
"v_mfma_f32_16x16x32_bf16 a[184:187], a[28:31], a[116:119], a[184:187]\n" \
"v_mfma_f32_16x16x32_bf16 a[180:183], a[24:27], a[104:107], a[180:183]\n" \
"ds_read_b128 a[20:23], v97 offset:4096\n" \
"v_mfma_f32_16x16x32_bf16 a[180:183], a[28:31], a[108:111], a[180:183]\n" \
"buffer_load_dwordx4 v67, s[36:39], s82 offen offset:3072 lds\n" \
"v_mfma_f32_16x16x32_bf16 a[176:179], a[24:27], a[96:99], a[176:179]\n" \
"s_mov_b64 s[64:65], s[12:13]\n" \
"s_add_u32 s68, s67, 0x300000\n" \
"ds_read_b64_tr_b16 a[82:83], v102 offset:4608\n" \
"v_mfma_f32_16x16x32_bf16 a[176:179], a[28:31], a[100:103], a[176:179]\n" \
"s_add_u32 s64, s64, s68\n" \
"s_addc_u32 s65, s65, 0\n" \
"v_mfma_f32_16x16x32_bf16 a[192:195], a[32:35], a[96:99], a[192:195]\n" \
"ds_read_b128 a[24:27], v96 offset:6144\n" \
"v_mfma_f32_16x16x32_bf16 a[192:195], a[36:39], a[100:103], a[192:195]\n" \
"global_load_dword v238, v234, s[64:65]\n" \
"v_mfma_f32_16x16x32_bf16 a[196:199], a[32:35], a[104:107], a[196:199]\n" \
"s_mov_b64 s[64:65], s[12:13]\n" \
"s_add_u32 s68, s67, 0x400000\n" \
"ds_read_b64_tr_b16 a[84:85], v102 offset:16896\n" \
"v_mfma_f32_16x16x32_bf16 a[196:199], a[36:39], a[108:111], a[196:199]\n" \
"s_add_u32 s64, s64, s68\n" \
"s_addc_u32 s65, s65, 0\n" \
"v_mfma_f32_16x16x32_bf16 a[200:203], a[32:35], a[112:115], a[200:203]\n" \
"ds_read_b128 a[28:31], v97 offset:6144\n" \
"v_mfma_f32_16x16x32_bf16 a[200:203], a[36:39], a[116:119], a[200:203]\n" \
"global_load_dword v238, v234, s[64:65]\n" \
"v_mfma_f32_16x16x32_bf16 a[204:207], a[32:35], a[120:123], a[204:207]\n" \
"s_mov_b64 s[64:65], s[12:13]\n" \
"s_add_u32 s68, s67, 0x600000\n" \
"ds_read_b64_tr_b16 a[86:87], v102 offset:20992\n" \
"v_mfma_f32_16x16x32_bf16 a[204:207], a[36:39], a[124:127], a[204:207]\n" \
"s_add_u32 s64, s64, s68\n" \
"s_addc_u32 s65, s65, 0\n" \
"v_mfma_f32_16x16x32_bf16 a[220:223], a[40:43], a[120:123], a[220:223]\n" \
"ds_read_b128 a[32:35], v96 offset:8192\n" \
"v_mfma_f32_16x16x32_bf16 a[220:223], a[44:47], a[124:127], a[220:223]\n" \
"global_load_dword v238, v234, s[64:65]\n" \
"v_mfma_f32_16x16x32_bf16 a[216:219], a[40:43], a[112:115], a[216:219]\n" \
"ds_read_b64_tr_b16 a[88:89], v103 offset:512\n" \
"v_mfma_f32_16x16x32_bf16 a[216:219], a[44:47], a[116:119], a[216:219]\n" \
"v_mfma_f32_16x16x32_bf16 a[212:215], a[40:43], a[104:107], a[212:215]\n" \
"ds_read_b128 a[36:39], v97 offset:8192\n" \
"v_mfma_f32_16x16x32_bf16 a[212:215], a[44:47], a[108:111], a[212:215]\n" \
"v_mfma_f32_16x16x32_bf16 a[208:211], a[40:43], a[96:99], a[208:211]\n" \
"ds_read_b64_tr_b16 a[90:91], v103 offset:4608\n" \
"v_mfma_f32_16x16x32_bf16 a[208:211], a[44:47], a[100:103], a[208:211]\n" \
"v_mfma_f32_16x16x32_bf16 a[224:227], a[48:51], a[96:99], a[224:227]\n" \
"ds_read_b128 a[40:43], v96 offset:10240\n" \
"v_mfma_f32_16x16x32_bf16 a[224:227], a[52:55], a[100:103], a[224:227]\n" \
"v_mfma_f32_16x16x32_bf16 a[228:231], a[48:51], a[104:107], a[228:231]\n" \
"ds_read_b64_tr_b16 a[92:93], v103 offset:16896\n" \
"v_mfma_f32_16x16x32_bf16 a[228:231], a[52:55], a[108:111], a[228:231]\n" \
"v_mfma_f32_16x16x32_bf16 a[232:235], a[48:51], a[112:115], a[232:235]\n" \
"ds_read_b128 a[44:47], v97 offset:10240\n" \
"v_mfma_f32_16x16x32_bf16 a[232:235], a[52:55], a[116:119], a[232:235]\n" \
"v_mfma_f32_16x16x32_bf16 a[236:239], a[48:51], a[120:123], a[236:239]\n" \
"ds_read_b64_tr_b16 a[94:95], v103 offset:20992\n" \
"v_mfma_f32_16x16x32_bf16 a[236:239], a[52:55], a[124:127], a[236:239]\n" \
"v_mfma_f32_16x16x32_bf16 a[252:255], a[56:59], a[120:123], a[252:255]\n" \
"ds_read_b128 a[48:51], v96 offset:12288\n" \
"v_mfma_f32_16x16x32_bf16 a[252:255], a[60:63], a[124:127], a[252:255]\n" \
"v_mfma_f32_16x16x32_bf16 a[248:251], a[56:59], a[112:115], a[248:251]\n" \
"v_mfma_f32_16x16x32_bf16 a[248:251], a[60:63], a[116:119], a[248:251]\n" \
"v_mfma_f32_16x16x32_bf16 a[244:247], a[56:59], a[104:107], a[244:247]\n" \
"ds_read_b128 a[52:55], v97 offset:12288\n" \
"v_mfma_f32_16x16x32_bf16 a[244:247], a[60:63], a[108:111], a[244:247]\n" \
"v_mfma_f32_16x16x32_bf16 a[240:243], a[56:59], a[96:99], a[240:243]\n" \
"v_mfma_f32_16x16x32_bf16 a[240:243], a[60:63], a[100:103], a[240:243]\n" \
"s_waitcnt lgkmcnt(1)\n" \
"v_mfma_f32_16x16x32_bf16 v[0:3], a[64:67], a[0:3], v[0:3]\n" \
"ds_read_b128 a[56:59], v96 offset:14336\n" \
"Lpg1663_%=:\n" \
"Lst0x2_%=:\n" \
"s_cbranch_execz Lpg3897_%=\n" \
"v_mfma_f32_16x16x32_bf16 v[0:3], a[68:71], a[4:7], v[0:3]\n" \
"v_mfma_f32_16x16x32_bf16 v[4:7], a[72:75], a[0:3], v[4:7]\n" \
"s_add_u32 m0, s79, 0\n" \
"ds_read_b128 a[60:63], v97 offset:14336\n" \
"v_mfma_f32_16x16x32_bf16 v[4:7], a[76:79], a[4:7], v[4:7]\n" \
"buffer_load_dwordx4 v228, s[52:55], s78 offen offset:0 lds\n" \
"v_mfma_f32_16x16x32_bf16 v[8:11], a[80:83], a[0:3], v[8:11]\n" \
"ds_read_b64_tr_b16 a[96:97], v102 offset:1024\n" \
"v_mfma_f32_16x16x32_bf16 v[8:11], a[84:87], a[4:7], v[8:11]\n" \
"v_mfma_f32_16x16x32_bf16 v[12:15], a[88:91], a[0:3], v[12:15]\n" \
"ds_read_b64_tr_b16 a[98:99], v102 offset:5120\n" \
"v_mfma_f32_16x16x32_bf16 v[12:15], a[92:95], a[4:7], v[12:15]\n" \
"buffer_load_dwordx4 v229, s[52:55], s78 offen offset:1024 lds\n" \
"v_mfma_f32_16x16x32_bf16 v[28:31], a[88:91], a[8:11], v[28:31]\n" \
"ds_read_b64_tr_b16 a[100:101], v102 offset:17408\n" \
"v_mfma_f32_16x16x32_bf16 v[28:31], a[92:95], a[12:15], v[28:31]\n" \
"v_mfma_f32_16x16x32_bf16 v[24:27], a[80:83], a[8:11], v[24:27]\n" \
"s_xor_b32 s78, s78, 0x180000\n" \
"ds_read_b64_tr_b16 a[102:103], v102 offset:21504\n" \
"v_mfma_f32_16x16x32_bf16 v[24:27], a[84:87], a[12:15], v[24:27]\n" \
"buffer_load_dwordx4 v228, s[52:55], s89 offen offset:2048 lds\n" \
"v_mfma_f32_16x16x32_bf16 v[20:23], a[72:75], a[8:11], v[20:23]\n" \
"ds_read_b64_tr_b16 a[104:105], v103 offset:1024\n" \
"v_mfma_f32_16x16x32_bf16 v[20:23], a[76:79], a[12:15], v[20:23]\n" \
"v_mfma_f32_16x16x32_bf16 v[16:19], a[64:67], a[8:11], v[16:19]\n" \
"ds_read_b64_tr_b16 a[106:107], v103 offset:5120\n" \
"v_mfma_f32_16x16x32_bf16 v[16:19], a[68:71], a[12:15], v[16:19]\n" \
"buffer_load_dwordx4 v229, s[52:55], s89 offen offset:3072 lds\n" \
"v_mfma_f32_16x16x32_bf16 v[32:35], a[64:67], a[16:19], v[32:35]\n" \
"ds_read_b64_tr_b16 a[108:109], v103 offset:17408\n" \
"v_mfma_f32_16x16x32_bf16 v[32:35], a[68:71], a[20:23], v[32:35]\n" \
"s_xor_b32 s89, s89, 0x180000\n" \
"v_mfma_f32_16x16x32_bf16 v[36:39], a[72:75], a[16:19], v[36:39]\n" \
"s_add_u32 m0, s79, 16384\n" \
"ds_read_b64_tr_b16 a[110:111], v103 offset:21504\n" \
"v_mfma_f32_16x16x32_bf16 v[36:39], a[76:79], a[20:23], v[36:39]\n" \
"buffer_load_dwordx4 v228, s[52:55], s78 offen offset:0 lds\n" \
"v_mfma_f32_16x16x32_bf16 v[40:43], a[80:83], a[16:19], v[40:43]\n" \
"ds_read_b64_tr_b16 a[112:113], v102 offset:1536\n" \
"v_mfma_f32_16x16x32_bf16 v[40:43], a[84:87], a[20:23], v[40:43]\n" \
"v_mfma_f32_16x16x32_bf16 v[44:47], a[88:91], a[16:19], v[44:47]\n" \
"ds_read_b64_tr_b16 a[114:115], v102 offset:5632\n" \
"v_mfma_f32_16x16x32_bf16 v[44:47], a[92:95], a[20:23], v[44:47]\n" \
"buffer_load_dwordx4 v229, s[52:55], s78 offen offset:1024 lds\n" \
"v_mfma_f32_16x16x32_bf16 v[60:63], a[88:91], a[24:27], v[60:63]\n" \
"ds_read_b64_tr_b16 a[116:117], v102 offset:17920\n" \
"v_mfma_f32_16x16x32_bf16 v[60:63], a[92:95], a[28:31], v[60:63]\n" \
"v_mfma_f32_16x16x32_bf16 v[56:59], a[80:83], a[24:27], v[56:59]\n" \
"s_xor_b32 s78, s78, 0x400000\n" \
"ds_read_b64_tr_b16 a[118:119], v102 offset:22016\n" \
"v_mfma_f32_16x16x32_bf16 v[56:59], a[84:87], a[28:31], v[56:59]\n" \
"buffer_load_dwordx4 v228, s[52:55], s89 offen offset:2048 lds\n" \
"v_mfma_f32_16x16x32_bf16 v[52:55], a[72:75], a[24:27], v[52:55]\n" \
"ds_read_b64_tr_b16 a[120:121], v103 offset:1536\n" \
"v_mfma_f32_16x16x32_bf16 v[52:55], a[76:79], a[28:31], v[52:55]\n" \
"v_mfma_f32_16x16x32_bf16 v[48:51], a[64:67], a[24:27], v[48:51]\n" \
"ds_read_b64_tr_b16 a[122:123], v103 offset:5632\n" \
"v_mfma_f32_16x16x32_bf16 v[48:51], a[68:71], a[28:31], v[48:51]\n" \
"buffer_load_dwordx4 v229, s[52:55], s89 offen offset:3072 lds\n" \
"v_mfma_f32_16x16x32_bf16 v[144:147], a[64:67], a[32:35], v[144:147]\n" \
"ds_read_b64_tr_b16 a[124:125], v103 offset:17920\n" \
"v_mfma_f32_16x16x32_bf16 v[144:147], a[68:71], a[36:39], v[144:147]\n" \
"v_mfma_f32_16x16x32_bf16 v[148:151], a[72:75], a[32:35], v[148:151]\n" \
"s_xor_b32 s89, s89, 0x400000\n" \
"ds_read_b64_tr_b16 a[126:127], v103 offset:22016\n" \
"v_mfma_f32_16x16x32_bf16 v[148:151], a[76:79], a[36:39], v[148:151]\n" \
"v_mfma_f32_16x16x32_bf16 v[152:155], a[80:83], a[32:35], v[152:155]\n" \
"v_mfma_f32_16x16x32_bf16 v[152:155], a[84:87], a[36:39], v[152:155]\n" \
"v_mfma_f32_16x16x32_bf16 v[156:159], a[88:91], a[32:35], v[156:159]\n" \
"v_mfma_f32_16x16x32_bf16 v[156:159], a[92:95], a[36:39], v[156:159]\n" \
"v_mfma_f32_16x16x32_bf16 v[188:191], a[88:91], a[40:43], v[188:191]\n" \
"v_mfma_f32_16x16x32_bf16 v[188:191], a[92:95], a[44:47], v[188:191]\n" \
"v_mfma_f32_16x16x32_bf16 v[184:187], a[80:83], a[40:43], v[184:187]\n" \
"v_mfma_f32_16x16x32_bf16 v[184:187], a[84:87], a[44:47], v[184:187]\n" \
"v_mfma_f32_16x16x32_bf16 v[180:183], a[72:75], a[40:43], v[180:183]\n" \
"v_mfma_f32_16x16x32_bf16 v[180:183], a[76:79], a[44:47], v[180:183]\n" \
"v_mfma_f32_16x16x32_bf16 v[176:179], a[64:67], a[40:43], v[176:179]\n" \
"v_mfma_f32_16x16x32_bf16 v[176:179], a[68:71], a[44:47], v[176:179]\n" \
"s_waitcnt lgkmcnt(0)\n" \
"v_mfma_f32_16x16x32_bf16 v[192:195], a[64:67], a[48:51], v[192:195]\n" \
"v_mfma_f32_16x16x32_bf16 v[192:195], a[68:71], a[52:55], v[192:195]\n" \
"v_mfma_f32_16x16x32_bf16 v[196:199], a[72:75], a[48:51], v[196:199]\n" \
"v_mfma_f32_16x16x32_bf16 v[196:199], a[76:79], a[52:55], v[196:199]\n" \
"v_mfma_f32_16x16x32_bf16 v[200:203], a[80:83], a[48:51], v[200:203]\n" \
"v_mfma_f32_16x16x32_bf16 v[200:203], a[84:87], a[52:55], v[200:203]\n" \
"v_mfma_f32_16x16x32_bf16 v[204:207], a[88:91], a[48:51], v[204:207]\n" \
"v_mfma_f32_16x16x32_bf16 v[204:207], a[92:95], a[52:55], v[204:207]\n" \
"v_mfma_f32_16x16x32_bf16 v[220:223], a[88:91], a[56:59], v[220:223]\n" \
"v_mfma_f32_16x16x32_bf16 v[220:223], a[92:95], a[60:63], v[220:223]\n" \
"v_mfma_f32_16x16x32_bf16 v[216:219], a[80:83], a[56:59], v[216:219]\n" \
"v_mfma_f32_16x16x32_bf16 v[216:219], a[84:87], a[60:63], v[216:219]\n" \
"v_mfma_f32_16x16x32_bf16 v[212:215], a[72:75], a[56:59], v[212:215]\n" \
"v_mfma_f32_16x16x32_bf16 v[212:215], a[76:79], a[60:63], v[212:215]\n" \
"v_mfma_f32_16x16x32_bf16 v[208:211], a[64:67], a[56:59], v[208:211]\n" \
"v_mfma_f32_16x16x32_bf16 v[208:211], a[68:71], a[60:63], v[208:211]\n" \
"s_waitcnt vmcnt(11) lgkmcnt(0)\n" \
"s_barrier\n" \
"v_mfma_f32_16x16x32_bf16 a[128:131], a[0:3], a[96:99], a[128:131]\n" \
"s_add_u32 m0, s74, 131072\n" \
"ds_read_b64_tr_b16 a[64:65], v98 offset:32768\n" \
"v_mfma_f32_16x16x32_bf16 a[128:131], a[4:7], a[100:103], a[128:131]\n" \
"buffer_load_dwordx4 v224, s[48:51], s75 offen offset:0 lds\n" \
"v_mfma_f32_16x16x32_bf16 a[132:135], a[0:3], a[104:107], a[132:135]\n" \
"ds_read_b64_tr_b16 a[66:67], v98 offset:36864\n" \
"v_mfma_f32_16x16x32_bf16 a[132:135], a[4:7], a[108:111], a[132:135]\n" \
"v_mfma_f32_16x16x32_bf16 a[136:139], a[0:3], a[112:115], a[136:139]\n" \
"ds_read_b64_tr_b16 a[68:69], v98 offset:49152\n" \
"v_mfma_f32_16x16x32_bf16 a[136:139], a[4:7], a[116:119], a[136:139]\n" \
"buffer_load_dwordx4 v225, s[48:51], s75 offen offset:1024 lds\n" \
"v_mfma_f32_16x16x32_bf16 a[140:143], a[0:3], a[120:123], a[140:143]\n" \
"ds_read_b64_tr_b16 a[70:71], v98 offset:53248\n" \
"v_mfma_f32_16x16x32_bf16 a[140:143], a[4:7], a[124:127], a[140:143]\n" \
"v_mfma_f32_16x16x32_bf16 a[156:159], a[8:11], a[120:123], a[156:159]\n" \
"ds_read_b128 a[0:3], v72 offset:32768\n" \
"v_mfma_f32_16x16x32_bf16 a[156:159], a[12:15], a[124:127], a[156:159]\n" \
"buffer_load_dwordx4 v226, s[48:51], s75 offen offset:2048 lds\n" \
"v_mfma_f32_16x16x32_bf16 a[152:155], a[8:11], a[112:115], a[152:155]\n" \
"ds_read_b64_tr_b16 a[72:73], v99 offset:32768\n" \
"v_mfma_f32_16x16x32_bf16 a[152:155], a[12:15], a[116:119], a[152:155]\n" \
"v_mfma_f32_16x16x32_bf16 a[148:151], a[8:11], a[104:107], a[148:151]\n" \
"ds_read_b128 a[4:7], v73 offset:32768\n" \
"v_mfma_f32_16x16x32_bf16 a[148:151], a[12:15], a[108:111], a[148:151]\n" \
"buffer_load_dwordx4 v227, s[48:51], s75 offen offset:3072 lds\n" \
"v_mfma_f32_16x16x32_bf16 a[144:147], a[8:11], a[96:99], a[144:147]\n" \
"ds_read_b64_tr_b16 a[74:75], v99 offset:36864\n" \
"v_mfma_f32_16x16x32_bf16 a[144:147], a[12:15], a[100:103], a[144:147]\n" \
"s_xor_b32 s75, s75, 0x180\n" \
"v_mfma_f32_16x16x32_bf16 a[160:163], a[16:19], a[96:99], a[160:163]\n" \
"s_add_u32 m0, s74, 135168\n" \
"ds_read_b128 a[8:11], v72 offset:34816\n" \
"v_mfma_f32_16x16x32_bf16 a[160:163], a[20:23], a[100:103], a[160:163]\n" \
"buffer_load_dwordx4 v224, s[48:51], s77 offen offset:0 lds\n" \
"v_mfma_f32_16x16x32_bf16 a[164:167], a[16:19], a[104:107], a[164:167]\n" \
"ds_read_b64_tr_b16 a[76:77], v99 offset:49152\n" \
"v_mfma_f32_16x16x32_bf16 a[164:167], a[20:23], a[108:111], a[164:167]\n" \
"v_mfma_f32_16x16x32_bf16 a[168:171], a[16:19], a[112:115], a[168:171]\n" \
"ds_read_b128 a[12:15], v73 offset:34816\n" \
"v_mfma_f32_16x16x32_bf16 a[168:171], a[20:23], a[116:119], a[168:171]\n" \
"buffer_load_dwordx4 v225, s[48:51], s77 offen offset:1024 lds\n" \
"v_mfma_f32_16x16x32_bf16 a[172:175], a[16:19], a[120:123], a[172:175]\n" \
"ds_read_b64_tr_b16 a[78:79], v99 offset:53248\n" \
"v_mfma_f32_16x16x32_bf16 a[172:175], a[20:23], a[124:127], a[172:175]\n" \
"v_mfma_f32_16x16x32_bf16 a[188:191], a[24:27], a[120:123], a[188:191]\n" \
"ds_read_b128 a[16:19], v72 offset:36864\n" \
"v_mfma_f32_16x16x32_bf16 a[188:191], a[28:31], a[124:127], a[188:191]\n" \
"buffer_load_dwordx4 v226, s[48:51], s77 offen offset:2048 lds\n" \
"v_mfma_f32_16x16x32_bf16 a[184:187], a[24:27], a[112:115], a[184:187]\n" \
"ds_read_b64_tr_b16 a[80:81], v98 offset:33280\n" \
"v_mfma_f32_16x16x32_bf16 a[184:187], a[28:31], a[116:119], a[184:187]\n" \
"v_mfma_f32_16x16x32_bf16 a[180:183], a[24:27], a[104:107], a[180:183]\n" \
"ds_read_b128 a[20:23], v73 offset:36864\n" \
"v_mfma_f32_16x16x32_bf16 a[180:183], a[28:31], a[108:111], a[180:183]\n" \
"buffer_load_dwordx4 v227, s[48:51], s77 offen offset:3072 lds\n" \
"v_mfma_f32_16x16x32_bf16 a[176:179], a[24:27], a[96:99], a[176:179]\n" \
"s_mov_b64 s[64:65], s[12:13]\n" \
"s_add_u32 s68, s67, 0x700000\n" \
"ds_read_b64_tr_b16 a[82:83], v98 offset:37376\n" \
"v_mfma_f32_16x16x32_bf16 a[176:179], a[28:31], a[100:103], a[176:179]\n" \
"s_add_u32 s64, s64, s68\n" \
"s_addc_u32 s65, s65, 0\n" \
"v_mfma_f32_16x16x32_bf16 a[192:195], a[32:35], a[96:99], a[192:195]\n" \
"s_xor_b32 s77, s77, 0x180\n" \
"ds_read_b128 a[24:27], v72 offset:38912\n" \
"v_mfma_f32_16x16x32_bf16 a[192:195], a[36:39], a[100:103], a[192:195]\n" \
"global_load_dword v238, v234, s[64:65]\n" \
"v_mfma_f32_16x16x32_bf16 a[196:199], a[32:35], a[104:107], a[196:199]\n" \
"ds_read_b64_tr_b16 a[84:85], v98 offset:49664\n" \
"v_mfma_f32_16x16x32_bf16 a[196:199], a[36:39], a[108:111], a[196:199]\n" \
"v_mfma_f32_16x16x32_bf16 a[200:203], a[32:35], a[112:115], a[200:203]\n" \
"s_lshr_b32 s68, s66, 2\n" \
"s_and_b32 s68, s68, 0xffff\n" \
"s_add_u32 s68, s68, 65585152\n" \
"ds_read_b128 a[28:31], v73 offset:38912\n" \
"v_mfma_f32_16x16x32_bf16 a[200:203], a[36:39], a[116:119], a[200:203]\n" \
"buffer_load_dword v238, v242, s[56:59], s68 offen sc1\n" \
"v_mfma_f32_16x16x32_bf16 a[204:207], a[32:35], a[120:123], a[204:207]\n" \
"ds_read_b64_tr_b16 a[86:87], v98 offset:53760\n" \
"v_mfma_f32_16x16x32_bf16 a[204:207], a[36:39], a[124:127], a[204:207]\n" \
"v_mfma_f32_16x16x32_bf16 a[220:223], a[40:43], a[120:123], a[220:223]\n" \
"ds_read_b128 a[32:35], v72 offset:40960\n" \
"v_mfma_f32_16x16x32_bf16 a[220:223], a[44:47], a[124:127], a[220:223]\n" \
"v_mfma_f32_16x16x32_bf16 a[216:219], a[40:43], a[112:115], a[216:219]\n" \
"ds_read_b64_tr_b16 a[88:89], v99 offset:33280\n" \
"v_mfma_f32_16x16x32_bf16 a[216:219], a[44:47], a[116:119], a[216:219]\n" \
"v_mfma_f32_16x16x32_bf16 a[212:215], a[40:43], a[104:107], a[212:215]\n" \
"ds_read_b128 a[36:39], v73 offset:40960\n" \
"v_mfma_f32_16x16x32_bf16 a[212:215], a[44:47], a[108:111], a[212:215]\n" \
"v_mfma_f32_16x16x32_bf16 a[208:211], a[40:43], a[96:99], a[208:211]\n" \
"ds_read_b64_tr_b16 a[90:91], v99 offset:37376\n" \
"v_mfma_f32_16x16x32_bf16 a[208:211], a[44:47], a[100:103], a[208:211]\n" \
"v_mfma_f32_16x16x32_bf16 a[224:227], a[48:51], a[96:99], a[224:227]\n" \
"ds_read_b128 a[40:43], v72 offset:43008\n" \
"v_mfma_f32_16x16x32_bf16 a[224:227], a[52:55], a[100:103], a[224:227]\n" \
"v_mfma_f32_16x16x32_bf16 a[228:231], a[48:51], a[104:107], a[228:231]\n" \
"ds_read_b64_tr_b16 a[92:93], v99 offset:49664\n" \
"v_mfma_f32_16x16x32_bf16 a[228:231], a[52:55], a[108:111], a[228:231]\n" \
"v_mfma_f32_16x16x32_bf16 a[232:235], a[48:51], a[112:115], a[232:235]\n" \
"ds_read_b128 a[44:47], v73 offset:43008\n" \
"v_mfma_f32_16x16x32_bf16 a[232:235], a[52:55], a[116:119], a[232:235]\n" \
"v_mfma_f32_16x16x32_bf16 a[236:239], a[48:51], a[120:123], a[236:239]\n" \
"ds_read_b64_tr_b16 a[94:95], v99 offset:53760\n" \
"v_mfma_f32_16x16x32_bf16 a[236:239], a[52:55], a[124:127], a[236:239]\n" \
"v_mfma_f32_16x16x32_bf16 a[252:255], a[56:59], a[120:123], a[252:255]\n" \
"ds_read_b128 a[48:51], v72 offset:45056\n" \
"v_mfma_f32_16x16x32_bf16 a[252:255], a[60:63], a[124:127], a[252:255]\n" \
"v_mfma_f32_16x16x32_bf16 a[248:251], a[56:59], a[112:115], a[248:251]\n" \
"v_mfma_f32_16x16x32_bf16 a[248:251], a[60:63], a[116:119], a[248:251]\n" \
"v_mfma_f32_16x16x32_bf16 a[244:247], a[56:59], a[104:107], a[244:247]\n" \
"ds_read_b128 a[52:55], v73 offset:45056\n" \
"v_mfma_f32_16x16x32_bf16 a[244:247], a[60:63], a[108:111], a[244:247]\n" \
"v_mfma_f32_16x16x32_bf16 a[240:243], a[56:59], a[96:99], a[240:243]\n" \
"v_mfma_f32_16x16x32_bf16 a[240:243], a[60:63], a[100:103], a[240:243]\n" \
"s_waitcnt lgkmcnt(1)\n" \
"v_mfma_f32_16x16x32_bf16 v[0:3], a[64:67], a[0:3], v[0:3]\n" \
"ds_read_b128 a[56:59], v72 offset:47104\n" \
"v_mfma_f32_16x16x32_bf16 v[0:3], a[68:71], a[4:7], v[0:3]\n" \
"v_mfma_f32_16x16x32_bf16 v[4:7], a[72:75], a[0:3], v[4:7]\n" \
"s_add_u32 m0, s74, 65536\n" \
"ds_read_b128 a[60:63], v73 offset:47104\n" \
"v_mfma_f32_16x16x32_bf16 v[4:7], a[76:79], a[4:7], v[4:7]\n" \
"buffer_load_dwordx4 v224, s[48:51], s75 offen offset:0 lds\n" \
"v_mfma_f32_16x16x32_bf16 v[8:11], a[80:83], a[0:3], v[8:11]\n" \
"ds_read_b64_tr_b16 a[96:97], v98 offset:33792\n" \
"v_mfma_f32_16x16x32_bf16 v[8:11], a[84:87], a[4:7], v[8:11]\n" \
"v_mfma_f32_16x16x32_bf16 v[12:15], a[88:91], a[0:3], v[12:15]\n" \
"ds_read_b64_tr_b16 a[98:99], v98 offset:37888\n" \
"v_mfma_f32_16x16x32_bf16 v[12:15], a[92:95], a[4:7], v[12:15]\n" \
"buffer_load_dwordx4 v225, s[48:51], s75 offen offset:1024 lds\n" \
"v_mfma_f32_16x16x32_bf16 v[28:31], a[88:91], a[8:11], v[28:31]\n" \
"ds_read_b64_tr_b16 a[100:101], v98 offset:50176\n" \
"v_mfma_f32_16x16x32_bf16 v[28:31], a[92:95], a[12:15], v[28:31]\n" \
"v_mfma_f32_16x16x32_bf16 v[24:27], a[80:83], a[8:11], v[24:27]\n" \
"ds_read_b64_tr_b16 a[102:103], v98 offset:54272\n" \
"v_mfma_f32_16x16x32_bf16 v[24:27], a[84:87], a[12:15], v[24:27]\n" \
"buffer_load_dwordx4 v226, s[48:51], s75 offen offset:2048 lds\n" \
"v_mfma_f32_16x16x32_bf16 v[20:23], a[72:75], a[8:11], v[20:23]\n" \
"ds_read_b64_tr_b16 a[104:105], v99 offset:33792\n" \
"v_mfma_f32_16x16x32_bf16 v[20:23], a[76:79], a[12:15], v[20:23]\n" \
"v_mfma_f32_16x16x32_bf16 v[16:19], a[64:67], a[8:11], v[16:19]\n" \
"ds_read_b64_tr_b16 a[106:107], v99 offset:37888\n" \
"v_mfma_f32_16x16x32_bf16 v[16:19], a[68:71], a[12:15], v[16:19]\n" \
"buffer_load_dwordx4 v227, s[48:51], s75 offen offset:3072 lds\n" \
"v_mfma_f32_16x16x32_bf16 v[32:35], a[64:67], a[16:19], v[32:35]\n" \
"ds_read_b64_tr_b16 a[108:109], v99 offset:50176\n" \
"v_mfma_f32_16x16x32_bf16 v[32:35], a[68:71], a[20:23], v[32:35]\n" \
"s_xor_b32 s75, s75, 0x400\n" \
"v_mfma_f32_16x16x32_bf16 v[36:39], a[72:75], a[16:19], v[36:39]\n" \
"s_add_u32 m0, s74, 69632\n" \
"ds_read_b64_tr_b16 a[110:111], v99 offset:54272\n" \
"v_mfma_f32_16x16x32_bf16 v[36:39], a[76:79], a[20:23], v[36:39]\n" \
"buffer_load_dwordx4 v224, s[48:51], s77 offen offset:0 lds\n" \
"v_mfma_f32_16x16x32_bf16 v[40:43], a[80:83], a[16:19], v[40:43]\n" \
"ds_read_b64_tr_b16 a[112:113], v98 offset:34304\n" \
"v_mfma_f32_16x16x32_bf16 v[40:43], a[84:87], a[20:23], v[40:43]\n" \
"v_mfma_f32_16x16x32_bf16 v[44:47], a[88:91], a[16:19], v[44:47]\n" \
"ds_read_b64_tr_b16 a[114:115], v98 offset:38400\n" \
"v_mfma_f32_16x16x32_bf16 v[44:47], a[92:95], a[20:23], v[44:47]\n" \
"buffer_load_dwordx4 v225, s[48:51], s77 offen offset:1024 lds\n" \
"v_mfma_f32_16x16x32_bf16 v[60:63], a[88:91], a[24:27], v[60:63]\n" \
"ds_read_b64_tr_b16 a[116:117], v98 offset:50688\n" \
"v_mfma_f32_16x16x32_bf16 v[60:63], a[92:95], a[28:31], v[60:63]\n" \
"v_mfma_f32_16x16x32_bf16 v[56:59], a[80:83], a[24:27], v[56:59]\n" \
"ds_read_b64_tr_b16 a[118:119], v98 offset:54784\n" \
"v_mfma_f32_16x16x32_bf16 v[56:59], a[84:87], a[28:31], v[56:59]\n" \
"buffer_load_dwordx4 v226, s[48:51], s77 offen offset:2048 lds\n" \
"v_mfma_f32_16x16x32_bf16 v[52:55], a[72:75], a[24:27], v[52:55]\n" \
"ds_read_b64_tr_b16 a[120:121], v99 offset:34304\n" \
"v_mfma_f32_16x16x32_bf16 v[52:55], a[76:79], a[28:31], v[52:55]\n" \
"v_mfma_f32_16x16x32_bf16 v[48:51], a[64:67], a[24:27], v[48:51]\n" \
"ds_read_b64_tr_b16 a[122:123], v99 offset:38400\n" \
"v_mfma_f32_16x16x32_bf16 v[48:51], a[68:71], a[28:31], v[48:51]\n" \
"buffer_load_dwordx4 v227, s[48:51], s77 offen offset:3072 lds\n" \
"v_mfma_f32_16x16x32_bf16 v[144:147], a[64:67], a[32:35], v[144:147]\n" \
"ds_read_b64_tr_b16 a[124:125], v99 offset:50688\n" \
"v_mfma_f32_16x16x32_bf16 v[144:147], a[68:71], a[36:39], v[144:147]\n" \
"v_mfma_f32_16x16x32_bf16 v[148:151], a[72:75], a[32:35], v[148:151]\n" \
"s_xor_b32 s77, s77, 0x400\n" \
"ds_read_b64_tr_b16 a[126:127], v99 offset:54784\n" \
"v_mfma_f32_16x16x32_bf16 v[148:151], a[76:79], a[36:39], v[148:151]\n" \
"v_mfma_f32_16x16x32_bf16 v[152:155], a[80:83], a[32:35], v[152:155]\n" \
"v_mfma_f32_16x16x32_bf16 v[152:155], a[84:87], a[36:39], v[152:155]\n" \
"v_mfma_f32_16x16x32_bf16 v[156:159], a[88:91], a[32:35], v[156:159]\n" \
"v_mfma_f32_16x16x32_bf16 v[156:159], a[92:95], a[36:39], v[156:159]\n" \
"v_mfma_f32_16x16x32_bf16 v[188:191], a[88:91], a[40:43], v[188:191]\n" \
"v_mfma_f32_16x16x32_bf16 v[188:191], a[92:95], a[44:47], v[188:191]\n" \
"v_mfma_f32_16x16x32_bf16 v[184:187], a[80:83], a[40:43], v[184:187]\n" \
"v_mfma_f32_16x16x32_bf16 v[184:187], a[84:87], a[44:47], v[184:187]\n" \
"v_mfma_f32_16x16x32_bf16 v[180:183], a[72:75], a[40:43], v[180:183]\n" \
"v_mfma_f32_16x16x32_bf16 v[180:183], a[76:79], a[44:47], v[180:183]\n" \
"v_mfma_f32_16x16x32_bf16 v[176:179], a[64:67], a[40:43], v[176:179]\n" \
"v_mfma_f32_16x16x32_bf16 v[176:179], a[68:71], a[44:47], v[176:179]\n" \
"s_waitcnt lgkmcnt(0)\n" \
"v_mfma_f32_16x16x32_bf16 v[192:195], a[64:67], a[48:51], v[192:195]\n" \
"v_mfma_f32_16x16x32_bf16 v[192:195], a[68:71], a[52:55], v[192:195]\n" \
"v_mfma_f32_16x16x32_bf16 v[196:199], a[72:75], a[48:51], v[196:199]\n" \
"v_mfma_f32_16x16x32_bf16 v[196:199], a[76:79], a[52:55], v[196:199]\n" \
"v_mfma_f32_16x16x32_bf16 v[200:203], a[80:83], a[48:51], v[200:203]\n" \
"v_mfma_f32_16x16x32_bf16 v[200:203], a[84:87], a[52:55], v[200:203]\n" \
"v_mfma_f32_16x16x32_bf16 v[204:207], a[88:91], a[48:51], v[204:207]\n" \
"v_mfma_f32_16x16x32_bf16 v[204:207], a[92:95], a[52:55], v[204:207]\n" \
"v_mfma_f32_16x16x32_bf16 v[220:223], a[88:91], a[56:59], v[220:223]\n" \
"v_mfma_f32_16x16x32_bf16 v[220:223], a[92:95], a[60:63], v[220:223]\n" \
"v_mfma_f32_16x16x32_bf16 v[216:219], a[80:83], a[56:59], v[216:219]\n" \
"v_mfma_f32_16x16x32_bf16 v[216:219], a[84:87], a[60:63], v[216:219]\n" \
"v_mfma_f32_16x16x32_bf16 v[212:215], a[72:75], a[56:59], v[212:215]\n" \
"v_mfma_f32_16x16x32_bf16 v[212:215], a[76:79], a[60:63], v[212:215]\n" \
"v_mfma_f32_16x16x32_bf16 v[208:211], a[64:67], a[56:59], v[208:211]\n" \
"v_mfma_f32_16x16x32_bf16 v[208:211], a[68:71], a[60:63], v[208:211]\n" \
"s_waitcnt vmcnt(10) lgkmcnt(0)\n" \
"s_barrier\n" \
"v_mfma_f32_16x16x32_bf16 a[128:131], a[0:3], a[96:99], a[128:131]\n" \
"s_add_u32 m0, s74, 98304\n" \
"v_mfma_f32_16x16x32_bf16 a[128:131], a[4:7], a[100:103], a[128:131]\n" \
"buffer_load_dwordx4 v224, s[48:51], s75 offen offset:0 lds\n" \
"v_mfma_f32_16x16x32_bf16 a[132:135], a[0:3], a[104:107], a[132:135]\n" \
"ds_read_b64_tr_b16 a[64:65], v232 offset:0\n" \
"ds_read_b64_tr_b16 a[66:67], v232 offset:2048\n" \
"v_mfma_f32_16x16x32_bf16 a[132:135], a[4:7], a[108:111], a[132:135]\n" \
"v_mfma_f32_16x16x32_bf16 a[136:139], a[0:3], a[112:115], a[136:139]\n" \
"v_mfma_f32_16x16x32_bf16 a[136:139], a[4:7], a[116:119], a[136:139]\n" \
"buffer_load_dwordx4 v225, s[48:51], s75 offen offset:1024 lds\n" \
"v_mfma_f32_16x16x32_bf16 a[140:143], a[0:3], a[120:123], a[140:143]\n" \
"ds_read_b64_tr_b16 a[68:69], v232 offset:8192\n" \
"ds_read_b64_tr_b16 a[70:71], v232 offset:10240\n" \
"v_mfma_f32_16x16x32_bf16 a[140:143], a[4:7], a[124:127], a[140:143]\n" \
"v_mfma_f32_16x16x32_bf16 a[156:159], a[8:11], a[120:123], a[156:159]\n" \
"v_mul_f32 v192, s76, v192\n" \
"v_mul_f32 v193, s76, v193\n" \
"v_mfma_f32_16x16x32_bf16 a[156:159], a[12:15], a[124:127], a[156:159]\n" \
"buffer_load_dwordx4 v226, s[48:51], s75 offen offset:2048 lds\n" \
"v_mfma_f32_16x16x32_bf16 a[152:155], a[8:11], a[112:115], a[152:155]\n" \
"ds_read_b64_tr_b16 a[72:73], v233 offset:0\n" \
"ds_read_b64_tr_b16 a[74:75], v233 offset:2048\n" \
"v_mfma_f32_16x16x32_bf16 a[152:155], a[12:15], a[116:119], a[152:155]\n" \
"v_mul_f32 v194, s76, v194\n" \
"v_mul_f32 v195, s76, v195\n" \
"v_mfma_f32_16x16x32_bf16 a[148:151], a[8:11], a[104:107], a[148:151]\n" \
"v_cvt_pk_bf16_f32 v192, v192, v193\n" \
"v_cvt_pk_bf16_f32 v193, v194, v195\n" \
"v_mfma_f32_16x16x32_bf16 a[148:151], a[12:15], a[108:111], a[148:151]\n" \
"buffer_load_dwordx4 v227, s[48:51], s75 offen offset:3072 lds\n" \
"v_mfma_f32_16x16x32_bf16 a[144:147], a[8:11], a[96:99], a[144:147]\n" \
"ds_read_b64_tr_b16 a[76:77], v233 offset:8192\n" \
"ds_read_b64_tr_b16 a[78:79], v233 offset:10240\n" \
"v_mfma_f32_16x16x32_bf16 a[144:147], a[12:15], a[100:103], a[144:147]\n" \
"s_xor_b32 s75, s75, 0x500\n" \
"v_mul_f32 v196, s76, v196\n" \
"v_mul_f32 v197, s76, v197\n" \
"v_mfma_f32_16x16x32_bf16 a[160:163], a[16:19], a[96:99], a[160:163]\n" \
"s_add_u32 m0, s74, 102400\n" \
"v_mul_f32 v198, s76, v198\n" \
"v_mul_f32 v199, s76, v199\n" \
"v_mfma_f32_16x16x32_bf16 a[160:163], a[20:23], a[100:103], a[160:163]\n" \
"buffer_load_dwordx4 v224, s[48:51], s77 offen offset:0 lds\n" \
"v_mfma_f32_16x16x32_bf16 a[164:167], a[16:19], a[104:107], a[164:167]\n" \
"ds_read_b64_tr_b16 a[80:81], v232 offset:512\n" \
"ds_read_b64_tr_b16 a[82:83], v232 offset:2560\n" \
"v_mfma_f32_16x16x32_bf16 a[164:167], a[20:23], a[108:111], a[164:167]\n" \
"v_cvt_pk_bf16_f32 v194, v196, v197\n" \
"v_cvt_pk_bf16_f32 v195, v198, v199\n" \
"v_mfma_f32_16x16x32_bf16 a[168:171], a[16:19], a[112:115], a[168:171]\n" \
"v_mul_f32 v208, s76, v208\n" \
"v_mul_f32 v209, s76, v209\n" \
"v_mfma_f32_16x16x32_bf16 a[168:171], a[20:23], a[116:119], a[168:171]\n" \
"buffer_load_dwordx4 v225, s[48:51], s77 offen offset:1024 lds\n" \
"v_mfma_f32_16x16x32_bf16 a[172:175], a[16:19], a[120:123], a[172:175]\n" \
"ds_read_b64_tr_b16 a[84:85], v232 offset:8704\n" \
"ds_read_b64_tr_b16 a[86:87], v232 offset:10752\n" \
"v_mfma_f32_16x16x32_bf16 a[172:175], a[20:23], a[124:127], a[172:175]\n" \
"v_mul_f32 v210, s76, v210\n" \
"v_mul_f32 v211, s76, v211\n" \
"v_mfma_f32_16x16x32_bf16 a[188:191], a[24:27], a[120:123], a[188:191]\n" \
"v_cvt_pk_bf16_f32 v208, v208, v209\n" \
"v_cvt_pk_bf16_f32 v209, v210, v211\n" \
"v_mfma_f32_16x16x32_bf16 a[188:191], a[28:31], a[124:127], a[188:191]\n" \
"buffer_load_dwordx4 v226, s[48:51], s77 offen offset:2048 lds\n" \
"v_mfma_f32_16x16x32_bf16 a[184:187], a[24:27], a[112:115], a[184:187]\n" \
"ds_read_b64_tr_b16 a[88:89], v233 offset:512\n" \
"ds_read_b64_tr_b16 a[90:91], v233 offset:2560\n" \
"v_mfma_f32_16x16x32_bf16 a[184:187], a[28:31], a[116:119], a[184:187]\n" \
"v_mul_f32 v212, s76, v212\n" \
"v_mul_f32 v213, s76, v213\n" \
"v_mfma_f32_16x16x32_bf16 a[180:183], a[24:27], a[104:107], a[180:183]\n" \
"v_mul_f32 v214, s76, v214\n" \
"v_mul_f32 v215, s76, v215\n" \
"v_mfma_f32_16x16x32_bf16 a[180:183], a[28:31], a[108:111], a[180:183]\n" \
"buffer_load_dwordx4 v227, s[48:51], s77 offen offset:3072 lds\n" \
"v_mfma_f32_16x16x32_bf16 a[176:179], a[24:27], a[96:99], a[176:179]\n" \
"ds_read_b64_tr_b16 a[92:93], v233 offset:8704\n" \
"ds_read_b64_tr_b16 a[94:95], v233 offset:10752\n" \
"v_mfma_f32_16x16x32_bf16 a[176:179], a[28:31], a[100:103], a[176:179]\n" \
"s_xor_b32 s77, s77, 0x500\n" \
"v_cvt_pk_bf16_f32 v210, v212, v213\n" \
"v_cvt_pk_bf16_f32 v211, v214, v215\n" \
"v_mfma_f32_16x16x32_bf16 a[192:195], a[32:35], a[96:99], a[192:195]\n" \
"s_add_u32 m0, s79, 32768\n" \
"v_mul_f32 v200, s76, v200\n" \
"v_mul_f32 v201, s76, v201\n" \
"v_mfma_f32_16x16x32_bf16 a[192:195], a[36:39], a[100:103], a[192:195]\n" \
"buffer_load_dwordx4 v228, s[52:55], s78 offen offset:0 lds\n" \
"v_mfma_f32_16x16x32_bf16 a[196:199], a[32:35], a[104:107], a[196:199]\n" \
"ds_read_b128 a[0:3], v250 offset:0\n" \
"ds_read_b128 a[4:7], v251 offset:0\n" \
"v_mfma_f32_16x16x32_bf16 a[196:199], a[36:39], a[108:111], a[196:199]\n" \
"v_mul_f32 v202, s76, v202\n" \
"v_mul_f32 v203, s76, v203\n" \
"v_mfma_f32_16x16x32_bf16 a[200:203], a[32:35], a[112:115], a[200:203]\n" \
"v_cvt_pk_bf16_f32 v200, v200, v201\n" \
"v_cvt_pk_bf16_f32 v201, v202, v203\n" \
"v_mfma_f32_16x16x32_bf16 a[200:203], a[36:39], a[116:119], a[200:203]\n" \
"buffer_load_dwordx4 v229, s[52:55], s78 offen offset:1024 lds\n" \
"v_mfma_f32_16x16x32_bf16 a[204:207], a[32:35], a[120:123], a[204:207]\n" \
"ds_read_b128 a[8:11], v250 offset:2048\n" \
"ds_read_b128 a[12:15], v251 offset:2048\n" \
"v_mfma_f32_16x16x32_bf16 a[204:207], a[36:39], a[124:127], a[204:207]\n" \
"v_mul_f32 v204, s76, v204\n" \
"v_mul_f32 v205, s76, v205\n" \
"v_mfma_f32_16x16x32_bf16 a[220:223], a[40:43], a[120:123], a[220:223]\n" \
"s_xor_b32 s78, s78, 0x500000\n" \
"v_mul_f32 v206, s76, v206\n" \
"v_mul_f32 v207, s76, v207\n" \
"v_mfma_f32_16x16x32_bf16 a[220:223], a[44:47], a[124:127], a[220:223]\n" \
"buffer_load_dwordx4 v228, s[52:55], s89 offen offset:2048 lds\n" \
"v_mfma_f32_16x16x32_bf16 a[216:219], a[40:43], a[112:115], a[216:219]\n" \
"ds_read_b128 a[16:19], v250 offset:4096\n" \
"ds_read_b128 a[20:23], v251 offset:4096\n" \
"v_mfma_f32_16x16x32_bf16 a[216:219], a[44:47], a[116:119], a[216:219]\n" \
"v_cvt_pk_bf16_f32 v202, v204, v205\n" \
"v_cvt_pk_bf16_f32 v203, v206, v207\n" \
"v_mfma_f32_16x16x32_bf16 a[212:215], a[40:43], a[104:107], a[212:215]\n" \
"v_mul_f32 v216, s76, v216\n" \
"v_mul_f32 v217, s76, v217\n" \
"v_mfma_f32_16x16x32_bf16 a[212:215], a[44:47], a[108:111], a[212:215]\n" \
"buffer_load_dwordx4 v229, s[52:55], s89 offen offset:3072 lds\n" \
"v_mfma_f32_16x16x32_bf16 a[208:211], a[40:43], a[96:99], a[208:211]\n" \
"v_mul_f32 v218, s76, v218\n" \
"v_mul_f32 v219, s76, v219\n" \
"v_mfma_f32_16x16x32_bf16 a[208:211], a[44:47], a[100:103], a[208:211]\n" \
"s_xor_b32 s89, s89, 0x500000\n" \
"v_cvt_pk_bf16_f32 v216, v216, v217\n" \
"v_cvt_pk_bf16_f32 v217, v218, v219\n" \
"v_mfma_f32_16x16x32_bf16 a[224:227], a[48:51], a[96:99], a[224:227]\n" \
"s_add_u32 m0, s79, 49152\n" \
"v_mul_f32 v220, s76, v220\n" \
"v_mul_f32 v221, s76, v221\n" \
"v_mfma_f32_16x16x32_bf16 a[224:227], a[52:55], a[100:103], a[224:227]\n" \
"buffer_load_dwordx4 v228, s[52:55], s78 offen offset:0 lds\n" \
"v_mfma_f32_16x16x32_bf16 a[228:231], a[48:51], a[104:107], a[228:231]\n" \
"v_mul_f32 v222, s76, v222\n" \
"v_mul_f32 v223, s76, v223\n" \
"v_mfma_f32_16x16x32_bf16 a[228:231], a[52:55], a[108:111], a[228:231]\n" \
"v_cvt_pk_bf16_f32 v218, v220, v221\n" \
"v_cvt_pk_bf16_f32 v219, v222, v223\n" \
"v_mfma_f32_16x16x32_bf16 a[232:235], a[48:51], a[112:115], a[232:235]\n" \
"v_mul_f32 v144, s76, v144\n" \
"v_mul_f32 v145, s76, v145\n" \
"v_mfma_f32_16x16x32_bf16 a[232:235], a[52:55], a[116:119], a[232:235]\n" \
"buffer_load_dwordx4 v229, s[52:55], s78 offen offset:1024 lds\n" \
"v_mfma_f32_16x16x32_bf16 a[236:239], a[48:51], a[120:123], a[236:239]\n" \
"v_mul_f32 v146, s76, v146\n" \
"v_mul_f32 v147, s76, v147\n" \
"v_mfma_f32_16x16x32_bf16 a[236:239], a[52:55], a[124:127], a[236:239]\n" \
"v_cvt_pk_bf16_f32 v196, v144, v145\n" \
"v_cvt_pk_bf16_f32 v197, v146, v147\n" \
"v_mfma_f32_16x16x32_bf16 a[252:255], a[56:59], a[120:123], a[252:255]\n" \
"s_xor_b32 s78, s78, 0x180000\n" \
"v_mul_f32 v148, s76, v148\n" \
"v_mul_f32 v149, s76, v149\n" \
"v_mfma_f32_16x16x32_bf16 a[252:255], a[60:63], a[124:127], a[252:255]\n" \
"buffer_load_dwordx4 v228, s[52:55], s89 offen offset:2048 lds\n" \
"v_mfma_f32_16x16x32_bf16 a[248:251], a[56:59], a[112:115], a[248:251]\n" \
"v_mul_f32 v150, s76, v150\n" \
"v_mul_f32 v151, s76, v151\n" \
"v_mfma_f32_16x16x32_bf16 a[248:251], a[60:63], a[116:119], a[248:251]\n" \
"v_cvt_pk_bf16_f32 v198, v148, v149\n" \
"v_cvt_pk_bf16_f32 v199, v150, v151\n" \
"v_mfma_f32_16x16x32_bf16 a[244:247], a[56:59], a[104:107], a[244:247]\n" \
"v_mul_f32 v176, s76, v176\n" \
"v_mul_f32 v177, s76, v177\n" \
"v_mfma_f32_16x16x32_bf16 a[244:247], a[60:63], a[108:111], a[244:247]\n" \
"buffer_load_dwordx4 v229, s[52:55], s89 offen offset:3072 lds\n" \
"v_mfma_f32_16x16x32_bf16 a[240:243], a[56:59], a[96:99], a[240:243]\n" \
"s_waitcnt lgkmcnt(0)\n" \
"v_mul_f32 v178, s76, v178\n" \
"v_mul_f32 v179, s76, v179\n" \
"v_mfma_f32_16x16x32_bf16 a[240:243], a[60:63], a[100:103], a[240:243]\n" \
"s_xor_b32 s89, s89, 0x180000\n" \
"v_cvt_pk_bf16_f32 v212, v176, v177\n" \
"v_cvt_pk_bf16_f32 v213, v178, v179\n" \
"v_mfma_f32_16x16x32_bf16 v[64:67], a[64:67], a[0:3], 0\n" \
"ds_read_b128 a[24:27], v250 offset:6144\n" \
"v_mul_f32 v180, s76, v180\n" \
"v_mfma_f32_16x16x32_bf16 v[64:67], a[68:71], a[4:7], v[64:67]\n" \
"s_add_u32 m0, s74, 131072\n" \
"ds_read_b128 a[28:31], v251 offset:6144\n" \
"v_mul_f32 v181, s76, v181\n" \
"v_mfma_f32_16x16x32_bf16 v[68:71], a[72:75], a[0:3], 0\n" \
"ds_read_b64_tr_b16 a[96:97], v232 offset:1024\n" \
"v_mul_f32 v182, s76, v182\n" \
"v_mfma_f32_16x16x32_bf16 v[68:71], a[76:79], a[4:7], v[68:71]\n" \
"buffer_load_dwordx4 v224, s[48:51], s75 offen offset:0 lds\n" \
"v_mfma_f32_16x16x32_bf16 v[72:75], a[80:83], a[0:3], 0\n" \
"ds_read_b64_tr_b16 a[98:99], v232 offset:3072\n" \
"v_mul_f32 v183, s76, v183\n" \
"v_mfma_f32_16x16x32_bf16 v[72:75], a[84:87], a[4:7], v[72:75]\n" \
"ds_read_b64_tr_b16 a[100:101], v232 offset:9216\n" \
"v_cvt_pk_bf16_f32 v214, v180, v181\n" \
"v_mfma_f32_16x16x32_bf16 v[76:79], a[88:91], a[0:3], 0\n" \
"ds_read_b64_tr_b16 a[102:103], v232 offset:11264\n" \
"v_cvt_pk_bf16_f32 v215, v182, v183\n" \
"v_mfma_f32_16x16x32_bf16 v[76:79], a[92:95], a[4:7], v[76:79]\n" \
"buffer_load_dwordx4 v225, s[48:51], s75 offen offset:1024 lds\n" \
"v_mfma_f32_16x16x32_bf16 v[108:111], a[88:91], a[8:11], 0\n" \
"ds_read_b64_tr_b16 a[104:105], v233 offset:1024\n" \
"v_mul_f32 v152, s76, v152\n" \
"v_mfma_f32_16x16x32_bf16 v[108:111], a[92:95], a[12:15], v[108:111]\n" \
"ds_read_b64_tr_b16 a[106:107], v233 offset:3072\n" \
"v_mul_f32 v153, s76, v153\n" \
"v_mfma_f32_16x16x32_bf16 v[104:107], a[80:83], a[8:11], 0\n" \
"ds_read_b64_tr_b16 a[108:109], v233 offset:9216\n" \
"v_mul_f32 v154, s76, v154\n" \
"v_mfma_f32_16x16x32_bf16 v[104:107], a[84:87], a[12:15], v[104:107]\n" \
"buffer_load_dwordx4 v226, s[48:51], s75 offen offset:2048 lds\n" \
"v_mfma_f32_16x16x32_bf16 v[100:103], a[72:75], a[8:11], 0\n" \
"ds_read_b64_tr_b16 a[110:111], v233 offset:11264\n" \
"v_mul_f32 v155, s76, v155\n" \
"v_mfma_f32_16x16x32_bf16 v[100:103], a[76:79], a[12:15], v[100:103]\n" \
"ds_read_b64_tr_b16 a[112:113], v232 offset:1536\n" \
"v_cvt_pk_bf16_f32 v204, v152, v153\n" \
"v_mfma_f32_16x16x32_bf16 v[96:99], a[64:67], a[8:11], 0\n" \
"ds_read_b64_tr_b16 a[114:115], v232 offset:3584\n" \
"v_cvt_pk_bf16_f32 v205, v154, v155\n" \
"v_mfma_f32_16x16x32_bf16 v[96:99], a[68:71], a[12:15], v[96:99]\n" \
"buffer_load_dwordx4 v227, s[48:51], s75 offen offset:3072 lds\n" \
"v_mfma_f32_16x16x32_bf16 v[128:131], a[64:67], a[16:19], 0\n" \
"s_xor_b32 s75, s75, 0x180\n" \
"ds_read_b64_tr_b16 a[116:117], v232 offset:9728\n" \
"v_mul_f32 v156, s76, v156\n" \
"v_mfma_f32_16x16x32_bf16 v[128:131], a[68:71], a[20:23], v[128:131]\n" \
"s_add_u32 m0, s74, 135168\n" \
"ds_read_b64_tr_b16 a[118:119], v232 offset:11776\n" \
"v_mul_f32 v157, s76, v157\n" \
"v_mfma_f32_16x16x32_bf16 v[132:135], a[72:75], a[16:19], 0\n" \
"ds_read_b64_tr_b16 a[120:121], v233 offset:1536\n" \
"v_mul_f32 v158, s76, v158\n" \
"Lpg2209_%=:\n" \
"Lst0x3_%=:\n" \
"s_cbranch_execz Lpg4446_%=\n" \
"v_mfma_f32_16x16x32_bf16 v[132:135], a[76:79], a[20:23], v[132:135]\n" \
"buffer_load_dwordx4 v224, s[48:51], s77 offen offset:0 lds\n" \
"v_mfma_f32_16x16x32_bf16 v[136:139], a[80:83], a[16:19], 0\n" \
"ds_read_b64_tr_b16 a[122:123], v233 offset:3584\n" \
"v_mul_f32 v159, s76, v159\n" \
"v_mfma_f32_16x16x32_bf16 v[136:139], a[84:87], a[20:23], v[136:139]\n" \
"ds_read_b64_tr_b16 a[124:125], v233 offset:9728\n" \
"v_cvt_pk_bf16_f32 v206, v156, v157\n" \
"v_mfma_f32_16x16x32_bf16 v[140:143], a[88:91], a[16:19], 0\n" \
"s_waitcnt lgkmcnt(3)\n" \
"ds_read_b64_tr_b16 a[126:127], v233 offset:11776\n" \
"v_cvt_pk_bf16_f32 v207, v158, v159\n" \
"v_mfma_f32_16x16x32_bf16 v[140:143], a[92:95], a[20:23], v[140:143]\n" \
"buffer_load_dwordx4 v225, s[48:51], s77 offen offset:1024 lds\n" \
"v_mfma_f32_16x16x32_bf16 v[172:175], a[88:91], a[24:27], 0\n" \
"s_add_u32 s93, s87, 8192\n" \
"v_mul_f32 v184, s76, v184\n" \
"v_mul_f32 v185, s76, v185\n" \
"v_mfma_f32_16x16x32_bf16 v[172:175], a[92:95], a[28:31], v[172:175]\n" \
"buffer_store_dwordx4 v[192:195], v236, s[56:59], s93 offen offset:0 sc1\n" \
"v_mfma_f32_16x16x32_bf16 v[168:171], a[80:83], a[24:27], 0\n" \
"v_mul_f32 v186, s76, v186\n" \
"v_mul_f32 v187, s76, v187\n" \
"v_mfma_f32_16x16x32_bf16 v[168:171], a[84:87], a[28:31], v[168:171]\n" \
"buffer_load_dwordx4 v226, s[48:51], s77 offen offset:2048 lds\n" \
"v_mfma_f32_16x16x32_bf16 v[164:167], a[72:75], a[24:27], 0\n" \
"v_cvt_pk_bf16_f32 v220, v184, v185\n" \
"v_cvt_pk_bf16_f32 v221, v186, v187\n" \
"v_mfma_f32_16x16x32_bf16 v[164:167], a[76:79], a[28:31], v[164:167]\n" \
"buffer_load_dwordx4 v227, s[48:51], s77 offen offset:3072 lds\n" \
"v_mfma_f32_16x16x32_bf16 v[160:163], a[64:67], a[24:27], 0\n" \
"v_mul_f32 v188, s76, v188\n" \
"v_mul_f32 v189, s76, v189\n" \
"v_mfma_f32_16x16x32_bf16 v[160:163], a[68:71], a[28:31], v[160:163]\n" \
"s_waitcnt vmcnt(43) lgkmcnt(0)\n" \
"s_barrier\n" \
"v_mfma_f32_16x16x32_bf16 v[80:83], a[96:99], a[0:3], 0\n" \
"s_xor_b32 s77, s77, 0x180\n" \
"ds_read_b64_tr_b16 a[64:65], v232 offset:16384\n" \
"v_mul_f32 v190, s76, v190\n" \
"v_mfma_f32_16x16x32_bf16 v[80:83], a[100:103], a[4:7], v[80:83]\n" \
"ds_read_b64_tr_b16 a[66:67], v232 offset:18432\n" \
"v_mul_f32 v191, s76, v191\n" \
"v_mfma_f32_16x16x32_bf16 v[84:87], a[104:107], a[0:3], 0\n" \
"s_add_u32 m0, s79, 0\n" \
"ds_read_b64_tr_b16 a[68:69], v232 offset:24576\n" \
"v_cvt_pk_bf16_f32 v222, v188, v189\n" \
"v_mfma_f32_16x16x32_bf16 v[84:87], a[108:111], a[4:7], v[84:87]\n" \
"buffer_load_dwordx4 v228, s[52:55], s78 offen offset:0 lds\n" \
"v_mfma_f32_16x16x32_bf16 v[88:91], a[112:115], a[0:3], 0\n" \
"ds_read_b64_tr_b16 a[70:71], v232 offset:26624\n" \
"v_cvt_pk_bf16_f32 v223, v190, v191\n" \
"v_mfma_f32_16x16x32_bf16 v[88:91], a[116:119], a[4:7], v[88:91]\n" \
"ds_read_b64_tr_b16 a[72:73], v233 offset:16384\n" \
"v_accvgpr_read_b32 v243, a224\n" \
"v_mfma_f32_16x16x32_bf16 v[92:95], a[120:123], a[0:3], 0\n" \
"ds_read_b64_tr_b16 a[74:75], v233 offset:18432\n" \
"v_accvgpr_read_b32 v252, a225\n" \
"v_mfma_f32_16x16x32_bf16 v[92:95], a[124:127], a[4:7], v[92:95]\n" \
"buffer_load_dwordx4 v229, s[52:55], s78 offen offset:1024 lds\n" \
"v_mfma_f32_16x16x32_bf16 v[124:127], a[120:123], a[8:11], 0\n" \
"s_waitcnt vmcnt(33)\n" \
"ds_read_b128 a[0:3], v230 offset:0\n" \
"v_accvgpr_read_b32 v253, a226\n" \
"v_mfma_f32_16x16x32_bf16 v[124:127], a[124:127], a[12:15], v[124:127]\n" \
"ds_read_b128 a[4:7], v231 offset:0\n" \
"v_accvgpr_read_b32 v254, a227\n" \
"v_mfma_f32_16x16x32_bf16 v[120:123], a[112:115], a[8:11], 0\n" \
"s_xor_b32 s78, s78, 0x300000\n" \
"ds_read_b64_tr_b16 a[76:77], v233 offset:24576\n" \
"v_mul_f32 v243, s31, v243\n" \
"v_mfma_f32_16x16x32_bf16 v[120:123], a[116:119], a[12:15], v[120:123]\n" \
"buffer_load_dwordx4 v228, s[52:55], s89 offen offset:2048 lds\n" \
"v_mfma_f32_16x16x32_bf16 v[116:119], a[104:107], a[8:11], 0\n" \
"ds_read_b64_tr_b16 a[78:79], v233 offset:26624\n" \
"v_mul_f32 v252, s31, v252\n" \
"v_mfma_f32_16x16x32_bf16 v[116:119], a[108:111], a[12:15], v[116:119]\n" \
"ds_read_b64_tr_b16 a[80:81], v232 offset:16896\n" \
"v_mul_f32 v253, s31, v253\n" \
"v_mfma_f32_16x16x32_bf16 v[112:115], a[96:99], a[8:11], 0\n" \
"ds_read_b64_tr_b16 a[82:83], v232 offset:18944\n" \
"v_mul_f32 v254, s31, v254\n" \
"v_mfma_f32_16x16x32_bf16 v[112:115], a[100:103], a[12:15], v[112:115]\n" \
"buffer_load_dwordx4 v229, s[52:55], s89 offen offset:3072 lds\n" \
"v_mfma_f32_16x16x32_bf16 v[144:147], a[96:99], a[16:19], 0\n" \
"s_waitcnt vmcnt(33)\n" \
"s_xor_b32 s89, s89, 0x300000\n" \
"ds_read_b128 a[8:11], v230 offset:2048\n" \
"v_mfma_f32_16x16x32_bf16 v[144:147], a[100:103], a[20:23], v[144:147]\n" \
"s_add_u32 m0, s74, 65536\n" \
"ds_read_b128 a[12:15], v231 offset:2048\n" \
"v_cvt_pk_bf16_f32 v243, v243, v252\n" \
"v_mfma_f32_16x16x32_bf16 v[148:151], a[104:107], a[16:19], 0\n" \
"s_waitcnt lgkmcnt(6)\n" \
"ds_read_b64_tr_b16 a[84:85], v232 offset:25088\n" \
"v_cvt_pk_bf16_f32 v253, v253, v254\n" \
"v_mfma_f32_16x16x32_bf16 v[148:151], a[108:111], a[20:23], v[148:151]\n" \
"buffer_load_dwordx4 v224, s[48:51], s75 offen offset:0 lds\n" \
"v_mfma_f32_16x16x32_bf16 v[152:155], a[112:115], a[16:19], 0\n" \
"ds_read_b64_tr_b16 a[86:87], v232 offset:27136\n" \
"v_accvgpr_write_b32 a224, v243\n" \
"v_mfma_f32_16x16x32_bf16 v[152:155], a[116:119], a[20:23], v[152:155]\n" \
"ds_read_b64_tr_b16 a[88:89], v233 offset:16896\n" \
"v_accvgpr_write_b32 a225, v253\n" \
"v_mfma_f32_16x16x32_bf16 v[156:159], a[120:123], a[16:19], 0\n" \
"ds_read_b64_tr_b16 a[90:91], v233 offset:18944\n" \
"v_accvgpr_read_b32 v243, a240\n" \
"v_mfma_f32_16x16x32_bf16 v[156:159], a[124:127], a[20:23], v[156:159]\n" \
"buffer_load_dwordx4 v225, s[48:51], s75 offen offset:1024 lds\n" \
"v_mfma_f32_16x16x32_bf16 v[188:191], a[120:123], a[24:27], 0\n" \
"s_waitcnt vmcnt(33)\n" \
"ds_read_b128 a[16:19], v230 offset:4096\n" \
"v_accvgpr_read_b32 v252, a241\n" \
"v_mfma_f32_16x16x32_bf16 v[188:191], a[124:127], a[28:31], v[188:191]\n" \
"ds_read_b128 a[20:23], v231 offset:4096\n" \
"v_accvgpr_read_b32 v253, a242\n" \
"v_mfma_f32_16x16x32_bf16 v[184:187], a[112:115], a[24:27], 0\n" \
"s_waitcnt lgkmcnt(6)\n" \
"ds_read_b64_tr_b16 a[92:93], v233 offset:25088\n" \
"v_accvgpr_read_b32 v254, a243\n" \
"v_mfma_f32_16x16x32_bf16 v[184:187], a[116:119], a[28:31], v[184:187]\n" \
"buffer_load_dwordx4 v226, s[48:51], s75 offen offset:2048 lds\n" \
"v_mfma_f32_16x16x32_bf16 v[180:183], a[104:107], a[24:27], 0\n" \
"ds_read_b64_tr_b16 a[94:95], v233 offset:27136\n" \
"v_mul_f32 v243, s31, v243\n" \
"v_mfma_f32_16x16x32_bf16 v[180:183], a[108:111], a[28:31], v[180:183]\n" \
"v_mul_f32 v252, s31, v252\n" \
"v_mul_f32 v253, s31, v253\n" \
"v_mfma_f32_16x16x32_bf16 v[176:179], a[96:99], a[24:27], 0\n" \
"v_mul_f32 v254, s31, v254\n" \
"v_cvt_pk_bf16_f32 v243, v243, v252\n" \
"v_mfma_f32_16x16x32_bf16 v[176:179], a[100:103], a[28:31], v[176:179]\n" \
"buffer_load_dwordx4 v227, s[48:51], s75 offen offset:3072 lds\n" \
"v_mfma_f32_16x16x32_bf16 v[64:67], a[64:67], a[0:3], v[64:67]\n" \
"s_waitcnt vmcnt(33)\n" \
"s_xor_b32 s75, s75, 0x300\n" \
"ds_read_b128 a[24:27], v230 offset:6144\n" \
"v_mfma_f32_16x16x32_bf16 v[64:67], a[68:71], a[4:7], v[64:67]\n" \
"s_add_u32 m0, s74, 69632\n" \
"ds_read_b128 a[28:31], v231 offset:6144\n" \
"v_cvt_pk_bf16_f32 v253, v253, v254\n" \
"v_mfma_f32_16x16x32_bf16 v[68:71], a[72:75], a[0:3], v[68:71]\n" \
"s_waitcnt lgkmcnt(4)\n" \
"ds_read_b64_tr_b16 a[96:97], v232 offset:17408\n" \
"v_accvgpr_write_b32 a226, v243\n" \
"v_mfma_f32_16x16x32_bf16 v[68:71], a[76:79], a[4:7], v[68:71]\n" \
"buffer_load_dwordx4 v224, s[48:51], s77 offen offset:0 lds\n" \
"v_mfma_f32_16x16x32_bf16 v[72:75], a[80:83], a[0:3], v[72:75]\n" \
"ds_read_b64_tr_b16 a[98:99], v232 offset:19456\n" \
"v_accvgpr_write_b32 a227, v253\n" \
"v_mfma_f32_16x16x32_bf16 v[72:75], a[84:87], a[4:7], v[72:75]\n" \
"s_waitcnt lgkmcnt(3)\n" \
"ds_read_b64_tr_b16 a[100:101], v232 offset:25600\n" \
"v_accvgpr_read_b32 v243, a228\n" \
"v_mfma_f32_16x16x32_bf16 v[76:79], a[88:91], a[0:3], v[76:79]\n" \
"ds_read_b64_tr_b16 a[102:103], v232 offset:27648\n" \
"v_accvgpr_read_b32 v252, a229\n" \
"v_mfma_f32_16x16x32_bf16 v[76:79], a[92:95], a[4:7], v[76:79]\n" \
"buffer_load_dwordx4 v225, s[48:51], s77 offen offset:1024 lds\n" \
"v_mfma_f32_16x16x32_bf16 v[108:111], a[88:91], a[8:11], v[108:111]\n" \
"ds_read_b64_tr_b16 a[104:105], v233 offset:17408\n" \
"v_accvgpr_read_b32 v253, a230\n" \
"v_mfma_f32_16x16x32_bf16 v[108:111], a[92:95], a[12:15], v[108:111]\n" \
"ds_read_b64_tr_b16 a[106:107], v233 offset:19456\n" \
"v_accvgpr_read_b32 v254, a231\n" \
"v_mfma_f32_16x16x32_bf16 v[104:107], a[80:83], a[8:11], v[104:107]\n" \
"s_waitcnt lgkmcnt(6)\n" \
"ds_read_b64_tr_b16 a[108:109], v233 offset:25600\n" \
"v_mul_f32 v243, s31, v243\n" \
"v_mfma_f32_16x16x32_bf16 v[104:107], a[84:87], a[12:15], v[104:107]\n" \
"buffer_load_dwordx4 v226, s[48:51], s77 offen offset:2048 lds\n" \
"v_mfma_f32_16x16x32_bf16 v[100:103], a[72:75], a[8:11], v[100:103]\n" \
"ds_read_b64_tr_b16 a[110:111], v233 offset:27648\n" \
"v_mul_f32 v252, s31, v252\n" \
"v_mfma_f32_16x16x32_bf16 v[100:103], a[76:79], a[12:15], v[100:103]\n" \
"ds_read_b64_tr_b16 a[112:113], v232 offset:17920\n" \
"v_mul_f32 v253, s31, v253\n" \
"v_mfma_f32_16x16x32_bf16 v[96:99], a[64:67], a[8:11], v[96:99]\n" \
"ds_read_b64_tr_b16 a[114:115], v232 offset:19968\n" \
"v_mul_f32 v254, s31, v254\n" \
"v_mfma_f32_16x16x32_bf16 v[96:99], a[68:71], a[12:15], v[96:99]\n" \
"buffer_load_dwordx4 v227, s[48:51], s77 offen offset:3072 lds\n" \
"v_mfma_f32_16x16x32_bf16 v[128:131], a[64:67], a[16:19], v[128:131]\n" \
"ds_read_b64_tr_b16 a[116:117], v232 offset:26112\n" \
"v_cvt_pk_bf16_f32 v243, v243, v252\n" \
"v_mfma_f32_16x16x32_bf16 v[128:131], a[68:71], a[20:23], v[128:131]\n" \
"ds_read_b64_tr_b16 a[118:119], v232 offset:28160\n" \
"v_cvt_pk_bf16_f32 v253, v253, v254\n" \
"v_mfma_f32_16x16x32_bf16 v[132:135], a[72:75], a[16:19], v[132:135]\n" \
"s_xor_b32 s77, s77, 0x300\n" \
"ds_read_b64_tr_b16 a[120:121], v233 offset:17920\n" \
"v_accvgpr_write_b32 a228, v243\n" \
"v_mfma_f32_16x16x32_bf16 v[132:135], a[76:79], a[20:23], v[132:135]\n" \
"v_accvgpr_write_b32 a229, v253\n" \
"v_accvgpr_read_b32 v243, a244\n" \
"v_mfma_f32_16x16x32_bf16 v[136:139], a[80:83], a[16:19], v[136:139]\n" \
"ds_read_b64_tr_b16 a[122:123], v233 offset:19968\n" \
"v_accvgpr_read_b32 v252, a245\n" \
"v_mfma_f32_16x16x32_bf16 v[136:139], a[84:87], a[20:23], v[136:139]\n" \
"ds_read_b64_tr_b16 a[124:125], v233 offset:26112\n" \
"v_accvgpr_read_b32 v253, a246\n" \
"v_mfma_f32_16x16x32_bf16 v[140:143], a[88:91], a[16:19], v[140:143]\n" \
"ds_read_b64_tr_b16 a[126:127], v233 offset:28160\n" \
"v_accvgpr_read_b32 v254, a247\n" \
"v_mfma_f32_16x16x32_bf16 v[140:143], a[92:95], a[20:23], v[140:143]\n" \
"buffer_store_dwordx4 v[208:211], v236, s[56:59], s93 offen offset:256 sc1\n" \
"v_mfma_f32_16x16x32_bf16 v[172:175], a[88:91], a[24:27], v[172:175]\n" \
"v_mul_f32 v243, s31, v243\n" \
"v_mul_f32 v252, s31, v252\n" \
"v_mfma_f32_16x16x32_bf16 v[172:175], a[92:95], a[28:31], v[172:175]\n" \
"v_mul_f32 v253, s31, v253\n" \
"v_mul_f32 v254, s31, v254\n" \
"v_mfma_f32_16x16x32_bf16 v[168:171], a[80:83], a[24:27], v[168:171]\n" \
"v_cvt_pk_bf16_f32 v243, v243, v252\n" \
"v_cvt_pk_bf16_f32 v253, v253, v254\n" \
"v_mfma_f32_16x16x32_bf16 v[168:171], a[84:87], a[28:31], v[168:171]\n" \
"v_accvgpr_write_b32 a230, v243\n" \
"v_accvgpr_write_b32 a231, v253\n" \
"v_mfma_f32_16x16x32_bf16 v[164:167], a[72:75], a[24:27], v[164:167]\n" \
"v_accvgpr_read_b32 v243, a232\n" \
"v_accvgpr_read_b32 v252, a233\n" \
"v_mfma_f32_16x16x32_bf16 v[164:167], a[76:79], a[28:31], v[164:167]\n" \
"v_accvgpr_read_b32 v253, a234\n" \
"v_accvgpr_read_b32 v254, a235\n" \
"v_mfma_f32_16x16x32_bf16 v[160:163], a[64:67], a[24:27], v[160:163]\n" \
"v_mul_f32 v243, s31, v243\n" \
"v_mul_f32 v252, s31, v252\n" \
"v_mfma_f32_16x16x32_bf16 v[160:163], a[68:71], a[28:31], v[160:163]\n" \
"s_waitcnt vmcnt(26) lgkmcnt(0)\n" \
"s_barrier\n" \
"v_mfma_f32_16x16x32_bf16 v[80:83], a[96:99], a[0:3], v[80:83]\n" \
"ds_read_b64_tr_b16 a[64:65], v232 offset:32768\n" \
"v_mul_f32 v253, s31, v253\n" \
"v_mfma_f32_16x16x32_bf16 v[80:83], a[100:103], a[4:7], v[80:83]\n" \
"ds_read_b64_tr_b16 a[66:67], v232 offset:34816\n" \
"v_mul_f32 v254, s31, v254\n" \
"v_mfma_f32_16x16x32_bf16 v[84:87], a[104:107], a[0:3], v[84:87]\n" \
"s_add_u32 m0, s79, 16384\n" \
"ds_read_b64_tr_b16 a[68:69], v232 offset:40960\n" \
"v_cvt_pk_bf16_f32 v243, v243, v252\n" \
"v_mfma_f32_16x16x32_bf16 v[84:87], a[108:111], a[4:7], v[84:87]\n" \
"buffer_load_dwordx4 v228, s[52:55], s78 offen offset:0 lds\n" \
"v_mfma_f32_16x16x32_bf16 v[88:91], a[112:115], a[0:3], v[88:91]\n" \
"ds_read_b64_tr_b16 a[70:71], v232 offset:43008\n" \
"v_cvt_pk_bf16_f32 v253, v253, v254\n" \
"v_mfma_f32_16x16x32_bf16 v[88:91], a[116:119], a[4:7], v[88:91]\n" \
"ds_read_b64_tr_b16 a[72:73], v233 offset:32768\n" \
"v_accvgpr_write_b32 a232, v243\n" \
"v_mfma_f32_16x16x32_bf16 v[92:95], a[120:123], a[0:3], v[92:95]\n" \
"ds_read_b64_tr_b16 a[74:75], v233 offset:34816\n" \
"v_accvgpr_write_b32 a233, v253\n" \
"v_mfma_f32_16x16x32_bf16 v[92:95], a[124:127], a[4:7], v[92:95]\n" \
"buffer_load_dwordx4 v229, s[52:55], s78 offen offset:1024 lds\n" \
"v_mfma_f32_16x16x32_bf16 v[124:127], a[120:123], a[8:11], v[124:127]\n" \
"ds_read_b128 a[0:3], v230 offset:32768\n" \
"v_accvgpr_read_b32 v243, a248\n" \
"v_mfma_f32_16x16x32_bf16 v[124:127], a[124:127], a[12:15], v[124:127]\n" \
"ds_read_b128 a[4:7], v231 offset:32768\n" \
"v_accvgpr_read_b32 v252, a249\n" \
"v_mfma_f32_16x16x32_bf16 v[120:123], a[112:115], a[8:11], v[120:123]\n" \
"s_xor_b32 s78, s78, 0x80000\n" \
"ds_read_b64_tr_b16 a[76:77], v233 offset:40960\n" \
"v_accvgpr_read_b32 v253, a250\n" \
"v_mfma_f32_16x16x32_bf16 v[120:123], a[116:119], a[12:15], v[120:123]\n" \
"buffer_load_dwordx4 v228, s[52:55], s89 offen offset:2048 lds\n" \
"v_mfma_f32_16x16x32_bf16 v[116:119], a[104:107], a[8:11], v[116:119]\n" \
"ds_read_b64_tr_b16 a[78:79], v233 offset:43008\n" \
"v_accvgpr_read_b32 v254, a251\n" \
"v_mfma_f32_16x16x32_bf16 v[116:119], a[108:111], a[12:15], v[116:119]\n" \
"ds_read_b64_tr_b16 a[80:81], v232 offset:33280\n" \
"v_mul_f32 v243, s31, v243\n" \
"v_mfma_f32_16x16x32_bf16 v[112:115], a[96:99], a[8:11], v[112:115]\n" \
"ds_read_b64_tr_b16 a[82:83], v232 offset:35328\n" \
"v_mul_f32 v252, s31, v252\n" \
"v_mfma_f32_16x16x32_bf16 v[112:115], a[100:103], a[12:15], v[112:115]\n" \
"buffer_load_dwordx4 v229, s[52:55], s89 offen offset:3072 lds\n" \
"v_mfma_f32_16x16x32_bf16 v[144:147], a[96:99], a[16:19], v[144:147]\n" \
"s_xor_b32 s89, s89, 0x80000\n" \
"ds_read_b128 a[8:11], v230 offset:34816\n" \
"v_mfma_f32_16x16x32_bf16 v[144:147], a[100:103], a[20:23], v[144:147]\n" \
"s_add_u32 m0, s74, 98304\n" \
"ds_read_b128 a[12:15], v231 offset:34816\n" \
"v_mul_f32 v253, s31, v253\n" \
"v_mfma_f32_16x16x32_bf16 v[148:151], a[104:107], a[16:19], v[148:151]\n" \
"s_waitcnt lgkmcnt(6)\n" \
"ds_read_b64_tr_b16 a[84:85], v232 offset:41472\n" \
"v_mul_f32 v254, s31, v254\n" \
"v_mfma_f32_16x16x32_bf16 v[148:151], a[108:111], a[20:23], v[148:151]\n" \
"buffer_load_dwordx4 v224, s[48:51], s75 offen offset:0 lds\n" \
"v_mfma_f32_16x16x32_bf16 v[152:155], a[112:115], a[16:19], v[152:155]\n" \
"ds_read_b64_tr_b16 a[86:87], v232 offset:43520\n" \
"v_cvt_pk_bf16_f32 v243, v243, v252\n" \
"v_mfma_f32_16x16x32_bf16 v[152:155], a[116:119], a[20:23], v[152:155]\n" \
"ds_read_b64_tr_b16 a[88:89], v233 offset:33280\n" \
"v_cvt_pk_bf16_f32 v253, v253, v254\n" \
"v_mfma_f32_16x16x32_bf16 v[156:159], a[120:123], a[16:19], v[156:159]\n" \
"ds_read_b64_tr_b16 a[90:91], v233 offset:35328\n" \
"v_accvgpr_write_b32 a234, v243\n" \
"v_mfma_f32_16x16x32_bf16 v[156:159], a[124:127], a[20:23], v[156:159]\n" \
"buffer_load_dwordx4 v225, s[48:51], s75 offen offset:1024 lds\n" \
"v_mfma_f32_16x16x32_bf16 v[188:191], a[120:123], a[24:27], v[188:191]\n" \
"ds_read_b128 a[16:19], v230 offset:36864\n" \
"v_accvgpr_write_b32 a235, v253\n" \
"v_mfma_f32_16x16x32_bf16 v[188:191], a[124:127], a[28:31], v[188:191]\n" \
"ds_read_b128 a[20:23], v231 offset:36864\n" \
"v_accvgpr_read_b32 v243, a236\n" \
"v_mfma_f32_16x16x32_bf16 v[184:187], a[112:115], a[24:27], v[184:187]\n" \
"s_waitcnt lgkmcnt(6)\n" \
"ds_read_b64_tr_b16 a[92:93], v233 offset:41472\n" \
"v_accvgpr_read_b32 v252, a237\n" \
"v_mfma_f32_16x16x32_bf16 v[184:187], a[116:119], a[28:31], v[184:187]\n" \
"buffer_load_dwordx4 v226, s[48:51], s75 offen offset:2048 lds\n" \
"v_mfma_f32_16x16x32_bf16 v[180:183], a[104:107], a[24:27], v[180:183]\n" \
"ds_read_b64_tr_b16 a[94:95], v233 offset:43520\n" \
"v_accvgpr_read_b32 v253, a238\n" \
"v_mfma_f32_16x16x32_bf16 v[180:183], a[108:111], a[28:31], v[180:183]\n" \
"v_accvgpr_read_b32 v254, a239\n" \
"v_mul_f32 v243, s31, v243\n" \
"v_mfma_f32_16x16x32_bf16 v[176:179], a[96:99], a[24:27], v[176:179]\n" \
"v_mul_f32 v252, s31, v252\n" \
"v_mul_f32 v253, s31, v253\n" \
"v_mfma_f32_16x16x32_bf16 v[176:179], a[100:103], a[28:31], v[176:179]\n" \
"buffer_load_dwordx4 v227, s[48:51], s75 offen offset:3072 lds\n" \
"v_mfma_f32_16x16x32_bf16 v[64:67], a[64:67], a[0:3], v[64:67]\n" \
"s_xor_b32 s75, s75, 0x80\n" \
"ds_read_b128 a[24:27], v230 offset:38912\n" \
"v_mfma_f32_16x16x32_bf16 v[64:67], a[68:71], a[4:7], v[64:67]\n" \
"s_add_u32 m0, s74, 102400\n" \
"ds_read_b128 a[28:31], v231 offset:38912\n" \
"v_mul_f32 v254, s31, v254\n" \
"v_mfma_f32_16x16x32_bf16 v[68:71], a[72:75], a[0:3], v[68:71]\n" \
"s_waitcnt lgkmcnt(4)\n" \
"ds_read_b64_tr_b16 a[96:97], v232 offset:33792\n" \
"v_cvt_pk_bf16_f32 v243, v243, v252\n" \
"v_mfma_f32_16x16x32_bf16 v[68:71], a[76:79], a[4:7], v[68:71]\n" \
"buffer_load_dwordx4 v224, s[48:51], s77 offen offset:0 lds\n" \
"v_mfma_f32_16x16x32_bf16 v[72:75], a[80:83], a[0:3], v[72:75]\n" \
"ds_read_b64_tr_b16 a[98:99], v232 offset:35840\n" \
"v_cvt_pk_bf16_f32 v253, v253, v254\n" \
"v_mfma_f32_16x16x32_bf16 v[72:75], a[84:87], a[4:7], v[72:75]\n" \
"s_waitcnt lgkmcnt(3)\n" \
"ds_read_b64_tr_b16 a[100:101], v232 offset:41984\n" \
"v_accvgpr_write_b32 a236, v243\n" \
"v_mfma_f32_16x16x32_bf16 v[76:79], a[88:91], a[0:3], v[76:79]\n" \
"ds_read_b64_tr_b16 a[102:103], v232 offset:44032\n" \
"v_accvgpr_write_b32 a237, v253\n" \
"v_mfma_f32_16x16x32_bf16 v[76:79], a[92:95], a[4:7], v[76:79]\n" \
"buffer_load_dwordx4 v225, s[48:51], s77 offen offset:1024 lds\n" \
"v_mfma_f32_16x16x32_bf16 v[108:111], a[88:91], a[8:11], v[108:111]\n" \
"ds_read_b64_tr_b16 a[104:105], v233 offset:33792\n" \
"v_accvgpr_read_b32 v243, a252\n" \
"v_mfma_f32_16x16x32_bf16 v[108:111], a[92:95], a[12:15], v[108:111]\n" \
"ds_read_b64_tr_b16 a[106:107], v233 offset:35840\n" \
"v_accvgpr_read_b32 v252, a253\n" \
"v_mfma_f32_16x16x32_bf16 v[104:107], a[80:83], a[8:11], v[104:107]\n" \
"s_waitcnt lgkmcnt(6)\n" \
"ds_read_b64_tr_b16 a[108:109], v233 offset:41984\n" \
"v_accvgpr_read_b32 v253, a254\n" \
"v_mfma_f32_16x16x32_bf16 v[104:107], a[84:87], a[12:15], v[104:107]\n" \
"buffer_load_dwordx4 v226, s[48:51], s77 offen offset:2048 lds\n" \
"v_mfma_f32_16x16x32_bf16 v[100:103], a[72:75], a[8:11], v[100:103]\n" \
"ds_read_b64_tr_b16 a[110:111], v233 offset:44032\n" \
"v_accvgpr_read_b32 v254, a255\n" \
"v_mfma_f32_16x16x32_bf16 v[100:103], a[76:79], a[12:15], v[100:103]\n" \
"ds_read_b64_tr_b16 a[112:113], v232 offset:34304\n" \
"v_mul_f32 v243, s31, v243\n" \
"v_mfma_f32_16x16x32_bf16 v[96:99], a[64:67], a[8:11], v[96:99]\n" \
"ds_read_b64_tr_b16 a[114:115], v232 offset:36352\n" \
"v_mul_f32 v252, s31, v252\n" \
"v_mfma_f32_16x16x32_bf16 v[96:99], a[68:71], a[12:15], v[96:99]\n" \
"buffer_load_dwordx4 v227, s[48:51], s77 offen offset:3072 lds\n" \
"v_mfma_f32_16x16x32_bf16 v[128:131], a[64:67], a[16:19], v[128:131]\n" \
"ds_read_b64_tr_b16 a[116:117], v232 offset:42496\n" \
"v_mul_f32 v253, s31, v253\n" \
"v_mfma_f32_16x16x32_bf16 v[128:131], a[68:71], a[20:23], v[128:131]\n" \
"ds_read_b64_tr_b16 a[118:119], v232 offset:44544\n" \
"v_mul_f32 v254, s31, v254\n" \
"v_mfma_f32_16x16x32_bf16 v[132:135], a[72:75], a[16:19], v[132:135]\n" \
"s_xor_b32 s77, s77, 0x80\n" \
"ds_read_b64_tr_b16 a[120:121], v233 offset:34304\n" \
"v_cvt_pk_bf16_f32 v243, v243, v252\n" \
"v_mfma_f32_16x16x32_bf16 v[132:135], a[76:79], a[20:23], v[132:135]\n" \
"v_cvt_pk_bf16_f32 v253, v253, v254\n" \
"v_accvgpr_write_b32 a238, v243\n" \
"v_mfma_f32_16x16x32_bf16 v[136:139], a[80:83], a[16:19], v[136:139]\n" \
"ds_read_b64_tr_b16 a[122:123], v233 offset:36352\n" \
"v_accvgpr_write_b32 a239, v253\n" \
"v_mfma_f32_16x16x32_bf16 v[136:139], a[84:87], a[20:23], v[136:139]\n" \
"ds_read_b64_tr_b16 a[124:125], v233 offset:42496\n" \
"v_accvgpr_read_b32 v243, a192\n" \
"v_mfma_f32_16x16x32_bf16 v[140:143], a[88:91], a[16:19], v[140:143]\n" \
"ds_read_b64_tr_b16 a[126:127], v233 offset:44544\n" \
"v_accvgpr_read_b32 v252, a193\n" \
"v_mfma_f32_16x16x32_bf16 v[140:143], a[92:95], a[20:23], v[140:143]\n" \
"buffer_store_dwordx4 v[200:203], v236, s[56:59], s93 offen offset:2048 sc1\n" \
"v_mfma_f32_16x16x32_bf16 v[172:175], a[88:91], a[24:27], v[172:175]\n" \
"v_accvgpr_read_b32 v253, a194\n" \
"v_accvgpr_read_b32 v254, a195\n" \
"v_mfma_f32_16x16x32_bf16 v[172:175], a[92:95], a[28:31], v[172:175]\n" \
"v_mul_f32 v243, s31, v243\n" \
"v_mul_f32 v252, s31, v252\n" \
"v_mfma_f32_16x16x32_bf16 v[168:171], a[80:83], a[24:27], v[168:171]\n" \
"v_mul_f32 v253, s31, v253\n" \
"v_mul_f32 v254, s31, v254\n" \
"v_mfma_f32_16x16x32_bf16 v[168:171], a[84:87], a[28:31], v[168:171]\n" \
"v_cvt_pk_bf16_f32 v243, v243, v252\n" \
"v_cvt_pk_bf16_f32 v253, v253, v254\n" \
"v_mfma_f32_16x16x32_bf16 v[164:167], a[72:75], a[24:27], v[164:167]\n" \
"v_accvgpr_write_b32 a192, v243\n" \
"v_accvgpr_write_b32 a193, v253\n" \
"v_mfma_f32_16x16x32_bf16 v[164:167], a[76:79], a[28:31], v[164:167]\n" \
"v_accvgpr_read_b32 v243, a208\n" \
"v_accvgpr_read_b32 v252, a209\n" \
"v_mfma_f32_16x16x32_bf16 v[160:163], a[64:67], a[24:27], v[160:163]\n" \
"v_accvgpr_read_b32 v253, a210\n" \
"v_accvgpr_read_b32 v254, a211\n" \
"v_mfma_f32_16x16x32_bf16 v[160:163], a[68:71], a[28:31], v[160:163]\n" \
"s_waitcnt vmcnt(35) lgkmcnt(0)\n" \
"s_barrier\n" \
"v_mfma_f32_16x16x32_bf16 v[80:83], a[96:99], a[0:3], v[80:83]\n" \
"ds_read_b64_tr_b16 a[64:65], v232 offset:49152\n" \
"v_mul_f32 v243, s31, v243\n" \
"v_mfma_f32_16x16x32_bf16 v[80:83], a[100:103], a[4:7], v[80:83]\n" \
"ds_read_b64_tr_b16 a[66:67], v232 offset:51200\n" \
"v_mul_f32 v252, s31, v252\n" \
"v_mfma_f32_16x16x32_bf16 v[84:87], a[104:107], a[0:3], v[84:87]\n" \
"s_add_u32 m0, s79, 32768\n" \
"ds_read_b64_tr_b16 a[68:69], v232 offset:57344\n" \
"v_mul_f32 v253, s31, v253\n" \
"v_mfma_f32_16x16x32_bf16 v[84:87], a[108:111], a[4:7], v[84:87]\n" \
"buffer_load_dwordx4 v228, s[52:55], s78 offen offset:0 lds\n" \
"v_mfma_f32_16x16x32_bf16 v[88:91], a[112:115], a[0:3], v[88:91]\n" \
"ds_read_b64_tr_b16 a[70:71], v232 offset:59392\n" \
"v_mul_f32 v254, s31, v254\n" \
"v_mfma_f32_16x16x32_bf16 v[88:91], a[116:119], a[4:7], v[88:91]\n" \
"ds_read_b64_tr_b16 a[72:73], v233 offset:49152\n" \
"v_cvt_pk_bf16_f32 v243, v243, v252\n" \
"v_mfma_f32_16x16x32_bf16 v[92:95], a[120:123], a[0:3], v[92:95]\n" \
"ds_read_b64_tr_b16 a[74:75], v233 offset:51200\n" \
"v_cvt_pk_bf16_f32 v253, v253, v254\n" \
"v_mfma_f32_16x16x32_bf16 v[92:95], a[124:127], a[4:7], v[92:95]\n" \
"buffer_load_dwordx4 v229, s[52:55], s78 offen offset:1024 lds\n" \
"v_mfma_f32_16x16x32_bf16 v[124:127], a[120:123], a[8:11], v[124:127]\n" \
"s_waitcnt vmcnt(35)\n" \
"ds_read_b128 a[0:3], v250 offset:0\n" \
"v_accvgpr_write_b32 a194, v243\n" \
"v_mfma_f32_16x16x32_bf16 v[124:127], a[124:127], a[12:15], v[124:127]\n" \
"ds_read_b128 a[4:7], v251 offset:0\n" \
"v_accvgpr_write_b32 a195, v253\n" \
"v_mfma_f32_16x16x32_bf16 v[120:123], a[112:115], a[8:11], v[120:123]\n" \
"s_xor_b32 s78, s78, 0x180000\n" \
"ds_read_b64_tr_b16 a[76:77], v233 offset:57344\n" \
"v_accvgpr_read_b32 v243, a196\n" \
"v_mfma_f32_16x16x32_bf16 v[120:123], a[116:119], a[12:15], v[120:123]\n" \
"buffer_load_dwordx4 v228, s[52:55], s89 offen offset:2048 lds\n" \
"v_mfma_f32_16x16x32_bf16 v[116:119], a[104:107], a[8:11], v[116:119]\n" \
"ds_read_b64_tr_b16 a[78:79], v233 offset:59392\n" \
"v_accvgpr_read_b32 v252, a197\n" \
"v_mfma_f32_16x16x32_bf16 v[116:119], a[108:111], a[12:15], v[116:119]\n" \
"ds_read_b64_tr_b16 a[80:81], v232 offset:49664\n" \
"v_accvgpr_read_b32 v253, a198\n" \
"v_mfma_f32_16x16x32_bf16 v[112:115], a[96:99], a[8:11], v[112:115]\n" \
"ds_read_b64_tr_b16 a[82:83], v232 offset:51712\n" \
"v_accvgpr_read_b32 v254, a199\n" \
"v_mfma_f32_16x16x32_bf16 v[112:115], a[100:103], a[12:15], v[112:115]\n" \
"buffer_load_dwordx4 v229, s[52:55], s89 offen offset:3072 lds\n" \
"v_mfma_f32_16x16x32_bf16 v[144:147], a[96:99], a[16:19], v[144:147]\n" \
"s_waitcnt vmcnt(35)\n" \
"s_xor_b32 s89, s89, 0x180000\n" \
"ds_read_b128 a[8:11], v250 offset:2048\n" \
"v_mfma_f32_16x16x32_bf16 v[144:147], a[100:103], a[20:23], v[144:147]\n" \
"s_add_u32 m0, s74, 131072\n" \
"ds_read_b128 a[12:15], v251 offset:2048\n" \
"v_mul_f32 v243, s31, v243\n" \
"v_mfma_f32_16x16x32_bf16 v[148:151], a[104:107], a[16:19], v[148:151]\n" \
"s_waitcnt lgkmcnt(6)\n" \
"ds_read_b64_tr_b16 a[84:85], v232 offset:57856\n" \
"v_mul_f32 v252, s31, v252\n" \
"v_mfma_f32_16x16x32_bf16 v[148:151], a[108:111], a[20:23], v[148:151]\n" \
"buffer_load_dwordx4 v224, s[48:51], s75 offen offset:0 lds\n" \
"v_mfma_f32_16x16x32_bf16 v[152:155], a[112:115], a[16:19], v[152:155]\n" \
"ds_read_b64_tr_b16 a[86:87], v232 offset:59904\n" \
"v_mul_f32 v253, s31, v253\n" \
"v_mfma_f32_16x16x32_bf16 v[152:155], a[116:119], a[20:23], v[152:155]\n" \
"ds_read_b64_tr_b16 a[88:89], v233 offset:49664\n" \
"v_mul_f32 v254, s31, v254\n" \
"v_mfma_f32_16x16x32_bf16 v[156:159], a[120:123], a[16:19], v[156:159]\n" \
"ds_read_b64_tr_b16 a[90:91], v233 offset:51712\n" \
"v_cvt_pk_bf16_f32 v243, v243, v252\n" \
"v_mfma_f32_16x16x32_bf16 v[156:159], a[124:127], a[20:23], v[156:159]\n" \
"buffer_load_dwordx4 v225, s[48:51], s75 offen offset:1024 lds\n" \
"v_mfma_f32_16x16x32_bf16 v[188:191], a[120:123], a[24:27], v[188:191]\n" \
"s_waitcnt vmcnt(35)\n" \
"ds_read_b128 a[16:19], v250 offset:4096\n" \
"v_cvt_pk_bf16_f32 v253, v253, v254\n" \
"v_mfma_f32_16x16x32_bf16 v[188:191], a[124:127], a[28:31], v[188:191]\n" \
"ds_read_b128 a[20:23], v251 offset:4096\n" \
"v_accvgpr_write_b32 a196, v243\n" \
"v_mfma_f32_16x16x32_bf16 v[184:187], a[112:115], a[24:27], v[184:187]\n" \
"s_waitcnt lgkmcnt(6)\n" \
"ds_read_b64_tr_b16 a[92:93], v233 offset:57856\n" \
"v_accvgpr_write_b32 a197, v253\n" \
"v_mfma_f32_16x16x32_bf16 v[184:187], a[116:119], a[28:31], v[184:187]\n" \
"buffer_load_dwordx4 v226, s[48:51], s75 offen offset:2048 lds\n" \
"v_mfma_f32_16x16x32_bf16 v[180:183], a[104:107], a[24:27], v[180:183]\n" \
"ds_read_b64_tr_b16 a[94:95], v233 offset:59904\n" \
"v_accvgpr_read_b32 v243, a212\n" \
"v_mfma_f32_16x16x32_bf16 v[180:183], a[108:111], a[28:31], v[180:183]\n" \
"v_accvgpr_read_b32 v252, a213\n" \
"v_accvgpr_read_b32 v253, a214\n" \
"v_mfma_f32_16x16x32_bf16 v[176:179], a[96:99], a[24:27], v[176:179]\n" \
"v_accvgpr_read_b32 v254, a215\n" \
"v_mul_f32 v243, s31, v243\n" \
"v_mfma_f32_16x16x32_bf16 v[176:179], a[100:103], a[28:31], v[176:179]\n" \
"buffer_load_dwordx4 v227, s[48:51], s75 offen offset:3072 lds\n" \
"v_mfma_f32_16x16x32_bf16 v[64:67], a[64:67], a[0:3], v[64:67]\n" \
"s_waitcnt vmcnt(34)\n" \
"s_xor_b32 s75, s75, 0x180\n" \
"ds_read_b128 a[24:27], v250 offset:6144\n" \
"v_mfma_f32_16x16x32_bf16 v[64:67], a[68:71], a[4:7], v[64:67]\n" \
"s_add_u32 m0, s74, 135168\n" \
"ds_read_b128 a[28:31], v251 offset:6144\n" \
"v_mul_f32 v252, s31, v252\n" \
"v_mfma_f32_16x16x32_bf16 v[68:71], a[72:75], a[0:3], v[68:71]\n" \
"s_waitcnt lgkmcnt(4)\n" \
"ds_read_b64_tr_b16 a[96:97], v232 offset:50176\n" \
"v_mul_f32 v253, s31, v253\n" \
"v_mfma_f32_16x16x32_bf16 v[68:71], a[76:79], a[4:7], v[68:71]\n" \
"buffer_load_dwordx4 v224, s[48:51], s77 offen offset:0 lds\n" \
"v_mfma_f32_16x16x32_bf16 v[72:75], a[80:83], a[0:3], v[72:75]\n" \
"ds_read_b64_tr_b16 a[98:99], v232 offset:52224\n" \
"v_mul_f32 v254, s31, v254\n" \
"v_mfma_f32_16x16x32_bf16 v[72:75], a[84:87], a[4:7], v[72:75]\n" \
"s_waitcnt lgkmcnt(3)\n" \
"ds_read_b64_tr_b16 a[100:101], v232 offset:58368\n" \
"v_cvt_pk_bf16_f32 v243, v243, v252\n" \
"v_mfma_f32_16x16x32_bf16 v[76:79], a[88:91], a[0:3], v[76:79]\n" \
"ds_read_b64_tr_b16 a[102:103], v232 offset:60416\n" \
"v_cvt_pk_bf16_f32 v253, v253, v254\n" \
"v_mfma_f32_16x16x32_bf16 v[76:79], a[92:95], a[4:7], v[76:79]\n" \
"buffer_load_dwordx4 v225, s[48:51], s77 offen offset:1024 lds\n" \
"v_mfma_f32_16x16x32_bf16 v[108:111], a[88:91], a[8:11], v[108:111]\n" \
"ds_read_b64_tr_b16 a[104:105], v233 offset:50176\n" \
"v_accvgpr_write_b32 a198, v243\n" \
"v_mfma_f32_16x16x32_bf16 v[108:111], a[92:95], a[12:15], v[108:111]\n" \
"ds_read_b64_tr_b16 a[106:107], v233 offset:52224\n" \
"v_accvgpr_write_b32 a199, v253\n" \
"v_mfma_f32_16x16x32_bf16 v[104:107], a[80:83], a[8:11], v[104:107]\n" \
"s_waitcnt lgkmcnt(6)\n" \
"ds_read_b64_tr_b16 a[108:109], v233 offset:58368\n" \
"v_accvgpr_read_b32 v243, a200\n" \
"v_mfma_f32_16x16x32_bf16 v[104:107], a[84:87], a[12:15], v[104:107]\n" \
"buffer_load_dwordx4 v226, s[48:51], s77 offen offset:2048 lds\n" \
"v_mfma_f32_16x16x32_bf16 v[100:103], a[72:75], a[8:11], v[100:103]\n" \
"ds_read_b64_tr_b16 a[110:111], v233 offset:60416\n" \
"v_accvgpr_read_b32 v252, a201\n" \
"Lpg2766_%=:\n" \
"s_cbranch_execz Lpg5014_%=\n" \
"v_mfma_f32_16x16x32_bf16 v[100:103], a[76:79], a[12:15], v[100:103]\n" \
"ds_read_b64_tr_b16 a[112:113], v232 offset:50688\n" \
"v_accvgpr_read_b32 v253, a202\n" \
"v_mfma_f32_16x16x32_bf16 v[96:99], a[64:67], a[8:11], v[96:99]\n" \
"ds_read_b64_tr_b16 a[114:115], v232 offset:52736\n" \
"v_accvgpr_read_b32 v254, a203\n" \
"v_mfma_f32_16x16x32_bf16 v[96:99], a[68:71], a[12:15], v[96:99]\n" \
"buffer_load_dwordx4 v227, s[48:51], s77 offen offset:3072 lds\n" \
"v_mfma_f32_16x16x32_bf16 v[128:131], a[64:67], a[16:19], v[128:131]\n" \
"ds_read_b64_tr_b16 a[116:117], v232 offset:58880\n" \
"v_mul_f32 v243, s31, v243\n" \
"v_mfma_f32_16x16x32_bf16 v[128:131], a[68:71], a[20:23], v[128:131]\n" \
"ds_read_b64_tr_b16 a[118:119], v232 offset:60928\n" \
"v_mul_f32 v252, s31, v252\n" \
"v_mfma_f32_16x16x32_bf16 v[132:135], a[72:75], a[16:19], v[132:135]\n" \
"s_xor_b32 s77, s77, 0x180\n" \
"ds_read_b64_tr_b16 a[120:121], v233 offset:50688\n" \
"v_mul_f32 v253, s31, v253\n" \
"v_mfma_f32_16x16x32_bf16 v[132:135], a[76:79], a[20:23], v[132:135]\n" \
"v_mul_f32 v254, s31, v254\n" \
"v_cvt_pk_bf16_f32 v243, v243, v252\n" \
"v_mfma_f32_16x16x32_bf16 v[136:139], a[80:83], a[16:19], v[136:139]\n" \
"ds_read_b64_tr_b16 a[122:123], v233 offset:52736\n" \
"v_cvt_pk_bf16_f32 v253, v253, v254\n" \
"v_mfma_f32_16x16x32_bf16 v[136:139], a[84:87], a[20:23], v[136:139]\n" \
"ds_read_b64_tr_b16 a[124:125], v233 offset:58880\n" \
"v_accvgpr_write_b32 a200, v243\n" \
"v_mfma_f32_16x16x32_bf16 v[140:143], a[88:91], a[16:19], v[140:143]\n" \
"ds_read_b64_tr_b16 a[126:127], v233 offset:60928\n" \
"v_accvgpr_write_b32 a201, v253\n" \
"v_mfma_f32_16x16x32_bf16 v[140:143], a[92:95], a[20:23], v[140:143]\n" \
"buffer_store_dwordx4 v[216:219], v236, s[56:59], s93 offen offset:2304 sc1\n" \
"v_mfma_f32_16x16x32_bf16 v[172:175], a[88:91], a[24:27], v[172:175]\n" \
"v_accvgpr_read_b32 v243, a216\n" \
"v_accvgpr_read_b32 v252, a217\n" \
"v_mfma_f32_16x16x32_bf16 v[172:175], a[92:95], a[28:31], v[172:175]\n" \
"v_accvgpr_read_b32 v253, a218\n" \
"v_accvgpr_read_b32 v254, a219\n" \
"v_mfma_f32_16x16x32_bf16 v[168:171], a[80:83], a[24:27], v[168:171]\n" \
"v_mul_f32 v243, s31, v243\n" \
"v_mul_f32 v252, s31, v252\n" \
"v_mfma_f32_16x16x32_bf16 v[168:171], a[84:87], a[28:31], v[168:171]\n" \
"v_mul_f32 v253, s31, v253\n" \
"v_mul_f32 v254, s31, v254\n" \
"v_mfma_f32_16x16x32_bf16 v[164:167], a[72:75], a[24:27], v[164:167]\n" \
"v_cvt_pk_bf16_f32 v243, v243, v252\n" \
"v_cvt_pk_bf16_f32 v253, v253, v254\n" \
"v_mfma_f32_16x16x32_bf16 v[164:167], a[76:79], a[28:31], v[164:167]\n" \
"v_accvgpr_write_b32 a202, v243\n" \
"v_accvgpr_write_b32 a203, v253\n" \
"v_mfma_f32_16x16x32_bf16 v[160:163], a[64:67], a[24:27], v[160:163]\n" \
"v_accvgpr_read_b32 v243, a204\n" \
"v_accvgpr_read_b32 v252, a205\n" \
"v_mfma_f32_16x16x32_bf16 v[160:163], a[68:71], a[28:31], v[160:163]\n" \
"s_waitcnt vmcnt(35) lgkmcnt(0)\n" \
"s_barrier\n" \
"v_mfma_f32_16x16x32_bf16 v[80:83], a[96:99], a[0:3], v[80:83]\n" \
"ds_read_b64_tr_b16 a[64:65], v232 offset:0\n" \
"v_accvgpr_read_b32 v253, a206\n" \
"v_mfma_f32_16x16x32_bf16 v[80:83], a[100:103], a[4:7], v[80:83]\n" \
"ds_read_b64_tr_b16 a[66:67], v232 offset:2048\n" \
"v_accvgpr_read_b32 v254, a207\n" \
"v_mfma_f32_16x16x32_bf16 v[84:87], a[104:107], a[0:3], v[84:87]\n" \
"s_add_u32 m0, s79, 49152\n" \
"ds_read_b64_tr_b16 a[68:69], v232 offset:8192\n" \
"v_mul_f32 v243, s31, v243\n" \
"v_mfma_f32_16x16x32_bf16 v[84:87], a[108:111], a[4:7], v[84:87]\n" \
"buffer_load_dwordx4 v228, s[52:55], s78 offen offset:0 lds\n" \
"v_mfma_f32_16x16x32_bf16 v[88:91], a[112:115], a[0:3], v[88:91]\n" \
"ds_read_b64_tr_b16 a[70:71], v232 offset:10240\n" \
"v_mul_f32 v252, s31, v252\n" \
"v_mfma_f32_16x16x32_bf16 v[88:91], a[116:119], a[4:7], v[88:91]\n" \
"ds_read_b64_tr_b16 a[72:73], v233 offset:0\n" \
"v_mul_f32 v253, s31, v253\n" \
"v_mfma_f32_16x16x32_bf16 v[92:95], a[120:123], a[0:3], v[92:95]\n" \
"ds_read_b64_tr_b16 a[74:75], v233 offset:2048\n" \
"v_mul_f32 v254, s31, v254\n" \
"v_mfma_f32_16x16x32_bf16 v[92:95], a[124:127], a[4:7], v[92:95]\n" \
"buffer_load_dwordx4 v229, s[52:55], s78 offen offset:1024 lds\n" \
"v_mfma_f32_16x16x32_bf16 v[124:127], a[120:123], a[8:11], v[124:127]\n" \
"s_waitcnt vmcnt(35)\n" \
"ds_read_b128 a[0:3], v230 offset:0\n" \
"v_cvt_pk_bf16_f32 v243, v243, v252\n" \
"v_mfma_f32_16x16x32_bf16 v[124:127], a[124:127], a[12:15], v[124:127]\n" \
"ds_read_b128 a[4:7], v231 offset:0\n" \
"v_cvt_pk_bf16_f32 v253, v253, v254\n" \
"v_mfma_f32_16x16x32_bf16 v[120:123], a[112:115], a[8:11], v[120:123]\n" \
"s_xor_b32 s78, s78, 0x80000\n" \
"ds_read_b64_tr_b16 a[76:77], v233 offset:8192\n" \
"v_accvgpr_write_b32 a204, v243\n" \
"v_mfma_f32_16x16x32_bf16 v[120:123], a[116:119], a[12:15], v[120:123]\n" \
"buffer_load_dwordx4 v228, s[52:55], s89 offen offset:2048 lds\n" \
"v_mfma_f32_16x16x32_bf16 v[116:119], a[104:107], a[8:11], v[116:119]\n" \
"ds_read_b64_tr_b16 a[78:79], v233 offset:10240\n" \
"v_accvgpr_write_b32 a205, v253\n" \
"v_mfma_f32_16x16x32_bf16 v[116:119], a[108:111], a[12:15], v[116:119]\n" \
"ds_read_b64_tr_b16 a[80:81], v232 offset:512\n" \
"v_accvgpr_read_b32 v243, a220\n" \
"v_mfma_f32_16x16x32_bf16 v[112:115], a[96:99], a[8:11], v[112:115]\n" \
"ds_read_b64_tr_b16 a[82:83], v232 offset:2560\n" \
"v_accvgpr_read_b32 v252, a221\n" \
"v_mfma_f32_16x16x32_bf16 v[112:115], a[100:103], a[12:15], v[112:115]\n" \
"buffer_load_dwordx4 v229, s[52:55], s89 offen offset:3072 lds\n" \
"v_mfma_f32_16x16x32_bf16 v[144:147], a[96:99], a[16:19], v[144:147]\n" \
"s_waitcnt vmcnt(35)\n" \
"s_xor_b32 s89, s89, 0x80000\n" \
"ds_read_b128 a[8:11], v230 offset:2048\n" \
"v_mfma_f32_16x16x32_bf16 v[144:147], a[100:103], a[20:23], v[144:147]\n" \
"s_add_u32 m0, s74, 65536\n" \
"ds_read_b128 a[12:15], v231 offset:2048\n" \
"v_accvgpr_read_b32 v253, a222\n" \
"v_mfma_f32_16x16x32_bf16 v[148:151], a[104:107], a[16:19], v[148:151]\n" \
"s_waitcnt lgkmcnt(6)\n" \
"ds_read_b64_tr_b16 a[84:85], v232 offset:8704\n" \
"v_accvgpr_read_b32 v254, a223\n" \
"v_mfma_f32_16x16x32_bf16 v[148:151], a[108:111], a[20:23], v[148:151]\n" \
"buffer_load_dwordx4 v224, s[48:51], s75 offen offset:0 lds\n" \
"v_mfma_f32_16x16x32_bf16 v[152:155], a[112:115], a[16:19], v[152:155]\n" \
"ds_read_b64_tr_b16 a[86:87], v232 offset:10752\n" \
"v_mul_f32 v243, s31, v243\n" \
"v_mfma_f32_16x16x32_bf16 v[152:155], a[116:119], a[20:23], v[152:155]\n" \
"ds_read_b64_tr_b16 a[88:89], v233 offset:512\n" \
"v_mul_f32 v252, s31, v252\n" \
"v_mfma_f32_16x16x32_bf16 v[156:159], a[120:123], a[16:19], v[156:159]\n" \
"ds_read_b64_tr_b16 a[90:91], v233 offset:2560\n" \
"v_mul_f32 v253, s31, v253\n" \
"v_mfma_f32_16x16x32_bf16 v[156:159], a[124:127], a[20:23], v[156:159]\n" \
"buffer_load_dwordx4 v225, s[48:51], s75 offen offset:1024 lds\n" \
"v_mfma_f32_16x16x32_bf16 v[188:191], a[120:123], a[24:27], v[188:191]\n" \
"s_waitcnt vmcnt(35)\n" \
"ds_read_b128 a[16:19], v230 offset:4096\n" \
"v_mul_f32 v254, s31, v254\n" \
"v_mfma_f32_16x16x32_bf16 v[188:191], a[124:127], a[28:31], v[188:191]\n" \
"ds_read_b128 a[20:23], v231 offset:4096\n" \
"v_cvt_pk_bf16_f32 v243, v243, v252\n" \
"v_mfma_f32_16x16x32_bf16 v[184:187], a[112:115], a[24:27], v[184:187]\n" \
"s_waitcnt lgkmcnt(6)\n" \
"ds_read_b64_tr_b16 a[92:93], v233 offset:8704\n" \
"v_cvt_pk_bf16_f32 v253, v253, v254\n" \
"v_mfma_f32_16x16x32_bf16 v[184:187], a[116:119], a[28:31], v[184:187]\n" \
"buffer_load_dwordx4 v226, s[48:51], s75 offen offset:2048 lds\n" \
"v_mfma_f32_16x16x32_bf16 v[180:183], a[104:107], a[24:27], v[180:183]\n" \
"ds_read_b64_tr_b16 a[94:95], v233 offset:10752\n" \
"v_accvgpr_write_b32 a206, v243\n" \
"v_mfma_f32_16x16x32_bf16 v[180:183], a[108:111], a[28:31], v[180:183]\n" \
"v_accvgpr_write_b32 a207, v253\n" \
"v_mul_f32 v32, s76, v32\n" \
"v_mfma_f32_16x16x32_bf16 v[176:179], a[96:99], a[24:27], v[176:179]\n" \
"v_mul_f32 v33, s76, v33\n" \
"v_mul_f32 v34, s76, v34\n" \
"v_mfma_f32_16x16x32_bf16 v[176:179], a[100:103], a[28:31], v[176:179]\n" \
"buffer_load_dwordx4 v227, s[48:51], s75 offen offset:3072 lds\n" \
"v_mfma_f32_16x16x32_bf16 v[64:67], a[64:67], a[0:3], v[64:67]\n" \
"s_waitcnt vmcnt(35)\n" \
"s_xor_b32 s75, s75, 0x80\n" \
"ds_read_b128 a[24:27], v230 offset:6144\n" \
"v_mfma_f32_16x16x32_bf16 v[64:67], a[68:71], a[4:7], v[64:67]\n" \
"s_add_u32 m0, s74, 69632\n" \
"ds_read_b128 a[28:31], v231 offset:6144\n" \
"v_mul_f32 v35, s76, v35\n" \
"v_mfma_f32_16x16x32_bf16 v[68:71], a[72:75], a[0:3], v[68:71]\n" \
"s_waitcnt lgkmcnt(4)\n" \
"ds_read_b64_tr_b16 a[96:97], v232 offset:1024\n" \
"v_cvt_pk_bf16_f32 v32, v32, v33\n" \
"v_mfma_f32_16x16x32_bf16 v[68:71], a[76:79], a[4:7], v[68:71]\n" \
"buffer_load_dwordx4 v224, s[48:51], s77 offen offset:0 lds\n" \
"v_mfma_f32_16x16x32_bf16 v[72:75], a[80:83], a[0:3], v[72:75]\n" \
"ds_read_b64_tr_b16 a[98:99], v232 offset:3072\n" \
"v_cvt_pk_bf16_f32 v33, v34, v35\n" \
"v_mfma_f32_16x16x32_bf16 v[72:75], a[84:87], a[4:7], v[72:75]\n" \
"s_waitcnt lgkmcnt(3)\n" \
"ds_read_b64_tr_b16 a[100:101], v232 offset:9216\n" \
"v_mul_f32 v36, s76, v36\n" \
"v_mfma_f32_16x16x32_bf16 v[76:79], a[88:91], a[0:3], v[76:79]\n" \
"ds_read_b64_tr_b16 a[102:103], v232 offset:11264\n" \
"v_mul_f32 v37, s76, v37\n" \
"v_mfma_f32_16x16x32_bf16 v[76:79], a[92:95], a[4:7], v[76:79]\n" \
"buffer_load_dwordx4 v225, s[48:51], s77 offen offset:1024 lds\n" \
"v_mfma_f32_16x16x32_bf16 v[108:111], a[88:91], a[8:11], v[108:111]\n" \
"ds_read_b64_tr_b16 a[104:105], v233 offset:1024\n" \
"v_mul_f32 v38, s76, v38\n" \
"v_mfma_f32_16x16x32_bf16 v[108:111], a[92:95], a[12:15], v[108:111]\n" \
"ds_read_b64_tr_b16 a[106:107], v233 offset:3072\n" \
"v_mul_f32 v39, s76, v39\n" \
"v_mfma_f32_16x16x32_bf16 v[104:107], a[80:83], a[8:11], v[104:107]\n" \
"s_waitcnt lgkmcnt(6)\n" \
"ds_read_b64_tr_b16 a[108:109], v233 offset:9216\n" \
"v_cvt_pk_bf16_f32 v34, v36, v37\n" \
"v_mfma_f32_16x16x32_bf16 v[104:107], a[84:87], a[12:15], v[104:107]\n" \
"buffer_load_dwordx4 v226, s[48:51], s77 offen offset:2048 lds\n" \
"v_mfma_f32_16x16x32_bf16 v[100:103], a[72:75], a[8:11], v[100:103]\n" \
"ds_read_b64_tr_b16 a[110:111], v233 offset:11264\n" \
"v_cvt_pk_bf16_f32 v35, v38, v39\n" \
"v_mfma_f32_16x16x32_bf16 v[100:103], a[76:79], a[12:15], v[100:103]\n" \
"ds_read_b64_tr_b16 a[112:113], v232 offset:1536\n" \
"v_mul_f32 v48, s76, v48\n" \
"v_mfma_f32_16x16x32_bf16 v[96:99], a[64:67], a[8:11], v[96:99]\n" \
"ds_read_b64_tr_b16 a[114:115], v232 offset:3584\n" \
"v_mul_f32 v49, s76, v49\n" \
"v_mfma_f32_16x16x32_bf16 v[96:99], a[68:71], a[12:15], v[96:99]\n" \
"buffer_load_dwordx4 v227, s[48:51], s77 offen offset:3072 lds\n" \
"v_mfma_f32_16x16x32_bf16 v[128:131], a[64:67], a[16:19], v[128:131]\n" \
"ds_read_b64_tr_b16 a[116:117], v232 offset:9728\n" \
"v_mul_f32 v50, s76, v50\n" \
"v_mfma_f32_16x16x32_bf16 v[128:131], a[68:71], a[20:23], v[128:131]\n" \
"ds_read_b64_tr_b16 a[118:119], v232 offset:11776\n" \
"v_mul_f32 v51, s76, v51\n" \
"v_mfma_f32_16x16x32_bf16 v[132:135], a[72:75], a[16:19], v[132:135]\n" \
"s_xor_b32 s77, s77, 0x80\n" \
"ds_read_b64_tr_b16 a[120:121], v233 offset:1536\n" \
"v_cvt_pk_bf16_f32 v48, v48, v49\n" \
"v_mfma_f32_16x16x32_bf16 v[132:135], a[76:79], a[20:23], v[132:135]\n" \
"v_cvt_pk_bf16_f32 v49, v50, v51\n" \
"v_mul_f32 v52, s76, v52\n" \
"v_mfma_f32_16x16x32_bf16 v[136:139], a[80:83], a[16:19], v[136:139]\n" \
"ds_read_b64_tr_b16 a[122:123], v233 offset:3584\n" \
"v_mul_f32 v53, s76, v53\n" \
"v_mfma_f32_16x16x32_bf16 v[136:139], a[84:87], a[20:23], v[136:139]\n" \
"ds_read_b64_tr_b16 a[124:125], v233 offset:9728\n" \
"v_mul_f32 v54, s76, v54\n" \
"v_mfma_f32_16x16x32_bf16 v[140:143], a[88:91], a[16:19], v[140:143]\n" \
"s_add_u32 s93, s87, 0\n" \
"ds_read_b64_tr_b16 a[126:127], v233 offset:11776\n" \
"v_mul_f32 v55, s76, v55\n" \
"v_mfma_f32_16x16x32_bf16 v[140:143], a[92:95], a[20:23], v[140:143]\n" \
"buffer_store_dwordx4 v[196:199], v236, s[56:59], s93 offen offset:0 sc1\n" \
"v_mfma_f32_16x16x32_bf16 v[172:175], a[88:91], a[24:27], v[172:175]\n" \
"v_cvt_pk_bf16_f32 v50, v52, v53\n" \
"v_cvt_pk_bf16_f32 v51, v54, v55\n" \
"v_mfma_f32_16x16x32_bf16 v[172:175], a[92:95], a[28:31], v[172:175]\n" \
"v_mul_f32 v40, s76, v40\n" \
"v_mul_f32 v41, s76, v41\n" \
"v_mfma_f32_16x16x32_bf16 v[168:171], a[80:83], a[24:27], v[168:171]\n" \
"v_mul_f32 v42, s76, v42\n" \
"v_mul_f32 v43, s76, v43\n" \
"v_mfma_f32_16x16x32_bf16 v[168:171], a[84:87], a[28:31], v[168:171]\n" \
"v_cvt_pk_bf16_f32 v40, v40, v41\n" \
"v_cvt_pk_bf16_f32 v41, v42, v43\n" \
"v_mfma_f32_16x16x32_bf16 v[164:167], a[72:75], a[24:27], v[164:167]\n" \
"v_mul_f32 v44, s76, v44\n" \
"v_mul_f32 v45, s76, v45\n" \
"v_mfma_f32_16x16x32_bf16 v[164:167], a[76:79], a[28:31], v[164:167]\n" \
"v_mul_f32 v46, s76, v46\n" \
"v_mul_f32 v47, s76, v47\n" \
"v_mfma_f32_16x16x32_bf16 v[160:163], a[64:67], a[24:27], v[160:163]\n" \
"v_cvt_pk_bf16_f32 v42, v44, v45\n" \
"v_cvt_pk_bf16_f32 v43, v46, v47\n" \
"v_mfma_f32_16x16x32_bf16 v[160:163], a[68:71], a[28:31], v[160:163]\n" \
"s_waitcnt vmcnt(35) lgkmcnt(0)\n" \
"s_barrier\n" \
"v_mfma_f32_16x16x32_bf16 v[80:83], a[96:99], a[0:3], v[80:83]\n" \
"ds_read_b64_tr_b16 a[64:65], v232 offset:16384\n" \
"v_mul_f32 v56, s76, v56\n" \
"v_mfma_f32_16x16x32_bf16 v[80:83], a[100:103], a[4:7], v[80:83]\n" \
"ds_read_b64_tr_b16 a[66:67], v232 offset:18432\n" \
"v_mul_f32 v57, s76, v57\n" \
"v_mfma_f32_16x16x32_bf16 v[84:87], a[104:107], a[0:3], v[84:87]\n" \
"s_add_u32 m0, s79, 0\n" \
"ds_read_b64_tr_b16 a[68:69], v232 offset:24576\n" \
"v_mul_f32 v58, s76, v58\n" \
"v_mfma_f32_16x16x32_bf16 v[84:87], a[108:111], a[4:7], v[84:87]\n" \
"buffer_load_dwordx4 v228, s[52:55], s78 offen offset:0 lds\n" \
"v_mfma_f32_16x16x32_bf16 v[88:91], a[112:115], a[0:3], v[88:91]\n" \
"ds_read_b64_tr_b16 a[70:71], v232 offset:26624\n" \
"v_mul_f32 v59, s76, v59\n" \
"v_mfma_f32_16x16x32_bf16 v[88:91], a[116:119], a[4:7], v[88:91]\n" \
"ds_read_b64_tr_b16 a[72:73], v233 offset:16384\n" \
"v_cvt_pk_bf16_f32 v56, v56, v57\n" \
"v_mfma_f32_16x16x32_bf16 v[92:95], a[120:123], a[0:3], v[92:95]\n" \
"ds_read_b64_tr_b16 a[74:75], v233 offset:18432\n" \
"v_cvt_pk_bf16_f32 v57, v58, v59\n" \
"v_mfma_f32_16x16x32_bf16 v[92:95], a[124:127], a[4:7], v[92:95]\n" \
"buffer_load_dwordx4 v229, s[52:55], s78 offen offset:1024 lds\n" \
"v_mfma_f32_16x16x32_bf16 v[124:127], a[120:123], a[8:11], v[124:127]\n" \
"s_waitcnt vmcnt(35)\n" \
"ds_read_b128 a[0:3], v230 offset:32768\n" \
"v_mul_f32 v60, s76, v60\n" \
"v_mfma_f32_16x16x32_bf16 v[124:127], a[124:127], a[12:15], v[124:127]\n" \
"ds_read_b128 a[4:7], v231 offset:32768\n" \
"v_mul_f32 v61, s76, v61\n" \
"v_mfma_f32_16x16x32_bf16 v[120:123], a[112:115], a[8:11], v[120:123]\n" \
"s_xor_b32 s78, s78, 0x780000\n" \
"ds_read_b64_tr_b16 a[76:77], v233 offset:24576\n" \
"v_mul_f32 v62, s76, v62\n" \
"v_mfma_f32_16x16x32_bf16 v[120:123], a[116:119], a[12:15], v[120:123]\n" \
"buffer_load_dwordx4 v228, s[52:55], s89 offen offset:2048 lds\n" \
"v_mfma_f32_16x16x32_bf16 v[116:119], a[104:107], a[8:11], v[116:119]\n" \
"ds_read_b64_tr_b16 a[78:79], v233 offset:26624\n" \
"v_mul_f32 v63, s76, v63\n" \
"v_mfma_f32_16x16x32_bf16 v[116:119], a[108:111], a[12:15], v[116:119]\n" \
"ds_read_b64_tr_b16 a[80:81], v232 offset:16896\n" \
"v_cvt_pk_bf16_f32 v58, v60, v61\n" \
"v_mfma_f32_16x16x32_bf16 v[112:115], a[96:99], a[8:11], v[112:115]\n" \
"ds_read_b64_tr_b16 a[82:83], v232 offset:18944\n" \
"v_cvt_pk_bf16_f32 v59, v62, v63\n" \
"v_mfma_f32_16x16x32_bf16 v[112:115], a[100:103], a[12:15], v[112:115]\n" \
"buffer_load_dwordx4 v229, s[52:55], s89 offen offset:3072 lds\n" \
"v_mfma_f32_16x16x32_bf16 v[144:147], a[96:99], a[16:19], v[144:147]\n" \
"s_waitcnt vmcnt(35)\n" \
"s_xor_b32 s89, s89, 0x780000\n" \
"ds_read_b128 a[8:11], v230 offset:34816\n" \
"v_mfma_f32_16x16x32_bf16 v[144:147], a[100:103], a[20:23], v[144:147]\n" \
"s_add_u32 m0, s74, 98304\n" \
"ds_read_b128 a[12:15], v231 offset:34816\n" \
"v_mul_f32 v0, s76, v0\n" \
"v_mfma_f32_16x16x32_bf16 v[148:151], a[104:107], a[16:19], v[148:151]\n" \
"s_waitcnt lgkmcnt(6)\n" \
"ds_read_b64_tr_b16 a[84:85], v232 offset:25088\n" \
"v_mul_f32 v1, s76, v1\n" \
"v_mfma_f32_16x16x32_bf16 v[148:151], a[108:111], a[20:23], v[148:151]\n" \
"buffer_load_dwordx4 v224, s[48:51], s75 offen offset:0 lds\n" \
"v_mfma_f32_16x16x32_bf16 v[152:155], a[112:115], a[16:19], v[152:155]\n" \
"ds_read_b64_tr_b16 a[86:87], v232 offset:27136\n" \
"v_mul_f32 v2, s76, v2\n" \
"v_mfma_f32_16x16x32_bf16 v[152:155], a[116:119], a[20:23], v[152:155]\n" \
"ds_read_b64_tr_b16 a[88:89], v233 offset:16896\n" \
"v_mul_f32 v3, s76, v3\n" \
"v_mfma_f32_16x16x32_bf16 v[156:159], a[120:123], a[16:19], v[156:159]\n" \
"ds_read_b64_tr_b16 a[90:91], v233 offset:18944\n" \
"v_cvt_pk_bf16_f32 v0, v0, v1\n" \
"v_mfma_f32_16x16x32_bf16 v[156:159], a[124:127], a[20:23], v[156:159]\n" \
"buffer_load_dwordx4 v225, s[48:51], s75 offen offset:1024 lds\n" \
"v_mfma_f32_16x16x32_bf16 v[188:191], a[120:123], a[24:27], v[188:191]\n" \
"s_waitcnt vmcnt(35)\n" \
"ds_read_b128 a[16:19], v230 offset:36864\n" \
"v_cvt_pk_bf16_f32 v1, v2, v3\n" \
"v_mfma_f32_16x16x32_bf16 v[188:191], a[124:127], a[28:31], v[188:191]\n" \
"ds_read_b128 a[20:23], v231 offset:36864\n" \
"v_mul_f32 v4, s76, v4\n" \
"v_mfma_f32_16x16x32_bf16 v[184:187], a[112:115], a[24:27], v[184:187]\n" \
"s_waitcnt lgkmcnt(6)\n" \
"ds_read_b64_tr_b16 a[92:93], v233 offset:25088\n" \
"v_mul_f32 v5, s76, v5\n" \
"v_mfma_f32_16x16x32_bf16 v[184:187], a[116:119], a[28:31], v[184:187]\n" \
"buffer_load_dwordx4 v226, s[48:51], s75 offen offset:2048 lds\n" \
"v_mfma_f32_16x16x32_bf16 v[180:183], a[104:107], a[24:27], v[180:183]\n" \
"ds_read_b64_tr_b16 a[94:95], v233 offset:27136\n" \
"v_mul_f32 v6, s76, v6\n" \
"v_mfma_f32_16x16x32_bf16 v[180:183], a[108:111], a[28:31], v[180:183]\n" \
"v_mul_f32 v7, s76, v7\n" \
"v_cvt_pk_bf16_f32 v2, v4, v5\n" \
"v_mfma_f32_16x16x32_bf16 v[176:179], a[96:99], a[24:27], v[176:179]\n" \
"v_cvt_pk_bf16_f32 v3, v6, v7\n" \
"v_mul_f32 v16, s76, v16\n" \
"v_mfma_f32_16x16x32_bf16 v[176:179], a[100:103], a[28:31], v[176:179]\n" \
"buffer_load_dwordx4 v227, s[48:51], s75 offen offset:3072 lds\n" \
"v_mfma_f32_16x16x32_bf16 v[64:67], a[64:67], a[0:3], v[64:67]\n" \
"s_waitcnt vmcnt(35)\n" \
"s_xor_b32 s75, s75, 0x780\n" \
"ds_read_b128 a[24:27], v230 offset:38912\n" \
"v_mfma_f32_16x16x32_bf16 v[64:67], a[68:71], a[4:7], v[64:67]\n" \
"s_add_u32 m0, s74, 102400\n" \
"ds_read_b128 a[28:31], v231 offset:38912\n" \
"v_mul_f32 v17, s76, v17\n" \
"v_mfma_f32_16x16x32_bf16 v[68:71], a[72:75], a[0:3], v[68:71]\n" \
"s_waitcnt lgkmcnt(4)\n" \
"ds_read_b64_tr_b16 a[96:97], v232 offset:17408\n" \
"v_mul_f32 v18, s76, v18\n" \
"v_mfma_f32_16x16x32_bf16 v[68:71], a[76:79], a[4:7], v[68:71]\n" \
"buffer_load_dwordx4 v224, s[48:51], s77 offen offset:0 lds\n" \
"v_mfma_f32_16x16x32_bf16 v[72:75], a[80:83], a[0:3], v[72:75]\n" \
"ds_read_b64_tr_b16 a[98:99], v232 offset:19456\n" \
"v_mul_f32 v19, s76, v19\n" \
"v_mfma_f32_16x16x32_bf16 v[72:75], a[84:87], a[4:7], v[72:75]\n" \
"s_waitcnt lgkmcnt(3)\n" \
"ds_read_b64_tr_b16 a[100:101], v232 offset:25600\n" \
"v_cvt_pk_bf16_f32 v16, v16, v17\n" \
"v_mfma_f32_16x16x32_bf16 v[76:79], a[88:91], a[0:3], v[76:79]\n" \
"ds_read_b64_tr_b16 a[102:103], v232 offset:27648\n" \
"v_cvt_pk_bf16_f32 v17, v18, v19\n" \
"v_mfma_f32_16x16x32_bf16 v[76:79], a[92:95], a[4:7], v[76:79]\n" \
"buffer_load_dwordx4 v225, s[48:51], s77 offen offset:1024 lds\n" \
"v_mfma_f32_16x16x32_bf16 v[108:111], a[88:91], a[8:11], v[108:111]\n" \
"ds_read_b64_tr_b16 a[104:105], v233 offset:17408\n" \
"v_mul_f32 v20, s76, v20\n" \
"v_mfma_f32_16x16x32_bf16 v[108:111], a[92:95], a[12:15], v[108:111]\n" \
"ds_read_b64_tr_b16 a[106:107], v233 offset:19456\n" \
"v_mul_f32 v21, s76, v21\n" \
"v_mfma_f32_16x16x32_bf16 v[104:107], a[80:83], a[8:11], v[104:107]\n" \
"s_waitcnt lgkmcnt(6)\n" \
"ds_read_b64_tr_b16 a[108:109], v233 offset:25600\n" \
"v_mul_f32 v22, s76, v22\n" \
"v_mfma_f32_16x16x32_bf16 v[104:107], a[84:87], a[12:15], v[104:107]\n" \
"buffer_load_dwordx4 v226, s[48:51], s77 offen offset:2048 lds\n" \
"v_mfma_f32_16x16x32_bf16 v[100:103], a[72:75], a[8:11], v[100:103]\n" \
"ds_read_b64_tr_b16 a[110:111], v233 offset:27648\n" \
"v_mul_f32 v23, s76, v23\n" \
"v_mfma_f32_16x16x32_bf16 v[100:103], a[76:79], a[12:15], v[100:103]\n" \
"ds_read_b64_tr_b16 a[112:113], v232 offset:17920\n" \
"v_cvt_pk_bf16_f32 v18, v20, v21\n" \
"v_mfma_f32_16x16x32_bf16 v[96:99], a[64:67], a[8:11], v[96:99]\n" \
"ds_read_b64_tr_b16 a[114:115], v232 offset:19968\n" \
"v_cvt_pk_bf16_f32 v19, v22, v23\n" \
"v_mfma_f32_16x16x32_bf16 v[96:99], a[68:71], a[12:15], v[96:99]\n" \
"buffer_load_dwordx4 v227, s[48:51], s77 offen offset:3072 lds\n" \
"v_mfma_f32_16x16x32_bf16 v[128:131], a[64:67], a[16:19], v[128:131]\n" \
"ds_read_b64_tr_b16 a[116:117], v232 offset:26112\n" \
"v_mul_f32 v8, s76, v8\n" \
"v_mfma_f32_16x16x32_bf16 v[128:131], a[68:71], a[20:23], v[128:131]\n" \
"ds_read_b64_tr_b16 a[118:119], v232 offset:28160\n" \
"v_mul_f32 v9, s76, v9\n" \
"v_mfma_f32_16x16x32_bf16 v[132:135], a[72:75], a[16:19], v[132:135]\n" \
"s_xor_b32 s77, s77, 0x780\n" \
"ds_read_b64_tr_b16 a[120:121], v233 offset:17920\n" \
"v_mul_f32 v10, s76, v10\n" \
"v_mfma_f32_16x16x32_bf16 v[132:135], a[76:79], a[20:23], v[132:135]\n" \
"v_mul_f32 v11, s76, v11\n" \
"v_cvt_pk_bf16_f32 v8, v8, v9\n" \
"v_mfma_f32_16x16x32_bf16 v[136:139], a[80:83], a[16:19], v[136:139]\n" \
"ds_read_b64_tr_b16 a[122:123], v233 offset:19968\n" \
"v_cvt_pk_bf16_f32 v9, v10, v11\n" \
"v_mfma_f32_16x16x32_bf16 v[136:139], a[84:87], a[20:23], v[136:139]\n" \
"ds_read_b64_tr_b16 a[124:125], v233 offset:26112\n" \
"v_mul_f32 v12, s76, v12\n" \
"v_mfma_f32_16x16x32_bf16 v[140:143], a[88:91], a[16:19], v[140:143]\n" \
"ds_read_b64_tr_b16 a[126:127], v233 offset:28160\n" \
"v_mul_f32 v13, s76, v13\n" \
"v_mfma_f32_16x16x32_bf16 v[140:143], a[92:95], a[20:23], v[140:143]\n" \
"buffer_store_dwordx4 v[212:215], v236, s[56:59], s93 offen offset:256 sc1\n" \
"v_mfma_f32_16x16x32_bf16 v[172:175], a[88:91], a[24:27], v[172:175]\n" \
"v_mul_f32 v14, s76, v14\n" \
"v_mul_f32 v15, s76, v15\n" \
"v_mfma_f32_16x16x32_bf16 v[172:175], a[92:95], a[28:31], v[172:175]\n" \
"v_cvt_pk_bf16_f32 v10, v12, v13\n" \
"v_cvt_pk_bf16_f32 v11, v14, v15\n" \
"v_mfma_f32_16x16x32_bf16 v[168:171], a[80:83], a[24:27], v[168:171]\n" \
"v_mul_f32 v24, s76, v24\n" \
"v_mul_f32 v25, s76, v25\n" \
"v_mfma_f32_16x16x32_bf16 v[168:171], a[84:87], a[28:31], v[168:171]\n" \
"v_mul_f32 v26, s76, v26\n" \
"v_mul_f32 v27, s76, v27\n" \
"v_mfma_f32_16x16x32_bf16 v[164:167], a[72:75], a[24:27], v[164:167]\n" \
"v_cvt_pk_bf16_f32 v24, v24, v25\n" \
"v_cvt_pk_bf16_f32 v25, v26, v27\n" \
"v_mfma_f32_16x16x32_bf16 v[164:167], a[76:79], a[28:31], v[164:167]\n" \
"v_mul_f32 v28, s76, v28\n" \
"v_mul_f32 v29, s76, v29\n" \
"v_mfma_f32_16x16x32_bf16 v[160:163], a[64:67], a[24:27], v[160:163]\n" \
"v_mul_f32 v30, s76, v30\n" \
"v_mul_f32 v31, s76, v31\n" \
"v_mfma_f32_16x16x32_bf16 v[160:163], a[68:71], a[28:31], v[160:163]\n" \
"s_waitcnt vmcnt(35) lgkmcnt(0)\n" \
"s_barrier\n" \
"v_mfma_f32_16x16x32_bf16 v[80:83], a[96:99], a[0:3], v[80:83]\n" \
"ds_read_b64_tr_b16 a[64:65], v232 offset:32768\n" \
"v_cvt_pk_bf16_f32 v26, v28, v29\n" \
"v_mfma_f32_16x16x32_bf16 v[80:83], a[100:103], a[4:7], v[80:83]\n" \
"ds_read_b64_tr_b16 a[66:67], v232 offset:34816\n" \
"v_cvt_pk_bf16_f32 v27, v30, v31\n" \
"v_mfma_f32_16x16x32_bf16 v[84:87], a[104:107], a[0:3], v[84:87]\n" \
"s_add_u32 m0, s79, 16384\n" \
"ds_read_b64_tr_b16 a[68:69], v232 offset:40960\n" \
"v_accvgpr_read_b32 v243, a160\n" \
"v_mfma_f32_16x16x32_bf16 v[84:87], a[108:111], a[4:7], v[84:87]\n" \
"buffer_load_dwordx4 v228, s[52:55], s78 offen offset:0 lds\n" \
"v_mfma_f32_16x16x32_bf16 v[88:91], a[112:115], a[0:3], v[88:91]\n" \
"ds_read_b64_tr_b16 a[70:71], v232 offset:43008\n" \
"v_accvgpr_read_b32 v252, a161\n" \
"v_mfma_f32_16x16x32_bf16 v[88:91], a[116:119], a[4:7], v[88:91]\n" \
"ds_read_b64_tr_b16 a[72:73], v233 offset:32768\n" \
"v_accvgpr_read_b32 v253, a162\n" \
"v_mfma_f32_16x16x32_bf16 v[92:95], a[120:123], a[0:3], v[92:95]\n" \
"ds_read_b64_tr_b16 a[74:75], v233 offset:34816\n" \
"v_accvgpr_read_b32 v254, a163\n" \
"v_mfma_f32_16x16x32_bf16 v[92:95], a[124:127], a[4:7], v[92:95]\n" \
"buffer_load_dwordx4 v229, s[52:55], s78 offen offset:1024 lds\n" \
"v_mfma_f32_16x16x32_bf16 v[124:127], a[120:123], a[8:11], v[124:127]\n" \
"s_waitcnt vmcnt(35)\n" \
"ds_read_b128 a[0:3], v250 offset:0\n" \
"v_mul_f32 v243, s31, v243\n" \
"v_mfma_f32_16x16x32_bf16 v[124:127], a[124:127], a[12:15], v[124:127]\n" \
"ds_read_b128 a[4:7], v251 offset:0\n" \
"v_mul_f32 v252, s31, v252\n" \
"v_mfma_f32_16x16x32_bf16 v[120:123], a[112:115], a[8:11], v[120:123]\n" \
"s_xor_b32 s78, s78, 0x80000\n" \
"ds_read_b64_tr_b16 a[76:77], v233 offset:40960\n" \
"v_mul_f32 v253, s31, v253\n" \
"v_mfma_f32_16x16x32_bf16 v[120:123], a[116:119], a[12:15], v[120:123]\n" \
"buffer_load_dwordx4 v228, s[52:55], s89 offen offset:2048 lds\n" \
"v_mfma_f32_16x16x32_bf16 v[116:119], a[104:107], a[8:11], v[116:119]\n" \
"ds_read_b64_tr_b16 a[78:79], v233 offset:43008\n" \
"v_mul_f32 v254, s31, v254\n" \
"v_mfma_f32_16x16x32_bf16 v[116:119], a[108:111], a[12:15], v[116:119]\n" \
"ds_read_b64_tr_b16 a[80:81], v232 offset:33280\n" \
"v_cvt_pk_bf16_f32 v36, v243, v252\n" \
"v_mfma_f32_16x16x32_bf16 v[112:115], a[96:99], a[8:11], v[112:115]\n" \
"ds_read_b64_tr_b16 a[82:83], v232 offset:35328\n" \
"v_cvt_pk_bf16_f32 v37, v253, v254\n" \
"v_mfma_f32_16x16x32_bf16 v[112:115], a[100:103], a[12:15], v[112:115]\n" \
"buffer_load_dwordx4 v229, s[52:55], s89 offen offset:3072 lds\n" \
"v_mfma_f32_16x16x32_bf16 v[144:147], a[96:99], a[16:19], v[144:147]\n" \
"s_waitcnt vmcnt(35)\n" \
"s_xor_b32 s89, s89, 0x80000\n" \
"ds_read_b128 a[8:11], v250 offset:2048\n" \
"v_mfma_f32_16x16x32_bf16 v[144:147], a[100:103], a[20:23], v[144:147]\n" \
"s_add_u32 m0, s74, 131072\n" \
"ds_read_b128 a[12:15], v251 offset:2048\n" \
"v_accvgpr_read_b32 v243, a176\n" \
"v_mfma_f32_16x16x32_bf16 v[148:151], a[104:107], a[16:19], v[148:151]\n" \
"s_waitcnt lgkmcnt(6)\n" \
"ds_read_b64_tr_b16 a[84:85], v232 offset:41472\n" \
"v_accvgpr_read_b32 v252, a177\n" \
"v_mfma_f32_16x16x32_bf16 v[148:151], a[108:111], a[20:23], v[148:151]\n" \
"buffer_load_dwordx4 v224, s[48:51], s75 offen offset:0 lds\n" \
"v_mfma_f32_16x16x32_bf16 v[152:155], a[112:115], a[16:19], v[152:155]\n" \
"ds_read_b64_tr_b16 a[86:87], v232 offset:43520\n" \
"v_accvgpr_read_b32 v253, a178\n" \
"v_mfma_f32_16x16x32_bf16 v[152:155], a[116:119], a[20:23], v[152:155]\n" \
"ds_read_b64_tr_b16 a[88:89], v233 offset:33280\n" \
"v_accvgpr_read_b32 v254, a179\n" \
"v_mfma_f32_16x16x32_bf16 v[156:159], a[120:123], a[16:19], v[156:159]\n" \
"ds_read_b64_tr_b16 a[90:91], v233 offset:35328\n" \
"v_mul_f32 v243, s31, v243\n" \
"v_mfma_f32_16x16x32_bf16 v[156:159], a[124:127], a[20:23], v[156:159]\n" \
"buffer_load_dwordx4 v225, s[48:51], s75 offen offset:1024 lds\n" \
"v_mfma_f32_16x16x32_bf16 v[188:191], a[120:123], a[24:27], v[188:191]\n" \
"s_waitcnt vmcnt(35)\n" \
"ds_read_b128 a[16:19], v250 offset:4096\n" \
"v_mul_f32 v252, s31, v252\n" \
"v_mfma_f32_16x16x32_bf16 v[188:191], a[124:127], a[28:31], v[188:191]\n" \
"ds_read_b128 a[20:23], v251 offset:4096\n" \
"v_mul_f32 v253, s31, v253\n" \
"v_mfma_f32_16x16x32_bf16 v[184:187], a[112:115], a[24:27], v[184:187]\n" \
"s_waitcnt lgkmcnt(6)\n" \
"ds_read_b64_tr_b16 a[92:93], v233 offset:41472\n" \
"v_mul_f32 v254, s31, v254\n" \
"v_mfma_f32_16x16x32_bf16 v[184:187], a[116:119], a[28:31], v[184:187]\n" \
"buffer_load_dwordx4 v226, s[48:51], s75 offen offset:2048 lds\n" \
"v_mfma_f32_16x16x32_bf16 v[180:183], a[104:107], a[24:27], v[180:183]\n" \
"ds_read_b64_tr_b16 a[94:95], v233 offset:43520\n" \
"v_cvt_pk_bf16_f32 v38, v243, v252\n" \
"v_mfma_f32_16x16x32_bf16 v[180:183], a[108:111], a[28:31], v[180:183]\n" \
"v_cvt_pk_bf16_f32 v39, v253, v254\n" \
"v_accvgpr_read_b32 v243, a164\n" \
"v_mfma_f32_16x16x32_bf16 v[176:179], a[96:99], a[24:27], v[176:179]\n" \
"v_accvgpr_read_b32 v252, a165\n" \
"v_accvgpr_read_b32 v253, a166\n" \
"v_mfma_f32_16x16x32_bf16 v[176:179], a[100:103], a[28:31], v[176:179]\n" \
"buffer_load_dwordx4 v227, s[48:51], s75 offen offset:3072 lds\n" \
"v_mfma_f32_16x16x32_bf16 v[64:67], a[64:67], a[0:3], v[64:67]\n" \
"s_waitcnt vmcnt(35)\n" \
"s_xor_b32 s75, s75, 0x80\n" \
"ds_read_b128 a[24:27], v250 offset:6144\n" \
"v_mfma_f32_16x16x32_bf16 v[64:67], a[68:71], a[4:7], v[64:67]\n" \
"s_add_u32 m0, s74, 135168\n" \
"ds_read_b128 a[28:31], v251 offset:6144\n" \
"v_accvgpr_read_b32 v254, a167\n" \
"v_mfma_f32_16x16x32_bf16 v[68:71], a[72:75], a[0:3], v[68:71]\n" \
"s_waitcnt lgkmcnt(4)\n" \
"ds_read_b64_tr_b16 a[96:97], v232 offset:33792\n" \
"v_mul_f32 v243, s31, v243\n" \
"v_mfma_f32_16x16x32_bf16 v[68:71], a[76:79], a[4:7], v[68:71]\n" \
"buffer_load_dwordx4 v224, s[48:51], s77 offen offset:0 lds\n" \
"v_mfma_f32_16x16x32_bf16 v[72:75], a[80:83], a[0:3], v[72:75]\n" \
"ds_read_b64_tr_b16 a[98:99], v232 offset:35840\n" \
"v_mul_f32 v252, s31, v252\n" \
"v_mfma_f32_16x16x32_bf16 v[72:75], a[84:87], a[4:7], v[72:75]\n" \
"s_waitcnt lgkmcnt(3)\n" \
"ds_read_b64_tr_b16 a[100:101], v232 offset:41984\n" \
"v_mul_f32 v253, s31, v253\n" \
"v_mfma_f32_16x16x32_bf16 v[76:79], a[88:91], a[0:3], v[76:79]\n" \
"ds_read_b64_tr_b16 a[102:103], v232 offset:44032\n" \
"v_mul_f32 v254, s31, v254\n" \
"v_mfma_f32_16x16x32_bf16 v[76:79], a[92:95], a[4:7], v[76:79]\n" \
"buffer_load_dwordx4 v225, s[48:51], s77 offen offset:1024 lds\n" \
"v_mfma_f32_16x16x32_bf16 v[108:111], a[88:91], a[8:11], v[108:111]\n" \
"ds_read_b64_tr_b16 a[104:105], v233 offset:33792\n" \
"v_cvt_pk_bf16_f32 v52, v243, v252\n" \
"v_mfma_f32_16x16x32_bf16 v[108:111], a[92:95], a[12:15], v[108:111]\n" \
"ds_read_b64_tr_b16 a[106:107], v233 offset:35840\n" \
"v_cvt_pk_bf16_f32 v53, v253, v254\n" \
"v_mfma_f32_16x16x32_bf16 v[104:107], a[80:83], a[8:11], v[104:107]\n" \
"s_waitcnt lgkmcnt(6)\n" \
"ds_read_b64_tr_b16 a[108:109], v233 offset:41984\n" \
"v_accvgpr_read_b32 v243, a180\n" \
"Lpg3342_%=:\n" \
"s_cbranch_execz Lpg5604_%=\n" \
"v_mfma_f32_16x16x32_bf16 v[104:107], a[84:87], a[12:15], v[104:107]\n" \
"buffer_load_dwordx4 v226, s[48:51], s77 offen offset:2048 lds\n" \
"v_mfma_f32_16x16x32_bf16 v[100:103], a[72:75], a[8:11], v[100:103]\n" \
"ds_read_b64_tr_b16 a[110:111], v233 offset:44032\n" \
"v_accvgpr_read_b32 v252, a181\n" \
"v_mfma_f32_16x16x32_bf16 v[100:103], a[76:79], a[12:15], v[100:103]\n" \
"ds_read_b64_tr_b16 a[112:113], v232 offset:34304\n" \
"v_accvgpr_read_b32 v253, a182\n" \
"v_mfma_f32_16x16x32_bf16 v[96:99], a[64:67], a[8:11], v[96:99]\n" \
"ds_read_b64_tr_b16 a[114:115], v232 offset:36352\n" \
"v_accvgpr_read_b32 v254, a183\n" \
"v_mfma_f32_16x16x32_bf16 v[96:99], a[68:71], a[12:15], v[96:99]\n" \
"buffer_load_dwordx4 v227, s[48:51], s77 offen offset:3072 lds\n" \
"v_mfma_f32_16x16x32_bf16 v[128:131], a[64:67], a[16:19], v[128:131]\n" \
"ds_read_b64_tr_b16 a[116:117], v232 offset:42496\n" \
"v_mul_f32 v243, s31, v243\n" \
"v_mfma_f32_16x16x32_bf16 v[128:131], a[68:71], a[20:23], v[128:131]\n" \
"ds_read_b64_tr_b16 a[118:119], v232 offset:44544\n" \
"v_mul_f32 v252, s31, v252\n" \
"v_mfma_f32_16x16x32_bf16 v[132:135], a[72:75], a[16:19], v[132:135]\n" \
"s_xor_b32 s77, s77, 0x80\n" \
"ds_read_b64_tr_b16 a[120:121], v233 offset:34304\n" \
"v_mul_f32 v253, s31, v253\n" \
"v_mfma_f32_16x16x32_bf16 v[132:135], a[76:79], a[20:23], v[132:135]\n" \
"v_mul_f32 v254, s31, v254\n" \
"v_cvt_pk_bf16_f32 v54, v243, v252\n" \
"v_mfma_f32_16x16x32_bf16 v[136:139], a[80:83], a[16:19], v[136:139]\n" \
"ds_read_b64_tr_b16 a[122:123], v233 offset:36352\n" \
"v_cvt_pk_bf16_f32 v55, v253, v254\n" \
"v_mfma_f32_16x16x32_bf16 v[136:139], a[84:87], a[20:23], v[136:139]\n" \
"ds_read_b64_tr_b16 a[124:125], v233 offset:42496\n" \
"v_accvgpr_read_b32 v243, a168\n" \
"v_mfma_f32_16x16x32_bf16 v[140:143], a[88:91], a[16:19], v[140:143]\n" \
"ds_read_b64_tr_b16 a[126:127], v233 offset:44544\n" \
"v_accvgpr_read_b32 v252, a169\n" \
"v_mfma_f32_16x16x32_bf16 v[140:143], a[92:95], a[20:23], v[140:143]\n" \
"buffer_store_dwordx4 v[204:207], v236, s[56:59], s93 offen offset:2048 sc1\n" \
"v_mfma_f32_16x16x32_bf16 v[172:175], a[88:91], a[24:27], v[172:175]\n" \
"v_accvgpr_read_b32 v253, a170\n" \
"v_accvgpr_read_b32 v254, a171\n" \
"v_mfma_f32_16x16x32_bf16 v[172:175], a[92:95], a[28:31], v[172:175]\n" \
"v_mul_f32 v243, s31, v243\n" \
"v_mul_f32 v252, s31, v252\n" \
"v_mfma_f32_16x16x32_bf16 v[168:171], a[80:83], a[24:27], v[168:171]\n" \
"v_mul_f32 v253, s31, v253\n" \
"v_mul_f32 v254, s31, v254\n" \
"v_mfma_f32_16x16x32_bf16 v[168:171], a[84:87], a[28:31], v[168:171]\n" \
"v_cvt_pk_bf16_f32 v44, v243, v252\n" \
"v_cvt_pk_bf16_f32 v45, v253, v254\n" \
"v_mfma_f32_16x16x32_bf16 v[164:167], a[72:75], a[24:27], v[164:167]\n" \
"v_accvgpr_read_b32 v243, a184\n" \
"v_accvgpr_read_b32 v252, a185\n" \
"v_mfma_f32_16x16x32_bf16 v[164:167], a[76:79], a[28:31], v[164:167]\n" \
"v_accvgpr_read_b32 v253, a186\n" \
"v_accvgpr_read_b32 v254, a187\n" \
"v_mfma_f32_16x16x32_bf16 v[160:163], a[64:67], a[24:27], v[160:163]\n" \
"v_mul_f32 v243, s31, v243\n" \
"v_mul_f32 v252, s31, v252\n" \
"v_mfma_f32_16x16x32_bf16 v[160:163], a[68:71], a[28:31], v[160:163]\n" \
"s_waitcnt vmcnt(35) lgkmcnt(0)\n" \
"s_barrier\n" \
"v_mfma_f32_16x16x32_bf16 v[80:83], a[96:99], a[0:3], v[80:83]\n" \
"ds_read_b64_tr_b16 a[64:65], v232 offset:49152\n" \
"v_mul_f32 v253, s31, v253\n" \
"v_mfma_f32_16x16x32_bf16 v[80:83], a[100:103], a[4:7], v[80:83]\n" \
"ds_read_b64_tr_b16 a[66:67], v232 offset:51200\n" \
"v_mul_f32 v254, s31, v254\n" \
"v_mfma_f32_16x16x32_bf16 v[84:87], a[104:107], a[0:3], v[84:87]\n" \
"s_add_u32 m0, s79, 32768\n" \
"ds_read_b64_tr_b16 a[68:69], v232 offset:57344\n" \
"v_cvt_pk_bf16_f32 v46, v243, v252\n" \
"v_mfma_f32_16x16x32_bf16 v[84:87], a[108:111], a[4:7], v[84:87]\n" \
"buffer_load_dwordx4 v228, s[52:55], s78 offen offset:0 lds\n" \
"v_mfma_f32_16x16x32_bf16 v[88:91], a[112:115], a[0:3], v[88:91]\n" \
"ds_read_b64_tr_b16 a[70:71], v232 offset:59392\n" \
"v_cvt_pk_bf16_f32 v47, v253, v254\n" \
"v_mfma_f32_16x16x32_bf16 v[88:91], a[116:119], a[4:7], v[88:91]\n" \
"ds_read_b64_tr_b16 a[72:73], v233 offset:49152\n" \
"v_accvgpr_read_b32 v243, a172\n" \
"v_mfma_f32_16x16x32_bf16 v[92:95], a[120:123], a[0:3], v[92:95]\n" \
"ds_read_b64_tr_b16 a[74:75], v233 offset:51200\n" \
"v_accvgpr_read_b32 v252, a173\n" \
"v_mfma_f32_16x16x32_bf16 v[92:95], a[124:127], a[4:7], v[92:95]\n" \
"buffer_load_dwordx4 v229, s[52:55], s78 offen offset:1024 lds\n" \
"v_mfma_f32_16x16x32_bf16 v[124:127], a[120:123], a[8:11], v[124:127]\n" \
"s_waitcnt vmcnt(35)\n" \
"ds_read_b128 a[0:3], v230 offset:0\n" \
"v_accvgpr_read_b32 v253, a174\n" \
"v_mfma_f32_16x16x32_bf16 v[124:127], a[124:127], a[12:15], v[124:127]\n" \
"ds_read_b128 a[4:7], v231 offset:0\n" \
"v_accvgpr_read_b32 v254, a175\n" \
"v_mfma_f32_16x16x32_bf16 v[120:123], a[112:115], a[8:11], v[120:123]\n" \
"s_xor_b32 s78, s78, 0x180000\n" \
"ds_read_b64_tr_b16 a[76:77], v233 offset:57344\n" \
"v_mul_f32 v243, s31, v243\n" \
"v_mfma_f32_16x16x32_bf16 v[120:123], a[116:119], a[12:15], v[120:123]\n" \
"buffer_load_dwordx4 v228, s[52:55], s89 offen offset:2048 lds\n" \
"v_mfma_f32_16x16x32_bf16 v[116:119], a[104:107], a[8:11], v[116:119]\n" \
"ds_read_b64_tr_b16 a[78:79], v233 offset:59392\n" \
"v_mul_f32 v252, s31, v252\n" \
"v_mfma_f32_16x16x32_bf16 v[116:119], a[108:111], a[12:15], v[116:119]\n" \
"ds_read_b64_tr_b16 a[80:81], v232 offset:49664\n" \
"v_mul_f32 v253, s31, v253\n" \
"v_mfma_f32_16x16x32_bf16 v[112:115], a[96:99], a[8:11], v[112:115]\n" \
"ds_read_b64_tr_b16 a[82:83], v232 offset:51712\n" \
"v_mul_f32 v254, s31, v254\n" \
"v_mfma_f32_16x16x32_bf16 v[112:115], a[100:103], a[12:15], v[112:115]\n" \
"buffer_load_dwordx4 v229, s[52:55], s89 offen offset:3072 lds\n" \
"v_mfma_f32_16x16x32_bf16 v[144:147], a[96:99], a[16:19], v[144:147]\n" \
"s_waitcnt vmcnt(35)\n" \
"s_xor_b32 s89, s89, 0x180000\n" \
"ds_read_b128 a[8:11], v230 offset:2048\n" \
"v_mfma_f32_16x16x32_bf16 v[144:147], a[100:103], a[20:23], v[144:147]\n" \
"s_add_u32 m0, s74, 65536\n" \
"ds_read_b128 a[12:15], v231 offset:2048\n" \
"v_cvt_pk_bf16_f32 v60, v243, v252\n" \
"v_mfma_f32_16x16x32_bf16 v[148:151], a[104:107], a[16:19], v[148:151]\n" \
"s_waitcnt lgkmcnt(6)\n" \
"ds_read_b64_tr_b16 a[84:85], v232 offset:57856\n" \
"v_cvt_pk_bf16_f32 v61, v253, v254\n" \
"v_mfma_f32_16x16x32_bf16 v[148:151], a[108:111], a[20:23], v[148:151]\n" \
"buffer_load_dwordx4 v224, s[48:51], s75 offen offset:0 lds\n" \
"v_mfma_f32_16x16x32_bf16 v[152:155], a[112:115], a[16:19], v[152:155]\n" \
"ds_read_b64_tr_b16 a[86:87], v232 offset:59904\n" \
"v_accvgpr_read_b32 v243, a188\n" \
"v_mfma_f32_16x16x32_bf16 v[152:155], a[116:119], a[20:23], v[152:155]\n" \
"ds_read_b64_tr_b16 a[88:89], v233 offset:49664\n" \
"v_accvgpr_read_b32 v252, a189\n" \
"v_mfma_f32_16x16x32_bf16 v[156:159], a[120:123], a[16:19], v[156:159]\n" \
"ds_read_b64_tr_b16 a[90:91], v233 offset:51712\n" \
"v_accvgpr_read_b32 v253, a190\n" \
"v_mfma_f32_16x16x32_bf16 v[156:159], a[124:127], a[20:23], v[156:159]\n" \
"buffer_load_dwordx4 v225, s[48:51], s75 offen offset:1024 lds\n" \
"v_mfma_f32_16x16x32_bf16 v[188:191], a[120:123], a[24:27], v[188:191]\n" \
"s_waitcnt vmcnt(35)\n" \
"ds_read_b128 a[16:19], v230 offset:4096\n" \
"v_accvgpr_read_b32 v254, a191\n" \
"v_mfma_f32_16x16x32_bf16 v[188:191], a[124:127], a[28:31], v[188:191]\n" \
"ds_read_b128 a[20:23], v231 offset:4096\n" \
"v_mul_f32 v243, s31, v243\n" \
"v_mfma_f32_16x16x32_bf16 v[184:187], a[112:115], a[24:27], v[184:187]\n" \
"s_waitcnt lgkmcnt(6)\n" \
"ds_read_b64_tr_b16 a[92:93], v233 offset:57856\n" \
"v_mul_f32 v252, s31, v252\n" \
"v_mfma_f32_16x16x32_bf16 v[184:187], a[116:119], a[28:31], v[184:187]\n" \
"buffer_load_dwordx4 v226, s[48:51], s75 offen offset:2048 lds\n" \
"v_mfma_f32_16x16x32_bf16 v[180:183], a[104:107], a[24:27], v[180:183]\n" \
"ds_read_b64_tr_b16 a[94:95], v233 offset:59904\n" \
"v_mul_f32 v253, s31, v253\n" \
"v_mfma_f32_16x16x32_bf16 v[180:183], a[108:111], a[28:31], v[180:183]\n" \
"v_mul_f32 v254, s31, v254\n" \
"v_cvt_pk_bf16_f32 v62, v243, v252\n" \
"v_mfma_f32_16x16x32_bf16 v[176:179], a[96:99], a[24:27], v[176:179]\n" \
"v_cvt_pk_bf16_f32 v63, v253, v254\n" \
"v_accvgpr_read_b32 v243, a128\n" \
"v_mfma_f32_16x16x32_bf16 v[176:179], a[100:103], a[28:31], v[176:179]\n" \
"buffer_load_dwordx4 v227, s[48:51], s75 offen offset:3072 lds\n" \
"v_mfma_f32_16x16x32_bf16 v[64:67], a[64:67], a[0:3], v[64:67]\n" \
"s_waitcnt vmcnt(35)\n" \
"s_xor_b32 s75, s75, 0x180\n" \
"ds_read_b128 a[24:27], v230 offset:6144\n" \
"v_mfma_f32_16x16x32_bf16 v[64:67], a[68:71], a[4:7], v[64:67]\n" \
"s_add_u32 m0, s74, 69632\n" \
"ds_read_b128 a[28:31], v231 offset:6144\n" \
"v_accvgpr_read_b32 v252, a129\n" \
"v_mfma_f32_16x16x32_bf16 v[68:71], a[72:75], a[0:3], v[68:71]\n" \
"s_waitcnt lgkmcnt(4)\n" \
"ds_read_b64_tr_b16 a[96:97], v232 offset:50176\n" \
"v_accvgpr_read_b32 v253, a130\n" \
"v_mfma_f32_16x16x32_bf16 v[68:71], a[76:79], a[4:7], v[68:71]\n" \
"buffer_load_dwordx4 v224, s[48:51], s77 offen offset:0 lds\n" \
"v_mfma_f32_16x16x32_bf16 v[72:75], a[80:83], a[0:3], v[72:75]\n" \
"ds_read_b64_tr_b16 a[98:99], v232 offset:52224\n" \
"v_accvgpr_read_b32 v254, a131\n" \
"v_mfma_f32_16x16x32_bf16 v[72:75], a[84:87], a[4:7], v[72:75]\n" \
"s_waitcnt lgkmcnt(3)\n" \
"ds_read_b64_tr_b16 a[100:101], v232 offset:58368\n" \
"v_mul_f32 v243, s31, v243\n" \
"v_mfma_f32_16x16x32_bf16 v[76:79], a[88:91], a[0:3], v[76:79]\n" \
"ds_read_b64_tr_b16 a[102:103], v232 offset:60416\n" \
"v_mul_f32 v252, s31, v252\n" \
"v_mfma_f32_16x16x32_bf16 v[76:79], a[92:95], a[4:7], v[76:79]\n" \
"buffer_load_dwordx4 v225, s[48:51], s77 offen offset:1024 lds\n" \
"v_mfma_f32_16x16x32_bf16 v[108:111], a[88:91], a[8:11], v[108:111]\n" \
"ds_read_b64_tr_b16 a[104:105], v233 offset:50176\n" \
"v_mul_f32 v253, s31, v253\n" \
"v_mfma_f32_16x16x32_bf16 v[108:111], a[92:95], a[12:15], v[108:111]\n" \
"ds_read_b64_tr_b16 a[106:107], v233 offset:52224\n" \
"v_mul_f32 v254, s31, v254\n" \
"v_mfma_f32_16x16x32_bf16 v[104:107], a[80:83], a[8:11], v[104:107]\n" \
"s_waitcnt lgkmcnt(6)\n" \
"ds_read_b64_tr_b16 a[108:109], v233 offset:58368\n" \
"v_cvt_pk_bf16_f32 v4, v243, v252\n" \
"v_mfma_f32_16x16x32_bf16 v[104:107], a[84:87], a[12:15], v[104:107]\n" \
"buffer_load_dwordx4 v226, s[48:51], s77 offen offset:2048 lds\n" \
"v_mfma_f32_16x16x32_bf16 v[100:103], a[72:75], a[8:11], v[100:103]\n" \
"ds_read_b64_tr_b16 a[110:111], v233 offset:60416\n" \
"v_cvt_pk_bf16_f32 v5, v253, v254\n" \
"v_mfma_f32_16x16x32_bf16 v[100:103], a[76:79], a[12:15], v[100:103]\n" \
"ds_read_b64_tr_b16 a[112:113], v232 offset:50688\n" \
"v_accvgpr_read_b32 v243, a144\n" \
"v_mfma_f32_16x16x32_bf16 v[96:99], a[64:67], a[8:11], v[96:99]\n" \
"ds_read_b64_tr_b16 a[114:115], v232 offset:52736\n" \
"v_accvgpr_read_b32 v252, a145\n" \
"v_mfma_f32_16x16x32_bf16 v[96:99], a[68:71], a[12:15], v[96:99]\n" \
"buffer_load_dwordx4 v227, s[48:51], s77 offen offset:3072 lds\n" \
"v_mfma_f32_16x16x32_bf16 v[128:131], a[64:67], a[16:19], v[128:131]\n" \
"ds_read_b64_tr_b16 a[116:117], v232 offset:58880\n" \
"v_accvgpr_read_b32 v253, a146\n" \
"v_mfma_f32_16x16x32_bf16 v[128:131], a[68:71], a[20:23], v[128:131]\n" \
"ds_read_b64_tr_b16 a[118:119], v232 offset:60928\n" \
"v_accvgpr_read_b32 v254, a147\n" \
"v_mfma_f32_16x16x32_bf16 v[132:135], a[72:75], a[16:19], v[132:135]\n" \
"s_xor_b32 s77, s77, 0x180\n" \
"ds_read_b64_tr_b16 a[120:121], v233 offset:50688\n" \
"v_mul_f32 v243, s31, v243\n" \
"v_mfma_f32_16x16x32_bf16 v[132:135], a[76:79], a[20:23], v[132:135]\n" \
"v_mul_f32 v252, s31, v252\n" \
"v_mul_f32 v253, s31, v253\n" \
"v_mfma_f32_16x16x32_bf16 v[136:139], a[80:83], a[16:19], v[136:139]\n" \
"ds_read_b64_tr_b16 a[122:123], v233 offset:52736\n" \
"v_mul_f32 v254, s31, v254\n" \
"v_mfma_f32_16x16x32_bf16 v[136:139], a[84:87], a[20:23], v[136:139]\n" \
"ds_read_b64_tr_b16 a[124:125], v233 offset:58880\n" \
"v_cvt_pk_bf16_f32 v6, v243, v252\n" \
"v_mfma_f32_16x16x32_bf16 v[140:143], a[88:91], a[16:19], v[140:143]\n" \
"ds_read_b64_tr_b16 a[126:127], v233 offset:60928\n" \
"v_cvt_pk_bf16_f32 v7, v253, v254\n" \
"v_mfma_f32_16x16x32_bf16 v[140:143], a[92:95], a[20:23], v[140:143]\n" \
"buffer_store_dwordx4 v[220:223], v236, s[56:59], s93 offen offset:2304 sc1\n" \
"v_mfma_f32_16x16x32_bf16 v[172:175], a[88:91], a[24:27], v[172:175]\n" \
"v_accvgpr_read_b32 v243, a132\n" \
"v_accvgpr_read_b32 v252, a133\n" \
"v_mfma_f32_16x16x32_bf16 v[172:175], a[92:95], a[28:31], v[172:175]\n" \
"v_accvgpr_read_b32 v253, a134\n" \
"v_accvgpr_read_b32 v254, a135\n" \
"v_mfma_f32_16x16x32_bf16 v[168:171], a[80:83], a[24:27], v[168:171]\n" \
"v_mul_f32 v243, s31, v243\n" \
"v_mul_f32 v252, s31, v252\n" \
"v_mfma_f32_16x16x32_bf16 v[168:171], a[84:87], a[28:31], v[168:171]\n" \
"v_mul_f32 v253, s31, v253\n" \
"v_mul_f32 v254, s31, v254\n" \
"v_mfma_f32_16x16x32_bf16 v[164:167], a[72:75], a[24:27], v[164:167]\n" \
"v_cvt_pk_bf16_f32 v20, v243, v252\n" \
"v_cvt_pk_bf16_f32 v21, v253, v254\n" \
"v_mfma_f32_16x16x32_bf16 v[164:167], a[76:79], a[28:31], v[164:167]\n" \
"v_accvgpr_read_b32 v243, a148\n" \
"v_accvgpr_read_b32 v252, a149\n" \
"v_mfma_f32_16x16x32_bf16 v[160:163], a[64:67], a[24:27], v[160:163]\n" \
"v_accvgpr_read_b32 v253, a150\n" \
"v_accvgpr_read_b32 v254, a151\n" \
"v_mfma_f32_16x16x32_bf16 v[160:163], a[68:71], a[28:31], v[160:163]\n" \
"s_waitcnt vmcnt(35) lgkmcnt(0)\n" \
"s_barrier\n" \
"v_mfma_f32_16x16x32_bf16 v[80:83], a[96:99], a[0:3], v[80:83]\n" \
"ds_read_b64_tr_b16 a[64:65], v232 offset:0\n" \
"v_mul_f32 v243, s31, v243\n" \
"v_mfma_f32_16x16x32_bf16 v[80:83], a[100:103], a[4:7], v[80:83]\n" \
"ds_read_b64_tr_b16 a[66:67], v232 offset:2048\n" \
"v_mul_f32 v252, s31, v252\n" \
"v_mfma_f32_16x16x32_bf16 v[84:87], a[104:107], a[0:3], v[84:87]\n" \
"s_add_u32 m0, s79, 49152\n" \
"ds_read_b64_tr_b16 a[68:69], v232 offset:8192\n" \
"v_mul_f32 v253, s31, v253\n" \
"v_mfma_f32_16x16x32_bf16 v[84:87], a[108:111], a[4:7], v[84:87]\n" \
"buffer_load_dwordx4 v228, s[52:55], s78 offen offset:0 lds\n" \
"v_mfma_f32_16x16x32_bf16 v[88:91], a[112:115], a[0:3], v[88:91]\n" \
"ds_read_b64_tr_b16 a[70:71], v232 offset:10240\n" \
"v_mul_f32 v254, s31, v254\n" \
"v_mfma_f32_16x16x32_bf16 v[88:91], a[116:119], a[4:7], v[88:91]\n" \
"ds_read_b64_tr_b16 a[72:73], v233 offset:0\n" \
"v_cvt_pk_bf16_f32 v22, v243, v252\n" \
"v_mfma_f32_16x16x32_bf16 v[92:95], a[120:123], a[0:3], v[92:95]\n" \
"ds_read_b64_tr_b16 a[74:75], v233 offset:2048\n" \
"v_cvt_pk_bf16_f32 v23, v253, v254\n" \
"v_mfma_f32_16x16x32_bf16 v[92:95], a[124:127], a[4:7], v[92:95]\n" \
"buffer_load_dwordx4 v229, s[52:55], s78 offen offset:1024 lds\n" \
"v_mfma_f32_16x16x32_bf16 v[124:127], a[120:123], a[8:11], v[124:127]\n" \
"s_waitcnt vmcnt(35)\n" \
"ds_read_b128 a[0:3], v230 offset:32768\n" \
"v_accvgpr_read_b32 v243, a136\n" \
"v_mfma_f32_16x16x32_bf16 v[124:127], a[124:127], a[12:15], v[124:127]\n" \
"ds_read_b128 a[4:7], v231 offset:32768\n" \
"v_accvgpr_read_b32 v252, a137\n" \
"v_mfma_f32_16x16x32_bf16 v[120:123], a[112:115], a[8:11], v[120:123]\n" \
"s_xor_b32 s78, s78, 0x300000\n" \
"ds_read_b64_tr_b16 a[76:77], v233 offset:8192\n" \
"v_accvgpr_read_b32 v253, a138\n" \
"v_mfma_f32_16x16x32_bf16 v[120:123], a[116:119], a[12:15], v[120:123]\n" \
"buffer_load_dwordx4 v228, s[52:55], s89 offen offset:2048 lds\n" \
"v_mfma_f32_16x16x32_bf16 v[116:119], a[104:107], a[8:11], v[116:119]\n" \
"ds_read_b64_tr_b16 a[78:79], v233 offset:10240\n" \
"v_accvgpr_read_b32 v254, a139\n" \
"v_mfma_f32_16x16x32_bf16 v[116:119], a[108:111], a[12:15], v[116:119]\n" \
"ds_read_b64_tr_b16 a[80:81], v232 offset:512\n" \
"v_mul_f32 v243, s31, v243\n" \
"v_mfma_f32_16x16x32_bf16 v[112:115], a[96:99], a[8:11], v[112:115]\n" \
"ds_read_b64_tr_b16 a[82:83], v232 offset:2560\n" \
"v_mul_f32 v252, s31, v252\n" \
"v_mfma_f32_16x16x32_bf16 v[112:115], a[100:103], a[12:15], v[112:115]\n" \
"buffer_load_dwordx4 v229, s[52:55], s89 offen offset:3072 lds\n" \
"v_mfma_f32_16x16x32_bf16 v[144:147], a[96:99], a[16:19], v[144:147]\n" \
"s_waitcnt vmcnt(35)\n" \
"s_xor_b32 s89, s89, 0x300000\n" \
"ds_read_b128 a[8:11], v230 offset:34816\n" \
"v_mfma_f32_16x16x32_bf16 v[144:147], a[100:103], a[20:23], v[144:147]\n" \
"s_add_u32 m0, s74, 98304\n" \
"ds_read_b128 a[12:15], v231 offset:34816\n" \
"v_mul_f32 v253, s31, v253\n" \
"v_mfma_f32_16x16x32_bf16 v[148:151], a[104:107], a[16:19], v[148:151]\n" \
"s_waitcnt lgkmcnt(6)\n" \
"ds_read_b64_tr_b16 a[84:85], v232 offset:8704\n" \
"v_mul_f32 v254, s31, v254\n" \
"v_mfma_f32_16x16x32_bf16 v[148:151], a[108:111], a[20:23], v[148:151]\n" \
"buffer_load_dwordx4 v224, s[48:51], s75 offen offset:0 lds\n" \
"v_mfma_f32_16x16x32_bf16 v[152:155], a[112:115], a[16:19], v[152:155]\n" \
"ds_read_b64_tr_b16 a[86:87], v232 offset:10752\n" \
"v_cvt_pk_bf16_f32 v12, v243, v252\n" \
"v_mfma_f32_16x16x32_bf16 v[152:155], a[116:119], a[20:23], v[152:155]\n" \
"ds_read_b64_tr_b16 a[88:89], v233 offset:512\n" \
"v_cvt_pk_bf16_f32 v13, v253, v254\n" \
"v_mfma_f32_16x16x32_bf16 v[156:159], a[120:123], a[16:19], v[156:159]\n" \
"ds_read_b64_tr_b16 a[90:91], v233 offset:2560\n" \
"v_accvgpr_read_b32 v243, a152\n" \
"v_mfma_f32_16x16x32_bf16 v[156:159], a[124:127], a[20:23], v[156:159]\n" \
"buffer_load_dwordx4 v225, s[48:51], s75 offen offset:1024 lds\n" \
"v_mfma_f32_16x16x32_bf16 v[188:191], a[120:123], a[24:27], v[188:191]\n" \
"s_waitcnt vmcnt(35)\n" \
"ds_read_b128 a[16:19], v230 offset:36864\n" \
"v_accvgpr_read_b32 v252, a153\n" \
"v_mfma_f32_16x16x32_bf16 v[188:191], a[124:127], a[28:31], v[188:191]\n" \
"ds_read_b128 a[20:23], v231 offset:36864\n" \
"v_accvgpr_read_b32 v253, a154\n" \
"v_mfma_f32_16x16x32_bf16 v[184:187], a[112:115], a[24:27], v[184:187]\n" \
"s_waitcnt lgkmcnt(6)\n" \
"ds_read_b64_tr_b16 a[92:93], v233 offset:8704\n" \
"v_accvgpr_read_b32 v254, a155\n" \
"v_mfma_f32_16x16x32_bf16 v[184:187], a[116:119], a[28:31], v[184:187]\n" \
"buffer_load_dwordx4 v226, s[48:51], s75 offen offset:2048 lds\n" \
"v_mfma_f32_16x16x32_bf16 v[180:183], a[104:107], a[24:27], v[180:183]\n" \
"ds_read_b64_tr_b16 a[94:95], v233 offset:10752\n" \
"v_mul_f32 v243, s31, v243\n" \
"v_mfma_f32_16x16x32_bf16 v[180:183], a[108:111], a[28:31], v[180:183]\n" \
"v_mul_f32 v252, s31, v252\n" \
"v_mul_f32 v253, s31, v253\n" \
"v_mfma_f32_16x16x32_bf16 v[176:179], a[96:99], a[24:27], v[176:179]\n" \
"v_mul_f32 v254, s31, v254\n" \
"v_cvt_pk_bf16_f32 v14, v243, v252\n" \
"v_mfma_f32_16x16x32_bf16 v[176:179], a[100:103], a[28:31], v[176:179]\n" \
"buffer_load_dwordx4 v227, s[48:51], s75 offen offset:3072 lds\n" \
"v_mfma_f32_16x16x32_bf16 v[64:67], a[64:67], a[0:3], v[64:67]\n" \
"s_waitcnt vmcnt(35)\n" \
"s_xor_b32 s75, s75, 0x300\n" \
"ds_read_b128 a[24:27], v230 offset:38912\n" \
"v_mfma_f32_16x16x32_bf16 v[64:67], a[68:71], a[4:7], v[64:67]\n" \
"s_add_u32 m0, s74, 102400\n" \
"ds_read_b128 a[28:31], v231 offset:38912\n" \
"v_cvt_pk_bf16_f32 v15, v253, v254\n" \
"v_mfma_f32_16x16x32_bf16 v[68:71], a[72:75], a[0:3], v[68:71]\n" \
"s_waitcnt lgkmcnt(4)\n" \
"ds_read_b64_tr_b16 a[96:97], v232 offset:1024\n" \
"v_accvgpr_read_b32 v243, a140\n" \
"v_mfma_f32_16x16x32_bf16 v[68:71], a[76:79], a[4:7], v[68:71]\n" \
"buffer_load_dwordx4 v224, s[48:51], s77 offen offset:0 lds\n" \
"v_mfma_f32_16x16x32_bf16 v[72:75], a[80:83], a[0:3], v[72:75]\n" \
"ds_read_b64_tr_b16 a[98:99], v232 offset:3072\n" \
"v_accvgpr_read_b32 v252, a141\n" \
"v_mfma_f32_16x16x32_bf16 v[72:75], a[84:87], a[4:7], v[72:75]\n" \
"s_waitcnt lgkmcnt(3)\n" \
"ds_read_b64_tr_b16 a[100:101], v232 offset:9216\n" \
"v_accvgpr_read_b32 v253, a142\n" \
"v_mfma_f32_16x16x32_bf16 v[76:79], a[88:91], a[0:3], v[76:79]\n" \
"ds_read_b64_tr_b16 a[102:103], v232 offset:11264\n" \
"v_accvgpr_read_b32 v254, a143\n" \
"v_mfma_f32_16x16x32_bf16 v[76:79], a[92:95], a[4:7], v[76:79]\n" \
"buffer_load_dwordx4 v225, s[48:51], s77 offen offset:1024 lds\n" \
"v_mfma_f32_16x16x32_bf16 v[108:111], a[88:91], a[8:11], v[108:111]\n" \
"ds_read_b64_tr_b16 a[104:105], v233 offset:1024\n" \
"v_mul_f32 v243, s31, v243\n" \
"v_mfma_f32_16x16x32_bf16 v[108:111], a[92:95], a[12:15], v[108:111]\n" \
"ds_read_b64_tr_b16 a[106:107], v233 offset:3072\n" \
"v_mul_f32 v252, s31, v252\n" \
"v_mfma_f32_16x16x32_bf16 v[104:107], a[80:83], a[8:11], v[104:107]\n" \
"s_waitcnt lgkmcnt(6)\n" \
"ds_read_b64_tr_b16 a[108:109], v233 offset:9216\n" \
"v_mul_f32 v253, s31, v253\n" \
"v_mfma_f32_16x16x32_bf16 v[104:107], a[84:87], a[12:15], v[104:107]\n" \
"buffer_load_dwordx4 v226, s[48:51], s77 offen offset:2048 lds\n" \
"v_mfma_f32_16x16x32_bf16 v[100:103], a[72:75], a[8:11], v[100:103]\n" \
"ds_read_b64_tr_b16 a[110:111], v233 offset:11264\n" \
"v_mul_f32 v254, s31, v254\n" \
"v_mfma_f32_16x16x32_bf16 v[100:103], a[76:79], a[12:15], v[100:103]\n" \
"ds_read_b64_tr_b16 a[112:113], v232 offset:1536\n" \
"v_cvt_pk_bf16_f32 v28, v243, v252\n" \
"v_mfma_f32_16x16x32_bf16 v[96:99], a[64:67], a[8:11], v[96:99]\n" \
"ds_read_b64_tr_b16 a[114:115], v232 offset:3584\n" \
"v_cvt_pk_bf16_f32 v29, v253, v254\n" \
"v_mfma_f32_16x16x32_bf16 v[96:99], a[68:71], a[12:15], v[96:99]\n" \
"buffer_load_dwordx4 v227, s[48:51], s77 offen offset:3072 lds\n" \
"v_mfma_f32_16x16x32_bf16 v[128:131], a[64:67], a[16:19], v[128:131]\n" \
"ds_read_b64_tr_b16 a[116:117], v232 offset:9728\n" \
"v_accvgpr_read_b32 v243, a156\n" \
"v_mfma_f32_16x16x32_bf16 v[128:131], a[68:71], a[20:23], v[128:131]\n" \
"ds_read_b64_tr_b16 a[118:119], v232 offset:11776\n" \
"v_accvgpr_read_b32 v252, a157\n" \
"v_mfma_f32_16x16x32_bf16 v[132:135], a[72:75], a[16:19], v[132:135]\n" \
"s_xor_b32 s77, s77, 0x300\n" \
"ds_read_b64_tr_b16 a[120:121], v233 offset:1536\n" \
"v_accvgpr_read_b32 v253, a158\n" \
"v_mfma_f32_16x16x32_bf16 v[132:135], a[76:79], a[20:23], v[132:135]\n" \
"v_accvgpr_read_b32 v254, a159\n" \
"v_mul_f32 v243, s31, v243\n" \
"v_mfma_f32_16x16x32_bf16 v[136:139], a[80:83], a[16:19], v[136:139]\n" \
"ds_read_b64_tr_b16 a[122:123], v233 offset:3584\n" \
"v_mul_f32 v252, s31, v252\n" \
"v_mfma_f32_16x16x32_bf16 v[136:139], a[84:87], a[20:23], v[136:139]\n" \
"ds_read_b64_tr_b16 a[124:125], v233 offset:9728\n" \
"v_mul_f32 v253, s31, v253\n" \
"v_mfma_f32_16x16x32_bf16 v[140:143], a[88:91], a[16:19], v[140:143]\n" \
"s_add_u32 s93, s87, 17858560\n" \
"ds_read_b64_tr_b16 a[126:127], v233 offset:11776\n" \
"v_mul_f32 v254, s31, v254\n" \
"v_mfma_f32_16x16x32_bf16 v[140:143], a[92:95], a[20:23], v[140:143]\n" \
"buffer_store_dwordx4 a[224:227], v234, s[56:59], s93 offen offset:0 sc1\n" \
"v_mfma_f32_16x16x32_bf16 v[172:175], a[88:91], a[24:27], v[172:175]\n" \
"v_cvt_pk_bf16_f32 v30, v243, v252\n" \
"v_cvt_pk_bf16_f32 v31, v253, v254\n" \
"v_mfma_f32_16x16x32_bf16 v[172:175], a[92:95], a[28:31], v[172:175]\n" \
"v_accvgpr_write_b32 a56, v244\n" \
"v_accvgpr_write_b32 a57, v245\n" \
"v_mfma_f32_16x16x32_bf16 v[168:171], a[80:83], a[24:27], v[168:171]\n" \
"v_accvgpr_write_b32 a58, v246\n" \
"v_accvgpr_write_b32 a59, v247\n" \
"v_mfma_f32_16x16x32_bf16 v[168:171], a[84:87], a[28:31], v[168:171]\n" \
"v_accvgpr_write_b32 a60, v248\n" \
"v_accvgpr_write_b32 a61, v249\n" \
"v_mfma_f32_16x16x32_bf16 v[164:167], a[72:75], a[24:27], v[164:167]\n" \
"v_accvgpr_write_b32 a48, 0\n" \
"v_accvgpr_write_b32 a49, 0\n" \
"v_mfma_f32_16x16x32_bf16 v[164:167], a[76:79], a[28:31], v[164:167]\n" \
"v_accvgpr_write_b32 a50, 0\n" \
"v_accvgpr_write_b32 a51, 0\n" \
"v_mfma_f32_16x16x32_bf16 v[160:163], a[64:67], a[24:27], v[160:163]\n" \
"v_accvgpr_write_b32 a52, 0\n" \
"v_accvgpr_write_b32 a53, 0\n" \
"v_mfma_f32_16x16x32_bf16 v[160:163], a[68:71], a[28:31], v[160:163]\n" \
"s_waitcnt vmcnt(35) lgkmcnt(0)\n" \
"s_barrier\n" \
"v_mfma_f32_16x16x32_bf16 v[80:83], a[96:99], a[0:3], v[80:83]\n" \
"ds_read_b64_tr_b16 a[64:65], v232 offset:16384\n" \
"v_mfma_f32_16x16x32_bf16 v[80:83], a[100:103], a[4:7], v[80:83]\n" \
"ds_read_b64_tr_b16 a[66:67], v232 offset:18432\n" \
"v_mfma_f32_16x16x32_bf16 v[84:87], a[104:107], a[0:3], v[84:87]\n" \
"s_add_u32 m0, s79, 0\n" \
"ds_read_b64_tr_b16 a[68:69], v232 offset:24576\n" \
"v_mfma_f32_16x16x32_bf16 v[84:87], a[108:111], a[4:7], v[84:87]\n" \
"buffer_load_dwordx4 v228, s[52:55], s78 offen offset:0 lds\n" \
"v_mfma_f32_16x16x32_bf16 v[88:91], a[112:115], a[0:3], v[88:91]\n" \
"ds_read_b64_tr_b16 a[70:71], v232 offset:26624\n" \
"v_mfma_f32_16x16x32_bf16 v[88:91], a[116:119], a[4:7], v[88:91]\n" \
"ds_read_b64_tr_b16 a[72:73], v233 offset:16384\n" \
"v_mfma_f32_16x16x32_bf16 v[92:95], a[120:123], a[0:3], v[92:95]\n" \
"ds_read_b64_tr_b16 a[74:75], v233 offset:18432\n" \
"v_mfma_f32_16x16x32_bf16 v[92:95], a[124:127], a[4:7], v[92:95]\n" \
"buffer_load_dwordx4 v229, s[52:55], s78 offen offset:1024 lds\n" \
"v_mfma_f32_16x16x32_bf16 v[124:127], a[120:123], a[8:11], v[124:127]\n" \
"s_waitcnt vmcnt(35)\n" \
"ds_read_b128 a[0:3], v250 offset:0\n" \
"v_mfma_f32_16x16x32_bf16 v[124:127], a[124:127], a[12:15], v[124:127]\n" \
"ds_read_b128 a[4:7], v251 offset:0\n" \
"v_mfma_f32_16x16x32_bf16 v[120:123], a[112:115], a[8:11], v[120:123]\n" \
"s_xor_b32 s78, s78, 0x80000\n" \
"ds_read_b64_tr_b16 a[76:77], v233 offset:24576\n" \
"v_mfma_f32_16x16x32_bf16 v[120:123], a[116:119], a[12:15], v[120:123]\n" \
"buffer_load_dwordx4 v228, s[52:55], s89 offen offset:2048 lds\n" \
"v_mfma_f32_16x16x32_bf16 v[116:119], a[104:107], a[8:11], v[116:119]\n" \
"ds_read_b64_tr_b16 a[78:79], v233 offset:26624\n" \
"v_mfma_f32_16x16x32_bf16 v[116:119], a[108:111], a[12:15], v[116:119]\n" \
"ds_read_b64_tr_b16 a[80:81], v232 offset:16896\n" \
"v_mfma_f32_16x16x32_bf16 v[112:115], a[96:99], a[8:11], v[112:115]\n" \
"ds_read_b64_tr_b16 a[82:83], v232 offset:18944\n" \
"v_mfma_f32_16x16x32_bf16 v[112:115], a[100:103], a[12:15], v[112:115]\n" \
"buffer_load_dwordx4 v229, s[52:55], s89 offen offset:3072 lds\n" \
"v_mfma_f32_16x16x32_bf16 v[144:147], a[96:99], a[16:19], v[144:147]\n" \
"s_waitcnt vmcnt(35)\n" \
"s_xor_b32 s89, s89, 0x80000\n" \
"ds_read_b128 a[8:11], v250 offset:2048\n" \
"v_mfma_f32_16x16x32_bf16 v[144:147], a[100:103], a[20:23], v[144:147]\n" \
"s_add_u32 m0, s74, 131072\n" \
"ds_read_b128 a[12:15], v251 offset:2048\n" \
"v_mfma_f32_16x16x32_bf16 v[148:151], a[104:107], a[16:19], v[148:151]\n" \
"s_waitcnt lgkmcnt(6)\n" \
"ds_read_b64_tr_b16 a[84:85], v232 offset:25088\n" \
"v_mfma_f32_16x16x32_bf16 v[148:151], a[108:111], a[20:23], v[148:151]\n" \
"buffer_load_dwordx4 v224, s[48:51], s75 offen offset:0 lds\n" \
"v_mfma_f32_16x16x32_bf16 v[152:155], a[112:115], a[16:19], v[152:155]\n" \
"ds_read_b64_tr_b16 a[86:87], v232 offset:27136\n" \
"v_mfma_f32_16x16x32_bf16 v[152:155], a[116:119], a[20:23], v[152:155]\n" \
"ds_read_b64_tr_b16 a[88:89], v233 offset:16896\n" \
"v_mfma_f32_16x16x32_bf16 v[156:159], a[120:123], a[16:19], v[156:159]\n" \
"ds_read_b64_tr_b16 a[90:91], v233 offset:18944\n" \
"v_mfma_f32_16x16x32_bf16 v[156:159], a[124:127], a[20:23], v[156:159]\n" \
"buffer_load_dwordx4 v225, s[48:51], s75 offen offset:1024 lds\n" \
"v_mfma_f32_16x16x32_bf16 v[188:191], a[120:123], a[24:27], v[188:191]\n" \
"s_waitcnt vmcnt(35)\n" \
"ds_read_b128 a[16:19], v250 offset:4096\n" \
"v_mfma_f32_16x16x32_bf16 v[188:191], a[124:127], a[28:31], v[188:191]\n" \
"ds_read_b128 a[20:23], v251 offset:4096\n" \
"v_mfma_f32_16x16x32_bf16 v[184:187], a[112:115], a[24:27], v[184:187]\n" \
"s_waitcnt lgkmcnt(6)\n" \
"ds_read_b64_tr_b16 a[92:93], v233 offset:25088\n" \
"v_mfma_f32_16x16x32_bf16 v[184:187], a[116:119], a[28:31], v[184:187]\n" \
"buffer_load_dwordx4 v226, s[48:51], s75 offen offset:2048 lds\n" \
"v_mfma_f32_16x16x32_bf16 v[180:183], a[104:107], a[24:27], v[180:183]\n" \
"ds_read_b64_tr_b16 a[94:95], v233 offset:27136\n" \
"v_mfma_f32_16x16x32_bf16 v[180:183], a[108:111], a[28:31], v[180:183]\n" \
"v_mfma_f32_16x16x32_bf16 v[176:179], a[96:99], a[24:27], v[176:179]\n" \
"v_mfma_f32_16x16x32_bf16 v[176:179], a[100:103], a[28:31], v[176:179]\n" \
"buffer_load_dwordx4 v227, s[48:51], s75 offen offset:3072 lds\n" \
"v_mfma_f32_16x16x32_bf16 v[64:67], a[64:67], a[0:3], v[64:67]\n" \
"s_waitcnt vmcnt(35)\n" \
"s_xor_b32 s75, s75, 0x80\n" \
"ds_read_b128 a[24:27], v250 offset:6144\n" \
"v_mfma_f32_16x16x32_bf16 v[64:67], a[68:71], a[4:7], v[64:67]\n" \
"s_add_u32 m0, s74, 135168\n" \
"ds_read_b128 a[28:31], v251 offset:6144\n" \
"v_mfma_f32_16x16x32_bf16 v[68:71], a[72:75], a[0:3], v[68:71]\n" \
"s_waitcnt lgkmcnt(2)\n" \
"ds_read_b64_tr_b16 a[96:97], v232 offset:17408\n" \
"v_mfma_f32_16x16x32_bf16 v[68:71], a[76:79], a[4:7], v[68:71]\n" \
"buffer_load_dwordx4 v224, s[48:51], s77 offen offset:0 lds\n" \
"v_mfma_f32_16x16x32_bf16 v[72:75], a[80:83], a[0:3], v[72:75]\n" \
"ds_read_b64_tr_b16 a[98:99], v232 offset:19456\n" \
"v_mfma_f32_16x16x32_bf16 v[72:75], a[84:87], a[4:7], v[72:75]\n" \
"ds_read_b64_tr_b16 a[100:101], v232 offset:25600\n" \
"v_mfma_f32_16x16x32_bf16 v[76:79], a[88:91], a[0:3], v[76:79]\n" \
"ds_read_b64_tr_b16 a[102:103], v232 offset:27648\n" \
"v_mfma_f32_16x16x32_bf16 v[76:79], a[92:95], a[4:7], v[76:79]\n" \
"buffer_load_dwordx4 v225, s[48:51], s77 offen offset:1024 lds\n" \
"v_mfma_f32_16x16x32_bf16 v[108:111], a[88:91], a[8:11], v[108:111]\n" \
"ds_read_b64_tr_b16 a[104:105], v233 offset:17408\n" \
"v_mfma_f32_16x16x32_bf16 v[108:111], a[92:95], a[12:15], v[108:111]\n" \
"ds_read_b64_tr_b16 a[106:107], v233 offset:19456\n" \
"v_mfma_f32_16x16x32_bf16 v[104:107], a[80:83], a[8:11], v[104:107]\n" \
"s_waitcnt lgkmcnt(6)\n" \
"ds_read_b64_tr_b16 a[108:109], v233 offset:25600\n" \
"v_mfma_f32_16x16x32_bf16 v[104:107], a[84:87], a[12:15], v[104:107]\n" \
"buffer_load_dwordx4 v226, s[48:51], s77 offen offset:2048 lds\n" \
"v_mfma_f32_16x16x32_bf16 v[100:103], a[72:75], a[8:11], v[100:103]\n" \
"ds_read_b64_tr_b16 a[110:111], v233 offset:27648\n" \
"v_mfma_f32_16x16x32_bf16 v[100:103], a[76:79], a[12:15], v[100:103]\n" \
"ds_read_b64_tr_b16 a[112:113], v232 offset:17920\n" \
"v_mfma_f32_16x16x32_bf16 v[96:99], a[64:67], a[8:11], v[96:99]\n" \
"ds_read_b64_tr_b16 a[114:115], v232 offset:19968\n" \
"Lpg3897_%=:\n" \
"s_cbranch_execz Lpg6060_%=\n" \
"v_mfma_f32_16x16x32_bf16 v[96:99], a[68:71], a[12:15], v[96:99]\n" \
"buffer_load_dwordx4 v227, s[48:51], s77 offen offset:3072 lds\n" \
"v_mfma_f32_16x16x32_bf16 v[128:131], a[64:67], a[16:19], v[128:131]\n" \
"ds_read_b64_tr_b16 a[116:117], v232 offset:26112\n" \
"v_mfma_f32_16x16x32_bf16 v[128:131], a[68:71], a[20:23], v[128:131]\n" \
"ds_read_b64_tr_b16 a[118:119], v232 offset:28160\n" \
"v_mfma_f32_16x16x32_bf16 v[132:135], a[72:75], a[16:19], v[132:135]\n" \
"s_xor_b32 s77, s77, 0x80\n" \
"ds_read_b64_tr_b16 a[120:121], v233 offset:17920\n" \
"v_mfma_f32_16x16x32_bf16 v[132:135], a[76:79], a[20:23], v[132:135]\n" \
"buffer_store_dwordx4 a[228:231], v234, s[56:59], s93 offen offset:1024 sc1\n" \
"v_mfma_f32_16x16x32_bf16 v[136:139], a[80:83], a[16:19], v[136:139]\n" \
"ds_read_b64_tr_b16 a[122:123], v233 offset:19968\n" \
"v_mfma_f32_16x16x32_bf16 v[136:139], a[84:87], a[20:23], v[136:139]\n" \
"ds_read_b64_tr_b16 a[124:125], v233 offset:26112\n" \
"v_mfma_f32_16x16x32_bf16 v[140:143], a[88:91], a[16:19], v[140:143]\n" \
"ds_read_b64_tr_b16 a[126:127], v233 offset:28160\n" \
"v_mfma_f32_16x16x32_bf16 v[140:143], a[92:95], a[20:23], v[140:143]\n" \
"v_mfma_f32_16x16x32_bf16 v[172:175], a[88:91], a[24:27], v[172:175]\n" \
"v_mfma_f32_16x16x32_bf16 v[172:175], a[92:95], a[28:31], v[172:175]\n" \
"v_mfma_f32_16x16x32_bf16 v[168:171], a[80:83], a[24:27], v[168:171]\n" \
"v_mfma_f32_16x16x32_bf16 v[168:171], a[84:87], a[28:31], v[168:171]\n" \
"buffer_store_dwordx4 a[232:235], v234, s[56:59], s93 offen offset:2048 sc1\n" \
"v_mfma_f32_16x16x32_bf16 v[164:167], a[72:75], a[24:27], v[164:167]\n" \
"v_mfma_f32_16x16x32_bf16 v[164:167], a[76:79], a[28:31], v[164:167]\n" \
"v_mfma_f32_16x16x32_bf16 v[160:163], a[64:67], a[24:27], v[160:163]\n" \
"v_mfma_f32_16x16x32_bf16 v[160:163], a[68:71], a[28:31], v[160:163]\n" \
"s_waitcnt vmcnt(36) lgkmcnt(0)\n" \
"s_barrier\n" \
"v_mfma_f32_16x16x32_bf16 v[80:83], a[96:99], a[0:3], v[80:83]\n" \
"ds_read_b64_tr_b16 a[64:65], v232 offset:32768\n" \
"v_mfma_f32_16x16x32_bf16 v[80:83], a[100:103], a[4:7], v[80:83]\n" \
"ds_read_b64_tr_b16 a[66:67], v232 offset:34816\n" \
"v_mfma_f32_16x16x32_bf16 v[84:87], a[104:107], a[0:3], v[84:87]\n" \
"s_add_u32 m0, s79, 16384\n" \
"ds_read_b64_tr_b16 a[68:69], v232 offset:40960\n" \
"v_mfma_f32_16x16x32_bf16 v[84:87], a[108:111], a[4:7], v[84:87]\n" \
"buffer_load_dwordx4 v228, s[52:55], s78 offen offset:0 lds\n" \
"v_mfma_f32_16x16x32_bf16 v[88:91], a[112:115], a[0:3], v[88:91]\n" \
"ds_read_b64_tr_b16 a[70:71], v232 offset:43008\n" \
"v_mfma_f32_16x16x32_bf16 v[88:91], a[116:119], a[4:7], v[88:91]\n" \
"ds_read_b64_tr_b16 a[72:73], v233 offset:32768\n" \
"v_mfma_f32_16x16x32_bf16 v[92:95], a[120:123], a[0:3], v[92:95]\n" \
"ds_read_b64_tr_b16 a[74:75], v233 offset:34816\n" \
"v_mfma_f32_16x16x32_bf16 v[92:95], a[124:127], a[4:7], v[92:95]\n" \
"buffer_load_dwordx4 v229, s[52:55], s78 offen offset:1024 lds\n" \
"v_mfma_f32_16x16x32_bf16 v[124:127], a[120:123], a[8:11], v[124:127]\n" \
"s_waitcnt vmcnt(36)\n" \
"ds_read_b128 a[0:3], v230 offset:0\n" \
"v_mfma_f32_16x16x32_bf16 v[124:127], a[124:127], a[12:15], v[124:127]\n" \
"ds_read_b128 a[4:7], v231 offset:0\n" \
"v_mfma_f32_16x16x32_bf16 v[120:123], a[112:115], a[8:11], v[120:123]\n" \
"s_xor_b32 s78, s78, 0x180000\n" \
"ds_read_b64_tr_b16 a[76:77], v233 offset:40960\n" \
"v_mfma_f32_16x16x32_bf16 v[120:123], a[116:119], a[12:15], v[120:123]\n" \
"buffer_load_dwordx4 v228, s[52:55], s89 offen offset:2048 lds\n" \
"v_mfma_f32_16x16x32_bf16 v[116:119], a[104:107], a[8:11], v[116:119]\n" \
"ds_read_b64_tr_b16 a[78:79], v233 offset:43008\n" \
"v_mfma_f32_16x16x32_bf16 v[116:119], a[108:111], a[12:15], v[116:119]\n" \
"ds_read_b64_tr_b16 a[80:81], v232 offset:33280\n" \
"v_mfma_f32_16x16x32_bf16 v[112:115], a[96:99], a[8:11], v[112:115]\n" \
"ds_read_b64_tr_b16 a[82:83], v232 offset:35328\n" \
"v_mfma_f32_16x16x32_bf16 v[112:115], a[100:103], a[12:15], v[112:115]\n" \
"buffer_load_dwordx4 v229, s[52:55], s89 offen offset:3072 lds\n" \
"v_mfma_f32_16x16x32_bf16 v[144:147], a[96:99], a[16:19], v[144:147]\n" \
"s_waitcnt vmcnt(36)\n" \
"s_xor_b32 s89, s89, 0x180000\n" \
"ds_read_b128 a[8:11], v230 offset:2048\n" \
"v_mfma_f32_16x16x32_bf16 v[144:147], a[100:103], a[20:23], v[144:147]\n" \
"s_add_u32 m0, s74, 65536\n" \
"ds_read_b128 a[12:15], v231 offset:2048\n" \
"v_mfma_f32_16x16x32_bf16 v[148:151], a[104:107], a[16:19], v[148:151]\n" \
"s_waitcnt lgkmcnt(6)\n" \
"ds_read_b64_tr_b16 a[84:85], v232 offset:41472\n" \
"v_mfma_f32_16x16x32_bf16 v[148:151], a[108:111], a[20:23], v[148:151]\n" \
"buffer_load_dwordx4 v224, s[48:51], s75 offen offset:0 lds\n" \
"v_mfma_f32_16x16x32_bf16 v[152:155], a[112:115], a[16:19], v[152:155]\n" \
"ds_read_b64_tr_b16 a[86:87], v232 offset:43520\n" \
"v_mfma_f32_16x16x32_bf16 v[152:155], a[116:119], a[20:23], v[152:155]\n" \
"ds_read_b64_tr_b16 a[88:89], v233 offset:33280\n" \
"v_mfma_f32_16x16x32_bf16 v[156:159], a[120:123], a[16:19], v[156:159]\n" \
"ds_read_b64_tr_b16 a[90:91], v233 offset:35328\n" \
"v_mfma_f32_16x16x32_bf16 v[156:159], a[124:127], a[20:23], v[156:159]\n" \
"buffer_load_dwordx4 v225, s[48:51], s75 offen offset:1024 lds\n" \
"v_mfma_f32_16x16x32_bf16 v[188:191], a[120:123], a[24:27], v[188:191]\n" \
"s_waitcnt vmcnt(36)\n" \
"ds_read_b128 a[16:19], v230 offset:4096\n" \
"v_mfma_f32_16x16x32_bf16 v[188:191], a[124:127], a[28:31], v[188:191]\n" \
"ds_read_b128 a[20:23], v231 offset:4096\n" \
"v_mfma_f32_16x16x32_bf16 v[184:187], a[112:115], a[24:27], v[184:187]\n" \
"s_waitcnt lgkmcnt(6)\n" \
"ds_read_b64_tr_b16 a[92:93], v233 offset:41472\n" \
"v_mfma_f32_16x16x32_bf16 v[184:187], a[116:119], a[28:31], v[184:187]\n" \
"buffer_load_dwordx4 v226, s[48:51], s75 offen offset:2048 lds\n" \
"v_mfma_f32_16x16x32_bf16 v[180:183], a[104:107], a[24:27], v[180:183]\n" \
"ds_read_b64_tr_b16 a[94:95], v233 offset:43520\n" \
"v_mfma_f32_16x16x32_bf16 v[180:183], a[108:111], a[28:31], v[180:183]\n" \
"v_mfma_f32_16x16x32_bf16 v[176:179], a[96:99], a[24:27], v[176:179]\n" \
"v_mfma_f32_16x16x32_bf16 v[176:179], a[100:103], a[28:31], v[176:179]\n" \
"buffer_load_dwordx4 v227, s[48:51], s75 offen offset:3072 lds\n" \
"v_mfma_f32_16x16x32_bf16 v[64:67], a[64:67], a[0:3], v[64:67]\n" \
"s_waitcnt vmcnt(36)\n" \
"s_xor_b32 s75, s75, 0x180\n" \
"ds_read_b128 a[24:27], v230 offset:6144\n" \
"v_mfma_f32_16x16x32_bf16 v[64:67], a[68:71], a[4:7], v[64:67]\n" \
"s_add_u32 m0, s74, 69632\n" \
"ds_read_b128 a[28:31], v231 offset:6144\n" \
"v_mfma_f32_16x16x32_bf16 v[68:71], a[72:75], a[0:3], v[68:71]\n" \
"s_waitcnt lgkmcnt(2)\n" \
"ds_read_b64_tr_b16 a[96:97], v232 offset:33792\n" \
"v_mfma_f32_16x16x32_bf16 v[68:71], a[76:79], a[4:7], v[68:71]\n" \
"buffer_load_dwordx4 v224, s[48:51], s77 offen offset:0 lds\n" \
"v_mfma_f32_16x16x32_bf16 v[72:75], a[80:83], a[0:3], v[72:75]\n" \
"ds_read_b64_tr_b16 a[98:99], v232 offset:35840\n" \
"v_mfma_f32_16x16x32_bf16 v[72:75], a[84:87], a[4:7], v[72:75]\n" \
"ds_read_b64_tr_b16 a[100:101], v232 offset:41984\n" \
"v_mfma_f32_16x16x32_bf16 v[76:79], a[88:91], a[0:3], v[76:79]\n" \
"ds_read_b64_tr_b16 a[102:103], v232 offset:44032\n" \
"v_mfma_f32_16x16x32_bf16 v[76:79], a[92:95], a[4:7], v[76:79]\n" \
"buffer_load_dwordx4 v225, s[48:51], s77 offen offset:1024 lds\n" \
"v_mfma_f32_16x16x32_bf16 v[108:111], a[88:91], a[8:11], v[108:111]\n" \
"ds_read_b64_tr_b16 a[104:105], v233 offset:33792\n" \
"v_mfma_f32_16x16x32_bf16 v[108:111], a[92:95], a[12:15], v[108:111]\n" \
"ds_read_b64_tr_b16 a[106:107], v233 offset:35840\n" \
"v_mfma_f32_16x16x32_bf16 v[104:107], a[80:83], a[8:11], v[104:107]\n" \
"s_waitcnt lgkmcnt(6)\n" \
"ds_read_b64_tr_b16 a[108:109], v233 offset:41984\n" \
"v_mfma_f32_16x16x32_bf16 v[104:107], a[84:87], a[12:15], v[104:107]\n" \
"buffer_load_dwordx4 v226, s[48:51], s77 offen offset:2048 lds\n" \
"v_mfma_f32_16x16x32_bf16 v[100:103], a[72:75], a[8:11], v[100:103]\n" \
"ds_read_b64_tr_b16 a[110:111], v233 offset:44032\n" \
"v_mfma_f32_16x16x32_bf16 v[100:103], a[76:79], a[12:15], v[100:103]\n" \
"ds_read_b64_tr_b16 a[112:113], v232 offset:34304\n" \
"v_mfma_f32_16x16x32_bf16 v[96:99], a[64:67], a[8:11], v[96:99]\n" \
"ds_read_b64_tr_b16 a[114:115], v232 offset:36352\n" \
"v_mfma_f32_16x16x32_bf16 v[96:99], a[68:71], a[12:15], v[96:99]\n" \
"buffer_load_dwordx4 v227, s[48:51], s77 offen offset:3072 lds\n" \
"v_mfma_f32_16x16x32_bf16 v[128:131], a[64:67], a[16:19], v[128:131]\n" \
"ds_read_b64_tr_b16 a[116:117], v232 offset:42496\n" \
"v_mfma_f32_16x16x32_bf16 v[128:131], a[68:71], a[20:23], v[128:131]\n" \
"ds_read_b64_tr_b16 a[118:119], v232 offset:44544\n" \
"v_mfma_f32_16x16x32_bf16 v[132:135], a[72:75], a[16:19], v[132:135]\n" \
"s_xor_b32 s77, s77, 0x180\n" \
"ds_read_b64_tr_b16 a[120:121], v233 offset:34304\n" \
"v_mfma_f32_16x16x32_bf16 v[132:135], a[76:79], a[20:23], v[132:135]\n" \
"buffer_store_dwordx4 a[236:239], v234, s[56:59], s93 offen offset:3072 sc1\n" \
"v_mfma_f32_16x16x32_bf16 v[136:139], a[80:83], a[16:19], v[136:139]\n" \
"ds_read_b64_tr_b16 a[122:123], v233 offset:36352\n" \
"v_mfma_f32_16x16x32_bf16 v[136:139], a[84:87], a[20:23], v[136:139]\n" \
"ds_read_b64_tr_b16 a[124:125], v233 offset:42496\n" \
"v_mfma_f32_16x16x32_bf16 v[140:143], a[88:91], a[16:19], v[140:143]\n" \
"ds_read_b64_tr_b16 a[126:127], v233 offset:44544\n" \
"v_mfma_f32_16x16x32_bf16 v[140:143], a[92:95], a[20:23], v[140:143]\n" \
"v_mfma_f32_16x16x32_bf16 v[172:175], a[88:91], a[24:27], v[172:175]\n" \
"v_mfma_f32_16x16x32_bf16 v[172:175], a[92:95], a[28:31], v[172:175]\n" \
"v_mfma_f32_16x16x32_bf16 v[168:171], a[80:83], a[24:27], v[168:171]\n" \
"s_add_u32 s93, s87, 17850368\n" \
"v_mfma_f32_16x16x32_bf16 v[168:171], a[84:87], a[28:31], v[168:171]\n" \
"buffer_store_dwordx4 a[192:195], v234, s[56:59], s93 offen offset:0 sc1\n" \
"v_mfma_f32_16x16x32_bf16 v[164:167], a[72:75], a[24:27], v[164:167]\n" \
"v_mfma_f32_16x16x32_bf16 v[164:167], a[76:79], a[28:31], v[164:167]\n" \
"v_mfma_f32_16x16x32_bf16 v[160:163], a[64:67], a[24:27], v[160:163]\n" \
"v_mfma_f32_16x16x32_bf16 v[160:163], a[68:71], a[28:31], v[160:163]\n" \
"s_waitcnt vmcnt(37) lgkmcnt(0)\n" \
"s_barrier\n" \
"v_mfma_f32_16x16x32_bf16 v[80:83], a[96:99], a[0:3], v[80:83]\n" \
"ds_read_b64_tr_b16 a[64:65], v232 offset:49152\n" \
"v_mfma_f32_16x16x32_bf16 v[80:83], a[100:103], a[4:7], v[80:83]\n" \
"ds_read_b64_tr_b16 a[66:67], v232 offset:51200\n" \
"v_mfma_f32_16x16x32_bf16 v[84:87], a[104:107], a[0:3], v[84:87]\n" \
"s_add_u32 m0, s79, 32768\n" \
"ds_read_b64_tr_b16 a[68:69], v232 offset:57344\n" \
"v_mfma_f32_16x16x32_bf16 v[84:87], a[108:111], a[4:7], v[84:87]\n" \
"buffer_load_dwordx4 v228, s[52:55], s78 offen offset:0 lds\n" \
"v_mfma_f32_16x16x32_bf16 v[88:91], a[112:115], a[0:3], v[88:91]\n" \
"ds_read_b64_tr_b16 a[70:71], v232 offset:59392\n" \
"v_mfma_f32_16x16x32_bf16 v[88:91], a[116:119], a[4:7], v[88:91]\n" \
"ds_read_b64_tr_b16 a[72:73], v233 offset:49152\n" \
"v_mfma_f32_16x16x32_bf16 v[92:95], a[120:123], a[0:3], v[92:95]\n" \
"ds_read_b64_tr_b16 a[74:75], v233 offset:51200\n" \
"v_mfma_f32_16x16x32_bf16 v[92:95], a[124:127], a[4:7], v[92:95]\n" \
"buffer_load_dwordx4 v229, s[52:55], s78 offen offset:1024 lds\n" \
"v_mfma_f32_16x16x32_bf16 v[124:127], a[120:123], a[8:11], v[124:127]\n" \
"s_waitcnt vmcnt(37)\n" \
"ds_read_b128 a[0:3], v230 offset:32768\n" \
"v_mfma_f32_16x16x32_bf16 v[124:127], a[124:127], a[12:15], v[124:127]\n" \
"ds_read_b128 a[4:7], v231 offset:32768\n" \
"v_mfma_f32_16x16x32_bf16 v[120:123], a[112:115], a[8:11], v[120:123]\n" \
"s_xor_b32 s78, s78, 0x80000\n" \
"ds_read_b64_tr_b16 a[76:77], v233 offset:57344\n" \
"v_mfma_f32_16x16x32_bf16 v[120:123], a[116:119], a[12:15], v[120:123]\n" \
"buffer_load_dwordx4 v228, s[52:55], s89 offen offset:2048 lds\n" \
"v_mfma_f32_16x16x32_bf16 v[116:119], a[104:107], a[8:11], v[116:119]\n" \
"ds_read_b64_tr_b16 a[78:79], v233 offset:59392\n" \
"v_mfma_f32_16x16x32_bf16 v[116:119], a[108:111], a[12:15], v[116:119]\n" \
"ds_read_b64_tr_b16 a[80:81], v232 offset:49664\n" \
"v_mfma_f32_16x16x32_bf16 v[112:115], a[96:99], a[8:11], v[112:115]\n" \
"ds_read_b64_tr_b16 a[82:83], v232 offset:51712\n" \
"v_mfma_f32_16x16x32_bf16 v[112:115], a[100:103], a[12:15], v[112:115]\n" \
"buffer_load_dwordx4 v229, s[52:55], s89 offen offset:3072 lds\n" \
"v_mfma_f32_16x16x32_bf16 v[144:147], a[96:99], a[16:19], v[144:147]\n" \
"s_waitcnt vmcnt(37)\n" \
"s_xor_b32 s89, s89, 0x80000\n" \
"ds_read_b128 a[8:11], v230 offset:34816\n" \
"v_mfma_f32_16x16x32_bf16 v[144:147], a[100:103], a[20:23], v[144:147]\n" \
"s_add_u32 m0, s74, 98304\n" \
"ds_read_b128 a[12:15], v231 offset:34816\n" \
"v_mfma_f32_16x16x32_bf16 v[148:151], a[104:107], a[16:19], v[148:151]\n" \
"s_waitcnt lgkmcnt(6)\n" \
"ds_read_b64_tr_b16 a[84:85], v232 offset:57856\n" \
"v_mfma_f32_16x16x32_bf16 v[148:151], a[108:111], a[20:23], v[148:151]\n" \
"buffer_load_dwordx4 v224, s[48:51], s75 offen offset:0 lds\n" \
"v_mfma_f32_16x16x32_bf16 v[152:155], a[112:115], a[16:19], v[152:155]\n" \
"ds_read_b64_tr_b16 a[86:87], v232 offset:59904\n" \
"v_mfma_f32_16x16x32_bf16 v[152:155], a[116:119], a[20:23], v[152:155]\n" \
"ds_read_b64_tr_b16 a[88:89], v233 offset:49664\n" \
"v_mfma_f32_16x16x32_bf16 v[156:159], a[120:123], a[16:19], v[156:159]\n" \
"ds_read_b64_tr_b16 a[90:91], v233 offset:51712\n" \
"v_mfma_f32_16x16x32_bf16 v[156:159], a[124:127], a[20:23], v[156:159]\n" \
"buffer_load_dwordx4 v225, s[48:51], s75 offen offset:1024 lds\n" \
"v_mfma_f32_16x16x32_bf16 v[188:191], a[120:123], a[24:27], v[188:191]\n" \
"s_waitcnt vmcnt(37)\n" \
"ds_read_b128 a[16:19], v230 offset:36864\n" \
"v_mfma_f32_16x16x32_bf16 v[188:191], a[124:127], a[28:31], v[188:191]\n" \
"ds_read_b128 a[20:23], v231 offset:36864\n" \
"v_mfma_f32_16x16x32_bf16 v[184:187], a[112:115], a[24:27], v[184:187]\n" \
"s_waitcnt lgkmcnt(6)\n" \
"ds_read_b64_tr_b16 a[92:93], v233 offset:57856\n" \
"v_mfma_f32_16x16x32_bf16 v[184:187], a[116:119], a[28:31], v[184:187]\n" \
"buffer_load_dwordx4 v226, s[48:51], s75 offen offset:2048 lds\n" \
"v_mfma_f32_16x16x32_bf16 v[180:183], a[104:107], a[24:27], v[180:183]\n" \
"ds_read_b64_tr_b16 a[94:95], v233 offset:59904\n" \
"v_mfma_f32_16x16x32_bf16 v[180:183], a[108:111], a[28:31], v[180:183]\n" \
"v_mfma_f32_16x16x32_bf16 v[176:179], a[96:99], a[24:27], v[176:179]\n" \
"v_mfma_f32_16x16x32_bf16 v[176:179], a[100:103], a[28:31], v[176:179]\n" \
"buffer_load_dwordx4 v227, s[48:51], s75 offen offset:3072 lds\n" \
"v_mfma_f32_16x16x32_bf16 v[64:67], a[64:67], a[0:3], v[64:67]\n" \
"s_waitcnt vmcnt(37)\n" \
"s_xor_b32 s75, s75, 0x80\n" \
"ds_read_b128 a[24:27], v230 offset:38912\n" \
"v_mfma_f32_16x16x32_bf16 v[64:67], a[68:71], a[4:7], v[64:67]\n" \
"s_add_u32 m0, s74, 102400\n" \
"ds_read_b128 a[28:31], v231 offset:38912\n" \
"v_mfma_f32_16x16x32_bf16 v[68:71], a[72:75], a[0:3], v[68:71]\n" \
"s_waitcnt lgkmcnt(2)\n" \
"ds_read_b64_tr_b16 a[96:97], v232 offset:50176\n" \
"v_mfma_f32_16x16x32_bf16 v[68:71], a[76:79], a[4:7], v[68:71]\n" \
"buffer_load_dwordx4 v224, s[48:51], s77 offen offset:0 lds\n" \
"v_mfma_f32_16x16x32_bf16 v[72:75], a[80:83], a[0:3], v[72:75]\n" \
"ds_read_b64_tr_b16 a[98:99], v232 offset:52224\n" \
"v_mfma_f32_16x16x32_bf16 v[72:75], a[84:87], a[4:7], v[72:75]\n" \
"ds_read_b64_tr_b16 a[100:101], v232 offset:58368\n" \
"v_mfma_f32_16x16x32_bf16 v[76:79], a[88:91], a[0:3], v[76:79]\n" \
"ds_read_b64_tr_b16 a[102:103], v232 offset:60416\n" \
"v_mfma_f32_16x16x32_bf16 v[76:79], a[92:95], a[4:7], v[76:79]\n" \
"buffer_load_dwordx4 v225, s[48:51], s77 offen offset:1024 lds\n" \
"v_mfma_f32_16x16x32_bf16 v[108:111], a[88:91], a[8:11], v[108:111]\n" \
"ds_read_b64_tr_b16 a[104:105], v233 offset:50176\n" \
"v_mfma_f32_16x16x32_bf16 v[108:111], a[92:95], a[12:15], v[108:111]\n" \
"ds_read_b64_tr_b16 a[106:107], v233 offset:52224\n" \
"v_mfma_f32_16x16x32_bf16 v[104:107], a[80:83], a[8:11], v[104:107]\n" \
"s_waitcnt lgkmcnt(6)\n" \
"ds_read_b64_tr_b16 a[108:109], v233 offset:58368\n" \
"v_mfma_f32_16x16x32_bf16 v[104:107], a[84:87], a[12:15], v[104:107]\n" \
"buffer_load_dwordx4 v226, s[48:51], s77 offen offset:2048 lds\n" \
"v_mfma_f32_16x16x32_bf16 v[100:103], a[72:75], a[8:11], v[100:103]\n" \
"ds_read_b64_tr_b16 a[110:111], v233 offset:60416\n" \
"v_mfma_f32_16x16x32_bf16 v[100:103], a[76:79], a[12:15], v[100:103]\n" \
"ds_read_b64_tr_b16 a[112:113], v232 offset:50688\n" \
"v_mfma_f32_16x16x32_bf16 v[96:99], a[64:67], a[8:11], v[96:99]\n" \
"ds_read_b64_tr_b16 a[114:115], v232 offset:52736\n" \
"v_mfma_f32_16x16x32_bf16 v[96:99], a[68:71], a[12:15], v[96:99]\n" \
"buffer_load_dwordx4 v227, s[48:51], s77 offen offset:3072 lds\n" \
"v_mfma_f32_16x16x32_bf16 v[128:131], a[64:67], a[16:19], v[128:131]\n" \
"ds_read_b64_tr_b16 a[116:117], v232 offset:58880\n" \
"v_mfma_f32_16x16x32_bf16 v[128:131], a[68:71], a[20:23], v[128:131]\n" \
"ds_read_b64_tr_b16 a[118:119], v232 offset:60928\n" \
"v_mfma_f32_16x16x32_bf16 v[132:135], a[72:75], a[16:19], v[132:135]\n" \
"s_xor_b32 s77, s77, 0x80\n" \
"ds_read_b64_tr_b16 a[120:121], v233 offset:50688\n" \
"v_mfma_f32_16x16x32_bf16 v[132:135], a[76:79], a[20:23], v[132:135]\n" \
"buffer_store_dwordx4 a[196:199], v234, s[56:59], s93 offen offset:1024 sc1\n" \
"v_mfma_f32_16x16x32_bf16 v[136:139], a[80:83], a[16:19], v[136:139]\n" \
"ds_read_b64_tr_b16 a[122:123], v233 offset:52736\n" \
"v_mfma_f32_16x16x32_bf16 v[136:139], a[84:87], a[20:23], v[136:139]\n" \
"ds_read_b64_tr_b16 a[124:125], v233 offset:58880\n" \
"v_mfma_f32_16x16x32_bf16 v[140:143], a[88:91], a[16:19], v[140:143]\n" \
"ds_read_b64_tr_b16 a[126:127], v233 offset:60928\n" \
"v_mfma_f32_16x16x32_bf16 v[140:143], a[92:95], a[20:23], v[140:143]\n" \
"buffer_store_dwordx4 a[200:203], v234, s[56:59], s93 offen offset:2048 sc1\n" \
"v_mfma_f32_16x16x32_bf16 v[172:175], a[88:91], a[24:27], v[172:175]\n" \
"v_mfma_f32_16x16x32_bf16 v[172:175], a[92:95], a[28:31], v[172:175]\n" \
"v_mfma_f32_16x16x32_bf16 v[168:171], a[80:83], a[24:27], v[168:171]\n" \
"v_mfma_f32_16x16x32_bf16 v[168:171], a[84:87], a[28:31], v[168:171]\n" \
"buffer_store_dwordx4 a[204:207], v234, s[56:59], s93 offen offset:3072 sc1\n" \
"v_mfma_f32_16x16x32_bf16 v[164:167], a[72:75], a[24:27], v[164:167]\n" \
"v_mfma_f32_16x16x32_bf16 v[164:167], a[76:79], a[28:31], v[164:167]\n" \
"v_mfma_f32_16x16x32_bf16 v[160:163], a[64:67], a[24:27], v[160:163]\n" \
"v_mfma_f32_16x16x32_bf16 v[160:163], a[68:71], a[28:31], v[160:163]\n" \
"s_waitcnt vmcnt(39) lgkmcnt(0)\n" \
"s_barrier\n" \
"v_mfma_f32_16x16x32_bf16 v[80:83], a[96:99], a[0:3], v[80:83]\n" \
"ds_read_b64_tr_b16 a[64:65], v232 offset:0\n" \
"v_mfma_f32_16x16x32_bf16 v[80:83], a[100:103], a[4:7], v[80:83]\n" \
"ds_read_b64_tr_b16 a[66:67], v232 offset:2048\n" \
"v_mfma_f32_16x16x32_bf16 v[84:87], a[104:107], a[0:3], v[84:87]\n" \
"s_add_u32 m0, s79, 49152\n" \
"ds_read_b64_tr_b16 a[68:69], v232 offset:8192\n" \
"v_mfma_f32_16x16x32_bf16 v[84:87], a[108:111], a[4:7], v[84:87]\n" \
"buffer_load_dwordx4 v228, s[52:55], s78 offen offset:0 lds\n" \
"v_mfma_f32_16x16x32_bf16 v[88:91], a[112:115], a[0:3], v[88:91]\n" \
"s_mul_i32 s73, s62, 557824\n" \
"s_add_u32 s85, s73, 17850368\n" \
"ds_read_b64_tr_b16 a[70:71], v232 offset:10240\n" \
"v_mfma_f32_16x16x32_bf16 v[88:91], a[116:119], a[4:7], v[88:91]\n" \
"s_mul_i32 s83, s63, 34816\n" \
"s_sub_u32 s86, 557056, s83\n" \
"ds_read_b64_tr_b16 a[72:73], v233 offset:0\n" \
"v_mfma_f32_16x16x32_bf16 v[92:95], a[120:123], a[0:3], v[92:95]\n" \
"ds_read_b64_tr_b16 a[74:75], v233 offset:2048\n" \
"v_add_u32 v237, %3, v236\n" \
"v_mfma_f32_16x16x32_bf16 v[92:95], a[124:127], a[4:7], v[92:95]\n" \
"buffer_load_dwordx4 v229, s[52:55], s78 offen offset:1024 lds\n" \
"v_mfma_f32_16x16x32_bf16 v[124:127], a[120:123], a[8:11], v[124:127]\n" \
"s_waitcnt vmcnt(39)\n" \
"ds_read_b128 a[0:3], v250 offset:0\n" \
"v_mfma_f32_16x16x32_bf16 v[124:127], a[124:127], a[12:15], v[124:127]\n" \
"s_add_u32 s36, s56, s73\n" \
"s_addc_u32 s37, s57, 0\n" \
"ds_read_b128 a[4:7], v251 offset:0\n" \
"v_mfma_f32_16x16x32_bf16 v[120:123], a[112:115], a[8:11], v[120:123]\n" \
"s_mov_b32 s38, s86\n" \
"s_mov_b32 s39, 0x20000\n" \
"ds_read_b64_tr_b16 a[76:77], v233 offset:8192\n" \
"v_mfma_f32_16x16x32_bf16 v[120:123], a[116:119], a[12:15], v[120:123]\n" \
"buffer_load_dwordx4 v228, s[52:55], s89 offen offset:2048 lds\n" \
"v_mfma_f32_16x16x32_bf16 v[116:119], a[104:107], a[8:11], v[116:119]\n" \
"ds_read_b64_tr_b16 a[78:79], v233 offset:10240\n" \
"v_add_u32 v235, %3, v234\n" \
"v_mfma_f32_16x16x32_bf16 v[116:119], a[108:111], a[12:15], v[116:119]\n" \
"s_add_u32 s40, s56, s85\n" \
"s_addc_u32 s41, s57, 0\n" \
"ds_read_b64_tr_b16 a[80:81], v232 offset:512\n" \
"v_mfma_f32_16x16x32_bf16 v[112:115], a[96:99], a[8:11], v[112:115]\n" \
"s_mov_b32 s42, s86\n" \
"s_mov_b32 s43, 0x20000\n" \
"ds_read_b64_tr_b16 a[82:83], v232 offset:2560\n" \
"v_mfma_f32_16x16x32_bf16 v[112:115], a[100:103], a[12:15], v[112:115]\n" \
"buffer_load_dwordx4 v229, s[52:55], s89 offen offset:3072 lds\n" \
"v_mfma_f32_16x16x32_bf16 v[144:147], a[96:99], a[16:19], v[144:147]\n" \
"s_waitcnt vmcnt(39)\n" \
"s_add_u32 s85, s74, 131072\n" \
"ds_read_b128 a[8:11], v250 offset:2048\n" \
"v_mfma_f32_16x16x32_bf16 v[144:147], a[100:103], a[20:23], v[144:147]\n" \
"s_add_u32 m0, s74, 131072\n" \
"ds_read_b128 a[12:15], v251 offset:2048\n" \
"v_add_u32 v239, s85, v234\n" \
"v_mfma_f32_16x16x32_bf16 v[148:151], a[104:107], a[16:19], v[148:151]\n" \
"s_waitcnt lgkmcnt(6)\n" \
"s_lshl_b32 s73, s61, 12\n" \
"ds_read_b64_tr_b16 a[84:85], v232 offset:8704\n" \
"v_mfma_f32_16x16x32_bf16 v[148:151], a[108:111], a[20:23], v[148:151]\n" \
"buffer_load_dwordx4 v224, s[48:51], s75 offen offset:0 lds\n" \
"v_mfma_f32_16x16x32_bf16 v[152:155], a[112:115], a[16:19], v[152:155]\n" \
"s_add_u32 s80, s79, 0x10000\n" \
"s_add_u32 s83, s83, s73\n" \
"ds_read_b64_tr_b16 a[86:87], v232 offset:10752\n" \
"v_mfma_f32_16x16x32_bf16 v[152:155], a[116:119], a[20:23], v[152:155]\n" \
"s_cmp_eq_u32 s63, 0\n" \
"s_cselect_b32 s85, 34816, 0\n" \
"ds_read_b64_tr_b16 a[88:89], v233 offset:512\n" \
"v_mfma_f32_16x16x32_bf16 v[156:159], a[120:123], a[16:19], v[156:159]\n" \
"s_add_u32 s73, s73, s85\n" \
"ds_read_b64_tr_b16 a[90:91], v233 offset:2560\n" \
"v_mfma_f32_16x16x32_bf16 v[156:159], a[124:127], a[20:23], v[156:159]\n" \
"buffer_load_dwordx4 v225, s[48:51], s75 offen offset:1024 lds\n" \
"v_mfma_f32_16x16x32_bf16 v[188:191], a[120:123], a[24:27], v[188:191]\n" \
"s_waitcnt vmcnt(39)\n" \
"ds_read_b128 a[16:19], v250 offset:4096\n" \
"v_mfma_f32_16x16x32_bf16 v[188:191], a[124:127], a[28:31], v[188:191]\n" \
"ds_read_b128 a[20:23], v251 offset:4096\n" \
"v_mfma_f32_16x16x32_bf16 v[184:187], a[112:115], a[24:27], v[184:187]\n" \
"s_waitcnt lgkmcnt(6)\n" \
"ds_read_b64_tr_b16 a[92:93], v233 offset:8704\n" \
"v_mfma_f32_16x16x32_bf16 v[184:187], a[116:119], a[28:31], v[184:187]\n" \
"buffer_load_dwordx4 v226, s[48:51], s75 offen offset:2048 lds\n" \
"v_mfma_f32_16x16x32_bf16 v[180:183], a[104:107], a[24:27], v[180:183]\n" \
"ds_read_b64_tr_b16 a[94:95], v233 offset:10752\n" \
"v_mfma_f32_16x16x32_bf16 v[180:183], a[108:111], a[28:31], v[180:183]\n" \
"v_mfma_f32_16x16x32_bf16 v[176:179], a[96:99], a[24:27], v[176:179]\n" \
"v_mfma_f32_16x16x32_bf16 v[176:179], a[100:103], a[28:31], v[176:179]\n" \
"buffer_load_dwordx4 v227, s[48:51], s75 offen offset:3072 lds\n" \
"v_mfma_f32_16x16x32_bf16 v[64:67], a[64:67], a[0:3], v[64:67]\n" \
"s_waitcnt vmcnt(39)\n" \
"ds_read_b128 a[24:27], v250 offset:6144\n" \
"v_mfma_f32_16x16x32_bf16 v[64:67], a[68:71], a[4:7], v[64:67]\n" \
"s_add_u32 m0, s74, 135168\n" \
"ds_read_b128 a[28:31], v251 offset:6144\n" \
"v_mfma_f32_16x16x32_bf16 v[68:71], a[72:75], a[0:3], v[68:71]\n" \
"s_waitcnt lgkmcnt(2)\n" \
"ds_read_b64_tr_b16 a[96:97], v232 offset:1024\n" \
"v_mfma_f32_16x16x32_bf16 v[68:71], a[76:79], a[4:7], v[68:71]\n" \
"buffer_load_dwordx4 v224, s[48:51], s77 offen offset:0 lds\n" \
"v_mfma_f32_16x16x32_bf16 v[72:75], a[80:83], a[0:3], v[72:75]\n" \
"ds_read_b64_tr_b16 a[98:99], v232 offset:3072\n" \
"v_mfma_f32_16x16x32_bf16 v[72:75], a[84:87], a[4:7], v[72:75]\n" \
"ds_read_b64_tr_b16 a[100:101], v232 offset:9216\n" \
"v_mfma_f32_16x16x32_bf16 v[76:79], a[88:91], a[0:3], v[76:79]\n" \
"ds_read_b64_tr_b16 a[102:103], v232 offset:11264\n" \
"v_mfma_f32_16x16x32_bf16 v[76:79], a[92:95], a[4:7], v[76:79]\n" \
"buffer_load_dwordx4 v225, s[48:51], s77 offen offset:1024 lds\n" \
"v_mfma_f32_16x16x32_bf16 v[108:111], a[88:91], a[8:11], v[108:111]\n" \
"ds_read_b64_tr_b16 a[104:105], v233 offset:1024\n" \
"v_mfma_f32_16x16x32_bf16 v[108:111], a[92:95], a[12:15], v[108:111]\n" \
"ds_read_b64_tr_b16 a[106:107], v233 offset:3072\n" \
"v_mfma_f32_16x16x32_bf16 v[104:107], a[80:83], a[8:11], v[104:107]\n" \
"s_waitcnt lgkmcnt(6)\n" \
"ds_read_b64_tr_b16 a[108:109], v233 offset:9216\n" \
"v_mfma_f32_16x16x32_bf16 v[104:107], a[84:87], a[12:15], v[104:107]\n" \
"buffer_load_dwordx4 v226, s[48:51], s77 offen offset:2048 lds\n" \
"v_mfma_f32_16x16x32_bf16 v[100:103], a[72:75], a[8:11], v[100:103]\n" \
"ds_read_b64_tr_b16 a[110:111], v233 offset:11264\n" \
"v_mfma_f32_16x16x32_bf16 v[100:103], a[76:79], a[12:15], v[100:103]\n" \
"ds_read_b64_tr_b16 a[112:113], v232 offset:1536\n" \
"v_mfma_f32_16x16x32_bf16 v[96:99], a[64:67], a[8:11], v[96:99]\n" \
"ds_read_b64_tr_b16 a[114:115], v232 offset:3584\n" \
"v_mfma_f32_16x16x32_bf16 v[96:99], a[68:71], a[12:15], v[96:99]\n" \
"buffer_load_dwordx4 v227, s[48:51], s77 offen offset:3072 lds\n" \
"v_mfma_f32_16x16x32_bf16 v[128:131], a[64:67], a[16:19], v[128:131]\n" \
"ds_read_b64_tr_b16 a[116:117], v232 offset:9728\n" \
"v_mfma_f32_16x16x32_bf16 v[128:131], a[68:71], a[20:23], v[128:131]\n" \
"ds_read_b64_tr_b16 a[118:119], v232 offset:11776\n" \
"v_mfma_f32_16x16x32_bf16 v[132:135], a[72:75], a[16:19], v[132:135]\n" \
"s_add_u32 s93, s94, 8192\n" \
"ds_read_b64_tr_b16 a[120:121], v233 offset:1536\n" \
"v_mfma_f32_16x16x32_bf16 v[132:135], a[76:79], a[20:23], v[132:135]\n" \
"buffer_store_dwordx4 v[32:35], v236, s[56:59], s93 offen offset:0 sc1\n" \
"v_mfma_f32_16x16x32_bf16 v[136:139], a[80:83], a[16:19], v[136:139]\n" \
"ds_read_b64_tr_b16 a[122:123], v233 offset:3584\n" \
"v_mfma_f32_16x16x32_bf16 v[136:139], a[84:87], a[20:23], v[136:139]\n" \
"ds_read_b64_tr_b16 a[124:125], v233 offset:9728\n" \
"v_mfma_f32_16x16x32_bf16 v[140:143], a[88:91], a[16:19], v[140:143]\n" \
"ds_read_b64_tr_b16 a[126:127], v233 offset:11776\n" \
"v_mfma_f32_16x16x32_bf16 v[140:143], a[92:95], a[20:23], v[140:143]\n" \
"v_mfma_f32_16x16x32_bf16 v[172:175], a[88:91], a[24:27], v[172:175]\n" \
"v_mfma_f32_16x16x32_bf16 v[172:175], a[92:95], a[28:31], v[172:175]\n" \
"v_mfma_f32_16x16x32_bf16 v[168:171], a[80:83], a[24:27], v[168:171]\n" \
"v_mfma_f32_16x16x32_bf16 v[168:171], a[84:87], a[28:31], v[168:171]\n" \
"v_mfma_f32_16x16x32_bf16 v[164:167], a[72:75], a[24:27], v[164:167]\n" \
"v_mfma_f32_16x16x32_bf16 v[164:167], a[76:79], a[28:31], v[164:167]\n" \
"v_mfma_f32_16x16x32_bf16 v[160:163], a[64:67], a[24:27], v[160:163]\n" \
"v_mfma_f32_16x16x32_bf16 v[160:163], a[68:71], a[28:31], v[160:163]\n" \
"s_waitcnt vmcnt(38) lgkmcnt(0)\n" \
"s_barrier\n" \
"v_mfma_f32_16x16x32_bf16 v[80:83], a[96:99], a[0:3], v[80:83]\n" \
"s_lshr_b32 s71, s61, 1\n" \
"s_lshl_b32 s71, s71, 14\n" \
"ds_read_b64_tr_b16 a[64:65], v232 offset:16384\n" \
"v_mfma_f32_16x16x32_bf16 v[80:83], a[100:103], a[4:7], v[80:83]\n" \
"s_xor_b32 s71, s71, 0x4000\n" \
"s_and_b32 s70, s61, 1\n" \
"ds_read_b64_tr_b16 a[66:67], v232 offset:18432\n" \
"v_mfma_f32_16x16x32_bf16 v[84:87], a[104:107], a[0:3], v[84:87]\n" \
"s_lshl_b32 s70, s70, 12\n" \
"s_add_u32 s71, s71, s70\n" \
"ds_read_b64_tr_b16 a[68:69], v232 offset:24576\n" \
"v_mfma_f32_16x16x32_bf16 v[84:87], a[108:111], a[4:7], v[84:87]\n" \
"buffer_store_dwordx4 v[48:51], v236, s[56:59], s93 offen offset:256 sc1\n" \
"v_mfma_f32_16x16x32_bf16 v[88:91], a[112:115], a[0:3], v[88:91]\n" \
"ds_read_b64_tr_b16 a[70:71], v232 offset:26624\n" \
"v_add_u32 v224, s71, v237\n" \
"v_mfma_f32_16x16x32_bf16 v[88:91], a[116:119], a[4:7], v[88:91]\n" \
"s_and_b32 s70, s63, 1\n" \
"s_lshl_b32 s70, s70, 15\n" \
"ds_read_b64_tr_b16 a[72:73], v233 offset:16384\n" \
"v_mfma_f32_16x16x32_bf16 v[92:95], a[120:123], a[0:3], v[92:95]\n" \
"s_xor_b32 s70, s70, 0x18000\n" \
"s_add_u32 s71, s71, s70\n" \
"ds_read_b64_tr_b16 a[74:75], v233 offset:18432\n" \
"v_mfma_f32_16x16x32_bf16 v[92:95], a[124:127], a[4:7], v[92:95]\n" \
"v_add_u32 v225, s71, v235\n" \
"s_lshr_b32 s64, s22, 7\n" \
"v_mfma_f32_16x16x32_bf16 v[124:127], a[120:123], a[8:11], v[124:127]\n" \
"s_waitcnt vmcnt(37)\n" \
"s_sub_u32 s64, 3, s64\n" \
"ds_read_b128 a[0:3], v230 offset:0\n" \
"v_mfma_f32_16x16x32_bf16 v[124:127], a[124:127], a[12:15], v[124:127]\n" \
"s_and_b32 s64, s64, 7\n" \
"s_and_b32 s65, %1, 1\n" \
"ds_read_b128 a[4:7], v231 offset:0\n" \
"v_mfma_f32_16x16x32_bf16 v[120:123], a[112:115], a[8:11], v[120:123]\n" \
"s_lshl_b32 s65, s65, 3\n" \
"s_add_u32 s64, s64, s65\n" \
"ds_read_b64_tr_b16 a[76:77], v233 offset:24576\n" \
"v_mfma_f32_16x16x32_bf16 v[120:123], a[116:119], a[12:15], v[120:123]\n" \
"s_lshl_b32 s64, s64, 7\n" \
"s_lshr_b32 s65, %1, 3\n" \
"s_lshl_b32 s65, s65, 2\n" \
"v_mfma_f32_16x16x32_bf16 v[116:119], a[104:107], a[8:11], v[116:119]\n" \
"s_add_u32 s65, s65, s61\n" \
"s_mul_i32 s65, s65, 24576\n" \
"ds_read_b64_tr_b16 a[78:79], v233 offset:26624\n" \
"v_mfma_f32_16x16x32_bf16 v[116:119], a[108:111], a[12:15], v[116:119]\n" \
"s_add_u32 s64, s64, s65\n" \
"ds_read_b64_tr_b16 a[80:81], v232 offset:16896\n" \
"v_bfe_u32 v254, v234, 7, 2\n" \
"v_mfma_f32_16x16x32_bf16 v[112:115], a[96:99], a[8:11], v[112:115]\n" \
"ds_read_b64_tr_b16 a[82:83], v232 offset:18944\n" \
"v_mul_u32_u24 v254, 3, v254\n" \
"v_mfma_f32_16x16x32_bf16 v[112:115], a[100:103], a[12:15], v[112:115]\n" \
"buffer_store_dwordx4 v[40:43], v236, s[56:59], s93 offen offset:2048 sc1\n" \
"v_mfma_f32_16x16x32_bf16 v[144:147], a[96:99], a[16:19], v[144:147]\n" \
"s_waitcnt vmcnt(36)\n" \
"ds_read_b128 a[8:11], v230 offset:2048\n" \
"v_bfe_u32 v243, v234, 5, 2\n" \
"v_mfma_f32_16x16x32_bf16 v[144:147], a[100:103], a[20:23], v[144:147]\n" \
"ds_read_b128 a[12:15], v231 offset:2048\n" \
"v_min_u32 v243, 2, v243\n" \
"v_mfma_f32_16x16x32_bf16 v[148:151], a[104:107], a[16:19], v[148:151]\n" \
"ds_read_b64_tr_b16 a[84:85], v232 offset:25088\n" \
"v_add_u32 v254, v254, v243\n" \
"v_mfma_f32_16x16x32_bf16 v[148:151], a[108:111], a[20:23], v[148:151]\n" \
"v_and_b32 v243, 16, v234\n" \
"v_lshl_add_u32 v243, v243, 2, s64\n" \
"v_mfma_f32_16x16x32_bf16 v[152:155], a[112:115], a[16:19], v[152:155]\n" \
"ds_read_b64_tr_b16 a[86:87], v232 offset:27136\n" \
"v_lshl_add_u32 v254, v254, 11, v243\n" \
"v_mfma_f32_16x16x32_bf16 v[152:155], a[116:119], a[20:23], v[152:155]\n" \
"ds_read_b64_tr_b16 a[88:89], v233 offset:16896\n" \
"v_mfma_f32_16x16x32_bf16 v[156:159], a[120:123], a[16:19], v[156:159]\n" \
"ds_read_b64_tr_b16 a[90:91], v233 offset:18944\n" \
"v_mfma_f32_16x16x32_bf16 v[156:159], a[124:127], a[20:23], v[156:159]\n" \
"v_mfma_f32_16x16x32_bf16 v[188:191], a[120:123], a[24:27], v[188:191]\n" \
"s_waitcnt vmcnt(34)\n" \
"ds_read_b128 a[16:19], v230 offset:4096\n" \
"v_mfma_f32_16x16x32_bf16 v[188:191], a[124:127], a[28:31], v[188:191]\n" \
"ds_read_b128 a[20:23], v231 offset:4096\n" \
"v_mfma_f32_16x16x32_bf16 v[184:187], a[112:115], a[24:27], v[184:187]\n" \
"ds_read_b64_tr_b16 a[92:93], v233 offset:25088\n" \
"v_mfma_f32_16x16x32_bf16 v[184:187], a[116:119], a[28:31], v[184:187]\n" \
"v_mfma_f32_16x16x32_bf16 v[180:183], a[104:107], a[24:27], v[180:183]\n" \
"ds_read_b64_tr_b16 a[94:95], v233 offset:27136\n" \
"v_mfma_f32_16x16x32_bf16 v[180:183], a[108:111], a[28:31], v[180:183]\n" \
"v_mfma_f32_16x16x32_bf16 v[176:179], a[96:99], a[24:27], v[176:179]\n" \
"buffer_store_dwordx4 v[56:59], v236, s[56:59], s93 offen offset:2304 sc1\n" \
"v_mfma_f32_16x16x32_bf16 v[176:179], a[100:103], a[28:31], v[176:179]\n" \
"s_waitcnt lgkmcnt(1)\n" \
"v_mfma_f32_16x16x32_bf16 v[64:67], a[64:67], a[0:3], v[64:67]\n" \
"Lpg4446_%=:\n" \
"s_cbranch_execz Lpg6189_%=\n" \
"s_waitcnt vmcnt(33)\n" \
"s_lshl_b32 s68, s63, 2\n" \
"ds_read_b128 a[24:27], v230 offset:6144\n" \
"v_mfma_f32_16x16x32_bf16 v[64:67], a[68:71], a[4:7], v[64:67]\n" \
"s_add_u32 s68, s68, s90\n" \
"ds_read_b128 a[28:31], v231 offset:6144\n" \
"v_mfma_f32_16x16x32_bf16 v[68:71], a[72:75], a[0:3], v[68:71]\n" \
"ds_read_b64_tr_b16 a[96:97], v232 offset:17408\n" \
"v_mfma_f32_16x16x32_bf16 v[68:71], a[76:79], a[4:7], v[68:71]\n" \
"v_mov_b32 v228, s60\n" \
"v_lshlrev_b32 v229, 28, s61\n" \
"v_mfma_f32_16x16x32_bf16 v[72:75], a[80:83], a[0:3], v[72:75]\n" \
"ds_read_b64_tr_b16 a[98:99], v232 offset:19456\n" \
"v_mfma_f32_16x16x32_bf16 v[72:75], a[84:87], a[4:7], v[72:75]\n" \
"s_waitcnt lgkmcnt(3)\n" \
"ds_read_b64_tr_b16 a[100:101], v232 offset:25600\n" \
"v_mfma_f32_16x16x32_bf16 v[76:79], a[88:91], a[0:3], v[76:79]\n" \
"ds_read_b64_tr_b16 a[102:103], v232 offset:27648\n" \
"v_mfma_f32_16x16x32_bf16 v[76:79], a[92:95], a[4:7], v[76:79]\n" \
"v_mfma_f32_16x16x32_bf16 v[108:111], a[88:91], a[8:11], v[108:111]\n" \
"ds_read_b64_tr_b16 a[104:105], v233 offset:17408\n" \
"v_mfma_f32_16x16x32_bf16 v[108:111], a[92:95], a[12:15], v[108:111]\n" \
"ds_read_b64_tr_b16 a[106:107], v233 offset:19456\n" \
"v_mfma_f32_16x16x32_bf16 v[104:107], a[80:83], a[8:11], v[104:107]\n" \
"ds_read_b64_tr_b16 a[108:109], v233 offset:25600\n" \
"v_mfma_f32_16x16x32_bf16 v[104:107], a[84:87], a[12:15], v[104:107]\n" \
"v_mfma_f32_16x16x32_bf16 v[100:103], a[72:75], a[8:11], v[100:103]\n" \
"ds_read_b64_tr_b16 a[110:111], v233 offset:27648\n" \
"v_mfma_f32_16x16x32_bf16 v[100:103], a[76:79], a[12:15], v[100:103]\n" \
"ds_read_b64_tr_b16 a[112:113], v232 offset:17920\n" \
"v_mfma_f32_16x16x32_bf16 v[96:99], a[64:67], a[8:11], v[96:99]\n" \
"s_add_u32 s93, s94, 0\n" \
"ds_read_b64_tr_b16 a[114:115], v232 offset:19968\n" \
"v_mfma_f32_16x16x32_bf16 v[96:99], a[68:71], a[12:15], v[96:99]\n" \
"buffer_store_dwordx4 v[0:3], v236, s[56:59], s93 offen offset:0 sc1\n" \
"v_mfma_f32_16x16x32_bf16 v[128:131], a[64:67], a[16:19], v[128:131]\n" \
"ds_read_b64_tr_b16 a[116:117], v232 offset:26112\n" \
"v_mfma_f32_16x16x32_bf16 v[128:131], a[68:71], a[20:23], v[128:131]\n" \
"ds_read_b64_tr_b16 a[118:119], v232 offset:28160\n" \
"v_mfma_f32_16x16x32_bf16 v[132:135], a[72:75], a[16:19], v[132:135]\n" \
"ds_read_b64_tr_b16 a[120:121], v233 offset:17920\n" \
"v_mfma_f32_16x16x32_bf16 v[132:135], a[76:79], a[20:23], v[132:135]\n" \
"v_mfma_f32_16x16x32_bf16 v[136:139], a[80:83], a[16:19], v[136:139]\n" \
"ds_read_b64_tr_b16 a[122:123], v233 offset:19968\n" \
"v_mfma_f32_16x16x32_bf16 v[136:139], a[84:87], a[20:23], v[136:139]\n" \
"ds_read_b64_tr_b16 a[124:125], v233 offset:26112\n" \
"v_mfma_f32_16x16x32_bf16 v[140:143], a[88:91], a[16:19], v[140:143]\n" \
"ds_read_b64_tr_b16 a[126:127], v233 offset:28160\n" \
"v_mfma_f32_16x16x32_bf16 v[140:143], a[92:95], a[20:23], v[140:143]\n" \
"s_waitcnt lgkmcnt(3)\n" \
"v_mfma_f32_16x16x32_bf16 v[172:175], a[88:91], a[24:27], v[172:175]\n" \
"v_mfma_f32_16x16x32_bf16 v[172:175], a[92:95], a[28:31], v[172:175]\n" \
"v_mfma_f32_16x16x32_bf16 v[168:171], a[80:83], a[24:27], v[168:171]\n" \
"v_mfma_f32_16x16x32_bf16 v[168:171], a[84:87], a[28:31], v[168:171]\n" \
"v_mfma_f32_16x16x32_bf16 v[164:167], a[72:75], a[24:27], v[164:167]\n" \
"v_mfma_f32_16x16x32_bf16 v[164:167], a[76:79], a[28:31], v[164:167]\n" \
"buffer_store_dwordx4 v[16:19], v236, s[56:59], s93 offen offset:256 sc1\n" \
"v_mfma_f32_16x16x32_bf16 v[160:163], a[64:67], a[24:27], v[160:163]\n" \
"v_mfma_f32_16x16x32_bf16 v[160:163], a[68:71], a[28:31], v[160:163]\n" \
"s_waitcnt vmcnt(18) lgkmcnt(0)\n" \
"s_barrier\n" \
"v_mfma_f32_16x16x32_bf16 v[80:83], a[96:99], a[0:3], v[80:83]\n" \
"ds_read_b64_tr_b16 a[64:65], v232 offset:32768\n" \
"v_mfma_f32_16x16x32_bf16 v[80:83], a[100:103], a[4:7], v[80:83]\n" \
"ds_read_b64_tr_b16 a[66:67], v232 offset:34816\n" \
"buffer_store_dword v228, v229, s[56:59], s68 offen sc1\n" \
"v_mfma_f32_16x16x32_bf16 v[84:87], a[104:107], a[0:3], v[84:87]\n" \
"ds_read_b64_tr_b16 a[68:69], v232 offset:40960\n" \
"v_lshrrev_b32 v238, 4, v234\n" \
"v_mfma_f32_16x16x32_bf16 v[84:87], a[108:111], a[4:7], v[84:87]\n" \
"ds_write_b128 v224, v[192:195] offset:8192\n" \
"v_mfma_f32_16x16x32_bf16 v[88:91], a[112:115], a[0:3], v[88:91]\n" \
"ds_read_b64_tr_b16 a[70:71], v232 offset:43008\n" \
"v_lshrrev_b32 v240, 4, v238\n" \
"v_mfma_f32_16x16x32_bf16 v[88:91], a[116:119], a[4:7], v[88:91]\n" \
"ds_read_b64_tr_b16 a[72:73], v233 offset:32768\n" \
"v_lshlrev_b32 v240, 2, v240\n" \
"v_mfma_f32_16x16x32_bf16 v[92:95], a[120:123], a[0:3], v[92:95]\n" \
"ds_read_b64_tr_b16 a[74:75], v233 offset:34816\n" \
"v_and_b32 v238, 15, v238\n" \
"v_mfma_f32_16x16x32_bf16 v[92:95], a[124:127], a[4:7], v[92:95]\n" \
"v_sub_u32 v238, v238, v240\n" \
"v_subrev_u32 v248, 0, v238\n" \
"v_mfma_f32_16x16x32_bf16 v[124:127], a[120:123], a[8:11], v[124:127]\n" \
"ds_read_b128 a[0:3], v230 offset:32768\n" \
"v_subrev_u32 v249, 1, v238\n" \
"v_mfma_f32_16x16x32_bf16 v[124:127], a[124:127], a[12:15], v[124:127]\n" \
"ds_read_b128 a[4:7], v231 offset:32768\n" \
"v_subrev_u32 v250, 2, v238\n" \
"v_mfma_f32_16x16x32_bf16 v[120:123], a[112:115], a[8:11], v[120:123]\n" \
"ds_read_b64_tr_b16 a[76:77], v233 offset:40960\n" \
"v_subrev_u32 v251, 3, v238\n" \
"v_mfma_f32_16x16x32_bf16 v[120:123], a[116:119], a[12:15], v[120:123]\n" \
"ds_write_b128 v224, v[208:211] offset:8448\n" \
"v_mfma_f32_16x16x32_bf16 v[116:119], a[104:107], a[8:11], v[116:119]\n" \
"ds_read_b64_tr_b16 a[78:79], v233 offset:43008\n" \
"v_ashrrev_i32 v248, 31, v248\n" \
"v_mfma_f32_16x16x32_bf16 v[116:119], a[108:111], a[12:15], v[116:119]\n" \
"ds_read_b64_tr_b16 a[80:81], v232 offset:33280\n" \
"v_ashrrev_i32 v249, 31, v249\n" \
"v_mfma_f32_16x16x32_bf16 v[112:115], a[96:99], a[8:11], v[112:115]\n" \
"ds_read_b64_tr_b16 a[82:83], v232 offset:35328\n" \
"v_ashrrev_i32 v250, 31, v250\n" \
"v_mfma_f32_16x16x32_bf16 v[112:115], a[100:103], a[12:15], v[112:115]\n" \
"buffer_store_dwordx4 v[8:11], v236, s[56:59], s93 offen offset:2048 sc1\n" \
"v_mfma_f32_16x16x32_bf16 v[144:147], a[96:99], a[16:19], v[144:147]\n" \
"ds_read_b128 a[8:11], v230 offset:34816\n" \
"v_ashrrev_i32 v251, 31, v251\n" \
"v_mfma_f32_16x16x32_bf16 v[144:147], a[100:103], a[20:23], v[144:147]\n" \
"ds_read_b128 a[12:15], v231 offset:34816\n" \
"v_lshlrev_b32 v248, 23, v248\n" \
"v_mfma_f32_16x16x32_bf16 v[148:151], a[104:107], a[16:19], v[148:151]\n" \
"ds_read_b64_tr_b16 a[84:85], v232 offset:41472\n" \
"v_lshlrev_b32 v249, 23, v249\n" \
"v_mfma_f32_16x16x32_bf16 v[148:151], a[108:111], a[20:23], v[148:151]\n" \
"ds_write_b128 v224, v[200:203] offset:10240\n" \
"v_mfma_f32_16x16x32_bf16 v[152:155], a[112:115], a[16:19], v[152:155]\n" \
"ds_read_b64_tr_b16 a[86:87], v232 offset:43520\n" \
"v_lshlrev_b32 v250, 23, v250\n" \
"v_mfma_f32_16x16x32_bf16 v[152:155], a[116:119], a[20:23], v[152:155]\n" \
"ds_read_b64_tr_b16 a[88:89], v233 offset:33280\n" \
"v_lshlrev_b32 v251, 23, v251\n" \
"v_mfma_f32_16x16x32_bf16 v[156:159], a[120:123], a[16:19], v[156:159]\n" \
"ds_read_b64_tr_b16 a[90:91], v233 offset:35328\n" \
"v_mov_b32 v240, 0xff800000\n" \
"v_mfma_f32_16x16x32_bf16 v[156:159], a[124:127], a[20:23], v[156:159]\n" \
"ds_write_b128 v224, v[216:219] offset:10496\n" \
"v_mfma_f32_16x16x32_bf16 v[188:191], a[120:123], a[24:27], v[188:191]\n" \
"ds_read_b128 a[16:19], v230 offset:36864\n" \
"v_add_f32 v248, 0x42400000, v248\n" \
"v_mfma_f32_16x16x32_bf16 v[188:191], a[124:127], a[28:31], v[188:191]\n" \
"ds_read_b128 a[20:23], v231 offset:36864\n" \
"v_add_f32 v249, 0x42400000, v249\n" \
"v_mfma_f32_16x16x32_bf16 v[184:187], a[112:115], a[24:27], v[184:187]\n" \
"ds_read_b64_tr_b16 a[92:93], v233 offset:41472\n" \
"v_add_f32 v250, 0x42400000, v250\n" \
"v_mfma_f32_16x16x32_bf16 v[184:187], a[116:119], a[28:31], v[184:187]\n" \
"v_add_f32 v251, 0x42400000, v251\n" \
"s_mov_b32 s72, 0x42400000\n" \
"v_mfma_f32_16x16x32_bf16 v[180:183], a[104:107], a[24:27], v[180:183]\n" \
"s_cmp_gt_u32 s61, 0\n" \
"s_cselect_b32 s70, s72, 0xff800000\n" \
"ds_read_b64_tr_b16 a[94:95], v233 offset:43520\n" \
"v_mfma_f32_16x16x32_bf16 v[180:183], a[108:111], a[28:31], v[180:183]\n" \
"buffer_store_dwordx4 v[24:27], v236, s[56:59], s93 offen offset:2304 sc1\n" \
"v_mfma_f32_16x16x32_bf16 v[176:179], a[96:99], a[24:27], v[176:179]\n" \
"ds_write_b128 v224, v[196:199] offset:0\n" \
"v_mfma_f32_16x16x32_bf16 v[176:179], a[100:103], a[28:31], v[176:179]\n" \
"s_waitcnt lgkmcnt(2)\n" \
"v_mov_b32 v252, s70\n" \
"v_mfma_f32_16x16x32_bf16 v[64:67], a[64:67], a[0:3], v[64:67]\n" \
"ds_read_b128 a[24:27], v230 offset:38912\n" \
"v_mfma_f32_16x16x32_bf16 v[64:67], a[68:71], a[4:7], v[64:67]\n" \
"s_cmp_gt_u32 s61, 1\n" \
"s_cselect_b32 s70, s72, 0xff800000\n" \
"ds_read_b128 a[28:31], v231 offset:38912\n" \
"v_mfma_f32_16x16x32_bf16 v[68:71], a[72:75], a[0:3], v[68:71]\n" \
"ds_read_b64_tr_b16 a[96:97], v232 offset:33792\n" \
"v_mov_b32 v253, s70\n" \
"v_mfma_f32_16x16x32_bf16 v[68:71], a[76:79], a[4:7], v[68:71]\n" \
"s_cmp_gt_u32 s61, 2\n" \
"s_cselect_b32 s70, s72, 0xff800000\n" \
"v_mov_b32 v226, s70\n" \
"v_mfma_f32_16x16x32_bf16 v[72:75], a[80:83], a[0:3], v[72:75]\n" \
"ds_read_b64_tr_b16 a[98:99], v232 offset:35840\n" \
"v_bfe_i32 v243, v234, 4, 1\n" \
"v_mfma_f32_16x16x32_bf16 v[72:75], a[84:87], a[4:7], v[72:75]\n" \
"s_waitcnt lgkmcnt(3)\n" \
"ds_read_b64_tr_b16 a[100:101], v232 offset:41984\n" \
"v_and_b32 v243, 0xaaaa, v243\n" \
"v_mfma_f32_16x16x32_bf16 v[76:79], a[88:91], a[0:3], v[76:79]\n" \
"ds_read_b64_tr_b16 a[102:103], v232 offset:44032\n" \
"v_or_b32 v243, 0x4444, v243\n" \
"v_mfma_f32_16x16x32_bf16 v[76:79], a[92:95], a[4:7], v[76:79]\n" \
"ds_write_b128 v224, v[212:215] offset:256\n" \
"v_mfma_f32_16x16x32_bf16 v[108:111], a[88:91], a[8:11], v[108:111]\n" \
"ds_read_b64_tr_b16 a[104:105], v233 offset:33792\n" \
"v_bfe_i32 v244, v234, 5, 1\n" \
"v_mfma_f32_16x16x32_bf16 v[108:111], a[92:95], a[12:15], v[108:111]\n" \
"ds_read_b64_tr_b16 a[106:107], v233 offset:35840\n" \
"v_not_b32 v244, v244\n" \
"v_mfma_f32_16x16x32_bf16 v[104:107], a[80:83], a[8:11], v[104:107]\n" \
"ds_read_b64_tr_b16 a[108:109], v233 offset:41984\n" \
"v_and_b32 v244, 0x3f803f80, v244\n" \
"v_mfma_f32_16x16x32_bf16 v[104:107], a[84:87], a[12:15], v[104:107]\n" \
"v_mov_b32 v245, v244\n" \
"v_mov_b32 v246, v244\n" \
"v_mfma_f32_16x16x32_bf16 v[100:103], a[72:75], a[8:11], v[100:103]\n" \
"ds_read_b64_tr_b16 a[110:111], v233 offset:44032\n" \
"v_mov_b32 v247, v244\n" \
"v_mfma_f32_16x16x32_bf16 v[100:103], a[76:79], a[12:15], v[100:103]\n" \
"s_mov_b32 s76, s73\n" \
"s_and_b32 s77, s63, 1\n" \
"ds_read_b64_tr_b16 a[112:113], v232 offset:34304\n" \
"v_mfma_f32_16x16x32_bf16 v[96:99], a[64:67], a[8:11], v[96:99]\n" \
"s_add_u32 s93, s94, 17858560\n" \
"s_lshl_b32 s77, s77, 15\n" \
"ds_read_b64_tr_b16 a[114:115], v232 offset:36352\n" \
"v_mfma_f32_16x16x32_bf16 v[96:99], a[68:71], a[12:15], v[96:99]\n" \
"buffer_store_dwordx4 v[36:39], v234, s[56:59], s93 offen offset:0 sc1\n" \
"v_mfma_f32_16x16x32_bf16 v[128:131], a[64:67], a[16:19], v[128:131]\n" \
"s_xor_b32 s89, s77, 0x8000\n" \
"s_add_u32 s82, s89, 0x10000\n" \
"ds_read_b64_tr_b16 a[116:117], v232 offset:42496\n" \
"v_mfma_f32_16x16x32_bf16 v[128:131], a[68:71], a[20:23], v[128:131]\n" \
"s_mov_b32 s74, 0\n" \
"s_add_u32 s73, s73, 34816\n" \
"ds_read_b64_tr_b16 a[118:119], v232 offset:44544\n" \
"v_mfma_f32_16x16x32_bf16 v[132:135], a[72:75], a[16:19], v[132:135]\n" \
"s_cmp_eq_u32 s73, s83\n" \
"s_cselect_b32 s70, 34816, 0\n" \
"ds_read_b64_tr_b16 a[120:121], v233 offset:34304\n" \
"v_mfma_f32_16x16x32_bf16 v[132:135], a[76:79], a[20:23], v[132:135]\n" \
"ds_write_b128 v224, v[204:207] offset:2048\n" \
"v_mfma_f32_16x16x32_bf16 v[136:139], a[80:83], a[16:19], v[136:139]\n" \
"s_add_u32 s73, s73, s70\n" \
"s_lshr_b32 s70, s61, 1\n" \
"ds_read_b64_tr_b16 a[122:123], v233 offset:36352\n" \
"v_mfma_f32_16x16x32_bf16 v[136:139], a[84:87], a[20:23], v[136:139]\n" \
"s_lshl_b32 s85, s63, 2\n" \
"s_sub_u32 s86, 60, s85\n" \
"ds_read_b64_tr_b16 a[124:125], v233 offset:42496\n" \
"v_mfma_f32_16x16x32_bf16 v[140:143], a[88:91], a[16:19], v[140:143]\n" \
"s_add_u32 s85, s85, s70\n" \
"s_add_u32 s86, s86, s70\n" \
"ds_read_b64_tr_b16 a[126:127], v233 offset:44544\n" \
"v_mfma_f32_16x16x32_bf16 v[140:143], a[92:95], a[20:23], v[140:143]\n" \
"s_waitcnt lgkmcnt(4)\n" \
"ds_write_b128 v224, v[220:223] offset:2304\n" \
"v_mfma_f32_16x16x32_bf16 v[172:175], a[88:91], a[24:27], v[172:175]\n" \
"s_mul_i32 s87, s85, 266240\n" \
"s_mul_i32 s88, s86, 266240\n" \
"v_add_u32 v230, 0x10000, v230\n" \
"v_mfma_f32_16x16x32_bf16 v[172:175], a[92:95], a[28:31], v[172:175]\n" \
"s_and_b32 s75, s61, 1\n" \
"s_lshl_b32 s70, s75, 8\n" \
"v_add_u32 v231, 0x10000, v231\n" \
"v_mfma_f32_16x16x32_bf16 v[168:171], a[80:83], a[24:27], v[168:171]\n" \
"s_lshl_b32 s91, s62, 13\n" \
"s_add_u32 s91, s91, s70\n" \
"s_add_u32 s91, s91, 35700736\n" \
"v_mfma_f32_16x16x32_bf16 v[168:171], a[84:87], a[28:31], v[168:171]\n" \
"buffer_store_dwordx4 v[52:55], v234, s[56:59], s93 offen offset:1024 sc1\n" \
"v_mfma_f32_16x16x32_bf16 v[164:167], a[72:75], a[24:27], v[164:167]\n" \
"s_add_u32 s87, s87, s91\n" \
"s_add_u32 s88, s88, s91\n" \
"s_lshl_b32 s75, s75, 14\n" \
"v_mfma_f32_16x16x32_bf16 v[164:167], a[76:79], a[28:31], v[164:167]\n" \
"s_cmp_eq_u32 s63, 7\n" \
"s_cselect_b32 s75, 0, s75\n" \
"s_xor_b32 s78, s75, 0xc000\n" \
"s_mov_b32 s81, s78\n" \
"s_cmp_eq_u32 s63, 0\n" \
"s_cselect_b32 s91, s75, 0\n" \
"s_add_u32 s91, s91, 0x8000\n" \
"s_xor_b32 s75, s75, 0x8000\n" \
"s_sub_u32 s84, 7, s63\n" \
"v_mfma_f32_16x16x32_bf16 v[160:163], a[64:67], a[24:27], v[160:163]\n" \
"v_mfma_f32_16x16x32_bf16 v[160:163], a[68:71], a[28:31], v[160:163]\n" \
"s_waitcnt vmcnt(19) lgkmcnt(0)\n" \
"s_barrier\n" \
"v_mfma_f32_16x16x32_bf16 v[144:147], a[96:99], a[16:19], v[144:147]\n" \
"s_mov_b64 s[44:45], s[10:11]\n" \
"s_mov_b32 s46, 0\n" \
"ds_read_b64_tr_b16 a[64:65], v232 offset:49152\n" \
"v_mfma_f32_16x16x32_bf16 v[144:147], a[100:103], a[20:23], v[144:147]\n" \
"s_mov_b32 s47, 0\n" \
"s_cmp_eq_u32 s63, 7\n" \
"s_cselect_b64 s[44:45], s[18:19], s[44:45]\n" \
"s_cselect_b32 s46, 5, s46\n" \
"s_cmp_eq_u32 s63, 3\n" \
"s_cselect_b64 s[44:45], s[18:19], s[44:45]\n" \
"s_cselect_b32 s46, 3, s46\n" \
"ds_read_b64_tr_b16 a[66:67], v232 offset:51200\n" \
"v_mfma_f32_16x16x32_bf16 v[148:151], a[104:107], a[16:19], v[148:151]\n" \
"s_cmp_eq_u32 s63, 3\n" \
"s_cselect_b32 s47, 640, s47\n" \
"ds_read_b64_tr_b16 a[68:69], v232 offset:57344\n" \
"v_mfma_f32_16x16x32_bf16 v[148:151], a[108:111], a[20:23], v[148:151]\n" \
"ds_write_b128 v225, a[192:195] offset:0\n" \
"v_mfma_f32_16x16x32_bf16 v[152:155], a[112:115], a[16:19], v[152:155]\n" \
"ds_read_b64_tr_b16 a[70:71], v232 offset:59392\n" \
"v_mfma_f32_16x16x32_bf16 v[152:155], a[116:119], a[20:23], v[152:155]\n" \
"s_cmp_eq_u32 s63, 6\n" \
"s_cselect_b64 s[44:45], s[22:23], s[44:45]\n" \
"s_cselect_b32 s46, 6, s46\n" \
"ds_read_b64_tr_b16 a[72:73], v233 offset:49152\n" \
"v_mfma_f32_16x16x32_bf16 v[156:159], a[120:123], a[16:19], v[156:159]\n" \
"ds_read_b64_tr_b16 a[74:75], v233 offset:51200\n" \
"v_mfma_f32_16x16x32_bf16 v[156:159], a[124:127], a[20:23], v[156:159]\n" \
"s_cmp_eq_u32 s63, 4\n" \
"s_cselect_b64 s[44:45], s[20:21], s[44:45]\n" \
"s_cselect_b32 s46, 4, s46\n" \
"v_mfma_f32_16x16x32_bf16 v[188:191], a[120:123], a[24:27], v[188:191]\n" \
"s_waitcnt vmcnt(17)\n" \
"ds_read_b128 a[16:19], v230 offset:0\n" \
"v_mfma_f32_16x16x32_bf16 v[188:191], a[124:127], a[28:31], v[188:191]\n" \
"s_cmp_eq_u32 s63, 2\n" \
"s_cselect_b64 s[44:45], s[24:25], s[44:45]\n" \
"s_cselect_b32 s46, 2, s46\n" \
"ds_read_b128 a[20:23], v231 offset:0\n" \
"v_mfma_f32_16x16x32_bf16 v[184:187], a[112:115], a[24:27], v[184:187]\n" \
"s_lshl_b32 s48, s62, 2\n" \
"s_add_u32 s48, s48, s61\n" \
"ds_read_b64_tr_b16 a[76:77], v233 offset:57344\n" \
"v_mfma_f32_16x16x32_bf16 v[184:187], a[116:119], a[28:31], v[184:187]\n" \
"buffer_store_dwordx4 v[44:47], v234, s[56:59], s93 offen offset:2048 sc1\n" \
"v_mfma_f32_16x16x32_bf16 v[180:183], a[104:107], a[24:27], v[180:183]\n" \
"s_add_u32 s48, s48, s47\n" \
"s_lshl_b32 s48, s48, 13\n" \
"ds_read_b64_tr_b16 a[78:79], v233 offset:59392\n" \
"v_mfma_f32_16x16x32_bf16 v[180:183], a[108:111], a[28:31], v[180:183]\n" \
"s_add_u32 s44, s44, s48\n" \
"s_addc_u32 s45, s45, 0\n" \
"ds_read_b64_tr_b16 a[80:81], v232 offset:49664\n" \
"v_mfma_f32_16x16x32_bf16 v[176:179], a[96:99], a[24:27], v[176:179]\n" \
"s_cmp_eq_u32 s46, 0\n" \
"s_cselect_b32 s48, 28, 3\n" \
"ds_read_b64_tr_b16 a[82:83], v232 offset:51712\n" \
"v_mfma_f32_16x16x32_bf16 v[176:179], a[100:103], a[28:31], v[176:179]\n" \
"ds_write_b128 v225, a[196:199] offset:1024\n" \
"v_mfma_f32_16x16x32_bf16 v[80:83], a[96:99], a[0:3], v[80:83]\n" \
"s_waitcnt vmcnt(16)\n" \
"ds_read_b128 a[24:27], v230 offset:2048\n" \
"v_mfma_f32_16x16x32_bf16 v[80:83], a[100:103], a[4:7], v[80:83]\n" \
"ds_read_b128 a[28:31], v231 offset:2048\n" \
"v_mfma_f32_16x16x32_bf16 v[84:87], a[104:107], a[0:3], v[84:87]\n" \
"ds_read_b64_tr_b16 a[84:85], v232 offset:57856\n" \
"v_mfma_f32_16x16x32_bf16 v[84:87], a[108:111], a[4:7], v[84:87]\n" \
"ds_write_b128 v225, a[200:203] offset:2048\n" \
"v_mfma_f32_16x16x32_bf16 v[88:91], a[112:115], a[0:3], v[88:91]\n" \
"ds_read_b64_tr_b16 a[86:87], v232 offset:59904\n" \
"v_mfma_f32_16x16x32_bf16 v[88:91], a[116:119], a[4:7], v[88:91]\n" \
"ds_read_b64_tr_b16 a[88:89], v233 offset:49664\n" \
"v_mfma_f32_16x16x32_bf16 v[92:95], a[120:123], a[0:3], v[92:95]\n" \
"ds_read_b64_tr_b16 a[90:91], v233 offset:51712\n" \
"v_mfma_f32_16x16x32_bf16 v[92:95], a[124:127], a[4:7], v[92:95]\n" \
"ds_write_b128 v225, a[204:207] offset:3072\n" \
"v_mfma_f32_16x16x32_bf16 v[124:127], a[120:123], a[8:11], v[124:127]\n" \
"s_waitcnt vmcnt(14)\n" \
"ds_read_b128 a[0:3], v230 offset:4096\n" \
"v_mfma_f32_16x16x32_bf16 v[124:127], a[124:127], a[12:15], v[124:127]\n" \
"ds_read_b128 a[4:7], v231 offset:4096\n" \
"v_mfma_f32_16x16x32_bf16 v[120:123], a[112:115], a[8:11], v[120:123]\n" \
"ds_read_b64_tr_b16 a[92:93], v233 offset:57856\n" \
"v_mfma_f32_16x16x32_bf16 v[120:123], a[116:119], a[12:15], v[120:123]\n" \
"buffer_store_dwordx4 v[60:63], v234, s[56:59], s93 offen offset:3072 sc1\n" \
"v_mfma_f32_16x16x32_bf16 v[116:119], a[104:107], a[8:11], v[116:119]\n" \
"ds_read_b64_tr_b16 a[94:95], v233 offset:59904\n" \
"v_mfma_f32_16x16x32_bf16 v[116:119], a[108:111], a[12:15], v[116:119]\n" \
"v_mfma_f32_16x16x32_bf16 v[112:115], a[96:99], a[8:11], v[112:115]\n" \
"ds_write_b128 v225, a[224:227] offset:8192\n" \
"v_mfma_f32_16x16x32_bf16 v[112:115], a[100:103], a[12:15], v[112:115]\n" \
"s_waitcnt lgkmcnt(2)\n" \
"v_mfma_f32_16x16x32_bf16 v[64:67], a[64:67], a[16:19], v[64:67]\n" \
"s_waitcnt vmcnt(13)\n" \
"ds_read_b128 a[8:11], v230 offset:6144\n" \
"ds_read_b128 a[12:15], v231 offset:6144\n" \
"v_mfma_f32_16x16x32_bf16 v[64:67], a[68:71], a[20:23], v[64:67]\n" \
"ds_read_b64_tr_b16 a[96:97], v232 offset:50176\n" \
"ds_read_b64_tr_b16 a[98:99], v232 offset:52224\n" \
"v_mfma_f32_16x16x32_bf16 v[68:71], a[72:75], a[16:19], v[68:71]\n" \
"s_waitcnt lgkmcnt(5)\n" \
"ds_read_b64_tr_b16 a[100:101], v232 offset:58368\n" \
"ds_read_b64_tr_b16 a[102:103], v232 offset:60416\n" \
"v_mfma_f32_16x16x32_bf16 v[68:71], a[76:79], a[20:23], v[68:71]\n" \
"s_getpc_b64 s[64:65]\n" \
"Lpd1_%=:\n" \
"s_lshl_b32 s66, s62, 2\n" \
"ds_read_b64_tr_b16 a[104:105], v233 offset:50176\n" \
"ds_read_b64_tr_b16 a[106:107], v233 offset:52224\n" \
"v_mfma_f32_16x16x32_bf16 v[72:75], a[80:83], a[16:19], v[72:75]\n" \
"s_add_u32 s66, s66, s61\n" \
"ds_read_b64_tr_b16 a[108:109], v233 offset:58368\n" \
"ds_read_b64_tr_b16 a[110:111], v233 offset:60416\n" \
"v_mfma_f32_16x16x32_bf16 v[72:75], a[84:87], a[20:23], v[72:75]\n" \
"s_and_b32 s66, s66, 15\n" \
"ds_read_b64_tr_b16 a[112:113], v232 offset:50688\n" \
"ds_read_b64_tr_b16 a[114:115], v232 offset:52736\n" \
"v_mfma_f32_16x16x32_bf16 v[76:79], a[88:91], a[16:19], v[76:79]\n" \
"s_lshl_b32 s66, s66, 12\n" \
"ds_read_b64_tr_b16 a[116:117], v232 offset:58880\n" \
"ds_read_b64_tr_b16 a[118:119], v232 offset:60928\n" \
"v_mfma_f32_16x16x32_bf16 v[76:79], a[92:95], a[20:23], v[76:79]\n" \
"ds_read_b64_tr_b16 a[120:121], v233 offset:50688\n" \
"ds_read_b64_tr_b16 a[122:123], v233 offset:52736\n" \
"v_mfma_f32_16x16x32_bf16 v[96:99], a[64:67], a[24:27], v[96:99]\n" \
"ds_read_b64_tr_b16 a[124:125], v233 offset:58880\n" \
"ds_read_b64_tr_b16 a[126:127], v233 offset:60928\n" \
"v_mfma_f32_16x16x32_bf16 v[96:99], a[68:71], a[28:31], v[96:99]\n" \
"s_add_u32 s93, s94, 17850368\n" \
"s_add_u32 s64, s64, Lloop2_%=-Lpd1_%=\n" \
"s_addc_u32 s65, s65, 0\n" \
"s_mov_b32 s67, Lcode_end_%=-Lloop2_%=-4\n" \
"v_mfma_f32_16x16x32_bf16 v[100:103], a[72:75], a[24:27], v[100:103]\n" \
"buffer_store_dwordx4 v[4:7], v234, s[56:59], s93 offen offset:0 sc1\n" \
"v_mfma_f32_16x16x32_bf16 v[100:103], a[76:79], a[28:31], v[100:103]\n" \
"ds_write_b128 v225, a[228:231] offset:9216\n" \
"v_mfma_f32_16x16x32_bf16 v[104:107], a[80:83], a[24:27], v[104:107]\n" \
"buffer_load_dword v227, v242, s[56:59], s90 offen sc1\n" \
"v_mfma_f32_16x16x32_bf16 v[104:107], a[84:87], a[28:31], v[104:107]\n" \
"s_waitcnt lgkmcnt(1)\n" \
"v_lshl_add_u32 v230, v234, 2, s66\n" \
"v_min_u32 v230, s67, v230\n" \
"v_mfma_f32_16x16x32_bf16 v[108:111], a[88:91], a[24:27], v[108:111]\n" \
"ds_write_b128 v225, a[232:235] offset:10240\n" \
"v_mfma_f32_16x16x32_bf16 v[108:111], a[92:95], a[28:31], v[108:111]\n" \
"global_load_dword v230, v230, s[64:65]\n" \
"v_mfma_f32_16x16x32_bf16 v[80:83], a[96:99], a[16:19], v[80:83]\n" \
"v_cvt_pk_bf16_f32 v64, v64, v65\n" \
"v_cvt_pk_bf16_f32 v65, v66, v67\n" \
"v_mfma_f32_16x16x32_bf16 v[80:83], a[100:103], a[20:23], v[80:83]\n" \
"v_cvt_pk_bf16_f32 v66, v68, v69\n" \
"v_cvt_pk_bf16_f32 v67, v70, v71\n" \
"v_accvgpr_write_b32 a128, v64\n" \
"v_mfma_f32_16x16x32_bf16 v[84:87], a[104:107], a[16:19], v[84:87]\n" \
"v_accvgpr_write_b32 a129, v65\n" \
"v_accvgpr_write_b32 a130, v66\n" \
"v_mfma_f32_16x16x32_bf16 v[84:87], a[108:111], a[20:23], v[84:87]\n" \
"ds_write_b128 v225, a[236:239] offset:11264\n" \
"v_mfma_f32_16x16x32_bf16 v[88:91], a[112:115], a[16:19], v[88:91]\n" \
"v_accvgpr_write_b32 a131, v67\n" \
"v_cvt_pk_bf16_f32 v72, v72, v73\n" \
"v_mfma_f32_16x16x32_bf16 v[88:91], a[116:119], a[20:23], v[88:91]\n" \
"v_cvt_pk_bf16_f32 v73, v74, v75\n" \
"v_cvt_pk_bf16_f32 v74, v76, v77\n" \
"v_cvt_pk_bf16_f32 v75, v78, v79\n" \
"v_mfma_f32_16x16x32_bf16 v[92:95], a[120:123], a[16:19], v[92:95]\n" \
"v_accvgpr_write_b32 a132, v72\n" \
"v_accvgpr_write_b32 a133, v73\n" \
"v_mfma_f32_16x16x32_bf16 v[92:95], a[124:127], a[20:23], v[92:95]\n" \
"v_accvgpr_write_b32 a134, v74\n" \
"v_accvgpr_write_b32 a135, v75\n" \
"v_mfma_f32_16x16x32_bf16 v[112:115], a[96:99], a[24:27], v[112:115]\n" \
"v_cvt_pk_bf16_f32 v96, v96, v97\n" \
"v_cvt_pk_bf16_f32 v97, v98, v99\n" \
"v_mfma_f32_16x16x32_bf16 v[112:115], a[100:103], a[28:31], v[112:115]\n" \
"buffer_store_dwordx4 v[20:23], v234, s[56:59], s93 offen offset:1024 sc1\n" \
"v_mfma_f32_16x16x32_bf16 v[116:119], a[104:107], a[24:27], v[116:119]\n" \
"v_cvt_pk_bf16_f32 v98, v100, v101\n" \
"v_cvt_pk_bf16_f32 v99, v102, v103\n" \
"v_mfma_f32_16x16x32_bf16 v[116:119], a[108:111], a[28:31], v[116:119]\n" \
"v_cvt_pk_bf16_f32 v104, v104, v105\n" \
"v_cvt_pk_bf16_f32 v105, v106, v107\n" \
"v_cvt_pk_bf16_f32 v106, v108, v109\n" \
"v_mfma_f32_16x16x32_bf16 v[120:123], a[112:115], a[24:27], v[120:123]\n" \
"v_cvt_pk_bf16_f32 v107, v110, v111\n" \
"v_mfma_f32_16x16x32_bf16 v[120:123], a[116:119], a[28:31], v[120:123]\n" \
"v_cvt_pk_bf16_f32 v80, v80, v81\n" \
"v_cvt_pk_bf16_f32 v81, v82, v83\n" \
"v_mfma_f32_16x16x32_bf16 v[124:127], a[120:123], a[24:27], v[124:127]\n" \
"v_cvt_pk_bf16_f32 v82, v84, v85\n" \
"v_cvt_pk_bf16_f32 v83, v86, v87\n" \
"v_mfma_f32_16x16x32_bf16 v[124:127], a[124:127], a[28:31], v[124:127]\n" \
"v_accvgpr_write_b32 a136, v80\n" \
"s_waitcnt lgkmcnt(0)\n" \
"s_barrier\n" \
"v_mfma_f32_16x16x32_bf16 v[128:131], a[64:67], a[0:3], v[128:131]\n" \
"v_accvgpr_write_b32 a137, v81\n" \
"v_accvgpr_write_b32 a138, v82\n" \
"v_mfma_f32_16x16x32_bf16 v[128:131], a[68:71], a[4:7], v[128:131]\n" \
"ds_read_b128 a[192:195], v237 offset:0\n" \
"v_accvgpr_write_b32 a139, v83\n" \
"v_mfma_f32_16x16x32_bf16 v[132:135], a[72:75], a[0:3], v[132:135]\n" \
"v_cvt_pk_bf16_f32 v88, v88, v89\n" \
"v_cvt_pk_bf16_f32 v89, v90, v91\n" \
"v_mfma_f32_16x16x32_bf16 v[132:135], a[76:79], a[4:7], v[132:135]\n" \
"ds_read_b128 a[196:199], v237 offset:2048\n" \
"v_cvt_pk_bf16_f32 v90, v92, v93\n" \
"v_mfma_f32_16x16x32_bf16 v[136:139], a[80:83], a[0:3], v[136:139]\n" \
"v_cvt_pk_bf16_f32 v91, v94, v95\n" \
"v_med3_f32 v208, v248, v252, s72\n" \
"v_mfma_f32_16x16x32_bf16 v[136:139], a[84:87], a[4:7], v[136:139]\n" \
"ds_read_b128 a[200:203], v237 offset:4096\n" \
"v_med3_f32 v209, v249, v252, s72\n" \
"v_med3_f32 v210, v250, v252, s72\n" \
"v_mfma_f32_16x16x32_bf16 v[140:143], a[88:91], a[0:3], v[140:143]\n" \
"v_med3_f32 v211, v251, v252, s72\n" \
"v_cvt_pk_bf16_f32 v112, v112, v113\n" \
"v_mfma_f32_16x16x32_bf16 v[140:143], a[92:95], a[4:7], v[140:143]\n" \
"ds_read_b128 a[204:207], v237 offset:6144\n" \
"v_cvt_pk_bf16_f32 v113, v114, v115\n" \
"v_mfma_f32_16x16x32_bf16 v[144:147], a[96:99], a[0:3], v[144:147]\n" \
"v_cvt_pk_bf16_f32 v114, v116, v117\n" \
"v_cvt_pk_bf16_f32 v115, v118, v119\n" \
"v_mfma_f32_16x16x32_bf16 v[144:147], a[100:103], a[4:7], v[144:147]\n" \
"ds_read_b128 a[208:211], v237 offset:256\n" \
"v_cvt_pk_bf16_f32 v120, v120, v121\n" \
"v_cvt_pk_bf16_f32 v121, v122, v123\n" \
"v_mfma_f32_16x16x32_bf16 v[148:151], a[104:107], a[0:3], v[148:151]\n" \
"buffer_store_dwordx4 v[12:15], v234, s[56:59], s93 offen offset:2048 sc1\n" \
"v_mfma_f32_16x16x32_bf16 v[148:151], a[108:111], a[4:7], v[148:151]\n" \
"ds_read_b128 a[212:215], v237 offset:2304\n" \
"v_cvt_pk_bf16_f32 v122, v124, v125\n" \
"v_mfma_f32_16x16x32_bf16 v[152:155], a[112:115], a[0:3], v[152:155]\n" \
"v_cvt_pk_bf16_f32 v123, v126, v127\n" \
"v_mfma_f32_16x16x32_bf16 v[152:155], a[116:119], a[4:7], v[152:155]\n" \
"ds_read_b128 a[216:219], v237 offset:4352\n" \
"v_cvt_pk_bf16_f32 v128, v128, v129\n" \
"v_mfma_f32_16x16x32_bf16 v[156:159], a[120:123], a[0:3], v[156:159]\n" \
"v_cvt_pk_bf16_f32 v129, v130, v131\n" \
"v_cvt_pk_bf16_f32 v130, v132, v133\n" \
"v_mfma_f32_16x16x32_bf16 v[156:159], a[124:127], a[4:7], v[156:159]\n" \
"ds_read_b128 a[220:223], v237 offset:6400\n" \
"v_cvt_pk_bf16_f32 v131, v134, v135\n" \
"v_cvt_pk_bf16_f32 v136, v136, v137\n" \
"v_mfma_f32_16x16x32_bf16 v[160:163], a[64:67], a[8:11], v[160:163]\n" \
"v_cvt_pk_bf16_f32 v137, v138, v139\n" \
"v_cvt_pk_bf16_f32 v138, v140, v141\n" \
"v_mfma_f32_16x16x32_bf16 v[160:163], a[68:71], a[12:15], v[160:163]\n" \
"ds_read_b128 a[224:227], v237 offset:8192\n" \
"v_cvt_pk_bf16_f32 v139, v142, v143\n" \
"v_mfma_f32_16x16x32_bf16 v[164:167], a[72:75], a[8:11], v[164:167]\n" \
"v_mfma_f32_16x16x32_bf16 v[164:167], a[76:79], a[12:15], v[164:167]\n" \
"ds_read_b128 a[228:231], v237 offset:10240\n" \
"v_mfma_f32_16x16x32_bf16 v[168:171], a[80:83], a[8:11], v[168:171]\n" \
"v_mfma_f32_16x16x32_bf16 v[168:171], a[84:87], a[12:15], v[168:171]\n" \
"ds_read_b128 a[232:235], v237 offset:12288\n" \
"v_cvt_pk_bf16_f32 v144, v144, v145\n" \
"v_cvt_pk_bf16_f32 v145, v146, v147\n" \
"v_mfma_f32_16x16x32_bf16 v[172:175], a[88:91], a[8:11], v[172:175]\n" \
"v_cvt_pk_bf16_f32 v146, v148, v149\n" \
"v_cvt_pk_bf16_f32 v147, v150, v151\n" \
"v_mfma_f32_16x16x32_bf16 v[172:175], a[92:95], a[12:15], v[172:175]\n" \
"ds_read_b128 a[236:239], v237 offset:14336\n" \
"v_cvt_pk_bf16_f32 v152, v152, v153\n" \
"v_mfma_f32_16x16x32_bf16 v[176:179], a[96:99], a[8:11], v[176:179]\n" \
"v_cvt_pk_bf16_f32 v153, v154, v155\n" \
"v_cvt_pk_bf16_f32 v154, v156, v157\n" \
"v_mfma_f32_16x16x32_bf16 v[176:179], a[100:103], a[12:15], v[176:179]\n" \
"ds_read_b128 a[240:243], v237 offset:8448\n" \
"v_cvt_pk_bf16_f32 v155, v158, v159\n" \
"v_mfma_f32_16x16x32_bf16 v[180:183], a[104:107], a[8:11], v[180:183]\n" \
"buffer_store_dwordx4 v[28:31], v234, s[56:59], s93 offen offset:3072 sc1\n" \
"v_mfma_f32_16x16x32_bf16 v[180:183], a[108:111], a[12:15], v[180:183]\n" \
"ds_read_b128 a[244:247], v237 offset:10496\n" \
"ds_write_b128 v239, v[128:131] offset:0\n" \
"v_mov_b32 v196, 0x42400000\n" \
"v_mfma_f32_16x16x32_bf16 v[184:187], a[112:115], a[8:11], v[184:187]\n" \
"v_mov_b32 v197, 0x42400000\n" \
"v_mov_b32 v198, 0x42400000\n" \
"v_mfma_f32_16x16x32_bf16 v[184:187], a[116:119], a[12:15], v[184:187]\n" \
"ds_read_b128 a[248:251], v237 offset:12544\n" \
"v_mov_b32 v199, 0x42400000\n" \
"v_cvt_pk_bf16_f32 v160, v160, v161\n" \
"v_mfma_f32_16x16x32_bf16 v[188:191], a[120:123], a[8:11], v[188:191]\n" \
"v_cvt_pk_bf16_f32 v161, v162, v163\n" \
"v_cvt_pk_bf16_f32 v162, v164, v165\n" \
"v_mfma_f32_16x16x32_bf16 v[188:191], a[124:127], a[12:15], v[188:191]\n" \
"s_waitcnt lgkmcnt(3)\n" \
"ds_read_b128 a[252:255], v237 offset:14592\n" \
"v_cvt_pk_bf16_f32 v163, v166, v167\n" \
"v_mfma_f32_16x16x32_bf16 v[16:19], a[192:195], v[96:99], v[208:211]\n" \
"v_cvt_pk_bf16_f32 v168, v168, v169\n" \
"v_cvt_pk_bf16_f32 v169, v170, v171\n" \
"v_mfma_f32_16x16x32_bf16 v[16:19], a[196:199], v[104:107], v[16:19]\n" \
"v_cvt_pk_bf16_f32 v170, v172, v173\n" \
"v_cvt_pk_bf16_f32 v171, v174, v175\n" \
"v_mfma_f32_16x16x32_bf16 v[16:19], a[200:203], v[112:115], v[16:19]\n" \
"v_cvt_pk_bf16_f32 v176, v176, v177\n" \
"v_cvt_pk_bf16_f32 v177, v178, v179\n" \
"v_mfma_f32_16x16x32_bf16 v[16:19], a[204:207], v[120:123], v[16:19]\n" \
"v_cvt_pk_bf16_f32 v178, v180, v181\n" \
"v_cvt_pk_bf16_f32 v179, v182, v183\n" \
"v_mfma_f32_16x16x32_bf16 v[32:35], a[204:207], v[152:155], v[196:199]\n" \
"v_cvt_pk_bf16_f32 v184, v184, v185\n" \
"v_cvt_pk_bf16_f32 v185, v186, v187\n" \
"Lpg5014_%=:\n" \
"s_cbranch_execz Lpg6200_%=\n" \
"v_mfma_f32_16x16x32_bf16 v[32:35], a[200:203], v[144:147], v[32:35]\n" \
"v_cvt_pk_bf16_f32 v186, v188, v189\n" \
"v_cvt_pk_bf16_f32 v187, v190, v191\n" \
"v_mfma_f32_16x16x32_bf16 v[32:35], a[196:199], v[136:139], v[32:35]\n" \
"v_med3_f32 v212, v248, v253, v252\n" \
"v_med3_f32 v213, v249, v253, v252\n" \
"v_mfma_f32_16x16x32_bf16 v[32:35], a[192:195], v[128:131], v[32:35]\n" \
"v_med3_f32 v214, v250, v253, v252\n" \
"v_med3_f32 v215, v251, v253, v252\n" \
"v_mfma_f32_16x16x32_bf16 v[48:51], a[192:195], v[160:163], v[196:199]\n" \
"v_med3_f32 v216, v248, v226, v253\n" \
"v_med3_f32 v217, v249, v226, v253\n" \
"v_mfma_f32_16x16x32_bf16 v[48:51], a[196:199], v[168:171], v[48:51]\n" \
"ds_read_b128 a[192:195], v237 offset:16384\n" \
"v_med3_f32 v218, v250, v226, v253\n" \
"v_mfma_f32_16x16x32_bf16 v[48:51], a[200:203], v[176:179], v[48:51]\n" \
"ds_read_b128 a[196:199], v237 offset:18432\n" \
"v_med3_f32 v219, v251, v226, v253\n" \
"v_mfma_f32_16x16x32_bf16 v[48:51], a[204:207], v[184:187], v[48:51]\n" \
"ds_read_b128 a[200:203], v237 offset:20480\n" \
"v_med3_f32 v220, v248, v240, v226\n" \
"v_mfma_f32_16x16x32_bf16 v[52:55], a[220:223], v[184:187], v[196:199]\n" \
"ds_read_b128 a[204:207], v237 offset:22528\n" \
"v_med3_f32 v221, v249, v240, v226\n" \
"v_mfma_f32_16x16x32_bf16 v[52:55], a[216:219], v[176:179], v[52:55]\n" \
"ds_write_b128 v239, v[136:139] offset:1024\n" \
"v_mfma_f32_16x16x32_bf16 v[52:55], a[212:215], v[168:171], v[52:55]\n" \
"v_med3_f32 v222, v250, v240, v226\n" \
"v_med3_f32 v223, v251, v240, v226\n" \
"v_mfma_f32_16x16x32_bf16 v[52:55], a[208:211], v[160:163], v[52:55]\n" \
"v_accvgpr_write_b32 a144, v96\n" \
"v_accvgpr_write_b32 a145, v97\n" \
"v_mfma_f32_16x16x32_bf16 v[36:39], a[208:211], v[128:131], v[196:199]\n" \
"ds_write_b128 v239, v[144:147] offset:2048\n" \
"v_mfma_f32_16x16x32_bf16 v[36:39], a[212:215], v[136:139], v[36:39]\n" \
"v_accvgpr_write_b32 a146, v98\n" \
"v_accvgpr_write_b32 a147, v99\n" \
"v_mfma_f32_16x16x32_bf16 v[36:39], a[216:219], v[144:147], v[36:39]\n" \
"v_accvgpr_write_b32 a148, v104\n" \
"v_accvgpr_write_b32 a149, v105\n" \
"v_mfma_f32_16x16x32_bf16 v[36:39], a[220:223], v[152:155], v[36:39]\n" \
"ds_write_b128 v239, v[152:155] offset:3072\n" \
"v_mfma_f32_16x16x32_bf16 v[20:23], a[220:223], v[120:123], v[212:215]\n" \
"v_accvgpr_write_b32 a150, v106\n" \
"v_accvgpr_write_b32 a151, v107\n" \
"v_mfma_f32_16x16x32_bf16 v[20:23], a[216:219], v[112:115], v[20:23]\n" \
"ds_read_b128 a[220:223], v237 offset:22784\n" \
"v_accvgpr_write_b32 a152, v112\n" \
"v_mfma_f32_16x16x32_bf16 v[20:23], a[212:215], v[104:107], v[20:23]\n" \
"ds_read_b128 a[216:219], v237 offset:20736\n" \
"v_accvgpr_write_b32 a153, v113\n" \
"v_mfma_f32_16x16x32_bf16 v[20:23], a[208:211], v[96:99], v[20:23]\n" \
"ds_read_b128 a[212:215], v237 offset:18688\n" \
"v_accvgpr_write_b32 a154, v114\n" \
"v_mfma_f32_16x16x32_bf16 v[24:27], a[224:227], v[96:99], v[216:219]\n" \
"ds_read_b128 a[208:211], v237 offset:16640\n" \
"v_accvgpr_write_b32 a155, v115\n" \
"v_mfma_f32_16x16x32_bf16 v[24:27], a[228:231], v[104:107], v[24:27]\n" \
"ds_write_b128 v239, v[160:163] offset:4096\n" \
"v_mfma_f32_16x16x32_bf16 v[24:27], a[232:235], v[112:115], v[24:27]\n" \
"v_accvgpr_write_b32 a156, v120\n" \
"v_accvgpr_write_b32 a157, v121\n" \
"v_mfma_f32_16x16x32_bf16 v[24:27], a[236:239], v[120:123], v[24:27]\n" \
"v_accvgpr_write_b32 a158, v122\n" \
"v_accvgpr_write_b32 a159, v123\n" \
"v_mfma_f32_16x16x32_bf16 v[40:43], a[236:239], v[152:155], v[196:199]\n" \
"ds_write_b128 v239, v[168:171] offset:5120\n" \
"v_mfma_f32_16x16x32_bf16 v[40:43], a[232:235], v[144:147], v[40:43]\n" \
"v_accvgpr_write_b32 a140, v88\n" \
"v_accvgpr_write_b32 a141, v89\n" \
"v_mfma_f32_16x16x32_bf16 v[40:43], a[228:231], v[136:139], v[40:43]\n" \
"v_accvgpr_write_b32 a142, v90\n" \
"v_accvgpr_write_b32 a143, v91\n" \
"v_mfma_f32_16x16x32_bf16 v[40:43], a[224:227], v[128:131], v[40:43]\n" \
"ds_write_b128 v239, v[176:179] offset:6144\n" \
"v_mfma_f32_16x16x32_bf16 v[56:59], a[224:227], v[160:163], v[196:199]\n" \
"v_add_u32 v233, s82, v235\n" \
"v_mfma_f32_16x16x32_bf16 v[56:59], a[228:231], v[168:171], v[56:59]\n" \
"ds_read_b128 a[224:227], v237 offset:24576\n" \
"v_mfma_f32_16x16x32_bf16 v[56:59], a[232:235], v[176:179], v[56:59]\n" \
"ds_read_b128 a[228:231], v237 offset:26624\n" \
"ds_write_b128 v239, v[184:187] offset:7168\n" \
"v_exp_f32 v16, v16\n" \
"v_mfma_f32_16x16x32_bf16 v[56:59], a[236:239], v[184:187], v[56:59]\n" \
"s_waitcnt lgkmcnt(4)\n" \
"ds_read_b128 a[232:235], v237 offset:28672\n" \
"v_exp_f32 v17, v17\n" \
"v_mfma_f32_16x16x32_bf16 v[60:63], a[252:255], v[184:187], v[196:199]\n" \
"ds_read_b128 a[236:239], v237 offset:30720\n" \
"v_mfma_f32_16x16x32_bf16 v[60:63], a[248:251], v[176:179], v[60:63]\n" \
"s_waitcnt vmcnt(4)\n" \
"v_cmp_eq_u32 vcc, s60, v227\n" \
"v_mfma_f32_16x16x32_bf16 v[60:63], a[244:247], v[168:171], v[60:63]\n" \
"v_exp_f32 v18, v18\n" \
"v_mfma_f32_16x16x32_bf16 v[60:63], a[240:243], v[160:163], v[60:63]\n" \
"s_andn2_b32 s70, 0xff, vcc_lo\n" \
"s_cbranch_scc1 Lpl1_%=\n" \
"Lpr1_%=:\n" \
"v_cvt_pk_bf16_f32 v16, v16, v17\n" \
"v_exp_f32 v19, v19\n" \
"v_exp_f32 v20, v20\n" \
"v_mfma_f32_16x16x32_bf16 v[44:47], a[240:243], v[128:131], v[196:199]\n" \
"v_exp_f32 v21, v21\n" \
"v_mfma_f32_16x16x32_bf16 v[44:47], a[244:247], v[136:139], v[44:47]\n" \
"s_add_u32 m0, s79, 32768\n" \
"v_exp_f32 v22, v22\n" \
"v_mfma_f32_16x16x32_bf16 v[44:47], a[248:251], v[144:147], v[44:47]\n" \
"s_add_u32 s71, s76, 0\n" \
"v_cvt_pk_bf16_f32 v17, v18, v19\n" \
"v_cvt_pk_bf16_f32 v18, v20, v21\n" \
"v_mfma_f32_16x16x32_bf16 v[44:47], a[252:255], v[152:155], v[44:47]\n" \
"buffer_load_dwordx4 v234, s[36:39], s71 offen offset:0 sc1 lds\n" \
"v_exp_f32 v23, v23\n" \
"v_exp_f32 v32, v32\n" \
"v_mfma_f32_16x16x32_bf16 v[28:31], a[252:255], v[120:123], v[220:223]\n" \
"v_exp_f32 v33, v33\n" \
"v_mfma_f32_16x16x32_bf16 v[28:31], a[248:251], v[112:115], v[28:31]\n" \
"ds_read_b128 a[252:255], v237 offset:30976\n" \
"v_cvt_pk_bf16_f32 v19, v22, v23\n" \
"v_mfma_f32_16x16x32_bf16 v[28:31], a[244:247], v[104:107], v[28:31]\n" \
"buffer_load_dwordx4 v234, s[36:39], s71 offen offset:1024 sc1 lds\n" \
"v_mfma_f32_16x16x32_bf16 v[28:31], a[240:243], v[96:99], v[28:31]\n" \
"ds_read_b128 a[244:247], v237 offset:26880\n" \
"ds_read_b128 a[248:251], v237 offset:28928\n" \
"v_exp_f32 v34, v34\n" \
"v_exp_f32 v35, v35\n" \
"v_mfma_f32_16x16x32_bf16 v[80:83], a[192:195], a[144:147], v[196:199]\n" \
"ds_read_b128 a[240:243], v237 offset:24832\n" \
"v_cvt_pk_bf16_f32 v32, v32, v33\n" \
"v_mfma_f32_16x16x32_bf16 v[80:83], a[196:199], a[148:151], v[80:83]\n" \
"buffer_load_dwordx4 v234, s[36:39], s71 offen offset:2048 sc1 lds\n" \
"v_mfma_f32_16x16x32_bf16 v[80:83], a[200:203], a[152:155], v[80:83]\n" \
"s_lshl_b32 s68, s63, 2\n" \
"s_add_u32 s68, s68, s95\n" \
"s_mov_b32 s69, 0\n" \
"v_mfma_f32_16x16x32_bf16 v[80:83], a[204:207], a[156:159], v[80:83]\n" \
"s_mov_b32 s89, s63\n" \
"ds_read_b128 a[44:47], v233 offset:15360\n" \
"v_mov_b32 v192, s60\n" \
"v_lshlrev_b32 v193, 28, s61\n" \
"v_exp_f32 v36, v36\n" \
"v_exp_f32 v37, v37\n" \
"v_mfma_f32_16x16x32_bf16 v[96:99], a[204:207], v[152:155], v[196:199]\n" \
"buffer_load_dwordx4 v234, s[36:39], s71 offen offset:3072 sc1 lds\n" \
"v_mfma_f32_16x16x32_bf16 v[96:99], a[200:203], v[144:147], v[96:99]\n" \
"s_add_u32 m0, s79, 49152\n" \
"s_mov_b32 s90, 0\n" \
"ds_read_b128 a[40:43], v233 offset:7168\n" \
"v_mfma_f32_16x16x32_bf16 v[96:99], a[196:199], v[136:139], v[96:99]\n" \
"s_add_u32 s71, s76, 17408\n" \
"v_exp_f32 v38, v38\n" \
"v_mfma_f32_16x16x32_bf16 v[96:99], a[192:195], v[128:131], v[96:99]\n" \
"buffer_load_dwordx4 v234, s[36:39], s71 offen offset:0 sc1 lds\n" \
"v_cvt_pk_bf16_f32 v33, v34, v35\n" \
"v_exp_f32 v39, v39\n" \
"v_mfma_f32_16x16x32_bf16 v[112:115], a[192:195], v[160:163], v[196:199]\n" \
"ds_read_b128 a[36:39], v233 offset:14336\n" \
"v_cvt_pk_bf16_f32 v34, v36, v37\n" \
"v_mfma_f32_16x16x32_bf16 v[112:115], a[196:199], v[168:171], v[112:115]\n" \
"v_exp_f32 v48, v48\n" \
"v_mfma_f32_16x16x32_bf16 v[112:115], a[200:203], v[176:179], v[112:115]\n" \
"buffer_load_dwordx4 v234, s[36:39], s71 offen offset:1024 sc1 lds\n" \
"v_mfma_f32_16x16x32_bf16 v[112:115], a[204:207], v[184:187], v[112:115]\n" \
"ds_read_b128 a[32:35], v233 offset:6144\n" \
"v_cvt_pk_bf16_f32 v35, v38, v39\n" \
"v_exp_f32 v49, v49\n" \
"v_exp_f32 v50, v50\n" \
"v_mfma_f32_16x16x32_bf16 v[64:67], a[204:207], a[140:143], v[208:211]\n" \
"v_exp_f32 v51, v51\n" \
"v_mfma_f32_16x16x32_bf16 v[64:67], a[200:203], a[136:139], v[64:67]\n" \
"buffer_load_dwordx4 v234, s[36:39], s71 offen offset:2048 sc1 lds\n" \
"v_mfma_f32_16x16x32_bf16 v[64:67], a[196:199], a[132:135], v[64:67]\n" \
"ds_read_b128 a[204:207], v233 offset:9216\n" \
"v_cvt_pk_bf16_f32 v48, v48, v49\n" \
"v_mfma_f32_16x16x32_bf16 v[64:67], a[192:195], a[128:131], v[64:67]\n" \
"v_exp_f32 v52, v52\n" \
"v_exp_f32 v53, v53\n" \
"v_exp_f32 v54, v54\n" \
"v_mfma_f32_16x16x32_bf16 v[68:71], a[208:211], a[128:131], v[212:215]\n" \
"buffer_load_dwordx4 v234, s[36:39], s71 offen offset:3072 sc1 lds\n" \
"v_mfma_f32_16x16x32_bf16 v[68:71], a[212:215], a[132:135], v[68:71]\n" \
"ds_read_b128 a[200:203], v233 offset:1024\n" \
"v_cvt_pk_bf16_f32 v49, v50, v51\n" \
"v_mfma_f32_16x16x32_bf16 v[68:71], a[216:219], a[136:139], v[68:71]\n" \
"s_add_u32 m0, s77, s80\n" \
"v_exp_f32 v55, v55\n" \
"v_cvt_pk_bf16_f32 v50, v52, v53\n" \
"v_mfma_f32_16x16x32_bf16 v[68:71], a[220:223], a[140:143], v[68:71]\n" \
"buffer_load_dwordx4 v234, s[40:43], s76 offen offset:0 sc1 lds\n" \
"v_cvt_pk_bf16_f32 v51, v54, v55\n" \
"v_mfma_f32_16x16x32_bf16 v[116:119], a[220:223], v[184:187], v[196:199]\n" \
"ds_read_b128 a[196:199], v233 offset:8192\n" \
"v_mfma_f32_16x16x32_bf16 v[116:119], a[216:219], v[176:179], v[116:119]\n" \
"ds_read_b128 a[160:163], v239 offset:0\n" \
"v_mfma_f32_16x16x32_bf16 v[116:119], a[212:215], v[168:171], v[116:119]\n" \
"s_add_u32 s71, s76, 17408\n" \
"ds_read_b128 a[192:195], v233 offset:0\n" \
"v_exp_f32 v24, v24\n" \
"v_mfma_f32_16x16x32_bf16 v[116:119], a[208:211], v[160:163], v[116:119]\n" \
"buffer_load_dwordx4 v234, s[40:43], s76 offen offset:1024 sc1 lds\n" \
"v_mfma_f32_16x16x32_bf16 v[100:103], a[208:211], v[128:131], v[196:199]\n" \
"ds_read_b128 a[164:167], v239 offset:1024\n" \
"v_mfma_f32_16x16x32_bf16 v[100:103], a[212:215], v[136:139], v[100:103]\n" \
"v_exp_f32 v25, v25\n" \
"v_mfma_f32_16x16x32_bf16 v[100:103], a[216:219], v[144:147], v[100:103]\n" \
"v_exp_f32 v26, v26\n" \
"v_mfma_f32_16x16x32_bf16 v[100:103], a[220:223], v[152:155], v[100:103]\n" \
"buffer_load_dwordx4 v234, s[40:43], s76 offen offset:2048 sc1 lds\n" \
"v_exp_f32 v27, v27\n" \
"v_exp_f32 v28, v28\n" \
"v_mfma_f32_16x16x32_bf16 v[84:87], a[220:223], a[156:159], v[196:199]\n" \
"ds_read_b128 a[168:171], v239 offset:2048\n" \
"v_cvt_pk_bf16_f32 v20, v24, v25\n" \
"v_mfma_f32_16x16x32_bf16 v[84:87], a[216:219], a[152:155], v[84:87]\n" \
"ds_read_b128 a[220:223], v233 offset:11264\n" \
"v_cvt_pk_bf16_f32 v21, v26, v27\n" \
"v_mfma_f32_16x16x32_bf16 v[84:87], a[212:215], a[148:151], v[84:87]\n" \
"s_waitcnt lgkmcnt(2)\n" \
"v_exp_f32 v29, v29\n" \
"v_mfma_f32_16x16x32_bf16 v[84:87], a[208:211], a[144:147], v[84:87]\n" \
"buffer_load_dwordx4 v234, s[40:43], s76 offen offset:3072 sc1 lds\n" \
"v_exp_f32 v30, v30\n" \
"v_exp_f32 v31, v31\n" \
"v_mfma_f32_16x16x32_bf16 v[88:91], a[224:227], a[144:147], v[196:199]\n" \
"ds_read_b128 a[216:219], v233 offset:3072\n" \
"v_cvt_pk_bf16_f32 v22, v28, v29\n" \
"v_mfma_f32_16x16x32_bf16 v[88:91], a[228:231], a[148:151], v[88:91]\n" \
"ds_read_b128 a[172:175], v239 offset:3072\n" \
"v_mfma_f32_16x16x32_bf16 v[88:91], a[232:235], a[152:155], v[88:91]\n" \
"s_add_u32 m0, m0, 0x4000\n" \
"ds_read_b128 a[212:215], v233 offset:10240\n" \
"v_xor_b32 v248, 0x2000, v239\n" \
"v_mfma_f32_16x16x32_bf16 v[88:91], a[236:239], a[156:159], v[88:91]\n" \
"buffer_load_dwordx4 v234, s[40:43], s71 offen offset:0 sc1 lds\n" \
"v_exp_f32 v40, v40\n" \
"v_exp_f32 v41, v41\n" \
"v_mfma_f32_16x16x32_bf16 v[104:107], a[236:239], v[152:155], v[196:199]\n" \
"ds_read_b128 a[208:211], v233 offset:2048\n" \
"v_lshlrev_b32 v252, s48, v234\n" \
"v_mfma_f32_16x16x32_bf16 v[104:107], a[232:235], v[144:147], v[104:107]\n" \
"ds_read_b128 a[176:179], v239 offset:4096\n" \
"v_mov_b32 v224, 0\n" \
"v_mfma_f32_16x16x32_bf16 v[104:107], a[228:231], v[136:139], v[104:107]\n" \
"v_mov_b32 v225, 0\n" \
"v_mov_b32 v228, 0\n" \
"v_mfma_f32_16x16x32_bf16 v[104:107], a[224:227], v[128:131], v[104:107]\n" \
"buffer_load_dwordx4 v234, s[40:43], s71 offen offset:1024 sc1 lds\n" \
"v_exp_f32 v42, v42\n" \
"v_cvt_pk_bf16_f32 v23, v30, v31\n" \
"v_mfma_f32_16x16x32_bf16 v[120:123], a[224:227], v[160:163], v[196:199]\n" \
"ds_read_b128 a[180:183], v239 offset:5120\n" \
"v_mov_b32 v229, 0\n" \
"v_mfma_f32_16x16x32_bf16 v[120:123], a[228:231], v[168:171], v[120:123]\n" \
"v_exp_f32 v43, v43\n" \
"v_mfma_f32_16x16x32_bf16 v[120:123], a[232:235], v[176:179], v[120:123]\n" \
"v_exp_f32 v44, v44\n" \
"v_cvt_pk_bf16_f32 v36, v40, v41\n" \
"v_mfma_f32_16x16x32_bf16 v[120:123], a[236:239], v[184:187], v[120:123]\n" \
"buffer_load_dwordx4 v234, s[40:43], s71 offen offset:2048 sc1 lds\n" \
"v_exp_f32 v45, v45\n" \
"v_mfma_f32_16x16x32_bf16 v[72:75], a[236:239], a[140:143], v[216:219]\n" \
"ds_read_b128 a[184:187], v239 offset:6144\n" \
"v_cvt_pk_bf16_f32 v37, v42, v43\n" \
"v_mfma_f32_16x16x32_bf16 v[72:75], a[232:235], a[136:139], v[72:75]\n" \
"ds_read_b128 a[236:239], v233 offset:13312\n" \
"v_mfma_f32_16x16x32_bf16 v[72:75], a[228:231], a[132:135], v[72:75]\n" \
"v_exp_f32 v46, v46\n" \
"v_mfma_f32_16x16x32_bf16 v[72:75], a[224:227], a[128:131], v[72:75]\n" \
"buffer_load_dwordx4 v234, s[40:43], s71 offen offset:3072 sc1 lds\n" \
"v_exp_f32 v47, v47\n" \
"v_mfma_f32_16x16x32_bf16 v[76:79], a[240:243], a[128:131], v[220:223]\n" \
"ds_read_b128 a[232:235], v233 offset:5120\n" \
"v_cvt_pk_bf16_f32 v38, v44, v45\n" \
"v_mfma_f32_16x16x32_bf16 v[76:79], a[244:247], a[132:135], v[76:79]\n" \
"ds_read_b128 a[188:191], v239 offset:7168\n" \
"v_exp_f32 v56, v56\n" \
"v_mfma_f32_16x16x32_bf16 v[76:79], a[248:251], a[136:139], v[76:79]\n" \
"ds_read_b128 a[228:231], v233 offset:12288\n" \
"v_cvt_pk_bf16_f32 v39, v46, v47\n" \
"v_mfma_f32_16x16x32_bf16 v[76:79], a[252:255], a[140:143], v[76:79]\n" \
"v_exp_f32 v57, v57\n" \
"v_exp_f32 v58, v58\n" \
"v_mfma_f32_16x16x32_bf16 v[124:127], a[252:255], v[184:187], v[196:199]\n" \
"ds_read_b128 a[224:227], v233 offset:4096\n" \
"v_cvt_pk_bf16_f32 v52, v56, v57\n" \
"v_mfma_f32_16x16x32_bf16 v[124:127], a[248:251], v[176:179], v[124:127]\n" \
"v_exp_f32 v59, v59\n" \
"v_mfma_f32_16x16x32_bf16 v[124:127], a[244:247], v[168:171], v[124:127]\n" \
"v_exp_f32 v60, v60\n" \
"v_mfma_f32_16x16x32_bf16 v[124:127], a[240:243], v[160:163], v[124:127]\n" \
"v_exp_f32 v61, v61\n" \
"v_exp_f32 v62, v62\n" \
"v_cvt_pk_bf16_f32 v53, v58, v59\n" \
"v_mfma_f32_16x16x32_bf16 v[108:111], a[240:243], v[128:131], v[196:199]\n" \
"v_exp_f32 v63, v63\n" \
"v_mfma_f32_16x16x32_bf16 v[108:111], a[244:247], v[136:139], v[108:111]\n" \
"v_cvt_pk_bf16_f32 v54, v60, v61\n" \
"v_mfma_f32_16x16x32_bf16 v[108:111], a[248:251], v[144:147], v[108:111]\n" \
"v_cvt_pk_bf16_f32 v55, v62, v63\n" \
"v_mfma_f32_16x16x32_bf16 v[108:111], a[252:255], v[152:155], v[108:111]\n" \
"v_exp_f32 v80, v80\n" \
"v_mfma_f32_16x16x32_bf16 v[92:95], a[252:255], a[156:159], v[196:199]\n" \
"v_add_u32 v232, s91, v237\n" \
"v_mfma_f32_16x16x32_bf16 v[92:95], a[248:251], a[152:155], v[92:95]\n" \
"v_exp_f32 v81, v81\n" \
"v_mfma_f32_16x16x32_bf16 v[92:95], a[244:247], a[148:151], v[92:95]\n" \
"s_waitcnt vmcnt(8) lgkmcnt(0)\n" \
"s_barrier\n" \
"v_mfma_f32_16x16x32_bf16 v[92:95], a[240:243], a[144:147], v[92:95]\n" \
"buffer_store_dword v192, v193, s[56:59], s68 offen sc1\n" \
"v_exp_f32 v82, v82\n" \
"v_mfma_f32_16x16x32_bf16 v[160:163], a[192:195], v[16:19], 0\n" \
"s_add_u32 m0, s74, s79\n" \
"ds_read_b128 a[240:243], v232 offset:8448\n" \
"v_xor_b32 v233, 0x4000, v233\n" \
"v_mfma_f32_16x16x32_bf16 v[160:163], a[196:199], v[20:23], v[160:163]\n" \
"buffer_load_dwordx4 v234, s[36:39], s73 offen offset:0 sc1 lds\n" \
"v_mfma_f32_16x16x32_bf16 a[64:67], a[192:195], v[32:35], 0\n" \
"ds_read_b128 a[244:247], v232 offset:10496\n" \
"v_cvt_pk_bf16_f32 v80, v80, v81\n" \
"v_mfma_f32_16x16x32_bf16 a[64:67], a[196:199], v[36:39], a[64:67]\n" \
"ds_read_b128 a[0:3], v248 offset:0\n" \
"v_exp_f32 v83, v83\n" \
"v_mfma_f32_16x16x32_bf16 a[96:99], a[192:195], v[48:51], 0\n" \
"v_exp_f32 v84, v84\n" \
"v_mfma_f32_16x16x32_bf16 a[96:99], a[196:199], v[52:55], a[96:99]\n" \
"s_add_u32 s71, s73, 17408\n" \
"ds_read_b128 a[192:195], v232 offset:0\n" \
"v_cvt_pk_bf16_f32 v81, v82, v83\n" \
"v_mfma_f32_16x16x32_bf16 a[100:103], a[200:203], v[48:51], 0\n" \
"buffer_load_dwordx4 v234, s[36:39], s73 offen offset:1024 sc1 lds\n" \
"v_mfma_f32_16x16x32_bf16 a[100:103], a[204:207], v[52:55], a[100:103]\n" \
"ds_read_b128 a[196:199], v232 offset:2048\n" \
"v_exp_f32 v85, v85\n" \
"v_mfma_f32_16x16x32_bf16 a[68:71], a[200:203], v[32:35], 0\n" \
"ds_read_b128 a[4:7], v248 offset:1024\n" \
"v_mfma_f32_16x16x32_bf16 a[68:71], a[204:207], v[36:39], a[68:71]\n" \
"v_exp_f32 v86, v86\n" \
"v_mfma_f32_16x16x32_bf16 v[164:167], a[200:203], v[16:19], 0\n" \
"ds_read_b128 a[8:11], v248 offset:2048\n" \
"v_cvt_pk_bf16_f32 v82, v84, v85\n" \
"v_mfma_f32_16x16x32_bf16 v[164:167], a[204:207], v[20:23], v[164:167]\n" \
"buffer_load_dwordx4 v234, s[36:39], s73 offen offset:2048 sc1 lds\n" \
"v_exp_f32 v87, v87\n" \
"v_mfma_f32_16x16x32_bf16 v[168:171], a[208:211], v[16:19], 0\n" \
"ds_read_b128 a[200:203], v232 offset:4096\n" \
"v_mfma_f32_16x16x32_bf16 v[168:171], a[212:215], v[20:23], v[168:171]\n" \
"ds_read_b128 a[204:207], v232 offset:6144\n" \
"v_exp_f32 v96, v96\n" \
"v_mfma_f32_16x16x32_bf16 a[72:75], a[208:211], v[32:35], 0\n" \
"ds_read_b128 a[12:15], v248 offset:3072\n" \
"v_cvt_pk_bf16_f32 v83, v86, v87\n" \
"v_mfma_f32_16x16x32_bf16 a[72:75], a[212:215], v[36:39], a[72:75]\n" \
"v_exp_f32 v97, v97\n" \
"v_mfma_f32_16x16x32_bf16 a[104:107], a[208:211], v[48:51], 0\n" \
"buffer_load_dwordx4 v234, s[36:39], s73 offen offset:3072 sc1 lds\n" \
"v_mfma_f32_16x16x32_bf16 a[104:107], a[212:215], v[52:55], a[104:107]\n" \
"ds_read_b128 a[208:211], v232 offset:256\n" \
"v_exp_f32 v98, v98\n" \
"v_smfmac_f32_16x16x64_bf16 a[52:55], v[244:247], v[16:23], v243\n" \
"ds_read_b128 a[212:215], v232 offset:2304\n" \
"v_cvt_pk_bf16_f32 v96, v96, v97\n" \
"v_mfma_f32_16x16x32_bf16 a[108:111], a[216:219], v[48:51], 0\n" \
"ds_read_b128 a[16:19], v248 offset:4096\n" \
"v_mfma_f32_16x16x32_bf16 a[108:111], a[220:223], v[52:55], a[108:111]\n" \
"s_add_u32 m0, m0, 0x4000\n" \
"v_exp_f32 v99, v99\n" \
"v_mfma_f32_16x16x32_bf16 a[76:79], a[216:219], v[32:35], 0\n" \
"buffer_load_dwordx4 v234, s[36:39], s71 offen offset:0 sc1 lds\n" \
"v_mfma_f32_16x16x32_bf16 a[76:79], a[220:223], v[36:39], a[76:79]\n" \
"ds_read_b128 a[20:23], v248 offset:5120\n" \
"v_exp_f32 v100, v100\n" \
"v_mfma_f32_16x16x32_bf16 v[172:175], a[216:219], v[16:19], 0\n" \
"v_exp_f32 v101, v101\n" \
"v_mfma_f32_16x16x32_bf16 v[172:175], a[220:223], v[20:23], v[172:175]\n" \
"ds_read_b128 a[216:219], v232 offset:4352\n" \
"v_cvt_pk_bf16_f32 v97, v98, v99\n" \
"v_mfma_f32_32x32x16_bf16 v[128:143], a[48:51], a[48:51], 0\n" \
"ds_read_b128 a[220:223], v232 offset:6400\n" \
"v_exp_f32 v102, v102\n" \
"v_mfma_f32_16x16x32_bf16 v[176:179], a[224:227], v[16:19], 0\n" \
"buffer_load_dwordx4 v234, s[36:39], s71 offen offset:1024 sc1 lds\n" \
"v_mfma_f32_16x16x32_bf16 v[176:179], a[228:231], v[20:23], v[176:179]\n" \
"ds_read_b128 a[24:27], v248 offset:6144\n" \
"v_cvt_pk_bf16_f32 v98, v100, v101\n" \
"v_mfma_f32_16x16x32_bf16 a[80:83], a[224:227], v[32:35], 0\n" \
"v_exp_f32 v103, v103\n" \
"v_mfma_f32_16x16x32_bf16 a[80:83], a[228:231], v[36:39], a[80:83]\n" \
"ds_read_b128 a[28:31], v248 offset:7168\n" \
"v_exp_f32 v112, v112\n" \
"v_mfma_f32_16x16x32_bf16 a[112:115], a[224:227], v[48:51], 0\n" \
"v_exp_f32 v113, v113\n" \
"v_mfma_f32_16x16x32_bf16 a[112:115], a[228:231], v[52:55], a[112:115]\n" \
"buffer_load_dwordx4 v234, s[36:39], s71 offen offset:2048 sc1 lds\n" \
"v_exp_f32 v114, v114\n" \
"v_smfmac_f32_16x16x64_bf16 v[224:227], v[244:247], v[32:39], v243\n" \
"ds_read_b128 a[224:227], v232 offset:8192\n" \
"v_cvt_pk_bf16_f32 v99, v102, v103\n" \
"v_mfma_f32_16x16x32_bf16 a[116:119], a[232:235], v[48:51], 0\n" \
"ds_read_b128 a[228:231], v232 offset:10240\n" \
"v_cvt_pk_bf16_f32 v112, v112, v113\n" \
"v_mfma_f32_16x16x32_bf16 a[116:119], a[236:239], v[52:55], a[116:119]\n" \
"v_exp_f32 v115, v115\n" \
"v_mfma_f32_16x16x32_bf16 a[84:87], a[232:235], v[32:35], 0\n" \
"v_exp_f32 v116, v116\n" \
"v_mfma_f32_16x16x32_bf16 a[84:87], a[236:239], v[36:39], a[84:87]\n" \
"buffer_load_dwordx4 v234, s[36:39], s71 offen offset:3072 sc1 lds\n" \
"v_exp_f32 v117, v117\n" \
"v_mfma_f32_16x16x32_bf16 v[180:183], a[232:235], v[16:19], 0\n" \
"v_exp_f32 v118, v118\n" \
"v_mfma_f32_16x16x32_bf16 v[180:183], a[236:239], v[20:23], v[180:183]\n" \
"s_mov_b32 s76, s73\n" \
"s_add_u32 s73, s73, 34816\n" \
"ds_read_b128 a[232:235], v232 offset:12288\n" \
"v_mfma_f32_32x32x16_bf16 v[144:159], a[48:51], a[48:51], 0\n" \
"s_cmp_eq_u32 s73, s83\n" \
"s_cselect_b32 s70, 34816, 0\n" \
"ds_read_b128 a[236:239], v232 offset:14336\n" \
"v_cvt_pk_bf16_f32 v113, v114, v115\n" \
"v_mfma_f32_16x16x32_bf16 v[184:187], a[32:35], v[16:19], 0\n" \
"s_add_u32 s73, s73, s70\n" \
"s_xor_b32 s74, s74, 0x8000\n" \
"ds_read_b128 a[248:251], v232 offset:12544\n" \
"v_mfma_f32_16x16x32_bf16 v[184:187], a[36:39], v[20:23], v[184:187]\n" \
"s_xor_b32 s77, s77, 0x8000\n" \
"ds_read_b128 a[252:255], v232 offset:14592\n" \
"v_cvt_pk_bf16_f32 v114, v116, v117\n" \
"v_mfma_f32_16x16x32_bf16 a[88:91], a[32:35], v[32:35], 0\n" \
"v_exp_f32 v119, v119\n" \
"v_mfma_f32_16x16x32_bf16 a[88:91], a[36:39], v[36:39], a[88:91]\n" \
"v_exp_f32 v64, v64\n" \
"v_mfma_f32_16x16x32_bf16 a[120:123], a[32:35], v[48:51], 0\n" \
"v_exp_f32 v65, v65\n" \
"v_mfma_f32_16x16x32_bf16 a[120:123], a[36:39], v[52:55], a[120:123]\n" \
"v_exp_f32 v66, v66\n" \
"v_cvt_pk_bf16_f32 v115, v118, v119\n" \
"v_smfmac_f32_16x16x64_bf16 v[228:231], v[244:247], v[48:55], v243\n" \
"v_cvt_pk_bf16_f32 v64, v64, v65\n" \
"v_mfma_f32_16x16x32_bf16 a[124:127], a[40:43], v[48:51], 0\n" \
"v_exp_f32 v67, v67\n" \
"v_mfma_f32_16x16x32_bf16 a[124:127], a[44:47], v[52:55], a[124:127]\n" \
"v_exp_f32 v68, v68\n" \
"v_exp_f32 v69, v69\n" \
"v_mfma_f32_16x16x32_bf16 a[92:95], a[40:43], v[32:35], 0\n" \
"v_exp_f32 v70, v70\n" \
"v_mfma_f32_16x16x32_bf16 a[92:95], a[44:47], v[36:39], a[92:95]\n" \
"v_cvt_pk_bf16_f32 v65, v66, v67\n" \
"v_cvt_pk_bf16_f32 v66, v68, v69\n" \
"v_mfma_f32_16x16x32_bf16 v[188:191], a[40:43], v[16:19], 0\n" \
"s_cmp_eq_u32 s63, 0\n" \
"v_exp_f32 v71, v71\n" \
"v_mfma_f32_16x16x32_bf16 v[188:191], a[44:47], v[20:23], v[188:191]\n" \
"s_waitcnt lgkmcnt(0)\n" \
"v_cvt_pk_bf16_f32 v67, v70, v71\n" \
"s_cbranch_scc1 Lloop2_%=\n" \
"s_nop 0\n" \
"s_nop 0\n" \
"s_nop 0\n" \
"s_nop 0\n" \
"s_nop 0\n" \
"s_nop 0\n" \
"Lloop_%=:\n" \
"v_mfma_f32_16x16x32_bf16 v[0:3], a[192:195], a[128:131], v[196:199]\n" \
"v_exp_f32 v72, v72\n" \
"v_mfma_f32_16x16x32_bf16 v[0:3], a[196:199], a[132:135], v[0:3]\n" \
"v_mfma_f32_16x16x32_bf16 v[0:3], a[200:203], a[136:139], v[0:3]\n" \
"v_exp_f32 v73, v73\n" \
"v_mfma_f32_16x16x32_bf16 v[0:3], a[204:207], a[140:143], v[0:3]\n" \
"ds_read_b128 a[32:35], v233 offset:6144\n" \
"v_cvt_pk_bf16_f32 v68, v72, v73\n" \
"v_mfma_f32_16x16x32_bf16 v[16:19], a[204:207], a[156:159], v[196:199]\n" \
"v_exp_f32 v74, v74\n" \
"v_mfma_f32_16x16x32_bf16 v[16:19], a[200:203], a[152:155], v[16:19]\n" \
"global_load_dword v253, v252, s[44:45]\n" \
"v_mfma_f32_16x16x32_bf16 v[16:19], a[196:199], a[148:151], v[16:19]\n" \
"s_sub_u32 s46, s46, 1\n" \
"v_exp_f32 v75, v75\n" \
"v_mfma_f32_16x16x32_bf16 v[16:19], a[192:195], a[144:147], v[16:19]\n" \
"ds_read_b128 a[36:39], v233 offset:14336\n" \
"v_cvt_pk_bf16_f32 v69, v74, v75\n" \
"v_mfma_f32_16x16x32_bf16 v[32:35], a[192:195], a[160:163], v[196:199]\n" \
"v_exp_f32 v76, v76\n" \
"v_mfma_f32_16x16x32_bf16 v[32:35], a[196:199], a[164:167], v[32:35]\n" \
"s_cmp_gt_i32 s46, 0\n" \
"s_cselect_b32 s47, 0x100000, 0\n" \
"v_mfma_f32_16x16x32_bf16 v[32:35], a[200:203], a[168:171], v[32:35]\n" \
"v_exp_f32 v77, v77\n" \
"v_mfma_f32_16x16x32_bf16 v[32:35], a[204:207], a[172:175], v[32:35]\n" \
"s_add_u32 s44, s44, s47\n" \
"s_addc_u32 s45, s45, 0\n" \
"ds_read_b128 a[40:43], v233 offset:7168\n" \
"v_cvt_pk_bf16_f32 v70, v76, v77\n" \
"v_mfma_f32_16x16x32_bf16 v[48:51], a[204:207], a[188:191], v[196:199]\n" \
"v_exp_f32 v78, v78\n" \
"v_mfma_f32_16x16x32_bf16 v[48:51], a[200:203], a[184:187], v[48:51]\n" \
"v_xor_b32 v232, 0x4000, v232\n" \
"v_mfma_f32_16x16x32_bf16 v[48:51], a[196:199], a[180:183], v[48:51]\n" \
"v_exp_f32 v79, v79\n" \
"v_mfma_f32_16x16x32_bf16 v[48:51], a[192:195], a[176:179], v[48:51]\n" \
"ds_read_b128 a[204:207], v233 offset:9216\n" \
"v_cvt_pk_bf16_f32 v71, v78, v79\n" \
"v_mfma_f32_16x16x32_bf16 v[52:55], a[208:211], a[176:179], v[196:199]\n" \
"v_exp_f32 v88, v88\n" \
"v_mfma_f32_16x16x32_bf16 v[52:55], a[212:215], a[180:183], v[52:55]\n" \
"v_mfma_f32_16x16x32_bf16 v[52:55], a[216:219], a[184:187], v[52:55]\n" \
"v_exp_f32 v89, v89\n" \
"v_mfma_f32_16x16x32_bf16 v[52:55], a[220:223], a[188:191], v[52:55]\n" \
"ds_read_b128 a[200:203], v233 offset:1024\n" \
"v_cvt_pk_bf16_f32 v84, v88, v89\n" \
"v_mfma_f32_16x16x32_bf16 v[36:39], a[220:223], a[172:175], v[196:199]\n" \
"v_exp_f32 v90, v90\n" \
"v_mfma_f32_16x16x32_bf16 v[36:39], a[216:219], a[168:171], v[36:39]\n" \
"v_mfma_f32_16x16x32_bf16 v[36:39], a[212:215], a[164:167], v[36:39]\n" \
"v_exp_f32 v91, v91\n" \
"v_mfma_f32_16x16x32_bf16 v[36:39], a[208:211], a[160:163], v[36:39]\n" \
"ds_read_b128 a[196:199], v233 offset:8192\n" \
"v_cvt_pk_bf16_f32 v85, v90, v91\n" \
"v_mfma_f32_16x16x32_bf16 v[20:23], a[208:211], a[144:147], v[196:199]\n" \
"v_exp_f32 v92, v92\n" \
"v_mfma_f32_16x16x32_bf16 v[20:23], a[212:215], a[148:151], v[20:23]\n" \
"v_mfma_f32_16x16x32_bf16 v[20:23], a[216:219], a[152:155], v[20:23]\n" \
"v_exp_f32 v93, v93\n" \
"v_mfma_f32_16x16x32_bf16 v[20:23], a[220:223], a[156:159], v[20:23]\n" \
"ds_read_b128 a[192:195], v233 offset:0\n" \
"v_cvt_pk_bf16_f32 v86, v92, v93\n" \
"v_mfma_f32_16x16x32_bf16 v[4:7], a[220:223], a[140:143], v[196:199]\n" \
"v_exp_f32 v94, v94\n" \
"v_mfma_f32_16x16x32_bf16 v[4:7], a[216:219], a[136:139], v[4:7]\n" \
"v_mfma_f32_16x16x32_bf16 v[4:7], a[212:215], a[132:135], v[4:7]\n" \
"s_waitcnt lgkmcnt(7)\n" \
"v_exp_f32 v95, v95\n" \
"v_mfma_f32_16x16x32_bf16 v[4:7], a[208:211], a[128:131], v[4:7]\n" \
"ds_read_b128 a[220:223], v233 offset:11264\n" \
"v_cvt_pk_bf16_f32 v87, v94, v95\n" \
"v_mfma_f32_16x16x32_bf16 v[8:11], a[224:227], a[128:131], v[196:199]\n" \
"v_exp_f32 v104, v104\n" \
"v_mfma_f32_16x16x32_bf16 v[8:11], a[228:231], a[132:135], v[8:11]\n" \
"v_mfma_f32_16x16x32_bf16 v[8:11], a[232:235], a[136:139], v[8:11]\n" \
"v_exp_f32 v105, v105\n" \
"v_mfma_f32_16x16x32_bf16 v[8:11], a[236:239], a[140:143], v[8:11]\n" \
"ds_read_b128 a[216:219], v233 offset:3072\n" \
"v_cvt_pk_bf16_f32 v100, v104, v105\n" \
"v_mfma_f32_16x16x32_bf16 v[24:27], a[236:239], a[156:159], v[196:199]\n" \
"v_exp_f32 v106, v106\n" \
"v_mfma_f32_16x16x32_bf16 v[24:27], a[232:235], a[152:155], v[24:27]\n" \
"v_mfma_f32_16x16x32_bf16 v[24:27], a[228:231], a[148:151], v[24:27]\n" \
"v_exp_f32 v107, v107\n" \
"v_mfma_f32_16x16x32_bf16 v[24:27], a[224:227], a[144:147], v[24:27]\n" \
"ds_read_b128 a[212:215], v233 offset:10240\n" \
"v_cvt_pk_bf16_f32 v101, v106, v107\n" \
"v_mfma_f32_16x16x32_bf16 v[40:43], a[224:227], a[160:163], v[196:199]\n" \
"v_exp_f32 v108, v108\n" \
"v_mfma_f32_16x16x32_bf16 v[40:43], a[228:231], a[164:167], v[40:43]\n" \
"v_mfma_f32_16x16x32_bf16 v[40:43], a[232:235], a[168:171], v[40:43]\n" \
"v_exp_f32 v109, v109\n" \
"v_mfma_f32_16x16x32_bf16 v[40:43], a[236:239], a[172:175], v[40:43]\n" \
"ds_read_b128 a[208:211], v233 offset:2048\n" \
"v_cvt_pk_bf16_f32 v102, v108, v109\n" \
"v_mfma_f32_16x16x32_bf16 v[56:59], a[236:239], a[188:191], v[196:199]\n" \
"v_exp_f32 v110, v110\n" \
"v_mfma_f32_16x16x32_bf16 v[56:59], a[232:235], a[184:187], v[56:59]\n" \
"v_mfma_f32_16x16x32_bf16 v[56:59], a[228:231], a[180:183], v[56:59]\n" \
"v_exp_f32 v111, v111\n" \
"v_mfma_f32_16x16x32_bf16 v[56:59], a[224:227], a[176:179], v[56:59]\n" \
"ds_read_b128 a[236:239], v233 offset:13312\n" \
"v_cvt_pk_bf16_f32 v103, v110, v111\n" \
"v_mfma_f32_16x16x32_bf16 v[60:63], a[240:243], a[176:179], v[196:199]\n" \
"v_exp_f32 v120, v120\n" \
"v_mfma_f32_16x16x32_bf16 v[60:63], a[244:247], a[180:183], v[60:63]\n" \
"v_mfma_f32_16x16x32_bf16 v[60:63], a[248:251], a[184:187], v[60:63]\n" \
"v_exp_f32 v121, v121\n" \
"v_mfma_f32_16x16x32_bf16 v[60:63], a[252:255], a[188:191], v[60:63]\n" \
"ds_read_b128 a[232:235], v233 offset:5120\n" \
"v_cvt_pk_bf16_f32 v116, v120, v121\n" \
"v_mfma_f32_16x16x32_bf16 v[44:47], a[252:255], a[172:175], v[196:199]\n" \
"v_exp_f32 v122, v122\n" \
"v_mfma_f32_16x16x32_bf16 v[44:47], a[248:251], a[168:171], v[44:47]\n" \
"v_mfma_f32_16x16x32_bf16 v[44:47], a[244:247], a[164:167], v[44:47]\n" \
"v_exp_f32 v123, v123\n" \
"v_mfma_f32_16x16x32_bf16 v[44:47], a[240:243], a[160:163], v[44:47]\n" \
"ds_read_b128 a[228:231], v233 offset:12288\n" \
"v_cvt_pk_bf16_f32 v117, v122, v123\n" \
"v_mfma_f32_16x16x32_bf16 v[28:31], a[240:243], a[144:147], v[196:199]\n" \
"v_exp_f32 v124, v124\n" \
"v_mfma_f32_16x16x32_bf16 v[28:31], a[244:247], a[148:151], v[28:31]\n" \
"v_mfma_f32_16x16x32_bf16 v[28:31], a[248:251], a[152:155], v[28:31]\n" \
"v_exp_f32 v125, v125\n" \
"v_mfma_f32_16x16x32_bf16 v[28:31], a[252:255], a[156:159], v[28:31]\n" \
"ds_read_b128 a[224:227], v233 offset:4096\n" \
"Lpg5604_%=:\n" \
"s_cbranch_execz Lpg6793_%=\n" \
"v_cvt_pk_bf16_f32 v118, v124, v125\n" \
"v_mfma_f32_16x16x32_bf16 v[12:15], a[252:255], a[140:143], v[196:199]\n" \
"v_exp_f32 v126, v126\n" \
"v_mfma_f32_16x16x32_bf16 v[12:15], a[248:251], a[136:139], v[12:15]\n" \
"v_mfma_f32_16x16x32_bf16 v[12:15], a[244:247], a[132:135], v[12:15]\n" \
"s_waitcnt lgkmcnt(2)\n" \
"v_exp_f32 v127, v127\n" \
"v_mfma_f32_16x16x32_bf16 v[12:15], a[240:243], a[128:131], v[12:15]\n" \
"ds_read_b128 a[44:47], v233 offset:15360\n" \
"v_cvt_pk_bf16_f32 v119, v126, v127\n" \
"v_mfma_f32_16x16x32_bf16 v[128:131], a[192:195], v[64:67], v[128:131]\n" \
"v_exp_f32 v0, v0\n" \
"v_mfma_f32_16x16x32_bf16 v[128:131], a[196:199], v[68:71], v[128:131]\n" \
"ds_read_b128 a[244:247], v232 offset:10496\n" \
"v_mfma_f32_16x16x32_bf16 v[160:163], a[192:195], v[80:83], v[160:163]\n" \
"v_exp_f32 v1, v1\n" \
"v_mfma_f32_16x16x32_bf16 v[160:163], a[196:199], v[84:87], v[160:163]\n" \
"ds_read_b128 a[240:243], v232 offset:8448\n" \
"v_cvt_pk_bf16_f32 v0, v0, v1\n" \
"v_mfma_f32_16x16x32_bf16 a[64:67], a[192:195], v[96:99], a[64:67]\n" \
"v_exp_f32 v2, v2\n" \
"v_mfma_f32_16x16x32_bf16 a[64:67], a[196:199], v[100:103], a[64:67]\n" \
"ds_read_b128 a[248:251], v232 offset:12544\n" \
"v_xor_b32 v233, 0xc000, v233\n" \
"v_mfma_f32_16x16x32_bf16 a[96:99], a[192:195], v[112:115], a[96:99]\n" \
"v_exp_f32 v3, v3\n" \
"v_mfma_f32_16x16x32_bf16 a[96:99], a[196:199], v[116:119], a[96:99]\n" \
"ds_read_b128 a[192:195], v232 offset:0\n" \
"v_cvt_pk_bf16_f32 v1, v2, v3\n" \
"v_mfma_f32_16x16x32_bf16 a[100:103], a[200:203], v[112:115], a[100:103]\n" \
"v_exp_f32 v4, v4\n" \
"v_mfma_f32_16x16x32_bf16 a[100:103], a[204:207], v[116:119], a[100:103]\n" \
"ds_read_b128 a[252:255], v232 offset:14592\n" \
"v_mfma_f32_16x16x32_bf16 a[68:71], a[200:203], v[96:99], a[68:71]\n" \
"v_exp_f32 v5, v5\n" \
"v_mfma_f32_16x16x32_bf16 a[68:71], a[204:207], v[100:103], a[68:71]\n" \
"ds_read_b128 a[196:199], v232 offset:2048\n" \
"v_cvt_pk_bf16_f32 v2, v4, v5\n" \
"v_smfmac_f32_16x16x64_bf16 a[48:51], v[244:247], v[64:71], v243\n" \
"v_mfma_f32_16x16x32_bf16 v[164:167], a[200:203], v[80:83], v[164:167]\n" \
"v_exp_f32 v6, v6\n" \
"v_mfma_f32_16x16x32_bf16 v[164:167], a[204:207], v[84:87], v[164:167]\n" \
"v_mfma_f32_16x16x32_bf16 v[132:135], a[200:203], v[64:67], v[132:135]\n" \
"v_exp_f32 v7, v7\n" \
"v_mfma_f32_16x16x32_bf16 v[132:135], a[204:207], v[68:71], v[132:135]\n" \
"ds_read_b128 a[200:203], v232 offset:4096\n" \
"v_cvt_pk_bf16_f32 v3, v6, v7\n" \
"v_mfma_f32_16x16x32_bf16 v[136:139], a[208:211], v[64:67], v[136:139]\n" \
"v_exp_f32 v16, v16\n" \
"v_mfma_f32_16x16x32_bf16 v[136:139], a[212:215], v[68:71], v[136:139]\n" \
"v_mfma_f32_16x16x32_bf16 v[168:171], a[208:211], v[80:83], v[168:171]\n" \
"v_exp_f32 v17, v17\n" \
"v_mfma_f32_16x16x32_bf16 v[168:171], a[212:215], v[84:87], v[168:171]\n" \
"ds_read_b128 a[204:207], v232 offset:6144\n" \
"v_cvt_pk_bf16_f32 v16, v16, v17\n" \
"v_mfma_f32_16x16x32_bf16 a[72:75], a[208:211], v[96:99], a[72:75]\n" \
"v_exp_f32 v18, v18\n" \
"v_mfma_f32_16x16x32_bf16 a[72:75], a[212:215], v[100:103], a[72:75]\n" \
"v_mfma_f32_16x16x32_bf16 a[104:107], a[208:211], v[112:115], a[104:107]\n" \
"v_exp_f32 v19, v19\n" \
"v_mfma_f32_16x16x32_bf16 a[104:107], a[212:215], v[116:119], a[104:107]\n" \
"ds_read_b128 a[208:211], v232 offset:256\n" \
"v_cvt_pk_bf16_f32 v17, v18, v19\n" \
"v_mfma_f32_16x16x32_bf16 a[108:111], a[216:219], v[112:115], a[108:111]\n" \
"v_exp_f32 v20, v20\n" \
"v_mfma_f32_16x16x32_bf16 a[108:111], a[220:223], v[116:119], a[108:111]\n" \
"v_mfma_f32_16x16x32_bf16 a[76:79], a[216:219], v[96:99], a[76:79]\n" \
"v_exp_f32 v21, v21\n" \
"v_mfma_f32_16x16x32_bf16 a[76:79], a[220:223], v[100:103], a[76:79]\n" \
"ds_read_b128 a[212:215], v232 offset:2304\n" \
"v_cvt_pk_bf16_f32 v18, v20, v21\n" \
"v_smfmac_f32_16x16x64_bf16 a[52:55], v[244:247], v[80:87], v243\n" \
"v_mfma_f32_16x16x32_bf16 v[172:175], a[216:219], v[80:83], v[172:175]\n" \
"v_exp_f32 v22, v22\n" \
"v_mfma_f32_16x16x32_bf16 v[172:175], a[220:223], v[84:87], v[172:175]\n" \
"v_mfma_f32_16x16x32_bf16 v[140:143], a[216:219], v[64:67], v[140:143]\n" \
"s_waitcnt lgkmcnt(10)\n" \
"v_exp_f32 v23, v23\n" \
"v_mfma_f32_16x16x32_bf16 v[140:143], a[220:223], v[68:71], v[140:143]\n" \
"ds_read_b128 a[216:219], v232 offset:4352\n" \
"v_cvt_pk_bf16_f32 v19, v22, v23\n" \
"v_mfma_f32_16x16x32_bf16 v[144:147], a[224:227], v[64:67], v[144:147]\n" \
"v_exp_f32 v32, v32\n" \
"v_mfma_f32_16x16x32_bf16 v[144:147], a[228:231], v[68:71], v[144:147]\n" \
"v_mfma_f32_16x16x32_bf16 v[176:179], a[224:227], v[80:83], v[176:179]\n" \
"v_exp_f32 v33, v33\n" \
"v_mfma_f32_16x16x32_bf16 v[176:179], a[228:231], v[84:87], v[176:179]\n" \
"ds_read_b128 a[220:223], v232 offset:6400\n" \
"v_cvt_pk_bf16_f32 v32, v32, v33\n" \
"v_mfma_f32_16x16x32_bf16 a[80:83], a[224:227], v[96:99], a[80:83]\n" \
"v_exp_f32 v34, v34\n" \
"v_mfma_f32_16x16x32_bf16 a[80:83], a[228:231], v[100:103], a[80:83]\n" \
"v_mfma_f32_16x16x32_bf16 a[112:115], a[224:227], v[112:115], a[112:115]\n" \
"v_exp_f32 v35, v35\n" \
"v_mfma_f32_16x16x32_bf16 a[112:115], a[228:231], v[116:119], a[112:115]\n" \
"ds_read_b128 a[224:227], v232 offset:8192\n" \
"v_cvt_pk_bf16_f32 v33, v34, v35\n" \
"v_mfma_f32_16x16x32_bf16 a[116:119], a[232:235], v[112:115], a[116:119]\n" \
"v_exp_f32 v36, v36\n" \
"v_mfma_f32_16x16x32_bf16 a[116:119], a[236:239], v[116:119], a[116:119]\n" \
"v_mfma_f32_16x16x32_bf16 a[84:87], a[232:235], v[96:99], a[84:87]\n" \
"v_exp_f32 v37, v37\n" \
"v_mfma_f32_16x16x32_bf16 a[84:87], a[236:239], v[100:103], a[84:87]\n" \
"ds_read_b128 a[228:231], v232 offset:10240\n" \
"v_cvt_pk_bf16_f32 v34, v36, v37\n" \
"v_smfmac_f32_16x16x64_bf16 v[224:227], v[244:247], v[96:103], v243\n" \
"v_mfma_f32_16x16x32_bf16 v[180:183], a[232:235], v[80:83], v[180:183]\n" \
"v_exp_f32 v38, v38\n" \
"v_mfma_f32_16x16x32_bf16 v[180:183], a[236:239], v[84:87], v[180:183]\n" \
"v_mfma_f32_16x16x32_bf16 v[148:151], a[232:235], v[64:67], v[148:151]\n" \
"v_exp_f32 v39, v39\n" \
"v_mfma_f32_16x16x32_bf16 v[148:151], a[236:239], v[68:71], v[148:151]\n" \
"ds_read_b128 a[232:235], v232 offset:12288\n" \
"v_cvt_pk_bf16_f32 v35, v38, v39\n" \
"v_mfma_f32_16x16x32_bf16 v[152:155], a[32:35], v[64:67], v[152:155]\n" \
"v_exp_f32 v48, v48\n" \
"v_mfma_f32_16x16x32_bf16 v[152:155], a[36:39], v[68:71], v[152:155]\n" \
"v_mfma_f32_16x16x32_bf16 v[184:187], a[32:35], v[80:83], v[184:187]\n" \
"v_exp_f32 v49, v49\n" \
"v_mfma_f32_16x16x32_bf16 v[184:187], a[36:39], v[84:87], v[184:187]\n" \
"ds_read_b128 a[236:239], v232 offset:14336\n" \
"v_cvt_pk_bf16_f32 v48, v48, v49\n" \
"v_mfma_f32_16x16x32_bf16 a[88:91], a[32:35], v[96:99], a[88:91]\n" \
"v_exp_f32 v50, v50\n" \
"v_mfma_f32_16x16x32_bf16 a[88:91], a[36:39], v[100:103], a[88:91]\n" \
"v_mfma_f32_16x16x32_bf16 a[120:123], a[32:35], v[112:115], a[120:123]\n" \
"v_exp_f32 v51, v51\n" \
"v_mfma_f32_16x16x32_bf16 a[120:123], a[36:39], v[116:119], a[120:123]\n" \
"v_cvt_pk_bf16_f32 v49, v50, v51\n" \
"v_mfma_f32_16x16x32_bf16 a[124:127], a[40:43], v[112:115], a[124:127]\n" \
"v_exp_f32 v52, v52\n" \
"v_mfma_f32_16x16x32_bf16 a[124:127], a[44:47], v[116:119], a[124:127]\n" \
"v_mfma_f32_16x16x32_bf16 a[92:95], a[40:43], v[96:99], a[92:95]\n" \
"v_exp_f32 v53, v53\n" \
"v_mfma_f32_16x16x32_bf16 a[92:95], a[44:47], v[100:103], a[92:95]\n" \
"v_cvt_pk_bf16_f32 v50, v52, v53\n" \
"v_smfmac_f32_16x16x64_bf16 v[228:231], v[244:247], v[112:119], v243\n" \
"v_mfma_f32_16x16x32_bf16 v[188:191], a[40:43], v[80:83], v[188:191]\n" \
"v_exp_f32 v54, v54\n" \
"v_mfma_f32_16x16x32_bf16 v[188:191], a[44:47], v[84:87], v[188:191]\n" \
"v_mfma_f32_16x16x32_bf16 v[156:159], a[40:43], v[64:67], v[156:159]\n" \
"s_waitcnt vmcnt(1) lgkmcnt(0)\n" \
"v_exp_f32 v55, v55\n" \
"v_mfma_f32_16x16x32_bf16 v[156:159], a[44:47], v[68:71], v[156:159]\n" \
"s_barrier\n" \
"v_cvt_pk_bf16_f32 v51, v54, v55\n" \
"v_mfma_f32_16x16x32_bf16 v[64:67], a[192:195], a[128:131], v[196:199]\n" \
"v_exp_f32 v8, v8\n" \
"v_mfma_f32_16x16x32_bf16 v[64:67], a[196:199], a[132:135], v[64:67]\n" \
"v_cmp_eq_u32 vcc, s60, v239\n" \
"v_mfma_f32_16x16x32_bf16 v[64:67], a[200:203], a[136:139], v[64:67]\n" \
"v_exp_f32 v9, v9\n" \
"v_mfma_f32_16x16x32_bf16 v[64:67], a[204:207], a[140:143], v[64:67]\n" \
"s_andn2_b32 s70, s69, vcc_lo\n" \
"s_cbranch_scc1 Lpl2_%=\n" \
"Lpr2_%=:\n" \
"ds_read_b128 a[32:35], v233 offset:6144\n" \
"v_cvt_pk_bf16_f32 v4, v8, v9\n" \
"v_mfma_f32_16x16x32_bf16 v[80:83], a[204:207], a[156:159], v[196:199]\n" \
"s_add_u32 m0, s77, s80\n" \
"v_exp_f32 v10, v10\n" \
"v_mfma_f32_16x16x32_bf16 v[80:83], a[200:203], a[152:155], v[80:83]\n" \
"buffer_load_dwordx4 v234, s[40:43], s76 offen offset:0 sc1 lds\n" \
"v_mfma_f32_16x16x32_bf16 v[80:83], a[196:199], a[148:151], v[80:83]\n" \
"s_sub_u32 s89, s89, 1\n" \
"v_exp_f32 v11, v11\n" \
"v_mfma_f32_16x16x32_bf16 v[80:83], a[192:195], a[144:147], v[80:83]\n" \
"s_cmp_eq_u32 s89, 0\n" \
"s_cselect_b32 s70, s78, 0xc000\n" \
"ds_read_b128 a[36:39], v233 offset:14336\n" \
"v_cvt_pk_bf16_f32 v5, v10, v11\n" \
"v_mfma_f32_16x16x32_bf16 v[96:99], a[192:195], a[160:163], v[196:199]\n" \
"s_add_u32 s71, s76, 17408\n" \
"v_exp_f32 v12, v12\n" \
"v_mfma_f32_16x16x32_bf16 v[96:99], a[196:199], a[164:167], v[96:99]\n" \
"buffer_load_dwordx4 v234, s[40:43], s76 offen offset:1024 sc1 lds\n" \
"v_mfma_f32_16x16x32_bf16 v[96:99], a[200:203], a[168:171], v[96:99]\n" \
"v_exp_f32 v13, v13\n" \
"v_mfma_f32_16x16x32_bf16 v[96:99], a[204:207], a[172:175], v[96:99]\n" \
"ds_read_b128 a[40:43], v233 offset:7168\n" \
"v_cvt_pk_bf16_f32 v6, v12, v13\n" \
"v_mfma_f32_16x16x32_bf16 v[112:115], a[204:207], a[188:191], v[196:199]\n" \
"v_exp_f32 v14, v14\n" \
"v_mfma_f32_16x16x32_bf16 v[112:115], a[200:203], a[184:187], v[112:115]\n" \
"v_xor_b32 v232, s70, v232\n" \
"v_mfma_f32_16x16x32_bf16 v[112:115], a[196:199], a[180:183], v[112:115]\n" \
"v_exp_f32 v15, v15\n" \
"v_mfma_f32_16x16x32_bf16 v[112:115], a[192:195], a[176:179], v[112:115]\n" \
"ds_read_b128 a[204:207], v233 offset:9216\n" \
"v_cvt_pk_bf16_f32 v7, v14, v15\n" \
"v_mfma_f32_16x16x32_bf16 v[116:119], a[208:211], a[176:179], v[196:199]\n" \
"v_exp_f32 v24, v24\n" \
"v_mfma_f32_16x16x32_bf16 v[116:119], a[212:215], a[180:183], v[116:119]\n" \
"buffer_load_dwordx4 v234, s[40:43], s76 offen offset:2048 sc1 lds\n" \
"v_mfma_f32_16x16x32_bf16 v[116:119], a[216:219], a[184:187], v[116:119]\n" \
"v_exp_f32 v25, v25\n" \
"v_mfma_f32_16x16x32_bf16 v[116:119], a[220:223], a[188:191], v[116:119]\n" \
"ds_read_b128 a[200:203], v233 offset:1024\n" \
"v_cvt_pk_bf16_f32 v20, v24, v25\n" \
"v_mfma_f32_16x16x32_bf16 v[100:103], a[220:223], a[172:175], v[196:199]\n" \
"v_exp_f32 v26, v26\n" \
"v_mfma_f32_16x16x32_bf16 v[100:103], a[216:219], a[168:171], v[100:103]\n" \
"buffer_load_dwordx4 v234, s[40:43], s76 offen offset:3072 sc1 lds\n" \
"v_mfma_f32_16x16x32_bf16 v[100:103], a[212:215], a[164:167], v[100:103]\n" \
"v_exp_f32 v27, v27\n" \
"v_mfma_f32_16x16x32_bf16 v[100:103], a[208:211], a[160:163], v[100:103]\n" \
"ds_read_b128 a[196:199], v233 offset:8192\n" \
"v_cvt_pk_bf16_f32 v21, v26, v27\n" \
"v_mfma_f32_16x16x32_bf16 v[84:87], a[208:211], a[144:147], v[196:199]\n" \
"v_exp_f32 v28, v28\n" \
"v_mfma_f32_16x16x32_bf16 v[84:87], a[212:215], a[148:151], v[84:87]\n" \
"v_mfma_f32_16x16x32_bf16 v[84:87], a[216:219], a[152:155], v[84:87]\n" \
"v_exp_f32 v29, v29\n" \
"v_mfma_f32_16x16x32_bf16 v[84:87], a[220:223], a[156:159], v[84:87]\n" \
"ds_read_b128 a[192:195], v233 offset:0\n" \
"v_cvt_pk_bf16_f32 v22, v28, v29\n" \
"v_mfma_f32_16x16x32_bf16 v[68:71], a[220:223], a[140:143], v[196:199]\n" \
"s_add_u32 m0, m0, 0x4000\n" \
"v_exp_f32 v30, v30\n" \
"v_mfma_f32_16x16x32_bf16 v[68:71], a[216:219], a[136:139], v[68:71]\n" \
"buffer_load_dwordx4 v234, s[40:43], s71 offen offset:0 sc1 lds\n" \
"v_mfma_f32_16x16x32_bf16 v[68:71], a[212:215], a[132:135], v[68:71]\n" \
"v_exp_f32 v31, v31\n" \
"v_mfma_f32_16x16x32_bf16 v[68:71], a[208:211], a[128:131], v[68:71]\n" \
"ds_read_b128 a[220:223], v233 offset:11264\n" \
"v_cvt_pk_bf16_f32 v23, v30, v31\n" \
"v_mfma_f32_16x16x32_bf16 v[72:75], a[224:227], a[128:131], v[196:199]\n" \
"v_exp_f32 v40, v40\n" \
"v_mfma_f32_16x16x32_bf16 v[72:75], a[228:231], a[132:135], v[72:75]\n" \
"buffer_load_dwordx4 v234, s[40:43], s71 offen offset:1024 sc1 lds\n" \
"v_mfma_f32_16x16x32_bf16 v[72:75], a[232:235], a[136:139], v[72:75]\n" \
"v_exp_f32 v41, v41\n" \
"v_mfma_f32_16x16x32_bf16 v[72:75], a[236:239], a[140:143], v[72:75]\n" \
"ds_read_b128 a[216:219], v233 offset:3072\n" \
"v_cvt_pk_bf16_f32 v36, v40, v41\n" \
"v_mfma_f32_16x16x32_bf16 v[88:91], a[236:239], a[156:159], v[196:199]\n" \
"v_exp_f32 v42, v42\n" \
"v_mfma_f32_16x16x32_bf16 v[88:91], a[232:235], a[152:155], v[88:91]\n" \
"v_mfma_f32_16x16x32_bf16 v[88:91], a[228:231], a[148:151], v[88:91]\n" \
"v_exp_f32 v43, v43\n" \
"v_mfma_f32_16x16x32_bf16 v[88:91], a[224:227], a[144:147], v[88:91]\n" \
"ds_read_b128 a[212:215], v233 offset:10240\n" \
"v_cvt_pk_bf16_f32 v37, v42, v43\n" \
"v_mfma_f32_16x16x32_bf16 v[104:107], a[224:227], a[160:163], v[196:199]\n" \
"v_exp_f32 v44, v44\n" \
"v_mfma_f32_16x16x32_bf16 v[104:107], a[228:231], a[164:167], v[104:107]\n" \
"buffer_load_dwordx4 v234, s[40:43], s71 offen offset:2048 sc1 lds\n" \
"v_mfma_f32_16x16x32_bf16 v[104:107], a[232:235], a[168:171], v[104:107]\n" \
"v_exp_f32 v45, v45\n" \
"v_mfma_f32_16x16x32_bf16 v[104:107], a[236:239], a[172:175], v[104:107]\n" \
"ds_read_b128 a[208:211], v233 offset:2048\n" \
"v_cvt_pk_bf16_f32 v38, v44, v45\n" \
"v_mfma_f32_16x16x32_bf16 v[120:123], a[236:239], a[188:191], v[196:199]\n" \
"v_exp_f32 v46, v46\n" \
"v_mfma_f32_16x16x32_bf16 v[120:123], a[232:235], a[184:187], v[120:123]\n" \
"buffer_load_dwordx4 v234, s[40:43], s71 offen offset:3072 sc1 lds\n" \
"v_mfma_f32_16x16x32_bf16 v[120:123], a[228:231], a[180:183], v[120:123]\n" \
"v_exp_f32 v47, v47\n" \
"v_mfma_f32_16x16x32_bf16 v[120:123], a[224:227], a[176:179], v[120:123]\n" \
"ds_read_b128 a[236:239], v233 offset:13312\n" \
"v_cvt_pk_bf16_f32 v39, v46, v47\n" \
"v_mfma_f32_16x16x32_bf16 v[124:127], a[240:243], a[176:179], v[196:199]\n" \
"v_exp_f32 v56, v56\n" \
"v_mfma_f32_16x16x32_bf16 v[124:127], a[244:247], a[180:183], v[124:127]\n" \
"v_mfma_f32_16x16x32_bf16 v[124:127], a[248:251], a[184:187], v[124:127]\n" \
"v_exp_f32 v57, v57\n" \
"v_mfma_f32_16x16x32_bf16 v[124:127], a[252:255], a[188:191], v[124:127]\n" \
"ds_read_b128 a[232:235], v233 offset:5120\n" \
"v_cvt_pk_bf16_f32 v52, v56, v57\n" \
"v_mfma_f32_16x16x32_bf16 v[108:111], a[252:255], a[172:175], v[196:199]\n" \
"v_exp_f32 v58, v58\n" \
"v_mfma_f32_16x16x32_bf16 v[108:111], a[248:251], a[168:171], v[108:111]\n" \
"v_mfma_f32_16x16x32_bf16 v[108:111], a[244:247], a[164:167], v[108:111]\n" \
"v_exp_f32 v59, v59\n" \
"v_mfma_f32_16x16x32_bf16 v[108:111], a[240:243], a[160:163], v[108:111]\n" \
"ds_read_b128 a[228:231], v233 offset:12288\n" \
"v_cvt_pk_bf16_f32 v53, v58, v59\n" \
"v_mfma_f32_16x16x32_bf16 v[92:95], a[240:243], a[144:147], v[196:199]\n" \
"v_exp_f32 v60, v60\n" \
"v_mfma_f32_16x16x32_bf16 v[92:95], a[244:247], a[148:151], v[92:95]\n" \
"v_mfma_f32_16x16x32_bf16 v[92:95], a[248:251], a[152:155], v[92:95]\n" \
"v_exp_f32 v61, v61\n" \
"v_mfma_f32_16x16x32_bf16 v[92:95], a[252:255], a[156:159], v[92:95]\n" \
"ds_read_b128 a[224:227], v233 offset:4096\n" \
"v_cvt_pk_bf16_f32 v54, v60, v61\n" \
"v_mfma_f32_16x16x32_bf16 v[76:79], a[252:255], a[140:143], v[196:199]\n" \
"v_exp_f32 v62, v62\n" \
"v_mfma_f32_16x16x32_bf16 v[76:79], a[248:251], a[136:139], v[76:79]\n" \
"v_mfma_f32_16x16x32_bf16 v[76:79], a[244:247], a[132:135], v[76:79]\n" \
"s_waitcnt lgkmcnt(2)\n" \
"v_exp_f32 v63, v63\n" \
"v_mfma_f32_16x16x32_bf16 v[76:79], a[240:243], a[128:131], v[76:79]\n" \
"ds_read_b128 a[44:47], v233 offset:15360\n" \
"v_cvt_pk_bf16_f32 v55, v62, v63\n" \
"v_mfma_f32_16x16x32_bf16 v[128:131], a[192:195], v[0:3], v[128:131]\n" \
"s_add_u32 m0, s74, s79\n" \
"v_exp_f32 v64, v64\n" \
"v_mfma_f32_16x16x32_bf16 v[128:131], a[196:199], v[4:7], v[128:131]\n" \
"buffer_load_dwordx4 v234, s[36:39], s73 offen offset:0 sc1 lds\n" \
"v_mfma_f32_16x16x32_bf16 v[160:163], a[192:195], v[16:19], v[160:163]\n" \
"v_exp_f32 v65, v65\n" \
"v_mfma_f32_16x16x32_bf16 v[160:163], a[196:199], v[20:23], v[160:163]\n" \
"ds_read_b128 a[240:243], v232 offset:8448\n" \
"v_cvt_pk_bf16_f32 v64, v64, v65\n" \
"v_mfma_f32_16x16x32_bf16 a[64:67], a[192:195], v[32:35], a[64:67]\n" \
"s_add_u32 s71, s73, 17408\n" \
"v_exp_f32 v66, v66\n" \
"v_mfma_f32_16x16x32_bf16 a[64:67], a[196:199], v[36:39], a[64:67]\n" \
"buffer_load_dwordx4 v234, s[36:39], s73 offen offset:1024 sc1 lds\n" \
"v_mfma_f32_16x16x32_bf16 a[96:99], a[192:195], v[48:51], a[96:99]\n" \
"v_exp_f32 v67, v67\n" \
"v_mfma_f32_16x16x32_bf16 a[96:99], a[196:199], v[52:55], a[96:99]\n" \
"ds_read_b128 a[192:195], v232 offset:0\n" \
"v_cvt_pk_bf16_f32 v65, v66, v67\n" \
"v_mfma_f32_16x16x32_bf16 a[100:103], a[200:203], v[48:51], a[100:103]\n" \
"v_exp_f32 v68, v68\n" \
"v_mfma_f32_16x16x32_bf16 a[100:103], a[204:207], v[52:55], a[100:103]\n" \
"v_xor_b32 v233, 0x4000, v233\n" \
"v_mfma_f32_16x16x32_bf16 a[68:71], a[200:203], v[32:35], a[68:71]\n" \
"v_exp_f32 v69, v69\n" \
"v_mfma_f32_16x16x32_bf16 a[68:71], a[204:207], v[36:39], a[68:71]\n" \
"ds_read_b128 a[196:199], v232 offset:2048\n" \
"v_cvt_pk_bf16_f32 v66, v68, v69\n" \
"v_smfmac_f32_16x16x64_bf16 a[48:51], v[244:247], v[0:7], v243\n" \
"buffer_load_dwordx4 v234, s[36:39], s73 offen offset:2048 sc1 lds\n" \
"v_mfma_f32_16x16x32_bf16 v[164:167], a[200:203], v[16:19], v[164:167]\n" \
"v_exp_f32 v70, v70\n" \
"v_mfma_f32_16x16x32_bf16 v[164:167], a[204:207], v[20:23], v[164:167]\n" \
"v_mfma_f32_16x16x32_bf16 v[132:135], a[200:203], v[0:3], v[132:135]\n" \
"v_exp_f32 v71, v71\n" \
"v_mfma_f32_16x16x32_bf16 v[132:135], a[204:207], v[4:7], v[132:135]\n" \
"ds_read_b128 a[200:203], v232 offset:4096\n" \
"v_cvt_pk_bf16_f32 v67, v70, v71\n" \
"v_mfma_f32_16x16x32_bf16 v[136:139], a[208:211], v[0:3], v[136:139]\n" \
"v_exp_f32 v80, v80\n" \
"v_mfma_f32_16x16x32_bf16 v[136:139], a[212:215], v[4:7], v[136:139]\n" \
"buffer_load_dwordx4 v234, s[36:39], s73 offen offset:3072 sc1 lds\n" \
"v_mfma_f32_16x16x32_bf16 v[168:171], a[208:211], v[16:19], v[168:171]\n" \
"v_exp_f32 v81, v81\n" \
"v_mfma_f32_16x16x32_bf16 v[168:171], a[212:215], v[20:23], v[168:171]\n" \
"ds_read_b128 a[204:207], v232 offset:6144\n" \
"v_cvt_pk_bf16_f32 v80, v80, v81\n" \
"v_mfma_f32_16x16x32_bf16 a[72:75], a[208:211], v[32:35], a[72:75]\n" \
"v_exp_f32 v82, v82\n" \
"v_mfma_f32_16x16x32_bf16 a[72:75], a[212:215], v[36:39], a[72:75]\n" \
"v_mfma_f32_16x16x32_bf16 a[104:107], a[208:211], v[48:51], a[104:107]\n" \
"v_exp_f32 v83, v83\n" \
"v_mfma_f32_16x16x32_bf16 a[104:107], a[212:215], v[52:55], a[104:107]\n" \
"ds_read_b128 a[208:211], v232 offset:256\n" \
"v_cvt_pk_bf16_f32 v81, v82, v83\n" \
"v_mfma_f32_16x16x32_bf16 a[108:111], a[216:219], v[48:51], a[108:111]\n" \
"s_add_u32 m0, m0, 0x4000\n" \
"v_exp_f32 v84, v84\n" \
"v_mfma_f32_16x16x32_bf16 a[108:111], a[220:223], v[52:55], a[108:111]\n" \
"buffer_load_dwordx4 v234, s[36:39], s71 offen offset:0 sc1 lds\n" \
"v_mfma_f32_16x16x32_bf16 a[76:79], a[216:219], v[32:35], a[76:79]\n" \
"v_exp_f32 v85, v85\n" \
"v_mfma_f32_16x16x32_bf16 a[76:79], a[220:223], v[36:39], a[76:79]\n" \
"ds_read_b128 a[212:215], v232 offset:2304\n" \
"v_cvt_pk_bf16_f32 v82, v84, v85\n" \
"v_smfmac_f32_16x16x64_bf16 a[52:55], v[244:247], v[16:23], v243\n" \
"v_mfma_f32_16x16x32_bf16 v[172:175], a[216:219], v[16:19], v[172:175]\n" \
"v_exp_f32 v86, v86\n" \
"v_mfma_f32_16x16x32_bf16 v[172:175], a[220:223], v[20:23], v[172:175]\n" \
"buffer_load_dwordx4 v234, s[36:39], s71 offen offset:1024 sc1 lds\n" \
"v_mfma_f32_16x16x32_bf16 v[140:143], a[216:219], v[0:3], v[140:143]\n" \
"s_waitcnt lgkmcnt(7)\n" \
"v_exp_f32 v87, v87\n" \
"v_mfma_f32_16x16x32_bf16 v[140:143], a[220:223], v[4:7], v[140:143]\n" \
"ds_read_b128 a[216:219], v232 offset:4352\n" \
"v_cvt_pk_bf16_f32 v83, v86, v87\n" \
"v_mfma_f32_16x16x32_bf16 v[144:147], a[224:227], v[0:3], v[144:147]\n" \
"v_exp_f32 v96, v96\n" \
"v_mfma_f32_16x16x32_bf16 v[144:147], a[228:231], v[4:7], v[144:147]\n" \
"buffer_load_dwordx4 v234, s[36:39], s71 offen offset:2048 sc1 lds\n" \
"v_mfma_f32_16x16x32_bf16 v[176:179], a[224:227], v[16:19], v[176:179]\n" \
"v_exp_f32 v97, v97\n" \
"v_mfma_f32_16x16x32_bf16 v[176:179], a[228:231], v[20:23], v[176:179]\n" \
"ds_read_b128 a[220:223], v232 offset:6400\n" \
"v_cvt_pk_bf16_f32 v96, v96, v97\n" \
"v_mfma_f32_16x16x32_bf16 a[80:83], a[224:227], v[32:35], a[80:83]\n" \
"v_exp_f32 v98, v98\n" \
"v_mfma_f32_16x16x32_bf16 a[80:83], a[228:231], v[36:39], a[80:83]\n" \
"v_mfma_f32_16x16x32_bf16 a[112:115], a[224:227], v[48:51], a[112:115]\n" \
"v_exp_f32 v99, v99\n" \
"v_mfma_f32_16x16x32_bf16 a[112:115], a[228:231], v[52:55], a[112:115]\n" \
"ds_read_b128 a[224:227], v232 offset:8192\n" \
"v_cvt_pk_bf16_f32 v97, v98, v99\n" \
"v_mfma_f32_16x16x32_bf16 a[116:119], a[232:235], v[48:51], a[116:119]\n" \
"v_exp_f32 v100, v100\n" \
"v_mfma_f32_16x16x32_bf16 a[116:119], a[236:239], v[52:55], a[116:119]\n" \
"buffer_load_dwordx4 v234, s[36:39], s71 offen offset:3072 sc1 lds\n" \
"v_mfma_f32_16x16x32_bf16 a[84:87], a[232:235], v[32:35], a[84:87]\n" \
"v_exp_f32 v101, v101\n" \
"v_mfma_f32_16x16x32_bf16 a[84:87], a[236:239], v[36:39], a[84:87]\n" \
"ds_read_b128 a[228:231], v232 offset:10240\n" \
"v_cvt_pk_bf16_f32 v98, v100, v101\n" \
"v_smfmac_f32_16x16x64_bf16 v[224:227], v[244:247], v[32:39], v243\n" \
"v_mfma_f32_16x16x32_bf16 v[180:183], a[232:235], v[16:19], v[180:183]\n" \
"v_exp_f32 v102, v102\n" \
"v_mfma_f32_16x16x32_bf16 v[180:183], a[236:239], v[20:23], v[180:183]\n" \
"buffer_load_dword v239, v242, s[56:59], s95 offen sc1\n" \
"v_mfma_f32_16x16x32_bf16 v[148:151], a[232:235], v[0:3], v[148:151]\n" \
"s_mov_b32 s69, s92\n" \
"v_exp_f32 v103, v103\n" \
"v_mfma_f32_16x16x32_bf16 v[148:151], a[236:239], v[4:7], v[148:151]\n" \
"s_mov_b32 s76, s73\n" \
"ds_read_b128 a[232:235], v232 offset:12288\n" \
"v_cvt_pk_bf16_f32 v99, v102, v103\n" \
"v_mfma_f32_16x16x32_bf16 v[152:155], a[32:35], v[0:3], v[152:155]\n" \
"s_add_u32 s73, s73, 34816\n" \
"v_exp_f32 v112, v112\n" \
"v_mfma_f32_16x16x32_bf16 v[152:155], a[36:39], v[4:7], v[152:155]\n" \
"s_cmp_eq_u32 s73, s83\n" \
"s_cselect_b32 s70, 34816, 0\n" \
"s_add_u32 s73, s73, s70\n" \
"v_mfma_f32_16x16x32_bf16 v[184:187], a[32:35], v[16:19], v[184:187]\n" \
"s_xor_b32 s74, s74, 0x8000\n" \
"v_exp_f32 v113, v113\n" \
"v_mfma_f32_16x16x32_bf16 v[184:187], a[36:39], v[20:23], v[184:187]\n" \
"s_xor_b32 s77, s77, 0x8000\n" \
"ds_read_b128 a[236:239], v232 offset:14336\n" \
"v_cvt_pk_bf16_f32 v112, v112, v113\n" \
"v_mfma_f32_16x16x32_bf16 a[88:91], a[32:35], v[32:35], a[88:91]\n" \
"v_exp_f32 v114, v114\n" \
"v_mfma_f32_16x16x32_bf16 a[88:91], a[36:39], v[36:39], a[88:91]\n" \
"v_mfma_f32_16x16x32_bf16 a[120:123], a[32:35], v[48:51], a[120:123]\n" \
"v_exp_f32 v115, v115\n" \
"v_mfma_f32_16x16x32_bf16 a[120:123], a[36:39], v[52:55], a[120:123]\n" \
"ds_read_b128 a[244:247], v232 offset:10496\n" \
"v_cvt_pk_bf16_f32 v113, v114, v115\n" \
"v_mfma_f32_16x16x32_bf16 a[124:127], a[40:43], v[48:51], a[124:127]\n" \
"v_exp_f32 v116, v116\n" \
"v_mfma_f32_16x16x32_bf16 a[124:127], a[44:47], v[52:55], a[124:127]\n" \
"v_mfma_f32_16x16x32_bf16 a[92:95], a[40:43], v[32:35], a[92:95]\n" \
"v_exp_f32 v117, v117\n" \
"v_mfma_f32_16x16x32_bf16 a[92:95], a[44:47], v[36:39], a[92:95]\n" \
"ds_read_b128 a[248:251], v232 offset:12544\n" \
"v_cvt_pk_bf16_f32 v114, v116, v117\n" \
"v_smfmac_f32_16x16x64_bf16 v[228:231], v[244:247], v[48:55], v243\n" \
"v_mfma_f32_16x16x32_bf16 v[188:191], a[40:43], v[16:19], v[188:191]\n" \
"v_exp_f32 v118, v118\n" \
"v_mfma_f32_16x16x32_bf16 v[188:191], a[44:47], v[20:23], v[188:191]\n" \
"s_cmp_lg_u32 s89, 0\n" \
"v_exp_f32 v119, v119\n" \
"v_mfma_f32_16x16x32_bf16 v[156:159], a[40:43], v[0:3], v[156:159]\n" \
"s_waitcnt lgkmcnt(2)\n" \
"ds_read_b128 a[252:255], v232 offset:14592\n" \
"v_cvt_pk_bf16_f32 v115, v118, v119\n" \
"v_mfma_f32_16x16x32_bf16 v[156:159], a[44:47], v[4:7], v[156:159]\n" \
"s_cbranch_scc1 Lloop_%=\n" \
"s_cmp_eq_u32 s63, 7\n" \
"s_cbranch_scc0 Lloop2_%=\n" \
"s_branch Lg_%=\n" \
"Lep_%=:\n" \
"Lpg6060_%=:\n" \
"s_cbranch_execz Lpg6797_%=\n" \
"s_nop 4\n" \
"v_accvgpr_read_b32 v92, a48\n" \
"v_accvgpr_read_b32 v93, a49\n" \
"v_accvgpr_read_b32 v94, a52\n" \
"v_accvgpr_read_b32 v95, a53\n" \
"v_accvgpr_write_b32 a48, 0\n" \
"v_accvgpr_write_b32 a49, 0\n" \
"v_accvgpr_write_b32 a52, 0\n" \
"v_accvgpr_write_b32 a53, 0\n" \
"v_add_f32 v86, v92, v93\n" \
"v_add_f32 v87, v94, v95\n" \
"v_rcp_f32 v84, v86\n" \
"v_rcp_f32 v85, v87\n" \
"v_add_u32 v240, 0x1000, v236\n" \
"v_add_f32 v80, v86, v87\n" \
"v_mul_f32 v80, 0x43800000, v80\n" \
"v_add_f32 v86, v84, v85\n" \
"v_fmac_f32 v80, 0x5f800000, v86\n" \
"v_mov_b32 v81, 0\n" \
"v_mov_b32 v82, 0\n" \
"v_mov_b32 v83, 0\n" \
"v_mov_b32 v88, 0\n" \
"v_mov_b32 v89, 0\n" \
"v_mov_b32 v90, 0\n" \
"v_mov_b32 v91, 0\n" \
"v_pk_mul_f32 v[128:129], v[128:129], v[84:85] op_sel:[0,0] op_sel_hi:[1,0]\n" \
"v_pk_mul_f32 v[130:131], v[130:131], v[84:85] op_sel:[0,0] op_sel_hi:[1,0]\n" \
"v_pk_mul_f32 v[132:133], v[132:133], v[84:85] op_sel:[0,0] op_sel_hi:[1,0]\n" \
"v_pk_mul_f32 v[134:135], v[134:135], v[84:85] op_sel:[0,0] op_sel_hi:[1,0]\n" \
"v_cvt_pk_bf16_f32 v64, v128, v129\n" \
"v_cvt_pk_bf16_f32 v65, v130, v131\n" \
"v_cvt_pk_bf16_f32 v66, v132, v133\n" \
"v_cvt_pk_bf16_f32 v67, v134, v135\n" \
"buffer_store_dwordx4 v[64:67], v236, s[56:59], s87 offen offset:0 sc1\n" \
"v_pk_mul_f32 v[136:137], v[136:137], v[84:85] op_sel:[0,0] op_sel_hi:[1,0]\n" \
"v_pk_mul_f32 v[138:139], v[138:139], v[84:85] op_sel:[0,0] op_sel_hi:[1,0]\n" \
"v_pk_mul_f32 v[140:141], v[140:141], v[84:85] op_sel:[0,0] op_sel_hi:[1,0]\n" \
"v_pk_mul_f32 v[142:143], v[142:143], v[84:85] op_sel:[0,0] op_sel_hi:[1,0]\n" \
"v_cvt_pk_bf16_f32 v68, v136, v137\n" \
"v_cvt_pk_bf16_f32 v69, v138, v139\n" \
"v_cvt_pk_bf16_f32 v70, v140, v141\n" \
"v_cvt_pk_bf16_f32 v71, v142, v143\n" \
"buffer_store_dwordx4 v[68:71], v236, s[56:59], s87 offen offset:2048 sc1\n" \
"v_pk_mul_f32 v[144:145], v[144:145], v[84:85] op_sel:[0,0] op_sel_hi:[1,0]\n" \
"v_pk_mul_f32 v[146:147], v[146:147], v[84:85] op_sel:[0,0] op_sel_hi:[1,0]\n" \
"v_pk_mul_f32 v[148:149], v[148:149], v[84:85] op_sel:[0,0] op_sel_hi:[1,0]\n" \
"v_pk_mul_f32 v[150:151], v[150:151], v[84:85] op_sel:[0,0] op_sel_hi:[1,0]\n" \
"v_cvt_pk_bf16_f32 v72, v144, v145\n" \
"v_cvt_pk_bf16_f32 v73, v146, v147\n" \
"v_cvt_pk_bf16_f32 v74, v148, v149\n" \
"v_cvt_pk_bf16_f32 v75, v150, v151\n" \
"buffer_store_dwordx4 v[72:75], v240, s[56:59], s87 offen offset:0 sc1\n" \
"v_pk_mul_f32 v[152:153], v[152:153], v[84:85] op_sel:[0,0] op_sel_hi:[1,0]\n" \
"v_pk_mul_f32 v[154:155], v[154:155], v[84:85] op_sel:[0,0] op_sel_hi:[1,0]\n" \
"v_pk_mul_f32 v[156:157], v[156:157], v[84:85] op_sel:[0,0] op_sel_hi:[1,0]\n" \
"v_pk_mul_f32 v[158:159], v[158:159], v[84:85] op_sel:[0,0] op_sel_hi:[1,0]\n" \
"v_cvt_pk_bf16_f32 v76, v152, v153\n" \
"v_cvt_pk_bf16_f32 v77, v154, v155\n" \
"v_cvt_pk_bf16_f32 v78, v156, v157\n" \
"v_cvt_pk_bf16_f32 v79, v158, v159\n" \
"buffer_store_dwordx4 v[76:79], v240, s[56:59], s87 offen offset:2048 sc1\n" \
"v_smfmac_f32_16x16x64_bf16 v[80:83], v[244:247], v[64:71], v243\n" \
"v_smfmac_f32_16x16x64_bf16 v[80:83], v[244:247], v[72:79], v243\n" \
"v_mfma_f32_32x32x16_bf16 v[128:143], v[88:91], v[88:91], 0\n" \
"v_mfma_f32_32x32x16_bf16 v[144:159], v[88:91], v[88:91], 0\n" \
"s_mov_b64 exec, 0\n" \
"s_cmp_eq_u32 s61, 0\n" \
"s_cbranch_scc1 Lst1x0_%=\n" \
"s_cmp_eq_u32 s61, 1\n" \
"s_cbranch_scc1 Lst1x1_%=\n" \
"s_cmp_eq_u32 s61, 2\n" \
"s_cbranch_scc1 Lst1x2_%=\n" \
"s_branch Lst1x3_%=\n" \
"Lcb1_%=:\n" \
"s_mov_b64 exec, -1\n" \
"s_add_u32 s72, s87, 532480\n" \
"v_pk_mul_f32 v[160:161], v[160:161], v[84:85] op_sel:[0,1] op_sel_hi:[1,1]\n" \
"v_pk_mul_f32 v[162:163], v[162:163], v[84:85] op_sel:[0,1] op_sel_hi:[1,1]\n" \
"v_pk_mul_f32 v[164:165], v[164:165], v[84:85] op_sel:[0,1] op_sel_hi:[1,1]\n" \
"v_pk_mul_f32 v[166:167], v[166:167], v[84:85] op_sel:[0,1] op_sel_hi:[1,1]\n" \
"v_cvt_pk_bf16_f32 v64, v160, v161\n" \
"v_cvt_pk_bf16_f32 v65, v162, v163\n" \
"v_cvt_pk_bf16_f32 v66, v164, v165\n" \
"v_cvt_pk_bf16_f32 v67, v166, v167\n" \
"buffer_store_dwordx4 v[64:67], v236, s[56:59], s72 offen offset:0 sc1\n" \
"v_pk_mul_f32 v[168:169], v[168:169], v[84:85] op_sel:[0,1] op_sel_hi:[1,1]\n" \
"v_pk_mul_f32 v[170:171], v[170:171], v[84:85] op_sel:[0,1] op_sel_hi:[1,1]\n" \
"v_pk_mul_f32 v[172:173], v[172:173], v[84:85] op_sel:[0,1] op_sel_hi:[1,1]\n" \
"v_pk_mul_f32 v[174:175], v[174:175], v[84:85] op_sel:[0,1] op_sel_hi:[1,1]\n" \
"v_cvt_pk_bf16_f32 v68, v168, v169\n" \
"v_cvt_pk_bf16_f32 v69, v170, v171\n" \
"v_cvt_pk_bf16_f32 v70, v172, v173\n" \
"v_cvt_pk_bf16_f32 v71, v174, v175\n" \
"buffer_store_dwordx4 v[68:71], v236, s[56:59], s72 offen offset:2048 sc1\n" \
"v_pk_mul_f32 v[176:177], v[176:177], v[84:85] op_sel:[0,1] op_sel_hi:[1,1]\n" \
"v_pk_mul_f32 v[178:179], v[178:179], v[84:85] op_sel:[0,1] op_sel_hi:[1,1]\n" \
"v_pk_mul_f32 v[180:181], v[180:181], v[84:85] op_sel:[0,1] op_sel_hi:[1,1]\n" \
"v_pk_mul_f32 v[182:183], v[182:183], v[84:85] op_sel:[0,1] op_sel_hi:[1,1]\n" \
"v_cvt_pk_bf16_f32 v72, v176, v177\n" \
"v_cvt_pk_bf16_f32 v73, v178, v179\n" \
"v_cvt_pk_bf16_f32 v74, v180, v181\n" \
"v_cvt_pk_bf16_f32 v75, v182, v183\n" \
"buffer_store_dwordx4 v[72:75], v240, s[56:59], s72 offen offset:0 sc1\n" \
"v_pk_mul_f32 v[184:185], v[184:185], v[84:85] op_sel:[0,1] op_sel_hi:[1,1]\n" \
"v_pk_mul_f32 v[186:187], v[186:187], v[84:85] op_sel:[0,1] op_sel_hi:[1,1]\n" \
"v_pk_mul_f32 v[188:189], v[188:189], v[84:85] op_sel:[0,1] op_sel_hi:[1,1]\n" \
"v_pk_mul_f32 v[190:191], v[190:191], v[84:85] op_sel:[0,1] op_sel_hi:[1,1]\n" \
"v_cvt_pk_bf16_f32 v76, v184, v185\n" \
"v_cvt_pk_bf16_f32 v77, v186, v187\n" \
"v_cvt_pk_bf16_f32 v78, v188, v189\n" \
"v_cvt_pk_bf16_f32 v79, v190, v191\n" \
"buffer_store_dwordx4 v[76:79], v240, s[56:59], s72 offen offset:2048 sc1\n" \
"v_smfmac_f32_16x16x64_bf16 v[80:83], v[244:247], v[64:71], v243\n" \
"v_smfmac_f32_16x16x64_bf16 v[80:83], v[244:247], v[72:79], v243\n" \
"v_mfma_f32_32x32x16_bf16 v[160:175], v[88:91], v[88:91], 0\n" \
"v_mfma_f32_32x32x16_bf16 v[176:191], v[88:91], v[88:91], 0\n" \
"s_nop 5\n" \
"v_add_f32 v80, v80, v81\n" \
"s_movk_i32 s64, 0x207\n" \
"v_cmp_class_f32 vcc, v80, s64\n" \
"s_nop 1\n" \
"s_cmp_lg_u64 vcc, 0\n" \
"s_addc_u32 s90, s90, 0\n" \
"s_waitcnt vmcnt(8)\n" \
"s_cmp_eq_u32 s63, 7\n" \
"s_cbranch_scc0 Lepr_%=\n" \
"s_branch Lepg_%=\n" \
"Lloop2_%=:\n" \
"Lpg6189_%=:\n" \
"s_cbranch_execz Lpg7384_%=\n" \
"v_mfma_f32_16x16x32_bf16 v[0:3], a[192:195], a[0:3], v[196:199]\n" \
"v_exp_f32 v72, v72\n" \
"v_mfma_f32_16x16x32_bf16 v[0:3], a[196:199], a[4:7], v[0:3]\n" \
"ds_read_b128 a[44:47], v233 offset:15360\n" \
"v_mfma_f32_16x16x32_bf16 v[0:3], a[200:203], a[8:11], v[0:3]\n" \
"v_exp_f32 v73, v73\n" \
"v_mfma_f32_16x16x32_bf16 v[0:3], a[204:207], a[12:15], v[0:3]\n" \
"ds_read_b128 a[32:35], v233 offset:6144\n" \
"v_cvt_pk_bf16_f32 v68, v72, v73\n" \
"v_mfma_f32_16x16x32_bf16 v[16:19], a[204:207], a[28:31], v[196:199]\n" \
"Lpg6200_%=:\n" \
"s_cbranch_execz Lcb0_%=\n" \
"v_exp_f32 v74, v74\n" \
"v_mfma_f32_16x16x32_bf16 v[16:19], a[200:203], a[24:27], v[16:19]\n" \
"v_xor_b32 v232, 0x8000, v232\n" \
"v_mfma_f32_16x16x32_bf16 v[16:19], a[196:199], a[20:23], v[16:19]\n" \
"v_exp_f32 v75, v75\n" \
"v_mfma_f32_16x16x32_bf16 v[16:19], a[192:195], a[16:19], v[16:19]\n" \
"ds_read_b128 a[36:39], v233 offset:14336\n" \
"v_cvt_pk_bf16_f32 v69, v74, v75\n" \
"v_mfma_f32_16x16x32_bf16 v[32:35], a[192:195], a[160:163], v[196:199]\n" \
"v_exp_f32 v76, v76\n" \
"v_mfma_f32_16x16x32_bf16 v[32:35], a[196:199], a[164:167], v[32:35]\n" \
"v_mfma_f32_16x16x32_bf16 v[32:35], a[200:203], a[168:171], v[32:35]\n" \
"v_exp_f32 v77, v77\n" \
"v_mfma_f32_16x16x32_bf16 v[32:35], a[204:207], a[172:175], v[32:35]\n" \
"ds_read_b128 a[40:43], v233 offset:7168\n" \
"v_cvt_pk_bf16_f32 v70, v76, v77\n" \
"v_mfma_f32_16x16x32_bf16 v[48:51], a[204:207], a[188:191], v[196:199]\n" \
"v_exp_f32 v78, v78\n" \
"v_mfma_f32_16x16x32_bf16 v[48:51], a[200:203], a[184:187], v[48:51]\n" \
"v_mfma_f32_16x16x32_bf16 v[48:51], a[196:199], a[180:183], v[48:51]\n" \
"v_exp_f32 v79, v79\n" \
"v_mfma_f32_16x16x32_bf16 v[48:51], a[192:195], a[176:179], v[48:51]\n" \
"ds_read_b128 a[204:207], v233 offset:9216\n" \
"v_cvt_pk_bf16_f32 v71, v78, v79\n" \
"v_mfma_f32_16x16x32_bf16 v[52:55], a[208:211], a[176:179], v[196:199]\n" \
"v_exp_f32 v88, v88\n" \
"v_mfma_f32_16x16x32_bf16 v[52:55], a[212:215], a[180:183], v[52:55]\n" \
"v_mfma_f32_16x16x32_bf16 v[52:55], a[216:219], a[184:187], v[52:55]\n" \
"v_exp_f32 v89, v89\n" \
"v_mfma_f32_16x16x32_bf16 v[52:55], a[220:223], a[188:191], v[52:55]\n" \
"ds_read_b128 a[200:203], v233 offset:1024\n" \
"v_cvt_pk_bf16_f32 v84, v88, v89\n" \
"v_mfma_f32_16x16x32_bf16 v[36:39], a[220:223], a[172:175], v[196:199]\n" \
"v_exp_f32 v90, v90\n" \
"v_mfma_f32_16x16x32_bf16 v[36:39], a[216:219], a[168:171], v[36:39]\n" \
"v_mfma_f32_16x16x32_bf16 v[36:39], a[212:215], a[164:167], v[36:39]\n" \
"v_exp_f32 v91, v91\n" \
"v_mfma_f32_16x16x32_bf16 v[36:39], a[208:211], a[160:163], v[36:39]\n" \
"ds_read_b128 a[196:199], v233 offset:8192\n" \
"v_cvt_pk_bf16_f32 v85, v90, v91\n" \
"v_mfma_f32_16x16x32_bf16 v[20:23], a[208:211], a[16:19], v[196:199]\n" \
"v_exp_f32 v92, v92\n" \
"v_mfma_f32_16x16x32_bf16 v[20:23], a[212:215], a[20:23], v[20:23]\n" \
"v_mfma_f32_16x16x32_bf16 v[20:23], a[216:219], a[24:27], v[20:23]\n" \
"v_exp_f32 v93, v93\n" \
"v_mfma_f32_16x16x32_bf16 v[20:23], a[220:223], a[28:31], v[20:23]\n" \
"ds_read_b128 a[192:195], v233 offset:0\n" \
"v_cvt_pk_bf16_f32 v86, v92, v93\n" \
"v_mfma_f32_16x16x32_bf16 v[4:7], a[220:223], a[12:15], v[196:199]\n" \
"v_exp_f32 v94, v94\n" \
"v_mfma_f32_16x16x32_bf16 v[4:7], a[216:219], a[8:11], v[4:7]\n" \
"v_mfma_f32_16x16x32_bf16 v[4:7], a[212:215], a[4:7], v[4:7]\n" \
"s_waitcnt lgkmcnt(8)\n" \
"v_exp_f32 v95, v95\n" \
"v_mfma_f32_16x16x32_bf16 v[4:7], a[208:211], a[0:3], v[4:7]\n" \
"ds_read_b128 a[220:223], v233 offset:11264\n" \
"v_cvt_pk_bf16_f32 v87, v94, v95\n" \
"v_mfma_f32_16x16x32_bf16 v[8:11], a[224:227], a[0:3], v[196:199]\n" \
"v_exp_f32 v104, v104\n" \
"v_mfma_f32_16x16x32_bf16 v[8:11], a[228:231], a[4:7], v[8:11]\n" \
"v_mfma_f32_16x16x32_bf16 v[8:11], a[232:235], a[8:11], v[8:11]\n" \
"v_exp_f32 v105, v105\n" \
"v_mfma_f32_16x16x32_bf16 v[8:11], a[236:239], a[12:15], v[8:11]\n" \
"ds_read_b128 a[216:219], v233 offset:3072\n" \
"v_cvt_pk_bf16_f32 v100, v104, v105\n" \
"v_mfma_f32_16x16x32_bf16 v[24:27], a[236:239], a[28:31], v[196:199]\n" \
"v_exp_f32 v106, v106\n" \
"v_mfma_f32_16x16x32_bf16 v[24:27], a[232:235], a[24:27], v[24:27]\n" \
"v_mfma_f32_16x16x32_bf16 v[24:27], a[228:231], a[20:23], v[24:27]\n" \
"v_exp_f32 v107, v107\n" \
"v_mfma_f32_16x16x32_bf16 v[24:27], a[224:227], a[16:19], v[24:27]\n" \
"ds_read_b128 a[212:215], v233 offset:10240\n" \
"v_cvt_pk_bf16_f32 v101, v106, v107\n" \
"v_mfma_f32_16x16x32_bf16 v[40:43], a[224:227], a[160:163], v[196:199]\n" \
"v_exp_f32 v108, v108\n" \
"v_mfma_f32_16x16x32_bf16 v[40:43], a[228:231], a[164:167], v[40:43]\n" \
"v_mfma_f32_16x16x32_bf16 v[40:43], a[232:235], a[168:171], v[40:43]\n" \
"v_exp_f32 v109, v109\n" \
"v_mfma_f32_16x16x32_bf16 v[40:43], a[236:239], a[172:175], v[40:43]\n" \
"ds_read_b128 a[208:211], v233 offset:2048\n" \
"v_cvt_pk_bf16_f32 v102, v108, v109\n" \
"v_mfma_f32_16x16x32_bf16 v[56:59], a[236:239], a[188:191], v[196:199]\n" \
"v_exp_f32 v110, v110\n" \
"v_mfma_f32_16x16x32_bf16 v[56:59], a[232:235], a[184:187], v[56:59]\n" \
"v_mfma_f32_16x16x32_bf16 v[56:59], a[228:231], a[180:183], v[56:59]\n" \
"v_exp_f32 v111, v111\n" \
"v_mfma_f32_16x16x32_bf16 v[56:59], a[224:227], a[176:179], v[56:59]\n" \
"ds_read_b128 a[236:239], v233 offset:13312\n" \
"v_cvt_pk_bf16_f32 v103, v110, v111\n" \
"v_mfma_f32_16x16x32_bf16 v[60:63], a[240:243], a[176:179], v[196:199]\n" \
"v_exp_f32 v120, v120\n" \
"v_mfma_f32_16x16x32_bf16 v[60:63], a[244:247], a[180:183], v[60:63]\n" \
"ds_read_b128 a[232:235], v233 offset:5120\n" \
"v_mfma_f32_16x16x32_bf16 v[60:63], a[248:251], a[184:187], v[60:63]\n" \
"v_exp_f32 v121, v121\n" \
"v_mfma_f32_16x16x32_bf16 v[60:63], a[252:255], a[188:191], v[60:63]\n" \
"ds_read_b128 a[228:231], v233 offset:12288\n" \
"v_cvt_pk_bf16_f32 v116, v120, v121\n" \
"v_mfma_f32_16x16x32_bf16 v[44:47], a[252:255], a[172:175], v[196:199]\n" \
"v_exp_f32 v122, v122\n" \
"v_mfma_f32_16x16x32_bf16 v[44:47], a[248:251], a[168:171], v[44:47]\n" \
"ds_read_b128 a[224:227], v233 offset:4096\n" \
"v_mfma_f32_16x16x32_bf16 v[44:47], a[244:247], a[164:167], v[44:47]\n" \
"v_exp_f32 v123, v123\n" \
"v_mfma_f32_16x16x32_bf16 v[44:47], a[240:243], a[160:163], v[44:47]\n" \
"v_cvt_pk_bf16_f32 v117, v122, v123\n" \
"v_mfma_f32_16x16x32_bf16 v[28:31], a[240:243], a[16:19], v[196:199]\n" \
"v_exp_f32 v124, v124\n" \
"v_mfma_f32_16x16x32_bf16 v[28:31], a[244:247], a[20:23], v[28:31]\n" \
"v_mfma_f32_16x16x32_bf16 v[28:31], a[248:251], a[24:27], v[28:31]\n" \
"v_exp_f32 v125, v125\n" \
"v_mfma_f32_16x16x32_bf16 v[28:31], a[252:255], a[28:31], v[28:31]\n" \
"v_cvt_pk_bf16_f32 v118, v124, v125\n" \
"v_mfma_f32_16x16x32_bf16 v[12:15], a[252:255], a[12:15], v[196:199]\n" \
"v_exp_f32 v126, v126\n" \
"v_mfma_f32_16x16x32_bf16 v[12:15], a[248:251], a[8:11], v[12:15]\n" \
"v_mfma_f32_16x16x32_bf16 v[12:15], a[244:247], a[4:7], v[12:15]\n" \
"s_waitcnt vmcnt(0) lgkmcnt(0)\n" \
"v_exp_f32 v127, v127\n" \
"v_mfma_f32_16x16x32_bf16 v[12:15], a[240:243], a[0:3], v[12:15]\n" \
"s_barrier\n" \
"v_cvt_pk_bf16_f32 v119, v126, v127\n" \
"v_mfma_f32_16x16x32_bf16 v[128:131], a[192:195], v[64:67], v[128:131]\n" \
"v_exp_f32 v0, v0\n" \
"v_mfma_f32_16x16x32_bf16 v[128:131], a[196:199], v[68:71], v[128:131]\n" \
"v_cmp_eq_u32 vcc, s60, v239\n" \
"v_mfma_f32_16x16x32_bf16 v[160:163], a[192:195], v[80:83], v[160:163]\n" \
"v_exp_f32 v1, v1\n" \
"v_mfma_f32_16x16x32_bf16 v[160:163], a[196:199], v[84:87], v[160:163]\n" \
"s_andn2_b32 s70, s69, vcc_lo\n" \
"s_cbranch_scc1 Lpl3_%=\n" \
"Lpr3_%=:\n" \
"ds_read_b128 a[240:243], v232 offset:8448\n" \
"v_cvt_pk_bf16_f32 v0, v0, v1\n" \
"v_mfma_f32_16x16x32_bf16 a[64:67], a[192:195], v[96:99], a[64:67]\n" \
"s_add_u32 m0, s74, s79\n" \
"v_exp_f32 v2, v2\n" \
"v_mfma_f32_16x16x32_bf16 a[64:67], a[196:199], v[100:103], a[64:67]\n" \
"buffer_load_dwordx4 v234, s[36:39], s73 offen offset:0 sc1 lds\n" \
"v_mfma_f32_16x16x32_bf16 a[96:99], a[192:195], v[112:115], a[96:99]\n" \
"v_exp_f32 v3, v3\n" \
"v_mfma_f32_16x16x32_bf16 a[96:99], a[196:199], v[116:119], a[96:99]\n" \
"ds_read_b128 a[192:195], v232 offset:0\n" \
"v_cvt_pk_bf16_f32 v1, v2, v3\n" \
"v_mfma_f32_16x16x32_bf16 a[100:103], a[200:203], v[112:115], a[100:103]\n" \
"s_add_u32 s71, s73, 17408\n" \
"v_exp_f32 v4, v4\n" \
"v_mfma_f32_16x16x32_bf16 a[100:103], a[204:207], v[116:119], a[100:103]\n" \
"buffer_load_dwordx4 v234, s[36:39], s73 offen offset:1024 sc1 lds\n" \
"v_mfma_f32_16x16x32_bf16 a[68:71], a[200:203], v[96:99], a[68:71]\n" \
"v_exp_f32 v5, v5\n" \
"v_mfma_f32_16x16x32_bf16 a[68:71], a[204:207], v[100:103], a[68:71]\n" \
"ds_read_b128 a[196:199], v232 offset:2048\n" \
"v_cvt_pk_bf16_f32 v2, v4, v5\n" \
"v_smfmac_f32_16x16x64_bf16 a[48:51], v[244:247], v[64:71], v243\n" \
"buffer_load_dwordx4 v234, s[36:39], s73 offen offset:2048 sc1 lds\n" \
"v_mfma_f32_16x16x32_bf16 v[164:167], a[200:203], v[80:83], v[164:167]\n" \
"v_exp_f32 v6, v6\n" \
"v_mfma_f32_16x16x32_bf16 v[164:167], a[204:207], v[84:87], v[164:167]\n" \
"buffer_load_dwordx4 v234, s[36:39], s73 offen offset:3072 sc1 lds\n" \
"v_mfma_f32_16x16x32_bf16 v[132:135], a[200:203], v[64:67], v[132:135]\n" \
"v_exp_f32 v7, v7\n" \
"v_mfma_f32_16x16x32_bf16 v[132:135], a[204:207], v[68:71], v[132:135]\n" \
"ds_read_b128 a[200:203], v232 offset:4096\n" \
"v_cvt_pk_bf16_f32 v3, v6, v7\n" \
"v_mfma_f32_16x16x32_bf16 v[136:139], a[208:211], v[64:67], v[136:139]\n" \
"s_add_u32 m0, m0, 0x4000\n" \
"v_exp_f32 v16, v16\n" \
"v_mfma_f32_16x16x32_bf16 v[136:139], a[212:215], v[68:71], v[136:139]\n" \
"buffer_load_dwordx4 v234, s[36:39], s71 offen offset:0 sc1 lds\n" \
"v_mfma_f32_16x16x32_bf16 v[168:171], a[208:211], v[80:83], v[168:171]\n" \
"v_exp_f32 v17, v17\n" \
"v_mfma_f32_16x16x32_bf16 v[168:171], a[212:215], v[84:87], v[168:171]\n" \
"ds_read_b128 a[204:207], v232 offset:6144\n" \
"v_cvt_pk_bf16_f32 v16, v16, v17\n" \
"v_mfma_f32_16x16x32_bf16 a[72:75], a[208:211], v[96:99], a[72:75]\n" \
"v_exp_f32 v18, v18\n" \
"v_mfma_f32_16x16x32_bf16 a[72:75], a[212:215], v[100:103], a[72:75]\n" \
"buffer_load_dwordx4 v234, s[36:39], s71 offen offset:1024 sc1 lds\n" \
"v_mfma_f32_16x16x32_bf16 a[104:107], a[208:211], v[112:115], a[104:107]\n" \
"v_exp_f32 v19, v19\n" \
"v_mfma_f32_16x16x32_bf16 a[104:107], a[212:215], v[116:119], a[104:107]\n" \
"ds_read_b128 a[208:211], v232 offset:256\n" \
"v_cvt_pk_bf16_f32 v17, v18, v19\n" \
"v_mfma_f32_16x16x32_bf16 a[108:111], a[216:219], v[112:115], a[108:111]\n" \
"v_exp_f32 v20, v20\n" \
"v_mfma_f32_16x16x32_bf16 a[108:111], a[220:223], v[116:119], a[108:111]\n" \
"buffer_load_dwordx4 v234, s[36:39], s71 offen offset:2048 sc1 lds\n" \
"v_mfma_f32_16x16x32_bf16 a[76:79], a[216:219], v[96:99], a[76:79]\n" \
"v_exp_f32 v21, v21\n" \
"v_mfma_f32_16x16x32_bf16 a[76:79], a[220:223], v[100:103], a[76:79]\n" \
"ds_read_b128 a[212:215], v232 offset:2304\n" \
"v_cvt_pk_bf16_f32 v18, v20, v21\n" \
"v_smfmac_f32_16x16x64_bf16 a[52:55], v[244:247], v[80:87], v243\n" \
"buffer_load_dwordx4 v234, s[36:39], s71 offen offset:3072 sc1 lds\n" \
"v_mfma_f32_16x16x32_bf16 v[172:175], a[216:219], v[80:83], v[172:175]\n" \
"s_add_u32 m0, s77, s80\n" \
"v_exp_f32 v22, v22\n" \
"v_mfma_f32_16x16x32_bf16 v[172:175], a[220:223], v[84:87], v[172:175]\n" \
"buffer_load_dwordx4 v234, s[40:43], s76 offen offset:0 sc1 lds\n" \
"v_mfma_f32_16x16x32_bf16 v[140:143], a[216:219], v[64:67], v[140:143]\n" \
"v_exp_f32 v23, v23\n" \
"v_mfma_f32_16x16x32_bf16 v[140:143], a[220:223], v[68:71], v[140:143]\n" \
"ds_read_b128 a[216:219], v232 offset:4352\n" \
"v_cvt_pk_bf16_f32 v19, v22, v23\n" \
"v_mfma_f32_16x16x32_bf16 v[144:147], a[224:227], v[64:67], v[144:147]\n" \
"s_add_u32 s71, s76, 17408\n" \
"v_exp_f32 v32, v32\n" \
"v_mfma_f32_16x16x32_bf16 v[144:147], a[228:231], v[68:71], v[144:147]\n" \
"buffer_load_dwordx4 v234, s[40:43], s76 offen offset:1024 sc1 lds\n" \
"v_mfma_f32_16x16x32_bf16 v[176:179], a[224:227], v[80:83], v[176:179]\n" \
"v_exp_f32 v33, v33\n" \
"v_mfma_f32_16x16x32_bf16 v[176:179], a[228:231], v[84:87], v[176:179]\n" \
"ds_read_b128 a[220:223], v232 offset:6400\n" \
"v_cvt_pk_bf16_f32 v32, v32, v33\n" \
"v_mfma_f32_16x16x32_bf16 a[80:83], a[224:227], v[96:99], a[80:83]\n" \
"v_exp_f32 v34, v34\n" \
"v_mfma_f32_16x16x32_bf16 a[80:83], a[228:231], v[100:103], a[80:83]\n" \
"buffer_load_dwordx4 v234, s[40:43], s76 offen offset:2048 sc1 lds\n" \
"v_mfma_f32_16x16x32_bf16 a[112:115], a[224:227], v[112:115], a[112:115]\n" \
"v_exp_f32 v35, v35\n" \
"v_mfma_f32_16x16x32_bf16 a[112:115], a[228:231], v[116:119], a[112:115]\n" \
"ds_read_b128 a[224:227], v232 offset:8192\n" \
"v_cvt_pk_bf16_f32 v33, v34, v35\n" \
"v_mfma_f32_16x16x32_bf16 a[116:119], a[232:235], v[112:115], a[116:119]\n" \
"v_exp_f32 v36, v36\n" \
"v_mfma_f32_16x16x32_bf16 a[116:119], a[236:239], v[116:119], a[116:119]\n" \
"buffer_load_dwordx4 v234, s[40:43], s76 offen offset:3072 sc1 lds\n" \
"v_mfma_f32_16x16x32_bf16 a[84:87], a[232:235], v[96:99], a[84:87]\n" \
"v_exp_f32 v37, v37\n" \
"v_mfma_f32_16x16x32_bf16 a[84:87], a[236:239], v[100:103], a[84:87]\n" \
"s_add_u32 m0, m0, 0x4000\n" \
"ds_read_b128 a[228:231], v232 offset:10240\n" \
"v_cvt_pk_bf16_f32 v34, v36, v37\n" \
"v_smfmac_f32_16x16x64_bf16 v[224:227], v[244:247], v[96:103], v243\n" \
"buffer_load_dwordx4 v234, s[40:43], s71 offen offset:0 sc1 lds\n" \
"v_mfma_f32_16x16x32_bf16 v[180:183], a[232:235], v[80:83], v[180:183]\n" \
"v_exp_f32 v38, v38\n" \
"v_mfma_f32_16x16x32_bf16 v[180:183], a[236:239], v[84:87], v[180:183]\n" \
"buffer_load_dwordx4 v234, s[40:43], s71 offen offset:1024 sc1 lds\n" \
"v_mfma_f32_16x16x32_bf16 v[148:151], a[232:235], v[64:67], v[148:151]\n" \
"v_exp_f32 v39, v39\n" \
"v_mfma_f32_16x16x32_bf16 v[148:151], a[236:239], v[68:71], v[148:151]\n" \
"ds_read_b128 a[232:235], v232 offset:12288\n" \
"v_cvt_pk_bf16_f32 v35, v38, v39\n" \
"v_mfma_f32_16x16x32_bf16 v[152:155], a[32:35], v[64:67], v[152:155]\n" \
"v_exp_f32 v48, v48\n" \
"v_mfma_f32_16x16x32_bf16 v[152:155], a[36:39], v[68:71], v[152:155]\n" \
"buffer_load_dwordx4 v234, s[40:43], s71 offen offset:2048 sc1 lds\n" \
"v_mfma_f32_16x16x32_bf16 v[184:187], a[32:35], v[80:83], v[184:187]\n" \
"v_exp_f32 v49, v49\n" \
"v_mfma_f32_16x16x32_bf16 v[184:187], a[36:39], v[84:87], v[184:187]\n" \
"ds_read_b128 a[236:239], v232 offset:14336\n" \
"v_cvt_pk_bf16_f32 v48, v48, v49\n" \
"v_mfma_f32_16x16x32_bf16 a[88:91], a[32:35], v[96:99], a[88:91]\n" \
"v_exp_f32 v50, v50\n" \
"v_mfma_f32_16x16x32_bf16 a[88:91], a[36:39], v[100:103], a[88:91]\n" \
"buffer_load_dwordx4 v234, s[40:43], s71 offen offset:3072 sc1 lds\n" \
"v_mfma_f32_16x16x32_bf16 a[120:123], a[32:35], v[112:115], a[120:123]\n" \
"s_mov_b32 s76, s73\n" \
"v_exp_f32 v51, v51\n" \
"v_mfma_f32_16x16x32_bf16 a[120:123], a[36:39], v[116:119], a[120:123]\n" \
"s_add_u32 s73, s73, 34816\n" \
"ds_read_b128 a[244:247], v232 offset:10496\n" \
"v_cvt_pk_bf16_f32 v49, v50, v51\n" \
"v_mfma_f32_16x16x32_bf16 a[124:127], a[40:43], v[112:115], a[124:127]\n" \
"s_xor_b32 s74, s74, 0x8000\n" \
"v_exp_f32 v52, v52\n" \
"v_mfma_f32_16x16x32_bf16 a[124:127], a[44:47], v[116:119], a[124:127]\n" \
"s_xor_b32 s77, s77, 0x8000\n" \
"v_xor_b32 v233, s81, v233\n" \
"s_mov_b32 s81, 0x8000\n" \
"v_mfma_f32_16x16x32_bf16 a[92:95], a[40:43], v[96:99], a[92:95]\n" \
"v_exp_f32 v53, v53\n" \
"v_mfma_f32_16x16x32_bf16 a[92:95], a[44:47], v[100:103], a[92:95]\n" \
"ds_read_b128 a[248:251], v232 offset:12544\n" \
"v_cvt_pk_bf16_f32 v50, v52, v53\n" \
"v_smfmac_f32_16x16x64_bf16 v[228:231], v[244:247], v[112:119], v243\n" \
"v_mfma_f32_16x16x32_bf16 v[188:191], a[40:43], v[80:83], v[188:191]\n" \
"v_exp_f32 v54, v54\n" \
"v_mfma_f32_16x16x32_bf16 v[188:191], a[44:47], v[84:87], v[188:191]\n" \
"s_cmp_lg_u32 s82, 0\n" \
"v_exp_f32 v55, v55\n" \
"v_mfma_f32_16x16x32_bf16 v[156:159], a[40:43], v[64:67], v[156:159]\n" \
"s_waitcnt lgkmcnt(2)\n" \
"ds_read_b128 a[252:255], v232 offset:14592\n" \
"v_cvt_pk_bf16_f32 v51, v54, v55\n" \
"v_mfma_f32_16x16x32_bf16 v[156:159], a[44:47], v[68:71], v[156:159]\n" \
"s_cbranch_scc1 Lep_%=\n" \
"Lepr_%=:\n" \
"v_mfma_f32_16x16x32_bf16 v[64:67], a[192:195], a[0:3], v[196:199]\n" \
"v_exp_f32 v8, v8\n" \
"v_mfma_f32_16x16x32_bf16 v[64:67], a[196:199], a[4:7], v[64:67]\n" \
"ds_read_b128 a[44:47], v233 offset:15360\n" \
"v_mfma_f32_16x16x32_bf16 v[64:67], a[200:203], a[8:11], v[64:67]\n" \
"v_exp_f32 v9, v9\n" \
"v_mfma_f32_16x16x32_bf16 v[64:67], a[204:207], a[12:15], v[64:67]\n" \
"ds_read_b128 a[32:35], v233 offset:6144\n" \
"v_cvt_pk_bf16_f32 v4, v8, v9\n" \
"v_mfma_f32_16x16x32_bf16 v[80:83], a[204:207], a[28:31], v[196:199]\n" \
"s_sub_u32 s84, s84, 1\n" \
"v_exp_f32 v10, v10\n" \
"v_mfma_f32_16x16x32_bf16 v[80:83], a[200:203], a[24:27], v[80:83]\n" \
"s_cmp_eq_u32 s84, 0\n" \
"s_cselect_b32 s70, s75, 0x8000\n" \
"v_xor_b32 v232, s70, v232\n" \
"v_mfma_f32_16x16x32_bf16 v[80:83], a[196:199], a[20:23], v[80:83]\n" \
"v_exp_f32 v11, v11\n" \
"v_mfma_f32_16x16x32_bf16 v[80:83], a[192:195], a[16:19], v[80:83]\n" \
"ds_read_b128 a[36:39], v233 offset:14336\n" \
"v_cvt_pk_bf16_f32 v5, v10, v11\n" \
"v_mfma_f32_16x16x32_bf16 v[96:99], a[192:195], a[160:163], v[196:199]\n" \
"v_exp_f32 v12, v12\n" \
"v_mfma_f32_16x16x32_bf16 v[96:99], a[196:199], a[164:167], v[96:99]\n" \
"v_mfma_f32_16x16x32_bf16 v[96:99], a[200:203], a[168:171], v[96:99]\n" \
"v_exp_f32 v13, v13\n" \
"v_mfma_f32_16x16x32_bf16 v[96:99], a[204:207], a[172:175], v[96:99]\n" \
"ds_read_b128 a[40:43], v233 offset:7168\n" \
"v_cvt_pk_bf16_f32 v6, v12, v13\n" \
"v_mfma_f32_16x16x32_bf16 v[112:115], a[204:207], a[188:191], v[196:199]\n" \
"v_exp_f32 v14, v14\n" \
"v_mfma_f32_16x16x32_bf16 v[112:115], a[200:203], a[184:187], v[112:115]\n" \
"v_mfma_f32_16x16x32_bf16 v[112:115], a[196:199], a[180:183], v[112:115]\n" \
"v_exp_f32 v15, v15\n" \
"v_mfma_f32_16x16x32_bf16 v[112:115], a[192:195], a[176:179], v[112:115]\n" \
"ds_read_b128 a[204:207], v233 offset:9216\n" \
"v_cvt_pk_bf16_f32 v7, v14, v15\n" \
"v_mfma_f32_16x16x32_bf16 v[116:119], a[208:211], a[176:179], v[196:199]\n" \
"v_exp_f32 v24, v24\n" \
"v_mfma_f32_16x16x32_bf16 v[116:119], a[212:215], a[180:183], v[116:119]\n" \
"v_mfma_f32_16x16x32_bf16 v[116:119], a[216:219], a[184:187], v[116:119]\n" \
"v_exp_f32 v25, v25\n" \
"v_mfma_f32_16x16x32_bf16 v[116:119], a[220:223], a[188:191], v[116:119]\n" \
"ds_read_b128 a[200:203], v233 offset:1024\n" \
"v_cvt_pk_bf16_f32 v20, v24, v25\n" \
"v_mfma_f32_16x16x32_bf16 v[100:103], a[220:223], a[172:175], v[196:199]\n" \
"v_exp_f32 v26, v26\n" \
"v_mfma_f32_16x16x32_bf16 v[100:103], a[216:219], a[168:171], v[100:103]\n" \
"v_mfma_f32_16x16x32_bf16 v[100:103], a[212:215], a[164:167], v[100:103]\n" \
"v_exp_f32 v27, v27\n" \
"v_mfma_f32_16x16x32_bf16 v[100:103], a[208:211], a[160:163], v[100:103]\n" \
"ds_read_b128 a[196:199], v233 offset:8192\n" \
"v_cvt_pk_bf16_f32 v21, v26, v27\n" \
"v_mfma_f32_16x16x32_bf16 v[84:87], a[208:211], a[16:19], v[196:199]\n" \
"v_exp_f32 v28, v28\n" \
"v_mfma_f32_16x16x32_bf16 v[84:87], a[212:215], a[20:23], v[84:87]\n" \
"v_mfma_f32_16x16x32_bf16 v[84:87], a[216:219], a[24:27], v[84:87]\n" \
"v_exp_f32 v29, v29\n" \
"v_mfma_f32_16x16x32_bf16 v[84:87], a[220:223], a[28:31], v[84:87]\n" \
"ds_read_b128 a[192:195], v233 offset:0\n" \
"v_cvt_pk_bf16_f32 v22, v28, v29\n" \
"v_mfma_f32_16x16x32_bf16 v[68:71], a[220:223], a[12:15], v[196:199]\n" \
"v_exp_f32 v30, v30\n" \
"v_mfma_f32_16x16x32_bf16 v[68:71], a[216:219], a[8:11], v[68:71]\n" \
"v_mfma_f32_16x16x32_bf16 v[68:71], a[212:215], a[4:7], v[68:71]\n" \
"s_waitcnt lgkmcnt(8)\n" \
"v_exp_f32 v31, v31\n" \
"v_mfma_f32_16x16x32_bf16 v[68:71], a[208:211], a[0:3], v[68:71]\n" \
"ds_read_b128 a[220:223], v233 offset:11264\n" \
"v_cvt_pk_bf16_f32 v23, v30, v31\n" \
"v_mfma_f32_16x16x32_bf16 v[72:75], a[224:227], a[0:3], v[196:199]\n" \
"v_exp_f32 v40, v40\n" \
"v_mfma_f32_16x16x32_bf16 v[72:75], a[228:231], a[4:7], v[72:75]\n" \
"v_mfma_f32_16x16x32_bf16 v[72:75], a[232:235], a[8:11], v[72:75]\n" \
"v_exp_f32 v41, v41\n" \
"v_mfma_f32_16x16x32_bf16 v[72:75], a[236:239], a[12:15], v[72:75]\n" \
"ds_read_b128 a[216:219], v233 offset:3072\n" \
"v_cvt_pk_bf16_f32 v36, v40, v41\n" \
"v_mfma_f32_16x16x32_bf16 v[88:91], a[236:239], a[28:31], v[196:199]\n" \
"v_exp_f32 v42, v42\n" \
"v_mfma_f32_16x16x32_bf16 v[88:91], a[232:235], a[24:27], v[88:91]\n" \
"v_mfma_f32_16x16x32_bf16 v[88:91], a[228:231], a[20:23], v[88:91]\n" \
"v_exp_f32 v43, v43\n" \
"v_mfma_f32_16x16x32_bf16 v[88:91], a[224:227], a[16:19], v[88:91]\n" \
"ds_read_b128 a[212:215], v233 offset:10240\n" \
"v_cvt_pk_bf16_f32 v37, v42, v43\n" \
"v_mfma_f32_16x16x32_bf16 v[104:107], a[224:227], a[160:163], v[196:199]\n" \
"v_exp_f32 v44, v44\n" \
"v_mfma_f32_16x16x32_bf16 v[104:107], a[228:231], a[164:167], v[104:107]\n" \
"v_mfma_f32_16x16x32_bf16 v[104:107], a[232:235], a[168:171], v[104:107]\n" \
"v_exp_f32 v45, v45\n" \
"v_mfma_f32_16x16x32_bf16 v[104:107], a[236:239], a[172:175], v[104:107]\n" \
"ds_read_b128 a[208:211], v233 offset:2048\n" \
"v_cvt_pk_bf16_f32 v38, v44, v45\n" \
"v_mfma_f32_16x16x32_bf16 v[120:123], a[236:239], a[188:191], v[196:199]\n" \
"v_exp_f32 v46, v46\n" \
"v_mfma_f32_16x16x32_bf16 v[120:123], a[232:235], a[184:187], v[120:123]\n" \
"v_mfma_f32_16x16x32_bf16 v[120:123], a[228:231], a[180:183], v[120:123]\n" \
"v_exp_f32 v47, v47\n" \
"v_mfma_f32_16x16x32_bf16 v[120:123], a[224:227], a[176:179], v[120:123]\n" \
"ds_read_b128 a[236:239], v233 offset:13312\n" \
"v_cvt_pk_bf16_f32 v39, v46, v47\n" \
"v_mfma_f32_16x16x32_bf16 v[124:127], a[240:243], a[176:179], v[196:199]\n" \
"v_exp_f32 v56, v56\n" \
"v_mfma_f32_16x16x32_bf16 v[124:127], a[244:247], a[180:183], v[124:127]\n" \
"ds_read_b128 a[232:235], v233 offset:5120\n" \
"v_mfma_f32_16x16x32_bf16 v[124:127], a[248:251], a[184:187], v[124:127]\n" \
"v_exp_f32 v57, v57\n" \
"v_mfma_f32_16x16x32_bf16 v[124:127], a[252:255], a[188:191], v[124:127]\n" \
"ds_read_b128 a[228:231], v233 offset:12288\n" \
"v_cvt_pk_bf16_f32 v52, v56, v57\n" \
"v_mfma_f32_16x16x32_bf16 v[108:111], a[252:255], a[172:175], v[196:199]\n" \
"v_exp_f32 v58, v58\n" \
"v_mfma_f32_16x16x32_bf16 v[108:111], a[248:251], a[168:171], v[108:111]\n" \
"ds_read_b128 a[224:227], v233 offset:4096\n" \
"v_mfma_f32_16x16x32_bf16 v[108:111], a[244:247], a[164:167], v[108:111]\n" \
"v_exp_f32 v59, v59\n" \
"v_mfma_f32_16x16x32_bf16 v[108:111], a[240:243], a[160:163], v[108:111]\n" \
"v_cvt_pk_bf16_f32 v53, v58, v59\n" \
"v_mfma_f32_16x16x32_bf16 v[92:95], a[240:243], a[16:19], v[196:199]\n" \
"v_exp_f32 v60, v60\n" \
"v_mfma_f32_16x16x32_bf16 v[92:95], a[244:247], a[20:23], v[92:95]\n" \
"v_mfma_f32_16x16x32_bf16 v[92:95], a[248:251], a[24:27], v[92:95]\n" \
"v_exp_f32 v61, v61\n" \
"v_mfma_f32_16x16x32_bf16 v[92:95], a[252:255], a[28:31], v[92:95]\n" \
"v_cvt_pk_bf16_f32 v54, v60, v61\n" \
"v_mfma_f32_16x16x32_bf16 v[76:79], a[252:255], a[12:15], v[196:199]\n" \
"v_exp_f32 v62, v62\n" \
"v_mfma_f32_16x16x32_bf16 v[76:79], a[248:251], a[8:11], v[76:79]\n" \
"s_cmp_lg_u32 s82, 0\n" \
"s_cbranch_scc1 Lnw_%=\n" \
"s_waitcnt vmcnt(0)\n" \
"Lnw_%=:\n" \
"v_mfma_f32_16x16x32_bf16 v[76:79], a[244:247], a[4:7], v[76:79]\n" \
"s_waitcnt lgkmcnt(0)\n" \
"v_exp_f32 v63, v63\n" \
"v_mfma_f32_16x16x32_bf16 v[76:79], a[240:243], a[0:3], v[76:79]\n" \
"s_barrier\n" \
"v_cvt_pk_bf16_f32 v55, v62, v63\n" \
"v_mfma_f32_16x16x32_bf16 v[128:131], a[192:195], v[0:3], v[128:131]\n" \
"s_add_u32 m0, s74, s79\n" \
"v_exp_f32 v64, v64\n" \
"v_mfma_f32_16x16x32_bf16 v[128:131], a[196:199], v[4:7], v[128:131]\n" \
"buffer_load_dwordx4 v234, s[36:39], s73 offen offset:0 sc1 lds\n" \
"v_mfma_f32_16x16x32_bf16 v[160:163], a[192:195], v[16:19], v[160:163]\n" \
"v_exp_f32 v65, v65\n" \
"v_mfma_f32_16x16x32_bf16 v[160:163], a[196:199], v[20:23], v[160:163]\n" \
"ds_read_b128 a[240:243], v232 offset:8448\n" \
"v_cvt_pk_bf16_f32 v64, v64, v65\n" \
"v_mfma_f32_16x16x32_bf16 a[64:67], a[192:195], v[32:35], a[64:67]\n" \
"s_add_u32 s71, s73, 17408\n" \
"v_exp_f32 v66, v66\n" \
"v_mfma_f32_16x16x32_bf16 a[64:67], a[196:199], v[36:39], a[64:67]\n" \
"buffer_load_dwordx4 v234, s[36:39], s73 offen offset:1024 sc1 lds\n" \
"v_mfma_f32_16x16x32_bf16 a[96:99], a[192:195], v[48:51], a[96:99]\n" \
"v_exp_f32 v67, v67\n" \
"v_mfma_f32_16x16x32_bf16 a[96:99], a[196:199], v[52:55], a[96:99]\n" \
"ds_read_b128 a[192:195], v232 offset:0\n" \
"v_cvt_pk_bf16_f32 v65, v66, v67\n" \
"v_mfma_f32_16x16x32_bf16 a[100:103], a[200:203], v[48:51], a[100:103]\n" \
"v_exp_f32 v68, v68\n" \
"v_mfma_f32_16x16x32_bf16 a[100:103], a[204:207], v[52:55], a[100:103]\n" \
"buffer_load_dwordx4 v234, s[36:39], s73 offen offset:2048 sc1 lds\n" \
"v_mfma_f32_16x16x32_bf16 a[68:71], a[200:203], v[32:35], a[68:71]\n" \
"v_exp_f32 v69, v69\n" \
"v_mfma_f32_16x16x32_bf16 a[68:71], a[204:207], v[36:39], a[68:71]\n" \
"ds_read_b128 a[196:199], v232 offset:2048\n" \
"v_cvt_pk_bf16_f32 v66, v68, v69\n" \
"v_smfmac_f32_16x16x64_bf16 a[48:51], v[244:247], v[0:7], v243\n" \
"buffer_load_dwordx4 v234, s[36:39], s73 offen offset:3072 sc1 lds\n" \
"v_mfma_f32_16x16x32_bf16 v[164:167], a[200:203], v[16:19], v[164:167]\n" \
"s_add_u32 m0, m0, 0x4000\n" \
"v_exp_f32 v70, v70\n" \
"v_mfma_f32_16x16x32_bf16 v[164:167], a[204:207], v[20:23], v[164:167]\n" \
"buffer_load_dwordx4 v234, s[36:39], s71 offen offset:0 sc1 lds\n" \
"v_mfma_f32_16x16x32_bf16 v[132:135], a[200:203], v[0:3], v[132:135]\n" \
"v_exp_f32 v71, v71\n" \
"v_mfma_f32_16x16x32_bf16 v[132:135], a[204:207], v[4:7], v[132:135]\n" \
"ds_read_b128 a[200:203], v232 offset:4096\n" \
"v_cvt_pk_bf16_f32 v67, v70, v71\n" \
"v_mfma_f32_16x16x32_bf16 v[136:139], a[208:211], v[0:3], v[136:139]\n" \
"v_exp_f32 v80, v80\n" \
"v_mfma_f32_16x16x32_bf16 v[136:139], a[212:215], v[4:7], v[136:139]\n" \
"buffer_load_dwordx4 v234, s[36:39], s71 offen offset:1024 sc1 lds\n" \
"v_mfma_f32_16x16x32_bf16 v[168:171], a[208:211], v[16:19], v[168:171]\n" \
"v_exp_f32 v81, v81\n" \
"v_mfma_f32_16x16x32_bf16 v[168:171], a[212:215], v[20:23], v[168:171]\n" \
"ds_read_b128 a[204:207], v232 offset:6144\n" \
"v_cvt_pk_bf16_f32 v80, v80, v81\n" \
"v_mfma_f32_16x16x32_bf16 a[72:75], a[208:211], v[32:35], a[72:75]\n" \
"v_exp_f32 v82, v82\n" \
"v_mfma_f32_16x16x32_bf16 a[72:75], a[212:215], v[36:39], a[72:75]\n" \
"buffer_load_dwordx4 v234, s[36:39], s71 offen offset:2048 sc1 lds\n" \
"v_mfma_f32_16x16x32_bf16 a[104:107], a[208:211], v[48:51], a[104:107]\n" \
"v_exp_f32 v83, v83\n" \
"v_mfma_f32_16x16x32_bf16 a[104:107], a[212:215], v[52:55], a[104:107]\n" \
"ds_read_b128 a[208:211], v232 offset:256\n" \
"v_cvt_pk_bf16_f32 v81, v82, v83\n" \
"v_mfma_f32_16x16x32_bf16 a[108:111], a[216:219], v[48:51], a[108:111]\n" \
"v_exp_f32 v84, v84\n" \
"v_mfma_f32_16x16x32_bf16 a[108:111], a[220:223], v[52:55], a[108:111]\n" \
"buffer_load_dwordx4 v234, s[36:39], s71 offen offset:3072 sc1 lds\n" \
"v_mfma_f32_16x16x32_bf16 a[76:79], a[216:219], v[32:35], a[76:79]\n" \
"v_exp_f32 v85, v85\n" \
"v_mfma_f32_16x16x32_bf16 a[76:79], a[220:223], v[36:39], a[76:79]\n" \
"s_add_u32 m0, s77, s80\n" \
"ds_read_b128 a[212:215], v232 offset:2304\n" \
"v_cvt_pk_bf16_f32 v82, v84, v85\n" \
"v_smfmac_f32_16x16x64_bf16 a[52:55], v[244:247], v[16:23], v243\n" \
"buffer_load_dwordx4 v234, s[40:43], s76 offen offset:0 sc1 lds\n" \
"v_mfma_f32_16x16x32_bf16 v[172:175], a[216:219], v[16:19], v[172:175]\n" \
"s_add_u32 s71, s76, 17408\n" \
"v_exp_f32 v86, v86\n" \
"v_mfma_f32_16x16x32_bf16 v[172:175], a[220:223], v[20:23], v[172:175]\n" \
"buffer_load_dwordx4 v234, s[40:43], s76 offen offset:1024 sc1 lds\n" \
"v_mfma_f32_16x16x32_bf16 v[140:143], a[216:219], v[0:3], v[140:143]\n" \
"v_exp_f32 v87, v87\n" \
"v_mfma_f32_16x16x32_bf16 v[140:143], a[220:223], v[4:7], v[140:143]\n" \
"ds_read_b128 a[216:219], v232 offset:4352\n" \
"v_cvt_pk_bf16_f32 v83, v86, v87\n" \
"v_mfma_f32_16x16x32_bf16 v[144:147], a[224:227], v[0:3], v[144:147]\n" \
"v_exp_f32 v96, v96\n" \
"v_mfma_f32_16x16x32_bf16 v[144:147], a[228:231], v[4:7], v[144:147]\n" \
"buffer_load_dwordx4 v234, s[40:43], s76 offen offset:2048 sc1 lds\n" \
"v_mfma_f32_16x16x32_bf16 v[176:179], a[224:227], v[16:19], v[176:179]\n" \
"v_exp_f32 v97, v97\n" \
"v_mfma_f32_16x16x32_bf16 v[176:179], a[228:231], v[20:23], v[176:179]\n" \
"ds_read_b128 a[220:223], v232 offset:6400\n" \
"v_cvt_pk_bf16_f32 v96, v96, v97\n" \
"v_mfma_f32_16x16x32_bf16 a[80:83], a[224:227], v[32:35], a[80:83]\n" \
"v_exp_f32 v98, v98\n" \
"v_mfma_f32_16x16x32_bf16 a[80:83], a[228:231], v[36:39], a[80:83]\n" \
"buffer_load_dwordx4 v234, s[40:43], s76 offen offset:3072 sc1 lds\n" \
"v_mfma_f32_16x16x32_bf16 a[112:115], a[224:227], v[48:51], a[112:115]\n" \
"v_exp_f32 v99, v99\n" \
"v_mfma_f32_16x16x32_bf16 a[112:115], a[228:231], v[52:55], a[112:115]\n" \
"ds_read_b128 a[224:227], v232 offset:8192\n" \
"v_cvt_pk_bf16_f32 v97, v98, v99\n" \
"v_mfma_f32_16x16x32_bf16 a[116:119], a[232:235], v[48:51], a[116:119]\n" \
"s_add_u32 m0, m0, 0x4000\n" \
"v_exp_f32 v100, v100\n" \
"v_mfma_f32_16x16x32_bf16 a[116:119], a[236:239], v[52:55], a[116:119]\n" \
"buffer_load_dwordx4 v234, s[40:43], s71 offen offset:0 sc1 lds\n" \
"v_mfma_f32_16x16x32_bf16 a[84:87], a[232:235], v[32:35], a[84:87]\n" \
"v_exp_f32 v101, v101\n" \
"v_mfma_f32_16x16x32_bf16 a[84:87], a[236:239], v[36:39], a[84:87]\n" \
"ds_read_b128 a[228:231], v232 offset:10240\n" \
"v_cvt_pk_bf16_f32 v98, v100, v101\n" \
"v_smfmac_f32_16x16x64_bf16 v[224:227], v[244:247], v[32:39], v243\n" \
"buffer_load_dwordx4 v234, s[40:43], s71 offen offset:1024 sc1 lds\n" \
"v_mfma_f32_16x16x32_bf16 v[180:183], a[232:235], v[16:19], v[180:183]\n" \
"v_exp_f32 v102, v102\n" \
"v_mfma_f32_16x16x32_bf16 v[180:183], a[236:239], v[20:23], v[180:183]\n" \
"buffer_load_dwordx4 v234, s[40:43], s71 offen offset:2048 sc1 lds\n" \
"v_mfma_f32_16x16x32_bf16 v[148:151], a[232:235], v[0:3], v[148:151]\n" \
"v_exp_f32 v103, v103\n" \
"v_mfma_f32_16x16x32_bf16 v[148:151], a[236:239], v[4:7], v[148:151]\n" \
"ds_read_b128 a[232:235], v232 offset:12288\n" \
"v_cvt_pk_bf16_f32 v99, v102, v103\n" \
"v_mfma_f32_16x16x32_bf16 v[152:155], a[32:35], v[0:3], v[152:155]\n" \
"v_exp_f32 v112, v112\n" \
"v_mfma_f32_16x16x32_bf16 v[152:155], a[36:39], v[4:7], v[152:155]\n" \
"buffer_load_dwordx4 v234, s[40:43], s71 offen offset:3072 sc1 lds\n" \
"v_mfma_f32_16x16x32_bf16 v[184:187], a[32:35], v[16:19], v[184:187]\n" \
"v_exp_f32 v113, v113\n" \
"v_mfma_f32_16x16x32_bf16 v[184:187], a[36:39], v[20:23], v[184:187]\n" \
"ds_read_b128 a[236:239], v232 offset:14336\n" \
"v_cvt_pk_bf16_f32 v112, v112, v113\n" \
"v_mfma_f32_16x16x32_bf16 a[88:91], a[32:35], v[32:35], a[88:91]\n" \
"v_exp_f32 v114, v114\n" \
"v_mfma_f32_16x16x32_bf16 a[88:91], a[36:39], v[36:39], a[88:91]\n" \
"buffer_load_dword v239, v242, s[56:59], s95 offen sc1\n" \
"v_mfma_f32_16x16x32_bf16 a[120:123], a[32:35], v[48:51], a[120:123]\n" \
"s_mov_b32 s69, s92\n" \
"v_exp_f32 v115, v115\n" \
"v_mfma_f32_16x16x32_bf16 a[120:123], a[36:39], v[52:55], a[120:123]\n" \
"s_mov_b32 s76, s73\n" \
"ds_read_b128 a[244:247], v232 offset:10496\n" \
"v_cvt_pk_bf16_f32 v113, v114, v115\n" \
"v_mfma_f32_16x16x32_bf16 a[124:127], a[40:43], v[48:51], a[124:127]\n" \
"s_add_u32 s73, s73, 34816\n" \
"v_exp_f32 v116, v116\n" \
"v_mfma_f32_16x16x32_bf16 a[124:127], a[44:47], v[52:55], a[124:127]\n" \
"s_xor_b32 s74, s74, 0x8000\n" \
"s_xor_b32 s77, s77, 0x8000\n" \
"v_xor_b32 v233, 0x8000, v233\n" \
"v_mfma_f32_16x16x32_bf16 a[92:95], a[40:43], v[32:35], a[92:95]\n" \
"s_mov_b32 s82, 0\n" \
"v_exp_f32 v117, v117\n" \
"v_mfma_f32_16x16x32_bf16 a[92:95], a[44:47], v[36:39], a[92:95]\n" \
"ds_read_b128 a[248:251], v232 offset:12544\n" \
"v_cvt_pk_bf16_f32 v114, v116, v117\n" \
"v_smfmac_f32_16x16x64_bf16 v[228:231], v[244:247], v[48:55], v243\n" \
"v_mfma_f32_16x16x32_bf16 v[188:191], a[40:43], v[16:19], v[188:191]\n" \
"v_exp_f32 v118, v118\n" \
"v_mfma_f32_16x16x32_bf16 v[188:191], a[44:47], v[20:23], v[188:191]\n" \
"s_cmp_lg_u32 s84, 0\n" \
"v_exp_f32 v119, v119\n" \
"v_mfma_f32_16x16x32_bf16 v[156:159], a[40:43], v[0:3], v[156:159]\n" \
"s_waitcnt lgkmcnt(2)\n" \
"ds_read_b128 a[252:255], v232 offset:14592\n" \
"v_cvt_pk_bf16_f32 v115, v118, v119\n" \
"Lpg6793_%=:\n" \
"s_cbranch_execz Lcb0_%=\n" \
"v_mfma_f32_16x16x32_bf16 v[156:159], a[44:47], v[4:7], v[156:159]\n" \
"s_cbranch_scc1 Lloop2_%=\n" \
"Lg_%=:\n" \
"Lpg6797_%=:\n" \
"s_cbranch_execz Lcb0_%=\n" \
"v_mfma_f32_16x16x32_bf16 v[32:35], a[192:195], a[160:163], v[208:211]\n" \
"s_mov_b64 s[44:45], s[20:21]\n" \
"s_memrealtime s[14:15]\n" \
"v_exp_f32 v72, v72\n" \
"v_mfma_f32_16x16x32_bf16 v[32:35], a[196:199], a[164:167], v[32:35]\n" \
"s_and_b32 s45, s45, 0xffff\n" \
"ds_read_b128 a[32:35], v233 offset:6144\n" \
"ds_read_b128 a[36:39], v233 offset:14336\n" \
"v_mfma_f32_16x16x32_bf16 v[32:35], a[200:203], a[168:171], v[32:35]\n" \
"s_mov_b32 s46, 4194304\n" \
"v_exp_f32 v73, v73\n" \
"v_mfma_f32_16x16x32_bf16 v[32:35], a[204:207], a[172:175], v[32:35]\n" \
"ds_read_b128 a[40:43], v233 offset:7168\n" \
"ds_read_b128 a[44:47], v233 offset:15360\n" \
"global_load_dword v253, v254, s[22:23]\n" \
"v_mfma_f32_16x16x32_bf16 v[48:51], a[204:207], a[188:191], v[196:199]\n" \
"s_mov_b32 s47, 0x20000\n" \
"v_exp_f32 v74, v74\n" \
"v_mfma_f32_16x16x32_bf16 v[48:51], a[200:203], a[184:187], v[48:51]\n" \
"s_mov_b64 s[48:49], s[22:23]\n" \
"v_exp_f32 v75, v75\n" \
"v_mfma_f32_16x16x32_bf16 v[48:51], a[196:199], a[180:183], v[48:51]\n" \
"s_and_b32 s49, s49, 0xffff\n" \
"v_exp_f32 v76, v76\n" \
"v_mfma_f32_16x16x32_bf16 v[48:51], a[192:195], a[176:179], v[48:51]\n" \
"s_mov_b32 s50, 6291456\n" \
"ds_read_b128 a[200:203], v233 offset:1024\n" \
"ds_read_b128 a[204:207], v233 offset:9216\n" \
"v_mfma_f32_16x16x32_bf16 v[52:55], a[208:211], a[176:179], v[196:199]\n" \
"s_mov_b32 s51, 0x20000\n" \
"v_cvt_pk_bf16_f32 v68, v72, v73\n" \
"v_cvt_pk_bf16_f32 v69, v74, v75\n" \
"v_mfma_f32_16x16x32_bf16 v[52:55], a[212:215], a[180:183], v[52:55]\n" \
"s_bfe_u32 s94, %1, 0x20001\n" \
"v_exp_f32 v77, v77\n" \
"v_mfma_f32_16x16x32_bf16 v[52:55], a[216:219], a[184:187], v[52:55]\n" \
"s_lshl_b32 s94, s94, 2\n" \
"v_exp_f32 v78, v78\n" \
"v_mfma_f32_16x16x32_bf16 v[52:55], a[220:223], a[188:191], v[52:55]\n" \
"s_lshr_b32 s70, %1, 6\n" \
"ds_read_b128 a[192:195], v233 offset:0\n" \
"ds_read_b128 a[196:199], v233 offset:8192\n" \
"v_mfma_f32_16x16x32_bf16 v[36:39], a[220:223], a[172:175], v[212:215]\n" \
"s_add_u32 s94, s94, s70\n" \
"v_exp_f32 v79, v79\n" \
"v_mfma_f32_16x16x32_bf16 v[36:39], a[216:219], a[168:171], v[36:39]\n" \
"s_lshr_b32 s71, s22, 7\n" \
"v_exp_f32 v88, v88\n" \
"v_mfma_f32_16x16x32_bf16 v[36:39], a[212:215], a[164:167], v[36:39]\n" \
"s_sub_u32 s71, s63, s71\n" \
"v_cvt_pk_bf16_f32 v70, v76, v77\n" \
"v_cvt_pk_bf16_f32 v71, v78, v79\n" \
"v_mfma_f32_16x16x32_bf16 v[36:39], a[208:211], a[160:163], v[36:39]\n" \
"s_add_u32 s71, s71, 6\n" \
"ds_read_b128 a[216:219], v233 offset:3072\n" \
"ds_read_b128 a[220:223], v233 offset:11264\n" \
"v_mfma_f32_16x16x32_bf16 v[40:43], a[224:227], a[160:163], v[216:219]\n" \
"s_and_b32 s71, s71, 7\n" \
"v_exp_f32 v89, v89\n" \
"v_mfma_f32_16x16x32_bf16 v[40:43], a[228:231], a[164:167], v[40:43]\n" \
"s_and_b32 s70, %1, 1\n" \
"v_exp_f32 v90, v90\n" \
"v_mfma_f32_16x16x32_bf16 v[40:43], a[232:235], a[168:171], v[40:43]\n" \
"s_lshl_b32 s70, s70, 3\n" \
"v_exp_f32 v91, v91\n" \
"v_mfma_f32_16x16x32_bf16 v[40:43], a[236:239], a[172:175], v[40:43]\n" \
"s_add_u32 s70, s70, s71\n" \
"ds_read_b128 a[208:211], v233 offset:2048\n" \
"ds_read_b128 a[212:215], v233 offset:10240\n" \
"v_mfma_f32_16x16x32_bf16 v[56:59], a[236:239], a[188:191], v[196:199]\n" \
"s_lshl_b32 s78, s70, 7\n" \
"v_exp_f32 v92, v92\n" \
"v_mfma_f32_16x16x32_bf16 v[56:59], a[232:235], a[184:187], v[56:59]\n" \
"s_lshr_b32 s80, %1, 6\n" \
"v_cvt_pk_bf16_f32 v84, v88, v89\n" \
"v_cvt_pk_bf16_f32 v85, v90, v91\n" \
"v_mfma_f32_16x16x32_bf16 v[56:59], a[228:231], a[180:183], v[56:59]\n" \
"s_xor_b32 s80, s80, s63\n" \
"v_exp_f32 v93, v93\n" \
"v_mfma_f32_16x16x32_bf16 v[56:59], a[224:227], a[176:179], v[56:59]\n" \
"s_and_b32 s80, s80, 0\n" \
"ds_read_b128 a[232:235], v233 offset:5120\n" \
"ds_read_b128 a[236:239], v233 offset:13312\n" \
"v_mfma_f32_16x16x32_bf16 v[60:63], a[240:243], a[176:179], v[196:199]\n" \
"s_waitcnt lgkmcnt(2)\n" \
"s_lshl_b32 s93, s70, 12\n" \
"v_exp_f32 v94, v94\n" \
"v_mfma_f32_16x16x32_bf16 v[60:63], a[244:247], a[180:183], v[60:63]\n" \
"s_mul_i32 s91, s61, 6144\n" \
"v_exp_f32 v95, v95\n" \
"v_mfma_f32_16x16x32_bf16 v[60:63], a[248:251], a[184:187], v[60:63]\n" \
"s_add_u32 s91, s91, %3\n" \
"v_cvt_pk_bf16_f32 v86, v92, v93\n" \
"v_mfma_f32_16x16x32_bf16 v[60:63], a[252:255], a[188:191], v[60:63]\n" \
"s_lshl_b32 s71, s80, 7\n" \
"ds_read_b128 a[224:227], v233 offset:4096\n" \
"ds_read_b128 a[228:231], v233 offset:12288\n" \
"v_mfma_f32_16x16x32_bf16 v[44:47], a[252:255], a[172:175], v[220:223]\n" \
"s_lshl_b32 s75, s94, 18\n" \
"v_cvt_pk_bf16_f32 v87, v94, v95\n" \
"v_mfma_f32_16x16x32_bf16 v[44:47], a[248:251], a[168:171], v[44:47]\n" \
"s_lshl_b32 s70, s61, 16\n" \
"v_exp_f32 v104, v104\n" \
"v_mfma_f32_16x16x32_bf16 v[44:47], a[244:247], a[164:167], v[44:47]\n" \
"s_add_u32 s73, s75, s70\n" \
"v_accvgpr_read_b32 v197, a56\n" \
"v_accvgpr_read_b32 v192, a57\n" \
"v_mfma_f32_16x16x32_bf16 v[44:47], a[240:243], a[160:163], v[44:47]\n" \
"s_add_u32 s73, s73, s71\n" \
"v_accvgpr_read_b32 v196, a58\n" \
"v_add_u32 v198, 1024, v197\n" \
"v_mfma_f32_16x16x32_bf16 v[128:131], a[192:195], v[64:67], v[128:131]\n" \
"s_max_u32 s75, s75, 32768\n" \
"v_add_u32 v199, 2048, v197\n" \
"v_add_u32 v193, 31744, v192\n" \
"v_mfma_f32_16x16x32_bf16 v[128:131], a[196:199], v[68:71], v[128:131]\n" \
"s_sub_u32 s75, s75, 32768\n" \
"ds_read_b128 a[0:3], v232 offset:16384\n" \
"ds_read_b128 a[4:7], v232 offset:18432\n" \
"v_mfma_f32_16x16x32_bf16 v[160:163], a[192:195], v[80:83], v[160:163]\n" \
"s_add_u32 s75, s75, s71\n" \
"s_lshl_b32 s89, s61, 10\n" \
"v_mfma_f32_16x16x32_bf16 v[160:163], a[196:199], v[84:87], v[160:163]\n" \
"s_mul_i32 s76, s80, 393216\n" \
"s_add_u32 s89, s89, %3\n" \
"v_exp_f32 v105, v105\n" \
"v_mfma_f32_16x16x32_bf16 v[164:167], a[200:203], v[80:83], v[164:167]\n" \
"s_add_u32 s76, s76, s78\n" \
"ds_read_b128 a[8:11], v232 offset:20480\n" \
"ds_read_b128 a[12:15], v232 offset:22528\n" \
"v_mfma_f32_16x16x32_bf16 v[164:167], a[204:207], v[84:87], v[164:167]\n" \
"s_add_u32 m0, s91, 139264\n" \
"s_add_u32 s89, s89, 16384\n" \
"v_exp_f32 v106, v106\n" \
"v_mfma_f32_16x16x32_bf16 v[132:135], a[200:203], v[64:67], v[132:135]\n" \
"buffer_load_dwordx4 v197, s[48:51], s76 offen offset:0 lds\n" \
"v_mfma_f32_16x16x32_bf16 v[132:135], a[204:207], v[68:71], v[132:135]\n" \
"s_and_b32 s70, s61, 1\n" \
"ds_read_b128 a[16:19], v232 offset:16640\n" \
"ds_read_b128 a[20:23], v232 offset:18688\n" \
"v_smfmac_f32_16x16x64_bf16 a[48:51], v[244:247], v[64:71], v243\n" \
"s_add_u32 s77, s76, 24576\n" \
"v_cvt_pk_bf16_f32 v100, v104, v105\n" \
"v_mfma_f32_16x16x32_bf16 v[136:139], a[208:211], v[64:67], v[136:139]\n" \
"buffer_load_dwordx4 v198, s[48:51], s76 offen offset:1024 lds\n" \
"v_mfma_f32_16x16x32_bf16 v[136:139], a[212:215], v[68:71], v[136:139]\n" \
"s_lshl_b32 s70, s70, 11\n" \
"ds_read_b128 a[24:27], v232 offset:20736\n" \
"ds_read_b128 a[28:31], v232 offset:22784\n" \
"v_mfma_f32_16x16x32_bf16 v[168:171], a[208:211], v[80:83], v[168:171]\n" \
"s_xor_b32 s80, s80, 0x3\n" \
"v_exp_f32 v107, v107\n" \
"v_mfma_f32_16x16x32_bf16 v[168:171], a[212:215], v[84:87], v[168:171]\n" \
"buffer_load_dwordx4 v199, s[48:51], s76 offen offset:2048 lds\n" \
"v_mfma_f32_16x16x32_bf16 v[172:175], a[216:219], v[80:83], v[172:175]\n" \
"s_add_u32 s93, s93, s70\n" \
"ds_read_b128 v[0:3], v232 offset:24576\n" \
"ds_read_b128 v[4:7], v232 offset:26624\n" \
"v_mfma_f32_16x16x32_bf16 v[172:175], a[220:223], v[84:87], v[172:175]\n" \
"s_add_u32 m0, m0, 3072\n" \
"v_exp_f32 v108, v108\n" \
"v_mfma_f32_16x16x32_bf16 v[140:143], a[216:219], v[64:67], v[140:143]\n" \
"buffer_load_dwordx4 v197, s[48:51], s77 offen offset:0 lds\n" \
"v_mfma_f32_16x16x32_bf16 v[140:143], a[220:223], v[68:71], v[140:143]\n" \
"s_add_u32 s93, s93, 52740096\n" \
"ds_read_b128 v[8:11], v232 offset:28672\n" \
"ds_read_b128 v[12:15], v232 offset:30720\n" \
"v_exp_f32 v109, v109\n" \
"v_smfmac_f32_16x16x64_bf16 a[52:55], v[244:247], v[80:87], v243\n" \
"s_waitcnt lgkmcnt(4)\n" \
"s_cmp_eq_u32 s94, 0\n" \
"s_cselect_b32 s92, -1, 0\n" \
"v_cvt_pk_bf16_f32 v101, v106, v107\n" \
"v_mfma_f32_16x16x32_bf16 v[144:147], a[224:227], v[64:67], v[144:147]\n" \
"buffer_load_dwordx4 v198, s[48:51], s77 offen offset:1024 lds\n" \
"v_mfma_f32_16x16x32_bf16 v[144:147], a[228:231], v[68:71], v[144:147]\n" \
"s_mul_i32 s70, s94, 278528\n" \
"ds_read_b128 v[200:203], v232 offset:24832\n" \
"ds_read_b128 v[204:207], v232 offset:26880\n" \
"v_mfma_f32_16x16x32_bf16 v[176:179], a[224:227], v[80:83], v[176:179]\n" \
"s_add_u32 s93, s93, s70\n" \
"v_exp_f32 v110, v110\n" \
"v_mfma_f32_16x16x32_bf16 v[176:179], a[228:231], v[84:87], v[176:179]\n" \
"buffer_load_dwordx4 v199, s[48:51], s77 offen offset:2048 lds\n" \
"v_mfma_f32_16x16x32_bf16 v[180:183], a[232:235], v[80:83], v[180:183]\n" \
"s_lshr_b32 s70, s61, 1\n" \
"ds_read_b128 a[240:243], v232 offset:28928\n" \
"ds_read_b128 a[244:247], v232 offset:30976\n" \
"v_mfma_f32_16x16x32_bf16 v[180:183], a[236:239], v[84:87], v[180:183]\n" \
"s_lshl_b32 s70, s70, 8\n" \
"v_exp_f32 v111, v111\n" \
"v_mfma_f32_16x16x32_bf16 v[148:151], a[232:235], v[64:67], v[148:151]\n" \
"s_add_u32 s93, s93, s70\n" \
"v_exp_f32 v120, v120\n" \
"v_mfma_f32_16x16x32_bf16 v[148:151], a[236:239], v[68:71], v[148:151]\n" \
"v_cvt_pk_bf16_f32 v102, v108, v109\n" \
"v_cvt_pk_bf16_f32 v103, v110, v111\n" \
"v_exp_f32 v121, v121\n" \
"v_mfma_f32_16x16x32_bf16 v[152:155], a[32:35], v[64:67], v[152:155]\n" \
"v_add_u32 v233, 0x10000, v235\n" \
"s_lshl_b32 s72, s61, 14\n" \
"v_mfma_f32_16x16x32_bf16 v[152:155], a[36:39], v[68:71], v[152:155]\n" \
"v_add_u32 v232, s72, v235\n" \
"v_mfma_f32_16x16x32_bf16 v[184:187], a[32:35], v[80:83], v[184:187]\n" \
"v_exp_f32 v122, v122\n" \
"v_mfma_f32_16x16x32_bf16 v[184:187], a[36:39], v[84:87], v[184:187]\n" \
"v_exp_f32 v123, v123\n" \
"v_mfma_f32_16x16x32_bf16 v[188:191], a[40:43], v[80:83], v[188:191]\n" \
"v_exp_f32 v124, v124\n" \
"v_mfma_f32_16x16x32_bf16 v[188:191], a[44:47], v[84:87], v[188:191]\n" \
"v_cvt_pk_bf16_f32 v116, v120, v121\n" \
"v_cvt_pk_bf16_f32 v117, v122, v123\n" \
"v_mfma_f32_16x16x32_bf16 v[156:159], a[40:43], v[64:67], v[156:159]\n" \
"v_exp_f32 v125, v125\n" \
"v_mfma_f32_16x16x32_bf16 v[156:159], a[44:47], v[68:71], v[156:159]\n" \
"s_cmp_eq_u32 s63, 7\n" \
"s_waitcnt vmcnt(7) lgkmcnt(0)\n" \
"s_barrier\n" \
"s_cbranch_scc1 Lep_%=\n" \
"Lepg_%=:\n" \
"v_mfma_f32_16x16x32_bf16 v[16:19], a[0:3], a[176:179], v[208:211]\n" \
"s_add_u32 m0, s79, 110592\n" \
"ds_read_b128 v[64:67], v233 offset:16384\n" \
"ds_read_b128 v[68:71], v233 offset:24576\n" \
"v_mfma_f32_16x16x32_bf16 v[16:19], a[4:7], a[180:183], v[16:19]\n" \
"buffer_load_dwordx4 v192, s[44:47], s73 offen offset:0 lds\n" \
"v_mfma_f32_16x16x32_bf16 v[16:19], a[8:11], a[184:187], v[16:19]\n" \
"ds_read_b128 v[72:75], v233 offset:17408\n" \
"ds_read_b128 v[76:79], v233 offset:25600\n" \
"v_mfma_f32_16x16x32_bf16 v[16:19], a[12:15], a[188:191], v[16:19]\n" \
"ds_write_b128 v232, v[128:131] offset:0\n" \
"v_exp_f32 v126, v126\n" \
"v_mfma_f32_16x16x32_bf16 v[20:23], a[28:31], a[188:191], v[212:215]\n" \
"ds_read_b128 v[80:83], v233 offset:18432\n" \
"ds_read_b128 v[84:87], v233 offset:26624\n" \
"v_mfma_f32_16x16x32_bf16 v[20:23], a[24:27], a[184:187], v[20:23]\n" \
"buffer_load_dwordx4 v193, s[44:47], s73 offen offset:1024 lds\n" \
"v_mfma_f32_16x16x32_bf16 v[20:23], a[20:23], a[180:183], v[20:23]\n" \
"ds_write_b128 v232, v[132:135] offset:1024\n" \
"v_xor_b32 v248, 0x4000, v232\n" \
"v_mfma_f32_16x16x32_bf16 v[20:23], a[16:19], a[176:179], v[20:23]\n" \
"ds_read_b128 v[88:91], v233 offset:19456\n" \
"ds_read_b128 v[92:95], v233 offset:27648\n" \
"v_mfma_f32_16x16x32_bf16 v[24:27], v[0:3], a[176:179], v[216:219]\n" \
"ds_read_b128 a[248:251], v233 offset:7168\n" \
"ds_read_b128 a[252:255], v233 offset:15360\n" \
"v_mfma_f32_16x16x32_bf16 v[24:27], v[4:7], a[180:183], v[24:27]\n" \
"buffer_load_dwordx4 v192, s[44:47], s73 offen offset:2048 lds\n" \
"v_mfma_f32_16x16x32_bf16 v[24:27], v[8:11], a[184:187], v[24:27]\n" \
"ds_write_b128 v232, v[136:139] offset:2048\n" \
"v_mfma_f32_16x16x32_bf16 v[24:27], v[12:15], a[188:191], v[24:27]\n" \
"v_exp_f32 v127, v127\n" \
"v_cvt_pk_bf16_f32 v118, v124, v125\n" \
"v_mfma_f32_16x16x32_bf16 v[28:31], a[244:247], a[188:191], v[220:223]\n" \
"v_cvt_pk_bf16_f32 v119, v126, v127\n" \
"v_lshrrev_b32 v249, 1, v234\n" \
"v_mfma_f32_16x16x32_bf16 v[28:31], a[240:243], a[184:187], v[28:31]\n" \
"buffer_load_dwordx4 v193, s[44:47], s73 offen offset:3072 lds\n" \
"v_mfma_f32_16x16x32_bf16 v[28:31], v[204:207], a[180:183], v[28:31]\n" \
"ds_write_b128 v232, v[140:143] offset:3072\n" \
"s_lshl_b32 s72, s61, 9\n" \
"s_add_u32 s72, s72, 0x20000\n" \
"v_exp_f32 v32, v32\n" \
"v_exp_f32 v33, v33\n" \
"v_mfma_f32_16x16x32_bf16 v[28:31], v[200:203], a[176:179], v[28:31]\n" \
"s_add_u32 m0, s89, 110592\n" \
"v_exp_f32 v34, v34\n" \
"v_exp_f32 v35, v35\n" \
"v_mfma_f32_16x16x32_bf16 a[64:67], a[192:195], v[96:99], a[64:67]\n" \
"s_xor_b32 s73, s73, 0x180\n" \
"v_exp_f32 v36, v36\n" \
"v_mfma_f32_16x16x32_bf16 a[64:67], a[196:199], v[100:103], a[64:67]\n" \
"buffer_load_dwordx4 v196, s[44:47], s75 offen lds\n" \
"s_add_u32 s72, s72, %3\n" \
"v_cvt_pk_bf16_f32 v32, v32, v33\n" \
"v_exp_f32 v37, v37\n" \
"v_add_u32 v249, s72, v249\n" \
"v_mfma_f32_16x16x32_bf16 a[96:99], a[192:195], v[112:115], a[96:99]\n" \
"ds_write_b128 v232, v[144:147] offset:4096\n" \
"v_mfma_f32_16x16x32_bf16 a[96:99], a[196:199], v[116:119], a[96:99]\n" \
"ds_read_b128 a[192:195], v233 offset:0\n" \
"ds_read_b128 a[196:199], v233 offset:8192\n" \
"v_exp_f32 v38, v38\n" \
"v_cvt_pk_bf16_f32 v33, v34, v35\n" \
"v_accvgpr_read_b32 v250, a48\n" \
"v_mfma_f32_16x16x32_bf16 a[100:103], a[200:203], v[112:115], a[100:103]\n" \
"s_xor_b32 s75, s75, 0x180\n" \
"v_exp_f32 v39, v39\n" \
"v_mfma_f32_16x16x32_bf16 a[100:103], a[204:207], v[116:119], a[100:103]\n" \
"v_exp_f32 v48, v48\n" \
"v_cvt_pk_bf16_f32 v34, v36, v37\n" \
"v_exp_f32 v49, v49\n" \
"v_mfma_f32_16x16x32_bf16 a[68:71], a[200:203], v[96:99], a[68:71]\n" \
"ds_write_b128 v232, v[148:151] offset:5120\n" \
"v_mfma_f32_16x16x32_bf16 a[68:71], a[204:207], v[100:103], a[68:71]\n" \
"ds_read_b128 a[200:203], v233 offset:1024\n" \
"ds_read_b128 a[204:207], v233 offset:9216\n" \
"v_exp_f32 v50, v50\n" \
"v_cvt_pk_bf16_f32 v35, v38, v39\n" \
"v_smfmac_f32_16x16x64_bf16 v[224:227], v[244:247], v[96:103], v243\n" \
"v_cvt_pk_bf16_f32 v48, v48, v49\n" \
"v_mfma_f32_16x16x32_bf16 a[72:75], a[208:211], v[96:99], a[72:75]\n" \
"v_exp_f32 v51, v51\n" \
"v_mfma_f32_16x16x32_bf16 a[72:75], a[212:215], v[100:103], a[72:75]\n" \
"ds_write_b128 v232, v[152:155] offset:6144\n" \
"v_exp_f32 v52, v52\n" \
"v_exp_f32 v53, v53\n" \
"v_accvgpr_read_b32 v240, a49\n" \
"v_mfma_f32_16x16x32_bf16 a[104:107], a[208:211], v[112:115], a[104:107]\n" \
"v_exp_f32 v54, v54\n" \
"v_mfma_f32_16x16x32_bf16 a[104:107], a[212:215], v[116:119], a[104:107]\n" \
"ds_read_b128 a[208:211], v233 offset:2048\n" \
"ds_read_b128 a[212:215], v233 offset:10240\n" \
"v_cvt_pk_bf16_f32 v49, v50, v51\n" \
"v_exp_f32 v55, v55\n" \
"v_mfma_f32_16x16x32_bf16 a[108:111], a[216:219], v[112:115], a[108:111]\n" \
"v_exp_f32 v40, v40\n" \
"v_mfma_f32_16x16x32_bf16 a[108:111], a[220:223], v[116:119], a[108:111]\n" \
"ds_write_b128 v232, v[156:159] offset:7168\n" \
"v_cvt_pk_bf16_f32 v50, v52, v53\n" \
"v_exp_f32 v41, v41\n" \
"v_mfma_f32_16x16x32_bf16 a[76:79], a[216:219], v[96:99], a[76:79]\n" \
"v_exp_f32 v42, v42\n" \
"v_mfma_f32_16x16x32_bf16 a[76:79], a[220:223], v[100:103], a[76:79]\n" \
"ds_read_b128 a[216:219], v233 offset:3072\n" \
"ds_read_b128 a[220:223], v233 offset:11264\n" \
"v_cvt_pk_bf16_f32 v51, v54, v55\n" \
"v_exp_f32 v43, v43\n" \
"v_smfmac_f32_16x16x64_bf16 v[228:231], v[244:247], v[112:119], v243\n" \
"ds_write_b128 v232, v[160:163] offset:8192\n" \
"v_exp_f32 v44, v44\n" \
"v_add_f32 v250, v250, v240\n" \
"v_mfma_f32_16x16x32_bf16 a[80:83], a[224:227], v[96:99], a[80:83]\n" \
"v_cvt_pk_bf16_f32 v36, v40, v41\n" \
"v_cvt_pk_bf16_f32 v37, v42, v43\n" \
"v_mfma_f32_16x16x32_bf16 a[80:83], a[228:231], v[100:103], a[80:83]\n" \
"v_exp_f32 v45, v45\n" \
"v_exp_f32 v46, v46\n" \
"v_mfma_f32_16x16x32_bf16 a[112:115], a[224:227], v[112:115], a[112:115]\n" \
"v_exp_f32 v47, v47\n" \
"v_mfma_f32_16x16x32_bf16 a[112:115], a[228:231], v[116:119], a[112:115]\n" \
"ds_write_b128 v232, v[164:167] offset:9216\n" \
"v_exp_f32 v56, v56\n" \
"v_cvt_pk_bf16_f32 v38, v44, v45\n" \
"v_mfma_f32_16x16x32_bf16 a[116:119], a[232:235], v[112:115], a[116:119]\n" \
"ds_read_b128 a[224:227], v233 offset:4096\n" \
"ds_read_b128 a[228:231], v233 offset:12288\n" \
"v_mfma_f32_16x16x32_bf16 a[116:119], a[236:239], v[116:119], a[116:119]\n" \
"v_exp_f32 v57, v57\n" \
"v_exp_f32 v58, v58\n" \
"v_cvt_pk_bf16_f32 v39, v46, v47\n" \
"v_accvgpr_read_b32 v251, a52\n" \
"v_mfma_f32_16x16x32_bf16 a[84:87], a[232:235], v[96:99], a[84:87]\n" \
"v_exp_f32 v59, v59\n" \
"v_mfma_f32_16x16x32_bf16 a[84:87], a[236:239], v[100:103], a[84:87]\n" \
"ds_write_b128 v232, v[168:171] offset:10240\n" \
"v_exp_f32 v60, v60\n" \
"v_cvt_pk_bf16_f32 v52, v56, v57\n" \
"v_mfma_f32_16x16x32_bf16 a[88:91], a[32:35], v[96:99], a[88:91]\n" \
"ds_read_b128 a[232:235], v233 offset:5120\n" \
"ds_read_b128 a[236:239], v233 offset:13312\n" \
"v_mfma_f32_16x16x32_bf16 a[88:91], a[36:39], v[100:103], a[88:91]\n" \
"ds_read_b128 a[240:243], v233 offset:6144\n" \
"ds_read_b128 a[244:247], v233 offset:14336\n" \
"v_exp_f32 v61, v61\n" \
"v_exp_f32 v62, v62\n" \
"v_mfma_f32_16x16x32_bf16 a[120:123], a[32:35], v[112:115], a[120:123]\n" \
"ds_read_b128 v[0:3], v233 offset:20480\n" \
"ds_read_b128 v[4:7], v233 offset:28672\n" \
"v_mfma_f32_16x16x32_bf16 a[120:123], a[36:39], v[116:119], a[120:123]\n" \
"ds_write_b128 v232, v[172:175] offset:11264\n" \
"v_cvt_pk_bf16_f32 v53, v58, v59\n" \
"v_exp_f32 v63, v63\n" \
"v_mfma_f32_16x16x32_bf16 a[124:127], a[40:43], v[112:115], a[124:127]\n" \
"ds_read_b128 v[8:11], v233 offset:21504\n" \
"ds_read_b128 v[12:15], v233 offset:29696\n" \
"v_mfma_f32_16x16x32_bf16 a[124:127], a[44:47], v[116:119], a[124:127]\n" \
"v_cvt_pk_bf16_f32 v54, v60, v61\n" \
"v_accvgpr_read_b32 v240, a53\n" \
"v_add_f32 v251, v251, v240\n" \
"v_mfma_f32_16x16x32_bf16 a[92:95], a[40:43], v[96:99], a[92:95]\n" \
"s_waitcnt lgkmcnt(5)\n" \
"v_cvt_pk_bf16_f32 v55, v62, v63\n" \
"v_mfma_f32_16x16x32_bf16 a[92:95], a[44:47], v[100:103], a[92:95]\n" \
"ds_write_b128 v232, v[176:179] offset:12288\n" \
"ds_write_b64 v249, v[250:251]\n" \
"v_mfma_f32_16x16x32_bf16 a[64:67], a[192:195], v[32:35], a[64:67]\n" \
"ds_read_b128 v[96:99], v233 offset:22528\n" \
"ds_read_b128 v[100:103], v233 offset:30720\n" \
"v_mfma_f32_16x16x32_bf16 a[64:67], a[196:199], v[36:39], a[64:67]\n" \
"ds_read_b128 v[104:107], v233 offset:23552\n" \
"ds_read_b128 v[108:111], v233 offset:31744\n" \
"v_exp_f32 v16, v16\n" \
"v_mfma_f32_16x16x32_bf16 a[96:99], a[192:195], v[48:51], a[96:99]\n" \
"v_exp_f32 v17, v17\n" \
"v_mfma_f32_16x16x32_bf16 a[96:99], a[196:199], v[52:55], a[96:99]\n" \
"ds_write_b128 v232, v[180:183] offset:13312\n" \
"v_exp_f32 v18, v18\n" \
"v_xor_b32 v249, 0x200, v249\n" \
"v_accvgpr_read_b32 v200, a59\n" \
"v_mfma_f32_16x16x32_bf16 a[100:103], a[200:203], v[48:51], a[100:103]\n" \
"v_exp_f32 v19, v19\n" \
"v_mfma_f32_16x16x32_bf16 a[100:103], a[204:207], v[52:55], a[100:103]\n" \
"v_exp_f32 v20, v20\n" \
"v_cvt_pk_bf16_f32 v16, v16, v17\n" \
"v_accvgpr_read_b32 v202, a60\n" \
"v_accvgpr_read_b32 v204, a61\n" \
"v_mfma_f32_16x16x32_bf16 a[68:71], a[200:203], v[32:35], a[68:71]\n" \
"v_exp_f32 v21, v21\n" \
"v_mfma_f32_16x16x32_bf16 a[68:71], a[204:207], v[36:39], a[68:71]\n" \
"ds_write_b128 v232, v[184:187] offset:14336\n" \
"v_exp_f32 v22, v22\n" \
"v_xor_b32 v201, 64, v200\n" \
"v_xor_b32 v203, 64, v202\n" \
"v_xor_b32 v222, 32, v204\n" \
"v_smfmac_f32_16x16x64_bf16 v[224:227], v[244:247], v[32:39], v243\n" \
"v_cvt_pk_bf16_f32 v17, v18, v19\n" \
"v_add_u32 v205, 0x10000, v200\n" \
"v_mfma_f32_16x16x32_bf16 a[72:75], a[208:211], v[32:35], a[72:75]\n" \
"v_exp_f32 v23, v23\n" \
"v_mfma_f32_16x16x32_bf16 a[72:75], a[212:215], v[36:39], a[72:75]\n" \
"v_exp_f32 v24, v24\n" \
"v_cvt_pk_bf16_f32 v18, v20, v21\n" \
"v_add_u32 v207, 0x10000, v202\n" \
"v_add_u32 v194, 139264, v204\n" \
"v_add_u32 v206, 0x10000, v201\n" \
"v_mfma_f32_16x16x32_bf16 a[104:107], a[208:211], v[48:51], a[104:107]\n" \
"ds_write_b128 v232, v[188:191] offset:15360\n" \
"v_exp_f32 v25, v25\n" \
"v_add_u32 v208, 0x10000, v203\n" \
"v_mfma_f32_16x16x32_bf16 a[104:107], a[212:215], v[52:55], a[104:107]\n" \
"v_exp_f32 v26, v26\n" \
"v_cvt_pk_bf16_f32 v19, v22, v23\n" \
"v_add_u32 v195, 139264, v222\n" \
"v_mfma_f32_16x16x32_bf16 a[108:111], a[216:219], v[48:51], a[108:111]\n" \
"s_mul_i32 s76, s80, 393216\n" \
"s_add_u32 s76, s76, s78\n" \
"v_cvt_pk_bf16_f32 v20, v24, v25\n" \
"v_mfma_f32_16x16x32_bf16 a[108:111], a[220:223], v[52:55], a[108:111]\n" \
"s_waitcnt vmcnt(0) lgkmcnt(0)\n" \
"s_barrier\n" \
"v_mfma_f32_16x16x32_bf16 a[76:79], a[216:219], v[32:35], a[76:79]\n" \
"s_add_u32 m0, s91, 86016\n" \
"ds_read_b64_tr_b16 a[32:33], v194 offset:1024\n" \
"ds_read_b64_tr_b16 a[34:35], v194 offset:4096\n" \
"v_mfma_f32_16x16x32_bf16 a[76:79], a[220:223], v[36:39], a[76:79]\n" \
"buffer_load_dwordx4 v197, s[48:51], s76 offen offset:0 lds\n" \
"v_exp_f32 v27, v27\n" \
"v_smfmac_f32_16x16x64_bf16 v[228:231], v[244:247], v[48:55], v243\n" \
"s_and_b32 s15, %1, 7\n" \
"ds_read_b64_tr_b16 a[36:37], v194 offset:13312\n" \
"ds_read_b64_tr_b16 a[38:39], v194 offset:16384\n" \
"v_exp_f32 v28, v28\n" \
"v_mfma_f32_16x16x32_bf16 a[80:83], a[224:227], v[32:35], a[80:83]\n" \
"s_add_u32 s77, s76, 24576\n" \
"ds_read_b64_tr_b16 a[40:41], v194 offset:0\n" \
"ds_read_b64_tr_b16 a[42:43], v194 offset:3072\n" \
"v_mfma_f32_16x16x32_bf16 a[80:83], a[228:231], v[36:39], a[80:83]\n" \
"buffer_load_dwordx4 v198, s[48:51], s76 offen offset:1024 lds\n" \
"v_mfma_f32_16x16x32_bf16 a[112:115], a[224:227], v[48:51], a[112:115]\n" \
"s_lshl3_add_u32 s14, s14, s15\n" \
"ds_read_b64_tr_b16 a[44:45], v194 offset:12288\n" \
"ds_read_b64_tr_b16 a[46:47], v194 offset:15360\n" \
"v_mfma_f32_16x16x32_bf16 a[112:115], a[228:231], v[52:55], a[112:115]\n" \
"s_xor_b32 s80, s80, 0x8\n" \
"ds_read_b64_tr_b16 a[48:49], v195 offset:0\n" \
"ds_read_b64_tr_b16 a[50:51], v195 offset:3072\n" \
"v_exp_f32 v29, v29\n" \
"v_mfma_f32_16x16x32_bf16 a[116:119], a[232:235], v[48:51], a[116:119]\n" \
"buffer_load_dwordx4 v199, s[48:51], s76 offen offset:2048 lds\n" \
"v_mfma_f32_16x16x32_bf16 a[116:119], a[236:239], v[52:55], a[116:119]\n" \
"ds_read_b64_tr_b16 a[52:53], v195 offset:12288\n" \
"ds_read_b64_tr_b16 a[54:55], v195 offset:15360\n" \
"v_exp_f32 v30, v30\n" \
"v_mfma_f32_16x16x32_bf16 a[84:87], a[232:235], v[32:35], a[84:87]\n" \
"s_add_u32 m0, m0, 3072\n" \
"ds_read_b128 a[0:3], v207 offset:45056\n" \
"ds_read_b128 a[4:7], v208 offset:45056\n" \
"v_mfma_f32_16x16x32_bf16 a[84:87], a[236:239], v[36:39], a[84:87]\n" \
"buffer_load_dwordx4 v197, s[48:51], s77 offen offset:0 lds\n" \
"v_cvt_pk_bf16_f32 v21, v26, v27\n" \
"v_mfma_f32_16x16x32_bf16 a[88:91], a[240:243], v[32:35], a[88:91]\n" \
"ds_read_b128 a[8:11], v205 offset:45056\n" \
"ds_read_b128 a[12:15], v206 offset:45056\n" \
"v_mfma_f32_16x16x32_bf16 a[88:91], a[244:247], v[36:39], a[88:91]\n" \
"ds_read_b128 a[16:19], v205 offset:49152\n" \
"ds_read_b128 a[20:23], v206 offset:49152\n" \
"v_exp_f32 v31, v31\n" \
"v_mfma_f32_16x16x32_bf16 a[120:123], a[240:243], v[48:51], a[120:123]\n" \
"buffer_load_dwordx4 v198, s[48:51], s77 offen offset:1024 lds\n" \
"v_mfma_f32_16x16x32_bf16 a[120:123], a[244:247], v[52:55], a[120:123]\n" \
"ds_read_b128 a[24:27], v205 offset:53248\n" \
"ds_read_b128 a[28:31], v206 offset:53248\n" \
"v_cvt_pk_bf16_f32 v22, v28, v29\n" \
"v_mfma_f32_16x16x32_bf16 a[124:127], a[248:251], v[48:51], a[124:127]\n" \
"ds_read_b64 v[250:251], v249\n" \
"ds_read_b128 v[128:131], v248 offset:0\n" \
"v_mfma_f32_16x16x32_bf16 a[124:127], a[252:255], v[52:55], a[124:127]\n" \
"buffer_load_dwordx4 v199, s[48:51], s77 offen offset:2048 lds\n" \
"v_cvt_pk_bf16_f32 v23, v30, v31\n" \
"v_mfma_f32_16x16x32_bf16 a[92:95], a[248:251], v[32:35], a[92:95]\n" \
"ds_read_b128 v[132:135], v248 offset:1024\n" \
"ds_read_b128 v[136:139], v248 offset:2048\n" \
"v_mfma_f32_16x16x32_bf16 a[92:95], a[252:255], v[36:39], a[92:95]\n" \
"s_add_u32 m0, s79, 65536\n" \
"ds_read_b128 v[140:143], v248 offset:3072\n" \
"ds_read_b128 v[144:147], v248 offset:4096\n" \
"v_mfma_f32_16x16x32_bf16 a[96:99], v[64:67], v[16:19], a[96:99]\n" \
"buffer_load_dwordx4 v192, s[44:47], s73 offen offset:0 lds\n" \
"v_mfma_f32_16x16x32_bf16 a[96:99], v[68:71], v[20:23], a[96:99]\n" \
"ds_read_b128 v[148:151], v248 offset:5120\n" \
"ds_read_b128 v[152:155], v248 offset:6144\n" \
"v_mfma_f32_16x16x32_bf16 a[100:103], v[72:75], v[16:19], a[100:103]\n" \
"ds_read_b128 v[156:159], v248 offset:7168\n" \
"ds_read_b128 v[160:163], v248 offset:8192\n" \
"v_mfma_f32_16x16x32_bf16 a[100:103], v[76:79], v[20:23], a[100:103]\n" \
"buffer_load_dwordx4 v193, s[44:47], s73 offen offset:1024 lds\n" \
"v_smfmac_f32_16x16x64_bf16 v[228:231], v[244:247], v[16:23], v243\n" \
"ds_read_b128 v[164:167], v248 offset:9216\n" \
"ds_read_b128 v[168:171], v248 offset:10240\n" \
"v_mfma_f32_16x16x32_bf16 a[104:107], v[80:83], v[16:19], a[104:107]\n" \
"ds_read_b128 v[172:175], v248 offset:11264\n" \
"ds_read_b128 v[176:179], v248 offset:12288\n" \
"v_mfma_f32_16x16x32_bf16 a[104:107], v[84:87], v[20:23], a[104:107]\n" \
"buffer_load_dwordx4 v192, s[44:47], s73 offen offset:2048 lds\n" \
"v_mfma_f32_16x16x32_bf16 a[108:111], v[88:91], v[16:19], a[108:111]\n" \
"ds_read_b128 v[180:183], v248 offset:13312\n" \
"ds_read_b128 v[184:187], v248 offset:14336\n" \
"v_mfma_f32_16x16x32_bf16 a[108:111], v[92:95], v[20:23], a[108:111]\n" \
"ds_read_b128 v[188:191], v248 offset:15360\n" \
"v_mov_b32 v218, s60\n" \
"v_mfma_f32_16x16x32_bf16 a[112:115], v[0:3], v[16:19], a[112:115]\n" \
"buffer_load_dwordx4 v193, s[44:47], s73 offen offset:3072 lds\n" \
"v_mfma_f32_16x16x32_bf16 a[112:115], v[4:7], v[20:23], a[112:115]\n" \
"s_add_u32 m0, s89, 65536\n" \
"v_mov_b32 v219, s14\n" \
"v_lshlrev_b32 v220, 28, s61\n" \
"v_mfma_f32_16x16x32_bf16 a[116:119], v[8:11], v[16:19], a[116:119]\n" \
"s_xor_b32 s73, s73, 0x400\n" \
"v_mfma_f32_16x16x32_bf16 a[116:119], v[12:15], v[20:23], a[116:119]\n" \
"buffer_load_dwordx4 v196, s[44:47], s75 offen lds\n" \
"v_mfma_f32_16x16x32_bf16 a[120:123], v[96:99], v[16:19], a[120:123]\n" \
"s_lshl3_add_u32 s15, s15, 65593856\n" \
"v_mfma_f32_16x16x32_bf16 a[120:123], v[100:103], v[20:23], a[120:123]\n" \
"s_lshr_b32 s14, %1, 3\n" \
"s_cmp_eq_u32 s14, 3\n" \
"s_cselect_b32 s15, s15, 0x20000000\n" \
"buffer_store_dwordx2 v[218:219], v220, s[56:59], s15 offen sc1\n" \
"v_mfma_f32_16x16x32_bf16 a[124:127], v[104:107], v[16:19], a[124:127]\n" \
"s_xor_b32 s75, s75, 0x400\n" \
"v_mfma_f32_16x16x32_bf16 a[124:127], v[108:111], v[20:23], a[124:127]\n" \
"s_waitcnt lgkmcnt(0)\n" \
"s_barrier\n" \
"v_mfma_f32_16x16x32_bf16 v[32:35], a[32:35], a[0:3], 0\n" \
"s_add_u32 m0, s79, 0\n" \
"ds_read_b128 a[192:195], v205 offset:57344\n" \
"v_mfma_f32_16x16x32_bf16 v[32:35], a[36:39], a[4:7], v[32:35]\n" \
"buffer_load_dwordx4 v192, s[44:47], s73 offen offset:0 lds\n" \
"v_mfma_f32_16x16x32_bf16 v[40:43], a[32:35], a[8:11], 0\n" \
"ds_read_b128 a[196:199], v206 offset:57344\n" \
"v_add_u32 v209, 86016, v204\n" \
"v_mfma_f32_16x16x32_bf16 v[40:43], a[36:39], a[12:15], v[40:43]\n" \
"buffer_load_dwordx4 v193, s[44:47], s73 offen offset:1024 lds\n" \
"v_mfma_f32_16x16x32_bf16 v[0:3], a[40:43], a[8:11], 0\n" \
"ds_read_b64_tr_b16 a[200:201], v195 offset:1024\n" \
"ds_read_b64_tr_b16 a[202:203], v195 offset:4096\n" \
"v_mfma_f32_16x16x32_bf16 v[0:3], a[44:47], a[12:15], v[0:3]\n" \
"buffer_load_dwordx4 v192, s[44:47], s73 offen offset:2048 lds\n" \
"v_add_f32 v248, v224, v225\n" \
"v_mfma_f32_16x16x32_bf16 v[4:7], a[48:51], a[8:11], 0\n" \
"ds_read_b64_tr_b16 a[204:205], v195 offset:13312\n" \
"ds_read_b64_tr_b16 a[206:207], v195 offset:16384\n" \
"v_mfma_f32_16x16x32_bf16 v[4:7], a[52:55], a[12:15], v[4:7]\n" \
"buffer_load_dwordx4 v193, s[44:47], s73 offen offset:3072 lds\n" \
"v_mfma_f32_16x16x32_bf16 v[12:15], a[48:51], a[16:19], 0\n" \
"s_add_u32 m0, s89, 0\n" \
"s_xor_b32 s73, s73, 0x500\n" \
"ds_read_b64_tr_b16 a[208:209], v194 offset:2048\n" \
"ds_read_b64_tr_b16 a[210:211], v194 offset:5120\n" \
"v_add_f32 v249, v228, v229\n" \
"v_mfma_f32_16x16x32_bf16 v[12:15], a[52:55], a[20:23], v[12:15]\n" \
"buffer_load_dwordx4 v196, s[44:47], s75 offen lds\n" \
"v_mfma_f32_16x16x32_bf16 v[8:11], a[40:43], a[16:19], 0\n" \
"s_xor_b32 s75, s75, 0x500\n" \
"s_mul_i32 s76, s80, 393216\n" \
"s_add_u32 s76, s76, s78\n" \
"Lpg7384_%=:\n" \
"s_cbranch_execz Lcb0_%=\n" \
"s_add_u32 m0, s91, 20480\n" \
"ds_read_b64_tr_b16 a[212:213], v194 offset:14336\n" \
"ds_read_b64_tr_b16 a[214:215], v194 offset:17408\n" \
"v_mfma_f32_16x16x32_bf16 v[8:11], a[44:47], a[20:23], v[8:11]\n" \
"buffer_load_dwordx4 v197, s[48:51], s76 offen offset:0 lds\n" \
"v_add_f32 v248, v248, v250\n" \
"v_mfma_f32_16x16x32_bf16 v[48:51], a[32:35], a[16:19], 0\n" \
"s_add_u32 s77, s76, 24576\n" \
"ds_read_b64_tr_b16 a[216:217], v195 offset:2048\n" \
"v_add_u32 v210, 86016, v222\n" \
"v_mfma_f32_16x16x32_bf16 v[48:51], a[36:39], a[20:23], v[48:51]\n" \
"buffer_load_dwordx4 v198, s[48:51], s76 offen offset:1024 lds\n" \
"v_mfma_f32_16x16x32_bf16 v[56:59], a[32:35], a[24:27], 0\n" \
"s_xor_b32 s80, s80, 0xa\n" \
"ds_read_b64_tr_b16 a[218:219], v195 offset:5120\n" \
"v_add_f32 v249, v249, v251\n" \
"v_mfma_f32_16x16x32_bf16 v[56:59], a[36:39], a[28:31], v[56:59]\n" \
"buffer_load_dwordx4 v199, s[48:51], s76 offen offset:2048 lds\n" \
"v_mfma_f32_16x16x32_bf16 v[16:19], a[40:43], a[24:27], 0\n" \
"s_add_u32 m0, m0, 3072\n" \
"ds_read_b64_tr_b16 a[220:221], v195 offset:14336\n" \
"v_mfma_f32_16x16x32_bf16 v[16:19], a[44:47], a[28:31], v[16:19]\n" \
"buffer_load_dwordx4 v197, s[48:51], s77 offen offset:0 lds\n" \
"v_mfma_f32_16x16x32_bf16 v[20:23], a[48:51], a[24:27], 0\n" \
"s_waitcnt lgkmcnt(2)\n" \
"ds_read_b64_tr_b16 a[222:223], v195 offset:17408\n" \
"v_mfma_f32_16x16x32_bf16 v[20:23], a[52:55], a[28:31], v[20:23]\n" \
"buffer_load_dwordx4 v198, s[48:51], s77 offen offset:1024 lds\n" \
"v_rcp_f32 v232, v248\n" \
"v_mfma_f32_16x16x32_bf16 v[28:31], a[48:51], a[192:195], 0\n" \
"v_rcp_f32 v233, v249\n" \
"v_mfma_f32_16x16x32_bf16 v[28:31], a[52:55], a[196:199], v[28:31]\n" \
"buffer_load_dwordx4 v199, s[48:51], s77 offen offset:2048 lds\n" \
"v_add_u32 v240, 0x1000, v236\n" \
"v_mfma_f32_16x16x32_bf16 v[24:27], a[40:43], a[192:195], 0\n" \
"v_add_f32 v224, v248, v249\n" \
"v_mul_f32 v224, 0x43800000, v224\n" \
"v_mfma_f32_16x16x32_bf16 v[24:27], a[44:47], a[196:199], v[24:27]\n" \
"v_add_f32 v248, v232, v233\n" \
"v_fmac_f32 v224, 0x5f800000, v248\n" \
"v_mfma_f32_16x16x32_bf16 v[64:67], a[32:35], a[192:195], 0\n" \
"v_mov_b32 v225, 0\n" \
"v_mov_b32 v226, 0\n" \
"v_mfma_f32_16x16x32_bf16 v[64:67], a[36:39], a[196:199], v[64:67]\n" \
"v_mov_b32 v227, 0\n" \
"s_waitcnt vmcnt(12) lgkmcnt(0)\n" \
"s_barrier\n" \
"v_mfma_f32_16x16x32_bf16 v[68:71], a[200:203], a[192:195], 0\n" \
"ds_read_b64_tr_b16 a[32:33], v209 offset:1024\n" \
"ds_read_b64_tr_b16 a[34:35], v209 offset:4096\n" \
"v_mfma_f32_16x16x32_bf16 v[68:71], a[204:207], a[196:199], v[68:71]\n" \
"ds_read_b64_tr_b16 a[36:37], v209 offset:13312\n" \
"ds_read_b64_tr_b16 a[38:39], v209 offset:16384\n" \
"v_mfma_f32_16x16x32_bf16 v[60:63], a[200:203], a[24:27], 0\n" \
"s_add_u32 m0, s79, 110592\n" \
"ds_read_b64_tr_b16 a[40:41], v209 offset:0\n" \
"v_accvgpr_read_b32 v112, a64\n" \
"v_mfma_f32_16x16x32_bf16 v[60:63], a[204:207], a[28:31], v[60:63]\n" \
"buffer_load_dwordx4 v192, s[44:47], s73 offen offset:0 lds\n" \
"v_accvgpr_read_b32 v113, a65\n" \
"v_mfma_f32_16x16x32_bf16 v[52:55], a[200:203], a[16:19], 0\n" \
"ds_read_b64_tr_b16 a[42:43], v209 offset:3072\n" \
"v_accvgpr_read_b32 v114, a66\n" \
"v_mfma_f32_16x16x32_bf16 v[52:55], a[204:207], a[20:23], v[52:55]\n" \
"ds_read_b64_tr_b16 a[44:45], v209 offset:12288\n" \
"v_accvgpr_read_b32 v115, a67\n" \
"v_accvgpr_read_b32 v116, a68\n" \
"v_mfma_f32_16x16x32_bf16 v[44:47], a[200:203], a[8:11], 0\n" \
"ds_read_b64_tr_b16 a[46:47], v209 offset:15360\n" \
"v_accvgpr_read_b32 v117, a69\n" \
"v_mfma_f32_16x16x32_bf16 v[44:47], a[204:207], a[12:15], v[44:47]\n" \
"buffer_load_dwordx4 v193, s[44:47], s73 offen offset:1024 lds\n" \
"v_mfma_f32_16x16x32_bf16 v[72:75], a[0:3], a[208:211], 0\n" \
"ds_read_b64_tr_b16 a[48:49], v210 offset:0\n" \
"v_accvgpr_read_b32 v118, a70\n" \
"v_mfma_f32_16x16x32_bf16 v[72:75], a[4:7], a[212:215], v[72:75]\n" \
"ds_read_b128 a[0:3], v207 offset:0\n" \
"v_accvgpr_read_b32 v119, a71\n" \
"v_add_f32 v112, v112, v128\n" \
"v_mfma_f32_16x16x32_bf16 v[80:83], a[8:11], a[208:211], 0\n" \
"ds_read_b128 a[4:7], v208 offset:0\n" \
"v_add_f32 v113, v113, v129\n" \
"v_mfma_f32_16x16x32_bf16 v[80:83], a[12:15], a[212:215], v[80:83]\n" \
"buffer_load_dwordx4 v192, s[44:47], s73 offen offset:2048 lds\n" \
"v_mfma_f32_16x16x32_bf16 v[84:87], a[8:11], a[216:219], 0\n" \
"ds_read_b64_tr_b16 a[50:51], v210 offset:3072\n" \
"v_add_f32 v114, v114, v130\n" \
"v_mfma_f32_16x16x32_bf16 v[84:87], a[12:15], a[220:223], v[84:87]\n" \
"ds_read_b128 a[8:11], v205 offset:0\n" \
"v_add_f32 v115, v115, v131\n" \
"v_add_f32 v116, v116, v132\n" \
"v_mfma_f32_16x16x32_bf16 v[92:95], a[16:19], a[216:219], 0\n" \
"ds_read_b128 a[12:15], v206 offset:0\n" \
"v_add_f32 v117, v117, v133\n" \
"v_mfma_f32_16x16x32_bf16 v[92:95], a[20:23], a[220:223], v[92:95]\n" \
"buffer_load_dwordx4 v193, s[44:47], s73 offen offset:3072 lds\n" \
"v_add_f32 v118, v118, v134\n" \
"v_mfma_f32_16x16x32_bf16 v[88:91], a[16:19], a[208:211], 0\n" \
"ds_read_b64_tr_b16 a[52:53], v210 offset:12288\n" \
"v_add_f32 v119, v119, v135\n" \
"v_mfma_f32_16x16x32_bf16 v[88:91], a[20:23], a[212:215], v[88:91]\n" \
"s_add_u32 m0, s89, 110592\n" \
"ds_read_b128 a[16:19], v205 offset:4096\n" \
"v_mul_f32 v112, v112, v232\n" \
"v_mfma_f32_16x16x32_bf16 v[96:99], a[24:27], a[208:211], 0\n" \
"s_xor_b32 s73, s73, 0x180\n" \
"ds_read_b128 a[20:23], v206 offset:4096\n" \
"v_mul_f32 v113, v113, v232\n" \
"v_mfma_f32_16x16x32_bf16 v[96:99], a[28:31], a[212:215], v[96:99]\n" \
"buffer_load_dwordx4 v196, s[44:47], s75 offen lds\n" \
"v_mul_f32 v114, v114, v232\n" \
"v_mfma_f32_16x16x32_bf16 v[100:103], a[24:27], a[216:219], 0\n" \
"s_xor_b32 s75, s75, 0x180\n" \
"s_mul_i32 s76, s80, 393216\n" \
"ds_read_b64_tr_b16 a[54:55], v210 offset:15360\n" \
"v_mfma_f32_16x16x32_bf16 v[100:103], a[28:31], a[220:223], v[100:103]\n" \
"s_add_u32 s76, s76, s78\n" \
"ds_read_b128 a[24:27], v205 offset:8192\n" \
"v_mul_f32 v115, v115, v232\n" \
"v_mfma_f32_16x16x32_bf16 v[108:111], a[192:195], a[216:219], 0\n" \
"s_add_u32 m0, s91, 139264\n" \
"ds_read_b128 a[28:31], v206 offset:8192\n" \
"v_mul_f32 v116, v116, v232\n" \
"v_mfma_f32_16x16x32_bf16 v[108:111], a[196:199], a[220:223], v[108:111]\n" \
"buffer_load_dwordx4 v197, s[48:51], s76 offen offset:0 lds\n" \
"v_mul_f32 v117, v117, v232\n" \
"v_mfma_f32_16x16x32_bf16 v[104:107], a[192:195], a[208:211], 0\n" \
"v_mul_f32 v118, v118, v232\n" \
"v_mul_f32 v119, v119, v232\n" \
"v_mfma_f32_16x16x32_bf16 v[104:107], a[196:199], a[212:215], v[104:107]\n" \
"s_waitcnt lgkmcnt(2)\n" \
"v_cvt_pk_bf16_f32 v120, v112, v113\n" \
"v_cvt_pk_bf16_f32 v121, v114, v115\n" \
"v_cvt_pk_bf16_f32 v122, v116, v117\n" \
"v_mfma_f32_16x16x32_bf16 v[32:35], a[32:35], a[0:3], v[32:35]\n" \
"s_add_u32 s77, s76, 24576\n" \
"ds_read_b128 a[192:195], v205 offset:12288\n" \
"v_cvt_pk_bf16_f32 v123, v118, v119\n" \
"v_mfma_f32_16x16x32_bf16 v[32:35], a[36:39], a[4:7], v[32:35]\n" \
"buffer_load_dwordx4 v198, s[48:51], s76 offen offset:1024 lds\n" \
"v_mfma_f32_16x16x32_bf16 v[40:43], a[32:35], a[8:11], v[40:43]\n" \
"ds_read_b128 a[196:199], v206 offset:12288\n" \
"v_mfma_f32_16x16x32_bf16 v[40:43], a[36:39], a[12:15], v[40:43]\n" \
"ds_read_b64_tr_b16 a[200:201], v210 offset:1024\n" \
"buffer_store_dwordx4 v[120:123], v236, s[56:59], s88 offen offset:0 sc1\n" \
"v_mfma_f32_16x16x32_bf16 v[0:3], a[40:43], a[8:11], v[0:3]\n" \
"s_xor_b32 s80, s80, 0x3\n" \
"ds_read_b64_tr_b16 a[202:203], v210 offset:4096\n" \
"v_mfma_f32_16x16x32_bf16 v[0:3], a[44:47], a[12:15], v[0:3]\n" \
"buffer_load_dwordx4 v199, s[48:51], s76 offen offset:2048 lds\n" \
"v_mfma_f32_16x16x32_bf16 v[224:227], v[244:247], v[120:123], v[224:227]\n" \
"v_accvgpr_read_b32 v112, a72\n" \
"v_accvgpr_read_b32 v113, a73\n" \
"v_accvgpr_read_b32 v114, a74\n" \
"v_mfma_f32_16x16x32_bf16 v[4:7], a[48:51], a[8:11], v[4:7]\n" \
"ds_read_b64_tr_b16 a[204:205], v210 offset:13312\n" \
"v_accvgpr_read_b32 v115, a75\n" \
"v_mfma_f32_16x16x32_bf16 v[4:7], a[52:55], a[12:15], v[4:7]\n" \
"ds_read_b64_tr_b16 a[206:207], v210 offset:16384\n" \
"v_accvgpr_read_b32 v116, a76\n" \
"v_accvgpr_read_b32 v117, a77\n" \
"v_mfma_f32_16x16x32_bf16 v[12:15], a[48:51], a[16:19], v[12:15]\n" \
"s_add_u32 m0, m0, 3072\n" \
"ds_read_b64_tr_b16 a[208:209], v209 offset:2048\n" \
"v_accvgpr_read_b32 v118, a78\n" \
"v_mfma_f32_16x16x32_bf16 v[12:15], a[52:55], a[20:23], v[12:15]\n" \
"buffer_load_dwordx4 v197, s[48:51], s77 offen offset:0 lds\n" \
"v_accvgpr_read_b32 v119, a79\n" \
"v_mfma_f32_16x16x32_bf16 v[8:11], a[40:43], a[16:19], v[8:11]\n" \
"ds_read_b64_tr_b16 a[210:211], v209 offset:5120\n" \
"v_add_f32 v112, v112, v136\n" \
"v_mfma_f32_16x16x32_bf16 v[8:11], a[44:47], a[20:23], v[8:11]\n" \
"ds_read_b64_tr_b16 a[212:213], v209 offset:14336\n" \
"v_add_f32 v113, v113, v137\n" \
"v_mfma_f32_16x16x32_bf16 v[48:51], a[32:35], a[16:19], v[48:51]\n" \
"s_waitcnt lgkmcnt(3)\n" \
"ds_read_b64_tr_b16 a[214:215], v209 offset:17408\n" \
"v_add_f32 v114, v114, v138\n" \
"v_mfma_f32_16x16x32_bf16 v[48:51], a[36:39], a[20:23], v[48:51]\n" \
"buffer_load_dwordx4 v198, s[48:51], s77 offen offset:1024 lds\n" \
"v_add_f32 v115, v115, v139\n" \
"v_mfma_f32_16x16x32_bf16 v[56:59], a[32:35], a[24:27], v[56:59]\n" \
"ds_read_b64_tr_b16 a[216:217], v210 offset:2048\n" \
"v_add_f32 v116, v116, v140\n" \
"v_mfma_f32_16x16x32_bf16 v[56:59], a[36:39], a[28:31], v[56:59]\n" \
"ds_read_b64_tr_b16 a[218:219], v210 offset:5120\n" \
"v_add_f32 v117, v117, v141\n" \
"v_mfma_f32_16x16x32_bf16 v[16:19], a[40:43], a[24:27], v[16:19]\n" \
"ds_read_b64_tr_b16 a[220:221], v210 offset:14336\n" \
"v_add_f32 v118, v118, v142\n" \
"v_mfma_f32_16x16x32_bf16 v[16:19], a[44:47], a[28:31], v[16:19]\n" \
"buffer_load_dwordx4 v199, s[48:51], s77 offen offset:2048 lds\n" \
"v_add_f32 v119, v119, v143\n" \
"v_mfma_f32_16x16x32_bf16 v[20:23], a[48:51], a[24:27], v[20:23]\n" \
"ds_read_b64_tr_b16 a[222:223], v210 offset:17408\n" \
"v_mul_f32 v112, v112, v232\n" \
"v_mfma_f32_16x16x32_bf16 v[20:23], a[52:55], a[28:31], v[20:23]\n" \
"v_mul_f32 v113, v113, v232\n" \
"v_mul_f32 v114, v114, v232\n" \
"v_mul_f32 v115, v115, v232\n" \
"v_mfma_f32_16x16x32_bf16 v[28:31], a[48:51], a[192:195], v[28:31]\n" \
"v_mul_f32 v116, v116, v232\n" \
"v_mul_f32 v117, v117, v232\n" \
"v_mfma_f32_16x16x32_bf16 v[28:31], a[52:55], a[196:199], v[28:31]\n" \
"v_mul_f32 v118, v118, v232\n" \
"v_mul_f32 v119, v119, v232\n" \
"v_mfma_f32_16x16x32_bf16 v[24:27], a[40:43], a[192:195], v[24:27]\n" \
"v_cvt_pk_bf16_f32 v124, v112, v113\n" \
"v_cvt_pk_bf16_f32 v125, v114, v115\n" \
"v_mfma_f32_16x16x32_bf16 v[24:27], a[44:47], a[196:199], v[24:27]\n" \
"v_cvt_pk_bf16_f32 v126, v116, v117\n" \
"v_cvt_pk_bf16_f32 v127, v118, v119\n" \
"v_mfma_f32_16x16x32_bf16 v[64:67], a[32:35], a[192:195], v[64:67]\n" \
"buffer_store_dwordx4 v[124:127], v236, s[56:59], s88 offen offset:2048 sc1\n" \
"v_mfma_f32_16x16x32_bf16 v[64:67], a[36:39], a[196:199], v[64:67]\n" \
"v_mfma_f32_16x16x32_bf16 v[224:227], v[244:247], v[124:127], v[224:227]\n" \
"v_accvgpr_read_b32 v112, a80\n" \
"s_waitcnt vmcnt(13) lgkmcnt(0)\n" \
"s_barrier\n" \
"v_mfma_f32_16x16x32_bf16 v[68:71], a[200:203], a[192:195], v[68:71]\n" \
"ds_read_b64_tr_b16 a[32:33], v204 offset:21504\n" \
"ds_read_b64_tr_b16 a[34:35], v204 offset:24576\n" \
"v_mfma_f32_16x16x32_bf16 v[68:71], a[204:207], a[196:199], v[68:71]\n" \
"ds_read_b64_tr_b16 a[36:37], v204 offset:33792\n" \
"ds_read_b64_tr_b16 a[38:39], v204 offset:36864\n" \
"v_accvgpr_read_b32 v113, a81\n" \
"v_mfma_f32_16x16x32_bf16 v[60:63], a[200:203], a[24:27], v[60:63]\n" \
"s_add_u32 m0, s79, 65536\n" \
"ds_read_b64_tr_b16 a[40:41], v204 offset:20480\n" \
"v_accvgpr_read_b32 v114, a82\n" \
"v_mfma_f32_16x16x32_bf16 v[60:63], a[204:207], a[28:31], v[60:63]\n" \
"buffer_load_dwordx4 v192, s[44:47], s73 offen offset:0 lds\n" \
"v_accvgpr_read_b32 v115, a83\n" \
"v_mfma_f32_16x16x32_bf16 v[52:55], a[200:203], a[16:19], v[52:55]\n" \
"ds_read_b64_tr_b16 a[42:43], v204 offset:23552\n" \
"v_accvgpr_read_b32 v116, a84\n" \
"v_mfma_f32_16x16x32_bf16 v[52:55], a[204:207], a[20:23], v[52:55]\n" \
"ds_read_b64_tr_b16 a[44:45], v204 offset:32768\n" \
"v_accvgpr_read_b32 v117, a85\n" \
"v_mfma_f32_16x16x32_bf16 v[44:47], a[200:203], a[8:11], v[44:47]\n" \
"ds_read_b64_tr_b16 a[46:47], v204 offset:35840\n" \
"v_accvgpr_read_b32 v118, a86\n" \
"v_mfma_f32_16x16x32_bf16 v[44:47], a[204:207], a[12:15], v[44:47]\n" \
"buffer_load_dwordx4 v193, s[44:47], s73 offen offset:1024 lds\n" \
"v_accvgpr_read_b32 v119, a87\n" \
"v_mfma_f32_16x16x32_bf16 v[72:75], a[0:3], a[208:211], v[72:75]\n" \
"ds_read_b64_tr_b16 a[48:49], v222 offset:20480\n" \
"v_add_f32 v112, v112, v144\n" \
"v_mfma_f32_16x16x32_bf16 v[72:75], a[4:7], a[212:215], v[72:75]\n" \
"ds_read_b128 a[0:3], v202 offset:0\n" \
"v_add_f32 v113, v113, v145\n" \
"v_mfma_f32_16x16x32_bf16 v[80:83], a[8:11], a[208:211], v[80:83]\n" \
"ds_read_b128 a[4:7], v203 offset:0\n" \
"v_add_f32 v114, v114, v146\n" \
"v_mfma_f32_16x16x32_bf16 v[80:83], a[12:15], a[212:215], v[80:83]\n" \
"buffer_load_dwordx4 v192, s[44:47], s73 offen offset:2048 lds\n" \
"v_add_f32 v115, v115, v147\n" \
"v_mfma_f32_16x16x32_bf16 v[84:87], a[8:11], a[216:219], v[84:87]\n" \
"ds_read_b64_tr_b16 a[50:51], v222 offset:23552\n" \
"v_add_f32 v116, v116, v148\n" \
"v_mfma_f32_16x16x32_bf16 v[84:87], a[12:15], a[220:223], v[84:87]\n" \
"ds_read_b128 a[8:11], v200 offset:0\n" \
"v_add_f32 v117, v117, v149\n" \
"v_add_f32 v118, v118, v150\n" \
"v_mfma_f32_16x16x32_bf16 v[92:95], a[16:19], a[216:219], v[92:95]\n" \
"ds_read_b128 a[12:15], v201 offset:0\n" \
"v_add_f32 v119, v119, v151\n" \
"v_mfma_f32_16x16x32_bf16 v[92:95], a[20:23], a[220:223], v[92:95]\n" \
"buffer_load_dwordx4 v193, s[44:47], s73 offen offset:3072 lds\n" \
"v_mfma_f32_16x16x32_bf16 v[88:91], a[16:19], a[208:211], v[88:91]\n" \
"ds_read_b64_tr_b16 a[52:53], v222 offset:32768\n" \
"v_mul_f32 v112, v112, v232\n" \
"v_mfma_f32_16x16x32_bf16 v[88:91], a[20:23], a[212:215], v[88:91]\n" \
"s_add_u32 m0, s89, 65536\n" \
"ds_read_b128 a[16:19], v200 offset:4096\n" \
"v_mul_f32 v113, v113, v232\n" \
"v_mul_f32 v114, v114, v232\n" \
"v_mfma_f32_16x16x32_bf16 v[96:99], a[24:27], a[208:211], v[96:99]\n" \
"s_xor_b32 s73, s73, 0x300\n" \
"ds_read_b128 a[20:23], v201 offset:4096\n" \
"v_mul_f32 v115, v115, v232\n" \
"v_mfma_f32_16x16x32_bf16 v[96:99], a[28:31], a[212:215], v[96:99]\n" \
"buffer_load_dwordx4 v196, s[44:47], s75 offen lds\n" \
"v_mfma_f32_16x16x32_bf16 v[100:103], a[24:27], a[216:219], v[100:103]\n" \
"s_xor_b32 s75, s75, 0x300\n" \
"s_mul_i32 s76, s80, 393216\n" \
"ds_read_b64_tr_b16 a[54:55], v222 offset:35840\n" \
"v_mfma_f32_16x16x32_bf16 v[100:103], a[28:31], a[220:223], v[100:103]\n" \
"s_add_u32 s76, s76, s78\n" \
"ds_read_b128 a[24:27], v200 offset:8192\n" \
"v_mul_f32 v116, v116, v232\n" \
"v_mul_f32 v117, v117, v232\n" \
"v_mfma_f32_16x16x32_bf16 v[108:111], a[192:195], a[216:219], v[108:111]\n" \
"s_add_u32 m0, s91, 86016\n" \
"ds_read_b128 a[28:31], v201 offset:8192\n" \
"v_mul_f32 v118, v118, v232\n" \
"v_mfma_f32_16x16x32_bf16 v[108:111], a[196:199], a[220:223], v[108:111]\n" \
"buffer_load_dwordx4 v197, s[48:51], s76 offen offset:0 lds\n" \
"v_mul_f32 v119, v119, v232\n" \
"v_mfma_f32_16x16x32_bf16 v[104:107], a[192:195], a[208:211], v[104:107]\n" \
"v_cvt_pk_bf16_f32 v120, v112, v113\n" \
"v_cvt_pk_bf16_f32 v121, v114, v115\n" \
"v_mfma_f32_16x16x32_bf16 v[104:107], a[196:199], a[212:215], v[104:107]\n" \
"s_waitcnt lgkmcnt(2)\n" \
"v_cvt_pk_bf16_f32 v122, v116, v117\n" \
"v_cvt_pk_bf16_f32 v123, v118, v119\n" \
"v_mfma_f32_16x16x32_bf16 v[32:35], a[32:35], a[0:3], v[32:35]\n" \
"s_add_u32 s77, s76, 24576\n" \
"ds_read_b128 a[192:195], v200 offset:12288\n" \
"v_mfma_f32_16x16x32_bf16 v[32:35], a[36:39], a[4:7], v[32:35]\n" \
"buffer_load_dwordx4 v198, s[48:51], s76 offen offset:1024 lds\n" \
"v_mfma_f32_16x16x32_bf16 v[40:43], a[32:35], a[8:11], v[40:43]\n" \
"ds_read_b128 a[196:199], v201 offset:12288\n" \
"v_mfma_f32_16x16x32_bf16 v[40:43], a[36:39], a[12:15], v[40:43]\n" \
"ds_read_b64_tr_b16 a[200:201], v222 offset:21504\n" \
"buffer_store_dwordx4 v[120:123], v240, s[56:59], s88 offen offset:0 sc1\n" \
"v_mfma_f32_16x16x32_bf16 v[0:3], a[40:43], a[8:11], v[0:3]\n" \
"s_xor_b32 s80, s80, 0x6\n" \
"ds_read_b64_tr_b16 a[202:203], v222 offset:24576\n" \
"v_mfma_f32_16x16x32_bf16 v[224:227], v[244:247], v[120:123], v[224:227]\n" \
"v_accvgpr_read_b32 v112, a88\n" \
"v_accvgpr_read_b32 v113, a89\n" \
"v_mfma_f32_16x16x32_bf16 v[0:3], a[44:47], a[12:15], v[0:3]\n" \
"buffer_load_dwordx4 v199, s[48:51], s76 offen offset:2048 lds\n" \
"v_mfma_f32_16x16x32_bf16 v[4:7], a[48:51], a[8:11], v[4:7]\n" \
"ds_read_b64_tr_b16 a[204:205], v222 offset:33792\n" \
"v_accvgpr_read_b32 v114, a90\n" \
"v_mfma_f32_16x16x32_bf16 v[4:7], a[52:55], a[12:15], v[4:7]\n" \
"ds_read_b64_tr_b16 a[206:207], v222 offset:36864\n" \
"v_accvgpr_read_b32 v115, a91\n" \
"v_accvgpr_read_b32 v116, a92\n" \
"v_mfma_f32_16x16x32_bf16 v[12:15], a[48:51], a[16:19], v[12:15]\n" \
"s_add_u32 m0, m0, 3072\n" \
"ds_read_b64_tr_b16 a[208:209], v204 offset:22528\n" \
"v_accvgpr_read_b32 v117, a93\n" \
"v_mfma_f32_16x16x32_bf16 v[12:15], a[52:55], a[20:23], v[12:15]\n" \
"buffer_load_dwordx4 v197, s[48:51], s77 offen offset:0 lds\n" \
"v_mfma_f32_16x16x32_bf16 v[8:11], a[40:43], a[16:19], v[8:11]\n" \
"ds_read_b64_tr_b16 a[210:211], v204 offset:25600\n" \
"v_accvgpr_read_b32 v118, a94\n" \
"v_mfma_f32_16x16x32_bf16 v[8:11], a[44:47], a[20:23], v[8:11]\n" \
"ds_read_b64_tr_b16 a[212:213], v204 offset:34816\n" \
"v_accvgpr_read_b32 v119, a95\n" \
"v_add_f32 v112, v112, v152\n" \
"v_mfma_f32_16x16x32_bf16 v[48:51], a[32:35], a[16:19], v[48:51]\n" \
"s_waitcnt lgkmcnt(3)\n" \
"ds_read_b64_tr_b16 a[214:215], v204 offset:37888\n" \
"v_add_f32 v113, v113, v153\n" \
"v_mfma_f32_16x16x32_bf16 v[48:51], a[36:39], a[20:23], v[48:51]\n" \
"buffer_load_dwordx4 v198, s[48:51], s77 offen offset:1024 lds\n" \
"v_mfma_f32_16x16x32_bf16 v[56:59], a[32:35], a[24:27], v[56:59]\n" \
"ds_read_b64_tr_b16 a[216:217], v222 offset:22528\n" \
"v_add_f32 v114, v114, v154\n" \
"v_mfma_f32_16x16x32_bf16 v[56:59], a[36:39], a[28:31], v[56:59]\n" \
"ds_read_b64_tr_b16 a[218:219], v222 offset:25600\n" \
"v_add_f32 v115, v115, v155\n" \
"v_add_f32 v116, v116, v156\n" \
"v_mfma_f32_16x16x32_bf16 v[16:19], a[40:43], a[24:27], v[16:19]\n" \
"ds_read_b64_tr_b16 a[220:221], v222 offset:34816\n" \
"v_add_f32 v117, v117, v157\n" \
"v_mfma_f32_16x16x32_bf16 v[16:19], a[44:47], a[28:31], v[16:19]\n" \
"buffer_load_dwordx4 v199, s[48:51], s77 offen offset:2048 lds\n" \
"v_add_f32 v118, v118, v158\n" \
"v_mfma_f32_16x16x32_bf16 v[20:23], a[48:51], a[24:27], v[20:23]\n" \
"ds_read_b64_tr_b16 a[222:223], v222 offset:37888\n" \
"v_add_f32 v119, v119, v159\n" \
"v_mfma_f32_16x16x32_bf16 v[20:23], a[52:55], a[28:31], v[20:23]\n" \
"v_mul_f32 v112, v112, v232\n" \
"v_mul_f32 v113, v113, v232\n" \
"v_mfma_f32_16x16x32_bf16 v[28:31], a[48:51], a[192:195], v[28:31]\n" \
"v_mul_f32 v114, v114, v232\n" \
"v_mul_f32 v115, v115, v232\n" \
"v_mfma_f32_16x16x32_bf16 v[28:31], a[52:55], a[196:199], v[28:31]\n" \
"v_mul_f32 v116, v116, v232\n" \
"v_mul_f32 v117, v117, v232\n" \
"v_mul_f32 v118, v118, v232\n" \
"v_mfma_f32_16x16x32_bf16 v[24:27], a[40:43], a[192:195], v[24:27]\n" \
"v_mul_f32 v119, v119, v232\n" \
"v_cvt_pk_bf16_f32 v124, v112, v113\n" \
"v_mfma_f32_16x16x32_bf16 v[24:27], a[44:47], a[196:199], v[24:27]\n" \
"v_cvt_pk_bf16_f32 v125, v114, v115\n" \
"v_cvt_pk_bf16_f32 v126, v116, v117\n" \
"v_mfma_f32_16x16x32_bf16 v[64:67], a[32:35], a[192:195], v[64:67]\n" \
"v_cvt_pk_bf16_f32 v127, v118, v119\n" \
"v_mfma_f32_16x16x32_bf16 v[64:67], a[36:39], a[196:199], v[64:67]\n" \
"buffer_store_dwordx4 v[124:127], v240, s[56:59], s88 offen offset:2048 sc1\n" \
"s_waitcnt vmcnt(14) lgkmcnt(0)\n" \
"s_barrier\n" \
"v_mfma_f32_16x16x32_bf16 v[68:71], a[200:203], a[192:195], v[68:71]\n" \
"ds_read_b64_tr_b16 a[32:33], v194 offset:1024\n" \
"ds_read_b64_tr_b16 a[34:35], v194 offset:4096\n" \
"v_mfma_f32_16x16x32_bf16 v[68:71], a[204:207], a[196:199], v[68:71]\n" \
"ds_read_b64_tr_b16 a[36:37], v194 offset:13312\n" \
"ds_read_b64_tr_b16 a[38:39], v194 offset:16384\n" \
"v_mfma_f32_16x16x32_bf16 v[224:227], v[244:247], v[124:127], v[224:227]\n" \
"s_add_u32 s82, s88, 532480\n" \
"v_accvgpr_read_b32 v112, a96\n" \
"v_accvgpr_read_b32 v113, a97\n" \
"v_accvgpr_read_b32 v114, a98\n" \
"v_accvgpr_read_b32 v115, a99\n" \
"v_mfma_f32_16x16x32_bf16 v[60:63], a[200:203], a[24:27], v[60:63]\n" \
"s_add_u32 m0, s79, 0\n" \
"ds_read_b64_tr_b16 a[40:41], v194 offset:0\n" \
"v_accvgpr_read_b32 v116, a100\n" \
"v_mfma_f32_16x16x32_bf16 v[60:63], a[204:207], a[28:31], v[60:63]\n" \
"buffer_load_dwordx4 v192, s[44:47], s73 offen offset:0 lds\n" \
"v_mfma_f32_16x16x32_bf16 v[52:55], a[200:203], a[16:19], v[52:55]\n" \
"ds_read_b64_tr_b16 a[42:43], v194 offset:3072\n" \
"v_accvgpr_read_b32 v117, a101\n" \
"v_mfma_f32_16x16x32_bf16 v[52:55], a[204:207], a[20:23], v[52:55]\n" \
"ds_read_b64_tr_b16 a[44:45], v194 offset:12288\n" \
"v_accvgpr_read_b32 v118, a102\n" \
"v_accvgpr_read_b32 v119, a103\n" \
"v_mfma_f32_16x16x32_bf16 v[44:47], a[200:203], a[8:11], v[44:47]\n" \
"ds_read_b64_tr_b16 a[46:47], v194 offset:15360\n" \
"v_add_f32 v112, v112, v160\n" \
"v_mfma_f32_16x16x32_bf16 v[44:47], a[204:207], a[12:15], v[44:47]\n" \
"buffer_load_dwordx4 v193, s[44:47], s73 offen offset:1024 lds\n" \
"v_mfma_f32_16x16x32_bf16 v[72:75], a[0:3], a[208:211], v[72:75]\n" \
"ds_read_b64_tr_b16 a[48:49], v195 offset:0\n" \
"v_add_f32 v113, v113, v161\n" \
"v_mfma_f32_16x16x32_bf16 v[72:75], a[4:7], a[212:215], v[72:75]\n" \
"ds_read_b128 a[0:3], v207 offset:45056\n" \
"v_add_f32 v114, v114, v162\n" \
"v_add_f32 v115, v115, v163\n" \
"v_mfma_f32_16x16x32_bf16 v[80:83], a[8:11], a[208:211], v[80:83]\n" \
"ds_read_b128 a[4:7], v208 offset:45056\n" \
"v_add_f32 v116, v116, v164\n" \
"v_mfma_f32_16x16x32_bf16 v[80:83], a[12:15], a[212:215], v[80:83]\n" \
"buffer_load_dwordx4 v192, s[44:47], s73 offen offset:2048 lds\n" \
"v_add_f32 v117, v117, v165\n" \
"v_mfma_f32_16x16x32_bf16 v[84:87], a[8:11], a[216:219], v[84:87]\n" \
"ds_read_b64_tr_b16 a[50:51], v195 offset:3072\n" \
"v_add_f32 v118, v118, v166\n" \
"v_mfma_f32_16x16x32_bf16 v[84:87], a[12:15], a[220:223], v[84:87]\n" \
"ds_read_b128 a[8:11], v205 offset:45056\n" \
"v_add_f32 v119, v119, v167\n" \
"v_mfma_f32_16x16x32_bf16 v[92:95], a[16:19], a[216:219], v[92:95]\n" \
"ds_read_b128 a[12:15], v206 offset:45056\n" \
"v_mul_f32 v112, v112, v233\n" \
"v_mfma_f32_16x16x32_bf16 v[92:95], a[20:23], a[220:223], v[92:95]\n" \
"buffer_load_dwordx4 v193, s[44:47], s73 offen offset:3072 lds\n" \
"v_mul_f32 v113, v113, v233\n" \
"v_mfma_f32_16x16x32_bf16 v[88:91], a[16:19], a[208:211], v[88:91]\n" \
"ds_read_b64_tr_b16 a[52:53], v195 offset:12288\n" \
"v_mul_f32 v114, v114, v233\n" \
"v_mfma_f32_16x16x32_bf16 v[88:91], a[20:23], a[212:215], v[88:91]\n" \
"s_add_u32 m0, s89, 0\n" \
"ds_read_b128 a[16:19], v205 offset:49152\n" \
"v_mul_f32 v115, v115, v233\n" \
"v_mfma_f32_16x16x32_bf16 v[96:99], a[24:27], a[208:211], v[96:99]\n" \
"s_xor_b32 s73, s73, 0x80\n" \
"ds_read_b128 a[20:23], v206 offset:49152\n" \
"v_mul_f32 v116, v116, v233\n" \
"v_mfma_f32_16x16x32_bf16 v[96:99], a[28:31], a[212:215], v[96:99]\n" \
"buffer_load_dwordx4 v196, s[44:47], s75 offen lds\n" \
"v_mul_f32 v117, v117, v233\n" \
"v_mfma_f32_16x16x32_bf16 v[100:103], a[24:27], a[216:219], v[100:103]\n" \
"s_xor_b32 s75, s75, 0x80\n" \
"s_mul_i32 s76, s80, 393216\n" \
"ds_read_b64_tr_b16 a[54:55], v195 offset:15360\n" \
"v_mfma_f32_16x16x32_bf16 v[100:103], a[28:31], a[220:223], v[100:103]\n" \
"s_add_u32 s76, s76, s78\n" \
"ds_read_b128 a[24:27], v205 offset:53248\n" \
"v_mul_f32 v118, v118, v233\n" \
"v_mul_f32 v119, v119, v233\n" \
"v_mfma_f32_16x16x32_bf16 v[108:111], a[192:195], a[216:219], v[108:111]\n" \
"s_add_u32 m0, s91, 20480\n" \
"ds_read_b128 a[28:31], v206 offset:53248\n" \
"v_cvt_pk_bf16_f32 v120, v112, v113\n" \
"v_mfma_f32_16x16x32_bf16 v[108:111], a[196:199], a[220:223], v[108:111]\n" \
"buffer_load_dwordx4 v197, s[48:51], s76 offen offset:0 lds\n" \
"v_mfma_f32_16x16x32_bf16 v[104:107], a[192:195], a[208:211], v[104:107]\n" \
"v_cvt_pk_bf16_f32 v121, v114, v115\n" \
"v_cvt_pk_bf16_f32 v122, v116, v117\n" \
"v_mfma_f32_16x16x32_bf16 v[104:107], a[196:199], a[212:215], v[104:107]\n" \
"s_waitcnt lgkmcnt(2)\n" \
"v_cvt_pk_bf16_f32 v123, v118, v119\n" \
"v_mfma_f32_16x16x32_bf16 v[32:35], a[32:35], a[0:3], v[32:35]\n" \
"s_add_u32 s77, s76, 24576\n" \
"ds_read_b128 a[192:195], v205 offset:57344\n" \
"v_mfma_f32_16x16x32_bf16 v[32:35], a[36:39], a[4:7], v[32:35]\n" \
"buffer_load_dwordx4 v198, s[48:51], s76 offen offset:1024 lds\n" \
"v_mfma_f32_16x16x32_bf16 v[40:43], a[32:35], a[8:11], v[40:43]\n" \
"ds_read_b128 a[196:199], v206 offset:57344\n" \
"v_mfma_f32_16x16x32_bf16 v[40:43], a[36:39], a[12:15], v[40:43]\n" \
"ds_read_b64_tr_b16 a[200:201], v195 offset:1024\n" \
"buffer_store_dwordx4 v[120:123], v236, s[56:59], s82 offen offset:0 sc1\n" \
"v_mfma_f32_16x16x32_bf16 v[0:3], a[40:43], a[8:11], v[0:3]\n" \
"s_xor_b32 s80, s80, 0x1\n" \
"ds_read_b64_tr_b16 a[202:203], v195 offset:4096\n" \
"v_mfma_f32_16x16x32_bf16 v[224:227], v[244:247], v[120:123], v[224:227]\n" \
"v_accvgpr_read_b32 v112, a104\n" \
"v_accvgpr_read_b32 v113, a105\n" \
"v_mfma_f32_16x16x32_bf16 v[0:3], a[44:47], a[12:15], v[0:3]\n" \
"buffer_load_dwordx4 v199, s[48:51], s76 offen offset:2048 lds\n" \
"v_accvgpr_read_b32 v114, a106\n" \
"v_mfma_f32_16x16x32_bf16 v[4:7], a[48:51], a[8:11], v[4:7]\n" \
"ds_read_b64_tr_b16 a[204:205], v195 offset:13312\n" \
"v_accvgpr_read_b32 v115, a107\n" \
"v_mfma_f32_16x16x32_bf16 v[4:7], a[52:55], a[12:15], v[4:7]\n" \
"ds_read_b64_tr_b16 a[206:207], v195 offset:16384\n" \
"v_accvgpr_read_b32 v116, a108\n" \
"v_mfma_f32_16x16x32_bf16 v[12:15], a[48:51], a[16:19], v[12:15]\n" \
"s_add_u32 m0, m0, 3072\n" \
"ds_read_b64_tr_b16 a[208:209], v194 offset:2048\n" \
"v_accvgpr_read_b32 v117, a109\n" \
"v_mfma_f32_16x16x32_bf16 v[12:15], a[52:55], a[20:23], v[12:15]\n" \
"buffer_load_dwordx4 v197, s[48:51], s77 offen offset:0 lds\n" \
"v_accvgpr_read_b32 v118, a110\n" \
"v_mfma_f32_16x16x32_bf16 v[8:11], a[40:43], a[16:19], v[8:11]\n" \
"ds_read_b64_tr_b16 a[210:211], v194 offset:5120\n" \
"v_accvgpr_read_b32 v119, a111\n" \
"v_mfma_f32_16x16x32_bf16 v[8:11], a[44:47], a[20:23], v[8:11]\n" \
"ds_read_b64_tr_b16 a[212:213], v194 offset:14336\n" \
"v_add_f32 v112, v112, v168\n" \
"v_mfma_f32_16x16x32_bf16 v[48:51], a[32:35], a[16:19], v[48:51]\n" \
"s_waitcnt lgkmcnt(3)\n" \
"ds_read_b64_tr_b16 a[214:215], v194 offset:17408\n" \
"v_add_f32 v113, v113, v169\n" \
"v_mfma_f32_16x16x32_bf16 v[48:51], a[36:39], a[20:23], v[48:51]\n" \
"buffer_load_dwordx4 v198, s[48:51], s77 offen offset:1024 lds\n" \
"v_add_f32 v114, v114, v170\n" \
"v_mfma_f32_16x16x32_bf16 v[56:59], a[32:35], a[24:27], v[56:59]\n" \
"ds_read_b64_tr_b16 a[216:217], v195 offset:2048\n" \
"v_add_f32 v115, v115, v171\n" \
"v_mfma_f32_16x16x32_bf16 v[56:59], a[36:39], a[28:31], v[56:59]\n" \
"ds_read_b64_tr_b16 a[218:219], v195 offset:5120\n" \
"v_add_f32 v116, v116, v172\n" \
"v_add_f32 v117, v117, v173\n" \
"v_mfma_f32_16x16x32_bf16 v[16:19], a[40:43], a[24:27], v[16:19]\n" \
"ds_read_b64_tr_b16 a[220:221], v195 offset:14336\n" \
"v_add_f32 v118, v118, v174\n" \
"v_mfma_f32_16x16x32_bf16 v[16:19], a[44:47], a[28:31], v[16:19]\n" \
"buffer_load_dwordx4 v199, s[48:51], s77 offen offset:2048 lds\n" \
"v_mfma_f32_16x16x32_bf16 v[20:23], a[48:51], a[24:27], v[20:23]\n" \
"ds_read_b64_tr_b16 a[222:223], v195 offset:17408\n" \
"v_add_f32 v119, v119, v175\n" \
"v_mfma_f32_16x16x32_bf16 v[20:23], a[52:55], a[28:31], v[20:23]\n" \
"v_mul_f32 v112, v112, v233\n" \
"v_mul_f32 v113, v113, v233\n" \
"v_mul_f32 v114, v114, v233\n" \
"v_mfma_f32_16x16x32_bf16 v[28:31], a[48:51], a[192:195], v[28:31]\n" \
"v_mul_f32 v115, v115, v233\n" \
"v_mul_f32 v116, v116, v233\n" \
"v_mfma_f32_16x16x32_bf16 v[28:31], a[52:55], a[196:199], v[28:31]\n" \
"v_mul_f32 v117, v117, v233\n" \
"v_mul_f32 v118, v118, v233\n" \
"v_mfma_f32_16x16x32_bf16 v[24:27], a[40:43], a[192:195], v[24:27]\n" \
"v_mul_f32 v119, v119, v233\n" \
"v_cvt_pk_bf16_f32 v124, v112, v113\n" \
"v_mfma_f32_16x16x32_bf16 v[24:27], a[44:47], a[196:199], v[24:27]\n" \
"v_cvt_pk_bf16_f32 v125, v114, v115\n" \
"v_cvt_pk_bf16_f32 v126, v116, v117\n" \
"v_cvt_pk_bf16_f32 v127, v118, v119\n" \
"v_mfma_f32_16x16x32_bf16 v[64:67], a[32:35], a[192:195], v[64:67]\n" \
"buffer_store_dwordx4 v[124:127], v236, s[56:59], s82 offen offset:2048 sc1\n" \
"v_mfma_f32_16x16x32_bf16 v[64:67], a[36:39], a[196:199], v[64:67]\n" \
"v_mfma_f32_16x16x32_bf16 v[224:227], v[244:247], v[124:127], v[224:227]\n" \
"v_accvgpr_read_b32 v112, a112\n" \
"s_waitcnt vmcnt(14) lgkmcnt(0)\n" \
"s_barrier\n" \
"v_mfma_f32_16x16x32_bf16 v[68:71], a[200:203], a[192:195], v[68:71]\n" \
"ds_read_b64_tr_b16 a[32:33], v209 offset:1024\n" \
"ds_read_b64_tr_b16 a[34:35], v209 offset:4096\n" \
"v_mfma_f32_16x16x32_bf16 v[68:71], a[204:207], a[196:199], v[68:71]\n" \
"ds_read_b64_tr_b16 a[36:37], v209 offset:13312\n" \
"ds_read_b64_tr_b16 a[38:39], v209 offset:16384\n" \
"v_mfma_f32_16x16x32_bf16 v[60:63], a[200:203], a[24:27], v[60:63]\n" \
"s_add_u32 m0, s79, 110592\n" \
"ds_read_b64_tr_b16 a[40:41], v209 offset:0\n" \
"v_accvgpr_read_b32 v113, a113\n" \
"v_mfma_f32_16x16x32_bf16 v[60:63], a[204:207], a[28:31], v[60:63]\n" \
"buffer_load_dwordx4 v192, s[44:47], s73 offen offset:0 lds\n" \
"v_accvgpr_read_b32 v114, a114\n" \
"v_mfma_f32_16x16x32_bf16 v[52:55], a[200:203], a[16:19], v[52:55]\n" \
"Lpg7961_%=:\n" \
"Lst1x0_%=:\n" \
"s_cbranch_execz Lpg10257_%=\n" \
"ds_read_b64_tr_b16 a[42:43], v209 offset:3072\n" \
"v_accvgpr_read_b32 v115, a115\n" \
"v_mfma_f32_16x16x32_bf16 v[52:55], a[204:207], a[20:23], v[52:55]\n" \
"ds_read_b64_tr_b16 a[44:45], v209 offset:12288\n" \
"v_accvgpr_read_b32 v116, a116\n" \
"v_mfma_f32_16x16x32_bf16 v[44:47], a[200:203], a[8:11], v[44:47]\n" \
"ds_read_b64_tr_b16 a[46:47], v209 offset:15360\n" \
"v_accvgpr_read_b32 v117, a117\n" \
"v_mfma_f32_16x16x32_bf16 v[44:47], a[204:207], a[12:15], v[44:47]\n" \
"buffer_load_dwordx4 v193, s[44:47], s73 offen offset:1024 lds\n" \
"v_accvgpr_read_b32 v118, a118\n" \
"v_mfma_f32_16x16x32_bf16 v[72:75], a[0:3], a[208:211], v[72:75]\n" \
"ds_read_b64_tr_b16 a[48:49], v210 offset:0\n" \
"v_accvgpr_read_b32 v119, a119\n" \
"v_mfma_f32_16x16x32_bf16 v[72:75], a[4:7], a[212:215], v[72:75]\n" \
"ds_read_b128 a[0:3], v207 offset:0\n" \
"v_add_f32 v112, v112, v176\n" \
"v_add_f32 v113, v113, v177\n" \
"v_mfma_f32_16x16x32_bf16 v[80:83], a[8:11], a[208:211], v[80:83]\n" \
"ds_read_b128 a[4:7], v208 offset:0\n" \
"v_add_f32 v114, v114, v178\n" \
"v_mfma_f32_16x16x32_bf16 v[80:83], a[12:15], a[212:215], v[80:83]\n" \
"buffer_load_dwordx4 v192, s[44:47], s73 offen offset:2048 lds\n" \
"v_mfma_f32_16x16x32_bf16 v[84:87], a[8:11], a[216:219], v[84:87]\n" \
"ds_read_b64_tr_b16 a[50:51], v210 offset:3072\n" \
"v_add_f32 v115, v115, v179\n" \
"v_mfma_f32_16x16x32_bf16 v[84:87], a[12:15], a[220:223], v[84:87]\n" \
"ds_read_b128 a[8:11], v205 offset:0\n" \
"v_add_f32 v116, v116, v180\n" \
"v_add_f32 v117, v117, v181\n" \
"v_mfma_f32_16x16x32_bf16 v[92:95], a[16:19], a[216:219], v[92:95]\n" \
"ds_read_b128 a[12:15], v206 offset:0\n" \
"v_add_f32 v118, v118, v182\n" \
"v_mfma_f32_16x16x32_bf16 v[92:95], a[20:23], a[220:223], v[92:95]\n" \
"buffer_load_dwordx4 v193, s[44:47], s73 offen offset:3072 lds\n" \
"v_mfma_f32_16x16x32_bf16 v[88:91], a[16:19], a[208:211], v[88:91]\n" \
"ds_read_b64_tr_b16 a[52:53], v210 offset:12288\n" \
"v_add_f32 v119, v119, v183\n" \
"v_mfma_f32_16x16x32_bf16 v[88:91], a[20:23], a[212:215], v[88:91]\n" \
"s_add_u32 m0, s89, 110592\n" \
"ds_read_b128 a[16:19], v205 offset:4096\n" \
"v_mul_f32 v112, v112, v233\n" \
"v_mul_f32 v113, v113, v233\n" \
"v_mfma_f32_16x16x32_bf16 v[96:99], a[24:27], a[208:211], v[96:99]\n" \
"s_xor_b32 s73, s73, 0x180\n" \
"ds_read_b128 a[20:23], v206 offset:4096\n" \
"v_mul_f32 v114, v114, v233\n" \
"v_mfma_f32_16x16x32_bf16 v[96:99], a[28:31], a[212:215], v[96:99]\n" \
"buffer_load_dwordx4 v196, s[44:47], s75 offen lds\n" \
"v_mul_f32 v115, v115, v233\n" \
"v_mfma_f32_16x16x32_bf16 v[100:103], a[24:27], a[216:219], v[100:103]\n" \
"s_xor_b32 s75, s75, 0x180\n" \
"s_mul_i32 s76, s80, 393216\n" \
"ds_read_b64_tr_b16 a[54:55], v210 offset:15360\n" \
"v_mfma_f32_16x16x32_bf16 v[100:103], a[28:31], a[220:223], v[100:103]\n" \
"s_add_u32 s76, s76, s78\n" \
"ds_read_b128 a[24:27], v205 offset:8192\n" \
"v_mul_f32 v116, v116, v233\n" \
"v_mfma_f32_16x16x32_bf16 v[108:111], a[192:195], a[216:219], v[108:111]\n" \
"s_add_u32 m0, s91, 139264\n" \
"ds_read_b128 a[28:31], v206 offset:8192\n" \
"v_mul_f32 v117, v117, v233\n" \
"v_mfma_f32_16x16x32_bf16 v[108:111], a[196:199], a[220:223], v[108:111]\n" \
"buffer_load_dwordx4 v197, s[48:51], s76 offen offset:0 lds\n" \
"v_mul_f32 v118, v118, v233\n" \
"v_mfma_f32_16x16x32_bf16 v[104:107], a[192:195], a[208:211], v[104:107]\n" \
"v_mul_f32 v119, v119, v233\n" \
"v_cvt_pk_bf16_f32 v120, v112, v113\n" \
"v_mfma_f32_16x16x32_bf16 v[104:107], a[196:199], a[212:215], v[104:107]\n" \
"s_waitcnt lgkmcnt(2)\n" \
"v_cvt_pk_bf16_f32 v121, v114, v115\n" \
"v_cvt_pk_bf16_f32 v122, v116, v117\n" \
"v_mfma_f32_16x16x32_bf16 v[32:35], a[32:35], a[0:3], v[32:35]\n" \
"s_add_u32 s77, s76, 24576\n" \
"ds_read_b128 a[192:195], v205 offset:12288\n" \
"v_cvt_pk_bf16_f32 v123, v118, v119\n" \
"v_mfma_f32_16x16x32_bf16 v[32:35], a[36:39], a[4:7], v[32:35]\n" \
"buffer_load_dwordx4 v198, s[48:51], s76 offen offset:1024 lds\n" \
"v_mfma_f32_16x16x32_bf16 v[40:43], a[32:35], a[8:11], v[40:43]\n" \
"ds_read_b128 a[196:199], v206 offset:12288\n" \
"v_mfma_f32_16x16x32_bf16 v[40:43], a[36:39], a[12:15], v[40:43]\n" \
"ds_read_b64_tr_b16 a[200:201], v210 offset:1024\n" \
"buffer_store_dwordx4 v[120:123], v240, s[56:59], s82 offen offset:0 sc1\n" \
"v_mfma_f32_16x16x32_bf16 v[0:3], a[40:43], a[8:11], v[0:3]\n" \
"s_xor_b32 s80, s80, 0x3\n" \
"ds_read_b64_tr_b16 a[202:203], v210 offset:4096\n" \
"v_mfma_f32_16x16x32_bf16 v[224:227], v[244:247], v[120:123], v[224:227]\n" \
"v_accvgpr_read_b32 v112, a120\n" \
"v_accvgpr_read_b32 v113, a121\n" \
"v_mfma_f32_16x16x32_bf16 v[0:3], a[44:47], a[12:15], v[0:3]\n" \
"buffer_load_dwordx4 v199, s[48:51], s76 offen offset:2048 lds\n" \
"v_mfma_f32_16x16x32_bf16 v[4:7], a[48:51], a[8:11], v[4:7]\n" \
"ds_read_b64_tr_b16 a[204:205], v210 offset:13312\n" \
"v_accvgpr_read_b32 v114, a122\n" \
"v_mfma_f32_16x16x32_bf16 v[4:7], a[52:55], a[12:15], v[4:7]\n" \
"ds_read_b64_tr_b16 a[206:207], v210 offset:16384\n" \
"v_accvgpr_read_b32 v115, a123\n" \
"v_accvgpr_read_b32 v116, a124\n" \
"v_mfma_f32_16x16x32_bf16 v[12:15], a[48:51], a[16:19], v[12:15]\n" \
"s_add_u32 m0, m0, 3072\n" \
"ds_read_b64_tr_b16 a[208:209], v209 offset:2048\n" \
"v_accvgpr_read_b32 v117, a125\n" \
"v_mfma_f32_16x16x32_bf16 v[12:15], a[52:55], a[20:23], v[12:15]\n" \
"buffer_load_dwordx4 v197, s[48:51], s77 offen offset:0 lds\n" \
"v_mfma_f32_16x16x32_bf16 v[8:11], a[40:43], a[16:19], v[8:11]\n" \
"ds_read_b64_tr_b16 a[210:211], v209 offset:5120\n" \
"v_accvgpr_read_b32 v118, a126\n" \
"v_mfma_f32_16x16x32_bf16 v[8:11], a[44:47], a[20:23], v[8:11]\n" \
"ds_read_b64_tr_b16 a[212:213], v209 offset:14336\n" \
"v_accvgpr_read_b32 v119, a127\n" \
"v_add_f32 v112, v112, v184\n" \
"v_mfma_f32_16x16x32_bf16 v[48:51], a[32:35], a[16:19], v[48:51]\n" \
"s_waitcnt lgkmcnt(3)\n" \
"ds_read_b64_tr_b16 a[214:215], v209 offset:17408\n" \
"v_add_f32 v113, v113, v185\n" \
"v_mfma_f32_16x16x32_bf16 v[48:51], a[36:39], a[20:23], v[48:51]\n" \
"buffer_load_dwordx4 v198, s[48:51], s77 offen offset:1024 lds\n" \
"v_add_f32 v114, v114, v186\n" \
"v_mfma_f32_16x16x32_bf16 v[56:59], a[32:35], a[24:27], v[56:59]\n" \
"ds_read_b64_tr_b16 a[216:217], v210 offset:2048\n" \
"v_add_f32 v115, v115, v187\n" \
"v_mfma_f32_16x16x32_bf16 v[56:59], a[36:39], a[28:31], v[56:59]\n" \
"ds_read_b64_tr_b16 a[218:219], v210 offset:5120\n" \
"v_add_f32 v116, v116, v188\n" \
"v_mfma_f32_16x16x32_bf16 v[16:19], a[40:43], a[24:27], v[16:19]\n" \
"ds_read_b64_tr_b16 a[220:221], v210 offset:14336\n" \
"v_add_f32 v117, v117, v189\n" \
"v_mfma_f32_16x16x32_bf16 v[16:19], a[44:47], a[28:31], v[16:19]\n" \
"buffer_load_dwordx4 v199, s[48:51], s77 offen offset:2048 lds\n" \
"v_add_f32 v118, v118, v190\n" \
"v_mfma_f32_16x16x32_bf16 v[20:23], a[48:51], a[24:27], v[20:23]\n" \
"ds_read_b64_tr_b16 a[222:223], v210 offset:17408\n" \
"v_add_f32 v119, v119, v191\n" \
"v_mfma_f32_16x16x32_bf16 v[20:23], a[52:55], a[28:31], v[20:23]\n" \
"v_mul_f32 v112, v112, v233\n" \
"v_mul_f32 v113, v113, v233\n" \
"v_mfma_f32_16x16x32_bf16 v[28:31], a[48:51], a[192:195], v[28:31]\n" \
"v_mul_f32 v114, v114, v233\n" \
"v_mul_f32 v115, v115, v233\n" \
"v_mfma_f32_16x16x32_bf16 v[28:31], a[52:55], a[196:199], v[28:31]\n" \
"v_mul_f32 v116, v116, v233\n" \
"v_mul_f32 v117, v117, v233\n" \
"v_mul_f32 v118, v118, v233\n" \
"v_mfma_f32_16x16x32_bf16 v[24:27], a[40:43], a[192:195], v[24:27]\n" \
"v_mul_f32 v119, v119, v233\n" \
"v_cvt_pk_bf16_f32 v124, v112, v113\n" \
"v_mfma_f32_16x16x32_bf16 v[24:27], a[44:47], a[196:199], v[24:27]\n" \
"v_cvt_pk_bf16_f32 v125, v114, v115\n" \
"v_cvt_pk_bf16_f32 v126, v116, v117\n" \
"v_cvt_pk_bf16_f32 v127, v118, v119\n" \
"v_mfma_f32_16x16x32_bf16 v[64:67], a[32:35], a[192:195], v[64:67]\n" \
"buffer_store_dwordx4 v[124:127], v240, s[56:59], s82 offen offset:2048 sc1\n" \
"v_mfma_f32_16x16x32_bf16 v[64:67], a[36:39], a[196:199], v[64:67]\n" \
"v_mfma_f32_16x16x32_bf16 v[224:227], v[244:247], v[124:127], v[224:227]\n" \
"s_waitcnt vmcnt(14) lgkmcnt(0)\n" \
"s_barrier\n" \
"v_mfma_f32_16x16x32_bf16 v[68:71], a[200:203], a[192:195], v[68:71]\n" \
"ds_read_b64_tr_b16 a[32:33], v204 offset:21504\n" \
"ds_read_b64_tr_b16 a[34:35], v204 offset:24576\n" \
"v_mfma_f32_16x16x32_bf16 v[68:71], a[204:207], a[196:199], v[68:71]\n" \
"ds_read_b64_tr_b16 a[36:37], v204 offset:33792\n" \
"ds_read_b64_tr_b16 a[38:39], v204 offset:36864\n" \
"v_mfma_f32_16x16x32_bf16 v[60:63], a[200:203], a[24:27], v[60:63]\n" \
"s_add_u32 m0, s79, 65536\n" \
"ds_read_b64_tr_b16 a[40:41], v204 offset:20480\n" \
"v_mfma_f32_16x16x32_bf16 v[60:63], a[204:207], a[28:31], v[60:63]\n" \
"buffer_load_dwordx4 v192, s[44:47], s73 offen offset:0 lds\n" \
"v_mfma_f32_16x16x32_bf16 v[52:55], a[200:203], a[16:19], v[52:55]\n" \
"ds_read_b64_tr_b16 a[42:43], v204 offset:23552\n" \
"v_mfma_f32_16x16x32_bf16 v[52:55], a[204:207], a[20:23], v[52:55]\n" \
"ds_read_b64_tr_b16 a[44:45], v204 offset:32768\n" \
"v_mfma_f32_16x16x32_bf16 v[44:47], a[200:203], a[8:11], v[44:47]\n" \
"ds_read_b64_tr_b16 a[46:47], v204 offset:35840\n" \
"v_mfma_f32_16x16x32_bf16 v[44:47], a[204:207], a[12:15], v[44:47]\n" \
"buffer_load_dwordx4 v193, s[44:47], s73 offen offset:1024 lds\n" \
"v_mfma_f32_16x16x32_bf16 v[72:75], a[0:3], a[208:211], v[72:75]\n" \
"ds_read_b64_tr_b16 a[48:49], v222 offset:20480\n" \
"v_mfma_f32_16x16x32_bf16 v[72:75], a[4:7], a[212:215], v[72:75]\n" \
"ds_read_b128 a[0:3], v202 offset:0\n" \
"v_mfma_f32_16x16x32_bf16 v[80:83], a[8:11], a[208:211], v[80:83]\n" \
"ds_read_b128 a[4:7], v203 offset:0\n" \
"v_mfma_f32_16x16x32_bf16 v[80:83], a[12:15], a[212:215], v[80:83]\n" \
"buffer_load_dwordx4 v192, s[44:47], s73 offen offset:2048 lds\n" \
"v_mfma_f32_16x16x32_bf16 v[84:87], a[8:11], a[216:219], v[84:87]\n" \
"ds_read_b64_tr_b16 a[50:51], v222 offset:23552\n" \
"v_mfma_f32_16x16x32_bf16 v[84:87], a[12:15], a[220:223], v[84:87]\n" \
"ds_read_b128 a[8:11], v200 offset:0\n" \
"v_mfma_f32_16x16x32_bf16 v[92:95], a[16:19], a[216:219], v[92:95]\n" \
"ds_read_b128 a[12:15], v201 offset:0\n" \
"v_mfma_f32_16x16x32_bf16 v[92:95], a[20:23], a[220:223], v[92:95]\n" \
"buffer_load_dwordx4 v193, s[44:47], s73 offen offset:3072 lds\n" \
"v_mfma_f32_16x16x32_bf16 v[88:91], a[16:19], a[208:211], v[88:91]\n" \
"ds_read_b64_tr_b16 a[52:53], v222 offset:32768\n" \
"v_mfma_f32_16x16x32_bf16 v[88:91], a[20:23], a[212:215], v[88:91]\n" \
"s_add_u32 m0, s89, 65536\n" \
"ds_read_b128 a[16:19], v200 offset:4096\n" \
"v_mfma_f32_16x16x32_bf16 v[96:99], a[24:27], a[208:211], v[96:99]\n" \
"s_xor_b32 s73, s73, 0x80\n" \
"ds_read_b128 a[20:23], v201 offset:4096\n" \
"v_mfma_f32_16x16x32_bf16 v[96:99], a[28:31], a[212:215], v[96:99]\n" \
"buffer_load_dwordx4 v196, s[44:47], s75 offen lds\n" \
"v_mfma_f32_16x16x32_bf16 v[100:103], a[24:27], a[216:219], v[100:103]\n" \
"s_xor_b32 s75, s75, 0x80\n" \
"s_mul_i32 s76, s80, 393216\n" \
"ds_read_b64_tr_b16 a[54:55], v222 offset:35840\n" \
"v_mfma_f32_16x16x32_bf16 v[100:103], a[28:31], a[220:223], v[100:103]\n" \
"s_add_u32 s76, s76, s78\n" \
"ds_read_b128 a[24:27], v200 offset:8192\n" \
"v_mfma_f32_16x16x32_bf16 v[108:111], a[192:195], a[216:219], v[108:111]\n" \
"s_add_u32 m0, s91, 86016\n" \
"ds_read_b128 a[28:31], v201 offset:8192\n" \
"v_mfma_f32_16x16x32_bf16 v[108:111], a[196:199], a[220:223], v[108:111]\n" \
"buffer_load_dwordx4 v197, s[48:51], s76 offen offset:0 lds\n" \
"v_mfma_f32_16x16x32_bf16 v[104:107], a[192:195], a[208:211], v[104:107]\n" \
"v_mfma_f32_16x16x32_bf16 v[104:107], a[196:199], a[212:215], v[104:107]\n" \
"s_waitcnt lgkmcnt(2)\n" \
"v_mfma_f32_16x16x32_bf16 v[32:35], a[32:35], a[0:3], v[32:35]\n" \
"s_add_u32 s77, s76, 24576\n" \
"ds_read_b128 a[192:195], v200 offset:12288\n" \
"v_mfma_f32_16x16x32_bf16 v[32:35], a[36:39], a[4:7], v[32:35]\n" \
"buffer_load_dwordx4 v198, s[48:51], s76 offen offset:1024 lds\n" \
"v_mfma_f32_16x16x32_bf16 v[40:43], a[32:35], a[8:11], v[40:43]\n" \
"ds_read_b128 a[196:199], v201 offset:12288\n" \
"v_mfma_f32_16x16x32_bf16 v[40:43], a[36:39], a[12:15], v[40:43]\n" \
"ds_read_b64_tr_b16 a[200:201], v222 offset:21504\n" \
"v_mfma_f32_16x16x32_bf16 v[0:3], a[40:43], a[8:11], v[0:3]\n" \
"s_xor_b32 s80, s80, 0x1\n" \
"ds_read_b64_tr_b16 a[202:203], v222 offset:24576\n" \
"v_mfma_f32_16x16x32_bf16 v[0:3], a[44:47], a[12:15], v[0:3]\n" \
"buffer_load_dwordx4 v199, s[48:51], s76 offen offset:2048 lds\n" \
"v_mfma_f32_16x16x32_bf16 v[4:7], a[48:51], a[8:11], v[4:7]\n" \
"ds_read_b64_tr_b16 a[204:205], v222 offset:33792\n" \
"v_mfma_f32_16x16x32_bf16 v[4:7], a[52:55], a[12:15], v[4:7]\n" \
"ds_read_b64_tr_b16 a[206:207], v222 offset:36864\n" \
"v_mfma_f32_16x16x32_bf16 v[12:15], a[48:51], a[16:19], v[12:15]\n" \
"s_add_u32 m0, m0, 3072\n" \
"ds_read_b64_tr_b16 a[208:209], v204 offset:22528\n" \
"v_mfma_f32_16x16x32_bf16 v[12:15], a[52:55], a[20:23], v[12:15]\n" \
"buffer_load_dwordx4 v197, s[48:51], s77 offen offset:0 lds\n" \
"v_mfma_f32_16x16x32_bf16 v[8:11], a[40:43], a[16:19], v[8:11]\n" \
"ds_read_b64_tr_b16 a[210:211], v204 offset:25600\n" \
"v_mfma_f32_16x16x32_bf16 v[8:11], a[44:47], a[20:23], v[8:11]\n" \
"ds_read_b64_tr_b16 a[212:213], v204 offset:34816\n" \
"v_mfma_f32_16x16x32_bf16 v[48:51], a[32:35], a[16:19], v[48:51]\n" \
"s_waitcnt lgkmcnt(3)\n" \
"ds_read_b64_tr_b16 a[214:215], v204 offset:37888\n" \
"v_mfma_f32_16x16x32_bf16 v[48:51], a[36:39], a[20:23], v[48:51]\n" \
"buffer_load_dwordx4 v198, s[48:51], s77 offen offset:1024 lds\n" \
"v_mfma_f32_16x16x32_bf16 v[56:59], a[32:35], a[24:27], v[56:59]\n" \
"ds_read_b64_tr_b16 a[216:217], v222 offset:22528\n" \
"v_mfma_f32_16x16x32_bf16 v[56:59], a[36:39], a[28:31], v[56:59]\n" \
"ds_read_b64_tr_b16 a[218:219], v222 offset:25600\n" \
"v_mfma_f32_16x16x32_bf16 v[16:19], a[40:43], a[24:27], v[16:19]\n" \
"ds_read_b64_tr_b16 a[220:221], v222 offset:34816\n" \
"v_mfma_f32_16x16x32_bf16 v[16:19], a[44:47], a[28:31], v[16:19]\n" \
"buffer_load_dwordx4 v199, s[48:51], s77 offen offset:2048 lds\n" \
"v_mfma_f32_16x16x32_bf16 v[20:23], a[48:51], a[24:27], v[20:23]\n" \
"ds_read_b64_tr_b16 a[222:223], v222 offset:37888\n" \
"v_mfma_f32_16x16x32_bf16 v[20:23], a[52:55], a[28:31], v[20:23]\n" \
"v_mfma_f32_16x16x32_bf16 v[28:31], a[48:51], a[192:195], v[28:31]\n" \
"v_mfma_f32_16x16x32_bf16 v[28:31], a[52:55], a[196:199], v[28:31]\n" \
"v_mfma_f32_16x16x32_bf16 v[24:27], a[40:43], a[192:195], v[24:27]\n" \
"v_mfma_f32_16x16x32_bf16 v[24:27], a[44:47], a[196:199], v[24:27]\n" \
"v_mfma_f32_16x16x32_bf16 v[64:67], a[32:35], a[192:195], v[64:67]\n" \
"v_mfma_f32_16x16x32_bf16 v[64:67], a[36:39], a[196:199], v[64:67]\n" \
"s_waitcnt vmcnt(12) lgkmcnt(0)\n" \
"s_barrier\n" \
"s_movk_i32 s64, 0x207\n" \
"v_cmp_class_f32 vcc, v224, s64\n" \
"s_nop 1\n" \
"s_cmp_lg_u64 vcc, 0\n" \
"s_addc_u32 s90, s90, 0\n" \
"s_cmp_lg_u32 s90, 0\n" \
"s_cbranch_scc1 Lexact_%=\n" \
"Lexr_%=:\n" \
"v_mfma_f32_16x16x32_bf16 v[68:71], a[200:203], a[192:195], v[68:71]\n" \
"s_lshl_b32 s15, s63, 7\n" \
"ds_read_b64_tr_b16 a[32:33], v194 offset:1024\n" \
"ds_read_b64_tr_b16 a[34:35], v194 offset:4096\n" \
"v_mfma_f32_16x16x32_bf16 v[68:71], a[204:207], a[196:199], v[68:71]\n" \
"s_lshl2_add_u32 s16, s62, 65589248\n" \
"ds_read_b64_tr_b16 a[36:37], v194 offset:13312\n" \
"ds_read_b64_tr_b16 a[38:39], v194 offset:16384\n" \
"v_mfma_f32_16x16x32_bf16 v[60:63], a[200:203], a[24:27], v[60:63]\n" \
"s_add_u32 m0, s79, 0\n" \
"s_sub_u32 s17, s16, s15\n" \
"ds_read_b64_tr_b16 a[40:41], v194 offset:0\n" \
"v_mfma_f32_16x16x32_bf16 v[60:63], a[204:207], a[28:31], v[60:63]\n" \
"buffer_load_dwordx4 v192, s[44:47], s73 offen offset:0 lds\n" \
"v_mfma_f32_16x16x32_bf16 v[52:55], a[200:203], a[16:19], v[52:55]\n" \
"s_add_u32 s15, s15, s16\n" \
"s_add_u32 s17, s17, 1920\n" \
"ds_read_b64_tr_b16 a[42:43], v194 offset:3072\n" \
"v_mfma_f32_16x16x32_bf16 v[52:55], a[204:207], a[20:23], v[52:55]\n" \
"ds_read_b64_tr_b16 a[44:45], v194 offset:12288\n" \
"v_mov_b32 v146, s60\n" \
"v_mfma_f32_16x16x32_bf16 v[44:47], a[200:203], a[8:11], v[44:47]\n" \
"ds_read_b64_tr_b16 a[46:47], v194 offset:15360\n" \
"v_lshlrev_b32 v147, 28, s61\n" \
"v_mfma_f32_16x16x32_bf16 v[44:47], a[204:207], a[12:15], v[44:47]\n" \
"buffer_load_dwordx4 v193, s[44:47], s73 offen offset:1024 lds\n" \
"v_mfma_f32_16x16x32_bf16 v[72:75], a[0:3], a[208:211], v[72:75]\n" \
"ds_read_b64_tr_b16 a[48:49], v195 offset:0\n" \
"v_mfma_f32_16x16x32_bf16 v[72:75], a[4:7], a[212:215], v[72:75]\n" \
"ds_read_b128 a[0:3], v207 offset:45056\n" \
"v_mfma_f32_16x16x32_bf16 v[80:83], a[8:11], a[208:211], v[80:83]\n" \
"ds_read_b128 a[4:7], v208 offset:45056\n" \
"v_mfma_f32_16x16x32_bf16 v[80:83], a[12:15], a[212:215], v[80:83]\n" \
"buffer_load_dwordx4 v192, s[44:47], s73 offen offset:2048 lds\n" \
"v_mfma_f32_16x16x32_bf16 v[84:87], a[8:11], a[216:219], v[84:87]\n" \
"ds_read_b64_tr_b16 a[50:51], v195 offset:3072\n" \
"v_mfma_f32_16x16x32_bf16 v[84:87], a[12:15], a[220:223], v[84:87]\n" \
"ds_read_b128 a[8:11], v205 offset:45056\n" \
"v_mfma_f32_16x16x32_bf16 v[92:95], a[16:19], a[216:219], v[92:95]\n" \
"ds_read_b128 a[12:15], v206 offset:45056\n" \
"v_mfma_f32_16x16x32_bf16 v[92:95], a[20:23], a[220:223], v[92:95]\n" \
"buffer_load_dwordx4 v193, s[44:47], s73 offen offset:3072 lds\n" \
"v_mfma_f32_16x16x32_bf16 v[88:91], a[16:19], a[208:211], v[88:91]\n" \
"ds_read_b64_tr_b16 a[52:53], v195 offset:12288\n" \
"v_mfma_f32_16x16x32_bf16 v[88:91], a[20:23], a[212:215], v[88:91]\n" \
"s_add_u32 m0, s89, 0\n" \
"ds_read_b128 a[16:19], v205 offset:49152\n" \
"v_mfma_f32_16x16x32_bf16 v[96:99], a[24:27], a[208:211], v[96:99]\n" \
"s_xor_b32 s73, s73, 0x780\n" \
"ds_read_b128 a[20:23], v206 offset:49152\n" \
"v_mfma_f32_16x16x32_bf16 v[96:99], a[28:31], a[212:215], v[96:99]\n" \
"buffer_load_dwordx4 v196, s[44:47], s75 offen lds\n" \
"v_mfma_f32_16x16x32_bf16 v[100:103], a[24:27], a[216:219], v[100:103]\n" \
"s_xor_b32 s75, s75, 0x780\n" \
"s_mul_i32 s76, s80, 393216\n" \
"ds_read_b64_tr_b16 a[54:55], v195 offset:15360\n" \
"v_mfma_f32_16x16x32_bf16 v[100:103], a[28:31], a[220:223], v[100:103]\n" \
"s_add_u32 s76, s76, s78\n" \
"ds_read_b128 a[24:27], v205 offset:53248\n" \
"v_mfma_f32_16x16x32_bf16 v[108:111], a[192:195], a[216:219], v[108:111]\n" \
"s_add_u32 m0, s91, 20480\n" \
"ds_read_b128 a[28:31], v206 offset:53248\n" \
"v_mfma_f32_16x16x32_bf16 v[108:111], a[196:199], a[220:223], v[108:111]\n" \
"buffer_load_dwordx4 v197, s[48:51], s76 offen offset:0 lds\n" \
"v_mfma_f32_16x16x32_bf16 v[104:107], a[192:195], a[208:211], v[104:107]\n" \
"v_mfma_f32_16x16x32_bf16 v[104:107], a[196:199], a[212:215], v[104:107]\n" \
"s_waitcnt lgkmcnt(2)\n" \
"v_mfma_f32_16x16x32_bf16 v[32:35], a[32:35], a[0:3], v[32:35]\n" \
"s_add_u32 s77, s76, 24576\n" \
"ds_read_b128 a[192:195], v205 offset:57344\n" \
"v_mfma_f32_16x16x32_bf16 v[32:35], a[36:39], a[4:7], v[32:35]\n" \
"buffer_load_dwordx4 v198, s[48:51], s76 offen offset:1024 lds\n" \
"v_mfma_f32_16x16x32_bf16 v[40:43], a[32:35], a[8:11], v[40:43]\n" \
"ds_read_b128 a[196:199], v206 offset:57344\n" \
"v_mfma_f32_16x16x32_bf16 v[40:43], a[36:39], a[12:15], v[40:43]\n" \
"ds_read_b64_tr_b16 a[200:201], v195 offset:1024\n" \
"v_mfma_f32_16x16x32_bf16 v[0:3], a[40:43], a[8:11], v[0:3]\n" \
"s_xor_b32 s80, s80, 0xf\n" \
"ds_read_b64_tr_b16 a[202:203], v195 offset:4096\n" \
"v_mfma_f32_16x16x32_bf16 v[0:3], a[44:47], a[12:15], v[0:3]\n" \
"buffer_load_dwordx4 v199, s[48:51], s76 offen offset:2048 lds\n" \
"v_mfma_f32_16x16x32_bf16 v[4:7], a[48:51], a[8:11], v[4:7]\n" \
"ds_read_b64_tr_b16 a[204:205], v195 offset:13312\n" \
"v_mfma_f32_16x16x32_bf16 v[4:7], a[52:55], a[12:15], v[4:7]\n" \
"ds_read_b64_tr_b16 a[206:207], v195 offset:16384\n" \
"v_mfma_f32_16x16x32_bf16 v[12:15], a[48:51], a[16:19], v[12:15]\n" \
"s_add_u32 m0, m0, 3072\n" \
"ds_read_b64_tr_b16 a[208:209], v194 offset:2048\n" \
"v_mfma_f32_16x16x32_bf16 v[12:15], a[52:55], a[20:23], v[12:15]\n" \
"buffer_load_dwordx4 v197, s[48:51], s77 offen offset:0 lds\n" \
"v_mfma_f32_16x16x32_bf16 v[8:11], a[40:43], a[16:19], v[8:11]\n" \
"ds_read_b64_tr_b16 a[210:211], v194 offset:5120\n" \
"v_mfma_f32_16x16x32_bf16 v[8:11], a[44:47], a[20:23], v[8:11]\n" \
"ds_read_b64_tr_b16 a[212:213], v194 offset:14336\n" \
"v_mfma_f32_16x16x32_bf16 v[48:51], a[32:35], a[16:19], v[48:51]\n" \
"s_waitcnt lgkmcnt(3)\n" \
"ds_read_b64_tr_b16 a[214:215], v194 offset:17408\n" \
"v_mfma_f32_16x16x32_bf16 v[48:51], a[36:39], a[20:23], v[48:51]\n" \
"buffer_load_dwordx4 v198, s[48:51], s77 offen offset:1024 lds\n" \
"v_mfma_f32_16x16x32_bf16 v[56:59], a[32:35], a[24:27], v[56:59]\n" \
"ds_read_b64_tr_b16 a[216:217], v195 offset:2048\n" \
"v_mfma_f32_16x16x32_bf16 v[56:59], a[36:39], a[28:31], v[56:59]\n" \
"ds_read_b64_tr_b16 a[218:219], v195 offset:5120\n" \
"v_mfma_f32_16x16x32_bf16 v[16:19], a[40:43], a[24:27], v[16:19]\n" \
"ds_read_b64_tr_b16 a[220:221], v195 offset:14336\n" \
"v_mfma_f32_16x16x32_bf16 v[16:19], a[44:47], a[28:31], v[16:19]\n" \
"buffer_load_dwordx4 v199, s[48:51], s77 offen offset:2048 lds\n" \
"v_mfma_f32_16x16x32_bf16 v[20:23], a[48:51], a[24:27], v[20:23]\n" \
"ds_read_b64_tr_b16 a[222:223], v195 offset:17408\n" \
"v_mfma_f32_16x16x32_bf16 v[20:23], a[52:55], a[28:31], v[20:23]\n" \
"v_mfma_f32_16x16x32_bf16 v[28:31], a[48:51], a[192:195], v[28:31]\n" \
"v_add_u32 v254, 3145728, v254\n" \
"v_mfma_f32_16x16x32_bf16 v[28:31], a[52:55], a[196:199], v[28:31]\n" \
"global_load_dword v253, v254, s[22:23]\n" \
"v_mfma_f32_16x16x32_bf16 v[24:27], a[40:43], a[192:195], v[24:27]\n" \
"v_mfma_f32_16x16x32_bf16 v[24:27], a[44:47], a[196:199], v[24:27]\n" \
"v_mfma_f32_16x16x32_bf16 v[64:67], a[32:35], a[192:195], v[64:67]\n" \
"v_mfma_f32_16x16x32_bf16 v[64:67], a[36:39], a[196:199], v[64:67]\n" \
"s_waitcnt vmcnt(12) lgkmcnt(0)\n" \
"s_barrier\n" \
"v_mfma_f32_16x16x32_bf16 v[68:71], a[200:203], a[192:195], v[68:71]\n" \
"ds_read_b64_tr_b16 a[32:33], v209 offset:1024\n" \
"ds_read_b64_tr_b16 a[34:35], v209 offset:4096\n" \
"v_mfma_f32_16x16x32_bf16 v[68:71], a[204:207], a[196:199], v[68:71]\n" \
"ds_read_b64_tr_b16 a[36:37], v209 offset:13312\n" \
"ds_read_b64_tr_b16 a[38:39], v209 offset:16384\n" \
"v_mfma_f32_16x16x32_bf16 v[60:63], a[200:203], a[24:27], v[60:63]\n" \
"s_add_u32 m0, s79, 110592\n" \
"ds_read_b64_tr_b16 a[40:41], v209 offset:0\n" \
"buffer_store_dword v146, v147, s[56:59], s15 offen sc1\n" \
"v_mfma_f32_16x16x32_bf16 v[60:63], a[204:207], a[28:31], v[60:63]\n" \
"buffer_load_dwordx4 v192, s[44:47], s73 offen offset:0 lds\n" \
"v_mfma_f32_16x16x32_bf16 v[52:55], a[200:203], a[16:19], v[52:55]\n" \
"ds_read_b64_tr_b16 a[42:43], v209 offset:3072\n" \
"v_mfma_f32_16x16x32_bf16 v[52:55], a[204:207], a[20:23], v[52:55]\n" \
"ds_read_b64_tr_b16 a[44:45], v209 offset:12288\n" \
"buffer_store_dword v146, v147, s[56:59], s17 offen sc1\n" \
"v_mfma_f32_16x16x32_bf16 v[44:47], a[200:203], a[8:11], v[44:47]\n" \
"s_mov_b64 s[36:37], s[18:19]\n" \
"ds_read_b64_tr_b16 a[46:47], v209 offset:15360\n" \
"v_mfma_f32_16x16x32_bf16 v[44:47], a[204:207], a[12:15], v[44:47]\n" \
"buffer_load_dwordx4 v193, s[44:47], s73 offen offset:1024 lds\n" \
"v_mfma_f32_16x16x32_bf16 v[72:75], a[0:3], a[208:211], v[72:75]\n" \
"s_sub_u32 s36, s36, 4096\n" \
"s_subb_u32 s37, s37, 0\n" \
"ds_read_b64_tr_b16 a[48:49], v210 offset:0\n" \
"v_mfma_f32_16x16x32_bf16 v[72:75], a[4:7], a[212:215], v[72:75]\n" \
"s_and_b32 s37, s37, 0xffff\n" \
"s_mov_b32 s38, 8392704\n" \
"ds_read_b128 a[0:3], v207 offset:0\n" \
"v_mfma_f32_16x16x32_bf16 v[80:83], a[8:11], a[208:211], v[80:83]\n" \
"s_mov_b32 s39, 0x20000\n" \
"s_mov_b64 s[40:41], s[24:25]\n" \
"ds_read_b128 a[4:7], v208 offset:0\n" \
"v_mfma_f32_16x16x32_bf16 v[80:83], a[12:15], a[212:215], v[80:83]\n" \
"buffer_load_dwordx4 v192, s[44:47], s73 offen offset:2048 lds\n" \
"v_mfma_f32_16x16x32_bf16 v[84:87], a[8:11], a[216:219], v[84:87]\n" \
"s_sub_u32 s40, s40, 4096\n" \
"s_subb_u32 s41, s41, 0\n" \
"ds_read_b64_tr_b16 a[50:51], v210 offset:3072\n" \
"v_mfma_f32_16x16x32_bf16 v[84:87], a[12:15], a[220:223], v[84:87]\n" \
"s_and_b32 s41, s41, 0xffff\n" \
"s_mov_b32 s42, 2101248\n" \
"ds_read_b128 a[8:11], v205 offset:0\n" \
"v_mfma_f32_16x16x32_bf16 v[92:95], a[16:19], a[216:219], v[92:95]\n" \
"s_mov_b32 s43, 0x20000\n" \
"s_mov_b64 s[52:53], s[26:27]\n" \
"ds_read_b128 a[12:15], v206 offset:0\n" \
"v_mfma_f32_16x16x32_bf16 v[92:95], a[20:23], a[220:223], v[92:95]\n" \
"buffer_load_dwordx4 v193, s[44:47], s73 offen offset:3072 lds\n" \
"v_mfma_f32_16x16x32_bf16 v[88:91], a[16:19], a[208:211], v[88:91]\n" \
"s_and_b32 s53, s53, 0xffff\n" \
"s_mov_b32 s54, 8388608\n" \
"ds_read_b64_tr_b16 a[52:53], v210 offset:12288\n" \
"v_mfma_f32_16x16x32_bf16 v[88:91], a[20:23], a[212:215], v[88:91]\n" \
"s_add_u32 m0, s89, 110592\n" \
"s_mov_b32 s55, 0x20000\n" \
"ds_read_b128 a[16:19], v205 offset:4096\n" \
"v_mfma_f32_16x16x32_bf16 v[96:99], a[24:27], a[208:211], v[96:99]\n" \
"s_xor_b32 s73, s73, 0x80\n" \
"ds_read_b128 a[20:23], v206 offset:4096\n" \
"v_lshrrev_b32 v221, 2, v234\n" \
"v_mfma_f32_16x16x32_bf16 v[96:99], a[28:31], a[212:215], v[96:99]\n" \
"buffer_load_dwordx4 v196, s[44:47], s75 offen lds\n" \
"v_mfma_f32_16x16x32_bf16 v[100:103], a[24:27], a[216:219], v[100:103]\n" \
"s_xor_b32 s75, s75, 0x80\n" \
"s_mul_i32 s76, s80, 393216\n" \
"ds_read_b64_tr_b16 a[54:55], v210 offset:15360\n" \
"v_mfma_f32_16x16x32_bf16 v[100:103], a[28:31], a[220:223], v[100:103]\n" \
"s_add_u32 s76, s76, s78\n" \
"ds_read_b128 a[24:27], v205 offset:8192\n" \
"v_lshrrev_b32 v244, 4, v234\n" \
"v_mfma_f32_16x16x32_bf16 v[108:111], a[192:195], a[216:219], v[108:111]\n" \
"s_add_u32 m0, s91, 139264\n" \
"s_lshl_b32 s12, s94, 7\n" \
"ds_read_b128 a[28:31], v206 offset:8192\n" \
"v_mfma_f32_16x16x32_bf16 v[108:111], a[196:199], a[220:223], v[108:111]\n" \
"buffer_load_dwordx4 v197, s[48:51], s76 offen offset:0 lds\n" \
"v_mfma_f32_16x16x32_bf16 v[104:107], a[192:195], a[208:211], v[104:107]\n" \
"s_add_u32 s12, s12, 65591296\n" \
"s_and_b32 s70, %1, 1\n" \
"s_lshl3_add_u32 s70, s70, s63\n" \
"v_mfma_f32_16x16x32_bf16 v[104:107], a[196:199], a[212:215], v[104:107]\n" \
"s_waitcnt lgkmcnt(2)\n" \
"s_lshl2_add_u32 s12, s70, s12\n" \
"s_lshr_b32 s68, s63, 1\n" \
"s_add_u32 s68, s68, 2\n" \
"v_mfma_f32_16x16x32_bf16 v[32:35], a[32:35], a[0:3], v[32:35]\n" \
"s_add_u32 s77, s76, 24576\n" \
"s_mov_b32 s13, 65593856\n" \
"ds_read_b128 a[192:195], v205 offset:12288\n" \
"v_mfma_f32_16x16x32_bf16 v[32:35], a[36:39], a[4:7], v[32:35]\n" \
"buffer_load_dwordx4 v198, s[48:51], s76 offen offset:1024 lds\n" \
"v_mfma_f32_16x16x32_bf16 v[40:43], a[32:35], a[8:11], v[40:43]\n" \
"s_lshr_b32 s70, s18, 8\n" \
"ds_read_b128 a[196:199], v206 offset:12288\n" \
"v_lshrrev_b32 v228, 1, v234\n" \
"v_mfma_f32_16x16x32_bf16 v[40:43], a[36:39], a[12:15], v[40:43]\n" \
"s_sub_u32 s68, s68, s70\n" \
"s_and_b32 s68, s68, 3\n" \
"ds_read_b64_tr_b16 a[200:201], v210 offset:1024\n" \
"v_mfma_f32_16x16x32_bf16 v[0:3], a[40:43], a[8:11], v[0:3]\n" \
"s_xor_b32 s80, s80, 0x1\n" \
"s_and_b32 s70, s63, 1\n" \
"ds_read_b64_tr_b16 a[202:203], v210 offset:4096\n" \
"v_mfma_f32_16x16x32_bf16 v[0:3], a[44:47], a[12:15], v[0:3]\n" \
"buffer_load_dwordx4 v199, s[48:51], s76 offen offset:2048 lds\n" \
"v_mfma_f32_16x16x32_bf16 v[4:7], a[48:51], a[8:11], v[4:7]\n" \
"s_lshl_b32 s70, s70, 2\n" \
"s_or_b32 s68, s68, s70\n" \
"ds_read_b64_tr_b16 a[204:205], v210 offset:13312\n" \
"v_mfma_f32_16x16x32_bf16 v[4:7], a[52:55], a[12:15], v[4:7]\n" \
"s_lshr_b32 s84, s61, 1\n" \
"s_lshl_b32 s84, s84, 16\n" \
"ds_read_b64_tr_b16 a[206:207], v210 offset:16384\n" \
"v_mfma_f32_16x16x32_bf16 v[12:15], a[48:51], a[16:19], v[12:15]\n" \
"s_add_u32 m0, m0, 3072\n" \
"s_and_b32 s70, s61, 1\n" \
"ds_read_b64_tr_b16 a[208:209], v209 offset:2048\n" \
"v_mfma_f32_16x16x32_bf16 v[12:15], a[52:55], a[20:23], v[12:15]\n" \
"buffer_load_dwordx4 v197, s[48:51], s77 offen offset:0 lds\n" \
"v_mfma_f32_16x16x32_bf16 v[8:11], a[40:43], a[16:19], v[8:11]\n" \
"s_lshl_b32 s70, s70, 14\n" \
"s_add_u32 s84, s84, s70\n" \
"ds_read_b64_tr_b16 a[210:211], v209 offset:5120\n" \
"v_mfma_f32_16x16x32_bf16 v[8:11], a[44:47], a[20:23], v[8:11]\n" \
"s_lshl_b32 s88, s68, 8\n" \
"s_add_u32 s84, s84, s88\n" \
"ds_read_b64_tr_b16 a[212:213], v209 offset:14336\n" \
"v_mfma_f32_16x16x32_bf16 v[48:51], a[32:35], a[16:19], v[48:51]\n" \
"s_waitcnt lgkmcnt(3)\n" \
"ds_read_b64_tr_b16 a[214:215], v209 offset:17408\n" \
"v_bfe_u32 v245, v244, 2, 3\n" \
"v_mfma_f32_16x16x32_bf16 v[48:51], a[36:39], a[20:23], v[48:51]\n" \
"buffer_load_dwordx4 v198, s[48:51], s77 offen offset:1024 lds\n" \
"v_mfma_f32_16x16x32_bf16 v[56:59], a[32:35], a[24:27], v[56:59]\n" \
"ds_read_b64_tr_b16 a[216:217], v210 offset:2048\n" \
"v_lshlrev_b32 v245, 11, v245\n" \
"v_mfma_f32_16x16x32_bf16 v[56:59], a[36:39], a[28:31], v[56:59]\n" \
"ds_read_b64_tr_b16 a[218:219], v210 offset:5120\n" \
"v_lshrrev_b32 v246, 5, v244\n" \
"v_mfma_f32_16x16x32_bf16 v[16:19], a[40:43], a[24:27], v[16:19]\n" \
"ds_read_b64_tr_b16 a[220:221], v210 offset:14336\n" \
"v_lshl_add_u32 v245, v246, 6, v245\n" \
"v_mfma_f32_16x16x32_bf16 v[16:19], a[44:47], a[28:31], v[16:19]\n" \
"buffer_load_dwordx4 v199, s[48:51], s77 offen offset:2048 lds\n" \
"v_mfma_f32_16x16x32_bf16 v[20:23], a[48:51], a[24:27], v[20:23]\n" \
"ds_read_b64_tr_b16 a[222:223], v210 offset:17408\n" \
"v_and_b32 v246, 3, v244\n" \
"v_mfma_f32_16x16x32_bf16 v[20:23], a[52:55], a[28:31], v[20:23]\n" \
"v_bfe_u32 v247, v244, 4, 1\n" \
"v_lshlrev_b32 v247, 1, v247\n" \
"v_mfma_f32_16x16x32_bf16 v[28:31], a[48:51], a[192:195], v[28:31]\n" \
"v_xor_b32 v246, v246, v247\n" \
"v_lshl_add_u32 v245, v246, 4, v245\n" \
"v_mfma_f32_16x16x32_bf16 v[28:31], a[52:55], a[196:199], v[28:31]\n" \
"buffer_load_dwordx2 v[226:227], v228, s[56:59], s13 offen sc1\n" \
"v_mfma_f32_16x16x32_bf16 v[24:27], a[40:43], a[192:195], v[24:27]\n" \
"v_add_u32 v216, 4096, v245\n" \
"v_add_u32 v217, 3200, v245\n" \
"v_mfma_f32_16x16x32_bf16 v[24:27], a[44:47], a[196:199], v[24:27]\n" \
"v_add_u32 v218, 34816, v245\n" \
"v_add_u32 v219, 33920, v245\n" \
"v_mfma_f32_16x16x32_bf16 v[64:67], a[32:35], a[192:195], v[64:67]\n" \
"s_mov_b32 s86, 65536\n" \
"s_lshr_b32 s70, s61, 1\n" \
"s_lshl_b32 s71, s70, 13\n" \
"v_mfma_f32_16x16x32_bf16 v[64:67], a[36:39], a[196:199], v[64:67]\n" \
"Lpg8525_%=:\n" \
"Lst1x1_%=:\n" \
"s_cbranch_execz Lpg10852_%=\n" \
"s_add_u32 s71, s71, %3\n" \
"s_waitcnt vmcnt(15) lgkmcnt(0)\n" \
"s_barrier\n" \
"v_mfma_f32_16x16x32_bf16 v[68:71], a[200:203], a[192:195], v[68:71]\n" \
"ds_read_b64_tr_b16 a[32:33], v204 offset:21504\n" \
"ds_read_b64_tr_b16 a[34:35], v204 offset:24576\n" \
"v_mfma_f32_16x16x32_bf16 v[68:71], a[204:207], a[196:199], v[68:71]\n" \
"ds_read_b64_tr_b16 a[36:37], v204 offset:33792\n" \
"ds_read_b64_tr_b16 a[38:39], v204 offset:36864\n" \
"v_mfma_f32_16x16x32_bf16 v[60:63], a[200:203], a[24:27], v[60:63]\n" \
"s_add_u32 m0, s79, 65536\n" \
"ds_read_b64_tr_b16 a[40:41], v204 offset:20480\n" \
"v_add_u32 v211, s71, v236\n" \
"v_mfma_f32_16x16x32_bf16 v[60:63], a[204:207], a[28:31], v[60:63]\n" \
"buffer_load_dwordx4 v192, s[44:47], s73 offen offset:0 lds\n" \
"v_mfma_f32_16x16x32_bf16 v[52:55], a[200:203], a[16:19], v[52:55]\n" \
"s_lshl_b32 s70, s70, 18\n" \
"s_add_u32 s88, s88, s70\n" \
"ds_read_b64_tr_b16 a[42:43], v204 offset:23552\n" \
"v_mfma_f32_16x16x32_bf16 v[52:55], a[204:207], a[20:23], v[52:55]\n" \
"s_and_b32 s70, s61, 1\n" \
"s_lshl_b32 s71, s70, 7\n" \
"ds_read_b64_tr_b16 a[44:45], v204 offset:32768\n" \
"v_mfma_f32_16x16x32_bf16 v[44:47], a[200:203], a[8:11], v[44:47]\n" \
"s_add_u32 s88, s88, s71\n" \
"ds_read_b64_tr_b16 a[46:47], v204 offset:35840\n" \
"v_and_b32 v245, 3, v244\n" \
"v_mfma_f32_16x16x32_bf16 v[44:47], a[204:207], a[12:15], v[44:47]\n" \
"buffer_load_dwordx4 v193, s[44:47], s73 offen offset:1024 lds\n" \
"v_mfma_f32_16x16x32_bf16 v[72:75], a[0:3], a[208:211], v[72:75]\n" \
"ds_read_b64_tr_b16 a[48:49], v222 offset:20480\n" \
"v_lshlrev_b32 v245, 3, v245\n" \
"v_mfma_f32_16x16x32_bf16 v[72:75], a[4:7], a[212:215], v[72:75]\n" \
"ds_read_b128 a[0:3], v202 offset:0\n" \
"v_bfe_u32 v246, v244, 2, 2\n" \
"v_mfma_f32_16x16x32_bf16 v[80:83], a[8:11], a[208:211], v[80:83]\n" \
"ds_read_b128 a[4:7], v203 offset:0\n" \
"v_lshl_add_u32 v245, v246, 6, v245\n" \
"v_mfma_f32_16x16x32_bf16 v[80:83], a[12:15], a[212:215], v[80:83]\n" \
"buffer_load_dwordx4 v192, s[44:47], s73 offen offset:2048 lds\n" \
"v_mfma_f32_16x16x32_bf16 v[84:87], a[8:11], a[216:219], v[84:87]\n" \
"ds_read_b64_tr_b16 a[50:51], v222 offset:23552\n" \
"v_bfe_u32 v246, v244, 4, 1\n" \
"v_mfma_f32_16x16x32_bf16 v[84:87], a[12:15], a[220:223], v[84:87]\n" \
"ds_read_b128 a[8:11], v200 offset:0\n" \
"v_lshl_add_u32 v245, v246, 5, v245\n" \
"v_mfma_f32_16x16x32_bf16 v[92:95], a[16:19], a[216:219], v[92:95]\n" \
"ds_read_b128 a[12:15], v201 offset:0\n" \
"v_lshl_add_u32 v245, v246, 8, v245\n" \
"v_mfma_f32_16x16x32_bf16 v[92:95], a[20:23], a[220:223], v[92:95]\n" \
"buffer_load_dwordx4 v193, s[44:47], s73 offen offset:3072 lds\n" \
"v_mfma_f32_16x16x32_bf16 v[88:91], a[16:19], a[208:211], v[88:91]\n" \
"ds_read_b64_tr_b16 a[52:53], v222 offset:32768\n" \
"v_lshrrev_b32 v246, 5, v244\n" \
"v_mfma_f32_16x16x32_bf16 v[88:91], a[20:23], a[212:215], v[88:91]\n" \
"s_add_u32 m0, s89, 65536\n" \
"ds_read_b128 a[16:19], v200 offset:4096\n" \
"v_lshl_add_u32 v245, v246, 12, v245\n" \
"v_mfma_f32_16x16x32_bf16 v[96:99], a[24:27], a[208:211], v[96:99]\n" \
"s_xor_b32 s73, s73, 0x180\n" \
"s_lshl_b32 s71, s70, 10\n" \
"ds_read_b128 a[20:23], v201 offset:4096\n" \
"v_mfma_f32_16x16x32_bf16 v[96:99], a[28:31], a[212:215], v[96:99]\n" \
"buffer_load_dwordx4 v196, s[44:47], s75 offen lds\n" \
"v_mfma_f32_16x16x32_bf16 v[100:103], a[24:27], a[216:219], v[100:103]\n" \
"s_xor_b32 s75, s75, 0x180\n" \
"s_mul_i32 s76, s80, 393216\n" \
"ds_read_b64_tr_b16 a[54:55], v222 offset:35840\n" \
"v_mfma_f32_16x16x32_bf16 v[100:103], a[28:31], a[220:223], v[100:103]\n" \
"s_add_u32 s76, s76, s78\n" \
"s_add_u32 s71, s71, %3\n" \
"ds_read_b128 a[24:27], v200 offset:8192\n" \
"v_mfma_f32_16x16x32_bf16 v[108:111], a[192:195], a[216:219], v[108:111]\n" \
"s_add_u32 m0, s91, 86016\n" \
"ds_read_b128 a[28:31], v201 offset:8192\n" \
"v_add_u32 v212, s71, v245\n" \
"v_mfma_f32_16x16x32_bf16 v[108:111], a[196:199], a[220:223], v[108:111]\n" \
"buffer_load_dwordx4 v197, s[48:51], s76 offen offset:0 lds\n" \
"v_mfma_f32_16x16x32_bf16 v[104:107], a[192:195], a[208:211], v[104:107]\n" \
"s_mov_b32 s69, 1\n" \
"s_mov_b32 s30, 0x0\n" \
"s_mov_b32 s85, 98304\n" \
"v_mfma_f32_16x16x32_bf16 v[104:107], a[196:199], a[212:215], v[104:107]\n" \
"s_waitcnt lgkmcnt(2)\n" \
"s_sub_u32 s85, s85, 32768\n" \
"s_cselect_b32 s85, 131072, s85\n" \
"v_add_u32 v213, s85, v211\n" \
"v_mfma_f32_16x16x32_bf16 v[32:35], a[32:35], a[0:3], v[32:35]\n" \
"s_add_u32 s77, s76, 24576\n" \
"ds_read_b128 a[192:195], v200 offset:12288\n" \
"v_add_u32 v214, s85, v212\n" \
"v_mfma_f32_16x16x32_bf16 v[32:35], a[36:39], a[4:7], v[32:35]\n" \
"buffer_load_dwordx4 v198, s[48:51], s76 offen offset:1024 lds\n" \
"v_mfma_f32_16x16x32_bf16 v[40:43], a[32:35], a[8:11], v[40:43]\n" \
"ds_read_b128 a[196:199], v201 offset:12288\n" \
"v_xor_b32 v215, 32, v214\n" \
"v_mfma_f32_16x16x32_bf16 v[40:43], a[36:39], a[12:15], v[40:43]\n" \
"ds_read_b64_tr_b16 a[200:201], v222 offset:21504\n" \
"v_mov_b32 v230, s60\n" \
"v_mfma_f32_16x16x32_bf16 v[0:3], a[40:43], a[8:11], v[0:3]\n" \
"s_xor_b32 s80, s80, 0x3\n" \
"ds_read_b64_tr_b16 a[202:203], v222 offset:24576\n" \
"v_lshlrev_b32 v231, 28, s61\n" \
"v_mfma_f32_16x16x32_bf16 v[0:3], a[44:47], a[12:15], v[0:3]\n" \
"buffer_load_dwordx4 v199, s[48:51], s76 offen offset:2048 lds\n" \
"v_mfma_f32_16x16x32_bf16 v[4:7], a[48:51], a[8:11], v[4:7]\n" \
"ds_read_b64_tr_b16 a[204:205], v222 offset:33792\n" \
"v_mfma_f32_16x16x32_bf16 v[4:7], a[52:55], a[12:15], v[4:7]\n" \
"ds_read_b64_tr_b16 a[206:207], v222 offset:36864\n" \
"v_mfma_f32_16x16x32_bf16 v[12:15], a[48:51], a[16:19], v[12:15]\n" \
"s_add_u32 m0, m0, 3072\n" \
"ds_read_b64_tr_b16 a[208:209], v204 offset:22528\n" \
"v_mfma_f32_16x16x32_bf16 v[12:15], a[52:55], a[20:23], v[12:15]\n" \
"buffer_load_dwordx4 v197, s[48:51], s77 offen offset:0 lds\n" \
"v_mfma_f32_16x16x32_bf16 v[8:11], a[40:43], a[16:19], v[8:11]\n" \
"ds_read_b64_tr_b16 a[210:211], v204 offset:25600\n" \
"v_mfma_f32_16x16x32_bf16 v[8:11], a[44:47], a[20:23], v[8:11]\n" \
"ds_read_b64_tr_b16 a[212:213], v204 offset:34816\n" \
"v_mfma_f32_16x16x32_bf16 v[48:51], a[32:35], a[16:19], v[48:51]\n" \
"s_waitcnt lgkmcnt(3)\n" \
"ds_read_b64_tr_b16 a[214:215], v204 offset:37888\n" \
"v_mfma_f32_16x16x32_bf16 v[48:51], a[36:39], a[20:23], v[48:51]\n" \
"buffer_load_dwordx4 v198, s[48:51], s77 offen offset:1024 lds\n" \
"v_mfma_f32_16x16x32_bf16 v[56:59], a[32:35], a[24:27], v[56:59]\n" \
"ds_read_b64_tr_b16 a[216:217], v222 offset:22528\n" \
"v_mfma_f32_16x16x32_bf16 v[56:59], a[36:39], a[28:31], v[56:59]\n" \
"ds_read_b64_tr_b16 a[218:219], v222 offset:25600\n" \
"v_mfma_f32_16x16x32_bf16 v[16:19], a[40:43], a[24:27], v[16:19]\n" \
"ds_read_b64_tr_b16 a[220:221], v222 offset:34816\n" \
"v_mfma_f32_16x16x32_bf16 v[16:19], a[44:47], a[28:31], v[16:19]\n" \
"buffer_load_dwordx4 v199, s[48:51], s77 offen offset:2048 lds\n" \
"v_mfma_f32_16x16x32_bf16 v[20:23], a[48:51], a[24:27], v[20:23]\n" \
"ds_read_b64_tr_b16 a[222:223], v222 offset:37888\n" \
"v_mfma_f32_16x16x32_bf16 v[20:23], a[52:55], a[28:31], v[20:23]\n" \
"v_mfma_f32_16x16x32_bf16 v[28:31], a[48:51], a[192:195], v[28:31]\n" \
"v_mfma_f32_16x16x32_bf16 v[28:31], a[52:55], a[196:199], v[28:31]\n" \
"v_mfma_f32_16x16x32_bf16 v[24:27], a[40:43], a[192:195], v[24:27]\n" \
"v_mfma_f32_16x16x32_bf16 v[24:27], a[44:47], a[196:199], v[24:27]\n" \
"v_mfma_f32_16x16x32_bf16 v[64:67], a[32:35], a[192:195], v[64:67]\n" \
"v_mfma_f32_16x16x32_bf16 v[64:67], a[36:39], a[196:199], v[64:67]\n" \
"s_waitcnt vmcnt(12) lgkmcnt(0)\n" \
"s_barrier\n" \
"v_mfma_f32_16x16x32_bf16 v[68:71], a[200:203], a[192:195], v[68:71]\n" \
"ds_read_b64_tr_b16 a[32:33], v194 offset:1024\n" \
"ds_read_b64_tr_b16 a[34:35], v194 offset:4096\n" \
"v_mfma_f32_16x16x32_bf16 v[68:71], a[204:207], a[196:199], v[68:71]\n" \
"ds_read_b64_tr_b16 a[36:37], v194 offset:13312\n" \
"ds_read_b64_tr_b16 a[38:39], v194 offset:16384\n" \
"v_mfma_f32_16x16x32_bf16 v[60:63], a[200:203], a[24:27], v[60:63]\n" \
"s_add_u32 m0, s79, 0\n" \
"ds_read_b64_tr_b16 a[40:41], v194 offset:0\n" \
"v_mfma_f32_16x16x32_bf16 v[60:63], a[204:207], a[28:31], v[60:63]\n" \
"buffer_load_dwordx4 v192, s[44:47], s73 offen offset:0 lds\n" \
"v_mfma_f32_16x16x32_bf16 v[52:55], a[200:203], a[16:19], v[52:55]\n" \
"ds_read_b64_tr_b16 a[42:43], v194 offset:3072\n" \
"v_mfma_f32_16x16x32_bf16 v[52:55], a[204:207], a[20:23], v[52:55]\n" \
"ds_read_b64_tr_b16 a[44:45], v194 offset:12288\n" \
"v_mfma_f32_16x16x32_bf16 v[44:47], a[200:203], a[8:11], v[44:47]\n" \
"ds_read_b64_tr_b16 a[46:47], v194 offset:15360\n" \
"v_mfma_f32_16x16x32_bf16 v[44:47], a[204:207], a[12:15], v[44:47]\n" \
"buffer_load_dwordx4 v193, s[44:47], s73 offen offset:1024 lds\n" \
"v_mfma_f32_16x16x32_bf16 v[72:75], a[0:3], a[208:211], v[72:75]\n" \
"ds_read_b64_tr_b16 a[48:49], v195 offset:0\n" \
"v_mfma_f32_16x16x32_bf16 v[72:75], a[4:7], a[212:215], v[72:75]\n" \
"ds_read_b128 a[0:3], v207 offset:45056\n" \
"v_mfma_f32_16x16x32_bf16 v[80:83], a[8:11], a[208:211], v[80:83]\n" \
"ds_read_b128 a[4:7], v208 offset:45056\n" \
"v_mfma_f32_16x16x32_bf16 v[80:83], a[12:15], a[212:215], v[80:83]\n" \
"buffer_load_dwordx4 v192, s[44:47], s73 offen offset:2048 lds\n" \
"v_mfma_f32_16x16x32_bf16 v[84:87], a[8:11], a[216:219], v[84:87]\n" \
"ds_read_b64_tr_b16 a[50:51], v195 offset:3072\n" \
"v_mfma_f32_16x16x32_bf16 v[84:87], a[12:15], a[220:223], v[84:87]\n" \
"ds_read_b128 a[8:11], v205 offset:45056\n" \
"v_mfma_f32_16x16x32_bf16 v[92:95], a[16:19], a[216:219], v[92:95]\n" \
"ds_read_b128 a[12:15], v206 offset:45056\n" \
"v_mfma_f32_16x16x32_bf16 v[92:95], a[20:23], a[220:223], v[92:95]\n" \
"buffer_load_dwordx4 v193, s[44:47], s73 offen offset:3072 lds\n" \
"v_mfma_f32_16x16x32_bf16 v[88:91], a[16:19], a[208:211], v[88:91]\n" \
"ds_read_b64_tr_b16 a[52:53], v195 offset:12288\n" \
"v_mfma_f32_16x16x32_bf16 v[88:91], a[20:23], a[212:215], v[88:91]\n" \
"s_add_u32 m0, s89, 0\n" \
"ds_read_b128 a[16:19], v205 offset:49152\n" \
"v_mfma_f32_16x16x32_bf16 v[96:99], a[24:27], a[208:211], v[96:99]\n" \
"s_xor_b32 s73, s73, 0x300\n" \
"ds_read_b128 a[20:23], v206 offset:49152\n" \
"v_mfma_f32_16x16x32_bf16 v[96:99], a[28:31], a[212:215], v[96:99]\n" \
"buffer_load_dwordx4 v196, s[44:47], s75 offen lds\n" \
"v_mfma_f32_16x16x32_bf16 v[100:103], a[24:27], a[216:219], v[100:103]\n" \
"s_xor_b32 s75, s75, 0x300\n" \
"s_mul_i32 s76, s80, 393216\n" \
"ds_read_b64_tr_b16 a[54:55], v195 offset:15360\n" \
"v_mfma_f32_16x16x32_bf16 v[100:103], a[28:31], a[220:223], v[100:103]\n" \
"s_add_u32 s76, s76, s78\n" \
"ds_read_b128 a[24:27], v205 offset:53248\n" \
"v_mfma_f32_16x16x32_bf16 v[108:111], a[192:195], a[216:219], v[108:111]\n" \
"s_add_u32 m0, s91, 20480\n" \
"ds_read_b128 a[28:31], v206 offset:53248\n" \
"v_mfma_f32_16x16x32_bf16 v[108:111], a[196:199], a[220:223], v[108:111]\n" \
"buffer_load_dwordx4 v197, s[48:51], s76 offen offset:0 lds\n" \
"v_mfma_f32_16x16x32_bf16 v[104:107], a[192:195], a[208:211], v[104:107]\n" \
"v_mfma_f32_16x16x32_bf16 v[104:107], a[196:199], a[212:215], v[104:107]\n" \
"s_waitcnt lgkmcnt(2)\n" \
"v_mfma_f32_16x16x32_bf16 v[32:35], a[32:35], a[0:3], v[32:35]\n" \
"s_add_u32 s77, s76, 24576\n" \
"ds_read_b128 a[192:195], v205 offset:57344\n" \
"v_mfma_f32_16x16x32_bf16 v[32:35], a[36:39], a[4:7], v[32:35]\n" \
"buffer_load_dwordx4 v198, s[48:51], s76 offen offset:1024 lds\n" \
"v_mfma_f32_16x16x32_bf16 v[40:43], a[32:35], a[8:11], v[40:43]\n" \
"ds_read_b128 a[196:199], v206 offset:57344\n" \
"v_mfma_f32_16x16x32_bf16 v[40:43], a[36:39], a[12:15], v[40:43]\n" \
"ds_read_b64_tr_b16 a[200:201], v195 offset:1024\n" \
"v_mfma_f32_16x16x32_bf16 v[0:3], a[40:43], a[8:11], v[0:3]\n" \
"s_xor_b32 s80, s80, 0x6\n" \
"ds_read_b64_tr_b16 a[202:203], v195 offset:4096\n" \
"v_mfma_f32_16x16x32_bf16 v[0:3], a[44:47], a[12:15], v[0:3]\n" \
"buffer_load_dwordx4 v199, s[48:51], s76 offen offset:2048 lds\n" \
"v_mfma_f32_16x16x32_bf16 v[4:7], a[48:51], a[8:11], v[4:7]\n" \
"ds_read_b64_tr_b16 a[204:205], v195 offset:13312\n" \
"v_mfma_f32_16x16x32_bf16 v[4:7], a[52:55], a[12:15], v[4:7]\n" \
"ds_read_b64_tr_b16 a[206:207], v195 offset:16384\n" \
"v_mfma_f32_16x16x32_bf16 v[12:15], a[48:51], a[16:19], v[12:15]\n" \
"s_add_u32 m0, m0, 3072\n" \
"ds_read_b64_tr_b16 a[208:209], v194 offset:2048\n" \
"v_mfma_f32_16x16x32_bf16 v[12:15], a[52:55], a[20:23], v[12:15]\n" \
"buffer_load_dwordx4 v197, s[48:51], s77 offen offset:0 lds\n" \
"v_mfma_f32_16x16x32_bf16 v[8:11], a[40:43], a[16:19], v[8:11]\n" \
"ds_read_b64_tr_b16 a[210:211], v194 offset:5120\n" \
"v_mfma_f32_16x16x32_bf16 v[8:11], a[44:47], a[20:23], v[8:11]\n" \
"ds_read_b64_tr_b16 a[212:213], v194 offset:14336\n" \
"v_mfma_f32_16x16x32_bf16 v[48:51], a[32:35], a[16:19], v[48:51]\n" \
"s_waitcnt lgkmcnt(3)\n" \
"ds_read_b64_tr_b16 a[214:215], v194 offset:17408\n" \
"v_mfma_f32_16x16x32_bf16 v[48:51], a[36:39], a[20:23], v[48:51]\n" \
"buffer_load_dwordx4 v198, s[48:51], s77 offen offset:1024 lds\n" \
"v_mfma_f32_16x16x32_bf16 v[56:59], a[32:35], a[24:27], v[56:59]\n" \
"ds_read_b64_tr_b16 a[216:217], v195 offset:2048\n" \
"v_mfma_f32_16x16x32_bf16 v[56:59], a[36:39], a[28:31], v[56:59]\n" \
"ds_read_b64_tr_b16 a[218:219], v195 offset:5120\n" \
"v_mfma_f32_16x16x32_bf16 v[16:19], a[40:43], a[24:27], v[16:19]\n" \
"ds_read_b64_tr_b16 a[220:221], v195 offset:14336\n" \
"v_mfma_f32_16x16x32_bf16 v[16:19], a[44:47], a[28:31], v[16:19]\n" \
"buffer_load_dwordx4 v199, s[48:51], s77 offen offset:2048 lds\n" \
"v_mfma_f32_16x16x32_bf16 v[20:23], a[48:51], a[24:27], v[20:23]\n" \
"ds_read_b64_tr_b16 a[222:223], v195 offset:17408\n" \
"v_mfma_f32_16x16x32_bf16 v[20:23], a[52:55], a[28:31], v[20:23]\n" \
"v_mfma_f32_16x16x32_bf16 v[28:31], a[48:51], a[192:195], v[28:31]\n" \
"v_mfma_f32_16x16x32_bf16 v[28:31], a[52:55], a[196:199], v[28:31]\n" \
"v_mfma_f32_16x16x32_bf16 v[24:27], a[40:43], a[192:195], v[24:27]\n" \
"v_mfma_f32_16x16x32_bf16 v[24:27], a[44:47], a[196:199], v[24:27]\n" \
"v_mfma_f32_16x16x32_bf16 v[64:67], a[32:35], a[192:195], v[64:67]\n" \
"v_mfma_f32_16x16x32_bf16 v[64:67], a[36:39], a[196:199], v[64:67]\n" \
"s_waitcnt vmcnt(11) lgkmcnt(0)\n" \
"s_barrier\n" \
"v_mfma_f32_16x16x32_bf16 v[68:71], a[200:203], a[192:195], v[68:71]\n" \
"ds_read_b64_tr_b16 a[32:33], v209 offset:1024\n" \
"ds_read_b64_tr_b16 a[34:35], v209 offset:4096\n" \
"v_mfma_f32_16x16x32_bf16 v[68:71], a[204:207], a[196:199], v[68:71]\n" \
"ds_read_b64_tr_b16 a[36:37], v209 offset:13312\n" \
"ds_read_b64_tr_b16 a[38:39], v209 offset:16384\n" \
"v_mfma_f32_16x16x32_bf16 v[60:63], a[200:203], a[24:27], v[60:63]\n" \
"s_add_u32 m0, s79, 110592\n" \
"ds_read_b64_tr_b16 a[40:41], v209 offset:0\n" \
"v_cmp_eq_u32 vcc, s60, v226\n" \
"v_mfma_f32_16x16x32_bf16 v[60:63], a[204:207], a[28:31], v[60:63]\n" \
"buffer_load_dwordx4 v192, s[44:47], s73 offen offset:0 lds\n" \
"v_mfma_f32_16x16x32_bf16 v[52:55], a[200:203], a[16:19], v[52:55]\n" \
"s_andn2_b32 s70, 0xff, vcc_lo\n" \
"s_cbranch_scc1 Lpl4_%=\n" \
"Lpr4_%=:\n" \
"ds_read_b64_tr_b16 a[42:43], v209 offset:3072\n" \
"v_mfma_f32_16x16x32_bf16 v[52:55], a[204:207], a[20:23], v[52:55]\n" \
"s_and_b32 s70, %1, 7\n" \
"ds_read_b64_tr_b16 a[44:45], v209 offset:12288\n" \
"v_readlane_b32 s70, v227, s70\n" \
"v_mfma_f32_16x16x32_bf16 v[44:47], a[200:203], a[8:11], v[44:47]\n" \
"ds_read_b64_tr_b16 a[46:47], v209 offset:15360\n" \
"v_mfma_f32_16x16x32_bf16 v[44:47], a[204:207], a[12:15], v[44:47]\n" \
"buffer_load_dwordx4 v193, s[44:47], s73 offen offset:1024 lds\n" \
"v_mfma_f32_16x16x32_bf16 v[72:75], a[0:3], a[208:211], v[72:75]\n" \
"ds_read_b64_tr_b16 a[48:49], v210 offset:0\n" \
"v_subrev_u32 v227, s70, v227\n" \
"v_mfma_f32_16x16x32_bf16 v[72:75], a[4:7], a[212:215], v[72:75]\n" \
"ds_read_b128 a[0:3], v207 offset:0\n" \
"v_cmp_gt_i32 vcc, 0, v227\n" \
"v_mfma_f32_16x16x32_bf16 v[80:83], a[8:11], a[208:211], v[80:83]\n" \
"ds_read_b128 a[4:7], v208 offset:0\n" \
"v_mfma_f32_16x16x32_bf16 v[80:83], a[12:15], a[212:215], v[80:83]\n" \
"buffer_load_dwordx4 v192, s[44:47], s73 offen offset:2048 lds\n" \
"v_mfma_f32_16x16x32_bf16 v[84:87], a[8:11], a[216:219], v[84:87]\n" \
"s_and_b32 s13, vcc_lo, 0xff\n" \
"s_bcnt1_i32_b32 s13, s13\n" \
"ds_read_b64_tr_b16 a[50:51], v210 offset:3072\n" \
"v_mfma_f32_16x16x32_bf16 v[84:87], a[12:15], a[220:223], v[84:87]\n" \
"s_lshr_b32 s90, s13, 2\n" \
"s_mul_i32 s70, s90, 7\n" \
"ds_read_b128 a[8:11], v205 offset:0\n" \
"v_mfma_f32_16x16x32_bf16 v[92:95], a[16:19], a[216:219], v[92:95]\n" \
"s_xor_b32 s13, s13, s70\n" \
"s_lshl_b32 s70, s13, 3\n" \
"ds_read_b128 a[12:15], v206 offset:0\n" \
"v_mfma_f32_16x16x32_bf16 v[92:95], a[20:23], a[220:223], v[92:95]\n" \
"buffer_load_dwordx4 v193, s[44:47], s73 offen offset:3072 lds\n" \
"v_mfma_f32_16x16x32_bf16 v[88:91], a[16:19], a[208:211], v[88:91]\n" \
"s_lshr_b32 s9, 0x2a2b2d2f, s70\n" \
"s_and_b32 s9, s9, 0xff\n" \
"ds_read_b64_tr_b16 a[52:53], v210 offset:12288\n" \
"v_mfma_f32_16x16x32_bf16 v[88:91], a[20:23], a[212:215], v[88:91]\n" \
"s_add_u32 m0, s89, 110592\n" \
"s_lshr_b32 s8, %1, 6\n" \
"ds_read_b128 a[16:19], v205 offset:4096\n" \
"v_mfma_f32_16x16x32_bf16 v[96:99], a[24:27], a[208:211], v[96:99]\n" \
"s_xor_b32 s73, s73, 0x80\n" \
"s_lshl2_add_u32 s8, s13, s8\n" \
"ds_read_b128 a[20:23], v206 offset:4096\n" \
"v_mfma_f32_16x16x32_bf16 v[96:99], a[28:31], a[212:215], v[96:99]\n" \
"buffer_load_dwordx4 v196, s[44:47], s75 offen lds\n" \
"v_mfma_f32_16x16x32_bf16 v[100:103], a[24:27], a[216:219], v[100:103]\n" \
"s_xor_b32 s75, s75, 0x80\n" \
"s_mul_i32 s76, s80, 393216\n" \
"ds_read_b64_tr_b16 a[54:55], v210 offset:15360\n" \
"v_mfma_f32_16x16x32_bf16 v[100:103], a[28:31], a[220:223], v[100:103]\n" \
"s_add_u32 s76, s76, s78\n" \
"ds_read_b128 a[24:27], v205 offset:8192\n" \
"v_max_i32_dpp v226, v227, v227 quad_perm:[1,0,3,2] row_mask:0xf bank_mask:0xf\n" \
"v_mfma_f32_16x16x32_bf16 v[108:111], a[192:195], a[216:219], v[108:111]\n" \
"s_add_u32 m0, s91, 139264\n" \
"s_mov_b32 s14, 0\n" \
"ds_read_b128 a[28:31], v206 offset:8192\n" \
"v_mfma_f32_16x16x32_bf16 v[108:111], a[196:199], a[220:223], v[108:111]\n" \
"buffer_load_dwordx4 v197, s[48:51], s76 offen offset:0 lds\n" \
"v_mfma_f32_16x16x32_bf16 v[104:107], a[192:195], a[208:211], v[104:107]\n" \
"v_readlane_b32 s15, v226, 2\n" \
"v_readlane_b32 s16, v226, 4\n" \
"v_mfma_f32_16x16x32_bf16 v[104:107], a[196:199], a[212:215], v[104:107]\n" \
"s_waitcnt lgkmcnt(2)\n" \
"v_readlane_b32 s17, v226, 6\n" \
"v_cmp_gt_i32 vcc, s15, v226\n" \
"v_mfma_f32_16x16x32_bf16 v[32:35], a[32:35], a[0:3], v[32:35]\n" \
"s_add_u32 s77, s76, 24576\n" \
"ds_read_b128 a[192:195], v205 offset:12288\n" \
"v_mfma_f32_16x16x32_bf16 v[32:35], a[36:39], a[4:7], v[32:35]\n" \
"buffer_load_dwordx4 v198, s[48:51], s76 offen offset:1024 lds\n" \
"v_mfma_f32_16x16x32_bf16 v[40:43], a[32:35], a[8:11], v[40:43]\n" \
"s_and_b32 s70, vcc_lo, 0x55\n" \
"s_bcnt1_i32_b32 s70, s70\n" \
"ds_read_b128 a[196:199], v206 offset:12288\n" \
"v_mfma_f32_16x16x32_bf16 v[40:43], a[36:39], a[12:15], v[40:43]\n" \
"s_cmp_eq_u32 s70, s13\n" \
"s_cselect_b32 s14, 4, s14\n" \
"ds_read_b64_tr_b16 a[200:201], v210 offset:1024\n" \
"v_mfma_f32_16x16x32_bf16 v[0:3], a[40:43], a[8:11], v[0:3]\n" \
"s_xor_b32 s80, s80, 0x1\n" \
"ds_read_b64_tr_b16 a[202:203], v210 offset:4096\n" \
"v_cmp_gt_i32 vcc, s16, v226\n" \
"v_mfma_f32_16x16x32_bf16 v[0:3], a[44:47], a[12:15], v[0:3]\n" \
"buffer_load_dwordx4 v199, s[48:51], s76 offen offset:2048 lds\n" \
"v_mfma_f32_16x16x32_bf16 v[4:7], a[48:51], a[8:11], v[4:7]\n" \
"s_and_b32 s70, vcc_lo, 0x55\n" \
"s_bcnt1_i32_b32 s70, s70\n" \
"ds_read_b64_tr_b16 a[204:205], v210 offset:13312\n" \
"v_mfma_f32_16x16x32_bf16 v[4:7], a[52:55], a[12:15], v[4:7]\n" \
"s_cmp_eq_u32 s70, s13\n" \
"s_cselect_b32 s14, 8, s14\n" \
"ds_read_b64_tr_b16 a[206:207], v210 offset:16384\n" \
"v_mfma_f32_16x16x32_bf16 v[12:15], a[48:51], a[16:19], v[12:15]\n" \
"s_add_u32 m0, m0, 3072\n" \
"ds_read_b64_tr_b16 a[208:209], v209 offset:2048\n" \
"v_cmp_gt_i32 vcc, s17, v226\n" \
"v_mfma_f32_16x16x32_bf16 v[12:15], a[52:55], a[20:23], v[12:15]\n" \
"buffer_load_dwordx4 v197, s[48:51], s77 offen offset:0 lds\n" \
"v_mfma_f32_16x16x32_bf16 v[8:11], a[40:43], a[16:19], v[8:11]\n" \
"s_and_b32 s70, vcc_lo, 0x55\n" \
"s_bcnt1_i32_b32 s70, s70\n" \
"ds_read_b64_tr_b16 a[210:211], v209 offset:5120\n" \
"v_mfma_f32_16x16x32_bf16 v[8:11], a[44:47], a[20:23], v[8:11]\n" \
"s_cmp_eq_u32 s70, s13\n" \
"s_cselect_b32 s14, 12, s14\n" \
"ds_read_b64_tr_b16 a[212:213], v209 offset:14336\n" \
"v_mfma_f32_16x16x32_bf16 v[48:51], a[32:35], a[16:19], v[48:51]\n" \
"s_lshr_b32 s70, %1, 6\n" \
"s_add_u32 s14, s14, s70\n" \
"ds_read_b64_tr_b16 a[214:215], v209 offset:17408\n" \
"v_mfma_f32_16x16x32_bf16 v[48:51], a[36:39], a[20:23], v[48:51]\n" \
"s_waitcnt lgkmcnt(3)\n" \
"buffer_load_dwordx4 v198, s[48:51], s77 offen offset:1024 lds\n" \
"v_mfma_f32_16x16x32_bf16 v[56:59], a[32:35], a[24:27], v[56:59]\n" \
"s_cmp_eq_u32 s90, 0\n" \
"s_cselect_b32 s14, s8, s14\n" \
"ds_read_b64_tr_b16 a[216:217], v210 offset:2048\n" \
"v_mfma_f32_16x16x32_bf16 v[56:59], a[36:39], a[28:31], v[56:59]\n" \
"s_sub_u32 s10, 49, s9\n" \
"s_lshl_b32 s11, s9, 1\n" \
"ds_read_b64_tr_b16 a[218:219], v210 offset:5120\n" \
"v_mfma_f32_16x16x32_bf16 v[16:19], a[40:43], a[24:27], v[16:19]\n" \
"s_sub_u32 s11, s11, 71\n" \
"s_lshl2_add_u32 s70, s8, s61\n" \
"ds_read_b64_tr_b16 a[220:221], v210 offset:14336\n" \
"v_mfma_f32_16x16x32_bf16 v[16:19], a[44:47], a[28:31], v[16:19]\n" \
"buffer_load_dwordx4 v199, s[48:51], s77 offen offset:2048 lds\n" \
"v_mfma_f32_16x16x32_bf16 v[20:23], a[48:51], a[24:27], v[20:23]\n" \
"s_mul_i32 s81, s70, 266240\n" \
"s_mul_i32 s82, s90, s9\n" \
"ds_read_b64_tr_b16 a[222:223], v210 offset:17408\n" \
"v_mfma_f32_16x16x32_bf16 v[20:23], a[52:55], a[28:31], v[20:23]\n" \
"s_lshl_b32 s71, s82, 12\n" \
"s_add_u32 s81, s81, s71\n" \
"s_add_u32 s81, s81, 35700736\n" \
"v_mfma_f32_16x16x32_bf16 v[28:31], a[48:51], a[192:195], v[28:31]\n" \
"s_lshl2_add_u32 s70, s14, s61\n" \
"s_mul_i32 s83, s70, 69632\n" \
"s_add_u32 s83, s83, 52740096\n" \
"v_mfma_f32_16x16x32_bf16 v[28:31], a[52:55], a[196:199], v[28:31]\n" \
"s_lshl_b32 s82, s82, 17\n" \
"s_add_u32 s82, s82, s84\n" \
"s_lshl_b32 s70, s14, 19\n" \
"v_mfma_f32_16x16x32_bf16 v[24:27], a[40:43], a[192:195], v[24:27]\n" \
"s_add_u32 s88, s88, s70\n" \
"s_lshl_b32 s71, s90, 11\n" \
"s_add_u32 s88, s88, s71\n" \
"v_mfma_f32_16x16x32_bf16 v[24:27], a[44:47], a[196:199], v[24:27]\n" \
"s_lshl3_add_u32 s70, s8, s68\n" \
"s_lshl2_add_u32 s87, s70, s61\n" \
"s_lshl_b32 s87, s87, 14\n" \
"v_mfma_f32_16x16x32_bf16 v[64:67], a[32:35], a[192:195], v[64:67]\n" \
"s_add_u32 s87, s87, 57196544\n" \
"s_lshl_b32 s64, s70, 7\n" \
"s_add_u32 s64, s64, 65601536\n" \
"v_mfma_f32_16x16x32_bf16 v[64:67], a[36:39], a[196:199], v[64:67]\n" \
"s_lshl_b32 s66, s8, 7\n" \
"s_waitcnt vmcnt(11) lgkmcnt(0)\n" \
"s_barrier\n" \
"v_mfma_f32_16x16x32_bf16 v[68:71], a[200:203], a[192:195], v[68:71]\n" \
"s_lshl_b32 s67, s14, 7\n" \
"ds_read_b64_tr_b16 a[32:33], v204 offset:21504\n" \
"ds_read_b64_tr_b16 a[34:35], v204 offset:24576\n" \
"v_mfma_f32_16x16x32_bf16 v[68:71], a[204:207], a[196:199], v[68:71]\n" \
"s_add_u32 s67, s67, 65591296\n" \
"ds_read_b64_tr_b16 a[36:37], v204 offset:33792\n" \
"ds_read_b64_tr_b16 a[38:39], v204 offset:36864\n" \
"v_mfma_f32_16x16x32_bf16 v[60:63], a[200:203], a[24:27], v[60:63]\n" \
"s_add_u32 m0, s79, 65536\n" \
"s_add_u32 s66, s66, 65589248\n" \
"ds_read_b64_tr_b16 a[40:41], v204 offset:20480\n" \
"v_mfma_f32_16x16x32_bf16 v[60:63], a[204:207], a[28:31], v[60:63]\n" \
"buffer_load_dwordx4 v192, s[44:47], s73 offen offset:0 lds\n" \
"v_mfma_f32_16x16x32_bf16 v[52:55], a[200:203], a[16:19], v[52:55]\n" \
"s_add_u32 s70, s9, 1\n" \
"s_sub_u32 s70, s70, s90\n" \
"ds_read_b64_tr_b16 a[42:43], v204 offset:23552\n" \
"v_mfma_f32_16x16x32_bf16 v[52:55], a[204:207], a[20:23], v[52:55]\n" \
"s_lshr_b32 s70, s70, 1\n" \
"s_bfm_b32 s65, s70, 0\n" \
"ds_read_b64_tr_b16 a[44:45], v204 offset:32768\n" \
"v_mfma_f32_16x16x32_bf16 v[44:47], a[200:203], a[8:11], v[44:47]\n" \
"s_sub_u32 s70, 0, s90\n" \
"s_xor_b32 s65, s65, s70\n" \
"ds_read_b64_tr_b16 a[46:47], v204 offset:35840\n" \
"v_mfma_f32_16x16x32_bf16 v[44:47], a[204:207], a[12:15], v[44:47]\n" \
"buffer_load_dwordx4 v193, s[44:47], s73 offen offset:1024 lds\n" \
"v_mfma_f32_16x16x32_bf16 v[72:75], a[0:3], a[208:211], v[72:75]\n" \
"s_and_b32 s74, s70, 0xffff\n" \
"s_xor_b32 s68, s68, s8\n" \
"ds_read_b64_tr_b16 a[48:49], v222 offset:20480\n" \
"v_mfma_f32_16x16x32_bf16 v[72:75], a[4:7], a[212:215], v[72:75]\n" \
"s_and_b32 s68, s68, 3\n" \
"s_lshl_b32 s68, s68, 12\n" \
"ds_read_b128 a[0:3], v202 offset:0\n" \
"v_mfma_f32_16x16x32_bf16 v[80:83], a[8:11], a[208:211], v[80:83]\n" \
"ds_read_b128 a[4:7], v203 offset:0\n" \
"v_mfma_f32_16x16x32_bf16 v[80:83], a[12:15], a[212:215], v[80:83]\n" \
"buffer_load_dwordx4 v192, s[44:47], s73 offen offset:2048 lds\n" \
"v_mfma_f32_16x16x32_bf16 v[84:87], a[8:11], a[216:219], v[84:87]\n" \
"ds_read_b64_tr_b16 a[50:51], v222 offset:23552\n" \
"v_mfma_f32_16x16x32_bf16 v[84:87], a[12:15], a[220:223], v[84:87]\n" \
"ds_read_b128 a[8:11], v200 offset:0\n" \
"v_mfma_f32_16x16x32_bf16 v[92:95], a[16:19], a[216:219], v[92:95]\n" \
"ds_read_b128 a[12:15], v201 offset:0\n" \
"v_mfma_f32_16x16x32_bf16 v[92:95], a[20:23], a[220:223], v[92:95]\n" \
"buffer_load_dwordx4 v193, s[44:47], s73 offen offset:3072 lds\n" \
"v_mfma_f32_16x16x32_bf16 v[88:91], a[16:19], a[208:211], v[88:91]\n" \
"ds_read_b64_tr_b16 a[52:53], v222 offset:32768\n" \
"v_mfma_f32_16x16x32_bf16 v[88:91], a[20:23], a[212:215], v[88:91]\n" \
"s_add_u32 m0, s89, 65536\n" \
"ds_read_b128 a[16:19], v200 offset:4096\n" \
"v_mfma_f32_16x16x32_bf16 v[96:99], a[24:27], a[208:211], v[96:99]\n" \
"s_xor_b32 s73, s73, 0x180\n" \
"ds_read_b128 a[20:23], v201 offset:4096\n" \
"v_mfma_f32_16x16x32_bf16 v[96:99], a[28:31], a[212:215], v[96:99]\n" \
"buffer_load_dwordx4 v196, s[44:47], s75 offen lds\n" \
"v_mfma_f32_16x16x32_bf16 v[100:103], a[24:27], a[216:219], v[100:103]\n" \
"s_xor_b32 s75, s75, 0x180\n" \
"s_mul_i32 s76, s80, 393216\n" \
"ds_read_b64_tr_b16 a[54:55], v222 offset:35840\n" \
"v_mfma_f32_16x16x32_bf16 v[100:103], a[28:31], a[220:223], v[100:103]\n" \
"s_add_u32 s76, s76, s78\n" \
"ds_read_b128 a[24:27], v200 offset:8192\n" \
"v_mfma_f32_16x16x32_bf16 v[108:111], a[192:195], a[216:219], v[108:111]\n" \
"s_add_u32 m0, s91, 86016\n" \
"ds_read_b128 a[28:31], v201 offset:8192\n" \
"v_mfma_f32_16x16x32_bf16 v[108:111], a[196:199], a[220:223], v[108:111]\n" \
"buffer_load_dwordx4 v197, s[48:51], s76 offen offset:0 lds\n" \
"v_mfma_f32_16x16x32_bf16 v[104:107], a[192:195], a[208:211], v[104:107]\n" \
"v_mfma_f32_16x16x32_bf16 v[104:107], a[196:199], a[212:215], v[104:107]\n" \
"s_waitcnt lgkmcnt(2)\n" \
"v_mfma_f32_16x16x32_bf16 v[32:35], a[32:35], a[0:3], v[32:35]\n" \
"s_add_u32 s77, s76, 24576\n" \
"ds_read_b128 a[192:195], v200 offset:12288\n" \
"v_mfma_f32_16x16x32_bf16 v[32:35], a[36:39], a[4:7], v[32:35]\n" \
"buffer_load_dwordx4 v198, s[48:51], s76 offen offset:1024 lds\n" \
"v_mfma_f32_16x16x32_bf16 v[40:43], a[32:35], a[8:11], v[40:43]\n" \
"ds_read_b128 a[196:199], v201 offset:12288\n" \
"v_mfma_f32_16x16x32_bf16 v[40:43], a[36:39], a[12:15], v[40:43]\n" \
"ds_read_b64_tr_b16 a[200:201], v222 offset:21504\n" \
"v_mfma_f32_16x16x32_bf16 v[0:3], a[40:43], a[8:11], v[0:3]\n" \
"s_xor_b32 s80, s80, 0x3\n" \
"ds_read_b64_tr_b16 a[202:203], v222 offset:24576\n" \
"v_mfma_f32_16x16x32_bf16 v[0:3], a[44:47], a[12:15], v[0:3]\n" \
"buffer_load_dwordx4 v199, s[48:51], s76 offen offset:2048 lds\n" \
"v_mfma_f32_16x16x32_bf16 v[4:7], a[48:51], a[8:11], v[4:7]\n" \
"ds_read_b64_tr_b16 a[204:205], v222 offset:33792\n" \
"v_mfma_f32_16x16x32_bf16 v[4:7], a[52:55], a[12:15], v[4:7]\n" \
"ds_read_b64_tr_b16 a[206:207], v222 offset:36864\n" \
"v_mfma_f32_16x16x32_bf16 v[12:15], a[48:51], a[16:19], v[12:15]\n" \
"s_add_u32 m0, m0, 3072\n" \
"ds_read_b64_tr_b16 a[208:209], v204 offset:22528\n" \
"v_mfma_f32_16x16x32_bf16 v[12:15], a[52:55], a[20:23], v[12:15]\n" \
"buffer_load_dwordx4 v197, s[48:51], s77 offen offset:0 lds\n" \
"v_mfma_f32_16x16x32_bf16 v[8:11], a[40:43], a[16:19], v[8:11]\n" \
"ds_read_b64_tr_b16 a[210:211], v204 offset:25600\n" \
"v_mfma_f32_16x16x32_bf16 v[8:11], a[44:47], a[20:23], v[8:11]\n" \
"ds_read_b64_tr_b16 a[212:213], v204 offset:34816\n" \
"v_mfma_f32_16x16x32_bf16 v[48:51], a[32:35], a[16:19], v[48:51]\n" \
"s_waitcnt lgkmcnt(3)\n" \
"ds_read_b64_tr_b16 a[214:215], v204 offset:37888\n" \
"v_mfma_f32_16x16x32_bf16 v[48:51], a[36:39], a[20:23], v[48:51]\n" \
"buffer_load_dwordx4 v198, s[48:51], s77 offen offset:1024 lds\n" \
"v_mfma_f32_16x16x32_bf16 v[56:59], a[32:35], a[24:27], v[56:59]\n" \
"ds_read_b64_tr_b16 a[216:217], v222 offset:22528\n" \
"v_mfma_f32_16x16x32_bf16 v[56:59], a[36:39], a[28:31], v[56:59]\n" \
"ds_read_b64_tr_b16 a[218:219], v222 offset:25600\n" \
"v_mfma_f32_16x16x32_bf16 v[16:19], a[40:43], a[24:27], v[16:19]\n" \
"ds_read_b64_tr_b16 a[220:221], v222 offset:34816\n" \
"v_mfma_f32_16x16x32_bf16 v[16:19], a[44:47], a[28:31], v[16:19]\n" \
"buffer_load_dwordx4 v199, s[48:51], s77 offen offset:2048 lds\n" \
"v_mfma_f32_16x16x32_bf16 v[20:23], a[48:51], a[24:27], v[20:23]\n" \
"ds_read_b64_tr_b16 a[222:223], v222 offset:37888\n" \
"v_mfma_f32_16x16x32_bf16 v[20:23], a[52:55], a[28:31], v[20:23]\n" \
"v_mfma_f32_16x16x32_bf16 v[28:31], a[48:51], a[192:195], v[28:31]\n" \
"v_mfma_f32_16x16x32_bf16 v[28:31], a[52:55], a[196:199], v[28:31]\n" \
"v_mfma_f32_16x16x32_bf16 v[24:27], a[40:43], a[192:195], v[24:27]\n" \
"v_mfma_f32_16x16x32_bf16 v[24:27], a[44:47], a[196:199], v[24:27]\n" \
"v_mfma_f32_16x16x32_bf16 v[64:67], a[32:35], a[192:195], v[64:67]\n" \
"v_mfma_f32_16x16x32_bf16 v[64:67], a[36:39], a[196:199], v[64:67]\n" \
"s_waitcnt vmcnt(11) lgkmcnt(0)\n" \
"s_barrier\n" \
"v_mfma_f32_16x16x32_bf16 v[68:71], a[200:203], a[192:195], v[68:71]\n" \
"ds_read_b64_tr_b16 a[32:33], v194 offset:1024\n" \
"ds_read_b64_tr_b16 a[34:35], v194 offset:4096\n" \
"v_mfma_f32_16x16x32_bf16 v[68:71], a[204:207], a[196:199], v[68:71]\n" \
"ds_read_b64_tr_b16 a[36:37], v194 offset:13312\n" \
"ds_read_b64_tr_b16 a[38:39], v194 offset:16384\n" \
"v_mfma_f32_16x16x32_bf16 v[60:63], a[200:203], a[24:27], v[60:63]\n" \
"s_add_u32 m0, s79, 0\n" \
"ds_read_b64_tr_b16 a[40:41], v194 offset:0\n" \
"v_mfma_f32_16x16x32_bf16 v[60:63], a[204:207], a[28:31], v[60:63]\n" \
"Lpg9091_%=:\n" \
"Lst1x2_%=:\n" \
"s_cbranch_execz Lpg10946_%=\n" \
"buffer_load_dwordx4 v192, s[44:47], s73 offen offset:0 lds\n" \
"v_mfma_f32_16x16x32_bf16 v[52:55], a[200:203], a[16:19], v[52:55]\n" \
"ds_read_b64_tr_b16 a[42:43], v194 offset:3072\n" \
"v_mfma_f32_16x16x32_bf16 v[52:55], a[204:207], a[20:23], v[52:55]\n" \
"ds_read_b64_tr_b16 a[44:45], v194 offset:12288\n" \
"v_mfma_f32_16x16x32_bf16 v[44:47], a[200:203], a[8:11], v[44:47]\n" \
"ds_read_b64_tr_b16 a[46:47], v194 offset:15360\n" \
"v_mfma_f32_16x16x32_bf16 v[44:47], a[204:207], a[12:15], v[44:47]\n" \
"buffer_load_dwordx4 v193, s[44:47], s73 offen offset:1024 lds\n" \
"v_mfma_f32_16x16x32_bf16 v[72:75], a[0:3], a[208:211], v[72:75]\n" \
"ds_read_b64_tr_b16 a[48:49], v195 offset:0\n" \
"v_mfma_f32_16x16x32_bf16 v[72:75], a[4:7], a[212:215], v[72:75]\n" \
"ds_read_b128 a[0:3], v207 offset:45056\n" \
"v_mfma_f32_16x16x32_bf16 v[80:83], a[8:11], a[208:211], v[80:83]\n" \
"ds_read_b128 a[4:7], v208 offset:45056\n" \
"v_mfma_f32_16x16x32_bf16 v[80:83], a[12:15], a[212:215], v[80:83]\n" \
"buffer_load_dwordx4 v192, s[44:47], s73 offen offset:2048 lds\n" \
"v_mfma_f32_16x16x32_bf16 v[84:87], a[8:11], a[216:219], v[84:87]\n" \
"ds_read_b64_tr_b16 a[50:51], v195 offset:3072\n" \
"v_mfma_f32_16x16x32_bf16 v[84:87], a[12:15], a[220:223], v[84:87]\n" \
"ds_read_b128 a[8:11], v205 offset:45056\n" \
"v_mfma_f32_16x16x32_bf16 v[92:95], a[16:19], a[216:219], v[92:95]\n" \
"ds_read_b128 a[12:15], v206 offset:45056\n" \
"v_mfma_f32_16x16x32_bf16 v[92:95], a[20:23], a[220:223], v[92:95]\n" \
"buffer_load_dwordx4 v193, s[44:47], s73 offen offset:3072 lds\n" \
"v_mfma_f32_16x16x32_bf16 v[88:91], a[16:19], a[208:211], v[88:91]\n" \
"ds_read_b64_tr_b16 a[52:53], v195 offset:12288\n" \
"v_mfma_f32_16x16x32_bf16 v[88:91], a[20:23], a[212:215], v[88:91]\n" \
"s_add_u32 m0, s89, 0\n" \
"ds_read_b128 a[16:19], v205 offset:49152\n" \
"v_mfma_f32_16x16x32_bf16 v[96:99], a[24:27], a[208:211], v[96:99]\n" \
"s_xor_b32 s73, s73, 0x80\n" \
"ds_read_b128 a[20:23], v206 offset:49152\n" \
"v_mfma_f32_16x16x32_bf16 v[96:99], a[28:31], a[212:215], v[96:99]\n" \
"buffer_load_dwordx4 v196, s[44:47], s75 offen lds\n" \
"v_mfma_f32_16x16x32_bf16 v[100:103], a[24:27], a[216:219], v[100:103]\n" \
"s_xor_b32 s75, s75, 0x80\n" \
"s_mul_i32 s76, s80, 393216\n" \
"ds_read_b64_tr_b16 a[54:55], v195 offset:15360\n" \
"v_mfma_f32_16x16x32_bf16 v[100:103], a[28:31], a[220:223], v[100:103]\n" \
"s_add_u32 s76, s76, s78\n" \
"ds_read_b128 a[24:27], v205 offset:53248\n" \
"v_mfma_f32_16x16x32_bf16 v[108:111], a[192:195], a[216:219], v[108:111]\n" \
"s_add_u32 m0, s91, 20480\n" \
"ds_read_b128 a[28:31], v206 offset:53248\n" \
"v_mfma_f32_16x16x32_bf16 v[108:111], a[196:199], a[220:223], v[108:111]\n" \
"buffer_load_dwordx4 v197, s[48:51], s76 offen offset:0 lds\n" \
"v_mfma_f32_16x16x32_bf16 v[104:107], a[192:195], a[208:211], v[104:107]\n" \
"v_mfma_f32_16x16x32_bf16 v[104:107], a[196:199], a[212:215], v[104:107]\n" \
"s_waitcnt lgkmcnt(2)\n" \
"v_mfma_f32_16x16x32_bf16 v[32:35], a[32:35], a[0:3], v[32:35]\n" \
"s_add_u32 s77, s76, 24576\n" \
"ds_read_b128 a[192:195], v205 offset:57344\n" \
"v_mfma_f32_16x16x32_bf16 v[32:35], a[36:39], a[4:7], v[32:35]\n" \
"buffer_load_dwordx4 v198, s[48:51], s76 offen offset:1024 lds\n" \
"v_mfma_f32_16x16x32_bf16 v[40:43], a[32:35], a[8:11], v[40:43]\n" \
"ds_read_b128 a[196:199], v206 offset:57344\n" \
"v_mfma_f32_16x16x32_bf16 v[40:43], a[36:39], a[12:15], v[40:43]\n" \
"ds_read_b64_tr_b16 a[200:201], v195 offset:1024\n" \
"v_mfma_f32_16x16x32_bf16 v[0:3], a[40:43], a[8:11], v[0:3]\n" \
"s_xor_b32 s80, s80, 0x1\n" \
"ds_read_b64_tr_b16 a[202:203], v195 offset:4096\n" \
"v_mfma_f32_16x16x32_bf16 v[0:3], a[44:47], a[12:15], v[0:3]\n" \
"buffer_load_dwordx4 v199, s[48:51], s76 offen offset:2048 lds\n" \
"v_mfma_f32_16x16x32_bf16 v[4:7], a[48:51], a[8:11], v[4:7]\n" \
"ds_read_b64_tr_b16 a[204:205], v195 offset:13312\n" \
"v_mfma_f32_16x16x32_bf16 v[4:7], a[52:55], a[12:15], v[4:7]\n" \
"ds_read_b64_tr_b16 a[206:207], v195 offset:16384\n" \
"v_mfma_f32_16x16x32_bf16 v[12:15], a[48:51], a[16:19], v[12:15]\n" \
"s_add_u32 m0, m0, 3072\n" \
"ds_read_b64_tr_b16 a[208:209], v194 offset:2048\n" \
"v_mfma_f32_16x16x32_bf16 v[12:15], a[52:55], a[20:23], v[12:15]\n" \
"buffer_load_dwordx4 v197, s[48:51], s77 offen offset:0 lds\n" \
"v_mfma_f32_16x16x32_bf16 v[8:11], a[40:43], a[16:19], v[8:11]\n" \
"ds_read_b64_tr_b16 a[210:211], v194 offset:5120\n" \
"v_mfma_f32_16x16x32_bf16 v[8:11], a[44:47], a[20:23], v[8:11]\n" \
"ds_read_b64_tr_b16 a[212:213], v194 offset:14336\n" \
"v_mfma_f32_16x16x32_bf16 v[48:51], a[32:35], a[16:19], v[48:51]\n" \
"s_waitcnt lgkmcnt(3)\n" \
"ds_read_b64_tr_b16 a[214:215], v194 offset:17408\n" \
"v_mfma_f32_16x16x32_bf16 v[48:51], a[36:39], a[20:23], v[48:51]\n" \
"buffer_load_dwordx4 v198, s[48:51], s77 offen offset:1024 lds\n" \
"v_mfma_f32_16x16x32_bf16 v[56:59], a[32:35], a[24:27], v[56:59]\n" \
"ds_read_b64_tr_b16 a[216:217], v195 offset:2048\n" \
"v_mfma_f32_16x16x32_bf16 v[56:59], a[36:39], a[28:31], v[56:59]\n" \
"ds_read_b64_tr_b16 a[218:219], v195 offset:5120\n" \
"v_mfma_f32_16x16x32_bf16 v[16:19], a[40:43], a[24:27], v[16:19]\n" \
"ds_read_b64_tr_b16 a[220:221], v195 offset:14336\n" \
"v_mfma_f32_16x16x32_bf16 v[16:19], a[44:47], a[28:31], v[16:19]\n" \
"buffer_load_dwordx4 v199, s[48:51], s77 offen offset:2048 lds\n" \
"v_mfma_f32_16x16x32_bf16 v[20:23], a[48:51], a[24:27], v[20:23]\n" \
"ds_read_b64_tr_b16 a[222:223], v195 offset:17408\n" \
"v_mfma_f32_16x16x32_bf16 v[20:23], a[52:55], a[28:31], v[20:23]\n" \
"v_mfma_f32_16x16x32_bf16 v[28:31], a[48:51], a[192:195], v[28:31]\n" \
"v_mfma_f32_16x16x32_bf16 v[28:31], a[52:55], a[196:199], v[28:31]\n" \
"buffer_load_dword v220, v221, s[56:59], s66 offen sc1\n" \
"v_mfma_f32_16x16x32_bf16 v[24:27], a[40:43], a[192:195], v[24:27]\n" \
"v_mfma_f32_16x16x32_bf16 v[24:27], a[44:47], a[196:199], v[24:27]\n" \
"v_mfma_f32_16x16x32_bf16 v[64:67], a[32:35], a[192:195], v[64:67]\n" \
"v_mfma_f32_16x16x32_bf16 v[64:67], a[36:39], a[196:199], v[64:67]\n" \
"s_waitcnt vmcnt(12) lgkmcnt(0)\n" \
"s_barrier\n" \
"v_mfma_f32_16x16x32_bf16 v[68:71], a[200:203], a[192:195], v[68:71]\n" \
"ds_read_b64_tr_b16 a[32:33], v209 offset:1024\n" \
"ds_read_b64_tr_b16 a[34:35], v209 offset:4096\n" \
"v_mfma_f32_16x16x32_bf16 v[68:71], a[204:207], a[196:199], v[68:71]\n" \
"ds_read_b64_tr_b16 a[36:37], v209 offset:13312\n" \
"ds_read_b64_tr_b16 a[38:39], v209 offset:16384\n" \
"v_mfma_f32_16x16x32_bf16 v[60:63], a[200:203], a[24:27], v[60:63]\n" \
"s_add_u32 m0, s79, 110592\n" \
"ds_read_b64_tr_b16 a[40:41], v209 offset:0\n" \
"v_mfma_f32_16x16x32_bf16 v[60:63], a[204:207], a[28:31], v[60:63]\n" \
"buffer_load_dwordx4 v192, s[44:47], s73 offen offset:0 lds\n" \
"v_mfma_f32_16x16x32_bf16 v[52:55], a[200:203], a[16:19], v[52:55]\n" \
"ds_read_b64_tr_b16 a[42:43], v209 offset:3072\n" \
"v_mfma_f32_16x16x32_bf16 v[52:55], a[204:207], a[20:23], v[52:55]\n" \
"ds_read_b64_tr_b16 a[44:45], v209 offset:12288\n" \
"v_mfma_f32_16x16x32_bf16 v[44:47], a[200:203], a[8:11], v[44:47]\n" \
"ds_read_b64_tr_b16 a[46:47], v209 offset:15360\n" \
"v_mfma_f32_16x16x32_bf16 v[44:47], a[204:207], a[12:15], v[44:47]\n" \
"buffer_load_dwordx4 v193, s[44:47], s73 offen offset:1024 lds\n" \
"v_mfma_f32_16x16x32_bf16 v[72:75], a[0:3], a[208:211], v[72:75]\n" \
"ds_read_b64_tr_b16 a[48:49], v210 offset:0\n" \
"v_mfma_f32_16x16x32_bf16 v[72:75], a[4:7], a[212:215], v[72:75]\n" \
"ds_read_b128 a[0:3], v207 offset:0\n" \
"v_mfma_f32_16x16x32_bf16 v[80:83], a[8:11], a[208:211], v[80:83]\n" \
"ds_read_b128 a[4:7], v208 offset:0\n" \
"v_mfma_f32_16x16x32_bf16 v[80:83], a[12:15], a[212:215], v[80:83]\n" \
"buffer_load_dwordx4 v192, s[44:47], s73 offen offset:2048 lds\n" \
"v_mfma_f32_16x16x32_bf16 v[84:87], a[8:11], a[216:219], v[84:87]\n" \
"ds_read_b64_tr_b16 a[50:51], v210 offset:3072\n" \
"v_mfma_f32_16x16x32_bf16 v[84:87], a[12:15], a[220:223], v[84:87]\n" \
"ds_read_b128 a[8:11], v205 offset:0\n" \
"v_mfma_f32_16x16x32_bf16 v[92:95], a[16:19], a[216:219], v[92:95]\n" \
"ds_read_b128 a[12:15], v206 offset:0\n" \
"v_mfma_f32_16x16x32_bf16 v[92:95], a[20:23], a[220:223], v[92:95]\n" \
"buffer_load_dwordx4 v193, s[44:47], s73 offen offset:3072 lds\n" \
"v_mfma_f32_16x16x32_bf16 v[88:91], a[16:19], a[208:211], v[88:91]\n" \
"ds_read_b64_tr_b16 a[52:53], v210 offset:12288\n" \
"v_mfma_f32_16x16x32_bf16 v[88:91], a[20:23], a[212:215], v[88:91]\n" \
"s_add_u32 m0, s89, 110592\n" \
"ds_read_b128 a[16:19], v205 offset:4096\n" \
"v_mfma_f32_16x16x32_bf16 v[96:99], a[24:27], a[208:211], v[96:99]\n" \
"s_xor_b32 s73, s73, 0x80\n" \
"ds_read_b128 a[20:23], v206 offset:4096\n" \
"v_mfma_f32_16x16x32_bf16 v[96:99], a[28:31], a[212:215], v[96:99]\n" \
"buffer_load_dwordx4 v196, s[44:47], s75 offen lds\n" \
"v_mfma_f32_16x16x32_bf16 v[100:103], a[24:27], a[216:219], v[100:103]\n" \
"s_xor_b32 s75, s75, 0x80\n" \
"s_mul_i32 s76, s80, 393216\n" \
"ds_read_b64_tr_b16 a[54:55], v210 offset:15360\n" \
"v_mfma_f32_16x16x32_bf16 v[100:103], a[28:31], a[220:223], v[100:103]\n" \
"s_add_u32 s76, s76, s78\n" \
"ds_read_b128 a[24:27], v205 offset:8192\n" \
"v_mfma_f32_16x16x32_bf16 v[108:111], a[192:195], a[216:219], v[108:111]\n" \
"s_add_u32 m0, s91, 139264\n" \
"ds_read_b128 a[28:31], v206 offset:8192\n" \
"v_mfma_f32_16x16x32_bf16 v[108:111], a[196:199], a[220:223], v[108:111]\n" \
"buffer_load_dwordx4 v197, s[48:51], s76 offen offset:0 lds\n" \
"v_mfma_f32_16x16x32_bf16 v[104:107], a[192:195], a[208:211], v[104:107]\n" \
"v_mfma_f32_16x16x32_bf16 v[104:107], a[196:199], a[212:215], v[104:107]\n" \
"s_waitcnt lgkmcnt(2)\n" \
"v_mfma_f32_16x16x32_bf16 v[32:35], a[32:35], a[0:3], v[32:35]\n" \
"s_add_u32 s77, s76, 24576\n" \
"ds_read_b128 a[192:195], v205 offset:12288\n" \
"v_mfma_f32_16x16x32_bf16 v[32:35], a[36:39], a[4:7], v[32:35]\n" \
"buffer_load_dwordx4 v198, s[48:51], s76 offen offset:1024 lds\n" \
"v_mfma_f32_16x16x32_bf16 v[40:43], a[32:35], a[8:11], v[40:43]\n" \
"ds_read_b128 a[196:199], v206 offset:12288\n" \
"v_mfma_f32_16x16x32_bf16 v[40:43], a[36:39], a[12:15], v[40:43]\n" \
"ds_read_b64_tr_b16 a[200:201], v210 offset:1024\n" \
"v_mfma_f32_16x16x32_bf16 v[0:3], a[40:43], a[8:11], v[0:3]\n" \
"s_xor_b32 s80, s80, 0x1\n" \
"ds_read_b64_tr_b16 a[202:203], v210 offset:4096\n" \
"v_mfma_f32_16x16x32_bf16 v[0:3], a[44:47], a[12:15], v[0:3]\n" \
"buffer_load_dwordx4 v199, s[48:51], s76 offen offset:2048 lds\n" \
"v_mfma_f32_16x16x32_bf16 v[4:7], a[48:51], a[8:11], v[4:7]\n" \
"ds_read_b64_tr_b16 a[204:205], v210 offset:13312\n" \
"v_mfma_f32_16x16x32_bf16 v[4:7], a[52:55], a[12:15], v[4:7]\n" \
"ds_read_b64_tr_b16 a[206:207], v210 offset:16384\n" \
"v_mfma_f32_16x16x32_bf16 v[12:15], a[48:51], a[16:19], v[12:15]\n" \
"s_add_u32 m0, m0, 3072\n" \
"ds_read_b64_tr_b16 a[208:209], v209 offset:2048\n" \
"v_mfma_f32_16x16x32_bf16 v[12:15], a[52:55], a[20:23], v[12:15]\n" \
"buffer_load_dwordx4 v197, s[48:51], s77 offen offset:0 lds\n" \
"v_mfma_f32_16x16x32_bf16 v[8:11], a[40:43], a[16:19], v[8:11]\n" \
"ds_read_b64_tr_b16 a[210:211], v209 offset:5120\n" \
"v_mfma_f32_16x16x32_bf16 v[8:11], a[44:47], a[20:23], v[8:11]\n" \
"ds_read_b64_tr_b16 a[212:213], v209 offset:14336\n" \
"v_mfma_f32_16x16x32_bf16 v[48:51], a[32:35], a[16:19], v[48:51]\n" \
"s_waitcnt lgkmcnt(3)\n" \
"ds_read_b64_tr_b16 a[214:215], v209 offset:17408\n" \
"v_mfma_f32_16x16x32_bf16 v[48:51], a[36:39], a[20:23], v[48:51]\n" \
"buffer_load_dwordx4 v198, s[48:51], s77 offen offset:1024 lds\n" \
"v_mfma_f32_16x16x32_bf16 v[56:59], a[32:35], a[24:27], v[56:59]\n" \
"ds_read_b64_tr_b16 a[216:217], v210 offset:2048\n" \
"v_mfma_f32_16x16x32_bf16 v[56:59], a[36:39], a[28:31], v[56:59]\n" \
"ds_read_b64_tr_b16 a[218:219], v210 offset:5120\n" \
"v_mfma_f32_16x16x32_bf16 v[16:19], a[40:43], a[24:27], v[16:19]\n" \
"ds_read_b64_tr_b16 a[220:221], v210 offset:14336\n" \
"v_mfma_f32_16x16x32_bf16 v[16:19], a[44:47], a[28:31], v[16:19]\n" \
"buffer_load_dwordx4 v199, s[48:51], s77 offen offset:2048 lds\n" \
"v_mfma_f32_16x16x32_bf16 v[20:23], a[48:51], a[24:27], v[20:23]\n" \
"s_mov_b32 s46, s81\n" \
"s_mov_b32 s47, s82\n" \
"ds_read_b64_tr_b16 a[222:223], v210 offset:17408\n" \
"v_mfma_f32_16x16x32_bf16 v[20:23], a[52:55], a[28:31], v[20:23]\n" \
"s_mov_b32 s48, 0\n" \
"s_mov_b32 s49, s68\n" \
"s_sub_u32 s50, 64, s9\n" \
"v_mfma_f32_16x16x32_bf16 v[28:31], a[48:51], a[192:195], v[28:31]\n" \
"s_cmp_eq_u32 s90, 0\n" \
"s_cselect_b32 s50, s9, s50\n" \
"s_andn2_b32 s50, s50, 3\n" \
"v_mfma_f32_16x16x32_bf16 v[28:31], a[52:55], a[196:199], v[28:31]\n" \
"s_lshl_b32 s50, s50, 12\n" \
"s_waitcnt vmcnt(11)\n" \
"v_cmp_eq_u32 vcc, s60, v220\n" \
"v_mfma_f32_16x16x32_bf16 v[24:27], a[40:43], a[192:195], v[24:27]\n" \
"v_mfma_f32_16x16x32_bf16 v[24:27], a[44:47], a[196:199], v[24:27]\n" \
"s_andn2_b32 s70, s65, vcc_lo\n" \
"s_cbranch_scc1 Lpl5_%=\n" \
"Lpr5_%=:\n" \
"v_mfma_f32_16x16x32_bf16 v[64:67], a[32:35], a[192:195], v[64:67]\n" \
"v_mfma_f32_16x16x32_bf16 v[64:67], a[36:39], a[196:199], v[64:67]\n" \
"s_xor_b32 s82, s48, s49\n" \
"s_cmp_lt_u32 s48, s50\n" \
"s_cselect_b32 s82, s82, s48\n" \
"s_waitcnt vmcnt(12) lgkmcnt(0)\n" \
"s_barrier\n" \
"v_mfma_f32_16x16x32_bf16 v[68:71], a[200:203], a[192:195], v[68:71]\n" \
"ds_read_b64_tr_b16 a[32:33], v204 offset:21504\n" \
"ds_read_b64_tr_b16 a[34:35], v204 offset:24576\n" \
"v_mfma_f32_16x16x32_bf16 v[68:71], a[204:207], a[196:199], v[68:71]\n" \
"s_add_u32 s81, s82, s46\n" \
"s_lshr_b32 s78, s61, 1\n" \
"s_lshl_b32 s80, s78, 9\n" \
"s_add_u32 s80, s80, 511\n" \
"ds_read_b64_tr_b16 a[36:37], v204 offset:33792\n" \
"ds_read_b64_tr_b16 a[38:39], v204 offset:36864\n" \
"v_mfma_f32_16x16x32_bf16 v[60:63], a[200:203], a[24:27], v[60:63]\n" \
"s_add_u32 m0, s86, s79\n" \
"ds_read_b64_tr_b16 a[40:41], v204 offset:20480\n" \
"v_sub_u32 v145, s80, v234\n" \
"v_mfma_f32_16x16x32_bf16 v[60:63], a[204:207], a[28:31], v[60:63]\n" \
"buffer_load_dwordx4 v234, s[56:59], s81 offen offset:0 sc1 lds\n" \
"v_mfma_f32_16x16x32_bf16 v[52:55], a[200:203], a[16:19], v[52:55]\n" \
"s_lshl_b32 s80, s78, 3\n" \
"s_not_b32 s80, s80\n" \
"ds_read_b64_tr_b16 a[42:43], v204 offset:23552\n" \
"v_mfma_f32_16x16x32_bf16 v[52:55], a[204:207], a[20:23], v[52:55]\n" \
"s_lshl_b32 s82, s82, 5\n" \
"ds_read_b64_tr_b16 a[44:45], v204 offset:32768\n" \
"v_sub_u32 v146, s80, v238\n" \
"v_mfma_f32_16x16x32_bf16 v[44:47], a[200:203], a[8:11], v[44:47]\n" \
"s_add_u32 s82, s82, s47\n" \
"ds_read_b64_tr_b16 a[46:47], v204 offset:35840\n" \
"v_subrev_u32 v152, 0, v238\n" \
"v_mfma_f32_16x16x32_bf16 v[44:47], a[204:207], a[12:15], v[44:47]\n" \
"buffer_load_dwordx4 v234, s[56:59], s81 offen offset:1024 sc1 lds\n" \
"v_mfma_f32_16x16x32_bf16 v[72:75], a[0:3], a[208:211], v[72:75]\n" \
"ds_read_b64_tr_b16 a[48:49], v222 offset:20480\n" \
"v_sub_u32 v144, 7, v238\n" \
"v_mfma_f32_16x16x32_bf16 v[72:75], a[4:7], a[212:215], v[72:75]\n" \
"s_add_u32 s48, s48, 0x1000\n" \
"ds_read_b128 a[0:3], v202 offset:0\n" \
"v_or_b32 v152, v152, v144\n" \
"v_mfma_f32_16x16x32_bf16 v[80:83], a[8:11], a[208:211], v[80:83]\n" \
"s_sub_u32 s86, s86, 32768\n" \
"s_cselect_b32 s86, 131072, s86\n" \
"ds_read_b128 a[4:7], v203 offset:0\n" \
"v_mfma_f32_16x16x32_bf16 v[80:83], a[12:15], a[212:215], v[80:83]\n" \
"buffer_load_dwordx4 v234, s[56:59], s81 offen offset:2048 sc1 lds\n" \
"v_mfma_f32_16x16x32_bf16 v[84:87], a[8:11], a[216:219], v[84:87]\n" \
"ds_read_b64_tr_b16 a[50:51], v222 offset:23552\n" \
"v_ashrrev_i32 v152, 31, v152\n" \
"v_mfma_f32_16x16x32_bf16 v[84:87], a[12:15], a[220:223], v[84:87]\n" \
"ds_read_b128 a[8:11], v200 offset:0\n" \
"v_and_b32 v152, 0xff800000, v152\n" \
"v_mfma_f32_16x16x32_bf16 v[92:95], a[16:19], a[216:219], v[92:95]\n" \
"ds_read_b128 a[12:15], v201 offset:0\n" \
"v_sub_u32 v156, -9, v238\n" \
"v_mfma_f32_16x16x32_bf16 v[92:95], a[20:23], a[220:223], v[92:95]\n" \
"buffer_load_dwordx4 v234, s[56:59], s81 offen offset:3072 sc1 lds\n" \
"v_mfma_f32_16x16x32_bf16 v[88:91], a[16:19], a[208:211], v[88:91]\n" \
"ds_read_b64_tr_b16 a[52:53], v222 offset:32768\n" \
"v_ashrrev_i32 v156, 31, v156\n" \
"v_mfma_f32_16x16x32_bf16 v[88:91], a[20:23], a[212:215], v[88:91]\n" \
"ds_read_b128 a[16:19], v200 offset:4096\n" \
"v_and_b32 v156, 0xff800000, v156\n" \
"v_mfma_f32_16x16x32_bf16 v[96:99], a[24:27], a[208:211], v[96:99]\n" \
"s_add_u32 m0, m0, 0x4000\n" \
"ds_read_b128 a[20:23], v201 offset:4096\n" \
"v_add_u32 v148, 0, v146\n" \
"v_mfma_f32_16x16x32_bf16 v[96:99], a[28:31], a[212:215], v[96:99]\n" \
"buffer_load_dwordx4 v216, s[36:39], s82 offen offset:0 lds\n" \
"v_mfma_f32_16x16x32_bf16 v[100:103], a[24:27], a[216:219], v[100:103]\n" \
"ds_read_b64_tr_b16 a[54:55], v222 offset:35840\n" \
"v_or3_b32 v148, v148, v145, s92\n" \
"v_mfma_f32_16x16x32_bf16 v[100:103], a[28:31], a[220:223], v[100:103]\n" \
"ds_read_b128 a[24:27], v200 offset:8192\n" \
"v_ashrrev_i32 v148, 31, v148\n" \
"v_mfma_f32_16x16x32_bf16 v[108:111], a[192:195], a[216:219], v[108:111]\n" \
"ds_read_b128 a[28:31], v201 offset:8192\n" \
"v_and_b32 v148, 0xff800000, v148\n" \
"v_mfma_f32_16x16x32_bf16 v[108:111], a[196:199], a[220:223], v[108:111]\n" \
"buffer_load_dwordx4 v217, s[36:39], s82 offen offset:1024 lds\n" \
"v_mfma_f32_16x16x32_bf16 v[104:107], a[192:195], a[208:211], v[104:107]\n" \
"v_subrev_u32 v153, 1, v238\n" \
"v_sub_u32 v144, 8, v238\n" \
"v_mfma_f32_16x16x32_bf16 v[104:107], a[196:199], a[212:215], v[104:107]\n" \
"s_waitcnt lgkmcnt(2)\n" \
"v_or_b32 v153, v153, v144\n" \
"v_ashrrev_i32 v153, 31, v153\n" \
"v_mfma_f32_16x16x32_bf16 v[32:35], a[32:35], a[0:3], v[32:35]\n" \
"ds_read_b128 a[192:195], v200 offset:12288\n" \
"v_and_b32 v153, 0xff800000, v153\n" \
"v_mfma_f32_16x16x32_bf16 v[32:35], a[36:39], a[4:7], v[32:35]\n" \
"buffer_load_dwordx4 v218, s[36:39], s82 offen offset:2048 lds\n" \
"v_mfma_f32_16x16x32_bf16 v[40:43], a[32:35], a[8:11], v[40:43]\n" \
"ds_read_b128 a[196:199], v201 offset:12288\n" \
"v_sub_u32 v157, -8, v238\n" \
"v_mfma_f32_16x16x32_bf16 v[40:43], a[36:39], a[12:15], v[40:43]\n" \
"ds_read_b64_tr_b16 a[200:201], v222 offset:21504\n" \
"v_ashrrev_i32 v157, 31, v157\n" \
"v_mfma_f32_16x16x32_bf16 v[0:3], a[40:43], a[8:11], v[0:3]\n" \
"ds_read_b64_tr_b16 a[202:203], v222 offset:24576\n" \
"v_and_b32 v157, 0xff800000, v157\n" \
"v_mfma_f32_16x16x32_bf16 v[0:3], a[44:47], a[12:15], v[0:3]\n" \
"buffer_load_dwordx4 v219, s[36:39], s82 offen offset:3072 lds\n" \
"v_mfma_f32_16x16x32_bf16 v[4:7], a[48:51], a[8:11], v[4:7]\n" \
"ds_read_b64_tr_b16 a[204:205], v222 offset:33792\n" \
"v_add_u32 v149, 1, v146\n" \
"v_mfma_f32_16x16x32_bf16 v[4:7], a[52:55], a[12:15], v[4:7]\n" \
"ds_read_b64_tr_b16 a[206:207], v222 offset:36864\n" \
"v_or3_b32 v149, v149, v145, s92\n" \
"v_mfma_f32_16x16x32_bf16 v[12:15], a[48:51], a[16:19], v[12:15]\n" \
"ds_read_b64_tr_b16 a[208:209], v204 offset:22528\n" \
"v_ashrrev_i32 v149, 31, v149\n" \
"v_mfma_f32_16x16x32_bf16 v[12:15], a[52:55], a[20:23], v[12:15]\n" \
"v_and_b32 v149, 0xff800000, v149\n" \
"v_subrev_u32 v154, 2, v238\n" \
"v_mfma_f32_16x16x32_bf16 v[8:11], a[40:43], a[16:19], v[8:11]\n" \
"ds_read_b64_tr_b16 a[210:211], v204 offset:25600\n" \
"v_sub_u32 v144, 9, v238\n" \
"v_mfma_f32_16x16x32_bf16 v[8:11], a[44:47], a[20:23], v[8:11]\n" \
"ds_read_b64_tr_b16 a[212:213], v204 offset:34816\n" \
"v_or_b32 v154, v154, v144\n" \
"v_mfma_f32_16x16x32_bf16 v[48:51], a[32:35], a[16:19], v[48:51]\n" \
"ds_read_b64_tr_b16 a[214:215], v204 offset:37888\n" \
"v_ashrrev_i32 v154, 31, v154\n" \
"v_mfma_f32_16x16x32_bf16 v[48:51], a[36:39], a[20:23], v[48:51]\n" \
"s_waitcnt lgkmcnt(3)\n" \
"v_and_b32 v154, 0xff800000, v154\n" \
"v_sub_u32 v158, -7, v238\n" \
"v_mfma_f32_16x16x32_bf16 v[56:59], a[32:35], a[24:27], v[56:59]\n" \
"ds_read_b64_tr_b16 a[216:217], v222 offset:22528\n" \
"v_ashrrev_i32 v158, 31, v158\n" \
"v_mfma_f32_16x16x32_bf16 v[56:59], a[36:39], a[28:31], v[56:59]\n" \
"ds_read_b64_tr_b16 a[218:219], v222 offset:25600\n" \
"v_and_b32 v158, 0xff800000, v158\n" \
"v_mfma_f32_16x16x32_bf16 v[16:19], a[40:43], a[24:27], v[16:19]\n" \
"ds_read_b64_tr_b16 a[220:221], v222 offset:34816\n" \
"v_add_u32 v150, 2, v146\n" \
"v_mfma_f32_16x16x32_bf16 v[16:19], a[44:47], a[28:31], v[16:19]\n" \
"v_or3_b32 v150, v150, v145, s92\n" \
"v_ashrrev_i32 v150, 31, v150\n" \
"v_mfma_f32_16x16x32_bf16 v[20:23], a[48:51], a[24:27], v[20:23]\n" \
"ds_read_b64_tr_b16 a[222:223], v222 offset:37888\n" \
"v_and_b32 v150, 0xff800000, v150\n" \
"v_mfma_f32_16x16x32_bf16 v[20:23], a[52:55], a[28:31], v[20:23]\n" \
"v_subrev_u32 v155, 3, v238\n" \
"v_sub_u32 v144, 10, v238\n" \
"v_mfma_f32_16x16x32_bf16 v[28:31], a[48:51], a[192:195], v[28:31]\n" \
"v_or_b32 v155, v155, v144\n" \
"v_ashrrev_i32 v155, 31, v155\n" \
"v_mfma_f32_16x16x32_bf16 v[28:31], a[52:55], a[196:199], v[28:31]\n" \
"v_and_b32 v155, 0xff800000, v155\n" \
"v_sub_u32 v159, -6, v238\n" \
"v_mfma_f32_16x16x32_bf16 v[24:27], a[40:43], a[192:195], v[24:27]\n" \
"v_ashrrev_i32 v159, 31, v159\n" \
"v_and_b32 v159, 0xff800000, v159\n" \
"v_mfma_f32_16x16x32_bf16 v[24:27], a[44:47], a[196:199], v[24:27]\n" \
"v_add_u32 v151, 3, v146\n" \
"v_or3_b32 v151, v151, v145, s92\n" \
"v_mfma_f32_16x16x32_bf16 v[64:67], a[32:35], a[192:195], v[64:67]\n" \
"v_ashrrev_i32 v151, 31, v151\n" \
"v_and_b32 v151, 0xff800000, v151\n" \
"v_mfma_f32_16x16x32_bf16 v[64:67], a[36:39], a[196:199], v[64:67]\n" \
"s_xor_b32 s82, s48, s49\n" \
"s_cmp_lt_u32 s48, s50\n" \
"s_cselect_b32 s82, s82, s48\n" \
"s_waitcnt vmcnt(8) lgkmcnt(0)\n" \
"s_barrier\n" \
"v_mfma_f32_16x16x32_bf16 v[68:71], a[200:203], a[192:195], v[68:71]\n" \
"ds_read_b64_tr_b16 a[32:33], v194 offset:1024\n" \
"ds_read_b64_tr_b16 a[34:35], v194 offset:4096\n" \
"v_mfma_f32_16x16x32_bf16 v[68:71], a[204:207], a[196:199], v[68:71]\n" \
"s_add_u32 s81, s82, s46\n" \
"ds_read_b64_tr_b16 a[36:37], v194 offset:13312\n" \
"ds_read_b64_tr_b16 a[38:39], v194 offset:16384\n" \
"v_mfma_f32_16x16x32_bf16 v[60:63], a[200:203], a[24:27], v[60:63]\n" \
"s_add_u32 m0, s86, s79\n" \
"ds_read_b64_tr_b16 a[40:41], v194 offset:0\n" \
"v_mov_b32 v186, 0xffff0000\n" \
"v_mfma_f32_16x16x32_bf16 v[60:63], a[204:207], a[28:31], v[60:63]\n" \
"buffer_load_dwordx4 v234, s[56:59], s81 offen offset:0 sc1 lds\n" \
"v_mfma_f32_16x16x32_bf16 v[52:55], a[200:203], a[16:19], v[52:55]\n" \
"ds_read_b64_tr_b16 a[42:43], v194 offset:3072\n" \
"v_subrev_u32 v144, 0, v238\n" \
"v_mfma_f32_16x16x32_bf16 v[52:55], a[204:207], a[20:23], v[52:55]\n" \
"s_lshl_b32 s82, s82, 5\n" \
"ds_read_b64_tr_b16 a[44:45], v194 offset:12288\n" \
"v_subrev_u32 v184, 1, v238\n" \
"v_mfma_f32_16x16x32_bf16 v[44:47], a[200:203], a[8:11], v[44:47]\n" \
"s_add_u32 s82, s82, s47\n" \
"ds_read_b64_tr_b16 a[46:47], v194 offset:15360\n" \
"v_lshrrev_b32 v144, 16, v144\n" \
"v_mfma_f32_16x16x32_bf16 v[44:47], a[204:207], a[12:15], v[44:47]\n" \
"buffer_load_dwordx4 v234, s[56:59], s81 offen offset:1024 sc1 lds\n" \
"v_mfma_f32_16x16x32_bf16 v[72:75], a[0:3], a[208:211], v[72:75]\n" \
"ds_read_b64_tr_b16 a[48:49], v195 offset:0\n" \
"v_and_or_b32 v184, v184, v186, v144\n" \
"v_mfma_f32_16x16x32_bf16 v[72:75], a[4:7], a[212:215], v[72:75]\n" \
"s_add_u32 s48, s48, 0x1000\n" \
"ds_read_b128 a[0:3], v207 offset:45056\n" \
"v_subrev_u32 v144, 2, v238\n" \
"v_mfma_f32_16x16x32_bf16 v[80:83], a[8:11], a[208:211], v[80:83]\n" \
"s_sub_u32 s86, s86, 32768\n" \
"s_cselect_b32 s86, 131072, s86\n" \
"ds_read_b128 a[4:7], v208 offset:45056\n" \
"v_mfma_f32_16x16x32_bf16 v[80:83], a[12:15], a[212:215], v[80:83]\n" \
"buffer_load_dwordx4 v234, s[56:59], s81 offen offset:2048 sc1 lds\n" \
"v_mfma_f32_16x16x32_bf16 v[84:87], a[8:11], a[216:219], v[84:87]\n" \
"ds_read_b64_tr_b16 a[50:51], v195 offset:3072\n" \
"v_subrev_u32 v185, 3, v238\n" \
"v_mfma_f32_16x16x32_bf16 v[84:87], a[12:15], a[220:223], v[84:87]\n" \
"ds_read_b128 a[8:11], v205 offset:45056\n" \
"v_lshrrev_b32 v144, 16, v144\n" \
"v_mfma_f32_16x16x32_bf16 v[92:95], a[16:19], a[216:219], v[92:95]\n" \
"ds_read_b128 a[12:15], v206 offset:45056\n" \
"v_and_or_b32 v185, v185, v186, v144\n" \
"v_mfma_f32_16x16x32_bf16 v[92:95], a[20:23], a[220:223], v[92:95]\n" \
"buffer_load_dwordx4 v234, s[56:59], s81 offen offset:3072 sc1 lds\n" \
"v_mfma_f32_16x16x32_bf16 v[88:91], a[16:19], a[208:211], v[88:91]\n" \
"ds_read_b64_tr_b16 a[52:53], v195 offset:12288\n" \
"v_mov_b32 v144, 0x3e8293ee\n" \
"v_mfma_f32_16x16x32_bf16 v[88:91], a[20:23], a[212:215], v[88:91]\n" \
"s_add_u32 s80, s79, 117760\n" \
"ds_read_b128 a[16:19], v205 offset:49152\n" \
"v_add_u32 v223, s80, v234\n" \
"v_mfma_f32_16x16x32_bf16 v[96:99], a[24:27], a[208:211], v[96:99]\n" \
"s_add_u32 m0, m0, 0x4000\n" \
"ds_read_b128 a[20:23], v206 offset:49152\n" \
"v_mfma_f32_16x16x32_bf16 v[96:99], a[28:31], a[212:215], v[96:99]\n" \
"buffer_load_dwordx4 v216, s[36:39], s82 offen offset:0 lds\n" \
"v_mfma_f32_16x16x32_bf16 v[100:103], a[24:27], a[216:219], v[100:103]\n" \
"s_cmp_lg_u32 s78, 0\n" \
"s_cselect_b64 s[76:77], -1, 0\n" \
"ds_read_b64_tr_b16 a[54:55], v195 offset:15360\n" \
"v_mfma_f32_16x16x32_bf16 v[100:103], a[28:31], a[220:223], v[100:103]\n" \
"ds_read_b128 a[24:27], v205 offset:53248\n" \
"v_mfma_f32_16x16x32_bf16 v[108:111], a[192:195], a[216:219], v[108:111]\n" \
"ds_read_b128 a[28:31], v206 offset:53248\n" \
"v_mfma_f32_16x16x32_bf16 v[108:111], a[196:199], a[220:223], v[108:111]\n" \
"buffer_load_dwordx4 v217, s[36:39], s82 offen offset:1024 lds\n" \
"v_mfma_f32_16x16x32_bf16 v[104:107], a[192:195], a[208:211], v[104:107]\n" \
"v_mfma_f32_16x16x32_bf16 v[104:107], a[196:199], a[212:215], v[104:107]\n" \
"s_waitcnt lgkmcnt(2)\n" \
"v_mfma_f32_16x16x32_bf16 v[32:35], a[32:35], a[0:3], v[32:35]\n" \
"ds_read_b128 a[192:195], v205 offset:57344\n" \
"v_mfma_f32_16x16x32_bf16 v[32:35], a[36:39], a[4:7], v[32:35]\n" \
"buffer_load_dwordx4 v218, s[36:39], s82 offen offset:2048 lds\n" \
"v_mfma_f32_16x16x32_bf16 v[40:43], a[32:35], a[8:11], v[40:43]\n" \
"ds_read_b128 a[196:199], v206 offset:57344\n" \
"v_mfma_f32_16x16x32_bf16 v[40:43], a[36:39], a[12:15], v[40:43]\n" \
"ds_read_b64_tr_b16 a[200:201], v195 offset:1024\n" \
"v_mfma_f32_16x16x32_bf16 v[0:3], a[40:43], a[8:11], v[0:3]\n" \
"ds_read_b64_tr_b16 a[202:203], v195 offset:4096\n" \
"v_mfma_f32_16x16x32_bf16 v[0:3], a[44:47], a[12:15], v[0:3]\n" \
"buffer_load_dwordx4 v219, s[36:39], s82 offen offset:3072 lds\n" \
"v_mfma_f32_16x16x32_bf16 v[4:7], a[48:51], a[8:11], v[4:7]\n" \
"s_xor_b32 s82, s48, s49\n" \
"s_cmp_lt_u32 s48, s50\n" \
"s_cselect_b32 s82, s82, s48\n" \
"ds_read_b64_tr_b16 a[204:205], v195 offset:13312\n" \
"v_mfma_f32_16x16x32_bf16 v[4:7], a[52:55], a[12:15], v[4:7]\n" \
"s_add_u32 s81, s82, s46\n" \
"ds_read_b64_tr_b16 a[206:207], v195 offset:16384\n" \
"v_mfma_f32_16x16x32_bf16 v[12:15], a[48:51], a[16:19], v[12:15]\n" \
"s_add_u32 m0, s86, s79\n" \
"ds_read_b64_tr_b16 a[208:209], v194 offset:2048\n" \
"v_mfma_f32_16x16x32_bf16 v[12:15], a[52:55], a[20:23], v[12:15]\n" \
"buffer_load_dwordx4 v234, s[56:59], s81 offen offset:0 sc1 lds\n" \
"v_mfma_f32_16x16x32_bf16 v[8:11], a[40:43], a[16:19], v[8:11]\n" \
"ds_read_b64_tr_b16 a[210:211], v194 offset:5120\n" \
"v_mfma_f32_16x16x32_bf16 v[8:11], a[44:47], a[20:23], v[8:11]\n" \
"s_lshl_b32 s82, s82, 5\n" \
"ds_read_b64_tr_b16 a[212:213], v194 offset:14336\n" \
"v_mfma_f32_16x16x32_bf16 v[48:51], a[32:35], a[16:19], v[48:51]\n" \
"s_waitcnt lgkmcnt(3)\n" \
"s_add_u32 s82, s82, s47\n" \
"ds_read_b64_tr_b16 a[214:215], v194 offset:17408\n" \
"v_mfma_f32_16x16x32_bf16 v[48:51], a[36:39], a[20:23], v[48:51]\n" \
"buffer_load_dwordx4 v234, s[56:59], s81 offen offset:1024 sc1 lds\n" \
"v_mfma_f32_16x16x32_bf16 v[56:59], a[32:35], a[24:27], v[56:59]\n" \
"ds_read_b64_tr_b16 a[216:217], v195 offset:2048\n" \
"v_mfma_f32_16x16x32_bf16 v[56:59], a[36:39], a[28:31], v[56:59]\n" \
"s_add_u32 s48, s48, 0x1000\n" \
"ds_read_b64_tr_b16 a[218:219], v195 offset:5120\n" \
"v_mfma_f32_16x16x32_bf16 v[16:19], a[40:43], a[24:27], v[16:19]\n" \
"s_sub_u32 s86, s86, 32768\n" \
"s_cselect_b32 s86, 131072, s86\n" \
"ds_read_b64_tr_b16 a[220:221], v195 offset:14336\n" \
"v_mfma_f32_16x16x32_bf16 v[16:19], a[44:47], a[28:31], v[16:19]\n" \
"buffer_load_dwordx4 v234, s[56:59], s81 offen offset:2048 sc1 lds\n" \
"v_mfma_f32_16x16x32_bf16 v[20:23], a[48:51], a[24:27], v[20:23]\n" \
"ds_read_b64_tr_b16 a[222:223], v195 offset:17408\n" \
"v_mfma_f32_16x16x32_bf16 v[20:23], a[52:55], a[28:31], v[20:23]\n" \
"v_mfma_f32_16x16x32_bf16 v[28:31], a[48:51], a[192:195], v[28:31]\n" \
"v_mfma_f32_16x16x32_bf16 v[28:31], a[52:55], a[196:199], v[28:31]\n" \
"buffer_load_dwordx4 v234, s[56:59], s81 offen offset:3072 sc1 lds\n" \
"v_mfma_f32_16x16x32_bf16 v[24:27], a[40:43], a[192:195], v[24:27]\n" \
"v_mfma_f32_16x16x32_bf16 v[24:27], a[44:47], a[196:199], v[24:27]\n" \
"v_mfma_f32_16x16x32_bf16 v[64:67], a[32:35], a[192:195], v[64:67]\n" \
"s_add_u32 m0, m0, 0x4000\n" \
"v_mfma_f32_16x16x32_bf16 v[64:67], a[36:39], a[196:199], v[64:67]\n" \
"buffer_load_dwordx4 v216, s[36:39], s82 offen offset:0 lds\n" \
"s_waitcnt vmcnt(13) lgkmcnt(0)\n" \
"s_barrier\n" \
"v_mfma_f32_16x16x32_bf16 v[44:47], a[200:203], a[8:11], v[44:47]\n" \
"ds_read_b64_tr_b16 a[96:97], v214 offset:16384\n" \
"v_mfma_f32_16x16x32_bf16 v[44:47], a[204:207], a[12:15], v[44:47]\n" \
"ds_read_b64_tr_b16 a[98:99], v214 offset:18432\n" \
"v_mfma_f32_16x16x32_bf16 v[52:55], a[200:203], a[16:19], v[52:55]\n" \
"ds_read_b64_tr_b16 a[100:101], v214 offset:24576\n" \
"v_mfma_f32_16x16x32_bf16 v[52:55], a[204:207], a[20:23], v[52:55]\n" \
"buffer_load_dwordx4 v217, s[36:39], s82 offen offset:1024 lds\n" \
"v_mfma_f32_16x16x32_bf16 v[60:63], a[200:203], a[24:27], v[60:63]\n" \
"ds_read_b64_tr_b16 a[102:103], v214 offset:26624\n" \
"v_cvt_pk_bf16_f32 v112, v0, v1\n" \
"v_mfma_f32_16x16x32_bf16 v[60:63], a[204:207], a[28:31], v[60:63]\n" \
"ds_read_b128 a[64:67], v213 offset:0\n" \
"v_cvt_pk_bf16_f32 v113, v2, v3\n" \
"v_mfma_f32_16x16x32_bf16 v[68:71], a[200:203], a[192:195], v[68:71]\n" \
"ds_read_b128 a[68:71], v213 offset:2048\n" \
"v_cvt_pk_bf16_f32 v114, v4, v5\n" \
"v_mfma_f32_16x16x32_bf16 v[68:71], a[204:207], a[196:199], v[68:71]\n" \
"buffer_load_dwordx4 v218, s[36:39], s82 offen offset:2048 lds\n" \
"v_mfma_f32_16x16x32_bf16 v[72:75], a[0:3], a[208:211], v[72:75]\n" \
"ds_read_b64_tr_b16 a[104:105], v215 offset:16384\n" \
"v_cvt_pk_bf16_f32 v115, v6, v7\n" \
"v_mfma_f32_16x16x32_bf16 v[72:75], a[4:7], a[212:215], v[72:75]\n" \
"ds_read_b64_tr_b16 a[106:107], v215 offset:18432\n" \
"v_cvt_pk_bf16_f32 v116, v8, v9\n" \
"v_mfma_f32_16x16x32_bf16 v[80:83], a[8:11], a[208:211], v[80:83]\n" \
"ds_read_b64_tr_b16 a[108:109], v215 offset:24576\n" \
"v_cvt_pk_bf16_f32 v117, v10, v11\n" \
"v_mfma_f32_16x16x32_bf16 v[80:83], a[12:15], a[212:215], v[80:83]\n" \
"buffer_load_dwordx4 v219, s[36:39], s82 offen offset:3072 lds\n" \
"v_mfma_f32_16x16x32_bf16 v[84:87], a[8:11], a[216:219], v[84:87]\n" \
"s_xor_b32 s82, s48, s49\n" \
"s_cmp_lt_u32 s48, s50\n" \
"s_cselect_b32 s82, s82, s48\n" \
"ds_read_b64_tr_b16 a[110:111], v215 offset:26624\n" \
"Lpg9657_%=:\n" \
"Lst1x3_%=:\n" \
"s_cbranch_execz Lpg11508_%=\n" \
"v_mfma_f32_16x16x32_bf16 v[84:87], a[12:15], a[220:223], v[84:87]\n" \
"s_add_u32 s81, s82, s46\n" \
"ds_read_b128 a[72:75], v213 offset:256\n" \
"v_cvt_pk_bf16_f32 v118, v12, v13\n" \
"v_mfma_f32_16x16x32_bf16 v[92:95], a[16:19], a[216:219], v[92:95]\n" \
"s_add_u32 m0, s86, s79\n" \
"ds_read_b128 a[76:79], v213 offset:2304\n" \
"v_cvt_pk_bf16_f32 v119, v14, v15\n" \
"v_mfma_f32_16x16x32_bf16 v[92:95], a[20:23], a[220:223], v[92:95]\n" \
"buffer_load_dwordx4 v234, s[56:59], s81 offen offset:0 sc1 lds\n" \
"v_mfma_f32_16x16x32_bf16 v[88:91], a[16:19], a[208:211], v[88:91]\n" \
"ds_read_b128 a[80:83], v213 offset:4096\n" \
"v_cvt_pk_bf16_f32 v120, v16, v17\n" \
"v_mfma_f32_16x16x32_bf16 v[88:91], a[20:23], a[212:215], v[88:91]\n" \
"s_lshl_b32 s82, s82, 5\n" \
"ds_read_b128 a[84:87], v213 offset:6144\n" \
"v_cvt_pk_bf16_f32 v121, v18, v19\n" \
"v_mfma_f32_16x16x32_bf16 v[96:99], a[24:27], a[208:211], v[96:99]\n" \
"s_add_u32 s82, s82, s47\n" \
"v_cvt_pk_bf16_f32 v122, v20, v21\n" \
"v_cvt_pk_bf16_f32 v123, v22, v23\n" \
"v_mfma_f32_16x16x32_bf16 v[96:99], a[28:31], a[212:215], v[96:99]\n" \
"buffer_load_dwordx4 v234, s[56:59], s81 offen offset:1024 sc1 lds\n" \
"v_mfma_f32_16x16x32_bf16 v[100:103], a[24:27], a[216:219], v[100:103]\n" \
"v_cvt_pk_bf16_f32 v124, v24, v25\n" \
"v_cvt_pk_bf16_f32 v125, v26, v27\n" \
"v_mfma_f32_16x16x32_bf16 v[100:103], a[28:31], a[220:223], v[100:103]\n" \
"s_add_u32 s48, s48, 0x1000\n" \
"v_cvt_pk_bf16_f32 v126, v28, v29\n" \
"v_cvt_pk_bf16_f32 v127, v30, v31\n" \
"v_mfma_f32_16x16x32_bf16 v[108:111], a[192:195], a[216:219], v[108:111]\n" \
"s_sub_u32 s86, s86, 32768\n" \
"s_cselect_b32 s86, 131072, s86\n" \
"v_cvt_pk_bf16_f32 v128, v32, v33\n" \
"v_mfma_f32_16x16x32_bf16 v[108:111], a[196:199], a[220:223], v[108:111]\n" \
"buffer_load_dwordx4 v234, s[56:59], s81 offen offset:2048 sc1 lds\n" \
"v_mfma_f32_16x16x32_bf16 v[104:107], a[192:195], a[208:211], v[104:107]\n" \
"v_cvt_pk_bf16_f32 v129, v34, v35\n" \
"v_xor_b32 v145, 0x2000, v223\n" \
"v_mfma_f32_16x16x32_bf16 v[104:107], a[196:199], a[212:215], v[104:107]\n" \
"s_waitcnt lgkmcnt(0)\n" \
"v_cvt_pk_bf16_f32 v192, v72, v73\n" \
"v_cvt_pk_bf16_f32 v193, v74, v75\n" \
"ds_write_b64 v145, v[128:129]\n" \
"s_nop 0\n" \
"ds_write_b64 v145, v[192:193] offset:8\n" \
"v_cvt_pk_bf16_f32 v132, v40, v41\n" \
"v_cvt_pk_bf16_f32 v133, v42, v43\n" \
"v_cvt_pk_bf16_f32 v134, v44, v45\n" \
"v_cvt_pk_bf16_f32 v135, v46, v47\n" \
"v_cvt_pk_bf16_f32 v136, v48, v49\n" \
"v_cvt_pk_bf16_f32 v137, v50, v51\n" \
"v_cvt_pk_bf16_f32 v138, v52, v53\n" \
"v_cvt_pk_bf16_f32 v139, v54, v55\n" \
"v_cvt_pk_bf16_f32 v140, v56, v57\n" \
"v_cvt_pk_bf16_f32 v141, v58, v59\n" \
"v_cvt_pk_bf16_f32 v142, v60, v61\n" \
"v_cvt_pk_bf16_f32 v143, v62, v63\n" \
"v_mfma_f32_16x16x32_bf16 v[0:3], a[96:99], a[64:67], 0\n" \
"ds_read_b128 a[88:91], v213 offset:4352\n" \
"v_mfma_f32_16x16x32_bf16 v[0:3], a[100:103], a[68:71], v[0:3]\n" \
"ds_read_b128 a[92:95], v213 offset:6400\n" \
"v_mfma_f32_16x16x32_bf16 v[4:7], a[104:107], a[64:67], 0\n" \
"ds_read_b64_tr_b16 a[112:113], v214 offset:16896\n" \
"v_mfma_f32_16x16x32_bf16 v[4:7], a[108:111], a[68:71], v[4:7]\n" \
"ds_read_b64_tr_b16 a[114:115], v214 offset:18944\n" \
"v_mfma_f32_16x16x32_bf16 v[20:23], a[104:107], a[72:75], 0\n" \
"buffer_load_dwordx4 v234, s[56:59], s81 offen offset:3072 sc1 lds\n" \
"v_mfma_f32_16x16x32_bf16 v[20:23], a[108:111], a[76:79], v[20:23]\n" \
"ds_read_b64_tr_b16 a[116:117], v214 offset:25088\n" \
"v_cvt_pk_bf16_f32 v64, v64, v65\n" \
"v_cvt_pk_bf16_f32 v65, v66, v67\n" \
"v_cvt_pk_bf16_f32 v66, v68, v69\n" \
"v_mfma_f32_16x16x32_bf16 v[16:19], a[96:99], a[72:75], 0\n" \
"ds_read_b64_tr_b16 a[118:119], v214 offset:27136\n" \
"v_cvt_pk_bf16_f32 v67, v70, v71\n" \
"v_mfma_f32_16x16x32_bf16 v[16:19], a[100:103], a[76:79], v[16:19]\n" \
"s_add_u32 m0, m0, 0x4000\n" \
"ds_read_b64_tr_b16 a[120:121], v215 offset:16896\n" \
"v_cvt_pk_bf16_f32 v194, v80, v81\n" \
"v_cvt_pk_bf16_f32 v195, v82, v83\n" \
"v_cvt_pk_bf16_f32 v74, v84, v85\n" \
"v_cvt_pk_bf16_f32 v75, v86, v87\n" \
"v_mfma_f32_16x16x32_bf16 v[32:35], a[96:99], a[80:83], 0\n" \
"buffer_load_dwordx4 v216, s[36:39], s82 offen offset:0 lds\n" \
"v_mfma_f32_16x16x32_bf16 v[32:35], a[100:103], a[84:87], v[32:35]\n" \
"s_sub_u32 s85, s85, 32768\n" \
"s_cselect_b32 s85, 131072, s85\n" \
"ds_read_b64_tr_b16 a[122:123], v215 offset:18944\n" \
"v_cvt_pk_bf16_f32 v196, v88, v89\n" \
"v_cvt_pk_bf16_f32 v197, v90, v91\n" \
"v_mfma_f32_16x16x32_bf16 v[176:179], v[64:67], v[124:127], v[152:155]\n" \
"v_mfma_f32_16x16x32_bf16 v[172:175], v[140:143], v[124:127], v[156:159]\n" \
"v_mfma_f32_16x16x32_bf16 v[168:171], v[140:143], v[120:123], v[152:155]\n" \
"v_mfma_f32_16x16x32_bf16 v[164:167], v[136:139], v[120:123], v[156:159]\n" \
"v_mfma_f32_16x16x32_bf16 v[160:163], v[136:139], v[116:119], v[152:155]\n" \
"v_mfma_f32_16x16x32_bf16 v[156:159], v[132:135], v[116:119], v[156:159]\n" \
"v_mfma_f32_16x16x32_bf16 v[152:155], v[132:135], v[112:115], v[152:155]\n" \
"v_mfma_f32_16x16x32_bf16 v[36:39], a[104:107], a[80:83], 0\n" \
"ds_read_b64_tr_b16 a[124:125], v215 offset:25088\n" \
"v_add_u32 v213, s85, v211\n" \
"v_mfma_f32_16x16x32_bf16 v[36:39], a[108:111], a[84:87], v[36:39]\n" \
"s_waitcnt lgkmcnt(3)\n" \
"ds_read_b64_tr_b16 a[126:127], v215 offset:27136\n" \
"v_mfma_f32_16x16x32_bf16 v[52:55], a[104:107], a[88:91], 0\n" \
"buffer_load_dwordx4 v217, s[36:39], s82 offen offset:1024 lds\n" \
"v_mfma_f32_16x16x32_bf16 v[52:55], a[108:111], a[92:95], v[52:55]\n" \
"v_add_u32 v214, s85, v212\n" \
"v_xor_b32 v215, 32, v214\n" \
"v_mfma_f32_16x16x32_bf16 v[48:51], a[96:99], a[88:91], 0\n" \
"buffer_load_dwordx4 v218, s[36:39], s82 offen offset:2048 lds\n" \
"v_mfma_f32_16x16x32_bf16 v[48:51], a[100:103], a[92:95], v[48:51]\n" \
"s_waitcnt vmcnt(15) lgkmcnt(0)\n" \
"s_barrier\n" \
"v_mfma_f32_16x16x32_bf16 v[8:11], a[112:115], a[64:67], 0\n" \
"ds_read_b64 v[130:131], v223\n" \
"ds_read_b64_tr_b16 a[96:97], v214 offset:16384\n" \
"ds_read_b64_tr_b16 a[98:99], v214 offset:18432\n" \
"v_mfma_f32_16x16x32_bf16 v[8:11], a[116:119], a[68:71], v[8:11]\n" \
"ds_read_b64 v[72:73], v223 offset:8\n" \
"ds_read_b64_tr_b16 a[100:101], v214 offset:24576\n" \
"ds_read_b64_tr_b16 a[102:103], v214 offset:26624\n" \
"v_mfma_f32_16x16x32_bf16 v[12:15], a[120:123], a[64:67], 0\n" \
"buffer_load_dwordx4 v219, s[36:39], s82 offen offset:3072 lds\n" \
"v_mfma_f32_16x16x32_bf16 v[12:15], a[124:127], a[68:71], v[12:15]\n" \
"s_xor_b32 s82, s48, s49\n" \
"s_cmp_lt_u32 s48, s50\n" \
"s_cselect_b32 s82, s82, s48\n" \
"ds_read_b128 a[64:67], v213 offset:0\n" \
"v_mfma_f32_16x16x32_bf16 v[28:31], a[120:123], a[72:75], 0\n" \
"s_add_u32 s81, s82, s46\n" \
"ds_read_b128 a[68:71], v213 offset:2048\n" \
"v_mfma_f32_16x16x32_bf16 v[28:31], a[124:127], a[76:79], v[28:31]\n" \
"s_add_u32 m0, s86, s79\n" \
"ds_read_b64_tr_b16 a[104:105], v215 offset:16384\n" \
"ds_read_b64_tr_b16 a[106:107], v215 offset:18432\n" \
"v_mfma_f32_16x16x32_bf16 v[24:27], a[112:115], a[72:75], 0\n" \
"buffer_load_dwordx4 v234, s[56:59], s81 offen offset:0 sc1 lds\n" \
"v_mfma_f32_16x16x32_bf16 v[24:27], a[116:119], a[76:79], v[24:27]\n" \
"s_waitcnt lgkmcnt(4)\n" \
"ds_read_b128 a[72:75], v213 offset:256\n" \
"v_mfma_f32_16x16x32_bf16 v[40:43], a[112:115], a[80:83], 0\n" \
"s_lshl_b32 s82, s82, 5\n" \
"ds_read_b128 a[76:79], v213 offset:2304\n" \
"v_mfma_f32_16x16x32_bf16 v[148:151], v[128:131], v[112:115], v[148:151]\n" \
"v_max_f32 v172, v172, v176\n" \
"v_max_f32 v173, v173, v177\n" \
"v_max_f32 v174, v174, v178\n" \
"v_max_f32 v175, v175, v179\n" \
"v_max_f32 v164, v164, v168\n" \
"v_max_f32 v165, v165, v169\n" \
"v_max_f32 v166, v166, v170\n" \
"v_max_f32 v167, v167, v171\n" \
"v_max_f32 v156, v156, v160\n" \
"v_max_f32 v157, v157, v161\n" \
"v_max_f32 v158, v158, v162\n" \
"v_max_f32 v159, v159, v163\n" \
"v_max_f32 v148, v148, v152\n" \
"v_max_f32 v149, v149, v153\n" \
"v_max_f32 v150, v150, v154\n" \
"v_max_f32 v151, v151, v155\n" \
"v_max3_f32 v183, v172, v173, v174\n" \
"v_max3_f32 v182, v164, v165, v166\n" \
"v_max3_f32 v181, v156, v157, v158\n" \
"v_mfma_f32_16x16x32_bf16 v[40:43], a[116:119], a[84:87], v[40:43]\n" \
"s_add_u32 s82, s82, s47\n" \
"ds_read_b64_tr_b16 a[108:109], v215 offset:24576\n" \
"ds_read_b64_tr_b16 a[110:111], v215 offset:26624\n" \
"v_max3_f32 v180, v148, v149, v150\n" \
"v_mfma_f32_16x16x32_bf16 v[44:47], a[120:123], a[80:83], 0\n" \
"buffer_load_dwordx4 v234, s[56:59], s81 offen offset:1024 sc1 lds\n" \
"v_mfma_f32_16x16x32_bf16 v[44:47], a[124:127], a[84:87], v[44:47]\n" \
"s_add_u32 s48, s48, 0x1000\n" \
"s_sub_u32 s86, s86, 32768\n" \
"s_cselect_b32 s86, 131072, s86\n" \
"ds_read_b128 a[80:83], v213 offset:4096\n" \
"v_max_f32 v183, v183, v175\n" \
"v_max_f32 v182, v182, v167\n" \
"v_max_f32 v181, v181, v159\n" \
"v_mfma_f32_16x16x32_bf16 v[60:63], a[120:123], a[88:91], 0\n" \
"s_waitcnt lgkmcnt(4)\n" \
"buffer_load_dwordx4 v234, s[56:59], s81 offen offset:2048 sc1 lds\n" \
"v_max_f32 v180, v180, v151\n" \
"v_mfma_f32_16x16x32_bf16 v[60:63], a[124:127], a[92:95], v[60:63]\n" \
"ds_read_b128 a[84:87], v213 offset:6144\n" \
"v_cvt_pk_bf16_f32 v76, v92, v93\n" \
"v_cvt_pk_bf16_f32 v77, v94, v95\n" \
"v_mfma_f32_16x16x32_bf16 v[56:59], a[112:115], a[88:91], 0\n" \
"buffer_load_dwordx4 v234, s[56:59], s81 offen offset:3072 sc1 lds\n" \
"v_mfma_f32_16x16x32_bf16 v[56:59], a[116:119], a[92:95], v[56:59]\n" \
"s_getpc_b64 s[44:45]\n" \
"Lop_%=:\n" \
"s_add_u32 s44, s44, Lor_%=-Lop_%=\n" \
"s_addc_u32 s45, s45, 0\n" \
"v_permlane32_swap_b32 v180, v181\n" \
"v_permlane32_swap_b32 v182, v183\n" \
"v_max_f32 v180, v180, v181\n" \
"v_mfma_f32_16x16x32_bf16 v[0:3], a[96:99], a[64:67], v[0:3]\n" \
"ds_read_b128 a[88:91], v213 offset:4352\n" \
"v_max_f32 v182, v182, v183\n" \
"v_mfma_f32_16x16x32_bf16 v[0:3], a[100:103], a[68:71], v[0:3]\n" \
"s_waitcnt lgkmcnt(2)\n" \
"ds_read_b128 a[92:95], v213 offset:6400\n" \
"v_cvt_pk_bf16_f32 v198, v96, v97\n" \
"v_cvt_pk_bf16_f32 v199, v98, v99\n" \
"v_permlane16_swap_b32 v180, v182\n" \
"v_mfma_f32_16x16x32_bf16 v[4:7], a[104:107], a[64:67], v[4:7]\n" \
"ds_read_b64_tr_b16 a[112:113], v214 offset:16896\n" \
"v_max_f32 v180, v180, v182\n" \
"v_mfma_f32_16x16x32_bf16 v[4:7], a[108:111], a[68:71], v[4:7]\n" \
"s_add_u32 m0, m0, 0x4000\n" \
"ds_read_b64_tr_b16 a[114:115], v214 offset:18944\n" \
"v_mul_f32 v180, v180, v144\n" \
"v_cvt_pk_bf16_f32 v78, v100, v101\n" \
"v_cvt_pk_bf16_f32 v79, v102, v103\n" \
"v_mov_b32 v182, v180\n" \
"v_mfma_f32_16x16x32_bf16 v[20:23], a[104:107], a[72:75], v[20:23]\n" \
"buffer_load_dwordx4 v216, s[36:39], s82 offen offset:0 lds\n" \
"v_mfma_f32_16x16x32_bf16 v[20:23], a[108:111], a[76:79], v[20:23]\n" \
"ds_read_b64_tr_b16 a[116:117], v214 offset:25088\n" \
"v_cvt_pk_bf16_f32 v200, v104, v105\n" \
"v_cvt_pk_bf16_f32 v201, v106, v107\n" \
"v_permlane16_swap_b32 v180, v182\n" \
"v_mfma_f32_16x16x32_bf16 v[16:19], a[96:99], a[72:75], v[16:19]\n" \
"ds_read_b64_tr_b16 a[118:119], v214 offset:27136\n" \
"v_mov_b32 v181, v180\n" \
"v_mfma_f32_16x16x32_bf16 v[16:19], a[100:103], a[76:79], v[16:19]\n" \
"s_waitcnt lgkmcnt(3)\n" \
"ds_read_b64_tr_b16 a[120:121], v215 offset:16896\n" \
"v_mov_b32 v183, v182\n" \
"v_cvt_pk_bf16_f32 v80, v108, v109\n" \
"v_cvt_pk_bf16_f32 v81, v110, v111\n" \
"v_permlane32_swap_b32 v180, v181\n" \
"v_mfma_f32_16x16x32_bf16 v[32:35], a[96:99], a[80:83], v[32:35]\n" \
"buffer_load_dwordx4 v217, s[36:39], s82 offen offset:1024 lds\n" \
"v_mfma_f32_16x16x32_bf16 v[32:35], a[100:103], a[84:87], v[32:35]\n" \
"s_sub_u32 s85, s85, 32768\n" \
"s_cselect_b32 s85, 131072, s85\n" \
"ds_read_b64_tr_b16 a[122:123], v215 offset:18944\n" \
"v_permlane32_swap_b32 v182, v183\n" \
"v_fma_f32 v172, v172, v144, -v183\n" \
"v_mfma_f32_16x16x32_bf16 v[36:39], a[104:107], a[80:83], v[36:39]\n" \
"ds_read_b64_tr_b16 a[124:125], v215 offset:25088\n" \
"v_add_u32 v213, s85, v211\n" \
"v_mfma_f32_16x16x32_bf16 v[36:39], a[108:111], a[84:87], v[36:39]\n" \
"ds_read_b64_tr_b16 a[126:127], v215 offset:27136\n" \
"v_fma_f32 v164, v164, v144, -v182\n" \
"v_fma_f32 v156, v156, v144, -v181\n" \
"v_fma_f32 v148, v148, v144, -v180\n" \
"v_fma_f32 v173, v173, v144, -v183\n" \
"v_mfma_f32_16x16x32_bf16 v[52:55], a[104:107], a[88:91], v[52:55]\n" \
"buffer_load_dwordx4 v218, s[36:39], s82 offen offset:2048 lds\n" \
"v_mfma_f32_16x16x32_bf16 v[52:55], a[108:111], a[92:95], v[52:55]\n" \
"v_add_u32 v214, s85, v212\n" \
"v_xor_b32 v215, 32, v214\n" \
"v_fma_f32 v165, v165, v144, -v182\n" \
"v_fma_f32 v157, v157, v144, -v181\n" \
"v_mfma_f32_16x16x32_bf16 v[48:51], a[96:99], a[88:91], v[48:51]\n" \
"buffer_load_dwordx4 v219, s[36:39], s82 offen offset:3072 lds\n" \
"v_mfma_f32_16x16x32_bf16 v[48:51], a[100:103], a[92:95], v[48:51]\n" \
"s_xor_b32 s82, s48, s49\n" \
"s_cmp_lt_u32 s48, s50\n" \
"s_cselect_b32 s82, s82, s48\n" \
"v_fma_f32 v149, v149, v144, -v180\n" \
"v_fma_f32 v174, v174, v144, -v183\n" \
"v_fma_f32 v166, v166, v144, -v182\n" \
"s_waitcnt vmcnt(16) lgkmcnt(0)\n" \
"s_barrier\n" \
"v_mfma_f32_16x16x32_bf16 v[8:11], a[112:115], a[64:67], v[8:11]\n" \
"s_add_u32 s81, s82, s46\n" \
"ds_read_b64_tr_b16 a[96:97], v214 offset:16384\n" \
"ds_read_b64_tr_b16 a[98:99], v214 offset:18432\n" \
"v_mfma_f32_16x16x32_bf16 v[8:11], a[116:119], a[68:71], v[8:11]\n" \
"s_add_u32 m0, s86, s79\n" \
"ds_read_b64_tr_b16 a[100:101], v214 offset:24576\n" \
"ds_read_b64_tr_b16 a[102:103], v214 offset:26624\n" \
"v_fma_f32 v158, v158, v144, -v181\n" \
"v_fma_f32 v150, v150, v144, -v180\n" \
"v_mfma_f32_16x16x32_bf16 v[12:15], a[120:123], a[64:67], v[12:15]\n" \
"buffer_load_dwordx4 v234, s[56:59], s81 offen offset:0 sc1 lds\n" \
"v_mfma_f32_16x16x32_bf16 v[12:15], a[124:127], a[68:71], v[12:15]\n" \
"ds_read_b128 a[64:67], v213 offset:0\n" \
"v_fma_f32 v175, v175, v144, -v183\n" \
"v_fma_f32 v167, v167, v144, -v182\n" \
"v_fma_f32 v159, v159, v144, -v181\n" \
"v_fma_f32 v151, v151, v144, -v180\n" \
"v_mfma_f32_16x16x32_bf16 v[28:31], a[120:123], a[72:75], v[28:31]\n" \
"s_lshl_b32 s82, s82, 5\n" \
"ds_read_b128 a[68:71], v213 offset:2048\n" \
"v_mfma_f32_16x16x32_bf16 v[28:31], a[124:127], a[76:79], v[28:31]\n" \
"s_add_u32 s82, s82, s47\n" \
"ds_read_b64_tr_b16 a[104:105], v215 offset:16384\n" \
"ds_read_b64_tr_b16 a[106:107], v215 offset:18432\n" \
"v_exp_f32 v172, v172\n" \
"v_mfma_f32_16x16x32_bf16 v[24:27], a[112:115], a[72:75], v[24:27]\n" \
"buffer_load_dwordx4 v234, s[56:59], s81 offen offset:1024 sc1 lds\n" \
"v_mfma_f32_16x16x32_bf16 v[24:27], a[116:119], a[76:79], v[24:27]\n" \
"ds_read_b128 a[72:75], v213 offset:256\n" \
"v_exp_f32 v164, v164\n" \
"v_exp_f32 v156, v156\n" \
"v_mfma_f32_16x16x32_bf16 v[40:43], a[112:115], a[80:83], v[40:43]\n" \
"s_add_u32 s48, s48, 0x1000\n" \
"ds_read_b128 a[76:79], v213 offset:2304\n" \
"v_mfma_f32_16x16x32_bf16 v[40:43], a[116:119], a[84:87], v[40:43]\n" \
"s_sub_u32 s86, s86, 32768\n" \
"s_cselect_b32 s86, 131072, s86\n" \
"ds_read_b64_tr_b16 a[108:109], v215 offset:24576\n" \
"ds_read_b64_tr_b16 a[110:111], v215 offset:26624\n" \
"v_exp_f32 v148, v148\n" \
"v_mfma_f32_16x16x32_bf16 v[44:47], a[120:123], a[80:83], v[44:47]\n" \
"buffer_load_dwordx4 v234, s[56:59], s81 offen offset:2048 sc1 lds\n" \
"v_mfma_f32_16x16x32_bf16 v[44:47], a[124:127], a[84:87], v[44:47]\n" \
"ds_read_b128 a[80:83], v213 offset:4096\n" \
"v_exp_f32 v173, v173\n" \
"v_exp_f32 v165, v165\n" \
"v_mfma_f32_16x16x32_bf16 v[60:63], a[120:123], a[88:91], v[60:63]\n" \
"s_waitcnt lgkmcnt(4)\n" \
"buffer_load_dwordx4 v234, s[56:59], s81 offen offset:3072 sc1 lds\n" \
"v_mfma_f32_16x16x32_bf16 v[60:63], a[124:127], a[92:95], v[60:63]\n" \
"s_add_u32 m0, m0, 0x4000\n" \
"ds_read_b128 a[84:87], v213 offset:6144\n" \
"v_exp_f32 v157, v157\n" \
"v_mfma_f32_16x16x32_bf16 v[56:59], a[112:115], a[88:91], v[56:59]\n" \
"buffer_load_dwordx4 v216, s[36:39], s82 offen offset:0 lds\n" \
"v_mfma_f32_16x16x32_bf16 v[56:59], a[116:119], a[92:95], v[56:59]\n" \
"v_exp_f32 v149, v149\n" \
"v_exp_f32 v174, v174\n" \
"v_exp_f32 v166, v166\n" \
"v_mfma_f32_16x16x32_bf16 v[0:3], a[96:99], a[64:67], v[0:3]\n" \
"ds_read_b128 a[88:91], v213 offset:4352\n" \
"v_mfma_f32_16x16x32_bf16 v[0:3], a[100:103], a[68:71], v[0:3]\n" \
"s_waitcnt lgkmcnt(2)\n" \
"ds_read_b128 a[92:95], v213 offset:6400\n" \
"v_exp_f32 v158, v158\n" \
"v_mfma_f32_16x16x32_bf16 v[4:7], a[104:107], a[64:67], v[4:7]\n" \
"ds_read_b64_tr_b16 a[112:113], v214 offset:16896\n" \
"v_mfma_f32_16x16x32_bf16 v[4:7], a[108:111], a[68:71], v[4:7]\n" \
"ds_read_b64_tr_b16 a[114:115], v214 offset:18944\n" \
"v_exp_f32 v150, v150\n" \
"v_exp_f32 v175, v175\n" \
"v_mfma_f32_16x16x32_bf16 v[20:23], a[104:107], a[72:75], v[20:23]\n" \
"buffer_load_dwordx4 v217, s[36:39], s82 offen offset:1024 lds\n" \
"v_mfma_f32_16x16x32_bf16 v[20:23], a[108:111], a[76:79], v[20:23]\n" \
"ds_read_b64_tr_b16 a[116:117], v214 offset:25088\n" \
"v_exp_f32 v167, v167\n" \
"v_exp_f32 v159, v159\n" \
"v_mfma_f32_16x16x32_bf16 v[16:19], a[96:99], a[72:75], v[16:19]\n" \
"ds_read_b64_tr_b16 a[118:119], v214 offset:27136\n" \
"v_mfma_f32_16x16x32_bf16 v[16:19], a[100:103], a[76:79], v[16:19]\n" \
"s_waitcnt lgkmcnt(3)\n" \
"ds_read_b64_tr_b16 a[120:121], v215 offset:16896\n" \
"v_exp_f32 v151, v151\n" \
"v_add_f32 v191, v172, v173\n" \
"v_add_f32 v190, v164, v165\n" \
"v_mfma_f32_16x16x32_bf16 v[32:35], a[96:99], a[80:83], v[32:35]\n" \
"buffer_load_dwordx4 v218, s[36:39], s82 offen offset:2048 lds\n" \
"v_mfma_f32_16x16x32_bf16 v[32:35], a[100:103], a[84:87], v[32:35]\n" \
"s_sub_u32 s85, s85, 32768\n" \
"s_cselect_b32 s85, 131072, s85\n" \
"ds_read_b64_tr_b16 a[122:123], v215 offset:18944\n" \
"v_add_f32 v189, v156, v157\n" \
"v_add_f32 v188, v148, v149\n" \
"v_mfma_f32_16x16x32_bf16 v[36:39], a[104:107], a[80:83], v[36:39]\n" \
"ds_read_b64_tr_b16 a[124:125], v215 offset:25088\n" \
"v_add_u32 v213, s85, v211\n" \
"v_mfma_f32_16x16x32_bf16 v[36:39], a[108:111], a[84:87], v[36:39]\n" \
"ds_read_b64_tr_b16 a[126:127], v215 offset:27136\n" \
"v_add_f32 v191, v191, v174\n" \
"v_add_f32 v190, v190, v166\n" \
"v_add_f32 v189, v189, v158\n" \
"v_add_f32 v188, v188, v150\n" \
"v_mfma_f32_16x16x32_bf16 v[52:55], a[104:107], a[88:91], v[52:55]\n" \
"buffer_load_dwordx4 v219, s[36:39], s82 offen offset:3072 lds\n" \
"v_mfma_f32_16x16x32_bf16 v[52:55], a[108:111], a[92:95], v[52:55]\n" \
"s_xor_b32 s82, s48, s49\n" \
"s_cmp_lt_u32 s48, s50\n" \
"s_cselect_b32 s82, s82, s48\n" \
"s_add_u32 s81, s82, s46\n" \
"s_add_u32 m0, s86, s79\n" \
"v_add_u32 v214, s85, v212\n" \
"v_xor_b32 v215, 32, v214\n" \
"v_add_f32 v191, v191, v175\n" \
"v_add_f32 v190, v190, v167\n" \
"v_mfma_f32_16x16x32_bf16 v[48:51], a[96:99], a[88:91], v[48:51]\n" \
"buffer_load_dwordx4 v234, s[56:59], s81 offen offset:0 sc1 lds\n" \
"v_mfma_f32_16x16x32_bf16 v[48:51], a[100:103], a[92:95], v[48:51]\n" \
"v_add_f32 v189, v189, v159\n" \
"v_add_f32 v188, v188, v151\n" \
"s_waitcnt vmcnt(17) lgkmcnt(0)\n" \
"s_barrier\n" \
"v_mfma_f32_16x16x32_bf16 v[8:11], a[112:115], a[64:67], v[8:11]\n" \
"s_lshl_b32 s82, s82, 5\n" \
"ds_read_b64_tr_b16 a[96:97], v214 offset:16384\n" \
"ds_read_b64_tr_b16 a[98:99], v214 offset:18432\n" \
"v_mfma_f32_16x16x32_bf16 v[8:11], a[116:119], a[68:71], v[8:11]\n" \
"s_add_u32 s82, s82, s47\n" \
"ds_read_b64_tr_b16 a[100:101], v214 offset:24576\n" \
"ds_read_b64_tr_b16 a[102:103], v214 offset:26624\n" \
"v_permlane32_swap_b32 v188, v189\n" \
"v_permlane32_swap_b32 v190, v191\n" \
"v_add_f32 v188, v188, v189\n" \
"v_mfma_f32_16x16x32_bf16 v[12:15], a[120:123], a[64:67], v[12:15]\n" \
"buffer_load_dwordx4 v234, s[56:59], s81 offen offset:1024 sc1 lds\n" \
"v_mfma_f32_16x16x32_bf16 v[12:15], a[124:127], a[68:71], v[12:15]\n" \
"ds_read_b128 a[64:67], v213 offset:0\n" \
"v_add_f32 v190, v190, v191\n" \
"v_mfma_f32_16x16x32_bf16 v[28:31], a[120:123], a[72:75], v[28:31]\n" \
"s_add_u32 s48, s48, 0x1000\n" \
"ds_read_b128 a[68:71], v213 offset:2048\n" \
"v_mfma_f32_16x16x32_bf16 v[28:31], a[124:127], a[76:79], v[28:31]\n" \
"s_sub_u32 s86, s86, 32768\n" \
"s_cselect_b32 s86, 131072, s86\n" \
"ds_read_b64_tr_b16 a[104:105], v215 offset:16384\n" \
"ds_read_b64_tr_b16 a[106:107], v215 offset:18432\n" \
"v_permlane16_swap_b32 v188, v190\n" \
"v_add_f32 v188, v188, v190\n" \
"v_rcp_f32 v188, v188\n" \
"v_mfma_f32_16x16x32_bf16 v[24:27], a[112:115], a[72:75], v[24:27]\n" \
"buffer_load_dwordx4 v234, s[56:59], s81 offen offset:2048 sc1 lds\n" \
"v_mfma_f32_16x16x32_bf16 v[24:27], a[116:119], a[76:79], v[24:27]\n" \
"ds_read_b128 a[72:75], v213 offset:256\n" \
"v_mov_b32 v190, v188\n" \
"v_mfma_f32_16x16x32_bf16 v[40:43], a[112:115], a[80:83], v[40:43]\n" \
"ds_read_b128 a[76:79], v213 offset:2304\n" \
"v_mfma_f32_16x16x32_bf16 v[40:43], a[116:119], a[84:87], v[40:43]\n" \
"ds_read_b64_tr_b16 a[108:109], v215 offset:24576\n" \
"ds_read_b64_tr_b16 a[110:111], v215 offset:26624\n" \
"v_permlane16_swap_b32 v188, v190\n" \
"v_mov_b32 v189, v188\n" \
"v_mov_b32 v191, v190\n" \
"v_mfma_f32_16x16x32_bf16 v[44:47], a[120:123], a[80:83], v[44:47]\n" \
"buffer_load_dwordx4 v234, s[56:59], s81 offen offset:3072 sc1 lds\n" \
"v_mfma_f32_16x16x32_bf16 v[44:47], a[124:127], a[84:87], v[44:47]\n" \
"s_add_u32 m0, m0, 0x4000\n" \
"ds_read_b128 a[80:83], v213 offset:4096\n" \
"v_permlane32_swap_b32 v188, v189\n" \
"v_permlane32_swap_b32 v190, v191\n" \
"v_mul_f32 v172, v172, v191\n" \
"v_mul_f32 v164, v164, v190\n" \
"v_mul_f32 v156, v156, v189\n" \
"v_mul_f32 v148, v148, v188\n" \
"v_mul_f32 v173, v173, v191\n" \
"v_mfma_f32_16x16x32_bf16 v[60:63], a[120:123], a[88:91], v[60:63]\n" \
"s_waitcnt lgkmcnt(4)\n" \
"buffer_load_dwordx4 v216, s[36:39], s82 offen offset:0 lds\n" \
"v_mul_f32 v165, v165, v190\n" \
"v_mfma_f32_16x16x32_bf16 v[60:63], a[124:127], a[92:95], v[60:63]\n" \
"ds_read_b128 a[84:87], v213 offset:6144\n" \
"v_mul_f32 v157, v157, v189\n" \
"v_mul_f32 v149, v149, v188\n" \
"v_mfma_f32_16x16x32_bf16 v[56:59], a[112:115], a[88:91], v[56:59]\n" \
"buffer_load_dwordx4 v217, s[36:39], s82 offen offset:1024 lds\n" \
"v_mfma_f32_16x16x32_bf16 v[56:59], a[116:119], a[92:95], v[56:59]\n" \
"v_mul_f32 v174, v174, v191\n" \
"v_mul_f32 v166, v166, v190\n" \
"v_mul_f32 v158, v158, v189\n" \
"v_mul_f32 v150, v150, v188\n" \
"v_mul_f32 v175, v175, v191\n" \
"v_mfma_f32_16x16x32_bf16 v[0:3], a[96:99], a[64:67], v[0:3]\n" \
"ds_read_b128 a[88:91], v213 offset:4352\n" \
"v_mul_f32 v167, v167, v190\n" \
"v_mfma_f32_16x16x32_bf16 v[0:3], a[100:103], a[68:71], v[0:3]\n" \
"s_waitcnt lgkmcnt(2)\n" \
"ds_read_b128 a[92:95], v213 offset:6400\n" \
"v_mul_f32 v159, v159, v189\n" \
"v_mul_f32 v151, v151, v188\n" \
"v_cvt_pk_bf16_f32 v178, v172, v173\n" \
"v_mfma_f32_16x16x32_bf16 v[4:7], a[104:107], a[64:67], v[4:7]\n" \
"ds_read_b64_tr_b16 a[112:113], v214 offset:16896\n" \
"v_cvt_pk_bf16_f32 v179, v174, v175\n" \
"v_mfma_f32_16x16x32_bf16 v[4:7], a[108:111], a[68:71], v[4:7]\n" \
"ds_read_b64_tr_b16 a[114:115], v214 offset:18944\n" \
"v_cvt_pk_bf16_f32 v170, v164, v165\n" \
"v_cvt_pk_bf16_f32 v171, v166, v167\n" \
"v_cvt_pk_bf16_f32 v162, v156, v157\n" \
"v_cvt_pk_bf16_f32 v163, v158, v159\n" \
"v_mfma_f32_16x16x32_bf16 v[20:23], a[104:107], a[72:75], v[20:23]\n" \
"buffer_load_dwordx4 v218, s[36:39], s82 offen offset:2048 lds\n" \
"v_mfma_f32_16x16x32_bf16 v[20:23], a[108:111], a[76:79], v[20:23]\n" \
"ds_read_b64_tr_b16 a[116:117], v214 offset:25088\n" \
"v_cvt_pk_bf16_f32 v154, v148, v149\n" \
"v_cvt_pk_bf16_f32 v155, v150, v151\n" \
"v_and_b32 v176, v178, v184\n" \
"v_mfma_f32_16x16x32_bf16 v[16:19], a[96:99], a[72:75], v[16:19]\n" \
"ds_read_b64_tr_b16 a[118:119], v214 offset:27136\n" \
"v_and_b32 v177, v179, v185\n" \
"v_mfma_f32_16x16x32_bf16 v[16:19], a[100:103], a[76:79], v[16:19]\n" \
"s_waitcnt lgkmcnt(3)\n" \
"ds_read_b64_tr_b16 a[120:121], v215 offset:16896\n" \
"v_xor_b32 v178, v178, v176\n" \
"v_xor_b32 v179, v179, v177\n" \
"v_and_b32 v168, v170, v184\n" \
"v_and_b32 v169, v171, v185\n" \
"v_mfma_f32_16x16x32_bf16 v[32:35], a[96:99], a[80:83], v[32:35]\n" \
"buffer_load_dwordx4 v219, s[36:39], s82 offen offset:3072 lds\n" \
"v_mfma_f32_16x16x32_bf16 v[32:35], a[100:103], a[84:87], v[32:35]\n" \
"s_sub_u32 s85, s85, 32768\n" \
"s_cselect_b32 s85, 131072, s85\n" \
"s_xor_b32 s82, s48, s49\n" \
"s_cmp_lt_u32 s48, s50\n" \
"s_cselect_b32 s82, s82, s48\n" \
"ds_read_b64_tr_b16 a[122:123], v215 offset:18944\n" \
"v_xor_b32 v170, v170, v168\n" \
"v_xor_b32 v171, v171, v169\n" \
"v_mfma_f32_16x16x32_bf16 v[36:39], a[104:107], a[80:83], v[36:39]\n" \
"s_add_u32 s81, s82, s46\n" \
"ds_read_b64_tr_b16 a[124:125], v215 offset:25088\n" \
"v_add_u32 v213, s85, v211\n" \
"v_mfma_f32_16x16x32_bf16 v[36:39], a[108:111], a[84:87], v[36:39]\n" \
"s_add_u32 m0, s86, s79\n" \
"ds_read_b64_tr_b16 a[126:127], v215 offset:27136\n" \
"v_and_b32 v160, v162, v184\n" \
"v_and_b32 v161, v163, v185\n" \
"v_xor_b32 v162, v162, v160\n" \
"v_xor_b32 v163, v163, v161\n" \
"v_mfma_f32_16x16x32_bf16 v[52:55], a[104:107], a[88:91], v[52:55]\n" \
"buffer_load_dwordx4 v234, s[56:59], s81 offen offset:0 sc1 lds\n" \
"v_mfma_f32_16x16x32_bf16 v[52:55], a[108:111], a[92:95], v[52:55]\n" \
"s_lshl_b32 s82, s82, 5\n" \
"s_add_u32 s82, s82, s47\n" \
"v_add_u32 v214, s85, v212\n" \
"v_xor_b32 v215, 32, v214\n" \
"v_and_b32 v152, v154, v184\n" \
"v_and_b32 v153, v155, v185\n" \
"v_mfma_f32_16x16x32_bf16 v[48:51], a[96:99], a[88:91], v[48:51]\n" \
"buffer_load_dwordx4 v234, s[56:59], s81 offen offset:1024 sc1 lds\n" \
"v_mfma_f32_16x16x32_bf16 v[48:51], a[100:103], a[92:95], v[48:51]\n" \
"v_xor_b32 v154, v154, v152\n" \
"v_xor_b32 v155, v155, v153\n" \
"s_waitcnt vmcnt(18) lgkmcnt(0)\n" \
"s_barrier\n" \
"v_mfma_f32_16x16x32_bf16 v[8:11], a[112:115], a[64:67], v[8:11]\n" \
"s_add_u32 s48, s48, 0x1000\n" \
"ds_read_b64_tr_b16 a[96:97], v214 offset:16384\n" \
"ds_read_b64_tr_b16 a[98:99], v214 offset:18432\n" \
"v_mfma_f32_16x16x32_bf16 v[136:139], v[198:201], v[176:179], 0\n" \
"v_mfma_f32_16x16x32_bf16 v[140:143], v[78:81], v[176:179], 0\n" \
"v_mfma_f32_16x16x32_bf16 v[128:131], v[196:199], v[168:171], 0\n" \
"v_mfma_f32_16x16x32_bf16 v[132:135], v[76:79], v[168:171], 0\n" \
"v_mfma_f32_16x16x32_bf16 v[120:123], v[194:197], v[160:163], 0\n" \
"v_mfma_f32_16x16x32_bf16 v[124:127], v[74:77], v[160:163], 0\n" \
"v_mfma_f32_16x16x32_bf16 v[112:115], v[192:195], v[152:155], 0\n" \
"v_mfma_f32_16x16x32_bf16 v[116:119], v[72:75], v[152:155], 0\n" \
"v_mfma_f32_16x16x32_bf16 v[8:11], a[116:119], a[68:71], v[8:11]\n" \
"s_sub_u32 s86, s86, 32768\n" \
"s_cselect_b32 s86, 131072, s86\n" \
"ds_read_b64_tr_b16 a[100:101], v214 offset:24576\n" \
"ds_read_b64_tr_b16 a[102:103], v214 offset:26624\n" \
"v_mfma_f32_16x16x32_bf16 v[12:15], a[120:123], a[64:67], v[12:15]\n" \
"buffer_load_dwordx4 v234, s[56:59], s81 offen offset:2048 sc1 lds\n" \
"v_mfma_f32_16x16x32_bf16 v[12:15], a[124:127], a[68:71], v[12:15]\n" \
"ds_read_b128 a[64:67], v213 offset:0\n" \
"v_cvt_pk_bf16_f32 v136, v136, v137\n" \
"v_cvt_pk_bf16_f32 v137, v138, v139\n" \
"v_cvt_pk_bf16_f32 v138, v140, v141\n" \
"v_cvt_pk_bf16_f32 v139, v142, v143\n" \
"v_cndmask_b32 v140, v136, v138, s[76:77]\n" \
"v_mfma_f32_16x16x32_bf16 v[28:31], a[120:123], a[72:75], v[28:31]\n" \
"ds_read_b128 a[68:71], v213 offset:2048\n" \
"v_cndmask_b32 v141, v137, v139, s[76:77]\n" \
"v_mfma_f32_16x16x32_bf16 v[28:31], a[124:127], a[76:79], v[28:31]\n" \
"ds_read_b64_tr_b16 a[104:105], v215 offset:16384\n" \
"ds_read_b64_tr_b16 a[106:107], v215 offset:18432\n" \
"v_cndmask_b32 v142, v138, v136, s[76:77]\n" \
"v_cndmask_b32 v143, v139, v137, s[76:77]\n" \
"v_mfma_f32_16x16x32_bf16 v[24:27], a[112:115], a[72:75], v[24:27]\n" \
"buffer_load_dwordx4 v234, s[56:59], s81 offen offset:3072 sc1 lds\n" \
"v_mfma_f32_16x16x32_bf16 v[24:27], a[116:119], a[76:79], v[24:27]\n" \
"ds_read_b128 a[72:75], v213 offset:256\n" \
"v_cvt_pk_bf16_f32 v128, v128, v129\n" \
"v_cvt_pk_bf16_f32 v129, v130, v131\n" \
"v_cvt_pk_bf16_f32 v130, v132, v133\n" \
"v_cvt_pk_bf16_f32 v131, v134, v135\n" \
"v_mfma_f32_16x16x32_bf16 v[40:43], a[112:115], a[80:83], v[40:43]\n" \
"ds_read_b128 a[76:79], v213 offset:2304\n" \
"v_cndmask_b32 v132, v128, v130, s[76:77]\n" \
"v_mfma_f32_16x16x32_bf16 v[40:43], a[116:119], a[84:87], v[40:43]\n" \
"s_add_u32 m0, m0, 0x4000\n" \
"ds_read_b64_tr_b16 a[108:109], v215 offset:24576\n" \
"ds_read_b64_tr_b16 a[110:111], v215 offset:26624\n" \
"v_cndmask_b32 v133, v129, v131, s[76:77]\n" \
"v_cndmask_b32 v134, v130, v128, s[76:77]\n" \
"v_mfma_f32_16x16x32_bf16 v[44:47], a[120:123], a[80:83], v[44:47]\n" \
"buffer_load_dwordx4 v216, s[36:39], s82 offen offset:0 lds\n" \
"v_mfma_f32_16x16x32_bf16 v[44:47], a[124:127], a[84:87], v[44:47]\n" \
"ds_read_b128 a[80:83], v213 offset:4096\n" \
"v_cndmask_b32 v135, v131, v129, s[76:77]\n" \
"v_cvt_pk_bf16_f32 v120, v120, v121\n" \
"v_cvt_pk_bf16_f32 v121, v122, v123\n" \
"v_cvt_pk_bf16_f32 v122, v124, v125\n" \
"v_mfma_f32_16x16x32_bf16 v[60:63], a[120:123], a[88:91], v[60:63]\n" \
"s_waitcnt lgkmcnt(4)\n" \
"buffer_load_dwordx4 v217, s[36:39], s82 offen offset:1024 lds\n" \
"v_cvt_pk_bf16_f32 v123, v126, v127\n" \
"v_mfma_f32_16x16x32_bf16 v[60:63], a[124:127], a[92:95], v[60:63]\n" \
"ds_read_b128 a[84:87], v213 offset:6144\n" \
"v_cndmask_b32 v124, v120, v122, s[76:77]\n" \
"v_cndmask_b32 v125, v121, v123, s[76:77]\n" \
"v_mfma_f32_16x16x32_bf16 v[56:59], a[112:115], a[88:91], v[56:59]\n" \
"Lpg10257_%=:\n" \
"s_cbranch_execz Lcb1_%=\n" \
"buffer_load_dwordx4 v218, s[36:39], s82 offen offset:2048 lds\n" \
"v_mfma_f32_16x16x32_bf16 v[56:59], a[116:119], a[92:95], v[56:59]\n" \
"v_cndmask_b32 v126, v122, v120, s[76:77]\n" \
"v_cndmask_b32 v127, v123, v121, s[76:77]\n" \
"v_cvt_pk_bf16_f32 v112, v112, v113\n" \
"v_cvt_pk_bf16_f32 v113, v114, v115\n" \
"v_cvt_pk_bf16_f32 v114, v116, v117\n" \
"v_mfma_f32_16x16x32_bf16 v[0:3], a[96:99], a[64:67], v[0:3]\n" \
"ds_read_b128 a[88:91], v213 offset:4352\n" \
"v_cvt_pk_bf16_f32 v115, v118, v119\n" \
"v_mfma_f32_16x16x32_bf16 v[0:3], a[100:103], a[68:71], v[0:3]\n" \
"s_waitcnt lgkmcnt(2)\n" \
"ds_read_b128 a[92:95], v213 offset:6400\n" \
"v_cndmask_b32 v116, v112, v114, s[76:77]\n" \
"v_cndmask_b32 v117, v113, v115, s[76:77]\n" \
"v_cndmask_b32 v118, v114, v112, s[76:77]\n" \
"v_mfma_f32_16x16x32_bf16 v[4:7], a[104:107], a[64:67], v[4:7]\n" \
"ds_read_b64_tr_b16 a[112:113], v214 offset:16896\n" \
"v_cndmask_b32 v119, v115, v113, s[76:77]\n" \
"v_mfma_f32_16x16x32_bf16 v[4:7], a[108:111], a[68:71], v[4:7]\n" \
"s_add_u32 s71, s93, 208896\n" \
"ds_read_b64_tr_b16 a[114:115], v214 offset:18944\n" \
"buffer_store_dwordx4 v[140:143], v236, s[56:59], s71 offen sc1\n" \
"s_add_u32 s71, s93, 139264\n" \
"v_mfma_f32_16x16x32_bf16 v[20:23], a[104:107], a[72:75], v[20:23]\n" \
"buffer_load_dwordx4 v219, s[36:39], s82 offen offset:3072 lds\n" \
"v_mfma_f32_16x16x32_bf16 v[20:23], a[108:111], a[76:79], v[20:23]\n" \
"s_xor_b32 s82, s48, s49\n" \
"s_cmp_lt_u32 s48, s50\n" \
"s_cselect_b32 s82, s82, s48\n" \
"ds_read_b64_tr_b16 a[116:117], v214 offset:25088\n" \
"buffer_store_dwordx4 v[132:135], v236, s[56:59], s71 offen sc1\n" \
"s_add_u32 s71, s93, 69632\n" \
"v_mfma_f32_16x16x32_bf16 v[16:19], a[96:99], a[72:75], v[16:19]\n" \
"s_waitcnt lgkmcnt(3)\n" \
"s_add_u32 s81, s82, s46\n" \
"ds_read_b64_tr_b16 a[118:119], v214 offset:27136\n" \
"v_mfma_f32_16x16x32_bf16 v[16:19], a[100:103], a[76:79], v[16:19]\n" \
"s_add_u32 m0, s86, s79\n" \
"ds_read_b64_tr_b16 a[120:121], v215 offset:16896\n" \
"buffer_store_dwordx4 v[124:127], v236, s[56:59], s71 offen sc1\n" \
"s_add_u32 s71, s93, 0\n" \
"v_mfma_f32_16x16x32_bf16 v[32:35], a[96:99], a[80:83], v[32:35]\n" \
"buffer_load_dwordx4 v234, s[56:59], s81 offen offset:0 sc1 lds\n" \
"v_mfma_f32_16x16x32_bf16 v[32:35], a[100:103], a[84:87], v[32:35]\n" \
"s_sub_u32 s85, s85, 32768\n" \
"s_cselect_b32 s85, 131072, s85\n" \
"ds_read_b64_tr_b16 a[122:123], v215 offset:18944\n" \
"buffer_store_dwordx4 v[116:119], v236, s[56:59], s71 offen sc1\n" \
"v_mfma_f32_16x16x32_bf16 v[36:39], a[104:107], a[80:83], v[36:39]\n" \
"s_lshl_b32 s82, s82, 5\n" \
"ds_read_b64_tr_b16 a[124:125], v215 offset:25088\n" \
"v_add_u32 v213, s85, v211\n" \
"v_mfma_f32_16x16x32_bf16 v[36:39], a[108:111], a[84:87], v[36:39]\n" \
"s_add_u32 s82, s82, s47\n" \
"ds_read_b64_tr_b16 a[126:127], v215 offset:27136\n" \
"v_mfma_f32_16x16x32_bf16 v[52:55], a[104:107], a[88:91], v[52:55]\n" \
"buffer_load_dwordx4 v234, s[56:59], s81 offen offset:1024 sc1 lds\n" \
"v_mfma_f32_16x16x32_bf16 v[52:55], a[108:111], a[92:95], v[52:55]\n" \
"s_add_u32 s48, s48, 0x1000\n" \
"s_sub_u32 s86, s86, 32768\n" \
"s_cselect_b32 s86, 131072, s86\n" \
"v_add_u32 v214, s85, v212\n" \
"v_xor_b32 v215, 32, v214\n" \
"v_mfma_f32_16x16x32_bf16 v[48:51], a[96:99], a[88:91], v[48:51]\n" \
"buffer_load_dwordx4 v234, s[56:59], s81 offen offset:2048 sc1 lds\n" \
"v_mfma_f32_16x16x32_bf16 v[48:51], a[100:103], a[92:95], v[48:51]\n" \
"s_waitcnt vmcnt(23) lgkmcnt(0)\n" \
"s_barrier\n" \
"v_mfma_f32_16x16x32_bf16 v[8:11], a[112:115], a[64:67], v[8:11]\n" \
"ds_read_b64_tr_b16 a[96:97], v214 offset:16384\n" \
"ds_read_b64_tr_b16 a[98:99], v214 offset:18432\n" \
"v_mfma_f32_16x16x32_bf16 v[8:11], a[116:119], a[68:71], v[8:11]\n" \
"ds_read_b64_tr_b16 a[100:101], v214 offset:24576\n" \
"ds_read_b64_tr_b16 a[102:103], v214 offset:26624\n" \
"v_mfma_f32_16x16x32_bf16 v[12:15], a[120:123], a[64:67], v[12:15]\n" \
"buffer_load_dwordx4 v234, s[56:59], s81 offen offset:3072 sc1 lds\n" \
"v_mfma_f32_16x16x32_bf16 v[12:15], a[124:127], a[68:71], v[12:15]\n" \
"ds_read_b128 a[64:67], v213 offset:0\n" \
"v_mfma_f32_16x16x32_bf16 v[28:31], a[120:123], a[72:75], v[28:31]\n" \
"ds_read_b128 a[68:71], v213 offset:2048\n" \
"v_mfma_f32_16x16x32_bf16 v[28:31], a[124:127], a[76:79], v[28:31]\n" \
"s_add_u32 m0, m0, 0x4000\n" \
"ds_read_b64_tr_b16 a[104:105], v215 offset:16384\n" \
"ds_read_b64_tr_b16 a[106:107], v215 offset:18432\n" \
"v_mfma_f32_16x16x32_bf16 v[24:27], a[112:115], a[72:75], v[24:27]\n" \
"buffer_load_dwordx4 v216, s[36:39], s82 offen offset:0 lds\n" \
"v_mfma_f32_16x16x32_bf16 v[24:27], a[116:119], a[76:79], v[24:27]\n" \
"ds_read_b128 a[72:75], v213 offset:256\n" \
"v_mfma_f32_16x16x32_bf16 v[40:43], a[112:115], a[80:83], v[40:43]\n" \
"ds_read_b128 a[76:79], v213 offset:2304\n" \
"v_mfma_f32_16x16x32_bf16 v[40:43], a[116:119], a[84:87], v[40:43]\n" \
"ds_read_b64_tr_b16 a[108:109], v215 offset:24576\n" \
"ds_read_b64_tr_b16 a[110:111], v215 offset:26624\n" \
"v_mfma_f32_16x16x32_bf16 v[44:47], a[120:123], a[80:83], v[44:47]\n" \
"buffer_load_dwordx4 v217, s[36:39], s82 offen offset:1024 lds\n" \
"v_mfma_f32_16x16x32_bf16 v[44:47], a[124:127], a[84:87], v[44:47]\n" \
"ds_read_b128 a[80:83], v213 offset:4096\n" \
"v_mfma_f32_16x16x32_bf16 v[60:63], a[120:123], a[88:91], v[60:63]\n" \
"s_waitcnt lgkmcnt(4)\n" \
"buffer_load_dwordx4 v218, s[36:39], s82 offen offset:2048 lds\n" \
"v_mfma_f32_16x16x32_bf16 v[60:63], a[124:127], a[92:95], v[60:63]\n" \
"ds_read_b128 a[84:87], v213 offset:6144\n" \
"v_mfma_f32_16x16x32_bf16 v[56:59], a[112:115], a[88:91], v[56:59]\n" \
"buffer_load_dwordx4 v219, s[36:39], s82 offen offset:3072 lds\n" \
"v_mfma_f32_16x16x32_bf16 v[56:59], a[116:119], a[92:95], v[56:59]\n" \
"s_mov_b32 s72, 2\n" \
"Lo0_%=:\n" \
"v_mfma_f32_16x16x32_bf16 v[0:3], a[96:99], a[64:67], v[0:3]\n" \
"s_waitcnt lgkmcnt(1)\n" \
"s_xor_b32 s82, s48, s49\n" \
"ds_read_b128 a[88:91], v213 offset:4352\n" \
"v_mfma_f32_16x16x32_bf16 v[0:3], a[100:103], a[68:71], v[0:3]\n" \
"s_cmp_lt_u32 s48, s50\n" \
"s_cselect_b32 s82, s82, s48\n" \
"ds_read_b128 a[92:95], v213 offset:6400\n" \
"v_mfma_f32_16x16x32_bf16 v[4:7], a[104:107], a[64:67], v[4:7]\n" \
"s_add_u32 s81, s82, s46\n" \
"ds_read_b64_tr_b16 a[112:113], v214 offset:16896\n" \
"v_mfma_f32_16x16x32_bf16 v[4:7], a[108:111], a[68:71], v[4:7]\n" \
"s_add_u32 m0, s86, s79\n" \
"ds_read_b64_tr_b16 a[114:115], v214 offset:18944\n" \
"v_mfma_f32_16x16x32_bf16 v[20:23], a[104:107], a[72:75], v[20:23]\n" \
"buffer_load_dwordx4 v234, s[56:59], s81 offen offset:0 sc1 lds\n" \
"v_mfma_f32_16x16x32_bf16 v[20:23], a[108:111], a[76:79], v[20:23]\n" \
"ds_read_b64_tr_b16 a[116:117], v214 offset:25088\n" \
"v_mfma_f32_16x16x32_bf16 v[16:19], a[96:99], a[72:75], v[16:19]\n" \
"s_lshl_b32 s82, s82, 5\n" \
"ds_read_b64_tr_b16 a[118:119], v214 offset:27136\n" \
"v_mfma_f32_16x16x32_bf16 v[16:19], a[100:103], a[76:79], v[16:19]\n" \
"s_waitcnt lgkmcnt(3)\n" \
"s_add_u32 s82, s82, s47\n" \
"ds_read_b64_tr_b16 a[120:121], v215 offset:16896\n" \
"v_mfma_f32_16x16x32_bf16 v[32:35], a[96:99], a[80:83], v[32:35]\n" \
"buffer_load_dwordx4 v234, s[56:59], s81 offen offset:1024 sc1 lds\n" \
"v_mfma_f32_16x16x32_bf16 v[32:35], a[100:103], a[84:87], v[32:35]\n" \
"s_sub_u32 s85, s85, 32768\n" \
"s_cselect_b32 s85, 131072, s85\n" \
"ds_read_b64_tr_b16 a[122:123], v215 offset:18944\n" \
"v_mfma_f32_16x16x32_bf16 v[36:39], a[104:107], a[80:83], v[36:39]\n" \
"s_add_u32 s48, s48, 0x1000\n" \
"ds_read_b64_tr_b16 a[124:125], v215 offset:25088\n" \
"v_add_u32 v213, s85, v211\n" \
"v_mfma_f32_16x16x32_bf16 v[36:39], a[108:111], a[84:87], v[36:39]\n" \
"s_sub_u32 s86, s86, 32768\n" \
"s_cselect_b32 s86, 131072, s86\n" \
"ds_read_b64_tr_b16 a[126:127], v215 offset:27136\n" \
"v_mfma_f32_16x16x32_bf16 v[52:55], a[104:107], a[88:91], v[52:55]\n" \
"buffer_load_dwordx4 v234, s[56:59], s81 offen offset:2048 sc1 lds\n" \
"v_mfma_f32_16x16x32_bf16 v[52:55], a[108:111], a[92:95], v[52:55]\n" \
"v_add_u32 v214, s85, v212\n" \
"v_xor_b32 v215, 32, v214\n" \
"v_mfma_f32_16x16x32_bf16 v[48:51], a[96:99], a[88:91], v[48:51]\n" \
"buffer_load_dwordx4 v234, s[56:59], s81 offen offset:3072 sc1 lds\n" \
"v_mfma_f32_16x16x32_bf16 v[48:51], a[100:103], a[92:95], v[48:51]\n" \
"s_waitcnt vmcnt(20) lgkmcnt(0)\n" \
"s_barrier\n" \
"v_mfma_f32_16x16x32_bf16 v[8:11], a[112:115], a[64:67], v[8:11]\n" \
"ds_read_b64_tr_b16 a[96:97], v214 offset:16384\n" \
"ds_read_b64_tr_b16 a[98:99], v214 offset:18432\n" \
"v_mfma_f32_16x16x32_bf16 v[8:11], a[116:119], a[68:71], v[8:11]\n" \
"s_add_u32 m0, m0, 0x4000\n" \
"ds_read_b64_tr_b16 a[100:101], v214 offset:24576\n" \
"ds_read_b64_tr_b16 a[102:103], v214 offset:26624\n" \
"v_mfma_f32_16x16x32_bf16 v[12:15], a[120:123], a[64:67], v[12:15]\n" \
"buffer_load_dwordx4 v216, s[36:39], s82 offen offset:0 lds\n" \
"v_mfma_f32_16x16x32_bf16 v[12:15], a[124:127], a[68:71], v[12:15]\n" \
"ds_read_b128 a[64:67], v213 offset:0\n" \
"v_mfma_f32_16x16x32_bf16 v[28:31], a[120:123], a[72:75], v[28:31]\n" \
"ds_read_b128 a[68:71], v213 offset:2048\n" \
"v_mfma_f32_16x16x32_bf16 v[28:31], a[124:127], a[76:79], v[28:31]\n" \
"ds_read_b64_tr_b16 a[104:105], v215 offset:16384\n" \
"ds_read_b64_tr_b16 a[106:107], v215 offset:18432\n" \
"v_mfma_f32_16x16x32_bf16 v[24:27], a[112:115], a[72:75], v[24:27]\n" \
"buffer_load_dwordx4 v217, s[36:39], s82 offen offset:1024 lds\n" \
"v_mfma_f32_16x16x32_bf16 v[24:27], a[116:119], a[76:79], v[24:27]\n" \
"ds_read_b128 a[72:75], v213 offset:256\n" \
"v_mfma_f32_16x16x32_bf16 v[40:43], a[112:115], a[80:83], v[40:43]\n" \
"ds_read_b128 a[76:79], v213 offset:2304\n" \
"v_mfma_f32_16x16x32_bf16 v[40:43], a[116:119], a[84:87], v[40:43]\n" \
"ds_read_b64_tr_b16 a[108:109], v215 offset:24576\n" \
"ds_read_b64_tr_b16 a[110:111], v215 offset:26624\n" \
"v_mfma_f32_16x16x32_bf16 v[44:47], a[120:123], a[80:83], v[44:47]\n" \
"buffer_load_dwordx4 v218, s[36:39], s82 offen offset:2048 lds\n" \
"v_mfma_f32_16x16x32_bf16 v[44:47], a[124:127], a[84:87], v[44:47]\n" \
"ds_read_b128 a[80:83], v213 offset:4096\n" \
"v_mfma_f32_16x16x32_bf16 v[60:63], a[120:123], a[88:91], v[60:63]\n" \
"s_waitcnt lgkmcnt(4)\n" \
"v_mfma_f32_16x16x32_bf16 v[60:63], a[124:127], a[92:95], v[60:63]\n" \
"s_sub_u32 s72, s72, 1\n" \
"ds_read_b128 a[84:87], v213 offset:6144\n" \
"v_mfma_f32_16x16x32_bf16 v[56:59], a[112:115], a[88:91], v[56:59]\n" \
"buffer_load_dwordx4 v219, s[36:39], s82 offen offset:3072 lds\n" \
"v_mfma_f32_16x16x32_bf16 v[56:59], a[116:119], a[92:95], v[56:59]\n" \
"s_cbranch_scc0 Lo0_%=\n" \
"s_setpc_b64 s[44:45]\n" \
"Lor_%=:\n" \
"buffer_store_dword v230, v231, s[56:59], s12 offen sc1\n" \
"s_mov_b32 s72, s10\n" \
"s_call_b64 s[44:45], Lo0_%=\n" \
"buffer_load_dword v220, v221, s[56:59], s67 offen sc1\n" \
"s_mov_b32 s72, 1\n" \
"s_call_b64 s[44:45], Lo0_%=\n" \
"s_waitcnt vmcnt(16)\n" \
"v_cmp_eq_u32 vcc, s60, v220\n" \
"s_nop 1\n" \
"s_andn2_b32 s70, s74, vcc_lo\n" \
"s_cbranch_scc1 Lpl6_%=\n" \
"Lpr6_%=:\n" \
"s_cmp_eq_u32 s90, 0\n" \
"s_cselect_b32 s46, s46, s83\n" \
"s_cselect_b32 s47, s47, s84\n" \
"s_cselect_b32 s48, s48, 0\n" \
"s_cselect_b64 s[36:37], s[36:37], s[40:41]\n" \
"s_cselect_b32 s38, s38, s42\n" \
"s_mov_b32 s72, 3\n" \
"s_call_b64 s[44:45], Lo0_%=\n" \
"s_cmp_eq_u32 s90, 0\n" \
"s_cbranch_scc0 Lrole1_%=\n" \
"s_mov_b32 s72, s11\n" \
"s_call_b64 s[44:45], Lo0_%=\n" \
"buffer_load_dword v220, v241, s[56:59], s64 offen sc1\n" \
"s_mov_b32 s72, 1\n" \
"s_call_b64 s[44:45], Lo0_%=\n" \
"v_mfma_f32_16x16x32_bf16 v[0:3], a[96:99], a[64:67], v[0:3]\n" \
"s_waitcnt lgkmcnt(1)\n" \
"ds_read_b128 a[88:91], v213 offset:4352\n" \
"v_mov_b32 v74, 0x3f80\n" \
"v_mfma_f32_16x16x32_bf16 v[0:3], a[100:103], a[68:71], v[0:3]\n" \
"s_add_u32 s48, s86, 32768\n" \
"s_cmp_eq_u32 s48, 163840\n" \
"s_cselect_b32 s48, 0, s48\n" \
"ds_read_b128 a[92:95], v213 offset:6400\n" \
"v_mfma_f32_16x16x32_bf16 v[4:7], a[104:107], a[64:67], v[4:7]\n" \
"ds_read_b64_tr_b16 a[112:113], v214 offset:16896\n" \
"v_mov_b32 v64, 0\n" \
"v_mfma_f32_16x16x32_bf16 v[4:7], a[108:111], a[68:71], v[4:7]\n" \
"ds_read_b64_tr_b16 a[114:115], v214 offset:18944\n" \
"v_mov_b32 v65, 0\n" \
"v_mfma_f32_16x16x32_bf16 v[20:23], a[104:107], a[72:75], v[20:23]\n" \
"s_add_u32 s49, s48, 32768\n" \
"s_cmp_eq_u32 s49, 163840\n" \
"s_cselect_b32 s49, 0, s49\n" \
"v_mfma_f32_16x16x32_bf16 v[20:23], a[108:111], a[76:79], v[20:23]\n" \
"ds_read_b64_tr_b16 a[116:117], v214 offset:25088\n" \
"v_add_u32 v224, s49, v211\n" \
"v_mfma_f32_16x16x32_bf16 v[16:19], a[96:99], a[72:75], v[16:19]\n" \
"ds_read_b64_tr_b16 a[118:119], v214 offset:27136\n" \
"v_add_u32 v225, s49, v212\n" \
"v_mfma_f32_16x16x32_bf16 v[16:19], a[100:103], a[76:79], v[16:19]\n" \
"s_waitcnt lgkmcnt(3)\n" \
"ds_read_b64_tr_b16 a[120:121], v215 offset:16896\n" \
"v_xor_b32 v226, 32, v225\n" \
"v_mfma_f32_16x16x32_bf16 v[32:35], a[96:99], a[80:83], v[32:35]\n" \
"v_add_u32 v227, s48, v211\n" \
"v_add_u32 v228, s48, v212\n" \
"v_mfma_f32_16x16x32_bf16 v[32:35], a[100:103], a[84:87], v[32:35]\n" \
"s_sub_u32 s85, s85, 32768\n" \
"s_cselect_b32 s85, 131072, s85\n" \
"ds_read_b64_tr_b16 a[122:123], v215 offset:18944\n" \
"v_mfma_f32_16x16x32_bf16 v[36:39], a[104:107], a[80:83], v[36:39]\n" \
"ds_read_b64_tr_b16 a[124:125], v215 offset:25088\n" \
"v_add_u32 v213, s85, v211\n" \
"v_mfma_f32_16x16x32_bf16 v[36:39], a[108:111], a[84:87], v[36:39]\n" \
"ds_read_b64_tr_b16 a[126:127], v215 offset:27136\n" \
"v_xor_b32 v229, 32, v228\n" \
"v_mfma_f32_16x16x32_bf16 v[52:55], a[104:107], a[88:91], v[52:55]\n" \
"v_mov_b32 v68, 0\n" \
"v_mov_b32 v69, 0\n" \
"v_mfma_f32_16x16x32_bf16 v[52:55], a[108:111], a[92:95], v[52:55]\n" \
"v_add_u32 v214, s85, v212\n" \
"v_xor_b32 v215, 32, v214\n" \
"v_mfma_f32_16x16x32_bf16 v[48:51], a[96:99], a[88:91], v[48:51]\n" \
"v_lshrrev_b32 v72, 8, v234\n" \
"v_bfe_u32 v73, v234, 6, 2\n" \
"v_mfma_f32_16x16x32_bf16 v[48:51], a[100:103], a[92:95], v[48:51]\n" \
"s_waitcnt vmcnt(8) lgkmcnt(0)\n" \
"s_barrier\n" \
"s_waitcnt vmcnt(16)\n" \
"v_cmp_eq_u32 vcc, s60, v220\n" \
"s_nop 1\n" \
"s_andn2_b32 s70, s69, vcc_lo\n" \
"s_cbranch_scc1 Lpl7_%=\n" \
"Lpr7_%=:\n" \
"v_mfma_f32_16x16x32_bf16 v[8:11], a[112:115], a[64:67], v[8:11]\n" \
"s_add_u32 s71, s87, 0\n" \
"ds_read_b128 a[0:3], v213 offset:0\n" \
"ds_read_b128 a[4:7], v213 offset:2048\n" \
"v_mfma_f32_16x16x32_bf16 v[8:11], a[116:119], a[68:71], v[8:11]\n" \
"buffer_load_dwordx4 v[176:179], v234, s[56:59], s71 offen offset:0 sc1\n" \
"v_mfma_f32_16x16x32_bf16 v[12:15], a[120:123], a[64:67], v[12:15]\n" \
"ds_read_b64_tr_b16 a[128:129], v214 offset:16384\n" \
"ds_read_b64_tr_b16 a[130:131], v214 offset:18432\n" \
"v_mfma_f32_16x16x32_bf16 v[12:15], a[124:127], a[68:71], v[12:15]\n" \
"buffer_load_dwordx4 v[180:183], v234, s[56:59], s71 offen offset:1024 sc1\n" \
"v_mfma_f32_16x16x32_bf16 v[28:31], a[120:123], a[72:75], v[28:31]\n" \
"ds_read_b64_tr_b16 a[132:133], v214 offset:24576\n" \
"ds_read_b64_tr_b16 a[134:135], v214 offset:26624\n" \
"v_mfma_f32_16x16x32_bf16 v[28:31], a[124:127], a[76:79], v[28:31]\n" \
"buffer_load_dwordx4 v[184:187], v234, s[56:59], s71 offen offset:2048 sc1\n" \
"v_mfma_f32_16x16x32_bf16 v[24:27], a[112:115], a[72:75], v[24:27]\n" \
"ds_read_b64_tr_b16 a[136:137], v215 offset:16384\n" \
"ds_read_b64_tr_b16 a[138:139], v215 offset:18432\n" \
"v_mfma_f32_16x16x32_bf16 v[24:27], a[116:119], a[76:79], v[24:27]\n" \
"buffer_load_dwordx4 v[188:191], v234, s[56:59], s71 offen offset:3072 sc1\n" \
"v_mfma_f32_16x16x32_bf16 v[40:43], a[112:115], a[80:83], v[40:43]\n" \
"ds_read_b64_tr_b16 a[140:141], v215 offset:24576\n" \
"ds_read_b64_tr_b16 a[142:143], v215 offset:26624\n" \
"v_mfma_f32_16x16x32_bf16 v[40:43], a[116:119], a[84:87], v[40:43]\n" \
"ds_read_b64_tr_b16 a[144:145], v214 offset:16896\n" \
"ds_read_b64_tr_b16 a[146:147], v214 offset:18944\n" \
"v_mfma_f32_16x16x32_bf16 v[44:47], a[120:123], a[80:83], v[44:47]\n" \
"ds_read_b64_tr_b16 a[148:149], v214 offset:25088\n" \
"ds_read_b64_tr_b16 a[150:151], v214 offset:27136\n" \
"v_mfma_f32_16x16x32_bf16 v[44:47], a[124:127], a[84:87], v[44:47]\n" \
"ds_read_b64_tr_b16 a[152:153], v215 offset:16896\n" \
"ds_read_b64_tr_b16 a[154:155], v215 offset:18944\n" \
"v_mfma_f32_16x16x32_bf16 v[60:63], a[120:123], a[88:91], v[60:63]\n" \
"ds_read_b64_tr_b16 a[156:157], v215 offset:25088\n" \
"ds_read_b64_tr_b16 a[158:159], v215 offset:27136\n" \
"v_mfma_f32_16x16x32_bf16 v[60:63], a[124:127], a[92:95], v[60:63]\n" \
"ds_read_b128 a[24:27], v213 offset:256\n" \
"ds_read_b128 a[28:31], v213 offset:2304\n" \
"v_mfma_f32_16x16x32_bf16 v[56:59], a[112:115], a[88:91], v[56:59]\n" \
"ds_read_b128 a[8:11], v224 offset:0\n" \
"ds_read_b128 a[12:15], v224 offset:2048\n" \
"v_mfma_f32_16x16x32_bf16 v[56:59], a[116:119], a[92:95], v[56:59]\n" \
"s_waitcnt lgkmcnt(8)\n" \
"ds_read_b64_tr_b16 a[160:161], v225 offset:16384\n" \
"ds_read_b64_tr_b16 a[162:163], v225 offset:18432\n" \
"v_mfma_f32_16x16x32_bf16 v[0:3], a[128:131], a[0:3], v[0:3]\n" \
"ds_read_b64_tr_b16 a[164:165], v225 offset:24576\n" \
"ds_read_b64_tr_b16 a[166:167], v225 offset:26624\n" \
"v_mfma_f32_16x16x32_bf16 v[0:3], a[132:135], a[4:7], v[0:3]\n" \
"ds_read_b64_tr_b16 a[168:169], v226 offset:16384\n" \
"ds_read_b64_tr_b16 a[170:171], v226 offset:18432\n" \
"v_mfma_f32_16x16x32_bf16 v[4:7], a[136:139], a[0:3], v[4:7]\n" \
"s_mov_b32 s70, 0x20000000\n" \
"ds_read_b64_tr_b16 a[172:173], v226 offset:24576\n" \
"ds_read_b64_tr_b16 a[174:175], v226 offset:26624\n" \
"v_mfma_f32_16x16x32_bf16 v[4:7], a[140:143], a[4:7], v[4:7]\n" \
"s_cmp_eq_u32 s64, 65616896\n" \
"s_cselect_b32 s70, 65593344, s70\n" \
"ds_read_b64_tr_b16 a[176:177], v225 offset:16896\n" \
"ds_read_b64_tr_b16 a[178:179], v225 offset:18944\n" \
"buffer_store_dword v230, v231, s[56:59], s70 offen sc1\n" \
"v_mfma_f32_16x16x32_bf16 v[8:11], a[144:147], a[0:3], v[8:11]\n" \
"s_waitcnt lgkmcnt(8)\n" \
"ds_read_b64_tr_b16 a[180:181], v225 offset:25088\n" \
"ds_read_b64_tr_b16 a[182:183], v225 offset:27136\n" \
"v_mfma_f32_16x16x32_bf16 v[8:11], a[148:151], a[4:7], v[8:11]\n" \
"s_sub_u32 s68, s86, 32768\n" \
"s_cselect_b32 s68, 131072, s68\n" \
"ds_read_b64_tr_b16 a[184:185], v226 offset:16896\n" \
"ds_read_b64_tr_b16 a[186:187], v226 offset:18944\n" \
"v_mfma_f32_16x16x32_bf16 v[12:15], a[152:155], a[0:3], v[12:15]\n" \
"ds_read_b64_tr_b16 a[188:189], v226 offset:25088\n" \
"ds_read_b64_tr_b16 a[190:191], v226 offset:27136\n" \
"v_mfma_f32_16x16x32_bf16 v[12:15], a[156:159], a[4:7], v[12:15]\n" \
"s_cmp_lt_u32 s61, 2\n" \
"s_cselect_b32 s68, s86, s68\n" \
"v_cmp_eq_u32 s[16:17], v72, v73\n" \
"v_and_b32 v72, 16, v234\n" \
"v_mfma_f32_16x16x32_bf16 v[28:31], a[152:155], a[24:27], v[28:31]\n" \
"s_and_b32 s50, s61, 1\n" \
"v_lshlrev_b32 v72, v72, v74\n" \
"v_cndmask_b32 v72, 0, v72, s[16:17]\n" \
"v_mfma_f32_16x16x32_bf16 v[28:31], a[156:159], a[28:31], v[28:31]\n" \
"s_mul_i32 s50, s50, 9216\n" \
"v_bfe_i32 v73, v234, 5, 1\n" \
"v_and_b32 v67, v72, v73\n" \
"v_mfma_f32_16x16x32_bf16 v[24:27], a[144:147], a[24:27], v[24:27]\n" \
"s_add_u32 s68, s68, s50\n" \
"s_add_u32 s68, s68, %3\n" \
"v_xor_b32 v66, v72, v67\n" \
"v_mfma_f32_16x16x32_bf16 v[24:27], a[148:151], a[28:31], v[24:27]\n" \
"v_lshrrev_b32 v132, 4, v234\n" \
"v_and_b32 v133, 15, v132\n" \
"v_mul_u32_u24 v128, 144, v133\n" \
"v_mfma_f32_16x16x32_bf16 v[20:23], a[136:139], a[24:27], v[20:23]\n" \
"s_waitcnt vmcnt(5)\n" \
"s_barrier\n" \
"v_mfma_f32_16x16x32_bf16 v[20:23], a[140:143], a[28:31], v[20:23]\n" \
"s_add_u32 s71, s87, 4096\n" \
"ds_read_b128 a[32:35], v224 offset:256\n" \
"ds_read_b128 a[36:39], v224 offset:2304\n" \
"v_mfma_f32_16x16x32_bf16 v[16:19], a[128:131], a[24:27], v[16:19]\n" \
"buffer_load_dwordx4 v[192:195], v234, s[56:59], s71 offen offset:0 sc1\n" \
"v_mfma_f32_16x16x32_bf16 v[16:19], a[132:135], a[28:31], v[16:19]\n" \
"s_waitcnt lgkmcnt(2)\n" \
"ds_read_b128 a[16:19], v227 offset:0\n" \
"ds_read_b128 a[20:23], v227 offset:2048\n" \
"v_mfma_f32_16x16x32_bf16 v[0:3], a[160:163], a[8:11], v[0:3]\n" \
"ds_read_b64_tr_b16 a[192:193], v228 offset:16384\n" \
"ds_read_b64_tr_b16 a[194:195], v228 offset:18432\n" \
"v_mfma_f32_16x16x32_bf16 v[0:3], a[164:167], a[12:15], v[0:3]\n" \
"buffer_load_dwordx4 v[196:199], v234, s[56:59], s71 offen offset:1024 sc1\n" \
"v_mfma_f32_16x16x32_bf16 v[4:7], a[168:171], a[8:11], v[4:7]\n" \
"ds_read_b64_tr_b16 a[196:197], v228 offset:24576\n" \
"ds_read_b64_tr_b16 a[198:199], v228 offset:26624\n" \
"v_mfma_f32_16x16x32_bf16 v[4:7], a[172:175], a[12:15], v[4:7]\n" \
"ds_read_b64_tr_b16 a[200:201], v229 offset:16384\n" \
"ds_read_b64_tr_b16 a[202:203], v229 offset:18432\n" \
"v_mfma_f32_16x16x32_bf16 v[8:11], a[176:179], a[8:11], v[8:11]\n" \
"buffer_load_dwordx4 v[200:203], v234, s[56:59], s71 offen offset:2048 sc1\n" \
"v_mfma_f32_16x16x32_bf16 v[8:11], a[180:183], a[12:15], v[8:11]\n" \
"ds_read_b64_tr_b16 a[204:205], v229 offset:24576\n" \
"ds_read_b64_tr_b16 a[206:207], v229 offset:26624\n" \
"v_lshrrev_b32 v133, 4, v132\n" \
"v_mfma_f32_16x16x32_bf16 v[12:15], a[184:187], a[8:11], v[12:15]\n" \
"s_waitcnt lgkmcnt(6)\n" \
"ds_read_b64_tr_b16 a[208:209], v228 offset:16896\n" \
"ds_read_b64_tr_b16 a[210:211], v228 offset:18944\n" \
"v_mfma_f32_16x16x32_bf16 v[12:15], a[188:191], a[12:15], v[12:15]\n" \
"buffer_load_dwordx4 v[204:207], v234, s[56:59], s71 offen offset:3072 sc1\n" \
"v_mfma_f32_16x16x32_bf16 v[28:31], a[184:187], a[32:35], v[28:31]\n" \
"ds_read_b64_tr_b16 a[212:213], v228 offset:25088\n" \
"ds_read_b64_tr_b16 a[214:215], v228 offset:27136\n" \
"v_mfma_f32_16x16x32_bf16 v[28:31], a[188:191], a[36:39], v[28:31]\n" \
"ds_read_b64_tr_b16 a[216:217], v229 offset:16896\n" \
"ds_read_b64_tr_b16 a[218:219], v229 offset:18944\n" \
"v_mfma_f32_16x16x32_bf16 v[24:27], a[176:179], a[32:35], v[24:27]\n" \
"ds_read_b64_tr_b16 a[220:221], v229 offset:25088\n" \
"ds_read_b64_tr_b16 a[222:223], v229 offset:27136\n" \
"v_mfma_f32_16x16x32_bf16 v[24:27], a[180:183], a[36:39], v[24:27]\n" \
"ds_read_b128 a[40:43], v227 offset:256\n" \
"ds_read_b128 a[44:47], v227 offset:2304\n" \
"v_mfma_f32_16x16x32_bf16 v[20:23], a[168:171], a[32:35], v[20:23]\n" \
"ds_read_b128 a[248:251], v213 offset:4096\n" \
"ds_read_b128 a[252:255], v213 offset:6144\n" \
"v_mfma_f32_16x16x32_bf16 v[20:23], a[172:175], a[36:39], v[20:23]\n" \
"ds_read_b128 a[48:51], v224 offset:4096\n" \
"ds_read_b128 a[52:55], v224 offset:6144\n" \
"v_mfma_f32_16x16x32_bf16 v[16:19], a[160:163], a[32:35], v[16:19]\n" \
"ds_read_b128 a[56:59], v227 offset:4096\n" \
"ds_read_b128 a[60:63], v227 offset:6144\n" \
"v_mfma_f32_16x16x32_bf16 v[16:19], a[164:167], a[36:39], v[16:19]\n" \
"s_waitcnt lgkmcnt(8)\n" \
"ds_read_b128 a[224:227], v213 offset:4352\n" \
"ds_read_b128 a[228:231], v213 offset:6400\n" \
"v_lshl_add_u32 v128, v133, 3, v128\n" \
"v_mfma_f32_16x16x32_bf16 v[0:3], a[192:195], a[16:19], v[0:3]\n" \
"s_waitcnt vmcnt(7)\n" \
"ds_read_b128 a[232:235], v224 offset:4352\n" \
"ds_read_b128 a[236:239], v224 offset:6400\n" \
"v_mfma_f32_16x16x32_bf16 v[0:3], a[196:199], a[20:23], v[0:3]\n" \
"ds_read_b128 a[240:243], v227 offset:4352\n" \
"ds_read_b128 a[244:247], v227 offset:6400\n" \
"v_mfma_f32_16x16x32_bf16 v[0:3], v[66:69], v[176:179], v[0:3]\n" \
"v_add_u32 v128, s68, v128\n" \
"v_lshrrev_b32 v133, 3, v132\n" \
"v_mfma_f32_16x16x32_bf16 v[4:7], a[200:203], a[16:19], v[4:7]\n" \
"v_and_b32 v134, 7, v132\n" \
"v_mul_u32_u24 v129, 144, v133\n" \
"v_mfma_f32_16x16x32_bf16 v[4:7], a[204:207], a[20:23], v[4:7]\n" \
"v_lshl_add_u32 v129, v134, 4, v129\n" \
"v_add_u32 v129, s68, v129\n" \
"v_mfma_f32_16x16x32_bf16 v[4:7], v[64:67], v[176:179], v[4:7]\n" \
"v_lshlrev_b32 v134, 4, v134\n" \
"v_lshl_add_u32 v130, v133, 12, v134\n" \
"v_mfma_f32_16x16x32_bf16 v[8:11], a[208:211], a[16:19], v[8:11]\n" \
"v_mul_f32 v0, 4.0, v0\n" \
"v_mul_f32 v1, 4.0, v1\n" \
"v_mul_f32 v2, 4.0, v2\n" \
"v_mul_f32 v3, 4.0, v3\n" \
"v_cvt_pk_bf16_f32 v136, v0, v1\n" \
"v_cvt_pk_bf16_f32 v137, v2, v3\n" \
"v_mfma_f32_16x16x32_bf16 v[8:11], a[212:215], a[20:23], v[8:11]\n" \
"v_mfma_f32_16x16x32_bf16 v[8:11], v[66:69], v[180:183], v[8:11]\n" \
"ds_write_b64 v128, v[136:137] offset:0\n" \
"v_mfma_f32_16x16x32_bf16 v[12:15], a[216:219], a[16:19], v[12:15]\n" \
"v_mul_f32 v4, 4.0, v4\n" \
"v_mul_f32 v5, 4.0, v5\n" \
"v_mfma_f32_16x16x32_bf16 v[12:15], a[220:223], a[20:23], v[12:15]\n" \
"v_mul_f32 v6, 4.0, v6\n" \
"v_mul_f32 v7, 4.0, v7\n" \
"v_mfma_f32_16x16x32_bf16 v[12:15], v[64:67], v[180:183], v[12:15]\n" \
"s_waitcnt lgkmcnt(1)\n" \
"v_cvt_pk_bf16_f32 v138, v4, v5\n" \
"v_cvt_pk_bf16_f32 v139, v6, v7\n" \
"v_mfma_f32_16x16x32_bf16 v[16:19], a[192:195], a[40:43], v[16:19]\n" \
"s_waitcnt vmcnt(5)\n" \
"v_mfma_f32_16x16x32_bf16 v[16:19], a[196:199], a[44:47], v[16:19]\n" \
"v_mfma_f32_16x16x32_bf16 v[16:19], v[66:69], v[184:187], v[16:19]\n" \
"ds_write_b64 v128, v[138:139] offset:32\n" \
"v_mul_f32 v8, 4.0, v8\n" \
"v_mfma_f32_16x16x32_bf16 v[20:23], a[200:203], a[40:43], v[20:23]\n" \
"v_mul_f32 v9, 4.0, v9\n" \
"v_mul_f32 v10, 4.0, v10\n" \
"v_mfma_f32_16x16x32_bf16 v[20:23], a[204:207], a[44:47], v[20:23]\n" \
"v_mul_f32 v11, 4.0, v11\n" \
"v_cvt_pk_bf16_f32 v136, v8, v9\n" \
"v_mfma_f32_16x16x32_bf16 v[20:23], v[64:67], v[184:187], v[20:23]\n" \
"v_cvt_pk_bf16_f32 v137, v10, v11\n" \
"v_mfma_f32_16x16x32_bf16 v[24:27], a[208:211], a[40:43], v[24:27]\n" \
"v_mfma_f32_16x16x32_bf16 v[24:27], a[212:215], a[44:47], v[24:27]\n" \
"v_mfma_f32_16x16x32_bf16 v[24:27], v[66:69], v[188:191], v[24:27]\n" \
"ds_write_b64 v128, v[136:137] offset:64\n" \
"v_mul_f32 v12, 4.0, v12\n" \
"v_mfma_f32_16x16x32_bf16 v[28:31], a[216:219], a[40:43], v[28:31]\n" \
"v_mul_f32 v13, 4.0, v13\n" \
"v_mul_f32 v14, 4.0, v14\n" \
"v_mfma_f32_16x16x32_bf16 v[28:31], a[220:223], a[44:47], v[28:31]\n" \
"v_mul_f32 v15, 4.0, v15\n" \
"v_cvt_pk_bf16_f32 v138, v12, v13\n" \
"v_mfma_f32_16x16x32_bf16 v[28:31], v[64:67], v[188:191], v[28:31]\n" \
"v_cvt_pk_bf16_f32 v139, v14, v15\n" \
"v_mfma_f32_16x16x32_bf16 v[32:35], a[128:131], a[248:251], v[32:35]\n" \
"ds_write_b64 v128, v[138:139] offset:96\n" \
"v_mfma_f32_16x16x32_bf16 v[32:35], a[132:135], a[252:255], v[32:35]\n" \
"ds_read_b128 v[140:143], v129 offset:0\n" \
"ds_read_b128 v[144:147], v129 offset:1152\n" \
"v_mfma_f32_16x16x32_bf16 v[32:35], a[160:163], a[48:51], v[32:35]\n" \
"s_waitcnt vmcnt(2)\n" \
"v_mul_f32 v16, 4.0, v16\n" \
"v_mul_f32 v17, 4.0, v17\n" \
"v_mfma_f32_16x16x32_bf16 v[32:35], a[164:167], a[52:55], v[32:35]\n" \
"v_mul_f32 v18, 4.0, v18\n" \
"v_mul_f32 v19, 4.0, v19\n" \
"v_mfma_f32_16x16x32_bf16 v[32:35], v[66:69], v[192:195], v[32:35]\n" \
"s_add_u32 s70, s88, 0\n" \
"s_waitcnt lgkmcnt(1)\n" \
"v_cvt_pk_bf16_f32 v136, v16, v17\n" \
"v_mfma_f32_16x16x32_bf16 v[32:35], a[192:195], a[56:59], v[32:35]\n" \
"buffer_store_dwordx4 v[140:143], v130, s[52:55], s70 offen sc1\n" \
"v_cvt_pk_bf16_f32 v137, v18, v19\n" \
"v_mfma_f32_16x16x32_bf16 v[32:35], a[196:199], a[60:63], v[32:35]\n" \
"ds_write_b64 v128, v[136:137] offset:2304\n" \
"v_mfma_f32_16x16x32_bf16 v[36:39], a[204:207], a[60:63], v[36:39]\n" \
"s_add_u32 s70, s88, 32768\n" \
"s_waitcnt lgkmcnt(1)\n" \
"v_mul_f32 v20, 4.0, v20\n" \
"v_mfma_f32_16x16x32_bf16 v[36:39], a[200:203], a[56:59], v[36:39]\n" \
"buffer_store_dwordx4 v[144:147], v130, s[52:55], s70 offen sc1\n" \
"v_mfma_f32_16x16x32_bf16 v[36:39], a[172:175], a[52:55], v[36:39]\n" \
"v_mul_f32 v21, 4.0, v21\n" \
"v_mul_f32 v22, 4.0, v22\n" \
"v_mfma_f32_16x16x32_bf16 v[36:39], a[168:171], a[48:51], v[36:39]\n" \
"v_mul_f32 v23, 4.0, v23\n" \
"v_cvt_pk_bf16_f32 v138, v20, v21\n" \
"v_mfma_f32_16x16x32_bf16 v[36:39], v[64:67], v[192:195], v[36:39]\n" \
"v_cvt_pk_bf16_f32 v139, v22, v23\n" \
"v_mfma_f32_16x16x32_bf16 v[36:39], a[140:143], a[252:255], v[36:39]\n" \
"v_mfma_f32_16x16x32_bf16 v[36:39], a[136:139], a[248:251], v[36:39]\n" \
"ds_write_b64 v128, v[138:139] offset:2336\n" \
"v_mul_f32 v24, 4.0, v24\n" \
"v_mul_f32 v25, 4.0, v25\n" \
"v_mfma_f32_16x16x32_bf16 v[40:43], a[144:147], a[248:251], v[40:43]\n" \
"v_mul_f32 v26, 4.0, v26\n" \
"v_mul_f32 v27, 4.0, v27\n" \
"v_mfma_f32_16x16x32_bf16 v[40:43], a[148:151], a[252:255], v[40:43]\n" \
"v_cvt_pk_bf16_f32 v136, v24, v25\n" \
"v_cvt_pk_bf16_f32 v137, v26, v27\n" \
"v_mfma_f32_16x16x32_bf16 v[40:43], a[176:179], a[48:51], v[40:43]\n" \
"ds_write_b64 v128, v[136:137] offset:2368\n" \
"v_mfma_f32_16x16x32_bf16 v[40:43], a[180:183], a[52:55], v[40:43]\n" \
"v_mul_f32 v28, 4.0, v28\n" \
"v_mul_f32 v29, 4.0, v29\n" \
"v_mfma_f32_16x16x32_bf16 v[40:43], v[66:69], v[196:199], v[40:43]\n" \
"v_mul_f32 v30, 4.0, v30\n" \
"v_mul_f32 v31, 4.0, v31\n" \
"v_mfma_f32_16x16x32_bf16 v[40:43], a[208:211], a[56:59], v[40:43]\n" \
"v_cvt_pk_bf16_f32 v138, v28, v29\n" \
"v_cvt_pk_bf16_f32 v139, v30, v31\n" \
"v_mfma_f32_16x16x32_bf16 v[40:43], a[212:215], a[60:63], v[40:43]\n" \
"ds_write_b64 v128, v[138:139] offset:2400\n" \
"v_mfma_f32_16x16x32_bf16 v[44:47], a[220:223], a[60:63], v[44:47]\n" \
"ds_read_b128 v[148:151], v129 offset:2304\n" \
"ds_read_b128 v[152:155], v129 offset:3456\n" \
"v_mfma_f32_16x16x32_bf16 v[44:47], a[216:219], a[56:59], v[44:47]\n" \
"v_mul_f32 v32, 4.0, v32\n" \
"v_mul_f32 v33, 4.0, v33\n" \
"v_mfma_f32_16x16x32_bf16 v[44:47], a[188:191], a[52:55], v[44:47]\n" \
"v_mul_f32 v34, 4.0, v34\n" \
"v_mul_f32 v35, 4.0, v35\n" \
"v_mfma_f32_16x16x32_bf16 v[44:47], a[184:187], a[48:51], v[44:47]\n" \
"s_add_u32 s70, s88, 65536\n" \
"s_waitcnt lgkmcnt(1)\n" \
"v_cvt_pk_bf16_f32 v136, v32, v33\n" \
"v_mfma_f32_16x16x32_bf16 v[44:47], v[64:67], v[196:199], v[44:47]\n" \
"buffer_store_dwordx4 v[148:151], v130, s[52:55], s70 offen sc1\n" \
"v_cvt_pk_bf16_f32 v137, v34, v35\n" \
"v_mfma_f32_16x16x32_bf16 v[44:47], a[156:159], a[252:255], v[44:47]\n" \
"v_mfma_f32_16x16x32_bf16 v[44:47], a[152:155], a[248:251], v[44:47]\n" \
"s_add_u32 s70, s88, 98304\n" \
"s_waitcnt lgkmcnt(0)\n" \
"ds_write_b64 v128, v[136:137] offset:4608\n" \
"v_mul_f32 v36, 4.0, v36\n" \
"v_mfma_f32_16x16x32_bf16 v[48:51], a[128:131], a[224:227], v[48:51]\n" \
"buffer_store_dwordx4 v[152:155], v130, s[52:55], s70 offen sc1\n" \
"v_mul_f32 v37, 4.0, v37\n" \
"v_mfma_f32_16x16x32_bf16 v[48:51], a[132:135], a[228:231], v[48:51]\n" \
"v_mul_f32 v38, 4.0, v38\n" \
"v_mul_f32 v39, 4.0, v39\n" \
"Lpg10852_%=:\n" \
"s_cbranch_execz Lcb1_%=\n" \
"v_mfma_f32_16x16x32_bf16 v[48:51], a[160:163], a[232:235], v[48:51]\n" \
"s_waitcnt vmcnt(4)\n" \
"v_cvt_pk_bf16_f32 v138, v36, v37\n" \
"v_cvt_pk_bf16_f32 v139, v38, v39\n" \
"v_mfma_f32_16x16x32_bf16 v[48:51], a[164:167], a[236:239], v[48:51]\n" \
"ds_write_b64 v128, v[138:139] offset:4640\n" \
"v_mul_f32 v40, 4.0, v40\n" \
"v_mfma_f32_16x16x32_bf16 v[48:51], v[66:69], v[200:203], v[48:51]\n" \
"v_mul_f32 v41, 4.0, v41\n" \
"v_mul_f32 v42, 4.0, v42\n" \
"v_mfma_f32_16x16x32_bf16 v[48:51], a[192:195], a[240:243], v[48:51]\n" \
"v_mul_f32 v43, 4.0, v43\n" \
"v_cvt_pk_bf16_f32 v136, v40, v41\n" \
"v_mfma_f32_16x16x32_bf16 v[48:51], a[196:199], a[244:247], v[48:51]\n" \
"v_cvt_pk_bf16_f32 v137, v42, v43\n" \
"v_mfma_f32_16x16x32_bf16 v[52:55], a[204:207], a[244:247], v[52:55]\n" \
"ds_write_b64 v128, v[136:137] offset:4672\n" \
"v_mul_f32 v44, 4.0, v44\n" \
"v_mul_f32 v45, 4.0, v45\n" \
"v_mfma_f32_16x16x32_bf16 v[52:55], a[200:203], a[240:243], v[52:55]\n" \
"v_mul_f32 v46, 4.0, v46\n" \
"v_mul_f32 v47, 4.0, v47\n" \
"v_mfma_f32_16x16x32_bf16 v[52:55], a[172:175], a[236:239], v[52:55]\n" \
"v_cvt_pk_bf16_f32 v138, v44, v45\n" \
"v_cvt_pk_bf16_f32 v139, v46, v47\n" \
"v_mfma_f32_16x16x32_bf16 v[52:55], a[168:171], a[232:235], v[52:55]\n" \
"ds_write_b64 v128, v[138:139] offset:4704\n" \
"v_mfma_f32_16x16x32_bf16 v[52:55], v[64:67], v[200:203], v[52:55]\n" \
"ds_read_b128 v[156:159], v129 offset:4608\n" \
"ds_read_b128 v[160:163], v129 offset:5760\n" \
"v_mfma_f32_16x16x32_bf16 v[52:55], a[140:143], a[228:231], v[52:55]\n" \
"v_mul_f32 v48, 4.0, v48\n" \
"v_mul_f32 v49, 4.0, v49\n" \
"v_mfma_f32_16x16x32_bf16 v[52:55], a[136:139], a[224:227], v[52:55]\n" \
"v_mul_f32 v50, 4.0, v50\n" \
"v_mul_f32 v51, 4.0, v51\n" \
"v_mfma_f32_16x16x32_bf16 v[56:59], a[144:147], a[224:227], v[56:59]\n" \
"s_add_u32 s70, s88, 131072\n" \
"s_waitcnt lgkmcnt(1)\n" \
"v_cvt_pk_bf16_f32 v136, v48, v49\n" \
"v_mfma_f32_16x16x32_bf16 v[56:59], a[148:151], a[228:231], v[56:59]\n" \
"buffer_store_dwordx4 v[156:159], v130, s[52:55], s70 offen sc1\n" \
"v_cvt_pk_bf16_f32 v137, v50, v51\n" \
"v_mfma_f32_16x16x32_bf16 v[56:59], a[176:179], a[232:235], v[56:59]\n" \
"ds_write_b64 v128, v[136:137] offset:6912\n" \
"v_mfma_f32_16x16x32_bf16 v[56:59], a[180:183], a[236:239], v[56:59]\n" \
"s_add_u32 s70, s88, 163840\n" \
"s_waitcnt lgkmcnt(1)\n" \
"v_mul_f32 v52, 4.0, v52\n" \
"v_mfma_f32_16x16x32_bf16 v[56:59], v[66:69], v[204:207], v[56:59]\n" \
"buffer_store_dwordx4 v[160:163], v130, s[52:55], s70 offen sc1\n" \
"v_mfma_f32_16x16x32_bf16 v[56:59], a[208:211], a[240:243], v[56:59]\n" \
"v_mul_f32 v53, 4.0, v53\n" \
"v_mul_f32 v54, 4.0, v54\n" \
"v_mfma_f32_16x16x32_bf16 v[56:59], a[212:215], a[244:247], v[56:59]\n" \
"v_mul_f32 v55, 4.0, v55\n" \
"v_cvt_pk_bf16_f32 v138, v52, v53\n" \
"v_cvt_pk_bf16_f32 v139, v54, v55\n" \
"v_mfma_f32_16x16x32_bf16 v[60:63], a[220:223], a[244:247], v[60:63]\n" \
"ds_write_b64 v128, v[138:139] offset:6944\n" \
"v_mfma_f32_16x16x32_bf16 v[60:63], a[216:219], a[240:243], v[60:63]\n" \
"v_mfma_f32_16x16x32_bf16 v[60:63], a[188:191], a[236:239], v[60:63]\n" \
"v_mfma_f32_16x16x32_bf16 v[60:63], a[184:187], a[232:235], v[60:63]\n" \
"v_mul_f32 v56, 4.0, v56\n" \
"v_mul_f32 v57, 4.0, v57\n" \
"v_mfma_f32_16x16x32_bf16 v[60:63], v[64:67], v[204:207], v[60:63]\n" \
"v_mul_f32 v58, 4.0, v58\n" \
"v_mul_f32 v59, 4.0, v59\n" \
"v_mfma_f32_16x16x32_bf16 v[60:63], a[156:159], a[228:231], v[60:63]\n" \
"v_cvt_pk_bf16_f32 v136, v56, v57\n" \
"v_cvt_pk_bf16_f32 v137, v58, v59\n" \
"v_mfma_f32_16x16x32_bf16 v[60:63], a[152:155], a[224:227], v[60:63]\n" \
"ds_write_b64 v128, v[136:137] offset:6976\n" \
"s_nop 7\n" \
"v_mul_f32 v60, 4.0, v60\n" \
"v_mul_f32 v61, 4.0, v61\n" \
"v_mul_f32 v62, 4.0, v62\n" \
"v_mul_f32 v63, 4.0, v63\n" \
"v_cvt_pk_bf16_f32 v138, v60, v61\n" \
"v_cvt_pk_bf16_f32 v139, v62, v63\n" \
"ds_write_b64 v128, v[138:139] offset:7008\n" \
"ds_read_b128 v[164:167], v129 offset:6912\n" \
"ds_read_b128 v[168:171], v129 offset:8064\n" \
"s_add_u32 s70, s88, 196608\n" \
"s_waitcnt lgkmcnt(1)\n" \
"buffer_store_dwordx4 v[164:167], v130, s[52:55], s70 offen sc1\n" \
"s_add_u32 s70, s88, 229376\n" \
"s_waitcnt lgkmcnt(0)\n" \
"buffer_store_dwordx4 v[168:171], v130, s[52:55], s70 offen sc1\n" \
"s_endpgm\n" \
".p2align 12\n" \
"Lrole1_%=:\n" \
"v_mfma_f32_16x16x32_bf16 v[64:67], a[96:99], a[64:67], 0\n" \
"Lpg10946_%=:\n" \
"s_cbranch_execz Lcb1_%=\n" \
"s_waitcnt lgkmcnt(1)\n" \
"s_xor_b32 s82, s48, s49\n" \
"ds_read_b128 a[88:91], v213 offset:4352\n" \
"v_mfma_f32_16x16x32_bf16 v[64:67], a[100:103], a[68:71], v[64:67]\n" \
"s_cmp_lt_u32 s48, s50\n" \
"s_cselect_b32 s82, s82, s48\n" \
"ds_read_b128 a[92:95], v213 offset:6400\n" \
"v_mfma_f32_16x16x32_bf16 v[68:71], a[104:107], a[64:67], 0\n" \
"s_add_u32 s81, s82, s46\n" \
"ds_read_b64_tr_b16 a[112:113], v214 offset:16896\n" \
"v_mfma_f32_16x16x32_bf16 v[68:71], a[108:111], a[68:71], v[68:71]\n" \
"s_add_u32 m0, s86, s79\n" \
"ds_read_b64_tr_b16 a[114:115], v214 offset:18944\n" \
"v_mfma_f32_16x16x32_bf16 v[84:87], a[104:107], a[72:75], 0\n" \
"buffer_load_dwordx4 v234, s[56:59], s81 offen offset:0 sc1 lds\n" \
"v_mfma_f32_16x16x32_bf16 v[84:87], a[108:111], a[76:79], v[84:87]\n" \
"s_waitcnt lgkmcnt(3)\n" \
"ds_read_b64_tr_b16 a[116:117], v214 offset:25088\n" \
"v_mfma_f32_16x16x32_bf16 v[80:83], a[96:99], a[72:75], 0\n" \
"s_lshl_b32 s82, s82, 5\n" \
"ds_read_b64_tr_b16 a[118:119], v214 offset:27136\n" \
"v_cvt_pk_bf16_f32 v0, v0, v1\n" \
"v_mfma_f32_16x16x32_bf16 v[80:83], a[100:103], a[76:79], v[80:83]\n" \
"s_add_u32 s82, s82, s47\n" \
"ds_read_b64_tr_b16 a[120:121], v215 offset:16896\n" \
"v_cvt_pk_bf16_f32 v1, v2, v3\n" \
"v_mfma_f32_16x16x32_bf16 v[96:99], a[96:99], a[80:83], 0\n" \
"buffer_load_dwordx4 v234, s[56:59], s81 offen offset:1024 sc1 lds\n" \
"v_mfma_f32_16x16x32_bf16 v[96:99], a[100:103], a[84:87], v[96:99]\n" \
"s_sub_u32 s85, s85, 32768\n" \
"s_cselect_b32 s85, 131072, s85\n" \
"ds_read_b64_tr_b16 a[122:123], v215 offset:18944\n" \
"v_cvt_pk_bf16_f32 v2, v4, v5\n" \
"v_mfma_f32_16x16x32_bf16 v[100:103], a[104:107], a[80:83], 0\n" \
"s_add_u32 s48, s48, 0x1000\n" \
"ds_read_b64_tr_b16 a[124:125], v215 offset:25088\n" \
"v_add_u32 v213, s85, v211\n" \
"v_mfma_f32_16x16x32_bf16 v[100:103], a[108:111], a[84:87], v[100:103]\n" \
"s_waitcnt lgkmcnt(3)\n" \
"s_sub_u32 s86, s86, 32768\n" \
"s_cselect_b32 s86, 131072, s86\n" \
"ds_read_b64_tr_b16 a[126:127], v215 offset:27136\n" \
"v_mfma_f32_16x16x32_bf16 v[116:119], a[104:107], a[88:91], 0\n" \
"buffer_load_dwordx4 v234, s[56:59], s81 offen offset:2048 sc1 lds\n" \
"v_mfma_f32_16x16x32_bf16 v[116:119], a[108:111], a[92:95], v[116:119]\n" \
"v_add_u32 v214, s85, v212\n" \
"v_xor_b32 v215, 32, v214\n" \
"v_cvt_pk_bf16_f32 v3, v6, v7\n" \
"v_mfma_f32_16x16x32_bf16 v[112:115], a[96:99], a[88:91], 0\n" \
"buffer_load_dwordx4 v234, s[56:59], s81 offen offset:3072 sc1 lds\n" \
"v_mfma_f32_16x16x32_bf16 v[112:115], a[100:103], a[92:95], v[112:115]\n" \
"s_waitcnt vmcnt(20) lgkmcnt(0)\n" \
"s_barrier\n" \
"v_mfma_f32_16x16x32_bf16 v[72:75], a[112:115], a[64:67], 0\n" \
"ds_read_b64_tr_b16 a[96:97], v214 offset:16384\n" \
"ds_read_b64_tr_b16 a[98:99], v214 offset:18432\n" \
"v_mfma_f32_16x16x32_bf16 v[72:75], a[116:119], a[68:71], v[72:75]\n" \
"s_add_u32 m0, m0, 0x4000\n" \
"ds_read_b64_tr_b16 a[100:101], v214 offset:24576\n" \
"ds_read_b64_tr_b16 a[102:103], v214 offset:26624\n" \
"v_cvt_pk_bf16_f32 v4, v8, v9\n" \
"v_mfma_f32_16x16x32_bf16 v[76:79], a[120:123], a[64:67], 0\n" \
"buffer_load_dwordx4 v216, s[36:39], s82 offen offset:0 lds\n" \
"v_mfma_f32_16x16x32_bf16 v[76:79], a[124:127], a[68:71], v[76:79]\n" \
"ds_read_b128 a[64:67], v213 offset:0\n" \
"v_cvt_pk_bf16_f32 v5, v10, v11\n" \
"v_mfma_f32_16x16x32_bf16 v[92:95], a[120:123], a[72:75], 0\n" \
"ds_read_b128 a[68:71], v213 offset:2048\n" \
"v_cvt_pk_bf16_f32 v6, v12, v13\n" \
"v_mfma_f32_16x16x32_bf16 v[92:95], a[124:127], a[76:79], v[92:95]\n" \
"ds_read_b64_tr_b16 a[104:105], v215 offset:16384\n" \
"ds_read_b64_tr_b16 a[106:107], v215 offset:18432\n" \
"v_cvt_pk_bf16_f32 v7, v14, v15\n" \
"v_mfma_f32_16x16x32_bf16 v[88:91], a[112:115], a[72:75], 0\n" \
"buffer_load_dwordx4 v217, s[36:39], s82 offen offset:1024 lds\n" \
"v_mfma_f32_16x16x32_bf16 v[88:91], a[116:119], a[76:79], v[88:91]\n" \
"ds_read_b128 a[72:75], v213 offset:256\n" \
"v_cvt_pk_bf16_f32 v8, v16, v17\n" \
"v_mfma_f32_16x16x32_bf16 v[104:107], a[112:115], a[80:83], 0\n" \
"ds_read_b128 a[76:79], v213 offset:2304\n" \
"v_cvt_pk_bf16_f32 v9, v18, v19\n" \
"v_mfma_f32_16x16x32_bf16 v[104:107], a[116:119], a[84:87], v[104:107]\n" \
"ds_read_b64_tr_b16 a[108:109], v215 offset:24576\n" \
"ds_read_b64_tr_b16 a[110:111], v215 offset:26624\n" \
"v_cvt_pk_bf16_f32 v10, v20, v21\n" \
"v_mfma_f32_16x16x32_bf16 v[108:111], a[120:123], a[80:83], 0\n" \
"buffer_load_dwordx4 v218, s[36:39], s82 offen offset:2048 lds\n" \
"v_mfma_f32_16x16x32_bf16 v[108:111], a[124:127], a[84:87], v[108:111]\n" \
"s_add_u32 s71, s87, 0\n" \
"ds_read_b128 a[80:83], v213 offset:4096\n" \
"v_cvt_pk_bf16_f32 v11, v22, v23\n" \
"v_mfma_f32_16x16x32_bf16 v[124:127], a[120:123], a[88:91], 0\n" \
"s_waitcnt lgkmcnt(4)\n" \
"buffer_store_dwordx4 v[0:3], v234, s[56:59], s71 offen offset:0 sc1\n" \
"v_mfma_f32_16x16x32_bf16 v[124:127], a[124:127], a[92:95], v[124:127]\n" \
"ds_read_b128 a[84:87], v213 offset:6144\n" \
"v_cvt_pk_bf16_f32 v12, v24, v25\n" \
"v_mfma_f32_16x16x32_bf16 v[120:123], a[112:115], a[88:91], 0\n" \
"buffer_load_dwordx4 v219, s[36:39], s82 offen offset:3072 lds\n" \
"v_mfma_f32_16x16x32_bf16 v[120:123], a[116:119], a[92:95], v[120:123]\n" \
"buffer_store_dwordx4 v[4:7], v234, s[56:59], s71 offen offset:1024 sc1\n" \
"buffer_store_dwordx4 v[8:11], v234, s[56:59], s71 offen offset:2048 sc1\n" \
"v_mfma_f32_16x16x32_bf16 v[64:67], a[96:99], a[64:67], v[64:67]\n" \
"s_xor_b32 s82, s48, s49\n" \
"ds_read_b128 a[88:91], v213 offset:4352\n" \
"v_cvt_pk_bf16_f32 v13, v26, v27\n" \
"v_mfma_f32_16x16x32_bf16 v[64:67], a[100:103], a[68:71], v[64:67]\n" \
"s_waitcnt lgkmcnt(2)\n" \
"s_cmp_lt_u32 s48, s50\n" \
"s_cselect_b32 s82, s82, s48\n" \
"ds_read_b128 a[92:95], v213 offset:6400\n" \
"v_mfma_f32_16x16x32_bf16 v[68:71], a[104:107], a[64:67], v[68:71]\n" \
"s_add_u32 s81, s82, s46\n" \
"ds_read_b64_tr_b16 a[112:113], v214 offset:16896\n" \
"v_cvt_pk_bf16_f32 v14, v28, v29\n" \
"v_mfma_f32_16x16x32_bf16 v[68:71], a[108:111], a[68:71], v[68:71]\n" \
"s_add_u32 m0, s86, s79\n" \
"ds_read_b64_tr_b16 a[114:115], v214 offset:18944\n" \
"v_cvt_pk_bf16_f32 v15, v30, v31\n" \
"v_mfma_f32_16x16x32_bf16 v[84:87], a[104:107], a[72:75], v[84:87]\n" \
"buffer_load_dwordx4 v234, s[56:59], s81 offen offset:0 sc1 lds\n" \
"v_mfma_f32_16x16x32_bf16 v[84:87], a[108:111], a[76:79], v[84:87]\n" \
"s_waitcnt lgkmcnt(3)\n" \
"ds_read_b64_tr_b16 a[116:117], v214 offset:25088\n" \
"v_cvt_pk_bf16_f32 v16, v32, v33\n" \
"v_mfma_f32_16x16x32_bf16 v[80:83], a[96:99], a[72:75], v[80:83]\n" \
"s_lshl_b32 s82, s82, 5\n" \
"ds_read_b64_tr_b16 a[118:119], v214 offset:27136\n" \
"v_cvt_pk_bf16_f32 v17, v34, v35\n" \
"v_mfma_f32_16x16x32_bf16 v[80:83], a[100:103], a[76:79], v[80:83]\n" \
"s_add_u32 s82, s82, s47\n" \
"ds_read_b64_tr_b16 a[120:121], v215 offset:16896\n" \
"v_cvt_pk_bf16_f32 v18, v36, v37\n" \
"v_mfma_f32_16x16x32_bf16 v[96:99], a[96:99], a[80:83], v[96:99]\n" \
"buffer_load_dwordx4 v234, s[56:59], s81 offen offset:1024 sc1 lds\n" \
"v_mfma_f32_16x16x32_bf16 v[96:99], a[100:103], a[84:87], v[96:99]\n" \
"s_sub_u32 s85, s85, 32768\n" \
"s_cselect_b32 s85, 131072, s85\n" \
"ds_read_b64_tr_b16 a[122:123], v215 offset:18944\n" \
"v_mfma_f32_16x16x32_bf16 v[100:103], a[104:107], a[80:83], v[100:103]\n" \
"s_add_u32 s48, s48, 0x1000\n" \
"ds_read_b64_tr_b16 a[124:125], v215 offset:25088\n" \
"v_add_u32 v213, s85, v211\n" \
"v_mfma_f32_16x16x32_bf16 v[100:103], a[108:111], a[84:87], v[100:103]\n" \
"s_waitcnt lgkmcnt(3)\n" \
"s_sub_u32 s86, s86, 32768\n" \
"s_cselect_b32 s86, 131072, s86\n" \
"ds_read_b64_tr_b16 a[126:127], v215 offset:27136\n" \
"v_mfma_f32_16x16x32_bf16 v[116:119], a[104:107], a[88:91], v[116:119]\n" \
"buffer_load_dwordx4 v234, s[56:59], s81 offen offset:2048 sc1 lds\n" \
"v_mfma_f32_16x16x32_bf16 v[116:119], a[108:111], a[92:95], v[116:119]\n" \
"v_add_u32 v214, s85, v212\n" \
"v_xor_b32 v215, 32, v214\n" \
"v_mfma_f32_16x16x32_bf16 v[112:115], a[96:99], a[88:91], v[112:115]\n" \
"buffer_load_dwordx4 v234, s[56:59], s81 offen offset:3072 sc1 lds\n" \
"v_mfma_f32_16x16x32_bf16 v[112:115], a[100:103], a[92:95], v[112:115]\n" \
"s_waitcnt vmcnt(23) lgkmcnt(0)\n" \
"s_barrier\n" \
"v_mfma_f32_16x16x32_bf16 v[72:75], a[112:115], a[64:67], v[72:75]\n" \
"ds_read_b64_tr_b16 a[96:97], v214 offset:16384\n" \
"ds_read_b64_tr_b16 a[98:99], v214 offset:18432\n" \
"v_mfma_f32_16x16x32_bf16 v[72:75], a[116:119], a[68:71], v[72:75]\n" \
"s_add_u32 m0, m0, 0x4000\n" \
"ds_read_b64_tr_b16 a[100:101], v214 offset:24576\n" \
"ds_read_b64_tr_b16 a[102:103], v214 offset:26624\n" \
"v_mfma_f32_16x16x32_bf16 v[76:79], a[120:123], a[64:67], v[76:79]\n" \
"buffer_load_dwordx4 v216, s[36:39], s82 offen offset:0 lds\n" \
"v_mfma_f32_16x16x32_bf16 v[76:79], a[124:127], a[68:71], v[76:79]\n" \
"ds_read_b128 a[64:67], v213 offset:0\n" \
"v_cvt_pk_bf16_f32 v19, v38, v39\n" \
"v_mfma_f32_16x16x32_bf16 v[92:95], a[120:123], a[72:75], v[92:95]\n" \
"ds_read_b128 a[68:71], v213 offset:2048\n" \
"v_cvt_pk_bf16_f32 v20, v40, v41\n" \
"v_mfma_f32_16x16x32_bf16 v[92:95], a[124:127], a[76:79], v[92:95]\n" \
"ds_read_b64_tr_b16 a[104:105], v215 offset:16384\n" \
"ds_read_b64_tr_b16 a[106:107], v215 offset:18432\n" \
"v_mfma_f32_16x16x32_bf16 v[88:91], a[112:115], a[72:75], v[88:91]\n" \
"buffer_load_dwordx4 v217, s[36:39], s82 offen offset:1024 lds\n" \
"v_mfma_f32_16x16x32_bf16 v[88:91], a[116:119], a[76:79], v[88:91]\n" \
"ds_read_b128 a[72:75], v213 offset:256\n" \
"v_cvt_pk_bf16_f32 v21, v42, v43\n" \
"v_mfma_f32_16x16x32_bf16 v[104:107], a[112:115], a[80:83], v[104:107]\n" \
"ds_read_b128 a[76:79], v213 offset:2304\n" \
"v_cvt_pk_bf16_f32 v22, v44, v45\n" \
"v_mfma_f32_16x16x32_bf16 v[104:107], a[116:119], a[84:87], v[104:107]\n" \
"ds_read_b64_tr_b16 a[108:109], v215 offset:24576\n" \
"ds_read_b64_tr_b16 a[110:111], v215 offset:26624\n" \
"v_mfma_f32_16x16x32_bf16 v[108:111], a[120:123], a[80:83], v[108:111]\n" \
"buffer_load_dwordx4 v218, s[36:39], s82 offen offset:2048 lds\n" \
"v_mfma_f32_16x16x32_bf16 v[108:111], a[124:127], a[84:87], v[108:111]\n" \
"ds_read_b128 a[80:83], v213 offset:4096\n" \
"v_cvt_pk_bf16_f32 v23, v46, v47\n" \
"v_mfma_f32_16x16x32_bf16 v[124:127], a[120:123], a[88:91], v[124:127]\n" \
"s_waitcnt lgkmcnt(4)\n" \
"buffer_store_dwordx4 v[12:15], v234, s[56:59], s71 offen offset:3072 sc1\n" \
"v_mfma_f32_16x16x32_bf16 v[124:127], a[124:127], a[92:95], v[124:127]\n" \
"s_add_u32 s71, s87, 4096\n" \
"ds_read_b128 a[84:87], v213 offset:6144\n" \
"v_cvt_pk_bf16_f32 v24, v48, v49\n" \
"v_mfma_f32_16x16x32_bf16 v[120:123], a[112:115], a[88:91], v[120:123]\n" \
"buffer_load_dwordx4 v219, s[36:39], s82 offen offset:3072 lds\n" \
"v_mfma_f32_16x16x32_bf16 v[120:123], a[116:119], a[92:95], v[120:123]\n" \
"buffer_store_dwordx4 v[16:19], v234, s[56:59], s71 offen offset:0 sc1\n" \
"buffer_store_dwordx4 v[20:23], v234, s[56:59], s71 offen offset:1024 sc1\n" \
"v_mfma_f32_16x16x32_bf16 v[64:67], a[96:99], a[64:67], v[64:67]\n" \
"s_xor_b32 s82, s48, s49\n" \
"ds_read_b128 a[88:91], v213 offset:4352\n" \
"v_cvt_pk_bf16_f32 v25, v50, v51\n" \
"v_mfma_f32_16x16x32_bf16 v[64:67], a[100:103], a[68:71], v[64:67]\n" \
"s_waitcnt lgkmcnt(2)\n" \
"s_cmp_lt_u32 s48, s50\n" \
"s_cselect_b32 s82, s82, s48\n" \
"ds_read_b128 a[92:95], v213 offset:6400\n" \
"v_mfma_f32_16x16x32_bf16 v[68:71], a[104:107], a[64:67], v[68:71]\n" \
"s_add_u32 s81, s82, s46\n" \
"ds_read_b64_tr_b16 a[112:113], v214 offset:16896\n" \
"v_cvt_pk_bf16_f32 v26, v52, v53\n" \
"v_mfma_f32_16x16x32_bf16 v[68:71], a[108:111], a[68:71], v[68:71]\n" \
"s_add_u32 m0, s86, s79\n" \
"ds_read_b64_tr_b16 a[114:115], v214 offset:18944\n" \
"v_cvt_pk_bf16_f32 v27, v54, v55\n" \
"v_mfma_f32_16x16x32_bf16 v[84:87], a[104:107], a[72:75], v[84:87]\n" \
"buffer_load_dwordx4 v234, s[56:59], s81 offen offset:0 sc1 lds\n" \
"v_mfma_f32_16x16x32_bf16 v[84:87], a[108:111], a[76:79], v[84:87]\n" \
"s_waitcnt lgkmcnt(3)\n" \
"ds_read_b64_tr_b16 a[116:117], v214 offset:25088\n" \
"v_cvt_pk_bf16_f32 v28, v56, v57\n" \
"v_mfma_f32_16x16x32_bf16 v[80:83], a[96:99], a[72:75], v[80:83]\n" \
"s_lshl_b32 s82, s82, 5\n" \
"ds_read_b64_tr_b16 a[118:119], v214 offset:27136\n" \
"v_cvt_pk_bf16_f32 v29, v58, v59\n" \
"v_mfma_f32_16x16x32_bf16 v[80:83], a[100:103], a[76:79], v[80:83]\n" \
"s_add_u32 s82, s82, s47\n" \
"ds_read_b64_tr_b16 a[120:121], v215 offset:16896\n" \
"v_cvt_pk_bf16_f32 v30, v60, v61\n" \
"v_mfma_f32_16x16x32_bf16 v[96:99], a[96:99], a[80:83], v[96:99]\n" \
"buffer_load_dwordx4 v234, s[56:59], s81 offen offset:1024 sc1 lds\n" \
"v_mfma_f32_16x16x32_bf16 v[96:99], a[100:103], a[84:87], v[96:99]\n" \
"s_sub_u32 s85, s85, 32768\n" \
"s_cselect_b32 s85, 131072, s85\n" \
"ds_read_b64_tr_b16 a[122:123], v215 offset:18944\n" \
"v_mfma_f32_16x16x32_bf16 v[100:103], a[104:107], a[80:83], v[100:103]\n" \
"s_add_u32 s48, s48, 0x1000\n" \
"ds_read_b64_tr_b16 a[124:125], v215 offset:25088\n" \
"v_add_u32 v213, s85, v211\n" \
"v_mfma_f32_16x16x32_bf16 v[100:103], a[108:111], a[84:87], v[100:103]\n" \
"s_waitcnt lgkmcnt(3)\n" \
"s_sub_u32 s86, s86, 32768\n" \
"s_cselect_b32 s86, 131072, s86\n" \
"ds_read_b64_tr_b16 a[126:127], v215 offset:27136\n" \
"v_mfma_f32_16x16x32_bf16 v[116:119], a[104:107], a[88:91], v[116:119]\n" \
"buffer_load_dwordx4 v234, s[56:59], s81 offen offset:2048 sc1 lds\n" \
"v_mfma_f32_16x16x32_bf16 v[116:119], a[108:111], a[92:95], v[116:119]\n" \
"v_add_u32 v214, s85, v212\n" \
"v_xor_b32 v215, 32, v214\n" \
"v_mfma_f32_16x16x32_bf16 v[112:115], a[96:99], a[88:91], v[112:115]\n" \
"buffer_load_dwordx4 v234, s[56:59], s81 offen offset:3072 sc1 lds\n" \
"v_mfma_f32_16x16x32_bf16 v[112:115], a[100:103], a[92:95], v[112:115]\n" \
"s_waitcnt vmcnt(26) lgkmcnt(0)\n" \
"s_barrier\n" \
"v_mfma_f32_16x16x32_bf16 v[72:75], a[112:115], a[64:67], v[72:75]\n" \
"ds_read_b64_tr_b16 a[96:97], v214 offset:16384\n" \
"ds_read_b64_tr_b16 a[98:99], v214 offset:18432\n" \
"v_mfma_f32_16x16x32_bf16 v[72:75], a[116:119], a[68:71], v[72:75]\n" \
"s_add_u32 m0, m0, 0x4000\n" \
"ds_read_b64_tr_b16 a[100:101], v214 offset:24576\n" \
"ds_read_b64_tr_b16 a[102:103], v214 offset:26624\n" \
"v_mfma_f32_16x16x32_bf16 v[76:79], a[120:123], a[64:67], v[76:79]\n" \
"buffer_load_dwordx4 v216, s[36:39], s82 offen offset:0 lds\n" \
"v_mfma_f32_16x16x32_bf16 v[76:79], a[124:127], a[68:71], v[76:79]\n" \
"ds_read_b128 a[64:67], v213 offset:0\n" \
"v_cvt_pk_bf16_f32 v31, v62, v63\n" \
"v_mfma_f32_16x16x32_bf16 v[92:95], a[120:123], a[72:75], v[92:95]\n" \
"ds_read_b128 a[68:71], v213 offset:2048\n" \
"v_mfma_f32_16x16x32_bf16 v[92:95], a[124:127], a[76:79], v[92:95]\n" \
"ds_read_b64_tr_b16 a[104:105], v215 offset:16384\n" \
"ds_read_b64_tr_b16 a[106:107], v215 offset:18432\n" \
"v_mfma_f32_16x16x32_bf16 v[88:91], a[112:115], a[72:75], v[88:91]\n" \
"buffer_load_dwordx4 v217, s[36:39], s82 offen offset:1024 lds\n" \
"v_mfma_f32_16x16x32_bf16 v[88:91], a[116:119], a[76:79], v[88:91]\n" \
"ds_read_b128 a[72:75], v213 offset:256\n" \
"v_mfma_f32_16x16x32_bf16 v[104:107], a[112:115], a[80:83], v[104:107]\n" \
"ds_read_b128 a[76:79], v213 offset:2304\n" \
"v_mfma_f32_16x16x32_bf16 v[104:107], a[116:119], a[84:87], v[104:107]\n" \
"ds_read_b64_tr_b16 a[108:109], v215 offset:24576\n" \
"ds_read_b64_tr_b16 a[110:111], v215 offset:26624\n" \
"v_mfma_f32_16x16x32_bf16 v[108:111], a[120:123], a[80:83], v[108:111]\n" \
"buffer_load_dwordx4 v218, s[36:39], s82 offen offset:2048 lds\n" \
"v_mfma_f32_16x16x32_bf16 v[108:111], a[124:127], a[84:87], v[108:111]\n" \
"ds_read_b128 a[80:83], v213 offset:4096\n" \
"v_mfma_f32_16x16x32_bf16 v[124:127], a[120:123], a[88:91], v[124:127]\n" \
"s_waitcnt lgkmcnt(4)\n" \
"buffer_store_dwordx4 v[24:27], v234, s[56:59], s71 offen offset:2048 sc1\n" \
"v_mfma_f32_16x16x32_bf16 v[124:127], a[124:127], a[92:95], v[124:127]\n" \
"ds_read_b128 a[84:87], v213 offset:6144\n" \
"v_mfma_f32_16x16x32_bf16 v[120:123], a[112:115], a[88:91], v[120:123]\n" \
"buffer_load_dwordx4 v219, s[36:39], s82 offen offset:3072 lds\n" \
"v_mfma_f32_16x16x32_bf16 v[120:123], a[116:119], a[92:95], v[120:123]\n" \
"buffer_store_dwordx4 v[28:31], v234, s[56:59], s71 offen offset:3072 sc1\n" \
"v_mfma_f32_16x16x32_bf16 v[64:67], a[96:99], a[64:67], v[64:67]\n" \
"s_waitcnt lgkmcnt(1)\n" \
"s_xor_b32 s82, s48, s49\n" \
"ds_read_b128 a[88:91], v213 offset:4352\n" \
"v_mfma_f32_16x16x32_bf16 v[64:67], a[100:103], a[68:71], v[64:67]\n" \
"s_cmp_lt_u32 s48, s50\n" \
"s_cselect_b32 s82, s82, s48\n" \
"ds_read_b128 a[92:95], v213 offset:6400\n" \
"v_mfma_f32_16x16x32_bf16 v[68:71], a[104:107], a[64:67], v[68:71]\n" \
"s_add_u32 s81, s82, s46\n" \
"ds_read_b64_tr_b16 a[112:113], v214 offset:16896\n" \
"v_mfma_f32_16x16x32_bf16 v[68:71], a[108:111], a[68:71], v[68:71]\n" \
"s_add_u32 m0, s86, s79\n" \
"ds_read_b64_tr_b16 a[114:115], v214 offset:18944\n" \
"v_mfma_f32_16x16x32_bf16 v[84:87], a[104:107], a[72:75], v[84:87]\n" \
"buffer_load_dwordx4 v234, s[56:59], s81 offen offset:0 sc1 lds\n" \
"v_mfma_f32_16x16x32_bf16 v[84:87], a[108:111], a[76:79], v[84:87]\n" \
"ds_read_b64_tr_b16 a[116:117], v214 offset:25088\n" \
"v_mfma_f32_16x16x32_bf16 v[80:83], a[96:99], a[72:75], v[80:83]\n" \
"s_lshl_b32 s82, s82, 5\n" \
"ds_read_b64_tr_b16 a[118:119], v214 offset:27136\n" \
"v_mfma_f32_16x16x32_bf16 v[80:83], a[100:103], a[76:79], v[80:83]\n" \
"s_waitcnt lgkmcnt(3)\n" \
"s_add_u32 s82, s82, s47\n" \
"ds_read_b64_tr_b16 a[120:121], v215 offset:16896\n" \
"v_mfma_f32_16x16x32_bf16 v[96:99], a[96:99], a[80:83], v[96:99]\n" \
"buffer_load_dwordx4 v234, s[56:59], s81 offen offset:1024 sc1 lds\n" \
"v_mfma_f32_16x16x32_bf16 v[96:99], a[100:103], a[84:87], v[96:99]\n" \
"s_sub_u32 s85, s85, 32768\n" \
"s_cselect_b32 s85, 131072, s85\n" \
"ds_read_b64_tr_b16 a[122:123], v215 offset:18944\n" \
"v_mfma_f32_16x16x32_bf16 v[100:103], a[104:107], a[80:83], v[100:103]\n" \
"s_add_u32 s48, s48, 0x1000\n" \
"ds_read_b64_tr_b16 a[124:125], v215 offset:25088\n" \
"v_add_u32 v213, s85, v211\n" \
"v_mfma_f32_16x16x32_bf16 v[100:103], a[108:111], a[84:87], v[100:103]\n" \
"s_sub_u32 s86, s86, 32768\n" \
"s_cselect_b32 s86, 131072, s86\n" \
"ds_read_b64_tr_b16 a[126:127], v215 offset:27136\n" \
"v_mfma_f32_16x16x32_bf16 v[116:119], a[104:107], a[88:91], v[116:119]\n" \
"buffer_load_dwordx4 v234, s[56:59], s81 offen offset:2048 sc1 lds\n" \
"v_mfma_f32_16x16x32_bf16 v[116:119], a[108:111], a[92:95], v[116:119]\n" \
"v_add_u32 v214, s85, v212\n" \
"v_xor_b32 v215, 32, v214\n" \
"v_mfma_f32_16x16x32_bf16 v[112:115], a[96:99], a[88:91], v[112:115]\n" \
"buffer_load_dwordx4 v234, s[56:59], s81 offen offset:3072 sc1 lds\n" \
"v_mfma_f32_16x16x32_bf16 v[112:115], a[100:103], a[92:95], v[112:115]\n" \
"s_waitcnt vmcnt(27) lgkmcnt(0)\n" \
"s_barrier\n" \
"v_mfma_f32_16x16x32_bf16 v[72:75], a[112:115], a[64:67], v[72:75]\n" \
"ds_read_b64_tr_b16 a[96:97], v214 offset:16384\n" \
"ds_read_b64_tr_b16 a[98:99], v214 offset:18432\n" \
"v_mfma_f32_16x16x32_bf16 v[72:75], a[116:119], a[68:71], v[72:75]\n" \
"s_add_u32 m0, m0, 0x4000\n" \
"ds_read_b64_tr_b16 a[100:101], v214 offset:24576\n" \
"ds_read_b64_tr_b16 a[102:103], v214 offset:26624\n" \
"v_mfma_f32_16x16x32_bf16 v[76:79], a[120:123], a[64:67], v[76:79]\n" \
"buffer_load_dwordx4 v216, s[36:39], s82 offen offset:0 lds\n" \
"v_mfma_f32_16x16x32_bf16 v[76:79], a[124:127], a[68:71], v[76:79]\n" \
"ds_read_b128 a[64:67], v213 offset:0\n" \
"v_mfma_f32_16x16x32_bf16 v[92:95], a[120:123], a[72:75], v[92:95]\n" \
"ds_read_b128 a[68:71], v213 offset:2048\n" \
"v_mfma_f32_16x16x32_bf16 v[92:95], a[124:127], a[76:79], v[92:95]\n" \
"ds_read_b64_tr_b16 a[104:105], v215 offset:16384\n" \
"ds_read_b64_tr_b16 a[106:107], v215 offset:18432\n" \
"v_mfma_f32_16x16x32_bf16 v[88:91], a[112:115], a[72:75], v[88:91]\n" \
"buffer_load_dwordx4 v217, s[36:39], s82 offen offset:1024 lds\n" \
"v_mfma_f32_16x16x32_bf16 v[88:91], a[116:119], a[76:79], v[88:91]\n" \
"ds_read_b128 a[72:75], v213 offset:256\n" \
"v_mfma_f32_16x16x32_bf16 v[104:107], a[112:115], a[80:83], v[104:107]\n" \
"ds_read_b128 a[76:79], v213 offset:2304\n" \
"v_mfma_f32_16x16x32_bf16 v[104:107], a[116:119], a[84:87], v[104:107]\n" \
"ds_read_b64_tr_b16 a[108:109], v215 offset:24576\n" \
"ds_read_b64_tr_b16 a[110:111], v215 offset:26624\n" \
"v_mfma_f32_16x16x32_bf16 v[108:111], a[120:123], a[80:83], v[108:111]\n" \
"buffer_load_dwordx4 v218, s[36:39], s82 offen offset:2048 lds\n" \
"v_mfma_f32_16x16x32_bf16 v[108:111], a[124:127], a[84:87], v[108:111]\n" \
"ds_read_b128 a[80:83], v213 offset:4096\n" \
"v_mfma_f32_16x16x32_bf16 v[124:127], a[120:123], a[88:91], v[124:127]\n" \
"s_waitcnt lgkmcnt(4)\n" \
"v_mfma_f32_16x16x32_bf16 v[124:127], a[124:127], a[92:95], v[124:127]\n" \
"ds_read_b128 a[84:87], v213 offset:6144\n" \
"v_mfma_f32_16x16x32_bf16 v[120:123], a[112:115], a[88:91], v[120:123]\n" \
"buffer_load_dwordx4 v219, s[36:39], s82 offen offset:3072 lds\n" \
"v_mfma_f32_16x16x32_bf16 v[120:123], a[116:119], a[92:95], v[120:123]\n" \
"v_mfma_f32_16x16x32_bf16 v[64:67], a[96:99], a[64:67], v[64:67]\n" \
"s_waitcnt lgkmcnt(1)\n" \
"s_xor_b32 s82, s48, s49\n" \
"ds_read_b128 a[88:91], v213 offset:4352\n" \
"v_mfma_f32_16x16x32_bf16 v[64:67], a[100:103], a[68:71], v[64:67]\n" \
"s_cmp_lt_u32 s48, s50\n" \
"s_cselect_b32 s82, s82, s48\n" \
"ds_read_b128 a[92:95], v213 offset:6400\n" \
"v_mfma_f32_16x16x32_bf16 v[68:71], a[104:107], a[64:67], v[68:71]\n" \
"s_add_u32 s81, s82, s46\n" \
"ds_read_b64_tr_b16 a[112:113], v214 offset:16896\n" \
"v_mfma_f32_16x16x32_bf16 v[68:71], a[108:111], a[68:71], v[68:71]\n" \
"s_add_u32 m0, s86, s79\n" \
"ds_read_b64_tr_b16 a[114:115], v214 offset:18944\n" \
"v_mfma_f32_16x16x32_bf16 v[84:87], a[104:107], a[72:75], v[84:87]\n" \
"buffer_load_dwordx4 v234, s[56:59], s81 offen offset:0 sc1 lds\n" \
"v_mfma_f32_16x16x32_bf16 v[84:87], a[108:111], a[76:79], v[84:87]\n" \
"ds_read_b64_tr_b16 a[116:117], v214 offset:25088\n" \
"v_mfma_f32_16x16x32_bf16 v[80:83], a[96:99], a[72:75], v[80:83]\n" \
"s_lshl_b32 s82, s82, 5\n" \
"ds_read_b64_tr_b16 a[118:119], v214 offset:27136\n" \
"v_mfma_f32_16x16x32_bf16 v[80:83], a[100:103], a[76:79], v[80:83]\n" \
"s_waitcnt lgkmcnt(3)\n" \
"s_add_u32 s82, s82, s47\n" \
"ds_read_b64_tr_b16 a[120:121], v215 offset:16896\n" \
"v_mfma_f32_16x16x32_bf16 v[96:99], a[96:99], a[80:83], v[96:99]\n" \
"buffer_load_dwordx4 v234, s[56:59], s81 offen offset:1024 sc1 lds\n" \
"v_mfma_f32_16x16x32_bf16 v[96:99], a[100:103], a[84:87], v[96:99]\n" \
"s_sub_u32 s85, s85, 32768\n" \
"s_cselect_b32 s85, 131072, s85\n" \
"ds_read_b64_tr_b16 a[122:123], v215 offset:18944\n" \
"v_mfma_f32_16x16x32_bf16 v[100:103], a[104:107], a[80:83], v[100:103]\n" \
"s_add_u32 s48, s48, 0x1000\n" \
"ds_read_b64_tr_b16 a[124:125], v215 offset:25088\n" \
"v_add_u32 v213, s85, v211\n" \
"v_mfma_f32_16x16x32_bf16 v[100:103], a[108:111], a[84:87], v[100:103]\n" \
"s_sub_u32 s86, s86, 32768\n" \
"s_cselect_b32 s86, 131072, s86\n" \
"ds_read_b64_tr_b16 a[126:127], v215 offset:27136\n" \
"v_mfma_f32_16x16x32_bf16 v[116:119], a[104:107], a[88:91], v[116:119]\n" \
"buffer_load_dwordx4 v234, s[56:59], s81 offen offset:2048 sc1 lds\n" \
"v_mfma_f32_16x16x32_bf16 v[116:119], a[108:111], a[92:95], v[116:119]\n" \
"v_add_u32 v214, s85, v212\n" \
"v_xor_b32 v215, 32, v214\n" \
"v_mfma_f32_16x16x32_bf16 v[112:115], a[96:99], a[88:91], v[112:115]\n" \
"buffer_load_dwordx4 v234, s[56:59], s81 offen offset:3072 sc1 lds\n" \
"v_mfma_f32_16x16x32_bf16 v[112:115], a[100:103], a[92:95], v[112:115]\n" \
"s_waitcnt vmcnt(24) lgkmcnt(0)\n" \
"s_barrier\n" \
"v_mfma_f32_16x16x32_bf16 v[72:75], a[112:115], a[64:67], v[72:75]\n" \
"ds_read_b64_tr_b16 a[96:97], v214 offset:16384\n" \
"ds_read_b64_tr_b16 a[98:99], v214 offset:18432\n" \
"v_mfma_f32_16x16x32_bf16 v[72:75], a[116:119], a[68:71], v[72:75]\n" \
"s_add_u32 m0, m0, 0x4000\n" \
"ds_read_b64_tr_b16 a[100:101], v214 offset:24576\n" \
"ds_read_b64_tr_b16 a[102:103], v214 offset:26624\n" \
"v_mfma_f32_16x16x32_bf16 v[76:79], a[120:123], a[64:67], v[76:79]\n" \
"buffer_load_dwordx4 v216, s[36:39], s82 offen offset:0 lds\n" \
"v_mfma_f32_16x16x32_bf16 v[76:79], a[124:127], a[68:71], v[76:79]\n" \
"ds_read_b128 a[64:67], v213 offset:0\n" \
"v_mfma_f32_16x16x32_bf16 v[92:95], a[120:123], a[72:75], v[92:95]\n" \
"ds_read_b128 a[68:71], v213 offset:2048\n" \
"v_mfma_f32_16x16x32_bf16 v[92:95], a[124:127], a[76:79], v[92:95]\n" \
"ds_read_b64_tr_b16 a[104:105], v215 offset:16384\n" \
"ds_read_b64_tr_b16 a[106:107], v215 offset:18432\n" \
"v_mfma_f32_16x16x32_bf16 v[88:91], a[112:115], a[72:75], v[88:91]\n" \
"buffer_load_dwordx4 v217, s[36:39], s82 offen offset:1024 lds\n" \
"v_mfma_f32_16x16x32_bf16 v[88:91], a[116:119], a[76:79], v[88:91]\n" \
"ds_read_b128 a[72:75], v213 offset:256\n" \
"v_mfma_f32_16x16x32_bf16 v[104:107], a[112:115], a[80:83], v[104:107]\n" \
"ds_read_b128 a[76:79], v213 offset:2304\n" \
"v_mfma_f32_16x16x32_bf16 v[104:107], a[116:119], a[84:87], v[104:107]\n" \
"ds_read_b64_tr_b16 a[108:109], v215 offset:24576\n" \
"ds_read_b64_tr_b16 a[110:111], v215 offset:26624\n" \
"v_mfma_f32_16x16x32_bf16 v[108:111], a[120:123], a[80:83], v[108:111]\n" \
"buffer_load_dwordx4 v218, s[36:39], s82 offen offset:2048 lds\n" \
"v_mfma_f32_16x16x32_bf16 v[108:111], a[124:127], a[84:87], v[108:111]\n" \
"ds_read_b128 a[80:83], v213 offset:4096\n" \
"v_mfma_f32_16x16x32_bf16 v[124:127], a[120:123], a[88:91], v[124:127]\n" \
"s_waitcnt lgkmcnt(4)\n" \
"v_mfma_f32_16x16x32_bf16 v[124:127], a[124:127], a[92:95], v[124:127]\n" \
"ds_read_b128 a[84:87], v213 offset:6144\n" \
"v_mfma_f32_16x16x32_bf16 v[120:123], a[112:115], a[88:91], v[120:123]\n" \
"buffer_load_dwordx4 v219, s[36:39], s82 offen offset:3072 lds\n" \
"v_mfma_f32_16x16x32_bf16 v[120:123], a[116:119], a[92:95], v[120:123]\n" \
"v_mfma_f32_16x16x32_bf16 v[64:67], a[96:99], a[64:67], v[64:67]\n" \
"s_waitcnt lgkmcnt(1)\n" \
"s_xor_b32 s82, s48, s49\n" \
"ds_read_b128 a[88:91], v213 offset:4352\n" \
"v_mfma_f32_16x16x32_bf16 v[64:67], a[100:103], a[68:71], v[64:67]\n" \
"s_cmp_lt_u32 s48, s50\n" \
"s_cselect_b32 s82, s82, s48\n" \
"ds_read_b128 a[92:95], v213 offset:6400\n" \
"v_mfma_f32_16x16x32_bf16 v[68:71], a[104:107], a[64:67], v[68:71]\n" \
"s_add_u32 s81, s82, s46\n" \
"ds_read_b64_tr_b16 a[112:113], v214 offset:16896\n" \
"v_mfma_f32_16x16x32_bf16 v[68:71], a[108:111], a[68:71], v[68:71]\n" \
"s_add_u32 m0, s86, s79\n" \
"ds_read_b64_tr_b16 a[114:115], v214 offset:18944\n" \
"v_mfma_f32_16x16x32_bf16 v[84:87], a[104:107], a[72:75], v[84:87]\n" \
"buffer_load_dwordx4 v234, s[56:59], s81 offen offset:0 sc1 lds\n" \
"v_mfma_f32_16x16x32_bf16 v[84:87], a[108:111], a[76:79], v[84:87]\n" \
"ds_read_b64_tr_b16 a[116:117], v214 offset:25088\n" \
"v_mfma_f32_16x16x32_bf16 v[80:83], a[96:99], a[72:75], v[80:83]\n" \
"s_lshl_b32 s82, s82, 5\n" \
"ds_read_b64_tr_b16 a[118:119], v214 offset:27136\n" \
"v_mfma_f32_16x16x32_bf16 v[80:83], a[100:103], a[76:79], v[80:83]\n" \
"s_waitcnt lgkmcnt(3)\n" \
"s_add_u32 s82, s82, s47\n" \
"ds_read_b64_tr_b16 a[120:121], v215 offset:16896\n" \
"v_mfma_f32_16x16x32_bf16 v[96:99], a[96:99], a[80:83], v[96:99]\n" \
"buffer_load_dwordx4 v234, s[56:59], s81 offen offset:1024 sc1 lds\n" \
"v_mfma_f32_16x16x32_bf16 v[96:99], a[100:103], a[84:87], v[96:99]\n" \
"s_sub_u32 s85, s85, 32768\n" \
"s_cselect_b32 s85, 131072, s85\n" \
"ds_read_b64_tr_b16 a[122:123], v215 offset:18944\n" \
"v_mfma_f32_16x16x32_bf16 v[100:103], a[104:107], a[80:83], v[100:103]\n" \
"s_add_u32 s48, s48, 0x1000\n" \
"ds_read_b64_tr_b16 a[124:125], v215 offset:25088\n" \
"v_add_u32 v213, s85, v211\n" \
"v_mfma_f32_16x16x32_bf16 v[100:103], a[108:111], a[84:87], v[100:103]\n" \
"s_sub_u32 s86, s86, 32768\n" \
"s_cselect_b32 s86, 131072, s86\n" \
"ds_read_b64_tr_b16 a[126:127], v215 offset:27136\n" \
"v_mfma_f32_16x16x32_bf16 v[116:119], a[104:107], a[88:91], v[116:119]\n" \
"buffer_load_dwordx4 v234, s[56:59], s81 offen offset:2048 sc1 lds\n" \
"v_mfma_f32_16x16x32_bf16 v[116:119], a[108:111], a[92:95], v[116:119]\n" \
"v_add_u32 v214, s85, v212\n" \
"v_xor_b32 v215, 32, v214\n" \
"v_mfma_f32_16x16x32_bf16 v[112:115], a[96:99], a[88:91], v[112:115]\n" \
"buffer_load_dwordx4 v234, s[56:59], s81 offen offset:3072 sc1 lds\n" \
"v_mfma_f32_16x16x32_bf16 v[112:115], a[100:103], a[92:95], v[112:115]\n" \
"s_waitcnt vmcnt(20) lgkmcnt(0)\n" \
"s_barrier\n" \
"buffer_store_dword v230, v231, s[56:59], s64 offen sc1\n" \
"v_mfma_f32_16x16x32_bf16 v[72:75], a[112:115], a[64:67], v[72:75]\n" \
"ds_read_b64_tr_b16 a[96:97], v214 offset:16384\n" \
"ds_read_b64_tr_b16 a[98:99], v214 offset:18432\n" \
"v_mfma_f32_16x16x32_bf16 v[72:75], a[116:119], a[68:71], v[72:75]\n" \
"s_add_u32 m0, m0, 0x4000\n" \
"ds_read_b64_tr_b16 a[100:101], v214 offset:24576\n" \
"ds_read_b64_tr_b16 a[102:103], v214 offset:26624\n" \
"v_mfma_f32_16x16x32_bf16 v[76:79], a[120:123], a[64:67], v[76:79]\n" \
"buffer_load_dwordx4 v216, s[36:39], s82 offen offset:0 lds\n" \
"v_mfma_f32_16x16x32_bf16 v[76:79], a[124:127], a[68:71], v[76:79]\n" \
"ds_read_b128 a[64:67], v213 offset:0\n" \
"v_mfma_f32_16x16x32_bf16 v[92:95], a[120:123], a[72:75], v[92:95]\n" \
"ds_read_b128 a[68:71], v213 offset:2048\n" \
"v_mfma_f32_16x16x32_bf16 v[92:95], a[124:127], a[76:79], v[92:95]\n" \
"ds_read_b64_tr_b16 a[104:105], v215 offset:16384\n" \
"ds_read_b64_tr_b16 a[106:107], v215 offset:18432\n" \
"v_mfma_f32_16x16x32_bf16 v[88:91], a[112:115], a[72:75], v[88:91]\n" \
"buffer_load_dwordx4 v217, s[36:39], s82 offen offset:1024 lds\n" \
"v_mfma_f32_16x16x32_bf16 v[88:91], a[116:119], a[76:79], v[88:91]\n" \
"ds_read_b128 a[72:75], v213 offset:256\n" \
"v_mfma_f32_16x16x32_bf16 v[104:107], a[112:115], a[80:83], v[104:107]\n" \
"ds_read_b128 a[76:79], v213 offset:2304\n" \
"v_mfma_f32_16x16x32_bf16 v[104:107], a[116:119], a[84:87], v[104:107]\n" \
"ds_read_b64_tr_b16 a[108:109], v215 offset:24576\n" \
"ds_read_b64_tr_b16 a[110:111], v215 offset:26624\n" \
"v_mfma_f32_16x16x32_bf16 v[108:111], a[120:123], a[80:83], v[108:111]\n" \
"buffer_load_dwordx4 v218, s[36:39], s82 offen offset:2048 lds\n" \
"v_mfma_f32_16x16x32_bf16 v[108:111], a[124:127], a[84:87], v[108:111]\n" \
"ds_read_b128 a[80:83], v213 offset:4096\n" \
"v_mfma_f32_16x16x32_bf16 v[124:127], a[120:123], a[88:91], v[124:127]\n" \
"s_waitcnt lgkmcnt(4)\n" \
"v_mfma_f32_16x16x32_bf16 v[124:127], a[124:127], a[92:95], v[124:127]\n" \
"ds_read_b128 a[84:87], v213 offset:6144\n" \
"v_mfma_f32_16x16x32_bf16 v[120:123], a[112:115], a[88:91], v[120:123]\n" \
"buffer_load_dwordx4 v219, s[36:39], s82 offen offset:3072 lds\n" \
"v_mfma_f32_16x16x32_bf16 v[120:123], a[116:119], a[92:95], v[120:123]\n" \
"s_mov_b32 s72, 5\n" \
"Lo64_%=:\n" \
"v_mfma_f32_16x16x32_bf16 v[64:67], a[96:99], a[64:67], v[64:67]\n" \
"s_waitcnt lgkmcnt(1)\n" \
"s_xor_b32 s82, s48, s49\n" \
"ds_read_b128 a[88:91], v213 offset:4352\n" \
"Lpg11508_%=:\n" \
"s_cbranch_execz Lcb1_%=\n" \
"v_mfma_f32_16x16x32_bf16 v[64:67], a[100:103], a[68:71], v[64:67]\n" \
"s_cmp_lt_u32 s48, s50\n" \
"s_cselect_b32 s82, s82, s48\n" \
"ds_read_b128 a[92:95], v213 offset:6400\n" \
"v_mfma_f32_16x16x32_bf16 v[68:71], a[104:107], a[64:67], v[68:71]\n" \
"s_add_u32 s81, s82, s46\n" \
"ds_read_b64_tr_b16 a[112:113], v214 offset:16896\n" \
"v_mfma_f32_16x16x32_bf16 v[68:71], a[108:111], a[68:71], v[68:71]\n" \
"s_add_u32 m0, s86, s79\n" \
"ds_read_b64_tr_b16 a[114:115], v214 offset:18944\n" \
"v_mfma_f32_16x16x32_bf16 v[84:87], a[104:107], a[72:75], v[84:87]\n" \
"buffer_load_dwordx4 v234, s[56:59], s81 offen offset:0 sc1 lds\n" \
"v_mfma_f32_16x16x32_bf16 v[84:87], a[108:111], a[76:79], v[84:87]\n" \
"ds_read_b64_tr_b16 a[116:117], v214 offset:25088\n" \
"v_mfma_f32_16x16x32_bf16 v[80:83], a[96:99], a[72:75], v[80:83]\n" \
"s_lshl_b32 s82, s82, 5\n" \
"ds_read_b64_tr_b16 a[118:119], v214 offset:27136\n" \
"v_mfma_f32_16x16x32_bf16 v[80:83], a[100:103], a[76:79], v[80:83]\n" \
"s_waitcnt lgkmcnt(3)\n" \
"s_add_u32 s82, s82, s47\n" \
"ds_read_b64_tr_b16 a[120:121], v215 offset:16896\n" \
"v_mfma_f32_16x16x32_bf16 v[96:99], a[96:99], a[80:83], v[96:99]\n" \
"buffer_load_dwordx4 v234, s[56:59], s81 offen offset:1024 sc1 lds\n" \
"v_mfma_f32_16x16x32_bf16 v[96:99], a[100:103], a[84:87], v[96:99]\n" \
"s_sub_u32 s85, s85, 32768\n" \
"s_cselect_b32 s85, 131072, s85\n" \
"ds_read_b64_tr_b16 a[122:123], v215 offset:18944\n" \
"v_mfma_f32_16x16x32_bf16 v[100:103], a[104:107], a[80:83], v[100:103]\n" \
"s_add_u32 s48, s48, 0x1000\n" \
"ds_read_b64_tr_b16 a[124:125], v215 offset:25088\n" \
"v_add_u32 v213, s85, v211\n" \
"v_mfma_f32_16x16x32_bf16 v[100:103], a[108:111], a[84:87], v[100:103]\n" \
"s_sub_u32 s86, s86, 32768\n" \
"s_cselect_b32 s86, 131072, s86\n" \
"ds_read_b64_tr_b16 a[126:127], v215 offset:27136\n" \
"v_mfma_f32_16x16x32_bf16 v[116:119], a[104:107], a[88:91], v[116:119]\n" \
"buffer_load_dwordx4 v234, s[56:59], s81 offen offset:2048 sc1 lds\n" \
"v_mfma_f32_16x16x32_bf16 v[116:119], a[108:111], a[92:95], v[116:119]\n" \
"v_add_u32 v214, s85, v212\n" \
"v_xor_b32 v215, 32, v214\n" \
"v_mfma_f32_16x16x32_bf16 v[112:115], a[96:99], a[88:91], v[112:115]\n" \
"buffer_load_dwordx4 v234, s[56:59], s81 offen offset:3072 sc1 lds\n" \
"v_mfma_f32_16x16x32_bf16 v[112:115], a[100:103], a[92:95], v[112:115]\n" \
"s_waitcnt vmcnt(20) lgkmcnt(0)\n" \
"s_barrier\n" \
"v_mfma_f32_16x16x32_bf16 v[72:75], a[112:115], a[64:67], v[72:75]\n" \
"ds_read_b64_tr_b16 a[96:97], v214 offset:16384\n" \
"ds_read_b64_tr_b16 a[98:99], v214 offset:18432\n" \
"v_mfma_f32_16x16x32_bf16 v[72:75], a[116:119], a[68:71], v[72:75]\n" \
"s_add_u32 m0, m0, 0x4000\n" \
"ds_read_b64_tr_b16 a[100:101], v214 offset:24576\n" \
"ds_read_b64_tr_b16 a[102:103], v214 offset:26624\n" \
"v_mfma_f32_16x16x32_bf16 v[76:79], a[120:123], a[64:67], v[76:79]\n" \
"buffer_load_dwordx4 v216, s[36:39], s82 offen offset:0 lds\n" \
"v_mfma_f32_16x16x32_bf16 v[76:79], a[124:127], a[68:71], v[76:79]\n" \
"ds_read_b128 a[64:67], v213 offset:0\n" \
"v_mfma_f32_16x16x32_bf16 v[92:95], a[120:123], a[72:75], v[92:95]\n" \
"ds_read_b128 a[68:71], v213 offset:2048\n" \
"v_mfma_f32_16x16x32_bf16 v[92:95], a[124:127], a[76:79], v[92:95]\n" \
"ds_read_b64_tr_b16 a[104:105], v215 offset:16384\n" \
"ds_read_b64_tr_b16 a[106:107], v215 offset:18432\n" \
"v_mfma_f32_16x16x32_bf16 v[88:91], a[112:115], a[72:75], v[88:91]\n" \
"buffer_load_dwordx4 v217, s[36:39], s82 offen offset:1024 lds\n" \
"v_mfma_f32_16x16x32_bf16 v[88:91], a[116:119], a[76:79], v[88:91]\n" \
"ds_read_b128 a[72:75], v213 offset:256\n" \
"v_mfma_f32_16x16x32_bf16 v[104:107], a[112:115], a[80:83], v[104:107]\n" \
"ds_read_b128 a[76:79], v213 offset:2304\n" \
"v_mfma_f32_16x16x32_bf16 v[104:107], a[116:119], a[84:87], v[104:107]\n" \
"ds_read_b64_tr_b16 a[108:109], v215 offset:24576\n" \
"ds_read_b64_tr_b16 a[110:111], v215 offset:26624\n" \
"v_mfma_f32_16x16x32_bf16 v[108:111], a[120:123], a[80:83], v[108:111]\n" \
"buffer_load_dwordx4 v218, s[36:39], s82 offen offset:2048 lds\n" \
"v_mfma_f32_16x16x32_bf16 v[108:111], a[124:127], a[84:87], v[108:111]\n" \
"ds_read_b128 a[80:83], v213 offset:4096\n" \
"v_mfma_f32_16x16x32_bf16 v[124:127], a[120:123], a[88:91], v[124:127]\n" \
"s_waitcnt lgkmcnt(4)\n" \
"v_mfma_f32_16x16x32_bf16 v[124:127], a[124:127], a[92:95], v[124:127]\n" \
"s_sub_u32 s72, s72, 1\n" \
"ds_read_b128 a[84:87], v213 offset:6144\n" \
"v_mfma_f32_16x16x32_bf16 v[120:123], a[112:115], a[88:91], v[120:123]\n" \
"buffer_load_dwordx4 v219, s[36:39], s82 offen offset:3072 lds\n" \
"v_mfma_f32_16x16x32_bf16 v[120:123], a[116:119], a[92:95], v[120:123]\n" \
"s_cbranch_scc0 Lo64_%=\n" \
"v_mfma_f32_16x16x32_bf16 v[64:67], a[96:99], a[64:67], v[64:67]\n" \
"s_waitcnt lgkmcnt(1)\n" \
"ds_read_b128 a[88:91], v213 offset:4352\n" \
"v_mfma_f32_16x16x32_bf16 v[64:67], a[100:103], a[68:71], v[64:67]\n" \
"s_add_u32 s48, s86, 32768\n" \
"s_cmp_eq_u32 s48, 163840\n" \
"s_cselect_b32 s48, 0, s48\n" \
"ds_read_b128 a[92:95], v213 offset:6400\n" \
"v_mfma_f32_16x16x32_bf16 v[68:71], a[104:107], a[64:67], v[68:71]\n" \
"ds_read_b64_tr_b16 a[112:113], v214 offset:16896\n" \
"v_mfma_f32_16x16x32_bf16 v[68:71], a[108:111], a[68:71], v[68:71]\n" \
"ds_read_b64_tr_b16 a[114:115], v214 offset:18944\n" \
"v_mfma_f32_16x16x32_bf16 v[84:87], a[104:107], a[72:75], v[84:87]\n" \
"s_add_u32 s49, s48, 32768\n" \
"s_cmp_eq_u32 s49, 163840\n" \
"s_cselect_b32 s49, 0, s49\n" \
"v_mfma_f32_16x16x32_bf16 v[84:87], a[108:111], a[76:79], v[84:87]\n" \
"ds_read_b64_tr_b16 a[116:117], v214 offset:25088\n" \
"v_add_u32 v224, s49, v211\n" \
"v_mfma_f32_16x16x32_bf16 v[80:83], a[96:99], a[72:75], v[80:83]\n" \
"ds_read_b64_tr_b16 a[118:119], v214 offset:27136\n" \
"v_add_u32 v225, s49, v212\n" \
"v_mfma_f32_16x16x32_bf16 v[80:83], a[100:103], a[76:79], v[80:83]\n" \
"s_waitcnt lgkmcnt(3)\n" \
"ds_read_b64_tr_b16 a[120:121], v215 offset:16896\n" \
"v_xor_b32 v226, 32, v225\n" \
"v_mfma_f32_16x16x32_bf16 v[96:99], a[96:99], a[80:83], v[96:99]\n" \
"v_add_u32 v227, s48, v211\n" \
"v_add_u32 v228, s48, v212\n" \
"v_mfma_f32_16x16x32_bf16 v[96:99], a[100:103], a[84:87], v[96:99]\n" \
"s_sub_u32 s85, s85, 32768\n" \
"s_cselect_b32 s85, 131072, s85\n" \
"ds_read_b64_tr_b16 a[122:123], v215 offset:18944\n" \
"v_mfma_f32_16x16x32_bf16 v[100:103], a[104:107], a[80:83], v[100:103]\n" \
"ds_read_b64_tr_b16 a[124:125], v215 offset:25088\n" \
"v_add_u32 v213, s85, v211\n" \
"v_mfma_f32_16x16x32_bf16 v[100:103], a[108:111], a[84:87], v[100:103]\n" \
"ds_read_b64_tr_b16 a[126:127], v215 offset:27136\n" \
"v_xor_b32 v229, 32, v228\n" \
"v_mfma_f32_16x16x32_bf16 v[116:119], a[104:107], a[88:91], v[116:119]\n" \
"v_mfma_f32_16x16x32_bf16 v[116:119], a[108:111], a[92:95], v[116:119]\n" \
"v_add_u32 v214, s85, v212\n" \
"v_xor_b32 v215, 32, v214\n" \
"v_mfma_f32_16x16x32_bf16 v[112:115], a[96:99], a[88:91], v[112:115]\n" \
"v_mfma_f32_16x16x32_bf16 v[112:115], a[100:103], a[92:95], v[112:115]\n" \
"s_waitcnt vmcnt(8) lgkmcnt(0)\n" \
"s_barrier\n" \
"v_mfma_f32_16x16x32_bf16 v[72:75], a[112:115], a[64:67], v[72:75]\n" \
"ds_read_b128 a[0:3], v213 offset:0\n" \
"ds_read_b128 a[4:7], v213 offset:2048\n" \
"v_mfma_f32_16x16x32_bf16 v[72:75], a[116:119], a[68:71], v[72:75]\n" \
"s_sub_u32 s68, s86, 32768\n" \
"s_cselect_b32 s68, 131072, s68\n" \
"ds_read_b64_tr_b16 a[128:129], v214 offset:16384\n" \
"ds_read_b64_tr_b16 a[130:131], v214 offset:18432\n" \
"v_mfma_f32_16x16x32_bf16 v[76:79], a[120:123], a[64:67], v[76:79]\n" \
"ds_read_b64_tr_b16 a[132:133], v214 offset:24576\n" \
"ds_read_b64_tr_b16 a[134:135], v214 offset:26624\n" \
"v_mfma_f32_16x16x32_bf16 v[76:79], a[124:127], a[68:71], v[76:79]\n" \
"s_cmp_lt_u32 s61, 2\n" \
"s_cselect_b32 s68, s86, s68\n" \
"ds_read_b64_tr_b16 a[136:137], v215 offset:16384\n" \
"ds_read_b64_tr_b16 a[138:139], v215 offset:18432\n" \
"v_mfma_f32_16x16x32_bf16 v[92:95], a[120:123], a[72:75], v[92:95]\n" \
"s_and_b32 s50, s61, 1\n" \
"ds_read_b64_tr_b16 a[140:141], v215 offset:24576\n" \
"ds_read_b64_tr_b16 a[142:143], v215 offset:26624\n" \
"v_mfma_f32_16x16x32_bf16 v[92:95], a[124:127], a[76:79], v[92:95]\n" \
"s_mul_i32 s50, s50, 9216\n" \
"ds_read_b64_tr_b16 a[144:145], v214 offset:16896\n" \
"ds_read_b64_tr_b16 a[146:147], v214 offset:18944\n" \
"v_mfma_f32_16x16x32_bf16 v[88:91], a[112:115], a[72:75], v[88:91]\n" \
"s_add_u32 s68, s68, s50\n" \
"ds_read_b64_tr_b16 a[148:149], v214 offset:25088\n" \
"ds_read_b64_tr_b16 a[150:151], v214 offset:27136\n" \
"v_mfma_f32_16x16x32_bf16 v[88:91], a[116:119], a[76:79], v[88:91]\n" \
"s_add_u32 s68, s68, %3\n" \
"ds_read_b64_tr_b16 a[152:153], v215 offset:16896\n" \
"ds_read_b64_tr_b16 a[154:155], v215 offset:18944\n" \
"v_mfma_f32_16x16x32_bf16 v[104:107], a[112:115], a[80:83], v[104:107]\n" \
"ds_read_b64_tr_b16 a[156:157], v215 offset:25088\n" \
"ds_read_b64_tr_b16 a[158:159], v215 offset:27136\n" \
"v_mfma_f32_16x16x32_bf16 v[104:107], a[116:119], a[84:87], v[104:107]\n" \
"ds_read_b128 a[24:27], v213 offset:256\n" \
"ds_read_b128 a[28:31], v213 offset:2304\n" \
"v_mfma_f32_16x16x32_bf16 v[108:111], a[120:123], a[80:83], v[108:111]\n" \
"ds_read_b128 a[8:11], v224 offset:0\n" \
"ds_read_b128 a[12:15], v224 offset:2048\n" \
"v_mfma_f32_16x16x32_bf16 v[108:111], a[124:127], a[84:87], v[108:111]\n" \
"ds_read_b64_tr_b16 a[160:161], v225 offset:16384\n" \
"ds_read_b64_tr_b16 a[162:163], v225 offset:18432\n" \
"v_mfma_f32_16x16x32_bf16 v[124:127], a[120:123], a[88:91], v[124:127]\n" \
"ds_read_b64_tr_b16 a[164:165], v225 offset:24576\n" \
"ds_read_b64_tr_b16 a[166:167], v225 offset:26624\n" \
"v_mfma_f32_16x16x32_bf16 v[124:127], a[124:127], a[92:95], v[124:127]\n" \
"ds_read_b64_tr_b16 a[168:169], v226 offset:16384\n" \
"ds_read_b64_tr_b16 a[170:171], v226 offset:18432\n" \
"v_mfma_f32_16x16x32_bf16 v[120:123], a[112:115], a[88:91], v[120:123]\n" \
"ds_read_b64_tr_b16 a[172:173], v226 offset:24576\n" \
"ds_read_b64_tr_b16 a[174:175], v226 offset:26624\n" \
"v_mfma_f32_16x16x32_bf16 v[120:123], a[116:119], a[92:95], v[120:123]\n" \
"s_waitcnt lgkmcnt(8)\n" \
"ds_read_b64_tr_b16 a[176:177], v225 offset:16896\n" \
"ds_read_b64_tr_b16 a[178:179], v225 offset:18944\n" \
"v_mfma_f32_16x16x32_bf16 v[64:67], a[128:131], a[0:3], v[64:67]\n" \
"ds_read_b64_tr_b16 a[180:181], v225 offset:25088\n" \
"ds_read_b64_tr_b16 a[182:183], v225 offset:27136\n" \
"v_mfma_f32_16x16x32_bf16 v[64:67], a[132:135], a[4:7], v[64:67]\n" \
"ds_read_b64_tr_b16 a[184:185], v226 offset:16896\n" \
"ds_read_b64_tr_b16 a[186:187], v226 offset:18944\n" \
"v_mfma_f32_16x16x32_bf16 v[68:71], a[136:139], a[0:3], v[68:71]\n" \
"ds_read_b64_tr_b16 a[188:189], v226 offset:25088\n" \
"ds_read_b64_tr_b16 a[190:191], v226 offset:27136\n" \
"v_mfma_f32_16x16x32_bf16 v[68:71], a[140:143], a[4:7], v[68:71]\n" \
"v_lshrrev_b32 v132, 4, v234\n" \
"v_and_b32 v133, 15, v132\n" \
"v_mfma_f32_16x16x32_bf16 v[72:75], a[144:147], a[0:3], v[72:75]\n" \
"v_mul_u32_u24 v128, 144, v133\n" \
"v_lshrrev_b32 v133, 4, v132\n" \
"v_mfma_f32_16x16x32_bf16 v[72:75], a[148:151], a[4:7], v[72:75]\n" \
"v_lshl_add_u32 v128, v133, 3, v128\n" \
"v_add_u32 v128, s68, v128\n" \
"v_mfma_f32_16x16x32_bf16 v[76:79], a[152:155], a[0:3], v[76:79]\n" \
"v_lshrrev_b32 v133, 3, v132\n" \
"v_and_b32 v134, 7, v132\n" \
"v_mfma_f32_16x16x32_bf16 v[76:79], a[156:159], a[4:7], v[76:79]\n" \
"v_mul_u32_u24 v129, 144, v133\n" \
"v_lshl_add_u32 v129, v134, 4, v129\n" \
"v_mfma_f32_16x16x32_bf16 v[92:95], a[152:155], a[24:27], v[92:95]\n" \
"v_add_u32 v129, s68, v129\n" \
"v_lshlrev_b32 v134, 4, v134\n" \
"v_mfma_f32_16x16x32_bf16 v[92:95], a[156:159], a[28:31], v[92:95]\n" \
"v_lshl_add_u32 v130, v133, 13, v134\n" \
"v_mfma_f32_16x16x32_bf16 v[88:91], a[144:147], a[24:27], v[88:91]\n" \
"v_mfma_f32_16x16x32_bf16 v[88:91], a[148:151], a[28:31], v[88:91]\n" \
"v_mfma_f32_16x16x32_bf16 v[84:87], a[136:139], a[24:27], v[84:87]\n" \
"s_waitcnt vmcnt(0)\n" \
"s_barrier\n" \
"v_mfma_f32_16x16x32_bf16 v[84:87], a[140:143], a[28:31], v[84:87]\n" \
"ds_read_b128 a[32:35], v224 offset:256\n" \
"ds_read_b128 a[36:39], v224 offset:2304\n" \
"v_mfma_f32_16x16x32_bf16 v[80:83], a[128:131], a[24:27], v[80:83]\n" \
"ds_read_b128 a[16:19], v227 offset:0\n" \
"ds_read_b128 a[20:23], v227 offset:2048\n" \
"v_mfma_f32_16x16x32_bf16 v[80:83], a[132:135], a[28:31], v[80:83]\n" \
"s_waitcnt lgkmcnt(4)\n" \
"ds_read_b64_tr_b16 a[192:193], v228 offset:16384\n" \
"ds_read_b64_tr_b16 a[194:195], v228 offset:18432\n" \
"v_mfma_f32_16x16x32_bf16 v[64:67], a[160:163], a[8:11], v[64:67]\n" \
"ds_read_b64_tr_b16 a[196:197], v228 offset:24576\n" \
"ds_read_b64_tr_b16 a[198:199], v228 offset:26624\n" \
"v_mfma_f32_16x16x32_bf16 v[64:67], a[164:167], a[12:15], v[64:67]\n" \
"ds_read_b64_tr_b16 a[200:201], v229 offset:16384\n" \
"ds_read_b64_tr_b16 a[202:203], v229 offset:18432\n" \
"v_mfma_f32_16x16x32_bf16 v[68:71], a[168:171], a[8:11], v[68:71]\n" \
"ds_read_b64_tr_b16 a[204:205], v229 offset:24576\n" \
"ds_read_b64_tr_b16 a[206:207], v229 offset:26624\n" \
"v_mfma_f32_16x16x32_bf16 v[68:71], a[172:175], a[12:15], v[68:71]\n" \
"ds_read_b64_tr_b16 a[208:209], v228 offset:16896\n" \
"ds_read_b64_tr_b16 a[210:211], v228 offset:18944\n" \
"v_mfma_f32_16x16x32_bf16 v[72:75], a[176:179], a[8:11], v[72:75]\n" \
"ds_read_b64_tr_b16 a[212:213], v228 offset:25088\n" \
"ds_read_b64_tr_b16 a[214:215], v228 offset:27136\n" \
"v_mfma_f32_16x16x32_bf16 v[72:75], a[180:183], a[12:15], v[72:75]\n" \
"ds_read_b64_tr_b16 a[216:217], v229 offset:16896\n" \
"ds_read_b64_tr_b16 a[218:219], v229 offset:18944\n" \
"v_mfma_f32_16x16x32_bf16 v[76:79], a[184:187], a[8:11], v[76:79]\n" \
"ds_read_b64_tr_b16 a[220:221], v229 offset:25088\n" \
"ds_read_b64_tr_b16 a[222:223], v229 offset:27136\n" \
"v_mfma_f32_16x16x32_bf16 v[76:79], a[188:191], a[12:15], v[76:79]\n" \
"s_waitcnt lgkmcnt(8)\n" \
"ds_read_b128 a[40:43], v227 offset:256\n" \
"ds_read_b128 a[44:47], v227 offset:2304\n" \
"v_mfma_f32_16x16x32_bf16 v[92:95], a[184:187], a[32:35], v[92:95]\n" \
"ds_read_b128 a[248:251], v213 offset:4096\n" \
"ds_read_b128 a[252:255], v213 offset:6144\n" \
"v_mfma_f32_16x16x32_bf16 v[92:95], a[188:191], a[36:39], v[92:95]\n" \
"ds_read_b128 a[48:51], v224 offset:4096\n" \
"ds_read_b128 a[52:55], v224 offset:6144\n" \
"v_mfma_f32_16x16x32_bf16 v[88:91], a[176:179], a[32:35], v[88:91]\n" \
"ds_read_b128 a[56:59], v227 offset:4096\n" \
"ds_read_b128 a[60:63], v227 offset:6144\n" \
"v_mfma_f32_16x16x32_bf16 v[88:91], a[180:183], a[36:39], v[88:91]\n" \
"ds_read_b128 a[224:227], v213 offset:4352\n" \
"ds_read_b128 a[228:231], v213 offset:6400\n" \
"v_mfma_f32_16x16x32_bf16 v[84:87], a[168:171], a[32:35], v[84:87]\n" \
"ds_read_b128 a[232:235], v224 offset:4352\n" \
"ds_read_b128 a[236:239], v224 offset:6400\n" \
"v_mfma_f32_16x16x32_bf16 v[84:87], a[172:175], a[36:39], v[84:87]\n" \
"ds_read_b128 a[240:243], v227 offset:4352\n" \
"ds_read_b128 a[244:247], v227 offset:6400\n" \
"v_mfma_f32_16x16x32_bf16 v[80:83], a[160:163], a[32:35], v[80:83]\n" \
"v_mfma_f32_16x16x32_bf16 v[80:83], a[164:167], a[36:39], v[80:83]\n" \
"v_mfma_f32_16x16x32_bf16 v[64:67], a[192:195], a[16:19], v[64:67]\n" \
"v_mfma_f32_16x16x32_bf16 v[64:67], a[196:199], a[20:23], v[64:67]\n" \
"v_mfma_f32_16x16x32_bf16 v[68:71], a[200:203], a[16:19], v[68:71]\n" \
"v_mfma_f32_16x16x32_bf16 v[68:71], a[204:207], a[20:23], v[68:71]\n" \
"s_waitcnt lgkmcnt(0)\n" \
"v_mfma_f32_16x16x32_bf16 v[72:75], a[208:211], a[16:19], v[72:75]\n" \
"v_mfma_f32_16x16x32_bf16 v[72:75], a[212:215], a[20:23], v[72:75]\n" \
"v_cvt_pk_bf16_f32 v136, v64, v65\n" \
"v_cvt_pk_bf16_f32 v137, v66, v67\n" \
"v_mfma_f32_16x16x32_bf16 v[76:79], a[216:219], a[16:19], v[76:79]\n" \
"v_mfma_f32_16x16x32_bf16 v[76:79], a[220:223], a[20:23], v[76:79]\n" \
"ds_write_b64 v128, v[136:137] offset:0\n" \
"v_mfma_f32_16x16x32_bf16 v[80:83], a[192:195], a[40:43], v[80:83]\n" \
"v_cvt_pk_bf16_f32 v138, v68, v69\n" \
"v_cvt_pk_bf16_f32 v139, v70, v71\n" \
"v_mfma_f32_16x16x32_bf16 v[80:83], a[196:199], a[44:47], v[80:83]\n" \
"ds_write_b64 v128, v[138:139] offset:32\n" \
"v_mfma_f32_16x16x32_bf16 v[84:87], a[200:203], a[40:43], v[84:87]\n" \
"v_cvt_pk_bf16_f32 v136, v72, v73\n" \
"v_cvt_pk_bf16_f32 v137, v74, v75\n" \
"v_mfma_f32_16x16x32_bf16 v[84:87], a[204:207], a[44:47], v[84:87]\n" \
"ds_write_b64 v128, v[136:137] offset:64\n" \
"v_mfma_f32_16x16x32_bf16 v[88:91], a[208:211], a[40:43], v[88:91]\n" \
"v_cvt_pk_bf16_f32 v138, v76, v77\n" \
"v_cvt_pk_bf16_f32 v139, v78, v79\n" \
"v_mfma_f32_16x16x32_bf16 v[88:91], a[212:215], a[44:47], v[88:91]\n" \
"ds_write_b64 v128, v[138:139] offset:96\n" \
"v_mfma_f32_16x16x32_bf16 v[92:95], a[216:219], a[40:43], v[92:95]\n" \
"ds_read_b128 v[140:143], v129 offset:0\n" \
"ds_read_b128 v[144:147], v129 offset:1152\n" \
"v_mfma_f32_16x16x32_bf16 v[92:95], a[220:223], a[44:47], v[92:95]\n" \
"v_cvt_pk_bf16_f32 v136, v80, v81\n" \
"v_cvt_pk_bf16_f32 v137, v82, v83\n" \
"v_mfma_f32_16x16x32_bf16 v[96:99], a[128:131], a[248:251], v[96:99]\n" \
"ds_write_b64 v128, v[136:137] offset:2304\n" \
"v_mfma_f32_16x16x32_bf16 v[96:99], a[132:135], a[252:255], v[96:99]\n" \
"s_add_u32 s70, s88, 0\n" \
"s_waitcnt lgkmcnt(2)\n" \
"v_cvt_pk_bf16_f32 v138, v84, v85\n" \
"v_mfma_f32_16x16x32_bf16 v[96:99], a[160:163], a[48:51], v[96:99]\n" \
"buffer_store_dwordx4 v[140:143], v130, s[52:55], s70 offen sc1\n" \
"v_mfma_f32_16x16x32_bf16 v[96:99], a[164:167], a[52:55], v[96:99]\n" \
"v_cvt_pk_bf16_f32 v139, v86, v87\n" \
"v_mfma_f32_16x16x32_bf16 v[96:99], a[192:195], a[56:59], v[96:99]\n" \
"s_add_u32 s70, s88, 65536\n" \
"s_waitcnt lgkmcnt(1)\n" \
"v_mfma_f32_16x16x32_bf16 v[96:99], a[196:199], a[60:63], v[96:99]\n" \
"buffer_store_dwordx4 v[144:147], v130, s[52:55], s70 offen sc1\n" \
"ds_write_b64 v128, v[138:139] offset:2336\n" \
"v_mfma_f32_16x16x32_bf16 v[100:103], a[204:207], a[60:63], v[100:103]\n" \
"v_cvt_pk_bf16_f32 v136, v88, v89\n" \
"v_cvt_pk_bf16_f32 v137, v90, v91\n" \
"v_mfma_f32_16x16x32_bf16 v[100:103], a[200:203], a[56:59], v[100:103]\n" \
"ds_write_b64 v128, v[136:137] offset:2368\n" \
"v_mfma_f32_16x16x32_bf16 v[100:103], a[172:175], a[52:55], v[100:103]\n" \
"v_cvt_pk_bf16_f32 v138, v92, v93\n" \
"v_cvt_pk_bf16_f32 v139, v94, v95\n" \
"v_mfma_f32_16x16x32_bf16 v[100:103], a[168:171], a[48:51], v[100:103]\n" \
"v_mfma_f32_16x16x32_bf16 v[100:103], a[140:143], a[252:255], v[100:103]\n" \
"v_mfma_f32_16x16x32_bf16 v[100:103], a[136:139], a[248:251], v[100:103]\n" \
"ds_write_b64 v128, v[138:139] offset:2400\n" \
"v_mfma_f32_16x16x32_bf16 v[104:107], a[144:147], a[248:251], v[104:107]\n" \
"ds_read_b128 v[148:151], v129 offset:2304\n" \
"ds_read_b128 v[152:155], v129 offset:3456\n" \
"v_mfma_f32_16x16x32_bf16 v[104:107], a[148:151], a[252:255], v[104:107]\n" \
"v_cvt_pk_bf16_f32 v136, v96, v97\n" \
"v_cvt_pk_bf16_f32 v137, v98, v99\n" \
"v_mfma_f32_16x16x32_bf16 v[104:107], a[176:179], a[48:51], v[104:107]\n" \
"ds_write_b64 v128, v[136:137] offset:4608\n" \
"v_mfma_f32_16x16x32_bf16 v[104:107], a[180:183], a[52:55], v[104:107]\n" \
"v_cvt_pk_bf16_f32 v138, v100, v101\n" \
"v_cvt_pk_bf16_f32 v139, v102, v103\n" \
"v_mfma_f32_16x16x32_bf16 v[104:107], a[208:211], a[56:59], v[104:107]\n" \
"s_add_u32 s70, s88, 4096\n" \
"s_waitcnt lgkmcnt(2)\n" \
"v_mfma_f32_16x16x32_bf16 v[104:107], a[212:215], a[60:63], v[104:107]\n" \
"buffer_store_dwordx4 v[148:151], v130, s[52:55], s70 offen sc1\n" \
"ds_write_b64 v128, v[138:139] offset:4640\n" \
"v_mfma_f32_16x16x32_bf16 v[108:111], a[220:223], a[60:63], v[108:111]\n" \
"s_add_u32 s70, s88, 69632\n" \
"s_waitcnt lgkmcnt(2)\n" \
"v_mfma_f32_16x16x32_bf16 v[108:111], a[216:219], a[56:59], v[108:111]\n" \
"buffer_store_dwordx4 v[152:155], v130, s[52:55], s70 offen sc1\n" \
"v_mfma_f32_16x16x32_bf16 v[108:111], a[188:191], a[52:55], v[108:111]\n" \
"v_mfma_f32_16x16x32_bf16 v[108:111], a[184:187], a[48:51], v[108:111]\n" \
"v_cvt_pk_bf16_f32 v136, v104, v105\n" \
"v_cvt_pk_bf16_f32 v137, v106, v107\n" \
"v_mfma_f32_16x16x32_bf16 v[108:111], a[156:159], a[252:255], v[108:111]\n" \
"v_mfma_f32_16x16x32_bf16 v[108:111], a[152:155], a[248:251], v[108:111]\n" \
"ds_write_b64 v128, v[136:137] offset:4672\n" \
"v_mfma_f32_16x16x32_bf16 v[112:115], a[128:131], a[224:227], v[112:115]\n" \
"v_mfma_f32_16x16x32_bf16 v[112:115], a[132:135], a[228:231], v[112:115]\n" \
"v_mfma_f32_16x16x32_bf16 v[112:115], a[160:163], a[232:235], v[112:115]\n" \
"v_mfma_f32_16x16x32_bf16 v[112:115], a[164:167], a[236:239], v[112:115]\n" \
"v_cvt_pk_bf16_f32 v138, v108, v109\n" \
"v_cvt_pk_bf16_f32 v139, v110, v111\n" \
"v_mfma_f32_16x16x32_bf16 v[112:115], a[192:195], a[240:243], v[112:115]\n" \
"v_mfma_f32_16x16x32_bf16 v[112:115], a[196:199], a[244:247], v[112:115]\n" \
"ds_write_b64 v128, v[138:139] offset:4704\n" \
"v_mfma_f32_16x16x32_bf16 v[116:119], a[204:207], a[244:247], v[116:119]\n" \
"ds_read_b128 v[156:159], v129 offset:4608\n" \
"ds_read_b128 v[160:163], v129 offset:5760\n" \
"v_mfma_f32_16x16x32_bf16 v[116:119], a[200:203], a[240:243], v[116:119]\n" \
"v_mfma_f32_16x16x32_bf16 v[116:119], a[172:175], a[236:239], v[116:119]\n" \
"v_mfma_f32_16x16x32_bf16 v[116:119], a[168:171], a[232:235], v[116:119]\n" \
"v_cvt_pk_bf16_f32 v136, v112, v113\n" \
"v_cvt_pk_bf16_f32 v137, v114, v115\n" \
"v_mfma_f32_16x16x32_bf16 v[116:119], a[140:143], a[228:231], v[116:119]\n" \
"s_add_u32 s70, s88, 131072\n" \
"s_waitcnt lgkmcnt(1)\n" \
"v_mfma_f32_16x16x32_bf16 v[116:119], a[136:139], a[224:227], v[116:119]\n" \
"buffer_store_dwordx4 v[156:159], v130, s[52:55], s70 offen sc1\n" \
"ds_write_b64 v128, v[136:137] offset:6912\n" \
"v_mfma_f32_16x16x32_bf16 v[120:123], a[144:147], a[224:227], v[120:123]\n" \
"s_add_u32 s70, s88, 196608\n" \
"s_waitcnt lgkmcnt(1)\n" \
"v_mfma_f32_16x16x32_bf16 v[120:123], a[148:151], a[228:231], v[120:123]\n" \
"buffer_store_dwordx4 v[160:163], v130, s[52:55], s70 offen sc1\n" \
"v_mfma_f32_16x16x32_bf16 v[120:123], a[176:179], a[232:235], v[120:123]\n" \
"v_mfma_f32_16x16x32_bf16 v[120:123], a[180:183], a[236:239], v[120:123]\n" \
"v_cvt_pk_bf16_f32 v138, v116, v117\n" \
"v_cvt_pk_bf16_f32 v139, v118, v119\n" \
"v_mfma_f32_16x16x32_bf16 v[120:123], a[208:211], a[240:243], v[120:123]\n" \
"v_mfma_f32_16x16x32_bf16 v[120:123], a[212:215], a[244:247], v[120:123]\n" \
"ds_write_b64 v128, v[138:139] offset:6944\n" \
"v_mfma_f32_16x16x32_bf16 v[124:127], a[220:223], a[244:247], v[124:127]\n" \
"v_mfma_f32_16x16x32_bf16 v[124:127], a[216:219], a[240:243], v[124:127]\n" \
"v_mfma_f32_16x16x32_bf16 v[124:127], a[188:191], a[236:239], v[124:127]\n" \
"v_mfma_f32_16x16x32_bf16 v[124:127], a[184:187], a[232:235], v[124:127]\n" \
"v_cvt_pk_bf16_f32 v136, v120, v121\n" \
"v_cvt_pk_bf16_f32 v137, v122, v123\n" \
"v_mfma_f32_16x16x32_bf16 v[124:127], a[156:159], a[228:231], v[124:127]\n" \
"v_mfma_f32_16x16x32_bf16 v[124:127], a[152:155], a[224:227], v[124:127]\n" \
"ds_write_b64 v128, v[136:137] offset:6976\n" \
"s_nop 7\n" \
"v_cvt_pk_bf16_f32 v138, v124, v125\n" \
"v_cvt_pk_bf16_f32 v139, v126, v127\n" \
"ds_write_b64 v128, v[138:139] offset:7008\n" \
"ds_read_b128 v[164:167], v129 offset:6912\n" \
"ds_read_b128 v[168:171], v129 offset:8064\n" \
"s_add_u32 s70, s88, 135168\n" \
"s_waitcnt lgkmcnt(1)\n" \
"buffer_store_dwordx4 v[164:167], v130, s[52:55], s70 offen sc1\n" \
"s_add_u32 s70, s88, 200704\n" \
"s_waitcnt lgkmcnt(0)\n" \
"buffer_store_dwordx4 v[168:171], v130, s[52:55], s70 offen sc1\n" \
"s_endpgm\n" \
"Lhot_end_%=:\n" \
"Lpl1_%=:\n" \
"s_mov_b32 s72, 0\n" \
"Lpk1_%=:\n" \
"s_sleep 2\n" \
"buffer_load_dword v227, v242, s[56:59], s90 offen sc1\n" \
"s_waitcnt vmcnt(0)\n" \
"v_cmp_eq_u32 vcc, s60, v227\n" \
"s_nop 1\n" \
"s_andn2_b32 s70, 0xff, vcc_lo\n" \
"s_cbranch_scc0 Lpr1_%=\n" \
"s_add_u32 s72, s72, 1\n" \
"s_cmp_lt_u32 s72, 0x40000\n" \
"s_cbranch_scc1 Lpk1_%=\n" \
"s_branch Lpr1_%=\n" \
"Lpl2_%=:\n" \
"s_mov_b32 s72, 0\n" \
"Lpk2_%=:\n" \
"s_sleep 2\n" \
"buffer_load_dword v239, v242, s[56:59], s95 offen sc1\n" \
"s_waitcnt vmcnt(0)\n" \
"v_cmp_eq_u32 vcc, s60, v239\n" \
"s_nop 1\n" \
"s_andn2_b32 s70, s69, vcc_lo\n" \
"s_cbranch_scc0 Lpr2_%=\n" \
"s_add_u32 s72, s72, 1\n" \
"s_cmp_lt_u32 s72, 0x40000\n" \
"s_cbranch_scc1 Lpk2_%=\n" \
"s_branch Lpr2_%=\n" \
"Lpl3_%=:\n" \
"s_mov_b32 s72, 0\n" \
"Lpk3_%=:\n" \
"s_sleep 2\n" \
"buffer_load_dword v239, v242, s[56:59], s95 offen sc1\n" \
"s_waitcnt vmcnt(0)\n" \
"v_cmp_eq_u32 vcc, s60, v239\n" \
"s_nop 1\n" \
"s_andn2_b32 s70, s69, vcc_lo\n" \
"s_cbranch_scc0 Lpr3_%=\n" \
"s_add_u32 s72, s72, 1\n" \
"s_cmp_lt_u32 s72, 0x40000\n" \
"s_cbranch_scc1 Lpk3_%=\n" \
"s_branch Lpr3_%=\n" \
"Lexact_%=:\n" \
"v_mov_b32 v239, 0xff800000\n" \
"s_and_b32 s70, s61, 1\n" \
"s_lshl_b32 s70, s70, 4\n" \
"v_add_u32 v186, s70, v238\n" \
"v_accvgpr_read_b32 v160, a128\n" \
"v_accvgpr_read_b32 v161, a129\n" \
"v_accvgpr_read_b32 v162, a130\n" \
"v_accvgpr_read_b32 v163, a131\n" \
"v_accvgpr_read_b32 v164, a132\n" \
"v_accvgpr_read_b32 v165, a133\n" \
"v_accvgpr_read_b32 v166, a134\n" \
"v_accvgpr_read_b32 v167, a135\n" \
"v_accvgpr_read_b32 v168, a136\n" \
"v_accvgpr_read_b32 v169, a137\n" \
"v_accvgpr_read_b32 v170, a138\n" \
"v_accvgpr_read_b32 v171, a139\n" \
"v_accvgpr_read_b32 v172, a140\n" \
"v_accvgpr_read_b32 v173, a141\n" \
"v_accvgpr_read_b32 v174, a142\n" \
"v_accvgpr_read_b32 v175, a143\n" \
"s_add_u32 s83, s85, 0\n" \
"s_add_u32 s84, s87, 0\n" \
"s_call_b64 s[64:65], Lex1_%=\n" \
"v_accvgpr_read_b32 v160, a144\n" \
"v_accvgpr_read_b32 v161, a145\n" \
"v_accvgpr_read_b32 v162, a146\n" \
"v_accvgpr_read_b32 v163, a147\n" \
"v_accvgpr_read_b32 v164, a148\n" \
"v_accvgpr_read_b32 v165, a149\n" \
"v_accvgpr_read_b32 v166, a150\n" \
"v_accvgpr_read_b32 v167, a151\n" \
"v_accvgpr_read_b32 v168, a152\n" \
"v_accvgpr_read_b32 v169, a153\n" \
"v_accvgpr_read_b32 v170, a154\n" \
"v_accvgpr_read_b32 v171, a155\n" \
"v_accvgpr_read_b32 v172, a156\n" \
"v_accvgpr_read_b32 v173, a157\n" \
"v_accvgpr_read_b32 v174, a158\n" \
"v_accvgpr_read_b32 v175, a159\n" \
"s_add_u32 s83, s85, 2\n" \
"s_add_u32 s84, s87, 532480\n" \
"s_call_b64 s[64:65], Lex1_%=\n" \
"v_accvgpr_read_b32 v160, a160\n" \
"v_accvgpr_read_b32 v161, a161\n" \
"v_accvgpr_read_b32 v162, a162\n" \
"v_accvgpr_read_b32 v163, a163\n" \
"v_accvgpr_read_b32 v164, a164\n" \
"v_accvgpr_read_b32 v165, a165\n" \
"v_accvgpr_read_b32 v166, a166\n" \
"v_accvgpr_read_b32 v167, a167\n" \
"v_accvgpr_read_b32 v168, a168\n" \
"v_accvgpr_read_b32 v169, a169\n" \
"v_accvgpr_read_b32 v170, a170\n" \
"v_accvgpr_read_b32 v171, a171\n" \
"v_accvgpr_read_b32 v172, a172\n" \
"v_accvgpr_read_b32 v173, a173\n" \
"v_accvgpr_read_b32 v174, a174\n" \
"v_accvgpr_read_b32 v175, a175\n" \
"s_add_u32 s83, s86, 0\n" \
"s_add_u32 s84, s88, 0\n" \
"s_call_b64 s[64:65], Lex1_%=\n" \
"v_accvgpr_read_b32 v160, a176\n" \
"v_accvgpr_read_b32 v161, a177\n" \
"v_accvgpr_read_b32 v162, a178\n" \
"v_accvgpr_read_b32 v163, a179\n" \
"v_accvgpr_read_b32 v164, a180\n" \
"v_accvgpr_read_b32 v165, a181\n" \
"v_accvgpr_read_b32 v166, a182\n" \
"v_accvgpr_read_b32 v167, a183\n" \
"v_accvgpr_read_b32 v168, a184\n" \
"v_accvgpr_read_b32 v169, a185\n" \
"v_accvgpr_read_b32 v170, a186\n" \
"v_accvgpr_read_b32 v171, a187\n" \
"v_accvgpr_read_b32 v172, a188\n" \
"v_accvgpr_read_b32 v173, a189\n" \
"v_accvgpr_read_b32 v174, a190\n" \
"v_accvgpr_read_b32 v175, a191\n" \
"s_add_u32 s83, s86, 2\n" \
"s_add_u32 s84, s88, 532480\n" \
"s_call_b64 s[64:65], Lex1_%=\n" \
"s_waitcnt vmcnt(0)\n" \
"s_branch Lexr_%=\n" \
"Lex1_%=:\n" \
"v_mov_b32 v128, 0\n" \
"v_mov_b32 v129, 0\n" \
"v_mov_b32 v130, 0\n" \
"v_mov_b32 v131, 0\n" \
"v_mov_b32 v132, 0\n" \
"v_mov_b32 v133, 0\n" \
"v_mov_b32 v134, 0\n" \
"v_mov_b32 v135, 0\n" \
"v_mov_b32 v136, 0\n" \
"v_mov_b32 v137, 0\n" \
"v_mov_b32 v138, 0\n" \
"v_mov_b32 v139, 0\n" \
"v_mov_b32 v140, 0\n" \
"v_mov_b32 v141, 0\n" \
"v_mov_b32 v142, 0\n" \
"v_mov_b32 v143, 0\n" \
"v_mov_b32 v144, 0\n" \
"v_mov_b32 v145, 0\n" \
"v_mov_b32 v146, 0\n" \
"v_mov_b32 v147, 0\n" \
"v_mov_b32 v148, 0\n" \
"v_mov_b32 v149, 0\n" \
"v_mov_b32 v150, 0\n" \
"v_mov_b32 v151, 0\n" \
"v_mov_b32 v152, 0\n" \
"v_mov_b32 v153, 0\n" \
"v_mov_b32 v154, 0\n" \
"v_mov_b32 v155, 0\n" \
"v_mov_b32 v156, 0\n" \
"v_mov_b32 v157, 0\n" \
"v_mov_b32 v158, 0\n" \
"v_mov_b32 v159, 0\n" \
"v_mov_b32 v184, 0xf149f2ca\n" \
"v_mov_b32 v185, 0\n" \
"s_mov_b32 s81, 0\n" \
"s_mul_i32 s82, s62, 557824\n" \
"Lex2_%=:\n" \
"s_add_u32 s70, s82, 4096\n" \
"buffer_load_dwordx4 a[64:67], v236, s[56:59], s82 offen offset:0 sc1\n" \
"buffer_load_dwordx4 a[68:71], v236, s[56:59], s82 offen offset:2048 sc1\n" \
"buffer_load_dwordx4 a[72:75], v236, s[56:59], s70 offen offset:0 sc1\n" \
"buffer_load_dwordx4 a[76:79], v236, s[56:59], s70 offen offset:2048 sc1\n" \
"buffer_load_dwordx4 a[80:83], v236, s[56:59], s82 offen offset:256 sc1\n" \
"buffer_load_dwordx4 a[84:87], v236, s[56:59], s82 offen offset:2304 sc1\n" \
"buffer_load_dwordx4 a[88:91], v236, s[56:59], s70 offen offset:256 sc1\n" \
"buffer_load_dwordx4 a[92:95], v236, s[56:59], s70 offen offset:2304 sc1\n" \
"s_add_u32 s70, s82, 17850368\n" \
"s_add_u32 s71, s70, 4096\n" \
"buffer_load_dwordx4 a[96:99], v234, s[56:59], s70 offen offset:0 sc1\n" \
"buffer_load_dwordx4 a[100:103], v234, s[56:59], s70 offen offset:1024 sc1\n" \
"buffer_load_dwordx4 a[104:107], v234, s[56:59], s70 offen offset:2048 sc1\n" \
"buffer_load_dwordx4 a[108:111], v234, s[56:59], s70 offen offset:3072 sc1\n" \
"buffer_load_dwordx4 a[112:115], v234, s[56:59], s71 offen offset:0 sc1\n" \
"buffer_load_dwordx4 a[116:119], v234, s[56:59], s71 offen offset:1024 sc1\n" \
"buffer_load_dwordx4 a[120:123], v234, s[56:59], s71 offen offset:2048 sc1\n" \
"buffer_load_dwordx4 a[124:127], v234, s[56:59], s71 offen offset:3072 sc1\n" \
"s_waitcnt vmcnt(0)\n" \
"v_mfma_f32_16x16x32_bf16 v[176:179], a[64:67], v[160:163], 0\n" \
"v_mfma_f32_16x16x32_bf16 v[176:179], a[68:71], v[164:167], v[176:179]\n" \
"v_mfma_f32_16x16x32_bf16 v[176:179], a[72:75], v[168:171], v[176:179]\n" \
"v_mfma_f32_16x16x32_bf16 v[176:179], a[76:79], v[172:175], v[176:179]\n" \
"v_mfma_f32_16x16x32_bf16 v[180:183], a[80:83], v[160:163], 0\n" \
"v_mfma_f32_16x16x32_bf16 v[180:183], a[84:87], v[164:167], v[180:183]\n" \
"v_mfma_f32_16x16x32_bf16 v[180:183], a[88:91], v[168:171], v[180:183]\n" \
"v_mfma_f32_16x16x32_bf16 v[180:183], a[92:95], v[172:175], v[180:183]\n" \
"s_nop 7\n" \
"s_nop 1\n" \
"s_cmp_eq_u32 s81, s83\n" \
"s_cbranch_scc0 Lex3_%=\n" \
"v_cmp_gt_i32 vcc, 0, v186\n" \
"s_nop 1\n" \
"v_cndmask_b32 v176, v176, v239, vcc\n" \
"v_cmp_gt_i32 vcc, 1, v186\n" \
"s_nop 1\n" \
"v_cndmask_b32 v177, v177, v239, vcc\n" \
"v_cmp_gt_i32 vcc, 2, v186\n" \
"s_nop 1\n" \
"v_cndmask_b32 v178, v178, v239, vcc\n" \
"v_cmp_gt_i32 vcc, 3, v186\n" \
"s_nop 1\n" \
"v_cndmask_b32 v179, v179, v239, vcc\n" \
"v_cmp_gt_i32 vcc, 16, v186\n" \
"s_nop 1\n" \
"v_cndmask_b32 v180, v180, v239, vcc\n" \
"v_cmp_gt_i32 vcc, 17, v186\n" \
"s_nop 1\n" \
"v_cndmask_b32 v181, v181, v239, vcc\n" \
"v_cmp_gt_i32 vcc, 18, v186\n" \
"s_nop 1\n" \
"v_cndmask_b32 v182, v182, v239, vcc\n" \
"v_cmp_gt_i32 vcc, 19, v186\n" \
"s_nop 1\n" \
"v_cndmask_b32 v183, v183, v239, vcc\n" \
"Lex3_%=:\n" \
"v_max3_f32 v187, v176, v177, v178\n" \
"v_max3_f32 v187, v187, v179, v180\n" \
"v_max3_f32 v187, v187, v181, v182\n" \
"v_max3_f32 v187, v187, v183, v184\n" \
"v_mov_b32 v188, v187\n" \
"s_nop 1\n" \
"v_permlane16_swap_b32 v188, v187\n" \
"v_max_f32 v187, v187, v188\n" \
"v_mov_b32 v188, v187\n" \
"s_nop 1\n" \
"v_permlane32_swap_b32 v188, v187\n" \
"v_max_f32 v187, v187, v188\n" \
"v_sub_f32 v189, v184, v187\n" \
"v_exp_f32 v189, v189\n" \
"v_mov_b32 v184, v187\n" \
"v_sub_f32 v176, v176, v187\n" \
"v_sub_f32 v177, v177, v187\n" \
"v_sub_f32 v178, v178, v187\n" \
"v_sub_f32 v179, v179, v187\n" \
"v_sub_f32 v180, v180, v187\n" \
"v_sub_f32 v181, v181, v187\n" \
"v_sub_f32 v182, v182, v187\n" \
"v_sub_f32 v183, v183, v187\n" \
"v_exp_f32 v176, v176\n" \
"v_exp_f32 v177, v177\n" \
"v_exp_f32 v178, v178\n" \
"v_exp_f32 v179, v179\n" \
"v_exp_f32 v180, v180\n" \
"v_exp_f32 v181, v181\n" \
"v_exp_f32 v182, v182\n" \
"v_exp_f32 v183, v183\n" \
"v_mul_f32 v185, v185, v189\n" \
"v_add_f32 v185, v185, v176\n" \
"v_add_f32 v185, v185, v177\n" \
"v_add_f32 v185, v185, v178\n" \
"v_add_f32 v185, v185, v179\n" \
"v_add_f32 v185, v185, v180\n" \
"v_add_f32 v185, v185, v181\n" \
"v_add_f32 v185, v185, v182\n" \
"v_add_f32 v185, v185, v183\n" \
"v_cvt_pk_bf16_f32 v112, v176, v177\n" \
"v_cvt_pk_bf16_f32 v113, v178, v179\n" \
"v_cvt_pk_bf16_f32 v114, v180, v181\n" \
"v_cvt_pk_bf16_f32 v115, v182, v183\n" \
"v_mul_f32 v128, v128, v189\n" \
"v_mul_f32 v129, v129, v189\n" \
"v_mul_f32 v130, v130, v189\n" \
"v_mul_f32 v131, v131, v189\n" \
"v_mul_f32 v132, v132, v189\n" \
"v_mul_f32 v133, v133, v189\n" \
"v_mul_f32 v134, v134, v189\n" \
"v_mul_f32 v135, v135, v189\n" \
"v_mul_f32 v136, v136, v189\n" \
"v_mul_f32 v137, v137, v189\n" \
"v_mul_f32 v138, v138, v189\n" \
"v_mul_f32 v139, v139, v189\n" \
"v_mul_f32 v140, v140, v189\n" \
"v_mul_f32 v141, v141, v189\n" \
"v_mul_f32 v142, v142, v189\n" \
"v_mul_f32 v143, v143, v189\n" \
"v_mul_f32 v144, v144, v189\n" \
"v_mul_f32 v145, v145, v189\n" \
"v_mul_f32 v146, v146, v189\n" \
"v_mul_f32 v147, v147, v189\n" \
"v_mul_f32 v148, v148, v189\n" \
"v_mul_f32 v149, v149, v189\n" \
"v_mul_f32 v150, v150, v189\n" \
"v_mul_f32 v151, v151, v189\n" \
"v_mul_f32 v152, v152, v189\n" \
"v_mul_f32 v153, v153, v189\n" \
"v_mul_f32 v154, v154, v189\n" \
"v_mul_f32 v155, v155, v189\n" \
"v_mul_f32 v156, v156, v189\n" \
"v_mul_f32 v157, v157, v189\n" \
"v_mul_f32 v158, v158, v189\n" \
"v_mul_f32 v159, v159, v189\n" \
"s_nop 1\n" \
"v_mfma_f32_16x16x32_bf16 v[128:131], a[96:99], v[112:115], v[128:131]\n" \
"v_mfma_f32_16x16x32_bf16 v[132:135], a[100:103], v[112:115], v[132:135]\n" \
"v_mfma_f32_16x16x32_bf16 v[136:139], a[104:107], v[112:115], v[136:139]\n" \
"v_mfma_f32_16x16x32_bf16 v[140:143], a[108:111], v[112:115], v[140:143]\n" \
"v_mfma_f32_16x16x32_bf16 v[144:147], a[112:115], v[112:115], v[144:147]\n" \
"v_mfma_f32_16x16x32_bf16 v[148:151], a[116:119], v[112:115], v[148:151]\n" \
"v_mfma_f32_16x16x32_bf16 v[152:155], a[120:123], v[112:115], v[152:155]\n" \
"v_mfma_f32_16x16x32_bf16 v[156:159], a[124:127], v[112:115], v[156:159]\n" \
"s_nop 7\n" \
"s_nop 1\n" \
"s_and_b32 s70, s81, 1\n" \
"s_mul_i32 s70, s70, 1024\n" \
"s_add_u32 s82, s82, s70\n" \
"s_add_u32 s82, s82, 8192\n" \
"s_add_u32 s81, s81, 1\n" \
"s_cmp_le_u32 s81, s83\n" \
"s_cbranch_scc1 Lex2_%=\n" \
"v_mov_b32 v187, v185\n" \
"s_nop 1\n" \
"v_permlane16_swap_b32 v187, v185\n" \
"v_add_f32 v185, v185, v187\n" \
"v_mov_b32 v187, v185\n" \
"s_nop 1\n" \
"v_permlane32_swap_b32 v187, v185\n" \
"v_add_f32 v185, v185, v187\n" \
"v_rcp_f32 v185, v185\n" \
"s_nop 0\n" \
"v_mul_f32 v128, v128, v185\n" \
"v_mul_f32 v129, v129, v185\n" \
"v_mul_f32 v130, v130, v185\n" \
"v_mul_f32 v131, v131, v185\n" \
"v_mul_f32 v132, v132, v185\n" \
"v_mul_f32 v133, v133, v185\n" \
"v_mul_f32 v134, v134, v185\n" \
"v_mul_f32 v135, v135, v185\n" \
"v_mul_f32 v136, v136, v185\n" \
"v_mul_f32 v137, v137, v185\n" \
"v_mul_f32 v138, v138, v185\n" \
"v_mul_f32 v139, v139, v185\n" \
"v_mul_f32 v140, v140, v185\n" \
"v_mul_f32 v141, v141, v185\n" \
"v_mul_f32 v142, v142, v185\n" \
"v_mul_f32 v143, v143, v185\n" \
"v_mul_f32 v144, v144, v185\n" \
"v_mul_f32 v145, v145, v185\n" \
"v_mul_f32 v146, v146, v185\n" \
"v_mul_f32 v147, v147, v185\n" \
"v_mul_f32 v148, v148, v185\n" \
"v_mul_f32 v149, v149, v185\n" \
"v_mul_f32 v150, v150, v185\n" \
"v_mul_f32 v151, v151, v185\n" \
"v_mul_f32 v152, v152, v185\n" \
"v_mul_f32 v153, v153, v185\n" \
"v_mul_f32 v154, v154, v185\n" \
"v_mul_f32 v155, v155, v185\n" \
"v_mul_f32 v156, v156, v185\n" \
"v_mul_f32 v157, v157, v185\n" \
"v_mul_f32 v158, v158, v185\n" \
"v_mul_f32 v159, v159, v185\n" \
"s_add_u32 s70, s84, 4096\n" \
"v_cvt_pk_bf16_f32 v112, v128, v129\n" \
"v_cvt_pk_bf16_f32 v113, v130, v131\n" \
"v_cvt_pk_bf16_f32 v114, v132, v133\n" \
"v_cvt_pk_bf16_f32 v115, v134, v135\n" \
"buffer_store_dwordx4 v[112:115], v236, s[56:59], s84 offen offset:0 sc1\n" \
"v_cvt_pk_bf16_f32 v116, v136, v137\n" \
"v_cvt_pk_bf16_f32 v117, v138, v139\n" \
"v_cvt_pk_bf16_f32 v118, v140, v141\n" \
"v_cvt_pk_bf16_f32 v119, v142, v143\n" \
"buffer_store_dwordx4 v[116:119], v236, s[56:59], s84 offen offset:2048 sc1\n" \
"v_cvt_pk_bf16_f32 v120, v144, v145\n" \
"v_cvt_pk_bf16_f32 v121, v146, v147\n" \
"v_cvt_pk_bf16_f32 v122, v148, v149\n" \
"v_cvt_pk_bf16_f32 v123, v150, v151\n" \
"buffer_store_dwordx4 v[120:123], v236, s[56:59], s70 offen offset:0 sc1\n" \
"v_cvt_pk_bf16_f32 v124, v152, v153\n" \
"v_cvt_pk_bf16_f32 v125, v154, v155\n" \
"v_cvt_pk_bf16_f32 v126, v156, v157\n" \
"v_cvt_pk_bf16_f32 v127, v158, v159\n" \
"buffer_store_dwordx4 v[124:127], v236, s[56:59], s70 offen offset:2048 sc1\n" \
"s_waitcnt vmcnt(0)\n" \
"s_setpc_b64 s[64:65]\n" \
"Lpl4_%=:\n" \
"s_mov_b32 s72, 0\n" \
"Lpk4_%=:\n" \
"s_sleep 2\n" \
"buffer_load_dwordx2 v[226:227], v228, s[56:59], s13 offen sc1\n" \
"s_waitcnt vmcnt(0)\n" \
"v_cmp_eq_u32 vcc, s60, v226\n" \
"s_nop 1\n" \
"s_andn2_b32 s70, 0xff, vcc_lo\n" \
"s_cbranch_scc0 Lpr4_%=\n" \
"s_add_u32 s72, s72, 1\n" \
"s_cmp_lt_u32 s72, 0x40000\n" \
"s_cbranch_scc1 Lpk4_%=\n" \
"s_branch Lpr4_%=\n" \
"Lpl5_%=:\n" \
"s_mov_b32 s72, 0\n" \
"Lpk5_%=:\n" \
"s_sleep 2\n" \
"buffer_load_dword v220, v221, s[56:59], s66 offen sc1\n" \
"s_waitcnt vmcnt(0)\n" \
"v_cmp_eq_u32 vcc, s60, v220\n" \
"s_nop 1\n" \
"s_andn2_b32 s70, s65, vcc_lo\n" \
"s_cbranch_scc0 Lpr5_%=\n" \
"s_add_u32 s72, s72, 1\n" \
"s_cmp_lt_u32 s72, 0x40000\n" \
"s_cbranch_scc1 Lpk5_%=\n" \
"s_branch Lpr5_%=\n" \
"Lpl6_%=:\n" \
"s_mov_b32 s72, 0\n" \
"Lpk6_%=:\n" \
"s_sleep 2\n" \
"buffer_load_dword v220, v221, s[56:59], s67 offen sc1\n" \
"s_waitcnt vmcnt(0)\n" \
"v_cmp_eq_u32 vcc, s60, v220\n" \
"s_nop 1\n" \
"s_andn2_b32 s70, s74, vcc_lo\n" \
"s_cbranch_scc0 Lpr6_%=\n" \
"s_add_u32 s72, s72, 1\n" \
"s_cmp_lt_u32 s72, 0x40000\n" \
"s_cbranch_scc1 Lpk6_%=\n" \
"s_branch Lpr6_%=\n" \
"Lpl7_%=:\n" \
"s_mov_b32 s72, 0\n" \
"Lpk7_%=:\n" \
"s_sleep 2\n" \
"buffer_load_dword v220, v241, s[56:59], s64 offen sc1\n" \
"s_waitcnt vmcnt(0)\n" \
"v_cmp_eq_u32 vcc, s60, v220\n" \
"s_nop 1\n" \
"s_andn2_b32 s70, s69, vcc_lo\n" \
"s_cbranch_scc0 Lpr7_%=\n" \
"s_add_u32 s72, s72, 1\n" \
"s_cmp_lt_u32 s72, 0x40000\n" \
"s_cbranch_scc1 Lpk7_%=\n" \
"s_branch Lpr7_%=\n" \
"Lcode_end_%=:\n" \
".p2align 12\n" \
""
#define MK_ASM_CLOBBERS "v1", "v2", "v3", "v4", "v5", "v6", "v7", "v8", "v9", "v10", "v11", "v12", "v13", "v14", "v15", "v16", "v17", "v18", "v19", "v20", "v21", "v22", "v23", "v24", "v25", "v26", "v27", "v28", "v29", "v30", "v31", "v32", "v33", "v34", "v35", "v36", "v37", "v38", "v39", "v40", "v41", "v42", "v43", "v44", "v45", "v46", "v47", "v48", "v49", "v50", "v51", "v52", "v53", "v54", "v55", "v56", "v57", "v58", "v59", "v60", "v61", "v62", "v63", "v64", "v65", "v66", "v67", "v68", "v69", "v70", "v71", "v72", "v73", "v74", "v75", "v76", "v77", "v78", "v79", "v80", "v81", "v82", "v83", "v84", "v85", "v86", "v87", "v88", "v89", "v90", "v91", "v92", "v93", "v94", "v95", "v96", "v97", "v98", "v99", "v100", "v101", "v102", "v103", "v104", "v105", "v106", "v107", "v108", "v109", "v110", "v111", "v112", "v113", "v114", "v115", "v116", "v117", "v118", "v119", "v120", "v121", "v122", "v123", "v124", "v125", "v126", "v127", "v128", "v129", "v130", "v131", "v132", "v133", "v134", "v135", "v136", "v137", "v138", "v139", "v140", "v141", "v142", "v143", "v144", "v145", "v146", "v147", "v148", "v149", "v150", "v151", "v152", "v153", "v154", "v155", "v156", "v157", "v158", "v159", "v160", "v161", "v162", "v163", "v164", "v165", "v166", "v167", "v168", "v169", "v170", "v171", "v172", "v173", "v174", "v175", "v176", "v177", "v178", "v179", "v180", "v181", "v182", "v183", "v184", "v185", "v186", "v187", "v188", "v189", "v190", "v191", "v192", "v193", "v194", "v195", "v196", "v197", "v198", "v199", "v200", "v201", "v202", "v203", "v204", "v205", "v206", "v207", "v208", "v209", "v210", "v211", "v212", "v213", "v214", "v215", "v216", "v217", "v218", "v219", "v220", "v221", "v222", "v223", "v224", "v225", "v226", "v227", "v228", "v229", "v230", "v231", "v232", "v233", "v234", "v235", "v236", "v237", "v238", "v239", "v240", "v241", "v242", "v243", "v244", "v245", "v246", "v247", "v248", "v249", "v250", "v251", "v252", "v253", "v254", "v255", "a0", "a1", "a2", "a3", "a4", "a5", "a6", "a7", "a8", "a9", "a10", "a11", "a12", "a13", "a14", "a15", "a16", "a17", "a18", "a19", "a20", "a21", "a22", "a23", "a24", "a25", "a26", "a27", "a28", "a29", "a30", "a31", "a32", "a33", "a34", "a35", "a36", "a37", "a38", "a39", "a40", "a41", "a42", "a43", "a44", "a45", "a46", "a47", "a48", "a49", "a50", "a51", "a52", "a53", "a54", "a55", "a56", "a57", "a58", "a59", "a60", "a61", "a62", "a63", "a64", "a65", "a66", "a67", "a68", "a69", "a70", "a71", "a72", "a73", "a74", "a75", "a76", "a77", "a78", "a79", "a80", "a81", "a82", "a83", "a84", "a85", "a86", "a87", "a88", "a89", "a90", "a91", "a92", "a93", "a94", "a95", "a96", "a97", "a98", "a99", "a100", "a101", "a102", "a103", "a104", "a105", "a106", "a107", "a108", "a109", "a110", "a111", "a112", "a113", "a114", "a115", "a116", "a117", "a118", "a119", "a120", "a121", "a122", "a123", "a124", "a125", "a126", "a127", "a128", "a129", "a130", "a131", "a132", "a133", "a134", "a135", "a136", "a137", "a138", "a139", "a140", "a141", "a142", "a143", "a144", "a145", "a146", "a147", "a148", "a149", "a150", "a151", "a152", "a153", "a154", "a155", "a156", "a157", "a158", "a159", "a160", "a161", "a162", "a163", "a164", "a165", "a166", "a167", "a168", "a169", "a170", "a171", "a172", "a173", "a174", "a175", "a176", "a177", "a178", "a179", "a180", "a181", "a182", "a183", "a184", "a185", "a186", "a187", "a188", "a189", "a190", "a191", "a192", "a193", "a194", "a195", "a196", "a197", "a198", "a199", "a200", "a201", "a202", "a203", "a204", "a205", "a206", "a207", "a208", "a209", "a210", "a211", "a212", "a213", "a214", "a215", "a216", "a217", "a218", "a219", "a220", "a221", "a222", "a223", "a224", "a225", "a226", "a227", "a228", "a229", "a230", "a231", "a232", "a233", "a234", "a235", "a236", "a237", "a238", "a239", "a240", "a241", "a242", "a243", "a244", "a245", "a246", "a247", "a248", "a249", "a250", "a251", "a252", "a253", "a254", "a255", "s8", "s9", "s10", "s11", "s12", "s13", "s14", "s15", "s16", "s17", "s18", "s19", "s20", "s21", "s22", "s23", "s24", "s25", "s26", "s27", "s28", "s29", "s30", "s31", "s36", "s37", "s38", "s39", "s40", "s41", "s42", "s43", "s44", "s45", "s46", "s47", "s48", "s49", "s50", "s51", "s52", "s53", "s54", "s55", "s56", "s57", "s58", "s59", "s60", "s61", "s62", "s63", "s64", "s65", "s66", "s67", "s68", "s69", "s70", "s71", "s72", "s73", "s74", "s75", "s76", "s77", "s78", "s79", "s80", "s81", "s82", "s83", "s84", "s85", "s86", "s87", "s88", "s89", "s90", "s91", "s92", "s93", "s94", "s95", "vcc", "scc", "memory"
namespace mk {
constexpr int LDS_BYTES = 163840;
struct Args { const void* in[9]; void* out; void* ws; };
__global__ __launch_bounds__(256, 1) void mega_k(const Args a) {
    extern __shared__ __attribute__((aligned(16))) unsigned char shm[];
    const unsigned ldsb = (unsigned)(size_t)(LAS unsigned char*)shm;
    asm volatile(MK_ASM_BODY
                 :
                 : "s"(__builtin_amdgcn_kernarg_segment_ptr()), "s"(blockIdx.x), "v"(threadIdx.x), "s"(ldsb)
                 : MK_ASM_CLOBBERS);
}
}
extern "C" void kernel_launch(void* const* d_in, const int* in_sizes, int n_in, void* d_out, int out_size, void* d_ws, size_t ws_size, hipStream_t stream) {
    (void)hipFuncSetAttribute((const void*)mk::mega_k, hipFuncAttributeMaxDynamicSharedMemorySize, mk::LDS_BYTES);
    mk::Args a;
    for (int i = 0; i < 9; ++i) a.in[i] = d_in[i];
    a.out = d_out; a.ws = (char*)d_ws + ((ws_size - WS_END) & ~(size_t)0x1fffff);
    mk::mega_k<<<256, 256, mk::LDS_BYTES, stream>>>(a);
}
```

```cpp
#include <hip/hip_runtime.h>
#include <stdint.h>
#define LAS __attribute__((address_space(3)))
constexpr size_t LAY_TS = 17408, LAY_HS = 557824, LAY_CRS = 266240, LAY_ORS = 69632, WS_K = 0, WS_V = 17850368, WS_CTX = 35700736, WS_OB = 52740096, WS_FL = 65585152, WS_END = 65634304, LAY_VS = 2;
#define MK_ASM_BODY \
"s_load_dwordx16 s[8:23], %0, 0x0\n" \
"s_load_dwordx4 s[24:27], %0, 0x40\n" \
"s_load_dwordx2 s[28:29], %0, 0x50\n" \
"v_readfirstlane_b32 s61, %2\n" \
"s_lshr_b32 s61, s61, 6\n" \
"v_mbcnt_lo_u32_b32 v78, -1, 0\n" \
"v_mbcnt_hi_u32_b32 v78, -1, v78\n" \
"v_lshlrev_b32 v234, 4, v78\n" \
"s_getpc_b64 s[64:65]\n" \
"Lpc_%=:\n" \
"s_lshr_b32 s70, %1, 3\n" \
"s_lshl_b32 s70, s70, 2\n" \
"s_add_u32 s70, s70, s61\n" \
"s_and_b32 s71, s70, 31\n" \
"s_lshl_b32 s71, s71, 12\n" \
"v_lshl_add_u32 v237, v234, 2, s71\n" \
"s_mov_b32 s71, Lcode_end_%=-Lpc_%=-4\n" \
"v_min_u32 v237, s71, v237\n" \
"global_load_dword v238, v237, s[64:65]\n" \
"s_and_b32 s62, %1, 7\n" \
"s_lshl_b32 s62, s62, 2\n" \
"s_lshr_b32 s70, %1, 6\n" \
"s_add_u32 s62, s62, s70\n" \
"s_bfe_u32 s63, %1, 0x30003\n" \
"s_lshl_b32 s74, s61, 13\n" \
"s_add_u32 s74, s74, %3\n" \
"v_bfe_u32 v79, v78, 4, 1\n" \
"v_bfe_u32 v80, v78, 2, 2\n" \
"v_lshl_add_u32 v80, v79, 3, v80\n" \
"s_lshl_b32 s71, s61, 4\n" \
"v_add_u32 v80, s71, v80\n" \
"v_lshlrev_b32 v80, 13, v80\n" \
"v_lshlrev_b32 v79, 1, v79\n" \
"v_and_b32 v81, 3, v78\n" \
"v_xor_b32 v79, v79, v81\n" \
"v_lshl_add_u32 v80, v79, 4, v80\n" \
"v_lshrrev_b32 v90, 5, v78\n" \
"v_lshl_add_u32 v84, v90, 6, v80\n" \
"v_add_u32 v68, 4096, v84\n" \
"v_add_u32 v69, 3072, v84\n" \
"v_add_u32 v70, 2176, v84\n" \
"v_add_u32 v71, 1152, v84\n" \
"s_lshl_b32 s83, s62, 8\n" \
"s_add_u32 s84, s83, 32768\n" \
"v_lshrrev_b32 v85, 3, v78\n" \
"v_and_b32 v86, 7, v78\n" \
"v_xor_b32 v86, v86, v85\n" \
"v_lshlrev_b32 v86, 4, v86\n" \
"v_lshl_add_u32 v64, v85, 10, v86\n" \
"v_add_u32 v65, 7168, v64\n" \
"v_add_u32 v66, 14336, v64\n" \
"v_add_u32 v67, 21504, v64\n" \
"s_bitcmp1_b32 s61, 0\n" \
"s_cselect_b32 s70, 0, 15\n" \
"s_xor_b32 s81, s63, s70\n" \
"s_lshl_b32 s81, s81, 17\n" \
"s_lshr_b32 s70, s61, 1\n" \
"s_lshl_b32 s71, s70, 16\n" \
"s_add_u32 s81, s81, s71\n" \
"s_add_u32 s82, s81, 32768\n" \
"s_mov_b32 s42, 4198400\n" \
"s_mov_b32 s43, 0x20000\n" \
"s_mov_b32 s46, 4198400\n" \
"s_mov_b32 s47, 0x20000\n" \
"s_mov_b32 s38, 2097152\n" \
"s_mov_b32 s39, 0x20000\n" \
"s_waitcnt lgkmcnt(0)\n" \
"s_mov_b64 s[40:41], s[14:15]\n" \
"s_sub_u32 s40, s40, 4096\n" \
"s_subb_u32 s41, s41, 0\n" \
"s_and_b32 s41, s41, 0xffff\n" \
"s_mov_b64 s[44:45], s[16:17]\n" \
"s_sub_u32 s44, s44, 4096\n" \
"s_subb_u32 s45, s45, 0\n" \
"s_and_b32 s45, s45, 0xffff\n" \
"s_mov_b64 s[36:37], s[10:11]\n" \
"s_and_b32 s37, s37, 0xffff\n" \
"s_add_u32 m0, s74, 32768\n" \
"s_nop 0\n" \
"buffer_load_dwordx4 v68, s[40:43], s83 offen offset:0 lds\n" \
"buffer_load_dwordx4 v69, s[44:47], s83 offen offset:1024 lds\n" \
"buffer_load_dwordx4 v70, s[40:43], s83 offen offset:2048 lds\n" \
"buffer_load_dwordx4 v71, s[44:47], s83 offen offset:3072 lds\n" \
"s_xor_b32 s83, s83, 0x80000\n" \
"s_add_u32 m0, s74, 36864\n" \
"s_nop 0\n" \
"buffer_load_dwordx4 v68, s[40:43], s84 offen offset:0 lds\n" \
"buffer_load_dwordx4 v69, s[44:47], s84 offen offset:1024 lds\n" \
"buffer_load_dwordx4 v70, s[40:43], s84 offen offset:2048 lds\n" \
"buffer_load_dwordx4 v71, s[44:47], s84 offen offset:3072 lds\n" \
"s_xor_b32 s84, s84, 0x80000\n" \
"s_add_u32 m0, s74, 0\n" \
"s_nop 0\n" \
"buffer_load_dwordx4 v64, s[36:39], s81 offen offset:0 lds\n" \
"buffer_load_dwordx4 v65, s[36:39], s81 offen offset:1024 lds\n" \
"buffer_load_dwordx4 v66, s[36:39], s81 offen offset:2048 lds\n" \
"buffer_load_dwordx4 v67, s[36:39], s81 offen offset:3072 lds\n" \
"s_xor_b32 s81, s81, 0x80\n" \
"s_add_u32 m0, s74, 4096\n" \
"s_nop 0\n" \
"buffer_load_dwordx4 v64, s[36:39], s82 offen offset:0 lds\n" \
"buffer_load_dwordx4 v65, s[36:39], s82 offen offset:1024 lds\n" \
"buffer_load_dwordx4 v66, s[36:39], s82 offen offset:2048 lds\n" \
"buffer_load_dwordx4 v67, s[36:39], s82 offen offset:3072 lds\n" \
"s_xor_b32 s82, s82, 0x80\n" \
"v_lshrrev_b32 v87, 4, v78\n" \
"v_and_b32 v79, 15, v78\n" \
"v_and_b32 v80, 7, v78\n" \
"v_xor_b32 v80, v80, v87\n" \
"v_lshlrev_b32 v79, 7, v79\n" \
"s_lshl_b32 s70, s70, 14\n" \
"s_add_u32 s70, s70, %3\n" \
"v_lshl_add_u32 v88, v80, 4, v79\n" \
"v_add_u32 v72, s70, v88\n" \
"v_xor_b32 v73, 64, v72\n" \
"v_lshlrev_b32 v79, 3, v81\n" \
"v_bfe_u32 v80, v78, 2, 2\n" \
"v_lshl_add_u32 v79, v80, 6, v79\n" \
"v_bfe_u32 v80, v78, 4, 1\n" \
"v_lshl_add_u32 v79, v80, 8, v79\n" \
"v_lshl_add_u32 v89, v80, 5, v79\n" \
"s_and_b32 s70, s61, 1\n" \
"s_lshl_b32 s70, s70, 11\n" \
"s_add_u32 s70, s70, %3\n" \
"v_lshl_add_u32 v82, v90, 13, v89\n" \
"v_add_u32 v74, s70, v82\n" \
"v_xor_b32 v75, 32, v74\n" \
"s_mov_b64 s[56:57], s[28:29]\n" \
"s_and_b32 s57, s57, 0xffff\n" \
"s_mov_b32 s58, 65634304\n" \
"s_mov_b32 s59, 0x20000\n" \
"v_mov_b32 v241, 0\n" \
"s_lshl_b32 s66, %1, 2\n" \
"s_add_u32 s66, s66, s61\n" \
"s_lshl_b32 s66, s66, 10\n" \
"s_lshr_b32 s67, %1, 3\n" \
"s_lshl_b32 s67, s67, 2\n" \
"s_add_u32 s67, s67, s61\n" \
"s_lshl_b32 s67, s67, 13\n" \
"s_and_b32 s64, %1, 7\n" \
"s_lshl_b32 s64, s64, 10\n" \
"s_add_u32 s67, s67, s64\n" \
"s_mov_b64 exec, 0\n" \
"s_cmp_eq_u32 s61, 0\n" \
"s_cbranch_scc1 Lst0x0_%=\n" \
"s_cmp_eq_u32 s61, 1\n" \
"s_cbranch_scc1 Lst0x1_%=\n" \
"s_cmp_eq_u32 s61, 2\n" \
"s_cbranch_scc1 Lst0x2_%=\n" \
"s_branch Lst0x3_%=\n" \
"Lcb0_%=:\n" \
"s_mov_b64 exec, -1\n" \
"s_add_u32 m0, s74, 131072\n" \
"s_nop 0\n" \
"buffer_load_dwordx4 v68, s[40:43], s83 offen offset:0 lds\n" \
"buffer_load_dwordx4 v69, s[44:47], s83 offen offset:1024 lds\n" \
"buffer_load_dwordx4 v70, s[40:43], s83 offen offset:2048 lds\n" \
"buffer_load_dwordx4 v71, s[44:47], s83 offen offset:3072 lds\n" \
"s_xor_b32 s83, s83, 0x180000\n" \
"s_add_u32 m0, s74, 135168\n" \
"s_nop 0\n" \
"buffer_load_dwordx4 v68, s[40:43], s84 offen offset:0 lds\n" \
"buffer_load_dwordx4 v69, s[44:47], s84 offen offset:1024 lds\n" \
"buffer_load_dwordx4 v70, s[40:43], s84 offen offset:2048 lds\n" \
"buffer_load_dwordx4 v71, s[44:47], s84 offen offset:3072 lds\n" \
"s_xor_b32 s84, s84, 0x180000\n" \
"s_mov_b32 s93, 65593344\n" \
"buffer_load_dword v240, v241, s[56:59], s93 offen sc1\n" \
"s_mov_b64 s[64:65], s[10:11]\n" \
"s_add_u32 s68, s66, 0x0\n" \
"s_add_u32 s64, s64, s68\n" \
"s_addc_u32 s65, s65, 0\n" \
"global_load_dword v238, v234, s[64:65]\n" \
"s_mov_b64 s[64:65], s[10:11]\n" \
"s_add_u32 s68, s66, 0x100000\n" \
"s_add_u32 s64, s64, s68\n" \
"s_addc_u32 s65, s65, 0\n" \
"global_load_dword v238, v234, s[64:65]\n" \
"s_mov_b64 s[64:65], s[14:15]\n" \
"s_add_u32 s68, s67, 0x100000\n" \
"s_add_u32 s64, s64, s68\n" \
"s_addc_u32 s65, s65, 0\n" \
"global_load_dword v238, v234, s[64:65]\n" \
"s_mov_b64 s[64:65], s[16:17]\n" \
"s_add_u32 s68, s67, 0x100000\n" \
"s_add_u32 s64, s64, s68\n" \
"s_addc_u32 s65, s65, 0\n" \
"global_load_dword v238, v234, s[64:65]\n" \
"s_waitcnt vmcnt(13)\n" \
"s_barrier\n" \
"ds_read_b64_tr_b16 a[64:65], v74 offset:32768\n" \
"ds_read_b64_tr_b16 a[66:67], v74 offset:36864\n" \
"ds_read_b64_tr_b16 a[68:69], v74 offset:49152\n" \
"ds_read_b64_tr_b16 a[70:71], v74 offset:53248\n" \
"ds_read_b128 a[0:3], v72 offset:0\n" \
"ds_read_b128 a[4:7], v73 offset:0\n" \
"ds_read_b64_tr_b16 a[72:73], v75 offset:32768\n" \
"ds_read_b64_tr_b16 a[74:75], v75 offset:36864\n" \
"ds_read_b64_tr_b16 a[76:77], v75 offset:49152\n" \
"ds_read_b64_tr_b16 a[78:79], v75 offset:53248\n" \
"ds_read_b64_tr_b16 a[80:81], v74 offset:33280\n" \
"ds_read_b64_tr_b16 a[82:83], v74 offset:37376\n" \
"ds_read_b64_tr_b16 a[84:85], v74 offset:49664\n" \
"ds_read_b64_tr_b16 a[86:87], v74 offset:53760\n" \
"ds_read_b64_tr_b16 a[88:89], v75 offset:33280\n" \
"ds_read_b64_tr_b16 a[90:91], v75 offset:37376\n" \
"ds_read_b64_tr_b16 a[92:93], v75 offset:49664\n" \
"ds_read_b64_tr_b16 a[94:95], v75 offset:53760\n" \
"ds_read_b128 a[8:11], v72 offset:2048\n" \
"ds_read_b128 a[12:15], v73 offset:2048\n" \
"s_waitcnt lgkmcnt(14)\n" \
"v_mfma_f32_16x16x32_bf16 v[0:3], a[64:67], a[0:3], 0\n" \
"s_add_u32 m0, s74, 65536\n" \
"ds_read_b128 a[16:19], v72 offset:4096\n" \
"v_mfma_f32_16x16x32_bf16 v[0:3], a[68:71], a[4:7], v[0:3]\n" \
"s_waitcnt lgkmcnt(11)\n" \
"buffer_load_dwordx4 v64, s[36:39], s81 offen offset:0 lds\n" \
"v_mfma_f32_16x16x32_bf16 v[4:7], a[72:75], a[0:3], 0\n" \
"ds_read_b128 a[20:23], v73 offset:4096\n" \
"v_mfma_f32_16x16x32_bf16 v[4:7], a[76:79], a[4:7], v[4:7]\n" \
"s_waitcnt lgkmcnt(8)\n" \
"v_mfma_f32_16x16x32_bf16 v[8:11], a[80:83], a[0:3], 0\n" \
"s_waitcnt lgkmcnt(2)\n" \
"ds_read_b128 a[24:27], v72 offset:6144\n" \
"v_mfma_f32_16x16x32_bf16 v[8:11], a[84:87], a[4:7], v[8:11]\n" \
"buffer_load_dwordx4 v65, s[36:39], s81 offen offset:1024 lds\n" \
"v_mfma_f32_16x16x32_bf16 v[12:15], a[88:91], a[0:3], 0\n" \
"ds_read_b128 a[28:31], v73 offset:6144\n" \
"v_mfma_f32_16x16x32_bf16 v[12:15], a[92:95], a[4:7], v[12:15]\n" \
"v_mfma_f32_16x16x32_bf16 v[28:31], a[88:91], a[8:11], 0\n" \
"ds_read_b128 a[32:35], v72 offset:8192\n" \
"v_mfma_f32_16x16x32_bf16 v[28:31], a[92:95], a[12:15], v[28:31]\n" \
"buffer_load_dwordx4 v66, s[36:39], s81 offen offset:2048 lds\n" \
"v_mfma_f32_16x16x32_bf16 v[24:27], a[80:83], a[8:11], 0\n" \
"ds_read_b128 a[36:39], v73 offset:8192\n" \
"v_mfma_f32_16x16x32_bf16 v[24:27], a[84:87], a[12:15], v[24:27]\n" \
"v_mfma_f32_16x16x32_bf16 v[20:23], a[72:75], a[8:11], 0\n" \
"ds_read_b128 a[40:43], v72 offset:10240\n" \
"v_mfma_f32_16x16x32_bf16 v[20:23], a[76:79], a[12:15], v[20:23]\n" \
"buffer_load_dwordx4 v67, s[36:39], s81 offen offset:3072 lds\n" \
"v_mfma_f32_16x16x32_bf16 v[16:19], a[64:67], a[8:11], 0\n" \
"ds_read_b128 a[44:47], v73 offset:10240\n" \
"v_mfma_f32_16x16x32_bf16 v[16:19], a[68:71], a[12:15], v[16:19]\n" \
"s_waitcnt lgkmcnt(2)\n" \
"s_xor_b32 s81, s81, 0x180\n" \
"v_mfma_f32_16x16x32_bf16 v[32:35], a[64:67], a[16:19], 0\n" \
"s_add_u32 m0, s74, 69632\n" \
"ds_read_b128 a[48:51], v72 offset:12288\n" \
"v_mfma_f32_16x16x32_bf16 v[32:35], a[68:71], a[20:23], v[32:35]\n" \
"buffer_load_dwordx4 v64, s[36:39], s82 offen offset:0 lds\n" \
"v_mfma_f32_16x16x32_bf16 v[36:39], a[72:75], a[16:19], 0\n" \
"ds_read_b128 a[52:55], v73 offset:12288\n" \
"v_mfma_f32_16x16x32_bf16 v[36:39], a[76:79], a[20:23], v[36:39]\n" \
"v_mfma_f32_16x16x32_bf16 v[40:43], a[80:83], a[16:19], 0\n" \
"ds_read_b128 a[56:59], v72 offset:14336\n" \
"v_mfma_f32_16x16x32_bf16 v[40:43], a[84:87], a[20:23], v[40:43]\n" \
"buffer_load_dwordx4 v65, s[36:39], s82 offen offset:1024 lds\n" \
"v_mfma_f32_16x16x32_bf16 v[44:47], a[88:91], a[16:19], 0\n" \
"ds_read_b128 a[60:63], v73 offset:14336\n" \
"v_mfma_f32_16x16x32_bf16 v[44:47], a[92:95], a[20:23], v[44:47]\n" \
"v_mfma_f32_16x16x32_bf16 v[60:63], a[88:91], a[24:27], 0\n" \
"ds_read_b64_tr_b16 a[96:97], v74 offset:33792\n" \
"v_mfma_f32_16x16x32_bf16 v[60:63], a[92:95], a[28:31], v[60:63]\n" \
"buffer_load_dwordx4 v66, s[36:39], s82 offen offset:2048 lds\n" \
"v_mfma_f32_16x16x32_bf16 v[56:59], a[80:83], a[24:27], 0\n" \
"ds_read_b64_tr_b16 a[98:99], v74 offset:37888\n" \
"v_mfma_f32_16x16x32_bf16 v[56:59], a[84:87], a[28:31], v[56:59]\n" \
"v_mfma_f32_16x16x32_bf16 v[52:55], a[72:75], a[24:27], 0\n" \
"ds_read_b64_tr_b16 a[100:101], v74 offset:50176\n" \
"v_mfma_f32_16x16x32_bf16 v[52:55], a[76:79], a[28:31], v[52:55]\n" \
"buffer_load_dwordx4 v67, s[36:39], s82 offen offset:3072 lds\n" \
"v_mfma_f32_16x16x32_bf16 v[48:51], a[64:67], a[24:27], 0\n" \
"ds_read_b64_tr_b16 a[102:103], v74 offset:54272\n" \
"v_mfma_f32_16x16x32_bf16 v[48:51], a[68:71], a[28:31], v[48:51]\n" \
"v_mfma_f32_16x16x32_bf16 v[144:147], a[64:67], a[32:35], 0\n" \
"s_xor_b32 s82, s82, 0x180\n" \
"ds_read_b64_tr_b16 a[104:105], v75 offset:33792\n" \
"v_mfma_f32_16x16x32_bf16 v[144:147], a[68:71], a[36:39], v[144:147]\n" \
"v_mfma_f32_16x16x32_bf16 v[148:151], a[72:75], a[32:35], 0\n" \
"ds_read_b64_tr_b16 a[106:107], v75 offset:37888\n" \
"v_mfma_f32_16x16x32_bf16 v[148:151], a[76:79], a[36:39], v[148:151]\n" \
"v_mfma_f32_16x16x32_bf16 v[152:155], a[80:83], a[32:35], 0\n" \
"ds_read_b64_tr_b16 a[108:109], v75 offset:50176\n" \
"v_mfma_f32_16x16x32_bf16 v[152:155], a[84:87], a[36:39], v[152:155]\n" \
"v_mfma_f32_16x16x32_bf16 v[156:159], a[88:91], a[32:35], 0\n" \
"ds_read_b64_tr_b16 a[110:111], v75 offset:54272\n" \
"v_mfma_f32_16x16x32_bf16 v[156:159], a[92:95], a[36:39], v[156:159]\n" \
"s_waitcnt lgkmcnt(2)\n" \
"v_mfma_f32_16x16x32_bf16 v[188:191], a[88:91], a[40:43], 0\n" \
"ds_read_b64_tr_b16 a[112:113], v74 offset:34304\n" \
"v_add_u32 v96, 0x10000, v72\n" \
"v_mfma_f32_16x16x32_bf16 v[188:191], a[92:95], a[44:47], v[188:191]\n" \
"v_mfma_f32_16x16x32_bf16 v[184:187], a[80:83], a[40:43], 0\n" \
"ds_read_b64_tr_b16 a[114:115], v74 offset:38400\n" \
"v_add_u32 v97, 0x10000, v73\n" \
"v_mfma_f32_16x16x32_bf16 v[184:187], a[84:87], a[44:47], v[184:187]\n" \
"v_mfma_f32_16x16x32_bf16 v[180:183], a[72:75], a[40:43], 0\n" \
"ds_read_b64_tr_b16 a[116:117], v74 offset:50688\n" \
"v_add_u32 v98, 0x10000, v74\n" \
"v_mfma_f32_16x16x32_bf16 v[180:183], a[76:79], a[44:47], v[180:183]\n" \
"v_mfma_f32_16x16x32_bf16 v[176:179], a[64:67], a[40:43], 0\n" \
"ds_read_b64_tr_b16 a[118:119], v74 offset:54784\n" \
"v_add_u32 v99, 0x10000, v75\n" \
"v_mfma_f32_16x16x32_bf16 v[176:179], a[68:71], a[44:47], v[176:179]\n" \
"v_mfma_f32_16x16x32_bf16 v[192:195], a[64:67], a[48:51], 0\n" \
"s_add_u32 m0, s74, 98304\n" \
"ds_read_b64_tr_b16 a[120:121], v75 offset:34304\n" \
"v_add_u32 v100, 0x20000, v72\n" \
"v_mfma_f32_16x16x32_bf16 v[192:195], a[68:71], a[52:55], v[192:195]\n" \
"buffer_load_dwordx4 v68, s[40:43], s83 offen offset:0 lds\n" \
"v_mfma_f32_16x16x32_bf16 v[196:199], a[72:75], a[48:51], 0\n" \
"ds_read_b64_tr_b16 a[122:123], v75 offset:38400\n" \
"v_add_u32 v101, 0x20000, v73\n" \
"v_mfma_f32_16x16x32_bf16 v[196:199], a[76:79], a[52:55], v[196:199]\n" \
"v_mfma_f32_16x16x32_bf16 v[200:203], a[80:83], a[48:51], 0\n" \
"ds_read_b64_tr_b16 a[124:125], v75 offset:50688\n" \
"v_add_u32 v102, 0x20000, v74\n" \
"v_mfma_f32_16x16x32_bf16 v[200:203], a[84:87], a[52:55], v[200:203]\n" \
"buffer_load_dwordx4 v69, s[44:47], s83 offen offset:1024 lds\n" \
"v_mfma_f32_16x16x32_bf16 v[204:207], a[88:91], a[48:51], 0\n" \
"ds_read_b64_tr_b16 a[126:127], v75 offset:54784\n" \
"v_add_u32 v103, 0x20000, v75\n" \
"v_mfma_f32_16x16x32_bf16 v[204:207], a[92:95], a[52:55], v[204:207]\n" \
"v_mfma_f32_16x16x32_bf16 v[220:223], a[88:91], a[56:59], 0\n" \
"v_mfma_f32_16x16x32_bf16 v[220:223], a[92:95], a[60:63], v[220:223]\n" \
"buffer_load_dwordx4 v70, s[40:43], s83 offen offset:2048 lds\n" \
"v_mfma_f32_16x16x32_bf16 v[216:219], a[80:83], a[56:59], 0\n" \
"v_mfma_f32_16x16x32_bf16 v[216:219], a[84:87], a[60:63], v[216:219]\n" \
"v_mfma_f32_16x16x32_bf16 v[212:215], a[72:75], a[56:59], 0\n" \
"v_mfma_f32_16x16x32_bf16 v[212:215], a[76:79], a[60:63], v[212:215]\n" \
"buffer_load_dwordx4 v71, s[44:47], s83 offen offset:3072 lds\n" \
"v_mfma_f32_16x16x32_bf16 v[208:211], a[64:67], a[56:59], 0\n" \
"v_mfma_f32_16x16x32_bf16 v[208:211], a[68:71], a[60:63], v[208:211]\n" \
"s_xor_b32 s83, s83, 0x80000\n" \
"s_waitcnt lgkmcnt(0)\n" \
"s_barrier\n" \
"v_mfma_f32_16x16x32_bf16 a[128:131], a[0:3], a[96:99], 0\n" \
"s_add_u32 m0, s74, 102400\n" \
"v_mfma_f32_16x16x32_bf16 a[128:131], a[4:7], a[100:103], a[128:131]\n" \
"buffer_load_dwordx4 v68, s[40:43], s84 offen offset:0 lds\n" \
"v_mfma_f32_16x16x32_bf16 a[132:135], a[0:3], a[104:107], 0\n" \
"v_mfma_f32_16x16x32_bf16 a[132:135], a[4:7], a[108:111], a[132:135]\n" \
"v_mfma_f32_16x16x32_bf16 a[136:139], a[0:3], a[112:115], 0\n" \
"v_mfma_f32_16x16x32_bf16 a[136:139], a[4:7], a[116:119], a[136:139]\n" \
"buffer_load_dwordx4 v69, s[44:47], s84 offen offset:1024 lds\n" \
"v_mfma_f32_16x16x32_bf16 a[140:143], a[0:3], a[120:123], 0\n" \
"v_mfma_f32_16x16x32_bf16 a[140:143], a[4:7], a[124:127], a[140:143]\n" \
"v_mfma_f32_16x16x32_bf16 a[156:159], a[8:11], a[120:123], 0\n" \
"v_mfma_f32_16x16x32_bf16 a[156:159], a[12:15], a[124:127], a[156:159]\n" \
"buffer_load_dwordx4 v70, s[40:43], s84 offen offset:2048 lds\n" \
"v_mfma_f32_16x16x32_bf16 a[152:155], a[8:11], a[112:115], 0\n" \
"v_mfma_f32_16x16x32_bf16 a[152:155], a[12:15], a[116:119], a[152:155]\n" \
"v_mfma_f32_16x16x32_bf16 a[148:151], a[8:11], a[104:107], 0\n" \
"v_mfma_f32_16x16x32_bf16 a[148:151], a[12:15], a[108:111], a[148:151]\n" \
"buffer_load_dwordx4 v71, s[44:47], s84 offen offset:3072 lds\n" \
"v_mfma_f32_16x16x32_bf16 a[144:147], a[8:11], a[96:99], 0\n" \
"v_mfma_f32_16x16x32_bf16 a[144:147], a[12:15], a[100:103], a[144:147]\n" \
"v_mfma_f32_16x16x32_bf16 a[160:163], a[16:19], a[96:99], 0\n" \
"s_xor_b32 s84, s84, 0x80000\n" \
"v_mfma_f32_16x16x32_bf16 a[160:163], a[20:23], a[100:103], a[160:163]\n" \
"v_mfma_f32_16x16x32_bf16 a[164:167], a[16:19], a[104:107], 0\n" \
"v_mfma_f32_16x16x32_bf16 a[164:167], a[20:23], a[108:111], a[164:167]\n" \
"v_mfma_f32_16x16x32_bf16 a[168:171], a[16:19], a[112:115], 0\n" \
"s_add_u32 m0, s74, 32768\n" \
"v_mfma_f32_16x16x32_bf16 a[168:171], a[20:23], a[116:119], a[168:171]\n" \
"buffer_load_dwordx4 v64, s[36:39], s81 offen offset:0 lds\n" \
"v_mfma_f32_16x16x32_bf16 a[172:175], a[16:19], a[120:123], 0\n" \
"v_mfma_f32_16x16x32_bf16 a[172:175], a[20:23], a[124:127], a[172:175]\n" \
"v_mfma_f32_16x16x32_bf16 a[188:191], a[24:27], a[120:123], 0\n" \
"v_mfma_f32_16x16x32_bf16 a[188:191], a[28:31], a[124:127], a[188:191]\n" \
"buffer_load_dwordx4 v65, s[36:39], s81 offen offset:1024 lds\n" \
"v_mfma_f32_16x16x32_bf16 a[184:187], a[24:27], a[112:115], 0\n" \
"v_mfma_f32_16x16x32_bf16 a[184:187], a[28:31], a[116:119], a[184:187]\n" \
"v_mfma_f32_16x16x32_bf16 a[180:183], a[24:27], a[104:107], 0\n" \
"v_mfma_f32_16x16x32_bf16 a[180:183], a[28:31], a[108:111], a[180:183]\n" \
"buffer_load_dwordx4 v66, s[36:39], s81 offen offset:2048 lds\n" \
"v_mfma_f32_16x16x32_bf16 a[176:179], a[24:27], a[96:99], 0\n" \
"v_mfma_f32_16x16x32_bf16 a[176:179], a[28:31], a[100:103], a[176:179]\n" \
"v_mfma_f32_16x16x32_bf16 a[192:195], a[32:35], a[96:99], 0\n" \
"v_mfma_f32_16x16x32_bf16 a[192:195], a[36:39], a[100:103], a[192:195]\n" \
"buffer_load_dwordx4 v67, s[36:39], s81 offen offset:3072 lds\n" \
"v_mfma_f32_16x16x32_bf16 a[196:199], a[32:35], a[104:107], 0\n" \
"v_mfma_f32_16x16x32_bf16 a[196:199], a[36:39], a[108:111], a[196:199]\n" \
"s_xor_b32 s81, s81, 0x80\n" \
"v_mfma_f32_16x16x32_bf16 a[200:203], a[32:35], a[112:115], 0\n" \
"s_add_u32 m0, s74, 36864\n" \
"v_mfma_f32_16x16x32_bf16 a[200:203], a[36:39], a[116:119], a[200:203]\n" \
"buffer_load_dwordx4 v64, s[36:39], s82 offen offset:0 lds\n" \
"v_mfma_f32_16x16x32_bf16 a[204:207], a[32:35], a[120:123], 0\n" \
"v_mfma_f32_16x16x32_bf16 a[204:207], a[36:39], a[124:127], a[204:207]\n" \
"s_waitcnt vmcnt(13) lgkmcnt(0)\n" \
"s_barrier\n" \
"v_mfma_f32_16x16x32_bf16 a[220:223], a[40:43], a[120:123], 0\n" \
"ds_read_b64_tr_b16 a[64:65], v102 offset:0\n" \
"ds_read_b64_tr_b16 a[66:67], v102 offset:4096\n" \
"v_mfma_f32_16x16x32_bf16 a[220:223], a[44:47], a[124:127], a[220:223]\n" \
"buffer_load_dwordx4 v65, s[36:39], s82 offen offset:1024 lds\n" \
"v_mfma_f32_16x16x32_bf16 a[216:219], a[40:43], a[112:115], 0\n" \
"ds_read_b64_tr_b16 a[68:69], v102 offset:16384\n" \
"ds_read_b64_tr_b16 a[70:71], v102 offset:20480\n" \
"v_mfma_f32_16x16x32_bf16 a[216:219], a[44:47], a[116:119], a[216:219]\n" \
"ds_read_b128 a[0:3], v96 offset:0\n" \
"ds_read_b128 a[4:7], v97 offset:0\n" \
"v_mfma_f32_16x16x32_bf16 a[212:215], a[40:43], a[104:107], 0\n" \
"ds_read_b64_tr_b16 a[72:73], v103 offset:0\n" \
"ds_read_b64_tr_b16 a[74:75], v103 offset:4096\n" \
"v_mfma_f32_16x16x32_bf16 a[212:215], a[44:47], a[108:111], a[212:215]\n" \
"buffer_load_dwordx4 v66, s[36:39], s82 offen offset:2048 lds\n" \
"v_mfma_f32_16x16x32_bf16 a[208:211], a[40:43], a[96:99], 0\n" \
"ds_read_b64_tr_b16 a[76:77], v103 offset:16384\n" \
"ds_read_b64_tr_b16 a[78:79], v103 offset:20480\n" \
"v_mfma_f32_16x16x32_bf16 a[208:211], a[44:47], a[100:103], a[208:211]\n" \
"ds_read_b64_tr_b16 a[80:81], v102 offset:512\n" \
"ds_read_b64_tr_b16 a[82:83], v102 offset:4608\n" \
"v_mfma_f32_16x16x32_bf16 a[224:227], a[48:51], a[96:99], 0\n" \
"ds_read_b64_tr_b16 a[84:85], v102 offset:16896\n" \
"ds_read_b64_tr_b16 a[86:87], v102 offset:20992\n" \
"v_mfma_f32_16x16x32_bf16 a[224:227], a[52:55], a[100:103], a[224:227]\n" \
"buffer_load_dwordx4 v67, s[36:39], s82 offen offset:3072 lds\n" \
"v_mfma_f32_16x16x32_bf16 a[228:231], a[48:51], a[104:107], 0\n" \
"s_mov_b64 s[64:65], s[14:15]\n" \
"s_add_u32 s68, s67, 0x200000\n" \
"ds_read_b64_tr_b16 a[88:89], v103 offset:512\n" \
"ds_read_b64_tr_b16 a[90:91], v103 offset:4608\n" \
"v_mfma_f32_16x16x32_bf16 a[228:231], a[52:55], a[108:111], a[228:231]\n" \
"s_add_u32 s64, s64, s68\n" \
"s_addc_u32 s65, s65, 0\n" \
"ds_read_b64_tr_b16 a[92:93], v103 offset:16896\n" \
"ds_read_b64_tr_b16 a[94:95], v103 offset:20992\n" \
"v_mfma_f32_16x16x32_bf16 a[232:235], a[48:51], a[112:115], 0\n" \
"s_xor_b32 s82, s82, 0x80\n" \
"ds_read_b128 a[8:11], v96 offset:2048\n" \
"ds_read_b128 a[12:15], v97 offset:2048\n" \
"v_mfma_f32_16x16x32_bf16 a[232:235], a[52:55], a[116:119], a[232:235]\n" \
"global_load_dword v238, v234, s[64:65]\n" \
"v_mfma_f32_16x16x32_bf16 a[236:239], a[48:51], a[120:123], 0\n" \
"s_mov_b64 s[64:65], s[16:17]\n" \
"s_add_u32 s68, s67, 0x200000\n" \
"ds_read_b128 a[16:19], v96 offset:4096\n" \
"ds_read_b128 a[20:23], v97 offset:4096\n" \
"v_mfma_f32_16x16x32_bf16 a[236:239], a[52:55], a[124:127], a[236:239]\n" \
"s_add_u32 s64, s64, s68\n" \
"s_addc_u32 s65, s65, 0\n" \
"ds_read_b128 a[24:27], v96 offset:6144\n" \
"ds_read_b128 a[28:31], v97 offset:6144\n" \
"v_mfma_f32_16x16x32_bf16 a[252:255], a[56:59], a[120:123], 0\n" \
"ds_read_b128 a[32:35], v96 offset:8192\n" \
"ds_read_b128 a[36:39], v97 offset:8192\n" \
"v_mfma_f32_16x16x32_bf16 a[252:255], a[60:63], a[124:127], a[252:255]\n" \
"global_load_dword v238, v234, s[64:65]\n" \
"v_mfma_f32_16x16x32_bf16 a[248:251], a[56:59], a[112:115], 0\n" \
"ds_read_b128 a[40:43], v96 offset:10240\n" \
"ds_read_b128 a[44:47], v97 offset:10240\n" \
"v_mfma_f32_16x16x32_bf16 a[248:251], a[60:63], a[116:119], a[248:251]\n" \
"ds_read_b128 a[48:51], v96 offset:12288\n" \
"ds_read_b128 a[52:55], v97 offset:12288\n" \
"v_mfma_f32_16x16x32_bf16 a[244:247], a[56:59], a[104:107], 0\n" \
"v_mfma_f32_16x16x32_bf16 a[244:247], a[60:63], a[108:111], a[244:247]\n" \
"v_mfma_f32_16x16x32_bf16 a[240:243], a[56:59], a[96:99], 0\n" \
"v_mfma_f32_16x16x32_bf16 a[240:243], a[60:63], a[100:103], a[240:243]\n" \
"s_waitcnt lgkmcnt(2)\n" \
"v_mfma_f32_16x16x32_bf16 v[0:3], a[64:67], a[0:3], v[0:3]\n" \
"s_lshl_b32 s79, s61, 12\n" \
"ds_read_b128 a[56:59], v96 offset:14336\n" \
"v_mfma_f32_16x16x32_bf16 v[0:3], a[68:71], a[4:7], v[0:3]\n" \
"s_add_u32 s79, s79, %3\n" \
"v_add_u32 v228, 4096, v84\n" \
"v_add_u32 v229, 3200, v84\n" \
"v_mfma_f32_16x16x32_bf16 v[4:7], a[72:75], a[0:3], v[4:7]\n" \
"s_add_u32 m0, s74, 0\n" \
"ds_read_b128 a[60:63], v97 offset:14336\n" \
"v_mfma_f32_16x16x32_bf16 v[4:7], a[76:79], a[4:7], v[4:7]\n" \
"buffer_load_dwordx4 v68, s[40:43], s83 offen offset:0 lds\n" \
"v_mfma_f32_16x16x32_bf16 v[8:11], a[80:83], a[0:3], v[8:11]\n" \
"s_lshl_b32 s78, s62, 8\n" \
"ds_read_b64_tr_b16 a[96:97], v102 offset:1024\n" \
"v_mfma_f32_16x16x32_bf16 v[8:11], a[84:87], a[4:7], v[8:11]\n" \
"s_add_u32 s89, s78, 30720\n" \
"v_mfma_f32_16x16x32_bf16 v[12:15], a[88:91], a[0:3], v[12:15]\n" \
"s_mov_b64 s[48:49], s[8:9]\n" \
"ds_read_b64_tr_b16 a[98:99], v102 offset:5120\n" \
"v_mfma_f32_16x16x32_bf16 v[12:15], a[92:95], a[4:7], v[12:15]\n" \
"buffer_load_dwordx4 v69, s[44:47], s83 offen offset:1024 lds\n" \
"v_mfma_f32_16x16x32_bf16 v[28:31], a[88:91], a[8:11], v[28:31]\n" \
"s_and_b32 s49, s49, 0xffff\n" \
"ds_read_b64_tr_b16 a[100:101], v102 offset:17408\n" \
"v_mfma_f32_16x16x32_bf16 v[28:31], a[92:95], a[12:15], v[28:31]\n" \
"s_mov_b32 s50, 4194304\n" \
"v_mfma_f32_16x16x32_bf16 v[24:27], a[80:83], a[8:11], v[24:27]\n" \
"s_mov_b32 s51, 0x20000\n" \
"ds_read_b64_tr_b16 a[102:103], v102 offset:21504\n" \
"v_mfma_f32_16x16x32_bf16 v[24:27], a[84:87], a[12:15], v[24:27]\n" \
"buffer_load_dwordx4 v70, s[40:43], s83 offen offset:2048 lds\n" \
"v_mfma_f32_16x16x32_bf16 v[20:23], a[72:75], a[8:11], v[20:23]\n" \
"s_mov_b64 s[52:53], s[12:13]\n" \
"ds_read_b64_tr_b16 a[104:105], v103 offset:1024\n" \
"v_mfma_f32_16x16x32_bf16 v[20:23], a[76:79], a[12:15], v[20:23]\n" \
"s_sub_u32 s52, s52, 4096\n" \
"s_subb_u32 s53, s53, 0\n" \
"v_mfma_f32_16x16x32_bf16 v[16:19], a[64:67], a[8:11], v[16:19]\n" \
"s_and_b32 s53, s53, 0xffff\n" \
"ds_read_b64_tr_b16 a[106:107], v103 offset:5120\n" \
"v_mfma_f32_16x16x32_bf16 v[16:19], a[68:71], a[12:15], v[16:19]\n" \
"buffer_load_dwordx4 v71, s[44:47], s83 offen offset:3072 lds\n" \
"v_mfma_f32_16x16x32_bf16 v[32:35], a[64:67], a[16:19], v[32:35]\n" \
"s_mov_b32 s54, 8392704\n" \
"s_mov_b32 s55, 0x20000\n" \
"ds_read_b64_tr_b16 a[108:109], v103 offset:17408\n" \
"v_mfma_f32_16x16x32_bf16 v[32:35], a[68:71], a[20:23], v[32:35]\n" \
"s_xor_b32 s83, s83, 0x380000\n" \
"v_lshl_add_u32 v224, v85, 11, v86\n" \
"v_add_u32 v225, 15360, v224\n" \
"v_mfma_f32_16x16x32_bf16 v[36:39], a[72:75], a[16:19], v[36:39]\n" \
"s_add_u32 m0, s74, 4096\n" \
"ds_read_b64_tr_b16 a[110:111], v103 offset:21504\n" \
"v_add_u32 v226, 129024, v224\n" \
"v_mfma_f32_16x16x32_bf16 v[36:39], a[76:79], a[20:23], v[36:39]\n" \
"buffer_load_dwordx4 v68, s[40:43], s84 offen offset:0 lds\n" \
"v_mfma_f32_16x16x32_bf16 v[40:43], a[80:83], a[16:19], v[40:43]\n" \
"ds_read_b64_tr_b16 a[112:113], v102 offset:1536\n" \
"v_add_u32 v227, 144384, v224\n" \
"v_mfma_f32_16x16x32_bf16 v[40:43], a[84:87], a[20:23], v[40:43]\n" \
"s_lshl_b32 s70, s61, 15\n" \
"v_mfma_f32_16x16x32_bf16 v[44:47], a[88:91], a[16:19], v[44:47]\n" \
"s_lshl_b32 s75, s63, 18\n" \
"ds_read_b64_tr_b16 a[114:115], v102 offset:5632\n" \
"v_mfma_f32_16x16x32_bf16 v[44:47], a[92:95], a[20:23], v[44:47]\n" \
"buffer_load_dwordx4 v69, s[44:47], s84 offen offset:1024 lds\n" \
"v_mfma_f32_16x16x32_bf16 v[60:63], a[88:91], a[24:27], v[60:63]\n" \
"s_sub_u32 s77, 3932160, s75\n" \
"ds_read_b64_tr_b16 a[116:117], v102 offset:17920\n" \
"v_mfma_f32_16x16x32_bf16 v[60:63], a[92:95], a[28:31], v[60:63]\n" \
"s_lshr_b32 s71, %1, 6\n" \
"v_mfma_f32_16x16x32_bf16 v[56:59], a[80:83], a[24:27], v[56:59]\n" \
"s_xor_b32 s71, s71, s63\n" \
"ds_read_b64_tr_b16 a[118:119], v102 offset:22016\n" \
"v_mfma_f32_16x16x32_bf16 v[56:59], a[84:87], a[28:31], v[56:59]\n" \
"buffer_load_dwordx4 v70, s[40:43], s84 offen offset:2048 lds\n" \
"v_mfma_f32_16x16x32_bf16 v[52:55], a[72:75], a[24:27], v[52:55]\n" \
"s_and_b32 s71, s71, 0\n" \
"ds_read_b64_tr_b16 a[120:121], v103 offset:1536\n" \
"v_mfma_f32_16x16x32_bf16 v[52:55], a[76:79], a[28:31], v[52:55]\n" \
"s_lshl_b32 s71, s71, 7\n" \
"v_mfma_f32_16x16x32_bf16 v[48:51], a[64:67], a[24:27], v[48:51]\n" \
"s_add_u32 s70, s70, s71\n" \
"ds_read_b64_tr_b16 a[122:123], v103 offset:5632\n" \
"v_mfma_f32_16x16x32_bf16 v[48:51], a[68:71], a[28:31], v[48:51]\n" \
"buffer_load_dwordx4 v71, s[44:47], s84 offen offset:3072 lds\n" \
"v_mfma_f32_16x16x32_bf16 v[144:147], a[64:67], a[32:35], v[144:147]\n" \
"s_add_u32 s75, s75, s70\n" \
"ds_read_b64_tr_b16 a[124:125], v103 offset:17920\n" \
"v_mfma_f32_16x16x32_bf16 v[144:147], a[68:71], a[36:39], v[144:147]\n" \
"s_add_u32 s77, s77, s70\n" \
"v_mfma_f32_16x16x32_bf16 v[148:151], a[72:75], a[32:35], v[148:151]\n" \
"s_xor_b32 s84, s84, 0x380000\n" \
"ds_read_b64_tr_b16 a[126:127], v103 offset:22016\n" \
"v_mfma_f32_16x16x32_bf16 v[148:151], a[76:79], a[36:39], v[148:151]\n" \
"s_lshl_b32 s71, s71, 12\n" \
"v_mfma_f32_16x16x32_bf16 v[152:155], a[80:83], a[32:35], v[152:155]\n" \
"s_add_u32 s78, s78, s71\n" \
"v_mfma_f32_16x16x32_bf16 v[152:155], a[84:87], a[36:39], v[152:155]\n" \
"s_add_u32 s89, s89, s71\n" \
"v_lshlrev_b32 v242, 2, v78\n" \
"v_and_b32 v236, 0xf0, v234\n" \
"v_mfma_f32_16x16x32_bf16 v[156:159], a[88:91], a[32:35], v[156:159]\n" \
"v_lshl_add_u32 v236, v87, 9, v236\n" \
"v_mfma_f32_16x16x32_bf16 v[156:159], a[92:95], a[36:39], v[156:159]\n" \
"s_add_u32 s71, s74, 0x10000\n" \
"v_add_u32 v230, s71, v88\n" \
"v_xor_b32 v231, 64, v230\n" \
"v_mfma_f32_16x16x32_bf16 v[188:191], a[88:91], a[40:43], v[188:191]\n" \
"v_add_u32 v250, 0x10000, v230\n" \
"v_add_u32 v251, 0x10000, v231\n" \
"v_mfma_f32_16x16x32_bf16 v[188:191], a[92:95], a[44:47], v[188:191]\n" \
"v_lshl_add_u32 v82, v90, 12, v89\n" \
"v_add_u32 v232, %3, v82\n" \
"v_mfma_f32_16x16x32_bf16 v[184:187], a[80:83], a[40:43], v[184:187]\n" \
"v_xor_b32 v233, 32, v232\n" \
"v_mfma_f32_16x16x32_bf16 v[184:187], a[84:87], a[44:47], v[184:187]\n" \
"s_mul_i32 s87, s62, 557824\n" \
"v_mfma_f32_16x16x32_bf16 v[180:183], a[72:75], a[40:43], v[180:183]\n" \
"s_lshr_b32 s70, s61, 1\n" \
"v_mfma_f32_16x16x32_bf16 v[180:183], a[76:79], a[44:47], v[180:183]\n" \
"s_mul_i32 s70, s70, 17408\n" \
"v_mfma_f32_16x16x32_bf16 v[176:179], a[64:67], a[40:43], v[176:179]\n" \
"Lpg585_%=:\n" \
"Lst0x0_%=:\n" \
"s_cbranch_execz Lpg2766_%=\n" \
"s_add_u32 s87, s87, s70\n" \
"v_mfma_f32_16x16x32_bf16 v[176:179], a[68:71], a[44:47], v[176:179]\n" \
"s_waitcnt lgkmcnt(0)\n" \
"s_and_b32 s70, s61, 1\n" \
"v_mfma_f32_16x16x32_bf16 v[192:195], a[64:67], a[48:51], v[192:195]\n" \
"s_lshl_b32 s70, s70, 12\n" \
"v_mfma_f32_16x16x32_bf16 v[192:195], a[68:71], a[52:55], v[192:195]\n" \
"s_add_u32 s87, s87, s70\n" \
"v_mfma_f32_16x16x32_bf16 v[196:199], a[72:75], a[48:51], v[196:199]\n" \
"s_mul_i32 s70, s63, 34816\n" \
"v_mfma_f32_16x16x32_bf16 v[196:199], a[76:79], a[52:55], v[196:199]\n" \
"s_add_u32 s94, s87, 522240\n" \
"v_mfma_f32_16x16x32_bf16 v[200:203], a[80:83], a[48:51], v[200:203]\n" \
"s_sub_u32 s94, s94, s70\n" \
"v_mfma_f32_16x16x32_bf16 v[200:203], a[84:87], a[52:55], v[200:203]\n" \
"s_add_u32 s87, s87, s70\n" \
"v_mfma_f32_16x16x32_bf16 v[204:207], a[88:91], a[48:51], v[204:207]\n" \
"s_lshl_b32 s90, s62, 7\n" \
"v_mfma_f32_16x16x32_bf16 v[204:207], a[92:95], a[52:55], v[204:207]\n" \
"s_add_u32 s95, s90, 65597440\n" \
"v_mfma_f32_16x16x32_bf16 v[220:223], a[88:91], a[56:59], v[220:223]\n" \
"s_add_u32 s90, s90, 65585152\n" \
"v_mfma_f32_16x16x32_bf16 v[220:223], a[92:95], a[60:63], v[220:223]\n" \
"s_lshl_b32 s92, 0xff, s63\n" \
"v_mfma_f32_16x16x32_bf16 v[216:219], a[80:83], a[56:59], v[216:219]\n" \
"s_and_b32 s92, s92, 0xff\n" \
"v_mfma_f32_16x16x32_bf16 v[216:219], a[84:87], a[60:63], v[216:219]\n" \
"s_mov_b32 s76, 0x3e0293ee\n" \
"v_mfma_f32_16x16x32_bf16 v[212:215], a[72:75], a[56:59], v[212:215]\n" \
"s_mov_b32 s31, 0x3e800000\n" \
"v_mfma_f32_16x16x32_bf16 v[212:215], a[76:79], a[60:63], v[212:215]\n" \
"v_mfma_f32_16x16x32_bf16 v[208:211], a[64:67], a[56:59], v[208:211]\n" \
"v_mfma_f32_16x16x32_bf16 v[208:211], a[68:71], a[60:63], v[208:211]\n" \
"s_waitcnt vmcnt(10) lgkmcnt(0)\n" \
"s_barrier\n" \
"v_readfirstlane_b32 s60, v240\n" \
"s_add_u32 s60, s60, 0x9e3779b9\n" \
"v_mfma_f32_16x16x32_bf16 a[128:131], a[0:3], a[96:99], a[128:131]\n" \
"s_add_u32 m0, s74, 131072\n" \
"ds_read_b64_tr_b16 a[64:65], v98 offset:32768\n" \
"v_mfma_f32_16x16x32_bf16 a[128:131], a[4:7], a[100:103], a[128:131]\n" \
"buffer_load_dwordx4 v64, s[36:39], s81 offen offset:0 lds\n" \
"v_mfma_f32_16x16x32_bf16 a[132:135], a[0:3], a[104:107], a[132:135]\n" \
"ds_read_b64_tr_b16 a[66:67], v98 offset:36864\n" \
"v_mfma_f32_16x16x32_bf16 a[132:135], a[4:7], a[108:111], a[132:135]\n" \
"v_mfma_f32_16x16x32_bf16 a[136:139], a[0:3], a[112:115], a[136:139]\n" \
"ds_read_b64_tr_b16 a[68:69], v98 offset:49152\n" \
"v_mfma_f32_16x16x32_bf16 a[136:139], a[4:7], a[116:119], a[136:139]\n" \
"buffer_load_dwordx4 v65, s[36:39], s81 offen offset:1024 lds\n" \
"v_mfma_f32_16x16x32_bf16 a[140:143], a[0:3], a[120:123], a[140:143]\n" \
"ds_read_b64_tr_b16 a[70:71], v98 offset:53248\n" \
"v_mfma_f32_16x16x32_bf16 a[140:143], a[4:7], a[124:127], a[140:143]\n" \
"v_mfma_f32_16x16x32_bf16 a[156:159], a[8:11], a[120:123], a[156:159]\n" \
"ds_read_b128 a[0:3], v72 offset:32768\n" \
"v_mfma_f32_16x16x32_bf16 a[156:159], a[12:15], a[124:127], a[156:159]\n" \
"buffer_load_dwordx4 v66, s[36:39], s81 offen offset:2048 lds\n" \
"v_mfma_f32_16x16x32_bf16 a[152:155], a[8:11], a[112:115], a[152:155]\n" \
"ds_read_b64_tr_b16 a[72:73], v99 offset:32768\n" \
"v_mfma_f32_16x16x32_bf16 a[152:155], a[12:15], a[116:119], a[152:155]\n" \
"v_mfma_f32_16x16x32_bf16 a[148:151], a[8:11], a[104:107], a[148:151]\n" \
"ds_read_b128 a[4:7], v73 offset:32768\n" \
"v_mfma_f32_16x16x32_bf16 a[148:151], a[12:15], a[108:111], a[148:151]\n" \
"buffer_load_dwordx4 v67, s[36:39], s81 offen offset:3072 lds\n" \
"v_mfma_f32_16x16x32_bf16 a[144:147], a[8:11], a[96:99], a[144:147]\n" \
"ds_read_b64_tr_b16 a[74:75], v99 offset:36864\n" \
"v_mfma_f32_16x16x32_bf16 a[144:147], a[12:15], a[100:103], a[144:147]\n" \
"s_xor_b32 s81, s81, 0x380\n" \
"v_mfma_f32_16x16x32_bf16 a[160:163], a[16:19], a[96:99], a[160:163]\n" \
"s_add_u32 m0, s74, 135168\n" \
"ds_read_b128 a[8:11], v72 offset:34816\n" \
"v_mfma_f32_16x16x32_bf16 a[160:163], a[20:23], a[100:103], a[160:163]\n" \
"buffer_load_dwordx4 v64, s[36:39], s82 offen offset:0 lds\n" \
"v_mfma_f32_16x16x32_bf16 a[164:167], a[16:19], a[104:107], a[164:167]\n" \
"ds_read_b64_tr_b16 a[76:77], v99 offset:49152\n" \
"v_mfma_f32_16x16x32_bf16 a[164:167], a[20:23], a[108:111], a[164:167]\n" \
"v_mfma_f32_16x16x32_bf16 a[168:171], a[16:19], a[112:115], a[168:171]\n" \
"ds_read_b128 a[12:15], v73 offset:34816\n" \
"v_mfma_f32_16x16x32_bf16 a[168:171], a[20:23], a[116:119], a[168:171]\n" \
"buffer_load_dwordx4 v65, s[36:39], s82 offen offset:1024 lds\n" \
"v_mfma_f32_16x16x32_bf16 a[172:175], a[16:19], a[120:123], a[172:175]\n" \
"ds_read_b64_tr_b16 a[78:79], v99 offset:53248\n" \
"v_mfma_f32_16x16x32_bf16 a[172:175], a[20:23], a[124:127], a[172:175]\n" \
"v_mfma_f32_16x16x32_bf16 a[188:191], a[24:27], a[120:123], a[188:191]\n" \
"ds_read_b128 a[16:19], v72 offset:36864\n" \
"v_mfma_f32_16x16x32_bf16 a[188:191], a[28:31], a[124:127], a[188:191]\n" \
"buffer_load_dwordx4 v66, s[36:39], s82 offen offset:2048 lds\n" \
"v_mfma_f32_16x16x32_bf16 a[184:187], a[24:27], a[112:115], a[184:187]\n" \
"ds_read_b64_tr_b16 a[80:81], v98 offset:33280\n" \
"v_mfma_f32_16x16x32_bf16 a[184:187], a[28:31], a[116:119], a[184:187]\n" \
"v_mfma_f32_16x16x32_bf16 a[180:183], a[24:27], a[104:107], a[180:183]\n" \
"ds_read_b128 a[20:23], v73 offset:36864\n" \
"v_mfma_f32_16x16x32_bf16 a[180:183], a[28:31], a[108:111], a[180:183]\n" \
"buffer_load_dwordx4 v67, s[36:39], s82 offen offset:3072 lds\n" \
"v_mfma_f32_16x16x32_bf16 a[176:179], a[24:27], a[96:99], a[176:179]\n" \
"s_mov_b64 s[64:65], s[14:15]\n" \
"s_add_u32 s68, s67, 0x300000\n" \
"ds_read_b64_tr_b16 a[82:83], v98 offset:37376\n" \
"v_mfma_f32_16x16x32_bf16 a[176:179], a[28:31], a[100:103], a[176:179]\n" \
"s_add_u32 s64, s64, s68\n" \
"s_addc_u32 s65, s65, 0\n" \
"v_mfma_f32_16x16x32_bf16 a[192:195], a[32:35], a[96:99], a[192:195]\n" \
"s_xor_b32 s82, s82, 0x380\n" \
"ds_read_b128 a[24:27], v72 offset:38912\n" \
"v_mfma_f32_16x16x32_bf16 a[192:195], a[36:39], a[100:103], a[192:195]\n" \
"global_load_dword v238, v234, s[64:65]\n" \
"v_mfma_f32_16x16x32_bf16 a[196:199], a[32:35], a[104:107], a[196:199]\n" \
"s_mov_b64 s[64:65], s[16:17]\n" \
"s_add_u32 s68, s67, 0x300000\n" \
"ds_read_b64_tr_b16 a[84:85], v98 offset:49664\n" \
"v_mfma_f32_16x16x32_bf16 a[196:199], a[36:39], a[108:111], a[196:199]\n" \
"s_add_u32 s64, s64, s68\n" \
"s_addc_u32 s65, s65, 0\n" \
"v_mfma_f32_16x16x32_bf16 a[200:203], a[32:35], a[112:115], a[200:203]\n" \
"ds_read_b128 a[28:31], v73 offset:38912\n" \
"v_mfma_f32_16x16x32_bf16 a[200:203], a[36:39], a[116:119], a[200:203]\n" \
"global_load_dword v238, v234, s[64:65]\n" \
"v_mfma_f32_16x16x32_bf16 a[204:207], a[32:35], a[120:123], a[204:207]\n" \
"ds_read_b64_tr_b16 a[86:87], v98 offset:53760\n" \
"v_mfma_f32_16x16x32_bf16 a[204:207], a[36:39], a[124:127], a[204:207]\n" \
"v_mfma_f32_16x16x32_bf16 a[220:223], a[40:43], a[120:123], a[220:223]\n" \
"ds_read_b128 a[32:35], v72 offset:40960\n" \
"v_mfma_f32_16x16x32_bf16 a[220:223], a[44:47], a[124:127], a[220:223]\n" \
"v_mfma_f32_16x16x32_bf16 a[216:219], a[40:43], a[112:115], a[216:219]\n" \
"ds_read_b64_tr_b16 a[88:89], v99 offset:33280\n" \
"v_mfma_f32_16x16x32_bf16 a[216:219], a[44:47], a[116:119], a[216:219]\n" \
"v_mfma_f32_16x16x32_bf16 a[212:215], a[40:43], a[104:107], a[212:215]\n" \
"ds_read_b128 a[36:39], v73 offset:40960\n" \
"v_mfma_f32_16x16x32_bf16 a[212:215], a[44:47], a[108:111], a[212:215]\n" \
"v_mfma_f32_16x16x32_bf16 a[208:211], a[40:43], a[96:99], a[208:211]\n" \
"ds_read_b64_tr_b16 a[90:91], v99 offset:37376\n" \
"v_mfma_f32_16x16x32_bf16 a[208:211], a[44:47], a[100:103], a[208:211]\n" \
"v_mfma_f32_16x16x32_bf16 a[224:227], a[48:51], a[96:99], a[224:227]\n" \
"ds_read_b128 a[40:43], v72 offset:43008\n" \
"v_mfma_f32_16x16x32_bf16 a[224:227], a[52:55], a[100:103], a[224:227]\n" \
"v_mfma_f32_16x16x32_bf16 a[228:231], a[48:51], a[104:107], a[228:231]\n" \
"ds_read_b64_tr_b16 a[92:93], v99 offset:49664\n" \
"v_mfma_f32_16x16x32_bf16 a[228:231], a[52:55], a[108:111], a[228:231]\n" \
"v_mfma_f32_16x16x32_bf16 a[232:235], a[48:51], a[112:115], a[232:235]\n" \
"ds_read_b128 a[44:47], v73 offset:43008\n" \
"v_mfma_f32_16x16x32_bf16 a[232:235], a[52:55], a[116:119], a[232:235]\n" \
"v_mfma_f32_16x16x32_bf16 a[236:239], a[48:51], a[120:123], a[236:239]\n" \
"ds_read_b64_tr_b16 a[94:95], v99 offset:53760\n" \
"v_mfma_f32_16x16x32_bf16 a[236:239], a[52:55], a[124:127], a[236:239]\n" \
"v_mfma_f32_16x16x32_bf16 a[252:255], a[56:59], a[120:123], a[252:255]\n" \
"ds_read_b128 a[48:51], v72 offset:45056\n" \
"v_mfma_f32_16x16x32_bf16 a[252:255], a[60:63], a[124:127], a[252:255]\n" \
"v_mfma_f32_16x16x32_bf16 a[248:251], a[56:59], a[112:115], a[248:251]\n" \
"v_mfma_f32_16x16x32_bf16 a[248:251], a[60:63], a[116:119], a[248:251]\n" \
"v_mfma_f32_16x16x32_bf16 a[244:247], a[56:59], a[104:107], a[244:247]\n" \
"ds_read_b128 a[52:55], v73 offset:45056\n" \
"v_mfma_f32_16x16x32_bf16 a[244:247], a[60:63], a[108:111], a[244:247]\n" \
"v_mfma_f32_16x16x32_bf16 a[240:243], a[56:59], a[96:99], a[240:243]\n" \
"v_mfma_f32_16x16x32_bf16 a[240:243], a[60:63], a[100:103], a[240:243]\n" \
"s_waitcnt lgkmcnt(1)\n" \
"v_mfma_f32_16x16x32_bf16 v[0:3], a[64:67], a[0:3], v[0:3]\n" \
"ds_read_b128 a[56:59], v72 offset:47104\n" \
"v_lshrrev_b32 v104, 4, v234\n" \
"v_mfma_f32_16x16x32_bf16 v[0:3], a[68:71], a[4:7], v[0:3]\n" \
"v_bfe_u32 v105, v104, 4, 1\n" \
"v_bfe_u32 v106, v104, 2, 2\n" \
"v_mfma_f32_16x16x32_bf16 v[4:7], a[72:75], a[0:3], v[4:7]\n" \
"s_add_u32 m0, s74, 65536\n" \
"ds_read_b128 a[60:63], v73 offset:47104\n" \
"v_lshl_add_u32 v106, v105, 3, v106\n" \
"v_mfma_f32_16x16x32_bf16 v[4:7], a[76:79], a[4:7], v[4:7]\n" \
"buffer_load_dwordx4 v68, s[40:43], s83 offen offset:0 lds\n" \
"v_mfma_f32_16x16x32_bf16 v[8:11], a[80:83], a[0:3], v[8:11]\n" \
"s_lshl_b32 s71, s61, 4\n" \
"ds_read_b64_tr_b16 a[96:97], v98 offset:33792\n" \
"v_add_u32 v106, s71, v106\n" \
"v_mfma_f32_16x16x32_bf16 v[8:11], a[84:87], a[4:7], v[8:11]\n" \
"v_mul_u32_u24 v106, 6144, v106\n" \
"v_lshlrev_b32 v105, 1, v105\n" \
"v_mfma_f32_16x16x32_bf16 v[12:15], a[88:91], a[0:3], v[12:15]\n" \
"ds_read_b64_tr_b16 a[98:99], v98 offset:37888\n" \
"v_and_b32 v107, 3, v104\n" \
"v_mfma_f32_16x16x32_bf16 v[12:15], a[92:95], a[4:7], v[12:15]\n" \
"buffer_load_dwordx4 v69, s[44:47], s83 offen offset:1024 lds\n" \
"v_mfma_f32_16x16x32_bf16 v[28:31], a[88:91], a[8:11], v[28:31]\n" \
"ds_read_b64_tr_b16 a[100:101], v98 offset:50176\n" \
"v_xor_b32 v105, v105, v107\n" \
"v_mfma_f32_16x16x32_bf16 v[28:31], a[92:95], a[12:15], v[28:31]\n" \
"v_lshl_add_u32 v106, v105, 4, v106\n" \
"v_lshrrev_b32 v105, 5, v104\n" \
"v_mfma_f32_16x16x32_bf16 v[24:27], a[80:83], a[8:11], v[24:27]\n" \
"ds_read_b64_tr_b16 a[102:103], v98 offset:54272\n" \
"v_lshl_add_u32 v244, v105, 6, v106\n" \
"v_mfma_f32_16x16x32_bf16 v[24:27], a[84:87], a[12:15], v[24:27]\n" \
"buffer_load_dwordx4 v70, s[40:43], s83 offen offset:2048 lds\n" \
"v_mfma_f32_16x16x32_bf16 v[20:23], a[72:75], a[8:11], v[20:23]\n" \
"ds_read_b64_tr_b16 a[104:105], v99 offset:33792\n" \
"v_lshrrev_b32 v107, 3, v104\n" \
"v_mfma_f32_16x16x32_bf16 v[20:23], a[76:79], a[12:15], v[20:23]\n" \
"v_and_b32 v108, 7, v104\n" \
"v_xor_b32 v109, v108, v107\n" \
"v_mfma_f32_16x16x32_bf16 v[16:19], a[64:67], a[8:11], v[16:19]\n" \
"ds_read_b64_tr_b16 a[106:107], v99 offset:37888\n" \
"v_lshlrev_b32 v109, 4, v109\n" \
"v_mfma_f32_16x16x32_bf16 v[16:19], a[68:71], a[12:15], v[16:19]\n" \
"buffer_load_dwordx4 v71, s[44:47], s83 offen offset:3072 lds\n" \
"v_mfma_f32_16x16x32_bf16 v[32:35], a[64:67], a[16:19], v[32:35]\n" \
"ds_read_b64_tr_b16 a[108:109], v99 offset:50176\n" \
"v_lshl_add_u32 v245, v107, 12, v109\n" \
"v_mfma_f32_16x16x32_bf16 v[32:35], a[68:71], a[20:23], v[32:35]\n" \
"s_xor_b32 s83, s83, 0x80000\n" \
"v_mfma_f32_16x16x32_bf16 v[36:39], a[72:75], a[16:19], v[36:39]\n" \
"s_add_u32 m0, s74, 69632\n" \
"ds_read_b64_tr_b16 a[110:111], v99 offset:54272\n" \
"v_mfma_f32_16x16x32_bf16 v[36:39], a[76:79], a[20:23], v[36:39]\n" \
"buffer_load_dwordx4 v68, s[40:43], s84 offen offset:0 lds\n" \
"v_mfma_f32_16x16x32_bf16 v[40:43], a[80:83], a[16:19], v[40:43]\n" \
"s_and_b32 s70, s61, 1\n" \
"ds_read_b64_tr_b16 a[112:113], v98 offset:34304\n" \
"v_mfma_f32_16x16x32_bf16 v[40:43], a[84:87], a[20:23], v[40:43]\n" \
"s_lshl_b32 s71, s70, 2\n" \
"v_add_u32 v107, s71, v107\n" \
"v_xor_b32 v109, v108, v107\n" \
"v_mfma_f32_16x16x32_bf16 v[44:47], a[88:91], a[16:19], v[44:47]\n" \
"ds_read_b64_tr_b16 a[114:115], v98 offset:38400\n" \
"v_lshlrev_b32 v109, 4, v109\n" \
"v_mfma_f32_16x16x32_bf16 v[44:47], a[92:95], a[20:23], v[44:47]\n" \
"buffer_load_dwordx4 v69, s[44:47], s84 offen offset:1024 lds\n" \
"v_mfma_f32_16x16x32_bf16 v[60:63], a[88:91], a[24:27], v[60:63]\n" \
"ds_read_b64_tr_b16 a[116:117], v98 offset:50688\n" \
"v_lshl_add_u32 v246, v107, 12, v109\n" \
"v_mfma_f32_16x16x32_bf16 v[60:63], a[92:95], a[28:31], v[60:63]\n" \
"s_lshr_b32 s71, s61, 1\n" \
"v_mfma_f32_16x16x32_bf16 v[56:59], a[80:83], a[24:27], v[56:59]\n" \
"s_lshl_b32 s70, s71, 11\n" \
"ds_read_b64_tr_b16 a[118:119], v98 offset:54784\n" \
"v_lshl_add_u32 v246, v105, 30, v246\n" \
"v_mfma_f32_16x16x32_bf16 v[56:59], a[84:87], a[28:31], v[56:59]\n" \
"buffer_load_dwordx4 v70, s[40:43], s84 offen offset:2048 lds\n" \
"v_mfma_f32_16x16x32_bf16 v[52:55], a[72:75], a[24:27], v[52:55]\n" \
"ds_read_b64_tr_b16 a[120:121], v99 offset:34304\n" \
"v_add_u32 v246, s70, v246\n" \
"v_mfma_f32_16x16x32_bf16 v[52:55], a[76:79], a[28:31], v[52:55]\n" \
"v_lshrrev_b32 v107, 4, v104\n" \
"v_xor_b32 v107, v107, v108\n" \
"v_mfma_f32_16x16x32_bf16 v[48:51], a[64:67], a[24:27], v[48:51]\n" \
"ds_read_b64_tr_b16 a[122:123], v99 offset:38400\n" \
"v_and_b32 v109, 15, v104\n" \
"v_mfma_f32_16x16x32_bf16 v[48:51], a[68:71], a[28:31], v[48:51]\n" \
"buffer_load_dwordx4 v71, s[44:47], s84 offen offset:3072 lds\n" \
"v_mfma_f32_16x16x32_bf16 v[144:147], a[64:67], a[32:35], v[144:147]\n" \
"ds_read_b64_tr_b16 a[124:125], v99 offset:50688\n" \
"v_lshlrev_b32 v106, 7, v109\n" \
"v_mfma_f32_16x16x32_bf16 v[144:147], a[68:71], a[36:39], v[144:147]\n" \
"v_lshl_add_u32 v106, v107, 4, v106\n" \
"v_mfma_f32_16x16x32_bf16 v[148:151], a[72:75], a[32:35], v[148:151]\n" \
"s_xor_b32 s84, s84, 0x80000\n" \
"ds_read_b64_tr_b16 a[126:127], v99 offset:54784\n" \
"v_mfma_f32_16x16x32_bf16 v[148:151], a[76:79], a[36:39], v[148:151]\n" \
"s_add_u32 s70, s70, %3\n" \
"v_add_u32 v247, s70, v106\n" \
"v_lshrrev_b32 v109, 2, v109\n" \
"v_mfma_f32_16x16x32_bf16 v[152:155], a[80:83], a[32:35], v[152:155]\n" \
"v_lshl_add_u32 v109, v109, 9, v106\n" \
"v_mfma_f32_16x16x32_bf16 v[152:155], a[84:87], a[36:39], v[152:155]\n" \
"s_add_u32 s70, %3, 16384\n" \
"v_add_u32 v248, s70, v109\n" \
"v_and_b32 v106, 3, v104\n" \
"v_mfma_f32_16x16x32_bf16 v[156:159], a[88:91], a[32:35], v[156:159]\n" \
"v_lshlrev_b32 v106, 3, v106\n" \
"v_bfe_u32 v107, v104, 2, 2\n" \
"v_mfma_f32_16x16x32_bf16 v[156:159], a[92:95], a[36:39], v[156:159]\n" \
"v_lshl_add_u32 v106, v107, 6, v106\n" \
"v_bfe_u32 v107, v104, 4, 1\n" \
"v_mfma_f32_16x16x32_bf16 v[188:191], a[88:91], a[40:43], v[188:191]\n" \
"v_mul_u32_u24 v107, 288, v107\n" \
"v_mul_u32_u24 v108, 6144, v105\n" \
"v_mfma_f32_16x16x32_bf16 v[188:191], a[92:95], a[44:47], v[188:191]\n" \
"v_add3_u32 v106, v106, v107, v108\n" \
"v_mfma_f32_16x16x32_bf16 v[184:187], a[80:83], a[40:43], v[184:187]\n" \
"s_and_b32 s70, s61, 1\n" \
"v_mfma_f32_16x16x32_bf16 v[184:187], a[84:87], a[44:47], v[184:187]\n" \
"s_lshl_b32 s70, s70, 9\n" \
"v_mfma_f32_16x16x32_bf16 v[180:183], a[72:75], a[40:43], v[180:183]\n" \
"s_add_u32 s70, s70, %3\n" \
"v_add_u32 v249, s70, v106\n" \
"v_mfma_f32_16x16x32_bf16 v[180:183], a[76:79], a[44:47], v[180:183]\n" \
"s_lshl_b32 s70, s71, 5\n" \
"v_xor_b32 v249, s70, v249\n" \
"v_mfma_f32_16x16x32_bf16 v[176:179], a[64:67], a[40:43], v[176:179]\n" \
"v_mfma_f32_16x16x32_bf16 v[176:179], a[68:71], a[44:47], v[176:179]\n" \
"s_waitcnt lgkmcnt(0)\n" \
"v_mfma_f32_16x16x32_bf16 v[192:195], a[64:67], a[48:51], v[192:195]\n" \
"v_mfma_f32_16x16x32_bf16 v[192:195], a[68:71], a[52:55], v[192:195]\n" \
"v_mfma_f32_16x16x32_bf16 v[196:199], a[72:75], a[48:51], v[196:199]\n" \
"v_mfma_f32_16x16x32_bf16 v[196:199], a[76:79], a[52:55], v[196:199]\n" \
"v_mfma_f32_16x16x32_bf16 v[200:203], a[80:83], a[48:51], v[200:203]\n" \
"v_mfma_f32_16x16x32_bf16 v[200:203], a[84:87], a[52:55], v[200:203]\n" \
"v_mfma_f32_16x16x32_bf16 v[204:207], a[88:91], a[48:51], v[204:207]\n" \
"v_mfma_f32_16x16x32_bf16 v[204:207], a[92:95], a[52:55], v[204:207]\n" \
"v_mfma_f32_16x16x32_bf16 v[220:223], a[88:91], a[56:59], v[220:223]\n" \
"v_mfma_f32_16x16x32_bf16 v[220:223], a[92:95], a[60:63], v[220:223]\n" \
"v_mfma_f32_16x16x32_bf16 v[216:219], a[80:83], a[56:59], v[216:219]\n" \
"v_mfma_f32_16x16x32_bf16 v[216:219], a[84:87], a[60:63], v[216:219]\n" \
"v_mfma_f32_16x16x32_bf16 v[212:215], a[72:75], a[56:59], v[212:215]\n" \
"v_mfma_f32_16x16x32_bf16 v[212:215], a[76:79], a[60:63], v[212:215]\n" \
"v_mfma_f32_16x16x32_bf16 v[208:211], a[64:67], a[56:59], v[208:211]\n" \
"v_mfma_f32_16x16x32_bf16 v[208:211], a[68:71], a[60:63], v[208:211]\n" \
"s_waitcnt vmcnt(10) lgkmcnt(0)\n" \
"s_barrier\n" \
"v_mfma_f32_16x16x32_bf16 a[128:131], a[0:3], a[96:99], a[128:131]\n" \
"s_add_u32 m0, s74, 98304\n" \
"ds_read_b64_tr_b16 a[64:65], v74 offset:0\n" \
"v_mfma_f32_16x16x32_bf16 a[128:131], a[4:7], a[100:103], a[128:131]\n" \
"buffer_load_dwordx4 v64, s[36:39], s81 offen offset:0 lds\n" \
"v_mfma_f32_16x16x32_bf16 a[132:135], a[0:3], a[104:107], a[132:135]\n" \
"ds_read_b64_tr_b16 a[66:67], v74 offset:4096\n" \
"v_mfma_f32_16x16x32_bf16 a[132:135], a[4:7], a[108:111], a[132:135]\n" \
"v_mfma_f32_16x16x32_bf16 a[136:139], a[0:3], a[112:115], a[136:139]\n" \
"ds_read_b64_tr_b16 a[68:69], v74 offset:16384\n" \
"v_mfma_f32_16x16x32_bf16 a[136:139], a[4:7], a[116:119], a[136:139]\n" \
"buffer_load_dwordx4 v65, s[36:39], s81 offen offset:1024 lds\n" \
"v_mfma_f32_16x16x32_bf16 a[140:143], a[0:3], a[120:123], a[140:143]\n" \
"ds_read_b64_tr_b16 a[70:71], v74 offset:20480\n" \
"v_mfma_f32_16x16x32_bf16 a[140:143], a[4:7], a[124:127], a[140:143]\n" \
"v_mfma_f32_16x16x32_bf16 a[156:159], a[8:11], a[120:123], a[156:159]\n" \
"ds_read_b128 a[0:3], v100 offset:0\n" \
"v_mfma_f32_16x16x32_bf16 a[156:159], a[12:15], a[124:127], a[156:159]\n" \
"buffer_load_dwordx4 v66, s[36:39], s81 offen offset:2048 lds\n" \
"v_mfma_f32_16x16x32_bf16 a[152:155], a[8:11], a[112:115], a[152:155]\n" \
"ds_read_b64_tr_b16 a[72:73], v75 offset:0\n" \
"v_mfma_f32_16x16x32_bf16 a[152:155], a[12:15], a[116:119], a[152:155]\n" \
"v_mfma_f32_16x16x32_bf16 a[148:151], a[8:11], a[104:107], a[148:151]\n" \
"ds_read_b128 a[4:7], v101 offset:0\n" \
"v_mfma_f32_16x16x32_bf16 a[148:151], a[12:15], a[108:111], a[148:151]\n" \
"buffer_load_dwordx4 v67, s[36:39], s81 offen offset:3072 lds\n" \
"v_mfma_f32_16x16x32_bf16 a[144:147], a[8:11], a[96:99], a[144:147]\n" \
"ds_read_b64_tr_b16 a[74:75], v75 offset:4096\n" \
"v_mfma_f32_16x16x32_bf16 a[144:147], a[12:15], a[100:103], a[144:147]\n" \
"s_xor_b32 s81, s81, 0x80\n" \
"v_mfma_f32_16x16x32_bf16 a[160:163], a[16:19], a[96:99], a[160:163]\n" \
"s_add_u32 m0, s74, 102400\n" \
"ds_read_b128 a[8:11], v100 offset:2048\n" \
"v_mfma_f32_16x16x32_bf16 a[160:163], a[20:23], a[100:103], a[160:163]\n" \
"buffer_load_dwordx4 v64, s[36:39], s82 offen offset:0 lds\n" \
"v_mfma_f32_16x16x32_bf16 a[164:167], a[16:19], a[104:107], a[164:167]\n" \
"ds_read_b64_tr_b16 a[76:77], v75 offset:16384\n" \
"v_mfma_f32_16x16x32_bf16 a[164:167], a[20:23], a[108:111], a[164:167]\n" \
"v_mfma_f32_16x16x32_bf16 a[168:171], a[16:19], a[112:115], a[168:171]\n" \
"ds_read_b128 a[12:15], v101 offset:2048\n" \
"v_mfma_f32_16x16x32_bf16 a[168:171], a[20:23], a[116:119], a[168:171]\n" \
"buffer_load_dwordx4 v65, s[36:39], s82 offen offset:1024 lds\n" \
"v_mfma_f32_16x16x32_bf16 a[172:175], a[16:19], a[120:123], a[172:175]\n" \
"ds_read_b64_tr_b16 a[78:79], v75 offset:20480\n" \
"v_mfma_f32_16x16x32_bf16 a[172:175], a[20:23], a[124:127], a[172:175]\n" \
"v_mfma_f32_16x16x32_bf16 a[188:191], a[24:27], a[120:123], a[188:191]\n" \
"ds_read_b128 a[16:19], v100 offset:4096\n" \
"v_mfma_f32_16x16x32_bf16 a[188:191], a[28:31], a[124:127], a[188:191]\n" \
"buffer_load_dwordx4 v66, s[36:39], s82 offen offset:2048 lds\n" \
"v_mfma_f32_16x16x32_bf16 a[184:187], a[24:27], a[112:115], a[184:187]\n" \
"ds_read_b64_tr_b16 a[80:81], v74 offset:512\n" \
"v_mfma_f32_16x16x32_bf16 a[184:187], a[28:31], a[116:119], a[184:187]\n" \
"v_mfma_f32_16x16x32_bf16 a[180:183], a[24:27], a[104:107], a[180:183]\n" \
"ds_read_b128 a[20:23], v101 offset:4096\n" \
"v_mfma_f32_16x16x32_bf16 a[180:183], a[28:31], a[108:111], a[180:183]\n" \
"buffer_load_dwordx4 v67, s[36:39], s82 offen offset:3072 lds\n" \
"v_mfma_f32_16x16x32_bf16 a[176:179], a[24:27], a[96:99], a[176:179]\n" \
"s_mov_b64 s[64:65], s[8:9]\n" \
"s_add_u32 s68, s66, 0x0\n" \
"ds_read_b64_tr_b16 a[82:83], v74 offset:4608\n" \
"v_mfma_f32_16x16x32_bf16 a[176:179], a[28:31], a[100:103], a[176:179]\n" \
"s_add_u32 s64, s64, s68\n" \
"s_addc_u32 s65, s65, 0\n" \
"v_mfma_f32_16x16x32_bf16 a[192:195], a[32:35], a[96:99], a[192:195]\n" \
"s_xor_b32 s82, s82, 0x80\n" \
"ds_read_b128 a[24:27], v100 offset:6144\n" \
"v_mfma_f32_16x16x32_bf16 a[192:195], a[36:39], a[100:103], a[192:195]\n" \
"global_load_dword v238, v234, s[64:65]\n" \
"v_mfma_f32_16x16x32_bf16 a[196:199], a[32:35], a[104:107], a[196:199]\n" \
"s_mov_b64 s[64:65], s[8:9]\n" \
"s_add_u32 s68, s66, 0x100000\n" \
"ds_read_b64_tr_b16 a[84:85], v74 offset:16896\n" \
"v_mfma_f32_16x16x32_bf16 a[196:199], a[36:39], a[108:111], a[196:199]\n" \
"s_add_u32 s64, s64, s68\n" \
"s_addc_u32 s65, s65, 0\n" \
"v_mfma_f32_16x16x32_bf16 a[200:203], a[32:35], a[112:115], a[200:203]\n" \
"ds_read_b128 a[28:31], v101 offset:6144\n" \
"v_mfma_f32_16x16x32_bf16 a[200:203], a[36:39], a[116:119], a[200:203]\n" \
"global_load_dword v238, v234, s[64:65]\n" \
"v_mfma_f32_16x16x32_bf16 a[204:207], a[32:35], a[120:123], a[204:207]\n" \
"ds_read_b64_tr_b16 a[86:87], v74 offset:20992\n" \
"v_mfma_f32_16x16x32_bf16 a[204:207], a[36:39], a[124:127], a[204:207]\n" \
"v_mfma_f32_16x16x32_bf16 a[220:223], a[40:43], a[120:123], a[220:223]\n" \
"ds_read_b128 a[32:35], v100 offset:8192\n" \
"v_mfma_f32_16x16x32_bf16 a[220:223], a[44:47], a[124:127], a[220:223]\n" \
"v_mfma_f32_16x16x32_bf16 a[216:219], a[40:43], a[112:115], a[216:219]\n" \
"ds_read_b64_tr_b16 a[88:89], v75 offset:512\n" \
"v_mfma_f32_16x16x32_bf16 a[216:219], a[44:47], a[116:119], a[216:219]\n" \
"v_mfma_f32_16x16x32_bf16 a[212:215], a[40:43], a[104:107], a[212:215]\n" \
"ds_read_b128 a[36:39], v101 offset:8192\n" \
"v_mfma_f32_16x16x32_bf16 a[212:215], a[44:47], a[108:111], a[212:215]\n" \
"v_mfma_f32_16x16x32_bf16 a[208:211], a[40:43], a[96:99], a[208:211]\n" \
"ds_read_b64_tr_b16 a[90:91], v75 offset:4608\n" \
"v_mfma_f32_16x16x32_bf16 a[208:211], a[44:47], a[100:103], a[208:211]\n" \
"v_mfma_f32_16x16x32_bf16 a[224:227], a[48:51], a[96:99], a[224:227]\n" \
"ds_read_b128 a[40:43], v100 offset:10240\n" \
"v_mfma_f32_16x16x32_bf16 a[224:227], a[52:55], a[100:103], a[224:227]\n" \
"v_mfma_f32_16x16x32_bf16 a[228:231], a[48:51], a[104:107], a[228:231]\n" \
"ds_read_b64_tr_b16 a[92:93], v75 offset:16896\n" \
"v_mfma_f32_16x16x32_bf16 a[228:231], a[52:55], a[108:111], a[228:231]\n" \
"v_mfma_f32_16x16x32_bf16 a[232:235], a[48:51], a[112:115], a[232:235]\n" \
"ds_read_b128 a[44:47], v101 offset:10240\n" \
"v_mfma_f32_16x16x32_bf16 a[232:235], a[52:55], a[116:119], a[232:235]\n" \
"v_mfma_f32_16x16x32_bf16 a[236:239], a[48:51], a[120:123], a[236:239]\n" \
"ds_read_b64_tr_b16 a[94:95], v75 offset:20992\n" \
"v_mfma_f32_16x16x32_bf16 a[236:239], a[52:55], a[124:127], a[236:239]\n" \
"v_mfma_f32_16x16x32_bf16 a[252:255], a[56:59], a[120:123], a[252:255]\n" \
"ds_read_b128 a[48:51], v100 offset:12288\n" \
"v_mfma_f32_16x16x32_bf16 a[252:255], a[60:63], a[124:127], a[252:255]\n" \
"v_mfma_f32_16x16x32_bf16 a[248:251], a[56:59], a[112:115], a[248:251]\n" \
"v_mfma_f32_16x16x32_bf16 a[248:251], a[60:63], a[116:119], a[248:251]\n" \
"v_mfma_f32_16x16x32_bf16 a[244:247], a[56:59], a[104:107], a[244:247]\n" \
"ds_read_b128 a[52:55], v101 offset:12288\n" \
"v_mfma_f32_16x16x32_bf16 a[244:247], a[60:63], a[108:111], a[244:247]\n" \
"v_mfma_f32_16x16x32_bf16 a[240:243], a[56:59], a[96:99], a[240:243]\n" \
"v_mfma_f32_16x16x32_bf16 a[240:243], a[60:63], a[100:103], a[240:243]\n" \
"s_waitcnt lgkmcnt(1)\n" \
"v_mfma_f32_16x16x32_bf16 v[0:3], a[64:67], a[0:3], v[0:3]\n" \
"ds_read_b128 a[56:59], v100 offset:14336\n" \
"v_mfma_f32_16x16x32_bf16 v[0:3], a[68:71], a[4:7], v[0:3]\n" \
"v_mfma_f32_16x16x32_bf16 v[4:7], a[72:75], a[0:3], v[4:7]\n" \
"s_add_u32 m0, s74, 32768\n" \
"ds_read_b128 a[60:63], v101 offset:14336\n" \
"v_mfma_f32_16x16x32_bf16 v[4:7], a[76:79], a[4:7], v[4:7]\n" \
"buffer_load_dwordx4 v68, s[40:43], s83 offen offset:0 lds\n" \
"v_mfma_f32_16x16x32_bf16 v[8:11], a[80:83], a[0:3], v[8:11]\n" \
"ds_read_b64_tr_b16 a[96:97], v74 offset:1024\n" \
"v_mfma_f32_16x16x32_bf16 v[8:11], a[84:87], a[4:7], v[8:11]\n" \
"v_mfma_f32_16x16x32_bf16 v[12:15], a[88:91], a[0:3], v[12:15]\n" \
"ds_read_b64_tr_b16 a[98:99], v74 offset:5120\n" \
"v_mfma_f32_16x16x32_bf16 v[12:15], a[92:95], a[4:7], v[12:15]\n" \
"buffer_load_dwordx4 v69, s[44:47], s83 offen offset:1024 lds\n" \
"v_mfma_f32_16x16x32_bf16 v[28:31], a[88:91], a[8:11], v[28:31]\n" \
"ds_read_b64_tr_b16 a[100:101], v74 offset:17408\n" \
"v_mfma_f32_16x16x32_bf16 v[28:31], a[92:95], a[12:15], v[28:31]\n" \
"v_mfma_f32_16x16x32_bf16 v[24:27], a[80:83], a[8:11], v[24:27]\n" \
"ds_read_b64_tr_b16 a[102:103], v74 offset:21504\n" \
"v_mfma_f32_16x16x32_bf16 v[24:27], a[84:87], a[12:15], v[24:27]\n" \
"buffer_load_dwordx4 v70, s[40:43], s83 offen offset:2048 lds\n" \
"v_mfma_f32_16x16x32_bf16 v[20:23], a[72:75], a[8:11], v[20:23]\n" \
"ds_read_b64_tr_b16 a[104:105], v75 offset:1024\n" \
"v_mfma_f32_16x16x32_bf16 v[20:23], a[76:79], a[12:15], v[20:23]\n" \
"v_mfma_f32_16x16x32_bf16 v[16:19], a[64:67], a[8:11], v[16:19]\n" \
"ds_read_b64_tr_b16 a[106:107], v75 offset:5120\n" \
"v_mfma_f32_16x16x32_bf16 v[16:19], a[68:71], a[12:15], v[16:19]\n" \
"buffer_load_dwordx4 v71, s[44:47], s83 offen offset:3072 lds\n" \
"v_mfma_f32_16x16x32_bf16 v[32:35], a[64:67], a[16:19], v[32:35]\n" \
"ds_read_b64_tr_b16 a[108:109], v75 offset:17408\n" \
"v_mfma_f32_16x16x32_bf16 v[32:35], a[68:71], a[20:23], v[32:35]\n" \
"s_xor_b32 s83, s83, 0x180000\n" \
"v_mfma_f32_16x16x32_bf16 v[36:39], a[72:75], a[16:19], v[36:39]\n" \
"s_add_u32 m0, s74, 36864\n" \
"ds_read_b64_tr_b16 a[110:111], v75 offset:21504\n" \
"v_mfma_f32_16x16x32_bf16 v[36:39], a[76:79], a[20:23], v[36:39]\n" \
"buffer_load_dwordx4 v68, s[40:43], s84 offen offset:0 lds\n" \
"v_mfma_f32_16x16x32_bf16 v[40:43], a[80:83], a[16:19], v[40:43]\n" \
"ds_read_b64_tr_b16 a[112:113], v74 offset:1536\n" \
"v_mfma_f32_16x16x32_bf16 v[40:43], a[84:87], a[20:23], v[40:43]\n" \
"v_mfma_f32_16x16x32_bf16 v[44:47], a[88:91], a[16:19], v[44:47]\n" \
"ds_read_b64_tr_b16 a[114:115], v74 offset:5632\n" \
"v_mfma_f32_16x16x32_bf16 v[44:47], a[92:95], a[20:23], v[44:47]\n" \
"buffer_load_dwordx4 v69, s[44:47], s84 offen offset:1024 lds\n" \
"v_mfma_f32_16x16x32_bf16 v[60:63], a[88:91], a[24:27], v[60:63]\n" \
"ds_read_b64_tr_b16 a[116:117], v74 offset:17920\n" \
"v_mfma_f32_16x16x32_bf16 v[60:63], a[92:95], a[28:31], v[60:63]\n" \
"v_mfma_f32_16x16x32_bf16 v[56:59], a[80:83], a[24:27], v[56:59]\n" \
"ds_read_b64_tr_b16 a[118:119], v74 offset:22016\n" \
"v_mfma_f32_16x16x32_bf16 v[56:59], a[84:87], a[28:31], v[56:59]\n" \
"buffer_load_dwordx4 v70, s[40:43], s84 offen offset:2048 lds\n" \
"v_mfma_f32_16x16x32_bf16 v[52:55], a[72:75], a[24:27], v[52:55]\n" \
"ds_read_b64_tr_b16 a[120:121], v75 offset:1536\n" \
"v_mfma_f32_16x16x32_bf16 v[52:55], a[76:79], a[28:31], v[52:55]\n" \
"v_mfma_f32_16x16x32_bf16 v[48:51], a[64:67], a[24:27], v[48:51]\n" \
"ds_read_b64_tr_b16 a[122:123], v75 offset:5632\n" \
"v_mfma_f32_16x16x32_bf16 v[48:51], a[68:71], a[28:31], v[48:51]\n" \
"buffer_load_dwordx4 v71, s[44:47], s84 offen offset:3072 lds\n" \
"v_mfma_f32_16x16x32_bf16 v[144:147], a[64:67], a[32:35], v[144:147]\n" \
"ds_read_b64_tr_b16 a[124:125], v75 offset:17920\n" \
"v_mfma_f32_16x16x32_bf16 v[144:147], a[68:71], a[36:39], v[144:147]\n" \
"v_mfma_f32_16x16x32_bf16 v[148:151], a[72:75], a[32:35], v[148:151]\n" \
"s_xor_b32 s84, s84, 0x180000\n" \
"ds_read_b64_tr_b16 a[126:127], v75 offset:22016\n" \
"v_mfma_f32_16x16x32_bf16 v[148:151], a[76:79], a[36:39], v[148:151]\n" \
"v_mfma_f32_16x16x32_bf16 v[152:155], a[80:83], a[32:35], v[152:155]\n" \
"v_mfma_f32_16x16x32_bf16 v[152:155], a[84:87], a[36:39], v[152:155]\n" \
"v_mfma_f32_16x16x32_bf16 v[156:159], a[88:91], a[32:35], v[156:159]\n" \
"v_mfma_f32_16x16x32_bf16 v[156:159], a[92:95], a[36:39], v[156:159]\n" \
"v_mfma_f32_16x16x32_bf16 v[188:191], a[88:91], a[40:43], v[188:191]\n" \
"v_mfma_f32_16x16x32_bf16 v[188:191], a[92:95], a[44:47], v[188:191]\n" \
"v_mfma_f32_16x16x32_bf16 v[184:187], a[80:83], a[40:43], v[184:187]\n" \
"v_mfma_f32_16x16x32_bf16 v[184:187], a[84:87], a[44:47], v[184:187]\n" \
"v_mfma_f32_16x16x32_bf16 v[180:183], a[72:75], a[40:43], v[180:183]\n" \
"v_mfma_f32_16x16x32_bf16 v[180:183], a[76:79], a[44:47], v[180:183]\n" \
"v_mfma_f32_16x16x32_bf16 v[176:179], a[64:67], a[40:43], v[176:179]\n" \
"v_mfma_f32_16x16x32_bf16 v[176:179], a[68:71], a[44:47], v[176:179]\n" \
"s_waitcnt lgkmcnt(0)\n" \
"v_mfma_f32_16x16x32_bf16 v[192:195], a[64:67], a[48:51], v[192:195]\n" \
"v_mfma_f32_16x16x32_bf16 v[192:195], a[68:71], a[52:55], v[192:195]\n" \
"v_mfma_f32_16x16x32_bf16 v[196:199], a[72:75], a[48:51], v[196:199]\n" \
"v_mfma_f32_16x16x32_bf16 v[196:199], a[76:79], a[52:55], v[196:199]\n" \
"v_mfma_f32_16x16x32_bf16 v[200:203], a[80:83], a[48:51], v[200:203]\n" \
"v_mfma_f32_16x16x32_bf16 v[200:203], a[84:87], a[52:55], v[200:203]\n" \
"v_mfma_f32_16x16x32_bf16 v[204:207], a[88:91], a[48:51], v[204:207]\n" \
"v_mfma_f32_16x16x32_bf16 v[204:207], a[92:95], a[52:55], v[204:207]\n" \
"v_mfma_f32_16x16x32_bf16 v[220:223], a[88:91], a[56:59], v[220:223]\n" \
"v_mfma_f32_16x16x32_bf16 v[220:223], a[92:95], a[60:63], v[220:223]\n" \
"v_mfma_f32_16x16x32_bf16 v[216:219], a[80:83], a[56:59], v[216:219]\n" \
"v_mfma_f32_16x16x32_bf16 v[216:219], a[84:87], a[60:63], v[216:219]\n" \
"v_mfma_f32_16x16x32_bf16 v[212:215], a[72:75], a[56:59], v[212:215]\n" \
"v_mfma_f32_16x16x32_bf16 v[212:215], a[76:79], a[60:63], v[212:215]\n" \
"v_mfma_f32_16x16x32_bf16 v[208:211], a[64:67], a[56:59], v[208:211]\n" \
"v_mfma_f32_16x16x32_bf16 v[208:211], a[68:71], a[60:63], v[208:211]\n" \
"s_waitcnt vmcnt(10) lgkmcnt(0)\n" \
"s_barrier\n" \
"v_mfma_f32_16x16x32_bf16 a[128:131], a[0:3], a[96:99], a[128:131]\n" \
"s_add_u32 m0, s74, 0\n" \
"ds_read_b64_tr_b16 a[64:65], v98 offset:0\n" \
"v_mfma_f32_16x16x32_bf16 a[128:131], a[4:7], a[100:103], a[128:131]\n" \
"buffer_load_dwordx4 v64, s[36:39], s81 offen offset:0 lds\n" \
"v_mfma_f32_16x16x32_bf16 a[132:135], a[0:3], a[104:107], a[132:135]\n" \
"ds_read_b64_tr_b16 a[66:67], v98 offset:4096\n" \
"v_mfma_f32_16x16x32_bf16 a[132:135], a[4:7], a[108:111], a[132:135]\n" \
"v_mfma_f32_16x16x32_bf16 a[136:139], a[0:3], a[112:115], a[136:139]\n" \
"ds_read_b64_tr_b16 a[68:69], v98 offset:16384\n" \
"v_mfma_f32_16x16x32_bf16 a[136:139], a[4:7], a[116:119], a[136:139]\n" \
"buffer_load_dwordx4 v65, s[36:39], s81 offen offset:1024 lds\n" \
"v_mfma_f32_16x16x32_bf16 a[140:143], a[0:3], a[120:123], a[140:143]\n" \
"ds_read_b64_tr_b16 a[70:71], v98 offset:20480\n" \
"v_mfma_f32_16x16x32_bf16 a[140:143], a[4:7], a[124:127], a[140:143]\n" \
"v_mfma_f32_16x16x32_bf16 a[156:159], a[8:11], a[120:123], a[156:159]\n" \
"ds_read_b128 a[0:3], v96 offset:32768\n" \
"v_mfma_f32_16x16x32_bf16 a[156:159], a[12:15], a[124:127], a[156:159]\n" \
"buffer_load_dwordx4 v66, s[36:39], s81 offen offset:2048 lds\n" \
"v_mfma_f32_16x16x32_bf16 a[152:155], a[8:11], a[112:115], a[152:155]\n" \
"ds_read_b64_tr_b16 a[72:73], v99 offset:0\n" \
"v_mfma_f32_16x16x32_bf16 a[152:155], a[12:15], a[116:119], a[152:155]\n" \
"v_mfma_f32_16x16x32_bf16 a[148:151], a[8:11], a[104:107], a[148:151]\n" \
"ds_read_b128 a[4:7], v97 offset:32768\n" \
"v_mfma_f32_16x16x32_bf16 a[148:151], a[12:15], a[108:111], a[148:151]\n" \
"buffer_load_dwordx4 v67, s[36:39], s81 offen offset:3072 lds\n" \
"v_mfma_f32_16x16x32_bf16 a[144:147], a[8:11], a[96:99], a[144:147]\n" \
"ds_read_b64_tr_b16 a[74:75], v99 offset:4096\n" \
"Lpg1132_%=:\n" \
"Lst0x1_%=:\n" \
"s_cbranch_execz Lpg3342_%=\n" \
"v_mfma_f32_16x16x32_bf16 a[144:147], a[12:15], a[100:103], a[144:147]\n" \
"s_xor_b32 s81, s81, 0x180\n" \
"v_mfma_f32_16x16x32_bf16 a[160:163], a[16:19], a[96:99], a[160:163]\n" \
"s_add_u32 m0, s74, 4096\n" \
"ds_read_b128 a[8:11], v96 offset:34816\n" \
"v_mfma_f32_16x16x32_bf16 a[160:163], a[20:23], a[100:103], a[160:163]\n" \
"buffer_load_dwordx4 v64, s[36:39], s82 offen offset:0 lds\n" \
"v_mfma_f32_16x16x32_bf16 a[164:167], a[16:19], a[104:107], a[164:167]\n" \
"ds_read_b64_tr_b16 a[76:77], v99 offset:16384\n" \
"v_mfma_f32_16x16x32_bf16 a[164:167], a[20:23], a[108:111], a[164:167]\n" \
"v_mfma_f32_16x16x32_bf16 a[168:171], a[16:19], a[112:115], a[168:171]\n" \
"ds_read_b128 a[12:15], v97 offset:34816\n" \
"v_mfma_f32_16x16x32_bf16 a[168:171], a[20:23], a[116:119], a[168:171]\n" \
"buffer_load_dwordx4 v65, s[36:39], s82 offen offset:1024 lds\n" \
"v_mfma_f32_16x16x32_bf16 a[172:175], a[16:19], a[120:123], a[172:175]\n" \
"ds_read_b64_tr_b16 a[78:79], v99 offset:20480\n" \
"v_mfma_f32_16x16x32_bf16 a[172:175], a[20:23], a[124:127], a[172:175]\n" \
"v_mfma_f32_16x16x32_bf16 a[188:191], a[24:27], a[120:123], a[188:191]\n" \
"ds_read_b128 a[16:19], v96 offset:36864\n" \
"v_mfma_f32_16x16x32_bf16 a[188:191], a[28:31], a[124:127], a[188:191]\n" \
"buffer_load_dwordx4 v66, s[36:39], s82 offen offset:2048 lds\n" \
"v_mfma_f32_16x16x32_bf16 a[184:187], a[24:27], a[112:115], a[184:187]\n" \
"ds_read_b64_tr_b16 a[80:81], v98 offset:512\n" \
"v_mfma_f32_16x16x32_bf16 a[184:187], a[28:31], a[116:119], a[184:187]\n" \
"v_mfma_f32_16x16x32_bf16 a[180:183], a[24:27], a[104:107], a[180:183]\n" \
"ds_read_b128 a[20:23], v97 offset:36864\n" \
"v_mfma_f32_16x16x32_bf16 a[180:183], a[28:31], a[108:111], a[180:183]\n" \
"buffer_load_dwordx4 v67, s[36:39], s82 offen offset:3072 lds\n" \
"v_mfma_f32_16x16x32_bf16 a[176:179], a[24:27], a[96:99], a[176:179]\n" \
"s_mov_b64 s[64:65], s[8:9]\n" \
"s_add_u32 s68, s66, 0x200000\n" \
"ds_read_b64_tr_b16 a[82:83], v98 offset:4608\n" \
"v_mfma_f32_16x16x32_bf16 a[176:179], a[28:31], a[100:103], a[176:179]\n" \
"s_add_u32 s64, s64, s68\n" \
"s_addc_u32 s65, s65, 0\n" \
"v_mfma_f32_16x16x32_bf16 a[192:195], a[32:35], a[96:99], a[192:195]\n" \
"s_xor_b32 s82, s82, 0x180\n" \
"ds_read_b128 a[24:27], v96 offset:38912\n" \
"v_mfma_f32_16x16x32_bf16 a[192:195], a[36:39], a[100:103], a[192:195]\n" \
"global_load_dword v238, v234, s[64:65]\n" \
"v_mfma_f32_16x16x32_bf16 a[196:199], a[32:35], a[104:107], a[196:199]\n" \
"s_mov_b64 s[64:65], s[8:9]\n" \
"s_add_u32 s68, s66, 0x300000\n" \
"ds_read_b64_tr_b16 a[84:85], v98 offset:16896\n" \
"v_mfma_f32_16x16x32_bf16 a[196:199], a[36:39], a[108:111], a[196:199]\n" \
"s_add_u32 s64, s64, s68\n" \
"s_addc_u32 s65, s65, 0\n" \
"v_mfma_f32_16x16x32_bf16 a[200:203], a[32:35], a[112:115], a[200:203]\n" \
"ds_read_b128 a[28:31], v97 offset:38912\n" \
"v_mfma_f32_16x16x32_bf16 a[200:203], a[36:39], a[116:119], a[200:203]\n" \
"global_load_dword v238, v234, s[64:65]\n" \
"v_mfma_f32_16x16x32_bf16 a[204:207], a[32:35], a[120:123], a[204:207]\n" \
"s_mov_b64 s[64:65], s[12:13]\n" \
"s_add_u32 s68, s67, 0x0\n" \
"ds_read_b64_tr_b16 a[86:87], v98 offset:20992\n" \
"v_mfma_f32_16x16x32_bf16 a[204:207], a[36:39], a[124:127], a[204:207]\n" \
"s_add_u32 s64, s64, s68\n" \
"s_addc_u32 s65, s65, 0\n" \
"v_mfma_f32_16x16x32_bf16 a[220:223], a[40:43], a[120:123], a[220:223]\n" \
"ds_read_b128 a[32:35], v96 offset:40960\n" \
"v_mfma_f32_16x16x32_bf16 a[220:223], a[44:47], a[124:127], a[220:223]\n" \
"global_load_dword v238, v234, s[64:65]\n" \
"v_mfma_f32_16x16x32_bf16 a[216:219], a[40:43], a[112:115], a[216:219]\n" \
"ds_read_b64_tr_b16 a[88:89], v99 offset:512\n" \
"v_mfma_f32_16x16x32_bf16 a[216:219], a[44:47], a[116:119], a[216:219]\n" \
"v_mfma_f32_16x16x32_bf16 a[212:215], a[40:43], a[104:107], a[212:215]\n" \
"ds_read_b128 a[36:39], v97 offset:40960\n" \
"v_mfma_f32_16x16x32_bf16 a[212:215], a[44:47], a[108:111], a[212:215]\n" \
"v_mfma_f32_16x16x32_bf16 a[208:211], a[40:43], a[96:99], a[208:211]\n" \
"ds_read_b64_tr_b16 a[90:91], v99 offset:4608\n" \
"v_mfma_f32_16x16x32_bf16 a[208:211], a[44:47], a[100:103], a[208:211]\n" \
"v_mfma_f32_16x16x32_bf16 a[224:227], a[48:51], a[96:99], a[224:227]\n" \
"ds_read_b128 a[40:43], v96 offset:43008\n" \
"v_mfma_f32_16x16x32_bf16 a[224:227], a[52:55], a[100:103], a[224:227]\n" \
"v_mfma_f32_16x16x32_bf16 a[228:231], a[48:51], a[104:107], a[228:231]\n" \
"ds_read_b64_tr_b16 a[92:93], v99 offset:16896\n" \
"v_mfma_f32_16x16x32_bf16 a[228:231], a[52:55], a[108:111], a[228:231]\n" \
"v_mfma_f32_16x16x32_bf16 a[232:235], a[48:51], a[112:115], a[232:235]\n" \
"ds_read_b128 a[44:47], v97 offset:43008\n" \
"v_mfma_f32_16x16x32_bf16 a[232:235], a[52:55], a[116:119], a[232:235]\n" \
"v_mfma_f32_16x16x32_bf16 a[236:239], a[48:51], a[120:123], a[236:239]\n" \
"ds_read_b64_tr_b16 a[94:95], v99 offset:20992\n" \
"v_mfma_f32_16x16x32_bf16 a[236:239], a[52:55], a[124:127], a[236:239]\n" \
"v_mfma_f32_16x16x32_bf16 a[252:255], a[56:59], a[120:123], a[252:255]\n" \
"ds_read_b128 a[48:51], v96 offset:45056\n" \
"v_mfma_f32_16x16x32_bf16 a[252:255], a[60:63], a[124:127], a[252:255]\n" \
"v_mfma_f32_16x16x32_bf16 a[248:251], a[56:59], a[112:115], a[248:251]\n" \
"v_mfma_f32_16x16x32_bf16 a[248:251], a[60:63], a[116:119], a[248:251]\n" \
"v_mfma_f32_16x16x32_bf16 a[244:247], a[56:59], a[104:107], a[244:247]\n" \
"ds_read_b128 a[52:55], v97 offset:45056\n" \
"v_mfma_f32_16x16x32_bf16 a[244:247], a[60:63], a[108:111], a[244:247]\n" \
"v_mfma_f32_16x16x32_bf16 a[240:243], a[56:59], a[96:99], a[240:243]\n" \
"v_mfma_f32_16x16x32_bf16 a[240:243], a[60:63], a[100:103], a[240:243]\n" \
"s_waitcnt lgkmcnt(1)\n" \
"v_mfma_f32_16x16x32_bf16 v[0:3], a[64:67], a[0:3], v[0:3]\n" \
"ds_read_b128 a[56:59], v96 offset:47104\n" \
"v_mfma_f32_16x16x32_bf16 v[0:3], a[68:71], a[4:7], v[0:3]\n" \
"v_mfma_f32_16x16x32_bf16 v[4:7], a[72:75], a[0:3], v[4:7]\n" \
"s_add_u32 m0, s74, 131072\n" \
"ds_read_b128 a[60:63], v97 offset:47104\n" \
"v_mfma_f32_16x16x32_bf16 v[4:7], a[76:79], a[4:7], v[4:7]\n" \
"buffer_load_dwordx4 v68, s[40:43], s83 offen offset:0 lds\n" \
"v_mfma_f32_16x16x32_bf16 v[8:11], a[80:83], a[0:3], v[8:11]\n" \
"ds_read_b64_tr_b16 a[96:97], v98 offset:1024\n" \
"v_mfma_f32_16x16x32_bf16 v[8:11], a[84:87], a[4:7], v[8:11]\n" \
"v_mfma_f32_16x16x32_bf16 v[12:15], a[88:91], a[0:3], v[12:15]\n" \
"ds_read_b64_tr_b16 a[98:99], v98 offset:5120\n" \
"v_mfma_f32_16x16x32_bf16 v[12:15], a[92:95], a[4:7], v[12:15]\n" \
"buffer_load_dwordx4 v69, s[44:47], s83 offen offset:1024 lds\n" \
"v_mfma_f32_16x16x32_bf16 v[28:31], a[88:91], a[8:11], v[28:31]\n" \
"ds_read_b64_tr_b16 a[100:101], v98 offset:17408\n" \
"v_mfma_f32_16x16x32_bf16 v[28:31], a[92:95], a[12:15], v[28:31]\n" \
"v_mfma_f32_16x16x32_bf16 v[24:27], a[80:83], a[8:11], v[24:27]\n" \
"ds_read_b64_tr_b16 a[102:103], v98 offset:21504\n" \
"v_mfma_f32_16x16x32_bf16 v[24:27], a[84:87], a[12:15], v[24:27]\n" \
"buffer_load_dwordx4 v70, s[40:43], s83 offen offset:2048 lds\n" \
"v_mfma_f32_16x16x32_bf16 v[20:23], a[72:75], a[8:11], v[20:23]\n" \
"ds_read_b64_tr_b16 a[104:105], v99 offset:1024\n" \
"v_mfma_f32_16x16x32_bf16 v[20:23], a[76:79], a[12:15], v[20:23]\n" \
"v_mfma_f32_16x16x32_bf16 v[16:19], a[64:67], a[8:11], v[16:19]\n" \
"ds_read_b64_tr_b16 a[106:107], v99 offset:5120\n" \
"v_mfma_f32_16x16x32_bf16 v[16:19], a[68:71], a[12:15], v[16:19]\n" \
"buffer_load_dwordx4 v71, s[44:47], s83 offen offset:3072 lds\n" \
"v_mfma_f32_16x16x32_bf16 v[32:35], a[64:67], a[16:19], v[32:35]\n" \
"ds_read_b64_tr_b16 a[108:109], v99 offset:17408\n" \
"v_mfma_f32_16x16x32_bf16 v[32:35], a[68:71], a[20:23], v[32:35]\n" \
"s_xor_b32 s83, s83, 0x80000\n" \
"v_mfma_f32_16x16x32_bf16 v[36:39], a[72:75], a[16:19], v[36:39]\n" \
"s_add_u32 m0, s74, 135168\n" \
"ds_read_b64_tr_b16 a[110:111], v99 offset:21504\n" \
"v_mfma_f32_16x16x32_bf16 v[36:39], a[76:79], a[20:23], v[36:39]\n" \
"buffer_load_dwordx4 v68, s[40:43], s84 offen offset:0 lds\n" \
"v_mfma_f32_16x16x32_bf16 v[40:43], a[80:83], a[16:19], v[40:43]\n" \
"ds_read_b64_tr_b16 a[112:113], v98 offset:1536\n" \
"v_mfma_f32_16x16x32_bf16 v[40:43], a[84:87], a[20:23], v[40:43]\n" \
"v_mfma_f32_16x16x32_bf16 v[44:47], a[88:91], a[16:19], v[44:47]\n" \
"ds_read_b64_tr_b16 a[114:115], v98 offset:5632\n" \
"v_mfma_f32_16x16x32_bf16 v[44:47], a[92:95], a[20:23], v[44:47]\n" \
"buffer_load_dwordx4 v69, s[44:47], s84 offen offset:1024 lds\n" \
"v_mfma_f32_16x16x32_bf16 v[60:63], a[88:91], a[24:27], v[60:63]\n" \
"ds_read_b64_tr_b16 a[116:117], v98 offset:17920\n" \
"v_mfma_f32_16x16x32_bf16 v[60:63], a[92:95], a[28:31], v[60:63]\n" \
"v_mfma_f32_16x16x32_bf16 v[56:59], a[80:83], a[24:27], v[56:59]\n" \
"ds_read_b64_tr_b16 a[118:119], v98 offset:22016\n" \
"v_mfma_f32_16x16x32_bf16 v[56:59], a[84:87], a[28:31], v[56:59]\n" \
"buffer_load_dwordx4 v70, s[40:43], s84 offen offset:2048 lds\n" \
"v_mfma_f32_16x16x32_bf16 v[52:55], a[72:75], a[24:27], v[52:55]\n" \
"ds_read_b64_tr_b16 a[120:121], v99 offset:1536\n" \
"v_mfma_f32_16x16x32_bf16 v[52:55], a[76:79], a[28:31], v[52:55]\n" \
"v_mfma_f32_16x16x32_bf16 v[48:51], a[64:67], a[24:27], v[48:51]\n" \
"ds_read_b64_tr_b16 a[122:123], v99 offset:5632\n" \
"v_mfma_f32_16x16x32_bf16 v[48:51], a[68:71], a[28:31], v[48:51]\n" \
"buffer_load_dwordx4 v71, s[44:47], s84 offen offset:3072 lds\n" \
"v_mfma_f32_16x16x32_bf16 v[144:147], a[64:67], a[32:35], v[144:147]\n" \
"ds_read_b64_tr_b16 a[124:125], v99 offset:17920\n" \
"v_mfma_f32_16x16x32_bf16 v[144:147], a[68:71], a[36:39], v[144:147]\n" \
"v_mfma_f32_16x16x32_bf16 v[148:151], a[72:75], a[32:35], v[148:151]\n" \
"s_xor_b32 s84, s84, 0x80000\n" \
"ds_read_b64_tr_b16 a[126:127], v99 offset:22016\n" \
"v_mfma_f32_16x16x32_bf16 v[148:151], a[76:79], a[36:39], v[148:151]\n" \
"v_mfma_f32_16x16x32_bf16 v[152:155], a[80:83], a[32:35], v[152:155]\n" \
"v_mfma_f32_16x16x32_bf16 v[152:155], a[84:87], a[36:39], v[152:155]\n" \
"v_mfma_f32_16x16x32_bf16 v[156:159], a[88:91], a[32:35], v[156:159]\n" \
"v_mfma_f32_16x16x32_bf16 v[156:159], a[92:95], a[36:39], v[156:159]\n" \
"v_mfma_f32_16x16x32_bf16 v[188:191], a[88:91], a[40:43], v[188:191]\n" \
"v_mfma_f32_16x16x32_bf16 v[188:191], a[92:95], a[44:47], v[188:191]\n" \
"v_mfma_f32_16x16x32_bf16 v[184:187], a[80:83], a[40:43], v[184:187]\n" \
"v_mfma_f32_16x16x32_bf16 v[184:187], a[84:87], a[44:47], v[184:187]\n" \
"v_mfma_f32_16x16x32_bf16 v[180:183], a[72:75], a[40:43], v[180:183]\n" \
"v_mfma_f32_16x16x32_bf16 v[180:183], a[76:79], a[44:47], v[180:183]\n" \
"v_mfma_f32_16x16x32_bf16 v[176:179], a[64:67], a[40:43], v[176:179]\n" \
"v_mfma_f32_16x16x32_bf16 v[176:179], a[68:71], a[44:47], v[176:179]\n" \
"s_waitcnt lgkmcnt(0)\n" \
"v_mfma_f32_16x16x32_bf16 v[192:195], a[64:67], a[48:51], v[192:195]\n" \
"v_mfma_f32_16x16x32_bf16 v[192:195], a[68:71], a[52:55], v[192:195]\n" \
"v_mfma_f32_16x16x32_bf16 v[196:199], a[72:75], a[48:51], v[196:199]\n" \
"v_mfma_f32_16x16x32_bf16 v[196:199], a[76:79], a[52:55], v[196:199]\n" \
"v_mfma_f32_16x16x32_bf16 v[200:203], a[80:83], a[48:51], v[200:203]\n" \
"v_mfma_f32_16x16x32_bf16 v[200:203], a[84:87], a[52:55], v[200:203]\n" \
"v_mfma_f32_16x16x32_bf16 v[204:207], a[88:91], a[48:51], v[204:207]\n" \
"v_mfma_f32_16x16x32_bf16 v[204:207], a[92:95], a[52:55], v[204:207]\n" \
"v_mfma_f32_16x16x32_bf16 v[220:223], a[88:91], a[56:59], v[220:223]\n" \
"v_mfma_f32_16x16x32_bf16 v[220:223], a[92:95], a[60:63], v[220:223]\n" \
"v_mfma_f32_16x16x32_bf16 v[216:219], a[80:83], a[56:59], v[216:219]\n" \
"v_mfma_f32_16x16x32_bf16 v[216:219], a[84:87], a[60:63], v[216:219]\n" \
"v_mfma_f32_16x16x32_bf16 v[212:215], a[72:75], a[56:59], v[212:215]\n" \
"v_mfma_f32_16x16x32_bf16 v[212:215], a[76:79], a[60:63], v[212:215]\n" \
"v_mfma_f32_16x16x32_bf16 v[208:211], a[64:67], a[56:59], v[208:211]\n" \
"v_mfma_f32_16x16x32_bf16 v[208:211], a[68:71], a[60:63], v[208:211]\n" \
"s_waitcnt vmcnt(11) lgkmcnt(0)\n" \
"s_barrier\n" \
"v_mfma_f32_16x16x32_bf16 a[128:131], a[0:3], a[96:99], a[128:131]\n" \
"s_add_u32 m0, s74, 65536\n" \
"ds_read_b64_tr_b16 a[64:65], v74 offset:32768\n" \
"v_mfma_f32_16x16x32_bf16 a[128:131], a[4:7], a[100:103], a[128:131]\n" \
"buffer_load_dwordx4 v64, s[36:39], s81 offen offset:0 lds\n" \
"v_mfma_f32_16x16x32_bf16 a[132:135], a[0:3], a[104:107], a[132:135]\n" \
"ds_read_b64_tr_b16 a[66:67], v74 offset:36864\n" \
"v_mfma_f32_16x16x32_bf16 a[132:135], a[4:7], a[108:111], a[132:135]\n" \
"v_mfma_f32_16x16x32_bf16 a[136:139], a[0:3], a[112:115], a[136:139]\n" \
"ds_read_b64_tr_b16 a[68:69], v74 offset:49152\n" \
"v_mfma_f32_16x16x32_bf16 a[136:139], a[4:7], a[116:119], a[136:139]\n" \
"buffer_load_dwordx4 v65, s[36:39], s81 offen offset:1024 lds\n" \
"v_mfma_f32_16x16x32_bf16 a[140:143], a[0:3], a[120:123], a[140:143]\n" \
"ds_read_b64_tr_b16 a[70:71], v74 offset:53248\n" \
"v_mfma_f32_16x16x32_bf16 a[140:143], a[4:7], a[124:127], a[140:143]\n" \
"v_mfma_f32_16x16x32_bf16 a[156:159], a[8:11], a[120:123], a[156:159]\n" \
"ds_read_b128 a[0:3], v72 offset:0\n" \
"v_mfma_f32_16x16x32_bf16 a[156:159], a[12:15], a[124:127], a[156:159]\n" \
"buffer_load_dwordx4 v66, s[36:39], s81 offen offset:2048 lds\n" \
"v_mfma_f32_16x16x32_bf16 a[152:155], a[8:11], a[112:115], a[152:155]\n" \
"ds_read_b64_tr_b16 a[72:73], v75 offset:32768\n" \
"v_mfma_f32_16x16x32_bf16 a[152:155], a[12:15], a[116:119], a[152:155]\n" \
"v_mfma_f32_16x16x32_bf16 a[148:151], a[8:11], a[104:107], a[148:151]\n" \
"ds_read_b128 a[4:7], v73 offset:0\n" \
"v_mfma_f32_16x16x32_bf16 a[148:151], a[12:15], a[108:111], a[148:151]\n" \
"buffer_load_dwordx4 v67, s[36:39], s81 offen offset:3072 lds\n" \
"v_mfma_f32_16x16x32_bf16 a[144:147], a[8:11], a[96:99], a[144:147]\n" \
"ds_read_b64_tr_b16 a[74:75], v75 offset:36864\n" \
"v_mfma_f32_16x16x32_bf16 a[144:147], a[12:15], a[100:103], a[144:147]\n" \
"s_xor_b32 s81, s81, 0x80\n" \
"v_mfma_f32_16x16x32_bf16 a[160:163], a[16:19], a[96:99], a[160:163]\n" \
"s_add_u32 m0, s74, 69632\n" \
"ds_read_b128 a[8:11], v72 offset:2048\n" \
"v_mfma_f32_16x16x32_bf16 a[160:163], a[20:23], a[100:103], a[160:163]\n" \
"buffer_load_dwordx4 v64, s[36:39], s82 offen offset:0 lds\n" \
"v_mfma_f32_16x16x32_bf16 a[164:167], a[16:19], a[104:107], a[164:167]\n" \
"ds_read_b64_tr_b16 a[76:77], v75 offset:49152\n" \
"v_mfma_f32_16x16x32_bf16 a[164:167], a[20:23], a[108:111], a[164:167]\n" \
"v_mfma_f32_16x16x32_bf16 a[168:171], a[16:19], a[112:115], a[168:171]\n" \
"ds_read_b128 a[12:15], v73 offset:2048\n" \
"v_mfma_f32_16x16x32_bf16 a[168:171], a[20:23], a[116:119], a[168:171]\n" \
"buffer_load_dwordx4 v65, s[36:39], s82 offen offset:1024 lds\n" \
"v_mfma_f32_16x16x32_bf16 a[172:175], a[16:19], a[120:123], a[172:175]\n" \
"ds_read_b64_tr_b16 a[78:79], v75 offset:53248\n" \
"v_mfma_f32_16x16x32_bf16 a[172:175], a[20:23], a[124:127], a[172:175]\n" \
"v_mfma_f32_16x16x32_bf16 a[188:191], a[24:27], a[120:123], a[188:191]\n" \
"ds_read_b128 a[16:19], v72 offset:4096\n" \
"v_mfma_f32_16x16x32_bf16 a[188:191], a[28:31], a[124:127], a[188:191]\n" \
"buffer_load_dwordx4 v66, s[36:39], s82 offen offset:2048 lds\n" \
"v_mfma_f32_16x16x32_bf16 a[184:187], a[24:27], a[112:115], a[184:187]\n" \
"ds_read_b64_tr_b16 a[80:81], v74 offset:33280\n" \
"v_mfma_f32_16x16x32_bf16 a[184:187], a[28:31], a[116:119], a[184:187]\n" \
"v_mfma_f32_16x16x32_bf16 a[180:183], a[24:27], a[104:107], a[180:183]\n" \
"ds_read_b128 a[20:23], v73 offset:4096\n" \
"v_mfma_f32_16x16x32_bf16 a[180:183], a[28:31], a[108:111], a[180:183]\n" \
"buffer_load_dwordx4 v67, s[36:39], s82 offen offset:3072 lds\n" \
"v_mfma_f32_16x16x32_bf16 a[176:179], a[24:27], a[96:99], a[176:179]\n" \
"s_mov_b64 s[64:65], s[12:13]\n" \
"s_add_u32 s68, s67, 0x100000\n" \
"ds_read_b64_tr_b16 a[82:83], v74 offset:37376\n" \
"v_mfma_f32_16x16x32_bf16 a[176:179], a[28:31], a[100:103], a[176:179]\n" \
"s_add_u32 s64, s64, s68\n" \
"s_addc_u32 s65, s65, 0\n" \
"v_mfma_f32_16x16x32_bf16 a[192:195], a[32:35], a[96:99], a[192:195]\n" \
"s_xor_b32 s82, s82, 0x80\n" \
"ds_read_b128 a[24:27], v72 offset:6144\n" \
"v_mfma_f32_16x16x32_bf16 a[192:195], a[36:39], a[100:103], a[192:195]\n" \
"global_load_dword v238, v234, s[64:65]\n" \
"v_mfma_f32_16x16x32_bf16 a[196:199], a[32:35], a[104:107], a[196:199]\n" \
"s_mov_b64 s[64:65], s[12:13]\n" \
"s_add_u32 s68, s67, 0x500000\n" \
"ds_read_b64_tr_b16 a[84:85], v74 offset:49664\n" \
"v_mfma_f32_16x16x32_bf16 a[196:199], a[36:39], a[108:111], a[196:199]\n" \
"s_add_u32 s64, s64, s68\n" \
"s_addc_u32 s65, s65, 0\n" \
"v_mfma_f32_16x16x32_bf16 a[200:203], a[32:35], a[112:115], a[200:203]\n" \
"ds_read_b128 a[28:31], v73 offset:6144\n" \
"v_mfma_f32_16x16x32_bf16 a[200:203], a[36:39], a[116:119], a[200:203]\n" \
"global_load_dword v238, v234, s[64:65]\n" \
"v_mfma_f32_16x16x32_bf16 a[204:207], a[32:35], a[120:123], a[204:207]\n" \
"s_mov_b64 s[64:65], s[12:13]\n" \
"s_add_u32 s68, s67, 0x200000\n" \
"ds_read_b64_tr_b16 a[86:87], v74 offset:53760\n" \
"v_mfma_f32_16x16x32_bf16 a[204:207], a[36:39], a[124:127], a[204:207]\n" \
"s_add_u32 s64, s64, s68\n" \
"s_addc_u32 s65, s65, 0\n" \
"v_mfma_f32_16x16x32_bf16 a[220:223], a[40:43], a[120:123], a[220:223]\n" \
"ds_read_b128 a[32:35], v72 offset:8192\n" \
"v_mfma_f32_16x16x32_bf16 a[220:223], a[44:47], a[124:127], a[220:223]\n" \
"global_load_dword v238, v234, s[64:65]\n" \
"v_mfma_f32_16x16x32_bf16 a[216:219], a[40:43], a[112:115], a[216:219]\n" \
"ds_read_b64_tr_b16 a[88:89], v75 offset:33280\n" \
"v_mfma_f32_16x16x32_bf16 a[216:219], a[44:47], a[116:119], a[216:219]\n" \
"v_mfma_f32_16x16x32_bf16 a[212:215], a[40:43], a[104:107], a[212:215]\n" \
"ds_read_b128 a[36:39], v73 offset:8192\n" \
"v_mfma_f32_16x16x32_bf16 a[212:215], a[44:47], a[108:111], a[212:215]\n" \
"v_mfma_f32_16x16x32_bf16 a[208:211], a[40:43], a[96:99], a[208:211]\n" \
"ds_read_b64_tr_b16 a[90:91], v75 offset:37376\n" \
"v_mfma_f32_16x16x32_bf16 a[208:211], a[44:47], a[100:103], a[208:211]\n" \
"v_mfma_f32_16x16x32_bf16 a[224:227], a[48:51], a[96:99], a[224:227]\n" \
"ds_read_b128 a[40:43], v72 offset:10240\n" \
"v_mfma_f32_16x16x32_bf16 a[224:227], a[52:55], a[100:103], a[224:227]\n" \
"v_mfma_f32_16x16x32_bf16 a[228:231], a[48:51], a[104:107], a[228:231]\n" \
"ds_read_b64_tr_b16 a[92:93], v75 offset:49664\n" \
"v_mfma_f32_16x16x32_bf16 a[228:231], a[52:55], a[108:111], a[228:231]\n" \
"v_mfma_f32_16x16x32_bf16 a[232:235], a[48:51], a[112:115], a[232:235]\n" \
"ds_read_b128 a[44:47], v73 offset:10240\n" \
"v_mfma_f32_16x16x32_bf16 a[232:235], a[52:55], a[116:119], a[232:235]\n" \
"v_mfma_f32_16x16x32_bf16 a[236:239], a[48:51], a[120:123], a[236:239]\n" \
"ds_read_b64_tr_b16 a[94:95], v75 offset:53760\n" \
"v_mfma_f32_16x16x32_bf16 a[236:239], a[52:55], a[124:127], a[236:239]\n" \
"v_mfma_f32_16x16x32_bf16 a[252:255], a[56:59], a[120:123], a[252:255]\n" \
"ds_read_b128 a[48:51], v72 offset:12288\n" \
"v_mfma_f32_16x16x32_bf16 a[252:255], a[60:63], a[124:127], a[252:255]\n" \
"v_mfma_f32_16x16x32_bf16 a[248:251], a[56:59], a[112:115], a[248:251]\n" \
"v_mfma_f32_16x16x32_bf16 a[248:251], a[60:63], a[116:119], a[248:251]\n" \
"v_mfma_f32_16x16x32_bf16 a[244:247], a[56:59], a[104:107], a[244:247]\n" \
"ds_read_b128 a[52:55], v73 offset:12288\n" \
"v_mfma_f32_16x16x32_bf16 a[244:247], a[60:63], a[108:111], a[244:247]\n" \
"v_mfma_f32_16x16x32_bf16 a[240:243], a[56:59], a[96:99], a[240:243]\n" \
"v_mfma_f32_16x16x32_bf16 a[240:243], a[60:63], a[100:103], a[240:243]\n" \
"s_waitcnt lgkmcnt(1)\n" \
"v_mfma_f32_16x16x32_bf16 v[0:3], a[64:67], a[0:3], v[0:3]\n" \
"ds_read_b128 a[56:59], v72 offset:14336\n" \
"v_mfma_f32_16x16x32_bf16 v[0:3], a[68:71], a[4:7], v[0:3]\n" \
"v_mfma_f32_16x16x32_bf16 v[4:7], a[72:75], a[0:3], v[4:7]\n" \
"s_add_u32 m0, s74, 98304\n" \
"ds_read_b128 a[60:63], v73 offset:14336\n" \
"v_mfma_f32_16x16x32_bf16 v[4:7], a[76:79], a[4:7], v[4:7]\n" \
"buffer_load_dwordx4 v68, s[40:43], s83 offen offset:0 lds\n" \
"v_mfma_f32_16x16x32_bf16 v[8:11], a[80:83], a[0:3], v[8:11]\n" \
"ds_read_b64_tr_b16 a[96:97], v74 offset:33792\n" \
"v_mfma_f32_16x16x32_bf16 v[8:11], a[84:87], a[4:7], v[8:11]\n" \
"v_mfma_f32_16x16x32_bf16 v[12:15], a[88:91], a[0:3], v[12:15]\n" \
"ds_read_b64_tr_b16 a[98:99], v74 offset:37888\n" \
"v_mfma_f32_16x16x32_bf16 v[12:15], a[92:95], a[4:7], v[12:15]\n" \
"buffer_load_dwordx4 v69, s[44:47], s83 offen offset:1024 lds\n" \
"v_mfma_f32_16x16x32_bf16 v[28:31], a[88:91], a[8:11], v[28:31]\n" \
"ds_read_b64_tr_b16 a[100:101], v74 offset:50176\n" \
"v_mfma_f32_16x16x32_bf16 v[28:31], a[92:95], a[12:15], v[28:31]\n" \
"v_mfma_f32_16x16x32_bf16 v[24:27], a[80:83], a[8:11], v[24:27]\n" \
"ds_read_b64_tr_b16 a[102:103], v74 offset:54272\n" \
"v_mfma_f32_16x16x32_bf16 v[24:27], a[84:87], a[12:15], v[24:27]\n" \
"buffer_load_dwordx4 v70, s[40:43], s83 offen offset:2048 lds\n" \
"v_mfma_f32_16x16x32_bf16 v[20:23], a[72:75], a[8:11], v[20:23]\n" \
"ds_read_b64_tr_b16 a[104:105], v75 offset:33792\n" \
"v_mfma_f32_16x16x32_bf16 v[20:23], a[76:79], a[12:15], v[20:23]\n" \
"v_mfma_f32_16x16x32_bf16 v[16:19], a[64:67], a[8:11], v[16:19]\n" \
"ds_read_b64_tr_b16 a[106:107], v75 offset:37888\n" \
"v_mfma_f32_16x16x32_bf16 v[16:19], a[68:71], a[12:15], v[16:19]\n" \
"buffer_load_dwordx4 v71, s[44:47], s83 offen offset:3072 lds\n" \
"v_mfma_f32_16x16x32_bf16 v[32:35], a[64:67], a[16:19], v[32:35]\n" \
"ds_read_b64_tr_b16 a[108:109], v75 offset:50176\n" \
"v_mfma_f32_16x16x32_bf16 v[32:35], a[68:71], a[20:23], v[32:35]\n" \
"v_mfma_f32_16x16x32_bf16 v[36:39], a[72:75], a[16:19], v[36:39]\n" \
"s_add_u32 m0, s74, 102400\n" \
"ds_read_b64_tr_b16 a[110:111], v75 offset:54272\n" \
"v_mfma_f32_16x16x32_bf16 v[36:39], a[76:79], a[20:23], v[36:39]\n" \
"buffer_load_dwordx4 v68, s[40:43], s84 offen offset:0 lds\n" \
"v_mfma_f32_16x16x32_bf16 v[40:43], a[80:83], a[16:19], v[40:43]\n" \
"ds_read_b64_tr_b16 a[112:113], v74 offset:34304\n" \
"v_mfma_f32_16x16x32_bf16 v[40:43], a[84:87], a[20:23], v[40:43]\n" \
"v_mfma_f32_16x16x32_bf16 v[44:47], a[88:91], a[16:19], v[44:47]\n" \
"ds_read_b64_tr_b16 a[114:115], v74 offset:38400\n" \
"v_mfma_f32_16x16x32_bf16 v[44:47], a[92:95], a[20:23], v[44:47]\n" \
"buffer_load_dwordx4 v69, s[44:47], s84 offen offset:1024 lds\n" \
"v_mfma_f32_16x16x32_bf16 v[60:63], a[88:91], a[24:27], v[60:63]\n" \
"ds_read_b64_tr_b16 a[116:117], v74 offset:50688\n" \
"v_mfma_f32_16x16x32_bf16 v[60:63], a[92:95], a[28:31], v[60:63]\n" \
"v_mfma_f32_16x16x32_bf16 v[56:59], a[80:83], a[24:27], v[56:59]\n" \
"ds_read_b64_tr_b16 a[118:119], v74 offset:54784\n" \
"v_mfma_f32_16x16x32_bf16 v[56:59], a[84:87], a[28:31], v[56:59]\n" \
"buffer_load_dwordx4 v70, s[40:43], s84 offen offset:2048 lds\n" \
"v_mfma_f32_16x16x32_bf16 v[52:55], a[72:75], a[24:27], v[52:55]\n" \
"ds_read_b64_tr_b16 a[120:121], v75 offset:34304\n" \
"v_mfma_f32_16x16x32_bf16 v[52:55], a[76:79], a[28:31], v[52:55]\n" \
"v_mfma_f32_16x16x32_bf16 v[48:51], a[64:67], a[24:27], v[48:51]\n" \
"ds_read_b64_tr_b16 a[122:123], v75 offset:38400\n" \
"v_mfma_f32_16x16x32_bf16 v[48:51], a[68:71], a[28:31], v[48:51]\n" \
"buffer_load_dwordx4 v71, s[44:47], s84 offen offset:3072 lds\n" \
"v_mfma_f32_16x16x32_bf16 v[144:147], a[64:67], a[32:35], v[144:147]\n" \
"ds_read_b64_tr_b16 a[124:125], v75 offset:50688\n" \
"v_mfma_f32_16x16x32_bf16 v[144:147], a[68:71], a[36:39], v[144:147]\n" \
"v_mfma_f32_16x16x32_bf16 v[148:151], a[72:75], a[32:35], v[148:151]\n" \
"ds_read_b64_tr_b16 a[126:127], v75 offset:54784\n" \
"v_mfma_f32_16x16x32_bf16 v[148:151], a[76:79], a[36:39], v[148:151]\n" \
"v_mfma_f32_16x16x32_bf16 v[152:155], a[80:83], a[32:35], v[152:155]\n" \
"v_mfma_f32_16x16x32_bf16 v[152:155], a[84:87], a[36:39], v[152:155]\n" \
"v_mfma_f32_16x16x32_bf16 v[156:159], a[88:91], a[32:35], v[156:159]\n" \
"v_mfma_f32_16x16x32_bf16 v[156:159], a[92:95], a[36:39], v[156:159]\n" \
"v_mfma_f32_16x16x32_bf16 v[188:191], a[88:91], a[40:43], v[188:191]\n" \
"v_mfma_f32_16x16x32_bf16 v[188:191], a[92:95], a[44:47], v[188:191]\n" \
"v_mfma_f32_16x16x32_bf16 v[184:187], a[80:83], a[40:43], v[184:187]\n" \
"v_mfma_f32_16x16x32_bf16 v[184:187], a[84:87], a[44:47], v[184:187]\n" \
"v_mfma_f32_16x16x32_bf16 v[180:183], a[72:75], a[40:43], v[180:183]\n" \
"v_mfma_f32_16x16x32_bf16 v[180:183], a[76:79], a[44:47], v[180:183]\n" \
"v_mfma_f32_16x16x32_bf16 v[176:179], a[64:67], a[40:43], v[176:179]\n" \
"v_mfma_f32_16x16x32_bf16 v[176:179], a[68:71], a[44:47], v[176:179]\n" \
"s_waitcnt lgkmcnt(0)\n" \
"v_mfma_f32_16x16x32_bf16 v[192:195], a[64:67], a[48:51], v[192:195]\n" \
"v_mfma_f32_16x16x32_bf16 v[192:195], a[68:71], a[52:55], v[192:195]\n" \
"v_mfma_f32_16x16x32_bf16 v[196:199], a[72:75], a[48:51], v[196:199]\n" \
"v_mfma_f32_16x16x32_bf16 v[196:199], a[76:79], a[52:55], v[196:199]\n" \
"v_mfma_f32_16x16x32_bf16 v[200:203], a[80:83], a[48:51], v[200:203]\n" \
"v_mfma_f32_16x16x32_bf16 v[200:203], a[84:87], a[52:55], v[200:203]\n" \
"v_mfma_f32_16x16x32_bf16 v[204:207], a[88:91], a[48:51], v[204:207]\n" \
"v_mfma_f32_16x16x32_bf16 v[204:207], a[92:95], a[52:55], v[204:207]\n" \
"v_mfma_f32_16x16x32_bf16 v[220:223], a[88:91], a[56:59], v[220:223]\n" \
"v_mfma_f32_16x16x32_bf16 v[220:223], a[92:95], a[60:63], v[220:223]\n" \
"v_mfma_f32_16x16x32_bf16 v[216:219], a[80:83], a[56:59], v[216:219]\n" \
"v_mfma_f32_16x16x32_bf16 v[216:219], a[84:87], a[60:63], v[216:219]\n" \
"v_mfma_f32_16x16x32_bf16 v[212:215], a[72:75], a[56:59], v[212:215]\n" \
"v_mfma_f32_16x16x32_bf16 v[212:215], a[76:79], a[60:63], v[212:215]\n" \
"v_mfma_f32_16x16x32_bf16 v[208:211], a[64:67], a[56:59], v[208:211]\n" \
"v_mfma_f32_16x16x32_bf16 v[208:211], a[68:71], a[60:63], v[208:211]\n" \
"s_waitcnt vmcnt(11) lgkmcnt(0)\n" \
"s_barrier\n" \
"v_mfma_f32_16x16x32_bf16 a[128:131], a[0:3], a[96:99], a[128:131]\n" \
"s_add_u32 m0, s74, 32768\n" \
"ds_read_b64_tr_b16 a[64:65], v102 offset:0\n" \
"v_mfma_f32_16x16x32_bf16 a[128:131], a[4:7], a[100:103], a[128:131]\n" \
"buffer_load_dwordx4 v64, s[36:39], s81 offen offset:0 lds\n" \
"v_mfma_f32_16x16x32_bf16 a[132:135], a[0:3], a[104:107], a[132:135]\n" \
"ds_read_b64_tr_b16 a[66:67], v102 offset:4096\n" \
"v_mfma_f32_16x16x32_bf16 a[132:135], a[4:7], a[108:111], a[132:135]\n" \
"v_mfma_f32_16x16x32_bf16 a[136:139], a[0:3], a[112:115], a[136:139]\n" \
"ds_read_b64_tr_b16 a[68:69], v102 offset:16384\n" \
"v_mfma_f32_16x16x32_bf16 a[136:139], a[4:7], a[116:119], a[136:139]\n" \
"buffer_load_dwordx4 v65, s[36:39], s81 offen offset:1024 lds\n" \
"v_mfma_f32_16x16x32_bf16 a[140:143], a[0:3], a[120:123], a[140:143]\n" \
"ds_read_b64_tr_b16 a[70:71], v102 offset:20480\n" \
"v_mfma_f32_16x16x32_bf16 a[140:143], a[4:7], a[124:127], a[140:143]\n" \
"v_mfma_f32_16x16x32_bf16 a[156:159], a[8:11], a[120:123], a[156:159]\n" \
"ds_read_b128 a[0:3], v96 offset:0\n" \
"v_mfma_f32_16x16x32_bf16 a[156:159], a[12:15], a[124:127], a[156:159]\n" \
"buffer_load_dwordx4 v66, s[36:39], s81 offen offset:2048 lds\n" \
"v_mfma_f32_16x16x32_bf16 a[152:155], a[8:11], a[112:115], a[152:155]\n" \
"ds_read_b64_tr_b16 a[72:73], v103 offset:0\n" \
"v_mfma_f32_16x16x32_bf16 a[152:155], a[12:15], a[116:119], a[152:155]\n" \
"v_mfma_f32_16x16x32_bf16 a[148:151], a[8:11], a[104:107], a[148:151]\n" \
"ds_read_b128 a[4:7], v97 offset:0\n" \
"v_mfma_f32_16x16x32_bf16 a[148:151], a[12:15], a[108:111], a[148:151]\n" \
"buffer_load_dwordx4 v67, s[36:39], s81 offen offset:3072 lds\n" \
"v_mfma_f32_16x16x32_bf16 a[144:147], a[8:11], a[96:99], a[144:147]\n" \
"ds_read_b64_tr_b16 a[74:75], v103 offset:4096\n" \
"v_mfma_f32_16x16x32_bf16 a[144:147], a[12:15], a[100:103], a[144:147]\n" \
"v_mfma_f32_16x16x32_bf16 a[160:163], a[16:19], a[96:99], a[160:163]\n" \
"s_add_u32 m0, s74, 36864\n" \
"ds_read_b128 a[8:11], v96 offset:2048\n" \
"v_mfma_f32_16x16x32_bf16 a[160:163], a[20:23], a[100:103], a[160:163]\n" \
"buffer_load_dwordx4 v64, s[36:39], s82 offen offset:0 lds\n" \
"v_mfma_f32_16x16x32_bf16 a[164:167], a[16:19], a[104:107], a[164:167]\n" \
"ds_read_b64_tr_b16 a[76:77], v103 offset:16384\n" \
"v_mfma_f32_16x16x32_bf16 a[164:167], a[20:23], a[108:111], a[164:167]\n" \
"v_mfma_f32_16x16x32_bf16 a[168:171], a[16:19], a[112:115], a[168:171]\n" \
"ds_read_b128 a[12:15], v97 offset:2048\n" \
"v_mfma_f32_16x16x32_bf16 a[168:171], a[20:23], a[116:119], a[168:171]\n" \
"buffer_load_dwordx4 v65, s[36:39], s82 offen offset:1024 lds\n" \
"v_mfma_f32_16x16x32_bf16 a[172:175], a[16:19], a[120:123], a[172:175]\n" \
"ds_read_b64_tr_b16 a[78:79], v103 offset:20480\n" \
"v_mfma_f32_16x16x32_bf16 a[172:175], a[20:23], a[124:127], a[172:175]\n" \
"v_mfma_f32_16x16x32_bf16 a[188:191], a[24:27], a[120:123], a[188:191]\n" \
"ds_read_b128 a[16:19], v96 offset:4096\n" \
"v_mfma_f32_16x16x32_bf16 a[188:191], a[28:31], a[124:127], a[188:191]\n" \
"buffer_load_dwordx4 v66, s[36:39], s82 offen offset:2048 lds\n" \
"v_mfma_f32_16x16x32_bf16 a[184:187], a[24:27], a[112:115], a[184:187]\n" \
"ds_read_b64_tr_b16 a[80:81], v102 offset:512\n" \
"v_mfma_f32_16x16x32_bf16 a[184:187], a[28:31], a[116:119], a[184:187]\n" \
"v_mfma_f32_16x16x32_bf16 a[180:183], a[24:27], a[104:107], a[180:183]\n" \
"ds_read_b128 a[20:23], v97 offset:4096\n" \
"v_mfma_f32_16x16x32_bf16 a[180:183], a[28:31], a[108:111], a[180:183]\n" \
"buffer_load_dwordx4 v67, s[36:39], s82 offen offset:3072 lds\n" \
"v_mfma_f32_16x16x32_bf16 a[176:179], a[24:27], a[96:99], a[176:179]\n" \
"s_mov_b64 s[64:65], s[12:13]\n" \
"s_add_u32 s68, s67, 0x300000\n" \
"ds_read_b64_tr_b16 a[82:83], v102 offset:4608\n" \
"v_mfma_f32_16x16x32_bf16 a[176:179], a[28:31], a[100:103], a[176:179]\n" \
"s_add_u32 s64, s64, s68\n" \
"s_addc_u32 s65, s65, 0\n" \
"v_mfma_f32_16x16x32_bf16 a[192:195], a[32:35], a[96:99], a[192:195]\n" \
"ds_read_b128 a[24:27], v96 offset:6144\n" \
"v_mfma_f32_16x16x32_bf16 a[192:195], a[36:39], a[100:103], a[192:195]\n" \
"global_load_dword v238, v234, s[64:65]\n" \
"v_mfma_f32_16x16x32_bf16 a[196:199], a[32:35], a[104:107], a[196:199]\n" \
"s_mov_b64 s[64:65], s[12:13]\n" \
"s_add_u32 s68, s67, 0x400000\n" \
"ds_read_b64_tr_b16 a[84:85], v102 offset:16896\n" \
"v_mfma_f32_16x16x32_bf16 a[196:199], a[36:39], a[108:111], a[196:199]\n" \
"s_add_u32 s64, s64, s68\n" \
"s_addc_u32 s65, s65, 0\n" \
"v_mfma_f32_16x16x32_bf16 a[200:203], a[32:35], a[112:115], a[200:203]\n" \
"ds_read_b128 a[28:31], v97 offset:6144\n" \
"v_mfma_f32_16x16x32_bf16 a[200:203], a[36:39], a[116:119], a[200:203]\n" \
"global_load_dword v238, v234, s[64:65]\n" \
"v_mfma_f32_16x16x32_bf16 a[204:207], a[32:35], a[120:123], a[204:207]\n" \
"s_mov_b64 s[64:65], s[12:13]\n" \
"s_add_u32 s68, s67, 0x600000\n" \
"ds_read_b64_tr_b16 a[86:87], v102 offset:20992\n" \
"v_mfma_f32_16x16x32_bf16 a[204:207], a[36:39], a[124:127], a[204:207]\n" \
"s_add_u32 s64, s64, s68\n" \
"s_addc_u32 s65, s65, 0\n" \
"v_mfma_f32_16x16x32_bf16 a[220:223], a[40:43], a[120:123], a[220:223]\n" \
"ds_read_b128 a[32:35], v96 offset:8192\n" \
"v_mfma_f32_16x16x32_bf16 a[220:223], a[44:47], a[124:127], a[220:223]\n" \
"global_load_dword v238, v234, s[64:65]\n" \
"v_mfma_f32_16x16x32_bf16 a[216:219], a[40:43], a[112:115], a[216:219]\n" \
"ds_read_b64_tr_b16 a[88:89], v103 offset:512\n" \
"v_mfma_f32_16x16x32_bf16 a[216:219], a[44:47], a[116:119], a[216:219]\n" \
"v_mfma_f32_16x16x32_bf16 a[212:215], a[40:43], a[104:107], a[212:215]\n" \
"ds_read_b128 a[36:39], v97 offset:8192\n" \
"v_mfma_f32_16x16x32_bf16 a[212:215], a[44:47], a[108:111], a[212:215]\n" \
"v_mfma_f32_16x16x32_bf16 a[208:211], a[40:43], a[96:99], a[208:211]\n" \
"ds_read_b64_tr_b16 a[90:91], v103 offset:4608\n" \
"v_mfma_f32_16x16x32_bf16 a[208:211], a[44:47], a[100:103], a[208:211]\n" \
"v_mfma_f32_16x16x32_bf16 a[224:227], a[48:51], a[96:99], a[224:227]\n" \
"ds_read_b128 a[40:43], v96 offset:10240\n" \
"v_mfma_f32_16x16x32_bf16 a[224:227], a[52:55], a[100:103], a[224:227]\n" \
"v_mfma_f32_16x16x32_bf16 a[228:231], a[48:51], a[104:107], a[228:231]\n" \
"ds_read_b64_tr_b16 a[92:93], v103 offset:16896\n" \
"v_mfma_f32_16x16x32_bf16 a[228:231], a[52:55], a[108:111], a[228:231]\n" \
"v_mfma_f32_16x16x32_bf16 a[232:235], a[48:51], a[112:115], a[232:235]\n" \
"ds_read_b128 a[44:47], v97 offset:10240\n" \
"v_mfma_f32_16x16x32_bf16 a[232:235], a[52:55], a[116:119], a[232:235]\n" \
"v_mfma_f32_16x16x32_bf16 a[236:239], a[48:51], a[120:123], a[236:239]\n" \
"ds_read_b64_tr_b16 a[94:95], v103 offset:20992\n" \
"v_mfma_f32_16x16x32_bf16 a[236:239], a[52:55], a[124:127], a[236:239]\n" \
"v_mfma_f32_16x16x32_bf16 a[252:255], a[56:59], a[120:123], a[252:255]\n" \
"ds_read_b128 a[48:51], v96 offset:12288\n" \
"v_mfma_f32_16x16x32_bf16 a[252:255], a[60:63], a[124:127], a[252:255]\n" \
"v_mfma_f32_16x16x32_bf16 a[248:251], a[56:59], a[112:115], a[248:251]\n" \
"v_mfma_f32_16x16x32_bf16 a[248:251], a[60:63], a[116:119], a[248:251]\n" \
"v_mfma_f32_16x16x32_bf16 a[244:247], a[56:59], a[104:107], a[244:247]\n" \
"ds_read_b128 a[52:55], v97 offset:12288\n" \
"v_mfma_f32_16x16x32_bf16 a[244:247], a[60:63], a[108:111], a[244:247]\n" \
"v_mfma_f32_16x16x32_bf16 a[240:243], a[56:59], a[96:99], a[240:243]\n" \
"v_mfma_f32_16x16x32_bf16 a[240:243], a[60:63], a[100:103], a[240:243]\n" \
"s_waitcnt lgkmcnt(1)\n" \
"v_mfma_f32_16x16x32_bf16 v[0:3], a[64:67], a[0:3], v[0:3]\n" \
"ds_read_b128 a[56:59], v96 offset:14336\n" \
"Lpg1663_%=:\n" \
"Lst0x2_%=:\n" \
"s_cbranch_execz Lpg3897_%=\n" \
"v_mfma_f32_16x16x32_bf16 v[0:3], a[68:71], a[4:7], v[0:3]\n" \
"v_mfma_f32_16x16x32_bf16 v[4:7], a[72:75], a[0:3], v[4:7]\n" \
"s_add_u32 m0, s79, 0\n" \
"ds_read_b128 a[60:63], v97 offset:14336\n" \
"v_mfma_f32_16x16x32_bf16 v[4:7], a[76:79], a[4:7], v[4:7]\n" \
"buffer_load_dwordx4 v228, s[52:55], s78 offen offset:0 lds\n" \
"v_mfma_f32_16x16x32_bf16 v[8:11], a[80:83], a[0:3], v[8:11]\n" \
"ds_read_b64_tr_b16 a[96:97], v102 offset:1024\n" \
"v_mfma_f32_16x16x32_bf16 v[8:11], a[84:87], a[4:7], v[8:11]\n" \
"v_mfma_f32_16x16x32_bf16 v[12:15], a[88:91], a[0:3], v[12:15]\n" \
"ds_read_b64_tr_b16 a[98:99], v102 offset:5120\n" \
"v_mfma_f32_16x16x32_bf16 v[12:15], a[92:95], a[4:7], v[12:15]\n" \
"buffer_load_dwordx4 v229, s[52:55], s78 offen offset:1024 lds\n" \
"v_mfma_f32_16x16x32_bf16 v[28:31], a[88:91], a[8:11], v[28:31]\n" \
"ds_read_b64_tr_b16 a[100:101], v102 offset:17408\n" \
"v_mfma_f32_16x16x32_bf16 v[28:31], a[92:95], a[12:15], v[28:31]\n" \
"v_mfma_f32_16x16x32_bf16 v[24:27], a[80:83], a[8:11], v[24:27]\n" \
"s_xor_b32 s78, s78, 0x180000\n" \
"ds_read_b64_tr_b16 a[102:103], v102 offset:21504\n" \
"v_mfma_f32_16x16x32_bf16 v[24:27], a[84:87], a[12:15], v[24:27]\n" \
"buffer_load_dwordx4 v228, s[52:55], s89 offen offset:2048 lds\n" \
"v_mfma_f32_16x16x32_bf16 v[20:23], a[72:75], a[8:11], v[20:23]\n" \
"ds_read_b64_tr_b16 a[104:105], v103 offset:1024\n" \
"v_mfma_f32_16x16x32_bf16 v[20:23], a[76:79], a[12:15], v[20:23]\n" \
"v_mfma_f32_16x16x32_bf16 v[16:19], a[64:67], a[8:11], v[16:19]\n" \
"ds_read_b64_tr_b16 a[106:107], v103 offset:5120\n" \
"v_mfma_f32_16x16x32_bf16 v[16:19], a[68:71], a[12:15], v[16:19]\n" \
"buffer_load_dwordx4 v229, s[52:55], s89 offen offset:3072 lds\n" \
"v_mfma_f32_16x16x32_bf16 v[32:35], a[64:67], a[16:19], v[32:35]\n" \
"ds_read_b64_tr_b16 a[108:109], v103 offset:17408\n" \
"v_mfma_f32_16x16x32_bf16 v[32:35], a[68:71], a[20:23], v[32:35]\n" \
"s_xor_b32 s89, s89, 0x180000\n" \
"v_mfma_f32_16x16x32_bf16 v[36:39], a[72:75], a[16:19], v[36:39]\n" \
"s_add_u32 m0, s79, 16384\n" \
"ds_read_b64_tr_b16 a[110:111], v103 offset:21504\n" \
"v_mfma_f32_16x16x32_bf16 v[36:39], a[76:79], a[20:23], v[36:39]\n" \
"buffer_load_dwordx4 v228, s[52:55], s78 offen offset:0 lds\n" \
"v_mfma_f32_16x16x32_bf16 v[40:43], a[80:83], a[16:19], v[40:43]\n" \
"ds_read_b64_tr_b16 a[112:113], v102 offset:1536\n" \
"v_mfma_f32_16x16x32_bf16 v[40:43], a[84:87], a[20:23], v[40:43]\n" \
"v_mfma_f32_16x16x32_bf16 v[44:47], a[88:91], a[16:19], v[44:47]\n" \
"ds_read_b64_tr_b16 a[114:115], v102 offset:5632\n" \
"v_mfma_f32_16x16x32_bf16 v[44:47], a[92:95], a[20:23], v[44:47]\n" \
"buffer_load_dwordx4 v229, s[52:55], s78 offen offset:1024 lds\n" \
"v_mfma_f32_16x16x32_bf16 v[60:63], a[88:91], a[24:27], v[60:63]\n" \
"ds_read_b64_tr_b16 a[116:117], v102 offset:17920\n" \
"v_mfma_f32_16x16x32_bf16 v[60:63], a[92:95], a[28:31], v[60:63]\n" \
"v_mfma_f32_16x16x32_bf16 v[56:59], a[80:83], a[24:27], v[56:59]\n" \
"s_xor_b32 s78, s78, 0x400000\n" \
"ds_read_b64_tr_b16 a[118:119], v102 offset:22016\n" \
"v_mfma_f32_16x16x32_bf16 v[56:59], a[84:87], a[28:31], v[56:59]\n" \
"buffer_load_dwordx4 v228, s[52:55], s89 offen offset:2048 lds\n" \
"v_mfma_f32_16x16x32_bf16 v[52:55], a[72:75], a[24:27], v[52:55]\n" \
"ds_read_b64_tr_b16 a[120:121], v103 offset:1536\n" \
"v_mfma_f32_16x16x32_bf16 v[52:55], a[76:79], a[28:31], v[52:55]\n" \
"v_mfma_f32_16x16x32_bf16 v[48:51], a[64:67], a[24:27], v[48:51]\n" \
"ds_read_b64_tr_b16 a[122:123], v103 offset:5632\n" \
"v_mfma_f32_16x16x32_bf16 v[48:51], a[68:71], a[28:31], v[48:51]\n" \
"buffer_load_dwordx4 v229, s[52:55], s89 offen offset:3072 lds\n" \
"v_mfma_f32_16x16x32_bf16 v[144:147], a[64:67], a[32:35], v[144:147]\n" \
"ds_read_b64_tr_b16 a[124:125], v103 offset:17920\n" \
"v_mfma_f32_16x16x32_bf16 v[144:147], a[68:71], a[36:39], v[144:147]\n" \
"v_mfma_f32_16x16x32_bf16 v[148:151], a[72:75], a[32:35], v[148:151]\n" \
"s_xor_b32 s89, s89, 0x400000\n" \
"ds_read_b64_tr_b16 a[126:127], v103 offset:22016\n" \
"v_mfma_f32_16x16x32_bf16 v[148:151], a[76:79], a[36:39], v[148:151]\n" \
"v_mfma_f32_16x16x32_bf16 v[152:155], a[80:83], a[32:35], v[152:155]\n" \
"v_mfma_f32_16x16x32_bf16 v[152:155], a[84:87], a[36:39], v[152:155]\n" \
"v_mfma_f32_16x16x32_bf16 v[156:159], a[88:91], a[32:35], v[156:159]\n" \
"v_mfma_f32_16x16x32_bf16 v[156:159], a[92:95], a[36:39], v[156:159]\n" \
"v_mfma_f32_16x16x32_bf16 v[188:191], a[88:91], a[40:43], v[188:191]\n" \
"v_mfma_f32_16x16x32_bf16 v[188:191], a[92:95], a[44:47], v[188:191]\n" \
"v_mfma_f32_16x16x32_bf16 v[184:187], a[80:83], a[40:43], v[184:187]\n" \
"v_mfma_f32_16x16x32_bf16 v[184:187], a[84:87], a[44:47], v[184:187]\n" \
"v_mfma_f32_16x16x32_bf16 v[180:183], a[72:75], a[40:43], v[180:183]\n" \
"v_mfma_f32_16x16x32_bf16 v[180:183], a[76:79], a[44:47], v[180:183]\n" \
"v_mfma_f32_16x16x32_bf16 v[176:179], a[64:67], a[40:43], v[176:179]\n" \
"v_mfma_f32_16x16x32_bf16 v[176:179], a[68:71], a[44:47], v[176:179]\n" \
"s_waitcnt lgkmcnt(0)\n" \
"v_mfma_f32_16x16x32_bf16 v[192:195], a[64:67], a[48:51], v[192:195]\n" \
"v_mfma_f32_16x16x32_bf16 v[192:195], a[68:71], a[52:55], v[192:195]\n" \
"v_mfma_f32_16x16x32_bf16 v[196:199], a[72:75], a[48:51], v[196:199]\n" \
"v_mfma_f32_16x16x32_bf16 v[196:199], a[76:79], a[52:55], v[196:199]\n" \
"v_mfma_f32_16x16x32_bf16 v[200:203], a[80:83], a[48:51], v[200:203]\n" \
"v_mfma_f32_16x16x32_bf16 v[200:203], a[84:87], a[52:55], v[200:203]\n" \
"v_mfma_f32_16x16x32_bf16 v[204:207], a[88:91], a[48:51], v[204:207]\n" \
"v_mfma_f32_16x16x32_bf16 v[204:207], a[92:95], a[52:55], v[204:207]\n" \
"v_mfma_f32_16x16x32_bf16 v[220:223], a[88:91], a[56:59], v[220:223]\n" \
"v_mfma_f32_16x16x32_bf16 v[220:223], a[92:95], a[60:63], v[220:223]\n" \
"v_mfma_f32_16x16x32_bf16 v[216:219], a[80:83], a[56:59], v[216:219]\n" \
"v_mfma_f32_16x16x32_bf16 v[216:219], a[84:87], a[60:63], v[216:219]\n" \
"v_mfma_f32_16x16x32_bf16 v[212:215], a[72:75], a[56:59], v[212:215]\n" \
"v_mfma_f32_16x16x32_bf16 v[212:215], a[76:79], a[60:63], v[212:215]\n" \
"v_mfma_f32_16x16x32_bf16 v[208:211], a[64:67], a[56:59], v[208:211]\n" \
"v_mfma_f32_16x16x32_bf16 v[208:211], a[68:71], a[60:63], v[208:211]\n" \
"s_waitcnt vmcnt(11) lgkmcnt(0)\n" \
"s_barrier\n" \
"v_mfma_f32_16x16x32_bf16 a[128:131], a[0:3], a[96:99], a[128:131]\n" \
"s_add_u32 m0, s74, 131072\n" \
"ds_read_b64_tr_b16 a[64:65], v98 offset:32768\n" \
"v_mfma_f32_16x16x32_bf16 a[128:131], a[4:7], a[100:103], a[128:131]\n" \
"buffer_load_dwordx4 v224, s[48:51], s75 offen offset:0 lds\n" \
"v_mfma_f32_16x16x32_bf16 a[132:135], a[0:3], a[104:107], a[132:135]\n" \
"ds_read_b64_tr_b16 a[66:67], v98 offset:36864\n" \
"v_mfma_f32_16x16x32_bf16 a[132:135], a[4:7], a[108:111], a[132:135]\n" \
"v_mfma_f32_16x16x32_bf16 a[136:139], a[0:3], a[112:115], a[136:139]\n" \
"ds_read_b64_tr_b16 a[68:69], v98 offset:49152\n" \
"v_mfma_f32_16x16x32_bf16 a[136:139], a[4:7], a[116:119], a[136:139]\n" \
"buffer_load_dwordx4 v225, s[48:51], s75 offen offset:1024 lds\n" \
"v_mfma_f32_16x16x32_bf16 a[140:143], a[0:3], a[120:123], a[140:143]\n" \
"ds_read_b64_tr_b16 a[70:71], v98 offset:53248\n" \
"v_mfma_f32_16x16x32_bf16 a[140:143], a[4:7], a[124:127], a[140:143]\n" \
"v_mfma_f32_16x16x32_bf16 a[156:159], a[8:11], a[120:123], a[156:159]\n" \
"ds_read_b128 a[0:3], v72 offset:32768\n" \
"v_mfma_f32_16x16x32_bf16 a[156:159], a[12:15], a[124:127], a[156:159]\n" \
"buffer_load_dwordx4 v226, s[48:51], s75 offen offset:2048 lds\n" \
"v_mfma_f32_16x16x32_bf16 a[152:155], a[8:11], a[112:115], a[152:155]\n" \
"ds_read_b64_tr_b16 a[72:73], v99 offset:32768\n" \
"v_mfma_f32_16x16x32_bf16 a[152:155], a[12:15], a[116:119], a[152:155]\n" \
"v_mfma_f32_16x16x32_bf16 a[148:151], a[8:11], a[104:107], a[148:151]\n" \
"ds_read_b128 a[4:7], v73 offset:32768\n" \
"v_mfma_f32_16x16x32_bf16 a[148:151], a[12:15], a[108:111], a[148:151]\n" \
"buffer_load_dwordx4 v227, s[48:51], s75 offen offset:3072 lds\n" \
"v_mfma_f32_16x16x32_bf16 a[144:147], a[8:11], a[96:99], a[144:147]\n" \
"ds_read_b64_tr_b16 a[74:75], v99 offset:36864\n" \
"v_mfma_f32_16x16x32_bf16 a[144:147], a[12:15], a[100:103], a[144:147]\n" \
"s_xor_b32 s75, s75, 0x180\n" \
"v_mfma_f32_16x16x32_bf16 a[160:163], a[16:19], a[96:99], a[160:163]\n" \
"s_add_u32 m0, s74, 135168\n" \
"ds_read_b128 a[8:11], v72 offset:34816\n" \
"v_mfma_f32_16x16x32_bf16 a[160:163], a[20:23], a[100:103], a[160:163]\n" \
"buffer_load_dwordx4 v224, s[48:51], s77 offen offset:0 lds\n" \
"v_mfma_f32_16x16x32_bf16 a[164:167], a[16:19], a[104:107], a[164:167]\n" \
"ds_read_b64_tr_b16 a[76:77], v99 offset:49152\n" \
"v_mfma_f32_16x16x32_bf16 a[164:167], a[20:23], a[108:111], a[164:167]\n" \
"v_mfma_f32_16x16x32_bf16 a[168:171], a[16:19], a[112:115], a[168:171]\n" \
"ds_read_b128 a[12:15], v73 offset:34816\n" \
"v_mfma_f32_16x16x32_bf16 a[168:171], a[20:23], a[116:119], a[168:171]\n" \
"buffer_load_dwordx4 v225, s[48:51], s77 offen offset:1024 lds\n" \
"v_mfma_f32_16x16x32_bf16 a[172:175], a[16:19], a[120:123], a[172:175]\n" \
"ds_read_b64_tr_b16 a[78:79], v99 offset:53248\n" \
"v_mfma_f32_16x16x32_bf16 a[172:175], a[20:23], a[124:127], a[172:175]\n" \
"v_mfma_f32_16x16x32_bf16 a[188:191], a[24:27], a[120:123], a[188:191]\n" \
"ds_read_b128 a[16:19], v72 offset:36864\n" \
"v_mfma_f32_16x16x32_bf16 a[188:191], a[28:31], a[124:127], a[188:191]\n" \
"buffer_load_dwordx4 v226, s[48:51], s77 offen offset:2048 lds\n" \
"v_mfma_f32_16x16x32_bf16 a[184:187], a[24:27], a[112:115], a[184:187]\n" \
"ds_read_b64_tr_b16 a[80:81], v98 offset:33280\n" \
"v_mfma_f32_16x16x32_bf16 a[184:187], a[28:31], a[116:119], a[184:187]\n" \
"v_mfma_f32_16x16x32_bf16 a[180:183], a[24:27], a[104:107], a[180:183]\n" \
"ds_read_b128 a[20:23], v73 offset:36864\n" \
"v_mfma_f32_16x16x32_bf16 a[180:183], a[28:31], a[108:111], a[180:183]\n" \
"buffer_load_dwordx4 v227, s[48:51], s77 offen offset:3072 lds\n" \
"v_mfma_f32_16x16x32_bf16 a[176:179], a[24:27], a[96:99], a[176:179]\n" \
"s_mov_b64 s[64:65], s[12:13]\n" \
"s_add_u32 s68, s67, 0x700000\n" \
"ds_read_b64_tr_b16 a[82:83], v98 offset:37376\n" \
"v_mfma_f32_16x16x32_bf16 a[176:179], a[28:31], a[100:103], a[176:179]\n" \
"s_add_u32 s64, s64, s68\n" \
"s_addc_u32 s65, s65, 0\n" \
"v_mfma_f32_16x16x32_bf16 a[192:195], a[32:35], a[96:99], a[192:195]\n" \
"s_xor_b32 s77, s77, 0x180\n" \
"ds_read_b128 a[24:27], v72 offset:38912\n" \
"v_mfma_f32_16x16x32_bf16 a[192:195], a[36:39], a[100:103], a[192:195]\n" \
"global_load_dword v238, v234, s[64:65]\n" \
"v_mfma_f32_16x16x32_bf16 a[196:199], a[32:35], a[104:107], a[196:199]\n" \
"ds_read_b64_tr_b16 a[84:85], v98 offset:49664\n" \
"v_mfma_f32_16x16x32_bf16 a[196:199], a[36:39], a[108:111], a[196:199]\n" \
"v_mfma_f32_16x16x32_bf16 a[200:203], a[32:35], a[112:115], a[200:203]\n" \
"s_lshr_b32 s68, s66, 2\n" \
"s_and_b32 s68, s68, 0xffff\n" \
"s_add_u32 s68, s68, 65585152\n" \
"ds_read_b128 a[28:31], v73 offset:38912\n" \
"v_mfma_f32_16x16x32_bf16 a[200:203], a[36:39], a[116:119], a[200:203]\n" \
"buffer_load_dword v238, v242, s[56:59], s68 offen sc1\n" \
"v_mfma_f32_16x16x32_bf16 a[204:207], a[32:35], a[120:123], a[204:207]\n" \
"ds_read_b64_tr_b16 a[86:87], v98 offset:53760\n" \
"v_mfma_f32_16x16x32_bf16 a[204:207], a[36:39], a[124:127], a[204:207]\n" \
"v_mfma_f32_16x16x32_bf16 a[220:223], a[40:43], a[120:123], a[220:223]\n" \
"ds_read_b128 a[32:35], v72 offset:40960\n" \
"v_mfma_f32_16x16x32_bf16 a[220:223], a[44:47], a[124:127], a[220:223]\n" \
"v_mfma_f32_16x16x32_bf16 a[216:219], a[40:43], a[112:115], a[216:219]\n" \
"ds_read_b64_tr_b16 a[88:89], v99 offset:33280\n" \
"v_mfma_f32_16x16x32_bf16 a[216:219], a[44:47], a[116:119], a[216:219]\n" \
"v_mfma_f32_16x16x32_bf16 a[212:215], a[40:43], a[104:107], a[212:215]\n" \
"ds_read_b128 a[36:39], v73 offset:40960\n" \
"v_mfma_f32_16x16x32_bf16 a[212:215], a[44:47], a[108:111], a[212:215]\n" \
"v_mfma_f32_16x16x32_bf16 a[208:211], a[40:43], a[96:99], a[208:211]\n" \
"ds_read_b64_tr_b16 a[90:91], v99 offset:37376\n" \
"v_mfma_f32_16x16x32_bf16 a[208:211], a[44:47], a[100:103], a[208:211]\n" \
"v_mfma_f32_16x16x32_bf16 a[224:227], a[48:51], a[96:99], a[224:227]\n" \
"ds_read_b128 a[40:43], v72 offset:43008\n" \
"v_mfma_f32_16x16x32_bf16 a[224:227], a[52:55], a[100:103], a[224:227]\n" \
"v_mfma_f32_16x16x32_bf16 a[228:231], a[48:51], a[104:107], a[228:231]\n" \
"ds_read_b64_tr_b16 a[92:93], v99 offset:49664\n" \
"v_mfma_f32_16x16x32_bf16 a[228:231], a[52:55], a[108:111], a[228:231]\n" \
"v_mfma_f32_16x16x32_bf16 a[232:235], a[48:51], a[112:115], a[232:235]\n" \
"ds_read_b128 a[44:47], v73 offset:43008\n" \
"v_mfma_f32_16x16x32_bf16 a[232:235], a[52:55], a[116:119], a[232:235]\n" \
"v_mfma_f32_16x16x32_bf16 a[236:239], a[48:51], a[120:123], a[236:239]\n" \
"ds_read_b64_tr_b16 a[94:95], v99 offset:53760\n" \
"v_mfma_f32_16x16x32_bf16 a[236:239], a[52:55], a[124:127], a[236:239]\n" \
"v_mfma_f32_16x16x32_bf16 a[252:255], a[56:59], a[120:123], a[252:255]\n" \
"ds_read_b128 a[48:51], v72 offset:45056\n" \
"v_mfma_f32_16x16x32_bf16 a[252:255], a[60:63], a[124:127], a[252:255]\n" \
"v_mfma_f32_16x16x32_bf16 a[248:251], a[56:59], a[112:115], a[248:251]\n" \
"v_mfma_f32_16x16x32_bf16 a[248:251], a[60:63], a[116:119], a[248:251]\n" \
"v_mfma_f32_16x16x32_bf16 a[244:247], a[56:59], a[104:107], a[244:247]\n" \
"ds_read_b128 a[52:55], v73 offset:45056\n" \
"v_mfma_f32_16x16x32_bf16 a[244:247], a[60:63], a[108:111], a[244:247]\n" \
"v_mfma_f32_16x16x32_bf16 a[240:243], a[56:59], a[96:99], a[240:243]\n" \
"v_mfma_f32_16x16x32_bf16 a[240:243], a[60:63], a[100:103], a[240:243]\n" \
"s_waitcnt lgkmcnt(1)\n" \
"v_mfma_f32_16x16x32_bf16 v[0:3], a[64:67], a[0:3], v[0:3]\n" \
"ds_read_b128 a[56:59], v72 offset:47104\n" \
"v_mfma_f32_16x16x32_bf16 v[0:3], a[68:71], a[4:7], v[0:3]\n" \
"v_mfma_f32_16x16x32_bf16 v[4:7], a[72:75], a[0:3], v[4:7]\n" \
"s_add_u32 m0, s74, 65536\n" \
"ds_read_b128 a[60:63], v73 offset:47104\n" \
"v_mfma_f32_16x16x32_bf16 v[4:7], a[76:79], a[4:7], v[4:7]\n" \
"buffer_load_dwordx4 v224, s[48:51], s75 offen offset:0 lds\n" \
"v_mfma_f32_16x16x32_bf16 v[8:11], a[80:83], a[0:3], v[8:11]\n" \
"ds_read_b64_tr_b16 a[96:97], v98 offset:33792\n" \
"v_mfma_f32_16x16x32_bf16 v[8:11], a[84:87], a[4:7], v[8:11]\n" \
"v_mfma_f32_16x16x32_bf16 v[12:15], a[88:91], a[0:3], v[12:15]\n" \
"ds_read_b64_tr_b16 a[98:99], v98 offset:37888\n" \
"v_mfma_f32_16x16x32_bf16 v[12:15], a[92:95], a[4:7], v[12:15]\n" \
"buffer_load_dwordx4 v225, s[48:51], s75 offen offset:1024 lds\n" \
"v_mfma_f32_16x16x32_bf16 v[28:31], a[88:91], a[8:11], v[28:31]\n" \
"ds_read_b64_tr_b16 a[100:101], v98 offset:50176\n" \
"v_mfma_f32_16x16x32_bf16 v[28:31], a[92:95], a[12:15], v[28:31]\n" \
"v_mfma_f32_16x16x32_bf16 v[24:27], a[80:83], a[8:11], v[24:27]\n" \
"ds_read_b64_tr_b16 a[102:103], v98 offset:54272\n" \
"v_mfma_f32_16x16x32_bf16 v[24:27], a[84:87], a[12:15], v[24:27]\n" \
"buffer_load_dwordx4 v226, s[48:51], s75 offen offset:2048 lds\n" \
"v_mfma_f32_16x16x32_bf16 v[20:23], a[72:75], a[8:11], v[20:23]\n" \
"ds_read_b64_tr_b16 a[104:105], v99 offset:33792\n" \
"v_mfma_f32_16x16x32_bf16 v[20:23], a[76:79], a[12:15], v[20:23]\n" \
"v_mfma_f32_16x16x32_bf16 v[16:19], a[64:67], a[8:11], v[16:19]\n" \
"ds_read_b64_tr_b16 a[106:107], v99 offset:37888\n" \
"v_mfma_f32_16x16x32_bf16 v[16:19], a[68:71], a[12:15], v[16:19]\n" \
"buffer_load_dwordx4 v227, s[48:51], s75 offen offset:3072 lds\n" \
"v_mfma_f32_16x16x32_bf16 v[32:35], a[64:67], a[16:19], v[32:35]\n" \
"ds_read_b64_tr_b16 a[108:109], v99 offset:50176\n" \
"v_mfma_f32_16x16x32_bf16 v[32:35], a[68:71], a[20:23], v[32:35]\n" \
"s_xor_b32 s75, s75, 0x400\n" \
"v_mfma_f32_16x16x32_bf16 v[36:39], a[72:75], a[16:19], v[36:39]\n" \
"s_add_u32 m0, s74, 69632\n" \
"ds_read_b64_tr_b16 a[110:111], v99 offset:54272\n" \
"v_mfma_f32_16x16x32_bf16 v[36:39], a[76:79], a[20:23], v[36:39]\n" \
"buffer_load_dwordx4 v224, s[48:51], s77 offen offset:0 lds\n" \
"v_mfma_f32_16x16x32_bf16 v[40:43], a[80:83], a[16:19], v[40:43]\n" \
"ds_read_b64_tr_b16 a[112:113], v98 offset:34304\n" \
"v_mfma_f32_16x16x32_bf16 v[40:43], a[84:87], a[20:23], v[40:43]\n" \
"v_mfma_f32_16x16x32_bf16 v[44:47], a[88:91], a[16:19], v[44:47]\n" \
"ds_read_b64_tr_b16 a[114:115], v98 offset:38400\n" \
"v_mfma_f32_16x16x32_bf16 v[44:47], a[92:95], a[20:23], v[44:47]\n" \
"buffer_load_dwordx4 v225, s[48:51], s77 offen offset:1024 lds\n" \
"v_mfma_f32_16x16x32_bf16 v[60:63], a[88:91], a[24:27], v[60:63]\n" \
"ds_read_b64_tr_b16 a[116:117], v98 offset:50688\n" \
"v_mfma_f32_16x16x32_bf16 v[60:63], a[92:95], a[28:31], v[60:63]\n" \
"v_mfma_f32_16x16x32_bf16 v[56:59], a[80:83], a[24:27], v[56:59]\n" \
"ds_read_b64_tr_b16 a[118:119], v98 offset:54784\n" \
"v_mfma_f32_16x16x32_bf16 v[56:59], a[84:87], a[28:31], v[56:59]\n" \
"buffer_load_dwordx4 v226, s[48:51], s77 offen offset:2048 lds\n" \
"v_mfma_f32_16x16x32_bf16 v[52:55], a[72:75], a[24:27], v[52:55]\n" \
"ds_read_b64_tr_b16 a[120:121], v99 offset:34304\n" \
"v_mfma_f32_16x16x32_bf16 v[52:55], a[76:79], a[28:31], v[52:55]\n" \
"v_mfma_f32_16x16x32_bf16 v[48:51], a[64:67], a[24:27], v[48:51]\n" \
"ds_read_b64_tr_b16 a[122:123], v99 offset:38400\n" \
"v_mfma_f32_16x16x32_bf16 v[48:51], a[68:71], a[28:31], v[48:51]\n" \
"buffer_load_dwordx4 v227, s[48:51], s77 offen offset:3072 lds\n" \
"v_mfma_f32_16x16x32_bf16 v[144:147], a[64:67], a[32:35], v[144:147]\n" \
"ds_read_b64_tr_b16 a[124:125], v99 offset:50688\n" \
"v_mfma_f32_16x16x32_bf16 v[144:147], a[68:71], a[36:39], v[144:147]\n" \
"v_mfma_f32_16x16x32_bf16 v[148:151], a[72:75], a[32:35], v[148:151]\n" \
"s_xor_b32 s77, s77, 0x400\n" \
"ds_read_b64_tr_b16 a[126:127], v99 offset:54784\n" \
"v_mfma_f32_16x16x32_bf16 v[148:151], a[76:79], a[36:39], v[148:151]\n" \
"v_mfma_f32_16x16x32_bf16 v[152:155], a[80:83], a[32:35], v[152:155]\n" \
"v_mfma_f32_16x16x32_bf16 v[152:155], a[84:87], a[36:39], v[152:155]\n" \
"v_mfma_f32_16x16x32_bf16 v[156:159], a[88:91], a[32:35], v[156:159]\n" \
"v_mfma_f32_16x16x32_bf16 v[156:159], a[92:95], a[36:39], v[156:159]\n" \
"v_mfma_f32_16x16x32_bf16 v[188:191], a[88:91], a[40:43], v[188:191]\n" \
"v_mfma_f32_16x16x32_bf16 v[188:191], a[92:95], a[44:47], v[188:191]\n" \
"v_mfma_f32_16x16x32_bf16 v[184:187], a[80:83], a[40:43], v[184:187]\n" \
"v_mfma_f32_16x16x32_bf16 v[184:187], a[84:87], a[44:47], v[184:187]\n" \
"v_mfma_f32_16x16x32_bf16 v[180:183], a[72:75], a[40:43], v[180:183]\n" \
"v_mfma_f32_16x16x32_bf16 v[180:183], a[76:79], a[44:47], v[180:183]\n" \
"v_mfma_f32_16x16x32_bf16 v[176:179], a[64:67], a[40:43], v[176:179]\n" \
"v_mfma_f32_16x16x32_bf16 v[176:179], a[68:71], a[44:47], v[176:179]\n" \
"s_waitcnt lgkmcnt(0)\n" \
"v_mfma_f32_16x16x32_bf16 v[192:195], a[64:67], a[48:51], v[192:195]\n" \
"v_mfma_f32_16x16x32_bf16 v[192:195], a[68:71], a[52:55], v[192:195]\n" \
"v_mfma_f32_16x16x32_bf16 v[196:199], a[72:75], a[48:51], v[196:199]\n" \
"v_mfma_f32_16x16x32_bf16 v[196:199], a[76:79], a[52:55], v[196:199]\n" \
"v_mfma_f32_16x16x32_bf16 v[200:203], a[80:83], a[48:51], v[200:203]\n" \
"v_mfma_f32_16x16x32_bf16 v[200:203], a[84:87], a[52:55], v[200:203]\n" \
"v_mfma_f32_16x16x32_bf16 v[204:207], a[88:91], a[48:51], v[204:207]\n" \
"v_mfma_f32_16x16x32_bf16 v[204:207], a[92:95], a[52:55], v[204:207]\n" \
"v_mfma_f32_16x16x32_bf16 v[220:223], a[88:91], a[56:59], v[220:223]\n" \
"v_mfma_f32_16x16x32_bf16 v[220:223], a[92:95], a[60:63], v[220:223]\n" \
"v_mfma_f32_16x16x32_bf16 v[216:219], a[80:83], a[56:59], v[216:219]\n" \
"v_mfma_f32_16x16x32_bf16 v[216:219], a[84:87], a[60:63], v[216:219]\n" \
"v_mfma_f32_16x16x32_bf16 v[212:215], a[72:75], a[56:59], v[212:215]\n" \
"v_mfma_f32_16x16x32_bf16 v[212:215], a[76:79], a[60:63], v[212:215]\n" \
"v_mfma_f32_16x16x32_bf16 v[208:211], a[64:67], a[56:59], v[208:211]\n" \
"v_mfma_f32_16x16x32_bf16 v[208:211], a[68:71], a[60:63], v[208:211]\n" \
"s_waitcnt vmcnt(10) lgkmcnt(0)\n" \
"s_barrier\n" \
"v_mfma_f32_16x16x32_bf16 a[128:131], a[0:3], a[96:99], a[128:131]\n" \
"s_add_u32 m0, s74, 98304\n" \
"v_mfma_f32_16x16x32_bf16 a[128:131], a[4:7], a[100:103], a[128:131]\n" \
"buffer_load_dwordx4 v224, s[48:51], s75 offen offset:0 lds\n" \
"v_mfma_f32_16x16x32_bf16 a[132:135], a[0:3], a[104:107], a[132:135]\n" \
"ds_read_b64_tr_b16 a[64:65], v232 offset:0\n" \
"ds_read_b64_tr_b16 a[66:67], v232 offset:2048\n" \
"v_mfma_f32_16x16x32_bf16 a[132:135], a[4:7], a[108:111], a[132:135]\n" \
"v_mfma_f32_16x16x32_bf16 a[136:139], a[0:3], a[112:115], a[136:139]\n" \
"v_mfma_f32_16x16x32_bf16 a[136:139], a[4:7], a[116:119], a[136:139]\n" \
"buffer_load_dwordx4 v225, s[48:51], s75 offen offset:1024 lds\n" \
"v_mfma_f32_16x16x32_bf16 a[140:143], a[0:3], a[120:123], a[140:143]\n" \
"ds_read_b64_tr_b16 a[68:69], v232 offset:8192\n" \
"ds_read_b64_tr_b16 a[70:71], v232 offset:10240\n" \
"v_mfma_f32_16x16x32_bf16 a[140:143], a[4:7], a[124:127], a[140:143]\n" \
"v_mfma_f32_16x16x32_bf16 a[156:159], a[8:11], a[120:123], a[156:159]\n" \
"v_mul_f32 v192, s76, v192\n" \
"v_mul_f32 v193, s76, v193\n" \
"v_mfma_f32_16x16x32_bf16 a[156:159], a[12:15], a[124:127], a[156:159]\n" \
"buffer_load_dwordx4 v226, s[48:51], s75 offen offset:2048 lds\n" \
"v_mfma_f32_16x16x32_bf16 a[152:155], a[8:11], a[112:115], a[152:155]\n" \
"ds_read_b64_tr_b16 a[72:73], v233 offset:0\n" \
"ds_read_b64_tr_b16 a[74:75], v233 offset:2048\n" \
"v_mfma_f32_16x16x32_bf16 a[152:155], a[12:15], a[116:119], a[152:155]\n" \
"v_mul_f32 v194, s76, v194\n" \
"v_mul_f32 v195, s76, v195\n" \
"v_mfma_f32_16x16x32_bf16 a[148:151], a[8:11], a[104:107], a[148:151]\n" \
"v_cvt_pk_bf16_f32 v192, v192, v193\n" \
"v_cvt_pk_bf16_f32 v193, v194, v195\n" \
"v_mfma_f32_16x16x32_bf16 a[148:151], a[12:15], a[108:111], a[148:151]\n" \
"buffer_load_dwordx4 v227, s[48:51], s75 offen offset:3072 lds\n" \
"v_mfma_f32_16x16x32_bf16 a[144:147], a[8:11], a[96:99], a[144:147]\n" \
"ds_read_b64_tr_b16 a[76:77], v233 offset:8192\n" \
"ds_read_b64_tr_b16 a[78:79], v233 offset:10240\n" \
"v_mfma_f32_16x16x32_bf16 a[144:147], a[12:15], a[100:103], a[144:147]\n" \
"s_xor_b32 s75, s75, 0x500\n" \
"v_mul_f32 v196, s76, v196\n" \
"v_mul_f32 v197, s76, v197\n" \
"v_mfma_f32_16x16x32_bf16 a[160:163], a[16:19], a[96:99], a[160:163]\n" \
"s_add_u32 m0, s74, 102400\n" \
"v_mul_f32 v198, s76, v198\n" \
"v_mul_f32 v199, s76, v199\n" \
"v_mfma_f32_16x16x32_bf16 a[160:163], a[20:23], a[100:103], a[160:163]\n" \
"buffer_load_dwordx4 v224, s[48:51], s77 offen offset:0 lds\n" \
"v_mfma_f32_16x16x32_bf16 a[164:167], a[16:19], a[104:107], a[164:167]\n" \
"ds_read_b64_tr_b16 a[80:81], v232 offset:512\n" \
"ds_read_b64_tr_b16 a[82:83], v232 offset:2560\n" \
"v_mfma_f32_16x16x32_bf16 a[164:167], a[20:23], a[108:111], a[164:167]\n" \
"v_cvt_pk_bf16_f32 v194, v196, v197\n" \
"v_cvt_pk_bf16_f32 v195, v198, v199\n" \
"v_mfma_f32_16x16x32_bf16 a[168:171], a[16:19], a[112:115], a[168:171]\n" \
"v_mul_f32 v208, s76, v208\n" \
"v_mul_f32 v209, s76, v209\n" \
"v_mfma_f32_16x16x32_bf16 a[168:171], a[20:23], a[116:119], a[168:171]\n" \
"buffer_load_dwordx4 v225, s[48:51], s77 offen offset:1024 lds\n" \
"v_mfma_f32_16x16x32_bf16 a[172:175], a[16:19], a[120:123], a[172:175]\n" \
"ds_read_b64_tr_b16 a[84:85], v232 offset:8704\n" \
"ds_read_b64_tr_b16 a[86:87], v232 offset:10752\n" \
"v_mfma_f32_16x16x32_bf16 a[172:175], a[20:23], a[124:127], a[172:175]\n" \
"v_mul_f32 v210, s76, v210\n" \
"v_mul_f32 v211, s76, v211\n" \
"v_mfma_f32_16x16x32_bf16 a[188:191], a[24:27], a[120:123], a[188:191]\n" \
"v_cvt_pk_bf16_f32 v208, v208, v209\n" \
"v_cvt_pk_bf16_f32 v209, v210, v211\n" \
"v_mfma_f32_16x16x32_bf16 a[188:191], a[28:31], a[124:127], a[188:191]\n" \
"buffer_load_dwordx4 v226, s[48:51], s77 offen offset:2048 lds\n" \
"v_mfma_f32_16x16x32_bf16 a[184:187], a[24:27], a[112:115], a[184:187]\n" \
"ds_read_b64_tr_b16 a[88:89], v233 offset:512\n" \
"ds_read_b64_tr_b16 a[90:91], v233 offset:2560\n" \
"v_mfma_f32_16x16x32_bf16 a[184:187], a[28:31], a[116:119], a[184:187]\n" \
"v_mul_f32 v212, s76, v212\n" \
"v_mul_f32 v213, s76, v213\n" \
"v_mfma_f32_16x16x32_bf16 a[180:183], a[24:27], a[104:107], a[180:183]\n" \
"v_mul_f32 v214, s76, v214\n" \
"v_mul_f32 v215, s76, v215\n" \
"v_mfma_f32_16x16x32_bf16 a[180:183], a[28:31], a[108:111], a[180:183]\n" \
"buffer_load_dwordx4 v227, s[48:51], s77 offen offset:3072 lds\n" \
"v_mfma_f32_16x16x32_bf16 a[176:179], a[24:27], a[96:99], a[176:179]\n" \
"ds_read_b64_tr_b16 a[92:93], v233 offset:8704\n" \
"ds_read_b64_tr_b16 a[94:95], v233 offset:10752\n" \
"v_mfma_f32_16x16x32_bf16 a[176:179], a[28:31], a[100:103], a[176:179]\n" \
"s_xor_b32 s77, s77, 0x500\n" \
"v_cvt_pk_bf16_f32 v210, v212, v213\n" \
"v_cvt_pk_bf16_f32 v211, v214, v215\n" \
"v_mfma_f32_16x16x32_bf16 a[192:195], a[32:35], a[96:99], a[192:195]\n" \
"s_add_u32 m0, s79, 32768\n" \
"v_mul_f32 v200, s76, v200\n" \
"v_mul_f32 v201, s76, v201\n" \
"v_mfma_f32_16x16x32_bf16 a[192:195], a[36:39], a[100:103], a[192:195]\n" \
"buffer_load_dwordx4 v228, s[52:55], s78 offen offset:0 lds\n" \
"v_mfma_f32_16x16x32_bf16 a[196:199], a[32:35], a[104:107], a[196:199]\n" \
"ds_read_b128 a[0:3], v250 offset:0\n" \
"ds_read_b128 a[4:7], v251 offset:0\n" \
"v_mfma_f32_16x16x32_bf16 a[196:199], a[36:39], a[108:111], a[196:199]\n" \
"v_mul_f32 v202, s76, v202\n" \
"v_mul_f32 v203, s76, v203\n" \
"v_mfma_f32_16x16x32_bf16 a[200:203], a[32:35], a[112:115], a[200:203]\n" \
"v_cvt_pk_bf16_f32 v200, v200, v201\n" \
"v_cvt_pk_bf16_f32 v201, v202, v203\n" \
"v_mfma_f32_16x16x32_bf16 a[200:203], a[36:39], a[116:119], a[200:203]\n" \
"buffer_load_dwordx4 v229, s[52:55], s78 offen offset:1024 lds\n" \
"v_mfma_f32_16x16x32_bf16 a[204:207], a[32:35], a[120:123], a[204:207]\n" \
"ds_read_b128 a[8:11], v250 offset:2048\n" \
"ds_read_b128 a[12:15], v251 offset:2048\n" \
"v_mfma_f32_16x16x32_bf16 a[204:207], a[36:39], a[124:127], a[204:207]\n" \
"v_mul_f32 v204, s76, v204\n" \
"v_mul_f32 v205, s76, v205\n" \
"v_mfma_f32_16x16x32_bf16 a[220:223], a[40:43], a[120:123], a[220:223]\n" \
"s_xor_b32 s78, s78, 0x500000\n" \
"v_mul_f32 v206, s76, v206\n" \
"v_mul_f32 v207, s76, v207\n" \
"v_mfma_f32_16x16x32_bf16 a[220:223], a[44:47], a[124:127], a[220:223]\n" \
"buffer_load_dwordx4 v228, s[52:55], s89 offen offset:2048 lds\n" \
"v_mfma_f32_16x16x32_bf16 a[216:219], a[40:43], a[112:115], a[216:219]\n" \
"ds_read_b128 a[16:19], v250 offset:4096\n" \
"ds_read_b128 a[20:23], v251 offset:4096\n" \
"v_mfma_f32_16x16x32_bf16 a[216:219], a[44:47], a[116:119], a[216:219]\n" \
"v_cvt_pk_bf16_f32 v202, v204, v205\n" \
"v_cvt_pk_bf16_f32 v203, v206, v207\n" \
"v_mfma_f32_16x16x32_bf16 a[212:215], a[40:43], a[104:107], a[212:215]\n" \
"v_mul_f32 v216, s76, v216\n" \
"v_mul_f32 v217, s76, v217\n" \
"v_mfma_f32_16x16x32_bf16 a[212:215], a[44:47], a[108:111], a[212:215]\n" \
"buffer_load_dwordx4 v229, s[52:55], s89 offen offset:3072 lds\n" \
"v_mfma_f32_16x16x32_bf16 a[208:211], a[40:43], a[96:99], a[208:211]\n" \
"v_mul_f32 v218, s76, v218\n" \
"v_mul_f32 v219, s76, v219\n" \
"v_mfma_f32_16x16x32_bf16 a[208:211], a[44:47], a[100:103], a[208:211]\n" \
"s_xor_b32 s89, s89, 0x500000\n" \
"v_cvt_pk_bf16_f32 v216, v216, v217\n" \
"v_cvt_pk_bf16_f32 v217, v218, v219\n" \
"v_mfma_f32_16x16x32_bf16 a[224:227], a[48:51], a[96:99], a[224:227]\n" \
"s_add_u32 m0, s79, 49152\n" \
"v_mul_f32 v220, s76, v220\n" \
"v_mul_f32 v221, s76, v221\n" \
"v_mfma_f32_16x16x32_bf16 a[224:227], a[52:55], a[100:103], a[224:227]\n" \
"buffer_load_dwordx4 v228, s[52:55], s78 offen offset:0 lds\n" \
"v_mfma_f32_16x16x32_bf16 a[228:231], a[48:51], a[104:107], a[228:231]\n" \
"v_mul_f32 v222, s76, v222\n" \
"v_mul_f32 v223, s76, v223\n" \
"v_mfma_f32_16x16x32_bf16 a[228:231], a[52:55], a[108:111], a[228:231]\n" \
"v_cvt_pk_bf16_f32 v218, v220, v221\n" \
"v_cvt_pk_bf16_f32 v219, v222, v223\n" \
"v_mfma_f32_16x16x32_bf16 a[232:235], a[48:51], a[112:115], a[232:235]\n" \
"v_mul_f32 v144, s76, v144\n" \
"v_mul_f32 v145, s76, v145\n" \
"v_mfma_f32_16x16x32_bf16 a[232:235], a[52:55], a[116:119], a[232:235]\n" \
"buffer_load_dwordx4 v229, s[52:55], s78 offen offset:1024 lds\n" \
"v_mfma_f32_16x16x32_bf16 a[236:239], a[48:51], a[120:123], a[236:239]\n" \
"v_mul_f32 v146, s76, v146\n" \
"v_mul_f32 v147, s76, v147\n" \
"v_mfma_f32_16x16x32_bf16 a[236:239], a[52:55], a[124:127], a[236:239]\n" \
"v_cvt_pk_bf16_f32 v196, v144, v145\n" \
"v_cvt_pk_bf16_f32 v197, v146, v147\n" \
"v_mfma_f32_16x16x32_bf16 a[252:255], a[56:59], a[120:123], a[252:255]\n" \
"s_xor_b32 s78, s78, 0x180000\n" \
"v_mul_f32 v148, s76, v148\n" \
"v_mul_f32 v149, s76, v149\n" \
"v_mfma_f32_16x16x32_bf16 a[252:255], a[60:63], a[124:127], a[252:255]\n" \
"buffer_load_dwordx4 v228, s[52:55], s89 offen offset:2048 lds\n" \
"v_mfma_f32_16x16x32_bf16 a[248:251], a[56:59], a[112:115], a[248:251]\n" \
"v_mul_f32 v150, s76, v150\n" \
"v_mul_f32 v151, s76, v151\n" \
"v_mfma_f32_16x16x32_bf16 a[248:251], a[60:63], a[116:119], a[248:251]\n" \
"v_cvt_pk_bf16_f32 v198, v148, v149\n" \
"v_cvt_pk_bf16_f32 v199, v150, v151\n" \
"v_mfma_f32_16x16x32_bf16 a[244:247], a[56:59], a[104:107], a[244:247]\n" \
"v_mul_f32 v176, s76, v176\n" \
"v_mul_f32 v177, s76, v177\n" \
"v_mfma_f32_16x16x32_bf16 a[244:247], a[60:63], a[108:111], a[244:247]\n" \
"buffer_load_dwordx4 v229, s[52:55], s89 offen offset:3072 lds\n" \
"v_mfma_f32_16x16x32_bf16 a[240:243], a[56:59], a[96:99], a[240:243]\n" \
"s_waitcnt lgkmcnt(0)\n" \
"v_mul_f32 v178, s76, v178\n" \
"v_mul_f32 v179, s76, v179\n" \
"v_mfma_f32_16x16x32_bf16 a[240:243], a[60:63], a[100:103], a[240:243]\n" \
"s_xor_b32 s89, s89, 0x180000\n" \
"v_cvt_pk_bf16_f32 v212, v176, v177\n" \
"v_cvt_pk_bf16_f32 v213, v178, v179\n" \
"v_mfma_f32_16x16x32_bf16 v[64:67], a[64:67], a[0:3], 0\n" \
"ds_read_b128 a[24:27], v250 offset:6144\n" \
"v_mul_f32 v180, s76, v180\n" \
"v_mfma_f32_16x16x32_bf16 v[64:67], a[68:71], a[4:7], v[64:67]\n" \
"s_add_u32 m0, s74, 131072\n" \
"ds_read_b128 a[28:31], v251 offset:6144\n" \
"v_mul_f32 v181, s76, v181\n" \
"v_mfma_f32_16x16x32_bf16 v[68:71], a[72:75], a[0:3], 0\n" \
"ds_read_b64_tr_b16 a[96:97], v232 offset:1024\n" \
"v_mul_f32 v182, s76, v182\n" \
"v_mfma_f32_16x16x32_bf16 v[68:71], a[76:79], a[4:7], v[68:71]\n" \
"buffer_load_dwordx4 v224, s[48:51], s75 offen offset:0 lds\n" \
"v_mfma_f32_16x16x32_bf16 v[72:75], a[80:83], a[0:3], 0\n" \
"ds_read_b64_tr_b16 a[98:99], v232 offset:3072\n" \
"v_mul_f32 v183, s76, v183\n" \
"v_mfma_f32_16x16x32_bf16 v[72:75], a[84:87], a[4:7], v[72:75]\n" \
"ds_read_b64_tr_b16 a[100:101], v232 offset:9216\n" \
"v_cvt_pk_bf16_f32 v214, v180, v181\n" \
"v_mfma_f32_16x16x32_bf16 v[76:79], a[88:91], a[0:3], 0\n" \
"ds_read_b64_tr_b16 a[102:103], v232 offset:11264\n" \
"v_cvt_pk_bf16_f32 v215, v182, v183\n" \
"v_mfma_f32_16x16x32_bf16 v[76:79], a[92:95], a[4:7], v[76:79]\n" \
"buffer_load_dwordx4 v225, s[48:51], s75 offen offset:1024 lds\n" \
"v_mfma_f32_16x16x32_bf16 v[108:111], a[88:91], a[8:11], 0\n" \
"ds_read_b64_tr_b16 a[104:105], v233 offset:1024\n" \
"v_mul_f32 v152, s76, v152\n" \
"v_mfma_f32_16x16x32_bf16 v[108:111], a[92:95], a[12:15], v[108:111]\n" \
"ds_read_b64_tr_b16 a[106:107], v233 offset:3072\n" \
"v_mul_f32 v153, s76, v153\n" \
"v_mfma_f32_16x16x32_bf16 v[104:107], a[80:83], a[8:11], 0\n" \
"ds_read_b64_tr_b16 a[108:109], v233 offset:9216\n" \
"v_mul_f32 v154, s76, v154\n" \
"v_mfma_f32_16x16x32_bf16 v[104:107], a[84:87], a[12:15], v[104:107]\n" \
"buffer_load_dwordx4 v226, s[48:51], s75 offen offset:2048 lds\n" \
"v_mfma_f32_16x16x32_bf16 v[100:103], a[72:75], a[8:11], 0\n" \
"ds_read_b64_tr_b16 a[110:111], v233 offset:11264\n" \
"v_mul_f32 v155, s76, v155\n" \
"v_mfma_f32_16x16x32_bf16 v[100:103], a[76:79], a[12:15], v[100:103]\n" \
"ds_read_b64_tr_b16 a[112:113], v232 offset:1536\n" \
"v_cvt_pk_bf16_f32 v204, v152, v153\n" \
"v_mfma_f32_16x16x32_bf16 v[96:99], a[64:67], a[8:11], 0\n" \
"ds_read_b64_tr_b16 a[114:115], v232 offset:3584\n" \
"v_cvt_pk_bf16_f32 v205, v154, v155\n" \
"v_mfma_f32_16x16x32_bf16 v[96:99], a[68:71], a[12:15], v[96:99]\n" \
"buffer_load_dwordx4 v227, s[48:51], s75 offen offset:3072 lds\n" \
"v_mfma_f32_16x16x32_bf16 v[128:131], a[64:67], a[16:19], 0\n" \
"s_xor_b32 s75, s75, 0x180\n" \
"ds_read_b64_tr_b16 a[116:117], v232 offset:9728\n" \
"v_mul_f32 v156, s76, v156\n" \
"v_mfma_f32_16x16x32_bf16 v[128:131], a[68:71], a[20:23], v[128:131]\n" \
"s_add_u32 m0, s74, 135168\n" \
"ds_read_b64_tr_b16 a[118:119], v232 offset:11776\n" \
"v_mul_f32 v157, s76, v157\n" \
"v_mfma_f32_16x16x32_bf16 v[132:135], a[72:75], a[16:19], 0\n" \
"ds_read_b64_tr_b16 a[120:121], v233 offset:1536\n" \
"v_mul_f32 v158, s76, v158\n" \
"Lpg2209_%=:\n" \
"Lst0x3_%=:\n" \
"s_cbranch_execz Lpg4446_%=\n" \
"v_mfma_f32_16x16x32_bf16 v[132:135], a[76:79], a[20:23], v[132:135]\n" \
"buffer_load_dwordx4 v224, s[48:51], s77 offen offset:0 lds\n" \
"v_mfma_f32_16x16x32_bf16 v[136:139], a[80:83], a[16:19], 0\n" \
"ds_read_b64_tr_b16 a[122:123], v233 offset:3584\n" \
"v_mul_f32 v159, s76, v159\n" \
"v_mfma_f32_16x16x32_bf16 v[136:139], a[84:87], a[20:23], v[136:139]\n" \
"ds_read_b64_tr_b16 a[124:125], v233 offset:9728\n" \
"v_cvt_pk_bf16_f32 v206, v156, v157\n" \
"v_mfma_f32_16x16x32_bf16 v[140:143], a[88:91], a[16:19], 0\n" \
"s_waitcnt lgkmcnt(3)\n" \
"ds_read_b64_tr_b16 a[126:127], v233 offset:11776\n" \
"v_cvt_pk_bf16_f32 v207, v158, v159\n" \
"v_mfma_f32_16x16x32_bf16 v[140:143], a[92:95], a[20:23], v[140:143]\n" \
"buffer_load_dwordx4 v225, s[48:51], s77 offen offset:1024 lds\n" \
"v_mfma_f32_16x16x32_bf16 v[172:175], a[88:91], a[24:27], 0\n" \
"s_add_u32 s93, s87, 8192\n" \
"v_mul_f32 v184, s76, v184\n" \
"v_mul_f32 v185, s76, v185\n" \
"v_mfma_f32_16x16x32_bf16 v[172:175], a[92:95], a[28:31], v[172:175]\n" \
"buffer_store_dwordx4 v[192:195], v236, s[56:59], s93 offen offset:0 sc1\n" \
"v_mfma_f32_16x16x32_bf16 v[168:171], a[80:83], a[24:27], 0\n" \
"v_mul_f32 v186, s76, v186\n" \
"v_mul_f32 v187, s76, v187\n" \
"v_mfma_f32_16x16x32_bf16 v[168:171], a[84:87], a[28:31], v[168:171]\n" \
"buffer_load_dwordx4 v226, s[48:51], s77 offen offset:2048 lds\n" \
"v_mfma_f32_16x16x32_bf16 v[164:167], a[72:75], a[24:27], 0\n" \
"v_cvt_pk_bf16_f32 v220, v184, v185\n" \
"v_cvt_pk_bf16_f32 v221, v186, v187\n" \
"v_mfma_f32_16x16x32_bf16 v[164:167], a[76:79], a[28:31], v[164:167]\n" \
"buffer_load_dwordx4 v227, s[48:51], s77 offen offset:3072 lds\n" \
"v_mfma_f32_16x16x32_bf16 v[160:163], a[64:67], a[24:27], 0\n" \
"v_mul_f32 v188, s76, v188\n" \
"v_mul_f32 v189, s76, v189\n" \
"v_mfma_f32_16x16x32_bf16 v[160:163], a[68:71], a[28:31], v[160:163]\n" \
"s_waitcnt vmcnt(43) lgkmcnt(0)\n" \
"s_barrier\n" \
"v_mfma_f32_16x16x32_bf16 v[80:83], a[96:99], a[0:3], 0\n" \
"s_xor_b32 s77, s77, 0x180\n" \
"ds_read_b64_tr_b16 a[64:65], v232 offset:16384\n" \
"v_mul_f32 v190, s76, v190\n" \
"v_mfma_f32_16x16x32_bf16 v[80:83], a[100:103], a[4:7], v[80:83]\n" \
"ds_read_b64_tr_b16 a[66:67], v232 offset:18432\n" \
"v_mul_f32 v191, s76, v191\n" \
"v_mfma_f32_16x16x32_bf16 v[84:87], a[104:107], a[0:3], 0\n" \
"s_add_u32 m0, s79, 0\n" \
"ds_read_b64_tr_b16 a[68:69], v232 offset:24576\n" \
"v_cvt_pk_bf16_f32 v222, v188, v189\n" \
"v_mfma_f32_16x16x32_bf16 v[84:87], a[108:111], a[4:7], v[84:87]\n" \
"buffer_load_dwordx4 v228, s[52:55], s78 offen offset:0 lds\n" \
"v_mfma_f32_16x16x32_bf16 v[88:91], a[112:115], a[0:3], 0\n" \
"ds_read_b64_tr_b16 a[70:71], v232 offset:26624\n" \
"v_cvt_pk_bf16_f32 v223, v190, v191\n" \
"v_mfma_f32_16x16x32_bf16 v[88:91], a[116:119], a[4:7], v[88:91]\n" \
"ds_read_b64_tr_b16 a[72:73], v233 offset:16384\n" \
"v_accvgpr_read_b32 v243, a224\n" \
"v_mfma_f32_16x16x32_bf16 v[92:95], a[120:123], a[0:3], 0\n" \
"ds_read_b64_tr_b16 a[74:75], v233 offset:18432\n" \
"v_accvgpr_read_b32 v252, a225\n" \
"v_mfma_f32_16x16x32_bf16 v[92:95], a[124:127], a[4:7], v[92:95]\n" \
"buffer_load_dwordx4 v229, s[52:55], s78 offen offset:1024 lds\n" \
"v_mfma_f32_16x16x32_bf16 v[124:127], a[120:123], a[8:11], 0\n" \
"s_waitcnt vmcnt(33)\n" \
"ds_read_b128 a[0:3], v230 offset:0\n" \
"v_accvgpr_read_b32 v253, a226\n" \
"v_mfma_f32_16x16x32_bf16 v[124:127], a[124:127], a[12:15], v[124:127]\n" \
"ds_read_b128 a[4:7], v231 offset:0\n" \
"v_accvgpr_read_b32 v254, a227\n" \
"v_mfma_f32_16x16x32_bf16 v[120:123], a[112:115], a[8:11], 0\n" \
"s_xor_b32 s78, s78, 0x300000\n" \
"ds_read_b64_tr_b16 a[76:77], v233 offset:24576\n" \
"v_mul_f32 v243, s31, v243\n" \
"v_mfma_f32_16x16x32_bf16 v[120:123], a[116:119], a[12:15], v[120:123]\n" \
"buffer_load_dwordx4 v228, s[52:55], s89 offen offset:2048 lds\n" \
"v_mfma_f32_16x16x32_bf16 v[116:119], a[104:107], a[8:11], 0\n" \
"ds_read_b64_tr_b16 a[78:79], v233 offset:26624\n" \
"v_mul_f32 v252, s31, v252\n" \
"v_mfma_f32_16x16x32_bf16 v[116:119], a[108:111], a[12:15], v[116:119]\n" \
"ds_read_b64_tr_b16 a[80:81], v232 offset:16896\n" \
"v_mul_f32 v253, s31, v253\n" \
"v_mfma_f32_16x16x32_bf16 v[112:115], a[96:99], a[8:11], 0\n" \
"ds_read_b64_tr_b16 a[82:83], v232 offset:18944\n" \
"v_mul_f32 v254, s31, v254\n" \
"v_mfma_f32_16x16x32_bf16 v[112:115], a[100:103], a[12:15], v[112:115]\n" \
"buffer_load_dwordx4 v229, s[52:55], s89 offen offset:3072 lds\n" \
"v_mfma_f32_16x16x32_bf16 v[144:147], a[96:99], a[16:19], 0\n" \
"s_waitcnt vmcnt(33)\n" \
"s_xor_b32 s89, s89, 0x300000\n" \
"ds_read_b128 a[8:11], v230 offset:2048\n" \
"v_mfma_f32_16x16x32_bf16 v[144:147], a[100:103], a[20:23], v[144:147]\n" \
"s_add_u32 m0, s74, 65536\n" \
"ds_read_b128 a[12:15], v231 offset:2048\n" \
"v_cvt_pk_bf16_f32 v243, v243, v252\n" \
"v_mfma_f32_16x16x32_bf16 v[148:151], a[104:107], a[16:19], 0\n" \
"s_waitcnt lgkmcnt(6)\n" \
"ds_read_b64_tr_b16 a[84:85], v232 offset:25088\n" \
"v_cvt_pk_bf16_f32 v253, v253, v254\n" \
"v_mfma_f32_16x16x32_bf16 v[148:151], a[108:111], a[20:23], v[148:151]\n" \
"buffer_load_dwordx4 v224, s[48:51], s75 offen offset:0 lds\n" \
"v_mfma_f32_16x16x32_bf16 v[152:155], a[112:115], a[16:19], 0\n" \
"ds_read_b64_tr_b16 a[86:87], v232 offset:27136\n" \
"v_accvgpr_write_b32 a224, v243\n" \
"v_mfma_f32_16x16x32_bf16 v[152:155], a[116:119], a[20:23], v[152:155]\n" \
"ds_read_b64_tr_b16 a[88:89], v233 offset:16896\n" \
"v_accvgpr_write_b32 a225, v253\n" \
"v_mfma_f32_16x16x32_bf16 v[156:159], a[120:123], a[16:19], 0\n" \
"ds_read_b64_tr_b16 a[90:91], v233 offset:18944\n" \
"v_accvgpr_read_b32 v243, a240\n" \
"v_mfma_f32_16x16x32_bf16 v[156:159], a[124:127], a[20:23], v[156:159]\n" \
"buffer_load_dwordx4 v225, s[48:51], s75 offen offset:1024 lds\n" \
"v_mfma_f32_16x16x32_bf16 v[188:191], a[120:123], a[24:27], 0\n" \
"s_waitcnt vmcnt(33)\n" \
"ds_read_b128 a[16:19], v230 offset:4096\n" \
"v_accvgpr_read_b32 v252, a241\n" \
"v_mfma_f32_16x16x32_bf16 v[188:191], a[124:127], a[28:31], v[188:191]\n" \
"ds_read_b128 a[20:23], v231 offset:4096\n" \
"v_accvgpr_read_b32 v253, a242\n" \
"v_mfma_f32_16x16x32_bf16 v[184:187], a[112:115], a[24:27], 0\n" \
"s_waitcnt lgkmcnt(6)\n" \
"ds_read_b64_tr_b16 a[92:93], v233 offset:25088\n" \
"v_accvgpr_read_b32 v254, a243\n" \
"v_mfma_f32_16x16x32_bf16 v[184:187], a[116:119], a[28:31], v[184:187]\n" \
"buffer_load_dwordx4 v226, s[48:51], s75 offen offset:2048 lds\n" \
"v_mfma_f32_16x16x32_bf16 v[180:183], a[104:107], a[24:27], 0\n" \
"ds_read_b64_tr_b16 a[94:95], v233 offset:27136\n" \
"v_mul_f32 v243, s31, v243\n" \
"v_mfma_f32_16x16x32_bf16 v[180:183], a[108:111], a[28:31], v[180:183]\n" \
"v_mul_f32 v252, s31, v252\n" \
"v_mul_f32 v253, s31, v253\n" \
"v_mfma_f32_16x16x32_bf16 v[176:179], a[96:99], a[24:27], 0\n" \
"v_mul_f32 v254, s31, v254\n" \
"v_cvt_pk_bf16_f32 v243, v243, v252\n" \
"v_mfma_f32_16x16x32_bf16 v[176:179], a[100:103], a[28:31], v[176:179]\n" \
"buffer_load_dwordx4 v227, s[48:51], s75 offen offset:3072 lds\n" \
"v_mfma_f32_16x16x32_bf16 v[64:67], a[64:67], a[0:3], v[64:67]\n" \
"s_waitcnt vmcnt(33)\n" \
"s_xor_b32 s75, s75, 0x300\n" \
"ds_read_b128 a[24:27], v230 offset:6144\n" \
"v_mfma_f32_16x16x32_bf16 v[64:67], a[68:71], a[4:7], v[64:67]\n" \
"s_add_u32 m0, s74, 69632\n" \
"ds_read_b128 a[28:31], v231 offset:6144\n" \
"v_cvt_pk_bf16_f32 v253, v253, v254\n" \
"v_mfma_f32_16x16x32_bf16 v[68:71], a[72:75], a[0:3], v[68:71]\n" \
"s_waitcnt lgkmcnt(4)\n" \
"ds_read_b64_tr_b16 a[96:97], v232 offset:17408\n" \
"v_accvgpr_write_b32 a226, v243\n" \
"v_mfma_f32_16x16x32_bf16 v[68:71], a[76:79], a[4:7], v[68:71]\n" \
"buffer_load_dwordx4 v224, s[48:51], s77 offen offset:0 lds\n" \
"v_mfma_f32_16x16x32_bf16 v[72:75], a[80:83], a[0:3], v[72:75]\n" \
"ds_read_b64_tr_b16 a[98:99], v232 offset:19456\n" \
"v_accvgpr_write_b32 a227, v253\n" \
"v_mfma_f32_16x16x32_bf16 v[72:75], a[84:87], a[4:7], v[72:75]\n" \
"s_waitcnt lgkmcnt(3)\n" \
"ds_read_b64_tr_b16 a[100:101], v232 offset:25600\n" \
"v_accvgpr_read_b32 v243, a228\n" \
"v_mfma_f32_16x16x32_bf16 v[76:79], a[88:91], a[0:3], v[76:79]\n" \
"ds_read_b64_tr_b16 a[102:103], v232 offset:27648\n" \
"v_accvgpr_read_b32 v252, a229\n" \
"v_mfma_f32_16x16x32_bf16 v[76:79], a[92:95], a[4:7], v[76:79]\n" \
"buffer_load_dwordx4 v225, s[48:51], s77 offen offset:1024 lds\n" \
"v_mfma_f32_16x16x32_bf16 v[108:111], a[88:91], a[8:11], v[108:111]\n" \
"ds_read_b64_tr_b16 a[104:105], v233 offset:17408\n" \
"v_accvgpr_read_b32 v253, a230\n" \
"v_mfma_f32_16x16x32_bf16 v[108:111], a[92:95], a[12:15], v[108:111]\n" \
"ds_read_b64_tr_b16 a[106:107], v233 offset:19456\n" \
"v_accvgpr_read_b32 v254, a231\n" \
"v_mfma_f32_16x16x32_bf16 v[104:107], a[80:83], a[8:11], v[104:107]\n" \
"s_waitcnt lgkmcnt(6)\n" \
"ds_read_b64_tr_b16 a[108:109], v233 offset:25600\n" \
"v_mul_f32 v243, s31, v243\n" \
"v_mfma_f32_16x16x32_bf16 v[104:107], a[84:87], a[12:15], v[104:107]\n" \
"buffer_load_dwordx4 v226, s[48:51], s77 offen offset:2048 lds\n" \
"v_mfma_f32_16x16x32_bf16 v[100:103], a[72:75], a[8:11], v[100:103]\n" \
"ds_read_b64_tr_b16 a[110:111], v233 offset:27648\n" \
"v_mul_f32 v252, s31, v252\n" \
"v_mfma_f32_16x16x32_bf16 v[100:103], a[76:79], a[12:15], v[100:103]\n" \
"ds_read_b64_tr_b16 a[112:113], v232 offset:17920\n" \
"v_mul_f32 v253, s31, v253\n" \
"v_mfma_f32_16x16x32_bf16 v[96:99], a[64:67], a[8:11], v[96:99]\n" \
"ds_read_b64_tr_b16 a[114:115], v232 offset:19968\n" \
"v_mul_f32 v254, s31, v254\n" \
"v_mfma_f32_16x16x32_bf16 v[96:99], a[68:71], a[12:15], v[96:99]\n" \
"buffer_load_dwordx4 v227, s[48:51], s77 offen offset:3072 lds\n" \
"v_mfma_f32_16x16x32_bf16 v[128:131], a[64:67], a[16:19], v[128:131]\n" \
"ds_read_b64_tr_b16 a[116:117], v232 offset:26112\n" \
"v_cvt_pk_bf16_f32 v243, v243, v252\n" \
"v_mfma_f32_16x16x32_bf16 v[128:131], a[68:71], a[20:23], v[128:131]\n" \
"ds_read_b64_tr_b16 a[118:119], v232 offset:28160\n" \
"v_cvt_pk_bf16_f32 v253, v253, v254\n" \
"v_mfma_f32_16x16x32_bf16 v[132:135], a[72:75], a[16:19], v[132:135]\n" \
"s_xor_b32 s77, s77, 0x300\n" \
"ds_read_b64_tr_b16 a[120:121], v233 offset:17920\n" \
"v_accvgpr_write_b32 a228, v243\n" \
"v_mfma_f32_16x16x32_bf16 v[132:135], a[76:79], a[20:23], v[132:135]\n" \
"v_accvgpr_write_b32 a229, v253\n" \
"v_accvgpr_read_b32 v243, a244\n" \
"v_mfma_f32_16x16x32_bf16 v[136:139], a[80:83], a[16:19], v[136:139]\n" \
"ds_read_b64_tr_b16 a[122:123], v233 offset:19968\n" \
"v_accvgpr_read_b32 v252, a245\n" \
"v_mfma_f32_16x16x32_bf16 v[136:139], a[84:87], a[20:23], v[136:139]\n" \
"ds_read_b64_tr_b16 a[124:125], v233 offset:26112\n" \
"v_accvgpr_read_b32 v253, a246\n" \
"v_mfma_f32_16x16x32_bf16 v[140:143], a[88:91], a[16:19], v[140:143]\n" \
"ds_read_b64_tr_b16 a[126:127], v233 offset:28160\n" \
"v_accvgpr_read_b32 v254, a247\n" \
"v_mfma_f32_16x16x32_bf16 v[140:143], a[92:95], a[20:23], v[140:143]\n" \
"buffer_store_dwordx4 v[208:211], v236, s[56:59], s93 offen offset:256 sc1\n" \
"v_mfma_f32_16x16x32_bf16 v[172:175], a[88:91], a[24:27], v[172:175]\n" \
"v_mul_f32 v243, s31, v243\n" \
"v_mul_f32 v252, s31, v252\n" \
"v_mfma_f32_16x16x32_bf16 v[172:175], a[92:95], a[28:31], v[172:175]\n" \
"v_mul_f32 v253, s31, v253\n" \
"v_mul_f32 v254, s31, v254\n" \
"v_mfma_f32_16x16x32_bf16 v[168:171], a[80:83], a[24:27], v[168:171]\n" \
"v_cvt_pk_bf16_f32 v243, v243, v252\n" \
"v_cvt_pk_bf16_f32 v253, v253, v254\n" \
"v_mfma_f32_16x16x32_bf16 v[168:171], a[84:87], a[28:31], v[168:171]\n" \
"v_accvgpr_write_b32 a230, v243\n" \
"v_accvgpr_write_b32 a231, v253\n" \
"v_mfma_f32_16x16x32_bf16 v[164:167], a[72:75], a[24:27], v[164:167]\n" \
"v_accvgpr_read_b32 v243, a232\n" \
"v_accvgpr_read_b32 v252, a233\n" \
"v_mfma_f32_16x16x32_bf16 v[164:167], a[76:79], a[28:31], v[164:167]\n" \
"v_accvgpr_read_b32 v253, a234\n" \
"v_accvgpr_read_b32 v254, a235\n" \
"v_mfma_f32_16x16x32_bf16 v[160:163], a[64:67], a[24:27], v[160:163]\n" \
"v_mul_f32 v243, s31, v243\n" \
"v_mul_f32 v252, s31, v252\n" \
"v_mfma_f32_16x16x32_bf16 v[160:163], a[68:71], a[28:31], v[160:163]\n" \
"s_waitcnt vmcnt(26) lgkmcnt(0)\n" \
"s_barrier\n" \
"v_mfma_f32_16x16x32_bf16 v[80:83], a[96:99], a[0:3], v[80:83]\n" \
"ds_read_b64_tr_b16 a[64:65], v232 offset:32768\n" \
"v_mul_f32 v253, s31, v253\n" \
"v_mfma_f32_16x16x32_bf16 v[80:83], a[100:103], a[4:7], v[80:83]\n" \
"ds_read_b64_tr_b16 a[66:67], v232 offset:34816\n" \
"v_mul_f32 v254, s31, v254\n" \
"v_mfma_f32_16x16x32_bf16 v[84:87], a[104:107], a[0:3], v[84:87]\n" \
"s_add_u32 m0, s79, 16384\n" \
"ds_read_b64_tr_b16 a[68:69], v232 offset:40960\n" \
"v_cvt_pk_bf16_f32 v243, v243, v252\n" \
"v_mfma_f32_16x16x32_bf16 v[84:87], a[108:111], a[4:7], v[84:87]\n" \
"buffer_load_dwordx4 v228, s[52:55], s78 offen offset:0 lds\n" \
"v_mfma_f32_16x16x32_bf16 v[88:91], a[112:115], a[0:3], v[88:91]\n" \
"ds_read_b64_tr_b16 a[70:71], v232 offset:43008\n" \
"v_cvt_pk_bf16_f32 v253, v253, v254\n" \
"v_mfma_f32_16x16x32_bf16 v[88:91], a[116:119], a[4:7], v[88:91]\n" \
"ds_read_b64_tr_b16 a[72:73], v233 offset:32768\n" \
"v_accvgpr_write_b32 a232, v243\n" \
"v_mfma_f32_16x16x32_bf16 v[92:95], a[120:123], a[0:3], v[92:95]\n" \
"ds_read_b64_tr_b16 a[74:75], v233 offset:34816\n" \
"v_accvgpr_write_b32 a233, v253\n" \
"v_mfma_f32_16x16x32_bf16 v[92:95], a[124:127], a[4:7], v[92:95]\n" \
"buffer_load_dwordx4 v229, s[52:55], s78 offen offset:1024 lds\n" \
"v_mfma_f32_16x16x32_bf16 v[124:127], a[120:123], a[8:11], v[124:127]\n" \
"ds_read_b128 a[0:3], v230 offset:32768\n" \
"v_accvgpr_read_b32 v243, a248\n" \
"v_mfma_f32_16x16x32_bf16 v[124:127], a[124:127], a[12:15], v[124:127]\n" \
"ds_read_b128 a[4:7], v231 offset:32768\n" \
"v_accvgpr_read_b32 v252, a249\n" \
"v_mfma_f32_16x16x32_bf16 v[120:123], a[112:115], a[8:11], v[120:123]\n" \
"s_xor_b32 s78, s78, 0x80000\n" \
"ds_read_b64_tr_b16 a[76:77], v233 offset:40960\n" \
"v_accvgpr_read_b32 v253, a250\n" \
"v_mfma_f32_16x16x32_bf16 v[120:123], a[116:119], a[12:15], v[120:123]\n" \
"buffer_load_dwordx4 v228, s[52:55], s89 offen offset:2048 lds\n" \
"v_mfma_f32_16x16x32_bf16 v[116:119], a[104:107], a[8:11], v[116:119]\n" \
"ds_read_b64_tr_b16 a[78:79], v233 offset:43008\n" \
"v_accvgpr_read_b32 v254, a251\n" \
"v_mfma_f32_16x16x32_bf16 v[116:119], a[108:111], a[12:15], v[116:119]\n" \
"ds_read_b64_tr_b16 a[80:81], v232 offset:33280\n" \
"v_mul_f32 v243, s31, v243\n" \
"v_mfma_f32_16x16x32_bf16 v[112:115], a[96:99], a[8:11], v[112:115]\n" \
"ds_read_b64_tr_b16 a[82:83], v232 offset:35328\n" \
"v_mul_f32 v252, s31, v252\n" \
"v_mfma_f32_16x16x32_bf16 v[112:115], a[100:103], a[12:15], v[112:115]\n" \
"buffer_load_dwordx4 v229, s[52:55], s89 offen offset:3072 lds\n" \
"v_mfma_f32_16x16x32_bf16 v[144:147], a[96:99], a[16:19], v[144:147]\n" \
"s_xor_b32 s89, s89, 0x80000\n" \
"ds_read_b128 a[8:11], v230 offset:34816\n" \
"v_mfma_f32_16x16x32_bf16 v[144:147], a[100:103], a[20:23], v[144:147]\n" \
"s_add_u32 m0, s74, 98304\n" \
"ds_read_b128 a[12:15], v231 offset:34816\n" \
"v_mul_f32 v253, s31, v253\n" \
"v_mfma_f32_16x16x32_bf16 v[148:151], a[104:107], a[16:19], v[148:151]\n" \
"s_waitcnt lgkmcnt(6)\n" \
"ds_read_b64_tr_b16 a[84:85], v232 offset:41472\n" \
"v_mul_f32 v254, s31, v254\n" \
"v_mfma_f32_16x16x32_bf16 v[148:151], a[108:111], a[20:23], v[148:151]\n" \
"buffer_load_dwordx4 v224, s[48:51], s75 offen offset:0 lds\n" \
"v_mfma_f32_16x16x32_bf16 v[152:155], a[112:115], a[16:19], v[152:155]\n" \
"ds_read_b64_tr_b16 a[86:87], v232 offset:43520\n" \
"v_cvt_pk_bf16_f32 v243, v243, v252\n" \
"v_mfma_f32_16x16x32_bf16 v[152:155], a[116:119], a[20:23], v[152:155]\n" \
"ds_read_b64_tr_b16 a[88:89], v233 offset:33280\n" \
"v_cvt_pk_bf16_f32 v253, v253, v254\n" \
"v_mfma_f32_16x16x32_bf16 v[156:159], a[120:123], a[16:19], v[156:159]\n" \
"ds_read_b64_tr_b16 a[90:91], v233 offset:35328\n" \
"v_accvgpr_write_b32 a234, v243\n" \
"v_mfma_f32_16x16x32_bf16 v[156:159], a[124:127], a[20:23], v[156:159]\n" \
"buffer_load_dwordx4 v225, s[48:51], s75 offen offset:1024 lds\n" \
"v_mfma_f32_16x16x32_bf16 v[188:191], a[120:123], a[24:27], v[188:191]\n" \
"ds_read_b128 a[16:19], v230 offset:36864\n" \
"v_accvgpr_write_b32 a235, v253\n" \
"v_mfma_f32_16x16x32_bf16 v[188:191], a[124:127], a[28:31], v[188:191]\n" \
"ds_read_b128 a[20:23], v231 offset:36864\n" \
"v_accvgpr_read_b32 v243, a236\n" \
"v_mfma_f32_16x16x32_bf16 v[184:187], a[112:115], a[24:27], v[184:187]\n" \
"s_waitcnt lgkmcnt(6)\n" \
"ds_read_b64_tr_b16 a[92:93], v233 offset:41472\n" \
"v_accvgpr_read_b32 v252, a237\n" \
"v_mfma_f32_16x16x32_bf16 v[184:187], a[116:119], a[28:31], v[184:187]\n" \
"buffer_load_dwordx4 v226, s[48:51], s75 offen offset:2048 lds\n" \
"v_mfma_f32_16x16x32_bf16 v[180:183], a[104:107], a[24:27], v[180:183]\n" \
"ds_read_b64_tr_b16 a[94:95], v233 offset:43520\n" \
"v_accvgpr_read_b32 v253, a238\n" \
"v_mfma_f32_16x16x32_bf16 v[180:183], a[108:111], a[28:31], v[180:183]\n" \
"v_accvgpr_read_b32 v254, a239\n" \
"v_mul_f32 v243, s31, v243\n" \
"v_mfma_f32_16x16x32_bf16 v[176:179], a[96:99], a[24:27], v[176:179]\n" \
"v_mul_f32 v252, s31, v252\n" \
"v_mul_f32 v253, s31, v253\n" \
"v_mfma_f32_16x16x32_bf16 v[176:179], a[100:103], a[28:31], v[176:179]\n" \
"buffer_load_dwordx4 v227, s[48:51], s75 offen offset:3072 lds\n" \
"v_mfma_f32_16x16x32_bf16 v[64:67], a[64:67], a[0:3], v[64:67]\n" \
"s_xor_b32 s75, s75, 0x80\n" \
"ds_read_b128 a[24:27], v230 offset:38912\n" \
"v_mfma_f32_16x16x32_bf16 v[64:67], a[68:71], a[4:7], v[64:67]\n" \
"s_add_u32 m0, s74, 102400\n" \
"ds_read_b128 a[28:31], v231 offset:38912\n" \
"v_mul_f32 v254, s31, v254\n" \
"v_mfma_f32_16x16x32_bf16 v[68:71], a[72:75], a[0:3], v[68:71]\n" \
"s_waitcnt lgkmcnt(4)\n" \
"ds_read_b64_tr_b16 a[96:97], v232 offset:33792\n" \
"v_cvt_pk_bf16_f32 v243, v243, v252\n" \
"v_mfma_f32_16x16x32_bf16 v[68:71], a[76:79], a[4:7], v[68:71]\n" \
"buffer_load_dwordx4 v224, s[48:51], s77 offen offset:0 lds\n" \
"v_mfma_f32_16x16x32_bf16 v[72:75], a[80:83], a[0:3], v[72:75]\n" \
"ds_read_b64_tr_b16 a[98:99], v232 offset:35840\n" \
"v_cvt_pk_bf16_f32 v253, v253, v254\n" \
"v_mfma_f32_16x16x32_bf16 v[72:75], a[84:87], a[4:7], v[72:75]\n" \
"s_waitcnt lgkmcnt(3)\n" \
"ds_read_b64_tr_b16 a[100:101], v232 offset:41984\n" \
"v_accvgpr_write_b32 a236, v243\n" \
"v_mfma_f32_16x16x32_bf16 v[76:79], a[88:91], a[0:3], v[76:79]\n" \
"ds_read_b64_tr_b16 a[102:103], v232 offset:44032\n" \
"v_accvgpr_write_b32 a237, v253\n" \
"v_mfma_f32_16x16x32_bf16 v[76:79], a[92:95], a[4:7], v[76:79]\n" \
"buffer_load_dwordx4 v225, s[48:51], s77 offen offset:1024 lds\n" \
"v_mfma_f32_16x16x32_bf16 v[108:111], a[88:91], a[8:11], v[108:111]\n" \
"ds_read_b64_tr_b16 a[104:105], v233 offset:33792\n" \
"v_accvgpr_read_b32 v243, a252\n" \
"v_mfma_f32_16x16x32_bf16 v[108:111], a[92:95], a[12:15], v[108:111]\n" \
"ds_read_b64_tr_b16 a[106:107], v233 offset:35840\n" \
"v_accvgpr_read_b32 v252, a253\n" \
"v_mfma_f32_16x16x32_bf16 v[104:107], a[80:83], a[8:11], v[104:107]\n" \
"s_waitcnt lgkmcnt(6)\n" \
"ds_read_b64_tr_b16 a[108:109], v233 offset:41984\n" \
"v_accvgpr_read_b32 v253, a254\n" \
"v_mfma_f32_16x16x32_bf16 v[104:107], a[84:87], a[12:15], v[104:107]\n" \
"buffer_load_dwordx4 v226, s[48:51], s77 offen offset:2048 lds\n" \
"v_mfma_f32_16x16x32_bf16 v[100:103], a[72:75], a[8:11], v[100:103]\n" \
"ds_read_b64_tr_b16 a[110:111], v233 offset:44032\n" \
"v_accvgpr_read_b32 v254, a255\n" \
"v_mfma_f32_16x16x32_bf16 v[100:103], a[76:79], a[12:15], v[100:103]\n" \
"ds_read_b64_tr_b16 a[112:113], v232 offset:34304\n" \
"v_mul_f32 v243, s31, v243\n" \
"v_mfma_f32_16x16x32_bf16 v[96:99], a[64:67], a[8:11], v[96:99]\n" \
"ds_read_b64_tr_b16 a[114:115], v232 offset:36352\n" \
"v_mul_f32 v252, s31, v252\n" \
"v_mfma_f32_16x16x32_bf16 v[96:99], a[68:71], a[12:15], v[96:99]\n" \
"buffer_load_dwordx4 v227, s[48:51], s77 offen offset:3072 lds\n" \
"v_mfma_f32_16x16x32_bf16 v[128:131], a[64:67], a[16:19], v[128:131]\n" \
"ds_read_b64_tr_b16 a[116:117], v232 offset:42496\n" \
"v_mul_f32 v253, s31, v253\n" \
"v_mfma_f32_16x16x32_bf16 v[128:131], a[68:71], a[20:23], v[128:131]\n" \
"ds_read_b64_tr_b16 a[118:119], v232 offset:44544\n" \
"v_mul_f32 v254, s31, v254\n" \
"v_mfma_f32_16x16x32_bf16 v[132:135], a[72:75], a[16:19], v[132:135]\n" \
"s_xor_b32 s77, s77, 0x80\n" \
"ds_read_b64_tr_b16 a[120:121], v233 offset:34304\n" \
"v_cvt_pk_bf16_f32 v243, v243, v252\n" \
"v_mfma_f32_16x16x32_bf16 v[132:135], a[76:79], a[20:23], v[132:135]\n" \
"v_cvt_pk_bf16_f32 v253, v253, v254\n" \
"v_accvgpr_write_b32 a238, v243\n" \
"v_mfma_f32_16x16x32_bf16 v[136:139], a[80:83], a[16:19], v[136:139]\n" \
"ds_read_b64_tr_b16 a[122:123], v233 offset:36352\n" \
"v_accvgpr_write_b32 a239, v253\n" \
"v_mfma_f32_16x16x32_bf16 v[136:139], a[84:87], a[20:23], v[136:139]\n" \
"ds_read_b64_tr_b16 a[124:125], v233 offset:42496\n" \
"v_accvgpr_read_b32 v243, a192\n" \
"v_mfma_f32_16x16x32_bf16 v[140:143], a[88:91], a[16:19], v[140:143]\n" \
"ds_read_b64_tr_b16 a[126:127], v233 offset:44544\n" \
"v_accvgpr_read_b32 v252, a193\n" \
"v_mfma_f32_16x16x32_bf16 v[140:143], a[92:95], a[20:23], v[140:143]\n" \
"buffer_store_dwordx4 v[200:203], v236, s[56:59], s93 offen offset:2048 sc1\n" \
"v_mfma_f32_16x16x32_bf16 v[172:175], a[88:91], a[24:27], v[172:175]\n" \
"v_accvgpr_read_b32 v253, a194\n" \
"v_accvgpr_read_b32 v254, a195\n" \
"v_mfma_f32_16x16x32_bf16 v[172:175], a[92:95], a[28:31], v[172:175]\n" \
"v_mul_f32 v243, s31, v243\n" \
"v_mul_f32 v252, s31, v252\n" \
"v_mfma_f32_16x16x32_bf16 v[168:171], a[80:83], a[24:27], v[168:171]\n" \
"v_mul_f32 v253, s31, v253\n" \
"v_mul_f32 v254, s31, v254\n" \
"v_mfma_f32_16x16x32_bf16 v[168:171], a[84:87], a[28:31], v[168:171]\n" \
"v_cvt_pk_bf16_f32 v243, v243, v252\n" \
"v_cvt_pk_bf16_f32 v253, v253, v254\n" \
"v_mfma_f32_16x16x32_bf16 v[164:167], a[72:75], a[24:27], v[164:167]\n" \
"v_accvgpr_write_b32 a192, v243\n" \
"v_accvgpr_write_b32 a193, v253\n" \
"v_mfma_f32_16x16x32_bf16 v[164:167], a[76:79], a[28:31], v[164:167]\n" \
"v_accvgpr_read_b32 v243, a208\n" \
"v_accvgpr_read_b32 v252, a209\n" \
"v_mfma_f32_16x16x32_bf16 v[160:163], a[64:67], a[24:27], v[160:163]\n" \
"v_accvgpr_read_b32 v253, a210\n" \
"v_accvgpr_read_b32 v254, a211\n" \
"v_mfma_f32_16x16x32_bf16 v[160:163], a[68:71], a[28:31], v[160:163]\n" \
"s_waitcnt vmcnt(35) lgkmcnt(0)\n" \
"s_barrier\n" \
"v_mfma_f32_16x16x32_bf16 v[80:83], a[96:99], a[0:3], v[80:83]\n" \
"ds_read_b64_tr_b16 a[64:65], v232 offset:49152\n" \
"v_mul_f32 v243, s31, v243\n" \
"v_mfma_f32_16x16x32_bf16 v[80:83], a[100:103], a[4:7], v[80:83]\n" \
"ds_read_b64_tr_b16 a[66:67], v232 offset:51200\n" \
"v_mul_f32 v252, s31, v252\n" \
"v_mfma_f32_16x16x32_bf16 v[84:87], a[104:107], a[0:3], v[84:87]\n" \
"s_add_u32 m0, s79, 32768\n" \
"ds_read_b64_tr_b16 a[68:69], v232 offset:57344\n" \
"v_mul_f32 v253, s31, v253\n" \
"v_mfma_f32_16x16x32_bf16 v[84:87], a[108:111], a[4:7], v[84:87]\n" \
"buffer_load_dwordx4 v228, s[52:55], s78 offen offset:0 lds\n" \
"v_mfma_f32_16x16x32_bf16 v[88:91], a[112:115], a[0:3], v[88:91]\n" \
"ds_read_b64_tr_b16 a[70:71], v232 offset:59392\n" \
"v_mul_f32 v254, s31, v254\n" \
"v_mfma_f32_16x16x32_bf16 v[88:91], a[116:119], a[4:7], v[88:91]\n" \
"ds_read_b64_tr_b16 a[72:73], v233 offset:49152\n" \
"v_cvt_pk_bf16_f32 v243, v243, v252\n" \
"v_mfma_f32_16x16x32_bf16 v[92:95], a[120:123], a[0:3], v[92:95]\n" \
"ds_read_b64_tr_b16 a[74:75], v233 offset:51200\n" \
"v_cvt_pk_bf16_f32 v253, v253, v254\n" \
"v_mfma_f32_16x16x32_bf16 v[92:95], a[124:127], a[4:7], v[92:95]\n" \
"buffer_load_dwordx4 v229, s[52:55], s78 offen offset:1024 lds\n" \
"v_mfma_f32_16x16x32_bf16 v[124:127], a[120:123], a[8:11], v[124:127]\n" \
"s_waitcnt vmcnt(35)\n" \
"ds_read_b128 a[0:3], v250 offset:0\n" \
"v_accvgpr_write_b32 a194, v243\n" \
"v_mfma_f32_16x16x32_bf16 v[124:127], a[124:127], a[12:15], v[124:127]\n" \
"ds_read_b128 a[4:7], v251 offset:0\n" \
"v_accvgpr_write_b32 a195, v253\n" \
"v_mfma_f32_16x16x32_bf16 v[120:123], a[112:115], a[8:11], v[120:123]\n" \
"s_xor_b32 s78, s78, 0x180000\n" \
"ds_read_b64_tr_b16 a[76:77], v233 offset:57344\n" \
"v_accvgpr_read_b32 v243, a196\n" \
"v_mfma_f32_16x16x32_bf16 v[120:123], a[116:119], a[12:15], v[120:123]\n" \
"buffer_load_dwordx4 v228, s[52:55], s89 offen offset:2048 lds\n" \
"v_mfma_f32_16x16x32_bf16 v[116:119], a[104:107], a[8:11], v[116:119]\n" \
"ds_read_b64_tr_b16 a[78:79], v233 offset:59392\n" \
"v_accvgpr_read_b32 v252, a197\n" \
"v_mfma_f32_16x16x32_bf16 v[116:119], a[108:111], a[12:15], v[116:119]\n" \
"ds_read_b64_tr_b16 a[80:81], v232 offset:49664\n" \
"v_accvgpr_read_b32 v253, a198\n" \
"v_mfma_f32_16x16x32_bf16 v[112:115], a[96:99], a[8:11], v[112:115]\n" \
"ds_read_b64_tr_b16 a[82:83], v232 offset:51712\n" \
"v_accvgpr_read_b32 v254, a199\n" \
"v_mfma_f32_16x16x32_bf16 v[112:115], a[100:103], a[12:15], v[112:115]\n" \
"buffer_load_dwordx4 v229, s[52:55], s89 offen offset:3072 lds\n" \
"v_mfma_f32_16x16x32_bf16 v[144:147], a[96:99], a[16:19], v[144:147]\n" \
"s_waitcnt vmcnt(35)\n" \
"s_xor_b32 s89, s89, 0x180000\n" \
"ds_read_b128 a[8:11], v250 offset:2048\n" \
"v_mfma_f32_16x16x32_bf16 v[144:147], a[100:103], a[20:23], v[144:147]\n" \
"s_add_u32 m0, s74, 131072\n" \
"ds_read_b128 a[12:15], v251 offset:2048\n" \
"v_mul_f32 v243, s31, v243\n" \
"v_mfma_f32_16x16x32_bf16 v[148:151], a[104:107], a[16:19], v[148:151]\n" \
"s_waitcnt lgkmcnt(6)\n" \
"ds_read_b64_tr_b16 a[84:85], v232 offset:57856\n" \
"v_mul_f32 v252, s31, v252\n" \
"v_mfma_f32_16x16x32_bf16 v[148:151], a[108:111], a[20:23], v[148:151]\n" \
"buffer_load_dwordx4 v224, s[48:51], s75 offen offset:0 lds\n" \
"v_mfma_f32_16x16x32_bf16 v[152:155], a[112:115], a[16:19], v[152:155]\n" \
"ds_read_b64_tr_b16 a[86:87], v232 offset:59904\n" \
"v_mul_f32 v253, s31, v253\n" \
"v_mfma_f32_16x16x32_bf16 v[152:155], a[116:119], a[20:23], v[152:155]\n" \
"ds_read_b64_tr_b16 a[88:89], v233 offset:49664\n" \
"v_mul_f32 v254, s31, v254\n" \
"v_mfma_f32_16x16x32_bf16 v[156:159], a[120:123], a[16:19], v[156:159]\n" \
"ds_read_b64_tr_b16 a[90:91], v233 offset:51712\n" \
"v_cvt_pk_bf16_f32 v243, v243, v252\n" \
"v_mfma_f32_16x16x32_bf16 v[156:159], a[124:127], a[20:23], v[156:159]\n" \
"buffer_load_dwordx4 v225, s[48:51], s75 offen offset:1024 lds\n" \
"v_mfma_f32_16x16x32_bf16 v[188:191], a[120:123], a[24:27], v[188:191]\n" \
"s_waitcnt vmcnt(35)\n" \
"ds_read_b128 a[16:19], v250 offset:4096\n" \
"v_cvt_pk_bf16_f32 v253, v253, v254\n" \
"v_mfma_f32_16x16x32_bf16 v[188:191], a[124:127], a[28:31], v[188:191]\n" \
"ds_read_b128 a[20:23], v251 offset:4096\n" \
"v_accvgpr_write_b32 a196, v243\n" \
"v_mfma_f32_16x16x32_bf16 v[184:187], a[112:115], a[24:27], v[184:187]\n" \
"s_waitcnt lgkmcnt(6)\n" \
"ds_read_b64_tr_b16 a[92:93], v233 offset:57856\n" \
"v_accvgpr_write_b32 a197, v253\n" \
"v_mfma_f32_16x16x32_bf16 v[184:187], a[116:119], a[28:31], v[184:187]\n" \
"buffer_load_dwordx4 v226, s[48:51], s75 offen offset:2048 lds\n" \
"v_mfma_f32_16x16x32_bf16 v[180:183], a[104:107], a[24:27], v[180:183]\n" \
"ds_read_b64_tr_b16 a[94:95], v233 offset:59904\n" \
"v_accvgpr_read_b32 v243, a212\n" \
"v_mfma_f32_16x16x32_bf16 v[180:183], a[108:111], a[28:31], v[180:183]\n" \
"v_accvgpr_read_b32 v252, a213\n" \
"v_accvgpr_read_b32 v253, a214\n" \
"v_mfma_f32_16x16x32_bf16 v[176:179], a[96:99], a[24:27], v[176:179]\n" \
"v_accvgpr_read_b32 v254, a215\n" \
"v_mul_f32 v243, s31, v243\n" \
"v_mfma_f32_16x16x32_bf16 v[176:179], a[100:103], a[28:31], v[176:179]\n" \
"buffer_load_dwordx4 v227, s[48:51], s75 offen offset:3072 lds\n" \
"v_mfma_f32_16x16x32_bf16 v[64:67], a[64:67], a[0:3], v[64:67]\n" \
"s_waitcnt vmcnt(34)\n" \
"s_xor_b32 s75, s75, 0x180\n" \
"ds_read_b128 a[24:27], v250 offset:6144\n" \
"v_mfma_f32_16x16x32_bf16 v[64:67], a[68:71], a[4:7], v[64:67]\n" \
"s_add_u32 m0, s74, 135168\n" \
"ds_read_b128 a[28:31], v251 offset:6144\n" \
"v_mul_f32 v252, s31, v252\n" \
"v_mfma_f32_16x16x32_bf16 v[68:71], a[72:75], a[0:3], v[68:71]\n" \
"s_waitcnt lgkmcnt(4)\n" \
"ds_read_b64_tr_b16 a[96:97], v232 offset:50176\n" \
"v_mul_f32 v253, s31, v253\n" \
"v_mfma_f32_16x16x32_bf16 v[68:71], a[76:79], a[4:7], v[68:71]\n" \
"buffer_load_dwordx4 v224, s[48:51], s77 offen offset:0 lds\n" \
"v_mfma_f32_16x16x32_bf16 v[72:75], a[80:83], a[0:3], v[72:75]\n" \
"ds_read_b64_tr_b16 a[98:99], v232 offset:52224\n" \
"v_mul_f32 v254, s31, v254\n" \
"v_mfma_f32_16x16x32_bf16 v[72:75], a[84:87], a[4:7], v[72:75]\n" \
"s_waitcnt lgkmcnt(3)\n" \
"ds_read_b64_tr_b16 a[100:101], v232 offset:58368\n" \
"v_cvt_pk_bf16_f32 v243, v243, v252\n" \
"v_mfma_f32_16x16x32_bf16 v[76:79], a[88:91], a[0:3], v[76:79]\n" \
"ds_read_b64_tr_b16 a[102:103], v232 offset:60416\n" \
"v_cvt_pk_bf16_f32 v253, v253, v254\n" \
"v_mfma_f32_16x16x32_bf16 v[76:79], a[92:95], a[4:7], v[76:79]\n" \
"buffer_load_dwordx4 v225, s[48:51], s77 offen offset:1024 lds\n" \
"v_mfma_f32_16x16x32_bf16 v[108:111], a[88:91], a[8:11], v[108:111]\n" \
"ds_read_b64_tr_b16 a[104:105], v233 offset:50176\n" \
"v_accvgpr_write_b32 a198, v243\n" \
"v_mfma_f32_16x16x32_bf16 v[108:111], a[92:95], a[12:15], v[108:111]\n" \
"ds_read_b64_tr_b16 a[106:107], v233 offset:52224\n" \
"v_accvgpr_write_b32 a199, v253\n" \
"v_mfma_f32_16x16x32_bf16 v[104:107], a[80:83], a[8:11], v[104:107]\n" \
"s_waitcnt lgkmcnt(6)\n" \
"ds_read_b64_tr_b16 a[108:109], v233 offset:58368\n" \
"v_accvgpr_read_b32 v243, a200\n" \
"v_mfma_f32_16x16x32_bf16 v[104:107], a[84:87], a[12:15], v[104:107]\n" \
"buffer_load_dwordx4 v226, s[48:51], s77 offen offset:2048 lds\n" \
"v_mfma_f32_16x16x32_bf16 v[100:103], a[72:75], a[8:11], v[100:103]\n" \
"ds_read_b64_tr_b16 a[110:111], v233 offset:60416\n" \
"v_accvgpr_read_b32 v252, a201\n" \
"Lpg2766_%=:\n" \
"s_cbranch_execz Lpg5014_%=\n" \
"v_mfma_f32_16x16x32_bf16 v[100:103], a[76:79], a[12:15], v[100:103]\n" \
"ds_read_b64_tr_b16 a[112:113], v232 offset:50688\n" \
"v_accvgpr_read_b32 v253, a202\n" \
"v_mfma_f32_16x16x32_bf16 v[96:99], a[64:67], a[8:11], v[96:99]\n" \
"ds_read_b64_tr_b16 a[114:115], v232 offset:52736\n" \
"v_accvgpr_read_b32 v254, a203\n" \
"v_mfma_f32_16x16x32_bf16 v[96:99], a[68:71], a[12:15], v[96:99]\n" \
"buffer_load_dwordx4 v227, s[48:51], s77 offen offset:3072 lds\n" \
"v_mfma_f32_16x16x32_bf16 v[128:131], a[64:67], a[16:19], v[128:131]\n" \
"ds_read_b64_tr_b16 a[116:117], v232 offset:58880\n" \
"v_mul_f32 v243, s31, v243\n" \
"v_mfma_f32_16x16x32_bf16 v[128:131], a[68:71], a[20:23], v[128:131]\n" \
"ds_read_b64_tr_b16 a[118:119], v232 offset:60928\n" \
"v_mul_f32 v252, s31, v252\n" \
"v_mfma_f32_16x16x32_bf16 v[132:135], a[72:75], a[16:19], v[132:135]\n" \
"s_xor_b32 s77, s77, 0x180\n" \
"ds_read_b64_tr_b16 a[120:121], v233 offset:50688\n" \
"v_mul_f32 v253, s31, v253\n" \
"v_mfma_f32_16x16x32_bf16 v[132:135], a[76:79], a[20:23], v[132:135]\n" \
"v_mul_f32 v254, s31, v254\n" \
"v_cvt_pk_bf16_f32 v243, v243, v252\n" \
"v_mfma_f32_16x16x32_bf16 v[136:139], a[80:83], a[16:19], v[136:139]\n" \
"ds_read_b64_tr_b16 a[122:123], v233 offset:52736\n" \
"v_cvt_pk_bf16_f32 v253, v253, v254\n" \
"v_mfma_f32_16x16x32_bf16 v[136:139], a[84:87], a[20:23], v[136:139]\n" \
"ds_read_b64_tr_b16 a[124:125], v233 offset:58880\n" \
"v_accvgpr_write_b32 a200, v243\n" \
"v_mfma_f32_16x16x32_bf16 v[140:143], a[88:91], a[16:19], v[140:143]\n" \
"ds_read_b64_tr_b16 a[126:127], v233 offset:60928\n" \
"v_accvgpr_write_b32 a201, v253\n" \
"v_mfma_f32_16x16x32_bf16 v[140:143], a[92:95], a[20:23], v[140:143]\n" \
"buffer_store_dwordx4 v[216:219], v236, s[56:59], s93 offen offset:2304 sc1\n" \
"v_mfma_f32_16x16x32_bf16 v[172:175], a[88:91], a[24:27], v[172:175]\n" \
"v_accvgpr_read_b32 v243, a216\n" \
"v_accvgpr_read_b32 v252, a217\n" \
"v_mfma_f32_16x16x32_bf16 v[172:175], a[92:95], a[28:31], v[172:175]\n" \
"v_accvgpr_read_b32 v253, a218\n" \
"v_accvgpr_read_b32 v254, a219\n" \
"v_mfma_f32_16x16x32_bf16 v[168:171], a[80:83], a[24:27], v[168:171]\n" \
"v_mul_f32 v243, s31, v243\n" \
"v_mul_f32 v252, s31, v252\n" \
"v_mfma_f32_16x16x32_bf16 v[168:171], a[84:87], a[28:31], v[168:171]\n" \
"v_mul_f32 v253, s31, v253\n" \
"v_mul_f32 v254, s31, v254\n" \
"v_mfma_f32_16x16x32_bf16 v[164:167], a[72:75], a[24:27], v[164:167]\n" \
"v_cvt_pk_bf16_f32 v243, v243, v252\n" \
"v_cvt_pk_bf16_f32 v253, v253, v254\n" \
"v_mfma_f32_16x16x32_bf16 v[164:167], a[76:79], a[28:31], v[164:167]\n" \
"v_accvgpr_write_b32 a202, v243\n" \
"v_accvgpr_write_b32 a203, v253\n" \
"v_mfma_f32_16x16x32_bf16 v[160:163], a[64:67], a[24:27], v[160:163]\n" \
"v_accvgpr_read_b32 v243, a204\n" \
"v_accvgpr_read_b32 v252, a205\n" \
"v_mfma_f32_16x16x32_bf16 v[160:163], a[68:71], a[28:31], v[160:163]\n" \
"s_waitcnt vmcnt(35) lgkmcnt(0)\n" \
"s_barrier\n" \
"v_mfma_f32_16x16x32_bf16 v[80:83], a[96:99], a[0:3], v[80:83]\n" \
"ds_read_b64_tr_b16 a[64:65], v232 offset:0\n" \
"v_accvgpr_read_b32 v253, a206\n" \
"v_mfma_f32_16x16x32_bf16 v[80:83], a[100:103], a[4:7], v[80:83]\n" \
"ds_read_b64_tr_b16 a[66:67], v232 offset:2048\n" \
"v_accvgpr_read_b32 v254, a207\n" \
"v_mfma_f32_16x16x32_bf16 v[84:87], a[104:107], a[0:3], v[84:87]\n" \
"s_add_u32 m0, s79, 49152\n" \
"ds_read_b64_tr_b16 a[68:69], v232 offset:8192\n" \
"v_mul_f32 v243, s31, v243\n" \
"v_mfma_f32_16x16x32_bf16 v[84:87], a[108:111], a[4:7], v[84:87]\n" \
"buffer_load_dwordx4 v228, s[52:55], s78 offen offset:0 lds\n" \
"v_mfma_f32_16x16x32_bf16 v[88:91], a[112:115], a[0:3], v[88:91]\n" \
"ds_read_b64_tr_b16 a[70:71], v232 offset:10240\n" \
"v_mul_f32 v252, s31, v252\n" \
"v_mfma_f32_16x16x32_bf16 v[88:91], a[116:119], a[4:7], v[88:91]\n" \
"ds_read_b64_tr_b16 a[72:73], v233 offset:0\n" \
"v_mul_f32 v253, s31, v253\n" \
"v_mfma_f32_16x16x32_bf16 v[92:95], a[120:123], a[0:3], v[92:95]\n" \
"ds_read_b64_tr_b16 a[74:75], v233 offset:2048\n" \
"v_mul_f32 v254, s31, v254\n" \
"v_mfma_f32_16x16x32_bf16 v[92:95], a[124:127], a[4:7], v[92:95]\n" \
"buffer_load_dwordx4 v229, s[52:55], s78 offen offset:1024 lds\n" \
"v_mfma_f32_16x16x32_bf16 v[124:127], a[120:123], a[8:11], v[124:127]\n" \
"s_waitcnt vmcnt(35)\n" \
"ds_read_b128 a[0:3], v230 offset:0\n" \
"v_cvt_pk_bf16_f32 v243, v243, v252\n" \
"v_mfma_f32_16x16x32_bf16 v[124:127], a[124:127], a[12:15], v[124:127]\n" \
"ds_read_b128 a[4:7], v231 offset:0\n" \
"v_cvt_pk_bf16_f32 v253, v253, v254\n" \
"v_mfma_f32_16x16x32_bf16 v[120:123], a[112:115], a[8:11], v[120:123]\n" \
"s_xor_b32 s78, s78, 0x80000\n" \
"ds_read_b64_tr_b16 a[76:77], v233 offset:8192\n" \
"v_accvgpr_write_b32 a204, v243\n" \
"v_mfma_f32_16x16x32_bf16 v[120:123], a[116:119], a[12:15], v[120:123]\n" \
"buffer_load_dwordx4 v228, s[52:55], s89 offen offset:2048 lds\n" \
"v_mfma_f32_16x16x32_bf16 v[116:119], a[104:107], a[8:11], v[116:119]\n" \
"ds_read_b64_tr_b16 a[78:79], v233 offset:10240\n" \
"v_accvgpr_write_b32 a205, v253\n" \
"v_mfma_f32_16x16x32_bf16 v[116:119], a[108:111], a[12:15], v[116:119]\n" \
"ds_read_b64_tr_b16 a[80:81], v232 offset:512\n" \
"v_accvgpr_read_b32 v243, a220\n" \
"v_mfma_f32_16x16x32_bf16 v[112:115], a[96:99], a[8:11], v[112:115]\n" \
"ds_read_b64_tr_b16 a[82:83], v232 offset:2560\n" \
"v_accvgpr_read_b32 v252, a221\n" \
"v_mfma_f32_16x16x32_bf16 v[112:115], a[100:103], a[12:15], v[112:115]\n" \
"buffer_load_dwordx4 v229, s[52:55], s89 offen offset:3072 lds\n" \
"v_mfma_f32_16x16x32_bf16 v[144:147], a[96:99], a[16:19], v[144:147]\n" \
"s_waitcnt vmcnt(35)\n" \
"s_xor_b32 s89, s89, 0x80000\n" \
"ds_read_b128 a[8:11], v230 offset:2048\n" \
"v_mfma_f32_16x16x32_bf16 v[144:147], a[100:103], a[20:23], v[144:147]\n" \
"s_add_u32 m0, s74, 65536\n" \
"ds_read_b128 a[12:15], v231 offset:2048\n" \
"v_accvgpr_read_b32 v253, a222\n" \
"v_mfma_f32_16x16x32_bf16 v[148:151], a[104:107], a[16:19], v[148:151]\n" \
"s_waitcnt lgkmcnt(6)\n" \
"ds_read_b64_tr_b16 a[84:85], v232 offset:8704\n" \
"v_accvgpr_read_b32 v254, a223\n" \
"v_mfma_f32_16x16x32_bf16 v[148:151], a[108:111], a[20:23], v[148:151]\n" \
"buffer_load_dwordx4 v224, s[48:51], s75 offen offset:0 lds\n" \
"v_mfma_f32_16x16x32_bf16 v[152:155], a[112:115], a[16:19], v[152:155]\n" \
"ds_read_b64_tr_b16 a[86:87], v232 offset:10752\n" \
"v_mul_f32 v243, s31, v243\n" \
"v_mfma_f32_16x16x32_bf16 v[152:155], a[116:119], a[20:23], v[152:155]\n" \
"ds_read_b64_tr_b16 a[88:89], v233 offset:512\n" \
"v_mul_f32 v252, s31, v252\n" \
"v_mfma_f32_16x16x32_bf16 v[156:159], a[120:123], a[16:19], v[156:159]\n" \
"ds_read_b64_tr_b16 a[90:91], v233 offset:2560\n" \
"v_mul_f32 v253, s31, v253\n" \
"v_mfma_f32_16x16x32_bf16 v[156:159], a[124:127], a[20:23], v[156:159]\n" \
"buffer_load_dwordx4 v225, s[48:51], s75 offen offset:1024 lds\n" \
"v_mfma_f32_16x16x32_bf16 v[188:191], a[120:123], a[24:27], v[188:191]\n" \
"s_waitcnt vmcnt(35)\n" \
"ds_read_b128 a[16:19], v230 offset:4096\n" \
"v_mul_f32 v254, s31, v254\n" \
"v_mfma_f32_16x16x32_bf16 v[188:191], a[124:127], a[28:31], v[188:191]\n" \
"ds_read_b128 a[20:23], v231 offset:4096\n" \
"v_cvt_pk_bf16_f32 v243, v243, v252\n" \
"v_mfma_f32_16x16x32_bf16 v[184:187], a[112:115], a[24:27], v[184:187]\n" \
"s_waitcnt lgkmcnt(6)\n" \
"ds_read_b64_tr_b16 a[92:93], v233 offset:8704\n" \
"v_cvt_pk_bf16_f32 v253, v253, v254\n" \
"v_mfma_f32_16x16x32_bf16 v[184:187], a[116:119], a[28:31], v[184:187]\n" \
"buffer_load_dwordx4 v226, s[48:51], s75 offen offset:2048 lds\n" \
"v_mfma_f32_16x16x32_bf16 v[180:183], a[104:107], a[24:27], v[180:183]\n" \
"ds_read_b64_tr_b16 a[94:95], v233 offset:10752\n" \
"v_accvgpr_write_b32 a206, v243\n" \
"v_mfma_f32_16x16x32_bf16 v[180:183], a[108:111], a[28:31], v[180:183]\n" \
"v_accvgpr_write_b32 a207, v253\n" \
"v_mul_f32 v32, s76, v32\n" \
"v_mfma_f32_16x16x32_bf16 v[176:179], a[96:99], a[24:27], v[176:179]\n" \
"v_mul_f32 v33, s76, v33\n" \
"v_mul_f32 v34, s76, v34\n" \
"v_mfma_f32_16x16x32_bf16 v[176:179], a[100:103], a[28:31], v[176:179]\n" \
"buffer_load_dwordx4 v227, s[48:51], s75 offen offset:3072 lds\n" \
"v_mfma_f32_16x16x32_bf16 v[64:67], a[64:67], a[0:3], v[64:67]\n" \
"s_waitcnt vmcnt(35)\n" \
"s_xor_b32 s75, s75, 0x80\n" \
"ds_read_b128 a[24:27], v230 offset:6144\n" \
"v_mfma_f32_16x16x32_bf16 v[64:67], a[68:71], a[4:7], v[64:67]\n" \
"s_add_u32 m0, s74, 69632\n" \
"ds_read_b128 a[28:31], v231 offset:6144\n" \
"v_mul_f32 v35, s76, v35\n" \
"v_mfma_f32_16x16x32_bf16 v[68:71], a[72:75], a[0:3], v[68:71]\n" \
"s_waitcnt lgkmcnt(4)\n" \
"ds_read_b64_tr_b16 a[96:97], v232 offset:1024\n" \
"v_cvt_pk_bf16_f32 v32, v32, v33\n" \
"v_mfma_f32_16x16x32_bf16 v[68:71], a[76:79], a[4:7], v[68:71]\n" \
"buffer_load_dwordx4 v224, s[48:51], s77 offen offset:0 lds\n" \
"v_mfma_f32_16x16x32_bf16 v[72:75], a[80:83], a[0:3], v[72:75]\n" \
"ds_read_b64_tr_b16 a[98:99], v232 offset:3072\n" \
"v_cvt_pk_bf16_f32 v33, v34, v35\n" \
"v_mfma_f32_16x16x32_bf16 v[72:75], a[84:87], a[4:7], v[72:75]\n" \
"s_waitcnt lgkmcnt(3)\n" \
"ds_read_b64_tr_b16 a[100:101], v232 offset:9216\n" \
"v_mul_f32 v36, s76, v36\n" \
"v_mfma_f32_16x16x32_bf16 v[76:79], a[88:91], a[0:3], v[76:79]\n" \
"ds_read_b64_tr_b16 a[102:103], v232 offset:11264\n" \
"v_mul_f32 v37, s76, v37\n" \
"v_mfma_f32_16x16x32_bf16 v[76:79], a[92:95], a[4:7], v[76:79]\n" \
"buffer_load_dwordx4 v225, s[48:51], s77 offen offset:1024 lds\n" \
"v_mfma_f32_16x16x32_bf16 v[108:111], a[88:91], a[8:11], v[108:111]\n" \
"ds_read_b64_tr_b16 a[104:105], v233 offset:1024\n" \
"v_mul_f32 v38, s76, v38\n" \
"v_mfma_f32_16x16x32_bf16 v[108:111], a[92:95], a[12:15], v[108:111]\n" \
"ds_read_b64_tr_b16 a[106:107], v233 offset:3072\n" \
"v_mul_f32 v39, s76, v39\n" \
"v_mfma_f32_16x16x32_bf16 v[104:107], a[80:83], a[8:11], v[104:107]\n" \
"s_waitcnt lgkmcnt(6)\n" \
"ds_read_b64_tr_b16 a[108:109], v233 offset:9216\n" \
"v_cvt_pk_bf16_f32 v34, v36, v37\n" \
"v_mfma_f32_16x16x32_bf16 v[104:107], a[84:87], a[12:15], v[104:107]\n" \
"buffer_load_dwordx4 v226, s[48:51], s77 offen offset:2048 lds\n" \
"v_mfma_f32_16x16x32_bf16 v[100:103], a[72:75], a[8:11], v[100:103]\n" \
"ds_read_b64_tr_b16 a[110:111], v233 offset:11264\n" \
"v_cvt_pk_bf16_f32 v35, v38, v39\n" \
"v_mfma_f32_16x16x32_bf16 v[100:103], a[76:79], a[12:15], v[100:103]\n" \
"ds_read_b64_tr_b16 a[112:113], v232 offset:1536\n" \
"v_mul_f32 v48, s76, v48\n" \
"v_mfma_f32_16x16x32_bf16 v[96:99], a[64:67], a[8:11], v[96:99]\n" \
"ds_read_b64_tr_b16 a[114:115], v232 offset:3584\n" \
"v_mul_f32 v49, s76, v49\n" \
"v_mfma_f32_16x16x32_bf16 v[96:99], a[68:71], a[12:15], v[96:99]\n" \
"buffer_load_dwordx4 v227, s[48:51], s77 offen offset:3072 lds\n" \
"v_mfma_f32_16x16x32_bf16 v[128:131], a[64:67], a[16:19], v[128:131]\n" \
"ds_read_b64_tr_b16 a[116:117], v232 offset:9728\n" \
"v_mul_f32 v50, s76, v50\n" \
"v_mfma_f32_16x16x32_bf16 v[128:131], a[68:71], a[20:23], v[128:131]\n" \
"ds_read_b64_tr_b16 a[118:119], v232 offset:11776\n" \
"v_mul_f32 v51, s76, v51\n" \
"v_mfma_f32_16x16x32_bf16 v[132:135], a[72:75], a[16:19], v[132:135]\n" \
"s_xor_b32 s77, s77, 0x80\n" \
"ds_read_b64_tr_b16 a[120:121], v233 offset:1536\n" \
"v_cvt_pk_bf16_f32 v48, v48, v49\n" \
"v_mfma_f32_16x16x32_bf16 v[132:135], a[76:79], a[20:23], v[132:135]\n" \
"v_cvt_pk_bf16_f32 v49, v50, v51\n" \
"v_mul_f32 v52, s76, v52\n" \
"v_mfma_f32_16x16x32_bf16 v[136:139], a[80:83], a[16:19], v[136:139]\n" \
"ds_read_b64_tr_b16 a[122:123], v233 offset:3584\n" \
"v_mul_f32 v53, s76, v53\n" \
"v_mfma_f32_16x16x32_bf16 v[136:139], a[84:87], a[20:23], v[136:139]\n" \
"ds_read_b64_tr_b16 a[124:125], v233 offset:9728\n" \
"v_mul_f32 v54, s76, v54\n" \
"v_mfma_f32_16x16x32_bf16 v[140:143], a[88:91], a[16:19], v[140:143]\n" \
"s_add_u32 s93, s87, 0\n" \
"ds_read_b64_tr_b16 a[126:127], v233 offset:11776\n" \
"v_mul_f32 v55, s76, v55\n" \
"v_mfma_f32_16x16x32_bf16 v[140:143], a[92:95], a[20:23], v[140:143]\n" \
"buffer_store_dwordx4 v[196:199], v236, s[56:59], s93 offen offset:0 sc1\n" \
"v_mfma_f32_16x16x32_bf16 v[172:175], a[88:91], a[24:27], v[172:175]\n" \
"v_cvt_pk_bf16_f32 v50, v52, v53\n" \
"v_cvt_pk_bf16_f32 v51, v54, v55\n" \
"v_mfma_f32_16x16x32_bf16 v[172:175], a[92:95], a[28:31], v[172:175]\n" \
"v_mul_f32 v40, s76, v40\n" \
"v_mul_f32 v41, s76, v41\n" \
"v_mfma_f32_16x16x32_bf16 v[168:171], a[80:83], a[24:27], v[168:171]\n" \
"v_mul_f32 v42, s76, v42\n" \
"v_mul_f32 v43, s76, v43\n" \
"v_mfma_f32_16x16x32_bf16 v[168:171], a[84:87], a[28:31], v[168:171]\n" \
"v_cvt_pk_bf16_f32 v40, v40, v41\n" \
"v_cvt_pk_bf16_f32 v41, v42, v43\n" \
"v_mfma_f32_16x16x32_bf16 v[164:167], a[72:75], a[24:27], v[164:167]\n" \
"v_mul_f32 v44, s76, v44\n" \
"v_mul_f32 v45, s76, v45\n" \
"v_mfma_f32_16x16x32_bf16 v[164:167], a[76:79], a[28:31], v[164:167]\n" \
"v_mul_f32 v46, s76, v46\n" \
"v_mul_f32 v47, s76, v47\n" \
"v_mfma_f32_16x16x32_bf16 v[160:163], a[64:67], a[24:27], v[160:163]\n" \
"v_cvt_pk_bf16_f32 v42, v44, v45\n" \
"v_cvt_pk_bf16_f32 v43, v46, v47\n" \
"v_mfma_f32_16x16x32_bf16 v[160:163], a[68:71], a[28:31], v[160:163]\n" \
"s_waitcnt vmcnt(35) lgkmcnt(0)\n" \
"s_barrier\n" \
"v_mfma_f32_16x16x32_bf16 v[80:83], a[96:99], a[0:3], v[80:83]\n" \
"ds_read_b64_tr_b16 a[64:65], v232 offset:16384\n" \
"v_mul_f32 v56, s76, v56\n" \
"v_mfma_f32_16x16x32_bf16 v[80:83], a[100:103], a[4:7], v[80:83]\n" \
"ds_read_b64_tr_b16 a[66:67], v232 offset:18432\n" \
"v_mul_f32 v57, s76, v57\n" \
"v_mfma_f32_16x16x32_bf16 v[84:87], a[104:107], a[0:3], v[84:87]\n" \
"s_add_u32 m0, s79, 0\n" \
"ds_read_b64_tr_b16 a[68:69], v232 offset:24576\n" \
"v_mul_f32 v58, s76, v58\n" \
"v_mfma_f32_16x16x32_bf16 v[84:87], a[108:111], a[4:7], v[84:87]\n" \
"buffer_load_dwordx4 v228, s[52:55], s78 offen offset:0 lds\n" \
"v_mfma_f32_16x16x32_bf16 v[88:91], a[112:115], a[0:3], v[88:91]\n" \
"ds_read_b64_tr_b16 a[70:71], v232 offset:26624\n" \
"v_mul_f32 v59, s76, v59\n" \
"v_mfma_f32_16x16x32_bf16 v[88:91], a[116:119], a[4:7], v[88:91]\n" \
"ds_read_b64_tr_b16 a[72:73], v233 offset:16384\n" \
"v_cvt_pk_bf16_f32 v56, v56, v57\n" \
"v_mfma_f32_16x16x32_bf16 v[92:95], a[120:123], a[0:3], v[92:95]\n" \
"ds_read_b64_tr_b16 a[74:75], v233 offset:18432\n" \
"v_cvt_pk_bf16_f32 v57, v58, v59\n" \
"v_mfma_f32_16x16x32_bf16 v[92:95], a[124:127], a[4:7], v[92:95]\n" \
"buffer_load_dwordx4 v229, s[52:55], s78 offen offset:1024 lds\n" \
"v_mfma_f32_16x16x32_bf16 v[124:127], a[120:123], a[8:11], v[124:127]\n" \
"s_waitcnt vmcnt(35)\n" \
"ds_read_b128 a[0:3], v230 offset:32768\n" \
"v_mul_f32 v60, s76, v60\n" \
"v_mfma_f32_16x16x32_bf16 v[124:127], a[124:127], a[12:15], v[124:127]\n" \
"ds_read_b128 a[4:7], v231 offset:32768\n" \
"v_mul_f32 v61, s76, v61\n" \
"v_mfma_f32_16x16x32_bf16 v[120:123], a[112:115], a[8:11], v[120:123]\n" \
"s_xor_b32 s78, s78, 0x780000\n" \
"ds_read_b64_tr_b16 a[76:77], v233 offset:24576\n" \
"v_mul_f32 v62, s76, v62\n" \
"v_mfma_f32_16x16x32_bf16 v[120:123], a[116:119], a[12:15], v[120:123]\n" \
"buffer_load_dwordx4 v228, s[52:55], s89 offen offset:2048 lds\n" \
"v_mfma_f32_16x16x32_bf16 v[116:119], a[104:107], a[8:11], v[116:119]\n" \
"ds_read_b64_tr_b16 a[78:79], v233 offset:26624\n" \
"v_mul_f32 v63, s76, v63\n" \
"v_mfma_f32_16x16x32_bf16 v[116:119], a[108:111], a[12:15], v[116:119]\n" \
"ds_read_b64_tr_b16 a[80:81], v232 offset:16896\n" \
"v_cvt_pk_bf16_f32 v58, v60, v61\n" \
"v_mfma_f32_16x16x32_bf16 v[112:115], a[96:99], a[8:11], v[112:115]\n" \
"ds_read_b64_tr_b16 a[82:83], v232 offset:18944\n" \
"v_cvt_pk_bf16_f32 v59, v62, v63\n" \
"v_mfma_f32_16x16x32_bf16 v[112:115], a[100:103], a[12:15], v[112:115]\n" \
"buffer_load_dwordx4 v229, s[52:55], s89 offen offset:3072 lds\n" \
"v_mfma_f32_16x16x32_bf16 v[144:147], a[96:99], a[16:19], v[144:147]\n" \
"s_waitcnt vmcnt(35)\n" \
"s_xor_b32 s89, s89, 0x780000\n" \
"ds_read_b128 a[8:11], v230 offset:34816\n" \
"v_mfma_f32_16x16x32_bf16 v[144:147], a[100:103], a[20:23], v[144:147]\n" \
"s_add_u32 m0, s74, 98304\n" \
"ds_read_b128 a[12:15], v231 offset:34816\n" \
"v_mul_f32 v0, s76, v0\n" \
"v_mfma_f32_16x16x32_bf16 v[148:151], a[104:107], a[16:19], v[148:151]\n" \
"s_waitcnt lgkmcnt(6)\n" \
"ds_read_b64_tr_b16 a[84:85], v232 offset:25088\n" \
"v_mul_f32 v1, s76, v1\n" \
"v_mfma_f32_16x16x32_bf16 v[148:151], a[108:111], a[20:23], v[148:151]\n" \
"buffer_load_dwordx4 v224, s[48:51], s75 offen offset:0 lds\n" \
"v_mfma_f32_16x16x32_bf16 v[152:155], a[112:115], a[16:19], v[152:155]\n" \
"ds_read_b64_tr_b16 a[86:87], v232 offset:27136\n" \
"v_mul_f32 v2, s76, v2\n" \
"v_mfma_f32_16x16x32_bf16 v[152:155], a[116:119], a[20:23], v[152:155]\n" \
"ds_read_b64_tr_b16 a[88:89], v233 offset:16896\n" \
"v_mul_f32 v3, s76, v3\n" \
"v_mfma_f32_16x16x32_bf16 v[156:159], a[120:123], a[16:19], v[156:159]\n" \
"ds_read_b64_tr_b16 a[90:91], v233 offset:18944\n" \
"v_cvt_pk_bf16_f32 v0, v0, v1\n" \
"v_mfma_f32_16x16x32_bf16 v[156:159], a[124:127], a[20:23], v[156:159]\n" \
"buffer_load_dwordx4 v225, s[48:51], s75 offen offset:1024 lds\n" \
"v_mfma_f32_16x16x32_bf16 v[188:191], a[120:123], a[24:27], v[188:191]\n" \
"s_waitcnt vmcnt(35)\n" \
"ds_read_b128 a[16:19], v230 offset:36864\n" \
"v_cvt_pk_bf16_f32 v1, v2, v3\n" \
"v_mfma_f32_16x16x32_bf16 v[188:191], a[124:127], a[28:31], v[188:191]\n" \
"ds_read_b128 a[20:23], v231 offset:36864\n" \
"v_mul_f32 v4, s76, v4\n" \
"v_mfma_f32_16x16x32_bf16 v[184:187], a[112:115], a[24:27], v[184:187]\n" \
"s_waitcnt lgkmcnt(6)\n" \
"ds_read_b64_tr_b16 a[92:93], v233 offset:25088\n" \
"v_mul_f32 v5, s76, v5\n" \
"v_mfma_f32_16x16x32_bf16 v[184:187], a[116:119], a[28:31], v[184:187]\n" \
"buffer_load_dwordx4 v226, s[48:51], s75 offen offset:2048 lds\n" \
"v_mfma_f32_16x16x32_bf16 v[180:183], a[104:107], a[24:27], v[180:183]\n" \
"ds_read_b64_tr_b16 a[94:95], v233 offset:27136\n" \
"v_mul_f32 v6, s76, v6\n" \
"v_mfma_f32_16x16x32_bf16 v[180:183], a[108:111], a[28:31], v[180:183]\n" \
"v_mul_f32 v7, s76, v7\n" \
"v_cvt_pk_bf16_f32 v2, v4, v5\n" \
"v_mfma_f32_16x16x32_bf16 v[176:179], a[96:99], a[24:27], v[176:179]\n" \
"v_cvt_pk_bf16_f32 v3, v6, v7\n" \
"v_mul_f32 v16, s76, v16\n" \
"v_mfma_f32_16x16x32_bf16 v[176:179], a[100:103], a[28:31], v[176:179]\n" \
"buffer_load_dwordx4 v227, s[48:51], s75 offen offset:3072 lds\n" \
"v_mfma_f32_16x16x32_bf16 v[64:67], a[64:67], a[0:3], v[64:67]\n" \
"s_waitcnt vmcnt(35)\n" \
"s_xor_b32 s75, s75, 0x780\n" \
"ds_read_b128 a[24:27], v230 offset:38912\n" \
"v_mfma_f32_16x16x32_bf16 v[64:67], a[68:71], a[4:7], v[64:67]\n" \
"s_add_u32 m0, s74, 102400\n" \
"ds_read_b128 a[28:31], v231 offset:38912\n" \
"v_mul_f32 v17, s76, v17\n" \
"v_mfma_f32_16x16x32_bf16 v[68:71], a[72:75], a[0:3], v[68:71]\n" \
"s_waitcnt lgkmcnt(4)\n" \
"ds_read_b64_tr_b16 a[96:97], v232 offset:17408\n" \
"v_mul_f32 v18, s76, v18\n" \
"v_mfma_f32_16x16x32_bf16 v[68:71], a[76:79], a[4:7], v[68:71]\n" \
"buffer_load_dwordx4 v224, s[48:51], s77 offen offset:0 lds\n" \
"v_mfma_f32_16x16x32_bf16 v[72:75], a[80:83], a[0:3], v[72:75]\n" \
"ds_read_b64_tr_b16 a[98:99], v232 offset:19456\n" \
"v_mul_f32 v19, s76, v19\n" \
"v_mfma_f32_16x16x32_bf16 v[72:75], a[84:87], a[4:7], v[72:75]\n" \
"s_waitcnt lgkmcnt(3)\n" \
"ds_read_b64_tr_b16 a[100:101], v232 offset:25600\n" \
"v_cvt_pk_bf16_f32 v16, v16, v17\n" \
"v_mfma_f32_16x16x32_bf16 v[76:79], a[88:91], a[0:3], v[76:79]\n" \
"ds_read_b64_tr_b16 a[102:103], v232 offset:27648\n" \
"v_cvt_pk_bf16_f32 v17, v18, v19\n" \
"v_mfma_f32_16x16x32_bf16 v[76:79], a[92:95], a[4:7], v[76:79]\n" \
"buffer_load_dwordx4 v225, s[48:51], s77 offen offset:1024 lds\n" \
"v_mfma_f32_16x16x32_bf16 v[108:111], a[88:91], a[8:11], v[108:111]\n" \
"ds_read_b64_tr_b16 a[104:105], v233 offset:17408\n" \
"v_mul_f32 v20, s76, v20\n" \
"v_mfma_f32_16x16x32_bf16 v[108:111], a[92:95], a[12:15], v[108:111]\n" \
"ds_read_b64_tr_b16 a[106:107], v233 offset:19456\n" \
"v_mul_f32 v21, s76, v21\n" \
"v_mfma_f32_16x16x32_bf16 v[104:107], a[80:83], a[8:11], v[104:107]\n" \
"s_waitcnt lgkmcnt(6)\n" \
"ds_read_b64_tr_b16 a[108:109], v233 offset:25600\n" \
"v_mul_f32 v22, s76, v22\n" \
"v_mfma_f32_16x16x32_bf16 v[104:107], a[84:87], a[12:15], v[104:107]\n" \
"buffer_load_dwordx4 v226, s[48:51], s77 offen offset:2048 lds\n" \
"v_mfma_f32_16x16x32_bf16 v[100:103], a[72:75], a[8:11], v[100:103]\n" \
"ds_read_b64_tr_b16 a[110:111], v233 offset:27648\n" \
"v_mul_f32 v23, s76, v23\n" \
"v_mfma_f32_16x16x32_bf16 v[100:103], a[76:79], a[12:15], v[100:103]\n" \
"ds_read_b64_tr_b16 a[112:113], v232 offset:17920\n" \
"v_cvt_pk_bf16_f32 v18, v20, v21\n" \
"v_mfma_f32_16x16x32_bf16 v[96:99], a[64:67], a[8:11], v[96:99]\n" \
"ds_read_b64_tr_b16 a[114:115], v232 offset:19968\n" \
"v_cvt_pk_bf16_f32 v19, v22, v23\n" \
"v_mfma_f32_16x16x32_bf16 v[96:99], a[68:71], a[12:15], v[96:99]\n" \
"buffer_load_dwordx4 v227, s[48:51], s77 offen offset:3072 lds\n" \
"v_mfma_f32_16x16x32_bf16 v[128:131], a[64:67], a[16:19], v[128:131]\n" \
"ds_read_b64_tr_b16 a[116:117], v232 offset:26112\n" \
"v_mul_f32 v8, s76, v8\n" \
"v_mfma_f32_16x16x32_bf16 v[128:131], a[68:71], a[20:23], v[128:131]\n" \
"ds_read_b64_tr_b16 a[118:119], v232 offset:28160\n" \
"v_mul_f32 v9, s76, v9\n" \
"v_mfma_f32_16x16x32_bf16 v[132:135], a[72:75], a[16:19], v[132:135]\n" \
"s_xor_b32 s77, s77, 0x780\n" \
"ds_read_b64_tr_b16 a[120:121], v233 offset:17920\n" \
"v_mul_f32 v10, s76, v10\n" \
"v_mfma_f32_16x16x32_bf16 v[132:135], a[76:79], a[20:23], v[132:135]\n" \
"v_mul_f32 v11, s76, v11\n" \
"v_cvt_pk_bf16_f32 v8, v8, v9\n" \
"v_mfma_f32_16x16x32_bf16 v[136:139], a[80:83], a[16:19], v[136:139]\n" \
"ds_read_b64_tr_b16 a[122:123], v233 offset:19968\n" \
"v_cvt_pk_bf16_f32 v9, v10, v11\n" \
"v_mfma_f32_16x16x32_bf16 v[136:139], a[84:87], a[20:23], v[136:139]\n" \
"ds_read_b64_tr_b16 a[124:125], v233 offset:26112\n" \
"v_mul_f32 v12, s76, v12\n" \
"v_mfma_f32_16x16x32_bf16 v[140:143], a[88:91], a[16:19], v[140:143]\n" \
"ds_read_b64_tr_b16 a[126:127], v233 offset:28160\n" \
"v_mul_f32 v13, s76, v13\n" \
"v_mfma_f32_16x16x32_bf16 v[140:143], a[92:95], a[20:23], v[140:143]\n" \
"buffer_store_dwordx4 v[212:215], v236, s[56:59], s93 offen offset:256 sc1\n" \
"v_mfma_f32_16x16x32_bf16 v[172:175], a[88:91], a[24:27], v[172:175]\n" \
"v_mul_f32 v14, s76, v14\n" \
"v_mul_f32 v15, s76, v15\n" \
"v_mfma_f32_16x16x32_bf16 v[172:175], a[92:95], a[28:31], v[172:175]\n" \
"v_cvt_pk_bf16_f32 v10, v12, v13\n" \
"v_cvt_pk_bf16_f32 v11, v14, v15\n" \
"v_mfma_f32_16x16x32_bf16 v[168:171], a[80:83], a[24:27], v[168:171]\n" \
"v_mul_f32 v24, s76, v24\n" \
"v_mul_f32 v25, s76, v25\n" \
"v_mfma_f32_16x16x32_bf16 v[168:171], a[84:87], a[28:31], v[168:171]\n" \
"v_mul_f32 v26, s76, v26\n" \
"v_mul_f32 v27, s76, v27\n" \
"v_mfma_f32_16x16x32_bf16 v[164:167], a[72:75], a[24:27], v[164:167]\n" \
"v_cvt_pk_bf16_f32 v24, v24, v25\n" \
"v_cvt_pk_bf16_f32 v25, v26, v27\n" \
"v_mfma_f32_16x16x32_bf16 v[164:167], a[76:79], a[28:31], v[164:167]\n" \
"v_mul_f32 v28, s76, v28\n" \
"v_mul_f32 v29, s76, v29\n" \
"v_mfma_f32_16x16x32_bf16 v[160:163], a[64:67], a[24:27], v[160:163]\n" \
"v_mul_f32 v30, s76, v30\n" \
"v_mul_f32 v31, s76, v31\n" \
"v_mfma_f32_16x16x32_bf16 v[160:163], a[68:71], a[28:31], v[160:163]\n" \
"s_waitcnt vmcnt(35) lgkmcnt(0)\n" \
"s_barrier\n" \
"v_mfma_f32_16x16x32_bf16 v[80:83], a[96:99], a[0:3], v[80:83]\n" \
"ds_read_b64_tr_b16 a[64:65], v232 offset:32768\n" \
"v_cvt_pk_bf16_f32 v26, v28, v29\n" \
"v_mfma_f32_16x16x32_bf16 v[80:83], a[100:103], a[4:7], v[80:83]\n" \
"ds_read_b64_tr_b16 a[66:67], v232 offset:34816\n" \
"v_cvt_pk_bf16_f32 v27, v30, v31\n" \
"v_mfma_f32_16x16x32_bf16 v[84:87], a[104:107], a[0:3], v[84:87]\n" \
"s_add_u32 m0, s79, 16384\n" \
"ds_read_b64_tr_b16 a[68:69], v232 offset:40960\n" \
"v_accvgpr_read_b32 v243, a160\n" \
"v_mfma_f32_16x16x32_bf16 v[84:87], a[108:111], a[4:7], v[84:87]\n" \
"buffer_load_dwordx4 v228, s[52:55], s78 offen offset:0 lds\n" \
"v_mfma_f32_16x16x32_bf16 v[88:91], a[112:115], a[0:3], v[88:91]\n" \
"ds_read_b64_tr_b16 a[70:71], v232 offset:43008\n" \
"v_accvgpr_read_b32 v252, a161\n" \
"v_mfma_f32_16x16x32_bf16 v[88:91], a[116:119], a[4:7], v[88:91]\n" \
"ds_read_b64_tr_b16 a[72:73], v233 offset:32768\n" \
"v_accvgpr_read_b32 v253, a162\n" \
"v_mfma_f32_16x16x32_bf16 v[92:95], a[120:123], a[0:3], v[92:95]\n" \
"ds_read_b64_tr_b16 a[74:75], v233 offset:34816\n" \
"v_accvgpr_read_b32 v254, a163\n" \
"v_mfma_f32_16x16x32_bf16 v[92:95], a[124:127], a[4:7], v[92:95]\n" \
"buffer_load_dwordx4 v229, s[52:55], s78 offen offset:1024 lds\n" \
"v_mfma_f32_16x16x32_bf16 v[124:127], a[120:123], a[8:11], v[124:127]\n" \
"s_waitcnt vmcnt(35)\n" \
"ds_read_b128 a[0:3], v250 offset:0\n" \
"v_mul_f32 v243, s31, v243\n" \
"v_mfma_f32_16x16x32_bf16 v[124:127], a[124:127], a[12:15], v[124:127]\n" \
"ds_read_b128 a[4:7], v251 offset:0\n" \
"v_mul_f32 v252, s31, v252\n" \
"v_mfma_f32_16x16x32_bf16 v[120:123], a[112:115], a[8:11], v[120:123]\n" \
"s_xor_b32 s78, s78, 0x80000\n" \
"ds_read_b64_tr_b16 a[76:77], v233 offset:40960\n" \
"v_mul_f32 v253, s31, v253\n" \
"v_mfma_f32_16x16x32_bf16 v[120:123], a[116:119], a[12:15], v[120:123]\n" \
"buffer_load_dwordx4 v228, s[52:55], s89 offen offset:2048 lds\n" \
"v_mfma_f32_16x16x32_bf16 v[116:119], a[104:107], a[8:11], v[116:119]\n" \
"ds_read_b64_tr_b16 a[78:79], v233 offset:43008\n" \
"v_mul_f32 v254, s31, v254\n" \
"v_mfma_f32_16x16x32_bf16 v[116:119], a[108:111], a[12:15], v[116:119]\n" \
"ds_read_b64_tr_b16 a[80:81], v232 offset:33280\n" \
"v_cvt_pk_bf16_f32 v36, v243, v252\n" \
"v_mfma_f32_16x16x32_bf16 v[112:115], a[96:99], a[8:11], v[112:115]\n" \
"ds_read_b64_tr_b16 a[82:83], v232 offset:35328\n" \
"v_cvt_pk_bf16_f32 v37, v253, v254\n" \
"v_mfma_f32_16x16x32_bf16 v[112:115], a[100:103], a[12:15], v[112:115]\n" \
"buffer_load_dwordx4 v229, s[52:55], s89 offen offset:3072 lds\n" \
"v_mfma_f32_16x16x32_bf16 v[144:147], a[96:99], a[16:19], v[144:147]\n" \
"s_waitcnt vmcnt(35)\n" \
"s_xor_b32 s89, s89, 0x80000\n" \
"ds_read_b128 a[8:11], v250 offset:2048\n" \
"v_mfma_f32_16x16x32_bf16 v[144:147], a[100:103], a[20:23], v[144:147]\n" \
"s_add_u32 m0, s74, 131072\n" \
"ds_read_b128 a[12:15], v251 offset:2048\n" \
"v_accvgpr_read_b32 v243, a176\n" \
"v_mfma_f32_16x16x32_bf16 v[148:151], a[104:107], a[16:19], v[148:151]\n" \
"s_waitcnt lgkmcnt(6)\n" \
"ds_read_b64_tr_b16 a[84:85], v232 offset:41472\n" \
"v_accvgpr_read_b32 v252, a177\n" \
"v_mfma_f32_16x16x32_bf16 v[148:151], a[108:111], a[20:23], v[148:151]\n" \
"buffer_load_dwordx4 v224, s[48:51], s75 offen offset:0 lds\n" \
"v_mfma_f32_16x16x32_bf16 v[152:155], a[112:115], a[16:19], v[152:155]\n" \
"ds_read_b64_tr_b16 a[86:87], v232 offset:43520\n" \
"v_accvgpr_read_b32 v253, a178\n" \
"v_mfma_f32_16x16x32_bf16 v[152:155], a[116:119], a[20:23], v[152:155]\n" \
"ds_read_b64_tr_b16 a[88:89], v233 offset:33280\n" \
"v_accvgpr_read_b32 v254, a179\n" \
"v_mfma_f32_16x16x32_bf16 v[156:159], a[120:123], a[16:19], v[156:159]\n" \
"ds_read_b64_tr_b16 a[90:91], v233 offset:35328\n" \
"v_mul_f32 v243, s31, v243\n" \
"v_mfma_f32_16x16x32_bf16 v[156:159], a[124:127], a[20:23], v[156:159]\n" \
"buffer_load_dwordx4 v225, s[48:51], s75 offen offset:1024 lds\n" \
"v_mfma_f32_16x16x32_bf16 v[188:191], a[120:123], a[24:27], v[188:191]\n" \
"s_waitcnt vmcnt(35)\n" \
"ds_read_b128 a[16:19], v250 offset:4096\n" \
"v_mul_f32 v252, s31, v252\n" \
"v_mfma_f32_16x16x32_bf16 v[188:191], a[124:127], a[28:31], v[188:191]\n" \
"ds_read_b128 a[20:23], v251 offset:4096\n" \
"v_mul_f32 v253, s31, v253\n" \
"v_mfma_f32_16x16x32_bf16 v[184:187], a[112:115], a[24:27], v[184:187]\n" \
"s_waitcnt lgkmcnt(6)\n" \
"ds_read_b64_tr_b16 a[92:93], v233 offset:41472\n" \
"v_mul_f32 v254, s31, v254\n" \
"v_mfma_f32_16x16x32_bf16 v[184:187], a[116:119], a[28:31], v[184:187]\n" \
"buffer_load_dwordx4 v226, s[48:51], s75 offen offset:2048 lds\n" \
"v_mfma_f32_16x16x32_bf16 v[180:183], a[104:107], a[24:27], v[180:183]\n" \
"ds_read_b64_tr_b16 a[94:95], v233 offset:43520\n" \
"v_cvt_pk_bf16_f32 v38, v243, v252\n" \
"v_mfma_f32_16x16x32_bf16 v[180:183], a[108:111], a[28:31], v[180:183]\n" \
"v_cvt_pk_bf16_f32 v39, v253, v254\n" \
"v_accvgpr_read_b32 v243, a164\n" \
"v_mfma_f32_16x16x32_bf16 v[176:179], a[96:99], a[24:27], v[176:179]\n" \
"v_accvgpr_read_b32 v252, a165\n" \
"v_accvgpr_read_b32 v253, a166\n" \
"v_mfma_f32_16x16x32_bf16 v[176:179], a[100:103], a[28:31], v[176:179]\n" \
"buffer_load_dwordx4 v227, s[48:51], s75 offen offset:3072 lds\n" \
"v_mfma_f32_16x16x32_bf16 v[64:67], a[64:67], a[0:3], v[64:67]\n" \
"s_waitcnt vmcnt(35)\n" \
"s_xor_b32 s75, s75, 0x80\n" \
"ds_read_b128 a[24:27], v250 offset:6144\n" \
"v_mfma_f32_16x16x32_bf16 v[64:67], a[68:71], a[4:7], v[64:67]\n" \
"s_add_u32 m0, s74, 135168\n" \
"ds_read_b128 a[28:31], v251 offset:6144\n" \
"v_accvgpr_read_b32 v254, a167\n" \
"v_mfma_f32_16x16x32_bf16 v[68:71], a[72:75], a[0:3], v[68:71]\n" \
"s_waitcnt lgkmcnt(4)\n" \
"ds_read_b64_tr_b16 a[96:97], v232 offset:33792\n" \
"v_mul_f32 v243, s31, v243\n" \
"v_mfma_f32_16x16x32_bf16 v[68:71], a[76:79], a[4:7], v[68:71]\n" \
"buffer_load_dwordx4 v224, s[48:51], s77 offen offset:0 lds\n" \
"v_mfma_f32_16x16x32_bf16 v[72:75], a[80:83], a[0:3], v[72:75]\n" \
"ds_read_b64_tr_b16 a[98:99], v232 offset:35840\n" \
"v_mul_f32 v252, s31, v252\n" \
"v_mfma_f32_16x16x32_bf16 v[72:75], a[84:87], a[4:7], v[72:75]\n" \
"s_waitcnt lgkmcnt(3)\n" \
"ds_read_b64_tr_b16 a[100:101], v232 offset:41984\n" \
"v_mul_f32 v253, s31, v253\n" \
"v_mfma_f32_16x16x32_bf16 v[76:79], a[88:91], a[0:3], v[76:79]\n" \
"ds_read_b64_tr_b16 a[102:103], v232 offset:44032\n" \
"v_mul_f32 v254, s31, v254\n" \
"v_mfma_f32_16x16x32_bf16 v[76:79], a[92:95], a[4:7], v[76:79]\n" \
"buffer_load_dwordx4 v225, s[48:51], s77 offen offset:1024 lds\n" \
"v_mfma_f32_16x16x32_bf16 v[108:111], a[88:91], a[8:11], v[108:111]\n" \
"ds_read_b64_tr_b16 a[104:105], v233 offset:33792\n" \
"v_cvt_pk_bf16_f32 v52, v243, v252\n" \
"v_mfma_f32_16x16x32_bf16 v[108:111], a[92:95], a[12:15], v[108:111]\n" \
"ds_read_b64_tr_b16 a[106:107], v233 offset:35840\n" \
"v_cvt_pk_bf16_f32 v53, v253, v254\n" \
"v_mfma_f32_16x16x32_bf16 v[104:107], a[80:83], a[8:11], v[104:107]\n" \
"s_waitcnt lgkmcnt(6)\n" \
"ds_read_b64_tr_b16 a[108:109], v233 offset:41984\n" \
"v_accvgpr_read_b32 v243, a180\n" \
"Lpg3342_%=:\n" \
"s_cbranch_execz Lpg5601_%=\n" \
"v_mfma_f32_16x16x32_bf16 v[104:107], a[84:87], a[12:15], v[104:107]\n" \
"buffer_load_dwordx4 v226, s[48:51], s77 offen offset:2048 lds\n" \
"v_mfma_f32_16x16x32_bf16 v[100:103], a[72:75], a[8:11], v[100:103]\n" \
"ds_read_b64_tr_b16 a[110:111], v233 offset:44032\n" \
"v_accvgpr_read_b32 v252, a181\n" \
"v_mfma_f32_16x16x32_bf16 v[100:103], a[76:79], a[12:15], v[100:103]\n" \
"ds_read_b64_tr_b16 a[112:113], v232 offset:34304\n" \
"v_accvgpr_read_b32 v253, a182\n" \
"v_mfma_f32_16x16x32_bf16 v[96:99], a[64:67], a[8:11], v[96:99]\n" \
"ds_read_b64_tr_b16 a[114:115], v232 offset:36352\n" \
"v_accvgpr_read_b32 v254, a183\n" \
"v_mfma_f32_16x16x32_bf16 v[96:99], a[68:71], a[12:15], v[96:99]\n" \
"buffer_load_dwordx4 v227, s[48:51], s77 offen offset:3072 lds\n" \
"v_mfma_f32_16x16x32_bf16 v[128:131], a[64:67], a[16:19], v[128:131]\n" \
"ds_read_b64_tr_b16 a[116:117], v232 offset:42496\n" \
"v_mul_f32 v243, s31, v243\n" \
"v_mfma_f32_16x16x32_bf16 v[128:131], a[68:71], a[20:23], v[128:131]\n" \
"ds_read_b64_tr_b16 a[118:119], v232 offset:44544\n" \
"v_mul_f32 v252, s31, v252\n" \
"v_mfma_f32_16x16x32_bf16 v[132:135], a[72:75], a[16:19], v[132:135]\n" \
"s_xor_b32 s77, s77, 0x80\n" \
"ds_read_b64_tr_b16 a[120:121], v233 offset:34304\n" \
"v_mul_f32 v253, s31, v253\n" \
"v_mfma_f32_16x16x32_bf16 v[132:135], a[76:79], a[20:23], v[132:135]\n" \
"v_mul_f32 v254, s31, v254\n" \
"v_cvt_pk_bf16_f32 v54, v243, v252\n" \
"v_mfma_f32_16x16x32_bf16 v[136:139], a[80:83], a[16:19], v[136:139]\n" \
"ds_read_b64_tr_b16 a[122:123], v233 offset:36352\n" \
"v_cvt_pk_bf16_f32 v55, v253, v254\n" \
"v_mfma_f32_16x16x32_bf16 v[136:139], a[84:87], a[20:23], v[136:139]\n" \
"ds_read_b64_tr_b16 a[124:125], v233 offset:42496\n" \
"v_accvgpr_read_b32 v243, a168\n" \
"v_mfma_f32_16x16x32_bf16 v[140:143], a[88:91], a[16:19], v[140:143]\n" \
"ds_read_b64_tr_b16 a[126:127], v233 offset:44544\n" \
"v_accvgpr_read_b32 v252, a169\n" \
"v_mfma_f32_16x16x32_bf16 v[140:143], a[92:95], a[20:23], v[140:143]\n" \
"buffer_store_dwordx4 v[204:207], v236, s[56:59], s93 offen offset:2048 sc1\n" \
"v_mfma_f32_16x16x32_bf16 v[172:175], a[88:91], a[24:27], v[172:175]\n" \
"v_accvgpr_read_b32 v253, a170\n" \
"v_accvgpr_read_b32 v254, a171\n" \
"v_mfma_f32_16x16x32_bf16 v[172:175], a[92:95], a[28:31], v[172:175]\n" \
"v_mul_f32 v243, s31, v243\n" \
"v_mul_f32 v252, s31, v252\n" \
"v_mfma_f32_16x16x32_bf16 v[168:171], a[80:83], a[24:27], v[168:171]\n" \
"v_mul_f32 v253, s31, v253\n" \
"v_mul_f32 v254, s31, v254\n" \
"v_mfma_f32_16x16x32_bf16 v[168:171], a[84:87], a[28:31], v[168:171]\n" \
"v_cvt_pk_bf16_f32 v44, v243, v252\n" \
"v_cvt_pk_bf16_f32 v45, v253, v254\n" \
"v_mfma_f32_16x16x32_bf16 v[164:167], a[72:75], a[24:27], v[164:167]\n" \
"v_accvgpr_read_b32 v243, a184\n" \
"v_accvgpr_read_b32 v252, a185\n" \
"v_mfma_f32_16x16x32_bf16 v[164:167], a[76:79], a[28:31], v[164:167]\n" \
"v_accvgpr_read_b32 v253, a186\n" \
"v_accvgpr_read_b32 v254, a187\n" \
"v_mfma_f32_16x16x32_bf16 v[160:163], a[64:67], a[24:27], v[160:163]\n" \
"v_mul_f32 v243, s31, v243\n" \
"v_mul_f32 v252, s31, v252\n" \
"v_mfma_f32_16x16x32_bf16 v[160:163], a[68:71], a[28:31], v[160:163]\n" \
"s_waitcnt vmcnt(35) lgkmcnt(0)\n" \
"s_barrier\n" \
"v_mfma_f32_16x16x32_bf16 v[80:83], a[96:99], a[0:3], v[80:83]\n" \
"ds_read_b64_tr_b16 a[64:65], v232 offset:49152\n" \
"v_mul_f32 v253, s31, v253\n" \
"v_mfma_f32_16x16x32_bf16 v[80:83], a[100:103], a[4:7], v[80:83]\n" \
"ds_read_b64_tr_b16 a[66:67], v232 offset:51200\n" \
"v_mul_f32 v254, s31, v254\n" \
"v_mfma_f32_16x16x32_bf16 v[84:87], a[104:107], a[0:3], v[84:87]\n" \
"s_add_u32 m0, s79, 32768\n" \
"ds_read_b64_tr_b16 a[68:69], v232 offset:57344\n" \
"v_cvt_pk_bf16_f32 v46, v243, v252\n" \
"v_mfma_f32_16x16x32_bf16 v[84:87], a[108:111], a[4:7], v[84:87]\n" \
"buffer_load_dwordx4 v228, s[52:55], s78 offen offset:0 lds\n" \
"v_mfma_f32_16x16x32_bf16 v[88:91], a[112:115], a[0:3], v[88:91]\n" \
"ds_read_b64_tr_b16 a[70:71], v232 offset:59392\n" \
"v_cvt_pk_bf16_f32 v47, v253, v254\n" \
"v_mfma_f32_16x16x32_bf16 v[88:91], a[116:119], a[4:7], v[88:91]\n" \
"ds_read_b64_tr_b16 a[72:73], v233 offset:49152\n" \
"v_accvgpr_read_b32 v243, a172\n" \
"v_mfma_f32_16x16x32_bf16 v[92:95], a[120:123], a[0:3], v[92:95]\n" \
"ds_read_b64_tr_b16 a[74:75], v233 offset:51200\n" \
"v_accvgpr_read_b32 v252, a173\n" \
"v_mfma_f32_16x16x32_bf16 v[92:95], a[124:127], a[4:7], v[92:95]\n" \
"buffer_load_dwordx4 v229, s[52:55], s78 offen offset:1024 lds\n" \
"v_mfma_f32_16x16x32_bf16 v[124:127], a[120:123], a[8:11], v[124:127]\n" \
"s_waitcnt vmcnt(35)\n" \
"ds_read_b128 a[0:3], v230 offset:0\n" \
"v_accvgpr_read_b32 v253, a174\n" \
"v_mfma_f32_16x16x32_bf16 v[124:127], a[124:127], a[12:15], v[124:127]\n" \
"ds_read_b128 a[4:7], v231 offset:0\n" \
"v_accvgpr_read_b32 v254, a175\n" \
"v_mfma_f32_16x16x32_bf16 v[120:123], a[112:115], a[8:11], v[120:123]\n" \
"s_xor_b32 s78, s78, 0x180000\n" \
"ds_read_b64_tr_b16 a[76:77], v233 offset:57344\n" \
"v_mul_f32 v243, s31, v243\n" \
"v_mfma_f32_16x16x32_bf16 v[120:123], a[116:119], a[12:15], v[120:123]\n" \
"buffer_load_dwordx4 v228, s[52:55], s89 offen offset:2048 lds\n" \
"v_mfma_f32_16x16x32_bf16 v[116:119], a[104:107], a[8:11], v[116:119]\n" \
"ds_read_b64_tr_b16 a[78:79], v233 offset:59392\n" \
"v_mul_f32 v252, s31, v252\n" \
"v_mfma_f32_16x16x32_bf16 v[116:119], a[108:111], a[12:15], v[116:119]\n" \
"ds_read_b64_tr_b16 a[80:81], v232 offset:49664\n" \
"v_mul_f32 v253, s31, v253\n" \
"v_mfma_f32_16x16x32_bf16 v[112:115], a[96:99], a[8:11], v[112:115]\n" \
"ds_read_b64_tr_b16 a[82:83], v232 offset:51712\n" \
"v_mul_f32 v254, s31, v254\n" \
"v_mfma_f32_16x16x32_bf16 v[112:115], a[100:103], a[12:15], v[112:115]\n" \
"buffer_load_dwordx4 v229, s[52:55], s89 offen offset:3072 lds\n" \
"v_mfma_f32_16x16x32_bf16 v[144:147], a[96:99], a[16:19], v[144:147]\n" \
"s_waitcnt vmcnt(35)\n" \
"s_xor_b32 s89, s89, 0x180000\n" \
"ds_read_b128 a[8:11], v230 offset:2048\n" \
"v_mfma_f32_16x16x32_bf16 v[144:147], a[100:103], a[20:23], v[144:147]\n" \
"s_add_u32 m0, s74, 65536\n" \
"ds_read_b128 a[12:15], v231 offset:2048\n" \
"v_cvt_pk_bf16_f32 v60, v243, v252\n" \
"v_mfma_f32_16x16x32_bf16 v[148:151], a[104:107], a[16:19], v[148:151]\n" \
"s_waitcnt lgkmcnt(6)\n" \
"ds_read_b64_tr_b16 a[84:85], v232 offset:57856\n" \
"v_cvt_pk_bf16_f32 v61, v253, v254\n" \
"v_mfma_f32_16x16x32_bf16 v[148:151], a[108:111], a[20:23], v[148:151]\n" \
"buffer_load_dwordx4 v224, s[48:51], s75 offen offset:0 lds\n" \
"v_mfma_f32_16x16x32_bf16 v[152:155], a[112:115], a[16:19], v[152:155]\n" \
"ds_read_b64_tr_b16 a[86:87], v232 offset:59904\n" \
"v_accvgpr_read_b32 v243, a188\n" \
"v_mfma_f32_16x16x32_bf16 v[152:155], a[116:119], a[20:23], v[152:155]\n" \
"ds_read_b64_tr_b16 a[88:89], v233 offset:49664\n" \
"v_accvgpr_read_b32 v252, a189\n" \
"v_mfma_f32_16x16x32_bf16 v[156:159], a[120:123], a[16:19], v[156:159]\n" \
"ds_read_b64_tr_b16 a[90:91], v233 offset:51712\n" \
"v_accvgpr_read_b32 v253, a190\n" \
"v_mfma_f32_16x16x32_bf16 v[156:159], a[124:127], a[20:23], v[156:159]\n" \
"buffer_load_dwordx4 v225, s[48:51], s75 offen offset:1024 lds\n" \
"v_mfma_f32_16x16x32_bf16 v[188:191], a[120:123], a[24:27], v[188:191]\n" \
"s_waitcnt vmcnt(35)\n" \
"ds_read_b128 a[16:19], v230 offset:4096\n" \
"v_accvgpr_read_b32 v254, a191\n" \
"v_mfma_f32_16x16x32_bf16 v[188:191], a[124:127], a[28:31], v[188:191]\n" \
"ds_read_b128 a[20:23], v231 offset:4096\n" \
"v_mul_f32 v243, s31, v243\n" \
"v_mfma_f32_16x16x32_bf16 v[184:187], a[112:115], a[24:27], v[184:187]\n" \
"s_waitcnt lgkmcnt(6)\n" \
"ds_read_b64_tr_b16 a[92:93], v233 offset:57856\n" \
"v_mul_f32 v252, s31, v252\n" \
"v_mfma_f32_16x16x32_bf16 v[184:187], a[116:119], a[28:31], v[184:187]\n" \
"buffer_load_dwordx4 v226, s[48:51], s75 offen offset:2048 lds\n" \
"v_mfma_f32_16x16x32_bf16 v[180:183], a[104:107], a[24:27], v[180:183]\n" \
"ds_read_b64_tr_b16 a[94:95], v233 offset:59904\n" \
"v_mul_f32 v253, s31, v253\n" \
"v_mfma_f32_16x16x32_bf16 v[180:183], a[108:111], a[28:31], v[180:183]\n" \
"v_mul_f32 v254, s31, v254\n" \
"v_cvt_pk_bf16_f32 v62, v243, v252\n" \
"v_mfma_f32_16x16x32_bf16 v[176:179], a[96:99], a[24:27], v[176:179]\n" \
"v_cvt_pk_bf16_f32 v63, v253, v254\n" \
"v_accvgpr_read_b32 v243, a128\n" \
"v_mfma_f32_16x16x32_bf16 v[176:179], a[100:103], a[28:31], v[176:179]\n" \
"buffer_load_dwordx4 v227, s[48:51], s75 offen offset:3072 lds\n" \
"v_mfma_f32_16x16x32_bf16 v[64:67], a[64:67], a[0:3], v[64:67]\n" \
"s_waitcnt vmcnt(35)\n" \
"s_xor_b32 s75, s75, 0x180\n" \
"ds_read_b128 a[24:27], v230 offset:6144\n" \
"v_mfma_f32_16x16x32_bf16 v[64:67], a[68:71], a[4:7], v[64:67]\n" \
"s_add_u32 m0, s74, 69632\n" \
"ds_read_b128 a[28:31], v231 offset:6144\n" \
"v_accvgpr_read_b32 v252, a129\n" \
"v_mfma_f32_16x16x32_bf16 v[68:71], a[72:75], a[0:3], v[68:71]\n" \
"s_waitcnt lgkmcnt(4)\n" \
"ds_read_b64_tr_b16 a[96:97], v232 offset:50176\n" \
"v_accvgpr_read_b32 v253, a130\n" \
"v_mfma_f32_16x16x32_bf16 v[68:71], a[76:79], a[4:7], v[68:71]\n" \
"buffer_load_dwordx4 v224, s[48:51], s77 offen offset:0 lds\n" \
"v_mfma_f32_16x16x32_bf16 v[72:75], a[80:83], a[0:3], v[72:75]\n" \
"ds_read_b64_tr_b16 a[98:99], v232 offset:52224\n" \
"v_accvgpr_read_b32 v254, a131\n" \
"v_mfma_f32_16x16x32_bf16 v[72:75], a[84:87], a[4:7], v[72:75]\n" \
"s_waitcnt lgkmcnt(3)\n" \
"ds_read_b64_tr_b16 a[100:101], v232 offset:58368\n" \
"v_mul_f32 v243, s31, v243\n" \
"v_mfma_f32_16x16x32_bf16 v[76:79], a[88:91], a[0:3], v[76:79]\n" \
"ds_read_b64_tr_b16 a[102:103], v232 offset:60416\n" \
"v_mul_f32 v252, s31, v252\n" \
"v_mfma_f32_16x16x32_bf16 v[76:79], a[92:95], a[4:7], v[76:79]\n" \
"buffer_load_dwordx4 v225, s[48:51], s77 offen offset:1024 lds\n" \
"v_mfma_f32_16x16x32_bf16 v[108:111], a[88:91], a[8:11], v[108:111]\n" \
"ds_read_b64_tr_b16 a[104:105], v233 offset:50176\n" \
"v_mul_f32 v253, s31, v253\n" \
"v_mfma_f32_16x16x32_bf16 v[108:111], a[92:95], a[12:15], v[108:111]\n" \
"ds_read_b64_tr_b16 a[106:107], v233 offset:52224\n" \
"v_mul_f32 v254, s31, v254\n" \
"v_mfma_f32_16x16x32_bf16 v[104:107], a[80:83], a[8:11], v[104:107]\n" \
"s_waitcnt lgkmcnt(6)\n" \
"ds_read_b64_tr_b16 a[108:109], v233 offset:58368\n" \
"v_cvt_pk_bf16_f32 v4, v243, v252\n" \
"v_mfma_f32_16x16x32_bf16 v[104:107], a[84:87], a[12:15], v[104:107]\n" \
"buffer_load_dwordx4 v226, s[48:51], s77 offen offset:2048 lds\n" \
"v_mfma_f32_16x16x32_bf16 v[100:103], a[72:75], a[8:11], v[100:103]\n" \
"ds_read_b64_tr_b16 a[110:111], v233 offset:60416\n" \
"v_cvt_pk_bf16_f32 v5, v253, v254\n" \
"v_mfma_f32_16x16x32_bf16 v[100:103], a[76:79], a[12:15], v[100:103]\n" \
"ds_read_b64_tr_b16 a[112:113], v232 offset:50688\n" \
"v_accvgpr_read_b32 v243, a144\n" \
"v_mfma_f32_16x16x32_bf16 v[96:99], a[64:67], a[8:11], v[96:99]\n" \
"ds_read_b64_tr_b16 a[114:115], v232 offset:52736\n" \
"v_accvgpr_read_b32 v252, a145\n" \
"v_mfma_f32_16x16x32_bf16 v[96:99], a[68:71], a[12:15], v[96:99]\n" \
"buffer_load_dwordx4 v227, s[48:51], s77 offen offset:3072 lds\n" \
"v_mfma_f32_16x16x32_bf16 v[128:131], a[64:67], a[16:19], v[128:131]\n" \
"ds_read_b64_tr_b16 a[116:117], v232 offset:58880\n" \
"v_accvgpr_read_b32 v253, a146\n" \
"v_mfma_f32_16x16x32_bf16 v[128:131], a[68:71], a[20:23], v[128:131]\n" \
"ds_read_b64_tr_b16 a[118:119], v232 offset:60928\n" \
"v_accvgpr_read_b32 v254, a147\n" \
"v_mfma_f32_16x16x32_bf16 v[132:135], a[72:75], a[16:19], v[132:135]\n" \
"s_xor_b32 s77, s77, 0x180\n" \
"ds_read_b64_tr_b16 a[120:121], v233 offset:50688\n" \
"v_mul_f32 v243, s31, v243\n" \
"v_mfma_f32_16x16x32_bf16 v[132:135], a[76:79], a[20:23], v[132:135]\n" \
"v_mul_f32 v252, s31, v252\n" \
"v_mul_f32 v253, s31, v253\n" \
"v_mfma_f32_16x16x32_bf16 v[136:139], a[80:83], a[16:19], v[136:139]\n" \
"ds_read_b64_tr_b16 a[122:123], v233 offset:52736\n" \
"v_mul_f32 v254, s31, v254\n" \
"v_mfma_f32_16x16x32_bf16 v[136:139], a[84:87], a[20:23], v[136:139]\n" \
"ds_read_b64_tr_b16 a[124:125], v233 offset:58880\n" \
"v_cvt_pk_bf16_f32 v6, v243, v252\n" \
"v_mfma_f32_16x16x32_bf16 v[140:143], a[88:91], a[16:19], v[140:143]\n" \
"ds_read_b64_tr_b16 a[126:127], v233 offset:60928\n" \
"v_cvt_pk_bf16_f32 v7, v253, v254\n" \
"v_mfma_f32_16x16x32_bf16 v[140:143], a[92:95], a[20:23], v[140:143]\n" \
"buffer_store_dwordx4 v[220:223], v236, s[56:59], s93 offen offset:2304 sc1\n" \
"v_mfma_f32_16x16x32_bf16 v[172:175], a[88:91], a[24:27], v[172:175]\n" \
"v_accvgpr_read_b32 v243, a132\n" \
"v_accvgpr_read_b32 v252, a133\n" \
"v_mfma_f32_16x16x32_bf16 v[172:175], a[92:95], a[28:31], v[172:175]\n" \
"v_accvgpr_read_b32 v253, a134\n" \
"v_accvgpr_read_b32 v254, a135\n" \
"v_mfma_f32_16x16x32_bf16 v[168:171], a[80:83], a[24:27], v[168:171]\n" \
"v_mul_f32 v243, s31, v243\n" \
"v_mul_f32 v252, s31, v252\n" \
"v_mfma_f32_16x16x32_bf16 v[168:171], a[84:87], a[28:31], v[168:171]\n" \
"v_mul_f32 v253, s31, v253\n" \
"v_mul_f32 v254, s31, v254\n" \
"v_mfma_f32_16x16x32_bf16 v[164:167], a[72:75], a[24:27], v[164:167]\n" \
"v_cvt_pk_bf16_f32 v20, v243, v252\n" \
"v_cvt_pk_bf16_f32 v21, v253, v254\n" \
"v_mfma_f32_16x16x32_bf16 v[164:167], a[76:79], a[28:31], v[164:167]\n" \
"v_accvgpr_read_b32 v243, a148\n" \
"v_accvgpr_read_b32 v252, a149\n" \
"v_mfma_f32_16x16x32_bf16 v[160:163], a[64:67], a[24:27], v[160:163]\n" \
"v_accvgpr_read_b32 v253, a150\n" \
"v_accvgpr_read_b32 v254, a151\n" \
"v_mfma_f32_16x16x32_bf16 v[160:163], a[68:71], a[28:31], v[160:163]\n" \
"s_waitcnt vmcnt(35) lgkmcnt(0)\n" \
"s_barrier\n" \
"v_mfma_f32_16x16x32_bf16 v[80:83], a[96:99], a[0:3], v[80:83]\n" \
"ds_read_b64_tr_b16 a[64:65], v232 offset:0\n" \
"v_mul_f32 v243, s31, v243\n" \
"v_mfma_f32_16x16x32_bf16 v[80:83], a[100:103], a[4:7], v[80:83]\n" \
"ds_read_b64_tr_b16 a[66:67], v232 offset:2048\n" \
"v_mul_f32 v252, s31, v252\n" \
"v_mfma_f32_16x16x32_bf16 v[84:87], a[104:107], a[0:3], v[84:87]\n" \
"s_add_u32 m0, s79, 49152\n" \
"ds_read_b64_tr_b16 a[68:69], v232 offset:8192\n" \
"v_mul_f32 v253, s31, v253\n" \
"v_mfma_f32_16x16x32_bf16 v[84:87], a[108:111], a[4:7], v[84:87]\n" \
"buffer_load_dwordx4 v228, s[52:55], s78 offen offset:0 lds\n" \
"v_mfma_f32_16x16x32_bf16 v[88:91], a[112:115], a[0:3], v[88:91]\n" \
"ds_read_b64_tr_b16 a[70:71], v232 offset:10240\n" \
"v_mul_f32 v254, s31, v254\n" \
"v_mfma_f32_16x16x32_bf16 v[88:91], a[116:119], a[4:7], v[88:91]\n" \
"ds_read_b64_tr_b16 a[72:73], v233 offset:0\n" \
"v_cvt_pk_bf16_f32 v22, v243, v252\n" \
"v_mfma_f32_16x16x32_bf16 v[92:95], a[120:123], a[0:3], v[92:95]\n" \
"ds_read_b64_tr_b16 a[74:75], v233 offset:2048\n" \
"v_cvt_pk_bf16_f32 v23, v253, v254\n" \
"v_mfma_f32_16x16x32_bf16 v[92:95], a[124:127], a[4:7], v[92:95]\n" \
"buffer_load_dwordx4 v229, s[52:55], s78 offen offset:1024 lds\n" \
"v_mfma_f32_16x16x32_bf16 v[124:127], a[120:123], a[8:11], v[124:127]\n" \
"s_waitcnt vmcnt(35)\n" \
"ds_read_b128 a[0:3], v230 offset:32768\n" \
"v_accvgpr_read_b32 v243, a136\n" \
"v_mfma_f32_16x16x32_bf16 v[124:127], a[124:127], a[12:15], v[124:127]\n" \
"ds_read_b128 a[4:7], v231 offset:32768\n" \
"v_accvgpr_read_b32 v252, a137\n" \
"v_mfma_f32_16x16x32_bf16 v[120:123], a[112:115], a[8:11], v[120:123]\n" \
"s_xor_b32 s78, s78, 0x300000\n" \
"ds_read_b64_tr_b16 a[76:77], v233 offset:8192\n" \
"v_accvgpr_read_b32 v253, a138\n" \
"v_mfma_f32_16x16x32_bf16 v[120:123], a[116:119], a[12:15], v[120:123]\n" \
"buffer_load_dwordx4 v228, s[52:55], s89 offen offset:2048 lds\n" \
"v_mfma_f32_16x16x32_bf16 v[116:119], a[104:107], a[8:11], v[116:119]\n" \
"ds_read_b64_tr_b16 a[78:79], v233 offset:10240\n" \
"v_accvgpr_read_b32 v254, a139\n" \
"v_mfma_f32_16x16x32_bf16 v[116:119], a[108:111], a[12:15], v[116:119]\n" \
"ds_read_b64_tr_b16 a[80:81], v232 offset:512\n" \
"v_mul_f32 v243, s31, v243\n" \
"v_mfma_f32_16x16x32_bf16 v[112:115], a[96:99], a[8:11], v[112:115]\n" \
"ds_read_b64_tr_b16 a[82:83], v232 offset:2560\n" \
"v_mul_f32 v252, s31, v252\n" \
"v_mfma_f32_16x16x32_bf16 v[112:115], a[100:103], a[12:15], v[112:115]\n" \
"buffer_load_dwordx4 v229, s[52:55], s89 offen offset:3072 lds\n" \
"v_mfma_f32_16x16x32_bf16 v[144:147], a[96:99], a[16:19], v[144:147]\n" \
"s_waitcnt vmcnt(35)\n" \
"s_xor_b32 s89, s89, 0x300000\n" \
"ds_read_b128 a[8:11], v230 offset:34816\n" \
"v_mfma_f32_16x16x32_bf16 v[144:147], a[100:103], a[20:23], v[144:147]\n" \
"s_add_u32 m0, s74, 98304\n" \
"ds_read_b128 a[12:15], v231 offset:34816\n" \
"v_mul_f32 v253, s31, v253\n" \
"v_mfma_f32_16x16x32_bf16 v[148:151], a[104:107], a[16:19], v[148:151]\n" \
"s_waitcnt lgkmcnt(6)\n" \
"ds_read_b64_tr_b16 a[84:85], v232 offset:8704\n" \
"v_mul_f32 v254, s31, v254\n" \
"v_mfma_f32_16x16x32_bf16 v[148:151], a[108:111], a[20:23], v[148:151]\n" \
"buffer_load_dwordx4 v224, s[48:51], s75 offen offset:0 lds\n" \
"v_mfma_f32_16x16x32_bf16 v[152:155], a[112:115], a[16:19], v[152:155]\n" \
"ds_read_b64_tr_b16 a[86:87], v232 offset:10752\n" \
"v_cvt_pk_bf16_f32 v12, v243, v252\n" \
"v_mfma_f32_16x16x32_bf16 v[152:155], a[116:119], a[20:23], v[152:155]\n" \
"ds_read_b64_tr_b16 a[88:89], v233 offset:512\n" \
"v_cvt_pk_bf16_f32 v13, v253, v254\n" \
"v_mfma_f32_16x16x32_bf16 v[156:159], a[120:123], a[16:19], v[156:159]\n" \
"ds_read_b64_tr_b16 a[90:91], v233 offset:2560\n" \
"v_accvgpr_read_b32 v243, a152\n" \
"v_mfma_f32_16x16x32_bf16 v[156:159], a[124:127], a[20:23], v[156:159]\n" \
"buffer_load_dwordx4 v225, s[48:51], s75 offen offset:1024 lds\n" \
"v_mfma_f32_16x16x32_bf16 v[188:191], a[120:123], a[24:27], v[188:191]\n" \
"s_waitcnt vmcnt(35)\n" \
"ds_read_b128 a[16:19], v230 offset:36864\n" \
"v_accvgpr_read_b32 v252, a153\n" \
"v_mfma_f32_16x16x32_bf16 v[188:191], a[124:127], a[28:31], v[188:191]\n" \
"ds_read_b128 a[20:23], v231 offset:36864\n" \
"v_accvgpr_read_b32 v253, a154\n" \
"v_mfma_f32_16x16x32_bf16 v[184:187], a[112:115], a[24:27], v[184:187]\n" \
"s_waitcnt lgkmcnt(6)\n" \
"ds_read_b64_tr_b16 a[92:93], v233 offset:8704\n" \
"v_accvgpr_read_b32 v254, a155\n" \
"v_mfma_f32_16x16x32_bf16 v[184:187], a[116:119], a[28:31], v[184:187]\n" \
"buffer_load_dwordx4 v226, s[48:51], s75 offen offset:2048 lds\n" \
"v_mfma_f32_16x16x32_bf16 v[180:183], a[104:107], a[24:27], v[180:183]\n" \
"ds_read_b64_tr_b16 a[94:95], v233 offset:10752\n" \
"v_mul_f32 v243, s31, v243\n" \
"v_mfma_f32_16x16x32_bf16 v[180:183], a[108:111], a[28:31], v[180:183]\n" \
"v_mul_f32 v252, s31, v252\n" \
"v_mul_f32 v253, s31, v253\n" \
"v_mfma_f32_16x16x32_bf16 v[176:179], a[96:99], a[24:27], v[176:179]\n" \
"v_mul_f32 v254, s31, v254\n" \
"v_cvt_pk_bf16_f32 v14, v243, v252\n" \
"v_mfma_f32_16x16x32_bf16 v[176:179], a[100:103], a[28:31], v[176:179]\n" \
"buffer_load_dwordx4 v227, s[48:51], s75 offen offset:3072 lds\n" \
"v_mfma_f32_16x16x32_bf16 v[64:67], a[64:67], a[0:3], v[64:67]\n" \
"s_waitcnt vmcnt(35)\n" \
"s_xor_b32 s75, s75, 0x300\n" \
"ds_read_b128 a[24:27], v230 offset:38912\n" \
"v_mfma_f32_16x16x32_bf16 v[64:67], a[68:71], a[4:7], v[64:67]\n" \
"s_add_u32 m0, s74, 102400\n" \
"ds_read_b128 a[28:31], v231 offset:38912\n" \
"v_cvt_pk_bf16_f32 v15, v253, v254\n" \
"v_mfma_f32_16x16x32_bf16 v[68:71], a[72:75], a[0:3], v[68:71]\n" \
"s_waitcnt lgkmcnt(4)\n" \
"ds_read_b64_tr_b16 a[96:97], v232 offset:1024\n" \
"v_accvgpr_read_b32 v243, a140\n" \
"v_mfma_f32_16x16x32_bf16 v[68:71], a[76:79], a[4:7], v[68:71]\n" \
"buffer_load_dwordx4 v224, s[48:51], s77 offen offset:0 lds\n" \
"v_mfma_f32_16x16x32_bf16 v[72:75], a[80:83], a[0:3], v[72:75]\n" \
"ds_read_b64_tr_b16 a[98:99], v232 offset:3072\n" \
"v_accvgpr_read_b32 v252, a141\n" \
"v_mfma_f32_16x16x32_bf16 v[72:75], a[84:87], a[4:7], v[72:75]\n" \
"s_waitcnt lgkmcnt(3)\n" \
"ds_read_b64_tr_b16 a[100:101], v232 offset:9216\n" \
"v_accvgpr_read_b32 v253, a142\n" \
"v_mfma_f32_16x16x32_bf16 v[76:79], a[88:91], a[0:3], v[76:79]\n" \
"ds_read_b64_tr_b16 a[102:103], v232 offset:11264\n" \
"v_accvgpr_read_b32 v254, a143\n" \
"v_mfma_f32_16x16x32_bf16 v[76:79], a[92:95], a[4:7], v[76:79]\n" \
"buffer_load_dwordx4 v225, s[48:51], s77 offen offset:1024 lds\n" \
"v_mfma_f32_16x16x32_bf16 v[108:111], a[88:91], a[8:11], v[108:111]\n" \
"ds_read_b64_tr_b16 a[104:105], v233 offset:1024\n" \
"v_mul_f32 v243, s31, v243\n" \
"v_mfma_f32_16x16x32_bf16 v[108:111], a[92:95], a[12:15], v[108:111]\n" \
"ds_read_b64_tr_b16 a[106:107], v233 offset:3072\n" \
"v_mul_f32 v252, s31, v252\n" \
"v_mfma_f32_16x16x32_bf16 v[104:107], a[80:83], a[8:11], v[104:107]\n" \
"s_waitcnt lgkmcnt(6)\n" \
"ds_read_b64_tr_b16 a[108:109], v233 offset:9216\n" \
"v_mul_f32 v253, s31, v253\n" \
"v_mfma_f32_16x16x32_bf16 v[104:107], a[84:87], a[12:15], v[104:107]\n" \
"buffer_load_dwordx4 v226, s[48:51], s77 offen offset:2048 lds\n" \
"v_mfma_f32_16x16x32_bf16 v[100:103], a[72:75], a[8:11], v[100:103]\n" \
"ds_read_b64_tr_b16 a[110:111], v233 offset:11264\n" \
"v_mul_f32 v254, s31, v254\n" \
"v_mfma_f32_16x16x32_bf16 v[100:103], a[76:79], a[12:15], v[100:103]\n" \
"ds_read_b64_tr_b16 a[112:113], v232 offset:1536\n" \
"v_cvt_pk_bf16_f32 v28, v243, v252\n" \
"v_mfma_f32_16x16x32_bf16 v[96:99], a[64:67], a[8:11], v[96:99]\n" \
"ds_read_b64_tr_b16 a[114:115], v232 offset:3584\n" \
"v_cvt_pk_bf16_f32 v29, v253, v254\n" \
"v_mfma_f32_16x16x32_bf16 v[96:99], a[68:71], a[12:15], v[96:99]\n" \
"buffer_load_dwordx4 v227, s[48:51], s77 offen offset:3072 lds\n" \
"v_mfma_f32_16x16x32_bf16 v[128:131], a[64:67], a[16:19], v[128:131]\n" \
"ds_read_b64_tr_b16 a[116:117], v232 offset:9728\n" \
"v_accvgpr_read_b32 v243, a156\n" \
"v_mfma_f32_16x16x32_bf16 v[128:131], a[68:71], a[20:23], v[128:131]\n" \
"ds_read_b64_tr_b16 a[118:119], v232 offset:11776\n" \
"v_accvgpr_read_b32 v252, a157\n" \
"v_mfma_f32_16x16x32_bf16 v[132:135], a[72:75], a[16:19], v[132:135]\n" \
"s_xor_b32 s77, s77, 0x300\n" \
"ds_read_b64_tr_b16 a[120:121], v233 offset:1536\n" \
"v_accvgpr_read_b32 v253, a158\n" \
"v_mfma_f32_16x16x32_bf16 v[132:135], a[76:79], a[20:23], v[132:135]\n" \
"v_accvgpr_read_b32 v254, a159\n" \
"v_mul_f32 v243, s31, v243\n" \
"v_mfma_f32_16x16x32_bf16 v[136:139], a[80:83], a[16:19], v[136:139]\n" \
"ds_read_b64_tr_b16 a[122:123], v233 offset:3584\n" \
"v_mul_f32 v252, s31, v252\n" \
"v_mfma_f32_16x16x32_bf16 v[136:139], a[84:87], a[20:23], v[136:139]\n" \
"ds_read_b64_tr_b16 a[124:125], v233 offset:9728\n" \
"v_mul_f32 v253, s31, v253\n" \
"v_mfma_f32_16x16x32_bf16 v[140:143], a[88:91], a[16:19], v[140:143]\n" \
"s_add_u32 s93, s87, 17858560\n" \
"ds_read_b64_tr_b16 a[126:127], v233 offset:11776\n" \
"v_mul_f32 v254, s31, v254\n" \
"v_mfma_f32_16x16x32_bf16 v[140:143], a[92:95], a[20:23], v[140:143]\n" \
"buffer_store_dwordx4 a[224:227], v234, s[56:59], s93 offen offset:0 sc1\n" \
"v_mfma_f32_16x16x32_bf16 v[172:175], a[88:91], a[24:27], v[172:175]\n" \
"v_cvt_pk_bf16_f32 v30, v243, v252\n" \
"v_cvt_pk_bf16_f32 v31, v253, v254\n" \
"v_mfma_f32_16x16x32_bf16 v[172:175], a[92:95], a[28:31], v[172:175]\n" \
"v_accvgpr_write_b32 a56, v244\n" \
"v_accvgpr_write_b32 a57, v245\n" \
"v_mfma_f32_16x16x32_bf16 v[168:171], a[80:83], a[24:27], v[168:171]\n" \
"v_accvgpr_write_b32 a58, v246\n" \
"v_accvgpr_write_b32 a59, v247\n" \
"v_mfma_f32_16x16x32_bf16 v[168:171], a[84:87], a[28:31], v[168:171]\n" \
"v_accvgpr_write_b32 a60, v248\n" \
"v_accvgpr_write_b32 a61, v249\n" \
"v_mfma_f32_16x16x32_bf16 v[164:167], a[72:75], a[24:27], v[164:167]\n" \
"v_accvgpr_write_b32 a48, 0\n" \
"v_accvgpr_write_b32 a49, 0\n" \
"v_mfma_f32_16x16x32_bf16 v[164:167], a[76:79], a[28:31], v[164:167]\n" \
"v_accvgpr_write_b32 a50, 0\n" \
"v_accvgpr_write_b32 a51, 0\n" \
"v_mfma_f32_16x16x32_bf16 v[160:163], a[64:67], a[24:27], v[160:163]\n" \
"v_accvgpr_write_b32 a52, 0\n" \
"v_accvgpr_write_b32 a53, 0\n" \
"v_mfma_f32_16x16x32_bf16 v[160:163], a[68:71], a[28:31], v[160:163]\n" \
"s_waitcnt vmcnt(35) lgkmcnt(0)\n" \
"s_barrier\n" \
"v_mfma_f32_16x16x32_bf16 v[80:83], a[96:99], a[0:3], v[80:83]\n" \
"ds_read_b64_tr_b16 a[64:65], v232 offset:16384\n" \
"v_mfma_f32_16x16x32_bf16 v[80:83], a[100:103], a[4:7], v[80:83]\n" \
"ds_read_b64_tr_b16 a[66:67], v232 offset:18432\n" \
"v_mfma_f32_16x16x32_bf16 v[84:87], a[104:107], a[0:3], v[84:87]\n" \
"s_add_u32 m0, s79, 0\n" \
"ds_read_b64_tr_b16 a[68:69], v232 offset:24576\n" \
"v_mfma_f32_16x16x32_bf16 v[84:87], a[108:111], a[4:7], v[84:87]\n" \
"buffer_load_dwordx4 v228, s[52:55], s78 offen offset:0 lds\n" \
"v_mfma_f32_16x16x32_bf16 v[88:91], a[112:115], a[0:3], v[88:91]\n" \
"ds_read_b64_tr_b16 a[70:71], v232 offset:26624\n" \
"v_mfma_f32_16x16x32_bf16 v[88:91], a[116:119], a[4:7], v[88:91]\n" \
"ds_read_b64_tr_b16 a[72:73], v233 offset:16384\n" \
"v_mfma_f32_16x16x32_bf16 v[92:95], a[120:123], a[0:3], v[92:95]\n" \
"ds_read_b64_tr_b16 a[74:75], v233 offset:18432\n" \
"v_mfma_f32_16x16x32_bf16 v[92:95], a[124:127], a[4:7], v[92:95]\n" \
"buffer_load_dwordx4 v229, s[52:55], s78 offen offset:1024 lds\n" \
"v_mfma_f32_16x16x32_bf16 v[124:127], a[120:123], a[8:11], v[124:127]\n" \
"s_waitcnt vmcnt(35)\n" \
"ds_read_b128 a[0:3], v250 offset:0\n" \
"v_mfma_f32_16x16x32_bf16 v[124:127], a[124:127], a[12:15], v[124:127]\n" \
"ds_read_b128 a[4:7], v251 offset:0\n" \
"v_mfma_f32_16x16x32_bf16 v[120:123], a[112:115], a[8:11], v[120:123]\n" \
"s_xor_b32 s78, s78, 0x80000\n" \
"ds_read_b64_tr_b16 a[76:77], v233 offset:24576\n" \
"v_mfma_f32_16x16x32_bf16 v[120:123], a[116:119], a[12:15], v[120:123]\n" \
"buffer_load_dwordx4 v228, s[52:55], s89 offen offset:2048 lds\n" \
"v_mfma_f32_16x16x32_bf16 v[116:119], a[104:107], a[8:11], v[116:119]\n" \
"ds_read_b64_tr_b16 a[78:79], v233 offset:26624\n" \
"v_mfma_f32_16x16x32_bf16 v[116:119], a[108:111], a[12:15], v[116:119]\n" \
"ds_read_b64_tr_b16 a[80:81], v232 offset:16896\n" \
"v_mfma_f32_16x16x32_bf16 v[112:115], a[96:99], a[8:11], v[112:115]\n" \
"ds_read_b64_tr_b16 a[82:83], v232 offset:18944\n" \
"v_mfma_f32_16x16x32_bf16 v[112:115], a[100:103], a[12:15], v[112:115]\n" \
"buffer_load_dwordx4 v229, s[52:55], s89 offen offset:3072 lds\n" \
"v_mfma_f32_16x16x32_bf16 v[144:147], a[96:99], a[16:19], v[144:147]\n" \
"s_waitcnt vmcnt(35)\n" \
"s_xor_b32 s89, s89, 0x80000\n" \
"ds_read_b128 a[8:11], v250 offset:2048\n" \
"v_mfma_f32_16x16x32_bf16 v[144:147], a[100:103], a[20:23], v[144:147]\n" \
"s_add_u32 m0, s74, 131072\n" \
"ds_read_b128 a[12:15], v251 offset:2048\n" \
"v_mfma_f32_16x16x32_bf16 v[148:151], a[104:107], a[16:19], v[148:151]\n" \
"s_waitcnt lgkmcnt(6)\n" \
"ds_read_b64_tr_b16 a[84:85], v232 offset:25088\n" \
"v_mfma_f32_16x16x32_bf16 v[148:151], a[108:111], a[20:23], v[148:151]\n" \
"buffer_load_dwordx4 v224, s[48:51], s75 offen offset:0 lds\n" \
"v_mfma_f32_16x16x32_bf16 v[152:155], a[112:115], a[16:19], v[152:155]\n" \
"ds_read_b64_tr_b16 a[86:87], v232 offset:27136\n" \
"v_mfma_f32_16x16x32_bf16 v[152:155], a[116:119], a[20:23], v[152:155]\n" \
"ds_read_b64_tr_b16 a[88:89], v233 offset:16896\n" \
"v_mfma_f32_16x16x32_bf16 v[156:159], a[120:123], a[16:19], v[156:159]\n" \
"ds_read_b64_tr_b16 a[90:91], v233 offset:18944\n" \
"v_mfma_f32_16x16x32_bf16 v[156:159], a[124:127], a[20:23], v[156:159]\n" \
"buffer_load_dwordx4 v225, s[48:51], s75 offen offset:1024 lds\n" \
"v_mfma_f32_16x16x32_bf16 v[188:191], a[120:123], a[24:27], v[188:191]\n" \
"s_waitcnt vmcnt(35)\n" \
"ds_read_b128 a[16:19], v250 offset:4096\n" \
"v_mfma_f32_16x16x32_bf16 v[188:191], a[124:127], a[28:31], v[188:191]\n" \
"ds_read_b128 a[20:23], v251 offset:4096\n" \
"v_mfma_f32_16x16x32_bf16 v[184:187], a[112:115], a[24:27], v[184:187]\n" \
"s_waitcnt lgkmcnt(6)\n" \
"ds_read_b64_tr_b16 a[92:93], v233 offset:25088\n" \
"v_mfma_f32_16x16x32_bf16 v[184:187], a[116:119], a[28:31], v[184:187]\n" \
"buffer_load_dwordx4 v226, s[48:51], s75 offen offset:2048 lds\n" \
"v_mfma_f32_16x16x32_bf16 v[180:183], a[104:107], a[24:27], v[180:183]\n" \
"ds_read_b64_tr_b16 a[94:95], v233 offset:27136\n" \
"v_mfma_f32_16x16x32_bf16 v[180:183], a[108:111], a[28:31], v[180:183]\n" \
"v_mfma_f32_16x16x32_bf16 v[176:179], a[96:99], a[24:27], v[176:179]\n" \
"v_mfma_f32_16x16x32_bf16 v[176:179], a[100:103], a[28:31], v[176:179]\n" \
"buffer_load_dwordx4 v227, s[48:51], s75 offen offset:3072 lds\n" \
"v_mfma_f32_16x16x32_bf16 v[64:67], a[64:67], a[0:3], v[64:67]\n" \
"s_waitcnt vmcnt(35)\n" \
"s_xor_b32 s75, s75, 0x80\n" \
"ds_read_b128 a[24:27], v250 offset:6144\n" \
"v_mfma_f32_16x16x32_bf16 v[64:67], a[68:71], a[4:7], v[64:67]\n" \
"s_add_u32 m0, s74, 135168\n" \
"ds_read_b128 a[28:31], v251 offset:6144\n" \
"v_mfma_f32_16x16x32_bf16 v[68:71], a[72:75], a[0:3], v[68:71]\n" \
"s_waitcnt lgkmcnt(2)\n" \
"ds_read_b64_tr_b16 a[96:97], v232 offset:17408\n" \
"v_mfma_f32_16x16x32_bf16 v[68:71], a[76:79], a[4:7], v[68:71]\n" \
"buffer_load_dwordx4 v224, s[48:51], s77 offen offset:0 lds\n" \
"v_mfma_f32_16x16x32_bf16 v[72:75], a[80:83], a[0:3], v[72:75]\n" \
"ds_read_b64_tr_b16 a[98:99], v232 offset:19456\n" \
"v_mfma_f32_16x16x32_bf16 v[72:75], a[84:87], a[4:7], v[72:75]\n" \
"ds_read_b64_tr_b16 a[100:101], v232 offset:25600\n" \
"v_mfma_f32_16x16x32_bf16 v[76:79], a[88:91], a[0:3], v[76:79]\n" \
"ds_read_b64_tr_b16 a[102:103], v232 offset:27648\n" \
"v_mfma_f32_16x16x32_bf16 v[76:79], a[92:95], a[4:7], v[76:79]\n" \
"buffer_load_dwordx4 v225, s[48:51], s77 offen offset:1024 lds\n" \
"v_mfma_f32_16x16x32_bf16 v[108:111], a[88:91], a[8:11], v[108:111]\n" \
"ds_read_b64_tr_b16 a[104:105], v233 offset:17408\n" \
"v_mfma_f32_16x16x32_bf16 v[108:111], a[92:95], a[12:15], v[108:111]\n" \
"ds_read_b64_tr_b16 a[106:107], v233 offset:19456\n" \
"v_mfma_f32_16x16x32_bf16 v[104:107], a[80:83], a[8:11], v[104:107]\n" \
"s_waitcnt lgkmcnt(6)\n" \
"ds_read_b64_tr_b16 a[108:109], v233 offset:25600\n" \
"v_mfma_f32_16x16x32_bf16 v[104:107], a[84:87], a[12:15], v[104:107]\n" \
"buffer_load_dwordx4 v226, s[48:51], s77 offen offset:2048 lds\n" \
"v_mfma_f32_16x16x32_bf16 v[100:103], a[72:75], a[8:11], v[100:103]\n" \
"ds_read_b64_tr_b16 a[110:111], v233 offset:27648\n" \
"v_mfma_f32_16x16x32_bf16 v[100:103], a[76:79], a[12:15], v[100:103]\n" \
"ds_read_b64_tr_b16 a[112:113], v232 offset:17920\n" \
"v_mfma_f32_16x16x32_bf16 v[96:99], a[64:67], a[8:11], v[96:99]\n" \
"ds_read_b64_tr_b16 a[114:115], v232 offset:19968\n" \
"Lpg3897_%=:\n" \
"s_cbranch_execz Lpg6056_%=\n" \
"v_mfma_f32_16x16x32_bf16 v[96:99], a[68:71], a[12:15], v[96:99]\n" \
"buffer_load_dwordx4 v227, s[48:51], s77 offen offset:3072 lds\n" \
"v_mfma_f32_16x16x32_bf16 v[128:131], a[64:67], a[16:19], v[128:131]\n" \
"ds_read_b64_tr_b16 a[116:117], v232 offset:26112\n" \
"v_mfma_f32_16x16x32_bf16 v[128:131], a[68:71], a[20:23], v[128:131]\n" \
"ds_read_b64_tr_b16 a[118:119], v232 offset:28160\n" \
"v_mfma_f32_16x16x32_bf16 v[132:135], a[72:75], a[16:19], v[132:135]\n" \
"s_xor_b32 s77, s77, 0x80\n" \
"ds_read_b64_tr_b16 a[120:121], v233 offset:17920\n" \
"v_mfma_f32_16x16x32_bf16 v[132:135], a[76:79], a[20:23], v[132:135]\n" \
"buffer_store_dwordx4 a[228:231], v234, s[56:59], s93 offen offset:1024 sc1\n" \
"v_mfma_f32_16x16x32_bf16 v[136:139], a[80:83], a[16:19], v[136:139]\n" \
"ds_read_b64_tr_b16 a[122:123], v233 offset:19968\n" \
"v_mfma_f32_16x16x32_bf16 v[136:139], a[84:87], a[20:23], v[136:139]\n" \
"ds_read_b64_tr_b16 a[124:125], v233 offset:26112\n" \
"v_mfma_f32_16x16x32_bf16 v[140:143], a[88:91], a[16:19], v[140:143]\n" \
"ds_read_b64_tr_b16 a[126:127], v233 offset:28160\n" \
"v_mfma_f32_16x16x32_bf16 v[140:143], a[92:95], a[20:23], v[140:143]\n" \
"v_mfma_f32_16x16x32_bf16 v[172:175], a[88:91], a[24:27], v[172:175]\n" \
"v_mfma_f32_16x16x32_bf16 v[172:175], a[92:95], a[28:31], v[172:175]\n" \
"v_mfma_f32_16x16x32_bf16 v[168:171], a[80:83], a[24:27], v[168:171]\n" \
"v_mfma_f32_16x16x32_bf16 v[168:171], a[84:87], a[28:31], v[168:171]\n" \
"buffer_store_dwordx4 a[232:235], v234, s[56:59], s93 offen offset:2048 sc1\n" \
"v_mfma_f32_16x16x32_bf16 v[164:167], a[72:75], a[24:27], v[164:167]\n" \
"v_mfma_f32_16x16x32_bf16 v[164:167], a[76:79], a[28:31], v[164:167]\n" \
"v_mfma_f32_16x16x32_bf16 v[160:163], a[64:67], a[24:27], v[160:163]\n" \
"v_mfma_f32_16x16x32_bf16 v[160:163], a[68:71], a[28:31], v[160:163]\n" \
"s_waitcnt vmcnt(36) lgkmcnt(0)\n" \
"s_barrier\n" \
"v_mfma_f32_16x16x32_bf16 v[80:83], a[96:99], a[0:3], v[80:83]\n" \
"ds_read_b64_tr_b16 a[64:65], v232 offset:32768\n" \
"v_mfma_f32_16x16x32_bf16 v[80:83], a[100:103], a[4:7], v[80:83]\n" \
"ds_read_b64_tr_b16 a[66:67], v232 offset:34816\n" \
"v_mfma_f32_16x16x32_bf16 v[84:87], a[104:107], a[0:3], v[84:87]\n" \
"s_add_u32 m0, s79, 16384\n" \
"ds_read_b64_tr_b16 a[68:69], v232 offset:40960\n" \
"v_mfma_f32_16x16x32_bf16 v[84:87], a[108:111], a[4:7], v[84:87]\n" \
"buffer_load_dwordx4 v228, s[52:55], s78 offen offset:0 lds\n" \
"v_mfma_f32_16x16x32_bf16 v[88:91], a[112:115], a[0:3], v[88:91]\n" \
"ds_read_b64_tr_b16 a[70:71], v232 offset:43008\n" \
"v_mfma_f32_16x16x32_bf16 v[88:91], a[116:119], a[4:7], v[88:91]\n" \
"ds_read_b64_tr_b16 a[72:73], v233 offset:32768\n" \
"v_mfma_f32_16x16x32_bf16 v[92:95], a[120:123], a[0:3], v[92:95]\n" \
"ds_read_b64_tr_b16 a[74:75], v233 offset:34816\n" \
"v_mfma_f32_16x16x32_bf16 v[92:95], a[124:127], a[4:7], v[92:95]\n" \
"buffer_load_dwordx4 v229, s[52:55], s78 offen offset:1024 lds\n" \
"v_mfma_f32_16x16x32_bf16 v[124:127], a[120:123], a[8:11], v[124:127]\n" \
"s_waitcnt vmcnt(36)\n" \
"ds_read_b128 a[0:3], v230 offset:0\n" \
"v_mfma_f32_16x16x32_bf16 v[124:127], a[124:127], a[12:15], v[124:127]\n" \
"ds_read_b128 a[4:7], v231 offset:0\n" \
"v_mfma_f32_16x16x32_bf16 v[120:123], a[112:115], a[8:11], v[120:123]\n" \
"s_xor_b32 s78, s78, 0x180000\n" \
"ds_read_b64_tr_b16 a[76:77], v233 offset:40960\n" \
"v_mfma_f32_16x16x32_bf16 v[120:123], a[116:119], a[12:15], v[120:123]\n" \
"buffer_load_dwordx4 v228, s[52:55], s89 offen offset:2048 lds\n" \
"v_mfma_f32_16x16x32_bf16 v[116:119], a[104:107], a[8:11], v[116:119]\n" \
"ds_read_b64_tr_b16 a[78:79], v233 offset:43008\n" \
"v_mfma_f32_16x16x32_bf16 v[116:119], a[108:111], a[12:15], v[116:119]\n" \
"ds_read_b64_tr_b16 a[80:81], v232 offset:33280\n" \
"v_mfma_f32_16x16x32_bf16 v[112:115], a[96:99], a[8:11], v[112:115]\n" \
"ds_read_b64_tr_b16 a[82:83], v232 offset:35328\n" \
"v_mfma_f32_16x16x32_bf16 v[112:115], a[100:103], a[12:15], v[112:115]\n" \
"buffer_load_dwordx4 v229, s[52:55], s89 offen offset:3072 lds\n" \
"v_mfma_f32_16x16x32_bf16 v[144:147], a[96:99], a[16:19], v[144:147]\n" \
"s_waitcnt vmcnt(36)\n" \
"s_xor_b32 s89, s89, 0x180000\n" \
"ds_read_b128 a[8:11], v230 offset:2048\n" \
"v_mfma_f32_16x16x32_bf16 v[144:147], a[100:103], a[20:23], v[144:147]\n" \
"s_add_u32 m0, s74, 65536\n" \
"ds_read_b128 a[12:15], v231 offset:2048\n" \
"v_mfma_f32_16x16x32_bf16 v[148:151], a[104:107], a[16:19], v[148:151]\n" \
"s_waitcnt lgkmcnt(6)\n" \
"ds_read_b64_tr_b16 a[84:85], v232 offset:41472\n" \
"v_mfma_f32_16x16x32_bf16 v[148:151], a[108:111], a[20:23], v[148:151]\n" \
"buffer_load_dwordx4 v224, s[48:51], s75 offen offset:0 lds\n" \
"v_mfma_f32_16x16x32_bf16 v[152:155], a[112:115], a[16:19], v[152:155]\n" \
"ds_read_b64_tr_b16 a[86:87], v232 offset:43520\n" \
"v_mfma_f32_16x16x32_bf16 v[152:155], a[116:119], a[20:23], v[152:155]\n" \
"ds_read_b64_tr_b16 a[88:89], v233 offset:33280\n" \
"v_mfma_f32_16x16x32_bf16 v[156:159], a[120:123], a[16:19], v[156:159]\n" \
"ds_read_b64_tr_b16 a[90:91], v233 offset:35328\n" \
"v_mfma_f32_16x16x32_bf16 v[156:159], a[124:127], a[20:23], v[156:159]\n" \
"buffer_load_dwordx4 v225, s[48:51], s75 offen offset:1024 lds\n" \
"v_mfma_f32_16x16x32_bf16 v[188:191], a[120:123], a[24:27], v[188:191]\n" \
"s_waitcnt vmcnt(36)\n" \
"ds_read_b128 a[16:19], v230 offset:4096\n" \
"v_mfma_f32_16x16x32_bf16 v[188:191], a[124:127], a[28:31], v[188:191]\n" \
"ds_read_b128 a[20:23], v231 offset:4096\n" \
"v_mfma_f32_16x16x32_bf16 v[184:187], a[112:115], a[24:27], v[184:187]\n" \
"s_waitcnt lgkmcnt(6)\n" \
"ds_read_b64_tr_b16 a[92:93], v233 offset:41472\n" \
"v_mfma_f32_16x16x32_bf16 v[184:187], a[116:119], a[28:31], v[184:187]\n" \
"buffer_load_dwordx4 v226, s[48:51], s75 offen offset:2048 lds\n" \
"v_mfma_f32_16x16x32_bf16 v[180:183], a[104:107], a[24:27], v[180:183]\n" \
"ds_read_b64_tr_b16 a[94:95], v233 offset:43520\n" \
"v_mfma_f32_16x16x32_bf16 v[180:183], a[108:111], a[28:31], v[180:183]\n" \
"v_mfma_f32_16x16x32_bf16 v[176:179], a[96:99], a[24:27], v[176:179]\n" \
"v_mfma_f32_16x16x32_bf16 v[176:179], a[100:103], a[28:31], v[176:179]\n" \
"buffer_load_dwordx4 v227, s[48:51], s75 offen offset:3072 lds\n" \
"v_mfma_f32_16x16x32_bf16 v[64:67], a[64:67], a[0:3], v[64:67]\n" \
"s_waitcnt vmcnt(36)\n" \
"s_xor_b32 s75, s75, 0x180\n" \
"ds_read_b128 a[24:27], v230 offset:6144\n" \
"v_mfma_f32_16x16x32_bf16 v[64:67], a[68:71], a[4:7], v[64:67]\n" \
"s_add_u32 m0, s74, 69632\n" \
"ds_read_b128 a[28:31], v231 offset:6144\n" \
"v_mfma_f32_16x16x32_bf16 v[68:71], a[72:75], a[0:3], v[68:71]\n" \
"s_waitcnt lgkmcnt(2)\n" \
"ds_read_b64_tr_b16 a[96:97], v232 offset:33792\n" \
"v_mfma_f32_16x16x32_bf16 v[68:71], a[76:79], a[4:7], v[68:71]\n" \
"buffer_load_dwordx4 v224, s[48:51], s77 offen offset:0 lds\n" \
"v_mfma_f32_16x16x32_bf16 v[72:75], a[80:83], a[0:3], v[72:75]\n" \
"ds_read_b64_tr_b16 a[98:99], v232 offset:35840\n" \
"v_mfma_f32_16x16x32_bf16 v[72:75], a[84:87], a[4:7], v[72:75]\n" \
"ds_read_b64_tr_b16 a[100:101], v232 offset:41984\n" \
"v_mfma_f32_16x16x32_bf16 v[76:79], a[88:91], a[0:3], v[76:79]\n" \
"ds_read_b64_tr_b16 a[102:103], v232 offset:44032\n" \
"v_mfma_f32_16x16x32_bf16 v[76:79], a[92:95], a[4:7], v[76:79]\n" \
"buffer_load_dwordx4 v225, s[48:51], s77 offen offset:1024 lds\n" \
"v_mfma_f32_16x16x32_bf16 v[108:111], a[88:91], a[8:11], v[108:111]\n" \
"ds_read_b64_tr_b16 a[104:105], v233 offset:33792\n" \
"v_mfma_f32_16x16x32_bf16 v[108:111], a[92:95], a[12:15], v[108:111]\n" \
"ds_read_b64_tr_b16 a[106:107], v233 offset:35840\n" \
"v_mfma_f32_16x16x32_bf16 v[104:107], a[80:83], a[8:11], v[104:107]\n" \
"s_waitcnt lgkmcnt(6)\n" \
"ds_read_b64_tr_b16 a[108:109], v233 offset:41984\n" \
"v_mfma_f32_16x16x32_bf16 v[104:107], a[84:87], a[12:15], v[104:107]\n" \
"buffer_load_dwordx4 v226, s[48:51], s77 offen offset:2048 lds\n" \
"v_mfma_f32_16x16x32_bf16 v[100:103], a[72:75], a[8:11], v[100:103]\n" \
"ds_read_b64_tr_b16 a[110:111], v233 offset:44032\n" \
"v_mfma_f32_16x16x32_bf16 v[100:103], a[76:79], a[12:15], v[100:103]\n" \
"ds_read_b64_tr_b16 a[112:113], v232 offset:34304\n" \
"v_mfma_f32_16x16x32_bf16 v[96:99], a[64:67], a[8:11], v[96:99]\n" \
"ds_read_b64_tr_b16 a[114:115], v232 offset:36352\n" \
"v_mfma_f32_16x16x32_bf16 v[96:99], a[68:71], a[12:15], v[96:99]\n" \
"buffer_load_dwordx4 v227, s[48:51], s77 offen offset:3072 lds\n" \
"v_mfma_f32_16x16x32_bf16 v[128:131], a[64:67], a[16:19], v[128:131]\n" \
"ds_read_b64_tr_b16 a[116:117], v232 offset:42496\n" \
"v_mfma_f32_16x16x32_bf16 v[128:131], a[68:71], a[20:23], v[128:131]\n" \
"ds_read_b64_tr_b16 a[118:119], v232 offset:44544\n" \
"v_mfma_f32_16x16x32_bf16 v[132:135], a[72:75], a[16:19], v[132:135]\n" \
"s_xor_b32 s77, s77, 0x180\n" \
"ds_read_b64_tr_b16 a[120:121], v233 offset:34304\n" \
"v_mfma_f32_16x16x32_bf16 v[132:135], a[76:79], a[20:23], v[132:135]\n" \
"buffer_store_dwordx4 a[236:239], v234, s[56:59], s93 offen offset:3072 sc1\n" \
"v_mfma_f32_16x16x32_bf16 v[136:139], a[80:83], a[16:19], v[136:139]\n" \
"ds_read_b64_tr_b16 a[122:123], v233 offset:36352\n" \
"v_mfma_f32_16x16x32_bf16 v[136:139], a[84:87], a[20:23], v[136:139]\n" \
"ds_read_b64_tr_b16 a[124:125], v233 offset:42496\n" \
"v_mfma_f32_16x16x32_bf16 v[140:143], a[88:91], a[16:19], v[140:143]\n" \
"ds_read_b64_tr_b16 a[126:127], v233 offset:44544\n" \
"v_mfma_f32_16x16x32_bf16 v[140:143], a[92:95], a[20:23], v[140:143]\n" \
"v_mfma_f32_16x16x32_bf16 v[172:175], a[88:91], a[24:27], v[172:175]\n" \
"v_mfma_f32_16x16x32_bf16 v[172:175], a[92:95], a[28:31], v[172:175]\n" \
"v_mfma_f32_16x16x32_bf16 v[168:171], a[80:83], a[24:27], v[168:171]\n" \
"s_add_u32 s93, s87, 17850368\n" \
"v_mfma_f32_16x16x32_bf16 v[168:171], a[84:87], a[28:31], v[168:171]\n" \
"buffer_store_dwordx4 a[192:195], v234, s[56:59], s93 offen offset:0 sc1\n" \
"v_mfma_f32_16x16x32_bf16 v[164:167], a[72:75], a[24:27], v[164:167]\n" \
"v_mfma_f32_16x16x32_bf16 v[164:167], a[76:79], a[28:31], v[164:167]\n" \
"v_mfma_f32_16x16x32_bf16 v[160:163], a[64:67], a[24:27], v[160:163]\n" \
"v_mfma_f32_16x16x32_bf16 v[160:163], a[68:71], a[28:31], v[160:163]\n" \
"s_waitcnt vmcnt(37) lgkmcnt(0)\n" \
"s_barrier\n" \
"v_mfma_f32_16x16x32_bf16 v[80:83], a[96:99], a[0:3], v[80:83]\n" \
"ds_read_b64_tr_b16 a[64:65], v232 offset:49152\n" \
"v_mfma_f32_16x16x32_bf16 v[80:83], a[100:103], a[4:7], v[80:83]\n" \
"ds_read_b64_tr_b16 a[66:67], v232 offset:51200\n" \
"v_mfma_f32_16x16x32_bf16 v[84:87], a[104:107], a[0:3], v[84:87]\n" \
"s_add_u32 m0, s79, 32768\n" \
"ds_read_b64_tr_b16 a[68:69], v232 offset:57344\n" \
"v_mfma_f32_16x16x32_bf16 v[84:87], a[108:111], a[4:7], v[84:87]\n" \
"buffer_load_dwordx4 v228, s[52:55], s78 offen offset:0 lds\n" \
"v_mfma_f32_16x16x32_bf16 v[88:91], a[112:115], a[0:3], v[88:91]\n" \
"ds_read_b64_tr_b16 a[70:71], v232 offset:59392\n" \
"v_mfma_f32_16x16x32_bf16 v[88:91], a[116:119], a[4:7], v[88:91]\n" \
"ds_read_b64_tr_b16 a[72:73], v233 offset:49152\n" \
"v_mfma_f32_16x16x32_bf16 v[92:95], a[120:123], a[0:3], v[92:95]\n" \
"ds_read_b64_tr_b16 a[74:75], v233 offset:51200\n" \
"v_mfma_f32_16x16x32_bf16 v[92:95], a[124:127], a[4:7], v[92:95]\n" \
"buffer_load_dwordx4 v229, s[52:55], s78 offen offset:1024 lds\n" \
"v_mfma_f32_16x16x32_bf16 v[124:127], a[120:123], a[8:11], v[124:127]\n" \
"s_waitcnt vmcnt(37)\n" \
"ds_read_b128 a[0:3], v230 offset:32768\n" \
"v_mfma_f32_16x16x32_bf16 v[124:127], a[124:127], a[12:15], v[124:127]\n" \
"ds_read_b128 a[4:7], v231 offset:32768\n" \
"v_mfma_f32_16x16x32_bf16 v[120:123], a[112:115], a[8:11], v[120:123]\n" \
"s_xor_b32 s78, s78, 0x80000\n" \
"ds_read_b64_tr_b16 a[76:77], v233 offset:57344\n" \
"v_mfma_f32_16x16x32_bf16 v[120:123], a[116:119], a[12:15], v[120:123]\n" \
"buffer_load_dwordx4 v228, s[52:55], s89 offen offset:2048 lds\n" \
"v_mfma_f32_16x16x32_bf16 v[116:119], a[104:107], a[8:11], v[116:119]\n" \
"ds_read_b64_tr_b16 a[78:79], v233 offset:59392\n" \
"v_mfma_f32_16x16x32_bf16 v[116:119], a[108:111], a[12:15], v[116:119]\n" \
"ds_read_b64_tr_b16 a[80:81], v232 offset:49664\n" \
"v_mfma_f32_16x16x32_bf16 v[112:115], a[96:99], a[8:11], v[112:115]\n" \
"ds_read_b64_tr_b16 a[82:83], v232 offset:51712\n" \
"v_mfma_f32_16x16x32_bf16 v[112:115], a[100:103], a[12:15], v[112:115]\n" \
"buffer_load_dwordx4 v229, s[52:55], s89 offen offset:3072 lds\n" \
"v_mfma_f32_16x16x32_bf16 v[144:147], a[96:99], a[16:19], v[144:147]\n" \
"s_waitcnt vmcnt(37)\n" \
"s_xor_b32 s89, s89, 0x80000\n" \
"ds_read_b128 a[8:11], v230 offset:34816\n" \
"v_mfma_f32_16x16x32_bf16 v[144:147], a[100:103], a[20:23], v[144:147]\n" \
"s_add_u32 m0, s74, 98304\n" \
"ds_read_b128 a[12:15], v231 offset:34816\n" \
"v_mfma_f32_16x16x32_bf16 v[148:151], a[104:107], a[16:19], v[148:151]\n" \
"s_waitcnt lgkmcnt(6)\n" \
"ds_read_b64_tr_b16 a[84:85], v232 offset:57856\n" \
"v_mfma_f32_16x16x32_bf16 v[148:151], a[108:111], a[20:23], v[148:151]\n" \
"buffer_load_dwordx4 v224, s[48:51], s75 offen offset:0 lds\n" \
"v_mfma_f32_16x16x32_bf16 v[152:155], a[112:115], a[16:19], v[152:155]\n" \
"ds_read_b64_tr_b16 a[86:87], v232 offset:59904\n" \
"v_mfma_f32_16x16x32_bf16 v[152:155], a[116:119], a[20:23], v[152:155]\n" \
"ds_read_b64_tr_b16 a[88:89], v233 offset:49664\n" \
"v_mfma_f32_16x16x32_bf16 v[156:159], a[120:123], a[16:19], v[156:159]\n" \
"ds_read_b64_tr_b16 a[90:91], v233 offset:51712\n" \
"v_mfma_f32_16x16x32_bf16 v[156:159], a[124:127], a[20:23], v[156:159]\n" \
"buffer_load_dwordx4 v225, s[48:51], s75 offen offset:1024 lds\n" \
"v_mfma_f32_16x16x32_bf16 v[188:191], a[120:123], a[24:27], v[188:191]\n" \
"s_waitcnt vmcnt(37)\n" \
"ds_read_b128 a[16:19], v230 offset:36864\n" \
"v_mfma_f32_16x16x32_bf16 v[188:191], a[124:127], a[28:31], v[188:191]\n" \
"ds_read_b128 a[20:23], v231 offset:36864\n" \
"v_mfma_f32_16x16x32_bf16 v[184:187], a[112:115], a[24:27], v[184:187]\n" \
"s_waitcnt lgkmcnt(6)\n" \
"ds_read_b64_tr_b16 a[92:93], v233 offset:57856\n" \
"v_mfma_f32_16x16x32_bf16 v[184:187], a[116:119], a[28:31], v[184:187]\n" \
"buffer_load_dwordx4 v226, s[48:51], s75 offen offset:2048 lds\n" \
"v_mfma_f32_16x16x32_bf16 v[180:183], a[104:107], a[24:27], v[180:183]\n" \
"ds_read_b64_tr_b16 a[94:95], v233 offset:59904\n" \
"v_mfma_f32_16x16x32_bf16 v[180:183], a[108:111], a[28:31], v[180:183]\n" \
"v_mfma_f32_16x16x32_bf16 v[176:179], a[96:99], a[24:27], v[176:179]\n" \
"v_mfma_f32_16x16x32_bf16 v[176:179], a[100:103], a[28:31], v[176:179]\n" \
"buffer_load_dwordx4 v227, s[48:51], s75 offen offset:3072 lds\n" \
"v_mfma_f32_16x16x32_bf16 v[64:67], a[64:67], a[0:3], v[64:67]\n" \
"s_waitcnt vmcnt(37)\n" \
"s_xor_b32 s75, s75, 0x80\n" \
"ds_read_b128 a[24:27], v230 offset:38912\n" \
"v_mfma_f32_16x16x32_bf16 v[64:67], a[68:71], a[4:7], v[64:67]\n" \
"s_add_u32 m0, s74, 102400\n" \
"ds_read_b128 a[28:31], v231 offset:38912\n" \
"v_mfma_f32_16x16x32_bf16 v[68:71], a[72:75], a[0:3], v[68:71]\n" \
"s_waitcnt lgkmcnt(2)\n" \
"ds_read_b64_tr_b16 a[96:97], v232 offset:50176\n" \
"v_mfma_f32_16x16x32_bf16 v[68:71], a[76:79], a[4:7], v[68:71]\n" \
"buffer_load_dwordx4 v224, s[48:51], s77 offen offset:0 lds\n" \
"v_mfma_f32_16x16x32_bf16 v[72:75], a[80:83], a[0:3], v[72:75]\n" \
"ds_read_b64_tr_b16 a[98:99], v232 offset:52224\n" \
"v_mfma_f32_16x16x32_bf16 v[72:75], a[84:87], a[4:7], v[72:75]\n" \
"ds_read_b64_tr_b16 a[100:101], v232 offset:58368\n" \
"v_mfma_f32_16x16x32_bf16 v[76:79], a[88:91], a[0:3], v[76:79]\n" \
"ds_read_b64_tr_b16 a[102:103], v232 offset:60416\n" \
"v_mfma_f32_16x16x32_bf16 v[76:79], a[92:95], a[4:7], v[76:79]\n" \
"buffer_load_dwordx4 v225, s[48:51], s77 offen offset:1024 lds\n" \
"v_mfma_f32_16x16x32_bf16 v[108:111], a[88:91], a[8:11], v[108:111]\n" \
"ds_read_b64_tr_b16 a[104:105], v233 offset:50176\n" \
"v_mfma_f32_16x16x32_bf16 v[108:111], a[92:95], a[12:15], v[108:111]\n" \
"ds_read_b64_tr_b16 a[106:107], v233 offset:52224\n" \
"v_mfma_f32_16x16x32_bf16 v[104:107], a[80:83], a[8:11], v[104:107]\n" \
"s_waitcnt lgkmcnt(6)\n" \
"ds_read_b64_tr_b16 a[108:109], v233 offset:58368\n" \
"v_mfma_f32_16x16x32_bf16 v[104:107], a[84:87], a[12:15], v[104:107]\n" \
"buffer_load_dwordx4 v226, s[48:51], s77 offen offset:2048 lds\n" \
"v_mfma_f32_16x16x32_bf16 v[100:103], a[72:75], a[8:11], v[100:103]\n" \
"ds_read_b64_tr_b16 a[110:111], v233 offset:60416\n" \
"v_mfma_f32_16x16x32_bf16 v[100:103], a[76:79], a[12:15], v[100:103]\n" \
"ds_read_b64_tr_b16 a[112:113], v232 offset:50688\n" \
"v_mfma_f32_16x16x32_bf16 v[96:99], a[64:67], a[8:11], v[96:99]\n" \
"ds_read_b64_tr_b16 a[114:115], v232 offset:52736\n" \
"v_mfma_f32_16x16x32_bf16 v[96:99], a[68:71], a[12:15], v[96:99]\n" \
"buffer_load_dwordx4 v227, s[48:51], s77 offen offset:3072 lds\n" \
"v_mfma_f32_16x16x32_bf16 v[128:131], a[64:67], a[16:19], v[128:131]\n" \
"ds_read_b64_tr_b16 a[116:117], v232 offset:58880\n" \
"v_mfma_f32_16x16x32_bf16 v[128:131], a[68:71], a[20:23], v[128:131]\n" \
"ds_read_b64_tr_b16 a[118:119], v232 offset:60928\n" \
"v_mfma_f32_16x16x32_bf16 v[132:135], a[72:75], a[16:19], v[132:135]\n" \
"s_xor_b32 s77, s77, 0x80\n" \
"ds_read_b64_tr_b16 a[120:121], v233 offset:50688\n" \
"v_mfma_f32_16x16x32_bf16 v[132:135], a[76:79], a[20:23], v[132:135]\n" \
"buffer_store_dwordx4 a[196:199], v234, s[56:59], s93 offen offset:1024 sc1\n" \
"v_mfma_f32_16x16x32_bf16 v[136:139], a[80:83], a[16:19], v[136:139]\n" \
"ds_read_b64_tr_b16 a[122:123], v233 offset:52736\n" \
"v_mfma_f32_16x16x32_bf16 v[136:139], a[84:87], a[20:23], v[136:139]\n" \
"ds_read_b64_tr_b16 a[124:125], v233 offset:58880\n" \
"v_mfma_f32_16x16x32_bf16 v[140:143], a[88:91], a[16:19], v[140:143]\n" \
"ds_read_b64_tr_b16 a[126:127], v233 offset:60928\n" \
"v_mfma_f32_16x16x32_bf16 v[140:143], a[92:95], a[20:23], v[140:143]\n" \
"buffer_store_dwordx4 a[200:203], v234, s[56:59], s93 offen offset:2048 sc1\n" \
"v_mfma_f32_16x16x32_bf16 v[172:175], a[88:91], a[24:27], v[172:175]\n" \
"v_mfma_f32_16x16x32_bf16 v[172:175], a[92:95], a[28:31], v[172:175]\n" \
"v_mfma_f32_16x16x32_bf16 v[168:171], a[80:83], a[24:27], v[168:171]\n" \
"v_mfma_f32_16x16x32_bf16 v[168:171], a[84:87], a[28:31], v[168:171]\n" \
"buffer_store_dwordx4 a[204:207], v234, s[56:59], s93 offen offset:3072 sc1\n" \
"v_mfma_f32_16x16x32_bf16 v[164:167], a[72:75], a[24:27], v[164:167]\n" \
"v_mfma_f32_16x16x32_bf16 v[164:167], a[76:79], a[28:31], v[164:167]\n" \
"v_mfma_f32_16x16x32_bf16 v[160:163], a[64:67], a[24:27], v[160:163]\n" \
"v_mfma_f32_16x16x32_bf16 v[160:163], a[68:71], a[28:31], v[160:163]\n" \
"s_waitcnt vmcnt(39) lgkmcnt(0)\n" \
"s_barrier\n" \
"v_mfma_f32_16x16x32_bf16 v[80:83], a[96:99], a[0:3], v[80:83]\n" \
"ds_read_b64_tr_b16 a[64:65], v232 offset:0\n" \
"v_mfma_f32_16x16x32_bf16 v[80:83], a[100:103], a[4:7], v[80:83]\n" \
"ds_read_b64_tr_b16 a[66:67], v232 offset:2048\n" \
"v_mfma_f32_16x16x32_bf16 v[84:87], a[104:107], a[0:3], v[84:87]\n" \
"s_add_u32 m0, s79, 49152\n" \
"ds_read_b64_tr_b16 a[68:69], v232 offset:8192\n" \
"v_mfma_f32_16x16x32_bf16 v[84:87], a[108:111], a[4:7], v[84:87]\n" \
"buffer_load_dwordx4 v228, s[52:55], s78 offen offset:0 lds\n" \
"v_mfma_f32_16x16x32_bf16 v[88:91], a[112:115], a[0:3], v[88:91]\n" \
"s_mul_i32 s73, s62, 557824\n" \
"s_add_u32 s85, s73, 17850368\n" \
"ds_read_b64_tr_b16 a[70:71], v232 offset:10240\n" \
"v_mfma_f32_16x16x32_bf16 v[88:91], a[116:119], a[4:7], v[88:91]\n" \
"s_mul_i32 s83, s63, 34816\n" \
"s_sub_u32 s86, 557056, s83\n" \
"ds_read_b64_tr_b16 a[72:73], v233 offset:0\n" \
"v_mfma_f32_16x16x32_bf16 v[92:95], a[120:123], a[0:3], v[92:95]\n" \
"ds_read_b64_tr_b16 a[74:75], v233 offset:2048\n" \
"v_add_u32 v237, %3, v236\n" \
"v_mfma_f32_16x16x32_bf16 v[92:95], a[124:127], a[4:7], v[92:95]\n" \
"buffer_load_dwordx4 v229, s[52:55], s78 offen offset:1024 lds\n" \
"v_mfma_f32_16x16x32_bf16 v[124:127], a[120:123], a[8:11], v[124:127]\n" \
"s_waitcnt vmcnt(39)\n" \
"ds_read_b128 a[0:3], v250 offset:0\n" \
"v_mfma_f32_16x16x32_bf16 v[124:127], a[124:127], a[12:15], v[124:127]\n" \
"s_add_u32 s36, s56, s73\n" \
"s_addc_u32 s37, s57, 0\n" \
"ds_read_b128 a[4:7], v251 offset:0\n" \
"v_mfma_f32_16x16x32_bf16 v[120:123], a[112:115], a[8:11], v[120:123]\n" \
"s_mov_b32 s38, s86\n" \
"s_mov_b32 s39, 0x20000\n" \
"ds_read_b64_tr_b16 a[76:77], v233 offset:8192\n" \
"v_mfma_f32_16x16x32_bf16 v[120:123], a[116:119], a[12:15], v[120:123]\n" \
"buffer_load_dwordx4 v228, s[52:55], s89 offen offset:2048 lds\n" \
"v_mfma_f32_16x16x32_bf16 v[116:119], a[104:107], a[8:11], v[116:119]\n" \
"ds_read_b64_tr_b16 a[78:79], v233 offset:10240\n" \
"v_add_u32 v235, %3, v234\n" \
"v_mfma_f32_16x16x32_bf16 v[116:119], a[108:111], a[12:15], v[116:119]\n" \
"s_add_u32 s40, s56, s85\n" \
"s_addc_u32 s41, s57, 0\n" \
"ds_read_b64_tr_b16 a[80:81], v232 offset:512\n" \
"v_mfma_f32_16x16x32_bf16 v[112:115], a[96:99], a[8:11], v[112:115]\n" \
"s_mov_b32 s42, s86\n" \
"s_mov_b32 s43, 0x20000\n" \
"ds_read_b64_tr_b16 a[82:83], v232 offset:2560\n" \
"v_mfma_f32_16x16x32_bf16 v[112:115], a[100:103], a[12:15], v[112:115]\n" \
"buffer_load_dwordx4 v229, s[52:55], s89 offen offset:3072 lds\n" \
"v_mfma_f32_16x16x32_bf16 v[144:147], a[96:99], a[16:19], v[144:147]\n" \
"s_waitcnt vmcnt(39)\n" \
"s_add_u32 s85, s74, 131072\n" \
"ds_read_b128 a[8:11], v250 offset:2048\n" \
"v_mfma_f32_16x16x32_bf16 v[144:147], a[100:103], a[20:23], v[144:147]\n" \
"s_add_u32 m0, s74, 131072\n" \
"ds_read_b128 a[12:15], v251 offset:2048\n" \
"v_add_u32 v239, s85, v234\n" \
"v_mfma_f32_16x16x32_bf16 v[148:151], a[104:107], a[16:19], v[148:151]\n" \
"s_waitcnt lgkmcnt(6)\n" \
"s_lshl_b32 s73, s61, 12\n" \
"ds_read_b64_tr_b16 a[84:85], v232 offset:8704\n" \
"v_mfma_f32_16x16x32_bf16 v[148:151], a[108:111], a[20:23], v[148:151]\n" \
"buffer_load_dwordx4 v224, s[48:51], s75 offen offset:0 lds\n" \
"v_mfma_f32_16x16x32_bf16 v[152:155], a[112:115], a[16:19], v[152:155]\n" \
"s_add_u32 s80, s79, 0x10000\n" \
"s_add_u32 s83, s83, s73\n" \
"ds_read_b64_tr_b16 a[86:87], v232 offset:10752\n" \
"v_mfma_f32_16x16x32_bf16 v[152:155], a[116:119], a[20:23], v[152:155]\n" \
"s_cmp_eq_u32 s63, 0\n" \
"s_cselect_b32 s85, 34816, 0\n" \
"ds_read_b64_tr_b16 a[88:89], v233 offset:512\n" \
"v_mfma_f32_16x16x32_bf16 v[156:159], a[120:123], a[16:19], v[156:159]\n" \
"s_add_u32 s73, s73, s85\n" \
"ds_read_b64_tr_b16 a[90:91], v233 offset:2560\n" \
"v_mfma_f32_16x16x32_bf16 v[156:159], a[124:127], a[20:23], v[156:159]\n" \
"buffer_load_dwordx4 v225, s[48:51], s75 offen offset:1024 lds\n" \
"v_mfma_f32_16x16x32_bf16 v[188:191], a[120:123], a[24:27], v[188:191]\n" \
"s_waitcnt vmcnt(39)\n" \
"ds_read_b128 a[16:19], v250 offset:4096\n" \
"v_mfma_f32_16x16x32_bf16 v[188:191], a[124:127], a[28:31], v[188:191]\n" \
"ds_read_b128 a[20:23], v251 offset:4096\n" \
"v_mfma_f32_16x16x32_bf16 v[184:187], a[112:115], a[24:27], v[184:187]\n" \
"s_waitcnt lgkmcnt(6)\n" \
"ds_read_b64_tr_b16 a[92:93], v233 offset:8704\n" \
"v_mfma_f32_16x16x32_bf16 v[184:187], a[116:119], a[28:31], v[184:187]\n" \
"buffer_load_dwordx4 v226, s[48:51], s75 offen offset:2048 lds\n" \
"v_mfma_f32_16x16x32_bf16 v[180:183], a[104:107], a[24:27], v[180:183]\n" \
"ds_read_b64_tr_b16 a[94:95], v233 offset:10752\n" \
"v_mfma_f32_16x16x32_bf16 v[180:183], a[108:111], a[28:31], v[180:183]\n" \
"v_mfma_f32_16x16x32_bf16 v[176:179], a[96:99], a[24:27], v[176:179]\n" \
"v_mfma_f32_16x16x32_bf16 v[176:179], a[100:103], a[28:31], v[176:179]\n" \
"buffer_load_dwordx4 v227, s[48:51], s75 offen offset:3072 lds\n" \
"v_mfma_f32_16x16x32_bf16 v[64:67], a[64:67], a[0:3], v[64:67]\n" \
"s_waitcnt vmcnt(39)\n" \
"ds_read_b128 a[24:27], v250 offset:6144\n" \
"v_mfma_f32_16x16x32_bf16 v[64:67], a[68:71], a[4:7], v[64:67]\n" \
"s_add_u32 m0, s74, 135168\n" \
"ds_read_b128 a[28:31], v251 offset:6144\n" \
"v_mfma_f32_16x16x32_bf16 v[68:71], a[72:75], a[0:3], v[68:71]\n" \
"s_waitcnt lgkmcnt(2)\n" \
"ds_read_b64_tr_b16 a[96:97], v232 offset:1024\n" \
"v_mfma_f32_16x16x32_bf16 v[68:71], a[76:79], a[4:7], v[68:71]\n" \
"buffer_load_dwordx4 v224, s[48:51], s77 offen offset:0 lds\n" \
"v_mfma_f32_16x16x32_bf16 v[72:75], a[80:83], a[0:3], v[72:75]\n" \
"ds_read_b64_tr_b16 a[98:99], v232 offset:3072\n" \
"v_mfma_f32_16x16x32_bf16 v[72:75], a[84:87], a[4:7], v[72:75]\n" \
"ds_read_b64_tr_b16 a[100:101], v232 offset:9216\n" \
"v_mfma_f32_16x16x32_bf16 v[76:79], a[88:91], a[0:3], v[76:79]\n" \
"ds_read_b64_tr_b16 a[102:103], v232 offset:11264\n" \
"v_mfma_f32_16x16x32_bf16 v[76:79], a[92:95], a[4:7], v[76:79]\n" \
"buffer_load_dwordx4 v225, s[48:51], s77 offen offset:1024 lds\n" \
"v_mfma_f32_16x16x32_bf16 v[108:111], a[88:91], a[8:11], v[108:111]\n" \
"ds_read_b64_tr_b16 a[104:105], v233 offset:1024\n" \
"v_mfma_f32_16x16x32_bf16 v[108:111], a[92:95], a[12:15], v[108:111]\n" \
"ds_read_b64_tr_b16 a[106:107], v233 offset:3072\n" \
"v_mfma_f32_16x16x32_bf16 v[104:107], a[80:83], a[8:11], v[104:107]\n" \
"s_waitcnt lgkmcnt(6)\n" \
"ds_read_b64_tr_b16 a[108:109], v233 offset:9216\n" \
"v_mfma_f32_16x16x32_bf16 v[104:107], a[84:87], a[12:15], v[104:107]\n" \
"buffer_load_dwordx4 v226, s[48:51], s77 offen offset:2048 lds\n" \
"v_mfma_f32_16x16x32_bf16 v[100:103], a[72:75], a[8:11], v[100:103]\n" \
"ds_read_b64_tr_b16 a[110:111], v233 offset:11264\n" \
"v_mfma_f32_16x16x32_bf16 v[100:103], a[76:79], a[12:15], v[100:103]\n" \
"ds_read_b64_tr_b16 a[112:113], v232 offset:1536\n" \
"v_mfma_f32_16x16x32_bf16 v[96:99], a[64:67], a[8:11], v[96:99]\n" \
"ds_read_b64_tr_b16 a[114:115], v232 offset:3584\n" \
"v_mfma_f32_16x16x32_bf16 v[96:99], a[68:71], a[12:15], v[96:99]\n" \
"buffer_load_dwordx4 v227, s[48:51], s77 offen offset:3072 lds\n" \
"v_mfma_f32_16x16x32_bf16 v[128:131], a[64:67], a[16:19], v[128:131]\n" \
"ds_read_b64_tr_b16 a[116:117], v232 offset:9728\n" \
"v_mfma_f32_16x16x32_bf16 v[128:131], a[68:71], a[20:23], v[128:131]\n" \
"ds_read_b64_tr_b16 a[118:119], v232 offset:11776\n" \
"v_mfma_f32_16x16x32_bf16 v[132:135], a[72:75], a[16:19], v[132:135]\n" \
"s_add_u32 s93, s94, 8192\n" \
"ds_read_b64_tr_b16 a[120:121], v233 offset:1536\n" \
"v_mfma_f32_16x16x32_bf16 v[132:135], a[76:79], a[20:23], v[132:135]\n" \
"buffer_store_dwordx4 v[32:35], v236, s[56:59], s93 offen offset:0 sc1\n" \
"v_mfma_f32_16x16x32_bf16 v[136:139], a[80:83], a[16:19], v[136:139]\n" \
"ds_read_b64_tr_b16 a[122:123], v233 offset:3584\n" \
"v_mfma_f32_16x16x32_bf16 v[136:139], a[84:87], a[20:23], v[136:139]\n" \
"ds_read_b64_tr_b16 a[124:125], v233 offset:9728\n" \
"v_mfma_f32_16x16x32_bf16 v[140:143], a[88:91], a[16:19], v[140:143]\n" \
"ds_read_b64_tr_b16 a[126:127], v233 offset:11776\n" \
"v_mfma_f32_16x16x32_bf16 v[140:143], a[92:95], a[20:23], v[140:143]\n" \
"v_mfma_f32_16x16x32_bf16 v[172:175], a[88:91], a[24:27], v[172:175]\n" \
"v_mfma_f32_16x16x32_bf16 v[172:175], a[92:95], a[28:31], v[172:175]\n" \
"v_mfma_f32_16x16x32_bf16 v[168:171], a[80:83], a[24:27], v[168:171]\n" \
"v_mfma_f32_16x16x32_bf16 v[168:171], a[84:87], a[28:31], v[168:171]\n" \
"v_mfma_f32_16x16x32_bf16 v[164:167], a[72:75], a[24:27], v[164:167]\n" \
"v_mfma_f32_16x16x32_bf16 v[164:167], a[76:79], a[28:31], v[164:167]\n" \
"v_mfma_f32_16x16x32_bf16 v[160:163], a[64:67], a[24:27], v[160:163]\n" \
"v_mfma_f32_16x16x32_bf16 v[160:163], a[68:71], a[28:31], v[160:163]\n" \
"s_waitcnt vmcnt(38) lgkmcnt(0)\n" \
"s_barrier\n" \
"v_mfma_f32_16x16x32_bf16 v[80:83], a[96:99], a[0:3], v[80:83]\n" \
"s_lshr_b32 s71, s61, 1\n" \
"s_lshl_b32 s71, s71, 14\n" \
"ds_read_b64_tr_b16 a[64:65], v232 offset:16384\n" \
"v_mfma_f32_16x16x32_bf16 v[80:83], a[100:103], a[4:7], v[80:83]\n" \
"s_xor_b32 s71, s71, 0x4000\n" \
"s_and_b32 s70, s61, 1\n" \
"ds_read_b64_tr_b16 a[66:67], v232 offset:18432\n" \
"v_mfma_f32_16x16x32_bf16 v[84:87], a[104:107], a[0:3], v[84:87]\n" \
"s_lshl_b32 s70, s70, 12\n" \
"s_add_u32 s71, s71, s70\n" \
"ds_read_b64_tr_b16 a[68:69], v232 offset:24576\n" \
"v_mfma_f32_16x16x32_bf16 v[84:87], a[108:111], a[4:7], v[84:87]\n" \
"buffer_store_dwordx4 v[48:51], v236, s[56:59], s93 offen offset:256 sc1\n" \
"v_mfma_f32_16x16x32_bf16 v[88:91], a[112:115], a[0:3], v[88:91]\n" \
"ds_read_b64_tr_b16 a[70:71], v232 offset:26624\n" \
"v_add_u32 v224, s71, v237\n" \
"v_mfma_f32_16x16x32_bf16 v[88:91], a[116:119], a[4:7], v[88:91]\n" \
"s_and_b32 s70, s63, 1\n" \
"s_lshl_b32 s70, s70, 15\n" \
"ds_read_b64_tr_b16 a[72:73], v233 offset:16384\n" \
"v_mfma_f32_16x16x32_bf16 v[92:95], a[120:123], a[0:3], v[92:95]\n" \
"s_xor_b32 s70, s70, 0x18000\n" \
"s_add_u32 s71, s71, s70\n" \
"ds_read_b64_tr_b16 a[74:75], v233 offset:18432\n" \
"v_mfma_f32_16x16x32_bf16 v[92:95], a[124:127], a[4:7], v[92:95]\n" \
"v_add_u32 v225, s71, v235\n" \
"s_lshr_b32 s64, s22, 7\n" \
"v_mfma_f32_16x16x32_bf16 v[124:127], a[120:123], a[8:11], v[124:127]\n" \
"s_waitcnt vmcnt(37)\n" \
"s_sub_u32 s64, 3, s64\n" \
"ds_read_b128 a[0:3], v230 offset:0\n" \
"v_mfma_f32_16x16x32_bf16 v[124:127], a[124:127], a[12:15], v[124:127]\n" \
"s_and_b32 s64, s64, 7\n" \
"s_and_b32 s65, %1, 1\n" \
"ds_read_b128 a[4:7], v231 offset:0\n" \
"v_mfma_f32_16x16x32_bf16 v[120:123], a[112:115], a[8:11], v[120:123]\n" \
"s_lshl_b32 s65, s65, 3\n" \
"s_add_u32 s64, s64, s65\n" \
"ds_read_b64_tr_b16 a[76:77], v233 offset:24576\n" \
"v_mfma_f32_16x16x32_bf16 v[120:123], a[116:119], a[12:15], v[120:123]\n" \
"s_lshl_b32 s64, s64, 7\n" \
"s_lshr_b32 s65, %1, 3\n" \
"s_lshl_b32 s65, s65, 2\n" \
"v_mfma_f32_16x16x32_bf16 v[116:119], a[104:107], a[8:11], v[116:119]\n" \
"s_add_u32 s65, s65, s61\n" \
"s_mul_i32 s65, s65, 24576\n" \
"ds_read_b64_tr_b16 a[78:79], v233 offset:26624\n" \
"v_mfma_f32_16x16x32_bf16 v[116:119], a[108:111], a[12:15], v[116:119]\n" \
"s_add_u32 s64, s64, s65\n" \
"ds_read_b64_tr_b16 a[80:81], v232 offset:16896\n" \
"v_bfe_u32 v254, v234, 7, 2\n" \
"v_mfma_f32_16x16x32_bf16 v[112:115], a[96:99], a[8:11], v[112:115]\n" \
"ds_read_b64_tr_b16 a[82:83], v232 offset:18944\n" \
"v_mul_u32_u24 v254, 3, v254\n" \
"v_mfma_f32_16x16x32_bf16 v[112:115], a[100:103], a[12:15], v[112:115]\n" \
"buffer_store_dwordx4 v[40:43], v236, s[56:59], s93 offen offset:2048 sc1\n" \
"v_mfma_f32_16x16x32_bf16 v[144:147], a[96:99], a[16:19], v[144:147]\n" \
"s_waitcnt vmcnt(36)\n" \
"ds_read_b128 a[8:11], v230 offset:2048\n" \
"v_bfe_u32 v243, v234, 5, 2\n" \
"v_mfma_f32_16x16x32_bf16 v[144:147], a[100:103], a[20:23], v[144:147]\n" \
"ds_read_b128 a[12:15], v231 offset:2048\n" \
"v_min_u32 v243, 2, v243\n" \
"v_mfma_f32_16x16x32_bf16 v[148:151], a[104:107], a[16:19], v[148:151]\n" \
"ds_read_b64_tr_b16 a[84:85], v232 offset:25088\n" \
"v_add_u32 v254, v254, v243\n" \
"v_mfma_f32_16x16x32_bf16 v[148:151], a[108:111], a[20:23], v[148:151]\n" \
"v_and_b32 v243, 16, v234\n" \
"v_lshl_add_u32 v243, v243, 2, s64\n" \
"v_mfma_f32_16x16x32_bf16 v[152:155], a[112:115], a[16:19], v[152:155]\n" \
"ds_read_b64_tr_b16 a[86:87], v232 offset:27136\n" \
"v_lshl_add_u32 v254, v254, 11, v243\n" \
"v_mfma_f32_16x16x32_bf16 v[152:155], a[116:119], a[20:23], v[152:155]\n" \
"ds_read_b64_tr_b16 a[88:89], v233 offset:16896\n" \
"v_mfma_f32_16x16x32_bf16 v[156:159], a[120:123], a[16:19], v[156:159]\n" \
"ds_read_b64_tr_b16 a[90:91], v233 offset:18944\n" \
"v_mfma_f32_16x16x32_bf16 v[156:159], a[124:127], a[20:23], v[156:159]\n" \
"v_mfma_f32_16x16x32_bf16 v[188:191], a[120:123], a[24:27], v[188:191]\n" \
"s_waitcnt vmcnt(34)\n" \
"ds_read_b128 a[16:19], v230 offset:4096\n" \
"v_mfma_f32_16x16x32_bf16 v[188:191], a[124:127], a[28:31], v[188:191]\n" \
"ds_read_b128 a[20:23], v231 offset:4096\n" \
"v_mfma_f32_16x16x32_bf16 v[184:187], a[112:115], a[24:27], v[184:187]\n" \
"ds_read_b64_tr_b16 a[92:93], v233 offset:25088\n" \
"v_mfma_f32_16x16x32_bf16 v[184:187], a[116:119], a[28:31], v[184:187]\n" \
"v_mfma_f32_16x16x32_bf16 v[180:183], a[104:107], a[24:27], v[180:183]\n" \
"ds_read_b64_tr_b16 a[94:95], v233 offset:27136\n" \
"v_mfma_f32_16x16x32_bf16 v[180:183], a[108:111], a[28:31], v[180:183]\n" \
"v_mfma_f32_16x16x32_bf16 v[176:179], a[96:99], a[24:27], v[176:179]\n" \
"buffer_store_dwordx4 v[56:59], v236, s[56:59], s93 offen offset:2304 sc1\n" \
"v_mfma_f32_16x16x32_bf16 v[176:179], a[100:103], a[28:31], v[176:179]\n" \
"s_waitcnt lgkmcnt(1)\n" \
"v_mfma_f32_16x16x32_bf16 v[64:67], a[64:67], a[0:3], v[64:67]\n" \
"Lpg4446_%=:\n" \
"s_cbranch_execz Lpg6190_%=\n" \
"s_waitcnt vmcnt(33)\n" \
"s_lshl_b32 s68, s63, 2\n" \
"ds_read_b128 a[24:27], v230 offset:6144\n" \
"v_mfma_f32_16x16x32_bf16 v[64:67], a[68:71], a[4:7], v[64:67]\n" \
"s_add_u32 s68, s68, s90\n" \
"ds_read_b128 a[28:31], v231 offset:6144\n" \
"v_mfma_f32_16x16x32_bf16 v[68:71], a[72:75], a[0:3], v[68:71]\n" \
"ds_read_b64_tr_b16 a[96:97], v232 offset:17408\n" \
"v_mfma_f32_16x16x32_bf16 v[68:71], a[76:79], a[4:7], v[68:71]\n" \
"v_mov_b32 v228, s60\n" \
"v_lshlrev_b32 v229, 28, s61\n" \
"v_mfma_f32_16x16x32_bf16 v[72:75], a[80:83], a[0:3], v[72:75]\n" \
"ds_read_b64_tr_b16 a[98:99], v232 offset:19456\n" \
"v_mfma_f32_16x16x32_bf16 v[72:75], a[84:87], a[4:7], v[72:75]\n" \
"s_waitcnt lgkmcnt(3)\n" \
"ds_read_b64_tr_b16 a[100:101], v232 offset:25600\n" \
"v_mfma_f32_16x16x32_bf16 v[76:79], a[88:91], a[0:3], v[76:79]\n" \
"ds_read_b64_tr_b16 a[102:103], v232 offset:27648\n" \
"v_mfma_f32_16x16x32_bf16 v[76:79], a[92:95], a[4:7], v[76:79]\n" \
"v_mfma_f32_16x16x32_bf16 v[108:111], a[88:91], a[8:11], v[108:111]\n" \
"ds_read_b64_tr_b16 a[104:105], v233 offset:17408\n" \
"v_mfma_f32_16x16x32_bf16 v[108:111], a[92:95], a[12:15], v[108:111]\n" \
"ds_read_b64_tr_b16 a[106:107], v233 offset:19456\n" \
"v_mfma_f32_16x16x32_bf16 v[104:107], a[80:83], a[8:11], v[104:107]\n" \
"ds_read_b64_tr_b16 a[108:109], v233 offset:25600\n" \
"v_mfma_f32_16x16x32_bf16 v[104:107], a[84:87], a[12:15], v[104:107]\n" \
"v_mfma_f32_16x16x32_bf16 v[100:103], a[72:75], a[8:11], v[100:103]\n" \
"ds_read_b64_tr_b16 a[110:111], v233 offset:27648\n" \
"v_mfma_f32_16x16x32_bf16 v[100:103], a[76:79], a[12:15], v[100:103]\n" \
"ds_read_b64_tr_b16 a[112:113], v232 offset:17920\n" \
"v_mfma_f32_16x16x32_bf16 v[96:99], a[64:67], a[8:11], v[96:99]\n" \
"s_add_u32 s93, s94, 0\n" \
"ds_read_b64_tr_b16 a[114:115], v232 offset:19968\n" \
"v_mfma_f32_16x16x32_bf16 v[96:99], a[68:71], a[12:15], v[96:99]\n" \
"buffer_store_dwordx4 v[0:3], v236, s[56:59], s93 offen offset:0 sc1\n" \
"v_mfma_f32_16x16x32_bf16 v[128:131], a[64:67], a[16:19], v[128:131]\n" \
"ds_read_b64_tr_b16 a[116:117], v232 offset:26112\n" \
"v_mfma_f32_16x16x32_bf16 v[128:131], a[68:71], a[20:23], v[128:131]\n" \
"ds_read_b64_tr_b16 a[118:119], v232 offset:28160\n" \
"v_mfma_f32_16x16x32_bf16 v[132:135], a[72:75], a[16:19], v[132:135]\n" \
"ds_read_b64_tr_b16 a[120:121], v233 offset:17920\n" \
"v_mfma_f32_16x16x32_bf16 v[132:135], a[76:79], a[20:23], v[132:135]\n" \
"v_mfma_f32_16x16x32_bf16 v[136:139], a[80:83], a[16:19], v[136:139]\n" \
"ds_read_b64_tr_b16 a[122:123], v233 offset:19968\n" \
"v_mfma_f32_16x16x32_bf16 v[136:139], a[84:87], a[20:23], v[136:139]\n" \
"ds_read_b64_tr_b16 a[124:125], v233 offset:26112\n" \
"v_mfma_f32_16x16x32_bf16 v[140:143], a[88:91], a[16:19], v[140:143]\n" \
"ds_read_b64_tr_b16 a[126:127], v233 offset:28160\n" \
"v_mfma_f32_16x16x32_bf16 v[140:143], a[92:95], a[20:23], v[140:143]\n" \
"s_waitcnt lgkmcnt(3)\n" \
"v_mfma_f32_16x16x32_bf16 v[172:175], a[88:91], a[24:27], v[172:175]\n" \
"v_mfma_f32_16x16x32_bf16 v[172:175], a[92:95], a[28:31], v[172:175]\n" \
"v_mfma_f32_16x16x32_bf16 v[168:171], a[80:83], a[24:27], v[168:171]\n" \
"v_mfma_f32_16x16x32_bf16 v[168:171], a[84:87], a[28:31], v[168:171]\n" \
"v_mfma_f32_16x16x32_bf16 v[164:167], a[72:75], a[24:27], v[164:167]\n" \
"v_mfma_f32_16x16x32_bf16 v[164:167], a[76:79], a[28:31], v[164:167]\n" \
"buffer_store_dwordx4 v[16:19], v236, s[56:59], s93 offen offset:256 sc1\n" \
"v_mfma_f32_16x16x32_bf16 v[160:163], a[64:67], a[24:27], v[160:163]\n" \
"v_mfma_f32_16x16x32_bf16 v[160:163], a[68:71], a[28:31], v[160:163]\n" \
"s_waitcnt vmcnt(18) lgkmcnt(0)\n" \
"s_barrier\n" \
"v_mfma_f32_16x16x32_bf16 v[80:83], a[96:99], a[0:3], v[80:83]\n" \
"ds_read_b64_tr_b16 a[64:65], v232 offset:32768\n" \
"v_mfma_f32_16x16x32_bf16 v[80:83], a[100:103], a[4:7], v[80:83]\n" \
"ds_read_b64_tr_b16 a[66:67], v232 offset:34816\n" \
"buffer_store_dword v228, v229, s[56:59], s68 offen sc1\n" \
"v_mfma_f32_16x16x32_bf16 v[84:87], a[104:107], a[0:3], v[84:87]\n" \
"ds_read_b64_tr_b16 a[68:69], v232 offset:40960\n" \
"v_lshrrev_b32 v238, 4, v234\n" \
"v_mfma_f32_16x16x32_bf16 v[84:87], a[108:111], a[4:7], v[84:87]\n" \
"ds_write_b128 v224, v[192:195] offset:8192\n" \
"v_mfma_f32_16x16x32_bf16 v[88:91], a[112:115], a[0:3], v[88:91]\n" \
"ds_read_b64_tr_b16 a[70:71], v232 offset:43008\n" \
"v_lshrrev_b32 v240, 4, v238\n" \
"v_mfma_f32_16x16x32_bf16 v[88:91], a[116:119], a[4:7], v[88:91]\n" \
"ds_read_b64_tr_b16 a[72:73], v233 offset:32768\n" \
"v_lshlrev_b32 v240, 2, v240\n" \
"v_mfma_f32_16x16x32_bf16 v[92:95], a[120:123], a[0:3], v[92:95]\n" \
"ds_read_b64_tr_b16 a[74:75], v233 offset:34816\n" \
"v_and_b32 v238, 15, v238\n" \
"v_mfma_f32_16x16x32_bf16 v[92:95], a[124:127], a[4:7], v[92:95]\n" \
"v_sub_u32 v238, v238, v240\n" \
"v_subrev_u32 v248, 0, v238\n" \
"v_mfma_f32_16x16x32_bf16 v[124:127], a[120:123], a[8:11], v[124:127]\n" \
"ds_read_b128 a[0:3], v230 offset:32768\n" \
"v_subrev_u32 v249, 1, v238\n" \
"v_mfma_f32_16x16x32_bf16 v[124:127], a[124:127], a[12:15], v[124:127]\n" \
"ds_read_b128 a[4:7], v231 offset:32768\n" \
"v_subrev_u32 v250, 2, v238\n" \
"v_mfma_f32_16x16x32_bf16 v[120:123], a[112:115], a[8:11], v[120:123]\n" \
"ds_read_b64_tr_b16 a[76:77], v233 offset:40960\n" \
"v_subrev_u32 v251, 3, v238\n" \
"v_mfma_f32_16x16x32_bf16 v[120:123], a[116:119], a[12:15], v[120:123]\n" \
"ds_write_b128 v224, v[208:211] offset:8448\n" \
"v_mfma_f32_16x16x32_bf16 v[116:119], a[104:107], a[8:11], v[116:119]\n" \
"ds_read_b64_tr_b16 a[78:79], v233 offset:43008\n" \
"v_ashrrev_i32 v248, 31, v248\n" \
"v_mfma_f32_16x16x32_bf16 v[116:119], a[108:111], a[12:15], v[116:119]\n" \
"ds_read_b64_tr_b16 a[80:81], v232 offset:33280\n" \
"v_ashrrev_i32 v249, 31, v249\n" \
"v_mfma_f32_16x16x32_bf16 v[112:115], a[96:99], a[8:11], v[112:115]\n" \
"ds_read_b64_tr_b16 a[82:83], v232 offset:35328\n" \
"v_ashrrev_i32 v250, 31, v250\n" \
"v_mfma_f32_16x16x32_bf16 v[112:115], a[100:103], a[12:15], v[112:115]\n" \
"buffer_store_dwordx4 v[8:11], v236, s[56:59], s93 offen offset:2048 sc1\n" \
"v_mfma_f32_16x16x32_bf16 v[144:147], a[96:99], a[16:19], v[144:147]\n" \
"ds_read_b128 a[8:11], v230 offset:34816\n" \
"v_ashrrev_i32 v251, 31, v251\n" \
"v_mfma_f32_16x16x32_bf16 v[144:147], a[100:103], a[20:23], v[144:147]\n" \
"ds_read_b128 a[12:15], v231 offset:34816\n" \
"v_lshlrev_b32 v248, 23, v248\n" \
"v_mfma_f32_16x16x32_bf16 v[148:151], a[104:107], a[16:19], v[148:151]\n" \
"ds_read_b64_tr_b16 a[84:85], v232 offset:41472\n" \
"v_lshlrev_b32 v249, 23, v249\n" \
"v_mfma_f32_16x16x32_bf16 v[148:151], a[108:111], a[20:23], v[148:151]\n" \
"ds_write_b128 v224, v[200:203] offset:10240\n" \
"v_mfma_f32_16x16x32_bf16 v[152:155], a[112:115], a[16:19], v[152:155]\n" \
"ds_read_b64_tr_b16 a[86:87], v232 offset:43520\n" \
"v_lshlrev_b32 v250, 23, v250\n" \
"v_mfma_f32_16x16x32_bf16 v[152:155], a[116:119], a[20:23], v[152:155]\n" \
"ds_read_b64_tr_b16 a[88:89], v233 offset:33280\n" \
"v_lshlrev_b32 v251, 23, v251\n" \
"v_mfma_f32_16x16x32_bf16 v[156:159], a[120:123], a[16:19], v[156:159]\n" \
"ds_read_b64_tr_b16 a[90:91], v233 offset:35328\n" \
"v_mov_b32 v240, 0xff800000\n" \
"v_mfma_f32_16x16x32_bf16 v[156:159], a[124:127], a[20:23], v[156:159]\n" \
"ds_write_b128 v224, v[216:219] offset:10496\n" \
"v_mfma_f32_16x16x32_bf16 v[188:191], a[120:123], a[24:27], v[188:191]\n" \
"ds_read_b128 a[16:19], v230 offset:36864\n" \
"v_add_f32 v248, 0x42400000, v248\n" \
"v_mfma_f32_16x16x32_bf16 v[188:191], a[124:127], a[28:31], v[188:191]\n" \
"ds_read_b128 a[20:23], v231 offset:36864\n" \
"v_add_f32 v249, 0x42400000, v249\n" \
"v_mfma_f32_16x16x32_bf16 v[184:187], a[112:115], a[24:27], v[184:187]\n" \
"ds_read_b64_tr_b16 a[92:93], v233 offset:41472\n" \
"v_add_f32 v250, 0x42400000, v250\n" \
"v_mfma_f32_16x16x32_bf16 v[184:187], a[116:119], a[28:31], v[184:187]\n" \
"v_add_f32 v251, 0x42400000, v251\n" \
"s_mov_b32 s72, 0x42400000\n" \
"v_mfma_f32_16x16x32_bf16 v[180:183], a[104:107], a[24:27], v[180:183]\n" \
"s_cmp_gt_u32 s61, 0\n" \
"s_cselect_b32 s70, s72, 0xff800000\n" \
"ds_read_b64_tr_b16 a[94:95], v233 offset:43520\n" \
"v_mfma_f32_16x16x32_bf16 v[180:183], a[108:111], a[28:31], v[180:183]\n" \
"buffer_store_dwordx4 v[24:27], v236, s[56:59], s93 offen offset:2304 sc1\n" \
"v_mfma_f32_16x16x32_bf16 v[176:179], a[96:99], a[24:27], v[176:179]\n" \
"ds_write_b128 v224, v[196:199] offset:0\n" \
"v_mfma_f32_16x16x32_bf16 v[176:179], a[100:103], a[28:31], v[176:179]\n" \
"s_waitcnt lgkmcnt(2)\n" \
"v_mov_b32 v252, s70\n" \
"v_mfma_f32_16x16x32_bf16 v[64:67], a[64:67], a[0:3], v[64:67]\n" \
"ds_read_b128 a[24:27], v230 offset:38912\n" \
"v_mfma_f32_16x16x32_bf16 v[64:67], a[68:71], a[4:7], v[64:67]\n" \
"s_cmp_gt_u32 s61, 1\n" \
"s_cselect_b32 s70, s72, 0xff800000\n" \
"ds_read_b128 a[28:31], v231 offset:38912\n" \
"v_mfma_f32_16x16x32_bf16 v[68:71], a[72:75], a[0:3], v[68:71]\n" \
"ds_read_b64_tr_b16 a[96:97], v232 offset:33792\n" \
"v_mov_b32 v253, s70\n" \
"v_mfma_f32_16x16x32_bf16 v[68:71], a[76:79], a[4:7], v[68:71]\n" \
"s_cmp_gt_u32 s61, 2\n" \
"s_cselect_b32 s70, s72, 0xff800000\n" \
"v_mov_b32 v226, s70\n" \
"v_mfma_f32_16x16x32_bf16 v[72:75], a[80:83], a[0:3], v[72:75]\n" \
"ds_read_b64_tr_b16 a[98:99], v232 offset:35840\n" \
"v_bfe_i32 v243, v234, 4, 1\n" \
"v_mfma_f32_16x16x32_bf16 v[72:75], a[84:87], a[4:7], v[72:75]\n" \
"s_waitcnt lgkmcnt(3)\n" \
"ds_read_b64_tr_b16 a[100:101], v232 offset:41984\n" \
"v_and_b32 v243, 0xaaaa, v243\n" \
"v_mfma_f32_16x16x32_bf16 v[76:79], a[88:91], a[0:3], v[76:79]\n" \
"ds_read_b64_tr_b16 a[102:103], v232 offset:44032\n" \
"v_or_b32 v243, 0x4444, v243\n" \
"v_mfma_f32_16x16x32_bf16 v[76:79], a[92:95], a[4:7], v[76:79]\n" \
"ds_write_b128 v224, v[212:215] offset:256\n" \
"v_mfma_f32_16x16x32_bf16 v[108:111], a[88:91], a[8:11], v[108:111]\n" \
"ds_read_b64_tr_b16 a[104:105], v233 offset:33792\n" \
"v_bfe_i32 v244, v234, 5, 1\n" \
"v_mfma_f32_16x16x32_bf16 v[108:111], a[92:95], a[12:15], v[108:111]\n" \
"ds_read_b64_tr_b16 a[106:107], v233 offset:35840\n" \
"v_not_b32 v244, v244\n" \
"v_mfma_f32_16x16x32_bf16 v[104:107], a[80:83], a[8:11], v[104:107]\n" \
"ds_read_b64_tr_b16 a[108:109], v233 offset:41984\n" \
"v_and_b32 v244, 0x3f803f80, v244\n" \
"v_mfma_f32_16x16x32_bf16 v[104:107], a[84:87], a[12:15], v[104:107]\n" \
"v_mov_b32 v245, v244\n" \
"v_mov_b32 v246, v244\n" \
"v_mfma_f32_16x16x32_bf16 v[100:103], a[72:75], a[8:11], v[100:103]\n" \
"ds_read_b64_tr_b16 a[110:111], v233 offset:44032\n" \
"v_mov_b32 v247, v244\n" \
"v_mfma_f32_16x16x32_bf16 v[100:103], a[76:79], a[12:15], v[100:103]\n" \
"s_mov_b32 s76, s73\n" \
"s_and_b32 s77, s63, 1\n" \
"ds_read_b64_tr_b16 a[112:113], v232 offset:34304\n" \
"v_mfma_f32_16x16x32_bf16 v[96:99], a[64:67], a[8:11], v[96:99]\n" \
"s_add_u32 s93, s94, 17858560\n" \
"s_lshl_b32 s77, s77, 15\n" \
"ds_read_b64_tr_b16 a[114:115], v232 offset:36352\n" \
"v_mfma_f32_16x16x32_bf16 v[96:99], a[68:71], a[12:15], v[96:99]\n" \
"buffer_store_dwordx4 v[36:39], v234, s[56:59], s93 offen offset:0 sc1\n" \
"v_mfma_f32_16x16x32_bf16 v[128:131], a[64:67], a[16:19], v[128:131]\n" \
"s_xor_b32 s89, s77, 0x8000\n" \
"s_add_u32 s82, s89, 0x10000\n" \
"ds_read_b64_tr_b16 a[116:117], v232 offset:42496\n" \
"v_mfma_f32_16x16x32_bf16 v[128:131], a[68:71], a[20:23], v[128:131]\n" \
"s_mov_b32 s74, 0\n" \
"s_add_u32 s73, s73, 34816\n" \
"ds_read_b64_tr_b16 a[118:119], v232 offset:44544\n" \
"v_mfma_f32_16x16x32_bf16 v[132:135], a[72:75], a[16:19], v[132:135]\n" \
"s_cmp_eq_u32 s73, s83\n" \
"s_cselect_b32 s70, 34816, 0\n" \
"ds_read_b64_tr_b16 a[120:121], v233 offset:34304\n" \
"v_mfma_f32_16x16x32_bf16 v[132:135], a[76:79], a[20:23], v[132:135]\n" \
"ds_write_b128 v224, v[204:207] offset:2048\n" \
"v_mfma_f32_16x16x32_bf16 v[136:139], a[80:83], a[16:19], v[136:139]\n" \
"s_add_u32 s73, s73, s70\n" \
"s_lshr_b32 s70, s61, 1\n" \
"ds_read_b64_tr_b16 a[122:123], v233 offset:36352\n" \
"v_mfma_f32_16x16x32_bf16 v[136:139], a[84:87], a[20:23], v[136:139]\n" \
"s_lshl_b32 s85, s63, 2\n" \
"s_sub_u32 s86, 60, s85\n" \
"ds_read_b64_tr_b16 a[124:125], v233 offset:42496\n" \
"v_mfma_f32_16x16x32_bf16 v[140:143], a[88:91], a[16:19], v[140:143]\n" \
"s_add_u32 s85, s85, s70\n" \
"s_add_u32 s86, s86, s70\n" \
"ds_read_b64_tr_b16 a[126:127], v233 offset:44544\n" \
"v_mfma_f32_16x16x32_bf16 v[140:143], a[92:95], a[20:23], v[140:143]\n" \
"s_waitcnt lgkmcnt(4)\n" \
"ds_write_b128 v224, v[220:223] offset:2304\n" \
"v_mfma_f32_16x16x32_bf16 v[172:175], a[88:91], a[24:27], v[172:175]\n" \
"s_mul_i32 s87, s85, 266240\n" \
"s_mul_i32 s88, s86, 266240\n" \
"v_add_u32 v230, 0x10000, v230\n" \
"v_mfma_f32_16x16x32_bf16 v[172:175], a[92:95], a[28:31], v[172:175]\n" \
"s_and_b32 s75, s61, 1\n" \
"s_lshl_b32 s70, s75, 8\n" \
"v_add_u32 v231, 0x10000, v231\n" \
"v_mfma_f32_16x16x32_bf16 v[168:171], a[80:83], a[24:27], v[168:171]\n" \
"s_lshl_b32 s91, s62, 13\n" \
"s_add_u32 s91, s91, s70\n" \
"s_add_u32 s91, s91, 35700736\n" \
"v_mfma_f32_16x16x32_bf16 v[168:171], a[84:87], a[28:31], v[168:171]\n" \
"buffer_store_dwordx4 v[52:55], v234, s[56:59], s93 offen offset:1024 sc1\n" \
"v_mfma_f32_16x16x32_bf16 v[164:167], a[72:75], a[24:27], v[164:167]\n" \
"s_add_u32 s87, s87, s91\n" \
"s_add_u32 s88, s88, s91\n" \
"s_lshl_b32 s75, s75, 14\n" \
"v_mfma_f32_16x16x32_bf16 v[164:167], a[76:79], a[28:31], v[164:167]\n" \
"s_cmp_eq_u32 s63, 7\n" \
"s_cselect_b32 s75, 0, s75\n" \
"s_xor_b32 s78, s75, 0xc000\n" \
"s_mov_b32 s81, s78\n" \
"s_cmp_eq_u32 s63, 0\n" \
"s_cselect_b32 s91, s75, 0\n" \
"s_add_u32 s91, s91, 0x8000\n" \
"s_xor_b32 s75, s75, 0x8000\n" \
"s_sub_u32 s84, 7, s63\n" \
"v_mfma_f32_16x16x32_bf16 v[160:163], a[64:67], a[24:27], v[160:163]\n" \
"v_mfma_f32_16x16x32_bf16 v[160:163], a[68:71], a[28:31], v[160:163]\n" \
"s_waitcnt vmcnt(19) lgkmcnt(0)\n" \
"s_barrier\n" \
"v_mfma_f32_16x16x32_bf16 v[144:147], a[96:99], a[16:19], v[144:147]\n" \
"s_mov_b64 s[44:45], s[10:11]\n" \
"s_mov_b32 s46, 0\n" \
"ds_read_b64_tr_b16 a[64:65], v232 offset:49152\n" \
"v_mfma_f32_16x16x32_bf16 v[144:147], a[100:103], a[20:23], v[144:147]\n" \
"s_mov_b32 s47, 0\n" \
"s_cmp_eq_u32 s63, 7\n" \
"s_cselect_b64 s[44:45], s[18:19], s[44:45]\n" \
"s_cselect_b32 s46, 5, s46\n" \
"s_cmp_eq_u32 s63, 3\n" \
"s_cselect_b64 s[44:45], s[18:19], s[44:45]\n" \
"s_cselect_b32 s46, 3, s46\n" \
"ds_read_b64_tr_b16 a[66:67], v232 offset:51200\n" \
"v_mfma_f32_16x16x32_bf16 v[148:151], a[104:107], a[16:19], v[148:151]\n" \
"s_cmp_eq_u32 s63, 3\n" \
"s_cselect_b32 s47, 640, s47\n" \
"ds_read_b64_tr_b16 a[68:69], v232 offset:57344\n" \
"v_mfma_f32_16x16x32_bf16 v[148:151], a[108:111], a[20:23], v[148:151]\n" \
"ds_write_b128 v225, a[192:195] offset:0\n" \
"v_mfma_f32_16x16x32_bf16 v[152:155], a[112:115], a[16:19], v[152:155]\n" \
"ds_read_b64_tr_b16 a[70:71], v232 offset:59392\n" \
"v_mfma_f32_16x16x32_bf16 v[152:155], a[116:119], a[20:23], v[152:155]\n" \
"s_cmp_eq_u32 s63, 6\n" \
"s_cselect_b64 s[44:45], s[22:23], s[44:45]\n" \
"s_cselect_b32 s46, 6, s46\n" \
"ds_read_b64_tr_b16 a[72:73], v233 offset:49152\n" \
"v_mfma_f32_16x16x32_bf16 v[156:159], a[120:123], a[16:19], v[156:159]\n" \
"ds_read_b64_tr_b16 a[74:75], v233 offset:51200\n" \
"v_mfma_f32_16x16x32_bf16 v[156:159], a[124:127], a[20:23], v[156:159]\n" \
"s_cmp_eq_u32 s63, 4\n" \
"s_cselect_b64 s[44:45], s[20:21], s[44:45]\n" \
"s_cselect_b32 s46, 4, s46\n" \
"v_mfma_f32_16x16x32_bf16 v[188:191], a[120:123], a[24:27], v[188:191]\n" \
"s_waitcnt vmcnt(17)\n" \
"ds_read_b128 a[16:19], v230 offset:0\n" \
"v_mfma_f32_16x16x32_bf16 v[188:191], a[124:127], a[28:31], v[188:191]\n" \
"s_cmp_eq_u32 s63, 2\n" \
"s_cselect_b64 s[44:45], s[24:25], s[44:45]\n" \
"s_cselect_b32 s46, 2, s46\n" \
"ds_read_b128 a[20:23], v231 offset:0\n" \
"v_mfma_f32_16x16x32_bf16 v[184:187], a[112:115], a[24:27], v[184:187]\n" \
"s_lshl_b32 s48, s62, 2\n" \
"s_add_u32 s48, s48, s61\n" \
"ds_read_b64_tr_b16 a[76:77], v233 offset:57344\n" \
"v_mfma_f32_16x16x32_bf16 v[184:187], a[116:119], a[28:31], v[184:187]\n" \
"buffer_store_dwordx4 v[44:47], v234, s[56:59], s93 offen offset:2048 sc1\n" \
"v_mfma_f32_16x16x32_bf16 v[180:183], a[104:107], a[24:27], v[180:183]\n" \
"s_add_u32 s48, s48, s47\n" \
"s_lshl_b32 s48, s48, 13\n" \
"ds_read_b64_tr_b16 a[78:79], v233 offset:59392\n" \
"v_mfma_f32_16x16x32_bf16 v[180:183], a[108:111], a[28:31], v[180:183]\n" \
"s_add_u32 s44, s44, s48\n" \
"s_addc_u32 s45, s45, 0\n" \
"ds_read_b64_tr_b16 a[80:81], v232 offset:49664\n" \
"v_mfma_f32_16x16x32_bf16 v[176:179], a[96:99], a[24:27], v[176:179]\n" \
"s_cmp_eq_u32 s46, 0\n" \
"s_cselect_b32 s48, 28, 3\n" \
"ds_read_b64_tr_b16 a[82:83], v232 offset:51712\n" \
"v_mfma_f32_16x16x32_bf16 v[176:179], a[100:103], a[28:31], v[176:179]\n" \
"ds_write_b128 v225, a[196:199] offset:1024\n" \
"v_mfma_f32_16x16x32_bf16 v[80:83], a[96:99], a[0:3], v[80:83]\n" \
"s_waitcnt vmcnt(16)\n" \
"ds_read_b128 a[24:27], v230 offset:2048\n" \
"v_mfma_f32_16x16x32_bf16 v[80:83], a[100:103], a[4:7], v[80:83]\n" \
"ds_read_b128 a[28:31], v231 offset:2048\n" \
"v_mfma_f32_16x16x32_bf16 v[84:87], a[104:107], a[0:3], v[84:87]\n" \
"ds_read_b64_tr_b16 a[84:85], v232 offset:57856\n" \
"v_mfma_f32_16x16x32_bf16 v[84:87], a[108:111], a[4:7], v[84:87]\n" \
"ds_write_b128 v225, a[200:203] offset:2048\n" \
"v_mfma_f32_16x16x32_bf16 v[88:91], a[112:115], a[0:3], v[88:91]\n" \
"ds_read_b64_tr_b16 a[86:87], v232 offset:59904\n" \
"v_mfma_f32_16x16x32_bf16 v[88:91], a[116:119], a[4:7], v[88:91]\n" \
"ds_read_b64_tr_b16 a[88:89], v233 offset:49664\n" \
"v_mfma_f32_16x16x32_bf16 v[92:95], a[120:123], a[0:3], v[92:95]\n" \
"ds_read_b64_tr_b16 a[90:91], v233 offset:51712\n" \
"v_mfma_f32_16x16x32_bf16 v[92:95], a[124:127], a[4:7], v[92:95]\n" \
"ds_write_b128 v225, a[204:207] offset:3072\n" \
"v_mfma_f32_16x16x32_bf16 v[124:127], a[120:123], a[8:11], v[124:127]\n" \
"s_waitcnt vmcnt(14)\n" \
"ds_read_b128 a[0:3], v230 offset:4096\n" \
"v_mfma_f32_16x16x32_bf16 v[124:127], a[124:127], a[12:15], v[124:127]\n" \
"ds_read_b128 a[4:7], v231 offset:4096\n" \
"v_mfma_f32_16x16x32_bf16 v[120:123], a[112:115], a[8:11], v[120:123]\n" \
"ds_read_b64_tr_b16 a[92:93], v233 offset:57856\n" \
"v_mfma_f32_16x16x32_bf16 v[120:123], a[116:119], a[12:15], v[120:123]\n" \
"buffer_store_dwordx4 v[60:63], v234, s[56:59], s93 offen offset:3072 sc1\n" \
"v_mfma_f32_16x16x32_bf16 v[116:119], a[104:107], a[8:11], v[116:119]\n" \
"ds_read_b64_tr_b16 a[94:95], v233 offset:59904\n" \
"v_mfma_f32_16x16x32_bf16 v[116:119], a[108:111], a[12:15], v[116:119]\n" \
"v_mfma_f32_16x16x32_bf16 v[112:115], a[96:99], a[8:11], v[112:115]\n" \
"ds_write_b128 v225, a[224:227] offset:8192\n" \
"v_mfma_f32_16x16x32_bf16 v[112:115], a[100:103], a[12:15], v[112:115]\n" \
"s_waitcnt lgkmcnt(2)\n" \
"v_mfma_f32_16x16x32_bf16 v[64:67], a[64:67], a[16:19], v[64:67]\n" \
"s_waitcnt vmcnt(13)\n" \
"ds_read_b128 a[8:11], v230 offset:6144\n" \
"ds_read_b128 a[12:15], v231 offset:6144\n" \
"v_mfma_f32_16x16x32_bf16 v[64:67], a[68:71], a[20:23], v[64:67]\n" \
"ds_read_b64_tr_b16 a[96:97], v232 offset:50176\n" \
"ds_read_b64_tr_b16 a[98:99], v232 offset:52224\n" \
"v_mfma_f32_16x16x32_bf16 v[68:71], a[72:75], a[16:19], v[68:71]\n" \
"s_waitcnt lgkmcnt(5)\n" \
"ds_read_b64_tr_b16 a[100:101], v232 offset:58368\n" \
"ds_read_b64_tr_b16 a[102:103], v232 offset:60416\n" \
"v_mfma_f32_16x16x32_bf16 v[68:71], a[76:79], a[20:23], v[68:71]\n" \
"s_getpc_b64 s[64:65]\n" \
"Lpd1_%=:\n" \
"s_lshl_b32 s66, s62, 2\n" \
"ds_read_b64_tr_b16 a[104:105], v233 offset:50176\n" \
"ds_read_b64_tr_b16 a[106:107], v233 offset:52224\n" \
"v_mfma_f32_16x16x32_bf16 v[72:75], a[80:83], a[16:19], v[72:75]\n" \
"s_add_u32 s66, s66, s61\n" \
"ds_read_b64_tr_b16 a[108:109], v233 offset:58368\n" \
"ds_read_b64_tr_b16 a[110:111], v233 offset:60416\n" \
"v_mfma_f32_16x16x32_bf16 v[72:75], a[84:87], a[20:23], v[72:75]\n" \
"s_and_b32 s66, s66, 15\n" \
"ds_read_b64_tr_b16 a[112:113], v232 offset:50688\n" \
"ds_read_b64_tr_b16 a[114:115], v232 offset:52736\n" \
"v_mfma_f32_16x16x32_bf16 v[76:79], a[88:91], a[16:19], v[76:79]\n" \
"s_lshl_b32 s66, s66, 12\n" \
"ds_read_b64_tr_b16 a[116:117], v232 offset:58880\n" \
"ds_read_b64_tr_b16 a[118:119], v232 offset:60928\n" \
"v_mfma_f32_16x16x32_bf16 v[76:79], a[92:95], a[20:23], v[76:79]\n" \
"ds_read_b64_tr_b16 a[120:121], v233 offset:50688\n" \
"ds_read_b64_tr_b16 a[122:123], v233 offset:52736\n" \
"v_mfma_f32_16x16x32_bf16 v[96:99], a[64:67], a[24:27], v[96:99]\n" \
"ds_read_b64_tr_b16 a[124:125], v233 offset:58880\n" \
"ds_read_b64_tr_b16 a[126:127], v233 offset:60928\n" \
"v_mfma_f32_16x16x32_bf16 v[96:99], a[68:71], a[28:31], v[96:99]\n" \
"s_add_u32 s93, s94, 17850368\n" \
"s_add_u32 s64, s64, Lloop2_%=-Lpd1_%=\n" \
"s_addc_u32 s65, s65, 0\n" \
"s_mov_b32 s67, Lcode_end_%=-Lloop2_%=-4\n" \
"v_mfma_f32_16x16x32_bf16 v[100:103], a[72:75], a[24:27], v[100:103]\n" \
"buffer_store_dwordx4 v[4:7], v234, s[56:59], s93 offen offset:0 sc1\n" \
"v_mfma_f32_16x16x32_bf16 v[100:103], a[76:79], a[28:31], v[100:103]\n" \
"ds_write_b128 v225, a[228:231] offset:9216\n" \
"v_mfma_f32_16x16x32_bf16 v[104:107], a[80:83], a[24:27], v[104:107]\n" \
"buffer_load_dword v227, v242, s[56:59], s90 offen sc1\n" \
"v_mfma_f32_16x16x32_bf16 v[104:107], a[84:87], a[28:31], v[104:107]\n" \
"s_waitcnt lgkmcnt(1)\n" \
"v_lshl_add_u32 v230, v234, 2, s66\n" \
"v_min_u32 v230, s67, v230\n" \
"v_mfma_f32_16x16x32_bf16 v[108:111], a[88:91], a[24:27], v[108:111]\n" \
"ds_write_b128 v225, a[232:235] offset:10240\n" \
"v_mfma_f32_16x16x32_bf16 v[108:111], a[92:95], a[28:31], v[108:111]\n" \
"global_load_dword v230, v230, s[64:65]\n" \
"v_mfma_f32_16x16x32_bf16 v[80:83], a[96:99], a[16:19], v[80:83]\n" \
"v_cvt_pk_bf16_f32 v64, v64, v65\n" \
"v_cvt_pk_bf16_f32 v65, v66, v67\n" \
"v_mfma_f32_16x16x32_bf16 v[80:83], a[100:103], a[20:23], v[80:83]\n" \
"v_cvt_pk_bf16_f32 v66, v68, v69\n" \
"v_cvt_pk_bf16_f32 v67, v70, v71\n" \
"v_accvgpr_write_b32 a128, v64\n" \
"v_mfma_f32_16x16x32_bf16 v[84:87], a[104:107], a[16:19], v[84:87]\n" \
"v_accvgpr_write_b32 a129, v65\n" \
"v_accvgpr_write_b32 a130, v66\n" \
"v_mfma_f32_16x16x32_bf16 v[84:87], a[108:111], a[20:23], v[84:87]\n" \
"ds_write_b128 v225, a[236:239] offset:11264\n" \
"v_mfma_f32_16x16x32_bf16 v[88:91], a[112:115], a[16:19], v[88:91]\n" \
"v_accvgpr_write_b32 a131, v67\n" \
"v_cvt_pk_bf16_f32 v72, v72, v73\n" \
"v_mfma_f32_16x16x32_bf16 v[88:91], a[116:119], a[20:23], v[88:91]\n" \
"v_cvt_pk_bf16_f32 v73, v74, v75\n" \
"v_cvt_pk_bf16_f32 v74, v76, v77\n" \
"v_cvt_pk_bf16_f32 v75, v78, v79\n" \
"v_mfma_f32_16x16x32_bf16 v[92:95], a[120:123], a[16:19], v[92:95]\n" \
"v_accvgpr_write_b32 a132, v72\n" \
"v_accvgpr_write_b32 a133, v73\n" \
"v_mfma_f32_16x16x32_bf16 v[92:95], a[124:127], a[20:23], v[92:95]\n" \
"v_accvgpr_write_b32 a134, v74\n" \
"v_accvgpr_write_b32 a135, v75\n" \
"v_mfma_f32_16x16x32_bf16 v[112:115], a[96:99], a[24:27], v[112:115]\n" \
"v_cvt_pk_bf16_f32 v96, v96, v97\n" \
"v_cvt_pk_bf16_f32 v97, v98, v99\n" \
"v_mfma_f32_16x16x32_bf16 v[112:115], a[100:103], a[28:31], v[112:115]\n" \
"buffer_store_dwordx4 v[20:23], v234, s[56:59], s93 offen offset:1024 sc1\n" \
"v_mfma_f32_16x16x32_bf16 v[116:119], a[104:107], a[24:27], v[116:119]\n" \
"v_cvt_pk_bf16_f32 v98, v100, v101\n" \
"v_cvt_pk_bf16_f32 v99, v102, v103\n" \
"v_mfma_f32_16x16x32_bf16 v[116:119], a[108:111], a[28:31], v[116:119]\n" \
"v_cvt_pk_bf16_f32 v104, v104, v105\n" \
"v_cvt_pk_bf16_f32 v105, v106, v107\n" \
"v_cvt_pk_bf16_f32 v106, v108, v109\n" \
"v_mfma_f32_16x16x32_bf16 v[120:123], a[112:115], a[24:27], v[120:123]\n" \
"v_cvt_pk_bf16_f32 v107, v110, v111\n" \
"v_mfma_f32_16x16x32_bf16 v[120:123], a[116:119], a[28:31], v[120:123]\n" \
"v_cvt_pk_bf16_f32 v80, v80, v81\n" \
"v_cvt_pk_bf16_f32 v81, v82, v83\n" \
"v_mfma_f32_16x16x32_bf16 v[124:127], a[120:123], a[24:27], v[124:127]\n" \
"v_cvt_pk_bf16_f32 v82, v84, v85\n" \
"v_cvt_pk_bf16_f32 v83, v86, v87\n" \
"v_mfma_f32_16x16x32_bf16 v[124:127], a[124:127], a[28:31], v[124:127]\n" \
"v_accvgpr_write_b32 a136, v80\n" \
"s_waitcnt lgkmcnt(0)\n" \
"s_barrier\n" \
"v_mfma_f32_16x16x32_bf16 v[128:131], a[64:67], a[0:3], v[128:131]\n" \
"v_accvgpr_write_b32 a137, v81\n" \
"v_accvgpr_write_b32 a138, v82\n" \
"v_mfma_f32_16x16x32_bf16 v[128:131], a[68:71], a[4:7], v[128:131]\n" \
"ds_read_b128 a[192:195], v237 offset:0\n" \
"v_accvgpr_write_b32 a139, v83\n" \
"v_mfma_f32_16x16x32_bf16 v[132:135], a[72:75], a[0:3], v[132:135]\n" \
"v_cvt_pk_bf16_f32 v88, v88, v89\n" \
"v_cvt_pk_bf16_f32 v89, v90, v91\n" \
"v_mfma_f32_16x16x32_bf16 v[132:135], a[76:79], a[4:7], v[132:135]\n" \
"ds_read_b128 a[196:199], v237 offset:2048\n" \
"v_cvt_pk_bf16_f32 v90, v92, v93\n" \
"v_mfma_f32_16x16x32_bf16 v[136:139], a[80:83], a[0:3], v[136:139]\n" \
"v_cvt_pk_bf16_f32 v91, v94, v95\n" \
"v_med3_f32 v208, v248, v252, s72\n" \
"v_mfma_f32_16x16x32_bf16 v[136:139], a[84:87], a[4:7], v[136:139]\n" \
"ds_read_b128 a[200:203], v237 offset:4096\n" \
"v_med3_f32 v209, v249, v252, s72\n" \
"v_med3_f32 v210, v250, v252, s72\n" \
"v_mfma_f32_16x16x32_bf16 v[140:143], a[88:91], a[0:3], v[140:143]\n" \
"v_med3_f32 v211, v251, v252, s72\n" \
"v_cvt_pk_bf16_f32 v112, v112, v113\n" \
"v_mfma_f32_16x16x32_bf16 v[140:143], a[92:95], a[4:7], v[140:143]\n" \
"ds_read_b128 a[204:207], v237 offset:6144\n" \
"v_cvt_pk_bf16_f32 v113, v114, v115\n" \
"v_mfma_f32_16x16x32_bf16 v[144:147], a[96:99], a[0:3], v[144:147]\n" \
"v_cvt_pk_bf16_f32 v114, v116, v117\n" \
"v_cvt_pk_bf16_f32 v115, v118, v119\n" \
"v_mfma_f32_16x16x32_bf16 v[144:147], a[100:103], a[4:7], v[144:147]\n" \
"ds_read_b128 a[208:211], v237 offset:256\n" \
"v_cvt_pk_bf16_f32 v120, v120, v121\n" \
"v_cvt_pk_bf16_f32 v121, v122, v123\n" \
"v_mfma_f32_16x16x32_bf16 v[148:151], a[104:107], a[0:3], v[148:151]\n" \
"buffer_store_dwordx4 v[12:15], v234, s[56:59], s93 offen offset:2048 sc1\n" \
"v_mfma_f32_16x16x32_bf16 v[148:151], a[108:111], a[4:7], v[148:151]\n" \
"ds_read_b128 a[212:215], v237 offset:2304\n" \
"v_cvt_pk_bf16_f32 v122, v124, v125\n" \
"v_mfma_f32_16x16x32_bf16 v[152:155], a[112:115], a[0:3], v[152:155]\n" \
"v_cvt_pk_bf16_f32 v123, v126, v127\n" \
"v_mfma_f32_16x16x32_bf16 v[152:155], a[116:119], a[4:7], v[152:155]\n" \
"ds_read_b128 a[216:219], v237 offset:4352\n" \
"v_cvt_pk_bf16_f32 v128, v128, v129\n" \
"v_mfma_f32_16x16x32_bf16 v[156:159], a[120:123], a[0:3], v[156:159]\n" \
"v_cvt_pk_bf16_f32 v129, v130, v131\n" \
"v_cvt_pk_bf16_f32 v130, v132, v133\n" \
"v_mfma_f32_16x16x32_bf16 v[156:159], a[124:127], a[4:7], v[156:159]\n" \
"ds_read_b128 a[220:223], v237 offset:6400\n" \
"v_cvt_pk_bf16_f32 v131, v134, v135\n" \
"v_cvt_pk_bf16_f32 v136, v136, v137\n" \
"v_mfma_f32_16x16x32_bf16 v[160:163], a[64:67], a[8:11], v[160:163]\n" \
"v_cvt_pk_bf16_f32 v137, v138, v139\n" \
"v_cvt_pk_bf16_f32 v138, v140, v141\n" \
"v_mfma_f32_16x16x32_bf16 v[160:163], a[68:71], a[12:15], v[160:163]\n" \
"ds_read_b128 a[224:227], v237 offset:8192\n" \
"v_cvt_pk_bf16_f32 v139, v142, v143\n" \
"v_mfma_f32_16x16x32_bf16 v[164:167], a[72:75], a[8:11], v[164:167]\n" \
"v_mfma_f32_16x16x32_bf16 v[164:167], a[76:79], a[12:15], v[164:167]\n" \
"ds_read_b128 a[228:231], v237 offset:10240\n" \
"v_mfma_f32_16x16x32_bf16 v[168:171], a[80:83], a[8:11], v[168:171]\n" \
"v_mfma_f32_16x16x32_bf16 v[168:171], a[84:87], a[12:15], v[168:171]\n" \
"ds_read_b128 a[232:235], v237 offset:12288\n" \
"v_cvt_pk_bf16_f32 v144, v144, v145\n" \
"v_cvt_pk_bf16_f32 v145, v146, v147\n" \
"v_mfma_f32_16x16x32_bf16 v[172:175], a[88:91], a[8:11], v[172:175]\n" \
"v_cvt_pk_bf16_f32 v146, v148, v149\n" \
"v_cvt_pk_bf16_f32 v147, v150, v151\n" \
"v_mfma_f32_16x16x32_bf16 v[172:175], a[92:95], a[12:15], v[172:175]\n" \
"ds_read_b128 a[236:239], v237 offset:14336\n" \
"v_cvt_pk_bf16_f32 v152, v152, v153\n" \
"v_mfma_f32_16x16x32_bf16 v[176:179], a[96:99], a[8:11], v[176:179]\n" \
"v_cvt_pk_bf16_f32 v153, v154, v155\n" \
"v_cvt_pk_bf16_f32 v154, v156, v157\n" \
"v_mfma_f32_16x16x32_bf16 v[176:179], a[100:103], a[12:15], v[176:179]\n" \
"ds_read_b128 a[240:243], v237 offset:8448\n" \
"v_cvt_pk_bf16_f32 v155, v158, v159\n" \
"v_mfma_f32_16x16x32_bf16 v[180:183], a[104:107], a[8:11], v[180:183]\n" \
"buffer_store_dwordx4 v[28:31], v234, s[56:59], s93 offen offset:3072 sc1\n" \
"v_mfma_f32_16x16x32_bf16 v[180:183], a[108:111], a[12:15], v[180:183]\n" \
"ds_read_b128 a[244:247], v237 offset:10496\n" \
"ds_write_b128 v239, v[128:131] offset:0\n" \
"v_mov_b32 v196, 0x42400000\n" \
"v_mfma_f32_16x16x32_bf16 v[184:187], a[112:115], a[8:11], v[184:187]\n" \
"v_mov_b32 v197, 0x42400000\n" \
"v_mov_b32 v198, 0x42400000\n" \
"v_mfma_f32_16x16x32_bf16 v[184:187], a[116:119], a[12:15], v[184:187]\n" \
"ds_read_b128 a[248:251], v237 offset:12544\n" \
"v_mov_b32 v199, 0x42400000\n" \
"v_cvt_pk_bf16_f32 v160, v160, v161\n" \
"v_mfma_f32_16x16x32_bf16 v[188:191], a[120:123], a[8:11], v[188:191]\n" \
"v_cvt_pk_bf16_f32 v161, v162, v163\n" \
"v_cvt_pk_bf16_f32 v162, v164, v165\n" \
"v_mfma_f32_16x16x32_bf16 v[188:191], a[124:127], a[12:15], v[188:191]\n" \
"s_waitcnt lgkmcnt(3)\n" \
"ds_read_b128 a[252:255], v237 offset:14592\n" \
"v_cvt_pk_bf16_f32 v163, v166, v167\n" \
"v_mfma_f32_16x16x32_bf16 v[16:19], a[192:195], v[96:99], v[208:211]\n" \
"v_cvt_pk_bf16_f32 v168, v168, v169\n" \
"v_cvt_pk_bf16_f32 v169, v170, v171\n" \
"v_mfma_f32_16x16x32_bf16 v[16:19], a[196:199], v[104:107], v[16:19]\n" \
"v_cvt_pk_bf16_f32 v170, v172, v173\n" \
"v_cvt_pk_bf16_f32 v171, v174, v175\n" \
"v_mfma_f32_16x16x32_bf16 v[16:19], a[200:203], v[112:115], v[16:19]\n" \
"v_cvt_pk_bf16_f32 v176, v176, v177\n" \
"v_cvt_pk_bf16_f32 v177, v178, v179\n" \
"v_mfma_f32_16x16x32_bf16 v[16:19], a[204:207], v[120:123], v[16:19]\n" \
"v_cvt_pk_bf16_f32 v178, v180, v181\n" \
"v_cvt_pk_bf16_f32 v179, v182, v183\n" \
"v_mfma_f32_16x16x32_bf16 v[32:35], a[204:207], v[152:155], v[196:199]\n" \
"v_cvt_pk_bf16_f32 v184, v184, v185\n" \
"v_cvt_pk_bf16_f32 v185, v186, v187\n" \
"Lpg5014_%=:\n" \
"s_cbranch_execz Lpg6200_%=\n" \
"v_mfma_f32_16x16x32_bf16 v[32:35], a[200:203], v[144:147], v[32:35]\n" \
"v_cvt_pk_bf16_f32 v186, v188, v189\n" \
"v_cvt_pk_bf16_f32 v187, v190, v191\n" \
"v_mfma_f32_16x16x32_bf16 v[32:35], a[196:199], v[136:139], v[32:35]\n" \
"v_med3_f32 v212, v248, v253, v252\n" \
"v_med3_f32 v213, v249, v253, v252\n" \
"v_mfma_f32_16x16x32_bf16 v[32:35], a[192:195], v[128:131], v[32:35]\n" \
"v_med3_f32 v214, v250, v253, v252\n" \
"v_med3_f32 v215, v251, v253, v252\n" \
"v_mfma_f32_16x16x32_bf16 v[48:51], a[192:195], v[160:163], v[196:199]\n" \
"v_med3_f32 v216, v248, v226, v253\n" \
"v_med3_f32 v217, v249, v226, v253\n" \
"v_mfma_f32_16x16x32_bf16 v[48:51], a[196:199], v[168:171], v[48:51]\n" \
"ds_read_b128 a[192:195], v237 offset:16384\n" \
"v_med3_f32 v218, v250, v226, v253\n" \
"v_mfma_f32_16x16x32_bf16 v[48:51], a[200:203], v[176:179], v[48:51]\n" \
"ds_read_b128 a[196:199], v237 offset:18432\n" \
"v_med3_f32 v219, v251, v226, v253\n" \
"v_mfma_f32_16x16x32_bf16 v[48:51], a[204:207], v[184:187], v[48:51]\n" \
"ds_read_b128 a[200:203], v237 offset:20480\n" \
"v_med3_f32 v220, v248, v240, v226\n" \
"v_mfma_f32_16x16x32_bf16 v[52:55], a[220:223], v[184:187], v[196:199]\n" \
"ds_read_b128 a[204:207], v237 offset:22528\n" \
"v_med3_f32 v221, v249, v240, v226\n" \
"v_mfma_f32_16x16x32_bf16 v[52:55], a[216:219], v[176:179], v[52:55]\n" \
"ds_write_b128 v239, v[136:139] offset:1024\n" \
"v_mfma_f32_16x16x32_bf16 v[52:55], a[212:215], v[168:171], v[52:55]\n" \
"v_med3_f32 v222, v250, v240, v226\n" \
"v_med3_f32 v223, v251, v240, v226\n" \
"v_mfma_f32_16x16x32_bf16 v[52:55], a[208:211], v[160:163], v[52:55]\n" \
"v_accvgpr_write_b32 a144, v96\n" \
"v_accvgpr_write_b32 a145, v97\n" \
"v_mfma_f32_16x16x32_bf16 v[36:39], a[208:211], v[128:131], v[196:199]\n" \
"ds_write_b128 v239, v[144:147] offset:2048\n" \
"v_mfma_f32_16x16x32_bf16 v[36:39], a[212:215], v[136:139], v[36:39]\n" \
"v_accvgpr_write_b32 a146, v98\n" \
"v_accvgpr_write_b32 a147, v99\n" \
"v_mfma_f32_16x16x32_bf16 v[36:39], a[216:219], v[144:147], v[36:39]\n" \
"v_accvgpr_write_b32 a148, v104\n" \
"v_accvgpr_write_b32 a149, v105\n" \
"v_mfma_f32_16x16x32_bf16 v[36:39], a[220:223], v[152:155], v[36:39]\n" \
"ds_write_b128 v239, v[152:155] offset:3072\n" \
"v_mfma_f32_16x16x32_bf16 v[20:23], a[220:223], v[120:123], v[212:215]\n" \
"v_accvgpr_write_b32 a150, v106\n" \
"v_accvgpr_write_b32 a151, v107\n" \
"v_mfma_f32_16x16x32_bf16 v[20:23], a[216:219], v[112:115], v[20:23]\n" \
"ds_read_b128 a[220:223], v237 offset:22784\n" \
"v_accvgpr_write_b32 a152, v112\n" \
"v_mfma_f32_16x16x32_bf16 v[20:23], a[212:215], v[104:107], v[20:23]\n" \
"ds_read_b128 a[216:219], v237 offset:20736\n" \
"v_accvgpr_write_b32 a153, v113\n" \
"v_mfma_f32_16x16x32_bf16 v[20:23], a[208:211], v[96:99], v[20:23]\n" \
"ds_read_b128 a[212:215], v237 offset:18688\n" \
"v_accvgpr_write_b32 a154, v114\n" \
"v_mfma_f32_16x16x32_bf16 v[24:27], a[224:227], v[96:99], v[216:219]\n" \
"ds_read_b128 a[208:211], v237 offset:16640\n" \
"v_accvgpr_write_b32 a155, v115\n" \
"v_mfma_f32_16x16x32_bf16 v[24:27], a[228:231], v[104:107], v[24:27]\n" \
"ds_write_b128 v239, v[160:163] offset:4096\n" \
"v_mfma_f32_16x16x32_bf16 v[24:27], a[232:235], v[112:115], v[24:27]\n" \
"v_accvgpr_write_b32 a156, v120\n" \
"v_accvgpr_write_b32 a157, v121\n" \
"v_mfma_f32_16x16x32_bf16 v[24:27], a[236:239], v[120:123], v[24:27]\n" \
"v_accvgpr_write_b32 a158, v122\n" \
"v_accvgpr_write_b32 a159, v123\n" \
"v_mfma_f32_16x16x32_bf16 v[40:43], a[236:239], v[152:155], v[196:199]\n" \
"ds_write_b128 v239, v[168:171] offset:5120\n" \
"v_mfma_f32_16x16x32_bf16 v[40:43], a[232:235], v[144:147], v[40:43]\n" \
"v_accvgpr_write_b32 a140, v88\n" \
"v_accvgpr_write_b32 a141, v89\n" \
"v_mfma_f32_16x16x32_bf16 v[40:43], a[228:231], v[136:139], v[40:43]\n" \
"v_accvgpr_write_b32 a142, v90\n" \
"v_accvgpr_write_b32 a143, v91\n" \
"v_mfma_f32_16x16x32_bf16 v[40:43], a[224:227], v[128:131], v[40:43]\n" \
"ds_write_b128 v239, v[176:179] offset:6144\n" \
"v_mfma_f32_16x16x32_bf16 v[56:59], a[224:227], v[160:163], v[196:199]\n" \
"v_add_u32 v233, s82, v235\n" \
"v_mfma_f32_16x16x32_bf16 v[56:59], a[228:231], v[168:171], v[56:59]\n" \
"ds_read_b128 a[224:227], v237 offset:24576\n" \
"v_mfma_f32_16x16x32_bf16 v[56:59], a[232:235], v[176:179], v[56:59]\n" \
"ds_read_b128 a[228:231], v237 offset:26624\n" \
"ds_write_b128 v239, v[184:187] offset:7168\n" \
"v_exp_f32 v16, v16\n" \
"v_mfma_f32_16x16x32_bf16 v[56:59], a[236:239], v[184:187], v[56:59]\n" \
"s_waitcnt lgkmcnt(4)\n" \
"ds_read_b128 a[232:235], v237 offset:28672\n" \
"v_exp_f32 v17, v17\n" \
"v_mfma_f32_16x16x32_bf16 v[60:63], a[252:255], v[184:187], v[196:199]\n" \
"ds_read_b128 a[236:239], v237 offset:30720\n" \
"v_mfma_f32_16x16x32_bf16 v[60:63], a[248:251], v[176:179], v[60:63]\n" \
"s_waitcnt vmcnt(4)\n" \
"v_cmp_eq_u32 vcc, s60, v227\n" \
"v_mfma_f32_16x16x32_bf16 v[60:63], a[244:247], v[168:171], v[60:63]\n" \
"v_exp_f32 v18, v18\n" \
"v_mfma_f32_16x16x32_bf16 v[60:63], a[240:243], v[160:163], v[60:63]\n" \
"s_andn2_b32 s70, 0xff, vcc_lo\n" \
"s_cbranch_scc1 Lpl1_%=\n" \
"Lpr1_%=:\n" \
"v_cvt_pk_bf16_f32 v16, v16, v17\n" \
"v_exp_f32 v19, v19\n" \
"v_exp_f32 v20, v20\n" \
"v_mfma_f32_16x16x32_bf16 v[44:47], a[240:243], v[128:131], v[196:199]\n" \
"v_exp_f32 v21, v21\n" \
"v_mfma_f32_16x16x32_bf16 v[44:47], a[244:247], v[136:139], v[44:47]\n" \
"s_add_u32 m0, s79, 32768\n" \
"v_exp_f32 v22, v22\n" \
"v_mfma_f32_16x16x32_bf16 v[44:47], a[248:251], v[144:147], v[44:47]\n" \
"s_add_u32 s71, s76, 0\n" \
"v_cvt_pk_bf16_f32 v17, v18, v19\n" \
"v_cvt_pk_bf16_f32 v18, v20, v21\n" \
"v_mfma_f32_16x16x32_bf16 v[44:47], a[252:255], v[152:155], v[44:47]\n" \
"buffer_load_dwordx4 v234, s[36:39], s71 offen offset:0 sc1 lds\n" \
"v_exp_f32 v23, v23\n" \
"v_exp_f32 v32, v32\n" \
"v_mfma_f32_16x16x32_bf16 v[28:31], a[252:255], v[120:123], v[220:223]\n" \
"v_exp_f32 v33, v33\n" \
"v_mfma_f32_16x16x32_bf16 v[28:31], a[248:251], v[112:115], v[28:31]\n" \
"ds_read_b128 a[252:255], v237 offset:30976\n" \
"v_cvt_pk_bf16_f32 v19, v22, v23\n" \
"v_mfma_f32_16x16x32_bf16 v[28:31], a[244:247], v[104:107], v[28:31]\n" \
"buffer_load_dwordx4 v234, s[36:39], s71 offen offset:1024 sc1 lds\n" \
"v_mfma_f32_16x16x32_bf16 v[28:31], a[240:243], v[96:99], v[28:31]\n" \
"ds_read_b128 a[244:247], v237 offset:26880\n" \
"ds_read_b128 a[248:251], v237 offset:28928\n" \
"v_exp_f32 v34, v34\n" \
"v_exp_f32 v35, v35\n" \
"v_mfma_f32_16x16x32_bf16 v[80:83], a[192:195], a[144:147], v[196:199]\n" \
"ds_read_b128 a[240:243], v237 offset:24832\n" \
"v_cvt_pk_bf16_f32 v32, v32, v33\n" \
"v_mfma_f32_16x16x32_bf16 v[80:83], a[196:199], a[148:151], v[80:83]\n" \
"buffer_load_dwordx4 v234, s[36:39], s71 offen offset:2048 sc1 lds\n" \
"v_mfma_f32_16x16x32_bf16 v[80:83], a[200:203], a[152:155], v[80:83]\n" \
"s_lshl_b32 s68, s63, 2\n" \
"s_add_u32 s68, s68, s95\n" \
"s_mov_b32 s69, 0\n" \
"v_mfma_f32_16x16x32_bf16 v[80:83], a[204:207], a[156:159], v[80:83]\n" \
"s_mov_b32 s89, s63\n" \
"ds_read_b128 a[44:47], v233 offset:15360\n" \
"v_mov_b32 v192, s60\n" \
"v_lshlrev_b32 v193, 28, s61\n" \
"v_exp_f32 v36, v36\n" \
"v_exp_f32 v37, v37\n" \
"v_mfma_f32_16x16x32_bf16 v[96:99], a[204:207], v[152:155], v[196:199]\n" \
"buffer_load_dwordx4 v234, s[36:39], s71 offen offset:3072 sc1 lds\n" \
"v_mfma_f32_16x16x32_bf16 v[96:99], a[200:203], v[144:147], v[96:99]\n" \
"s_add_u32 m0, s79, 49152\n" \
"s_mov_b32 s90, 0\n" \
"ds_read_b128 a[40:43], v233 offset:7168\n" \
"v_mfma_f32_16x16x32_bf16 v[96:99], a[196:199], v[136:139], v[96:99]\n" \
"s_add_u32 s71, s76, 17408\n" \
"v_exp_f32 v38, v38\n" \
"v_mfma_f32_16x16x32_bf16 v[96:99], a[192:195], v[128:131], v[96:99]\n" \
"buffer_load_dwordx4 v234, s[36:39], s71 offen offset:0 sc1 lds\n" \
"v_cvt_pk_bf16_f32 v33, v34, v35\n" \
"v_exp_f32 v39, v39\n" \
"v_mfma_f32_16x16x32_bf16 v[112:115], a[192:195], v[160:163], v[196:199]\n" \
"ds_read_b128 a[36:39], v233 offset:14336\n" \
"v_cvt_pk_bf16_f32 v34, v36, v37\n" \
"v_mfma_f32_16x16x32_bf16 v[112:115], a[196:199], v[168:171], v[112:115]\n" \
"v_exp_f32 v48, v48\n" \
"v_mfma_f32_16x16x32_bf16 v[112:115], a[200:203], v[176:179], v[112:115]\n" \
"buffer_load_dwordx4 v234, s[36:39], s71 offen offset:1024 sc1 lds\n" \
"v_mfma_f32_16x16x32_bf16 v[112:115], a[204:207], v[184:187], v[112:115]\n" \
"ds_read_b128 a[32:35], v233 offset:6144\n" \
"v_cvt_pk_bf16_f32 v35, v38, v39\n" \
"v_exp_f32 v49, v49\n" \
"v_exp_f32 v50, v50\n" \
"v_mfma_f32_16x16x32_bf16 v[64:67], a[204:207], a[140:143], v[208:211]\n" \
"v_exp_f32 v51, v51\n" \
"v_mfma_f32_16x16x32_bf16 v[64:67], a[200:203], a[136:139], v[64:67]\n" \
"buffer_load_dwordx4 v234, s[36:39], s71 offen offset:2048 sc1 lds\n" \
"v_mfma_f32_16x16x32_bf16 v[64:67], a[196:199], a[132:135], v[64:67]\n" \
"ds_read_b128 a[204:207], v233 offset:9216\n" \
"v_cvt_pk_bf16_f32 v48, v48, v49\n" \
"v_mfma_f32_16x16x32_bf16 v[64:67], a[192:195], a[128:131], v[64:67]\n" \
"v_exp_f32 v52, v52\n" \
"v_exp_f32 v53, v53\n" \
"v_exp_f32 v54, v54\n" \
"v_mfma_f32_16x16x32_bf16 v[68:71], a[208:211], a[128:131], v[212:215]\n" \
"buffer_load_dwordx4 v234, s[36:39], s71 offen offset:3072 sc1 lds\n" \
"v_mfma_f32_16x16x32_bf16 v[68:71], a[212:215], a[132:135], v[68:71]\n" \
"ds_read_b128 a[200:203], v233 offset:1024\n" \
"v_cvt_pk_bf16_f32 v49, v50, v51\n" \
"v_mfma_f32_16x16x32_bf16 v[68:71], a[216:219], a[136:139], v[68:71]\n" \
"s_add_u32 m0, s77, s80\n" \
"v_exp_f32 v55, v55\n" \
"v_cvt_pk_bf16_f32 v50, v52, v53\n" \
"v_mfma_f32_16x16x32_bf16 v[68:71], a[220:223], a[140:143], v[68:71]\n" \
"buffer_load_dwordx4 v234, s[40:43], s76 offen offset:0 sc1 lds\n" \
"v_cvt_pk_bf16_f32 v51, v54, v55\n" \
"v_mfma_f32_16x16x32_bf16 v[116:119], a[220:223], v[184:187], v[196:199]\n" \
"ds_read_b128 a[196:199], v233 offset:8192\n" \
"v_mfma_f32_16x16x32_bf16 v[116:119], a[216:219], v[176:179], v[116:119]\n" \
"ds_read_b128 a[160:163], v239 offset:0\n" \
"v_mfma_f32_16x16x32_bf16 v[116:119], a[212:215], v[168:171], v[116:119]\n" \
"s_add_u32 s71, s76, 17408\n" \
"ds_read_b128 a[192:195], v233 offset:0\n" \
"v_exp_f32 v24, v24\n" \
"v_mfma_f32_16x16x32_bf16 v[116:119], a[208:211], v[160:163], v[116:119]\n" \
"buffer_load_dwordx4 v234, s[40:43], s76 offen offset:1024 sc1 lds\n" \
"v_mfma_f32_16x16x32_bf16 v[100:103], a[208:211], v[128:131], v[196:199]\n" \
"ds_read_b128 a[164:167], v239 offset:1024\n" \
"v_mfma_f32_16x16x32_bf16 v[100:103], a[212:215], v[136:139], v[100:103]\n" \
"v_exp_f32 v25, v25\n" \
"v_mfma_f32_16x16x32_bf16 v[100:103], a[216:219], v[144:147], v[100:103]\n" \
"v_exp_f32 v26, v26\n" \
"v_mfma_f32_16x16x32_bf16 v[100:103], a[220:223], v[152:155], v[100:103]\n" \
"buffer_load_dwordx4 v234, s[40:43], s76 offen offset:2048 sc1 lds\n" \
"v_exp_f32 v27, v27\n" \
"v_exp_f32 v28, v28\n" \
"v_mfma_f32_16x16x32_bf16 v[84:87], a[220:223], a[156:159], v[196:199]\n" \
"ds_read_b128 a[168:171], v239 offset:2048\n" \
"v_cvt_pk_bf16_f32 v20, v24, v25\n" \
"v_mfma_f32_16x16x32_bf16 v[84:87], a[216:219], a[152:155], v[84:87]\n" \
"ds_read_b128 a[220:223], v233 offset:11264\n" \
"v_cvt_pk_bf16_f32 v21, v26, v27\n" \
"v_mfma_f32_16x16x32_bf16 v[84:87], a[212:215], a[148:151], v[84:87]\n" \
"s_waitcnt lgkmcnt(2)\n" \
"v_exp_f32 v29, v29\n" \
"v_mfma_f32_16x16x32_bf16 v[84:87], a[208:211], a[144:147], v[84:87]\n" \
"buffer_load_dwordx4 v234, s[40:43], s76 offen offset:3072 sc1 lds\n" \
"v_exp_f32 v30, v30\n" \
"v_exp_f32 v31, v31\n" \
"v_mfma_f32_16x16x32_bf16 v[88:91], a[224:227], a[144:147], v[196:199]\n" \
"ds_read_b128 a[216:219], v233 offset:3072\n" \
"v_cvt_pk_bf16_f32 v22, v28, v29\n" \
"v_mfma_f32_16x16x32_bf16 v[88:91], a[228:231], a[148:151], v[88:91]\n" \
"ds_read_b128 a[172:175], v239 offset:3072\n" \
"v_mfma_f32_16x16x32_bf16 v[88:91], a[232:235], a[152:155], v[88:91]\n" \
"s_add_u32 m0, m0, 0x4000\n" \
"ds_read_b128 a[212:215], v233 offset:10240\n" \
"v_xor_b32 v248, 0x2000, v239\n" \
"v_mfma_f32_16x16x32_bf16 v[88:91], a[236:239], a[156:159], v[88:91]\n" \
"buffer_load_dwordx4 v234, s[40:43], s71 offen offset:0 sc1 lds\n" \
"v_exp_f32 v40, v40\n" \
"v_exp_f32 v41, v41\n" \
"v_mfma_f32_16x16x32_bf16 v[104:107], a[236:239], v[152:155], v[196:199]\n" \
"ds_read_b128 a[208:211], v233 offset:2048\n" \
"v_lshlrev_b32 v252, s48, v234\n" \
"v_mfma_f32_16x16x32_bf16 v[104:107], a[232:235], v[144:147], v[104:107]\n" \
"ds_read_b128 a[176:179], v239 offset:4096\n" \
"v_mov_b32 v224, 0\n" \
"v_mfma_f32_16x16x32_bf16 v[104:107], a[228:231], v[136:139], v[104:107]\n" \
"v_mov_b32 v225, 0\n" \
"v_mov_b32 v228, 0\n" \
"v_mfma_f32_16x16x32_bf16 v[104:107], a[224:227], v[128:131], v[104:107]\n" \
"buffer_load_dwordx4 v234, s[40:43], s71 offen offset:1024 sc1 lds\n" \
"v_exp_f32 v42, v42\n" \
"v_cvt_pk_bf16_f32 v23, v30, v31\n" \
"v_mfma_f32_16x16x32_bf16 v[120:123], a[224:227], v[160:163], v[196:199]\n" \
"ds_read_b128 a[180:183], v239 offset:5120\n" \
"v_mov_b32 v229, 0\n" \
"v_mfma_f32_16x16x32_bf16 v[120:123], a[228:231], v[168:171], v[120:123]\n" \
"v_exp_f32 v43, v43\n" \
"v_mfma_f32_16x16x32_bf16 v[120:123], a[232:235], v[176:179], v[120:123]\n" \
"v_exp_f32 v44, v44\n" \
"v_cvt_pk_bf16_f32 v36, v40, v41\n" \
"v_mfma_f32_16x16x32_bf16 v[120:123], a[236:239], v[184:187], v[120:123]\n" \
"buffer_load_dwordx4 v234, s[40:43], s71 offen offset:2048 sc1 lds\n" \
"v_exp_f32 v45, v45\n" \
"v_mfma_f32_16x16x32_bf16 v[72:75], a[236:239], a[140:143], v[216:219]\n" \
"ds_read_b128 a[184:187], v239 offset:6144\n" \
"v_cvt_pk_bf16_f32 v37, v42, v43\n" \
"v_mfma_f32_16x16x32_bf16 v[72:75], a[232:235], a[136:139], v[72:75]\n" \
"ds_read_b128 a[236:239], v233 offset:13312\n" \
"v_mfma_f32_16x16x32_bf16 v[72:75], a[228:231], a[132:135], v[72:75]\n" \
"v_exp_f32 v46, v46\n" \
"v_mfma_f32_16x16x32_bf16 v[72:75], a[224:227], a[128:131], v[72:75]\n" \
"buffer_load_dwordx4 v234, s[40:43], s71 offen offset:3072 sc1 lds\n" \
"v_exp_f32 v47, v47\n" \
"v_mfma_f32_16x16x32_bf16 v[76:79], a[240:243], a[128:131], v[220:223]\n" \
"ds_read_b128 a[232:235], v233 offset:5120\n" \
"v_cvt_pk_bf16_f32 v38, v44, v45\n" \
"v_mfma_f32_16x16x32_bf16 v[76:79], a[244:247], a[132:135], v[76:79]\n" \
"ds_read_b128 a[188:191], v239 offset:7168\n" \
"v_exp_f32 v56, v56\n" \
"v_mfma_f32_16x16x32_bf16 v[76:79], a[248:251], a[136:139], v[76:79]\n" \
"ds_read_b128 a[228:231], v233 offset:12288\n" \
"v_cvt_pk_bf16_f32 v39, v46, v47\n" \
"v_mfma_f32_16x16x32_bf16 v[76:79], a[252:255], a[140:143], v[76:79]\n" \
"v_exp_f32 v57, v57\n" \
"v_exp_f32 v58, v58\n" \
"v_mfma_f32_16x16x32_bf16 v[124:127], a[252:255], v[184:187], v[196:199]\n" \
"ds_read_b128 a[224:227], v233 offset:4096\n" \
"v_cvt_pk_bf16_f32 v52, v56, v57\n" \
"v_mfma_f32_16x16x32_bf16 v[124:127], a[248:251], v[176:179], v[124:127]\n" \
"v_exp_f32 v59, v59\n" \
"v_mfma_f32_16x16x32_bf16 v[124:127], a[244:247], v[168:171], v[124:127]\n" \
"v_exp_f32 v60, v60\n" \
"v_mfma_f32_16x16x32_bf16 v[124:127], a[240:243], v[160:163], v[124:127]\n" \
"v_exp_f32 v61, v61\n" \
"v_exp_f32 v62, v62\n" \
"v_cvt_pk_bf16_f32 v53, v58, v59\n" \
"v_mfma_f32_16x16x32_bf16 v[108:111], a[240:243], v[128:131], v[196:199]\n" \
"v_exp_f32 v63, v63\n" \
"v_mfma_f32_16x16x32_bf16 v[108:111], a[244:247], v[136:139], v[108:111]\n" \
"v_cvt_pk_bf16_f32 v54, v60, v61\n" \
"v_mfma_f32_16x16x32_bf16 v[108:111], a[248:251], v[144:147], v[108:111]\n" \
"v_cvt_pk_bf16_f32 v55, v62, v63\n" \
"v_mfma_f32_16x16x32_bf16 v[108:111], a[252:255], v[152:155], v[108:111]\n" \
"v_exp_f32 v80, v80\n" \
"v_mfma_f32_16x16x32_bf16 v[92:95], a[252:255], a[156:159], v[196:199]\n" \
"v_add_u32 v232, s91, v237\n" \
"v_mfma_f32_16x16x32_bf16 v[92:95], a[248:251], a[152:155], v[92:95]\n" \
"v_exp_f32 v81, v81\n" \
"v_mfma_f32_16x16x32_bf16 v[92:95], a[244:247], a[148:151], v[92:95]\n" \
"s_waitcnt vmcnt(8) lgkmcnt(0)\n" \
"s_barrier\n" \
"v_mfma_f32_16x16x32_bf16 v[92:95], a[240:243], a[144:147], v[92:95]\n" \
"buffer_store_dword v192, v193, s[56:59], s68 offen sc1\n" \
"v_exp_f32 v82, v82\n" \
"v_mfma_f32_16x16x32_bf16 v[160:163], a[192:195], v[16:19], 0\n" \
"s_add_u32 m0, s74, s79\n" \
"ds_read_b128 a[240:243], v232 offset:8448\n" \
"v_xor_b32 v233, 0x4000, v233\n" \
"v_mfma_f32_16x16x32_bf16 v[160:163], a[196:199], v[20:23], v[160:163]\n" \
"buffer_load_dwordx4 v234, s[36:39], s73 offen offset:0 sc1 lds\n" \
"v_mfma_f32_16x16x32_bf16 a[64:67], a[192:195], v[32:35], 0\n" \
"ds_read_b128 a[244:247], v232 offset:10496\n" \
"v_cvt_pk_bf16_f32 v80, v80, v81\n" \
"v_mfma_f32_16x16x32_bf16 a[64:67], a[196:199], v[36:39], a[64:67]\n" \
"ds_read_b128 a[0:3], v248 offset:0\n" \
"v_exp_f32 v83, v83\n" \
"v_mfma_f32_16x16x32_bf16 a[96:99], a[192:195], v[48:51], 0\n" \
"v_exp_f32 v84, v84\n" \
"v_mfma_f32_16x16x32_bf16 a[96:99], a[196:199], v[52:55], a[96:99]\n" \
"s_add_u32 s71, s73, 17408\n" \
"ds_read_b128 a[192:195], v232 offset:0\n" \
"v_cvt_pk_bf16_f32 v81, v82, v83\n" \
"v_mfma_f32_16x16x32_bf16 a[100:103], a[200:203], v[48:51], 0\n" \
"buffer_load_dwordx4 v234, s[36:39], s73 offen offset:1024 sc1 lds\n" \
"v_mfma_f32_16x16x32_bf16 a[100:103], a[204:207], v[52:55], a[100:103]\n" \
"ds_read_b128 a[196:199], v232 offset:2048\n" \
"v_exp_f32 v85, v85\n" \
"v_mfma_f32_16x16x32_bf16 a[68:71], a[200:203], v[32:35], 0\n" \
"ds_read_b128 a[4:7], v248 offset:1024\n" \
"v_mfma_f32_16x16x32_bf16 a[68:71], a[204:207], v[36:39], a[68:71]\n" \
"v_exp_f32 v86, v86\n" \
"v_mfma_f32_16x16x32_bf16 v[164:167], a[200:203], v[16:19], 0\n" \
"ds_read_b128 a[8:11], v248 offset:2048\n" \
"v_cvt_pk_bf16_f32 v82, v84, v85\n" \
"v_mfma_f32_16x16x32_bf16 v[164:167], a[204:207], v[20:23], v[164:167]\n" \
"buffer_load_dwordx4 v234, s[36:39], s73 offen offset:2048 sc1 lds\n" \
"v_exp_f32 v87, v87\n" \
"v_mfma_f32_16x16x32_bf16 v[168:171], a[208:211], v[16:19], 0\n" \
"ds_read_b128 a[200:203], v232 offset:4096\n" \
"v_mfma_f32_16x16x32_bf16 v[168:171], a[212:215], v[20:23], v[168:171]\n" \
"ds_read_b128 a[204:207], v232 offset:6144\n" \
"v_exp_f32 v96, v96\n" \
"v_mfma_f32_16x16x32_bf16 a[72:75], a[208:211], v[32:35], 0\n" \
"ds_read_b128 a[12:15], v248 offset:3072\n" \
"v_cvt_pk_bf16_f32 v83, v86, v87\n" \
"v_mfma_f32_16x16x32_bf16 a[72:75], a[212:215], v[36:39], a[72:75]\n" \
"v_exp_f32 v97, v97\n" \
"v_mfma_f32_16x16x32_bf16 a[104:107], a[208:211], v[48:51], 0\n" \
"buffer_load_dwordx4 v234, s[36:39], s73 offen offset:3072 sc1 lds\n" \
"v_mfma_f32_16x16x32_bf16 a[104:107], a[212:215], v[52:55], a[104:107]\n" \
"ds_read_b128 a[208:211], v232 offset:256\n" \
"v_exp_f32 v98, v98\n" \
"v_smfmac_f32_16x16x64_bf16 a[52:55], v[244:247], v[16:23], v243\n" \
"ds_read_b128 a[212:215], v232 offset:2304\n" \
"v_cvt_pk_bf16_f32 v96, v96, v97\n" \
"v_mfma_f32_16x16x32_bf16 a[108:111], a[216:219], v[48:51], 0\n" \
"ds_read_b128 a[16:19], v248 offset:4096\n" \
"v_mfma_f32_16x16x32_bf16 a[108:111], a[220:223], v[52:55], a[108:111]\n" \
"s_add_u32 m0, m0, 0x4000\n" \
"v_exp_f32 v99, v99\n" \
"v_mfma_f32_16x16x32_bf16 a[76:79], a[216:219], v[32:35], 0\n" \
"buffer_load_dwordx4 v234, s[36:39], s71 offen offset:0 sc1 lds\n" \
"v_mfma_f32_16x16x32_bf16 a[76:79], a[220:223], v[36:39], a[76:79]\n" \
"ds_read_b128 a[20:23], v248 offset:5120\n" \
"v_exp_f32 v100, v100\n" \
"v_mfma_f32_16x16x32_bf16 v[172:175], a[216:219], v[16:19], 0\n" \
"v_exp_f32 v101, v101\n" \
"v_mfma_f32_16x16x32_bf16 v[172:175], a[220:223], v[20:23], v[172:175]\n" \
"ds_read_b128 a[216:219], v232 offset:4352\n" \
"v_cvt_pk_bf16_f32 v97, v98, v99\n" \
"v_mfma_f32_32x32x16_bf16 v[128:143], a[48:51], a[48:51], 0\n" \
"ds_read_b128 a[220:223], v232 offset:6400\n" \
"v_exp_f32 v102, v102\n" \
"v_mfma_f32_16x16x32_bf16 v[176:179], a[224:227], v[16:19], 0\n" \
"buffer_load_dwordx4 v234, s[36:39], s71 offen offset:1024 sc1 lds\n" \
"v_mfma_f32_16x16x32_bf16 v[176:179], a[228:231], v[20:23], v[176:179]\n" \
"ds_read_b128 a[24:27], v248 offset:6144\n" \
"v_cvt_pk_bf16_f32 v98, v100, v101\n" \
"v_mfma_f32_16x16x32_bf16 a[80:83], a[224:227], v[32:35], 0\n" \
"v_exp_f32 v103, v103\n" \
"v_mfma_f32_16x16x32_bf16 a[80:83], a[228:231], v[36:39], a[80:83]\n" \
"ds_read_b128 a[28:31], v248 offset:7168\n" \
"v_exp_f32 v112, v112\n" \
"v_mfma_f32_16x16x32_bf16 a[112:115], a[224:227], v[48:51], 0\n" \
"v_exp_f32 v113, v113\n" \
"v_mfma_f32_16x16x32_bf16 a[112:115], a[228:231], v[52:55], a[112:115]\n" \
"buffer_load_dwordx4 v234, s[36:39], s71 offen offset:2048 sc1 lds\n" \
"v_exp_f32 v114, v114\n" \
"v_smfmac_f32_16x16x64_bf16 v[224:227], v[244:247], v[32:39], v243\n" \
"ds_read_b128 a[224:227], v232 offset:8192\n" \
"v_cvt_pk_bf16_f32 v99, v102, v103\n" \
"v_mfma_f32_16x16x32_bf16 a[116:119], a[232:235], v[48:51], 0\n" \
"ds_read_b128 a[228:231], v232 offset:10240\n" \
"v_cvt_pk_bf16_f32 v112, v112, v113\n" \
"v_mfma_f32_16x16x32_bf16 a[116:119], a[236:239], v[52:55], a[116:119]\n" \
"v_exp_f32 v115, v115\n" \
"v_mfma_f32_16x16x32_bf16 a[84:87], a[232:235], v[32:35], 0\n" \
"v_exp_f32 v116, v116\n" \
"v_mfma_f32_16x16x32_bf16 a[84:87], a[236:239], v[36:39], a[84:87]\n" \
"buffer_load_dwordx4 v234, s[36:39], s71 offen offset:3072 sc1 lds\n" \
"v_exp_f32 v117, v117\n" \
"v_mfma_f32_16x16x32_bf16 v[180:183], a[232:235], v[16:19], 0\n" \
"v_exp_f32 v118, v118\n" \
"v_mfma_f32_16x16x32_bf16 v[180:183], a[236:239], v[20:23], v[180:183]\n" \
"s_mov_b32 s76, s73\n" \
"s_add_u32 s73, s73, 34816\n" \
"ds_read_b128 a[232:235], v232 offset:12288\n" \
"v_mfma_f32_32x32x16_bf16 v[144:159], a[48:51], a[48:51], 0\n" \
"s_cmp_eq_u32 s73, s83\n" \
"s_cselect_b32 s70, 34816, 0\n" \
"ds_read_b128 a[236:239], v232 offset:14336\n" \
"v_cvt_pk_bf16_f32 v113, v114, v115\n" \
"v_mfma_f32_16x16x32_bf16 v[184:187], a[32:35], v[16:19], 0\n" \
"s_add_u32 s73, s73, s70\n" \
"s_xor_b32 s74, s74, 0x8000\n" \
"ds_read_b128 a[248:251], v232 offset:12544\n" \
"v_mfma_f32_16x16x32_bf16 v[184:187], a[36:39], v[20:23], v[184:187]\n" \
"s_xor_b32 s77, s77, 0x8000\n" \
"ds_read_b128 a[252:255], v232 offset:14592\n" \
"v_cvt_pk_bf16_f32 v114, v116, v117\n" \
"v_mfma_f32_16x16x32_bf16 a[88:91], a[32:35], v[32:35], 0\n" \
"v_exp_f32 v119, v119\n" \
"v_mfma_f32_16x16x32_bf16 a[88:91], a[36:39], v[36:39], a[88:91]\n" \
"v_exp_f32 v64, v64\n" \
"v_mfma_f32_16x16x32_bf16 a[120:123], a[32:35], v[48:51], 0\n" \
"v_exp_f32 v65, v65\n" \
"v_mfma_f32_16x16x32_bf16 a[120:123], a[36:39], v[52:55], a[120:123]\n" \
"v_exp_f32 v66, v66\n" \
"v_cvt_pk_bf16_f32 v115, v118, v119\n" \
"v_smfmac_f32_16x16x64_bf16 v[228:231], v[244:247], v[48:55], v243\n" \
"v_cvt_pk_bf16_f32 v64, v64, v65\n" \
"v_mfma_f32_16x16x32_bf16 a[124:127], a[40:43], v[48:51], 0\n" \
"v_exp_f32 v67, v67\n" \
"v_mfma_f32_16x16x32_bf16 a[124:127], a[44:47], v[52:55], a[124:127]\n" \
"v_exp_f32 v68, v68\n" \
"v_exp_f32 v69, v69\n" \
"v_mfma_f32_16x16x32_bf16 a[92:95], a[40:43], v[32:35], 0\n" \
"v_exp_f32 v70, v70\n" \
"v_mfma_f32_16x16x32_bf16 a[92:95], a[44:47], v[36:39], a[92:95]\n" \
"v_cvt_pk_bf16_f32 v65, v66, v67\n" \
"v_cvt_pk_bf16_f32 v66, v68, v69\n" \
"v_mfma_f32_16x16x32_bf16 v[188:191], a[40:43], v[16:19], 0\n" \
"s_cmp_eq_u32 s63, 0\n" \
"v_exp_f32 v71, v71\n" \
"v_mfma_f32_16x16x32_bf16 v[188:191], a[44:47], v[20:23], v[188:191]\n" \
"s_waitcnt lgkmcnt(0)\n" \
"v_cvt_pk_bf16_f32 v67, v70, v71\n" \
"s_cbranch_scc1 Lloop2_%=\n" \
"Lloop_%=:\n" \
"v_mfma_f32_16x16x32_bf16 v[0:3], a[192:195], a[128:131], v[196:199]\n" \
"v_exp_f32 v72, v72\n" \
"v_mfma_f32_16x16x32_bf16 v[0:3], a[196:199], a[132:135], v[0:3]\n" \
"v_mfma_f32_16x16x32_bf16 v[0:3], a[200:203], a[136:139], v[0:3]\n" \
"v_exp_f32 v73, v73\n" \
"v_mfma_f32_16x16x32_bf16 v[0:3], a[204:207], a[140:143], v[0:3]\n" \
"ds_read_b128 a[32:35], v233 offset:6144\n" \
"v_cvt_pk_bf16_f32 v68, v72, v73\n" \
"v_mfma_f32_16x16x32_bf16 v[16:19], a[204:207], a[156:159], v[196:199]\n" \
"v_exp_f32 v74, v74\n" \
"v_mfma_f32_16x16x32_bf16 v[16:19], a[200:203], a[152:155], v[16:19]\n" \
"global_load_dword v253, v252, s[44:45]\n" \
"v_mfma_f32_16x16x32_bf16 v[16:19], a[196:199], a[148:151], v[16:19]\n" \
"s_sub_u32 s46, s46, 1\n" \
"v_exp_f32 v75, v75\n" \
"v_mfma_f32_16x16x32_bf16 v[16:19], a[192:195], a[144:147], v[16:19]\n" \
"ds_read_b128 a[36:39], v233 offset:14336\n" \
"v_cvt_pk_bf16_f32 v69, v74, v75\n" \
"v_mfma_f32_16x16x32_bf16 v[32:35], a[192:195], a[160:163], v[196:199]\n" \
"v_exp_f32 v76, v76\n" \
"v_mfma_f32_16x16x32_bf16 v[32:35], a[196:199], a[164:167], v[32:35]\n" \
"s_cmp_gt_i32 s46, 0\n" \
"s_cselect_b32 s47, 0x100000, 0\n" \
"v_mfma_f32_16x16x32_bf16 v[32:35], a[200:203], a[168:171], v[32:35]\n" \
"v_exp_f32 v77, v77\n" \
"v_mfma_f32_16x16x32_bf16 v[32:35], a[204:207], a[172:175], v[32:35]\n" \
"s_add_u32 s44, s44, s47\n" \
"s_addc_u32 s45, s45, 0\n" \
"ds_read_b128 a[40:43], v233 offset:7168\n" \
"v_cvt_pk_bf16_f32 v70, v76, v77\n" \
"v_mfma_f32_16x16x32_bf16 v[48:51], a[204:207], a[188:191], v[196:199]\n" \
"v_exp_f32 v78, v78\n" \
"v_mfma_f32_16x16x32_bf16 v[48:51], a[200:203], a[184:187], v[48:51]\n" \
"v_xor_b32 v232, 0x4000, v232\n" \
"v_mfma_f32_16x16x32_bf16 v[48:51], a[196:199], a[180:183], v[48:51]\n" \
"v_exp_f32 v79, v79\n" \
"v_mfma_f32_16x16x32_bf16 v[48:51], a[192:195], a[176:179], v[48:51]\n" \
"ds_read_b128 a[204:207], v233 offset:9216\n" \
"v_cvt_pk_bf16_f32 v71, v78, v79\n" \
"v_mfma_f32_16x16x32_bf16 v[52:55], a[208:211], a[176:179], v[196:199]\n" \
"v_exp_f32 v88, v88\n" \
"v_mfma_f32_16x16x32_bf16 v[52:55], a[212:215], a[180:183], v[52:55]\n" \
"v_mfma_f32_16x16x32_bf16 v[52:55], a[216:219], a[184:187], v[52:55]\n" \
"v_exp_f32 v89, v89\n" \
"v_mfma_f32_16x16x32_bf16 v[52:55], a[220:223], a[188:191], v[52:55]\n" \
"ds_read_b128 a[200:203], v233 offset:1024\n" \
"v_cvt_pk_bf16_f32 v84, v88, v89\n" \
"v_mfma_f32_16x16x32_bf16 v[36:39], a[220:223], a[172:175], v[196:199]\n" \
"v_exp_f32 v90, v90\n" \
"v_mfma_f32_16x16x32_bf16 v[36:39], a[216:219], a[168:171], v[36:39]\n" \
"v_mfma_f32_16x16x32_bf16 v[36:39], a[212:215], a[164:167], v[36:39]\n" \
"v_exp_f32 v91, v91\n" \
"v_mfma_f32_16x16x32_bf16 v[36:39], a[208:211], a[160:163], v[36:39]\n" \
"ds_read_b128 a[196:199], v233 offset:8192\n" \
"v_cvt_pk_bf16_f32 v85, v90, v91\n" \
"v_mfma_f32_16x16x32_bf16 v[20:23], a[208:211], a[144:147], v[196:199]\n" \
"v_exp_f32 v92, v92\n" \
"v_mfma_f32_16x16x32_bf16 v[20:23], a[212:215], a[148:151], v[20:23]\n" \
"v_mfma_f32_16x16x32_bf16 v[20:23], a[216:219], a[152:155], v[20:23]\n" \
"v_exp_f32 v93, v93\n" \
"v_mfma_f32_16x16x32_bf16 v[20:23], a[220:223], a[156:159], v[20:23]\n" \
"ds_read_b128 a[192:195], v233 offset:0\n" \
"v_cvt_pk_bf16_f32 v86, v92, v93\n" \
"v_mfma_f32_16x16x32_bf16 v[4:7], a[220:223], a[140:143], v[196:199]\n" \
"v_exp_f32 v94, v94\n" \
"v_mfma_f32_16x16x32_bf16 v[4:7], a[216:219], a[136:139], v[4:7]\n" \
"v_mfma_f32_16x16x32_bf16 v[4:7], a[212:215], a[132:135], v[4:7]\n" \
"s_waitcnt lgkmcnt(7)\n" \
"v_exp_f32 v95, v95\n" \
"v_mfma_f32_16x16x32_bf16 v[4:7], a[208:211], a[128:131], v[4:7]\n" \
"ds_read_b128 a[220:223], v233 offset:11264\n" \
"v_cvt_pk_bf16_f32 v87, v94, v95\n" \
"v_mfma_f32_16x16x32_bf16 v[8:11], a[224:227], a[128:131], v[196:199]\n" \
"v_exp_f32 v104, v104\n" \
"v_mfma_f32_16x16x32_bf16 v[8:11], a[228:231], a[132:135], v[8:11]\n" \
"v_mfma_f32_16x16x32_bf16 v[8:11], a[232:235], a[136:139], v[8:11]\n" \
"v_exp_f32 v105, v105\n" \
"v_mfma_f32_16x16x32_bf16 v[8:11], a[236:239], a[140:143], v[8:11]\n" \
"ds_read_b128 a[216:219], v233 offset:3072\n" \
"v_cvt_pk_bf16_f32 v100, v104, v105\n" \
"v_mfma_f32_16x16x32_bf16 v[24:27], a[236:239], a[156:159], v[196:199]\n" \
"v_exp_f32 v106, v106\n" \
"v_mfma_f32_16x16x32_bf16 v[24:27], a[232:235], a[152:155], v[24:27]\n" \
"v_mfma_f32_16x16x32_bf16 v[24:27], a[228:231], a[148:151], v[24:27]\n" \
"v_exp_f32 v107, v107\n" \
"v_mfma_f32_16x16x32_bf16 v[24:27], a[224:227], a[144:147], v[24:27]\n" \
"ds_read_b128 a[212:215], v233 offset:10240\n" \
"v_cvt_pk_bf16_f32 v101, v106, v107\n" \
"v_mfma_f32_16x16x32_bf16 v[40:43], a[224:227], a[160:163], v[196:199]\n" \
"v_exp_f32 v108, v108\n" \
"v_mfma_f32_16x16x32_bf16 v[40:43], a[228:231], a[164:167], v[40:43]\n" \
"v_mfma_f32_16x16x32_bf16 v[40:43], a[232:235], a[168:171], v[40:43]\n" \
"v_exp_f32 v109, v109\n" \
"v_mfma_f32_16x16x32_bf16 v[40:43], a[236:239], a[172:175], v[40:43]\n" \
"ds_read_b128 a[208:211], v233 offset:2048\n" \
"v_cvt_pk_bf16_f32 v102, v108, v109\n" \
"v_mfma_f32_16x16x32_bf16 v[56:59], a[236:239], a[188:191], v[196:199]\n" \
"v_exp_f32 v110, v110\n" \
"v_mfma_f32_16x16x32_bf16 v[56:59], a[232:235], a[184:187], v[56:59]\n" \
"v_mfma_f32_16x16x32_bf16 v[56:59], a[228:231], a[180:183], v[56:59]\n" \
"v_exp_f32 v111, v111\n" \
"v_mfma_f32_16x16x32_bf16 v[56:59], a[224:227], a[176:179], v[56:59]\n" \
"ds_read_b128 a[236:239], v233 offset:13312\n" \
"v_cvt_pk_bf16_f32 v103, v110, v111\n" \
"v_mfma_f32_16x16x32_bf16 v[60:63], a[240:243], a[176:179], v[196:199]\n" \
"v_exp_f32 v120, v120\n" \
"v_mfma_f32_16x16x32_bf16 v[60:63], a[244:247], a[180:183], v[60:63]\n" \
"v_mfma_f32_16x16x32_bf16 v[60:63], a[248:251], a[184:187], v[60:63]\n" \
"v_exp_f32 v121, v121\n" \
"v_mfma_f32_16x16x32_bf16 v[60:63], a[252:255], a[188:191], v[60:63]\n" \
"ds_read_b128 a[232:235], v233 offset:5120\n" \
"v_cvt_pk_bf16_f32 v116, v120, v121\n" \
"v_mfma_f32_16x16x32_bf16 v[44:47], a[252:255], a[172:175], v[196:199]\n" \
"v_exp_f32 v122, v122\n" \
"v_mfma_f32_16x16x32_bf16 v[44:47], a[248:251], a[168:171], v[44:47]\n" \
"v_mfma_f32_16x16x32_bf16 v[44:47], a[244:247], a[164:167], v[44:47]\n" \
"v_exp_f32 v123, v123\n" \
"v_mfma_f32_16x16x32_bf16 v[44:47], a[240:243], a[160:163], v[44:47]\n" \
"ds_read_b128 a[228:231], v233 offset:12288\n" \
"v_cvt_pk_bf16_f32 v117, v122, v123\n" \
"v_mfma_f32_16x16x32_bf16 v[28:31], a[240:243], a[144:147], v[196:199]\n" \
"v_exp_f32 v124, v124\n" \
"v_mfma_f32_16x16x32_bf16 v[28:31], a[244:247], a[148:151], v[28:31]\n" \
"v_mfma_f32_16x16x32_bf16 v[28:31], a[248:251], a[152:155], v[28:31]\n" \
"v_exp_f32 v125, v125\n" \
"v_mfma_f32_16x16x32_bf16 v[28:31], a[252:255], a[156:159], v[28:31]\n" \
"ds_read_b128 a[224:227], v233 offset:4096\n" \
"v_cvt_pk_bf16_f32 v118, v124, v125\n" \
"v_mfma_f32_16x16x32_bf16 v[12:15], a[252:255], a[140:143], v[196:199]\n" \
"v_exp_f32 v126, v126\n" \
"Lpg5601_%=:\n" \
"s_cbranch_execz Lpg6793_%=\n" \
"v_mfma_f32_16x16x32_bf16 v[12:15], a[248:251], a[136:139], v[12:15]\n" \
"v_mfma_f32_16x16x32_bf16 v[12:15], a[244:247], a[132:135], v[12:15]\n" \
"s_waitcnt lgkmcnt(2)\n" \
"v_exp_f32 v127, v127\n" \
"v_mfma_f32_16x16x32_bf16 v[12:15], a[240:243], a[128:131], v[12:15]\n" \
"ds_read_b128 a[44:47], v233 offset:15360\n" \
"v_cvt_pk_bf16_f32 v119, v126, v127\n" \
"v_mfma_f32_16x16x32_bf16 v[128:131], a[192:195], v[64:67], v[128:131]\n" \
"v_exp_f32 v0, v0\n" \
"v_mfma_f32_16x16x32_bf16 v[128:131], a[196:199], v[68:71], v[128:131]\n" \
"ds_read_b128 a[244:247], v232 offset:10496\n" \
"v_mfma_f32_16x16x32_bf16 v[160:163], a[192:195], v[80:83], v[160:163]\n" \
"v_exp_f32 v1, v1\n" \
"v_mfma_f32_16x16x32_bf16 v[160:163], a[196:199], v[84:87], v[160:163]\n" \
"ds_read_b128 a[240:243], v232 offset:8448\n" \
"v_cvt_pk_bf16_f32 v0, v0, v1\n" \
"v_mfma_f32_16x16x32_bf16 a[64:67], a[192:195], v[96:99], a[64:67]\n" \
"v_exp_f32 v2, v2\n" \
"v_mfma_f32_16x16x32_bf16 a[64:67], a[196:199], v[100:103], a[64:67]\n" \
"ds_read_b128 a[248:251], v232 offset:12544\n" \
"v_xor_b32 v233, 0xc000, v233\n" \
"v_mfma_f32_16x16x32_bf16 a[96:99], a[192:195], v[112:115], a[96:99]\n" \
"v_exp_f32 v3, v3\n" \
"v_mfma_f32_16x16x32_bf16 a[96:99], a[196:199], v[116:119], a[96:99]\n" \
"ds_read_b128 a[192:195], v232 offset:0\n" \
"v_cvt_pk_bf16_f32 v1, v2, v3\n" \
"v_mfma_f32_16x16x32_bf16 a[100:103], a[200:203], v[112:115], a[100:103]\n" \
"v_exp_f32 v4, v4\n" \
"v_mfma_f32_16x16x32_bf16 a[100:103], a[204:207], v[116:119], a[100:103]\n" \
"ds_read_b128 a[252:255], v232 offset:14592\n" \
"v_mfma_f32_16x16x32_bf16 a[68:71], a[200:203], v[96:99], a[68:71]\n" \
"v_exp_f32 v5, v5\n" \
"v_mfma_f32_16x16x32_bf16 a[68:71], a[204:207], v[100:103], a[68:71]\n" \
"ds_read_b128 a[196:199], v232 offset:2048\n" \
"v_cvt_pk_bf16_f32 v2, v4, v5\n" \
"v_smfmac_f32_16x16x64_bf16 a[48:51], v[244:247], v[64:71], v243\n" \
"v_mfma_f32_16x16x32_bf16 v[164:167], a[200:203], v[80:83], v[164:167]\n" \
"v_exp_f32 v6, v6\n" \
"v_mfma_f32_16x16x32_bf16 v[164:167], a[204:207], v[84:87], v[164:167]\n" \
"v_mfma_f32_16x16x32_bf16 v[132:135], a[200:203], v[64:67], v[132:135]\n" \
"v_exp_f32 v7, v7\n" \
"v_mfma_f32_16x16x32_bf16 v[132:135], a[204:207], v[68:71], v[132:135]\n" \
"ds_read_b128 a[200:203], v232 offset:4096\n" \
"v_cvt_pk_bf16_f32 v3, v6, v7\n" \
"v_mfma_f32_16x16x32_bf16 v[136:139], a[208:211], v[64:67], v[136:139]\n" \
"v_exp_f32 v16, v16\n" \
"v_mfma_f32_16x16x32_bf16 v[136:139], a[212:215], v[68:71], v[136:139]\n" \
"v_mfma_f32_16x16x32_bf16 v[168:171], a[208:211], v[80:83], v[168:171]\n" \
"v_exp_f32 v17, v17\n" \
"v_mfma_f32_16x16x32_bf16 v[168:171], a[212:215], v[84:87], v[168:171]\n" \
"ds_read_b128 a[204:207], v232 offset:6144\n" \
"v_cvt_pk_bf16_f32 v16, v16, v17\n" \
"v_mfma_f32_16x16x32_bf16 a[72:75], a[208:211], v[96:99], a[72:75]\n" \
"v_exp_f32 v18, v18\n" \
"v_mfma_f32_16x16x32_bf16 a[72:75], a[212:215], v[100:103], a[72:75]\n" \
"v_mfma_f32_16x16x32_bf16 a[104:107], a[208:211], v[112:115], a[104:107]\n" \
"v_exp_f32 v19, v19\n" \
"v_mfma_f32_16x16x32_bf16 a[104:107], a[212:215], v[116:119], a[104:107]\n" \
"ds_read_b128 a[208:211], v232 offset:256\n" \
"v_cvt_pk_bf16_f32 v17, v18, v19\n" \
"v_mfma_f32_16x16x32_bf16 a[108:111], a[216:219], v[112:115], a[108:111]\n" \
"v_exp_f32 v20, v20\n" \
"v_mfma_f32_16x16x32_bf16 a[108:111], a[220:223], v[116:119], a[108:111]\n" \
"v_mfma_f32_16x16x32_bf16 a[76:79], a[216:219], v[96:99], a[76:79]\n" \
"v_exp_f32 v21, v21\n" \
"v_mfma_f32_16x16x32_bf16 a[76:79], a[220:223], v[100:103], a[76:79]\n" \
"ds_read_b128 a[212:215], v232 offset:2304\n" \
"v_cvt_pk_bf16_f32 v18, v20, v21\n" \
"v_smfmac_f32_16x16x64_bf16 a[52:55], v[244:247], v[80:87], v243\n" \
"v_mfma_f32_16x16x32_bf16 v[172:175], a[216:219], v[80:83], v[172:175]\n" \
"v_exp_f32 v22, v22\n" \
"v_mfma_f32_16x16x32_bf16 v[172:175], a[220:223], v[84:87], v[172:175]\n" \
"v_mfma_f32_16x16x32_bf16 v[140:143], a[216:219], v[64:67], v[140:143]\n" \
"s_waitcnt lgkmcnt(10)\n" \
"v_exp_f32 v23, v23\n" \
"v_mfma_f32_16x16x32_bf16 v[140:143], a[220:223], v[68:71], v[140:143]\n" \
"ds_read_b128 a[216:219], v232 offset:4352\n" \
"v_cvt_pk_bf16_f32 v19, v22, v23\n" \
"v_mfma_f32_16x16x32_bf16 v[144:147], a[224:227], v[64:67], v[144:147]\n" \
"v_exp_f32 v32, v32\n" \
"v_mfma_f32_16x16x32_bf16 v[144:147], a[228:231], v[68:71], v[144:147]\n" \
"v_mfma_f32_16x16x32_bf16 v[176:179], a[224:227], v[80:83], v[176:179]\n" \
"v_exp_f32 v33, v33\n" \
"v_mfma_f32_16x16x32_bf16 v[176:179], a[228:231], v[84:87], v[176:179]\n" \
"ds_read_b128 a[220:223], v232 offset:6400\n" \
"v_cvt_pk_bf16_f32 v32, v32, v33\n" \
"v_mfma_f32_16x16x32_bf16 a[80:83], a[224:227], v[96:99], a[80:83]\n" \
"v_exp_f32 v34, v34\n" \
"v_mfma_f32_16x16x32_bf16 a[80:83], a[228:231], v[100:103], a[80:83]\n" \
"v_mfma_f32_16x16x32_bf16 a[112:115], a[224:227], v[112:115], a[112:115]\n" \
"v_exp_f32 v35, v35\n" \
"v_mfma_f32_16x16x32_bf16 a[112:115], a[228:231], v[116:119], a[112:115]\n" \
"ds_read_b128 a[224:227], v232 offset:8192\n" \
"v_cvt_pk_bf16_f32 v33, v34, v35\n" \
"v_mfma_f32_16x16x32_bf16 a[116:119], a[232:235], v[112:115], a[116:119]\n" \
"v_exp_f32 v36, v36\n" \
"v_mfma_f32_16x16x32_bf16 a[116:119], a[236:239], v[116:119], a[116:119]\n" \
"v_mfma_f32_16x16x32_bf16 a[84:87], a[232:235], v[96:99], a[84:87]\n" \
"v_exp_f32 v37, v37\n" \
"v_mfma_f32_16x16x32_bf16 a[84:87], a[236:239], v[100:103], a[84:87]\n" \
"ds_read_b128 a[228:231], v232 offset:10240\n" \
"v_cvt_pk_bf16_f32 v34, v36, v37\n" \
"v_smfmac_f32_16x16x64_bf16 v[224:227], v[244:247], v[96:103], v243\n" \
"v_mfma_f32_16x16x32_bf16 v[180:183], a[232:235], v[80:83], v[180:183]\n" \
"v_exp_f32 v38, v38\n" \
"v_mfma_f32_16x16x32_bf16 v[180:183], a[236:239], v[84:87], v[180:183]\n" \
"v_mfma_f32_16x16x32_bf16 v[148:151], a[232:235], v[64:67], v[148:151]\n" \
"v_exp_f32 v39, v39\n" \
"v_mfma_f32_16x16x32_bf16 v[148:151], a[236:239], v[68:71], v[148:151]\n" \
"ds_read_b128 a[232:235], v232 offset:12288\n" \
"v_cvt_pk_bf16_f32 v35, v38, v39\n" \
"v_mfma_f32_16x16x32_bf16 v[152:155], a[32:35], v[64:67], v[152:155]\n" \
"v_exp_f32 v48, v48\n" \
"v_mfma_f32_16x16x32_bf16 v[152:155], a[36:39], v[68:71], v[152:155]\n" \
"v_mfma_f32_16x16x32_bf16 v[184:187], a[32:35], v[80:83], v[184:187]\n" \
"v_exp_f32 v49, v49\n" \
"v_mfma_f32_16x16x32_bf16 v[184:187], a[36:39], v[84:87], v[184:187]\n" \
"ds_read_b128 a[236:239], v232 offset:14336\n" \
"v_cvt_pk_bf16_f32 v48, v48, v49\n" \
"v_mfma_f32_16x16x32_bf16 a[88:91], a[32:35], v[96:99], a[88:91]\n" \
"v_exp_f32 v50, v50\n" \
"v_mfma_f32_16x16x32_bf16 a[88:91], a[36:39], v[100:103], a[88:91]\n" \
"v_mfma_f32_16x16x32_bf16 a[120:123], a[32:35], v[112:115], a[120:123]\n" \
"v_exp_f32 v51, v51\n" \
"v_mfma_f32_16x16x32_bf16 a[120:123], a[36:39], v[116:119], a[120:123]\n" \
"v_cvt_pk_bf16_f32 v49, v50, v51\n" \
"v_mfma_f32_16x16x32_bf16 a[124:127], a[40:43], v[112:115], a[124:127]\n" \
"v_exp_f32 v52, v52\n" \
"v_mfma_f32_16x16x32_bf16 a[124:127], a[44:47], v[116:119], a[124:127]\n" \
"v_mfma_f32_16x16x32_bf16 a[92:95], a[40:43], v[96:99], a[92:95]\n" \
"v_exp_f32 v53, v53\n" \
"v_mfma_f32_16x16x32_bf16 a[92:95], a[44:47], v[100:103], a[92:95]\n" \
"v_cvt_pk_bf16_f32 v50, v52, v53\n" \
"v_smfmac_f32_16x16x64_bf16 v[228:231], v[244:247], v[112:119], v243\n" \
"v_mfma_f32_16x16x32_bf16 v[188:191], a[40:43], v[80:83], v[188:191]\n" \
"v_exp_f32 v54, v54\n" \
"v_mfma_f32_16x16x32_bf16 v[188:191], a[44:47], v[84:87], v[188:191]\n" \
"v_mfma_f32_16x16x32_bf16 v[156:159], a[40:43], v[64:67], v[156:159]\n" \
"s_waitcnt vmcnt(1) lgkmcnt(0)\n" \
"v_exp_f32 v55, v55\n" \
"v_mfma_f32_16x16x32_bf16 v[156:159], a[44:47], v[68:71], v[156:159]\n" \
"s_barrier\n" \
"v_cvt_pk_bf16_f32 v51, v54, v55\n" \
"v_mfma_f32_16x16x32_bf16 v[64:67], a[192:195], a[128:131], v[196:199]\n" \
"v_exp_f32 v8, v8\n" \
"v_mfma_f32_16x16x32_bf16 v[64:67], a[196:199], a[132:135], v[64:67]\n" \
"v_cmp_eq_u32 vcc, s60, v239\n" \
"v_mfma_f32_16x16x32_bf16 v[64:67], a[200:203], a[136:139], v[64:67]\n" \
"v_exp_f32 v9, v9\n" \
"v_mfma_f32_16x16x32_bf16 v[64:67], a[204:207], a[140:143], v[64:67]\n" \
"s_andn2_b32 s70, s69, vcc_lo\n" \
"s_cbranch_scc1 Lpl2_%=\n" \
"Lpr2_%=:\n" \
"ds_read_b128 a[32:35], v233 offset:6144\n" \
"v_cvt_pk_bf16_f32 v4, v8, v9\n" \
"v_mfma_f32_16x16x32_bf16 v[80:83], a[204:207], a[156:159], v[196:199]\n" \
"s_add_u32 m0, s77, s80\n" \
"v_exp_f32 v10, v10\n" \
"v_mfma_f32_16x16x32_bf16 v[80:83], a[200:203], a[152:155], v[80:83]\n" \
"buffer_load_dwordx4 v234, s[40:43], s76 offen offset:0 sc1 lds\n" \
"v_mfma_f32_16x16x32_bf16 v[80:83], a[196:199], a[148:151], v[80:83]\n" \
"s_sub_u32 s89, s89, 1\n" \
"v_exp_f32 v11, v11\n" \
"v_mfma_f32_16x16x32_bf16 v[80:83], a[192:195], a[144:147], v[80:83]\n" \
"s_cmp_eq_u32 s89, 0\n" \
"s_cselect_b32 s70, s78, 0xc000\n" \
"ds_read_b128 a[36:39], v233 offset:14336\n" \
"v_cvt_pk_bf16_f32 v5, v10, v11\n" \
"v_mfma_f32_16x16x32_bf16 v[96:99], a[192:195], a[160:163], v[196:199]\n" \
"s_add_u32 s71, s76, 17408\n" \
"v_exp_f32 v12, v12\n" \
"v_mfma_f32_16x16x32_bf16 v[96:99], a[196:199], a[164:167], v[96:99]\n" \
"buffer_load_dwordx4 v234, s[40:43], s76 offen offset:1024 sc1 lds\n" \
"v_mfma_f32_16x16x32_bf16 v[96:99], a[200:203], a[168:171], v[96:99]\n" \
"v_exp_f32 v13, v13\n" \
"v_mfma_f32_16x16x32_bf16 v[96:99], a[204:207], a[172:175], v[96:99]\n" \
"ds_read_b128 a[40:43], v233 offset:7168\n" \
"v_cvt_pk_bf16_f32 v6, v12, v13\n" \
"v_mfma_f32_16x16x32_bf16 v[112:115], a[204:207], a[188:191], v[196:199]\n" \
"v_exp_f32 v14, v14\n" \
"v_mfma_f32_16x16x32_bf16 v[112:115], a[200:203], a[184:187], v[112:115]\n" \
"v_xor_b32 v232, s70, v232\n" \
"v_mfma_f32_16x16x32_bf16 v[112:115], a[196:199], a[180:183], v[112:115]\n" \
"v_exp_f32 v15, v15\n" \
"v_mfma_f32_16x16x32_bf16 v[112:115], a[192:195], a[176:179], v[112:115]\n" \
"ds_read_b128 a[204:207], v233 offset:9216\n" \
"v_cvt_pk_bf16_f32 v7, v14, v15\n" \
"v_mfma_f32_16x16x32_bf16 v[116:119], a[208:211], a[176:179], v[196:199]\n" \
"v_exp_f32 v24, v24\n" \
"v_mfma_f32_16x16x32_bf16 v[116:119], a[212:215], a[180:183], v[116:119]\n" \
"buffer_load_dwordx4 v234, s[40:43], s76 offen offset:2048 sc1 lds\n" \
"v_mfma_f32_16x16x32_bf16 v[116:119], a[216:219], a[184:187], v[116:119]\n" \
"v_exp_f32 v25, v25\n" \
"v_mfma_f32_16x16x32_bf16 v[116:119], a[220:223], a[188:191], v[116:119]\n" \
"ds_read_b128 a[200:203], v233 offset:1024\n" \
"v_cvt_pk_bf16_f32 v20, v24, v25\n" \
"v_mfma_f32_16x16x32_bf16 v[100:103], a[220:223], a[172:175], v[196:199]\n" \
"v_exp_f32 v26, v26\n" \
"v_mfma_f32_16x16x32_bf16 v[100:103], a[216:219], a[168:171], v[100:103]\n" \
"buffer_load_dwordx4 v234, s[40:43], s76 offen offset:3072 sc1 lds\n" \
"v_mfma_f32_16x16x32_bf16 v[100:103], a[212:215], a[164:167], v[100:103]\n" \
"v_exp_f32 v27, v27\n" \
"v_mfma_f32_16x16x32_bf16 v[100:103], a[208:211], a[160:163], v[100:103]\n" \
"ds_read_b128 a[196:199], v233 offset:8192\n" \
"v_cvt_pk_bf16_f32 v21, v26, v27\n" \
"v_mfma_f32_16x16x32_bf16 v[84:87], a[208:211], a[144:147], v[196:199]\n" \
"v_exp_f32 v28, v28\n" \
"v_mfma_f32_16x16x32_bf16 v[84:87], a[212:215], a[148:151], v[84:87]\n" \
"v_mfma_f32_16x16x32_bf16 v[84:87], a[216:219], a[152:155], v[84:87]\n" \
"v_exp_f32 v29, v29\n" \
"v_mfma_f32_16x16x32_bf16 v[84:87], a[220:223], a[156:159], v[84:87]\n" \
"ds_read_b128 a[192:195], v233 offset:0\n" \
"v_cvt_pk_bf16_f32 v22, v28, v29\n" \
"v_mfma_f32_16x16x32_bf16 v[68:71], a[220:223], a[140:143], v[196:199]\n" \
"s_add_u32 m0, m0, 0x4000\n" \
"v_exp_f32 v30, v30\n" \
"v_mfma_f32_16x16x32_bf16 v[68:71], a[216:219], a[136:139], v[68:71]\n" \
"buffer_load_dwordx4 v234, s[40:43], s71 offen offset:0 sc1 lds\n" \
"v_mfma_f32_16x16x32_bf16 v[68:71], a[212:215], a[132:135], v[68:71]\n" \
"v_exp_f32 v31, v31\n" \
"v_mfma_f32_16x16x32_bf16 v[68:71], a[208:211], a[128:131], v[68:71]\n" \
"ds_read_b128 a[220:223], v233 offset:11264\n" \
"v_cvt_pk_bf16_f32 v23, v30, v31\n" \
"v_mfma_f32_16x16x32_bf16 v[72:75], a[224:227], a[128:131], v[196:199]\n" \
"v_exp_f32 v40, v40\n" \
"v_mfma_f32_16x16x32_bf16 v[72:75], a[228:231], a[132:135], v[72:75]\n" \
"buffer_load_dwordx4 v234, s[40:43], s71 offen offset:1024 sc1 lds\n" \
"v_mfma_f32_16x16x32_bf16 v[72:75], a[232:235], a[136:139], v[72:75]\n" \
"v_exp_f32 v41, v41\n" \
"v_mfma_f32_16x16x32_bf16 v[72:75], a[236:239], a[140:143], v[72:75]\n" \
"ds_read_b128 a[216:219], v233 offset:3072\n" \
"v_cvt_pk_bf16_f32 v36, v40, v41\n" \
"v_mfma_f32_16x16x32_bf16 v[88:91], a[236:239], a[156:159], v[196:199]\n" \
"v_exp_f32 v42, v42\n" \
"v_mfma_f32_16x16x32_bf16 v[88:91], a[232:235], a[152:155], v[88:91]\n" \
"v_mfma_f32_16x16x32_bf16 v[88:91], a[228:231], a[148:151], v[88:91]\n" \
"v_exp_f32 v43, v43\n" \
"v_mfma_f32_16x16x32_bf16 v[88:91], a[224:227], a[144:147], v[88:91]\n" \
"ds_read_b128 a[212:215], v233 offset:10240\n" \
"v_cvt_pk_bf16_f32 v37, v42, v43\n" \
"v_mfma_f32_16x16x32_bf16 v[104:107], a[224:227], a[160:163], v[196:199]\n" \
"v_exp_f32 v44, v44\n" \
"v_mfma_f32_16x16x32_bf16 v[104:107], a[228:231], a[164:167], v[104:107]\n" \
"buffer_load_dwordx4 v234, s[40:43], s71 offen offset:2048 sc1 lds\n" \
"v_mfma_f32_16x16x32_bf16 v[104:107], a[232:235], a[168:171], v[104:107]\n" \
"v_exp_f32 v45, v45\n" \
"v_mfma_f32_16x16x32_bf16 v[104:107], a[236:239], a[172:175], v[104:107]\n" \
"ds_read_b128 a[208:211], v233 offset:2048\n" \
"v_cvt_pk_bf16_f32 v38, v44, v45\n" \
"v_mfma_f32_16x16x32_bf16 v[120:123], a[236:239], a[188:191], v[196:199]\n" \
"v_exp_f32 v46, v46\n" \
"v_mfma_f32_16x16x32_bf16 v[120:123], a[232:235], a[184:187], v[120:123]\n" \
"buffer_load_dwordx4 v234, s[40:43], s71 offen offset:3072 sc1 lds\n" \
"v_mfma_f32_16x16x32_bf16 v[120:123], a[228:231], a[180:183], v[120:123]\n" \
"v_exp_f32 v47, v47\n" \
"v_mfma_f32_16x16x32_bf16 v[120:123], a[224:227], a[176:179], v[120:123]\n" \
"ds_read_b128 a[236:239], v233 offset:13312\n" \
"v_cvt_pk_bf16_f32 v39, v46, v47\n" \
"v_mfma_f32_16x16x32_bf16 v[124:127], a[240:243], a[176:179], v[196:199]\n" \
"v_exp_f32 v56, v56\n" \
"v_mfma_f32_16x16x32_bf16 v[124:127], a[244:247], a[180:183], v[124:127]\n" \
"v_mfma_f32_16x16x32_bf16 v[124:127], a[248:251], a[184:187], v[124:127]\n" \
"v_exp_f32 v57, v57\n" \
"v_mfma_f32_16x16x32_bf16 v[124:127], a[252:255], a[188:191], v[124:127]\n" \
"ds_read_b128 a[232:235], v233 offset:5120\n" \
"v_cvt_pk_bf16_f32 v52, v56, v57\n" \
"v_mfma_f32_16x16x32_bf16 v[108:111], a[252:255], a[172:175], v[196:199]\n" \
"v_exp_f32 v58, v58\n" \
"v_mfma_f32_16x16x32_bf16 v[108:111], a[248:251], a[168:171], v[108:111]\n" \
"v_mfma_f32_16x16x32_bf16 v[108:111], a[244:247], a[164:167], v[108:111]\n" \
"v_exp_f32 v59, v59\n" \
"v_mfma_f32_16x16x32_bf16 v[108:111], a[240:243], a[160:163], v[108:111]\n" \
"ds_read_b128 a[228:231], v233 offset:12288\n" \
"v_cvt_pk_bf16_f32 v53, v58, v59\n" \
"v_mfma_f32_16x16x32_bf16 v[92:95], a[240:243], a[144:147], v[196:199]\n" \
"v_exp_f32 v60, v60\n" \
"v_mfma_f32_16x16x32_bf16 v[92:95], a[244:247], a[148:151], v[92:95]\n" \
"v_mfma_f32_16x16x32_bf16 v[92:95], a[248:251], a[152:155], v[92:95]\n" \
"v_exp_f32 v61, v61\n" \
"v_mfma_f32_16x16x32_bf16 v[92:95], a[252:255], a[156:159], v[92:95]\n" \
"ds_read_b128 a[224:227], v233 offset:4096\n" \
"v_cvt_pk_bf16_f32 v54, v60, v61\n" \
"v_mfma_f32_16x16x32_bf16 v[76:79], a[252:255], a[140:143], v[196:199]\n" \
"v_exp_f32 v62, v62\n" \
"v_mfma_f32_16x16x32_bf16 v[76:79], a[248:251], a[136:139], v[76:79]\n" \
"v_mfma_f32_16x16x32_bf16 v[76:79], a[244:247], a[132:135], v[76:79]\n" \
"s_waitcnt lgkmcnt(2)\n" \
"v_exp_f32 v63, v63\n" \
"v_mfma_f32_16x16x32_bf16 v[76:79], a[240:243], a[128:131], v[76:79]\n" \
"ds_read_b128 a[44:47], v233 offset:15360\n" \
"v_cvt_pk_bf16_f32 v55, v62, v63\n" \
"v_mfma_f32_16x16x32_bf16 v[128:131], a[192:195], v[0:3], v[128:131]\n" \
"s_add_u32 m0, s74, s79\n" \
"v_exp_f32 v64, v64\n" \
"v_mfma_f32_16x16x32_bf16 v[128:131], a[196:199], v[4:7], v[128:131]\n" \
"buffer_load_dwordx4 v234, s[36:39], s73 offen offset:0 sc1 lds\n" \
"v_mfma_f32_16x16x32_bf16 v[160:163], a[192:195], v[16:19], v[160:163]\n" \
"v_exp_f32 v65, v65\n" \
"v_mfma_f32_16x16x32_bf16 v[160:163], a[196:199], v[20:23], v[160:163]\n" \
"ds_read_b128 a[240:243], v232 offset:8448\n" \
"v_cvt_pk_bf16_f32 v64, v64, v65\n" \
"v_mfma_f32_16x16x32_bf16 a[64:67], a[192:195], v[32:35], a[64:67]\n" \
"s_add_u32 s71, s73, 17408\n" \
"v_exp_f32 v66, v66\n" \
"v_mfma_f32_16x16x32_bf16 a[64:67], a[196:199], v[36:39], a[64:67]\n" \
"buffer_load_dwordx4 v234, s[36:39], s73 offen offset:1024 sc1 lds\n" \
"v_mfma_f32_16x16x32_bf16 a[96:99], a[192:195], v[48:51], a[96:99]\n" \
"v_exp_f32 v67, v67\n" \
"v_mfma_f32_16x16x32_bf16 a[96:99], a[196:199], v[52:55], a[96:99]\n" \
"ds_read_b128 a[192:195], v232 offset:0\n" \
"v_cvt_pk_bf16_f32 v65, v66, v67\n" \
"v_mfma_f32_16x16x32_bf16 a[100:103], a[200:203], v[48:51], a[100:103]\n" \
"v_exp_f32 v68, v68\n" \
"v_mfma_f32_16x16x32_bf16 a[100:103], a[204:207], v[52:55], a[100:103]\n" \
"v_xor_b32 v233, 0x4000, v233\n" \
"v_mfma_f32_16x16x32_bf16 a[68:71], a[200:203], v[32:35], a[68:71]\n" \
"v_exp_f32 v69, v69\n" \
"v_mfma_f32_16x16x32_bf16 a[68:71], a[204:207], v[36:39], a[68:71]\n" \
"ds_read_b128 a[196:199], v232 offset:2048\n" \
"v_cvt_pk_bf16_f32 v66, v68, v69\n" \
"v_smfmac_f32_16x16x64_bf16 a[48:51], v[244:247], v[0:7], v243\n" \
"buffer_load_dwordx4 v234, s[36:39], s73 offen offset:2048 sc1 lds\n" \
"v_mfma_f32_16x16x32_bf16 v[164:167], a[200:203], v[16:19], v[164:167]\n" \
"v_exp_f32 v70, v70\n" \
"v_mfma_f32_16x16x32_bf16 v[164:167], a[204:207], v[20:23], v[164:167]\n" \
"v_mfma_f32_16x16x32_bf16 v[132:135], a[200:203], v[0:3], v[132:135]\n" \
"v_exp_f32 v71, v71\n" \
"v_mfma_f32_16x16x32_bf16 v[132:135], a[204:207], v[4:7], v[132:135]\n" \
"ds_read_b128 a[200:203], v232 offset:4096\n" \
"v_cvt_pk_bf16_f32 v67, v70, v71\n" \
"v_mfma_f32_16x16x32_bf16 v[136:139], a[208:211], v[0:3], v[136:139]\n" \
"v_exp_f32 v80, v80\n" \
"v_mfma_f32_16x16x32_bf16 v[136:139], a[212:215], v[4:7], v[136:139]\n" \
"buffer_load_dwordx4 v234, s[36:39], s73 offen offset:3072 sc1 lds\n" \
"v_mfma_f32_16x16x32_bf16 v[168:171], a[208:211], v[16:19], v[168:171]\n" \
"v_exp_f32 v81, v81\n" \
"v_mfma_f32_16x16x32_bf16 v[168:171], a[212:215], v[20:23], v[168:171]\n" \
"ds_read_b128 a[204:207], v232 offset:6144\n" \
"v_cvt_pk_bf16_f32 v80, v80, v81\n" \
"v_mfma_f32_16x16x32_bf16 a[72:75], a[208:211], v[32:35], a[72:75]\n" \
"v_exp_f32 v82, v82\n" \
"v_mfma_f32_16x16x32_bf16 a[72:75], a[212:215], v[36:39], a[72:75]\n" \
"v_mfma_f32_16x16x32_bf16 a[104:107], a[208:211], v[48:51], a[104:107]\n" \
"v_exp_f32 v83, v83\n" \
"v_mfma_f32_16x16x32_bf16 a[104:107], a[212:215], v[52:55], a[104:107]\n" \
"ds_read_b128 a[208:211], v232 offset:256\n" \
"v_cvt_pk_bf16_f32 v81, v82, v83\n" \
"v_mfma_f32_16x16x32_bf16 a[108:111], a[216:219], v[48:51], a[108:111]\n" \
"s_add_u32 m0, m0, 0x4000\n" \
"v_exp_f32 v84, v84\n" \
"v_mfma_f32_16x16x32_bf16 a[108:111], a[220:223], v[52:55], a[108:111]\n" \
"buffer_load_dwordx4 v234, s[36:39], s71 offen offset:0 sc1 lds\n" \
"v_mfma_f32_16x16x32_bf16 a[76:79], a[216:219], v[32:35], a[76:79]\n" \
"v_exp_f32 v85, v85\n" \
"v_mfma_f32_16x16x32_bf16 a[76:79], a[220:223], v[36:39], a[76:79]\n" \
"ds_read_b128 a[212:215], v232 offset:2304\n" \
"v_cvt_pk_bf16_f32 v82, v84, v85\n" \
"v_smfmac_f32_16x16x64_bf16 a[52:55], v[244:247], v[16:23], v243\n" \
"v_mfma_f32_16x16x32_bf16 v[172:175], a[216:219], v[16:19], v[172:175]\n" \
"v_exp_f32 v86, v86\n" \
"v_mfma_f32_16x16x32_bf16 v[172:175], a[220:223], v[20:23], v[172:175]\n" \
"buffer_load_dwordx4 v234, s[36:39], s71 offen offset:1024 sc1 lds\n" \
"v_mfma_f32_16x16x32_bf16 v[140:143], a[216:219], v[0:3], v[140:143]\n" \
"s_waitcnt lgkmcnt(7)\n" \
"v_exp_f32 v87, v87\n" \
"v_mfma_f32_16x16x32_bf16 v[140:143], a[220:223], v[4:7], v[140:143]\n" \
"ds_read_b128 a[216:219], v232 offset:4352\n" \
"v_cvt_pk_bf16_f32 v83, v86, v87\n" \
"v_mfma_f32_16x16x32_bf16 v[144:147], a[224:227], v[0:3], v[144:147]\n" \
"v_exp_f32 v96, v96\n" \
"v_mfma_f32_16x16x32_bf16 v[144:147], a[228:231], v[4:7], v[144:147]\n" \
"buffer_load_dwordx4 v234, s[36:39], s71 offen offset:2048 sc1 lds\n" \
"v_mfma_f32_16x16x32_bf16 v[176:179], a[224:227], v[16:19], v[176:179]\n" \
"v_exp_f32 v97, v97\n" \
"v_mfma_f32_16x16x32_bf16 v[176:179], a[228:231], v[20:23], v[176:179]\n" \
"ds_read_b128 a[220:223], v232 offset:6400\n" \
"v_cvt_pk_bf16_f32 v96, v96, v97\n" \
"v_mfma_f32_16x16x32_bf16 a[80:83], a[224:227], v[32:35], a[80:83]\n" \
"v_exp_f32 v98, v98\n" \
"v_mfma_f32_16x16x32_bf16 a[80:83], a[228:231], v[36:39], a[80:83]\n" \
"v_mfma_f32_16x16x32_bf16 a[112:115], a[224:227], v[48:51], a[112:115]\n" \
"v_exp_f32 v99, v99\n" \
"v_mfma_f32_16x16x32_bf16 a[112:115], a[228:231], v[52:55], a[112:115]\n" \
"ds_read_b128 a[224:227], v232 offset:8192\n" \
"v_cvt_pk_bf16_f32 v97, v98, v99\n" \
"v_mfma_f32_16x16x32_bf16 a[116:119], a[232:235], v[48:51], a[116:119]\n" \
"v_exp_f32 v100, v100\n" \
"v_mfma_f32_16x16x32_bf16 a[116:119], a[236:239], v[52:55], a[116:119]\n" \
"buffer_load_dwordx4 v234, s[36:39], s71 offen offset:3072 sc1 lds\n" \
"v_mfma_f32_16x16x32_bf16 a[84:87], a[232:235], v[32:35], a[84:87]\n" \
"v_exp_f32 v101, v101\n" \
"v_mfma_f32_16x16x32_bf16 a[84:87], a[236:239], v[36:39], a[84:87]\n" \
"ds_read_b128 a[228:231], v232 offset:10240\n" \
"v_cvt_pk_bf16_f32 v98, v100, v101\n" \
"v_smfmac_f32_16x16x64_bf16 v[224:227], v[244:247], v[32:39], v243\n" \
"v_mfma_f32_16x16x32_bf16 v[180:183], a[232:235], v[16:19], v[180:183]\n" \
"v_exp_f32 v102, v102\n" \
"v_mfma_f32_16x16x32_bf16 v[180:183], a[236:239], v[20:23], v[180:183]\n" \
"buffer_load_dword v239, v242, s[56:59], s95 offen sc1\n" \
"v_mfma_f32_16x16x32_bf16 v[148:151], a[232:235], v[0:3], v[148:151]\n" \
"s_mov_b32 s69, s92\n" \
"v_exp_f32 v103, v103\n" \
"v_mfma_f32_16x16x32_bf16 v[148:151], a[236:239], v[4:7], v[148:151]\n" \
"s_mov_b32 s76, s73\n" \
"ds_read_b128 a[232:235], v232 offset:12288\n" \
"v_cvt_pk_bf16_f32 v99, v102, v103\n" \
"v_mfma_f32_16x16x32_bf16 v[152:155], a[32:35], v[0:3], v[152:155]\n" \
"s_add_u32 s73, s73, 34816\n" \
"v_exp_f32 v112, v112\n" \
"v_mfma_f32_16x16x32_bf16 v[152:155], a[36:39], v[4:7], v[152:155]\n" \
"s_cmp_eq_u32 s73, s83\n" \
"s_cselect_b32 s70, 34816, 0\n" \
"s_add_u32 s73, s73, s70\n" \
"v_mfma_f32_16x16x32_bf16 v[184:187], a[32:35], v[16:19], v[184:187]\n" \
"s_xor_b32 s74, s74, 0x8000\n" \
"v_exp_f32 v113, v113\n" \
"v_mfma_f32_16x16x32_bf16 v[184:187], a[36:39], v[20:23], v[184:187]\n" \
"s_xor_b32 s77, s77, 0x8000\n" \
"ds_read_b128 a[236:239], v232 offset:14336\n" \
"v_cvt_pk_bf16_f32 v112, v112, v113\n" \
"v_mfma_f32_16x16x32_bf16 a[88:91], a[32:35], v[32:35], a[88:91]\n" \
"v_exp_f32 v114, v114\n" \
"v_mfma_f32_16x16x32_bf16 a[88:91], a[36:39], v[36:39], a[88:91]\n" \
"v_mfma_f32_16x16x32_bf16 a[120:123], a[32:35], v[48:51], a[120:123]\n" \
"v_exp_f32 v115, v115\n" \
"v_mfma_f32_16x16x32_bf16 a[120:123], a[36:39], v[52:55], a[120:123]\n" \
"ds_read_b128 a[244:247], v232 offset:10496\n" \
"v_cvt_pk_bf16_f32 v113, v114, v115\n" \
"v_mfma_f32_16x16x32_bf16 a[124:127], a[40:43], v[48:51], a[124:127]\n" \
"v_exp_f32 v116, v116\n" \
"v_mfma_f32_16x16x32_bf16 a[124:127], a[44:47], v[52:55], a[124:127]\n" \
"v_mfma_f32_16x16x32_bf16 a[92:95], a[40:43], v[32:35], a[92:95]\n" \
"v_exp_f32 v117, v117\n" \
"v_mfma_f32_16x16x32_bf16 a[92:95], a[44:47], v[36:39], a[92:95]\n" \
"ds_read_b128 a[248:251], v232 offset:12544\n" \
"v_cvt_pk_bf16_f32 v114, v116, v117\n" \
"v_smfmac_f32_16x16x64_bf16 v[228:231], v[244:247], v[48:55], v243\n" \
"v_mfma_f32_16x16x32_bf16 v[188:191], a[40:43], v[16:19], v[188:191]\n" \
"v_exp_f32 v118, v118\n" \
"v_mfma_f32_16x16x32_bf16 v[188:191], a[44:47], v[20:23], v[188:191]\n" \
"s_cmp_lg_u32 s89, 0\n" \
"v_exp_f32 v119, v119\n" \
"v_mfma_f32_16x16x32_bf16 v[156:159], a[40:43], v[0:3], v[156:159]\n" \
"s_waitcnt lgkmcnt(2)\n" \
"ds_read_b128 a[252:255], v232 offset:14592\n" \
"v_cvt_pk_bf16_f32 v115, v118, v119\n" \
"v_mfma_f32_16x16x32_bf16 v[156:159], a[44:47], v[4:7], v[156:159]\n" \
"s_cbranch_scc1 Lloop_%=\n" \
"s_cmp_eq_u32 s63, 7\n" \
"s_cbranch_scc0 Lloop2_%=\n" \
"s_branch Lg_%=\n" \
"s_nop 0\n" \
"s_nop 0\n" \
"Lep_%=:\n" \
"Lpg6056_%=:\n" \
"s_cbranch_execz Lpg6798_%=\n" \
"s_nop 4\n" \
"v_accvgpr_read_b32 v92, a48\n" \
"v_accvgpr_read_b32 v93, a49\n" \
"v_accvgpr_read_b32 v94, a52\n" \
"v_accvgpr_read_b32 v95, a53\n" \
"v_accvgpr_write_b32 a48, 0\n" \
"v_accvgpr_write_b32 a49, 0\n" \
"v_accvgpr_write_b32 a52, 0\n" \
"v_accvgpr_write_b32 a53, 0\n" \
"v_add_f32 v86, v92, v93\n" \
"v_add_f32 v87, v94, v95\n" \
"v_rcp_f32 v84, v86\n" \
"v_rcp_f32 v85, v87\n" \
"v_add_u32 v240, 0x1000, v236\n" \
"v_add_f32 v80, v86, v87\n" \
"v_mul_f32 v80, 0x43800000, v80\n" \
"v_add_f32 v86, v84, v85\n" \
"v_fmac_f32 v80, 0x5f800000, v86\n" \
"v_mov_b32 v81, 0\n" \
"v_mov_b32 v82, 0\n" \
"v_mov_b32 v83, 0\n" \
"v_mov_b32 v88, 0\n" \
"v_mov_b32 v89, 0\n" \
"v_mov_b32 v90, 0\n" \
"v_mov_b32 v91, 0\n" \
"v_pk_mul_f32 v[128:129], v[128:129], v[84:85] op_sel:[0,0] op_sel_hi:[1,0]\n" \
"v_pk_mul_f32 v[130:131], v[130:131], v[84:85] op_sel:[0,0] op_sel_hi:[1,0]\n" \
"v_pk_mul_f32 v[132:133], v[132:133], v[84:85] op_sel:[0,0] op_sel_hi:[1,0]\n" \
"v_pk_mul_f32 v[134:135], v[134:135], v[84:85] op_sel:[0,0] op_sel_hi:[1,0]\n" \
"v_cvt_pk_bf16_f32 v64, v128, v129\n" \
"v_cvt_pk_bf16_f32 v65, v130, v131\n" \
"v_cvt_pk_bf16_f32 v66, v132, v133\n" \
"v_cvt_pk_bf16_f32 v67, v134, v135\n" \
"buffer_store_dwordx4 v[64:67], v236, s[56:59], s87 offen offset:0 sc1\n" \
"v_pk_mul_f32 v[136:137], v[136:137], v[84:85] op_sel:[0,0] op_sel_hi:[1,0]\n" \
"v_pk_mul_f32 v[138:139], v[138:139], v[84:85] op_sel:[0,0] op_sel_hi:[1,0]\n" \
"v_pk_mul_f32 v[140:141], v[140:141], v[84:85] op_sel:[0,0] op_sel_hi:[1,0]\n" \
"v_pk_mul_f32 v[142:143], v[142:143], v[84:85] op_sel:[0,0] op_sel_hi:[1,0]\n" \
"v_cvt_pk_bf16_f32 v68, v136, v137\n" \
"v_cvt_pk_bf16_f32 v69, v138, v139\n" \
"v_cvt_pk_bf16_f32 v70, v140, v141\n" \
"v_cvt_pk_bf16_f32 v71, v142, v143\n" \
"buffer_store_dwordx4 v[68:71], v236, s[56:59], s87 offen offset:2048 sc1\n" \
"v_pk_mul_f32 v[144:145], v[144:145], v[84:85] op_sel:[0,0] op_sel_hi:[1,0]\n" \
"v_pk_mul_f32 v[146:147], v[146:147], v[84:85] op_sel:[0,0] op_sel_hi:[1,0]\n" \
"v_pk_mul_f32 v[148:149], v[148:149], v[84:85] op_sel:[0,0] op_sel_hi:[1,0]\n" \
"v_pk_mul_f32 v[150:151], v[150:151], v[84:85] op_sel:[0,0] op_sel_hi:[1,0]\n" \
"v_cvt_pk_bf16_f32 v72, v144, v145\n" \
"v_cvt_pk_bf16_f32 v73, v146, v147\n" \
"v_cvt_pk_bf16_f32 v74, v148, v149\n" \
"v_cvt_pk_bf16_f32 v75, v150, v151\n" \
"buffer_store_dwordx4 v[72:75], v240, s[56:59], s87 offen offset:0 sc1\n" \
"v_pk_mul_f32 v[152:153], v[152:153], v[84:85] op_sel:[0,0] op_sel_hi:[1,0]\n" \
"v_pk_mul_f32 v[154:155], v[154:155], v[84:85] op_sel:[0,0] op_sel_hi:[1,0]\n" \
"v_pk_mul_f32 v[156:157], v[156:157], v[84:85] op_sel:[0,0] op_sel_hi:[1,0]\n" \
"v_pk_mul_f32 v[158:159], v[158:159], v[84:85] op_sel:[0,0] op_sel_hi:[1,0]\n" \
"v_cvt_pk_bf16_f32 v76, v152, v153\n" \
"v_cvt_pk_bf16_f32 v77, v154, v155\n" \
"v_cvt_pk_bf16_f32 v78, v156, v157\n" \
"v_cvt_pk_bf16_f32 v79, v158, v159\n" \
"buffer_store_dwordx4 v[76:79], v240, s[56:59], s87 offen offset:2048 sc1\n" \
"v_smfmac_f32_16x16x64_bf16 v[80:83], v[244:247], v[64:71], v243\n" \
"v_smfmac_f32_16x16x64_bf16 v[80:83], v[244:247], v[72:79], v243\n" \
"v_mfma_f32_32x32x16_bf16 v[128:143], v[88:91], v[88:91], 0\n" \
"v_mfma_f32_32x32x16_bf16 v[144:159], v[88:91], v[88:91], 0\n" \
"s_mov_b64 exec, 0\n" \
"s_cmp_eq_u32 s61, 0\n" \
"s_cbranch_scc1 Lst1x0_%=\n" \
"s_cmp_eq_u32 s61, 1\n" \
"s_cbranch_scc1 Lst1x1_%=\n" \
"s_cmp_eq_u32 s61, 2\n" \
"s_cbranch_scc1 Lst1x2_%=\n" \
"s_branch Lst1x3_%=\n" \
"Lcb1_%=:\n" \
"s_mov_b64 exec, -1\n" \
"s_add_u32 s72, s87, 532480\n" \
"v_pk_mul_f32 v[160:161], v[160:161], v[84:85] op_sel:[0,1] op_sel_hi:[1,1]\n" \
"v_pk_mul_f32 v[162:163], v[162:163], v[84:85] op_sel:[0,1] op_sel_hi:[1,1]\n" \
"v_pk_mul_f32 v[164:165], v[164:165], v[84:85] op_sel:[0,1] op_sel_hi:[1,1]\n" \
"v_pk_mul_f32 v[166:167], v[166:167], v[84:85] op_sel:[0,1] op_sel_hi:[1,1]\n" \
"v_cvt_pk_bf16_f32 v64, v160, v161\n" \
"v_cvt_pk_bf16_f32 v65, v162, v163\n" \
"v_cvt_pk_bf16_f32 v66, v164, v165\n" \
"v_cvt_pk_bf16_f32 v67, v166, v167\n" \
"buffer_store_dwordx4 v[64:67], v236, s[56:59], s72 offen offset:0 sc1\n" \
"v_pk_mul_f32 v[168:169], v[168:169], v[84:85] op_sel:[0,1] op_sel_hi:[1,1]\n" \
"v_pk_mul_f32 v[170:171], v[170:171], v[84:85] op_sel:[0,1] op_sel_hi:[1,1]\n" \
"v_pk_mul_f32 v[172:173], v[172:173], v[84:85] op_sel:[0,1] op_sel_hi:[1,1]\n" \
"v_pk_mul_f32 v[174:175], v[174:175], v[84:85] op_sel:[0,1] op_sel_hi:[1,1]\n" \
"v_cvt_pk_bf16_f32 v68, v168, v169\n" \
"v_cvt_pk_bf16_f32 v69, v170, v171\n" \
"v_cvt_pk_bf16_f32 v70, v172, v173\n" \
"v_cvt_pk_bf16_f32 v71, v174, v175\n" \
"buffer_store_dwordx4 v[68:71], v236, s[56:59], s72 offen offset:2048 sc1\n" \
"v_pk_mul_f32 v[176:177], v[176:177], v[84:85] op_sel:[0,1] op_sel_hi:[1,1]\n" \
"v_pk_mul_f32 v[178:179], v[178:179], v[84:85] op_sel:[0,1] op_sel_hi:[1,1]\n" \
"v_pk_mul_f32 v[180:181], v[180:181], v[84:85] op_sel:[0,1] op_sel_hi:[1,1]\n" \
"v_pk_mul_f32 v[182:183], v[182:183], v[84:85] op_sel:[0,1] op_sel_hi:[1,1]\n" \
"v_cvt_pk_bf16_f32 v72, v176, v177\n" \
"v_cvt_pk_bf16_f32 v73, v178, v179\n" \
"v_cvt_pk_bf16_f32 v74, v180, v181\n" \
"v_cvt_pk_bf16_f32 v75, v182, v183\n" \
"buffer_store_dwordx4 v[72:75], v240, s[56:59], s72 offen offset:0 sc1\n" \
"v_pk_mul_f32 v[184:185], v[184:185], v[84:85] op_sel:[0,1] op_sel_hi:[1,1]\n" \
"v_pk_mul_f32 v[186:187], v[186:187], v[84:85] op_sel:[0,1] op_sel_hi:[1,1]\n" \
"v_pk_mul_f32 v[188:189], v[188:189], v[84:85] op_sel:[0,1] op_sel_hi:[1,1]\n" \
"v_pk_mul_f32 v[190:191], v[190:191], v[84:85] op_sel:[0,1] op_sel_hi:[1,1]\n" \
"v_cvt_pk_bf16_f32 v76, v184, v185\n" \
"v_cvt_pk_bf16_f32 v77, v186, v187\n" \
"v_cvt_pk_bf16_f32 v78, v188, v189\n" \
"v_cvt_pk_bf16_f32 v79, v190, v191\n" \
"buffer_store_dwordx4 v[76:79], v240, s[56:59], s72 offen offset:2048 sc1\n" \
"v_smfmac_f32_16x16x64_bf16 v[80:83], v[244:247], v[64:71], v243\n" \
"v_smfmac_f32_16x16x64_bf16 v[80:83], v[244:247], v[72:79], v243\n" \
"v_mfma_f32_32x32x16_bf16 v[160:175], v[88:91], v[88:91], 0\n" \
"v_mfma_f32_32x32x16_bf16 v[176:191], v[88:91], v[88:91], 0\n" \
"s_nop 5\n" \
"v_add_f32 v80, v80, v81\n" \
"s_movk_i32 s64, 0x207\n" \
"v_cmp_class_f32 vcc, v80, s64\n" \
"s_nop 1\n" \
"s_cmp_lg_u64 vcc, 0\n" \
"s_addc_u32 s90, s90, 0\n" \
"s_waitcnt vmcnt(8)\n" \
"s_cmp_eq_u32 s63, 7\n" \
"s_cbranch_scc0 Lepr_%=\n" \
"s_branch Lepg_%=\n" \
"s_nop 0\n" \
"s_nop 0\n" \
"s_nop 0\n" \
"s_nop 0\n" \
"s_nop 0\n" \
"Lloop2_%=:\n" \
"Lpg6190_%=:\n" \
"s_cbranch_execz Lpg7384_%=\n" \
"v_mfma_f32_16x16x32_bf16 v[0:3], a[192:195], a[0:3], v[196:199]\n" \
"v_exp_f32 v72, v72\n" \
"v_mfma_f32_16x16x32_bf16 v[0:3], a[196:199], a[4:7], v[0:3]\n" \
"ds_read_b128 a[44:47], v233 offset:15360\n" \
"v_mfma_f32_16x16x32_bf16 v[0:3], a[200:203], a[8:11], v[0:3]\n" \
"v_exp_f32 v73, v73\n" \
"v_mfma_f32_16x16x32_bf16 v[0:3], a[204:207], a[12:15], v[0:3]\n" \
"ds_read_b128 a[32:35], v233 offset:6144\n" \
"v_cvt_pk_bf16_f32 v68, v72, v73\n" \
"Lpg6200_%=:\n" \
"s_cbranch_execz Lcb0_%=\n" \
"v_mfma_f32_16x16x32_bf16 v[16:19], a[204:207], a[28:31], v[196:199]\n" \
"v_exp_f32 v74, v74\n" \
"v_mfma_f32_16x16x32_bf16 v[16:19], a[200:203], a[24:27], v[16:19]\n" \
"v_xor_b32 v232, 0x8000, v232\n" \
"v_mfma_f32_16x16x32_bf16 v[16:19], a[196:199], a[20:23], v[16:19]\n" \
"v_exp_f32 v75, v75\n" \
"v_mfma_f32_16x16x32_bf16 v[16:19], a[192:195], a[16:19], v[16:19]\n" \
"ds_read_b128 a[36:39], v233 offset:14336\n" \
"v_cvt_pk_bf16_f32 v69, v74, v75\n" \
"v_mfma_f32_16x16x32_bf16 v[32:35], a[192:195], a[160:163], v[196:199]\n" \
"v_exp_f32 v76, v76\n" \
"v_mfma_f32_16x16x32_bf16 v[32:35], a[196:199], a[164:167], v[32:35]\n" \
"v_mfma_f32_16x16x32_bf16 v[32:35], a[200:203], a[168:171], v[32:35]\n" \
"v_exp_f32 v77, v77\n" \
"v_mfma_f32_16x16x32_bf16 v[32:35], a[204:207], a[172:175], v[32:35]\n" \
"ds_read_b128 a[40:43], v233 offset:7168\n" \
"v_cvt_pk_bf16_f32 v70, v76, v77\n" \
"v_mfma_f32_16x16x32_bf16 v[48:51], a[204:207], a[188:191], v[196:199]\n" \
"v_exp_f32 v78, v78\n" \
"v_mfma_f32_16x16x32_bf16 v[48:51], a[200:203], a[184:187], v[48:51]\n" \
"v_mfma_f32_16x16x32_bf16 v[48:51], a[196:199], a[180:183], v[48:51]\n" \
"v_exp_f32 v79, v79\n" \
"v_mfma_f32_16x16x32_bf16 v[48:51], a[192:195], a[176:179], v[48:51]\n" \
"ds_read_b128 a[204:207], v233 offset:9216\n" \
"v_cvt_pk_bf16_f32 v71, v78, v79\n" \
"v_mfma_f32_16x16x32_bf16 v[52:55], a[208:211], a[176:179], v[196:199]\n" \
"v_exp_f32 v88, v88\n" \
"v_mfma_f32_16x16x32_bf16 v[52:55], a[212:215], a[180:183], v[52:55]\n" \
"v_mfma_f32_16x16x32_bf16 v[52:55], a[216:219], a[184:187], v[52:55]\n" \
"v_exp_f32 v89, v89\n" \
"v_mfma_f32_16x16x32_bf16 v[52:55], a[220:223], a[188:191], v[52:55]\n" \
"ds_read_b128 a[200:203], v233 offset:1024\n" \
"v_cvt_pk_bf16_f32 v84, v88, v89\n" \
"v_mfma_f32_16x16x32_bf16 v[36:39], a[220:223], a[172:175], v[196:199]\n" \
"v_exp_f32 v90, v90\n" \
"v_mfma_f32_16x16x32_bf16 v[36:39], a[216:219], a[168:171], v[36:39]\n" \
"v_mfma_f32_16x16x32_bf16 v[36:39], a[212:215], a[164:167], v[36:39]\n" \
"v_exp_f32 v91, v91\n" \
"v_mfma_f32_16x16x32_bf16 v[36:39], a[208:211], a[160:163], v[36:39]\n" \
"ds_read_b128 a[196:199], v233 offset:8192\n" \
"v_cvt_pk_bf16_f32 v85, v90, v91\n" \
"v_mfma_f32_16x16x32_bf16 v[20:23], a[208:211], a[16:19], v[196:199]\n" \
"v_exp_f32 v92, v92\n" \
"v_mfma_f32_16x16x32_bf16 v[20:23], a[212:215], a[20:23], v[20:23]\n" \
"v_mfma_f32_16x16x32_bf16 v[20:23], a[216:219], a[24:27], v[20:23]\n" \
"v_exp_f32 v93, v93\n" \
"v_mfma_f32_16x16x32_bf16 v[20:23], a[220:223], a[28:31], v[20:23]\n" \
"ds_read_b128 a[192:195], v233 offset:0\n" \
"v_cvt_pk_bf16_f32 v86, v92, v93\n" \
"v_mfma_f32_16x16x32_bf16 v[4:7], a[220:223], a[12:15], v[196:199]\n" \
"v_exp_f32 v94, v94\n" \
"v_mfma_f32_16x16x32_bf16 v[4:7], a[216:219], a[8:11], v[4:7]\n" \
"v_mfma_f32_16x16x32_bf16 v[4:7], a[212:215], a[4:7], v[4:7]\n" \
"s_waitcnt lgkmcnt(8)\n" \
"v_exp_f32 v95, v95\n" \
"v_mfma_f32_16x16x32_bf16 v[4:7], a[208:211], a[0:3], v[4:7]\n" \
"ds_read_b128 a[220:223], v233 offset:11264\n" \
"v_cvt_pk_bf16_f32 v87, v94, v95\n" \
"v_mfma_f32_16x16x32_bf16 v[8:11], a[224:227], a[0:3], v[196:199]\n" \
"v_exp_f32 v104, v104\n" \
"v_mfma_f32_16x16x32_bf16 v[8:11], a[228:231], a[4:7], v[8:11]\n" \
"v_mfma_f32_16x16x32_bf16 v[8:11], a[232:235], a[8:11], v[8:11]\n" \
"v_exp_f32 v105, v105\n" \
"v_mfma_f32_16x16x32_bf16 v[8:11], a[236:239], a[12:15], v[8:11]\n" \
"ds_read_b128 a[216:219], v233 offset:3072\n" \
"v_cvt_pk_bf16_f32 v100, v104, v105\n" \
"v_mfma_f32_16x16x32_bf16 v[24:27], a[236:239], a[28:31], v[196:199]\n" \
"v_exp_f32 v106, v106\n" \
"v_mfma_f32_16x16x32_bf16 v[24:27], a[232:235], a[24:27], v[24:27]\n" \
"v_mfma_f32_16x16x32_bf16 v[24:27], a[228:231], a[20:23], v[24:27]\n" \
"v_exp_f32 v107, v107\n" \
"v_mfma_f32_16x16x32_bf16 v[24:27], a[224:227], a[16:19], v[24:27]\n" \
"ds_read_b128 a[212:215], v233 offset:10240\n" \
"v_cvt_pk_bf16_f32 v101, v106, v107\n" \
"v_mfma_f32_16x16x32_bf16 v[40:43], a[224:227], a[160:163], v[196:199]\n" \
"v_exp_f32 v108, v108\n" \
"v_mfma_f32_16x16x32_bf16 v[40:43], a[228:231], a[164:167], v[40:43]\n" \
"v_mfma_f32_16x16x32_bf16 v[40:43], a[232:235], a[168:171], v[40:43]\n" \
"v_exp_f32 v109, v109\n" \
"v_mfma_f32_16x16x32_bf16 v[40:43], a[236:239], a[172:175], v[40:43]\n" \
"ds_read_b128 a[208:211], v233 offset:2048\n" \
"v_cvt_pk_bf16_f32 v102, v108, v109\n" \
"v_mfma_f32_16x16x32_bf16 v[56:59], a[236:239], a[188:191], v[196:199]\n" \
"v_exp_f32 v110, v110\n" \
"v_mfma_f32_16x16x32_bf16 v[56:59], a[232:235], a[184:187], v[56:59]\n" \
"v_mfma_f32_16x16x32_bf16 v[56:59], a[228:231], a[180:183], v[56:59]\n" \
"v_exp_f32 v111, v111\n" \
"v_mfma_f32_16x16x32_bf16 v[56:59], a[224:227], a[176:179], v[56:59]\n" \
"ds_read_b128 a[236:239], v233 offset:13312\n" \
"v_cvt_pk_bf16_f32 v103, v110, v111\n" \
"v_mfma_f32_16x16x32_bf16 v[60:63], a[240:243], a[176:179], v[196:199]\n" \
"v_exp_f32 v120, v120\n" \
"v_mfma_f32_16x16x32_bf16 v[60:63], a[244:247], a[180:183], v[60:63]\n" \
"ds_read_b128 a[232:235], v233 offset:5120\n" \
"v_mfma_f32_16x16x32_bf16 v[60:63], a[248:251], a[184:187], v[60:63]\n" \
"v_exp_f32 v121, v121\n" \
"v_mfma_f32_16x16x32_bf16 v[60:63], a[252:255], a[188:191], v[60:63]\n" \
"ds_read_b128 a[228:231], v233 offset:12288\n" \
"v_cvt_pk_bf16_f32 v116, v120, v121\n" \
"v_mfma_f32_16x16x32_bf16 v[44:47], a[252:255], a[172:175], v[196:199]\n" \
"v_exp_f32 v122, v122\n" \
"v_mfma_f32_16x16x32_bf16 v[44:47], a[248:251], a[168:171], v[44:47]\n" \
"ds_read_b128 a[224:227], v233 offset:4096\n" \
"v_mfma_f32_16x16x32_bf16 v[44:47], a[244:247], a[164:167], v[44:47]\n" \
"v_exp_f32 v123, v123\n" \
"v_mfma_f32_16x16x32_bf16 v[44:47], a[240:243], a[160:163], v[44:47]\n" \
"v_cvt_pk_bf16_f32 v117, v122, v123\n" \
"v_mfma_f32_16x16x32_bf16 v[28:31], a[240:243], a[16:19], v[196:199]\n" \
"v_exp_f32 v124, v124\n" \
"v_mfma_f32_16x16x32_bf16 v[28:31], a[244:247], a[20:23], v[28:31]\n" \
"v_mfma_f32_16x16x32_bf16 v[28:31], a[248:251], a[24:27], v[28:31]\n" \
"v_exp_f32 v125, v125\n" \
"v_mfma_f32_16x16x32_bf16 v[28:31], a[252:255], a[28:31], v[28:31]\n" \
"v_cvt_pk_bf16_f32 v118, v124, v125\n" \
"v_mfma_f32_16x16x32_bf16 v[12:15], a[252:255], a[12:15], v[196:199]\n" \
"v_exp_f32 v126, v126\n" \
"v_mfma_f32_16x16x32_bf16 v[12:15], a[248:251], a[8:11], v[12:15]\n" \
"v_mfma_f32_16x16x32_bf16 v[12:15], a[244:247], a[4:7], v[12:15]\n" \
"s_waitcnt vmcnt(0) lgkmcnt(0)\n" \
"v_exp_f32 v127, v127\n" \
"v_mfma_f32_16x16x32_bf16 v[12:15], a[240:243], a[0:3], v[12:15]\n" \
"s_barrier\n" \
"v_cvt_pk_bf16_f32 v119, v126, v127\n" \
"v_mfma_f32_16x16x32_bf16 v[128:131], a[192:195], v[64:67], v[128:131]\n" \
"v_exp_f32 v0, v0\n" \
"v_mfma_f32_16x16x32_bf16 v[128:131], a[196:199], v[68:71], v[128:131]\n" \
"v_cmp_eq_u32 vcc, s60, v239\n" \
"v_mfma_f32_16x16x32_bf16 v[160:163], a[192:195], v[80:83], v[160:163]\n" \
"v_exp_f32 v1, v1\n" \
"v_mfma_f32_16x16x32_bf16 v[160:163], a[196:199], v[84:87], v[160:163]\n" \
"s_andn2_b32 s70, s69, vcc_lo\n" \
"s_cbranch_scc1 Lpl3_%=\n" \
"Lpr3_%=:\n" \
"ds_read_b128 a[240:243], v232 offset:8448\n" \
"v_cvt_pk_bf16_f32 v0, v0, v1\n" \
"v_mfma_f32_16x16x32_bf16 a[64:67], a[192:195], v[96:99], a[64:67]\n" \
"s_add_u32 m0, s74, s79\n" \
"v_exp_f32 v2, v2\n" \
"v_mfma_f32_16x16x32_bf16 a[64:67], a[196:199], v[100:103], a[64:67]\n" \
"buffer_load_dwordx4 v234, s[36:39], s73 offen offset:0 sc1 lds\n" \
"v_mfma_f32_16x16x32_bf16 a[96:99], a[192:195], v[112:115], a[96:99]\n" \
"v_exp_f32 v3, v3\n" \
"v_mfma_f32_16x16x32_bf16 a[96:99], a[196:199], v[116:119], a[96:99]\n" \
"ds_read_b128 a[192:195], v232 offset:0\n" \
"v_cvt_pk_bf16_f32 v1, v2, v3\n" \
"v_mfma_f32_16x16x32_bf16 a[100:103], a[200:203], v[112:115], a[100:103]\n" \
"s_add_u32 s71, s73, 17408\n" \
"v_exp_f32 v4, v4\n" \
"v_mfma_f32_16x16x32_bf16 a[100:103], a[204:207], v[116:119], a[100:103]\n" \
"buffer_load_dwordx4 v234, s[36:39], s73 offen offset:1024 sc1 lds\n" \
"v_mfma_f32_16x16x32_bf16 a[68:71], a[200:203], v[96:99], a[68:71]\n" \
"v_exp_f32 v5, v5\n" \
"v_mfma_f32_16x16x32_bf16 a[68:71], a[204:207], v[100:103], a[68:71]\n" \
"ds_read_b128 a[196:199], v232 offset:2048\n" \
"v_cvt_pk_bf16_f32 v2, v4, v5\n" \
"v_smfmac_f32_16x16x64_bf16 a[48:51], v[244:247], v[64:71], v243\n" \
"buffer_load_dwordx4 v234, s[36:39], s73 offen offset:2048 sc1 lds\n" \
"v_mfma_f32_16x16x32_bf16 v[164:167], a[200:203], v[80:83], v[164:167]\n" \
"v_exp_f32 v6, v6\n" \
"v_mfma_f32_16x16x32_bf16 v[164:167], a[204:207], v[84:87], v[164:167]\n" \
"buffer_load_dwordx4 v234, s[36:39], s73 offen offset:3072 sc1 lds\n" \
"v_mfma_f32_16x16x32_bf16 v[132:135], a[200:203], v[64:67], v[132:135]\n" \
"v_exp_f32 v7, v7\n" \
"v_mfma_f32_16x16x32_bf16 v[132:135], a[204:207], v[68:71], v[132:135]\n" \
"ds_read_b128 a[200:203], v232 offset:4096\n" \
"v_cvt_pk_bf16_f32 v3, v6, v7\n" \
"v_mfma_f32_16x16x32_bf16 v[136:139], a[208:211], v[64:67], v[136:139]\n" \
"s_add_u32 m0, m0, 0x4000\n" \
"v_exp_f32 v16, v16\n" \
"v_mfma_f32_16x16x32_bf16 v[136:139], a[212:215], v[68:71], v[136:139]\n" \
"buffer_load_dwordx4 v234, s[36:39], s71 offen offset:0 sc1 lds\n" \
"v_mfma_f32_16x16x32_bf16 v[168:171], a[208:211], v[80:83], v[168:171]\n" \
"v_exp_f32 v17, v17\n" \
"v_mfma_f32_16x16x32_bf16 v[168:171], a[212:215], v[84:87], v[168:171]\n" \
"ds_read_b128 a[204:207], v232 offset:6144\n" \
"v_cvt_pk_bf16_f32 v16, v16, v17\n" \
"v_mfma_f32_16x16x32_bf16 a[72:75], a[208:211], v[96:99], a[72:75]\n" \
"v_exp_f32 v18, v18\n" \
"v_mfma_f32_16x16x32_bf16 a[72:75], a[212:215], v[100:103], a[72:75]\n" \
"buffer_load_dwordx4 v234, s[36:39], s71 offen offset:1024 sc1 lds\n" \
"v_mfma_f32_16x16x32_bf16 a[104:107], a[208:211], v[112:115], a[104:107]\n" \
"v_exp_f32 v19, v19\n" \
"v_mfma_f32_16x16x32_bf16 a[104:107], a[212:215], v[116:119], a[104:107]\n" \
"ds_read_b128 a[208:211], v232 offset:256\n" \
"v_cvt_pk_bf16_f32 v17, v18, v19\n" \
"v_mfma_f32_16x16x32_bf16 a[108:111], a[216:219], v[112:115], a[108:111]\n" \
"v_exp_f32 v20, v20\n" \
"v_mfma_f32_16x16x32_bf16 a[108:111], a[220:223], v[116:119], a[108:111]\n" \
"buffer_load_dwordx4 v234, s[36:39], s71 offen offset:2048 sc1 lds\n" \
"v_mfma_f32_16x16x32_bf16 a[76:79], a[216:219], v[96:99], a[76:79]\n" \
"v_exp_f32 v21, v21\n" \
"v_mfma_f32_16x16x32_bf16 a[76:79], a[220:223], v[100:103], a[76:79]\n" \
"ds_read_b128 a[212:215], v232 offset:2304\n" \
"v_cvt_pk_bf16_f32 v18, v20, v21\n" \
"v_smfmac_f32_16x16x64_bf16 a[52:55], v[244:247], v[80:87], v243\n" \
"buffer_load_dwordx4 v234, s[36:39], s71 offen offset:3072 sc1 lds\n" \
"v_mfma_f32_16x16x32_bf16 v[172:175], a[216:219], v[80:83], v[172:175]\n" \
"s_add_u32 m0, s77, s80\n" \
"v_exp_f32 v22, v22\n" \
"v_mfma_f32_16x16x32_bf16 v[172:175], a[220:223], v[84:87], v[172:175]\n" \
"buffer_load_dwordx4 v234, s[40:43], s76 offen offset:0 sc1 lds\n" \
"v_mfma_f32_16x16x32_bf16 v[140:143], a[216:219], v[64:67], v[140:143]\n" \
"v_exp_f32 v23, v23\n" \
"v_mfma_f32_16x16x32_bf16 v[140:143], a[220:223], v[68:71], v[140:143]\n" \
"ds_read_b128 a[216:219], v232 offset:4352\n" \
"v_cvt_pk_bf16_f32 v19, v22, v23\n" \
"v_mfma_f32_16x16x32_bf16 v[144:147], a[224:227], v[64:67], v[144:147]\n" \
"s_add_u32 s71, s76, 17408\n" \
"v_exp_f32 v32, v32\n" \
"v_mfma_f32_16x16x32_bf16 v[144:147], a[228:231], v[68:71], v[144:147]\n" \
"buffer_load_dwordx4 v234, s[40:43], s76 offen offset:1024 sc1 lds\n" \
"v_mfma_f32_16x16x32_bf16 v[176:179], a[224:227], v[80:83], v[176:179]\n" \
"v_exp_f32 v33, v33\n" \
"v_mfma_f32_16x16x32_bf16 v[176:179], a[228:231], v[84:87], v[176:179]\n" \
"ds_read_b128 a[220:223], v232 offset:6400\n" \
"v_cvt_pk_bf16_f32 v32, v32, v33\n" \
"v_mfma_f32_16x16x32_bf16 a[80:83], a[224:227], v[96:99], a[80:83]\n" \
"v_exp_f32 v34, v34\n" \
"v_mfma_f32_16x16x32_bf16 a[80:83], a[228:231], v[100:103], a[80:83]\n" \
"buffer_load_dwordx4 v234, s[40:43], s76 offen offset:2048 sc1 lds\n" \
"v_mfma_f32_16x16x32_bf16 a[112:115], a[224:227], v[112:115], a[112:115]\n" \
"v_exp_f32 v35, v35\n" \
"v_mfma_f32_16x16x32_bf16 a[112:115], a[228:231], v[116:119], a[112:115]\n" \
"ds_read_b128 a[224:227], v232 offset:8192\n" \
"v_cvt_pk_bf16_f32 v33, v34, v35\n" \
"v_mfma_f32_16x16x32_bf16 a[116:119], a[232:235], v[112:115], a[116:119]\n" \
"v_exp_f32 v36, v36\n" \
"v_mfma_f32_16x16x32_bf16 a[116:119], a[236:239], v[116:119], a[116:119]\n" \
"buffer_load_dwordx4 v234, s[40:43], s76 offen offset:3072 sc1 lds\n" \
"v_mfma_f32_16x16x32_bf16 a[84:87], a[232:235], v[96:99], a[84:87]\n" \
"v_exp_f32 v37, v37\n" \
"v_mfma_f32_16x16x32_bf16 a[84:87], a[236:239], v[100:103], a[84:87]\n" \
"s_add_u32 m0, m0, 0x4000\n" \
"ds_read_b128 a[228:231], v232 offset:10240\n" \
"v_cvt_pk_bf16_f32 v34, v36, v37\n" \
"v_smfmac_f32_16x16x64_bf16 v[224:227], v[244:247], v[96:103], v243\n" \
"buffer_load_dwordx4 v234, s[40:43], s71 offen offset:0 sc1 lds\n" \
"v_mfma_f32_16x16x32_bf16 v[180:183], a[232:235], v[80:83], v[180:183]\n" \
"v_exp_f32 v38, v38\n" \
"v_mfma_f32_16x16x32_bf16 v[180:183], a[236:239], v[84:87], v[180:183]\n" \
"buffer_load_dwordx4 v234, s[40:43], s71 offen offset:1024 sc1 lds\n" \
"v_mfma_f32_16x16x32_bf16 v[148:151], a[232:235], v[64:67], v[148:151]\n" \
"v_exp_f32 v39, v39\n" \
"v_mfma_f32_16x16x32_bf16 v[148:151], a[236:239], v[68:71], v[148:151]\n" \
"ds_read_b128 a[232:235], v232 offset:12288\n" \
"v_cvt_pk_bf16_f32 v35, v38, v39\n" \
"v_mfma_f32_16x16x32_bf16 v[152:155], a[32:35], v[64:67], v[152:155]\n" \
"v_exp_f32 v48, v48\n" \
"v_mfma_f32_16x16x32_bf16 v[152:155], a[36:39], v[68:71], v[152:155]\n" \
"buffer_load_dwordx4 v234, s[40:43], s71 offen offset:2048 sc1 lds\n" \
"v_mfma_f32_16x16x32_bf16 v[184:187], a[32:35], v[80:83], v[184:187]\n" \
"v_exp_f32 v49, v49\n" \
"v_mfma_f32_16x16x32_bf16 v[184:187], a[36:39], v[84:87], v[184:187]\n" \
"ds_read_b128 a[236:239], v232 offset:14336\n" \
"v_cvt_pk_bf16_f32 v48, v48, v49\n" \
"v_mfma_f32_16x16x32_bf16 a[88:91], a[32:35], v[96:99], a[88:91]\n" \
"v_exp_f32 v50, v50\n" \
"v_mfma_f32_16x16x32_bf16 a[88:91], a[36:39], v[100:103], a[88:91]\n" \
"buffer_load_dwordx4 v234, s[40:43], s71 offen offset:3072 sc1 lds\n" \
"v_mfma_f32_16x16x32_bf16 a[120:123], a[32:35], v[112:115], a[120:123]\n" \
"s_mov_b32 s76, s73\n" \
"v_exp_f32 v51, v51\n" \
"v_mfma_f32_16x16x32_bf16 a[120:123], a[36:39], v[116:119], a[120:123]\n" \
"s_add_u32 s73, s73, 34816\n" \
"ds_read_b128 a[244:247], v232 offset:10496\n" \
"v_cvt_pk_bf16_f32 v49, v50, v51\n" \
"v_mfma_f32_16x16x32_bf16 a[124:127], a[40:43], v[112:115], a[124:127]\n" \
"s_xor_b32 s74, s74, 0x8000\n" \
"v_exp_f32 v52, v52\n" \
"v_mfma_f32_16x16x32_bf16 a[124:127], a[44:47], v[116:119], a[124:127]\n" \
"s_xor_b32 s77, s77, 0x8000\n" \
"v_xor_b32 v233, s81, v233\n" \
"s_mov_b32 s81, 0x8000\n" \
"v_mfma_f32_16x16x32_bf16 a[92:95], a[40:43], v[96:99], a[92:95]\n" \
"v_exp_f32 v53, v53\n" \
"v_mfma_f32_16x16x32_bf16 a[92:95], a[44:47], v[100:103], a[92:95]\n" \
"ds_read_b128 a[248:251], v232 offset:12544\n" \
"v_cvt_pk_bf16_f32 v50, v52, v53\n" \
"v_smfmac_f32_16x16x64_bf16 v[228:231], v[244:247], v[112:119], v243\n" \
"v_mfma_f32_16x16x32_bf16 v[188:191], a[40:43], v[80:83], v[188:191]\n" \
"v_exp_f32 v54, v54\n" \
"v_mfma_f32_16x16x32_bf16 v[188:191], a[44:47], v[84:87], v[188:191]\n" \
"s_cmp_lg_u32 s82, 0\n" \
"v_exp_f32 v55, v55\n" \
"v_mfma_f32_16x16x32_bf16 v[156:159], a[40:43], v[64:67], v[156:159]\n" \
"s_waitcnt lgkmcnt(2)\n" \
"ds_read_b128 a[252:255], v232 offset:14592\n" \
"v_cvt_pk_bf16_f32 v51, v54, v55\n" \
"v_mfma_f32_16x16x32_bf16 v[156:159], a[44:47], v[68:71], v[156:159]\n" \
"s_cbranch_scc1 Lep_%=\n" \
"Lepr_%=:\n" \
"v_mfma_f32_16x16x32_bf16 v[64:67], a[192:195], a[0:3], v[196:199]\n" \
"v_exp_f32 v8, v8\n" \
"v_mfma_f32_16x16x32_bf16 v[64:67], a[196:199], a[4:7], v[64:67]\n" \
"ds_read_b128 a[44:47], v233 offset:15360\n" \
"v_mfma_f32_16x16x32_bf16 v[64:67], a[200:203], a[8:11], v[64:67]\n" \
"v_exp_f32 v9, v9\n" \
"v_mfma_f32_16x16x32_bf16 v[64:67], a[204:207], a[12:15], v[64:67]\n" \
"ds_read_b128 a[32:35], v233 offset:6144\n" \
"v_cvt_pk_bf16_f32 v4, v8, v9\n" \
"v_mfma_f32_16x16x32_bf16 v[80:83], a[204:207], a[28:31], v[196:199]\n" \
"s_sub_u32 s84, s84, 1\n" \
"v_exp_f32 v10, v10\n" \
"v_mfma_f32_16x16x32_bf16 v[80:83], a[200:203], a[24:27], v[80:83]\n" \
"s_cmp_eq_u32 s84, 0\n" \
"s_cselect_b32 s70, s75, 0x8000\n" \
"v_xor_b32 v232, s70, v232\n" \
"v_mfma_f32_16x16x32_bf16 v[80:83], a[196:199], a[20:23], v[80:83]\n" \
"v_exp_f32 v11, v11\n" \
"v_mfma_f32_16x16x32_bf16 v[80:83], a[192:195], a[16:19], v[80:83]\n" \
"ds_read_b128 a[36:39], v233 offset:14336\n" \
"v_cvt_pk_bf16_f32 v5, v10, v11\n" \
"v_mfma_f32_16x16x32_bf16 v[96:99], a[192:195], a[160:163], v[196:199]\n" \
"v_exp_f32 v12, v12\n" \
"v_mfma_f32_16x16x32_bf16 v[96:99], a[196:199], a[164:167], v[96:99]\n" \
"v_mfma_f32_16x16x32_bf16 v[96:99], a[200:203], a[168:171], v[96:99]\n" \
"v_exp_f32 v13, v13\n" \
"v_mfma_f32_16x16x32_bf16 v[96:99], a[204:207], a[172:175], v[96:99]\n" \
"ds_read_b128 a[40:43], v233 offset:7168\n" \
"v_cvt_pk_bf16_f32 v6, v12, v13\n" \
"v_mfma_f32_16x16x32_bf16 v[112:115], a[204:207], a[188:191], v[196:199]\n" \
"v_exp_f32 v14, v14\n" \
"v_mfma_f32_16x16x32_bf16 v[112:115], a[200:203], a[184:187], v[112:115]\n" \
"v_mfma_f32_16x16x32_bf16 v[112:115], a[196:199], a[180:183], v[112:115]\n" \
"v_exp_f32 v15, v15\n" \
"v_mfma_f32_16x16x32_bf16 v[112:115], a[192:195], a[176:179], v[112:115]\n" \
"ds_read_b128 a[204:207], v233 offset:9216\n" \
"v_cvt_pk_bf16_f32 v7, v14, v15\n" \
"v_mfma_f32_16x16x32_bf16 v[116:119], a[208:211], a[176:179], v[196:199]\n" \
"v_exp_f32 v24, v24\n" \
"v_mfma_f32_16x16x32_bf16 v[116:119], a[212:215], a[180:183], v[116:119]\n" \
"v_mfma_f32_16x16x32_bf16 v[116:119], a[216:219], a[184:187], v[116:119]\n" \
"v_exp_f32 v25, v25\n" \
"v_mfma_f32_16x16x32_bf16 v[116:119], a[220:223], a[188:191], v[116:119]\n" \
"ds_read_b128 a[200:203], v233 offset:1024\n" \
"v_cvt_pk_bf16_f32 v20, v24, v25\n" \
"v_mfma_f32_16x16x32_bf16 v[100:103], a[220:223], a[172:175], v[196:199]\n" \
"v_exp_f32 v26, v26\n" \
"v_mfma_f32_16x16x32_bf16 v[100:103], a[216:219], a[168:171], v[100:103]\n" \
"v_mfma_f32_16x16x32_bf16 v[100:103], a[212:215], a[164:167], v[100:103]\n" \
"v_exp_f32 v27, v27\n" \
"v_mfma_f32_16x16x32_bf16 v[100:103], a[208:211], a[160:163], v[100:103]\n" \
"ds_read_b128 a[196:199], v233 offset:8192\n" \
"v_cvt_pk_bf16_f32 v21, v26, v27\n" \
"v_mfma_f32_16x16x32_bf16 v[84:87], a[208:211], a[16:19], v[196:199]\n" \
"v_exp_f32 v28, v28\n" \
"v_mfma_f32_16x16x32_bf16 v[84:87], a[212:215], a[20:23], v[84:87]\n" \
"v_mfma_f32_16x16x32_bf16 v[84:87], a[216:219], a[24:27], v[84:87]\n" \
"v_exp_f32 v29, v29\n" \
"v_mfma_f32_16x16x32_bf16 v[84:87], a[220:223], a[28:31], v[84:87]\n" \
"ds_read_b128 a[192:195], v233 offset:0\n" \
"v_cvt_pk_bf16_f32 v22, v28, v29\n" \
"v_mfma_f32_16x16x32_bf16 v[68:71], a[220:223], a[12:15], v[196:199]\n" \
"v_exp_f32 v30, v30\n" \
"v_mfma_f32_16x16x32_bf16 v[68:71], a[216:219], a[8:11], v[68:71]\n" \
"v_mfma_f32_16x16x32_bf16 v[68:71], a[212:215], a[4:7], v[68:71]\n" \
"s_waitcnt lgkmcnt(8)\n" \
"v_exp_f32 v31, v31\n" \
"v_mfma_f32_16x16x32_bf16 v[68:71], a[208:211], a[0:3], v[68:71]\n" \
"ds_read_b128 a[220:223], v233 offset:11264\n" \
"v_cvt_pk_bf16_f32 v23, v30, v31\n" \
"v_mfma_f32_16x16x32_bf16 v[72:75], a[224:227], a[0:3], v[196:199]\n" \
"v_exp_f32 v40, v40\n" \
"v_mfma_f32_16x16x32_bf16 v[72:75], a[228:231], a[4:7], v[72:75]\n" \
"v_mfma_f32_16x16x32_bf16 v[72:75], a[232:235], a[8:11], v[72:75]\n" \
"v_exp_f32 v41, v41\n" \
"v_mfma_f32_16x16x32_bf16 v[72:75], a[236:239], a[12:15], v[72:75]\n" \
"ds_read_b128 a[216:219], v233 offset:3072\n" \
"v_cvt_pk_bf16_f32 v36, v40, v41\n" \
"v_mfma_f32_16x16x32_bf16 v[88:91], a[236:239], a[28:31], v[196:199]\n" \
"v_exp_f32 v42, v42\n" \
"v_mfma_f32_16x16x32_bf16 v[88:91], a[232:235], a[24:27], v[88:91]\n" \
"v_mfma_f32_16x16x32_bf16 v[88:91], a[228:231], a[20:23], v[88:91]\n" \
"v_exp_f32 v43, v43\n" \
"v_mfma_f32_16x16x32_bf16 v[88:91], a[224:227], a[16:19], v[88:91]\n" \
"ds_read_b128 a[212:215], v233 offset:10240\n" \
"v_cvt_pk_bf16_f32 v37, v42, v43\n" \
"v_mfma_f32_16x16x32_bf16 v[104:107], a[224:227], a[160:163], v[196:199]\n" \
"v_exp_f32 v44, v44\n" \
"v_mfma_f32_16x16x32_bf16 v[104:107], a[228:231], a[164:167], v[104:107]\n" \
"v_mfma_f32_16x16x32_bf16 v[104:107], a[232:235], a[168:171], v[104:107]\n" \
"v_exp_f32 v45, v45\n" \
"v_mfma_f32_16x16x32_bf16 v[104:107], a[236:239], a[172:175], v[104:107]\n" \
"ds_read_b128 a[208:211], v233 offset:2048\n" \
"v_cvt_pk_bf16_f32 v38, v44, v45\n" \
"v_mfma_f32_16x16x32_bf16 v[120:123], a[236:239], a[188:191], v[196:199]\n" \
"v_exp_f32 v46, v46\n" \
"v_mfma_f32_16x16x32_bf16 v[120:123], a[232:235], a[184:187], v[120:123]\n" \
"v_mfma_f32_16x16x32_bf16 v[120:123], a[228:231], a[180:183], v[120:123]\n" \
"v_exp_f32 v47, v47\n" \
"v_mfma_f32_16x16x32_bf16 v[120:123], a[224:227], a[176:179], v[120:123]\n" \
"ds_read_b128 a[236:239], v233 offset:13312\n" \
"v_cvt_pk_bf16_f32 v39, v46, v47\n" \
"v_mfma_f32_16x16x32_bf16 v[124:127], a[240:243], a[176:179], v[196:199]\n" \
"v_exp_f32 v56, v56\n" \
"v_mfma_f32_16x16x32_bf16 v[124:127], a[244:247], a[180:183], v[124:127]\n" \
"ds_read_b128 a[232:235], v233 offset:5120\n" \
"v_mfma_f32_16x16x32_bf16 v[124:127], a[248:251], a[184:187], v[124:127]\n" \
"v_exp_f32 v57, v57\n" \
"v_mfma_f32_16x16x32_bf16 v[124:127], a[252:255], a[188:191], v[124:127]\n" \
"ds_read_b128 a[228:231], v233 offset:12288\n" \
"v_cvt_pk_bf16_f32 v52, v56, v57\n" \
"v_mfma_f32_16x16x32_bf16 v[108:111], a[252:255], a[172:175], v[196:199]\n" \
"v_exp_f32 v58, v58\n" \
"v_mfma_f32_16x16x32_bf16 v[108:111], a[248:251], a[168:171], v[108:111]\n" \
"ds_read_b128 a[224:227], v233 offset:4096\n" \
"v_mfma_f32_16x16x32_bf16 v[108:111], a[244:247], a[164:167], v[108:111]\n" \
"v_exp_f32 v59, v59\n" \
"v_mfma_f32_16x16x32_bf16 v[108:111], a[240:243], a[160:163], v[108:111]\n" \
"v_cvt_pk_bf16_f32 v53, v58, v59\n" \
"v_mfma_f32_16x16x32_bf16 v[92:95], a[240:243], a[16:19], v[196:199]\n" \
"v_exp_f32 v60, v60\n" \
"v_mfma_f32_16x16x32_bf16 v[92:95], a[244:247], a[20:23], v[92:95]\n" \
"v_mfma_f32_16x16x32_bf16 v[92:95], a[248:251], a[24:27], v[92:95]\n" \
"v_exp_f32 v61, v61\n" \
"v_mfma_f32_16x16x32_bf16 v[92:95], a[252:255], a[28:31], v[92:95]\n" \
"v_cvt_pk_bf16_f32 v54, v60, v61\n" \
"v_mfma_f32_16x16x32_bf16 v[76:79], a[252:255], a[12:15], v[196:199]\n" \
"v_exp_f32 v62, v62\n" \
"v_mfma_f32_16x16x32_bf16 v[76:79], a[248:251], a[8:11], v[76:79]\n" \
"s_cmp_lg_u32 s82, 0\n" \
"s_cbranch_scc1 Lnw_%=\n" \
"s_waitcnt vmcnt(0)\n" \
"Lnw_%=:\n" \
"v_mfma_f32_16x16x32_bf16 v[76:79], a[244:247], a[4:7], v[76:79]\n" \
"s_waitcnt lgkmcnt(0)\n" \
"v_exp_f32 v63, v63\n" \
"v_mfma_f32_16x16x32_bf16 v[76:79], a[240:243], a[0:3], v[76:79]\n" \
"s_barrier\n" \
"v_cvt_pk_bf16_f32 v55, v62, v63\n" \
"v_mfma_f32_16x16x32_bf16 v[128:131], a[192:195], v[0:3], v[128:131]\n" \
"s_add_u32 m0, s74, s79\n" \
"v_exp_f32 v64, v64\n" \
"v_mfma_f32_16x16x32_bf16 v[128:131], a[196:199], v[4:7], v[128:131]\n" \
"buffer_load_dwordx4 v234, s[36:39], s73 offen offset:0 sc1 lds\n" \
"v_mfma_f32_16x16x32_bf16 v[160:163], a[192:195], v[16:19], v[160:163]\n" \
"v_exp_f32 v65, v65\n" \
"v_mfma_f32_16x16x32_bf16 v[160:163], a[196:199], v[20:23], v[160:163]\n" \
"ds_read_b128 a[240:243], v232 offset:8448\n" \
"v_cvt_pk_bf16_f32 v64, v64, v65\n" \
"v_mfma_f32_16x16x32_bf16 a[64:67], a[192:195], v[32:35], a[64:67]\n" \
"s_add_u32 s71, s73, 17408\n" \
"v_exp_f32 v66, v66\n" \
"v_mfma_f32_16x16x32_bf16 a[64:67], a[196:199], v[36:39], a[64:67]\n" \
"buffer_load_dwordx4 v234, s[36:39], s73 offen offset:1024 sc1 lds\n" \
"v_mfma_f32_16x16x32_bf16 a[96:99], a[192:195], v[48:51], a[96:99]\n" \
"v_exp_f32 v67, v67\n" \
"v_mfma_f32_16x16x32_bf16 a[96:99], a[196:199], v[52:55], a[96:99]\n" \
"ds_read_b128 a[192:195], v232 offset:0\n" \
"v_cvt_pk_bf16_f32 v65, v66, v67\n" \
"v_mfma_f32_16x16x32_bf16 a[100:103], a[200:203], v[48:51], a[100:103]\n" \
"v_exp_f32 v68, v68\n" \
"v_mfma_f32_16x16x32_bf16 a[100:103], a[204:207], v[52:55], a[100:103]\n" \
"buffer_load_dwordx4 v234, s[36:39], s73 offen offset:2048 sc1 lds\n" \
"v_mfma_f32_16x16x32_bf16 a[68:71], a[200:203], v[32:35], a[68:71]\n" \
"v_exp_f32 v69, v69\n" \
"v_mfma_f32_16x16x32_bf16 a[68:71], a[204:207], v[36:39], a[68:71]\n" \
"ds_read_b128 a[196:199], v232 offset:2048\n" \
"v_cvt_pk_bf16_f32 v66, v68, v69\n" \
"v_smfmac_f32_16x16x64_bf16 a[48:51], v[244:247], v[0:7], v243\n" \
"buffer_load_dwordx4 v234, s[36:39], s73 offen offset:3072 sc1 lds\n" \
"v_mfma_f32_16x16x32_bf16 v[164:167], a[200:203], v[16:19], v[164:167]\n" \
"s_add_u32 m0, m0, 0x4000\n" \
"v_exp_f32 v70, v70\n" \
"v_mfma_f32_16x16x32_bf16 v[164:167], a[204:207], v[20:23], v[164:167]\n" \
"buffer_load_dwordx4 v234, s[36:39], s71 offen offset:0 sc1 lds\n" \
"v_mfma_f32_16x16x32_bf16 v[132:135], a[200:203], v[0:3], v[132:135]\n" \
"v_exp_f32 v71, v71\n" \
"v_mfma_f32_16x16x32_bf16 v[132:135], a[204:207], v[4:7], v[132:135]\n" \
"ds_read_b128 a[200:203], v232 offset:4096\n" \
"v_cvt_pk_bf16_f32 v67, v70, v71\n" \
"v_mfma_f32_16x16x32_bf16 v[136:139], a[208:211], v[0:3], v[136:139]\n" \
"v_exp_f32 v80, v80\n" \
"v_mfma_f32_16x16x32_bf16 v[136:139], a[212:215], v[4:7], v[136:139]\n" \
"buffer_load_dwordx4 v234, s[36:39], s71 offen offset:1024 sc1 lds\n" \
"v_mfma_f32_16x16x32_bf16 v[168:171], a[208:211], v[16:19], v[168:171]\n" \
"v_exp_f32 v81, v81\n" \
"v_mfma_f32_16x16x32_bf16 v[168:171], a[212:215], v[20:23], v[168:171]\n" \
"ds_read_b128 a[204:207], v232 offset:6144\n" \
"v_cvt_pk_bf16_f32 v80, v80, v81\n" \
"v_mfma_f32_16x16x32_bf16 a[72:75], a[208:211], v[32:35], a[72:75]\n" \
"v_exp_f32 v82, v82\n" \
"v_mfma_f32_16x16x32_bf16 a[72:75], a[212:215], v[36:39], a[72:75]\n" \
"buffer_load_dwordx4 v234, s[36:39], s71 offen offset:2048 sc1 lds\n" \
"v_mfma_f32_16x16x32_bf16 a[104:107], a[208:211], v[48:51], a[104:107]\n" \
"v_exp_f32 v83, v83\n" \
"v_mfma_f32_16x16x32_bf16 a[104:107], a[212:215], v[52:55], a[104:107]\n" \
"ds_read_b128 a[208:211], v232 offset:256\n" \
"v_cvt_pk_bf16_f32 v81, v82, v83\n" \
"v_mfma_f32_16x16x32_bf16 a[108:111], a[216:219], v[48:51], a[108:111]\n" \
"v_exp_f32 v84, v84\n" \
"v_mfma_f32_16x16x32_bf16 a[108:111], a[220:223], v[52:55], a[108:111]\n" \
"buffer_load_dwordx4 v234, s[36:39], s71 offen offset:3072 sc1 lds\n" \
"v_mfma_f32_16x16x32_bf16 a[76:79], a[216:219], v[32:35], a[76:79]\n" \
"v_exp_f32 v85, v85\n" \
"v_mfma_f32_16x16x32_bf16 a[76:79], a[220:223], v[36:39], a[76:79]\n" \
"s_add_u32 m0, s77, s80\n" \
"ds_read_b128 a[212:215], v232 offset:2304\n" \
"v_cvt_pk_bf16_f32 v82, v84, v85\n" \
"v_smfmac_f32_16x16x64_bf16 a[52:55], v[244:247], v[16:23], v243\n" \
"buffer_load_dwordx4 v234, s[40:43], s76 offen offset:0 sc1 lds\n" \
"v_mfma_f32_16x16x32_bf16 v[172:175], a[216:219], v[16:19], v[172:175]\n" \
"s_add_u32 s71, s76, 17408\n" \
"v_exp_f32 v86, v86\n" \
"v_mfma_f32_16x16x32_bf16 v[172:175], a[220:223], v[20:23], v[172:175]\n" \
"buffer_load_dwordx4 v234, s[40:43], s76 offen offset:1024 sc1 lds\n" \
"v_mfma_f32_16x16x32_bf16 v[140:143], a[216:219], v[0:3], v[140:143]\n" \
"v_exp_f32 v87, v87\n" \
"v_mfma_f32_16x16x32_bf16 v[140:143], a[220:223], v[4:7], v[140:143]\n" \
"ds_read_b128 a[216:219], v232 offset:4352\n" \
"v_cvt_pk_bf16_f32 v83, v86, v87\n" \
"v_mfma_f32_16x16x32_bf16 v[144:147], a[224:227], v[0:3], v[144:147]\n" \
"v_exp_f32 v96, v96\n" \
"v_mfma_f32_16x16x32_bf16 v[144:147], a[228:231], v[4:7], v[144:147]\n" \
"buffer_load_dwordx4 v234, s[40:43], s76 offen offset:2048 sc1 lds\n" \
"v_mfma_f32_16x16x32_bf16 v[176:179], a[224:227], v[16:19], v[176:179]\n" \
"v_exp_f32 v97, v97\n" \
"v_mfma_f32_16x16x32_bf16 v[176:179], a[228:231], v[20:23], v[176:179]\n" \
"ds_read_b128 a[220:223], v232 offset:6400\n" \
"v_cvt_pk_bf16_f32 v96, v96, v97\n" \
"v_mfma_f32_16x16x32_bf16 a[80:83], a[224:227], v[32:35], a[80:83]\n" \
"v_exp_f32 v98, v98\n" \
"v_mfma_f32_16x16x32_bf16 a[80:83], a[228:231], v[36:39], a[80:83]\n" \
"buffer_load_dwordx4 v234, s[40:43], s76 offen offset:3072 sc1 lds\n" \
"v_mfma_f32_16x16x32_bf16 a[112:115], a[224:227], v[48:51], a[112:115]\n" \
"v_exp_f32 v99, v99\n" \
"v_mfma_f32_16x16x32_bf16 a[112:115], a[228:231], v[52:55], a[112:115]\n" \
"ds_read_b128 a[224:227], v232 offset:8192\n" \
"v_cvt_pk_bf16_f32 v97, v98, v99\n" \
"v_mfma_f32_16x16x32_bf16 a[116:119], a[232:235], v[48:51], a[116:119]\n" \
"s_add_u32 m0, m0, 0x4000\n" \
"v_exp_f32 v100, v100\n" \
"v_mfma_f32_16x16x32_bf16 a[116:119], a[236:239], v[52:55], a[116:119]\n" \
"buffer_load_dwordx4 v234, s[40:43], s71 offen offset:0 sc1 lds\n" \
"v_mfma_f32_16x16x32_bf16 a[84:87], a[232:235], v[32:35], a[84:87]\n" \
"v_exp_f32 v101, v101\n" \
"v_mfma_f32_16x16x32_bf16 a[84:87], a[236:239], v[36:39], a[84:87]\n" \
"ds_read_b128 a[228:231], v232 offset:10240\n" \
"v_cvt_pk_bf16_f32 v98, v100, v101\n" \
"v_smfmac_f32_16x16x64_bf16 v[224:227], v[244:247], v[32:39], v243\n" \
"buffer_load_dwordx4 v234, s[40:43], s71 offen offset:1024 sc1 lds\n" \
"v_mfma_f32_16x16x32_bf16 v[180:183], a[232:235], v[16:19], v[180:183]\n" \
"v_exp_f32 v102, v102\n" \
"v_mfma_f32_16x16x32_bf16 v[180:183], a[236:239], v[20:23], v[180:183]\n" \
"buffer_load_dwordx4 v234, s[40:43], s71 offen offset:2048 sc1 lds\n" \
"v_mfma_f32_16x16x32_bf16 v[148:151], a[232:235], v[0:3], v[148:151]\n" \
"v_exp_f32 v103, v103\n" \
"v_mfma_f32_16x16x32_bf16 v[148:151], a[236:239], v[4:7], v[148:151]\n" \
"ds_read_b128 a[232:235], v232 offset:12288\n" \
"v_cvt_pk_bf16_f32 v99, v102, v103\n" \
"v_mfma_f32_16x16x32_bf16 v[152:155], a[32:35], v[0:3], v[152:155]\n" \
"v_exp_f32 v112, v112\n" \
"v_mfma_f32_16x16x32_bf16 v[152:155], a[36:39], v[4:7], v[152:155]\n" \
"buffer_load_dwordx4 v234, s[40:43], s71 offen offset:3072 sc1 lds\n" \
"v_mfma_f32_16x16x32_bf16 v[184:187], a[32:35], v[16:19], v[184:187]\n" \
"v_exp_f32 v113, v113\n" \
"v_mfma_f32_16x16x32_bf16 v[184:187], a[36:39], v[20:23], v[184:187]\n" \
"ds_read_b128 a[236:239], v232 offset:14336\n" \
"v_cvt_pk_bf16_f32 v112, v112, v113\n" \
"v_mfma_f32_16x16x32_bf16 a[88:91], a[32:35], v[32:35], a[88:91]\n" \
"v_exp_f32 v114, v114\n" \
"v_mfma_f32_16x16x32_bf16 a[88:91], a[36:39], v[36:39], a[88:91]\n" \
"buffer_load_dword v239, v242, s[56:59], s95 offen sc1\n" \
"v_mfma_f32_16x16x32_bf16 a[120:123], a[32:35], v[48:51], a[120:123]\n" \
"s_mov_b32 s69, s92\n" \
"v_exp_f32 v115, v115\n" \
"v_mfma_f32_16x16x32_bf16 a[120:123], a[36:39], v[52:55], a[120:123]\n" \
"s_mov_b32 s76, s73\n" \
"ds_read_b128 a[244:247], v232 offset:10496\n" \
"v_cvt_pk_bf16_f32 v113, v114, v115\n" \
"v_mfma_f32_16x16x32_bf16 a[124:127], a[40:43], v[48:51], a[124:127]\n" \
"s_add_u32 s73, s73, 34816\n" \
"v_exp_f32 v116, v116\n" \
"v_mfma_f32_16x16x32_bf16 a[124:127], a[44:47], v[52:55], a[124:127]\n" \
"s_xor_b32 s74, s74, 0x8000\n" \
"s_xor_b32 s77, s77, 0x8000\n" \
"v_xor_b32 v233, 0x8000, v233\n" \
"v_mfma_f32_16x16x32_bf16 a[92:95], a[40:43], v[32:35], a[92:95]\n" \
"s_mov_b32 s82, 0\n" \
"v_exp_f32 v117, v117\n" \
"v_mfma_f32_16x16x32_bf16 a[92:95], a[44:47], v[36:39], a[92:95]\n" \
"ds_read_b128 a[248:251], v232 offset:12544\n" \
"v_cvt_pk_bf16_f32 v114, v116, v117\n" \
"v_smfmac_f32_16x16x64_bf16 v[228:231], v[244:247], v[48:55], v243\n" \
"v_mfma_f32_16x16x32_bf16 v[188:191], a[40:43], v[16:19], v[188:191]\n" \
"v_exp_f32 v118, v118\n" \
"v_mfma_f32_16x16x32_bf16 v[188:191], a[44:47], v[20:23], v[188:191]\n" \
"s_cmp_lg_u32 s84, 0\n" \
"v_exp_f32 v119, v119\n" \
"v_mfma_f32_16x16x32_bf16 v[156:159], a[40:43], v[0:3], v[156:159]\n" \
"s_waitcnt lgkmcnt(2)\n" \
"ds_read_b128 a[252:255], v232 offset:14592\n" \
"Lpg6793_%=:\n" \
"s_cbranch_execz Lcb0_%=\n" \
"v_cvt_pk_bf16_f32 v115, v118, v119\n" \
"v_mfma_f32_16x16x32_bf16 v[156:159], a[44:47], v[4:7], v[156:159]\n" \
"s_cbranch_scc1 Lloop2_%=\n" \
"Lg_%=:\n" \
"Lpg6798_%=:\n" \
"s_cbranch_execz Lcb0_%=\n" \
"v_mfma_f32_16x16x32_bf16 v[32:35], a[192:195], a[160:163], v[208:211]\n" \
"s_mov_b64 s[44:45], s[20:21]\n" \
"s_memrealtime s[14:15]\n" \
"v_exp_f32 v72, v72\n" \
"v_mfma_f32_16x16x32_bf16 v[32:35], a[196:199], a[164:167], v[32:35]\n" \
"s_and_b32 s45, s45, 0xffff\n" \
"ds_read_b128 a[32:35], v233 offset:6144\n" \
"ds_read_b128 a[36:39], v233 offset:14336\n" \
"v_mfma_f32_16x16x32_bf16 v[32:35], a[200:203], a[168:171], v[32:35]\n" \
"s_mov_b32 s46, 4194304\n" \
"v_exp_f32 v73, v73\n" \
"v_mfma_f32_16x16x32_bf16 v[32:35], a[204:207], a[172:175], v[32:35]\n" \
"ds_read_b128 a[40:43], v233 offset:7168\n" \
"ds_read_b128 a[44:47], v233 offset:15360\n" \
"global_load_dword v253, v254, s[22:23]\n" \
"v_mfma_f32_16x16x32_bf16 v[48:51], a[204:207], a[188:191], v[196:199]\n" \
"s_mov_b32 s47, 0x20000\n" \
"v_exp_f32 v74, v74\n" \
"v_mfma_f32_16x16x32_bf16 v[48:51], a[200:203], a[184:187], v[48:51]\n" \
"s_mov_b64 s[48:49], s[22:23]\n" \
"v_exp_f32 v75, v75\n" \
"v_mfma_f32_16x16x32_bf16 v[48:51], a[196:199], a[180:183], v[48:51]\n" \
"s_and_b32 s49, s49, 0xffff\n" \
"v_exp_f32 v76, v76\n" \
"v_mfma_f32_16x16x32_bf16 v[48:51], a[192:195], a[176:179], v[48:51]\n" \
"s_mov_b32 s50, 6291456\n" \
"ds_read_b128 a[200:203], v233 offset:1024\n" \
"ds_read_b128 a[204:207], v233 offset:9216\n" \
"v_mfma_f32_16x16x32_bf16 v[52:55], a[208:211], a[176:179], v[196:199]\n" \
"s_mov_b32 s51, 0x20000\n" \
"v_cvt_pk_bf16_f32 v68, v72, v73\n" \
"v_cvt_pk_bf16_f32 v69, v74, v75\n" \
"v_mfma_f32_16x16x32_bf16 v[52:55], a[212:215], a[180:183], v[52:55]\n" \
"s_bfe_u32 s94, %1, 0x20001\n" \
"v_exp_f32 v77, v77\n" \
"v_mfma_f32_16x16x32_bf16 v[52:55], a[216:219], a[184:187], v[52:55]\n" \
"s_lshl_b32 s94, s94, 2\n" \
"v_exp_f32 v78, v78\n" \
"v_mfma_f32_16x16x32_bf16 v[52:55], a[220:223], a[188:191], v[52:55]\n" \
"s_lshr_b32 s70, %1, 6\n" \
"ds_read_b128 a[192:195], v233 offset:0\n" \
"ds_read_b128 a[196:199], v233 offset:8192\n" \
"v_mfma_f32_16x16x32_bf16 v[36:39], a[220:223], a[172:175], v[212:215]\n" \
"s_add_u32 s94, s94, s70\n" \
"v_exp_f32 v79, v79\n" \
"v_mfma_f32_16x16x32_bf16 v[36:39], a[216:219], a[168:171], v[36:39]\n" \
"s_lshr_b32 s71, s22, 7\n" \
"v_exp_f32 v88, v88\n" \
"v_mfma_f32_16x16x32_bf16 v[36:39], a[212:215], a[164:167], v[36:39]\n" \
"s_sub_u32 s71, s63, s71\n" \
"v_cvt_pk_bf16_f32 v70, v76, v77\n" \
"v_cvt_pk_bf16_f32 v71, v78, v79\n" \
"v_mfma_f32_16x16x32_bf16 v[36:39], a[208:211], a[160:163], v[36:39]\n" \
"s_add_u32 s71, s71, 6\n" \
"ds_read_b128 a[216:219], v233 offset:3072\n" \
"ds_read_b128 a[220:223], v233 offset:11264\n" \
"v_mfma_f32_16x16x32_bf16 v[40:43], a[224:227], a[160:163], v[216:219]\n" \
"s_and_b32 s71, s71, 7\n" \
"v_exp_f32 v89, v89\n" \
"v_mfma_f32_16x16x32_bf16 v[40:43], a[228:231], a[164:167], v[40:43]\n" \
"s_and_b32 s70, %1, 1\n" \
"v_exp_f32 v90, v90\n" \
"v_mfma_f32_16x16x32_bf16 v[40:43], a[232:235], a[168:171], v[40:43]\n" \
"s_lshl_b32 s70, s70, 3\n" \
"v_exp_f32 v91, v91\n" \
"v_mfma_f32_16x16x32_bf16 v[40:43], a[236:239], a[172:175], v[40:43]\n" \
"s_add_u32 s70, s70, s71\n" \
"ds_read_b128 a[208:211], v233 offset:2048\n" \
"ds_read_b128 a[212:215], v233 offset:10240\n" \
"v_mfma_f32_16x16x32_bf16 v[56:59], a[236:239], a[188:191], v[196:199]\n" \
"s_lshl_b32 s78, s70, 7\n" \
"v_exp_f32 v92, v92\n" \
"v_mfma_f32_16x16x32_bf16 v[56:59], a[232:235], a[184:187], v[56:59]\n" \
"s_lshr_b32 s80, %1, 6\n" \
"v_cvt_pk_bf16_f32 v84, v88, v89\n" \
"v_cvt_pk_bf16_f32 v85, v90, v91\n" \
"v_mfma_f32_16x16x32_bf16 v[56:59], a[228:231], a[180:183], v[56:59]\n" \
"s_xor_b32 s80, s80, s63\n" \
"v_exp_f32 v93, v93\n" \
"v_mfma_f32_16x16x32_bf16 v[56:59], a[224:227], a[176:179], v[56:59]\n" \
"s_and_b32 s80, s80, 0\n" \
"ds_read_b128 a[232:235], v233 offset:5120\n" \
"ds_read_b128 a[236:239], v233 offset:13312\n" \
"v_mfma_f32_16x16x32_bf16 v[60:63], a[240:243], a[176:179], v[196:199]\n" \
"s_waitcnt lgkmcnt(2)\n" \
"s_lshl_b32 s93, s70, 12\n" \
"v_exp_f32 v94, v94\n" \
"v_mfma_f32_16x16x32_bf16 v[60:63], a[244:247], a[180:183], v[60:63]\n" \
"s_mul_i32 s91, s61, 6144\n" \
"v_exp_f32 v95, v95\n" \
"v_mfma_f32_16x16x32_bf16 v[60:63], a[248:251], a[184:187], v[60:63]\n" \
"s_add_u32 s91, s91, %3\n" \
"v_cvt_pk_bf16_f32 v86, v92, v93\n" \
"v_mfma_f32_16x16x32_bf16 v[60:63], a[252:255], a[188:191], v[60:63]\n" \
"s_lshl_b32 s71, s80, 7\n" \
"ds_read_b128 a[224:227], v233 offset:4096\n" \
"ds_read_b128 a[228:231], v233 offset:12288\n" \
"v_mfma_f32_16x16x32_bf16 v[44:47], a[252:255], a[172:175], v[220:223]\n" \
"s_lshl_b32 s75, s94, 18\n" \
"v_cvt_pk_bf16_f32 v87, v94, v95\n" \
"v_mfma_f32_16x16x32_bf16 v[44:47], a[248:251], a[168:171], v[44:47]\n" \
"s_lshl_b32 s70, s61, 16\n" \
"v_exp_f32 v104, v104\n" \
"v_mfma_f32_16x16x32_bf16 v[44:47], a[244:247], a[164:167], v[44:47]\n" \
"s_add_u32 s73, s75, s70\n" \
"v_accvgpr_read_b32 v197, a56\n" \
"v_accvgpr_read_b32 v192, a57\n" \
"v_mfma_f32_16x16x32_bf16 v[44:47], a[240:243], a[160:163], v[44:47]\n" \
"s_add_u32 s73, s73, s71\n" \
"v_accvgpr_read_b32 v196, a58\n" \
"v_add_u32 v198, 1024, v197\n" \
"v_mfma_f32_16x16x32_bf16 v[128:131], a[192:195], v[64:67], v[128:131]\n" \
"s_max_u32 s75, s75, 32768\n" \
"v_add_u32 v199, 2048, v197\n" \
"v_add_u32 v193, 31744, v192\n" \
"v_mfma_f32_16x16x32_bf16 v[128:131], a[196:199], v[68:71], v[128:131]\n" \
"s_sub_u32 s75, s75, 32768\n" \
"ds_read_b128 a[0:3], v232 offset:16384\n" \
"ds_read_b128 a[4:7], v232 offset:18432\n" \
"v_mfma_f32_16x16x32_bf16 v[160:163], a[192:195], v[80:83], v[160:163]\n" \
"s_add_u32 s75, s75, s71\n" \
"s_lshl_b32 s89, s61, 10\n" \
"v_mfma_f32_16x16x32_bf16 v[160:163], a[196:199], v[84:87], v[160:163]\n" \
"s_mul_i32 s76, s80, 393216\n" \
"s_add_u32 s89, s89, %3\n" \
"v_exp_f32 v105, v105\n" \
"v_mfma_f32_16x16x32_bf16 v[164:167], a[200:203], v[80:83], v[164:167]\n" \
"s_add_u32 s76, s76, s78\n" \
"ds_read_b128 a[8:11], v232 offset:20480\n" \
"ds_read_b128 a[12:15], v232 offset:22528\n" \
"v_mfma_f32_16x16x32_bf16 v[164:167], a[204:207], v[84:87], v[164:167]\n" \
"s_add_u32 m0, s91, 139264\n" \
"s_add_u32 s89, s89, 16384\n" \
"v_exp_f32 v106, v106\n" \
"v_mfma_f32_16x16x32_bf16 v[132:135], a[200:203], v[64:67], v[132:135]\n" \
"buffer_load_dwordx4 v197, s[48:51], s76 offen offset:0 lds\n" \
"v_mfma_f32_16x16x32_bf16 v[132:135], a[204:207], v[68:71], v[132:135]\n" \
"s_and_b32 s70, s61, 1\n" \
"ds_read_b128 a[16:19], v232 offset:16640\n" \
"ds_read_b128 a[20:23], v232 offset:18688\n" \
"v_smfmac_f32_16x16x64_bf16 a[48:51], v[244:247], v[64:71], v243\n" \
"s_add_u32 s77, s76, 24576\n" \
"v_cvt_pk_bf16_f32 v100, v104, v105\n" \
"v_mfma_f32_16x16x32_bf16 v[136:139], a[208:211], v[64:67], v[136:139]\n" \
"buffer_load_dwordx4 v198, s[48:51], s76 offen offset:1024 lds\n" \
"v_mfma_f32_16x16x32_bf16 v[136:139], a[212:215], v[68:71], v[136:139]\n" \
"s_lshl_b32 s70, s70, 11\n" \
"ds_read_b128 a[24:27], v232 offset:20736\n" \
"ds_read_b128 a[28:31], v232 offset:22784\n" \
"v_mfma_f32_16x16x32_bf16 v[168:171], a[208:211], v[80:83], v[168:171]\n" \
"s_xor_b32 s80, s80, 0x3\n" \
"v_exp_f32 v107, v107\n" \
"v_mfma_f32_16x16x32_bf16 v[168:171], a[212:215], v[84:87], v[168:171]\n" \
"buffer_load_dwordx4 v199, s[48:51], s76 offen offset:2048 lds\n" \
"v_mfma_f32_16x16x32_bf16 v[172:175], a[216:219], v[80:83], v[172:175]\n" \
"s_add_u32 s93, s93, s70\n" \
"ds_read_b128 v[0:3], v232 offset:24576\n" \
"ds_read_b128 v[4:7], v232 offset:26624\n" \
"v_mfma_f32_16x16x32_bf16 v[172:175], a[220:223], v[84:87], v[172:175]\n" \
"s_add_u32 m0, m0, 3072\n" \
"v_exp_f32 v108, v108\n" \
"v_mfma_f32_16x16x32_bf16 v[140:143], a[216:219], v[64:67], v[140:143]\n" \
"buffer_load_dwordx4 v197, s[48:51], s77 offen offset:0 lds\n" \
"v_mfma_f32_16x16x32_bf16 v[140:143], a[220:223], v[68:71], v[140:143]\n" \
"s_add_u32 s93, s93, 52740096\n" \
"ds_read_b128 v[8:11], v232 offset:28672\n" \
"ds_read_b128 v[12:15], v232 offset:30720\n" \
"v_exp_f32 v109, v109\n" \
"v_smfmac_f32_16x16x64_bf16 a[52:55], v[244:247], v[80:87], v243\n" \
"s_waitcnt lgkmcnt(4)\n" \
"s_cmp_eq_u32 s94, 0\n" \
"s_cselect_b32 s92, -1, 0\n" \
"v_cvt_pk_bf16_f32 v101, v106, v107\n" \
"v_mfma_f32_16x16x32_bf16 v[144:147], a[224:227], v[64:67], v[144:147]\n" \
"buffer_load_dwordx4 v198, s[48:51], s77 offen offset:1024 lds\n" \
"v_mfma_f32_16x16x32_bf16 v[144:147], a[228:231], v[68:71], v[144:147]\n" \
"s_mul_i32 s70, s94, 278528\n" \
"ds_read_b128 v[200:203], v232 offset:24832\n" \
"ds_read_b128 v[204:207], v232 offset:26880\n" \
"v_mfma_f32_16x16x32_bf16 v[176:179], a[224:227], v[80:83], v[176:179]\n" \
"s_add_u32 s93, s93, s70\n" \
"v_exp_f32 v110, v110\n" \
"v_mfma_f32_16x16x32_bf16 v[176:179], a[228:231], v[84:87], v[176:179]\n" \
"buffer_load_dwordx4 v199, s[48:51], s77 offen offset:2048 lds\n" \
"v_mfma_f32_16x16x32_bf16 v[180:183], a[232:235], v[80:83], v[180:183]\n" \
"s_lshr_b32 s70, s61, 1\n" \
"ds_read_b128 a[240:243], v232 offset:28928\n" \
"ds_read_b128 a[244:247], v232 offset:30976\n" \
"v_mfma_f32_16x16x32_bf16 v[180:183], a[236:239], v[84:87], v[180:183]\n" \
"s_lshl_b32 s70, s70, 8\n" \
"v_exp_f32 v111, v111\n" \
"v_mfma_f32_16x16x32_bf16 v[148:151], a[232:235], v[64:67], v[148:151]\n" \
"s_add_u32 s93, s93, s70\n" \
"v_exp_f32 v120, v120\n" \
"v_mfma_f32_16x16x32_bf16 v[148:151], a[236:239], v[68:71], v[148:151]\n" \
"v_cvt_pk_bf16_f32 v102, v108, v109\n" \
"v_cvt_pk_bf16_f32 v103, v110, v111\n" \
"v_exp_f32 v121, v121\n" \
"v_mfma_f32_16x16x32_bf16 v[152:155], a[32:35], v[64:67], v[152:155]\n" \
"v_add_u32 v233, 0x10000, v235\n" \
"s_lshl_b32 s72, s61, 14\n" \
"v_mfma_f32_16x16x32_bf16 v[152:155], a[36:39], v[68:71], v[152:155]\n" \
"v_add_u32 v232, s72, v235\n" \
"v_mfma_f32_16x16x32_bf16 v[184:187], a[32:35], v[80:83], v[184:187]\n" \
"v_exp_f32 v122, v122\n" \
"v_mfma_f32_16x16x32_bf16 v[184:187], a[36:39], v[84:87], v[184:187]\n" \
"v_exp_f32 v123, v123\n" \
"v_mfma_f32_16x16x32_bf16 v[188:191], a[40:43], v[80:83], v[188:191]\n" \
"v_exp_f32 v124, v124\n" \
"v_mfma_f32_16x16x32_bf16 v[188:191], a[44:47], v[84:87], v[188:191]\n" \
"v_cvt_pk_bf16_f32 v116, v120, v121\n" \
"v_cvt_pk_bf16_f32 v117, v122, v123\n" \
"v_mfma_f32_16x16x32_bf16 v[156:159], a[40:43], v[64:67], v[156:159]\n" \
"v_exp_f32 v125, v125\n" \
"v_mfma_f32_16x16x32_bf16 v[156:159], a[44:47], v[68:71], v[156:159]\n" \
"s_cmp_eq_u32 s63, 7\n" \
"s_waitcnt vmcnt(7) lgkmcnt(0)\n" \
"s_barrier\n" \
"s_cbranch_scc1 Lep_%=\n" \
"Lepg_%=:\n" \
"v_mfma_f32_16x16x32_bf16 v[16:19], a[0:3], a[176:179], v[208:211]\n" \
"s_add_u32 m0, s79, 110592\n" \
"ds_read_b128 v[64:67], v233 offset:16384\n" \
"ds_read_b128 v[68:71], v233 offset:24576\n" \
"v_mfma_f32_16x16x32_bf16 v[16:19], a[4:7], a[180:183], v[16:19]\n" \
"buffer_load_dwordx4 v192, s[44:47], s73 offen offset:0 lds\n" \
"v_mfma_f32_16x16x32_bf16 v[16:19], a[8:11], a[184:187], v[16:19]\n" \
"ds_read_b128 v[72:75], v233 offset:17408\n" \
"ds_read_b128 v[76:79], v233 offset:25600\n" \
"v_mfma_f32_16x16x32_bf16 v[16:19], a[12:15], a[188:191], v[16:19]\n" \
"ds_write_b128 v232, v[128:131] offset:0\n" \
"v_exp_f32 v126, v126\n" \
"v_mfma_f32_16x16x32_bf16 v[20:23], a[28:31], a[188:191], v[212:215]\n" \
"ds_read_b128 v[80:83], v233 offset:18432\n" \
"ds_read_b128 v[84:87], v233 offset:26624\n" \
"v_mfma_f32_16x16x32_bf16 v[20:23], a[24:27], a[184:187], v[20:23]\n" \
"buffer_load_dwordx4 v193, s[44:47], s73 offen offset:1024 lds\n" \
"v_mfma_f32_16x16x32_bf16 v[20:23], a[20:23], a[180:183], v[20:23]\n" \
"ds_write_b128 v232, v[132:135] offset:1024\n" \
"v_xor_b32 v248, 0x4000, v232\n" \
"v_mfma_f32_16x16x32_bf16 v[20:23], a[16:19], a[176:179], v[20:23]\n" \
"ds_read_b128 v[88:91], v233 offset:19456\n" \
"ds_read_b128 v[92:95], v233 offset:27648\n" \
"v_mfma_f32_16x16x32_bf16 v[24:27], v[0:3], a[176:179], v[216:219]\n" \
"ds_read_b128 a[248:251], v233 offset:7168\n" \
"ds_read_b128 a[252:255], v233 offset:15360\n" \
"v_mfma_f32_16x16x32_bf16 v[24:27], v[4:7], a[180:183], v[24:27]\n" \
"buffer_load_dwordx4 v192, s[44:47], s73 offen offset:2048 lds\n" \
"v_mfma_f32_16x16x32_bf16 v[24:27], v[8:11], a[184:187], v[24:27]\n" \
"ds_write_b128 v232, v[136:139] offset:2048\n" \
"v_mfma_f32_16x16x32_bf16 v[24:27], v[12:15], a[188:191], v[24:27]\n" \
"v_exp_f32 v127, v127\n" \
"v_cvt_pk_bf16_f32 v118, v124, v125\n" \
"v_mfma_f32_16x16x32_bf16 v[28:31], a[244:247], a[188:191], v[220:223]\n" \
"v_cvt_pk_bf16_f32 v119, v126, v127\n" \
"v_lshrrev_b32 v249, 1, v234\n" \
"v_mfma_f32_16x16x32_bf16 v[28:31], a[240:243], a[184:187], v[28:31]\n" \
"buffer_load_dwordx4 v193, s[44:47], s73 offen offset:3072 lds\n" \
"v_mfma_f32_16x16x32_bf16 v[28:31], v[204:207], a[180:183], v[28:31]\n" \
"ds_write_b128 v232, v[140:143] offset:3072\n" \
"s_lshl_b32 s72, s61, 9\n" \
"s_add_u32 s72, s72, 0x20000\n" \
"v_exp_f32 v32, v32\n" \
"v_exp_f32 v33, v33\n" \
"v_mfma_f32_16x16x32_bf16 v[28:31], v[200:203], a[176:179], v[28:31]\n" \
"s_add_u32 m0, s89, 110592\n" \
"v_exp_f32 v34, v34\n" \
"v_exp_f32 v35, v35\n" \
"v_mfma_f32_16x16x32_bf16 a[64:67], a[192:195], v[96:99], a[64:67]\n" \
"s_xor_b32 s73, s73, 0x180\n" \
"v_exp_f32 v36, v36\n" \
"v_mfma_f32_16x16x32_bf16 a[64:67], a[196:199], v[100:103], a[64:67]\n" \
"buffer_load_dwordx4 v196, s[44:47], s75 offen lds\n" \
"s_add_u32 s72, s72, %3\n" \
"v_cvt_pk_bf16_f32 v32, v32, v33\n" \
"v_exp_f32 v37, v37\n" \
"v_add_u32 v249, s72, v249\n" \
"v_mfma_f32_16x16x32_bf16 a[96:99], a[192:195], v[112:115], a[96:99]\n" \
"ds_write_b128 v232, v[144:147] offset:4096\n" \
"v_mfma_f32_16x16x32_bf16 a[96:99], a[196:199], v[116:119], a[96:99]\n" \
"ds_read_b128 a[192:195], v233 offset:0\n" \
"ds_read_b128 a[196:199], v233 offset:8192\n" \
"v_exp_f32 v38, v38\n" \
"v_cvt_pk_bf16_f32 v33, v34, v35\n" \
"v_accvgpr_read_b32 v250, a48\n" \
"v_mfma_f32_16x16x32_bf16 a[100:103], a[200:203], v[112:115], a[100:103]\n" \
"s_xor_b32 s75, s75, 0x180\n" \
"v_exp_f32 v39, v39\n" \
"v_mfma_f32_16x16x32_bf16 a[100:103], a[204:207], v[116:119], a[100:103]\n" \
"v_exp_f32 v48, v48\n" \
"v_cvt_pk_bf16_f32 v34, v36, v37\n" \
"v_exp_f32 v49, v49\n" \
"v_mfma_f32_16x16x32_bf16 a[68:71], a[200:203], v[96:99], a[68:71]\n" \
"ds_write_b128 v232, v[148:151] offset:5120\n" \
"v_mfma_f32_16x16x32_bf16 a[68:71], a[204:207], v[100:103], a[68:71]\n" \
"ds_read_b128 a[200:203], v233 offset:1024\n" \
"ds_read_b128 a[204:207], v233 offset:9216\n" \
"v_exp_f32 v50, v50\n" \
"v_cvt_pk_bf16_f32 v35, v38, v39\n" \
"v_smfmac_f32_16x16x64_bf16 v[224:227], v[244:247], v[96:103], v243\n" \
"v_cvt_pk_bf16_f32 v48, v48, v49\n" \
"v_mfma_f32_16x16x32_bf16 a[72:75], a[208:211], v[96:99], a[72:75]\n" \
"v_exp_f32 v51, v51\n" \
"v_mfma_f32_16x16x32_bf16 a[72:75], a[212:215], v[100:103], a[72:75]\n" \
"ds_write_b128 v232, v[152:155] offset:6144\n" \
"v_exp_f32 v52, v52\n" \
"v_exp_f32 v53, v53\n" \
"v_accvgpr_read_b32 v240, a49\n" \
"v_mfma_f32_16x16x32_bf16 a[104:107], a[208:211], v[112:115], a[104:107]\n" \
"v_exp_f32 v54, v54\n" \
"v_mfma_f32_16x16x32_bf16 a[104:107], a[212:215], v[116:119], a[104:107]\n" \
"ds_read_b128 a[208:211], v233 offset:2048\n" \
"ds_read_b128 a[212:215], v233 offset:10240\n" \
"v_cvt_pk_bf16_f32 v49, v50, v51\n" \
"v_exp_f32 v55, v55\n" \
"v_mfma_f32_16x16x32_bf16 a[108:111], a[216:219], v[112:115], a[108:111]\n" \
"v_exp_f32 v40, v40\n" \
"v_mfma_f32_16x16x32_bf16 a[108:111], a[220:223], v[116:119], a[108:111]\n" \
"ds_write_b128 v232, v[156:159] offset:7168\n" \
"v_cvt_pk_bf16_f32 v50, v52, v53\n" \
"v_exp_f32 v41, v41\n" \
"v_mfma_f32_16x16x32_bf16 a[76:79], a[216:219], v[96:99], a[76:79]\n" \
"v_exp_f32 v42, v42\n" \
"v_mfma_f32_16x16x32_bf16 a[76:79], a[220:223], v[100:103], a[76:79]\n" \
"ds_read_b128 a[216:219], v233 offset:3072\n" \
"ds_read_b128 a[220:223], v233 offset:11264\n" \
"v_cvt_pk_bf16_f32 v51, v54, v55\n" \
"v_exp_f32 v43, v43\n" \
"v_smfmac_f32_16x16x64_bf16 v[228:231], v[244:247], v[112:119], v243\n" \
"ds_write_b128 v232, v[160:163] offset:8192\n" \
"v_exp_f32 v44, v44\n" \
"v_add_f32 v250, v250, v240\n" \
"v_mfma_f32_16x16x32_bf16 a[80:83], a[224:227], v[96:99], a[80:83]\n" \
"v_cvt_pk_bf16_f32 v36, v40, v41\n" \
"v_cvt_pk_bf16_f32 v37, v42, v43\n" \
"v_mfma_f32_16x16x32_bf16 a[80:83], a[228:231], v[100:103], a[80:83]\n" \
"v_exp_f32 v45, v45\n" \
"v_exp_f32 v46, v46\n" \
"v_mfma_f32_16x16x32_bf16 a[112:115], a[224:227], v[112:115], a[112:115]\n" \
"v_exp_f32 v47, v47\n" \
"v_mfma_f32_16x16x32_bf16 a[112:115], a[228:231], v[116:119], a[112:115]\n" \
"ds_write_b128 v232, v[164:167] offset:9216\n" \
"v_exp_f32 v56, v56\n" \
"v_cvt_pk_bf16_f32 v38, v44, v45\n" \
"v_mfma_f32_16x16x32_bf16 a[116:119], a[232:235], v[112:115], a[116:119]\n" \
"ds_read_b128 a[224:227], v233 offset:4096\n" \
"ds_read_b128 a[228:231], v233 offset:12288\n" \
"v_mfma_f32_16x16x32_bf16 a[116:119], a[236:239], v[116:119], a[116:119]\n" \
"v_exp_f32 v57, v57\n" \
"v_exp_f32 v58, v58\n" \
"v_cvt_pk_bf16_f32 v39, v46, v47\n" \
"v_accvgpr_read_b32 v251, a52\n" \
"v_mfma_f32_16x16x32_bf16 a[84:87], a[232:235], v[96:99], a[84:87]\n" \
"v_exp_f32 v59, v59\n" \
"v_mfma_f32_16x16x32_bf16 a[84:87], a[236:239], v[100:103], a[84:87]\n" \
"ds_write_b128 v232, v[168:171] offset:10240\n" \
"v_exp_f32 v60, v60\n" \
"v_cvt_pk_bf16_f32 v52, v56, v57\n" \
"v_mfma_f32_16x16x32_bf16 a[88:91], a[32:35], v[96:99], a[88:91]\n" \
"ds_read_b128 a[232:235], v233 offset:5120\n" \
"ds_read_b128 a[236:239], v233 offset:13312\n" \
"v_mfma_f32_16x16x32_bf16 a[88:91], a[36:39], v[100:103], a[88:91]\n" \
"ds_read_b128 a[240:243], v233 offset:6144\n" \
"ds_read_b128 a[244:247], v233 offset:14336\n" \
"v_exp_f32 v61, v61\n" \
"v_exp_f32 v62, v62\n" \
"v_mfma_f32_16x16x32_bf16 a[120:123], a[32:35], v[112:115], a[120:123]\n" \
"ds_read_b128 v[0:3], v233 offset:20480\n" \
"ds_read_b128 v[4:7], v233 offset:28672\n" \
"v_mfma_f32_16x16x32_bf16 a[120:123], a[36:39], v[116:119], a[120:123]\n" \
"ds_write_b128 v232, v[172:175] offset:11264\n" \
"v_cvt_pk_bf16_f32 v53, v58, v59\n" \
"v_exp_f32 v63, v63\n" \
"v_mfma_f32_16x16x32_bf16 a[124:127], a[40:43], v[112:115], a[124:127]\n" \
"ds_read_b128 v[8:11], v233 offset:21504\n" \
"ds_read_b128 v[12:15], v233 offset:29696\n" \
"v_mfma_f32_16x16x32_bf16 a[124:127], a[44:47], v[116:119], a[124:127]\n" \
"v_cvt_pk_bf16_f32 v54, v60, v61\n" \
"v_accvgpr_read_b32 v240, a53\n" \
"v_add_f32 v251, v251, v240\n" \
"v_mfma_f32_16x16x32_bf16 a[92:95], a[40:43], v[96:99], a[92:95]\n" \
"s_waitcnt lgkmcnt(5)\n" \
"v_cvt_pk_bf16_f32 v55, v62, v63\n" \
"v_mfma_f32_16x16x32_bf16 a[92:95], a[44:47], v[100:103], a[92:95]\n" \
"ds_write_b128 v232, v[176:179] offset:12288\n" \
"ds_write_b64 v249, v[250:251]\n" \
"v_mfma_f32_16x16x32_bf16 a[64:67], a[192:195], v[32:35], a[64:67]\n" \
"ds_read_b128 v[96:99], v233 offset:22528\n" \
"ds_read_b128 v[100:103], v233 offset:30720\n" \
"v_mfma_f32_16x16x32_bf16 a[64:67], a[196:199], v[36:39], a[64:67]\n" \
"ds_read_b128 v[104:107], v233 offset:23552\n" \
"ds_read_b128 v[108:111], v233 offset:31744\n" \
"v_exp_f32 v16, v16\n" \
"v_mfma_f32_16x16x32_bf16 a[96:99], a[192:195], v[48:51], a[96:99]\n" \
"v_exp_f32 v17, v17\n" \
"v_mfma_f32_16x16x32_bf16 a[96:99], a[196:199], v[52:55], a[96:99]\n" \
"ds_write_b128 v232, v[180:183] offset:13312\n" \
"v_exp_f32 v18, v18\n" \
"v_xor_b32 v249, 0x200, v249\n" \
"v_accvgpr_read_b32 v200, a59\n" \
"v_mfma_f32_16x16x32_bf16 a[100:103], a[200:203], v[48:51], a[100:103]\n" \
"v_exp_f32 v19, v19\n" \
"v_mfma_f32_16x16x32_bf16 a[100:103], a[204:207], v[52:55], a[100:103]\n" \
"v_exp_f32 v20, v20\n" \
"v_cvt_pk_bf16_f32 v16, v16, v17\n" \
"v_accvgpr_read_b32 v202, a60\n" \
"v_accvgpr_read_b32 v204, a61\n" \
"v_mfma_f32_16x16x32_bf16 a[68:71], a[200:203], v[32:35], a[68:71]\n" \
"v_exp_f32 v21, v21\n" \
"v_mfma_f32_16x16x32_bf16 a[68:71], a[204:207], v[36:39], a[68:71]\n" \
"ds_write_b128 v232, v[184:187] offset:14336\n" \
"v_exp_f32 v22, v22\n" \
"v_xor_b32 v201, 64, v200\n" \
"v_xor_b32 v203, 64, v202\n" \
"v_xor_b32 v222, 32, v204\n" \
"v_smfmac_f32_16x16x64_bf16 v[224:227], v[244:247], v[32:39], v243\n" \
"v_cvt_pk_bf16_f32 v17, v18, v19\n" \
"v_add_u32 v205, 0x10000, v200\n" \
"v_mfma_f32_16x16x32_bf16 a[72:75], a[208:211], v[32:35], a[72:75]\n" \
"v_exp_f32 v23, v23\n" \
"v_mfma_f32_16x16x32_bf16 a[72:75], a[212:215], v[36:39], a[72:75]\n" \
"v_exp_f32 v24, v24\n" \
"v_cvt_pk_bf16_f32 v18, v20, v21\n" \
"v_add_u32 v207, 0x10000, v202\n" \
"v_add_u32 v194, 139264, v204\n" \
"v_add_u32 v206, 0x10000, v201\n" \
"v_mfma_f32_16x16x32_bf16 a[104:107], a[208:211], v[48:51], a[104:107]\n" \
"ds_write_b128 v232, v[188:191] offset:15360\n" \
"v_exp_f32 v25, v25\n" \
"v_add_u32 v208, 0x10000, v203\n" \
"v_mfma_f32_16x16x32_bf16 a[104:107], a[212:215], v[52:55], a[104:107]\n" \
"v_exp_f32 v26, v26\n" \
"v_cvt_pk_bf16_f32 v19, v22, v23\n" \
"v_add_u32 v195, 139264, v222\n" \
"v_mfma_f32_16x16x32_bf16 a[108:111], a[216:219], v[48:51], a[108:111]\n" \
"s_mul_i32 s76, s80, 393216\n" \
"s_add_u32 s76, s76, s78\n" \
"v_cvt_pk_bf16_f32 v20, v24, v25\n" \
"v_mfma_f32_16x16x32_bf16 a[108:111], a[220:223], v[52:55], a[108:111]\n" \
"s_waitcnt vmcnt(0) lgkmcnt(0)\n" \
"s_barrier\n" \
"v_mfma_f32_16x16x32_bf16 a[76:79], a[216:219], v[32:35], a[76:79]\n" \
"s_add_u32 m0, s91, 86016\n" \
"ds_read_b64_tr_b16 a[32:33], v194 offset:1024\n" \
"ds_read_b64_tr_b16 a[34:35], v194 offset:4096\n" \
"v_mfma_f32_16x16x32_bf16 a[76:79], a[220:223], v[36:39], a[76:79]\n" \
"buffer_load_dwordx4 v197, s[48:51], s76 offen offset:0 lds\n" \
"v_exp_f32 v27, v27\n" \
"v_smfmac_f32_16x16x64_bf16 v[228:231], v[244:247], v[48:55], v243\n" \
"s_and_b32 s15, %1, 7\n" \
"ds_read_b64_tr_b16 a[36:37], v194 offset:13312\n" \
"ds_read_b64_tr_b16 a[38:39], v194 offset:16384\n" \
"v_exp_f32 v28, v28\n" \
"v_mfma_f32_16x16x32_bf16 a[80:83], a[224:227], v[32:35], a[80:83]\n" \
"s_add_u32 s77, s76, 24576\n" \
"ds_read_b64_tr_b16 a[40:41], v194 offset:0\n" \
"ds_read_b64_tr_b16 a[42:43], v194 offset:3072\n" \
"v_mfma_f32_16x16x32_bf16 a[80:83], a[228:231], v[36:39], a[80:83]\n" \
"buffer_load_dwordx4 v198, s[48:51], s76 offen offset:1024 lds\n" \
"v_mfma_f32_16x16x32_bf16 a[112:115], a[224:227], v[48:51], a[112:115]\n" \
"s_lshl3_add_u32 s14, s14, s15\n" \
"ds_read_b64_tr_b16 a[44:45], v194 offset:12288\n" \
"ds_read_b64_tr_b16 a[46:47], v194 offset:15360\n" \
"v_mfma_f32_16x16x32_bf16 a[112:115], a[228:231], v[52:55], a[112:115]\n" \
"s_xor_b32 s80, s80, 0x8\n" \
"ds_read_b64_tr_b16 a[48:49], v195 offset:0\n" \
"ds_read_b64_tr_b16 a[50:51], v195 offset:3072\n" \
"v_exp_f32 v29, v29\n" \
"v_mfma_f32_16x16x32_bf16 a[116:119], a[232:235], v[48:51], a[116:119]\n" \
"buffer_load_dwordx4 v199, s[48:51], s76 offen offset:2048 lds\n" \
"v_mfma_f32_16x16x32_bf16 a[116:119], a[236:239], v[52:55], a[116:119]\n" \
"ds_read_b64_tr_b16 a[52:53], v195 offset:12288\n" \
"ds_read_b64_tr_b16 a[54:55], v195 offset:15360\n" \
"v_exp_f32 v30, v30\n" \
"v_mfma_f32_16x16x32_bf16 a[84:87], a[232:235], v[32:35], a[84:87]\n" \
"s_add_u32 m0, m0, 3072\n" \
"ds_read_b128 a[0:3], v207 offset:45056\n" \
"ds_read_b128 a[4:7], v208 offset:45056\n" \
"v_mfma_f32_16x16x32_bf16 a[84:87], a[236:239], v[36:39], a[84:87]\n" \
"buffer_load_dwordx4 v197, s[48:51], s77 offen offset:0 lds\n" \
"v_cvt_pk_bf16_f32 v21, v26, v27\n" \
"v_mfma_f32_16x16x32_bf16 a[88:91], a[240:243], v[32:35], a[88:91]\n" \
"ds_read_b128 a[8:11], v205 offset:45056\n" \
"ds_read_b128 a[12:15], v206 offset:45056\n" \
"v_mfma_f32_16x16x32_bf16 a[88:91], a[244:247], v[36:39], a[88:91]\n" \
"ds_read_b128 a[16:19], v205 offset:49152\n" \
"ds_read_b128 a[20:23], v206 offset:49152\n" \
"v_exp_f32 v31, v31\n" \
"v_mfma_f32_16x16x32_bf16 a[120:123], a[240:243], v[48:51], a[120:123]\n" \
"buffer_load_dwordx4 v198, s[48:51], s77 offen offset:1024 lds\n" \
"v_mfma_f32_16x16x32_bf16 a[120:123], a[244:247], v[52:55], a[120:123]\n" \
"ds_read_b128 a[24:27], v205 offset:53248\n" \
"ds_read_b128 a[28:31], v206 offset:53248\n" \
"v_cvt_pk_bf16_f32 v22, v28, v29\n" \
"v_mfma_f32_16x16x32_bf16 a[124:127], a[248:251], v[48:51], a[124:127]\n" \
"ds_read_b64 v[250:251], v249\n" \
"ds_read_b128 v[128:131], v248 offset:0\n" \
"v_mfma_f32_16x16x32_bf16 a[124:127], a[252:255], v[52:55], a[124:127]\n" \
"buffer_load_dwordx4 v199, s[48:51], s77 offen offset:2048 lds\n" \
"v_cvt_pk_bf16_f32 v23, v30, v31\n" \
"v_mfma_f32_16x16x32_bf16 a[92:95], a[248:251], v[32:35], a[92:95]\n" \
"ds_read_b128 v[132:135], v248 offset:1024\n" \
"ds_read_b128 v[136:139], v248 offset:2048\n" \
"v_mfma_f32_16x16x32_bf16 a[92:95], a[252:255], v[36:39], a[92:95]\n" \
"s_add_u32 m0, s79, 65536\n" \
"ds_read_b128 v[140:143], v248 offset:3072\n" \
"ds_read_b128 v[144:147], v248 offset:4096\n" \
"v_mfma_f32_16x16x32_bf16 a[96:99], v[64:67], v[16:19], a[96:99]\n" \
"buffer_load_dwordx4 v192, s[44:47], s73 offen offset:0 lds\n" \
"v_mfma_f32_16x16x32_bf16 a[96:99], v[68:71], v[20:23], a[96:99]\n" \
"ds_read_b128 v[148:151], v248 offset:5120\n" \
"ds_read_b128 v[152:155], v248 offset:6144\n" \
"v_mfma_f32_16x16x32_bf16 a[100:103], v[72:75], v[16:19], a[100:103]\n" \
"ds_read_b128 v[156:159], v248 offset:7168\n" \
"ds_read_b128 v[160:163], v248 offset:8192\n" \
"v_mfma_f32_16x16x32_bf16 a[100:103], v[76:79], v[20:23], a[100:103]\n" \
"buffer_load_dwordx4 v193, s[44:47], s73 offen offset:1024 lds\n" \
"v_smfmac_f32_16x16x64_bf16 v[228:231], v[244:247], v[16:23], v243\n" \
"ds_read_b128 v[164:167], v248 offset:9216\n" \
"ds_read_b128 v[168:171], v248 offset:10240\n" \
"v_mfma_f32_16x16x32_bf16 a[104:107], v[80:83], v[16:19], a[104:107]\n" \
"ds_read_b128 v[172:175], v248 offset:11264\n" \
"ds_read_b128 v[176:179], v248 offset:12288\n" \
"v_mfma_f32_16x16x32_bf16 a[104:107], v[84:87], v[20:23], a[104:107]\n" \
"buffer_load_dwordx4 v192, s[44:47], s73 offen offset:2048 lds\n" \
"v_mfma_f32_16x16x32_bf16 a[108:111], v[88:91], v[16:19], a[108:111]\n" \
"ds_read_b128 v[180:183], v248 offset:13312\n" \
"ds_read_b128 v[184:187], v248 offset:14336\n" \
"v_mfma_f32_16x16x32_bf16 a[108:111], v[92:95], v[20:23], a[108:111]\n" \
"ds_read_b128 v[188:191], v248 offset:15360\n" \
"v_mov_b32 v218, s60\n" \
"v_mfma_f32_16x16x32_bf16 a[112:115], v[0:3], v[16:19], a[112:115]\n" \
"buffer_load_dwordx4 v193, s[44:47], s73 offen offset:3072 lds\n" \
"v_mfma_f32_16x16x32_bf16 a[112:115], v[4:7], v[20:23], a[112:115]\n" \
"s_add_u32 m0, s89, 65536\n" \
"v_mov_b32 v219, s14\n" \
"v_lshlrev_b32 v220, 28, s61\n" \
"v_mfma_f32_16x16x32_bf16 a[116:119], v[8:11], v[16:19], a[116:119]\n" \
"s_xor_b32 s73, s73, 0x400\n" \
"v_mfma_f32_16x16x32_bf16 a[116:119], v[12:15], v[20:23], a[116:119]\n" \
"buffer_load_dwordx4 v196, s[44:47], s75 offen lds\n" \
"v_mfma_f32_16x16x32_bf16 a[120:123], v[96:99], v[16:19], a[120:123]\n" \
"s_lshl3_add_u32 s15, s15, 65593856\n" \
"v_mfma_f32_16x16x32_bf16 a[120:123], v[100:103], v[20:23], a[120:123]\n" \
"s_lshr_b32 s14, %1, 3\n" \
"s_cmp_eq_u32 s14, 3\n" \
"s_cselect_b32 s15, s15, 0x20000000\n" \
"buffer_store_dwordx2 v[218:219], v220, s[56:59], s15 offen sc1\n" \
"v_mfma_f32_16x16x32_bf16 a[124:127], v[104:107], v[16:19], a[124:127]\n" \
"s_xor_b32 s75, s75, 0x400\n" \
"v_mfma_f32_16x16x32_bf16 a[124:127], v[108:111], v[20:23], a[124:127]\n" \
"s_waitcnt lgkmcnt(0)\n" \
"s_barrier\n" \
"v_mfma_f32_16x16x32_bf16 v[32:35], a[32:35], a[0:3], 0\n" \
"s_add_u32 m0, s79, 0\n" \
"ds_read_b128 a[192:195], v205 offset:57344\n" \
"v_mfma_f32_16x16x32_bf16 v[32:35], a[36:39], a[4:7], v[32:35]\n" \
"buffer_load_dwordx4 v192, s[44:47], s73 offen offset:0 lds\n" \
"v_mfma_f32_16x16x32_bf16 v[40:43], a[32:35], a[8:11], 0\n" \
"ds_read_b128 a[196:199], v206 offset:57344\n" \
"v_add_u32 v209, 86016, v204\n" \
"v_mfma_f32_16x16x32_bf16 v[40:43], a[36:39], a[12:15], v[40:43]\n" \
"buffer_load_dwordx4 v193, s[44:47], s73 offen offset:1024 lds\n" \
"v_mfma_f32_16x16x32_bf16 v[0:3], a[40:43], a[8:11], 0\n" \
"ds_read_b64_tr_b16 a[200:201], v195 offset:1024\n" \
"ds_read_b64_tr_b16 a[202:203], v195 offset:4096\n" \
"v_mfma_f32_16x16x32_bf16 v[0:3], a[44:47], a[12:15], v[0:3]\n" \
"buffer_load_dwordx4 v192, s[44:47], s73 offen offset:2048 lds\n" \
"v_add_f32 v248, v224, v225\n" \
"v_mfma_f32_16x16x32_bf16 v[4:7], a[48:51], a[8:11], 0\n" \
"ds_read_b64_tr_b16 a[204:205], v195 offset:13312\n" \
"ds_read_b64_tr_b16 a[206:207], v195 offset:16384\n" \
"v_mfma_f32_16x16x32_bf16 v[4:7], a[52:55], a[12:15], v[4:7]\n" \
"buffer_load_dwordx4 v193, s[44:47], s73 offen offset:3072 lds\n" \
"v_mfma_f32_16x16x32_bf16 v[12:15], a[48:51], a[16:19], 0\n" \
"s_add_u32 m0, s89, 0\n" \
"s_xor_b32 s73, s73, 0x500\n" \
"ds_read_b64_tr_b16 a[208:209], v194 offset:2048\n" \
"ds_read_b64_tr_b16 a[210:211], v194 offset:5120\n" \
"v_add_f32 v249, v228, v229\n" \
"v_mfma_f32_16x16x32_bf16 v[12:15], a[52:55], a[20:23], v[12:15]\n" \
"buffer_load_dwordx4 v196, s[44:47], s75 offen lds\n" \
"v_mfma_f32_16x16x32_bf16 v[8:11], a[40:43], a[16:19], 0\n" \
"s_xor_b32 s75, s75, 0x500\n" \
"s_mul_i32 s76, s80, 393216\n" \
"Lpg7384_%=:\n" \
"s_cbranch_execz Lcb0_%=\n" \
"s_add_u32 s76, s76, s78\n" \
"s_add_u32 m0, s91, 20480\n" \
"ds_read_b64_tr_b16 a[212:213], v194 offset:14336\n" \
"ds_read_b64_tr_b16 a[214:215], v194 offset:17408\n" \
"v_mfma_f32_16x16x32_bf16 v[8:11], a[44:47], a[20:23], v[8:11]\n" \
"buffer_load_dwordx4 v197, s[48:51], s76 offen offset:0 lds\n" \
"v_add_f32 v248, v248, v250\n" \
"v_mfma_f32_16x16x32_bf16 v[48:51], a[32:35], a[16:19], 0\n" \
"s_add_u32 s77, s76, 24576\n" \
"ds_read_b64_tr_b16 a[216:217], v195 offset:2048\n" \
"v_add_u32 v210, 86016, v222\n" \
"v_mfma_f32_16x16x32_bf16 v[48:51], a[36:39], a[20:23], v[48:51]\n" \
"buffer_load_dwordx4 v198, s[48:51], s76 offen offset:1024 lds\n" \
"v_mfma_f32_16x16x32_bf16 v[56:59], a[32:35], a[24:27], 0\n" \
"s_xor_b32 s80, s80, 0xa\n" \
"ds_read_b64_tr_b16 a[218:219], v195 offset:5120\n" \
"v_add_f32 v249, v249, v251\n" \
"v_mfma_f32_16x16x32_bf16 v[56:59], a[36:39], a[28:31], v[56:59]\n" \
"buffer_load_dwordx4 v199, s[48:51], s76 offen offset:2048 lds\n" \
"v_mfma_f32_16x16x32_bf16 v[16:19], a[40:43], a[24:27], 0\n" \
"s_add_u32 m0, m0, 3072\n" \
"ds_read_b64_tr_b16 a[220:221], v195 offset:14336\n" \
"v_mfma_f32_16x16x32_bf16 v[16:19], a[44:47], a[28:31], v[16:19]\n" \
"buffer_load_dwordx4 v197, s[48:51], s77 offen offset:0 lds\n" \
"v_mfma_f32_16x16x32_bf16 v[20:23], a[48:51], a[24:27], 0\n" \
"s_waitcnt lgkmcnt(2)\n" \
"ds_read_b64_tr_b16 a[222:223], v195 offset:17408\n" \
"v_mfma_f32_16x16x32_bf16 v[20:23], a[52:55], a[28:31], v[20:23]\n" \
"buffer_load_dwordx4 v198, s[48:51], s77 offen offset:1024 lds\n" \
"v_rcp_f32 v232, v248\n" \
"v_mfma_f32_16x16x32_bf16 v[28:31], a[48:51], a[192:195], 0\n" \
"v_rcp_f32 v233, v249\n" \
"v_mfma_f32_16x16x32_bf16 v[28:31], a[52:55], a[196:199], v[28:31]\n" \
"buffer_load_dwordx4 v199, s[48:51], s77 offen offset:2048 lds\n" \
"v_add_u32 v240, 0x1000, v236\n" \
"v_mfma_f32_16x16x32_bf16 v[24:27], a[40:43], a[192:195], 0\n" \
"v_add_f32 v224, v248, v249\n" \
"v_mul_f32 v224, 0x43800000, v224\n" \
"v_mfma_f32_16x16x32_bf16 v[24:27], a[44:47], a[196:199], v[24:27]\n" \
"v_add_f32 v248, v232, v233\n" \
"v_fmac_f32 v224, 0x5f800000, v248\n" \
"v_mfma_f32_16x16x32_bf16 v[64:67], a[32:35], a[192:195], 0\n" \
"v_mov_b32 v225, 0\n" \
"v_mov_b32 v226, 0\n" \
"v_mfma_f32_16x16x32_bf16 v[64:67], a[36:39], a[196:199], v[64:67]\n" \
"v_mov_b32 v227, 0\n" \
"s_waitcnt vmcnt(12) lgkmcnt(0)\n" \
"s_barrier\n" \
"v_mfma_f32_16x16x32_bf16 v[68:71], a[200:203], a[192:195], 0\n" \
"ds_read_b64_tr_b16 a[32:33], v209 offset:1024\n" \
"ds_read_b64_tr_b16 a[34:35], v209 offset:4096\n" \
"v_mfma_f32_16x16x32_bf16 v[68:71], a[204:207], a[196:199], v[68:71]\n" \
"ds_read_b64_tr_b16 a[36:37], v209 offset:13312\n" \
"ds_read_b64_tr_b16 a[38:39], v209 offset:16384\n" \
"v_mfma_f32_16x16x32_bf16 v[60:63], a[200:203], a[24:27], 0\n" \
"s_add_u32 m0, s79, 110592\n" \
"ds_read_b64_tr_b16 a[40:41], v209 offset:0\n" \
"v_accvgpr_read_b32 v112, a64\n" \
"v_mfma_f32_16x16x32_bf16 v[60:63], a[204:207], a[28:31], v[60:63]\n" \
"buffer_load_dwordx4 v192, s[44:47], s73 offen offset:0 lds\n" \
"v_accvgpr_read_b32 v113, a65\n" \
"v_mfma_f32_16x16x32_bf16 v[52:55], a[200:203], a[16:19], 0\n" \
"ds_read_b64_tr_b16 a[42:43], v209 offset:3072\n" \
"v_accvgpr_read_b32 v114, a66\n" \
"v_mfma_f32_16x16x32_bf16 v[52:55], a[204:207], a[20:23], v[52:55]\n" \
"ds_read_b64_tr_b16 a[44:45], v209 offset:12288\n" \
"v_accvgpr_read_b32 v115, a67\n" \
"v_accvgpr_read_b32 v116, a68\n" \
"v_mfma_f32_16x16x32_bf16 v[44:47], a[200:203], a[8:11], 0\n" \
"ds_read_b64_tr_b16 a[46:47], v209 offset:15360\n" \
"v_accvgpr_read_b32 v117, a69\n" \
"v_mfma_f32_16x16x32_bf16 v[44:47], a[204:207], a[12:15], v[44:47]\n" \
"buffer_load_dwordx4 v193, s[44:47], s73 offen offset:1024 lds\n" \
"v_mfma_f32_16x16x32_bf16 v[72:75], a[0:3], a[208:211], 0\n" \
"ds_read_b64_tr_b16 a[48:49], v210 offset:0\n" \
"v_accvgpr_read_b32 v118, a70\n" \
"v_mfma_f32_16x16x32_bf16 v[72:75], a[4:7], a[212:215], v[72:75]\n" \
"ds_read_b128 a[0:3], v207 offset:0\n" \
"v_accvgpr_read_b32 v119, a71\n" \
"v_add_f32 v112, v112, v128\n" \
"v_mfma_f32_16x16x32_bf16 v[80:83], a[8:11], a[208:211], 0\n" \
"ds_read_b128 a[4:7], v208 offset:0\n" \
"v_add_f32 v113, v113, v129\n" \
"v_mfma_f32_16x16x32_bf16 v[80:83], a[12:15], a[212:215], v[80:83]\n" \
"buffer_load_dwordx4 v192, s[44:47], s73 offen offset:2048 lds\n" \
"v_mfma_f32_16x16x32_bf16 v[84:87], a[8:11], a[216:219], 0\n" \
"ds_read_b64_tr_b16 a[50:51], v210 offset:3072\n" \
"v_add_f32 v114, v114, v130\n" \
"v_mfma_f32_16x16x32_bf16 v[84:87], a[12:15], a[220:223], v[84:87]\n" \
"ds_read_b128 a[8:11], v205 offset:0\n" \
"v_add_f32 v115, v115, v131\n" \
"v_add_f32 v116, v116, v132\n" \
"v_mfma_f32_16x16x32_bf16 v[92:95], a[16:19], a[216:219], 0\n" \
"ds_read_b128 a[12:15], v206 offset:0\n" \
"v_add_f32 v117, v117, v133\n" \
"v_mfma_f32_16x16x32_bf16 v[92:95], a[20:23], a[220:223], v[92:95]\n" \
"buffer_load_dwordx4 v193, s[44:47], s73 offen offset:3072 lds\n" \
"v_add_f32 v118, v118, v134\n" \
"v_mfma_f32_16x16x32_bf16 v[88:91], a[16:19], a[208:211], 0\n" \
"ds_read_b64_tr_b16 a[52:53], v210 offset:12288\n" \
"v_add_f32 v119, v119, v135\n" \
"v_mfma_f32_16x16x32_bf16 v[88:91], a[20:23], a[212:215], v[88:91]\n" \
"s_add_u32 m0, s89, 110592\n" \
"ds_read_b128 a[16:19], v205 offset:4096\n" \
"v_mul_f32 v112, v112, v232\n" \
"v_mfma_f32_16x16x32_bf16 v[96:99], a[24:27], a[208:211], 0\n" \
"s_xor_b32 s73, s73, 0x180\n" \
"ds_read_b128 a[20:23], v206 offset:4096\n" \
"v_mul_f32 v113, v113, v232\n" \
"v_mfma_f32_16x16x32_bf16 v[96:99], a[28:31], a[212:215], v[96:99]\n" \
"buffer_load_dwordx4 v196, s[44:47], s75 offen lds\n" \
"v_mul_f32 v114, v114, v232\n" \
"v_mfma_f32_16x16x32_bf16 v[100:103], a[24:27], a[216:219], 0\n" \
"s_xor_b32 s75, s75, 0x180\n" \
"s_mul_i32 s76, s80, 393216\n" \
"ds_read_b64_tr_b16 a[54:55], v210 offset:15360\n" \
"v_mfma_f32_16x16x32_bf16 v[100:103], a[28:31], a[220:223], v[100:103]\n" \
"s_add_u32 s76, s76, s78\n" \
"ds_read_b128 a[24:27], v205 offset:8192\n" \
"v_mul_f32 v115, v115, v232\n" \
"v_mfma_f32_16x16x32_bf16 v[108:111], a[192:195], a[216:219], 0\n" \
"s_add_u32 m0, s91, 139264\n" \
"ds_read_b128 a[28:31], v206 offset:8192\n" \
"v_mul_f32 v116, v116, v232\n" \
"v_mfma_f32_16x16x32_bf16 v[108:111], a[196:199], a[220:223], v[108:111]\n" \
"buffer_load_dwordx4 v197, s[48:51], s76 offen offset:0 lds\n" \
"v_mul_f32 v117, v117, v232\n" \
"v_mfma_f32_16x16x32_bf16 v[104:107], a[192:195], a[208:211], 0\n" \
"v_mul_f32 v118, v118, v232\n" \
"v_mul_f32 v119, v119, v232\n" \
"v_mfma_f32_16x16x32_bf16 v[104:107], a[196:199], a[212:215], v[104:107]\n" \
"s_waitcnt lgkmcnt(2)\n" \
"v_cvt_pk_bf16_f32 v120, v112, v113\n" \
"v_cvt_pk_bf16_f32 v121, v114, v115\n" \
"v_cvt_pk_bf16_f32 v122, v116, v117\n" \
"v_mfma_f32_16x16x32_bf16 v[32:35], a[32:35], a[0:3], v[32:35]\n" \
"s_add_u32 s77, s76, 24576\n" \
"ds_read_b128 a[192:195], v205 offset:12288\n" \
"v_cvt_pk_bf16_f32 v123, v118, v119\n" \
"v_mfma_f32_16x16x32_bf16 v[32:35], a[36:39], a[4:7], v[32:35]\n" \
"buffer_load_dwordx4 v198, s[48:51], s76 offen offset:1024 lds\n" \
"v_mfma_f32_16x16x32_bf16 v[40:43], a[32:35], a[8:11], v[40:43]\n" \
"ds_read_b128 a[196:199], v206 offset:12288\n" \
"v_mfma_f32_16x16x32_bf16 v[40:43], a[36:39], a[12:15], v[40:43]\n" \
"ds_read_b64_tr_b16 a[200:201], v210 offset:1024\n" \
"buffer_store_dwordx4 v[120:123], v236, s[56:59], s88 offen offset:0 sc1\n" \
"v_mfma_f32_16x16x32_bf16 v[0:3], a[40:43], a[8:11], v[0:3]\n" \
"s_xor_b32 s80, s80, 0x3\n" \
"ds_read_b64_tr_b16 a[202:203], v210 offset:4096\n" \
"v_mfma_f32_16x16x32_bf16 v[0:3], a[44:47], a[12:15], v[0:3]\n" \
"buffer_load_dwordx4 v199, s[48:51], s76 offen offset:2048 lds\n" \
"v_mfma_f32_16x16x32_bf16 v[224:227], v[244:247], v[120:123], v[224:227]\n" \
"v_accvgpr_read_b32 v112, a72\n" \
"v_accvgpr_read_b32 v113, a73\n" \
"v_accvgpr_read_b32 v114, a74\n" \
"v_mfma_f32_16x16x32_bf16 v[4:7], a[48:51], a[8:11], v[4:7]\n" \
"ds_read_b64_tr_b16 a[204:205], v210 offset:13312\n" \
"v_accvgpr_read_b32 v115, a75\n" \
"v_mfma_f32_16x16x32_bf16 v[4:7], a[52:55], a[12:15], v[4:7]\n" \
"ds_read_b64_tr_b16 a[206:207], v210 offset:16384\n" \
"v_accvgpr_read_b32 v116, a76\n" \
"v_accvgpr_read_b32 v117, a77\n" \
"v_mfma_f32_16x16x32_bf16 v[12:15], a[48:51], a[16:19], v[12:15]\n" \
"s_add_u32 m0, m0, 3072\n" \
"ds_read_b64_tr_b16 a[208:209], v209 offset:2048\n" \
"v_accvgpr_read_b32 v118, a78\n" \
"v_mfma_f32_16x16x32_bf16 v[12:15], a[52:55], a[20:23], v[12:15]\n" \
"buffer_load_dwordx4 v197, s[48:51], s77 offen offset:0 lds\n" \
"v_accvgpr_read_b32 v119, a79\n" \
"v_mfma_f32_16x16x32_bf16 v[8:11], a[40:43], a[16:19], v[8:11]\n" \
"ds_read_b64_tr_b16 a[210:211], v209 offset:5120\n" \
"v_add_f32 v112, v112, v136\n" \
"v_mfma_f32_16x16x32_bf16 v[8:11], a[44:47], a[20:23], v[8:11]\n" \
"ds_read_b64_tr_b16 a[212:213], v209 offset:14336\n" \
"v_add_f32 v113, v113, v137\n" \
"v_mfma_f32_16x16x32_bf16 v[48:51], a[32:35], a[16:19], v[48:51]\n" \
"s_waitcnt lgkmcnt(3)\n" \
"ds_read_b64_tr_b16 a[214:215], v209 offset:17408\n" \
"v_add_f32 v114, v114, v138\n" \
"v_mfma_f32_16x16x32_bf16 v[48:51], a[36:39], a[20:23], v[48:51]\n" \
"buffer_load_dwordx4 v198, s[48:51], s77 offen offset:1024 lds\n" \
"v_add_f32 v115, v115, v139\n" \
"v_mfma_f32_16x16x32_bf16 v[56:59], a[32:35], a[24:27], v[56:59]\n" \
"ds_read_b64_tr_b16 a[216:217], v210 offset:2048\n" \
"v_add_f32 v116, v116, v140\n" \
"v_mfma_f32_16x16x32_bf16 v[56:59], a[36:39], a[28:31], v[56:59]\n" \
"ds_read_b64_tr_b16 a[218:219], v210 offset:5120\n" \
"v_add_f32 v117, v117, v141\n" \
"v_mfma_f32_16x16x32_bf16 v[16:19], a[40:43], a[24:27], v[16:19]\n" \
"ds_read_b64_tr_b16 a[220:221], v210 offset:14336\n" \
"v_add_f32 v118, v118, v142\n" \
"v_mfma_f32_16x16x32_bf16 v[16:19], a[44:47], a[28:31], v[16:19]\n" \
"buffer_load_dwordx4 v199, s[48:51], s77 offen offset:2048 lds\n" \
"v_add_f32 v119, v119, v143\n" \
"v_mfma_f32_16x16x32_bf16 v[20:23], a[48:51], a[24:27], v[20:23]\n" \
"ds_read_b64_tr_b16 a[222:223], v210 offset:17408\n" \
"v_mul_f32 v112, v112, v232\n" \
"v_mfma_f32_16x16x32_bf16 v[20:23], a[52:55], a[28:31], v[20:23]\n" \
"v_mul_f32 v113, v113, v232\n" \
"v_mul_f32 v114, v114, v232\n" \
"v_mul_f32 v115, v115, v232\n" \
"v_mfma_f32_16x16x32_bf16 v[28:31], a[48:51], a[192:195], v[28:31]\n" \
"v_mul_f32 v116, v116, v232\n" \
"v_mul_f32 v117, v117, v232\n" \
"v_mfma_f32_16x16x32_bf16 v[28:31], a[52:55], a[196:199], v[28:31]\n" \
"v_mul_f32 v118, v118, v232\n" \
"v_mul_f32 v119, v119, v232\n" \
"v_mfma_f32_16x16x32_bf16 v[24:27], a[40:43], a[192:195], v[24:27]\n" \
"v_cvt_pk_bf16_f32 v124, v112, v113\n" \
"v_cvt_pk_bf16_f32 v125, v114, v115\n" \
"v_mfma_f32_16x16x32_bf16 v[24:27], a[44:47], a[196:199], v[24:27]\n" \
"v_cvt_pk_bf16_f32 v126, v116, v117\n" \
"v_cvt_pk_bf16_f32 v127, v118, v119\n" \
"v_mfma_f32_16x16x32_bf16 v[64:67], a[32:35], a[192:195], v[64:67]\n" \
"buffer_store_dwordx4 v[124:127], v236, s[56:59], s88 offen offset:2048 sc1\n" \
"v_mfma_f32_16x16x32_bf16 v[64:67], a[36:39], a[196:199], v[64:67]\n" \
"v_mfma_f32_16x16x32_bf16 v[224:227], v[244:247], v[124:127], v[224:227]\n" \
"v_accvgpr_read_b32 v112, a80\n" \
"s_waitcnt vmcnt(13) lgkmcnt(0)\n" \
"s_barrier\n" \
"v_mfma_f32_16x16x32_bf16 v[68:71], a[200:203], a[192:195], v[68:71]\n" \
"ds_read_b64_tr_b16 a[32:33], v204 offset:21504\n" \
"ds_read_b64_tr_b16 a[34:35], v204 offset:24576\n" \
"v_mfma_f32_16x16x32_bf16 v[68:71], a[204:207], a[196:199], v[68:71]\n" \
"ds_read_b64_tr_b16 a[36:37], v204 offset:33792\n" \
"ds_read_b64_tr_b16 a[38:39], v204 offset:36864\n" \
"v_accvgpr_read_b32 v113, a81\n" \
"v_mfma_f32_16x16x32_bf16 v[60:63], a[200:203], a[24:27], v[60:63]\n" \
"s_add_u32 m0, s79, 65536\n" \
"ds_read_b64_tr_b16 a[40:41], v204 offset:20480\n" \
"v_accvgpr_read_b32 v114, a82\n" \
"v_mfma_f32_16x16x32_bf16 v[60:63], a[204:207], a[28:31], v[60:63]\n" \
"buffer_load_dwordx4 v192, s[44:47], s73 offen offset:0 lds\n" \
"v_accvgpr_read_b32 v115, a83\n" \
"v_mfma_f32_16x16x32_bf16 v[52:55], a[200:203], a[16:19], v[52:55]\n" \
"ds_read_b64_tr_b16 a[42:43], v204 offset:23552\n" \
"v_accvgpr_read_b32 v116, a84\n" \
"v_mfma_f32_16x16x32_bf16 v[52:55], a[204:207], a[20:23], v[52:55]\n" \
"ds_read_b64_tr_b16 a[44:45], v204 offset:32768\n" \
"v_accvgpr_read_b32 v117, a85\n" \
"v_mfma_f32_16x16x32_bf16 v[44:47], a[200:203], a[8:11], v[44:47]\n" \
"ds_read_b64_tr_b16 a[46:47], v204 offset:35840\n" \
"v_accvgpr_read_b32 v118, a86\n" \
"v_mfma_f32_16x16x32_bf16 v[44:47], a[204:207], a[12:15], v[44:47]\n" \
"buffer_load_dwordx4 v193, s[44:47], s73 offen offset:1024 lds\n" \
"v_accvgpr_read_b32 v119, a87\n" \
"v_mfma_f32_16x16x32_bf16 v[72:75], a[0:3], a[208:211], v[72:75]\n" \
"ds_read_b64_tr_b16 a[48:49], v222 offset:20480\n" \
"v_add_f32 v112, v112, v144\n" \
"v_mfma_f32_16x16x32_bf16 v[72:75], a[4:7], a[212:215], v[72:75]\n" \
"ds_read_b128 a[0:3], v202 offset:0\n" \
"v_add_f32 v113, v113, v145\n" \
"v_mfma_f32_16x16x32_bf16 v[80:83], a[8:11], a[208:211], v[80:83]\n" \
"ds_read_b128 a[4:7], v203 offset:0\n" \
"v_add_f32 v114, v114, v146\n" \
"v_mfma_f32_16x16x32_bf16 v[80:83], a[12:15], a[212:215], v[80:83]\n" \
"buffer_load_dwordx4 v192, s[44:47], s73 offen offset:2048 lds\n" \
"v_add_f32 v115, v115, v147\n" \
"v_mfma_f32_16x16x32_bf16 v[84:87], a[8:11], a[216:219], v[84:87]\n" \
"ds_read_b64_tr_b16 a[50:51], v222 offset:23552\n" \
"v_add_f32 v116, v116, v148\n" \
"v_mfma_f32_16x16x32_bf16 v[84:87], a[12:15], a[220:223], v[84:87]\n" \
"ds_read_b128 a[8:11], v200 offset:0\n" \
"v_add_f32 v117, v117, v149\n" \
"v_add_f32 v118, v118, v150\n" \
"v_mfma_f32_16x16x32_bf16 v[92:95], a[16:19], a[216:219], v[92:95]\n" \
"ds_read_b128 a[12:15], v201 offset:0\n" \
"v_add_f32 v119, v119, v151\n" \
"v_mfma_f32_16x16x32_bf16 v[92:95], a[20:23], a[220:223], v[92:95]\n" \
"buffer_load_dwordx4 v193, s[44:47], s73 offen offset:3072 lds\n" \
"v_mfma_f32_16x16x32_bf16 v[88:91], a[16:19], a[208:211], v[88:91]\n" \
"ds_read_b64_tr_b16 a[52:53], v222 offset:32768\n" \
"v_mul_f32 v112, v112, v232\n" \
"v_mfma_f32_16x16x32_bf16 v[88:91], a[20:23], a[212:215], v[88:91]\n" \
"s_add_u32 m0, s89, 65536\n" \
"ds_read_b128 a[16:19], v200 offset:4096\n" \
"v_mul_f32 v113, v113, v232\n" \
"v_mul_f32 v114, v114, v232\n" \
"v_mfma_f32_16x16x32_bf16 v[96:99], a[24:27], a[208:211], v[96:99]\n" \
"s_xor_b32 s73, s73, 0x300\n" \
"ds_read_b128 a[20:23], v201 offset:4096\n" \
"v_mul_f32 v115, v115, v232\n" \
"v_mfma_f32_16x16x32_bf16 v[96:99], a[28:31], a[212:215], v[96:99]\n" \
"buffer_load_dwordx4 v196, s[44:47], s75 offen lds\n" \
"v_mfma_f32_16x16x32_bf16 v[100:103], a[24:27], a[216:219], v[100:103]\n" \
"s_xor_b32 s75, s75, 0x300\n" \
"s_mul_i32 s76, s80, 393216\n" \
"ds_read_b64_tr_b16 a[54:55], v222 offset:35840\n" \
"v_mfma_f32_16x16x32_bf16 v[100:103], a[28:31], a[220:223], v[100:103]\n" \
"s_add_u32 s76, s76, s78\n" \
"ds_read_b128 a[24:27], v200 offset:8192\n" \
"v_mul_f32 v116, v116, v232\n" \
"v_mul_f32 v117, v117, v232\n" \
"v_mfma_f32_16x16x32_bf16 v[108:111], a[192:195], a[216:219], v[108:111]\n" \
"s_add_u32 m0, s91, 86016\n" \
"ds_read_b128 a[28:31], v201 offset:8192\n" \
"v_mul_f32 v118, v118, v232\n" \
"v_mfma_f32_16x16x32_bf16 v[108:111], a[196:199], a[220:223], v[108:111]\n" \
"buffer_load_dwordx4 v197, s[48:51], s76 offen offset:0 lds\n" \
"v_mul_f32 v119, v119, v232\n" \
"v_mfma_f32_16x16x32_bf16 v[104:107], a[192:195], a[208:211], v[104:107]\n" \
"v_cvt_pk_bf16_f32 v120, v112, v113\n" \
"v_cvt_pk_bf16_f32 v121, v114, v115\n" \
"v_mfma_f32_16x16x32_bf16 v[104:107], a[196:199], a[212:215], v[104:107]\n" \
"s_waitcnt lgkmcnt(2)\n" \
"v_cvt_pk_bf16_f32 v122, v116, v117\n" \
"v_cvt_pk_bf16_f32 v123, v118, v119\n" \
"v_mfma_f32_16x16x32_bf16 v[32:35], a[32:35], a[0:3], v[32:35]\n" \
"s_add_u32 s77, s76, 24576\n" \
"ds_read_b128 a[192:195], v200 offset:12288\n" \
"v_mfma_f32_16x16x32_bf16 v[32:35], a[36:39], a[4:7], v[32:35]\n" \
"buffer_load_dwordx4 v198, s[48:51], s76 offen offset:1024 lds\n" \
"v_mfma_f32_16x16x32_bf16 v[40:43], a[32:35], a[8:11], v[40:43]\n" \
"ds_read_b128 a[196:199], v201 offset:12288\n" \
"v_mfma_f32_16x16x32_bf16 v[40:43], a[36:39], a[12:15], v[40:43]\n" \
"ds_read_b64_tr_b16 a[200:201], v222 offset:21504\n" \
"buffer_store_dwordx4 v[120:123], v240, s[56:59], s88 offen offset:0 sc1\n" \
"v_mfma_f32_16x16x32_bf16 v[0:3], a[40:43], a[8:11], v[0:3]\n" \
"s_xor_b32 s80, s80, 0x6\n" \
"ds_read_b64_tr_b16 a[202:203], v222 offset:24576\n" \
"v_mfma_f32_16x16x32_bf16 v[224:227], v[244:247], v[120:123], v[224:227]\n" \
"v_accvgpr_read_b32 v112, a88\n" \
"v_accvgpr_read_b32 v113, a89\n" \
"v_mfma_f32_16x16x32_bf16 v[0:3], a[44:47], a[12:15], v[0:3]\n" \
"buffer_load_dwordx4 v199, s[48:51], s76 offen offset:2048 lds\n" \
"v_mfma_f32_16x16x32_bf16 v[4:7], a[48:51], a[8:11], v[4:7]\n" \
"ds_read_b64_tr_b16 a[204:205], v222 offset:33792\n" \
"v_accvgpr_read_b32 v114, a90\n" \
"v_mfma_f32_16x16x32_bf16 v[4:7], a[52:55], a[12:15], v[4:7]\n" \
"ds_read_b64_tr_b16 a[206:207], v222 offset:36864\n" \
"v_accvgpr_read_b32 v115, a91\n" \
"v_accvgpr_read_b32 v116, a92\n" \
"v_mfma_f32_16x16x32_bf16 v[12:15], a[48:51], a[16:19], v[12:15]\n" \
"s_add_u32 m0, m0, 3072\n" \
"ds_read_b64_tr_b16 a[208:209], v204 offset:22528\n" \
"v_accvgpr_read_b32 v117, a93\n" \
"v_mfma_f32_16x16x32_bf16 v[12:15], a[52:55], a[20:23], v[12:15]\n" \
"buffer_load_dwordx4 v197, s[48:51], s77 offen offset:0 lds\n" \
"v_mfma_f32_16x16x32_bf16 v[8:11], a[40:43], a[16:19], v[8:11]\n" \
"ds_read_b64_tr_b16 a[210:211], v204 offset:25600\n" \
"v_accvgpr_read_b32 v118, a94\n" \
"v_mfma_f32_16x16x32_bf16 v[8:11], a[44:47], a[20:23], v[8:11]\n" \
"ds_read_b64_tr_b16 a[212:213], v204 offset:34816\n" \
"v_accvgpr_read_b32 v119, a95\n" \
"v_add_f32 v112, v112, v152\n" \
"v_mfma_f32_16x16x32_bf16 v[48:51], a[32:35], a[16:19], v[48:51]\n" \
"s_waitcnt lgkmcnt(3)\n" \
"ds_read_b64_tr_b16 a[214:215], v204 offset:37888\n" \
"v_add_f32 v113, v113, v153\n" \
"v_mfma_f32_16x16x32_bf16 v[48:51], a[36:39], a[20:23], v[48:51]\n" \
"buffer_load_dwordx4 v198, s[48:51], s77 offen offset:1024 lds\n" \
"v_mfma_f32_16x16x32_bf16 v[56:59], a[32:35], a[24:27], v[56:59]\n" \
"ds_read_b64_tr_b16 a[216:217], v222 offset:22528\n" \
"v_add_f32 v114, v114, v154\n" \
"v_mfma_f32_16x16x32_bf16 v[56:59], a[36:39], a[28:31], v[56:59]\n" \
"ds_read_b64_tr_b16 a[218:219], v222 offset:25600\n" \
"v_add_f32 v115, v115, v155\n" \
"v_add_f32 v116, v116, v156\n" \
"v_mfma_f32_16x16x32_bf16 v[16:19], a[40:43], a[24:27], v[16:19]\n" \
"ds_read_b64_tr_b16 a[220:221], v222 offset:34816\n" \
"v_add_f32 v117, v117, v157\n" \
"v_mfma_f32_16x16x32_bf16 v[16:19], a[44:47], a[28:31], v[16:19]\n" \
"buffer_load_dwordx4 v199, s[48:51], s77 offen offset:2048 lds\n" \
"v_add_f32 v118, v118, v158\n" \
"v_mfma_f32_16x16x32_bf16 v[20:23], a[48:51], a[24:27], v[20:23]\n" \
"ds_read_b64_tr_b16 a[222:223], v222 offset:37888\n" \
"v_add_f32 v119, v119, v159\n" \
"v_mfma_f32_16x16x32_bf16 v[20:23], a[52:55], a[28:31], v[20:23]\n" \
"v_mul_f32 v112, v112, v232\n" \
"v_mul_f32 v113, v113, v232\n" \
"v_mfma_f32_16x16x32_bf16 v[28:31], a[48:51], a[192:195], v[28:31]\n" \
"v_mul_f32 v114, v114, v232\n" \
"v_mul_f32 v115, v115, v232\n" \
"v_mfma_f32_16x16x32_bf16 v[28:31], a[52:55], a[196:199], v[28:31]\n" \
"v_mul_f32 v116, v116, v232\n" \
"v_mul_f32 v117, v117, v232\n" \
"v_mul_f32 v118, v118, v232\n" \
"v_mfma_f32_16x16x32_bf16 v[24:27], a[40:43], a[192:195], v[24:27]\n" \
"v_mul_f32 v119, v119, v232\n" \
"v_cvt_pk_bf16_f32 v124, v112, v113\n" \
"v_mfma_f32_16x16x32_bf16 v[24:27], a[44:47], a[196:199], v[24:27]\n" \
"v_cvt_pk_bf16_f32 v125, v114, v115\n" \
"v_cvt_pk_bf16_f32 v126, v116, v117\n" \
"v_mfma_f32_16x16x32_bf16 v[64:67], a[32:35], a[192:195], v[64:67]\n" \
"v_cvt_pk_bf16_f32 v127, v118, v119\n" \
"v_mfma_f32_16x16x32_bf16 v[64:67], a[36:39], a[196:199], v[64:67]\n" \
"buffer_store_dwordx4 v[124:127], v240, s[56:59], s88 offen offset:2048 sc1\n" \
"s_waitcnt vmcnt(14) lgkmcnt(0)\n" \
"s_barrier\n" \
"v_mfma_f32_16x16x32_bf16 v[68:71], a[200:203], a[192:195], v[68:71]\n" \
"ds_read_b64_tr_b16 a[32:33], v194 offset:1024\n" \
"ds_read_b64_tr_b16 a[34:35], v194 offset:4096\n" \
"v_mfma_f32_16x16x32_bf16 v[68:71], a[204:207], a[196:199], v[68:71]\n" \
"ds_read_b64_tr_b16 a[36:37], v194 offset:13312\n" \
"ds_read_b64_tr_b16 a[38:39], v194 offset:16384\n" \
"v_mfma_f32_16x16x32_bf16 v[224:227], v[244:247], v[124:127], v[224:227]\n" \
"s_add_u32 s82, s88, 532480\n" \
"v_accvgpr_read_b32 v112, a96\n" \
"v_accvgpr_read_b32 v113, a97\n" \
"v_accvgpr_read_b32 v114, a98\n" \
"v_accvgpr_read_b32 v115, a99\n" \
"v_mfma_f32_16x16x32_bf16 v[60:63], a[200:203], a[24:27], v[60:63]\n" \
"s_add_u32 m0, s79, 0\n" \
"ds_read_b64_tr_b16 a[40:41], v194 offset:0\n" \
"v_accvgpr_read_b32 v116, a100\n" \
"v_mfma_f32_16x16x32_bf16 v[60:63], a[204:207], a[28:31], v[60:63]\n" \
"buffer_load_dwordx4 v192, s[44:47], s73 offen offset:0 lds\n" \
"v_mfma_f32_16x16x32_bf16 v[52:55], a[200:203], a[16:19], v[52:55]\n" \
"ds_read_b64_tr_b16 a[42:43], v194 offset:3072\n" \
"v_accvgpr_read_b32 v117, a101\n" \
"v_mfma_f32_16x16x32_bf16 v[52:55], a[204:207], a[20:23], v[52:55]\n" \
"ds_read_b64_tr_b16 a[44:45], v194 offset:12288\n" \
"v_accvgpr_read_b32 v118, a102\n" \
"v_accvgpr_read_b32 v119, a103\n" \
"v_mfma_f32_16x16x32_bf16 v[44:47], a[200:203], a[8:11], v[44:47]\n" \
"ds_read_b64_tr_b16 a[46:47], v194 offset:15360\n" \
"v_add_f32 v112, v112, v160\n" \
"v_mfma_f32_16x16x32_bf16 v[44:47], a[204:207], a[12:15], v[44:47]\n" \
"buffer_load_dwordx4 v193, s[44:47], s73 offen offset:1024 lds\n" \
"v_mfma_f32_16x16x32_bf16 v[72:75], a[0:3], a[208:211], v[72:75]\n" \
"ds_read_b64_tr_b16 a[48:49], v195 offset:0\n" \
"v_add_f32 v113, v113, v161\n" \
"v_mfma_f32_16x16x32_bf16 v[72:75], a[4:7], a[212:215], v[72:75]\n" \
"ds_read_b128 a[0:3], v207 offset:45056\n" \
"v_add_f32 v114, v114, v162\n" \
"v_add_f32 v115, v115, v163\n" \
"v_mfma_f32_16x16x32_bf16 v[80:83], a[8:11], a[208:211], v[80:83]\n" \
"ds_read_b128 a[4:7], v208 offset:45056\n" \
"v_add_f32 v116, v116, v164\n" \
"v_mfma_f32_16x16x32_bf16 v[80:83], a[12:15], a[212:215], v[80:83]\n" \
"buffer_load_dwordx4 v192, s[44:47], s73 offen offset:2048 lds\n" \
"v_add_f32 v117, v117, v165\n" \
"v_mfma_f32_16x16x32_bf16 v[84:87], a[8:11], a[216:219], v[84:87]\n" \
"ds_read_b64_tr_b16 a[50:51], v195 offset:3072\n" \
"v_add_f32 v118, v118, v166\n" \
"v_mfma_f32_16x16x32_bf16 v[84:87], a[12:15], a[220:223], v[84:87]\n" \
"ds_read_b128 a[8:11], v205 offset:45056\n" \
"v_add_f32 v119, v119, v167\n" \
"v_mfma_f32_16x16x32_bf16 v[92:95], a[16:19], a[216:219], v[92:95]\n" \
"ds_read_b128 a[12:15], v206 offset:45056\n" \
"v_mul_f32 v112, v112, v233\n" \
"v_mfma_f32_16x16x32_bf16 v[92:95], a[20:23], a[220:223], v[92:95]\n" \
"buffer_load_dwordx4 v193, s[44:47], s73 offen offset:3072 lds\n" \
"v_mul_f32 v113, v113, v233\n" \
"v_mfma_f32_16x16x32_bf16 v[88:91], a[16:19], a[208:211], v[88:91]\n" \
"ds_read_b64_tr_b16 a[52:53], v195 offset:12288\n" \
"v_mul_f32 v114, v114, v233\n" \
"v_mfma_f32_16x16x32_bf16 v[88:91], a[20:23], a[212:215], v[88:91]\n" \
"s_add_u32 m0, s89, 0\n" \
"ds_read_b128 a[16:19], v205 offset:49152\n" \
"v_mul_f32 v115, v115, v233\n" \
"v_mfma_f32_16x16x32_bf16 v[96:99], a[24:27], a[208:211], v[96:99]\n" \
"s_xor_b32 s73, s73, 0x80\n" \
"ds_read_b128 a[20:23], v206 offset:49152\n" \
"v_mul_f32 v116, v116, v233\n" \
"v_mfma_f32_16x16x32_bf16 v[96:99], a[28:31], a[212:215], v[96:99]\n" \
"buffer_load_dwordx4 v196, s[44:47], s75 offen lds\n" \
"v_mul_f32 v117, v117, v233\n" \
"v_mfma_f32_16x16x32_bf16 v[100:103], a[24:27], a[216:219], v[100:103]\n" \
"s_xor_b32 s75, s75, 0x80\n" \
"s_mul_i32 s76, s80, 393216\n" \
"ds_read_b64_tr_b16 a[54:55], v195 offset:15360\n" \
"v_mfma_f32_16x16x32_bf16 v[100:103], a[28:31], a[220:223], v[100:103]\n" \
"s_add_u32 s76, s76, s78\n" \
"ds_read_b128 a[24:27], v205 offset:53248\n" \
"v_mul_f32 v118, v118, v233\n" \
"v_mul_f32 v119, v119, v233\n" \
"v_mfma_f32_16x16x32_bf16 v[108:111], a[192:195], a[216:219], v[108:111]\n" \
"s_add_u32 m0, s91, 20480\n" \
"ds_read_b128 a[28:31], v206 offset:53248\n" \
"v_cvt_pk_bf16_f32 v120, v112, v113\n" \
"v_mfma_f32_16x16x32_bf16 v[108:111], a[196:199], a[220:223], v[108:111]\n" \
"buffer_load_dwordx4 v197, s[48:51], s76 offen offset:0 lds\n" \
"v_mfma_f32_16x16x32_bf16 v[104:107], a[192:195], a[208:211], v[104:107]\n" \
"v_cvt_pk_bf16_f32 v121, v114, v115\n" \
"v_cvt_pk_bf16_f32 v122, v116, v117\n" \
"v_mfma_f32_16x16x32_bf16 v[104:107], a[196:199], a[212:215], v[104:107]\n" \
"s_waitcnt lgkmcnt(2)\n" \
"v_cvt_pk_bf16_f32 v123, v118, v119\n" \
"v_mfma_f32_16x16x32_bf16 v[32:35], a[32:35], a[0:3], v[32:35]\n" \
"s_add_u32 s77, s76, 24576\n" \
"ds_read_b128 a[192:195], v205 offset:57344\n" \
"v_mfma_f32_16x16x32_bf16 v[32:35], a[36:39], a[4:7], v[32:35]\n" \
"buffer_load_dwordx4 v198, s[48:51], s76 offen offset:1024 lds\n" \
"v_mfma_f32_16x16x32_bf16 v[40:43], a[32:35], a[8:11], v[40:43]\n" \
"ds_read_b128 a[196:199], v206 offset:57344\n" \
"v_mfma_f32_16x16x32_bf16 v[40:43], a[36:39], a[12:15], v[40:43]\n" \
"ds_read_b64_tr_b16 a[200:201], v195 offset:1024\n" \
"buffer_store_dwordx4 v[120:123], v236, s[56:59], s82 offen offset:0 sc1\n" \
"v_mfma_f32_16x16x32_bf16 v[0:3], a[40:43], a[8:11], v[0:3]\n" \
"s_xor_b32 s80, s80, 0x1\n" \
"ds_read_b64_tr_b16 a[202:203], v195 offset:4096\n" \
"v_mfma_f32_16x16x32_bf16 v[224:227], v[244:247], v[120:123], v[224:227]\n" \
"v_accvgpr_read_b32 v112, a104\n" \
"v_accvgpr_read_b32 v113, a105\n" \
"v_mfma_f32_16x16x32_bf16 v[0:3], a[44:47], a[12:15], v[0:3]\n" \
"buffer_load_dwordx4 v199, s[48:51], s76 offen offset:2048 lds\n" \
"v_accvgpr_read_b32 v114, a106\n" \
"v_mfma_f32_16x16x32_bf16 v[4:7], a[48:51], a[8:11], v[4:7]\n" \
"ds_read_b64_tr_b16 a[204:205], v195 offset:13312\n" \
"v_accvgpr_read_b32 v115, a107\n" \
"v_mfma_f32_16x16x32_bf16 v[4:7], a[52:55], a[12:15], v[4:7]\n" \
"ds_read_b64_tr_b16 a[206:207], v195 offset:16384\n" \
"v_accvgpr_read_b32 v116, a108\n" \
"v_mfma_f32_16x16x32_bf16 v[12:15], a[48:51], a[16:19], v[12:15]\n" \
"s_add_u32 m0, m0, 3072\n" \
"ds_read_b64_tr_b16 a[208:209], v194 offset:2048\n" \
"v_accvgpr_read_b32 v117, a109\n" \
"v_mfma_f32_16x16x32_bf16 v[12:15], a[52:55], a[20:23], v[12:15]\n" \
"buffer_load_dwordx4 v197, s[48:51], s77 offen offset:0 lds\n" \
"v_accvgpr_read_b32 v118, a110\n" \
"v_mfma_f32_16x16x32_bf16 v[8:11], a[40:43], a[16:19], v[8:11]\n" \
"ds_read_b64_tr_b16 a[210:211], v194 offset:5120\n" \
"v_accvgpr_read_b32 v119, a111\n" \
"v_mfma_f32_16x16x32_bf16 v[8:11], a[44:47], a[20:23], v[8:11]\n" \
"ds_read_b64_tr_b16 a[212:213], v194 offset:14336\n" \
"v_add_f32 v112, v112, v168\n" \
"v_mfma_f32_16x16x32_bf16 v[48:51], a[32:35], a[16:19], v[48:51]\n" \
"s_waitcnt lgkmcnt(3)\n" \
"ds_read_b64_tr_b16 a[214:215], v194 offset:17408\n" \
"v_add_f32 v113, v113, v169\n" \
"v_mfma_f32_16x16x32_bf16 v[48:51], a[36:39], a[20:23], v[48:51]\n" \
"buffer_load_dwordx4 v198, s[48:51], s77 offen offset:1024 lds\n" \
"v_add_f32 v114, v114, v170\n" \
"v_mfma_f32_16x16x32_bf16 v[56:59], a[32:35], a[24:27], v[56:59]\n" \
"ds_read_b64_tr_b16 a[216:217], v195 offset:2048\n" \
"v_add_f32 v115, v115, v171\n" \
"v_mfma_f32_16x16x32_bf16 v[56:59], a[36:39], a[28:31], v[56:59]\n" \
"ds_read_b64_tr_b16 a[218:219], v195 offset:5120\n" \
"v_add_f32 v116, v116, v172\n" \
"v_add_f32 v117, v117, v173\n" \
"v_mfma_f32_16x16x32_bf16 v[16:19], a[40:43], a[24:27], v[16:19]\n" \
"ds_read_b64_tr_b16 a[220:221], v195 offset:14336\n" \
"v_add_f32 v118, v118, v174\n" \
"v_mfma_f32_16x16x32_bf16 v[16:19], a[44:47], a[28:31], v[16:19]\n" \
"buffer_load_dwordx4 v199, s[48:51], s77 offen offset:2048 lds\n" \
"v_mfma_f32_16x16x32_bf16 v[20:23], a[48:51], a[24:27], v[20:23]\n" \
"ds_read_b64_tr_b16 a[222:223], v195 offset:17408\n" \
"v_add_f32 v119, v119, v175\n" \
"v_mfma_f32_16x16x32_bf16 v[20:23], a[52:55], a[28:31], v[20:23]\n" \
"v_mul_f32 v112, v112, v233\n" \
"v_mul_f32 v113, v113, v233\n" \
"v_mul_f32 v114, v114, v233\n" \
"v_mfma_f32_16x16x32_bf16 v[28:31], a[48:51], a[192:195], v[28:31]\n" \
"v_mul_f32 v115, v115, v233\n" \
"v_mul_f32 v116, v116, v233\n" \
"v_mfma_f32_16x16x32_bf16 v[28:31], a[52:55], a[196:199], v[28:31]\n" \
"v_mul_f32 v117, v117, v233\n" \
"v_mul_f32 v118, v118, v233\n" \
"v_mfma_f32_16x16x32_bf16 v[24:27], a[40:43], a[192:195], v[24:27]\n" \
"v_mul_f32 v119, v119, v233\n" \
"v_cvt_pk_bf16_f32 v124, v112, v113\n" \
"v_mfma_f32_16x16x32_bf16 v[24:27], a[44:47], a[196:199], v[24:27]\n" \
"v_cvt_pk_bf16_f32 v125, v114, v115\n" \
"v_cvt_pk_bf16_f32 v126, v116, v117\n" \
"v_cvt_pk_bf16_f32 v127, v118, v119\n" \
"v_mfma_f32_16x16x32_bf16 v[64:67], a[32:35], a[192:195], v[64:67]\n" \
"buffer_store_dwordx4 v[124:127], v236, s[56:59], s82 offen offset:2048 sc1\n" \
"v_mfma_f32_16x16x32_bf16 v[64:67], a[36:39], a[196:199], v[64:67]\n" \
"v_mfma_f32_16x16x32_bf16 v[224:227], v[244:247], v[124:127], v[224:227]\n" \
"v_accvgpr_read_b32 v112, a112\n" \
"s_waitcnt vmcnt(14) lgkmcnt(0)\n" \
"s_barrier\n" \
"v_mfma_f32_16x16x32_bf16 v[68:71], a[200:203], a[192:195], v[68:71]\n" \
"ds_read_b64_tr_b16 a[32:33], v209 offset:1024\n" \
"ds_read_b64_tr_b16 a[34:35], v209 offset:4096\n" \
"v_mfma_f32_16x16x32_bf16 v[68:71], a[204:207], a[196:199], v[68:71]\n" \
"ds_read_b64_tr_b16 a[36:37], v209 offset:13312\n" \
"ds_read_b64_tr_b16 a[38:39], v209 offset:16384\n" \
"v_mfma_f32_16x16x32_bf16 v[60:63], a[200:203], a[24:27], v[60:63]\n" \
"s_add_u32 m0, s79, 110592\n" \
"ds_read_b64_tr_b16 a[40:41], v209 offset:0\n" \
"v_accvgpr_read_b32 v113, a113\n" \
"v_mfma_f32_16x16x32_bf16 v[60:63], a[204:207], a[28:31], v[60:63]\n" \
"buffer_load_dwordx4 v192, s[44:47], s73 offen offset:0 lds\n" \
"v_accvgpr_read_b32 v114, a114\n" \
"v_mfma_f32_16x16x32_bf16 v[52:55], a[200:203], a[16:19], v[52:55]\n" \
"Lpg7962_%=:\n" \
"Lst1x0_%=:\n" \
"s_cbranch_execz Lpg10257_%=\n" \
"ds_read_b64_tr_b16 a[42:43], v209 offset:3072\n" \
"v_accvgpr_read_b32 v115, a115\n" \
"v_mfma_f32_16x16x32_bf16 v[52:55], a[204:207], a[20:23], v[52:55]\n" \
"ds_read_b64_tr_b16 a[44:45], v209 offset:12288\n" \
"v_accvgpr_read_b32 v116, a116\n" \
"v_mfma_f32_16x16x32_bf16 v[44:47], a[200:203], a[8:11], v[44:47]\n" \
"ds_read_b64_tr_b16 a[46:47], v209 offset:15360\n" \
"v_accvgpr_read_b32 v117, a117\n" \
"v_mfma_f32_16x16x32_bf16 v[44:47], a[204:207], a[12:15], v[44:47]\n" \
"buffer_load_dwordx4 v193, s[44:47], s73 offen offset:1024 lds\n" \
"v_accvgpr_read_b32 v118, a118\n" \
"v_mfma_f32_16x16x32_bf16 v[72:75], a[0:3], a[208:211], v[72:75]\n" \
"ds_read_b64_tr_b16 a[48:49], v210 offset:0\n" \
"v_accvgpr_read_b32 v119, a119\n" \
"v_mfma_f32_16x16x32_bf16 v[72:75], a[4:7], a[212:215], v[72:75]\n" \
"ds_read_b128 a[0:3], v207 offset:0\n" \
"v_add_f32 v112, v112, v176\n" \
"v_add_f32 v113, v113, v177\n" \
"v_mfma_f32_16x16x32_bf16 v[80:83], a[8:11], a[208:211], v[80:83]\n" \
"ds_read_b128 a[4:7], v208 offset:0\n" \
"v_add_f32 v114, v114, v178\n" \
"v_mfma_f32_16x16x32_bf16 v[80:83], a[12:15], a[212:215], v[80:83]\n" \
"buffer_load_dwordx4 v192, s[44:47], s73 offen offset:2048 lds\n" \
"v_mfma_f32_16x16x32_bf16 v[84:87], a[8:11], a[216:219], v[84:87]\n" \
"ds_read_b64_tr_b16 a[50:51], v210 offset:3072\n" \
"v_add_f32 v115, v115, v179\n" \
"v_mfma_f32_16x16x32_bf16 v[84:87], a[12:15], a[220:223], v[84:87]\n" \
"ds_read_b128 a[8:11], v205 offset:0\n" \
"v_add_f32 v116, v116, v180\n" \
"v_add_f32 v117, v117, v181\n" \
"v_mfma_f32_16x16x32_bf16 v[92:95], a[16:19], a[216:219], v[92:95]\n" \
"ds_read_b128 a[12:15], v206 offset:0\n" \
"v_add_f32 v118, v118, v182\n" \
"v_mfma_f32_16x16x32_bf16 v[92:95], a[20:23], a[220:223], v[92:95]\n" \
"buffer_load_dwordx4 v193, s[44:47], s73 offen offset:3072 lds\n" \
"v_mfma_f32_16x16x32_bf16 v[88:91], a[16:19], a[208:211], v[88:91]\n" \
"ds_read_b64_tr_b16 a[52:53], v210 offset:12288\n" \
"v_add_f32 v119, v119, v183\n" \
"v_mfma_f32_16x16x32_bf16 v[88:91], a[20:23], a[212:215], v[88:91]\n" \
"s_add_u32 m0, s89, 110592\n" \
"ds_read_b128 a[16:19], v205 offset:4096\n" \
"v_mul_f32 v112, v112, v233\n" \
"v_mul_f32 v113, v113, v233\n" \
"v_mfma_f32_16x16x32_bf16 v[96:99], a[24:27], a[208:211], v[96:99]\n" \
"s_xor_b32 s73, s73, 0x180\n" \
"ds_read_b128 a[20:23], v206 offset:4096\n" \
"v_mul_f32 v114, v114, v233\n" \
"v_mfma_f32_16x16x32_bf16 v[96:99], a[28:31], a[212:215], v[96:99]\n" \
"buffer_load_dwordx4 v196, s[44:47], s75 offen lds\n" \
"v_mul_f32 v115, v115, v233\n" \
"v_mfma_f32_16x16x32_bf16 v[100:103], a[24:27], a[216:219], v[100:103]\n" \
"s_xor_b32 s75, s75, 0x180\n" \
"s_mul_i32 s76, s80, 393216\n" \
"ds_read_b64_tr_b16 a[54:55], v210 offset:15360\n" \
"v_mfma_f32_16x16x32_bf16 v[100:103], a[28:31], a[220:223], v[100:103]\n" \
"s_add_u32 s76, s76, s78\n" \
"ds_read_b128 a[24:27], v205 offset:8192\n" \
"v_mul_f32 v116, v116, v233\n" \
"v_mfma_f32_16x16x32_bf16 v[108:111], a[192:195], a[216:219], v[108:111]\n" \
"s_add_u32 m0, s91, 139264\n" \
"ds_read_b128 a[28:31], v206 offset:8192\n" \
"v_mul_f32 v117, v117, v233\n" \
"v_mfma_f32_16x16x32_bf16 v[108:111], a[196:199], a[220:223], v[108:111]\n" \
"buffer_load_dwordx4 v197, s[48:51], s76 offen offset:0 lds\n" \
"v_mul_f32 v118, v118, v233\n" \
"v_mfma_f32_16x16x32_bf16 v[104:107], a[192:195], a[208:211], v[104:107]\n" \
"v_mul_f32 v119, v119, v233\n" \
"v_cvt_pk_bf16_f32 v120, v112, v113\n" \
"v_mfma_f32_16x16x32_bf16 v[104:107], a[196:199], a[212:215], v[104:107]\n" \
"s_waitcnt lgkmcnt(2)\n" \
"v_cvt_pk_bf16_f32 v121, v114, v115\n" \
"v_cvt_pk_bf16_f32 v122, v116, v117\n" \
"v_mfma_f32_16x16x32_bf16 v[32:35], a[32:35], a[0:3], v[32:35]\n" \
"s_add_u32 s77, s76, 24576\n" \
"ds_read_b128 a[192:195], v205 offset:12288\n" \
"v_cvt_pk_bf16_f32 v123, v118, v119\n" \
"v_mfma_f32_16x16x32_bf16 v[32:35], a[36:39], a[4:7], v[32:35]\n" \
"buffer_load_dwordx4 v198, s[48:51], s76 offen offset:1024 lds\n" \
"v_mfma_f32_16x16x32_bf16 v[40:43], a[32:35], a[8:11], v[40:43]\n" \
"ds_read_b128 a[196:199], v206 offset:12288\n" \
"v_mfma_f32_16x16x32_bf16 v[40:43], a[36:39], a[12:15], v[40:43]\n" \
"ds_read_b64_tr_b16 a[200:201], v210 offset:1024\n" \
"buffer_store_dwordx4 v[120:123], v240, s[56:59], s82 offen offset:0 sc1\n" \
"v_mfma_f32_16x16x32_bf16 v[0:3], a[40:43], a[8:11], v[0:3]\n" \
"s_xor_b32 s80, s80, 0x3\n" \
"ds_read_b64_tr_b16 a[202:203], v210 offset:4096\n" \
"v_mfma_f32_16x16x32_bf16 v[224:227], v[244:247], v[120:123], v[224:227]\n" \
"v_accvgpr_read_b32 v112, a120\n" \
"v_accvgpr_read_b32 v113, a121\n" \
"v_mfma_f32_16x16x32_bf16 v[0:3], a[44:47], a[12:15], v[0:3]\n" \
"buffer_load_dwordx4 v199, s[48:51], s76 offen offset:2048 lds\n" \
"v_mfma_f32_16x16x32_bf16 v[4:7], a[48:51], a[8:11], v[4:7]\n" \
"ds_read_b64_tr_b16 a[204:205], v210 offset:13312\n" \
"v_accvgpr_read_b32 v114, a122\n" \
"v_mfma_f32_16x16x32_bf16 v[4:7], a[52:55], a[12:15], v[4:7]\n" \
"ds_read_b64_tr_b16 a[206:207], v210 offset:16384\n" \
"v_accvgpr_read_b32 v115, a123\n" \
"v_accvgpr_read_b32 v116, a124\n" \
"v_mfma_f32_16x16x32_bf16 v[12:15], a[48:51], a[16:19], v[12:15]\n" \
"s_add_u32 m0, m0, 3072\n" \
"ds_read_b64_tr_b16 a[208:209], v209 offset:2048\n" \
"v_accvgpr_read_b32 v117, a125\n" \
"v_mfma_f32_16x16x32_bf16 v[12:15], a[52:55], a[20:23], v[12:15]\n" \
"buffer_load_dwordx4 v197, s[48:51], s77 offen offset:0 lds\n" \
"v_mfma_f32_16x16x32_bf16 v[8:11], a[40:43], a[16:19], v[8:11]\n" \
"ds_read_b64_tr_b16 a[210:211], v209 offset:5120\n" \
"v_accvgpr_read_b32 v118, a126\n" \
"v_mfma_f32_16x16x32_bf16 v[8:11], a[44:47], a[20:23], v[8:11]\n" \
"ds_read_b64_tr_b16 a[212:213], v209 offset:14336\n" \
"v_accvgpr_read_b32 v119, a127\n" \
"v_add_f32 v112, v112, v184\n" \
"v_mfma_f32_16x16x32_bf16 v[48:51], a[32:35], a[16:19], v[48:51]\n" \
"s_waitcnt lgkmcnt(3)\n" \
"ds_read_b64_tr_b16 a[214:215], v209 offset:17408\n" \
"v_add_f32 v113, v113, v185\n" \
"v_mfma_f32_16x16x32_bf16 v[48:51], a[36:39], a[20:23], v[48:51]\n" \
"buffer_load_dwordx4 v198, s[48:51], s77 offen offset:1024 lds\n" \
"v_add_f32 v114, v114, v186\n" \
"v_mfma_f32_16x16x32_bf16 v[56:59], a[32:35], a[24:27], v[56:59]\n" \
"ds_read_b64_tr_b16 a[216:217], v210 offset:2048\n" \
"v_add_f32 v115, v115, v187\n" \
"v_mfma_f32_16x16x32_bf16 v[56:59], a[36:39], a[28:31], v[56:59]\n" \
"ds_read_b64_tr_b16 a[218:219], v210 offset:5120\n" \
"v_add_f32 v116, v116, v188\n" \
"v_mfma_f32_16x16x32_bf16 v[16:19], a[40:43], a[24:27], v[16:19]\n" \
"ds_read_b64_tr_b16 a[220:221], v210 offset:14336\n" \
"v_add_f32 v117, v117, v189\n" \
"v_mfma_f32_16x16x32_bf16 v[16:19], a[44:47], a[28:31], v[16:19]\n" \
"buffer_load_dwordx4 v199, s[48:51], s77 offen offset:2048 lds\n" \
"v_add_f32 v118, v118, v190\n" \
"v_mfma_f32_16x16x32_bf16 v[20:23], a[48:51], a[24:27], v[20:23]\n" \
"ds_read_b64_tr_b16 a[222:223], v210 offset:17408\n" \
"v_add_f32 v119, v119, v191\n" \
"v_mfma_f32_16x16x32_bf16 v[20:23], a[52:55], a[28:31], v[20:23]\n" \
"v_mul_f32 v112, v112, v233\n" \
"v_mul_f32 v113, v113, v233\n" \
"v_mfma_f32_16x16x32_bf16 v[28:31], a[48:51], a[192:195], v[28:31]\n" \
"v_mul_f32 v114, v114, v233\n" \
"v_mul_f32 v115, v115, v233\n" \
"v_mfma_f32_16x16x32_bf16 v[28:31], a[52:55], a[196:199], v[28:31]\n" \
"v_mul_f32 v116, v116, v233\n" \
"v_mul_f32 v117, v117, v233\n" \
"v_mul_f32 v118, v118, v233\n" \
"v_mfma_f32_16x16x32_bf16 v[24:27], a[40:43], a[192:195], v[24:27]\n" \
"v_mul_f32 v119, v119, v233\n" \
"v_cvt_pk_bf16_f32 v124, v112, v113\n" \
"v_mfma_f32_16x16x32_bf16 v[24:27], a[44:47], a[196:199], v[24:27]\n" \
"v_cvt_pk_bf16_f32 v125, v114, v115\n" \
"v_cvt_pk_bf16_f32 v126, v116, v117\n" \
"v_cvt_pk_bf16_f32 v127, v118, v119\n" \
"v_mfma_f32_16x16x32_bf16 v[64:67], a[32:35], a[192:195], v[64:67]\n" \
"buffer_store_dwordx4 v[124:127], v240, s[56:59], s82 offen offset:2048 sc1\n" \
"v_mfma_f32_16x16x32_bf16 v[64:67], a[36:39], a[196:199], v[64:67]\n" \
"v_mfma_f32_16x16x32_bf16 v[224:227], v[244:247], v[124:127], v[224:227]\n" \
"s_waitcnt vmcnt(14) lgkmcnt(0)\n" \
"s_barrier\n" \
"v_mfma_f32_16x16x32_bf16 v[68:71], a[200:203], a[192:195], v[68:71]\n" \
"ds_read_b64_tr_b16 a[32:33], v204 offset:21504\n" \
"ds_read_b64_tr_b16 a[34:35], v204 offset:24576\n" \
"v_mfma_f32_16x16x32_bf16 v[68:71], a[204:207], a[196:199], v[68:71]\n" \
"ds_read_b64_tr_b16 a[36:37], v204 offset:33792\n" \
"ds_read_b64_tr_b16 a[38:39], v204 offset:36864\n" \
"v_mfma_f32_16x16x32_bf16 v[60:63], a[200:203], a[24:27], v[60:63]\n" \
"s_add_u32 m0, s79, 65536\n" \
"ds_read_b64_tr_b16 a[40:41], v204 offset:20480\n" \
"v_mfma_f32_16x16x32_bf16 v[60:63], a[204:207], a[28:31], v[60:63]\n" \
"buffer_load_dwordx4 v192, s[44:47], s73 offen offset:0 lds\n" \
"v_mfma_f32_16x16x32_bf16 v[52:55], a[200:203], a[16:19], v[52:55]\n" \
"ds_read_b64_tr_b16 a[42:43], v204 offset:23552\n" \
"v_mfma_f32_16x16x32_bf16 v[52:55], a[204:207], a[20:23], v[52:55]\n" \
"ds_read_b64_tr_b16 a[44:45], v204 offset:32768\n" \
"v_mfma_f32_16x16x32_bf16 v[44:47], a[200:203], a[8:11], v[44:47]\n" \
"ds_read_b64_tr_b16 a[46:47], v204 offset:35840\n" \
"v_mfma_f32_16x16x32_bf16 v[44:47], a[204:207], a[12:15], v[44:47]\n" \
"buffer_load_dwordx4 v193, s[44:47], s73 offen offset:1024 lds\n" \
"v_mfma_f32_16x16x32_bf16 v[72:75], a[0:3], a[208:211], v[72:75]\n" \
"ds_read_b64_tr_b16 a[48:49], v222 offset:20480\n" \
"v_mfma_f32_16x16x32_bf16 v[72:75], a[4:7], a[212:215], v[72:75]\n" \
"ds_read_b128 a[0:3], v202 offset:0\n" \
"v_mfma_f32_16x16x32_bf16 v[80:83], a[8:11], a[208:211], v[80:83]\n" \
"ds_read_b128 a[4:7], v203 offset:0\n" \
"v_mfma_f32_16x16x32_bf16 v[80:83], a[12:15], a[212:215], v[80:83]\n" \
"buffer_load_dwordx4 v192, s[44:47], s73 offen offset:2048 lds\n" \
"v_mfma_f32_16x16x32_bf16 v[84:87], a[8:11], a[216:219], v[84:87]\n" \
"ds_read_b64_tr_b16 a[50:51], v222 offset:23552\n" \
"v_mfma_f32_16x16x32_bf16 v[84:87], a[12:15], a[220:223], v[84:87]\n" \
"ds_read_b128 a[8:11], v200 offset:0\n" \
"v_mfma_f32_16x16x32_bf16 v[92:95], a[16:19], a[216:219], v[92:95]\n" \
"ds_read_b128 a[12:15], v201 offset:0\n" \
"v_mfma_f32_16x16x32_bf16 v[92:95], a[20:23], a[220:223], v[92:95]\n" \
"buffer_load_dwordx4 v193, s[44:47], s73 offen offset:3072 lds\n" \
"v_mfma_f32_16x16x32_bf16 v[88:91], a[16:19], a[208:211], v[88:91]\n" \
"ds_read_b64_tr_b16 a[52:53], v222 offset:32768\n" \
"v_mfma_f32_16x16x32_bf16 v[88:91], a[20:23], a[212:215], v[88:91]\n" \
"s_add_u32 m0, s89, 65536\n" \
"ds_read_b128 a[16:19], v200 offset:4096\n" \
"v_mfma_f32_16x16x32_bf16 v[96:99], a[24:27], a[208:211], v[96:99]\n" \
"s_xor_b32 s73, s73, 0x80\n" \
"ds_read_b128 a[20:23], v201 offset:4096\n" \
"v_mfma_f32_16x16x32_bf16 v[96:99], a[28:31], a[212:215], v[96:99]\n" \
"buffer_load_dwordx4 v196, s[44:47], s75 offen lds\n" \
"v_mfma_f32_16x16x32_bf16 v[100:103], a[24:27], a[216:219], v[100:103]\n" \
"s_xor_b32 s75, s75, 0x80\n" \
"s_mul_i32 s76, s80, 393216\n" \
"ds_read_b64_tr_b16 a[54:55], v222 offset:35840\n" \
"v_mfma_f32_16x16x32_bf16 v[100:103], a[28:31], a[220:223], v[100:103]\n" \
"s_add_u32 s76, s76, s78\n" \
"ds_read_b128 a[24:27], v200 offset:8192\n" \
"v_mfma_f32_16x16x32_bf16 v[108:111], a[192:195], a[216:219], v[108:111]\n" \
"s_add_u32 m0, s91, 86016\n" \
"ds_read_b128 a[28:31], v201 offset:8192\n" \
"v_mfma_f32_16x16x32_bf16 v[108:111], a[196:199], a[220:223], v[108:111]\n" \
"buffer_load_dwordx4 v197, s[48:51], s76 offen offset:0 lds\n" \
"v_mfma_f32_16x16x32_bf16 v[104:107], a[192:195], a[208:211], v[104:107]\n" \
"v_mfma_f32_16x16x32_bf16 v[104:107], a[196:199], a[212:215], v[104:107]\n" \
"s_waitcnt lgkmcnt(2)\n" \
"v_mfma_f32_16x16x32_bf16 v[32:35], a[32:35], a[0:3], v[32:35]\n" \
"s_add_u32 s77, s76, 24576\n" \
"ds_read_b128 a[192:195], v200 offset:12288\n" \
"v_mfma_f32_16x16x32_bf16 v[32:35], a[36:39], a[4:7], v[32:35]\n" \
"buffer_load_dwordx4 v198, s[48:51], s76 offen offset:1024 lds\n" \
"v_mfma_f32_16x16x32_bf16 v[40:43], a[32:35], a[8:11], v[40:43]\n" \
"ds_read_b128 a[196:199], v201 offset:12288\n" \
"v_mfma_f32_16x16x32_bf16 v[40:43], a[36:39], a[12:15], v[40:43]\n" \
"ds_read_b64_tr_b16 a[200:201], v222 offset:21504\n" \
"v_mfma_f32_16x16x32_bf16 v[0:3], a[40:43], a[8:11], v[0:3]\n" \
"s_xor_b32 s80, s80, 0x1\n" \
"ds_read_b64_tr_b16 a[202:203], v222 offset:24576\n" \
"v_mfma_f32_16x16x32_bf16 v[0:3], a[44:47], a[12:15], v[0:3]\n" \
"buffer_load_dwordx4 v199, s[48:51], s76 offen offset:2048 lds\n" \
"v_mfma_f32_16x16x32_bf16 v[4:7], a[48:51], a[8:11], v[4:7]\n" \
"ds_read_b64_tr_b16 a[204:205], v222 offset:33792\n" \
"v_mfma_f32_16x16x32_bf16 v[4:7], a[52:55], a[12:15], v[4:7]\n" \
"ds_read_b64_tr_b16 a[206:207], v222 offset:36864\n" \
"v_mfma_f32_16x16x32_bf16 v[12:15], a[48:51], a[16:19], v[12:15]\n" \
"s_add_u32 m0, m0, 3072\n" \
"ds_read_b64_tr_b16 a[208:209], v204 offset:22528\n" \
"v_mfma_f32_16x16x32_bf16 v[12:15], a[52:55], a[20:23], v[12:15]\n" \
"buffer_load_dwordx4 v197, s[48:51], s77 offen offset:0 lds\n" \
"v_mfma_f32_16x16x32_bf16 v[8:11], a[40:43], a[16:19], v[8:11]\n" \
"ds_read_b64_tr_b16 a[210:211], v204 offset:25600\n" \
"v_mfma_f32_16x16x32_bf16 v[8:11], a[44:47], a[20:23], v[8:11]\n" \
"ds_read_b64_tr_b16 a[212:213], v204 offset:34816\n" \
"v_mfma_f32_16x16x32_bf16 v[48:51], a[32:35], a[16:19], v[48:51]\n" \
"s_waitcnt lgkmcnt(3)\n" \
"ds_read_b64_tr_b16 a[214:215], v204 offset:37888\n" \
"v_mfma_f32_16x16x32_bf16 v[48:51], a[36:39], a[20:23], v[48:51]\n" \
"buffer_load_dwordx4 v198, s[48:51], s77 offen offset:1024 lds\n" \
"v_mfma_f32_16x16x32_bf16 v[56:59], a[32:35], a[24:27], v[56:59]\n" \
"ds_read_b64_tr_b16 a[216:217], v222 offset:22528\n" \
"v_mfma_f32_16x16x32_bf16 v[56:59], a[36:39], a[28:31], v[56:59]\n" \
"ds_read_b64_tr_b16 a[218:219], v222 offset:25600\n" \
"v_mfma_f32_16x16x32_bf16 v[16:19], a[40:43], a[24:27], v[16:19]\n" \
"ds_read_b64_tr_b16 a[220:221], v222 offset:34816\n" \
"v_mfma_f32_16x16x32_bf16 v[16:19], a[44:47], a[28:31], v[16:19]\n" \
"buffer_load_dwordx4 v199, s[48:51], s77 offen offset:2048 lds\n" \
"v_mfma_f32_16x16x32_bf16 v[20:23], a[48:51], a[24:27], v[20:23]\n" \
"ds_read_b64_tr_b16 a[222:223], v222 offset:37888\n" \
"v_mfma_f32_16x16x32_bf16 v[20:23], a[52:55], a[28:31], v[20:23]\n" \
"v_mfma_f32_16x16x32_bf16 v[28:31], a[48:51], a[192:195], v[28:31]\n" \
"v_mfma_f32_16x16x32_bf16 v[28:31], a[52:55], a[196:199], v[28:31]\n" \
"v_mfma_f32_16x16x32_bf16 v[24:27], a[40:43], a[192:195], v[24:27]\n" \
"v_mfma_f32_16x16x32_bf16 v[24:27], a[44:47], a[196:199], v[24:27]\n" \
"v_mfma_f32_16x16x32_bf16 v[64:67], a[32:35], a[192:195], v[64:67]\n" \
"v_mfma_f32_16x16x32_bf16 v[64:67], a[36:39], a[196:199], v[64:67]\n" \
"s_waitcnt vmcnt(12) lgkmcnt(0)\n" \
"s_barrier\n" \
"s_movk_i32 s64, 0x207\n" \
"v_cmp_class_f32 vcc, v224, s64\n" \
"s_nop 1\n" \
"s_cmp_lg_u64 vcc, 0\n" \
"s_addc_u32 s90, s90, 0\n" \
"s_cmp_lg_u32 s90, 0\n" \
"s_cbranch_scc1 Lexact_%=\n" \
"Lexr_%=:\n" \
"v_mfma_f32_16x16x32_bf16 v[68:71], a[200:203], a[192:195], v[68:71]\n" \
"s_lshl_b32 s15, s63, 7\n" \
"ds_read_b64_tr_b16 a[32:33], v194 offset:1024\n" \
"ds_read_b64_tr_b16 a[34:35], v194 offset:4096\n" \
"v_mfma_f32_16x16x32_bf16 v[68:71], a[204:207], a[196:199], v[68:71]\n" \
"s_lshl2_add_u32 s16, s62, 65589248\n" \
"ds_read_b64_tr_b16 a[36:37], v194 offset:13312\n" \
"ds_read_b64_tr_b16 a[38:39], v194 offset:16384\n" \
"v_mfma_f32_16x16x32_bf16 v[60:63], a[200:203], a[24:27], v[60:63]\n" \
"s_add_u32 m0, s79, 0\n" \
"s_sub_u32 s17, s16, s15\n" \
"ds_read_b64_tr_b16 a[40:41], v194 offset:0\n" \
"v_mfma_f32_16x16x32_bf16 v[60:63], a[204:207], a[28:31], v[60:63]\n" \
"buffer_load_dwordx4 v192, s[44:47], s73 offen offset:0 lds\n" \
"v_mfma_f32_16x16x32_bf16 v[52:55], a[200:203], a[16:19], v[52:55]\n" \
"s_add_u32 s15, s15, s16\n" \
"s_add_u32 s17, s17, 1920\n" \
"ds_read_b64_tr_b16 a[42:43], v194 offset:3072\n" \
"v_mfma_f32_16x16x32_bf16 v[52:55], a[204:207], a[20:23], v[52:55]\n" \
"ds_read_b64_tr_b16 a[44:45], v194 offset:12288\n" \
"v_mov_b32 v146, s60\n" \
"v_mfma_f32_16x16x32_bf16 v[44:47], a[200:203], a[8:11], v[44:47]\n" \
"ds_read_b64_tr_b16 a[46:47], v194 offset:15360\n" \
"v_lshlrev_b32 v147, 28, s61\n" \
"v_mfma_f32_16x16x32_bf16 v[44:47], a[204:207], a[12:15], v[44:47]\n" \
"buffer_load_dwordx4 v193, s[44:47], s73 offen offset:1024 lds\n" \
"v_mfma_f32_16x16x32_bf16 v[72:75], a[0:3], a[208:211], v[72:75]\n" \
"ds_read_b64_tr_b16 a[48:49], v195 offset:0\n" \
"v_mfma_f32_16x16x32_bf16 v[72:75], a[4:7], a[212:215], v[72:75]\n" \
"ds_read_b128 a[0:3], v207 offset:45056\n" \
"v_mfma_f32_16x16x32_bf16 v[80:83], a[8:11], a[208:211], v[80:83]\n" \
"ds_read_b128 a[4:7], v208 offset:45056\n" \
"v_mfma_f32_16x16x32_bf16 v[80:83], a[12:15], a[212:215], v[80:83]\n" \
"buffer_load_dwordx4 v192, s[44:47], s73 offen offset:2048 lds\n" \
"v_mfma_f32_16x16x32_bf16 v[84:87], a[8:11], a[216:219], v[84:87]\n" \
"ds_read_b64_tr_b16 a[50:51], v195 offset:3072\n" \
"v_mfma_f32_16x16x32_bf16 v[84:87], a[12:15], a[220:223], v[84:87]\n" \
"ds_read_b128 a[8:11], v205 offset:45056\n" \
"v_mfma_f32_16x16x32_bf16 v[92:95], a[16:19], a[216:219], v[92:95]\n" \
"ds_read_b128 a[12:15], v206 offset:45056\n" \
"v_mfma_f32_16x16x32_bf16 v[92:95], a[20:23], a[220:223], v[92:95]\n" \
"buffer_load_dwordx4 v193, s[44:47], s73 offen offset:3072 lds\n" \
"v_mfma_f32_16x16x32_bf16 v[88:91], a[16:19], a[208:211], v[88:91]\n" \
"ds_read_b64_tr_b16 a[52:53], v195 offset:12288\n" \
"v_mfma_f32_16x16x32_bf16 v[88:91], a[20:23], a[212:215], v[88:91]\n" \
"s_add_u32 m0, s89, 0\n" \
"ds_read_b128 a[16:19], v205 offset:49152\n" \
"v_mfma_f32_16x16x32_bf16 v[96:99], a[24:27], a[208:211], v[96:99]\n" \
"s_xor_b32 s73, s73, 0x780\n" \
"ds_read_b128 a[20:23], v206 offset:49152\n" \
"v_mfma_f32_16x16x32_bf16 v[96:99], a[28:31], a[212:215], v[96:99]\n" \
"buffer_load_dwordx4 v196, s[44:47], s75 offen lds\n" \
"v_mfma_f32_16x16x32_bf16 v[100:103], a[24:27], a[216:219], v[100:103]\n" \
"s_xor_b32 s75, s75, 0x780\n" \
"s_mul_i32 s76, s80, 393216\n" \
"ds_read_b64_tr_b16 a[54:55], v195 offset:15360\n" \
"v_mfma_f32_16x16x32_bf16 v[100:103], a[28:31], a[220:223], v[100:103]\n" \
"s_add_u32 s76, s76, s78\n" \
"ds_read_b128 a[24:27], v205 offset:53248\n" \
"v_mfma_f32_16x16x32_bf16 v[108:111], a[192:195], a[216:219], v[108:111]\n" \
"s_add_u32 m0, s91, 20480\n" \
"ds_read_b128 a[28:31], v206 offset:53248\n" \
"v_mfma_f32_16x16x32_bf16 v[108:111], a[196:199], a[220:223], v[108:111]\n" \
"buffer_load_dwordx4 v197, s[48:51], s76 offen offset:0 lds\n" \
"v_mfma_f32_16x16x32_bf16 v[104:107], a[192:195], a[208:211], v[104:107]\n" \
"v_mfma_f32_16x16x32_bf16 v[104:107], a[196:199], a[212:215], v[104:107]\n" \
"s_waitcnt lgkmcnt(2)\n" \
"v_mfma_f32_16x16x32_bf16 v[32:35], a[32:35], a[0:3], v[32:35]\n" \
"s_add_u32 s77, s76, 24576\n" \
"ds_read_b128 a[192:195], v205 offset:57344\n" \
"v_mfma_f32_16x16x32_bf16 v[32:35], a[36:39], a[4:7], v[32:35]\n" \
"buffer_load_dwordx4 v198, s[48:51], s76 offen offset:1024 lds\n" \
"v_mfma_f32_16x16x32_bf16 v[40:43], a[32:35], a[8:11], v[40:43]\n" \
"ds_read_b128 a[196:199], v206 offset:57344\n" \
"v_mfma_f32_16x16x32_bf16 v[40:43], a[36:39], a[12:15], v[40:43]\n" \
"ds_read_b64_tr_b16 a[200:201], v195 offset:1024\n" \
"v_mfma_f32_16x16x32_bf16 v[0:3], a[40:43], a[8:11], v[0:3]\n" \
"s_xor_b32 s80, s80, 0xf\n" \
"ds_read_b64_tr_b16 a[202:203], v195 offset:4096\n" \
"v_mfma_f32_16x16x32_bf16 v[0:3], a[44:47], a[12:15], v[0:3]\n" \
"buffer_load_dwordx4 v199, s[48:51], s76 offen offset:2048 lds\n" \
"v_mfma_f32_16x16x32_bf16 v[4:7], a[48:51], a[8:11], v[4:7]\n" \
"ds_read_b64_tr_b16 a[204:205], v195 offset:13312\n" \
"v_mfma_f32_16x16x32_bf16 v[4:7], a[52:55], a[12:15], v[4:7]\n" \
"ds_read_b64_tr_b16 a[206:207], v195 offset:16384\n" \
"v_mfma_f32_16x16x32_bf16 v[12:15], a[48:51], a[16:19], v[12:15]\n" \
"s_add_u32 m0, m0, 3072\n" \
"ds_read_b64_tr_b16 a[208:209], v194 offset:2048\n" \
"v_mfma_f32_16x16x32_bf16 v[12:15], a[52:55], a[20:23], v[12:15]\n" \
"buffer_load_dwordx4 v197, s[48:51], s77 offen offset:0 lds\n" \
"v_mfma_f32_16x16x32_bf16 v[8:11], a[40:43], a[16:19], v[8:11]\n" \
"ds_read_b64_tr_b16 a[210:211], v194 offset:5120\n" \
"v_mfma_f32_16x16x32_bf16 v[8:11], a[44:47], a[20:23], v[8:11]\n" \
"ds_read_b64_tr_b16 a[212:213], v194 offset:14336\n" \
"v_mfma_f32_16x16x32_bf16 v[48:51], a[32:35], a[16:19], v[48:51]\n" \
"s_waitcnt lgkmcnt(3)\n" \
"ds_read_b64_tr_b16 a[214:215], v194 offset:17408\n" \
"v_mfma_f32_16x16x32_bf16 v[48:51], a[36:39], a[20:23], v[48:51]\n" \
"buffer_load_dwordx4 v198, s[48:51], s77 offen offset:1024 lds\n" \
"v_mfma_f32_16x16x32_bf16 v[56:59], a[32:35], a[24:27], v[56:59]\n" \
"ds_read_b64_tr_b16 a[216:217], v195 offset:2048\n" \
"v_mfma_f32_16x16x32_bf16 v[56:59], a[36:39], a[28:31], v[56:59]\n" \
"ds_read_b64_tr_b16 a[218:219], v195 offset:5120\n" \
"v_mfma_f32_16x16x32_bf16 v[16:19], a[40:43], a[24:27], v[16:19]\n" \
"ds_read_b64_tr_b16 a[220:221], v195 offset:14336\n" \
"v_mfma_f32_16x16x32_bf16 v[16:19], a[44:47], a[28:31], v[16:19]\n" \
"buffer_load_dwordx4 v199, s[48:51], s77 offen offset:2048 lds\n" \
"v_mfma_f32_16x16x32_bf16 v[20:23], a[48:51], a[24:27], v[20:23]\n" \
"ds_read_b64_tr_b16 a[222:223], v195 offset:17408\n" \
"v_mfma_f32_16x16x32_bf16 v[20:23], a[52:55], a[28:31], v[20:23]\n" \
"v_mfma_f32_16x16x32_bf16 v[28:31], a[48:51], a[192:195], v[28:31]\n" \
"v_add_u32 v254, 3145728, v254\n" \
"v_mfma_f32_16x16x32_bf16 v[28:31], a[52:55], a[196:199], v[28:31]\n" \
"global_load_dword v253, v254, s[22:23]\n" \
"v_mfma_f32_16x16x32_bf16 v[24:27], a[40:43], a[192:195], v[24:27]\n" \
"v_mfma_f32_16x16x32_bf16 v[24:27], a[44:47], a[196:199], v[24:27]\n" \
"v_mfma_f32_16x16x32_bf16 v[64:67], a[32:35], a[192:195], v[64:67]\n" \
"v_mfma_f32_16x16x32_bf16 v[64:67], a[36:39], a[196:199], v[64:67]\n" \
"s_waitcnt vmcnt(12) lgkmcnt(0)\n" \
"s_barrier\n" \
"v_mfma_f32_16x16x32_bf16 v[68:71], a[200:203], a[192:195], v[68:71]\n" \
"ds_read_b64_tr_b16 a[32:33], v209 offset:1024\n" \
"ds_read_b64_tr_b16 a[34:35], v209 offset:4096\n" \
"v_mfma_f32_16x16x32_bf16 v[68:71], a[204:207], a[196:199], v[68:71]\n" \
"ds_read_b64_tr_b16 a[36:37], v209 offset:13312\n" \
"ds_read_b64_tr_b16 a[38:39], v209 offset:16384\n" \
"v_mfma_f32_16x16x32_bf16 v[60:63], a[200:203], a[24:27], v[60:63]\n" \
"s_add_u32 m0, s79, 110592\n" \
"ds_read_b64_tr_b16 a[40:41], v209 offset:0\n" \
"buffer_store_dword v146, v147, s[56:59], s15 offen sc1\n" \
"v_mfma_f32_16x16x32_bf16 v[60:63], a[204:207], a[28:31], v[60:63]\n" \
"buffer_load_dwordx4 v192, s[44:47], s73 offen offset:0 lds\n" \
"v_mfma_f32_16x16x32_bf16 v[52:55], a[200:203], a[16:19], v[52:55]\n" \
"ds_read_b64_tr_b16 a[42:43], v209 offset:3072\n" \
"v_mfma_f32_16x16x32_bf16 v[52:55], a[204:207], a[20:23], v[52:55]\n" \
"ds_read_b64_tr_b16 a[44:45], v209 offset:12288\n" \
"buffer_store_dword v146, v147, s[56:59], s17 offen sc1\n" \
"v_mfma_f32_16x16x32_bf16 v[44:47], a[200:203], a[8:11], v[44:47]\n" \
"s_mov_b64 s[36:37], s[18:19]\n" \
"ds_read_b64_tr_b16 a[46:47], v209 offset:15360\n" \
"v_mfma_f32_16x16x32_bf16 v[44:47], a[204:207], a[12:15], v[44:47]\n" \
"buffer_load_dwordx4 v193, s[44:47], s73 offen offset:1024 lds\n" \
"v_mfma_f32_16x16x32_bf16 v[72:75], a[0:3], a[208:211], v[72:75]\n" \
"s_sub_u32 s36, s36, 4096\n" \
"s_subb_u32 s37, s37, 0\n" \
"ds_read_b64_tr_b16 a[48:49], v210 offset:0\n" \
"v_mfma_f32_16x16x32_bf16 v[72:75], a[4:7], a[212:215], v[72:75]\n" \
"s_and_b32 s37, s37, 0xffff\n" \
"s_mov_b32 s38, 8392704\n" \
"ds_read_b128 a[0:3], v207 offset:0\n" \
"v_mfma_f32_16x16x32_bf16 v[80:83], a[8:11], a[208:211], v[80:83]\n" \
"s_mov_b32 s39, 0x20000\n" \
"s_mov_b64 s[40:41], s[24:25]\n" \
"ds_read_b128 a[4:7], v208 offset:0\n" \
"v_mfma_f32_16x16x32_bf16 v[80:83], a[12:15], a[212:215], v[80:83]\n" \
"buffer_load_dwordx4 v192, s[44:47], s73 offen offset:2048 lds\n" \
"v_mfma_f32_16x16x32_bf16 v[84:87], a[8:11], a[216:219], v[84:87]\n" \
"s_sub_u32 s40, s40, 4096\n" \
"s_subb_u32 s41, s41, 0\n" \
"ds_read_b64_tr_b16 a[50:51], v210 offset:3072\n" \
"v_mfma_f32_16x16x32_bf16 v[84:87], a[12:15], a[220:223], v[84:87]\n" \
"s_and_b32 s41, s41, 0xffff\n" \
"s_mov_b32 s42, 2101248\n" \
"ds_read_b128 a[8:11], v205 offset:0\n" \
"v_mfma_f32_16x16x32_bf16 v[92:95], a[16:19], a[216:219], v[92:95]\n" \
"s_mov_b32 s43, 0x20000\n" \
"s_mov_b64 s[52:53], s[26:27]\n" \
"ds_read_b128 a[12:15], v206 offset:0\n" \
"v_mfma_f32_16x16x32_bf16 v[92:95], a[20:23], a[220:223], v[92:95]\n" \
"buffer_load_dwordx4 v193, s[44:47], s73 offen offset:3072 lds\n" \
"v_mfma_f32_16x16x32_bf16 v[88:91], a[16:19], a[208:211], v[88:91]\n" \
"s_and_b32 s53, s53, 0xffff\n" \
"s_mov_b32 s54, 8388608\n" \
"ds_read_b64_tr_b16 a[52:53], v210 offset:12288\n" \
"v_mfma_f32_16x16x32_bf16 v[88:91], a[20:23], a[212:215], v[88:91]\n" \
"s_add_u32 m0, s89, 110592\n" \
"s_mov_b32 s55, 0x20000\n" \
"ds_read_b128 a[16:19], v205 offset:4096\n" \
"v_mfma_f32_16x16x32_bf16 v[96:99], a[24:27], a[208:211], v[96:99]\n" \
"s_xor_b32 s73, s73, 0x80\n" \
"ds_read_b128 a[20:23], v206 offset:4096\n" \
"v_lshrrev_b32 v221, 2, v234\n" \
"v_mfma_f32_16x16x32_bf16 v[96:99], a[28:31], a[212:215], v[96:99]\n" \
"buffer_load_dwordx4 v196, s[44:47], s75 offen lds\n" \
"v_mfma_f32_16x16x32_bf16 v[100:103], a[24:27], a[216:219], v[100:103]\n" \
"s_xor_b32 s75, s75, 0x80\n" \
"s_mul_i32 s76, s80, 393216\n" \
"ds_read_b64_tr_b16 a[54:55], v210 offset:15360\n" \
"v_mfma_f32_16x16x32_bf16 v[100:103], a[28:31], a[220:223], v[100:103]\n" \
"s_add_u32 s76, s76, s78\n" \
"ds_read_b128 a[24:27], v205 offset:8192\n" \
"v_lshrrev_b32 v244, 4, v234\n" \
"v_mfma_f32_16x16x32_bf16 v[108:111], a[192:195], a[216:219], v[108:111]\n" \
"s_add_u32 m0, s91, 139264\n" \
"s_lshl_b32 s12, s94, 7\n" \
"ds_read_b128 a[28:31], v206 offset:8192\n" \
"v_mfma_f32_16x16x32_bf16 v[108:111], a[196:199], a[220:223], v[108:111]\n" \
"buffer_load_dwordx4 v197, s[48:51], s76 offen offset:0 lds\n" \
"v_mfma_f32_16x16x32_bf16 v[104:107], a[192:195], a[208:211], v[104:107]\n" \
"s_add_u32 s12, s12, 65591296\n" \
"s_and_b32 s70, %1, 1\n" \
"s_lshl3_add_u32 s70, s70, s63\n" \
"v_mfma_f32_16x16x32_bf16 v[104:107], a[196:199], a[212:215], v[104:107]\n" \
"s_waitcnt lgkmcnt(2)\n" \
"s_lshl2_add_u32 s12, s70, s12\n" \
"s_lshr_b32 s68, s63, 1\n" \
"s_add_u32 s68, s68, 2\n" \
"v_mfma_f32_16x16x32_bf16 v[32:35], a[32:35], a[0:3], v[32:35]\n" \
"s_add_u32 s77, s76, 24576\n" \
"s_mov_b32 s13, 65593856\n" \
"ds_read_b128 a[192:195], v205 offset:12288\n" \
"v_mfma_f32_16x16x32_bf16 v[32:35], a[36:39], a[4:7], v[32:35]\n" \
"buffer_load_dwordx4 v198, s[48:51], s76 offen offset:1024 lds\n" \
"v_mfma_f32_16x16x32_bf16 v[40:43], a[32:35], a[8:11], v[40:43]\n" \
"s_lshr_b32 s70, s18, 8\n" \
"ds_read_b128 a[196:199], v206 offset:12288\n" \
"v_lshrrev_b32 v228, 1, v234\n" \
"v_mfma_f32_16x16x32_bf16 v[40:43], a[36:39], a[12:15], v[40:43]\n" \
"s_sub_u32 s68, s68, s70\n" \
"s_and_b32 s68, s68, 3\n" \
"ds_read_b64_tr_b16 a[200:201], v210 offset:1024\n" \
"v_mfma_f32_16x16x32_bf16 v[0:3], a[40:43], a[8:11], v[0:3]\n" \
"s_xor_b32 s80, s80, 0x1\n" \
"s_and_b32 s70, s63, 1\n" \
"ds_read_b64_tr_b16 a[202:203], v210 offset:4096\n" \
"v_mfma_f32_16x16x32_bf16 v[0:3], a[44:47], a[12:15], v[0:3]\n" \
"buffer_load_dwordx4 v199, s[48:51], s76 offen offset:2048 lds\n" \
"v_mfma_f32_16x16x32_bf16 v[4:7], a[48:51], a[8:11], v[4:7]\n" \
"s_lshl_b32 s70, s70, 2\n" \
"s_or_b32 s68, s68, s70\n" \
"ds_read_b64_tr_b16 a[204:205], v210 offset:13312\n" \
"v_mfma_f32_16x16x32_bf16 v[4:7], a[52:55], a[12:15], v[4:7]\n" \
"s_lshr_b32 s84, s61, 1\n" \
"s_lshl_b32 s84, s84, 16\n" \
"ds_read_b64_tr_b16 a[206:207], v210 offset:16384\n" \
"v_mfma_f32_16x16x32_bf16 v[12:15], a[48:51], a[16:19], v[12:15]\n" \
"s_add_u32 m0, m0, 3072\n" \
"s_and_b32 s70, s61, 1\n" \
"ds_read_b64_tr_b16 a[208:209], v209 offset:2048\n" \
"v_mfma_f32_16x16x32_bf16 v[12:15], a[52:55], a[20:23], v[12:15]\n" \
"buffer_load_dwordx4 v197, s[48:51], s77 offen offset:0 lds\n" \
"v_mfma_f32_16x16x32_bf16 v[8:11], a[40:43], a[16:19], v[8:11]\n" \
"s_lshl_b32 s70, s70, 14\n" \
"s_add_u32 s84, s84, s70\n" \
"ds_read_b64_tr_b16 a[210:211], v209 offset:5120\n" \
"v_mfma_f32_16x16x32_bf16 v[8:11], a[44:47], a[20:23], v[8:11]\n" \
"s_lshl_b32 s88, s68, 8\n" \
"s_add_u32 s84, s84, s88\n" \
"ds_read_b64_tr_b16 a[212:213], v209 offset:14336\n" \
"v_mfma_f32_16x16x32_bf16 v[48:51], a[32:35], a[16:19], v[48:51]\n" \
"s_waitcnt lgkmcnt(3)\n" \
"ds_read_b64_tr_b16 a[214:215], v209 offset:17408\n" \
"v_bfe_u32 v245, v244, 2, 3\n" \
"v_mfma_f32_16x16x32_bf16 v[48:51], a[36:39], a[20:23], v[48:51]\n" \
"buffer_load_dwordx4 v198, s[48:51], s77 offen offset:1024 lds\n" \
"v_mfma_f32_16x16x32_bf16 v[56:59], a[32:35], a[24:27], v[56:59]\n" \
"ds_read_b64_tr_b16 a[216:217], v210 offset:2048\n" \
"v_lshlrev_b32 v245, 11, v245\n" \
"v_mfma_f32_16x16x32_bf16 v[56:59], a[36:39], a[28:31], v[56:59]\n" \
"ds_read_b64_tr_b16 a[218:219], v210 offset:5120\n" \
"v_lshrrev_b32 v246, 5, v244\n" \
"v_mfma_f32_16x16x32_bf16 v[16:19], a[40:43], a[24:27], v[16:19]\n" \
"ds_read_b64_tr_b16 a[220:221], v210 offset:14336\n" \
"v_lshl_add_u32 v245, v246, 6, v245\n" \
"v_mfma_f32_16x16x32_bf16 v[16:19], a[44:47], a[28:31], v[16:19]\n" \
"buffer_load_dwordx4 v199, s[48:51], s77 offen offset:2048 lds\n" \
"v_mfma_f32_16x16x32_bf16 v[20:23], a[48:51], a[24:27], v[20:23]\n" \
"ds_read_b64_tr_b16 a[222:223], v210 offset:17408\n" \
"v_and_b32 v246, 3, v244\n" \
"v_mfma_f32_16x16x32_bf16 v[20:23], a[52:55], a[28:31], v[20:23]\n" \
"v_bfe_u32 v247, v244, 4, 1\n" \
"v_lshlrev_b32 v247, 1, v247\n" \
"v_mfma_f32_16x16x32_bf16 v[28:31], a[48:51], a[192:195], v[28:31]\n" \
"v_xor_b32 v246, v246, v247\n" \
"v_lshl_add_u32 v245, v246, 4, v245\n" \
"v_mfma_f32_16x16x32_bf16 v[28:31], a[52:55], a[196:199], v[28:31]\n" \
"buffer_load_dwordx2 v[226:227], v228, s[56:59], s13 offen sc1\n" \
"v_mfma_f32_16x16x32_bf16 v[24:27], a[40:43], a[192:195], v[24:27]\n" \
"v_add_u32 v216, 4096, v245\n" \
"v_add_u32 v217, 3200, v245\n" \
"v_mfma_f32_16x16x32_bf16 v[24:27], a[44:47], a[196:199], v[24:27]\n" \
"v_add_u32 v218, 34816, v245\n" \
"v_add_u32 v219, 33920, v245\n" \
"v_mfma_f32_16x16x32_bf16 v[64:67], a[32:35], a[192:195], v[64:67]\n" \
"s_mov_b32 s86, 65536\n" \
"s_lshr_b32 s70, s61, 1\n" \
"s_lshl_b32 s71, s70, 13\n" \
"Lpg8525_%=:\n" \
"Lst1x1_%=:\n" \
"s_cbranch_execz Lpg10850_%=\n" \
"v_mfma_f32_16x16x32_bf16 v[64:67], a[36:39], a[196:199], v[64:67]\n" \
"s_add_u32 s71, s71, %3\n" \
"s_waitcnt vmcnt(15) lgkmcnt(0)\n" \
"s_barrier\n" \
"v_mfma_f32_16x16x32_bf16 v[68:71], a[200:203], a[192:195], v[68:71]\n" \
"ds_read_b64_tr_b16 a[32:33], v204 offset:21504\n" \
"ds_read_b64_tr_b16 a[34:35], v204 offset:24576\n" \
"v_mfma_f32_16x16x32_bf16 v[68:71], a[204:207], a[196:199], v[68:71]\n" \
"ds_read_b64_tr_b16 a[36:37], v204 offset:33792\n" \
"ds_read_b64_tr_b16 a[38:39], v204 offset:36864\n" \
"v_mfma_f32_16x16x32_bf16 v[60:63], a[200:203], a[24:27], v[60:63]\n" \
"s_add_u32 m0, s79, 65536\n" \
"ds_read_b64_tr_b16 a[40:41], v204 offset:20480\n" \
"v_add_u32 v211, s71, v236\n" \
"v_mfma_f32_16x16x32_bf16 v[60:63], a[204:207], a[28:31], v[60:63]\n" \
"buffer_load_dwordx4 v192, s[44:47], s73 offen offset:0 lds\n" \
"v_mfma_f32_16x16x32_bf16 v[52:55], a[200:203], a[16:19], v[52:55]\n" \
"s_lshl_b32 s70, s70, 18\n" \
"s_add_u32 s88, s88, s70\n" \
"ds_read_b64_tr_b16 a[42:43], v204 offset:23552\n" \
"v_mfma_f32_16x16x32_bf16 v[52:55], a[204:207], a[20:23], v[52:55]\n" \
"s_and_b32 s70, s61, 1\n" \
"s_lshl_b32 s71, s70, 7\n" \
"ds_read_b64_tr_b16 a[44:45], v204 offset:32768\n" \
"v_mfma_f32_16x16x32_bf16 v[44:47], a[200:203], a[8:11], v[44:47]\n" \
"s_add_u32 s88, s88, s71\n" \
"ds_read_b64_tr_b16 a[46:47], v204 offset:35840\n" \
"v_and_b32 v245, 3, v244\n" \
"v_mfma_f32_16x16x32_bf16 v[44:47], a[204:207], a[12:15], v[44:47]\n" \
"buffer_load_dwordx4 v193, s[44:47], s73 offen offset:1024 lds\n" \
"v_mfma_f32_16x16x32_bf16 v[72:75], a[0:3], a[208:211], v[72:75]\n" \
"ds_read_b64_tr_b16 a[48:49], v222 offset:20480\n" \
"v_lshlrev_b32 v245, 3, v245\n" \
"v_mfma_f32_16x16x32_bf16 v[72:75], a[4:7], a[212:215], v[72:75]\n" \
"ds_read_b128 a[0:3], v202 offset:0\n" \
"v_bfe_u32 v246, v244, 2, 2\n" \
"v_mfma_f32_16x16x32_bf16 v[80:83], a[8:11], a[208:211], v[80:83]\n" \
"ds_read_b128 a[4:7], v203 offset:0\n" \
"v_lshl_add_u32 v245, v246, 6, v245\n" \
"v_mfma_f32_16x16x32_bf16 v[80:83], a[12:15], a[212:215], v[80:83]\n" \
"buffer_load_dwordx4 v192, s[44:47], s73 offen offset:2048 lds\n" \
"v_mfma_f32_16x16x32_bf16 v[84:87], a[8:11], a[216:219], v[84:87]\n" \
"ds_read_b64_tr_b16 a[50:51], v222 offset:23552\n" \
"v_bfe_u32 v246, v244, 4, 1\n" \
"v_mfma_f32_16x16x32_bf16 v[84:87], a[12:15], a[220:223], v[84:87]\n" \
"ds_read_b128 a[8:11], v200 offset:0\n" \
"v_lshl_add_u32 v245, v246, 5, v245\n" \
"v_mfma_f32_16x16x32_bf16 v[92:95], a[16:19], a[216:219], v[92:95]\n" \
"ds_read_b128 a[12:15], v201 offset:0\n" \
"v_lshl_add_u32 v245, v246, 8, v245\n" \
"v_mfma_f32_16x16x32_bf16 v[92:95], a[20:23], a[220:223], v[92:95]\n" \
"buffer_load_dwordx4 v193, s[44:47], s73 offen offset:3072 lds\n" \
"v_mfma_f32_16x16x32_bf16 v[88:91], a[16:19], a[208:211], v[88:91]\n" \
"ds_read_b64_tr_b16 a[52:53], v222 offset:32768\n" \
"v_lshrrev_b32 v246, 5, v244\n" \
"v_mfma_f32_16x16x32_bf16 v[88:91], a[20:23], a[212:215], v[88:91]\n" \
"s_add_u32 m0, s89, 65536\n" \
"ds_read_b128 a[16:19], v200 offset:4096\n" \
"v_lshl_add_u32 v245, v246, 12, v245\n" \
"v_mfma_f32_16x16x32_bf16 v[96:99], a[24:27], a[208:211], v[96:99]\n" \
"s_xor_b32 s73, s73, 0x180\n" \
"s_lshl_b32 s71, s70, 10\n" \
"ds_read_b128 a[20:23], v201 offset:4096\n" \
"v_mfma_f32_16x16x32_bf16 v[96:99], a[28:31], a[212:215], v[96:99]\n" \
"buffer_load_dwordx4 v196, s[44:47], s75 offen lds\n" \
"v_mfma_f32_16x16x32_bf16 v[100:103], a[24:27], a[216:219], v[100:103]\n" \
"s_xor_b32 s75, s75, 0x180\n" \
"s_mul_i32 s76, s80, 393216\n" \
"ds_read_b64_tr_b16 a[54:55], v222 offset:35840\n" \
"v_mfma_f32_16x16x32_bf16 v[100:103], a[28:31], a[220:223], v[100:103]\n" \
"s_add_u32 s76, s76, s78\n" \
"s_add_u32 s71, s71, %3\n" \
"ds_read_b128 a[24:27], v200 offset:8192\n" \
"v_mfma_f32_16x16x32_bf16 v[108:111], a[192:195], a[216:219], v[108:111]\n" \
"s_add_u32 m0, s91, 86016\n" \
"ds_read_b128 a[28:31], v201 offset:8192\n" \
"v_add_u32 v212, s71, v245\n" \
"v_mfma_f32_16x16x32_bf16 v[108:111], a[196:199], a[220:223], v[108:111]\n" \
"buffer_load_dwordx4 v197, s[48:51], s76 offen offset:0 lds\n" \
"v_mfma_f32_16x16x32_bf16 v[104:107], a[192:195], a[208:211], v[104:107]\n" \
"s_mov_b32 s69, 1\n" \
"s_mov_b32 s30, 0x0\n" \
"s_mov_b32 s85, 98304\n" \
"v_mfma_f32_16x16x32_bf16 v[104:107], a[196:199], a[212:215], v[104:107]\n" \
"s_waitcnt lgkmcnt(2)\n" \
"s_sub_u32 s85, s85, 32768\n" \
"s_cselect_b32 s85, 131072, s85\n" \
"v_add_u32 v213, s85, v211\n" \
"v_mfma_f32_16x16x32_bf16 v[32:35], a[32:35], a[0:3], v[32:35]\n" \
"s_add_u32 s77, s76, 24576\n" \
"ds_read_b128 a[192:195], v200 offset:12288\n" \
"v_add_u32 v214, s85, v212\n" \
"v_mfma_f32_16x16x32_bf16 v[32:35], a[36:39], a[4:7], v[32:35]\n" \
"buffer_load_dwordx4 v198, s[48:51], s76 offen offset:1024 lds\n" \
"v_mfma_f32_16x16x32_bf16 v[40:43], a[32:35], a[8:11], v[40:43]\n" \
"ds_read_b128 a[196:199], v201 offset:12288\n" \
"v_xor_b32 v215, 32, v214\n" \
"v_mfma_f32_16x16x32_bf16 v[40:43], a[36:39], a[12:15], v[40:43]\n" \
"ds_read_b64_tr_b16 a[200:201], v222 offset:21504\n" \
"v_mov_b32_e64 v230, s60\n" \
"v_mfma_f32_16x16x32_bf16 v[0:3], a[40:43], a[8:11], v[0:3]\n" \
"s_xor_b32 s80, s80, 0x3\n" \
"ds_read_b64_tr_b16 a[202:203], v222 offset:24576\n" \
"v_lshlrev_b32 v231, 28, s61\n" \
"v_mfma_f32_16x16x32_bf16 v[0:3], a[44:47], a[12:15], v[0:3]\n" \
"buffer_load_dwordx4 v199, s[48:51], s76 offen offset:2048 lds\n" \
"v_mfma_f32_16x16x32_bf16 v[4:7], a[48:51], a[8:11], v[4:7]\n" \
"ds_read_b64_tr_b16 a[204:205], v222 offset:33792\n" \
"v_mfma_f32_16x16x32_bf16 v[4:7], a[52:55], a[12:15], v[4:7]\n" \
"ds_read_b64_tr_b16 a[206:207], v222 offset:36864\n" \
"v_mfma_f32_16x16x32_bf16 v[12:15], a[48:51], a[16:19], v[12:15]\n" \
"s_add_u32 m0, m0, 3072\n" \
"ds_read_b64_tr_b16 a[208:209], v204 offset:22528\n" \
"v_mfma_f32_16x16x32_bf16 v[12:15], a[52:55], a[20:23], v[12:15]\n" \
"buffer_load_dwordx4 v197, s[48:51], s77 offen offset:0 lds\n" \
"v_mfma_f32_16x16x32_bf16 v[8:11], a[40:43], a[16:19], v[8:11]\n" \
"ds_read_b64_tr_b16 a[210:211], v204 offset:25600\n" \
"v_mfma_f32_16x16x32_bf16 v[8:11], a[44:47], a[20:23], v[8:11]\n" \
"ds_read_b64_tr_b16 a[212:213], v204 offset:34816\n" \
"v_mfma_f32_16x16x32_bf16 v[48:51], a[32:35], a[16:19], v[48:51]\n" \
"s_waitcnt lgkmcnt(3)\n" \
"ds_read_b64_tr_b16 a[214:215], v204 offset:37888\n" \
"v_mfma_f32_16x16x32_bf16 v[48:51], a[36:39], a[20:23], v[48:51]\n" \
"buffer_load_dwordx4 v198, s[48:51], s77 offen offset:1024 lds\n" \
"v_mfma_f32_16x16x32_bf16 v[56:59], a[32:35], a[24:27], v[56:59]\n" \
"ds_read_b64_tr_b16 a[216:217], v222 offset:22528\n" \
"v_mfma_f32_16x16x32_bf16 v[56:59], a[36:39], a[28:31], v[56:59]\n" \
"ds_read_b64_tr_b16 a[218:219], v222 offset:25600\n" \
"v_mfma_f32_16x16x32_bf16 v[16:19], a[40:43], a[24:27], v[16:19]\n" \
"ds_read_b64_tr_b16 a[220:221], v222 offset:34816\n" \
"v_mfma_f32_16x16x32_bf16 v[16:19], a[44:47], a[28:31], v[16:19]\n" \
"buffer_load_dwordx4 v199, s[48:51], s77 offen offset:2048 lds\n" \
"v_mfma_f32_16x16x32_bf16 v[20:23], a[48:51], a[24:27], v[20:23]\n" \
"ds_read_b64_tr_b16 a[222:223], v222 offset:37888\n" \
"v_mfma_f32_16x16x32_bf16 v[20:23], a[52:55], a[28:31], v[20:23]\n" \
"v_mfma_f32_16x16x32_bf16 v[28:31], a[48:51], a[192:195], v[28:31]\n" \
"v_mfma_f32_16x16x32_bf16 v[28:31], a[52:55], a[196:199], v[28:31]\n" \
"v_mfma_f32_16x16x32_bf16 v[24:27], a[40:43], a[192:195], v[24:27]\n" \
"v_mfma_f32_16x16x32_bf16 v[24:27], a[44:47], a[196:199], v[24:27]\n" \
"v_mfma_f32_16x16x32_bf16 v[64:67], a[32:35], a[192:195], v[64:67]\n" \
"v_mfma_f32_16x16x32_bf16 v[64:67], a[36:39], a[196:199], v[64:67]\n" \
"s_waitcnt vmcnt(12) lgkmcnt(0)\n" \
"s_barrier\n" \
"v_mfma_f32_16x16x32_bf16 v[68:71], a[200:203], a[192:195], v[68:71]\n" \
"ds_read_b64_tr_b16 a[32:33], v194 offset:1024\n" \
"ds_read_b64_tr_b16 a[34:35], v194 offset:4096\n" \
"v_mfma_f32_16x16x32_bf16 v[68:71], a[204:207], a[196:199], v[68:71]\n" \
"ds_read_b64_tr_b16 a[36:37], v194 offset:13312\n" \
"ds_read_b64_tr_b16 a[38:39], v194 offset:16384\n" \
"v_mfma_f32_16x16x32_bf16 v[60:63], a[200:203], a[24:27], v[60:63]\n" \
"s_add_u32 m0, s79, 0\n" \
"ds_read_b64_tr_b16 a[40:41], v194 offset:0\n" \
"v_mfma_f32_16x16x32_bf16 v[60:63], a[204:207], a[28:31], v[60:63]\n" \
"buffer_load_dwordx4 v192, s[44:47], s73 offen offset:0 lds\n" \
"v_mfma_f32_16x16x32_bf16 v[52:55], a[200:203], a[16:19], v[52:55]\n" \
"ds_read_b64_tr_b16 a[42:43], v194 offset:3072\n" \
"v_mfma_f32_16x16x32_bf16 v[52:55], a[204:207], a[20:23], v[52:55]\n" \
"ds_read_b64_tr_b16 a[44:45], v194 offset:12288\n" \
"v_mfma_f32_16x16x32_bf16 v[44:47], a[200:203], a[8:11], v[44:47]\n" \
"ds_read_b64_tr_b16 a[46:47], v194 offset:15360\n" \
"v_mfma_f32_16x16x32_bf16 v[44:47], a[204:207], a[12:15], v[44:47]\n" \
"buffer_load_dwordx4 v193, s[44:47], s73 offen offset:1024 lds\n" \
"v_mfma_f32_16x16x32_bf16 v[72:75], a[0:3], a[208:211], v[72:75]\n" \
"ds_read_b64_tr_b16 a[48:49], v195 offset:0\n" \
"v_mfma_f32_16x16x32_bf16 v[72:75], a[4:7], a[212:215], v[72:75]\n" \
"ds_read_b128 a[0:3], v207 offset:45056\n" \
"v_mfma_f32_16x16x32_bf16 v[80:83], a[8:11], a[208:211], v[80:83]\n" \
"ds_read_b128 a[4:7], v208 offset:45056\n" \
"v_mfma_f32_16x16x32_bf16 v[80:83], a[12:15], a[212:215], v[80:83]\n" \
"buffer_load_dwordx4 v192, s[44:47], s73 offen offset:2048 lds\n" \
"v_mfma_f32_16x16x32_bf16 v[84:87], a[8:11], a[216:219], v[84:87]\n" \
"ds_read_b64_tr_b16 a[50:51], v195 offset:3072\n" \
"v_mfma_f32_16x16x32_bf16 v[84:87], a[12:15], a[220:223], v[84:87]\n" \
"ds_read_b128 a[8:11], v205 offset:45056\n" \
"v_mfma_f32_16x16x32_bf16 v[92:95], a[16:19], a[216:219], v[92:95]\n" \
"ds_read_b128 a[12:15], v206 offset:45056\n" \
"v_mfma_f32_16x16x32_bf16 v[92:95], a[20:23], a[220:223], v[92:95]\n" \
"buffer_load_dwordx4 v193, s[44:47], s73 offen offset:3072 lds\n" \
"v_mfma_f32_16x16x32_bf16 v[88:91], a[16:19], a[208:211], v[88:91]\n" \
"ds_read_b64_tr_b16 a[52:53], v195 offset:12288\n" \
"v_mfma_f32_16x16x32_bf16 v[88:91], a[20:23], a[212:215], v[88:91]\n" \
"s_add_u32 m0, s89, 0\n" \
"ds_read_b128 a[16:19], v205 offset:49152\n" \
"v_mfma_f32_16x16x32_bf16 v[96:99], a[24:27], a[208:211], v[96:99]\n" \
"s_xor_b32 s73, s73, 0x300\n" \
"ds_read_b128 a[20:23], v206 offset:49152\n" \
"v_mfma_f32_16x16x32_bf16 v[96:99], a[28:31], a[212:215], v[96:99]\n" \
"buffer_load_dwordx4 v196, s[44:47], s75 offen lds\n" \
"v_mfma_f32_16x16x32_bf16 v[100:103], a[24:27], a[216:219], v[100:103]\n" \
"s_xor_b32 s75, s75, 0x300\n" \
"s_mul_i32 s76, s80, 393216\n" \
"ds_read_b64_tr_b16 a[54:55], v195 offset:15360\n" \
"v_mfma_f32_16x16x32_bf16 v[100:103], a[28:31], a[220:223], v[100:103]\n" \
"s_add_u32 s76, s76, s78\n" \
"ds_read_b128 a[24:27], v205 offset:53248\n" \
"v_mfma_f32_16x16x32_bf16 v[108:111], a[192:195], a[216:219], v[108:111]\n" \
"s_add_u32 m0, s91, 20480\n" \
"ds_read_b128 a[28:31], v206 offset:53248\n" \
"v_mfma_f32_16x16x32_bf16 v[108:111], a[196:199], a[220:223], v[108:111]\n" \
"buffer_load_dwordx4 v197, s[48:51], s76 offen offset:0 lds\n" \
"v_mfma_f32_16x16x32_bf16 v[104:107], a[192:195], a[208:211], v[104:107]\n" \
"v_mfma_f32_16x16x32_bf16 v[104:107], a[196:199], a[212:215], v[104:107]\n" \
"s_waitcnt lgkmcnt(2)\n" \
"v_mfma_f32_16x16x32_bf16 v[32:35], a[32:35], a[0:3], v[32:35]\n" \
"s_add_u32 s77, s76, 24576\n" \
"ds_read_b128 a[192:195], v205 offset:57344\n" \
"v_mfma_f32_16x16x32_bf16 v[32:35], a[36:39], a[4:7], v[32:35]\n" \
"buffer_load_dwordx4 v198, s[48:51], s76 offen offset:1024 lds\n" \
"v_mfma_f32_16x16x32_bf16 v[40:43], a[32:35], a[8:11], v[40:43]\n" \
"ds_read_b128 a[196:199], v206 offset:57344\n" \
"v_mfma_f32_16x16x32_bf16 v[40:43], a[36:39], a[12:15], v[40:43]\n" \
"ds_read_b64_tr_b16 a[200:201], v195 offset:1024\n" \
"v_mfma_f32_16x16x32_bf16 v[0:3], a[40:43], a[8:11], v[0:3]\n" \
"s_xor_b32 s80, s80, 0x6\n" \
"ds_read_b64_tr_b16 a[202:203], v195 offset:4096\n" \
"v_mfma_f32_16x16x32_bf16 v[0:3], a[44:47], a[12:15], v[0:3]\n" \
"buffer_load_dwordx4 v199, s[48:51], s76 offen offset:2048 lds\n" \
"v_mfma_f32_16x16x32_bf16 v[4:7], a[48:51], a[8:11], v[4:7]\n" \
"ds_read_b64_tr_b16 a[204:205], v195 offset:13312\n" \
"v_mfma_f32_16x16x32_bf16 v[4:7], a[52:55], a[12:15], v[4:7]\n" \
"ds_read_b64_tr_b16 a[206:207], v195 offset:16384\n" \
"v_mfma_f32_16x16x32_bf16 v[12:15], a[48:51], a[16:19], v[12:15]\n" \
"s_add_u32 m0, m0, 3072\n" \
"ds_read_b64_tr_b16 a[208:209], v194 offset:2048\n" \
"v_mfma_f32_16x16x32_bf16 v[12:15], a[52:55], a[20:23], v[12:15]\n" \
"buffer_load_dwordx4 v197, s[48:51], s77 offen offset:0 lds\n" \
"v_mfma_f32_16x16x32_bf16 v[8:11], a[40:43], a[16:19], v[8:11]\n" \
"ds_read_b64_tr_b16 a[210:211], v194 offset:5120\n" \
"v_mfma_f32_16x16x32_bf16 v[8:11], a[44:47], a[20:23], v[8:11]\n" \
"ds_read_b64_tr_b16 a[212:213], v194 offset:14336\n" \
"v_mfma_f32_16x16x32_bf16 v[48:51], a[32:35], a[16:19], v[48:51]\n" \
"s_waitcnt lgkmcnt(3)\n" \
"ds_read_b64_tr_b16 a[214:215], v194 offset:17408\n" \
"v_mfma_f32_16x16x32_bf16 v[48:51], a[36:39], a[20:23], v[48:51]\n" \
"buffer_load_dwordx4 v198, s[48:51], s77 offen offset:1024 lds\n" \
"v_mfma_f32_16x16x32_bf16 v[56:59], a[32:35], a[24:27], v[56:59]\n" \
"ds_read_b64_tr_b16 a[216:217], v195 offset:2048\n" \
"v_mfma_f32_16x16x32_bf16 v[56:59], a[36:39], a[28:31], v[56:59]\n" \
"ds_read_b64_tr_b16 a[218:219], v195 offset:5120\n" \
"v_mfma_f32_16x16x32_bf16 v[16:19], a[40:43], a[24:27], v[16:19]\n" \
"ds_read_b64_tr_b16 a[220:221], v195 offset:14336\n" \
"v_mfma_f32_16x16x32_bf16 v[16:19], a[44:47], a[28:31], v[16:19]\n" \
"buffer_load_dwordx4 v199, s[48:51], s77 offen offset:2048 lds\n" \
"v_mfma_f32_16x16x32_bf16 v[20:23], a[48:51], a[24:27], v[20:23]\n" \
"ds_read_b64_tr_b16 a[222:223], v195 offset:17408\n" \
"v_mfma_f32_16x16x32_bf16 v[20:23], a[52:55], a[28:31], v[20:23]\n" \
"v_mfma_f32_16x16x32_bf16 v[28:31], a[48:51], a[192:195], v[28:31]\n" \
"v_mfma_f32_16x16x32_bf16 v[28:31], a[52:55], a[196:199], v[28:31]\n" \
"v_mfma_f32_16x16x32_bf16 v[24:27], a[40:43], a[192:195], v[24:27]\n" \
"v_mfma_f32_16x16x32_bf16 v[24:27], a[44:47], a[196:199], v[24:27]\n" \
"v_mfma_f32_16x16x32_bf16 v[64:67], a[32:35], a[192:195], v[64:67]\n" \
"v_mfma_f32_16x16x32_bf16 v[64:67], a[36:39], a[196:199], v[64:67]\n" \
"s_waitcnt vmcnt(11) lgkmcnt(0)\n" \
"s_barrier\n" \
"v_mfma_f32_16x16x32_bf16 v[68:71], a[200:203], a[192:195], v[68:71]\n" \
"ds_read_b64_tr_b16 a[32:33], v209 offset:1024\n" \
"ds_read_b64_tr_b16 a[34:35], v209 offset:4096\n" \
"v_mfma_f32_16x16x32_bf16 v[68:71], a[204:207], a[196:199], v[68:71]\n" \
"ds_read_b64_tr_b16 a[36:37], v209 offset:13312\n" \
"ds_read_b64_tr_b16 a[38:39], v209 offset:16384\n" \
"v_mfma_f32_16x16x32_bf16 v[60:63], a[200:203], a[24:27], v[60:63]\n" \
"s_add_u32 m0, s79, 110592\n" \
"ds_read_b64_tr_b16 a[40:41], v209 offset:0\n" \
"v_cmp_eq_u32 vcc, s60, v226\n" \
"v_mfma_f32_16x16x32_bf16 v[60:63], a[204:207], a[28:31], v[60:63]\n" \
"buffer_load_dwordx4 v192, s[44:47], s73 offen offset:0 lds\n" \
"v_mfma_f32_16x16x32_bf16 v[52:55], a[200:203], a[16:19], v[52:55]\n" \
"s_andn2_b32 s70, 0xff, vcc_lo\n" \
"s_cbranch_scc1 Lpl4_%=\n" \
"Lpr4_%=:\n" \
"ds_read_b64_tr_b16 a[42:43], v209 offset:3072\n" \
"v_mfma_f32_16x16x32_bf16 v[52:55], a[204:207], a[20:23], v[52:55]\n" \
"s_and_b32 s70, %1, 7\n" \
"ds_read_b64_tr_b16 a[44:45], v209 offset:12288\n" \
"v_readlane_b32 s70, v227, s70\n" \
"v_mfma_f32_16x16x32_bf16 v[44:47], a[200:203], a[8:11], v[44:47]\n" \
"ds_read_b64_tr_b16 a[46:47], v209 offset:15360\n" \
"v_mfma_f32_16x16x32_bf16 v[44:47], a[204:207], a[12:15], v[44:47]\n" \
"buffer_load_dwordx4 v193, s[44:47], s73 offen offset:1024 lds\n" \
"v_mfma_f32_16x16x32_bf16 v[72:75], a[0:3], a[208:211], v[72:75]\n" \
"ds_read_b64_tr_b16 a[48:49], v210 offset:0\n" \
"v_subrev_u32 v227, s70, v227\n" \
"v_mfma_f32_16x16x32_bf16 v[72:75], a[4:7], a[212:215], v[72:75]\n" \
"ds_read_b128 a[0:3], v207 offset:0\n" \
"v_cmp_gt_i32 vcc, 0, v227\n" \
"v_mfma_f32_16x16x32_bf16 v[80:83], a[8:11], a[208:211], v[80:83]\n" \
"ds_read_b128 a[4:7], v208 offset:0\n" \
"v_mfma_f32_16x16x32_bf16 v[80:83], a[12:15], a[212:215], v[80:83]\n" \
"buffer_load_dwordx4 v192, s[44:47], s73 offen offset:2048 lds\n" \
"v_mfma_f32_16x16x32_bf16 v[84:87], a[8:11], a[216:219], v[84:87]\n" \
"s_and_b32 s13, vcc_lo, 0xff\n" \
"s_bcnt1_i32_b32 s13, s13\n" \
"ds_read_b64_tr_b16 a[50:51], v210 offset:3072\n" \
"v_mfma_f32_16x16x32_bf16 v[84:87], a[12:15], a[220:223], v[84:87]\n" \
"s_lshr_b32 s90, s13, 2\n" \
"s_mul_i32 s70, s90, 7\n" \
"ds_read_b128 a[8:11], v205 offset:0\n" \
"v_mfma_f32_16x16x32_bf16 v[92:95], a[16:19], a[216:219], v[92:95]\n" \
"s_xor_b32 s13, s13, s70\n" \
"s_lshl_b32 s70, s13, 3\n" \
"ds_read_b128 a[12:15], v206 offset:0\n" \
"v_mfma_f32_16x16x32_bf16 v[92:95], a[20:23], a[220:223], v[92:95]\n" \
"buffer_load_dwordx4 v193, s[44:47], s73 offen offset:3072 lds\n" \
"v_mfma_f32_16x16x32_bf16 v[88:91], a[16:19], a[208:211], v[88:91]\n" \
"s_lshr_b32 s9, 0x2a2b2d2f, s70\n" \
"s_and_b32 s9, s9, 0xff\n" \
"ds_read_b64_tr_b16 a[52:53], v210 offset:12288\n" \
"v_mfma_f32_16x16x32_bf16 v[88:91], a[20:23], a[212:215], v[88:91]\n" \
"s_add_u32 m0, s89, 110592\n" \
"s_lshr_b32 s8, %1, 6\n" \
"ds_read_b128 a[16:19], v205 offset:4096\n" \
"v_mfma_f32_16x16x32_bf16 v[96:99], a[24:27], a[208:211], v[96:99]\n" \
"s_xor_b32 s73, s73, 0x80\n" \
"s_lshl2_add_u32 s8, s13, s8\n" \
"ds_read_b128 a[20:23], v206 offset:4096\n" \
"v_mfma_f32_16x16x32_bf16 v[96:99], a[28:31], a[212:215], v[96:99]\n" \
"buffer_load_dwordx4 v196, s[44:47], s75 offen lds\n" \
"v_mfma_f32_16x16x32_bf16 v[100:103], a[24:27], a[216:219], v[100:103]\n" \
"s_xor_b32 s75, s75, 0x80\n" \
"s_mul_i32 s76, s80, 393216\n" \
"ds_read_b64_tr_b16 a[54:55], v210 offset:15360\n" \
"v_mfma_f32_16x16x32_bf16 v[100:103], a[28:31], a[220:223], v[100:103]\n" \
"s_add_u32 s76, s76, s78\n" \
"ds_read_b128 a[24:27], v205 offset:8192\n" \
"v_max_i32_dpp v226, v227, v227 quad_perm:[1,0,3,2] row_mask:0xf bank_mask:0xf\n" \
"v_mfma_f32_16x16x32_bf16 v[108:111], a[192:195], a[216:219], v[108:111]\n" \
"s_add_u32 m0, s91, 139264\n" \
"s_mov_b32 s14, 0\n" \
"ds_read_b128 a[28:31], v206 offset:8192\n" \
"v_mfma_f32_16x16x32_bf16 v[108:111], a[196:199], a[220:223], v[108:111]\n" \
"buffer_load_dwordx4 v197, s[48:51], s76 offen offset:0 lds\n" \
"v_mfma_f32_16x16x32_bf16 v[104:107], a[192:195], a[208:211], v[104:107]\n" \
"v_readlane_b32 s15, v226, 2\n" \
"v_readlane_b32 s16, v226, 4\n" \
"v_mfma_f32_16x16x32_bf16 v[104:107], a[196:199], a[212:215], v[104:107]\n" \
"s_waitcnt lgkmcnt(2)\n" \
"v_readlane_b32 s17, v226, 6\n" \
"v_cmp_gt_i32 vcc, s15, v226\n" \
"v_mfma_f32_16x16x32_bf16 v[32:35], a[32:35], a[0:3], v[32:35]\n" \
"s_add_u32 s77, s76, 24576\n" \
"ds_read_b128 a[192:195], v205 offset:12288\n" \
"v_mfma_f32_16x16x32_bf16 v[32:35], a[36:39], a[4:7], v[32:35]\n" \
"buffer_load_dwordx4 v198, s[48:51], s76 offen offset:1024 lds\n" \
"v_mfma_f32_16x16x32_bf16 v[40:43], a[32:35], a[8:11], v[40:43]\n" \
"s_and_b32 s70, vcc_lo, 0x55\n" \
"s_bcnt1_i32_b32 s70, s70\n" \
"ds_read_b128 a[196:199], v206 offset:12288\n" \
"v_mfma_f32_16x16x32_bf16 v[40:43], a[36:39], a[12:15], v[40:43]\n" \
"s_cmp_eq_u32 s70, s13\n" \
"s_cselect_b32 s14, 4, s14\n" \
"ds_read_b64_tr_b16 a[200:201], v210 offset:1024\n" \
"v_mfma_f32_16x16x32_bf16 v[0:3], a[40:43], a[8:11], v[0:3]\n" \
"s_xor_b32 s80, s80, 0x1\n" \
"ds_read_b64_tr_b16 a[202:203], v210 offset:4096\n" \
"v_cmp_gt_i32 vcc, s16, v226\n" \
"v_mfma_f32_16x16x32_bf16 v[0:3], a[44:47], a[12:15], v[0:3]\n" \
"buffer_load_dwordx4 v199, s[48:51], s76 offen offset:2048 lds\n" \
"v_mfma_f32_16x16x32_bf16 v[4:7], a[48:51], a[8:11], v[4:7]\n" \
"s_and_b32 s70, vcc_lo, 0x55\n" \
"s_bcnt1_i32_b32 s70, s70\n" \
"ds_read_b64_tr_b16 a[204:205], v210 offset:13312\n" \
"v_mfma_f32_16x16x32_bf16 v[4:7], a[52:55], a[12:15], v[4:7]\n" \
"s_cmp_eq_u32 s70, s13\n" \
"s_cselect_b32 s14, 8, s14\n" \
"ds_read_b64_tr_b16 a[206:207], v210 offset:16384\n" \
"v_mfma_f32_16x16x32_bf16 v[12:15], a[48:51], a[16:19], v[12:15]\n" \
"s_add_u32 m0, m0, 3072\n" \
"ds_read_b64_tr_b16 a[208:209], v209 offset:2048\n" \
"v_cmp_gt_i32 vcc, s17, v226\n" \
"v_mfma_f32_16x16x32_bf16 v[12:15], a[52:55], a[20:23], v[12:15]\n" \
"buffer_load_dwordx4 v197, s[48:51], s77 offen offset:0 lds\n" \
"v_mfma_f32_16x16x32_bf16 v[8:11], a[40:43], a[16:19], v[8:11]\n" \
"s_and_b32 s70, vcc_lo, 0x55\n" \
"s_bcnt1_i32_b32 s70, s70\n" \
"ds_read_b64_tr_b16 a[210:211], v209 offset:5120\n" \
"v_mfma_f32_16x16x32_bf16 v[8:11], a[44:47], a[20:23], v[8:11]\n" \
"s_cmp_eq_u32 s70, s13\n" \
"s_cselect_b32 s14, 12, s14\n" \
"ds_read_b64_tr_b16 a[212:213], v209 offset:14336\n" \
"v_mfma_f32_16x16x32_bf16 v[48:51], a[32:35], a[16:19], v[48:51]\n" \
"s_lshr_b32 s70, %1, 6\n" \
"s_add_u32 s14, s14, s70\n" \
"ds_read_b64_tr_b16 a[214:215], v209 offset:17408\n" \
"v_mfma_f32_16x16x32_bf16 v[48:51], a[36:39], a[20:23], v[48:51]\n" \
"s_waitcnt lgkmcnt(3)\n" \
"buffer_load_dwordx4 v198, s[48:51], s77 offen offset:1024 lds\n" \
"v_mfma_f32_16x16x32_bf16 v[56:59], a[32:35], a[24:27], v[56:59]\n" \
"s_cmp_eq_u32 s90, 0\n" \
"s_cselect_b32 s14, s8, s14\n" \
"ds_read_b64_tr_b16 a[216:217], v210 offset:2048\n" \
"v_mfma_f32_16x16x32_bf16 v[56:59], a[36:39], a[28:31], v[56:59]\n" \
"s_sub_u32 s10, 49, s9\n" \
"s_lshl_b32 s11, s9, 1\n" \
"ds_read_b64_tr_b16 a[218:219], v210 offset:5120\n" \
"v_mfma_f32_16x16x32_bf16 v[16:19], a[40:43], a[24:27], v[16:19]\n" \
"s_sub_u32 s11, s11, 71\n" \
"s_lshl2_add_u32 s70, s8, s61\n" \
"ds_read_b64_tr_b16 a[220:221], v210 offset:14336\n" \
"v_mfma_f32_16x16x32_bf16 v[16:19], a[44:47], a[28:31], v[16:19]\n" \
"buffer_load_dwordx4 v199, s[48:51], s77 offen offset:2048 lds\n" \
"v_mfma_f32_16x16x32_bf16 v[20:23], a[48:51], a[24:27], v[20:23]\n" \
"s_mul_i32 s81, s70, 266240\n" \
"s_mul_i32 s82, s90, s9\n" \
"ds_read_b64_tr_b16 a[222:223], v210 offset:17408\n" \
"v_mfma_f32_16x16x32_bf16 v[20:23], a[52:55], a[28:31], v[20:23]\n" \
"s_lshl_b32 s71, s82, 12\n" \
"s_add_u32 s81, s81, s71\n" \
"s_add_u32 s81, s81, 35700736\n" \
"v_mfma_f32_16x16x32_bf16 v[28:31], a[48:51], a[192:195], v[28:31]\n" \
"s_lshl2_add_u32 s70, s14, s61\n" \
"s_mul_i32 s83, s70, 69632\n" \
"s_add_u32 s83, s83, 52740096\n" \
"v_mfma_f32_16x16x32_bf16 v[28:31], a[52:55], a[196:199], v[28:31]\n" \
"s_lshl_b32 s82, s82, 17\n" \
"s_add_u32 s82, s82, s84\n" \
"s_lshl_b32 s70, s14, 19\n" \
"v_mfma_f32_16x16x32_bf16 v[24:27], a[40:43], a[192:195], v[24:27]\n" \
"s_add_u32 s88, s88, s70\n" \
"s_lshl_b32 s71, s90, 11\n" \
"s_add_u32 s88, s88, s71\n" \
"v_mfma_f32_16x16x32_bf16 v[24:27], a[44:47], a[196:199], v[24:27]\n" \
"s_lshl3_add_u32 s70, s8, s68\n" \
"s_lshl2_add_u32 s87, s70, s61\n" \
"s_lshl_b32 s87, s87, 14\n" \
"v_mfma_f32_16x16x32_bf16 v[64:67], a[32:35], a[192:195], v[64:67]\n" \
"s_add_u32 s87, s87, 57196544\n" \
"s_lshl_b32 s64, s70, 7\n" \
"s_add_u32 s64, s64, 65601536\n" \
"v_mfma_f32_16x16x32_bf16 v[64:67], a[36:39], a[196:199], v[64:67]\n" \
"s_lshl_b32 s66, s8, 7\n" \
"s_waitcnt vmcnt(11) lgkmcnt(0)\n" \
"s_barrier\n" \
"v_mfma_f32_16x16x32_bf16 v[68:71], a[200:203], a[192:195], v[68:71]\n" \
"s_lshl_b32 s67, s14, 7\n" \
"ds_read_b64_tr_b16 a[32:33], v204 offset:21504\n" \
"ds_read_b64_tr_b16 a[34:35], v204 offset:24576\n" \
"v_mfma_f32_16x16x32_bf16 v[68:71], a[204:207], a[196:199], v[68:71]\n" \
"s_add_u32 s67, s67, 65591296\n" \
"ds_read_b64_tr_b16 a[36:37], v204 offset:33792\n" \
"ds_read_b64_tr_b16 a[38:39], v204 offset:36864\n" \
"v_mfma_f32_16x16x32_bf16 v[60:63], a[200:203], a[24:27], v[60:63]\n" \
"s_add_u32 m0, s79, 65536\n" \
"s_add_u32 s66, s66, 65589248\n" \
"ds_read_b64_tr_b16 a[40:41], v204 offset:20480\n" \
"v_mfma_f32_16x16x32_bf16 v[60:63], a[204:207], a[28:31], v[60:63]\n" \
"buffer_load_dwordx4 v192, s[44:47], s73 offen offset:0 lds\n" \
"v_mfma_f32_16x16x32_bf16 v[52:55], a[200:203], a[16:19], v[52:55]\n" \
"s_add_u32 s70, s9, 1\n" \
"s_sub_u32 s70, s70, s90\n" \
"ds_read_b64_tr_b16 a[42:43], v204 offset:23552\n" \
"v_mfma_f32_16x16x32_bf16 v[52:55], a[204:207], a[20:23], v[52:55]\n" \
"s_lshr_b32 s70, s70, 1\n" \
"s_bfm_b32 s65, s70, 0\n" \
"ds_read_b64_tr_b16 a[44:45], v204 offset:32768\n" \
"v_mfma_f32_16x16x32_bf16 v[44:47], a[200:203], a[8:11], v[44:47]\n" \
"s_sub_u32 s70, 0, s90\n" \
"s_xor_b32 s65, s65, s70\n" \
"ds_read_b64_tr_b16 a[46:47], v204 offset:35840\n" \
"v_mfma_f32_16x16x32_bf16 v[44:47], a[204:207], a[12:15], v[44:47]\n" \
"buffer_load_dwordx4 v193, s[44:47], s73 offen offset:1024 lds\n" \
"v_mfma_f32_16x16x32_bf16 v[72:75], a[0:3], a[208:211], v[72:75]\n" \
"s_and_b32 s74, s70, 0xffff\n" \
"s_xor_b32 s68, s68, s8\n" \
"ds_read_b64_tr_b16 a[48:49], v222 offset:20480\n" \
"v_mfma_f32_16x16x32_bf16 v[72:75], a[4:7], a[212:215], v[72:75]\n" \
"s_and_b32 s68, s68, 3\n" \
"s_lshl_b32 s68, s68, 12\n" \
"ds_read_b128 a[0:3], v202 offset:0\n" \
"v_mfma_f32_16x16x32_bf16 v[80:83], a[8:11], a[208:211], v[80:83]\n" \
"ds_read_b128 a[4:7], v203 offset:0\n" \
"v_mfma_f32_16x16x32_bf16 v[80:83], a[12:15], a[212:215], v[80:83]\n" \
"buffer_load_dwordx4 v192, s[44:47], s73 offen offset:2048 lds\n" \
"v_mfma_f32_16x16x32_bf16 v[84:87], a[8:11], a[216:219], v[84:87]\n" \
"ds_read_b64_tr_b16 a[50:51], v222 offset:23552\n" \
"v_mfma_f32_16x16x32_bf16 v[84:87], a[12:15], a[220:223], v[84:87]\n" \
"ds_read_b128 a[8:11], v200 offset:0\n" \
"v_mfma_f32_16x16x32_bf16 v[92:95], a[16:19], a[216:219], v[92:95]\n" \
"ds_read_b128 a[12:15], v201 offset:0\n" \
"v_mfma_f32_16x16x32_bf16 v[92:95], a[20:23], a[220:223], v[92:95]\n" \
"buffer_load_dwordx4 v193, s[44:47], s73 offen offset:3072 lds\n" \
"v_mfma_f32_16x16x32_bf16 v[88:91], a[16:19], a[208:211], v[88:91]\n" \
"ds_read_b64_tr_b16 a[52:53], v222 offset:32768\n" \
"v_mfma_f32_16x16x32_bf16 v[88:91], a[20:23], a[212:215], v[88:91]\n" \
"s_add_u32 m0, s89, 65536\n" \
"ds_read_b128 a[16:19], v200 offset:4096\n" \
"v_mfma_f32_16x16x32_bf16 v[96:99], a[24:27], a[208:211], v[96:99]\n" \
"s_xor_b32 s73, s73, 0x180\n" \
"ds_read_b128 a[20:23], v201 offset:4096\n" \
"v_mfma_f32_16x16x32_bf16 v[96:99], a[28:31], a[212:215], v[96:99]\n" \
"buffer_load_dwordx4 v196, s[44:47], s75 offen lds\n" \
"v_mfma_f32_16x16x32_bf16 v[100:103], a[24:27], a[216:219], v[100:103]\n" \
"s_xor_b32 s75, s75, 0x180\n" \
"s_mul_i32 s76, s80, 393216\n" \
"ds_read_b64_tr_b16 a[54:55], v222 offset:35840\n" \
"v_mfma_f32_16x16x32_bf16 v[100:103], a[28:31], a[220:223], v[100:103]\n" \
"s_add_u32 s76, s76, s78\n" \
"ds_read_b128 a[24:27], v200 offset:8192\n" \
"v_mfma_f32_16x16x32_bf16 v[108:111], a[192:195], a[216:219], v[108:111]\n" \
"s_add_u32 m0, s91, 86016\n" \
"ds_read_b128 a[28:31], v201 offset:8192\n" \
"v_mfma_f32_16x16x32_bf16 v[108:111], a[196:199], a[220:223], v[108:111]\n" \
"buffer_load_dwordx4 v197, s[48:51], s76 offen offset:0 lds\n" \
"v_mfma_f32_16x16x32_bf16 v[104:107], a[192:195], a[208:211], v[104:107]\n" \
"v_mfma_f32_16x16x32_bf16 v[104:107], a[196:199], a[212:215], v[104:107]\n" \
"s_waitcnt lgkmcnt(2)\n" \
"v_mfma_f32_16x16x32_bf16 v[32:35], a[32:35], a[0:3], v[32:35]\n" \
"s_add_u32 s77, s76, 24576\n" \
"ds_read_b128 a[192:195], v200 offset:12288\n" \
"v_mfma_f32_16x16x32_bf16 v[32:35], a[36:39], a[4:7], v[32:35]\n" \
"buffer_load_dwordx4 v198, s[48:51], s76 offen offset:1024 lds\n" \
"v_mfma_f32_16x16x32_bf16 v[40:43], a[32:35], a[8:11], v[40:43]\n" \
"ds_read_b128 a[196:199], v201 offset:12288\n" \
"v_mfma_f32_16x16x32_bf16 v[40:43], a[36:39], a[12:15], v[40:43]\n" \
"ds_read_b64_tr_b16 a[200:201], v222 offset:21504\n" \
"v_mfma_f32_16x16x32_bf16 v[0:3], a[40:43], a[8:11], v[0:3]\n" \
"s_xor_b32 s80, s80, 0x3\n" \
"ds_read_b64_tr_b16 a[202:203], v222 offset:24576\n" \
"v_mfma_f32_16x16x32_bf16 v[0:3], a[44:47], a[12:15], v[0:3]\n" \
"buffer_load_dwordx4 v199, s[48:51], s76 offen offset:2048 lds\n" \
"v_mfma_f32_16x16x32_bf16 v[4:7], a[48:51], a[8:11], v[4:7]\n" \
"ds_read_b64_tr_b16 a[204:205], v222 offset:33792\n" \
"v_mfma_f32_16x16x32_bf16 v[4:7], a[52:55], a[12:15], v[4:7]\n" \
"ds_read_b64_tr_b16 a[206:207], v222 offset:36864\n" \
"v_mfma_f32_16x16x32_bf16 v[12:15], a[48:51], a[16:19], v[12:15]\n" \
"s_add_u32 m0, m0, 3072\n" \
"ds_read_b64_tr_b16 a[208:209], v204 offset:22528\n" \
"v_mfma_f32_16x16x32_bf16 v[12:15], a[52:55], a[20:23], v[12:15]\n" \
"buffer_load_dwordx4 v197, s[48:51], s77 offen offset:0 lds\n" \
"v_mfma_f32_16x16x32_bf16 v[8:11], a[40:43], a[16:19], v[8:11]\n" \
"ds_read_b64_tr_b16 a[210:211], v204 offset:25600\n" \
"v_mfma_f32_16x16x32_bf16 v[8:11], a[44:47], a[20:23], v[8:11]\n" \
"ds_read_b64_tr_b16 a[212:213], v204 offset:34816\n" \
"v_mfma_f32_16x16x32_bf16 v[48:51], a[32:35], a[16:19], v[48:51]\n" \
"s_waitcnt lgkmcnt(3)\n" \
"ds_read_b64_tr_b16 a[214:215], v204 offset:37888\n" \
"v_mfma_f32_16x16x32_bf16 v[48:51], a[36:39], a[20:23], v[48:51]\n" \
"buffer_load_dwordx4 v198, s[48:51], s77 offen offset:1024 lds\n" \
"v_mfma_f32_16x16x32_bf16 v[56:59], a[32:35], a[24:27], v[56:59]\n" \
"ds_read_b64_tr_b16 a[216:217], v222 offset:22528\n" \
"v_mfma_f32_16x16x32_bf16 v[56:59], a[36:39], a[28:31], v[56:59]\n" \
"ds_read_b64_tr_b16 a[218:219], v222 offset:25600\n" \
"v_mfma_f32_16x16x32_bf16 v[16:19], a[40:43], a[24:27], v[16:19]\n" \
"ds_read_b64_tr_b16 a[220:221], v222 offset:34816\n" \
"v_mfma_f32_16x16x32_bf16 v[16:19], a[44:47], a[28:31], v[16:19]\n" \
"buffer_load_dwordx4 v199, s[48:51], s77 offen offset:2048 lds\n" \
"v_mfma_f32_16x16x32_bf16 v[20:23], a[48:51], a[24:27], v[20:23]\n" \
"ds_read_b64_tr_b16 a[222:223], v222 offset:37888\n" \
"v_mfma_f32_16x16x32_bf16 v[20:23], a[52:55], a[28:31], v[20:23]\n" \
"v_mfma_f32_16x16x32_bf16 v[28:31], a[48:51], a[192:195], v[28:31]\n" \
"v_mfma_f32_16x16x32_bf16 v[28:31], a[52:55], a[196:199], v[28:31]\n" \
"v_mfma_f32_16x16x32_bf16 v[24:27], a[40:43], a[192:195], v[24:27]\n" \
"v_mfma_f32_16x16x32_bf16 v[24:27], a[44:47], a[196:199], v[24:27]\n" \
"v_mfma_f32_16x16x32_bf16 v[64:67], a[32:35], a[192:195], v[64:67]\n" \
"v_mfma_f32_16x16x32_bf16 v[64:67], a[36:39], a[196:199], v[64:67]\n" \
"s_waitcnt vmcnt(11) lgkmcnt(0)\n" \
"s_barrier\n" \
"v_mfma_f32_16x16x32_bf16 v[68:71], a[200:203], a[192:195], v[68:71]\n" \
"ds_read_b64_tr_b16 a[32:33], v194 offset:1024\n" \
"ds_read_b64_tr_b16 a[34:35], v194 offset:4096\n" \
"v_mfma_f32_16x16x32_bf16 v[68:71], a[204:207], a[196:199], v[68:71]\n" \
"ds_read_b64_tr_b16 a[36:37], v194 offset:13312\n" \
"ds_read_b64_tr_b16 a[38:39], v194 offset:16384\n" \
"v_mfma_f32_16x16x32_bf16 v[60:63], a[200:203], a[24:27], v[60:63]\n" \
"s_add_u32 m0, s79, 0\n" \
"ds_read_b64_tr_b16 a[40:41], v194 offset:0\n" \
"Lpg9091_%=:\n" \
"Lst1x2_%=:\n" \
"s_cbranch_execz Lpg10950_%=\n" \
"v_mfma_f32_16x16x32_bf16 v[60:63], a[204:207], a[28:31], v[60:63]\n" \
"buffer_load_dwordx4 v192, s[44:47], s73 offen offset:0 lds\n" \
"v_mfma_f32_16x16x32_bf16 v[52:55], a[200:203], a[16:19], v[52:55]\n" \
"ds_read_b64_tr_b16 a[42:43], v194 offset:3072\n" \
"v_mfma_f32_16x16x32_bf16 v[52:55], a[204:207], a[20:23], v[52:55]\n" \
"ds_read_b64_tr_b16 a[44:45], v194 offset:12288\n" \
"v_mfma_f32_16x16x32_bf16 v[44:47], a[200:203], a[8:11], v[44:47]\n" \
"ds_read_b64_tr_b16 a[46:47], v194 offset:15360\n" \
"v_mfma_f32_16x16x32_bf16 v[44:47], a[204:207], a[12:15], v[44:47]\n" \
"buffer_load_dwordx4 v193, s[44:47], s73 offen offset:1024 lds\n" \
"v_mfma_f32_16x16x32_bf16 v[72:75], a[0:3], a[208:211], v[72:75]\n" \
"ds_read_b64_tr_b16 a[48:49], v195 offset:0\n" \
"v_mfma_f32_16x16x32_bf16 v[72:75], a[4:7], a[212:215], v[72:75]\n" \
"ds_read_b128 a[0:3], v207 offset:45056\n" \
"v_mfma_f32_16x16x32_bf16 v[80:83], a[8:11], a[208:211], v[80:83]\n" \
"ds_read_b128 a[4:7], v208 offset:45056\n" \
"v_mfma_f32_16x16x32_bf16 v[80:83], a[12:15], a[212:215], v[80:83]\n" \
"buffer_load_dwordx4 v192, s[44:47], s73 offen offset:2048 lds\n" \
"v_mfma_f32_16x16x32_bf16 v[84:87], a[8:11], a[216:219], v[84:87]\n" \
"ds_read_b64_tr_b16 a[50:51], v195 offset:3072\n" \
"v_mfma_f32_16x16x32_bf16 v[84:87], a[12:15], a[220:223], v[84:87]\n" \
"ds_read_b128 a[8:11], v205 offset:45056\n" \
"v_mfma_f32_16x16x32_bf16 v[92:95], a[16:19], a[216:219], v[92:95]\n" \
"ds_read_b128 a[12:15], v206 offset:45056\n" \
"v_mfma_f32_16x16x32_bf16 v[92:95], a[20:23], a[220:223], v[92:95]\n" \
"buffer_load_dwordx4 v193, s[44:47], s73 offen offset:3072 lds\n" \
"v_mfma_f32_16x16x32_bf16 v[88:91], a[16:19], a[208:211], v[88:91]\n" \
"ds_read_b64_tr_b16 a[52:53], v195 offset:12288\n" \
"v_mfma_f32_16x16x32_bf16 v[88:91], a[20:23], a[212:215], v[88:91]\n" \
"s_add_u32 m0, s89, 0\n" \
"ds_read_b128 a[16:19], v205 offset:49152\n" \
"v_mfma_f32_16x16x32_bf16 v[96:99], a[24:27], a[208:211], v[96:99]\n" \
"s_xor_b32 s73, s73, 0x80\n" \
"ds_read_b128 a[20:23], v206 offset:49152\n" \
"v_mfma_f32_16x16x32_bf16 v[96:99], a[28:31], a[212:215], v[96:99]\n" \
"buffer_load_dwordx4 v196, s[44:47], s75 offen lds\n" \
"v_mfma_f32_16x16x32_bf16 v[100:103], a[24:27], a[216:219], v[100:103]\n" \
"s_xor_b32 s75, s75, 0x80\n" \
"s_mul_i32 s76, s80, 393216\n" \
"ds_read_b64_tr_b16 a[54:55], v195 offset:15360\n" \
"v_mfma_f32_16x16x32_bf16 v[100:103], a[28:31], a[220:223], v[100:103]\n" \
"s_add_u32 s76, s76, s78\n" \
"ds_read_b128 a[24:27], v205 offset:53248\n" \
"v_mfma_f32_16x16x32_bf16 v[108:111], a[192:195], a[216:219], v[108:111]\n" \
"s_add_u32 m0, s91, 20480\n" \
"ds_read_b128 a[28:31], v206 offset:53248\n" \
"v_mfma_f32_16x16x32_bf16 v[108:111], a[196:199], a[220:223], v[108:111]\n" \
"buffer_load_dwordx4 v197, s[48:51], s76 offen offset:0 lds\n" \
"v_mfma_f32_16x16x32_bf16 v[104:107], a[192:195], a[208:211], v[104:107]\n" \
"v_mfma_f32_16x16x32_bf16 v[104:107], a[196:199], a[212:215], v[104:107]\n" \
"s_waitcnt lgkmcnt(2)\n" \
"v_mfma_f32_16x16x32_bf16 v[32:35], a[32:35], a[0:3], v[32:35]\n" \
"s_add_u32 s77, s76, 24576\n" \
"ds_read_b128 a[192:195], v205 offset:57344\n" \
"v_mfma_f32_16x16x32_bf16 v[32:35], a[36:39], a[4:7], v[32:35]\n" \
"buffer_load_dwordx4 v198, s[48:51], s76 offen offset:1024 lds\n" \
"v_mfma_f32_16x16x32_bf16 v[40:43], a[32:35], a[8:11], v[40:43]\n" \
"ds_read_b128 a[196:199], v206 offset:57344\n" \
"v_mfma_f32_16x16x32_bf16 v[40:43], a[36:39], a[12:15], v[40:43]\n" \
"ds_read_b64_tr_b16 a[200:201], v195 offset:1024\n" \
"v_mfma_f32_16x16x32_bf16 v[0:3], a[40:43], a[8:11], v[0:3]\n" \
"s_xor_b32 s80, s80, 0x1\n" \
"ds_read_b64_tr_b16 a[202:203], v195 offset:4096\n" \
"v_mfma_f32_16x16x32_bf16 v[0:3], a[44:47], a[12:15], v[0:3]\n" \
"buffer_load_dwordx4 v199, s[48:51], s76 offen offset:2048 lds\n" \
"v_mfma_f32_16x16x32_bf16 v[4:7], a[48:51], a[8:11], v[4:7]\n" \
"ds_read_b64_tr_b16 a[204:205], v195 offset:13312\n" \
"v_mfma_f32_16x16x32_bf16 v[4:7], a[52:55], a[12:15], v[4:7]\n" \
"ds_read_b64_tr_b16 a[206:207], v195 offset:16384\n" \
"v_mfma_f32_16x16x32_bf16 v[12:15], a[48:51], a[16:19], v[12:15]\n" \
"s_add_u32 m0, m0, 3072\n" \
"ds_read_b64_tr_b16 a[208:209], v194 offset:2048\n" \
"v_mfma_f32_16x16x32_bf16 v[12:15], a[52:55], a[20:23], v[12:15]\n" \
"buffer_load_dwordx4 v197, s[48:51], s77 offen offset:0 lds\n" \
"v_mfma_f32_16x16x32_bf16 v[8:11], a[40:43], a[16:19], v[8:11]\n" \
"ds_read_b64_tr_b16 a[210:211], v194 offset:5120\n" \
"v_mfma_f32_16x16x32_bf16 v[8:11], a[44:47], a[20:23], v[8:11]\n" \
"ds_read_b64_tr_b16 a[212:213], v194 offset:14336\n" \
"v_mfma_f32_16x16x32_bf16 v[48:51], a[32:35], a[16:19], v[48:51]\n" \
"s_waitcnt lgkmcnt(3)\n" \
"ds_read_b64_tr_b16 a[214:215], v194 offset:17408\n" \
"v_mfma_f32_16x16x32_bf16 v[48:51], a[36:39], a[20:23], v[48:51]\n" \
"buffer_load_dwordx4 v198, s[48:51], s77 offen offset:1024 lds\n" \
"v_mfma_f32_16x16x32_bf16 v[56:59], a[32:35], a[24:27], v[56:59]\n" \
"ds_read_b64_tr_b16 a[216:217], v195 offset:2048\n" \
"v_mfma_f32_16x16x32_bf16 v[56:59], a[36:39], a[28:31], v[56:59]\n" \
"ds_read_b64_tr_b16 a[218:219], v195 offset:5120\n" \
"v_mfma_f32_16x16x32_bf16 v[16:19], a[40:43], a[24:27], v[16:19]\n" \
"ds_read_b64_tr_b16 a[220:221], v195 offset:14336\n" \
"v_mfma_f32_16x16x32_bf16 v[16:19], a[44:47], a[28:31], v[16:19]\n" \
"buffer_load_dwordx4 v199, s[48:51], s77 offen offset:2048 lds\n" \
"v_mfma_f32_16x16x32_bf16 v[20:23], a[48:51], a[24:27], v[20:23]\n" \
"ds_read_b64_tr_b16 a[222:223], v195 offset:17408\n" \
"v_mfma_f32_16x16x32_bf16 v[20:23], a[52:55], a[28:31], v[20:23]\n" \
"v_mfma_f32_16x16x32_bf16 v[28:31], a[48:51], a[192:195], v[28:31]\n" \
"v_mfma_f32_16x16x32_bf16 v[28:31], a[52:55], a[196:199], v[28:31]\n" \
"buffer_load_dword v220, v221, s[56:59], s66 offen sc1\n" \
"v_mfma_f32_16x16x32_bf16 v[24:27], a[40:43], a[192:195], v[24:27]\n" \
"v_mfma_f32_16x16x32_bf16 v[24:27], a[44:47], a[196:199], v[24:27]\n" \
"v_mfma_f32_16x16x32_bf16 v[64:67], a[32:35], a[192:195], v[64:67]\n" \
"v_mfma_f32_16x16x32_bf16 v[64:67], a[36:39], a[196:199], v[64:67]\n" \
"s_waitcnt vmcnt(12) lgkmcnt(0)\n" \
"s_barrier\n" \
"v_mfma_f32_16x16x32_bf16 v[68:71], a[200:203], a[192:195], v[68:71]\n" \
"ds_read_b64_tr_b16 a[32:33], v209 offset:1024\n" \
"ds_read_b64_tr_b16 a[34:35], v209 offset:4096\n" \
"v_mfma_f32_16x16x32_bf16 v[68:71], a[204:207], a[196:199], v[68:71]\n" \
"ds_read_b64_tr_b16 a[36:37], v209 offset:13312\n" \
"ds_read_b64_tr_b16 a[38:39], v209 offset:16384\n" \
"v_mfma_f32_16x16x32_bf16 v[60:63], a[200:203], a[24:27], v[60:63]\n" \
"s_add_u32 m0, s79, 110592\n" \
"ds_read_b64_tr_b16 a[40:41], v209 offset:0\n" \
"v_mfma_f32_16x16x32_bf16 v[60:63], a[204:207], a[28:31], v[60:63]\n" \
"buffer_load_dwordx4 v192, s[44:47], s73 offen offset:0 lds\n" \
"v_mfma_f32_16x16x32_bf16 v[52:55], a[200:203], a[16:19], v[52:55]\n" \
"ds_read_b64_tr_b16 a[42:43], v209 offset:3072\n" \
"v_mfma_f32_16x16x32_bf16 v[52:55], a[204:207], a[20:23], v[52:55]\n" \
"ds_read_b64_tr_b16 a[44:45], v209 offset:12288\n" \
"v_mfma_f32_16x16x32_bf16 v[44:47], a[200:203], a[8:11], v[44:47]\n" \
"ds_read_b64_tr_b16 a[46:47], v209 offset:15360\n" \
"v_mfma_f32_16x16x32_bf16 v[44:47], a[204:207], a[12:15], v[44:47]\n" \
"buffer_load_dwordx4 v193, s[44:47], s73 offen offset:1024 lds\n" \
"v_mfma_f32_16x16x32_bf16 v[72:75], a[0:3], a[208:211], v[72:75]\n" \
"ds_read_b64_tr_b16 a[48:49], v210 offset:0\n" \
"v_mfma_f32_16x16x32_bf16 v[72:75], a[4:7], a[212:215], v[72:75]\n" \
"ds_read_b128 a[0:3], v207 offset:0\n" \
"v_mfma_f32_16x16x32_bf16 v[80:83], a[8:11], a[208:211], v[80:83]\n" \
"ds_read_b128 a[4:7], v208 offset:0\n" \
"v_mfma_f32_16x16x32_bf16 v[80:83], a[12:15], a[212:215], v[80:83]\n" \
"buffer_load_dwordx4 v192, s[44:47], s73 offen offset:2048 lds\n" \
"v_mfma_f32_16x16x32_bf16 v[84:87], a[8:11], a[216:219], v[84:87]\n" \
"ds_read_b64_tr_b16 a[50:51], v210 offset:3072\n" \
"v_mfma_f32_16x16x32_bf16 v[84:87], a[12:15], a[220:223], v[84:87]\n" \
"ds_read_b128 a[8:11], v205 offset:0\n" \
"v_mfma_f32_16x16x32_bf16 v[92:95], a[16:19], a[216:219], v[92:95]\n" \
"ds_read_b128 a[12:15], v206 offset:0\n" \
"v_mfma_f32_16x16x32_bf16 v[92:95], a[20:23], a[220:223], v[92:95]\n" \
"buffer_load_dwordx4 v193, s[44:47], s73 offen offset:3072 lds\n" \
"v_mfma_f32_16x16x32_bf16 v[88:91], a[16:19], a[208:211], v[88:91]\n" \
"ds_read_b64_tr_b16 a[52:53], v210 offset:12288\n" \
"v_mfma_f32_16x16x32_bf16 v[88:91], a[20:23], a[212:215], v[88:91]\n" \
"s_add_u32 m0, s89, 110592\n" \
"ds_read_b128 a[16:19], v205 offset:4096\n" \
"v_mfma_f32_16x16x32_bf16 v[96:99], a[24:27], a[208:211], v[96:99]\n" \
"s_xor_b32 s73, s73, 0x80\n" \
"ds_read_b128 a[20:23], v206 offset:4096\n" \
"v_mfma_f32_16x16x32_bf16 v[96:99], a[28:31], a[212:215], v[96:99]\n" \
"buffer_load_dwordx4 v196, s[44:47], s75 offen lds\n" \
"v_mfma_f32_16x16x32_bf16 v[100:103], a[24:27], a[216:219], v[100:103]\n" \
"s_xor_b32 s75, s75, 0x80\n" \
"s_mul_i32 s76, s80, 393216\n" \
"ds_read_b64_tr_b16 a[54:55], v210 offset:15360\n" \
"v_mfma_f32_16x16x32_bf16 v[100:103], a[28:31], a[220:223], v[100:103]\n" \
"s_add_u32 s76, s76, s78\n" \
"ds_read_b128 a[24:27], v205 offset:8192\n" \
"v_mfma_f32_16x16x32_bf16 v[108:111], a[192:195], a[216:219], v[108:111]\n" \
"s_add_u32 m0, s91, 139264\n" \
"ds_read_b128 a[28:31], v206 offset:8192\n" \
"v_mfma_f32_16x16x32_bf16 v[108:111], a[196:199], a[220:223], v[108:111]\n" \
"buffer_load_dwordx4 v197, s[48:51], s76 offen offset:0 lds\n" \
"v_mfma_f32_16x16x32_bf16 v[104:107], a[192:195], a[208:211], v[104:107]\n" \
"v_mfma_f32_16x16x32_bf16 v[104:107], a[196:199], a[212:215], v[104:107]\n" \
"s_waitcnt lgkmcnt(2)\n" \
"v_mfma_f32_16x16x32_bf16 v[32:35], a[32:35], a[0:3], v[32:35]\n" \
"s_add_u32 s77, s76, 24576\n" \
"ds_read_b128 a[192:195], v205 offset:12288\n" \
"v_mfma_f32_16x16x32_bf16 v[32:35], a[36:39], a[4:7], v[32:35]\n" \
"buffer_load_dwordx4 v198, s[48:51], s76 offen offset:1024 lds\n" \
"v_mfma_f32_16x16x32_bf16 v[40:43], a[32:35], a[8:11], v[40:43]\n" \
"ds_read_b128 a[196:199], v206 offset:12288\n" \
"v_mfma_f32_16x16x32_bf16 v[40:43], a[36:39], a[12:15], v[40:43]\n" \
"ds_read_b64_tr_b16 a[200:201], v210 offset:1024\n" \
"v_mfma_f32_16x16x32_bf16 v[0:3], a[40:43], a[8:11], v[0:3]\n" \
"s_xor_b32 s80, s80, 0x1\n" \
"ds_read_b64_tr_b16 a[202:203], v210 offset:4096\n" \
"v_mfma_f32_16x16x32_bf16 v[0:3], a[44:47], a[12:15], v[0:3]\n" \
"buffer_load_dwordx4 v199, s[48:51], s76 offen offset:2048 lds\n" \
"v_mfma_f32_16x16x32_bf16 v[4:7], a[48:51], a[8:11], v[4:7]\n" \
"ds_read_b64_tr_b16 a[204:205], v210 offset:13312\n" \
"v_mfma_f32_16x16x32_bf16 v[4:7], a[52:55], a[12:15], v[4:7]\n" \
"ds_read_b64_tr_b16 a[206:207], v210 offset:16384\n" \
"v_mfma_f32_16x16x32_bf16 v[12:15], a[48:51], a[16:19], v[12:15]\n" \
"s_add_u32 m0, m0, 3072\n" \
"ds_read_b64_tr_b16 a[208:209], v209 offset:2048\n" \
"v_mfma_f32_16x16x32_bf16 v[12:15], a[52:55], a[20:23], v[12:15]\n" \
"buffer_load_dwordx4 v197, s[48:51], s77 offen offset:0 lds\n" \
"v_mfma_f32_16x16x32_bf16 v[8:11], a[40:43], a[16:19], v[8:11]\n" \
"ds_read_b64_tr_b16 a[210:211], v209 offset:5120\n" \
"v_mfma_f32_16x16x32_bf16 v[8:11], a[44:47], a[20:23], v[8:11]\n" \
"ds_read_b64_tr_b16 a[212:213], v209 offset:14336\n" \
"v_mfma_f32_16x16x32_bf16 v[48:51], a[32:35], a[16:19], v[48:51]\n" \
"s_waitcnt lgkmcnt(3)\n" \
"ds_read_b64_tr_b16 a[214:215], v209 offset:17408\n" \
"v_mfma_f32_16x16x32_bf16 v[48:51], a[36:39], a[20:23], v[48:51]\n" \
"buffer_load_dwordx4 v198, s[48:51], s77 offen offset:1024 lds\n" \
"v_mfma_f32_16x16x32_bf16 v[56:59], a[32:35], a[24:27], v[56:59]\n" \
"ds_read_b64_tr_b16 a[216:217], v210 offset:2048\n" \
"v_mfma_f32_16x16x32_bf16 v[56:59], a[36:39], a[28:31], v[56:59]\n" \
"ds_read_b64_tr_b16 a[218:219], v210 offset:5120\n" \
"v_mfma_f32_16x16x32_bf16 v[16:19], a[40:43], a[24:27], v[16:19]\n" \
"ds_read_b64_tr_b16 a[220:221], v210 offset:14336\n" \
"v_mfma_f32_16x16x32_bf16 v[16:19], a[44:47], a[28:31], v[16:19]\n" \
"buffer_load_dwordx4 v199, s[48:51], s77 offen offset:2048 lds\n" \
"v_mfma_f32_16x16x32_bf16 v[20:23], a[48:51], a[24:27], v[20:23]\n" \
"s_mov_b32 s46, s81\n" \
"s_mov_b32 s47, s82\n" \
"ds_read_b64_tr_b16 a[222:223], v210 offset:17408\n" \
"v_mfma_f32_16x16x32_bf16 v[20:23], a[52:55], a[28:31], v[20:23]\n" \
"s_mov_b32 s48, 0\n" \
"s_mov_b32 s49, s68\n" \
"s_sub_u32 s50, 64, s9\n" \
"v_mfma_f32_16x16x32_bf16 v[28:31], a[48:51], a[192:195], v[28:31]\n" \
"s_cmp_eq_u32 s90, 0\n" \
"s_cselect_b32 s50, s9, s50\n" \
"s_andn2_b32 s50, s50, 3\n" \
"v_mfma_f32_16x16x32_bf16 v[28:31], a[52:55], a[196:199], v[28:31]\n" \
"s_lshl_b32 s50, s50, 12\n" \
"s_waitcnt vmcnt(11)\n" \
"v_cmp_eq_u32 vcc, s60, v220\n" \
"v_mfma_f32_16x16x32_bf16 v[24:27], a[40:43], a[192:195], v[24:27]\n" \
"v_mfma_f32_16x16x32_bf16 v[24:27], a[44:47], a[196:199], v[24:27]\n" \
"s_andn2_b32 s70, s65, vcc_lo\n" \
"s_cbranch_scc1 Lpl5_%=\n" \
"Lpr5_%=:\n" \
"v_mfma_f32_16x16x32_bf16 v[64:67], a[32:35], a[192:195], v[64:67]\n" \
"v_mfma_f32_16x16x32_bf16 v[64:67], a[36:39], a[196:199], v[64:67]\n" \
"s_xor_b32 s82, s48, s49\n" \
"s_cmp_lt_u32 s48, s50\n" \
"s_cselect_b32 s82, s82, s48\n" \
"s_waitcnt vmcnt(12) lgkmcnt(0)\n" \
"s_barrier\n" \
"v_mfma_f32_16x16x32_bf16 v[68:71], a[200:203], a[192:195], v[68:71]\n" \
"ds_read_b64_tr_b16 a[32:33], v204 offset:21504\n" \
"ds_read_b64_tr_b16 a[34:35], v204 offset:24576\n" \
"v_mfma_f32_16x16x32_bf16 v[68:71], a[204:207], a[196:199], v[68:71]\n" \
"s_add_u32 s81, s82, s46\n" \
"s_lshr_b32 s78, s61, 1\n" \
"s_lshl_b32 s80, s78, 9\n" \
"s_add_u32 s80, s80, 511\n" \
"ds_read_b64_tr_b16 a[36:37], v204 offset:33792\n" \
"ds_read_b64_tr_b16 a[38:39], v204 offset:36864\n" \
"v_mfma_f32_16x16x32_bf16 v[60:63], a[200:203], a[24:27], v[60:63]\n" \
"s_add_u32 m0, s86, s79\n" \
"ds_read_b64_tr_b16 a[40:41], v204 offset:20480\n" \
"v_sub_u32 v145, s80, v234\n" \
"v_mfma_f32_16x16x32_bf16 v[60:63], a[204:207], a[28:31], v[60:63]\n" \
"buffer_load_dwordx4 v234, s[56:59], s81 offen offset:0 sc1 lds\n" \
"v_mfma_f32_16x16x32_bf16 v[52:55], a[200:203], a[16:19], v[52:55]\n" \
"s_lshl_b32 s80, s78, 3\n" \
"s_not_b32 s80, s80\n" \
"ds_read_b64_tr_b16 a[42:43], v204 offset:23552\n" \
"v_mfma_f32_16x16x32_bf16 v[52:55], a[204:207], a[20:23], v[52:55]\n" \
"s_lshl_b32 s82, s82, 5\n" \
"ds_read_b64_tr_b16 a[44:45], v204 offset:32768\n" \
"v_sub_u32 v146, s80, v238\n" \
"v_mfma_f32_16x16x32_bf16 v[44:47], a[200:203], a[8:11], v[44:47]\n" \
"s_add_u32 s82, s82, s47\n" \
"ds_read_b64_tr_b16 a[46:47], v204 offset:35840\n" \
"v_subrev_u32 v152, 0, v238\n" \
"v_mfma_f32_16x16x32_bf16 v[44:47], a[204:207], a[12:15], v[44:47]\n" \
"buffer_load_dwordx4 v234, s[56:59], s81 offen offset:1024 sc1 lds\n" \
"v_mfma_f32_16x16x32_bf16 v[72:75], a[0:3], a[208:211], v[72:75]\n" \
"ds_read_b64_tr_b16 a[48:49], v222 offset:20480\n" \
"v_sub_u32 v144, 7, v238\n" \
"v_mfma_f32_16x16x32_bf16 v[72:75], a[4:7], a[212:215], v[72:75]\n" \
"s_add_u32 s48, s48, 0x1000\n" \
"ds_read_b128 a[0:3], v202 offset:0\n" \
"v_or_b32 v152, v152, v144\n" \
"v_mfma_f32_16x16x32_bf16 v[80:83], a[8:11], a[208:211], v[80:83]\n" \
"s_sub_u32 s86, s86, 32768\n" \
"s_cselect_b32 s86, 131072, s86\n" \
"ds_read_b128 a[4:7], v203 offset:0\n" \
"v_mfma_f32_16x16x32_bf16 v[80:83], a[12:15], a[212:215], v[80:83]\n" \
"buffer_load_dwordx4 v234, s[56:59], s81 offen offset:2048 sc1 lds\n" \
"v_mfma_f32_16x16x32_bf16 v[84:87], a[8:11], a[216:219], v[84:87]\n" \
"ds_read_b64_tr_b16 a[50:51], v222 offset:23552\n" \
"v_ashrrev_i32 v152, 31, v152\n" \
"v_mfma_f32_16x16x32_bf16 v[84:87], a[12:15], a[220:223], v[84:87]\n" \
"ds_read_b128 a[8:11], v200 offset:0\n" \
"v_and_b32 v152, 0xff800000, v152\n" \
"v_mfma_f32_16x16x32_bf16 v[92:95], a[16:19], a[216:219], v[92:95]\n" \
"ds_read_b128 a[12:15], v201 offset:0\n" \
"v_sub_u32 v156, -9, v238\n" \
"v_mfma_f32_16x16x32_bf16 v[92:95], a[20:23], a[220:223], v[92:95]\n" \
"buffer_load_dwordx4 v234, s[56:59], s81 offen offset:3072 sc1 lds\n" \
"v_mfma_f32_16x16x32_bf16 v[88:91], a[16:19], a[208:211], v[88:91]\n" \
"ds_read_b64_tr_b16 a[52:53], v222 offset:32768\n" \
"v_ashrrev_i32 v156, 31, v156\n" \
"v_mfma_f32_16x16x32_bf16 v[88:91], a[20:23], a[212:215], v[88:91]\n" \
"ds_read_b128 a[16:19], v200 offset:4096\n" \
"v_and_b32 v156, 0xff800000, v156\n" \
"v_mfma_f32_16x16x32_bf16 v[96:99], a[24:27], a[208:211], v[96:99]\n" \
"s_add_u32 m0, m0, 0x4000\n" \
"ds_read_b128 a[20:23], v201 offset:4096\n" \
"v_add_u32 v148, 0, v146\n" \
"v_mfma_f32_16x16x32_bf16 v[96:99], a[28:31], a[212:215], v[96:99]\n" \
"buffer_load_dwordx4 v216, s[36:39], s82 offen offset:0 lds\n" \
"v_mfma_f32_16x16x32_bf16 v[100:103], a[24:27], a[216:219], v[100:103]\n" \
"ds_read_b64_tr_b16 a[54:55], v222 offset:35840\n" \
"v_or3_b32 v148, v148, v145, s92\n" \
"v_mfma_f32_16x16x32_bf16 v[100:103], a[28:31], a[220:223], v[100:103]\n" \
"ds_read_b128 a[24:27], v200 offset:8192\n" \
"v_ashrrev_i32 v148, 31, v148\n" \
"v_mfma_f32_16x16x32_bf16 v[108:111], a[192:195], a[216:219], v[108:111]\n" \
"ds_read_b128 a[28:31], v201 offset:8192\n" \
"v_and_b32 v148, 0xff800000, v148\n" \
"v_mfma_f32_16x16x32_bf16 v[108:111], a[196:199], a[220:223], v[108:111]\n" \
"buffer_load_dwordx4 v217, s[36:39], s82 offen offset:1024 lds\n" \
"v_mfma_f32_16x16x32_bf16 v[104:107], a[192:195], a[208:211], v[104:107]\n" \
"v_subrev_u32 v153, 1, v238\n" \
"v_sub_u32 v144, 8, v238\n" \
"v_mfma_f32_16x16x32_bf16 v[104:107], a[196:199], a[212:215], v[104:107]\n" \
"s_waitcnt lgkmcnt(2)\n" \
"v_or_b32 v153, v153, v144\n" \
"v_ashrrev_i32 v153, 31, v153\n" \
"v_mfma_f32_16x16x32_bf16 v[32:35], a[32:35], a[0:3], v[32:35]\n" \
"ds_read_b128 a[192:195], v200 offset:12288\n" \
"v_and_b32 v153, 0xff800000, v153\n" \
"v_mfma_f32_16x16x32_bf16 v[32:35], a[36:39], a[4:7], v[32:35]\n" \
"buffer_load_dwordx4 v218, s[36:39], s82 offen offset:2048 lds\n" \
"v_mfma_f32_16x16x32_bf16 v[40:43], a[32:35], a[8:11], v[40:43]\n" \
"ds_read_b128 a[196:199], v201 offset:12288\n" \
"v_sub_u32 v157, -8, v238\n" \
"v_mfma_f32_16x16x32_bf16 v[40:43], a[36:39], a[12:15], v[40:43]\n" \
"ds_read_b64_tr_b16 a[200:201], v222 offset:21504\n" \
"v_ashrrev_i32 v157, 31, v157\n" \
"v_mfma_f32_16x16x32_bf16 v[0:3], a[40:43], a[8:11], v[0:3]\n" \
"ds_read_b64_tr_b16 a[202:203], v222 offset:24576\n" \
"v_and_b32 v157, 0xff800000, v157\n" \
"v_mfma_f32_16x16x32_bf16 v[0:3], a[44:47], a[12:15], v[0:3]\n" \
"buffer_load_dwordx4 v219, s[36:39], s82 offen offset:3072 lds\n" \
"v_mfma_f32_16x16x32_bf16 v[4:7], a[48:51], a[8:11], v[4:7]\n" \
"ds_read_b64_tr_b16 a[204:205], v222 offset:33792\n" \
"v_add_u32 v149, 1, v146\n" \
"v_mfma_f32_16x16x32_bf16 v[4:7], a[52:55], a[12:15], v[4:7]\n" \
"ds_read_b64_tr_b16 a[206:207], v222 offset:36864\n" \
"v_or3_b32 v149, v149, v145, s92\n" \
"v_mfma_f32_16x16x32_bf16 v[12:15], a[48:51], a[16:19], v[12:15]\n" \
"ds_read_b64_tr_b16 a[208:209], v204 offset:22528\n" \
"v_ashrrev_i32 v149, 31, v149\n" \
"v_mfma_f32_16x16x32_bf16 v[12:15], a[52:55], a[20:23], v[12:15]\n" \
"v_and_b32 v149, 0xff800000, v149\n" \
"v_subrev_u32 v154, 2, v238\n" \
"v_mfma_f32_16x16x32_bf16 v[8:11], a[40:43], a[16:19], v[8:11]\n" \
"ds_read_b64_tr_b16 a[210:211], v204 offset:25600\n" \
"v_sub_u32 v144, 9, v238\n" \
"v_mfma_f32_16x16x32_bf16 v[8:11], a[44:47], a[20:23], v[8:11]\n" \
"ds_read_b64_tr_b16 a[212:213], v204 offset:34816\n" \
"v_or_b32 v154, v154, v144\n" \
"v_mfma_f32_16x16x32_bf16 v[48:51], a[32:35], a[16:19], v[48:51]\n" \
"ds_read_b64_tr_b16 a[214:215], v204 offset:37888\n" \
"v_ashrrev_i32 v154, 31, v154\n" \
"v_mfma_f32_16x16x32_bf16 v[48:51], a[36:39], a[20:23], v[48:51]\n" \
"s_waitcnt lgkmcnt(3)\n" \
"v_and_b32 v154, 0xff800000, v154\n" \
"v_sub_u32 v158, -7, v238\n" \
"v_mfma_f32_16x16x32_bf16 v[56:59], a[32:35], a[24:27], v[56:59]\n" \
"ds_read_b64_tr_b16 a[216:217], v222 offset:22528\n" \
"v_ashrrev_i32 v158, 31, v158\n" \
"v_mfma_f32_16x16x32_bf16 v[56:59], a[36:39], a[28:31], v[56:59]\n" \
"ds_read_b64_tr_b16 a[218:219], v222 offset:25600\n" \
"v_and_b32 v158, 0xff800000, v158\n" \
"v_mfma_f32_16x16x32_bf16 v[16:19], a[40:43], a[24:27], v[16:19]\n" \
"ds_read_b64_tr_b16 a[220:221], v222 offset:34816\n" \
"v_add_u32 v150, 2, v146\n" \
"v_mfma_f32_16x16x32_bf16 v[16:19], a[44:47], a[28:31], v[16:19]\n" \
"v_or3_b32 v150, v150, v145, s92\n" \
"v_ashrrev_i32 v150, 31, v150\n" \
"v_mfma_f32_16x16x32_bf16 v[20:23], a[48:51], a[24:27], v[20:23]\n" \
"ds_read_b64_tr_b16 a[222:223], v222 offset:37888\n" \
"v_and_b32 v150, 0xff800000, v150\n" \
"v_mfma_f32_16x16x32_bf16 v[20:23], a[52:55], a[28:31], v[20:23]\n" \
"v_subrev_u32 v155, 3, v238\n" \
"v_sub_u32 v144, 10, v238\n" \
"v_mfma_f32_16x16x32_bf16 v[28:31], a[48:51], a[192:195], v[28:31]\n" \
"v_or_b32 v155, v155, v144\n" \
"v_ashrrev_i32 v155, 31, v155\n" \
"v_mfma_f32_16x16x32_bf16 v[28:31], a[52:55], a[196:199], v[28:31]\n" \
"v_and_b32 v155, 0xff800000, v155\n" \
"v_sub_u32 v159, -6, v238\n" \
"v_mfma_f32_16x16x32_bf16 v[24:27], a[40:43], a[192:195], v[24:27]\n" \
"v_ashrrev_i32 v159, 31, v159\n" \
"v_and_b32 v159, 0xff800000, v159\n" \
"v_mfma_f32_16x16x32_bf16 v[24:27], a[44:47], a[196:199], v[24:27]\n" \
"v_add_u32 v151, 3, v146\n" \
"v_or3_b32 v151, v151, v145, s92\n" \
"v_mfma_f32_16x16x32_bf16 v[64:67], a[32:35], a[192:195], v[64:67]\n" \
"v_ashrrev_i32 v151, 31, v151\n" \
"v_and_b32 v151, 0xff800000, v151\n" \
"v_mfma_f32_16x16x32_bf16 v[64:67], a[36:39], a[196:199], v[64:67]\n" \
"s_xor_b32 s82, s48, s49\n" \
"s_cmp_lt_u32 s48, s50\n" \
"s_cselect_b32 s82, s82, s48\n" \
"s_waitcnt vmcnt(8) lgkmcnt(0)\n" \
"s_barrier\n" \
"v_mfma_f32_16x16x32_bf16 v[68:71], a[200:203], a[192:195], v[68:71]\n" \
"ds_read_b64_tr_b16 a[32:33], v194 offset:1024\n" \
"ds_read_b64_tr_b16 a[34:35], v194 offset:4096\n" \
"v_mfma_f32_16x16x32_bf16 v[68:71], a[204:207], a[196:199], v[68:71]\n" \
"s_add_u32 s81, s82, s46\n" \
"ds_read_b64_tr_b16 a[36:37], v194 offset:13312\n" \
"ds_read_b64_tr_b16 a[38:39], v194 offset:16384\n" \
"v_mfma_f32_16x16x32_bf16 v[60:63], a[200:203], a[24:27], v[60:63]\n" \
"s_add_u32 m0, s86, s79\n" \
"ds_read_b64_tr_b16 a[40:41], v194 offset:0\n" \
"v_mov_b32 v186, 0xffff0000\n" \
"v_mfma_f32_16x16x32_bf16 v[60:63], a[204:207], a[28:31], v[60:63]\n" \
"buffer_load_dwordx4 v234, s[56:59], s81 offen offset:0 sc1 lds\n" \
"v_mfma_f32_16x16x32_bf16 v[52:55], a[200:203], a[16:19], v[52:55]\n" \
"ds_read_b64_tr_b16 a[42:43], v194 offset:3072\n" \
"v_subrev_u32 v144, 0, v238\n" \
"v_mfma_f32_16x16x32_bf16 v[52:55], a[204:207], a[20:23], v[52:55]\n" \
"s_lshl_b32 s82, s82, 5\n" \
"ds_read_b64_tr_b16 a[44:45], v194 offset:12288\n" \
"v_subrev_u32 v184, 1, v238\n" \
"v_mfma_f32_16x16x32_bf16 v[44:47], a[200:203], a[8:11], v[44:47]\n" \
"s_add_u32 s82, s82, s47\n" \
"ds_read_b64_tr_b16 a[46:47], v194 offset:15360\n" \
"v_lshrrev_b32 v144, 16, v144\n" \
"v_mfma_f32_16x16x32_bf16 v[44:47], a[204:207], a[12:15], v[44:47]\n" \
"buffer_load_dwordx4 v234, s[56:59], s81 offen offset:1024 sc1 lds\n" \
"v_mfma_f32_16x16x32_bf16 v[72:75], a[0:3], a[208:211], v[72:75]\n" \
"ds_read_b64_tr_b16 a[48:49], v195 offset:0\n" \
"v_and_or_b32 v184, v184, v186, v144\n" \
"v_mfma_f32_16x16x32_bf16 v[72:75], a[4:7], a[212:215], v[72:75]\n" \
"s_add_u32 s48, s48, 0x1000\n" \
"ds_read_b128 a[0:3], v207 offset:45056\n" \
"v_subrev_u32 v144, 2, v238\n" \
"v_mfma_f32_16x16x32_bf16 v[80:83], a[8:11], a[208:211], v[80:83]\n" \
"s_sub_u32 s86, s86, 32768\n" \
"s_cselect_b32 s86, 131072, s86\n" \
"ds_read_b128 a[4:7], v208 offset:45056\n" \
"v_mfma_f32_16x16x32_bf16 v[80:83], a[12:15], a[212:215], v[80:83]\n" \
"buffer_load_dwordx4 v234, s[56:59], s81 offen offset:2048 sc1 lds\n" \
"v_mfma_f32_16x16x32_bf16 v[84:87], a[8:11], a[216:219], v[84:87]\n" \
"ds_read_b64_tr_b16 a[50:51], v195 offset:3072\n" \
"v_subrev_u32 v185, 3, v238\n" \
"v_mfma_f32_16x16x32_bf16 v[84:87], a[12:15], a[220:223], v[84:87]\n" \
"ds_read_b128 a[8:11], v205 offset:45056\n" \
"v_lshrrev_b32 v144, 16, v144\n" \
"v_mfma_f32_16x16x32_bf16 v[92:95], a[16:19], a[216:219], v[92:95]\n" \
"ds_read_b128 a[12:15], v206 offset:45056\n" \
"v_and_or_b32 v185, v185, v186, v144\n" \
"v_mfma_f32_16x16x32_bf16 v[92:95], a[20:23], a[220:223], v[92:95]\n" \
"buffer_load_dwordx4 v234, s[56:59], s81 offen offset:3072 sc1 lds\n" \
"v_mfma_f32_16x16x32_bf16 v[88:91], a[16:19], a[208:211], v[88:91]\n" \
"ds_read_b64_tr_b16 a[52:53], v195 offset:12288\n" \
"v_mov_b32 v144, 0x3e8293ee\n" \
"v_mfma_f32_16x16x32_bf16 v[88:91], a[20:23], a[212:215], v[88:91]\n" \
"s_add_u32 s80, s79, 117760\n" \
"ds_read_b128 a[16:19], v205 offset:49152\n" \
"v_add_u32 v223, s80, v234\n" \
"v_mfma_f32_16x16x32_bf16 v[96:99], a[24:27], a[208:211], v[96:99]\n" \
"s_add_u32 m0, m0, 0x4000\n" \
"ds_read_b128 a[20:23], v206 offset:49152\n" \
"v_mfma_f32_16x16x32_bf16 v[96:99], a[28:31], a[212:215], v[96:99]\n" \
"buffer_load_dwordx4 v216, s[36:39], s82 offen offset:0 lds\n" \
"v_mfma_f32_16x16x32_bf16 v[100:103], a[24:27], a[216:219], v[100:103]\n" \
"s_cmp_lg_u32 s78, 0\n" \
"s_cselect_b64 s[76:77], -1, 0\n" \
"ds_read_b64_tr_b16 a[54:55], v195 offset:15360\n" \
"v_mfma_f32_16x16x32_bf16 v[100:103], a[28:31], a[220:223], v[100:103]\n" \
"ds_read_b128 a[24:27], v205 offset:53248\n" \
"v_mfma_f32_16x16x32_bf16 v[108:111], a[192:195], a[216:219], v[108:111]\n" \
"ds_read_b128 a[28:31], v206 offset:53248\n" \
"v_mfma_f32_16x16x32_bf16 v[108:111], a[196:199], a[220:223], v[108:111]\n" \
"buffer_load_dwordx4 v217, s[36:39], s82 offen offset:1024 lds\n" \
"v_mfma_f32_16x16x32_bf16 v[104:107], a[192:195], a[208:211], v[104:107]\n" \
"v_mfma_f32_16x16x32_bf16 v[104:107], a[196:199], a[212:215], v[104:107]\n" \
"s_waitcnt lgkmcnt(2)\n" \
"v_mfma_f32_16x16x32_bf16 v[32:35], a[32:35], a[0:3], v[32:35]\n" \
"ds_read_b128 a[192:195], v205 offset:57344\n" \
"v_mfma_f32_16x16x32_bf16 v[32:35], a[36:39], a[4:7], v[32:35]\n" \
"buffer_load_dwordx4 v218, s[36:39], s82 offen offset:2048 lds\n" \
"v_mfma_f32_16x16x32_bf16 v[40:43], a[32:35], a[8:11], v[40:43]\n" \
"ds_read_b128 a[196:199], v206 offset:57344\n" \
"v_mfma_f32_16x16x32_bf16 v[40:43], a[36:39], a[12:15], v[40:43]\n" \
"ds_read_b64_tr_b16 a[200:201], v195 offset:1024\n" \
"v_mfma_f32_16x16x32_bf16 v[0:3], a[40:43], a[8:11], v[0:3]\n" \
"ds_read_b64_tr_b16 a[202:203], v195 offset:4096\n" \
"v_mfma_f32_16x16x32_bf16 v[0:3], a[44:47], a[12:15], v[0:3]\n" \
"buffer_load_dwordx4 v219, s[36:39], s82 offen offset:3072 lds\n" \
"v_mfma_f32_16x16x32_bf16 v[4:7], a[48:51], a[8:11], v[4:7]\n" \
"s_xor_b32 s82, s48, s49\n" \
"s_cmp_lt_u32 s48, s50\n" \
"s_cselect_b32 s82, s82, s48\n" \
"ds_read_b64_tr_b16 a[204:205], v195 offset:13312\n" \
"v_mfma_f32_16x16x32_bf16 v[4:7], a[52:55], a[12:15], v[4:7]\n" \
"s_add_u32 s81, s82, s46\n" \
"ds_read_b64_tr_b16 a[206:207], v195 offset:16384\n" \
"v_mfma_f32_16x16x32_bf16 v[12:15], a[48:51], a[16:19], v[12:15]\n" \
"s_add_u32 m0, s86, s79\n" \
"ds_read_b64_tr_b16 a[208:209], v194 offset:2048\n" \
"v_mfma_f32_16x16x32_bf16 v[12:15], a[52:55], a[20:23], v[12:15]\n" \
"buffer_load_dwordx4 v234, s[56:59], s81 offen offset:0 sc1 lds\n" \
"v_mfma_f32_16x16x32_bf16 v[8:11], a[40:43], a[16:19], v[8:11]\n" \
"ds_read_b64_tr_b16 a[210:211], v194 offset:5120\n" \
"v_mfma_f32_16x16x32_bf16 v[8:11], a[44:47], a[20:23], v[8:11]\n" \
"s_lshl_b32 s82, s82, 5\n" \
"ds_read_b64_tr_b16 a[212:213], v194 offset:14336\n" \
"v_mfma_f32_16x16x32_bf16 v[48:51], a[32:35], a[16:19], v[48:51]\n" \
"s_waitcnt lgkmcnt(3)\n" \
"s_add_u32 s82, s82, s47\n" \
"ds_read_b64_tr_b16 a[214:215], v194 offset:17408\n" \
"v_mfma_f32_16x16x32_bf16 v[48:51], a[36:39], a[20:23], v[48:51]\n" \
"buffer_load_dwordx4 v234, s[56:59], s81 offen offset:1024 sc1 lds\n" \
"v_mfma_f32_16x16x32_bf16 v[56:59], a[32:35], a[24:27], v[56:59]\n" \
"ds_read_b64_tr_b16 a[216:217], v195 offset:2048\n" \
"v_mfma_f32_16x16x32_bf16 v[56:59], a[36:39], a[28:31], v[56:59]\n" \
"s_add_u32 s48, s48, 0x1000\n" \
"ds_read_b64_tr_b16 a[218:219], v195 offset:5120\n" \
"v_mfma_f32_16x16x32_bf16 v[16:19], a[40:43], a[24:27], v[16:19]\n" \
"s_sub_u32 s86, s86, 32768\n" \
"s_cselect_b32 s86, 131072, s86\n" \
"ds_read_b64_tr_b16 a[220:221], v195 offset:14336\n" \
"v_mfma_f32_16x16x32_bf16 v[16:19], a[44:47], a[28:31], v[16:19]\n" \
"buffer_load_dwordx4 v234, s[56:59], s81 offen offset:2048 sc1 lds\n" \
"v_mfma_f32_16x16x32_bf16 v[20:23], a[48:51], a[24:27], v[20:23]\n" \
"ds_read_b64_tr_b16 a[222:223], v195 offset:17408\n" \
"v_mfma_f32_16x16x32_bf16 v[20:23], a[52:55], a[28:31], v[20:23]\n" \
"v_mfma_f32_16x16x32_bf16 v[28:31], a[48:51], a[192:195], v[28:31]\n" \
"v_mfma_f32_16x16x32_bf16 v[28:31], a[52:55], a[196:199], v[28:31]\n" \
"buffer_load_dwordx4 v234, s[56:59], s81 offen offset:3072 sc1 lds\n" \
"v_mfma_f32_16x16x32_bf16 v[24:27], a[40:43], a[192:195], v[24:27]\n" \
"v_mfma_f32_16x16x32_bf16 v[24:27], a[44:47], a[196:199], v[24:27]\n" \
"v_mfma_f32_16x16x32_bf16 v[64:67], a[32:35], a[192:195], v[64:67]\n" \
"s_add_u32 m0, m0, 0x4000\n" \
"v_mfma_f32_16x16x32_bf16 v[64:67], a[36:39], a[196:199], v[64:67]\n" \
"buffer_load_dwordx4 v216, s[36:39], s82 offen offset:0 lds\n" \
"s_waitcnt vmcnt(13) lgkmcnt(0)\n" \
"s_barrier\n" \
"v_mfma_f32_16x16x32_bf16 v[44:47], a[200:203], a[8:11], v[44:47]\n" \
"ds_read_b64_tr_b16 a[96:97], v214 offset:16384\n" \
"v_mfma_f32_16x16x32_bf16 v[44:47], a[204:207], a[12:15], v[44:47]\n" \
"ds_read_b64_tr_b16 a[98:99], v214 offset:18432\n" \
"v_mfma_f32_16x16x32_bf16 v[52:55], a[200:203], a[16:19], v[52:55]\n" \
"ds_read_b64_tr_b16 a[100:101], v214 offset:24576\n" \
"v_mfma_f32_16x16x32_bf16 v[52:55], a[204:207], a[20:23], v[52:55]\n" \
"buffer_load_dwordx4 v217, s[36:39], s82 offen offset:1024 lds\n" \
"v_mfma_f32_16x16x32_bf16 v[60:63], a[200:203], a[24:27], v[60:63]\n" \
"ds_read_b64_tr_b16 a[102:103], v214 offset:26624\n" \
"v_cvt_pk_bf16_f32 v112, v0, v1\n" \
"v_mfma_f32_16x16x32_bf16 v[60:63], a[204:207], a[28:31], v[60:63]\n" \
"ds_read_b128 a[64:67], v213 offset:0\n" \
"v_cvt_pk_bf16_f32 v113, v2, v3\n" \
"v_mfma_f32_16x16x32_bf16 v[68:71], a[200:203], a[192:195], v[68:71]\n" \
"ds_read_b128 a[68:71], v213 offset:2048\n" \
"v_cvt_pk_bf16_f32 v114, v4, v5\n" \
"v_mfma_f32_16x16x32_bf16 v[68:71], a[204:207], a[196:199], v[68:71]\n" \
"buffer_load_dwordx4 v218, s[36:39], s82 offen offset:2048 lds\n" \
"v_mfma_f32_16x16x32_bf16 v[72:75], a[0:3], a[208:211], v[72:75]\n" \
"ds_read_b64_tr_b16 a[104:105], v215 offset:16384\n" \
"v_cvt_pk_bf16_f32 v115, v6, v7\n" \
"v_mfma_f32_16x16x32_bf16 v[72:75], a[4:7], a[212:215], v[72:75]\n" \
"ds_read_b64_tr_b16 a[106:107], v215 offset:18432\n" \
"v_cvt_pk_bf16_f32 v116, v8, v9\n" \
"v_mfma_f32_16x16x32_bf16 v[80:83], a[8:11], a[208:211], v[80:83]\n" \
"ds_read_b64_tr_b16 a[108:109], v215 offset:24576\n" \
"v_cvt_pk_bf16_f32 v117, v10, v11\n" \
"v_mfma_f32_16x16x32_bf16 v[80:83], a[12:15], a[212:215], v[80:83]\n" \
"buffer_load_dwordx4 v219, s[36:39], s82 offen offset:3072 lds\n" \
"v_mfma_f32_16x16x32_bf16 v[84:87], a[8:11], a[216:219], v[84:87]\n" \
"s_xor_b32 s82, s48, s49\n" \
"s_cmp_lt_u32 s48, s50\n" \
"Lpg9656_%=:\n" \
"Lst1x3_%=:\n" \
"s_cbranch_execz Lpg11512_%=\n" \
"s_cselect_b32 s82, s82, s48\n" \
"ds_read_b64_tr_b16 a[110:111], v215 offset:26624\n" \
"v_mfma_f32_16x16x32_bf16 v[84:87], a[12:15], a[220:223], v[84:87]\n" \
"s_add_u32 s81, s82, s46\n" \
"ds_read_b128 a[72:75], v213 offset:256\n" \
"v_cvt_pk_bf16_f32 v118, v12, v13\n" \
"v_mfma_f32_16x16x32_bf16 v[92:95], a[16:19], a[216:219], v[92:95]\n" \
"s_add_u32 m0, s86, s79\n" \
"ds_read_b128 a[76:79], v213 offset:2304\n" \
"v_cvt_pk_bf16_f32 v119, v14, v15\n" \
"v_mfma_f32_16x16x32_bf16 v[92:95], a[20:23], a[220:223], v[92:95]\n" \
"buffer_load_dwordx4 v234, s[56:59], s81 offen offset:0 sc1 lds\n" \
"v_mfma_f32_16x16x32_bf16 v[88:91], a[16:19], a[208:211], v[88:91]\n" \
"ds_read_b128 a[80:83], v213 offset:4096\n" \
"v_cvt_pk_bf16_f32 v120, v16, v17\n" \
"v_mfma_f32_16x16x32_bf16 v[88:91], a[20:23], a[212:215], v[88:91]\n" \
"s_lshl_b32 s82, s82, 5\n" \
"ds_read_b128 a[84:87], v213 offset:6144\n" \
"v_cvt_pk_bf16_f32 v121, v18, v19\n" \
"v_mfma_f32_16x16x32_bf16 v[96:99], a[24:27], a[208:211], v[96:99]\n" \
"s_add_u32 s82, s82, s47\n" \
"v_cvt_pk_bf16_f32 v122, v20, v21\n" \
"v_cvt_pk_bf16_f32 v123, v22, v23\n" \
"v_mfma_f32_16x16x32_bf16 v[96:99], a[28:31], a[212:215], v[96:99]\n" \
"buffer_load_dwordx4 v234, s[56:59], s81 offen offset:1024 sc1 lds\n" \
"v_mfma_f32_16x16x32_bf16 v[100:103], a[24:27], a[216:219], v[100:103]\n" \
"v_cvt_pk_bf16_f32 v124, v24, v25\n" \
"v_cvt_pk_bf16_f32 v125, v26, v27\n" \
"v_mfma_f32_16x16x32_bf16 v[100:103], a[28:31], a[220:223], v[100:103]\n" \
"s_add_u32 s48, s48, 0x1000\n" \
"v_cvt_pk_bf16_f32 v126, v28, v29\n" \
"v_cvt_pk_bf16_f32 v127, v30, v31\n" \
"v_mfma_f32_16x16x32_bf16 v[108:111], a[192:195], a[216:219], v[108:111]\n" \
"s_sub_u32 s86, s86, 32768\n" \
"s_cselect_b32 s86, 131072, s86\n" \
"v_cvt_pk_bf16_f32 v128, v32, v33\n" \
"v_mfma_f32_16x16x32_bf16 v[108:111], a[196:199], a[220:223], v[108:111]\n" \
"buffer_load_dwordx4 v234, s[56:59], s81 offen offset:2048 sc1 lds\n" \
"v_mfma_f32_16x16x32_bf16 v[104:107], a[192:195], a[208:211], v[104:107]\n" \
"v_cvt_pk_bf16_f32 v129, v34, v35\n" \
"v_xor_b32 v145, 0x2000, v223\n" \
"v_mfma_f32_16x16x32_bf16 v[104:107], a[196:199], a[212:215], v[104:107]\n" \
"s_waitcnt lgkmcnt(0)\n" \
"v_cvt_pk_bf16_f32 v192, v72, v73\n" \
"v_cvt_pk_bf16_f32 v193, v74, v75\n" \
"ds_write_b64 v145, v[128:129]\n" \
"s_nop 0\n" \
"ds_write_b64 v145, v[192:193] offset:8\n" \
"v_cvt_pk_bf16_f32 v132, v40, v41\n" \
"v_cvt_pk_bf16_f32 v133, v42, v43\n" \
"v_cvt_pk_bf16_f32 v134, v44, v45\n" \
"v_cvt_pk_bf16_f32 v135, v46, v47\n" \
"v_cvt_pk_bf16_f32 v136, v48, v49\n" \
"v_cvt_pk_bf16_f32 v137, v50, v51\n" \
"v_cvt_pk_bf16_f32 v138, v52, v53\n" \
"v_cvt_pk_bf16_f32 v139, v54, v55\n" \
"v_cvt_pk_bf16_f32 v140, v56, v57\n" \
"v_cvt_pk_bf16_f32 v141, v58, v59\n" \
"v_cvt_pk_bf16_f32 v142, v60, v61\n" \
"v_cvt_pk_bf16_f32 v143, v62, v63\n" \
"v_mfma_f32_16x16x32_bf16 v[0:3], a[96:99], a[64:67], 0\n" \
"ds_read_b128 a[88:91], v213 offset:4352\n" \
"v_mfma_f32_16x16x32_bf16 v[0:3], a[100:103], a[68:71], v[0:3]\n" \
"ds_read_b128 a[92:95], v213 offset:6400\n" \
"v_mfma_f32_16x16x32_bf16 v[4:7], a[104:107], a[64:67], 0\n" \
"ds_read_b64_tr_b16 a[112:113], v214 offset:16896\n" \
"v_mfma_f32_16x16x32_bf16 v[4:7], a[108:111], a[68:71], v[4:7]\n" \
"ds_read_b64_tr_b16 a[114:115], v214 offset:18944\n" \
"v_mfma_f32_16x16x32_bf16 v[20:23], a[104:107], a[72:75], 0\n" \
"buffer_load_dwordx4 v234, s[56:59], s81 offen offset:3072 sc1 lds\n" \
"v_mfma_f32_16x16x32_bf16 v[20:23], a[108:111], a[76:79], v[20:23]\n" \
"ds_read_b64_tr_b16 a[116:117], v214 offset:25088\n" \
"v_cvt_pk_bf16_f32 v64, v64, v65\n" \
"v_cvt_pk_bf16_f32 v65, v66, v67\n" \
"v_cvt_pk_bf16_f32 v66, v68, v69\n" \
"v_mfma_f32_16x16x32_bf16 v[16:19], a[96:99], a[72:75], 0\n" \
"ds_read_b64_tr_b16 a[118:119], v214 offset:27136\n" \
"v_cvt_pk_bf16_f32 v67, v70, v71\n" \
"v_mfma_f32_16x16x32_bf16 v[16:19], a[100:103], a[76:79], v[16:19]\n" \
"s_add_u32 m0, m0, 0x4000\n" \
"ds_read_b64_tr_b16 a[120:121], v215 offset:16896\n" \
"v_cvt_pk_bf16_f32 v194, v80, v81\n" \
"v_cvt_pk_bf16_f32 v195, v82, v83\n" \
"v_cvt_pk_bf16_f32 v74, v84, v85\n" \
"v_cvt_pk_bf16_f32 v75, v86, v87\n" \
"v_mfma_f32_16x16x32_bf16 v[32:35], a[96:99], a[80:83], 0\n" \
"buffer_load_dwordx4 v216, s[36:39], s82 offen offset:0 lds\n" \
"v_mfma_f32_16x16x32_bf16 v[32:35], a[100:103], a[84:87], v[32:35]\n" \
"s_sub_u32 s85, s85, 32768\n" \
"s_cselect_b32 s85, 131072, s85\n" \
"ds_read_b64_tr_b16 a[122:123], v215 offset:18944\n" \
"v_cvt_pk_bf16_f32 v196, v88, v89\n" \
"v_cvt_pk_bf16_f32 v197, v90, v91\n" \
"v_mfma_f32_16x16x32_bf16 v[176:179], v[64:67], v[124:127], v[152:155]\n" \
"v_mfma_f32_16x16x32_bf16 v[172:175], v[140:143], v[124:127], v[156:159]\n" \
"v_mfma_f32_16x16x32_bf16 v[168:171], v[140:143], v[120:123], v[152:155]\n" \
"v_mfma_f32_16x16x32_bf16 v[164:167], v[136:139], v[120:123], v[156:159]\n" \
"v_mfma_f32_16x16x32_bf16 v[160:163], v[136:139], v[116:119], v[152:155]\n" \
"v_mfma_f32_16x16x32_bf16 v[156:159], v[132:135], v[116:119], v[156:159]\n" \
"v_mfma_f32_16x16x32_bf16 v[152:155], v[132:135], v[112:115], v[152:155]\n" \
"v_mfma_f32_16x16x32_bf16 v[36:39], a[104:107], a[80:83], 0\n" \
"ds_read_b64_tr_b16 a[124:125], v215 offset:25088\n" \
"v_add_u32 v213, s85, v211\n" \
"v_mfma_f32_16x16x32_bf16 v[36:39], a[108:111], a[84:87], v[36:39]\n" \
"s_waitcnt lgkmcnt(3)\n" \
"ds_read_b64_tr_b16 a[126:127], v215 offset:27136\n" \
"v_mfma_f32_16x16x32_bf16 v[52:55], a[104:107], a[88:91], 0\n" \
"buffer_load_dwordx4 v217, s[36:39], s82 offen offset:1024 lds\n" \
"v_mfma_f32_16x16x32_bf16 v[52:55], a[108:111], a[92:95], v[52:55]\n" \
"v_add_u32 v214, s85, v212\n" \
"v_xor_b32 v215, 32, v214\n" \
"v_mfma_f32_16x16x32_bf16 v[48:51], a[96:99], a[88:91], 0\n" \
"buffer_load_dwordx4 v218, s[36:39], s82 offen offset:2048 lds\n" \
"v_mfma_f32_16x16x32_bf16 v[48:51], a[100:103], a[92:95], v[48:51]\n" \
"s_waitcnt vmcnt(15) lgkmcnt(0)\n" \
"s_barrier\n" \
"v_mfma_f32_16x16x32_bf16 v[8:11], a[112:115], a[64:67], 0\n" \
"ds_read_b64 v[130:131], v223\n" \
"ds_read_b64_tr_b16 a[96:97], v214 offset:16384\n" \
"ds_read_b64_tr_b16 a[98:99], v214 offset:18432\n" \
"v_mfma_f32_16x16x32_bf16 v[8:11], a[116:119], a[68:71], v[8:11]\n" \
"ds_read_b64 v[72:73], v223 offset:8\n" \
"ds_read_b64_tr_b16 a[100:101], v214 offset:24576\n" \
"ds_read_b64_tr_b16 a[102:103], v214 offset:26624\n" \
"v_mfma_f32_16x16x32_bf16 v[12:15], a[120:123], a[64:67], 0\n" \
"buffer_load_dwordx4 v219, s[36:39], s82 offen offset:3072 lds\n" \
"v_mfma_f32_16x16x32_bf16 v[12:15], a[124:127], a[68:71], v[12:15]\n" \
"s_xor_b32 s82, s48, s49\n" \
"s_cmp_lt_u32 s48, s50\n" \
"s_cselect_b32 s82, s82, s48\n" \
"ds_read_b128 a[64:67], v213 offset:0\n" \
"v_mfma_f32_16x16x32_bf16 v[28:31], a[120:123], a[72:75], 0\n" \
"s_add_u32 s81, s82, s46\n" \
"ds_read_b128 a[68:71], v213 offset:2048\n" \
"v_mfma_f32_16x16x32_bf16 v[28:31], a[124:127], a[76:79], v[28:31]\n" \
"s_add_u32 m0, s86, s79\n" \
"ds_read_b64_tr_b16 a[104:105], v215 offset:16384\n" \
"ds_read_b64_tr_b16 a[106:107], v215 offset:18432\n" \
"v_mfma_f32_16x16x32_bf16 v[24:27], a[112:115], a[72:75], 0\n" \
"buffer_load_dwordx4 v234, s[56:59], s81 offen offset:0 sc1 lds\n" \
"v_mfma_f32_16x16x32_bf16 v[24:27], a[116:119], a[76:79], v[24:27]\n" \
"s_waitcnt lgkmcnt(4)\n" \
"ds_read_b128 a[72:75], v213 offset:256\n" \
"v_mfma_f32_16x16x32_bf16 v[40:43], a[112:115], a[80:83], 0\n" \
"s_lshl_b32 s82, s82, 5\n" \
"ds_read_b128 a[76:79], v213 offset:2304\n" \
"v_mfma_f32_16x16x32_bf16 v[148:151], v[128:131], v[112:115], v[148:151]\n" \
"v_max_f32 v172, v172, v176\n" \
"v_max_f32 v173, v173, v177\n" \
"v_max_f32 v174, v174, v178\n" \
"v_max_f32 v175, v175, v179\n" \
"v_max_f32 v164, v164, v168\n" \
"v_max_f32 v165, v165, v169\n" \
"v_max_f32 v166, v166, v170\n" \
"v_max_f32 v167, v167, v171\n" \
"v_max_f32 v156, v156, v160\n" \
"v_max_f32 v157, v157, v161\n" \
"v_max_f32 v158, v158, v162\n" \
"v_max_f32 v159, v159, v163\n" \
"v_max_f32 v148, v148, v152\n" \
"v_max_f32 v149, v149, v153\n" \
"v_max_f32 v150, v150, v154\n" \
"v_max_f32 v151, v151, v155\n" \
"v_max3_f32 v183, v172, v173, v174\n" \
"v_max3_f32 v182, v164, v165, v166\n" \
"v_max3_f32 v181, v156, v157, v158\n" \
"v_mfma_f32_16x16x32_bf16 v[40:43], a[116:119], a[84:87], v[40:43]\n" \
"s_add_u32 s82, s82, s47\n" \
"ds_read_b64_tr_b16 a[108:109], v215 offset:24576\n" \
"ds_read_b64_tr_b16 a[110:111], v215 offset:26624\n" \
"v_max3_f32 v180, v148, v149, v150\n" \
"v_mfma_f32_16x16x32_bf16 v[44:47], a[120:123], a[80:83], 0\n" \
"buffer_load_dwordx4 v234, s[56:59], s81 offen offset:1024 sc1 lds\n" \
"v_mfma_f32_16x16x32_bf16 v[44:47], a[124:127], a[84:87], v[44:47]\n" \
"s_add_u32 s48, s48, 0x1000\n" \
"s_sub_u32 s86, s86, 32768\n" \
"s_cselect_b32 s86, 131072, s86\n" \
"ds_read_b128 a[80:83], v213 offset:4096\n" \
"v_max_f32 v183, v183, v175\n" \
"v_max_f32 v182, v182, v167\n" \
"v_max_f32 v181, v181, v159\n" \
"v_mfma_f32_16x16x32_bf16 v[60:63], a[120:123], a[88:91], 0\n" \
"s_waitcnt lgkmcnt(4)\n" \
"buffer_load_dwordx4 v234, s[56:59], s81 offen offset:2048 sc1 lds\n" \
"v_max_f32 v180, v180, v151\n" \
"v_mfma_f32_16x16x32_bf16 v[60:63], a[124:127], a[92:95], v[60:63]\n" \
"ds_read_b128 a[84:87], v213 offset:6144\n" \
"v_cvt_pk_bf16_f32 v76, v92, v93\n" \
"v_cvt_pk_bf16_f32 v77, v94, v95\n" \
"v_mfma_f32_16x16x32_bf16 v[56:59], a[112:115], a[88:91], 0\n" \
"buffer_load_dwordx4 v234, s[56:59], s81 offen offset:3072 sc1 lds\n" \
"v_mfma_f32_16x16x32_bf16 v[56:59], a[116:119], a[92:95], v[56:59]\n" \
"s_getpc_b64 s[44:45]\n" \
"Lop_%=:\n" \
"s_add_u32 s44, s44, Lor_%=-Lop_%=\n" \
"s_addc_u32 s45, s45, 0\n" \
"v_permlane32_swap_b32 v180, v181\n" \
"v_permlane32_swap_b32 v182, v183\n" \
"v_max_f32 v180, v180, v181\n" \
"v_mfma_f32_16x16x32_bf16 v[0:3], a[96:99], a[64:67], v[0:3]\n" \
"ds_read_b128 a[88:91], v213 offset:4352\n" \
"v_max_f32 v182, v182, v183\n" \
"v_mfma_f32_16x16x32_bf16 v[0:3], a[100:103], a[68:71], v[0:3]\n" \
"s_waitcnt lgkmcnt(2)\n" \
"ds_read_b128 a[92:95], v213 offset:6400\n" \
"v_cvt_pk_bf16_f32 v198, v96, v97\n" \
"v_cvt_pk_bf16_f32 v199, v98, v99\n" \
"v_permlane16_swap_b32 v180, v182\n" \
"v_mfma_f32_16x16x32_bf16 v[4:7], a[104:107], a[64:67], v[4:7]\n" \
"ds_read_b64_tr_b16 a[112:113], v214 offset:16896\n" \
"v_max_f32 v180, v180, v182\n" \
"v_mfma_f32_16x16x32_bf16 v[4:7], a[108:111], a[68:71], v[4:7]\n" \
"s_add_u32 m0, m0, 0x4000\n" \
"ds_read_b64_tr_b16 a[114:115], v214 offset:18944\n" \
"v_mul_f32 v180, v180, v144\n" \
"v_cvt_pk_bf16_f32 v78, v100, v101\n" \
"v_cvt_pk_bf16_f32 v79, v102, v103\n" \
"v_mov_b32 v182, v180\n" \
"v_mfma_f32_16x16x32_bf16 v[20:23], a[104:107], a[72:75], v[20:23]\n" \
"buffer_load_dwordx4 v216, s[36:39], s82 offen offset:0 lds\n" \
"v_mfma_f32_16x16x32_bf16 v[20:23], a[108:111], a[76:79], v[20:23]\n" \
"ds_read_b64_tr_b16 a[116:117], v214 offset:25088\n" \
"v_cvt_pk_bf16_f32 v200, v104, v105\n" \
"v_cvt_pk_bf16_f32 v201, v106, v107\n" \
"v_permlane16_swap_b32 v180, v182\n" \
"v_mfma_f32_16x16x32_bf16 v[16:19], a[96:99], a[72:75], v[16:19]\n" \
"ds_read_b64_tr_b16 a[118:119], v214 offset:27136\n" \
"v_mov_b32 v181, v180\n" \
"v_mfma_f32_16x16x32_bf16 v[16:19], a[100:103], a[76:79], v[16:19]\n" \
"s_waitcnt lgkmcnt(3)\n" \
"ds_read_b64_tr_b16 a[120:121], v215 offset:16896\n" \
"v_mov_b32 v183, v182\n" \
"v_cvt_pk_bf16_f32 v80, v108, v109\n" \
"v_cvt_pk_bf16_f32 v81, v110, v111\n" \
"v_permlane32_swap_b32 v180, v181\n" \
"v_mfma_f32_16x16x32_bf16 v[32:35], a[96:99], a[80:83], v[32:35]\n" \
"buffer_load_dwordx4 v217, s[36:39], s82 offen offset:1024 lds\n" \
"v_mfma_f32_16x16x32_bf16 v[32:35], a[100:103], a[84:87], v[32:35]\n" \
"s_sub_u32 s85, s85, 32768\n" \
"s_cselect_b32 s85, 131072, s85\n" \
"ds_read_b64_tr_b16 a[122:123], v215 offset:18944\n" \
"v_permlane32_swap_b32 v182, v183\n" \
"v_fma_f32 v172, v172, v144, -v183\n" \
"v_mfma_f32_16x16x32_bf16 v[36:39], a[104:107], a[80:83], v[36:39]\n" \
"ds_read_b64_tr_b16 a[124:125], v215 offset:25088\n" \
"v_add_u32 v213, s85, v211\n" \
"v_mfma_f32_16x16x32_bf16 v[36:39], a[108:111], a[84:87], v[36:39]\n" \
"ds_read_b64_tr_b16 a[126:127], v215 offset:27136\n" \
"v_fma_f32 v164, v164, v144, -v182\n" \
"v_fma_f32 v156, v156, v144, -v181\n" \
"v_fma_f32 v148, v148, v144, -v180\n" \
"v_fma_f32 v173, v173, v144, -v183\n" \
"v_mfma_f32_16x16x32_bf16 v[52:55], a[104:107], a[88:91], v[52:55]\n" \
"buffer_load_dwordx4 v218, s[36:39], s82 offen offset:2048 lds\n" \
"v_mfma_f32_16x16x32_bf16 v[52:55], a[108:111], a[92:95], v[52:55]\n" \
"v_add_u32 v214, s85, v212\n" \
"v_xor_b32 v215, 32, v214\n" \
"v_fma_f32 v165, v165, v144, -v182\n" \
"v_fma_f32 v157, v157, v144, -v181\n" \
"v_mfma_f32_16x16x32_bf16 v[48:51], a[96:99], a[88:91], v[48:51]\n" \
"buffer_load_dwordx4 v219, s[36:39], s82 offen offset:3072 lds\n" \
"v_mfma_f32_16x16x32_bf16 v[48:51], a[100:103], a[92:95], v[48:51]\n" \
"s_xor_b32 s82, s48, s49\n" \
"s_cmp_lt_u32 s48, s50\n" \
"s_cselect_b32 s82, s82, s48\n" \
"v_fma_f32 v149, v149, v144, -v180\n" \
"v_fma_f32 v174, v174, v144, -v183\n" \
"v_fma_f32 v166, v166, v144, -v182\n" \
"s_waitcnt vmcnt(16) lgkmcnt(0)\n" \
"s_barrier\n" \
"v_mfma_f32_16x16x32_bf16 v[8:11], a[112:115], a[64:67], v[8:11]\n" \
"s_add_u32 s81, s82, s46\n" \
"ds_read_b64_tr_b16 a[96:97], v214 offset:16384\n" \
"ds_read_b64_tr_b16 a[98:99], v214 offset:18432\n" \
"v_mfma_f32_16x16x32_bf16 v[8:11], a[116:119], a[68:71], v[8:11]\n" \
"s_add_u32 m0, s86, s79\n" \
"ds_read_b64_tr_b16 a[100:101], v214 offset:24576\n" \
"ds_read_b64_tr_b16 a[102:103], v214 offset:26624\n" \
"v_fma_f32 v158, v158, v144, -v181\n" \
"v_fma_f32 v150, v150, v144, -v180\n" \
"v_mfma_f32_16x16x32_bf16 v[12:15], a[120:123], a[64:67], v[12:15]\n" \
"buffer_load_dwordx4 v234, s[56:59], s81 offen offset:0 sc1 lds\n" \
"v_mfma_f32_16x16x32_bf16 v[12:15], a[124:127], a[68:71], v[12:15]\n" \
"ds_read_b128 a[64:67], v213 offset:0\n" \
"v_fma_f32 v175, v175, v144, -v183\n" \
"v_fma_f32 v167, v167, v144, -v182\n" \
"v_fma_f32 v159, v159, v144, -v181\n" \
"v_fma_f32 v151, v151, v144, -v180\n" \
"v_mfma_f32_16x16x32_bf16 v[28:31], a[120:123], a[72:75], v[28:31]\n" \
"s_lshl_b32 s82, s82, 5\n" \
"ds_read_b128 a[68:71], v213 offset:2048\n" \
"v_mfma_f32_16x16x32_bf16 v[28:31], a[124:127], a[76:79], v[28:31]\n" \
"s_add_u32 s82, s82, s47\n" \
"ds_read_b64_tr_b16 a[104:105], v215 offset:16384\n" \
"ds_read_b64_tr_b16 a[106:107], v215 offset:18432\n" \
"v_exp_f32 v172, v172\n" \
"v_mfma_f32_16x16x32_bf16 v[24:27], a[112:115], a[72:75], v[24:27]\n" \
"buffer_load_dwordx4 v234, s[56:59], s81 offen offset:1024 sc1 lds\n" \
"v_mfma_f32_16x16x32_bf16 v[24:27], a[116:119], a[76:79], v[24:27]\n" \
"ds_read_b128 a[72:75], v213 offset:256\n" \
"v_exp_f32 v164, v164\n" \
"v_exp_f32 v156, v156\n" \
"v_mfma_f32_16x16x32_bf16 v[40:43], a[112:115], a[80:83], v[40:43]\n" \
"s_add_u32 s48, s48, 0x1000\n" \
"ds_read_b128 a[76:79], v213 offset:2304\n" \
"v_mfma_f32_16x16x32_bf16 v[40:43], a[116:119], a[84:87], v[40:43]\n" \
"s_sub_u32 s86, s86, 32768\n" \
"s_cselect_b32 s86, 131072, s86\n" \
"ds_read_b64_tr_b16 a[108:109], v215 offset:24576\n" \
"ds_read_b64_tr_b16 a[110:111], v215 offset:26624\n" \
"v_exp_f32 v148, v148\n" \
"v_mfma_f32_16x16x32_bf16 v[44:47], a[120:123], a[80:83], v[44:47]\n" \
"buffer_load_dwordx4 v234, s[56:59], s81 offen offset:2048 sc1 lds\n" \
"v_mfma_f32_16x16x32_bf16 v[44:47], a[124:127], a[84:87], v[44:47]\n" \
"ds_read_b128 a[80:83], v213 offset:4096\n" \
"v_exp_f32 v173, v173\n" \
"v_exp_f32 v165, v165\n" \
"v_mfma_f32_16x16x32_bf16 v[60:63], a[120:123], a[88:91], v[60:63]\n" \
"s_waitcnt lgkmcnt(4)\n" \
"buffer_load_dwordx4 v234, s[56:59], s81 offen offset:3072 sc1 lds\n" \
"v_mfma_f32_16x16x32_bf16 v[60:63], a[124:127], a[92:95], v[60:63]\n" \
"s_add_u32 m0, m0, 0x4000\n" \
"ds_read_b128 a[84:87], v213 offset:6144\n" \
"v_exp_f32 v157, v157\n" \
"v_mfma_f32_16x16x32_bf16 v[56:59], a[112:115], a[88:91], v[56:59]\n" \
"buffer_load_dwordx4 v216, s[36:39], s82 offen offset:0 lds\n" \
"v_mfma_f32_16x16x32_bf16 v[56:59], a[116:119], a[92:95], v[56:59]\n" \
"v_exp_f32 v149, v149\n" \
"v_exp_f32 v174, v174\n" \
"v_exp_f32 v166, v166\n" \
"v_mfma_f32_16x16x32_bf16 v[0:3], a[96:99], a[64:67], v[0:3]\n" \
"ds_read_b128 a[88:91], v213 offset:4352\n" \
"v_mfma_f32_16x16x32_bf16 v[0:3], a[100:103], a[68:71], v[0:3]\n" \
"s_waitcnt lgkmcnt(2)\n" \
"ds_read_b128 a[92:95], v213 offset:6400\n" \
"v_exp_f32 v158, v158\n" \
"v_mfma_f32_16x16x32_bf16 v[4:7], a[104:107], a[64:67], v[4:7]\n" \
"ds_read_b64_tr_b16 a[112:113], v214 offset:16896\n" \
"v_mfma_f32_16x16x32_bf16 v[4:7], a[108:111], a[68:71], v[4:7]\n" \
"ds_read_b64_tr_b16 a[114:115], v214 offset:18944\n" \
"v_exp_f32 v150, v150\n" \
"v_exp_f32 v175, v175\n" \
"v_mfma_f32_16x16x32_bf16 v[20:23], a[104:107], a[72:75], v[20:23]\n" \
"buffer_load_dwordx4 v217, s[36:39], s82 offen offset:1024 lds\n" \
"v_mfma_f32_16x16x32_bf16 v[20:23], a[108:111], a[76:79], v[20:23]\n" \
"ds_read_b64_tr_b16 a[116:117], v214 offset:25088\n" \
"v_exp_f32 v167, v167\n" \
"v_exp_f32 v159, v159\n" \
"v_mfma_f32_16x16x32_bf16 v[16:19], a[96:99], a[72:75], v[16:19]\n" \
"ds_read_b64_tr_b16 a[118:119], v214 offset:27136\n" \
"v_mfma_f32_16x16x32_bf16 v[16:19], a[100:103], a[76:79], v[16:19]\n" \
"s_waitcnt lgkmcnt(3)\n" \
"ds_read_b64_tr_b16 a[120:121], v215 offset:16896\n" \
"v_exp_f32 v151, v151\n" \
"v_add_f32 v191, v172, v173\n" \
"v_add_f32 v190, v164, v165\n" \
"v_mfma_f32_16x16x32_bf16 v[32:35], a[96:99], a[80:83], v[32:35]\n" \
"buffer_load_dwordx4 v218, s[36:39], s82 offen offset:2048 lds\n" \
"v_mfma_f32_16x16x32_bf16 v[32:35], a[100:103], a[84:87], v[32:35]\n" \
"s_sub_u32 s85, s85, 32768\n" \
"s_cselect_b32 s85, 131072, s85\n" \
"ds_read_b64_tr_b16 a[122:123], v215 offset:18944\n" \
"v_add_f32 v189, v156, v157\n" \
"v_add_f32 v188, v148, v149\n" \
"v_mfma_f32_16x16x32_bf16 v[36:39], a[104:107], a[80:83], v[36:39]\n" \
"ds_read_b64_tr_b16 a[124:125], v215 offset:25088\n" \
"v_add_u32 v213, s85, v211\n" \
"v_mfma_f32_16x16x32_bf16 v[36:39], a[108:111], a[84:87], v[36:39]\n" \
"ds_read_b64_tr_b16 a[126:127], v215 offset:27136\n" \
"v_add_f32 v191, v191, v174\n" \
"v_add_f32 v190, v190, v166\n" \
"v_add_f32 v189, v189, v158\n" \
"v_add_f32 v188, v188, v150\n" \
"v_mfma_f32_16x16x32_bf16 v[52:55], a[104:107], a[88:91], v[52:55]\n" \
"buffer_load_dwordx4 v219, s[36:39], s82 offen offset:3072 lds\n" \
"v_mfma_f32_16x16x32_bf16 v[52:55], a[108:111], a[92:95], v[52:55]\n" \
"s_xor_b32 s82, s48, s49\n" \
"s_cmp_lt_u32 s48, s50\n" \
"s_cselect_b32 s82, s82, s48\n" \
"s_add_u32 s81, s82, s46\n" \
"s_add_u32 m0, s86, s79\n" \
"v_add_u32 v214, s85, v212\n" \
"v_xor_b32 v215, 32, v214\n" \
"v_add_f32 v191, v191, v175\n" \
"v_add_f32 v190, v190, v167\n" \
"v_mfma_f32_16x16x32_bf16 v[48:51], a[96:99], a[88:91], v[48:51]\n" \
"buffer_load_dwordx4 v234, s[56:59], s81 offen offset:0 sc1 lds\n" \
"v_mfma_f32_16x16x32_bf16 v[48:51], a[100:103], a[92:95], v[48:51]\n" \
"v_add_f32 v189, v189, v159\n" \
"v_add_f32 v188, v188, v151\n" \
"s_waitcnt vmcnt(17) lgkmcnt(0)\n" \
"s_barrier\n" \
"v_mfma_f32_16x16x32_bf16 v[8:11], a[112:115], a[64:67], v[8:11]\n" \
"s_lshl_b32 s82, s82, 5\n" \
"ds_read_b64_tr_b16 a[96:97], v214 offset:16384\n" \
"ds_read_b64_tr_b16 a[98:99], v214 offset:18432\n" \
"v_mfma_f32_16x16x32_bf16 v[8:11], a[116:119], a[68:71], v[8:11]\n" \
"s_add_u32 s82, s82, s47\n" \
"ds_read_b64_tr_b16 a[100:101], v214 offset:24576\n" \
"ds_read_b64_tr_b16 a[102:103], v214 offset:26624\n" \
"v_permlane32_swap_b32 v188, v189\n" \
"v_permlane32_swap_b32 v190, v191\n" \
"v_add_f32 v188, v188, v189\n" \
"v_mfma_f32_16x16x32_bf16 v[12:15], a[120:123], a[64:67], v[12:15]\n" \
"buffer_load_dwordx4 v234, s[56:59], s81 offen offset:1024 sc1 lds\n" \
"v_mfma_f32_16x16x32_bf16 v[12:15], a[124:127], a[68:71], v[12:15]\n" \
"ds_read_b128 a[64:67], v213 offset:0\n" \
"v_add_f32 v190, v190, v191\n" \
"v_mfma_f32_16x16x32_bf16 v[28:31], a[120:123], a[72:75], v[28:31]\n" \
"s_add_u32 s48, s48, 0x1000\n" \
"ds_read_b128 a[68:71], v213 offset:2048\n" \
"v_mfma_f32_16x16x32_bf16 v[28:31], a[124:127], a[76:79], v[28:31]\n" \
"s_sub_u32 s86, s86, 32768\n" \
"s_cselect_b32 s86, 131072, s86\n" \
"ds_read_b64_tr_b16 a[104:105], v215 offset:16384\n" \
"ds_read_b64_tr_b16 a[106:107], v215 offset:18432\n" \
"v_permlane16_swap_b32 v188, v190\n" \
"v_add_f32 v188, v188, v190\n" \
"v_rcp_f32 v188, v188\n" \
"v_mfma_f32_16x16x32_bf16 v[24:27], a[112:115], a[72:75], v[24:27]\n" \
"buffer_load_dwordx4 v234, s[56:59], s81 offen offset:2048 sc1 lds\n" \
"v_mfma_f32_16x16x32_bf16 v[24:27], a[116:119], a[76:79], v[24:27]\n" \
"ds_read_b128 a[72:75], v213 offset:256\n" \
"v_mov_b32 v190, v188\n" \
"v_mfma_f32_16x16x32_bf16 v[40:43], a[112:115], a[80:83], v[40:43]\n" \
"ds_read_b128 a[76:79], v213 offset:2304\n" \
"v_mfma_f32_16x16x32_bf16 v[40:43], a[116:119], a[84:87], v[40:43]\n" \
"ds_read_b64_tr_b16 a[108:109], v215 offset:24576\n" \
"ds_read_b64_tr_b16 a[110:111], v215 offset:26624\n" \
"v_permlane16_swap_b32 v188, v190\n" \
"v_mov_b32 v189, v188\n" \
"v_mov_b32 v191, v190\n" \
"v_mfma_f32_16x16x32_bf16 v[44:47], a[120:123], a[80:83], v[44:47]\n" \
"buffer_load_dwordx4 v234, s[56:59], s81 offen offset:3072 sc1 lds\n" \
"v_mfma_f32_16x16x32_bf16 v[44:47], a[124:127], a[84:87], v[44:47]\n" \
"s_add_u32 m0, m0, 0x4000\n" \
"ds_read_b128 a[80:83], v213 offset:4096\n" \
"v_permlane32_swap_b32 v188, v189\n" \
"v_permlane32_swap_b32 v190, v191\n" \
"v_mul_f32 v172, v172, v191\n" \
"v_mul_f32 v164, v164, v190\n" \
"v_mul_f32 v156, v156, v189\n" \
"v_mul_f32 v148, v148, v188\n" \
"v_mul_f32 v173, v173, v191\n" \
"v_mfma_f32_16x16x32_bf16 v[60:63], a[120:123], a[88:91], v[60:63]\n" \
"s_waitcnt lgkmcnt(4)\n" \
"buffer_load_dwordx4 v216, s[36:39], s82 offen offset:0 lds\n" \
"v_mul_f32 v165, v165, v190\n" \
"v_mfma_f32_16x16x32_bf16 v[60:63], a[124:127], a[92:95], v[60:63]\n" \
"ds_read_b128 a[84:87], v213 offset:6144\n" \
"v_mul_f32 v157, v157, v189\n" \
"v_mul_f32 v149, v149, v188\n" \
"v_mfma_f32_16x16x32_bf16 v[56:59], a[112:115], a[88:91], v[56:59]\n" \
"buffer_load_dwordx4 v217, s[36:39], s82 offen offset:1024 lds\n" \
"v_mfma_f32_16x16x32_bf16 v[56:59], a[116:119], a[92:95], v[56:59]\n" \
"v_mul_f32 v174, v174, v191\n" \
"v_mul_f32 v166, v166, v190\n" \
"v_mul_f32 v158, v158, v189\n" \
"v_mul_f32 v150, v150, v188\n" \
"v_mul_f32 v175, v175, v191\n" \
"v_mfma_f32_16x16x32_bf16 v[0:3], a[96:99], a[64:67], v[0:3]\n" \
"ds_read_b128 a[88:91], v213 offset:4352\n" \
"v_mul_f32 v167, v167, v190\n" \
"v_mfma_f32_16x16x32_bf16 v[0:3], a[100:103], a[68:71], v[0:3]\n" \
"s_waitcnt lgkmcnt(2)\n" \
"ds_read_b128 a[92:95], v213 offset:6400\n" \
"v_mul_f32 v159, v159, v189\n" \
"v_mul_f32 v151, v151, v188\n" \
"v_cvt_pk_bf16_f32 v178, v172, v173\n" \
"v_mfma_f32_16x16x32_bf16 v[4:7], a[104:107], a[64:67], v[4:7]\n" \
"ds_read_b64_tr_b16 a[112:113], v214 offset:16896\n" \
"v_cvt_pk_bf16_f32 v179, v174, v175\n" \
"v_mfma_f32_16x16x32_bf16 v[4:7], a[108:111], a[68:71], v[4:7]\n" \
"ds_read_b64_tr_b16 a[114:115], v214 offset:18944\n" \
"v_cvt_pk_bf16_f32 v170, v164, v165\n" \
"v_cvt_pk_bf16_f32 v171, v166, v167\n" \
"v_cvt_pk_bf16_f32 v162, v156, v157\n" \
"v_cvt_pk_bf16_f32 v163, v158, v159\n" \
"v_mfma_f32_16x16x32_bf16 v[20:23], a[104:107], a[72:75], v[20:23]\n" \
"buffer_load_dwordx4 v218, s[36:39], s82 offen offset:2048 lds\n" \
"v_mfma_f32_16x16x32_bf16 v[20:23], a[108:111], a[76:79], v[20:23]\n" \
"ds_read_b64_tr_b16 a[116:117], v214 offset:25088\n" \
"v_cvt_pk_bf16_f32 v154, v148, v149\n" \
"v_cvt_pk_bf16_f32 v155, v150, v151\n" \
"v_and_b32 v176, v178, v184\n" \
"v_mfma_f32_16x16x32_bf16 v[16:19], a[96:99], a[72:75], v[16:19]\n" \
"ds_read_b64_tr_b16 a[118:119], v214 offset:27136\n" \
"v_and_b32 v177, v179, v185\n" \
"v_mfma_f32_16x16x32_bf16 v[16:19], a[100:103], a[76:79], v[16:19]\n" \
"s_waitcnt lgkmcnt(3)\n" \
"ds_read_b64_tr_b16 a[120:121], v215 offset:16896\n" \
"v_xor_b32 v178, v178, v176\n" \
"v_xor_b32 v179, v179, v177\n" \
"v_and_b32 v168, v170, v184\n" \
"v_and_b32 v169, v171, v185\n" \
"v_mfma_f32_16x16x32_bf16 v[32:35], a[96:99], a[80:83], v[32:35]\n" \
"buffer_load_dwordx4 v219, s[36:39], s82 offen offset:3072 lds\n" \
"v_mfma_f32_16x16x32_bf16 v[32:35], a[100:103], a[84:87], v[32:35]\n" \
"s_sub_u32 s85, s85, 32768\n" \
"s_cselect_b32 s85, 131072, s85\n" \
"s_xor_b32 s82, s48, s49\n" \
"s_cmp_lt_u32 s48, s50\n" \
"s_cselect_b32 s82, s82, s48\n" \
"ds_read_b64_tr_b16 a[122:123], v215 offset:18944\n" \
"v_xor_b32 v170, v170, v168\n" \
"v_xor_b32 v171, v171, v169\n" \
"v_mfma_f32_16x16x32_bf16 v[36:39], a[104:107], a[80:83], v[36:39]\n" \
"s_add_u32 s81, s82, s46\n" \
"ds_read_b64_tr_b16 a[124:125], v215 offset:25088\n" \
"v_add_u32 v213, s85, v211\n" \
"v_mfma_f32_16x16x32_bf16 v[36:39], a[108:111], a[84:87], v[36:39]\n" \
"s_add_u32 m0, s86, s79\n" \
"ds_read_b64_tr_b16 a[126:127], v215 offset:27136\n" \
"v_and_b32 v160, v162, v184\n" \
"v_and_b32 v161, v163, v185\n" \
"v_xor_b32 v162, v162, v160\n" \
"v_xor_b32 v163, v163, v161\n" \
"v_mfma_f32_16x16x32_bf16 v[52:55], a[104:107], a[88:91], v[52:55]\n" \
"buffer_load_dwordx4 v234, s[56:59], s81 offen offset:0 sc1 lds\n" \
"v_mfma_f32_16x16x32_bf16 v[52:55], a[108:111], a[92:95], v[52:55]\n" \
"s_lshl_b32 s82, s82, 5\n" \
"s_add_u32 s82, s82, s47\n" \
"v_add_u32 v214, s85, v212\n" \
"v_xor_b32 v215, 32, v214\n" \
"v_and_b32 v152, v154, v184\n" \
"v_and_b32 v153, v155, v185\n" \
"v_mfma_f32_16x16x32_bf16 v[48:51], a[96:99], a[88:91], v[48:51]\n" \
"buffer_load_dwordx4 v234, s[56:59], s81 offen offset:1024 sc1 lds\n" \
"v_mfma_f32_16x16x32_bf16 v[48:51], a[100:103], a[92:95], v[48:51]\n" \
"v_xor_b32 v154, v154, v152\n" \
"v_xor_b32 v155, v155, v153\n" \
"s_waitcnt vmcnt(18) lgkmcnt(0)\n" \
"s_barrier\n" \
"v_mfma_f32_16x16x32_bf16 v[8:11], a[112:115], a[64:67], v[8:11]\n" \
"s_add_u32 s48, s48, 0x1000\n" \
"ds_read_b64_tr_b16 a[96:97], v214 offset:16384\n" \
"ds_read_b64_tr_b16 a[98:99], v214 offset:18432\n" \
"v_mfma_f32_16x16x32_bf16 v[136:139], v[198:201], v[176:179], 0\n" \
"v_mfma_f32_16x16x32_bf16 v[140:143], v[78:81], v[176:179], 0\n" \
"v_mfma_f32_16x16x32_bf16 v[128:131], v[196:199], v[168:171], 0\n" \
"v_mfma_f32_16x16x32_bf16 v[132:135], v[76:79], v[168:171], 0\n" \
"v_mfma_f32_16x16x32_bf16 v[120:123], v[194:197], v[160:163], 0\n" \
"v_mfma_f32_16x16x32_bf16 v[124:127], v[74:77], v[160:163], 0\n" \
"v_mfma_f32_16x16x32_bf16 v[112:115], v[192:195], v[152:155], 0\n" \
"v_mfma_f32_16x16x32_bf16 v[116:119], v[72:75], v[152:155], 0\n" \
"v_mfma_f32_16x16x32_bf16 v[8:11], a[116:119], a[68:71], v[8:11]\n" \
"s_sub_u32 s86, s86, 32768\n" \
"s_cselect_b32 s86, 131072, s86\n" \
"ds_read_b64_tr_b16 a[100:101], v214 offset:24576\n" \
"ds_read_b64_tr_b16 a[102:103], v214 offset:26624\n" \
"v_mfma_f32_16x16x32_bf16 v[12:15], a[120:123], a[64:67], v[12:15]\n" \
"buffer_load_dwordx4 v234, s[56:59], s81 offen offset:2048 sc1 lds\n" \
"v_mfma_f32_16x16x32_bf16 v[12:15], a[124:127], a[68:71], v[12:15]\n" \
"ds_read_b128 a[64:67], v213 offset:0\n" \
"v_cvt_pk_bf16_f32 v136, v136, v137\n" \
"v_cvt_pk_bf16_f32 v137, v138, v139\n" \
"v_cvt_pk_bf16_f32 v138, v140, v141\n" \
"v_cvt_pk_bf16_f32 v139, v142, v143\n" \
"v_cndmask_b32 v140, v136, v138, s[76:77]\n" \
"v_mfma_f32_16x16x32_bf16 v[28:31], a[120:123], a[72:75], v[28:31]\n" \
"ds_read_b128 a[68:71], v213 offset:2048\n" \
"v_cndmask_b32 v141, v137, v139, s[76:77]\n" \
"v_mfma_f32_16x16x32_bf16 v[28:31], a[124:127], a[76:79], v[28:31]\n" \
"ds_read_b64_tr_b16 a[104:105], v215 offset:16384\n" \
"ds_read_b64_tr_b16 a[106:107], v215 offset:18432\n" \
"v_cndmask_b32 v142, v138, v136, s[76:77]\n" \
"v_cndmask_b32 v143, v139, v137, s[76:77]\n" \
"v_mfma_f32_16x16x32_bf16 v[24:27], a[112:115], a[72:75], v[24:27]\n" \
"buffer_load_dwordx4 v234, s[56:59], s81 offen offset:3072 sc1 lds\n" \
"v_mfma_f32_16x16x32_bf16 v[24:27], a[116:119], a[76:79], v[24:27]\n" \
"ds_read_b128 a[72:75], v213 offset:256\n" \
"v_cvt_pk_bf16_f32 v128, v128, v129\n" \
"v_cvt_pk_bf16_f32 v129, v130, v131\n" \
"v_cvt_pk_bf16_f32 v130, v132, v133\n" \
"v_cvt_pk_bf16_f32 v131, v134, v135\n" \
"v_mfma_f32_16x16x32_bf16 v[40:43], a[112:115], a[80:83], v[40:43]\n" \
"ds_read_b128 a[76:79], v213 offset:2304\n" \
"v_cndmask_b32 v132, v128, v130, s[76:77]\n" \
"v_mfma_f32_16x16x32_bf16 v[40:43], a[116:119], a[84:87], v[40:43]\n" \
"s_add_u32 m0, m0, 0x4000\n" \
"ds_read_b64_tr_b16 a[108:109], v215 offset:24576\n" \
"ds_read_b64_tr_b16 a[110:111], v215 offset:26624\n" \
"v_cndmask_b32 v133, v129, v131, s[76:77]\n" \
"v_cndmask_b32 v134, v130, v128, s[76:77]\n" \
"v_mfma_f32_16x16x32_bf16 v[44:47], a[120:123], a[80:83], v[44:47]\n" \
"buffer_load_dwordx4 v216, s[36:39], s82 offen offset:0 lds\n" \
"v_mfma_f32_16x16x32_bf16 v[44:47], a[124:127], a[84:87], v[44:47]\n" \
"ds_read_b128 a[80:83], v213 offset:4096\n" \
"v_cndmask_b32 v135, v131, v129, s[76:77]\n" \
"v_cvt_pk_bf16_f32 v120, v120, v121\n" \
"v_cvt_pk_bf16_f32 v121, v122, v123\n" \
"v_cvt_pk_bf16_f32 v122, v124, v125\n" \
"v_mfma_f32_16x16x32_bf16 v[60:63], a[120:123], a[88:91], v[60:63]\n" \
"s_waitcnt lgkmcnt(4)\n" \
"buffer_load_dwordx4 v217, s[36:39], s82 offen offset:1024 lds\n" \
"v_cvt_pk_bf16_f32 v123, v126, v127\n" \
"v_mfma_f32_16x16x32_bf16 v[60:63], a[124:127], a[92:95], v[60:63]\n" \
"ds_read_b128 a[84:87], v213 offset:6144\n" \
"v_cndmask_b32 v124, v120, v122, s[76:77]\n" \
"v_cndmask_b32 v125, v121, v123, s[76:77]\n" \
"Lpg10257_%=:\n" \
"s_cbranch_execz Lcb1_%=\n" \
"v_mfma_f32_16x16x32_bf16 v[56:59], a[112:115], a[88:91], v[56:59]\n" \
"buffer_load_dwordx4 v218, s[36:39], s82 offen offset:2048 lds\n" \
"v_mfma_f32_16x16x32_bf16 v[56:59], a[116:119], a[92:95], v[56:59]\n" \
"v_cndmask_b32 v126, v122, v120, s[76:77]\n" \
"v_cndmask_b32 v127, v123, v121, s[76:77]\n" \
"v_cvt_pk_bf16_f32 v112, v112, v113\n" \
"v_cvt_pk_bf16_f32 v113, v114, v115\n" \
"v_cvt_pk_bf16_f32 v114, v116, v117\n" \
"v_mfma_f32_16x16x32_bf16 v[0:3], a[96:99], a[64:67], v[0:3]\n" \
"ds_read_b128 a[88:91], v213 offset:4352\n" \
"v_cvt_pk_bf16_f32 v115, v118, v119\n" \
"v_mfma_f32_16x16x32_bf16 v[0:3], a[100:103], a[68:71], v[0:3]\n" \
"s_waitcnt lgkmcnt(2)\n" \
"ds_read_b128 a[92:95], v213 offset:6400\n" \
"v_cndmask_b32 v116, v112, v114, s[76:77]\n" \
"v_cndmask_b32 v117, v113, v115, s[76:77]\n" \
"v_cndmask_b32 v118, v114, v112, s[76:77]\n" \
"v_mfma_f32_16x16x32_bf16 v[4:7], a[104:107], a[64:67], v[4:7]\n" \
"ds_read_b64_tr_b16 a[112:113], v214 offset:16896\n" \
"v_cndmask_b32 v119, v115, v113, s[76:77]\n" \
"v_mfma_f32_16x16x32_bf16 v[4:7], a[108:111], a[68:71], v[4:7]\n" \
"s_add_u32 s71, s93, 208896\n" \
"ds_read_b64_tr_b16 a[114:115], v214 offset:18944\n" \
"buffer_store_dwordx4 v[140:143], v236, s[56:59], s71 offen sc1\n" \
"s_add_u32 s71, s93, 139264\n" \
"v_mfma_f32_16x16x32_bf16 v[20:23], a[104:107], a[72:75], v[20:23]\n" \
"buffer_load_dwordx4 v219, s[36:39], s82 offen offset:3072 lds\n" \
"v_mfma_f32_16x16x32_bf16 v[20:23], a[108:111], a[76:79], v[20:23]\n" \
"s_xor_b32 s82, s48, s49\n" \
"s_cmp_lt_u32 s48, s50\n" \
"s_cselect_b32 s82, s82, s48\n" \
"ds_read_b64_tr_b16 a[116:117], v214 offset:25088\n" \
"buffer_store_dwordx4 v[132:135], v236, s[56:59], s71 offen sc1\n" \
"s_add_u32 s71, s93, 69632\n" \
"v_mfma_f32_16x16x32_bf16 v[16:19], a[96:99], a[72:75], v[16:19]\n" \
"s_waitcnt lgkmcnt(3)\n" \
"s_add_u32 s81, s82, s46\n" \
"ds_read_b64_tr_b16 a[118:119], v214 offset:27136\n" \
"v_mfma_f32_16x16x32_bf16 v[16:19], a[100:103], a[76:79], v[16:19]\n" \
"s_add_u32 m0, s86, s79\n" \
"ds_read_b64_tr_b16 a[120:121], v215 offset:16896\n" \
"buffer_store_dwordx4 v[124:127], v236, s[56:59], s71 offen sc1\n" \
"s_add_u32 s71, s93, 0\n" \
"v_mfma_f32_16x16x32_bf16 v[32:35], a[96:99], a[80:83], v[32:35]\n" \
"buffer_load_dwordx4 v234, s[56:59], s81 offen offset:0 sc1 lds\n" \
"v_mfma_f32_16x16x32_bf16 v[32:35], a[100:103], a[84:87], v[32:35]\n" \
"s_sub_u32 s85, s85, 32768\n" \
"s_cselect_b32 s85, 131072, s85\n" \
"ds_read_b64_tr_b16 a[122:123], v215 offset:18944\n" \
"buffer_store_dwordx4 v[116:119], v236, s[56:59], s71 offen sc1\n" \
"v_mfma_f32_16x16x32_bf16 v[36:39], a[104:107], a[80:83], v[36:39]\n" \
"s_lshl_b32 s82, s82, 5\n" \
"ds_read_b64_tr_b16 a[124:125], v215 offset:25088\n" \
"v_add_u32_e64 v213, s85, v211\n" \
"v_mfma_f32_16x16x32_bf16 v[36:39], a[108:111], a[84:87], v[36:39]\n" \
"s_add_u32 s82, s82, s47\n" \
"ds_read_b64_tr_b16 a[126:127], v215 offset:27136\n" \
"v_mfma_f32_16x16x32_bf16 v[52:55], a[104:107], a[88:91], v[52:55]\n" \
"buffer_load_dwordx4 v234, s[56:59], s81 offen offset:1024 sc1 lds\n" \
"v_mfma_f32_16x16x32_bf16 v[52:55], a[108:111], a[92:95], v[52:55]\n" \
"s_add_u32 s48, s48, 0x1000\n" \
"s_sub_u32 s86, s86, 32768\n" \
"s_cselect_b32 s86, 131072, s86\n" \
"v_add_u32_e64 v214, s85, v212\n" \
"v_xor_b32_e64 v215, 32, v214\n" \
"v_mfma_f32_16x16x32_bf16 v[48:51], a[96:99], a[88:91], v[48:51]\n" \
"buffer_load_dwordx4 v234, s[56:59], s81 offen offset:2048 sc1 lds\n" \
"v_mfma_f32_16x16x32_bf16 v[48:51], a[100:103], a[92:95], v[48:51]\n" \
"s_waitcnt vmcnt(23) lgkmcnt(0)\n" \
"s_barrier\n" \
"v_mfma_f32_16x16x32_bf16 v[8:11], a[112:115], a[64:67], v[8:11]\n" \
"ds_read_b64_tr_b16 a[96:97], v214 offset:16384\n" \
"ds_read_b64_tr_b16 a[98:99], v214 offset:18432\n" \
"v_mfma_f32_16x16x32_bf16 v[8:11], a[116:119], a[68:71], v[8:11]\n" \
"ds_read_b64_tr_b16 a[100:101], v214 offset:24576\n" \
"ds_read_b64_tr_b16 a[102:103], v214 offset:26624\n" \
"v_mfma_f32_16x16x32_bf16 v[12:15], a[120:123], a[64:67], v[12:15]\n" \
"buffer_load_dwordx4 v234, s[56:59], s81 offen offset:3072 sc1 lds\n" \
"v_mfma_f32_16x16x32_bf16 v[12:15], a[124:127], a[68:71], v[12:15]\n" \
"ds_read_b128 a[64:67], v213 offset:0\n" \
"v_mfma_f32_16x16x32_bf16 v[28:31], a[120:123], a[72:75], v[28:31]\n" \
"ds_read_b128 a[68:71], v213 offset:2048\n" \
"v_mfma_f32_16x16x32_bf16 v[28:31], a[124:127], a[76:79], v[28:31]\n" \
"s_add_u32 m0, m0, 0x4000\n" \
"ds_read_b64_tr_b16 a[104:105], v215 offset:16384\n" \
"ds_read_b64_tr_b16 a[106:107], v215 offset:18432\n" \
"v_mfma_f32_16x16x32_bf16 v[24:27], a[112:115], a[72:75], v[24:27]\n" \
"buffer_load_dwordx4 v216, s[36:39], s82 offen offset:0 lds\n" \
"v_mfma_f32_16x16x32_bf16 v[24:27], a[116:119], a[76:79], v[24:27]\n" \
"ds_read_b128 a[72:75], v213 offset:256\n" \
"v_mfma_f32_16x16x32_bf16 v[40:43], a[112:115], a[80:83], v[40:43]\n" \
"ds_read_b128 a[76:79], v213 offset:2304\n" \
"v_mfma_f32_16x16x32_bf16 v[40:43], a[116:119], a[84:87], v[40:43]\n" \
"ds_read_b64_tr_b16 a[108:109], v215 offset:24576\n" \
"ds_read_b64_tr_b16 a[110:111], v215 offset:26624\n" \
"v_mfma_f32_16x16x32_bf16 v[44:47], a[120:123], a[80:83], v[44:47]\n" \
"buffer_load_dwordx4 v217, s[36:39], s82 offen offset:1024 lds\n" \
"v_mfma_f32_16x16x32_bf16 v[44:47], a[124:127], a[84:87], v[44:47]\n" \
"ds_read_b128 a[80:83], v213 offset:4096\n" \
"v_mfma_f32_16x16x32_bf16 v[60:63], a[120:123], a[88:91], v[60:63]\n" \
"s_waitcnt lgkmcnt(4)\n" \
"buffer_load_dwordx4 v218, s[36:39], s82 offen offset:2048 lds\n" \
"v_mfma_f32_16x16x32_bf16 v[60:63], a[124:127], a[92:95], v[60:63]\n" \
"ds_read_b128 a[84:87], v213 offset:6144\n" \
"v_mfma_f32_16x16x32_bf16 v[56:59], a[112:115], a[88:91], v[56:59]\n" \
"buffer_load_dwordx4 v219, s[36:39], s82 offen offset:3072 lds\n" \
"v_mfma_f32_16x16x32_bf16 v[56:59], a[116:119], a[92:95], v[56:59]\n" \
"s_mov_b32 s72, 2\n" \
"s_nop 0\n" \
"s_nop 0\n" \
"s_nop 0\n" \
"Lo0_%=:\n" \
"v_mfma_f32_16x16x32_bf16 v[0:3], a[96:99], a[64:67], v[0:3]\n" \
"s_waitcnt lgkmcnt(1)\n" \
"s_xor_b32 s82, s48, s49\n" \
"ds_read_b128 a[88:91], v213 offset:4352\n" \
"v_mfma_f32_16x16x32_bf16 v[0:3], a[100:103], a[68:71], v[0:3]\n" \
"s_cmp_lt_u32 s48, s50\n" \
"s_cselect_b32 s82, s82, s48\n" \
"ds_read_b128 a[92:95], v213 offset:6400\n" \
"v_mfma_f32_16x16x32_bf16 v[4:7], a[104:107], a[64:67], v[4:7]\n" \
"s_add_u32 s81, s82, s46\n" \
"ds_read_b64_tr_b16 a[112:113], v214 offset:16896\n" \
"v_mfma_f32_16x16x32_bf16 v[4:7], a[108:111], a[68:71], v[4:7]\n" \
"s_add_u32 m0, s86, s79\n" \
"ds_read_b64_tr_b16 a[114:115], v214 offset:18944\n" \
"v_mfma_f32_16x16x32_bf16 v[20:23], a[104:107], a[72:75], v[20:23]\n" \
"buffer_load_dwordx4 v234, s[56:59], s81 offen offset:0 sc1 lds\n" \
"v_mfma_f32_16x16x32_bf16 v[20:23], a[108:111], a[76:79], v[20:23]\n" \
"ds_read_b64_tr_b16 a[116:117], v214 offset:25088\n" \
"v_mfma_f32_16x16x32_bf16 v[16:19], a[96:99], a[72:75], v[16:19]\n" \
"s_lshl_b32 s82, s82, 5\n" \
"ds_read_b64_tr_b16 a[118:119], v214 offset:27136\n" \
"v_mfma_f32_16x16x32_bf16 v[16:19], a[100:103], a[76:79], v[16:19]\n" \
"s_waitcnt lgkmcnt(3)\n" \
"s_add_u32 s82, s82, s47\n" \
"ds_read_b64_tr_b16 a[120:121], v215 offset:16896\n" \
"v_mfma_f32_16x16x32_bf16 v[32:35], a[96:99], a[80:83], v[32:35]\n" \
"buffer_load_dwordx4 v234, s[56:59], s81 offen offset:1024 sc1 lds\n" \
"v_mfma_f32_16x16x32_bf16 v[32:35], a[100:103], a[84:87], v[32:35]\n" \
"s_sub_u32 s85, s85, 32768\n" \
"s_cselect_b32 s85, 131072, s85\n" \
"ds_read_b64_tr_b16 a[122:123], v215 offset:18944\n" \
"v_mfma_f32_16x16x32_bf16 v[36:39], a[104:107], a[80:83], v[36:39]\n" \
"s_add_u32 s48, s48, 0x1000\n" \
"ds_read_b64_tr_b16 a[124:125], v215 offset:25088\n" \
"v_add_u32 v213, s85, v211\n" \
"v_mfma_f32_16x16x32_bf16 v[36:39], a[108:111], a[84:87], v[36:39]\n" \
"s_sub_u32 s86, s86, 32768\n" \
"s_cselect_b32 s86, 131072, s86\n" \
"ds_read_b64_tr_b16 a[126:127], v215 offset:27136\n" \
"v_mfma_f32_16x16x32_bf16 v[52:55], a[104:107], a[88:91], v[52:55]\n" \
"buffer_load_dwordx4 v234, s[56:59], s81 offen offset:2048 sc1 lds\n" \
"v_mfma_f32_16x16x32_bf16 v[52:55], a[108:111], a[92:95], v[52:55]\n" \
"v_add_u32 v214, s85, v212\n" \
"v_xor_b32 v215, 32, v214\n" \
"v_mfma_f32_16x16x32_bf16 v[48:51], a[96:99], a[88:91], v[48:51]\n" \
"buffer_load_dwordx4 v234, s[56:59], s81 offen offset:3072 sc1 lds\n" \
"v_mfma_f32_16x16x32_bf16 v[48:51], a[100:103], a[92:95], v[48:51]\n" \
"s_waitcnt vmcnt(20) lgkmcnt(0)\n" \
"s_barrier\n" \
"v_mfma_f32_16x16x32_bf16 v[8:11], a[112:115], a[64:67], v[8:11]\n" \
"ds_read_b64_tr_b16 a[96:97], v214 offset:16384\n" \
"ds_read_b64_tr_b16 a[98:99], v214 offset:18432\n" \
"v_mfma_f32_16x16x32_bf16 v[8:11], a[116:119], a[68:71], v[8:11]\n" \
"s_add_u32 m0, m0, 0x4000\n" \
"ds_read_b64_tr_b16 a[100:101], v214 offset:24576\n" \
"ds_read_b64_tr_b16 a[102:103], v214 offset:26624\n" \
"v_mfma_f32_16x16x32_bf16 v[12:15], a[120:123], a[64:67], v[12:15]\n" \
"buffer_load_dwordx4 v216, s[36:39], s82 offen offset:0 lds\n" \
"v_mfma_f32_16x16x32_bf16 v[12:15], a[124:127], a[68:71], v[12:15]\n" \
"ds_read_b128 a[64:67], v213 offset:0\n" \
"v_mfma_f32_16x16x32_bf16 v[28:31], a[120:123], a[72:75], v[28:31]\n" \
"ds_read_b128 a[68:71], v213 offset:2048\n" \
"v_mfma_f32_16x16x32_bf16 v[28:31], a[124:127], a[76:79], v[28:31]\n" \
"ds_read_b64_tr_b16 a[104:105], v215 offset:16384\n" \
"ds_read_b64_tr_b16 a[106:107], v215 offset:18432\n" \
"v_mfma_f32_16x16x32_bf16 v[24:27], a[112:115], a[72:75], v[24:27]\n" \
"buffer_load_dwordx4 v217, s[36:39], s82 offen offset:1024 lds\n" \
"v_mfma_f32_16x16x32_bf16 v[24:27], a[116:119], a[76:79], v[24:27]\n" \
"ds_read_b128 a[72:75], v213 offset:256\n" \
"v_mfma_f32_16x16x32_bf16 v[40:43], a[112:115], a[80:83], v[40:43]\n" \
"ds_read_b128 a[76:79], v213 offset:2304\n" \
"v_mfma_f32_16x16x32_bf16 v[40:43], a[116:119], a[84:87], v[40:43]\n" \
"ds_read_b64_tr_b16 a[108:109], v215 offset:24576\n" \
"ds_read_b64_tr_b16 a[110:111], v215 offset:26624\n" \
"v_mfma_f32_16x16x32_bf16 v[44:47], a[120:123], a[80:83], v[44:47]\n" \
"buffer_load_dwordx4 v218, s[36:39], s82 offen offset:2048 lds\n" \
"v_mfma_f32_16x16x32_bf16 v[44:47], a[124:127], a[84:87], v[44:47]\n" \
"ds_read_b128 a[80:83], v213 offset:4096\n" \
"v_mfma_f32_16x16x32_bf16 v[60:63], a[120:123], a[88:91], v[60:63]\n" \
"s_waitcnt lgkmcnt(4)\n" \
"v_mfma_f32_16x16x32_bf16 v[60:63], a[124:127], a[92:95], v[60:63]\n" \
"s_sub_u32 s72, s72, 1\n" \
"ds_read_b128 a[84:87], v213 offset:6144\n" \
"v_mfma_f32_16x16x32_bf16 v[56:59], a[112:115], a[88:91], v[56:59]\n" \
"buffer_load_dwordx4 v219, s[36:39], s82 offen offset:3072 lds\n" \
"v_mfma_f32_16x16x32_bf16 v[56:59], a[116:119], a[92:95], v[56:59]\n" \
"s_cbranch_scc0 Lo0_%=\n" \
"s_setpc_b64 s[44:45]\n" \
"Lor_%=:\n" \
"buffer_store_dword v230, v231, s[56:59], s12 offen sc1\n" \
"s_mov_b32 s72, s10\n" \
"s_call_b64 s[44:45], Lo0_%=\n" \
"buffer_load_dword v220, v221, s[56:59], s67 offen sc1\n" \
"s_mov_b32 s72, 1\n" \
"s_call_b64 s[44:45], Lo0_%=\n" \
"s_waitcnt vmcnt(16)\n" \
"v_cmp_eq_u32 vcc, s60, v220\n" \
"s_nop 1\n" \
"s_andn2_b32 s70, s74, vcc_lo\n" \
"s_cbranch_scc1 Lpl6_%=\n" \
"Lpr6_%=:\n" \
"s_cmp_eq_u32 s90, 0\n" \
"s_cselect_b32 s46, s46, s83\n" \
"s_cselect_b32 s47, s47, s84\n" \
"s_cselect_b32 s48, s48, 0\n" \
"s_cselect_b64 s[36:37], s[36:37], s[40:41]\n" \
"s_cselect_b32 s38, s38, s42\n" \
"s_mov_b32 s72, 3\n" \
"s_call_b64 s[44:45], Lo0_%=\n" \
"s_cmp_eq_u32 s90, 0\n" \
"s_cbranch_scc0 Lrole1_%=\n" \
"s_mov_b32 s72, s11\n" \
"s_call_b64 s[44:45], Lo0_%=\n" \
"buffer_load_dword v220, v241, s[56:59], s64 offen sc1\n" \
"s_mov_b32 s72, 1\n" \
"s_call_b64 s[44:45], Lo0_%=\n" \
"v_mfma_f32_16x16x32_bf16 v[0:3], a[96:99], a[64:67], v[0:3]\n" \
"s_waitcnt lgkmcnt(1)\n" \
"ds_read_b128 a[88:91], v213 offset:4352\n" \
"v_mov_b32 v74, 0x3f80\n" \
"v_mfma_f32_16x16x32_bf16 v[0:3], a[100:103], a[68:71], v[0:3]\n" \
"s_add_u32 s48, s86, 32768\n" \
"s_cmp_eq_u32 s48, 163840\n" \
"s_cselect_b32 s48, 0, s48\n" \
"ds_read_b128 a[92:95], v213 offset:6400\n" \
"v_mfma_f32_16x16x32_bf16 v[4:7], a[104:107], a[64:67], v[4:7]\n" \
"ds_read_b64_tr_b16 a[112:113], v214 offset:16896\n" \
"v_mov_b32 v64, 0\n" \
"v_mfma_f32_16x16x32_bf16 v[4:7], a[108:111], a[68:71], v[4:7]\n" \
"ds_read_b64_tr_b16 a[114:115], v214 offset:18944\n" \
"v_mov_b32 v65, 0\n" \
"v_mfma_f32_16x16x32_bf16 v[20:23], a[104:107], a[72:75], v[20:23]\n" \
"s_add_u32 s49, s48, 32768\n" \
"s_cmp_eq_u32 s49, 163840\n" \
"s_cselect_b32 s49, 0, s49\n" \
"v_mfma_f32_16x16x32_bf16 v[20:23], a[108:111], a[76:79], v[20:23]\n" \
"ds_read_b64_tr_b16 a[116:117], v214 offset:25088\n" \
"v_add_u32 v224, s49, v211\n" \
"v_mfma_f32_16x16x32_bf16 v[16:19], a[96:99], a[72:75], v[16:19]\n" \
"ds_read_b64_tr_b16 a[118:119], v214 offset:27136\n" \
"v_add_u32 v225, s49, v212\n" \
"v_mfma_f32_16x16x32_bf16 v[16:19], a[100:103], a[76:79], v[16:19]\n" \
"s_waitcnt lgkmcnt(3)\n" \
"ds_read_b64_tr_b16 a[120:121], v215 offset:16896\n" \
"v_xor_b32 v226, 32, v225\n" \
"v_mfma_f32_16x16x32_bf16 v[32:35], a[96:99], a[80:83], v[32:35]\n" \
"v_add_u32 v227, s48, v211\n" \
"v_add_u32 v228, s48, v212\n" \
"v_mfma_f32_16x16x32_bf16 v[32:35], a[100:103], a[84:87], v[32:35]\n" \
"s_sub_u32 s85, s85, 32768\n" \
"s_cselect_b32 s85, 131072, s85\n" \
"ds_read_b64_tr_b16 a[122:123], v215 offset:18944\n" \
"v_mfma_f32_16x16x32_bf16 v[36:39], a[104:107], a[80:83], v[36:39]\n" \
"ds_read_b64_tr_b16 a[124:125], v215 offset:25088\n" \
"v_add_u32 v213, s85, v211\n" \
"v_mfma_f32_16x16x32_bf16 v[36:39], a[108:111], a[84:87], v[36:39]\n" \
"ds_read_b64_tr_b16 a[126:127], v215 offset:27136\n" \
"v_xor_b32 v229, 32, v228\n" \
"v_mfma_f32_16x16x32_bf16 v[52:55], a[104:107], a[88:91], v[52:55]\n" \
"v_mov_b32 v68, 0\n" \
"v_mov_b32 v69, 0\n" \
"v_mfma_f32_16x16x32_bf16 v[52:55], a[108:111], a[92:95], v[52:55]\n" \
"v_add_u32 v214, s85, v212\n" \
"v_xor_b32 v215, 32, v214\n" \
"v_mfma_f32_16x16x32_bf16 v[48:51], a[96:99], a[88:91], v[48:51]\n" \
"v_lshrrev_b32_e64 v72, 8, v234\n" \
"v_bfe_u32 v73, v234, 6, 2\n" \
"v_mfma_f32_16x16x32_bf16 v[48:51], a[100:103], a[92:95], v[48:51]\n" \
"s_waitcnt vmcnt(8) lgkmcnt(0)\n" \
"s_barrier\n" \
"s_waitcnt vmcnt(16)\n" \
"v_cmp_eq_u32 vcc, s60, v220\n" \
"s_nop 1\n" \
"s_andn2_b32 s70, s69, vcc_lo\n" \
"s_cbranch_scc1 Lpl7_%=\n" \
"Lpr7_%=:\n" \
"v_mfma_f32_16x16x32_bf16 v[8:11], a[112:115], a[64:67], v[8:11]\n" \
"s_add_u32 s71, s87, 0\n" \
"ds_read_b128 a[0:3], v213 offset:0\n" \
"ds_read_b128 a[4:7], v213 offset:2048\n" \
"v_mfma_f32_16x16x32_bf16 v[8:11], a[116:119], a[68:71], v[8:11]\n" \
"buffer_load_dwordx4 v[176:179], v234, s[56:59], s71 offen offset:0 sc1\n" \
"v_mfma_f32_16x16x32_bf16 v[12:15], a[120:123], a[64:67], v[12:15]\n" \
"ds_read_b64_tr_b16 a[128:129], v214 offset:16384\n" \
"ds_read_b64_tr_b16 a[130:131], v214 offset:18432\n" \
"v_mfma_f32_16x16x32_bf16 v[12:15], a[124:127], a[68:71], v[12:15]\n" \
"buffer_load_dwordx4 v[180:183], v234, s[56:59], s71 offen offset:1024 sc1\n" \
"v_mfma_f32_16x16x32_bf16 v[28:31], a[120:123], a[72:75], v[28:31]\n" \
"ds_read_b64_tr_b16 a[132:133], v214 offset:24576\n" \
"ds_read_b64_tr_b16 a[134:135], v214 offset:26624\n" \
"v_mfma_f32_16x16x32_bf16 v[28:31], a[124:127], a[76:79], v[28:31]\n" \
"buffer_load_dwordx4 v[184:187], v234, s[56:59], s71 offen offset:2048 sc1\n" \
"v_mfma_f32_16x16x32_bf16 v[24:27], a[112:115], a[72:75], v[24:27]\n" \
"ds_read_b64_tr_b16 a[136:137], v215 offset:16384\n" \
"ds_read_b64_tr_b16 a[138:139], v215 offset:18432\n" \
"v_mfma_f32_16x16x32_bf16 v[24:27], a[116:119], a[76:79], v[24:27]\n" \
"buffer_load_dwordx4 v[188:191], v234, s[56:59], s71 offen offset:3072 sc1\n" \
"v_mfma_f32_16x16x32_bf16 v[40:43], a[112:115], a[80:83], v[40:43]\n" \
"ds_read_b64_tr_b16 a[140:141], v215 offset:24576\n" \
"ds_read_b64_tr_b16 a[142:143], v215 offset:26624\n" \
"v_mfma_f32_16x16x32_bf16 v[40:43], a[116:119], a[84:87], v[40:43]\n" \
"ds_read_b64_tr_b16 a[144:145], v214 offset:16896\n" \
"ds_read_b64_tr_b16 a[146:147], v214 offset:18944\n" \
"v_mfma_f32_16x16x32_bf16 v[44:47], a[120:123], a[80:83], v[44:47]\n" \
"ds_read_b64_tr_b16 a[148:149], v214 offset:25088\n" \
"ds_read_b64_tr_b16 a[150:151], v214 offset:27136\n" \
"v_mfma_f32_16x16x32_bf16 v[44:47], a[124:127], a[84:87], v[44:47]\n" \
"ds_read_b64_tr_b16 a[152:153], v215 offset:16896\n" \
"ds_read_b64_tr_b16 a[154:155], v215 offset:18944\n" \
"v_mfma_f32_16x16x32_bf16 v[60:63], a[120:123], a[88:91], v[60:63]\n" \
"ds_read_b64_tr_b16 a[156:157], v215 offset:25088\n" \
"ds_read_b64_tr_b16 a[158:159], v215 offset:27136\n" \
"v_mfma_f32_16x16x32_bf16 v[60:63], a[124:127], a[92:95], v[60:63]\n" \
"ds_read_b128 a[24:27], v213 offset:256\n" \
"ds_read_b128 a[28:31], v213 offset:2304\n" \
"v_mfma_f32_16x16x32_bf16 v[56:59], a[112:115], a[88:91], v[56:59]\n" \
"ds_read_b128 a[8:11], v224 offset:0\n" \
"ds_read_b128 a[12:15], v224 offset:2048\n" \
"v_mfma_f32_16x16x32_bf16 v[56:59], a[116:119], a[92:95], v[56:59]\n" \
"s_waitcnt lgkmcnt(8)\n" \
"ds_read_b64_tr_b16 a[160:161], v225 offset:16384\n" \
"ds_read_b64_tr_b16 a[162:163], v225 offset:18432\n" \
"v_mfma_f32_16x16x32_bf16 v[0:3], a[128:131], a[0:3], v[0:3]\n" \
"ds_read_b64_tr_b16 a[164:165], v225 offset:24576\n" \
"ds_read_b64_tr_b16 a[166:167], v225 offset:26624\n" \
"v_mfma_f32_16x16x32_bf16 v[0:3], a[132:135], a[4:7], v[0:3]\n" \
"ds_read_b64_tr_b16 a[168:169], v226 offset:16384\n" \
"ds_read_b64_tr_b16 a[170:171], v226 offset:18432\n" \
"v_mfma_f32_16x16x32_bf16 v[4:7], a[136:139], a[0:3], v[4:7]\n" \
"s_mov_b32 s70, 0x20000000\n" \
"ds_read_b64_tr_b16 a[172:173], v226 offset:24576\n" \
"ds_read_b64_tr_b16 a[174:175], v226 offset:26624\n" \
"v_mfma_f32_16x16x32_bf16 v[4:7], a[140:143], a[4:7], v[4:7]\n" \
"s_cmp_eq_u32 s64, 65616896\n" \
"s_cselect_b32 s70, 65593344, s70\n" \
"ds_read_b64_tr_b16 a[176:177], v225 offset:16896\n" \
"ds_read_b64_tr_b16 a[178:179], v225 offset:18944\n" \
"buffer_store_dword v230, v231, s[56:59], s70 offen sc1\n" \
"v_mfma_f32_16x16x32_bf16 v[8:11], a[144:147], a[0:3], v[8:11]\n" \
"s_waitcnt lgkmcnt(8)\n" \
"ds_read_b64_tr_b16 a[180:181], v225 offset:25088\n" \
"ds_read_b64_tr_b16 a[182:183], v225 offset:27136\n" \
"v_mfma_f32_16x16x32_bf16 v[8:11], a[148:151], a[4:7], v[8:11]\n" \
"s_sub_u32 s68, s86, 32768\n" \
"s_cselect_b32 s68, 131072, s68\n" \
"ds_read_b64_tr_b16 a[184:185], v226 offset:16896\n" \
"ds_read_b64_tr_b16 a[186:187], v226 offset:18944\n" \
"v_mfma_f32_16x16x32_bf16 v[12:15], a[152:155], a[0:3], v[12:15]\n" \
"ds_read_b64_tr_b16 a[188:189], v226 offset:25088\n" \
"ds_read_b64_tr_b16 a[190:191], v226 offset:27136\n" \
"v_mfma_f32_16x16x32_bf16 v[12:15], a[156:159], a[4:7], v[12:15]\n" \
"s_cmp_lt_u32 s61, 2\n" \
"s_cselect_b32 s68, s86, s68\n" \
"v_cmp_eq_u32 s[16:17], v72, v73\n" \
"v_and_b32 v72, 16, v234\n" \
"v_mfma_f32_16x16x32_bf16 v[28:31], a[152:155], a[24:27], v[28:31]\n" \
"s_and_b32 s50, s61, 1\n" \
"v_lshlrev_b32 v72, v72, v74\n" \
"v_cndmask_b32 v72, 0, v72, s[16:17]\n" \
"v_mfma_f32_16x16x32_bf16 v[28:31], a[156:159], a[28:31], v[28:31]\n" \
"s_mul_i32 s50, s50, 9216\n" \
"v_bfe_i32 v73, v234, 5, 1\n" \
"v_and_b32 v67, v72, v73\n" \
"v_mfma_f32_16x16x32_bf16 v[24:27], a[144:147], a[24:27], v[24:27]\n" \
"s_add_u32 s68, s68, s50\n" \
"s_add_u32 s68, s68, %3\n" \
"v_xor_b32 v66, v72, v67\n" \
"v_mfma_f32_16x16x32_bf16 v[24:27], a[148:151], a[28:31], v[24:27]\n" \
"v_lshrrev_b32 v132, 4, v234\n" \
"v_and_b32 v133, 15, v132\n" \
"v_mul_u32_u24 v128, 144, v133\n" \
"v_mfma_f32_16x16x32_bf16 v[20:23], a[136:139], a[24:27], v[20:23]\n" \
"s_waitcnt vmcnt(5)\n" \
"s_barrier\n" \
"v_mfma_f32_16x16x32_bf16 v[20:23], a[140:143], a[28:31], v[20:23]\n" \
"s_add_u32 s71, s87, 4096\n" \
"ds_read_b128 a[32:35], v224 offset:256\n" \
"ds_read_b128 a[36:39], v224 offset:2304\n" \
"v_mfma_f32_16x16x32_bf16 v[16:19], a[128:131], a[24:27], v[16:19]\n" \
"buffer_load_dwordx4 v[192:195], v234, s[56:59], s71 offen offset:0 sc1\n" \
"v_mfma_f32_16x16x32_bf16 v[16:19], a[132:135], a[28:31], v[16:19]\n" \
"s_waitcnt lgkmcnt(2)\n" \
"ds_read_b128 a[16:19], v227 offset:0\n" \
"ds_read_b128 a[20:23], v227 offset:2048\n" \
"v_mfma_f32_16x16x32_bf16 v[0:3], a[160:163], a[8:11], v[0:3]\n" \
"ds_read_b64_tr_b16 a[192:193], v228 offset:16384\n" \
"ds_read_b64_tr_b16 a[194:195], v228 offset:18432\n" \
"v_mfma_f32_16x16x32_bf16 v[0:3], a[164:167], a[12:15], v[0:3]\n" \
"buffer_load_dwordx4 v[196:199], v234, s[56:59], s71 offen offset:1024 sc1\n" \
"v_mfma_f32_16x16x32_bf16 v[4:7], a[168:171], a[8:11], v[4:7]\n" \
"ds_read_b64_tr_b16 a[196:197], v228 offset:24576\n" \
"ds_read_b64_tr_b16 a[198:199], v228 offset:26624\n" \
"v_mfma_f32_16x16x32_bf16 v[4:7], a[172:175], a[12:15], v[4:7]\n" \
"ds_read_b64_tr_b16 a[200:201], v229 offset:16384\n" \
"ds_read_b64_tr_b16 a[202:203], v229 offset:18432\n" \
"v_mfma_f32_16x16x32_bf16 v[8:11], a[176:179], a[8:11], v[8:11]\n" \
"buffer_load_dwordx4 v[200:203], v234, s[56:59], s71 offen offset:2048 sc1\n" \
"v_mfma_f32_16x16x32_bf16 v[8:11], a[180:183], a[12:15], v[8:11]\n" \
"ds_read_b64_tr_b16 a[204:205], v229 offset:24576\n" \
"ds_read_b64_tr_b16 a[206:207], v229 offset:26624\n" \
"v_lshrrev_b32 v133, 4, v132\n" \
"v_mfma_f32_16x16x32_bf16 v[12:15], a[184:187], a[8:11], v[12:15]\n" \
"s_waitcnt lgkmcnt(6)\n" \
"ds_read_b64_tr_b16 a[208:209], v228 offset:16896\n" \
"ds_read_b64_tr_b16 a[210:211], v228 offset:18944\n" \
"v_mfma_f32_16x16x32_bf16 v[12:15], a[188:191], a[12:15], v[12:15]\n" \
"buffer_load_dwordx4 v[204:207], v234, s[56:59], s71 offen offset:3072 sc1\n" \
"v_mfma_f32_16x16x32_bf16 v[28:31], a[184:187], a[32:35], v[28:31]\n" \
"ds_read_b64_tr_b16 a[212:213], v228 offset:25088\n" \
"ds_read_b64_tr_b16 a[214:215], v228 offset:27136\n" \
"v_mfma_f32_16x16x32_bf16 v[28:31], a[188:191], a[36:39], v[28:31]\n" \
"ds_read_b64_tr_b16 a[216:217], v229 offset:16896\n" \
"ds_read_b64_tr_b16 a[218:219], v229 offset:18944\n" \
"v_mfma_f32_16x16x32_bf16 v[24:27], a[176:179], a[32:35], v[24:27]\n" \
"ds_read_b64_tr_b16 a[220:221], v229 offset:25088\n" \
"ds_read_b64_tr_b16 a[222:223], v229 offset:27136\n" \
"v_mfma_f32_16x16x32_bf16 v[24:27], a[180:183], a[36:39], v[24:27]\n" \
"ds_read_b128 a[40:43], v227 offset:256\n" \
"ds_read_b128 a[44:47], v227 offset:2304\n" \
"v_mfma_f32_16x16x32_bf16 v[20:23], a[168:171], a[32:35], v[20:23]\n" \
"ds_read_b128 a[248:251], v213 offset:4096\n" \
"ds_read_b128 a[252:255], v213 offset:6144\n" \
"v_mfma_f32_16x16x32_bf16 v[20:23], a[172:175], a[36:39], v[20:23]\n" \
"ds_read_b128 a[48:51], v224 offset:4096\n" \
"ds_read_b128 a[52:55], v224 offset:6144\n" \
"v_mfma_f32_16x16x32_bf16 v[16:19], a[160:163], a[32:35], v[16:19]\n" \
"ds_read_b128 a[56:59], v227 offset:4096\n" \
"ds_read_b128 a[60:63], v227 offset:6144\n" \
"v_mfma_f32_16x16x32_bf16 v[16:19], a[164:167], a[36:39], v[16:19]\n" \
"s_waitcnt lgkmcnt(8)\n" \
"ds_read_b128 a[224:227], v213 offset:4352\n" \
"ds_read_b128 a[228:231], v213 offset:6400\n" \
"v_lshl_add_u32 v128, v133, 3, v128\n" \
"v_mfma_f32_16x16x32_bf16 v[0:3], a[192:195], a[16:19], v[0:3]\n" \
"s_waitcnt vmcnt(7)\n" \
"ds_read_b128 a[232:235], v224 offset:4352\n" \
"ds_read_b128 a[236:239], v224 offset:6400\n" \
"v_mfma_f32_16x16x32_bf16 v[0:3], a[196:199], a[20:23], v[0:3]\n" \
"ds_read_b128 a[240:243], v227 offset:4352\n" \
"ds_read_b128 a[244:247], v227 offset:6400\n" \
"v_mfma_f32_16x16x32_bf16 v[0:3], v[66:69], v[176:179], v[0:3]\n" \
"v_add_u32 v128, s68, v128\n" \
"v_lshrrev_b32 v133, 3, v132\n" \
"v_mfma_f32_16x16x32_bf16 v[4:7], a[200:203], a[16:19], v[4:7]\n" \
"v_and_b32 v134, 7, v132\n" \
"v_mul_u32_u24 v129, 144, v133\n" \
"v_mfma_f32_16x16x32_bf16 v[4:7], a[204:207], a[20:23], v[4:7]\n" \
"v_lshl_add_u32 v129, v134, 4, v129\n" \
"v_add_u32 v129, s68, v129\n" \
"v_mfma_f32_16x16x32_bf16 v[4:7], v[64:67], v[176:179], v[4:7]\n" \
"v_lshlrev_b32 v134, 4, v134\n" \
"v_lshl_add_u32 v130, v133, 12, v134\n" \
"v_mfma_f32_16x16x32_bf16 v[8:11], a[208:211], a[16:19], v[8:11]\n" \
"v_mul_f32 v0, 4.0, v0\n" \
"v_mul_f32 v1, 4.0, v1\n" \
"v_mul_f32 v2, 4.0, v2\n" \
"v_mul_f32 v3, 4.0, v3\n" \
"v_cvt_pk_bf16_f32 v136, v0, v1\n" \
"v_cvt_pk_bf16_f32 v137, v2, v3\n" \
"v_mfma_f32_16x16x32_bf16 v[8:11], a[212:215], a[20:23], v[8:11]\n" \
"v_mfma_f32_16x16x32_bf16 v[8:11], v[66:69], v[180:183], v[8:11]\n" \
"ds_write_b64 v128, v[136:137] offset:0\n" \
"v_mfma_f32_16x16x32_bf16 v[12:15], a[216:219], a[16:19], v[12:15]\n" \
"v_mul_f32 v4, 4.0, v4\n" \
"v_mul_f32 v5, 4.0, v5\n" \
"v_mfma_f32_16x16x32_bf16 v[12:15], a[220:223], a[20:23], v[12:15]\n" \
"v_mul_f32 v6, 4.0, v6\n" \
"v_mul_f32 v7, 4.0, v7\n" \
"v_mfma_f32_16x16x32_bf16 v[12:15], v[64:67], v[180:183], v[12:15]\n" \
"s_waitcnt lgkmcnt(1)\n" \
"v_cvt_pk_bf16_f32 v138, v4, v5\n" \
"v_cvt_pk_bf16_f32 v139, v6, v7\n" \
"v_mfma_f32_16x16x32_bf16 v[16:19], a[192:195], a[40:43], v[16:19]\n" \
"s_waitcnt vmcnt(5)\n" \
"v_mfma_f32_16x16x32_bf16 v[16:19], a[196:199], a[44:47], v[16:19]\n" \
"v_mfma_f32_16x16x32_bf16 v[16:19], v[66:69], v[184:187], v[16:19]\n" \
"ds_write_b64 v128, v[138:139] offset:32\n" \
"v_mul_f32 v8, 4.0, v8\n" \
"v_mfma_f32_16x16x32_bf16 v[20:23], a[200:203], a[40:43], v[20:23]\n" \
"v_mul_f32 v9, 4.0, v9\n" \
"v_mul_f32 v10, 4.0, v10\n" \
"v_mfma_f32_16x16x32_bf16 v[20:23], a[204:207], a[44:47], v[20:23]\n" \
"v_mul_f32 v11, 4.0, v11\n" \
"v_cvt_pk_bf16_f32 v136, v8, v9\n" \
"v_mfma_f32_16x16x32_bf16 v[20:23], v[64:67], v[184:187], v[20:23]\n" \
"v_cvt_pk_bf16_f32 v137, v10, v11\n" \
"v_mfma_f32_16x16x32_bf16 v[24:27], a[208:211], a[40:43], v[24:27]\n" \
"v_mfma_f32_16x16x32_bf16 v[24:27], a[212:215], a[44:47], v[24:27]\n" \
"v_mfma_f32_16x16x32_bf16 v[24:27], v[66:69], v[188:191], v[24:27]\n" \
"ds_write_b64 v128, v[136:137] offset:64\n" \
"v_mul_f32 v12, 4.0, v12\n" \
"v_mfma_f32_16x16x32_bf16 v[28:31], a[216:219], a[40:43], v[28:31]\n" \
"v_mul_f32 v13, 4.0, v13\n" \
"v_mul_f32 v14, 4.0, v14\n" \
"v_mfma_f32_16x16x32_bf16 v[28:31], a[220:223], a[44:47], v[28:31]\n" \
"v_mul_f32 v15, 4.0, v15\n" \
"v_cvt_pk_bf16_f32 v138, v12, v13\n" \
"v_mfma_f32_16x16x32_bf16 v[28:31], v[64:67], v[188:191], v[28:31]\n" \
"v_cvt_pk_bf16_f32 v139, v14, v15\n" \
"v_mfma_f32_16x16x32_bf16 v[32:35], a[128:131], a[248:251], v[32:35]\n" \
"ds_write_b64 v128, v[138:139] offset:96\n" \
"v_mfma_f32_16x16x32_bf16 v[32:35], a[132:135], a[252:255], v[32:35]\n" \
"ds_read_b128 v[140:143], v129 offset:0\n" \
"ds_read_b128 v[144:147], v129 offset:1152\n" \
"v_mfma_f32_16x16x32_bf16 v[32:35], a[160:163], a[48:51], v[32:35]\n" \
"s_waitcnt vmcnt(2)\n" \
"v_mul_f32 v16, 4.0, v16\n" \
"v_mul_f32 v17, 4.0, v17\n" \
"v_mfma_f32_16x16x32_bf16 v[32:35], a[164:167], a[52:55], v[32:35]\n" \
"v_mul_f32 v18, 4.0, v18\n" \
"v_mul_f32 v19, 4.0, v19\n" \
"v_mfma_f32_16x16x32_bf16 v[32:35], v[66:69], v[192:195], v[32:35]\n" \
"s_add_u32 s70, s88, 0\n" \
"s_waitcnt lgkmcnt(1)\n" \
"v_cvt_pk_bf16_f32 v136, v16, v17\n" \
"v_mfma_f32_16x16x32_bf16 v[32:35], a[192:195], a[56:59], v[32:35]\n" \
"buffer_store_dwordx4 v[140:143], v130, s[52:55], s70 offen sc1\n" \
"v_cvt_pk_bf16_f32 v137, v18, v19\n" \
"v_mfma_f32_16x16x32_bf16 v[32:35], a[196:199], a[60:63], v[32:35]\n" \
"ds_write_b64 v128, v[136:137] offset:2304\n" \
"v_mfma_f32_16x16x32_bf16 v[36:39], a[204:207], a[60:63], v[36:39]\n" \
"s_add_u32 s70, s88, 32768\n" \
"s_waitcnt lgkmcnt(1)\n" \
"v_mul_f32 v20, 4.0, v20\n" \
"v_mfma_f32_16x16x32_bf16 v[36:39], a[200:203], a[56:59], v[36:39]\n" \
"buffer_store_dwordx4 v[144:147], v130, s[52:55], s70 offen sc1\n" \
"v_mfma_f32_16x16x32_bf16 v[36:39], a[172:175], a[52:55], v[36:39]\n" \
"v_mul_f32 v21, 4.0, v21\n" \
"v_mul_f32 v22, 4.0, v22\n" \
"v_mfma_f32_16x16x32_bf16 v[36:39], a[168:171], a[48:51], v[36:39]\n" \
"v_mul_f32 v23, 4.0, v23\n" \
"v_cvt_pk_bf16_f32 v138, v20, v21\n" \
"v_mfma_f32_16x16x32_bf16 v[36:39], v[64:67], v[192:195], v[36:39]\n" \
"v_cvt_pk_bf16_f32 v139, v22, v23\n" \
"v_mfma_f32_16x16x32_bf16 v[36:39], a[140:143], a[252:255], v[36:39]\n" \
"v_mfma_f32_16x16x32_bf16 v[36:39], a[136:139], a[248:251], v[36:39]\n" \
"ds_write_b64 v128, v[138:139] offset:2336\n" \
"v_mul_f32 v24, 4.0, v24\n" \
"v_mul_f32 v25, 4.0, v25\n" \
"v_mfma_f32_16x16x32_bf16 v[40:43], a[144:147], a[248:251], v[40:43]\n" \
"v_mul_f32 v26, 4.0, v26\n" \
"v_mul_f32 v27, 4.0, v27\n" \
"v_mfma_f32_16x16x32_bf16 v[40:43], a[148:151], a[252:255], v[40:43]\n" \
"v_cvt_pk_bf16_f32 v136, v24, v25\n" \
"v_cvt_pk_bf16_f32 v137, v26, v27\n" \
"v_mfma_f32_16x16x32_bf16 v[40:43], a[176:179], a[48:51], v[40:43]\n" \
"ds_write_b64 v128, v[136:137] offset:2368\n" \
"v_mfma_f32_16x16x32_bf16 v[40:43], a[180:183], a[52:55], v[40:43]\n" \
"v_mul_f32 v28, 4.0, v28\n" \
"v_mul_f32 v29, 4.0, v29\n" \
"v_mfma_f32_16x16x32_bf16 v[40:43], v[66:69], v[196:199], v[40:43]\n" \
"v_mul_f32 v30, 4.0, v30\n" \
"v_mul_f32 v31, 4.0, v31\n" \
"v_mfma_f32_16x16x32_bf16 v[40:43], a[208:211], a[56:59], v[40:43]\n" \
"v_cvt_pk_bf16_f32 v138, v28, v29\n" \
"v_cvt_pk_bf16_f32 v139, v30, v31\n" \
"v_mfma_f32_16x16x32_bf16 v[40:43], a[212:215], a[60:63], v[40:43]\n" \
"ds_write_b64 v128, v[138:139] offset:2400\n" \
"v_mfma_f32_16x16x32_bf16 v[44:47], a[220:223], a[60:63], v[44:47]\n" \
"ds_read_b128 v[148:151], v129 offset:2304\n" \
"ds_read_b128 v[152:155], v129 offset:3456\n" \
"v_mfma_f32_16x16x32_bf16 v[44:47], a[216:219], a[56:59], v[44:47]\n" \
"v_mul_f32 v32, 4.0, v32\n" \
"v_mul_f32 v33, 4.0, v33\n" \
"v_mfma_f32_16x16x32_bf16 v[44:47], a[188:191], a[52:55], v[44:47]\n" \
"v_mul_f32 v34, 4.0, v34\n" \
"v_mul_f32 v35, 4.0, v35\n" \
"v_mfma_f32_16x16x32_bf16 v[44:47], a[184:187], a[48:51], v[44:47]\n" \
"s_add_u32 s70, s88, 65536\n" \
"s_waitcnt lgkmcnt(1)\n" \
"v_cvt_pk_bf16_f32 v136, v32, v33\n" \
"v_mfma_f32_16x16x32_bf16 v[44:47], v[64:67], v[196:199], v[44:47]\n" \
"buffer_store_dwordx4 v[148:151], v130, s[52:55], s70 offen sc1\n" \
"v_cvt_pk_bf16_f32 v137, v34, v35\n" \
"v_mfma_f32_16x16x32_bf16 v[44:47], a[156:159], a[252:255], v[44:47]\n" \
"v_mfma_f32_16x16x32_bf16 v[44:47], a[152:155], a[248:251], v[44:47]\n" \
"s_add_u32 s70, s88, 98304\n" \
"s_waitcnt lgkmcnt(0)\n" \
"ds_write_b64 v128, v[136:137] offset:4608\n" \
"v_mul_f32 v36, 4.0, v36\n" \
"Lpg10850_%=:\n" \
"s_cbranch_execz Lcb1_%=\n" \
"v_mfma_f32_16x16x32_bf16 v[48:51], a[128:131], a[224:227], v[48:51]\n" \
"buffer_store_dwordx4 v[152:155], v130, s[52:55], s70 offen sc1\n" \
"v_mul_f32 v37, 4.0, v37\n" \
"v_mfma_f32_16x16x32_bf16 v[48:51], a[132:135], a[228:231], v[48:51]\n" \
"v_mul_f32 v38, 4.0, v38\n" \
"v_mul_f32 v39, 4.0, v39\n" \
"v_mfma_f32_16x16x32_bf16 v[48:51], a[160:163], a[232:235], v[48:51]\n" \
"s_waitcnt vmcnt(4)\n" \
"v_cvt_pk_bf16_f32 v138, v36, v37\n" \
"v_cvt_pk_bf16_f32 v139, v38, v39\n" \
"v_mfma_f32_16x16x32_bf16 v[48:51], a[164:167], a[236:239], v[48:51]\n" \
"ds_write_b64 v128, v[138:139] offset:4640\n" \
"v_mul_f32 v40, 4.0, v40\n" \
"v_mfma_f32_16x16x32_bf16 v[48:51], v[66:69], v[200:203], v[48:51]\n" \
"v_mul_f32 v41, 4.0, v41\n" \
"v_mul_f32 v42, 4.0, v42\n" \
"v_mfma_f32_16x16x32_bf16 v[48:51], a[192:195], a[240:243], v[48:51]\n" \
"v_mul_f32 v43, 4.0, v43\n" \
"v_cvt_pk_bf16_f32 v136, v40, v41\n" \
"v_mfma_f32_16x16x32_bf16 v[48:51], a[196:199], a[244:247], v[48:51]\n" \
"v_cvt_pk_bf16_f32 v137, v42, v43\n" \
"v_mfma_f32_16x16x32_bf16 v[52:55], a[204:207], a[244:247], v[52:55]\n" \
"ds_write_b64 v128, v[136:137] offset:4672\n" \
"v_mul_f32 v44, 4.0, v44\n" \
"v_mul_f32 v45, 4.0, v45\n" \
"v_mfma_f32_16x16x32_bf16 v[52:55], a[200:203], a[240:243], v[52:55]\n" \
"v_mul_f32 v46, 4.0, v46\n" \
"v_mul_f32 v47, 4.0, v47\n" \
"v_mfma_f32_16x16x32_bf16 v[52:55], a[172:175], a[236:239], v[52:55]\n" \
"v_cvt_pk_bf16_f32 v138, v44, v45\n" \
"v_cvt_pk_bf16_f32 v139, v46, v47\n" \
"v_mfma_f32_16x16x32_bf16 v[52:55], a[168:171], a[232:235], v[52:55]\n" \
"ds_write_b64 v128, v[138:139] offset:4704\n" \
"v_mfma_f32_16x16x32_bf16 v[52:55], v[64:67], v[200:203], v[52:55]\n" \
"ds_read_b128 v[156:159], v129 offset:4608\n" \
"ds_read_b128 v[160:163], v129 offset:5760\n" \
"v_mfma_f32_16x16x32_bf16 v[52:55], a[140:143], a[228:231], v[52:55]\n" \
"v_mul_f32 v48, 4.0, v48\n" \
"v_mul_f32 v49, 4.0, v49\n" \
"v_mfma_f32_16x16x32_bf16 v[52:55], a[136:139], a[224:227], v[52:55]\n" \
"v_mul_f32 v50, 4.0, v50\n" \
"v_mul_f32 v51, 4.0, v51\n" \
"v_mfma_f32_16x16x32_bf16 v[56:59], a[144:147], a[224:227], v[56:59]\n" \
"s_add_u32 s70, s88, 131072\n" \
"s_waitcnt lgkmcnt(1)\n" \
"v_cvt_pk_bf16_f32 v136, v48, v49\n" \
"v_mfma_f32_16x16x32_bf16 v[56:59], a[148:151], a[228:231], v[56:59]\n" \
"buffer_store_dwordx4 v[156:159], v130, s[52:55], s70 offen sc1\n" \
"v_cvt_pk_bf16_f32 v137, v50, v51\n" \
"v_mfma_f32_16x16x32_bf16 v[56:59], a[176:179], a[232:235], v[56:59]\n" \
"ds_write_b64 v128, v[136:137] offset:6912\n" \
"v_mfma_f32_16x16x32_bf16 v[56:59], a[180:183], a[236:239], v[56:59]\n" \
"s_add_u32 s70, s88, 163840\n" \
"s_waitcnt lgkmcnt(1)\n" \
"v_mul_f32 v52, 4.0, v52\n" \
"v_mfma_f32_16x16x32_bf16 v[56:59], v[66:69], v[204:207], v[56:59]\n" \
"buffer_store_dwordx4 v[160:163], v130, s[52:55], s70 offen sc1\n" \
"v_mfma_f32_16x16x32_bf16 v[56:59], a[208:211], a[240:243], v[56:59]\n" \
"v_mul_f32 v53, 4.0, v53\n" \
"v_mul_f32 v54, 4.0, v54\n" \
"v_mfma_f32_16x16x32_bf16 v[56:59], a[212:215], a[244:247], v[56:59]\n" \
"v_mul_f32 v55, 4.0, v55\n" \
"v_cvt_pk_bf16_f32 v138, v52, v53\n" \
"v_cvt_pk_bf16_f32 v139, v54, v55\n" \
"v_mfma_f32_16x16x32_bf16 v[60:63], a[220:223], a[244:247], v[60:63]\n" \
"ds_write_b64 v128, v[138:139] offset:6944\n" \
"v_mfma_f32_16x16x32_bf16 v[60:63], a[216:219], a[240:243], v[60:63]\n" \
"v_mfma_f32_16x16x32_bf16 v[60:63], a[188:191], a[236:239], v[60:63]\n" \
"v_mfma_f32_16x16x32_bf16 v[60:63], a[184:187], a[232:235], v[60:63]\n" \
"v_mul_f32 v56, 4.0, v56\n" \
"v_mul_f32 v57, 4.0, v57\n" \
"v_mfma_f32_16x16x32_bf16 v[60:63], v[64:67], v[204:207], v[60:63]\n" \
"v_mul_f32 v58, 4.0, v58\n" \
"v_mul_f32 v59, 4.0, v59\n" \
"v_mfma_f32_16x16x32_bf16 v[60:63], a[156:159], a[228:231], v[60:63]\n" \
"v_cvt_pk_bf16_f32 v136, v56, v57\n" \
"v_cvt_pk_bf16_f32 v137, v58, v59\n" \
"v_mfma_f32_16x16x32_bf16 v[60:63], a[152:155], a[224:227], v[60:63]\n" \
"ds_write_b64 v128, v[136:137] offset:6976\n" \
"s_nop 7\n" \
"v_mul_f32 v60, 4.0, v60\n" \
"v_mul_f32 v61, 4.0, v61\n" \
"v_mul_f32 v62, 4.0, v62\n" \
"v_mul_f32 v63, 4.0, v63\n" \
"v_cvt_pk_bf16_f32 v138, v60, v61\n" \
"v_cvt_pk_bf16_f32 v139, v62, v63\n" \
"ds_write_b64 v128, v[138:139] offset:7008\n" \
"ds_read_b128 v[164:167], v129 offset:6912\n" \
"ds_read_b128 v[168:171], v129 offset:8064\n" \
"s_add_u32 s70, s88, 196608\n" \
"s_waitcnt lgkmcnt(1)\n" \
"buffer_store_dwordx4 v[164:167], v130, s[52:55], s70 offen sc1\n" \
"s_add_u32 s70, s88, 229376\n" \
"s_waitcnt lgkmcnt(0)\n" \
"buffer_store_dwordx4 v[168:171], v130, s[52:55], s70 offen sc1\n" \
"s_endpgm\n" \
".p2align 12\n" \
"Lrole1_%=:\n" \
"v_mfma_f32_16x16x32_bf16 v[64:67], a[96:99], a[64:67], 0\n" \
"Lpg10950_%=:\n" \
"s_cbranch_execz Lcb1_%=\n" \
"s_waitcnt lgkmcnt(1)\n" \
"s_xor_b32 s82, s48, s49\n" \
"ds_read_b128 a[88:91], v213 offset:4352\n" \
"v_mfma_f32_16x16x32_bf16 v[64:67], a[100:103], a[68:71], v[64:67]\n" \
"s_cmp_lt_u32 s48, s50\n" \
"s_cselect_b32 s82, s82, s48\n" \
"ds_read_b128 a[92:95], v213 offset:6400\n" \
"v_mfma_f32_16x16x32_bf16 v[68:71], a[104:107], a[64:67], 0\n" \
"s_add_u32 s81, s82, s46\n" \
"ds_read_b64_tr_b16 a[112:113], v214 offset:16896\n" \
"v_mfma_f32_16x16x32_bf16 v[68:71], a[108:111], a[68:71], v[68:71]\n" \
"s_add_u32 m0, s86, s79\n" \
"ds_read_b64_tr_b16 a[114:115], v214 offset:18944\n" \
"v_mfma_f32_16x16x32_bf16 v[84:87], a[104:107], a[72:75], 0\n" \
"buffer_load_dwordx4 v234, s[56:59], s81 offen offset:0 sc1 lds\n" \
"v_mfma_f32_16x16x32_bf16 v[84:87], a[108:111], a[76:79], v[84:87]\n" \
"s_waitcnt lgkmcnt(3)\n" \
"ds_read_b64_tr_b16 a[116:117], v214 offset:25088\n" \
"v_mfma_f32_16x16x32_bf16 v[80:83], a[96:99], a[72:75], 0\n" \
"s_lshl_b32 s82, s82, 5\n" \
"ds_read_b64_tr_b16 a[118:119], v214 offset:27136\n" \
"v_cvt_pk_bf16_f32 v0, v0, v1\n" \
"v_mfma_f32_16x16x32_bf16 v[80:83], a[100:103], a[76:79], v[80:83]\n" \
"s_add_u32 s82, s82, s47\n" \
"ds_read_b64_tr_b16 a[120:121], v215 offset:16896\n" \
"v_cvt_pk_bf16_f32 v1, v2, v3\n" \
"v_mfma_f32_16x16x32_bf16 v[96:99], a[96:99], a[80:83], 0\n" \
"buffer_load_dwordx4 v234, s[56:59], s81 offen offset:1024 sc1 lds\n" \
"v_mfma_f32_16x16x32_bf16 v[96:99], a[100:103], a[84:87], v[96:99]\n" \
"s_sub_u32 s85, s85, 32768\n" \
"s_cselect_b32 s85, 131072, s85\n" \
"ds_read_b64_tr_b16 a[122:123], v215 offset:18944\n" \
"v_cvt_pk_bf16_f32 v2, v4, v5\n" \
"v_mfma_f32_16x16x32_bf16 v[100:103], a[104:107], a[80:83], 0\n" \
"s_add_u32 s48, s48, 0x1000\n" \
"ds_read_b64_tr_b16 a[124:125], v215 offset:25088\n" \
"v_add_u32 v213, s85, v211\n" \
"v_mfma_f32_16x16x32_bf16 v[100:103], a[108:111], a[84:87], v[100:103]\n" \
"s_waitcnt lgkmcnt(3)\n" \
"s_sub_u32 s86, s86, 32768\n" \
"s_cselect_b32 s86, 131072, s86\n" \
"ds_read_b64_tr_b16 a[126:127], v215 offset:27136\n" \
"v_mfma_f32_16x16x32_bf16 v[116:119], a[104:107], a[88:91], 0\n" \
"buffer_load_dwordx4 v234, s[56:59], s81 offen offset:2048 sc1 lds\n" \
"v_mfma_f32_16x16x32_bf16 v[116:119], a[108:111], a[92:95], v[116:119]\n" \
"v_add_u32 v214, s85, v212\n" \
"v_xor_b32 v215, 32, v214\n" \
"v_cvt_pk_bf16_f32 v3, v6, v7\n" \
"v_mfma_f32_16x16x32_bf16 v[112:115], a[96:99], a[88:91], 0\n" \
"buffer_load_dwordx4 v234, s[56:59], s81 offen offset:3072 sc1 lds\n" \
"v_mfma_f32_16x16x32_bf16 v[112:115], a[100:103], a[92:95], v[112:115]\n" \
"s_waitcnt vmcnt(20) lgkmcnt(0)\n" \
"s_barrier\n" \
"v_mfma_f32_16x16x32_bf16 v[72:75], a[112:115], a[64:67], 0\n" \
"ds_read_b64_tr_b16 a[96:97], v214 offset:16384\n" \
"ds_read_b64_tr_b16 a[98:99], v214 offset:18432\n" \
"v_mfma_f32_16x16x32_bf16 v[72:75], a[116:119], a[68:71], v[72:75]\n" \
"s_add_u32 m0, m0, 0x4000\n" \
"ds_read_b64_tr_b16 a[100:101], v214 offset:24576\n" \
"ds_read_b64_tr_b16 a[102:103], v214 offset:26624\n" \
"v_cvt_pk_bf16_f32 v4, v8, v9\n" \
"v_mfma_f32_16x16x32_bf16 v[76:79], a[120:123], a[64:67], 0\n" \
"buffer_load_dwordx4 v216, s[36:39], s82 offen offset:0 lds\n" \
"v_mfma_f32_16x16x32_bf16 v[76:79], a[124:127], a[68:71], v[76:79]\n" \
"ds_read_b128 a[64:67], v213 offset:0\n" \
"v_cvt_pk_bf16_f32 v5, v10, v11\n" \
"v_mfma_f32_16x16x32_bf16 v[92:95], a[120:123], a[72:75], 0\n" \
"ds_read_b128 a[68:71], v213 offset:2048\n" \
"v_cvt_pk_bf16_f32 v6, v12, v13\n" \
"v_mfma_f32_16x16x32_bf16 v[92:95], a[124:127], a[76:79], v[92:95]\n" \
"ds_read_b64_tr_b16 a[104:105], v215 offset:16384\n" \
"ds_read_b64_tr_b16 a[106:107], v215 offset:18432\n" \
"v_cvt_pk_bf16_f32 v7, v14, v15\n" \
"v_mfma_f32_16x16x32_bf16 v[88:91], a[112:115], a[72:75], 0\n" \
"buffer_load_dwordx4 v217, s[36:39], s82 offen offset:1024 lds\n" \
"v_mfma_f32_16x16x32_bf16 v[88:91], a[116:119], a[76:79], v[88:91]\n" \
"ds_read_b128 a[72:75], v213 offset:256\n" \
"v_cvt_pk_bf16_f32 v8, v16, v17\n" \
"v_mfma_f32_16x16x32_bf16 v[104:107], a[112:115], a[80:83], 0\n" \
"ds_read_b128 a[76:79], v213 offset:2304\n" \
"v_cvt_pk_bf16_f32 v9, v18, v19\n" \
"v_mfma_f32_16x16x32_bf16 v[104:107], a[116:119], a[84:87], v[104:107]\n" \
"ds_read_b64_tr_b16 a[108:109], v215 offset:24576\n" \
"ds_read_b64_tr_b16 a[110:111], v215 offset:26624\n" \
"v_cvt_pk_bf16_f32 v10, v20, v21\n" \
"v_mfma_f32_16x16x32_bf16 v[108:111], a[120:123], a[80:83], 0\n" \
"buffer_load_dwordx4 v218, s[36:39], s82 offen offset:2048 lds\n" \
"v_mfma_f32_16x16x32_bf16 v[108:111], a[124:127], a[84:87], v[108:111]\n" \
"s_add_u32 s71, s87, 0\n" \
"ds_read_b128 a[80:83], v213 offset:4096\n" \
"v_cvt_pk_bf16_f32 v11, v22, v23\n" \
"v_mfma_f32_16x16x32_bf16 v[124:127], a[120:123], a[88:91], 0\n" \
"s_waitcnt lgkmcnt(4)\n" \
"buffer_store_dwordx4 v[0:3], v234, s[56:59], s71 offen offset:0 sc1\n" \
"v_mfma_f32_16x16x32_bf16 v[124:127], a[124:127], a[92:95], v[124:127]\n" \
"ds_read_b128 a[84:87], v213 offset:6144\n" \
"v_cvt_pk_bf16_f32 v12, v24, v25\n" \
"v_mfma_f32_16x16x32_bf16 v[120:123], a[112:115], a[88:91], 0\n" \
"buffer_load_dwordx4 v219, s[36:39], s82 offen offset:3072 lds\n" \
"v_mfma_f32_16x16x32_bf16 v[120:123], a[116:119], a[92:95], v[120:123]\n" \
"buffer_store_dwordx4 v[4:7], v234, s[56:59], s71 offen offset:1024 sc1\n" \
"buffer_store_dwordx4 v[8:11], v234, s[56:59], s71 offen offset:2048 sc1\n" \
"v_mfma_f32_16x16x32_bf16 v[64:67], a[96:99], a[64:67], v[64:67]\n" \
"s_xor_b32 s82, s48, s49\n" \
"ds_read_b128 a[88:91], v213 offset:4352\n" \
"v_cvt_pk_bf16_f32 v13, v26, v27\n" \
"v_mfma_f32_16x16x32_bf16 v[64:67], a[100:103], a[68:71], v[64:67]\n" \
"s_waitcnt lgkmcnt(2)\n" \
"s_cmp_lt_u32 s48, s50\n" \
"s_cselect_b32 s82, s82, s48\n" \
"ds_read_b128 a[92:95], v213 offset:6400\n" \
"v_mfma_f32_16x16x32_bf16 v[68:71], a[104:107], a[64:67], v[68:71]\n" \
"s_add_u32 s81, s82, s46\n" \
"ds_read_b64_tr_b16 a[112:113], v214 offset:16896\n" \
"v_cvt_pk_bf16_f32 v14, v28, v29\n" \
"v_mfma_f32_16x16x32_bf16 v[68:71], a[108:111], a[68:71], v[68:71]\n" \
"s_add_u32 m0, s86, s79\n" \
"ds_read_b64_tr_b16 a[114:115], v214 offset:18944\n" \
"v_cvt_pk_bf16_f32 v15, v30, v31\n" \
"v_mfma_f32_16x16x32_bf16 v[84:87], a[104:107], a[72:75], v[84:87]\n" \
"buffer_load_dwordx4 v234, s[56:59], s81 offen offset:0 sc1 lds\n" \
"v_mfma_f32_16x16x32_bf16 v[84:87], a[108:111], a[76:79], v[84:87]\n" \
"s_waitcnt lgkmcnt(3)\n" \
"ds_read_b64_tr_b16 a[116:117], v214 offset:25088\n" \
"v_cvt_pk_bf16_f32 v16, v32, v33\n" \
"v_mfma_f32_16x16x32_bf16 v[80:83], a[96:99], a[72:75], v[80:83]\n" \
"s_lshl_b32 s82, s82, 5\n" \
"ds_read_b64_tr_b16 a[118:119], v214 offset:27136\n" \
"v_cvt_pk_bf16_f32 v17, v34, v35\n" \
"v_mfma_f32_16x16x32_bf16 v[80:83], a[100:103], a[76:79], v[80:83]\n" \
"s_add_u32 s82, s82, s47\n" \
"ds_read_b64_tr_b16 a[120:121], v215 offset:16896\n" \
"v_cvt_pk_bf16_f32 v18, v36, v37\n" \
"v_mfma_f32_16x16x32_bf16 v[96:99], a[96:99], a[80:83], v[96:99]\n" \
"buffer_load_dwordx4 v234, s[56:59], s81 offen offset:1024 sc1 lds\n" \
"v_mfma_f32_16x16x32_bf16 v[96:99], a[100:103], a[84:87], v[96:99]\n" \
"s_sub_u32 s85, s85, 32768\n" \
"s_cselect_b32 s85, 131072, s85\n" \
"ds_read_b64_tr_b16 a[122:123], v215 offset:18944\n" \
"v_mfma_f32_16x16x32_bf16 v[100:103], a[104:107], a[80:83], v[100:103]\n" \
"s_add_u32 s48, s48, 0x1000\n" \
"ds_read_b64_tr_b16 a[124:125], v215 offset:25088\n" \
"v_add_u32 v213, s85, v211\n" \
"v_mfma_f32_16x16x32_bf16 v[100:103], a[108:111], a[84:87], v[100:103]\n" \
"s_waitcnt lgkmcnt(3)\n" \
"s_sub_u32 s86, s86, 32768\n" \
"s_cselect_b32 s86, 131072, s86\n" \
"ds_read_b64_tr_b16 a[126:127], v215 offset:27136\n" \
"v_mfma_f32_16x16x32_bf16 v[116:119], a[104:107], a[88:91], v[116:119]\n" \
"buffer_load_dwordx4 v234, s[56:59], s81 offen offset:2048 sc1 lds\n" \
"v_mfma_f32_16x16x32_bf16 v[116:119], a[108:111], a[92:95], v[116:119]\n" \
"v_add_u32 v214, s85, v212\n" \
"v_xor_b32 v215, 32, v214\n" \
"v_mfma_f32_16x16x32_bf16 v[112:115], a[96:99], a[88:91], v[112:115]\n" \
"buffer_load_dwordx4 v234, s[56:59], s81 offen offset:3072 sc1 lds\n" \
"v_mfma_f32_16x16x32_bf16 v[112:115], a[100:103], a[92:95], v[112:115]\n" \
"s_waitcnt vmcnt(23) lgkmcnt(0)\n" \
"s_barrier\n" \
"v_mfma_f32_16x16x32_bf16 v[72:75], a[112:115], a[64:67], v[72:75]\n" \
"ds_read_b64_tr_b16 a[96:97], v214 offset:16384\n" \
"ds_read_b64_tr_b16 a[98:99], v214 offset:18432\n" \
"v_mfma_f32_16x16x32_bf16 v[72:75], a[116:119], a[68:71], v[72:75]\n" \
"s_add_u32 m0, m0, 0x4000\n" \
"ds_read_b64_tr_b16 a[100:101], v214 offset:24576\n" \
"ds_read_b64_tr_b16 a[102:103], v214 offset:26624\n" \
"v_mfma_f32_16x16x32_bf16 v[76:79], a[120:123], a[64:67], v[76:79]\n" \
"buffer_load_dwordx4 v216, s[36:39], s82 offen offset:0 lds\n" \
"v_mfma_f32_16x16x32_bf16 v[76:79], a[124:127], a[68:71], v[76:79]\n" \
"ds_read_b128 a[64:67], v213 offset:0\n" \
"v_cvt_pk_bf16_f32 v19, v38, v39\n" \
"v_mfma_f32_16x16x32_bf16 v[92:95], a[120:123], a[72:75], v[92:95]\n" \
"ds_read_b128 a[68:71], v213 offset:2048\n" \
"v_cvt_pk_bf16_f32 v20, v40, v41\n" \
"v_mfma_f32_16x16x32_bf16 v[92:95], a[124:127], a[76:79], v[92:95]\n" \
"ds_read_b64_tr_b16 a[104:105], v215 offset:16384\n" \
"ds_read_b64_tr_b16 a[106:107], v215 offset:18432\n" \
"v_mfma_f32_16x16x32_bf16 v[88:91], a[112:115], a[72:75], v[88:91]\n" \
"buffer_load_dwordx4 v217, s[36:39], s82 offen offset:1024 lds\n" \
"v_mfma_f32_16x16x32_bf16 v[88:91], a[116:119], a[76:79], v[88:91]\n" \
"ds_read_b128 a[72:75], v213 offset:256\n" \
"v_cvt_pk_bf16_f32 v21, v42, v43\n" \
"v_mfma_f32_16x16x32_bf16 v[104:107], a[112:115], a[80:83], v[104:107]\n" \
"ds_read_b128 a[76:79], v213 offset:2304\n" \
"v_cvt_pk_bf16_f32 v22, v44, v45\n" \
"v_mfma_f32_16x16x32_bf16 v[104:107], a[116:119], a[84:87], v[104:107]\n" \
"ds_read_b64_tr_b16 a[108:109], v215 offset:24576\n" \
"ds_read_b64_tr_b16 a[110:111], v215 offset:26624\n" \
"v_mfma_f32_16x16x32_bf16 v[108:111], a[120:123], a[80:83], v[108:111]\n" \
"buffer_load_dwordx4 v218, s[36:39], s82 offen offset:2048 lds\n" \
"v_mfma_f32_16x16x32_bf16 v[108:111], a[124:127], a[84:87], v[108:111]\n" \
"ds_read_b128 a[80:83], v213 offset:4096\n" \
"v_cvt_pk_bf16_f32 v23, v46, v47\n" \
"v_mfma_f32_16x16x32_bf16 v[124:127], a[120:123], a[88:91], v[124:127]\n" \
"s_waitcnt lgkmcnt(4)\n" \
"buffer_store_dwordx4 v[12:15], v234, s[56:59], s71 offen offset:3072 sc1\n" \
"v_mfma_f32_16x16x32_bf16 v[124:127], a[124:127], a[92:95], v[124:127]\n" \
"s_add_u32 s71, s87, 4096\n" \
"ds_read_b128 a[84:87], v213 offset:6144\n" \
"v_cvt_pk_bf16_f32 v24, v48, v49\n" \
"v_mfma_f32_16x16x32_bf16 v[120:123], a[112:115], a[88:91], v[120:123]\n" \
"buffer_load_dwordx4 v219, s[36:39], s82 offen offset:3072 lds\n" \
"v_mfma_f32_16x16x32_bf16 v[120:123], a[116:119], a[92:95], v[120:123]\n" \
"buffer_store_dwordx4 v[16:19], v234, s[56:59], s71 offen offset:0 sc1\n" \
"buffer_store_dwordx4 v[20:23], v234, s[56:59], s71 offen offset:1024 sc1\n" \
"v_mfma_f32_16x16x32_bf16 v[64:67], a[96:99], a[64:67], v[64:67]\n" \
"s_xor_b32 s82, s48, s49\n" \
"ds_read_b128 a[88:91], v213 offset:4352\n" \
"v_cvt_pk_bf16_f32 v25, v50, v51\n" \
"v_mfma_f32_16x16x32_bf16 v[64:67], a[100:103], a[68:71], v[64:67]\n" \
"s_waitcnt lgkmcnt(2)\n" \
"s_cmp_lt_u32 s48, s50\n" \
"s_cselect_b32 s82, s82, s48\n" \
"ds_read_b128 a[92:95], v213 offset:6400\n" \
"v_mfma_f32_16x16x32_bf16 v[68:71], a[104:107], a[64:67], v[68:71]\n" \
"s_add_u32 s81, s82, s46\n" \
"ds_read_b64_tr_b16 a[112:113], v214 offset:16896\n" \
"v_cvt_pk_bf16_f32 v26, v52, v53\n" \
"v_mfma_f32_16x16x32_bf16 v[68:71], a[108:111], a[68:71], v[68:71]\n" \
"s_add_u32 m0, s86, s79\n" \
"ds_read_b64_tr_b16 a[114:115], v214 offset:18944\n" \
"v_cvt_pk_bf16_f32 v27, v54, v55\n" \
"v_mfma_f32_16x16x32_bf16 v[84:87], a[104:107], a[72:75], v[84:87]\n" \
"buffer_load_dwordx4 v234, s[56:59], s81 offen offset:0 sc1 lds\n" \
"v_mfma_f32_16x16x32_bf16 v[84:87], a[108:111], a[76:79], v[84:87]\n" \
"s_waitcnt lgkmcnt(3)\n" \
"ds_read_b64_tr_b16 a[116:117], v214 offset:25088\n" \
"v_cvt_pk_bf16_f32 v28, v56, v57\n" \
"v_mfma_f32_16x16x32_bf16 v[80:83], a[96:99], a[72:75], v[80:83]\n" \
"s_lshl_b32 s82, s82, 5\n" \
"ds_read_b64_tr_b16 a[118:119], v214 offset:27136\n" \
"v_cvt_pk_bf16_f32 v29, v58, v59\n" \
"v_mfma_f32_16x16x32_bf16 v[80:83], a[100:103], a[76:79], v[80:83]\n" \
"s_add_u32 s82, s82, s47\n" \
"ds_read_b64_tr_b16 a[120:121], v215 offset:16896\n" \
"v_cvt_pk_bf16_f32 v30, v60, v61\n" \
"v_mfma_f32_16x16x32_bf16 v[96:99], a[96:99], a[80:83], v[96:99]\n" \
"buffer_load_dwordx4 v234, s[56:59], s81 offen offset:1024 sc1 lds\n" \
"v_mfma_f32_16x16x32_bf16 v[96:99], a[100:103], a[84:87], v[96:99]\n" \
"s_sub_u32 s85, s85, 32768\n" \
"s_cselect_b32 s85, 131072, s85\n" \
"ds_read_b64_tr_b16 a[122:123], v215 offset:18944\n" \
"v_mfma_f32_16x16x32_bf16 v[100:103], a[104:107], a[80:83], v[100:103]\n" \
"s_add_u32 s48, s48, 0x1000\n" \
"ds_read_b64_tr_b16 a[124:125], v215 offset:25088\n" \
"v_add_u32 v213, s85, v211\n" \
"v_mfma_f32_16x16x32_bf16 v[100:103], a[108:111], a[84:87], v[100:103]\n" \
"s_waitcnt lgkmcnt(3)\n" \
"s_sub_u32 s86, s86, 32768\n" \
"s_cselect_b32 s86, 131072, s86\n" \
"ds_read_b64_tr_b16 a[126:127], v215 offset:27136\n" \
"v_mfma_f32_16x16x32_bf16 v[116:119], a[104:107], a[88:91], v[116:119]\n" \
"buffer_load_dwordx4 v234, s[56:59], s81 offen offset:2048 sc1 lds\n" \
"v_mfma_f32_16x16x32_bf16 v[116:119], a[108:111], a[92:95], v[116:119]\n" \
"v_add_u32 v214, s85, v212\n" \
"v_xor_b32 v215, 32, v214\n" \
"v_mfma_f32_16x16x32_bf16 v[112:115], a[96:99], a[88:91], v[112:115]\n" \
"buffer_load_dwordx4 v234, s[56:59], s81 offen offset:3072 sc1 lds\n" \
"v_mfma_f32_16x16x32_bf16 v[112:115], a[100:103], a[92:95], v[112:115]\n" \
"s_waitcnt vmcnt(26) lgkmcnt(0)\n" \
"s_barrier\n" \
"v_mfma_f32_16x16x32_bf16 v[72:75], a[112:115], a[64:67], v[72:75]\n" \
"ds_read_b64_tr_b16 a[96:97], v214 offset:16384\n" \
"ds_read_b64_tr_b16 a[98:99], v214 offset:18432\n" \
"v_mfma_f32_16x16x32_bf16 v[72:75], a[116:119], a[68:71], v[72:75]\n" \
"s_add_u32 m0, m0, 0x4000\n" \
"ds_read_b64_tr_b16 a[100:101], v214 offset:24576\n" \
"ds_read_b64_tr_b16 a[102:103], v214 offset:26624\n" \
"v_mfma_f32_16x16x32_bf16 v[76:79], a[120:123], a[64:67], v[76:79]\n" \
"buffer_load_dwordx4 v216, s[36:39], s82 offen offset:0 lds\n" \
"v_mfma_f32_16x16x32_bf16 v[76:79], a[124:127], a[68:71], v[76:79]\n" \
"ds_read_b128 a[64:67], v213 offset:0\n" \
"v_cvt_pk_bf16_f32 v31, v62, v63\n" \
"v_mfma_f32_16x16x32_bf16 v[92:95], a[120:123], a[72:75], v[92:95]\n" \
"ds_read_b128 a[68:71], v213 offset:2048\n" \
"v_mfma_f32_16x16x32_bf16 v[92:95], a[124:127], a[76:79], v[92:95]\n" \
"ds_read_b64_tr_b16 a[104:105], v215 offset:16384\n" \
"ds_read_b64_tr_b16 a[106:107], v215 offset:18432\n" \
"v_mfma_f32_16x16x32_bf16 v[88:91], a[112:115], a[72:75], v[88:91]\n" \
"buffer_load_dwordx4 v217, s[36:39], s82 offen offset:1024 lds\n" \
"v_mfma_f32_16x16x32_bf16 v[88:91], a[116:119], a[76:79], v[88:91]\n" \
"ds_read_b128 a[72:75], v213 offset:256\n" \
"v_mfma_f32_16x16x32_bf16 v[104:107], a[112:115], a[80:83], v[104:107]\n" \
"ds_read_b128 a[76:79], v213 offset:2304\n" \
"v_mfma_f32_16x16x32_bf16 v[104:107], a[116:119], a[84:87], v[104:107]\n" \
"ds_read_b64_tr_b16 a[108:109], v215 offset:24576\n" \
"ds_read_b64_tr_b16 a[110:111], v215 offset:26624\n" \
"v_mfma_f32_16x16x32_bf16 v[108:111], a[120:123], a[80:83], v[108:111]\n" \
"buffer_load_dwordx4 v218, s[36:39], s82 offen offset:2048 lds\n" \
"v_mfma_f32_16x16x32_bf16 v[108:111], a[124:127], a[84:87], v[108:111]\n" \
"ds_read_b128 a[80:83], v213 offset:4096\n" \
"v_mfma_f32_16x16x32_bf16 v[124:127], a[120:123], a[88:91], v[124:127]\n" \
"s_waitcnt lgkmcnt(4)\n" \
"buffer_store_dwordx4 v[24:27], v234, s[56:59], s71 offen offset:2048 sc1\n" \
"v_mfma_f32_16x16x32_bf16 v[124:127], a[124:127], a[92:95], v[124:127]\n" \
"ds_read_b128 a[84:87], v213 offset:6144\n" \
"v_mfma_f32_16x16x32_bf16 v[120:123], a[112:115], a[88:91], v[120:123]\n" \
"buffer_load_dwordx4 v219, s[36:39], s82 offen offset:3072 lds\n" \
"v_mfma_f32_16x16x32_bf16 v[120:123], a[116:119], a[92:95], v[120:123]\n" \
"buffer_store_dwordx4 v[28:31], v234, s[56:59], s71 offen offset:3072 sc1\n" \
"v_mfma_f32_16x16x32_bf16 v[64:67], a[96:99], a[64:67], v[64:67]\n" \
"s_waitcnt lgkmcnt(1)\n" \
"s_xor_b32 s82, s48, s49\n" \
"ds_read_b128 a[88:91], v213 offset:4352\n" \
"v_mfma_f32_16x16x32_bf16 v[64:67], a[100:103], a[68:71], v[64:67]\n" \
"s_cmp_lt_u32 s48, s50\n" \
"s_cselect_b32 s82, s82, s48\n" \
"ds_read_b128 a[92:95], v213 offset:6400\n" \
"v_mfma_f32_16x16x32_bf16 v[68:71], a[104:107], a[64:67], v[68:71]\n" \
"s_add_u32 s81, s82, s46\n" \
"ds_read_b64_tr_b16 a[112:113], v214 offset:16896\n" \
"v_mfma_f32_16x16x32_bf16 v[68:71], a[108:111], a[68:71], v[68:71]\n" \
"s_add_u32 m0, s86, s79\n" \
"ds_read_b64_tr_b16 a[114:115], v214 offset:18944\n" \
"v_mfma_f32_16x16x32_bf16 v[84:87], a[104:107], a[72:75], v[84:87]\n" \
"buffer_load_dwordx4 v234, s[56:59], s81 offen offset:0 sc1 lds\n" \
"v_mfma_f32_16x16x32_bf16 v[84:87], a[108:111], a[76:79], v[84:87]\n" \
"ds_read_b64_tr_b16 a[116:117], v214 offset:25088\n" \
"v_mfma_f32_16x16x32_bf16 v[80:83], a[96:99], a[72:75], v[80:83]\n" \
"s_lshl_b32 s82, s82, 5\n" \
"ds_read_b64_tr_b16 a[118:119], v214 offset:27136\n" \
"v_mfma_f32_16x16x32_bf16 v[80:83], a[100:103], a[76:79], v[80:83]\n" \
"s_waitcnt lgkmcnt(3)\n" \
"s_add_u32 s82, s82, s47\n" \
"ds_read_b64_tr_b16 a[120:121], v215 offset:16896\n" \
"v_mfma_f32_16x16x32_bf16 v[96:99], a[96:99], a[80:83], v[96:99]\n" \
"buffer_load_dwordx4 v234, s[56:59], s81 offen offset:1024 sc1 lds\n" \
"v_mfma_f32_16x16x32_bf16 v[96:99], a[100:103], a[84:87], v[96:99]\n" \
"s_sub_u32 s85, s85, 32768\n" \
"s_cselect_b32 s85, 131072, s85\n" \
"ds_read_b64_tr_b16 a[122:123], v215 offset:18944\n" \
"v_mfma_f32_16x16x32_bf16 v[100:103], a[104:107], a[80:83], v[100:103]\n" \
"s_add_u32 s48, s48, 0x1000\n" \
"ds_read_b64_tr_b16 a[124:125], v215 offset:25088\n" \
"v_add_u32 v213, s85, v211\n" \
"v_mfma_f32_16x16x32_bf16 v[100:103], a[108:111], a[84:87], v[100:103]\n" \
"s_sub_u32 s86, s86, 32768\n" \
"s_cselect_b32 s86, 131072, s86\n" \
"ds_read_b64_tr_b16 a[126:127], v215 offset:27136\n" \
"v_mfma_f32_16x16x32_bf16 v[116:119], a[104:107], a[88:91], v[116:119]\n" \
"buffer_load_dwordx4 v234, s[56:59], s81 offen offset:2048 sc1 lds\n" \
"v_mfma_f32_16x16x32_bf16 v[116:119], a[108:111], a[92:95], v[116:119]\n" \
"v_add_u32 v214, s85, v212\n" \
"v_xor_b32 v215, 32, v214\n" \
"v_mfma_f32_16x16x32_bf16 v[112:115], a[96:99], a[88:91], v[112:115]\n" \
"buffer_load_dwordx4 v234, s[56:59], s81 offen offset:3072 sc1 lds\n" \
"v_mfma_f32_16x16x32_bf16 v[112:115], a[100:103], a[92:95], v[112:115]\n" \
"s_waitcnt vmcnt(27) lgkmcnt(0)\n" \
"s_barrier\n" \
"v_mfma_f32_16x16x32_bf16 v[72:75], a[112:115], a[64:67], v[72:75]\n" \
"ds_read_b64_tr_b16 a[96:97], v214 offset:16384\n" \
"ds_read_b64_tr_b16 a[98:99], v214 offset:18432\n" \
"v_mfma_f32_16x16x32_bf16 v[72:75], a[116:119], a[68:71], v[72:75]\n" \
"s_add_u32 m0, m0, 0x4000\n" \
"ds_read_b64_tr_b16 a[100:101], v214 offset:24576\n" \
"ds_read_b64_tr_b16 a[102:103], v214 offset:26624\n" \
"v_mfma_f32_16x16x32_bf16 v[76:79], a[120:123], a[64:67], v[76:79]\n" \
"buffer_load_dwordx4 v216, s[36:39], s82 offen offset:0 lds\n" \
"v_mfma_f32_16x16x32_bf16 v[76:79], a[124:127], a[68:71], v[76:79]\n" \
"ds_read_b128 a[64:67], v213 offset:0\n" \
"v_mfma_f32_16x16x32_bf16 v[92:95], a[120:123], a[72:75], v[92:95]\n" \
"ds_read_b128 a[68:71], v213 offset:2048\n" \
"v_mfma_f32_16x16x32_bf16 v[92:95], a[124:127], a[76:79], v[92:95]\n" \
"ds_read_b64_tr_b16 a[104:105], v215 offset:16384\n" \
"ds_read_b64_tr_b16 a[106:107], v215 offset:18432\n" \
"v_mfma_f32_16x16x32_bf16 v[88:91], a[112:115], a[72:75], v[88:91]\n" \
"buffer_load_dwordx4 v217, s[36:39], s82 offen offset:1024 lds\n" \
"v_mfma_f32_16x16x32_bf16 v[88:91], a[116:119], a[76:79], v[88:91]\n" \
"ds_read_b128 a[72:75], v213 offset:256\n" \
"v_mfma_f32_16x16x32_bf16 v[104:107], a[112:115], a[80:83], v[104:107]\n" \
"ds_read_b128 a[76:79], v213 offset:2304\n" \
"v_mfma_f32_16x16x32_bf16 v[104:107], a[116:119], a[84:87], v[104:107]\n" \
"ds_read_b64_tr_b16 a[108:109], v215 offset:24576\n" \
"ds_read_b64_tr_b16 a[110:111], v215 offset:26624\n" \
"v_mfma_f32_16x16x32_bf16 v[108:111], a[120:123], a[80:83], v[108:111]\n" \
"buffer_load_dwordx4 v218, s[36:39], s82 offen offset:2048 lds\n" \
"v_mfma_f32_16x16x32_bf16 v[108:111], a[124:127], a[84:87], v[108:111]\n" \
"ds_read_b128 a[80:83], v213 offset:4096\n" \
"v_mfma_f32_16x16x32_bf16 v[124:127], a[120:123], a[88:91], v[124:127]\n" \
"s_waitcnt lgkmcnt(4)\n" \
"v_mfma_f32_16x16x32_bf16 v[124:127], a[124:127], a[92:95], v[124:127]\n" \
"ds_read_b128 a[84:87], v213 offset:6144\n" \
"v_mfma_f32_16x16x32_bf16 v[120:123], a[112:115], a[88:91], v[120:123]\n" \
"buffer_load_dwordx4 v219, s[36:39], s82 offen offset:3072 lds\n" \
"v_mfma_f32_16x16x32_bf16 v[120:123], a[116:119], a[92:95], v[120:123]\n" \
"v_mfma_f32_16x16x32_bf16 v[64:67], a[96:99], a[64:67], v[64:67]\n" \
"s_waitcnt lgkmcnt(1)\n" \
"s_xor_b32 s82, s48, s49\n" \
"ds_read_b128 a[88:91], v213 offset:4352\n" \
"v_mfma_f32_16x16x32_bf16 v[64:67], a[100:103], a[68:71], v[64:67]\n" \
"s_cmp_lt_u32 s48, s50\n" \
"s_cselect_b32 s82, s82, s48\n" \
"ds_read_b128 a[92:95], v213 offset:6400\n" \
"v_mfma_f32_16x16x32_bf16 v[68:71], a[104:107], a[64:67], v[68:71]\n" \
"s_add_u32 s81, s82, s46\n" \
"ds_read_b64_tr_b16 a[112:113], v214 offset:16896\n" \
"v_mfma_f32_16x16x32_bf16 v[68:71], a[108:111], a[68:71], v[68:71]\n" \
"s_add_u32 m0, s86, s79\n" \
"ds_read_b64_tr_b16 a[114:115], v214 offset:18944\n" \
"v_mfma_f32_16x16x32_bf16 v[84:87], a[104:107], a[72:75], v[84:87]\n" \
"buffer_load_dwordx4 v234, s[56:59], s81 offen offset:0 sc1 lds\n" \
"v_mfma_f32_16x16x32_bf16 v[84:87], a[108:111], a[76:79], v[84:87]\n" \
"ds_read_b64_tr_b16 a[116:117], v214 offset:25088\n" \
"v_mfma_f32_16x16x32_bf16 v[80:83], a[96:99], a[72:75], v[80:83]\n" \
"s_lshl_b32 s82, s82, 5\n" \
"ds_read_b64_tr_b16 a[118:119], v214 offset:27136\n" \
"v_mfma_f32_16x16x32_bf16 v[80:83], a[100:103], a[76:79], v[80:83]\n" \
"s_waitcnt lgkmcnt(3)\n" \
"s_add_u32 s82, s82, s47\n" \
"ds_read_b64_tr_b16 a[120:121], v215 offset:16896\n" \
"v_mfma_f32_16x16x32_bf16 v[96:99], a[96:99], a[80:83], v[96:99]\n" \
"buffer_load_dwordx4 v234, s[56:59], s81 offen offset:1024 sc1 lds\n" \
"v_mfma_f32_16x16x32_bf16 v[96:99], a[100:103], a[84:87], v[96:99]\n" \
"s_sub_u32 s85, s85, 32768\n" \
"s_cselect_b32 s85, 131072, s85\n" \
"ds_read_b64_tr_b16 a[122:123], v215 offset:18944\n" \
"v_mfma_f32_16x16x32_bf16 v[100:103], a[104:107], a[80:83], v[100:103]\n" \
"s_add_u32 s48, s48, 0x1000\n" \
"ds_read_b64_tr_b16 a[124:125], v215 offset:25088\n" \
"v_add_u32 v213, s85, v211\n" \
"v_mfma_f32_16x16x32_bf16 v[100:103], a[108:111], a[84:87], v[100:103]\n" \
"s_sub_u32 s86, s86, 32768\n" \
"s_cselect_b32 s86, 131072, s86\n" \
"ds_read_b64_tr_b16 a[126:127], v215 offset:27136\n" \
"v_mfma_f32_16x16x32_bf16 v[116:119], a[104:107], a[88:91], v[116:119]\n" \
"buffer_load_dwordx4 v234, s[56:59], s81 offen offset:2048 sc1 lds\n" \
"v_mfma_f32_16x16x32_bf16 v[116:119], a[108:111], a[92:95], v[116:119]\n" \
"v_add_u32 v214, s85, v212\n" \
"v_xor_b32 v215, 32, v214\n" \
"v_mfma_f32_16x16x32_bf16 v[112:115], a[96:99], a[88:91], v[112:115]\n" \
"buffer_load_dwordx4 v234, s[56:59], s81 offen offset:3072 sc1 lds\n" \
"v_mfma_f32_16x16x32_bf16 v[112:115], a[100:103], a[92:95], v[112:115]\n" \
"s_waitcnt vmcnt(24) lgkmcnt(0)\n" \
"s_barrier\n" \
"v_mfma_f32_16x16x32_bf16 v[72:75], a[112:115], a[64:67], v[72:75]\n" \
"ds_read_b64_tr_b16 a[96:97], v214 offset:16384\n" \
"ds_read_b64_tr_b16 a[98:99], v214 offset:18432\n" \
"v_mfma_f32_16x16x32_bf16 v[72:75], a[116:119], a[68:71], v[72:75]\n" \
"s_add_u32 m0, m0, 0x4000\n" \
"ds_read_b64_tr_b16 a[100:101], v214 offset:24576\n" \
"ds_read_b64_tr_b16 a[102:103], v214 offset:26624\n" \
"v_mfma_f32_16x16x32_bf16 v[76:79], a[120:123], a[64:67], v[76:79]\n" \
"buffer_load_dwordx4 v216, s[36:39], s82 offen offset:0 lds\n" \
"v_mfma_f32_16x16x32_bf16 v[76:79], a[124:127], a[68:71], v[76:79]\n" \
"ds_read_b128 a[64:67], v213 offset:0\n" \
"v_mfma_f32_16x16x32_bf16 v[92:95], a[120:123], a[72:75], v[92:95]\n" \
"ds_read_b128 a[68:71], v213 offset:2048\n" \
"v_mfma_f32_16x16x32_bf16 v[92:95], a[124:127], a[76:79], v[92:95]\n" \
"ds_read_b64_tr_b16 a[104:105], v215 offset:16384\n" \
"ds_read_b64_tr_b16 a[106:107], v215 offset:18432\n" \
"v_mfma_f32_16x16x32_bf16 v[88:91], a[112:115], a[72:75], v[88:91]\n" \
"buffer_load_dwordx4 v217, s[36:39], s82 offen offset:1024 lds\n" \
"v_mfma_f32_16x16x32_bf16 v[88:91], a[116:119], a[76:79], v[88:91]\n" \
"ds_read_b128 a[72:75], v213 offset:256\n" \
"v_mfma_f32_16x16x32_bf16 v[104:107], a[112:115], a[80:83], v[104:107]\n" \
"ds_read_b128 a[76:79], v213 offset:2304\n" \
"v_mfma_f32_16x16x32_bf16 v[104:107], a[116:119], a[84:87], v[104:107]\n" \
"ds_read_b64_tr_b16 a[108:109], v215 offset:24576\n" \
"ds_read_b64_tr_b16 a[110:111], v215 offset:26624\n" \
"v_mfma_f32_16x16x32_bf16 v[108:111], a[120:123], a[80:83], v[108:111]\n" \
"buffer_load_dwordx4 v218, s[36:39], s82 offen offset:2048 lds\n" \
"v_mfma_f32_16x16x32_bf16 v[108:111], a[124:127], a[84:87], v[108:111]\n" \
"ds_read_b128 a[80:83], v213 offset:4096\n" \
"v_mfma_f32_16x16x32_bf16 v[124:127], a[120:123], a[88:91], v[124:127]\n" \
"s_waitcnt lgkmcnt(4)\n" \
"v_mfma_f32_16x16x32_bf16 v[124:127], a[124:127], a[92:95], v[124:127]\n" \
"ds_read_b128 a[84:87], v213 offset:6144\n" \
"v_mfma_f32_16x16x32_bf16 v[120:123], a[112:115], a[88:91], v[120:123]\n" \
"buffer_load_dwordx4 v219, s[36:39], s82 offen offset:3072 lds\n" \
"v_mfma_f32_16x16x32_bf16 v[120:123], a[116:119], a[92:95], v[120:123]\n" \
"v_mfma_f32_16x16x32_bf16 v[64:67], a[96:99], a[64:67], v[64:67]\n" \
"s_waitcnt lgkmcnt(1)\n" \
"s_xor_b32 s82, s48, s49\n" \
"ds_read_b128 a[88:91], v213 offset:4352\n" \
"v_mfma_f32_16x16x32_bf16 v[64:67], a[100:103], a[68:71], v[64:67]\n" \
"s_cmp_lt_u32 s48, s50\n" \
"s_cselect_b32 s82, s82, s48\n" \
"ds_read_b128 a[92:95], v213 offset:6400\n" \
"v_mfma_f32_16x16x32_bf16 v[68:71], a[104:107], a[64:67], v[68:71]\n" \
"s_add_u32 s81, s82, s46\n" \
"ds_read_b64_tr_b16 a[112:113], v214 offset:16896\n" \
"v_mfma_f32_16x16x32_bf16 v[68:71], a[108:111], a[68:71], v[68:71]\n" \
"s_add_u32 m0, s86, s79\n" \
"ds_read_b64_tr_b16 a[114:115], v214 offset:18944\n" \
"v_mfma_f32_16x16x32_bf16 v[84:87], a[104:107], a[72:75], v[84:87]\n" \
"buffer_load_dwordx4 v234, s[56:59], s81 offen offset:0 sc1 lds\n" \
"v_mfma_f32_16x16x32_bf16 v[84:87], a[108:111], a[76:79], v[84:87]\n" \
"ds_read_b64_tr_b16 a[116:117], v214 offset:25088\n" \
"v_mfma_f32_16x16x32_bf16 v[80:83], a[96:99], a[72:75], v[80:83]\n" \
"s_lshl_b32 s82, s82, 5\n" \
"ds_read_b64_tr_b16 a[118:119], v214 offset:27136\n" \
"v_mfma_f32_16x16x32_bf16 v[80:83], a[100:103], a[76:79], v[80:83]\n" \
"s_waitcnt lgkmcnt(3)\n" \
"s_add_u32 s82, s82, s47\n" \
"ds_read_b64_tr_b16 a[120:121], v215 offset:16896\n" \
"v_mfma_f32_16x16x32_bf16 v[96:99], a[96:99], a[80:83], v[96:99]\n" \
"buffer_load_dwordx4 v234, s[56:59], s81 offen offset:1024 sc1 lds\n" \
"v_mfma_f32_16x16x32_bf16 v[96:99], a[100:103], a[84:87], v[96:99]\n" \
"s_sub_u32 s85, s85, 32768\n" \
"s_cselect_b32 s85, 131072, s85\n" \
"ds_read_b64_tr_b16 a[122:123], v215 offset:18944\n" \
"v_mfma_f32_16x16x32_bf16 v[100:103], a[104:107], a[80:83], v[100:103]\n" \
"s_add_u32 s48, s48, 0x1000\n" \
"ds_read_b64_tr_b16 a[124:125], v215 offset:25088\n" \
"v_add_u32 v213, s85, v211\n" \
"v_mfma_f32_16x16x32_bf16 v[100:103], a[108:111], a[84:87], v[100:103]\n" \
"s_sub_u32 s86, s86, 32768\n" \
"s_cselect_b32 s86, 131072, s86\n" \
"ds_read_b64_tr_b16 a[126:127], v215 offset:27136\n" \
"v_mfma_f32_16x16x32_bf16 v[116:119], a[104:107], a[88:91], v[116:119]\n" \
"buffer_load_dwordx4 v234, s[56:59], s81 offen offset:2048 sc1 lds\n" \
"v_mfma_f32_16x16x32_bf16 v[116:119], a[108:111], a[92:95], v[116:119]\n" \
"v_add_u32 v214, s85, v212\n" \
"v_xor_b32 v215, 32, v214\n" \
"v_mfma_f32_16x16x32_bf16 v[112:115], a[96:99], a[88:91], v[112:115]\n" \
"buffer_load_dwordx4 v234, s[56:59], s81 offen offset:3072 sc1 lds\n" \
"v_mfma_f32_16x16x32_bf16 v[112:115], a[100:103], a[92:95], v[112:115]\n" \
"s_waitcnt vmcnt(20) lgkmcnt(0)\n" \
"s_barrier\n" \
"buffer_store_dword v230, v231, s[56:59], s64 offen sc1\n" \
"v_mfma_f32_16x16x32_bf16 v[72:75], a[112:115], a[64:67], v[72:75]\n" \
"ds_read_b64_tr_b16 a[96:97], v214 offset:16384\n" \
"ds_read_b64_tr_b16 a[98:99], v214 offset:18432\n" \
"v_mfma_f32_16x16x32_bf16 v[72:75], a[116:119], a[68:71], v[72:75]\n" \
"s_add_u32 m0, m0, 0x4000\n" \
"ds_read_b64_tr_b16 a[100:101], v214 offset:24576\n" \
"ds_read_b64_tr_b16 a[102:103], v214 offset:26624\n" \
"v_mfma_f32_16x16x32_bf16 v[76:79], a[120:123], a[64:67], v[76:79]\n" \
"buffer_load_dwordx4 v216, s[36:39], s82 offen offset:0 lds\n" \
"v_mfma_f32_16x16x32_bf16 v[76:79], a[124:127], a[68:71], v[76:79]\n" \
"ds_read_b128 a[64:67], v213 offset:0\n" \
"v_mfma_f32_16x16x32_bf16 v[92:95], a[120:123], a[72:75], v[92:95]\n" \
"ds_read_b128 a[68:71], v213 offset:2048\n" \
"v_mfma_f32_16x16x32_bf16 v[92:95], a[124:127], a[76:79], v[92:95]\n" \
"ds_read_b64_tr_b16 a[104:105], v215 offset:16384\n" \
"ds_read_b64_tr_b16 a[106:107], v215 offset:18432\n" \
"v_mfma_f32_16x16x32_bf16 v[88:91], a[112:115], a[72:75], v[88:91]\n" \
"buffer_load_dwordx4 v217, s[36:39], s82 offen offset:1024 lds\n" \
"v_mfma_f32_16x16x32_bf16 v[88:91], a[116:119], a[76:79], v[88:91]\n" \
"ds_read_b128 a[72:75], v213 offset:256\n" \
"v_mfma_f32_16x16x32_bf16 v[104:107], a[112:115], a[80:83], v[104:107]\n" \
"ds_read_b128 a[76:79], v213 offset:2304\n" \
"v_mfma_f32_16x16x32_bf16 v[104:107], a[116:119], a[84:87], v[104:107]\n" \
"ds_read_b64_tr_b16 a[108:109], v215 offset:24576\n" \
"ds_read_b64_tr_b16 a[110:111], v215 offset:26624\n" \
"v_mfma_f32_16x16x32_bf16 v[108:111], a[120:123], a[80:83], v[108:111]\n" \
"buffer_load_dwordx4 v218, s[36:39], s82 offen offset:2048 lds\n" \
"v_mfma_f32_16x16x32_bf16 v[108:111], a[124:127], a[84:87], v[108:111]\n" \
"ds_read_b128 a[80:83], v213 offset:4096\n" \
"v_mfma_f32_16x16x32_bf16 v[124:127], a[120:123], a[88:91], v[124:127]\n" \
"s_waitcnt lgkmcnt(4)\n" \
"v_mfma_f32_16x16x32_bf16 v[124:127], a[124:127], a[92:95], v[124:127]\n" \
"ds_read_b128 a[84:87], v213 offset:6144\n" \
"v_mfma_f32_16x16x32_bf16 v[120:123], a[112:115], a[88:91], v[120:123]\n" \
"buffer_load_dwordx4 v219, s[36:39], s82 offen offset:3072 lds\n" \
"v_mfma_f32_16x16x32_bf16 v[120:123], a[116:119], a[92:95], v[120:123]\n" \
"s_mov_b32 s72, 5\n" \
"Lo64_%=:\n" \
"v_mfma_f32_16x16x32_bf16 v[64:67], a[96:99], a[64:67], v[64:67]\n" \
"s_waitcnt lgkmcnt(1)\n" \
"s_xor_b32 s82, s48, s49\n" \
"ds_read_b128 a[88:91], v213 offset:4352\n" \
"Lpg11512_%=:\n" \
"s_cbranch_execz Lcb1_%=\n" \
"v_mfma_f32_16x16x32_bf16 v[64:67], a[100:103], a[68:71], v[64:67]\n" \
"s_cmp_lt_u32 s48, s50\n" \
"s_cselect_b32 s82, s82, s48\n" \
"ds_read_b128 a[92:95], v213 offset:6400\n" \
"v_mfma_f32_16x16x32_bf16 v[68:71], a[104:107], a[64:67], v[68:71]\n" \
"s_add_u32 s81, s82, s46\n" \
"ds_read_b64_tr_b16 a[112:113], v214 offset:16896\n" \
"v_mfma_f32_16x16x32_bf16 v[68:71], a[108:111], a[68:71], v[68:71]\n" \
"s_add_u32 m0, s86, s79\n" \
"ds_read_b64_tr_b16 a[114:115], v214 offset:18944\n" \
"v_mfma_f32_16x16x32_bf16 v[84:87], a[104:107], a[72:75], v[84:87]\n" \
"buffer_load_dwordx4 v234, s[56:59], s81 offen offset:0 sc1 lds\n" \
"v_mfma_f32_16x16x32_bf16 v[84:87], a[108:111], a[76:79], v[84:87]\n" \
"ds_read_b64_tr_b16 a[116:117], v214 offset:25088\n" \
"v_mfma_f32_16x16x32_bf16 v[80:83], a[96:99], a[72:75], v[80:83]\n" \
"s_lshl_b32 s82, s82, 5\n" \
"ds_read_b64_tr_b16 a[118:119], v214 offset:27136\n" \
"v_mfma_f32_16x16x32_bf16 v[80:83], a[100:103], a[76:79], v[80:83]\n" \
"s_waitcnt lgkmcnt(3)\n" \
"s_add_u32 s82, s82, s47\n" \
"ds_read_b64_tr_b16 a[120:121], v215 offset:16896\n" \
"v_mfma_f32_16x16x32_bf16 v[96:99], a[96:99], a[80:83], v[96:99]\n" \
"buffer_load_dwordx4 v234, s[56:59], s81 offen offset:1024 sc1 lds\n" \
"v_mfma_f32_16x16x32_bf16 v[96:99], a[100:103], a[84:87], v[96:99]\n" \
"s_sub_u32 s85, s85, 32768\n" \
"s_cselect_b32 s85, 131072, s85\n" \
"ds_read_b64_tr_b16 a[122:123], v215 offset:18944\n" \
"v_mfma_f32_16x16x32_bf16 v[100:103], a[104:107], a[80:83], v[100:103]\n" \
"s_add_u32 s48, s48, 0x1000\n" \
"ds_read_b64_tr_b16 a[124:125], v215 offset:25088\n" \
"v_add_u32 v213, s85, v211\n" \
"v_mfma_f32_16x16x32_bf16 v[100:103], a[108:111], a[84:87], v[100:103]\n" \
"s_sub_u32 s86, s86, 32768\n" \
"s_cselect_b32 s86, 131072, s86\n" \
"ds_read_b64_tr_b16 a[126:127], v215 offset:27136\n" \
"v_mfma_f32_16x16x32_bf16 v[116:119], a[104:107], a[88:91], v[116:119]\n" \
"buffer_load_dwordx4 v234, s[56:59], s81 offen offset:2048 sc1 lds\n" \
"v_mfma_f32_16x16x32_bf16 v[116:119], a[108:111], a[92:95], v[116:119]\n" \
"v_add_u32 v214, s85, v212\n" \
"v_xor_b32 v215, 32, v214\n" \
"v_mfma_f32_16x16x32_bf16 v[112:115], a[96:99], a[88:91], v[112:115]\n" \
"buffer_load_dwordx4 v234, s[56:59], s81 offen offset:3072 sc1 lds\n" \
"v_mfma_f32_16x16x32_bf16 v[112:115], a[100:103], a[92:95], v[112:115]\n" \
"s_waitcnt vmcnt(20) lgkmcnt(0)\n" \
"s_barrier\n" \
"v_mfma_f32_16x16x32_bf16 v[72:75], a[112:115], a[64:67], v[72:75]\n" \
"ds_read_b64_tr_b16 a[96:97], v214 offset:16384\n" \
"ds_read_b64_tr_b16 a[98:99], v214 offset:18432\n" \
"v_mfma_f32_16x16x32_bf16 v[72:75], a[116:119], a[68:71], v[72:75]\n" \
"s_add_u32 m0, m0, 0x4000\n" \
"ds_read_b64_tr_b16 a[100:101], v214 offset:24576\n" \
"ds_read_b64_tr_b16 a[102:103], v214 offset:26624\n" \
"v_mfma_f32_16x16x32_bf16 v[76:79], a[120:123], a[64:67], v[76:79]\n" \
"buffer_load_dwordx4 v216, s[36:39], s82 offen offset:0 lds\n" \
"v_mfma_f32_16x16x32_bf16 v[76:79], a[124:127], a[68:71], v[76:79]\n" \
"ds_read_b128 a[64:67], v213 offset:0\n" \
"v_mfma_f32_16x16x32_bf16 v[92:95], a[120:123], a[72:75], v[92:95]\n" \
"ds_read_b128 a[68:71], v213 offset:2048\n" \
"v_mfma_f32_16x16x32_bf16 v[92:95], a[124:127], a[76:79], v[92:95]\n" \
"ds_read_b64_tr_b16 a[104:105], v215 offset:16384\n" \
"ds_read_b64_tr_b16 a[106:107], v215 offset:18432\n" \
"v_mfma_f32_16x16x32_bf16 v[88:91], a[112:115], a[72:75], v[88:91]\n" \
"buffer_load_dwordx4 v217, s[36:39], s82 offen offset:1024 lds\n" \
"v_mfma_f32_16x16x32_bf16 v[88:91], a[116:119], a[76:79], v[88:91]\n" \
"ds_read_b128 a[72:75], v213 offset:256\n" \
"v_mfma_f32_16x16x32_bf16 v[104:107], a[112:115], a[80:83], v[104:107]\n" \
"ds_read_b128 a[76:79], v213 offset:2304\n" \
"v_mfma_f32_16x16x32_bf16 v[104:107], a[116:119], a[84:87], v[104:107]\n" \
"ds_read_b64_tr_b16 a[108:109], v215 offset:24576\n" \
"ds_read_b64_tr_b16 a[110:111], v215 offset:26624\n" \
"v_mfma_f32_16x16x32_bf16 v[108:111], a[120:123], a[80:83], v[108:111]\n" \
"buffer_load_dwordx4 v218, s[36:39], s82 offen offset:2048 lds\n" \
"v_mfma_f32_16x16x32_bf16 v[108:111], a[124:127], a[84:87], v[108:111]\n" \
"ds_read_b128 a[80:83], v213 offset:4096\n" \
"v_mfma_f32_16x16x32_bf16 v[124:127], a[120:123], a[88:91], v[124:127]\n" \
"s_waitcnt lgkmcnt(4)\n" \
"v_mfma_f32_16x16x32_bf16 v[124:127], a[124:127], a[92:95], v[124:127]\n" \
"s_sub_u32 s72, s72, 1\n" \
"ds_read_b128 a[84:87], v213 offset:6144\n" \
"v_mfma_f32_16x16x32_bf16 v[120:123], a[112:115], a[88:91], v[120:123]\n" \
"buffer_load_dwordx4 v219, s[36:39], s82 offen offset:3072 lds\n" \
"v_mfma_f32_16x16x32_bf16 v[120:123], a[116:119], a[92:95], v[120:123]\n" \
"s_cbranch_scc0 Lo64_%=\n" \
"v_mfma_f32_16x16x32_bf16 v[64:67], a[96:99], a[64:67], v[64:67]\n" \
"s_waitcnt lgkmcnt(1)\n" \
"ds_read_b128 a[88:91], v213 offset:4352\n" \
"v_mfma_f32_16x16x32_bf16 v[64:67], a[100:103], a[68:71], v[64:67]\n" \
"s_add_u32 s48, s86, 32768\n" \
"s_cmp_eq_u32 s48, 163840\n" \
"s_cselect_b32 s48, 0, s48\n" \
"ds_read_b128 a[92:95], v213 offset:6400\n" \
"v_mfma_f32_16x16x32_bf16 v[68:71], a[104:107], a[64:67], v[68:71]\n" \
"ds_read_b64_tr_b16 a[112:113], v214 offset:16896\n" \
"v_mfma_f32_16x16x32_bf16 v[68:71], a[108:111], a[68:71], v[68:71]\n" \
"ds_read_b64_tr_b16 a[114:115], v214 offset:18944\n" \
"v_mfma_f32_16x16x32_bf16 v[84:87], a[104:107], a[72:75], v[84:87]\n" \
"s_add_u32 s49, s48, 32768\n" \
"s_cmp_eq_u32 s49, 163840\n" \
"s_cselect_b32 s49, 0, s49\n" \
"v_mfma_f32_16x16x32_bf16 v[84:87], a[108:111], a[76:79], v[84:87]\n" \
"ds_read_b64_tr_b16 a[116:117], v214 offset:25088\n" \
"v_add_u32 v224, s49, v211\n" \
"v_mfma_f32_16x16x32_bf16 v[80:83], a[96:99], a[72:75], v[80:83]\n" \
"ds_read_b64_tr_b16 a[118:119], v214 offset:27136\n" \
"v_add_u32 v225, s49, v212\n" \
"v_mfma_f32_16x16x32_bf16 v[80:83], a[100:103], a[76:79], v[80:83]\n" \
"s_waitcnt lgkmcnt(3)\n" \
"ds_read_b64_tr_b16 a[120:121], v215 offset:16896\n" \
"v_xor_b32 v226, 32, v225\n" \
"v_mfma_f32_16x16x32_bf16 v[96:99], a[96:99], a[80:83], v[96:99]\n" \
"v_add_u32 v227, s48, v211\n" \
"v_add_u32 v228, s48, v212\n" \
"v_mfma_f32_16x16x32_bf16 v[96:99], a[100:103], a[84:87], v[96:99]\n" \
"s_sub_u32 s85, s85, 32768\n" \
"s_cselect_b32 s85, 131072, s85\n" \
"ds_read_b64_tr_b16 a[122:123], v215 offset:18944\n" \
"v_mfma_f32_16x16x32_bf16 v[100:103], a[104:107], a[80:83], v[100:103]\n" \
"ds_read_b64_tr_b16 a[124:125], v215 offset:25088\n" \
"v_add_u32 v213, s85, v211\n" \
"v_mfma_f32_16x16x32_bf16 v[100:103], a[108:111], a[84:87], v[100:103]\n" \
"ds_read_b64_tr_b16 a[126:127], v215 offset:27136\n" \
"v_xor_b32 v229, 32, v228\n" \
"v_mfma_f32_16x16x32_bf16 v[116:119], a[104:107], a[88:91], v[116:119]\n" \
"v_mfma_f32_16x16x32_bf16 v[116:119], a[108:111], a[92:95], v[116:119]\n" \
"v_add_u32 v214, s85, v212\n" \
"v_xor_b32 v215, 32, v214\n" \
"v_mfma_f32_16x16x32_bf16 v[112:115], a[96:99], a[88:91], v[112:115]\n" \
"v_mfma_f32_16x16x32_bf16 v[112:115], a[100:103], a[92:95], v[112:115]\n" \
"s_waitcnt vmcnt(8) lgkmcnt(0)\n" \
"s_barrier\n" \
"v_mfma_f32_16x16x32_bf16 v[72:75], a[112:115], a[64:67], v[72:75]\n" \
"ds_read_b128 a[0:3], v213 offset:0\n" \
"ds_read_b128 a[4:7], v213 offset:2048\n" \
"v_mfma_f32_16x16x32_bf16 v[72:75], a[116:119], a[68:71], v[72:75]\n" \
"s_sub_u32 s68, s86, 32768\n" \
"s_cselect_b32 s68, 131072, s68\n" \
"ds_read_b64_tr_b16 a[128:129], v214 offset:16384\n" \
"ds_read_b64_tr_b16 a[130:131], v214 offset:18432\n" \
"v_mfma_f32_16x16x32_bf16 v[76:79], a[120:123], a[64:67], v[76:79]\n" \
"ds_read_b64_tr_b16 a[132:133], v214 offset:24576\n" \
"ds_read_b64_tr_b16 a[134:135], v214 offset:26624\n" \
"v_mfma_f32_16x16x32_bf16 v[76:79], a[124:127], a[68:71], v[76:79]\n" \
"s_cmp_lt_u32 s61, 2\n" \
"s_cselect_b32 s68, s86, s68\n" \
"ds_read_b64_tr_b16 a[136:137], v215 offset:16384\n" \
"ds_read_b64_tr_b16 a[138:139], v215 offset:18432\n" \
"v_mfma_f32_16x16x32_bf16 v[92:95], a[120:123], a[72:75], v[92:95]\n" \
"s_and_b32 s50, s61, 1\n" \
"ds_read_b64_tr_b16 a[140:141], v215 offset:24576\n" \
"ds_read_b64_tr_b16 a[142:143], v215 offset:26624\n" \
"v_mfma_f32_16x16x32_bf16 v[92:95], a[124:127], a[76:79], v[92:95]\n" \
"s_mul_i32 s50, s50, 9216\n" \
"ds_read_b64_tr_b16 a[144:145], v214 offset:16896\n" \
"ds_read_b64_tr_b16 a[146:147], v214 offset:18944\n" \
"v_mfma_f32_16x16x32_bf16 v[88:91], a[112:115], a[72:75], v[88:91]\n" \
"s_add_u32 s68, s68, s50\n" \
"ds_read_b64_tr_b16 a[148:149], v214 offset:25088\n" \
"ds_read_b64_tr_b16 a[150:151], v214 offset:27136\n" \
"v_mfma_f32_16x16x32_bf16 v[88:91], a[116:119], a[76:79], v[88:91]\n" \
"s_add_u32 s68, s68, %3\n" \
"ds_read_b64_tr_b16 a[152:153], v215 offset:16896\n" \
"ds_read_b64_tr_b16 a[154:155], v215 offset:18944\n" \
"v_mfma_f32_16x16x32_bf16 v[104:107], a[112:115], a[80:83], v[104:107]\n" \
"ds_read_b64_tr_b16 a[156:157], v215 offset:25088\n" \
"ds_read_b64_tr_b16 a[158:159], v215 offset:27136\n" \
"v_mfma_f32_16x16x32_bf16 v[104:107], a[116:119], a[84:87], v[104:107]\n" \
"ds_read_b128 a[24:27], v213 offset:256\n" \
"ds_read_b128 a[28:31], v213 offset:2304\n" \
"v_mfma_f32_16x16x32_bf16 v[108:111], a[120:123], a[80:83], v[108:111]\n" \
"ds_read_b128 a[8:11], v224 offset:0\n" \
"ds_read_b128 a[12:15], v224 offset:2048\n" \
"v_mfma_f32_16x16x32_bf16 v[108:111], a[124:127], a[84:87], v[108:111]\n" \
"ds_read_b64_tr_b16 a[160:161], v225 offset:16384\n" \
"ds_read_b64_tr_b16 a[162:163], v225 offset:18432\n" \
"v_mfma_f32_16x16x32_bf16 v[124:127], a[120:123], a[88:91], v[124:127]\n" \
"ds_read_b64_tr_b16 a[164:165], v225 offset:24576\n" \
"ds_read_b64_tr_b16 a[166:167], v225 offset:26624\n" \
"v_mfma_f32_16x16x32_bf16 v[124:127], a[124:127], a[92:95], v[124:127]\n" \
"ds_read_b64_tr_b16 a[168:169], v226 offset:16384\n" \
"ds_read_b64_tr_b16 a[170:171], v226 offset:18432\n" \
"v_mfma_f32_16x16x32_bf16 v[120:123], a[112:115], a[88:91], v[120:123]\n" \
"ds_read_b64_tr_b16 a[172:173], v226 offset:24576\n" \
"ds_read_b64_tr_b16 a[174:175], v226 offset:26624\n" \
"v_mfma_f32_16x16x32_bf16 v[120:123], a[116:119], a[92:95], v[120:123]\n" \
"s_waitcnt lgkmcnt(8)\n" \
"ds_read_b64_tr_b16 a[176:177], v225 offset:16896\n" \
"ds_read_b64_tr_b16 a[178:179], v225 offset:18944\n" \
"v_mfma_f32_16x16x32_bf16 v[64:67], a[128:131], a[0:3], v[64:67]\n" \
"ds_read_b64_tr_b16 a[180:181], v225 offset:25088\n" \
"ds_read_b64_tr_b16 a[182:183], v225 offset:27136\n" \
"v_mfma_f32_16x16x32_bf16 v[64:67], a[132:135], a[4:7], v[64:67]\n" \
"ds_read_b64_tr_b16 a[184:185], v226 offset:16896\n" \
"ds_read_b64_tr_b16 a[186:187], v226 offset:18944\n" \
"v_mfma_f32_16x16x32_bf16 v[68:71], a[136:139], a[0:3], v[68:71]\n" \
"ds_read_b64_tr_b16 a[188:189], v226 offset:25088\n" \
"ds_read_b64_tr_b16 a[190:191], v226 offset:27136\n" \
"v_mfma_f32_16x16x32_bf16 v[68:71], a[140:143], a[4:7], v[68:71]\n" \
"v_lshrrev_b32 v132, 4, v234\n" \
"v_and_b32 v133, 15, v132\n" \
"v_mfma_f32_16x16x32_bf16 v[72:75], a[144:147], a[0:3], v[72:75]\n" \
"v_mul_u32_u24 v128, 144, v133\n" \
"v_lshrrev_b32 v133, 4, v132\n" \
"v_mfma_f32_16x16x32_bf16 v[72:75], a[148:151], a[4:7], v[72:75]\n" \
"v_lshl_add_u32 v128, v133, 3, v128\n" \
"v_add_u32 v128, s68, v128\n" \
"v_mfma_f32_16x16x32_bf16 v[76:79], a[152:155], a[0:3], v[76:79]\n" \
"v_lshrrev_b32 v133, 3, v132\n" \
"v_and_b32 v134, 7, v132\n" \
"v_mfma_f32_16x16x32_bf16 v[76:79], a[156:159], a[4:7], v[76:79]\n" \
"v_mul_u32_u24 v129, 144, v133\n" \
"v_lshl_add_u32 v129, v134, 4, v129\n" \
"v_mfma_f32_16x16x32_bf16 v[92:95], a[152:155], a[24:27], v[92:95]\n" \
"v_add_u32 v129, s68, v129\n" \
"v_lshlrev_b32 v134, 4, v134\n" \
"v_mfma_f32_16x16x32_bf16 v[92:95], a[156:159], a[28:31], v[92:95]\n" \
"v_lshl_add_u32 v130, v133, 13, v134\n" \
"v_mfma_f32_16x16x32_bf16 v[88:91], a[144:147], a[24:27], v[88:91]\n" \
"v_mfma_f32_16x16x32_bf16 v[88:91], a[148:151], a[28:31], v[88:91]\n" \
"v_mfma_f32_16x16x32_bf16 v[84:87], a[136:139], a[24:27], v[84:87]\n" \
"s_waitcnt vmcnt(0)\n" \
"s_barrier\n" \
"v_mfma_f32_16x16x32_bf16 v[84:87], a[140:143], a[28:31], v[84:87]\n" \
"ds_read_b128 a[32:35], v224 offset:256\n" \
"ds_read_b128 a[36:39], v224 offset:2304\n" \
"v_mfma_f32_16x16x32_bf16 v[80:83], a[128:131], a[24:27], v[80:83]\n" \
"ds_read_b128 a[16:19], v227 offset:0\n" \
"ds_read_b128 a[20:23], v227 offset:2048\n" \
"v_mfma_f32_16x16x32_bf16 v[80:83], a[132:135], a[28:31], v[80:83]\n" \
"s_waitcnt lgkmcnt(4)\n" \
"ds_read_b64_tr_b16 a[192:193], v228 offset:16384\n" \
"ds_read_b64_tr_b16 a[194:195], v228 offset:18432\n" \
"v_mfma_f32_16x16x32_bf16 v[64:67], a[160:163], a[8:11], v[64:67]\n" \
"ds_read_b64_tr_b16 a[196:197], v228 offset:24576\n" \
"ds_read_b64_tr_b16 a[198:199], v228 offset:26624\n" \
"v_mfma_f32_16x16x32_bf16 v[64:67], a[164:167], a[12:15], v[64:67]\n" \
"ds_read_b64_tr_b16 a[200:201], v229 offset:16384\n" \
"ds_read_b64_tr_b16 a[202:203], v229 offset:18432\n" \
"v_mfma_f32_16x16x32_bf16 v[68:71], a[168:171], a[8:11], v[68:71]\n" \
"ds_read_b64_tr_b16 a[204:205], v229 offset:24576\n" \
"ds_read_b64_tr_b16 a[206:207], v229 offset:26624\n" \
"v_mfma_f32_16x16x32_bf16 v[68:71], a[172:175], a[12:15], v[68:71]\n" \
"ds_read_b64_tr_b16 a[208:209], v228 offset:16896\n" \
"ds_read_b64_tr_b16 a[210:211], v228 offset:18944\n" \
"v_mfma_f32_16x16x32_bf16 v[72:75], a[176:179], a[8:11], v[72:75]\n" \
"ds_read_b64_tr_b16 a[212:213], v228 offset:25088\n" \
"ds_read_b64_tr_b16 a[214:215], v228 offset:27136\n" \
"v_mfma_f32_16x16x32_bf16 v[72:75], a[180:183], a[12:15], v[72:75]\n" \
"ds_read_b64_tr_b16 a[216:217], v229 offset:16896\n" \
"ds_read_b64_tr_b16 a[218:219], v229 offset:18944\n" \
"v_mfma_f32_16x16x32_bf16 v[76:79], a[184:187], a[8:11], v[76:79]\n" \
"ds_read_b64_tr_b16 a[220:221], v229 offset:25088\n" \
"ds_read_b64_tr_b16 a[222:223], v229 offset:27136\n" \
"v_mfma_f32_16x16x32_bf16 v[76:79], a[188:191], a[12:15], v[76:79]\n" \
"s_waitcnt lgkmcnt(8)\n" \
"ds_read_b128 a[40:43], v227 offset:256\n" \
"ds_read_b128 a[44:47], v227 offset:2304\n" \
"v_mfma_f32_16x16x32_bf16 v[92:95], a[184:187], a[32:35], v[92:95]\n" \
"ds_read_b128 a[248:251], v213 offset:4096\n" \
"ds_read_b128 a[252:255], v213 offset:6144\n" \
"v_mfma_f32_16x16x32_bf16 v[92:95], a[188:191], a[36:39], v[92:95]\n" \
"ds_read_b128 a[48:51], v224 offset:4096\n" \
"ds_read_b128 a[52:55], v224 offset:6144\n" \
"v_mfma_f32_16x16x32_bf16 v[88:91], a[176:179], a[32:35], v[88:91]\n" \
"ds_read_b128 a[56:59], v227 offset:4096\n" \
"ds_read_b128 a[60:63], v227 offset:6144\n" \
"v_mfma_f32_16x16x32_bf16 v[88:91], a[180:183], a[36:39], v[88:91]\n" \
"ds_read_b128 a[224:227], v213 offset:4352\n" \
"ds_read_b128 a[228:231], v213 offset:6400\n" \
"v_mfma_f32_16x16x32_bf16 v[84:87], a[168:171], a[32:35], v[84:87]\n" \
"ds_read_b128 a[232:235], v224 offset:4352\n" \
"ds_read_b128 a[236:239], v224 offset:6400\n" \
"v_mfma_f32_16x16x32_bf16 v[84:87], a[172:175], a[36:39], v[84:87]\n" \
"ds_read_b128 a[240:243], v227 offset:4352\n" \
"ds_read_b128 a[244:247], v227 offset:6400\n" \
"v_mfma_f32_16x16x32_bf16 v[80:83], a[160:163], a[32:35], v[80:83]\n" \
"v_mfma_f32_16x16x32_bf16 v[80:83], a[164:167], a[36:39], v[80:83]\n" \
"v_mfma_f32_16x16x32_bf16 v[64:67], a[192:195], a[16:19], v[64:67]\n" \
"v_mfma_f32_16x16x32_bf16 v[64:67], a[196:199], a[20:23], v[64:67]\n" \
"v_mfma_f32_16x16x32_bf16 v[68:71], a[200:203], a[16:19], v[68:71]\n" \
"v_mfma_f32_16x16x32_bf16 v[68:71], a[204:207], a[20:23], v[68:71]\n" \
"s_waitcnt lgkmcnt(0)\n" \
"v_mfma_f32_16x16x32_bf16 v[72:75], a[208:211], a[16:19], v[72:75]\n" \
"v_mfma_f32_16x16x32_bf16 v[72:75], a[212:215], a[20:23], v[72:75]\n" \
"v_cvt_pk_bf16_f32 v136, v64, v65\n" \
"v_cvt_pk_bf16_f32 v137, v66, v67\n" \
"v_mfma_f32_16x16x32_bf16 v[76:79], a[216:219], a[16:19], v[76:79]\n" \
"v_mfma_f32_16x16x32_bf16 v[76:79], a[220:223], a[20:23], v[76:79]\n" \
"ds_write_b64 v128, v[136:137] offset:0\n" \
"v_mfma_f32_16x16x32_bf16 v[80:83], a[192:195], a[40:43], v[80:83]\n" \
"v_cvt_pk_bf16_f32 v138, v68, v69\n" \
"v_cvt_pk_bf16_f32 v139, v70, v71\n" \
"v_mfma_f32_16x16x32_bf16 v[80:83], a[196:199], a[44:47], v[80:83]\n" \
"ds_write_b64 v128, v[138:139] offset:32\n" \
"v_mfma_f32_16x16x32_bf16 v[84:87], a[200:203], a[40:43], v[84:87]\n" \
"v_cvt_pk_bf16_f32 v136, v72, v73\n" \
"v_cvt_pk_bf16_f32 v137, v74, v75\n" \
"v_mfma_f32_16x16x32_bf16 v[84:87], a[204:207], a[44:47], v[84:87]\n" \
"ds_write_b64 v128, v[136:137] offset:64\n" \
"v_mfma_f32_16x16x32_bf16 v[88:91], a[208:211], a[40:43], v[88:91]\n" \
"v_cvt_pk_bf16_f32 v138, v76, v77\n" \
"v_cvt_pk_bf16_f32 v139, v78, v79\n" \
"v_mfma_f32_16x16x32_bf16 v[88:91], a[212:215], a[44:47], v[88:91]\n" \
"ds_write_b64 v128, v[138:139] offset:96\n" \
"v_mfma_f32_16x16x32_bf16 v[92:95], a[216:219], a[40:43], v[92:95]\n" \
"ds_read_b128 v[140:143], v129 offset:0\n" \
"ds_read_b128 v[144:147], v129 offset:1152\n" \
"v_mfma_f32_16x16x32_bf16 v[92:95], a[220:223], a[44:47], v[92:95]\n" \
"v_cvt_pk_bf16_f32 v136, v80, v81\n" \
"v_cvt_pk_bf16_f32 v137, v82, v83\n" \
"v_mfma_f32_16x16x32_bf16 v[96:99], a[128:131], a[248:251], v[96:99]\n" \
"ds_write_b64 v128, v[136:137] offset:2304\n" \
"v_mfma_f32_16x16x32_bf16 v[96:99], a[132:135], a[252:255], v[96:99]\n" \
"s_add_u32 s70, s88, 0\n" \
"s_waitcnt lgkmcnt(2)\n" \
"v_cvt_pk_bf16_f32 v138, v84, v85\n" \
"v_mfma_f32_16x16x32_bf16 v[96:99], a[160:163], a[48:51], v[96:99]\n" \
"buffer_store_dwordx4 v[140:143], v130, s[52:55], s70 offen sc1\n" \
"v_mfma_f32_16x16x32_bf16 v[96:99], a[164:167], a[52:55], v[96:99]\n" \
"v_cvt_pk_bf16_f32 v139, v86, v87\n" \
"v_mfma_f32_16x16x32_bf16 v[96:99], a[192:195], a[56:59], v[96:99]\n" \
"s_add_u32 s70, s88, 65536\n" \
"s_waitcnt lgkmcnt(1)\n" \
"v_mfma_f32_16x16x32_bf16 v[96:99], a[196:199], a[60:63], v[96:99]\n" \
"buffer_store_dwordx4 v[144:147], v130, s[52:55], s70 offen sc1\n" \
"ds_write_b64 v128, v[138:139] offset:2336\n" \
"v_mfma_f32_16x16x32_bf16 v[100:103], a[204:207], a[60:63], v[100:103]\n" \
"v_cvt_pk_bf16_f32 v136, v88, v89\n" \
"v_cvt_pk_bf16_f32 v137, v90, v91\n" \
"v_mfma_f32_16x16x32_bf16 v[100:103], a[200:203], a[56:59], v[100:103]\n" \
"ds_write_b64 v128, v[136:137] offset:2368\n" \
"v_mfma_f32_16x16x32_bf16 v[100:103], a[172:175], a[52:55], v[100:103]\n" \
"v_cvt_pk_bf16_f32 v138, v92, v93\n" \
"v_cvt_pk_bf16_f32 v139, v94, v95\n" \
"v_mfma_f32_16x16x32_bf16 v[100:103], a[168:171], a[48:51], v[100:103]\n" \
"v_mfma_f32_16x16x32_bf16 v[100:103], a[140:143], a[252:255], v[100:103]\n" \
"v_mfma_f32_16x16x32_bf16 v[100:103], a[136:139], a[248:251], v[100:103]\n" \
"ds_write_b64 v128, v[138:139] offset:2400\n" \
"v_mfma_f32_16x16x32_bf16 v[104:107], a[144:147], a[248:251], v[104:107]\n" \
"ds_read_b128 v[148:151], v129 offset:2304\n" \
"ds_read_b128 v[152:155], v129 offset:3456\n" \
"v_mfma_f32_16x16x32_bf16 v[104:107], a[148:151], a[252:255], v[104:107]\n" \
"v_cvt_pk_bf16_f32 v136, v96, v97\n" \
"v_cvt_pk_bf16_f32 v137, v98, v99\n" \
"v_mfma_f32_16x16x32_bf16 v[104:107], a[176:179], a[48:51], v[104:107]\n" \
"ds_write_b64 v128, v[136:137] offset:4608\n" \
"v_mfma_f32_16x16x32_bf16 v[104:107], a[180:183], a[52:55], v[104:107]\n" \
"v_cvt_pk_bf16_f32 v138, v100, v101\n" \
"v_cvt_pk_bf16_f32 v139, v102, v103\n" \
"v_mfma_f32_16x16x32_bf16 v[104:107], a[208:211], a[56:59], v[104:107]\n" \
"s_add_u32 s70, s88, 4096\n" \
"s_waitcnt lgkmcnt(2)\n" \
"v_mfma_f32_16x16x32_bf16 v[104:107], a[212:215], a[60:63], v[104:107]\n" \
"buffer_store_dwordx4 v[148:151], v130, s[52:55], s70 offen sc1\n" \
"ds_write_b64 v128, v[138:139] offset:4640\n" \
"v_mfma_f32_16x16x32_bf16 v[108:111], a[220:223], a[60:63], v[108:111]\n" \
"s_add_u32 s70, s88, 69632\n" \
"s_waitcnt lgkmcnt(2)\n" \
"v_mfma_f32_16x16x32_bf16 v[108:111], a[216:219], a[56:59], v[108:111]\n" \
"buffer_store_dwordx4 v[152:155], v130, s[52:55], s70 offen sc1\n" \
"v_mfma_f32_16x16x32_bf16 v[108:111], a[188:191], a[52:55], v[108:111]\n" \
"v_mfma_f32_16x16x32_bf16 v[108:111], a[184:187], a[48:51], v[108:111]\n" \
"v_cvt_pk_bf16_f32 v136, v104, v105\n" \
"v_cvt_pk_bf16_f32 v137, v106, v107\n" \
"v_mfma_f32_16x16x32_bf16 v[108:111], a[156:159], a[252:255], v[108:111]\n" \
"v_mfma_f32_16x16x32_bf16 v[108:111], a[152:155], a[248:251], v[108:111]\n" \
"ds_write_b64 v128, v[136:137] offset:4672\n" \
"v_mfma_f32_16x16x32_bf16 v[112:115], a[128:131], a[224:227], v[112:115]\n" \
"v_mfma_f32_16x16x32_bf16 v[112:115], a[132:135], a[228:231], v[112:115]\n" \
"v_mfma_f32_16x16x32_bf16 v[112:115], a[160:163], a[232:235], v[112:115]\n" \
"v_mfma_f32_16x16x32_bf16 v[112:115], a[164:167], a[236:239], v[112:115]\n" \
"v_cvt_pk_bf16_f32 v138, v108, v109\n" \
"v_cvt_pk_bf16_f32 v139, v110, v111\n" \
"v_mfma_f32_16x16x32_bf16 v[112:115], a[192:195], a[240:243], v[112:115]\n" \
"v_mfma_f32_16x16x32_bf16 v[112:115], a[196:199], a[244:247], v[112:115]\n" \
"ds_write_b64 v128, v[138:139] offset:4704\n" \
"v_mfma_f32_16x16x32_bf16 v[116:119], a[204:207], a[244:247], v[116:119]\n" \
"ds_read_b128 v[156:159], v129 offset:4608\n" \
"ds_read_b128 v[160:163], v129 offset:5760\n" \
"v_mfma_f32_16x16x32_bf16 v[116:119], a[200:203], a[240:243], v[116:119]\n" \
"v_mfma_f32_16x16x32_bf16 v[116:119], a[172:175], a[236:239], v[116:119]\n" \
"v_mfma_f32_16x16x32_bf16 v[116:119], a[168:171], a[232:235], v[116:119]\n" \
"v_cvt_pk_bf16_f32 v136, v112, v113\n" \
"v_cvt_pk_bf16_f32 v137, v114, v115\n" \
"v_mfma_f32_16x16x32_bf16 v[116:119], a[140:143], a[228:231], v[116:119]\n" \
"s_add_u32 s70, s88, 131072\n" \
"s_waitcnt lgkmcnt(1)\n" \
"v_mfma_f32_16x16x32_bf16 v[116:119], a[136:139], a[224:227], v[116:119]\n" \
"buffer_store_dwordx4 v[156:159], v130, s[52:55], s70 offen sc1\n" \
"ds_write_b64 v128, v[136:137] offset:6912\n" \
"v_mfma_f32_16x16x32_bf16 v[120:123], a[144:147], a[224:227], v[120:123]\n" \
"s_add_u32 s70, s88, 196608\n" \
"s_waitcnt lgkmcnt(1)\n" \
"v_mfma_f32_16x16x32_bf16 v[120:123], a[148:151], a[228:231], v[120:123]\n" \
"buffer_store_dwordx4 v[160:163], v130, s[52:55], s70 offen sc1\n" \
"v_mfma_f32_16x16x32_bf16 v[120:123], a[176:179], a[232:235], v[120:123]\n" \
"v_mfma_f32_16x16x32_bf16 v[120:123], a[180:183], a[236:239], v[120:123]\n" \
"v_cvt_pk_bf16_f32 v138, v116, v117\n" \
"v_cvt_pk_bf16_f32 v139, v118, v119\n" \
"v_mfma_f32_16x16x32_bf16 v[120:123], a[208:211], a[240:243], v[120:123]\n" \
"v_mfma_f32_16x16x32_bf16 v[120:123], a[212:215], a[244:247], v[120:123]\n" \
"ds_write_b64 v128, v[138:139] offset:6944\n" \
"v_mfma_f32_16x16x32_bf16 v[124:127], a[220:223], a[244:247], v[124:127]\n" \
"v_mfma_f32_16x16x32_bf16 v[124:127], a[216:219], a[240:243], v[124:127]\n" \
"v_mfma_f32_16x16x32_bf16 v[124:127], a[188:191], a[236:239], v[124:127]\n" \
"v_mfma_f32_16x16x32_bf16 v[124:127], a[184:187], a[232:235], v[124:127]\n" \
"v_cvt_pk_bf16_f32 v136, v120, v121\n" \
"v_cvt_pk_bf16_f32 v137, v122, v123\n" \
"v_mfma_f32_16x16x32_bf16 v[124:127], a[156:159], a[228:231], v[124:127]\n" \
"v_mfma_f32_16x16x32_bf16 v[124:127], a[152:155], a[224:227], v[124:127]\n" \
"ds_write_b64 v128, v[136:137] offset:6976\n" \
"s_nop 7\n" \
"v_cvt_pk_bf16_f32 v138, v124, v125\n" \
"v_cvt_pk_bf16_f32 v139, v126, v127\n" \
"ds_write_b64 v128, v[138:139] offset:7008\n" \
"ds_read_b128 v[164:167], v129 offset:6912\n" \
"ds_read_b128 v[168:171], v129 offset:8064\n" \
"s_add_u32 s70, s88, 135168\n" \
"s_waitcnt lgkmcnt(1)\n" \
"buffer_store_dwordx4 v[164:167], v130, s[52:55], s70 offen sc1\n" \
"s_add_u32 s70, s88, 200704\n" \
"s_waitcnt lgkmcnt(0)\n" \
"buffer_store_dwordx4 v[168:171], v130, s[52:55], s70 offen sc1\n" \
"s_endpgm\n" \
"Lhot_end_%=:\n" \
"Lpl1_%=:\n" \
"s_mov_b32 s72, 0\n" \
"Lpk1_%=:\n" \
"s_sleep 2\n" \
"buffer_load_dword v227, v242, s[56:59], s90 offen sc1\n" \
"s_waitcnt vmcnt(0)\n" \
"v_cmp_eq_u32 vcc, s60, v227\n" \
"s_nop 1\n" \
"s_andn2_b32 s70, 0xff, vcc_lo\n" \
"s_cbranch_scc0 Lpr1_%=\n" \
"s_add_u32 s72, s72, 1\n" \
"s_cmp_lt_u32 s72, 0x40000\n" \
"s_cbranch_scc1 Lpk1_%=\n" \
"s_branch Lpr1_%=\n" \
"Lpl2_%=:\n" \
"s_mov_b32 s72, 0\n" \
"Lpk2_%=:\n" \
"s_sleep 2\n" \
"buffer_load_dword v239, v242, s[56:59], s95 offen sc1\n" \
"s_waitcnt vmcnt(0)\n" \
"v_cmp_eq_u32 vcc, s60, v239\n" \
"s_nop 1\n" \
"s_andn2_b32 s70, s69, vcc_lo\n" \
"s_cbranch_scc0 Lpr2_%=\n" \
"s_add_u32 s72, s72, 1\n" \
"s_cmp_lt_u32 s72, 0x40000\n" \
"s_cbranch_scc1 Lpk2_%=\n" \
"s_branch Lpr2_%=\n" \
"Lpl3_%=:\n" \
"s_mov_b32 s72, 0\n" \
"Lpk3_%=:\n" \
"s_sleep 2\n" \
"buffer_load_dword v239, v242, s[56:59], s95 offen sc1\n" \
"s_waitcnt vmcnt(0)\n" \
"v_cmp_eq_u32 vcc, s60, v239\n" \
"s_nop 1\n" \
"s_andn2_b32 s70, s69, vcc_lo\n" \
"s_cbranch_scc0 Lpr3_%=\n" \
"s_add_u32 s72, s72, 1\n" \
"s_cmp_lt_u32 s72, 0x40000\n" \
"s_cbranch_scc1 Lpk3_%=\n" \
"s_branch Lpr3_%=\n" \
"Lexact_%=:\n" \
"v_mov_b32 v239, 0xff800000\n" \
"s_and_b32 s70, s61, 1\n" \
"s_lshl_b32 s70, s70, 4\n" \
"v_add_u32 v186, s70, v238\n" \
"v_accvgpr_read_b32 v160, a128\n" \
"v_accvgpr_read_b32 v161, a129\n" \
"v_accvgpr_read_b32 v162, a130\n" \
"v_accvgpr_read_b32 v163, a131\n" \
"v_accvgpr_read_b32 v164, a132\n" \
"v_accvgpr_read_b32 v165, a133\n" \
"v_accvgpr_read_b32 v166, a134\n" \
"v_accvgpr_read_b32 v167, a135\n" \
"v_accvgpr_read_b32 v168, a136\n" \
"v_accvgpr_read_b32 v169, a137\n" \
"v_accvgpr_read_b32 v170, a138\n" \
"v_accvgpr_read_b32 v171, a139\n" \
"v_accvgpr_read_b32 v172, a140\n" \
"v_accvgpr_read_b32 v173, a141\n" \
"v_accvgpr_read_b32 v174, a142\n" \
"v_accvgpr_read_b32 v175, a143\n" \
"s_add_u32 s83, s85, 0\n" \
"s_add_u32 s84, s87, 0\n" \
"s_call_b64 s[64:65], Lex1_%=\n" \
"v_accvgpr_read_b32 v160, a144\n" \
"v_accvgpr_read_b32 v161, a145\n" \
"v_accvgpr_read_b32 v162, a146\n" \
"v_accvgpr_read_b32 v163, a147\n" \
"v_accvgpr_read_b32 v164, a148\n" \
"v_accvgpr_read_b32 v165, a149\n" \
"v_accvgpr_read_b32 v166, a150\n" \
"v_accvgpr_read_b32 v167, a151\n" \
"v_accvgpr_read_b32 v168, a152\n" \
"v_accvgpr_read_b32 v169, a153\n" \
"v_accvgpr_read_b32 v170, a154\n" \
"v_accvgpr_read_b32 v171, a155\n" \
"v_accvgpr_read_b32 v172, a156\n" \
"v_accvgpr_read_b32 v173, a157\n" \
"v_accvgpr_read_b32 v174, a158\n" \
"v_accvgpr_read_b32 v175, a159\n" \
"s_add_u32 s83, s85, 2\n" \
"s_add_u32 s84, s87, 532480\n" \
"s_call_b64 s[64:65], Lex1_%=\n" \
"v_accvgpr_read_b32 v160, a160\n" \
"v_accvgpr_read_b32 v161, a161\n" \
"v_accvgpr_read_b32 v162, a162\n" \
"v_accvgpr_read_b32 v163, a163\n" \
"v_accvgpr_read_b32 v164, a164\n" \
"v_accvgpr_read_b32 v165, a165\n" \
"v_accvgpr_read_b32 v166, a166\n" \
"v_accvgpr_read_b32 v167, a167\n" \
"v_accvgpr_read_b32 v168, a168\n" \
"v_accvgpr_read_b32 v169, a169\n" \
"v_accvgpr_read_b32 v170, a170\n" \
"v_accvgpr_read_b32 v171, a171\n" \
"v_accvgpr_read_b32 v172, a172\n" \
"v_accvgpr_read_b32 v173, a173\n" \
"v_accvgpr_read_b32 v174, a174\n" \
"v_accvgpr_read_b32 v175, a175\n" \
"s_add_u32 s83, s86, 0\n" \
"s_add_u32 s84, s88, 0\n" \
"s_call_b64 s[64:65], Lex1_%=\n" \
"v_accvgpr_read_b32 v160, a176\n" \
"v_accvgpr_read_b32 v161, a177\n" \
"v_accvgpr_read_b32 v162, a178\n" \
"v_accvgpr_read_b32 v163, a179\n" \
"v_accvgpr_read_b32 v164, a180\n" \
"v_accvgpr_read_b32 v165, a181\n" \
"v_accvgpr_read_b32 v166, a182\n" \
"v_accvgpr_read_b32 v167, a183\n" \
"v_accvgpr_read_b32 v168, a184\n" \
"v_accvgpr_read_b32 v169, a185\n" \
"v_accvgpr_read_b32 v170, a186\n" \
"v_accvgpr_read_b32 v171, a187\n" \
"v_accvgpr_read_b32 v172, a188\n" \
"v_accvgpr_read_b32 v173, a189\n" \
"v_accvgpr_read_b32 v174, a190\n" \
"v_accvgpr_read_b32 v175, a191\n" \
"s_add_u32 s83, s86, 2\n" \
"s_add_u32 s84, s88, 532480\n" \
"s_call_b64 s[64:65], Lex1_%=\n" \
"s_waitcnt vmcnt(0)\n" \
"s_branch Lexr_%=\n" \
"Lex1_%=:\n" \
"v_mov_b32 v128, 0\n" \
"v_mov_b32 v129, 0\n" \
"v_mov_b32 v130, 0\n" \
"v_mov_b32 v131, 0\n" \
"v_mov_b32 v132, 0\n" \
"v_mov_b32 v133, 0\n" \
"v_mov_b32 v134, 0\n" \
"v_mov_b32 v135, 0\n" \
"v_mov_b32 v136, 0\n" \
"v_mov_b32 v137, 0\n" \
"v_mov_b32 v138, 0\n" \
"v_mov_b32 v139, 0\n" \
"v_mov_b32 v140, 0\n" \
"v_mov_b32 v141, 0\n" \
"v_mov_b32 v142, 0\n" \
"v_mov_b32 v143, 0\n" \
"v_mov_b32 v144, 0\n" \
"v_mov_b32 v145, 0\n" \
"v_mov_b32 v146, 0\n" \
"v_mov_b32 v147, 0\n" \
"v_mov_b32 v148, 0\n" \
"v_mov_b32 v149, 0\n" \
"v_mov_b32 v150, 0\n" \
"v_mov_b32 v151, 0\n" \
"v_mov_b32 v152, 0\n" \
"v_mov_b32 v153, 0\n" \
"v_mov_b32 v154, 0\n" \
"v_mov_b32 v155, 0\n" \
"v_mov_b32 v156, 0\n" \
"v_mov_b32 v157, 0\n" \
"v_mov_b32 v158, 0\n" \
"v_mov_b32 v159, 0\n" \
"v_mov_b32 v184, 0xf149f2ca\n" \
"v_mov_b32 v185, 0\n" \
"s_mov_b32 s81, 0\n" \
"s_mul_i32 s82, s62, 557824\n" \
"Lex2_%=:\n" \
"s_add_u32 s70, s82, 4096\n" \
"buffer_load_dwordx4 a[64:67], v236, s[56:59], s82 offen offset:0 sc1\n" \
"buffer_load_dwordx4 a[68:71], v236, s[56:59], s82 offen offset:2048 sc1\n" \
"buffer_load_dwordx4 a[72:75], v236, s[56:59], s70 offen offset:0 sc1\n" \
"buffer_load_dwordx4 a[76:79], v236, s[56:59], s70 offen offset:2048 sc1\n" \
"buffer_load_dwordx4 a[80:83], v236, s[56:59], s82 offen offset:256 sc1\n" \
"buffer_load_dwordx4 a[84:87], v236, s[56:59], s82 offen offset:2304 sc1\n" \
"buffer_load_dwordx4 a[88:91], v236, s[56:59], s70 offen offset:256 sc1\n" \
"buffer_load_dwordx4 a[92:95], v236, s[56:59], s70 offen offset:2304 sc1\n" \
"s_add_u32 s70, s82, 17850368\n" \
"s_add_u32 s71, s70, 4096\n" \
"buffer_load_dwordx4 a[96:99], v234, s[56:59], s70 offen offset:0 sc1\n" \
"buffer_load_dwordx4 a[100:103], v234, s[56:59], s70 offen offset:1024 sc1\n" \
"buffer_load_dwordx4 a[104:107], v234, s[56:59], s70 offen offset:2048 sc1\n" \
"buffer_load_dwordx4 a[108:111], v234, s[56:59], s70 offen offset:3072 sc1\n" \
"buffer_load_dwordx4 a[112:115], v234, s[56:59], s71 offen offset:0 sc1\n" \
"buffer_load_dwordx4 a[116:119], v234, s[56:59], s71 offen offset:1024 sc1\n" \
"buffer_load_dwordx4 a[120:123], v234, s[56:59], s71 offen offset:2048 sc1\n" \
"buffer_load_dwordx4 a[124:127], v234, s[56:59], s71 offen offset:3072 sc1\n" \
"s_waitcnt vmcnt(0)\n" \
"v_mfma_f32_16x16x32_bf16 v[176:179], a[64:67], v[160:163], 0\n" \
"v_mfma_f32_16x16x32_bf16 v[176:179], a[68:71], v[164:167], v[176:179]\n" \
"v_mfma_f32_16x16x32_bf16 v[176:179], a[72:75], v[168:171], v[176:179]\n" \
"v_mfma_f32_16x16x32_bf16 v[176:179], a[76:79], v[172:175], v[176:179]\n" \
"v_mfma_f32_16x16x32_bf16 v[180:183], a[80:83], v[160:163], 0\n" \
"v_mfma_f32_16x16x32_bf16 v[180:183], a[84:87], v[164:167], v[180:183]\n" \
"v_mfma_f32_16x16x32_bf16 v[180:183], a[88:91], v[168:171], v[180:183]\n" \
"v_mfma_f32_16x16x32_bf16 v[180:183], a[92:95], v[172:175], v[180:183]\n" \
"s_nop 7\n" \
"s_nop 1\n" \
"s_cmp_eq_u32 s81, s83\n" \
"s_cbranch_scc0 Lex3_%=\n" \
"v_cmp_gt_i32 vcc, 0, v186\n" \
"s_nop 1\n" \
"v_cndmask_b32 v176, v176, v239, vcc\n" \
"v_cmp_gt_i32 vcc, 1, v186\n" \
"s_nop 1\n" \
"v_cndmask_b32 v177, v177, v239, vcc\n" \
"v_cmp_gt_i32 vcc, 2, v186\n" \
"s_nop 1\n" \
"v_cndmask_b32 v178, v178, v239, vcc\n" \
"v_cmp_gt_i32 vcc, 3, v186\n" \
"s_nop 1\n" \
"v_cndmask_b32 v179, v179, v239, vcc\n" \
"v_cmp_gt_i32 vcc, 16, v186\n" \
"s_nop 1\n" \
"v_cndmask_b32 v180, v180, v239, vcc\n" \
"v_cmp_gt_i32 vcc, 17, v186\n" \
"s_nop 1\n" \
"v_cndmask_b32 v181, v181, v239, vcc\n" \
"v_cmp_gt_i32 vcc, 18, v186\n" \
"s_nop 1\n" \
"v_cndmask_b32 v182, v182, v239, vcc\n" \
"v_cmp_gt_i32 vcc, 19, v186\n" \
"s_nop 1\n" \
"v_cndmask_b32 v183, v183, v239, vcc\n" \
"Lex3_%=:\n" \
"v_max3_f32 v187, v176, v177, v178\n" \
"v_max3_f32 v187, v187, v179, v180\n" \
"v_max3_f32 v187, v187, v181, v182\n" \
"v_max3_f32 v187, v187, v183, v184\n" \
"v_mov_b32 v188, v187\n" \
"s_nop 1\n" \
"v_permlane16_swap_b32 v188, v187\n" \
"v_max_f32 v187, v187, v188\n" \
"v_mov_b32 v188, v187\n" \
"s_nop 1\n" \
"v_permlane32_swap_b32 v188, v187\n" \
"v_max_f32 v187, v187, v188\n" \
"v_sub_f32 v189, v184, v187\n" \
"v_exp_f32 v189, v189\n" \
"v_mov_b32 v184, v187\n" \
"v_sub_f32 v176, v176, v187\n" \
"v_sub_f32 v177, v177, v187\n" \
"v_sub_f32 v178, v178, v187\n" \
"v_sub_f32 v179, v179, v187\n" \
"v_sub_f32 v180, v180, v187\n" \
"v_sub_f32 v181, v181, v187\n" \
"v_sub_f32 v182, v182, v187\n" \
"v_sub_f32 v183, v183, v187\n" \
"v_exp_f32 v176, v176\n" \
"v_exp_f32 v177, v177\n" \
"v_exp_f32 v178, v178\n" \
"v_exp_f32 v179, v179\n" \
"v_exp_f32 v180, v180\n" \
"v_exp_f32 v181, v181\n" \
"v_exp_f32 v182, v182\n" \
"v_exp_f32 v183, v183\n" \
"v_mul_f32 v185, v185, v189\n" \
"v_add_f32 v185, v185, v176\n" \
"v_add_f32 v185, v185, v177\n" \
"v_add_f32 v185, v185, v178\n" \
"v_add_f32 v185, v185, v179\n" \
"v_add_f32 v185, v185, v180\n" \
"v_add_f32 v185, v185, v181\n" \
"v_add_f32 v185, v185, v182\n" \
"v_add_f32 v185, v185, v183\n" \
"v_cvt_pk_bf16_f32 v112, v176, v177\n" \
"v_cvt_pk_bf16_f32 v113, v178, v179\n" \
"v_cvt_pk_bf16_f32 v114, v180, v181\n" \
"v_cvt_pk_bf16_f32 v115, v182, v183\n" \
"v_mul_f32 v128, v128, v189\n" \
"v_mul_f32 v129, v129, v189\n" \
"v_mul_f32 v130, v130, v189\n" \
"v_mul_f32 v131, v131, v189\n" \
"v_mul_f32 v132, v132, v189\n" \
"v_mul_f32 v133, v133, v189\n" \
"v_mul_f32 v134, v134, v189\n" \
"v_mul_f32 v135, v135, v189\n" \
"v_mul_f32 v136, v136, v189\n" \
"v_mul_f32 v137, v137, v189\n" \
"v_mul_f32 v138, v138, v189\n" \
"v_mul_f32 v139, v139, v189\n" \
"v_mul_f32 v140, v140, v189\n" \
"v_mul_f32 v141, v141, v189\n" \
"v_mul_f32 v142, v142, v189\n" \
"v_mul_f32 v143, v143, v189\n" \
"v_mul_f32 v144, v144, v189\n" \
"v_mul_f32 v145, v145, v189\n" \
"v_mul_f32 v146, v146, v189\n" \
"v_mul_f32 v147, v147, v189\n" \
"v_mul_f32 v148, v148, v189\n" \
"v_mul_f32 v149, v149, v189\n" \
"v_mul_f32 v150, v150, v189\n" \
"v_mul_f32 v151, v151, v189\n" \
"v_mul_f32 v152, v152, v189\n" \
"v_mul_f32 v153, v153, v189\n" \
"v_mul_f32 v154, v154, v189\n" \
"v_mul_f32 v155, v155, v189\n" \
"v_mul_f32 v156, v156, v189\n" \
"v_mul_f32 v157, v157, v189\n" \
"v_mul_f32 v158, v158, v189\n" \
"v_mul_f32 v159, v159, v189\n" \
"s_nop 1\n" \
"v_mfma_f32_16x16x32_bf16 v[128:131], a[96:99], v[112:115], v[128:131]\n" \
"v_mfma_f32_16x16x32_bf16 v[132:135], a[100:103], v[112:115], v[132:135]\n" \
"v_mfma_f32_16x16x32_bf16 v[136:139], a[104:107], v[112:115], v[136:139]\n" \
"v_mfma_f32_16x16x32_bf16 v[140:143], a[108:111], v[112:115], v[140:143]\n" \
"v_mfma_f32_16x16x32_bf16 v[144:147], a[112:115], v[112:115], v[144:147]\n" \
"v_mfma_f32_16x16x32_bf16 v[148:151], a[116:119], v[112:115], v[148:151]\n" \
"v_mfma_f32_16x16x32_bf16 v[152:155], a[120:123], v[112:115], v[152:155]\n" \
"v_mfma_f32_16x16x32_bf16 v[156:159], a[124:127], v[112:115], v[156:159]\n" \
"s_nop 7\n" \
"s_nop 1\n" \
"s_and_b32 s70, s81, 1\n" \
"s_mul_i32 s70, s70, 1024\n" \
"s_add_u32 s82, s82, s70\n" \
"s_add_u32 s82, s82, 8192\n" \
"s_add_u32 s81, s81, 1\n" \
"s_cmp_le_u32 s81, s83\n" \
"s_cbranch_scc1 Lex2_%=\n" \
"v_mov_b32 v187, v185\n" \
"s_nop 1\n" \
"v_permlane16_swap_b32 v187, v185\n" \
"v_add_f32 v185, v185, v187\n" \
"v_mov_b32 v187, v185\n" \
"s_nop 1\n" \
"v_permlane32_swap_b32 v187, v185\n" \
"v_add_f32 v185, v185, v187\n" \
"v_rcp_f32 v185, v185\n" \
"s_nop 0\n" \
"v_mul_f32 v128, v128, v185\n" \
"v_mul_f32 v129, v129, v185\n" \
"v_mul_f32 v130, v130, v185\n" \
"v_mul_f32 v131, v131, v185\n" \
"v_mul_f32 v132, v132, v185\n" \
"v_mul_f32 v133, v133, v185\n" \
"v_mul_f32 v134, v134, v185\n" \
"v_mul_f32 v135, v135, v185\n" \
"v_mul_f32 v136, v136, v185\n" \
"v_mul_f32 v137, v137, v185\n" \
"v_mul_f32 v138, v138, v185\n" \
"v_mul_f32 v139, v139, v185\n" \
"v_mul_f32 v140, v140, v185\n" \
"v_mul_f32 v141, v141, v185\n" \
"v_mul_f32 v142, v142, v185\n" \
"v_mul_f32 v143, v143, v185\n" \
"v_mul_f32 v144, v144, v185\n" \
"v_mul_f32 v145, v145, v185\n" \
"v_mul_f32 v146, v146, v185\n" \
"v_mul_f32 v147, v147, v185\n" \
"v_mul_f32 v148, v148, v185\n" \
"v_mul_f32 v149, v149, v185\n" \
"v_mul_f32 v150, v150, v185\n" \
"v_mul_f32 v151, v151, v185\n" \
"v_mul_f32 v152, v152, v185\n" \
"v_mul_f32 v153, v153, v185\n" \
"v_mul_f32 v154, v154, v185\n" \
"v_mul_f32 v155, v155, v185\n" \
"v_mul_f32 v156, v156, v185\n" \
"v_mul_f32 v157, v157, v185\n" \
"v_mul_f32 v158, v158, v185\n" \
"v_mul_f32 v159, v159, v185\n" \
"s_add_u32 s70, s84, 4096\n" \
"v_cvt_pk_bf16_f32 v112, v128, v129\n" \
"v_cvt_pk_bf16_f32 v113, v130, v131\n" \
"v_cvt_pk_bf16_f32 v114, v132, v133\n" \
"v_cvt_pk_bf16_f32 v115, v134, v135\n" \
"buffer_store_dwordx4 v[112:115], v236, s[56:59], s84 offen offset:0 sc1\n" \
"v_cvt_pk_bf16_f32 v116, v136, v137\n" \
"v_cvt_pk_bf16_f32 v117, v138, v139\n" \
"v_cvt_pk_bf16_f32 v118, v140, v141\n" \
"v_cvt_pk_bf16_f32 v119, v142, v143\n" \
"buffer_store_dwordx4 v[116:119], v236, s[56:59], s84 offen offset:2048 sc1\n" \
"v_cvt_pk_bf16_f32 v120, v144, v145\n" \
"v_cvt_pk_bf16_f32 v121, v146, v147\n" \
"v_cvt_pk_bf16_f32 v122, v148, v149\n" \
"v_cvt_pk_bf16_f32 v123, v150, v151\n" \
"buffer_store_dwordx4 v[120:123], v236, s[56:59], s70 offen offset:0 sc1\n" \
"v_cvt_pk_bf16_f32 v124, v152, v153\n" \
"v_cvt_pk_bf16_f32 v125, v154, v155\n" \
"v_cvt_pk_bf16_f32 v126, v156, v157\n" \
"v_cvt_pk_bf16_f32 v127, v158, v159\n" \
"buffer_store_dwordx4 v[124:127], v236, s[56:59], s70 offen offset:2048 sc1\n" \
"s_waitcnt vmcnt(0)\n" \
"s_setpc_b64 s[64:65]\n" \
"Lpl4_%=:\n" \
"s_mov_b32 s72, 0\n" \
"Lpk4_%=:\n" \
"s_sleep 2\n" \
"buffer_load_dwordx2 v[226:227], v228, s[56:59], s13 offen sc1\n" \
"s_waitcnt vmcnt(0)\n" \
"v_cmp_eq_u32 vcc, s60, v226\n" \
"s_nop 1\n" \
"s_andn2_b32 s70, 0xff, vcc_lo\n" \
"s_cbranch_scc0 Lpr4_%=\n" \
"s_add_u32 s72, s72, 1\n" \
"s_cmp_lt_u32 s72, 0x40000\n" \
"s_cbranch_scc1 Lpk4_%=\n" \
"s_branch Lpr4_%=\n" \
"Lpl5_%=:\n" \
"s_mov_b32 s72, 0\n" \
"Lpk5_%=:\n" \
"s_sleep 2\n" \
"buffer_load_dword v220, v221, s[56:59], s66 offen sc1\n" \
"s_waitcnt vmcnt(0)\n" \
"v_cmp_eq_u32 vcc, s60, v220\n" \
"s_nop 1\n" \
"s_andn2_b32 s70, s65, vcc_lo\n" \
"s_cbranch_scc0 Lpr5_%=\n" \
"s_add_u32 s72, s72, 1\n" \
"s_cmp_lt_u32 s72, 0x40000\n" \
"s_cbranch_scc1 Lpk5_%=\n" \
"s_branch Lpr5_%=\n" \
"Lpl6_%=:\n" \
"s_mov_b32 s72, 0\n" \
"Lpk6_%=:\n" \
"s_sleep 2\n" \
"buffer_load_dword v220, v221, s[56:59], s67 offen sc1\n" \
"s_waitcnt vmcnt(0)\n" \
"v_cmp_eq_u32 vcc, s60, v220\n" \
"s_nop 1\n" \
"s_andn2_b32 s70, s74, vcc_lo\n" \
"s_cbranch_scc0 Lpr6_%=\n" \
"s_add_u32 s72, s72, 1\n" \
"s_cmp_lt_u32 s72, 0x40000\n" \
"s_cbranch_scc1 Lpk6_%=\n" \
"s_branch Lpr6_%=\n" \
"Lpl7_%=:\n" \
"s_mov_b32 s72, 0\n" \
"Lpk7_%=:\n" \
"s_sleep 2\n" \
"buffer_load_dword v220, v241, s[56:59], s64 offen sc1\n" \
"s_waitcnt vmcnt(0)\n" \
"v_cmp_eq_u32 vcc, s60, v220\n" \
"s_nop 1\n" \
"s_andn2_b32 s70, s69, vcc_lo\n" \
"s_cbranch_scc0 Lpr7_%=\n" \
"s_add_u32 s72, s72, 1\n" \
"s_cmp_lt_u32 s72, 0x40000\n" \
"s_cbranch_scc1 Lpk7_%=\n" \
"s_branch Lpr7_%=\n" \
"Lcode_end_%=:\n" \
".p2align 12\n" \
""
#define MK_ASM_CLOBBERS "v1", "v2", "v3", "v4", "v5", "v6", "v7", "v8", "v9", "v10", "v11", "v12", "v13", "v14", "v15", "v16", "v17", "v18", "v19", "v20", "v21", "v22", "v23", "v24", "v25", "v26", "v27", "v28", "v29", "v30", "v31", "v32", "v33", "v34", "v35", "v36", "v37", "v38", "v39", "v40", "v41", "v42", "v43", "v44", "v45", "v46", "v47", "v48", "v49", "v50", "v51", "v52", "v53", "v54", "v55", "v56", "v57", "v58", "v59", "v60", "v61", "v62", "v63", "v64", "v65", "v66", "v67", "v68", "v69", "v70", "v71", "v72", "v73", "v74", "v75", "v76", "v77", "v78", "v79", "v80", "v81", "v82", "v83", "v84", "v85", "v86", "v87", "v88", "v89", "v90", "v91", "v92", "v93", "v94", "v95", "v96", "v97", "v98", "v99", "v100", "v101", "v102", "v103", "v104", "v105", "v106", "v107", "v108", "v109", "v110", "v111", "v112", "v113", "v114", "v115", "v116", "v117", "v118", "v119", "v120", "v121", "v122", "v123", "v124", "v125", "v126", "v127", "v128", "v129", "v130", "v131", "v132", "v133", "v134", "v135", "v136", "v137", "v138", "v139", "v140", "v141", "v142", "v143", "v144", "v145", "v146", "v147", "v148", "v149", "v150", "v151", "v152", "v153", "v154", "v155", "v156", "v157", "v158", "v159", "v160", "v161", "v162", "v163", "v164", "v165", "v166", "v167", "v168", "v169", "v170", "v171", "v172", "v173", "v174", "v175", "v176", "v177", "v178", "v179", "v180", "v181", "v182", "v183", "v184", "v185", "v186", "v187", "v188", "v189", "v190", "v191", "v192", "v193", "v194", "v195", "v196", "v197", "v198", "v199", "v200", "v201", "v202", "v203", "v204", "v205", "v206", "v207", "v208", "v209", "v210", "v211", "v212", "v213", "v214", "v215", "v216", "v217", "v218", "v219", "v220", "v221", "v222", "v223", "v224", "v225", "v226", "v227", "v228", "v229", "v230", "v231", "v232", "v233", "v234", "v235", "v236", "v237", "v238", "v239", "v240", "v241", "v242", "v243", "v244", "v245", "v246", "v247", "v248", "v249", "v250", "v251", "v252", "v253", "v254", "v255", "a0", "a1", "a2", "a3", "a4", "a5", "a6", "a7", "a8", "a9", "a10", "a11", "a12", "a13", "a14", "a15", "a16", "a17", "a18", "a19", "a20", "a21", "a22", "a23", "a24", "a25", "a26", "a27", "a28", "a29", "a30", "a31", "a32", "a33", "a34", "a35", "a36", "a37", "a38", "a39", "a40", "a41", "a42", "a43", "a44", "a45", "a46", "a47", "a48", "a49", "a50", "a51", "a52", "a53", "a54", "a55", "a56", "a57", "a58", "a59", "a60", "a61", "a62", "a63", "a64", "a65", "a66", "a67", "a68", "a69", "a70", "a71", "a72", "a73", "a74", "a75", "a76", "a77", "a78", "a79", "a80", "a81", "a82", "a83", "a84", "a85", "a86", "a87", "a88", "a89", "a90", "a91", "a92", "a93", "a94", "a95", "a96", "a97", "a98", "a99", "a100", "a101", "a102", "a103", "a104", "a105", "a106", "a107", "a108", "a109", "a110", "a111", "a112", "a113", "a114", "a115", "a116", "a117", "a118", "a119", "a120", "a121", "a122", "a123", "a124", "a125", "a126", "a127", "a128", "a129", "a130", "a131", "a132", "a133", "a134", "a135", "a136", "a137", "a138", "a139", "a140", "a141", "a142", "a143", "a144", "a145", "a146", "a147", "a148", "a149", "a150", "a151", "a152", "a153", "a154", "a155", "a156", "a157", "a158", "a159", "a160", "a161", "a162", "a163", "a164", "a165", "a166", "a167", "a168", "a169", "a170", "a171", "a172", "a173", "a174", "a175", "a176", "a177", "a178", "a179", "a180", "a181", "a182", "a183", "a184", "a185", "a186", "a187", "a188", "a189", "a190", "a191", "a192", "a193", "a194", "a195", "a196", "a197", "a198", "a199", "a200", "a201", "a202", "a203", "a204", "a205", "a206", "a207", "a208", "a209", "a210", "a211", "a212", "a213", "a214", "a215", "a216", "a217", "a218", "a219", "a220", "a221", "a222", "a223", "a224", "a225", "a226", "a227", "a228", "a229", "a230", "a231", "a232", "a233", "a234", "a235", "a236", "a237", "a238", "a239", "a240", "a241", "a242", "a243", "a244", "a245", "a246", "a247", "a248", "a249", "a250", "a251", "a252", "a253", "a254", "a255", "s8", "s9", "s10", "s11", "s12", "s13", "s14", "s15", "s16", "s17", "s18", "s19", "s20", "s21", "s22", "s23", "s24", "s25", "s26", "s27", "s28", "s29", "s30", "s31", "s36", "s37", "s38", "s39", "s40", "s41", "s42", "s43", "s44", "s45", "s46", "s47", "s48", "s49", "s50", "s51", "s52", "s53", "s54", "s55", "s56", "s57", "s58", "s59", "s60", "s61", "s62", "s63", "s64", "s65", "s66", "s67", "s68", "s69", "s70", "s71", "s72", "s73", "s74", "s75", "s76", "s77", "s78", "s79", "s80", "s81", "s82", "s83", "s84", "s85", "s86", "s87", "s88", "s89", "s90", "s91", "s92", "s93", "s94", "s95", "vcc", "scc", "memory"
namespace mk {
constexpr int LDS_BYTES = 163840;
struct Args { const void* in[9]; void* out; void* ws; };
__global__ __launch_bounds__(256, 1) void mega_k(const Args a) {
    extern __shared__ __attribute__((aligned(16))) unsigned char shm[];
    const unsigned ldsb = (unsigned)(size_t)(LAS unsigned char*)shm;
    asm volatile(MK_ASM_BODY
                 :
                 : "s"(__builtin_amdgcn_kernarg_segment_ptr()), "s"(blockIdx.x), "v"(threadIdx.x), "s"(ldsb)
                 : MK_ASM_CLOBBERS);
}
}
extern "C" void kernel_launch(void* const* d_in, const int* in_sizes, int n_in, void* d_out, int out_size, void* d_ws, size_t ws_size, hipStream_t stream) {
    (void)hipFuncSetAttribute((const void*)mk::mega_k, hipFuncAttributeMaxDynamicSharedMemorySize, mk::LDS_BYTES);
    mk::Args a;
    for (int i = 0; i < 9; ++i) a.in[i] = d_in[i];
    a.out = d_out; a.ws = (char*)d_ws + ((ws_size - WS_END) & ~(size_t)0x1fffff);
    mk::mega_k<<<256, 256, mk::LDS_BYTES, stream>>>(a);
}
```

```cpp
#include <hip/hip_runtime.h>
#include <stdint.h>
#define LAS __attribute__((address_space(3)))
constexpr size_t LAY_TS = 17408, LAY_HS = 557824, LAY_CRS = 266240, LAY_ORS = 69632, WS_K = 0, WS_V = 17850368, WS_CTX = 35700736, WS_OB = 52740096, WS_FL = 65585152, WS_END = 65634304, LAY_VS = 2;
#define MK_ASM_BODY \
"s_load_dwordx16 s[8:23], %0, 0x0\n" \
"s_load_dwordx4 s[24:27], %0, 0x40\n" \
"s_load_dwordx2 s[28:29], %0, 0x50\n" \
"v_readfirstlane_b32 s61, %2\n" \
"s_lshr_b32 s61, s61, 6\n" \
"v_mbcnt_lo_u32_b32 v78, -1, 0\n" \
"v_mbcnt_hi_u32_b32 v78, -1, v78\n" \
"v_lshlrev_b32 v234, 4, v78\n" \
"s_getpc_b64 s[64:65]\n" \
"Lpc_%=:\n" \
"s_lshr_b32 s70, %1, 3\n" \
"s_lshl_b32 s70, s70, 2\n" \
"s_add_u32 s70, s70, s61\n" \
"s_and_b32 s71, s70, 31\n" \
"s_lshl_b32 s71, s71, 12\n" \
"v_lshl_add_u32 v237, v234, 2, s71\n" \
"s_mov_b32 s71, Lcode_end_%=-Lpc_%=-4\n" \
"v_min_u32 v237, s71, v237\n" \
"global_load_dword v238, v237, s[64:65]\n" \
"s_and_b32 s62, %1, 7\n" \
"s_lshl_b32 s62, s62, 2\n" \
"s_lshr_b32 s70, %1, 6\n" \
"s_add_u32 s62, s62, s70\n" \
"s_bfe_u32 s63, %1, 0x30003\n" \
"s_lshl_b32 s74, s61, 13\n" \
"s_add_u32 s74, s74, %3\n" \
"v_bfe_u32 v79, v78, 4, 1\n" \
"v_bfe_u32 v80, v78, 2, 2\n" \
"v_lshl_add_u32 v80, v79, 3, v80\n" \
"s_lshl_b32 s71, s61, 4\n" \
"v_add_u32 v80, s71, v80\n" \
"v_lshlrev_b32 v80, 13, v80\n" \
"v_lshlrev_b32 v79, 1, v79\n" \
"v_and_b32 v81, 3, v78\n" \
"v_xor_b32 v79, v79, v81\n" \
"v_lshl_add_u32 v80, v79, 4, v80\n" \
"v_lshrrev_b32 v90, 5, v78\n" \
"v_lshl_add_u32 v84, v90, 6, v80\n" \
"v_add_u32 v68, 4096, v84\n" \
"v_add_u32 v69, 3072, v84\n" \
"v_add_u32 v70, 2176, v84\n" \
"v_add_u32 v71, 1152, v84\n" \
"s_lshl_b32 s83, s62, 8\n" \
"s_add_u32 s84, s83, 32768\n" \
"v_lshrrev_b32 v85, 3, v78\n" \
"v_and_b32 v86, 7, v78\n" \
"v_xor_b32 v86, v86, v85\n" \
"v_lshlrev_b32 v86, 4, v86\n" \
"v_lshl_add_u32 v64, v85, 10, v86\n" \
"v_add_u32 v65, 7168, v64\n" \
"v_add_u32 v66, 14336, v64\n" \
"v_add_u32 v67, 21504, v64\n" \
"s_bitcmp1_b32 s61, 0\n" \
"s_cselect_b32 s70, 0, 15\n" \
"s_xor_b32 s81, s63, s70\n" \
"s_lshl_b32 s81, s81, 17\n" \
"s_lshr_b32 s70, s61, 1\n" \
"s_lshl_b32 s71, s70, 16\n" \
"s_add_u32 s81, s81, s71\n" \
"s_add_u32 s82, s81, 32768\n" \
"s_mov_b32 s42, 4198400\n" \
"s_mov_b32 s43, 0x20000\n" \
"s_mov_b32 s46, 4198400\n" \
"s_mov_b32 s47, 0x20000\n" \
"s_mov_b32 s38, 2097152\n" \
"s_mov_b32 s39, 0x20000\n" \
"s_waitcnt lgkmcnt(0)\n" \
"s_mov_b64 s[40:41], s[14:15]\n" \
"s_sub_u32 s40, s40, 4096\n" \
"s_subb_u32 s41, s41, 0\n" \
"s_and_b32 s41, s41, 0xffff\n" \
"s_mov_b64 s[44:45], s[16:17]\n" \
"s_sub_u32 s44, s44, 4096\n" \
"s_subb_u32 s45, s45, 0\n" \
"s_and_b32 s45, s45, 0xffff\n" \
"s_mov_b64 s[36:37], s[10:11]\n" \
"s_and_b32 s37, s37, 0xffff\n" \
"s_add_u32 m0, s74, 32768\n" \
"s_nop 0\n" \
"buffer_load_dwordx4 v68, s[40:43], s83 offen offset:0 lds\n" \
"buffer_load_dwordx4 v69, s[44:47], s83 offen offset:1024 lds\n" \
"buffer_load_dwordx4 v70, s[40:43], s83 offen offset:2048 lds\n" \
"buffer_load_dwordx4 v71, s[44:47], s83 offen offset:3072 lds\n" \
"s_xor_b32 s83, s83, 0x80000\n" \
"s_add_u32 m0, s74, 36864\n" \
"s_nop 0\n" \
"buffer_load_dwordx4 v68, s[40:43], s84 offen offset:0 lds\n" \
"buffer_load_dwordx4 v69, s[44:47], s84 offen offset:1024 lds\n" \
"buffer_load_dwordx4 v70, s[40:43], s84 offen offset:2048 lds\n" \
"buffer_load_dwordx4 v71, s[44:47], s84 offen offset:3072 lds\n" \
"s_xor_b32 s84, s84, 0x80000\n" \
"s_add_u32 m0, s74, 0\n" \
"s_nop 0\n" \
"buffer_load_dwordx4 v64, s[36:39], s81 offen offset:0 lds\n" \
"buffer_load_dwordx4 v65, s[36:39], s81 offen offset:1024 lds\n" \
"buffer_load_dwordx4 v66, s[36:39], s81 offen offset:2048 lds\n" \
"buffer_load_dwordx4 v67, s[36:39], s81 offen offset:3072 lds\n" \
"s_xor_b32 s81, s81, 0x80\n" \
"s_add_u32 m0, s74, 4096\n" \
"s_nop 0\n" \
"buffer_load_dwordx4 v64, s[36:39], s82 offen offset:0 lds\n" \
"buffer_load_dwordx4 v65, s[36:39], s82 offen offset:1024 lds\n" \
"buffer_load_dwordx4 v66, s[36:39], s82 offen offset:2048 lds\n" \
"buffer_load_dwordx4 v67, s[36:39], s82 offen offset:3072 lds\n" \
"s_xor_b32 s82, s82, 0x80\n" \
"v_lshrrev_b32 v87, 4, v78\n" \
"v_and_b32 v79, 15, v78\n" \
"v_and_b32 v80, 7, v78\n" \
"v_xor_b32 v80, v80, v87\n" \
"v_lshlrev_b32 v79, 7, v79\n" \
"s_lshl_b32 s70, s70, 14\n" \
"s_add_u32 s70, s70, %3\n" \
"v_lshl_add_u32 v88, v80, 4, v79\n" \
"v_add_u32 v72, s70, v88\n" \
"v_xor_b32 v73, 64, v72\n" \
"v_lshlrev_b32 v79, 3, v81\n" \
"v_bfe_u32 v80, v78, 2, 2\n" \
"v_lshl_add_u32 v79, v80, 6, v79\n" \
"v_bfe_u32 v80, v78, 4, 1\n" \
"v_lshl_add_u32 v79, v80, 8, v79\n" \
"v_lshl_add_u32 v89, v80, 5, v79\n" \
"s_and_b32 s70, s61, 1\n" \
"s_lshl_b32 s70, s70, 11\n" \
"s_add_u32 s70, s70, %3\n" \
"v_lshl_add_u32 v82, v90, 13, v89\n" \
"v_add_u32 v74, s70, v82\n" \
"v_xor_b32 v75, 32, v74\n" \
"s_mov_b64 s[56:57], s[28:29]\n" \
"s_and_b32 s57, s57, 0xffff\n" \
"s_mov_b32 s58, 65634304\n" \
"s_mov_b32 s59, 0x20000\n" \
"v_mov_b32 v241, 0\n" \
"s_lshl_b32 s66, %1, 2\n" \
"s_add_u32 s66, s66, s61\n" \
"s_lshl_b32 s66, s66, 10\n" \
"s_lshr_b32 s67, %1, 3\n" \
"s_lshl_b32 s67, s67, 2\n" \
"s_add_u32 s67, s67, s61\n" \
"s_lshl_b32 s67, s67, 13\n" \
"s_and_b32 s64, %1, 7\n" \
"s_lshl_b32 s64, s64, 10\n" \
"s_add_u32 s67, s67, s64\n" \
"s_mov_b64 exec, 0\n" \
"s_cmp_eq_u32 s61, 0\n" \
"s_cbranch_scc1 Lst0x0_%=\n" \
"s_cmp_eq_u32 s61, 1\n" \
"s_cbranch_scc1 Lst0x1_%=\n" \
"s_cmp_eq_u32 s61, 2\n" \
"s_cbranch_scc1 Lst0x2_%=\n" \
"s_branch Lst0x3_%=\n" \
"Lcb0_%=:\n" \
"s_mov_b64 exec, -1\n" \
"s_add_u32 m0, s74, 131072\n" \
"s_nop 0\n" \
"buffer_load_dwordx4 v68, s[40:43], s83 offen offset:0 lds\n" \
"buffer_load_dwordx4 v69, s[44:47], s83 offen offset:1024 lds\n" \
"buffer_load_dwordx4 v70, s[40:43], s83 offen offset:2048 lds\n" \
"buffer_load_dwordx4 v71, s[44:47], s83 offen offset:3072 lds\n" \
"s_xor_b32 s83, s83, 0x180000\n" \
"s_add_u32 m0, s74, 135168\n" \
"s_nop 0\n" \
"buffer_load_dwordx4 v68, s[40:43], s84 offen offset:0 lds\n" \
"buffer_load_dwordx4 v69, s[44:47], s84 offen offset:1024 lds\n" \
"buffer_load_dwordx4 v70, s[40:43], s84 offen offset:2048 lds\n" \
"buffer_load_dwordx4 v71, s[44:47], s84 offen offset:3072 lds\n" \
"s_xor_b32 s84, s84, 0x180000\n" \
"s_mov_b32 s93, 65593344\n" \
"buffer_load_dword v240, v241, s[56:59], s93 offen sc1\n" \
"s_mov_b64 s[64:65], s[10:11]\n" \
"s_add_u32 s68, s66, 0x0\n" \
"s_add_u32 s64, s64, s68\n" \
"s_addc_u32 s65, s65, 0\n" \
"global_load_dword v238, v234, s[64:65]\n" \
"s_mov_b64 s[64:65], s[10:11]\n" \
"s_add_u32 s68, s66, 0x100000\n" \
"s_add_u32 s64, s64, s68\n" \
"s_addc_u32 s65, s65, 0\n" \
"global_load_dword v238, v234, s[64:65]\n" \
"s_mov_b64 s[64:65], s[14:15]\n" \
"s_add_u32 s68, s67, 0x100000\n" \
"s_add_u32 s64, s64, s68\n" \
"s_addc_u32 s65, s65, 0\n" \
"global_load_dword v238, v234, s[64:65]\n" \
"s_mov_b64 s[64:65], s[16:17]\n" \
"s_add_u32 s68, s67, 0x100000\n" \
"s_add_u32 s64, s64, s68\n" \
"s_addc_u32 s65, s65, 0\n" \
"global_load_dword v238, v234, s[64:65]\n" \
"s_waitcnt vmcnt(13)\n" \
"s_barrier\n" \
"ds_read_b64_tr_b16 a[64:65], v74 offset:32768\n" \
"ds_read_b64_tr_b16 a[66:67], v74 offset:36864\n" \
"ds_read_b64_tr_b16 a[68:69], v74 offset:49152\n" \
"ds_read_b64_tr_b16 a[70:71], v74 offset:53248\n" \
"ds_read_b128 a[0:3], v72 offset:0\n" \
"ds_read_b128 a[4:7], v73 offset:0\n" \
"ds_read_b64_tr_b16 a[72:73], v75 offset:32768\n" \
"ds_read_b64_tr_b16 a[74:75], v75 offset:36864\n" \
"ds_read_b64_tr_b16 a[76:77], v75 offset:49152\n" \
"ds_read_b64_tr_b16 a[78:79], v75 offset:53248\n" \
"ds_read_b64_tr_b16 a[80:81], v74 offset:33280\n" \
"ds_read_b64_tr_b16 a[82:83], v74 offset:37376\n" \
"ds_read_b64_tr_b16 a[84:85], v74 offset:49664\n" \
"ds_read_b64_tr_b16 a[86:87], v74 offset:53760\n" \
"ds_read_b64_tr_b16 a[88:89], v75 offset:33280\n" \
"ds_read_b64_tr_b16 a[90:91], v75 offset:37376\n" \
"ds_read_b64_tr_b16 a[92:93], v75 offset:49664\n" \
"ds_read_b64_tr_b16 a[94:95], v75 offset:53760\n" \
"ds_read_b128 a[8:11], v72 offset:2048\n" \
"ds_read_b128 a[12:15], v73 offset:2048\n" \
"s_waitcnt lgkmcnt(14)\n" \
"v_mfma_f32_16x16x32_bf16 v[0:3], a[64:67], a[0:3], 0\n" \
"s_add_u32 m0, s74, 65536\n" \
"ds_read_b128 a[16:19], v72 offset:4096\n" \
"v_mfma_f32_16x16x32_bf16 v[0:3], a[68:71], a[4:7], v[0:3]\n" \
"s_waitcnt lgkmcnt(11)\n" \
"buffer_load_dwordx4 v64, s[36:39], s81 offen offset:0 lds\n" \
"v_mfma_f32_16x16x32_bf16 v[4:7], a[72:75], a[0:3], 0\n" \
"ds_read_b128 a[20:23], v73 offset:4096\n" \
"v_mfma_f32_16x16x32_bf16 v[4:7], a[76:79], a[4:7], v[4:7]\n" \
"s_waitcnt lgkmcnt(8)\n" \
"v_mfma_f32_16x16x32_bf16 v[8:11], a[80:83], a[0:3], 0\n" \
"s_waitcnt lgkmcnt(2)\n" \
"ds_read_b128 a[24:27], v72 offset:6144\n" \
"v_mfma_f32_16x16x32_bf16 v[8:11], a[84:87], a[4:7], v[8:11]\n" \
"buffer_load_dwordx4 v65, s[36:39], s81 offen offset:1024 lds\n" \
"v_mfma_f32_16x16x32_bf16 v[12:15], a[88:91], a[0:3], 0\n" \
"ds_read_b128 a[28:31], v73 offset:6144\n" \
"v_mfma_f32_16x16x32_bf16 v[12:15], a[92:95], a[4:7], v[12:15]\n" \
"v_mfma_f32_16x16x32_bf16 v[28:31], a[88:91], a[8:11], 0\n" \
"ds_read_b128 a[32:35], v72 offset:8192\n" \
"v_mfma_f32_16x16x32_bf16 v[28:31], a[92:95], a[12:15], v[28:31]\n" \
"buffer_load_dwordx4 v66, s[36:39], s81 offen offset:2048 lds\n" \
"v_mfma_f32_16x16x32_bf16 v[24:27], a[80:83], a[8:11], 0\n" \
"ds_read_b128 a[36:39], v73 offset:8192\n" \
"v_mfma_f32_16x16x32_bf16 v[24:27], a[84:87], a[12:15], v[24:27]\n" \
"v_mfma_f32_16x16x32_bf16 v[20:23], a[72:75], a[8:11], 0\n" \
"ds_read_b128 a[40:43], v72 offset:10240\n" \
"v_mfma_f32_16x16x32_bf16 v[20:23], a[76:79], a[12:15], v[20:23]\n" \
"buffer_load_dwordx4 v67, s[36:39], s81 offen offset:3072 lds\n" \
"v_mfma_f32_16x16x32_bf16 v[16:19], a[64:67], a[8:11], 0\n" \
"ds_read_b128 a[44:47], v73 offset:10240\n" \
"v_mfma_f32_16x16x32_bf16 v[16:19], a[68:71], a[12:15], v[16:19]\n" \
"s_waitcnt lgkmcnt(2)\n" \
"s_xor_b32 s81, s81, 0x180\n" \
"v_mfma_f32_16x16x32_bf16 v[32:35], a[64:67], a[16:19], 0\n" \
"s_add_u32 m0, s74, 69632\n" \
"ds_read_b128 a[48:51], v72 offset:12288\n" \
"v_mfma_f32_16x16x32_bf16 v[32:35], a[68:71], a[20:23], v[32:35]\n" \
"buffer_load_dwordx4 v64, s[36:39], s82 offen offset:0 lds\n" \
"v_mfma_f32_16x16x32_bf16 v[36:39], a[72:75], a[16:19], 0\n" \
"ds_read_b128 a[52:55], v73 offset:12288\n" \
"v_mfma_f32_16x16x32_bf16 v[36:39], a[76:79], a[20:23], v[36:39]\n" \
"v_mfma_f32_16x16x32_bf16 v[40:43], a[80:83], a[16:19], 0\n" \
"ds_read_b128 a[56:59], v72 offset:14336\n" \
"v_mfma_f32_16x16x32_bf16 v[40:43], a[84:87], a[20:23], v[40:43]\n" \
"buffer_load_dwordx4 v65, s[36:39], s82 offen offset:1024 lds\n" \
"v_mfma_f32_16x16x32_bf16 v[44:47], a[88:91], a[16:19], 0\n" \
"ds_read_b128 a[60:63], v73 offset:14336\n" \
"v_mfma_f32_16x16x32_bf16 v[44:47], a[92:95], a[20:23], v[44:47]\n" \
"v_mfma_f32_16x16x32_bf16 v[60:63], a[88:91], a[24:27], 0\n" \
"ds_read_b64_tr_b16 a[96:97], v74 offset:33792\n" \
"v_mfma_f32_16x16x32_bf16 v[60:63], a[92:95], a[28:31], v[60:63]\n" \
"buffer_load_dwordx4 v66, s[36:39], s82 offen offset:2048 lds\n" \
"v_mfma_f32_16x16x32_bf16 v[56:59], a[80:83], a[24:27], 0\n" \
"ds_read_b64_tr_b16 a[98:99], v74 offset:37888\n" \
"v_mfma_f32_16x16x32_bf16 v[56:59], a[84:87], a[28:31], v[56:59]\n" \
"v_mfma_f32_16x16x32_bf16 v[52:55], a[72:75], a[24:27], 0\n" \
"ds_read_b64_tr_b16 a[100:101], v74 offset:50176\n" \
"v_mfma_f32_16x16x32_bf16 v[52:55], a[76:79], a[28:31], v[52:55]\n" \
"buffer_load_dwordx4 v67, s[36:39], s82 offen offset:3072 lds\n" \
"v_mfma_f32_16x16x32_bf16 v[48:51], a[64:67], a[24:27], 0\n" \
"ds_read_b64_tr_b16 a[102:103], v74 offset:54272\n" \
"v_mfma_f32_16x16x32_bf16 v[48:51], a[68:71], a[28:31], v[48:51]\n" \
"v_mfma_f32_16x16x32_bf16 v[144:147], a[64:67], a[32:35], 0\n" \
"s_xor_b32 s82, s82, 0x180\n" \
"ds_read_b64_tr_b16 a[104:105], v75 offset:33792\n" \
"v_mfma_f32_16x16x32_bf16 v[144:147], a[68:71], a[36:39], v[144:147]\n" \
"v_mfma_f32_16x16x32_bf16 v[148:151], a[72:75], a[32:35], 0\n" \
"ds_read_b64_tr_b16 a[106:107], v75 offset:37888\n" \
"v_mfma_f32_16x16x32_bf16 v[148:151], a[76:79], a[36:39], v[148:151]\n" \
"v_mfma_f32_16x16x32_bf16 v[152:155], a[80:83], a[32:35], 0\n" \
"ds_read_b64_tr_b16 a[108:109], v75 offset:50176\n" \
"v_mfma_f32_16x16x32_bf16 v[152:155], a[84:87], a[36:39], v[152:155]\n" \
"v_mfma_f32_16x16x32_bf16 v[156:159], a[88:91], a[32:35], 0\n" \
"ds_read_b64_tr_b16 a[110:111], v75 offset:54272\n" \
"v_mfma_f32_16x16x32_bf16 v[156:159], a[92:95], a[36:39], v[156:159]\n" \
"s_waitcnt lgkmcnt(2)\n" \
"v_mfma_f32_16x16x32_bf16 v[188:191], a[88:91], a[40:43], 0\n" \
"ds_read_b64_tr_b16 a[112:113], v74 offset:34304\n" \
"v_add_u32 v96, 0x10000, v72\n" \
"v_mfma_f32_16x16x32_bf16 v[188:191], a[92:95], a[44:47], v[188:191]\n" \
"v_mfma_f32_16x16x32_bf16 v[184:187], a[80:83], a[40:43], 0\n" \
"ds_read_b64_tr_b16 a[114:115], v74 offset:38400\n" \
"v_add_u32 v97, 0x10000, v73\n" \
"v_mfma_f32_16x16x32_bf16 v[184:187], a[84:87], a[44:47], v[184:187]\n" \
"v_mfma_f32_16x16x32_bf16 v[180:183], a[72:75], a[40:43], 0\n" \
"ds_read_b64_tr_b16 a[116:117], v74 offset:50688\n" \
"v_add_u32 v98, 0x10000, v74\n" \
"v_mfma_f32_16x16x32_bf16 v[180:183], a[76:79], a[44:47], v[180:183]\n" \
"v_mfma_f32_16x16x32_bf16 v[176:179], a[64:67], a[40:43], 0\n" \
"ds_read_b64_tr_b16 a[118:119], v74 offset:54784\n" \
"v_add_u32 v99, 0x10000, v75\n" \
"v_mfma_f32_16x16x32_bf16 v[176:179], a[68:71], a[44:47], v[176:179]\n" \
"v_mfma_f32_16x16x32_bf16 v[192:195], a[64:67], a[48:51], 0\n" \
"s_add_u32 m0, s74, 98304\n" \
"ds_read_b64_tr_b16 a[120:121], v75 offset:34304\n" \
"v_add_u32 v100, 0x20000, v72\n" \
"v_mfma_f32_16x16x32_bf16 v[192:195], a[68:71], a[52:55], v[192:195]\n" \
"buffer_load_dwordx4 v68, s[40:43], s83 offen offset:0 lds\n" \
"v_mfma_f32_16x16x32_bf16 v[196:199], a[72:75], a[48:51], 0\n" \
"ds_read_b64_tr_b16 a[122:123], v75 offset:38400\n" \
"v_add_u32 v101, 0x20000, v73\n" \
"v_mfma_f32_16x16x32_bf16 v[196:199], a[76:79], a[52:55], v[196:199]\n" \
"v_mfma_f32_16x16x32_bf16 v[200:203], a[80:83], a[48:51], 0\n" \
"ds_read_b64_tr_b16 a[124:125], v75 offset:50688\n" \
"v_add_u32 v102, 0x20000, v74\n" \
"v_mfma_f32_16x16x32_bf16 v[200:203], a[84:87], a[52:55], v[200:203]\n" \
"buffer_load_dwordx4 v69, s[44:47], s83 offen offset:1024 lds\n" \
"v_mfma_f32_16x16x32_bf16 v[204:207], a[88:91], a[48:51], 0\n" \
"ds_read_b64_tr_b16 a[126:127], v75 offset:54784\n" \
"v_add_u32 v103, 0x20000, v75\n" \
"v_mfma_f32_16x16x32_bf16 v[204:207], a[92:95], a[52:55], v[204:207]\n" \
"v_mfma_f32_16x16x32_bf16 v[220:223], a[88:91], a[56:59], 0\n" \
"v_mfma_f32_16x16x32_bf16 v[220:223], a[92:95], a[60:63], v[220:223]\n" \
"buffer_load_dwordx4 v70, s[40:43], s83 offen offset:2048 lds\n" \
"v_mfma_f32_16x16x32_bf16 v[216:219], a[80:83], a[56:59], 0\n" \
"v_mfma_f32_16x16x32_bf16 v[216:219], a[84:87], a[60:63], v[216:219]\n" \
"v_mfma_f32_16x16x32_bf16 v[212:215], a[72:75], a[56:59], 0\n" \
"v_mfma_f32_16x16x32_bf16 v[212:215], a[76:79], a[60:63], v[212:215]\n" \
"buffer_load_dwordx4 v71, s[44:47], s83 offen offset:3072 lds\n" \
"v_mfma_f32_16x16x32_bf16 v[208:211], a[64:67], a[56:59], 0\n" \
"v_mfma_f32_16x16x32_bf16 v[208:211], a[68:71], a[60:63], v[208:211]\n" \
"s_xor_b32 s83, s83, 0x80000\n" \
"s_waitcnt lgkmcnt(0)\n" \
"s_barrier\n" \
"v_mfma_f32_16x16x32_bf16 a[128:131], a[0:3], a[96:99], 0\n" \
"s_add_u32 m0, s74, 102400\n" \
"v_mfma_f32_16x16x32_bf16 a[128:131], a[4:7], a[100:103], a[128:131]\n" \
"buffer_load_dwordx4 v68, s[40:43], s84 offen offset:0 lds\n" \
"v_mfma_f32_16x16x32_bf16 a[132:135], a[0:3], a[104:107], 0\n" \
"v_mfma_f32_16x16x32_bf16 a[132:135], a[4:7], a[108:111], a[132:135]\n" \
"v_mfma_f32_16x16x32_bf16 a[136:139], a[0:3], a[112:115], 0\n" \
"v_mfma_f32_16x16x32_bf16 a[136:139], a[4:7], a[116:119], a[136:139]\n" \
"buffer_load_dwordx4 v69, s[44:47], s84 offen offset:1024 lds\n" \
"v_mfma_f32_16x16x32_bf16 a[140:143], a[0:3], a[120:123], 0\n" \
"v_mfma_f32_16x16x32_bf16 a[140:143], a[4:7], a[124:127], a[140:143]\n" \
"v_mfma_f32_16x16x32_bf16 a[156:159], a[8:11], a[120:123], 0\n" \
"v_mfma_f32_16x16x32_bf16 a[156:159], a[12:15], a[124:127], a[156:159]\n" \
"buffer_load_dwordx4 v70, s[40:43], s84 offen offset:2048 lds\n" \
"v_mfma_f32_16x16x32_bf16 a[152:155], a[8:11], a[112:115], 0\n" \
"v_mfma_f32_16x16x32_bf16 a[152:155], a[12:15], a[116:119], a[152:155]\n" \
"v_mfma_f32_16x16x32_bf16 a[148:151], a[8:11], a[104:107], 0\n" \
"v_mfma_f32_16x16x32_bf16 a[148:151], a[12:15], a[108:111], a[148:151]\n" \
"buffer_load_dwordx4 v71, s[44:47], s84 offen offset:3072 lds\n" \
"v_mfma_f32_16x16x32_bf16 a[144:147], a[8:11], a[96:99], 0\n" \
"v_mfma_f32_16x16x32_bf16 a[144:147], a[12:15], a[100:103], a[144:147]\n" \
"v_mfma_f32_16x16x32_bf16 a[160:163], a[16:19], a[96:99], 0\n" \
"s_xor_b32 s84, s84, 0x80000\n" \
"v_mfma_f32_16x16x32_bf16 a[160:163], a[20:23], a[100:103], a[160:163]\n" \
"v_mfma_f32_16x16x32_bf16 a[164:167], a[16:19], a[104:107], 0\n" \
"v_mfma_f32_16x16x32_bf16 a[164:167], a[20:23], a[108:111], a[164:167]\n" \
"v_mfma_f32_16x16x32_bf16 a[168:171], a[16:19], a[112:115], 0\n" \
"s_add_u32 m0, s74, 32768\n" \
"v_mfma_f32_16x16x32_bf16 a[168:171], a[20:23], a[116:119], a[168:171]\n" \
"buffer_load_dwordx4 v64, s[36:39], s81 offen offset:0 lds\n" \
"v_mfma_f32_16x16x32_bf16 a[172:175], a[16:19], a[120:123], 0\n" \
"v_mfma_f32_16x16x32_bf16 a[172:175], a[20:23], a[124:127], a[172:175]\n" \
"v_mfma_f32_16x16x32_bf16 a[188:191], a[24:27], a[120:123], 0\n" \
"v_mfma_f32_16x16x32_bf16 a[188:191], a[28:31], a[124:127], a[188:191]\n" \
"buffer_load_dwordx4 v65, s[36:39], s81 offen offset:1024 lds\n" \
"v_mfma_f32_16x16x32_bf16 a[184:187], a[24:27], a[112:115], 0\n" \
"v_mfma_f32_16x16x32_bf16 a[184:187], a[28:31], a[116:119], a[184:187]\n" \
"v_mfma_f32_16x16x32_bf16 a[180:183], a[24:27], a[104:107], 0\n" \
"v_mfma_f32_16x16x32_bf16 a[180:183], a[28:31], a[108:111], a[180:183]\n" \
"buffer_load_dwordx4 v66, s[36:39], s81 offen offset:2048 lds\n" \
"v_mfma_f32_16x16x32_bf16 a[176:179], a[24:27], a[96:99], 0\n" \
"v_mfma_f32_16x16x32_bf16 a[176:179], a[28:31], a[100:103], a[176:179]\n" \
"v_mfma_f32_16x16x32_bf16 a[192:195], a[32:35], a[96:99], 0\n" \
"v_mfma_f32_16x16x32_bf16 a[192:195], a[36:39], a[100:103], a[192:195]\n" \
"buffer_load_dwordx4 v67, s[36:39], s81 offen offset:3072 lds\n" \
"v_mfma_f32_16x16x32_bf16 a[196:199], a[32:35], a[104:107], 0\n" \
"v_mfma_f32_16x16x32_bf16 a[196:199], a[36:39], a[108:111], a[196:199]\n" \
"s_xor_b32 s81, s81, 0x80\n" \
"v_mfma_f32_16x16x32_bf16 a[200:203], a[32:35], a[112:115], 0\n" \
"s_add_u32 m0, s74, 36864\n" \
"v_mfma_f32_16x16x32_bf16 a[200:203], a[36:39], a[116:119], a[200:203]\n" \
"buffer_load_dwordx4 v64, s[36:39], s82 offen offset:0 lds\n" \
"v_mfma_f32_16x16x32_bf16 a[204:207], a[32:35], a[120:123], 0\n" \
"v_mfma_f32_16x16x32_bf16 a[204:207], a[36:39], a[124:127], a[204:207]\n" \
"s_waitcnt vmcnt(13) lgkmcnt(0)\n" \
"s_barrier\n" \
"v_mfma_f32_16x16x32_bf16 a[220:223], a[40:43], a[120:123], 0\n" \
"ds_read_b64_tr_b16 a[64:65], v102 offset:0\n" \
"ds_read_b64_tr_b16 a[66:67], v102 offset:4096\n" \
"v_mfma_f32_16x16x32_bf16 a[220:223], a[44:47], a[124:127], a[220:223]\n" \
"buffer_load_dwordx4 v65, s[36:39], s82 offen offset:1024 lds\n" \
"v_mfma_f32_16x16x32_bf16 a[216:219], a[40:43], a[112:115], 0\n" \
"ds_read_b64_tr_b16 a[68:69], v102 offset:16384\n" \
"ds_read_b64_tr_b16 a[70:71], v102 offset:20480\n" \
"v_mfma_f32_16x16x32_bf16 a[216:219], a[44:47], a[116:119], a[216:219]\n" \
"ds_read_b128 a[0:3], v96 offset:0\n" \
"ds_read_b128 a[4:7], v97 offset:0\n" \
"v_mfma_f32_16x16x32_bf16 a[212:215], a[40:43], a[104:107], 0\n" \
"ds_read_b64_tr_b16 a[72:73], v103 offset:0\n" \
"ds_read_b64_tr_b16 a[74:75], v103 offset:4096\n" \
"v_mfma_f32_16x16x32_bf16 a[212:215], a[44:47], a[108:111], a[212:215]\n" \
"buffer_load_dwordx4 v66, s[36:39], s82 offen offset:2048 lds\n" \
"v_mfma_f32_16x16x32_bf16 a[208:211], a[40:43], a[96:99], 0\n" \
"ds_read_b64_tr_b16 a[76:77], v103 offset:16384\n" \
"ds_read_b64_tr_b16 a[78:79], v103 offset:20480\n" \
"v_mfma_f32_16x16x32_bf16 a[208:211], a[44:47], a[100:103], a[208:211]\n" \
"ds_read_b64_tr_b16 a[80:81], v102 offset:512\n" \
"ds_read_b64_tr_b16 a[82:83], v102 offset:4608\n" \
"v_mfma_f32_16x16x32_bf16 a[224:227], a[48:51], a[96:99], 0\n" \
"ds_read_b64_tr_b16 a[84:85], v102 offset:16896\n" \
"ds_read_b64_tr_b16 a[86:87], v102 offset:20992\n" \
"v_mfma_f32_16x16x32_bf16 a[224:227], a[52:55], a[100:103], a[224:227]\n" \
"buffer_load_dwordx4 v67, s[36:39], s82 offen offset:3072 lds\n" \
"v_mfma_f32_16x16x32_bf16 a[228:231], a[48:51], a[104:107], 0\n" \
"s_mov_b64 s[64:65], s[14:15]\n" \
"s_add_u32 s68, s67, 0x200000\n" \
"ds_read_b64_tr_b16 a[88:89], v103 offset:512\n" \
"ds_read_b64_tr_b16 a[90:91], v103 offset:4608\n" \
"v_mfma_f32_16x16x32_bf16 a[228:231], a[52:55], a[108:111], a[228:231]\n" \
"s_add_u32 s64, s64, s68\n" \
"s_addc_u32 s65, s65, 0\n" \
"ds_read_b64_tr_b16 a[92:93], v103 offset:16896\n" \
"ds_read_b64_tr_b16 a[94:95], v103 offset:20992\n" \
"v_mfma_f32_16x16x32_bf16 a[232:235], a[48:51], a[112:115], 0\n" \
"s_xor_b32 s82, s82, 0x80\n" \
"ds_read_b128 a[8:11], v96 offset:2048\n" \
"ds_read_b128 a[12:15], v97 offset:2048\n" \
"v_mfma_f32_16x16x32_bf16 a[232:235], a[52:55], a[116:119], a[232:235]\n" \
"global_load_dword v238, v234, s[64:65]\n" \
"v_mfma_f32_16x16x32_bf16 a[236:239], a[48:51], a[120:123], 0\n" \
"s_mov_b64 s[64:65], s[16:17]\n" \
"s_add_u32 s68, s67, 0x200000\n" \
"ds_read_b128 a[16:19], v96 offset:4096\n" \
"ds_read_b128 a[20:23], v97 offset:4096\n" \
"v_mfma_f32_16x16x32_bf16 a[236:239], a[52:55], a[124:127], a[236:239]\n" \
"s_add_u32 s64, s64, s68\n" \
"s_addc_u32 s65, s65, 0\n" \
"ds_read_b128 a[24:27], v96 offset:6144\n" \
"ds_read_b128 a[28:31], v97 offset:6144\n" \
"v_mfma_f32_16x16x32_bf16 a[252:255], a[56:59], a[120:123], 0\n" \
"ds_read_b128 a[32:35], v96 offset:8192\n" \
"ds_read_b128 a[36:39], v97 offset:8192\n" \
"v_mfma_f32_16x16x32_bf16 a[252:255], a[60:63], a[124:127], a[252:255]\n" \
"global_load_dword v238, v234, s[64:65]\n" \
"v_mfma_f32_16x16x32_bf16 a[248:251], a[56:59], a[112:115], 0\n" \
"ds_read_b128 a[40:43], v96 offset:10240\n" \
"ds_read_b128 a[44:47], v97 offset:10240\n" \
"v_mfma_f32_16x16x32_bf16 a[248:251], a[60:63], a[116:119], a[248:251]\n" \
"ds_read_b128 a[48:51], v96 offset:12288\n" \
"ds_read_b128 a[52:55], v97 offset:12288\n" \
"v_mfma_f32_16x16x32_bf16 a[244:247], a[56:59], a[104:107], 0\n" \
"v_mfma_f32_16x16x32_bf16 a[244:247], a[60:63], a[108:111], a[244:247]\n" \
"v_mfma_f32_16x16x32_bf16 a[240:243], a[56:59], a[96:99], 0\n" \
"v_mfma_f32_16x16x32_bf16 a[240:243], a[60:63], a[100:103], a[240:243]\n" \
"s_waitcnt lgkmcnt(2)\n" \
"v_mfma_f32_16x16x32_bf16 v[0:3], a[64:67], a[0:3], v[0:3]\n" \
"s_lshl_b32 s79, s61, 12\n" \
"ds_read_b128 a[56:59], v96 offset:14336\n" \
"v_mfma_f32_16x16x32_bf16 v[0:3], a[68:71], a[4:7], v[0:3]\n" \
"s_add_u32 s79, s79, %3\n" \
"v_add_u32 v228, 4096, v84\n" \
"v_add_u32 v229, 3200, v84\n" \
"v_mfma_f32_16x16x32_bf16 v[4:7], a[72:75], a[0:3], v[4:7]\n" \
"s_add_u32 m0, s74, 0\n" \
"ds_read_b128 a[60:63], v97 offset:14336\n" \
"v_mfma_f32_16x16x32_bf16 v[4:7], a[76:79], a[4:7], v[4:7]\n" \
"buffer_load_dwordx4 v68, s[40:43], s83 offen offset:0 lds\n" \
"v_mfma_f32_16x16x32_bf16 v[8:11], a[80:83], a[0:3], v[8:11]\n" \
"s_lshl_b32 s78, s62, 8\n" \
"ds_read_b64_tr_b16 a[96:97], v102 offset:1024\n" \
"v_mfma_f32_16x16x32_bf16 v[8:11], a[84:87], a[4:7], v[8:11]\n" \
"s_add_u32 s89, s78, 30720\n" \
"v_mfma_f32_16x16x32_bf16 v[12:15], a[88:91], a[0:3], v[12:15]\n" \
"s_mov_b64 s[48:49], s[8:9]\n" \
"ds_read_b64_tr_b16 a[98:99], v102 offset:5120\n" \
"v_mfma_f32_16x16x32_bf16 v[12:15], a[92:95], a[4:7], v[12:15]\n" \
"buffer_load_dwordx4 v69, s[44:47], s83 offen offset:1024 lds\n" \
"v_mfma_f32_16x16x32_bf16 v[28:31], a[88:91], a[8:11], v[28:31]\n" \
"s_and_b32 s49, s49, 0xffff\n" \
"ds_read_b64_tr_b16 a[100:101], v102 offset:17408\n" \
"v_mfma_f32_16x16x32_bf16 v[28:31], a[92:95], a[12:15], v[28:31]\n" \
"s_mov_b32 s50, 4194304\n" \
"v_mfma_f32_16x16x32_bf16 v[24:27], a[80:83], a[8:11], v[24:27]\n" \
"s_mov_b32 s51, 0x20000\n" \
"ds_read_b64_tr_b16 a[102:103], v102 offset:21504\n" \
"v_mfma_f32_16x16x32_bf16 v[24:27], a[84:87], a[12:15], v[24:27]\n" \
"buffer_load_dwordx4 v70, s[40:43], s83 offen offset:2048 lds\n" \
"v_mfma_f32_16x16x32_bf16 v[20:23], a[72:75], a[8:11], v[20:23]\n" \
"s_mov_b64 s[52:53], s[12:13]\n" \
"ds_read_b64_tr_b16 a[104:105], v103 offset:1024\n" \
"v_mfma_f32_16x16x32_bf16 v[20:23], a[76:79], a[12:15], v[20:23]\n" \
"s_sub_u32 s52, s52, 4096\n" \
"s_subb_u32 s53, s53, 0\n" \
"v_mfma_f32_16x16x32_bf16 v[16:19], a[64:67], a[8:11], v[16:19]\n" \
"s_and_b32 s53, s53, 0xffff\n" \
"ds_read_b64_tr_b16 a[106:107], v103 offset:5120\n" \
"v_mfma_f32_16x16x32_bf16 v[16:19], a[68:71], a[12:15], v[16:19]\n" \
"buffer_load_dwordx4 v71, s[44:47], s83 offen offset:3072 lds\n" \
"v_mfma_f32_16x16x32_bf16 v[32:35], a[64:67], a[16:19], v[32:35]\n" \
"s_mov_b32 s54, 8392704\n" \
"s_mov_b32 s55, 0x20000\n" \
"ds_read_b64_tr_b16 a[108:109], v103 offset:17408\n" \
"v_mfma_f32_16x16x32_bf16 v[32:35], a[68:71], a[20:23], v[32:35]\n" \
"s_xor_b32 s83, s83, 0x380000\n" \
"v_lshl_add_u32 v224, v85, 11, v86\n" \
"v_add_u32 v225, 15360, v224\n" \
"v_mfma_f32_16x16x32_bf16 v[36:39], a[72:75], a[16:19], v[36:39]\n" \
"s_add_u32 m0, s74, 4096\n" \
"ds_read_b64_tr_b16 a[110:111], v103 offset:21504\n" \
"v_add_u32 v226, 129024, v224\n" \
"v_mfma_f32_16x16x32_bf16 v[36:39], a[76:79], a[20:23], v[36:39]\n" \
"buffer_load_dwordx4 v68, s[40:43], s84 offen offset:0 lds\n" \
"v_mfma_f32_16x16x32_bf16 v[40:43], a[80:83], a[16:19], v[40:43]\n" \
"ds_read_b64_tr_b16 a[112:113], v102 offset:1536\n" \
"v_add_u32 v227, 144384, v224\n" \
"v_mfma_f32_16x16x32_bf16 v[40:43], a[84:87], a[20:23], v[40:43]\n" \
"s_lshl_b32 s70, s61, 15\n" \
"v_mfma_f32_16x16x32_bf16 v[44:47], a[88:91], a[16:19], v[44:47]\n" \
"s_lshl_b32 s75, s63, 18\n" \
"ds_read_b64_tr_b16 a[114:115], v102 offset:5632\n" \
"v_mfma_f32_16x16x32_bf16 v[44:47], a[92:95], a[20:23], v[44:47]\n" \
"buffer_load_dwordx4 v69, s[44:47], s84 offen offset:1024 lds\n" \
"v_mfma_f32_16x16x32_bf16 v[60:63], a[88:91], a[24:27], v[60:63]\n" \
"s_sub_u32 s77, 3932160, s75\n" \
"ds_read_b64_tr_b16 a[116:117], v102 offset:17920\n" \
"v_mfma_f32_16x16x32_bf16 v[60:63], a[92:95], a[28:31], v[60:63]\n" \
"s_lshr_b32 s71, %1, 6\n" \
"v_mfma_f32_16x16x32_bf16 v[56:59], a[80:83], a[24:27], v[56:59]\n" \
"s_xor_b32 s71, s71, s63\n" \
"ds_read_b64_tr_b16 a[118:119], v102 offset:22016\n" \
"v_mfma_f32_16x16x32_bf16 v[56:59], a[84:87], a[28:31], v[56:59]\n" \
"buffer_load_dwordx4 v70, s[40:43], s84 offen offset:2048 lds\n" \
"v_mfma_f32_16x16x32_bf16 v[52:55], a[72:75], a[24:27], v[52:55]\n" \
"s_and_b32 s71, s71, 0\n" \
"ds_read_b64_tr_b16 a[120:121], v103 offset:1536\n" \
"v_mfma_f32_16x16x32_bf16 v[52:55], a[76:79], a[28:31], v[52:55]\n" \
"s_lshl_b32 s71, s71, 7\n" \
"v_mfma_f32_16x16x32_bf16 v[48:51], a[64:67], a[24:27], v[48:51]\n" \
"s_add_u32 s70, s70, s71\n" \
"ds_read_b64_tr_b16 a[122:123], v103 offset:5632\n" \
"v_mfma_f32_16x16x32_bf16 v[48:51], a[68:71], a[28:31], v[48:51]\n" \
"buffer_load_dwordx4 v71, s[44:47], s84 offen offset:3072 lds\n" \
"v_mfma_f32_16x16x32_bf16 v[144:147], a[64:67], a[32:35], v[144:147]\n" \
"s_add_u32 s75, s75, s70\n" \
"ds_read_b64_tr_b16 a[124:125], v103 offset:17920\n" \
"v_mfma_f32_16x16x32_bf16 v[144:147], a[68:71], a[36:39], v[144:147]\n" \
"s_add_u32 s77, s77, s70\n" \
"v_mfma_f32_16x16x32_bf16 v[148:151], a[72:75], a[32:35], v[148:151]\n" \
"s_xor_b32 s84, s84, 0x380000\n" \
"ds_read_b64_tr_b16 a[126:127], v103 offset:22016\n" \
"v_mfma_f32_16x16x32_bf16 v[148:151], a[76:79], a[36:39], v[148:151]\n" \
"s_lshl_b32 s71, s71, 12\n" \
"v_mfma_f32_16x16x32_bf16 v[152:155], a[80:83], a[32:35], v[152:155]\n" \
"s_add_u32 s78, s78, s71\n" \
"v_mfma_f32_16x16x32_bf16 v[152:155], a[84:87], a[36:39], v[152:155]\n" \
"s_add_u32 s89, s89, s71\n" \
"v_lshlrev_b32 v242, 2, v78\n" \
"v_and_b32 v236, 0xf0, v234\n" \
"v_mfma_f32_16x16x32_bf16 v[156:159], a[88:91], a[32:35], v[156:159]\n" \
"v_lshl_add_u32 v236, v87, 9, v236\n" \
"v_mfma_f32_16x16x32_bf16 v[156:159], a[92:95], a[36:39], v[156:159]\n" \
"s_add_u32 s71, s74, 0x10000\n" \
"v_add_u32 v230, s71, v88\n" \
"v_xor_b32 v231, 64, v230\n" \
"v_mfma_f32_16x16x32_bf16 v[188:191], a[88:91], a[40:43], v[188:191]\n" \
"v_add_u32 v250, 0x10000, v230\n" \
"v_add_u32 v251, 0x10000, v231\n" \
"v_mfma_f32_16x16x32_bf16 v[188:191], a[92:95], a[44:47], v[188:191]\n" \
"v_lshl_add_u32 v82, v90, 12, v89\n" \
"v_add_u32 v232, %3, v82\n" \
"v_mfma_f32_16x16x32_bf16 v[184:187], a[80:83], a[40:43], v[184:187]\n" \
"v_xor_b32 v233, 32, v232\n" \
"v_mfma_f32_16x16x32_bf16 v[184:187], a[84:87], a[44:47], v[184:187]\n" \
"s_mul_i32 s87, s62, 557824\n" \
"v_mfma_f32_16x16x32_bf16 v[180:183], a[72:75], a[40:43], v[180:183]\n" \
"s_lshr_b32 s70, s61, 1\n" \
"v_mfma_f32_16x16x32_bf16 v[180:183], a[76:79], a[44:47], v[180:183]\n" \
"s_mul_i32 s70, s70, 17408\n" \
"v_mfma_f32_16x16x32_bf16 v[176:179], a[64:67], a[40:43], v[176:179]\n" \
"Lpg585_%=:\n" \
"Lst0x0_%=:\n" \
"s_cbranch_execz Lpg2766_%=\n" \
"s_add_u32 s87, s87, s70\n" \
"v_mfma_f32_16x16x32_bf16 v[176:179], a[68:71], a[44:47], v[176:179]\n" \
"s_waitcnt lgkmcnt(0)\n" \
"s_and_b32 s70, s61, 1\n" \
"v_mfma_f32_16x16x32_bf16 v[192:195], a[64:67], a[48:51], v[192:195]\n" \
"s_lshl_b32 s70, s70, 12\n" \
"v_mfma_f32_16x16x32_bf16 v[192:195], a[68:71], a[52:55], v[192:195]\n" \
"s_add_u32 s87, s87, s70\n" \
"v_mfma_f32_16x16x32_bf16 v[196:199], a[72:75], a[48:51], v[196:199]\n" \
"s_mul_i32 s70, s63, 34816\n" \
"v_mfma_f32_16x16x32_bf16 v[196:199], a[76:79], a[52:55], v[196:199]\n" \
"s_add_u32 s94, s87, 522240\n" \
"v_mfma_f32_16x16x32_bf16 v[200:203], a[80:83], a[48:51], v[200:203]\n" \
"s_sub_u32 s94, s94, s70\n" \
"v_mfma_f32_16x16x32_bf16 v[200:203], a[84:87], a[52:55], v[200:203]\n" \
"s_add_u32 s87, s87, s70\n" \
"v_mfma_f32_16x16x32_bf16 v[204:207], a[88:91], a[48:51], v[204:207]\n" \
"s_lshl_b32 s90, s62, 7\n" \
"v_mfma_f32_16x16x32_bf16 v[204:207], a[92:95], a[52:55], v[204:207]\n" \
"s_add_u32 s95, s90, 65597440\n" \
"v_mfma_f32_16x16x32_bf16 v[220:223], a[88:91], a[56:59], v[220:223]\n" \
"s_add_u32 s90, s90, 65585152\n" \
"v_mfma_f32_16x16x32_bf16 v[220:223], a[92:95], a[60:63], v[220:223]\n" \
"s_lshl_b32 s92, 0xff, s63\n" \
"v_mfma_f32_16x16x32_bf16 v[216:219], a[80:83], a[56:59], v[216:219]\n" \
"s_and_b32 s92, s92, 0xff\n" \
"v_mfma_f32_16x16x32_bf16 v[216:219], a[84:87], a[60:63], v[216:219]\n" \
"s_mov_b32 s76, 0x3e0293ee\n" \
"v_mfma_f32_16x16x32_bf16 v[212:215], a[72:75], a[56:59], v[212:215]\n" \
"s_mov_b32 s31, 0x3e800000\n" \
"v_mfma_f32_16x16x32_bf16 v[212:215], a[76:79], a[60:63], v[212:215]\n" \
"v_mfma_f32_16x16x32_bf16 v[208:211], a[64:67], a[56:59], v[208:211]\n" \
"v_mfma_f32_16x16x32_bf16 v[208:211], a[68:71], a[60:63], v[208:211]\n" \
"s_waitcnt vmcnt(10) lgkmcnt(0)\n" \
"s_barrier\n" \
"v_readfirstlane_b32 s60, v240\n" \
"s_add_u32 s60, s60, 0x9e3779b9\n" \
"v_mfma_f32_16x16x32_bf16 a[128:131], a[0:3], a[96:99], a[128:131]\n" \
"s_add_u32 m0, s74, 131072\n" \
"ds_read_b64_tr_b16 a[64:65], v98 offset:32768\n" \
"v_mfma_f32_16x16x32_bf16 a[128:131], a[4:7], a[100:103], a[128:131]\n" \
"buffer_load_dwordx4 v64, s[36:39], s81 offen offset:0 lds\n" \
"v_mfma_f32_16x16x32_bf16 a[132:135], a[0:3], a[104:107], a[132:135]\n" \
"ds_read_b64_tr_b16 a[66:67], v98 offset:36864\n" \
"v_mfma_f32_16x16x32_bf16 a[132:135], a[4:7], a[108:111], a[132:135]\n" \
"v_mfma_f32_16x16x32_bf16 a[136:139], a[0:3], a[112:115], a[136:139]\n" \
"ds_read_b64_tr_b16 a[68:69], v98 offset:49152\n" \
"v_mfma_f32_16x16x32_bf16 a[136:139], a[4:7], a[116:119], a[136:139]\n" \
"buffer_load_dwordx4 v65, s[36:39], s81 offen offset:1024 lds\n" \
"v_mfma_f32_16x16x32_bf16 a[140:143], a[0:3], a[120:123], a[140:143]\n" \
"ds_read_b64_tr_b16 a[70:71], v98 offset:53248\n" \
"v_mfma_f32_16x16x32_bf16 a[140:143], a[4:7], a[124:127], a[140:143]\n" \
"v_mfma_f32_16x16x32_bf16 a[156:159], a[8:11], a[120:123], a[156:159]\n" \
"ds_read_b128 a[0:3], v72 offset:32768\n" \
"v_mfma_f32_16x16x32_bf16 a[156:159], a[12:15], a[124:127], a[156:159]\n" \
"buffer_load_dwordx4 v66, s[36:39], s81 offen offset:2048 lds\n" \
"v_mfma_f32_16x16x32_bf16 a[152:155], a[8:11], a[112:115], a[152:155]\n" \
"ds_read_b64_tr_b16 a[72:73], v99 offset:32768\n" \
"v_mfma_f32_16x16x32_bf16 a[152:155], a[12:15], a[116:119], a[152:155]\n" \
"v_mfma_f32_16x16x32_bf16 a[148:151], a[8:11], a[104:107], a[148:151]\n" \
"ds_read_b128 a[4:7], v73 offset:32768\n" \
"v_mfma_f32_16x16x32_bf16 a[148:151], a[12:15], a[108:111], a[148:151]\n" \
"buffer_load_dwordx4 v67, s[36:39], s81 offen offset:3072 lds\n" \
"v_mfma_f32_16x16x32_bf16 a[144:147], a[8:11], a[96:99], a[144:147]\n" \
"ds_read_b64_tr_b16 a[74:75], v99 offset:36864\n" \
"v_mfma_f32_16x16x32_bf16 a[144:147], a[12:15], a[100:103], a[144:147]\n" \
"s_xor_b32 s81, s81, 0x380\n" \
"v_mfma_f32_16x16x32_bf16 a[160:163], a[16:19], a[96:99], a[160:163]\n" \
"s_add_u32 m0, s74, 135168\n" \
"ds_read_b128 a[8:11], v72 offset:34816\n" \
"v_mfma_f32_16x16x32_bf16 a[160:163], a[20:23], a[100:103], a[160:163]\n" \
"buffer_load_dwordx4 v64, s[36:39], s82 offen offset:0 lds\n" \
"v_mfma_f32_16x16x32_bf16 a[164:167], a[16:19], a[104:107], a[164:167]\n" \
"ds_read_b64_tr_b16 a[76:77], v99 offset:49152\n" \
"v_mfma_f32_16x16x32_bf16 a[164:167], a[20:23], a[108:111], a[164:167]\n" \
"v_mfma_f32_16x16x32_bf16 a[168:171], a[16:19], a[112:115], a[168:171]\n" \
"ds_read_b128 a[12:15], v73 offset:34816\n" \
"v_mfma_f32_16x16x32_bf16 a[168:171], a[20:23], a[116:119], a[168:171]\n" \
"buffer_load_dwordx4 v65, s[36:39], s82 offen offset:1024 lds\n" \
"v_mfma_f32_16x16x32_bf16 a[172:175], a[16:19], a[120:123], a[172:175]\n" \
"ds_read_b64_tr_b16 a[78:79], v99 offset:53248\n" \
"v_mfma_f32_16x16x32_bf16 a[172:175], a[20:23], a[124:127], a[172:175]\n" \
"v_mfma_f32_16x16x32_bf16 a[188:191], a[24:27], a[120:123], a[188:191]\n" \
"ds_read_b128 a[16:19], v72 offset:36864\n" \
"v_mfma_f32_16x16x32_bf16 a[188:191], a[28:31], a[124:127], a[188:191]\n" \
"buffer_load_dwordx4 v66, s[36:39], s82 offen offset:2048 lds\n" \
"v_mfma_f32_16x16x32_bf16 a[184:187], a[24:27], a[112:115], a[184:187]\n" \
"ds_read_b64_tr_b16 a[80:81], v98 offset:33280\n" \
"v_mfma_f32_16x16x32_bf16 a[184:187], a[28:31], a[116:119], a[184:187]\n" \
"v_mfma_f32_16x16x32_bf16 a[180:183], a[24:27], a[104:107], a[180:183]\n" \
"ds_read_b128 a[20:23], v73 offset:36864\n" \
"v_mfma_f32_16x16x32_bf16 a[180:183], a[28:31], a[108:111], a[180:183]\n" \
"buffer_load_dwordx4 v67, s[36:39], s82 offen offset:3072 lds\n" \
"v_mfma_f32_16x16x32_bf16 a[176:179], a[24:27], a[96:99], a[176:179]\n" \
"s_mov_b64 s[64:65], s[14:15]\n" \
"s_add_u32 s68, s67, 0x300000\n" \
"ds_read_b64_tr_b16 a[82:83], v98 offset:37376\n" \
"v_mfma_f32_16x16x32_bf16 a[176:179], a[28:31], a[100:103], a[176:179]\n" \
"s_add_u32 s64, s64, s68\n" \
"s_addc_u32 s65, s65, 0\n" \
"v_mfma_f32_16x16x32_bf16 a[192:195], a[32:35], a[96:99], a[192:195]\n" \
"s_xor_b32 s82, s82, 0x380\n" \
"ds_read_b128 a[24:27], v72 offset:38912\n" \
"v_mfma_f32_16x16x32_bf16 a[192:195], a[36:39], a[100:103], a[192:195]\n" \
"global_load_dword v238, v234, s[64:65]\n" \
"v_mfma_f32_16x16x32_bf16 a[196:199], a[32:35], a[104:107], a[196:199]\n" \
"s_mov_b64 s[64:65], s[16:17]\n" \
"s_add_u32 s68, s67, 0x300000\n" \
"ds_read_b64_tr_b16 a[84:85], v98 offset:49664\n" \
"v_mfma_f32_16x16x32_bf16 a[196:199], a[36:39], a[108:111], a[196:199]\n" \
"s_add_u32 s64, s64, s68\n" \
"s_addc_u32 s65, s65, 0\n" \
"v_mfma_f32_16x16x32_bf16 a[200:203], a[32:35], a[112:115], a[200:203]\n" \
"ds_read_b128 a[28:31], v73 offset:38912\n" \
"v_mfma_f32_16x16x32_bf16 a[200:203], a[36:39], a[116:119], a[200:203]\n" \
"global_load_dword v238, v234, s[64:65]\n" \
"v_mfma_f32_16x16x32_bf16 a[204:207], a[32:35], a[120:123], a[204:207]\n" \
"ds_read_b64_tr_b16 a[86:87], v98 offset:53760\n" \
"v_mfma_f32_16x16x32_bf16 a[204:207], a[36:39], a[124:127], a[204:207]\n" \
"v_mfma_f32_16x16x32_bf16 a[220:223], a[40:43], a[120:123], a[220:223]\n" \
"ds_read_b128 a[32:35], v72 offset:40960\n" \
"v_mfma_f32_16x16x32_bf16 a[220:223], a[44:47], a[124:127], a[220:223]\n" \
"v_mfma_f32_16x16x32_bf16 a[216:219], a[40:43], a[112:115], a[216:219]\n" \
"ds_read_b64_tr_b16 a[88:89], v99 offset:33280\n" \
"v_mfma_f32_16x16x32_bf16 a[216:219], a[44:47], a[116:119], a[216:219]\n" \
"v_mfma_f32_16x16x32_bf16 a[212:215], a[40:43], a[104:107], a[212:215]\n" \
"ds_read_b128 a[36:39], v73 offset:40960\n" \
"v_mfma_f32_16x16x32_bf16 a[212:215], a[44:47], a[108:111], a[212:215]\n" \
"v_mfma_f32_16x16x32_bf16 a[208:211], a[40:43], a[96:99], a[208:211]\n" \
"ds_read_b64_tr_b16 a[90:91], v99 offset:37376\n" \
"v_mfma_f32_16x16x32_bf16 a[208:211], a[44:47], a[100:103], a[208:211]\n" \
"v_mfma_f32_16x16x32_bf16 a[224:227], a[48:51], a[96:99], a[224:227]\n" \
"ds_read_b128 a[40:43], v72 offset:43008\n" \
"v_mfma_f32_16x16x32_bf16 a[224:227], a[52:55], a[100:103], a[224:227]\n" \
"v_mfma_f32_16x16x32_bf16 a[228:231], a[48:51], a[104:107], a[228:231]\n" \
"ds_read_b64_tr_b16 a[92:93], v99 offset:49664\n" \
"v_mfma_f32_16x16x32_bf16 a[228:231], a[52:55], a[108:111], a[228:231]\n" \
"v_mfma_f32_16x16x32_bf16 a[232:235], a[48:51], a[112:115], a[232:235]\n" \
"ds_read_b128 a[44:47], v73 offset:43008\n" \
"v_mfma_f32_16x16x32_bf16 a[232:235], a[52:55], a[116:119], a[232:235]\n" \
"v_mfma_f32_16x16x32_bf16 a[236:239], a[48:51], a[120:123], a[236:239]\n" \
"ds_read_b64_tr_b16 a[94:95], v99 offset:53760\n" \
"v_mfma_f32_16x16x32_bf16 a[236:239], a[52:55], a[124:127], a[236:239]\n" \
"v_mfma_f32_16x16x32_bf16 a[252:255], a[56:59], a[120:123], a[252:255]\n" \
"ds_read_b128 a[48:51], v72 offset:45056\n" \
"v_mfma_f32_16x16x32_bf16 a[252:255], a[60:63], a[124:127], a[252:255]\n" \
"v_mfma_f32_16x16x32_bf16 a[248:251], a[56:59], a[112:115], a[248:251]\n" \
"v_mfma_f32_16x16x32_bf16 a[248:251], a[60:63], a[116:119], a[248:251]\n" \
"v_mfma_f32_16x16x32_bf16 a[244:247], a[56:59], a[104:107], a[244:247]\n" \
"ds_read_b128 a[52:55], v73 offset:45056\n" \
"v_mfma_f32_16x16x32_bf16 a[244:247], a[60:63], a[108:111], a[244:247]\n" \
"v_mfma_f32_16x16x32_bf16 a[240:243], a[56:59], a[96:99], a[240:243]\n" \
"v_mfma_f32_16x16x32_bf16 a[240:243], a[60:63], a[100:103], a[240:243]\n" \
"s_waitcnt lgkmcnt(1)\n" \
"v_mfma_f32_16x16x32_bf16 v[0:3], a[64:67], a[0:3], v[0:3]\n" \
"ds_read_b128 a[56:59], v72 offset:47104\n" \
"v_lshrrev_b32 v104, 4, v234\n" \
"v_mfma_f32_16x16x32_bf16 v[0:3], a[68:71], a[4:7], v[0:3]\n" \
"v_bfe_u32 v105, v104, 4, 1\n" \
"v_bfe_u32 v106, v104, 2, 2\n" \
"v_mfma_f32_16x16x32_bf16 v[4:7], a[72:75], a[0:3], v[4:7]\n" \
"s_add_u32 m0, s74, 65536\n" \
"ds_read_b128 a[60:63], v73 offset:47104\n" \
"v_lshl_add_u32 v106, v105, 3, v106\n" \
"v_mfma_f32_16x16x32_bf16 v[4:7], a[76:79], a[4:7], v[4:7]\n" \
"buffer_load_dwordx4 v68, s[40:43], s83 offen offset:0 lds\n" \
"v_mfma_f32_16x16x32_bf16 v[8:11], a[80:83], a[0:3], v[8:11]\n" \
"s_lshl_b32 s71, s61, 4\n" \
"ds_read_b64_tr_b16 a[96:97], v98 offset:33792\n" \
"v_add_u32 v106, s71, v106\n" \
"v_mfma_f32_16x16x32_bf16 v[8:11], a[84:87], a[4:7], v[8:11]\n" \
"v_mul_u32_u24 v106, 6144, v106\n" \
"v_lshlrev_b32 v105, 1, v105\n" \
"v_mfma_f32_16x16x32_bf16 v[12:15], a[88:91], a[0:3], v[12:15]\n" \
"ds_read_b64_tr_b16 a[98:99], v98 offset:37888\n" \
"v_and_b32 v107, 3, v104\n" \
"v_mfma_f32_16x16x32_bf16 v[12:15], a[92:95], a[4:7], v[12:15]\n" \
"buffer_load_dwordx4 v69, s[44:47], s83 offen offset:1024 lds\n" \
"v_mfma_f32_16x16x32_bf16 v[28:31], a[88:91], a[8:11], v[28:31]\n" \
"ds_read_b64_tr_b16 a[100:101], v98 offset:50176\n" \
"v_xor_b32 v105, v105, v107\n" \
"v_mfma_f32_16x16x32_bf16 v[28:31], a[92:95], a[12:15], v[28:31]\n" \
"v_lshl_add_u32 v106, v105, 4, v106\n" \
"v_lshrrev_b32 v105, 5, v104\n" \
"v_mfma_f32_16x16x32_bf16 v[24:27], a[80:83], a[8:11], v[24:27]\n" \
"ds_read_b64_tr_b16 a[102:103], v98 offset:54272\n" \
"v_lshl_add_u32 v244, v105, 6, v106\n" \
"v_mfma_f32_16x16x32_bf16 v[24:27], a[84:87], a[12:15], v[24:27]\n" \
"buffer_load_dwordx4 v70, s[40:43], s83 offen offset:2048 lds\n" \
"v_mfma_f32_16x16x32_bf16 v[20:23], a[72:75], a[8:11], v[20:23]\n" \
"ds_read_b64_tr_b16 a[104:105], v99 offset:33792\n" \
"v_lshrrev_b32 v107, 3, v104\n" \
"v_mfma_f32_16x16x32_bf16 v[20:23], a[76:79], a[12:15], v[20:23]\n" \
"v_and_b32 v108, 7, v104\n" \
"v_xor_b32 v109, v108, v107\n" \
"v_mfma_f32_16x16x32_bf16 v[16:19], a[64:67], a[8:11], v[16:19]\n" \
"ds_read_b64_tr_b16 a[106:107], v99 offset:37888\n" \
"v_lshlrev_b32 v109, 4, v109\n" \
"v_mfma_f32_16x16x32_bf16 v[16:19], a[68:71], a[12:15], v[16:19]\n" \
"buffer_load_dwordx4 v71, s[44:47], s83 offen offset:3072 lds\n" \
"v_mfma_f32_16x16x32_bf16 v[32:35], a[64:67], a[16:19], v[32:35]\n" \
"ds_read_b64_tr_b16 a[108:109], v99 offset:50176\n" \
"v_lshl_add_u32 v245, v107, 12, v109\n" \
"v_mfma_f32_16x16x32_bf16 v[32:35], a[68:71], a[20:23], v[32:35]\n" \
"s_xor_b32 s83, s83, 0x80000\n" \
"v_mfma_f32_16x16x32_bf16 v[36:39], a[72:75], a[16:19], v[36:39]\n" \
"s_add_u32 m0, s74, 69632\n" \
"ds_read_b64_tr_b16 a[110:111], v99 offset:54272\n" \
"v_mfma_f32_16x16x32_bf16 v[36:39], a[76:79], a[20:23], v[36:39]\n" \
"buffer_load_dwordx4 v68, s[40:43], s84 offen offset:0 lds\n" \
"v_mfma_f32_16x16x32_bf16 v[40:43], a[80:83], a[16:19], v[40:43]\n" \
"s_and_b32 s70, s61, 1\n" \
"ds_read_b64_tr_b16 a[112:113], v98 offset:34304\n" \
"v_mfma_f32_16x16x32_bf16 v[40:43], a[84:87], a[20:23], v[40:43]\n" \
"s_lshl_b32 s71, s70, 2\n" \
"v_add_u32 v107, s71, v107\n" \
"v_xor_b32 v109, v108, v107\n" \
"v_mfma_f32_16x16x32_bf16 v[44:47], a[88:91], a[16:19], v[44:47]\n" \
"ds_read_b64_tr_b16 a[114:115], v98 offset:38400\n" \
"v_lshlrev_b32 v109, 4, v109\n" \
"v_mfma_f32_16x16x32_bf16 v[44:47], a[92:95], a[20:23], v[44:47]\n" \
"buffer_load_dwordx4 v69, s[44:47], s84 offen offset:1024 lds\n" \
"v_mfma_f32_16x16x32_bf16 v[60:63], a[88:91], a[24:27], v[60:63]\n" \
"ds_read_b64_tr_b16 a[116:117], v98 offset:50688\n" \
"v_lshl_add_u32 v246, v107, 12, v109\n" \
"v_mfma_f32_16x16x32_bf16 v[60:63], a[92:95], a[28:31], v[60:63]\n" \
"s_lshr_b32 s71, s61, 1\n" \
"v_mfma_f32_16x16x32_bf16 v[56:59], a[80:83], a[24:27], v[56:59]\n" \
"s_lshl_b32 s70, s71, 11\n" \
"ds_read_b64_tr_b16 a[118:119], v98 offset:54784\n" \
"v_lshl_add_u32 v246, v105, 30, v246\n" \
"v_mfma_f32_16x16x32_bf16 v[56:59], a[84:87], a[28:31], v[56:59]\n" \
"buffer_load_dwordx4 v70, s[40:43], s84 offen offset:2048 lds\n" \
"v_mfma_f32_16x16x32_bf16 v[52:55], a[72:75], a[24:27], v[52:55]\n" \
"ds_read_b64_tr_b16 a[120:121], v99 offset:34304\n" \
"v_add_u32 v246, s70, v246\n" \
"v_mfma_f32_16x16x32_bf16 v[52:55], a[76:79], a[28:31], v[52:55]\n" \
"v_lshrrev_b32 v107, 4, v104\n" \
"v_xor_b32 v107, v107, v108\n" \
"v_mfma_f32_16x16x32_bf16 v[48:51], a[64:67], a[24:27], v[48:51]\n" \
"ds_read_b64_tr_b16 a[122:123], v99 offset:38400\n" \
"v_and_b32 v109, 15, v104\n" \
"v_mfma_f32_16x16x32_bf16 v[48:51], a[68:71], a[28:31], v[48:51]\n" \
"buffer_load_dwordx4 v71, s[44:47], s84 offen offset:3072 lds\n" \
"v_mfma_f32_16x16x32_bf16 v[144:147], a[64:67], a[32:35], v[144:147]\n" \
"ds_read_b64_tr_b16 a[124:125], v99 offset:50688\n" \
"v_lshlrev_b32 v106, 7, v109\n" \
"v_mfma_f32_16x16x32_bf16 v[144:147], a[68:71], a[36:39], v[144:147]\n" \
"v_lshl_add_u32 v106, v107, 4, v106\n" \
"v_mfma_f32_16x16x32_bf16 v[148:151], a[72:75], a[32:35], v[148:151]\n" \
"s_xor_b32 s84, s84, 0x80000\n" \
"ds_read_b64_tr_b16 a[126:127], v99 offset:54784\n" \
"v_mfma_f32_16x16x32_bf16 v[148:151], a[76:79], a[36:39], v[148:151]\n" \
"s_add_u32 s70, s70, %3\n" \
"v_add_u32 v247, s70, v106\n" \
"v_lshrrev_b32 v109, 2, v109\n" \
"v_mfma_f32_16x16x32_bf16 v[152:155], a[80:83], a[32:35], v[152:155]\n" \
"v_lshl_add_u32 v109, v109, 9, v106\n" \
"v_mfma_f32_16x16x32_bf16 v[152:155], a[84:87], a[36:39], v[152:155]\n" \
"s_add_u32 s70, %3, 16384\n" \
"v_add_u32 v248, s70, v109\n" \
"v_and_b32 v106, 3, v104\n" \
"v_mfma_f32_16x16x32_bf16 v[156:159], a[88:91], a[32:35], v[156:159]\n" \
"v_lshlrev_b32 v106, 3, v106\n" \
"v_bfe_u32 v107, v104, 2, 2\n" \
"v_mfma_f32_16x16x32_bf16 v[156:159], a[92:95], a[36:39], v[156:159]\n" \
"v_lshl_add_u32 v106, v107, 6, v106\n" \
"v_bfe_u32 v107, v104, 4, 1\n" \
"v_mfma_f32_16x16x32_bf16 v[188:191], a[88:91], a[40:43], v[188:191]\n" \
"v_mul_u32_u24 v107, 288, v107\n" \
"v_mul_u32_u24 v108, 6144, v105\n" \
"v_mfma_f32_16x16x32_bf16 v[188:191], a[92:95], a[44:47], v[188:191]\n" \
"v_add3_u32 v106, v106, v107, v108\n" \
"v_mfma_f32_16x16x32_bf16 v[184:187], a[80:83], a[40:43], v[184:187]\n" \
"s_and_b32 s70, s61, 1\n" \
"v_mfma_f32_16x16x32_bf16 v[184:187], a[84:87], a[44:47], v[184:187]\n" \
"s_lshl_b32 s70, s70, 9\n" \
"v_mfma_f32_16x16x32_bf16 v[180:183], a[72:75], a[40:43], v[180:183]\n" \
"s_add_u32 s70, s70, %3\n" \
"v_add_u32 v249, s70, v106\n" \
"v_mfma_f32_16x16x32_bf16 v[180:183], a[76:79], a[44:47], v[180:183]\n" \
"s_lshl_b32 s70, s71, 5\n" \
"v_xor_b32 v249, s70, v249\n" \
"v_mfma_f32_16x16x32_bf16 v[176:179], a[64:67], a[40:43], v[176:179]\n" \
"v_mfma_f32_16x16x32_bf16 v[176:179], a[68:71], a[44:47], v[176:179]\n" \
"s_waitcnt lgkmcnt(0)\n" \
"v_mfma_f32_16x16x32_bf16 v[192:195], a[64:67], a[48:51], v[192:195]\n" \
"v_mfma_f32_16x16x32_bf16 v[192:195], a[68:71], a[52:55], v[192:195]\n" \
"v_mfma_f32_16x16x32_bf16 v[196:199], a[72:75], a[48:51], v[196:199]\n" \
"v_mfma_f32_16x16x32_bf16 v[196:199], a[76:79], a[52:55], v[196:199]\n" \
"v_mfma_f32_16x16x32_bf16 v[200:203], a[80:83], a[48:51], v[200:203]\n" \
"v_mfma_f32_16x16x32_bf16 v[200:203], a[84:87], a[52:55], v[200:203]\n" \
"v_mfma_f32_16x16x32_bf16 v[204:207], a[88:91], a[48:51], v[204:207]\n" \
"v_mfma_f32_16x16x32_bf16 v[204:207], a[92:95], a[52:55], v[204:207]\n" \
"v_mfma_f32_16x16x32_bf16 v[220:223], a[88:91], a[56:59], v[220:223]\n" \
"v_mfma_f32_16x16x32_bf16 v[220:223], a[92:95], a[60:63], v[220:223]\n" \
"v_mfma_f32_16x16x32_bf16 v[216:219], a[80:83], a[56:59], v[216:219]\n" \
"v_mfma_f32_16x16x32_bf16 v[216:219], a[84:87], a[60:63], v[216:219]\n" \
"v_mfma_f32_16x16x32_bf16 v[212:215], a[72:75], a[56:59], v[212:215]\n" \
"v_mfma_f32_16x16x32_bf16 v[212:215], a[76:79], a[60:63], v[212:215]\n" \
"v_mfma_f32_16x16x32_bf16 v[208:211], a[64:67], a[56:59], v[208:211]\n" \
"v_mfma_f32_16x16x32_bf16 v[208:211], a[68:71], a[60:63], v[208:211]\n" \
"s_waitcnt vmcnt(10) lgkmcnt(0)\n" \
"s_barrier\n" \
"v_mfma_f32_16x16x32_bf16 a[128:131], a[0:3], a[96:99], a[128:131]\n" \
"s_add_u32 m0, s74, 98304\n" \
"ds_read_b64_tr_b16 a[64:65], v74 offset:0\n" \
"v_mfma_f32_16x16x32_bf16 a[128:131], a[4:7], a[100:103], a[128:131]\n" \
"buffer_load_dwordx4 v64, s[36:39], s81 offen offset:0 lds\n" \
"v_mfma_f32_16x16x32_bf16 a[132:135], a[0:3], a[104:107], a[132:135]\n" \
"ds_read_b64_tr_b16 a[66:67], v74 offset:4096\n" \
"v_mfma_f32_16x16x32_bf16 a[132:135], a[4:7], a[108:111], a[132:135]\n" \
"v_mfma_f32_16x16x32_bf16 a[136:139], a[0:3], a[112:115], a[136:139]\n" \
"ds_read_b64_tr_b16 a[68:69], v74 offset:16384\n" \
"v_mfma_f32_16x16x32_bf16 a[136:139], a[4:7], a[116:119], a[136:139]\n" \
"buffer_load_dwordx4 v65, s[36:39], s81 offen offset:1024 lds\n" \
"v_mfma_f32_16x16x32_bf16 a[140:143], a[0:3], a[120:123], a[140:143]\n" \
"ds_read_b64_tr_b16 a[70:71], v74 offset:20480\n" \
"v_mfma_f32_16x16x32_bf16 a[140:143], a[4:7], a[124:127], a[140:143]\n" \
"v_mfma_f32_16x16x32_bf16 a[156:159], a[8:11], a[120:123], a[156:159]\n" \
"ds_read_b128 a[0:3], v100 offset:0\n" \
"v_mfma_f32_16x16x32_bf16 a[156:159], a[12:15], a[124:127], a[156:159]\n" \
"buffer_load_dwordx4 v66, s[36:39], s81 offen offset:2048 lds\n" \
"v_mfma_f32_16x16x32_bf16 a[152:155], a[8:11], a[112:115], a[152:155]\n" \
"ds_read_b64_tr_b16 a[72:73], v75 offset:0\n" \
"v_mfma_f32_16x16x32_bf16 a[152:155], a[12:15], a[116:119], a[152:155]\n" \
"v_mfma_f32_16x16x32_bf16 a[148:151], a[8:11], a[104:107], a[148:151]\n" \
"ds_read_b128 a[4:7], v101 offset:0\n" \
"v_mfma_f32_16x16x32_bf16 a[148:151], a[12:15], a[108:111], a[148:151]\n" \
"buffer_load_dwordx4 v67, s[36:39], s81 offen offset:3072 lds\n" \
"v_mfma_f32_16x16x32_bf16 a[144:147], a[8:11], a[96:99], a[144:147]\n" \
"ds_read_b64_tr_b16 a[74:75], v75 offset:4096\n" \
"v_mfma_f32_16x16x32_bf16 a[144:147], a[12:15], a[100:103], a[144:147]\n" \
"s_xor_b32 s81, s81, 0x80\n" \
"v_mfma_f32_16x16x32_bf16 a[160:163], a[16:19], a[96:99], a[160:163]\n" \
"s_add_u32 m0, s74, 102400\n" \
"ds_read_b128 a[8:11], v100 offset:2048\n" \
"v_mfma_f32_16x16x32_bf16 a[160:163], a[20:23], a[100:103], a[160:163]\n" \
"buffer_load_dwordx4 v64, s[36:39], s82 offen offset:0 lds\n" \
"v_mfma_f32_16x16x32_bf16 a[164:167], a[16:19], a[104:107], a[164:167]\n" \
"ds_read_b64_tr_b16 a[76:77], v75 offset:16384\n" \
"v_mfma_f32_16x16x32_bf16 a[164:167], a[20:23], a[108:111], a[164:167]\n" \
"v_mfma_f32_16x16x32_bf16 a[168:171], a[16:19], a[112:115], a[168:171]\n" \
"ds_read_b128 a[12:15], v101 offset:2048\n" \
"v_mfma_f32_16x16x32_bf16 a[168:171], a[20:23], a[116:119], a[168:171]\n" \
"buffer_load_dwordx4 v65, s[36:39], s82 offen offset:1024 lds\n" \
"v_mfma_f32_16x16x32_bf16 a[172:175], a[16:19], a[120:123], a[172:175]\n" \
"ds_read_b64_tr_b16 a[78:79], v75 offset:20480\n" \
"v_mfma_f32_16x16x32_bf16 a[172:175], a[20:23], a[124:127], a[172:175]\n" \
"v_mfma_f32_16x16x32_bf16 a[188:191], a[24:27], a[120:123], a[188:191]\n" \
"ds_read_b128 a[16:19], v100 offset:4096\n" \
"v_mfma_f32_16x16x32_bf16 a[188:191], a[28:31], a[124:127], a[188:191]\n" \
"buffer_load_dwordx4 v66, s[36:39], s82 offen offset:2048 lds\n" \
"v_mfma_f32_16x16x32_bf16 a[184:187], a[24:27], a[112:115], a[184:187]\n" \
"ds_read_b64_tr_b16 a[80:81], v74 offset:512\n" \
"v_mfma_f32_16x16x32_bf16 a[184:187], a[28:31], a[116:119], a[184:187]\n" \
"v_mfma_f32_16x16x32_bf16 a[180:183], a[24:27], a[104:107], a[180:183]\n" \
"ds_read_b128 a[20:23], v101 offset:4096\n" \
"v_mfma_f32_16x16x32_bf16 a[180:183], a[28:31], a[108:111], a[180:183]\n" \
"buffer_load_dwordx4 v67, s[36:39], s82 offen offset:3072 lds\n" \
"v_mfma_f32_16x16x32_bf16 a[176:179], a[24:27], a[96:99], a[176:179]\n" \
"s_mov_b64 s[64:65], s[8:9]\n" \
"s_add_u32 s68, s66, 0x0\n" \
"ds_read_b64_tr_b16 a[82:83], v74 offset:4608\n" \
"v_mfma_f32_16x16x32_bf16 a[176:179], a[28:31], a[100:103], a[176:179]\n" \
"s_add_u32 s64, s64, s68\n" \
"s_addc_u32 s65, s65, 0\n" \
"v_mfma_f32_16x16x32_bf16 a[192:195], a[32:35], a[96:99], a[192:195]\n" \
"s_xor_b32 s82, s82, 0x80\n" \
"ds_read_b128 a[24:27], v100 offset:6144\n" \
"v_mfma_f32_16x16x32_bf16 a[192:195], a[36:39], a[100:103], a[192:195]\n" \
"global_load_dword v238, v234, s[64:65]\n" \
"v_mfma_f32_16x16x32_bf16 a[196:199], a[32:35], a[104:107], a[196:199]\n" \
"s_mov_b64 s[64:65], s[8:9]\n" \
"s_add_u32 s68, s66, 0x100000\n" \
"ds_read_b64_tr_b16 a[84:85], v74 offset:16896\n" \
"v_mfma_f32_16x16x32_bf16 a[196:199], a[36:39], a[108:111], a[196:199]\n" \
"s_add_u32 s64, s64, s68\n" \
"s_addc_u32 s65, s65, 0\n" \
"v_mfma_f32_16x16x32_bf16 a[200:203], a[32:35], a[112:115], a[200:203]\n" \
"ds_read_b128 a[28:31], v101 offset:6144\n" \
"v_mfma_f32_16x16x32_bf16 a[200:203], a[36:39], a[116:119], a[200:203]\n" \
"global_load_dword v238, v234, s[64:65]\n" \
"v_mfma_f32_16x16x32_bf16 a[204:207], a[32:35], a[120:123], a[204:207]\n" \
"ds_read_b64_tr_b16 a[86:87], v74 offset:20992\n" \
"v_mfma_f32_16x16x32_bf16 a[204:207], a[36:39], a[124:127], a[204:207]\n" \
"v_mfma_f32_16x16x32_bf16 a[220:223], a[40:43], a[120:123], a[220:223]\n" \
"ds_read_b128 a[32:35], v100 offset:8192\n" \
"v_mfma_f32_16x16x32_bf16 a[220:223], a[44:47], a[124:127], a[220:223]\n" \
"v_mfma_f32_16x16x32_bf16 a[216:219], a[40:43], a[112:115], a[216:219]\n" \
"ds_read_b64_tr_b16 a[88:89], v75 offset:512\n" \
"v_mfma_f32_16x16x32_bf16 a[216:219], a[44:47], a[116:119], a[216:219]\n" \
"v_mfma_f32_16x16x32_bf16 a[212:215], a[40:43], a[104:107], a[212:215]\n" \
"ds_read_b128 a[36:39], v101 offset:8192\n" \
"v_mfma_f32_16x16x32_bf16 a[212:215], a[44:47], a[108:111], a[212:215]\n" \
"v_mfma_f32_16x16x32_bf16 a[208:211], a[40:43], a[96:99], a[208:211]\n" \
"ds_read_b64_tr_b16 a[90:91], v75 offset:4608\n" \
"v_mfma_f32_16x16x32_bf16 a[208:211], a[44:47], a[100:103], a[208:211]\n" \
"v_mfma_f32_16x16x32_bf16 a[224:227], a[48:51], a[96:99], a[224:227]\n" \
"ds_read_b128 a[40:43], v100 offset:10240\n" \
"v_mfma_f32_16x16x32_bf16 a[224:227], a[52:55], a[100:103], a[224:227]\n" \
"v_mfma_f32_16x16x32_bf16 a[228:231], a[48:51], a[104:107], a[228:231]\n" \
"ds_read_b64_tr_b16 a[92:93], v75 offset:16896\n" \
"v_mfma_f32_16x16x32_bf16 a[228:231], a[52:55], a[108:111], a[228:231]\n" \
"v_mfma_f32_16x16x32_bf16 a[232:235], a[48:51], a[112:115], a[232:235]\n" \
"ds_read_b128 a[44:47], v101 offset:10240\n" \
"v_mfma_f32_16x16x32_bf16 a[232:235], a[52:55], a[116:119], a[232:235]\n" \
"v_mfma_f32_16x16x32_bf16 a[236:239], a[48:51], a[120:123], a[236:239]\n" \
"ds_read_b64_tr_b16 a[94:95], v75 offset:20992\n" \
"v_mfma_f32_16x16x32_bf16 a[236:239], a[52:55], a[124:127], a[236:239]\n" \
"v_mfma_f32_16x16x32_bf16 a[252:255], a[56:59], a[120:123], a[252:255]\n" \
"ds_read_b128 a[48:51], v100 offset:12288\n" \
"v_mfma_f32_16x16x32_bf16 a[252:255], a[60:63], a[124:127], a[252:255]\n" \
"v_mfma_f32_16x16x32_bf16 a[248:251], a[56:59], a[112:115], a[248:251]\n" \
"v_mfma_f32_16x16x32_bf16 a[248:251], a[60:63], a[116:119], a[248:251]\n" \
"v_mfma_f32_16x16x32_bf16 a[244:247], a[56:59], a[104:107], a[244:247]\n" \
"ds_read_b128 a[52:55], v101 offset:12288\n" \
"v_mfma_f32_16x16x32_bf16 a[244:247], a[60:63], a[108:111], a[244:247]\n" \
"v_mfma_f32_16x16x32_bf16 a[240:243], a[56:59], a[96:99], a[240:243]\n" \
"v_mfma_f32_16x16x32_bf16 a[240:243], a[60:63], a[100:103], a[240:243]\n" \
"s_waitcnt lgkmcnt(1)\n" \
"v_mfma_f32_16x16x32_bf16 v[0:3], a[64:67], a[0:3], v[0:3]\n" \
"ds_read_b128 a[56:59], v100 offset:14336\n" \
"v_mfma_f32_16x16x32_bf16 v[0:3], a[68:71], a[4:7], v[0:3]\n" \
"v_mfma_f32_16x16x32_bf16 v[4:7], a[72:75], a[0:3], v[4:7]\n" \
"s_add_u32 m0, s74, 32768\n" \
"ds_read_b128 a[60:63], v101 offset:14336\n" \
"v_mfma_f32_16x16x32_bf16 v[4:7], a[76:79], a[4:7], v[4:7]\n" \
"buffer_load_dwordx4 v68, s[40:43], s83 offen offset:0 lds\n" \
"v_mfma_f32_16x16x32_bf16 v[8:11], a[80:83], a[0:3], v[8:11]\n" \
"ds_read_b64_tr_b16 a[96:97], v74 offset:1024\n" \
"v_mfma_f32_16x16x32_bf16 v[8:11], a[84:87], a[4:7], v[8:11]\n" \
"v_mfma_f32_16x16x32_bf16 v[12:15], a[88:91], a[0:3], v[12:15]\n" \
"ds_read_b64_tr_b16 a[98:99], v74 offset:5120\n" \
"v_mfma_f32_16x16x32_bf16 v[12:15], a[92:95], a[4:7], v[12:15]\n" \
"buffer_load_dwordx4 v69, s[44:47], s83 offen offset:1024 lds\n" \
"v_mfma_f32_16x16x32_bf16 v[28:31], a[88:91], a[8:11], v[28:31]\n" \
"ds_read_b64_tr_b16 a[100:101], v74 offset:17408\n" \
"v_mfma_f32_16x16x32_bf16 v[28:31], a[92:95], a[12:15], v[28:31]\n" \
"v_mfma_f32_16x16x32_bf16 v[24:27], a[80:83], a[8:11], v[24:27]\n" \
"ds_read_b64_tr_b16 a[102:103], v74 offset:21504\n" \
"v_mfma_f32_16x16x32_bf16 v[24:27], a[84:87], a[12:15], v[24:27]\n" \
"buffer_load_dwordx4 v70, s[40:43], s83 offen offset:2048 lds\n" \
"v_mfma_f32_16x16x32_bf16 v[20:23], a[72:75], a[8:11], v[20:23]\n" \
"ds_read_b64_tr_b16 a[104:105], v75 offset:1024\n" \
"v_mfma_f32_16x16x32_bf16 v[20:23], a[76:79], a[12:15], v[20:23]\n" \
"v_mfma_f32_16x16x32_bf16 v[16:19], a[64:67], a[8:11], v[16:19]\n" \
"ds_read_b64_tr_b16 a[106:107], v75 offset:5120\n" \
"v_mfma_f32_16x16x32_bf16 v[16:19], a[68:71], a[12:15], v[16:19]\n" \
"buffer_load_dwordx4 v71, s[44:47], s83 offen offset:3072 lds\n" \
"v_mfma_f32_16x16x32_bf16 v[32:35], a[64:67], a[16:19], v[32:35]\n" \
"ds_read_b64_tr_b16 a[108:109], v75 offset:17408\n" \
"v_mfma_f32_16x16x32_bf16 v[32:35], a[68:71], a[20:23], v[32:35]\n" \
"s_xor_b32 s83, s83, 0x180000\n" \
"v_mfma_f32_16x16x32_bf16 v[36:39], a[72:75], a[16:19], v[36:39]\n" \
"s_add_u32 m0, s74, 36864\n" \
"ds_read_b64_tr_b16 a[110:111], v75 offset:21504\n" \
"v_mfma_f32_16x16x32_bf16 v[36:39], a[76:79], a[20:23], v[36:39]\n" \
"buffer_load_dwordx4 v68, s[40:43], s84 offen offset:0 lds\n" \
"v_mfma_f32_16x16x32_bf16 v[40:43], a[80:83], a[16:19], v[40:43]\n" \
"ds_read_b64_tr_b16 a[112:113], v74 offset:1536\n" \
"v_mfma_f32_16x16x32_bf16 v[40:43], a[84:87], a[20:23], v[40:43]\n" \
"v_mfma_f32_16x16x32_bf16 v[44:47], a[88:91], a[16:19], v[44:47]\n" \
"ds_read_b64_tr_b16 a[114:115], v74 offset:5632\n" \
"v_mfma_f32_16x16x32_bf16 v[44:47], a[92:95], a[20:23], v[44:47]\n" \
"buffer_load_dwordx4 v69, s[44:47], s84 offen offset:1024 lds\n" \
"v_mfma_f32_16x16x32_bf16 v[60:63], a[88:91], a[24:27], v[60:63]\n" \
"ds_read_b64_tr_b16 a[116:117], v74 offset:17920\n" \
"v_mfma_f32_16x16x32_bf16 v[60:63], a[92:95], a[28:31], v[60:63]\n" \
"v_mfma_f32_16x16x32_bf16 v[56:59], a[80:83], a[24:27], v[56:59]\n" \
"ds_read_b64_tr_b16 a[118:119], v74 offset:22016\n" \
"v_mfma_f32_16x16x32_bf16 v[56:59], a[84:87], a[28:31], v[56:59]\n" \
"buffer_load_dwordx4 v70, s[40:43], s84 offen offset:2048 lds\n" \
"v_mfma_f32_16x16x32_bf16 v[52:55], a[72:75], a[24:27], v[52:55]\n" \
"ds_read_b64_tr_b16 a[120:121], v75 offset:1536\n" \
"v_mfma_f32_16x16x32_bf16 v[52:55], a[76:79], a[28:31], v[52:55]\n" \
"v_mfma_f32_16x16x32_bf16 v[48:51], a[64:67], a[24:27], v[48:51]\n" \
"ds_read_b64_tr_b16 a[122:123], v75 offset:5632\n" \
"v_mfma_f32_16x16x32_bf16 v[48:51], a[68:71], a[28:31], v[48:51]\n" \
"buffer_load_dwordx4 v71, s[44:47], s84 offen offset:3072 lds\n" \
"v_mfma_f32_16x16x32_bf16 v[144:147], a[64:67], a[32:35], v[144:147]\n" \
"ds_read_b64_tr_b16 a[124:125], v75 offset:17920\n" \
"v_mfma_f32_16x16x32_bf16 v[144:147], a[68:71], a[36:39], v[144:147]\n" \
"v_mfma_f32_16x16x32_bf16 v[148:151], a[72:75], a[32:35], v[148:151]\n" \
"s_xor_b32 s84, s84, 0x180000\n" \
"ds_read_b64_tr_b16 a[126:127], v75 offset:22016\n" \
"v_mfma_f32_16x16x32_bf16 v[148:151], a[76:79], a[36:39], v[148:151]\n" \
"v_mfma_f32_16x16x32_bf16 v[152:155], a[80:83], a[32:35], v[152:155]\n" \
"v_mfma_f32_16x16x32_bf16 v[152:155], a[84:87], a[36:39], v[152:155]\n" \
"v_mfma_f32_16x16x32_bf16 v[156:159], a[88:91], a[32:35], v[156:159]\n" \
"v_mfma_f32_16x16x32_bf16 v[156:159], a[92:95], a[36:39], v[156:159]\n" \
"v_mfma_f32_16x16x32_bf16 v[188:191], a[88:91], a[40:43], v[188:191]\n" \
"v_mfma_f32_16x16x32_bf16 v[188:191], a[92:95], a[44:47], v[188:191]\n" \
"v_mfma_f32_16x16x32_bf16 v[184:187], a[80:83], a[40:43], v[184:187]\n" \
"v_mfma_f32_16x16x32_bf16 v[184:187], a[84:87], a[44:47], v[184:187]\n" \
"v_mfma_f32_16x16x32_bf16 v[180:183], a[72:75], a[40:43], v[180:183]\n" \
"v_mfma_f32_16x16x32_bf16 v[180:183], a[76:79], a[44:47], v[180:183]\n" \
"v_mfma_f32_16x16x32_bf16 v[176:179], a[64:67], a[40:43], v[176:179]\n" \
"v_mfma_f32_16x16x32_bf16 v[176:179], a[68:71], a[44:47], v[176:179]\n" \
"s_waitcnt lgkmcnt(0)\n" \
"v_mfma_f32_16x16x32_bf16 v[192:195], a[64:67], a[48:51], v[192:195]\n" \
"v_mfma_f32_16x16x32_bf16 v[192:195], a[68:71], a[52:55], v[192:195]\n" \
"v_mfma_f32_16x16x32_bf16 v[196:199], a[72:75], a[48:51], v[196:199]\n" \
"v_mfma_f32_16x16x32_bf16 v[196:199], a[76:79], a[52:55], v[196:199]\n" \
"v_mfma_f32_16x16x32_bf16 v[200:203], a[80:83], a[48:51], v[200:203]\n" \
"v_mfma_f32_16x16x32_bf16 v[200:203], a[84:87], a[52:55], v[200:203]\n" \
"v_mfma_f32_16x16x32_bf16 v[204:207], a[88:91], a[48:51], v[204:207]\n" \
"v_mfma_f32_16x16x32_bf16 v[204:207], a[92:95], a[52:55], v[204:207]\n" \
"v_mfma_f32_16x16x32_bf16 v[220:223], a[88:91], a[56:59], v[220:223]\n" \
"v_mfma_f32_16x16x32_bf16 v[220:223], a[92:95], a[60:63], v[220:223]\n" \
"v_mfma_f32_16x16x32_bf16 v[216:219], a[80:83], a[56:59], v[216:219]\n" \
"v_mfma_f32_16x16x32_bf16 v[216:219], a[84:87], a[60:63], v[216:219]\n" \
"v_mfma_f32_16x16x32_bf16 v[212:215], a[72:75], a[56:59], v[212:215]\n" \
"v_mfma_f32_16x16x32_bf16 v[212:215], a[76:79], a[60:63], v[212:215]\n" \
"v_mfma_f32_16x16x32_bf16 v[208:211], a[64:67], a[56:59], v[208:211]\n" \
"v_mfma_f32_16x16x32_bf16 v[208:211], a[68:71], a[60:63], v[208:211]\n" \
"s_waitcnt vmcnt(10) lgkmcnt(0)\n" \
"s_barrier\n" \
"v_mfma_f32_16x16x32_bf16 a[128:131], a[0:3], a[96:99], a[128:131]\n" \
"s_add_u32 m0, s74, 0\n" \
"ds_read_b64_tr_b16 a[64:65], v98 offset:0\n" \
"v_mfma_f32_16x16x32_bf16 a[128:131], a[4:7], a[100:103], a[128:131]\n" \
"buffer_load_dwordx4 v64, s[36:39], s81 offen offset:0 lds\n" \
"v_mfma_f32_16x16x32_bf16 a[132:135], a[0:3], a[104:107], a[132:135]\n" \
"ds_read_b64_tr_b16 a[66:67], v98 offset:4096\n" \
"v_mfma_f32_16x16x32_bf16 a[132:135], a[4:7], a[108:111], a[132:135]\n" \
"v_mfma_f32_16x16x32_bf16 a[136:139], a[0:3], a[112:115], a[136:139]\n" \
"ds_read_b64_tr_b16 a[68:69], v98 offset:16384\n" \
"v_mfma_f32_16x16x32_bf16 a[136:139], a[4:7], a[116:119], a[136:139]\n" \
"buffer_load_dwordx4 v65, s[36:39], s81 offen offset:1024 lds\n" \
"v_mfma_f32_16x16x32_bf16 a[140:143], a[0:3], a[120:123], a[140:143]\n" \
"ds_read_b64_tr_b16 a[70:71], v98 offset:20480\n" \
"v_mfma_f32_16x16x32_bf16 a[140:143], a[4:7], a[124:127], a[140:143]\n" \
"v_mfma_f32_16x16x32_bf16 a[156:159], a[8:11], a[120:123], a[156:159]\n" \
"ds_read_b128 a[0:3], v96 offset:32768\n" \
"v_mfma_f32_16x16x32_bf16 a[156:159], a[12:15], a[124:127], a[156:159]\n" \
"buffer_load_dwordx4 v66, s[36:39], s81 offen offset:2048 lds\n" \
"v_mfma_f32_16x16x32_bf16 a[152:155], a[8:11], a[112:115], a[152:155]\n" \
"ds_read_b64_tr_b16 a[72:73], v99 offset:0\n" \
"v_mfma_f32_16x16x32_bf16 a[152:155], a[12:15], a[116:119], a[152:155]\n" \
"v_mfma_f32_16x16x32_bf16 a[148:151], a[8:11], a[104:107], a[148:151]\n" \
"ds_read_b128 a[4:7], v97 offset:32768\n" \
"v_mfma_f32_16x16x32_bf16 a[148:151], a[12:15], a[108:111], a[148:151]\n" \
"buffer_load_dwordx4 v67, s[36:39], s81 offen offset:3072 lds\n" \
"v_mfma_f32_16x16x32_bf16 a[144:147], a[8:11], a[96:99], a[144:147]\n" \
"ds_read_b64_tr_b16 a[74:75], v99 offset:4096\n" \
"Lpg1132_%=:\n" \
"Lst0x1_%=:\n" \
"s_cbranch_execz Lpg3342_%=\n" \
"v_mfma_f32_16x16x32_bf16 a[144:147], a[12:15], a[100:103], a[144:147]\n" \
"s_xor_b32 s81, s81, 0x180\n" \
"v_mfma_f32_16x16x32_bf16 a[160:163], a[16:19], a[96:99], a[160:163]\n" \
"s_add_u32 m0, s74, 4096\n" \
"ds_read_b128 a[8:11], v96 offset:34816\n" \
"v_mfma_f32_16x16x32_bf16 a[160:163], a[20:23], a[100:103], a[160:163]\n" \
"buffer_load_dwordx4 v64, s[36:39], s82 offen offset:0 lds\n" \
"v_mfma_f32_16x16x32_bf16 a[164:167], a[16:19], a[104:107], a[164:167]\n" \
"ds_read_b64_tr_b16 a[76:77], v99 offset:16384\n" \
"v_mfma_f32_16x16x32_bf16 a[164:167], a[20:23], a[108:111], a[164:167]\n" \
"v_mfma_f32_16x16x32_bf16 a[168:171], a[16:19], a[112:115], a[168:171]\n" \
"ds_read_b128 a[12:15], v97 offset:34816\n" \
"v_mfma_f32_16x16x32_bf16 a[168:171], a[20:23], a[116:119], a[168:171]\n" \
"buffer_load_dwordx4 v65, s[36:39], s82 offen offset:1024 lds\n" \
"v_mfma_f32_16x16x32_bf16 a[172:175], a[16:19], a[120:123], a[172:175]\n" \
"ds_read_b64_tr_b16 a[78:79], v99 offset:20480\n" \
"v_mfma_f32_16x16x32_bf16 a[172:175], a[20:23], a[124:127], a[172:175]\n" \
"v_mfma_f32_16x16x32_bf16 a[188:191], a[24:27], a[120:123], a[188:191]\n" \
"ds_read_b128 a[16:19], v96 offset:36864\n" \
"v_mfma_f32_16x16x32_bf16 a[188:191], a[28:31], a[124:127], a[188:191]\n" \
"buffer_load_dwordx4 v66, s[36:39], s82 offen offset:2048 lds\n" \
"v_mfma_f32_16x16x32_bf16 a[184:187], a[24:27], a[112:115], a[184:187]\n" \
"ds_read_b64_tr_b16 a[80:81], v98 offset:512\n" \
"v_mfma_f32_16x16x32_bf16 a[184:187], a[28:31], a[116:119], a[184:187]\n" \
"v_mfma_f32_16x16x32_bf16 a[180:183], a[24:27], a[104:107], a[180:183]\n" \
"ds_read_b128 a[20:23], v97 offset:36864\n" \
"v_mfma_f32_16x16x32_bf16 a[180:183], a[28:31], a[108:111], a[180:183]\n" \
"buffer_load_dwordx4 v67, s[36:39], s82 offen offset:3072 lds\n" \
"v_mfma_f32_16x16x32_bf16 a[176:179], a[24:27], a[96:99], a[176:179]\n" \
"s_mov_b64 s[64:65], s[8:9]\n" \
"s_add_u32 s68, s66, 0x200000\n" \
"ds_read_b64_tr_b16 a[82:83], v98 offset:4608\n" \
"v_mfma_f32_16x16x32_bf16 a[176:179], a[28:31], a[100:103], a[176:179]\n" \
"s_add_u32 s64, s64, s68\n" \
"s_addc_u32 s65, s65, 0\n" \
"v_mfma_f32_16x16x32_bf16 a[192:195], a[32:35], a[96:99], a[192:195]\n" \
"s_xor_b32 s82, s82, 0x180\n" \
"ds_read_b128 a[24:27], v96 offset:38912\n" \
"v_mfma_f32_16x16x32_bf16 a[192:195], a[36:39], a[100:103], a[192:195]\n" \
"global_load_dword v238, v234, s[64:65]\n" \
"v_mfma_f32_16x16x32_bf16 a[196:199], a[32:35], a[104:107], a[196:199]\n" \
"s_mov_b64 s[64:65], s[8:9]\n" \
"s_add_u32 s68, s66, 0x300000\n" \
"ds_read_b64_tr_b16 a[84:85], v98 offset:16896\n" \
"v_mfma_f32_16x16x32_bf16 a[196:199], a[36:39], a[108:111], a[196:199]\n" \
"s_add_u32 s64, s64, s68\n" \
"s_addc_u32 s65, s65, 0\n" \
"v_mfma_f32_16x16x32_bf16 a[200:203], a[32:35], a[112:115], a[200:203]\n" \
"ds_read_b128 a[28:31], v97 offset:38912\n" \
"v_mfma_f32_16x16x32_bf16 a[200:203], a[36:39], a[116:119], a[200:203]\n" \
"global_load_dword v238, v234, s[64:65]\n" \
"v_mfma_f32_16x16x32_bf16 a[204:207], a[32:35], a[120:123], a[204:207]\n" \
"s_mov_b64 s[64:65], s[12:13]\n" \
"s_add_u32 s68, s67, 0x0\n" \
"ds_read_b64_tr_b16 a[86:87], v98 offset:20992\n" \
"v_mfma_f32_16x16x32_bf16 a[204:207], a[36:39], a[124:127], a[204:207]\n" \
"s_add_u32 s64, s64, s68\n" \
"s_addc_u32 s65, s65, 0\n" \
"v_mfma_f32_16x16x32_bf16 a[220:223], a[40:43], a[120:123], a[220:223]\n" \
"ds_read_b128 a[32:35], v96 offset:40960\n" \
"v_mfma_f32_16x16x32_bf16 a[220:223], a[44:47], a[124:127], a[220:223]\n" \
"global_load_dword v238, v234, s[64:65]\n" \
"v_mfma_f32_16x16x32_bf16 a[216:219], a[40:43], a[112:115], a[216:219]\n" \
"ds_read_b64_tr_b16 a[88:89], v99 offset:512\n" \
"v_mfma_f32_16x16x32_bf16 a[216:219], a[44:47], a[116:119], a[216:219]\n" \
"v_mfma_f32_16x16x32_bf16 a[212:215], a[40:43], a[104:107], a[212:215]\n" \
"ds_read_b128 a[36:39], v97 offset:40960\n" \
"v_mfma_f32_16x16x32_bf16 a[212:215], a[44:47], a[108:111], a[212:215]\n" \
"v_mfma_f32_16x16x32_bf16 a[208:211], a[40:43], a[96:99], a[208:211]\n" \
"ds_read_b64_tr_b16 a[90:91], v99 offset:4608\n" \
"v_mfma_f32_16x16x32_bf16 a[208:211], a[44:47], a[100:103], a[208:211]\n" \
"v_mfma_f32_16x16x32_bf16 a[224:227], a[48:51], a[96:99], a[224:227]\n" \
"ds_read_b128 a[40:43], v96 offset:43008\n" \
"v_mfma_f32_16x16x32_bf16 a[224:227], a[52:55], a[100:103], a[224:227]\n" \
"v_mfma_f32_16x16x32_bf16 a[228:231], a[48:51], a[104:107], a[228:231]\n" \
"ds_read_b64_tr_b16 a[92:93], v99 offset:16896\n" \
"v_mfma_f32_16x16x32_bf16 a[228:231], a[52:55], a[108:111], a[228:231]\n" \
"v_mfma_f32_16x16x32_bf16 a[232:235], a[48:51], a[112:115], a[232:235]\n" \
"ds_read_b128 a[44:47], v97 offset:43008\n" \
"v_mfma_f32_16x16x32_bf16 a[232:235], a[52:55], a[116:119], a[232:235]\n" \
"v_mfma_f32_16x16x32_bf16 a[236:239], a[48:51], a[120:123], a[236:239]\n" \
"ds_read_b64_tr_b16 a[94:95], v99 offset:20992\n" \
"v_mfma_f32_16x16x32_bf16 a[236:239], a[52:55], a[124:127], a[236:239]\n" \
"v_mfma_f32_16x16x32_bf16 a[252:255], a[56:59], a[120:123], a[252:255]\n" \
"ds_read_b128 a[48:51], v96 offset:45056\n" \
"v_mfma_f32_16x16x32_bf16 a[252:255], a[60:63], a[124:127], a[252:255]\n" \
"v_mfma_f32_16x16x32_bf16 a[248:251], a[56:59], a[112:115], a[248:251]\n" \
"v_mfma_f32_16x16x32_bf16 a[248:251], a[60:63], a[116:119], a[248:251]\n" \
"v_mfma_f32_16x16x32_bf16 a[244:247], a[56:59], a[104:107], a[244:247]\n" \
"ds_read_b128 a[52:55], v97 offset:45056\n" \
"v_mfma_f32_16x16x32_bf16 a[244:247], a[60:63], a[108:111], a[244:247]\n" \
"v_mfma_f32_16x16x32_bf16 a[240:243], a[56:59], a[96:99], a[240:243]\n" \
"v_mfma_f32_16x16x32_bf16 a[240:243], a[60:63], a[100:103], a[240:243]\n" \
"s_waitcnt lgkmcnt(1)\n" \
"v_mfma_f32_16x16x32_bf16 v[0:3], a[64:67], a[0:3], v[0:3]\n" \
"ds_read_b128 a[56:59], v96 offset:47104\n" \
"v_mfma_f32_16x16x32_bf16 v[0:3], a[68:71], a[4:7], v[0:3]\n" \
"v_mfma_f32_16x16x32_bf16 v[4:7], a[72:75], a[0:3], v[4:7]\n" \
"s_add_u32 m0, s74, 131072\n" \
"ds_read_b128 a[60:63], v97 offset:47104\n" \
"v_mfma_f32_16x16x32_bf16 v[4:7], a[76:79], a[4:7], v[4:7]\n" \
"buffer_load_dwordx4 v68, s[40:43], s83 offen offset:0 lds\n" \
"v_mfma_f32_16x16x32_bf16 v[8:11], a[80:83], a[0:3], v[8:11]\n" \
"ds_read_b64_tr_b16 a[96:97], v98 offset:1024\n" \
"v_mfma_f32_16x16x32_bf16 v[8:11], a[84:87], a[4:7], v[8:11]\n" \
"v_mfma_f32_16x16x32_bf16 v[12:15], a[88:91], a[0:3], v[12:15]\n" \
"ds_read_b64_tr_b16 a[98:99], v98 offset:5120\n" \
"v_mfma_f32_16x16x32_bf16 v[12:15], a[92:95], a[4:7], v[12:15]\n" \
"buffer_load_dwordx4 v69, s[44:47], s83 offen offset:1024 lds\n" \
"v_mfma_f32_16x16x32_bf16 v[28:31], a[88:91], a[8:11], v[28:31]\n" \
"ds_read_b64_tr_b16 a[100:101], v98 offset:17408\n" \
"v_mfma_f32_16x16x32_bf16 v[28:31], a[92:95], a[12:15], v[28:31]\n" \
"v_mfma_f32_16x16x32_bf16 v[24:27], a[80:83], a[8:11], v[24:27]\n" \
"ds_read_b64_tr_b16 a[102:103], v98 offset:21504\n" \
"v_mfma_f32_16x16x32_bf16 v[24:27], a[84:87], a[12:15], v[24:27]\n" \
"buffer_load_dwordx4 v70, s[40:43], s83 offen offset:2048 lds\n" \
"v_mfma_f32_16x16x32_bf16 v[20:23], a[72:75], a[8:11], v[20:23]\n" \
"ds_read_b64_tr_b16 a[104:105], v99 offset:1024\n" \
"v_mfma_f32_16x16x32_bf16 v[20:23], a[76:79], a[12:15], v[20:23]\n" \
"v_mfma_f32_16x16x32_bf16 v[16:19], a[64:67], a[8:11], v[16:19]\n" \
"ds_read_b64_tr_b16 a[106:107], v99 offset:5120\n" \
"v_mfma_f32_16x16x32_bf16 v[16:19], a[68:71], a[12:15], v[16:19]\n" \
"buffer_load_dwordx4 v71, s[44:47], s83 offen offset:3072 lds\n" \
"v_mfma_f32_16x16x32_bf16 v[32:35], a[64:67], a[16:19], v[32:35]\n" \
"ds_read_b64_tr_b16 a[108:109], v99 offset:17408\n" \
"v_mfma_f32_16x16x32_bf16 v[32:35], a[68:71], a[20:23], v[32:35]\n" \
"s_xor_b32 s83, s83, 0x80000\n" \
"v_mfma_f32_16x16x32_bf16 v[36:39], a[72:75], a[16:19], v[36:39]\n" \
"s_add_u32 m0, s74, 135168\n" \
"ds_read_b64_tr_b16 a[110:111], v99 offset:21504\n" \
"v_mfma_f32_16x16x32_bf16 v[36:39], a[76:79], a[20:23], v[36:39]\n" \
"buffer_load_dwordx4 v68, s[40:43], s84 offen offset:0 lds\n" \
"v_mfma_f32_16x16x32_bf16 v[40:43], a[80:83], a[16:19], v[40:43]\n" \
"ds_read_b64_tr_b16 a[112:113], v98 offset:1536\n" \
"v_mfma_f32_16x16x32_bf16 v[40:43], a[84:87], a[20:23], v[40:43]\n" \
"v_mfma_f32_16x16x32_bf16 v[44:47], a[88:91], a[16:19], v[44:47]\n" \
"ds_read_b64_tr_b16 a[114:115], v98 offset:5632\n" \
"v_mfma_f32_16x16x32_bf16 v[44:47], a[92:95], a[20:23], v[44:47]\n" \
"buffer_load_dwordx4 v69, s[44:47], s84 offen offset:1024 lds\n" \
"v_mfma_f32_16x16x32_bf16 v[60:63], a[88:91], a[24:27], v[60:63]\n" \
"ds_read_b64_tr_b16 a[116:117], v98 offset:17920\n" \
"v_mfma_f32_16x16x32_bf16 v[60:63], a[92:95], a[28:31], v[60:63]\n" \
"v_mfma_f32_16x16x32_bf16 v[56:59], a[80:83], a[24:27], v[56:59]\n" \
"ds_read_b64_tr_b16 a[118:119], v98 offset:22016\n" \
"v_mfma_f32_16x16x32_bf16 v[56:59], a[84:87], a[28:31], v[56:59]\n" \
"buffer_load_dwordx4 v70, s[40:43], s84 offen offset:2048 lds\n" \
"v_mfma_f32_16x16x32_bf16 v[52:55], a[72:75], a[24:27], v[52:55]\n" \
"ds_read_b64_tr_b16 a[120:121], v99 offset:1536\n" \
"v_mfma_f32_16x16x32_bf16 v[52:55], a[76:79], a[28:31], v[52:55]\n" \
"v_mfma_f32_16x16x32_bf16 v[48:51], a[64:67], a[24:27], v[48:51]\n" \
"ds_read_b64_tr_b16 a[122:123], v99 offset:5632\n" \
"v_mfma_f32_16x16x32_bf16 v[48:51], a[68:71], a[28:31], v[48:51]\n" \
"buffer_load_dwordx4 v71, s[44:47], s84 offen offset:3072 lds\n" \
"v_mfma_f32_16x16x32_bf16 v[144:147], a[64:67], a[32:35], v[144:147]\n" \
"ds_read_b64_tr_b16 a[124:125], v99 offset:17920\n" \
"v_mfma_f32_16x16x32_bf16 v[144:147], a[68:71], a[36:39], v[144:147]\n" \
"v_mfma_f32_16x16x32_bf16 v[148:151], a[72:75], a[32:35], v[148:151]\n" \
"s_xor_b32 s84, s84, 0x80000\n" \
"ds_read_b64_tr_b16 a[126:127], v99 offset:22016\n" \
"v_mfma_f32_16x16x32_bf16 v[148:151], a[76:79], a[36:39], v[148:151]\n" \
"v_mfma_f32_16x16x32_bf16 v[152:155], a[80:83], a[32:35], v[152:155]\n" \
"v_mfma_f32_16x16x32_bf16 v[152:155], a[84:87], a[36:39], v[152:155]\n" \
"v_mfma_f32_16x16x32_bf16 v[156:159], a[88:91], a[32:35], v[156:159]\n" \
"v_mfma_f32_16x16x32_bf16 v[156:159], a[92:95], a[36:39], v[156:159]\n" \
"v_mfma_f32_16x16x32_bf16 v[188:191], a[88:91], a[40:43], v[188:191]\n" \
"v_mfma_f32_16x16x32_bf16 v[188:191], a[92:95], a[44:47], v[188:191]\n" \
"v_mfma_f32_16x16x32_bf16 v[184:187], a[80:83], a[40:43], v[184:187]\n" \
"v_mfma_f32_16x16x32_bf16 v[184:187], a[84:87], a[44:47], v[184:187]\n" \
"v_mfma_f32_16x16x32_bf16 v[180:183], a[72:75], a[40:43], v[180:183]\n" \
"v_mfma_f32_16x16x32_bf16 v[180:183], a[76:79], a[44:47], v[180:183]\n" \
"v_mfma_f32_16x16x32_bf16 v[176:179], a[64:67], a[40:43], v[176:179]\n" \
"v_mfma_f32_16x16x32_bf16 v[176:179], a[68:71], a[44:47], v[176:179]\n" \
"s_waitcnt lgkmcnt(0)\n" \
"v_mfma_f32_16x16x32_bf16 v[192:195], a[64:67], a[48:51], v[192:195]\n" \
"v_mfma_f32_16x16x32_bf16 v[192:195], a[68:71], a[52:55], v[192:195]\n" \
"v_mfma_f32_16x16x32_bf16 v[196:199], a[72:75], a[48:51], v[196:199]\n" \
"v_mfma_f32_16x16x32_bf16 v[196:199], a[76:79], a[52:55], v[196:199]\n" \
"v_mfma_f32_16x16x32_bf16 v[200:203], a[80:83], a[48:51], v[200:203]\n" \
"v_mfma_f32_16x16x32_bf16 v[200:203], a[84:87], a[52:55], v[200:203]\n" \
"v_mfma_f32_16x16x32_bf16 v[204:207], a[88:91], a[48:51], v[204:207]\n" \
"v_mfma_f32_16x16x32_bf16 v[204:207], a[92:95], a[52:55], v[204:207]\n" \
"v_mfma_f32_16x16x32_bf16 v[220:223], a[88:91], a[56:59], v[220:223]\n" \
"v_mfma_f32_16x16x32_bf16 v[220:223], a[92:95], a[60:63], v[220:223]\n" \
"v_mfma_f32_16x16x32_bf16 v[216:219], a[80:83], a[56:59], v[216:219]\n" \
"v_mfma_f32_16x16x32_bf16 v[216:219], a[84:87], a[60:63], v[216:219]\n" \
"v_mfma_f32_16x16x32_bf16 v[212:215], a[72:75], a[56:59], v[212:215]\n" \
"v_mfma_f32_16x16x32_bf16 v[212:215], a[76:79], a[60:63], v[212:215]\n" \
"v_mfma_f32_16x16x32_bf16 v[208:211], a[64:67], a[56:59], v[208:211]\n" \
"v_mfma_f32_16x16x32_bf16 v[208:211], a[68:71], a[60:63], v[208:211]\n" \
"s_waitcnt vmcnt(11) lgkmcnt(0)\n" \
"s_barrier\n" \
"v_mfma_f32_16x16x32_bf16 a[128:131], a[0:3], a[96:99], a[128:131]\n" \
"s_add_u32 m0, s74, 65536\n" \
"ds_read_b64_tr_b16 a[64:65], v74 offset:32768\n" \
"v_mfma_f32_16x16x32_bf16 a[128:131], a[4:7], a[100:103], a[128:131]\n" \
"buffer_load_dwordx4 v64, s[36:39], s81 offen offset:0 lds\n" \
"v_mfma_f32_16x16x32_bf16 a[132:135], a[0:3], a[104:107], a[132:135]\n" \
"ds_read_b64_tr_b16 a[66:67], v74 offset:36864\n" \
"v_mfma_f32_16x16x32_bf16 a[132:135], a[4:7], a[108:111], a[132:135]\n" \
"v_mfma_f32_16x16x32_bf16 a[136:139], a[0:3], a[112:115], a[136:139]\n" \
"ds_read_b64_tr_b16 a[68:69], v74 offset:49152\n" \
"v_mfma_f32_16x16x32_bf16 a[136:139], a[4:7], a[116:119], a[136:139]\n" \
"buffer_load_dwordx4 v65, s[36:39], s81 offen offset:1024 lds\n" \
"v_mfma_f32_16x16x32_bf16 a[140:143], a[0:3], a[120:123], a[140:143]\n" \
"ds_read_b64_tr_b16 a[70:71], v74 offset:53248\n" \
"v_mfma_f32_16x16x32_bf16 a[140:143], a[4:7], a[124:127], a[140:143]\n" \
"v_mfma_f32_16x16x32_bf16 a[156:159], a[8:11], a[120:123], a[156:159]\n" \
"ds_read_b128 a[0:3], v72 offset:0\n" \
"v_mfma_f32_16x16x32_bf16 a[156:159], a[12:15], a[124:127], a[156:159]\n" \
"buffer_load_dwordx4 v66, s[36:39], s81 offen offset:2048 lds\n" \
"v_mfma_f32_16x16x32_bf16 a[152:155], a[8:11], a[112:115], a[152:155]\n" \
"ds_read_b64_tr_b16 a[72:73], v75 offset:32768\n" \
"v_mfma_f32_16x16x32_bf16 a[152:155], a[12:15], a[116:119], a[152:155]\n" \
"v_mfma_f32_16x16x32_bf16 a[148:151], a[8:11], a[104:107], a[148:151]\n" \
"ds_read_b128 a[4:7], v73 offset:0\n" \
"v_mfma_f32_16x16x32_bf16 a[148:151], a[12:15], a[108:111], a[148:151]\n" \
"buffer_load_dwordx4 v67, s[36:39], s81 offen offset:3072 lds\n" \
"v_mfma_f32_16x16x32_bf16 a[144:147], a[8:11], a[96:99], a[144:147]\n" \
"ds_read_b64_tr_b16 a[74:75], v75 offset:36864\n" \
"v_mfma_f32_16x16x32_bf16 a[144:147], a[12:15], a[100:103], a[144:147]\n" \
"s_xor_b32 s81, s81, 0x80\n" \
"v_mfma_f32_16x16x32_bf16 a[160:163], a[16:19], a[96:99], a[160:163]\n" \
"s_add_u32 m0, s74, 69632\n" \
"ds_read_b128 a[8:11], v72 offset:2048\n" \
"v_mfma_f32_16x16x32_bf16 a[160:163], a[20:23], a[100:103], a[160:163]\n" \
"buffer_load_dwordx4 v64, s[36:39], s82 offen offset:0 lds\n" \
"v_mfma_f32_16x16x32_bf16 a[164:167], a[16:19], a[104:107], a[164:167]\n" \
"ds_read_b64_tr_b16 a[76:77], v75 offset:49152\n" \
"v_mfma_f32_16x16x32_bf16 a[164:167], a[20:23], a[108:111], a[164:167]\n" \
"v_mfma_f32_16x16x32_bf16 a[168:171], a[16:19], a[112:115], a[168:171]\n" \
"ds_read_b128 a[12:15], v73 offset:2048\n" \
"v_mfma_f32_16x16x32_bf16 a[168:171], a[20:23], a[116:119], a[168:171]\n" \
"buffer_load_dwordx4 v65, s[36:39], s82 offen offset:1024 lds\n" \
"v_mfma_f32_16x16x32_bf16 a[172:175], a[16:19], a[120:123], a[172:175]\n" \
"ds_read_b64_tr_b16 a[78:79], v75 offset:53248\n" \
"v_mfma_f32_16x16x32_bf16 a[172:175], a[20:23], a[124:127], a[172:175]\n" \
"v_mfma_f32_16x16x32_bf16 a[188:191], a[24:27], a[120:123], a[188:191]\n" \
"ds_read_b128 a[16:19], v72 offset:4096\n" \
"v_mfma_f32_16x16x32_bf16 a[188:191], a[28:31], a[124:127], a[188:191]\n" \
"buffer_load_dwordx4 v66, s[36:39], s82 offen offset:2048 lds\n" \
"v_mfma_f32_16x16x32_bf16 a[184:187], a[24:27], a[112:115], a[184:187]\n" \
"ds_read_b64_tr_b16 a[80:81], v74 offset:33280\n" \
"v_mfma_f32_16x16x32_bf16 a[184:187], a[28:31], a[116:119], a[184:187]\n" \
"v_mfma_f32_16x16x32_bf16 a[180:183], a[24:27], a[104:107], a[180:183]\n" \
"ds_read_b128 a[20:23], v73 offset:4096\n" \
"v_mfma_f32_16x16x32_bf16 a[180:183], a[28:31], a[108:111], a[180:183]\n" \
"buffer_load_dwordx4 v67, s[36:39], s82 offen offset:3072 lds\n" \
"v_mfma_f32_16x16x32_bf16 a[176:179], a[24:27], a[96:99], a[176:179]\n" \
"s_mov_b64 s[64:65], s[12:13]\n" \
"s_add_u32 s68, s67, 0x100000\n" \
"ds_read_b64_tr_b16 a[82:83], v74 offset:37376\n" \
"v_mfma_f32_16x16x32_bf16 a[176:179], a[28:31], a[100:103], a[176:179]\n" \
"s_add_u32 s64, s64, s68\n" \
"s_addc_u32 s65, s65, 0\n" \
"v_mfma_f32_16x16x32_bf16 a[192:195], a[32:35], a[96:99], a[192:195]\n" \
"s_xor_b32 s82, s82, 0x80\n" \
"ds_read_b128 a[24:27], v72 offset:6144\n" \
"v_mfma_f32_16x16x32_bf16 a[192:195], a[36:39], a[100:103], a[192:195]\n" \
"global_load_dword v238, v234, s[64:65]\n" \
"v_mfma_f32_16x16x32_bf16 a[196:199], a[32:35], a[104:107], a[196:199]\n" \
"s_mov_b64 s[64:65], s[12:13]\n" \
"s_add_u32 s68, s67, 0x500000\n" \
"ds_read_b64_tr_b16 a[84:85], v74 offset:49664\n" \
"v_mfma_f32_16x16x32_bf16 a[196:199], a[36:39], a[108:111], a[196:199]\n" \
"s_add_u32 s64, s64, s68\n" \
"s_addc_u32 s65, s65, 0\n" \
"v_mfma_f32_16x16x32_bf16 a[200:203], a[32:35], a[112:115], a[200:203]\n" \
"ds_read_b128 a[28:31], v73 offset:6144\n" \
"v_mfma_f32_16x16x32_bf16 a[200:203], a[36:39], a[116:119], a[200:203]\n" \
"global_load_dword v238, v234, s[64:65]\n" \
"v_mfma_f32_16x16x32_bf16 a[204:207], a[32:35], a[120:123], a[204:207]\n" \
"s_mov_b64 s[64:65], s[12:13]\n" \
"s_add_u32 s68, s67, 0x200000\n" \
"ds_read_b64_tr_b16 a[86:87], v74 offset:53760\n" \
"v_mfma_f32_16x16x32_bf16 a[204:207], a[36:39], a[124:127], a[204:207]\n" \
"s_add_u32 s64, s64, s68\n" \
"s_addc_u32 s65, s65, 0\n" \
"v_mfma_f32_16x16x32_bf16 a[220:223], a[40:43], a[120:123], a[220:223]\n" \
"ds_read_b128 a[32:35], v72 offset:8192\n" \
"v_mfma_f32_16x16x32_bf16 a[220:223], a[44:47], a[124:127], a[220:223]\n" \
"global_load_dword v238, v234, s[64:65]\n" \
"v_mfma_f32_16x16x32_bf16 a[216:219], a[40:43], a[112:115], a[216:219]\n" \
"ds_read_b64_tr_b16 a[88:89], v75 offset:33280\n" \
"v_mfma_f32_16x16x32_bf16 a[216:219], a[44:47], a[116:119], a[216:219]\n" \
"v_mfma_f32_16x16x32_bf16 a[212:215], a[40:43], a[104:107], a[212:215]\n" \
"ds_read_b128 a[36:39], v73 offset:8192\n" \
"v_mfma_f32_16x16x32_bf16 a[212:215], a[44:47], a[108:111], a[212:215]\n" \
"v_mfma_f32_16x16x32_bf16 a[208:211], a[40:43], a[96:99], a[208:211]\n" \
"ds_read_b64_tr_b16 a[90:91], v75 offset:37376\n" \
"v_mfma_f32_16x16x32_bf16 a[208:211], a[44:47], a[100:103], a[208:211]\n" \
"v_mfma_f32_16x16x32_bf16 a[224:227], a[48:51], a[96:99], a[224:227]\n" \
"ds_read_b128 a[40:43], v72 offset:10240\n" \
"v_mfma_f32_16x16x32_bf16 a[224:227], a[52:55], a[100:103], a[224:227]\n" \
"v_mfma_f32_16x16x32_bf16 a[228:231], a[48:51], a[104:107], a[228:231]\n" \
"ds_read_b64_tr_b16 a[92:93], v75 offset:49664\n" \
"v_mfma_f32_16x16x32_bf16 a[228:231], a[52:55], a[108:111], a[228:231]\n" \
"v_mfma_f32_16x16x32_bf16 a[232:235], a[48:51], a[112:115], a[232:235]\n" \
"ds_read_b128 a[44:47], v73 offset:10240\n" \
"v_mfma_f32_16x16x32_bf16 a[232:235], a[52:55], a[116:119], a[232:235]\n" \
"v_mfma_f32_16x16x32_bf16 a[236:239], a[48:51], a[120:123], a[236:239]\n" \
"ds_read_b64_tr_b16 a[94:95], v75 offset:53760\n" \
"v_mfma_f32_16x16x32_bf16 a[236:239], a[52:55], a[124:127], a[236:239]\n" \
"v_mfma_f32_16x16x32_bf16 a[252:255], a[56:59], a[120:123], a[252:255]\n" \
"ds_read_b128 a[48:51], v72 offset:12288\n" \
"v_mfma_f32_16x16x32_bf16 a[252:255], a[60:63], a[124:127], a[252:255]\n" \
"v_mfma_f32_16x16x32_bf16 a[248:251], a[56:59], a[112:115], a[248:251]\n" \
"v_mfma_f32_16x16x32_bf16 a[248:251], a[60:63], a[116:119], a[248:251]\n" \
"v_mfma_f32_16x16x32_bf16 a[244:247], a[56:59], a[104:107], a[244:247]\n" \
"ds_read_b128 a[52:55], v73 offset:12288\n" \
"v_mfma_f32_16x16x32_bf16 a[244:247], a[60:63], a[108:111], a[244:247]\n" \
"v_mfma_f32_16x16x32_bf16 a[240:243], a[56:59], a[96:99], a[240:243]\n" \
"v_mfma_f32_16x16x32_bf16 a[240:243], a[60:63], a[100:103], a[240:243]\n" \
"s_waitcnt lgkmcnt(1)\n" \
"v_mfma_f32_16x16x32_bf16 v[0:3], a[64:67], a[0:3], v[0:3]\n" \
"ds_read_b128 a[56:59], v72 offset:14336\n" \
"v_mfma_f32_16x16x32_bf16 v[0:3], a[68:71], a[4:7], v[0:3]\n" \
"v_mfma_f32_16x16x32_bf16 v[4:7], a[72:75], a[0:3], v[4:7]\n" \
"s_add_u32 m0, s74, 98304\n" \
"ds_read_b128 a[60:63], v73 offset:14336\n" \
"v_mfma_f32_16x16x32_bf16 v[4:7], a[76:79], a[4:7], v[4:7]\n" \
"buffer_load_dwordx4 v68, s[40:43], s83 offen offset:0 lds\n" \
"v_mfma_f32_16x16x32_bf16 v[8:11], a[80:83], a[0:3], v[8:11]\n" \
"ds_read_b64_tr_b16 a[96:97], v74 offset:33792\n" \
"v_mfma_f32_16x16x32_bf16 v[8:11], a[84:87], a[4:7], v[8:11]\n" \
"v_mfma_f32_16x16x32_bf16 v[12:15], a[88:91], a[0:3], v[12:15]\n" \
"ds_read_b64_tr_b16 a[98:99], v74 offset:37888\n" \
"v_mfma_f32_16x16x32_bf16 v[12:15], a[92:95], a[4:7], v[12:15]\n" \
"buffer_load_dwordx4 v69, s[44:47], s83 offen offset:1024 lds\n" \
"v_mfma_f32_16x16x32_bf16 v[28:31], a[88:91], a[8:11], v[28:31]\n" \
"ds_read_b64_tr_b16 a[100:101], v74 offset:50176\n" \
"v_mfma_f32_16x16x32_bf16 v[28:31], a[92:95], a[12:15], v[28:31]\n" \
"v_mfma_f32_16x16x32_bf16 v[24:27], a[80:83], a[8:11], v[24:27]\n" \
"ds_read_b64_tr_b16 a[102:103], v74 offset:54272\n" \
"v_mfma_f32_16x16x32_bf16 v[24:27], a[84:87], a[12:15], v[24:27]\n" \
"buffer_load_dwordx4 v70, s[40:43], s83 offen offset:2048 lds\n" \
"v_mfma_f32_16x16x32_bf16 v[20:23], a[72:75], a[8:11], v[20:23]\n" \
"ds_read_b64_tr_b16 a[104:105], v75 offset:33792\n" \
"v_mfma_f32_16x16x32_bf16 v[20:23], a[76:79], a[12:15], v[20:23]\n" \
"v_mfma_f32_16x16x32_bf16 v[16:19], a[64:67], a[8:11], v[16:19]\n" \
"ds_read_b64_tr_b16 a[106:107], v75 offset:37888\n" \
"v_mfma_f32_16x16x32_bf16 v[16:19], a[68:71], a[12:15], v[16:19]\n" \
"buffer_load_dwordx4 v71, s[44:47], s83 offen offset:3072 lds\n" \
"v_mfma_f32_16x16x32_bf16 v[32:35], a[64:67], a[16:19], v[32:35]\n" \
"ds_read_b64_tr_b16 a[108:109], v75 offset:50176\n" \
"v_mfma_f32_16x16x32_bf16 v[32:35], a[68:71], a[20:23], v[32:35]\n" \
"v_mfma_f32_16x16x32_bf16 v[36:39], a[72:75], a[16:19], v[36:39]\n" \
"s_add_u32 m0, s74, 102400\n" \
"ds_read_b64_tr_b16 a[110:111], v75 offset:54272\n" \
"v_mfma_f32_16x16x32_bf16 v[36:39], a[76:79], a[20:23], v[36:39]\n" \
"buffer_load_dwordx4 v68, s[40:43], s84 offen offset:0 lds\n" \
"v_mfma_f32_16x16x32_bf16 v[40:43], a[80:83], a[16:19], v[40:43]\n" \
"ds_read_b64_tr_b16 a[112:113], v74 offset:34304\n" \
"v_mfma_f32_16x16x32_bf16 v[40:43], a[84:87], a[20:23], v[40:43]\n" \
"v_mfma_f32_16x16x32_bf16 v[44:47], a[88:91], a[16:19], v[44:47]\n" \
"ds_read_b64_tr_b16 a[114:115], v74 offset:38400\n" \
"v_mfma_f32_16x16x32_bf16 v[44:47], a[92:95], a[20:23], v[44:47]\n" \
"buffer_load_dwordx4 v69, s[44:47], s84 offen offset:1024 lds\n" \
"v_mfma_f32_16x16x32_bf16 v[60:63], a[88:91], a[24:27], v[60:63]\n" \
"ds_read_b64_tr_b16 a[116:117], v74 offset:50688\n" \
"v_mfma_f32_16x16x32_bf16 v[60:63], a[92:95], a[28:31], v[60:63]\n" \
"v_mfma_f32_16x16x32_bf16 v[56:59], a[80:83], a[24:27], v[56:59]\n" \
"ds_read_b64_tr_b16 a[118:119], v74 offset:54784\n" \
"v_mfma_f32_16x16x32_bf16 v[56:59], a[84:87], a[28:31], v[56:59]\n" \
"buffer_load_dwordx4 v70, s[40:43], s84 offen offset:2048 lds\n" \
"v_mfma_f32_16x16x32_bf16 v[52:55], a[72:75], a[24:27], v[52:55]\n" \
"ds_read_b64_tr_b16 a[120:121], v75 offset:34304\n" \
"v_mfma_f32_16x16x32_bf16 v[52:55], a[76:79], a[28:31], v[52:55]\n" \
"v_mfma_f32_16x16x32_bf16 v[48:51], a[64:67], a[24:27], v[48:51]\n" \
"ds_read_b64_tr_b16 a[122:123], v75 offset:38400\n" \
"v_mfma_f32_16x16x32_bf16 v[48:51], a[68:71], a[28:31], v[48:51]\n" \
"buffer_load_dwordx4 v71, s[44:47], s84 offen offset:3072 lds\n" \
"v_mfma_f32_16x16x32_bf16 v[144:147], a[64:67], a[32:35], v[144:147]\n" \
"ds_read_b64_tr_b16 a[124:125], v75 offset:50688\n" \
"v_mfma_f32_16x16x32_bf16 v[144:147], a[68:71], a[36:39], v[144:147]\n" \
"v_mfma_f32_16x16x32_bf16 v[148:151], a[72:75], a[32:35], v[148:151]\n" \
"ds_read_b64_tr_b16 a[126:127], v75 offset:54784\n" \
"v_mfma_f32_16x16x32_bf16 v[148:151], a[76:79], a[36:39], v[148:151]\n" \
"v_mfma_f32_16x16x32_bf16 v[152:155], a[80:83], a[32:35], v[152:155]\n" \
"v_mfma_f32_16x16x32_bf16 v[152:155], a[84:87], a[36:39], v[152:155]\n" \
"v_mfma_f32_16x16x32_bf16 v[156:159], a[88:91], a[32:35], v[156:159]\n" \
"v_mfma_f32_16x16x32_bf16 v[156:159], a[92:95], a[36:39], v[156:159]\n" \
"v_mfma_f32_16x16x32_bf16 v[188:191], a[88:91], a[40:43], v[188:191]\n" \
"v_mfma_f32_16x16x32_bf16 v[188:191], a[92:95], a[44:47], v[188:191]\n" \
"v_mfma_f32_16x16x32_bf16 v[184:187], a[80:83], a[40:43], v[184:187]\n" \
"v_mfma_f32_16x16x32_bf16 v[184:187], a[84:87], a[44:47], v[184:187]\n" \
"v_mfma_f32_16x16x32_bf16 v[180:183], a[72:75], a[40:43], v[180:183]\n" \
"v_mfma_f32_16x16x32_bf16 v[180:183], a[76:79], a[44:47], v[180:183]\n" \
"v_mfma_f32_16x16x32_bf16 v[176:179], a[64:67], a[40:43], v[176:179]\n" \
"v_mfma_f32_16x16x32_bf16 v[176:179], a[68:71], a[44:47], v[176:179]\n" \
"s_waitcnt lgkmcnt(0)\n" \
"v_mfma_f32_16x16x32_bf16 v[192:195], a[64:67], a[48:51], v[192:195]\n" \
"v_mfma_f32_16x16x32_bf16 v[192:195], a[68:71], a[52:55], v[192:195]\n" \
"v_mfma_f32_16x16x32_bf16 v[196:199], a[72:75], a[48:51], v[196:199]\n" \
"v_mfma_f32_16x16x32_bf16 v[196:199], a[76:79], a[52:55], v[196:199]\n" \
"v_mfma_f32_16x16x32_bf16 v[200:203], a[80:83], a[48:51], v[200:203]\n" \
"v_mfma_f32_16x16x32_bf16 v[200:203], a[84:87], a[52:55], v[200:203]\n" \
"v_mfma_f32_16x16x32_bf16 v[204:207], a[88:91], a[48:51], v[204:207]\n" \
"v_mfma_f32_16x16x32_bf16 v[204:207], a[92:95], a[52:55], v[204:207]\n" \
"v_mfma_f32_16x16x32_bf16 v[220:223], a[88:91], a[56:59], v[220:223]\n" \
"v_mfma_f32_16x16x32_bf16 v[220:223], a[92:95], a[60:63], v[220:223]\n" \
"v_mfma_f32_16x16x32_bf16 v[216:219], a[80:83], a[56:59], v[216:219]\n" \
"v_mfma_f32_16x16x32_bf16 v[216:219], a[84:87], a[60:63], v[216:219]\n" \
"v_mfma_f32_16x16x32_bf16 v[212:215], a[72:75], a[56:59], v[212:215]\n" \
"v_mfma_f32_16x16x32_bf16 v[212:215], a[76:79], a[60:63], v[212:215]\n" \
"v_mfma_f32_16x16x32_bf16 v[208:211], a[64:67], a[56:59], v[208:211]\n" \
"v_mfma_f32_16x16x32_bf16 v[208:211], a[68:71], a[60:63], v[208:211]\n" \
"s_waitcnt vmcnt(11) lgkmcnt(0)\n" \
"s_barrier\n" \
"v_mfma_f32_16x16x32_bf16 a[128:131], a[0:3], a[96:99], a[128:131]\n" \
"s_add_u32 m0, s74, 32768\n" \
"ds_read_b64_tr_b16 a[64:65], v102 offset:0\n" \
"v_mfma_f32_16x16x32_bf16 a[128:131], a[4:7], a[100:103], a[128:131]\n" \
"buffer_load_dwordx4 v64, s[36:39], s81 offen offset:0 lds\n" \
"v_mfma_f32_16x16x32_bf16 a[132:135], a[0:3], a[104:107], a[132:135]\n" \
"ds_read_b64_tr_b16 a[66:67], v102 offset:4096\n" \
"v_mfma_f32_16x16x32_bf16 a[132:135], a[4:7], a[108:111], a[132:135]\n" \
"v_mfma_f32_16x16x32_bf16 a[136:139], a[0:3], a[112:115], a[136:139]\n" \
"ds_read_b64_tr_b16 a[68:69], v102 offset:16384\n" \
"v_mfma_f32_16x16x32_bf16 a[136:139], a[4:7], a[116:119], a[136:139]\n" \
"buffer_load_dwordx4 v65, s[36:39], s81 offen offset:1024 lds\n" \
"v_mfma_f32_16x16x32_bf16 a[140:143], a[0:3], a[120:123], a[140:143]\n" \
"ds_read_b64_tr_b16 a[70:71], v102 offset:20480\n" \
"v_mfma_f32_16x16x32_bf16 a[140:143], a[4:7], a[124:127], a[140:143]\n" \
"v_mfma_f32_16x16x32_bf16 a[156:159], a[8:11], a[120:123], a[156:159]\n" \
"ds_read_b128 a[0:3], v96 offset:0\n" \
"v_mfma_f32_16x16x32_bf16 a[156:159], a[12:15], a[124:127], a[156:159]\n" \
"buffer_load_dwordx4 v66, s[36:39], s81 offen offset:2048 lds\n" \
"v_mfma_f32_16x16x32_bf16 a[152:155], a[8:11], a[112:115], a[152:155]\n" \
"ds_read_b64_tr_b16 a[72:73], v103 offset:0\n" \
"v_mfma_f32_16x16x32_bf16 a[152:155], a[12:15], a[116:119], a[152:155]\n" \
"v_mfma_f32_16x16x32_bf16 a[148:151], a[8:11], a[104:107], a[148:151]\n" \
"ds_read_b128 a[4:7], v97 offset:0\n" \
"v_mfma_f32_16x16x32_bf16 a[148:151], a[12:15], a[108:111], a[148:151]\n" \
"buffer_load_dwordx4 v67, s[36:39], s81 offen offset:3072 lds\n" \
"v_mfma_f32_16x16x32_bf16 a[144:147], a[8:11], a[96:99], a[144:147]\n" \
"ds_read_b64_tr_b16 a[74:75], v103 offset:4096\n" \
"v_mfma_f32_16x16x32_bf16 a[144:147], a[12:15], a[100:103], a[144:147]\n" \
"v_mfma_f32_16x16x32_bf16 a[160:163], a[16:19], a[96:99], a[160:163]\n" \
"s_add_u32 m0, s74, 36864\n" \
"ds_read_b128 a[8:11], v96 offset:2048\n" \
"v_mfma_f32_16x16x32_bf16 a[160:163], a[20:23], a[100:103], a[160:163]\n" \
"buffer_load_dwordx4 v64, s[36:39], s82 offen offset:0 lds\n" \
"v_mfma_f32_16x16x32_bf16 a[164:167], a[16:19], a[104:107], a[164:167]\n" \
"ds_read_b64_tr_b16 a[76:77], v103 offset:16384\n" \
"v_mfma_f32_16x16x32_bf16 a[164:167], a[20:23], a[108:111], a[164:167]\n" \
"v_mfma_f32_16x16x32_bf16 a[168:171], a[16:19], a[112:115], a[168:171]\n" \
"ds_read_b128 a[12:15], v97 offset:2048\n" \
"v_mfma_f32_16x16x32_bf16 a[168:171], a[20:23], a[116:119], a[168:171]\n" \
"buffer_load_dwordx4 v65, s[36:39], s82 offen offset:1024 lds\n" \
"v_mfma_f32_16x16x32_bf16 a[172:175], a[16:19], a[120:123], a[172:175]\n" \
"ds_read_b64_tr_b16 a[78:79], v103 offset:20480\n" \
"v_mfma_f32_16x16x32_bf16 a[172:175], a[20:23], a[124:127], a[172:175]\n" \
"v_mfma_f32_16x16x32_bf16 a[188:191], a[24:27], a[120:123], a[188:191]\n" \
"ds_read_b128 a[16:19], v96 offset:4096\n" \
"v_mfma_f32_16x16x32_bf16 a[188:191], a[28:31], a[124:127], a[188:191]\n" \
"buffer_load_dwordx4 v66, s[36:39], s82 offen offset:2048 lds\n" \
"v_mfma_f32_16x16x32_bf16 a[184:187], a[24:27], a[112:115], a[184:187]\n" \
"ds_read_b64_tr_b16 a[80:81], v102 offset:512\n" \
"v_mfma_f32_16x16x32_bf16 a[184:187], a[28:31], a[116:119], a[184:187]\n" \
"v_mfma_f32_16x16x32_bf16 a[180:183], a[24:27], a[104:107], a[180:183]\n" \
"ds_read_b128 a[20:23], v97 offset:4096\n" \
"v_mfma_f32_16x16x32_bf16 a[180:183], a[28:31], a[108:111], a[180:183]\n" \
"buffer_load_dwordx4 v67, s[36:39], s82 offen offset:3072 lds\n" \
"v_mfma_f32_16x16x32_bf16 a[176:179], a[24:27], a[96:99], a[176:179]\n" \
"s_mov_b64 s[64:65], s[12:13]\n" \
"s_add_u32 s68, s67, 0x300000\n" \
"ds_read_b64_tr_b16 a[82:83], v102 offset:4608\n" \
"v_mfma_f32_16x16x32_bf16 a[176:179], a[28:31], a[100:103], a[176:179]\n" \
"s_add_u32 s64, s64, s68\n" \
"s_addc_u32 s65, s65, 0\n" \
"v_mfma_f32_16x16x32_bf16 a[192:195], a[32:35], a[96:99], a[192:195]\n" \
"ds_read_b128 a[24:27], v96 offset:6144\n" \
"v_mfma_f32_16x16x32_bf16 a[192:195], a[36:39], a[100:103], a[192:195]\n" \
"global_load_dword v238, v234, s[64:65]\n" \
"v_mfma_f32_16x16x32_bf16 a[196:199], a[32:35], a[104:107], a[196:199]\n" \
"s_mov_b64 s[64:65], s[12:13]\n" \
"s_add_u32 s68, s67, 0x400000\n" \
"ds_read_b64_tr_b16 a[84:85], v102 offset:16896\n" \
"v_mfma_f32_16x16x32_bf16 a[196:199], a[36:39], a[108:111], a[196:199]\n" \
"s_add_u32 s64, s64, s68\n" \
"s_addc_u32 s65, s65, 0\n" \
"v_mfma_f32_16x16x32_bf16 a[200:203], a[32:35], a[112:115], a[200:203]\n" \
"ds_read_b128 a[28:31], v97 offset:6144\n" \
"v_mfma_f32_16x16x32_bf16 a[200:203], a[36:39], a[116:119], a[200:203]\n" \
"global_load_dword v238, v234, s[64:65]\n" \
"v_mfma_f32_16x16x32_bf16 a[204:207], a[32:35], a[120:123], a[204:207]\n" \
"s_mov_b64 s[64:65], s[12:13]\n" \
"s_add_u32 s68, s67, 0x600000\n" \
"ds_read_b64_tr_b16 a[86:87], v102 offset:20992\n" \
"v_mfma_f32_16x16x32_bf16 a[204:207], a[36:39], a[124:127], a[204:207]\n" \
"s_add_u32 s64, s64, s68\n" \
"s_addc_u32 s65, s65, 0\n" \
"v_mfma_f32_16x16x32_bf16 a[220:223], a[40:43], a[120:123], a[220:223]\n" \
"ds_read_b128 a[32:35], v96 offset:8192\n" \
"v_mfma_f32_16x16x32_bf16 a[220:223], a[44:47], a[124:127], a[220:223]\n" \
"global_load_dword v238, v234, s[64:65]\n" \
"v_mfma_f32_16x16x32_bf16 a[216:219], a[40:43], a[112:115], a[216:219]\n" \
"ds_read_b64_tr_b16 a[88:89], v103 offset:512\n" \
"v_mfma_f32_16x16x32_bf16 a[216:219], a[44:47], a[116:119], a[216:219]\n" \
"v_mfma_f32_16x16x32_bf16 a[212:215], a[40:43], a[104:107], a[212:215]\n" \
"ds_read_b128 a[36:39], v97 offset:8192\n" \
"v_mfma_f32_16x16x32_bf16 a[212:215], a[44:47], a[108:111], a[212:215]\n" \
"v_mfma_f32_16x16x32_bf16 a[208:211], a[40:43], a[96:99], a[208:211]\n" \
"ds_read_b64_tr_b16 a[90:91], v103 offset:4608\n" \
"v_mfma_f32_16x16x32_bf16 a[208:211], a[44:47], a[100:103], a[208:211]\n" \
"v_mfma_f32_16x16x32_bf16 a[224:227], a[48:51], a[96:99], a[224:227]\n" \
"ds_read_b128 a[40:43], v96 offset:10240\n" \
"v_mfma_f32_16x16x32_bf16 a[224:227], a[52:55], a[100:103], a[224:227]\n" \
"v_mfma_f32_16x16x32_bf16 a[228:231], a[48:51], a[104:107], a[228:231]\n" \
"ds_read_b64_tr_b16 a[92:93], v103 offset:16896\n" \
"v_mfma_f32_16x16x32_bf16 a[228:231], a[52:55], a[108:111], a[228:231]\n" \
"v_mfma_f32_16x16x32_bf16 a[232:235], a[48:51], a[112:115], a[232:235]\n" \
"ds_read_b128 a[44:47], v97 offset:10240\n" \
"v_mfma_f32_16x16x32_bf16 a[232:235], a[52:55], a[116:119], a[232:235]\n" \
"v_mfma_f32_16x16x32_bf16 a[236:239], a[48:51], a[120:123], a[236:239]\n" \
"ds_read_b64_tr_b16 a[94:95], v103 offset:20992\n" \
"v_mfma_f32_16x16x32_bf16 a[236:239], a[52:55], a[124:127], a[236:239]\n" \
"v_mfma_f32_16x16x32_bf16 a[252:255], a[56:59], a[120:123], a[252:255]\n" \
"ds_read_b128 a[48:51], v96 offset:12288\n" \
"v_mfma_f32_16x16x32_bf16 a[252:255], a[60:63], a[124:127], a[252:255]\n" \
"v_mfma_f32_16x16x32_bf16 a[248:251], a[56:59], a[112:115], a[248:251]\n" \
"v_mfma_f32_16x16x32_bf16 a[248:251], a[60:63], a[116:119], a[248:251]\n" \
"v_mfma_f32_16x16x32_bf16 a[244:247], a[56:59], a[104:107], a[244:247]\n" \
"ds_read_b128 a[52:55], v97 offset:12288\n" \
"v_mfma_f32_16x16x32_bf16 a[244:247], a[60:63], a[108:111], a[244:247]\n" \
"v_mfma_f32_16x16x32_bf16 a[240:243], a[56:59], a[96:99], a[240:243]\n" \
"v_mfma_f32_16x16x32_bf16 a[240:243], a[60:63], a[100:103], a[240:243]\n" \
"s_waitcnt lgkmcnt(1)\n" \
"v_mfma_f32_16x16x32_bf16 v[0:3], a[64:67], a[0:3], v[0:3]\n" \
"ds_read_b128 a[56:59], v96 offset:14336\n" \
"Lpg1663_%=:\n" \
"Lst0x2_%=:\n" \
"s_cbranch_execz Lpg3897_%=\n" \
"v_mfma_f32_16x16x32_bf16 v[0:3], a[68:71], a[4:7], v[0:3]\n" \
"v_mfma_f32_16x16x32_bf16 v[4:7], a[72:75], a[0:3], v[4:7]\n" \
"s_add_u32 m0, s79, 0\n" \
"ds_read_b128 a[60:63], v97 offset:14336\n" \
"v_mfma_f32_16x16x32_bf16 v[4:7], a[76:79], a[4:7], v[4:7]\n" \
"buffer_load_dwordx4 v228, s[52:55], s78 offen offset:0 lds\n" \
"v_mfma_f32_16x16x32_bf16 v[8:11], a[80:83], a[0:3], v[8:11]\n" \
"ds_read_b64_tr_b16 a[96:97], v102 offset:1024\n" \
"v_mfma_f32_16x16x32_bf16 v[8:11], a[84:87], a[4:7], v[8:11]\n" \
"v_mfma_f32_16x16x32_bf16 v[12:15], a[88:91], a[0:3], v[12:15]\n" \
"ds_read_b64_tr_b16 a[98:99], v102 offset:5120\n" \
"v_mfma_f32_16x16x32_bf16 v[12:15], a[92:95], a[4:7], v[12:15]\n" \
"buffer_load_dwordx4 v229, s[52:55], s78 offen offset:1024 lds\n" \
"v_mfma_f32_16x16x32_bf16 v[28:31], a[88:91], a[8:11], v[28:31]\n" \
"ds_read_b64_tr_b16 a[100:101], v102 offset:17408\n" \
"v_mfma_f32_16x16x32_bf16 v[28:31], a[92:95], a[12:15], v[28:31]\n" \
"v_mfma_f32_16x16x32_bf16 v[24:27], a[80:83], a[8:11], v[24:27]\n" \
"s_xor_b32 s78, s78, 0x180000\n" \
"ds_read_b64_tr_b16 a[102:103], v102 offset:21504\n" \
"v_mfma_f32_16x16x32_bf16 v[24:27], a[84:87], a[12:15], v[24:27]\n" \
"buffer_load_dwordx4 v228, s[52:55], s89 offen offset:2048 lds\n" \
"v_mfma_f32_16x16x32_bf16 v[20:23], a[72:75], a[8:11], v[20:23]\n" \
"ds_read_b64_tr_b16 a[104:105], v103 offset:1024\n" \
"v_mfma_f32_16x16x32_bf16 v[20:23], a[76:79], a[12:15], v[20:23]\n" \
"v_mfma_f32_16x16x32_bf16 v[16:19], a[64:67], a[8:11], v[16:19]\n" \
"ds_read_b64_tr_b16 a[106:107], v103 offset:5120\n" \
"v_mfma_f32_16x16x32_bf16 v[16:19], a[68:71], a[12:15], v[16:19]\n" \
"buffer_load_dwordx4 v229, s[52:55], s89 offen offset:3072 lds\n" \
"v_mfma_f32_16x16x32_bf16 v[32:35], a[64:67], a[16:19], v[32:35]\n" \
"ds_read_b64_tr_b16 a[108:109], v103 offset:17408\n" \
"v_mfma_f32_16x16x32_bf16 v[32:35], a[68:71], a[20:23], v[32:35]\n" \
"s_xor_b32 s89, s89, 0x180000\n" \
"v_mfma_f32_16x16x32_bf16 v[36:39], a[72:75], a[16:19], v[36:39]\n" \
"s_add_u32 m0, s79, 16384\n" \
"ds_read_b64_tr_b16 a[110:111], v103 offset:21504\n" \
"v_mfma_f32_16x16x32_bf16 v[36:39], a[76:79], a[20:23], v[36:39]\n" \
"buffer_load_dwordx4 v228, s[52:55], s78 offen offset:0 lds\n" \
"v_mfma_f32_16x16x32_bf16 v[40:43], a[80:83], a[16:19], v[40:43]\n" \
"ds_read_b64_tr_b16 a[112:113], v102 offset:1536\n" \
"v_mfma_f32_16x16x32_bf16 v[40:43], a[84:87], a[20:23], v[40:43]\n" \
"v_mfma_f32_16x16x32_bf16 v[44:47], a[88:91], a[16:19], v[44:47]\n" \
"ds_read_b64_tr_b16 a[114:115], v102 offset:5632\n" \
"v_mfma_f32_16x16x32_bf16 v[44:47], a[92:95], a[20:23], v[44:47]\n" \
"buffer_load_dwordx4 v229, s[52:55], s78 offen offset:1024 lds\n" \
"v_mfma_f32_16x16x32_bf16 v[60:63], a[88:91], a[24:27], v[60:63]\n" \
"ds_read_b64_tr_b16 a[116:117], v102 offset:17920\n" \
"v_mfma_f32_16x16x32_bf16 v[60:63], a[92:95], a[28:31], v[60:63]\n" \
"v_mfma_f32_16x16x32_bf16 v[56:59], a[80:83], a[24:27], v[56:59]\n" \
"s_xor_b32 s78, s78, 0x400000\n" \
"ds_read_b64_tr_b16 a[118:119], v102 offset:22016\n" \
"v_mfma_f32_16x16x32_bf16 v[56:59], a[84:87], a[28:31], v[56:59]\n" \
"buffer_load_dwordx4 v228, s[52:55], s89 offen offset:2048 lds\n" \
"v_mfma_f32_16x16x32_bf16 v[52:55], a[72:75], a[24:27], v[52:55]\n" \
"ds_read_b64_tr_b16 a[120:121], v103 offset:1536\n" \
"v_mfma_f32_16x16x32_bf16 v[52:55], a[76:79], a[28:31], v[52:55]\n" \
"v_mfma_f32_16x16x32_bf16 v[48:51], a[64:67], a[24:27], v[48:51]\n" \
"ds_read_b64_tr_b16 a[122:123], v103 offset:5632\n" \
"v_mfma_f32_16x16x32_bf16 v[48:51], a[68:71], a[28:31], v[48:51]\n" \
"buffer_load_dwordx4 v229, s[52:55], s89 offen offset:3072 lds\n" \
"v_mfma_f32_16x16x32_bf16 v[144:147], a[64:67], a[32:35], v[144:147]\n" \
"ds_read_b64_tr_b16 a[124:125], v103 offset:17920\n" \
"v_mfma_f32_16x16x32_bf16 v[144:147], a[68:71], a[36:39], v[144:147]\n" \
"v_mfma_f32_16x16x32_bf16 v[148:151], a[72:75], a[32:35], v[148:151]\n" \
"s_xor_b32 s89, s89, 0x400000\n" \
"ds_read_b64_tr_b16 a[126:127], v103 offset:22016\n" \
"v_mfma_f32_16x16x32_bf16 v[148:151], a[76:79], a[36:39], v[148:151]\n" \
"v_mfma_f32_16x16x32_bf16 v[152:155], a[80:83], a[32:35], v[152:155]\n" \
"v_mfma_f32_16x16x32_bf16 v[152:155], a[84:87], a[36:39], v[152:155]\n" \
"v_mfma_f32_16x16x32_bf16 v[156:159], a[88:91], a[32:35], v[156:159]\n" \
"v_mfma_f32_16x16x32_bf16 v[156:159], a[92:95], a[36:39], v[156:159]\n" \
"v_mfma_f32_16x16x32_bf16 v[188:191], a[88:91], a[40:43], v[188:191]\n" \
"v_mfma_f32_16x16x32_bf16 v[188:191], a[92:95], a[44:47], v[188:191]\n" \
"v_mfma_f32_16x16x32_bf16 v[184:187], a[80:83], a[40:43], v[184:187]\n" \
"v_mfma_f32_16x16x32_bf16 v[184:187], a[84:87], a[44:47], v[184:187]\n" \
"v_mfma_f32_16x16x32_bf16 v[180:183], a[72:75], a[40:43], v[180:183]\n" \
"v_mfma_f32_16x16x32_bf16 v[180:183], a[76:79], a[44:47], v[180:183]\n" \
"v_mfma_f32_16x16x32_bf16 v[176:179], a[64:67], a[40:43], v[176:179]\n" \
"v_mfma_f32_16x16x32_bf16 v[176:179], a[68:71], a[44:47], v[176:179]\n" \
"s_waitcnt lgkmcnt(0)\n" \
"v_mfma_f32_16x16x32_bf16 v[192:195], a[64:67], a[48:51], v[192:195]\n" \
"v_mfma_f32_16x16x32_bf16 v[192:195], a[68:71], a[52:55], v[192:195]\n" \
"v_mfma_f32_16x16x32_bf16 v[196:199], a[72:75], a[48:51], v[196:199]\n" \
"v_mfma_f32_16x16x32_bf16 v[196:199], a[76:79], a[52:55], v[196:199]\n" \
"v_mfma_f32_16x16x32_bf16 v[200:203], a[80:83], a[48:51], v[200:203]\n" \
"v_mfma_f32_16x16x32_bf16 v[200:203], a[84:87], a[52:55], v[200:203]\n" \
"v_mfma_f32_16x16x32_bf16 v[204:207], a[88:91], a[48:51], v[204:207]\n" \
"v_mfma_f32_16x16x32_bf16 v[204:207], a[92:95], a[52:55], v[204:207]\n" \
"v_mfma_f32_16x16x32_bf16 v[220:223], a[88:91], a[56:59], v[220:223]\n" \
"v_mfma_f32_16x16x32_bf16 v[220:223], a[92:95], a[60:63], v[220:223]\n" \
"v_mfma_f32_16x16x32_bf16 v[216:219], a[80:83], a[56:59], v[216:219]\n" \
"v_mfma_f32_16x16x32_bf16 v[216:219], a[84:87], a[60:63], v[216:219]\n" \
"v_mfma_f32_16x16x32_bf16 v[212:215], a[72:75], a[56:59], v[212:215]\n" \
"v_mfma_f32_16x16x32_bf16 v[212:215], a[76:79], a[60:63], v[212:215]\n" \
"v_mfma_f32_16x16x32_bf16 v[208:211], a[64:67], a[56:59], v[208:211]\n" \
"v_mfma_f32_16x16x32_bf16 v[208:211], a[68:71], a[60:63], v[208:211]\n" \
"s_waitcnt vmcnt(11) lgkmcnt(0)\n" \
"s_barrier\n" \
"v_mfma_f32_16x16x32_bf16 a[128:131], a[0:3], a[96:99], a[128:131]\n" \
"s_add_u32 m0, s74, 131072\n" \
"ds_read_b64_tr_b16 a[64:65], v98 offset:32768\n" \
"v_mfma_f32_16x16x32_bf16 a[128:131], a[4:7], a[100:103], a[128:131]\n" \
"buffer_load_dwordx4 v224, s[48:51], s75 offen offset:0 lds\n" \
"v_mfma_f32_16x16x32_bf16 a[132:135], a[0:3], a[104:107], a[132:135]\n" \
"ds_read_b64_tr_b16 a[66:67], v98 offset:36864\n" \
"v_mfma_f32_16x16x32_bf16 a[132:135], a[4:7], a[108:111], a[132:135]\n" \
"v_mfma_f32_16x16x32_bf16 a[136:139], a[0:3], a[112:115], a[136:139]\n" \
"ds_read_b64_tr_b16 a[68:69], v98 offset:49152\n" \
"v_mfma_f32_16x16x32_bf16 a[136:139], a[4:7], a[116:119], a[136:139]\n" \
"buffer_load_dwordx4 v225, s[48:51], s75 offen offset:1024 lds\n" \
"v_mfma_f32_16x16x32_bf16 a[140:143], a[0:3], a[120:123], a[140:143]\n" \
"ds_read_b64_tr_b16 a[70:71], v98 offset:53248\n" \
"v_mfma_f32_16x16x32_bf16 a[140:143], a[4:7], a[124:127], a[140:143]\n" \
"v_mfma_f32_16x16x32_bf16 a[156:159], a[8:11], a[120:123], a[156:159]\n" \
"ds_read_b128 a[0:3], v72 offset:32768\n" \
"v_mfma_f32_16x16x32_bf16 a[156:159], a[12:15], a[124:127], a[156:159]\n" \
"buffer_load_dwordx4 v226, s[48:51], s75 offen offset:2048 lds\n" \
"v_mfma_f32_16x16x32_bf16 a[152:155], a[8:11], a[112:115], a[152:155]\n" \
"ds_read_b64_tr_b16 a[72:73], v99 offset:32768\n" \
"v_mfma_f32_16x16x32_bf16 a[152:155], a[12:15], a[116:119], a[152:155]\n" \
"v_mfma_f32_16x16x32_bf16 a[148:151], a[8:11], a[104:107], a[148:151]\n" \
"ds_read_b128 a[4:7], v73 offset:32768\n" \
"v_mfma_f32_16x16x32_bf16 a[148:151], a[12:15], a[108:111], a[148:151]\n" \
"buffer_load_dwordx4 v227, s[48:51], s75 offen offset:3072 lds\n" \
"v_mfma_f32_16x16x32_bf16 a[144:147], a[8:11], a[96:99], a[144:147]\n" \
"ds_read_b64_tr_b16 a[74:75], v99 offset:36864\n" \
"v_mfma_f32_16x16x32_bf16 a[144:147], a[12:15], a[100:103], a[144:147]\n" \
"s_xor_b32 s75, s75, 0x180\n" \
"v_mfma_f32_16x16x32_bf16 a[160:163], a[16:19], a[96:99], a[160:163]\n" \
"s_add_u32 m0, s74, 135168\n" \
"ds_read_b128 a[8:11], v72 offset:34816\n" \
"v_mfma_f32_16x16x32_bf16 a[160:163], a[20:23], a[100:103], a[160:163]\n" \
"buffer_load_dwordx4 v224, s[48:51], s77 offen offset:0 lds\n" \
"v_mfma_f32_16x16x32_bf16 a[164:167], a[16:19], a[104:107], a[164:167]\n" \
"ds_read_b64_tr_b16 a[76:77], v99 offset:49152\n" \
"v_mfma_f32_16x16x32_bf16 a[164:167], a[20:23], a[108:111], a[164:167]\n" \
"v_mfma_f32_16x16x32_bf16 a[168:171], a[16:19], a[112:115], a[168:171]\n" \
"ds_read_b128 a[12:15], v73 offset:34816\n" \
"v_mfma_f32_16x16x32_bf16 a[168:171], a[20:23], a[116:119], a[168:171]\n" \
"buffer_load_dwordx4 v225, s[48:51], s77 offen offset:1024 lds\n" \
"v_mfma_f32_16x16x32_bf16 a[172:175], a[16:19], a[120:123], a[172:175]\n" \
"ds_read_b64_tr_b16 a[78:79], v99 offset:53248\n" \
"v_mfma_f32_16x16x32_bf16 a[172:175], a[20:23], a[124:127], a[172:175]\n" \
"v_mfma_f32_16x16x32_bf16 a[188:191], a[24:27], a[120:123], a[188:191]\n" \
"ds_read_b128 a[16:19], v72 offset:36864\n" \
"v_mfma_f32_16x16x32_bf16 a[188:191], a[28:31], a[124:127], a[188:191]\n" \
"buffer_load_dwordx4 v226, s[48:51], s77 offen offset:2048 lds\n" \
"v_mfma_f32_16x16x32_bf16 a[184:187], a[24:27], a[112:115], a[184:187]\n" \
"ds_read_b64_tr_b16 a[80:81], v98 offset:33280\n" \
"v_mfma_f32_16x16x32_bf16 a[184:187], a[28:31], a[116:119], a[184:187]\n" \
"v_mfma_f32_16x16x32_bf16 a[180:183], a[24:27], a[104:107], a[180:183]\n" \
"ds_read_b128 a[20:23], v73 offset:36864\n" \
"v_mfma_f32_16x16x32_bf16 a[180:183], a[28:31], a[108:111], a[180:183]\n" \
"buffer_load_dwordx4 v227, s[48:51], s77 offen offset:3072 lds\n" \
"v_mfma_f32_16x16x32_bf16 a[176:179], a[24:27], a[96:99], a[176:179]\n" \
"s_mov_b64 s[64:65], s[12:13]\n" \
"s_add_u32 s68, s67, 0x700000\n" \
"ds_read_b64_tr_b16 a[82:83], v98 offset:37376\n" \
"v_mfma_f32_16x16x32_bf16 a[176:179], a[28:31], a[100:103], a[176:179]\n" \
"s_add_u32 s64, s64, s68\n" \
"s_addc_u32 s65, s65, 0\n" \
"v_mfma_f32_16x16x32_bf16 a[192:195], a[32:35], a[96:99], a[192:195]\n" \
"s_xor_b32 s77, s77, 0x180\n" \
"ds_read_b128 a[24:27], v72 offset:38912\n" \
"v_mfma_f32_16x16x32_bf16 a[192:195], a[36:39], a[100:103], a[192:195]\n" \
"global_load_dword v238, v234, s[64:65]\n" \
"v_mfma_f32_16x16x32_bf16 a[196:199], a[32:35], a[104:107], a[196:199]\n" \
"ds_read_b64_tr_b16 a[84:85], v98 offset:49664\n" \
"v_mfma_f32_16x16x32_bf16 a[196:199], a[36:39], a[108:111], a[196:199]\n" \
"v_mfma_f32_16x16x32_bf16 a[200:203], a[32:35], a[112:115], a[200:203]\n" \
"s_lshr_b32 s68, s66, 2\n" \
"s_and_b32 s68, s68, 0xffff\n" \
"s_add_u32 s68, s68, 65585152\n" \
"ds_read_b128 a[28:31], v73 offset:38912\n" \
"v_mfma_f32_16x16x32_bf16 a[200:203], a[36:39], a[116:119], a[200:203]\n" \
"buffer_load_dword v238, v242, s[56:59], s68 offen sc1\n" \
"v_mfma_f32_16x16x32_bf16 a[204:207], a[32:35], a[120:123], a[204:207]\n" \
"ds_read_b64_tr_b16 a[86:87], v98 offset:53760\n" \
"v_mfma_f32_16x16x32_bf16 a[204:207], a[36:39], a[124:127], a[204:207]\n" \
"v_mfma_f32_16x16x32_bf16 a[220:223], a[40:43], a[120:123], a[220:223]\n" \
"ds_read_b128 a[32:35], v72 offset:40960\n" \
"v_mfma_f32_16x16x32_bf16 a[220:223], a[44:47], a[124:127], a[220:223]\n" \
"v_mfma_f32_16x16x32_bf16 a[216:219], a[40:43], a[112:115], a[216:219]\n" \
"ds_read_b64_tr_b16 a[88:89], v99 offset:33280\n" \
"v_mfma_f32_16x16x32_bf16 a[216:219], a[44:47], a[116:119], a[216:219]\n" \
"v_mfma_f32_16x16x32_bf16 a[212:215], a[40:43], a[104:107], a[212:215]\n" \
"ds_read_b128 a[36:39], v73 offset:40960\n" \
"v_mfma_f32_16x16x32_bf16 a[212:215], a[44:47], a[108:111], a[212:215]\n" \
"v_mfma_f32_16x16x32_bf16 a[208:211], a[40:43], a[96:99], a[208:211]\n" \
"ds_read_b64_tr_b16 a[90:91], v99 offset:37376\n" \
"v_mfma_f32_16x16x32_bf16 a[208:211], a[44:47], a[100:103], a[208:211]\n" \
"v_mfma_f32_16x16x32_bf16 a[224:227], a[48:51], a[96:99], a[224:227]\n" \
"ds_read_b128 a[40:43], v72 offset:43008\n" \
"v_mfma_f32_16x16x32_bf16 a[224:227], a[52:55], a[100:103], a[224:227]\n" \
"v_mfma_f32_16x16x32_bf16 a[228:231], a[48:51], a[104:107], a[228:231]\n" \
"ds_read_b64_tr_b16 a[92:93], v99 offset:49664\n" \
"v_mfma_f32_16x16x32_bf16 a[228:231], a[52:55], a[108:111], a[228:231]\n" \
"v_mfma_f32_16x16x32_bf16 a[232:235], a[48:51], a[112:115], a[232:235]\n" \
"ds_read_b128 a[44:47], v73 offset:43008\n" \
"v_mfma_f32_16x16x32_bf16 a[232:235], a[52:55], a[116:119], a[232:235]\n" \
"v_mfma_f32_16x16x32_bf16 a[236:239], a[48:51], a[120:123], a[236:239]\n" \
"ds_read_b64_tr_b16 a[94:95], v99 offset:53760\n" \
"v_mfma_f32_16x16x32_bf16 a[236:239], a[52:55], a[124:127], a[236:239]\n" \
"v_mfma_f32_16x16x32_bf16 a[252:255], a[56:59], a[120:123], a[252:255]\n" \
"ds_read_b128 a[48:51], v72 offset:45056\n" \
"v_mfma_f32_16x16x32_bf16 a[252:255], a[60:63], a[124:127], a[252:255]\n" \
"v_mfma_f32_16x16x32_bf16 a[248:251], a[56:59], a[112:115], a[248:251]\n" \
"v_mfma_f32_16x16x32_bf16 a[248:251], a[60:63], a[116:119], a[248:251]\n" \
"v_mfma_f32_16x16x32_bf16 a[244:247], a[56:59], a[104:107], a[244:247]\n" \
"ds_read_b128 a[52:55], v73 offset:45056\n" \
"v_mfma_f32_16x16x32_bf16 a[244:247], a[60:63], a[108:111], a[244:247]\n" \
"v_mfma_f32_16x16x32_bf16 a[240:243], a[56:59], a[96:99], a[240:243]\n" \
"v_mfma_f32_16x16x32_bf16 a[240:243], a[60:63], a[100:103], a[240:243]\n" \
"s_waitcnt lgkmcnt(1)\n" \
"v_mfma_f32_16x16x32_bf16 v[0:3], a[64:67], a[0:3], v[0:3]\n" \
"ds_read_b128 a[56:59], v72 offset:47104\n" \
"v_mfma_f32_16x16x32_bf16 v[0:3], a[68:71], a[4:7], v[0:3]\n" \
"v_mfma_f32_16x16x32_bf16 v[4:7], a[72:75], a[0:3], v[4:7]\n" \
"s_add_u32 m0, s74, 65536\n" \
"ds_read_b128 a[60:63], v73 offset:47104\n" \
"v_mfma_f32_16x16x32_bf16 v[4:7], a[76:79], a[4:7], v[4:7]\n" \
"buffer_load_dwordx4 v224, s[48:51], s75 offen offset:0 lds\n" \
"v_mfma_f32_16x16x32_bf16 v[8:11], a[80:83], a[0:3], v[8:11]\n" \
"ds_read_b64_tr_b16 a[96:97], v98 offset:33792\n" \
"v_mfma_f32_16x16x32_bf16 v[8:11], a[84:87], a[4:7], v[8:11]\n" \
"v_mfma_f32_16x16x32_bf16 v[12:15], a[88:91], a[0:3], v[12:15]\n" \
"ds_read_b64_tr_b16 a[98:99], v98 offset:37888\n" \
"v_mfma_f32_16x16x32_bf16 v[12:15], a[92:95], a[4:7], v[12:15]\n" \
"buffer_load_dwordx4 v225, s[48:51], s75 offen offset:1024 lds\n" \
"v_mfma_f32_16x16x32_bf16 v[28:31], a[88:91], a[8:11], v[28:31]\n" \
"ds_read_b64_tr_b16 a[100:101], v98 offset:50176\n" \
"v_mfma_f32_16x16x32_bf16 v[28:31], a[92:95], a[12:15], v[28:31]\n" \
"v_mfma_f32_16x16x32_bf16 v[24:27], a[80:83], a[8:11], v[24:27]\n" \
"ds_read_b64_tr_b16 a[102:103], v98 offset:54272\n" \
"v_mfma_f32_16x16x32_bf16 v[24:27], a[84:87], a[12:15], v[24:27]\n" \
"buffer_load_dwordx4 v226, s[48:51], s75 offen offset:2048 lds\n" \
"v_mfma_f32_16x16x32_bf16 v[20:23], a[72:75], a[8:11], v[20:23]\n" \
"ds_read_b64_tr_b16 a[104:105], v99 offset:33792\n" \
"v_mfma_f32_16x16x32_bf16 v[20:23], a[76:79], a[12:15], v[20:23]\n" \
"v_mfma_f32_16x16x32_bf16 v[16:19], a[64:67], a[8:11], v[16:19]\n" \
"ds_read_b64_tr_b16 a[106:107], v99 offset:37888\n" \
"v_mfma_f32_16x16x32_bf16 v[16:19], a[68:71], a[12:15], v[16:19]\n" \
"buffer_load_dwordx4 v227, s[48:51], s75 offen offset:3072 lds\n" \
"v_mfma_f32_16x16x32_bf16 v[32:35], a[64:67], a[16:19], v[32:35]\n" \
"ds_read_b64_tr_b16 a[108:109], v99 offset:50176\n" \
"v_mfma_f32_16x16x32_bf16 v[32:35], a[68:71], a[20:23], v[32:35]\n" \
"s_xor_b32 s75, s75, 0x400\n" \
"v_mfma_f32_16x16x32_bf16 v[36:39], a[72:75], a[16:19], v[36:39]\n" \
"s_add_u32 m0, s74, 69632\n" \
"ds_read_b64_tr_b16 a[110:111], v99 offset:54272\n" \
"v_mfma_f32_16x16x32_bf16 v[36:39], a[76:79], a[20:23], v[36:39]\n" \
"buffer_load_dwordx4 v224, s[48:51], s77 offen offset:0 lds\n" \
"v_mfma_f32_16x16x32_bf16 v[40:43], a[80:83], a[16:19], v[40:43]\n" \
"ds_read_b64_tr_b16 a[112:113], v98 offset:34304\n" \
"v_mfma_f32_16x16x32_bf16 v[40:43], a[84:87], a[20:23], v[40:43]\n" \
"v_mfma_f32_16x16x32_bf16 v[44:47], a[88:91], a[16:19], v[44:47]\n" \
"ds_read_b64_tr_b16 a[114:115], v98 offset:38400\n" \
"v_mfma_f32_16x16x32_bf16 v[44:47], a[92:95], a[20:23], v[44:47]\n" \
"buffer_load_dwordx4 v225, s[48:51], s77 offen offset:1024 lds\n" \
"v_mfma_f32_16x16x32_bf16 v[60:63], a[88:91], a[24:27], v[60:63]\n" \
"ds_read_b64_tr_b16 a[116:117], v98 offset:50688\n" \
"v_mfma_f32_16x16x32_bf16 v[60:63], a[92:95], a[28:31], v[60:63]\n" \
"v_mfma_f32_16x16x32_bf16 v[56:59], a[80:83], a[24:27], v[56:59]\n" \
"ds_read_b64_tr_b16 a[118:119], v98 offset:54784\n" \
"v_mfma_f32_16x16x32_bf16 v[56:59], a[84:87], a[28:31], v[56:59]\n" \
"buffer_load_dwordx4 v226, s[48:51], s77 offen offset:2048 lds\n" \
"v_mfma_f32_16x16x32_bf16 v[52:55], a[72:75], a[24:27], v[52:55]\n" \
"ds_read_b64_tr_b16 a[120:121], v99 offset:34304\n" \
"v_mfma_f32_16x16x32_bf16 v[52:55], a[76:79], a[28:31], v[52:55]\n" \
"v_mfma_f32_16x16x32_bf16 v[48:51], a[64:67], a[24:27], v[48:51]\n" \
"ds_read_b64_tr_b16 a[122:123], v99 offset:38400\n" \
"v_mfma_f32_16x16x32_bf16 v[48:51], a[68:71], a[28:31], v[48:51]\n" \
"buffer_load_dwordx4 v227, s[48:51], s77 offen offset:3072 lds\n" \
"v_mfma_f32_16x16x32_bf16 v[144:147], a[64:67], a[32:35], v[144:147]\n" \
"ds_read_b64_tr_b16 a[124:125], v99 offset:50688\n" \
"v_mfma_f32_16x16x32_bf16 v[144:147], a[68:71], a[36:39], v[144:147]\n" \
"v_mfma_f32_16x16x32_bf16 v[148:151], a[72:75], a[32:35], v[148:151]\n" \
"s_xor_b32 s77, s77, 0x400\n" \
"ds_read_b64_tr_b16 a[126:127], v99 offset:54784\n" \
"v_mfma_f32_16x16x32_bf16 v[148:151], a[76:79], a[36:39], v[148:151]\n" \
"v_mfma_f32_16x16x32_bf16 v[152:155], a[80:83], a[32:35], v[152:155]\n" \
"v_mfma_f32_16x16x32_bf16 v[152:155], a[84:87], a[36:39], v[152:155]\n" \
"v_mfma_f32_16x16x32_bf16 v[156:159], a[88:91], a[32:35], v[156:159]\n" \
"v_mfma_f32_16x16x32_bf16 v[156:159], a[92:95], a[36:39], v[156:159]\n" \
"v_mfma_f32_16x16x32_bf16 v[188:191], a[88:91], a[40:43], v[188:191]\n" \
"v_mfma_f32_16x16x32_bf16 v[188:191], a[92:95], a[44:47], v[188:191]\n" \
"v_mfma_f32_16x16x32_bf16 v[184:187], a[80:83], a[40:43], v[184:187]\n" \
"v_mfma_f32_16x16x32_bf16 v[184:187], a[84:87], a[44:47], v[184:187]\n" \
"v_mfma_f32_16x16x32_bf16 v[180:183], a[72:75], a[40:43], v[180:183]\n" \
"v_mfma_f32_16x16x32_bf16 v[180:183], a[76:79], a[44:47], v[180:183]\n" \
"v_mfma_f32_16x16x32_bf16 v[176:179], a[64:67], a[40:43], v[176:179]\n" \
"v_mfma_f32_16x16x32_bf16 v[176:179], a[68:71], a[44:47], v[176:179]\n" \
"s_waitcnt lgkmcnt(0)\n" \
"v_mfma_f32_16x16x32_bf16 v[192:195], a[64:67], a[48:51], v[192:195]\n" \
"v_mfma_f32_16x16x32_bf16 v[192:195], a[68:71], a[52:55], v[192:195]\n" \
"v_mfma_f32_16x16x32_bf16 v[196:199], a[72:75], a[48:51], v[196:199]\n" \
"v_mfma_f32_16x16x32_bf16 v[196:199], a[76:79], a[52:55], v[196:199]\n" \
"v_mfma_f32_16x16x32_bf16 v[200:203], a[80:83], a[48:51], v[200:203]\n" \
"v_mfma_f32_16x16x32_bf16 v[200:203], a[84:87], a[52:55], v[200:203]\n" \
"v_mfma_f32_16x16x32_bf16 v[204:207], a[88:91], a[48:51], v[204:207]\n" \
"v_mfma_f32_16x16x32_bf16 v[204:207], a[92:95], a[52:55], v[204:207]\n" \
"v_mfma_f32_16x16x32_bf16 v[220:223], a[88:91], a[56:59], v[220:223]\n" \
"v_mfma_f32_16x16x32_bf16 v[220:223], a[92:95], a[60:63], v[220:223]\n" \
"v_mfma_f32_16x16x32_bf16 v[216:219], a[80:83], a[56:59], v[216:219]\n" \
"v_mfma_f32_16x16x32_bf16 v[216:219], a[84:87], a[60:63], v[216:219]\n" \
"v_mfma_f32_16x16x32_bf16 v[212:215], a[72:75], a[56:59], v[212:215]\n" \
"v_mfma_f32_16x16x32_bf16 v[212:215], a[76:79], a[60:63], v[212:215]\n" \
"v_mfma_f32_16x16x32_bf16 v[208:211], a[64:67], a[56:59], v[208:211]\n" \
"v_mfma_f32_16x16x32_bf16 v[208:211], a[68:71], a[60:63], v[208:211]\n" \
"s_waitcnt vmcnt(10) lgkmcnt(0)\n" \
"s_barrier\n" \
"v_mfma_f32_16x16x32_bf16 a[128:131], a[0:3], a[96:99], a[128:131]\n" \
"s_add_u32 m0, s74, 98304\n" \
"v_mfma_f32_16x16x32_bf16 a[128:131], a[4:7], a[100:103], a[128:131]\n" \
"buffer_load_dwordx4 v224, s[48:51], s75 offen offset:0 lds\n" \
"v_mfma_f32_16x16x32_bf16 a[132:135], a[0:3], a[104:107], a[132:135]\n" \
"ds_read_b64_tr_b16 a[64:65], v232 offset:0\n" \
"ds_read_b64_tr_b16 a[66:67], v232 offset:2048\n" \
"v_mfma_f32_16x16x32_bf16 a[132:135], a[4:7], a[108:111], a[132:135]\n" \
"v_mfma_f32_16x16x32_bf16 a[136:139], a[0:3], a[112:115], a[136:139]\n" \
"v_mfma_f32_16x16x32_bf16 a[136:139], a[4:7], a[116:119], a[136:139]\n" \
"buffer_load_dwordx4 v225, s[48:51], s75 offen offset:1024 lds\n" \
"v_mfma_f32_16x16x32_bf16 a[140:143], a[0:3], a[120:123], a[140:143]\n" \
"ds_read_b64_tr_b16 a[68:69], v232 offset:8192\n" \
"ds_read_b64_tr_b16 a[70:71], v232 offset:10240\n" \
"v_mfma_f32_16x16x32_bf16 a[140:143], a[4:7], a[124:127], a[140:143]\n" \
"v_mfma_f32_16x16x32_bf16 a[156:159], a[8:11], a[120:123], a[156:159]\n" \
"v_mul_f32 v192, s76, v192\n" \
"v_mul_f32 v193, s76, v193\n" \
"v_mfma_f32_16x16x32_bf16 a[156:159], a[12:15], a[124:127], a[156:159]\n" \
"buffer_load_dwordx4 v226, s[48:51], s75 offen offset:2048 lds\n" \
"v_mfma_f32_16x16x32_bf16 a[152:155], a[8:11], a[112:115], a[152:155]\n" \
"ds_read_b64_tr_b16 a[72:73], v233 offset:0\n" \
"ds_read_b64_tr_b16 a[74:75], v233 offset:2048\n" \
"v_mfma_f32_16x16x32_bf16 a[152:155], a[12:15], a[116:119], a[152:155]\n" \
"v_mul_f32 v194, s76, v194\n" \
"v_mul_f32 v195, s76, v195\n" \
"v_mfma_f32_16x16x32_bf16 a[148:151], a[8:11], a[104:107], a[148:151]\n" \
"v_cvt_pk_bf16_f32 v192, v192, v193\n" \
"v_cvt_pk_bf16_f32 v193, v194, v195\n" \
"v_mfma_f32_16x16x32_bf16 a[148:151], a[12:15], a[108:111], a[148:151]\n" \
"buffer_load_dwordx4 v227, s[48:51], s75 offen offset:3072 lds\n" \
"v_mfma_f32_16x16x32_bf16 a[144:147], a[8:11], a[96:99], a[144:147]\n" \
"ds_read_b64_tr_b16 a[76:77], v233 offset:8192\n" \
"ds_read_b64_tr_b16 a[78:79], v233 offset:10240\n" \
"v_mfma_f32_16x16x32_bf16 a[144:147], a[12:15], a[100:103], a[144:147]\n" \
"s_xor_b32 s75, s75, 0x500\n" \
"v_mul_f32 v196, s76, v196\n" \
"v_mul_f32 v197, s76, v197\n" \
"v_mfma_f32_16x16x32_bf16 a[160:163], a[16:19], a[96:99], a[160:163]\n" \
"s_add_u32 m0, s74, 102400\n" \
"v_mul_f32 v198, s76, v198\n" \
"v_mul_f32 v199, s76, v199\n" \
"v_mfma_f32_16x16x32_bf16 a[160:163], a[20:23], a[100:103], a[160:163]\n" \
"buffer_load_dwordx4 v224, s[48:51], s77 offen offset:0 lds\n" \
"v_mfma_f32_16x16x32_bf16 a[164:167], a[16:19], a[104:107], a[164:167]\n" \
"ds_read_b64_tr_b16 a[80:81], v232 offset:512\n" \
"ds_read_b64_tr_b16 a[82:83], v232 offset:2560\n" \
"v_mfma_f32_16x16x32_bf16 a[164:167], a[20:23], a[108:111], a[164:167]\n" \
"v_cvt_pk_bf16_f32 v194, v196, v197\n" \
"v_cvt_pk_bf16_f32 v195, v198, v199\n" \
"v_mfma_f32_16x16x32_bf16 a[168:171], a[16:19], a[112:115], a[168:171]\n" \
"v_mul_f32 v208, s76, v208\n" \
"v_mul_f32 v209, s76, v209\n" \
"v_mfma_f32_16x16x32_bf16 a[168:171], a[20:23], a[116:119], a[168:171]\n" \
"buffer_load_dwordx4 v225, s[48:51], s77 offen offset:1024 lds\n" \
"v_mfma_f32_16x16x32_bf16 a[172:175], a[16:19], a[120:123], a[172:175]\n" \
"ds_read_b64_tr_b16 a[84:85], v232 offset:8704\n" \
"ds_read_b64_tr_b16 a[86:87], v232 offset:10752\n" \
"v_mfma_f32_16x16x32_bf16 a[172:175], a[20:23], a[124:127], a[172:175]\n" \
"v_mul_f32 v210, s76, v210\n" \
"v_mul_f32 v211, s76, v211\n" \
"v_mfma_f32_16x16x32_bf16 a[188:191], a[24:27], a[120:123], a[188:191]\n" \
"v_cvt_pk_bf16_f32 v208, v208, v209\n" \
"v_cvt_pk_bf16_f32 v209, v210, v211\n" \
"v_mfma_f32_16x16x32_bf16 a[188:191], a[28:31], a[124:127], a[188:191]\n" \
"buffer_load_dwordx4 v226, s[48:51], s77 offen offset:2048 lds\n" \
"v_mfma_f32_16x16x32_bf16 a[184:187], a[24:27], a[112:115], a[184:187]\n" \
"ds_read_b64_tr_b16 a[88:89], v233 offset:512\n" \
"ds_read_b64_tr_b16 a[90:91], v233 offset:2560\n" \
"v_mfma_f32_16x16x32_bf16 a[184:187], a[28:31], a[116:119], a[184:187]\n" \
"v_mul_f32 v212, s76, v212\n" \
"v_mul_f32 v213, s76, v213\n" \
"v_mfma_f32_16x16x32_bf16 a[180:183], a[24:27], a[104:107], a[180:183]\n" \
"v_mul_f32 v214, s76, v214\n" \
"v_mul_f32 v215, s76, v215\n" \
"v_mfma_f32_16x16x32_bf16 a[180:183], a[28:31], a[108:111], a[180:183]\n" \
"buffer_load_dwordx4 v227, s[48:51], s77 offen offset:3072 lds\n" \
"v_mfma_f32_16x16x32_bf16 a[176:179], a[24:27], a[96:99], a[176:179]\n" \
"ds_read_b64_tr_b16 a[92:93], v233 offset:8704\n" \
"ds_read_b64_tr_b16 a[94:95], v233 offset:10752\n" \
"v_mfma_f32_16x16x32_bf16 a[176:179], a[28:31], a[100:103], a[176:179]\n" \
"s_xor_b32 s77, s77, 0x500\n" \
"v_cvt_pk_bf16_f32 v210, v212, v213\n" \
"v_cvt_pk_bf16_f32 v211, v214, v215\n" \
"v_mfma_f32_16x16x32_bf16 a[192:195], a[32:35], a[96:99], a[192:195]\n" \
"s_add_u32 m0, s79, 32768\n" \
"v_mul_f32 v200, s76, v200\n" \
"v_mul_f32 v201, s76, v201\n" \
"v_mfma_f32_16x16x32_bf16 a[192:195], a[36:39], a[100:103], a[192:195]\n" \
"buffer_load_dwordx4 v228, s[52:55], s78 offen offset:0 lds\n" \
"v_mfma_f32_16x16x32_bf16 a[196:199], a[32:35], a[104:107], a[196:199]\n" \
"ds_read_b128 a[0:3], v250 offset:0\n" \
"ds_read_b128 a[4:7], v251 offset:0\n" \
"v_mfma_f32_16x16x32_bf16 a[196:199], a[36:39], a[108:111], a[196:199]\n" \
"v_mul_f32 v202, s76, v202\n" \
"v_mul_f32 v203, s76, v203\n" \
"v_mfma_f32_16x16x32_bf16 a[200:203], a[32:35], a[112:115], a[200:203]\n" \
"v_cvt_pk_bf16_f32 v200, v200, v201\n" \
"v_cvt_pk_bf16_f32 v201, v202, v203\n" \
"v_mfma_f32_16x16x32_bf16 a[200:203], a[36:39], a[116:119], a[200:203]\n" \
"buffer_load_dwordx4 v229, s[52:55], s78 offen offset:1024 lds\n" \
"v_mfma_f32_16x16x32_bf16 a[204:207], a[32:35], a[120:123], a[204:207]\n" \
"ds_read_b128 a[8:11], v250 offset:2048\n" \
"ds_read_b128 a[12:15], v251 offset:2048\n" \
"v_mfma_f32_16x16x32_bf16 a[204:207], a[36:39], a[124:127], a[204:207]\n" \
"v_mul_f32 v204, s76, v204\n" \
"v_mul_f32 v205, s76, v205\n" \
"v_mfma_f32_16x16x32_bf16 a[220:223], a[40:43], a[120:123], a[220:223]\n" \
"s_xor_b32 s78, s78, 0x500000\n" \
"v_mul_f32 v206, s76, v206\n" \
"v_mul_f32 v207, s76, v207\n" \
"v_mfma_f32_16x16x32_bf16 a[220:223], a[44:47], a[124:127], a[220:223]\n" \
"buffer_load_dwordx4 v228, s[52:55], s89 offen offset:2048 lds\n" \
"v_mfma_f32_16x16x32_bf16 a[216:219], a[40:43], a[112:115], a[216:219]\n" \
"ds_read_b128 a[16:19], v250 offset:4096\n" \
"ds_read_b128 a[20:23], v251 offset:4096\n" \
"v_mfma_f32_16x16x32_bf16 a[216:219], a[44:47], a[116:119], a[216:219]\n" \
"v_cvt_pk_bf16_f32 v202, v204, v205\n" \
"v_cvt_pk_bf16_f32 v203, v206, v207\n" \
"v_mfma_f32_16x16x32_bf16 a[212:215], a[40:43], a[104:107], a[212:215]\n" \
"v_mul_f32 v216, s76, v216\n" \
"v_mul_f32 v217, s76, v217\n" \
"v_mfma_f32_16x16x32_bf16 a[212:215], a[44:47], a[108:111], a[212:215]\n" \
"buffer_load_dwordx4 v229, s[52:55], s89 offen offset:3072 lds\n" \
"v_mfma_f32_16x16x32_bf16 a[208:211], a[40:43], a[96:99], a[208:211]\n" \
"v_mul_f32 v218, s76, v218\n" \
"v_mul_f32 v219, s76, v219\n" \
"v_mfma_f32_16x16x32_bf16 a[208:211], a[44:47], a[100:103], a[208:211]\n" \
"s_xor_b32 s89, s89, 0x500000\n" \
"v_cvt_pk_bf16_f32 v216, v216, v217\n" \
"v_cvt_pk_bf16_f32 v217, v218, v219\n" \
"v_mfma_f32_16x16x32_bf16 a[224:227], a[48:51], a[96:99], a[224:227]\n" \
"s_add_u32 m0, s79, 49152\n" \
"v_mul_f32 v220, s76, v220\n" \
"v_mul_f32 v221, s76, v221\n" \
"v_mfma_f32_16x16x32_bf16 a[224:227], a[52:55], a[100:103], a[224:227]\n" \
"buffer_load_dwordx4 v228, s[52:55], s78 offen offset:0 lds\n" \
"v_mfma_f32_16x16x32_bf16 a[228:231], a[48:51], a[104:107], a[228:231]\n" \
"v_mul_f32 v222, s76, v222\n" \
"v_mul_f32 v223, s76, v223\n" \
"v_mfma_f32_16x16x32_bf16 a[228:231], a[52:55], a[108:111], a[228:231]\n" \
"v_cvt_pk_bf16_f32 v218, v220, v221\n" \
"v_cvt_pk_bf16_f32 v219, v222, v223\n" \
"v_mfma_f32_16x16x32_bf16 a[232:235], a[48:51], a[112:115], a[232:235]\n" \
"v_mul_f32 v144, s76, v144\n" \
"v_mul_f32 v145, s76, v145\n" \
"v_mfma_f32_16x16x32_bf16 a[232:235], a[52:55], a[116:119], a[232:235]\n" \
"buffer_load_dwordx4 v229, s[52:55], s78 offen offset:1024 lds\n" \
"v_mfma_f32_16x16x32_bf16 a[236:239], a[48:51], a[120:123], a[236:239]\n" \
"v_mul_f32 v146, s76, v146\n" \
"v_mul_f32 v147, s76, v147\n" \
"v_mfma_f32_16x16x32_bf16 a[236:239], a[52:55], a[124:127], a[236:239]\n" \
"v_cvt_pk_bf16_f32 v196, v144, v145\n" \
"v_cvt_pk_bf16_f32 v197, v146, v147\n" \
"v_mfma_f32_16x16x32_bf16 a[252:255], a[56:59], a[120:123], a[252:255]\n" \
"s_xor_b32 s78, s78, 0x180000\n" \
"v_mul_f32 v148, s76, v148\n" \
"v_mul_f32 v149, s76, v149\n" \
"v_mfma_f32_16x16x32_bf16 a[252:255], a[60:63], a[124:127], a[252:255]\n" \
"buffer_load_dwordx4 v228, s[52:55], s89 offen offset:2048 lds\n" \
"v_mfma_f32_16x16x32_bf16 a[248:251], a[56:59], a[112:115], a[248:251]\n" \
"v_mul_f32 v150, s76, v150\n" \
"v_mul_f32 v151, s76, v151\n" \
"v_mfma_f32_16x16x32_bf16 a[248:251], a[60:63], a[116:119], a[248:251]\n" \
"v_cvt_pk_bf16_f32 v198, v148, v149\n" \
"v_cvt_pk_bf16_f32 v199, v150, v151\n" \
"v_mfma_f32_16x16x32_bf16 a[244:247], a[56:59], a[104:107], a[244:247]\n" \
"v_mul_f32 v176, s76, v176\n" \
"v_mul_f32 v177, s76, v177\n" \
"v_mfma_f32_16x16x32_bf16 a[244:247], a[60:63], a[108:111], a[244:247]\n" \
"buffer_load_dwordx4 v229, s[52:55], s89 offen offset:3072 lds\n" \
"v_mfma_f32_16x16x32_bf16 a[240:243], a[56:59], a[96:99], a[240:243]\n" \
"s_waitcnt lgkmcnt(0)\n" \
"v_mul_f32 v178, s76, v178\n" \
"v_mul_f32 v179, s76, v179\n" \
"v_mfma_f32_16x16x32_bf16 a[240:243], a[60:63], a[100:103], a[240:243]\n" \
"s_xor_b32 s89, s89, 0x180000\n" \
"v_cvt_pk_bf16_f32 v212, v176, v177\n" \
"v_cvt_pk_bf16_f32 v213, v178, v179\n" \
"v_mfma_f32_16x16x32_bf16 v[64:67], a[64:67], a[0:3], 0\n" \
"ds_read_b128 a[24:27], v250 offset:6144\n" \
"v_mul_f32 v180, s76, v180\n" \
"v_mfma_f32_16x16x32_bf16 v[64:67], a[68:71], a[4:7], v[64:67]\n" \
"s_add_u32 m0, s74, 131072\n" \
"ds_read_b128 a[28:31], v251 offset:6144\n" \
"v_mul_f32 v181, s76, v181\n" \
"v_mfma_f32_16x16x32_bf16 v[68:71], a[72:75], a[0:3], 0\n" \
"ds_read_b64_tr_b16 a[96:97], v232 offset:1024\n" \
"v_mul_f32 v182, s76, v182\n" \
"v_mfma_f32_16x16x32_bf16 v[68:71], a[76:79], a[4:7], v[68:71]\n" \
"buffer_load_dwordx4 v224, s[48:51], s75 offen offset:0 lds\n" \
"v_mfma_f32_16x16x32_bf16 v[72:75], a[80:83], a[0:3], 0\n" \
"ds_read_b64_tr_b16 a[98:99], v232 offset:3072\n" \
"v_mul_f32 v183, s76, v183\n" \
"v_mfma_f32_16x16x32_bf16 v[72:75], a[84:87], a[4:7], v[72:75]\n" \
"ds_read_b64_tr_b16 a[100:101], v232 offset:9216\n" \
"v_cvt_pk_bf16_f32 v214, v180, v181\n" \
"v_mfma_f32_16x16x32_bf16 v[76:79], a[88:91], a[0:3], 0\n" \
"ds_read_b64_tr_b16 a[102:103], v232 offset:11264\n" \
"v_cvt_pk_bf16_f32 v215, v182, v183\n" \
"v_mfma_f32_16x16x32_bf16 v[76:79], a[92:95], a[4:7], v[76:79]\n" \
"buffer_load_dwordx4 v225, s[48:51], s75 offen offset:1024 lds\n" \
"v_mfma_f32_16x16x32_bf16 v[108:111], a[88:91], a[8:11], 0\n" \
"ds_read_b64_tr_b16 a[104:105], v233 offset:1024\n" \
"v_mul_f32 v152, s76, v152\n" \
"v_mfma_f32_16x16x32_bf16 v[108:111], a[92:95], a[12:15], v[108:111]\n" \
"ds_read_b64_tr_b16 a[106:107], v233 offset:3072\n" \
"v_mul_f32 v153, s76, v153\n" \
"v_mfma_f32_16x16x32_bf16 v[104:107], a[80:83], a[8:11], 0\n" \
"ds_read_b64_tr_b16 a[108:109], v233 offset:9216\n" \
"v_mul_f32 v154, s76, v154\n" \
"v_mfma_f32_16x16x32_bf16 v[104:107], a[84:87], a[12:15], v[104:107]\n" \
"buffer_load_dwordx4 v226, s[48:51], s75 offen offset:2048 lds\n" \
"v_mfma_f32_16x16x32_bf16 v[100:103], a[72:75], a[8:11], 0\n" \
"ds_read_b64_tr_b16 a[110:111], v233 offset:11264\n" \
"v_mul_f32 v155, s76, v155\n" \
"v_mfma_f32_16x16x32_bf16 v[100:103], a[76:79], a[12:15], v[100:103]\n" \
"ds_read_b64_tr_b16 a[112:113], v232 offset:1536\n" \
"v_cvt_pk_bf16_f32 v204, v152, v153\n" \
"v_mfma_f32_16x16x32_bf16 v[96:99], a[64:67], a[8:11], 0\n" \
"ds_read_b64_tr_b16 a[114:115], v232 offset:3584\n" \
"v_cvt_pk_bf16_f32 v205, v154, v155\n" \
"v_mfma_f32_16x16x32_bf16 v[96:99], a[68:71], a[12:15], v[96:99]\n" \
"buffer_load_dwordx4 v227, s[48:51], s75 offen offset:3072 lds\n" \
"v_mfma_f32_16x16x32_bf16 v[128:131], a[64:67], a[16:19], 0\n" \
"s_xor_b32 s75, s75, 0x180\n" \
"ds_read_b64_tr_b16 a[116:117], v232 offset:9728\n" \
"v_mul_f32 v156, s76, v156\n" \
"v_mfma_f32_16x16x32_bf16 v[128:131], a[68:71], a[20:23], v[128:131]\n" \
"s_add_u32 m0, s74, 135168\n" \
"ds_read_b64_tr_b16 a[118:119], v232 offset:11776\n" \
"v_mul_f32 v157, s76, v157\n" \
"v_mfma_f32_16x16x32_bf16 v[132:135], a[72:75], a[16:19], 0\n" \
"ds_read_b64_tr_b16 a[120:121], v233 offset:1536\n" \
"v_mul_f32 v158, s76, v158\n" \
"Lpg2209_%=:\n" \
"Lst0x3_%=:\n" \
"s_cbranch_execz Lpg4447_%=\n" \
"v_mfma_f32_16x16x32_bf16 v[132:135], a[76:79], a[20:23], v[132:135]\n" \
"buffer_load_dwordx4 v224, s[48:51], s77 offen offset:0 lds\n" \
"v_mfma_f32_16x16x32_bf16 v[136:139], a[80:83], a[16:19], 0\n" \
"ds_read_b64_tr_b16 a[122:123], v233 offset:3584\n" \
"v_mul_f32 v159, s76, v159\n" \
"v_mfma_f32_16x16x32_bf16 v[136:139], a[84:87], a[20:23], v[136:139]\n" \
"ds_read_b64_tr_b16 a[124:125], v233 offset:9728\n" \
"v_cvt_pk_bf16_f32 v206, v156, v157\n" \
"v_mfma_f32_16x16x32_bf16 v[140:143], a[88:91], a[16:19], 0\n" \
"s_waitcnt lgkmcnt(3)\n" \
"ds_read_b64_tr_b16 a[126:127], v233 offset:11776\n" \
"v_cvt_pk_bf16_f32 v207, v158, v159\n" \
"v_mfma_f32_16x16x32_bf16 v[140:143], a[92:95], a[20:23], v[140:143]\n" \
"buffer_load_dwordx4 v225, s[48:51], s77 offen offset:1024 lds\n" \
"v_mfma_f32_16x16x32_bf16 v[172:175], a[88:91], a[24:27], 0\n" \
"s_add_u32 s93, s87, 8192\n" \
"v_mul_f32 v184, s76, v184\n" \
"v_mul_f32 v185, s76, v185\n" \
"v_mfma_f32_16x16x32_bf16 v[172:175], a[92:95], a[28:31], v[172:175]\n" \
"buffer_store_dwordx4 v[192:195], v236, s[56:59], s93 offen offset:0 sc1\n" \
"v_mfma_f32_16x16x32_bf16 v[168:171], a[80:83], a[24:27], 0\n" \
"v_mul_f32 v186, s76, v186\n" \
"v_mul_f32 v187, s76, v187\n" \
"v_mfma_f32_16x16x32_bf16 v[168:171], a[84:87], a[28:31], v[168:171]\n" \
"buffer_load_dwordx4 v226, s[48:51], s77 offen offset:2048 lds\n" \
"v_mfma_f32_16x16x32_bf16 v[164:167], a[72:75], a[24:27], 0\n" \
"v_cvt_pk_bf16_f32 v220, v184, v185\n" \
"v_cvt_pk_bf16_f32 v221, v186, v187\n" \
"v_mfma_f32_16x16x32_bf16 v[164:167], a[76:79], a[28:31], v[164:167]\n" \
"buffer_load_dwordx4 v227, s[48:51], s77 offen offset:3072 lds\n" \
"v_mfma_f32_16x16x32_bf16 v[160:163], a[64:67], a[24:27], 0\n" \
"v_mul_f32 v188, s76, v188\n" \
"v_mul_f32 v189, s76, v189\n" \
"v_mfma_f32_16x16x32_bf16 v[160:163], a[68:71], a[28:31], v[160:163]\n" \
"s_waitcnt vmcnt(43) lgkmcnt(0)\n" \
"s_barrier\n" \
"v_mfma_f32_16x16x32_bf16 v[80:83], a[96:99], a[0:3], 0\n" \
"s_xor_b32 s77, s77, 0x180\n" \
"ds_read_b64_tr_b16 a[64:65], v232 offset:16384\n" \
"v_mul_f32 v190, s76, v190\n" \
"v_mfma_f32_16x16x32_bf16 v[80:83], a[100:103], a[4:7], v[80:83]\n" \
"ds_read_b64_tr_b16 a[66:67], v232 offset:18432\n" \
"v_mul_f32 v191, s76, v191\n" \
"v_mfma_f32_16x16x32_bf16 v[84:87], a[104:107], a[0:3], 0\n" \
"s_add_u32 m0, s79, 0\n" \
"ds_read_b64_tr_b16 a[68:69], v232 offset:24576\n" \
"v_cvt_pk_bf16_f32 v222, v188, v189\n" \
"v_mfma_f32_16x16x32_bf16 v[84:87], a[108:111], a[4:7], v[84:87]\n" \
"buffer_load_dwordx4 v228, s[52:55], s78 offen offset:0 lds\n" \
"v_mfma_f32_16x16x32_bf16 v[88:91], a[112:115], a[0:3], 0\n" \
"ds_read_b64_tr_b16 a[70:71], v232 offset:26624\n" \
"v_cvt_pk_bf16_f32 v223, v190, v191\n" \
"v_mfma_f32_16x16x32_bf16 v[88:91], a[116:119], a[4:7], v[88:91]\n" \
"ds_read_b64_tr_b16 a[72:73], v233 offset:16384\n" \
"v_accvgpr_read_b32 v243, a224\n" \
"v_mfma_f32_16x16x32_bf16 v[92:95], a[120:123], a[0:3], 0\n" \
"ds_read_b64_tr_b16 a[74:75], v233 offset:18432\n" \
"v_accvgpr_read_b32 v252, a225\n" \
"v_mfma_f32_16x16x32_bf16 v[92:95], a[124:127], a[4:7], v[92:95]\n" \
"buffer_load_dwordx4 v229, s[52:55], s78 offen offset:1024 lds\n" \
"v_mfma_f32_16x16x32_bf16 v[124:127], a[120:123], a[8:11], 0\n" \
"s_waitcnt vmcnt(33)\n" \
"ds_read_b128 a[0:3], v230 offset:0\n" \
"v_accvgpr_read_b32 v253, a226\n" \
"v_mfma_f32_16x16x32_bf16 v[124:127], a[124:127], a[12:15], v[124:127]\n" \
"ds_read_b128 a[4:7], v231 offset:0\n" \
"v_accvgpr_read_b32 v254, a227\n" \
"v_mfma_f32_16x16x32_bf16 v[120:123], a[112:115], a[8:11], 0\n" \
"s_xor_b32 s78, s78, 0x300000\n" \
"ds_read_b64_tr_b16 a[76:77], v233 offset:24576\n" \
"v_mul_f32 v243, s31, v243\n" \
"v_mfma_f32_16x16x32_bf16 v[120:123], a[116:119], a[12:15], v[120:123]\n" \
"buffer_load_dwordx4 v228, s[52:55], s89 offen offset:2048 lds\n" \
"v_mfma_f32_16x16x32_bf16 v[116:119], a[104:107], a[8:11], 0\n" \
"ds_read_b64_tr_b16 a[78:79], v233 offset:26624\n" \
"v_mul_f32 v252, s31, v252\n" \
"v_mfma_f32_16x16x32_bf16 v[116:119], a[108:111], a[12:15], v[116:119]\n" \
"ds_read_b64_tr_b16 a[80:81], v232 offset:16896\n" \
"v_mul_f32 v253, s31, v253\n" \
"v_mfma_f32_16x16x32_bf16 v[112:115], a[96:99], a[8:11], 0\n" \
"ds_read_b64_tr_b16 a[82:83], v232 offset:18944\n" \
"v_mul_f32 v254, s31, v254\n" \
"v_mfma_f32_16x16x32_bf16 v[112:115], a[100:103], a[12:15], v[112:115]\n" \
"buffer_load_dwordx4 v229, s[52:55], s89 offen offset:3072 lds\n" \
"v_mfma_f32_16x16x32_bf16 v[144:147], a[96:99], a[16:19], 0\n" \
"s_waitcnt vmcnt(33)\n" \
"s_xor_b32 s89, s89, 0x300000\n" \
"ds_read_b128 a[8:11], v230 offset:2048\n" \
"v_mfma_f32_16x16x32_bf16 v[144:147], a[100:103], a[20:23], v[144:147]\n" \
"s_add_u32 m0, s74, 65536\n" \
"ds_read_b128 a[12:15], v231 offset:2048\n" \
"v_cvt_pk_bf16_f32 v243, v243, v252\n" \
"v_mfma_f32_16x16x32_bf16 v[148:151], a[104:107], a[16:19], 0\n" \
"s_waitcnt lgkmcnt(6)\n" \
"ds_read_b64_tr_b16 a[84:85], v232 offset:25088\n" \
"v_cvt_pk_bf16_f32 v253, v253, v254\n" \
"v_mfma_f32_16x16x32_bf16 v[148:151], a[108:111], a[20:23], v[148:151]\n" \
"buffer_load_dwordx4 v224, s[48:51], s75 offen offset:0 lds\n" \
"v_mfma_f32_16x16x32_bf16 v[152:155], a[112:115], a[16:19], 0\n" \
"ds_read_b64_tr_b16 a[86:87], v232 offset:27136\n" \
"v_accvgpr_write_b32 a224, v243\n" \
"v_mfma_f32_16x16x32_bf16 v[152:155], a[116:119], a[20:23], v[152:155]\n" \
"ds_read_b64_tr_b16 a[88:89], v233 offset:16896\n" \
"v_accvgpr_write_b32 a225, v253\n" \
"v_mfma_f32_16x16x32_bf16 v[156:159], a[120:123], a[16:19], 0\n" \
"ds_read_b64_tr_b16 a[90:91], v233 offset:18944\n" \
"v_accvgpr_read_b32 v243, a240\n" \
"v_mfma_f32_16x16x32_bf16 v[156:159], a[124:127], a[20:23], v[156:159]\n" \
"buffer_load_dwordx4 v225, s[48:51], s75 offen offset:1024 lds\n" \
"v_mfma_f32_16x16x32_bf16 v[188:191], a[120:123], a[24:27], 0\n" \
"s_waitcnt vmcnt(33)\n" \
"ds_read_b128 a[16:19], v230 offset:4096\n" \
"v_accvgpr_read_b32 v252, a241\n" \
"v_mfma_f32_16x16x32_bf16 v[188:191], a[124:127], a[28:31], v[188:191]\n" \
"ds_read_b128 a[20:23], v231 offset:4096\n" \
"v_accvgpr_read_b32 v253, a242\n" \
"v_mfma_f32_16x16x32_bf16 v[184:187], a[112:115], a[24:27], 0\n" \
"s_waitcnt lgkmcnt(6)\n" \
"ds_read_b64_tr_b16 a[92:93], v233 offset:25088\n" \
"v_accvgpr_read_b32 v254, a243\n" \
"v_mfma_f32_16x16x32_bf16 v[184:187], a[116:119], a[28:31], v[184:187]\n" \
"buffer_load_dwordx4 v226, s[48:51], s75 offen offset:2048 lds\n" \
"v_mfma_f32_16x16x32_bf16 v[180:183], a[104:107], a[24:27], 0\n" \
"ds_read_b64_tr_b16 a[94:95], v233 offset:27136\n" \
"v_mul_f32 v243, s31, v243\n" \
"v_mfma_f32_16x16x32_bf16 v[180:183], a[108:111], a[28:31], v[180:183]\n" \
"v_mul_f32 v252, s31, v252\n" \
"v_mul_f32 v253, s31, v253\n" \
"v_mfma_f32_16x16x32_bf16 v[176:179], a[96:99], a[24:27], 0\n" \
"v_mul_f32 v254, s31, v254\n" \
"v_cvt_pk_bf16_f32 v243, v243, v252\n" \
"v_mfma_f32_16x16x32_bf16 v[176:179], a[100:103], a[28:31], v[176:179]\n" \
"buffer_load_dwordx4 v227, s[48:51], s75 offen offset:3072 lds\n" \
"v_mfma_f32_16x16x32_bf16 v[64:67], a[64:67], a[0:3], v[64:67]\n" \
"s_waitcnt vmcnt(33)\n" \
"s_xor_b32 s75, s75, 0x300\n" \
"ds_read_b128 a[24:27], v230 offset:6144\n" \
"v_mfma_f32_16x16x32_bf16 v[64:67], a[68:71], a[4:7], v[64:67]\n" \
"s_add_u32 m0, s74, 69632\n" \
"ds_read_b128 a[28:31], v231 offset:6144\n" \
"v_cvt_pk_bf16_f32 v253, v253, v254\n" \
"v_mfma_f32_16x16x32_bf16 v[68:71], a[72:75], a[0:3], v[68:71]\n" \
"s_waitcnt lgkmcnt(4)\n" \
"ds_read_b64_tr_b16 a[96:97], v232 offset:17408\n" \
"v_accvgpr_write_b32 a226, v243\n" \
"v_mfma_f32_16x16x32_bf16 v[68:71], a[76:79], a[4:7], v[68:71]\n" \
"buffer_load_dwordx4 v224, s[48:51], s77 offen offset:0 lds\n" \
"v_mfma_f32_16x16x32_bf16 v[72:75], a[80:83], a[0:3], v[72:75]\n" \
"ds_read_b64_tr_b16 a[98:99], v232 offset:19456\n" \
"v_accvgpr_write_b32 a227, v253\n" \
"v_mfma_f32_16x16x32_bf16 v[72:75], a[84:87], a[4:7], v[72:75]\n" \
"s_waitcnt lgkmcnt(3)\n" \
"ds_read_b64_tr_b16 a[100:101], v232 offset:25600\n" \
"v_accvgpr_read_b32 v243, a228\n" \
"v_mfma_f32_16x16x32_bf16 v[76:79], a[88:91], a[0:3], v[76:79]\n" \
"ds_read_b64_tr_b16 a[102:103], v232 offset:27648\n" \
"v_accvgpr_read_b32 v252, a229\n" \
"v_mfma_f32_16x16x32_bf16 v[76:79], a[92:95], a[4:7], v[76:79]\n" \
"buffer_load_dwordx4 v225, s[48:51], s77 offen offset:1024 lds\n" \
"v_mfma_f32_16x16x32_bf16 v[108:111], a[88:91], a[8:11], v[108:111]\n" \
"ds_read_b64_tr_b16 a[104:105], v233 offset:17408\n" \
"v_accvgpr_read_b32 v253, a230\n" \
"v_mfma_f32_16x16x32_bf16 v[108:111], a[92:95], a[12:15], v[108:111]\n" \
"ds_read_b64_tr_b16 a[106:107], v233 offset:19456\n" \
"v_accvgpr_read_b32 v254, a231\n" \
"v_mfma_f32_16x16x32_bf16 v[104:107], a[80:83], a[8:11], v[104:107]\n" \
"s_waitcnt lgkmcnt(6)\n" \
"ds_read_b64_tr_b16 a[108:109], v233 offset:25600\n" \
"v_mul_f32 v243, s31, v243\n" \
"v_mfma_f32_16x16x32_bf16 v[104:107], a[84:87], a[12:15], v[104:107]\n" \
"buffer_load_dwordx4 v226, s[48:51], s77 offen offset:2048 lds\n" \
"v_mfma_f32_16x16x32_bf16 v[100:103], a[72:75], a[8:11], v[100:103]\n" \
"ds_read_b64_tr_b16 a[110:111], v233 offset:27648\n" \
"v_mul_f32 v252, s31, v252\n" \
"v_mfma_f32_16x16x32_bf16 v[100:103], a[76:79], a[12:15], v[100:103]\n" \
"ds_read_b64_tr_b16 a[112:113], v232 offset:17920\n" \
"v_mul_f32 v253, s31, v253\n" \
"v_mfma_f32_16x16x32_bf16 v[96:99], a[64:67], a[8:11], v[96:99]\n" \
"ds_read_b64_tr_b16 a[114:115], v232 offset:19968\n" \
"v_mul_f32 v254, s31, v254\n" \
"v_mfma_f32_16x16x32_bf16 v[96:99], a[68:71], a[12:15], v[96:99]\n" \
"buffer_load_dwordx4 v227, s[48:51], s77 offen offset:3072 lds\n" \
"v_mfma_f32_16x16x32_bf16 v[128:131], a[64:67], a[16:19], v[128:131]\n" \
"ds_read_b64_tr_b16 a[116:117], v232 offset:26112\n" \
"v_cvt_pk_bf16_f32 v243, v243, v252\n" \
"v_mfma_f32_16x16x32_bf16 v[128:131], a[68:71], a[20:23], v[128:131]\n" \
"ds_read_b64_tr_b16 a[118:119], v232 offset:28160\n" \
"v_cvt_pk_bf16_f32 v253, v253, v254\n" \
"v_mfma_f32_16x16x32_bf16 v[132:135], a[72:75], a[16:19], v[132:135]\n" \
"s_xor_b32 s77, s77, 0x300\n" \
"ds_read_b64_tr_b16 a[120:121], v233 offset:17920\n" \
"v_accvgpr_write_b32 a228, v243\n" \
"v_mfma_f32_16x16x32_bf16 v[132:135], a[76:79], a[20:23], v[132:135]\n" \
"v_accvgpr_write_b32 a229, v253\n" \
"v_accvgpr_read_b32 v243, a244\n" \
"v_mfma_f32_16x16x32_bf16 v[136:139], a[80:83], a[16:19], v[136:139]\n" \
"ds_read_b64_tr_b16 a[122:123], v233 offset:19968\n" \
"v_accvgpr_read_b32 v252, a245\n" \
"v_mfma_f32_16x16x32_bf16 v[136:139], a[84:87], a[20:23], v[136:139]\n" \
"ds_read_b64_tr_b16 a[124:125], v233 offset:26112\n" \
"v_accvgpr_read_b32 v253, a246\n" \
"v_mfma_f32_16x16x32_bf16 v[140:143], a[88:91], a[16:19], v[140:143]\n" \
"ds_read_b64_tr_b16 a[126:127], v233 offset:28160\n" \
"v_accvgpr_read_b32 v254, a247\n" \
"v_mfma_f32_16x16x32_bf16 v[140:143], a[92:95], a[20:23], v[140:143]\n" \
"buffer_store_dwordx4 v[208:211], v236, s[56:59], s93 offen offset:256 sc1\n" \
"v_mfma_f32_16x16x32_bf16 v[172:175], a[88:91], a[24:27], v[172:175]\n" \
"v_mul_f32 v243, s31, v243\n" \
"v_mul_f32 v252, s31, v252\n" \
"v_mfma_f32_16x16x32_bf16 v[172:175], a[92:95], a[28:31], v[172:175]\n" \
"v_mul_f32 v253, s31, v253\n" \
"v_mul_f32 v254, s31, v254\n" \
"v_mfma_f32_16x16x32_bf16 v[168:171], a[80:83], a[24:27], v[168:171]\n" \
"v_cvt_pk_bf16_f32 v243, v243, v252\n" \
"v_cvt_pk_bf16_f32 v253, v253, v254\n" \
"v_mfma_f32_16x16x32_bf16 v[168:171], a[84:87], a[28:31], v[168:171]\n" \
"v_accvgpr_write_b32 a230, v243\n" \
"v_accvgpr_write_b32 a231, v253\n" \
"v_mfma_f32_16x16x32_bf16 v[164:167], a[72:75], a[24:27], v[164:167]\n" \
"v_accvgpr_read_b32 v243, a232\n" \
"v_accvgpr_read_b32 v252, a233\n" \
"v_mfma_f32_16x16x32_bf16 v[164:167], a[76:79], a[28:31], v[164:167]\n" \
"v_accvgpr_read_b32 v253, a234\n" \
"v_accvgpr_read_b32 v254, a235\n" \
"v_mfma_f32_16x16x32_bf16 v[160:163], a[64:67], a[24:27], v[160:163]\n" \
"v_mul_f32 v243, s31, v243\n" \
"v_mul_f32 v252, s31, v252\n" \
"v_mfma_f32_16x16x32_bf16 v[160:163], a[68:71], a[28:31], v[160:163]\n" \
"s_waitcnt vmcnt(26) lgkmcnt(0)\n" \
"s_barrier\n" \
"v_mfma_f32_16x16x32_bf16 v[80:83], a[96:99], a[0:3], v[80:83]\n" \
"ds_read_b64_tr_b16 a[64:65], v232 offset:32768\n" \
"v_mul_f32 v253, s31, v253\n" \
"v_mfma_f32_16x16x32_bf16 v[80:83], a[100:103], a[4:7], v[80:83]\n" \
"ds_read_b64_tr_b16 a[66:67], v232 offset:34816\n" \
"v_mul_f32 v254, s31, v254\n" \
"v_mfma_f32_16x16x32_bf16 v[84:87], a[104:107], a[0:3], v[84:87]\n" \
"s_add_u32 m0, s79, 16384\n" \
"ds_read_b64_tr_b16 a[68:69], v232 offset:40960\n" \
"v_cvt_pk_bf16_f32 v243, v243, v252\n" \
"v_mfma_f32_16x16x32_bf16 v[84:87], a[108:111], a[4:7], v[84:87]\n" \
"buffer_load_dwordx4 v228, s[52:55], s78 offen offset:0 lds\n" \
"v_mfma_f32_16x16x32_bf16 v[88:91], a[112:115], a[0:3], v[88:91]\n" \
"ds_read_b64_tr_b16 a[70:71], v232 offset:43008\n" \
"v_cvt_pk_bf16_f32 v253, v253, v254\n" \
"v_mfma_f32_16x16x32_bf16 v[88:91], a[116:119], a[4:7], v[88:91]\n" \
"ds_read_b64_tr_b16 a[72:73], v233 offset:32768\n" \
"v_accvgpr_write_b32 a232, v243\n" \
"v_mfma_f32_16x16x32_bf16 v[92:95], a[120:123], a[0:3], v[92:95]\n" \
"ds_read_b64_tr_b16 a[74:75], v233 offset:34816\n" \
"v_accvgpr_write_b32 a233, v253\n" \
"v_mfma_f32_16x16x32_bf16 v[92:95], a[124:127], a[4:7], v[92:95]\n" \
"buffer_load_dwordx4 v229, s[52:55], s78 offen offset:1024 lds\n" \
"v_mfma_f32_16x16x32_bf16 v[124:127], a[120:123], a[8:11], v[124:127]\n" \
"ds_read_b128 a[0:3], v230 offset:32768\n" \
"v_accvgpr_read_b32 v243, a248\n" \
"v_mfma_f32_16x16x32_bf16 v[124:127], a[124:127], a[12:15], v[124:127]\n" \
"ds_read_b128 a[4:7], v231 offset:32768\n" \
"v_accvgpr_read_b32 v252, a249\n" \
"v_mfma_f32_16x16x32_bf16 v[120:123], a[112:115], a[8:11], v[120:123]\n" \
"s_xor_b32 s78, s78, 0x80000\n" \
"ds_read_b64_tr_b16 a[76:77], v233 offset:40960\n" \
"v_accvgpr_read_b32 v253, a250\n" \
"v_mfma_f32_16x16x32_bf16 v[120:123], a[116:119], a[12:15], v[120:123]\n" \
"buffer_load_dwordx4 v228, s[52:55], s89 offen offset:2048 lds\n" \
"v_mfma_f32_16x16x32_bf16 v[116:119], a[104:107], a[8:11], v[116:119]\n" \
"ds_read_b64_tr_b16 a[78:79], v233 offset:43008\n" \
"v_accvgpr_read_b32 v254, a251\n" \
"v_mfma_f32_16x16x32_bf16 v[116:119], a[108:111], a[12:15], v[116:119]\n" \
"ds_read_b64_tr_b16 a[80:81], v232 offset:33280\n" \
"v_mul_f32 v243, s31, v243\n" \
"v_mfma_f32_16x16x32_bf16 v[112:115], a[96:99], a[8:11], v[112:115]\n" \
"ds_read_b64_tr_b16 a[82:83], v232 offset:35328\n" \
"v_mul_f32 v252, s31, v252\n" \
"v_mfma_f32_16x16x32_bf16 v[112:115], a[100:103], a[12:15], v[112:115]\n" \
"buffer_load_dwordx4 v229, s[52:55], s89 offen offset:3072 lds\n" \
"v_mfma_f32_16x16x32_bf16 v[144:147], a[96:99], a[16:19], v[144:147]\n" \
"s_xor_b32 s89, s89, 0x80000\n" \
"ds_read_b128 a[8:11], v230 offset:34816\n" \
"v_mfma_f32_16x16x32_bf16 v[144:147], a[100:103], a[20:23], v[144:147]\n" \
"s_add_u32 m0, s74, 98304\n" \
"ds_read_b128 a[12:15], v231 offset:34816\n" \
"v_mul_f32 v253, s31, v253\n" \
"v_mfma_f32_16x16x32_bf16 v[148:151], a[104:107], a[16:19], v[148:151]\n" \
"s_waitcnt lgkmcnt(6)\n" \
"ds_read_b64_tr_b16 a[84:85], v232 offset:41472\n" \
"v_mul_f32 v254, s31, v254\n" \
"v_mfma_f32_16x16x32_bf16 v[148:151], a[108:111], a[20:23], v[148:151]\n" \
"buffer_load_dwordx4 v224, s[48:51], s75 offen offset:0 lds\n" \
"v_mfma_f32_16x16x32_bf16 v[152:155], a[112:115], a[16:19], v[152:155]\n" \
"ds_read_b64_tr_b16 a[86:87], v232 offset:43520\n" \
"v_cvt_pk_bf16_f32 v243, v243, v252\n" \
"v_mfma_f32_16x16x32_bf16 v[152:155], a[116:119], a[20:23], v[152:155]\n" \
"ds_read_b64_tr_b16 a[88:89], v233 offset:33280\n" \
"v_cvt_pk_bf16_f32 v253, v253, v254\n" \
"v_mfma_f32_16x16x32_bf16 v[156:159], a[120:123], a[16:19], v[156:159]\n" \
"ds_read_b64_tr_b16 a[90:91], v233 offset:35328\n" \
"v_accvgpr_write_b32 a234, v243\n" \
"v_mfma_f32_16x16x32_bf16 v[156:159], a[124:127], a[20:23], v[156:159]\n" \
"buffer_load_dwordx4 v225, s[48:51], s75 offen offset:1024 lds\n" \
"v_mfma_f32_16x16x32_bf16 v[188:191], a[120:123], a[24:27], v[188:191]\n" \
"ds_read_b128 a[16:19], v230 offset:36864\n" \
"v_accvgpr_write_b32 a235, v253\n" \
"v_mfma_f32_16x16x32_bf16 v[188:191], a[124:127], a[28:31], v[188:191]\n" \
"ds_read_b128 a[20:23], v231 offset:36864\n" \
"v_accvgpr_read_b32 v243, a236\n" \
"v_mfma_f32_16x16x32_bf16 v[184:187], a[112:115], a[24:27], v[184:187]\n" \
"s_waitcnt lgkmcnt(6)\n" \
"ds_read_b64_tr_b16 a[92:93], v233 offset:41472\n" \
"v_accvgpr_read_b32 v252, a237\n" \
"v_mfma_f32_16x16x32_bf16 v[184:187], a[116:119], a[28:31], v[184:187]\n" \
"buffer_load_dwordx4 v226, s[48:51], s75 offen offset:2048 lds\n" \
"v_mfma_f32_16x16x32_bf16 v[180:183], a[104:107], a[24:27], v[180:183]\n" \
"ds_read_b64_tr_b16 a[94:95], v233 offset:43520\n" \
"v_accvgpr_read_b32 v253, a238\n" \
"v_mfma_f32_16x16x32_bf16 v[180:183], a[108:111], a[28:31], v[180:183]\n" \
"v_accvgpr_read_b32 v254, a239\n" \
"v_mul_f32 v243, s31, v243\n" \
"v_mfma_f32_16x16x32_bf16 v[176:179], a[96:99], a[24:27], v[176:179]\n" \
"v_mul_f32 v252, s31, v252\n" \
"v_mul_f32 v253, s31, v253\n" \
"v_mfma_f32_16x16x32_bf16 v[176:179], a[100:103], a[28:31], v[176:179]\n" \
"buffer_load_dwordx4 v227, s[48:51], s75 offen offset:3072 lds\n" \
"v_mfma_f32_16x16x32_bf16 v[64:67], a[64:67], a[0:3], v[64:67]\n" \
"s_xor_b32 s75, s75, 0x80\n" \
"ds_read_b128 a[24:27], v230 offset:38912\n" \
"v_mfma_f32_16x16x32_bf16 v[64:67], a[68:71], a[4:7], v[64:67]\n" \
"s_add_u32 m0, s74, 102400\n" \
"ds_read_b128 a[28:31], v231 offset:38912\n" \
"v_mul_f32 v254, s31, v254\n" \
"v_mfma_f32_16x16x32_bf16 v[68:71], a[72:75], a[0:3], v[68:71]\n" \
"s_waitcnt lgkmcnt(4)\n" \
"ds_read_b64_tr_b16 a[96:97], v232 offset:33792\n" \
"v_cvt_pk_bf16_f32 v243, v243, v252\n" \
"v_mfma_f32_16x16x32_bf16 v[68:71], a[76:79], a[4:7], v[68:71]\n" \
"buffer_load_dwordx4 v224, s[48:51], s77 offen offset:0 lds\n" \
"v_mfma_f32_16x16x32_bf16 v[72:75], a[80:83], a[0:3], v[72:75]\n" \
"ds_read_b64_tr_b16 a[98:99], v232 offset:35840\n" \
"v_cvt_pk_bf16_f32 v253, v253, v254\n" \
"v_mfma_f32_16x16x32_bf16 v[72:75], a[84:87], a[4:7], v[72:75]\n" \
"s_waitcnt lgkmcnt(3)\n" \
"ds_read_b64_tr_b16 a[100:101], v232 offset:41984\n" \
"v_accvgpr_write_b32 a236, v243\n" \
"v_mfma_f32_16x16x32_bf16 v[76:79], a[88:91], a[0:3], v[76:79]\n" \
"ds_read_b64_tr_b16 a[102:103], v232 offset:44032\n" \
"v_accvgpr_write_b32 a237, v253\n" \
"v_mfma_f32_16x16x32_bf16 v[76:79], a[92:95], a[4:7], v[76:79]\n" \
"buffer_load_dwordx4 v225, s[48:51], s77 offen offset:1024 lds\n" \
"v_mfma_f32_16x16x32_bf16 v[108:111], a[88:91], a[8:11], v[108:111]\n" \
"ds_read_b64_tr_b16 a[104:105], v233 offset:33792\n" \
"v_accvgpr_read_b32 v243, a252\n" \
"v_mfma_f32_16x16x32_bf16 v[108:111], a[92:95], a[12:15], v[108:111]\n" \
"ds_read_b64_tr_b16 a[106:107], v233 offset:35840\n" \
"v_accvgpr_read_b32 v252, a253\n" \
"v_mfma_f32_16x16x32_bf16 v[104:107], a[80:83], a[8:11], v[104:107]\n" \
"s_waitcnt lgkmcnt(6)\n" \
"ds_read_b64_tr_b16 a[108:109], v233 offset:41984\n" \
"v_accvgpr_read_b32 v253, a254\n" \
"v_mfma_f32_16x16x32_bf16 v[104:107], a[84:87], a[12:15], v[104:107]\n" \
"buffer_load_dwordx4 v226, s[48:51], s77 offen offset:2048 lds\n" \
"v_mfma_f32_16x16x32_bf16 v[100:103], a[72:75], a[8:11], v[100:103]\n" \
"ds_read_b64_tr_b16 a[110:111], v233 offset:44032\n" \
"v_accvgpr_read_b32 v254, a255\n" \
"v_mfma_f32_16x16x32_bf16 v[100:103], a[76:79], a[12:15], v[100:103]\n" \
"ds_read_b64_tr_b16 a[112:113], v232 offset:34304\n" \
"v_mul_f32 v243, s31, v243\n" \
"v_mfma_f32_16x16x32_bf16 v[96:99], a[64:67], a[8:11], v[96:99]\n" \
"ds_read_b64_tr_b16 a[114:115], v232 offset:36352\n" \
"v_mul_f32 v252, s31, v252\n" \
"v_mfma_f32_16x16x32_bf16 v[96:99], a[68:71], a[12:15], v[96:99]\n" \
"buffer_load_dwordx4 v227, s[48:51], s77 offen offset:3072 lds\n" \
"v_mfma_f32_16x16x32_bf16 v[128:131], a[64:67], a[16:19], v[128:131]\n" \
"ds_read_b64_tr_b16 a[116:117], v232 offset:42496\n" \
"v_mul_f32 v253, s31, v253\n" \
"v_mfma_f32_16x16x32_bf16 v[128:131], a[68:71], a[20:23], v[128:131]\n" \
"ds_read_b64_tr_b16 a[118:119], v232 offset:44544\n" \
"v_mul_f32 v254, s31, v254\n" \
"v_mfma_f32_16x16x32_bf16 v[132:135], a[72:75], a[16:19], v[132:135]\n" \
"s_xor_b32 s77, s77, 0x80\n" \
"ds_read_b64_tr_b16 a[120:121], v233 offset:34304\n" \
"v_cvt_pk_bf16_f32 v243, v243, v252\n" \
"v_mfma_f32_16x16x32_bf16 v[132:135], a[76:79], a[20:23], v[132:135]\n" \
"v_cvt_pk_bf16_f32 v253, v253, v254\n" \
"v_accvgpr_write_b32 a238, v243\n" \
"v_mfma_f32_16x16x32_bf16 v[136:139], a[80:83], a[16:19], v[136:139]\n" \
"ds_read_b64_tr_b16 a[122:123], v233 offset:36352\n" \
"v_accvgpr_write_b32 a239, v253\n" \
"v_mfma_f32_16x16x32_bf16 v[136:139], a[84:87], a[20:23], v[136:139]\n" \
"ds_read_b64_tr_b16 a[124:125], v233 offset:42496\n" \
"v_accvgpr_read_b32 v243, a192\n" \
"v_mfma_f32_16x16x32_bf16 v[140:143], a[88:91], a[16:19], v[140:143]\n" \
"ds_read_b64_tr_b16 a[126:127], v233 offset:44544\n" \
"v_accvgpr_read_b32 v252, a193\n" \
"v_mfma_f32_16x16x32_bf16 v[140:143], a[92:95], a[20:23], v[140:143]\n" \
"buffer_store_dwordx4 v[200:203], v236, s[56:59], s93 offen offset:2048 sc1\n" \
"v_mfma_f32_16x16x32_bf16 v[172:175], a[88:91], a[24:27], v[172:175]\n" \
"v_accvgpr_read_b32 v253, a194\n" \
"v_accvgpr_read_b32 v254, a195\n" \
"v_mfma_f32_16x16x32_bf16 v[172:175], a[92:95], a[28:31], v[172:175]\n" \
"v_mul_f32 v243, s31, v243\n" \
"v_mul_f32 v252, s31, v252\n" \
"v_mfma_f32_16x16x32_bf16 v[168:171], a[80:83], a[24:27], v[168:171]\n" \
"v_mul_f32 v253, s31, v253\n" \
"v_mul_f32 v254, s31, v254\n" \
"v_mfma_f32_16x16x32_bf16 v[168:171], a[84:87], a[28:31], v[168:171]\n" \
"v_cvt_pk_bf16_f32 v243, v243, v252\n" \
"v_cvt_pk_bf16_f32 v253, v253, v254\n" \
"v_mfma_f32_16x16x32_bf16 v[164:167], a[72:75], a[24:27], v[164:167]\n" \
"v_accvgpr_write_b32 a192, v243\n" \
"v_accvgpr_write_b32 a193, v253\n" \
"v_mfma_f32_16x16x32_bf16 v[164:167], a[76:79], a[28:31], v[164:167]\n" \
"v_accvgpr_read_b32 v243, a208\n" \
"v_accvgpr_read_b32 v252, a209\n" \
"v_mfma_f32_16x16x32_bf16 v[160:163], a[64:67], a[24:27], v[160:163]\n" \
"v_accvgpr_read_b32 v253, a210\n" \
"v_accvgpr_read_b32 v254, a211\n" \
"v_mfma_f32_16x16x32_bf16 v[160:163], a[68:71], a[28:31], v[160:163]\n" \
"s_waitcnt vmcnt(35) lgkmcnt(0)\n" \
"s_barrier\n" \
"v_mfma_f32_16x16x32_bf16 v[80:83], a[96:99], a[0:3], v[80:83]\n" \
"ds_read_b64_tr_b16 a[64:65], v232 offset:49152\n" \
"v_mul_f32 v243, s31, v243\n" \
"v_mfma_f32_16x16x32_bf16 v[80:83], a[100:103], a[4:7], v[80:83]\n" \
"ds_read_b64_tr_b16 a[66:67], v232 offset:51200\n" \
"v_mul_f32 v252, s31, v252\n" \
"v_mfma_f32_16x16x32_bf16 v[84:87], a[104:107], a[0:3], v[84:87]\n" \
"s_add_u32 m0, s79, 32768\n" \
"ds_read_b64_tr_b16 a[68:69], v232 offset:57344\n" \
"v_mul_f32 v253, s31, v253\n" \
"v_mfma_f32_16x16x32_bf16 v[84:87], a[108:111], a[4:7], v[84:87]\n" \
"buffer_load_dwordx4 v228, s[52:55], s78 offen offset:0 lds\n" \
"v_mfma_f32_16x16x32_bf16 v[88:91], a[112:115], a[0:3], v[88:91]\n" \
"ds_read_b64_tr_b16 a[70:71], v232 offset:59392\n" \
"v_mul_f32 v254, s31, v254\n" \
"v_mfma_f32_16x16x32_bf16 v[88:91], a[116:119], a[4:7], v[88:91]\n" \
"ds_read_b64_tr_b16 a[72:73], v233 offset:49152\n" \
"v_cvt_pk_bf16_f32 v243, v243, v252\n" \
"v_mfma_f32_16x16x32_bf16 v[92:95], a[120:123], a[0:3], v[92:95]\n" \
"ds_read_b64_tr_b16 a[74:75], v233 offset:51200\n" \
"v_cvt_pk_bf16_f32 v253, v253, v254\n" \
"v_mfma_f32_16x16x32_bf16 v[92:95], a[124:127], a[4:7], v[92:95]\n" \
"buffer_load_dwordx4 v229, s[52:55], s78 offen offset:1024 lds\n" \
"v_mfma_f32_16x16x32_bf16 v[124:127], a[120:123], a[8:11], v[124:127]\n" \
"s_waitcnt vmcnt(35)\n" \
"ds_read_b128 a[0:3], v250 offset:0\n" \
"v_accvgpr_write_b32 a194, v243\n" \
"v_mfma_f32_16x16x32_bf16 v[124:127], a[124:127], a[12:15], v[124:127]\n" \
"ds_read_b128 a[4:7], v251 offset:0\n" \
"v_accvgpr_write_b32 a195, v253\n" \
"v_mfma_f32_16x16x32_bf16 v[120:123], a[112:115], a[8:11], v[120:123]\n" \
"s_xor_b32 s78, s78, 0x180000\n" \
"ds_read_b64_tr_b16 a[76:77], v233 offset:57344\n" \
"v_accvgpr_read_b32 v243, a196\n" \
"v_mfma_f32_16x16x32_bf16 v[120:123], a[116:119], a[12:15], v[120:123]\n" \
"buffer_load_dwordx4 v228, s[52:55], s89 offen offset:2048 lds\n" \
"v_mfma_f32_16x16x32_bf16 v[116:119], a[104:107], a[8:11], v[116:119]\n" \
"ds_read_b64_tr_b16 a[78:79], v233 offset:59392\n" \
"v_accvgpr_read_b32 v252, a197\n" \
"v_mfma_f32_16x16x32_bf16 v[116:119], a[108:111], a[12:15], v[116:119]\n" \
"ds_read_b64_tr_b16 a[80:81], v232 offset:49664\n" \
"v_accvgpr_read_b32 v253, a198\n" \
"v_mfma_f32_16x16x32_bf16 v[112:115], a[96:99], a[8:11], v[112:115]\n" \
"ds_read_b64_tr_b16 a[82:83], v232 offset:51712\n" \
"v_accvgpr_read_b32 v254, a199\n" \
"v_mfma_f32_16x16x32_bf16 v[112:115], a[100:103], a[12:15], v[112:115]\n" \
"buffer_load_dwordx4 v229, s[52:55], s89 offen offset:3072 lds\n" \
"v_mfma_f32_16x16x32_bf16 v[144:147], a[96:99], a[16:19], v[144:147]\n" \
"s_waitcnt vmcnt(35)\n" \
"s_xor_b32 s89, s89, 0x180000\n" \
"ds_read_b128 a[8:11], v250 offset:2048\n" \
"v_mfma_f32_16x16x32_bf16 v[144:147], a[100:103], a[20:23], v[144:147]\n" \
"s_add_u32 m0, s74, 131072\n" \
"ds_read_b128 a[12:15], v251 offset:2048\n" \
"v_mul_f32 v243, s31, v243\n" \
"v_mfma_f32_16x16x32_bf16 v[148:151], a[104:107], a[16:19], v[148:151]\n" \
"s_waitcnt lgkmcnt(6)\n" \
"ds_read_b64_tr_b16 a[84:85], v232 offset:57856\n" \
"v_mul_f32 v252, s31, v252\n" \
"v_mfma_f32_16x16x32_bf16 v[148:151], a[108:111], a[20:23], v[148:151]\n" \
"buffer_load_dwordx4 v224, s[48:51], s75 offen offset:0 lds\n" \
"v_mfma_f32_16x16x32_bf16 v[152:155], a[112:115], a[16:19], v[152:155]\n" \
"ds_read_b64_tr_b16 a[86:87], v232 offset:59904\n" \
"v_mul_f32 v253, s31, v253\n" \
"v_mfma_f32_16x16x32_bf16 v[152:155], a[116:119], a[20:23], v[152:155]\n" \
"ds_read_b64_tr_b16 a[88:89], v233 offset:49664\n" \
"v_mul_f32 v254, s31, v254\n" \
"v_mfma_f32_16x16x32_bf16 v[156:159], a[120:123], a[16:19], v[156:159]\n" \
"ds_read_b64_tr_b16 a[90:91], v233 offset:51712\n" \
"v_cvt_pk_bf16_f32 v243, v243, v252\n" \
"v_mfma_f32_16x16x32_bf16 v[156:159], a[124:127], a[20:23], v[156:159]\n" \
"buffer_load_dwordx4 v225, s[48:51], s75 offen offset:1024 lds\n" \
"v_mfma_f32_16x16x32_bf16 v[188:191], a[120:123], a[24:27], v[188:191]\n" \
"s_waitcnt vmcnt(35)\n" \
"ds_read_b128 a[16:19], v250 offset:4096\n" \
"v_cvt_pk_bf16_f32 v253, v253, v254\n" \
"v_mfma_f32_16x16x32_bf16 v[188:191], a[124:127], a[28:31], v[188:191]\n" \
"ds_read_b128 a[20:23], v251 offset:4096\n" \
"v_accvgpr_write_b32 a196, v243\n" \
"v_mfma_f32_16x16x32_bf16 v[184:187], a[112:115], a[24:27], v[184:187]\n" \
"s_waitcnt lgkmcnt(6)\n" \
"ds_read_b64_tr_b16 a[92:93], v233 offset:57856\n" \
"v_accvgpr_write_b32 a197, v253\n" \
"v_mfma_f32_16x16x32_bf16 v[184:187], a[116:119], a[28:31], v[184:187]\n" \
"buffer_load_dwordx4 v226, s[48:51], s75 offen offset:2048 lds\n" \
"v_mfma_f32_16x16x32_bf16 v[180:183], a[104:107], a[24:27], v[180:183]\n" \
"ds_read_b64_tr_b16 a[94:95], v233 offset:59904\n" \
"v_accvgpr_read_b32 v243, a212\n" \
"v_mfma_f32_16x16x32_bf16 v[180:183], a[108:111], a[28:31], v[180:183]\n" \
"v_accvgpr_read_b32 v252, a213\n" \
"v_accvgpr_read_b32 v253, a214\n" \
"v_mfma_f32_16x16x32_bf16 v[176:179], a[96:99], a[24:27], v[176:179]\n" \
"v_accvgpr_read_b32 v254, a215\n" \
"v_mul_f32 v243, s31, v243\n" \
"v_mfma_f32_16x16x32_bf16 v[176:179], a[100:103], a[28:31], v[176:179]\n" \
"buffer_load_dwordx4 v227, s[48:51], s75 offen offset:3072 lds\n" \
"v_mfma_f32_16x16x32_bf16 v[64:67], a[64:67], a[0:3], v[64:67]\n" \
"s_waitcnt vmcnt(34)\n" \
"s_xor_b32 s75, s75, 0x180\n" \
"ds_read_b128 a[24:27], v250 offset:6144\n" \
"v_mfma_f32_16x16x32_bf16 v[64:67], a[68:71], a[4:7], v[64:67]\n" \
"s_add_u32 m0, s74, 135168\n" \
"ds_read_b128 a[28:31], v251 offset:6144\n" \
"v_mul_f32 v252, s31, v252\n" \
"v_mfma_f32_16x16x32_bf16 v[68:71], a[72:75], a[0:3], v[68:71]\n" \
"s_waitcnt lgkmcnt(4)\n" \
"ds_read_b64_tr_b16 a[96:97], v232 offset:50176\n" \
"v_mul_f32 v253, s31, v253\n" \
"v_mfma_f32_16x16x32_bf16 v[68:71], a[76:79], a[4:7], v[68:71]\n" \
"buffer_load_dwordx4 v224, s[48:51], s77 offen offset:0 lds\n" \
"v_mfma_f32_16x16x32_bf16 v[72:75], a[80:83], a[0:3], v[72:75]\n" \
"ds_read_b64_tr_b16 a[98:99], v232 offset:52224\n" \
"v_mul_f32 v254, s31, v254\n" \
"v_mfma_f32_16x16x32_bf16 v[72:75], a[84:87], a[4:7], v[72:75]\n" \
"s_waitcnt lgkmcnt(3)\n" \
"ds_read_b64_tr_b16 a[100:101], v232 offset:58368\n" \
"v_cvt_pk_bf16_f32 v243, v243, v252\n" \
"v_mfma_f32_16x16x32_bf16 v[76:79], a[88:91], a[0:3], v[76:79]\n" \
"ds_read_b64_tr_b16 a[102:103], v232 offset:60416\n" \
"v_cvt_pk_bf16_f32 v253, v253, v254\n" \
"v_mfma_f32_16x16x32_bf16 v[76:79], a[92:95], a[4:7], v[76:79]\n" \
"buffer_load_dwordx4 v225, s[48:51], s77 offen offset:1024 lds\n" \
"v_mfma_f32_16x16x32_bf16 v[108:111], a[88:91], a[8:11], v[108:111]\n" \
"ds_read_b64_tr_b16 a[104:105], v233 offset:50176\n" \
"v_accvgpr_write_b32 a198, v243\n" \
"v_mfma_f32_16x16x32_bf16 v[108:111], a[92:95], a[12:15], v[108:111]\n" \
"ds_read_b64_tr_b16 a[106:107], v233 offset:52224\n" \
"v_accvgpr_write_b32 a199, v253\n" \
"v_mfma_f32_16x16x32_bf16 v[104:107], a[80:83], a[8:11], v[104:107]\n" \
"s_waitcnt lgkmcnt(6)\n" \
"ds_read_b64_tr_b16 a[108:109], v233 offset:58368\n" \
"v_accvgpr_read_b32 v243, a200\n" \
"v_mfma_f32_16x16x32_bf16 v[104:107], a[84:87], a[12:15], v[104:107]\n" \
"buffer_load_dwordx4 v226, s[48:51], s77 offen offset:2048 lds\n" \
"v_mfma_f32_16x16x32_bf16 v[100:103], a[72:75], a[8:11], v[100:103]\n" \
"ds_read_b64_tr_b16 a[110:111], v233 offset:60416\n" \
"v_accvgpr_read_b32 v252, a201\n" \
"Lpg2766_%=:\n" \
"s_cbranch_execz Lpg5012_%=\n" \
"v_mfma_f32_16x16x32_bf16 v[100:103], a[76:79], a[12:15], v[100:103]\n" \
"ds_read_b64_tr_b16 a[112:113], v232 offset:50688\n" \
"v_accvgpr_read_b32 v253, a202\n" \
"v_mfma_f32_16x16x32_bf16 v[96:99], a[64:67], a[8:11], v[96:99]\n" \
"ds_read_b64_tr_b16 a[114:115], v232 offset:52736\n" \
"v_accvgpr_read_b32 v254, a203\n" \
"v_mfma_f32_16x16x32_bf16 v[96:99], a[68:71], a[12:15], v[96:99]\n" \
"buffer_load_dwordx4 v227, s[48:51], s77 offen offset:3072 lds\n" \
"v_mfma_f32_16x16x32_bf16 v[128:131], a[64:67], a[16:19], v[128:131]\n" \
"ds_read_b64_tr_b16 a[116:117], v232 offset:58880\n" \
"v_mul_f32 v243, s31, v243\n" \
"v_mfma_f32_16x16x32_bf16 v[128:131], a[68:71], a[20:23], v[128:131]\n" \
"ds_read_b64_tr_b16 a[118:119], v232 offset:60928\n" \
"v_mul_f32 v252, s31, v252\n" \
"v_mfma_f32_16x16x32_bf16 v[132:135], a[72:75], a[16:19], v[132:135]\n" \
"s_xor_b32 s77, s77, 0x180\n" \
"ds_read_b64_tr_b16 a[120:121], v233 offset:50688\n" \
"v_mul_f32 v253, s31, v253\n" \
"v_mfma_f32_16x16x32_bf16 v[132:135], a[76:79], a[20:23], v[132:135]\n" \
"v_mul_f32 v254, s31, v254\n" \
"v_cvt_pk_bf16_f32 v243, v243, v252\n" \
"v_mfma_f32_16x16x32_bf16 v[136:139], a[80:83], a[16:19], v[136:139]\n" \
"ds_read_b64_tr_b16 a[122:123], v233 offset:52736\n" \
"v_cvt_pk_bf16_f32 v253, v253, v254\n" \
"v_mfma_f32_16x16x32_bf16 v[136:139], a[84:87], a[20:23], v[136:139]\n" \
"ds_read_b64_tr_b16 a[124:125], v233 offset:58880\n" \
"v_accvgpr_write_b32 a200, v243\n" \
"v_mfma_f32_16x16x32_bf16 v[140:143], a[88:91], a[16:19], v[140:143]\n" \
"ds_read_b64_tr_b16 a[126:127], v233 offset:60928\n" \
"v_accvgpr_write_b32 a201, v253\n" \
"v_mfma_f32_16x16x32_bf16 v[140:143], a[92:95], a[20:23], v[140:143]\n" \
"buffer_store_dwordx4 v[216:219], v236, s[56:59], s93 offen offset:2304 sc1\n" \
"v_mfma_f32_16x16x32_bf16 v[172:175], a[88:91], a[24:27], v[172:175]\n" \
"v_accvgpr_read_b32 v243, a216\n" \
"v_accvgpr_read_b32 v252, a217\n" \
"v_mfma_f32_16x16x32_bf16 v[172:175], a[92:95], a[28:31], v[172:175]\n" \
"v_accvgpr_read_b32 v253, a218\n" \
"v_accvgpr_read_b32 v254, a219\n" \
"v_mfma_f32_16x16x32_bf16 v[168:171], a[80:83], a[24:27], v[168:171]\n" \
"v_mul_f32 v243, s31, v243\n" \
"v_mul_f32 v252, s31, v252\n" \
"v_mfma_f32_16x16x32_bf16 v[168:171], a[84:87], a[28:31], v[168:171]\n" \
"v_mul_f32 v253, s31, v253\n" \
"v_mul_f32 v254, s31, v254\n" \
"v_mfma_f32_16x16x32_bf16 v[164:167], a[72:75], a[24:27], v[164:167]\n" \
"v_cvt_pk_bf16_f32 v243, v243, v252\n" \
"v_cvt_pk_bf16_f32 v253, v253, v254\n" \
"v_mfma_f32_16x16x32_bf16 v[164:167], a[76:79], a[28:31], v[164:167]\n" \
"v_accvgpr_write_b32 a202, v243\n" \
"v_accvgpr_write_b32 a203, v253\n" \
"v_mfma_f32_16x16x32_bf16 v[160:163], a[64:67], a[24:27], v[160:163]\n" \
"v_accvgpr_read_b32 v243, a204\n" \
"v_accvgpr_read_b32 v252, a205\n" \
"v_mfma_f32_16x16x32_bf16 v[160:163], a[68:71], a[28:31], v[160:163]\n" \
"s_waitcnt vmcnt(35) lgkmcnt(0)\n" \
"s_barrier\n" \
"v_mfma_f32_16x16x32_bf16 v[80:83], a[96:99], a[0:3], v[80:83]\n" \
"ds_read_b64_tr_b16 a[64:65], v232 offset:0\n" \
"v_accvgpr_read_b32 v253, a206\n" \
"v_mfma_f32_16x16x32_bf16 v[80:83], a[100:103], a[4:7], v[80:83]\n" \
"ds_read_b64_tr_b16 a[66:67], v232 offset:2048\n" \
"v_accvgpr_read_b32 v254, a207\n" \
"v_mfma_f32_16x16x32_bf16 v[84:87], a[104:107], a[0:3], v[84:87]\n" \
"s_add_u32 m0, s79, 49152\n" \
"ds_read_b64_tr_b16 a[68:69], v232 offset:8192\n" \
"v_mul_f32 v243, s31, v243\n" \
"v_mfma_f32_16x16x32_bf16 v[84:87], a[108:111], a[4:7], v[84:87]\n" \
"buffer_load_dwordx4 v228, s[52:55], s78 offen offset:0 lds\n" \
"v_mfma_f32_16x16x32_bf16 v[88:91], a[112:115], a[0:3], v[88:91]\n" \
"ds_read_b64_tr_b16 a[70:71], v232 offset:10240\n" \
"v_mul_f32 v252, s31, v252\n" \
"v_mfma_f32_16x16x32_bf16 v[88:91], a[116:119], a[4:7], v[88:91]\n" \
"ds_read_b64_tr_b16 a[72:73], v233 offset:0\n" \
"v_mul_f32 v253, s31, v253\n" \
"v_mfma_f32_16x16x32_bf16 v[92:95], a[120:123], a[0:3], v[92:95]\n" \
"ds_read_b64_tr_b16 a[74:75], v233 offset:2048\n" \
"v_mul_f32 v254, s31, v254\n" \
"v_mfma_f32_16x16x32_bf16 v[92:95], a[124:127], a[4:7], v[92:95]\n" \
"buffer_load_dwordx4 v229, s[52:55], s78 offen offset:1024 lds\n" \
"v_mfma_f32_16x16x32_bf16 v[124:127], a[120:123], a[8:11], v[124:127]\n" \
"s_waitcnt vmcnt(35)\n" \
"ds_read_b128 a[0:3], v230 offset:0\n" \
"v_cvt_pk_bf16_f32 v243, v243, v252\n" \
"v_mfma_f32_16x16x32_bf16 v[124:127], a[124:127], a[12:15], v[124:127]\n" \
"ds_read_b128 a[4:7], v231 offset:0\n" \
"v_cvt_pk_bf16_f32 v253, v253, v254\n" \
"v_mfma_f32_16x16x32_bf16 v[120:123], a[112:115], a[8:11], v[120:123]\n" \
"s_xor_b32 s78, s78, 0x80000\n" \
"ds_read_b64_tr_b16 a[76:77], v233 offset:8192\n" \
"v_accvgpr_write_b32 a204, v243\n" \
"v_mfma_f32_16x16x32_bf16 v[120:123], a[116:119], a[12:15], v[120:123]\n" \
"buffer_load_dwordx4 v228, s[52:55], s89 offen offset:2048 lds\n" \
"v_mfma_f32_16x16x32_bf16 v[116:119], a[104:107], a[8:11], v[116:119]\n" \
"ds_read_b64_tr_b16 a[78:79], v233 offset:10240\n" \
"v_accvgpr_write_b32 a205, v253\n" \
"v_mfma_f32_16x16x32_bf16 v[116:119], a[108:111], a[12:15], v[116:119]\n" \
"ds_read_b64_tr_b16 a[80:81], v232 offset:512\n" \
"v_accvgpr_read_b32 v243, a220\n" \
"v_mfma_f32_16x16x32_bf16 v[112:115], a[96:99], a[8:11], v[112:115]\n" \
"ds_read_b64_tr_b16 a[82:83], v232 offset:2560\n" \
"v_accvgpr_read_b32 v252, a221\n" \
"v_mfma_f32_16x16x32_bf16 v[112:115], a[100:103], a[12:15], v[112:115]\n" \
"buffer_load_dwordx4 v229, s[52:55], s89 offen offset:3072 lds\n" \
"v_mfma_f32_16x16x32_bf16 v[144:147], a[96:99], a[16:19], v[144:147]\n" \
"s_waitcnt vmcnt(35)\n" \
"s_xor_b32 s89, s89, 0x80000\n" \
"ds_read_b128 a[8:11], v230 offset:2048\n" \
"v_mfma_f32_16x16x32_bf16 v[144:147], a[100:103], a[20:23], v[144:147]\n" \
"s_add_u32 m0, s74, 65536\n" \
"ds_read_b128 a[12:15], v231 offset:2048\n" \
"v_accvgpr_read_b32 v253, a222\n" \
"v_mfma_f32_16x16x32_bf16 v[148:151], a[104:107], a[16:19], v[148:151]\n" \
"s_waitcnt lgkmcnt(6)\n" \
"ds_read_b64_tr_b16 a[84:85], v232 offset:8704\n" \
"v_accvgpr_read_b32 v254, a223\n" \
"v_mfma_f32_16x16x32_bf16 v[148:151], a[108:111], a[20:23], v[148:151]\n" \
"buffer_load_dwordx4 v224, s[48:51], s75 offen offset:0 lds\n" \
"v_mfma_f32_16x16x32_bf16 v[152:155], a[112:115], a[16:19], v[152:155]\n" \
"ds_read_b64_tr_b16 a[86:87], v232 offset:10752\n" \
"v_mul_f32 v243, s31, v243\n" \
"v_mfma_f32_16x16x32_bf16 v[152:155], a[116:119], a[20:23], v[152:155]\n" \
"ds_read_b64_tr_b16 a[88:89], v233 offset:512\n" \
"v_mul_f32 v252, s31, v252\n" \
"v_mfma_f32_16x16x32_bf16 v[156:159], a[120:123], a[16:19], v[156:159]\n" \
"ds_read_b64_tr_b16 a[90:91], v233 offset:2560\n" \
"v_mul_f32 v253, s31, v253\n" \
"v_mfma_f32_16x16x32_bf16 v[156:159], a[124:127], a[20:23], v[156:159]\n" \
"buffer_load_dwordx4 v225, s[48:51], s75 offen offset:1024 lds\n" \
"v_mfma_f32_16x16x32_bf16 v[188:191], a[120:123], a[24:27], v[188:191]\n" \
"s_waitcnt vmcnt(35)\n" \
"ds_read_b128 a[16:19], v230 offset:4096\n" \
"v_mul_f32 v254, s31, v254\n" \
"v_mfma_f32_16x16x32_bf16 v[188:191], a[124:127], a[28:31], v[188:191]\n" \
"ds_read_b128 a[20:23], v231 offset:4096\n" \
"v_cvt_pk_bf16_f32 v243, v243, v252\n" \
"v_mfma_f32_16x16x32_bf16 v[184:187], a[112:115], a[24:27], v[184:187]\n" \
"s_waitcnt lgkmcnt(6)\n" \
"ds_read_b64_tr_b16 a[92:93], v233 offset:8704\n" \
"v_cvt_pk_bf16_f32 v253, v253, v254\n" \
"v_mfma_f32_16x16x32_bf16 v[184:187], a[116:119], a[28:31], v[184:187]\n" \
"buffer_load_dwordx4 v226, s[48:51], s75 offen offset:2048 lds\n" \
"v_mfma_f32_16x16x32_bf16 v[180:183], a[104:107], a[24:27], v[180:183]\n" \
"ds_read_b64_tr_b16 a[94:95], v233 offset:10752\n" \
"v_accvgpr_write_b32 a206, v243\n" \
"v_mfma_f32_16x16x32_bf16 v[180:183], a[108:111], a[28:31], v[180:183]\n" \
"v_accvgpr_write_b32 a207, v253\n" \
"v_mul_f32 v32, s76, v32\n" \
"v_mfma_f32_16x16x32_bf16 v[176:179], a[96:99], a[24:27], v[176:179]\n" \
"v_mul_f32 v33, s76, v33\n" \
"v_mul_f32 v34, s76, v34\n" \
"v_mfma_f32_16x16x32_bf16 v[176:179], a[100:103], a[28:31], v[176:179]\n" \
"buffer_load_dwordx4 v227, s[48:51], s75 offen offset:3072 lds\n" \
"v_mfma_f32_16x16x32_bf16 v[64:67], a[64:67], a[0:3], v[64:67]\n" \
"s_waitcnt vmcnt(35)\n" \
"s_xor_b32 s75, s75, 0x80\n" \
"ds_read_b128 a[24:27], v230 offset:6144\n" \
"v_mfma_f32_16x16x32_bf16 v[64:67], a[68:71], a[4:7], v[64:67]\n" \
"s_add_u32 m0, s74, 69632\n" \
"ds_read_b128 a[28:31], v231 offset:6144\n" \
"v_mul_f32 v35, s76, v35\n" \
"v_mfma_f32_16x16x32_bf16 v[68:71], a[72:75], a[0:3], v[68:71]\n" \
"s_waitcnt lgkmcnt(4)\n" \
"ds_read_b64_tr_b16 a[96:97], v232 offset:1024\n" \
"v_cvt_pk_bf16_f32 v32, v32, v33\n" \
"v_mfma_f32_16x16x32_bf16 v[68:71], a[76:79], a[4:7], v[68:71]\n" \
"buffer_load_dwordx4 v224, s[48:51], s77 offen offset:0 lds\n" \
"v_mfma_f32_16x16x32_bf16 v[72:75], a[80:83], a[0:3], v[72:75]\n" \
"ds_read_b64_tr_b16 a[98:99], v232 offset:3072\n" \
"v_cvt_pk_bf16_f32 v33, v34, v35\n" \
"v_mfma_f32_16x16x32_bf16 v[72:75], a[84:87], a[4:7], v[72:75]\n" \
"s_waitcnt lgkmcnt(3)\n" \
"ds_read_b64_tr_b16 a[100:101], v232 offset:9216\n" \
"v_mul_f32 v36, s76, v36\n" \
"v_mfma_f32_16x16x32_bf16 v[76:79], a[88:91], a[0:3], v[76:79]\n" \
"ds_read_b64_tr_b16 a[102:103], v232 offset:11264\n" \
"v_mul_f32 v37, s76, v37\n" \
"v_mfma_f32_16x16x32_bf16 v[76:79], a[92:95], a[4:7], v[76:79]\n" \
"buffer_load_dwordx4 v225, s[48:51], s77 offen offset:1024 lds\n" \
"v_mfma_f32_16x16x32_bf16 v[108:111], a[88:91], a[8:11], v[108:111]\n" \
"ds_read_b64_tr_b16 a[104:105], v233 offset:1024\n" \
"v_mul_f32 v38, s76, v38\n" \
"v_mfma_f32_16x16x32_bf16 v[108:111], a[92:95], a[12:15], v[108:111]\n" \
"ds_read_b64_tr_b16 a[106:107], v233 offset:3072\n" \
"v_mul_f32 v39, s76, v39\n" \
"v_mfma_f32_16x16x32_bf16 v[104:107], a[80:83], a[8:11], v[104:107]\n" \
"s_waitcnt lgkmcnt(6)\n" \
"ds_read_b64_tr_b16 a[108:109], v233 offset:9216\n" \
"v_cvt_pk_bf16_f32 v34, v36, v37\n" \
"v_mfma_f32_16x16x32_bf16 v[104:107], a[84:87], a[12:15], v[104:107]\n" \
"buffer_load_dwordx4 v226, s[48:51], s77 offen offset:2048 lds\n" \
"v_mfma_f32_16x16x32_bf16 v[100:103], a[72:75], a[8:11], v[100:103]\n" \
"ds_read_b64_tr_b16 a[110:111], v233 offset:11264\n" \
"v_cvt_pk_bf16_f32 v35, v38, v39\n" \
"v_mfma_f32_16x16x32_bf16 v[100:103], a[76:79], a[12:15], v[100:103]\n" \
"ds_read_b64_tr_b16 a[112:113], v232 offset:1536\n" \
"v_mul_f32 v48, s76, v48\n" \
"v_mfma_f32_16x16x32_bf16 v[96:99], a[64:67], a[8:11], v[96:99]\n" \
"ds_read_b64_tr_b16 a[114:115], v232 offset:3584\n" \
"v_mul_f32 v49, s76, v49\n" \
"v_mfma_f32_16x16x32_bf16 v[96:99], a[68:71], a[12:15], v[96:99]\n" \
"buffer_load_dwordx4 v227, s[48:51], s77 offen offset:3072 lds\n" \
"v_mfma_f32_16x16x32_bf16 v[128:131], a[64:67], a[16:19], v[128:131]\n" \
"ds_read_b64_tr_b16 a[116:117], v232 offset:9728\n" \
"v_mul_f32 v50, s76, v50\n" \
"v_mfma_f32_16x16x32_bf16 v[128:131], a[68:71], a[20:23], v[128:131]\n" \
"ds_read_b64_tr_b16 a[118:119], v232 offset:11776\n" \
"v_mul_f32 v51, s76, v51\n" \
"v_mfma_f32_16x16x32_bf16 v[132:135], a[72:75], a[16:19], v[132:135]\n" \
"s_xor_b32 s77, s77, 0x80\n" \
"ds_read_b64_tr_b16 a[120:121], v233 offset:1536\n" \
"v_cvt_pk_bf16_f32 v48, v48, v49\n" \
"v_mfma_f32_16x16x32_bf16 v[132:135], a[76:79], a[20:23], v[132:135]\n" \
"v_cvt_pk_bf16_f32 v49, v50, v51\n" \
"v_mul_f32 v52, s76, v52\n" \
"v_mfma_f32_16x16x32_bf16 v[136:139], a[80:83], a[16:19], v[136:139]\n" \
"ds_read_b64_tr_b16 a[122:123], v233 offset:3584\n" \
"v_mul_f32 v53, s76, v53\n" \
"v_mfma_f32_16x16x32_bf16 v[136:139], a[84:87], a[20:23], v[136:139]\n" \
"ds_read_b64_tr_b16 a[124:125], v233 offset:9728\n" \
"v_mul_f32 v54, s76, v54\n" \
"v_mfma_f32_16x16x32_bf16 v[140:143], a[88:91], a[16:19], v[140:143]\n" \
"s_add_u32 s93, s87, 0\n" \
"ds_read_b64_tr_b16 a[126:127], v233 offset:11776\n" \
"v_mul_f32 v55, s76, v55\n" \
"v_mfma_f32_16x16x32_bf16 v[140:143], a[92:95], a[20:23], v[140:143]\n" \
"buffer_store_dwordx4 v[196:199], v236, s[56:59], s93 offen offset:0 sc1\n" \
"v_mfma_f32_16x16x32_bf16 v[172:175], a[88:91], a[24:27], v[172:175]\n" \
"v_cvt_pk_bf16_f32 v50, v52, v53\n" \
"v_cvt_pk_bf16_f32 v51, v54, v55\n" \
"v_mfma_f32_16x16x32_bf16 v[172:175], a[92:95], a[28:31], v[172:175]\n" \
"v_mul_f32 v40, s76, v40\n" \
"v_mul_f32 v41, s76, v41\n" \
"v_mfma_f32_16x16x32_bf16 v[168:171], a[80:83], a[24:27], v[168:171]\n" \
"v_mul_f32 v42, s76, v42\n" \
"v_mul_f32 v43, s76, v43\n" \
"v_mfma_f32_16x16x32_bf16 v[168:171], a[84:87], a[28:31], v[168:171]\n" \
"v_cvt_pk_bf16_f32 v40, v40, v41\n" \
"v_cvt_pk_bf16_f32 v41, v42, v43\n" \
"v_mfma_f32_16x16x32_bf16 v[164:167], a[72:75], a[24:27], v[164:167]\n" \
"v_mul_f32 v44, s76, v44\n" \
"v_mul_f32 v45, s76, v45\n" \
"v_mfma_f32_16x16x32_bf16 v[164:167], a[76:79], a[28:31], v[164:167]\n" \
"v_mul_f32 v46, s76, v46\n" \
"v_mul_f32 v47, s76, v47\n" \
"v_mfma_f32_16x16x32_bf16 v[160:163], a[64:67], a[24:27], v[160:163]\n" \
"v_cvt_pk_bf16_f32 v42, v44, v45\n" \
"v_cvt_pk_bf16_f32 v43, v46, v47\n" \
"v_mfma_f32_16x16x32_bf16 v[160:163], a[68:71], a[28:31], v[160:163]\n" \
"s_waitcnt vmcnt(35) lgkmcnt(0)\n" \
"s_barrier\n" \
"v_mfma_f32_16x16x32_bf16 v[80:83], a[96:99], a[0:3], v[80:83]\n" \
"ds_read_b64_tr_b16 a[64:65], v232 offset:16384\n" \
"v_mul_f32 v56, s76, v56\n" \
"v_mfma_f32_16x16x32_bf16 v[80:83], a[100:103], a[4:7], v[80:83]\n" \
"ds_read_b64_tr_b16 a[66:67], v232 offset:18432\n" \
"v_mul_f32 v57, s76, v57\n" \
"v_mfma_f32_16x16x32_bf16 v[84:87], a[104:107], a[0:3], v[84:87]\n" \
"s_add_u32 m0, s79, 0\n" \
"ds_read_b64_tr_b16 a[68:69], v232 offset:24576\n" \
"v_mul_f32 v58, s76, v58\n" \
"v_mfma_f32_16x16x32_bf16 v[84:87], a[108:111], a[4:7], v[84:87]\n" \
"buffer_load_dwordx4 v228, s[52:55], s78 offen offset:0 lds\n" \
"v_mfma_f32_16x16x32_bf16 v[88:91], a[112:115], a[0:3], v[88:91]\n" \
"ds_read_b64_tr_b16 a[70:71], v232 offset:26624\n" \
"v_mul_f32 v59, s76, v59\n" \
"v_mfma_f32_16x16x32_bf16 v[88:91], a[116:119], a[4:7], v[88:91]\n" \
"ds_read_b64_tr_b16 a[72:73], v233 offset:16384\n" \
"v_cvt_pk_bf16_f32 v56, v56, v57\n" \
"v_mfma_f32_16x16x32_bf16 v[92:95], a[120:123], a[0:3], v[92:95]\n" \
"ds_read_b64_tr_b16 a[74:75], v233 offset:18432\n" \
"v_cvt_pk_bf16_f32 v57, v58, v59\n" \
"v_mfma_f32_16x16x32_bf16 v[92:95], a[124:127], a[4:7], v[92:95]\n" \
"buffer_load_dwordx4 v229, s[52:55], s78 offen offset:1024 lds\n" \
"v_mfma_f32_16x16x32_bf16 v[124:127], a[120:123], a[8:11], v[124:127]\n" \
"s_waitcnt vmcnt(35)\n" \
"ds_read_b128 a[0:3], v230 offset:32768\n" \
"v_mul_f32 v60, s76, v60\n" \
"v_mfma_f32_16x16x32_bf16 v[124:127], a[124:127], a[12:15], v[124:127]\n" \
"ds_read_b128 a[4:7], v231 offset:32768\n" \
"v_mul_f32 v61, s76, v61\n" \
"v_mfma_f32_16x16x32_bf16 v[120:123], a[112:115], a[8:11], v[120:123]\n" \
"s_xor_b32 s78, s78, 0x780000\n" \
"ds_read_b64_tr_b16 a[76:77], v233 offset:24576\n" \
"v_mul_f32 v62, s76, v62\n" \
"v_mfma_f32_16x16x32_bf16 v[120:123], a[116:119], a[12:15], v[120:123]\n" \
"buffer_load_dwordx4 v228, s[52:55], s89 offen offset:2048 lds\n" \
"v_mfma_f32_16x16x32_bf16 v[116:119], a[104:107], a[8:11], v[116:119]\n" \
"ds_read_b64_tr_b16 a[78:79], v233 offset:26624\n" \
"v_mul_f32 v63, s76, v63\n" \
"v_mfma_f32_16x16x32_bf16 v[116:119], a[108:111], a[12:15], v[116:119]\n" \
"ds_read_b64_tr_b16 a[80:81], v232 offset:16896\n" \
"v_cvt_pk_bf16_f32 v58, v60, v61\n" \
"v_mfma_f32_16x16x32_bf16 v[112:115], a[96:99], a[8:11], v[112:115]\n" \
"ds_read_b64_tr_b16 a[82:83], v232 offset:18944\n" \
"v_cvt_pk_bf16_f32 v59, v62, v63\n" \
"v_mfma_f32_16x16x32_bf16 v[112:115], a[100:103], a[12:15], v[112:115]\n" \
"buffer_load_dwordx4 v229, s[52:55], s89 offen offset:3072 lds\n" \
"v_mfma_f32_16x16x32_bf16 v[144:147], a[96:99], a[16:19], v[144:147]\n" \
"s_waitcnt vmcnt(35)\n" \
"s_xor_b32 s89, s89, 0x780000\n" \
"ds_read_b128 a[8:11], v230 offset:34816\n" \
"v_mfma_f32_16x16x32_bf16 v[144:147], a[100:103], a[20:23], v[144:147]\n" \
"s_add_u32 m0, s74, 98304\n" \
"ds_read_b128 a[12:15], v231 offset:34816\n" \
"v_mul_f32 v0, s76, v0\n" \
"v_mfma_f32_16x16x32_bf16 v[148:151], a[104:107], a[16:19], v[148:151]\n" \
"s_waitcnt lgkmcnt(6)\n" \
"ds_read_b64_tr_b16 a[84:85], v232 offset:25088\n" \
"v_mul_f32 v1, s76, v1\n" \
"v_mfma_f32_16x16x32_bf16 v[148:151], a[108:111], a[20:23], v[148:151]\n" \
"buffer_load_dwordx4 v224, s[48:51], s75 offen offset:0 lds\n" \
"v_mfma_f32_16x16x32_bf16 v[152:155], a[112:115], a[16:19], v[152:155]\n" \
"ds_read_b64_tr_b16 a[86:87], v232 offset:27136\n" \
"v_mul_f32 v2, s76, v2\n" \
"v_mfma_f32_16x16x32_bf16 v[152:155], a[116:119], a[20:23], v[152:155]\n" \
"ds_read_b64_tr_b16 a[88:89], v233 offset:16896\n" \
"v_mul_f32 v3, s76, v3\n" \
"v_mfma_f32_16x16x32_bf16 v[156:159], a[120:123], a[16:19], v[156:159]\n" \
"ds_read_b64_tr_b16 a[90:91], v233 offset:18944\n" \
"v_cvt_pk_bf16_f32 v36, v0, v1\n" \
"v_mfma_f32_16x16x32_bf16 v[156:159], a[124:127], a[20:23], v[156:159]\n" \
"buffer_load_dwordx4 v225, s[48:51], s75 offen offset:1024 lds\n" \
"v_mfma_f32_16x16x32_bf16 v[188:191], a[120:123], a[24:27], v[188:191]\n" \
"s_waitcnt vmcnt(35)\n" \
"ds_read_b128 a[16:19], v230 offset:36864\n" \
"v_cvt_pk_bf16_f32 v37, v2, v3\n" \
"v_mfma_f32_16x16x32_bf16 v[188:191], a[124:127], a[28:31], v[188:191]\n" \
"ds_read_b128 a[20:23], v231 offset:36864\n" \
"v_mul_f32 v4, s76, v4\n" \
"v_mfma_f32_16x16x32_bf16 v[184:187], a[112:115], a[24:27], v[184:187]\n" \
"s_waitcnt lgkmcnt(6)\n" \
"ds_read_b64_tr_b16 a[92:93], v233 offset:25088\n" \
"v_mul_f32 v5, s76, v5\n" \
"v_mfma_f32_16x16x32_bf16 v[184:187], a[116:119], a[28:31], v[184:187]\n" \
"buffer_load_dwordx4 v226, s[48:51], s75 offen offset:2048 lds\n" \
"v_mfma_f32_16x16x32_bf16 v[180:183], a[104:107], a[24:27], v[180:183]\n" \
"ds_read_b64_tr_b16 a[94:95], v233 offset:27136\n" \
"v_mul_f32 v6, s76, v6\n" \
"v_mfma_f32_16x16x32_bf16 v[180:183], a[108:111], a[28:31], v[180:183]\n" \
"v_mul_f32 v7, s76, v7\n" \
"v_cvt_pk_bf16_f32 v38, v4, v5\n" \
"v_mfma_f32_16x16x32_bf16 v[176:179], a[96:99], a[24:27], v[176:179]\n" \
"v_cvt_pk_bf16_f32 v39, v6, v7\n" \
"v_mul_f32 v16, s76, v16\n" \
"v_mfma_f32_16x16x32_bf16 v[176:179], a[100:103], a[28:31], v[176:179]\n" \
"buffer_load_dwordx4 v227, s[48:51], s75 offen offset:3072 lds\n" \
"v_mfma_f32_16x16x32_bf16 v[64:67], a[64:67], a[0:3], v[64:67]\n" \
"s_waitcnt vmcnt(35)\n" \
"s_xor_b32 s75, s75, 0x780\n" \
"ds_read_b128 a[24:27], v230 offset:38912\n" \
"v_mfma_f32_16x16x32_bf16 v[64:67], a[68:71], a[4:7], v[64:67]\n" \
"s_add_u32 m0, s74, 102400\n" \
"ds_read_b128 a[28:31], v231 offset:38912\n" \
"v_mul_f32 v17, s76, v17\n" \
"v_mfma_f32_16x16x32_bf16 v[68:71], a[72:75], a[0:3], v[68:71]\n" \
"s_waitcnt lgkmcnt(4)\n" \
"ds_read_b64_tr_b16 a[96:97], v232 offset:17408\n" \
"v_mul_f32 v18, s76, v18\n" \
"v_mfma_f32_16x16x32_bf16 v[68:71], a[76:79], a[4:7], v[68:71]\n" \
"buffer_load_dwordx4 v224, s[48:51], s77 offen offset:0 lds\n" \
"v_mfma_f32_16x16x32_bf16 v[72:75], a[80:83], a[0:3], v[72:75]\n" \
"ds_read_b64_tr_b16 a[98:99], v232 offset:19456\n" \
"v_mul_f32 v19, s76, v19\n" \
"v_mfma_f32_16x16x32_bf16 v[72:75], a[84:87], a[4:7], v[72:75]\n" \
"s_waitcnt lgkmcnt(3)\n" \
"ds_read_b64_tr_b16 a[100:101], v232 offset:25600\n" \
"v_cvt_pk_bf16_f32 v16, v16, v17\n" \
"v_mfma_f32_16x16x32_bf16 v[76:79], a[88:91], a[0:3], v[76:79]\n" \
"ds_read_b64_tr_b16 a[102:103], v232 offset:27648\n" \
"v_cvt_pk_bf16_f32 v17, v18, v19\n" \
"v_mfma_f32_16x16x32_bf16 v[76:79], a[92:95], a[4:7], v[76:79]\n" \
"buffer_load_dwordx4 v225, s[48:51], s77 offen offset:1024 lds\n" \
"v_mfma_f32_16x16x32_bf16 v[108:111], a[88:91], a[8:11], v[108:111]\n" \
"ds_read_b64_tr_b16 a[104:105], v233 offset:17408\n" \
"v_mul_f32 v20, s76, v20\n" \
"v_mfma_f32_16x16x32_bf16 v[108:111], a[92:95], a[12:15], v[108:111]\n" \
"ds_read_b64_tr_b16 a[106:107], v233 offset:19456\n" \
"v_mul_f32 v21, s76, v21\n" \
"v_mfma_f32_16x16x32_bf16 v[104:107], a[80:83], a[8:11], v[104:107]\n" \
"s_waitcnt lgkmcnt(6)\n" \
"ds_read_b64_tr_b16 a[108:109], v233 offset:25600\n" \
"v_mul_f32 v22, s76, v22\n" \
"v_mfma_f32_16x16x32_bf16 v[104:107], a[84:87], a[12:15], v[104:107]\n" \
"buffer_load_dwordx4 v226, s[48:51], s77 offen offset:2048 lds\n" \
"v_mfma_f32_16x16x32_bf16 v[100:103], a[72:75], a[8:11], v[100:103]\n" \
"ds_read_b64_tr_b16 a[110:111], v233 offset:27648\n" \
"v_mul_f32 v23, s76, v23\n" \
"v_mfma_f32_16x16x32_bf16 v[100:103], a[76:79], a[12:15], v[100:103]\n" \
"ds_read_b64_tr_b16 a[112:113], v232 offset:17920\n" \
"v_cvt_pk_bf16_f32 v18, v20, v21\n" \
"v_mfma_f32_16x16x32_bf16 v[96:99], a[64:67], a[8:11], v[96:99]\n" \
"ds_read_b64_tr_b16 a[114:115], v232 offset:19968\n" \
"v_cvt_pk_bf16_f32 v19, v22, v23\n" \
"v_mfma_f32_16x16x32_bf16 v[96:99], a[68:71], a[12:15], v[96:99]\n" \
"buffer_load_dwordx4 v227, s[48:51], s77 offen offset:3072 lds\n" \
"v_mfma_f32_16x16x32_bf16 v[128:131], a[64:67], a[16:19], v[128:131]\n" \
"ds_read_b64_tr_b16 a[116:117], v232 offset:26112\n" \
"v_mul_f32 v8, s76, v8\n" \
"v_mfma_f32_16x16x32_bf16 v[128:131], a[68:71], a[20:23], v[128:131]\n" \
"ds_read_b64_tr_b16 a[118:119], v232 offset:28160\n" \
"v_mul_f32 v9, s76, v9\n" \
"v_mfma_f32_16x16x32_bf16 v[132:135], a[72:75], a[16:19], v[132:135]\n" \
"s_xor_b32 s77, s77, 0x780\n" \
"ds_read_b64_tr_b16 a[120:121], v233 offset:17920\n" \
"v_mul_f32 v10, s76, v10\n" \
"v_mfma_f32_16x16x32_bf16 v[132:135], a[76:79], a[20:23], v[132:135]\n" \
"v_mul_f32 v11, s76, v11\n" \
"v_cvt_pk_bf16_f32 v52, v8, v9\n" \
"v_mfma_f32_16x16x32_bf16 v[136:139], a[80:83], a[16:19], v[136:139]\n" \
"ds_read_b64_tr_b16 a[122:123], v233 offset:19968\n" \
"v_cvt_pk_bf16_f32 v53, v10, v11\n" \
"v_mfma_f32_16x16x32_bf16 v[136:139], a[84:87], a[20:23], v[136:139]\n" \
"ds_read_b64_tr_b16 a[124:125], v233 offset:26112\n" \
"v_mul_f32 v12, s76, v12\n" \
"v_mfma_f32_16x16x32_bf16 v[140:143], a[88:91], a[16:19], v[140:143]\n" \
"ds_read_b64_tr_b16 a[126:127], v233 offset:28160\n" \
"v_mul_f32 v13, s76, v13\n" \
"v_mfma_f32_16x16x32_bf16 v[140:143], a[92:95], a[20:23], v[140:143]\n" \
"buffer_store_dwordx4 v[212:215], v236, s[56:59], s93 offen offset:256 sc1\n" \
"v_mfma_f32_16x16x32_bf16 v[172:175], a[88:91], a[24:27], v[172:175]\n" \
"v_mul_f32 v14, s76, v14\n" \
"v_mul_f32 v15, s76, v15\n" \
"v_mfma_f32_16x16x32_bf16 v[172:175], a[92:95], a[28:31], v[172:175]\n" \
"v_cvt_pk_bf16_f32 v54, v12, v13\n" \
"v_cvt_pk_bf16_f32 v55, v14, v15\n" \
"v_mfma_f32_16x16x32_bf16 v[168:171], a[80:83], a[24:27], v[168:171]\n" \
"v_mul_f32 v24, s76, v24\n" \
"v_mul_f32 v25, s76, v25\n" \
"v_mfma_f32_16x16x32_bf16 v[168:171], a[84:87], a[28:31], v[168:171]\n" \
"v_mul_f32 v26, s76, v26\n" \
"v_mul_f32 v27, s76, v27\n" \
"v_mfma_f32_16x16x32_bf16 v[164:167], a[72:75], a[24:27], v[164:167]\n" \
"v_cvt_pk_bf16_f32 v24, v24, v25\n" \
"v_cvt_pk_bf16_f32 v25, v26, v27\n" \
"v_mfma_f32_16x16x32_bf16 v[164:167], a[76:79], a[28:31], v[164:167]\n" \
"v_mul_f32 v28, s76, v28\n" \
"v_mul_f32 v29, s76, v29\n" \
"v_mfma_f32_16x16x32_bf16 v[160:163], a[64:67], a[24:27], v[160:163]\n" \
"v_mul_f32 v30, s76, v30\n" \
"v_mul_f32 v31, s76, v31\n" \
"v_mfma_f32_16x16x32_bf16 v[160:163], a[68:71], a[28:31], v[160:163]\n" \
"s_waitcnt vmcnt(35) lgkmcnt(0)\n" \
"s_barrier\n" \
"v_mfma_f32_16x16x32_bf16 v[80:83], a[96:99], a[0:3], v[80:83]\n" \
"ds_read_b64_tr_b16 a[64:65], v232 offset:32768\n" \
"v_cvt_pk_bf16_f32 v26, v28, v29\n" \
"v_mfma_f32_16x16x32_bf16 v[80:83], a[100:103], a[4:7], v[80:83]\n" \
"ds_read_b64_tr_b16 a[66:67], v232 offset:34816\n" \
"v_cvt_pk_bf16_f32 v27, v30, v31\n" \
"v_mfma_f32_16x16x32_bf16 v[84:87], a[104:107], a[0:3], v[84:87]\n" \
"s_add_u32 m0, s79, 16384\n" \
"ds_read_b64_tr_b16 a[68:69], v232 offset:40960\n" \
"v_accvgpr_read_b32 v243, a160\n" \
"v_mfma_f32_16x16x32_bf16 v[84:87], a[108:111], a[4:7], v[84:87]\n" \
"buffer_load_dwordx4 v228, s[52:55], s78 offen offset:0 lds\n" \
"v_mfma_f32_16x16x32_bf16 v[88:91], a[112:115], a[0:3], v[88:91]\n" \
"ds_read_b64_tr_b16 a[70:71], v232 offset:43008\n" \
"v_accvgpr_read_b32 v252, a161\n" \
"v_mfma_f32_16x16x32_bf16 v[88:91], a[116:119], a[4:7], v[88:91]\n" \
"ds_read_b64_tr_b16 a[72:73], v233 offset:32768\n" \
"v_accvgpr_read_b32 v253, a162\n" \
"v_mfma_f32_16x16x32_bf16 v[92:95], a[120:123], a[0:3], v[92:95]\n" \
"ds_read_b64_tr_b16 a[74:75], v233 offset:34816\n" \
"v_accvgpr_read_b32 v254, a163\n" \
"v_mfma_f32_16x16x32_bf16 v[92:95], a[124:127], a[4:7], v[92:95]\n" \
"buffer_load_dwordx4 v229, s[52:55], s78 offen offset:1024 lds\n" \
"v_mfma_f32_16x16x32_bf16 v[124:127], a[120:123], a[8:11], v[124:127]\n" \
"s_waitcnt vmcnt(35)\n" \
"ds_read_b128 a[0:3], v250 offset:0\n" \
"v_mul_f32 v243, s31, v243\n" \
"v_mfma_f32_16x16x32_bf16 v[124:127], a[124:127], a[12:15], v[124:127]\n" \
"ds_read_b128 a[4:7], v251 offset:0\n" \
"v_mul_f32 v252, s31, v252\n" \
"v_mfma_f32_16x16x32_bf16 v[120:123], a[112:115], a[8:11], v[120:123]\n" \
"s_xor_b32 s78, s78, 0x80000\n" \
"ds_read_b64_tr_b16 a[76:77], v233 offset:40960\n" \
"v_mul_f32 v253, s31, v253\n" \
"v_mfma_f32_16x16x32_bf16 v[120:123], a[116:119], a[12:15], v[120:123]\n" \
"buffer_load_dwordx4 v228, s[52:55], s89 offen offset:2048 lds\n" \
"v_mfma_f32_16x16x32_bf16 v[116:119], a[104:107], a[8:11], v[116:119]\n" \
"ds_read_b64_tr_b16 a[78:79], v233 offset:43008\n" \
"v_mul_f32 v254, s31, v254\n" \
"v_mfma_f32_16x16x32_bf16 v[116:119], a[108:111], a[12:15], v[116:119]\n" \
"ds_read_b64_tr_b16 a[80:81], v232 offset:33280\n" \
"v_cvt_pk_bf16_f32 v44, v243, v252\n" \
"v_mfma_f32_16x16x32_bf16 v[112:115], a[96:99], a[8:11], v[112:115]\n" \
"ds_read_b64_tr_b16 a[82:83], v232 offset:35328\n" \
"v_cvt_pk_bf16_f32 v45, v253, v254\n" \
"v_mfma_f32_16x16x32_bf16 v[112:115], a[100:103], a[12:15], v[112:115]\n" \
"buffer_load_dwordx4 v229, s[52:55], s89 offen offset:3072 lds\n" \
"v_mfma_f32_16x16x32_bf16 v[144:147], a[96:99], a[16:19], v[144:147]\n" \
"s_waitcnt vmcnt(35)\n" \
"s_xor_b32 s89, s89, 0x80000\n" \
"ds_read_b128 a[8:11], v250 offset:2048\n" \
"v_mfma_f32_16x16x32_bf16 v[144:147], a[100:103], a[20:23], v[144:147]\n" \
"s_add_u32 m0, s74, 131072\n" \
"ds_read_b128 a[12:15], v251 offset:2048\n" \
"v_accvgpr_read_b32 v243, a176\n" \
"v_mfma_f32_16x16x32_bf16 v[148:151], a[104:107], a[16:19], v[148:151]\n" \
"s_waitcnt lgkmcnt(6)\n" \
"ds_read_b64_tr_b16 a[84:85], v232 offset:41472\n" \
"v_accvgpr_read_b32 v252, a177\n" \
"v_mfma_f32_16x16x32_bf16 v[148:151], a[108:111], a[20:23], v[148:151]\n" \
"buffer_load_dwordx4 v224, s[48:51], s75 offen offset:0 lds\n" \
"v_mfma_f32_16x16x32_bf16 v[152:155], a[112:115], a[16:19], v[152:155]\n" \
"ds_read_b64_tr_b16 a[86:87], v232 offset:43520\n" \
"v_accvgpr_read_b32 v253, a178\n" \
"v_mfma_f32_16x16x32_bf16 v[152:155], a[116:119], a[20:23], v[152:155]\n" \
"ds_read_b64_tr_b16 a[88:89], v233 offset:33280\n" \
"v_accvgpr_read_b32 v254, a179\n" \
"v_mfma_f32_16x16x32_bf16 v[156:159], a[120:123], a[16:19], v[156:159]\n" \
"ds_read_b64_tr_b16 a[90:91], v233 offset:35328\n" \
"v_mul_f32 v243, s31, v243\n" \
"v_mfma_f32_16x16x32_bf16 v[156:159], a[124:127], a[20:23], v[156:159]\n" \
"buffer_load_dwordx4 v225, s[48:51], s75 offen offset:1024 lds\n" \
"v_mfma_f32_16x16x32_bf16 v[188:191], a[120:123], a[24:27], v[188:191]\n" \
"s_waitcnt vmcnt(35)\n" \
"ds_read_b128 a[16:19], v250 offset:4096\n" \
"v_mul_f32 v252, s31, v252\n" \
"v_mfma_f32_16x16x32_bf16 v[188:191], a[124:127], a[28:31], v[188:191]\n" \
"ds_read_b128 a[20:23], v251 offset:4096\n" \
"v_mul_f32 v253, s31, v253\n" \
"v_mfma_f32_16x16x32_bf16 v[184:187], a[112:115], a[24:27], v[184:187]\n" \
"s_waitcnt lgkmcnt(6)\n" \
"ds_read_b64_tr_b16 a[92:93], v233 offset:41472\n" \
"v_mul_f32 v254, s31, v254\n" \
"v_mfma_f32_16x16x32_bf16 v[184:187], a[116:119], a[28:31], v[184:187]\n" \
"buffer_load_dwordx4 v226, s[48:51], s75 offen offset:2048 lds\n" \
"v_mfma_f32_16x16x32_bf16 v[180:183], a[104:107], a[24:27], v[180:183]\n" \
"ds_read_b64_tr_b16 a[94:95], v233 offset:43520\n" \
"v_cvt_pk_bf16_f32 v46, v243, v252\n" \
"v_mfma_f32_16x16x32_bf16 v[180:183], a[108:111], a[28:31], v[180:183]\n" \
"v_cvt_pk_bf16_f32 v47, v253, v254\n" \
"v_accvgpr_read_b32 v243, a164\n" \
"v_mfma_f32_16x16x32_bf16 v[176:179], a[96:99], a[24:27], v[176:179]\n" \
"v_accvgpr_read_b32 v252, a165\n" \
"v_accvgpr_read_b32 v253, a166\n" \
"v_mfma_f32_16x16x32_bf16 v[176:179], a[100:103], a[28:31], v[176:179]\n" \
"buffer_load_dwordx4 v227, s[48:51], s75 offen offset:3072 lds\n" \
"v_mfma_f32_16x16x32_bf16 v[64:67], a[64:67], a[0:3], v[64:67]\n" \
"s_waitcnt vmcnt(35)\n" \
"s_xor_b32 s75, s75, 0x80\n" \
"ds_read_b128 a[24:27], v250 offset:6144\n" \
"v_mfma_f32_16x16x32_bf16 v[64:67], a[68:71], a[4:7], v[64:67]\n" \
"s_add_u32 m0, s74, 135168\n" \
"ds_read_b128 a[28:31], v251 offset:6144\n" \
"v_accvgpr_read_b32 v254, a167\n" \
"v_mfma_f32_16x16x32_bf16 v[68:71], a[72:75], a[0:3], v[68:71]\n" \
"s_waitcnt lgkmcnt(4)\n" \
"ds_read_b64_tr_b16 a[96:97], v232 offset:33792\n" \
"v_mul_f32 v243, s31, v243\n" \
"v_mfma_f32_16x16x32_bf16 v[68:71], a[76:79], a[4:7], v[68:71]\n" \
"buffer_load_dwordx4 v224, s[48:51], s77 offen offset:0 lds\n" \
"v_mfma_f32_16x16x32_bf16 v[72:75], a[80:83], a[0:3], v[72:75]\n" \
"ds_read_b64_tr_b16 a[98:99], v232 offset:35840\n" \
"v_mul_f32 v252, s31, v252\n" \
"v_mfma_f32_16x16x32_bf16 v[72:75], a[84:87], a[4:7], v[72:75]\n" \
"s_waitcnt lgkmcnt(3)\n" \
"ds_read_b64_tr_b16 a[100:101], v232 offset:41984\n" \
"v_mul_f32 v253, s31, v253\n" \
"v_mfma_f32_16x16x32_bf16 v[76:79], a[88:91], a[0:3], v[76:79]\n" \
"ds_read_b64_tr_b16 a[102:103], v232 offset:44032\n" \
"v_mul_f32 v254, s31, v254\n" \
"v_mfma_f32_16x16x32_bf16 v[76:79], a[92:95], a[4:7], v[76:79]\n" \
"buffer_load_dwordx4 v225, s[48:51], s77 offen offset:1024 lds\n" \
"v_mfma_f32_16x16x32_bf16 v[108:111], a[88:91], a[8:11], v[108:111]\n" \
"ds_read_b64_tr_b16 a[104:105], v233 offset:33792\n" \
"v_cvt_pk_bf16_f32 v60, v243, v252\n" \
"v_mfma_f32_16x16x32_bf16 v[108:111], a[92:95], a[12:15], v[108:111]\n" \
"ds_read_b64_tr_b16 a[106:107], v233 offset:35840\n" \
"v_cvt_pk_bf16_f32 v61, v253, v254\n" \
"v_mfma_f32_16x16x32_bf16 v[104:107], a[80:83], a[8:11], v[104:107]\n" \
"s_waitcnt lgkmcnt(6)\n" \
"ds_read_b64_tr_b16 a[108:109], v233 offset:41984\n" \
"v_accvgpr_read_b32 v243, a180\n" \
"Lpg3342_%=:\n" \
"s_cbranch_execz Lpg5598_%=\n" \
"v_mfma_f32_16x16x32_bf16 v[104:107], a[84:87], a[12:15], v[104:107]\n" \
"buffer_load_dwordx4 v226, s[48:51], s77 offen offset:2048 lds\n" \
"v_mfma_f32_16x16x32_bf16 v[100:103], a[72:75], a[8:11], v[100:103]\n" \
"ds_read_b64_tr_b16 a[110:111], v233 offset:44032\n" \
"v_accvgpr_read_b32 v252, a181\n" \
"v_mfma_f32_16x16x32_bf16 v[100:103], a[76:79], a[12:15], v[100:103]\n" \
"ds_read_b64_tr_b16 a[112:113], v232 offset:34304\n" \
"v_accvgpr_read_b32 v253, a182\n" \
"v_mfma_f32_16x16x32_bf16 v[96:99], a[64:67], a[8:11], v[96:99]\n" \
"ds_read_b64_tr_b16 a[114:115], v232 offset:36352\n" \
"v_accvgpr_read_b32 v254, a183\n" \
"v_mfma_f32_16x16x32_bf16 v[96:99], a[68:71], a[12:15], v[96:99]\n" \
"buffer_load_dwordx4 v227, s[48:51], s77 offen offset:3072 lds\n" \
"v_mfma_f32_16x16x32_bf16 v[128:131], a[64:67], a[16:19], v[128:131]\n" \
"ds_read_b64_tr_b16 a[116:117], v232 offset:42496\n" \
"v_mul_f32 v243, s31, v243\n" \
"v_mfma_f32_16x16x32_bf16 v[128:131], a[68:71], a[20:23], v[128:131]\n" \
"ds_read_b64_tr_b16 a[118:119], v232 offset:44544\n" \
"v_mul_f32 v252, s31, v252\n" \
"v_mfma_f32_16x16x32_bf16 v[132:135], a[72:75], a[16:19], v[132:135]\n" \
"s_xor_b32 s77, s77, 0x80\n" \
"ds_read_b64_tr_b16 a[120:121], v233 offset:34304\n" \
"v_mul_f32 v253, s31, v253\n" \
"v_mfma_f32_16x16x32_bf16 v[132:135], a[76:79], a[20:23], v[132:135]\n" \
"v_mul_f32 v254, s31, v254\n" \
"v_cvt_pk_bf16_f32 v62, v243, v252\n" \
"v_mfma_f32_16x16x32_bf16 v[136:139], a[80:83], a[16:19], v[136:139]\n" \
"ds_read_b64_tr_b16 a[122:123], v233 offset:36352\n" \
"v_cvt_pk_bf16_f32 v63, v253, v254\n" \
"v_mfma_f32_16x16x32_bf16 v[136:139], a[84:87], a[20:23], v[136:139]\n" \
"ds_read_b64_tr_b16 a[124:125], v233 offset:42496\n" \
"v_accvgpr_read_b32 v243, a168\n" \
"v_mfma_f32_16x16x32_bf16 v[140:143], a[88:91], a[16:19], v[140:143]\n" \
"ds_read_b64_tr_b16 a[126:127], v233 offset:44544\n" \
"v_accvgpr_read_b32 v252, a169\n" \
"v_mfma_f32_16x16x32_bf16 v[140:143], a[92:95], a[20:23], v[140:143]\n" \
"buffer_store_dwordx4 v[204:207], v236, s[56:59], s93 offen offset:2048 sc1\n" \
"v_mfma_f32_16x16x32_bf16 v[172:175], a[88:91], a[24:27], v[172:175]\n" \
"v_accvgpr_read_b32 v253, a170\n" \
"v_accvgpr_read_b32 v254, a171\n" \
"v_mfma_f32_16x16x32_bf16 v[172:175], a[92:95], a[28:31], v[172:175]\n" \
"v_mul_f32 v243, s31, v243\n" \
"v_mul_f32 v252, s31, v252\n" \
"v_mfma_f32_16x16x32_bf16 v[168:171], a[80:83], a[24:27], v[168:171]\n" \
"v_mul_f32 v253, s31, v253\n" \
"v_mul_f32 v254, s31, v254\n" \
"v_mfma_f32_16x16x32_bf16 v[168:171], a[84:87], a[28:31], v[168:171]\n" \
"v_cvt_pk_bf16_f32 v20, v243, v252\n" \
"v_cvt_pk_bf16_f32 v21, v253, v254\n" \
"v_mfma_f32_16x16x32_bf16 v[164:167], a[72:75], a[24:27], v[164:167]\n" \
"v_accvgpr_read_b32 v243, a184\n" \
"v_accvgpr_read_b32 v252, a185\n" \
"v_mfma_f32_16x16x32_bf16 v[164:167], a[76:79], a[28:31], v[164:167]\n" \
"v_accvgpr_read_b32 v253, a186\n" \
"v_accvgpr_read_b32 v254, a187\n" \
"v_mfma_f32_16x16x32_bf16 v[160:163], a[64:67], a[24:27], v[160:163]\n" \
"v_mul_f32 v243, s31, v243\n" \
"v_mul_f32 v252, s31, v252\n" \
"v_mfma_f32_16x16x32_bf16 v[160:163], a[68:71], a[28:31], v[160:163]\n" \
"s_waitcnt vmcnt(35) lgkmcnt(0)\n" \
"s_barrier\n" \
"v_mfma_f32_16x16x32_bf16 v[80:83], a[96:99], a[0:3], v[80:83]\n" \
"ds_read_b64_tr_b16 a[64:65], v232 offset:49152\n" \
"v_mul_f32 v253, s31, v253\n" \
"v_mfma_f32_16x16x32_bf16 v[80:83], a[100:103], a[4:7], v[80:83]\n" \
"ds_read_b64_tr_b16 a[66:67], v232 offset:51200\n" \
"v_mul_f32 v254, s31, v254\n" \
"v_mfma_f32_16x16x32_bf16 v[84:87], a[104:107], a[0:3], v[84:87]\n" \
"s_add_u32 m0, s79, 32768\n" \
"ds_read_b64_tr_b16 a[68:69], v232 offset:57344\n" \
"v_cvt_pk_bf16_f32 v22, v243, v252\n" \
"v_mfma_f32_16x16x32_bf16 v[84:87], a[108:111], a[4:7], v[84:87]\n" \
"buffer_load_dwordx4 v228, s[52:55], s78 offen offset:0 lds\n" \
"v_mfma_f32_16x16x32_bf16 v[88:91], a[112:115], a[0:3], v[88:91]\n" \
"ds_read_b64_tr_b16 a[70:71], v232 offset:59392\n" \
"v_cvt_pk_bf16_f32 v23, v253, v254\n" \
"v_mfma_f32_16x16x32_bf16 v[88:91], a[116:119], a[4:7], v[88:91]\n" \
"ds_read_b64_tr_b16 a[72:73], v233 offset:49152\n" \
"v_accvgpr_read_b32 v243, a172\n" \
"v_mfma_f32_16x16x32_bf16 v[92:95], a[120:123], a[0:3], v[92:95]\n" \
"ds_read_b64_tr_b16 a[74:75], v233 offset:51200\n" \
"v_accvgpr_read_b32 v252, a173\n" \
"v_mfma_f32_16x16x32_bf16 v[92:95], a[124:127], a[4:7], v[92:95]\n" \
"buffer_load_dwordx4 v229, s[52:55], s78 offen offset:1024 lds\n" \
"v_mfma_f32_16x16x32_bf16 v[124:127], a[120:123], a[8:11], v[124:127]\n" \
"s_waitcnt vmcnt(35)\n" \
"ds_read_b128 a[0:3], v230 offset:0\n" \
"v_accvgpr_read_b32 v253, a174\n" \
"v_mfma_f32_16x16x32_bf16 v[124:127], a[124:127], a[12:15], v[124:127]\n" \
"ds_read_b128 a[4:7], v231 offset:0\n" \
"v_accvgpr_read_b32 v254, a175\n" \
"v_mfma_f32_16x16x32_bf16 v[120:123], a[112:115], a[8:11], v[120:123]\n" \
"s_xor_b32 s78, s78, 0x180000\n" \
"ds_read_b64_tr_b16 a[76:77], v233 offset:57344\n" \
"v_mul_f32 v243, s31, v243\n" \
"v_mfma_f32_16x16x32_bf16 v[120:123], a[116:119], a[12:15], v[120:123]\n" \
"buffer_load_dwordx4 v228, s[52:55], s89 offen offset:2048 lds\n" \
"v_mfma_f32_16x16x32_bf16 v[116:119], a[104:107], a[8:11], v[116:119]\n" \
"ds_read_b64_tr_b16 a[78:79], v233 offset:59392\n" \
"v_mul_f32 v252, s31, v252\n" \
"v_mfma_f32_16x16x32_bf16 v[116:119], a[108:111], a[12:15], v[116:119]\n" \
"ds_read_b64_tr_b16 a[80:81], v232 offset:49664\n" \
"v_mul_f32 v253, s31, v253\n" \
"v_mfma_f32_16x16x32_bf16 v[112:115], a[96:99], a[8:11], v[112:115]\n" \
"ds_read_b64_tr_b16 a[82:83], v232 offset:51712\n" \
"v_mul_f32 v254, s31, v254\n" \
"v_mfma_f32_16x16x32_bf16 v[112:115], a[100:103], a[12:15], v[112:115]\n" \
"buffer_load_dwordx4 v229, s[52:55], s89 offen offset:3072 lds\n" \
"v_mfma_f32_16x16x32_bf16 v[144:147], a[96:99], a[16:19], v[144:147]\n" \
"s_waitcnt vmcnt(35)\n" \
"s_xor_b32 s89, s89, 0x180000\n" \
"ds_read_b128 a[8:11], v230 offset:2048\n" \
"v_mfma_f32_16x16x32_bf16 v[144:147], a[100:103], a[20:23], v[144:147]\n" \
"s_add_u32 m0, s74, 65536\n" \
"ds_read_b128 a[12:15], v231 offset:2048\n" \
"v_cvt_pk_bf16_f32 v28, v243, v252\n" \
"v_mfma_f32_16x16x32_bf16 v[148:151], a[104:107], a[16:19], v[148:151]\n" \
"s_waitcnt lgkmcnt(6)\n" \
"ds_read_b64_tr_b16 a[84:85], v232 offset:57856\n" \
"v_cvt_pk_bf16_f32 v29, v253, v254\n" \
"v_mfma_f32_16x16x32_bf16 v[148:151], a[108:111], a[20:23], v[148:151]\n" \
"buffer_load_dwordx4 v224, s[48:51], s75 offen offset:0 lds\n" \
"v_mfma_f32_16x16x32_bf16 v[152:155], a[112:115], a[16:19], v[152:155]\n" \
"ds_read_b64_tr_b16 a[86:87], v232 offset:59904\n" \
"v_accvgpr_read_b32 v243, a188\n" \
"v_mfma_f32_16x16x32_bf16 v[152:155], a[116:119], a[20:23], v[152:155]\n" \
"ds_read_b64_tr_b16 a[88:89], v233 offset:49664\n" \
"v_accvgpr_read_b32 v252, a189\n" \
"v_mfma_f32_16x16x32_bf16 v[156:159], a[120:123], a[16:19], v[156:159]\n" \
"ds_read_b64_tr_b16 a[90:91], v233 offset:51712\n" \
"v_accvgpr_read_b32 v253, a190\n" \
"v_mfma_f32_16x16x32_bf16 v[156:159], a[124:127], a[20:23], v[156:159]\n" \
"buffer_load_dwordx4 v225, s[48:51], s75 offen offset:1024 lds\n" \
"v_mfma_f32_16x16x32_bf16 v[188:191], a[120:123], a[24:27], v[188:191]\n" \
"s_waitcnt vmcnt(35)\n" \
"ds_read_b128 a[16:19], v230 offset:4096\n" \
"v_accvgpr_read_b32 v254, a191\n" \
"v_mfma_f32_16x16x32_bf16 v[188:191], a[124:127], a[28:31], v[188:191]\n" \
"ds_read_b128 a[20:23], v231 offset:4096\n" \
"v_mul_f32 v243, s31, v243\n" \
"v_mfma_f32_16x16x32_bf16 v[184:187], a[112:115], a[24:27], v[184:187]\n" \
"s_waitcnt lgkmcnt(6)\n" \
"ds_read_b64_tr_b16 a[92:93], v233 offset:57856\n" \
"v_mul_f32 v252, s31, v252\n" \
"v_mfma_f32_16x16x32_bf16 v[184:187], a[116:119], a[28:31], v[184:187]\n" \
"buffer_load_dwordx4 v226, s[48:51], s75 offen offset:2048 lds\n" \
"v_mfma_f32_16x16x32_bf16 v[180:183], a[104:107], a[24:27], v[180:183]\n" \
"ds_read_b64_tr_b16 a[94:95], v233 offset:59904\n" \
"v_mul_f32 v253, s31, v253\n" \
"v_mfma_f32_16x16x32_bf16 v[180:183], a[108:111], a[28:31], v[180:183]\n" \
"v_mul_f32 v254, s31, v254\n" \
"v_cvt_pk_bf16_f32 v30, v243, v252\n" \
"v_mfma_f32_16x16x32_bf16 v[176:179], a[96:99], a[24:27], v[176:179]\n" \
"v_cvt_pk_bf16_f32 v31, v253, v254\n" \
"v_accvgpr_read_b32 v243, a128\n" \
"v_mfma_f32_16x16x32_bf16 v[176:179], a[100:103], a[28:31], v[176:179]\n" \
"buffer_load_dwordx4 v227, s[48:51], s75 offen offset:3072 lds\n" \
"v_mfma_f32_16x16x32_bf16 v[64:67], a[64:67], a[0:3], v[64:67]\n" \
"s_waitcnt vmcnt(35)\n" \
"s_xor_b32 s75, s75, 0x180\n" \
"ds_read_b128 a[24:27], v230 offset:6144\n" \
"v_mfma_f32_16x16x32_bf16 v[64:67], a[68:71], a[4:7], v[64:67]\n" \
"s_add_u32 m0, s74, 69632\n" \
"ds_read_b128 a[28:31], v231 offset:6144\n" \
"v_accvgpr_read_b32 v252, a129\n" \
"v_mfma_f32_16x16x32_bf16 v[68:71], a[72:75], a[0:3], v[68:71]\n" \
"s_waitcnt lgkmcnt(4)\n" \
"ds_read_b64_tr_b16 a[96:97], v232 offset:50176\n" \
"v_accvgpr_read_b32 v253, a130\n" \
"v_mfma_f32_16x16x32_bf16 v[68:71], a[76:79], a[4:7], v[68:71]\n" \
"buffer_load_dwordx4 v224, s[48:51], s77 offen offset:0 lds\n" \
"v_mfma_f32_16x16x32_bf16 v[72:75], a[80:83], a[0:3], v[72:75]\n" \
"ds_read_b64_tr_b16 a[98:99], v232 offset:52224\n" \
"v_accvgpr_read_b32 v254, a131\n" \
"v_mfma_f32_16x16x32_bf16 v[72:75], a[84:87], a[4:7], v[72:75]\n" \
"s_waitcnt lgkmcnt(3)\n" \
"ds_read_b64_tr_b16 a[100:101], v232 offset:58368\n" \
"v_mul_f32 v243, s31, v243\n" \
"v_mfma_f32_16x16x32_bf16 v[76:79], a[88:91], a[0:3], v[76:79]\n" \
"ds_read_b64_tr_b16 a[102:103], v232 offset:60416\n" \
"v_mul_f32 v252, s31, v252\n" \
"v_mfma_f32_16x16x32_bf16 v[76:79], a[92:95], a[4:7], v[76:79]\n" \
"buffer_load_dwordx4 v225, s[48:51], s77 offen offset:1024 lds\n" \
"v_mfma_f32_16x16x32_bf16 v[108:111], a[88:91], a[8:11], v[108:111]\n" \
"ds_read_b64_tr_b16 a[104:105], v233 offset:50176\n" \
"v_mul_f32 v253, s31, v253\n" \
"v_mfma_f32_16x16x32_bf16 v[108:111], a[92:95], a[12:15], v[108:111]\n" \
"ds_read_b64_tr_b16 a[106:107], v233 offset:52224\n" \
"v_mul_f32 v254, s31, v254\n" \
"v_mfma_f32_16x16x32_bf16 v[104:107], a[80:83], a[8:11], v[104:107]\n" \
"s_waitcnt lgkmcnt(6)\n" \
"ds_read_b64_tr_b16 a[108:109], v233 offset:58368\n" \
"v_cvt_pk_bf16_f32 v0, v243, v252\n" \
"v_mfma_f32_16x16x32_bf16 v[104:107], a[84:87], a[12:15], v[104:107]\n" \
"buffer_load_dwordx4 v226, s[48:51], s77 offen offset:2048 lds\n" \
"v_mfma_f32_16x16x32_bf16 v[100:103], a[72:75], a[8:11], v[100:103]\n" \
"ds_read_b64_tr_b16 a[110:111], v233 offset:60416\n" \
"v_cvt_pk_bf16_f32 v1, v253, v254\n" \
"v_mfma_f32_16x16x32_bf16 v[100:103], a[76:79], a[12:15], v[100:103]\n" \
"ds_read_b64_tr_b16 a[112:113], v232 offset:50688\n" \
"v_accvgpr_read_b32 v243, a144\n" \
"v_mfma_f32_16x16x32_bf16 v[96:99], a[64:67], a[8:11], v[96:99]\n" \
"ds_read_b64_tr_b16 a[114:115], v232 offset:52736\n" \
"v_accvgpr_read_b32 v252, a145\n" \
"v_mfma_f32_16x16x32_bf16 v[96:99], a[68:71], a[12:15], v[96:99]\n" \
"buffer_load_dwordx4 v227, s[48:51], s77 offen offset:3072 lds\n" \
"v_mfma_f32_16x16x32_bf16 v[128:131], a[64:67], a[16:19], v[128:131]\n" \
"ds_read_b64_tr_b16 a[116:117], v232 offset:58880\n" \
"v_accvgpr_read_b32 v253, a146\n" \
"v_mfma_f32_16x16x32_bf16 v[128:131], a[68:71], a[20:23], v[128:131]\n" \
"ds_read_b64_tr_b16 a[118:119], v232 offset:60928\n" \
"v_accvgpr_read_b32 v254, a147\n" \
"v_mfma_f32_16x16x32_bf16 v[132:135], a[72:75], a[16:19], v[132:135]\n" \
"s_xor_b32 s77, s77, 0x180\n" \
"ds_read_b64_tr_b16 a[120:121], v233 offset:50688\n" \
"v_mul_f32 v243, s31, v243\n" \
"v_mfma_f32_16x16x32_bf16 v[132:135], a[76:79], a[20:23], v[132:135]\n" \
"v_mul_f32 v252, s31, v252\n" \
"v_mul_f32 v253, s31, v253\n" \
"v_mfma_f32_16x16x32_bf16 v[136:139], a[80:83], a[16:19], v[136:139]\n" \
"ds_read_b64_tr_b16 a[122:123], v233 offset:52736\n" \
"v_mul_f32 v254, s31, v254\n" \
"v_mfma_f32_16x16x32_bf16 v[136:139], a[84:87], a[20:23], v[136:139]\n" \
"ds_read_b64_tr_b16 a[124:125], v233 offset:58880\n" \
"v_cvt_pk_bf16_f32 v2, v243, v252\n" \
"v_mfma_f32_16x16x32_bf16 v[140:143], a[88:91], a[16:19], v[140:143]\n" \
"ds_read_b64_tr_b16 a[126:127], v233 offset:60928\n" \
"v_cvt_pk_bf16_f32 v3, v253, v254\n" \
"v_mfma_f32_16x16x32_bf16 v[140:143], a[92:95], a[20:23], v[140:143]\n" \
"buffer_store_dwordx4 v[220:223], v236, s[56:59], s93 offen offset:2304 sc1\n" \
"v_mfma_f32_16x16x32_bf16 v[172:175], a[88:91], a[24:27], v[172:175]\n" \
"v_accvgpr_read_b32 v243, a132\n" \
"v_accvgpr_read_b32 v252, a133\n" \
"v_mfma_f32_16x16x32_bf16 v[172:175], a[92:95], a[28:31], v[172:175]\n" \
"v_accvgpr_read_b32 v253, a134\n" \
"v_accvgpr_read_b32 v254, a135\n" \
"v_mfma_f32_16x16x32_bf16 v[168:171], a[80:83], a[24:27], v[168:171]\n" \
"v_mul_f32 v243, s31, v243\n" \
"v_mul_f32 v252, s31, v252\n" \
"v_mfma_f32_16x16x32_bf16 v[168:171], a[84:87], a[28:31], v[168:171]\n" \
"v_mul_f32 v253, s31, v253\n" \
"v_mul_f32 v254, s31, v254\n" \
"v_mfma_f32_16x16x32_bf16 v[164:167], a[72:75], a[24:27], v[164:167]\n" \
"v_cvt_pk_bf16_f32 v4, v243, v252\n" \
"v_cvt_pk_bf16_f32 v5, v253, v254\n" \
"v_mfma_f32_16x16x32_bf16 v[164:167], a[76:79], a[28:31], v[164:167]\n" \
"v_accvgpr_read_b32 v243, a148\n" \
"v_accvgpr_read_b32 v252, a149\n" \
"v_mfma_f32_16x16x32_bf16 v[160:163], a[64:67], a[24:27], v[160:163]\n" \
"v_accvgpr_read_b32 v253, a150\n" \
"v_accvgpr_read_b32 v254, a151\n" \
"v_mfma_f32_16x16x32_bf16 v[160:163], a[68:71], a[28:31], v[160:163]\n" \
"s_waitcnt vmcnt(35) lgkmcnt(0)\n" \
"s_barrier\n" \
"v_mfma_f32_16x16x32_bf16 v[80:83], a[96:99], a[0:3], v[80:83]\n" \
"ds_read_b64_tr_b16 a[64:65], v232 offset:0\n" \
"v_mul_f32 v243, s31, v243\n" \
"v_mfma_f32_16x16x32_bf16 v[80:83], a[100:103], a[4:7], v[80:83]\n" \
"ds_read_b64_tr_b16 a[66:67], v232 offset:2048\n" \
"v_mul_f32 v252, s31, v252\n" \
"v_mfma_f32_16x16x32_bf16 v[84:87], a[104:107], a[0:3], v[84:87]\n" \
"s_add_u32 m0, s79, 49152\n" \
"ds_read_b64_tr_b16 a[68:69], v232 offset:8192\n" \
"v_mul_f32 v253, s31, v253\n" \
"v_mfma_f32_16x16x32_bf16 v[84:87], a[108:111], a[4:7], v[84:87]\n" \
"buffer_load_dwordx4 v228, s[52:55], s78 offen offset:0 lds\n" \
"v_mfma_f32_16x16x32_bf16 v[88:91], a[112:115], a[0:3], v[88:91]\n" \
"ds_read_b64_tr_b16 a[70:71], v232 offset:10240\n" \
"v_mul_f32 v254, s31, v254\n" \
"v_mfma_f32_16x16x32_bf16 v[88:91], a[116:119], a[4:7], v[88:91]\n" \
"ds_read_b64_tr_b16 a[72:73], v233 offset:0\n" \
"v_cvt_pk_bf16_f32 v6, v243, v252\n" \
"v_mfma_f32_16x16x32_bf16 v[92:95], a[120:123], a[0:3], v[92:95]\n" \
"ds_read_b64_tr_b16 a[74:75], v233 offset:2048\n" \
"v_cvt_pk_bf16_f32 v7, v253, v254\n" \
"v_mfma_f32_16x16x32_bf16 v[92:95], a[124:127], a[4:7], v[92:95]\n" \
"buffer_load_dwordx4 v229, s[52:55], s78 offen offset:1024 lds\n" \
"v_mfma_f32_16x16x32_bf16 v[124:127], a[120:123], a[8:11], v[124:127]\n" \
"s_waitcnt vmcnt(35)\n" \
"ds_read_b128 a[0:3], v230 offset:32768\n" \
"v_accvgpr_read_b32 v243, a136\n" \
"v_mfma_f32_16x16x32_bf16 v[124:127], a[124:127], a[12:15], v[124:127]\n" \
"ds_read_b128 a[4:7], v231 offset:32768\n" \
"v_accvgpr_read_b32 v252, a137\n" \
"v_mfma_f32_16x16x32_bf16 v[120:123], a[112:115], a[8:11], v[120:123]\n" \
"s_xor_b32 s78, s78, 0x300000\n" \
"ds_read_b64_tr_b16 a[76:77], v233 offset:8192\n" \
"v_accvgpr_read_b32 v253, a138\n" \
"v_mfma_f32_16x16x32_bf16 v[120:123], a[116:119], a[12:15], v[120:123]\n" \
"buffer_load_dwordx4 v228, s[52:55], s89 offen offset:2048 lds\n" \
"v_mfma_f32_16x16x32_bf16 v[116:119], a[104:107], a[8:11], v[116:119]\n" \
"ds_read_b64_tr_b16 a[78:79], v233 offset:10240\n" \
"v_accvgpr_read_b32 v254, a139\n" \
"v_mfma_f32_16x16x32_bf16 v[116:119], a[108:111], a[12:15], v[116:119]\n" \
"ds_read_b64_tr_b16 a[80:81], v232 offset:512\n" \
"v_mul_f32 v243, s31, v243\n" \
"v_mfma_f32_16x16x32_bf16 v[112:115], a[96:99], a[8:11], v[112:115]\n" \
"ds_read_b64_tr_b16 a[82:83], v232 offset:2560\n" \
"v_mul_f32 v252, s31, v252\n" \
"v_mfma_f32_16x16x32_bf16 v[112:115], a[100:103], a[12:15], v[112:115]\n" \
"buffer_load_dwordx4 v229, s[52:55], s89 offen offset:3072 lds\n" \
"v_mfma_f32_16x16x32_bf16 v[144:147], a[96:99], a[16:19], v[144:147]\n" \
"s_waitcnt vmcnt(35)\n" \
"s_xor_b32 s89, s89, 0x300000\n" \
"ds_read_b128 a[8:11], v230 offset:34816\n" \
"v_mfma_f32_16x16x32_bf16 v[144:147], a[100:103], a[20:23], v[144:147]\n" \
"s_add_u32 m0, s74, 98304\n" \
"ds_read_b128 a[12:15], v231 offset:34816\n" \
"v_mul_f32 v253, s31, v253\n" \
"v_mfma_f32_16x16x32_bf16 v[148:151], a[104:107], a[16:19], v[148:151]\n" \
"s_waitcnt lgkmcnt(6)\n" \
"ds_read_b64_tr_b16 a[84:85], v232 offset:8704\n" \
"v_mul_f32 v254, s31, v254\n" \
"v_mfma_f32_16x16x32_bf16 v[148:151], a[108:111], a[20:23], v[148:151]\n" \
"buffer_load_dwordx4 v224, s[48:51], s75 offen offset:0 lds\n" \
"v_mfma_f32_16x16x32_bf16 v[152:155], a[112:115], a[16:19], v[152:155]\n" \
"ds_read_b64_tr_b16 a[86:87], v232 offset:10752\n" \
"v_cvt_pk_bf16_f32 v8, v243, v252\n" \
"v_mfma_f32_16x16x32_bf16 v[152:155], a[116:119], a[20:23], v[152:155]\n" \
"ds_read_b64_tr_b16 a[88:89], v233 offset:512\n" \
"v_cvt_pk_bf16_f32 v9, v253, v254\n" \
"v_mfma_f32_16x16x32_bf16 v[156:159], a[120:123], a[16:19], v[156:159]\n" \
"ds_read_b64_tr_b16 a[90:91], v233 offset:2560\n" \
"v_accvgpr_read_b32 v243, a152\n" \
"v_mfma_f32_16x16x32_bf16 v[156:159], a[124:127], a[20:23], v[156:159]\n" \
"buffer_load_dwordx4 v225, s[48:51], s75 offen offset:1024 lds\n" \
"v_mfma_f32_16x16x32_bf16 v[188:191], a[120:123], a[24:27], v[188:191]\n" \
"s_waitcnt vmcnt(35)\n" \
"ds_read_b128 a[16:19], v230 offset:36864\n" \
"v_accvgpr_read_b32 v252, a153\n" \
"v_mfma_f32_16x16x32_bf16 v[188:191], a[124:127], a[28:31], v[188:191]\n" \
"ds_read_b128 a[20:23], v231 offset:36864\n" \
"v_accvgpr_read_b32 v253, a154\n" \
"v_mfma_f32_16x16x32_bf16 v[184:187], a[112:115], a[24:27], v[184:187]\n" \
"s_waitcnt lgkmcnt(6)\n" \
"ds_read_b64_tr_b16 a[92:93], v233 offset:8704\n" \
"v_accvgpr_read_b32 v254, a155\n" \
"v_mfma_f32_16x16x32_bf16 v[184:187], a[116:119], a[28:31], v[184:187]\n" \
"buffer_load_dwordx4 v226, s[48:51], s75 offen offset:2048 lds\n" \
"v_mfma_f32_16x16x32_bf16 v[180:183], a[104:107], a[24:27], v[180:183]\n" \
"ds_read_b64_tr_b16 a[94:95], v233 offset:10752\n" \
"v_mul_f32 v243, s31, v243\n" \
"v_mfma_f32_16x16x32_bf16 v[180:183], a[108:111], a[28:31], v[180:183]\n" \
"v_mul_f32 v252, s31, v252\n" \
"v_mul_f32 v253, s31, v253\n" \
"v_mfma_f32_16x16x32_bf16 v[176:179], a[96:99], a[24:27], v[176:179]\n" \
"v_mul_f32 v254, s31, v254\n" \
"v_cvt_pk_bf16_f32 v10, v243, v252\n" \
"v_mfma_f32_16x16x32_bf16 v[176:179], a[100:103], a[28:31], v[176:179]\n" \
"buffer_load_dwordx4 v227, s[48:51], s75 offen offset:3072 lds\n" \
"v_mfma_f32_16x16x32_bf16 v[64:67], a[64:67], a[0:3], v[64:67]\n" \
"s_waitcnt vmcnt(35)\n" \
"s_xor_b32 s75, s75, 0x300\n" \
"ds_read_b128 a[24:27], v230 offset:38912\n" \
"v_mfma_f32_16x16x32_bf16 v[64:67], a[68:71], a[4:7], v[64:67]\n" \
"s_add_u32 m0, s74, 102400\n" \
"ds_read_b128 a[28:31], v231 offset:38912\n" \
"v_cvt_pk_bf16_f32 v11, v253, v254\n" \
"v_mfma_f32_16x16x32_bf16 v[68:71], a[72:75], a[0:3], v[68:71]\n" \
"s_waitcnt lgkmcnt(4)\n" \
"ds_read_b64_tr_b16 a[96:97], v232 offset:1024\n" \
"v_accvgpr_read_b32 v243, a140\n" \
"v_mfma_f32_16x16x32_bf16 v[68:71], a[76:79], a[4:7], v[68:71]\n" \
"buffer_load_dwordx4 v224, s[48:51], s77 offen offset:0 lds\n" \
"v_mfma_f32_16x16x32_bf16 v[72:75], a[80:83], a[0:3], v[72:75]\n" \
"ds_read_b64_tr_b16 a[98:99], v232 offset:3072\n" \
"v_accvgpr_read_b32 v252, a141\n" \
"v_mfma_f32_16x16x32_bf16 v[72:75], a[84:87], a[4:7], v[72:75]\n" \
"s_waitcnt lgkmcnt(3)\n" \
"ds_read_b64_tr_b16 a[100:101], v232 offset:9216\n" \
"v_accvgpr_read_b32 v253, a142\n" \
"v_mfma_f32_16x16x32_bf16 v[76:79], a[88:91], a[0:3], v[76:79]\n" \
"ds_read_b64_tr_b16 a[102:103], v232 offset:11264\n" \
"v_accvgpr_read_b32 v254, a143\n" \
"v_mfma_f32_16x16x32_bf16 v[76:79], a[92:95], a[4:7], v[76:79]\n" \
"buffer_load_dwordx4 v225, s[48:51], s77 offen offset:1024 lds\n" \
"v_mfma_f32_16x16x32_bf16 v[108:111], a[88:91], a[8:11], v[108:111]\n" \
"ds_read_b64_tr_b16 a[104:105], v233 offset:1024\n" \
"v_mul_f32 v243, s31, v243\n" \
"v_mfma_f32_16x16x32_bf16 v[108:111], a[92:95], a[12:15], v[108:111]\n" \
"ds_read_b64_tr_b16 a[106:107], v233 offset:3072\n" \
"v_mul_f32 v252, s31, v252\n" \
"v_mfma_f32_16x16x32_bf16 v[104:107], a[80:83], a[8:11], v[104:107]\n" \
"s_waitcnt lgkmcnt(6)\n" \
"ds_read_b64_tr_b16 a[108:109], v233 offset:9216\n" \
"v_mul_f32 v253, s31, v253\n" \
"v_mfma_f32_16x16x32_bf16 v[104:107], a[84:87], a[12:15], v[104:107]\n" \
"buffer_load_dwordx4 v226, s[48:51], s77 offen offset:2048 lds\n" \
"v_mfma_f32_16x16x32_bf16 v[100:103], a[72:75], a[8:11], v[100:103]\n" \
"ds_read_b64_tr_b16 a[110:111], v233 offset:11264\n" \
"v_mul_f32 v254, s31, v254\n" \
"v_mfma_f32_16x16x32_bf16 v[100:103], a[76:79], a[12:15], v[100:103]\n" \
"ds_read_b64_tr_b16 a[112:113], v232 offset:1536\n" \
"v_cvt_pk_bf16_f32 v12, v243, v252\n" \
"v_mfma_f32_16x16x32_bf16 v[96:99], a[64:67], a[8:11], v[96:99]\n" \
"ds_read_b64_tr_b16 a[114:115], v232 offset:3584\n" \
"v_cvt_pk_bf16_f32 v13, v253, v254\n" \
"v_mfma_f32_16x16x32_bf16 v[96:99], a[68:71], a[12:15], v[96:99]\n" \
"buffer_load_dwordx4 v227, s[48:51], s77 offen offset:3072 lds\n" \
"v_mfma_f32_16x16x32_bf16 v[128:131], a[64:67], a[16:19], v[128:131]\n" \
"ds_read_b64_tr_b16 a[116:117], v232 offset:9728\n" \
"v_accvgpr_read_b32 v243, a156\n" \
"v_mfma_f32_16x16x32_bf16 v[128:131], a[68:71], a[20:23], v[128:131]\n" \
"ds_read_b64_tr_b16 a[118:119], v232 offset:11776\n" \
"v_accvgpr_read_b32 v252, a157\n" \
"v_mfma_f32_16x16x32_bf16 v[132:135], a[72:75], a[16:19], v[132:135]\n" \
"s_xor_b32 s77, s77, 0x300\n" \
"ds_read_b64_tr_b16 a[120:121], v233 offset:1536\n" \
"v_accvgpr_read_b32 v253, a158\n" \
"v_mfma_f32_16x16x32_bf16 v[132:135], a[76:79], a[20:23], v[132:135]\n" \
"v_accvgpr_read_b32 v254, a159\n" \
"v_mul_f32 v243, s31, v243\n" \
"v_mfma_f32_16x16x32_bf16 v[136:139], a[80:83], a[16:19], v[136:139]\n" \
"ds_read_b64_tr_b16 a[122:123], v233 offset:3584\n" \
"v_mul_f32 v252, s31, v252\n" \
"v_mfma_f32_16x16x32_bf16 v[136:139], a[84:87], a[20:23], v[136:139]\n" \
"ds_read_b64_tr_b16 a[124:125], v233 offset:9728\n" \
"v_mul_f32 v253, s31, v253\n" \
"v_mfma_f32_16x16x32_bf16 v[140:143], a[88:91], a[16:19], v[140:143]\n" \
"s_add_u32 s93, s87, 17858560\n" \
"ds_read_b64_tr_b16 a[126:127], v233 offset:11776\n" \
"v_mul_f32 v254, s31, v254\n" \
"v_mfma_f32_16x16x32_bf16 v[140:143], a[92:95], a[20:23], v[140:143]\n" \
"buffer_store_dwordx4 a[224:227], v234, s[56:59], s93 offen offset:0 sc1\n" \
"v_mfma_f32_16x16x32_bf16 v[172:175], a[88:91], a[24:27], v[172:175]\n" \
"v_cvt_pk_bf16_f32 v14, v243, v252\n" \
"v_cvt_pk_bf16_f32 v15, v253, v254\n" \
"v_mfma_f32_16x16x32_bf16 v[172:175], a[92:95], a[28:31], v[172:175]\n" \
"v_accvgpr_write_b32 a56, v244\n" \
"v_accvgpr_write_b32 a57, v245\n" \
"v_mfma_f32_16x16x32_bf16 v[168:171], a[80:83], a[24:27], v[168:171]\n" \
"v_accvgpr_write_b32 a58, v246\n" \
"v_accvgpr_write_b32 a59, v247\n" \
"v_mfma_f32_16x16x32_bf16 v[168:171], a[84:87], a[28:31], v[168:171]\n" \
"v_accvgpr_write_b32 a60, v248\n" \
"v_accvgpr_write_b32 a61, v249\n" \
"v_mfma_f32_16x16x32_bf16 v[164:167], a[72:75], a[24:27], v[164:167]\n" \
"v_accvgpr_write_b32 a48, 0\n" \
"v_accvgpr_write_b32 a49, 0\n" \
"v_mfma_f32_16x16x32_bf16 v[164:167], a[76:79], a[28:31], v[164:167]\n" \
"v_accvgpr_write_b32 a50, 0\n" \
"v_accvgpr_write_b32 a51, 0\n" \
"v_mfma_f32_16x16x32_bf16 v[160:163], a[64:67], a[24:27], v[160:163]\n" \
"v_accvgpr_write_b32 a52, 0\n" \
"v_accvgpr_write_b32 a53, 0\n" \
"v_mfma_f32_16x16x32_bf16 v[160:163], a[68:71], a[28:31], v[160:163]\n" \
"s_waitcnt vmcnt(35) lgkmcnt(0)\n" \
"s_barrier\n" \
"v_mfma_f32_16x16x32_bf16 v[80:83], a[96:99], a[0:3], v[80:83]\n" \
"ds_read_b64_tr_b16 a[64:65], v232 offset:16384\n" \
"v_mfma_f32_16x16x32_bf16 v[80:83], a[100:103], a[4:7], v[80:83]\n" \
"ds_read_b64_tr_b16 a[66:67], v232 offset:18432\n" \
"v_mfma_f32_16x16x32_bf16 v[84:87], a[104:107], a[0:3], v[84:87]\n" \
"s_add_u32 m0, s79, 0\n" \
"ds_read_b64_tr_b16 a[68:69], v232 offset:24576\n" \
"v_mfma_f32_16x16x32_bf16 v[84:87], a[108:111], a[4:7], v[84:87]\n" \
"buffer_load_dwordx4 v228, s[52:55], s78 offen offset:0 lds\n" \
"v_mfma_f32_16x16x32_bf16 v[88:91], a[112:115], a[0:3], v[88:91]\n" \
"ds_read_b64_tr_b16 a[70:71], v232 offset:26624\n" \
"v_mfma_f32_16x16x32_bf16 v[88:91], a[116:119], a[4:7], v[88:91]\n" \
"ds_read_b64_tr_b16 a[72:73], v233 offset:16384\n" \
"v_mfma_f32_16x16x32_bf16 v[92:95], a[120:123], a[0:3], v[92:95]\n" \
"ds_read_b64_tr_b16 a[74:75], v233 offset:18432\n" \
"v_mfma_f32_16x16x32_bf16 v[92:95], a[124:127], a[4:7], v[92:95]\n" \
"buffer_load_dwordx4 v229, s[52:55], s78 offen offset:1024 lds\n" \
"v_mfma_f32_16x16x32_bf16 v[124:127], a[120:123], a[8:11], v[124:127]\n" \
"s_waitcnt vmcnt(35)\n" \
"ds_read_b128 a[0:3], v250 offset:0\n" \
"v_mfma_f32_16x16x32_bf16 v[124:127], a[124:127], a[12:15], v[124:127]\n" \
"ds_read_b128 a[4:7], v251 offset:0\n" \
"v_mfma_f32_16x16x32_bf16 v[120:123], a[112:115], a[8:11], v[120:123]\n" \
"s_xor_b32 s78, s78, 0x80000\n" \
"ds_read_b64_tr_b16 a[76:77], v233 offset:24576\n" \
"v_mfma_f32_16x16x32_bf16 v[120:123], a[116:119], a[12:15], v[120:123]\n" \
"buffer_load_dwordx4 v228, s[52:55], s89 offen offset:2048 lds\n" \
"v_mfma_f32_16x16x32_bf16 v[116:119], a[104:107], a[8:11], v[116:119]\n" \
"ds_read_b64_tr_b16 a[78:79], v233 offset:26624\n" \
"v_mfma_f32_16x16x32_bf16 v[116:119], a[108:111], a[12:15], v[116:119]\n" \
"ds_read_b64_tr_b16 a[80:81], v232 offset:16896\n" \
"v_mfma_f32_16x16x32_bf16 v[112:115], a[96:99], a[8:11], v[112:115]\n" \
"ds_read_b64_tr_b16 a[82:83], v232 offset:18944\n" \
"v_mfma_f32_16x16x32_bf16 v[112:115], a[100:103], a[12:15], v[112:115]\n" \
"buffer_load_dwordx4 v229, s[52:55], s89 offen offset:3072 lds\n" \
"v_mfma_f32_16x16x32_bf16 v[144:147], a[96:99], a[16:19], v[144:147]\n" \
"s_waitcnt vmcnt(35)\n" \
"s_xor_b32 s89, s89, 0x80000\n" \
"ds_read_b128 a[8:11], v250 offset:2048\n" \
"v_mfma_f32_16x16x32_bf16 v[144:147], a[100:103], a[20:23], v[144:147]\n" \
"s_add_u32 m0, s74, 131072\n" \
"ds_read_b128 a[12:15], v251 offset:2048\n" \
"v_mfma_f32_16x16x32_bf16 v[148:151], a[104:107], a[16:19], v[148:151]\n" \
"s_waitcnt lgkmcnt(6)\n" \
"ds_read_b64_tr_b16 a[84:85], v232 offset:25088\n" \
"v_mfma_f32_16x16x32_bf16 v[148:151], a[108:111], a[20:23], v[148:151]\n" \
"buffer_load_dwordx4 v224, s[48:51], s75 offen offset:0 lds\n" \
"v_mfma_f32_16x16x32_bf16 v[152:155], a[112:115], a[16:19], v[152:155]\n" \
"ds_read_b64_tr_b16 a[86:87], v232 offset:27136\n" \
"v_mfma_f32_16x16x32_bf16 v[152:155], a[116:119], a[20:23], v[152:155]\n" \
"ds_read_b64_tr_b16 a[88:89], v233 offset:16896\n" \
"v_mfma_f32_16x16x32_bf16 v[156:159], a[120:123], a[16:19], v[156:159]\n" \
"ds_read_b64_tr_b16 a[90:91], v233 offset:18944\n" \
"v_mfma_f32_16x16x32_bf16 v[156:159], a[124:127], a[20:23], v[156:159]\n" \
"buffer_load_dwordx4 v225, s[48:51], s75 offen offset:1024 lds\n" \
"v_mfma_f32_16x16x32_bf16 v[188:191], a[120:123], a[24:27], v[188:191]\n" \
"s_waitcnt vmcnt(35)\n" \
"ds_read_b128 a[16:19], v250 offset:4096\n" \
"v_mfma_f32_16x16x32_bf16 v[188:191], a[124:127], a[28:31], v[188:191]\n" \
"ds_read_b128 a[20:23], v251 offset:4096\n" \
"v_mfma_f32_16x16x32_bf16 v[184:187], a[112:115], a[24:27], v[184:187]\n" \
"s_waitcnt lgkmcnt(6)\n" \
"ds_read_b64_tr_b16 a[92:93], v233 offset:25088\n" \
"v_mfma_f32_16x16x32_bf16 v[184:187], a[116:119], a[28:31], v[184:187]\n" \
"buffer_load_dwordx4 v226, s[48:51], s75 offen offset:2048 lds\n" \
"v_mfma_f32_16x16x32_bf16 v[180:183], a[104:107], a[24:27], v[180:183]\n" \
"ds_read_b64_tr_b16 a[94:95], v233 offset:27136\n" \
"v_mfma_f32_16x16x32_bf16 v[180:183], a[108:111], a[28:31], v[180:183]\n" \
"v_mfma_f32_16x16x32_bf16 v[176:179], a[96:99], a[24:27], v[176:179]\n" \
"v_mfma_f32_16x16x32_bf16 v[176:179], a[100:103], a[28:31], v[176:179]\n" \
"buffer_load_dwordx4 v227, s[48:51], s75 offen offset:3072 lds\n" \
"v_mfma_f32_16x16x32_bf16 v[64:67], a[64:67], a[0:3], v[64:67]\n" \
"s_waitcnt vmcnt(35)\n" \
"s_xor_b32 s75, s75, 0x80\n" \
"ds_read_b128 a[24:27], v250 offset:6144\n" \
"v_mfma_f32_16x16x32_bf16 v[64:67], a[68:71], a[4:7], v[64:67]\n" \
"s_add_u32 m0, s74, 135168\n" \
"ds_read_b128 a[28:31], v251 offset:6144\n" \
"v_mfma_f32_16x16x32_bf16 v[68:71], a[72:75], a[0:3], v[68:71]\n" \
"s_waitcnt lgkmcnt(2)\n" \
"ds_read_b64_tr_b16 a[96:97], v232 offset:17408\n" \
"v_mfma_f32_16x16x32_bf16 v[68:71], a[76:79], a[4:7], v[68:71]\n" \
"buffer_load_dwordx4 v224, s[48:51], s77 offen offset:0 lds\n" \
"v_mfma_f32_16x16x32_bf16 v[72:75], a[80:83], a[0:3], v[72:75]\n" \
"ds_read_b64_tr_b16 a[98:99], v232 offset:19456\n" \
"v_mfma_f32_16x16x32_bf16 v[72:75], a[84:87], a[4:7], v[72:75]\n" \
"ds_read_b64_tr_b16 a[100:101], v232 offset:25600\n" \
"v_mfma_f32_16x16x32_bf16 v[76:79], a[88:91], a[0:3], v[76:79]\n" \
"ds_read_b64_tr_b16 a[102:103], v232 offset:27648\n" \
"v_mfma_f32_16x16x32_bf16 v[76:79], a[92:95], a[4:7], v[76:79]\n" \
"buffer_load_dwordx4 v225, s[48:51], s77 offen offset:1024 lds\n" \
"v_mfma_f32_16x16x32_bf16 v[108:111], a[88:91], a[8:11], v[108:111]\n" \
"ds_read_b64_tr_b16 a[104:105], v233 offset:17408\n" \
"v_mfma_f32_16x16x32_bf16 v[108:111], a[92:95], a[12:15], v[108:111]\n" \
"ds_read_b64_tr_b16 a[106:107], v233 offset:19456\n" \
"v_mfma_f32_16x16x32_bf16 v[104:107], a[80:83], a[8:11], v[104:107]\n" \
"s_waitcnt lgkmcnt(6)\n" \
"ds_read_b64_tr_b16 a[108:109], v233 offset:25600\n" \
"v_mfma_f32_16x16x32_bf16 v[104:107], a[84:87], a[12:15], v[104:107]\n" \
"buffer_load_dwordx4 v226, s[48:51], s77 offen offset:2048 lds\n" \
"v_mfma_f32_16x16x32_bf16 v[100:103], a[72:75], a[8:11], v[100:103]\n" \
"ds_read_b64_tr_b16 a[110:111], v233 offset:27648\n" \
"v_mfma_f32_16x16x32_bf16 v[100:103], a[76:79], a[12:15], v[100:103]\n" \
"ds_read_b64_tr_b16 a[112:113], v232 offset:17920\n" \
"v_mfma_f32_16x16x32_bf16 v[96:99], a[64:67], a[8:11], v[96:99]\n" \
"ds_read_b64_tr_b16 a[114:115], v232 offset:19968\n" \
"Lpg3897_%=:\n" \
"s_cbranch_execz Lpg6052_%=\n" \
"v_mfma_f32_16x16x32_bf16 v[96:99], a[68:71], a[12:15], v[96:99]\n" \
"buffer_load_dwordx4 v227, s[48:51], s77 offen offset:3072 lds\n" \
"v_mfma_f32_16x16x32_bf16 v[128:131], a[64:67], a[16:19], v[128:131]\n" \
"ds_read_b64_tr_b16 a[116:117], v232 offset:26112\n" \
"v_mfma_f32_16x16x32_bf16 v[128:131], a[68:71], a[20:23], v[128:131]\n" \
"ds_read_b64_tr_b16 a[118:119], v232 offset:28160\n" \
"v_mfma_f32_16x16x32_bf16 v[132:135], a[72:75], a[16:19], v[132:135]\n" \
"s_xor_b32 s77, s77, 0x80\n" \
"ds_read_b64_tr_b16 a[120:121], v233 offset:17920\n" \
"v_mfma_f32_16x16x32_bf16 v[132:135], a[76:79], a[20:23], v[132:135]\n" \
"v_mfma_f32_16x16x32_bf16 v[136:139], a[80:83], a[16:19], v[136:139]\n" \
"ds_read_b64_tr_b16 a[122:123], v233 offset:19968\n" \
"v_mfma_f32_16x16x32_bf16 v[136:139], a[84:87], a[20:23], v[136:139]\n" \
"ds_read_b64_tr_b16 a[124:125], v233 offset:26112\n" \
"v_mfma_f32_16x16x32_bf16 v[140:143], a[88:91], a[16:19], v[140:143]\n" \
"ds_read_b64_tr_b16 a[126:127], v233 offset:28160\n" \
"v_mfma_f32_16x16x32_bf16 v[140:143], a[92:95], a[20:23], v[140:143]\n" \
"buffer_store_dwordx4 a[228:231], v234, s[56:59], s93 offen offset:1024 sc1\n" \
"v_mfma_f32_16x16x32_bf16 v[172:175], a[88:91], a[24:27], v[172:175]\n" \
"v_mfma_f32_16x16x32_bf16 v[172:175], a[92:95], a[28:31], v[172:175]\n" \
"v_mfma_f32_16x16x32_bf16 v[168:171], a[80:83], a[24:27], v[168:171]\n" \
"v_mfma_f32_16x16x32_bf16 v[168:171], a[84:87], a[28:31], v[168:171]\n" \
"v_mfma_f32_16x16x32_bf16 v[164:167], a[72:75], a[24:27], v[164:167]\n" \
"v_mfma_f32_16x16x32_bf16 v[164:167], a[76:79], a[28:31], v[164:167]\n" \
"v_mfma_f32_16x16x32_bf16 v[160:163], a[64:67], a[24:27], v[160:163]\n" \
"v_mfma_f32_16x16x32_bf16 v[160:163], a[68:71], a[28:31], v[160:163]\n" \
"s_waitcnt vmcnt(35) lgkmcnt(0)\n" \
"s_barrier\n" \
"v_mfma_f32_16x16x32_bf16 v[80:83], a[96:99], a[0:3], v[80:83]\n" \
"ds_read_b64_tr_b16 a[64:65], v232 offset:32768\n" \
"v_mfma_f32_16x16x32_bf16 v[80:83], a[100:103], a[4:7], v[80:83]\n" \
"ds_read_b64_tr_b16 a[66:67], v232 offset:34816\n" \
"v_mfma_f32_16x16x32_bf16 v[84:87], a[104:107], a[0:3], v[84:87]\n" \
"s_add_u32 m0, s79, 16384\n" \
"ds_read_b64_tr_b16 a[68:69], v232 offset:40960\n" \
"v_mfma_f32_16x16x32_bf16 v[84:87], a[108:111], a[4:7], v[84:87]\n" \
"buffer_load_dwordx4 v228, s[52:55], s78 offen offset:0 lds\n" \
"v_mfma_f32_16x16x32_bf16 v[88:91], a[112:115], a[0:3], v[88:91]\n" \
"ds_read_b64_tr_b16 a[70:71], v232 offset:43008\n" \
"v_mfma_f32_16x16x32_bf16 v[88:91], a[116:119], a[4:7], v[88:91]\n" \
"ds_read_b64_tr_b16 a[72:73], v233 offset:32768\n" \
"v_mfma_f32_16x16x32_bf16 v[92:95], a[120:123], a[0:3], v[92:95]\n" \
"ds_read_b64_tr_b16 a[74:75], v233 offset:34816\n" \
"v_mfma_f32_16x16x32_bf16 v[92:95], a[124:127], a[4:7], v[92:95]\n" \
"buffer_load_dwordx4 v229, s[52:55], s78 offen offset:1024 lds\n" \
"v_mfma_f32_16x16x32_bf16 v[124:127], a[120:123], a[8:11], v[124:127]\n" \
"s_waitcnt vmcnt(35)\n" \
"ds_read_b128 a[0:3], v230 offset:0\n" \
"v_mfma_f32_16x16x32_bf16 v[124:127], a[124:127], a[12:15], v[124:127]\n" \
"ds_read_b128 a[4:7], v231 offset:0\n" \
"v_mfma_f32_16x16x32_bf16 v[120:123], a[112:115], a[8:11], v[120:123]\n" \
"s_xor_b32 s78, s78, 0x180000\n" \
"ds_read_b64_tr_b16 a[76:77], v233 offset:40960\n" \
"v_mfma_f32_16x16x32_bf16 v[120:123], a[116:119], a[12:15], v[120:123]\n" \
"buffer_load_dwordx4 v228, s[52:55], s89 offen offset:2048 lds\n" \
"v_mfma_f32_16x16x32_bf16 v[116:119], a[104:107], a[8:11], v[116:119]\n" \
"ds_read_b64_tr_b16 a[78:79], v233 offset:43008\n" \
"v_mfma_f32_16x16x32_bf16 v[116:119], a[108:111], a[12:15], v[116:119]\n" \
"ds_read_b64_tr_b16 a[80:81], v232 offset:33280\n" \
"v_mfma_f32_16x16x32_bf16 v[112:115], a[96:99], a[8:11], v[112:115]\n" \
"ds_read_b64_tr_b16 a[82:83], v232 offset:35328\n" \
"v_mfma_f32_16x16x32_bf16 v[112:115], a[100:103], a[12:15], v[112:115]\n" \
"buffer_load_dwordx4 v229, s[52:55], s89 offen offset:3072 lds\n" \
"v_mfma_f32_16x16x32_bf16 v[144:147], a[96:99], a[16:19], v[144:147]\n" \
"s_waitcnt vmcnt(35)\n" \
"s_xor_b32 s89, s89, 0x180000\n" \
"ds_read_b128 a[8:11], v230 offset:2048\n" \
"v_mfma_f32_16x16x32_bf16 v[144:147], a[100:103], a[20:23], v[144:147]\n" \
"s_add_u32 m0, s74, 65536\n" \
"ds_read_b128 a[12:15], v231 offset:2048\n" \
"v_mfma_f32_16x16x32_bf16 v[148:151], a[104:107], a[16:19], v[148:151]\n" \
"s_waitcnt lgkmcnt(6)\n" \
"ds_read_b64_tr_b16 a[84:85], v232 offset:41472\n" \
"v_mfma_f32_16x16x32_bf16 v[148:151], a[108:111], a[20:23], v[148:151]\n" \
"buffer_load_dwordx4 v224, s[48:51], s75 offen offset:0 lds\n" \
"v_mfma_f32_16x16x32_bf16 v[152:155], a[112:115], a[16:19], v[152:155]\n" \
"ds_read_b64_tr_b16 a[86:87], v232 offset:43520\n" \
"v_mfma_f32_16x16x32_bf16 v[152:155], a[116:119], a[20:23], v[152:155]\n" \
"ds_read_b64_tr_b16 a[88:89], v233 offset:33280\n" \
"v_mfma_f32_16x16x32_bf16 v[156:159], a[120:123], a[16:19], v[156:159]\n" \
"ds_read_b64_tr_b16 a[90:91], v233 offset:35328\n" \
"v_mfma_f32_16x16x32_bf16 v[156:159], a[124:127], a[20:23], v[156:159]\n" \
"buffer_load_dwordx4 v225, s[48:51], s75 offen offset:1024 lds\n" \
"v_mfma_f32_16x16x32_bf16 v[188:191], a[120:123], a[24:27], v[188:191]\n" \
"s_waitcnt vmcnt(35)\n" \
"ds_read_b128 a[16:19], v230 offset:4096\n" \
"v_mfma_f32_16x16x32_bf16 v[188:191], a[124:127], a[28:31], v[188:191]\n" \
"ds_read_b128 a[20:23], v231 offset:4096\n" \
"v_mfma_f32_16x16x32_bf16 v[184:187], a[112:115], a[24:27], v[184:187]\n" \
"s_waitcnt lgkmcnt(6)\n" \
"ds_read_b64_tr_b16 a[92:93], v233 offset:41472\n" \
"v_mfma_f32_16x16x32_bf16 v[184:187], a[116:119], a[28:31], v[184:187]\n" \
"buffer_load_dwordx4 v226, s[48:51], s75 offen offset:2048 lds\n" \
"v_mfma_f32_16x16x32_bf16 v[180:183], a[104:107], a[24:27], v[180:183]\n" \
"ds_read_b64_tr_b16 a[94:95], v233 offset:43520\n" \
"v_mfma_f32_16x16x32_bf16 v[180:183], a[108:111], a[28:31], v[180:183]\n" \
"v_mfma_f32_16x16x32_bf16 v[176:179], a[96:99], a[24:27], v[176:179]\n" \
"v_mfma_f32_16x16x32_bf16 v[176:179], a[100:103], a[28:31], v[176:179]\n" \
"buffer_load_dwordx4 v227, s[48:51], s75 offen offset:3072 lds\n" \
"v_mfma_f32_16x16x32_bf16 v[64:67], a[64:67], a[0:3], v[64:67]\n" \
"s_waitcnt vmcnt(35)\n" \
"s_xor_b32 s75, s75, 0x180\n" \
"ds_read_b128 a[24:27], v230 offset:6144\n" \
"v_mfma_f32_16x16x32_bf16 v[64:67], a[68:71], a[4:7], v[64:67]\n" \
"s_add_u32 m0, s74, 69632\n" \
"ds_read_b128 a[28:31], v231 offset:6144\n" \
"v_mfma_f32_16x16x32_bf16 v[68:71], a[72:75], a[0:3], v[68:71]\n" \
"s_waitcnt lgkmcnt(2)\n" \
"ds_read_b64_tr_b16 a[96:97], v232 offset:33792\n" \
"v_mfma_f32_16x16x32_bf16 v[68:71], a[76:79], a[4:7], v[68:71]\n" \
"buffer_load_dwordx4 v224, s[48:51], s77 offen offset:0 lds\n" \
"v_mfma_f32_16x16x32_bf16 v[72:75], a[80:83], a[0:3], v[72:75]\n" \
"ds_read_b64_tr_b16 a[98:99], v232 offset:35840\n" \
"v_mfma_f32_16x16x32_bf16 v[72:75], a[84:87], a[4:7], v[72:75]\n" \
"ds_read_b64_tr_b16 a[100:101], v232 offset:41984\n" \
"v_mfma_f32_16x16x32_bf16 v[76:79], a[88:91], a[0:3], v[76:79]\n" \
"ds_read_b64_tr_b16 a[102:103], v232 offset:44032\n" \
"v_mfma_f32_16x16x32_bf16 v[76:79], a[92:95], a[4:7], v[76:79]\n" \
"buffer_load_dwordx4 v225, s[48:51], s77 offen offset:1024 lds\n" \
"v_mfma_f32_16x16x32_bf16 v[108:111], a[88:91], a[8:11], v[108:111]\n" \
"ds_read_b64_tr_b16 a[104:105], v233 offset:33792\n" \
"v_mfma_f32_16x16x32_bf16 v[108:111], a[92:95], a[12:15], v[108:111]\n" \
"ds_read_b64_tr_b16 a[106:107], v233 offset:35840\n" \
"v_mfma_f32_16x16x32_bf16 v[104:107], a[80:83], a[8:11], v[104:107]\n" \
"s_waitcnt lgkmcnt(6)\n" \
"ds_read_b64_tr_b16 a[108:109], v233 offset:41984\n" \
"v_mfma_f32_16x16x32_bf16 v[104:107], a[84:87], a[12:15], v[104:107]\n" \
"buffer_load_dwordx4 v226, s[48:51], s77 offen offset:2048 lds\n" \
"v_mfma_f32_16x16x32_bf16 v[100:103], a[72:75], a[8:11], v[100:103]\n" \
"ds_read_b64_tr_b16 a[110:111], v233 offset:44032\n" \
"v_mfma_f32_16x16x32_bf16 v[100:103], a[76:79], a[12:15], v[100:103]\n" \
"ds_read_b64_tr_b16 a[112:113], v232 offset:34304\n" \
"v_mfma_f32_16x16x32_bf16 v[96:99], a[64:67], a[8:11], v[96:99]\n" \
"ds_read_b64_tr_b16 a[114:115], v232 offset:36352\n" \
"v_mfma_f32_16x16x32_bf16 v[96:99], a[68:71], a[12:15], v[96:99]\n" \
"buffer_load_dwordx4 v227, s[48:51], s77 offen offset:3072 lds\n" \
"v_mfma_f32_16x16x32_bf16 v[128:131], a[64:67], a[16:19], v[128:131]\n" \
"ds_read_b64_tr_b16 a[116:117], v232 offset:42496\n" \
"v_mfma_f32_16x16x32_bf16 v[128:131], a[68:71], a[20:23], v[128:131]\n" \
"ds_read_b64_tr_b16 a[118:119], v232 offset:44544\n" \
"v_mfma_f32_16x16x32_bf16 v[132:135], a[72:75], a[16:19], v[132:135]\n" \
"s_xor_b32 s77, s77, 0x180\n" \
"ds_read_b64_tr_b16 a[120:121], v233 offset:34304\n" \
"v_mfma_f32_16x16x32_bf16 v[132:135], a[76:79], a[20:23], v[132:135]\n" \
"buffer_store_dwordx4 a[232:235], v234, s[56:59], s93 offen offset:2048 sc1\n" \
"v_mfma_f32_16x16x32_bf16 v[136:139], a[80:83], a[16:19], v[136:139]\n" \
"ds_read_b64_tr_b16 a[122:123], v233 offset:36352\n" \
"v_mfma_f32_16x16x32_bf16 v[136:139], a[84:87], a[20:23], v[136:139]\n" \
"ds_read_b64_tr_b16 a[124:125], v233 offset:42496\n" \
"v_mfma_f32_16x16x32_bf16 v[140:143], a[88:91], a[16:19], v[140:143]\n" \
"ds_read_b64_tr_b16 a[126:127], v233 offset:44544\n" \
"v_mfma_f32_16x16x32_bf16 v[140:143], a[92:95], a[20:23], v[140:143]\n" \
"v_mfma_f32_16x16x32_bf16 v[172:175], a[88:91], a[24:27], v[172:175]\n" \
"v_mfma_f32_16x16x32_bf16 v[172:175], a[92:95], a[28:31], v[172:175]\n" \
"v_mfma_f32_16x16x32_bf16 v[168:171], a[80:83], a[24:27], v[168:171]\n" \
"v_mfma_f32_16x16x32_bf16 v[168:171], a[84:87], a[28:31], v[168:171]\n" \
"buffer_store_dwordx4 a[236:239], v234, s[56:59], s93 offen offset:3072 sc1\n" \
"v_mfma_f32_16x16x32_bf16 v[164:167], a[72:75], a[24:27], v[164:167]\n" \
"v_mfma_f32_16x16x32_bf16 v[164:167], a[76:79], a[28:31], v[164:167]\n" \
"v_mfma_f32_16x16x32_bf16 v[160:163], a[64:67], a[24:27], v[160:163]\n" \
"v_mfma_f32_16x16x32_bf16 v[160:163], a[68:71], a[28:31], v[160:163]\n" \
"s_waitcnt vmcnt(36) lgkmcnt(0)\n" \
"s_barrier\n" \
"v_mfma_f32_16x16x32_bf16 v[80:83], a[96:99], a[0:3], v[80:83]\n" \
"ds_read_b64_tr_b16 a[64:65], v232 offset:49152\n" \
"v_mfma_f32_16x16x32_bf16 v[80:83], a[100:103], a[4:7], v[80:83]\n" \
"ds_read_b64_tr_b16 a[66:67], v232 offset:51200\n" \
"v_mfma_f32_16x16x32_bf16 v[84:87], a[104:107], a[0:3], v[84:87]\n" \
"s_add_u32 m0, s79, 32768\n" \
"ds_read_b64_tr_b16 a[68:69], v232 offset:57344\n" \
"v_mfma_f32_16x16x32_bf16 v[84:87], a[108:111], a[4:7], v[84:87]\n" \
"buffer_load_dwordx4 v228, s[52:55], s78 offen offset:0 lds\n" \
"v_mfma_f32_16x16x32_bf16 v[88:91], a[112:115], a[0:3], v[88:91]\n" \
"ds_read_b64_tr_b16 a[70:71], v232 offset:59392\n" \
"v_mfma_f32_16x16x32_bf16 v[88:91], a[116:119], a[4:7], v[88:91]\n" \
"ds_read_b64_tr_b16 a[72:73], v233 offset:49152\n" \
"v_mfma_f32_16x16x32_bf16 v[92:95], a[120:123], a[0:3], v[92:95]\n" \
"ds_read_b64_tr_b16 a[74:75], v233 offset:51200\n" \
"v_mfma_f32_16x16x32_bf16 v[92:95], a[124:127], a[4:7], v[92:95]\n" \
"buffer_load_dwordx4 v229, s[52:55], s78 offen offset:1024 lds\n" \
"v_mfma_f32_16x16x32_bf16 v[124:127], a[120:123], a[8:11], v[124:127]\n" \
"s_waitcnt vmcnt(36)\n" \
"ds_read_b128 a[0:3], v230 offset:32768\n" \
"v_mfma_f32_16x16x32_bf16 v[124:127], a[124:127], a[12:15], v[124:127]\n" \
"ds_read_b128 a[4:7], v231 offset:32768\n" \
"v_mfma_f32_16x16x32_bf16 v[120:123], a[112:115], a[8:11], v[120:123]\n" \
"s_xor_b32 s78, s78, 0x80000\n" \
"ds_read_b64_tr_b16 a[76:77], v233 offset:57344\n" \
"v_mfma_f32_16x16x32_bf16 v[120:123], a[116:119], a[12:15], v[120:123]\n" \
"buffer_load_dwordx4 v228, s[52:55], s89 offen offset:2048 lds\n" \
"v_mfma_f32_16x16x32_bf16 v[116:119], a[104:107], a[8:11], v[116:119]\n" \
"ds_read_b64_tr_b16 a[78:79], v233 offset:59392\n" \
"v_mfma_f32_16x16x32_bf16 v[116:119], a[108:111], a[12:15], v[116:119]\n" \
"ds_read_b64_tr_b16 a[80:81], v232 offset:49664\n" \
"v_mfma_f32_16x16x32_bf16 v[112:115], a[96:99], a[8:11], v[112:115]\n" \
"ds_read_b64_tr_b16 a[82:83], v232 offset:51712\n" \
"v_mfma_f32_16x16x32_bf16 v[112:115], a[100:103], a[12:15], v[112:115]\n" \
"buffer_load_dwordx4 v229, s[52:55], s89 offen offset:3072 lds\n" \
"v_mfma_f32_16x16x32_bf16 v[144:147], a[96:99], a[16:19], v[144:147]\n" \
"s_waitcnt vmcnt(36)\n" \
"s_xor_b32 s89, s89, 0x80000\n" \
"ds_read_b128 a[8:11], v230 offset:34816\n" \
"v_mfma_f32_16x16x32_bf16 v[144:147], a[100:103], a[20:23], v[144:147]\n" \
"s_add_u32 m0, s74, 98304\n" \
"ds_read_b128 a[12:15], v231 offset:34816\n" \
"v_mfma_f32_16x16x32_bf16 v[148:151], a[104:107], a[16:19], v[148:151]\n" \
"s_waitcnt lgkmcnt(6)\n" \
"ds_read_b64_tr_b16 a[84:85], v232 offset:57856\n" \
"v_mfma_f32_16x16x32_bf16 v[148:151], a[108:111], a[20:23], v[148:151]\n" \
"buffer_load_dwordx4 v224, s[48:51], s75 offen offset:0 lds\n" \
"v_mfma_f32_16x16x32_bf16 v[152:155], a[112:115], a[16:19], v[152:155]\n" \
"ds_read_b64_tr_b16 a[86:87], v232 offset:59904\n" \
"v_mfma_f32_16x16x32_bf16 v[152:155], a[116:119], a[20:23], v[152:155]\n" \
"ds_read_b64_tr_b16 a[88:89], v233 offset:49664\n" \
"v_mfma_f32_16x16x32_bf16 v[156:159], a[120:123], a[16:19], v[156:159]\n" \
"ds_read_b64_tr_b16 a[90:91], v233 offset:51712\n" \
"v_mfma_f32_16x16x32_bf16 v[156:159], a[124:127], a[20:23], v[156:159]\n" \
"buffer_load_dwordx4 v225, s[48:51], s75 offen offset:1024 lds\n" \
"v_mfma_f32_16x16x32_bf16 v[188:191], a[120:123], a[24:27], v[188:191]\n" \
"s_waitcnt vmcnt(36)\n" \
"ds_read_b128 a[16:19], v230 offset:36864\n" \
"v_mfma_f32_16x16x32_bf16 v[188:191], a[124:127], a[28:31], v[188:191]\n" \
"ds_read_b128 a[20:23], v231 offset:36864\n" \
"v_mfma_f32_16x16x32_bf16 v[184:187], a[112:115], a[24:27], v[184:187]\n" \
"s_waitcnt lgkmcnt(6)\n" \
"ds_read_b64_tr_b16 a[92:93], v233 offset:57856\n" \
"v_mfma_f32_16x16x32_bf16 v[184:187], a[116:119], a[28:31], v[184:187]\n" \
"buffer_load_dwordx4 v226, s[48:51], s75 offen offset:2048 lds\n" \
"v_mfma_f32_16x16x32_bf16 v[180:183], a[104:107], a[24:27], v[180:183]\n" \
"ds_read_b64_tr_b16 a[94:95], v233 offset:59904\n" \
"v_mfma_f32_16x16x32_bf16 v[180:183], a[108:111], a[28:31], v[180:183]\n" \
"v_mfma_f32_16x16x32_bf16 v[176:179], a[96:99], a[24:27], v[176:179]\n" \
"v_mfma_f32_16x16x32_bf16 v[176:179], a[100:103], a[28:31], v[176:179]\n" \
"buffer_load_dwordx4 v227, s[48:51], s75 offen offset:3072 lds\n" \
"v_mfma_f32_16x16x32_bf16 v[64:67], a[64:67], a[0:3], v[64:67]\n" \
"s_waitcnt vmcnt(36)\n" \
"s_xor_b32 s75, s75, 0x80\n" \
"ds_read_b128 a[24:27], v230 offset:38912\n" \
"v_mfma_f32_16x16x32_bf16 v[64:67], a[68:71], a[4:7], v[64:67]\n" \
"s_add_u32 m0, s74, 102400\n" \
"ds_read_b128 a[28:31], v231 offset:38912\n" \
"v_mfma_f32_16x16x32_bf16 v[68:71], a[72:75], a[0:3], v[68:71]\n" \
"s_waitcnt lgkmcnt(2)\n" \
"ds_read_b64_tr_b16 a[96:97], v232 offset:50176\n" \
"v_mfma_f32_16x16x32_bf16 v[68:71], a[76:79], a[4:7], v[68:71]\n" \
"buffer_load_dwordx4 v224, s[48:51], s77 offen offset:0 lds\n" \
"v_mfma_f32_16x16x32_bf16 v[72:75], a[80:83], a[0:3], v[72:75]\n" \
"ds_read_b64_tr_b16 a[98:99], v232 offset:52224\n" \
"v_mfma_f32_16x16x32_bf16 v[72:75], a[84:87], a[4:7], v[72:75]\n" \
"ds_read_b64_tr_b16 a[100:101], v232 offset:58368\n" \
"v_mfma_f32_16x16x32_bf16 v[76:79], a[88:91], a[0:3], v[76:79]\n" \
"ds_read_b64_tr_b16 a[102:103], v232 offset:60416\n" \
"v_mfma_f32_16x16x32_bf16 v[76:79], a[92:95], a[4:7], v[76:79]\n" \
"buffer_load_dwordx4 v225, s[48:51], s77 offen offset:1024 lds\n" \
"v_mfma_f32_16x16x32_bf16 v[108:111], a[88:91], a[8:11], v[108:111]\n" \
"ds_read_b64_tr_b16 a[104:105], v233 offset:50176\n" \
"v_mfma_f32_16x16x32_bf16 v[108:111], a[92:95], a[12:15], v[108:111]\n" \
"ds_read_b64_tr_b16 a[106:107], v233 offset:52224\n" \
"v_mfma_f32_16x16x32_bf16 v[104:107], a[80:83], a[8:11], v[104:107]\n" \
"s_waitcnt lgkmcnt(6)\n" \
"ds_read_b64_tr_b16 a[108:109], v233 offset:58368\n" \
"v_mfma_f32_16x16x32_bf16 v[104:107], a[84:87], a[12:15], v[104:107]\n" \
"buffer_load_dwordx4 v226, s[48:51], s77 offen offset:2048 lds\n" \
"v_mfma_f32_16x16x32_bf16 v[100:103], a[72:75], a[8:11], v[100:103]\n" \
"ds_read_b64_tr_b16 a[110:111], v233 offset:60416\n" \
"v_mfma_f32_16x16x32_bf16 v[100:103], a[76:79], a[12:15], v[100:103]\n" \
"ds_read_b64_tr_b16 a[112:113], v232 offset:50688\n" \
"v_mfma_f32_16x16x32_bf16 v[96:99], a[64:67], a[8:11], v[96:99]\n" \
"ds_read_b64_tr_b16 a[114:115], v232 offset:52736\n" \
"v_mfma_f32_16x16x32_bf16 v[96:99], a[68:71], a[12:15], v[96:99]\n" \
"buffer_load_dwordx4 v227, s[48:51], s77 offen offset:3072 lds\n" \
"v_mfma_f32_16x16x32_bf16 v[128:131], a[64:67], a[16:19], v[128:131]\n" \
"ds_read_b64_tr_b16 a[116:117], v232 offset:58880\n" \
"v_mfma_f32_16x16x32_bf16 v[128:131], a[68:71], a[20:23], v[128:131]\n" \
"ds_read_b64_tr_b16 a[118:119], v232 offset:60928\n" \
"v_mfma_f32_16x16x32_bf16 v[132:135], a[72:75], a[16:19], v[132:135]\n" \
"s_xor_b32 s77, s77, 0x80\n" \
"s_add_u32 s93, s87, 17850368\n" \
"ds_read_b64_tr_b16 a[120:121], v233 offset:50688\n" \
"v_mfma_f32_16x16x32_bf16 v[132:135], a[76:79], a[20:23], v[132:135]\n" \
"buffer_store_dwordx4 a[192:195], v234, s[56:59], s93 offen offset:0 sc1\n" \
"v_mfma_f32_16x16x32_bf16 v[136:139], a[80:83], a[16:19], v[136:139]\n" \
"ds_read_b64_tr_b16 a[122:123], v233 offset:52736\n" \
"v_mfma_f32_16x16x32_bf16 v[136:139], a[84:87], a[20:23], v[136:139]\n" \
"ds_read_b64_tr_b16 a[124:125], v233 offset:58880\n" \
"v_mfma_f32_16x16x32_bf16 v[140:143], a[88:91], a[16:19], v[140:143]\n" \
"ds_read_b64_tr_b16 a[126:127], v233 offset:60928\n" \
"v_mfma_f32_16x16x32_bf16 v[140:143], a[92:95], a[20:23], v[140:143]\n" \
"v_mfma_f32_16x16x32_bf16 v[172:175], a[88:91], a[24:27], v[172:175]\n" \
"v_mfma_f32_16x16x32_bf16 v[172:175], a[92:95], a[28:31], v[172:175]\n" \
"v_mfma_f32_16x16x32_bf16 v[168:171], a[80:83], a[24:27], v[168:171]\n" \
"v_mfma_f32_16x16x32_bf16 v[168:171], a[84:87], a[28:31], v[168:171]\n" \
"buffer_store_dwordx4 a[196:199], v234, s[56:59], s93 offen offset:1024 sc1\n" \
"v_mfma_f32_16x16x32_bf16 v[164:167], a[72:75], a[24:27], v[164:167]\n" \
"v_mfma_f32_16x16x32_bf16 v[164:167], a[76:79], a[28:31], v[164:167]\n" \
"v_mfma_f32_16x16x32_bf16 v[160:163], a[64:67], a[24:27], v[160:163]\n" \
"v_mfma_f32_16x16x32_bf16 v[160:163], a[68:71], a[28:31], v[160:163]\n" \
"s_waitcnt vmcnt(37) lgkmcnt(0)\n" \
"s_barrier\n" \
"v_mfma_f32_16x16x32_bf16 v[80:83], a[96:99], a[0:3], v[80:83]\n" \
"ds_read_b64_tr_b16 a[64:65], v232 offset:0\n" \
"v_mfma_f32_16x16x32_bf16 v[80:83], a[100:103], a[4:7], v[80:83]\n" \
"ds_read_b64_tr_b16 a[66:67], v232 offset:2048\n" \
"v_mfma_f32_16x16x32_bf16 v[84:87], a[104:107], a[0:3], v[84:87]\n" \
"s_add_u32 m0, s79, 49152\n" \
"ds_read_b64_tr_b16 a[68:69], v232 offset:8192\n" \
"v_mfma_f32_16x16x32_bf16 v[84:87], a[108:111], a[4:7], v[84:87]\n" \
"buffer_load_dwordx4 v228, s[52:55], s78 offen offset:0 lds\n" \
"v_mfma_f32_16x16x32_bf16 v[88:91], a[112:115], a[0:3], v[88:91]\n" \
"s_mul_i32 s73, s62, 557824\n" \
"s_add_u32 s85, s73, 17850368\n" \
"ds_read_b64_tr_b16 a[70:71], v232 offset:10240\n" \
"v_mfma_f32_16x16x32_bf16 v[88:91], a[116:119], a[4:7], v[88:91]\n" \
"s_mul_i32 s83, s63, 34816\n" \
"s_sub_u32 s86, 557056, s83\n" \
"ds_read_b64_tr_b16 a[72:73], v233 offset:0\n" \
"v_mfma_f32_16x16x32_bf16 v[92:95], a[120:123], a[0:3], v[92:95]\n" \
"ds_read_b64_tr_b16 a[74:75], v233 offset:2048\n" \
"v_add_u32 v237, %3, v236\n" \
"v_mfma_f32_16x16x32_bf16 v[92:95], a[124:127], a[4:7], v[92:95]\n" \
"buffer_load_dwordx4 v229, s[52:55], s78 offen offset:1024 lds\n" \
"v_mfma_f32_16x16x32_bf16 v[124:127], a[120:123], a[8:11], v[124:127]\n" \
"s_waitcnt vmcnt(37)\n" \
"ds_read_b128 a[0:3], v250 offset:0\n" \
"v_mfma_f32_16x16x32_bf16 v[124:127], a[124:127], a[12:15], v[124:127]\n" \
"s_add_u32 s36, s56, s73\n" \
"s_addc_u32 s37, s57, 0\n" \
"ds_read_b128 a[4:7], v251 offset:0\n" \
"v_mfma_f32_16x16x32_bf16 v[120:123], a[112:115], a[8:11], v[120:123]\n" \
"s_mov_b32 s38, s86\n" \
"s_mov_b32 s39, 0x20000\n" \
"ds_read_b64_tr_b16 a[76:77], v233 offset:8192\n" \
"v_mfma_f32_16x16x32_bf16 v[120:123], a[116:119], a[12:15], v[120:123]\n" \
"buffer_load_dwordx4 v228, s[52:55], s89 offen offset:2048 lds\n" \
"v_mfma_f32_16x16x32_bf16 v[116:119], a[104:107], a[8:11], v[116:119]\n" \
"ds_read_b64_tr_b16 a[78:79], v233 offset:10240\n" \
"v_add_u32 v235, %3, v234\n" \
"v_mfma_f32_16x16x32_bf16 v[116:119], a[108:111], a[12:15], v[116:119]\n" \
"s_add_u32 s40, s56, s85\n" \
"s_addc_u32 s41, s57, 0\n" \
"ds_read_b64_tr_b16 a[80:81], v232 offset:512\n" \
"v_mfma_f32_16x16x32_bf16 v[112:115], a[96:99], a[8:11], v[112:115]\n" \
"s_mov_b32 s42, s86\n" \
"s_mov_b32 s43, 0x20000\n" \
"ds_read_b64_tr_b16 a[82:83], v232 offset:2560\n" \
"v_mfma_f32_16x16x32_bf16 v[112:115], a[100:103], a[12:15], v[112:115]\n" \
"buffer_load_dwordx4 v229, s[52:55], s89 offen offset:3072 lds\n" \
"v_mfma_f32_16x16x32_bf16 v[144:147], a[96:99], a[16:19], v[144:147]\n" \
"s_waitcnt vmcnt(37)\n" \
"s_add_u32 s85, s74, 131072\n" \
"ds_read_b128 a[8:11], v250 offset:2048\n" \
"v_mfma_f32_16x16x32_bf16 v[144:147], a[100:103], a[20:23], v[144:147]\n" \
"s_add_u32 m0, s74, 131072\n" \
"ds_read_b128 a[12:15], v251 offset:2048\n" \
"v_add_u32 v239, s85, v234\n" \
"v_mfma_f32_16x16x32_bf16 v[148:151], a[104:107], a[16:19], v[148:151]\n" \
"s_waitcnt lgkmcnt(6)\n" \
"s_lshl_b32 s73, s61, 12\n" \
"ds_read_b64_tr_b16 a[84:85], v232 offset:8704\n" \
"v_mfma_f32_16x16x32_bf16 v[148:151], a[108:111], a[20:23], v[148:151]\n" \
"buffer_load_dwordx4 v224, s[48:51], s75 offen offset:0 lds\n" \
"v_mfma_f32_16x16x32_bf16 v[152:155], a[112:115], a[16:19], v[152:155]\n" \
"s_add_u32 s80, s79, 0x10000\n" \
"s_add_u32 s83, s83, s73\n" \
"ds_read_b64_tr_b16 a[86:87], v232 offset:10752\n" \
"v_mfma_f32_16x16x32_bf16 v[152:155], a[116:119], a[20:23], v[152:155]\n" \
"s_cmp_eq_u32 s63, 0\n" \
"s_cselect_b32 s85, 34816, 0\n" \
"ds_read_b64_tr_b16 a[88:89], v233 offset:512\n" \
"v_mfma_f32_16x16x32_bf16 v[156:159], a[120:123], a[16:19], v[156:159]\n" \
"s_add_u32 s73, s73, s85\n" \
"ds_read_b64_tr_b16 a[90:91], v233 offset:2560\n" \
"v_mfma_f32_16x16x32_bf16 v[156:159], a[124:127], a[20:23], v[156:159]\n" \
"buffer_load_dwordx4 v225, s[48:51], s75 offen offset:1024 lds\n" \
"v_mfma_f32_16x16x32_bf16 v[188:191], a[120:123], a[24:27], v[188:191]\n" \
"s_waitcnt vmcnt(37)\n" \
"ds_read_b128 a[16:19], v250 offset:4096\n" \
"v_mfma_f32_16x16x32_bf16 v[188:191], a[124:127], a[28:31], v[188:191]\n" \
"ds_read_b128 a[20:23], v251 offset:4096\n" \
"v_mfma_f32_16x16x32_bf16 v[184:187], a[112:115], a[24:27], v[184:187]\n" \
"s_waitcnt lgkmcnt(6)\n" \
"ds_read_b64_tr_b16 a[92:93], v233 offset:8704\n" \
"v_mfma_f32_16x16x32_bf16 v[184:187], a[116:119], a[28:31], v[184:187]\n" \
"buffer_load_dwordx4 v226, s[48:51], s75 offen offset:2048 lds\n" \
"v_mfma_f32_16x16x32_bf16 v[180:183], a[104:107], a[24:27], v[180:183]\n" \
"ds_read_b64_tr_b16 a[94:95], v233 offset:10752\n" \
"v_mfma_f32_16x16x32_bf16 v[180:183], a[108:111], a[28:31], v[180:183]\n" \
"v_mfma_f32_16x16x32_bf16 v[176:179], a[96:99], a[24:27], v[176:179]\n" \
"v_mfma_f32_16x16x32_bf16 v[176:179], a[100:103], a[28:31], v[176:179]\n" \
"buffer_load_dwordx4 v227, s[48:51], s75 offen offset:3072 lds\n" \
"v_mfma_f32_16x16x32_bf16 v[64:67], a[64:67], a[0:3], v[64:67]\n" \
"s_waitcnt vmcnt(37)\n" \
"ds_read_b128 a[24:27], v250 offset:6144\n" \
"v_mfma_f32_16x16x32_bf16 v[64:67], a[68:71], a[4:7], v[64:67]\n" \
"s_add_u32 m0, s74, 135168\n" \
"ds_read_b128 a[28:31], v251 offset:6144\n" \
"v_mfma_f32_16x16x32_bf16 v[68:71], a[72:75], a[0:3], v[68:71]\n" \
"s_waitcnt lgkmcnt(2)\n" \
"ds_read_b64_tr_b16 a[96:97], v232 offset:1024\n" \
"v_mfma_f32_16x16x32_bf16 v[68:71], a[76:79], a[4:7], v[68:71]\n" \
"buffer_load_dwordx4 v224, s[48:51], s77 offen offset:0 lds\n" \
"v_mfma_f32_16x16x32_bf16 v[72:75], a[80:83], a[0:3], v[72:75]\n" \
"ds_read_b64_tr_b16 a[98:99], v232 offset:3072\n" \
"v_mfma_f32_16x16x32_bf16 v[72:75], a[84:87], a[4:7], v[72:75]\n" \
"ds_read_b64_tr_b16 a[100:101], v232 offset:9216\n" \
"v_mfma_f32_16x16x32_bf16 v[76:79], a[88:91], a[0:3], v[76:79]\n" \
"ds_read_b64_tr_b16 a[102:103], v232 offset:11264\n" \
"v_mfma_f32_16x16x32_bf16 v[76:79], a[92:95], a[4:7], v[76:79]\n" \
"buffer_load_dwordx4 v225, s[48:51], s77 offen offset:1024 lds\n" \
"v_mfma_f32_16x16x32_bf16 v[108:111], a[88:91], a[8:11], v[108:111]\n" \
"ds_read_b64_tr_b16 a[104:105], v233 offset:1024\n" \
"v_mfma_f32_16x16x32_bf16 v[108:111], a[92:95], a[12:15], v[108:111]\n" \
"ds_read_b64_tr_b16 a[106:107], v233 offset:3072\n" \
"v_mfma_f32_16x16x32_bf16 v[104:107], a[80:83], a[8:11], v[104:107]\n" \
"s_waitcnt lgkmcnt(6)\n" \
"ds_read_b64_tr_b16 a[108:109], v233 offset:9216\n" \
"v_mfma_f32_16x16x32_bf16 v[104:107], a[84:87], a[12:15], v[104:107]\n" \
"buffer_load_dwordx4 v226, s[48:51], s77 offen offset:2048 lds\n" \
"v_mfma_f32_16x16x32_bf16 v[100:103], a[72:75], a[8:11], v[100:103]\n" \
"ds_read_b64_tr_b16 a[110:111], v233 offset:11264\n" \
"v_mfma_f32_16x16x32_bf16 v[100:103], a[76:79], a[12:15], v[100:103]\n" \
"ds_read_b64_tr_b16 a[112:113], v232 offset:1536\n" \
"v_mfma_f32_16x16x32_bf16 v[96:99], a[64:67], a[8:11], v[96:99]\n" \
"ds_read_b64_tr_b16 a[114:115], v232 offset:3584\n" \
"v_mfma_f32_16x16x32_bf16 v[96:99], a[68:71], a[12:15], v[96:99]\n" \
"buffer_load_dwordx4 v227, s[48:51], s77 offen offset:3072 lds\n" \
"v_mfma_f32_16x16x32_bf16 v[128:131], a[64:67], a[16:19], v[128:131]\n" \
"ds_read_b64_tr_b16 a[116:117], v232 offset:9728\n" \
"v_mfma_f32_16x16x32_bf16 v[128:131], a[68:71], a[20:23], v[128:131]\n" \
"ds_read_b64_tr_b16 a[118:119], v232 offset:11776\n" \
"v_mfma_f32_16x16x32_bf16 v[132:135], a[72:75], a[16:19], v[132:135]\n" \
"ds_read_b64_tr_b16 a[120:121], v233 offset:1536\n" \
"v_mfma_f32_16x16x32_bf16 v[132:135], a[76:79], a[20:23], v[132:135]\n" \
"buffer_store_dwordx4 a[200:203], v234, s[56:59], s93 offen offset:2048 sc1\n" \
"v_mfma_f32_16x16x32_bf16 v[136:139], a[80:83], a[16:19], v[136:139]\n" \
"ds_read_b64_tr_b16 a[122:123], v233 offset:3584\n" \
"v_mfma_f32_16x16x32_bf16 v[136:139], a[84:87], a[20:23], v[136:139]\n" \
"ds_read_b64_tr_b16 a[124:125], v233 offset:9728\n" \
"v_mfma_f32_16x16x32_bf16 v[140:143], a[88:91], a[16:19], v[140:143]\n" \
"ds_read_b64_tr_b16 a[126:127], v233 offset:11776\n" \
"v_mfma_f32_16x16x32_bf16 v[140:143], a[92:95], a[20:23], v[140:143]\n" \
"v_mfma_f32_16x16x32_bf16 v[172:175], a[88:91], a[24:27], v[172:175]\n" \
"v_mfma_f32_16x16x32_bf16 v[172:175], a[92:95], a[28:31], v[172:175]\n" \
"v_mfma_f32_16x16x32_bf16 v[168:171], a[80:83], a[24:27], v[168:171]\n" \
"v_mfma_f32_16x16x32_bf16 v[168:171], a[84:87], a[28:31], v[168:171]\n" \
"buffer_store_dwordx4 a[204:207], v234, s[56:59], s93 offen offset:3072 sc1\n" \
"v_mfma_f32_16x16x32_bf16 v[164:167], a[72:75], a[24:27], v[164:167]\n" \
"v_mfma_f32_16x16x32_bf16 v[164:167], a[76:79], a[28:31], v[164:167]\n" \
"v_mfma_f32_16x16x32_bf16 v[160:163], a[64:67], a[24:27], v[160:163]\n" \
"v_mfma_f32_16x16x32_bf16 v[160:163], a[68:71], a[28:31], v[160:163]\n" \
"s_waitcnt vmcnt(38) lgkmcnt(0)\n" \
"s_barrier\n" \
"v_mfma_f32_16x16x32_bf16 v[80:83], a[96:99], a[0:3], v[80:83]\n" \
"s_lshr_b32 s71, s61, 1\n" \
"s_lshl_b32 s71, s71, 14\n" \
"ds_read_b64_tr_b16 a[64:65], v232 offset:16384\n" \
"v_mfma_f32_16x16x32_bf16 v[80:83], a[100:103], a[4:7], v[80:83]\n" \
"s_xor_b32 s71, s71, 0x4000\n" \
"s_and_b32 s70, s61, 1\n" \
"ds_read_b64_tr_b16 a[66:67], v232 offset:18432\n" \
"v_mfma_f32_16x16x32_bf16 v[84:87], a[104:107], a[0:3], v[84:87]\n" \
"s_lshl_b32 s70, s70, 12\n" \
"s_add_u32 s71, s71, s70\n" \
"ds_read_b64_tr_b16 a[68:69], v232 offset:24576\n" \
"v_mfma_f32_16x16x32_bf16 v[84:87], a[108:111], a[4:7], v[84:87]\n" \
"v_add_u32 v224, s71, v237\n" \
"s_and_b32 s70, s63, 1\n" \
"v_mfma_f32_16x16x32_bf16 v[88:91], a[112:115], a[0:3], v[88:91]\n" \
"s_lshl_b32 s70, s70, 15\n" \
"s_xor_b32 s70, s70, 0x18000\n" \
"ds_read_b64_tr_b16 a[70:71], v232 offset:26624\n" \
"v_mfma_f32_16x16x32_bf16 v[88:91], a[116:119], a[4:7], v[88:91]\n" \
"s_add_u32 s71, s71, s70\n" \
"ds_read_b64_tr_b16 a[72:73], v233 offset:16384\n" \
"v_add_u32 v225, s71, v235\n" \
"v_mfma_f32_16x16x32_bf16 v[92:95], a[120:123], a[0:3], v[92:95]\n" \
"s_lshr_b32 s64, s22, 7\n" \
"s_sub_u32 s64, 3, s64\n" \
"ds_read_b64_tr_b16 a[74:75], v233 offset:18432\n" \
"v_mfma_f32_16x16x32_bf16 v[92:95], a[124:127], a[4:7], v[92:95]\n" \
"s_and_b32 s64, s64, 7\n" \
"s_and_b32 s65, %1, 1\n" \
"s_lshl_b32 s65, s65, 3\n" \
"v_mfma_f32_16x16x32_bf16 v[124:127], a[120:123], a[8:11], v[124:127]\n" \
"s_waitcnt vmcnt(36)\n" \
"s_add_u32 s64, s64, s65\n" \
"ds_read_b128 a[0:3], v230 offset:0\n" \
"v_mfma_f32_16x16x32_bf16 v[124:127], a[124:127], a[12:15], v[124:127]\n" \
"s_lshl_b32 s64, s64, 7\n" \
"s_lshr_b32 s65, %1, 3\n" \
"ds_read_b128 a[4:7], v231 offset:0\n" \
"v_mfma_f32_16x16x32_bf16 v[120:123], a[112:115], a[8:11], v[120:123]\n" \
"s_lshl_b32 s65, s65, 2\n" \
"s_add_u32 s65, s65, s61\n" \
"ds_read_b64_tr_b16 a[76:77], v233 offset:24576\n" \
"v_mfma_f32_16x16x32_bf16 v[120:123], a[116:119], a[12:15], v[120:123]\n" \
"s_mul_i32 s65, s65, 24576\n" \
"s_add_u32 s64, s64, s65\n" \
"v_bfe_u32 v254, v234, 7, 2\n" \
"v_mfma_f32_16x16x32_bf16 v[116:119], a[104:107], a[8:11], v[116:119]\n" \
"ds_read_b64_tr_b16 a[78:79], v233 offset:26624\n" \
"v_mul_u32_u24 v254, 3, v254\n" \
"v_mfma_f32_16x16x32_bf16 v[116:119], a[108:111], a[12:15], v[116:119]\n" \
"ds_read_b64_tr_b16 a[80:81], v232 offset:16896\n" \
"v_bfe_u32 v243, v234, 5, 2\n" \
"v_mfma_f32_16x16x32_bf16 v[112:115], a[96:99], a[8:11], v[112:115]\n" \
"s_add_u32 s93, s94, 8192\n" \
"ds_read_b64_tr_b16 a[82:83], v232 offset:18944\n" \
"v_min_u32 v243, 2, v243\n" \
"v_mfma_f32_16x16x32_bf16 v[112:115], a[100:103], a[12:15], v[112:115]\n" \
"buffer_store_dwordx4 v[32:35], v236, s[56:59], s93 offen offset:0 sc1\n" \
"v_mfma_f32_16x16x32_bf16 v[144:147], a[96:99], a[16:19], v[144:147]\n" \
"s_waitcnt vmcnt(35)\n" \
"ds_read_b128 a[8:11], v230 offset:2048\n" \
"v_add_u32 v254, v254, v243\n" \
"v_mfma_f32_16x16x32_bf16 v[144:147], a[100:103], a[20:23], v[144:147]\n" \
"ds_read_b128 a[12:15], v231 offset:2048\n" \
"v_and_b32 v243, 16, v234\n" \
"v_mfma_f32_16x16x32_bf16 v[148:151], a[104:107], a[16:19], v[148:151]\n" \
"ds_read_b64_tr_b16 a[84:85], v232 offset:25088\n" \
"v_lshl_add_u32 v243, v243, 2, s64\n" \
"v_mfma_f32_16x16x32_bf16 v[148:151], a[108:111], a[20:23], v[148:151]\n" \
"v_lshl_add_u32 v254, v254, 11, v243\n" \
"v_mfma_f32_16x16x32_bf16 v[152:155], a[112:115], a[16:19], v[152:155]\n" \
"ds_read_b64_tr_b16 a[86:87], v232 offset:27136\n" \
"v_mfma_f32_16x16x32_bf16 v[152:155], a[116:119], a[20:23], v[152:155]\n" \
"ds_read_b64_tr_b16 a[88:89], v233 offset:16896\n" \
"v_mfma_f32_16x16x32_bf16 v[156:159], a[120:123], a[16:19], v[156:159]\n" \
"ds_read_b64_tr_b16 a[90:91], v233 offset:18944\n" \
"v_mfma_f32_16x16x32_bf16 v[156:159], a[124:127], a[20:23], v[156:159]\n" \
"v_mfma_f32_16x16x32_bf16 v[188:191], a[120:123], a[24:27], v[188:191]\n" \
"s_waitcnt vmcnt(33)\n" \
"ds_read_b128 a[16:19], v230 offset:4096\n" \
"v_mfma_f32_16x16x32_bf16 v[188:191], a[124:127], a[28:31], v[188:191]\n" \
"ds_read_b128 a[20:23], v231 offset:4096\n" \
"v_mfma_f32_16x16x32_bf16 v[184:187], a[112:115], a[24:27], v[184:187]\n" \
"ds_read_b64_tr_b16 a[92:93], v233 offset:25088\n" \
"v_mfma_f32_16x16x32_bf16 v[184:187], a[116:119], a[28:31], v[184:187]\n" \
"v_mfma_f32_16x16x32_bf16 v[180:183], a[104:107], a[24:27], v[180:183]\n" \
"ds_read_b64_tr_b16 a[94:95], v233 offset:27136\n" \
"v_mfma_f32_16x16x32_bf16 v[180:183], a[108:111], a[28:31], v[180:183]\n" \
"v_mfma_f32_16x16x32_bf16 v[176:179], a[96:99], a[24:27], v[176:179]\n" \
"buffer_store_dwordx4 v[48:51], v236, s[56:59], s93 offen offset:256 sc1\n" \
"v_mfma_f32_16x16x32_bf16 v[176:179], a[100:103], a[28:31], v[176:179]\n" \
"s_waitcnt lgkmcnt(1)\n" \
"v_mfma_f32_16x16x32_bf16 v[64:67], a[64:67], a[0:3], v[64:67]\n" \
"s_waitcnt vmcnt(32)\n" \
"s_lshl_b32 s68, s63, 2\n" \
"ds_read_b128 a[24:27], v230 offset:6144\n" \
"Lpg4447_%=:\n" \
"s_cbranch_execz Lpg6186_%=\n" \
"v_mfma_f32_16x16x32_bf16 v[64:67], a[68:71], a[4:7], v[64:67]\n" \
"s_add_u32 s68, s68, s90\n" \
"ds_read_b128 a[28:31], v231 offset:6144\n" \
"v_mfma_f32_16x16x32_bf16 v[68:71], a[72:75], a[0:3], v[68:71]\n" \
"ds_read_b64_tr_b16 a[96:97], v232 offset:17408\n" \
"v_mfma_f32_16x16x32_bf16 v[68:71], a[76:79], a[4:7], v[68:71]\n" \
"v_mov_b32 v228, s60\n" \
"v_lshlrev_b32 v229, 28, s61\n" \
"v_mfma_f32_16x16x32_bf16 v[72:75], a[80:83], a[0:3], v[72:75]\n" \
"ds_read_b64_tr_b16 a[98:99], v232 offset:19456\n" \
"v_mfma_f32_16x16x32_bf16 v[72:75], a[84:87], a[4:7], v[72:75]\n" \
"s_waitcnt lgkmcnt(3)\n" \
"ds_read_b64_tr_b16 a[100:101], v232 offset:25600\n" \
"v_mfma_f32_16x16x32_bf16 v[76:79], a[88:91], a[0:3], v[76:79]\n" \
"ds_read_b64_tr_b16 a[102:103], v232 offset:27648\n" \
"v_mfma_f32_16x16x32_bf16 v[76:79], a[92:95], a[4:7], v[76:79]\n" \
"v_mfma_f32_16x16x32_bf16 v[108:111], a[88:91], a[8:11], v[108:111]\n" \
"ds_read_b64_tr_b16 a[104:105], v233 offset:17408\n" \
"v_mfma_f32_16x16x32_bf16 v[108:111], a[92:95], a[12:15], v[108:111]\n" \
"ds_read_b64_tr_b16 a[106:107], v233 offset:19456\n" \
"v_mfma_f32_16x16x32_bf16 v[104:107], a[80:83], a[8:11], v[104:107]\n" \
"ds_read_b64_tr_b16 a[108:109], v233 offset:25600\n" \
"v_mfma_f32_16x16x32_bf16 v[104:107], a[84:87], a[12:15], v[104:107]\n" \
"v_mfma_f32_16x16x32_bf16 v[100:103], a[72:75], a[8:11], v[100:103]\n" \
"ds_read_b64_tr_b16 a[110:111], v233 offset:27648\n" \
"v_mfma_f32_16x16x32_bf16 v[100:103], a[76:79], a[12:15], v[100:103]\n" \
"ds_read_b64_tr_b16 a[112:113], v232 offset:17920\n" \
"v_mfma_f32_16x16x32_bf16 v[96:99], a[64:67], a[8:11], v[96:99]\n" \
"ds_read_b64_tr_b16 a[114:115], v232 offset:19968\n" \
"v_mfma_f32_16x16x32_bf16 v[96:99], a[68:71], a[12:15], v[96:99]\n" \
"v_mfma_f32_16x16x32_bf16 v[128:131], a[64:67], a[16:19], v[128:131]\n" \
"ds_read_b64_tr_b16 a[116:117], v232 offset:26112\n" \
"v_mfma_f32_16x16x32_bf16 v[128:131], a[68:71], a[20:23], v[128:131]\n" \
"ds_read_b64_tr_b16 a[118:119], v232 offset:28160\n" \
"v_mfma_f32_16x16x32_bf16 v[132:135], a[72:75], a[16:19], v[132:135]\n" \
"ds_read_b64_tr_b16 a[120:121], v233 offset:17920\n" \
"v_mfma_f32_16x16x32_bf16 v[132:135], a[76:79], a[20:23], v[132:135]\n" \
"buffer_store_dwordx4 v[40:43], v236, s[56:59], s93 offen offset:2048 sc1\n" \
"v_mfma_f32_16x16x32_bf16 v[136:139], a[80:83], a[16:19], v[136:139]\n" \
"ds_read_b64_tr_b16 a[122:123], v233 offset:19968\n" \
"v_mfma_f32_16x16x32_bf16 v[136:139], a[84:87], a[20:23], v[136:139]\n" \
"ds_read_b64_tr_b16 a[124:125], v233 offset:26112\n" \
"v_mfma_f32_16x16x32_bf16 v[140:143], a[88:91], a[16:19], v[140:143]\n" \
"ds_read_b64_tr_b16 a[126:127], v233 offset:28160\n" \
"v_mfma_f32_16x16x32_bf16 v[140:143], a[92:95], a[20:23], v[140:143]\n" \
"s_waitcnt lgkmcnt(3)\n" \
"v_mfma_f32_16x16x32_bf16 v[172:175], a[88:91], a[24:27], v[172:175]\n" \
"v_mfma_f32_16x16x32_bf16 v[172:175], a[92:95], a[28:31], v[172:175]\n" \
"v_mfma_f32_16x16x32_bf16 v[168:171], a[80:83], a[24:27], v[168:171]\n" \
"v_mfma_f32_16x16x32_bf16 v[168:171], a[84:87], a[28:31], v[168:171]\n" \
"v_mfma_f32_16x16x32_bf16 v[164:167], a[72:75], a[24:27], v[164:167]\n" \
"v_mfma_f32_16x16x32_bf16 v[164:167], a[76:79], a[28:31], v[164:167]\n" \
"v_mfma_f32_16x16x32_bf16 v[160:163], a[64:67], a[24:27], v[160:163]\n" \
"v_mfma_f32_16x16x32_bf16 v[160:163], a[68:71], a[28:31], v[160:163]\n" \
"s_waitcnt vmcnt(3) lgkmcnt(0)\n" \
"s_barrier\n" \
"v_mfma_f32_16x16x32_bf16 v[80:83], a[96:99], a[0:3], v[80:83]\n" \
"ds_read_b64_tr_b16 a[64:65], v232 offset:32768\n" \
"v_mfma_f32_16x16x32_bf16 v[80:83], a[100:103], a[4:7], v[80:83]\n" \
"ds_read_b64_tr_b16 a[66:67], v232 offset:34816\n" \
"buffer_store_dword v228, v229, s[56:59], s68 offen sc1\n" \
"v_mfma_f32_16x16x32_bf16 v[84:87], a[104:107], a[0:3], v[84:87]\n" \
"ds_read_b64_tr_b16 a[68:69], v232 offset:40960\n" \
"v_lshrrev_b32 v238, 4, v234\n" \
"v_mfma_f32_16x16x32_bf16 v[84:87], a[108:111], a[4:7], v[84:87]\n" \
"buffer_store_dwordx4 v[56:59], v236, s[56:59], s93 offen offset:2304 sc1\n" \
"v_mfma_f32_16x16x32_bf16 v[88:91], a[112:115], a[0:3], v[88:91]\n" \
"ds_read_b64_tr_b16 a[70:71], v232 offset:43008\n" \
"v_lshrrev_b32 v240, 4, v238\n" \
"v_mfma_f32_16x16x32_bf16 v[88:91], a[116:119], a[4:7], v[88:91]\n" \
"ds_read_b64_tr_b16 a[72:73], v233 offset:32768\n" \
"v_lshlrev_b32 v240, 2, v240\n" \
"v_mfma_f32_16x16x32_bf16 v[92:95], a[120:123], a[0:3], v[92:95]\n" \
"ds_read_b64_tr_b16 a[74:75], v233 offset:34816\n" \
"v_and_b32 v238, 15, v238\n" \
"v_mfma_f32_16x16x32_bf16 v[92:95], a[124:127], a[4:7], v[92:95]\n" \
"ds_write_b128 v224, v[192:195] offset:8192\n" \
"v_mfma_f32_16x16x32_bf16 v[124:127], a[120:123], a[8:11], v[124:127]\n" \
"ds_read_b128 a[0:3], v230 offset:32768\n" \
"v_sub_u32 v238, v238, v240\n" \
"v_mfma_f32_16x16x32_bf16 v[124:127], a[124:127], a[12:15], v[124:127]\n" \
"ds_read_b128 a[4:7], v231 offset:32768\n" \
"v_subrev_u32 v248, 0, v238\n" \
"v_mfma_f32_16x16x32_bf16 v[120:123], a[112:115], a[8:11], v[120:123]\n" \
"ds_read_b64_tr_b16 a[76:77], v233 offset:40960\n" \
"v_subrev_u32 v249, 1, v238\n" \
"v_mfma_f32_16x16x32_bf16 v[120:123], a[116:119], a[12:15], v[120:123]\n" \
"ds_write_b128 v224, v[208:211] offset:8448\n" \
"v_mfma_f32_16x16x32_bf16 v[116:119], a[104:107], a[8:11], v[116:119]\n" \
"ds_read_b64_tr_b16 a[78:79], v233 offset:43008\n" \
"v_subrev_u32 v250, 2, v238\n" \
"v_mfma_f32_16x16x32_bf16 v[116:119], a[108:111], a[12:15], v[116:119]\n" \
"ds_read_b64_tr_b16 a[80:81], v232 offset:33280\n" \
"v_subrev_u32 v251, 3, v238\n" \
"v_mfma_f32_16x16x32_bf16 v[112:115], a[96:99], a[8:11], v[112:115]\n" \
"ds_read_b64_tr_b16 a[82:83], v232 offset:35328\n" \
"v_ashrrev_i32 v248, 31, v248\n" \
"v_mfma_f32_16x16x32_bf16 v[112:115], a[100:103], a[12:15], v[112:115]\n" \
"v_ashrrev_i32 v249, 31, v249\n" \
"v_ashrrev_i32 v250, 31, v250\n" \
"v_mfma_f32_16x16x32_bf16 v[144:147], a[96:99], a[16:19], v[144:147]\n" \
"ds_read_b128 a[8:11], v230 offset:34816\n" \
"v_ashrrev_i32 v251, 31, v251\n" \
"v_mfma_f32_16x16x32_bf16 v[144:147], a[100:103], a[20:23], v[144:147]\n" \
"ds_read_b128 a[12:15], v231 offset:34816\n" \
"v_lshlrev_b32 v248, 23, v248\n" \
"v_mfma_f32_16x16x32_bf16 v[148:151], a[104:107], a[16:19], v[148:151]\n" \
"ds_read_b64_tr_b16 a[84:85], v232 offset:41472\n" \
"v_lshlrev_b32 v249, 23, v249\n" \
"v_mfma_f32_16x16x32_bf16 v[148:151], a[108:111], a[20:23], v[148:151]\n" \
"ds_write_b128 v224, v[200:203] offset:10240\n" \
"v_mfma_f32_16x16x32_bf16 v[152:155], a[112:115], a[16:19], v[152:155]\n" \
"ds_read_b64_tr_b16 a[86:87], v232 offset:43520\n" \
"v_lshlrev_b32 v250, 23, v250\n" \
"v_mfma_f32_16x16x32_bf16 v[152:155], a[116:119], a[20:23], v[152:155]\n" \
"ds_read_b64_tr_b16 a[88:89], v233 offset:33280\n" \
"v_lshlrev_b32 v251, 23, v251\n" \
"v_mfma_f32_16x16x32_bf16 v[156:159], a[120:123], a[16:19], v[156:159]\n" \
"s_add_u32 s93, s94, 0\n" \
"ds_read_b64_tr_b16 a[90:91], v233 offset:35328\n" \
"v_mov_b32 v240, 0xff800000\n" \
"v_mfma_f32_16x16x32_bf16 v[156:159], a[124:127], a[20:23], v[156:159]\n" \
"buffer_store_dwordx4 v[36:39], v236, s[56:59], s93 offen offset:0 sc1\n" \
"v_mfma_f32_16x16x32_bf16 v[188:191], a[120:123], a[24:27], v[188:191]\n" \
"ds_read_b128 a[16:19], v230 offset:36864\n" \
"v_add_f32 v248, 0x42400000, v248\n" \
"v_mfma_f32_16x16x32_bf16 v[188:191], a[124:127], a[28:31], v[188:191]\n" \
"ds_read_b128 a[20:23], v231 offset:36864\n" \
"v_add_f32 v249, 0x42400000, v249\n" \
"v_mfma_f32_16x16x32_bf16 v[184:187], a[112:115], a[24:27], v[184:187]\n" \
"ds_read_b64_tr_b16 a[92:93], v233 offset:41472\n" \
"v_add_f32 v250, 0x42400000, v250\n" \
"v_mfma_f32_16x16x32_bf16 v[184:187], a[116:119], a[28:31], v[184:187]\n" \
"ds_write_b128 v224, v[216:219] offset:10496\n" \
"v_mfma_f32_16x16x32_bf16 v[180:183], a[104:107], a[24:27], v[180:183]\n" \
"ds_read_b64_tr_b16 a[94:95], v233 offset:43520\n" \
"v_add_f32 v251, 0x42400000, v251\n" \
"v_mfma_f32_16x16x32_bf16 v[180:183], a[108:111], a[28:31], v[180:183]\n" \
"s_mov_b32 s72, 0x42400000\n" \
"s_cmp_gt_u32 s61, 0\n" \
"s_cselect_b32 s70, s72, 0xff800000\n" \
"v_mfma_f32_16x16x32_bf16 v[176:179], a[96:99], a[24:27], v[176:179]\n" \
"ds_write_b128 v224, v[196:199] offset:0\n" \
"v_mfma_f32_16x16x32_bf16 v[176:179], a[100:103], a[28:31], v[176:179]\n" \
"s_waitcnt lgkmcnt(3)\n" \
"v_mov_b32 v252, s70\n" \
"v_mfma_f32_16x16x32_bf16 v[64:67], a[64:67], a[0:3], v[64:67]\n" \
"ds_read_b128 a[24:27], v230 offset:38912\n" \
"v_mfma_f32_16x16x32_bf16 v[64:67], a[68:71], a[4:7], v[64:67]\n" \
"s_cmp_gt_u32 s61, 1\n" \
"s_cselect_b32 s70, s72, 0xff800000\n" \
"ds_read_b128 a[28:31], v231 offset:38912\n" \
"v_mfma_f32_16x16x32_bf16 v[68:71], a[72:75], a[0:3], v[68:71]\n" \
"ds_read_b64_tr_b16 a[96:97], v232 offset:33792\n" \
"v_mov_b32 v253, s70\n" \
"v_mfma_f32_16x16x32_bf16 v[68:71], a[76:79], a[4:7], v[68:71]\n" \
"s_cmp_gt_u32 s61, 2\n" \
"s_cselect_b32 s70, s72, 0xff800000\n" \
"v_mov_b32 v226, s70\n" \
"v_mfma_f32_16x16x32_bf16 v[72:75], a[80:83], a[0:3], v[72:75]\n" \
"ds_read_b64_tr_b16 a[98:99], v232 offset:35840\n" \
"v_bfe_i32 v243, v234, 4, 1\n" \
"v_mfma_f32_16x16x32_bf16 v[72:75], a[84:87], a[4:7], v[72:75]\n" \
"s_waitcnt lgkmcnt(3)\n" \
"ds_read_b64_tr_b16 a[100:101], v232 offset:41984\n" \
"v_and_b32 v243, 0xaaaa, v243\n" \
"v_mfma_f32_16x16x32_bf16 v[76:79], a[88:91], a[0:3], v[76:79]\n" \
"ds_read_b64_tr_b16 a[102:103], v232 offset:44032\n" \
"v_or_b32 v243, 0x4444, v243\n" \
"v_mfma_f32_16x16x32_bf16 v[76:79], a[92:95], a[4:7], v[76:79]\n" \
"ds_write_b128 v224, v[212:215] offset:256\n" \
"v_mfma_f32_16x16x32_bf16 v[108:111], a[88:91], a[8:11], v[108:111]\n" \
"ds_read_b64_tr_b16 a[104:105], v233 offset:33792\n" \
"v_bfe_i32 v244, v234, 5, 1\n" \
"v_mfma_f32_16x16x32_bf16 v[108:111], a[92:95], a[12:15], v[108:111]\n" \
"ds_read_b64_tr_b16 a[106:107], v233 offset:35840\n" \
"v_not_b32 v244, v244\n" \
"v_mfma_f32_16x16x32_bf16 v[104:107], a[80:83], a[8:11], v[104:107]\n" \
"ds_read_b64_tr_b16 a[108:109], v233 offset:41984\n" \
"v_and_b32 v244, 0x3f803f80, v244\n" \
"v_mfma_f32_16x16x32_bf16 v[104:107], a[84:87], a[12:15], v[104:107]\n" \
"buffer_store_dwordx4 v[16:19], v236, s[56:59], s93 offen offset:256 sc1\n" \
"v_mfma_f32_16x16x32_bf16 v[100:103], a[72:75], a[8:11], v[100:103]\n" \
"ds_read_b64_tr_b16 a[110:111], v233 offset:44032\n" \
"v_mov_b32 v245, v244\n" \
"v_mfma_f32_16x16x32_bf16 v[100:103], a[76:79], a[12:15], v[100:103]\n" \
"ds_read_b64_tr_b16 a[112:113], v232 offset:34304\n" \
"v_mov_b32 v246, v244\n" \
"v_mfma_f32_16x16x32_bf16 v[96:99], a[64:67], a[8:11], v[96:99]\n" \
"ds_read_b64_tr_b16 a[114:115], v232 offset:36352\n" \
"v_mov_b32 v247, v244\n" \
"v_mfma_f32_16x16x32_bf16 v[96:99], a[68:71], a[12:15], v[96:99]\n" \
"ds_write_b128 v224, v[204:207] offset:2048\n" \
"v_mfma_f32_16x16x32_bf16 v[128:131], a[64:67], a[16:19], v[128:131]\n" \
"s_mov_b32 s76, s73\n" \
"s_and_b32 s77, s63, 1\n" \
"ds_read_b64_tr_b16 a[116:117], v232 offset:42496\n" \
"v_mfma_f32_16x16x32_bf16 v[128:131], a[68:71], a[20:23], v[128:131]\n" \
"s_lshl_b32 s77, s77, 15\n" \
"s_xor_b32 s89, s77, 0x8000\n" \
"ds_read_b64_tr_b16 a[118:119], v232 offset:44544\n" \
"v_mfma_f32_16x16x32_bf16 v[132:135], a[72:75], a[16:19], v[132:135]\n" \
"s_add_u32 s82, s89, 0x10000\n" \
"s_mov_b32 s74, 0\n" \
"ds_read_b64_tr_b16 a[120:121], v233 offset:34304\n" \
"v_mfma_f32_16x16x32_bf16 v[132:135], a[76:79], a[20:23], v[132:135]\n" \
"ds_write_b128 v224, v[220:223] offset:2304\n" \
"v_mfma_f32_16x16x32_bf16 v[136:139], a[80:83], a[16:19], v[136:139]\n" \
"s_waitcnt lgkmcnt(4)\n" \
"s_add_u32 s73, s73, 34816\n" \
"ds_read_b64_tr_b16 a[122:123], v233 offset:36352\n" \
"v_mfma_f32_16x16x32_bf16 v[136:139], a[84:87], a[20:23], v[136:139]\n" \
"s_cmp_eq_u32 s73, s83\n" \
"s_cselect_b32 s70, 34816, 0\n" \
"ds_read_b64_tr_b16 a[124:125], v233 offset:42496\n" \
"v_mfma_f32_16x16x32_bf16 v[140:143], a[88:91], a[16:19], v[140:143]\n" \
"s_add_u32 s73, s73, s70\n" \
"s_lshr_b32 s70, s61, 1\n" \
"ds_read_b64_tr_b16 a[126:127], v233 offset:44544\n" \
"v_mfma_f32_16x16x32_bf16 v[140:143], a[92:95], a[20:23], v[140:143]\n" \
"s_lshl_b32 s85, s63, 2\n" \
"s_sub_u32 s86, 60, s85\n" \
"s_add_u32 s85, s85, s70\n" \
"v_mfma_f32_16x16x32_bf16 v[172:175], a[88:91], a[24:27], v[172:175]\n" \
"s_add_u32 s86, s86, s70\n" \
"s_mul_i32 s87, s85, 266240\n" \
"v_add_u32 v230, 0x10000, v230\n" \
"v_mfma_f32_16x16x32_bf16 v[172:175], a[92:95], a[28:31], v[172:175]\n" \
"s_mul_i32 s88, s86, 266240\n" \
"s_and_b32 s75, s61, 1\n" \
"v_add_u32 v231, 0x10000, v231\n" \
"v_mfma_f32_16x16x32_bf16 v[168:171], a[80:83], a[24:27], v[168:171]\n" \
"s_lshl_b32 s70, s75, 8\n" \
"s_lshl_b32 s91, s62, 13\n" \
"s_add_u32 s91, s91, s70\n" \
"v_mfma_f32_16x16x32_bf16 v[168:171], a[84:87], a[28:31], v[168:171]\n" \
"buffer_store_dwordx4 v[52:55], v236, s[56:59], s93 offen offset:2048 sc1\n" \
"v_mfma_f32_16x16x32_bf16 v[164:167], a[72:75], a[24:27], v[164:167]\n" \
"s_add_u32 s91, s91, 35700736\n" \
"s_add_u32 s87, s87, s91\n" \
"s_add_u32 s88, s88, s91\n" \
"v_mfma_f32_16x16x32_bf16 v[164:167], a[76:79], a[28:31], v[164:167]\n" \
"s_lshl_b32 s75, s75, 14\n" \
"s_cmp_eq_u32 s63, 7\n" \
"s_cselect_b32 s75, 0, s75\n" \
"s_xor_b32 s78, s75, 0xc000\n" \
"s_mov_b32 s81, s78\n" \
"s_cmp_eq_u32 s63, 0\n" \
"s_cselect_b32 s91, s75, 0\n" \
"s_add_u32 s91, s91, 0x8000\n" \
"s_xor_b32 s75, s75, 0x8000\n" \
"s_sub_u32 s84, 7, s63\n" \
"v_mfma_f32_16x16x32_bf16 v[160:163], a[64:67], a[24:27], v[160:163]\n" \
"v_mfma_f32_16x16x32_bf16 v[160:163], a[68:71], a[28:31], v[160:163]\n" \
"s_waitcnt vmcnt(18) lgkmcnt(0)\n" \
"s_barrier\n" \
"v_mfma_f32_16x16x32_bf16 v[144:147], a[96:99], a[16:19], v[144:147]\n" \
"s_mov_b64 s[44:45], s[10:11]\n" \
"s_mov_b32 s46, 0\n" \
"ds_read_b64_tr_b16 a[64:65], v232 offset:49152\n" \
"v_mfma_f32_16x16x32_bf16 v[144:147], a[100:103], a[20:23], v[144:147]\n" \
"s_mov_b32 s47, 0\n" \
"s_cmp_eq_u32 s63, 7\n" \
"s_cselect_b64 s[44:45], s[18:19], s[44:45]\n" \
"s_cselect_b32 s46, 5, s46\n" \
"s_cmp_eq_u32 s63, 3\n" \
"s_cselect_b64 s[44:45], s[18:19], s[44:45]\n" \
"s_cselect_b32 s46, 3, s46\n" \
"ds_read_b64_tr_b16 a[66:67], v232 offset:51200\n" \
"v_mfma_f32_16x16x32_bf16 v[148:151], a[104:107], a[16:19], v[148:151]\n" \
"s_cmp_eq_u32 s63, 3\n" \
"s_cselect_b32 s47, 640, s47\n" \
"ds_read_b64_tr_b16 a[68:69], v232 offset:57344\n" \
"v_mfma_f32_16x16x32_bf16 v[148:151], a[108:111], a[20:23], v[148:151]\n" \
"ds_write_b128 v225, a[192:195] offset:0\n" \
"v_mfma_f32_16x16x32_bf16 v[152:155], a[112:115], a[16:19], v[152:155]\n" \
"ds_read_b64_tr_b16 a[70:71], v232 offset:59392\n" \
"v_mfma_f32_16x16x32_bf16 v[152:155], a[116:119], a[20:23], v[152:155]\n" \
"s_cmp_eq_u32 s63, 6\n" \
"s_cselect_b64 s[44:45], s[22:23], s[44:45]\n" \
"s_cselect_b32 s46, 6, s46\n" \
"ds_read_b64_tr_b16 a[72:73], v233 offset:49152\n" \
"v_mfma_f32_16x16x32_bf16 v[156:159], a[120:123], a[16:19], v[156:159]\n" \
"ds_read_b64_tr_b16 a[74:75], v233 offset:51200\n" \
"v_mfma_f32_16x16x32_bf16 v[156:159], a[124:127], a[20:23], v[156:159]\n" \
"s_cmp_eq_u32 s63, 4\n" \
"s_cselect_b64 s[44:45], s[20:21], s[44:45]\n" \
"s_cselect_b32 s46, 4, s46\n" \
"v_mfma_f32_16x16x32_bf16 v[188:191], a[120:123], a[24:27], v[188:191]\n" \
"ds_read_b128 a[16:19], v230 offset:0\n" \
"v_mfma_f32_16x16x32_bf16 v[188:191], a[124:127], a[28:31], v[188:191]\n" \
"s_cmp_eq_u32 s63, 2\n" \
"s_cselect_b64 s[44:45], s[24:25], s[44:45]\n" \
"s_cselect_b32 s46, 2, s46\n" \
"ds_read_b128 a[20:23], v231 offset:0\n" \
"v_mfma_f32_16x16x32_bf16 v[184:187], a[112:115], a[24:27], v[184:187]\n" \
"s_lshl_b32 s48, s62, 2\n" \
"s_add_u32 s48, s48, s61\n" \
"ds_read_b64_tr_b16 a[76:77], v233 offset:57344\n" \
"v_mfma_f32_16x16x32_bf16 v[184:187], a[116:119], a[28:31], v[184:187]\n" \
"ds_write_b128 v225, a[196:199] offset:1024\n" \
"v_mfma_f32_16x16x32_bf16 v[180:183], a[104:107], a[24:27], v[180:183]\n" \
"s_add_u32 s48, s48, s47\n" \
"s_lshl_b32 s48, s48, 13\n" \
"ds_read_b64_tr_b16 a[78:79], v233 offset:59392\n" \
"v_mfma_f32_16x16x32_bf16 v[180:183], a[108:111], a[28:31], v[180:183]\n" \
"s_add_u32 s44, s44, s48\n" \
"s_addc_u32 s45, s45, 0\n" \
"ds_read_b64_tr_b16 a[80:81], v232 offset:49664\n" \
"v_mfma_f32_16x16x32_bf16 v[176:179], a[96:99], a[24:27], v[176:179]\n" \
"s_cmp_eq_u32 s46, 0\n" \
"s_cselect_b32 s48, 28, 3\n" \
"ds_read_b64_tr_b16 a[82:83], v232 offset:51712\n" \
"v_mfma_f32_16x16x32_bf16 v[176:179], a[100:103], a[28:31], v[176:179]\n" \
"buffer_store_dwordx4 v[24:27], v236, s[56:59], s93 offen offset:2304 sc1\n" \
"v_mfma_f32_16x16x32_bf16 v[80:83], a[96:99], a[0:3], v[80:83]\n" \
"ds_read_b128 a[24:27], v230 offset:2048\n" \
"v_mfma_f32_16x16x32_bf16 v[80:83], a[100:103], a[4:7], v[80:83]\n" \
"ds_read_b128 a[28:31], v231 offset:2048\n" \
"v_mfma_f32_16x16x32_bf16 v[84:87], a[104:107], a[0:3], v[84:87]\n" \
"ds_read_b64_tr_b16 a[84:85], v232 offset:57856\n" \
"v_mfma_f32_16x16x32_bf16 v[84:87], a[108:111], a[4:7], v[84:87]\n" \
"ds_write_b128 v225, a[200:203] offset:2048\n" \
"v_mfma_f32_16x16x32_bf16 v[88:91], a[112:115], a[0:3], v[88:91]\n" \
"ds_read_b64_tr_b16 a[86:87], v232 offset:59904\n" \
"v_mfma_f32_16x16x32_bf16 v[88:91], a[116:119], a[4:7], v[88:91]\n" \
"ds_read_b64_tr_b16 a[88:89], v233 offset:49664\n" \
"v_mfma_f32_16x16x32_bf16 v[92:95], a[120:123], a[0:3], v[92:95]\n" \
"ds_read_b64_tr_b16 a[90:91], v233 offset:51712\n" \
"v_mfma_f32_16x16x32_bf16 v[92:95], a[124:127], a[4:7], v[92:95]\n" \
"ds_write_b128 v225, a[204:207] offset:3072\n" \
"v_mfma_f32_16x16x32_bf16 v[124:127], a[120:123], a[8:11], v[124:127]\n" \
"ds_read_b128 a[0:3], v230 offset:4096\n" \
"v_mfma_f32_16x16x32_bf16 v[124:127], a[124:127], a[12:15], v[124:127]\n" \
"ds_read_b128 a[4:7], v231 offset:4096\n" \
"v_mfma_f32_16x16x32_bf16 v[120:123], a[112:115], a[8:11], v[120:123]\n" \
"ds_read_b64_tr_b16 a[92:93], v233 offset:57856\n" \
"v_mfma_f32_16x16x32_bf16 v[120:123], a[116:119], a[12:15], v[120:123]\n" \
"v_mfma_f32_16x16x32_bf16 v[116:119], a[104:107], a[8:11], v[116:119]\n" \
"ds_read_b64_tr_b16 a[94:95], v233 offset:59904\n" \
"v_mfma_f32_16x16x32_bf16 v[116:119], a[108:111], a[12:15], v[116:119]\n" \
"v_mfma_f32_16x16x32_bf16 v[112:115], a[96:99], a[8:11], v[112:115]\n" \
"ds_write_b128 v225, a[224:227] offset:8192\n" \
"v_mfma_f32_16x16x32_bf16 v[112:115], a[100:103], a[12:15], v[112:115]\n" \
"s_waitcnt lgkmcnt(2)\n" \
"v_mfma_f32_16x16x32_bf16 v[64:67], a[64:67], a[16:19], v[64:67]\n" \
"ds_read_b128 a[8:11], v230 offset:6144\n" \
"ds_read_b128 a[12:15], v231 offset:6144\n" \
"v_mfma_f32_16x16x32_bf16 v[64:67], a[68:71], a[20:23], v[64:67]\n" \
"ds_read_b64_tr_b16 a[96:97], v232 offset:50176\n" \
"ds_read_b64_tr_b16 a[98:99], v232 offset:52224\n" \
"v_mfma_f32_16x16x32_bf16 v[68:71], a[72:75], a[16:19], v[68:71]\n" \
"s_waitcnt lgkmcnt(5)\n" \
"ds_read_b64_tr_b16 a[100:101], v232 offset:58368\n" \
"ds_read_b64_tr_b16 a[102:103], v232 offset:60416\n" \
"v_mfma_f32_16x16x32_bf16 v[68:71], a[76:79], a[20:23], v[68:71]\n" \
"s_getpc_b64 s[64:65]\n" \
"Lpd1_%=:\n" \
"s_lshl_b32 s66, s62, 2\n" \
"ds_read_b64_tr_b16 a[104:105], v233 offset:50176\n" \
"ds_read_b64_tr_b16 a[106:107], v233 offset:52224\n" \
"v_mfma_f32_16x16x32_bf16 v[72:75], a[80:83], a[16:19], v[72:75]\n" \
"s_add_u32 s66, s66, s61\n" \
"ds_read_b64_tr_b16 a[108:109], v233 offset:58368\n" \
"ds_read_b64_tr_b16 a[110:111], v233 offset:60416\n" \
"v_mfma_f32_16x16x32_bf16 v[72:75], a[84:87], a[20:23], v[72:75]\n" \
"s_and_b32 s66, s66, 15\n" \
"ds_read_b64_tr_b16 a[112:113], v232 offset:50688\n" \
"ds_read_b64_tr_b16 a[114:115], v232 offset:52736\n" \
"v_mfma_f32_16x16x32_bf16 v[76:79], a[88:91], a[16:19], v[76:79]\n" \
"s_lshl_b32 s66, s66, 12\n" \
"ds_read_b64_tr_b16 a[116:117], v232 offset:58880\n" \
"ds_read_b64_tr_b16 a[118:119], v232 offset:60928\n" \
"v_mfma_f32_16x16x32_bf16 v[76:79], a[92:95], a[20:23], v[76:79]\n" \
"s_add_u32 s64, s64, Lloop2_%=-Lpd1_%=\n" \
"s_addc_u32 s65, s65, 0\n" \
"s_mov_b32 s67, Lcode_end_%=-Lloop2_%=-4\n" \
"ds_read_b64_tr_b16 a[120:121], v233 offset:50688\n" \
"ds_read_b64_tr_b16 a[122:123], v233 offset:52736\n" \
"v_mfma_f32_16x16x32_bf16 v[96:99], a[64:67], a[24:27], v[96:99]\n" \
"s_add_u32 s93, s94, 17858560\n" \
"ds_read_b64_tr_b16 a[124:125], v233 offset:58880\n" \
"ds_read_b64_tr_b16 a[126:127], v233 offset:60928\n" \
"v_mfma_f32_16x16x32_bf16 v[96:99], a[68:71], a[28:31], v[96:99]\n" \
"buffer_store_dwordx4 v[44:47], v234, s[56:59], s93 offen offset:0 sc1\n" \
"v_mfma_f32_16x16x32_bf16 v[100:103], a[72:75], a[24:27], v[100:103]\n" \
"ds_write_b128 v225, a[228:231] offset:9216\n" \
"v_mfma_f32_16x16x32_bf16 v[100:103], a[76:79], a[28:31], v[100:103]\n" \
"v_lshl_add_u32 v230, v234, 2, s66\n" \
"v_min_u32 v230, s67, v230\n" \
"v_mfma_f32_16x16x32_bf16 v[104:107], a[80:83], a[24:27], v[104:107]\n" \
"buffer_load_dword v227, v242, s[56:59], s90 offen sc1\n" \
"v_mfma_f32_16x16x32_bf16 v[104:107], a[84:87], a[28:31], v[104:107]\n" \
"ds_write_b128 v225, a[232:235] offset:10240\n" \
"v_mfma_f32_16x16x32_bf16 v[108:111], a[88:91], a[24:27], v[108:111]\n" \
"global_load_dword v230, v230, s[64:65]\n" \
"v_mfma_f32_16x16x32_bf16 v[108:111], a[92:95], a[28:31], v[108:111]\n" \
"s_waitcnt lgkmcnt(1)\n" \
"v_cvt_pk_bf16_f32 v64, v64, v65\n" \
"v_cvt_pk_bf16_f32 v65, v66, v67\n" \
"v_mfma_f32_16x16x32_bf16 v[80:83], a[96:99], a[16:19], v[80:83]\n" \
"v_cvt_pk_bf16_f32 v66, v68, v69\n" \
"v_cvt_pk_bf16_f32 v67, v70, v71\n" \
"v_mfma_f32_16x16x32_bf16 v[80:83], a[100:103], a[20:23], v[80:83]\n" \
"v_accvgpr_write_b32 a128, v64\n" \
"v_accvgpr_write_b32 a129, v65\n" \
"v_accvgpr_write_b32 a130, v66\n" \
"v_mfma_f32_16x16x32_bf16 v[84:87], a[104:107], a[16:19], v[84:87]\n" \
"v_accvgpr_write_b32 a131, v67\n" \
"v_cvt_pk_bf16_f32 v72, v72, v73\n" \
"v_mfma_f32_16x16x32_bf16 v[84:87], a[108:111], a[20:23], v[84:87]\n" \
"buffer_store_dwordx4 v[60:63], v234, s[56:59], s93 offen offset:1024 sc1\n" \
"v_mfma_f32_16x16x32_bf16 v[88:91], a[112:115], a[16:19], v[88:91]\n" \
"ds_write_b128 v225, a[236:239] offset:11264\n" \
"v_mfma_f32_16x16x32_bf16 v[88:91], a[116:119], a[20:23], v[88:91]\n" \
"v_cvt_pk_bf16_f32 v73, v74, v75\n" \
"v_cvt_pk_bf16_f32 v74, v76, v77\n" \
"v_cvt_pk_bf16_f32 v75, v78, v79\n" \
"v_mfma_f32_16x16x32_bf16 v[92:95], a[120:123], a[16:19], v[92:95]\n" \
"v_accvgpr_write_b32 a132, v72\n" \
"v_accvgpr_write_b32 a133, v73\n" \
"v_mfma_f32_16x16x32_bf16 v[92:95], a[124:127], a[20:23], v[92:95]\n" \
"v_accvgpr_write_b32 a134, v74\n" \
"v_accvgpr_write_b32 a135, v75\n" \
"v_mfma_f32_16x16x32_bf16 v[112:115], a[96:99], a[24:27], v[112:115]\n" \
"v_cvt_pk_bf16_f32 v96, v96, v97\n" \
"v_cvt_pk_bf16_f32 v97, v98, v99\n" \
"v_mfma_f32_16x16x32_bf16 v[112:115], a[100:103], a[28:31], v[112:115]\n" \
"v_cvt_pk_bf16_f32 v98, v100, v101\n" \
"v_cvt_pk_bf16_f32 v99, v102, v103\n" \
"v_mfma_f32_16x16x32_bf16 v[116:119], a[104:107], a[24:27], v[116:119]\n" \
"v_cvt_pk_bf16_f32 v104, v104, v105\n" \
"v_cvt_pk_bf16_f32 v105, v106, v107\n" \
"v_mfma_f32_16x16x32_bf16 v[116:119], a[108:111], a[28:31], v[116:119]\n" \
"v_cvt_pk_bf16_f32 v106, v108, v109\n" \
"v_cvt_pk_bf16_f32 v107, v110, v111\n" \
"v_mfma_f32_16x16x32_bf16 v[120:123], a[112:115], a[24:27], v[120:123]\n" \
"v_mfma_f32_16x16x32_bf16 v[120:123], a[116:119], a[28:31], v[120:123]\n" \
"v_cvt_pk_bf16_f32 v80, v80, v81\n" \
"v_cvt_pk_bf16_f32 v81, v82, v83\n" \
"v_cvt_pk_bf16_f32 v82, v84, v85\n" \
"v_mfma_f32_16x16x32_bf16 v[124:127], a[120:123], a[24:27], v[124:127]\n" \
"v_cvt_pk_bf16_f32 v83, v86, v87\n" \
"v_accvgpr_write_b32 a136, v80\n" \
"v_mfma_f32_16x16x32_bf16 v[124:127], a[124:127], a[28:31], v[124:127]\n" \
"v_accvgpr_write_b32 a137, v81\n" \
"s_waitcnt lgkmcnt(0)\n" \
"s_barrier\n" \
"v_mfma_f32_16x16x32_bf16 v[128:131], a[64:67], a[0:3], v[128:131]\n" \
"v_accvgpr_write_b32 a138, v82\n" \
"v_accvgpr_write_b32 a139, v83\n" \
"v_mfma_f32_16x16x32_bf16 v[128:131], a[68:71], a[4:7], v[128:131]\n" \
"ds_read_b128 a[192:195], v237 offset:0\n" \
"v_cvt_pk_bf16_f32 v88, v88, v89\n" \
"v_mfma_f32_16x16x32_bf16 v[132:135], a[72:75], a[0:3], v[132:135]\n" \
"v_cvt_pk_bf16_f32 v89, v90, v91\n" \
"v_cvt_pk_bf16_f32 v90, v92, v93\n" \
"v_mfma_f32_16x16x32_bf16 v[132:135], a[76:79], a[4:7], v[132:135]\n" \
"ds_read_b128 a[196:199], v237 offset:2048\n" \
"v_cvt_pk_bf16_f32 v91, v94, v95\n" \
"v_mfma_f32_16x16x32_bf16 v[136:139], a[80:83], a[0:3], v[136:139]\n" \
"v_med3_f32 v208, v248, v252, s72\n" \
"v_med3_f32 v209, v249, v252, s72\n" \
"v_mfma_f32_16x16x32_bf16 v[136:139], a[84:87], a[4:7], v[136:139]\n" \
"ds_read_b128 a[200:203], v237 offset:4096\n" \
"v_med3_f32 v210, v250, v252, s72\n" \
"v_med3_f32 v211, v251, v252, s72\n" \
"v_mfma_f32_16x16x32_bf16 v[140:143], a[88:91], a[0:3], v[140:143]\n" \
"buffer_store_dwordx4 v[20:23], v234, s[56:59], s93 offen offset:2048 sc1\n" \
"v_mfma_f32_16x16x32_bf16 v[140:143], a[92:95], a[4:7], v[140:143]\n" \
"ds_read_b128 a[204:207], v237 offset:6144\n" \
"v_cvt_pk_bf16_f32 v112, v112, v113\n" \
"v_mfma_f32_16x16x32_bf16 v[144:147], a[96:99], a[0:3], v[144:147]\n" \
"v_cvt_pk_bf16_f32 v113, v114, v115\n" \
"v_cvt_pk_bf16_f32 v114, v116, v117\n" \
"v_mfma_f32_16x16x32_bf16 v[144:147], a[100:103], a[4:7], v[144:147]\n" \
"ds_read_b128 a[208:211], v237 offset:256\n" \
"v_cvt_pk_bf16_f32 v115, v118, v119\n" \
"v_cvt_pk_bf16_f32 v120, v120, v121\n" \
"v_mfma_f32_16x16x32_bf16 v[148:151], a[104:107], a[0:3], v[148:151]\n" \
"v_cvt_pk_bf16_f32 v121, v122, v123\n" \
"v_cvt_pk_bf16_f32 v122, v124, v125\n" \
"v_mfma_f32_16x16x32_bf16 v[148:151], a[108:111], a[4:7], v[148:151]\n" \
"ds_read_b128 a[212:215], v237 offset:2304\n" \
"v_cvt_pk_bf16_f32 v123, v126, v127\n" \
"v_mfma_f32_16x16x32_bf16 v[152:155], a[112:115], a[0:3], v[152:155]\n" \
"v_mfma_f32_16x16x32_bf16 v[152:155], a[116:119], a[4:7], v[152:155]\n" \
"ds_read_b128 a[216:219], v237 offset:4352\n" \
"v_cvt_pk_bf16_f32 v128, v128, v129\n" \
"v_mfma_f32_16x16x32_bf16 v[156:159], a[120:123], a[0:3], v[156:159]\n" \
"v_cvt_pk_bf16_f32 v129, v130, v131\n" \
"v_cvt_pk_bf16_f32 v130, v132, v133\n" \
"v_mfma_f32_16x16x32_bf16 v[156:159], a[124:127], a[4:7], v[156:159]\n" \
"ds_read_b128 a[220:223], v237 offset:6400\n" \
"v_cvt_pk_bf16_f32 v131, v134, v135\n" \
"v_cvt_pk_bf16_f32 v136, v136, v137\n" \
"v_mfma_f32_16x16x32_bf16 v[160:163], a[64:67], a[8:11], v[160:163]\n" \
"v_cvt_pk_bf16_f32 v137, v138, v139\n" \
"v_cvt_pk_bf16_f32 v138, v140, v141\n" \
"v_mfma_f32_16x16x32_bf16 v[160:163], a[68:71], a[12:15], v[160:163]\n" \
"ds_read_b128 a[224:227], v237 offset:8192\n" \
"v_cvt_pk_bf16_f32 v139, v142, v143\n" \
"v_mfma_f32_16x16x32_bf16 v[164:167], a[72:75], a[8:11], v[164:167]\n" \
"v_mfma_f32_16x16x32_bf16 v[164:167], a[76:79], a[12:15], v[164:167]\n" \
"ds_read_b128 a[228:231], v237 offset:10240\n" \
"v_mfma_f32_16x16x32_bf16 v[168:171], a[80:83], a[8:11], v[168:171]\n" \
"v_mfma_f32_16x16x32_bf16 v[168:171], a[84:87], a[12:15], v[168:171]\n" \
"ds_read_b128 a[232:235], v237 offset:12288\n" \
"v_cvt_pk_bf16_f32 v144, v144, v145\n" \
"v_cvt_pk_bf16_f32 v145, v146, v147\n" \
"v_mfma_f32_16x16x32_bf16 v[172:175], a[88:91], a[8:11], v[172:175]\n" \
"v_cvt_pk_bf16_f32 v146, v148, v149\n" \
"v_cvt_pk_bf16_f32 v147, v150, v151\n" \
"v_mfma_f32_16x16x32_bf16 v[172:175], a[92:95], a[12:15], v[172:175]\n" \
"ds_read_b128 a[236:239], v237 offset:14336\n" \
"v_cvt_pk_bf16_f32 v152, v152, v153\n" \
"v_mfma_f32_16x16x32_bf16 v[176:179], a[96:99], a[8:11], v[176:179]\n" \
"v_cvt_pk_bf16_f32 v153, v154, v155\n" \
"v_cvt_pk_bf16_f32 v154, v156, v157\n" \
"v_mfma_f32_16x16x32_bf16 v[176:179], a[100:103], a[12:15], v[176:179]\n" \
"ds_read_b128 a[240:243], v237 offset:8448\n" \
"v_cvt_pk_bf16_f32 v155, v158, v159\n" \
"v_mfma_f32_16x16x32_bf16 v[180:183], a[104:107], a[8:11], v[180:183]\n" \
"buffer_store_dwordx4 v[28:31], v234, s[56:59], s93 offen offset:3072 sc1\n" \
"v_mfma_f32_16x16x32_bf16 v[180:183], a[108:111], a[12:15], v[180:183]\n" \
"ds_read_b128 a[244:247], v237 offset:10496\n" \
"ds_write_b128 v239, v[128:131] offset:0\n" \
"v_mov_b32 v196, 0x42400000\n" \
"v_mfma_f32_16x16x32_bf16 v[184:187], a[112:115], a[8:11], v[184:187]\n" \
"v_mov_b32 v197, 0x42400000\n" \
"v_mov_b32 v198, 0x42400000\n" \
"v_mfma_f32_16x16x32_bf16 v[184:187], a[116:119], a[12:15], v[184:187]\n" \
"ds_read_b128 a[248:251], v237 offset:12544\n" \
"v_mov_b32 v199, 0x42400000\n" \
"v_cvt_pk_bf16_f32 v160, v160, v161\n" \
"v_mfma_f32_16x16x32_bf16 v[188:191], a[120:123], a[8:11], v[188:191]\n" \
"v_cvt_pk_bf16_f32 v161, v162, v163\n" \
"v_cvt_pk_bf16_f32 v162, v164, v165\n" \
"v_mfma_f32_16x16x32_bf16 v[188:191], a[124:127], a[12:15], v[188:191]\n" \
"s_waitcnt lgkmcnt(3)\n" \
"ds_read_b128 a[252:255], v237 offset:14592\n" \
"v_cvt_pk_bf16_f32 v163, v166, v167\n" \
"v_mfma_f32_16x16x32_bf16 v[16:19], a[192:195], v[96:99], v[208:211]\n" \
"v_cvt_pk_bf16_f32 v168, v168, v169\n" \
"v_cvt_pk_bf16_f32 v169, v170, v171\n" \
"v_mfma_f32_16x16x32_bf16 v[16:19], a[196:199], v[104:107], v[16:19]\n" \
"v_cvt_pk_bf16_f32 v170, v172, v173\n" \
"v_cvt_pk_bf16_f32 v171, v174, v175\n" \
"v_mfma_f32_16x16x32_bf16 v[16:19], a[200:203], v[112:115], v[16:19]\n" \
"v_cvt_pk_bf16_f32 v176, v176, v177\n" \
"v_cvt_pk_bf16_f32 v177, v178, v179\n" \
"v_mfma_f32_16x16x32_bf16 v[16:19], a[204:207], v[120:123], v[16:19]\n" \
"v_cvt_pk_bf16_f32 v178, v180, v181\n" \
"v_cvt_pk_bf16_f32 v179, v182, v183\n" \
"v_mfma_f32_16x16x32_bf16 v[32:35], a[204:207], v[152:155], v[196:199]\n" \
"v_cvt_pk_bf16_f32 v184, v184, v185\n" \
"v_cvt_pk_bf16_f32 v185, v186, v187\n" \
"v_mfma_f32_16x16x32_bf16 v[32:35], a[200:203], v[144:147], v[32:35]\n" \
"v_cvt_pk_bf16_f32 v186, v188, v189\n" \
"v_cvt_pk_bf16_f32 v187, v190, v191\n" \
"v_mfma_f32_16x16x32_bf16 v[32:35], a[196:199], v[136:139], v[32:35]\n" \
"s_add_u32 s93, s94, 17850368\n" \
"v_med3_f32 v212, v248, v253, v252\n" \
"v_med3_f32 v213, v249, v253, v252\n" \
"Lpg5012_%=:\n" \
"s_cbranch_execz Lpg6196_%=\n" \
"v_mfma_f32_16x16x32_bf16 v[32:35], a[192:195], v[128:131], v[32:35]\n" \
"buffer_store_dwordx4 v[0:3], v234, s[56:59], s93 offen offset:0 sc1\n" \
"v_mfma_f32_16x16x32_bf16 v[48:51], a[192:195], v[160:163], v[196:199]\n" \
"v_med3_f32 v214, v250, v253, v252\n" \
"v_med3_f32 v215, v251, v253, v252\n" \
"v_mfma_f32_16x16x32_bf16 v[48:51], a[196:199], v[168:171], v[48:51]\n" \
"ds_read_b128 a[192:195], v237 offset:16384\n" \
"v_med3_f32 v216, v248, v226, v253\n" \
"v_mfma_f32_16x16x32_bf16 v[48:51], a[200:203], v[176:179], v[48:51]\n" \
"ds_read_b128 a[196:199], v237 offset:18432\n" \
"v_med3_f32 v217, v249, v226, v253\n" \
"v_mfma_f32_16x16x32_bf16 v[48:51], a[204:207], v[184:187], v[48:51]\n" \
"ds_read_b128 a[200:203], v237 offset:20480\n" \
"ds_write_b128 v239, v[136:139] offset:1024\n" \
"v_mfma_f32_16x16x32_bf16 v[52:55], a[220:223], v[184:187], v[196:199]\n" \
"ds_read_b128 a[204:207], v237 offset:22528\n" \
"v_med3_f32 v218, v250, v226, v253\n" \
"v_mfma_f32_16x16x32_bf16 v[52:55], a[216:219], v[176:179], v[52:55]\n" \
"v_med3_f32 v219, v251, v226, v253\n" \
"v_med3_f32 v220, v248, v240, v226\n" \
"v_mfma_f32_16x16x32_bf16 v[52:55], a[212:215], v[168:171], v[52:55]\n" \
"ds_write_b128 v239, v[144:147] offset:2048\n" \
"v_mfma_f32_16x16x32_bf16 v[52:55], a[208:211], v[160:163], v[52:55]\n" \
"buffer_store_dwordx4 v[4:7], v234, s[56:59], s93 offen offset:1024 sc1\n" \
"v_mfma_f32_16x16x32_bf16 v[36:39], a[208:211], v[128:131], v[196:199]\n" \
"v_med3_f32 v221, v249, v240, v226\n" \
"v_med3_f32 v222, v250, v240, v226\n" \
"v_mfma_f32_16x16x32_bf16 v[36:39], a[212:215], v[136:139], v[36:39]\n" \
"v_med3_f32 v223, v251, v240, v226\n" \
"v_accvgpr_write_b32 a144, v96\n" \
"v_mfma_f32_16x16x32_bf16 v[36:39], a[216:219], v[144:147], v[36:39]\n" \
"v_accvgpr_write_b32 a145, v97\n" \
"v_accvgpr_write_b32 a146, v98\n" \
"v_mfma_f32_16x16x32_bf16 v[36:39], a[220:223], v[152:155], v[36:39]\n" \
"ds_write_b128 v239, v[152:155] offset:3072\n" \
"v_mfma_f32_16x16x32_bf16 v[20:23], a[220:223], v[120:123], v[212:215]\n" \
"v_accvgpr_write_b32 a147, v99\n" \
"v_accvgpr_write_b32 a148, v104\n" \
"v_mfma_f32_16x16x32_bf16 v[20:23], a[216:219], v[112:115], v[20:23]\n" \
"ds_read_b128 a[220:223], v237 offset:22784\n" \
"v_accvgpr_write_b32 a149, v105\n" \
"v_mfma_f32_16x16x32_bf16 v[20:23], a[212:215], v[104:107], v[20:23]\n" \
"ds_read_b128 a[216:219], v237 offset:20736\n" \
"v_accvgpr_write_b32 a150, v106\n" \
"v_mfma_f32_16x16x32_bf16 v[20:23], a[208:211], v[96:99], v[20:23]\n" \
"ds_read_b128 a[212:215], v237 offset:18688\n" \
"ds_write_b128 v239, v[160:163] offset:4096\n" \
"v_mfma_f32_16x16x32_bf16 v[24:27], a[224:227], v[96:99], v[216:219]\n" \
"ds_read_b128 a[208:211], v237 offset:16640\n" \
"v_accvgpr_write_b32 a151, v107\n" \
"v_mfma_f32_16x16x32_bf16 v[24:27], a[228:231], v[104:107], v[24:27]\n" \
"v_accvgpr_write_b32 a152, v112\n" \
"v_accvgpr_write_b32 a153, v113\n" \
"v_mfma_f32_16x16x32_bf16 v[24:27], a[232:235], v[112:115], v[24:27]\n" \
"ds_write_b128 v239, v[168:171] offset:5120\n" \
"v_mfma_f32_16x16x32_bf16 v[24:27], a[236:239], v[120:123], v[24:27]\n" \
"buffer_store_dwordx4 v[8:11], v234, s[56:59], s93 offen offset:2048 sc1\n" \
"v_mfma_f32_16x16x32_bf16 v[40:43], a[236:239], v[152:155], v[196:199]\n" \
"v_accvgpr_write_b32 a154, v114\n" \
"v_accvgpr_write_b32 a155, v115\n" \
"v_mfma_f32_16x16x32_bf16 v[40:43], a[232:235], v[144:147], v[40:43]\n" \
"v_accvgpr_write_b32 a156, v120\n" \
"v_accvgpr_write_b32 a157, v121\n" \
"v_mfma_f32_16x16x32_bf16 v[40:43], a[228:231], v[136:139], v[40:43]\n" \
"v_accvgpr_write_b32 a158, v122\n" \
"v_accvgpr_write_b32 a159, v123\n" \
"v_mfma_f32_16x16x32_bf16 v[40:43], a[224:227], v[128:131], v[40:43]\n" \
"ds_write_b128 v239, v[176:179] offset:6144\n" \
"v_mfma_f32_16x16x32_bf16 v[56:59], a[224:227], v[160:163], v[196:199]\n" \
"v_accvgpr_write_b32 a140, v88\n" \
"v_accvgpr_write_b32 a141, v89\n" \
"v_mfma_f32_16x16x32_bf16 v[56:59], a[228:231], v[168:171], v[56:59]\n" \
"ds_read_b128 a[224:227], v237 offset:24576\n" \
"v_accvgpr_write_b32 a142, v90\n" \
"v_mfma_f32_16x16x32_bf16 v[56:59], a[232:235], v[176:179], v[56:59]\n" \
"s_waitcnt lgkmcnt(2)\n" \
"ds_read_b128 a[228:231], v237 offset:26624\n" \
"v_accvgpr_write_b32 a143, v91\n" \
"v_mfma_f32_16x16x32_bf16 v[56:59], a[236:239], v[184:187], v[56:59]\n" \
"buffer_store_dwordx4 v[12:15], v234, s[56:59], s93 offen offset:3072 sc1\n" \
"ds_write_b128 v239, v[184:187] offset:7168\n" \
"v_mfma_f32_16x16x32_bf16 v[60:63], a[252:255], v[184:187], v[196:199]\n" \
"ds_read_b128 a[232:235], v237 offset:28672\n" \
"v_add_u32_e64 v233, s82, v235\n" \
"v_mfma_f32_16x16x32_bf16 v[60:63], a[248:251], v[176:179], v[60:63]\n" \
"s_waitcnt vmcnt(8)\n" \
"ds_read_b128 a[236:239], v237 offset:30720\n" \
"v_cmp_eq_u32 vcc, s60, v227\n" \
"v_mfma_f32_16x16x32_bf16 v[60:63], a[244:247], v[168:171], v[60:63]\n" \
"v_exp_f32_e64 v16, v16\n" \
"v_mfma_f32_16x16x32_bf16 v[60:63], a[240:243], v[160:163], v[60:63]\n" \
"s_andn2_b32 s70, 0xff, vcc_lo\n" \
"s_cbranch_scc1 Lpl1_%=\n" \
"Lpr1_%=:\n" \
"v_exp_f32 v17, v17\n" \
"v_exp_f32 v18, v18\n" \
"v_mfma_f32_16x16x32_bf16 v[44:47], a[240:243], v[128:131], v[196:199]\n" \
"v_exp_f32 v19, v19\n" \
"v_mfma_f32_16x16x32_bf16 v[44:47], a[244:247], v[136:139], v[44:47]\n" \
"s_add_u32 m0, s79, 32768\n" \
"v_exp_f32 v20, v20\n" \
"v_mfma_f32_16x16x32_bf16 v[44:47], a[248:251], v[144:147], v[44:47]\n" \
"s_add_u32 s71, s76, 0\n" \
"v_cvt_pk_bf16_f32 v16, v16, v17\n" \
"v_cvt_pk_bf16_f32 v17, v18, v19\n" \
"v_mfma_f32_16x16x32_bf16 v[44:47], a[252:255], v[152:155], v[44:47]\n" \
"buffer_load_dwordx4 v234, s[36:39], s71 offen offset:0 sc1 lds\n" \
"v_exp_f32 v21, v21\n" \
"v_exp_f32 v22, v22\n" \
"v_mfma_f32_16x16x32_bf16 v[28:31], a[252:255], v[120:123], v[220:223]\n" \
"v_exp_f32 v23, v23\n" \
"v_mfma_f32_16x16x32_bf16 v[28:31], a[248:251], v[112:115], v[28:31]\n" \
"ds_read_b128 a[252:255], v237 offset:30976\n" \
"v_cvt_pk_bf16_f32 v18, v20, v21\n" \
"v_mfma_f32_16x16x32_bf16 v[28:31], a[244:247], v[104:107], v[28:31]\n" \
"buffer_load_dwordx4 v234, s[36:39], s71 offen offset:1024 sc1 lds\n" \
"v_mfma_f32_16x16x32_bf16 v[28:31], a[240:243], v[96:99], v[28:31]\n" \
"ds_read_b128 a[244:247], v237 offset:26880\n" \
"ds_read_b128 a[248:251], v237 offset:28928\n" \
"v_exp_f32 v32, v32\n" \
"v_exp_f32 v33, v33\n" \
"v_mfma_f32_16x16x32_bf16 v[80:83], a[192:195], a[144:147], v[196:199]\n" \
"ds_read_b128 a[240:243], v237 offset:24832\n" \
"v_cvt_pk_bf16_f32 v19, v22, v23\n" \
"v_mfma_f32_16x16x32_bf16 v[80:83], a[196:199], a[148:151], v[80:83]\n" \
"buffer_load_dwordx4 v234, s[36:39], s71 offen offset:2048 sc1 lds\n" \
"v_mfma_f32_16x16x32_bf16 v[80:83], a[200:203], a[152:155], v[80:83]\n" \
"s_lshl_b32 s68, s63, 2\n" \
"s_add_u32 s68, s68, s95\n" \
"s_mov_b32 s69, 0\n" \
"v_mfma_f32_16x16x32_bf16 v[80:83], a[204:207], a[156:159], v[80:83]\n" \
"s_mov_b32 s89, s63\n" \
"ds_read_b128 a[44:47], v233 offset:15360\n" \
"v_mov_b32 v192, s60\n" \
"v_lshlrev_b32 v193, 28, s61\n" \
"v_exp_f32 v34, v34\n" \
"v_exp_f32 v35, v35\n" \
"v_mfma_f32_16x16x32_bf16 v[96:99], a[204:207], v[152:155], v[196:199]\n" \
"buffer_load_dwordx4 v234, s[36:39], s71 offen offset:3072 sc1 lds\n" \
"v_mfma_f32_16x16x32_bf16 v[96:99], a[200:203], v[144:147], v[96:99]\n" \
"s_add_u32 m0, s79, 49152\n" \
"s_mov_b32 s90, 0\n" \
"ds_read_b128 a[40:43], v233 offset:7168\n" \
"v_mfma_f32_16x16x32_bf16 v[96:99], a[196:199], v[136:139], v[96:99]\n" \
"s_add_u32 s71, s76, 17408\n" \
"v_exp_f32 v36, v36\n" \
"v_cvt_pk_bf16_f32 v32, v32, v33\n" \
"v_mfma_f32_16x16x32_bf16 v[96:99], a[192:195], v[128:131], v[96:99]\n" \
"buffer_load_dwordx4 v234, s[36:39], s71 offen offset:0 sc1 lds\n" \
"v_mfma_f32_16x16x32_bf16 v[112:115], a[192:195], v[160:163], v[196:199]\n" \
"ds_read_b128 a[36:39], v233 offset:14336\n" \
"v_cvt_pk_bf16_f32 v33, v34, v35\n" \
"v_mfma_f32_16x16x32_bf16 v[112:115], a[196:199], v[168:171], v[112:115]\n" \
"v_exp_f32 v37, v37\n" \
"v_mfma_f32_16x16x32_bf16 v[112:115], a[200:203], v[176:179], v[112:115]\n" \
"buffer_load_dwordx4 v234, s[36:39], s71 offen offset:1024 sc1 lds\n" \
"v_mfma_f32_16x16x32_bf16 v[112:115], a[204:207], v[184:187], v[112:115]\n" \
"ds_read_b128 a[32:35], v233 offset:6144\n" \
"v_exp_f32 v38, v38\n" \
"v_exp_f32 v39, v39\n" \
"v_mfma_f32_16x16x32_bf16 v[64:67], a[204:207], a[140:143], v[208:211]\n" \
"v_exp_f32 v48, v48\n" \
"v_mfma_f32_16x16x32_bf16 v[64:67], a[200:203], a[136:139], v[64:67]\n" \
"buffer_load_dwordx4 v234, s[36:39], s71 offen offset:2048 sc1 lds\n" \
"v_cvt_pk_bf16_f32 v34, v36, v37\n" \
"v_mfma_f32_16x16x32_bf16 v[64:67], a[196:199], a[132:135], v[64:67]\n" \
"ds_read_b128 a[204:207], v233 offset:9216\n" \
"v_cvt_pk_bf16_f32 v35, v38, v39\n" \
"v_mfma_f32_16x16x32_bf16 v[64:67], a[192:195], a[128:131], v[64:67]\n" \
"v_exp_f32 v49, v49\n" \
"v_exp_f32 v50, v50\n" \
"v_mfma_f32_16x16x32_bf16 v[68:71], a[208:211], a[128:131], v[212:215]\n" \
"buffer_load_dwordx4 v234, s[36:39], s71 offen offset:3072 sc1 lds\n" \
"v_mfma_f32_16x16x32_bf16 v[68:71], a[212:215], a[132:135], v[68:71]\n" \
"ds_read_b128 a[200:203], v233 offset:1024\n" \
"v_cvt_pk_bf16_f32 v48, v48, v49\n" \
"v_mfma_f32_16x16x32_bf16 v[68:71], a[216:219], a[136:139], v[68:71]\n" \
"s_add_u32 m0, s77, s80\n" \
"v_exp_f32 v51, v51\n" \
"v_mfma_f32_16x16x32_bf16 v[68:71], a[220:223], a[140:143], v[68:71]\n" \
"buffer_load_dwordx4 v234, s[40:43], s76 offen offset:0 sc1 lds\n" \
"v_exp_f32 v52, v52\n" \
"v_exp_f32 v53, v53\n" \
"v_mfma_f32_16x16x32_bf16 v[116:119], a[220:223], v[184:187], v[196:199]\n" \
"ds_read_b128 a[196:199], v233 offset:8192\n" \
"v_cvt_pk_bf16_f32 v49, v50, v51\n" \
"v_mfma_f32_16x16x32_bf16 v[116:119], a[216:219], v[176:179], v[116:119]\n" \
"ds_read_b128 a[160:163], v239 offset:0\n" \
"v_mfma_f32_16x16x32_bf16 v[116:119], a[212:215], v[168:171], v[116:119]\n" \
"s_add_u32 s71, s76, 17408\n" \
"ds_read_b128 a[192:195], v233 offset:0\n" \
"v_exp_f32 v54, v54\n" \
"v_mfma_f32_16x16x32_bf16 v[116:119], a[208:211], v[160:163], v[116:119]\n" \
"buffer_load_dwordx4 v234, s[40:43], s76 offen offset:1024 sc1 lds\n" \
"v_mfma_f32_16x16x32_bf16 v[100:103], a[208:211], v[128:131], v[196:199]\n" \
"ds_read_b128 a[164:167], v239 offset:1024\n" \
"v_cvt_pk_bf16_f32 v50, v52, v53\n" \
"v_mfma_f32_16x16x32_bf16 v[100:103], a[212:215], v[136:139], v[100:103]\n" \
"v_exp_f32 v55, v55\n" \
"v_mfma_f32_16x16x32_bf16 v[100:103], a[216:219], v[144:147], v[100:103]\n" \
"v_cvt_pk_bf16_f32 v51, v54, v55\n" \
"v_exp_f32 v24, v24\n" \
"v_mfma_f32_16x16x32_bf16 v[100:103], a[220:223], v[152:155], v[100:103]\n" \
"buffer_load_dwordx4 v234, s[40:43], s76 offen offset:2048 sc1 lds\n" \
"v_exp_f32 v25, v25\n" \
"v_mfma_f32_16x16x32_bf16 v[84:87], a[220:223], a[156:159], v[196:199]\n" \
"ds_read_b128 a[168:171], v239 offset:2048\n" \
"v_mfma_f32_16x16x32_bf16 v[84:87], a[216:219], a[152:155], v[84:87]\n" \
"ds_read_b128 a[220:223], v233 offset:11264\n" \
"v_mfma_f32_16x16x32_bf16 v[84:87], a[212:215], a[148:151], v[84:87]\n" \
"s_waitcnt lgkmcnt(2)\n" \
"v_exp_f32 v26, v26\n" \
"v_mfma_f32_16x16x32_bf16 v[84:87], a[208:211], a[144:147], v[84:87]\n" \
"buffer_load_dwordx4 v234, s[40:43], s76 offen offset:3072 sc1 lds\n" \
"v_exp_f32 v27, v27\n" \
"v_mfma_f32_16x16x32_bf16 v[88:91], a[224:227], a[144:147], v[196:199]\n" \
"ds_read_b128 a[216:219], v233 offset:3072\n" \
"v_cvt_pk_bf16_f32 v20, v24, v25\n" \
"v_mfma_f32_16x16x32_bf16 v[88:91], a[228:231], a[148:151], v[88:91]\n" \
"ds_read_b128 a[172:175], v239 offset:3072\n" \
"v_exp_f32 v28, v28\n" \
"v_mfma_f32_16x16x32_bf16 v[88:91], a[232:235], a[152:155], v[88:91]\n" \
"s_add_u32 m0, m0, 0x4000\n" \
"ds_read_b128 a[212:215], v233 offset:10240\n" \
"v_xor_b32 v248, 0x2000, v239\n" \
"v_mfma_f32_16x16x32_bf16 v[88:91], a[236:239], a[156:159], v[88:91]\n" \
"buffer_load_dwordx4 v234, s[40:43], s71 offen offset:0 sc1 lds\n" \
"v_exp_f32 v29, v29\n" \
"v_mfma_f32_16x16x32_bf16 v[104:107], a[236:239], v[152:155], v[196:199]\n" \
"ds_read_b128 a[208:211], v233 offset:2048\n" \
"v_lshlrev_b32 v252, s48, v234\n" \
"v_mfma_f32_16x16x32_bf16 v[104:107], a[232:235], v[144:147], v[104:107]\n" \
"ds_read_b128 a[176:179], v239 offset:4096\n" \
"v_mov_b32 v224, 0\n" \
"v_mfma_f32_16x16x32_bf16 v[104:107], a[228:231], v[136:139], v[104:107]\n" \
"v_mov_b32 v225, 0\n" \
"v_mov_b32 v228, 0\n" \
"v_mfma_f32_16x16x32_bf16 v[104:107], a[224:227], v[128:131], v[104:107]\n" \
"buffer_load_dwordx4 v234, s[40:43], s71 offen offset:1024 sc1 lds\n" \
"v_exp_f32 v30, v30\n" \
"v_cvt_pk_bf16_f32 v21, v26, v27\n" \
"v_mfma_f32_16x16x32_bf16 v[120:123], a[224:227], v[160:163], v[196:199]\n" \
"ds_read_b128 a[180:183], v239 offset:5120\n" \
"v_mov_b32 v229, 0\n" \
"v_mfma_f32_16x16x32_bf16 v[120:123], a[228:231], v[168:171], v[120:123]\n" \
"v_exp_f32 v31, v31\n" \
"v_mfma_f32_16x16x32_bf16 v[120:123], a[232:235], v[176:179], v[120:123]\n" \
"v_exp_f32 v40, v40\n" \
"v_cvt_pk_bf16_f32 v22, v28, v29\n" \
"v_mfma_f32_16x16x32_bf16 v[120:123], a[236:239], v[184:187], v[120:123]\n" \
"buffer_load_dwordx4 v234, s[40:43], s71 offen offset:2048 sc1 lds\n" \
"v_exp_f32 v41, v41\n" \
"v_mfma_f32_16x16x32_bf16 v[72:75], a[236:239], a[140:143], v[216:219]\n" \
"ds_read_b128 a[184:187], v239 offset:6144\n" \
"v_cvt_pk_bf16_f32 v23, v30, v31\n" \
"v_mfma_f32_16x16x32_bf16 v[72:75], a[232:235], a[136:139], v[72:75]\n" \
"ds_read_b128 a[236:239], v233 offset:13312\n" \
"v_mfma_f32_16x16x32_bf16 v[72:75], a[228:231], a[132:135], v[72:75]\n" \
"v_exp_f32 v42, v42\n" \
"v_mfma_f32_16x16x32_bf16 v[72:75], a[224:227], a[128:131], v[72:75]\n" \
"buffer_load_dwordx4 v234, s[40:43], s71 offen offset:3072 sc1 lds\n" \
"v_exp_f32 v43, v43\n" \
"v_exp_f32 v44, v44\n" \
"v_mfma_f32_16x16x32_bf16 v[76:79], a[240:243], a[128:131], v[220:223]\n" \
"ds_read_b128 a[232:235], v233 offset:5120\n" \
"v_cvt_pk_bf16_f32 v36, v40, v41\n" \
"v_mfma_f32_16x16x32_bf16 v[76:79], a[244:247], a[132:135], v[76:79]\n" \
"ds_read_b128 a[188:191], v239 offset:7168\n" \
"v_cvt_pk_bf16_f32 v37, v42, v43\n" \
"v_mfma_f32_16x16x32_bf16 v[76:79], a[248:251], a[136:139], v[76:79]\n" \
"ds_read_b128 a[228:231], v233 offset:12288\n" \
"v_mfma_f32_16x16x32_bf16 v[76:79], a[252:255], a[140:143], v[76:79]\n" \
"v_exp_f32 v45, v45\n" \
"v_exp_f32 v46, v46\n" \
"v_mfma_f32_16x16x32_bf16 v[124:127], a[252:255], v[184:187], v[196:199]\n" \
"ds_read_b128 a[224:227], v233 offset:4096\n" \
"v_cvt_pk_bf16_f32 v38, v44, v45\n" \
"v_mfma_f32_16x16x32_bf16 v[124:127], a[248:251], v[176:179], v[124:127]\n" \
"v_exp_f32 v47, v47\n" \
"v_mfma_f32_16x16x32_bf16 v[124:127], a[244:247], v[168:171], v[124:127]\n" \
"v_exp_f32 v56, v56\n" \
"v_exp_f32 v57, v57\n" \
"v_mfma_f32_16x16x32_bf16 v[124:127], a[240:243], v[160:163], v[124:127]\n" \
"v_exp_f32 v58, v58\n" \
"v_cvt_pk_bf16_f32 v39, v46, v47\n" \
"v_mfma_f32_16x16x32_bf16 v[108:111], a[240:243], v[128:131], v[196:199]\n" \
"v_exp_f32 v59, v59\n" \
"v_mfma_f32_16x16x32_bf16 v[108:111], a[244:247], v[136:139], v[108:111]\n" \
"v_exp_f32 v60, v60\n" \
"v_mfma_f32_16x16x32_bf16 v[108:111], a[248:251], v[144:147], v[108:111]\n" \
"v_cvt_pk_bf16_f32 v52, v56, v57\n" \
"v_cvt_pk_bf16_f32 v53, v58, v59\n" \
"v_mfma_f32_16x16x32_bf16 v[108:111], a[252:255], v[152:155], v[108:111]\n" \
"v_exp_f32 v61, v61\n" \
"v_exp_f32 v62, v62\n" \
"v_exp_f32 v63, v63\n" \
"v_mfma_f32_16x16x32_bf16 v[92:95], a[252:255], a[156:159], v[196:199]\n" \
"v_add_u32 v232, s91, v237\n" \
"v_cvt_pk_bf16_f32 v54, v60, v61\n" \
"v_mfma_f32_16x16x32_bf16 v[92:95], a[248:251], a[152:155], v[92:95]\n" \
"v_cvt_pk_bf16_f32 v55, v62, v63\n" \
"v_mfma_f32_16x16x32_bf16 v[92:95], a[244:247], a[148:151], v[92:95]\n" \
"s_waitcnt vmcnt(8) lgkmcnt(0)\n" \
"s_barrier\n" \
"v_mfma_f32_16x16x32_bf16 v[92:95], a[240:243], a[144:147], v[92:95]\n" \
"buffer_store_dword v192, v193, s[56:59], s68 offen sc1\n" \
"v_mfma_f32_16x16x32_bf16 v[160:163], a[192:195], v[16:19], 0\n" \
"s_add_u32 m0, s74, s79\n" \
"ds_read_b128 a[240:243], v232 offset:8448\n" \
"v_xor_b32 v233, 0x4000, v233\n" \
"v_mfma_f32_16x16x32_bf16 v[160:163], a[196:199], v[20:23], v[160:163]\n" \
"buffer_load_dwordx4 v234, s[36:39], s73 offen offset:0 sc1 lds\n" \
"v_exp_f32 v80, v80\n" \
"v_mfma_f32_16x16x32_bf16 a[64:67], a[192:195], v[32:35], 0\n" \
"ds_read_b128 a[244:247], v232 offset:10496\n" \
"v_mfma_f32_16x16x32_bf16 a[64:67], a[196:199], v[36:39], a[64:67]\n" \
"ds_read_b128 a[0:3], v248 offset:0\n" \
"v_exp_f32 v81, v81\n" \
"v_mfma_f32_16x16x32_bf16 a[96:99], a[192:195], v[48:51], 0\n" \
"v_exp_f32 v82, v82\n" \
"v_mfma_f32_16x16x32_bf16 a[96:99], a[196:199], v[52:55], a[96:99]\n" \
"s_add_u32 s71, s73, 17408\n" \
"ds_read_b128 a[192:195], v232 offset:0\n" \
"v_cvt_pk_bf16_f32 v80, v80, v81\n" \
"v_mfma_f32_16x16x32_bf16 a[100:103], a[200:203], v[48:51], 0\n" \
"buffer_load_dwordx4 v234, s[36:39], s73 offen offset:1024 sc1 lds\n" \
"v_mfma_f32_16x16x32_bf16 a[100:103], a[204:207], v[52:55], a[100:103]\n" \
"ds_read_b128 a[196:199], v232 offset:2048\n" \
"v_exp_f32 v83, v83\n" \
"v_mfma_f32_16x16x32_bf16 a[68:71], a[200:203], v[32:35], 0\n" \
"ds_read_b128 a[4:7], v248 offset:1024\n" \
"v_mfma_f32_16x16x32_bf16 a[68:71], a[204:207], v[36:39], a[68:71]\n" \
"v_exp_f32 v84, v84\n" \
"v_exp_f32 v85, v85\n" \
"v_mfma_f32_16x16x32_bf16 v[164:167], a[200:203], v[16:19], 0\n" \
"ds_read_b128 a[8:11], v248 offset:2048\n" \
"v_cvt_pk_bf16_f32 v81, v82, v83\n" \
"v_mfma_f32_16x16x32_bf16 v[164:167], a[204:207], v[20:23], v[164:167]\n" \
"buffer_load_dwordx4 v234, s[36:39], s73 offen offset:2048 sc1 lds\n" \
"v_mfma_f32_16x16x32_bf16 v[168:171], a[208:211], v[16:19], 0\n" \
"ds_read_b128 a[200:203], v232 offset:4096\n" \
"v_mfma_f32_16x16x32_bf16 v[168:171], a[212:215], v[20:23], v[168:171]\n" \
"ds_read_b128 a[204:207], v232 offset:6144\n" \
"v_exp_f32 v86, v86\n" \
"v_mfma_f32_16x16x32_bf16 a[72:75], a[208:211], v[32:35], 0\n" \
"ds_read_b128 a[12:15], v248 offset:3072\n" \
"v_cvt_pk_bf16_f32 v82, v84, v85\n" \
"v_mfma_f32_16x16x32_bf16 a[72:75], a[212:215], v[36:39], a[72:75]\n" \
"v_exp_f32 v87, v87\n" \
"v_exp_f32 v96, v96\n" \
"v_mfma_f32_16x16x32_bf16 a[104:107], a[208:211], v[48:51], 0\n" \
"buffer_load_dwordx4 v234, s[36:39], s73 offen offset:3072 sc1 lds\n" \
"v_mfma_f32_16x16x32_bf16 a[104:107], a[212:215], v[52:55], a[104:107]\n" \
"ds_read_b128 a[208:211], v232 offset:256\n" \
"v_cvt_pk_bf16_f32 v83, v86, v87\n" \
"v_smfmac_f32_16x16x64_bf16 a[52:55], v[244:247], v[16:23], v243\n" \
"ds_read_b128 a[212:215], v232 offset:2304\n" \
"v_exp_f32 v97, v97\n" \
"v_mfma_f32_16x16x32_bf16 a[108:111], a[216:219], v[48:51], 0\n" \
"ds_read_b128 a[16:19], v248 offset:4096\n" \
"v_mfma_f32_16x16x32_bf16 a[108:111], a[220:223], v[52:55], a[108:111]\n" \
"s_add_u32 m0, m0, 0x4000\n" \
"v_exp_f32 v98, v98\n" \
"v_mfma_f32_16x16x32_bf16 a[76:79], a[216:219], v[32:35], 0\n" \
"buffer_load_dwordx4 v234, s[36:39], s71 offen offset:0 sc1 lds\n" \
"v_mfma_f32_16x16x32_bf16 a[76:79], a[220:223], v[36:39], a[76:79]\n" \
"ds_read_b128 a[20:23], v248 offset:5120\n" \
"v_cvt_pk_bf16_f32 v96, v96, v97\n" \
"v_exp_f32 v99, v99\n" \
"v_mfma_f32_16x16x32_bf16 v[172:175], a[216:219], v[16:19], 0\n" \
"v_exp_f32 v100, v100\n" \
"v_mfma_f32_16x16x32_bf16 v[172:175], a[220:223], v[20:23], v[172:175]\n" \
"ds_read_b128 a[216:219], v232 offset:4352\n" \
"v_cvt_pk_bf16_f32 v97, v98, v99\n" \
"v_mfma_f32_32x32x16_bf16 v[128:143], a[48:51], a[48:51], 0\n" \
"ds_read_b128 a[220:223], v232 offset:6400\n" \
"v_exp_f32 v101, v101\n" \
"v_mfma_f32_16x16x32_bf16 v[176:179], a[224:227], v[16:19], 0\n" \
"buffer_load_dwordx4 v234, s[36:39], s71 offen offset:1024 sc1 lds\n" \
"v_mfma_f32_16x16x32_bf16 v[176:179], a[228:231], v[20:23], v[176:179]\n" \
"ds_read_b128 a[24:27], v248 offset:6144\n" \
"v_exp_f32 v102, v102\n" \
"v_mfma_f32_16x16x32_bf16 a[80:83], a[224:227], v[32:35], 0\n" \
"v_exp_f32 v103, v103\n" \
"v_mfma_f32_16x16x32_bf16 a[80:83], a[228:231], v[36:39], a[80:83]\n" \
"ds_read_b128 a[28:31], v248 offset:7168\n" \
"v_cvt_pk_bf16_f32 v98, v100, v101\n" \
"v_exp_f32 v112, v112\n" \
"v_mfma_f32_16x16x32_bf16 a[112:115], a[224:227], v[48:51], 0\n" \
"v_exp_f32 v113, v113\n" \
"v_mfma_f32_16x16x32_bf16 a[112:115], a[228:231], v[52:55], a[112:115]\n" \
"buffer_load_dwordx4 v234, s[36:39], s71 offen offset:2048 sc1 lds\n" \
"v_smfmac_f32_16x16x64_bf16 v[224:227], v[244:247], v[32:39], v243\n" \
"ds_read_b128 a[224:227], v232 offset:8192\n" \
"v_cvt_pk_bf16_f32 v99, v102, v103\n" \
"v_mfma_f32_16x16x32_bf16 a[116:119], a[232:235], v[48:51], 0\n" \
"ds_read_b128 a[228:231], v232 offset:10240\n" \
"v_mfma_f32_16x16x32_bf16 a[116:119], a[236:239], v[52:55], a[116:119]\n" \
"v_exp_f32 v114, v114\n" \
"v_exp_f32 v115, v115\n" \
"v_mfma_f32_16x16x32_bf16 a[84:87], a[232:235], v[32:35], 0\n" \
"v_exp_f32 v116, v116\n" \
"v_mfma_f32_16x16x32_bf16 a[84:87], a[236:239], v[36:39], a[84:87]\n" \
"buffer_load_dwordx4 v234, s[36:39], s71 offen offset:3072 sc1 lds\n" \
"v_cvt_pk_bf16_f32 v112, v112, v113\n" \
"v_mfma_f32_16x16x32_bf16 v[180:183], a[232:235], v[16:19], 0\n" \
"v_exp_f32 v117, v117\n" \
"v_mfma_f32_16x16x32_bf16 v[180:183], a[236:239], v[20:23], v[180:183]\n" \
"s_mov_b32 s76, s73\n" \
"s_add_u32 s73, s73, 34816\n" \
"ds_read_b128 a[232:235], v232 offset:12288\n" \
"v_mfma_f32_32x32x16_bf16 v[144:159], a[48:51], a[48:51], 0\n" \
"s_cmp_eq_u32 s73, s83\n" \
"s_cselect_b32 s70, 34816, 0\n" \
"ds_read_b128 a[236:239], v232 offset:14336\n" \
"v_exp_f32 v118, v118\n" \
"v_mfma_f32_16x16x32_bf16 v[184:187], a[32:35], v[16:19], 0\n" \
"s_add_u32 s73, s73, s70\n" \
"s_xor_b32 s74, s74, 0x8000\n" \
"ds_read_b128 a[248:251], v232 offset:12544\n" \
"v_mfma_f32_16x16x32_bf16 v[184:187], a[36:39], v[20:23], v[184:187]\n" \
"s_xor_b32 s77, s77, 0x8000\n" \
"ds_read_b128 a[252:255], v232 offset:14592\n" \
"v_cvt_pk_bf16_f32 v113, v114, v115\n" \
"v_mfma_f32_16x16x32_bf16 a[88:91], a[32:35], v[32:35], 0\n" \
"v_exp_f32 v119, v119\n" \
"v_mfma_f32_16x16x32_bf16 a[88:91], a[36:39], v[36:39], a[88:91]\n" \
"v_exp_f32 v64, v64\n" \
"v_cvt_pk_bf16_f32 v114, v116, v117\n" \
"v_mfma_f32_16x16x32_bf16 a[120:123], a[32:35], v[48:51], 0\n" \
"v_exp_f32 v65, v65\n" \
"v_mfma_f32_16x16x32_bf16 a[120:123], a[36:39], v[52:55], a[120:123]\n" \
"v_exp_f32 v66, v66\n" \
"v_cvt_pk_bf16_f32 v115, v118, v119\n" \
"v_smfmac_f32_16x16x64_bf16 v[228:231], v[244:247], v[48:55], v243\n" \
"v_cvt_pk_bf16_f32 v64, v64, v65\n" \
"v_mfma_f32_16x16x32_bf16 a[124:127], a[40:43], v[48:51], 0\n" \
"v_exp_f32 v67, v67\n" \
"v_mfma_f32_16x16x32_bf16 a[124:127], a[44:47], v[52:55], a[124:127]\n" \
"v_exp_f32 v68, v68\n" \
"v_exp_f32 v69, v69\n" \
"v_mfma_f32_16x16x32_bf16 a[92:95], a[40:43], v[32:35], 0\n" \
"v_exp_f32 v70, v70\n" \
"v_mfma_f32_16x16x32_bf16 a[92:95], a[44:47], v[36:39], a[92:95]\n" \
"v_cvt_pk_bf16_f32 v65, v66, v67\n" \
"v_cvt_pk_bf16_f32 v66, v68, v69\n" \
"v_mfma_f32_16x16x32_bf16 v[188:191], a[40:43], v[16:19], 0\n" \
"s_cmp_eq_u32 s63, 0\n" \
"v_exp_f32 v71, v71\n" \
"v_mfma_f32_16x16x32_bf16 v[188:191], a[44:47], v[20:23], v[188:191]\n" \
"s_waitcnt lgkmcnt(0)\n" \
"v_cvt_pk_bf16_f32 v67, v70, v71\n" \
"s_cbranch_scc1 Lloop2_%=\n" \
"Lloop_%=:\n" \
"v_mfma_f32_16x16x32_bf16 v[0:3], a[192:195], a[128:131], v[196:199]\n" \
"v_exp_f32 v72, v72\n" \
"v_mfma_f32_16x16x32_bf16 v[0:3], a[196:199], a[132:135], v[0:3]\n" \
"v_mfma_f32_16x16x32_bf16 v[0:3], a[200:203], a[136:139], v[0:3]\n" \
"v_exp_f32 v73, v73\n" \
"v_mfma_f32_16x16x32_bf16 v[0:3], a[204:207], a[140:143], v[0:3]\n" \
"ds_read_b128 a[32:35], v233 offset:6144\n" \
"v_cvt_pk_bf16_f32 v68, v72, v73\n" \
"v_mfma_f32_16x16x32_bf16 v[16:19], a[204:207], a[156:159], v[196:199]\n" \
"v_exp_f32 v74, v74\n" \
"v_mfma_f32_16x16x32_bf16 v[16:19], a[200:203], a[152:155], v[16:19]\n" \
"global_load_dword v253, v252, s[44:45]\n" \
"v_mfma_f32_16x16x32_bf16 v[16:19], a[196:199], a[148:151], v[16:19]\n" \
"s_sub_u32 s46, s46, 1\n" \
"v_exp_f32 v75, v75\n" \
"v_mfma_f32_16x16x32_bf16 v[16:19], a[192:195], a[144:147], v[16:19]\n" \
"ds_read_b128 a[36:39], v233 offset:14336\n" \
"v_cvt_pk_bf16_f32 v69, v74, v75\n" \
"v_mfma_f32_16x16x32_bf16 v[32:35], a[192:195], a[160:163], v[196:199]\n" \
"v_exp_f32 v76, v76\n" \
"v_mfma_f32_16x16x32_bf16 v[32:35], a[196:199], a[164:167], v[32:35]\n" \
"s_cmp_gt_i32 s46, 0\n" \
"s_cselect_b32 s47, 0x100000, 0\n" \
"v_mfma_f32_16x16x32_bf16 v[32:35], a[200:203], a[168:171], v[32:35]\n" \
"v_exp_f32 v77, v77\n" \
"v_mfma_f32_16x16x32_bf16 v[32:35], a[204:207], a[172:175], v[32:35]\n" \
"s_add_u32 s44, s44, s47\n" \
"s_addc_u32 s45, s45, 0\n" \
"ds_read_b128 a[40:43], v233 offset:7168\n" \
"v_cvt_pk_bf16_f32 v70, v76, v77\n" \
"v_mfma_f32_16x16x32_bf16 v[48:51], a[204:207], a[188:191], v[196:199]\n" \
"v_exp_f32 v78, v78\n" \
"v_mfma_f32_16x16x32_bf16 v[48:51], a[200:203], a[184:187], v[48:51]\n" \
"v_xor_b32 v232, 0x4000, v232\n" \
"v_mfma_f32_16x16x32_bf16 v[48:51], a[196:199], a[180:183], v[48:51]\n" \
"v_exp_f32 v79, v79\n" \
"v_mfma_f32_16x16x32_bf16 v[48:51], a[192:195], a[176:179], v[48:51]\n" \
"ds_read_b128 a[204:207], v233 offset:9216\n" \
"v_cvt_pk_bf16_f32 v71, v78, v79\n" \
"v_mfma_f32_16x16x32_bf16 v[52:55], a[208:211], a[176:179], v[196:199]\n" \
"v_exp_f32 v88, v88\n" \
"v_mfma_f32_16x16x32_bf16 v[52:55], a[212:215], a[180:183], v[52:55]\n" \
"v_mfma_f32_16x16x32_bf16 v[52:55], a[216:219], a[184:187], v[52:55]\n" \
"v_exp_f32 v89, v89\n" \
"v_mfma_f32_16x16x32_bf16 v[52:55], a[220:223], a[188:191], v[52:55]\n" \
"ds_read_b128 a[200:203], v233 offset:1024\n" \
"v_cvt_pk_bf16_f32 v84, v88, v89\n" \
"v_mfma_f32_16x16x32_bf16 v[36:39], a[220:223], a[172:175], v[196:199]\n" \
"v_exp_f32 v90, v90\n" \
"v_mfma_f32_16x16x32_bf16 v[36:39], a[216:219], a[168:171], v[36:39]\n" \
"v_mfma_f32_16x16x32_bf16 v[36:39], a[212:215], a[164:167], v[36:39]\n" \
"v_exp_f32 v91, v91\n" \
"v_mfma_f32_16x16x32_bf16 v[36:39], a[208:211], a[160:163], v[36:39]\n" \
"ds_read_b128 a[196:199], v233 offset:8192\n" \
"v_cvt_pk_bf16_f32 v85, v90, v91\n" \
"v_mfma_f32_16x16x32_bf16 v[20:23], a[208:211], a[144:147], v[196:199]\n" \
"v_exp_f32 v92, v92\n" \
"v_mfma_f32_16x16x32_bf16 v[20:23], a[212:215], a[148:151], v[20:23]\n" \
"v_mfma_f32_16x16x32_bf16 v[20:23], a[216:219], a[152:155], v[20:23]\n" \
"v_exp_f32 v93, v93\n" \
"v_mfma_f32_16x16x32_bf16 v[20:23], a[220:223], a[156:159], v[20:23]\n" \
"ds_read_b128 a[192:195], v233 offset:0\n" \
"v_cvt_pk_bf16_f32 v86, v92, v93\n" \
"v_mfma_f32_16x16x32_bf16 v[4:7], a[220:223], a[140:143], v[196:199]\n" \
"v_exp_f32 v94, v94\n" \
"v_mfma_f32_16x16x32_bf16 v[4:7], a[216:219], a[136:139], v[4:7]\n" \
"v_mfma_f32_16x16x32_bf16 v[4:7], a[212:215], a[132:135], v[4:7]\n" \
"s_waitcnt lgkmcnt(7)\n" \
"v_exp_f32 v95, v95\n" \
"v_mfma_f32_16x16x32_bf16 v[4:7], a[208:211], a[128:131], v[4:7]\n" \
"ds_read_b128 a[220:223], v233 offset:11264\n" \
"v_cvt_pk_bf16_f32 v87, v94, v95\n" \
"v_mfma_f32_16x16x32_bf16 v[8:11], a[224:227], a[128:131], v[196:199]\n" \
"v_exp_f32 v104, v104\n" \
"v_mfma_f32_16x16x32_bf16 v[8:11], a[228:231], a[132:135], v[8:11]\n" \
"v_mfma_f32_16x16x32_bf16 v[8:11], a[232:235], a[136:139], v[8:11]\n" \
"v_exp_f32 v105, v105\n" \
"v_mfma_f32_16x16x32_bf16 v[8:11], a[236:239], a[140:143], v[8:11]\n" \
"ds_read_b128 a[216:219], v233 offset:3072\n" \
"v_cvt_pk_bf16_f32 v100, v104, v105\n" \
"v_mfma_f32_16x16x32_bf16 v[24:27], a[236:239], a[156:159], v[196:199]\n" \
"v_exp_f32 v106, v106\n" \
"v_mfma_f32_16x16x32_bf16 v[24:27], a[232:235], a[152:155], v[24:27]\n" \
"v_mfma_f32_16x16x32_bf16 v[24:27], a[228:231], a[148:151], v[24:27]\n" \
"v_exp_f32 v107, v107\n" \
"v_mfma_f32_16x16x32_bf16 v[24:27], a[224:227], a[144:147], v[24:27]\n" \
"ds_read_b128 a[212:215], v233 offset:10240\n" \
"v_cvt_pk_bf16_f32 v101, v106, v107\n" \
"v_mfma_f32_16x16x32_bf16 v[40:43], a[224:227], a[160:163], v[196:199]\n" \
"v_exp_f32 v108, v108\n" \
"v_mfma_f32_16x16x32_bf16 v[40:43], a[228:231], a[164:167], v[40:43]\n" \
"v_mfma_f32_16x16x32_bf16 v[40:43], a[232:235], a[168:171], v[40:43]\n" \
"v_exp_f32 v109, v109\n" \
"v_mfma_f32_16x16x32_bf16 v[40:43], a[236:239], a[172:175], v[40:43]\n" \
"ds_read_b128 a[208:211], v233 offset:2048\n" \
"v_cvt_pk_bf16_f32 v102, v108, v109\n" \
"v_mfma_f32_16x16x32_bf16 v[56:59], a[236:239], a[188:191], v[196:199]\n" \
"v_exp_f32 v110, v110\n" \
"v_mfma_f32_16x16x32_bf16 v[56:59], a[232:235], a[184:187], v[56:59]\n" \
"v_mfma_f32_16x16x32_bf16 v[56:59], a[228:231], a[180:183], v[56:59]\n" \
"v_exp_f32 v111, v111\n" \
"v_mfma_f32_16x16x32_bf16 v[56:59], a[224:227], a[176:179], v[56:59]\n" \
"ds_read_b128 a[236:239], v233 offset:13312\n" \
"v_cvt_pk_bf16_f32 v103, v110, v111\n" \
"v_mfma_f32_16x16x32_bf16 v[60:63], a[240:243], a[176:179], v[196:199]\n" \
"v_exp_f32 v120, v120\n" \
"v_mfma_f32_16x16x32_bf16 v[60:63], a[244:247], a[180:183], v[60:63]\n" \
"v_mfma_f32_16x16x32_bf16 v[60:63], a[248:251], a[184:187], v[60:63]\n" \
"v_exp_f32 v121, v121\n" \
"v_mfma_f32_16x16x32_bf16 v[60:63], a[252:255], a[188:191], v[60:63]\n" \
"ds_read_b128 a[232:235], v233 offset:5120\n" \
"v_cvt_pk_bf16_f32 v116, v120, v121\n" \
"v_mfma_f32_16x16x32_bf16 v[44:47], a[252:255], a[172:175], v[196:199]\n" \
"v_exp_f32 v122, v122\n" \
"v_mfma_f32_16x16x32_bf16 v[44:47], a[248:251], a[168:171], v[44:47]\n" \
"v_mfma_f32_16x16x32_bf16 v[44:47], a[244:247], a[164:167], v[44:47]\n" \
"v_exp_f32 v123, v123\n" \
"v_mfma_f32_16x16x32_bf16 v[44:47], a[240:243], a[160:163], v[44:47]\n" \
"ds_read_b128 a[228:231], v233 offset:12288\n" \
"v_cvt_pk_bf16_f32 v117, v122, v123\n" \
"v_mfma_f32_16x16x32_bf16 v[28:31], a[240:243], a[144:147], v[196:199]\n" \
"v_exp_f32 v124, v124\n" \
"v_mfma_f32_16x16x32_bf16 v[28:31], a[244:247], a[148:151], v[28:31]\n" \
"v_mfma_f32_16x16x32_bf16 v[28:31], a[248:251], a[152:155], v[28:31]\n" \
"v_exp_f32 v125, v125\n" \
"v_mfma_f32_16x16x32_bf16 v[28:31], a[252:255], a[156:159], v[28:31]\n" \
"ds_read_b128 a[224:227], v233 offset:4096\n" \
"v_cvt_pk_bf16_f32 v118, v124, v125\n" \
"v_mfma_f32_16x16x32_bf16 v[12:15], a[252:255], a[140:143], v[196:199]\n" \
"v_exp_f32 v126, v126\n" \
"v_mfma_f32_16x16x32_bf16 v[12:15], a[248:251], a[136:139], v[12:15]\n" \
"Lpg5598_%=:\n" \
"s_cbranch_execz Lpg6789_%=\n" \
"v_mfma_f32_16x16x32_bf16 v[12:15], a[244:247], a[132:135], v[12:15]\n" \
"s_waitcnt lgkmcnt(2)\n" \
"v_exp_f32 v127, v127\n" \
"v_mfma_f32_16x16x32_bf16 v[12:15], a[240:243], a[128:131], v[12:15]\n" \
"ds_read_b128 a[44:47], v233 offset:15360\n" \
"v_cvt_pk_bf16_f32 v119, v126, v127\n" \
"v_mfma_f32_16x16x32_bf16 v[128:131], a[192:195], v[64:67], v[128:131]\n" \
"v_exp_f32 v0, v0\n" \
"v_mfma_f32_16x16x32_bf16 v[128:131], a[196:199], v[68:71], v[128:131]\n" \
"ds_read_b128 a[244:247], v232 offset:10496\n" \
"v_mfma_f32_16x16x32_bf16 v[160:163], a[192:195], v[80:83], v[160:163]\n" \
"v_exp_f32 v1, v1\n" \
"v_mfma_f32_16x16x32_bf16 v[160:163], a[196:199], v[84:87], v[160:163]\n" \
"ds_read_b128 a[240:243], v232 offset:8448\n" \
"v_cvt_pk_bf16_f32 v0, v0, v1\n" \
"v_mfma_f32_16x16x32_bf16 a[64:67], a[192:195], v[96:99], a[64:67]\n" \
"v_exp_f32 v2, v2\n" \
"v_mfma_f32_16x16x32_bf16 a[64:67], a[196:199], v[100:103], a[64:67]\n" \
"ds_read_b128 a[248:251], v232 offset:12544\n" \
"v_xor_b32 v233, 0xc000, v233\n" \
"v_mfma_f32_16x16x32_bf16 a[96:99], a[192:195], v[112:115], a[96:99]\n" \
"v_exp_f32 v3, v3\n" \
"v_mfma_f32_16x16x32_bf16 a[96:99], a[196:199], v[116:119], a[96:99]\n" \
"ds_read_b128 a[192:195], v232 offset:0\n" \
"v_cvt_pk_bf16_f32 v1, v2, v3\n" \
"v_mfma_f32_16x16x32_bf16 a[100:103], a[200:203], v[112:115], a[100:103]\n" \
"v_exp_f32 v4, v4\n" \
"v_mfma_f32_16x16x32_bf16 a[100:103], a[204:207], v[116:119], a[100:103]\n" \
"ds_read_b128 a[252:255], v232 offset:14592\n" \
"v_mfma_f32_16x16x32_bf16 a[68:71], a[200:203], v[96:99], a[68:71]\n" \
"v_exp_f32 v5, v5\n" \
"v_mfma_f32_16x16x32_bf16 a[68:71], a[204:207], v[100:103], a[68:71]\n" \
"ds_read_b128 a[196:199], v232 offset:2048\n" \
"v_cvt_pk_bf16_f32 v2, v4, v5\n" \
"v_smfmac_f32_16x16x64_bf16 a[48:51], v[244:247], v[64:71], v243\n" \
"v_mfma_f32_16x16x32_bf16 v[164:167], a[200:203], v[80:83], v[164:167]\n" \
"v_exp_f32 v6, v6\n" \
"v_mfma_f32_16x16x32_bf16 v[164:167], a[204:207], v[84:87], v[164:167]\n" \
"v_mfma_f32_16x16x32_bf16 v[132:135], a[200:203], v[64:67], v[132:135]\n" \
"v_exp_f32 v7, v7\n" \
"v_mfma_f32_16x16x32_bf16 v[132:135], a[204:207], v[68:71], v[132:135]\n" \
"ds_read_b128 a[200:203], v232 offset:4096\n" \
"v_cvt_pk_bf16_f32 v3, v6, v7\n" \
"v_mfma_f32_16x16x32_bf16 v[136:139], a[208:211], v[64:67], v[136:139]\n" \
"v_exp_f32 v16, v16\n" \
"v_mfma_f32_16x16x32_bf16 v[136:139], a[212:215], v[68:71], v[136:139]\n" \
"v_mfma_f32_16x16x32_bf16 v[168:171], a[208:211], v[80:83], v[168:171]\n" \
"v_exp_f32 v17, v17\n" \
"v_mfma_f32_16x16x32_bf16 v[168:171], a[212:215], v[84:87], v[168:171]\n" \
"ds_read_b128 a[204:207], v232 offset:6144\n" \
"v_cvt_pk_bf16_f32 v16, v16, v17\n" \
"v_mfma_f32_16x16x32_bf16 a[72:75], a[208:211], v[96:99], a[72:75]\n" \
"v_exp_f32 v18, v18\n" \
"v_mfma_f32_16x16x32_bf16 a[72:75], a[212:215], v[100:103], a[72:75]\n" \
"v_mfma_f32_16x16x32_bf16 a[104:107], a[208:211], v[112:115], a[104:107]\n" \
"v_exp_f32 v19, v19\n" \
"v_mfma_f32_16x16x32_bf16 a[104:107], a[212:215], v[116:119], a[104:107]\n" \
"ds_read_b128 a[208:211], v232 offset:256\n" \
"v_cvt_pk_bf16_f32 v17, v18, v19\n" \
"v_mfma_f32_16x16x32_bf16 a[108:111], a[216:219], v[112:115], a[108:111]\n" \
"v_exp_f32 v20, v20\n" \
"v_mfma_f32_16x16x32_bf16 a[108:111], a[220:223], v[116:119], a[108:111]\n" \
"v_mfma_f32_16x16x32_bf16 a[76:79], a[216:219], v[96:99], a[76:79]\n" \
"v_exp_f32 v21, v21\n" \
"v_mfma_f32_16x16x32_bf16 a[76:79], a[220:223], v[100:103], a[76:79]\n" \
"ds_read_b128 a[212:215], v232 offset:2304\n" \
"v_cvt_pk_bf16_f32 v18, v20, v21\n" \
"v_smfmac_f32_16x16x64_bf16 a[52:55], v[244:247], v[80:87], v243\n" \
"v_mfma_f32_16x16x32_bf16 v[172:175], a[216:219], v[80:83], v[172:175]\n" \
"v_exp_f32 v22, v22\n" \
"v_mfma_f32_16x16x32_bf16 v[172:175], a[220:223], v[84:87], v[172:175]\n" \
"v_mfma_f32_16x16x32_bf16 v[140:143], a[216:219], v[64:67], v[140:143]\n" \
"s_waitcnt lgkmcnt(10)\n" \
"v_exp_f32 v23, v23\n" \
"v_mfma_f32_16x16x32_bf16 v[140:143], a[220:223], v[68:71], v[140:143]\n" \
"ds_read_b128 a[216:219], v232 offset:4352\n" \
"v_cvt_pk_bf16_f32 v19, v22, v23\n" \
"v_mfma_f32_16x16x32_bf16 v[144:147], a[224:227], v[64:67], v[144:147]\n" \
"v_exp_f32 v32, v32\n" \
"v_mfma_f32_16x16x32_bf16 v[144:147], a[228:231], v[68:71], v[144:147]\n" \
"v_mfma_f32_16x16x32_bf16 v[176:179], a[224:227], v[80:83], v[176:179]\n" \
"v_exp_f32 v33, v33\n" \
"v_mfma_f32_16x16x32_bf16 v[176:179], a[228:231], v[84:87], v[176:179]\n" \
"ds_read_b128 a[220:223], v232 offset:6400\n" \
"v_cvt_pk_bf16_f32 v32, v32, v33\n" \
"v_mfma_f32_16x16x32_bf16 a[80:83], a[224:227], v[96:99], a[80:83]\n" \
"v_exp_f32 v34, v34\n" \
"v_mfma_f32_16x16x32_bf16 a[80:83], a[228:231], v[100:103], a[80:83]\n" \
"v_mfma_f32_16x16x32_bf16 a[112:115], a[224:227], v[112:115], a[112:115]\n" \
"v_exp_f32 v35, v35\n" \
"v_mfma_f32_16x16x32_bf16 a[112:115], a[228:231], v[116:119], a[112:115]\n" \
"ds_read_b128 a[224:227], v232 offset:8192\n" \
"v_cvt_pk_bf16_f32 v33, v34, v35\n" \
"v_mfma_f32_16x16x32_bf16 a[116:119], a[232:235], v[112:115], a[116:119]\n" \
"v_exp_f32 v36, v36\n" \
"v_mfma_f32_16x16x32_bf16 a[116:119], a[236:239], v[116:119], a[116:119]\n" \
"v_mfma_f32_16x16x32_bf16 a[84:87], a[232:235], v[96:99], a[84:87]\n" \
"v_exp_f32 v37, v37\n" \
"v_mfma_f32_16x16x32_bf16 a[84:87], a[236:239], v[100:103], a[84:87]\n" \
"ds_read_b128 a[228:231], v232 offset:10240\n" \
"v_cvt_pk_bf16_f32 v34, v36, v37\n" \
"v_smfmac_f32_16x16x64_bf16 v[224:227], v[244:247], v[96:103], v243\n" \
"v_mfma_f32_16x16x32_bf16 v[180:183], a[232:235], v[80:83], v[180:183]\n" \
"v_exp_f32 v38, v38\n" \
"v_mfma_f32_16x16x32_bf16 v[180:183], a[236:239], v[84:87], v[180:183]\n" \
"v_mfma_f32_16x16x32_bf16 v[148:151], a[232:235], v[64:67], v[148:151]\n" \
"v_exp_f32 v39, v39\n" \
"v_mfma_f32_16x16x32_bf16 v[148:151], a[236:239], v[68:71], v[148:151]\n" \
"ds_read_b128 a[232:235], v232 offset:12288\n" \
"v_cvt_pk_bf16_f32 v35, v38, v39\n" \
"v_mfma_f32_16x16x32_bf16 v[152:155], a[32:35], v[64:67], v[152:155]\n" \
"v_exp_f32 v48, v48\n" \
"v_mfma_f32_16x16x32_bf16 v[152:155], a[36:39], v[68:71], v[152:155]\n" \
"v_mfma_f32_16x16x32_bf16 v[184:187], a[32:35], v[80:83], v[184:187]\n" \
"v_exp_f32 v49, v49\n" \
"v_mfma_f32_16x16x32_bf16 v[184:187], a[36:39], v[84:87], v[184:187]\n" \
"ds_read_b128 a[236:239], v232 offset:14336\n" \
"v_cvt_pk_bf16_f32 v48, v48, v49\n" \
"v_mfma_f32_16x16x32_bf16 a[88:91], a[32:35], v[96:99], a[88:91]\n" \
"v_exp_f32 v50, v50\n" \
"v_mfma_f32_16x16x32_bf16 a[88:91], a[36:39], v[100:103], a[88:91]\n" \
"v_mfma_f32_16x16x32_bf16 a[120:123], a[32:35], v[112:115], a[120:123]\n" \
"v_exp_f32 v51, v51\n" \
"v_mfma_f32_16x16x32_bf16 a[120:123], a[36:39], v[116:119], a[120:123]\n" \
"v_cvt_pk_bf16_f32 v49, v50, v51\n" \
"v_mfma_f32_16x16x32_bf16 a[124:127], a[40:43], v[112:115], a[124:127]\n" \
"v_exp_f32 v52, v52\n" \
"v_mfma_f32_16x16x32_bf16 a[124:127], a[44:47], v[116:119], a[124:127]\n" \
"v_mfma_f32_16x16x32_bf16 a[92:95], a[40:43], v[96:99], a[92:95]\n" \
"v_exp_f32 v53, v53\n" \
"v_mfma_f32_16x16x32_bf16 a[92:95], a[44:47], v[100:103], a[92:95]\n" \
"v_cvt_pk_bf16_f32 v50, v52, v53\n" \
"v_smfmac_f32_16x16x64_bf16 v[228:231], v[244:247], v[112:119], v243\n" \
"v_mfma_f32_16x16x32_bf16 v[188:191], a[40:43], v[80:83], v[188:191]\n" \
"v_exp_f32 v54, v54\n" \
"v_mfma_f32_16x16x32_bf16 v[188:191], a[44:47], v[84:87], v[188:191]\n" \
"v_mfma_f32_16x16x32_bf16 v[156:159], a[40:43], v[64:67], v[156:159]\n" \
"s_waitcnt vmcnt(1) lgkmcnt(0)\n" \
"v_exp_f32 v55, v55\n" \
"v_mfma_f32_16x16x32_bf16 v[156:159], a[44:47], v[68:71], v[156:159]\n" \
"s_barrier\n" \
"v_cvt_pk_bf16_f32 v51, v54, v55\n" \
"v_mfma_f32_16x16x32_bf16 v[64:67], a[192:195], a[128:131], v[196:199]\n" \
"v_exp_f32_e64 v8, v8\n" \
"v_mfma_f32_16x16x32_bf16 v[64:67], a[196:199], a[132:135], v[64:67]\n" \
"v_cmp_eq_u32 vcc, s60, v239\n" \
"v_mfma_f32_16x16x32_bf16 v[64:67], a[200:203], a[136:139], v[64:67]\n" \
"v_exp_f32_e64 v9, v9\n" \
"v_mfma_f32_16x16x32_bf16 v[64:67], a[204:207], a[140:143], v[64:67]\n" \
"s_andn2_b32 s70, s69, vcc_lo\n" \
"s_cbranch_scc1 Lpl2_%=\n" \
"Lpr2_%=:\n" \
"ds_read_b128 a[32:35], v233 offset:6144\n" \
"v_cvt_pk_bf16_f32 v4, v8, v9\n" \
"v_mfma_f32_16x16x32_bf16 v[80:83], a[204:207], a[156:159], v[196:199]\n" \
"s_add_u32 m0, s77, s80\n" \
"v_exp_f32 v10, v10\n" \
"v_mfma_f32_16x16x32_bf16 v[80:83], a[200:203], a[152:155], v[80:83]\n" \
"buffer_load_dwordx4 v234, s[40:43], s76 offen offset:0 sc1 lds\n" \
"v_mfma_f32_16x16x32_bf16 v[80:83], a[196:199], a[148:151], v[80:83]\n" \
"s_sub_u32 s89, s89, 1\n" \
"v_exp_f32 v11, v11\n" \
"v_mfma_f32_16x16x32_bf16 v[80:83], a[192:195], a[144:147], v[80:83]\n" \
"s_cmp_eq_u32 s89, 0\n" \
"s_cselect_b32 s70, s78, 0xc000\n" \
"ds_read_b128 a[36:39], v233 offset:14336\n" \
"v_cvt_pk_bf16_f32 v5, v10, v11\n" \
"v_mfma_f32_16x16x32_bf16 v[96:99], a[192:195], a[160:163], v[196:199]\n" \
"s_add_u32 s71, s76, 17408\n" \
"v_exp_f32 v12, v12\n" \
"v_mfma_f32_16x16x32_bf16 v[96:99], a[196:199], a[164:167], v[96:99]\n" \
"buffer_load_dwordx4 v234, s[40:43], s76 offen offset:1024 sc1 lds\n" \
"v_mfma_f32_16x16x32_bf16 v[96:99], a[200:203], a[168:171], v[96:99]\n" \
"v_exp_f32 v13, v13\n" \
"v_mfma_f32_16x16x32_bf16 v[96:99], a[204:207], a[172:175], v[96:99]\n" \
"ds_read_b128 a[40:43], v233 offset:7168\n" \
"v_cvt_pk_bf16_f32 v6, v12, v13\n" \
"v_mfma_f32_16x16x32_bf16 v[112:115], a[204:207], a[188:191], v[196:199]\n" \
"v_exp_f32 v14, v14\n" \
"v_mfma_f32_16x16x32_bf16 v[112:115], a[200:203], a[184:187], v[112:115]\n" \
"v_xor_b32 v232, s70, v232\n" \
"v_mfma_f32_16x16x32_bf16 v[112:115], a[196:199], a[180:183], v[112:115]\n" \
"v_exp_f32 v15, v15\n" \
"v_mfma_f32_16x16x32_bf16 v[112:115], a[192:195], a[176:179], v[112:115]\n" \
"ds_read_b128 a[204:207], v233 offset:9216\n" \
"v_cvt_pk_bf16_f32 v7, v14, v15\n" \
"v_mfma_f32_16x16x32_bf16 v[116:119], a[208:211], a[176:179], v[196:199]\n" \
"v_exp_f32 v24, v24\n" \
"v_mfma_f32_16x16x32_bf16 v[116:119], a[212:215], a[180:183], v[116:119]\n" \
"buffer_load_dwordx4 v234, s[40:43], s76 offen offset:2048 sc1 lds\n" \
"v_mfma_f32_16x16x32_bf16 v[116:119], a[216:219], a[184:187], v[116:119]\n" \
"v_exp_f32 v25, v25\n" \
"v_mfma_f32_16x16x32_bf16 v[116:119], a[220:223], a[188:191], v[116:119]\n" \
"ds_read_b128 a[200:203], v233 offset:1024\n" \
"v_cvt_pk_bf16_f32 v20, v24, v25\n" \
"v_mfma_f32_16x16x32_bf16 v[100:103], a[220:223], a[172:175], v[196:199]\n" \
"v_exp_f32 v26, v26\n" \
"v_mfma_f32_16x16x32_bf16 v[100:103], a[216:219], a[168:171], v[100:103]\n" \
"buffer_load_dwordx4 v234, s[40:43], s76 offen offset:3072 sc1 lds\n" \
"v_mfma_f32_16x16x32_bf16 v[100:103], a[212:215], a[164:167], v[100:103]\n" \
"v_exp_f32 v27, v27\n" \
"v_mfma_f32_16x16x32_bf16 v[100:103], a[208:211], a[160:163], v[100:103]\n" \
"ds_read_b128 a[196:199], v233 offset:8192\n" \
"v_cvt_pk_bf16_f32 v21, v26, v27\n" \
"v_mfma_f32_16x16x32_bf16 v[84:87], a[208:211], a[144:147], v[196:199]\n" \
"v_exp_f32 v28, v28\n" \
"v_mfma_f32_16x16x32_bf16 v[84:87], a[212:215], a[148:151], v[84:87]\n" \
"v_mfma_f32_16x16x32_bf16 v[84:87], a[216:219], a[152:155], v[84:87]\n" \
"v_exp_f32 v29, v29\n" \
"v_mfma_f32_16x16x32_bf16 v[84:87], a[220:223], a[156:159], v[84:87]\n" \
"ds_read_b128 a[192:195], v233 offset:0\n" \
"v_cvt_pk_bf16_f32 v22, v28, v29\n" \
"v_mfma_f32_16x16x32_bf16 v[68:71], a[220:223], a[140:143], v[196:199]\n" \
"s_add_u32 m0, m0, 0x4000\n" \
"v_exp_f32 v30, v30\n" \
"v_mfma_f32_16x16x32_bf16 v[68:71], a[216:219], a[136:139], v[68:71]\n" \
"buffer_load_dwordx4 v234, s[40:43], s71 offen offset:0 sc1 lds\n" \
"v_mfma_f32_16x16x32_bf16 v[68:71], a[212:215], a[132:135], v[68:71]\n" \
"v_exp_f32 v31, v31\n" \
"v_mfma_f32_16x16x32_bf16 v[68:71], a[208:211], a[128:131], v[68:71]\n" \
"ds_read_b128 a[220:223], v233 offset:11264\n" \
"v_cvt_pk_bf16_f32 v23, v30, v31\n" \
"v_mfma_f32_16x16x32_bf16 v[72:75], a[224:227], a[128:131], v[196:199]\n" \
"v_exp_f32 v40, v40\n" \
"v_mfma_f32_16x16x32_bf16 v[72:75], a[228:231], a[132:135], v[72:75]\n" \
"buffer_load_dwordx4 v234, s[40:43], s71 offen offset:1024 sc1 lds\n" \
"v_mfma_f32_16x16x32_bf16 v[72:75], a[232:235], a[136:139], v[72:75]\n" \
"v_exp_f32 v41, v41\n" \
"v_mfma_f32_16x16x32_bf16 v[72:75], a[236:239], a[140:143], v[72:75]\n" \
"ds_read_b128 a[216:219], v233 offset:3072\n" \
"v_cvt_pk_bf16_f32 v36, v40, v41\n" \
"v_mfma_f32_16x16x32_bf16 v[88:91], a[236:239], a[156:159], v[196:199]\n" \
"v_exp_f32 v42, v42\n" \
"v_mfma_f32_16x16x32_bf16 v[88:91], a[232:235], a[152:155], v[88:91]\n" \
"v_mfma_f32_16x16x32_bf16 v[88:91], a[228:231], a[148:151], v[88:91]\n" \
"v_exp_f32 v43, v43\n" \
"v_mfma_f32_16x16x32_bf16 v[88:91], a[224:227], a[144:147], v[88:91]\n" \
"ds_read_b128 a[212:215], v233 offset:10240\n" \
"v_cvt_pk_bf16_f32 v37, v42, v43\n" \
"v_mfma_f32_16x16x32_bf16 v[104:107], a[224:227], a[160:163], v[196:199]\n" \
"v_exp_f32 v44, v44\n" \
"v_mfma_f32_16x16x32_bf16 v[104:107], a[228:231], a[164:167], v[104:107]\n" \
"buffer_load_dwordx4 v234, s[40:43], s71 offen offset:2048 sc1 lds\n" \
"v_mfma_f32_16x16x32_bf16 v[104:107], a[232:235], a[168:171], v[104:107]\n" \
"v_exp_f32 v45, v45\n" \
"v_mfma_f32_16x16x32_bf16 v[104:107], a[236:239], a[172:175], v[104:107]\n" \
"ds_read_b128 a[208:211], v233 offset:2048\n" \
"v_cvt_pk_bf16_f32 v38, v44, v45\n" \
"v_mfma_f32_16x16x32_bf16 v[120:123], a[236:239], a[188:191], v[196:199]\n" \
"v_exp_f32 v46, v46\n" \
"v_mfma_f32_16x16x32_bf16 v[120:123], a[232:235], a[184:187], v[120:123]\n" \
"buffer_load_dwordx4 v234, s[40:43], s71 offen offset:3072 sc1 lds\n" \
"v_mfma_f32_16x16x32_bf16 v[120:123], a[228:231], a[180:183], v[120:123]\n" \
"v_exp_f32 v47, v47\n" \
"v_mfma_f32_16x16x32_bf16 v[120:123], a[224:227], a[176:179], v[120:123]\n" \
"ds_read_b128 a[236:239], v233 offset:13312\n" \
"v_cvt_pk_bf16_f32 v39, v46, v47\n" \
"v_mfma_f32_16x16x32_bf16 v[124:127], a[240:243], a[176:179], v[196:199]\n" \
"v_exp_f32 v56, v56\n" \
"v_mfma_f32_16x16x32_bf16 v[124:127], a[244:247], a[180:183], v[124:127]\n" \
"v_mfma_f32_16x16x32_bf16 v[124:127], a[248:251], a[184:187], v[124:127]\n" \
"v_exp_f32 v57, v57\n" \
"v_mfma_f32_16x16x32_bf16 v[124:127], a[252:255], a[188:191], v[124:127]\n" \
"ds_read_b128 a[232:235], v233 offset:5120\n" \
"v_cvt_pk_bf16_f32 v52, v56, v57\n" \
"v_mfma_f32_16x16x32_bf16 v[108:111], a[252:255], a[172:175], v[196:199]\n" \
"v_exp_f32 v58, v58\n" \
"v_mfma_f32_16x16x32_bf16 v[108:111], a[248:251], a[168:171], v[108:111]\n" \
"v_mfma_f32_16x16x32_bf16 v[108:111], a[244:247], a[164:167], v[108:111]\n" \
"v_exp_f32 v59, v59\n" \
"v_mfma_f32_16x16x32_bf16 v[108:111], a[240:243], a[160:163], v[108:111]\n" \
"ds_read_b128 a[228:231], v233 offset:12288\n" \
"v_cvt_pk_bf16_f32 v53, v58, v59\n" \
"v_mfma_f32_16x16x32_bf16 v[92:95], a[240:243], a[144:147], v[196:199]\n" \
"v_exp_f32 v60, v60\n" \
"v_mfma_f32_16x16x32_bf16 v[92:95], a[244:247], a[148:151], v[92:95]\n" \
"v_mfma_f32_16x16x32_bf16 v[92:95], a[248:251], a[152:155], v[92:95]\n" \
"v_exp_f32 v61, v61\n" \
"v_mfma_f32_16x16x32_bf16 v[92:95], a[252:255], a[156:159], v[92:95]\n" \
"ds_read_b128 a[224:227], v233 offset:4096\n" \
"v_cvt_pk_bf16_f32 v54, v60, v61\n" \
"v_mfma_f32_16x16x32_bf16 v[76:79], a[252:255], a[140:143], v[196:199]\n" \
"v_exp_f32 v62, v62\n" \
"v_mfma_f32_16x16x32_bf16 v[76:79], a[248:251], a[136:139], v[76:79]\n" \
"v_mfma_f32_16x16x32_bf16 v[76:79], a[244:247], a[132:135], v[76:79]\n" \
"s_waitcnt lgkmcnt(2)\n" \
"v_exp_f32 v63, v63\n" \
"v_mfma_f32_16x16x32_bf16 v[76:79], a[240:243], a[128:131], v[76:79]\n" \
"ds_read_b128 a[44:47], v233 offset:15360\n" \
"v_cvt_pk_bf16_f32 v55, v62, v63\n" \
"v_mfma_f32_16x16x32_bf16 v[128:131], a[192:195], v[0:3], v[128:131]\n" \
"s_add_u32 m0, s74, s79\n" \
"v_exp_f32 v64, v64\n" \
"v_mfma_f32_16x16x32_bf16 v[128:131], a[196:199], v[4:7], v[128:131]\n" \
"buffer_load_dwordx4 v234, s[36:39], s73 offen offset:0 sc1 lds\n" \
"v_mfma_f32_16x16x32_bf16 v[160:163], a[192:195], v[16:19], v[160:163]\n" \
"v_exp_f32 v65, v65\n" \
"v_mfma_f32_16x16x32_bf16 v[160:163], a[196:199], v[20:23], v[160:163]\n" \
"ds_read_b128 a[240:243], v232 offset:8448\n" \
"v_cvt_pk_bf16_f32 v64, v64, v65\n" \
"v_mfma_f32_16x16x32_bf16 a[64:67], a[192:195], v[32:35], a[64:67]\n" \
"s_add_u32 s71, s73, 17408\n" \
"v_exp_f32 v66, v66\n" \
"v_mfma_f32_16x16x32_bf16 a[64:67], a[196:199], v[36:39], a[64:67]\n" \
"buffer_load_dwordx4 v234, s[36:39], s73 offen offset:1024 sc1 lds\n" \
"v_mfma_f32_16x16x32_bf16 a[96:99], a[192:195], v[48:51], a[96:99]\n" \
"v_exp_f32 v67, v67\n" \
"v_mfma_f32_16x16x32_bf16 a[96:99], a[196:199], v[52:55], a[96:99]\n" \
"ds_read_b128 a[192:195], v232 offset:0\n" \
"v_cvt_pk_bf16_f32 v65, v66, v67\n" \
"v_mfma_f32_16x16x32_bf16 a[100:103], a[200:203], v[48:51], a[100:103]\n" \
"v_exp_f32 v68, v68\n" \
"v_mfma_f32_16x16x32_bf16 a[100:103], a[204:207], v[52:55], a[100:103]\n" \
"v_xor_b32 v233, 0x4000, v233\n" \
"v_mfma_f32_16x16x32_bf16 a[68:71], a[200:203], v[32:35], a[68:71]\n" \
"v_exp_f32 v69, v69\n" \
"v_mfma_f32_16x16x32_bf16 a[68:71], a[204:207], v[36:39], a[68:71]\n" \
"ds_read_b128 a[196:199], v232 offset:2048\n" \
"v_cvt_pk_bf16_f32 v66, v68, v69\n" \
"v_smfmac_f32_16x16x64_bf16 a[48:51], v[244:247], v[0:7], v243\n" \
"buffer_load_dwordx4 v234, s[36:39], s73 offen offset:2048 sc1 lds\n" \
"v_mfma_f32_16x16x32_bf16 v[164:167], a[200:203], v[16:19], v[164:167]\n" \
"v_exp_f32 v70, v70\n" \
"v_mfma_f32_16x16x32_bf16 v[164:167], a[204:207], v[20:23], v[164:167]\n" \
"v_mfma_f32_16x16x32_bf16 v[132:135], a[200:203], v[0:3], v[132:135]\n" \
"v_exp_f32 v71, v71\n" \
"v_mfma_f32_16x16x32_bf16 v[132:135], a[204:207], v[4:7], v[132:135]\n" \
"ds_read_b128 a[200:203], v232 offset:4096\n" \
"v_cvt_pk_bf16_f32 v67, v70, v71\n" \
"v_mfma_f32_16x16x32_bf16 v[136:139], a[208:211], v[0:3], v[136:139]\n" \
"v_exp_f32 v80, v80\n" \
"v_mfma_f32_16x16x32_bf16 v[136:139], a[212:215], v[4:7], v[136:139]\n" \
"buffer_load_dwordx4 v234, s[36:39], s73 offen offset:3072 sc1 lds\n" \
"v_mfma_f32_16x16x32_bf16 v[168:171], a[208:211], v[16:19], v[168:171]\n" \
"v_exp_f32 v81, v81\n" \
"v_mfma_f32_16x16x32_bf16 v[168:171], a[212:215], v[20:23], v[168:171]\n" \
"ds_read_b128 a[204:207], v232 offset:6144\n" \
"v_cvt_pk_bf16_f32 v80, v80, v81\n" \
"v_mfma_f32_16x16x32_bf16 a[72:75], a[208:211], v[32:35], a[72:75]\n" \
"v_exp_f32 v82, v82\n" \
"v_mfma_f32_16x16x32_bf16 a[72:75], a[212:215], v[36:39], a[72:75]\n" \
"v_mfma_f32_16x16x32_bf16 a[104:107], a[208:211], v[48:51], a[104:107]\n" \
"v_exp_f32 v83, v83\n" \
"v_mfma_f32_16x16x32_bf16 a[104:107], a[212:215], v[52:55], a[104:107]\n" \
"ds_read_b128 a[208:211], v232 offset:256\n" \
"v_cvt_pk_bf16_f32 v81, v82, v83\n" \
"v_mfma_f32_16x16x32_bf16 a[108:111], a[216:219], v[48:51], a[108:111]\n" \
"s_add_u32 m0, m0, 0x4000\n" \
"v_exp_f32 v84, v84\n" \
"v_mfma_f32_16x16x32_bf16 a[108:111], a[220:223], v[52:55], a[108:111]\n" \
"buffer_load_dwordx4 v234, s[36:39], s71 offen offset:0 sc1 lds\n" \
"v_mfma_f32_16x16x32_bf16 a[76:79], a[216:219], v[32:35], a[76:79]\n" \
"v_exp_f32 v85, v85\n" \
"v_mfma_f32_16x16x32_bf16 a[76:79], a[220:223], v[36:39], a[76:79]\n" \
"ds_read_b128 a[212:215], v232 offset:2304\n" \
"v_cvt_pk_bf16_f32 v82, v84, v85\n" \
"v_smfmac_f32_16x16x64_bf16 a[52:55], v[244:247], v[16:23], v243\n" \
"v_mfma_f32_16x16x32_bf16 v[172:175], a[216:219], v[16:19], v[172:175]\n" \
"v_exp_f32 v86, v86\n" \
"v_mfma_f32_16x16x32_bf16 v[172:175], a[220:223], v[20:23], v[172:175]\n" \
"buffer_load_dwordx4 v234, s[36:39], s71 offen offset:1024 sc1 lds\n" \
"v_mfma_f32_16x16x32_bf16 v[140:143], a[216:219], v[0:3], v[140:143]\n" \
"s_waitcnt lgkmcnt(7)\n" \
"v_exp_f32 v87, v87\n" \
"v_mfma_f32_16x16x32_bf16 v[140:143], a[220:223], v[4:7], v[140:143]\n" \
"ds_read_b128 a[216:219], v232 offset:4352\n" \
"v_cvt_pk_bf16_f32 v83, v86, v87\n" \
"v_mfma_f32_16x16x32_bf16 v[144:147], a[224:227], v[0:3], v[144:147]\n" \
"v_exp_f32 v96, v96\n" \
"v_mfma_f32_16x16x32_bf16 v[144:147], a[228:231], v[4:7], v[144:147]\n" \
"buffer_load_dwordx4 v234, s[36:39], s71 offen offset:2048 sc1 lds\n" \
"v_mfma_f32_16x16x32_bf16 v[176:179], a[224:227], v[16:19], v[176:179]\n" \
"v_exp_f32 v97, v97\n" \
"v_mfma_f32_16x16x32_bf16 v[176:179], a[228:231], v[20:23], v[176:179]\n" \
"ds_read_b128 a[220:223], v232 offset:6400\n" \
"v_cvt_pk_bf16_f32 v96, v96, v97\n" \
"v_mfma_f32_16x16x32_bf16 a[80:83], a[224:227], v[32:35], a[80:83]\n" \
"v_exp_f32 v98, v98\n" \
"v_mfma_f32_16x16x32_bf16 a[80:83], a[228:231], v[36:39], a[80:83]\n" \
"v_mfma_f32_16x16x32_bf16 a[112:115], a[224:227], v[48:51], a[112:115]\n" \
"v_exp_f32 v99, v99\n" \
"v_mfma_f32_16x16x32_bf16 a[112:115], a[228:231], v[52:55], a[112:115]\n" \
"ds_read_b128 a[224:227], v232 offset:8192\n" \
"v_cvt_pk_bf16_f32 v97, v98, v99\n" \
"v_mfma_f32_16x16x32_bf16 a[116:119], a[232:235], v[48:51], a[116:119]\n" \
"v_exp_f32 v100, v100\n" \
"v_mfma_f32_16x16x32_bf16 a[116:119], a[236:239], v[52:55], a[116:119]\n" \
"buffer_load_dwordx4 v234, s[36:39], s71 offen offset:3072 sc1 lds\n" \
"v_mfma_f32_16x16x32_bf16 a[84:87], a[232:235], v[32:35], a[84:87]\n" \
"v_exp_f32 v101, v101\n" \
"v_mfma_f32_16x16x32_bf16 a[84:87], a[236:239], v[36:39], a[84:87]\n" \
"ds_read_b128 a[228:231], v232 offset:10240\n" \
"v_cvt_pk_bf16_f32 v98, v100, v101\n" \
"v_smfmac_f32_16x16x64_bf16 v[224:227], v[244:247], v[32:39], v243\n" \
"v_mfma_f32_16x16x32_bf16 v[180:183], a[232:235], v[16:19], v[180:183]\n" \
"v_exp_f32 v102, v102\n" \
"v_mfma_f32_16x16x32_bf16 v[180:183], a[236:239], v[20:23], v[180:183]\n" \
"buffer_load_dword v239, v242, s[56:59], s95 offen sc1\n" \
"v_mfma_f32_16x16x32_bf16 v[148:151], a[232:235], v[0:3], v[148:151]\n" \
"s_mov_b32 s69, s92\n" \
"v_exp_f32 v103, v103\n" \
"v_mfma_f32_16x16x32_bf16 v[148:151], a[236:239], v[4:7], v[148:151]\n" \
"s_mov_b32 s76, s73\n" \
"ds_read_b128 a[232:235], v232 offset:12288\n" \
"v_cvt_pk_bf16_f32 v99, v102, v103\n" \
"v_mfma_f32_16x16x32_bf16 v[152:155], a[32:35], v[0:3], v[152:155]\n" \
"s_add_u32 s73, s73, 34816\n" \
"v_exp_f32 v112, v112\n" \
"v_mfma_f32_16x16x32_bf16 v[152:155], a[36:39], v[4:7], v[152:155]\n" \
"s_cmp_eq_u32 s73, s83\n" \
"s_cselect_b32 s70, 34816, 0\n" \
"s_add_u32 s73, s73, s70\n" \
"v_mfma_f32_16x16x32_bf16 v[184:187], a[32:35], v[16:19], v[184:187]\n" \
"s_xor_b32 s74, s74, 0x8000\n" \
"v_exp_f32 v113, v113\n" \
"v_mfma_f32_16x16x32_bf16 v[184:187], a[36:39], v[20:23], v[184:187]\n" \
"s_xor_b32 s77, s77, 0x8000\n" \
"ds_read_b128 a[236:239], v232 offset:14336\n" \
"v_cvt_pk_bf16_f32 v112, v112, v113\n" \
"v_mfma_f32_16x16x32_bf16 a[88:91], a[32:35], v[32:35], a[88:91]\n" \
"v_exp_f32 v114, v114\n" \
"v_mfma_f32_16x16x32_bf16 a[88:91], a[36:39], v[36:39], a[88:91]\n" \
"v_mfma_f32_16x16x32_bf16 a[120:123], a[32:35], v[48:51], a[120:123]\n" \
"v_exp_f32 v115, v115\n" \
"v_mfma_f32_16x16x32_bf16 a[120:123], a[36:39], v[52:55], a[120:123]\n" \
"ds_read_b128 a[244:247], v232 offset:10496\n" \
"v_cvt_pk_bf16_f32 v113, v114, v115\n" \
"v_mfma_f32_16x16x32_bf16 a[124:127], a[40:43], v[48:51], a[124:127]\n" \
"v_exp_f32 v116, v116\n" \
"v_mfma_f32_16x16x32_bf16 a[124:127], a[44:47], v[52:55], a[124:127]\n" \
"v_mfma_f32_16x16x32_bf16 a[92:95], a[40:43], v[32:35], a[92:95]\n" \
"v_exp_f32 v117, v117\n" \
"v_mfma_f32_16x16x32_bf16 a[92:95], a[44:47], v[36:39], a[92:95]\n" \
"ds_read_b128 a[248:251], v232 offset:12544\n" \
"v_cvt_pk_bf16_f32 v114, v116, v117\n" \
"v_smfmac_f32_16x16x64_bf16 v[228:231], v[244:247], v[48:55], v243\n" \
"v_mfma_f32_16x16x32_bf16 v[188:191], a[40:43], v[16:19], v[188:191]\n" \
"v_exp_f32 v118, v118\n" \
"v_mfma_f32_16x16x32_bf16 v[188:191], a[44:47], v[20:23], v[188:191]\n" \
"s_cmp_lg_u32 s89, 0\n" \
"v_exp_f32 v119, v119\n" \
"v_mfma_f32_16x16x32_bf16 v[156:159], a[40:43], v[0:3], v[156:159]\n" \
"s_waitcnt lgkmcnt(2)\n" \
"ds_read_b128 a[252:255], v232 offset:14592\n" \
"v_cvt_pk_bf16_f32 v115, v118, v119\n" \
"v_mfma_f32_16x16x32_bf16 v[156:159], a[44:47], v[4:7], v[156:159]\n" \
"s_cbranch_scc1 Lloop_%=\n" \
"s_cmp_eq_u32 s63, 7\n" \
"s_cbranch_scc0 Lloop2_%=\n" \
"s_branch Lg_%=\n" \
"s_nop 0\n" \
"s_nop 0\n" \
"Lep_%=:\n" \
"Lpg6052_%=:\n" \
"s_cbranch_execz Lpg6794_%=\n" \
"s_nop 4\n" \
"v_accvgpr_read_b32 v92, a48\n" \
"v_accvgpr_read_b32 v93, a49\n" \
"v_accvgpr_read_b32 v94, a52\n" \
"v_accvgpr_read_b32 v95, a53\n" \
"v_accvgpr_write_b32 a48, 0\n" \
"v_accvgpr_write_b32 a49, 0\n" \
"v_accvgpr_write_b32 a52, 0\n" \
"v_accvgpr_write_b32 a53, 0\n" \
"v_add_f32 v86, v92, v93\n" \
"v_add_f32 v87, v94, v95\n" \
"v_rcp_f32 v84, v86\n" \
"v_rcp_f32 v85, v87\n" \
"v_add_u32 v240, 0x1000, v236\n" \
"v_add_f32 v80, v86, v87\n" \
"v_mul_f32 v80, 0x43800000, v80\n" \
"v_add_f32 v86, v84, v85\n" \
"v_fmac_f32 v80, 0x5f800000, v86\n" \
"v_mov_b32 v81, 0\n" \
"v_mov_b32 v82, 0\n" \
"v_mov_b32 v83, 0\n" \
"v_mov_b32 v88, 0\n" \
"v_mov_b32 v89, 0\n" \
"v_mov_b32 v90, 0\n" \
"v_mov_b32 v91, 0\n" \
"v_pk_mul_f32 v[128:129], v[128:129], v[84:85] op_sel:[0,0] op_sel_hi:[1,0]\n" \
"v_pk_mul_f32 v[130:131], v[130:131], v[84:85] op_sel:[0,0] op_sel_hi:[1,0]\n" \
"v_pk_mul_f32 v[132:133], v[132:133], v[84:85] op_sel:[0,0] op_sel_hi:[1,0]\n" \
"v_pk_mul_f32 v[134:135], v[134:135], v[84:85] op_sel:[0,0] op_sel_hi:[1,0]\n" \
"v_cvt_pk_bf16_f32 v64, v128, v129\n" \
"v_cvt_pk_bf16_f32 v65, v130, v131\n" \
"v_cvt_pk_bf16_f32 v66, v132, v133\n" \
"v_cvt_pk_bf16_f32 v67, v134, v135\n" \
"buffer_store_dwordx4 v[64:67], v236, s[56:59], s87 offen offset:0 sc1\n" \
"v_pk_mul_f32 v[136:137], v[136:137], v[84:85] op_sel:[0,0] op_sel_hi:[1,0]\n" \
"v_pk_mul_f32 v[138:139], v[138:139], v[84:85] op_sel:[0,0] op_sel_hi:[1,0]\n" \
"v_pk_mul_f32 v[140:141], v[140:141], v[84:85] op_sel:[0,0] op_sel_hi:[1,0]\n" \
"v_pk_mul_f32 v[142:143], v[142:143], v[84:85] op_sel:[0,0] op_sel_hi:[1,0]\n" \
"v_cvt_pk_bf16_f32 v68, v136, v137\n" \
"v_cvt_pk_bf16_f32 v69, v138, v139\n" \
"v_cvt_pk_bf16_f32 v70, v140, v141\n" \
"v_cvt_pk_bf16_f32 v71, v142, v143\n" \
"buffer_store_dwordx4 v[68:71], v236, s[56:59], s87 offen offset:2048 sc1\n" \
"v_pk_mul_f32 v[144:145], v[144:145], v[84:85] op_sel:[0,0] op_sel_hi:[1,0]\n" \
"v_pk_mul_f32 v[146:147], v[146:147], v[84:85] op_sel:[0,0] op_sel_hi:[1,0]\n" \
"v_pk_mul_f32 v[148:149], v[148:149], v[84:85] op_sel:[0,0] op_sel_hi:[1,0]\n" \
"v_pk_mul_f32 v[150:151], v[150:151], v[84:85] op_sel:[0,0] op_sel_hi:[1,0]\n" \
"v_cvt_pk_bf16_f32 v72, v144, v145\n" \
"v_cvt_pk_bf16_f32 v73, v146, v147\n" \
"v_cvt_pk_bf16_f32 v74, v148, v149\n" \
"v_cvt_pk_bf16_f32 v75, v150, v151\n" \
"buffer_store_dwordx4 v[72:75], v240, s[56:59], s87 offen offset:0 sc1\n" \
"v_pk_mul_f32 v[152:153], v[152:153], v[84:85] op_sel:[0,0] op_sel_hi:[1,0]\n" \
"v_pk_mul_f32 v[154:155], v[154:155], v[84:85] op_sel:[0,0] op_sel_hi:[1,0]\n" \
"v_pk_mul_f32 v[156:157], v[156:157], v[84:85] op_sel:[0,0] op_sel_hi:[1,0]\n" \
"v_pk_mul_f32 v[158:159], v[158:159], v[84:85] op_sel:[0,0] op_sel_hi:[1,0]\n" \
"v_cvt_pk_bf16_f32 v76, v152, v153\n" \
"v_cvt_pk_bf16_f32 v77, v154, v155\n" \
"v_cvt_pk_bf16_f32 v78, v156, v157\n" \
"v_cvt_pk_bf16_f32 v79, v158, v159\n" \
"buffer_store_dwordx4 v[76:79], v240, s[56:59], s87 offen offset:2048 sc1\n" \
"v_smfmac_f32_16x16x64_bf16 v[80:83], v[244:247], v[64:71], v243\n" \
"v_smfmac_f32_16x16x64_bf16 v[80:83], v[244:247], v[72:79], v243\n" \
"v_mfma_f32_32x32x16_bf16 v[128:143], v[88:91], v[88:91], 0\n" \
"v_mfma_f32_32x32x16_bf16 v[144:159], v[88:91], v[88:91], 0\n" \
"s_mov_b64 exec, 0\n" \
"s_cmp_eq_u32 s61, 0\n" \
"s_cbranch_scc1 Lst1x0_%=\n" \
"s_cmp_eq_u32 s61, 1\n" \
"s_cbranch_scc1 Lst1x1_%=\n" \
"s_cmp_eq_u32 s61, 2\n" \
"s_cbranch_scc1 Lst1x2_%=\n" \
"s_branch Lst1x3_%=\n" \
"Lcb1_%=:\n" \
"s_mov_b64 exec, -1\n" \
"s_add_u32 s72, s87, 532480\n" \
"v_pk_mul_f32 v[160:161], v[160:161], v[84:85] op_sel:[0,1] op_sel_hi:[1,1]\n" \
"v_pk_mul_f32 v[162:163], v[162:163], v[84:85] op_sel:[0,1] op_sel_hi:[1,1]\n" \
"v_pk_mul_f32 v[164:165], v[164:165], v[84:85] op_sel:[0,1] op_sel_hi:[1,1]\n" \
"v_pk_mul_f32 v[166:167], v[166:167], v[84:85] op_sel:[0,1] op_sel_hi:[1,1]\n" \
"v_cvt_pk_bf16_f32 v64, v160, v161\n" \
"v_cvt_pk_bf16_f32 v65, v162, v163\n" \
"v_cvt_pk_bf16_f32 v66, v164, v165\n" \
"v_cvt_pk_bf16_f32 v67, v166, v167\n" \
"buffer_store_dwordx4 v[64:67], v236, s[56:59], s72 offen offset:0 sc1\n" \
"v_pk_mul_f32 v[168:169], v[168:169], v[84:85] op_sel:[0,1] op_sel_hi:[1,1]\n" \
"v_pk_mul_f32 v[170:171], v[170:171], v[84:85] op_sel:[0,1] op_sel_hi:[1,1]\n" \
"v_pk_mul_f32 v[172:173], v[172:173], v[84:85] op_sel:[0,1] op_sel_hi:[1,1]\n" \
"v_pk_mul_f32 v[174:175], v[174:175], v[84:85] op_sel:[0,1] op_sel_hi:[1,1]\n" \
"v_cvt_pk_bf16_f32 v68, v168, v169\n" \
"v_cvt_pk_bf16_f32 v69, v170, v171\n" \
"v_cvt_pk_bf16_f32 v70, v172, v173\n" \
"v_cvt_pk_bf16_f32 v71, v174, v175\n" \
"buffer_store_dwordx4 v[68:71], v236, s[56:59], s72 offen offset:2048 sc1\n" \
"v_pk_mul_f32 v[176:177], v[176:177], v[84:85] op_sel:[0,1] op_sel_hi:[1,1]\n" \
"v_pk_mul_f32 v[178:179], v[178:179], v[84:85] op_sel:[0,1] op_sel_hi:[1,1]\n" \
"v_pk_mul_f32 v[180:181], v[180:181], v[84:85] op_sel:[0,1] op_sel_hi:[1,1]\n" \
"v_pk_mul_f32 v[182:183], v[182:183], v[84:85] op_sel:[0,1] op_sel_hi:[1,1]\n" \
"v_cvt_pk_bf16_f32 v72, v176, v177\n" \
"v_cvt_pk_bf16_f32 v73, v178, v179\n" \
"v_cvt_pk_bf16_f32 v74, v180, v181\n" \
"v_cvt_pk_bf16_f32 v75, v182, v183\n" \
"buffer_store_dwordx4 v[72:75], v240, s[56:59], s72 offen offset:0 sc1\n" \
"v_pk_mul_f32 v[184:185], v[184:185], v[84:85] op_sel:[0,1] op_sel_hi:[1,1]\n" \
"v_pk_mul_f32 v[186:187], v[186:187], v[84:85] op_sel:[0,1] op_sel_hi:[1,1]\n" \
"v_pk_mul_f32 v[188:189], v[188:189], v[84:85] op_sel:[0,1] op_sel_hi:[1,1]\n" \
"v_pk_mul_f32 v[190:191], v[190:191], v[84:85] op_sel:[0,1] op_sel_hi:[1,1]\n" \
"v_cvt_pk_bf16_f32 v76, v184, v185\n" \
"v_cvt_pk_bf16_f32 v77, v186, v187\n" \
"v_cvt_pk_bf16_f32 v78, v188, v189\n" \
"v_cvt_pk_bf16_f32 v79, v190, v191\n" \
"buffer_store_dwordx4 v[76:79], v240, s[56:59], s72 offen offset:2048 sc1\n" \
"v_smfmac_f32_16x16x64_bf16 v[80:83], v[244:247], v[64:71], v243\n" \
"v_smfmac_f32_16x16x64_bf16 v[80:83], v[244:247], v[72:79], v243\n" \
"v_mfma_f32_32x32x16_bf16 v[160:175], v[88:91], v[88:91], 0\n" \
"v_mfma_f32_32x32x16_bf16 v[176:191], v[88:91], v[88:91], 0\n" \
"s_nop 5\n" \
"v_add_f32 v80, v80, v81\n" \
"s_movk_i32 s64, 0x207\n" \
"v_cmp_class_f32 vcc, v80, s64\n" \
"s_nop 1\n" \
"s_cmp_lg_u64 vcc, 0\n" \
"s_addc_u32 s90, s90, 0\n" \
"s_waitcnt vmcnt(8)\n" \
"s_cmp_eq_u32 s63, 7\n" \
"s_cbranch_scc0 Lepr_%=\n" \
"s_branch Lepg_%=\n" \
"s_nop 0\n" \
"s_nop 0\n" \
"s_nop 0\n" \
"s_nop 0\n" \
"s_nop 0\n" \
"Lloop2_%=:\n" \
"Lpg6186_%=:\n" \
"s_cbranch_execz Lpg7380_%=\n" \
"v_mfma_f32_16x16x32_bf16 v[0:3], a[192:195], a[0:3], v[196:199]\n" \
"v_exp_f32 v72, v72\n" \
"v_mfma_f32_16x16x32_bf16 v[0:3], a[196:199], a[4:7], v[0:3]\n" \
"ds_read_b128 a[44:47], v233 offset:15360\n" \
"v_mfma_f32_16x16x32_bf16 v[0:3], a[200:203], a[8:11], v[0:3]\n" \
"v_exp_f32 v73, v73\n" \
"v_mfma_f32_16x16x32_bf16 v[0:3], a[204:207], a[12:15], v[0:3]\n" \
"ds_read_b128 a[32:35], v233 offset:6144\n" \
"v_cvt_pk_bf16_f32 v68, v72, v73\n" \
"Lpg6196_%=:\n" \
"s_cbranch_execz Lcb0_%=\n" \
"v_mfma_f32_16x16x32_bf16 v[16:19], a[204:207], a[28:31], v[196:199]\n" \
"v_exp_f32 v74, v74\n" \
"v_mfma_f32_16x16x32_bf16 v[16:19], a[200:203], a[24:27], v[16:19]\n" \
"v_xor_b32 v232, 0x8000, v232\n" \
"v_mfma_f32_16x16x32_bf16 v[16:19], a[196:199], a[20:23], v[16:19]\n" \
"v_exp_f32 v75, v75\n" \
"v_mfma_f32_16x16x32_bf16 v[16:19], a[192:195], a[16:19], v[16:19]\n" \
"ds_read_b128 a[36:39], v233 offset:14336\n" \
"v_cvt_pk_bf16_f32 v69, v74, v75\n" \
"v_mfma_f32_16x16x32_bf16 v[32:35], a[192:195], a[160:163], v[196:199]\n" \
"v_exp_f32 v76, v76\n" \
"v_mfma_f32_16x16x32_bf16 v[32:35], a[196:199], a[164:167], v[32:35]\n" \
"v_mfma_f32_16x16x32_bf16 v[32:35], a[200:203], a[168:171], v[32:35]\n" \
"v_exp_f32 v77, v77\n" \
"v_mfma_f32_16x16x32_bf16 v[32:35], a[204:207], a[172:175], v[32:35]\n" \
"ds_read_b128 a[40:43], v233 offset:7168\n" \
"v_cvt_pk_bf16_f32 v70, v76, v77\n" \
"v_mfma_f32_16x16x32_bf16 v[48:51], a[204:207], a[188:191], v[196:199]\n" \
"v_exp_f32 v78, v78\n" \
"v_mfma_f32_16x16x32_bf16 v[48:51], a[200:203], a[184:187], v[48:51]\n" \
"v_mfma_f32_16x16x32_bf16 v[48:51], a[196:199], a[180:183], v[48:51]\n" \
"v_exp_f32 v79, v79\n" \
"v_mfma_f32_16x16x32_bf16 v[48:51], a[192:195], a[176:179], v[48:51]\n" \
"ds_read_b128 a[204:207], v233 offset:9216\n" \
"v_cvt_pk_bf16_f32 v71, v78, v79\n" \
"v_mfma_f32_16x16x32_bf16 v[52:55], a[208:211], a[176:179], v[196:199]\n" \
"v_exp_f32 v88, v88\n" \
"v_mfma_f32_16x16x32_bf16 v[52:55], a[212:215], a[180:183], v[52:55]\n" \
"v_mfma_f32_16x16x32_bf16 v[52:55], a[216:219], a[184:187], v[52:55]\n" \
"v_exp_f32 v89, v89\n" \
"v_mfma_f32_16x16x32_bf16 v[52:55], a[220:223], a[188:191], v[52:55]\n" \
"ds_read_b128 a[200:203], v233 offset:1024\n" \
"v_cvt_pk_bf16_f32 v84, v88, v89\n" \
"v_mfma_f32_16x16x32_bf16 v[36:39], a[220:223], a[172:175], v[196:199]\n" \
"v_exp_f32 v90, v90\n" \
"v_mfma_f32_16x16x32_bf16 v[36:39], a[216:219], a[168:171], v[36:39]\n" \
"v_mfma_f32_16x16x32_bf16 v[36:39], a[212:215], a[164:167], v[36:39]\n" \
"v_exp_f32 v91, v91\n" \
"v_mfma_f32_16x16x32_bf16 v[36:39], a[208:211], a[160:163], v[36:39]\n" \
"ds_read_b128 a[196:199], v233 offset:8192\n" \
"v_cvt_pk_bf16_f32 v85, v90, v91\n" \
"v_mfma_f32_16x16x32_bf16 v[20:23], a[208:211], a[16:19], v[196:199]\n" \
"v_exp_f32 v92, v92\n" \
"v_mfma_f32_16x16x32_bf16 v[20:23], a[212:215], a[20:23], v[20:23]\n" \
"v_mfma_f32_16x16x32_bf16 v[20:23], a[216:219], a[24:27], v[20:23]\n" \
"v_exp_f32 v93, v93\n" \
"v_mfma_f32_16x16x32_bf16 v[20:23], a[220:223], a[28:31], v[20:23]\n" \
"ds_read_b128 a[192:195], v233 offset:0\n" \
"v_cvt_pk_bf16_f32 v86, v92, v93\n" \
"v_mfma_f32_16x16x32_bf16 v[4:7], a[220:223], a[12:15], v[196:199]\n" \
"v_exp_f32 v94, v94\n" \
"v_mfma_f32_16x16x32_bf16 v[4:7], a[216:219], a[8:11], v[4:7]\n" \
"v_mfma_f32_16x16x32_bf16 v[4:7], a[212:215], a[4:7], v[4:7]\n" \
"s_waitcnt lgkmcnt(8)\n" \
"v_exp_f32 v95, v95\n" \
"v_mfma_f32_16x16x32_bf16 v[4:7], a[208:211], a[0:3], v[4:7]\n" \
"ds_read_b128 a[220:223], v233 offset:11264\n" \
"v_cvt_pk_bf16_f32 v87, v94, v95\n" \
"v_mfma_f32_16x16x32_bf16 v[8:11], a[224:227], a[0:3], v[196:199]\n" \
"v_exp_f32 v104, v104\n" \
"v_mfma_f32_16x16x32_bf16 v[8:11], a[228:231], a[4:7], v[8:11]\n" \
"v_mfma_f32_16x16x32_bf16 v[8:11], a[232:235], a[8:11], v[8:11]\n" \
"v_exp_f32 v105, v105\n" \
"v_mfma_f32_16x16x32_bf16 v[8:11], a[236:239], a[12:15], v[8:11]\n" \
"ds_read_b128 a[216:219], v233 offset:3072\n" \
"v_cvt_pk_bf16_f32 v100, v104, v105\n" \
"v_mfma_f32_16x16x32_bf16 v[24:27], a[236:239], a[28:31], v[196:199]\n" \
"v_exp_f32 v106, v106\n" \
"v_mfma_f32_16x16x32_bf16 v[24:27], a[232:235], a[24:27], v[24:27]\n" \
"v_mfma_f32_16x16x32_bf16 v[24:27], a[228:231], a[20:23], v[24:27]\n" \
"v_exp_f32 v107, v107\n" \
"v_mfma_f32_16x16x32_bf16 v[24:27], a[224:227], a[16:19], v[24:27]\n" \
"ds_read_b128 a[212:215], v233 offset:10240\n" \
"v_cvt_pk_bf16_f32 v101, v106, v107\n" \
"v_mfma_f32_16x16x32_bf16 v[40:43], a[224:227], a[160:163], v[196:199]\n" \
"v_exp_f32 v108, v108\n" \
"v_mfma_f32_16x16x32_bf16 v[40:43], a[228:231], a[164:167], v[40:43]\n" \
"v_mfma_f32_16x16x32_bf16 v[40:43], a[232:235], a[168:171], v[40:43]\n" \
"v_exp_f32 v109, v109\n" \
"v_mfma_f32_16x16x32_bf16 v[40:43], a[236:239], a[172:175], v[40:43]\n" \
"ds_read_b128 a[208:211], v233 offset:2048\n" \
"v_cvt_pk_bf16_f32 v102, v108, v109\n" \
"v_mfma_f32_16x16x32_bf16 v[56:59], a[236:239], a[188:191], v[196:199]\n" \
"v_exp_f32 v110, v110\n" \
"v_mfma_f32_16x16x32_bf16 v[56:59], a[232:235], a[184:187], v[56:59]\n" \
"v_mfma_f32_16x16x32_bf16 v[56:59], a[228:231], a[180:183], v[56:59]\n" \
"v_exp_f32 v111, v111\n" \
"v_mfma_f32_16x16x32_bf16 v[56:59], a[224:227], a[176:179], v[56:59]\n" \
"ds_read_b128 a[236:239], v233 offset:13312\n" \
"v_cvt_pk_bf16_f32 v103, v110, v111\n" \
"v_mfma_f32_16x16x32_bf16 v[60:63], a[240:243], a[176:179], v[196:199]\n" \
"v_exp_f32 v120, v120\n" \
"v_mfma_f32_16x16x32_bf16 v[60:63], a[244:247], a[180:183], v[60:63]\n" \
"ds_read_b128 a[232:235], v233 offset:5120\n" \
"v_mfma_f32_16x16x32_bf16 v[60:63], a[248:251], a[184:187], v[60:63]\n" \
"v_exp_f32 v121, v121\n" \
"v_mfma_f32_16x16x32_bf16 v[60:63], a[252:255], a[188:191], v[60:63]\n" \
"ds_read_b128 a[228:231], v233 offset:12288\n" \
"v_cvt_pk_bf16_f32 v116, v120, v121\n" \
"v_mfma_f32_16x16x32_bf16 v[44:47], a[252:255], a[172:175], v[196:199]\n" \
"v_exp_f32 v122, v122\n" \
"v_mfma_f32_16x16x32_bf16 v[44:47], a[248:251], a[168:171], v[44:47]\n" \
"ds_read_b128 a[224:227], v233 offset:4096\n" \
"v_mfma_f32_16x16x32_bf16 v[44:47], a[244:247], a[164:167], v[44:47]\n" \
"v_exp_f32 v123, v123\n" \
"v_mfma_f32_16x16x32_bf16 v[44:47], a[240:243], a[160:163], v[44:47]\n" \
"v_cvt_pk_bf16_f32 v117, v122, v123\n" \
"v_mfma_f32_16x16x32_bf16 v[28:31], a[240:243], a[16:19], v[196:199]\n" \
"v_exp_f32 v124, v124\n" \
"v_mfma_f32_16x16x32_bf16 v[28:31], a[244:247], a[20:23], v[28:31]\n" \
"v_mfma_f32_16x16x32_bf16 v[28:31], a[248:251], a[24:27], v[28:31]\n" \
"v_exp_f32 v125, v125\n" \
"v_mfma_f32_16x16x32_bf16 v[28:31], a[252:255], a[28:31], v[28:31]\n" \
"v_cvt_pk_bf16_f32 v118, v124, v125\n" \
"v_mfma_f32_16x16x32_bf16 v[12:15], a[252:255], a[12:15], v[196:199]\n" \
"v_exp_f32 v126, v126\n" \
"v_mfma_f32_16x16x32_bf16 v[12:15], a[248:251], a[8:11], v[12:15]\n" \
"v_mfma_f32_16x16x32_bf16 v[12:15], a[244:247], a[4:7], v[12:15]\n" \
"s_waitcnt vmcnt(0) lgkmcnt(0)\n" \
"v_exp_f32 v127, v127\n" \
"v_mfma_f32_16x16x32_bf16 v[12:15], a[240:243], a[0:3], v[12:15]\n" \
"s_barrier\n" \
"v_cvt_pk_bf16_f32 v119, v126, v127\n" \
"v_mfma_f32_16x16x32_bf16 v[128:131], a[192:195], v[64:67], v[128:131]\n" \
"v_exp_f32 v0, v0\n" \
"v_mfma_f32_16x16x32_bf16 v[128:131], a[196:199], v[68:71], v[128:131]\n" \
"v_cmp_eq_u32 vcc, s60, v239\n" \
"v_mfma_f32_16x16x32_bf16 v[160:163], a[192:195], v[80:83], v[160:163]\n" \
"v_exp_f32 v1, v1\n" \
"v_mfma_f32_16x16x32_bf16 v[160:163], a[196:199], v[84:87], v[160:163]\n" \
"s_andn2_b32 s70, s69, vcc_lo\n" \
"s_cbranch_scc1 Lpl3_%=\n" \
"Lpr3_%=:\n" \
"ds_read_b128 a[240:243], v232 offset:8448\n" \
"v_cvt_pk_bf16_f32 v0, v0, v1\n" \
"v_mfma_f32_16x16x32_bf16 a[64:67], a[192:195], v[96:99], a[64:67]\n" \
"s_add_u32 m0, s74, s79\n" \
"v_exp_f32 v2, v2\n" \
"v_mfma_f32_16x16x32_bf16 a[64:67], a[196:199], v[100:103], a[64:67]\n" \
"buffer_load_dwordx4 v234, s[36:39], s73 offen offset:0 sc1 lds\n" \
"v_mfma_f32_16x16x32_bf16 a[96:99], a[192:195], v[112:115], a[96:99]\n" \
"v_exp_f32 v3, v3\n" \
"v_mfma_f32_16x16x32_bf16 a[96:99], a[196:199], v[116:119], a[96:99]\n" \
"ds_read_b128 a[192:195], v232 offset:0\n" \
"v_cvt_pk_bf16_f32 v1, v2, v3\n" \
"v_mfma_f32_16x16x32_bf16 a[100:103], a[200:203], v[112:115], a[100:103]\n" \
"s_add_u32 s71, s73, 17408\n" \
"v_exp_f32 v4, v4\n" \
"v_mfma_f32_16x16x32_bf16 a[100:103], a[204:207], v[116:119], a[100:103]\n" \
"buffer_load_dwordx4 v234, s[36:39], s73 offen offset:1024 sc1 lds\n" \
"v_mfma_f32_16x16x32_bf16 a[68:71], a[200:203], v[96:99], a[68:71]\n" \
"v_exp_f32 v5, v5\n" \
"v_mfma_f32_16x16x32_bf16 a[68:71], a[204:207], v[100:103], a[68:71]\n" \
"ds_read_b128 a[196:199], v232 offset:2048\n" \
"v_cvt_pk_bf16_f32 v2, v4, v5\n" \
"v_smfmac_f32_16x16x64_bf16 a[48:51], v[244:247], v[64:71], v243\n" \
"buffer_load_dwordx4 v234, s[36:39], s73 offen offset:2048 sc1 lds\n" \
"v_mfma_f32_16x16x32_bf16 v[164:167], a[200:203], v[80:83], v[164:167]\n" \
"v_exp_f32 v6, v6\n" \
"v_mfma_f32_16x16x32_bf16 v[164:167], a[204:207], v[84:87], v[164:167]\n" \
"buffer_load_dwordx4 v234, s[36:39], s73 offen offset:3072 sc1 lds\n" \
"v_mfma_f32_16x16x32_bf16 v[132:135], a[200:203], v[64:67], v[132:135]\n" \
"v_exp_f32 v7, v7\n" \
"v_mfma_f32_16x16x32_bf16 v[132:135], a[204:207], v[68:71], v[132:135]\n" \
"ds_read_b128 a[200:203], v232 offset:4096\n" \
"v_cvt_pk_bf16_f32 v3, v6, v7\n" \
"v_mfma_f32_16x16x32_bf16 v[136:139], a[208:211], v[64:67], v[136:139]\n" \
"s_add_u32 m0, m0, 0x4000\n" \
"v_exp_f32 v16, v16\n" \
"v_mfma_f32_16x16x32_bf16 v[136:139], a[212:215], v[68:71], v[136:139]\n" \
"buffer_load_dwordx4 v234, s[36:39], s71 offen offset:0 sc1 lds\n" \
"v_mfma_f32_16x16x32_bf16 v[168:171], a[208:211], v[80:83], v[168:171]\n" \
"v_exp_f32 v17, v17\n" \
"v_mfma_f32_16x16x32_bf16 v[168:171], a[212:215], v[84:87], v[168:171]\n" \
"ds_read_b128 a[204:207], v232 offset:6144\n" \
"v_cvt_pk_bf16_f32 v16, v16, v17\n" \
"v_mfma_f32_16x16x32_bf16 a[72:75], a[208:211], v[96:99], a[72:75]\n" \
"v_exp_f32 v18, v18\n" \
"v_mfma_f32_16x16x32_bf16 a[72:75], a[212:215], v[100:103], a[72:75]\n" \
"buffer_load_dwordx4 v234, s[36:39], s71 offen offset:1024 sc1 lds\n" \
"v_mfma_f32_16x16x32_bf16 a[104:107], a[208:211], v[112:115], a[104:107]\n" \
"v_exp_f32 v19, v19\n" \
"v_mfma_f32_16x16x32_bf16 a[104:107], a[212:215], v[116:119], a[104:107]\n" \
"ds_read_b128 a[208:211], v232 offset:256\n" \
"v_cvt_pk_bf16_f32 v17, v18, v19\n" \
"v_mfma_f32_16x16x32_bf16 a[108:111], a[216:219], v[112:115], a[108:111]\n" \
"v_exp_f32 v20, v20\n" \
"v_mfma_f32_16x16x32_bf16 a[108:111], a[220:223], v[116:119], a[108:111]\n" \
"buffer_load_dwordx4 v234, s[36:39], s71 offen offset:2048 sc1 lds\n" \
"v_mfma_f32_16x16x32_bf16 a[76:79], a[216:219], v[96:99], a[76:79]\n" \
"v_exp_f32 v21, v21\n" \
"v_mfma_f32_16x16x32_bf16 a[76:79], a[220:223], v[100:103], a[76:79]\n" \
"ds_read_b128 a[212:215], v232 offset:2304\n" \
"v_cvt_pk_bf16_f32 v18, v20, v21\n" \
"v_smfmac_f32_16x16x64_bf16 a[52:55], v[244:247], v[80:87], v243\n" \
"buffer_load_dwordx4 v234, s[36:39], s71 offen offset:3072 sc1 lds\n" \
"v_mfma_f32_16x16x32_bf16 v[172:175], a[216:219], v[80:83], v[172:175]\n" \
"s_add_u32 m0, s77, s80\n" \
"v_exp_f32 v22, v22\n" \
"v_mfma_f32_16x16x32_bf16 v[172:175], a[220:223], v[84:87], v[172:175]\n" \
"buffer_load_dwordx4 v234, s[40:43], s76 offen offset:0 sc1 lds\n" \
"v_mfma_f32_16x16x32_bf16 v[140:143], a[216:219], v[64:67], v[140:143]\n" \
"v_exp_f32 v23, v23\n" \
"v_mfma_f32_16x16x32_bf16 v[140:143], a[220:223], v[68:71], v[140:143]\n" \
"ds_read_b128 a[216:219], v232 offset:4352\n" \
"v_cvt_pk_bf16_f32 v19, v22, v23\n" \
"v_mfma_f32_16x16x32_bf16 v[144:147], a[224:227], v[64:67], v[144:147]\n" \
"s_add_u32 s71, s76, 17408\n" \
"v_exp_f32 v32, v32\n" \
"v_mfma_f32_16x16x32_bf16 v[144:147], a[228:231], v[68:71], v[144:147]\n" \
"buffer_load_dwordx4 v234, s[40:43], s76 offen offset:1024 sc1 lds\n" \
"v_mfma_f32_16x16x32_bf16 v[176:179], a[224:227], v[80:83], v[176:179]\n" \
"v_exp_f32 v33, v33\n" \
"v_mfma_f32_16x16x32_bf16 v[176:179], a[228:231], v[84:87], v[176:179]\n" \
"ds_read_b128 a[220:223], v232 offset:6400\n" \
"v_cvt_pk_bf16_f32 v32, v32, v33\n" \
"v_mfma_f32_16x16x32_bf16 a[80:83], a[224:227], v[96:99], a[80:83]\n" \
"v_exp_f32 v34, v34\n" \
"v_mfma_f32_16x16x32_bf16 a[80:83], a[228:231], v[100:103], a[80:83]\n" \
"buffer_load_dwordx4 v234, s[40:43], s76 offen offset:2048 sc1 lds\n" \
"v_mfma_f32_16x16x32_bf16 a[112:115], a[224:227], v[112:115], a[112:115]\n" \
"v_exp_f32 v35, v35\n" \
"v_mfma_f32_16x16x32_bf16 a[112:115], a[228:231], v[116:119], a[112:115]\n" \
"ds_read_b128 a[224:227], v232 offset:8192\n" \
"v_cvt_pk_bf16_f32 v33, v34, v35\n" \
"v_mfma_f32_16x16x32_bf16 a[116:119], a[232:235], v[112:115], a[116:119]\n" \
"v_exp_f32 v36, v36\n" \
"v_mfma_f32_16x16x32_bf16 a[116:119], a[236:239], v[116:119], a[116:119]\n" \
"buffer_load_dwordx4 v234, s[40:43], s76 offen offset:3072 sc1 lds\n" \
"v_mfma_f32_16x16x32_bf16 a[84:87], a[232:235], v[96:99], a[84:87]\n" \
"v_exp_f32 v37, v37\n" \
"v_mfma_f32_16x16x32_bf16 a[84:87], a[236:239], v[100:103], a[84:87]\n" \
"s_add_u32 m0, m0, 0x4000\n" \
"ds_read_b128 a[228:231], v232 offset:10240\n" \
"v_cvt_pk_bf16_f32 v34, v36, v37\n" \
"v_smfmac_f32_16x16x64_bf16 v[224:227], v[244:247], v[96:103], v243\n" \
"buffer_load_dwordx4 v234, s[40:43], s71 offen offset:0 sc1 lds\n" \
"v_mfma_f32_16x16x32_bf16 v[180:183], a[232:235], v[80:83], v[180:183]\n" \
"v_exp_f32 v38, v38\n" \
"v_mfma_f32_16x16x32_bf16 v[180:183], a[236:239], v[84:87], v[180:183]\n" \
"buffer_load_dwordx4 v234, s[40:43], s71 offen offset:1024 sc1 lds\n" \
"v_mfma_f32_16x16x32_bf16 v[148:151], a[232:235], v[64:67], v[148:151]\n" \
"v_exp_f32 v39, v39\n" \
"v_mfma_f32_16x16x32_bf16 v[148:151], a[236:239], v[68:71], v[148:151]\n" \
"ds_read_b128 a[232:235], v232 offset:12288\n" \
"v_cvt_pk_bf16_f32 v35, v38, v39\n" \
"v_mfma_f32_16x16x32_bf16 v[152:155], a[32:35], v[64:67], v[152:155]\n" \
"v_exp_f32 v48, v48\n" \
"v_mfma_f32_16x16x32_bf16 v[152:155], a[36:39], v[68:71], v[152:155]\n" \
"buffer_load_dwordx4 v234, s[40:43], s71 offen offset:2048 sc1 lds\n" \
"v_mfma_f32_16x16x32_bf16 v[184:187], a[32:35], v[80:83], v[184:187]\n" \
"v_exp_f32 v49, v49\n" \
"v_mfma_f32_16x16x32_bf16 v[184:187], a[36:39], v[84:87], v[184:187]\n" \
"ds_read_b128 a[236:239], v232 offset:14336\n" \
"v_cvt_pk_bf16_f32 v48, v48, v49\n" \
"v_mfma_f32_16x16x32_bf16 a[88:91], a[32:35], v[96:99], a[88:91]\n" \
"v_exp_f32 v50, v50\n" \
"v_mfma_f32_16x16x32_bf16 a[88:91], a[36:39], v[100:103], a[88:91]\n" \
"buffer_load_dwordx4 v234, s[40:43], s71 offen offset:3072 sc1 lds\n" \
"v_mfma_f32_16x16x32_bf16 a[120:123], a[32:35], v[112:115], a[120:123]\n" \
"s_mov_b32 s76, s73\n" \
"v_exp_f32 v51, v51\n" \
"v_mfma_f32_16x16x32_bf16 a[120:123], a[36:39], v[116:119], a[120:123]\n" \
"s_add_u32 s73, s73, 34816\n" \
"ds_read_b128 a[244:247], v232 offset:10496\n" \
"v_cvt_pk_bf16_f32 v49, v50, v51\n" \
"v_mfma_f32_16x16x32_bf16 a[124:127], a[40:43], v[112:115], a[124:127]\n" \
"s_xor_b32 s74, s74, 0x8000\n" \
"v_exp_f32 v52, v52\n" \
"v_mfma_f32_16x16x32_bf16 a[124:127], a[44:47], v[116:119], a[124:127]\n" \
"s_xor_b32 s77, s77, 0x8000\n" \
"v_xor_b32 v233, s81, v233\n" \
"s_mov_b32 s81, 0x8000\n" \
"v_mfma_f32_16x16x32_bf16 a[92:95], a[40:43], v[96:99], a[92:95]\n" \
"v_exp_f32 v53, v53\n" \
"v_mfma_f32_16x16x32_bf16 a[92:95], a[44:47], v[100:103], a[92:95]\n" \
"ds_read_b128 a[248:251], v232 offset:12544\n" \
"v_cvt_pk_bf16_f32 v50, v52, v53\n" \
"v_smfmac_f32_16x16x64_bf16 v[228:231], v[244:247], v[112:119], v243\n" \
"v_mfma_f32_16x16x32_bf16 v[188:191], a[40:43], v[80:83], v[188:191]\n" \
"v_exp_f32 v54, v54\n" \
"v_mfma_f32_16x16x32_bf16 v[188:191], a[44:47], v[84:87], v[188:191]\n" \
"s_cmp_lg_u32 s82, 0\n" \
"v_exp_f32 v55, v55\n" \
"v_mfma_f32_16x16x32_bf16 v[156:159], a[40:43], v[64:67], v[156:159]\n" \
"s_waitcnt lgkmcnt(2)\n" \
"ds_read_b128 a[252:255], v232 offset:14592\n" \
"v_cvt_pk_bf16_f32 v51, v54, v55\n" \
"v_mfma_f32_16x16x32_bf16 v[156:159], a[44:47], v[68:71], v[156:159]\n" \
"s_cbranch_scc1 Lep_%=\n" \
"Lepr_%=:\n" \
"v_mfma_f32_16x16x32_bf16 v[64:67], a[192:195], a[0:3], v[196:199]\n" \
"v_exp_f32 v8, v8\n" \
"v_mfma_f32_16x16x32_bf16 v[64:67], a[196:199], a[4:7], v[64:67]\n" \
"ds_read_b128 a[44:47], v233 offset:15360\n" \
"v_mfma_f32_16x16x32_bf16 v[64:67], a[200:203], a[8:11], v[64:67]\n" \
"v_exp_f32 v9, v9\n" \
"v_mfma_f32_16x16x32_bf16 v[64:67], a[204:207], a[12:15], v[64:67]\n" \
"ds_read_b128 a[32:35], v233 offset:6144\n" \
"v_cvt_pk_bf16_f32 v4, v8, v9\n" \
"v_mfma_f32_16x16x32_bf16 v[80:83], a[204:207], a[28:31], v[196:199]\n" \
"s_sub_u32 s84, s84, 1\n" \
"v_exp_f32 v10, v10\n" \
"v_mfma_f32_16x16x32_bf16 v[80:83], a[200:203], a[24:27], v[80:83]\n" \
"s_cmp_eq_u32 s84, 0\n" \
"s_cselect_b32 s70, s75, 0x8000\n" \
"v_xor_b32 v232, s70, v232\n" \
"v_mfma_f32_16x16x32_bf16 v[80:83], a[196:199], a[20:23], v[80:83]\n" \
"v_exp_f32 v11, v11\n" \
"v_mfma_f32_16x16x32_bf16 v[80:83], a[192:195], a[16:19], v[80:83]\n" \
"ds_read_b128 a[36:39], v233 offset:14336\n" \
"v_cvt_pk_bf16_f32 v5, v10, v11\n" \
"v_mfma_f32_16x16x32_bf16 v[96:99], a[192:195], a[160:163], v[196:199]\n" \
"v_exp_f32 v12, v12\n" \
"v_mfma_f32_16x16x32_bf16 v[96:99], a[196:199], a[164:167], v[96:99]\n" \
"v_mfma_f32_16x16x32_bf16 v[96:99], a[200:203], a[168:171], v[96:99]\n" \
"v_exp_f32 v13, v13\n" \
"v_mfma_f32_16x16x32_bf16 v[96:99], a[204:207], a[172:175], v[96:99]\n" \
"ds_read_b128 a[40:43], v233 offset:7168\n" \
"v_cvt_pk_bf16_f32 v6, v12, v13\n" \
"v_mfma_f32_16x16x32_bf16 v[112:115], a[204:207], a[188:191], v[196:199]\n" \
"v_exp_f32 v14, v14\n" \
"v_mfma_f32_16x16x32_bf16 v[112:115], a[200:203], a[184:187], v[112:115]\n" \
"v_mfma_f32_16x16x32_bf16 v[112:115], a[196:199], a[180:183], v[112:115]\n" \
"v_exp_f32 v15, v15\n" \
"v_mfma_f32_16x16x32_bf16 v[112:115], a[192:195], a[176:179], v[112:115]\n" \
"ds_read_b128 a[204:207], v233 offset:9216\n" \
"v_cvt_pk_bf16_f32 v7, v14, v15\n" \
"v_mfma_f32_16x16x32_bf16 v[116:119], a[208:211], a[176:179], v[196:199]\n" \
"v_exp_f32 v24, v24\n" \
"v_mfma_f32_16x16x32_bf16 v[116:119], a[212:215], a[180:183], v[116:119]\n" \
"v_mfma_f32_16x16x32_bf16 v[116:119], a[216:219], a[184:187], v[116:119]\n" \
"v_exp_f32 v25, v25\n" \
"v_mfma_f32_16x16x32_bf16 v[116:119], a[220:223], a[188:191], v[116:119]\n" \
"ds_read_b128 a[200:203], v233 offset:1024\n" \
"v_cvt_pk_bf16_f32 v20, v24, v25\n" \
"v_mfma_f32_16x16x32_bf16 v[100:103], a[220:223], a[172:175], v[196:199]\n" \
"v_exp_f32 v26, v26\n" \
"v_mfma_f32_16x16x32_bf16 v[100:103], a[216:219], a[168:171], v[100:103]\n" \
"v_mfma_f32_16x16x32_bf16 v[100:103], a[212:215], a[164:167], v[100:103]\n" \
"v_exp_f32 v27, v27\n" \
"v_mfma_f32_16x16x32_bf16 v[100:103], a[208:211], a[160:163], v[100:103]\n" \
"ds_read_b128 a[196:199], v233 offset:8192\n" \
"v_cvt_pk_bf16_f32 v21, v26, v27\n" \
"v_mfma_f32_16x16x32_bf16 v[84:87], a[208:211], a[16:19], v[196:199]\n" \
"v_exp_f32 v28, v28\n" \
"v_mfma_f32_16x16x32_bf16 v[84:87], a[212:215], a[20:23], v[84:87]\n" \
"v_mfma_f32_16x16x32_bf16 v[84:87], a[216:219], a[24:27], v[84:87]\n" \
"v_exp_f32 v29, v29\n" \
"v_mfma_f32_16x16x32_bf16 v[84:87], a[220:223], a[28:31], v[84:87]\n" \
"ds_read_b128 a[192:195], v233 offset:0\n" \
"v_cvt_pk_bf16_f32 v22, v28, v29\n" \
"v_mfma_f32_16x16x32_bf16 v[68:71], a[220:223], a[12:15], v[196:199]\n" \
"v_exp_f32 v30, v30\n" \
"v_mfma_f32_16x16x32_bf16 v[68:71], a[216:219], a[8:11], v[68:71]\n" \
"v_mfma_f32_16x16x32_bf16 v[68:71], a[212:215], a[4:7], v[68:71]\n" \
"s_waitcnt lgkmcnt(8)\n" \
"v_exp_f32 v31, v31\n" \
"v_mfma_f32_16x16x32_bf16 v[68:71], a[208:211], a[0:3], v[68:71]\n" \
"ds_read_b128 a[220:223], v233 offset:11264\n" \
"v_cvt_pk_bf16_f32 v23, v30, v31\n" \
"v_mfma_f32_16x16x32_bf16 v[72:75], a[224:227], a[0:3], v[196:199]\n" \
"v_exp_f32 v40, v40\n" \
"v_mfma_f32_16x16x32_bf16 v[72:75], a[228:231], a[4:7], v[72:75]\n" \
"v_mfma_f32_16x16x32_bf16 v[72:75], a[232:235], a[8:11], v[72:75]\n" \
"v_exp_f32 v41, v41\n" \
"v_mfma_f32_16x16x32_bf16 v[72:75], a[236:239], a[12:15], v[72:75]\n" \
"ds_read_b128 a[216:219], v233 offset:3072\n" \
"v_cvt_pk_bf16_f32 v36, v40, v41\n" \
"v_mfma_f32_16x16x32_bf16 v[88:91], a[236:239], a[28:31], v[196:199]\n" \
"v_exp_f32 v42, v42\n" \
"v_mfma_f32_16x16x32_bf16 v[88:91], a[232:235], a[24:27], v[88:91]\n" \
"v_mfma_f32_16x16x32_bf16 v[88:91], a[228:231], a[20:23], v[88:91]\n" \
"v_exp_f32 v43, v43\n" \
"v_mfma_f32_16x16x32_bf16 v[88:91], a[224:227], a[16:19], v[88:91]\n" \
"ds_read_b128 a[212:215], v233 offset:10240\n" \
"v_cvt_pk_bf16_f32 v37, v42, v43\n" \
"v_mfma_f32_16x16x32_bf16 v[104:107], a[224:227], a[160:163], v[196:199]\n" \
"v_exp_f32 v44, v44\n" \
"v_mfma_f32_16x16x32_bf16 v[104:107], a[228:231], a[164:167], v[104:107]\n" \
"v_mfma_f32_16x16x32_bf16 v[104:107], a[232:235], a[168:171], v[104:107]\n" \
"v_exp_f32 v45, v45\n" \
"v_mfma_f32_16x16x32_bf16 v[104:107], a[236:239], a[172:175], v[104:107]\n" \
"ds_read_b128 a[208:211], v233 offset:2048\n" \
"v_cvt_pk_bf16_f32 v38, v44, v45\n" \
"v_mfma_f32_16x16x32_bf16 v[120:123], a[236:239], a[188:191], v[196:199]\n" \
"v_exp_f32 v46, v46\n" \
"v_mfma_f32_16x16x32_bf16 v[120:123], a[232:235], a[184:187], v[120:123]\n" \
"v_mfma_f32_16x16x32_bf16 v[120:123], a[228:231], a[180:183], v[120:123]\n" \
"v_exp_f32 v47, v47\n" \
"v_mfma_f32_16x16x32_bf16 v[120:123], a[224:227], a[176:179], v[120:123]\n" \
"ds_read_b128 a[236:239], v233 offset:13312\n" \
"v_cvt_pk_bf16_f32 v39, v46, v47\n" \
"v_mfma_f32_16x16x32_bf16 v[124:127], a[240:243], a[176:179], v[196:199]\n" \
"v_exp_f32 v56, v56\n" \
"v_mfma_f32_16x16x32_bf16 v[124:127], a[244:247], a[180:183], v[124:127]\n" \
"ds_read_b128 a[232:235], v233 offset:5120\n" \
"v_mfma_f32_16x16x32_bf16 v[124:127], a[248:251], a[184:187], v[124:127]\n" \
"v_exp_f32 v57, v57\n" \
"v_mfma_f32_16x16x32_bf16 v[124:127], a[252:255], a[188:191], v[124:127]\n" \
"ds_read_b128 a[228:231], v233 offset:12288\n" \
"v_cvt_pk_bf16_f32 v52, v56, v57\n" \
"v_mfma_f32_16x16x32_bf16 v[108:111], a[252:255], a[172:175], v[196:199]\n" \
"v_exp_f32 v58, v58\n" \
"v_mfma_f32_16x16x32_bf16 v[108:111], a[248:251], a[168:171], v[108:111]\n" \
"ds_read_b128 a[224:227], v233 offset:4096\n" \
"v_mfma_f32_16x16x32_bf16 v[108:111], a[244:247], a[164:167], v[108:111]\n" \
"v_exp_f32 v59, v59\n" \
"v_mfma_f32_16x16x32_bf16 v[108:111], a[240:243], a[160:163], v[108:111]\n" \
"v_cvt_pk_bf16_f32 v53, v58, v59\n" \
"v_mfma_f32_16x16x32_bf16 v[92:95], a[240:243], a[16:19], v[196:199]\n" \
"v_exp_f32 v60, v60\n" \
"v_mfma_f32_16x16x32_bf16 v[92:95], a[244:247], a[20:23], v[92:95]\n" \
"v_mfma_f32_16x16x32_bf16 v[92:95], a[248:251], a[24:27], v[92:95]\n" \
"v_exp_f32 v61, v61\n" \
"v_mfma_f32_16x16x32_bf16 v[92:95], a[252:255], a[28:31], v[92:95]\n" \
"v_cvt_pk_bf16_f32 v54, v60, v61\n" \
"v_mfma_f32_16x16x32_bf16 v[76:79], a[252:255], a[12:15], v[196:199]\n" \
"v_exp_f32 v62, v62\n" \
"v_mfma_f32_16x16x32_bf16 v[76:79], a[248:251], a[8:11], v[76:79]\n" \
"s_cmp_lg_u32 s82, 0\n" \
"s_cbranch_scc1 Lnw_%=\n" \
"s_waitcnt vmcnt(0)\n" \
"Lnw_%=:\n" \
"v_mfma_f32_16x16x32_bf16 v[76:79], a[244:247], a[4:7], v[76:79]\n" \
"s_waitcnt lgkmcnt(0)\n" \
"v_exp_f32 v63, v63\n" \
"v_mfma_f32_16x16x32_bf16 v[76:79], a[240:243], a[0:3], v[76:79]\n" \
"s_barrier\n" \
"v_cvt_pk_bf16_f32 v55, v62, v63\n" \
"v_mfma_f32_16x16x32_bf16 v[128:131], a[192:195], v[0:3], v[128:131]\n" \
"s_add_u32 m0, s74, s79\n" \
"v_exp_f32 v64, v64\n" \
"v_mfma_f32_16x16x32_bf16 v[128:131], a[196:199], v[4:7], v[128:131]\n" \
"buffer_load_dwordx4 v234, s[36:39], s73 offen offset:0 sc1 lds\n" \
"v_mfma_f32_16x16x32_bf16 v[160:163], a[192:195], v[16:19], v[160:163]\n" \
"v_exp_f32 v65, v65\n" \
"v_mfma_f32_16x16x32_bf16 v[160:163], a[196:199], v[20:23], v[160:163]\n" \
"ds_read_b128 a[240:243], v232 offset:8448\n" \
"v_cvt_pk_bf16_f32 v64, v64, v65\n" \
"v_mfma_f32_16x16x32_bf16 a[64:67], a[192:195], v[32:35], a[64:67]\n" \
"s_add_u32 s71, s73, 17408\n" \
"v_exp_f32 v66, v66\n" \
"v_mfma_f32_16x16x32_bf16 a[64:67], a[196:199], v[36:39], a[64:67]\n" \
"buffer_load_dwordx4 v234, s[36:39], s73 offen offset:1024 sc1 lds\n" \
"v_mfma_f32_16x16x32_bf16 a[96:99], a[192:195], v[48:51], a[96:99]\n" \
"v_exp_f32 v67, v67\n" \
"v_mfma_f32_16x16x32_bf16 a[96:99], a[196:199], v[52:55], a[96:99]\n" \
"ds_read_b128 a[192:195], v232 offset:0\n" \
"v_cvt_pk_bf16_f32 v65, v66, v67\n" \
"v_mfma_f32_16x16x32_bf16 a[100:103], a[200:203], v[48:51], a[100:103]\n" \
"v_exp_f32 v68, v68\n" \
"v_mfma_f32_16x16x32_bf16 a[100:103], a[204:207], v[52:55], a[100:103]\n" \
"buffer_load_dwordx4 v234, s[36:39], s73 offen offset:2048 sc1 lds\n" \
"v_mfma_f32_16x16x32_bf16 a[68:71], a[200:203], v[32:35], a[68:71]\n" \
"v_exp_f32 v69, v69\n" \
"v_mfma_f32_16x16x32_bf16 a[68:71], a[204:207], v[36:39], a[68:71]\n" \
"ds_read_b128 a[196:199], v232 offset:2048\n" \
"v_cvt_pk_bf16_f32 v66, v68, v69\n" \
"v_smfmac_f32_16x16x64_bf16 a[48:51], v[244:247], v[0:7], v243\n" \
"buffer_load_dwordx4 v234, s[36:39], s73 offen offset:3072 sc1 lds\n" \
"v_mfma_f32_16x16x32_bf16 v[164:167], a[200:203], v[16:19], v[164:167]\n" \
"s_add_u32 m0, m0, 0x4000\n" \
"v_exp_f32 v70, v70\n" \
"v_mfma_f32_16x16x32_bf16 v[164:167], a[204:207], v[20:23], v[164:167]\n" \
"buffer_load_dwordx4 v234, s[36:39], s71 offen offset:0 sc1 lds\n" \
"v_mfma_f32_16x16x32_bf16 v[132:135], a[200:203], v[0:3], v[132:135]\n" \
"v_exp_f32 v71, v71\n" \
"v_mfma_f32_16x16x32_bf16 v[132:135], a[204:207], v[4:7], v[132:135]\n" \
"ds_read_b128 a[200:203], v232 offset:4096\n" \
"v_cvt_pk_bf16_f32 v67, v70, v71\n" \
"v_mfma_f32_16x16x32_bf16 v[136:139], a[208:211], v[0:3], v[136:139]\n" \
"v_exp_f32 v80, v80\n" \
"v_mfma_f32_16x16x32_bf16 v[136:139], a[212:215], v[4:7], v[136:139]\n" \
"buffer_load_dwordx4 v234, s[36:39], s71 offen offset:1024 sc1 lds\n" \
"v_mfma_f32_16x16x32_bf16 v[168:171], a[208:211], v[16:19], v[168:171]\n" \
"v_exp_f32 v81, v81\n" \
"v_mfma_f32_16x16x32_bf16 v[168:171], a[212:215], v[20:23], v[168:171]\n" \
"ds_read_b128 a[204:207], v232 offset:6144\n" \
"v_cvt_pk_bf16_f32 v80, v80, v81\n" \
"v_mfma_f32_16x16x32_bf16 a[72:75], a[208:211], v[32:35], a[72:75]\n" \
"v_exp_f32 v82, v82\n" \
"v_mfma_f32_16x16x32_bf16 a[72:75], a[212:215], v[36:39], a[72:75]\n" \
"buffer_load_dwordx4 v234, s[36:39], s71 offen offset:2048 sc1 lds\n" \
"v_mfma_f32_16x16x32_bf16 a[104:107], a[208:211], v[48:51], a[104:107]\n" \
"v_exp_f32 v83, v83\n" \
"v_mfma_f32_16x16x32_bf16 a[104:107], a[212:215], v[52:55], a[104:107]\n" \
"ds_read_b128 a[208:211], v232 offset:256\n" \
"v_cvt_pk_bf16_f32 v81, v82, v83\n" \
"v_mfma_f32_16x16x32_bf16 a[108:111], a[216:219], v[48:51], a[108:111]\n" \
"v_exp_f32 v84, v84\n" \
"v_mfma_f32_16x16x32_bf16 a[108:111], a[220:223], v[52:55], a[108:111]\n" \
"buffer_load_dwordx4 v234, s[36:39], s71 offen offset:3072 sc1 lds\n" \
"v_mfma_f32_16x16x32_bf16 a[76:79], a[216:219], v[32:35], a[76:79]\n" \
"v_exp_f32 v85, v85\n" \
"v_mfma_f32_16x16x32_bf16 a[76:79], a[220:223], v[36:39], a[76:79]\n" \
"s_add_u32 m0, s77, s80\n" \
"ds_read_b128 a[212:215], v232 offset:2304\n" \
"v_cvt_pk_bf16_f32 v82, v84, v85\n" \
"v_smfmac_f32_16x16x64_bf16 a[52:55], v[244:247], v[16:23], v243\n" \
"buffer_load_dwordx4 v234, s[40:43], s76 offen offset:0 sc1 lds\n" \
"v_mfma_f32_16x16x32_bf16 v[172:175], a[216:219], v[16:19], v[172:175]\n" \
"s_add_u32 s71, s76, 17408\n" \
"v_exp_f32 v86, v86\n" \
"v_mfma_f32_16x16x32_bf16 v[172:175], a[220:223], v[20:23], v[172:175]\n" \
"buffer_load_dwordx4 v234, s[40:43], s76 offen offset:1024 sc1 lds\n" \
"v_mfma_f32_16x16x32_bf16 v[140:143], a[216:219], v[0:3], v[140:143]\n" \
"v_exp_f32 v87, v87\n" \
"v_mfma_f32_16x16x32_bf16 v[140:143], a[220:223], v[4:7], v[140:143]\n" \
"ds_read_b128 a[216:219], v232 offset:4352\n" \
"v_cvt_pk_bf16_f32 v83, v86, v87\n" \
"v_mfma_f32_16x16x32_bf16 v[144:147], a[224:227], v[0:3], v[144:147]\n" \
"v_exp_f32 v96, v96\n" \
"v_mfma_f32_16x16x32_bf16 v[144:147], a[228:231], v[4:7], v[144:147]\n" \
"buffer_load_dwordx4 v234, s[40:43], s76 offen offset:2048 sc1 lds\n" \
"v_mfma_f32_16x16x32_bf16 v[176:179], a[224:227], v[16:19], v[176:179]\n" \
"v_exp_f32 v97, v97\n" \
"v_mfma_f32_16x16x32_bf16 v[176:179], a[228:231], v[20:23], v[176:179]\n" \
"ds_read_b128 a[220:223], v232 offset:6400\n" \
"v_cvt_pk_bf16_f32 v96, v96, v97\n" \
"v_mfma_f32_16x16x32_bf16 a[80:83], a[224:227], v[32:35], a[80:83]\n" \
"v_exp_f32 v98, v98\n" \
"v_mfma_f32_16x16x32_bf16 a[80:83], a[228:231], v[36:39], a[80:83]\n" \
"buffer_load_dwordx4 v234, s[40:43], s76 offen offset:3072 sc1 lds\n" \
"v_mfma_f32_16x16x32_bf16 a[112:115], a[224:227], v[48:51], a[112:115]\n" \
"v_exp_f32 v99, v99\n" \
"v_mfma_f32_16x16x32_bf16 a[112:115], a[228:231], v[52:55], a[112:115]\n" \
"ds_read_b128 a[224:227], v232 offset:8192\n" \
"v_cvt_pk_bf16_f32 v97, v98, v99\n" \
"v_mfma_f32_16x16x32_bf16 a[116:119], a[232:235], v[48:51], a[116:119]\n" \
"s_add_u32 m0, m0, 0x4000\n" \
"v_exp_f32 v100, v100\n" \
"v_mfma_f32_16x16x32_bf16 a[116:119], a[236:239], v[52:55], a[116:119]\n" \
"buffer_load_dwordx4 v234, s[40:43], s71 offen offset:0 sc1 lds\n" \
"v_mfma_f32_16x16x32_bf16 a[84:87], a[232:235], v[32:35], a[84:87]\n" \
"v_exp_f32 v101, v101\n" \
"v_mfma_f32_16x16x32_bf16 a[84:87], a[236:239], v[36:39], a[84:87]\n" \
"ds_read_b128 a[228:231], v232 offset:10240\n" \
"v_cvt_pk_bf16_f32 v98, v100, v101\n" \
"v_smfmac_f32_16x16x64_bf16 v[224:227], v[244:247], v[32:39], v243\n" \
"buffer_load_dwordx4 v234, s[40:43], s71 offen offset:1024 sc1 lds\n" \
"v_mfma_f32_16x16x32_bf16 v[180:183], a[232:235], v[16:19], v[180:183]\n" \
"v_exp_f32 v102, v102\n" \
"v_mfma_f32_16x16x32_bf16 v[180:183], a[236:239], v[20:23], v[180:183]\n" \
"buffer_load_dwordx4 v234, s[40:43], s71 offen offset:2048 sc1 lds\n" \
"v_mfma_f32_16x16x32_bf16 v[148:151], a[232:235], v[0:3], v[148:151]\n" \
"v_exp_f32 v103, v103\n" \
"v_mfma_f32_16x16x32_bf16 v[148:151], a[236:239], v[4:7], v[148:151]\n" \
"ds_read_b128 a[232:235], v232 offset:12288\n" \
"v_cvt_pk_bf16_f32 v99, v102, v103\n" \
"v_mfma_f32_16x16x32_bf16 v[152:155], a[32:35], v[0:3], v[152:155]\n" \
"v_exp_f32 v112, v112\n" \
"v_mfma_f32_16x16x32_bf16 v[152:155], a[36:39], v[4:7], v[152:155]\n" \
"buffer_load_dwordx4 v234, s[40:43], s71 offen offset:3072 sc1 lds\n" \
"v_mfma_f32_16x16x32_bf16 v[184:187], a[32:35], v[16:19], v[184:187]\n" \
"v_exp_f32 v113, v113\n" \
"v_mfma_f32_16x16x32_bf16 v[184:187], a[36:39], v[20:23], v[184:187]\n" \
"ds_read_b128 a[236:239], v232 offset:14336\n" \
"v_cvt_pk_bf16_f32 v112, v112, v113\n" \
"v_mfma_f32_16x16x32_bf16 a[88:91], a[32:35], v[32:35], a[88:91]\n" \
"v_exp_f32 v114, v114\n" \
"v_mfma_f32_16x16x32_bf16 a[88:91], a[36:39], v[36:39], a[88:91]\n" \
"buffer_load_dword v239, v242, s[56:59], s95 offen sc1\n" \
"v_mfma_f32_16x16x32_bf16 a[120:123], a[32:35], v[48:51], a[120:123]\n" \
"s_mov_b32 s69, s92\n" \
"v_exp_f32 v115, v115\n" \
"v_mfma_f32_16x16x32_bf16 a[120:123], a[36:39], v[52:55], a[120:123]\n" \
"s_mov_b32 s76, s73\n" \
"ds_read_b128 a[244:247], v232 offset:10496\n" \
"v_cvt_pk_bf16_f32 v113, v114, v115\n" \
"v_mfma_f32_16x16x32_bf16 a[124:127], a[40:43], v[48:51], a[124:127]\n" \
"s_add_u32 s73, s73, 34816\n" \
"v_exp_f32 v116, v116\n" \
"v_mfma_f32_16x16x32_bf16 a[124:127], a[44:47], v[52:55], a[124:127]\n" \
"s_xor_b32 s74, s74, 0x8000\n" \
"s_xor_b32 s77, s77, 0x8000\n" \
"v_xor_b32 v233, 0x8000, v233\n" \
"v_mfma_f32_16x16x32_bf16 a[92:95], a[40:43], v[32:35], a[92:95]\n" \
"s_mov_b32 s82, 0\n" \
"v_exp_f32 v117, v117\n" \
"v_mfma_f32_16x16x32_bf16 a[92:95], a[44:47], v[36:39], a[92:95]\n" \
"ds_read_b128 a[248:251], v232 offset:12544\n" \
"v_cvt_pk_bf16_f32 v114, v116, v117\n" \
"v_smfmac_f32_16x16x64_bf16 v[228:231], v[244:247], v[48:55], v243\n" \
"v_mfma_f32_16x16x32_bf16 v[188:191], a[40:43], v[16:19], v[188:191]\n" \
"v_exp_f32 v118, v118\n" \
"v_mfma_f32_16x16x32_bf16 v[188:191], a[44:47], v[20:23], v[188:191]\n" \
"s_cmp_lg_u32 s84, 0\n" \
"v_exp_f32 v119, v119\n" \
"v_mfma_f32_16x16x32_bf16 v[156:159], a[40:43], v[0:3], v[156:159]\n" \
"s_waitcnt lgkmcnt(2)\n" \
"ds_read_b128 a[252:255], v232 offset:14592\n" \
"Lpg6789_%=:\n" \
"s_cbranch_execz Lcb0_%=\n" \
"v_cvt_pk_bf16_f32 v115, v118, v119\n" \
"v_mfma_f32_16x16x32_bf16 v[156:159], a[44:47], v[4:7], v[156:159]\n" \
"s_cbranch_scc1 Lloop2_%=\n" \
"Lg_%=:\n" \
"Lpg6794_%=:\n" \
"s_cbranch_execz Lcb0_%=\n" \
"v_mfma_f32_16x16x32_bf16 v[32:35], a[192:195], a[160:163], v[208:211]\n" \
"s_mov_b64 s[44:45], s[20:21]\n" \
"s_memrealtime s[14:15]\n" \
"v_exp_f32 v72, v72\n" \
"v_mfma_f32_16x16x32_bf16 v[32:35], a[196:199], a[164:167], v[32:35]\n" \
"s_and_b32 s45, s45, 0xffff\n" \
"ds_read_b128 a[32:35], v233 offset:6144\n" \
"ds_read_b128 a[36:39], v233 offset:14336\n" \
"v_mfma_f32_16x16x32_bf16 v[32:35], a[200:203], a[168:171], v[32:35]\n" \
"s_mov_b32 s46, 4194304\n" \
"v_exp_f32 v73, v73\n" \
"v_mfma_f32_16x16x32_bf16 v[32:35], a[204:207], a[172:175], v[32:35]\n" \
"ds_read_b128 a[40:43], v233 offset:7168\n" \
"ds_read_b128 a[44:47], v233 offset:15360\n" \
"global_load_dword v253, v254, s[22:23]\n" \
"v_mfma_f32_16x16x32_bf16 v[48:51], a[204:207], a[188:191], v[196:199]\n" \
"s_mov_b32 s47, 0x20000\n" \
"v_exp_f32 v74, v74\n" \
"v_mfma_f32_16x16x32_bf16 v[48:51], a[200:203], a[184:187], v[48:51]\n" \
"s_mov_b64 s[48:49], s[22:23]\n" \
"v_exp_f32 v75, v75\n" \
"v_mfma_f32_16x16x32_bf16 v[48:51], a[196:199], a[180:183], v[48:51]\n" \
"s_and_b32 s49, s49, 0xffff\n" \
"v_exp_f32 v76, v76\n" \
"v_mfma_f32_16x16x32_bf16 v[48:51], a[192:195], a[176:179], v[48:51]\n" \
"s_mov_b32 s50, 6291456\n" \
"ds_read_b128 a[200:203], v233 offset:1024\n" \
"ds_read_b128 a[204:207], v233 offset:9216\n" \
"v_mfma_f32_16x16x32_bf16 v[52:55], a[208:211], a[176:179], v[196:199]\n" \
"s_mov_b32 s51, 0x20000\n" \
"v_cvt_pk_bf16_f32 v68, v72, v73\n" \
"v_cvt_pk_bf16_f32 v69, v74, v75\n" \
"v_mfma_f32_16x16x32_bf16 v[52:55], a[212:215], a[180:183], v[52:55]\n" \
"s_bfe_u32 s94, %1, 0x20001\n" \
"v_exp_f32 v77, v77\n" \
"v_mfma_f32_16x16x32_bf16 v[52:55], a[216:219], a[184:187], v[52:55]\n" \
"s_lshl_b32 s94, s94, 2\n" \
"v_exp_f32 v78, v78\n" \
"v_mfma_f32_16x16x32_bf16 v[52:55], a[220:223], a[188:191], v[52:55]\n" \
"s_lshr_b32 s70, %1, 6\n" \
"ds_read_b128 a[192:195], v233 offset:0\n" \
"ds_read_b128 a[196:199], v233 offset:8192\n" \
"v_mfma_f32_16x16x32_bf16 v[36:39], a[220:223], a[172:175], v[212:215]\n" \
"s_add_u32 s94, s94, s70\n" \
"v_exp_f32 v79, v79\n" \
"v_mfma_f32_16x16x32_bf16 v[36:39], a[216:219], a[168:171], v[36:39]\n" \
"s_lshr_b32 s71, s22, 7\n" \
"v_exp_f32 v88, v88\n" \
"v_mfma_f32_16x16x32_bf16 v[36:39], a[212:215], a[164:167], v[36:39]\n" \
"s_sub_u32 s71, s63, s71\n" \
"v_cvt_pk_bf16_f32 v70, v76, v77\n" \
"v_cvt_pk_bf16_f32 v71, v78, v79\n" \
"v_mfma_f32_16x16x32_bf16 v[36:39], a[208:211], a[160:163], v[36:39]\n" \
"s_add_u32 s71, s71, 6\n" \
"ds_read_b128 a[216:219], v233 offset:3072\n" \
"ds_read_b128 a[220:223], v233 offset:11264\n" \
"v_mfma_f32_16x16x32_bf16 v[40:43], a[224:227], a[160:163], v[216:219]\n" \
"s_and_b32 s71, s71, 7\n" \
"v_exp_f32 v89, v89\n" \
"v_mfma_f32_16x16x32_bf16 v[40:43], a[228:231], a[164:167], v[40:43]\n" \
"s_and_b32 s70, %1, 1\n" \
"v_exp_f32 v90, v90\n" \
"v_mfma_f32_16x16x32_bf16 v[40:43], a[232:235], a[168:171], v[40:43]\n" \
"s_lshl_b32 s70, s70, 3\n" \
"v_exp_f32 v91, v91\n" \
"v_mfma_f32_16x16x32_bf16 v[40:43], a[236:239], a[172:175], v[40:43]\n" \
"s_add_u32 s70, s70, s71\n" \
"ds_read_b128 a[208:211], v233 offset:2048\n" \
"ds_read_b128 a[212:215], v233 offset:10240\n" \
"v_mfma_f32_16x16x32_bf16 v[56:59], a[236:239], a[188:191], v[196:199]\n" \
"s_lshl_b32 s78, s70, 7\n" \
"v_exp_f32 v92, v92\n" \
"v_mfma_f32_16x16x32_bf16 v[56:59], a[232:235], a[184:187], v[56:59]\n" \
"s_lshr_b32 s80, %1, 6\n" \
"v_cvt_pk_bf16_f32 v84, v88, v89\n" \
"v_cvt_pk_bf16_f32 v85, v90, v91\n" \
"v_mfma_f32_16x16x32_bf16 v[56:59], a[228:231], a[180:183], v[56:59]\n" \
"s_xor_b32 s80, s80, s63\n" \
"v_exp_f32 v93, v93\n" \
"v_mfma_f32_16x16x32_bf16 v[56:59], a[224:227], a[176:179], v[56:59]\n" \
"s_and_b32 s80, s80, 0\n" \
"ds_read_b128 a[232:235], v233 offset:5120\n" \
"ds_read_b128 a[236:239], v233 offset:13312\n" \
"v_mfma_f32_16x16x32_bf16 v[60:63], a[240:243], a[176:179], v[196:199]\n" \
"s_waitcnt lgkmcnt(2)\n" \
"s_lshl_b32 s93, s70, 12\n" \
"v_exp_f32 v94, v94\n" \
"v_mfma_f32_16x16x32_bf16 v[60:63], a[244:247], a[180:183], v[60:63]\n" \
"s_mul_i32 s91, s61, 6144\n" \
"v_exp_f32 v95, v95\n" \
"v_mfma_f32_16x16x32_bf16 v[60:63], a[248:251], a[184:187], v[60:63]\n" \
"s_add_u32 s91, s91, %3\n" \
"v_cvt_pk_bf16_f32 v86, v92, v93\n" \
"v_mfma_f32_16x16x32_bf16 v[60:63], a[252:255], a[188:191], v[60:63]\n" \
"s_lshl_b32 s71, s80, 7\n" \
"ds_read_b128 a[224:227], v233 offset:4096\n" \
"ds_read_b128 a[228:231], v233 offset:12288\n" \
"v_mfma_f32_16x16x32_bf16 v[44:47], a[252:255], a[172:175], v[220:223]\n" \
"s_lshl_b32 s75, s94, 18\n" \
"v_cvt_pk_bf16_f32 v87, v94, v95\n" \
"v_mfma_f32_16x16x32_bf16 v[44:47], a[248:251], a[168:171], v[44:47]\n" \
"s_lshl_b32 s70, s61, 16\n" \
"v_exp_f32 v104, v104\n" \
"v_mfma_f32_16x16x32_bf16 v[44:47], a[244:247], a[164:167], v[44:47]\n" \
"s_add_u32 s73, s75, s70\n" \
"v_accvgpr_read_b32 v197, a56\n" \
"v_accvgpr_read_b32 v192, a57\n" \
"v_mfma_f32_16x16x32_bf16 v[44:47], a[240:243], a[160:163], v[44:47]\n" \
"s_add_u32 s73, s73, s71\n" \
"v_accvgpr_read_b32 v196, a58\n" \
"v_add_u32 v198, 1024, v197\n" \
"v_mfma_f32_16x16x32_bf16 v[128:131], a[192:195], v[64:67], v[128:131]\n" \
"s_max_u32 s75, s75, 32768\n" \
"v_add_u32 v199, 2048, v197\n" \
"v_add_u32 v193, 31744, v192\n" \
"v_mfma_f32_16x16x32_bf16 v[128:131], a[196:199], v[68:71], v[128:131]\n" \
"s_sub_u32 s75, s75, 32768\n" \
"ds_read_b128 a[0:3], v232 offset:16384\n" \
"ds_read_b128 a[4:7], v232 offset:18432\n" \
"v_mfma_f32_16x16x32_bf16 v[160:163], a[192:195], v[80:83], v[160:163]\n" \
"s_add_u32 s75, s75, s71\n" \
"s_lshl_b32 s89, s61, 10\n" \
"v_mfma_f32_16x16x32_bf16 v[160:163], a[196:199], v[84:87], v[160:163]\n" \
"s_mul_i32 s76, s80, 393216\n" \
"s_add_u32 s89, s89, %3\n" \
"v_exp_f32 v105, v105\n" \
"v_mfma_f32_16x16x32_bf16 v[164:167], a[200:203], v[80:83], v[164:167]\n" \
"s_add_u32 s76, s76, s78\n" \
"ds_read_b128 a[8:11], v232 offset:20480\n" \
"ds_read_b128 a[12:15], v232 offset:22528\n" \
"v_mfma_f32_16x16x32_bf16 v[164:167], a[204:207], v[84:87], v[164:167]\n" \
"s_add_u32 m0, s91, 139264\n" \
"s_add_u32 s89, s89, 16384\n" \
"v_exp_f32 v106, v106\n" \
"v_mfma_f32_16x16x32_bf16 v[132:135], a[200:203], v[64:67], v[132:135]\n" \
"buffer_load_dwordx4 v197, s[48:51], s76 offen offset:0 lds\n" \
"v_mfma_f32_16x16x32_bf16 v[132:135], a[204:207], v[68:71], v[132:135]\n" \
"s_and_b32 s70, s61, 1\n" \
"ds_read_b128 a[16:19], v232 offset:16640\n" \
"ds_read_b128 a[20:23], v232 offset:18688\n" \
"v_smfmac_f32_16x16x64_bf16 a[48:51], v[244:247], v[64:71], v243\n" \
"s_add_u32 s77, s76, 24576\n" \
"v_cvt_pk_bf16_f32 v100, v104, v105\n" \
"v_mfma_f32_16x16x32_bf16 v[136:139], a[208:211], v[64:67], v[136:139]\n" \
"buffer_load_dwordx4 v198, s[48:51], s76 offen offset:1024 lds\n" \
"v_mfma_f32_16x16x32_bf16 v[136:139], a[212:215], v[68:71], v[136:139]\n" \
"s_lshl_b32 s70, s70, 11\n" \
"ds_read_b128 a[24:27], v232 offset:20736\n" \
"ds_read_b128 a[28:31], v232 offset:22784\n" \
"v_mfma_f32_16x16x32_bf16 v[168:171], a[208:211], v[80:83], v[168:171]\n" \
"s_xor_b32 s80, s80, 0x3\n" \
"v_exp_f32 v107, v107\n" \
"v_mfma_f32_16x16x32_bf16 v[168:171], a[212:215], v[84:87], v[168:171]\n" \
"buffer_load_dwordx4 v199, s[48:51], s76 offen offset:2048 lds\n" \
"v_mfma_f32_16x16x32_bf16 v[172:175], a[216:219], v[80:83], v[172:175]\n" \
"s_add_u32 s93, s93, s70\n" \
"ds_read_b128 v[0:3], v232 offset:24576\n" \
"ds_read_b128 v[4:7], v232 offset:26624\n" \
"v_mfma_f32_16x16x32_bf16 v[172:175], a[220:223], v[84:87], v[172:175]\n" \
"s_add_u32 m0, m0, 3072\n" \
"v_exp_f32 v108, v108\n" \
"v_mfma_f32_16x16x32_bf16 v[140:143], a[216:219], v[64:67], v[140:143]\n" \
"buffer_load_dwordx4 v197, s[48:51], s77 offen offset:0 lds\n" \
"v_mfma_f32_16x16x32_bf16 v[140:143], a[220:223], v[68:71], v[140:143]\n" \
"s_add_u32 s93, s93, 52740096\n" \
"ds_read_b128 v[8:11], v232 offset:28672\n" \
"ds_read_b128 v[12:15], v232 offset:30720\n" \
"v_exp_f32 v109, v109\n" \
"v_smfmac_f32_16x16x64_bf16 a[52:55], v[244:247], v[80:87], v243\n" \
"s_waitcnt lgkmcnt(4)\n" \
"s_cmp_eq_u32 s94, 0\n" \
"s_cselect_b32 s92, -1, 0\n" \
"v_cvt_pk_bf16_f32 v101, v106, v107\n" \
"v_mfma_f32_16x16x32_bf16 v[144:147], a[224:227], v[64:67], v[144:147]\n" \
"buffer_load_dwordx4 v198, s[48:51], s77 offen offset:1024 lds\n" \
"v_mfma_f32_16x16x32_bf16 v[144:147], a[228:231], v[68:71], v[144:147]\n" \
"s_mul_i32 s70, s94, 278528\n" \
"ds_read_b128 v[200:203], v232 offset:24832\n" \
"ds_read_b128 v[204:207], v232 offset:26880\n" \
"v_mfma_f32_16x16x32_bf16 v[176:179], a[224:227], v[80:83], v[176:179]\n" \
"s_add_u32 s93, s93, s70\n" \
"v_exp_f32 v110, v110\n" \
"v_mfma_f32_16x16x32_bf16 v[176:179], a[228:231], v[84:87], v[176:179]\n" \
"buffer_load_dwordx4 v199, s[48:51], s77 offen offset:2048 lds\n" \
"v_mfma_f32_16x16x32_bf16 v[180:183], a[232:235], v[80:83], v[180:183]\n" \
"s_lshr_b32 s70, s61, 1\n" \
"ds_read_b128 a[240:243], v232 offset:28928\n" \
"ds_read_b128 a[244:247], v232 offset:30976\n" \
"v_mfma_f32_16x16x32_bf16 v[180:183], a[236:239], v[84:87], v[180:183]\n" \
"s_lshl_b32 s70, s70, 8\n" \
"v_exp_f32 v111, v111\n" \
"v_mfma_f32_16x16x32_bf16 v[148:151], a[232:235], v[64:67], v[148:151]\n" \
"s_add_u32 s93, s93, s70\n" \
"v_exp_f32 v120, v120\n" \
"v_mfma_f32_16x16x32_bf16 v[148:151], a[236:239], v[68:71], v[148:151]\n" \
"v_cvt_pk_bf16_f32 v102, v108, v109\n" \
"v_cvt_pk_bf16_f32 v103, v110, v111\n" \
"v_exp_f32 v121, v121\n" \
"v_mfma_f32_16x16x32_bf16 v[152:155], a[32:35], v[64:67], v[152:155]\n" \
"v_add_u32 v233, 0x10000, v235\n" \
"s_lshl_b32 s72, s61, 14\n" \
"v_mfma_f32_16x16x32_bf16 v[152:155], a[36:39], v[68:71], v[152:155]\n" \
"v_add_u32 v232, s72, v235\n" \
"v_mfma_f32_16x16x32_bf16 v[184:187], a[32:35], v[80:83], v[184:187]\n" \
"v_exp_f32 v122, v122\n" \
"v_mfma_f32_16x16x32_bf16 v[184:187], a[36:39], v[84:87], v[184:187]\n" \
"v_exp_f32 v123, v123\n" \
"v_mfma_f32_16x16x32_bf16 v[188:191], a[40:43], v[80:83], v[188:191]\n" \
"v_exp_f32 v124, v124\n" \
"v_mfma_f32_16x16x32_bf16 v[188:191], a[44:47], v[84:87], v[188:191]\n" \
"v_cvt_pk_bf16_f32 v116, v120, v121\n" \
"v_cvt_pk_bf16_f32 v117, v122, v123\n" \
"v_mfma_f32_16x16x32_bf16 v[156:159], a[40:43], v[64:67], v[156:159]\n" \
"v_exp_f32 v125, v125\n" \
"v_mfma_f32_16x16x32_bf16 v[156:159], a[44:47], v[68:71], v[156:159]\n" \
"s_cmp_eq_u32 s63, 7\n" \
"s_waitcnt vmcnt(7) lgkmcnt(0)\n" \
"s_barrier\n" \
"s_cbranch_scc1 Lep_%=\n" \
"Lepg_%=:\n" \
"v_mfma_f32_16x16x32_bf16 v[16:19], a[0:3], a[176:179], v[208:211]\n" \
"s_add_u32 m0, s79, 110592\n" \
"ds_read_b128 v[64:67], v233 offset:16384\n" \
"ds_read_b128 v[68:71], v233 offset:24576\n" \
"v_mfma_f32_16x16x32_bf16 v[16:19], a[4:7], a[180:183], v[16:19]\n" \
"buffer_load_dwordx4 v192, s[44:47], s73 offen offset:0 lds\n" \
"v_mfma_f32_16x16x32_bf16 v[16:19], a[8:11], a[184:187], v[16:19]\n" \
"ds_read_b128 v[72:75], v233 offset:17408\n" \
"ds_read_b128 v[76:79], v233 offset:25600\n" \
"v_mfma_f32_16x16x32_bf16 v[16:19], a[12:15], a[188:191], v[16:19]\n" \
"ds_write_b128 v232, v[128:131] offset:0\n" \
"v_exp_f32 v126, v126\n" \
"v_mfma_f32_16x16x32_bf16 v[20:23], a[28:31], a[188:191], v[212:215]\n" \
"ds_read_b128 v[80:83], v233 offset:18432\n" \
"ds_read_b128 v[84:87], v233 offset:26624\n" \
"v_mfma_f32_16x16x32_bf16 v[20:23], a[24:27], a[184:187], v[20:23]\n" \
"buffer_load_dwordx4 v193, s[44:47], s73 offen offset:1024 lds\n" \
"v_mfma_f32_16x16x32_bf16 v[20:23], a[20:23], a[180:183], v[20:23]\n" \
"ds_write_b128 v232, v[132:135] offset:1024\n" \
"v_xor_b32 v248, 0x4000, v232\n" \
"v_mfma_f32_16x16x32_bf16 v[20:23], a[16:19], a[176:179], v[20:23]\n" \
"ds_read_b128 v[88:91], v233 offset:19456\n" \
"ds_read_b128 v[92:95], v233 offset:27648\n" \
"v_mfma_f32_16x16x32_bf16 v[24:27], v[0:3], a[176:179], v[216:219]\n" \
"ds_read_b128 a[248:251], v233 offset:7168\n" \
"ds_read_b128 a[252:255], v233 offset:15360\n" \
"v_mfma_f32_16x16x32_bf16 v[24:27], v[4:7], a[180:183], v[24:27]\n" \
"buffer_load_dwordx4 v192, s[44:47], s73 offen offset:2048 lds\n" \
"v_mfma_f32_16x16x32_bf16 v[24:27], v[8:11], a[184:187], v[24:27]\n" \
"ds_write_b128 v232, v[136:139] offset:2048\n" \
"v_mfma_f32_16x16x32_bf16 v[24:27], v[12:15], a[188:191], v[24:27]\n" \
"v_exp_f32 v127, v127\n" \
"v_cvt_pk_bf16_f32 v118, v124, v125\n" \
"v_mfma_f32_16x16x32_bf16 v[28:31], a[244:247], a[188:191], v[220:223]\n" \
"v_cvt_pk_bf16_f32 v119, v126, v127\n" \
"v_lshrrev_b32 v249, 1, v234\n" \
"v_mfma_f32_16x16x32_bf16 v[28:31], a[240:243], a[184:187], v[28:31]\n" \
"buffer_load_dwordx4 v193, s[44:47], s73 offen offset:3072 lds\n" \
"v_mfma_f32_16x16x32_bf16 v[28:31], v[204:207], a[180:183], v[28:31]\n" \
"ds_write_b128 v232, v[140:143] offset:3072\n" \
"s_lshl_b32 s72, s61, 9\n" \
"s_add_u32 s72, s72, 0x20000\n" \
"v_exp_f32 v32, v32\n" \
"v_exp_f32 v33, v33\n" \
"v_mfma_f32_16x16x32_bf16 v[28:31], v[200:203], a[176:179], v[28:31]\n" \
"s_add_u32 m0, s89, 110592\n" \
"v_exp_f32 v34, v34\n" \
"v_exp_f32 v35, v35\n" \
"v_mfma_f32_16x16x32_bf16 a[64:67], a[192:195], v[96:99], a[64:67]\n" \
"s_xor_b32 s73, s73, 0x180\n" \
"v_exp_f32 v36, v36\n" \
"v_mfma_f32_16x16x32_bf16 a[64:67], a[196:199], v[100:103], a[64:67]\n" \
"buffer_load_dwordx4 v196, s[44:47], s75 offen lds\n" \
"s_add_u32 s72, s72, %3\n" \
"v_cvt_pk_bf16_f32 v32, v32, v33\n" \
"v_exp_f32 v37, v37\n" \
"v_add_u32 v249, s72, v249\n" \
"v_mfma_f32_16x16x32_bf16 a[96:99], a[192:195], v[112:115], a[96:99]\n" \
"ds_write_b128 v232, v[144:147] offset:4096\n" \
"v_mfma_f32_16x16x32_bf16 a[96:99], a[196:199], v[116:119], a[96:99]\n" \
"ds_read_b128 a[192:195], v233 offset:0\n" \
"ds_read_b128 a[196:199], v233 offset:8192\n" \
"v_exp_f32 v38, v38\n" \
"v_cvt_pk_bf16_f32 v33, v34, v35\n" \
"v_accvgpr_read_b32 v250, a48\n" \
"v_mfma_f32_16x16x32_bf16 a[100:103], a[200:203], v[112:115], a[100:103]\n" \
"s_xor_b32 s75, s75, 0x180\n" \
"v_exp_f32 v39, v39\n" \
"v_mfma_f32_16x16x32_bf16 a[100:103], a[204:207], v[116:119], a[100:103]\n" \
"v_exp_f32 v48, v48\n" \
"v_cvt_pk_bf16_f32 v34, v36, v37\n" \
"v_exp_f32 v49, v49\n" \
"v_mfma_f32_16x16x32_bf16 a[68:71], a[200:203], v[96:99], a[68:71]\n" \
"ds_write_b128 v232, v[148:151] offset:5120\n" \
"v_mfma_f32_16x16x32_bf16 a[68:71], a[204:207], v[100:103], a[68:71]\n" \
"ds_read_b128 a[200:203], v233 offset:1024\n" \
"ds_read_b128 a[204:207], v233 offset:9216\n" \
"v_exp_f32 v50, v50\n" \
"v_cvt_pk_bf16_f32 v35, v38, v39\n" \
"v_smfmac_f32_16x16x64_bf16 v[224:227], v[244:247], v[96:103], v243\n" \
"v_cvt_pk_bf16_f32 v48, v48, v49\n" \
"v_mfma_f32_16x16x32_bf16 a[72:75], a[208:211], v[96:99], a[72:75]\n" \
"v_exp_f32 v51, v51\n" \
"v_mfma_f32_16x16x32_bf16 a[72:75], a[212:215], v[100:103], a[72:75]\n" \
"ds_write_b128 v232, v[152:155] offset:6144\n" \
"v_exp_f32 v52, v52\n" \
"v_exp_f32 v53, v53\n" \
"v_accvgpr_read_b32 v240, a49\n" \
"v_mfma_f32_16x16x32_bf16 a[104:107], a[208:211], v[112:115], a[104:107]\n" \
"v_exp_f32 v54, v54\n" \
"v_mfma_f32_16x16x32_bf16 a[104:107], a[212:215], v[116:119], a[104:107]\n" \
"ds_read_b128 a[208:211], v233 offset:2048\n" \
"ds_read_b128 a[212:215], v233 offset:10240\n" \
"v_cvt_pk_bf16_f32 v49, v50, v51\n" \
"v_exp_f32 v55, v55\n" \
"v_mfma_f32_16x16x32_bf16 a[108:111], a[216:219], v[112:115], a[108:111]\n" \
"v_exp_f32 v40, v40\n" \
"v_mfma_f32_16x16x32_bf16 a[108:111], a[220:223], v[116:119], a[108:111]\n" \
"ds_write_b128 v232, v[156:159] offset:7168\n" \
"v_cvt_pk_bf16_f32 v50, v52, v53\n" \
"v_exp_f32 v41, v41\n" \
"v_mfma_f32_16x16x32_bf16 a[76:79], a[216:219], v[96:99], a[76:79]\n" \
"v_exp_f32 v42, v42\n" \
"v_mfma_f32_16x16x32_bf16 a[76:79], a[220:223], v[100:103], a[76:79]\n" \
"ds_read_b128 a[216:219], v233 offset:3072\n" \
"ds_read_b128 a[220:223], v233 offset:11264\n" \
"v_cvt_pk_bf16_f32 v51, v54, v55\n" \
"v_exp_f32 v43, v43\n" \
"v_smfmac_f32_16x16x64_bf16 v[228:231], v[244:247], v[112:119], v243\n" \
"ds_write_b128 v232, v[160:163] offset:8192\n" \
"v_exp_f32 v44, v44\n" \
"v_add_f32 v250, v250, v240\n" \
"v_mfma_f32_16x16x32_bf16 a[80:83], a[224:227], v[96:99], a[80:83]\n" \
"v_cvt_pk_bf16_f32 v36, v40, v41\n" \
"v_cvt_pk_bf16_f32 v37, v42, v43\n" \
"v_mfma_f32_16x16x32_bf16 a[80:83], a[228:231], v[100:103], a[80:83]\n" \
"v_exp_f32 v45, v45\n" \
"v_exp_f32 v46, v46\n" \
"v_mfma_f32_16x16x32_bf16 a[112:115], a[224:227], v[112:115], a[112:115]\n" \
"v_exp_f32 v47, v47\n" \
"v_mfma_f32_16x16x32_bf16 a[112:115], a[228:231], v[116:119], a[112:115]\n" \
"ds_write_b128 v232, v[164:167] offset:9216\n" \
"v_exp_f32 v56, v56\n" \
"v_cvt_pk_bf16_f32 v38, v44, v45\n" \
"v_mfma_f32_16x16x32_bf16 a[116:119], a[232:235], v[112:115], a[116:119]\n" \
"ds_read_b128 a[224:227], v233 offset:4096\n" \
"ds_read_b128 a[228:231], v233 offset:12288\n" \
"v_mfma_f32_16x16x32_bf16 a[116:119], a[236:239], v[116:119], a[116:119]\n" \
"v_exp_f32 v57, v57\n" \
"v_exp_f32 v58, v58\n" \
"v_cvt_pk_bf16_f32 v39, v46, v47\n" \
"v_accvgpr_read_b32 v251, a52\n" \
"v_mfma_f32_16x16x32_bf16 a[84:87], a[232:235], v[96:99], a[84:87]\n" \
"v_exp_f32 v59, v59\n" \
"v_mfma_f32_16x16x32_bf16 a[84:87], a[236:239], v[100:103], a[84:87]\n" \
"ds_write_b128 v232, v[168:171] offset:10240\n" \
"v_exp_f32 v60, v60\n" \
"v_cvt_pk_bf16_f32 v52, v56, v57\n" \
"v_mfma_f32_16x16x32_bf16 a[88:91], a[32:35], v[96:99], a[88:91]\n" \
"ds_read_b128 a[232:235], v233 offset:5120\n" \
"ds_read_b128 a[236:239], v233 offset:13312\n" \
"v_mfma_f32_16x16x32_bf16 a[88:91], a[36:39], v[100:103], a[88:91]\n" \
"ds_read_b128 a[240:243], v233 offset:6144\n" \
"ds_read_b128 a[244:247], v233 offset:14336\n" \
"v_exp_f32 v61, v61\n" \
"v_exp_f32 v62, v62\n" \
"v_mfma_f32_16x16x32_bf16 a[120:123], a[32:35], v[112:115], a[120:123]\n" \
"ds_read_b128 v[0:3], v233 offset:20480\n" \
"ds_read_b128 v[4:7], v233 offset:28672\n" \
"v_mfma_f32_16x16x32_bf16 a[120:123], a[36:39], v[116:119], a[120:123]\n" \
"ds_write_b128 v232, v[172:175] offset:11264\n" \
"v_cvt_pk_bf16_f32 v53, v58, v59\n" \
"v_exp_f32 v63, v63\n" \
"v_mfma_f32_16x16x32_bf16 a[124:127], a[40:43], v[112:115], a[124:127]\n" \
"ds_read_b128 v[8:11], v233 offset:21504\n" \
"ds_read_b128 v[12:15], v233 offset:29696\n" \
"v_mfma_f32_16x16x32_bf16 a[124:127], a[44:47], v[116:119], a[124:127]\n" \
"v_cvt_pk_bf16_f32 v54, v60, v61\n" \
"v_accvgpr_read_b32 v240, a53\n" \
"v_add_f32 v251, v251, v240\n" \
"v_mfma_f32_16x16x32_bf16 a[92:95], a[40:43], v[96:99], a[92:95]\n" \
"s_waitcnt lgkmcnt(5)\n" \
"v_cvt_pk_bf16_f32 v55, v62, v63\n" \
"v_mfma_f32_16x16x32_bf16 a[92:95], a[44:47], v[100:103], a[92:95]\n" \
"ds_write_b128 v232, v[176:179] offset:12288\n" \
"ds_write_b64 v249, v[250:251]\n" \
"v_mfma_f32_16x16x32_bf16 a[64:67], a[192:195], v[32:35], a[64:67]\n" \
"ds_read_b128 v[96:99], v233 offset:22528\n" \
"ds_read_b128 v[100:103], v233 offset:30720\n" \
"v_mfma_f32_16x16x32_bf16 a[64:67], a[196:199], v[36:39], a[64:67]\n" \
"ds_read_b128 v[104:107], v233 offset:23552\n" \
"ds_read_b128 v[108:111], v233 offset:31744\n" \
"v_exp_f32 v16, v16\n" \
"v_mfma_f32_16x16x32_bf16 a[96:99], a[192:195], v[48:51], a[96:99]\n" \
"v_exp_f32 v17, v17\n" \
"v_mfma_f32_16x16x32_bf16 a[96:99], a[196:199], v[52:55], a[96:99]\n" \
"ds_write_b128 v232, v[180:183] offset:13312\n" \
"v_exp_f32 v18, v18\n" \
"v_xor_b32 v249, 0x200, v249\n" \
"v_accvgpr_read_b32 v200, a59\n" \
"v_mfma_f32_16x16x32_bf16 a[100:103], a[200:203], v[48:51], a[100:103]\n" \
"v_exp_f32 v19, v19\n" \
"v_mfma_f32_16x16x32_bf16 a[100:103], a[204:207], v[52:55], a[100:103]\n" \
"v_exp_f32 v20, v20\n" \
"v_cvt_pk_bf16_f32 v16, v16, v17\n" \
"v_accvgpr_read_b32 v202, a60\n" \
"v_accvgpr_read_b32 v204, a61\n" \
"v_mfma_f32_16x16x32_bf16 a[68:71], a[200:203], v[32:35], a[68:71]\n" \
"v_exp_f32 v21, v21\n" \
"v_mfma_f32_16x16x32_bf16 a[68:71], a[204:207], v[36:39], a[68:71]\n" \
"ds_write_b128 v232, v[184:187] offset:14336\n" \
"v_exp_f32 v22, v22\n" \
"v_xor_b32 v201, 64, v200\n" \
"v_xor_b32 v203, 64, v202\n" \
"v_xor_b32 v222, 32, v204\n" \
"v_smfmac_f32_16x16x64_bf16 v[224:227], v[244:247], v[32:39], v243\n" \
"v_cvt_pk_bf16_f32 v17, v18, v19\n" \
"v_add_u32 v205, 0x10000, v200\n" \
"v_mfma_f32_16x16x32_bf16 a[72:75], a[208:211], v[32:35], a[72:75]\n" \
"v_exp_f32 v23, v23\n" \
"v_mfma_f32_16x16x32_bf16 a[72:75], a[212:215], v[36:39], a[72:75]\n" \
"v_exp_f32 v24, v24\n" \
"v_cvt_pk_bf16_f32 v18, v20, v21\n" \
"v_add_u32 v207, 0x10000, v202\n" \
"v_add_u32 v194, 139264, v204\n" \
"v_add_u32 v206, 0x10000, v201\n" \
"v_mfma_f32_16x16x32_bf16 a[104:107], a[208:211], v[48:51], a[104:107]\n" \
"ds_write_b128 v232, v[188:191] offset:15360\n" \
"v_exp_f32 v25, v25\n" \
"v_add_u32 v208, 0x10000, v203\n" \
"v_mfma_f32_16x16x32_bf16 a[104:107], a[212:215], v[52:55], a[104:107]\n" \
"v_exp_f32 v26, v26\n" \
"v_cvt_pk_bf16_f32 v19, v22, v23\n" \
"v_add_u32 v195, 139264, v222\n" \
"v_mfma_f32_16x16x32_bf16 a[108:111], a[216:219], v[48:51], a[108:111]\n" \
"s_mul_i32 s76, s80, 393216\n" \
"s_add_u32 s76, s76, s78\n" \
"v_cvt_pk_bf16_f32 v20, v24, v25\n" \
"v_mfma_f32_16x16x32_bf16 a[108:111], a[220:223], v[52:55], a[108:111]\n" \
"s_waitcnt vmcnt(0) lgkmcnt(0)\n" \
"s_barrier\n" \
"v_mfma_f32_16x16x32_bf16 a[76:79], a[216:219], v[32:35], a[76:79]\n" \
"s_add_u32 m0, s91, 86016\n" \
"ds_read_b64_tr_b16 a[32:33], v194 offset:1024\n" \
"ds_read_b64_tr_b16 a[34:35], v194 offset:4096\n" \
"v_mfma_f32_16x16x32_bf16 a[76:79], a[220:223], v[36:39], a[76:79]\n" \
"buffer_load_dwordx4 v197, s[48:51], s76 offen offset:0 lds\n" \
"v_exp_f32 v27, v27\n" \
"v_smfmac_f32_16x16x64_bf16 v[228:231], v[244:247], v[48:55], v243\n" \
"s_and_b32 s15, %1, 7\n" \
"ds_read_b64_tr_b16 a[36:37], v194 offset:13312\n" \
"ds_read_b64_tr_b16 a[38:39], v194 offset:16384\n" \
"v_exp_f32 v28, v28\n" \
"v_mfma_f32_16x16x32_bf16 a[80:83], a[224:227], v[32:35], a[80:83]\n" \
"s_add_u32 s77, s76, 24576\n" \
"ds_read_b64_tr_b16 a[40:41], v194 offset:0\n" \
"ds_read_b64_tr_b16 a[42:43], v194 offset:3072\n" \
"v_mfma_f32_16x16x32_bf16 a[80:83], a[228:231], v[36:39], a[80:83]\n" \
"buffer_load_dwordx4 v198, s[48:51], s76 offen offset:1024 lds\n" \
"v_mfma_f32_16x16x32_bf16 a[112:115], a[224:227], v[48:51], a[112:115]\n" \
"s_lshl3_add_u32 s14, s14, s15\n" \
"ds_read_b64_tr_b16 a[44:45], v194 offset:12288\n" \
"ds_read_b64_tr_b16 a[46:47], v194 offset:15360\n" \
"v_mfma_f32_16x16x32_bf16 a[112:115], a[228:231], v[52:55], a[112:115]\n" \
"s_xor_b32 s80, s80, 0x8\n" \
"ds_read_b64_tr_b16 a[48:49], v195 offset:0\n" \
"ds_read_b64_tr_b16 a[50:51], v195 offset:3072\n" \
"v_exp_f32 v29, v29\n" \
"v_mfma_f32_16x16x32_bf16 a[116:119], a[232:235], v[48:51], a[116:119]\n" \
"buffer_load_dwordx4 v199, s[48:51], s76 offen offset:2048 lds\n" \
"v_mfma_f32_16x16x32_bf16 a[116:119], a[236:239], v[52:55], a[116:119]\n" \
"ds_read_b64_tr_b16 a[52:53], v195 offset:12288\n" \
"ds_read_b64_tr_b16 a[54:55], v195 offset:15360\n" \
"v_exp_f32 v30, v30\n" \
"v_mfma_f32_16x16x32_bf16 a[84:87], a[232:235], v[32:35], a[84:87]\n" \
"s_add_u32 m0, m0, 3072\n" \
"ds_read_b128 a[0:3], v207 offset:45056\n" \
"ds_read_b128 a[4:7], v208 offset:45056\n" \
"v_mfma_f32_16x16x32_bf16 a[84:87], a[236:239], v[36:39], a[84:87]\n" \
"buffer_load_dwordx4 v197, s[48:51], s77 offen offset:0 lds\n" \
"v_cvt_pk_bf16_f32 v21, v26, v27\n" \
"v_mfma_f32_16x16x32_bf16 a[88:91], a[240:243], v[32:35], a[88:91]\n" \
"ds_read_b128 a[8:11], v205 offset:45056\n" \
"ds_read_b128 a[12:15], v206 offset:45056\n" \
"v_mfma_f32_16x16x32_bf16 a[88:91], a[244:247], v[36:39], a[88:91]\n" \
"ds_read_b128 a[16:19], v205 offset:49152\n" \
"ds_read_b128 a[20:23], v206 offset:49152\n" \
"v_exp_f32 v31, v31\n" \
"v_mfma_f32_16x16x32_bf16 a[120:123], a[240:243], v[48:51], a[120:123]\n" \
"buffer_load_dwordx4 v198, s[48:51], s77 offen offset:1024 lds\n" \
"v_mfma_f32_16x16x32_bf16 a[120:123], a[244:247], v[52:55], a[120:123]\n" \
"ds_read_b128 a[24:27], v205 offset:53248\n" \
"ds_read_b128 a[28:31], v206 offset:53248\n" \
"v_cvt_pk_bf16_f32 v22, v28, v29\n" \
"v_mfma_f32_16x16x32_bf16 a[124:127], a[248:251], v[48:51], a[124:127]\n" \
"ds_read_b64 v[250:251], v249\n" \
"ds_read_b128 v[128:131], v248 offset:0\n" \
"v_mfma_f32_16x16x32_bf16 a[124:127], a[252:255], v[52:55], a[124:127]\n" \
"buffer_load_dwordx4 v199, s[48:51], s77 offen offset:2048 lds\n" \
"v_cvt_pk_bf16_f32 v23, v30, v31\n" \
"v_mfma_f32_16x16x32_bf16 a[92:95], a[248:251], v[32:35], a[92:95]\n" \
"ds_read_b128 v[132:135], v248 offset:1024\n" \
"ds_read_b128 v[136:139], v248 offset:2048\n" \
"v_mfma_f32_16x16x32_bf16 a[92:95], a[252:255], v[36:39], a[92:95]\n" \
"s_add_u32 m0, s79, 65536\n" \
"ds_read_b128 v[140:143], v248 offset:3072\n" \
"ds_read_b128 v[144:147], v248 offset:4096\n" \
"v_mfma_f32_16x16x32_bf16 a[96:99], v[64:67], v[16:19], a[96:99]\n" \
"buffer_load_dwordx4 v192, s[44:47], s73 offen offset:0 lds\n" \
"v_mfma_f32_16x16x32_bf16 a[96:99], v[68:71], v[20:23], a[96:99]\n" \
"ds_read_b128 v[148:151], v248 offset:5120\n" \
"ds_read_b128 v[152:155], v248 offset:6144\n" \
"v_mfma_f32_16x16x32_bf16 a[100:103], v[72:75], v[16:19], a[100:103]\n" \
"ds_read_b128 v[156:159], v248 offset:7168\n" \
"ds_read_b128 v[160:163], v248 offset:8192\n" \
"v_mfma_f32_16x16x32_bf16 a[100:103], v[76:79], v[20:23], a[100:103]\n" \
"buffer_load_dwordx4 v193, s[44:47], s73 offen offset:1024 lds\n" \
"v_smfmac_f32_16x16x64_bf16 v[228:231], v[244:247], v[16:23], v243\n" \
"ds_read_b128 v[164:167], v248 offset:9216\n" \
"ds_read_b128 v[168:171], v248 offset:10240\n" \
"v_mfma_f32_16x16x32_bf16 a[104:107], v[80:83], v[16:19], a[104:107]\n" \
"ds_read_b128 v[172:175], v248 offset:11264\n" \
"ds_read_b128 v[176:179], v248 offset:12288\n" \
"v_mfma_f32_16x16x32_bf16 a[104:107], v[84:87], v[20:23], a[104:107]\n" \
"buffer_load_dwordx4 v192, s[44:47], s73 offen offset:2048 lds\n" \
"v_mfma_f32_16x16x32_bf16 a[108:111], v[88:91], v[16:19], a[108:111]\n" \
"ds_read_b128 v[180:183], v248 offset:13312\n" \
"ds_read_b128 v[184:187], v248 offset:14336\n" \
"v_mfma_f32_16x16x32_bf16 a[108:111], v[92:95], v[20:23], a[108:111]\n" \
"ds_read_b128 v[188:191], v248 offset:15360\n" \
"v_mov_b32 v218, s60\n" \
"v_mfma_f32_16x16x32_bf16 a[112:115], v[0:3], v[16:19], a[112:115]\n" \
"buffer_load_dwordx4 v193, s[44:47], s73 offen offset:3072 lds\n" \
"v_mfma_f32_16x16x32_bf16 a[112:115], v[4:7], v[20:23], a[112:115]\n" \
"s_add_u32 m0, s89, 65536\n" \
"v_mov_b32 v219, s14\n" \
"v_lshlrev_b32 v220, 28, s61\n" \
"v_mfma_f32_16x16x32_bf16 a[116:119], v[8:11], v[16:19], a[116:119]\n" \
"s_xor_b32 s73, s73, 0x400\n" \
"v_mfma_f32_16x16x32_bf16 a[116:119], v[12:15], v[20:23], a[116:119]\n" \
"buffer_load_dwordx4 v196, s[44:47], s75 offen lds\n" \
"v_mfma_f32_16x16x32_bf16 a[120:123], v[96:99], v[16:19], a[120:123]\n" \
"s_lshl3_add_u32 s15, s15, 65593856\n" \
"v_mfma_f32_16x16x32_bf16 a[120:123], v[100:103], v[20:23], a[120:123]\n" \
"s_lshr_b32 s14, %1, 3\n" \
"s_cmp_eq_u32 s14, 3\n" \
"s_cselect_b32 s15, s15, 0x20000000\n" \
"buffer_store_dwordx2 v[218:219], v220, s[56:59], s15 offen sc1\n" \
"v_mfma_f32_16x16x32_bf16 a[124:127], v[104:107], v[16:19], a[124:127]\n" \
"s_xor_b32 s75, s75, 0x400\n" \
"v_mfma_f32_16x16x32_bf16 a[124:127], v[108:111], v[20:23], a[124:127]\n" \
"s_waitcnt lgkmcnt(0)\n" \
"s_barrier\n" \
"v_mfma_f32_16x16x32_bf16 v[32:35], a[32:35], a[0:3], 0\n" \
"s_add_u32 m0, s79, 0\n" \
"ds_read_b128 a[192:195], v205 offset:57344\n" \
"v_mfma_f32_16x16x32_bf16 v[32:35], a[36:39], a[4:7], v[32:35]\n" \
"buffer_load_dwordx4 v192, s[44:47], s73 offen offset:0 lds\n" \
"v_mfma_f32_16x16x32_bf16 v[40:43], a[32:35], a[8:11], 0\n" \
"ds_read_b128 a[196:199], v206 offset:57344\n" \
"v_add_u32 v209, 86016, v204\n" \
"v_mfma_f32_16x16x32_bf16 v[40:43], a[36:39], a[12:15], v[40:43]\n" \
"buffer_load_dwordx4 v193, s[44:47], s73 offen offset:1024 lds\n" \
"v_mfma_f32_16x16x32_bf16 v[0:3], a[40:43], a[8:11], 0\n" \
"ds_read_b64_tr_b16 a[200:201], v195 offset:1024\n" \
"ds_read_b64_tr_b16 a[202:203], v195 offset:4096\n" \
"v_mfma_f32_16x16x32_bf16 v[0:3], a[44:47], a[12:15], v[0:3]\n" \
"buffer_load_dwordx4 v192, s[44:47], s73 offen offset:2048 lds\n" \
"v_mfma_f32_16x16x32_bf16 v[4:7], a[48:51], a[8:11], 0\n" \
"ds_read_b64_tr_b16 a[204:205], v195 offset:13312\n" \
"ds_read_b64_tr_b16 a[206:207], v195 offset:16384\n" \
"v_mfma_f32_16x16x32_bf16 v[4:7], a[52:55], a[12:15], v[4:7]\n" \
"buffer_load_dwordx4 v193, s[44:47], s73 offen offset:3072 lds\n" \
"v_mfma_f32_16x16x32_bf16 v[12:15], a[48:51], a[16:19], 0\n" \
"s_add_u32 m0, s89, 0\n" \
"s_xor_b32 s73, s73, 0x500\n" \
"ds_read_b64_tr_b16 a[208:209], v194 offset:2048\n" \
"ds_read_b64_tr_b16 a[210:211], v194 offset:5120\n" \
"v_add_f32 v248, v224, v225\n" \
"v_mfma_f32_16x16x32_bf16 v[12:15], a[52:55], a[20:23], v[12:15]\n" \
"buffer_load_dwordx4 v196, s[44:47], s75 offen lds\n" \
"v_mfma_f32_16x16x32_bf16 v[8:11], a[40:43], a[16:19], 0\n" \
"s_xor_b32 s75, s75, 0x500\n" \
"s_mul_i32 s76, s80, 393216\n" \
"s_add_u32 s76, s76, s78\n" \
"Lpg7380_%=:\n" \
"s_cbranch_execz Lcb0_%=\n" \
"s_add_u32 m0, s91, 20480\n" \
"ds_read_b64_tr_b16 a[212:213], v194 offset:14336\n" \
"ds_read_b64_tr_b16 a[214:215], v194 offset:17408\n" \
"v_mfma_f32_16x16x32_bf16 v[8:11], a[44:47], a[20:23], v[8:11]\n" \
"buffer_load_dwordx4 v197, s[48:51], s76 offen offset:0 lds\n" \
"v_mfma_f32_16x16x32_bf16 v[48:51], a[32:35], a[16:19], 0\n" \
"s_add_u32 s77, s76, 24576\n" \
"ds_read_b64_tr_b16 a[216:217], v195 offset:2048\n" \
"v_add_u32 v210, 86016, v222\n" \
"v_mfma_f32_16x16x32_bf16 v[48:51], a[36:39], a[20:23], v[48:51]\n" \
"buffer_load_dwordx4 v198, s[48:51], s76 offen offset:1024 lds\n" \
"v_mfma_f32_16x16x32_bf16 v[56:59], a[32:35], a[24:27], 0\n" \
"s_xor_b32 s80, s80, 0xa\n" \
"ds_read_b64_tr_b16 a[218:219], v195 offset:5120\n" \
"v_add_f32 v249, v228, v229\n" \
"v_mfma_f32_16x16x32_bf16 v[56:59], a[36:39], a[28:31], v[56:59]\n" \
"buffer_load_dwordx4 v199, s[48:51], s76 offen offset:2048 lds\n" \
"v_add_f32 v248, v248, v250\n" \
"v_mfma_f32_16x16x32_bf16 v[16:19], a[40:43], a[24:27], 0\n" \
"s_add_u32 m0, m0, 3072\n" \
"ds_read_b64_tr_b16 a[220:221], v195 offset:14336\n" \
"v_add_f32 v249, v249, v251\n" \
"v_mfma_f32_16x16x32_bf16 v[16:19], a[44:47], a[28:31], v[16:19]\n" \
"buffer_load_dwordx4 v197, s[48:51], s77 offen offset:0 lds\n" \
"v_mfma_f32_16x16x32_bf16 v[20:23], a[48:51], a[24:27], 0\n" \
"s_waitcnt lgkmcnt(2)\n" \
"ds_read_b64_tr_b16 a[222:223], v195 offset:17408\n" \
"v_mfma_f32_16x16x32_bf16 v[20:23], a[52:55], a[28:31], v[20:23]\n" \
"buffer_load_dwordx4 v198, s[48:51], s77 offen offset:1024 lds\n" \
"v_mfma_f32_16x16x32_bf16 v[28:31], a[48:51], a[192:195], 0\n" \
"v_rcp_f32 v232, v248\n" \
"v_mfma_f32_16x16x32_bf16 v[28:31], a[52:55], a[196:199], v[28:31]\n" \
"buffer_load_dwordx4 v199, s[48:51], s77 offen offset:2048 lds\n" \
"v_mfma_f32_16x16x32_bf16 v[24:27], a[40:43], a[192:195], 0\n" \
"v_rcp_f32 v233, v249\n" \
"v_mfma_f32_16x16x32_bf16 v[24:27], a[44:47], a[196:199], v[24:27]\n" \
"v_add_u32 v240, 0x1000, v236\n" \
"v_add_f32 v224, v248, v249\n" \
"v_mul_f32 v224, 0x43800000, v224\n" \
"v_mfma_f32_16x16x32_bf16 v[64:67], a[32:35], a[192:195], 0\n" \
"v_add_f32 v248, v232, v233\n" \
"v_fmac_f32 v224, 0x5f800000, v248\n" \
"v_mfma_f32_16x16x32_bf16 v[64:67], a[36:39], a[196:199], v[64:67]\n" \
"s_waitcnt vmcnt(12) lgkmcnt(0)\n" \
"s_barrier\n" \
"v_mfma_f32_16x16x32_bf16 v[68:71], a[200:203], a[192:195], 0\n" \
"ds_read_b64_tr_b16 a[32:33], v209 offset:1024\n" \
"ds_read_b64_tr_b16 a[34:35], v209 offset:4096\n" \
"v_mfma_f32_16x16x32_bf16 v[68:71], a[204:207], a[196:199], v[68:71]\n" \
"ds_read_b64_tr_b16 a[36:37], v209 offset:13312\n" \
"ds_read_b64_tr_b16 a[38:39], v209 offset:16384\n" \
"v_mfma_f32_16x16x32_bf16 v[60:63], a[200:203], a[24:27], 0\n" \
"s_add_u32 m0, s79, 110592\n" \
"ds_read_b64_tr_b16 a[40:41], v209 offset:0\n" \
"v_mov_b32 v225, 0\n" \
"v_mfma_f32_16x16x32_bf16 v[60:63], a[204:207], a[28:31], v[60:63]\n" \
"buffer_load_dwordx4 v192, s[44:47], s73 offen offset:0 lds\n" \
"v_mov_b32 v226, 0\n" \
"v_mfma_f32_16x16x32_bf16 v[52:55], a[200:203], a[16:19], 0\n" \
"ds_read_b64_tr_b16 a[42:43], v209 offset:3072\n" \
"v_mov_b32 v227, 0\n" \
"v_mfma_f32_16x16x32_bf16 v[52:55], a[204:207], a[20:23], v[52:55]\n" \
"ds_read_b64_tr_b16 a[44:45], v209 offset:12288\n" \
"v_accvgpr_read_b32 v112, a64\n" \
"v_mfma_f32_16x16x32_bf16 v[44:47], a[200:203], a[8:11], 0\n" \
"ds_read_b64_tr_b16 a[46:47], v209 offset:15360\n" \
"v_accvgpr_read_b32 v113, a65\n" \
"v_mfma_f32_16x16x32_bf16 v[44:47], a[204:207], a[12:15], v[44:47]\n" \
"buffer_load_dwordx4 v193, s[44:47], s73 offen offset:1024 lds\n" \
"v_mfma_f32_16x16x32_bf16 v[72:75], a[0:3], a[208:211], 0\n" \
"ds_read_b64_tr_b16 a[48:49], v210 offset:0\n" \
"v_accvgpr_read_b32 v114, a66\n" \
"v_mfma_f32_16x16x32_bf16 v[72:75], a[4:7], a[212:215], v[72:75]\n" \
"ds_read_b128 a[0:3], v207 offset:0\n" \
"v_accvgpr_read_b32 v115, a67\n" \
"v_accvgpr_read_b32 v116, a68\n" \
"v_mfma_f32_16x16x32_bf16 v[80:83], a[8:11], a[208:211], 0\n" \
"ds_read_b128 a[4:7], v208 offset:0\n" \
"v_accvgpr_read_b32 v117, a69\n" \
"v_mfma_f32_16x16x32_bf16 v[80:83], a[12:15], a[212:215], v[80:83]\n" \
"buffer_load_dwordx4 v192, s[44:47], s73 offen offset:2048 lds\n" \
"v_mfma_f32_16x16x32_bf16 v[84:87], a[8:11], a[216:219], 0\n" \
"ds_read_b64_tr_b16 a[50:51], v210 offset:3072\n" \
"v_accvgpr_read_b32 v118, a70\n" \
"v_mfma_f32_16x16x32_bf16 v[84:87], a[12:15], a[220:223], v[84:87]\n" \
"ds_read_b128 a[8:11], v205 offset:0\n" \
"v_accvgpr_read_b32 v119, a71\n" \
"v_mfma_f32_16x16x32_bf16 v[92:95], a[16:19], a[216:219], 0\n" \
"ds_read_b128 a[12:15], v206 offset:0\n" \
"v_add_f32 v112, v112, v128\n" \
"v_mfma_f32_16x16x32_bf16 v[92:95], a[20:23], a[220:223], v[92:95]\n" \
"buffer_load_dwordx4 v193, s[44:47], s73 offen offset:3072 lds\n" \
"v_add_f32 v113, v113, v129\n" \
"v_mfma_f32_16x16x32_bf16 v[88:91], a[16:19], a[208:211], 0\n" \
"ds_read_b64_tr_b16 a[52:53], v210 offset:12288\n" \
"v_add_f32 v114, v114, v130\n" \
"v_mfma_f32_16x16x32_bf16 v[88:91], a[20:23], a[212:215], v[88:91]\n" \
"s_add_u32 m0, s89, 110592\n" \
"ds_read_b128 a[16:19], v205 offset:4096\n" \
"v_add_f32 v115, v115, v131\n" \
"v_mfma_f32_16x16x32_bf16 v[96:99], a[24:27], a[208:211], 0\n" \
"s_xor_b32 s73, s73, 0x180\n" \
"ds_read_b128 a[20:23], v206 offset:4096\n" \
"v_add_f32 v116, v116, v132\n" \
"v_mfma_f32_16x16x32_bf16 v[96:99], a[28:31], a[212:215], v[96:99]\n" \
"buffer_load_dwordx4 v196, s[44:47], s75 offen lds\n" \
"v_mfma_f32_16x16x32_bf16 v[100:103], a[24:27], a[216:219], 0\n" \
"s_xor_b32 s75, s75, 0x180\n" \
"s_mul_i32 s76, s80, 393216\n" \
"ds_read_b64_tr_b16 a[54:55], v210 offset:15360\n" \
"v_mfma_f32_16x16x32_bf16 v[100:103], a[28:31], a[220:223], v[100:103]\n" \
"s_add_u32 s76, s76, s78\n" \
"ds_read_b128 a[24:27], v205 offset:8192\n" \
"v_add_f32 v117, v117, v133\n" \
"v_mfma_f32_16x16x32_bf16 v[108:111], a[192:195], a[216:219], 0\n" \
"s_add_u32 m0, s91, 139264\n" \
"ds_read_b128 a[28:31], v206 offset:8192\n" \
"v_add_f32 v118, v118, v134\n" \
"v_mfma_f32_16x16x32_bf16 v[108:111], a[196:199], a[220:223], v[108:111]\n" \
"buffer_load_dwordx4 v197, s[48:51], s76 offen offset:0 lds\n" \
"v_add_f32 v119, v119, v135\n" \
"v_mfma_f32_16x16x32_bf16 v[104:107], a[192:195], a[208:211], 0\n" \
"v_mul_f32 v112, v112, v232\n" \
"v_mul_f32 v113, v113, v232\n" \
"v_mfma_f32_16x16x32_bf16 v[104:107], a[196:199], a[212:215], v[104:107]\n" \
"s_waitcnt lgkmcnt(2)\n" \
"v_mul_f32 v114, v114, v232\n" \
"v_mul_f32 v115, v115, v232\n" \
"v_mfma_f32_16x16x32_bf16 v[32:35], a[32:35], a[0:3], v[32:35]\n" \
"s_add_u32 s77, s76, 24576\n" \
"ds_read_b128 a[192:195], v205 offset:12288\n" \
"v_mul_f32 v116, v116, v232\n" \
"v_mfma_f32_16x16x32_bf16 v[32:35], a[36:39], a[4:7], v[32:35]\n" \
"buffer_load_dwordx4 v198, s[48:51], s76 offen offset:1024 lds\n" \
"v_mfma_f32_16x16x32_bf16 v[40:43], a[32:35], a[8:11], v[40:43]\n" \
"ds_read_b128 a[196:199], v206 offset:12288\n" \
"v_mul_f32 v117, v117, v232\n" \
"v_mfma_f32_16x16x32_bf16 v[40:43], a[36:39], a[12:15], v[40:43]\n" \
"ds_read_b64_tr_b16 a[200:201], v210 offset:1024\n" \
"v_mul_f32 v118, v118, v232\n" \
"v_mul_f32 v119, v119, v232\n" \
"v_mfma_f32_16x16x32_bf16 v[0:3], a[40:43], a[8:11], v[0:3]\n" \
"s_xor_b32 s80, s80, 0x3\n" \
"ds_read_b64_tr_b16 a[202:203], v210 offset:4096\n" \
"v_cvt_pk_bf16_f32 v120, v112, v113\n" \
"v_mfma_f32_16x16x32_bf16 v[0:3], a[44:47], a[12:15], v[0:3]\n" \
"buffer_load_dwordx4 v199, s[48:51], s76 offen offset:2048 lds\n" \
"v_mfma_f32_16x16x32_bf16 v[4:7], a[48:51], a[8:11], v[4:7]\n" \
"ds_read_b64_tr_b16 a[204:205], v210 offset:13312\n" \
"v_cvt_pk_bf16_f32 v121, v114, v115\n" \
"v_mfma_f32_16x16x32_bf16 v[4:7], a[52:55], a[12:15], v[4:7]\n" \
"ds_read_b64_tr_b16 a[206:207], v210 offset:16384\n" \
"v_cvt_pk_bf16_f32 v122, v116, v117\n" \
"v_mfma_f32_16x16x32_bf16 v[12:15], a[48:51], a[16:19], v[12:15]\n" \
"s_add_u32 m0, m0, 3072\n" \
"ds_read_b64_tr_b16 a[208:209], v209 offset:2048\n" \
"v_cvt_pk_bf16_f32 v123, v118, v119\n" \
"v_mfma_f32_16x16x32_bf16 v[12:15], a[52:55], a[20:23], v[12:15]\n" \
"buffer_load_dwordx4 v197, s[48:51], s77 offen offset:0 lds\n" \
"v_mfma_f32_16x16x32_bf16 v[8:11], a[40:43], a[16:19], v[8:11]\n" \
"ds_read_b64_tr_b16 a[210:211], v209 offset:5120\n" \
"v_mfma_f32_16x16x32_bf16 v[8:11], a[44:47], a[20:23], v[8:11]\n" \
"ds_read_b64_tr_b16 a[212:213], v209 offset:14336\n" \
"buffer_store_dwordx4 v[120:123], v236, s[56:59], s88 offen offset:0 sc1\n" \
"v_mfma_f32_16x16x32_bf16 v[48:51], a[32:35], a[16:19], v[48:51]\n" \
"s_waitcnt lgkmcnt(3)\n" \
"ds_read_b64_tr_b16 a[214:215], v209 offset:17408\n" \
"v_mfma_f32_16x16x32_bf16 v[48:51], a[36:39], a[20:23], v[48:51]\n" \
"buffer_load_dwordx4 v198, s[48:51], s77 offen offset:1024 lds\n" \
"v_mfma_f32_16x16x32_bf16 v[224:227], v[244:247], v[120:123], v[224:227]\n" \
"v_accvgpr_read_b32 v112, a72\n" \
"v_accvgpr_read_b32 v113, a73\n" \
"v_accvgpr_read_b32 v114, a74\n" \
"v_mfma_f32_16x16x32_bf16 v[56:59], a[32:35], a[24:27], v[56:59]\n" \
"ds_read_b64_tr_b16 a[216:217], v210 offset:2048\n" \
"v_accvgpr_read_b32 v115, a75\n" \
"v_mfma_f32_16x16x32_bf16 v[56:59], a[36:39], a[28:31], v[56:59]\n" \
"ds_read_b64_tr_b16 a[218:219], v210 offset:5120\n" \
"v_accvgpr_read_b32 v116, a76\n" \
"v_mfma_f32_16x16x32_bf16 v[16:19], a[40:43], a[24:27], v[16:19]\n" \
"ds_read_b64_tr_b16 a[220:221], v210 offset:14336\n" \
"v_accvgpr_read_b32 v117, a77\n" \
"v_mfma_f32_16x16x32_bf16 v[16:19], a[44:47], a[28:31], v[16:19]\n" \
"buffer_load_dwordx4 v199, s[48:51], s77 offen offset:2048 lds\n" \
"v_accvgpr_read_b32 v118, a78\n" \
"v_mfma_f32_16x16x32_bf16 v[20:23], a[48:51], a[24:27], v[20:23]\n" \
"ds_read_b64_tr_b16 a[222:223], v210 offset:17408\n" \
"v_accvgpr_read_b32 v119, a79\n" \
"v_mfma_f32_16x16x32_bf16 v[20:23], a[52:55], a[28:31], v[20:23]\n" \
"v_add_f32 v112, v112, v136\n" \
"v_add_f32 v113, v113, v137\n" \
"v_mfma_f32_16x16x32_bf16 v[28:31], a[48:51], a[192:195], v[28:31]\n" \
"v_add_f32 v114, v114, v138\n" \
"v_add_f32 v115, v115, v139\n" \
"v_mfma_f32_16x16x32_bf16 v[28:31], a[52:55], a[196:199], v[28:31]\n" \
"v_add_f32 v116, v116, v140\n" \
"v_add_f32 v117, v117, v141\n" \
"v_mfma_f32_16x16x32_bf16 v[24:27], a[40:43], a[192:195], v[24:27]\n" \
"v_add_f32 v118, v118, v142\n" \
"v_add_f32 v119, v119, v143\n" \
"v_mfma_f32_16x16x32_bf16 v[24:27], a[44:47], a[196:199], v[24:27]\n" \
"v_mul_f32 v112, v112, v232\n" \
"v_mul_f32 v113, v113, v232\n" \
"v_mul_f32 v114, v114, v232\n" \
"v_mfma_f32_16x16x32_bf16 v[64:67], a[32:35], a[192:195], v[64:67]\n" \
"v_mul_f32 v115, v115, v232\n" \
"v_mul_f32 v116, v116, v232\n" \
"v_mfma_f32_16x16x32_bf16 v[64:67], a[36:39], a[196:199], v[64:67]\n" \
"s_waitcnt vmcnt(12) lgkmcnt(0)\n" \
"s_barrier\n" \
"v_mfma_f32_16x16x32_bf16 v[68:71], a[200:203], a[192:195], v[68:71]\n" \
"ds_read_b64_tr_b16 a[32:33], v204 offset:21504\n" \
"ds_read_b64_tr_b16 a[34:35], v204 offset:24576\n" \
"v_mfma_f32_16x16x32_bf16 v[68:71], a[204:207], a[196:199], v[68:71]\n" \
"ds_read_b64_tr_b16 a[36:37], v204 offset:33792\n" \
"ds_read_b64_tr_b16 a[38:39], v204 offset:36864\n" \
"v_mfma_f32_16x16x32_bf16 v[60:63], a[200:203], a[24:27], v[60:63]\n" \
"s_add_u32 m0, s79, 65536\n" \
"ds_read_b64_tr_b16 a[40:41], v204 offset:20480\n" \
"v_mul_f32 v117, v117, v232\n" \
"v_mfma_f32_16x16x32_bf16 v[60:63], a[204:207], a[28:31], v[60:63]\n" \
"buffer_load_dwordx4 v192, s[44:47], s73 offen offset:0 lds\n" \
"v_mul_f32 v118, v118, v232\n" \
"v_mfma_f32_16x16x32_bf16 v[52:55], a[200:203], a[16:19], v[52:55]\n" \
"ds_read_b64_tr_b16 a[42:43], v204 offset:23552\n" \
"v_mul_f32 v119, v119, v232\n" \
"v_mfma_f32_16x16x32_bf16 v[52:55], a[204:207], a[20:23], v[52:55]\n" \
"ds_read_b64_tr_b16 a[44:45], v204 offset:32768\n" \
"v_cvt_pk_bf16_f32 v124, v112, v113\n" \
"v_mfma_f32_16x16x32_bf16 v[44:47], a[200:203], a[8:11], v[44:47]\n" \
"ds_read_b64_tr_b16 a[46:47], v204 offset:35840\n" \
"v_cvt_pk_bf16_f32 v125, v114, v115\n" \
"v_mfma_f32_16x16x32_bf16 v[44:47], a[204:207], a[12:15], v[44:47]\n" \
"buffer_load_dwordx4 v193, s[44:47], s73 offen offset:1024 lds\n" \
"v_mfma_f32_16x16x32_bf16 v[72:75], a[0:3], a[208:211], v[72:75]\n" \
"ds_read_b64_tr_b16 a[48:49], v222 offset:20480\n" \
"v_cvt_pk_bf16_f32 v126, v116, v117\n" \
"v_mfma_f32_16x16x32_bf16 v[72:75], a[4:7], a[212:215], v[72:75]\n" \
"ds_read_b128 a[0:3], v202 offset:0\n" \
"v_cvt_pk_bf16_f32 v127, v118, v119\n" \
"v_mfma_f32_16x16x32_bf16 v[80:83], a[8:11], a[208:211], v[80:83]\n" \
"ds_read_b128 a[4:7], v203 offset:0\n" \
"v_mfma_f32_16x16x32_bf16 v[80:83], a[12:15], a[212:215], v[80:83]\n" \
"buffer_load_dwordx4 v192, s[44:47], s73 offen offset:2048 lds\n" \
"v_mfma_f32_16x16x32_bf16 v[84:87], a[8:11], a[216:219], v[84:87]\n" \
"ds_read_b64_tr_b16 a[50:51], v222 offset:23552\n" \
"v_mfma_f32_16x16x32_bf16 v[84:87], a[12:15], a[220:223], v[84:87]\n" \
"ds_read_b128 a[8:11], v200 offset:0\n" \
"buffer_store_dwordx4 v[124:127], v236, s[56:59], s88 offen offset:2048 sc1\n" \
"v_mfma_f32_16x16x32_bf16 v[92:95], a[16:19], a[216:219], v[92:95]\n" \
"ds_read_b128 a[12:15], v201 offset:0\n" \
"v_mfma_f32_16x16x32_bf16 v[92:95], a[20:23], a[220:223], v[92:95]\n" \
"buffer_load_dwordx4 v193, s[44:47], s73 offen offset:3072 lds\n" \
"v_mfma_f32_16x16x32_bf16 v[224:227], v[244:247], v[124:127], v[224:227]\n" \
"v_accvgpr_read_b32 v112, a80\n" \
"v_accvgpr_read_b32 v113, a81\n" \
"v_accvgpr_read_b32 v114, a82\n" \
"v_mfma_f32_16x16x32_bf16 v[88:91], a[16:19], a[208:211], v[88:91]\n" \
"ds_read_b64_tr_b16 a[52:53], v222 offset:32768\n" \
"v_accvgpr_read_b32 v115, a83\n" \
"v_mfma_f32_16x16x32_bf16 v[88:91], a[20:23], a[212:215], v[88:91]\n" \
"s_add_u32 m0, s89, 65536\n" \
"ds_read_b128 a[16:19], v200 offset:4096\n" \
"v_accvgpr_read_b32 v116, a84\n" \
"v_accvgpr_read_b32 v117, a85\n" \
"v_mfma_f32_16x16x32_bf16 v[96:99], a[24:27], a[208:211], v[96:99]\n" \
"s_xor_b32 s73, s73, 0x300\n" \
"ds_read_b128 a[20:23], v201 offset:4096\n" \
"v_accvgpr_read_b32 v118, a86\n" \
"v_mfma_f32_16x16x32_bf16 v[96:99], a[28:31], a[212:215], v[96:99]\n" \
"buffer_load_dwordx4 v196, s[44:47], s75 offen lds\n" \
"v_mfma_f32_16x16x32_bf16 v[100:103], a[24:27], a[216:219], v[100:103]\n" \
"s_xor_b32 s75, s75, 0x300\n" \
"s_mul_i32 s76, s80, 393216\n" \
"ds_read_b64_tr_b16 a[54:55], v222 offset:35840\n" \
"v_mfma_f32_16x16x32_bf16 v[100:103], a[28:31], a[220:223], v[100:103]\n" \
"s_add_u32 s76, s76, s78\n" \
"ds_read_b128 a[24:27], v200 offset:8192\n" \
"v_accvgpr_read_b32 v119, a87\n" \
"v_mfma_f32_16x16x32_bf16 v[108:111], a[192:195], a[216:219], v[108:111]\n" \
"s_add_u32 m0, s91, 86016\n" \
"ds_read_b128 a[28:31], v201 offset:8192\n" \
"v_add_f32 v112, v112, v144\n" \
"v_mfma_f32_16x16x32_bf16 v[108:111], a[196:199], a[220:223], v[108:111]\n" \
"buffer_load_dwordx4 v197, s[48:51], s76 offen offset:0 lds\n" \
"v_add_f32 v113, v113, v145\n" \
"v_mfma_f32_16x16x32_bf16 v[104:107], a[192:195], a[208:211], v[104:107]\n" \
"v_add_f32 v114, v114, v146\n" \
"v_add_f32 v115, v115, v147\n" \
"v_mfma_f32_16x16x32_bf16 v[104:107], a[196:199], a[212:215], v[104:107]\n" \
"s_waitcnt lgkmcnt(2)\n" \
"v_add_f32 v116, v116, v148\n" \
"v_add_f32 v117, v117, v149\n" \
"v_mfma_f32_16x16x32_bf16 v[32:35], a[32:35], a[0:3], v[32:35]\n" \
"s_add_u32 s77, s76, 24576\n" \
"ds_read_b128 a[192:195], v200 offset:12288\n" \
"v_add_f32 v118, v118, v150\n" \
"v_mfma_f32_16x16x32_bf16 v[32:35], a[36:39], a[4:7], v[32:35]\n" \
"buffer_load_dwordx4 v198, s[48:51], s76 offen offset:1024 lds\n" \
"v_mfma_f32_16x16x32_bf16 v[40:43], a[32:35], a[8:11], v[40:43]\n" \
"ds_read_b128 a[196:199], v201 offset:12288\n" \
"v_add_f32 v119, v119, v151\n" \
"v_mfma_f32_16x16x32_bf16 v[40:43], a[36:39], a[12:15], v[40:43]\n" \
"ds_read_b64_tr_b16 a[200:201], v222 offset:21504\n" \
"v_mul_f32 v112, v112, v232\n" \
"v_mfma_f32_16x16x32_bf16 v[0:3], a[40:43], a[8:11], v[0:3]\n" \
"s_xor_b32 s80, s80, 0x6\n" \
"ds_read_b64_tr_b16 a[202:203], v222 offset:24576\n" \
"v_mul_f32 v113, v113, v232\n" \
"v_mfma_f32_16x16x32_bf16 v[0:3], a[44:47], a[12:15], v[0:3]\n" \
"buffer_load_dwordx4 v199, s[48:51], s76 offen offset:2048 lds\n" \
"v_mul_f32 v114, v114, v232\n" \
"v_mfma_f32_16x16x32_bf16 v[4:7], a[48:51], a[8:11], v[4:7]\n" \
"ds_read_b64_tr_b16 a[204:205], v222 offset:33792\n" \
"v_mul_f32 v115, v115, v232\n" \
"v_mfma_f32_16x16x32_bf16 v[4:7], a[52:55], a[12:15], v[4:7]\n" \
"ds_read_b64_tr_b16 a[206:207], v222 offset:36864\n" \
"v_mul_f32 v116, v116, v232\n" \
"v_mfma_f32_16x16x32_bf16 v[12:15], a[48:51], a[16:19], v[12:15]\n" \
"s_add_u32 m0, m0, 3072\n" \
"ds_read_b64_tr_b16 a[208:209], v204 offset:22528\n" \
"v_mul_f32 v117, v117, v232\n" \
"v_mfma_f32_16x16x32_bf16 v[12:15], a[52:55], a[20:23], v[12:15]\n" \
"buffer_load_dwordx4 v197, s[48:51], s77 offen offset:0 lds\n" \
"v_mfma_f32_16x16x32_bf16 v[8:11], a[40:43], a[16:19], v[8:11]\n" \
"ds_read_b64_tr_b16 a[210:211], v204 offset:25600\n" \
"v_mul_f32 v118, v118, v232\n" \
"v_mfma_f32_16x16x32_bf16 v[8:11], a[44:47], a[20:23], v[8:11]\n" \
"ds_read_b64_tr_b16 a[212:213], v204 offset:34816\n" \
"v_mul_f32 v119, v119, v232\n" \
"v_cvt_pk_bf16_f32 v120, v112, v113\n" \
"v_mfma_f32_16x16x32_bf16 v[48:51], a[32:35], a[16:19], v[48:51]\n" \
"s_waitcnt lgkmcnt(3)\n" \
"ds_read_b64_tr_b16 a[214:215], v204 offset:37888\n" \
"v_cvt_pk_bf16_f32 v121, v114, v115\n" \
"v_mfma_f32_16x16x32_bf16 v[48:51], a[36:39], a[20:23], v[48:51]\n" \
"buffer_load_dwordx4 v198, s[48:51], s77 offen offset:1024 lds\n" \
"v_mfma_f32_16x16x32_bf16 v[56:59], a[32:35], a[24:27], v[56:59]\n" \
"ds_read_b64_tr_b16 a[216:217], v222 offset:22528\n" \
"v_cvt_pk_bf16_f32 v122, v116, v117\n" \
"v_mfma_f32_16x16x32_bf16 v[56:59], a[36:39], a[28:31], v[56:59]\n" \
"ds_read_b64_tr_b16 a[218:219], v222 offset:25600\n" \
"v_cvt_pk_bf16_f32 v123, v118, v119\n" \
"v_mfma_f32_16x16x32_bf16 v[16:19], a[40:43], a[24:27], v[16:19]\n" \
"ds_read_b64_tr_b16 a[220:221], v222 offset:34816\n" \
"v_mfma_f32_16x16x32_bf16 v[16:19], a[44:47], a[28:31], v[16:19]\n" \
"buffer_load_dwordx4 v199, s[48:51], s77 offen offset:2048 lds\n" \
"v_mfma_f32_16x16x32_bf16 v[20:23], a[48:51], a[24:27], v[20:23]\n" \
"ds_read_b64_tr_b16 a[222:223], v222 offset:37888\n" \
"v_mfma_f32_16x16x32_bf16 v[20:23], a[52:55], a[28:31], v[20:23]\n" \
"buffer_store_dwordx4 v[120:123], v240, s[56:59], s88 offen offset:0 sc1\n" \
"v_mfma_f32_16x16x32_bf16 v[28:31], a[48:51], a[192:195], v[28:31]\n" \
"v_mfma_f32_16x16x32_bf16 v[28:31], a[52:55], a[196:199], v[28:31]\n" \
"v_mfma_f32_16x16x32_bf16 v[224:227], v[244:247], v[120:123], v[224:227]\n" \
"v_accvgpr_read_b32 v112, a88\n" \
"v_accvgpr_read_b32 v113, a89\n" \
"v_accvgpr_read_b32 v114, a90\n" \
"v_mfma_f32_16x16x32_bf16 v[24:27], a[40:43], a[192:195], v[24:27]\n" \
"v_accvgpr_read_b32 v115, a91\n" \
"v_accvgpr_read_b32 v116, a92\n" \
"v_mfma_f32_16x16x32_bf16 v[24:27], a[44:47], a[196:199], v[24:27]\n" \
"v_accvgpr_read_b32 v117, a93\n" \
"v_accvgpr_read_b32 v118, a94\n" \
"v_mfma_f32_16x16x32_bf16 v[64:67], a[32:35], a[192:195], v[64:67]\n" \
"v_accvgpr_read_b32 v119, a95\n" \
"v_add_f32 v112, v112, v152\n" \
"v_mfma_f32_16x16x32_bf16 v[64:67], a[36:39], a[196:199], v[64:67]\n" \
"v_add_f32 v113, v113, v153\n" \
"s_waitcnt vmcnt(13) lgkmcnt(0)\n" \
"s_barrier\n" \
"v_mfma_f32_16x16x32_bf16 v[68:71], a[200:203], a[192:195], v[68:71]\n" \
"ds_read_b64_tr_b16 a[32:33], v194 offset:1024\n" \
"ds_read_b64_tr_b16 a[34:35], v194 offset:4096\n" \
"v_mfma_f32_16x16x32_bf16 v[68:71], a[204:207], a[196:199], v[68:71]\n" \
"ds_read_b64_tr_b16 a[36:37], v194 offset:13312\n" \
"ds_read_b64_tr_b16 a[38:39], v194 offset:16384\n" \
"v_mfma_f32_16x16x32_bf16 v[60:63], a[200:203], a[24:27], v[60:63]\n" \
"s_add_u32 m0, s79, 0\n" \
"ds_read_b64_tr_b16 a[40:41], v194 offset:0\n" \
"v_add_f32 v114, v114, v154\n" \
"v_mfma_f32_16x16x32_bf16 v[60:63], a[204:207], a[28:31], v[60:63]\n" \
"buffer_load_dwordx4 v192, s[44:47], s73 offen offset:0 lds\n" \
"v_mfma_f32_16x16x32_bf16 v[52:55], a[200:203], a[16:19], v[52:55]\n" \
"ds_read_b64_tr_b16 a[42:43], v194 offset:3072\n" \
"v_add_f32 v115, v115, v155\n" \
"v_mfma_f32_16x16x32_bf16 v[52:55], a[204:207], a[20:23], v[52:55]\n" \
"ds_read_b64_tr_b16 a[44:45], v194 offset:12288\n" \
"v_add_f32 v116, v116, v156\n" \
"v_add_f32 v117, v117, v157\n" \
"v_mfma_f32_16x16x32_bf16 v[44:47], a[200:203], a[8:11], v[44:47]\n" \
"ds_read_b64_tr_b16 a[46:47], v194 offset:15360\n" \
"v_add_f32 v118, v118, v158\n" \
"v_mfma_f32_16x16x32_bf16 v[44:47], a[204:207], a[12:15], v[44:47]\n" \
"buffer_load_dwordx4 v193, s[44:47], s73 offen offset:1024 lds\n" \
"v_mfma_f32_16x16x32_bf16 v[72:75], a[0:3], a[208:211], v[72:75]\n" \
"ds_read_b64_tr_b16 a[48:49], v195 offset:0\n" \
"v_add_f32 v119, v119, v159\n" \
"v_mfma_f32_16x16x32_bf16 v[72:75], a[4:7], a[212:215], v[72:75]\n" \
"ds_read_b128 a[0:3], v207 offset:45056\n" \
"v_mul_f32 v112, v112, v232\n" \
"v_mfma_f32_16x16x32_bf16 v[80:83], a[8:11], a[208:211], v[80:83]\n" \
"ds_read_b128 a[4:7], v208 offset:45056\n" \
"v_mul_f32 v113, v113, v232\n" \
"v_mfma_f32_16x16x32_bf16 v[80:83], a[12:15], a[212:215], v[80:83]\n" \
"buffer_load_dwordx4 v192, s[44:47], s73 offen offset:2048 lds\n" \
"v_mul_f32 v114, v114, v232\n" \
"v_mfma_f32_16x16x32_bf16 v[84:87], a[8:11], a[216:219], v[84:87]\n" \
"ds_read_b64_tr_b16 a[50:51], v195 offset:3072\n" \
"v_mul_f32 v115, v115, v232\n" \
"v_mfma_f32_16x16x32_bf16 v[84:87], a[12:15], a[220:223], v[84:87]\n" \
"ds_read_b128 a[8:11], v205 offset:45056\n" \
"v_mul_f32 v116, v116, v232\n" \
"v_mfma_f32_16x16x32_bf16 v[92:95], a[16:19], a[216:219], v[92:95]\n" \
"ds_read_b128 a[12:15], v206 offset:45056\n" \
"v_mul_f32 v117, v117, v232\n" \
"v_mfma_f32_16x16x32_bf16 v[92:95], a[20:23], a[220:223], v[92:95]\n" \
"buffer_load_dwordx4 v193, s[44:47], s73 offen offset:3072 lds\n" \
"v_mfma_f32_16x16x32_bf16 v[88:91], a[16:19], a[208:211], v[88:91]\n" \
"ds_read_b64_tr_b16 a[52:53], v195 offset:12288\n" \
"v_mul_f32 v118, v118, v232\n" \
"v_mfma_f32_16x16x32_bf16 v[88:91], a[20:23], a[212:215], v[88:91]\n" \
"s_add_u32 m0, s89, 0\n" \
"ds_read_b128 a[16:19], v205 offset:49152\n" \
"v_mul_f32 v119, v119, v232\n" \
"v_mfma_f32_16x16x32_bf16 v[96:99], a[24:27], a[208:211], v[96:99]\n" \
"s_xor_b32 s73, s73, 0x80\n" \
"ds_read_b128 a[20:23], v206 offset:49152\n" \
"v_cvt_pk_bf16_f32 v124, v112, v113\n" \
"v_mfma_f32_16x16x32_bf16 v[96:99], a[28:31], a[212:215], v[96:99]\n" \
"buffer_load_dwordx4 v196, s[44:47], s75 offen lds\n" \
"v_cvt_pk_bf16_f32 v125, v114, v115\n" \
"v_mfma_f32_16x16x32_bf16 v[100:103], a[24:27], a[216:219], v[100:103]\n" \
"s_xor_b32 s75, s75, 0x80\n" \
"s_mul_i32 s76, s80, 393216\n" \
"ds_read_b64_tr_b16 a[54:55], v195 offset:15360\n" \
"v_mfma_f32_16x16x32_bf16 v[100:103], a[28:31], a[220:223], v[100:103]\n" \
"s_add_u32 s76, s76, s78\n" \
"ds_read_b128 a[24:27], v205 offset:53248\n" \
"v_cvt_pk_bf16_f32 v126, v116, v117\n" \
"v_mfma_f32_16x16x32_bf16 v[108:111], a[192:195], a[216:219], v[108:111]\n" \
"s_add_u32 m0, s91, 20480\n" \
"ds_read_b128 a[28:31], v206 offset:53248\n" \
"v_cvt_pk_bf16_f32 v127, v118, v119\n" \
"v_mfma_f32_16x16x32_bf16 v[108:111], a[196:199], a[220:223], v[108:111]\n" \
"buffer_load_dwordx4 v197, s[48:51], s76 offen offset:0 lds\n" \
"v_mfma_f32_16x16x32_bf16 v[104:107], a[192:195], a[208:211], v[104:107]\n" \
"s_waitcnt lgkmcnt(3)\n" \
"v_mfma_f32_16x16x32_bf16 v[104:107], a[196:199], a[212:215], v[104:107]\n" \
"buffer_store_dwordx4 v[124:127], v240, s[56:59], s88 offen offset:2048 sc1\n" \
"v_mfma_f32_16x16x32_bf16 v[32:35], a[32:35], a[0:3], v[32:35]\n" \
"s_add_u32 s77, s76, 24576\n" \
"ds_read_b128 a[192:195], v205 offset:57344\n" \
"v_mfma_f32_16x16x32_bf16 v[32:35], a[36:39], a[4:7], v[32:35]\n" \
"buffer_load_dwordx4 v198, s[48:51], s76 offen offset:1024 lds\n" \
"v_mfma_f32_16x16x32_bf16 v[224:227], v[244:247], v[124:127], v[224:227]\n" \
"s_add_u32 s82, s88, 532480\n" \
"v_accvgpr_read_b32 v112, a96\n" \
"v_accvgpr_read_b32 v113, a97\n" \
"v_accvgpr_read_b32 v114, a98\n" \
"v_mfma_f32_16x16x32_bf16 v[40:43], a[32:35], a[8:11], v[40:43]\n" \
"ds_read_b128 a[196:199], v206 offset:57344\n" \
"v_accvgpr_read_b32 v115, a99\n" \
"v_mfma_f32_16x16x32_bf16 v[40:43], a[36:39], a[12:15], v[40:43]\n" \
"s_waitcnt lgkmcnt(2)\n" \
"ds_read_b64_tr_b16 a[200:201], v195 offset:1024\n" \
"v_accvgpr_read_b32 v116, a100\n" \
"v_mfma_f32_16x16x32_bf16 v[0:3], a[40:43], a[8:11], v[0:3]\n" \
"s_xor_b32 s80, s80, 0x1\n" \
"ds_read_b64_tr_b16 a[202:203], v195 offset:4096\n" \
"v_accvgpr_read_b32 v117, a101\n" \
"v_mfma_f32_16x16x32_bf16 v[0:3], a[44:47], a[12:15], v[0:3]\n" \
"buffer_load_dwordx4 v199, s[48:51], s76 offen offset:2048 lds\n" \
"v_accvgpr_read_b32 v118, a102\n" \
"v_mfma_f32_16x16x32_bf16 v[4:7], a[48:51], a[8:11], v[4:7]\n" \
"ds_read_b64_tr_b16 a[204:205], v195 offset:13312\n" \
"v_accvgpr_read_b32 v119, a103\n" \
"v_mfma_f32_16x16x32_bf16 v[4:7], a[52:55], a[12:15], v[4:7]\n" \
"ds_read_b64_tr_b16 a[206:207], v195 offset:16384\n" \
"v_add_f32 v112, v112, v160\n" \
"v_mfma_f32_16x16x32_bf16 v[12:15], a[48:51], a[16:19], v[12:15]\n" \
"s_add_u32 m0, m0, 3072\n" \
"ds_read_b64_tr_b16 a[208:209], v194 offset:2048\n" \
"v_add_f32 v113, v113, v161\n" \
"v_mfma_f32_16x16x32_bf16 v[12:15], a[52:55], a[20:23], v[12:15]\n" \
"buffer_load_dwordx4 v197, s[48:51], s77 offen offset:0 lds\n" \
"v_mfma_f32_16x16x32_bf16 v[8:11], a[40:43], a[16:19], v[8:11]\n" \
"ds_read_b64_tr_b16 a[210:211], v194 offset:5120\n" \
"v_add_f32 v114, v114, v162\n" \
"v_mfma_f32_16x16x32_bf16 v[8:11], a[44:47], a[20:23], v[8:11]\n" \
"ds_read_b64_tr_b16 a[212:213], v194 offset:14336\n" \
"v_add_f32 v115, v115, v163\n" \
"v_mfma_f32_16x16x32_bf16 v[48:51], a[32:35], a[16:19], v[48:51]\n" \
"ds_read_b64_tr_b16 a[214:215], v194 offset:17408\n" \
"v_add_f32 v116, v116, v164\n" \
"v_mfma_f32_16x16x32_bf16 v[48:51], a[36:39], a[20:23], v[48:51]\n" \
"buffer_load_dwordx4 v198, s[48:51], s77 offen offset:1024 lds\n" \
"v_add_f32 v117, v117, v165\n" \
"v_mfma_f32_16x16x32_bf16 v[56:59], a[32:35], a[24:27], v[56:59]\n" \
"ds_read_b64_tr_b16 a[216:217], v195 offset:2048\n" \
"v_add_f32 v118, v118, v166\n" \
"v_mfma_f32_16x16x32_bf16 v[56:59], a[36:39], a[28:31], v[56:59]\n" \
"ds_read_b64_tr_b16 a[218:219], v195 offset:5120\n" \
"v_add_f32 v119, v119, v167\n" \
"v_mfma_f32_16x16x32_bf16 v[16:19], a[40:43], a[24:27], v[16:19]\n" \
"ds_read_b64_tr_b16 a[220:221], v195 offset:14336\n" \
"v_mul_f32 v112, v112, v233\n" \
"v_mfma_f32_16x16x32_bf16 v[16:19], a[44:47], a[28:31], v[16:19]\n" \
"buffer_load_dwordx4 v199, s[48:51], s77 offen offset:2048 lds\n" \
"v_mfma_f32_16x16x32_bf16 v[20:23], a[48:51], a[24:27], v[20:23]\n" \
"ds_read_b64_tr_b16 a[222:223], v195 offset:17408\n" \
"v_mul_f32 v113, v113, v233\n" \
"v_mfma_f32_16x16x32_bf16 v[20:23], a[52:55], a[28:31], v[20:23]\n" \
"s_waitcnt lgkmcnt(3)\n" \
"v_mul_f32 v114, v114, v233\n" \
"v_mul_f32 v115, v115, v233\n" \
"v_mul_f32 v116, v116, v233\n" \
"v_mfma_f32_16x16x32_bf16 v[28:31], a[48:51], a[192:195], v[28:31]\n" \
"v_mul_f32 v117, v117, v233\n" \
"v_mul_f32 v118, v118, v233\n" \
"v_mfma_f32_16x16x32_bf16 v[28:31], a[52:55], a[196:199], v[28:31]\n" \
"v_mul_f32 v119, v119, v233\n" \
"v_cvt_pk_bf16_f32 v120, v112, v113\n" \
"v_mfma_f32_16x16x32_bf16 v[24:27], a[40:43], a[192:195], v[24:27]\n" \
"v_cvt_pk_bf16_f32 v121, v114, v115\n" \
"v_cvt_pk_bf16_f32 v122, v116, v117\n" \
"v_mfma_f32_16x16x32_bf16 v[24:27], a[44:47], a[196:199], v[24:27]\n" \
"v_cvt_pk_bf16_f32 v123, v118, v119\n" \
"v_mfma_f32_16x16x32_bf16 v[64:67], a[32:35], a[192:195], v[64:67]\n" \
"buffer_store_dwordx4 v[120:123], v236, s[56:59], s82 offen offset:0 sc1\n" \
"v_mfma_f32_16x16x32_bf16 v[64:67], a[36:39], a[196:199], v[64:67]\n" \
"v_mfma_f32_16x16x32_bf16 v[224:227], v[244:247], v[120:123], v[224:227]\n" \
"v_accvgpr_read_b32 v112, a104\n" \
"s_waitcnt vmcnt(14) lgkmcnt(0)\n" \
"s_barrier\n" \
"v_mfma_f32_16x16x32_bf16 v[68:71], a[200:203], a[192:195], v[68:71]\n" \
"ds_read_b64_tr_b16 a[32:33], v209 offset:1024\n" \
"ds_read_b64_tr_b16 a[34:35], v209 offset:4096\n" \
"v_mfma_f32_16x16x32_bf16 v[68:71], a[204:207], a[196:199], v[68:71]\n" \
"ds_read_b64_tr_b16 a[36:37], v209 offset:13312\n" \
"ds_read_b64_tr_b16 a[38:39], v209 offset:16384\n" \
"v_mfma_f32_16x16x32_bf16 v[60:63], a[200:203], a[24:27], v[60:63]\n" \
"s_add_u32 m0, s79, 110592\n" \
"ds_read_b64_tr_b16 a[40:41], v209 offset:0\n" \
"v_accvgpr_read_b32 v113, a105\n" \
"v_mfma_f32_16x16x32_bf16 v[60:63], a[204:207], a[28:31], v[60:63]\n" \
"buffer_load_dwordx4 v192, s[44:47], s73 offen offset:0 lds\n" \
"v_mfma_f32_16x16x32_bf16 v[52:55], a[200:203], a[16:19], v[52:55]\n" \
"ds_read_b64_tr_b16 a[42:43], v209 offset:3072\n" \
"v_accvgpr_read_b32 v114, a106\n" \
"v_mfma_f32_16x16x32_bf16 v[52:55], a[204:207], a[20:23], v[52:55]\n" \
"ds_read_b64_tr_b16 a[44:45], v209 offset:12288\n" \
"v_accvgpr_read_b32 v115, a107\n" \
"v_mfma_f32_16x16x32_bf16 v[44:47], a[200:203], a[8:11], v[44:47]\n" \
"ds_read_b64_tr_b16 a[46:47], v209 offset:15360\n" \
"v_accvgpr_read_b32 v116, a108\n" \
"v_mfma_f32_16x16x32_bf16 v[44:47], a[204:207], a[12:15], v[44:47]\n" \
"buffer_load_dwordx4 v193, s[44:47], s73 offen offset:1024 lds\n" \
"v_accvgpr_read_b32 v117, a109\n" \
"v_mfma_f32_16x16x32_bf16 v[72:75], a[0:3], a[208:211], v[72:75]\n" \
"ds_read_b64_tr_b16 a[48:49], v210 offset:0\n" \
"v_accvgpr_read_b32 v118, a110\n" \
"v_mfma_f32_16x16x32_bf16 v[72:75], a[4:7], a[212:215], v[72:75]\n" \
"ds_read_b128 a[0:3], v207 offset:0\n" \
"v_accvgpr_read_b32 v119, a111\n" \
"v_mfma_f32_16x16x32_bf16 v[80:83], a[8:11], a[208:211], v[80:83]\n" \
"ds_read_b128 a[4:7], v208 offset:0\n" \
"v_add_f32 v112, v112, v168\n" \
"v_mfma_f32_16x16x32_bf16 v[80:83], a[12:15], a[212:215], v[80:83]\n" \
"buffer_load_dwordx4 v192, s[44:47], s73 offen offset:2048 lds\n" \
"v_mfma_f32_16x16x32_bf16 v[84:87], a[8:11], a[216:219], v[84:87]\n" \
"Lpg7951_%=:\n" \
"Lst1x0_%=:\n" \
"s_cbranch_execz Lpg10253_%=\n" \
"ds_read_b64_tr_b16 a[50:51], v210 offset:3072\n" \
"v_add_f32 v113, v113, v169\n" \
"v_mfma_f32_16x16x32_bf16 v[84:87], a[12:15], a[220:223], v[84:87]\n" \
"ds_read_b128 a[8:11], v205 offset:0\n" \
"v_add_f32 v114, v114, v170\n" \
"v_add_f32 v115, v115, v171\n" \
"v_mfma_f32_16x16x32_bf16 v[92:95], a[16:19], a[216:219], v[92:95]\n" \
"ds_read_b128 a[12:15], v206 offset:0\n" \
"v_add_f32 v116, v116, v172\n" \
"v_mfma_f32_16x16x32_bf16 v[92:95], a[20:23], a[220:223], v[92:95]\n" \
"buffer_load_dwordx4 v193, s[44:47], s73 offen offset:3072 lds\n" \
"v_mfma_f32_16x16x32_bf16 v[88:91], a[16:19], a[208:211], v[88:91]\n" \
"ds_read_b64_tr_b16 a[52:53], v210 offset:12288\n" \
"v_add_f32 v117, v117, v173\n" \
"v_mfma_f32_16x16x32_bf16 v[88:91], a[20:23], a[212:215], v[88:91]\n" \
"s_add_u32 m0, s89, 110592\n" \
"ds_read_b128 a[16:19], v205 offset:4096\n" \
"v_add_f32 v118, v118, v174\n" \
"v_mfma_f32_16x16x32_bf16 v[96:99], a[24:27], a[208:211], v[96:99]\n" \
"s_xor_b32 s73, s73, 0x180\n" \
"ds_read_b128 a[20:23], v206 offset:4096\n" \
"v_add_f32 v119, v119, v175\n" \
"v_mfma_f32_16x16x32_bf16 v[96:99], a[28:31], a[212:215], v[96:99]\n" \
"buffer_load_dwordx4 v196, s[44:47], s75 offen lds\n" \
"v_mul_f32 v112, v112, v233\n" \
"v_mfma_f32_16x16x32_bf16 v[100:103], a[24:27], a[216:219], v[100:103]\n" \
"s_xor_b32 s75, s75, 0x180\n" \
"s_mul_i32 s76, s80, 393216\n" \
"ds_read_b64_tr_b16 a[54:55], v210 offset:15360\n" \
"v_mfma_f32_16x16x32_bf16 v[100:103], a[28:31], a[220:223], v[100:103]\n" \
"s_add_u32 s76, s76, s78\n" \
"ds_read_b128 a[24:27], v205 offset:8192\n" \
"v_mul_f32 v113, v113, v233\n" \
"v_mfma_f32_16x16x32_bf16 v[108:111], a[192:195], a[216:219], v[108:111]\n" \
"s_add_u32 m0, s91, 139264\n" \
"ds_read_b128 a[28:31], v206 offset:8192\n" \
"v_mul_f32 v114, v114, v233\n" \
"v_mfma_f32_16x16x32_bf16 v[108:111], a[196:199], a[220:223], v[108:111]\n" \
"buffer_load_dwordx4 v197, s[48:51], s76 offen offset:0 lds\n" \
"v_mfma_f32_16x16x32_bf16 v[104:107], a[192:195], a[208:211], v[104:107]\n" \
"v_mul_f32 v115, v115, v233\n" \
"v_mul_f32 v116, v116, v233\n" \
"v_mfma_f32_16x16x32_bf16 v[104:107], a[196:199], a[212:215], v[104:107]\n" \
"s_waitcnt lgkmcnt(2)\n" \
"v_mul_f32 v117, v117, v233\n" \
"v_mul_f32 v118, v118, v233\n" \
"v_mfma_f32_16x16x32_bf16 v[32:35], a[32:35], a[0:3], v[32:35]\n" \
"s_add_u32 s77, s76, 24576\n" \
"ds_read_b128 a[192:195], v205 offset:12288\n" \
"v_mul_f32 v119, v119, v233\n" \
"v_mfma_f32_16x16x32_bf16 v[32:35], a[36:39], a[4:7], v[32:35]\n" \
"buffer_load_dwordx4 v198, s[48:51], s76 offen offset:1024 lds\n" \
"v_cvt_pk_bf16_f32 v124, v112, v113\n" \
"v_mfma_f32_16x16x32_bf16 v[40:43], a[32:35], a[8:11], v[40:43]\n" \
"ds_read_b128 a[196:199], v206 offset:12288\n" \
"v_cvt_pk_bf16_f32 v125, v114, v115\n" \
"v_mfma_f32_16x16x32_bf16 v[40:43], a[36:39], a[12:15], v[40:43]\n" \
"ds_read_b64_tr_b16 a[200:201], v210 offset:1024\n" \
"v_cvt_pk_bf16_f32 v126, v116, v117\n" \
"v_mfma_f32_16x16x32_bf16 v[0:3], a[40:43], a[8:11], v[0:3]\n" \
"s_xor_b32 s80, s80, 0x3\n" \
"ds_read_b64_tr_b16 a[202:203], v210 offset:4096\n" \
"v_cvt_pk_bf16_f32 v127, v118, v119\n" \
"v_mfma_f32_16x16x32_bf16 v[0:3], a[44:47], a[12:15], v[0:3]\n" \
"buffer_load_dwordx4 v199, s[48:51], s76 offen offset:2048 lds\n" \
"v_mfma_f32_16x16x32_bf16 v[4:7], a[48:51], a[8:11], v[4:7]\n" \
"ds_read_b64_tr_b16 a[204:205], v210 offset:13312\n" \
"v_mfma_f32_16x16x32_bf16 v[4:7], a[52:55], a[12:15], v[4:7]\n" \
"ds_read_b64_tr_b16 a[206:207], v210 offset:16384\n" \
"buffer_store_dwordx4 v[124:127], v236, s[56:59], s82 offen offset:2048 sc1\n" \
"v_mfma_f32_16x16x32_bf16 v[12:15], a[48:51], a[16:19], v[12:15]\n" \
"s_add_u32 m0, m0, 3072\n" \
"ds_read_b64_tr_b16 a[208:209], v209 offset:2048\n" \
"v_mfma_f32_16x16x32_bf16 v[12:15], a[52:55], a[20:23], v[12:15]\n" \
"buffer_load_dwordx4 v197, s[48:51], s77 offen offset:0 lds\n" \
"v_mfma_f32_16x16x32_bf16 v[224:227], v[244:247], v[124:127], v[224:227]\n" \
"v_accvgpr_read_b32 v112, a112\n" \
"v_accvgpr_read_b32 v113, a113\n" \
"v_accvgpr_read_b32 v114, a114\n" \
"v_mfma_f32_16x16x32_bf16 v[8:11], a[40:43], a[16:19], v[8:11]\n" \
"ds_read_b64_tr_b16 a[210:211], v209 offset:5120\n" \
"v_accvgpr_read_b32 v115, a115\n" \
"v_mfma_f32_16x16x32_bf16 v[8:11], a[44:47], a[20:23], v[8:11]\n" \
"ds_read_b64_tr_b16 a[212:213], v209 offset:14336\n" \
"v_accvgpr_read_b32 v116, a116\n" \
"v_mfma_f32_16x16x32_bf16 v[48:51], a[32:35], a[16:19], v[48:51]\n" \
"s_waitcnt lgkmcnt(3)\n" \
"ds_read_b64_tr_b16 a[214:215], v209 offset:17408\n" \
"v_accvgpr_read_b32 v117, a117\n" \
"v_mfma_f32_16x16x32_bf16 v[48:51], a[36:39], a[20:23], v[48:51]\n" \
"buffer_load_dwordx4 v198, s[48:51], s77 offen offset:1024 lds\n" \
"v_accvgpr_read_b32 v118, a118\n" \
"v_mfma_f32_16x16x32_bf16 v[56:59], a[32:35], a[24:27], v[56:59]\n" \
"ds_read_b64_tr_b16 a[216:217], v210 offset:2048\n" \
"v_accvgpr_read_b32 v119, a119\n" \
"v_mfma_f32_16x16x32_bf16 v[56:59], a[36:39], a[28:31], v[56:59]\n" \
"ds_read_b64_tr_b16 a[218:219], v210 offset:5120\n" \
"v_add_f32 v112, v112, v176\n" \
"v_mfma_f32_16x16x32_bf16 v[16:19], a[40:43], a[24:27], v[16:19]\n" \
"ds_read_b64_tr_b16 a[220:221], v210 offset:14336\n" \
"v_add_f32 v113, v113, v177\n" \
"v_mfma_f32_16x16x32_bf16 v[16:19], a[44:47], a[28:31], v[16:19]\n" \
"buffer_load_dwordx4 v199, s[48:51], s77 offen offset:2048 lds\n" \
"v_mfma_f32_16x16x32_bf16 v[20:23], a[48:51], a[24:27], v[20:23]\n" \
"ds_read_b64_tr_b16 a[222:223], v210 offset:17408\n" \
"v_add_f32 v114, v114, v178\n" \
"v_mfma_f32_16x16x32_bf16 v[20:23], a[52:55], a[28:31], v[20:23]\n" \
"v_add_f32 v115, v115, v179\n" \
"v_add_f32 v116, v116, v180\n" \
"v_mfma_f32_16x16x32_bf16 v[28:31], a[48:51], a[192:195], v[28:31]\n" \
"v_add_f32 v117, v117, v181\n" \
"v_add_f32 v118, v118, v182\n" \
"v_mfma_f32_16x16x32_bf16 v[28:31], a[52:55], a[196:199], v[28:31]\n" \
"v_add_f32 v119, v119, v183\n" \
"v_mul_f32 v112, v112, v233\n" \
"v_mul_f32 v113, v113, v233\n" \
"v_mfma_f32_16x16x32_bf16 v[24:27], a[40:43], a[192:195], v[24:27]\n" \
"v_mul_f32 v114, v114, v233\n" \
"v_mul_f32 v115, v115, v233\n" \
"v_mfma_f32_16x16x32_bf16 v[24:27], a[44:47], a[196:199], v[24:27]\n" \
"v_mul_f32 v116, v116, v233\n" \
"v_mul_f32 v117, v117, v233\n" \
"v_mfma_f32_16x16x32_bf16 v[64:67], a[32:35], a[192:195], v[64:67]\n" \
"v_mul_f32 v118, v118, v233\n" \
"v_mul_f32 v119, v119, v233\n" \
"v_mfma_f32_16x16x32_bf16 v[64:67], a[36:39], a[196:199], v[64:67]\n" \
"s_waitcnt vmcnt(13) lgkmcnt(0)\n" \
"s_barrier\n" \
"v_mfma_f32_16x16x32_bf16 v[68:71], a[200:203], a[192:195], v[68:71]\n" \
"ds_read_b64_tr_b16 a[32:33], v204 offset:21504\n" \
"ds_read_b64_tr_b16 a[34:35], v204 offset:24576\n" \
"v_mfma_f32_16x16x32_bf16 v[68:71], a[204:207], a[196:199], v[68:71]\n" \
"ds_read_b64_tr_b16 a[36:37], v204 offset:33792\n" \
"ds_read_b64_tr_b16 a[38:39], v204 offset:36864\n" \
"v_cvt_pk_bf16_f32 v120, v112, v113\n" \
"v_mfma_f32_16x16x32_bf16 v[60:63], a[200:203], a[24:27], v[60:63]\n" \
"s_add_u32 m0, s79, 65536\n" \
"ds_read_b64_tr_b16 a[40:41], v204 offset:20480\n" \
"v_cvt_pk_bf16_f32 v121, v114, v115\n" \
"v_mfma_f32_16x16x32_bf16 v[60:63], a[204:207], a[28:31], v[60:63]\n" \
"buffer_load_dwordx4 v192, s[44:47], s73 offen offset:0 lds\n" \
"v_mfma_f32_16x16x32_bf16 v[52:55], a[200:203], a[16:19], v[52:55]\n" \
"ds_read_b64_tr_b16 a[42:43], v204 offset:23552\n" \
"v_cvt_pk_bf16_f32 v122, v116, v117\n" \
"v_mfma_f32_16x16x32_bf16 v[52:55], a[204:207], a[20:23], v[52:55]\n" \
"ds_read_b64_tr_b16 a[44:45], v204 offset:32768\n" \
"v_cvt_pk_bf16_f32 v123, v118, v119\n" \
"v_mfma_f32_16x16x32_bf16 v[44:47], a[200:203], a[8:11], v[44:47]\n" \
"ds_read_b64_tr_b16 a[46:47], v204 offset:35840\n" \
"v_mfma_f32_16x16x32_bf16 v[44:47], a[204:207], a[12:15], v[44:47]\n" \
"buffer_load_dwordx4 v193, s[44:47], s73 offen offset:1024 lds\n" \
"v_mfma_f32_16x16x32_bf16 v[72:75], a[0:3], a[208:211], v[72:75]\n" \
"ds_read_b64_tr_b16 a[48:49], v222 offset:20480\n" \
"v_mfma_f32_16x16x32_bf16 v[72:75], a[4:7], a[212:215], v[72:75]\n" \
"ds_read_b128 a[0:3], v202 offset:0\n" \
"buffer_store_dwordx4 v[120:123], v240, s[56:59], s82 offen offset:0 sc1\n" \
"v_mfma_f32_16x16x32_bf16 v[80:83], a[8:11], a[208:211], v[80:83]\n" \
"ds_read_b128 a[4:7], v203 offset:0\n" \
"v_mfma_f32_16x16x32_bf16 v[80:83], a[12:15], a[212:215], v[80:83]\n" \
"buffer_load_dwordx4 v192, s[44:47], s73 offen offset:2048 lds\n" \
"v_mfma_f32_16x16x32_bf16 v[224:227], v[244:247], v[120:123], v[224:227]\n" \
"v_accvgpr_read_b32 v112, a120\n" \
"v_accvgpr_read_b32 v113, a121\n" \
"v_accvgpr_read_b32 v114, a122\n" \
"v_mfma_f32_16x16x32_bf16 v[84:87], a[8:11], a[216:219], v[84:87]\n" \
"ds_read_b64_tr_b16 a[50:51], v222 offset:23552\n" \
"v_accvgpr_read_b32 v115, a123\n" \
"v_mfma_f32_16x16x32_bf16 v[84:87], a[12:15], a[220:223], v[84:87]\n" \
"ds_read_b128 a[8:11], v200 offset:0\n" \
"v_accvgpr_read_b32 v116, a124\n" \
"v_mfma_f32_16x16x32_bf16 v[92:95], a[16:19], a[216:219], v[92:95]\n" \
"ds_read_b128 a[12:15], v201 offset:0\n" \
"v_accvgpr_read_b32 v117, a125\n" \
"v_mfma_f32_16x16x32_bf16 v[92:95], a[20:23], a[220:223], v[92:95]\n" \
"buffer_load_dwordx4 v193, s[44:47], s73 offen offset:3072 lds\n" \
"v_accvgpr_read_b32 v118, a126\n" \
"v_mfma_f32_16x16x32_bf16 v[88:91], a[16:19], a[208:211], v[88:91]\n" \
"ds_read_b64_tr_b16 a[52:53], v222 offset:32768\n" \
"v_accvgpr_read_b32 v119, a127\n" \
"v_mfma_f32_16x16x32_bf16 v[88:91], a[20:23], a[212:215], v[88:91]\n" \
"s_add_u32 m0, s89, 65536\n" \
"ds_read_b128 a[16:19], v200 offset:4096\n" \
"v_add_f32 v112, v112, v184\n" \
"v_mfma_f32_16x16x32_bf16 v[96:99], a[24:27], a[208:211], v[96:99]\n" \
"s_xor_b32 s73, s73, 0x80\n" \
"ds_read_b128 a[20:23], v201 offset:4096\n" \
"v_add_f32 v113, v113, v185\n" \
"v_mfma_f32_16x16x32_bf16 v[96:99], a[28:31], a[212:215], v[96:99]\n" \
"buffer_load_dwordx4 v196, s[44:47], s75 offen lds\n" \
"v_mfma_f32_16x16x32_bf16 v[100:103], a[24:27], a[216:219], v[100:103]\n" \
"s_xor_b32 s75, s75, 0x80\n" \
"s_mul_i32 s76, s80, 393216\n" \
"ds_read_b64_tr_b16 a[54:55], v222 offset:35840\n" \
"v_mfma_f32_16x16x32_bf16 v[100:103], a[28:31], a[220:223], v[100:103]\n" \
"s_add_u32 s76, s76, s78\n" \
"ds_read_b128 a[24:27], v200 offset:8192\n" \
"v_add_f32 v114, v114, v186\n" \
"v_add_f32 v115, v115, v187\n" \
"v_mfma_f32_16x16x32_bf16 v[108:111], a[192:195], a[216:219], v[108:111]\n" \
"s_add_u32 m0, s91, 86016\n" \
"ds_read_b128 a[28:31], v201 offset:8192\n" \
"v_add_f32 v116, v116, v188\n" \
"v_mfma_f32_16x16x32_bf16 v[108:111], a[196:199], a[220:223], v[108:111]\n" \
"buffer_load_dwordx4 v197, s[48:51], s76 offen offset:0 lds\n" \
"v_mfma_f32_16x16x32_bf16 v[104:107], a[192:195], a[208:211], v[104:107]\n" \
"v_add_f32 v117, v117, v189\n" \
"v_add_f32 v118, v118, v190\n" \
"v_mfma_f32_16x16x32_bf16 v[104:107], a[196:199], a[212:215], v[104:107]\n" \
"s_waitcnt lgkmcnt(2)\n" \
"v_add_f32 v119, v119, v191\n" \
"v_mul_f32 v112, v112, v233\n" \
"v_mfma_f32_16x16x32_bf16 v[32:35], a[32:35], a[0:3], v[32:35]\n" \
"s_add_u32 s77, s76, 24576\n" \
"ds_read_b128 a[192:195], v200 offset:12288\n" \
"v_mul_f32 v113, v113, v233\n" \
"v_mfma_f32_16x16x32_bf16 v[32:35], a[36:39], a[4:7], v[32:35]\n" \
"buffer_load_dwordx4 v198, s[48:51], s76 offen offset:1024 lds\n" \
"v_mul_f32 v114, v114, v233\n" \
"v_mfma_f32_16x16x32_bf16 v[40:43], a[32:35], a[8:11], v[40:43]\n" \
"ds_read_b128 a[196:199], v201 offset:12288\n" \
"v_mul_f32 v115, v115, v233\n" \
"v_mfma_f32_16x16x32_bf16 v[40:43], a[36:39], a[12:15], v[40:43]\n" \
"ds_read_b64_tr_b16 a[200:201], v222 offset:21504\n" \
"v_mul_f32 v116, v116, v233\n" \
"v_mfma_f32_16x16x32_bf16 v[0:3], a[40:43], a[8:11], v[0:3]\n" \
"s_xor_b32 s80, s80, 0x1\n" \
"ds_read_b64_tr_b16 a[202:203], v222 offset:24576\n" \
"v_mul_f32 v117, v117, v233\n" \
"v_mfma_f32_16x16x32_bf16 v[0:3], a[44:47], a[12:15], v[0:3]\n" \
"buffer_load_dwordx4 v199, s[48:51], s76 offen offset:2048 lds\n" \
"v_mfma_f32_16x16x32_bf16 v[4:7], a[48:51], a[8:11], v[4:7]\n" \
"ds_read_b64_tr_b16 a[204:205], v222 offset:33792\n" \
"v_mul_f32_e64 v118, v118, v233\n" \
"v_mfma_f32_16x16x32_bf16 v[4:7], a[52:55], a[12:15], v[4:7]\n" \
"ds_read_b64_tr_b16 a[206:207], v222 offset:36864\n" \
"v_mul_f32_e64 v119, v119, v233\n" \
"v_mfma_f32_16x16x32_bf16 v[12:15], a[48:51], a[16:19], v[12:15]\n" \
"s_add_u32 m0, m0, 3072\n" \
"ds_read_b64_tr_b16 a[208:209], v204 offset:22528\n" \
"v_cvt_pk_bf16_f32 v124, v112, v113\n" \
"v_mfma_f32_16x16x32_bf16 v[12:15], a[52:55], a[20:23], v[12:15]\n" \
"buffer_load_dwordx4 v197, s[48:51], s77 offen offset:0 lds\n" \
"v_cvt_pk_bf16_f32 v125, v114, v115\n" \
"v_mfma_f32_16x16x32_bf16 v[8:11], a[40:43], a[16:19], v[8:11]\n" \
"ds_read_b64_tr_b16 a[210:211], v204 offset:25600\n" \
"v_cvt_pk_bf16_f32 v126, v116, v117\n" \
"v_mfma_f32_16x16x32_bf16 v[8:11], a[44:47], a[20:23], v[8:11]\n" \
"ds_read_b64_tr_b16 a[212:213], v204 offset:34816\n" \
"v_cvt_pk_bf16_f32 v127, v118, v119\n" \
"v_mfma_f32_16x16x32_bf16 v[48:51], a[32:35], a[16:19], v[48:51]\n" \
"s_waitcnt lgkmcnt(3)\n" \
"ds_read_b64_tr_b16 a[214:215], v204 offset:37888\n" \
"v_mfma_f32_16x16x32_bf16 v[48:51], a[36:39], a[20:23], v[48:51]\n" \
"buffer_load_dwordx4 v198, s[48:51], s77 offen offset:1024 lds\n" \
"v_mfma_f32_16x16x32_bf16 v[56:59], a[32:35], a[24:27], v[56:59]\n" \
"ds_read_b64_tr_b16 a[216:217], v222 offset:22528\n" \
"v_mfma_f32_16x16x32_bf16 v[56:59], a[36:39], a[28:31], v[56:59]\n" \
"ds_read_b64_tr_b16 a[218:219], v222 offset:25600\n" \
"buffer_store_dwordx4 v[124:127], v240, s[56:59], s82 offen offset:2048 sc1\n" \
"v_mfma_f32_16x16x32_bf16 v[16:19], a[40:43], a[24:27], v[16:19]\n" \
"ds_read_b64_tr_b16 a[220:221], v222 offset:34816\n" \
"v_mfma_f32_16x16x32_bf16 v[16:19], a[44:47], a[28:31], v[16:19]\n" \
"buffer_load_dwordx4 v199, s[48:51], s77 offen offset:2048 lds\n" \
"v_mfma_f32_16x16x32_bf16 v[224:227], v[244:247], v[124:127], v[224:227]\n" \
"v_mfma_f32_16x16x32_bf16 v[20:23], a[48:51], a[24:27], v[20:23]\n" \
"ds_read_b64_tr_b16 a[222:223], v222 offset:37888\n" \
"v_mfma_f32_16x16x32_bf16 v[20:23], a[52:55], a[28:31], v[20:23]\n" \
"v_mfma_f32_16x16x32_bf16 v[28:31], a[48:51], a[192:195], v[28:31]\n" \
"v_mfma_f32_16x16x32_bf16 v[28:31], a[52:55], a[196:199], v[28:31]\n" \
"v_mfma_f32_16x16x32_bf16 v[24:27], a[40:43], a[192:195], v[24:27]\n" \
"v_mfma_f32_16x16x32_bf16 v[24:27], a[44:47], a[196:199], v[24:27]\n" \
"v_mfma_f32_16x16x32_bf16 v[64:67], a[32:35], a[192:195], v[64:67]\n" \
"v_mfma_f32_16x16x32_bf16 v[64:67], a[36:39], a[196:199], v[64:67]\n" \
"s_waitcnt vmcnt(13) lgkmcnt(0)\n" \
"s_barrier\n" \
"s_movk_i32 s64, 0x207\n" \
"v_cmp_class_f32 vcc, v224, s64\n" \
"s_nop 1\n" \
"s_cmp_lg_u64 vcc, 0\n" \
"s_addc_u32 s90, s90, 0\n" \
"s_cmp_lg_u32 s90, 0\n" \
"s_cbranch_scc1 Lexact_%=\n" \
"Lexr_%=:\n" \
"v_mfma_f32_16x16x32_bf16 v[68:71], a[200:203], a[192:195], v[68:71]\n" \
"s_lshl_b32 s15, s63, 7\n" \
"ds_read_b64_tr_b16 a[32:33], v194 offset:1024\n" \
"ds_read_b64_tr_b16 a[34:35], v194 offset:4096\n" \
"v_mfma_f32_16x16x32_bf16 v[68:71], a[204:207], a[196:199], v[68:71]\n" \
"s_lshl2_add_u32 s16, s62, 65589248\n" \
"ds_read_b64_tr_b16 a[36:37], v194 offset:13312\n" \
"ds_read_b64_tr_b16 a[38:39], v194 offset:16384\n" \
"v_mfma_f32_16x16x32_bf16 v[60:63], a[200:203], a[24:27], v[60:63]\n" \
"s_add_u32 m0, s79, 0\n" \
"s_sub_u32 s17, s16, s15\n" \
"ds_read_b64_tr_b16 a[40:41], v194 offset:0\n" \
"v_mfma_f32_16x16x32_bf16 v[60:63], a[204:207], a[28:31], v[60:63]\n" \
"buffer_load_dwordx4 v192, s[44:47], s73 offen offset:0 lds\n" \
"v_mfma_f32_16x16x32_bf16 v[52:55], a[200:203], a[16:19], v[52:55]\n" \
"s_add_u32 s15, s15, s16\n" \
"s_add_u32 s17, s17, 1920\n" \
"ds_read_b64_tr_b16 a[42:43], v194 offset:3072\n" \
"v_mfma_f32_16x16x32_bf16 v[52:55], a[204:207], a[20:23], v[52:55]\n" \
"ds_read_b64_tr_b16 a[44:45], v194 offset:12288\n" \
"v_mov_b32 v146, s60\n" \
"v_mfma_f32_16x16x32_bf16 v[44:47], a[200:203], a[8:11], v[44:47]\n" \
"ds_read_b64_tr_b16 a[46:47], v194 offset:15360\n" \
"v_lshlrev_b32 v147, 28, s61\n" \
"v_mfma_f32_16x16x32_bf16 v[44:47], a[204:207], a[12:15], v[44:47]\n" \
"buffer_load_dwordx4 v193, s[44:47], s73 offen offset:1024 lds\n" \
"v_mfma_f32_16x16x32_bf16 v[72:75], a[0:3], a[208:211], v[72:75]\n" \
"ds_read_b64_tr_b16 a[48:49], v195 offset:0\n" \
"v_mfma_f32_16x16x32_bf16 v[72:75], a[4:7], a[212:215], v[72:75]\n" \
"ds_read_b128 a[0:3], v207 offset:45056\n" \
"v_mfma_f32_16x16x32_bf16 v[80:83], a[8:11], a[208:211], v[80:83]\n" \
"ds_read_b128 a[4:7], v208 offset:45056\n" \
"v_mfma_f32_16x16x32_bf16 v[80:83], a[12:15], a[212:215], v[80:83]\n" \
"buffer_load_dwordx4 v192, s[44:47], s73 offen offset:2048 lds\n" \
"v_mfma_f32_16x16x32_bf16 v[84:87], a[8:11], a[216:219], v[84:87]\n" \
"ds_read_b64_tr_b16 a[50:51], v195 offset:3072\n" \
"v_mfma_f32_16x16x32_bf16 v[84:87], a[12:15], a[220:223], v[84:87]\n" \
"ds_read_b128 a[8:11], v205 offset:45056\n" \
"v_mfma_f32_16x16x32_bf16 v[92:95], a[16:19], a[216:219], v[92:95]\n" \
"ds_read_b128 a[12:15], v206 offset:45056\n" \
"v_mfma_f32_16x16x32_bf16 v[92:95], a[20:23], a[220:223], v[92:95]\n" \
"buffer_load_dwordx4 v193, s[44:47], s73 offen offset:3072 lds\n" \
"v_mfma_f32_16x16x32_bf16 v[88:91], a[16:19], a[208:211], v[88:91]\n" \
"ds_read_b64_tr_b16 a[52:53], v195 offset:12288\n" \
"v_mfma_f32_16x16x32_bf16 v[88:91], a[20:23], a[212:215], v[88:91]\n" \
"s_add_u32 m0, s89, 0\n" \
"ds_read_b128 a[16:19], v205 offset:49152\n" \
"v_mfma_f32_16x16x32_bf16 v[96:99], a[24:27], a[208:211], v[96:99]\n" \
"s_xor_b32 s73, s73, 0x780\n" \
"ds_read_b128 a[20:23], v206 offset:49152\n" \
"v_mfma_f32_16x16x32_bf16 v[96:99], a[28:31], a[212:215], v[96:99]\n" \
"buffer_load_dwordx4 v196, s[44:47], s75 offen lds\n" \
"v_mfma_f32_16x16x32_bf16 v[100:103], a[24:27], a[216:219], v[100:103]\n" \
"s_xor_b32 s75, s75, 0x780\n" \
"s_mul_i32 s76, s80, 393216\n" \
"ds_read_b64_tr_b16 a[54:55], v195 offset:15360\n" \
"v_mfma_f32_16x16x32_bf16 v[100:103], a[28:31], a[220:223], v[100:103]\n" \
"s_add_u32 s76, s76, s78\n" \
"ds_read_b128 a[24:27], v205 offset:53248\n" \
"v_mfma_f32_16x16x32_bf16 v[108:111], a[192:195], a[216:219], v[108:111]\n" \
"s_add_u32 m0, s91, 20480\n" \
"ds_read_b128 a[28:31], v206 offset:53248\n" \
"v_mfma_f32_16x16x32_bf16 v[108:111], a[196:199], a[220:223], v[108:111]\n" \
"buffer_load_dwordx4 v197, s[48:51], s76 offen offset:0 lds\n" \
"v_mfma_f32_16x16x32_bf16 v[104:107], a[192:195], a[208:211], v[104:107]\n" \
"v_mfma_f32_16x16x32_bf16 v[104:107], a[196:199], a[212:215], v[104:107]\n" \
"s_waitcnt lgkmcnt(2)\n" \
"v_mfma_f32_16x16x32_bf16 v[32:35], a[32:35], a[0:3], v[32:35]\n" \
"s_add_u32 s77, s76, 24576\n" \
"ds_read_b128 a[192:195], v205 offset:57344\n" \
"v_mfma_f32_16x16x32_bf16 v[32:35], a[36:39], a[4:7], v[32:35]\n" \
"buffer_load_dwordx4 v198, s[48:51], s76 offen offset:1024 lds\n" \
"v_mfma_f32_16x16x32_bf16 v[40:43], a[32:35], a[8:11], v[40:43]\n" \
"ds_read_b128 a[196:199], v206 offset:57344\n" \
"v_mfma_f32_16x16x32_bf16 v[40:43], a[36:39], a[12:15], v[40:43]\n" \
"ds_read_b64_tr_b16 a[200:201], v195 offset:1024\n" \
"v_mfma_f32_16x16x32_bf16 v[0:3], a[40:43], a[8:11], v[0:3]\n" \
"s_xor_b32 s80, s80, 0xf\n" \
"ds_read_b64_tr_b16 a[202:203], v195 offset:4096\n" \
"v_mfma_f32_16x16x32_bf16 v[0:3], a[44:47], a[12:15], v[0:3]\n" \
"buffer_load_dwordx4 v199, s[48:51], s76 offen offset:2048 lds\n" \
"v_mfma_f32_16x16x32_bf16 v[4:7], a[48:51], a[8:11], v[4:7]\n" \
"ds_read_b64_tr_b16 a[204:205], v195 offset:13312\n" \
"v_mfma_f32_16x16x32_bf16 v[4:7], a[52:55], a[12:15], v[4:7]\n" \
"ds_read_b64_tr_b16 a[206:207], v195 offset:16384\n" \
"v_mfma_f32_16x16x32_bf16 v[12:15], a[48:51], a[16:19], v[12:15]\n" \
"s_add_u32 m0, m0, 3072\n" \
"ds_read_b64_tr_b16 a[208:209], v194 offset:2048\n" \
"v_mfma_f32_16x16x32_bf16 v[12:15], a[52:55], a[20:23], v[12:15]\n" \
"buffer_load_dwordx4 v197, s[48:51], s77 offen offset:0 lds\n" \
"v_mfma_f32_16x16x32_bf16 v[8:11], a[40:43], a[16:19], v[8:11]\n" \
"ds_read_b64_tr_b16 a[210:211], v194 offset:5120\n" \
"v_mfma_f32_16x16x32_bf16 v[8:11], a[44:47], a[20:23], v[8:11]\n" \
"ds_read_b64_tr_b16 a[212:213], v194 offset:14336\n" \
"v_mfma_f32_16x16x32_bf16 v[48:51], a[32:35], a[16:19], v[48:51]\n" \
"s_waitcnt lgkmcnt(3)\n" \
"ds_read_b64_tr_b16 a[214:215], v194 offset:17408\n" \
"v_mfma_f32_16x16x32_bf16 v[48:51], a[36:39], a[20:23], v[48:51]\n" \
"buffer_load_dwordx4 v198, s[48:51], s77 offen offset:1024 lds\n" \
"v_mfma_f32_16x16x32_bf16 v[56:59], a[32:35], a[24:27], v[56:59]\n" \
"ds_read_b64_tr_b16 a[216:217], v195 offset:2048\n" \
"v_mfma_f32_16x16x32_bf16 v[56:59], a[36:39], a[28:31], v[56:59]\n" \
"ds_read_b64_tr_b16 a[218:219], v195 offset:5120\n" \
"v_mfma_f32_16x16x32_bf16 v[16:19], a[40:43], a[24:27], v[16:19]\n" \
"ds_read_b64_tr_b16 a[220:221], v195 offset:14336\n" \
"v_mfma_f32_16x16x32_bf16 v[16:19], a[44:47], a[28:31], v[16:19]\n" \
"buffer_load_dwordx4 v199, s[48:51], s77 offen offset:2048 lds\n" \
"v_mfma_f32_16x16x32_bf16 v[20:23], a[48:51], a[24:27], v[20:23]\n" \
"ds_read_b64_tr_b16 a[222:223], v195 offset:17408\n" \
"v_mfma_f32_16x16x32_bf16 v[20:23], a[52:55], a[28:31], v[20:23]\n" \
"v_mfma_f32_16x16x32_bf16 v[28:31], a[48:51], a[192:195], v[28:31]\n" \
"v_add_u32 v254, 3145728, v254\n" \
"v_mfma_f32_16x16x32_bf16 v[28:31], a[52:55], a[196:199], v[28:31]\n" \
"global_load_dword v253, v254, s[22:23]\n" \
"v_mfma_f32_16x16x32_bf16 v[24:27], a[40:43], a[192:195], v[24:27]\n" \
"v_mfma_f32_16x16x32_bf16 v[24:27], a[44:47], a[196:199], v[24:27]\n" \
"v_mfma_f32_16x16x32_bf16 v[64:67], a[32:35], a[192:195], v[64:67]\n" \
"v_mfma_f32_16x16x32_bf16 v[64:67], a[36:39], a[196:199], v[64:67]\n" \
"s_waitcnt vmcnt(12) lgkmcnt(0)\n" \
"s_barrier\n" \
"v_mfma_f32_16x16x32_bf16 v[68:71], a[200:203], a[192:195], v[68:71]\n" \
"ds_read_b64_tr_b16 a[32:33], v209 offset:1024\n" \
"ds_read_b64_tr_b16 a[34:35], v209 offset:4096\n" \
"v_mfma_f32_16x16x32_bf16 v[68:71], a[204:207], a[196:199], v[68:71]\n" \
"ds_read_b64_tr_b16 a[36:37], v209 offset:13312\n" \
"ds_read_b64_tr_b16 a[38:39], v209 offset:16384\n" \
"v_mfma_f32_16x16x32_bf16 v[60:63], a[200:203], a[24:27], v[60:63]\n" \
"s_add_u32 m0, s79, 110592\n" \
"ds_read_b64_tr_b16 a[40:41], v209 offset:0\n" \
"buffer_store_dword v146, v147, s[56:59], s15 offen sc1\n" \
"v_mfma_f32_16x16x32_bf16 v[60:63], a[204:207], a[28:31], v[60:63]\n" \
"buffer_load_dwordx4 v192, s[44:47], s73 offen offset:0 lds\n" \
"v_mfma_f32_16x16x32_bf16 v[52:55], a[200:203], a[16:19], v[52:55]\n" \
"ds_read_b64_tr_b16 a[42:43], v209 offset:3072\n" \
"v_mfma_f32_16x16x32_bf16 v[52:55], a[204:207], a[20:23], v[52:55]\n" \
"ds_read_b64_tr_b16 a[44:45], v209 offset:12288\n" \
"buffer_store_dword v146, v147, s[56:59], s17 offen sc1\n" \
"v_mfma_f32_16x16x32_bf16 v[44:47], a[200:203], a[8:11], v[44:47]\n" \
"s_mov_b64 s[36:37], s[18:19]\n" \
"ds_read_b64_tr_b16 a[46:47], v209 offset:15360\n" \
"v_mfma_f32_16x16x32_bf16 v[44:47], a[204:207], a[12:15], v[44:47]\n" \
"buffer_load_dwordx4 v193, s[44:47], s73 offen offset:1024 lds\n" \
"v_mfma_f32_16x16x32_bf16 v[72:75], a[0:3], a[208:211], v[72:75]\n" \
"s_sub_u32 s36, s36, 4096\n" \
"s_subb_u32 s37, s37, 0\n" \
"ds_read_b64_tr_b16 a[48:49], v210 offset:0\n" \
"v_mfma_f32_16x16x32_bf16 v[72:75], a[4:7], a[212:215], v[72:75]\n" \
"s_and_b32 s37, s37, 0xffff\n" \
"s_mov_b32 s38, 8392704\n" \
"ds_read_b128 a[0:3], v207 offset:0\n" \
"v_mfma_f32_16x16x32_bf16 v[80:83], a[8:11], a[208:211], v[80:83]\n" \
"s_mov_b32 s39, 0x20000\n" \
"s_mov_b64 s[40:41], s[24:25]\n" \
"ds_read_b128 a[4:7], v208 offset:0\n" \
"v_mfma_f32_16x16x32_bf16 v[80:83], a[12:15], a[212:215], v[80:83]\n" \
"buffer_load_dwordx4 v192, s[44:47], s73 offen offset:2048 lds\n" \
"v_mfma_f32_16x16x32_bf16 v[84:87], a[8:11], a[216:219], v[84:87]\n" \
"s_sub_u32 s40, s40, 4096\n" \
"s_subb_u32 s41, s41, 0\n" \
"ds_read_b64_tr_b16 a[50:51], v210 offset:3072\n" \
"v_mfma_f32_16x16x32_bf16 v[84:87], a[12:15], a[220:223], v[84:87]\n" \
"s_and_b32 s41, s41, 0xffff\n" \
"s_mov_b32 s42, 2101248\n" \
"ds_read_b128 a[8:11], v205 offset:0\n" \
"v_mfma_f32_16x16x32_bf16 v[92:95], a[16:19], a[216:219], v[92:95]\n" \
"s_mov_b32 s43, 0x20000\n" \
"s_mov_b64 s[52:53], s[26:27]\n" \
"ds_read_b128 a[12:15], v206 offset:0\n" \
"v_mfma_f32_16x16x32_bf16 v[92:95], a[20:23], a[220:223], v[92:95]\n" \
"buffer_load_dwordx4 v193, s[44:47], s73 offen offset:3072 lds\n" \
"v_mfma_f32_16x16x32_bf16 v[88:91], a[16:19], a[208:211], v[88:91]\n" \
"s_and_b32 s53, s53, 0xffff\n" \
"s_mov_b32 s54, 8388608\n" \
"ds_read_b64_tr_b16 a[52:53], v210 offset:12288\n" \
"v_mfma_f32_16x16x32_bf16 v[88:91], a[20:23], a[212:215], v[88:91]\n" \
"s_add_u32 m0, s89, 110592\n" \
"s_mov_b32 s55, 0x20000\n" \
"ds_read_b128 a[16:19], v205 offset:4096\n" \
"v_mfma_f32_16x16x32_bf16 v[96:99], a[24:27], a[208:211], v[96:99]\n" \
"s_xor_b32 s73, s73, 0x80\n" \
"ds_read_b128 a[20:23], v206 offset:4096\n" \
"v_lshrrev_b32 v221, 2, v234\n" \
"v_mfma_f32_16x16x32_bf16 v[96:99], a[28:31], a[212:215], v[96:99]\n" \
"buffer_load_dwordx4 v196, s[44:47], s75 offen lds\n" \
"v_mfma_f32_16x16x32_bf16 v[100:103], a[24:27], a[216:219], v[100:103]\n" \
"s_xor_b32 s75, s75, 0x80\n" \
"s_mul_i32 s76, s80, 393216\n" \
"ds_read_b64_tr_b16 a[54:55], v210 offset:15360\n" \
"v_mfma_f32_16x16x32_bf16 v[100:103], a[28:31], a[220:223], v[100:103]\n" \
"s_add_u32 s76, s76, s78\n" \
"ds_read_b128 a[24:27], v205 offset:8192\n" \
"v_lshrrev_b32 v244, 4, v234\n" \
"v_mfma_f32_16x16x32_bf16 v[108:111], a[192:195], a[216:219], v[108:111]\n" \
"s_add_u32 m0, s91, 139264\n" \
"s_lshl_b32 s12, s94, 7\n" \
"ds_read_b128 a[28:31], v206 offset:8192\n" \
"v_mfma_f32_16x16x32_bf16 v[108:111], a[196:199], a[220:223], v[108:111]\n" \
"buffer_load_dwordx4 v197, s[48:51], s76 offen offset:0 lds\n" \
"v_mfma_f32_16x16x32_bf16 v[104:107], a[192:195], a[208:211], v[104:107]\n" \
"s_add_u32 s12, s12, 65591296\n" \
"s_and_b32 s70, %1, 1\n" \
"s_lshl3_add_u32 s70, s70, s63\n" \
"v_mfma_f32_16x16x32_bf16 v[104:107], a[196:199], a[212:215], v[104:107]\n" \
"s_waitcnt lgkmcnt(2)\n" \
"s_lshl2_add_u32 s12, s70, s12\n" \
"s_lshr_b32 s68, s63, 1\n" \
"s_add_u32 s68, s68, 2\n" \
"v_mfma_f32_16x16x32_bf16 v[32:35], a[32:35], a[0:3], v[32:35]\n" \
"s_add_u32 s77, s76, 24576\n" \
"s_mov_b32 s13, 65593856\n" \
"ds_read_b128 a[192:195], v205 offset:12288\n" \
"v_mfma_f32_16x16x32_bf16 v[32:35], a[36:39], a[4:7], v[32:35]\n" \
"buffer_load_dwordx4 v198, s[48:51], s76 offen offset:1024 lds\n" \
"v_mfma_f32_16x16x32_bf16 v[40:43], a[32:35], a[8:11], v[40:43]\n" \
"s_lshr_b32 s70, s18, 8\n" \
"ds_read_b128 a[196:199], v206 offset:12288\n" \
"v_lshrrev_b32 v228, 1, v234\n" \
"v_mfma_f32_16x16x32_bf16 v[40:43], a[36:39], a[12:15], v[40:43]\n" \
"s_sub_u32 s68, s68, s70\n" \
"s_and_b32 s68, s68, 3\n" \
"ds_read_b64_tr_b16 a[200:201], v210 offset:1024\n" \
"v_mfma_f32_16x16x32_bf16 v[0:3], a[40:43], a[8:11], v[0:3]\n" \
"s_xor_b32 s80, s80, 0x1\n" \
"s_and_b32 s70, s63, 1\n" \
"ds_read_b64_tr_b16 a[202:203], v210 offset:4096\n" \
"v_mfma_f32_16x16x32_bf16 v[0:3], a[44:47], a[12:15], v[0:3]\n" \
"buffer_load_dwordx4 v199, s[48:51], s76 offen offset:2048 lds\n" \
"v_mfma_f32_16x16x32_bf16 v[4:7], a[48:51], a[8:11], v[4:7]\n" \
"s_lshl_b32 s70, s70, 2\n" \
"s_or_b32 s68, s68, s70\n" \
"ds_read_b64_tr_b16 a[204:205], v210 offset:13312\n" \
"v_mfma_f32_16x16x32_bf16 v[4:7], a[52:55], a[12:15], v[4:7]\n" \
"s_lshr_b32 s84, s61, 1\n" \
"s_lshl_b32 s84, s84, 16\n" \
"ds_read_b64_tr_b16 a[206:207], v210 offset:16384\n" \
"v_mfma_f32_16x16x32_bf16 v[12:15], a[48:51], a[16:19], v[12:15]\n" \
"s_add_u32 m0, m0, 3072\n" \
"s_and_b32 s70, s61, 1\n" \
"ds_read_b64_tr_b16 a[208:209], v209 offset:2048\n" \
"v_mfma_f32_16x16x32_bf16 v[12:15], a[52:55], a[20:23], v[12:15]\n" \
"buffer_load_dwordx4 v197, s[48:51], s77 offen offset:0 lds\n" \
"v_mfma_f32_16x16x32_bf16 v[8:11], a[40:43], a[16:19], v[8:11]\n" \
"s_lshl_b32 s70, s70, 14\n" \
"s_add_u32 s84, s84, s70\n" \
"ds_read_b64_tr_b16 a[210:211], v209 offset:5120\n" \
"v_mfma_f32_16x16x32_bf16 v[8:11], a[44:47], a[20:23], v[8:11]\n" \
"s_lshl_b32 s88, s68, 8\n" \
"s_add_u32 s84, s84, s88\n" \
"ds_read_b64_tr_b16 a[212:213], v209 offset:14336\n" \
"v_mfma_f32_16x16x32_bf16 v[48:51], a[32:35], a[16:19], v[48:51]\n" \
"s_waitcnt lgkmcnt(3)\n" \
"ds_read_b64_tr_b16 a[214:215], v209 offset:17408\n" \
"v_bfe_u32 v245, v244, 2, 3\n" \
"v_mfma_f32_16x16x32_bf16 v[48:51], a[36:39], a[20:23], v[48:51]\n" \
"buffer_load_dwordx4 v198, s[48:51], s77 offen offset:1024 lds\n" \
"v_mfma_f32_16x16x32_bf16 v[56:59], a[32:35], a[24:27], v[56:59]\n" \
"ds_read_b64_tr_b16 a[216:217], v210 offset:2048\n" \
"v_lshlrev_b32 v245, 11, v245\n" \
"v_mfma_f32_16x16x32_bf16 v[56:59], a[36:39], a[28:31], v[56:59]\n" \
"ds_read_b64_tr_b16 a[218:219], v210 offset:5120\n" \
"v_lshrrev_b32 v246, 5, v244\n" \
"v_mfma_f32_16x16x32_bf16 v[16:19], a[40:43], a[24:27], v[16:19]\n" \
"ds_read_b64_tr_b16 a[220:221], v210 offset:14336\n" \
"v_lshl_add_u32 v245, v246, 6, v245\n" \
"v_mfma_f32_16x16x32_bf16 v[16:19], a[44:47], a[28:31], v[16:19]\n" \
"buffer_load_dwordx4 v199, s[48:51], s77 offen offset:2048 lds\n" \
"v_mfma_f32_16x16x32_bf16 v[20:23], a[48:51], a[24:27], v[20:23]\n" \
"ds_read_b64_tr_b16 a[222:223], v210 offset:17408\n" \
"v_and_b32 v246, 3, v244\n" \
"v_mfma_f32_16x16x32_bf16 v[20:23], a[52:55], a[28:31], v[20:23]\n" \
"v_bfe_u32 v247, v244, 4, 1\n" \
"v_lshlrev_b32 v247, 1, v247\n" \
"v_mfma_f32_16x16x32_bf16 v[28:31], a[48:51], a[192:195], v[28:31]\n" \
"v_xor_b32 v246, v246, v247\n" \
"v_lshl_add_u32 v245, v246, 4, v245\n" \
"v_mfma_f32_16x16x32_bf16 v[28:31], a[52:55], a[196:199], v[28:31]\n" \
"buffer_load_dwordx2 v[226:227], v228, s[56:59], s13 offen sc1\n" \
"v_mfma_f32_16x16x32_bf16 v[24:27], a[40:43], a[192:195], v[24:27]\n" \
"v_add_u32 v216, 4096, v245\n" \
"v_add_u32 v217, 3200, v245\n" \
"v_mfma_f32_16x16x32_bf16 v[24:27], a[44:47], a[196:199], v[24:27]\n" \
"v_add_u32 v218, 34816, v245\n" \
"v_add_u32 v219, 33920, v245\n" \
"v_mfma_f32_16x16x32_bf16 v[64:67], a[32:35], a[192:195], v[64:67]\n" \
"s_mov_b32 s86, 65536\n" \
"Lpg8520_%=:\n" \
"Lst1x1_%=:\n" \
"s_cbranch_execz Lpg10845_%=\n" \
"s_lshr_b32 s70, s61, 1\n" \
"s_lshl_b32 s71, s70, 13\n" \
"v_mfma_f32_16x16x32_bf16 v[64:67], a[36:39], a[196:199], v[64:67]\n" \
"s_add_u32 s71, s71, %3\n" \
"s_waitcnt vmcnt(15) lgkmcnt(0)\n" \
"s_barrier\n" \
"v_mfma_f32_16x16x32_bf16 v[68:71], a[200:203], a[192:195], v[68:71]\n" \
"ds_read_b64_tr_b16 a[32:33], v204 offset:21504\n" \
"ds_read_b64_tr_b16 a[34:35], v204 offset:24576\n" \
"v_mfma_f32_16x16x32_bf16 v[68:71], a[204:207], a[196:199], v[68:71]\n" \
"ds_read_b64_tr_b16 a[36:37], v204 offset:33792\n" \
"ds_read_b64_tr_b16 a[38:39], v204 offset:36864\n" \
"v_mfma_f32_16x16x32_bf16 v[60:63], a[200:203], a[24:27], v[60:63]\n" \
"s_add_u32 m0, s79, 65536\n" \
"ds_read_b64_tr_b16 a[40:41], v204 offset:20480\n" \
"v_add_u32 v211, s71, v236\n" \
"v_mfma_f32_16x16x32_bf16 v[60:63], a[204:207], a[28:31], v[60:63]\n" \
"buffer_load_dwordx4 v192, s[44:47], s73 offen offset:0 lds\n" \
"v_mfma_f32_16x16x32_bf16 v[52:55], a[200:203], a[16:19], v[52:55]\n" \
"s_lshl_b32 s70, s70, 18\n" \
"s_add_u32 s88, s88, s70\n" \
"ds_read_b64_tr_b16 a[42:43], v204 offset:23552\n" \
"v_mfma_f32_16x16x32_bf16 v[52:55], a[204:207], a[20:23], v[52:55]\n" \
"s_and_b32 s70, s61, 1\n" \
"s_lshl_b32 s71, s70, 7\n" \
"ds_read_b64_tr_b16 a[44:45], v204 offset:32768\n" \
"v_mfma_f32_16x16x32_bf16 v[44:47], a[200:203], a[8:11], v[44:47]\n" \
"s_add_u32 s88, s88, s71\n" \
"ds_read_b64_tr_b16 a[46:47], v204 offset:35840\n" \
"v_and_b32 v245, 3, v244\n" \
"v_mfma_f32_16x16x32_bf16 v[44:47], a[204:207], a[12:15], v[44:47]\n" \
"buffer_load_dwordx4 v193, s[44:47], s73 offen offset:1024 lds\n" \
"v_mfma_f32_16x16x32_bf16 v[72:75], a[0:3], a[208:211], v[72:75]\n" \
"ds_read_b64_tr_b16 a[48:49], v222 offset:20480\n" \
"v_lshlrev_b32 v245, 3, v245\n" \
"v_mfma_f32_16x16x32_bf16 v[72:75], a[4:7], a[212:215], v[72:75]\n" \
"ds_read_b128 a[0:3], v202 offset:0\n" \
"v_bfe_u32 v246, v244, 2, 2\n" \
"v_mfma_f32_16x16x32_bf16 v[80:83], a[8:11], a[208:211], v[80:83]\n" \
"ds_read_b128 a[4:7], v203 offset:0\n" \
"v_lshl_add_u32 v245, v246, 6, v245\n" \
"v_mfma_f32_16x16x32_bf16 v[80:83], a[12:15], a[212:215], v[80:83]\n" \
"buffer_load_dwordx4 v192, s[44:47], s73 offen offset:2048 lds\n" \
"v_mfma_f32_16x16x32_bf16 v[84:87], a[8:11], a[216:219], v[84:87]\n" \
"ds_read_b64_tr_b16 a[50:51], v222 offset:23552\n" \
"v_bfe_u32 v246, v244, 4, 1\n" \
"v_mfma_f32_16x16x32_bf16 v[84:87], a[12:15], a[220:223], v[84:87]\n" \
"ds_read_b128 a[8:11], v200 offset:0\n" \
"v_lshl_add_u32 v245, v246, 5, v245\n" \
"v_mfma_f32_16x16x32_bf16 v[92:95], a[16:19], a[216:219], v[92:95]\n" \
"ds_read_b128 a[12:15], v201 offset:0\n" \
"v_lshl_add_u32 v245, v246, 8, v245\n" \
"v_mfma_f32_16x16x32_bf16 v[92:95], a[20:23], a[220:223], v[92:95]\n" \
"buffer_load_dwordx4 v193, s[44:47], s73 offen offset:3072 lds\n" \
"v_mfma_f32_16x16x32_bf16 v[88:91], a[16:19], a[208:211], v[88:91]\n" \
"ds_read_b64_tr_b16 a[52:53], v222 offset:32768\n" \
"v_lshrrev_b32 v246, 5, v244\n" \
"v_mfma_f32_16x16x32_bf16 v[88:91], a[20:23], a[212:215], v[88:91]\n" \
"s_add_u32 m0, s89, 65536\n" \
"ds_read_b128 a[16:19], v200 offset:4096\n" \
"v_lshl_add_u32 v245, v246, 12, v245\n" \
"v_mfma_f32_16x16x32_bf16 v[96:99], a[24:27], a[208:211], v[96:99]\n" \
"s_xor_b32 s73, s73, 0x180\n" \
"s_lshl_b32 s71, s70, 10\n" \
"ds_read_b128 a[20:23], v201 offset:4096\n" \
"v_mfma_f32_16x16x32_bf16 v[96:99], a[28:31], a[212:215], v[96:99]\n" \
"buffer_load_dwordx4 v196, s[44:47], s75 offen lds\n" \
"v_mfma_f32_16x16x32_bf16 v[100:103], a[24:27], a[216:219], v[100:103]\n" \
"s_xor_b32 s75, s75, 0x180\n" \
"s_mul_i32 s76, s80, 393216\n" \
"ds_read_b64_tr_b16 a[54:55], v222 offset:35840\n" \
"v_mfma_f32_16x16x32_bf16 v[100:103], a[28:31], a[220:223], v[100:103]\n" \
"s_add_u32 s76, s76, s78\n" \
"s_add_u32 s71, s71, %3\n" \
"ds_read_b128 a[24:27], v200 offset:8192\n" \
"v_mfma_f32_16x16x32_bf16 v[108:111], a[192:195], a[216:219], v[108:111]\n" \
"s_add_u32 m0, s91, 86016\n" \
"ds_read_b128 a[28:31], v201 offset:8192\n" \
"v_add_u32 v212, s71, v245\n" \
"v_mfma_f32_16x16x32_bf16 v[108:111], a[196:199], a[220:223], v[108:111]\n" \
"buffer_load_dwordx4 v197, s[48:51], s76 offen offset:0 lds\n" \
"v_mfma_f32_16x16x32_bf16 v[104:107], a[192:195], a[208:211], v[104:107]\n" \
"s_mov_b32 s69, 1\n" \
"s_mov_b32 s30, 0x0\n" \
"s_mov_b32 s85, 98304\n" \
"v_mfma_f32_16x16x32_bf16 v[104:107], a[196:199], a[212:215], v[104:107]\n" \
"s_waitcnt lgkmcnt(2)\n" \
"s_sub_u32 s85, s85, 32768\n" \
"s_cselect_b32 s85, 131072, s85\n" \
"v_add_u32 v213, s85, v211\n" \
"v_mfma_f32_16x16x32_bf16 v[32:35], a[32:35], a[0:3], v[32:35]\n" \
"s_add_u32 s77, s76, 24576\n" \
"ds_read_b128 a[192:195], v200 offset:12288\n" \
"v_add_u32 v214, s85, v212\n" \
"v_mfma_f32_16x16x32_bf16 v[32:35], a[36:39], a[4:7], v[32:35]\n" \
"buffer_load_dwordx4 v198, s[48:51], s76 offen offset:1024 lds\n" \
"v_mfma_f32_16x16x32_bf16 v[40:43], a[32:35], a[8:11], v[40:43]\n" \
"ds_read_b128 a[196:199], v201 offset:12288\n" \
"v_xor_b32 v215, 32, v214\n" \
"v_mfma_f32_16x16x32_bf16 v[40:43], a[36:39], a[12:15], v[40:43]\n" \
"ds_read_b64_tr_b16 a[200:201], v222 offset:21504\n" \
"v_mov_b32 v230, s60\n" \
"v_mfma_f32_16x16x32_bf16 v[0:3], a[40:43], a[8:11], v[0:3]\n" \
"s_xor_b32 s80, s80, 0x3\n" \
"ds_read_b64_tr_b16 a[202:203], v222 offset:24576\n" \
"v_lshlrev_b32 v231, 28, s61\n" \
"v_mfma_f32_16x16x32_bf16 v[0:3], a[44:47], a[12:15], v[0:3]\n" \
"buffer_load_dwordx4 v199, s[48:51], s76 offen offset:2048 lds\n" \
"v_mfma_f32_16x16x32_bf16 v[4:7], a[48:51], a[8:11], v[4:7]\n" \
"ds_read_b64_tr_b16 a[204:205], v222 offset:33792\n" \
"v_mfma_f32_16x16x32_bf16 v[4:7], a[52:55], a[12:15], v[4:7]\n" \
"ds_read_b64_tr_b16 a[206:207], v222 offset:36864\n" \
"v_mfma_f32_16x16x32_bf16 v[12:15], a[48:51], a[16:19], v[12:15]\n" \
"s_add_u32 m0, m0, 3072\n" \
"ds_read_b64_tr_b16 a[208:209], v204 offset:22528\n" \
"v_mfma_f32_16x16x32_bf16 v[12:15], a[52:55], a[20:23], v[12:15]\n" \
"buffer_load_dwordx4 v197, s[48:51], s77 offen offset:0 lds\n" \
"v_mfma_f32_16x16x32_bf16 v[8:11], a[40:43], a[16:19], v[8:11]\n" \
"ds_read_b64_tr_b16 a[210:211], v204 offset:25600\n" \
"v_mfma_f32_16x16x32_bf16 v[8:11], a[44:47], a[20:23], v[8:11]\n" \
"ds_read_b64_tr_b16 a[212:213], v204 offset:34816\n" \
"v_mfma_f32_16x16x32_bf16 v[48:51], a[32:35], a[16:19], v[48:51]\n" \
"s_waitcnt lgkmcnt(3)\n" \
"ds_read_b64_tr_b16 a[214:215], v204 offset:37888\n" \
"v_mfma_f32_16x16x32_bf16 v[48:51], a[36:39], a[20:23], v[48:51]\n" \
"buffer_load_dwordx4 v198, s[48:51], s77 offen offset:1024 lds\n" \
"v_mfma_f32_16x16x32_bf16 v[56:59], a[32:35], a[24:27], v[56:59]\n" \
"ds_read_b64_tr_b16 a[216:217], v222 offset:22528\n" \
"v_mfma_f32_16x16x32_bf16 v[56:59], a[36:39], a[28:31], v[56:59]\n" \
"ds_read_b64_tr_b16 a[218:219], v222 offset:25600\n" \
"v_mfma_f32_16x16x32_bf16 v[16:19], a[40:43], a[24:27], v[16:19]\n" \
"ds_read_b64_tr_b16 a[220:221], v222 offset:34816\n" \
"v_mfma_f32_16x16x32_bf16 v[16:19], a[44:47], a[28:31], v[16:19]\n" \
"buffer_load_dwordx4 v199, s[48:51], s77 offen offset:2048 lds\n" \
"v_mfma_f32_16x16x32_bf16 v[20:23], a[48:51], a[24:27], v[20:23]\n" \
"ds_read_b64_tr_b16 a[222:223], v222 offset:37888\n" \
"v_mfma_f32_16x16x32_bf16 v[20:23], a[52:55], a[28:31], v[20:23]\n" \
"v_mfma_f32_16x16x32_bf16 v[28:31], a[48:51], a[192:195], v[28:31]\n" \
"v_mfma_f32_16x16x32_bf16 v[28:31], a[52:55], a[196:199], v[28:31]\n" \
"v_mfma_f32_16x16x32_bf16 v[24:27], a[40:43], a[192:195], v[24:27]\n" \
"v_mfma_f32_16x16x32_bf16 v[24:27], a[44:47], a[196:199], v[24:27]\n" \
"v_mfma_f32_16x16x32_bf16 v[64:67], a[32:35], a[192:195], v[64:67]\n" \
"v_mfma_f32_16x16x32_bf16 v[64:67], a[36:39], a[196:199], v[64:67]\n" \
"s_waitcnt vmcnt(12) lgkmcnt(0)\n" \
"s_barrier\n" \
"v_mfma_f32_16x16x32_bf16 v[68:71], a[200:203], a[192:195], v[68:71]\n" \
"ds_read_b64_tr_b16 a[32:33], v194 offset:1024\n" \
"ds_read_b64_tr_b16 a[34:35], v194 offset:4096\n" \
"v_mfma_f32_16x16x32_bf16 v[68:71], a[204:207], a[196:199], v[68:71]\n" \
"ds_read_b64_tr_b16 a[36:37], v194 offset:13312\n" \
"ds_read_b64_tr_b16 a[38:39], v194 offset:16384\n" \
"v_mfma_f32_16x16x32_bf16 v[60:63], a[200:203], a[24:27], v[60:63]\n" \
"s_add_u32 m0, s79, 0\n" \
"ds_read_b64_tr_b16 a[40:41], v194 offset:0\n" \
"v_mfma_f32_16x16x32_bf16 v[60:63], a[204:207], a[28:31], v[60:63]\n" \
"buffer_load_dwordx4 v192, s[44:47], s73 offen offset:0 lds\n" \
"v_mfma_f32_16x16x32_bf16 v[52:55], a[200:203], a[16:19], v[52:55]\n" \
"ds_read_b64_tr_b16 a[42:43], v194 offset:3072\n" \
"v_mfma_f32_16x16x32_bf16 v[52:55], a[204:207], a[20:23], v[52:55]\n" \
"ds_read_b64_tr_b16 a[44:45], v194 offset:12288\n" \
"v_mfma_f32_16x16x32_bf16 v[44:47], a[200:203], a[8:11], v[44:47]\n" \
"ds_read_b64_tr_b16 a[46:47], v194 offset:15360\n" \
"v_mfma_f32_16x16x32_bf16 v[44:47], a[204:207], a[12:15], v[44:47]\n" \
"buffer_load_dwordx4 v193, s[44:47], s73 offen offset:1024 lds\n" \
"v_mfma_f32_16x16x32_bf16 v[72:75], a[0:3], a[208:211], v[72:75]\n" \
"ds_read_b64_tr_b16 a[48:49], v195 offset:0\n" \
"v_mfma_f32_16x16x32_bf16 v[72:75], a[4:7], a[212:215], v[72:75]\n" \
"ds_read_b128 a[0:3], v207 offset:45056\n" \
"v_mfma_f32_16x16x32_bf16 v[80:83], a[8:11], a[208:211], v[80:83]\n" \
"ds_read_b128 a[4:7], v208 offset:45056\n" \
"v_mfma_f32_16x16x32_bf16 v[80:83], a[12:15], a[212:215], v[80:83]\n" \
"buffer_load_dwordx4 v192, s[44:47], s73 offen offset:2048 lds\n" \
"v_mfma_f32_16x16x32_bf16 v[84:87], a[8:11], a[216:219], v[84:87]\n" \
"ds_read_b64_tr_b16 a[50:51], v195 offset:3072\n" \
"v_mfma_f32_16x16x32_bf16 v[84:87], a[12:15], a[220:223], v[84:87]\n" \
"ds_read_b128 a[8:11], v205 offset:45056\n" \
"v_mfma_f32_16x16x32_bf16 v[92:95], a[16:19], a[216:219], v[92:95]\n" \
"ds_read_b128 a[12:15], v206 offset:45056\n" \
"v_mfma_f32_16x16x32_bf16 v[92:95], a[20:23], a[220:223], v[92:95]\n" \
"buffer_load_dwordx4 v193, s[44:47], s73 offen offset:3072 lds\n" \
"v_mfma_f32_16x16x32_bf16 v[88:91], a[16:19], a[208:211], v[88:91]\n" \
"ds_read_b64_tr_b16 a[52:53], v195 offset:12288\n" \
"v_mfma_f32_16x16x32_bf16 v[88:91], a[20:23], a[212:215], v[88:91]\n" \
"s_add_u32 m0, s89, 0\n" \
"ds_read_b128 a[16:19], v205 offset:49152\n" \
"v_mfma_f32_16x16x32_bf16 v[96:99], a[24:27], a[208:211], v[96:99]\n" \
"s_xor_b32 s73, s73, 0x300\n" \
"ds_read_b128 a[20:23], v206 offset:49152\n" \
"v_mfma_f32_16x16x32_bf16 v[96:99], a[28:31], a[212:215], v[96:99]\n" \
"buffer_load_dwordx4 v196, s[44:47], s75 offen lds\n" \
"v_mfma_f32_16x16x32_bf16 v[100:103], a[24:27], a[216:219], v[100:103]\n" \
"s_xor_b32 s75, s75, 0x300\n" \
"s_mul_i32 s76, s80, 393216\n" \
"ds_read_b64_tr_b16 a[54:55], v195 offset:15360\n" \
"v_mfma_f32_16x16x32_bf16 v[100:103], a[28:31], a[220:223], v[100:103]\n" \
"s_add_u32 s76, s76, s78\n" \
"ds_read_b128 a[24:27], v205 offset:53248\n" \
"v_mfma_f32_16x16x32_bf16 v[108:111], a[192:195], a[216:219], v[108:111]\n" \
"s_add_u32 m0, s91, 20480\n" \
"ds_read_b128 a[28:31], v206 offset:53248\n" \
"v_mfma_f32_16x16x32_bf16 v[108:111], a[196:199], a[220:223], v[108:111]\n" \
"buffer_load_dwordx4 v197, s[48:51], s76 offen offset:0 lds\n" \
"v_mfma_f32_16x16x32_bf16 v[104:107], a[192:195], a[208:211], v[104:107]\n" \
"v_mfma_f32_16x16x32_bf16 v[104:107], a[196:199], a[212:215], v[104:107]\n" \
"s_waitcnt lgkmcnt(2)\n" \
"v_mfma_f32_16x16x32_bf16 v[32:35], a[32:35], a[0:3], v[32:35]\n" \
"s_add_u32 s77, s76, 24576\n" \
"ds_read_b128 a[192:195], v205 offset:57344\n" \
"v_mfma_f32_16x16x32_bf16 v[32:35], a[36:39], a[4:7], v[32:35]\n" \
"buffer_load_dwordx4 v198, s[48:51], s76 offen offset:1024 lds\n" \
"v_mfma_f32_16x16x32_bf16 v[40:43], a[32:35], a[8:11], v[40:43]\n" \
"ds_read_b128 a[196:199], v206 offset:57344\n" \
"v_mfma_f32_16x16x32_bf16 v[40:43], a[36:39], a[12:15], v[40:43]\n" \
"ds_read_b64_tr_b16 a[200:201], v195 offset:1024\n" \
"v_mfma_f32_16x16x32_bf16 v[0:3], a[40:43], a[8:11], v[0:3]\n" \
"s_xor_b32 s80, s80, 0x6\n" \
"ds_read_b64_tr_b16 a[202:203], v195 offset:4096\n" \
"v_mfma_f32_16x16x32_bf16 v[0:3], a[44:47], a[12:15], v[0:3]\n" \
"buffer_load_dwordx4 v199, s[48:51], s76 offen offset:2048 lds\n" \
"v_mfma_f32_16x16x32_bf16 v[4:7], a[48:51], a[8:11], v[4:7]\n" \
"ds_read_b64_tr_b16 a[204:205], v195 offset:13312\n" \
"v_mfma_f32_16x16x32_bf16 v[4:7], a[52:55], a[12:15], v[4:7]\n" \
"ds_read_b64_tr_b16 a[206:207], v195 offset:16384\n" \
"v_mfma_f32_16x16x32_bf16 v[12:15], a[48:51], a[16:19], v[12:15]\n" \
"s_add_u32 m0, m0, 3072\n" \
"ds_read_b64_tr_b16 a[208:209], v194 offset:2048\n" \
"v_mfma_f32_16x16x32_bf16 v[12:15], a[52:55], a[20:23], v[12:15]\n" \
"buffer_load_dwordx4 v197, s[48:51], s77 offen offset:0 lds\n" \
"v_mfma_f32_16x16x32_bf16 v[8:11], a[40:43], a[16:19], v[8:11]\n" \
"ds_read_b64_tr_b16 a[210:211], v194 offset:5120\n" \
"v_mfma_f32_16x16x32_bf16 v[8:11], a[44:47], a[20:23], v[8:11]\n" \
"ds_read_b64_tr_b16 a[212:213], v194 offset:14336\n" \
"v_mfma_f32_16x16x32_bf16 v[48:51], a[32:35], a[16:19], v[48:51]\n" \
"s_waitcnt lgkmcnt(3)\n" \
"ds_read_b64_tr_b16 a[214:215], v194 offset:17408\n" \
"v_mfma_f32_16x16x32_bf16 v[48:51], a[36:39], a[20:23], v[48:51]\n" \
"buffer_load_dwordx4 v198, s[48:51], s77 offen offset:1024 lds\n" \
"v_mfma_f32_16x16x32_bf16 v[56:59], a[32:35], a[24:27], v[56:59]\n" \
"ds_read_b64_tr_b16 a[216:217], v195 offset:2048\n" \
"v_mfma_f32_16x16x32_bf16 v[56:59], a[36:39], a[28:31], v[56:59]\n" \
"ds_read_b64_tr_b16 a[218:219], v195 offset:5120\n" \
"v_mfma_f32_16x16x32_bf16 v[16:19], a[40:43], a[24:27], v[16:19]\n" \
"ds_read_b64_tr_b16 a[220:221], v195 offset:14336\n" \
"v_mfma_f32_16x16x32_bf16 v[16:19], a[44:47], a[28:31], v[16:19]\n" \
"buffer_load_dwordx4 v199, s[48:51], s77 offen offset:2048 lds\n" \
"v_mfma_f32_16x16x32_bf16 v[20:23], a[48:51], a[24:27], v[20:23]\n" \
"ds_read_b64_tr_b16 a[222:223], v195 offset:17408\n" \
"v_mfma_f32_16x16x32_bf16 v[20:23], a[52:55], a[28:31], v[20:23]\n" \
"v_mfma_f32_16x16x32_bf16 v[28:31], a[48:51], a[192:195], v[28:31]\n" \
"v_mfma_f32_16x16x32_bf16 v[28:31], a[52:55], a[196:199], v[28:31]\n" \
"v_mfma_f32_16x16x32_bf16 v[24:27], a[40:43], a[192:195], v[24:27]\n" \
"v_mfma_f32_16x16x32_bf16 v[24:27], a[44:47], a[196:199], v[24:27]\n" \
"v_mfma_f32_16x16x32_bf16 v[64:67], a[32:35], a[192:195], v[64:67]\n" \
"v_mfma_f32_16x16x32_bf16 v[64:67], a[36:39], a[196:199], v[64:67]\n" \
"s_waitcnt vmcnt(11) lgkmcnt(0)\n" \
"s_barrier\n" \
"v_mfma_f32_16x16x32_bf16 v[68:71], a[200:203], a[192:195], v[68:71]\n" \
"ds_read_b64_tr_b16 a[32:33], v209 offset:1024\n" \
"ds_read_b64_tr_b16 a[34:35], v209 offset:4096\n" \
"v_mfma_f32_16x16x32_bf16 v[68:71], a[204:207], a[196:199], v[68:71]\n" \
"ds_read_b64_tr_b16 a[36:37], v209 offset:13312\n" \
"ds_read_b64_tr_b16 a[38:39], v209 offset:16384\n" \
"v_mfma_f32_16x16x32_bf16 v[60:63], a[200:203], a[24:27], v[60:63]\n" \
"s_add_u32 m0, s79, 110592\n" \
"ds_read_b64_tr_b16 a[40:41], v209 offset:0\n" \
"v_cmp_eq_u32 vcc, s60, v226\n" \
"v_mfma_f32_16x16x32_bf16 v[60:63], a[204:207], a[28:31], v[60:63]\n" \
"buffer_load_dwordx4 v192, s[44:47], s73 offen offset:0 lds\n" \
"v_mfma_f32_16x16x32_bf16 v[52:55], a[200:203], a[16:19], v[52:55]\n" \
"s_andn2_b32 s70, 0xff, vcc_lo\n" \
"s_cbranch_scc1 Lpl4_%=\n" \
"Lpr4_%=:\n" \
"ds_read_b64_tr_b16 a[42:43], v209 offset:3072\n" \
"v_mfma_f32_16x16x32_bf16 v[52:55], a[204:207], a[20:23], v[52:55]\n" \
"s_and_b32 s70, %1, 7\n" \
"ds_read_b64_tr_b16 a[44:45], v209 offset:12288\n" \
"v_readlane_b32 s70, v227, s70\n" \
"v_mfma_f32_16x16x32_bf16 v[44:47], a[200:203], a[8:11], v[44:47]\n" \
"ds_read_b64_tr_b16 a[46:47], v209 offset:15360\n" \
"v_mfma_f32_16x16x32_bf16 v[44:47], a[204:207], a[12:15], v[44:47]\n" \
"buffer_load_dwordx4 v193, s[44:47], s73 offen offset:1024 lds\n" \
"v_mfma_f32_16x16x32_bf16 v[72:75], a[0:3], a[208:211], v[72:75]\n" \
"ds_read_b64_tr_b16 a[48:49], v210 offset:0\n" \
"v_subrev_u32 v227, s70, v227\n" \
"v_mfma_f32_16x16x32_bf16 v[72:75], a[4:7], a[212:215], v[72:75]\n" \
"ds_read_b128 a[0:3], v207 offset:0\n" \
"v_cmp_gt_i32 vcc, 0, v227\n" \
"v_mfma_f32_16x16x32_bf16 v[80:83], a[8:11], a[208:211], v[80:83]\n" \
"ds_read_b128 a[4:7], v208 offset:0\n" \
"v_mfma_f32_16x16x32_bf16 v[80:83], a[12:15], a[212:215], v[80:83]\n" \
"buffer_load_dwordx4 v192, s[44:47], s73 offen offset:2048 lds\n" \
"v_mfma_f32_16x16x32_bf16 v[84:87], a[8:11], a[216:219], v[84:87]\n" \
"s_and_b32 s13, vcc_lo, 0xff\n" \
"s_bcnt1_i32_b32 s13, s13\n" \
"ds_read_b64_tr_b16 a[50:51], v210 offset:3072\n" \
"v_mfma_f32_16x16x32_bf16 v[84:87], a[12:15], a[220:223], v[84:87]\n" \
"s_lshr_b32 s90, s13, 2\n" \
"s_mul_i32 s70, s90, 7\n" \
"ds_read_b128 a[8:11], v205 offset:0\n" \
"v_mfma_f32_16x16x32_bf16 v[92:95], a[16:19], a[216:219], v[92:95]\n" \
"s_xor_b32 s13, s13, s70\n" \
"s_lshl_b32 s70, s13, 3\n" \
"ds_read_b128 a[12:15], v206 offset:0\n" \
"v_mfma_f32_16x16x32_bf16 v[92:95], a[20:23], a[220:223], v[92:95]\n" \
"buffer_load_dwordx4 v193, s[44:47], s73 offen offset:3072 lds\n" \
"v_mfma_f32_16x16x32_bf16 v[88:91], a[16:19], a[208:211], v[88:91]\n" \
"s_lshr_b32 s9, 0x2a2b2d2f, s70\n" \
"s_and_b32 s9, s9, 0xff\n" \
"ds_read_b64_tr_b16 a[52:53], v210 offset:12288\n" \
"v_mfma_f32_16x16x32_bf16 v[88:91], a[20:23], a[212:215], v[88:91]\n" \
"s_add_u32 m0, s89, 110592\n" \
"s_lshr_b32 s8, %1, 6\n" \
"ds_read_b128 a[16:19], v205 offset:4096\n" \
"v_mfma_f32_16x16x32_bf16 v[96:99], a[24:27], a[208:211], v[96:99]\n" \
"s_xor_b32 s73, s73, 0x80\n" \
"s_lshl2_add_u32 s8, s13, s8\n" \
"ds_read_b128 a[20:23], v206 offset:4096\n" \
"v_mfma_f32_16x16x32_bf16 v[96:99], a[28:31], a[212:215], v[96:99]\n" \
"buffer_load_dwordx4 v196, s[44:47], s75 offen lds\n" \
"v_mfma_f32_16x16x32_bf16 v[100:103], a[24:27], a[216:219], v[100:103]\n" \
"s_xor_b32 s75, s75, 0x80\n" \
"s_mul_i32 s76, s80, 393216\n" \
"ds_read_b64_tr_b16 a[54:55], v210 offset:15360\n" \
"v_mfma_f32_16x16x32_bf16 v[100:103], a[28:31], a[220:223], v[100:103]\n" \
"s_add_u32 s76, s76, s78\n" \
"ds_read_b128 a[24:27], v205 offset:8192\n" \
"v_max_i32_dpp v226, v227, v227 quad_perm:[1,0,3,2] row_mask:0xf bank_mask:0xf\n" \
"v_mfma_f32_16x16x32_bf16 v[108:111], a[192:195], a[216:219], v[108:111]\n" \
"s_add_u32 m0, s91, 139264\n" \
"s_mov_b32 s14, 0\n" \
"ds_read_b128 a[28:31], v206 offset:8192\n" \
"v_mfma_f32_16x16x32_bf16 v[108:111], a[196:199], a[220:223], v[108:111]\n" \
"buffer_load_dwordx4 v197, s[48:51], s76 offen offset:0 lds\n" \
"v_mfma_f32_16x16x32_bf16 v[104:107], a[192:195], a[208:211], v[104:107]\n" \
"v_readlane_b32 s15, v226, 2\n" \
"v_readlane_b32 s16, v226, 4\n" \
"v_mfma_f32_16x16x32_bf16 v[104:107], a[196:199], a[212:215], v[104:107]\n" \
"s_waitcnt lgkmcnt(2)\n" \
"v_readlane_b32 s17, v226, 6\n" \
"v_cmp_gt_i32 vcc, s15, v226\n" \
"v_mfma_f32_16x16x32_bf16 v[32:35], a[32:35], a[0:3], v[32:35]\n" \
"s_add_u32 s77, s76, 24576\n" \
"ds_read_b128 a[192:195], v205 offset:12288\n" \
"v_mfma_f32_16x16x32_bf16 v[32:35], a[36:39], a[4:7], v[32:35]\n" \
"buffer_load_dwordx4 v198, s[48:51], s76 offen offset:1024 lds\n" \
"v_mfma_f32_16x16x32_bf16 v[40:43], a[32:35], a[8:11], v[40:43]\n" \
"s_and_b32 s70, vcc_lo, 0x55\n" \
"s_bcnt1_i32_b32 s70, s70\n" \
"ds_read_b128 a[196:199], v206 offset:12288\n" \
"v_mfma_f32_16x16x32_bf16 v[40:43], a[36:39], a[12:15], v[40:43]\n" \
"s_cmp_eq_u32 s70, s13\n" \
"s_cselect_b32 s14, 4, s14\n" \
"ds_read_b64_tr_b16 a[200:201], v210 offset:1024\n" \
"v_mfma_f32_16x16x32_bf16 v[0:3], a[40:43], a[8:11], v[0:3]\n" \
"s_xor_b32 s80, s80, 0x1\n" \
"ds_read_b64_tr_b16 a[202:203], v210 offset:4096\n" \
"v_cmp_gt_i32 vcc, s16, v226\n" \
"v_mfma_f32_16x16x32_bf16 v[0:3], a[44:47], a[12:15], v[0:3]\n" \
"buffer_load_dwordx4 v199, s[48:51], s76 offen offset:2048 lds\n" \
"v_mfma_f32_16x16x32_bf16 v[4:7], a[48:51], a[8:11], v[4:7]\n" \
"s_and_b32 s70, vcc_lo, 0x55\n" \
"s_bcnt1_i32_b32 s70, s70\n" \
"ds_read_b64_tr_b16 a[204:205], v210 offset:13312\n" \
"v_mfma_f32_16x16x32_bf16 v[4:7], a[52:55], a[12:15], v[4:7]\n" \
"s_cmp_eq_u32 s70, s13\n" \
"s_cselect_b32 s14, 8, s14\n" \
"ds_read_b64_tr_b16 a[206:207], v210 offset:16384\n" \
"v_mfma_f32_16x16x32_bf16 v[12:15], a[48:51], a[16:19], v[12:15]\n" \
"s_add_u32 m0, m0, 3072\n" \
"ds_read_b64_tr_b16 a[208:209], v209 offset:2048\n" \
"v_cmp_gt_i32 vcc, s17, v226\n" \
"v_mfma_f32_16x16x32_bf16 v[12:15], a[52:55], a[20:23], v[12:15]\n" \
"buffer_load_dwordx4 v197, s[48:51], s77 offen offset:0 lds\n" \
"v_mfma_f32_16x16x32_bf16 v[8:11], a[40:43], a[16:19], v[8:11]\n" \
"s_and_b32 s70, vcc_lo, 0x55\n" \
"s_bcnt1_i32_b32 s70, s70\n" \
"ds_read_b64_tr_b16 a[210:211], v209 offset:5120\n" \
"v_mfma_f32_16x16x32_bf16 v[8:11], a[44:47], a[20:23], v[8:11]\n" \
"s_cmp_eq_u32 s70, s13\n" \
"s_cselect_b32 s14, 12, s14\n" \
"ds_read_b64_tr_b16 a[212:213], v209 offset:14336\n" \
"v_mfma_f32_16x16x32_bf16 v[48:51], a[32:35], a[16:19], v[48:51]\n" \
"s_lshr_b32 s70, %1, 6\n" \
"s_add_u32 s14, s14, s70\n" \
"ds_read_b64_tr_b16 a[214:215], v209 offset:17408\n" \
"v_mfma_f32_16x16x32_bf16 v[48:51], a[36:39], a[20:23], v[48:51]\n" \
"s_waitcnt lgkmcnt(3)\n" \
"buffer_load_dwordx4 v198, s[48:51], s77 offen offset:1024 lds\n" \
"v_mfma_f32_16x16x32_bf16 v[56:59], a[32:35], a[24:27], v[56:59]\n" \
"s_cmp_eq_u32 s90, 0\n" \
"s_cselect_b32 s14, s8, s14\n" \
"ds_read_b64_tr_b16 a[216:217], v210 offset:2048\n" \
"v_mfma_f32_16x16x32_bf16 v[56:59], a[36:39], a[28:31], v[56:59]\n" \
"s_sub_u32 s10, 49, s9\n" \
"s_lshl_b32 s11, s9, 1\n" \
"ds_read_b64_tr_b16 a[218:219], v210 offset:5120\n" \
"v_mfma_f32_16x16x32_bf16 v[16:19], a[40:43], a[24:27], v[16:19]\n" \
"s_sub_u32 s11, s11, 71\n" \
"s_lshl2_add_u32 s70, s8, s61\n" \
"ds_read_b64_tr_b16 a[220:221], v210 offset:14336\n" \
"v_mfma_f32_16x16x32_bf16 v[16:19], a[44:47], a[28:31], v[16:19]\n" \
"buffer_load_dwordx4 v199, s[48:51], s77 offen offset:2048 lds\n" \
"v_mfma_f32_16x16x32_bf16 v[20:23], a[48:51], a[24:27], v[20:23]\n" \
"s_mul_i32 s81, s70, 266240\n" \
"s_mul_i32 s82, s90, s9\n" \
"ds_read_b64_tr_b16 a[222:223], v210 offset:17408\n" \
"v_mfma_f32_16x16x32_bf16 v[20:23], a[52:55], a[28:31], v[20:23]\n" \
"s_lshl_b32 s71, s82, 12\n" \
"s_add_u32 s81, s81, s71\n" \
"s_add_u32 s81, s81, 35700736\n" \
"v_mfma_f32_16x16x32_bf16 v[28:31], a[48:51], a[192:195], v[28:31]\n" \
"s_lshl2_add_u32 s70, s14, s61\n" \
"s_mul_i32 s83, s70, 69632\n" \
"s_add_u32 s83, s83, 52740096\n" \
"v_mfma_f32_16x16x32_bf16 v[28:31], a[52:55], a[196:199], v[28:31]\n" \
"s_lshl_b32 s82, s82, 17\n" \
"s_add_u32 s82, s82, s84\n" \
"s_lshl_b32 s70, s14, 19\n" \
"v_mfma_f32_16x16x32_bf16 v[24:27], a[40:43], a[192:195], v[24:27]\n" \
"s_add_u32 s88, s88, s70\n" \
"s_lshl_b32 s71, s90, 11\n" \
"s_add_u32 s88, s88, s71\n" \
"v_mfma_f32_16x16x32_bf16 v[24:27], a[44:47], a[196:199], v[24:27]\n" \
"s_lshl3_add_u32 s70, s8, s68\n" \
"s_lshl2_add_u32 s87, s70, s61\n" \
"s_lshl_b32 s87, s87, 14\n" \
"v_mfma_f32_16x16x32_bf16 v[64:67], a[32:35], a[192:195], v[64:67]\n" \
"s_add_u32 s87, s87, 57196544\n" \
"s_lshl_b32 s64, s70, 7\n" \
"s_add_u32 s64, s64, 65601536\n" \
"v_mfma_f32_16x16x32_bf16 v[64:67], a[36:39], a[196:199], v[64:67]\n" \
"s_lshl_b32 s66, s8, 7\n" \
"s_waitcnt vmcnt(11) lgkmcnt(0)\n" \
"s_barrier\n" \
"v_mfma_f32_16x16x32_bf16 v[68:71], a[200:203], a[192:195], v[68:71]\n" \
"s_lshl_b32 s67, s14, 7\n" \
"ds_read_b64_tr_b16 a[32:33], v204 offset:21504\n" \
"ds_read_b64_tr_b16 a[34:35], v204 offset:24576\n" \
"v_mfma_f32_16x16x32_bf16 v[68:71], a[204:207], a[196:199], v[68:71]\n" \
"s_add_u32 s67, s67, 65591296\n" \
"ds_read_b64_tr_b16 a[36:37], v204 offset:33792\n" \
"ds_read_b64_tr_b16 a[38:39], v204 offset:36864\n" \
"v_mfma_f32_16x16x32_bf16 v[60:63], a[200:203], a[24:27], v[60:63]\n" \
"s_add_u32 m0, s79, 65536\n" \
"s_add_u32 s66, s66, 65589248\n" \
"ds_read_b64_tr_b16 a[40:41], v204 offset:20480\n" \
"v_mfma_f32_16x16x32_bf16 v[60:63], a[204:207], a[28:31], v[60:63]\n" \
"buffer_load_dwordx4 v192, s[44:47], s73 offen offset:0 lds\n" \
"v_mfma_f32_16x16x32_bf16 v[52:55], a[200:203], a[16:19], v[52:55]\n" \
"s_add_u32 s70, s9, 1\n" \
"s_sub_u32 s70, s70, s90\n" \
"ds_read_b64_tr_b16 a[42:43], v204 offset:23552\n" \
"v_mfma_f32_16x16x32_bf16 v[52:55], a[204:207], a[20:23], v[52:55]\n" \
"s_lshr_b32 s70, s70, 1\n" \
"s_bfm_b32 s65, s70, 0\n" \
"ds_read_b64_tr_b16 a[44:45], v204 offset:32768\n" \
"v_mfma_f32_16x16x32_bf16 v[44:47], a[200:203], a[8:11], v[44:47]\n" \
"s_sub_u32 s70, 0, s90\n" \
"s_xor_b32 s65, s65, s70\n" \
"ds_read_b64_tr_b16 a[46:47], v204 offset:35840\n" \
"v_mfma_f32_16x16x32_bf16 v[44:47], a[204:207], a[12:15], v[44:47]\n" \
"buffer_load_dwordx4 v193, s[44:47], s73 offen offset:1024 lds\n" \
"v_mfma_f32_16x16x32_bf16 v[72:75], a[0:3], a[208:211], v[72:75]\n" \
"s_and_b32 s74, s70, 0xffff\n" \
"s_xor_b32 s68, s68, s8\n" \
"ds_read_b64_tr_b16 a[48:49], v222 offset:20480\n" \
"v_mfma_f32_16x16x32_bf16 v[72:75], a[4:7], a[212:215], v[72:75]\n" \
"s_and_b32 s68, s68, 3\n" \
"s_lshl_b32 s68, s68, 12\n" \
"ds_read_b128 a[0:3], v202 offset:0\n" \
"v_mfma_f32_16x16x32_bf16 v[80:83], a[8:11], a[208:211], v[80:83]\n" \
"ds_read_b128 a[4:7], v203 offset:0\n" \
"v_mfma_f32_16x16x32_bf16 v[80:83], a[12:15], a[212:215], v[80:83]\n" \
"buffer_load_dwordx4 v192, s[44:47], s73 offen offset:2048 lds\n" \
"v_mfma_f32_16x16x32_bf16 v[84:87], a[8:11], a[216:219], v[84:87]\n" \
"ds_read_b64_tr_b16 a[50:51], v222 offset:23552\n" \
"v_mfma_f32_16x16x32_bf16 v[84:87], a[12:15], a[220:223], v[84:87]\n" \
"ds_read_b128 a[8:11], v200 offset:0\n" \
"v_mfma_f32_16x16x32_bf16 v[92:95], a[16:19], a[216:219], v[92:95]\n" \
"ds_read_b128 a[12:15], v201 offset:0\n" \
"v_mfma_f32_16x16x32_bf16 v[92:95], a[20:23], a[220:223], v[92:95]\n" \
"buffer_load_dwordx4 v193, s[44:47], s73 offen offset:3072 lds\n" \
"v_mfma_f32_16x16x32_bf16 v[88:91], a[16:19], a[208:211], v[88:91]\n" \
"ds_read_b64_tr_b16 a[52:53], v222 offset:32768\n" \
"v_mfma_f32_16x16x32_bf16 v[88:91], a[20:23], a[212:215], v[88:91]\n" \
"s_add_u32 m0, s89, 65536\n" \
"ds_read_b128 a[16:19], v200 offset:4096\n" \
"v_mfma_f32_16x16x32_bf16 v[96:99], a[24:27], a[208:211], v[96:99]\n" \
"s_xor_b32 s73, s73, 0x180\n" \
"ds_read_b128 a[20:23], v201 offset:4096\n" \
"v_mfma_f32_16x16x32_bf16 v[96:99], a[28:31], a[212:215], v[96:99]\n" \
"buffer_load_dwordx4 v196, s[44:47], s75 offen lds\n" \
"v_mfma_f32_16x16x32_bf16 v[100:103], a[24:27], a[216:219], v[100:103]\n" \
"s_xor_b32 s75, s75, 0x180\n" \
"s_mul_i32 s76, s80, 393216\n" \
"ds_read_b64_tr_b16 a[54:55], v222 offset:35840\n" \
"v_mfma_f32_16x16x32_bf16 v[100:103], a[28:31], a[220:223], v[100:103]\n" \
"s_add_u32 s76, s76, s78\n" \
"ds_read_b128 a[24:27], v200 offset:8192\n" \
"v_mfma_f32_16x16x32_bf16 v[108:111], a[192:195], a[216:219], v[108:111]\n" \
"s_add_u32 m0, s91, 86016\n" \
"ds_read_b128 a[28:31], v201 offset:8192\n" \
"v_mfma_f32_16x16x32_bf16 v[108:111], a[196:199], a[220:223], v[108:111]\n" \
"buffer_load_dwordx4 v197, s[48:51], s76 offen offset:0 lds\n" \
"v_mfma_f32_16x16x32_bf16 v[104:107], a[192:195], a[208:211], v[104:107]\n" \
"v_mfma_f32_16x16x32_bf16 v[104:107], a[196:199], a[212:215], v[104:107]\n" \
"s_waitcnt lgkmcnt(2)\n" \
"v_mfma_f32_16x16x32_bf16 v[32:35], a[32:35], a[0:3], v[32:35]\n" \
"s_add_u32 s77, s76, 24576\n" \
"ds_read_b128 a[192:195], v200 offset:12288\n" \
"v_mfma_f32_16x16x32_bf16 v[32:35], a[36:39], a[4:7], v[32:35]\n" \
"buffer_load_dwordx4 v198, s[48:51], s76 offen offset:1024 lds\n" \
"v_mfma_f32_16x16x32_bf16 v[40:43], a[32:35], a[8:11], v[40:43]\n" \
"ds_read_b128 a[196:199], v201 offset:12288\n" \
"v_mfma_f32_16x16x32_bf16 v[40:43], a[36:39], a[12:15], v[40:43]\n" \
"ds_read_b64_tr_b16 a[200:201], v222 offset:21504\n" \
"v_mfma_f32_16x16x32_bf16 v[0:3], a[40:43], a[8:11], v[0:3]\n" \
"s_xor_b32 s80, s80, 0x3\n" \
"ds_read_b64_tr_b16 a[202:203], v222 offset:24576\n" \
"v_mfma_f32_16x16x32_bf16 v[0:3], a[44:47], a[12:15], v[0:3]\n" \
"buffer_load_dwordx4 v199, s[48:51], s76 offen offset:2048 lds\n" \
"v_mfma_f32_16x16x32_bf16 v[4:7], a[48:51], a[8:11], v[4:7]\n" \
"ds_read_b64_tr_b16 a[204:205], v222 offset:33792\n" \
"v_mfma_f32_16x16x32_bf16 v[4:7], a[52:55], a[12:15], v[4:7]\n" \
"ds_read_b64_tr_b16 a[206:207], v222 offset:36864\n" \
"v_mfma_f32_16x16x32_bf16 v[12:15], a[48:51], a[16:19], v[12:15]\n" \
"s_add_u32 m0, m0, 3072\n" \
"ds_read_b64_tr_b16 a[208:209], v204 offset:22528\n" \
"v_mfma_f32_16x16x32_bf16 v[12:15], a[52:55], a[20:23], v[12:15]\n" \
"buffer_load_dwordx4 v197, s[48:51], s77 offen offset:0 lds\n" \
"v_mfma_f32_16x16x32_bf16 v[8:11], a[40:43], a[16:19], v[8:11]\n" \
"ds_read_b64_tr_b16 a[210:211], v204 offset:25600\n" \
"v_mfma_f32_16x16x32_bf16 v[8:11], a[44:47], a[20:23], v[8:11]\n" \
"ds_read_b64_tr_b16 a[212:213], v204 offset:34816\n" \
"v_mfma_f32_16x16x32_bf16 v[48:51], a[32:35], a[16:19], v[48:51]\n" \
"s_waitcnt lgkmcnt(3)\n" \
"ds_read_b64_tr_b16 a[214:215], v204 offset:37888\n" \
"v_mfma_f32_16x16x32_bf16 v[48:51], a[36:39], a[20:23], v[48:51]\n" \
"buffer_load_dwordx4 v198, s[48:51], s77 offen offset:1024 lds\n" \
"v_mfma_f32_16x16x32_bf16 v[56:59], a[32:35], a[24:27], v[56:59]\n" \
"ds_read_b64_tr_b16 a[216:217], v222 offset:22528\n" \
"v_mfma_f32_16x16x32_bf16 v[56:59], a[36:39], a[28:31], v[56:59]\n" \
"ds_read_b64_tr_b16 a[218:219], v222 offset:25600\n" \
"v_mfma_f32_16x16x32_bf16 v[16:19], a[40:43], a[24:27], v[16:19]\n" \
"ds_read_b64_tr_b16 a[220:221], v222 offset:34816\n" \
"v_mfma_f32_16x16x32_bf16 v[16:19], a[44:47], a[28:31], v[16:19]\n" \
"buffer_load_dwordx4 v199, s[48:51], s77 offen offset:2048 lds\n" \
"v_mfma_f32_16x16x32_bf16 v[20:23], a[48:51], a[24:27], v[20:23]\n" \
"ds_read_b64_tr_b16 a[222:223], v222 offset:37888\n" \
"v_mfma_f32_16x16x32_bf16 v[20:23], a[52:55], a[28:31], v[20:23]\n" \
"v_mfma_f32_16x16x32_bf16 v[28:31], a[48:51], a[192:195], v[28:31]\n" \
"v_mfma_f32_16x16x32_bf16 v[28:31], a[52:55], a[196:199], v[28:31]\n" \
"v_mfma_f32_16x16x32_bf16 v[24:27], a[40:43], a[192:195], v[24:27]\n" \
"v_mfma_f32_16x16x32_bf16 v[24:27], a[44:47], a[196:199], v[24:27]\n" \
"v_mfma_f32_16x16x32_bf16 v[64:67], a[32:35], a[192:195], v[64:67]\n" \
"v_mfma_f32_16x16x32_bf16 v[64:67], a[36:39], a[196:199], v[64:67]\n" \
"s_waitcnt vmcnt(11) lgkmcnt(0)\n" \
"s_barrier\n" \
"v_mfma_f32_16x16x32_bf16 v[68:71], a[200:203], a[192:195], v[68:71]\n" \
"ds_read_b64_tr_b16 a[32:33], v194 offset:1024\n" \
"ds_read_b64_tr_b16 a[34:35], v194 offset:4096\n" \
"v_mfma_f32_16x16x32_bf16 v[68:71], a[204:207], a[196:199], v[68:71]\n" \
"ds_read_b64_tr_b16 a[36:37], v194 offset:13312\n" \
"ds_read_b64_tr_b16 a[38:39], v194 offset:16384\n" \
"v_mfma_f32_16x16x32_bf16 v[60:63], a[200:203], a[24:27], v[60:63]\n" \
"Lpg9086_%=:\n" \
"Lst1x2_%=:\n" \
"s_cbranch_execz Lpg10945_%=\n" \
"s_add_u32 m0, s79, 0\n" \
"ds_read_b64_tr_b16 a[40:41], v194 offset:0\n" \
"v_mfma_f32_16x16x32_bf16 v[60:63], a[204:207], a[28:31], v[60:63]\n" \
"buffer_load_dwordx4 v192, s[44:47], s73 offen offset:0 lds\n" \
"v_mfma_f32_16x16x32_bf16 v[52:55], a[200:203], a[16:19], v[52:55]\n" \
"ds_read_b64_tr_b16 a[42:43], v194 offset:3072\n" \
"v_mfma_f32_16x16x32_bf16 v[52:55], a[204:207], a[20:23], v[52:55]\n" \
"ds_read_b64_tr_b16 a[44:45], v194 offset:12288\n" \
"v_mfma_f32_16x16x32_bf16 v[44:47], a[200:203], a[8:11], v[44:47]\n" \
"ds_read_b64_tr_b16 a[46:47], v194 offset:15360\n" \
"v_mfma_f32_16x16x32_bf16 v[44:47], a[204:207], a[12:15], v[44:47]\n" \
"buffer_load_dwordx4 v193, s[44:47], s73 offen offset:1024 lds\n" \
"v_mfma_f32_16x16x32_bf16 v[72:75], a[0:3], a[208:211], v[72:75]\n" \
"ds_read_b64_tr_b16 a[48:49], v195 offset:0\n" \
"v_mfma_f32_16x16x32_bf16 v[72:75], a[4:7], a[212:215], v[72:75]\n" \
"ds_read_b128 a[0:3], v207 offset:45056\n" \
"v_mfma_f32_16x16x32_bf16 v[80:83], a[8:11], a[208:211], v[80:83]\n" \
"ds_read_b128 a[4:7], v208 offset:45056\n" \
"v_mfma_f32_16x16x32_bf16 v[80:83], a[12:15], a[212:215], v[80:83]\n" \
"buffer_load_dwordx4 v192, s[44:47], s73 offen offset:2048 lds\n" \
"v_mfma_f32_16x16x32_bf16 v[84:87], a[8:11], a[216:219], v[84:87]\n" \
"ds_read_b64_tr_b16 a[50:51], v195 offset:3072\n" \
"v_mfma_f32_16x16x32_bf16 v[84:87], a[12:15], a[220:223], v[84:87]\n" \
"ds_read_b128 a[8:11], v205 offset:45056\n" \
"v_mfma_f32_16x16x32_bf16 v[92:95], a[16:19], a[216:219], v[92:95]\n" \
"ds_read_b128 a[12:15], v206 offset:45056\n" \
"v_mfma_f32_16x16x32_bf16 v[92:95], a[20:23], a[220:223], v[92:95]\n" \
"buffer_load_dwordx4 v193, s[44:47], s73 offen offset:3072 lds\n" \
"v_mfma_f32_16x16x32_bf16 v[88:91], a[16:19], a[208:211], v[88:91]\n" \
"ds_read_b64_tr_b16 a[52:53], v195 offset:12288\n" \
"v_mfma_f32_16x16x32_bf16 v[88:91], a[20:23], a[212:215], v[88:91]\n" \
"s_add_u32 m0, s89, 0\n" \
"ds_read_b128 a[16:19], v205 offset:49152\n" \
"v_mfma_f32_16x16x32_bf16 v[96:99], a[24:27], a[208:211], v[96:99]\n" \
"s_xor_b32 s73, s73, 0x80\n" \
"ds_read_b128 a[20:23], v206 offset:49152\n" \
"v_mfma_f32_16x16x32_bf16 v[96:99], a[28:31], a[212:215], v[96:99]\n" \
"buffer_load_dwordx4 v196, s[44:47], s75 offen lds\n" \
"v_mfma_f32_16x16x32_bf16 v[100:103], a[24:27], a[216:219], v[100:103]\n" \
"s_xor_b32 s75, s75, 0x80\n" \
"s_mul_i32 s76, s80, 393216\n" \
"ds_read_b64_tr_b16 a[54:55], v195 offset:15360\n" \
"v_mfma_f32_16x16x32_bf16 v[100:103], a[28:31], a[220:223], v[100:103]\n" \
"s_add_u32 s76, s76, s78\n" \
"ds_read_b128 a[24:27], v205 offset:53248\n" \
"v_mfma_f32_16x16x32_bf16 v[108:111], a[192:195], a[216:219], v[108:111]\n" \
"s_add_u32 m0, s91, 20480\n" \
"ds_read_b128 a[28:31], v206 offset:53248\n" \
"v_mfma_f32_16x16x32_bf16 v[108:111], a[196:199], a[220:223], v[108:111]\n" \
"buffer_load_dwordx4 v197, s[48:51], s76 offen offset:0 lds\n" \
"v_mfma_f32_16x16x32_bf16 v[104:107], a[192:195], a[208:211], v[104:107]\n" \
"v_mfma_f32_16x16x32_bf16 v[104:107], a[196:199], a[212:215], v[104:107]\n" \
"s_waitcnt lgkmcnt(2)\n" \
"v_mfma_f32_16x16x32_bf16 v[32:35], a[32:35], a[0:3], v[32:35]\n" \
"s_add_u32 s77, s76, 24576\n" \
"ds_read_b128 a[192:195], v205 offset:57344\n" \
"v_mfma_f32_16x16x32_bf16 v[32:35], a[36:39], a[4:7], v[32:35]\n" \
"buffer_load_dwordx4 v198, s[48:51], s76 offen offset:1024 lds\n" \
"v_mfma_f32_16x16x32_bf16 v[40:43], a[32:35], a[8:11], v[40:43]\n" \
"ds_read_b128 a[196:199], v206 offset:57344\n" \
"v_mfma_f32_16x16x32_bf16 v[40:43], a[36:39], a[12:15], v[40:43]\n" \
"ds_read_b64_tr_b16 a[200:201], v195 offset:1024\n" \
"v_mfma_f32_16x16x32_bf16 v[0:3], a[40:43], a[8:11], v[0:3]\n" \
"s_xor_b32 s80, s80, 0x1\n" \
"ds_read_b64_tr_b16 a[202:203], v195 offset:4096\n" \
"v_mfma_f32_16x16x32_bf16 v[0:3], a[44:47], a[12:15], v[0:3]\n" \
"buffer_load_dwordx4 v199, s[48:51], s76 offen offset:2048 lds\n" \
"v_mfma_f32_16x16x32_bf16 v[4:7], a[48:51], a[8:11], v[4:7]\n" \
"ds_read_b64_tr_b16 a[204:205], v195 offset:13312\n" \
"v_mfma_f32_16x16x32_bf16 v[4:7], a[52:55], a[12:15], v[4:7]\n" \
"ds_read_b64_tr_b16 a[206:207], v195 offset:16384\n" \
"v_mfma_f32_16x16x32_bf16 v[12:15], a[48:51], a[16:19], v[12:15]\n" \
"s_add_u32 m0, m0, 3072\n" \
"ds_read_b64_tr_b16 a[208:209], v194 offset:2048\n" \
"v_mfma_f32_16x16x32_bf16 v[12:15], a[52:55], a[20:23], v[12:15]\n" \
"buffer_load_dwordx4 v197, s[48:51], s77 offen offset:0 lds\n" \
"v_mfma_f32_16x16x32_bf16 v[8:11], a[40:43], a[16:19], v[8:11]\n" \
"ds_read_b64_tr_b16 a[210:211], v194 offset:5120\n" \
"v_mfma_f32_16x16x32_bf16 v[8:11], a[44:47], a[20:23], v[8:11]\n" \
"ds_read_b64_tr_b16 a[212:213], v194 offset:14336\n" \
"v_mfma_f32_16x16x32_bf16 v[48:51], a[32:35], a[16:19], v[48:51]\n" \
"s_waitcnt lgkmcnt(3)\n" \
"ds_read_b64_tr_b16 a[214:215], v194 offset:17408\n" \
"v_mfma_f32_16x16x32_bf16 v[48:51], a[36:39], a[20:23], v[48:51]\n" \
"buffer_load_dwordx4 v198, s[48:51], s77 offen offset:1024 lds\n" \
"v_mfma_f32_16x16x32_bf16 v[56:59], a[32:35], a[24:27], v[56:59]\n" \
"ds_read_b64_tr_b16 a[216:217], v195 offset:2048\n" \
"v_mfma_f32_16x16x32_bf16 v[56:59], a[36:39], a[28:31], v[56:59]\n" \
"ds_read_b64_tr_b16 a[218:219], v195 offset:5120\n" \
"v_mfma_f32_16x16x32_bf16 v[16:19], a[40:43], a[24:27], v[16:19]\n" \
"ds_read_b64_tr_b16 a[220:221], v195 offset:14336\n" \
"v_mfma_f32_16x16x32_bf16 v[16:19], a[44:47], a[28:31], v[16:19]\n" \
"buffer_load_dwordx4 v199, s[48:51], s77 offen offset:2048 lds\n" \
"v_mfma_f32_16x16x32_bf16 v[20:23], a[48:51], a[24:27], v[20:23]\n" \
"ds_read_b64_tr_b16 a[222:223], v195 offset:17408\n" \
"v_mfma_f32_16x16x32_bf16 v[20:23], a[52:55], a[28:31], v[20:23]\n" \
"v_mfma_f32_16x16x32_bf16 v[28:31], a[48:51], a[192:195], v[28:31]\n" \
"v_mfma_f32_16x16x32_bf16 v[28:31], a[52:55], a[196:199], v[28:31]\n" \
"buffer_load_dword v220, v221, s[56:59], s66 offen sc1\n" \
"v_mfma_f32_16x16x32_bf16 v[24:27], a[40:43], a[192:195], v[24:27]\n" \
"v_mfma_f32_16x16x32_bf16 v[24:27], a[44:47], a[196:199], v[24:27]\n" \
"v_mfma_f32_16x16x32_bf16 v[64:67], a[32:35], a[192:195], v[64:67]\n" \
"v_mfma_f32_16x16x32_bf16 v[64:67], a[36:39], a[196:199], v[64:67]\n" \
"s_waitcnt vmcnt(12) lgkmcnt(0)\n" \
"s_barrier\n" \
"v_mfma_f32_16x16x32_bf16 v[68:71], a[200:203], a[192:195], v[68:71]\n" \
"ds_read_b64_tr_b16 a[32:33], v209 offset:1024\n" \
"ds_read_b64_tr_b16 a[34:35], v209 offset:4096\n" \
"v_mfma_f32_16x16x32_bf16 v[68:71], a[204:207], a[196:199], v[68:71]\n" \
"ds_read_b64_tr_b16 a[36:37], v209 offset:13312\n" \
"ds_read_b64_tr_b16 a[38:39], v209 offset:16384\n" \
"v_mfma_f32_16x16x32_bf16 v[60:63], a[200:203], a[24:27], v[60:63]\n" \
"s_add_u32 m0, s79, 110592\n" \
"ds_read_b64_tr_b16 a[40:41], v209 offset:0\n" \
"v_mfma_f32_16x16x32_bf16 v[60:63], a[204:207], a[28:31], v[60:63]\n" \
"buffer_load_dwordx4 v192, s[44:47], s73 offen offset:0 lds\n" \
"v_mfma_f32_16x16x32_bf16 v[52:55], a[200:203], a[16:19], v[52:55]\n" \
"ds_read_b64_tr_b16 a[42:43], v209 offset:3072\n" \
"v_mfma_f32_16x16x32_bf16 v[52:55], a[204:207], a[20:23], v[52:55]\n" \
"ds_read_b64_tr_b16 a[44:45], v209 offset:12288\n" \
"v_mfma_f32_16x16x32_bf16 v[44:47], a[200:203], a[8:11], v[44:47]\n" \
"ds_read_b64_tr_b16 a[46:47], v209 offset:15360\n" \
"v_mfma_f32_16x16x32_bf16 v[44:47], a[204:207], a[12:15], v[44:47]\n" \
"buffer_load_dwordx4 v193, s[44:47], s73 offen offset:1024 lds\n" \
"v_mfma_f32_16x16x32_bf16 v[72:75], a[0:3], a[208:211], v[72:75]\n" \
"ds_read_b64_tr_b16 a[48:49], v210 offset:0\n" \
"v_mfma_f32_16x16x32_bf16 v[72:75], a[4:7], a[212:215], v[72:75]\n" \
"ds_read_b128 a[0:3], v207 offset:0\n" \
"v_mfma_f32_16x16x32_bf16 v[80:83], a[8:11], a[208:211], v[80:83]\n" \
"ds_read_b128 a[4:7], v208 offset:0\n" \
"v_mfma_f32_16x16x32_bf16 v[80:83], a[12:15], a[212:215], v[80:83]\n" \
"buffer_load_dwordx4 v192, s[44:47], s73 offen offset:2048 lds\n" \
"v_mfma_f32_16x16x32_bf16 v[84:87], a[8:11], a[216:219], v[84:87]\n" \
"ds_read_b64_tr_b16 a[50:51], v210 offset:3072\n" \
"v_mfma_f32_16x16x32_bf16 v[84:87], a[12:15], a[220:223], v[84:87]\n" \
"ds_read_b128 a[8:11], v205 offset:0\n" \
"v_mfma_f32_16x16x32_bf16 v[92:95], a[16:19], a[216:219], v[92:95]\n" \
"ds_read_b128 a[12:15], v206 offset:0\n" \
"v_mfma_f32_16x16x32_bf16 v[92:95], a[20:23], a[220:223], v[92:95]\n" \
"buffer_load_dwordx4 v193, s[44:47], s73 offen offset:3072 lds\n" \
"v_mfma_f32_16x16x32_bf16 v[88:91], a[16:19], a[208:211], v[88:91]\n" \
"ds_read_b64_tr_b16 a[52:53], v210 offset:12288\n" \
"v_mfma_f32_16x16x32_bf16 v[88:91], a[20:23], a[212:215], v[88:91]\n" \
"s_add_u32 m0, s89, 110592\n" \
"ds_read_b128 a[16:19], v205 offset:4096\n" \
"v_mfma_f32_16x16x32_bf16 v[96:99], a[24:27], a[208:211], v[96:99]\n" \
"s_xor_b32 s73, s73, 0x80\n" \
"ds_read_b128 a[20:23], v206 offset:4096\n" \
"v_mfma_f32_16x16x32_bf16 v[96:99], a[28:31], a[212:215], v[96:99]\n" \
"buffer_load_dwordx4 v196, s[44:47], s75 offen lds\n" \
"v_mfma_f32_16x16x32_bf16 v[100:103], a[24:27], a[216:219], v[100:103]\n" \
"s_xor_b32 s75, s75, 0x80\n" \
"s_mul_i32 s76, s80, 393216\n" \
"ds_read_b64_tr_b16 a[54:55], v210 offset:15360\n" \
"v_mfma_f32_16x16x32_bf16 v[100:103], a[28:31], a[220:223], v[100:103]\n" \
"s_add_u32 s76, s76, s78\n" \
"ds_read_b128 a[24:27], v205 offset:8192\n" \
"v_mfma_f32_16x16x32_bf16 v[108:111], a[192:195], a[216:219], v[108:111]\n" \
"s_add_u32 m0, s91, 139264\n" \
"ds_read_b128 a[28:31], v206 offset:8192\n" \
"v_mfma_f32_16x16x32_bf16 v[108:111], a[196:199], a[220:223], v[108:111]\n" \
"buffer_load_dwordx4 v197, s[48:51], s76 offen offset:0 lds\n" \
"v_mfma_f32_16x16x32_bf16 v[104:107], a[192:195], a[208:211], v[104:107]\n" \
"v_mfma_f32_16x16x32_bf16 v[104:107], a[196:199], a[212:215], v[104:107]\n" \
"s_waitcnt lgkmcnt(2)\n" \
"v_mfma_f32_16x16x32_bf16 v[32:35], a[32:35], a[0:3], v[32:35]\n" \
"s_add_u32 s77, s76, 24576\n" \
"ds_read_b128 a[192:195], v205 offset:12288\n" \
"v_mfma_f32_16x16x32_bf16 v[32:35], a[36:39], a[4:7], v[32:35]\n" \
"buffer_load_dwordx4 v198, s[48:51], s76 offen offset:1024 lds\n" \
"v_mfma_f32_16x16x32_bf16 v[40:43], a[32:35], a[8:11], v[40:43]\n" \
"ds_read_b128 a[196:199], v206 offset:12288\n" \
"v_mfma_f32_16x16x32_bf16 v[40:43], a[36:39], a[12:15], v[40:43]\n" \
"ds_read_b64_tr_b16 a[200:201], v210 offset:1024\n" \
"v_mfma_f32_16x16x32_bf16 v[0:3], a[40:43], a[8:11], v[0:3]\n" \
"s_xor_b32 s80, s80, 0x1\n" \
"ds_read_b64_tr_b16 a[202:203], v210 offset:4096\n" \
"v_mfma_f32_16x16x32_bf16 v[0:3], a[44:47], a[12:15], v[0:3]\n" \
"buffer_load_dwordx4 v199, s[48:51], s76 offen offset:2048 lds\n" \
"v_mfma_f32_16x16x32_bf16 v[4:7], a[48:51], a[8:11], v[4:7]\n" \
"ds_read_b64_tr_b16 a[204:205], v210 offset:13312\n" \
"v_mfma_f32_16x16x32_bf16 v[4:7], a[52:55], a[12:15], v[4:7]\n" \
"ds_read_b64_tr_b16 a[206:207], v210 offset:16384\n" \
"v_mfma_f32_16x16x32_bf16 v[12:15], a[48:51], a[16:19], v[12:15]\n" \
"s_add_u32 m0, m0, 3072\n" \
"ds_read_b64_tr_b16 a[208:209], v209 offset:2048\n" \
"v_mfma_f32_16x16x32_bf16 v[12:15], a[52:55], a[20:23], v[12:15]\n" \
"buffer_load_dwordx4 v197, s[48:51], s77 offen offset:0 lds\n" \
"v_mfma_f32_16x16x32_bf16 v[8:11], a[40:43], a[16:19], v[8:11]\n" \
"ds_read_b64_tr_b16 a[210:211], v209 offset:5120\n" \
"v_mfma_f32_16x16x32_bf16 v[8:11], a[44:47], a[20:23], v[8:11]\n" \
"ds_read_b64_tr_b16 a[212:213], v209 offset:14336\n" \
"v_mfma_f32_16x16x32_bf16 v[48:51], a[32:35], a[16:19], v[48:51]\n" \
"s_waitcnt lgkmcnt(3)\n" \
"ds_read_b64_tr_b16 a[214:215], v209 offset:17408\n" \
"v_mfma_f32_16x16x32_bf16 v[48:51], a[36:39], a[20:23], v[48:51]\n" \
"buffer_load_dwordx4 v198, s[48:51], s77 offen offset:1024 lds\n" \
"v_mfma_f32_16x16x32_bf16 v[56:59], a[32:35], a[24:27], v[56:59]\n" \
"ds_read_b64_tr_b16 a[216:217], v210 offset:2048\n" \
"v_mfma_f32_16x16x32_bf16 v[56:59], a[36:39], a[28:31], v[56:59]\n" \
"ds_read_b64_tr_b16 a[218:219], v210 offset:5120\n" \
"v_mfma_f32_16x16x32_bf16 v[16:19], a[40:43], a[24:27], v[16:19]\n" \
"ds_read_b64_tr_b16 a[220:221], v210 offset:14336\n" \
"v_mfma_f32_16x16x32_bf16 v[16:19], a[44:47], a[28:31], v[16:19]\n" \
"buffer_load_dwordx4 v199, s[48:51], s77 offen offset:2048 lds\n" \
"v_mfma_f32_16x16x32_bf16 v[20:23], a[48:51], a[24:27], v[20:23]\n" \
"s_mov_b32 s46, s81\n" \
"s_mov_b32 s47, s82\n" \
"ds_read_b64_tr_b16 a[222:223], v210 offset:17408\n" \
"v_mfma_f32_16x16x32_bf16 v[20:23], a[52:55], a[28:31], v[20:23]\n" \
"s_mov_b32 s48, 0\n" \
"s_mov_b32 s49, s68\n" \
"s_sub_u32 s50, 64, s9\n" \
"v_mfma_f32_16x16x32_bf16 v[28:31], a[48:51], a[192:195], v[28:31]\n" \
"s_cmp_eq_u32 s90, 0\n" \
"s_cselect_b32 s50, s9, s50\n" \
"s_andn2_b32 s50, s50, 3\n" \
"v_mfma_f32_16x16x32_bf16 v[28:31], a[52:55], a[196:199], v[28:31]\n" \
"s_lshl_b32 s50, s50, 12\n" \
"s_waitcnt vmcnt(11)\n" \
"v_cmp_eq_u32 vcc, s60, v220\n" \
"v_mfma_f32_16x16x32_bf16 v[24:27], a[40:43], a[192:195], v[24:27]\n" \
"v_mfma_f32_16x16x32_bf16 v[24:27], a[44:47], a[196:199], v[24:27]\n" \
"s_andn2_b32 s70, s65, vcc_lo\n" \
"s_cbranch_scc1 Lpl5_%=\n" \
"Lpr5_%=:\n" \
"v_mfma_f32_16x16x32_bf16 v[64:67], a[32:35], a[192:195], v[64:67]\n" \
"v_mfma_f32_16x16x32_bf16 v[64:67], a[36:39], a[196:199], v[64:67]\n" \
"s_xor_b32 s82, s48, s49\n" \
"s_cmp_lt_u32 s48, s50\n" \
"s_cselect_b32 s82, s82, s48\n" \
"s_waitcnt vmcnt(12) lgkmcnt(0)\n" \
"s_barrier\n" \
"v_mfma_f32_16x16x32_bf16 v[68:71], a[200:203], a[192:195], v[68:71]\n" \
"ds_read_b64_tr_b16 a[32:33], v204 offset:21504\n" \
"ds_read_b64_tr_b16 a[34:35], v204 offset:24576\n" \
"v_mfma_f32_16x16x32_bf16 v[68:71], a[204:207], a[196:199], v[68:71]\n" \
"s_add_u32 s81, s82, s46\n" \
"s_lshr_b32 s78, s61, 1\n" \
"s_lshl_b32 s80, s78, 9\n" \
"s_add_u32 s80, s80, 511\n" \
"ds_read_b64_tr_b16 a[36:37], v204 offset:33792\n" \
"ds_read_b64_tr_b16 a[38:39], v204 offset:36864\n" \
"v_mfma_f32_16x16x32_bf16 v[60:63], a[200:203], a[24:27], v[60:63]\n" \
"s_add_u32 m0, s86, s79\n" \
"ds_read_b64_tr_b16 a[40:41], v204 offset:20480\n" \
"v_sub_u32 v145, s80, v234\n" \
"v_mfma_f32_16x16x32_bf16 v[60:63], a[204:207], a[28:31], v[60:63]\n" \
"buffer_load_dwordx4 v234, s[56:59], s81 offen offset:0 sc1 lds\n" \
"v_mfma_f32_16x16x32_bf16 v[52:55], a[200:203], a[16:19], v[52:55]\n" \
"s_lshl_b32 s80, s78, 3\n" \
"s_not_b32 s80, s80\n" \
"ds_read_b64_tr_b16 a[42:43], v204 offset:23552\n" \
"v_mfma_f32_16x16x32_bf16 v[52:55], a[204:207], a[20:23], v[52:55]\n" \
"s_lshl_b32 s82, s82, 5\n" \
"ds_read_b64_tr_b16 a[44:45], v204 offset:32768\n" \
"v_sub_u32 v146, s80, v238\n" \
"v_mfma_f32_16x16x32_bf16 v[44:47], a[200:203], a[8:11], v[44:47]\n" \
"s_add_u32 s82, s82, s47\n" \
"ds_read_b64_tr_b16 a[46:47], v204 offset:35840\n" \
"v_subrev_u32 v152, 0, v238\n" \
"v_mfma_f32_16x16x32_bf16 v[44:47], a[204:207], a[12:15], v[44:47]\n" \
"buffer_load_dwordx4 v234, s[56:59], s81 offen offset:1024 sc1 lds\n" \
"v_mfma_f32_16x16x32_bf16 v[72:75], a[0:3], a[208:211], v[72:75]\n" \
"ds_read_b64_tr_b16 a[48:49], v222 offset:20480\n" \
"v_sub_u32 v144, 7, v238\n" \
"v_mfma_f32_16x16x32_bf16 v[72:75], a[4:7], a[212:215], v[72:75]\n" \
"s_add_u32 s48, s48, 0x1000\n" \
"ds_read_b128 a[0:3], v202 offset:0\n" \
"v_or_b32 v152, v152, v144\n" \
"v_mfma_f32_16x16x32_bf16 v[80:83], a[8:11], a[208:211], v[80:83]\n" \
"s_sub_u32 s86, s86, 32768\n" \
"s_cselect_b32 s86, 131072, s86\n" \
"ds_read_b128 a[4:7], v203 offset:0\n" \
"v_mfma_f32_16x16x32_bf16 v[80:83], a[12:15], a[212:215], v[80:83]\n" \
"buffer_load_dwordx4 v234, s[56:59], s81 offen offset:2048 sc1 lds\n" \
"v_mfma_f32_16x16x32_bf16 v[84:87], a[8:11], a[216:219], v[84:87]\n" \
"ds_read_b64_tr_b16 a[50:51], v222 offset:23552\n" \
"v_ashrrev_i32 v152, 31, v152\n" \
"v_mfma_f32_16x16x32_bf16 v[84:87], a[12:15], a[220:223], v[84:87]\n" \
"ds_read_b128 a[8:11], v200 offset:0\n" \
"v_and_b32 v152, 0xff800000, v152\n" \
"v_mfma_f32_16x16x32_bf16 v[92:95], a[16:19], a[216:219], v[92:95]\n" \
"ds_read_b128 a[12:15], v201 offset:0\n" \
"v_sub_u32 v156, -9, v238\n" \
"v_mfma_f32_16x16x32_bf16 v[92:95], a[20:23], a[220:223], v[92:95]\n" \
"buffer_load_dwordx4 v234, s[56:59], s81 offen offset:3072 sc1 lds\n" \
"v_mfma_f32_16x16x32_bf16 v[88:91], a[16:19], a[208:211], v[88:91]\n" \
"ds_read_b64_tr_b16 a[52:53], v222 offset:32768\n" \
"v_ashrrev_i32 v156, 31, v156\n" \
"v_mfma_f32_16x16x32_bf16 v[88:91], a[20:23], a[212:215], v[88:91]\n" \
"ds_read_b128 a[16:19], v200 offset:4096\n" \
"v_and_b32 v156, 0xff800000, v156\n" \
"v_mfma_f32_16x16x32_bf16 v[96:99], a[24:27], a[208:211], v[96:99]\n" \
"s_add_u32 m0, m0, 0x4000\n" \
"ds_read_b128 a[20:23], v201 offset:4096\n" \
"v_add_u32 v148, 0, v146\n" \
"v_mfma_f32_16x16x32_bf16 v[96:99], a[28:31], a[212:215], v[96:99]\n" \
"buffer_load_dwordx4 v216, s[36:39], s82 offen offset:0 lds\n" \
"v_mfma_f32_16x16x32_bf16 v[100:103], a[24:27], a[216:219], v[100:103]\n" \
"ds_read_b64_tr_b16 a[54:55], v222 offset:35840\n" \
"v_or3_b32 v148, v148, v145, s92\n" \
"v_mfma_f32_16x16x32_bf16 v[100:103], a[28:31], a[220:223], v[100:103]\n" \
"ds_read_b128 a[24:27], v200 offset:8192\n" \
"v_ashrrev_i32 v148, 31, v148\n" \
"v_mfma_f32_16x16x32_bf16 v[108:111], a[192:195], a[216:219], v[108:111]\n" \
"ds_read_b128 a[28:31], v201 offset:8192\n" \
"v_and_b32 v148, 0xff800000, v148\n" \
"v_mfma_f32_16x16x32_bf16 v[108:111], a[196:199], a[220:223], v[108:111]\n" \
"buffer_load_dwordx4 v217, s[36:39], s82 offen offset:1024 lds\n" \
"v_mfma_f32_16x16x32_bf16 v[104:107], a[192:195], a[208:211], v[104:107]\n" \
"v_subrev_u32 v153, 1, v238\n" \
"v_sub_u32 v144, 8, v238\n" \
"v_mfma_f32_16x16x32_bf16 v[104:107], a[196:199], a[212:215], v[104:107]\n" \
"s_waitcnt lgkmcnt(2)\n" \
"v_or_b32 v153, v153, v144\n" \
"v_ashrrev_i32 v153, 31, v153\n" \
"v_mfma_f32_16x16x32_bf16 v[32:35], a[32:35], a[0:3], v[32:35]\n" \
"ds_read_b128 a[192:195], v200 offset:12288\n" \
"v_and_b32 v153, 0xff800000, v153\n" \
"v_mfma_f32_16x16x32_bf16 v[32:35], a[36:39], a[4:7], v[32:35]\n" \
"buffer_load_dwordx4 v218, s[36:39], s82 offen offset:2048 lds\n" \
"v_mfma_f32_16x16x32_bf16 v[40:43], a[32:35], a[8:11], v[40:43]\n" \
"ds_read_b128 a[196:199], v201 offset:12288\n" \
"v_sub_u32 v157, -8, v238\n" \
"v_mfma_f32_16x16x32_bf16 v[40:43], a[36:39], a[12:15], v[40:43]\n" \
"ds_read_b64_tr_b16 a[200:201], v222 offset:21504\n" \
"v_ashrrev_i32 v157, 31, v157\n" \
"v_mfma_f32_16x16x32_bf16 v[0:3], a[40:43], a[8:11], v[0:3]\n" \
"ds_read_b64_tr_b16 a[202:203], v222 offset:24576\n" \
"v_and_b32 v157, 0xff800000, v157\n" \
"v_mfma_f32_16x16x32_bf16 v[0:3], a[44:47], a[12:15], v[0:3]\n" \
"buffer_load_dwordx4 v219, s[36:39], s82 offen offset:3072 lds\n" \
"v_mfma_f32_16x16x32_bf16 v[4:7], a[48:51], a[8:11], v[4:7]\n" \
"ds_read_b64_tr_b16 a[204:205], v222 offset:33792\n" \
"v_add_u32 v149, 1, v146\n" \
"v_mfma_f32_16x16x32_bf16 v[4:7], a[52:55], a[12:15], v[4:7]\n" \
"ds_read_b64_tr_b16 a[206:207], v222 offset:36864\n" \
"v_or3_b32 v149, v149, v145, s92\n" \
"v_mfma_f32_16x16x32_bf16 v[12:15], a[48:51], a[16:19], v[12:15]\n" \
"ds_read_b64_tr_b16 a[208:209], v204 offset:22528\n" \
"v_ashrrev_i32 v149, 31, v149\n" \
"v_mfma_f32_16x16x32_bf16 v[12:15], a[52:55], a[20:23], v[12:15]\n" \
"v_and_b32 v149, 0xff800000, v149\n" \
"v_subrev_u32 v154, 2, v238\n" \
"v_mfma_f32_16x16x32_bf16 v[8:11], a[40:43], a[16:19], v[8:11]\n" \
"ds_read_b64_tr_b16 a[210:211], v204 offset:25600\n" \
"v_sub_u32 v144, 9, v238\n" \
"v_mfma_f32_16x16x32_bf16 v[8:11], a[44:47], a[20:23], v[8:11]\n" \
"ds_read_b64_tr_b16 a[212:213], v204 offset:34816\n" \
"v_or_b32 v154, v154, v144\n" \
"v_mfma_f32_16x16x32_bf16 v[48:51], a[32:35], a[16:19], v[48:51]\n" \
"ds_read_b64_tr_b16 a[214:215], v204 offset:37888\n" \
"v_ashrrev_i32 v154, 31, v154\n" \
"v_mfma_f32_16x16x32_bf16 v[48:51], a[36:39], a[20:23], v[48:51]\n" \
"s_waitcnt lgkmcnt(3)\n" \
"v_and_b32 v154, 0xff800000, v154\n" \
"v_sub_u32 v158, -7, v238\n" \
"v_mfma_f32_16x16x32_bf16 v[56:59], a[32:35], a[24:27], v[56:59]\n" \
"ds_read_b64_tr_b16 a[216:217], v222 offset:22528\n" \
"v_ashrrev_i32 v158, 31, v158\n" \
"v_mfma_f32_16x16x32_bf16 v[56:59], a[36:39], a[28:31], v[56:59]\n" \
"ds_read_b64_tr_b16 a[218:219], v222 offset:25600\n" \
"v_and_b32 v158, 0xff800000, v158\n" \
"v_mfma_f32_16x16x32_bf16 v[16:19], a[40:43], a[24:27], v[16:19]\n" \
"ds_read_b64_tr_b16 a[220:221], v222 offset:34816\n" \
"v_add_u32 v150, 2, v146\n" \
"v_mfma_f32_16x16x32_bf16 v[16:19], a[44:47], a[28:31], v[16:19]\n" \
"v_or3_b32 v150, v150, v145, s92\n" \
"v_ashrrev_i32 v150, 31, v150\n" \
"v_mfma_f32_16x16x32_bf16 v[20:23], a[48:51], a[24:27], v[20:23]\n" \
"ds_read_b64_tr_b16 a[222:223], v222 offset:37888\n" \
"v_and_b32 v150, 0xff800000, v150\n" \
"v_mfma_f32_16x16x32_bf16 v[20:23], a[52:55], a[28:31], v[20:23]\n" \
"v_subrev_u32 v155, 3, v238\n" \
"v_sub_u32 v144, 10, v238\n" \
"v_mfma_f32_16x16x32_bf16 v[28:31], a[48:51], a[192:195], v[28:31]\n" \
"v_or_b32 v155, v155, v144\n" \
"v_ashrrev_i32 v155, 31, v155\n" \
"v_mfma_f32_16x16x32_bf16 v[28:31], a[52:55], a[196:199], v[28:31]\n" \
"v_and_b32 v155, 0xff800000, v155\n" \
"v_sub_u32 v159, -6, v238\n" \
"v_mfma_f32_16x16x32_bf16 v[24:27], a[40:43], a[192:195], v[24:27]\n" \
"v_ashrrev_i32 v159, 31, v159\n" \
"v_and_b32 v159, 0xff800000, v159\n" \
"v_mfma_f32_16x16x32_bf16 v[24:27], a[44:47], a[196:199], v[24:27]\n" \
"v_add_u32 v151, 3, v146\n" \
"v_or3_b32 v151, v151, v145, s92\n" \
"v_mfma_f32_16x16x32_bf16 v[64:67], a[32:35], a[192:195], v[64:67]\n" \
"v_ashrrev_i32 v151, 31, v151\n" \
"v_and_b32 v151, 0xff800000, v151\n" \
"v_mfma_f32_16x16x32_bf16 v[64:67], a[36:39], a[196:199], v[64:67]\n" \
"s_xor_b32 s82, s48, s49\n" \
"s_cmp_lt_u32 s48, s50\n" \
"s_cselect_b32 s82, s82, s48\n" \
"s_waitcnt vmcnt(8) lgkmcnt(0)\n" \
"s_barrier\n" \
"v_mfma_f32_16x16x32_bf16 v[68:71], a[200:203], a[192:195], v[68:71]\n" \
"ds_read_b64_tr_b16 a[32:33], v194 offset:1024\n" \
"ds_read_b64_tr_b16 a[34:35], v194 offset:4096\n" \
"v_mfma_f32_16x16x32_bf16 v[68:71], a[204:207], a[196:199], v[68:71]\n" \
"s_add_u32 s81, s82, s46\n" \
"ds_read_b64_tr_b16 a[36:37], v194 offset:13312\n" \
"ds_read_b64_tr_b16 a[38:39], v194 offset:16384\n" \
"v_mfma_f32_16x16x32_bf16 v[60:63], a[200:203], a[24:27], v[60:63]\n" \
"s_add_u32 m0, s86, s79\n" \
"ds_read_b64_tr_b16 a[40:41], v194 offset:0\n" \
"v_mov_b32 v186, 0xffff0000\n" \
"v_mfma_f32_16x16x32_bf16 v[60:63], a[204:207], a[28:31], v[60:63]\n" \
"buffer_load_dwordx4 v234, s[56:59], s81 offen offset:0 sc1 lds\n" \
"v_mfma_f32_16x16x32_bf16 v[52:55], a[200:203], a[16:19], v[52:55]\n" \
"ds_read_b64_tr_b16 a[42:43], v194 offset:3072\n" \
"v_subrev_u32 v144, 0, v238\n" \
"v_mfma_f32_16x16x32_bf16 v[52:55], a[204:207], a[20:23], v[52:55]\n" \
"s_lshl_b32 s82, s82, 5\n" \
"ds_read_b64_tr_b16 a[44:45], v194 offset:12288\n" \
"v_subrev_u32 v184, 1, v238\n" \
"v_mfma_f32_16x16x32_bf16 v[44:47], a[200:203], a[8:11], v[44:47]\n" \
"s_add_u32 s82, s82, s47\n" \
"ds_read_b64_tr_b16 a[46:47], v194 offset:15360\n" \
"v_lshrrev_b32 v144, 16, v144\n" \
"v_mfma_f32_16x16x32_bf16 v[44:47], a[204:207], a[12:15], v[44:47]\n" \
"buffer_load_dwordx4 v234, s[56:59], s81 offen offset:1024 sc1 lds\n" \
"v_mfma_f32_16x16x32_bf16 v[72:75], a[0:3], a[208:211], v[72:75]\n" \
"ds_read_b64_tr_b16 a[48:49], v195 offset:0\n" \
"v_and_or_b32 v184, v184, v186, v144\n" \
"v_mfma_f32_16x16x32_bf16 v[72:75], a[4:7], a[212:215], v[72:75]\n" \
"s_add_u32 s48, s48, 0x1000\n" \
"ds_read_b128 a[0:3], v207 offset:45056\n" \
"v_subrev_u32 v144, 2, v238\n" \
"v_mfma_f32_16x16x32_bf16 v[80:83], a[8:11], a[208:211], v[80:83]\n" \
"s_sub_u32 s86, s86, 32768\n" \
"s_cselect_b32 s86, 131072, s86\n" \
"ds_read_b128 a[4:7], v208 offset:45056\n" \
"v_mfma_f32_16x16x32_bf16 v[80:83], a[12:15], a[212:215], v[80:83]\n" \
"buffer_load_dwordx4 v234, s[56:59], s81 offen offset:2048 sc1 lds\n" \
"v_mfma_f32_16x16x32_bf16 v[84:87], a[8:11], a[216:219], v[84:87]\n" \
"ds_read_b64_tr_b16 a[50:51], v195 offset:3072\n" \
"v_subrev_u32 v185, 3, v238\n" \
"v_mfma_f32_16x16x32_bf16 v[84:87], a[12:15], a[220:223], v[84:87]\n" \
"ds_read_b128 a[8:11], v205 offset:45056\n" \
"v_lshrrev_b32 v144, 16, v144\n" \
"v_mfma_f32_16x16x32_bf16 v[92:95], a[16:19], a[216:219], v[92:95]\n" \
"ds_read_b128 a[12:15], v206 offset:45056\n" \
"v_and_or_b32 v185, v185, v186, v144\n" \
"v_mfma_f32_16x16x32_bf16 v[92:95], a[20:23], a[220:223], v[92:95]\n" \
"buffer_load_dwordx4 v234, s[56:59], s81 offen offset:3072 sc1 lds\n" \
"v_mfma_f32_16x16x32_bf16 v[88:91], a[16:19], a[208:211], v[88:91]\n" \
"ds_read_b64_tr_b16 a[52:53], v195 offset:12288\n" \
"v_mov_b32 v144, 0x3e8293ee\n" \
"v_mfma_f32_16x16x32_bf16 v[88:91], a[20:23], a[212:215], v[88:91]\n" \
"s_add_u32 s80, s79, 117760\n" \
"ds_read_b128 a[16:19], v205 offset:49152\n" \
"v_add_u32 v223, s80, v234\n" \
"v_mfma_f32_16x16x32_bf16 v[96:99], a[24:27], a[208:211], v[96:99]\n" \
"s_add_u32 m0, m0, 0x4000\n" \
"ds_read_b128 a[20:23], v206 offset:49152\n" \
"v_mfma_f32_16x16x32_bf16 v[96:99], a[28:31], a[212:215], v[96:99]\n" \
"buffer_load_dwordx4 v216, s[36:39], s82 offen offset:0 lds\n" \
"v_mfma_f32_16x16x32_bf16 v[100:103], a[24:27], a[216:219], v[100:103]\n" \
"s_cmp_lg_u32 s78, 0\n" \
"s_cselect_b64 s[76:77], -1, 0\n" \
"ds_read_b64_tr_b16 a[54:55], v195 offset:15360\n" \
"v_mfma_f32_16x16x32_bf16 v[100:103], a[28:31], a[220:223], v[100:103]\n" \
"ds_read_b128 a[24:27], v205 offset:53248\n" \
"v_mfma_f32_16x16x32_bf16 v[108:111], a[192:195], a[216:219], v[108:111]\n" \
"ds_read_b128 a[28:31], v206 offset:53248\n" \
"v_mfma_f32_16x16x32_bf16 v[108:111], a[196:199], a[220:223], v[108:111]\n" \
"buffer_load_dwordx4 v217, s[36:39], s82 offen offset:1024 lds\n" \
"v_mfma_f32_16x16x32_bf16 v[104:107], a[192:195], a[208:211], v[104:107]\n" \
"v_mfma_f32_16x16x32_bf16 v[104:107], a[196:199], a[212:215], v[104:107]\n" \
"s_waitcnt lgkmcnt(2)\n" \
"v_mfma_f32_16x16x32_bf16 v[32:35], a[32:35], a[0:3], v[32:35]\n" \
"ds_read_b128 a[192:195], v205 offset:57344\n" \
"v_mfma_f32_16x16x32_bf16 v[32:35], a[36:39], a[4:7], v[32:35]\n" \
"buffer_load_dwordx4 v218, s[36:39], s82 offen offset:2048 lds\n" \
"v_mfma_f32_16x16x32_bf16 v[40:43], a[32:35], a[8:11], v[40:43]\n" \
"ds_read_b128 a[196:199], v206 offset:57344\n" \
"v_mfma_f32_16x16x32_bf16 v[40:43], a[36:39], a[12:15], v[40:43]\n" \
"ds_read_b64_tr_b16 a[200:201], v195 offset:1024\n" \
"v_mfma_f32_16x16x32_bf16 v[0:3], a[40:43], a[8:11], v[0:3]\n" \
"ds_read_b64_tr_b16 a[202:203], v195 offset:4096\n" \
"v_mfma_f32_16x16x32_bf16 v[0:3], a[44:47], a[12:15], v[0:3]\n" \
"buffer_load_dwordx4 v219, s[36:39], s82 offen offset:3072 lds\n" \
"v_mfma_f32_16x16x32_bf16 v[4:7], a[48:51], a[8:11], v[4:7]\n" \
"s_xor_b32 s82, s48, s49\n" \
"s_cmp_lt_u32 s48, s50\n" \
"s_cselect_b32 s82, s82, s48\n" \
"ds_read_b64_tr_b16 a[204:205], v195 offset:13312\n" \
"v_mfma_f32_16x16x32_bf16 v[4:7], a[52:55], a[12:15], v[4:7]\n" \
"s_add_u32 s81, s82, s46\n" \
"ds_read_b64_tr_b16 a[206:207], v195 offset:16384\n" \
"v_mfma_f32_16x16x32_bf16 v[12:15], a[48:51], a[16:19], v[12:15]\n" \
"s_add_u32 m0, s86, s79\n" \
"ds_read_b64_tr_b16 a[208:209], v194 offset:2048\n" \
"v_mfma_f32_16x16x32_bf16 v[12:15], a[52:55], a[20:23], v[12:15]\n" \
"buffer_load_dwordx4 v234, s[56:59], s81 offen offset:0 sc1 lds\n" \
"v_mfma_f32_16x16x32_bf16 v[8:11], a[40:43], a[16:19], v[8:11]\n" \
"ds_read_b64_tr_b16 a[210:211], v194 offset:5120\n" \
"v_mfma_f32_16x16x32_bf16 v[8:11], a[44:47], a[20:23], v[8:11]\n" \
"s_lshl_b32 s82, s82, 5\n" \
"ds_read_b64_tr_b16 a[212:213], v194 offset:14336\n" \
"v_mfma_f32_16x16x32_bf16 v[48:51], a[32:35], a[16:19], v[48:51]\n" \
"s_waitcnt lgkmcnt(3)\n" \
"s_add_u32 s82, s82, s47\n" \
"ds_read_b64_tr_b16 a[214:215], v194 offset:17408\n" \
"v_mfma_f32_16x16x32_bf16 v[48:51], a[36:39], a[20:23], v[48:51]\n" \
"buffer_load_dwordx4 v234, s[56:59], s81 offen offset:1024 sc1 lds\n" \
"v_mfma_f32_16x16x32_bf16 v[56:59], a[32:35], a[24:27], v[56:59]\n" \
"ds_read_b64_tr_b16 a[216:217], v195 offset:2048\n" \
"v_mfma_f32_16x16x32_bf16 v[56:59], a[36:39], a[28:31], v[56:59]\n" \
"s_add_u32 s48, s48, 0x1000\n" \
"ds_read_b64_tr_b16 a[218:219], v195 offset:5120\n" \
"v_mfma_f32_16x16x32_bf16 v[16:19], a[40:43], a[24:27], v[16:19]\n" \
"s_sub_u32 s86, s86, 32768\n" \
"s_cselect_b32 s86, 131072, s86\n" \
"ds_read_b64_tr_b16 a[220:221], v195 offset:14336\n" \
"v_mfma_f32_16x16x32_bf16 v[16:19], a[44:47], a[28:31], v[16:19]\n" \
"buffer_load_dwordx4 v234, s[56:59], s81 offen offset:2048 sc1 lds\n" \
"v_mfma_f32_16x16x32_bf16 v[20:23], a[48:51], a[24:27], v[20:23]\n" \
"ds_read_b64_tr_b16 a[222:223], v195 offset:17408\n" \
"v_mfma_f32_16x16x32_bf16 v[20:23], a[52:55], a[28:31], v[20:23]\n" \
"v_mfma_f32_16x16x32_bf16 v[28:31], a[48:51], a[192:195], v[28:31]\n" \
"v_mfma_f32_16x16x32_bf16 v[28:31], a[52:55], a[196:199], v[28:31]\n" \
"buffer_load_dwordx4 v234, s[56:59], s81 offen offset:3072 sc1 lds\n" \
"v_mfma_f32_16x16x32_bf16 v[24:27], a[40:43], a[192:195], v[24:27]\n" \
"v_mfma_f32_16x16x32_bf16 v[24:27], a[44:47], a[196:199], v[24:27]\n" \
"v_mfma_f32_16x16x32_bf16 v[64:67], a[32:35], a[192:195], v[64:67]\n" \
"s_add_u32 m0, m0, 0x4000\n" \
"v_mfma_f32_16x16x32_bf16 v[64:67], a[36:39], a[196:199], v[64:67]\n" \
"buffer_load_dwordx4 v216, s[36:39], s82 offen offset:0 lds\n" \
"s_waitcnt vmcnt(13) lgkmcnt(0)\n" \
"s_barrier\n" \
"v_mfma_f32_16x16x32_bf16 v[44:47], a[200:203], a[8:11], v[44:47]\n" \
"ds_read_b64_tr_b16 a[96:97], v214 offset:16384\n" \
"v_mfma_f32_16x16x32_bf16 v[44:47], a[204:207], a[12:15], v[44:47]\n" \
"ds_read_b64_tr_b16 a[98:99], v214 offset:18432\n" \
"v_mfma_f32_16x16x32_bf16 v[52:55], a[200:203], a[16:19], v[52:55]\n" \
"ds_read_b64_tr_b16 a[100:101], v214 offset:24576\n" \
"v_mfma_f32_16x16x32_bf16 v[52:55], a[204:207], a[20:23], v[52:55]\n" \
"buffer_load_dwordx4 v217, s[36:39], s82 offen offset:1024 lds\n" \
"v_mfma_f32_16x16x32_bf16 v[60:63], a[200:203], a[24:27], v[60:63]\n" \
"ds_read_b64_tr_b16 a[102:103], v214 offset:26624\n" \
"v_cvt_pk_bf16_f32 v112, v0, v1\n" \
"v_mfma_f32_16x16x32_bf16 v[60:63], a[204:207], a[28:31], v[60:63]\n" \
"ds_read_b128 a[64:67], v213 offset:0\n" \
"v_cvt_pk_bf16_f32 v113, v2, v3\n" \
"v_mfma_f32_16x16x32_bf16 v[68:71], a[200:203], a[192:195], v[68:71]\n" \
"ds_read_b128 a[68:71], v213 offset:2048\n" \
"v_cvt_pk_bf16_f32 v114, v4, v5\n" \
"v_mfma_f32_16x16x32_bf16 v[68:71], a[204:207], a[196:199], v[68:71]\n" \
"buffer_load_dwordx4 v218, s[36:39], s82 offen offset:2048 lds\n" \
"v_mfma_f32_16x16x32_bf16 v[72:75], a[0:3], a[208:211], v[72:75]\n" \
"ds_read_b64_tr_b16 a[104:105], v215 offset:16384\n" \
"v_cvt_pk_bf16_f32 v115, v6, v7\n" \
"v_mfma_f32_16x16x32_bf16 v[72:75], a[4:7], a[212:215], v[72:75]\n" \
"ds_read_b64_tr_b16 a[106:107], v215 offset:18432\n" \
"v_cvt_pk_bf16_f32 v116, v8, v9\n" \
"v_mfma_f32_16x16x32_bf16 v[80:83], a[8:11], a[208:211], v[80:83]\n" \
"ds_read_b64_tr_b16 a[108:109], v215 offset:24576\n" \
"v_cvt_pk_bf16_f32 v117, v10, v11\n" \
"v_mfma_f32_16x16x32_bf16 v[80:83], a[12:15], a[212:215], v[80:83]\n" \
"buffer_load_dwordx4 v219, s[36:39], s82 offen offset:3072 lds\n" \
"v_mfma_f32_16x16x32_bf16 v[84:87], a[8:11], a[216:219], v[84:87]\n" \
"Lpg9651_%=:\n" \
"Lst1x3_%=:\n" \
"s_cbranch_execz Lpg11507_%=\n" \
"s_xor_b32 s82, s48, s49\n" \
"s_cmp_lt_u32 s48, s50\n" \
"s_cselect_b32 s82, s82, s48\n" \
"ds_read_b64_tr_b16 a[110:111], v215 offset:26624\n" \
"v_mfma_f32_16x16x32_bf16 v[84:87], a[12:15], a[220:223], v[84:87]\n" \
"s_add_u32 s81, s82, s46\n" \
"ds_read_b128 a[72:75], v213 offset:256\n" \
"v_cvt_pk_bf16_f32 v118, v12, v13\n" \
"v_mfma_f32_16x16x32_bf16 v[92:95], a[16:19], a[216:219], v[92:95]\n" \
"s_add_u32 m0, s86, s79\n" \
"ds_read_b128 a[76:79], v213 offset:2304\n" \
"v_cvt_pk_bf16_f32 v119, v14, v15\n" \
"v_mfma_f32_16x16x32_bf16 v[92:95], a[20:23], a[220:223], v[92:95]\n" \
"buffer_load_dwordx4 v234, s[56:59], s81 offen offset:0 sc1 lds\n" \
"v_mfma_f32_16x16x32_bf16 v[88:91], a[16:19], a[208:211], v[88:91]\n" \
"ds_read_b128 a[80:83], v213 offset:4096\n" \
"v_cvt_pk_bf16_f32 v120, v16, v17\n" \
"v_mfma_f32_16x16x32_bf16 v[88:91], a[20:23], a[212:215], v[88:91]\n" \
"s_lshl_b32 s82, s82, 5\n" \
"ds_read_b128 a[84:87], v213 offset:6144\n" \
"v_cvt_pk_bf16_f32 v121, v18, v19\n" \
"v_mfma_f32_16x16x32_bf16 v[96:99], a[24:27], a[208:211], v[96:99]\n" \
"s_add_u32 s82, s82, s47\n" \
"v_cvt_pk_bf16_f32 v122, v20, v21\n" \
"v_cvt_pk_bf16_f32 v123, v22, v23\n" \
"v_mfma_f32_16x16x32_bf16 v[96:99], a[28:31], a[212:215], v[96:99]\n" \
"buffer_load_dwordx4 v234, s[56:59], s81 offen offset:1024 sc1 lds\n" \
"v_mfma_f32_16x16x32_bf16 v[100:103], a[24:27], a[216:219], v[100:103]\n" \
"v_cvt_pk_bf16_f32 v124, v24, v25\n" \
"v_cvt_pk_bf16_f32 v125, v26, v27\n" \
"v_mfma_f32_16x16x32_bf16 v[100:103], a[28:31], a[220:223], v[100:103]\n" \
"s_add_u32 s48, s48, 0x1000\n" \
"v_cvt_pk_bf16_f32 v126, v28, v29\n" \
"v_cvt_pk_bf16_f32 v127, v30, v31\n" \
"v_mfma_f32_16x16x32_bf16 v[108:111], a[192:195], a[216:219], v[108:111]\n" \
"s_sub_u32 s86, s86, 32768\n" \
"s_cselect_b32 s86, 131072, s86\n" \
"v_cvt_pk_bf16_f32 v128, v32, v33\n" \
"v_mfma_f32_16x16x32_bf16 v[108:111], a[196:199], a[220:223], v[108:111]\n" \
"buffer_load_dwordx4 v234, s[56:59], s81 offen offset:2048 sc1 lds\n" \
"v_mfma_f32_16x16x32_bf16 v[104:107], a[192:195], a[208:211], v[104:107]\n" \
"v_cvt_pk_bf16_f32 v129, v34, v35\n" \
"v_xor_b32 v145, 0x2000, v223\n" \
"v_mfma_f32_16x16x32_bf16 v[104:107], a[196:199], a[212:215], v[104:107]\n" \
"s_waitcnt lgkmcnt(0)\n" \
"v_cvt_pk_bf16_f32 v192, v72, v73\n" \
"v_cvt_pk_bf16_f32 v193, v74, v75\n" \
"ds_write_b64 v145, v[128:129]\n" \
"s_nop 0\n" \
"ds_write_b64 v145, v[192:193] offset:8\n" \
"v_cvt_pk_bf16_f32 v132, v40, v41\n" \
"v_cvt_pk_bf16_f32 v133, v42, v43\n" \
"v_cvt_pk_bf16_f32 v134, v44, v45\n" \
"v_cvt_pk_bf16_f32 v135, v46, v47\n" \
"v_cvt_pk_bf16_f32 v136, v48, v49\n" \
"v_cvt_pk_bf16_f32 v137, v50, v51\n" \
"v_cvt_pk_bf16_f32 v138, v52, v53\n" \
"v_cvt_pk_bf16_f32 v139, v54, v55\n" \
"v_cvt_pk_bf16_f32 v140, v56, v57\n" \
"v_cvt_pk_bf16_f32 v141, v58, v59\n" \
"v_cvt_pk_bf16_f32 v142, v60, v61\n" \
"v_cvt_pk_bf16_f32 v143, v62, v63\n" \
"v_mfma_f32_16x16x32_bf16 v[0:3], a[96:99], a[64:67], 0\n" \
"ds_read_b128 a[88:91], v213 offset:4352\n" \
"v_mfma_f32_16x16x32_bf16 v[0:3], a[100:103], a[68:71], v[0:3]\n" \
"ds_read_b128 a[92:95], v213 offset:6400\n" \
"v_mfma_f32_16x16x32_bf16 v[4:7], a[104:107], a[64:67], 0\n" \
"ds_read_b64_tr_b16 a[112:113], v214 offset:16896\n" \
"v_mfma_f32_16x16x32_bf16 v[4:7], a[108:111], a[68:71], v[4:7]\n" \
"ds_read_b64_tr_b16 a[114:115], v214 offset:18944\n" \
"v_mfma_f32_16x16x32_bf16 v[20:23], a[104:107], a[72:75], 0\n" \
"buffer_load_dwordx4 v234, s[56:59], s81 offen offset:3072 sc1 lds\n" \
"v_mfma_f32_16x16x32_bf16 v[20:23], a[108:111], a[76:79], v[20:23]\n" \
"ds_read_b64_tr_b16 a[116:117], v214 offset:25088\n" \
"v_cvt_pk_bf16_f32 v64, v64, v65\n" \
"v_cvt_pk_bf16_f32 v65, v66, v67\n" \
"v_cvt_pk_bf16_f32 v66, v68, v69\n" \
"v_mfma_f32_16x16x32_bf16 v[16:19], a[96:99], a[72:75], 0\n" \
"ds_read_b64_tr_b16 a[118:119], v214 offset:27136\n" \
"v_cvt_pk_bf16_f32 v67, v70, v71\n" \
"v_mfma_f32_16x16x32_bf16 v[16:19], a[100:103], a[76:79], v[16:19]\n" \
"s_add_u32 m0, m0, 0x4000\n" \
"ds_read_b64_tr_b16 a[120:121], v215 offset:16896\n" \
"v_cvt_pk_bf16_f32 v194, v80, v81\n" \
"v_cvt_pk_bf16_f32 v195, v82, v83\n" \
"v_cvt_pk_bf16_f32 v74, v84, v85\n" \
"v_cvt_pk_bf16_f32 v75, v86, v87\n" \
"v_mfma_f32_16x16x32_bf16 v[32:35], a[96:99], a[80:83], 0\n" \
"buffer_load_dwordx4 v216, s[36:39], s82 offen offset:0 lds\n" \
"v_mfma_f32_16x16x32_bf16 v[32:35], a[100:103], a[84:87], v[32:35]\n" \
"s_sub_u32 s85, s85, 32768\n" \
"s_cselect_b32 s85, 131072, s85\n" \
"ds_read_b64_tr_b16 a[122:123], v215 offset:18944\n" \
"v_cvt_pk_bf16_f32 v196, v88, v89\n" \
"v_cvt_pk_bf16_f32 v197, v90, v91\n" \
"v_mfma_f32_16x16x32_bf16 v[176:179], v[64:67], v[124:127], v[152:155]\n" \
"v_mfma_f32_16x16x32_bf16 v[172:175], v[140:143], v[124:127], v[156:159]\n" \
"v_mfma_f32_16x16x32_bf16 v[168:171], v[140:143], v[120:123], v[152:155]\n" \
"v_mfma_f32_16x16x32_bf16 v[164:167], v[136:139], v[120:123], v[156:159]\n" \
"v_mfma_f32_16x16x32_bf16 v[160:163], v[136:139], v[116:119], v[152:155]\n" \
"v_mfma_f32_16x16x32_bf16 v[156:159], v[132:135], v[116:119], v[156:159]\n" \
"v_mfma_f32_16x16x32_bf16 v[152:155], v[132:135], v[112:115], v[152:155]\n" \
"v_mfma_f32_16x16x32_bf16 v[36:39], a[104:107], a[80:83], 0\n" \
"ds_read_b64_tr_b16 a[124:125], v215 offset:25088\n" \
"v_add_u32 v213, s85, v211\n" \
"v_mfma_f32_16x16x32_bf16 v[36:39], a[108:111], a[84:87], v[36:39]\n" \
"s_waitcnt lgkmcnt(3)\n" \
"ds_read_b64_tr_b16 a[126:127], v215 offset:27136\n" \
"v_mfma_f32_16x16x32_bf16 v[52:55], a[104:107], a[88:91], 0\n" \
"buffer_load_dwordx4 v217, s[36:39], s82 offen offset:1024 lds\n" \
"v_mfma_f32_16x16x32_bf16 v[52:55], a[108:111], a[92:95], v[52:55]\n" \
"v_add_u32 v214, s85, v212\n" \
"v_xor_b32 v215, 32, v214\n" \
"v_mfma_f32_16x16x32_bf16 v[48:51], a[96:99], a[88:91], 0\n" \
"buffer_load_dwordx4 v218, s[36:39], s82 offen offset:2048 lds\n" \
"v_mfma_f32_16x16x32_bf16 v[48:51], a[100:103], a[92:95], v[48:51]\n" \
"s_waitcnt vmcnt(15) lgkmcnt(0)\n" \
"s_barrier\n" \
"v_mfma_f32_16x16x32_bf16 v[8:11], a[112:115], a[64:67], 0\n" \
"ds_read_b64 v[130:131], v223\n" \
"ds_read_b64_tr_b16 a[96:97], v214 offset:16384\n" \
"ds_read_b64_tr_b16 a[98:99], v214 offset:18432\n" \
"v_mfma_f32_16x16x32_bf16 v[8:11], a[116:119], a[68:71], v[8:11]\n" \
"ds_read_b64 v[72:73], v223 offset:8\n" \
"ds_read_b64_tr_b16 a[100:101], v214 offset:24576\n" \
"ds_read_b64_tr_b16 a[102:103], v214 offset:26624\n" \
"v_mfma_f32_16x16x32_bf16 v[12:15], a[120:123], a[64:67], 0\n" \
"buffer_load_dwordx4 v219, s[36:39], s82 offen offset:3072 lds\n" \
"v_mfma_f32_16x16x32_bf16 v[12:15], a[124:127], a[68:71], v[12:15]\n" \
"s_xor_b32 s82, s48, s49\n" \
"s_cmp_lt_u32 s48, s50\n" \
"s_cselect_b32 s82, s82, s48\n" \
"ds_read_b128 a[64:67], v213 offset:0\n" \
"v_mfma_f32_16x16x32_bf16 v[28:31], a[120:123], a[72:75], 0\n" \
"s_add_u32 s81, s82, s46\n" \
"ds_read_b128 a[68:71], v213 offset:2048\n" \
"v_mfma_f32_16x16x32_bf16 v[28:31], a[124:127], a[76:79], v[28:31]\n" \
"s_add_u32 m0, s86, s79\n" \
"ds_read_b64_tr_b16 a[104:105], v215 offset:16384\n" \
"ds_read_b64_tr_b16 a[106:107], v215 offset:18432\n" \
"v_mfma_f32_16x16x32_bf16 v[24:27], a[112:115], a[72:75], 0\n" \
"buffer_load_dwordx4 v234, s[56:59], s81 offen offset:0 sc1 lds\n" \
"v_mfma_f32_16x16x32_bf16 v[24:27], a[116:119], a[76:79], v[24:27]\n" \
"s_waitcnt lgkmcnt(4)\n" \
"ds_read_b128 a[72:75], v213 offset:256\n" \
"v_mfma_f32_16x16x32_bf16 v[40:43], a[112:115], a[80:83], 0\n" \
"s_lshl_b32 s82, s82, 5\n" \
"ds_read_b128 a[76:79], v213 offset:2304\n" \
"v_mfma_f32_16x16x32_bf16 v[148:151], v[128:131], v[112:115], v[148:151]\n" \
"v_max_f32 v172, v172, v176\n" \
"v_max_f32 v173, v173, v177\n" \
"v_max_f32 v174, v174, v178\n" \
"v_max_f32 v175, v175, v179\n" \
"v_max_f32 v164, v164, v168\n" \
"v_max_f32 v165, v165, v169\n" \
"v_max_f32 v166, v166, v170\n" \
"v_max_f32 v167, v167, v171\n" \
"v_max_f32 v156, v156, v160\n" \
"v_max_f32 v157, v157, v161\n" \
"v_max_f32 v158, v158, v162\n" \
"v_max_f32 v159, v159, v163\n" \
"v_max_f32 v148, v148, v152\n" \
"v_max_f32 v149, v149, v153\n" \
"v_max_f32 v150, v150, v154\n" \
"v_max_f32 v151, v151, v155\n" \
"v_max3_f32 v183, v172, v173, v174\n" \
"v_max3_f32 v182, v164, v165, v166\n" \
"v_max3_f32 v181, v156, v157, v158\n" \
"v_mfma_f32_16x16x32_bf16 v[40:43], a[116:119], a[84:87], v[40:43]\n" \
"s_add_u32 s82, s82, s47\n" \
"ds_read_b64_tr_b16 a[108:109], v215 offset:24576\n" \
"ds_read_b64_tr_b16 a[110:111], v215 offset:26624\n" \
"v_max3_f32 v180, v148, v149, v150\n" \
"v_mfma_f32_16x16x32_bf16 v[44:47], a[120:123], a[80:83], 0\n" \
"buffer_load_dwordx4 v234, s[56:59], s81 offen offset:1024 sc1 lds\n" \
"v_mfma_f32_16x16x32_bf16 v[44:47], a[124:127], a[84:87], v[44:47]\n" \
"s_add_u32 s48, s48, 0x1000\n" \
"s_sub_u32 s86, s86, 32768\n" \
"s_cselect_b32 s86, 131072, s86\n" \
"ds_read_b128 a[80:83], v213 offset:4096\n" \
"v_max_f32 v183, v183, v175\n" \
"v_max_f32 v182, v182, v167\n" \
"v_max_f32 v181, v181, v159\n" \
"v_mfma_f32_16x16x32_bf16 v[60:63], a[120:123], a[88:91], 0\n" \
"s_waitcnt lgkmcnt(4)\n" \
"buffer_load_dwordx4 v234, s[56:59], s81 offen offset:2048 sc1 lds\n" \
"v_max_f32 v180, v180, v151\n" \
"v_mfma_f32_16x16x32_bf16 v[60:63], a[124:127], a[92:95], v[60:63]\n" \
"ds_read_b128 a[84:87], v213 offset:6144\n" \
"v_cvt_pk_bf16_f32 v76, v92, v93\n" \
"v_cvt_pk_bf16_f32 v77, v94, v95\n" \
"v_mfma_f32_16x16x32_bf16 v[56:59], a[112:115], a[88:91], 0\n" \
"buffer_load_dwordx4 v234, s[56:59], s81 offen offset:3072 sc1 lds\n" \
"v_mfma_f32_16x16x32_bf16 v[56:59], a[116:119], a[92:95], v[56:59]\n" \
"s_getpc_b64 s[44:45]\n" \
"Lop_%=:\n" \
"s_add_u32 s44, s44, Lor_%=-Lop_%=\n" \
"s_addc_u32 s45, s45, 0\n" \
"v_permlane32_swap_b32 v180, v181\n" \
"v_permlane32_swap_b32 v182, v183\n" \
"v_max_f32 v180, v180, v181\n" \
"v_mfma_f32_16x16x32_bf16 v[0:3], a[96:99], a[64:67], v[0:3]\n" \
"ds_read_b128 a[88:91], v213 offset:4352\n" \
"v_max_f32 v182, v182, v183\n" \
"v_mfma_f32_16x16x32_bf16 v[0:3], a[100:103], a[68:71], v[0:3]\n" \
"s_waitcnt lgkmcnt(2)\n" \
"ds_read_b128 a[92:95], v213 offset:6400\n" \
"v_cvt_pk_bf16_f32 v198, v96, v97\n" \
"v_cvt_pk_bf16_f32 v199, v98, v99\n" \
"v_permlane16_swap_b32 v180, v182\n" \
"v_mfma_f32_16x16x32_bf16 v[4:7], a[104:107], a[64:67], v[4:7]\n" \
"ds_read_b64_tr_b16 a[112:113], v214 offset:16896\n" \
"v_max_f32 v180, v180, v182\n" \
"v_mfma_f32_16x16x32_bf16 v[4:7], a[108:111], a[68:71], v[4:7]\n" \
"s_add_u32 m0, m0, 0x4000\n" \
"ds_read_b64_tr_b16 a[114:115], v214 offset:18944\n" \
"v_mul_f32 v180, v180, v144\n" \
"v_cvt_pk_bf16_f32 v78, v100, v101\n" \
"v_cvt_pk_bf16_f32 v79, v102, v103\n" \
"v_mov_b32 v182, v180\n" \
"v_mfma_f32_16x16x32_bf16 v[20:23], a[104:107], a[72:75], v[20:23]\n" \
"buffer_load_dwordx4 v216, s[36:39], s82 offen offset:0 lds\n" \
"v_mfma_f32_16x16x32_bf16 v[20:23], a[108:111], a[76:79], v[20:23]\n" \
"ds_read_b64_tr_b16 a[116:117], v214 offset:25088\n" \
"v_cvt_pk_bf16_f32 v200, v104, v105\n" \
"v_cvt_pk_bf16_f32 v201, v106, v107\n" \
"v_permlane16_swap_b32 v180, v182\n" \
"v_mfma_f32_16x16x32_bf16 v[16:19], a[96:99], a[72:75], v[16:19]\n" \
"ds_read_b64_tr_b16 a[118:119], v214 offset:27136\n" \
"v_mov_b32 v181, v180\n" \
"v_mfma_f32_16x16x32_bf16 v[16:19], a[100:103], a[76:79], v[16:19]\n" \
"s_waitcnt lgkmcnt(3)\n" \
"ds_read_b64_tr_b16 a[120:121], v215 offset:16896\n" \
"v_mov_b32 v183, v182\n" \
"v_cvt_pk_bf16_f32 v80, v108, v109\n" \
"v_cvt_pk_bf16_f32 v81, v110, v111\n" \
"v_permlane32_swap_b32 v180, v181\n" \
"v_mfma_f32_16x16x32_bf16 v[32:35], a[96:99], a[80:83], v[32:35]\n" \
"buffer_load_dwordx4 v217, s[36:39], s82 offen offset:1024 lds\n" \
"v_mfma_f32_16x16x32_bf16 v[32:35], a[100:103], a[84:87], v[32:35]\n" \
"s_sub_u32 s85, s85, 32768\n" \
"s_cselect_b32 s85, 131072, s85\n" \
"ds_read_b64_tr_b16 a[122:123], v215 offset:18944\n" \
"v_permlane32_swap_b32 v182, v183\n" \
"v_fma_f32 v172, v172, v144, -v183\n" \
"v_mfma_f32_16x16x32_bf16 v[36:39], a[104:107], a[80:83], v[36:39]\n" \
"ds_read_b64_tr_b16 a[124:125], v215 offset:25088\n" \
"v_add_u32 v213, s85, v211\n" \
"v_mfma_f32_16x16x32_bf16 v[36:39], a[108:111], a[84:87], v[36:39]\n" \
"ds_read_b64_tr_b16 a[126:127], v215 offset:27136\n" \
"v_fma_f32 v164, v164, v144, -v182\n" \
"v_fma_f32 v156, v156, v144, -v181\n" \
"v_fma_f32 v148, v148, v144, -v180\n" \
"v_fma_f32 v173, v173, v144, -v183\n" \
"v_mfma_f32_16x16x32_bf16 v[52:55], a[104:107], a[88:91], v[52:55]\n" \
"buffer_load_dwordx4 v218, s[36:39], s82 offen offset:2048 lds\n" \
"v_mfma_f32_16x16x32_bf16 v[52:55], a[108:111], a[92:95], v[52:55]\n" \
"v_add_u32 v214, s85, v212\n" \
"v_xor_b32 v215, 32, v214\n" \
"v_fma_f32 v165, v165, v144, -v182\n" \
"v_fma_f32 v157, v157, v144, -v181\n" \
"v_mfma_f32_16x16x32_bf16 v[48:51], a[96:99], a[88:91], v[48:51]\n" \
"buffer_load_dwordx4 v219, s[36:39], s82 offen offset:3072 lds\n" \
"v_mfma_f32_16x16x32_bf16 v[48:51], a[100:103], a[92:95], v[48:51]\n" \
"s_xor_b32 s82, s48, s49\n" \
"s_cmp_lt_u32 s48, s50\n" \
"s_cselect_b32 s82, s82, s48\n" \
"v_fma_f32 v149, v149, v144, -v180\n" \
"v_fma_f32 v174, v174, v144, -v183\n" \
"v_fma_f32 v166, v166, v144, -v182\n" \
"s_waitcnt vmcnt(16) lgkmcnt(0)\n" \
"s_barrier\n" \
"v_mfma_f32_16x16x32_bf16 v[8:11], a[112:115], a[64:67], v[8:11]\n" \
"s_add_u32 s81, s82, s46\n" \
"ds_read_b64_tr_b16 a[96:97], v214 offset:16384\n" \
"ds_read_b64_tr_b16 a[98:99], v214 offset:18432\n" \
"v_mfma_f32_16x16x32_bf16 v[8:11], a[116:119], a[68:71], v[8:11]\n" \
"s_add_u32 m0, s86, s79\n" \
"ds_read_b64_tr_b16 a[100:101], v214 offset:24576\n" \
"ds_read_b64_tr_b16 a[102:103], v214 offset:26624\n" \
"v_fma_f32 v158, v158, v144, -v181\n" \
"v_fma_f32 v150, v150, v144, -v180\n" \
"v_mfma_f32_16x16x32_bf16 v[12:15], a[120:123], a[64:67], v[12:15]\n" \
"buffer_load_dwordx4 v234, s[56:59], s81 offen offset:0 sc1 lds\n" \
"v_mfma_f32_16x16x32_bf16 v[12:15], a[124:127], a[68:71], v[12:15]\n" \
"ds_read_b128 a[64:67], v213 offset:0\n" \
"v_fma_f32 v175, v175, v144, -v183\n" \
"v_fma_f32 v167, v167, v144, -v182\n" \
"v_fma_f32 v159, v159, v144, -v181\n" \
"v_fma_f32 v151, v151, v144, -v180\n" \
"v_mfma_f32_16x16x32_bf16 v[28:31], a[120:123], a[72:75], v[28:31]\n" \
"s_lshl_b32 s82, s82, 5\n" \
"ds_read_b128 a[68:71], v213 offset:2048\n" \
"v_mfma_f32_16x16x32_bf16 v[28:31], a[124:127], a[76:79], v[28:31]\n" \
"s_add_u32 s82, s82, s47\n" \
"ds_read_b64_tr_b16 a[104:105], v215 offset:16384\n" \
"ds_read_b64_tr_b16 a[106:107], v215 offset:18432\n" \
"v_exp_f32 v172, v172\n" \
"v_mfma_f32_16x16x32_bf16 v[24:27], a[112:115], a[72:75], v[24:27]\n" \
"buffer_load_dwordx4 v234, s[56:59], s81 offen offset:1024 sc1 lds\n" \
"v_mfma_f32_16x16x32_bf16 v[24:27], a[116:119], a[76:79], v[24:27]\n" \
"ds_read_b128 a[72:75], v213 offset:256\n" \
"v_exp_f32 v164, v164\n" \
"v_exp_f32 v156, v156\n" \
"v_mfma_f32_16x16x32_bf16 v[40:43], a[112:115], a[80:83], v[40:43]\n" \
"s_add_u32 s48, s48, 0x1000\n" \
"ds_read_b128 a[76:79], v213 offset:2304\n" \
"v_mfma_f32_16x16x32_bf16 v[40:43], a[116:119], a[84:87], v[40:43]\n" \
"s_sub_u32 s86, s86, 32768\n" \
"s_cselect_b32 s86, 131072, s86\n" \
"ds_read_b64_tr_b16 a[108:109], v215 offset:24576\n" \
"ds_read_b64_tr_b16 a[110:111], v215 offset:26624\n" \
"v_exp_f32 v148, v148\n" \
"v_mfma_f32_16x16x32_bf16 v[44:47], a[120:123], a[80:83], v[44:47]\n" \
"buffer_load_dwordx4 v234, s[56:59], s81 offen offset:2048 sc1 lds\n" \
"v_mfma_f32_16x16x32_bf16 v[44:47], a[124:127], a[84:87], v[44:47]\n" \
"ds_read_b128 a[80:83], v213 offset:4096\n" \
"v_exp_f32 v173, v173\n" \
"v_exp_f32 v165, v165\n" \
"v_mfma_f32_16x16x32_bf16 v[60:63], a[120:123], a[88:91], v[60:63]\n" \
"s_waitcnt lgkmcnt(4)\n" \
"buffer_load_dwordx4 v234, s[56:59], s81 offen offset:3072 sc1 lds\n" \
"v_mfma_f32_16x16x32_bf16 v[60:63], a[124:127], a[92:95], v[60:63]\n" \
"s_add_u32 m0, m0, 0x4000\n" \
"ds_read_b128 a[84:87], v213 offset:6144\n" \
"v_exp_f32 v157, v157\n" \
"v_mfma_f32_16x16x32_bf16 v[56:59], a[112:115], a[88:91], v[56:59]\n" \
"buffer_load_dwordx4 v216, s[36:39], s82 offen offset:0 lds\n" \
"v_mfma_f32_16x16x32_bf16 v[56:59], a[116:119], a[92:95], v[56:59]\n" \
"v_exp_f32 v149, v149\n" \
"v_exp_f32 v174, v174\n" \
"v_exp_f32 v166, v166\n" \
"v_mfma_f32_16x16x32_bf16 v[0:3], a[96:99], a[64:67], v[0:3]\n" \
"ds_read_b128 a[88:91], v213 offset:4352\n" \
"v_mfma_f32_16x16x32_bf16 v[0:3], a[100:103], a[68:71], v[0:3]\n" \
"s_waitcnt lgkmcnt(2)\n" \
"ds_read_b128 a[92:95], v213 offset:6400\n" \
"v_exp_f32 v158, v158\n" \
"v_mfma_f32_16x16x32_bf16 v[4:7], a[104:107], a[64:67], v[4:7]\n" \
"ds_read_b64_tr_b16 a[112:113], v214 offset:16896\n" \
"v_mfma_f32_16x16x32_bf16 v[4:7], a[108:111], a[68:71], v[4:7]\n" \
"ds_read_b64_tr_b16 a[114:115], v214 offset:18944\n" \
"v_exp_f32 v150, v150\n" \
"v_exp_f32 v175, v175\n" \
"v_mfma_f32_16x16x32_bf16 v[20:23], a[104:107], a[72:75], v[20:23]\n" \
"buffer_load_dwordx4 v217, s[36:39], s82 offen offset:1024 lds\n" \
"v_mfma_f32_16x16x32_bf16 v[20:23], a[108:111], a[76:79], v[20:23]\n" \
"ds_read_b64_tr_b16 a[116:117], v214 offset:25088\n" \
"v_exp_f32 v167, v167\n" \
"v_exp_f32 v159, v159\n" \
"v_mfma_f32_16x16x32_bf16 v[16:19], a[96:99], a[72:75], v[16:19]\n" \
"ds_read_b64_tr_b16 a[118:119], v214 offset:27136\n" \
"v_mfma_f32_16x16x32_bf16 v[16:19], a[100:103], a[76:79], v[16:19]\n" \
"s_waitcnt lgkmcnt(3)\n" \
"ds_read_b64_tr_b16 a[120:121], v215 offset:16896\n" \
"v_exp_f32 v151, v151\n" \
"v_add_f32 v191, v172, v173\n" \
"v_add_f32 v190, v164, v165\n" \
"v_mfma_f32_16x16x32_bf16 v[32:35], a[96:99], a[80:83], v[32:35]\n" \
"buffer_load_dwordx4 v218, s[36:39], s82 offen offset:2048 lds\n" \
"v_mfma_f32_16x16x32_bf16 v[32:35], a[100:103], a[84:87], v[32:35]\n" \
"s_sub_u32 s85, s85, 32768\n" \
"s_cselect_b32 s85, 131072, s85\n" \
"ds_read_b64_tr_b16 a[122:123], v215 offset:18944\n" \
"v_add_f32 v189, v156, v157\n" \
"v_add_f32 v188, v148, v149\n" \
"v_mfma_f32_16x16x32_bf16 v[36:39], a[104:107], a[80:83], v[36:39]\n" \
"ds_read_b64_tr_b16 a[124:125], v215 offset:25088\n" \
"v_add_u32 v213, s85, v211\n" \
"v_mfma_f32_16x16x32_bf16 v[36:39], a[108:111], a[84:87], v[36:39]\n" \
"ds_read_b64_tr_b16 a[126:127], v215 offset:27136\n" \
"v_add_f32 v191, v191, v174\n" \
"v_add_f32 v190, v190, v166\n" \
"v_add_f32 v189, v189, v158\n" \
"v_add_f32 v188, v188, v150\n" \
"v_mfma_f32_16x16x32_bf16 v[52:55], a[104:107], a[88:91], v[52:55]\n" \
"buffer_load_dwordx4 v219, s[36:39], s82 offen offset:3072 lds\n" \
"v_mfma_f32_16x16x32_bf16 v[52:55], a[108:111], a[92:95], v[52:55]\n" \
"s_xor_b32 s82, s48, s49\n" \
"s_cmp_lt_u32 s48, s50\n" \
"s_cselect_b32 s82, s82, s48\n" \
"s_add_u32 s81, s82, s46\n" \
"s_add_u32 m0, s86, s79\n" \
"v_add_u32 v214, s85, v212\n" \
"v_xor_b32 v215, 32, v214\n" \
"v_add_f32 v191, v191, v175\n" \
"v_add_f32 v190, v190, v167\n" \
"v_mfma_f32_16x16x32_bf16 v[48:51], a[96:99], a[88:91], v[48:51]\n" \
"buffer_load_dwordx4 v234, s[56:59], s81 offen offset:0 sc1 lds\n" \
"v_mfma_f32_16x16x32_bf16 v[48:51], a[100:103], a[92:95], v[48:51]\n" \
"v_add_f32 v189, v189, v159\n" \
"v_add_f32 v188, v188, v151\n" \
"s_waitcnt vmcnt(17) lgkmcnt(0)\n" \
"s_barrier\n" \
"v_mfma_f32_16x16x32_bf16 v[8:11], a[112:115], a[64:67], v[8:11]\n" \
"s_lshl_b32 s82, s82, 5\n" \
"ds_read_b64_tr_b16 a[96:97], v214 offset:16384\n" \
"ds_read_b64_tr_b16 a[98:99], v214 offset:18432\n" \
"v_mfma_f32_16x16x32_bf16 v[8:11], a[116:119], a[68:71], v[8:11]\n" \
"s_add_u32 s82, s82, s47\n" \
"ds_read_b64_tr_b16 a[100:101], v214 offset:24576\n" \
"ds_read_b64_tr_b16 a[102:103], v214 offset:26624\n" \
"v_permlane32_swap_b32 v188, v189\n" \
"v_permlane32_swap_b32 v190, v191\n" \
"v_add_f32 v188, v188, v189\n" \
"v_mfma_f32_16x16x32_bf16 v[12:15], a[120:123], a[64:67], v[12:15]\n" \
"buffer_load_dwordx4 v234, s[56:59], s81 offen offset:1024 sc1 lds\n" \
"v_mfma_f32_16x16x32_bf16 v[12:15], a[124:127], a[68:71], v[12:15]\n" \
"ds_read_b128 a[64:67], v213 offset:0\n" \
"v_add_f32 v190, v190, v191\n" \
"v_mfma_f32_16x16x32_bf16 v[28:31], a[120:123], a[72:75], v[28:31]\n" \
"s_add_u32 s48, s48, 0x1000\n" \
"ds_read_b128 a[68:71], v213 offset:2048\n" \
"v_mfma_f32_16x16x32_bf16 v[28:31], a[124:127], a[76:79], v[28:31]\n" \
"s_sub_u32 s86, s86, 32768\n" \
"s_cselect_b32 s86, 131072, s86\n" \
"ds_read_b64_tr_b16 a[104:105], v215 offset:16384\n" \
"ds_read_b64_tr_b16 a[106:107], v215 offset:18432\n" \
"v_permlane16_swap_b32 v188, v190\n" \
"v_add_f32 v188, v188, v190\n" \
"v_rcp_f32 v188, v188\n" \
"v_mfma_f32_16x16x32_bf16 v[24:27], a[112:115], a[72:75], v[24:27]\n" \
"buffer_load_dwordx4 v234, s[56:59], s81 offen offset:2048 sc1 lds\n" \
"v_mfma_f32_16x16x32_bf16 v[24:27], a[116:119], a[76:79], v[24:27]\n" \
"ds_read_b128 a[72:75], v213 offset:256\n" \
"v_mov_b32 v190, v188\n" \
"v_mfma_f32_16x16x32_bf16 v[40:43], a[112:115], a[80:83], v[40:43]\n" \
"ds_read_b128 a[76:79], v213 offset:2304\n" \
"v_mfma_f32_16x16x32_bf16 v[40:43], a[116:119], a[84:87], v[40:43]\n" \
"ds_read_b64_tr_b16 a[108:109], v215 offset:24576\n" \
"ds_read_b64_tr_b16 a[110:111], v215 offset:26624\n" \
"v_permlane16_swap_b32 v188, v190\n" \
"v_mov_b32 v189, v188\n" \
"v_mov_b32 v191, v190\n" \
"v_mfma_f32_16x16x32_bf16 v[44:47], a[120:123], a[80:83], v[44:47]\n" \
"buffer_load_dwordx4 v234, s[56:59], s81 offen offset:3072 sc1 lds\n" \
"v_mfma_f32_16x16x32_bf16 v[44:47], a[124:127], a[84:87], v[44:47]\n" \
"s_add_u32 m0, m0, 0x4000\n" \
"ds_read_b128 a[80:83], v213 offset:4096\n" \
"v_permlane32_swap_b32 v188, v189\n" \
"v_permlane32_swap_b32 v190, v191\n" \
"v_mul_f32 v172, v172, v191\n" \
"v_mul_f32 v164, v164, v190\n" \
"v_mul_f32 v156, v156, v189\n" \
"v_mul_f32 v148, v148, v188\n" \
"v_mul_f32 v173, v173, v191\n" \
"v_mfma_f32_16x16x32_bf16 v[60:63], a[120:123], a[88:91], v[60:63]\n" \
"s_waitcnt lgkmcnt(4)\n" \
"buffer_load_dwordx4 v216, s[36:39], s82 offen offset:0 lds\n" \
"v_mul_f32 v165, v165, v190\n" \
"v_mfma_f32_16x16x32_bf16 v[60:63], a[124:127], a[92:95], v[60:63]\n" \
"ds_read_b128 a[84:87], v213 offset:6144\n" \
"v_mul_f32 v157, v157, v189\n" \
"v_mul_f32 v149, v149, v188\n" \
"v_mfma_f32_16x16x32_bf16 v[56:59], a[112:115], a[88:91], v[56:59]\n" \
"buffer_load_dwordx4 v217, s[36:39], s82 offen offset:1024 lds\n" \
"v_mfma_f32_16x16x32_bf16 v[56:59], a[116:119], a[92:95], v[56:59]\n" \
"v_mul_f32 v174, v174, v191\n" \
"v_mul_f32 v166, v166, v190\n" \
"v_mul_f32 v158, v158, v189\n" \
"v_mul_f32 v150, v150, v188\n" \
"v_mul_f32 v175, v175, v191\n" \
"v_mfma_f32_16x16x32_bf16 v[0:3], a[96:99], a[64:67], v[0:3]\n" \
"ds_read_b128 a[88:91], v213 offset:4352\n" \
"v_mul_f32 v167, v167, v190\n" \
"v_mfma_f32_16x16x32_bf16 v[0:3], a[100:103], a[68:71], v[0:3]\n" \
"s_waitcnt lgkmcnt(2)\n" \
"ds_read_b128 a[92:95], v213 offset:6400\n" \
"v_mul_f32 v159, v159, v189\n" \
"v_mul_f32 v151, v151, v188\n" \
"v_cvt_pk_bf16_f32 v178, v172, v173\n" \
"v_mfma_f32_16x16x32_bf16 v[4:7], a[104:107], a[64:67], v[4:7]\n" \
"ds_read_b64_tr_b16 a[112:113], v214 offset:16896\n" \
"v_cvt_pk_bf16_f32 v179, v174, v175\n" \
"v_mfma_f32_16x16x32_bf16 v[4:7], a[108:111], a[68:71], v[4:7]\n" \
"ds_read_b64_tr_b16 a[114:115], v214 offset:18944\n" \
"v_cvt_pk_bf16_f32 v170, v164, v165\n" \
"v_cvt_pk_bf16_f32 v171, v166, v167\n" \
"v_cvt_pk_bf16_f32 v162, v156, v157\n" \
"v_cvt_pk_bf16_f32 v163, v158, v159\n" \
"v_mfma_f32_16x16x32_bf16 v[20:23], a[104:107], a[72:75], v[20:23]\n" \
"buffer_load_dwordx4 v218, s[36:39], s82 offen offset:2048 lds\n" \
"v_mfma_f32_16x16x32_bf16 v[20:23], a[108:111], a[76:79], v[20:23]\n" \
"ds_read_b64_tr_b16 a[116:117], v214 offset:25088\n" \
"v_cvt_pk_bf16_f32 v154, v148, v149\n" \
"v_cvt_pk_bf16_f32 v155, v150, v151\n" \
"v_and_b32 v176, v178, v184\n" \
"v_mfma_f32_16x16x32_bf16 v[16:19], a[96:99], a[72:75], v[16:19]\n" \
"ds_read_b64_tr_b16 a[118:119], v214 offset:27136\n" \
"v_and_b32 v177, v179, v185\n" \
"v_mfma_f32_16x16x32_bf16 v[16:19], a[100:103], a[76:79], v[16:19]\n" \
"s_waitcnt lgkmcnt(3)\n" \
"ds_read_b64_tr_b16 a[120:121], v215 offset:16896\n" \
"v_xor_b32 v178, v178, v176\n" \
"v_xor_b32 v179, v179, v177\n" \
"v_and_b32 v168, v170, v184\n" \
"v_and_b32 v169, v171, v185\n" \
"v_mfma_f32_16x16x32_bf16 v[32:35], a[96:99], a[80:83], v[32:35]\n" \
"buffer_load_dwordx4 v219, s[36:39], s82 offen offset:3072 lds\n" \
"v_mfma_f32_16x16x32_bf16 v[32:35], a[100:103], a[84:87], v[32:35]\n" \
"s_sub_u32 s85, s85, 32768\n" \
"s_cselect_b32 s85, 131072, s85\n" \
"s_xor_b32 s82, s48, s49\n" \
"s_cmp_lt_u32 s48, s50\n" \
"s_cselect_b32 s82, s82, s48\n" \
"ds_read_b64_tr_b16 a[122:123], v215 offset:18944\n" \
"v_xor_b32 v170, v170, v168\n" \
"v_xor_b32 v171, v171, v169\n" \
"v_mfma_f32_16x16x32_bf16 v[36:39], a[104:107], a[80:83], v[36:39]\n" \
"s_add_u32 s81, s82, s46\n" \
"ds_read_b64_tr_b16 a[124:125], v215 offset:25088\n" \
"v_add_u32 v213, s85, v211\n" \
"v_mfma_f32_16x16x32_bf16 v[36:39], a[108:111], a[84:87], v[36:39]\n" \
"s_add_u32 m0, s86, s79\n" \
"ds_read_b64_tr_b16 a[126:127], v215 offset:27136\n" \
"v_and_b32 v160, v162, v184\n" \
"v_and_b32 v161, v163, v185\n" \
"v_xor_b32 v162, v162, v160\n" \
"v_xor_b32 v163, v163, v161\n" \
"v_mfma_f32_16x16x32_bf16 v[52:55], a[104:107], a[88:91], v[52:55]\n" \
"buffer_load_dwordx4 v234, s[56:59], s81 offen offset:0 sc1 lds\n" \
"v_mfma_f32_16x16x32_bf16 v[52:55], a[108:111], a[92:95], v[52:55]\n" \
"s_lshl_b32 s82, s82, 5\n" \
"s_add_u32 s82, s82, s47\n" \
"v_add_u32 v214, s85, v212\n" \
"v_xor_b32 v215, 32, v214\n" \
"v_and_b32 v152, v154, v184\n" \
"v_and_b32 v153, v155, v185\n" \
"v_mfma_f32_16x16x32_bf16 v[48:51], a[96:99], a[88:91], v[48:51]\n" \
"buffer_load_dwordx4 v234, s[56:59], s81 offen offset:1024 sc1 lds\n" \
"v_mfma_f32_16x16x32_bf16 v[48:51], a[100:103], a[92:95], v[48:51]\n" \
"v_xor_b32 v154, v154, v152\n" \
"v_xor_b32 v155, v155, v153\n" \
"s_waitcnt vmcnt(18) lgkmcnt(0)\n" \
"s_barrier\n" \
"v_mfma_f32_16x16x32_bf16 v[8:11], a[112:115], a[64:67], v[8:11]\n" \
"s_add_u32 s48, s48, 0x1000\n" \
"ds_read_b64_tr_b16 a[96:97], v214 offset:16384\n" \
"ds_read_b64_tr_b16 a[98:99], v214 offset:18432\n" \
"v_mfma_f32_16x16x32_bf16 v[136:139], v[198:201], v[176:179], 0\n" \
"v_mfma_f32_16x16x32_bf16 v[140:143], v[78:81], v[176:179], 0\n" \
"v_mfma_f32_16x16x32_bf16 v[128:131], v[196:199], v[168:171], 0\n" \
"v_mfma_f32_16x16x32_bf16 v[132:135], v[76:79], v[168:171], 0\n" \
"v_mfma_f32_16x16x32_bf16 v[120:123], v[194:197], v[160:163], 0\n" \
"v_mfma_f32_16x16x32_bf16 v[124:127], v[74:77], v[160:163], 0\n" \
"v_mfma_f32_16x16x32_bf16 v[112:115], v[192:195], v[152:155], 0\n" \
"v_mfma_f32_16x16x32_bf16 v[116:119], v[72:75], v[152:155], 0\n" \
"v_mfma_f32_16x16x32_bf16 v[8:11], a[116:119], a[68:71], v[8:11]\n" \
"s_sub_u32 s86, s86, 32768\n" \
"s_cselect_b32 s86, 131072, s86\n" \
"ds_read_b64_tr_b16 a[100:101], v214 offset:24576\n" \
"ds_read_b64_tr_b16 a[102:103], v214 offset:26624\n" \
"v_mfma_f32_16x16x32_bf16 v[12:15], a[120:123], a[64:67], v[12:15]\n" \
"buffer_load_dwordx4 v234, s[56:59], s81 offen offset:2048 sc1 lds\n" \
"v_mfma_f32_16x16x32_bf16 v[12:15], a[124:127], a[68:71], v[12:15]\n" \
"ds_read_b128 a[64:67], v213 offset:0\n" \
"v_cvt_pk_bf16_f32 v136, v136, v137\n" \
"v_cvt_pk_bf16_f32 v137, v138, v139\n" \
"v_cvt_pk_bf16_f32 v138, v140, v141\n" \
"v_cvt_pk_bf16_f32 v139, v142, v143\n" \
"v_cndmask_b32 v140, v136, v138, s[76:77]\n" \
"v_mfma_f32_16x16x32_bf16 v[28:31], a[120:123], a[72:75], v[28:31]\n" \
"ds_read_b128 a[68:71], v213 offset:2048\n" \
"v_cndmask_b32 v141, v137, v139, s[76:77]\n" \
"v_mfma_f32_16x16x32_bf16 v[28:31], a[124:127], a[76:79], v[28:31]\n" \
"ds_read_b64_tr_b16 a[104:105], v215 offset:16384\n" \
"ds_read_b64_tr_b16 a[106:107], v215 offset:18432\n" \
"v_cndmask_b32 v142, v138, v136, s[76:77]\n" \
"v_cndmask_b32 v143, v139, v137, s[76:77]\n" \
"v_mfma_f32_16x16x32_bf16 v[24:27], a[112:115], a[72:75], v[24:27]\n" \
"buffer_load_dwordx4 v234, s[56:59], s81 offen offset:3072 sc1 lds\n" \
"v_mfma_f32_16x16x32_bf16 v[24:27], a[116:119], a[76:79], v[24:27]\n" \
"ds_read_b128 a[72:75], v213 offset:256\n" \
"v_cvt_pk_bf16_f32 v128, v128, v129\n" \
"v_cvt_pk_bf16_f32 v129, v130, v131\n" \
"v_cvt_pk_bf16_f32 v130, v132, v133\n" \
"v_cvt_pk_bf16_f32 v131, v134, v135\n" \
"v_mfma_f32_16x16x32_bf16 v[40:43], a[112:115], a[80:83], v[40:43]\n" \
"ds_read_b128 a[76:79], v213 offset:2304\n" \
"v_cndmask_b32 v132, v128, v130, s[76:77]\n" \
"v_mfma_f32_16x16x32_bf16 v[40:43], a[116:119], a[84:87], v[40:43]\n" \
"s_add_u32 m0, m0, 0x4000\n" \
"ds_read_b64_tr_b16 a[108:109], v215 offset:24576\n" \
"ds_read_b64_tr_b16 a[110:111], v215 offset:26624\n" \
"v_cndmask_b32 v133, v129, v131, s[76:77]\n" \
"v_cndmask_b32 v134, v130, v128, s[76:77]\n" \
"v_mfma_f32_16x16x32_bf16 v[44:47], a[120:123], a[80:83], v[44:47]\n" \
"buffer_load_dwordx4 v216, s[36:39], s82 offen offset:0 lds\n" \
"v_mfma_f32_16x16x32_bf16 v[44:47], a[124:127], a[84:87], v[44:47]\n" \
"ds_read_b128 a[80:83], v213 offset:4096\n" \
"v_cndmask_b32 v135, v131, v129, s[76:77]\n" \
"v_cvt_pk_bf16_f32 v120, v120, v121\n" \
"v_cvt_pk_bf16_f32 v121, v122, v123\n" \
"v_cvt_pk_bf16_f32 v122, v124, v125\n" \
"v_mfma_f32_16x16x32_bf16 v[60:63], a[120:123], a[88:91], v[60:63]\n" \
"s_waitcnt lgkmcnt(4)\n" \
"buffer_load_dwordx4 v217, s[36:39], s82 offen offset:1024 lds\n" \
"v_cvt_pk_bf16_f32 v123, v126, v127\n" \
"v_mfma_f32_16x16x32_bf16 v[60:63], a[124:127], a[92:95], v[60:63]\n" \
"ds_read_b128 a[84:87], v213 offset:6144\n" \
"v_cndmask_b32 v124, v120, v122, s[76:77]\n" \
"Lpg10253_%=:\n" \
"s_cbranch_execz Lcb1_%=\n" \
"v_cndmask_b32 v125, v121, v123, s[76:77]\n" \
"v_mfma_f32_16x16x32_bf16 v[56:59], a[112:115], a[88:91], v[56:59]\n" \
"buffer_load_dwordx4 v218, s[36:39], s82 offen offset:2048 lds\n" \
"v_mfma_f32_16x16x32_bf16 v[56:59], a[116:119], a[92:95], v[56:59]\n" \
"v_cndmask_b32 v126, v122, v120, s[76:77]\n" \
"v_cndmask_b32 v127, v123, v121, s[76:77]\n" \
"v_cvt_pk_bf16_f32 v112, v112, v113\n" \
"v_cvt_pk_bf16_f32 v113, v114, v115\n" \
"v_cvt_pk_bf16_f32 v114, v116, v117\n" \
"v_mfma_f32_16x16x32_bf16 v[0:3], a[96:99], a[64:67], v[0:3]\n" \
"ds_read_b128 a[88:91], v213 offset:4352\n" \
"v_cvt_pk_bf16_f32 v115, v118, v119\n" \
"v_mfma_f32_16x16x32_bf16 v[0:3], a[100:103], a[68:71], v[0:3]\n" \
"s_waitcnt lgkmcnt(2)\n" \
"ds_read_b128 a[92:95], v213 offset:6400\n" \
"v_cndmask_b32 v116, v112, v114, s[76:77]\n" \
"v_cndmask_b32 v117, v113, v115, s[76:77]\n" \
"v_cndmask_b32 v118, v114, v112, s[76:77]\n" \
"v_mfma_f32_16x16x32_bf16 v[4:7], a[104:107], a[64:67], v[4:7]\n" \
"ds_read_b64_tr_b16 a[112:113], v214 offset:16896\n" \
"v_cndmask_b32 v119, v115, v113, s[76:77]\n" \
"v_mfma_f32_16x16x32_bf16 v[4:7], a[108:111], a[68:71], v[4:7]\n" \
"s_add_u32 s71, s93, 208896\n" \
"ds_read_b64_tr_b16 a[114:115], v214 offset:18944\n" \
"buffer_store_dwordx4 v[140:143], v236, s[56:59], s71 offen sc1\n" \
"s_add_u32 s71, s93, 139264\n" \
"v_mfma_f32_16x16x32_bf16 v[20:23], a[104:107], a[72:75], v[20:23]\n" \
"buffer_load_dwordx4 v219, s[36:39], s82 offen offset:3072 lds\n" \
"v_mfma_f32_16x16x32_bf16 v[20:23], a[108:111], a[76:79], v[20:23]\n" \
"s_xor_b32 s82, s48, s49\n" \
"s_cmp_lt_u32 s48, s50\n" \
"s_cselect_b32 s82, s82, s48\n" \
"ds_read_b64_tr_b16 a[116:117], v214 offset:25088\n" \
"buffer_store_dwordx4 v[132:135], v236, s[56:59], s71 offen sc1\n" \
"s_add_u32 s71, s93, 69632\n" \
"v_mfma_f32_16x16x32_bf16 v[16:19], a[96:99], a[72:75], v[16:19]\n" \
"s_waitcnt lgkmcnt(3)\n" \
"s_add_u32 s81, s82, s46\n" \
"ds_read_b64_tr_b16 a[118:119], v214 offset:27136\n" \
"v_mfma_f32_16x16x32_bf16 v[16:19], a[100:103], a[76:79], v[16:19]\n" \
"s_add_u32 m0, s86, s79\n" \
"ds_read_b64_tr_b16 a[120:121], v215 offset:16896\n" \
"buffer_store_dwordx4 v[124:127], v236, s[56:59], s71 offen sc1\n" \
"s_add_u32 s71, s93, 0\n" \
"v_mfma_f32_16x16x32_bf16 v[32:35], a[96:99], a[80:83], v[32:35]\n" \
"buffer_load_dwordx4 v234, s[56:59], s81 offen offset:0 sc1 lds\n" \
"v_mfma_f32_16x16x32_bf16 v[32:35], a[100:103], a[84:87], v[32:35]\n" \
"s_sub_u32 s85, s85, 32768\n" \
"s_cselect_b32 s85, 131072, s85\n" \
"ds_read_b64_tr_b16 a[122:123], v215 offset:18944\n" \
"buffer_store_dwordx4 v[116:119], v236, s[56:59], s71 offen sc1\n" \
"v_mfma_f32_16x16x32_bf16 v[36:39], a[104:107], a[80:83], v[36:39]\n" \
"s_lshl_b32 s82, s82, 5\n" \
"ds_read_b64_tr_b16 a[124:125], v215 offset:25088\n" \
"v_add_u32_e64 v213, s85, v211\n" \
"v_mfma_f32_16x16x32_bf16 v[36:39], a[108:111], a[84:87], v[36:39]\n" \
"s_add_u32 s82, s82, s47\n" \
"ds_read_b64_tr_b16 a[126:127], v215 offset:27136\n" \
"v_mfma_f32_16x16x32_bf16 v[52:55], a[104:107], a[88:91], v[52:55]\n" \
"buffer_load_dwordx4 v234, s[56:59], s81 offen offset:1024 sc1 lds\n" \
"v_mfma_f32_16x16x32_bf16 v[52:55], a[108:111], a[92:95], v[52:55]\n" \
"s_add_u32 s48, s48, 0x1000\n" \
"s_sub_u32 s86, s86, 32768\n" \
"s_cselect_b32 s86, 131072, s86\n" \
"v_add_u32_e64 v214, s85, v212\n" \
"v_xor_b32_e64 v215, 32, v214\n" \
"v_mfma_f32_16x16x32_bf16 v[48:51], a[96:99], a[88:91], v[48:51]\n" \
"buffer_load_dwordx4 v234, s[56:59], s81 offen offset:2048 sc1 lds\n" \
"v_mfma_f32_16x16x32_bf16 v[48:51], a[100:103], a[92:95], v[48:51]\n" \
"s_waitcnt vmcnt(23) lgkmcnt(0)\n" \
"s_barrier\n" \
"v_mfma_f32_16x16x32_bf16 v[8:11], a[112:115], a[64:67], v[8:11]\n" \
"ds_read_b64_tr_b16 a[96:97], v214 offset:16384\n" \
"ds_read_b64_tr_b16 a[98:99], v214 offset:18432\n" \
"v_mfma_f32_16x16x32_bf16 v[8:11], a[116:119], a[68:71], v[8:11]\n" \
"ds_read_b64_tr_b16 a[100:101], v214 offset:24576\n" \
"ds_read_b64_tr_b16 a[102:103], v214 offset:26624\n" \
"v_mfma_f32_16x16x32_bf16 v[12:15], a[120:123], a[64:67], v[12:15]\n" \
"buffer_load_dwordx4 v234, s[56:59], s81 offen offset:3072 sc1 lds\n" \
"v_mfma_f32_16x16x32_bf16 v[12:15], a[124:127], a[68:71], v[12:15]\n" \
"ds_read_b128 a[64:67], v213 offset:0\n" \
"v_mfma_f32_16x16x32_bf16 v[28:31], a[120:123], a[72:75], v[28:31]\n" \
"ds_read_b128 a[68:71], v213 offset:2048\n" \
"v_mfma_f32_16x16x32_bf16 v[28:31], a[124:127], a[76:79], v[28:31]\n" \
"s_add_u32 m0, m0, 0x4000\n" \
"ds_read_b64_tr_b16 a[104:105], v215 offset:16384\n" \
"ds_read_b64_tr_b16 a[106:107], v215 offset:18432\n" \
"v_mfma_f32_16x16x32_bf16 v[24:27], a[112:115], a[72:75], v[24:27]\n" \
"buffer_load_dwordx4 v216, s[36:39], s82 offen offset:0 lds\n" \
"v_mfma_f32_16x16x32_bf16 v[24:27], a[116:119], a[76:79], v[24:27]\n" \
"ds_read_b128 a[72:75], v213 offset:256\n" \
"v_mfma_f32_16x16x32_bf16 v[40:43], a[112:115], a[80:83], v[40:43]\n" \
"ds_read_b128 a[76:79], v213 offset:2304\n" \
"v_mfma_f32_16x16x32_bf16 v[40:43], a[116:119], a[84:87], v[40:43]\n" \
"ds_read_b64_tr_b16 a[108:109], v215 offset:24576\n" \
"ds_read_b64_tr_b16 a[110:111], v215 offset:26624\n" \
"v_mfma_f32_16x16x32_bf16 v[44:47], a[120:123], a[80:83], v[44:47]\n" \
"buffer_load_dwordx4 v217, s[36:39], s82 offen offset:1024 lds\n" \
"v_mfma_f32_16x16x32_bf16 v[44:47], a[124:127], a[84:87], v[44:47]\n" \
"ds_read_b128 a[80:83], v213 offset:4096\n" \
"v_mfma_f32_16x16x32_bf16 v[60:63], a[120:123], a[88:91], v[60:63]\n" \
"s_waitcnt lgkmcnt(4)\n" \
"buffer_load_dwordx4 v218, s[36:39], s82 offen offset:2048 lds\n" \
"v_mfma_f32_16x16x32_bf16 v[60:63], a[124:127], a[92:95], v[60:63]\n" \
"ds_read_b128 a[84:87], v213 offset:6144\n" \
"v_mfma_f32_16x16x32_bf16 v[56:59], a[112:115], a[88:91], v[56:59]\n" \
"buffer_load_dwordx4 v219, s[36:39], s82 offen offset:3072 lds\n" \
"v_mfma_f32_16x16x32_bf16 v[56:59], a[116:119], a[92:95], v[56:59]\n" \
"s_mov_b32 s72, 2\n" \
"s_nop 0\n" \
"Lo0_%=:\n" \
"v_mfma_f32_16x16x32_bf16 v[0:3], a[96:99], a[64:67], v[0:3]\n" \
"s_waitcnt lgkmcnt(1)\n" \
"s_xor_b32 s82, s48, s49\n" \
"ds_read_b128 a[88:91], v213 offset:4352\n" \
"v_mfma_f32_16x16x32_bf16 v[0:3], a[100:103], a[68:71], v[0:3]\n" \
"s_cmp_lt_u32 s48, s50\n" \
"s_cselect_b32 s82, s82, s48\n" \
"ds_read_b128 a[92:95], v213 offset:6400\n" \
"v_mfma_f32_16x16x32_bf16 v[4:7], a[104:107], a[64:67], v[4:7]\n" \
"s_add_u32 s81, s82, s46\n" \
"ds_read_b64_tr_b16 a[112:113], v214 offset:16896\n" \
"v_mfma_f32_16x16x32_bf16 v[4:7], a[108:111], a[68:71], v[4:7]\n" \
"s_add_u32 m0, s86, s79\n" \
"ds_read_b64_tr_b16 a[114:115], v214 offset:18944\n" \
"v_mfma_f32_16x16x32_bf16 v[20:23], a[104:107], a[72:75], v[20:23]\n" \
"buffer_load_dwordx4 v234, s[56:59], s81 offen offset:0 sc1 lds\n" \
"v_mfma_f32_16x16x32_bf16 v[20:23], a[108:111], a[76:79], v[20:23]\n" \
"ds_read_b64_tr_b16 a[116:117], v214 offset:25088\n" \
"v_mfma_f32_16x16x32_bf16 v[16:19], a[96:99], a[72:75], v[16:19]\n" \
"s_lshl_b32 s82, s82, 5\n" \
"ds_read_b64_tr_b16 a[118:119], v214 offset:27136\n" \
"v_mfma_f32_16x16x32_bf16 v[16:19], a[100:103], a[76:79], v[16:19]\n" \
"s_waitcnt lgkmcnt(3)\n" \
"s_add_u32 s82, s82, s47\n" \
"ds_read_b64_tr_b16 a[120:121], v215 offset:16896\n" \
"v_mfma_f32_16x16x32_bf16 v[32:35], a[96:99], a[80:83], v[32:35]\n" \
"buffer_load_dwordx4 v234, s[56:59], s81 offen offset:1024 sc1 lds\n" \
"v_mfma_f32_16x16x32_bf16 v[32:35], a[100:103], a[84:87], v[32:35]\n" \
"s_sub_u32 s85, s85, 32768\n" \
"s_cselect_b32 s85, 131072, s85\n" \
"ds_read_b64_tr_b16 a[122:123], v215 offset:18944\n" \
"v_mfma_f32_16x16x32_bf16 v[36:39], a[104:107], a[80:83], v[36:39]\n" \
"s_add_u32 s48, s48, 0x1000\n" \
"ds_read_b64_tr_b16 a[124:125], v215 offset:25088\n" \
"v_add_u32 v213, s85, v211\n" \
"v_mfma_f32_16x16x32_bf16 v[36:39], a[108:111], a[84:87], v[36:39]\n" \
"s_sub_u32 s86, s86, 32768\n" \
"s_cselect_b32 s86, 131072, s86\n" \
"ds_read_b64_tr_b16 a[126:127], v215 offset:27136\n" \
"v_mfma_f32_16x16x32_bf16 v[52:55], a[104:107], a[88:91], v[52:55]\n" \
"buffer_load_dwordx4 v234, s[56:59], s81 offen offset:2048 sc1 lds\n" \
"v_mfma_f32_16x16x32_bf16 v[52:55], a[108:111], a[92:95], v[52:55]\n" \
"v_add_u32 v214, s85, v212\n" \
"v_xor_b32 v215, 32, v214\n" \
"v_mfma_f32_16x16x32_bf16 v[48:51], a[96:99], a[88:91], v[48:51]\n" \
"buffer_load_dwordx4 v234, s[56:59], s81 offen offset:3072 sc1 lds\n" \
"v_mfma_f32_16x16x32_bf16 v[48:51], a[100:103], a[92:95], v[48:51]\n" \
"s_waitcnt vmcnt(20) lgkmcnt(0)\n" \
"s_barrier\n" \
"v_mfma_f32_16x16x32_bf16 v[8:11], a[112:115], a[64:67], v[8:11]\n" \
"ds_read_b64_tr_b16 a[96:97], v214 offset:16384\n" \
"ds_read_b64_tr_b16 a[98:99], v214 offset:18432\n" \
"v_mfma_f32_16x16x32_bf16 v[8:11], a[116:119], a[68:71], v[8:11]\n" \
"s_add_u32 m0, m0, 0x4000\n" \
"ds_read_b64_tr_b16 a[100:101], v214 offset:24576\n" \
"ds_read_b64_tr_b16 a[102:103], v214 offset:26624\n" \
"v_mfma_f32_16x16x32_bf16 v[12:15], a[120:123], a[64:67], v[12:15]\n" \
"buffer_load_dwordx4 v216, s[36:39], s82 offen offset:0 lds\n" \
"v_mfma_f32_16x16x32_bf16 v[12:15], a[124:127], a[68:71], v[12:15]\n" \
"ds_read_b128 a[64:67], v213 offset:0\n" \
"v_mfma_f32_16x16x32_bf16 v[28:31], a[120:123], a[72:75], v[28:31]\n" \
"ds_read_b128 a[68:71], v213 offset:2048\n" \
"v_mfma_f32_16x16x32_bf16 v[28:31], a[124:127], a[76:79], v[28:31]\n" \
"ds_read_b64_tr_b16 a[104:105], v215 offset:16384\n" \
"ds_read_b64_tr_b16 a[106:107], v215 offset:18432\n" \
"v_mfma_f32_16x16x32_bf16 v[24:27], a[112:115], a[72:75], v[24:27]\n" \
"buffer_load_dwordx4 v217, s[36:39], s82 offen offset:1024 lds\n" \
"v_mfma_f32_16x16x32_bf16 v[24:27], a[116:119], a[76:79], v[24:27]\n" \
"ds_read_b128 a[72:75], v213 offset:256\n" \
"v_mfma_f32_16x16x32_bf16 v[40:43], a[112:115], a[80:83], v[40:43]\n" \
"ds_read_b128 a[76:79], v213 offset:2304\n" \
"v_mfma_f32_16x16x32_bf16 v[40:43], a[116:119], a[84:87], v[40:43]\n" \
"ds_read_b64_tr_b16 a[108:109], v215 offset:24576\n" \
"ds_read_b64_tr_b16 a[110:111], v215 offset:26624\n" \
"v_mfma_f32_16x16x32_bf16 v[44:47], a[120:123], a[80:83], v[44:47]\n" \
"buffer_load_dwordx4 v218, s[36:39], s82 offen offset:2048 lds\n" \
"v_mfma_f32_16x16x32_bf16 v[44:47], a[124:127], a[84:87], v[44:47]\n" \
"ds_read_b128 a[80:83], v213 offset:4096\n" \
"v_mfma_f32_16x16x32_bf16 v[60:63], a[120:123], a[88:91], v[60:63]\n" \
"s_waitcnt lgkmcnt(4)\n" \
"v_mfma_f32_16x16x32_bf16 v[60:63], a[124:127], a[92:95], v[60:63]\n" \
"s_sub_u32 s72, s72, 1\n" \
"ds_read_b128 a[84:87], v213 offset:6144\n" \
"v_mfma_f32_16x16x32_bf16 v[56:59], a[112:115], a[88:91], v[56:59]\n" \
"buffer_load_dwordx4 v219, s[36:39], s82 offen offset:3072 lds\n" \
"v_mfma_f32_16x16x32_bf16 v[56:59], a[116:119], a[92:95], v[56:59]\n" \
"s_cbranch_scc0 Lo0_%=\n" \
"s_setpc_b64 s[44:45]\n" \
"Lor_%=:\n" \
"buffer_store_dword v230, v231, s[56:59], s12 offen sc1\n" \
"s_mov_b32 s72, s10\n" \
"s_call_b64 s[44:45], Lo0_%=\n" \
"buffer_load_dword v220, v221, s[56:59], s67 offen sc1\n" \
"s_mov_b32 s72, 1\n" \
"s_call_b64 s[44:45], Lo0_%=\n" \
"s_waitcnt vmcnt(16)\n" \
"v_cmp_eq_u32 vcc, s60, v220\n" \
"s_nop 1\n" \
"s_andn2_b32 s70, s74, vcc_lo\n" \
"s_cbranch_scc1 Lpl6_%=\n" \
"Lpr6_%=:\n" \
"s_cmp_eq_u32 s90, 0\n" \
"s_cselect_b32 s46, s46, s83\n" \
"s_cselect_b32 s47, s47, s84\n" \
"s_cselect_b32 s48, s48, 0\n" \
"s_cselect_b64 s[36:37], s[36:37], s[40:41]\n" \
"s_cselect_b32 s38, s38, s42\n" \
"s_mov_b32 s72, 3\n" \
"s_call_b64 s[44:45], Lo0_%=\n" \
"s_cmp_eq_u32 s90, 0\n" \
"s_cbranch_scc0 Lrole1_%=\n" \
"s_mov_b32 s72, s11\n" \
"s_call_b64 s[44:45], Lo0_%=\n" \
"buffer_load_dword v220, v241, s[56:59], s64 offen sc1\n" \
"s_mov_b32 s72, 1\n" \
"s_call_b64 s[44:45], Lo0_%=\n" \
"v_mfma_f32_16x16x32_bf16 v[0:3], a[96:99], a[64:67], v[0:3]\n" \
"s_waitcnt lgkmcnt(1)\n" \
"ds_read_b128 a[88:91], v213 offset:4352\n" \
"v_mov_b32 v74, 0x3f80\n" \
"v_mfma_f32_16x16x32_bf16 v[0:3], a[100:103], a[68:71], v[0:3]\n" \
"s_add_u32 s48, s86, 32768\n" \
"s_cmp_eq_u32 s48, 163840\n" \
"s_cselect_b32 s48, 0, s48\n" \
"ds_read_b128 a[92:95], v213 offset:6400\n" \
"v_mfma_f32_16x16x32_bf16 v[4:7], a[104:107], a[64:67], v[4:7]\n" \
"ds_read_b64_tr_b16 a[112:113], v214 offset:16896\n" \
"v_mov_b32 v64, 0\n" \
"v_mfma_f32_16x16x32_bf16 v[4:7], a[108:111], a[68:71], v[4:7]\n" \
"ds_read_b64_tr_b16 a[114:115], v214 offset:18944\n" \
"v_mov_b32 v65, 0\n" \
"v_mfma_f32_16x16x32_bf16 v[20:23], a[104:107], a[72:75], v[20:23]\n" \
"s_add_u32 s49, s48, 32768\n" \
"s_cmp_eq_u32 s49, 163840\n" \
"s_cselect_b32 s49, 0, s49\n" \
"v_mfma_f32_16x16x32_bf16 v[20:23], a[108:111], a[76:79], v[20:23]\n" \
"ds_read_b64_tr_b16 a[116:117], v214 offset:25088\n" \
"v_add_u32 v224, s49, v211\n" \
"v_mfma_f32_16x16x32_bf16 v[16:19], a[96:99], a[72:75], v[16:19]\n" \
"ds_read_b64_tr_b16 a[118:119], v214 offset:27136\n" \
"v_add_u32 v225, s49, v212\n" \
"v_mfma_f32_16x16x32_bf16 v[16:19], a[100:103], a[76:79], v[16:19]\n" \
"s_waitcnt lgkmcnt(3)\n" \
"ds_read_b64_tr_b16 a[120:121], v215 offset:16896\n" \
"v_xor_b32 v226, 32, v225\n" \
"v_mfma_f32_16x16x32_bf16 v[32:35], a[96:99], a[80:83], v[32:35]\n" \
"v_add_u32 v227, s48, v211\n" \
"v_add_u32 v228, s48, v212\n" \
"v_mfma_f32_16x16x32_bf16 v[32:35], a[100:103], a[84:87], v[32:35]\n" \
"s_sub_u32 s85, s85, 32768\n" \
"s_cselect_b32 s85, 131072, s85\n" \
"ds_read_b64_tr_b16 a[122:123], v215 offset:18944\n" \
"v_mfma_f32_16x16x32_bf16 v[36:39], a[104:107], a[80:83], v[36:39]\n" \
"ds_read_b64_tr_b16 a[124:125], v215 offset:25088\n" \
"v_add_u32 v213, s85, v211\n" \
"v_mfma_f32_16x16x32_bf16 v[36:39], a[108:111], a[84:87], v[36:39]\n" \
"ds_read_b64_tr_b16 a[126:127], v215 offset:27136\n" \
"v_xor_b32 v229, 32, v228\n" \
"v_mfma_f32_16x16x32_bf16 v[52:55], a[104:107], a[88:91], v[52:55]\n" \
"v_mov_b32 v68, 0\n" \
"v_mov_b32 v69, 0\n" \
"v_mfma_f32_16x16x32_bf16 v[52:55], a[108:111], a[92:95], v[52:55]\n" \
"v_add_u32 v214, s85, v212\n" \
"v_xor_b32 v215, 32, v214\n" \
"v_mfma_f32_16x16x32_bf16 v[48:51], a[96:99], a[88:91], v[48:51]\n" \
"v_lshrrev_b32_e64 v72, 8, v234\n" \
"v_bfe_u32 v73, v234, 6, 2\n" \
"v_mfma_f32_16x16x32_bf16 v[48:51], a[100:103], a[92:95], v[48:51]\n" \
"s_waitcnt vmcnt(8) lgkmcnt(0)\n" \
"s_barrier\n" \
"s_waitcnt vmcnt(16)\n" \
"v_cmp_eq_u32 vcc, s60, v220\n" \
"s_nop 1\n" \
"s_andn2_b32 s70, s69, vcc_lo\n" \
"s_cbranch_scc1 Lpl7_%=\n" \
"Lpr7_%=:\n" \
"v_mfma_f32_16x16x32_bf16 v[8:11], a[112:115], a[64:67], v[8:11]\n" \
"s_add_u32 s71, s87, 0\n" \
"ds_read_b128 a[0:3], v213 offset:0\n" \
"ds_read_b128 a[4:7], v213 offset:2048\n" \
"v_mfma_f32_16x16x32_bf16 v[8:11], a[116:119], a[68:71], v[8:11]\n" \
"buffer_load_dwordx4 v[176:179], v234, s[56:59], s71 offen offset:0 sc1\n" \
"v_mfma_f32_16x16x32_bf16 v[12:15], a[120:123], a[64:67], v[12:15]\n" \
"ds_read_b64_tr_b16 a[128:129], v214 offset:16384\n" \
"ds_read_b64_tr_b16 a[130:131], v214 offset:18432\n" \
"v_mfma_f32_16x16x32_bf16 v[12:15], a[124:127], a[68:71], v[12:15]\n" \
"buffer_load_dwordx4 v[180:183], v234, s[56:59], s71 offen offset:1024 sc1\n" \
"v_mfma_f32_16x16x32_bf16 v[28:31], a[120:123], a[72:75], v[28:31]\n" \
"ds_read_b64_tr_b16 a[132:133], v214 offset:24576\n" \
"ds_read_b64_tr_b16 a[134:135], v214 offset:26624\n" \
"v_mfma_f32_16x16x32_bf16 v[28:31], a[124:127], a[76:79], v[28:31]\n" \
"buffer_load_dwordx4 v[184:187], v234, s[56:59], s71 offen offset:2048 sc1\n" \
"v_mfma_f32_16x16x32_bf16 v[24:27], a[112:115], a[72:75], v[24:27]\n" \
"ds_read_b64_tr_b16 a[136:137], v215 offset:16384\n" \
"ds_read_b64_tr_b16 a[138:139], v215 offset:18432\n" \
"v_mfma_f32_16x16x32_bf16 v[24:27], a[116:119], a[76:79], v[24:27]\n" \
"buffer_load_dwordx4 v[188:191], v234, s[56:59], s71 offen offset:3072 sc1\n" \
"v_mfma_f32_16x16x32_bf16 v[40:43], a[112:115], a[80:83], v[40:43]\n" \
"ds_read_b64_tr_b16 a[140:141], v215 offset:24576\n" \
"ds_read_b64_tr_b16 a[142:143], v215 offset:26624\n" \
"v_mfma_f32_16x16x32_bf16 v[40:43], a[116:119], a[84:87], v[40:43]\n" \
"ds_read_b64_tr_b16 a[144:145], v214 offset:16896\n" \
"ds_read_b64_tr_b16 a[146:147], v214 offset:18944\n" \
"v_mfma_f32_16x16x32_bf16 v[44:47], a[120:123], a[80:83], v[44:47]\n" \
"ds_read_b64_tr_b16 a[148:149], v214 offset:25088\n" \
"ds_read_b64_tr_b16 a[150:151], v214 offset:27136\n" \
"v_mfma_f32_16x16x32_bf16 v[44:47], a[124:127], a[84:87], v[44:47]\n" \
"ds_read_b64_tr_b16 a[152:153], v215 offset:16896\n" \
"ds_read_b64_tr_b16 a[154:155], v215 offset:18944\n" \
"v_mfma_f32_16x16x32_bf16 v[60:63], a[120:123], a[88:91], v[60:63]\n" \
"ds_read_b64_tr_b16 a[156:157], v215 offset:25088\n" \
"ds_read_b64_tr_b16 a[158:159], v215 offset:27136\n" \
"v_mfma_f32_16x16x32_bf16 v[60:63], a[124:127], a[92:95], v[60:63]\n" \
"ds_read_b128 a[24:27], v213 offset:256\n" \
"ds_read_b128 a[28:31], v213 offset:2304\n" \
"v_mfma_f32_16x16x32_bf16 v[56:59], a[112:115], a[88:91], v[56:59]\n" \
"ds_read_b128 a[8:11], v224 offset:0\n" \
"ds_read_b128 a[12:15], v224 offset:2048\n" \
"v_mfma_f32_16x16x32_bf16 v[56:59], a[116:119], a[92:95], v[56:59]\n" \
"s_waitcnt lgkmcnt(8)\n" \
"ds_read_b64_tr_b16 a[160:161], v225 offset:16384\n" \
"ds_read_b64_tr_b16 a[162:163], v225 offset:18432\n" \
"v_mfma_f32_16x16x32_bf16 v[0:3], a[128:131], a[0:3], v[0:3]\n" \
"ds_read_b64_tr_b16 a[164:165], v225 offset:24576\n" \
"ds_read_b64_tr_b16 a[166:167], v225 offset:26624\n" \
"v_mfma_f32_16x16x32_bf16 v[0:3], a[132:135], a[4:7], v[0:3]\n" \
"ds_read_b64_tr_b16 a[168:169], v226 offset:16384\n" \
"ds_read_b64_tr_b16 a[170:171], v226 offset:18432\n" \
"v_mfma_f32_16x16x32_bf16 v[4:7], a[136:139], a[0:3], v[4:7]\n" \
"s_mov_b32 s70, 0x20000000\n" \
"ds_read_b64_tr_b16 a[172:173], v226 offset:24576\n" \
"ds_read_b64_tr_b16 a[174:175], v226 offset:26624\n" \
"v_mfma_f32_16x16x32_bf16 v[4:7], a[140:143], a[4:7], v[4:7]\n" \
"s_cmp_eq_u32 s64, 65616896\n" \
"s_cselect_b32 s70, 65593344, s70\n" \
"ds_read_b64_tr_b16 a[176:177], v225 offset:16896\n" \
"ds_read_b64_tr_b16 a[178:179], v225 offset:18944\n" \
"buffer_store_dword v230, v231, s[56:59], s70 offen sc1\n" \
"v_mfma_f32_16x16x32_bf16 v[8:11], a[144:147], a[0:3], v[8:11]\n" \
"s_waitcnt lgkmcnt(8)\n" \
"ds_read_b64_tr_b16 a[180:181], v225 offset:25088\n" \
"ds_read_b64_tr_b16 a[182:183], v225 offset:27136\n" \
"v_mfma_f32_16x16x32_bf16 v[8:11], a[148:151], a[4:7], v[8:11]\n" \
"s_sub_u32 s68, s86, 32768\n" \
"s_cselect_b32 s68, 131072, s68\n" \
"ds_read_b64_tr_b16 a[184:185], v226 offset:16896\n" \
"ds_read_b64_tr_b16 a[186:187], v226 offset:18944\n" \
"v_mfma_f32_16x16x32_bf16 v[12:15], a[152:155], a[0:3], v[12:15]\n" \
"ds_read_b64_tr_b16 a[188:189], v226 offset:25088\n" \
"ds_read_b64_tr_b16 a[190:191], v226 offset:27136\n" \
"v_mfma_f32_16x16x32_bf16 v[12:15], a[156:159], a[4:7], v[12:15]\n" \
"s_cmp_lt_u32 s61, 2\n" \
"s_cselect_b32 s68, s86, s68\n" \
"v_cmp_eq_u32 s[16:17], v72, v73\n" \
"v_and_b32 v72, 16, v234\n" \
"v_mfma_f32_16x16x32_bf16 v[28:31], a[152:155], a[24:27], v[28:31]\n" \
"s_and_b32 s50, s61, 1\n" \
"v_lshlrev_b32 v72, v72, v74\n" \
"v_cndmask_b32 v72, 0, v72, s[16:17]\n" \
"v_mfma_f32_16x16x32_bf16 v[28:31], a[156:159], a[28:31], v[28:31]\n" \
"s_mul_i32 s50, s50, 9216\n" \
"v_bfe_i32 v73, v234, 5, 1\n" \
"v_and_b32 v67, v72, v73\n" \
"v_mfma_f32_16x16x32_bf16 v[24:27], a[144:147], a[24:27], v[24:27]\n" \
"s_add_u32 s68, s68, s50\n" \
"s_add_u32 s68, s68, %3\n" \
"v_xor_b32 v66, v72, v67\n" \
"v_mfma_f32_16x16x32_bf16 v[24:27], a[148:151], a[28:31], v[24:27]\n" \
"v_lshrrev_b32 v132, 4, v234\n" \
"v_and_b32 v133, 15, v132\n" \
"v_mul_u32_u24 v128, 144, v133\n" \
"v_mfma_f32_16x16x32_bf16 v[20:23], a[136:139], a[24:27], v[20:23]\n" \
"s_waitcnt vmcnt(5)\n" \
"s_barrier\n" \
"v_mfma_f32_16x16x32_bf16 v[20:23], a[140:143], a[28:31], v[20:23]\n" \
"s_add_u32 s71, s87, 4096\n" \
"ds_read_b128 a[32:35], v224 offset:256\n" \
"ds_read_b128 a[36:39], v224 offset:2304\n" \
"v_mfma_f32_16x16x32_bf16 v[16:19], a[128:131], a[24:27], v[16:19]\n" \
"buffer_load_dwordx4 v[192:195], v234, s[56:59], s71 offen offset:0 sc1\n" \
"v_mfma_f32_16x16x32_bf16 v[16:19], a[132:135], a[28:31], v[16:19]\n" \
"s_waitcnt lgkmcnt(2)\n" \
"ds_read_b128 a[16:19], v227 offset:0\n" \
"ds_read_b128 a[20:23], v227 offset:2048\n" \
"v_mfma_f32_16x16x32_bf16 v[0:3], a[160:163], a[8:11], v[0:3]\n" \
"ds_read_b64_tr_b16 a[192:193], v228 offset:16384\n" \
"ds_read_b64_tr_b16 a[194:195], v228 offset:18432\n" \
"v_mfma_f32_16x16x32_bf16 v[0:3], a[164:167], a[12:15], v[0:3]\n" \
"buffer_load_dwordx4 v[196:199], v234, s[56:59], s71 offen offset:1024 sc1\n" \
"v_mfma_f32_16x16x32_bf16 v[4:7], a[168:171], a[8:11], v[4:7]\n" \
"ds_read_b64_tr_b16 a[196:197], v228 offset:24576\n" \
"ds_read_b64_tr_b16 a[198:199], v228 offset:26624\n" \
"v_mfma_f32_16x16x32_bf16 v[4:7], a[172:175], a[12:15], v[4:7]\n" \
"ds_read_b64_tr_b16 a[200:201], v229 offset:16384\n" \
"ds_read_b64_tr_b16 a[202:203], v229 offset:18432\n" \
"v_mfma_f32_16x16x32_bf16 v[8:11], a[176:179], a[8:11], v[8:11]\n" \
"buffer_load_dwordx4 v[200:203], v234, s[56:59], s71 offen offset:2048 sc1\n" \
"v_mfma_f32_16x16x32_bf16 v[8:11], a[180:183], a[12:15], v[8:11]\n" \
"ds_read_b64_tr_b16 a[204:205], v229 offset:24576\n" \
"ds_read_b64_tr_b16 a[206:207], v229 offset:26624\n" \
"v_lshrrev_b32 v133, 4, v132\n" \
"v_mfma_f32_16x16x32_bf16 v[12:15], a[184:187], a[8:11], v[12:15]\n" \
"s_waitcnt lgkmcnt(6)\n" \
"ds_read_b64_tr_b16 a[208:209], v228 offset:16896\n" \
"ds_read_b64_tr_b16 a[210:211], v228 offset:18944\n" \
"v_mfma_f32_16x16x32_bf16 v[12:15], a[188:191], a[12:15], v[12:15]\n" \
"buffer_load_dwordx4 v[204:207], v234, s[56:59], s71 offen offset:3072 sc1\n" \
"v_mfma_f32_16x16x32_bf16 v[28:31], a[184:187], a[32:35], v[28:31]\n" \
"ds_read_b64_tr_b16 a[212:213], v228 offset:25088\n" \
"ds_read_b64_tr_b16 a[214:215], v228 offset:27136\n" \
"v_mfma_f32_16x16x32_bf16 v[28:31], a[188:191], a[36:39], v[28:31]\n" \
"ds_read_b64_tr_b16 a[216:217], v229 offset:16896\n" \
"ds_read_b64_tr_b16 a[218:219], v229 offset:18944\n" \
"v_mfma_f32_16x16x32_bf16 v[24:27], a[176:179], a[32:35], v[24:27]\n" \
"ds_read_b64_tr_b16 a[220:221], v229 offset:25088\n" \
"ds_read_b64_tr_b16 a[222:223], v229 offset:27136\n" \
"v_mfma_f32_16x16x32_bf16 v[24:27], a[180:183], a[36:39], v[24:27]\n" \
"ds_read_b128 a[40:43], v227 offset:256\n" \
"ds_read_b128 a[44:47], v227 offset:2304\n" \
"v_mfma_f32_16x16x32_bf16 v[20:23], a[168:171], a[32:35], v[20:23]\n" \
"ds_read_b128 a[248:251], v213 offset:4096\n" \
"ds_read_b128 a[252:255], v213 offset:6144\n" \
"v_mfma_f32_16x16x32_bf16 v[20:23], a[172:175], a[36:39], v[20:23]\n" \
"ds_read_b128 a[48:51], v224 offset:4096\n" \
"ds_read_b128 a[52:55], v224 offset:6144\n" \
"v_mfma_f32_16x16x32_bf16 v[16:19], a[160:163], a[32:35], v[16:19]\n" \
"ds_read_b128 a[56:59], v227 offset:4096\n" \
"ds_read_b128 a[60:63], v227 offset:6144\n" \
"v_mfma_f32_16x16x32_bf16 v[16:19], a[164:167], a[36:39], v[16:19]\n" \
"s_waitcnt lgkmcnt(8)\n" \
"ds_read_b128 a[224:227], v213 offset:4352\n" \
"ds_read_b128 a[228:231], v213 offset:6400\n" \
"v_lshl_add_u32 v128, v133, 3, v128\n" \
"v_mfma_f32_16x16x32_bf16 v[0:3], a[192:195], a[16:19], v[0:3]\n" \
"s_waitcnt vmcnt(7)\n" \
"ds_read_b128 a[232:235], v224 offset:4352\n" \
"ds_read_b128 a[236:239], v224 offset:6400\n" \
"v_mfma_f32_16x16x32_bf16 v[0:3], a[196:199], a[20:23], v[0:3]\n" \
"ds_read_b128 a[240:243], v227 offset:4352\n" \
"ds_read_b128 a[244:247], v227 offset:6400\n" \
"v_mfma_f32_16x16x32_bf16 v[0:3], v[66:69], v[176:179], v[0:3]\n" \
"v_add_u32 v128, s68, v128\n" \
"v_lshrrev_b32 v133, 3, v132\n" \
"v_mfma_f32_16x16x32_bf16 v[4:7], a[200:203], a[16:19], v[4:7]\n" \
"v_and_b32 v134, 7, v132\n" \
"v_mul_u32_u24 v129, 144, v133\n" \
"v_mfma_f32_16x16x32_bf16 v[4:7], a[204:207], a[20:23], v[4:7]\n" \
"v_lshl_add_u32 v129, v134, 4, v129\n" \
"v_add_u32 v129, s68, v129\n" \
"v_mfma_f32_16x16x32_bf16 v[4:7], v[64:67], v[176:179], v[4:7]\n" \
"v_lshlrev_b32 v134, 4, v134\n" \
"v_lshl_add_u32 v130, v133, 12, v134\n" \
"v_mfma_f32_16x16x32_bf16 v[8:11], a[208:211], a[16:19], v[8:11]\n" \
"v_mul_f32 v0, 4.0, v0\n" \
"v_mul_f32 v1, 4.0, v1\n" \
"v_mul_f32 v2, 4.0, v2\n" \
"v_mul_f32 v3, 4.0, v3\n" \
"v_cvt_pk_bf16_f32 v136, v0, v1\n" \
"v_cvt_pk_bf16_f32 v137, v2, v3\n" \
"v_mfma_f32_16x16x32_bf16 v[8:11], a[212:215], a[20:23], v[8:11]\n" \
"v_mfma_f32_16x16x32_bf16 v[8:11], v[66:69], v[180:183], v[8:11]\n" \
"ds_write_b64 v128, v[136:137] offset:0\n" \
"v_mfma_f32_16x16x32_bf16 v[12:15], a[216:219], a[16:19], v[12:15]\n" \
"v_mul_f32 v4, 4.0, v4\n" \
"v_mul_f32 v5, 4.0, v5\n" \
"v_mfma_f32_16x16x32_bf16 v[12:15], a[220:223], a[20:23], v[12:15]\n" \
"v_mul_f32 v6, 4.0, v6\n" \
"v_mul_f32 v7, 4.0, v7\n" \
"v_mfma_f32_16x16x32_bf16 v[12:15], v[64:67], v[180:183], v[12:15]\n" \
"s_waitcnt lgkmcnt(1)\n" \
"v_cvt_pk_bf16_f32 v138, v4, v5\n" \
"v_cvt_pk_bf16_f32 v139, v6, v7\n" \
"v_mfma_f32_16x16x32_bf16 v[16:19], a[192:195], a[40:43], v[16:19]\n" \
"s_waitcnt vmcnt(5)\n" \
"v_mfma_f32_16x16x32_bf16 v[16:19], a[196:199], a[44:47], v[16:19]\n" \
"v_mfma_f32_16x16x32_bf16 v[16:19], v[66:69], v[184:187], v[16:19]\n" \
"ds_write_b64 v128, v[138:139] offset:32\n" \
"v_mul_f32 v8, 4.0, v8\n" \
"v_mfma_f32_16x16x32_bf16 v[20:23], a[200:203], a[40:43], v[20:23]\n" \
"v_mul_f32 v9, 4.0, v9\n" \
"v_mul_f32 v10, 4.0, v10\n" \
"v_mfma_f32_16x16x32_bf16 v[20:23], a[204:207], a[44:47], v[20:23]\n" \
"v_mul_f32 v11, 4.0, v11\n" \
"v_cvt_pk_bf16_f32 v136, v8, v9\n" \
"v_mfma_f32_16x16x32_bf16 v[20:23], v[64:67], v[184:187], v[20:23]\n" \
"v_cvt_pk_bf16_f32 v137, v10, v11\n" \
"v_mfma_f32_16x16x32_bf16 v[24:27], a[208:211], a[40:43], v[24:27]\n" \
"v_mfma_f32_16x16x32_bf16 v[24:27], a[212:215], a[44:47], v[24:27]\n" \
"v_mfma_f32_16x16x32_bf16 v[24:27], v[66:69], v[188:191], v[24:27]\n" \
"ds_write_b64 v128, v[136:137] offset:64\n" \
"v_mul_f32 v12, 4.0, v12\n" \
"v_mfma_f32_16x16x32_bf16 v[28:31], a[216:219], a[40:43], v[28:31]\n" \
"v_mul_f32 v13, 4.0, v13\n" \
"v_mul_f32 v14, 4.0, v14\n" \
"v_mfma_f32_16x16x32_bf16 v[28:31], a[220:223], a[44:47], v[28:31]\n" \
"v_mul_f32 v15, 4.0, v15\n" \
"v_cvt_pk_bf16_f32 v138, v12, v13\n" \
"v_mfma_f32_16x16x32_bf16 v[28:31], v[64:67], v[188:191], v[28:31]\n" \
"v_cvt_pk_bf16_f32 v139, v14, v15\n" \
"v_mfma_f32_16x16x32_bf16 v[32:35], a[128:131], a[248:251], v[32:35]\n" \
"ds_write_b64 v128, v[138:139] offset:96\n" \
"v_mfma_f32_16x16x32_bf16 v[32:35], a[132:135], a[252:255], v[32:35]\n" \
"ds_read_b128 v[140:143], v129 offset:0\n" \
"ds_read_b128 v[144:147], v129 offset:1152\n" \
"v_mfma_f32_16x16x32_bf16 v[32:35], a[160:163], a[48:51], v[32:35]\n" \
"s_waitcnt vmcnt(2)\n" \
"v_mul_f32 v16, 4.0, v16\n" \
"v_mul_f32 v17, 4.0, v17\n" \
"v_mfma_f32_16x16x32_bf16 v[32:35], a[164:167], a[52:55], v[32:35]\n" \
"v_mul_f32 v18, 4.0, v18\n" \
"v_mul_f32 v19, 4.0, v19\n" \
"v_mfma_f32_16x16x32_bf16 v[32:35], v[66:69], v[192:195], v[32:35]\n" \
"s_add_u32 s70, s88, 0\n" \
"s_waitcnt lgkmcnt(1)\n" \
"v_cvt_pk_bf16_f32 v136, v16, v17\n" \
"v_mfma_f32_16x16x32_bf16 v[32:35], a[192:195], a[56:59], v[32:35]\n" \
"buffer_store_dwordx4 v[140:143], v130, s[52:55], s70 offen sc1\n" \
"v_cvt_pk_bf16_f32 v137, v18, v19\n" \
"v_mfma_f32_16x16x32_bf16 v[32:35], a[196:199], a[60:63], v[32:35]\n" \
"ds_write_b64 v128, v[136:137] offset:2304\n" \
"v_mfma_f32_16x16x32_bf16 v[36:39], a[204:207], a[60:63], v[36:39]\n" \
"s_add_u32 s70, s88, 32768\n" \
"s_waitcnt lgkmcnt(1)\n" \
"v_mul_f32 v20, 4.0, v20\n" \
"v_mfma_f32_16x16x32_bf16 v[36:39], a[200:203], a[56:59], v[36:39]\n" \
"buffer_store_dwordx4 v[144:147], v130, s[52:55], s70 offen sc1\n" \
"v_mfma_f32_16x16x32_bf16 v[36:39], a[172:175], a[52:55], v[36:39]\n" \
"v_mul_f32 v21, 4.0, v21\n" \
"v_mul_f32 v22, 4.0, v22\n" \
"v_mfma_f32_16x16x32_bf16 v[36:39], a[168:171], a[48:51], v[36:39]\n" \
"v_mul_f32 v23, 4.0, v23\n" \
"v_cvt_pk_bf16_f32 v138, v20, v21\n" \
"v_mfma_f32_16x16x32_bf16 v[36:39], v[64:67], v[192:195], v[36:39]\n" \
"v_cvt_pk_bf16_f32 v139, v22, v23\n" \
"v_mfma_f32_16x16x32_bf16 v[36:39], a[140:143], a[252:255], v[36:39]\n" \
"v_mfma_f32_16x16x32_bf16 v[36:39], a[136:139], a[248:251], v[36:39]\n" \
"ds_write_b64 v128, v[138:139] offset:2336\n" \
"v_mul_f32 v24, 4.0, v24\n" \
"v_mul_f32 v25, 4.0, v25\n" \
"v_mfma_f32_16x16x32_bf16 v[40:43], a[144:147], a[248:251], v[40:43]\n" \
"v_mul_f32 v26, 4.0, v26\n" \
"v_mul_f32 v27, 4.0, v27\n" \
"v_mfma_f32_16x16x32_bf16 v[40:43], a[148:151], a[252:255], v[40:43]\n" \
"v_cvt_pk_bf16_f32 v136, v24, v25\n" \
"v_cvt_pk_bf16_f32 v137, v26, v27\n" \
"v_mfma_f32_16x16x32_bf16 v[40:43], a[176:179], a[48:51], v[40:43]\n" \
"ds_write_b64 v128, v[136:137] offset:2368\n" \
"v_mfma_f32_16x16x32_bf16 v[40:43], a[180:183], a[52:55], v[40:43]\n" \
"v_mul_f32 v28, 4.0, v28\n" \
"v_mul_f32 v29, 4.0, v29\n" \
"v_mfma_f32_16x16x32_bf16 v[40:43], v[66:69], v[196:199], v[40:43]\n" \
"v_mul_f32 v30, 4.0, v30\n" \
"v_mul_f32 v31, 4.0, v31\n" \
"v_mfma_f32_16x16x32_bf16 v[40:43], a[208:211], a[56:59], v[40:43]\n" \
"v_cvt_pk_bf16_f32 v138, v28, v29\n" \
"v_cvt_pk_bf16_f32 v139, v30, v31\n" \
"v_mfma_f32_16x16x32_bf16 v[40:43], a[212:215], a[60:63], v[40:43]\n" \
"ds_write_b64 v128, v[138:139] offset:2400\n" \
"v_mfma_f32_16x16x32_bf16 v[44:47], a[220:223], a[60:63], v[44:47]\n" \
"ds_read_b128 v[148:151], v129 offset:2304\n" \
"ds_read_b128 v[152:155], v129 offset:3456\n" \
"v_mfma_f32_16x16x32_bf16 v[44:47], a[216:219], a[56:59], v[44:47]\n" \
"v_mul_f32 v32, 4.0, v32\n" \
"v_mul_f32 v33, 4.0, v33\n" \
"v_mfma_f32_16x16x32_bf16 v[44:47], a[188:191], a[52:55], v[44:47]\n" \
"v_mul_f32 v34, 4.0, v34\n" \
"v_mul_f32 v35, 4.0, v35\n" \
"v_mfma_f32_16x16x32_bf16 v[44:47], a[184:187], a[48:51], v[44:47]\n" \
"s_add_u32 s70, s88, 65536\n" \
"s_waitcnt lgkmcnt(1)\n" \
"v_cvt_pk_bf16_f32 v136, v32, v33\n" \
"v_mfma_f32_16x16x32_bf16 v[44:47], v[64:67], v[196:199], v[44:47]\n" \
"buffer_store_dwordx4 v[148:151], v130, s[52:55], s70 offen sc1\n" \
"v_cvt_pk_bf16_f32 v137, v34, v35\n" \
"v_mfma_f32_16x16x32_bf16 v[44:47], a[156:159], a[252:255], v[44:47]\n" \
"v_mfma_f32_16x16x32_bf16 v[44:47], a[152:155], a[248:251], v[44:47]\n" \
"s_add_u32 s70, s88, 98304\n" \
"s_waitcnt lgkmcnt(0)\n" \
"ds_write_b64 v128, v[136:137] offset:4608\n" \
"v_mul_f32 v36, 4.0, v36\n" \
"Lpg10845_%=:\n" \
"s_cbranch_execz Lcb1_%=\n" \
"v_mfma_f32_16x16x32_bf16 v[48:51], a[128:131], a[224:227], v[48:51]\n" \
"buffer_store_dwordx4 v[152:155], v130, s[52:55], s70 offen sc1\n" \
"v_mul_f32 v37, 4.0, v37\n" \
"v_mfma_f32_16x16x32_bf16 v[48:51], a[132:135], a[228:231], v[48:51]\n" \
"v_mul_f32 v38, 4.0, v38\n" \
"v_mul_f32 v39, 4.0, v39\n" \
"v_mfma_f32_16x16x32_bf16 v[48:51], a[160:163], a[232:235], v[48:51]\n" \
"s_waitcnt vmcnt(4)\n" \
"v_cvt_pk_bf16_f32 v138, v36, v37\n" \
"v_cvt_pk_bf16_f32 v139, v38, v39\n" \
"v_mfma_f32_16x16x32_bf16 v[48:51], a[164:167], a[236:239], v[48:51]\n" \
"ds_write_b64 v128, v[138:139] offset:4640\n" \
"v_mul_f32 v40, 4.0, v40\n" \
"v_mfma_f32_16x16x32_bf16 v[48:51], v[66:69], v[200:203], v[48:51]\n" \
"v_mul_f32 v41, 4.0, v41\n" \
"v_mul_f32 v42, 4.0, v42\n" \
"v_mfma_f32_16x16x32_bf16 v[48:51], a[192:195], a[240:243], v[48:51]\n" \
"v_mul_f32 v43, 4.0, v43\n" \
"v_cvt_pk_bf16_f32 v136, v40, v41\n" \
"v_mfma_f32_16x16x32_bf16 v[48:51], a[196:199], a[244:247], v[48:51]\n" \
"v_cvt_pk_bf16_f32 v137, v42, v43\n" \
"v_mfma_f32_16x16x32_bf16 v[52:55], a[204:207], a[244:247], v[52:55]\n" \
"ds_write_b64 v128, v[136:137] offset:4672\n" \
"v_mul_f32 v44, 4.0, v44\n" \
"v_mul_f32 v45, 4.0, v45\n" \
"v_mfma_f32_16x16x32_bf16 v[52:55], a[200:203], a[240:243], v[52:55]\n" \
"v_mul_f32 v46, 4.0, v46\n" \
"v_mul_f32 v47, 4.0, v47\n" \
"v_mfma_f32_16x16x32_bf16 v[52:55], a[172:175], a[236:239], v[52:55]\n" \
"v_cvt_pk_bf16_f32 v138, v44, v45\n" \
"v_cvt_pk_bf16_f32 v139, v46, v47\n" \
"v_mfma_f32_16x16x32_bf16 v[52:55], a[168:171], a[232:235], v[52:55]\n" \
"ds_write_b64 v128, v[138:139] offset:4704\n" \
"v_mfma_f32_16x16x32_bf16 v[52:55], v[64:67], v[200:203], v[52:55]\n" \
"ds_read_b128 v[156:159], v129 offset:4608\n" \
"ds_read_b128 v[160:163], v129 offset:5760\n" \
"v_mfma_f32_16x16x32_bf16 v[52:55], a[140:143], a[228:231], v[52:55]\n" \
"v_mul_f32 v48, 4.0, v48\n" \
"v_mul_f32 v49, 4.0, v49\n" \
"v_mfma_f32_16x16x32_bf16 v[52:55], a[136:139], a[224:227], v[52:55]\n" \
"v_mul_f32 v50, 4.0, v50\n" \
"v_mul_f32 v51, 4.0, v51\n" \
"v_mfma_f32_16x16x32_bf16 v[56:59], a[144:147], a[224:227], v[56:59]\n" \
"s_add_u32 s70, s88, 131072\n" \
"s_waitcnt lgkmcnt(1)\n" \
"v_cvt_pk_bf16_f32 v136, v48, v49\n" \
"v_mfma_f32_16x16x32_bf16 v[56:59], a[148:151], a[228:231], v[56:59]\n" \
"buffer_store_dwordx4 v[156:159], v130, s[52:55], s70 offen sc1\n" \
"v_cvt_pk_bf16_f32 v137, v50, v51\n" \
"v_mfma_f32_16x16x32_bf16 v[56:59], a[176:179], a[232:235], v[56:59]\n" \
"ds_write_b64 v128, v[136:137] offset:6912\n" \
"v_mfma_f32_16x16x32_bf16 v[56:59], a[180:183], a[236:239], v[56:59]\n" \
"s_add_u32 s70, s88, 163840\n" \
"s_waitcnt lgkmcnt(1)\n" \
"v_mul_f32 v52, 4.0, v52\n" \
"v_mfma_f32_16x16x32_bf16 v[56:59], v[66:69], v[204:207], v[56:59]\n" \
"buffer_store_dwordx4 v[160:163], v130, s[52:55], s70 offen sc1\n" \
"v_mfma_f32_16x16x32_bf16 v[56:59], a[208:211], a[240:243], v[56:59]\n" \
"v_mul_f32 v53, 4.0, v53\n" \
"v_mul_f32 v54, 4.0, v54\n" \
"v_mfma_f32_16x16x32_bf16 v[56:59], a[212:215], a[244:247], v[56:59]\n" \
"v_mul_f32 v55, 4.0, v55\n" \
"v_cvt_pk_bf16_f32 v138, v52, v53\n" \
"v_cvt_pk_bf16_f32 v139, v54, v55\n" \
"v_mfma_f32_16x16x32_bf16 v[60:63], a[220:223], a[244:247], v[60:63]\n" \
"ds_write_b64 v128, v[138:139] offset:6944\n" \
"v_mfma_f32_16x16x32_bf16 v[60:63], a[216:219], a[240:243], v[60:63]\n" \
"v_mfma_f32_16x16x32_bf16 v[60:63], a[188:191], a[236:239], v[60:63]\n" \
"v_mfma_f32_16x16x32_bf16 v[60:63], a[184:187], a[232:235], v[60:63]\n" \
"v_mul_f32 v56, 4.0, v56\n" \
"v_mul_f32 v57, 4.0, v57\n" \
"v_mfma_f32_16x16x32_bf16 v[60:63], v[64:67], v[204:207], v[60:63]\n" \
"v_mul_f32 v58, 4.0, v58\n" \
"v_mul_f32 v59, 4.0, v59\n" \
"v_mfma_f32_16x16x32_bf16 v[60:63], a[156:159], a[228:231], v[60:63]\n" \
"v_cvt_pk_bf16_f32 v136, v56, v57\n" \
"v_cvt_pk_bf16_f32 v137, v58, v59\n" \
"v_mfma_f32_16x16x32_bf16 v[60:63], a[152:155], a[224:227], v[60:63]\n" \
"ds_write_b64 v128, v[136:137] offset:6976\n" \
"s_nop 7\n" \
"v_mul_f32 v60, 4.0, v60\n" \
"v_mul_f32 v61, 4.0, v61\n" \
"v_mul_f32 v62, 4.0, v62\n" \
"v_mul_f32 v63, 4.0, v63\n" \
"v_cvt_pk_bf16_f32 v138, v60, v61\n" \
"v_cvt_pk_bf16_f32 v139, v62, v63\n" \
"ds_write_b64 v128, v[138:139] offset:7008\n" \
"ds_read_b128 v[164:167], v129 offset:6912\n" \
"ds_read_b128 v[168:171], v129 offset:8064\n" \
"s_add_u32 s70, s88, 196608\n" \
"s_waitcnt lgkmcnt(1)\n" \
"buffer_store_dwordx4 v[164:167], v130, s[52:55], s70 offen sc1\n" \
"s_add_u32 s70, s88, 229376\n" \
"s_waitcnt lgkmcnt(0)\n" \
"buffer_store_dwordx4 v[168:171], v130, s[52:55], s70 offen sc1\n" \
"s_endpgm\n" \
".p2align 12\n" \
"Lrole1_%=:\n" \
"v_mfma_f32_16x16x32_bf16 v[64:67], a[96:99], a[64:67], 0\n" \
"Lpg10945_%=:\n" \
"s_cbranch_execz Lcb1_%=\n" \
"s_waitcnt lgkmcnt(1)\n" \
"s_xor_b32 s82, s48, s49\n" \
"ds_read_b128 a[88:91], v213 offset:4352\n" \
"v_mfma_f32_16x16x32_bf16 v[64:67], a[100:103], a[68:71], v[64:67]\n" \
"s_cmp_lt_u32 s48, s50\n" \
"s_cselect_b32 s82, s82, s48\n" \
"ds_read_b128 a[92:95], v213 offset:6400\n" \
"v_mfma_f32_16x16x32_bf16 v[68:71], a[104:107], a[64:67], 0\n" \
"s_add_u32 s81, s82, s46\n" \
"ds_read_b64_tr_b16 a[112:113], v214 offset:16896\n" \
"v_mfma_f32_16x16x32_bf16 v[68:71], a[108:111], a[68:71], v[68:71]\n" \
"s_add_u32 m0, s86, s79\n" \
"ds_read_b64_tr_b16 a[114:115], v214 offset:18944\n" \
"v_mfma_f32_16x16x32_bf16 v[84:87], a[104:107], a[72:75], 0\n" \
"buffer_load_dwordx4 v234, s[56:59], s81 offen offset:0 sc1 lds\n" \
"v_mfma_f32_16x16x32_bf16 v[84:87], a[108:111], a[76:79], v[84:87]\n" \
"s_waitcnt lgkmcnt(3)\n" \
"ds_read_b64_tr_b16 a[116:117], v214 offset:25088\n" \
"v_mfma_f32_16x16x32_bf16 v[80:83], a[96:99], a[72:75], 0\n" \
"s_lshl_b32 s82, s82, 5\n" \
"ds_read_b64_tr_b16 a[118:119], v214 offset:27136\n" \
"v_cvt_pk_bf16_f32 v0, v0, v1\n" \
"v_mfma_f32_16x16x32_bf16 v[80:83], a[100:103], a[76:79], v[80:83]\n" \
"s_add_u32 s82, s82, s47\n" \
"ds_read_b64_tr_b16 a[120:121], v215 offset:16896\n" \
"v_cvt_pk_bf16_f32 v1, v2, v3\n" \
"v_mfma_f32_16x16x32_bf16 v[96:99], a[96:99], a[80:83], 0\n" \
"buffer_load_dwordx4 v234, s[56:59], s81 offen offset:1024 sc1 lds\n" \
"v_mfma_f32_16x16x32_bf16 v[96:99], a[100:103], a[84:87], v[96:99]\n" \
"s_sub_u32 s85, s85, 32768\n" \
"s_cselect_b32 s85, 131072, s85\n" \
"ds_read_b64_tr_b16 a[122:123], v215 offset:18944\n" \
"v_cvt_pk_bf16_f32 v2, v4, v5\n" \
"v_mfma_f32_16x16x32_bf16 v[100:103], a[104:107], a[80:83], 0\n" \
"s_add_u32 s48, s48, 0x1000\n" \
"ds_read_b64_tr_b16 a[124:125], v215 offset:25088\n" \
"v_add_u32 v213, s85, v211\n" \
"v_mfma_f32_16x16x32_bf16 v[100:103], a[108:111], a[84:87], v[100:103]\n" \
"s_waitcnt lgkmcnt(3)\n" \
"s_sub_u32 s86, s86, 32768\n" \
"s_cselect_b32 s86, 131072, s86\n" \
"ds_read_b64_tr_b16 a[126:127], v215 offset:27136\n" \
"v_mfma_f32_16x16x32_bf16 v[116:119], a[104:107], a[88:91], 0\n" \
"buffer_load_dwordx4 v234, s[56:59], s81 offen offset:2048 sc1 lds\n" \
"v_mfma_f32_16x16x32_bf16 v[116:119], a[108:111], a[92:95], v[116:119]\n" \
"v_add_u32 v214, s85, v212\n" \
"v_xor_b32 v215, 32, v214\n" \
"v_cvt_pk_bf16_f32 v3, v6, v7\n" \
"v_mfma_f32_16x16x32_bf16 v[112:115], a[96:99], a[88:91], 0\n" \
"buffer_load_dwordx4 v234, s[56:59], s81 offen offset:3072 sc1 lds\n" \
"v_mfma_f32_16x16x32_bf16 v[112:115], a[100:103], a[92:95], v[112:115]\n" \
"s_waitcnt vmcnt(20) lgkmcnt(0)\n" \
"s_barrier\n" \
"v_mfma_f32_16x16x32_bf16 v[72:75], a[112:115], a[64:67], 0\n" \
"ds_read_b64_tr_b16 a[96:97], v214 offset:16384\n" \
"ds_read_b64_tr_b16 a[98:99], v214 offset:18432\n" \
"v_mfma_f32_16x16x32_bf16 v[72:75], a[116:119], a[68:71], v[72:75]\n" \
"s_add_u32 m0, m0, 0x4000\n" \
"ds_read_b64_tr_b16 a[100:101], v214 offset:24576\n" \
"ds_read_b64_tr_b16 a[102:103], v214 offset:26624\n" \
"v_cvt_pk_bf16_f32 v4, v8, v9\n" \
"v_mfma_f32_16x16x32_bf16 v[76:79], a[120:123], a[64:67], 0\n" \
"buffer_load_dwordx4 v216, s[36:39], s82 offen offset:0 lds\n" \
"v_mfma_f32_16x16x32_bf16 v[76:79], a[124:127], a[68:71], v[76:79]\n" \
"ds_read_b128 a[64:67], v213 offset:0\n" \
"v_cvt_pk_bf16_f32 v5, v10, v11\n" \
"v_mfma_f32_16x16x32_bf16 v[92:95], a[120:123], a[72:75], 0\n" \
"ds_read_b128 a[68:71], v213 offset:2048\n" \
"v_cvt_pk_bf16_f32 v6, v12, v13\n" \
"v_mfma_f32_16x16x32_bf16 v[92:95], a[124:127], a[76:79], v[92:95]\n" \
"ds_read_b64_tr_b16 a[104:105], v215 offset:16384\n" \
"ds_read_b64_tr_b16 a[106:107], v215 offset:18432\n" \
"v_cvt_pk_bf16_f32 v7, v14, v15\n" \
"v_mfma_f32_16x16x32_bf16 v[88:91], a[112:115], a[72:75], 0\n" \
"buffer_load_dwordx4 v217, s[36:39], s82 offen offset:1024 lds\n" \
"v_mfma_f32_16x16x32_bf16 v[88:91], a[116:119], a[76:79], v[88:91]\n" \
"ds_read_b128 a[72:75], v213 offset:256\n" \
"v_cvt_pk_bf16_f32 v8, v16, v17\n" \
"v_mfma_f32_16x16x32_bf16 v[104:107], a[112:115], a[80:83], 0\n" \
"ds_read_b128 a[76:79], v213 offset:2304\n" \
"v_cvt_pk_bf16_f32 v9, v18, v19\n" \
"v_mfma_f32_16x16x32_bf16 v[104:107], a[116:119], a[84:87], v[104:107]\n" \
"ds_read_b64_tr_b16 a[108:109], v215 offset:24576\n" \
"ds_read_b64_tr_b16 a[110:111], v215 offset:26624\n" \
"v_cvt_pk_bf16_f32 v10, v20, v21\n" \
"v_mfma_f32_16x16x32_bf16 v[108:111], a[120:123], a[80:83], 0\n" \
"buffer_load_dwordx4 v218, s[36:39], s82 offen offset:2048 lds\n" \
"v_mfma_f32_16x16x32_bf16 v[108:111], a[124:127], a[84:87], v[108:111]\n" \
"s_add_u32 s71, s87, 0\n" \
"ds_read_b128 a[80:83], v213 offset:4096\n" \
"v_cvt_pk_bf16_f32 v11, v22, v23\n" \
"v_mfma_f32_16x16x32_bf16 v[124:127], a[120:123], a[88:91], 0\n" \
"s_waitcnt lgkmcnt(4)\n" \
"buffer_store_dwordx4 v[0:3], v234, s[56:59], s71 offen offset:0 sc1\n" \
"v_mfma_f32_16x16x32_bf16 v[124:127], a[124:127], a[92:95], v[124:127]\n" \
"ds_read_b128 a[84:87], v213 offset:6144\n" \
"v_cvt_pk_bf16_f32 v12, v24, v25\n" \
"v_mfma_f32_16x16x32_bf16 v[120:123], a[112:115], a[88:91], 0\n" \
"buffer_load_dwordx4 v219, s[36:39], s82 offen offset:3072 lds\n" \
"v_mfma_f32_16x16x32_bf16 v[120:123], a[116:119], a[92:95], v[120:123]\n" \
"buffer_store_dwordx4 v[4:7], v234, s[56:59], s71 offen offset:1024 sc1\n" \
"buffer_store_dwordx4 v[8:11], v234, s[56:59], s71 offen offset:2048 sc1\n" \
"v_mfma_f32_16x16x32_bf16 v[64:67], a[96:99], a[64:67], v[64:67]\n" \
"s_xor_b32 s82, s48, s49\n" \
"ds_read_b128 a[88:91], v213 offset:4352\n" \
"v_cvt_pk_bf16_f32 v13, v26, v27\n" \
"v_mfma_f32_16x16x32_bf16 v[64:67], a[100:103], a[68:71], v[64:67]\n" \
"s_waitcnt lgkmcnt(2)\n" \
"s_cmp_lt_u32 s48, s50\n" \
"s_cselect_b32 s82, s82, s48\n" \
"ds_read_b128 a[92:95], v213 offset:6400\n" \
"v_mfma_f32_16x16x32_bf16 v[68:71], a[104:107], a[64:67], v[68:71]\n" \
"s_add_u32 s81, s82, s46\n" \
"ds_read_b64_tr_b16 a[112:113], v214 offset:16896\n" \
"v_cvt_pk_bf16_f32 v14, v28, v29\n" \
"v_mfma_f32_16x16x32_bf16 v[68:71], a[108:111], a[68:71], v[68:71]\n" \
"s_add_u32 m0, s86, s79\n" \
"ds_read_b64_tr_b16 a[114:115], v214 offset:18944\n" \
"v_cvt_pk_bf16_f32 v15, v30, v31\n" \
"v_mfma_f32_16x16x32_bf16 v[84:87], a[104:107], a[72:75], v[84:87]\n" \
"buffer_load_dwordx4 v234, s[56:59], s81 offen offset:0 sc1 lds\n" \
"v_mfma_f32_16x16x32_bf16 v[84:87], a[108:111], a[76:79], v[84:87]\n" \
"s_waitcnt lgkmcnt(3)\n" \
"ds_read_b64_tr_b16 a[116:117], v214 offset:25088\n" \
"v_cvt_pk_bf16_f32 v16, v32, v33\n" \
"v_mfma_f32_16x16x32_bf16 v[80:83], a[96:99], a[72:75], v[80:83]\n" \
"s_lshl_b32 s82, s82, 5\n" \
"ds_read_b64_tr_b16 a[118:119], v214 offset:27136\n" \
"v_cvt_pk_bf16_f32 v17, v34, v35\n" \
"v_mfma_f32_16x16x32_bf16 v[80:83], a[100:103], a[76:79], v[80:83]\n" \
"s_add_u32 s82, s82, s47\n" \
"ds_read_b64_tr_b16 a[120:121], v215 offset:16896\n" \
"v_cvt_pk_bf16_f32 v18, v36, v37\n" \
"v_mfma_f32_16x16x32_bf16 v[96:99], a[96:99], a[80:83], v[96:99]\n" \
"buffer_load_dwordx4 v234, s[56:59], s81 offen offset:1024 sc1 lds\n" \
"v_mfma_f32_16x16x32_bf16 v[96:99], a[100:103], a[84:87], v[96:99]\n" \
"s_sub_u32 s85, s85, 32768\n" \
"s_cselect_b32 s85, 131072, s85\n" \
"ds_read_b64_tr_b16 a[122:123], v215 offset:18944\n" \
"v_mfma_f32_16x16x32_bf16 v[100:103], a[104:107], a[80:83], v[100:103]\n" \
"s_add_u32 s48, s48, 0x1000\n" \
"ds_read_b64_tr_b16 a[124:125], v215 offset:25088\n" \
"v_add_u32 v213, s85, v211\n" \
"v_mfma_f32_16x16x32_bf16 v[100:103], a[108:111], a[84:87], v[100:103]\n" \
"s_waitcnt lgkmcnt(3)\n" \
"s_sub_u32 s86, s86, 32768\n" \
"s_cselect_b32 s86, 131072, s86\n" \
"ds_read_b64_tr_b16 a[126:127], v215 offset:27136\n" \
"v_mfma_f32_16x16x32_bf16 v[116:119], a[104:107], a[88:91], v[116:119]\n" \
"buffer_load_dwordx4 v234, s[56:59], s81 offen offset:2048 sc1 lds\n" \
"v_mfma_f32_16x16x32_bf16 v[116:119], a[108:111], a[92:95], v[116:119]\n" \
"v_add_u32 v214, s85, v212\n" \
"v_xor_b32 v215, 32, v214\n" \
"v_mfma_f32_16x16x32_bf16 v[112:115], a[96:99], a[88:91], v[112:115]\n" \
"buffer_load_dwordx4 v234, s[56:59], s81 offen offset:3072 sc1 lds\n" \
"v_mfma_f32_16x16x32_bf16 v[112:115], a[100:103], a[92:95], v[112:115]\n" \
"s_waitcnt vmcnt(23) lgkmcnt(0)\n" \
"s_barrier\n" \
"v_mfma_f32_16x16x32_bf16 v[72:75], a[112:115], a[64:67], v[72:75]\n" \
"ds_read_b64_tr_b16 a[96:97], v214 offset:16384\n" \
"ds_read_b64_tr_b16 a[98:99], v214 offset:18432\n" \
"v_mfma_f32_16x16x32_bf16 v[72:75], a[116:119], a[68:71], v[72:75]\n" \
"s_add_u32 m0, m0, 0x4000\n" \
"ds_read_b64_tr_b16 a[100:101], v214 offset:24576\n" \
"ds_read_b64_tr_b16 a[102:103], v214 offset:26624\n" \
"v_mfma_f32_16x16x32_bf16 v[76:79], a[120:123], a[64:67], v[76:79]\n" \
"buffer_load_dwordx4 v216, s[36:39], s82 offen offset:0 lds\n" \
"v_mfma_f32_16x16x32_bf16 v[76:79], a[124:127], a[68:71], v[76:79]\n" \
"ds_read_b128 a[64:67], v213 offset:0\n" \
"v_cvt_pk_bf16_f32 v19, v38, v39\n" \
"v_mfma_f32_16x16x32_bf16 v[92:95], a[120:123], a[72:75], v[92:95]\n" \
"ds_read_b128 a[68:71], v213 offset:2048\n" \
"v_cvt_pk_bf16_f32 v20, v40, v41\n" \
"v_mfma_f32_16x16x32_bf16 v[92:95], a[124:127], a[76:79], v[92:95]\n" \
"ds_read_b64_tr_b16 a[104:105], v215 offset:16384\n" \
"ds_read_b64_tr_b16 a[106:107], v215 offset:18432\n" \
"v_mfma_f32_16x16x32_bf16 v[88:91], a[112:115], a[72:75], v[88:91]\n" \
"buffer_load_dwordx4 v217, s[36:39], s82 offen offset:1024 lds\n" \
"v_mfma_f32_16x16x32_bf16 v[88:91], a[116:119], a[76:79], v[88:91]\n" \
"ds_read_b128 a[72:75], v213 offset:256\n" \
"v_cvt_pk_bf16_f32 v21, v42, v43\n" \
"v_mfma_f32_16x16x32_bf16 v[104:107], a[112:115], a[80:83], v[104:107]\n" \
"ds_read_b128 a[76:79], v213 offset:2304\n" \
"v_cvt_pk_bf16_f32 v22, v44, v45\n" \
"v_mfma_f32_16x16x32_bf16 v[104:107], a[116:119], a[84:87], v[104:107]\n" \
"ds_read_b64_tr_b16 a[108:109], v215 offset:24576\n" \
"ds_read_b64_tr_b16 a[110:111], v215 offset:26624\n" \
"v_mfma_f32_16x16x32_bf16 v[108:111], a[120:123], a[80:83], v[108:111]\n" \
"buffer_load_dwordx4 v218, s[36:39], s82 offen offset:2048 lds\n" \
"v_mfma_f32_16x16x32_bf16 v[108:111], a[124:127], a[84:87], v[108:111]\n" \
"ds_read_b128 a[80:83], v213 offset:4096\n" \
"v_cvt_pk_bf16_f32 v23, v46, v47\n" \
"v_mfma_f32_16x16x32_bf16 v[124:127], a[120:123], a[88:91], v[124:127]\n" \
"s_waitcnt lgkmcnt(4)\n" \
"buffer_store_dwordx4 v[12:15], v234, s[56:59], s71 offen offset:3072 sc1\n" \
"v_mfma_f32_16x16x32_bf16 v[124:127], a[124:127], a[92:95], v[124:127]\n" \
"s_add_u32 s71, s87, 4096\n" \
"ds_read_b128 a[84:87], v213 offset:6144\n" \
"v_cvt_pk_bf16_f32 v24, v48, v49\n" \
"v_mfma_f32_16x16x32_bf16 v[120:123], a[112:115], a[88:91], v[120:123]\n" \
"buffer_load_dwordx4 v219, s[36:39], s82 offen offset:3072 lds\n" \
"v_mfma_f32_16x16x32_bf16 v[120:123], a[116:119], a[92:95], v[120:123]\n" \
"buffer_store_dwordx4 v[16:19], v234, s[56:59], s71 offen offset:0 sc1\n" \
"buffer_store_dwordx4 v[20:23], v234, s[56:59], s71 offen offset:1024 sc1\n" \
"v_mfma_f32_16x16x32_bf16 v[64:67], a[96:99], a[64:67], v[64:67]\n" \
"s_xor_b32 s82, s48, s49\n" \
"ds_read_b128 a[88:91], v213 offset:4352\n" \
"v_cvt_pk_bf16_f32 v25, v50, v51\n" \
"v_mfma_f32_16x16x32_bf16 v[64:67], a[100:103], a[68:71], v[64:67]\n" \
"s_waitcnt lgkmcnt(2)\n" \
"s_cmp_lt_u32 s48, s50\n" \
"s_cselect_b32 s82, s82, s48\n" \
"ds_read_b128 a[92:95], v213 offset:6400\n" \
"v_mfma_f32_16x16x32_bf16 v[68:71], a[104:107], a[64:67], v[68:71]\n" \
"s_add_u32 s81, s82, s46\n" \
"ds_read_b64_tr_b16 a[112:113], v214 offset:16896\n" \
"v_cvt_pk_bf16_f32 v26, v52, v53\n" \
"v_mfma_f32_16x16x32_bf16 v[68:71], a[108:111], a[68:71], v[68:71]\n" \
"s_add_u32 m0, s86, s79\n" \
"ds_read_b64_tr_b16 a[114:115], v214 offset:18944\n" \
"v_cvt_pk_bf16_f32 v27, v54, v55\n" \
"v_mfma_f32_16x16x32_bf16 v[84:87], a[104:107], a[72:75], v[84:87]\n" \
"buffer_load_dwordx4 v234, s[56:59], s81 offen offset:0 sc1 lds\n" \
"v_mfma_f32_16x16x32_bf16 v[84:87], a[108:111], a[76:79], v[84:87]\n" \
"s_waitcnt lgkmcnt(3)\n" \
"ds_read_b64_tr_b16 a[116:117], v214 offset:25088\n" \
"v_cvt_pk_bf16_f32 v28, v56, v57\n" \
"v_mfma_f32_16x16x32_bf16 v[80:83], a[96:99], a[72:75], v[80:83]\n" \
"s_lshl_b32 s82, s82, 5\n" \
"ds_read_b64_tr_b16 a[118:119], v214 offset:27136\n" \
"v_cvt_pk_bf16_f32 v29, v58, v59\n" \
"v_mfma_f32_16x16x32_bf16 v[80:83], a[100:103], a[76:79], v[80:83]\n" \
"s_add_u32 s82, s82, s47\n" \
"ds_read_b64_tr_b16 a[120:121], v215 offset:16896\n" \
"v_cvt_pk_bf16_f32 v30, v60, v61\n" \
"v_mfma_f32_16x16x32_bf16 v[96:99], a[96:99], a[80:83], v[96:99]\n" \
"buffer_load_dwordx4 v234, s[56:59], s81 offen offset:1024 sc1 lds\n" \
"v_mfma_f32_16x16x32_bf16 v[96:99], a[100:103], a[84:87], v[96:99]\n" \
"s_sub_u32 s85, s85, 32768\n" \
"s_cselect_b32 s85, 131072, s85\n" \
"ds_read_b64_tr_b16 a[122:123], v215 offset:18944\n" \
"v_mfma_f32_16x16x32_bf16 v[100:103], a[104:107], a[80:83], v[100:103]\n" \
"s_add_u32 s48, s48, 0x1000\n" \
"ds_read_b64_tr_b16 a[124:125], v215 offset:25088\n" \
"v_add_u32 v213, s85, v211\n" \
"v_mfma_f32_16x16x32_bf16 v[100:103], a[108:111], a[84:87], v[100:103]\n" \
"s_waitcnt lgkmcnt(3)\n" \
"s_sub_u32 s86, s86, 32768\n" \
"s_cselect_b32 s86, 131072, s86\n" \
"ds_read_b64_tr_b16 a[126:127], v215 offset:27136\n" \
"v_mfma_f32_16x16x32_bf16 v[116:119], a[104:107], a[88:91], v[116:119]\n" \
"buffer_load_dwordx4 v234, s[56:59], s81 offen offset:2048 sc1 lds\n" \
"v_mfma_f32_16x16x32_bf16 v[116:119], a[108:111], a[92:95], v[116:119]\n" \
"v_add_u32 v214, s85, v212\n" \
"v_xor_b32 v215, 32, v214\n" \
"v_mfma_f32_16x16x32_bf16 v[112:115], a[96:99], a[88:91], v[112:115]\n" \
"buffer_load_dwordx4 v234, s[56:59], s81 offen offset:3072 sc1 lds\n" \
"v_mfma_f32_16x16x32_bf16 v[112:115], a[100:103], a[92:95], v[112:115]\n" \
"s_waitcnt vmcnt(26) lgkmcnt(0)\n" \
"s_barrier\n" \
"v_mfma_f32_16x16x32_bf16 v[72:75], a[112:115], a[64:67], v[72:75]\n" \
"ds_read_b64_tr_b16 a[96:97], v214 offset:16384\n" \
"ds_read_b64_tr_b16 a[98:99], v214 offset:18432\n" \
"v_mfma_f32_16x16x32_bf16 v[72:75], a[116:119], a[68:71], v[72:75]\n" \
"s_add_u32 m0, m0, 0x4000\n" \
"ds_read_b64_tr_b16 a[100:101], v214 offset:24576\n" \
"ds_read_b64_tr_b16 a[102:103], v214 offset:26624\n" \
"v_mfma_f32_16x16x32_bf16 v[76:79], a[120:123], a[64:67], v[76:79]\n" \
"buffer_load_dwordx4 v216, s[36:39], s82 offen offset:0 lds\n" \
"v_mfma_f32_16x16x32_bf16 v[76:79], a[124:127], a[68:71], v[76:79]\n" \
"ds_read_b128 a[64:67], v213 offset:0\n" \
"v_cvt_pk_bf16_f32 v31, v62, v63\n" \
"v_mfma_f32_16x16x32_bf16 v[92:95], a[120:123], a[72:75], v[92:95]\n" \
"ds_read_b128 a[68:71], v213 offset:2048\n" \
"v_mfma_f32_16x16x32_bf16 v[92:95], a[124:127], a[76:79], v[92:95]\n" \
"ds_read_b64_tr_b16 a[104:105], v215 offset:16384\n" \
"ds_read_b64_tr_b16 a[106:107], v215 offset:18432\n" \
"v_mfma_f32_16x16x32_bf16 v[88:91], a[112:115], a[72:75], v[88:91]\n" \
"buffer_load_dwordx4 v217, s[36:39], s82 offen offset:1024 lds\n" \
"v_mfma_f32_16x16x32_bf16 v[88:91], a[116:119], a[76:79], v[88:91]\n" \
"ds_read_b128 a[72:75], v213 offset:256\n" \
"v_mfma_f32_16x16x32_bf16 v[104:107], a[112:115], a[80:83], v[104:107]\n" \
"ds_read_b128 a[76:79], v213 offset:2304\n" \
"v_mfma_f32_16x16x32_bf16 v[104:107], a[116:119], a[84:87], v[104:107]\n" \
"ds_read_b64_tr_b16 a[108:109], v215 offset:24576\n" \
"ds_read_b64_tr_b16 a[110:111], v215 offset:26624\n" \
"v_mfma_f32_16x16x32_bf16 v[108:111], a[120:123], a[80:83], v[108:111]\n" \
"buffer_load_dwordx4 v218, s[36:39], s82 offen offset:2048 lds\n" \
"v_mfma_f32_16x16x32_bf16 v[108:111], a[124:127], a[84:87], v[108:111]\n" \
"ds_read_b128 a[80:83], v213 offset:4096\n" \
"v_mfma_f32_16x16x32_bf16 v[124:127], a[120:123], a[88:91], v[124:127]\n" \
"s_waitcnt lgkmcnt(4)\n" \
"buffer_store_dwordx4 v[24:27], v234, s[56:59], s71 offen offset:2048 sc1\n" \
"v_mfma_f32_16x16x32_bf16 v[124:127], a[124:127], a[92:95], v[124:127]\n" \
"ds_read_b128 a[84:87], v213 offset:6144\n" \
"v_mfma_f32_16x16x32_bf16 v[120:123], a[112:115], a[88:91], v[120:123]\n" \
"buffer_load_dwordx4 v219, s[36:39], s82 offen offset:3072 lds\n" \
"v_mfma_f32_16x16x32_bf16 v[120:123], a[116:119], a[92:95], v[120:123]\n" \
"buffer_store_dwordx4 v[28:31], v234, s[56:59], s71 offen offset:3072 sc1\n" \
"v_mfma_f32_16x16x32_bf16 v[64:67], a[96:99], a[64:67], v[64:67]\n" \
"s_waitcnt lgkmcnt(1)\n" \
"s_xor_b32 s82, s48, s49\n" \
"ds_read_b128 a[88:91], v213 offset:4352\n" \
"v_mfma_f32_16x16x32_bf16 v[64:67], a[100:103], a[68:71], v[64:67]\n" \
"s_cmp_lt_u32 s48, s50\n" \
"s_cselect_b32 s82, s82, s48\n" \
"ds_read_b128 a[92:95], v213 offset:6400\n" \
"v_mfma_f32_16x16x32_bf16 v[68:71], a[104:107], a[64:67], v[68:71]\n" \
"s_add_u32 s81, s82, s46\n" \
"ds_read_b64_tr_b16 a[112:113], v214 offset:16896\n" \
"v_mfma_f32_16x16x32_bf16 v[68:71], a[108:111], a[68:71], v[68:71]\n" \
"s_add_u32 m0, s86, s79\n" \
"ds_read_b64_tr_b16 a[114:115], v214 offset:18944\n" \
"v_mfma_f32_16x16x32_bf16 v[84:87], a[104:107], a[72:75], v[84:87]\n" \
"buffer_load_dwordx4 v234, s[56:59], s81 offen offset:0 sc1 lds\n" \
"v_mfma_f32_16x16x32_bf16 v[84:87], a[108:111], a[76:79], v[84:87]\n" \
"ds_read_b64_tr_b16 a[116:117], v214 offset:25088\n" \
"v_mfma_f32_16x16x32_bf16 v[80:83], a[96:99], a[72:75], v[80:83]\n" \
"s_lshl_b32 s82, s82, 5\n" \
"ds_read_b64_tr_b16 a[118:119], v214 offset:27136\n" \
"v_mfma_f32_16x16x32_bf16 v[80:83], a[100:103], a[76:79], v[80:83]\n" \
"s_waitcnt lgkmcnt(3)\n" \
"s_add_u32 s82, s82, s47\n" \
"ds_read_b64_tr_b16 a[120:121], v215 offset:16896\n" \
"v_mfma_f32_16x16x32_bf16 v[96:99], a[96:99], a[80:83], v[96:99]\n" \
"buffer_load_dwordx4 v234, s[56:59], s81 offen offset:1024 sc1 lds\n" \
"v_mfma_f32_16x16x32_bf16 v[96:99], a[100:103], a[84:87], v[96:99]\n" \
"s_sub_u32 s85, s85, 32768\n" \
"s_cselect_b32 s85, 131072, s85\n" \
"ds_read_b64_tr_b16 a[122:123], v215 offset:18944\n" \
"v_mfma_f32_16x16x32_bf16 v[100:103], a[104:107], a[80:83], v[100:103]\n" \
"s_add_u32 s48, s48, 0x1000\n" \
"ds_read_b64_tr_b16 a[124:125], v215 offset:25088\n" \
"v_add_u32 v213, s85, v211\n" \
"v_mfma_f32_16x16x32_bf16 v[100:103], a[108:111], a[84:87], v[100:103]\n" \
"s_sub_u32 s86, s86, 32768\n" \
"s_cselect_b32 s86, 131072, s86\n" \
"ds_read_b64_tr_b16 a[126:127], v215 offset:27136\n" \
"v_mfma_f32_16x16x32_bf16 v[116:119], a[104:107], a[88:91], v[116:119]\n" \
"buffer_load_dwordx4 v234, s[56:59], s81 offen offset:2048 sc1 lds\n" \
"v_mfma_f32_16x16x32_bf16 v[116:119], a[108:111], a[92:95], v[116:119]\n" \
"v_add_u32 v214, s85, v212\n" \
"v_xor_b32 v215, 32, v214\n" \
"v_mfma_f32_16x16x32_bf16 v[112:115], a[96:99], a[88:91], v[112:115]\n" \
"buffer_load_dwordx4 v234, s[56:59], s81 offen offset:3072 sc1 lds\n" \
"v_mfma_f32_16x16x32_bf16 v[112:115], a[100:103], a[92:95], v[112:115]\n" \
"s_waitcnt vmcnt(27) lgkmcnt(0)\n" \
"s_barrier\n" \
"v_mfma_f32_16x16x32_bf16 v[72:75], a[112:115], a[64:67], v[72:75]\n" \
"ds_read_b64_tr_b16 a[96:97], v214 offset:16384\n" \
"ds_read_b64_tr_b16 a[98:99], v214 offset:18432\n" \
"v_mfma_f32_16x16x32_bf16 v[72:75], a[116:119], a[68:71], v[72:75]\n" \
"s_add_u32 m0, m0, 0x4000\n" \
"ds_read_b64_tr_b16 a[100:101], v214 offset:24576\n" \
"ds_read_b64_tr_b16 a[102:103], v214 offset:26624\n" \
"v_mfma_f32_16x16x32_bf16 v[76:79], a[120:123], a[64:67], v[76:79]\n" \
"buffer_load_dwordx4 v216, s[36:39], s82 offen offset:0 lds\n" \
"v_mfma_f32_16x16x32_bf16 v[76:79], a[124:127], a[68:71], v[76:79]\n" \
"ds_read_b128 a[64:67], v213 offset:0\n" \
"v_mfma_f32_16x16x32_bf16 v[92:95], a[120:123], a[72:75], v[92:95]\n" \
"ds_read_b128 a[68:71], v213 offset:2048\n" \
"v_mfma_f32_16x16x32_bf16 v[92:95], a[124:127], a[76:79], v[92:95]\n" \
"ds_read_b64_tr_b16 a[104:105], v215 offset:16384\n" \
"ds_read_b64_tr_b16 a[106:107], v215 offset:18432\n" \
"v_mfma_f32_16x16x32_bf16 v[88:91], a[112:115], a[72:75], v[88:91]\n" \
"buffer_load_dwordx4 v217, s[36:39], s82 offen offset:1024 lds\n" \
"v_mfma_f32_16x16x32_bf16 v[88:91], a[116:119], a[76:79], v[88:91]\n" \
"ds_read_b128 a[72:75], v213 offset:256\n" \
"v_mfma_f32_16x16x32_bf16 v[104:107], a[112:115], a[80:83], v[104:107]\n" \
"ds_read_b128 a[76:79], v213 offset:2304\n" \
"v_mfma_f32_16x16x32_bf16 v[104:107], a[116:119], a[84:87], v[104:107]\n" \
"ds_read_b64_tr_b16 a[108:109], v215 offset:24576\n" \
"ds_read_b64_tr_b16 a[110:111], v215 offset:26624\n" \
"v_mfma_f32_16x16x32_bf16 v[108:111], a[120:123], a[80:83], v[108:111]\n" \
"buffer_load_dwordx4 v218, s[36:39], s82 offen offset:2048 lds\n" \
"v_mfma_f32_16x16x32_bf16 v[108:111], a[124:127], a[84:87], v[108:111]\n" \
"ds_read_b128 a[80:83], v213 offset:4096\n" \
"v_mfma_f32_16x16x32_bf16 v[124:127], a[120:123], a[88:91], v[124:127]\n" \
"s_waitcnt lgkmcnt(4)\n" \
"v_mfma_f32_16x16x32_bf16 v[124:127], a[124:127], a[92:95], v[124:127]\n" \
"ds_read_b128 a[84:87], v213 offset:6144\n" \
"v_mfma_f32_16x16x32_bf16 v[120:123], a[112:115], a[88:91], v[120:123]\n" \
"buffer_load_dwordx4 v219, s[36:39], s82 offen offset:3072 lds\n" \
"v_mfma_f32_16x16x32_bf16 v[120:123], a[116:119], a[92:95], v[120:123]\n" \
"v_mfma_f32_16x16x32_bf16 v[64:67], a[96:99], a[64:67], v[64:67]\n" \
"s_waitcnt lgkmcnt(1)\n" \
"s_xor_b32 s82, s48, s49\n" \
"ds_read_b128 a[88:91], v213 offset:4352\n" \
"v_mfma_f32_16x16x32_bf16 v[64:67], a[100:103], a[68:71], v[64:67]\n" \
"s_cmp_lt_u32 s48, s50\n" \
"s_cselect_b32 s82, s82, s48\n" \
"ds_read_b128 a[92:95], v213 offset:6400\n" \
"v_mfma_f32_16x16x32_bf16 v[68:71], a[104:107], a[64:67], v[68:71]\n" \
"s_add_u32 s81, s82, s46\n" \
"ds_read_b64_tr_b16 a[112:113], v214 offset:16896\n" \
"v_mfma_f32_16x16x32_bf16 v[68:71], a[108:111], a[68:71], v[68:71]\n" \
"s_add_u32 m0, s86, s79\n" \
"ds_read_b64_tr_b16 a[114:115], v214 offset:18944\n" \
"v_mfma_f32_16x16x32_bf16 v[84:87], a[104:107], a[72:75], v[84:87]\n" \
"buffer_load_dwordx4 v234, s[56:59], s81 offen offset:0 sc1 lds\n" \
"v_mfma_f32_16x16x32_bf16 v[84:87], a[108:111], a[76:79], v[84:87]\n" \
"ds_read_b64_tr_b16 a[116:117], v214 offset:25088\n" \
"v_mfma_f32_16x16x32_bf16 v[80:83], a[96:99], a[72:75], v[80:83]\n" \
"s_lshl_b32 s82, s82, 5\n" \
"ds_read_b64_tr_b16 a[118:119], v214 offset:27136\n" \
"v_mfma_f32_16x16x32_bf16 v[80:83], a[100:103], a[76:79], v[80:83]\n" \
"s_waitcnt lgkmcnt(3)\n" \
"s_add_u32 s82, s82, s47\n" \
"ds_read_b64_tr_b16 a[120:121], v215 offset:16896\n" \
"v_mfma_f32_16x16x32_bf16 v[96:99], a[96:99], a[80:83], v[96:99]\n" \
"buffer_load_dwordx4 v234, s[56:59], s81 offen offset:1024 sc1 lds\n" \
"v_mfma_f32_16x16x32_bf16 v[96:99], a[100:103], a[84:87], v[96:99]\n" \
"s_sub_u32 s85, s85, 32768\n" \
"s_cselect_b32 s85, 131072, s85\n" \
"ds_read_b64_tr_b16 a[122:123], v215 offset:18944\n" \
"v_mfma_f32_16x16x32_bf16 v[100:103], a[104:107], a[80:83], v[100:103]\n" \
"s_add_u32 s48, s48, 0x1000\n" \
"ds_read_b64_tr_b16 a[124:125], v215 offset:25088\n" \
"v_add_u32 v213, s85, v211\n" \
"v_mfma_f32_16x16x32_bf16 v[100:103], a[108:111], a[84:87], v[100:103]\n" \
"s_sub_u32 s86, s86, 32768\n" \
"s_cselect_b32 s86, 131072, s86\n" \
"ds_read_b64_tr_b16 a[126:127], v215 offset:27136\n" \
"v_mfma_f32_16x16x32_bf16 v[116:119], a[104:107], a[88:91], v[116:119]\n" \
"buffer_load_dwordx4 v234, s[56:59], s81 offen offset:2048 sc1 lds\n" \
"v_mfma_f32_16x16x32_bf16 v[116:119], a[108:111], a[92:95], v[116:119]\n" \
"v_add_u32 v214, s85, v212\n" \
"v_xor_b32 v215, 32, v214\n" \
"v_mfma_f32_16x16x32_bf16 v[112:115], a[96:99], a[88:91], v[112:115]\n" \
"buffer_load_dwordx4 v234, s[56:59], s81 offen offset:3072 sc1 lds\n" \
"v_mfma_f32_16x16x32_bf16 v[112:115], a[100:103], a[92:95], v[112:115]\n" \
"s_waitcnt vmcnt(24) lgkmcnt(0)\n" \
"s_barrier\n" \
"v_mfma_f32_16x16x32_bf16 v[72:75], a[112:115], a[64:67], v[72:75]\n" \
"ds_read_b64_tr_b16 a[96:97], v214 offset:16384\n" \
"ds_read_b64_tr_b16 a[98:99], v214 offset:18432\n" \
"v_mfma_f32_16x16x32_bf16 v[72:75], a[116:119], a[68:71], v[72:75]\n" \
"s_add_u32 m0, m0, 0x4000\n" \
"ds_read_b64_tr_b16 a[100:101], v214 offset:24576\n" \
"ds_read_b64_tr_b16 a[102:103], v214 offset:26624\n" \
"v_mfma_f32_16x16x32_bf16 v[76:79], a[120:123], a[64:67], v[76:79]\n" \
"buffer_load_dwordx4 v216, s[36:39], s82 offen offset:0 lds\n" \
"v_mfma_f32_16x16x32_bf16 v[76:79], a[124:127], a[68:71], v[76:79]\n" \
"ds_read_b128 a[64:67], v213 offset:0\n" \
"v_mfma_f32_16x16x32_bf16 v[92:95], a[120:123], a[72:75], v[92:95]\n" \
"ds_read_b128 a[68:71], v213 offset:2048\n" \
"v_mfma_f32_16x16x32_bf16 v[92:95], a[124:127], a[76:79], v[92:95]\n" \
"ds_read_b64_tr_b16 a[104:105], v215 offset:16384\n" \
"ds_read_b64_tr_b16 a[106:107], v215 offset:18432\n" \
"v_mfma_f32_16x16x32_bf16 v[88:91], a[112:115], a[72:75], v[88:91]\n" \
"buffer_load_dwordx4 v217, s[36:39], s82 offen offset:1024 lds\n" \
"v_mfma_f32_16x16x32_bf16 v[88:91], a[116:119], a[76:79], v[88:91]\n" \
"ds_read_b128 a[72:75], v213 offset:256\n" \
"v_mfma_f32_16x16x32_bf16 v[104:107], a[112:115], a[80:83], v[104:107]\n" \
"ds_read_b128 a[76:79], v213 offset:2304\n" \
"v_mfma_f32_16x16x32_bf16 v[104:107], a[116:119], a[84:87], v[104:107]\n" \
"ds_read_b64_tr_b16 a[108:109], v215 offset:24576\n" \
"ds_read_b64_tr_b16 a[110:111], v215 offset:26624\n" \
"v_mfma_f32_16x16x32_bf16 v[108:111], a[120:123], a[80:83], v[108:111]\n" \
"buffer_load_dwordx4 v218, s[36:39], s82 offen offset:2048 lds\n" \
"v_mfma_f32_16x16x32_bf16 v[108:111], a[124:127], a[84:87], v[108:111]\n" \
"ds_read_b128 a[80:83], v213 offset:4096\n" \
"v_mfma_f32_16x16x32_bf16 v[124:127], a[120:123], a[88:91], v[124:127]\n" \
"s_waitcnt lgkmcnt(4)\n" \
"v_mfma_f32_16x16x32_bf16 v[124:127], a[124:127], a[92:95], v[124:127]\n" \
"ds_read_b128 a[84:87], v213 offset:6144\n" \
"v_mfma_f32_16x16x32_bf16 v[120:123], a[112:115], a[88:91], v[120:123]\n" \
"buffer_load_dwordx4 v219, s[36:39], s82 offen offset:3072 lds\n" \
"v_mfma_f32_16x16x32_bf16 v[120:123], a[116:119], a[92:95], v[120:123]\n" \
"v_mfma_f32_16x16x32_bf16 v[64:67], a[96:99], a[64:67], v[64:67]\n" \
"s_waitcnt lgkmcnt(1)\n" \
"s_xor_b32 s82, s48, s49\n" \
"ds_read_b128 a[88:91], v213 offset:4352\n" \
"v_mfma_f32_16x16x32_bf16 v[64:67], a[100:103], a[68:71], v[64:67]\n" \
"s_cmp_lt_u32 s48, s50\n" \
"s_cselect_b32 s82, s82, s48\n" \
"ds_read_b128 a[92:95], v213 offset:6400\n" \
"v_mfma_f32_16x16x32_bf16 v[68:71], a[104:107], a[64:67], v[68:71]\n" \
"s_add_u32 s81, s82, s46\n" \
"ds_read_b64_tr_b16 a[112:113], v214 offset:16896\n" \
"v_mfma_f32_16x16x32_bf16 v[68:71], a[108:111], a[68:71], v[68:71]\n" \
"s_add_u32 m0, s86, s79\n" \
"ds_read_b64_tr_b16 a[114:115], v214 offset:18944\n" \
"v_mfma_f32_16x16x32_bf16 v[84:87], a[104:107], a[72:75], v[84:87]\n" \
"buffer_load_dwordx4 v234, s[56:59], s81 offen offset:0 sc1 lds\n" \
"v_mfma_f32_16x16x32_bf16 v[84:87], a[108:111], a[76:79], v[84:87]\n" \
"ds_read_b64_tr_b16 a[116:117], v214 offset:25088\n" \
"v_mfma_f32_16x16x32_bf16 v[80:83], a[96:99], a[72:75], v[80:83]\n" \
"s_lshl_b32 s82, s82, 5\n" \
"ds_read_b64_tr_b16 a[118:119], v214 offset:27136\n" \
"v_mfma_f32_16x16x32_bf16 v[80:83], a[100:103], a[76:79], v[80:83]\n" \
"s_waitcnt lgkmcnt(3)\n" \
"s_add_u32 s82, s82, s47\n" \
"ds_read_b64_tr_b16 a[120:121], v215 offset:16896\n" \
"v_mfma_f32_16x16x32_bf16 v[96:99], a[96:99], a[80:83], v[96:99]\n" \
"buffer_load_dwordx4 v234, s[56:59], s81 offen offset:1024 sc1 lds\n" \
"v_mfma_f32_16x16x32_bf16 v[96:99], a[100:103], a[84:87], v[96:99]\n" \
"s_sub_u32 s85, s85, 32768\n" \
"s_cselect_b32 s85, 131072, s85\n" \
"ds_read_b64_tr_b16 a[122:123], v215 offset:18944\n" \
"v_mfma_f32_16x16x32_bf16 v[100:103], a[104:107], a[80:83], v[100:103]\n" \
"s_add_u32 s48, s48, 0x1000\n" \
"ds_read_b64_tr_b16 a[124:125], v215 offset:25088\n" \
"v_add_u32 v213, s85, v211\n" \
"v_mfma_f32_16x16x32_bf16 v[100:103], a[108:111], a[84:87], v[100:103]\n" \
"s_sub_u32 s86, s86, 32768\n" \
"s_cselect_b32 s86, 131072, s86\n" \
"ds_read_b64_tr_b16 a[126:127], v215 offset:27136\n" \
"v_mfma_f32_16x16x32_bf16 v[116:119], a[104:107], a[88:91], v[116:119]\n" \
"buffer_load_dwordx4 v234, s[56:59], s81 offen offset:2048 sc1 lds\n" \
"v_mfma_f32_16x16x32_bf16 v[116:119], a[108:111], a[92:95], v[116:119]\n" \
"v_add_u32 v214, s85, v212\n" \
"v_xor_b32 v215, 32, v214\n" \
"v_mfma_f32_16x16x32_bf16 v[112:115], a[96:99], a[88:91], v[112:115]\n" \
"buffer_load_dwordx4 v234, s[56:59], s81 offen offset:3072 sc1 lds\n" \
"v_mfma_f32_16x16x32_bf16 v[112:115], a[100:103], a[92:95], v[112:115]\n" \
"s_waitcnt vmcnt(20) lgkmcnt(0)\n" \
"s_barrier\n" \
"buffer_store_dword v230, v231, s[56:59], s64 offen sc1\n" \
"v_mfma_f32_16x16x32_bf16 v[72:75], a[112:115], a[64:67], v[72:75]\n" \
"ds_read_b64_tr_b16 a[96:97], v214 offset:16384\n" \
"ds_read_b64_tr_b16 a[98:99], v214 offset:18432\n" \
"v_mfma_f32_16x16x32_bf16 v[72:75], a[116:119], a[68:71], v[72:75]\n" \
"s_add_u32 m0, m0, 0x4000\n" \
"ds_read_b64_tr_b16 a[100:101], v214 offset:24576\n" \
"ds_read_b64_tr_b16 a[102:103], v214 offset:26624\n" \
"v_mfma_f32_16x16x32_bf16 v[76:79], a[120:123], a[64:67], v[76:79]\n" \
"buffer_load_dwordx4 v216, s[36:39], s82 offen offset:0 lds\n" \
"v_mfma_f32_16x16x32_bf16 v[76:79], a[124:127], a[68:71], v[76:79]\n" \
"ds_read_b128 a[64:67], v213 offset:0\n" \
"v_mfma_f32_16x16x32_bf16 v[92:95], a[120:123], a[72:75], v[92:95]\n" \
"ds_read_b128 a[68:71], v213 offset:2048\n" \
"v_mfma_f32_16x16x32_bf16 v[92:95], a[124:127], a[76:79], v[92:95]\n" \
"ds_read_b64_tr_b16 a[104:105], v215 offset:16384\n" \
"ds_read_b64_tr_b16 a[106:107], v215 offset:18432\n" \
"v_mfma_f32_16x16x32_bf16 v[88:91], a[112:115], a[72:75], v[88:91]\n" \
"buffer_load_dwordx4 v217, s[36:39], s82 offen offset:1024 lds\n" \
"v_mfma_f32_16x16x32_bf16 v[88:91], a[116:119], a[76:79], v[88:91]\n" \
"ds_read_b128 a[72:75], v213 offset:256\n" \
"v_mfma_f32_16x16x32_bf16 v[104:107], a[112:115], a[80:83], v[104:107]\n" \
"ds_read_b128 a[76:79], v213 offset:2304\n" \
"v_mfma_f32_16x16x32_bf16 v[104:107], a[116:119], a[84:87], v[104:107]\n" \
"ds_read_b64_tr_b16 a[108:109], v215 offset:24576\n" \
"ds_read_b64_tr_b16 a[110:111], v215 offset:26624\n" \
"v_mfma_f32_16x16x32_bf16 v[108:111], a[120:123], a[80:83], v[108:111]\n" \
"buffer_load_dwordx4 v218, s[36:39], s82 offen offset:2048 lds\n" \
"v_mfma_f32_16x16x32_bf16 v[108:111], a[124:127], a[84:87], v[108:111]\n" \
"ds_read_b128 a[80:83], v213 offset:4096\n" \
"v_mfma_f32_16x16x32_bf16 v[124:127], a[120:123], a[88:91], v[124:127]\n" \
"s_waitcnt lgkmcnt(4)\n" \
"v_mfma_f32_16x16x32_bf16 v[124:127], a[124:127], a[92:95], v[124:127]\n" \
"ds_read_b128 a[84:87], v213 offset:6144\n" \
"v_mfma_f32_16x16x32_bf16 v[120:123], a[112:115], a[88:91], v[120:123]\n" \
"buffer_load_dwordx4 v219, s[36:39], s82 offen offset:3072 lds\n" \
"v_mfma_f32_16x16x32_bf16 v[120:123], a[116:119], a[92:95], v[120:123]\n" \
"s_mov_b32 s72, 5\n" \
"Lo64_%=:\n" \
"v_mfma_f32_16x16x32_bf16 v[64:67], a[96:99], a[64:67], v[64:67]\n" \
"s_waitcnt lgkmcnt(1)\n" \
"s_xor_b32 s82, s48, s49\n" \
"ds_read_b128 a[88:91], v213 offset:4352\n" \
"Lpg11507_%=:\n" \
"s_cbranch_execz Lcb1_%=\n" \
"v_mfma_f32_16x16x32_bf16 v[64:67], a[100:103], a[68:71], v[64:67]\n" \
"s_cmp_lt_u32 s48, s50\n" \
"s_cselect_b32 s82, s82, s48\n" \
"ds_read_b128 a[92:95], v213 offset:6400\n" \
"v_mfma_f32_16x16x32_bf16 v[68:71], a[104:107], a[64:67], v[68:71]\n" \
"s_add_u32 s81, s82, s46\n" \
"ds_read_b64_tr_b16 a[112:113], v214 offset:16896\n" \
"v_mfma_f32_16x16x32_bf16 v[68:71], a[108:111], a[68:71], v[68:71]\n" \
"s_add_u32 m0, s86, s79\n" \
"ds_read_b64_tr_b16 a[114:115], v214 offset:18944\n" \
"v_mfma_f32_16x16x32_bf16 v[84:87], a[104:107], a[72:75], v[84:87]\n" \
"buffer_load_dwordx4 v234, s[56:59], s81 offen offset:0 sc1 lds\n" \
"v_mfma_f32_16x16x32_bf16 v[84:87], a[108:111], a[76:79], v[84:87]\n" \
"ds_read_b64_tr_b16 a[116:117], v214 offset:25088\n" \
"v_mfma_f32_16x16x32_bf16 v[80:83], a[96:99], a[72:75], v[80:83]\n" \
"s_lshl_b32 s82, s82, 5\n" \
"ds_read_b64_tr_b16 a[118:119], v214 offset:27136\n" \
"v_mfma_f32_16x16x32_bf16 v[80:83], a[100:103], a[76:79], v[80:83]\n" \
"s_waitcnt lgkmcnt(3)\n" \
"s_add_u32 s82, s82, s47\n" \
"ds_read_b64_tr_b16 a[120:121], v215 offset:16896\n" \
"v_mfma_f32_16x16x32_bf16 v[96:99], a[96:99], a[80:83], v[96:99]\n" \
"buffer_load_dwordx4 v234, s[56:59], s81 offen offset:1024 sc1 lds\n" \
"v_mfma_f32_16x16x32_bf16 v[96:99], a[100:103], a[84:87], v[96:99]\n" \
"s_sub_u32 s85, s85, 32768\n" \
"s_cselect_b32 s85, 131072, s85\n" \
"ds_read_b64_tr_b16 a[122:123], v215 offset:18944\n" \
"v_mfma_f32_16x16x32_bf16 v[100:103], a[104:107], a[80:83], v[100:103]\n" \
"s_add_u32 s48, s48, 0x1000\n" \
"ds_read_b64_tr_b16 a[124:125], v215 offset:25088\n" \
"v_add_u32 v213, s85, v211\n" \
"v_mfma_f32_16x16x32_bf16 v[100:103], a[108:111], a[84:87], v[100:103]\n" \
"s_sub_u32 s86, s86, 32768\n" \
"s_cselect_b32 s86, 131072, s86\n" \
"ds_read_b64_tr_b16 a[126:127], v215 offset:27136\n" \
"v_mfma_f32_16x16x32_bf16 v[116:119], a[104:107], a[88:91], v[116:119]\n" \
"buffer_load_dwordx4 v234, s[56:59], s81 offen offset:2048 sc1 lds\n" \
"v_mfma_f32_16x16x32_bf16 v[116:119], a[108:111], a[92:95], v[116:119]\n" \
"v_add_u32 v214, s85, v212\n" \
"v_xor_b32 v215, 32, v214\n" \
"v_mfma_f32_16x16x32_bf16 v[112:115], a[96:99], a[88:91], v[112:115]\n" \
"buffer_load_dwordx4 v234, s[56:59], s81 offen offset:3072 sc1 lds\n" \
"v_mfma_f32_16x16x32_bf16 v[112:115], a[100:103], a[92:95], v[112:115]\n" \
"s_waitcnt vmcnt(20) lgkmcnt(0)\n" \
"s_barrier\n" \
"v_mfma_f32_16x16x32_bf16 v[72:75], a[112:115], a[64:67], v[72:75]\n" \
"ds_read_b64_tr_b16 a[96:97], v214 offset:16384\n" \
"ds_read_b64_tr_b16 a[98:99], v214 offset:18432\n" \
"v_mfma_f32_16x16x32_bf16 v[72:75], a[116:119], a[68:71], v[72:75]\n" \
"s_add_u32 m0, m0, 0x4000\n" \
"ds_read_b64_tr_b16 a[100:101], v214 offset:24576\n" \
"ds_read_b64_tr_b16 a[102:103], v214 offset:26624\n" \
"v_mfma_f32_16x16x32_bf16 v[76:79], a[120:123], a[64:67], v[76:79]\n" \
"buffer_load_dwordx4 v216, s[36:39], s82 offen offset:0 lds\n" \
"v_mfma_f32_16x16x32_bf16 v[76:79], a[124:127], a[68:71], v[76:79]\n" \
"ds_read_b128 a[64:67], v213 offset:0\n" \
"v_mfma_f32_16x16x32_bf16 v[92:95], a[120:123], a[72:75], v[92:95]\n" \
"ds_read_b128 a[68:71], v213 offset:2048\n" \
"v_mfma_f32_16x16x32_bf16 v[92:95], a[124:127], a[76:79], v[92:95]\n" \
"ds_read_b64_tr_b16 a[104:105], v215 offset:16384\n" \
"ds_read_b64_tr_b16 a[106:107], v215 offset:18432\n" \
"v_mfma_f32_16x16x32_bf16 v[88:91], a[112:115], a[72:75], v[88:91]\n" \
"buffer_load_dwordx4 v217, s[36:39], s82 offen offset:1024 lds\n" \
"v_mfma_f32_16x16x32_bf16 v[88:91], a[116:119], a[76:79], v[88:91]\n" \
"ds_read_b128 a[72:75], v213 offset:256\n" \
"v_mfma_f32_16x16x32_bf16 v[104:107], a[112:115], a[80:83], v[104:107]\n" \
"ds_read_b128 a[76:79], v213 offset:2304\n" \
"v_mfma_f32_16x16x32_bf16 v[104:107], a[116:119], a[84:87], v[104:107]\n" \
"ds_read_b64_tr_b16 a[108:109], v215 offset:24576\n" \
"ds_read_b64_tr_b16 a[110:111], v215 offset:26624\n" \
"v_mfma_f32_16x16x32_bf16 v[108:111], a[120:123], a[80:83], v[108:111]\n" \
"buffer_load_dwordx4 v218, s[36:39], s82 offen offset:2048 lds\n" \
"v_mfma_f32_16x16x32_bf16 v[108:111], a[124:127], a[84:87], v[108:111]\n" \
"ds_read_b128 a[80:83], v213 offset:4096\n" \
"v_mfma_f32_16x16x32_bf16 v[124:127], a[120:123], a[88:91], v[124:127]\n" \
"s_waitcnt lgkmcnt(4)\n" \
"v_mfma_f32_16x16x32_bf16 v[124:127], a[124:127], a[92:95], v[124:127]\n" \
"s_sub_u32 s72, s72, 1\n" \
"ds_read_b128 a[84:87], v213 offset:6144\n" \
"v_mfma_f32_16x16x32_bf16 v[120:123], a[112:115], a[88:91], v[120:123]\n" \
"buffer_load_dwordx4 v219, s[36:39], s82 offen offset:3072 lds\n" \
"v_mfma_f32_16x16x32_bf16 v[120:123], a[116:119], a[92:95], v[120:123]\n" \
"s_cbranch_scc0 Lo64_%=\n" \
"v_mfma_f32_16x16x32_bf16 v[64:67], a[96:99], a[64:67], v[64:67]\n" \
"s_waitcnt lgkmcnt(1)\n" \
"ds_read_b128 a[88:91], v213 offset:4352\n" \
"v_mfma_f32_16x16x32_bf16 v[64:67], a[100:103], a[68:71], v[64:67]\n" \
"s_add_u32 s48, s86, 32768\n" \
"s_cmp_eq_u32 s48, 163840\n" \
"s_cselect_b32 s48, 0, s48\n" \
"ds_read_b128 a[92:95], v213 offset:6400\n" \
"v_mfma_f32_16x16x32_bf16 v[68:71], a[104:107], a[64:67], v[68:71]\n" \
"ds_read_b64_tr_b16 a[112:113], v214 offset:16896\n" \
"v_mfma_f32_16x16x32_bf16 v[68:71], a[108:111], a[68:71], v[68:71]\n" \
"ds_read_b64_tr_b16 a[114:115], v214 offset:18944\n" \
"v_mfma_f32_16x16x32_bf16 v[84:87], a[104:107], a[72:75], v[84:87]\n" \
"s_add_u32 s49, s48, 32768\n" \
"s_cmp_eq_u32 s49, 163840\n" \
"s_cselect_b32 s49, 0, s49\n" \
"v_mfma_f32_16x16x32_bf16 v[84:87], a[108:111], a[76:79], v[84:87]\n" \
"ds_read_b64_tr_b16 a[116:117], v214 offset:25088\n" \
"v_add_u32 v224, s49, v211\n" \
"v_mfma_f32_16x16x32_bf16 v[80:83], a[96:99], a[72:75], v[80:83]\n" \
"ds_read_b64_tr_b16 a[118:119], v214 offset:27136\n" \
"v_add_u32 v225, s49, v212\n" \
"v_mfma_f32_16x16x32_bf16 v[80:83], a[100:103], a[76:79], v[80:83]\n" \
"s_waitcnt lgkmcnt(3)\n" \
"ds_read_b64_tr_b16 a[120:121], v215 offset:16896\n" \
"v_xor_b32 v226, 32, v225\n" \
"v_mfma_f32_16x16x32_bf16 v[96:99], a[96:99], a[80:83], v[96:99]\n" \
"v_add_u32 v227, s48, v211\n" \
"v_add_u32 v228, s48, v212\n" \
"v_mfma_f32_16x16x32_bf16 v[96:99], a[100:103], a[84:87], v[96:99]\n" \
"s_sub_u32 s85, s85, 32768\n" \
"s_cselect_b32 s85, 131072, s85\n" \
"ds_read_b64_tr_b16 a[122:123], v215 offset:18944\n" \
"v_mfma_f32_16x16x32_bf16 v[100:103], a[104:107], a[80:83], v[100:103]\n" \
"ds_read_b64_tr_b16 a[124:125], v215 offset:25088\n" \
"v_add_u32 v213, s85, v211\n" \
"v_mfma_f32_16x16x32_bf16 v[100:103], a[108:111], a[84:87], v[100:103]\n" \
"ds_read_b64_tr_b16 a[126:127], v215 offset:27136\n" \
"v_xor_b32 v229, 32, v228\n" \
"v_mfma_f32_16x16x32_bf16 v[116:119], a[104:107], a[88:91], v[116:119]\n" \
"v_mfma_f32_16x16x32_bf16 v[116:119], a[108:111], a[92:95], v[116:119]\n" \
"v_add_u32 v214, s85, v212\n" \
"v_xor_b32 v215, 32, v214\n" \
"v_mfma_f32_16x16x32_bf16 v[112:115], a[96:99], a[88:91], v[112:115]\n" \
"v_mfma_f32_16x16x32_bf16 v[112:115], a[100:103], a[92:95], v[112:115]\n" \
"s_waitcnt vmcnt(8) lgkmcnt(0)\n" \
"s_barrier\n" \
"v_mfma_f32_16x16x32_bf16 v[72:75], a[112:115], a[64:67], v[72:75]\n" \
"ds_read_b128 a[0:3], v213 offset:0\n" \
"ds_read_b128 a[4:7], v213 offset:2048\n" \
"v_mfma_f32_16x16x32_bf16 v[72:75], a[116:119], a[68:71], v[72:75]\n" \
"s_sub_u32 s68, s86, 32768\n" \
"s_cselect_b32 s68, 131072, s68\n" \
"ds_read_b64_tr_b16 a[128:129], v214 offset:16384\n" \
"ds_read_b64_tr_b16 a[130:131], v214 offset:18432\n" \
"v_mfma_f32_16x16x32_bf16 v[76:79], a[120:123], a[64:67], v[76:79]\n" \
"ds_read_b64_tr_b16 a[132:133], v214 offset:24576\n" \
"ds_read_b64_tr_b16 a[134:135], v214 offset:26624\n" \
"v_mfma_f32_16x16x32_bf16 v[76:79], a[124:127], a[68:71], v[76:79]\n" \
"s_cmp_lt_u32 s61, 2\n" \
"s_cselect_b32 s68, s86, s68\n" \
"ds_read_b64_tr_b16 a[136:137], v215 offset:16384\n" \
"ds_read_b64_tr_b16 a[138:139], v215 offset:18432\n" \
"v_mfma_f32_16x16x32_bf16 v[92:95], a[120:123], a[72:75], v[92:95]\n" \
"s_and_b32 s50, s61, 1\n" \
"ds_read_b64_tr_b16 a[140:141], v215 offset:24576\n" \
"ds_read_b64_tr_b16 a[142:143], v215 offset:26624\n" \
"v_mfma_f32_16x16x32_bf16 v[92:95], a[124:127], a[76:79], v[92:95]\n" \
"s_mul_i32 s50, s50, 9216\n" \
"ds_read_b64_tr_b16 a[144:145], v214 offset:16896\n" \
"ds_read_b64_tr_b16 a[146:147], v214 offset:18944\n" \
"v_mfma_f32_16x16x32_bf16 v[88:91], a[112:115], a[72:75], v[88:91]\n" \
"s_add_u32 s68, s68, s50\n" \
"ds_read_b64_tr_b16 a[148:149], v214 offset:25088\n" \
"ds_read_b64_tr_b16 a[150:151], v214 offset:27136\n" \
"v_mfma_f32_16x16x32_bf16 v[88:91], a[116:119], a[76:79], v[88:91]\n" \
"s_add_u32 s68, s68, %3\n" \
"ds_read_b64_tr_b16 a[152:153], v215 offset:16896\n" \
"ds_read_b64_tr_b16 a[154:155], v215 offset:18944\n" \
"v_mfma_f32_16x16x32_bf16 v[104:107], a[112:115], a[80:83], v[104:107]\n" \
"ds_read_b64_tr_b16 a[156:157], v215 offset:25088\n" \
"ds_read_b64_tr_b16 a[158:159], v215 offset:27136\n" \
"v_mfma_f32_16x16x32_bf16 v[104:107], a[116:119], a[84:87], v[104:107]\n" \
"ds_read_b128 a[24:27], v213 offset:256\n" \
"ds_read_b128 a[28:31], v213 offset:2304\n" \
"v_mfma_f32_16x16x32_bf16 v[108:111], a[120:123], a[80:83], v[108:111]\n" \
"ds_read_b128 a[8:11], v224 offset:0\n" \
"ds_read_b128 a[12:15], v224 offset:2048\n" \
"v_mfma_f32_16x16x32_bf16 v[108:111], a[124:127], a[84:87], v[108:111]\n" \
"ds_read_b64_tr_b16 a[160:161], v225 offset:16384\n" \
"ds_read_b64_tr_b16 a[162:163], v225 offset:18432\n" \
"v_mfma_f32_16x16x32_bf16 v[124:127], a[120:123], a[88:91], v[124:127]\n" \
"ds_read_b64_tr_b16 a[164:165], v225 offset:24576\n" \
"ds_read_b64_tr_b16 a[166:167], v225 offset:26624\n" \
"v_mfma_f32_16x16x32_bf16 v[124:127], a[124:127], a[92:95], v[124:127]\n" \
"ds_read_b64_tr_b16 a[168:169], v226 offset:16384\n" \
"ds_read_b64_tr_b16 a[170:171], v226 offset:18432\n" \
"v_mfma_f32_16x16x32_bf16 v[120:123], a[112:115], a[88:91], v[120:123]\n" \
"ds_read_b64_tr_b16 a[172:173], v226 offset:24576\n" \
"ds_read_b64_tr_b16 a[174:175], v226 offset:26624\n" \
"v_mfma_f32_16x16x32_bf16 v[120:123], a[116:119], a[92:95], v[120:123]\n" \
"s_waitcnt lgkmcnt(8)\n" \
"ds_read_b64_tr_b16 a[176:177], v225 offset:16896\n" \
"ds_read_b64_tr_b16 a[178:179], v225 offset:18944\n" \
"v_mfma_f32_16x16x32_bf16 v[64:67], a[128:131], a[0:3], v[64:67]\n" \
"ds_read_b64_tr_b16 a[180:181], v225 offset:25088\n" \
"ds_read_b64_tr_b16 a[182:183], v225 offset:27136\n" \
"v_mfma_f32_16x16x32_bf16 v[64:67], a[132:135], a[4:7], v[64:67]\n" \
"ds_read_b64_tr_b16 a[184:185], v226 offset:16896\n" \
"ds_read_b64_tr_b16 a[186:187], v226 offset:18944\n" \
"v_mfma_f32_16x16x32_bf16 v[68:71], a[136:139], a[0:3], v[68:71]\n" \
"ds_read_b64_tr_b16 a[188:189], v226 offset:25088\n" \
"ds_read_b64_tr_b16 a[190:191], v226 offset:27136\n" \
"v_mfma_f32_16x16x32_bf16 v[68:71], a[140:143], a[4:7], v[68:71]\n" \
"v_lshrrev_b32 v132, 4, v234\n" \
"v_and_b32 v133, 15, v132\n" \
"v_mfma_f32_16x16x32_bf16 v[72:75], a[144:147], a[0:3], v[72:75]\n" \
"v_mul_u32_u24 v128, 144, v133\n" \
"v_lshrrev_b32 v133, 4, v132\n" \
"v_mfma_f32_16x16x32_bf16 v[72:75], a[148:151], a[4:7], v[72:75]\n" \
"v_lshl_add_u32 v128, v133, 3, v128\n" \
"v_add_u32 v128, s68, v128\n" \
"v_mfma_f32_16x16x32_bf16 v[76:79], a[152:155], a[0:3], v[76:79]\n" \
"v_lshrrev_b32 v133, 3, v132\n" \
"v_and_b32 v134, 7, v132\n" \
"v_mfma_f32_16x16x32_bf16 v[76:79], a[156:159], a[4:7], v[76:79]\n" \
"v_mul_u32_u24 v129, 144, v133\n" \
"v_lshl_add_u32 v129, v134, 4, v129\n" \
"v_mfma_f32_16x16x32_bf16 v[92:95], a[152:155], a[24:27], v[92:95]\n" \
"v_add_u32 v129, s68, v129\n" \
"v_lshlrev_b32 v134, 4, v134\n" \
"v_mfma_f32_16x16x32_bf16 v[92:95], a[156:159], a[28:31], v[92:95]\n" \
"v_lshl_add_u32 v130, v133, 13, v134\n" \
"v_mfma_f32_16x16x32_bf16 v[88:91], a[144:147], a[24:27], v[88:91]\n" \
"v_mfma_f32_16x16x32_bf16 v[88:91], a[148:151], a[28:31], v[88:91]\n" \
"v_mfma_f32_16x16x32_bf16 v[84:87], a[136:139], a[24:27], v[84:87]\n" \
"s_waitcnt vmcnt(0)\n" \
"s_barrier\n" \
"v_mfma_f32_16x16x32_bf16 v[84:87], a[140:143], a[28:31], v[84:87]\n" \
"ds_read_b128 a[32:35], v224 offset:256\n" \
"ds_read_b128 a[36:39], v224 offset:2304\n" \
"v_mfma_f32_16x16x32_bf16 v[80:83], a[128:131], a[24:27], v[80:83]\n" \
"ds_read_b128 a[16:19], v227 offset:0\n" \
"ds_read_b128 a[20:23], v227 offset:2048\n" \
"v_mfma_f32_16x16x32_bf16 v[80:83], a[132:135], a[28:31], v[80:83]\n" \
"s_waitcnt lgkmcnt(4)\n" \
"ds_read_b64_tr_b16 a[192:193], v228 offset:16384\n" \
"ds_read_b64_tr_b16 a[194:195], v228 offset:18432\n" \
"v_mfma_f32_16x16x32_bf16 v[64:67], a[160:163], a[8:11], v[64:67]\n" \
"ds_read_b64_tr_b16 a[196:197], v228 offset:24576\n" \
"ds_read_b64_tr_b16 a[198:199], v228 offset:26624\n" \
"v_mfma_f32_16x16x32_bf16 v[64:67], a[164:167], a[12:15], v[64:67]\n" \
"ds_read_b64_tr_b16 a[200:201], v229 offset:16384\n" \
"ds_read_b64_tr_b16 a[202:203], v229 offset:18432\n" \
"v_mfma_f32_16x16x32_bf16 v[68:71], a[168:171], a[8:11], v[68:71]\n" \
"ds_read_b64_tr_b16 a[204:205], v229 offset:24576\n" \
"ds_read_b64_tr_b16 a[206:207], v229 offset:26624\n" \
"v_mfma_f32_16x16x32_bf16 v[68:71], a[172:175], a[12:15], v[68:71]\n" \
"ds_read_b64_tr_b16 a[208:209], v228 offset:16896\n" \
"ds_read_b64_tr_b16 a[210:211], v228 offset:18944\n" \
"v_mfma_f32_16x16x32_bf16 v[72:75], a[176:179], a[8:11], v[72:75]\n" \
"ds_read_b64_tr_b16 a[212:213], v228 offset:25088\n" \
"ds_read_b64_tr_b16 a[214:215], v228 offset:27136\n" \
"v_mfma_f32_16x16x32_bf16 v[72:75], a[180:183], a[12:15], v[72:75]\n" \
"ds_read_b64_tr_b16 a[216:217], v229 offset:16896\n" \
"ds_read_b64_tr_b16 a[218:219], v229 offset:18944\n" \
"v_mfma_f32_16x16x32_bf16 v[76:79], a[184:187], a[8:11], v[76:79]\n" \
"ds_read_b64_tr_b16 a[220:221], v229 offset:25088\n" \
"ds_read_b64_tr_b16 a[222:223], v229 offset:27136\n" \
"v_mfma_f32_16x16x32_bf16 v[76:79], a[188:191], a[12:15], v[76:79]\n" \
"s_waitcnt lgkmcnt(8)\n" \
"ds_read_b128 a[40:43], v227 offset:256\n" \
"ds_read_b128 a[44:47], v227 offset:2304\n" \
"v_mfma_f32_16x16x32_bf16 v[92:95], a[184:187], a[32:35], v[92:95]\n" \
"ds_read_b128 a[248:251], v213 offset:4096\n" \
"ds_read_b128 a[252:255], v213 offset:6144\n" \
"v_mfma_f32_16x16x32_bf16 v[92:95], a[188:191], a[36:39], v[92:95]\n" \
"ds_read_b128 a[48:51], v224 offset:4096\n" \
"ds_read_b128 a[52:55], v224 offset:6144\n" \
"v_mfma_f32_16x16x32_bf16 v[88:91], a[176:179], a[32:35], v[88:91]\n" \
"ds_read_b128 a[56:59], v227 offset:4096\n" \
"ds_read_b128 a[60:63], v227 offset:6144\n" \
"v_mfma_f32_16x16x32_bf16 v[88:91], a[180:183], a[36:39], v[88:91]\n" \
"ds_read_b128 a[224:227], v213 offset:4352\n" \
"ds_read_b128 a[228:231], v213 offset:6400\n" \
"v_mfma_f32_16x16x32_bf16 v[84:87], a[168:171], a[32:35], v[84:87]\n" \
"ds_read_b128 a[232:235], v224 offset:4352\n" \
"ds_read_b128 a[236:239], v224 offset:6400\n" \
"v_mfma_f32_16x16x32_bf16 v[84:87], a[172:175], a[36:39], v[84:87]\n" \
"ds_read_b128 a[240:243], v227 offset:4352\n" \
"ds_read_b128 a[244:247], v227 offset:6400\n" \
"v_mfma_f32_16x16x32_bf16 v[80:83], a[160:163], a[32:35], v[80:83]\n" \
"v_mfma_f32_16x16x32_bf16 v[80:83], a[164:167], a[36:39], v[80:83]\n" \
"v_mfma_f32_16x16x32_bf16 v[64:67], a[192:195], a[16:19], v[64:67]\n" \
"v_mfma_f32_16x16x32_bf16 v[64:67], a[196:199], a[20:23], v[64:67]\n" \
"v_mfma_f32_16x16x32_bf16 v[68:71], a[200:203], a[16:19], v[68:71]\n" \
"v_mfma_f32_16x16x32_bf16 v[68:71], a[204:207], a[20:23], v[68:71]\n" \
"s_waitcnt lgkmcnt(0)\n" \
"v_mfma_f32_16x16x32_bf16 v[72:75], a[208:211], a[16:19], v[72:75]\n" \
"v_mfma_f32_16x16x32_bf16 v[72:75], a[212:215], a[20:23], v[72:75]\n" \
"v_cvt_pk_bf16_f32 v136, v64, v65\n" \
"v_cvt_pk_bf16_f32 v137, v66, v67\n" \
"v_mfma_f32_16x16x32_bf16 v[76:79], a[216:219], a[16:19], v[76:79]\n" \
"v_mfma_f32_16x16x32_bf16 v[76:79], a[220:223], a[20:23], v[76:79]\n" \
"ds_write_b64 v128, v[136:137] offset:0\n" \
"v_mfma_f32_16x16x32_bf16 v[80:83], a[192:195], a[40:43], v[80:83]\n" \
"v_cvt_pk_bf16_f32 v138, v68, v69\n" \
"v_cvt_pk_bf16_f32 v139, v70, v71\n" \
"v_mfma_f32_16x16x32_bf16 v[80:83], a[196:199], a[44:47], v[80:83]\n" \
"ds_write_b64 v128, v[138:139] offset:32\n" \
"v_mfma_f32_16x16x32_bf16 v[84:87], a[200:203], a[40:43], v[84:87]\n" \
"v_cvt_pk_bf16_f32 v136, v72, v73\n" \
"v_cvt_pk_bf16_f32 v137, v74, v75\n" \
"v_mfma_f32_16x16x32_bf16 v[84:87], a[204:207], a[44:47], v[84:87]\n" \
"ds_write_b64 v128, v[136:137] offset:64\n" \
"v_mfma_f32_16x16x32_bf16 v[88:91], a[208:211], a[40:43], v[88:91]\n" \
"v_cvt_pk_bf16_f32 v138, v76, v77\n" \
"v_cvt_pk_bf16_f32 v139, v78, v79\n" \
"v_mfma_f32_16x16x32_bf16 v[88:91], a[212:215], a[44:47], v[88:91]\n" \
"ds_write_b64 v128, v[138:139] offset:96\n" \
"v_mfma_f32_16x16x32_bf16 v[92:95], a[216:219], a[40:43], v[92:95]\n" \
"ds_read_b128 v[140:143], v129 offset:0\n" \
"ds_read_b128 v[144:147], v129 offset:1152\n" \
"v_mfma_f32_16x16x32_bf16 v[92:95], a[220:223], a[44:47], v[92:95]\n" \
"v_cvt_pk_bf16_f32 v136, v80, v81\n" \
"v_cvt_pk_bf16_f32 v137, v82, v83\n" \
"v_mfma_f32_16x16x32_bf16 v[96:99], a[128:131], a[248:251], v[96:99]\n" \
"ds_write_b64 v128, v[136:137] offset:2304\n" \
"v_mfma_f32_16x16x32_bf16 v[96:99], a[132:135], a[252:255], v[96:99]\n" \
"s_add_u32 s70, s88, 0\n" \
"s_waitcnt lgkmcnt(2)\n" \
"v_cvt_pk_bf16_f32 v138, v84, v85\n" \
"v_mfma_f32_16x16x32_bf16 v[96:99], a[160:163], a[48:51], v[96:99]\n" \
"buffer_store_dwordx4 v[140:143], v130, s[52:55], s70 offen sc1\n" \
"v_mfma_f32_16x16x32_bf16 v[96:99], a[164:167], a[52:55], v[96:99]\n" \
"v_cvt_pk_bf16_f32 v139, v86, v87\n" \
"v_mfma_f32_16x16x32_bf16 v[96:99], a[192:195], a[56:59], v[96:99]\n" \
"s_add_u32 s70, s88, 65536\n" \
"s_waitcnt lgkmcnt(1)\n" \
"v_mfma_f32_16x16x32_bf16 v[96:99], a[196:199], a[60:63], v[96:99]\n" \
"buffer_store_dwordx4 v[144:147], v130, s[52:55], s70 offen sc1\n" \
"ds_write_b64 v128, v[138:139] offset:2336\n" \
"v_mfma_f32_16x16x32_bf16 v[100:103], a[204:207], a[60:63], v[100:103]\n" \
"v_cvt_pk_bf16_f32 v136, v88, v89\n" \
"v_cvt_pk_bf16_f32 v137, v90, v91\n" \
"v_mfma_f32_16x16x32_bf16 v[100:103], a[200:203], a[56:59], v[100:103]\n" \
"ds_write_b64 v128, v[136:137] offset:2368\n" \
"v_mfma_f32_16x16x32_bf16 v[100:103], a[172:175], a[52:55], v[100:103]\n" \
"v_cvt_pk_bf16_f32 v138, v92, v93\n" \
"v_cvt_pk_bf16_f32 v139, v94, v95\n" \
"v_mfma_f32_16x16x32_bf16 v[100:103], a[168:171], a[48:51], v[100:103]\n" \
"v_mfma_f32_16x16x32_bf16 v[100:103], a[140:143], a[252:255], v[100:103]\n" \
"v_mfma_f32_16x16x32_bf16 v[100:103], a[136:139], a[248:251], v[100:103]\n" \
"ds_write_b64 v128, v[138:139] offset:2400\n" \
"v_mfma_f32_16x16x32_bf16 v[104:107], a[144:147], a[248:251], v[104:107]\n" \
"ds_read_b128 v[148:151], v129 offset:2304\n" \
"ds_read_b128 v[152:155], v129 offset:3456\n" \
"v_mfma_f32_16x16x32_bf16 v[104:107], a[148:151], a[252:255], v[104:107]\n" \
"v_cvt_pk_bf16_f32 v136, v96, v97\n" \
"v_cvt_pk_bf16_f32 v137, v98, v99\n" \
"v_mfma_f32_16x16x32_bf16 v[104:107], a[176:179], a[48:51], v[104:107]\n" \
"ds_write_b64 v128, v[136:137] offset:4608\n" \
"v_mfma_f32_16x16x32_bf16 v[104:107], a[180:183], a[52:55], v[104:107]\n" \
"v_cvt_pk_bf16_f32 v138, v100, v101\n" \
"v_cvt_pk_bf16_f32 v139, v102, v103\n" \
"v_mfma_f32_16x16x32_bf16 v[104:107], a[208:211], a[56:59], v[104:107]\n" \
"s_add_u32 s70, s88, 4096\n" \
"s_waitcnt lgkmcnt(2)\n" \
"v_mfma_f32_16x16x32_bf16 v[104:107], a[212:215], a[60:63], v[104:107]\n" \
"buffer_store_dwordx4 v[148:151], v130, s[52:55], s70 offen sc1\n" \
"ds_write_b64 v128, v[138:139] offset:4640\n" \
"v_mfma_f32_16x16x32_bf16 v[108:111], a[220:223], a[60:63], v[108:111]\n" \
"s_add_u32 s70, s88, 69632\n" \
"s_waitcnt lgkmcnt(2)\n" \
"v_mfma_f32_16x16x32_bf16 v[108:111], a[216:219], a[56:59], v[108:111]\n" \
"buffer_store_dwordx4 v[152:155], v130, s[52:55], s70 offen sc1\n" \
"v_mfma_f32_16x16x32_bf16 v[108:111], a[188:191], a[52:55], v[108:111]\n" \
"v_mfma_f32_16x16x32_bf16 v[108:111], a[184:187], a[48:51], v[108:111]\n" \
"v_cvt_pk_bf16_f32 v136, v104, v105\n" \
"v_cvt_pk_bf16_f32 v137, v106, v107\n" \
"v_mfma_f32_16x16x32_bf16 v[108:111], a[156:159], a[252:255], v[108:111]\n" \
"v_mfma_f32_16x16x32_bf16 v[108:111], a[152:155], a[248:251], v[108:111]\n" \
"ds_write_b64 v128, v[136:137] offset:4672\n" \
"v_mfma_f32_16x16x32_bf16 v[112:115], a[128:131], a[224:227], v[112:115]\n" \
"v_mfma_f32_16x16x32_bf16 v[112:115], a[132:135], a[228:231], v[112:115]\n" \
"v_mfma_f32_16x16x32_bf16 v[112:115], a[160:163], a[232:235], v[112:115]\n" \
"v_mfma_f32_16x16x32_bf16 v[112:115], a[164:167], a[236:239], v[112:115]\n" \
"v_cvt_pk_bf16_f32 v138, v108, v109\n" \
"v_cvt_pk_bf16_f32 v139, v110, v111\n" \
"v_mfma_f32_16x16x32_bf16 v[112:115], a[192:195], a[240:243], v[112:115]\n" \
"v_mfma_f32_16x16x32_bf16 v[112:115], a[196:199], a[244:247], v[112:115]\n" \
"ds_write_b64 v128, v[138:139] offset:4704\n" \
"v_mfma_f32_16x16x32_bf16 v[116:119], a[204:207], a[244:247], v[116:119]\n" \
"ds_read_b128 v[156:159], v129 offset:4608\n" \
"ds_read_b128 v[160:163], v129 offset:5760\n" \
"v_mfma_f32_16x16x32_bf16 v[116:119], a[200:203], a[240:243], v[116:119]\n" \
"v_mfma_f32_16x16x32_bf16 v[116:119], a[172:175], a[236:239], v[116:119]\n" \
"v_mfma_f32_16x16x32_bf16 v[116:119], a[168:171], a[232:235], v[116:119]\n" \
"v_cvt_pk_bf16_f32 v136, v112, v113\n" \
"v_cvt_pk_bf16_f32 v137, v114, v115\n" \
"v_mfma_f32_16x16x32_bf16 v[116:119], a[140:143], a[228:231], v[116:119]\n" \
"s_add_u32 s70, s88, 131072\n" \
"s_waitcnt lgkmcnt(1)\n" \
"v_mfma_f32_16x16x32_bf16 v[116:119], a[136:139], a[224:227], v[116:119]\n" \
"buffer_store_dwordx4 v[156:159], v130, s[52:55], s70 offen sc1\n" \
"ds_write_b64 v128, v[136:137] offset:6912\n" \
"v_mfma_f32_16x16x32_bf16 v[120:123], a[144:147], a[224:227], v[120:123]\n" \
"s_add_u32 s70, s88, 196608\n" \
"s_waitcnt lgkmcnt(1)\n" \
"v_mfma_f32_16x16x32_bf16 v[120:123], a[148:151], a[228:231], v[120:123]\n" \
"buffer_store_dwordx4 v[160:163], v130, s[52:55], s70 offen sc1\n" \
"v_mfma_f32_16x16x32_bf16 v[120:123], a[176:179], a[232:235], v[120:123]\n" \
"v_mfma_f32_16x16x32_bf16 v[120:123], a[180:183], a[236:239], v[120:123]\n" \
"v_cvt_pk_bf16_f32 v138, v116, v117\n" \
"v_cvt_pk_bf16_f32 v139, v118, v119\n" \
"v_mfma_f32_16x16x32_bf16 v[120:123], a[208:211], a[240:243], v[120:123]\n" \
"v_mfma_f32_16x16x32_bf16 v[120:123], a[212:215], a[244:247], v[120:123]\n" \
"ds_write_b64 v128, v[138:139] offset:6944\n" \
"v_mfma_f32_16x16x32_bf16 v[124:127], a[220:223], a[244:247], v[124:127]\n" \
"v_mfma_f32_16x16x32_bf16 v[124:127], a[216:219], a[240:243], v[124:127]\n" \
"v_mfma_f32_16x16x32_bf16 v[124:127], a[188:191], a[236:239], v[124:127]\n" \
"v_mfma_f32_16x16x32_bf16 v[124:127], a[184:187], a[232:235], v[124:127]\n" \
"v_cvt_pk_bf16_f32 v136, v120, v121\n" \
"v_cvt_pk_bf16_f32 v137, v122, v123\n" \
"v_mfma_f32_16x16x32_bf16 v[124:127], a[156:159], a[228:231], v[124:127]\n" \
"v_mfma_f32_16x16x32_bf16 v[124:127], a[152:155], a[224:227], v[124:127]\n" \
"ds_write_b64 v128, v[136:137] offset:6976\n" \
"s_nop 7\n" \
"v_cvt_pk_bf16_f32 v138, v124, v125\n" \
"v_cvt_pk_bf16_f32 v139, v126, v127\n" \
"ds_write_b64 v128, v[138:139] offset:7008\n" \
"ds_read_b128 v[164:167], v129 offset:6912\n" \
"ds_read_b128 v[168:171], v129 offset:8064\n" \
"s_add_u32 s70, s88, 135168\n" \
"s_waitcnt lgkmcnt(1)\n" \
"buffer_store_dwordx4 v[164:167], v130, s[52:55], s70 offen sc1\n" \
"s_add_u32 s70, s88, 200704\n" \
"s_waitcnt lgkmcnt(0)\n" \
"buffer_store_dwordx4 v[168:171], v130, s[52:55], s70 offen sc1\n" \
"s_endpgm\n" \
"Lhot_end_%=:\n" \
"Lpl1_%=:\n" \
"s_mov_b32 s72, 0\n" \
"Lpk1_%=:\n" \
"s_sleep 2\n" \
"buffer_load_dword v227, v242, s[56:59], s90 offen sc1\n" \
"s_waitcnt vmcnt(0)\n" \
"v_cmp_eq_u32 vcc, s60, v227\n" \
"s_nop 1\n" \
"s_andn2_b32 s70, 0xff, vcc_lo\n" \
"s_cbranch_scc0 Lpr1_%=\n" \
"s_add_u32 s72, s72, 1\n" \
"s_cmp_lt_u32 s72, 0x40000\n" \
"s_cbranch_scc1 Lpk1_%=\n" \
"s_branch Lpr1_%=\n" \
"Lpl2_%=:\n" \
"s_mov_b32 s72, 0\n" \
"Lpk2_%=:\n" \
"s_sleep 2\n" \
"buffer_load_dword v239, v242, s[56:59], s95 offen sc1\n" \
"s_waitcnt vmcnt(0)\n" \
"v_cmp_eq_u32 vcc, s60, v239\n" \
"s_nop 1\n" \
"s_andn2_b32 s70, s69, vcc_lo\n" \
"s_cbranch_scc0 Lpr2_%=\n" \
"s_add_u32 s72, s72, 1\n" \
"s_cmp_lt_u32 s72, 0x40000\n" \
"s_cbranch_scc1 Lpk2_%=\n" \
"s_branch Lpr2_%=\n" \
"Lpl3_%=:\n" \
"s_mov_b32 s72, 0\n" \
"Lpk3_%=:\n" \
"s_sleep 2\n" \
"buffer_load_dword v239, v242, s[56:59], s95 offen sc1\n" \
"s_waitcnt vmcnt(0)\n" \
"v_cmp_eq_u32 vcc, s60, v239\n" \
"s_nop 1\n" \
"s_andn2_b32 s70, s69, vcc_lo\n" \
"s_cbranch_scc0 Lpr3_%=\n" \
"s_add_u32 s72, s72, 1\n" \
"s_cmp_lt_u32 s72, 0x40000\n" \
"s_cbranch_scc1 Lpk3_%=\n" \
"s_branch Lpr3_%=\n" \
"Lexact_%=:\n" \
"v_mov_b32 v239, 0xff800000\n" \
"s_and_b32 s70, s61, 1\n" \
"s_lshl_b32 s70, s70, 4\n" \
"v_add_u32 v186, s70, v238\n" \
"v_accvgpr_read_b32 v160, a128\n" \
"v_accvgpr_read_b32 v161, a129\n" \
"v_accvgpr_read_b32 v162, a130\n" \
"v_accvgpr_read_b32 v163, a131\n" \
"v_accvgpr_read_b32 v164, a132\n" \
"v_accvgpr_read_b32 v165, a133\n" \
"v_accvgpr_read_b32 v166, a134\n" \
"v_accvgpr_read_b32 v167, a135\n" \
"v_accvgpr_read_b32 v168, a136\n" \
"v_accvgpr_read_b32 v169, a137\n" \
"v_accvgpr_read_b32 v170, a138\n" \
"v_accvgpr_read_b32 v171, a139\n" \
"v_accvgpr_read_b32 v172, a140\n" \
"v_accvgpr_read_b32 v173, a141\n" \
"v_accvgpr_read_b32 v174, a142\n" \
"v_accvgpr_read_b32 v175, a143\n" \
"s_add_u32 s83, s85, 0\n" \
"s_add_u32 s84, s87, 0\n" \
"s_call_b64 s[64:65], Lex1_%=\n" \
"v_accvgpr_read_b32 v160, a144\n" \
"v_accvgpr_read_b32 v161, a145\n" \
"v_accvgpr_read_b32 v162, a146\n" \
"v_accvgpr_read_b32 v163, a147\n" \
"v_accvgpr_read_b32 v164, a148\n" \
"v_accvgpr_read_b32 v165, a149\n" \
"v_accvgpr_read_b32 v166, a150\n" \
"v_accvgpr_read_b32 v167, a151\n" \
"v_accvgpr_read_b32 v168, a152\n" \
"v_accvgpr_read_b32 v169, a153\n" \
"v_accvgpr_read_b32 v170, a154\n" \
"v_accvgpr_read_b32 v171, a155\n" \
"v_accvgpr_read_b32 v172, a156\n" \
"v_accvgpr_read_b32 v173, a157\n" \
"v_accvgpr_read_b32 v174, a158\n" \
"v_accvgpr_read_b32 v175, a159\n" \
"s_add_u32 s83, s85, 2\n" \
"s_add_u32 s84, s87, 532480\n" \
"s_call_b64 s[64:65], Lex1_%=\n" \
"v_accvgpr_read_b32 v160, a160\n" \
"v_accvgpr_read_b32 v161, a161\n" \
"v_accvgpr_read_b32 v162, a162\n" \
"v_accvgpr_read_b32 v163, a163\n" \
"v_accvgpr_read_b32 v164, a164\n" \
"v_accvgpr_read_b32 v165, a165\n" \
"v_accvgpr_read_b32 v166, a166\n" \
"v_accvgpr_read_b32 v167, a167\n" \
"v_accvgpr_read_b32 v168, a168\n" \
"v_accvgpr_read_b32 v169, a169\n" \
"v_accvgpr_read_b32 v170, a170\n" \
"v_accvgpr_read_b32 v171, a171\n" \
"v_accvgpr_read_b32 v172, a172\n" \
"v_accvgpr_read_b32 v173, a173\n" \
"v_accvgpr_read_b32 v174, a174\n" \
"v_accvgpr_read_b32 v175, a175\n" \
"s_add_u32 s83, s86, 0\n" \
"s_add_u32 s84, s88, 0\n" \
"s_call_b64 s[64:65], Lex1_%=\n" \
"v_accvgpr_read_b32 v160, a176\n" \
"v_accvgpr_read_b32 v161, a177\n" \
"v_accvgpr_read_b32 v162, a178\n" \
"v_accvgpr_read_b32 v163, a179\n" \
"v_accvgpr_read_b32 v164, a180\n" \
"v_accvgpr_read_b32 v165, a181\n" \
"v_accvgpr_read_b32 v166, a182\n" \
"v_accvgpr_read_b32 v167, a183\n" \
"v_accvgpr_read_b32 v168, a184\n" \
"v_accvgpr_read_b32 v169, a185\n" \
"v_accvgpr_read_b32 v170, a186\n" \
"v_accvgpr_read_b32 v171, a187\n" \
"v_accvgpr_read_b32 v172, a188\n" \
"v_accvgpr_read_b32 v173, a189\n" \
"v_accvgpr_read_b32 v174, a190\n" \
"v_accvgpr_read_b32 v175, a191\n" \
"s_add_u32 s83, s86, 2\n" \
"s_add_u32 s84, s88, 532480\n" \
"s_call_b64 s[64:65], Lex1_%=\n" \
"s_waitcnt vmcnt(0)\n" \
"s_branch Lexr_%=\n" \
"Lex1_%=:\n" \
"v_mov_b32 v128, 0\n" \
"v_mov_b32 v129, 0\n" \
"v_mov_b32 v130, 0\n" \
"v_mov_b32 v131, 0\n" \
"v_mov_b32 v132, 0\n" \
"v_mov_b32 v133, 0\n" \
"v_mov_b32 v134, 0\n" \
"v_mov_b32 v135, 0\n" \
"v_mov_b32 v136, 0\n" \
"v_mov_b32 v137, 0\n" \
"v_mov_b32 v138, 0\n" \
"v_mov_b32 v139, 0\n" \
"v_mov_b32 v140, 0\n" \
"v_mov_b32 v141, 0\n" \
"v_mov_b32 v142, 0\n" \
"v_mov_b32 v143, 0\n" \
"v_mov_b32 v144, 0\n" \
"v_mov_b32 v145, 0\n" \
"v_mov_b32 v146, 0\n" \
"v_mov_b32 v147, 0\n" \
"v_mov_b32 v148, 0\n" \
"v_mov_b32 v149, 0\n" \
"v_mov_b32 v150, 0\n" \
"v_mov_b32 v151, 0\n" \
"v_mov_b32 v152, 0\n" \
"v_mov_b32 v153, 0\n" \
"v_mov_b32 v154, 0\n" \
"v_mov_b32 v155, 0\n" \
"v_mov_b32 v156, 0\n" \
"v_mov_b32 v157, 0\n" \
"v_mov_b32 v158, 0\n" \
"v_mov_b32 v159, 0\n" \
"v_mov_b32 v184, 0xf149f2ca\n" \
"v_mov_b32 v185, 0\n" \
"s_mov_b32 s81, 0\n" \
"s_mul_i32 s82, s62, 557824\n" \
"Lex2_%=:\n" \
"s_add_u32 s70, s82, 4096\n" \
"buffer_load_dwordx4 a[64:67], v236, s[56:59], s82 offen offset:0 sc1\n" \
"buffer_load_dwordx4 a[68:71], v236, s[56:59], s82 offen offset:2048 sc1\n" \
"buffer_load_dwordx4 a[72:75], v236, s[56:59], s70 offen offset:0 sc1\n" \
"buffer_load_dwordx4 a[76:79], v236, s[56:59], s70 offen offset:2048 sc1\n" \
"buffer_load_dwordx4 a[80:83], v236, s[56:59], s82 offen offset:256 sc1\n" \
"buffer_load_dwordx4 a[84:87], v236, s[56:59], s82 offen offset:2304 sc1\n" \
"buffer_load_dwordx4 a[88:91], v236, s[56:59], s70 offen offset:256 sc1\n" \
"buffer_load_dwordx4 a[92:95], v236, s[56:59], s70 offen offset:2304 sc1\n" \
"s_add_u32 s70, s82, 17850368\n" \
"s_add_u32 s71, s70, 4096\n" \
"buffer_load_dwordx4 a[96:99], v234, s[56:59], s70 offen offset:0 sc1\n" \
"buffer_load_dwordx4 a[100:103], v234, s[56:59], s70 offen offset:1024 sc1\n" \
"buffer_load_dwordx4 a[104:107], v234, s[56:59], s70 offen offset:2048 sc1\n" \
"buffer_load_dwordx4 a[108:111], v234, s[56:59], s70 offen offset:3072 sc1\n" \
"buffer_load_dwordx4 a[112:115], v234, s[56:59], s71 offen offset:0 sc1\n" \
"buffer_load_dwordx4 a[116:119], v234, s[56:59], s71 offen offset:1024 sc1\n" \
"buffer_load_dwordx4 a[120:123], v234, s[56:59], s71 offen offset:2048 sc1\n" \
"buffer_load_dwordx4 a[124:127], v234, s[56:59], s71 offen offset:3072 sc1\n" \
"s_waitcnt vmcnt(0)\n" \
"v_mfma_f32_16x16x32_bf16 v[176:179], a[64:67], v[160:163], 0\n" \
"v_mfma_f32_16x16x32_bf16 v[176:179], a[68:71], v[164:167], v[176:179]\n" \
"v_mfma_f32_16x16x32_bf16 v[176:179], a[72:75], v[168:171], v[176:179]\n" \
"v_mfma_f32_16x16x32_bf16 v[176:179], a[76:79], v[172:175], v[176:179]\n" \
"v_mfma_f32_16x16x32_bf16 v[180:183], a[80:83], v[160:163], 0\n" \
"v_mfma_f32_16x16x32_bf16 v[180:183], a[84:87], v[164:167], v[180:183]\n" \
"v_mfma_f32_16x16x32_bf16 v[180:183], a[88:91], v[168:171], v[180:183]\n" \
"v_mfma_f32_16x16x32_bf16 v[180:183], a[92:95], v[172:175], v[180:183]\n" \
"s_nop 7\n" \
"s_nop 1\n" \
"s_cmp_eq_u32 s81, s83\n" \
"s_cbranch_scc0 Lex3_%=\n" \
"v_cmp_gt_i32 vcc, 0, v186\n" \
"s_nop 1\n" \
"v_cndmask_b32 v176, v176, v239, vcc\n" \
"v_cmp_gt_i32 vcc, 1, v186\n" \
"s_nop 1\n" \
"v_cndmask_b32 v177, v177, v239, vcc\n" \
"v_cmp_gt_i32 vcc, 2, v186\n" \
"s_nop 1\n" \
"v_cndmask_b32 v178, v178, v239, vcc\n" \
"v_cmp_gt_i32 vcc, 3, v186\n" \
"s_nop 1\n" \
"v_cndmask_b32 v179, v179, v239, vcc\n" \
"v_cmp_gt_i32 vcc, 16, v186\n" \
"s_nop 1\n" \
"v_cndmask_b32 v180, v180, v239, vcc\n" \
"v_cmp_gt_i32 vcc, 17, v186\n" \
"s_nop 1\n" \
"v_cndmask_b32 v181, v181, v239, vcc\n" \
"v_cmp_gt_i32 vcc, 18, v186\n" \
"s_nop 1\n" \
"v_cndmask_b32 v182, v182, v239, vcc\n" \
"v_cmp_gt_i32 vcc, 19, v186\n" \
"s_nop 1\n" \
"v_cndmask_b32 v183, v183, v239, vcc\n" \
"Lex3_%=:\n" \
"v_max3_f32 v187, v176, v177, v178\n" \
"v_max3_f32 v187, v187, v179, v180\n" \
"v_max3_f32 v187, v187, v181, v182\n" \
"v_max3_f32 v187, v187, v183, v184\n" \
"v_mov_b32 v188, v187\n" \
"s_nop 1\n" \
"v_permlane16_swap_b32 v188, v187\n" \
"v_max_f32 v187, v187, v188\n" \
"v_mov_b32 v188, v187\n" \
"s_nop 1\n" \
"v_permlane32_swap_b32 v188, v187\n" \
"v_max_f32 v187, v187, v188\n" \
"v_sub_f32 v189, v184, v187\n" \
"v_exp_f32 v189, v189\n" \
"v_mov_b32 v184, v187\n" \
"v_sub_f32 v176, v176, v187\n" \
"v_sub_f32 v177, v177, v187\n" \
"v_sub_f32 v178, v178, v187\n" \
"v_sub_f32 v179, v179, v187\n" \
"v_sub_f32 v180, v180, v187\n" \
"v_sub_f32 v181, v181, v187\n" \
"v_sub_f32 v182, v182, v187\n" \
"v_sub_f32 v183, v183, v187\n" \
"v_exp_f32 v176, v176\n" \
"v_exp_f32 v177, v177\n" \
"v_exp_f32 v178, v178\n" \
"v_exp_f32 v179, v179\n" \
"v_exp_f32 v180, v180\n" \
"v_exp_f32 v181, v181\n" \
"v_exp_f32 v182, v182\n" \
"v_exp_f32 v183, v183\n" \
"v_mul_f32 v185, v185, v189\n" \
"v_add_f32 v185, v185, v176\n" \
"v_add_f32 v185, v185, v177\n" \
"v_add_f32 v185, v185, v178\n" \
"v_add_f32 v185, v185, v179\n" \
"v_add_f32 v185, v185, v180\n" \
"v_add_f32 v185, v185, v181\n" \
"v_add_f32 v185, v185, v182\n" \
"v_add_f32 v185, v185, v183\n" \
"v_cvt_pk_bf16_f32 v112, v176, v177\n" \
"v_cvt_pk_bf16_f32 v113, v178, v179\n" \
"v_cvt_pk_bf16_f32 v114, v180, v181\n" \
"v_cvt_pk_bf16_f32 v115, v182, v183\n" \
"v_mul_f32 v128, v128, v189\n" \
"v_mul_f32 v129, v129, v189\n" \
"v_mul_f32 v130, v130, v189\n" \
"v_mul_f32 v131, v131, v189\n" \
"v_mul_f32 v132, v132, v189\n" \
"v_mul_f32 v133, v133, v189\n" \
"v_mul_f32 v134, v134, v189\n" \
"v_mul_f32 v135, v135, v189\n" \
"v_mul_f32 v136, v136, v189\n" \
"v_mul_f32 v137, v137, v189\n" \
"v_mul_f32 v138, v138, v189\n" \
"v_mul_f32 v139, v139, v189\n" \
"v_mul_f32 v140, v140, v189\n" \
"v_mul_f32 v141, v141, v189\n" \
"v_mul_f32 v142, v142, v189\n" \
"v_mul_f32 v143, v143, v189\n" \
"v_mul_f32 v144, v144, v189\n" \
"v_mul_f32 v145, v145, v189\n" \
"v_mul_f32 v146, v146, v189\n" \
"v_mul_f32 v147, v147, v189\n" \
"v_mul_f32 v148, v148, v189\n" \
"v_mul_f32 v149, v149, v189\n" \
"v_mul_f32 v150, v150, v189\n" \
"v_mul_f32 v151, v151, v189\n" \
"v_mul_f32 v152, v152, v189\n" \
"v_mul_f32 v153, v153, v189\n" \
"v_mul_f32 v154, v154, v189\n" \
"v_mul_f32 v155, v155, v189\n" \
"v_mul_f32 v156, v156, v189\n" \
"v_mul_f32 v157, v157, v189\n" \
"v_mul_f32 v158, v158, v189\n" \
"v_mul_f32 v159, v159, v189\n" \
"s_nop 1\n" \
"v_mfma_f32_16x16x32_bf16 v[128:131], a[96:99], v[112:115], v[128:131]\n" \
"v_mfma_f32_16x16x32_bf16 v[132:135], a[100:103], v[112:115], v[132:135]\n" \
"v_mfma_f32_16x16x32_bf16 v[136:139], a[104:107], v[112:115], v[136:139]\n" \
"v_mfma_f32_16x16x32_bf16 v[140:143], a[108:111], v[112:115], v[140:143]\n" \
"v_mfma_f32_16x16x32_bf16 v[144:147], a[112:115], v[112:115], v[144:147]\n" \
"v_mfma_f32_16x16x32_bf16 v[148:151], a[116:119], v[112:115], v[148:151]\n" \
"v_mfma_f32_16x16x32_bf16 v[152:155], a[120:123], v[112:115], v[152:155]\n" \
"v_mfma_f32_16x16x32_bf16 v[156:159], a[124:127], v[112:115], v[156:159]\n" \
"s_nop 7\n" \
"s_nop 1\n" \
"s_and_b32 s70, s81, 1\n" \
"s_mul_i32 s70, s70, 1024\n" \
"s_add_u32 s82, s82, s70\n" \
"s_add_u32 s82, s82, 8192\n" \
"s_add_u32 s81, s81, 1\n" \
"s_cmp_le_u32 s81, s83\n" \
"s_cbranch_scc1 Lex2_%=\n" \
"v_mov_b32 v187, v185\n" \
"s_nop 1\n" \
"v_permlane16_swap_b32 v187, v185\n" \
"v_add_f32 v185, v185, v187\n" \
"v_mov_b32 v187, v185\n" \
"s_nop 1\n" \
"v_permlane32_swap_b32 v187, v185\n" \
"v_add_f32 v185, v185, v187\n" \
"v_rcp_f32 v185, v185\n" \
"s_nop 0\n" \
"v_mul_f32 v128, v128, v185\n" \
"v_mul_f32 v129, v129, v185\n" \
"v_mul_f32 v130, v130, v185\n" \
"v_mul_f32 v131, v131, v185\n" \
"v_mul_f32 v132, v132, v185\n" \
"v_mul_f32 v133, v133, v185\n" \
"v_mul_f32 v134, v134, v185\n" \
"v_mul_f32 v135, v135, v185\n" \
"v_mul_f32 v136, v136, v185\n" \
"v_mul_f32 v137, v137, v185\n" \
"v_mul_f32 v138, v138, v185\n" \
"v_mul_f32 v139, v139, v185\n" \
"v_mul_f32 v140, v140, v185\n" \
"v_mul_f32 v141, v141, v185\n" \
"v_mul_f32 v142, v142, v185\n" \
"v_mul_f32 v143, v143, v185\n" \
"v_mul_f32 v144, v144, v185\n" \
"v_mul_f32 v145, v145, v185\n" \
"v_mul_f32 v146, v146, v185\n" \
"v_mul_f32 v147, v147, v185\n" \
"v_mul_f32 v148, v148, v185\n" \
"v_mul_f32 v149, v149, v185\n" \
"v_mul_f32 v150, v150, v185\n" \
"v_mul_f32 v151, v151, v185\n" \
"v_mul_f32 v152, v152, v185\n" \
"v_mul_f32 v153, v153, v185\n" \
"v_mul_f32 v154, v154, v185\n" \
"v_mul_f32 v155, v155, v185\n" \
"v_mul_f32 v156, v156, v185\n" \
"v_mul_f32 v157, v157, v185\n" \
"v_mul_f32 v158, v158, v185\n" \
"v_mul_f32 v159, v159, v185\n" \
"s_add_u32 s70, s84, 4096\n" \
"v_cvt_pk_bf16_f32 v112, v128, v129\n" \
"v_cvt_pk_bf16_f32 v113, v130, v131\n" \
"v_cvt_pk_bf16_f32 v114, v132, v133\n" \
"v_cvt_pk_bf16_f32 v115, v134, v135\n" \
"buffer_store_dwordx4 v[112:115], v236, s[56:59], s84 offen offset:0 sc1\n" \
"v_cvt_pk_bf16_f32 v116, v136, v137\n" \
"v_cvt_pk_bf16_f32 v117, v138, v139\n" \
"v_cvt_pk_bf16_f32 v118, v140, v141\n" \
"v_cvt_pk_bf16_f32 v119, v142, v143\n" \
"buffer_store_dwordx4 v[116:119], v236, s[56:59], s84 offen offset:2048 sc1\n" \
"v_cvt_pk_bf16_f32 v120, v144, v145\n" \
"v_cvt_pk_bf16_f32 v121, v146, v147\n" \
"v_cvt_pk_bf16_f32 v122, v148, v149\n" \
"v_cvt_pk_bf16_f32 v123, v150, v151\n" \
"buffer_store_dwordx4 v[120:123], v236, s[56:59], s70 offen offset:0 sc1\n" \
"v_cvt_pk_bf16_f32 v124, v152, v153\n" \
"v_cvt_pk_bf16_f32 v125, v154, v155\n" \
"v_cvt_pk_bf16_f32 v126, v156, v157\n" \
"v_cvt_pk_bf16_f32 v127, v158, v159\n" \
"buffer_store_dwordx4 v[124:127], v236, s[56:59], s70 offen offset:2048 sc1\n" \
"s_waitcnt vmcnt(0)\n" \
"s_setpc_b64 s[64:65]\n" \
"Lpl4_%=:\n" \
"s_mov_b32 s72, 0\n" \
"Lpk4_%=:\n" \
"s_sleep 2\n" \
"buffer_load_dwordx2 v[226:227], v228, s[56:59], s13 offen sc1\n" \
"s_waitcnt vmcnt(0)\n" \
"v_cmp_eq_u32 vcc, s60, v226\n" \
"s_nop 1\n" \
"s_andn2_b32 s70, 0xff, vcc_lo\n" \
"s_cbranch_scc0 Lpr4_%=\n" \
"s_add_u32 s72, s72, 1\n" \
"s_cmp_lt_u32 s72, 0x40000\n" \
"s_cbranch_scc1 Lpk4_%=\n" \
"s_branch Lpr4_%=\n" \
"Lpl5_%=:\n" \
"s_mov_b32 s72, 0\n" \
"Lpk5_%=:\n" \
"s_sleep 2\n" \
"buffer_load_dword v220, v221, s[56:59], s66 offen sc1\n" \
"s_waitcnt vmcnt(0)\n" \
"v_cmp_eq_u32 vcc, s60, v220\n" \
"s_nop 1\n" \
"s_andn2_b32 s70, s65, vcc_lo\n" \
"s_cbranch_scc0 Lpr5_%=\n" \
"s_add_u32 s72, s72, 1\n" \
"s_cmp_lt_u32 s72, 0x40000\n" \
"s_cbranch_scc1 Lpk5_%=\n" \
"s_branch Lpr5_%=\n" \
"Lpl6_%=:\n" \
"s_mov_b32 s72, 0\n" \
"Lpk6_%=:\n" \
"s_sleep 2\n" \
"buffer_load_dword v220, v221, s[56:59], s67 offen sc1\n" \
"s_waitcnt vmcnt(0)\n" \
"v_cmp_eq_u32 vcc, s60, v220\n" \
"s_nop 1\n" \
"s_andn2_b32 s70, s74, vcc_lo\n" \
"s_cbranch_scc0 Lpr6_%=\n" \
"s_add_u32 s72, s72, 1\n" \
"s_cmp_lt_u32 s72, 0x40000\n" \
"s_cbranch_scc1 Lpk6_%=\n" \
"s_branch Lpr6_%=\n" \
"Lpl7_%=:\n" \
"s_mov_b32 s72, 0\n" \
"Lpk7_%=:\n" \
"s_sleep 2\n" \
"buffer_load_dword v220, v241, s[56:59], s64 offen sc1\n" \
"s_waitcnt vmcnt(0)\n" \
"v_cmp_eq_u32 vcc, s60, v220\n" \
"s_nop 1\n" \
"s_andn2_b32 s70, s69, vcc_lo\n" \
"s_cbranch_scc0 Lpr7_%=\n" \
"s_add_u32 s72, s72, 1\n" \
"s_cmp_lt_u32 s72, 0x40000\n" \
"s_cbranch_scc1 Lpk7_%=\n" \
"s_branch Lpr7_%=\n" \
"Lcode_end_%=:\n" \
".p2align 12\n" \
""
#define MK_ASM_CLOBBERS "v1", "v2", "v3", "v4", "v5", "v6", "v7", "v8", "v9", "v10", "v11", "v12", "v13", "v14", "v15", "v16", "v17", "v18", "v19", "v20", "v21", "v22", "v23", "v24", "v25", "v26", "v27", "v28", "v29", "v30", "v31", "v32", "v33", "v34", "v35", "v36", "v37", "v38", "v39", "v40", "v41", "v42", "v43", "v44", "v45", "v46", "v47", "v48", "v49", "v50", "v51", "v52", "v53", "v54", "v55", "v56", "v57", "v58", "v59", "v60", "v61", "v62", "v63", "v64", "v65", "v66", "v67", "v68", "v69", "v70", "v71", "v72", "v73", "v74", "v75", "v76", "v77", "v78", "v79", "v80", "v81", "v82", "v83", "v84", "v85", "v86", "v87", "v88", "v89", "v90", "v91", "v92", "v93", "v94", "v95", "v96", "v97", "v98", "v99", "v100", "v101", "v102", "v103", "v104", "v105", "v106", "v107", "v108", "v109", "v110", "v111", "v112", "v113", "v114", "v115", "v116", "v117", "v118", "v119", "v120", "v121", "v122", "v123", "v124", "v125", "v126", "v127", "v128", "v129", "v130", "v131", "v132", "v133", "v134", "v135", "v136", "v137", "v138", "v139", "v140", "v141", "v142", "v143", "v144", "v145", "v146", "v147", "v148", "v149", "v150", "v151", "v152", "v153", "v154", "v155", "v156", "v157", "v158", "v159", "v160", "v161", "v162", "v163", "v164", "v165", "v166", "v167", "v168", "v169", "v170", "v171", "v172", "v173", "v174", "v175", "v176", "v177", "v178", "v179", "v180", "v181", "v182", "v183", "v184", "v185", "v186", "v187", "v188", "v189", "v190", "v191", "v192", "v193", "v194", "v195", "v196", "v197", "v198", "v199", "v200", "v201", "v202", "v203", "v204", "v205", "v206", "v207", "v208", "v209", "v210", "v211", "v212", "v213", "v214", "v215", "v216", "v217", "v218", "v219", "v220", "v221", "v222", "v223", "v224", "v225", "v226", "v227", "v228", "v229", "v230", "v231", "v232", "v233", "v234", "v235", "v236", "v237", "v238", "v239", "v240", "v241", "v242", "v243", "v244", "v245", "v246", "v247", "v248", "v249", "v250", "v251", "v252", "v253", "v254", "v255", "a0", "a1", "a2", "a3", "a4", "a5", "a6", "a7", "a8", "a9", "a10", "a11", "a12", "a13", "a14", "a15", "a16", "a17", "a18", "a19", "a20", "a21", "a22", "a23", "a24", "a25", "a26", "a27", "a28", "a29", "a30", "a31", "a32", "a33", "a34", "a35", "a36", "a37", "a38", "a39", "a40", "a41", "a42", "a43", "a44", "a45", "a46", "a47", "a48", "a49", "a50", "a51", "a52", "a53", "a54", "a55", "a56", "a57", "a58", "a59", "a60", "a61", "a62", "a63", "a64", "a65", "a66", "a67", "a68", "a69", "a70", "a71", "a72", "a73", "a74", "a75", "a76", "a77", "a78", "a79", "a80", "a81", "a82", "a83", "a84", "a85", "a86", "a87", "a88", "a89", "a90", "a91", "a92", "a93", "a94", "a95", "a96", "a97", "a98", "a99", "a100", "a101", "a102", "a103", "a104", "a105", "a106", "a107", "a108", "a109", "a110", "a111", "a112", "a113", "a114", "a115", "a116", "a117", "a118", "a119", "a120", "a121", "a122", "a123", "a124", "a125", "a126", "a127", "a128", "a129", "a130", "a131", "a132", "a133", "a134", "a135", "a136", "a137", "a138", "a139", "a140", "a141", "a142", "a143", "a144", "a145", "a146", "a147", "a148", "a149", "a150", "a151", "a152", "a153", "a154", "a155", "a156", "a157", "a158", "a159", "a160", "a161", "a162", "a163", "a164", "a165", "a166", "a167", "a168", "a169", "a170", "a171", "a172", "a173", "a174", "a175", "a176", "a177", "a178", "a179", "a180", "a181", "a182", "a183", "a184", "a185", "a186", "a187", "a188", "a189", "a190", "a191", "a192", "a193", "a194", "a195", "a196", "a197", "a198", "a199", "a200", "a201", "a202", "a203", "a204", "a205", "a206", "a207", "a208", "a209", "a210", "a211", "a212", "a213", "a214", "a215", "a216", "a217", "a218", "a219", "a220", "a221", "a222", "a223", "a224", "a225", "a226", "a227", "a228", "a229", "a230", "a231", "a232", "a233", "a234", "a235", "a236", "a237", "a238", "a239", "a240", "a241", "a242", "a243", "a244", "a245", "a246", "a247", "a248", "a249", "a250", "a251", "a252", "a253", "a254", "a255", "s8", "s9", "s10", "s11", "s12", "s13", "s14", "s15", "s16", "s17", "s18", "s19", "s20", "s21", "s22", "s23", "s24", "s25", "s26", "s27", "s28", "s29", "s30", "s31", "s36", "s37", "s38", "s39", "s40", "s41", "s42", "s43", "s44", "s45", "s46", "s47", "s48", "s49", "s50", "s51", "s52", "s53", "s54", "s55", "s56", "s57", "s58", "s59", "s60", "s61", "s62", "s63", "s64", "s65", "s66", "s67", "s68", "s69", "s70", "s71", "s72", "s73", "s74", "s75", "s76", "s77", "s78", "s79", "s80", "s81", "s82", "s83", "s84", "s85", "s86", "s87", "s88", "s89", "s90", "s91", "s92", "s93", "s94", "s95", "vcc", "scc", "memory"
namespace mk {
constexpr int LDS_BYTES = 163840;
struct Args { const void* in[9]; void* out; void* ws; };
__global__ __launch_bounds__(256, 1) void mega_k(const Args a) {
    extern __shared__ __attribute__((aligned(16))) unsigned char shm[];
    const unsigned ldsb = (unsigned)(size_t)(LAS unsigned char*)shm;
    asm volatile(MK_ASM_BODY
                 :
                 : "s"(__builtin_amdgcn_kernarg_segment_ptr()), "s"(blockIdx.x), "v"(threadIdx.x), "s"(ldsb)
                 : MK_ASM_CLOBBERS);
}
}
extern "C" void kernel_launch(void* const* d_in, const int* in_sizes, int n_in, void* d_out, int out_size, void* d_ws, size_t ws_size, hipStream_t stream) {
    (void)hipFuncSetAttribute((const void*)mk::mega_k, hipFuncAttributeMaxDynamicSharedMemorySize, mk::LDS_BYTES);
    mk::Args a;
    for (int i = 0; i < 9; ++i) a.in[i] = d_in[i];
    a.out = d_out; a.ws = (char*)d_ws + ((ws_size - WS_END) & ~(size_t)0x1fffff);
    mk::mega_k<<<256, 256, mk::LDS_BYTES, stream>>>(a);
}
```

```cpp
#include <hip/hip_runtime.h>
#include <stdint.h>
#define LAS __attribute__((address_space(3)))
constexpr size_t LAY_TS = 17408, LAY_HS = 557824, LAY_CRS = 266240, LAY_ORS = 69632, WS_K = 0, WS_V = 17850368, WS_CTX = 35700736, WS_OB = 52740096, WS_FL = 65585152, WS_END = 65634304, LAY_VS = 2;
#define MK_ASM_BODY \
"s_load_dwordx16 s[8:23], %0, 0x0\n" \
"s_load_dwordx4 s[24:27], %0, 0x40\n" \
"s_load_dwordx2 s[28:29], %0, 0x50\n" \
"v_readfirstlane_b32 s61, %2\n" \
"s_lshr_b32 s61, s61, 6\n" \
"v_mbcnt_lo_u32_b32 v78, -1, 0\n" \
"v_mbcnt_hi_u32_b32 v78, -1, v78\n" \
"v_lshlrev_b32 v234, 4, v78\n" \
"s_getpc_b64 s[64:65]\n" \
"Lpc_%=:\n" \
"s_lshr_b32 s70, %1, 3\n" \
"s_lshl_b32 s70, s70, 2\n" \
"s_add_u32 s70, s70, s61\n" \
"s_and_b32 s71, s70, 31\n" \
"s_lshl_b32 s71, s71, 12\n" \
"v_lshl_add_u32 v237, v234, 2, s71\n" \
"s_mov_b32 s71, Lcode_end_%=-Lpc_%=-4\n" \
"v_min_u32 v237, s71, v237\n" \
"global_load_dword v238, v237, s[64:65]\n" \
"s_and_b32 s62, %1, 7\n" \
"s_lshl_b32 s62, s62, 2\n" \
"s_lshr_b32 s70, %1, 6\n" \
"s_add_u32 s62, s62, s70\n" \
"s_bfe_u32 s63, %1, 0x30003\n" \
"s_lshl_b32 s74, s61, 13\n" \
"s_add_u32 s74, s74, %3\n" \
"v_bfe_u32 v79, v78, 4, 1\n" \
"v_bfe_u32 v80, v78, 2, 2\n" \
"v_lshl_add_u32 v80, v79, 3, v80\n" \
"s_lshl_b32 s71, s61, 4\n" \
"v_add_u32 v80, s71, v80\n" \
"v_lshlrev_b32 v80, 13, v80\n" \
"v_lshlrev_b32 v79, 1, v79\n" \
"v_and_b32 v81, 3, v78\n" \
"v_xor_b32 v79, v79, v81\n" \
"v_lshl_add_u32 v80, v79, 4, v80\n" \
"v_lshrrev_b32 v90, 5, v78\n" \
"v_lshl_add_u32 v84, v90, 6, v80\n" \
"v_add_u32 v68, 4096, v84\n" \
"v_add_u32 v69, 3072, v84\n" \
"v_add_u32 v70, 2176, v84\n" \
"v_add_u32 v71, 1152, v84\n" \
"s_lshl_b32 s83, s62, 8\n" \
"s_add_u32 s84, s83, 32768\n" \
"v_lshrrev_b32 v85, 3, v78\n" \
"v_and_b32 v86, 7, v78\n" \
"v_xor_b32 v86, v86, v85\n" \
"v_lshlrev_b32 v86, 4, v86\n" \
"v_lshl_add_u32 v64, v85, 10, v86\n" \
"v_add_u32 v65, 7168, v64\n" \
"v_add_u32 v66, 14336, v64\n" \
"v_add_u32 v67, 21504, v64\n" \
"s_bitcmp1_b32 s61, 0\n" \
"s_cselect_b32 s70, 0, 15\n" \
"s_xor_b32 s81, s63, s70\n" \
"s_lshl_b32 s81, s81, 17\n" \
"s_lshr_b32 s70, s61, 1\n" \
"s_lshl_b32 s71, s70, 16\n" \
"s_add_u32 s81, s81, s71\n" \
"s_add_u32 s82, s81, 32768\n" \
"s_mov_b32 s42, 4198400\n" \
"s_mov_b32 s43, 0x20000\n" \
"s_mov_b32 s46, 4198400\n" \
"s_mov_b32 s47, 0x20000\n" \
"s_mov_b32 s38, 2097152\n" \
"s_mov_b32 s39, 0x20000\n" \
"s_waitcnt lgkmcnt(0)\n" \
"s_mov_b64 s[40:41], s[14:15]\n" \
"s_sub_u32 s40, s40, 4096\n" \
"s_subb_u32 s41, s41, 0\n" \
"s_and_b32 s41, s41, 0xffff\n" \
"s_mov_b64 s[44:45], s[16:17]\n" \
"s_sub_u32 s44, s44, 4096\n" \
"s_subb_u32 s45, s45, 0\n" \
"s_and_b32 s45, s45, 0xffff\n" \
"s_mov_b64 s[36:37], s[10:11]\n" \
"s_and_b32 s37, s37, 0xffff\n" \
"s_add_u32 m0, s74, 32768\n" \
"s_nop 0\n" \
"buffer_load_dwordx4 v68, s[40:43], s83 offen offset:0 lds\n" \
"buffer_load_dwordx4 v69, s[44:47], s83 offen offset:1024 lds\n" \
"buffer_load_dwordx4 v70, s[40:43], s83 offen offset:2048 lds\n" \
"buffer_load_dwordx4 v71, s[44:47], s83 offen offset:3072 lds\n" \
"s_xor_b32 s83, s83, 0x80000\n" \
"s_add_u32 m0, s74, 36864\n" \
"s_nop 0\n" \
"buffer_load_dwordx4 v68, s[40:43], s84 offen offset:0 lds\n" \
"buffer_load_dwordx4 v69, s[44:47], s84 offen offset:1024 lds\n" \
"buffer_load_dwordx4 v70, s[40:43], s84 offen offset:2048 lds\n" \
"buffer_load_dwordx4 v71, s[44:47], s84 offen offset:3072 lds\n" \
"s_xor_b32 s84, s84, 0x80000\n" \
"s_add_u32 m0, s74, 0\n" \
"s_nop 0\n" \
"buffer_load_dwordx4 v64, s[36:39], s81 offen offset:0 lds\n" \
"buffer_load_dwordx4 v65, s[36:39], s81 offen offset:1024 lds\n" \
"buffer_load_dwordx4 v66, s[36:39], s81 offen offset:2048 lds\n" \
"buffer_load_dwordx4 v67, s[36:39], s81 offen offset:3072 lds\n" \
"s_xor_b32 s81, s81, 0x80\n" \
"s_add_u32 m0, s74, 4096\n" \
"s_nop 0\n" \
"buffer_load_dwordx4 v64, s[36:39], s82 offen offset:0 lds\n" \
"buffer_load_dwordx4 v65, s[36:39], s82 offen offset:1024 lds\n" \
"buffer_load_dwordx4 v66, s[36:39], s82 offen offset:2048 lds\n" \
"buffer_load_dwordx4 v67, s[36:39], s82 offen offset:3072 lds\n" \
"s_xor_b32 s82, s82, 0x80\n" \
"v_lshrrev_b32 v87, 4, v78\n" \
"v_and_b32 v79, 15, v78\n" \
"v_and_b32 v80, 7, v78\n" \
"v_xor_b32 v80, v80, v87\n" \
"v_lshlrev_b32 v79, 7, v79\n" \
"s_lshl_b32 s70, s70, 14\n" \
"s_add_u32 s70, s70, %3\n" \
"v_lshl_add_u32 v88, v80, 4, v79\n" \
"v_add_u32 v72, s70, v88\n" \
"v_xor_b32 v73, 64, v72\n" \
"v_lshlrev_b32 v79, 3, v81\n" \
"v_bfe_u32 v80, v78, 2, 2\n" \
"v_lshl_add_u32 v79, v80, 6, v79\n" \
"v_bfe_u32 v80, v78, 4, 1\n" \
"v_lshl_add_u32 v79, v80, 8, v79\n" \
"v_lshl_add_u32 v89, v80, 5, v79\n" \
"s_and_b32 s70, s61, 1\n" \
"s_lshl_b32 s70, s70, 11\n" \
"s_add_u32 s70, s70, %3\n" \
"v_lshl_add_u32 v82, v90, 13, v89\n" \
"v_add_u32 v74, s70, v82\n" \
"v_xor_b32 v75, 32, v74\n" \
"s_mov_b64 s[56:57], s[28:29]\n" \
"s_and_b32 s57, s57, 0xffff\n" \
"s_mov_b32 s58, 65634304\n" \
"s_mov_b32 s59, 0x20000\n" \
"v_mov_b32 v241, 0\n" \
"s_lshl_b32 s66, %1, 2\n" \
"s_add_u32 s66, s66, s61\n" \
"s_lshl_b32 s66, s66, 10\n" \
"s_lshr_b32 s67, %1, 3\n" \
"s_lshl_b32 s67, s67, 2\n" \
"s_add_u32 s67, s67, s61\n" \
"s_lshl_b32 s67, s67, 13\n" \
"s_and_b32 s64, %1, 7\n" \
"s_lshl_b32 s64, s64, 10\n" \
"s_add_u32 s67, s67, s64\n" \
"s_mov_b64 exec, 0\n" \
"s_cmp_eq_u32 s61, 0\n" \
"s_cbranch_scc1 Lst0x0_%=\n" \
"s_cmp_eq_u32 s61, 1\n" \
"s_cbranch_scc1 Lst0x1_%=\n" \
"s_cmp_eq_u32 s61, 2\n" \
"s_cbranch_scc1 Lst0x2_%=\n" \
"s_branch Lst0x3_%=\n" \
"Lcb0_%=:\n" \
"s_mov_b64 exec, -1\n" \
"s_add_u32 m0, s74, 131072\n" \
"s_nop 0\n" \
"buffer_load_dwordx4 v68, s[40:43], s83 offen offset:0 lds\n" \
"buffer_load_dwordx4 v69, s[44:47], s83 offen offset:1024 lds\n" \
"buffer_load_dwordx4 v70, s[40:43], s83 offen offset:2048 lds\n" \
"buffer_load_dwordx4 v71, s[44:47], s83 offen offset:3072 lds\n" \
"s_xor_b32 s83, s83, 0x180000\n" \
"s_add_u32 m0, s74, 135168\n" \
"s_nop 0\n" \
"buffer_load_dwordx4 v68, s[40:43], s84 offen offset:0 lds\n" \
"buffer_load_dwordx4 v69, s[44:47], s84 offen offset:1024 lds\n" \
"buffer_load_dwordx4 v70, s[40:43], s84 offen offset:2048 lds\n" \
"buffer_load_dwordx4 v71, s[44:47], s84 offen offset:3072 lds\n" \
"s_xor_b32 s84, s84, 0x180000\n" \
"s_mov_b32 s93, 65593344\n" \
"buffer_load_dword v240, v241, s[56:59], s93 offen sc1\n" \
"s_mov_b64 s[64:65], s[10:11]\n" \
"s_add_u32 s68, s66, 0x0\n" \
"s_add_u32 s64, s64, s68\n" \
"s_addc_u32 s65, s65, 0\n" \
"global_load_dword v238, v234, s[64:65]\n" \
"s_mov_b64 s[64:65], s[10:11]\n" \
"s_add_u32 s68, s66, 0x100000\n" \
"s_add_u32 s64, s64, s68\n" \
"s_addc_u32 s65, s65, 0\n" \
"global_load_dword v238, v234, s[64:65]\n" \
"s_mov_b64 s[64:65], s[14:15]\n" \
"s_add_u32 s68, s67, 0x100000\n" \
"s_add_u32 s64, s64, s68\n" \
"s_addc_u32 s65, s65, 0\n" \
"global_load_dword v238, v234, s[64:65]\n" \
"s_mov_b64 s[64:65], s[16:17]\n" \
"s_add_u32 s68, s67, 0x100000\n" \
"s_add_u32 s64, s64, s68\n" \
"s_addc_u32 s65, s65, 0\n" \
"global_load_dword v238, v234, s[64:65]\n" \
"s_waitcnt vmcnt(13)\n" \
"s_barrier\n" \
"ds_read_b64_tr_b16 a[64:65], v74 offset:32768\n" \
"ds_read_b64_tr_b16 a[66:67], v74 offset:36864\n" \
"ds_read_b64_tr_b16 a[68:69], v74 offset:49152\n" \
"ds_read_b64_tr_b16 a[70:71], v74 offset:53248\n" \
"ds_read_b128 a[0:3], v72 offset:0\n" \
"ds_read_b128 a[4:7], v73 offset:0\n" \
"ds_read_b64_tr_b16 a[72:73], v75 offset:32768\n" \
"ds_read_b64_tr_b16 a[74:75], v75 offset:36864\n" \
"ds_read_b64_tr_b16 a[76:77], v75 offset:49152\n" \
"ds_read_b64_tr_b16 a[78:79], v75 offset:53248\n" \
"ds_read_b64_tr_b16 a[80:81], v74 offset:33280\n" \
"ds_read_b64_tr_b16 a[82:83], v74 offset:37376\n" \
"ds_read_b64_tr_b16 a[84:85], v74 offset:49664\n" \
"ds_read_b64_tr_b16 a[86:87], v74 offset:53760\n" \
"ds_read_b64_tr_b16 a[88:89], v75 offset:33280\n" \
"ds_read_b64_tr_b16 a[90:91], v75 offset:37376\n" \
"ds_read_b64_tr_b16 a[92:93], v75 offset:49664\n" \
"ds_read_b64_tr_b16 a[94:95], v75 offset:53760\n" \
"ds_read_b128 a[8:11], v72 offset:2048\n" \
"ds_read_b128 a[12:15], v73 offset:2048\n" \
"s_waitcnt lgkmcnt(14)\n" \
"v_mfma_f32_16x16x32_bf16 v[0:3], a[64:67], a[0:3], 0\n" \
"s_add_u32 m0, s74, 65536\n" \
"ds_read_b128 a[16:19], v72 offset:4096\n" \
"v_mfma_f32_16x16x32_bf16 v[0:3], a[68:71], a[4:7], v[0:3]\n" \
"s_waitcnt lgkmcnt(11)\n" \
"buffer_load_dwordx4 v64, s[36:39], s81 offen offset:0 lds\n" \
"v_mfma_f32_16x16x32_bf16 v[4:7], a[72:75], a[0:3], 0\n" \
"ds_read_b128 a[20:23], v73 offset:4096\n" \
"v_mfma_f32_16x16x32_bf16 v[4:7], a[76:79], a[4:7], v[4:7]\n" \
"s_waitcnt lgkmcnt(8)\n" \
"v_mfma_f32_16x16x32_bf16 v[8:11], a[80:83], a[0:3], 0\n" \
"s_waitcnt lgkmcnt(2)\n" \
"ds_read_b128 a[24:27], v72 offset:6144\n" \
"v_mfma_f32_16x16x32_bf16 v[8:11], a[84:87], a[4:7], v[8:11]\n" \
"buffer_load_dwordx4 v65, s[36:39], s81 offen offset:1024 lds\n" \
"v_mfma_f32_16x16x32_bf16 v[12:15], a[88:91], a[0:3], 0\n" \
"ds_read_b128 a[28:31], v73 offset:6144\n" \
"v_mfma_f32_16x16x32_bf16 v[12:15], a[92:95], a[4:7], v[12:15]\n" \
"v_mfma_f32_16x16x32_bf16 v[28:31], a[88:91], a[8:11], 0\n" \
"ds_read_b128 a[32:35], v72 offset:8192\n" \
"v_mfma_f32_16x16x32_bf16 v[28:31], a[92:95], a[12:15], v[28:31]\n" \
"buffer_load_dwordx4 v66, s[36:39], s81 offen offset:2048 lds\n" \
"v_mfma_f32_16x16x32_bf16 v[24:27], a[80:83], a[8:11], 0\n" \
"ds_read_b128 a[36:39], v73 offset:8192\n" \
"v_mfma_f32_16x16x32_bf16 v[24:27], a[84:87], a[12:15], v[24:27]\n" \
"v_mfma_f32_16x16x32_bf16 v[20:23], a[72:75], a[8:11], 0\n" \
"ds_read_b128 a[40:43], v72 offset:10240\n" \
"v_mfma_f32_16x16x32_bf16 v[20:23], a[76:79], a[12:15], v[20:23]\n" \
"buffer_load_dwordx4 v67, s[36:39], s81 offen offset:3072 lds\n" \
"v_mfma_f32_16x16x32_bf16 v[16:19], a[64:67], a[8:11], 0\n" \
"ds_read_b128 a[44:47], v73 offset:10240\n" \
"v_mfma_f32_16x16x32_bf16 v[16:19], a[68:71], a[12:15], v[16:19]\n" \
"s_waitcnt lgkmcnt(2)\n" \
"s_xor_b32 s81, s81, 0x180\n" \
"v_mfma_f32_16x16x32_bf16 v[32:35], a[64:67], a[16:19], 0\n" \
"s_add_u32 m0, s74, 69632\n" \
"ds_read_b128 a[48:51], v72 offset:12288\n" \
"v_mfma_f32_16x16x32_bf16 v[32:35], a[68:71], a[20:23], v[32:35]\n" \
"buffer_load_dwordx4 v64, s[36:39], s82 offen offset:0 lds\n" \
"v_mfma_f32_16x16x32_bf16 v[36:39], a[72:75], a[16:19], 0\n" \
"ds_read_b128 a[52:55], v73 offset:12288\n" \
"v_mfma_f32_16x16x32_bf16 v[36:39], a[76:79], a[20:23], v[36:39]\n" \
"v_mfma_f32_16x16x32_bf16 v[40:43], a[80:83], a[16:19], 0\n" \
"ds_read_b128 a[56:59], v72 offset:14336\n" \
"v_mfma_f32_16x16x32_bf16 v[40:43], a[84:87], a[20:23], v[40:43]\n" \
"buffer_load_dwordx4 v65, s[36:39], s82 offen offset:1024 lds\n" \
"v_mfma_f32_16x16x32_bf16 v[44:47], a[88:91], a[16:19], 0\n" \
"ds_read_b128 a[60:63], v73 offset:14336\n" \
"v_mfma_f32_16x16x32_bf16 v[44:47], a[92:95], a[20:23], v[44:47]\n" \
"v_mfma_f32_16x16x32_bf16 v[60:63], a[88:91], a[24:27], 0\n" \
"ds_read_b64_tr_b16 a[96:97], v74 offset:33792\n" \
"v_mfma_f32_16x16x32_bf16 v[60:63], a[92:95], a[28:31], v[60:63]\n" \
"buffer_load_dwordx4 v66, s[36:39], s82 offen offset:2048 lds\n" \
"v_mfma_f32_16x16x32_bf16 v[56:59], a[80:83], a[24:27], 0\n" \
"ds_read_b64_tr_b16 a[98:99], v74 offset:37888\n" \
"v_mfma_f32_16x16x32_bf16 v[56:59], a[84:87], a[28:31], v[56:59]\n" \
"v_mfma_f32_16x16x32_bf16 v[52:55], a[72:75], a[24:27], 0\n" \
"ds_read_b64_tr_b16 a[100:101], v74 offset:50176\n" \
"v_mfma_f32_16x16x32_bf16 v[52:55], a[76:79], a[28:31], v[52:55]\n" \
"buffer_load_dwordx4 v67, s[36:39], s82 offen offset:3072 lds\n" \
"v_mfma_f32_16x16x32_bf16 v[48:51], a[64:67], a[24:27], 0\n" \
"ds_read_b64_tr_b16 a[102:103], v74 offset:54272\n" \
"v_mfma_f32_16x16x32_bf16 v[48:51], a[68:71], a[28:31], v[48:51]\n" \
"v_mfma_f32_16x16x32_bf16 v[144:147], a[64:67], a[32:35], 0\n" \
"s_xor_b32 s82, s82, 0x180\n" \
"ds_read_b64_tr_b16 a[104:105], v75 offset:33792\n" \
"v_mfma_f32_16x16x32_bf16 v[144:147], a[68:71], a[36:39], v[144:147]\n" \
"v_mfma_f32_16x16x32_bf16 v[148:151], a[72:75], a[32:35], 0\n" \
"ds_read_b64_tr_b16 a[106:107], v75 offset:37888\n" \
"v_mfma_f32_16x16x32_bf16 v[148:151], a[76:79], a[36:39], v[148:151]\n" \
"v_mfma_f32_16x16x32_bf16 v[152:155], a[80:83], a[32:35], 0\n" \
"ds_read_b64_tr_b16 a[108:109], v75 offset:50176\n" \
"v_mfma_f32_16x16x32_bf16 v[152:155], a[84:87], a[36:39], v[152:155]\n" \
"v_mfma_f32_16x16x32_bf16 v[156:159], a[88:91], a[32:35], 0\n" \
"ds_read_b64_tr_b16 a[110:111], v75 offset:54272\n" \
"v_mfma_f32_16x16x32_bf16 v[156:159], a[92:95], a[36:39], v[156:159]\n" \
"s_waitcnt lgkmcnt(2)\n" \
"v_mfma_f32_16x16x32_bf16 v[188:191], a[88:91], a[40:43], 0\n" \
"ds_read_b64_tr_b16 a[112:113], v74 offset:34304\n" \
"v_add_u32 v96, 0x10000, v72\n" \
"v_mfma_f32_16x16x32_bf16 v[188:191], a[92:95], a[44:47], v[188:191]\n" \
"v_mfma_f32_16x16x32_bf16 v[184:187], a[80:83], a[40:43], 0\n" \
"ds_read_b64_tr_b16 a[114:115], v74 offset:38400\n" \
"v_add_u32 v97, 0x10000, v73\n" \
"v_mfma_f32_16x16x32_bf16 v[184:187], a[84:87], a[44:47], v[184:187]\n" \
"v_mfma_f32_16x16x32_bf16 v[180:183], a[72:75], a[40:43], 0\n" \
"ds_read_b64_tr_b16 a[116:117], v74 offset:50688\n" \
"v_add_u32 v98, 0x10000, v74\n" \
"v_mfma_f32_16x16x32_bf16 v[180:183], a[76:79], a[44:47], v[180:183]\n" \
"v_mfma_f32_16x16x32_bf16 v[176:179], a[64:67], a[40:43], 0\n" \
"ds_read_b64_tr_b16 a[118:119], v74 offset:54784\n" \
"v_add_u32 v99, 0x10000, v75\n" \
"v_mfma_f32_16x16x32_bf16 v[176:179], a[68:71], a[44:47], v[176:179]\n" \
"v_mfma_f32_16x16x32_bf16 v[192:195], a[64:67], a[48:51], 0\n" \
"s_add_u32 m0, s74, 98304\n" \
"ds_read_b64_tr_b16 a[120:121], v75 offset:34304\n" \
"v_add_u32 v100, 0x20000, v72\n" \
"v_mfma_f32_16x16x32_bf16 v[192:195], a[68:71], a[52:55], v[192:195]\n" \
"buffer_load_dwordx4 v68, s[40:43], s83 offen offset:0 lds\n" \
"v_mfma_f32_16x16x32_bf16 v[196:199], a[72:75], a[48:51], 0\n" \
"ds_read_b64_tr_b16 a[122:123], v75 offset:38400\n" \
"v_add_u32 v101, 0x20000, v73\n" \
"v_mfma_f32_16x16x32_bf16 v[196:199], a[76:79], a[52:55], v[196:199]\n" \
"v_mfma_f32_16x16x32_bf16 v[200:203], a[80:83], a[48:51], 0\n" \
"ds_read_b64_tr_b16 a[124:125], v75 offset:50688\n" \
"v_add_u32 v102, 0x20000, v74\n" \
"v_mfma_f32_16x16x32_bf16 v[200:203], a[84:87], a[52:55], v[200:203]\n" \
"buffer_load_dwordx4 v69, s[44:47], s83 offen offset:1024 lds\n" \
"v_mfma_f32_16x16x32_bf16 v[204:207], a[88:91], a[48:51], 0\n" \
"ds_read_b64_tr_b16 a[126:127], v75 offset:54784\n" \
"v_add_u32 v103, 0x20000, v75\n" \
"v_mfma_f32_16x16x32_bf16 v[204:207], a[92:95], a[52:55], v[204:207]\n" \
"v_mfma_f32_16x16x32_bf16 v[220:223], a[88:91], a[56:59], 0\n" \
"v_mfma_f32_16x16x32_bf16 v[220:223], a[92:95], a[60:63], v[220:223]\n" \
"buffer_load_dwordx4 v70, s[40:43], s83 offen offset:2048 lds\n" \
"v_mfma_f32_16x16x32_bf16 v[216:219], a[80:83], a[56:59], 0\n" \
"v_mfma_f32_16x16x32_bf16 v[216:219], a[84:87], a[60:63], v[216:219]\n" \
"v_mfma_f32_16x16x32_bf16 v[212:215], a[72:75], a[56:59], 0\n" \
"v_mfma_f32_16x16x32_bf16 v[212:215], a[76:79], a[60:63], v[212:215]\n" \
"buffer_load_dwordx4 v71, s[44:47], s83 offen offset:3072 lds\n" \
"v_mfma_f32_16x16x32_bf16 v[208:211], a[64:67], a[56:59], 0\n" \
"v_mfma_f32_16x16x32_bf16 v[208:211], a[68:71], a[60:63], v[208:211]\n" \
"s_xor_b32 s83, s83, 0x80000\n" \
"s_waitcnt lgkmcnt(0)\n" \
"s_barrier\n" \
"v_mfma_f32_16x16x32_bf16 a[128:131], a[0:3], a[96:99], 0\n" \
"s_add_u32 m0, s74, 102400\n" \
"v_mfma_f32_16x16x32_bf16 a[128:131], a[4:7], a[100:103], a[128:131]\n" \
"buffer_load_dwordx4 v68, s[40:43], s84 offen offset:0 lds\n" \
"v_mfma_f32_16x16x32_bf16 a[132:135], a[0:3], a[104:107], 0\n" \
"v_mfma_f32_16x16x32_bf16 a[132:135], a[4:7], a[108:111], a[132:135]\n" \
"v_mfma_f32_16x16x32_bf16 a[136:139], a[0:3], a[112:115], 0\n" \
"v_mfma_f32_16x16x32_bf16 a[136:139], a[4:7], a[116:119], a[136:139]\n" \
"buffer_load_dwordx4 v69, s[44:47], s84 offen offset:1024 lds\n" \
"v_mfma_f32_16x16x32_bf16 a[140:143], a[0:3], a[120:123], 0\n" \
"v_mfma_f32_16x16x32_bf16 a[140:143], a[4:7], a[124:127], a[140:143]\n" \
"v_mfma_f32_16x16x32_bf16 a[156:159], a[8:11], a[120:123], 0\n" \
"v_mfma_f32_16x16x32_bf16 a[156:159], a[12:15], a[124:127], a[156:159]\n" \
"buffer_load_dwordx4 v70, s[40:43], s84 offen offset:2048 lds\n" \
"v_mfma_f32_16x16x32_bf16 a[152:155], a[8:11], a[112:115], 0\n" \
"v_mfma_f32_16x16x32_bf16 a[152:155], a[12:15], a[116:119], a[152:155]\n" \
"v_mfma_f32_16x16x32_bf16 a[148:151], a[8:11], a[104:107], 0\n" \
"v_mfma_f32_16x16x32_bf16 a[148:151], a[12:15], a[108:111], a[148:151]\n" \
"buffer_load_dwordx4 v71, s[44:47], s84 offen offset:3072 lds\n" \
"v_mfma_f32_16x16x32_bf16 a[144:147], a[8:11], a[96:99], 0\n" \
"v_mfma_f32_16x16x32_bf16 a[144:147], a[12:15], a[100:103], a[144:147]\n" \
"v_mfma_f32_16x16x32_bf16 a[160:163], a[16:19], a[96:99], 0\n" \
"s_xor_b32 s84, s84, 0x80000\n" \
"v_mfma_f32_16x16x32_bf16 a[160:163], a[20:23], a[100:103], a[160:163]\n" \
"v_mfma_f32_16x16x32_bf16 a[164:167], a[16:19], a[104:107], 0\n" \
"v_mfma_f32_16x16x32_bf16 a[164:167], a[20:23], a[108:111], a[164:167]\n" \
"v_mfma_f32_16x16x32_bf16 a[168:171], a[16:19], a[112:115], 0\n" \
"s_add_u32 m0, s74, 32768\n" \
"v_mfma_f32_16x16x32_bf16 a[168:171], a[20:23], a[116:119], a[168:171]\n" \
"buffer_load_dwordx4 v64, s[36:39], s81 offen offset:0 lds\n" \
"v_mfma_f32_16x16x32_bf16 a[172:175], a[16:19], a[120:123], 0\n" \
"v_mfma_f32_16x16x32_bf16 a[172:175], a[20:23], a[124:127], a[172:175]\n" \
"v_mfma_f32_16x16x32_bf16 a[188:191], a[24:27], a[120:123], 0\n" \
"v_mfma_f32_16x16x32_bf16 a[188:191], a[28:31], a[124:127], a[188:191]\n" \
"buffer_load_dwordx4 v65, s[36:39], s81 offen offset:1024 lds\n" \
"v_mfma_f32_16x16x32_bf16 a[184:187], a[24:27], a[112:115], 0\n" \
"v_mfma_f32_16x16x32_bf16 a[184:187], a[28:31], a[116:119], a[184:187]\n" \
"v_mfma_f32_16x16x32_bf16 a[180:183], a[24:27], a[104:107], 0\n" \
"v_mfma_f32_16x16x32_bf16 a[180:183], a[28:31], a[108:111], a[180:183]\n" \
"buffer_load_dwordx4 v66, s[36:39], s81 offen offset:2048 lds\n" \
"v_mfma_f32_16x16x32_bf16 a[176:179], a[24:27], a[96:99], 0\n" \
"v_mfma_f32_16x16x32_bf16 a[176:179], a[28:31], a[100:103], a[176:179]\n" \
"v_mfma_f32_16x16x32_bf16 a[192:195], a[32:35], a[96:99], 0\n" \
"v_mfma_f32_16x16x32_bf16 a[192:195], a[36:39], a[100:103], a[192:195]\n" \
"buffer_load_dwordx4 v67, s[36:39], s81 offen offset:3072 lds\n" \
"v_mfma_f32_16x16x32_bf16 a[196:199], a[32:35], a[104:107], 0\n" \
"v_mfma_f32_16x16x32_bf16 a[196:199], a[36:39], a[108:111], a[196:199]\n" \
"s_xor_b32 s81, s81, 0x80\n" \
"v_mfma_f32_16x16x32_bf16 a[200:203], a[32:35], a[112:115], 0\n" \
"s_add_u32 m0, s74, 36864\n" \
"v_mfma_f32_16x16x32_bf16 a[200:203], a[36:39], a[116:119], a[200:203]\n" \
"buffer_load_dwordx4 v64, s[36:39], s82 offen offset:0 lds\n" \
"v_mfma_f32_16x16x32_bf16 a[204:207], a[32:35], a[120:123], 0\n" \
"v_mfma_f32_16x16x32_bf16 a[204:207], a[36:39], a[124:127], a[204:207]\n" \
"s_waitcnt vmcnt(13) lgkmcnt(0)\n" \
"s_barrier\n" \
"v_mfma_f32_16x16x32_bf16 a[220:223], a[40:43], a[120:123], 0\n" \
"ds_read_b64_tr_b16 a[64:65], v102 offset:0\n" \
"ds_read_b64_tr_b16 a[66:67], v102 offset:4096\n" \
"v_mfma_f32_16x16x32_bf16 a[220:223], a[44:47], a[124:127], a[220:223]\n" \
"buffer_load_dwordx4 v65, s[36:39], s82 offen offset:1024 lds\n" \
"v_mfma_f32_16x16x32_bf16 a[216:219], a[40:43], a[112:115], 0\n" \
"ds_read_b64_tr_b16 a[68:69], v102 offset:16384\n" \
"ds_read_b64_tr_b16 a[70:71], v102 offset:20480\n" \
"v_mfma_f32_16x16x32_bf16 a[216:219], a[44:47], a[116:119], a[216:219]\n" \
"ds_read_b128 a[0:3], v96 offset:0\n" \
"ds_read_b128 a[4:7], v97 offset:0\n" \
"v_mfma_f32_16x16x32_bf16 a[212:215], a[40:43], a[104:107], 0\n" \
"ds_read_b64_tr_b16 a[72:73], v103 offset:0\n" \
"ds_read_b64_tr_b16 a[74:75], v103 offset:4096\n" \
"v_mfma_f32_16x16x32_bf16 a[212:215], a[44:47], a[108:111], a[212:215]\n" \
"buffer_load_dwordx4 v66, s[36:39], s82 offen offset:2048 lds\n" \
"v_mfma_f32_16x16x32_bf16 a[208:211], a[40:43], a[96:99], 0\n" \
"ds_read_b64_tr_b16 a[76:77], v103 offset:16384\n" \
"ds_read_b64_tr_b16 a[78:79], v103 offset:20480\n" \
"v_mfma_f32_16x16x32_bf16 a[208:211], a[44:47], a[100:103], a[208:211]\n" \
"ds_read_b64_tr_b16 a[80:81], v102 offset:512\n" \
"ds_read_b64_tr_b16 a[82:83], v102 offset:4608\n" \
"v_mfma_f32_16x16x32_bf16 a[224:227], a[48:51], a[96:99], 0\n" \
"ds_read_b64_tr_b16 a[84:85], v102 offset:16896\n" \
"ds_read_b64_tr_b16 a[86:87], v102 offset:20992\n" \
"v_mfma_f32_16x16x32_bf16 a[224:227], a[52:55], a[100:103], a[224:227]\n" \
"buffer_load_dwordx4 v67, s[36:39], s82 offen offset:3072 lds\n" \
"v_mfma_f32_16x16x32_bf16 a[228:231], a[48:51], a[104:107], 0\n" \
"s_mov_b64 s[64:65], s[14:15]\n" \
"s_add_u32 s68, s67, 0x200000\n" \
"ds_read_b64_tr_b16 a[88:89], v103 offset:512\n" \
"ds_read_b64_tr_b16 a[90:91], v103 offset:4608\n" \
"v_mfma_f32_16x16x32_bf16 a[228:231], a[52:55], a[108:111], a[228:231]\n" \
"s_add_u32 s64, s64, s68\n" \
"s_addc_u32 s65, s65, 0\n" \
"ds_read_b64_tr_b16 a[92:93], v103 offset:16896\n" \
"ds_read_b64_tr_b16 a[94:95], v103 offset:20992\n" \
"v_mfma_f32_16x16x32_bf16 a[232:235], a[48:51], a[112:115], 0\n" \
"s_xor_b32 s82, s82, 0x80\n" \
"ds_read_b128 a[8:11], v96 offset:2048\n" \
"ds_read_b128 a[12:15], v97 offset:2048\n" \
"v_mfma_f32_16x16x32_bf16 a[232:235], a[52:55], a[116:119], a[232:235]\n" \
"global_load_dword v238, v234, s[64:65]\n" \
"v_mfma_f32_16x16x32_bf16 a[236:239], a[48:51], a[120:123], 0\n" \
"s_mov_b64 s[64:65], s[16:17]\n" \
"s_add_u32 s68, s67, 0x200000\n" \
"ds_read_b128 a[16:19], v96 offset:4096\n" \
"ds_read_b128 a[20:23], v97 offset:4096\n" \
"v_mfma_f32_16x16x32_bf16 a[236:239], a[52:55], a[124:127], a[236:239]\n" \
"s_add_u32 s64, s64, s68\n" \
"s_addc_u32 s65, s65, 0\n" \
"ds_read_b128 a[24:27], v96 offset:6144\n" \
"ds_read_b128 a[28:31], v97 offset:6144\n" \
"v_mfma_f32_16x16x32_bf16 a[252:255], a[56:59], a[120:123], 0\n" \
"ds_read_b128 a[32:35], v96 offset:8192\n" \
"ds_read_b128 a[36:39], v97 offset:8192\n" \
"v_mfma_f32_16x16x32_bf16 a[252:255], a[60:63], a[124:127], a[252:255]\n" \
"global_load_dword v238, v234, s[64:65]\n" \
"v_mfma_f32_16x16x32_bf16 a[248:251], a[56:59], a[112:115], 0\n" \
"ds_read_b128 a[40:43], v96 offset:10240\n" \
"ds_read_b128 a[44:47], v97 offset:10240\n" \
"v_mfma_f32_16x16x32_bf16 a[248:251], a[60:63], a[116:119], a[248:251]\n" \
"ds_read_b128 a[48:51], v96 offset:12288\n" \
"ds_read_b128 a[52:55], v97 offset:12288\n" \
"v_mfma_f32_16x16x32_bf16 a[244:247], a[56:59], a[104:107], 0\n" \
"v_mfma_f32_16x16x32_bf16 a[244:247], a[60:63], a[108:111], a[244:247]\n" \
"v_mfma_f32_16x16x32_bf16 a[240:243], a[56:59], a[96:99], 0\n" \
"v_mfma_f32_16x16x32_bf16 a[240:243], a[60:63], a[100:103], a[240:243]\n" \
"s_waitcnt lgkmcnt(2)\n" \
"v_mfma_f32_16x16x32_bf16 v[0:3], a[64:67], a[0:3], v[0:3]\n" \
"s_lshl_b32 s79, s61, 12\n" \
"ds_read_b128 a[56:59], v96 offset:14336\n" \
"v_mfma_f32_16x16x32_bf16 v[0:3], a[68:71], a[4:7], v[0:3]\n" \
"s_add_u32 s79, s79, %3\n" \
"v_add_u32 v228, 4096, v84\n" \
"v_add_u32 v229, 3200, v84\n" \
"v_mfma_f32_16x16x32_bf16 v[4:7], a[72:75], a[0:3], v[4:7]\n" \
"s_add_u32 m0, s74, 0\n" \
"ds_read_b128 a[60:63], v97 offset:14336\n" \
"v_mfma_f32_16x16x32_bf16 v[4:7], a[76:79], a[4:7], v[4:7]\n" \
"buffer_load_dwordx4 v68, s[40:43], s83 offen offset:0 lds\n" \
"v_mfma_f32_16x16x32_bf16 v[8:11], a[80:83], a[0:3], v[8:11]\n" \
"s_lshl_b32 s78, s62, 8\n" \
"ds_read_b64_tr_b16 a[96:97], v102 offset:1024\n" \
"v_mfma_f32_16x16x32_bf16 v[8:11], a[84:87], a[4:7], v[8:11]\n" \
"s_add_u32 s89, s78, 30720\n" \
"v_mfma_f32_16x16x32_bf16 v[12:15], a[88:91], a[0:3], v[12:15]\n" \
"s_mov_b64 s[48:49], s[8:9]\n" \
"ds_read_b64_tr_b16 a[98:99], v102 offset:5120\n" \
"v_mfma_f32_16x16x32_bf16 v[12:15], a[92:95], a[4:7], v[12:15]\n" \
"buffer_load_dwordx4 v69, s[44:47], s83 offen offset:1024 lds\n" \
"v_mfma_f32_16x16x32_bf16 v[28:31], a[88:91], a[8:11], v[28:31]\n" \
"s_and_b32 s49, s49, 0xffff\n" \
"ds_read_b64_tr_b16 a[100:101], v102 offset:17408\n" \
"v_mfma_f32_16x16x32_bf16 v[28:31], a[92:95], a[12:15], v[28:31]\n" \
"s_mov_b32 s50, 4194304\n" \
"v_mfma_f32_16x16x32_bf16 v[24:27], a[80:83], a[8:11], v[24:27]\n" \
"s_mov_b32 s51, 0x20000\n" \
"ds_read_b64_tr_b16 a[102:103], v102 offset:21504\n" \
"v_mfma_f32_16x16x32_bf16 v[24:27], a[84:87], a[12:15], v[24:27]\n" \
"buffer_load_dwordx4 v70, s[40:43], s83 offen offset:2048 lds\n" \
"v_mfma_f32_16x16x32_bf16 v[20:23], a[72:75], a[8:11], v[20:23]\n" \
"s_mov_b64 s[52:53], s[12:13]\n" \
"ds_read_b64_tr_b16 a[104:105], v103 offset:1024\n" \
"v_mfma_f32_16x16x32_bf16 v[20:23], a[76:79], a[12:15], v[20:23]\n" \
"s_sub_u32 s52, s52, 4096\n" \
"s_subb_u32 s53, s53, 0\n" \
"v_mfma_f32_16x16x32_bf16 v[16:19], a[64:67], a[8:11], v[16:19]\n" \
"s_and_b32 s53, s53, 0xffff\n" \
"ds_read_b64_tr_b16 a[106:107], v103 offset:5120\n" \
"v_mfma_f32_16x16x32_bf16 v[16:19], a[68:71], a[12:15], v[16:19]\n" \
"buffer_load_dwordx4 v71, s[44:47], s83 offen offset:3072 lds\n" \
"v_mfma_f32_16x16x32_bf16 v[32:35], a[64:67], a[16:19], v[32:35]\n" \
"s_mov_b32 s54, 8392704\n" \
"s_mov_b32 s55, 0x20000\n" \
"ds_read_b64_tr_b16 a[108:109], v103 offset:17408\n" \
"v_mfma_f32_16x16x32_bf16 v[32:35], a[68:71], a[20:23], v[32:35]\n" \
"s_xor_b32 s83, s83, 0x380000\n" \
"v_lshl_add_u32 v224, v85, 11, v86\n" \
"v_add_u32 v225, 15360, v224\n" \
"v_mfma_f32_16x16x32_bf16 v[36:39], a[72:75], a[16:19], v[36:39]\n" \
"s_add_u32 m0, s74, 4096\n" \
"ds_read_b64_tr_b16 a[110:111], v103 offset:21504\n" \
"v_add_u32 v226, 129024, v224\n" \
"v_mfma_f32_16x16x32_bf16 v[36:39], a[76:79], a[20:23], v[36:39]\n" \
"buffer_load_dwordx4 v68, s[40:43], s84 offen offset:0 lds\n" \
"v_mfma_f32_16x16x32_bf16 v[40:43], a[80:83], a[16:19], v[40:43]\n" \
"ds_read_b64_tr_b16 a[112:113], v102 offset:1536\n" \
"v_add_u32 v227, 144384, v224\n" \
"v_mfma_f32_16x16x32_bf16 v[40:43], a[84:87], a[20:23], v[40:43]\n" \
"s_lshl_b32 s70, s61, 15\n" \
"v_mfma_f32_16x16x32_bf16 v[44:47], a[88:91], a[16:19], v[44:47]\n" \
"s_lshl_b32 s75, s63, 18\n" \
"ds_read_b64_tr_b16 a[114:115], v102 offset:5632\n" \
"v_mfma_f32_16x16x32_bf16 v[44:47], a[92:95], a[20:23], v[44:47]\n" \
"buffer_load_dwordx4 v69, s[44:47], s84 offen offset:1024 lds\n" \
"v_mfma_f32_16x16x32_bf16 v[60:63], a[88:91], a[24:27], v[60:63]\n" \
"s_sub_u32 s77, 3932160, s75\n" \
"ds_read_b64_tr_b16 a[116:117], v102 offset:17920\n" \
"v_mfma_f32_16x16x32_bf16 v[60:63], a[92:95], a[28:31], v[60:63]\n" \
"s_lshr_b32 s71, %1, 6\n" \
"v_mfma_f32_16x16x32_bf16 v[56:59], a[80:83], a[24:27], v[56:59]\n" \
"s_xor_b32 s71, s71, s63\n" \
"ds_read_b64_tr_b16 a[118:119], v102 offset:22016\n" \
"v_mfma_f32_16x16x32_bf16 v[56:59], a[84:87], a[28:31], v[56:59]\n" \
"buffer_load_dwordx4 v70, s[40:43], s84 offen offset:2048 lds\n" \
"v_mfma_f32_16x16x32_bf16 v[52:55], a[72:75], a[24:27], v[52:55]\n" \
"s_and_b32 s71, s71, 0\n" \
"ds_read_b64_tr_b16 a[120:121], v103 offset:1536\n" \
"v_mfma_f32_16x16x32_bf16 v[52:55], a[76:79], a[28:31], v[52:55]\n" \
"s_lshl_b32 s71, s71, 7\n" \
"v_mfma_f32_16x16x32_bf16 v[48:51], a[64:67], a[24:27], v[48:51]\n" \
"s_add_u32 s70, s70, s71\n" \
"ds_read_b64_tr_b16 a[122:123], v103 offset:5632\n" \
"v_mfma_f32_16x16x32_bf16 v[48:51], a[68:71], a[28:31], v[48:51]\n" \
"buffer_load_dwordx4 v71, s[44:47], s84 offen offset:3072 lds\n" \
"v_mfma_f32_16x16x32_bf16 v[144:147], a[64:67], a[32:35], v[144:147]\n" \
"s_add_u32 s75, s75, s70\n" \
"ds_read_b64_tr_b16 a[124:125], v103 offset:17920\n" \
"v_mfma_f32_16x16x32_bf16 v[144:147], a[68:71], a[36:39], v[144:147]\n" \
"s_add_u32 s77, s77, s70\n" \
"v_mfma_f32_16x16x32_bf16 v[148:151], a[72:75], a[32:35], v[148:151]\n" \
"s_xor_b32 s84, s84, 0x380000\n" \
"ds_read_b64_tr_b16 a[126:127], v103 offset:22016\n" \
"v_mfma_f32_16x16x32_bf16 v[148:151], a[76:79], a[36:39], v[148:151]\n" \
"s_lshl_b32 s71, s71, 12\n" \
"v_mfma_f32_16x16x32_bf16 v[152:155], a[80:83], a[32:35], v[152:155]\n" \
"s_add_u32 s78, s78, s71\n" \
"v_mfma_f32_16x16x32_bf16 v[152:155], a[84:87], a[36:39], v[152:155]\n" \
"s_add_u32 s89, s89, s71\n" \
"v_lshlrev_b32 v242, 2, v78\n" \
"v_and_b32 v236, 0xf0, v234\n" \
"v_mfma_f32_16x16x32_bf16 v[156:159], a[88:91], a[32:35], v[156:159]\n" \
"v_lshl_add_u32 v236, v87, 9, v236\n" \
"v_mfma_f32_16x16x32_bf16 v[156:159], a[92:95], a[36:39], v[156:159]\n" \
"s_add_u32 s71, s74, 0x10000\n" \
"v_add_u32 v230, s71, v88\n" \
"v_xor_b32 v231, 64, v230\n" \
"v_mfma_f32_16x16x32_bf16 v[188:191], a[88:91], a[40:43], v[188:191]\n" \
"v_add_u32 v250, 0x10000, v230\n" \
"v_add_u32 v251, 0x10000, v231\n" \
"v_mfma_f32_16x16x32_bf16 v[188:191], a[92:95], a[44:47], v[188:191]\n" \
"v_lshl_add_u32 v82, v90, 12, v89\n" \
"v_add_u32 v232, %3, v82\n" \
"v_mfma_f32_16x16x32_bf16 v[184:187], a[80:83], a[40:43], v[184:187]\n" \
"v_xor_b32 v233, 32, v232\n" \
"v_mfma_f32_16x16x32_bf16 v[184:187], a[84:87], a[44:47], v[184:187]\n" \
"s_mul_i32 s87, s62, 557824\n" \
"v_mfma_f32_16x16x32_bf16 v[180:183], a[72:75], a[40:43], v[180:183]\n" \
"s_lshr_b32 s70, s61, 1\n" \
"v_mfma_f32_16x16x32_bf16 v[180:183], a[76:79], a[44:47], v[180:183]\n" \
"s_mul_i32 s70, s70, 17408\n" \
"v_mfma_f32_16x16x32_bf16 v[176:179], a[64:67], a[40:43], v[176:179]\n" \
"Lpg585_%=:\n" \
"Lst0x0_%=:\n" \
"s_cbranch_execz Lpg2766_%=\n" \
"s_add_u32 s87, s87, s70\n" \
"v_mfma_f32_16x16x32_bf16 v[176:179], a[68:71], a[44:47], v[176:179]\n" \
"s_waitcnt lgkmcnt(0)\n" \
"s_and_b32 s70, s61, 1\n" \
"v_mfma_f32_16x16x32_bf16 v[192:195], a[64:67], a[48:51], v[192:195]\n" \
"s_lshl_b32 s70, s70, 12\n" \
"v_mfma_f32_16x16x32_bf16 v[192:195], a[68:71], a[52:55], v[192:195]\n" \
"s_add_u32 s87, s87, s70\n" \
"v_mfma_f32_16x16x32_bf16 v[196:199], a[72:75], a[48:51], v[196:199]\n" \
"s_mul_i32 s70, s63, 34816\n" \
"v_mfma_f32_16x16x32_bf16 v[196:199], a[76:79], a[52:55], v[196:199]\n" \
"s_add_u32 s94, s87, 522240\n" \
"v_mfma_f32_16x16x32_bf16 v[200:203], a[80:83], a[48:51], v[200:203]\n" \
"s_sub_u32 s94, s94, s70\n" \
"v_mfma_f32_16x16x32_bf16 v[200:203], a[84:87], a[52:55], v[200:203]\n" \
"s_add_u32 s87, s87, s70\n" \
"v_mfma_f32_16x16x32_bf16 v[204:207], a[88:91], a[48:51], v[204:207]\n" \
"s_lshl_b32 s90, s62, 7\n" \
"v_mfma_f32_16x16x32_bf16 v[204:207], a[92:95], a[52:55], v[204:207]\n" \
"s_add_u32 s95, s90, 65597440\n" \
"v_mfma_f32_16x16x32_bf16 v[220:223], a[88:91], a[56:59], v[220:223]\n" \
"s_add_u32 s90, s90, 65585152\n" \
"v_mfma_f32_16x16x32_bf16 v[220:223], a[92:95], a[60:63], v[220:223]\n" \
"s_lshl_b32 s92, 0xff, s63\n" \
"v_mfma_f32_16x16x32_bf16 v[216:219], a[80:83], a[56:59], v[216:219]\n" \
"s_and_b32 s92, s92, 0xff\n" \
"v_mfma_f32_16x16x32_bf16 v[216:219], a[84:87], a[60:63], v[216:219]\n" \
"s_mov_b32 s76, 0x3e0293ee\n" \
"v_mfma_f32_16x16x32_bf16 v[212:215], a[72:75], a[56:59], v[212:215]\n" \
"s_mov_b32 s31, 0x3e800000\n" \
"v_mfma_f32_16x16x32_bf16 v[212:215], a[76:79], a[60:63], v[212:215]\n" \
"v_mfma_f32_16x16x32_bf16 v[208:211], a[64:67], a[56:59], v[208:211]\n" \
"v_mfma_f32_16x16x32_bf16 v[208:211], a[68:71], a[60:63], v[208:211]\n" \
"s_waitcnt vmcnt(10) lgkmcnt(0)\n" \
"s_barrier\n" \
"v_readfirstlane_b32 s60, v240\n" \
"s_add_u32 s60, s60, 0x9e3779b9\n" \
"v_mfma_f32_16x16x32_bf16 a[128:131], a[0:3], a[96:99], a[128:131]\n" \
"s_add_u32 m0, s74, 131072\n" \
"ds_read_b64_tr_b16 a[64:65], v98 offset:32768\n" \
"v_mfma_f32_16x16x32_bf16 a[128:131], a[4:7], a[100:103], a[128:131]\n" \
"buffer_load_dwordx4 v64, s[36:39], s81 offen offset:0 lds\n" \
"v_mfma_f32_16x16x32_bf16 a[132:135], a[0:3], a[104:107], a[132:135]\n" \
"ds_read_b64_tr_b16 a[66:67], v98 offset:36864\n" \
"v_mfma_f32_16x16x32_bf16 a[132:135], a[4:7], a[108:111], a[132:135]\n" \
"v_mfma_f32_16x16x32_bf16 a[136:139], a[0:3], a[112:115], a[136:139]\n" \
"ds_read_b64_tr_b16 a[68:69], v98 offset:49152\n" \
"v_mfma_f32_16x16x32_bf16 a[136:139], a[4:7], a[116:119], a[136:139]\n" \
"buffer_load_dwordx4 v65, s[36:39], s81 offen offset:1024 lds\n" \
"v_mfma_f32_16x16x32_bf16 a[140:143], a[0:3], a[120:123], a[140:143]\n" \
"ds_read_b64_tr_b16 a[70:71], v98 offset:53248\n" \
"v_mfma_f32_16x16x32_bf16 a[140:143], a[4:7], a[124:127], a[140:143]\n" \
"v_mfma_f32_16x16x32_bf16 a[156:159], a[8:11], a[120:123], a[156:159]\n" \
"ds_read_b128 a[0:3], v72 offset:32768\n" \
"v_mfma_f32_16x16x32_bf16 a[156:159], a[12:15], a[124:127], a[156:159]\n" \
"buffer_load_dwordx4 v66, s[36:39], s81 offen offset:2048 lds\n" \
"v_mfma_f32_16x16x32_bf16 a[152:155], a[8:11], a[112:115], a[152:155]\n" \
"ds_read_b64_tr_b16 a[72:73], v99 offset:32768\n" \
"v_mfma_f32_16x16x32_bf16 a[152:155], a[12:15], a[116:119], a[152:155]\n" \
"v_mfma_f32_16x16x32_bf16 a[148:151], a[8:11], a[104:107], a[148:151]\n" \
"ds_read_b128 a[4:7], v73 offset:32768\n" \
"v_mfma_f32_16x16x32_bf16 a[148:151], a[12:15], a[108:111], a[148:151]\n" \
"buffer_load_dwordx4 v67, s[36:39], s81 offen offset:3072 lds\n" \
"v_mfma_f32_16x16x32_bf16 a[144:147], a[8:11], a[96:99], a[144:147]\n" \
"ds_read_b64_tr_b16 a[74:75], v99 offset:36864\n" \
"v_mfma_f32_16x16x32_bf16 a[144:147], a[12:15], a[100:103], a[144:147]\n" \
"s_xor_b32 s81, s81, 0x380\n" \
"v_mfma_f32_16x16x32_bf16 a[160:163], a[16:19], a[96:99], a[160:163]\n" \
"s_add_u32 m0, s74, 135168\n" \
"ds_read_b128 a[8:11], v72 offset:34816\n" \
"v_mfma_f32_16x16x32_bf16 a[160:163], a[20:23], a[100:103], a[160:163]\n" \
"buffer_load_dwordx4 v64, s[36:39], s82 offen offset:0 lds\n" \
"v_mfma_f32_16x16x32_bf16 a[164:167], a[16:19], a[104:107], a[164:167]\n" \
"ds_read_b64_tr_b16 a[76:77], v99 offset:49152\n" \
"v_mfma_f32_16x16x32_bf16 a[164:167], a[20:23], a[108:111], a[164:167]\n" \
"v_mfma_f32_16x16x32_bf16 a[168:171], a[16:19], a[112:115], a[168:171]\n" \
"ds_read_b128 a[12:15], v73 offset:34816\n" \
"v_mfma_f32_16x16x32_bf16 a[168:171], a[20:23], a[116:119], a[168:171]\n" \
"buffer_load_dwordx4 v65, s[36:39], s82 offen offset:1024 lds\n" \
"v_mfma_f32_16x16x32_bf16 a[172:175], a[16:19], a[120:123], a[172:175]\n" \
"ds_read_b64_tr_b16 a[78:79], v99 offset:53248\n" \
"v_mfma_f32_16x16x32_bf16 a[172:175], a[20:23], a[124:127], a[172:175]\n" \
"v_mfma_f32_16x16x32_bf16 a[188:191], a[24:27], a[120:123], a[188:191]\n" \
"ds_read_b128 a[16:19], v72 offset:36864\n" \
"v_mfma_f32_16x16x32_bf16 a[188:191], a[28:31], a[124:127], a[188:191]\n" \
"buffer_load_dwordx4 v66, s[36:39], s82 offen offset:2048 lds\n" \
"v_mfma_f32_16x16x32_bf16 a[184:187], a[24:27], a[112:115], a[184:187]\n" \
"ds_read_b64_tr_b16 a[80:81], v98 offset:33280\n" \
"v_mfma_f32_16x16x32_bf16 a[184:187], a[28:31], a[116:119], a[184:187]\n" \
"v_mfma_f32_16x16x32_bf16 a[180:183], a[24:27], a[104:107], a[180:183]\n" \
"ds_read_b128 a[20:23], v73 offset:36864\n" \
"v_mfma_f32_16x16x32_bf16 a[180:183], a[28:31], a[108:111], a[180:183]\n" \
"buffer_load_dwordx4 v67, s[36:39], s82 offen offset:3072 lds\n" \
"v_mfma_f32_16x16x32_bf16 a[176:179], a[24:27], a[96:99], a[176:179]\n" \
"s_mov_b64 s[64:65], s[14:15]\n" \
"s_add_u32 s68, s67, 0x300000\n" \
"ds_read_b64_tr_b16 a[82:83], v98 offset:37376\n" \
"v_mfma_f32_16x16x32_bf16 a[176:179], a[28:31], a[100:103], a[176:179]\n" \
"s_add_u32 s64, s64, s68\n" \
"s_addc_u32 s65, s65, 0\n" \
"v_mfma_f32_16x16x32_bf16 a[192:195], a[32:35], a[96:99], a[192:195]\n" \
"s_xor_b32 s82, s82, 0x380\n" \
"ds_read_b128 a[24:27], v72 offset:38912\n" \
"v_mfma_f32_16x16x32_bf16 a[192:195], a[36:39], a[100:103], a[192:195]\n" \
"global_load_dword v238, v234, s[64:65]\n" \
"v_mfma_f32_16x16x32_bf16 a[196:199], a[32:35], a[104:107], a[196:199]\n" \
"s_mov_b64 s[64:65], s[16:17]\n" \
"s_add_u32 s68, s67, 0x300000\n" \
"ds_read_b64_tr_b16 a[84:85], v98 offset:49664\n" \
"v_mfma_f32_16x16x32_bf16 a[196:199], a[36:39], a[108:111], a[196:199]\n" \
"s_add_u32 s64, s64, s68\n" \
"s_addc_u32 s65, s65, 0\n" \
"v_mfma_f32_16x16x32_bf16 a[200:203], a[32:35], a[112:115], a[200:203]\n" \
"ds_read_b128 a[28:31], v73 offset:38912\n" \
"v_mfma_f32_16x16x32_bf16 a[200:203], a[36:39], a[116:119], a[200:203]\n" \
"global_load_dword v238, v234, s[64:65]\n" \
"v_mfma_f32_16x16x32_bf16 a[204:207], a[32:35], a[120:123], a[204:207]\n" \
"ds_read_b64_tr_b16 a[86:87], v98 offset:53760\n" \
"v_mfma_f32_16x16x32_bf16 a[204:207], a[36:39], a[124:127], a[204:207]\n" \
"v_mfma_f32_16x16x32_bf16 a[220:223], a[40:43], a[120:123], a[220:223]\n" \
"ds_read_b128 a[32:35], v72 offset:40960\n" \
"v_mfma_f32_16x16x32_bf16 a[220:223], a[44:47], a[124:127], a[220:223]\n" \
"v_mfma_f32_16x16x32_bf16 a[216:219], a[40:43], a[112:115], a[216:219]\n" \
"ds_read_b64_tr_b16 a[88:89], v99 offset:33280\n" \
"v_mfma_f32_16x16x32_bf16 a[216:219], a[44:47], a[116:119], a[216:219]\n" \
"v_mfma_f32_16x16x32_bf16 a[212:215], a[40:43], a[104:107], a[212:215]\n" \
"ds_read_b128 a[36:39], v73 offset:40960\n" \
"v_mfma_f32_16x16x32_bf16 a[212:215], a[44:47], a[108:111], a[212:215]\n" \
"v_mfma_f32_16x16x32_bf16 a[208:211], a[40:43], a[96:99], a[208:211]\n" \
"ds_read_b64_tr_b16 a[90:91], v99 offset:37376\n" \
"v_mfma_f32_16x16x32_bf16 a[208:211], a[44:47], a[100:103], a[208:211]\n" \
"v_mfma_f32_16x16x32_bf16 a[224:227], a[48:51], a[96:99], a[224:227]\n" \
"ds_read_b128 a[40:43], v72 offset:43008\n" \
"v_mfma_f32_16x16x32_bf16 a[224:227], a[52:55], a[100:103], a[224:227]\n" \
"v_mfma_f32_16x16x32_bf16 a[228:231], a[48:51], a[104:107], a[228:231]\n" \
"ds_read_b64_tr_b16 a[92:93], v99 offset:49664\n" \
"v_mfma_f32_16x16x32_bf16 a[228:231], a[52:55], a[108:111], a[228:231]\n" \
"v_mfma_f32_16x16x32_bf16 a[232:235], a[48:51], a[112:115], a[232:235]\n" \
"ds_read_b128 a[44:47], v73 offset:43008\n" \
"v_mfma_f32_16x16x32_bf16 a[232:235], a[52:55], a[116:119], a[232:235]\n" \
"v_mfma_f32_16x16x32_bf16 a[236:239], a[48:51], a[120:123], a[236:239]\n" \
"ds_read_b64_tr_b16 a[94:95], v99 offset:53760\n" \
"v_mfma_f32_16x16x32_bf16 a[236:239], a[52:55], a[124:127], a[236:239]\n" \
"v_mfma_f32_16x16x32_bf16 a[252:255], a[56:59], a[120:123], a[252:255]\n" \
"ds_read_b128 a[48:51], v72 offset:45056\n" \
"v_mfma_f32_16x16x32_bf16 a[252:255], a[60:63], a[124:127], a[252:255]\n" \
"v_mfma_f32_16x16x32_bf16 a[248:251], a[56:59], a[112:115], a[248:251]\n" \
"v_mfma_f32_16x16x32_bf16 a[248:251], a[60:63], a[116:119], a[248:251]\n" \
"v_mfma_f32_16x16x32_bf16 a[244:247], a[56:59], a[104:107], a[244:247]\n" \
"ds_read_b128 a[52:55], v73 offset:45056\n" \
"v_mfma_f32_16x16x32_bf16 a[244:247], a[60:63], a[108:111], a[244:247]\n" \
"v_mfma_f32_16x16x32_bf16 a[240:243], a[56:59], a[96:99], a[240:243]\n" \
"v_mfma_f32_16x16x32_bf16 a[240:243], a[60:63], a[100:103], a[240:243]\n" \
"s_waitcnt lgkmcnt(1)\n" \
"v_mfma_f32_16x16x32_bf16 v[0:3], a[64:67], a[0:3], v[0:3]\n" \
"ds_read_b128 a[56:59], v72 offset:47104\n" \
"v_lshrrev_b32 v104, 4, v234\n" \
"v_mfma_f32_16x16x32_bf16 v[0:3], a[68:71], a[4:7], v[0:3]\n" \
"v_bfe_u32 v105, v104, 4, 1\n" \
"v_bfe_u32 v106, v104, 2, 2\n" \
"v_mfma_f32_16x16x32_bf16 v[4:7], a[72:75], a[0:3], v[4:7]\n" \
"s_add_u32 m0, s74, 65536\n" \
"ds_read_b128 a[60:63], v73 offset:47104\n" \
"v_lshl_add_u32 v106, v105, 3, v106\n" \
"v_mfma_f32_16x16x32_bf16 v[4:7], a[76:79], a[4:7], v[4:7]\n" \
"buffer_load_dwordx4 v68, s[40:43], s83 offen offset:0 lds\n" \
"v_mfma_f32_16x16x32_bf16 v[8:11], a[80:83], a[0:3], v[8:11]\n" \
"s_lshl_b32 s71, s61, 4\n" \
"ds_read_b64_tr_b16 a[96:97], v98 offset:33792\n" \
"v_add_u32 v106, s71, v106\n" \
"v_mfma_f32_16x16x32_bf16 v[8:11], a[84:87], a[4:7], v[8:11]\n" \
"v_mul_u32_u24 v106, 6144, v106\n" \
"v_lshlrev_b32 v105, 1, v105\n" \
"v_mfma_f32_16x16x32_bf16 v[12:15], a[88:91], a[0:3], v[12:15]\n" \
"ds_read_b64_tr_b16 a[98:99], v98 offset:37888\n" \
"v_and_b32 v107, 3, v104\n" \
"v_mfma_f32_16x16x32_bf16 v[12:15], a[92:95], a[4:7], v[12:15]\n" \
"buffer_load_dwordx4 v69, s[44:47], s83 offen offset:1024 lds\n" \
"v_mfma_f32_16x16x32_bf16 v[28:31], a[88:91], a[8:11], v[28:31]\n" \
"ds_read_b64_tr_b16 a[100:101], v98 offset:50176\n" \
"v_xor_b32 v105, v105, v107\n" \
"v_mfma_f32_16x16x32_bf16 v[28:31], a[92:95], a[12:15], v[28:31]\n" \
"v_lshl_add_u32 v106, v105, 4, v106\n" \
"v_lshrrev_b32 v105, 5, v104\n" \
"v_mfma_f32_16x16x32_bf16 v[24:27], a[80:83], a[8:11], v[24:27]\n" \
"ds_read_b64_tr_b16 a[102:103], v98 offset:54272\n" \
"v_lshl_add_u32 v244, v105, 6, v106\n" \
"v_mfma_f32_16x16x32_bf16 v[24:27], a[84:87], a[12:15], v[24:27]\n" \
"buffer_load_dwordx4 v70, s[40:43], s83 offen offset:2048 lds\n" \
"v_mfma_f32_16x16x32_bf16 v[20:23], a[72:75], a[8:11], v[20:23]\n" \
"ds_read_b64_tr_b16 a[104:105], v99 offset:33792\n" \
"v_lshrrev_b32 v107, 3, v104\n" \
"v_mfma_f32_16x16x32_bf16 v[20:23], a[76:79], a[12:15], v[20:23]\n" \
"v_and_b32 v108, 7, v104\n" \
"v_xor_b32 v109, v108, v107\n" \
"v_mfma_f32_16x16x32_bf16 v[16:19], a[64:67], a[8:11], v[16:19]\n" \
"ds_read_b64_tr_b16 a[106:107], v99 offset:37888\n" \
"v_lshlrev_b32 v109, 4, v109\n" \
"v_mfma_f32_16x16x32_bf16 v[16:19], a[68:71], a[12:15], v[16:19]\n" \
"buffer_load_dwordx4 v71, s[44:47], s83 offen offset:3072 lds\n" \
"v_mfma_f32_16x16x32_bf16 v[32:35], a[64:67], a[16:19], v[32:35]\n" \
"ds_read_b64_tr_b16 a[108:109], v99 offset:50176\n" \
"v_lshl_add_u32 v245, v107, 12, v109\n" \
"v_mfma_f32_16x16x32_bf16 v[32:35], a[68:71], a[20:23], v[32:35]\n" \
"s_xor_b32 s83, s83, 0x80000\n" \
"v_mfma_f32_16x16x32_bf16 v[36:39], a[72:75], a[16:19], v[36:39]\n" \
"s_add_u32 m0, s74, 69632\n" \
"ds_read_b64_tr_b16 a[110:111], v99 offset:54272\n" \
"v_mfma_f32_16x16x32_bf16 v[36:39], a[76:79], a[20:23], v[36:39]\n" \
"buffer_load_dwordx4 v68, s[40:43], s84 offen offset:0 lds\n" \
"v_mfma_f32_16x16x32_bf16 v[40:43], a[80:83], a[16:19], v[40:43]\n" \
"s_and_b32 s70, s61, 1\n" \
"ds_read_b64_tr_b16 a[112:113], v98 offset:34304\n" \
"v_mfma_f32_16x16x32_bf16 v[40:43], a[84:87], a[20:23], v[40:43]\n" \
"s_lshl_b32 s71, s70, 2\n" \
"v_add_u32 v107, s71, v107\n" \
"v_xor_b32 v109, v108, v107\n" \
"v_mfma_f32_16x16x32_bf16 v[44:47], a[88:91], a[16:19], v[44:47]\n" \
"ds_read_b64_tr_b16 a[114:115], v98 offset:38400\n" \
"v_lshlrev_b32 v109, 4, v109\n" \
"v_mfma_f32_16x16x32_bf16 v[44:47], a[92:95], a[20:23], v[44:47]\n" \
"buffer_load_dwordx4 v69, s[44:47], s84 offen offset:1024 lds\n" \
"v_mfma_f32_16x16x32_bf16 v[60:63], a[88:91], a[24:27], v[60:63]\n" \
"ds_read_b64_tr_b16 a[116:117], v98 offset:50688\n" \
"v_lshl_add_u32 v246, v107, 12, v109\n" \
"v_mfma_f32_16x16x32_bf16 v[60:63], a[92:95], a[28:31], v[60:63]\n" \
"s_lshr_b32 s71, s61, 1\n" \
"v_mfma_f32_16x16x32_bf16 v[56:59], a[80:83], a[24:27], v[56:59]\n" \
"s_lshl_b32 s70, s71, 11\n" \
"ds_read_b64_tr_b16 a[118:119], v98 offset:54784\n" \
"v_lshl_add_u32 v246, v105, 30, v246\n" \
"v_mfma_f32_16x16x32_bf16 v[56:59], a[84:87], a[28:31], v[56:59]\n" \
"buffer_load_dwordx4 v70, s[40:43], s84 offen offset:2048 lds\n" \
"v_mfma_f32_16x16x32_bf16 v[52:55], a[72:75], a[24:27], v[52:55]\n" \
"ds_read_b64_tr_b16 a[120:121], v99 offset:34304\n" \
"v_add_u32 v246, s70, v246\n" \
"v_mfma_f32_16x16x32_bf16 v[52:55], a[76:79], a[28:31], v[52:55]\n" \
"v_lshrrev_b32 v107, 4, v104\n" \
"v_xor_b32 v107, v107, v108\n" \
"v_mfma_f32_16x16x32_bf16 v[48:51], a[64:67], a[24:27], v[48:51]\n" \
"ds_read_b64_tr_b16 a[122:123], v99 offset:38400\n" \
"v_and_b32 v109, 15, v104\n" \
"v_mfma_f32_16x16x32_bf16 v[48:51], a[68:71], a[28:31], v[48:51]\n" \
"buffer_load_dwordx4 v71, s[44:47], s84 offen offset:3072 lds\n" \
"v_mfma_f32_16x16x32_bf16 v[144:147], a[64:67], a[32:35], v[144:147]\n" \
"ds_read_b64_tr_b16 a[124:125], v99 offset:50688\n" \
"v_lshlrev_b32 v106, 7, v109\n" \
"v_mfma_f32_16x16x32_bf16 v[144:147], a[68:71], a[36:39], v[144:147]\n" \
"v_lshl_add_u32 v106, v107, 4, v106\n" \
"v_mfma_f32_16x16x32_bf16 v[148:151], a[72:75], a[32:35], v[148:151]\n" \
"s_xor_b32 s84, s84, 0x80000\n" \
"ds_read_b64_tr_b16 a[126:127], v99 offset:54784\n" \
"v_mfma_f32_16x16x32_bf16 v[148:151], a[76:79], a[36:39], v[148:151]\n" \
"s_add_u32 s70, s70, %3\n" \
"v_add_u32 v247, s70, v106\n" \
"v_lshrrev_b32 v109, 2, v109\n" \
"v_mfma_f32_16x16x32_bf16 v[152:155], a[80:83], a[32:35], v[152:155]\n" \
"v_lshl_add_u32 v109, v109, 9, v106\n" \
"v_mfma_f32_16x16x32_bf16 v[152:155], a[84:87], a[36:39], v[152:155]\n" \
"s_add_u32 s70, %3, 16384\n" \
"v_add_u32 v248, s70, v109\n" \
"v_and_b32 v106, 3, v104\n" \
"v_mfma_f32_16x16x32_bf16 v[156:159], a[88:91], a[32:35], v[156:159]\n" \
"v_lshlrev_b32 v106, 3, v106\n" \
"v_bfe_u32 v107, v104, 2, 2\n" \
"v_mfma_f32_16x16x32_bf16 v[156:159], a[92:95], a[36:39], v[156:159]\n" \
"v_lshl_add_u32 v106, v107, 6, v106\n" \
"v_bfe_u32 v107, v104, 4, 1\n" \
"v_mfma_f32_16x16x32_bf16 v[188:191], a[88:91], a[40:43], v[188:191]\n" \
"v_mul_u32_u24 v107, 288, v107\n" \
"v_mul_u32_u24 v108, 6144, v105\n" \
"v_mfma_f32_16x16x32_bf16 v[188:191], a[92:95], a[44:47], v[188:191]\n" \
"v_add3_u32 v106, v106, v107, v108\n" \
"v_mfma_f32_16x16x32_bf16 v[184:187], a[80:83], a[40:43], v[184:187]\n" \
"s_and_b32 s70, s61, 1\n" \
"v_mfma_f32_16x16x32_bf16 v[184:187], a[84:87], a[44:47], v[184:187]\n" \
"s_lshl_b32 s70, s70, 9\n" \
"v_mfma_f32_16x16x32_bf16 v[180:183], a[72:75], a[40:43], v[180:183]\n" \
"s_add_u32 s70, s70, %3\n" \
"v_add_u32 v249, s70, v106\n" \
"v_mfma_f32_16x16x32_bf16 v[180:183], a[76:79], a[44:47], v[180:183]\n" \
"s_lshl_b32 s70, s71, 5\n" \
"v_xor_b32 v249, s70, v249\n" \
"v_mfma_f32_16x16x32_bf16 v[176:179], a[64:67], a[40:43], v[176:179]\n" \
"v_mfma_f32_16x16x32_bf16 v[176:179], a[68:71], a[44:47], v[176:179]\n" \
"s_waitcnt lgkmcnt(0)\n" \
"v_mfma_f32_16x16x32_bf16 v[192:195], a[64:67], a[48:51], v[192:195]\n" \
"v_mfma_f32_16x16x32_bf16 v[192:195], a[68:71], a[52:55], v[192:195]\n" \
"v_mfma_f32_16x16x32_bf16 v[196:199], a[72:75], a[48:51], v[196:199]\n" \
"v_mfma_f32_16x16x32_bf16 v[196:199], a[76:79], a[52:55], v[196:199]\n" \
"v_mfma_f32_16x16x32_bf16 v[200:203], a[80:83], a[48:51], v[200:203]\n" \
"v_mfma_f32_16x16x32_bf16 v[200:203], a[84:87], a[52:55], v[200:203]\n" \
"v_mfma_f32_16x16x32_bf16 v[204:207], a[88:91], a[48:51], v[204:207]\n" \
"v_mfma_f32_16x16x32_bf16 v[204:207], a[92:95], a[52:55], v[204:207]\n" \
"v_mfma_f32_16x16x32_bf16 v[220:223], a[88:91], a[56:59], v[220:223]\n" \
"v_mfma_f32_16x16x32_bf16 v[220:223], a[92:95], a[60:63], v[220:223]\n" \
"v_mfma_f32_16x16x32_bf16 v[216:219], a[80:83], a[56:59], v[216:219]\n" \
"v_mfma_f32_16x16x32_bf16 v[216:219], a[84:87], a[60:63], v[216:219]\n" \
"v_mfma_f32_16x16x32_bf16 v[212:215], a[72:75], a[56:59], v[212:215]\n" \
"v_mfma_f32_16x16x32_bf16 v[212:215], a[76:79], a[60:63], v[212:215]\n" \
"v_mfma_f32_16x16x32_bf16 v[208:211], a[64:67], a[56:59], v[208:211]\n" \
"v_mfma_f32_16x16x32_bf16 v[208:211], a[68:71], a[60:63], v[208:211]\n" \
"s_waitcnt vmcnt(10) lgkmcnt(0)\n" \
"s_barrier\n" \
"v_mfma_f32_16x16x32_bf16 a[128:131], a[0:3], a[96:99], a[128:131]\n" \
"s_add_u32 m0, s74, 98304\n" \
"ds_read_b64_tr_b16 a[64:65], v74 offset:0\n" \
"v_mfma_f32_16x16x32_bf16 a[128:131], a[4:7], a[100:103], a[128:131]\n" \
"buffer_load_dwordx4 v64, s[36:39], s81 offen offset:0 lds\n" \
"v_mfma_f32_16x16x32_bf16 a[132:135], a[0:3], a[104:107], a[132:135]\n" \
"ds_read_b64_tr_b16 a[66:67], v74 offset:4096\n" \
"v_mfma_f32_16x16x32_bf16 a[132:135], a[4:7], a[108:111], a[132:135]\n" \
"v_mfma_f32_16x16x32_bf16 a[136:139], a[0:3], a[112:115], a[136:139]\n" \
"ds_read_b64_tr_b16 a[68:69], v74 offset:16384\n" \
"v_mfma_f32_16x16x32_bf16 a[136:139], a[4:7], a[116:119], a[136:139]\n" \
"buffer_load_dwordx4 v65, s[36:39], s81 offen offset:1024 lds\n" \
"v_mfma_f32_16x16x32_bf16 a[140:143], a[0:3], a[120:123], a[140:143]\n" \
"ds_read_b64_tr_b16 a[70:71], v74 offset:20480\n" \
"v_mfma_f32_16x16x32_bf16 a[140:143], a[4:7], a[124:127], a[140:143]\n" \
"v_mfma_f32_16x16x32_bf16 a[156:159], a[8:11], a[120:123], a[156:159]\n" \
"ds_read_b128 a[0:3], v100 offset:0\n" \
"v_mfma_f32_16x16x32_bf16 a[156:159], a[12:15], a[124:127], a[156:159]\n" \
"buffer_load_dwordx4 v66, s[36:39], s81 offen offset:2048 lds\n" \
"v_mfma_f32_16x16x32_bf16 a[152:155], a[8:11], a[112:115], a[152:155]\n" \
"ds_read_b64_tr_b16 a[72:73], v75 offset:0\n" \
"v_mfma_f32_16x16x32_bf16 a[152:155], a[12:15], a[116:119], a[152:155]\n" \
"v_mfma_f32_16x16x32_bf16 a[148:151], a[8:11], a[104:107], a[148:151]\n" \
"ds_read_b128 a[4:7], v101 offset:0\n" \
"v_mfma_f32_16x16x32_bf16 a[148:151], a[12:15], a[108:111], a[148:151]\n" \
"buffer_load_dwordx4 v67, s[36:39], s81 offen offset:3072 lds\n" \
"v_mfma_f32_16x16x32_bf16 a[144:147], a[8:11], a[96:99], a[144:147]\n" \
"ds_read_b64_tr_b16 a[74:75], v75 offset:4096\n" \
"v_mfma_f32_16x16x32_bf16 a[144:147], a[12:15], a[100:103], a[144:147]\n" \
"s_xor_b32 s81, s81, 0x80\n" \
"v_mfma_f32_16x16x32_bf16 a[160:163], a[16:19], a[96:99], a[160:163]\n" \
"s_add_u32 m0, s74, 102400\n" \
"ds_read_b128 a[8:11], v100 offset:2048\n" \
"v_mfma_f32_16x16x32_bf16 a[160:163], a[20:23], a[100:103], a[160:163]\n" \
"buffer_load_dwordx4 v64, s[36:39], s82 offen offset:0 lds\n" \
"v_mfma_f32_16x16x32_bf16 a[164:167], a[16:19], a[104:107], a[164:167]\n" \
"ds_read_b64_tr_b16 a[76:77], v75 offset:16384\n" \
"v_mfma_f32_16x16x32_bf16 a[164:167], a[20:23], a[108:111], a[164:167]\n" \
"v_mfma_f32_16x16x32_bf16 a[168:171], a[16:19], a[112:115], a[168:171]\n" \
"ds_read_b128 a[12:15], v101 offset:2048\n" \
"v_mfma_f32_16x16x32_bf16 a[168:171], a[20:23], a[116:119], a[168:171]\n" \
"buffer_load_dwordx4 v65, s[36:39], s82 offen offset:1024 lds\n" \
"v_mfma_f32_16x16x32_bf16 a[172:175], a[16:19], a[120:123], a[172:175]\n" \
"ds_read_b64_tr_b16 a[78:79], v75 offset:20480\n" \
"v_mfma_f32_16x16x32_bf16 a[172:175], a[20:23], a[124:127], a[172:175]\n" \
"v_mfma_f32_16x16x32_bf16 a[188:191], a[24:27], a[120:123], a[188:191]\n" \
"ds_read_b128 a[16:19], v100 offset:4096\n" \
"v_mfma_f32_16x16x32_bf16 a[188:191], a[28:31], a[124:127], a[188:191]\n" \
"buffer_load_dwordx4 v66, s[36:39], s82 offen offset:2048 lds\n" \
"v_mfma_f32_16x16x32_bf16 a[184:187], a[24:27], a[112:115], a[184:187]\n" \
"ds_read_b64_tr_b16 a[80:81], v74 offset:512\n" \
"v_mfma_f32_16x16x32_bf16 a[184:187], a[28:31], a[116:119], a[184:187]\n" \
"v_mfma_f32_16x16x32_bf16 a[180:183], a[24:27], a[104:107], a[180:183]\n" \
"ds_read_b128 a[20:23], v101 offset:4096\n" \
"v_mfma_f32_16x16x32_bf16 a[180:183], a[28:31], a[108:111], a[180:183]\n" \
"buffer_load_dwordx4 v67, s[36:39], s82 offen offset:3072 lds\n" \
"v_mfma_f32_16x16x32_bf16 a[176:179], a[24:27], a[96:99], a[176:179]\n" \
"s_mov_b64 s[64:65], s[8:9]\n" \
"s_add_u32 s68, s66, 0x0\n" \
"ds_read_b64_tr_b16 a[82:83], v74 offset:4608\n" \
"v_mfma_f32_16x16x32_bf16 a[176:179], a[28:31], a[100:103], a[176:179]\n" \
"s_add_u32 s64, s64, s68\n" \
"s_addc_u32 s65, s65, 0\n" \
"v_mfma_f32_16x16x32_bf16 a[192:195], a[32:35], a[96:99], a[192:195]\n" \
"s_xor_b32 s82, s82, 0x80\n" \
"ds_read_b128 a[24:27], v100 offset:6144\n" \
"v_mfma_f32_16x16x32_bf16 a[192:195], a[36:39], a[100:103], a[192:195]\n" \
"global_load_dword v238, v234, s[64:65]\n" \
"v_mfma_f32_16x16x32_bf16 a[196:199], a[32:35], a[104:107], a[196:199]\n" \
"s_mov_b64 s[64:65], s[8:9]\n" \
"s_add_u32 s68, s66, 0x100000\n" \
"ds_read_b64_tr_b16 a[84:85], v74 offset:16896\n" \
"v_mfma_f32_16x16x32_bf16 a[196:199], a[36:39], a[108:111], a[196:199]\n" \
"s_add_u32 s64, s64, s68\n" \
"s_addc_u32 s65, s65, 0\n" \
"v_mfma_f32_16x16x32_bf16 a[200:203], a[32:35], a[112:115], a[200:203]\n" \
"ds_read_b128 a[28:31], v101 offset:6144\n" \
"v_mfma_f32_16x16x32_bf16 a[200:203], a[36:39], a[116:119], a[200:203]\n" \
"global_load_dword v238, v234, s[64:65]\n" \
"v_mfma_f32_16x16x32_bf16 a[204:207], a[32:35], a[120:123], a[204:207]\n" \
"ds_read_b64_tr_b16 a[86:87], v74 offset:20992\n" \
"v_mfma_f32_16x16x32_bf16 a[204:207], a[36:39], a[124:127], a[204:207]\n" \
"v_mfma_f32_16x16x32_bf16 a[220:223], a[40:43], a[120:123], a[220:223]\n" \
"ds_read_b128 a[32:35], v100 offset:8192\n" \
"v_mfma_f32_16x16x32_bf16 a[220:223], a[44:47], a[124:127], a[220:223]\n" \
"v_mfma_f32_16x16x32_bf16 a[216:219], a[40:43], a[112:115], a[216:219]\n" \
"ds_read_b64_tr_b16 a[88:89], v75 offset:512\n" \
"v_mfma_f32_16x16x32_bf16 a[216:219], a[44:47], a[116:119], a[216:219]\n" \
"v_mfma_f32_16x16x32_bf16 a[212:215], a[40:43], a[104:107], a[212:215]\n" \
"ds_read_b128 a[36:39], v101 offset:8192\n" \
"v_mfma_f32_16x16x32_bf16 a[212:215], a[44:47], a[108:111], a[212:215]\n" \
"v_mfma_f32_16x16x32_bf16 a[208:211], a[40:43], a[96:99], a[208:211]\n" \
"ds_read_b64_tr_b16 a[90:91], v75 offset:4608\n" \
"v_mfma_f32_16x16x32_bf16 a[208:211], a[44:47], a[100:103], a[208:211]\n" \
"v_mfma_f32_16x16x32_bf16 a[224:227], a[48:51], a[96:99], a[224:227]\n" \
"ds_read_b128 a[40:43], v100 offset:10240\n" \
"v_mfma_f32_16x16x32_bf16 a[224:227], a[52:55], a[100:103], a[224:227]\n" \
"v_mfma_f32_16x16x32_bf16 a[228:231], a[48:51], a[104:107], a[228:231]\n" \
"ds_read_b64_tr_b16 a[92:93], v75 offset:16896\n" \
"v_mfma_f32_16x16x32_bf16 a[228:231], a[52:55], a[108:111], a[228:231]\n" \
"v_mfma_f32_16x16x32_bf16 a[232:235], a[48:51], a[112:115], a[232:235]\n" \
"ds_read_b128 a[44:47], v101 offset:10240\n" \
"v_mfma_f32_16x16x32_bf16 a[232:235], a[52:55], a[116:119], a[232:235]\n" \
"v_mfma_f32_16x16x32_bf16 a[236:239], a[48:51], a[120:123], a[236:239]\n" \
"ds_read_b64_tr_b16 a[94:95], v75 offset:20992\n" \
"v_mfma_f32_16x16x32_bf16 a[236:239], a[52:55], a[124:127], a[236:239]\n" \
"v_mfma_f32_16x16x32_bf16 a[252:255], a[56:59], a[120:123], a[252:255]\n" \
"ds_read_b128 a[48:51], v100 offset:12288\n" \
"v_mfma_f32_16x16x32_bf16 a[252:255], a[60:63], a[124:127], a[252:255]\n" \
"v_mfma_f32_16x16x32_bf16 a[248:251], a[56:59], a[112:115], a[248:251]\n" \
"v_mfma_f32_16x16x32_bf16 a[248:251], a[60:63], a[116:119], a[248:251]\n" \
"v_mfma_f32_16x16x32_bf16 a[244:247], a[56:59], a[104:107], a[244:247]\n" \
"ds_read_b128 a[52:55], v101 offset:12288\n" \
"v_mfma_f32_16x16x32_bf16 a[244:247], a[60:63], a[108:111], a[244:247]\n" \
"v_mfma_f32_16x16x32_bf16 a[240:243], a[56:59], a[96:99], a[240:243]\n" \
"v_mfma_f32_16x16x32_bf16 a[240:243], a[60:63], a[100:103], a[240:243]\n" \
"s_waitcnt lgkmcnt(1)\n" \
"v_mfma_f32_16x16x32_bf16 v[0:3], a[64:67], a[0:3], v[0:3]\n" \
"ds_read_b128 a[56:59], v100 offset:14336\n" \
"v_mfma_f32_16x16x32_bf16 v[0:3], a[68:71], a[4:7], v[0:3]\n" \
"v_mfma_f32_16x16x32_bf16 v[4:7], a[72:75], a[0:3], v[4:7]\n" \
"s_add_u32 m0, s74, 32768\n" \
"ds_read_b128 a[60:63], v101 offset:14336\n" \
"v_mfma_f32_16x16x32_bf16 v[4:7], a[76:79], a[4:7], v[4:7]\n" \
"buffer_load_dwordx4 v68, s[40:43], s83 offen offset:0 lds\n" \
"v_mfma_f32_16x16x32_bf16 v[8:11], a[80:83], a[0:3], v[8:11]\n" \
"ds_read_b64_tr_b16 a[96:97], v74 offset:1024\n" \
"v_mfma_f32_16x16x32_bf16 v[8:11], a[84:87], a[4:7], v[8:11]\n" \
"v_mfma_f32_16x16x32_bf16 v[12:15], a[88:91], a[0:3], v[12:15]\n" \
"ds_read_b64_tr_b16 a[98:99], v74 offset:5120\n" \
"v_mfma_f32_16x16x32_bf16 v[12:15], a[92:95], a[4:7], v[12:15]\n" \
"buffer_load_dwordx4 v69, s[44:47], s83 offen offset:1024 lds\n" \
"v_mfma_f32_16x16x32_bf16 v[28:31], a[88:91], a[8:11], v[28:31]\n" \
"ds_read_b64_tr_b16 a[100:101], v74 offset:17408\n" \
"v_mfma_f32_16x16x32_bf16 v[28:31], a[92:95], a[12:15], v[28:31]\n" \
"v_mfma_f32_16x16x32_bf16 v[24:27], a[80:83], a[8:11], v[24:27]\n" \
"ds_read_b64_tr_b16 a[102:103], v74 offset:21504\n" \
"v_mfma_f32_16x16x32_bf16 v[24:27], a[84:87], a[12:15], v[24:27]\n" \
"buffer_load_dwordx4 v70, s[40:43], s83 offen offset:2048 lds\n" \
"v_mfma_f32_16x16x32_bf16 v[20:23], a[72:75], a[8:11], v[20:23]\n" \
"ds_read_b64_tr_b16 a[104:105], v75 offset:1024\n" \
"v_mfma_f32_16x16x32_bf16 v[20:23], a[76:79], a[12:15], v[20:23]\n" \
"v_mfma_f32_16x16x32_bf16 v[16:19], a[64:67], a[8:11], v[16:19]\n" \
"ds_read_b64_tr_b16 a[106:107], v75 offset:5120\n" \
"v_mfma_f32_16x16x32_bf16 v[16:19], a[68:71], a[12:15], v[16:19]\n" \
"buffer_load_dwordx4 v71, s[44:47], s83 offen offset:3072 lds\n" \
"v_mfma_f32_16x16x32_bf16 v[32:35], a[64:67], a[16:19], v[32:35]\n" \
"ds_read_b64_tr_b16 a[108:109], v75 offset:17408\n" \
"v_mfma_f32_16x16x32_bf16 v[32:35], a[68:71], a[20:23], v[32:35]\n" \
"s_xor_b32 s83, s83, 0x180000\n" \
"v_mfma_f32_16x16x32_bf16 v[36:39], a[72:75], a[16:19], v[36:39]\n" \
"s_add_u32 m0, s74, 36864\n" \
"ds_read_b64_tr_b16 a[110:111], v75 offset:21504\n" \
"v_mfma_f32_16x16x32_bf16 v[36:39], a[76:79], a[20:23], v[36:39]\n" \
"buffer_load_dwordx4 v68, s[40:43], s84 offen offset:0 lds\n" \
"v_mfma_f32_16x16x32_bf16 v[40:43], a[80:83], a[16:19], v[40:43]\n" \
"ds_read_b64_tr_b16 a[112:113], v74 offset:1536\n" \
"v_mfma_f32_16x16x32_bf16 v[40:43], a[84:87], a[20:23], v[40:43]\n" \
"v_mfma_f32_16x16x32_bf16 v[44:47], a[88:91], a[16:19], v[44:47]\n" \
"ds_read_b64_tr_b16 a[114:115], v74 offset:5632\n" \
"v_mfma_f32_16x16x32_bf16 v[44:47], a[92:95], a[20:23], v[44:47]\n" \
"buffer_load_dwordx4 v69, s[44:47], s84 offen offset:1024 lds\n" \
"v_mfma_f32_16x16x32_bf16 v[60:63], a[88:91], a[24:27], v[60:63]\n" \
"ds_read_b64_tr_b16 a[116:117], v74 offset:17920\n" \
"v_mfma_f32_16x16x32_bf16 v[60:63], a[92:95], a[28:31], v[60:63]\n" \
"v_mfma_f32_16x16x32_bf16 v[56:59], a[80:83], a[24:27], v[56:59]\n" \
"ds_read_b64_tr_b16 a[118:119], v74 offset:22016\n" \
"v_mfma_f32_16x16x32_bf16 v[56:59], a[84:87], a[28:31], v[56:59]\n" \
"buffer_load_dwordx4 v70, s[40:43], s84 offen offset:2048 lds\n" \
"v_mfma_f32_16x16x32_bf16 v[52:55], a[72:75], a[24:27], v[52:55]\n" \
"ds_read_b64_tr_b16 a[120:121], v75 offset:1536\n" \
"v_mfma_f32_16x16x32_bf16 v[52:55], a[76:79], a[28:31], v[52:55]\n" \
"v_mfma_f32_16x16x32_bf16 v[48:51], a[64:67], a[24:27], v[48:51]\n" \
"ds_read_b64_tr_b16 a[122:123], v75 offset:5632\n" \
"v_mfma_f32_16x16x32_bf16 v[48:51], a[68:71], a[28:31], v[48:51]\n" \
"buffer_load_dwordx4 v71, s[44:47], s84 offen offset:3072 lds\n" \
"v_mfma_f32_16x16x32_bf16 v[144:147], a[64:67], a[32:35], v[144:147]\n" \
"ds_read_b64_tr_b16 a[124:125], v75 offset:17920\n" \
"v_mfma_f32_16x16x32_bf16 v[144:147], a[68:71], a[36:39], v[144:147]\n" \
"v_mfma_f32_16x16x32_bf16 v[148:151], a[72:75], a[32:35], v[148:151]\n" \
"s_xor_b32 s84, s84, 0x180000\n" \
"ds_read_b64_tr_b16 a[126:127], v75 offset:22016\n" \
"v_mfma_f32_16x16x32_bf16 v[148:151], a[76:79], a[36:39], v[148:151]\n" \
"v_mfma_f32_16x16x32_bf16 v[152:155], a[80:83], a[32:35], v[152:155]\n" \
"v_mfma_f32_16x16x32_bf16 v[152:155], a[84:87], a[36:39], v[152:155]\n" \
"v_mfma_f32_16x16x32_bf16 v[156:159], a[88:91], a[32:35], v[156:159]\n" \
"v_mfma_f32_16x16x32_bf16 v[156:159], a[92:95], a[36:39], v[156:159]\n" \
"v_mfma_f32_16x16x32_bf16 v[188:191], a[88:91], a[40:43], v[188:191]\n" \
"v_mfma_f32_16x16x32_bf16 v[188:191], a[92:95], a[44:47], v[188:191]\n" \
"v_mfma_f32_16x16x32_bf16 v[184:187], a[80:83], a[40:43], v[184:187]\n" \
"v_mfma_f32_16x16x32_bf16 v[184:187], a[84:87], a[44:47], v[184:187]\n" \
"v_mfma_f32_16x16x32_bf16 v[180:183], a[72:75], a[40:43], v[180:183]\n" \
"v_mfma_f32_16x16x32_bf16 v[180:183], a[76:79], a[44:47], v[180:183]\n" \
"v_mfma_f32_16x16x32_bf16 v[176:179], a[64:67], a[40:43], v[176:179]\n" \
"v_mfma_f32_16x16x32_bf16 v[176:179], a[68:71], a[44:47], v[176:179]\n" \
"s_waitcnt lgkmcnt(0)\n" \
"v_mfma_f32_16x16x32_bf16 v[192:195], a[64:67], a[48:51], v[192:195]\n" \
"v_mfma_f32_16x16x32_bf16 v[192:195], a[68:71], a[52:55], v[192:195]\n" \
"v_mfma_f32_16x16x32_bf16 v[196:199], a[72:75], a[48:51], v[196:199]\n" \
"v_mfma_f32_16x16x32_bf16 v[196:199], a[76:79], a[52:55], v[196:199]\n" \
"v_mfma_f32_16x16x32_bf16 v[200:203], a[80:83], a[48:51], v[200:203]\n" \
"v_mfma_f32_16x16x32_bf16 v[200:203], a[84:87], a[52:55], v[200:203]\n" \
"v_mfma_f32_16x16x32_bf16 v[204:207], a[88:91], a[48:51], v[204:207]\n" \
"v_mfma_f32_16x16x32_bf16 v[204:207], a[92:95], a[52:55], v[204:207]\n" \
"v_mfma_f32_16x16x32_bf16 v[220:223], a[88:91], a[56:59], v[220:223]\n" \
"v_mfma_f32_16x16x32_bf16 v[220:223], a[92:95], a[60:63], v[220:223]\n" \
"v_mfma_f32_16x16x32_bf16 v[216:219], a[80:83], a[56:59], v[216:219]\n" \
"v_mfma_f32_16x16x32_bf16 v[216:219], a[84:87], a[60:63], v[216:219]\n" \
"v_mfma_f32_16x16x32_bf16 v[212:215], a[72:75], a[56:59], v[212:215]\n" \
"v_mfma_f32_16x16x32_bf16 v[212:215], a[76:79], a[60:63], v[212:215]\n" \
"v_mfma_f32_16x16x32_bf16 v[208:211], a[64:67], a[56:59], v[208:211]\n" \
"v_mfma_f32_16x16x32_bf16 v[208:211], a[68:71], a[60:63], v[208:211]\n" \
"s_waitcnt vmcnt(10) lgkmcnt(0)\n" \
"s_barrier\n" \
"v_mfma_f32_16x16x32_bf16 a[128:131], a[0:3], a[96:99], a[128:131]\n" \
"s_add_u32 m0, s74, 0\n" \
"ds_read_b64_tr_b16 a[64:65], v98 offset:0\n" \
"v_mfma_f32_16x16x32_bf16 a[128:131], a[4:7], a[100:103], a[128:131]\n" \
"buffer_load_dwordx4 v64, s[36:39], s81 offen offset:0 lds\n" \
"v_mfma_f32_16x16x32_bf16 a[132:135], a[0:3], a[104:107], a[132:135]\n" \
"ds_read_b64_tr_b16 a[66:67], v98 offset:4096\n" \
"v_mfma_f32_16x16x32_bf16 a[132:135], a[4:7], a[108:111], a[132:135]\n" \
"v_mfma_f32_16x16x32_bf16 a[136:139], a[0:3], a[112:115], a[136:139]\n" \
"ds_read_b64_tr_b16 a[68:69], v98 offset:16384\n" \
"v_mfma_f32_16x16x32_bf16 a[136:139], a[4:7], a[116:119], a[136:139]\n" \
"buffer_load_dwordx4 v65, s[36:39], s81 offen offset:1024 lds\n" \
"v_mfma_f32_16x16x32_bf16 a[140:143], a[0:3], a[120:123], a[140:143]\n" \
"ds_read_b64_tr_b16 a[70:71], v98 offset:20480\n" \
"v_mfma_f32_16x16x32_bf16 a[140:143], a[4:7], a[124:127], a[140:143]\n" \
"v_mfma_f32_16x16x32_bf16 a[156:159], a[8:11], a[120:123], a[156:159]\n" \
"ds_read_b128 a[0:3], v96 offset:32768\n" \
"v_mfma_f32_16x16x32_bf16 a[156:159], a[12:15], a[124:127], a[156:159]\n" \
"buffer_load_dwordx4 v66, s[36:39], s81 offen offset:2048 lds\n" \
"v_mfma_f32_16x16x32_bf16 a[152:155], a[8:11], a[112:115], a[152:155]\n" \
"ds_read_b64_tr_b16 a[72:73], v99 offset:0\n" \
"v_mfma_f32_16x16x32_bf16 a[152:155], a[12:15], a[116:119], a[152:155]\n" \
"v_mfma_f32_16x16x32_bf16 a[148:151], a[8:11], a[104:107], a[148:151]\n" \
"ds_read_b128 a[4:7], v97 offset:32768\n" \
"v_mfma_f32_16x16x32_bf16 a[148:151], a[12:15], a[108:111], a[148:151]\n" \
"buffer_load_dwordx4 v67, s[36:39], s81 offen offset:3072 lds\n" \
"v_mfma_f32_16x16x32_bf16 a[144:147], a[8:11], a[96:99], a[144:147]\n" \
"ds_read_b64_tr_b16 a[74:75], v99 offset:4096\n" \
"Lpg1132_%=:\n" \
"Lst0x1_%=:\n" \
"s_cbranch_execz Lpg3342_%=\n" \
"v_mfma_f32_16x16x32_bf16 a[144:147], a[12:15], a[100:103], a[144:147]\n" \
"s_xor_b32 s81, s81, 0x180\n" \
"v_mfma_f32_16x16x32_bf16 a[160:163], a[16:19], a[96:99], a[160:163]\n" \
"s_add_u32 m0, s74, 4096\n" \
"ds_read_b128 a[8:11], v96 offset:34816\n" \
"v_mfma_f32_16x16x32_bf16 a[160:163], a[20:23], a[100:103], a[160:163]\n" \
"buffer_load_dwordx4 v64, s[36:39], s82 offen offset:0 lds\n" \
"v_mfma_f32_16x16x32_bf16 a[164:167], a[16:19], a[104:107], a[164:167]\n" \
"ds_read_b64_tr_b16 a[76:77], v99 offset:16384\n" \
"v_mfma_f32_16x16x32_bf16 a[164:167], a[20:23], a[108:111], a[164:167]\n" \
"v_mfma_f32_16x16x32_bf16 a[168:171], a[16:19], a[112:115], a[168:171]\n" \
"ds_read_b128 a[12:15], v97 offset:34816\n" \
"v_mfma_f32_16x16x32_bf16 a[168:171], a[20:23], a[116:119], a[168:171]\n" \
"buffer_load_dwordx4 v65, s[36:39], s82 offen offset:1024 lds\n" \
"v_mfma_f32_16x16x32_bf16 a[172:175], a[16:19], a[120:123], a[172:175]\n" \
"ds_read_b64_tr_b16 a[78:79], v99 offset:20480\n" \
"v_mfma_f32_16x16x32_bf16 a[172:175], a[20:23], a[124:127], a[172:175]\n" \
"v_mfma_f32_16x16x32_bf16 a[188:191], a[24:27], a[120:123], a[188:191]\n" \
"ds_read_b128 a[16:19], v96 offset:36864\n" \
"v_mfma_f32_16x16x32_bf16 a[188:191], a[28:31], a[124:127], a[188:191]\n" \
"buffer_load_dwordx4 v66, s[36:39], s82 offen offset:2048 lds\n" \
"v_mfma_f32_16x16x32_bf16 a[184:187], a[24:27], a[112:115], a[184:187]\n" \
"ds_read_b64_tr_b16 a[80:81], v98 offset:512\n" \
"v_mfma_f32_16x16x32_bf16 a[184:187], a[28:31], a[116:119], a[184:187]\n" \
"v_mfma_f32_16x16x32_bf16 a[180:183], a[24:27], a[104:107], a[180:183]\n" \
"ds_read_b128 a[20:23], v97 offset:36864\n" \
"v_mfma_f32_16x16x32_bf16 a[180:183], a[28:31], a[108:111], a[180:183]\n" \
"buffer_load_dwordx4 v67, s[36:39], s82 offen offset:3072 lds\n" \
"v_mfma_f32_16x16x32_bf16 a[176:179], a[24:27], a[96:99], a[176:179]\n" \
"s_mov_b64 s[64:65], s[8:9]\n" \
"s_add_u32 s68, s66, 0x200000\n" \
"ds_read_b64_tr_b16 a[82:83], v98 offset:4608\n" \
"v_mfma_f32_16x16x32_bf16 a[176:179], a[28:31], a[100:103], a[176:179]\n" \
"s_add_u32 s64, s64, s68\n" \
"s_addc_u32 s65, s65, 0\n" \
"v_mfma_f32_16x16x32_bf16 a[192:195], a[32:35], a[96:99], a[192:195]\n" \
"s_xor_b32 s82, s82, 0x180\n" \
"ds_read_b128 a[24:27], v96 offset:38912\n" \
"v_mfma_f32_16x16x32_bf16 a[192:195], a[36:39], a[100:103], a[192:195]\n" \
"global_load_dword v238, v234, s[64:65]\n" \
"v_mfma_f32_16x16x32_bf16 a[196:199], a[32:35], a[104:107], a[196:199]\n" \
"s_mov_b64 s[64:65], s[8:9]\n" \
"s_add_u32 s68, s66, 0x300000\n" \
"ds_read_b64_tr_b16 a[84:85], v98 offset:16896\n" \
"v_mfma_f32_16x16x32_bf16 a[196:199], a[36:39], a[108:111], a[196:199]\n" \
"s_add_u32 s64, s64, s68\n" \
"s_addc_u32 s65, s65, 0\n" \
"v_mfma_f32_16x16x32_bf16 a[200:203], a[32:35], a[112:115], a[200:203]\n" \
"ds_read_b128 a[28:31], v97 offset:38912\n" \
"v_mfma_f32_16x16x32_bf16 a[200:203], a[36:39], a[116:119], a[200:203]\n" \
"global_load_dword v238, v234, s[64:65]\n" \
"v_mfma_f32_16x16x32_bf16 a[204:207], a[32:35], a[120:123], a[204:207]\n" \
"s_mov_b64 s[64:65], s[12:13]\n" \
"s_add_u32 s68, s67, 0x0\n" \
"ds_read_b64_tr_b16 a[86:87], v98 offset:20992\n" \
"v_mfma_f32_16x16x32_bf16 a[204:207], a[36:39], a[124:127], a[204:207]\n" \
"s_add_u32 s64, s64, s68\n" \
"s_addc_u32 s65, s65, 0\n" \
"v_mfma_f32_16x16x32_bf16 a[220:223], a[40:43], a[120:123], a[220:223]\n" \
"ds_read_b128 a[32:35], v96 offset:40960\n" \
"v_mfma_f32_16x16x32_bf16 a[220:223], a[44:47], a[124:127], a[220:223]\n" \
"global_load_dword v238, v234, s[64:65]\n" \
"v_mfma_f32_16x16x32_bf16 a[216:219], a[40:43], a[112:115], a[216:219]\n" \
"ds_read_b64_tr_b16 a[88:89], v99 offset:512\n" \
"v_mfma_f32_16x16x32_bf16 a[216:219], a[44:47], a[116:119], a[216:219]\n" \
"v_mfma_f32_16x16x32_bf16 a[212:215], a[40:43], a[104:107], a[212:215]\n" \
"ds_read_b128 a[36:39], v97 offset:40960\n" \
"v_mfma_f32_16x16x32_bf16 a[212:215], a[44:47], a[108:111], a[212:215]\n" \
"v_mfma_f32_16x16x32_bf16 a[208:211], a[40:43], a[96:99], a[208:211]\n" \
"ds_read_b64_tr_b16 a[90:91], v99 offset:4608\n" \
"v_mfma_f32_16x16x32_bf16 a[208:211], a[44:47], a[100:103], a[208:211]\n" \
"v_mfma_f32_16x16x32_bf16 a[224:227], a[48:51], a[96:99], a[224:227]\n" \
"ds_read_b128 a[40:43], v96 offset:43008\n" \
"v_mfma_f32_16x16x32_bf16 a[224:227], a[52:55], a[100:103], a[224:227]\n" \
"v_mfma_f32_16x16x32_bf16 a[228:231], a[48:51], a[104:107], a[228:231]\n" \
"ds_read_b64_tr_b16 a[92:93], v99 offset:16896\n" \
"v_mfma_f32_16x16x32_bf16 a[228:231], a[52:55], a[108:111], a[228:231]\n" \
"v_mfma_f32_16x16x32_bf16 a[232:235], a[48:51], a[112:115], a[232:235]\n" \
"ds_read_b128 a[44:47], v97 offset:43008\n" \
"v_mfma_f32_16x16x32_bf16 a[232:235], a[52:55], a[116:119], a[232:235]\n" \
"v_mfma_f32_16x16x32_bf16 a[236:239], a[48:51], a[120:123], a[236:239]\n" \
"ds_read_b64_tr_b16 a[94:95], v99 offset:20992\n" \
"v_mfma_f32_16x16x32_bf16 a[236:239], a[52:55], a[124:127], a[236:239]\n" \
"v_mfma_f32_16x16x32_bf16 a[252:255], a[56:59], a[120:123], a[252:255]\n" \
"ds_read_b128 a[48:51], v96 offset:45056\n" \
"v_mfma_f32_16x16x32_bf16 a[252:255], a[60:63], a[124:127], a[252:255]\n" \
"v_mfma_f32_16x16x32_bf16 a[248:251], a[56:59], a[112:115], a[248:251]\n" \
"v_mfma_f32_16x16x32_bf16 a[248:251], a[60:63], a[116:119], a[248:251]\n" \
"v_mfma_f32_16x16x32_bf16 a[244:247], a[56:59], a[104:107], a[244:247]\n" \
"ds_read_b128 a[52:55], v97 offset:45056\n" \
"v_mfma_f32_16x16x32_bf16 a[244:247], a[60:63], a[108:111], a[244:247]\n" \
"v_mfma_f32_16x16x32_bf16 a[240:243], a[56:59], a[96:99], a[240:243]\n" \
"v_mfma_f32_16x16x32_bf16 a[240:243], a[60:63], a[100:103], a[240:243]\n" \
"s_waitcnt lgkmcnt(1)\n" \
"v_mfma_f32_16x16x32_bf16 v[0:3], a[64:67], a[0:3], v[0:3]\n" \
"ds_read_b128 a[56:59], v96 offset:47104\n" \
"v_mfma_f32_16x16x32_bf16 v[0:3], a[68:71], a[4:7], v[0:3]\n" \
"v_mfma_f32_16x16x32_bf16 v[4:7], a[72:75], a[0:3], v[4:7]\n" \
"s_add_u32 m0, s74, 131072\n" \
"ds_read_b128 a[60:63], v97 offset:47104\n" \
"v_mfma_f32_16x16x32_bf16 v[4:7], a[76:79], a[4:7], v[4:7]\n" \
"buffer_load_dwordx4 v68, s[40:43], s83 offen offset:0 lds\n" \
"v_mfma_f32_16x16x32_bf16 v[8:11], a[80:83], a[0:3], v[8:11]\n" \
"ds_read_b64_tr_b16 a[96:97], v98 offset:1024\n" \
"v_mfma_f32_16x16x32_bf16 v[8:11], a[84:87], a[4:7], v[8:11]\n" \
"v_mfma_f32_16x16x32_bf16 v[12:15], a[88:91], a[0:3], v[12:15]\n" \
"ds_read_b64_tr_b16 a[98:99], v98 offset:5120\n" \
"v_mfma_f32_16x16x32_bf16 v[12:15], a[92:95], a[4:7], v[12:15]\n" \
"buffer_load_dwordx4 v69, s[44:47], s83 offen offset:1024 lds\n" \
"v_mfma_f32_16x16x32_bf16 v[28:31], a[88:91], a[8:11], v[28:31]\n" \
"ds_read_b64_tr_b16 a[100:101], v98 offset:17408\n" \
"v_mfma_f32_16x16x32_bf16 v[28:31], a[92:95], a[12:15], v[28:31]\n" \
"v_mfma_f32_16x16x32_bf16 v[24:27], a[80:83], a[8:11], v[24:27]\n" \
"ds_read_b64_tr_b16 a[102:103], v98 offset:21504\n" \
"v_mfma_f32_16x16x32_bf16 v[24:27], a[84:87], a[12:15], v[24:27]\n" \
"buffer_load_dwordx4 v70, s[40:43], s83 offen offset:2048 lds\n" \
"v_mfma_f32_16x16x32_bf16 v[20:23], a[72:75], a[8:11], v[20:23]\n" \
"ds_read_b64_tr_b16 a[104:105], v99 offset:1024\n" \
"v_mfma_f32_16x16x32_bf16 v[20:23], a[76:79], a[12:15], v[20:23]\n" \
"v_mfma_f32_16x16x32_bf16 v[16:19], a[64:67], a[8:11], v[16:19]\n" \
"ds_read_b64_tr_b16 a[106:107], v99 offset:5120\n" \
"v_mfma_f32_16x16x32_bf16 v[16:19], a[68:71], a[12:15], v[16:19]\n" \
"buffer_load_dwordx4 v71, s[44:47], s83 offen offset:3072 lds\n" \
"v_mfma_f32_16x16x32_bf16 v[32:35], a[64:67], a[16:19], v[32:35]\n" \
"ds_read_b64_tr_b16 a[108:109], v99 offset:17408\n" \
"v_mfma_f32_16x16x32_bf16 v[32:35], a[68:71], a[20:23], v[32:35]\n" \
"s_xor_b32 s83, s83, 0x80000\n" \
"v_mfma_f32_16x16x32_bf16 v[36:39], a[72:75], a[16:19], v[36:39]\n" \
"s_add_u32 m0, s74, 135168\n" \
"ds_read_b64_tr_b16 a[110:111], v99 offset:21504\n" \
"v_mfma_f32_16x16x32_bf16 v[36:39], a[76:79], a[20:23], v[36:39]\n" \
"buffer_load_dwordx4 v68, s[40:43], s84 offen offset:0 lds\n" \
"v_mfma_f32_16x16x32_bf16 v[40:43], a[80:83], a[16:19], v[40:43]\n" \
"ds_read_b64_tr_b16 a[112:113], v98 offset:1536\n" \
"v_mfma_f32_16x16x32_bf16 v[40:43], a[84:87], a[20:23], v[40:43]\n" \
"v_mfma_f32_16x16x32_bf16 v[44:47], a[88:91], a[16:19], v[44:47]\n" \
"ds_read_b64_tr_b16 a[114:115], v98 offset:5632\n" \
"v_mfma_f32_16x16x32_bf16 v[44:47], a[92:95], a[20:23], v[44:47]\n" \
"buffer_load_dwordx4 v69, s[44:47], s84 offen offset:1024 lds\n" \
"v_mfma_f32_16x16x32_bf16 v[60:63], a[88:91], a[24:27], v[60:63]\n" \
"ds_read_b64_tr_b16 a[116:117], v98 offset:17920\n" \
"v_mfma_f32_16x16x32_bf16 v[60:63], a[92:95], a[28:31], v[60:63]\n" \
"v_mfma_f32_16x16x32_bf16 v[56:59], a[80:83], a[24:27], v[56:59]\n" \
"ds_read_b64_tr_b16 a[118:119], v98 offset:22016\n" \
"v_mfma_f32_16x16x32_bf16 v[56:59], a[84:87], a[28:31], v[56:59]\n" \
"buffer_load_dwordx4 v70, s[40:43], s84 offen offset:2048 lds\n" \
"v_mfma_f32_16x16x32_bf16 v[52:55], a[72:75], a[24:27], v[52:55]\n" \
"ds_read_b64_tr_b16 a[120:121], v99 offset:1536\n" \
"v_mfma_f32_16x16x32_bf16 v[52:55], a[76:79], a[28:31], v[52:55]\n" \
"v_mfma_f32_16x16x32_bf16 v[48:51], a[64:67], a[24:27], v[48:51]\n" \
"ds_read_b64_tr_b16 a[122:123], v99 offset:5632\n" \
"v_mfma_f32_16x16x32_bf16 v[48:51], a[68:71], a[28:31], v[48:51]\n" \
"buffer_load_dwordx4 v71, s[44:47], s84 offen offset:3072 lds\n" \
"v_mfma_f32_16x16x32_bf16 v[144:147], a[64:67], a[32:35], v[144:147]\n" \
"ds_read_b64_tr_b16 a[124:125], v99 offset:17920\n" \
"v_mfma_f32_16x16x32_bf16 v[144:147], a[68:71], a[36:39], v[144:147]\n" \
"v_mfma_f32_16x16x32_bf16 v[148:151], a[72:75], a[32:35], v[148:151]\n" \
"s_xor_b32 s84, s84, 0x80000\n" \
"ds_read_b64_tr_b16 a[126:127], v99 offset:22016\n" \
"v_mfma_f32_16x16x32_bf16 v[148:151], a[76:79], a[36:39], v[148:151]\n" \
"v_mfma_f32_16x16x32_bf16 v[152:155], a[80:83], a[32:35], v[152:155]\n" \
"v_mfma_f32_16x16x32_bf16 v[152:155], a[84:87], a[36:39], v[152:155]\n" \
"v_mfma_f32_16x16x32_bf16 v[156:159], a[88:91], a[32:35], v[156:159]\n" \
"v_mfma_f32_16x16x32_bf16 v[156:159], a[92:95], a[36:39], v[156:159]\n" \
"v_mfma_f32_16x16x32_bf16 v[188:191], a[88:91], a[40:43], v[188:191]\n" \
"v_mfma_f32_16x16x32_bf16 v[188:191], a[92:95], a[44:47], v[188:191]\n" \
"v_mfma_f32_16x16x32_bf16 v[184:187], a[80:83], a[40:43], v[184:187]\n" \
"v_mfma_f32_16x16x32_bf16 v[184:187], a[84:87], a[44:47], v[184:187]\n" \
"v_mfma_f32_16x16x32_bf16 v[180:183], a[72:75], a[40:43], v[180:183]\n" \
"v_mfma_f32_16x16x32_bf16 v[180:183], a[76:79], a[44:47], v[180:183]\n" \
"v_mfma_f32_16x16x32_bf16 v[176:179], a[64:67], a[40:43], v[176:179]\n" \
"v_mfma_f32_16x16x32_bf16 v[176:179], a[68:71], a[44:47], v[176:179]\n" \
"s_waitcnt lgkmcnt(0)\n" \
"v_mfma_f32_16x16x32_bf16 v[192:195], a[64:67], a[48:51], v[192:195]\n" \
"v_mfma_f32_16x16x32_bf16 v[192:195], a[68:71], a[52:55], v[192:195]\n" \
"v_mfma_f32_16x16x32_bf16 v[196:199], a[72:75], a[48:51], v[196:199]\n" \
"v_mfma_f32_16x16x32_bf16 v[196:199], a[76:79], a[52:55], v[196:199]\n" \
"v_mfma_f32_16x16x32_bf16 v[200:203], a[80:83], a[48:51], v[200:203]\n" \
"v_mfma_f32_16x16x32_bf16 v[200:203], a[84:87], a[52:55], v[200:203]\n" \
"v_mfma_f32_16x16x32_bf16 v[204:207], a[88:91], a[48:51], v[204:207]\n" \
"v_mfma_f32_16x16x32_bf16 v[204:207], a[92:95], a[52:55], v[204:207]\n" \
"v_mfma_f32_16x16x32_bf16 v[220:223], a[88:91], a[56:59], v[220:223]\n" \
"v_mfma_f32_16x16x32_bf16 v[220:223], a[92:95], a[60:63], v[220:223]\n" \
"v_mfma_f32_16x16x32_bf16 v[216:219], a[80:83], a[56:59], v[216:219]\n" \
"v_mfma_f32_16x16x32_bf16 v[216:219], a[84:87], a[60:63], v[216:219]\n" \
"v_mfma_f32_16x16x32_bf16 v[212:215], a[72:75], a[56:59], v[212:215]\n" \
"v_mfma_f32_16x16x32_bf16 v[212:215], a[76:79], a[60:63], v[212:215]\n" \
"v_mfma_f32_16x16x32_bf16 v[208:211], a[64:67], a[56:59], v[208:211]\n" \
"v_mfma_f32_16x16x32_bf16 v[208:211], a[68:71], a[60:63], v[208:211]\n" \
"s_waitcnt vmcnt(11) lgkmcnt(0)\n" \
"s_barrier\n" \
"v_mfma_f32_16x16x32_bf16 a[128:131], a[0:3], a[96:99], a[128:131]\n" \
"s_add_u32 m0, s74, 65536\n" \
"ds_read_b64_tr_b16 a[64:65], v74 offset:32768\n" \
"v_mfma_f32_16x16x32_bf16 a[128:131], a[4:7], a[100:103], a[128:131]\n" \
"buffer_load_dwordx4 v64, s[36:39], s81 offen offset:0 lds\n" \
"v_mfma_f32_16x16x32_bf16 a[132:135], a[0:3], a[104:107], a[132:135]\n" \
"ds_read_b64_tr_b16 a[66:67], v74 offset:36864\n" \
"v_mfma_f32_16x16x32_bf16 a[132:135], a[4:7], a[108:111], a[132:135]\n" \
"v_mfma_f32_16x16x32_bf16 a[136:139], a[0:3], a[112:115], a[136:139]\n" \
"ds_read_b64_tr_b16 a[68:69], v74 offset:49152\n" \
"v_mfma_f32_16x16x32_bf16 a[136:139], a[4:7], a[116:119], a[136:139]\n" \
"buffer_load_dwordx4 v65, s[36:39], s81 offen offset:1024 lds\n" \
"v_mfma_f32_16x16x32_bf16 a[140:143], a[0:3], a[120:123], a[140:143]\n" \
"ds_read_b64_tr_b16 a[70:71], v74 offset:53248\n" \
"v_mfma_f32_16x16x32_bf16 a[140:143], a[4:7], a[124:127], a[140:143]\n" \
"v_mfma_f32_16x16x32_bf16 a[156:159], a[8:11], a[120:123], a[156:159]\n" \
"ds_read_b128 a[0:3], v72 offset:0\n" \
"v_mfma_f32_16x16x32_bf16 a[156:159], a[12:15], a[124:127], a[156:159]\n" \
"buffer_load_dwordx4 v66, s[36:39], s81 offen offset:2048 lds\n" \
"v_mfma_f32_16x16x32_bf16 a[152:155], a[8:11], a[112:115], a[152:155]\n" \
"ds_read_b64_tr_b16 a[72:73], v75 offset:32768\n" \
"v_mfma_f32_16x16x32_bf16 a[152:155], a[12:15], a[116:119], a[152:155]\n" \
"v_mfma_f32_16x16x32_bf16 a[148:151], a[8:11], a[104:107], a[148:151]\n" \
"ds_read_b128 a[4:7], v73 offset:0\n" \
"v_mfma_f32_16x16x32_bf16 a[148:151], a[12:15], a[108:111], a[148:151]\n" \
"buffer_load_dwordx4 v67, s[36:39], s81 offen offset:3072 lds\n" \
"v_mfma_f32_16x16x32_bf16 a[144:147], a[8:11], a[96:99], a[144:147]\n" \
"ds_read_b64_tr_b16 a[74:75], v75 offset:36864\n" \
"v_mfma_f32_16x16x32_bf16 a[144:147], a[12:15], a[100:103], a[144:147]\n" \
"s_xor_b32 s81, s81, 0x80\n" \
"v_mfma_f32_16x16x32_bf16 a[160:163], a[16:19], a[96:99], a[160:163]\n" \
"s_add_u32 m0, s74, 69632\n" \
"ds_read_b128 a[8:11], v72 offset:2048\n" \
"v_mfma_f32_16x16x32_bf16 a[160:163], a[20:23], a[100:103], a[160:163]\n" \
"buffer_load_dwordx4 v64, s[36:39], s82 offen offset:0 lds\n" \
"v_mfma_f32_16x16x32_bf16 a[164:167], a[16:19], a[104:107], a[164:167]\n" \
"ds_read_b64_tr_b16 a[76:77], v75 offset:49152\n" \
"v_mfma_f32_16x16x32_bf16 a[164:167], a[20:23], a[108:111], a[164:167]\n" \
"v_mfma_f32_16x16x32_bf16 a[168:171], a[16:19], a[112:115], a[168:171]\n" \
"ds_read_b128 a[12:15], v73 offset:2048\n" \
"v_mfma_f32_16x16x32_bf16 a[168:171], a[20:23], a[116:119], a[168:171]\n" \
"buffer_load_dwordx4 v65, s[36:39], s82 offen offset:1024 lds\n" \
"v_mfma_f32_16x16x32_bf16 a[172:175], a[16:19], a[120:123], a[172:175]\n" \
"ds_read_b64_tr_b16 a[78:79], v75 offset:53248\n" \
"v_mfma_f32_16x16x32_bf16 a[172:175], a[20:23], a[124:127], a[172:175]\n" \
"v_mfma_f32_16x16x32_bf16 a[188:191], a[24:27], a[120:123], a[188:191]\n" \
"ds_read_b128 a[16:19], v72 offset:4096\n" \
"v_mfma_f32_16x16x32_bf16 a[188:191], a[28:31], a[124:127], a[188:191]\n" \
"buffer_load_dwordx4 v66, s[36:39], s82 offen offset:2048 lds\n" \
"v_mfma_f32_16x16x32_bf16 a[184:187], a[24:27], a[112:115], a[184:187]\n" \
"ds_read_b64_tr_b16 a[80:81], v74 offset:33280\n" \
"v_mfma_f32_16x16x32_bf16 a[184:187], a[28:31], a[116:119], a[184:187]\n" \
"v_mfma_f32_16x16x32_bf16 a[180:183], a[24:27], a[104:107], a[180:183]\n" \
"ds_read_b128 a[20:23], v73 offset:4096\n" \
"v_mfma_f32_16x16x32_bf16 a[180:183], a[28:31], a[108:111], a[180:183]\n" \
"buffer_load_dwordx4 v67, s[36:39], s82 offen offset:3072 lds\n" \
"v_mfma_f32_16x16x32_bf16 a[176:179], a[24:27], a[96:99], a[176:179]\n" \
"s_mov_b64 s[64:65], s[12:13]\n" \
"s_add_u32 s68, s67, 0x100000\n" \
"ds_read_b64_tr_b16 a[82:83], v74 offset:37376\n" \
"v_mfma_f32_16x16x32_bf16 a[176:179], a[28:31], a[100:103], a[176:179]\n" \
"s_add_u32 s64, s64, s68\n" \
"s_addc_u32 s65, s65, 0\n" \
"v_mfma_f32_16x16x32_bf16 a[192:195], a[32:35], a[96:99], a[192:195]\n" \
"s_xor_b32 s82, s82, 0x80\n" \
"ds_read_b128 a[24:27], v72 offset:6144\n" \
"v_mfma_f32_16x16x32_bf16 a[192:195], a[36:39], a[100:103], a[192:195]\n" \
"global_load_dword v238, v234, s[64:65]\n" \
"v_mfma_f32_16x16x32_bf16 a[196:199], a[32:35], a[104:107], a[196:199]\n" \
"s_mov_b64 s[64:65], s[12:13]\n" \
"s_add_u32 s68, s67, 0x500000\n" \
"ds_read_b64_tr_b16 a[84:85], v74 offset:49664\n" \
"v_mfma_f32_16x16x32_bf16 a[196:199], a[36:39], a[108:111], a[196:199]\n" \
"s_add_u32 s64, s64, s68\n" \
"s_addc_u32 s65, s65, 0\n" \
"v_mfma_f32_16x16x32_bf16 a[200:203], a[32:35], a[112:115], a[200:203]\n" \
"ds_read_b128 a[28:31], v73 offset:6144\n" \
"v_mfma_f32_16x16x32_bf16 a[200:203], a[36:39], a[116:119], a[200:203]\n" \
"global_load_dword v238, v234, s[64:65]\n" \
"v_mfma_f32_16x16x32_bf16 a[204:207], a[32:35], a[120:123], a[204:207]\n" \
"s_mov_b64 s[64:65], s[12:13]\n" \
"s_add_u32 s68, s67, 0x200000\n" \
"ds_read_b64_tr_b16 a[86:87], v74 offset:53760\n" \
"v_mfma_f32_16x16x32_bf16 a[204:207], a[36:39], a[124:127], a[204:207]\n" \
"s_add_u32 s64, s64, s68\n" \
"s_addc_u32 s65, s65, 0\n" \
"v_mfma_f32_16x16x32_bf16 a[220:223], a[40:43], a[120:123], a[220:223]\n" \
"ds_read_b128 a[32:35], v72 offset:8192\n" \
"v_mfma_f32_16x16x32_bf16 a[220:223], a[44:47], a[124:127], a[220:223]\n" \
"global_load_dword v238, v234, s[64:65]\n" \
"v_mfma_f32_16x16x32_bf16 a[216:219], a[40:43], a[112:115], a[216:219]\n" \
"ds_read_b64_tr_b16 a[88:89], v75 offset:33280\n" \
"v_mfma_f32_16x16x32_bf16 a[216:219], a[44:47], a[116:119], a[216:219]\n" \
"v_mfma_f32_16x16x32_bf16 a[212:215], a[40:43], a[104:107], a[212:215]\n" \
"ds_read_b128 a[36:39], v73 offset:8192\n" \
"v_mfma_f32_16x16x32_bf16 a[212:215], a[44:47], a[108:111], a[212:215]\n" \
"v_mfma_f32_16x16x32_bf16 a[208:211], a[40:43], a[96:99], a[208:211]\n" \
"ds_read_b64_tr_b16 a[90:91], v75 offset:37376\n" \
"v_mfma_f32_16x16x32_bf16 a[208:211], a[44:47], a[100:103], a[208:211]\n" \
"v_mfma_f32_16x16x32_bf16 a[224:227], a[48:51], a[96:99], a[224:227]\n" \
"ds_read_b128 a[40:43], v72 offset:10240\n" \
"v_mfma_f32_16x16x32_bf16 a[224:227], a[52:55], a[100:103], a[224:227]\n" \
"v_mfma_f32_16x16x32_bf16 a[228:231], a[48:51], a[104:107], a[228:231]\n" \
"ds_read_b64_tr_b16 a[92:93], v75 offset:49664\n" \
"v_mfma_f32_16x16x32_bf16 a[228:231], a[52:55], a[108:111], a[228:231]\n" \
"v_mfma_f32_16x16x32_bf16 a[232:235], a[48:51], a[112:115], a[232:235]\n" \
"ds_read_b128 a[44:47], v73 offset:10240\n" \
"v_mfma_f32_16x16x32_bf16 a[232:235], a[52:55], a[116:119], a[232:235]\n" \
"v_mfma_f32_16x16x32_bf16 a[236:239], a[48:51], a[120:123], a[236:239]\n" \
"ds_read_b64_tr_b16 a[94:95], v75 offset:53760\n" \
"v_mfma_f32_16x16x32_bf16 a[236:239], a[52:55], a[124:127], a[236:239]\n" \
"v_mfma_f32_16x16x32_bf16 a[252:255], a[56:59], a[120:123], a[252:255]\n" \
"ds_read_b128 a[48:51], v72 offset:12288\n" \
"v_mfma_f32_16x16x32_bf16 a[252:255], a[60:63], a[124:127], a[252:255]\n" \
"v_mfma_f32_16x16x32_bf16 a[248:251], a[56:59], a[112:115], a[248:251]\n" \
"v_mfma_f32_16x16x32_bf16 a[248:251], a[60:63], a[116:119], a[248:251]\n" \
"v_mfma_f32_16x16x32_bf16 a[244:247], a[56:59], a[104:107], a[244:247]\n" \
"ds_read_b128 a[52:55], v73 offset:12288\n" \
"v_mfma_f32_16x16x32_bf16 a[244:247], a[60:63], a[108:111], a[244:247]\n" \
"v_mfma_f32_16x16x32_bf16 a[240:243], a[56:59], a[96:99], a[240:243]\n" \
"v_mfma_f32_16x16x32_bf16 a[240:243], a[60:63], a[100:103], a[240:243]\n" \
"s_waitcnt lgkmcnt(1)\n" \
"v_mfma_f32_16x16x32_bf16 v[0:3], a[64:67], a[0:3], v[0:3]\n" \
"ds_read_b128 a[56:59], v72 offset:14336\n" \
"v_mfma_f32_16x16x32_bf16 v[0:3], a[68:71], a[4:7], v[0:3]\n" \
"v_mfma_f32_16x16x32_bf16 v[4:7], a[72:75], a[0:3], v[4:7]\n" \
"s_add_u32 m0, s74, 98304\n" \
"ds_read_b128 a[60:63], v73 offset:14336\n" \
"v_mfma_f32_16x16x32_bf16 v[4:7], a[76:79], a[4:7], v[4:7]\n" \
"buffer_load_dwordx4 v68, s[40:43], s83 offen offset:0 lds\n" \
"v_mfma_f32_16x16x32_bf16 v[8:11], a[80:83], a[0:3], v[8:11]\n" \
"ds_read_b64_tr_b16 a[96:97], v74 offset:33792\n" \
"v_mfma_f32_16x16x32_bf16 v[8:11], a[84:87], a[4:7], v[8:11]\n" \
"v_mfma_f32_16x16x32_bf16 v[12:15], a[88:91], a[0:3], v[12:15]\n" \
"ds_read_b64_tr_b16 a[98:99], v74 offset:37888\n" \
"v_mfma_f32_16x16x32_bf16 v[12:15], a[92:95], a[4:7], v[12:15]\n" \
"buffer_load_dwordx4 v69, s[44:47], s83 offen offset:1024 lds\n" \
"v_mfma_f32_16x16x32_bf16 v[28:31], a[88:91], a[8:11], v[28:31]\n" \
"ds_read_b64_tr_b16 a[100:101], v74 offset:50176\n" \
"v_mfma_f32_16x16x32_bf16 v[28:31], a[92:95], a[12:15], v[28:31]\n" \
"v_mfma_f32_16x16x32_bf16 v[24:27], a[80:83], a[8:11], v[24:27]\n" \
"ds_read_b64_tr_b16 a[102:103], v74 offset:54272\n" \
"v_mfma_f32_16x16x32_bf16 v[24:27], a[84:87], a[12:15], v[24:27]\n" \
"buffer_load_dwordx4 v70, s[40:43], s83 offen offset:2048 lds\n" \
"v_mfma_f32_16x16x32_bf16 v[20:23], a[72:75], a[8:11], v[20:23]\n" \
"ds_read_b64_tr_b16 a[104:105], v75 offset:33792\n" \
"v_mfma_f32_16x16x32_bf16 v[20:23], a[76:79], a[12:15], v[20:23]\n" \
"v_mfma_f32_16x16x32_bf16 v[16:19], a[64:67], a[8:11], v[16:19]\n" \
"ds_read_b64_tr_b16 a[106:107], v75 offset:37888\n" \
"v_mfma_f32_16x16x32_bf16 v[16:19], a[68:71], a[12:15], v[16:19]\n" \
"buffer_load_dwordx4 v71, s[44:47], s83 offen offset:3072 lds\n" \
"v_mfma_f32_16x16x32_bf16 v[32:35], a[64:67], a[16:19], v[32:35]\n" \
"ds_read_b64_tr_b16 a[108:109], v75 offset:50176\n" \
"v_mfma_f32_16x16x32_bf16 v[32:35], a[68:71], a[20:23], v[32:35]\n" \
"v_mfma_f32_16x16x32_bf16 v[36:39], a[72:75], a[16:19], v[36:39]\n" \
"s_add_u32 m0, s74, 102400\n" \
"ds_read_b64_tr_b16 a[110:111], v75 offset:54272\n" \
"v_mfma_f32_16x16x32_bf16 v[36:39], a[76:79], a[20:23], v[36:39]\n" \
"buffer_load_dwordx4 v68, s[40:43], s84 offen offset:0 lds\n" \
"v_mfma_f32_16x16x32_bf16 v[40:43], a[80:83], a[16:19], v[40:43]\n" \
"ds_read_b64_tr_b16 a[112:113], v74 offset:34304\n" \
"v_mfma_f32_16x16x32_bf16 v[40:43], a[84:87], a[20:23], v[40:43]\n" \
"v_mfma_f32_16x16x32_bf16 v[44:47], a[88:91], a[16:19], v[44:47]\n" \
"ds_read_b64_tr_b16 a[114:115], v74 offset:38400\n" \
"v_mfma_f32_16x16x32_bf16 v[44:47], a[92:95], a[20:23], v[44:47]\n" \
"buffer_load_dwordx4 v69, s[44:47], s84 offen offset:1024 lds\n" \
"v_mfma_f32_16x16x32_bf16 v[60:63], a[88:91], a[24:27], v[60:63]\n" \
"ds_read_b64_tr_b16 a[116:117], v74 offset:50688\n" \
"v_mfma_f32_16x16x32_bf16 v[60:63], a[92:95], a[28:31], v[60:63]\n" \
"v_mfma_f32_16x16x32_bf16 v[56:59], a[80:83], a[24:27], v[56:59]\n" \
"ds_read_b64_tr_b16 a[118:119], v74 offset:54784\n" \
"v_mfma_f32_16x16x32_bf16 v[56:59], a[84:87], a[28:31], v[56:59]\n" \
"buffer_load_dwordx4 v70, s[40:43], s84 offen offset:2048 lds\n" \
"v_mfma_f32_16x16x32_bf16 v[52:55], a[72:75], a[24:27], v[52:55]\n" \
"ds_read_b64_tr_b16 a[120:121], v75 offset:34304\n" \
"v_mfma_f32_16x16x32_bf16 v[52:55], a[76:79], a[28:31], v[52:55]\n" \
"v_mfma_f32_16x16x32_bf16 v[48:51], a[64:67], a[24:27], v[48:51]\n" \
"ds_read_b64_tr_b16 a[122:123], v75 offset:38400\n" \
"v_mfma_f32_16x16x32_bf16 v[48:51], a[68:71], a[28:31], v[48:51]\n" \
"buffer_load_dwordx4 v71, s[44:47], s84 offen offset:3072 lds\n" \
"v_mfma_f32_16x16x32_bf16 v[144:147], a[64:67], a[32:35], v[144:147]\n" \
"ds_read_b64_tr_b16 a[124:125], v75 offset:50688\n" \
"v_mfma_f32_16x16x32_bf16 v[144:147], a[68:71], a[36:39], v[144:147]\n" \
"v_mfma_f32_16x16x32_bf16 v[148:151], a[72:75], a[32:35], v[148:151]\n" \
"ds_read_b64_tr_b16 a[126:127], v75 offset:54784\n" \
"v_mfma_f32_16x16x32_bf16 v[148:151], a[76:79], a[36:39], v[148:151]\n" \
"v_mfma_f32_16x16x32_bf16 v[152:155], a[80:83], a[32:35], v[152:155]\n" \
"v_mfma_f32_16x16x32_bf16 v[152:155], a[84:87], a[36:39], v[152:155]\n" \
"v_mfma_f32_16x16x32_bf16 v[156:159], a[88:91], a[32:35], v[156:159]\n" \
"v_mfma_f32_16x16x32_bf16 v[156:159], a[92:95], a[36:39], v[156:159]\n" \
"v_mfma_f32_16x16x32_bf16 v[188:191], a[88:91], a[40:43], v[188:191]\n" \
"v_mfma_f32_16x16x32_bf16 v[188:191], a[92:95], a[44:47], v[188:191]\n" \
"v_mfma_f32_16x16x32_bf16 v[184:187], a[80:83], a[40:43], v[184:187]\n" \
"v_mfma_f32_16x16x32_bf16 v[184:187], a[84:87], a[44:47], v[184:187]\n" \
"v_mfma_f32_16x16x32_bf16 v[180:183], a[72:75], a[40:43], v[180:183]\n" \
"v_mfma_f32_16x16x32_bf16 v[180:183], a[76:79], a[44:47], v[180:183]\n" \
"v_mfma_f32_16x16x32_bf16 v[176:179], a[64:67], a[40:43], v[176:179]\n" \
"v_mfma_f32_16x16x32_bf16 v[176:179], a[68:71], a[44:47], v[176:179]\n" \
"s_waitcnt lgkmcnt(0)\n" \
"v_mfma_f32_16x16x32_bf16 v[192:195], a[64:67], a[48:51], v[192:195]\n" \
"v_mfma_f32_16x16x32_bf16 v[192:195], a[68:71], a[52:55], v[192:195]\n" \
"v_mfma_f32_16x16x32_bf16 v[196:199], a[72:75], a[48:51], v[196:199]\n" \
"v_mfma_f32_16x16x32_bf16 v[196:199], a[76:79], a[52:55], v[196:199]\n" \
"v_mfma_f32_16x16x32_bf16 v[200:203], a[80:83], a[48:51], v[200:203]\n" \
"v_mfma_f32_16x16x32_bf16 v[200:203], a[84:87], a[52:55], v[200:203]\n" \
"v_mfma_f32_16x16x32_bf16 v[204:207], a[88:91], a[48:51], v[204:207]\n" \
"v_mfma_f32_16x16x32_bf16 v[204:207], a[92:95], a[52:55], v[204:207]\n" \
"v_mfma_f32_16x16x32_bf16 v[220:223], a[88:91], a[56:59], v[220:223]\n" \
"v_mfma_f32_16x16x32_bf16 v[220:223], a[92:95], a[60:63], v[220:223]\n" \
"v_mfma_f32_16x16x32_bf16 v[216:219], a[80:83], a[56:59], v[216:219]\n" \
"v_mfma_f32_16x16x32_bf16 v[216:219], a[84:87], a[60:63], v[216:219]\n" \
"v_mfma_f32_16x16x32_bf16 v[212:215], a[72:75], a[56:59], v[212:215]\n" \
"v_mfma_f32_16x16x32_bf16 v[212:215], a[76:79], a[60:63], v[212:215]\n" \
"v_mfma_f32_16x16x32_bf16 v[208:211], a[64:67], a[56:59], v[208:211]\n" \
"v_mfma_f32_16x16x32_bf16 v[208:211], a[68:71], a[60:63], v[208:211]\n" \
"s_waitcnt vmcnt(11) lgkmcnt(0)\n" \
"s_barrier\n" \
"v_mfma_f32_16x16x32_bf16 a[128:131], a[0:3], a[96:99], a[128:131]\n" \
"s_add_u32 m0, s74, 32768\n" \
"ds_read_b64_tr_b16 a[64:65], v102 offset:0\n" \
"v_mfma_f32_16x16x32_bf16 a[128:131], a[4:7], a[100:103], a[128:131]\n" \
"buffer_load_dwordx4 v64, s[36:39], s81 offen offset:0 lds\n" \
"v_mfma_f32_16x16x32_bf16 a[132:135], a[0:3], a[104:107], a[132:135]\n" \
"ds_read_b64_tr_b16 a[66:67], v102 offset:4096\n" \
"v_mfma_f32_16x16x32_bf16 a[132:135], a[4:7], a[108:111], a[132:135]\n" \
"v_mfma_f32_16x16x32_bf16 a[136:139], a[0:3], a[112:115], a[136:139]\n" \
"ds_read_b64_tr_b16 a[68:69], v102 offset:16384\n" \
"v_mfma_f32_16x16x32_bf16 a[136:139], a[4:7], a[116:119], a[136:139]\n" \
"buffer_load_dwordx4 v65, s[36:39], s81 offen offset:1024 lds\n" \
"v_mfma_f32_16x16x32_bf16 a[140:143], a[0:3], a[120:123], a[140:143]\n" \
"ds_read_b64_tr_b16 a[70:71], v102 offset:20480\n" \
"v_mfma_f32_16x16x32_bf16 a[140:143], a[4:7], a[124:127], a[140:143]\n" \
"v_mfma_f32_16x16x32_bf16 a[156:159], a[8:11], a[120:123], a[156:159]\n" \
"ds_read_b128 a[0:3], v96 offset:0\n" \
"v_mfma_f32_16x16x32_bf16 a[156:159], a[12:15], a[124:127], a[156:159]\n" \
"buffer_load_dwordx4 v66, s[36:39], s81 offen offset:2048 lds\n" \
"v_mfma_f32_16x16x32_bf16 a[152:155], a[8:11], a[112:115], a[152:155]\n" \
"ds_read_b64_tr_b16 a[72:73], v103 offset:0\n" \
"v_mfma_f32_16x16x32_bf16 a[152:155], a[12:15], a[116:119], a[152:155]\n" \
"v_mfma_f32_16x16x32_bf16 a[148:151], a[8:11], a[104:107], a[148:151]\n" \
"ds_read_b128 a[4:7], v97 offset:0\n" \
"v_mfma_f32_16x16x32_bf16 a[148:151], a[12:15], a[108:111], a[148:151]\n" \
"buffer_load_dwordx4 v67, s[36:39], s81 offen offset:3072 lds\n" \
"v_mfma_f32_16x16x32_bf16 a[144:147], a[8:11], a[96:99], a[144:147]\n" \
"ds_read_b64_tr_b16 a[74:75], v103 offset:4096\n" \
"v_mfma_f32_16x16x32_bf16 a[144:147], a[12:15], a[100:103], a[144:147]\n" \
"v_mfma_f32_16x16x32_bf16 a[160:163], a[16:19], a[96:99], a[160:163]\n" \
"s_add_u32 m0, s74, 36864\n" \
"ds_read_b128 a[8:11], v96 offset:2048\n" \
"v_mfma_f32_16x16x32_bf16 a[160:163], a[20:23], a[100:103], a[160:163]\n" \
"buffer_load_dwordx4 v64, s[36:39], s82 offen offset:0 lds\n" \
"v_mfma_f32_16x16x32_bf16 a[164:167], a[16:19], a[104:107], a[164:167]\n" \
"ds_read_b64_tr_b16 a[76:77], v103 offset:16384\n" \
"v_mfma_f32_16x16x32_bf16 a[164:167], a[20:23], a[108:111], a[164:167]\n" \
"v_mfma_f32_16x16x32_bf16 a[168:171], a[16:19], a[112:115], a[168:171]\n" \
"ds_read_b128 a[12:15], v97 offset:2048\n" \
"v_mfma_f32_16x16x32_bf16 a[168:171], a[20:23], a[116:119], a[168:171]\n" \
"buffer_load_dwordx4 v65, s[36:39], s82 offen offset:1024 lds\n" \
"v_mfma_f32_16x16x32_bf16 a[172:175], a[16:19], a[120:123], a[172:175]\n" \
"ds_read_b64_tr_b16 a[78:79], v103 offset:20480\n" \
"v_mfma_f32_16x16x32_bf16 a[172:175], a[20:23], a[124:127], a[172:175]\n" \
"v_mfma_f32_16x16x32_bf16 a[188:191], a[24:27], a[120:123], a[188:191]\n" \
"ds_read_b128 a[16:19], v96 offset:4096\n" \
"v_mfma_f32_16x16x32_bf16 a[188:191], a[28:31], a[124:127], a[188:191]\n" \
"buffer_load_dwordx4 v66, s[36:39], s82 offen offset:2048 lds\n" \
"v_mfma_f32_16x16x32_bf16 a[184:187], a[24:27], a[112:115], a[184:187]\n" \
"ds_read_b64_tr_b16 a[80:81], v102 offset:512\n" \
"v_mfma_f32_16x16x32_bf16 a[184:187], a[28:31], a[116:119], a[184:187]\n" \
"v_mfma_f32_16x16x32_bf16 a[180:183], a[24:27], a[104:107], a[180:183]\n" \
"ds_read_b128 a[20:23], v97 offset:4096\n" \
"v_mfma_f32_16x16x32_bf16 a[180:183], a[28:31], a[108:111], a[180:183]\n" \
"buffer_load_dwordx4 v67, s[36:39], s82 offen offset:3072 lds\n" \
"v_mfma_f32_16x16x32_bf16 a[176:179], a[24:27], a[96:99], a[176:179]\n" \
"s_mov_b64 s[64:65], s[12:13]\n" \
"s_add_u32 s68, s67, 0x300000\n" \
"ds_read_b64_tr_b16 a[82:83], v102 offset:4608\n" \
"v_mfma_f32_16x16x32_bf16 a[176:179], a[28:31], a[100:103], a[176:179]\n" \
"s_add_u32 s64, s64, s68\n" \
"s_addc_u32 s65, s65, 0\n" \
"v_mfma_f32_16x16x32_bf16 a[192:195], a[32:35], a[96:99], a[192:195]\n" \
"ds_read_b128 a[24:27], v96 offset:6144\n" \
"v_mfma_f32_16x16x32_bf16 a[192:195], a[36:39], a[100:103], a[192:195]\n" \
"global_load_dword v238, v234, s[64:65]\n" \
"v_mfma_f32_16x16x32_bf16 a[196:199], a[32:35], a[104:107], a[196:199]\n" \
"s_mov_b64 s[64:65], s[12:13]\n" \
"s_add_u32 s68, s67, 0x400000\n" \
"ds_read_b64_tr_b16 a[84:85], v102 offset:16896\n" \
"v_mfma_f32_16x16x32_bf16 a[196:199], a[36:39], a[108:111], a[196:199]\n" \
"s_add_u32 s64, s64, s68\n" \
"s_addc_u32 s65, s65, 0\n" \
"v_mfma_f32_16x16x32_bf16 a[200:203], a[32:35], a[112:115], a[200:203]\n" \
"ds_read_b128 a[28:31], v97 offset:6144\n" \
"v_mfma_f32_16x16x32_bf16 a[200:203], a[36:39], a[116:119], a[200:203]\n" \
"global_load_dword v238, v234, s[64:65]\n" \
"v_mfma_f32_16x16x32_bf16 a[204:207], a[32:35], a[120:123], a[204:207]\n" \
"s_mov_b64 s[64:65], s[12:13]\n" \
"s_add_u32 s68, s67, 0x600000\n" \
"ds_read_b64_tr_b16 a[86:87], v102 offset:20992\n" \
"v_mfma_f32_16x16x32_bf16 a[204:207], a[36:39], a[124:127], a[204:207]\n" \
"s_add_u32 s64, s64, s68\n" \
"s_addc_u32 s65, s65, 0\n" \
"v_mfma_f32_16x16x32_bf16 a[220:223], a[40:43], a[120:123], a[220:223]\n" \
"ds_read_b128 a[32:35], v96 offset:8192\n" \
"v_mfma_f32_16x16x32_bf16 a[220:223], a[44:47], a[124:127], a[220:223]\n" \
"global_load_dword v238, v234, s[64:65]\n" \
"v_mfma_f32_16x16x32_bf16 a[216:219], a[40:43], a[112:115], a[216:219]\n" \
"ds_read_b64_tr_b16 a[88:89], v103 offset:512\n" \
"v_mfma_f32_16x16x32_bf16 a[216:219], a[44:47], a[116:119], a[216:219]\n" \
"v_mfma_f32_16x16x32_bf16 a[212:215], a[40:43], a[104:107], a[212:215]\n" \
"ds_read_b128 a[36:39], v97 offset:8192\n" \
"v_mfma_f32_16x16x32_bf16 a[212:215], a[44:47], a[108:111], a[212:215]\n" \
"v_mfma_f32_16x16x32_bf16 a[208:211], a[40:43], a[96:99], a[208:211]\n" \
"ds_read_b64_tr_b16 a[90:91], v103 offset:4608\n" \
"v_mfma_f32_16x16x32_bf16 a[208:211], a[44:47], a[100:103], a[208:211]\n" \
"v_mfma_f32_16x16x32_bf16 a[224:227], a[48:51], a[96:99], a[224:227]\n" \
"ds_read_b128 a[40:43], v96 offset:10240\n" \
"v_mfma_f32_16x16x32_bf16 a[224:227], a[52:55], a[100:103], a[224:227]\n" \
"v_mfma_f32_16x16x32_bf16 a[228:231], a[48:51], a[104:107], a[228:231]\n" \
"ds_read_b64_tr_b16 a[92:93], v103 offset:16896\n" \
"v_mfma_f32_16x16x32_bf16 a[228:231], a[52:55], a[108:111], a[228:231]\n" \
"v_mfma_f32_16x16x32_bf16 a[232:235], a[48:51], a[112:115], a[232:235]\n" \
"ds_read_b128 a[44:47], v97 offset:10240\n" \
"v_mfma_f32_16x16x32_bf16 a[232:235], a[52:55], a[116:119], a[232:235]\n" \
"v_mfma_f32_16x16x32_bf16 a[236:239], a[48:51], a[120:123], a[236:239]\n" \
"ds_read_b64_tr_b16 a[94:95], v103 offset:20992\n" \
"v_mfma_f32_16x16x32_bf16 a[236:239], a[52:55], a[124:127], a[236:239]\n" \
"v_mfma_f32_16x16x32_bf16 a[252:255], a[56:59], a[120:123], a[252:255]\n" \
"ds_read_b128 a[48:51], v96 offset:12288\n" \
"v_mfma_f32_16x16x32_bf16 a[252:255], a[60:63], a[124:127], a[252:255]\n" \
"v_mfma_f32_16x16x32_bf16 a[248:251], a[56:59], a[112:115], a[248:251]\n" \
"v_mfma_f32_16x16x32_bf16 a[248:251], a[60:63], a[116:119], a[248:251]\n" \
"v_mfma_f32_16x16x32_bf16 a[244:247], a[56:59], a[104:107], a[244:247]\n" \
"ds_read_b128 a[52:55], v97 offset:12288\n" \
"v_mfma_f32_16x16x32_bf16 a[244:247], a[60:63], a[108:111], a[244:247]\n" \
"v_mfma_f32_16x16x32_bf16 a[240:243], a[56:59], a[96:99], a[240:243]\n" \
"v_mfma_f32_16x16x32_bf16 a[240:243], a[60:63], a[100:103], a[240:243]\n" \
"s_waitcnt lgkmcnt(1)\n" \
"v_mfma_f32_16x16x32_bf16 v[0:3], a[64:67], a[0:3], v[0:3]\n" \
"ds_read_b128 a[56:59], v96 offset:14336\n" \
"Lpg1663_%=:\n" \
"Lst0x2_%=:\n" \
"s_cbranch_execz Lpg3897_%=\n" \
"v_mfma_f32_16x16x32_bf16 v[0:3], a[68:71], a[4:7], v[0:3]\n" \
"v_mfma_f32_16x16x32_bf16 v[4:7], a[72:75], a[0:3], v[4:7]\n" \
"s_add_u32 m0, s79, 0\n" \
"ds_read_b128 a[60:63], v97 offset:14336\n" \
"v_mfma_f32_16x16x32_bf16 v[4:7], a[76:79], a[4:7], v[4:7]\n" \
"buffer_load_dwordx4 v228, s[52:55], s78 offen offset:0 lds\n" \
"v_mfma_f32_16x16x32_bf16 v[8:11], a[80:83], a[0:3], v[8:11]\n" \
"ds_read_b64_tr_b16 a[96:97], v102 offset:1024\n" \
"v_mfma_f32_16x16x32_bf16 v[8:11], a[84:87], a[4:7], v[8:11]\n" \
"v_mfma_f32_16x16x32_bf16 v[12:15], a[88:91], a[0:3], v[12:15]\n" \
"ds_read_b64_tr_b16 a[98:99], v102 offset:5120\n" \
"v_mfma_f32_16x16x32_bf16 v[12:15], a[92:95], a[4:7], v[12:15]\n" \
"buffer_load_dwordx4 v229, s[52:55], s78 offen offset:1024 lds\n" \
"v_mfma_f32_16x16x32_bf16 v[28:31], a[88:91], a[8:11], v[28:31]\n" \
"ds_read_b64_tr_b16 a[100:101], v102 offset:17408\n" \
"v_mfma_f32_16x16x32_bf16 v[28:31], a[92:95], a[12:15], v[28:31]\n" \
"v_mfma_f32_16x16x32_bf16 v[24:27], a[80:83], a[8:11], v[24:27]\n" \
"s_xor_b32 s78, s78, 0x180000\n" \
"ds_read_b64_tr_b16 a[102:103], v102 offset:21504\n" \
"v_mfma_f32_16x16x32_bf16 v[24:27], a[84:87], a[12:15], v[24:27]\n" \
"buffer_load_dwordx4 v228, s[52:55], s89 offen offset:2048 lds\n" \
"v_mfma_f32_16x16x32_bf16 v[20:23], a[72:75], a[8:11], v[20:23]\n" \
"ds_read_b64_tr_b16 a[104:105], v103 offset:1024\n" \
"v_mfma_f32_16x16x32_bf16 v[20:23], a[76:79], a[12:15], v[20:23]\n" \
"v_mfma_f32_16x16x32_bf16 v[16:19], a[64:67], a[8:11], v[16:19]\n" \
"ds_read_b64_tr_b16 a[106:107], v103 offset:5120\n" \
"v_mfma_f32_16x16x32_bf16 v[16:19], a[68:71], a[12:15], v[16:19]\n" \
"buffer_load_dwordx4 v229, s[52:55], s89 offen offset:3072 lds\n" \
"v_mfma_f32_16x16x32_bf16 v[32:35], a[64:67], a[16:19], v[32:35]\n" \
"ds_read_b64_tr_b16 a[108:109], v103 offset:17408\n" \
"v_mfma_f32_16x16x32_bf16 v[32:35], a[68:71], a[20:23], v[32:35]\n" \
"s_xor_b32 s89, s89, 0x180000\n" \
"v_mfma_f32_16x16x32_bf16 v[36:39], a[72:75], a[16:19], v[36:39]\n" \
"s_add_u32 m0, s79, 16384\n" \
"ds_read_b64_tr_b16 a[110:111], v103 offset:21504\n" \
"v_mfma_f32_16x16x32_bf16 v[36:39], a[76:79], a[20:23], v[36:39]\n" \
"buffer_load_dwordx4 v228, s[52:55], s78 offen offset:0 lds\n" \
"v_mfma_f32_16x16x32_bf16 v[40:43], a[80:83], a[16:19], v[40:43]\n" \
"ds_read_b64_tr_b16 a[112:113], v102 offset:1536\n" \
"v_mfma_f32_16x16x32_bf16 v[40:43], a[84:87], a[20:23], v[40:43]\n" \
"v_mfma_f32_16x16x32_bf16 v[44:47], a[88:91], a[16:19], v[44:47]\n" \
"ds_read_b64_tr_b16 a[114:115], v102 offset:5632\n" \
"v_mfma_f32_16x16x32_bf16 v[44:47], a[92:95], a[20:23], v[44:47]\n" \
"buffer_load_dwordx4 v229, s[52:55], s78 offen offset:1024 lds\n" \
"v_mfma_f32_16x16x32_bf16 v[60:63], a[88:91], a[24:27], v[60:63]\n" \
"ds_read_b64_tr_b16 a[116:117], v102 offset:17920\n" \
"v_mfma_f32_16x16x32_bf16 v[60:63], a[92:95], a[28:31], v[60:63]\n" \
"v_mfma_f32_16x16x32_bf16 v[56:59], a[80:83], a[24:27], v[56:59]\n" \
"s_xor_b32 s78, s78, 0x400000\n" \
"ds_read_b64_tr_b16 a[118:119], v102 offset:22016\n" \
"v_mfma_f32_16x16x32_bf16 v[56:59], a[84:87], a[28:31], v[56:59]\n" \
"buffer_load_dwordx4 v228, s[52:55], s89 offen offset:2048 lds\n" \
"v_mfma_f32_16x16x32_bf16 v[52:55], a[72:75], a[24:27], v[52:55]\n" \
"ds_read_b64_tr_b16 a[120:121], v103 offset:1536\n" \
"v_mfma_f32_16x16x32_bf16 v[52:55], a[76:79], a[28:31], v[52:55]\n" \
"v_mfma_f32_16x16x32_bf16 v[48:51], a[64:67], a[24:27], v[48:51]\n" \
"ds_read_b64_tr_b16 a[122:123], v103 offset:5632\n" \
"v_mfma_f32_16x16x32_bf16 v[48:51], a[68:71], a[28:31], v[48:51]\n" \
"buffer_load_dwordx4 v229, s[52:55], s89 offen offset:3072 lds\n" \
"v_mfma_f32_16x16x32_bf16 v[144:147], a[64:67], a[32:35], v[144:147]\n" \
"ds_read_b64_tr_b16 a[124:125], v103 offset:17920\n" \
"v_mfma_f32_16x16x32_bf16 v[144:147], a[68:71], a[36:39], v[144:147]\n" \
"v_mfma_f32_16x16x32_bf16 v[148:151], a[72:75], a[32:35], v[148:151]\n" \
"s_xor_b32 s89, s89, 0x400000\n" \
"ds_read_b64_tr_b16 a[126:127], v103 offset:22016\n" \
"v_mfma_f32_16x16x32_bf16 v[148:151], a[76:79], a[36:39], v[148:151]\n" \
"v_mfma_f32_16x16x32_bf16 v[152:155], a[80:83], a[32:35], v[152:155]\n" \
"v_mfma_f32_16x16x32_bf16 v[152:155], a[84:87], a[36:39], v[152:155]\n" \
"v_mfma_f32_16x16x32_bf16 v[156:159], a[88:91], a[32:35], v[156:159]\n" \
"v_mfma_f32_16x16x32_bf16 v[156:159], a[92:95], a[36:39], v[156:159]\n" \
"v_mfma_f32_16x16x32_bf16 v[188:191], a[88:91], a[40:43], v[188:191]\n" \
"v_mfma_f32_16x16x32_bf16 v[188:191], a[92:95], a[44:47], v[188:191]\n" \
"v_mfma_f32_16x16x32_bf16 v[184:187], a[80:83], a[40:43], v[184:187]\n" \
"v_mfma_f32_16x16x32_bf16 v[184:187], a[84:87], a[44:47], v[184:187]\n" \
"v_mfma_f32_16x16x32_bf16 v[180:183], a[72:75], a[40:43], v[180:183]\n" \
"v_mfma_f32_16x16x32_bf16 v[180:183], a[76:79], a[44:47], v[180:183]\n" \
"v_mfma_f32_16x16x32_bf16 v[176:179], a[64:67], a[40:43], v[176:179]\n" \
"v_mfma_f32_16x16x32_bf16 v[176:179], a[68:71], a[44:47], v[176:179]\n" \
"s_waitcnt lgkmcnt(0)\n" \
"v_mfma_f32_16x16x32_bf16 v[192:195], a[64:67], a[48:51], v[192:195]\n" \
"v_mfma_f32_16x16x32_bf16 v[192:195], a[68:71], a[52:55], v[192:195]\n" \
"v_mfma_f32_16x16x32_bf16 v[196:199], a[72:75], a[48:51], v[196:199]\n" \
"v_mfma_f32_16x16x32_bf16 v[196:199], a[76:79], a[52:55], v[196:199]\n" \
"v_mfma_f32_16x16x32_bf16 v[200:203], a[80:83], a[48:51], v[200:203]\n" \
"v_mfma_f32_16x16x32_bf16 v[200:203], a[84:87], a[52:55], v[200:203]\n" \
"v_mfma_f32_16x16x32_bf16 v[204:207], a[88:91], a[48:51], v[204:207]\n" \
"v_mfma_f32_16x16x32_bf16 v[204:207], a[92:95], a[52:55], v[204:207]\n" \
"v_mfma_f32_16x16x32_bf16 v[220:223], a[88:91], a[56:59], v[220:223]\n" \
"v_mfma_f32_16x16x32_bf16 v[220:223], a[92:95], a[60:63], v[220:223]\n" \
"v_mfma_f32_16x16x32_bf16 v[216:219], a[80:83], a[56:59], v[216:219]\n" \
"v_mfma_f32_16x16x32_bf16 v[216:219], a[84:87], a[60:63], v[216:219]\n" \
"v_mfma_f32_16x16x32_bf16 v[212:215], a[72:75], a[56:59], v[212:215]\n" \
"v_mfma_f32_16x16x32_bf16 v[212:215], a[76:79], a[60:63], v[212:215]\n" \
"v_mfma_f32_16x16x32_bf16 v[208:211], a[64:67], a[56:59], v[208:211]\n" \
"v_mfma_f32_16x16x32_bf16 v[208:211], a[68:71], a[60:63], v[208:211]\n" \
"s_waitcnt vmcnt(11) lgkmcnt(0)\n" \
"s_barrier\n" \
"v_mfma_f32_16x16x32_bf16 a[128:131], a[0:3], a[96:99], a[128:131]\n" \
"s_add_u32 m0, s74, 131072\n" \
"ds_read_b64_tr_b16 a[64:65], v98 offset:32768\n" \
"v_mfma_f32_16x16x32_bf16 a[128:131], a[4:7], a[100:103], a[128:131]\n" \
"buffer_load_dwordx4 v224, s[48:51], s75 offen offset:0 lds\n" \
"v_mfma_f32_16x16x32_bf16 a[132:135], a[0:3], a[104:107], a[132:135]\n" \
"ds_read_b64_tr_b16 a[66:67], v98 offset:36864\n" \
"v_mfma_f32_16x16x32_bf16 a[132:135], a[4:7], a[108:111], a[132:135]\n" \
"v_mfma_f32_16x16x32_bf16 a[136:139], a[0:3], a[112:115], a[136:139]\n" \
"ds_read_b64_tr_b16 a[68:69], v98 offset:49152\n" \
"v_mfma_f32_16x16x32_bf16 a[136:139], a[4:7], a[116:119], a[136:139]\n" \
"buffer_load_dwordx4 v225, s[48:51], s75 offen offset:1024 lds\n" \
"v_mfma_f32_16x16x32_bf16 a[140:143], a[0:3], a[120:123], a[140:143]\n" \
"ds_read_b64_tr_b16 a[70:71], v98 offset:53248\n" \
"v_mfma_f32_16x16x32_bf16 a[140:143], a[4:7], a[124:127], a[140:143]\n" \
"v_mfma_f32_16x16x32_bf16 a[156:159], a[8:11], a[120:123], a[156:159]\n" \
"ds_read_b128 a[0:3], v72 offset:32768\n" \
"v_mfma_f32_16x16x32_bf16 a[156:159], a[12:15], a[124:127], a[156:159]\n" \
"buffer_load_dwordx4 v226, s[48:51], s75 offen offset:2048 lds\n" \
"v_mfma_f32_16x16x32_bf16 a[152:155], a[8:11], a[112:115], a[152:155]\n" \
"ds_read_b64_tr_b16 a[72:73], v99 offset:32768\n" \
"v_mfma_f32_16x16x32_bf16 a[152:155], a[12:15], a[116:119], a[152:155]\n" \
"v_mfma_f32_16x16x32_bf16 a[148:151], a[8:11], a[104:107], a[148:151]\n" \
"ds_read_b128 a[4:7], v73 offset:32768\n" \
"v_mfma_f32_16x16x32_bf16 a[148:151], a[12:15], a[108:111], a[148:151]\n" \
"buffer_load_dwordx4 v227, s[48:51], s75 offen offset:3072 lds\n" \
"v_mfma_f32_16x16x32_bf16 a[144:147], a[8:11], a[96:99], a[144:147]\n" \
"ds_read_b64_tr_b16 a[74:75], v99 offset:36864\n" \
"v_mfma_f32_16x16x32_bf16 a[144:147], a[12:15], a[100:103], a[144:147]\n" \
"s_xor_b32 s75, s75, 0x180\n" \
"v_mfma_f32_16x16x32_bf16 a[160:163], a[16:19], a[96:99], a[160:163]\n" \
"s_add_u32 m0, s74, 135168\n" \
"ds_read_b128 a[8:11], v72 offset:34816\n" \
"v_mfma_f32_16x16x32_bf16 a[160:163], a[20:23], a[100:103], a[160:163]\n" \
"buffer_load_dwordx4 v224, s[48:51], s77 offen offset:0 lds\n" \
"v_mfma_f32_16x16x32_bf16 a[164:167], a[16:19], a[104:107], a[164:167]\n" \
"ds_read_b64_tr_b16 a[76:77], v99 offset:49152\n" \
"v_mfma_f32_16x16x32_bf16 a[164:167], a[20:23], a[108:111], a[164:167]\n" \
"v_mfma_f32_16x16x32_bf16 a[168:171], a[16:19], a[112:115], a[168:171]\n" \
"ds_read_b128 a[12:15], v73 offset:34816\n" \
"v_mfma_f32_16x16x32_bf16 a[168:171], a[20:23], a[116:119], a[168:171]\n" \
"buffer_load_dwordx4 v225, s[48:51], s77 offen offset:1024 lds\n" \
"v_mfma_f32_16x16x32_bf16 a[172:175], a[16:19], a[120:123], a[172:175]\n" \
"ds_read_b64_tr_b16 a[78:79], v99 offset:53248\n" \
"v_mfma_f32_16x16x32_bf16 a[172:175], a[20:23], a[124:127], a[172:175]\n" \
"v_mfma_f32_16x16x32_bf16 a[188:191], a[24:27], a[120:123], a[188:191]\n" \
"ds_read_b128 a[16:19], v72 offset:36864\n" \
"v_mfma_f32_16x16x32_bf16 a[188:191], a[28:31], a[124:127], a[188:191]\n" \
"buffer_load_dwordx4 v226, s[48:51], s77 offen offset:2048 lds\n" \
"v_mfma_f32_16x16x32_bf16 a[184:187], a[24:27], a[112:115], a[184:187]\n" \
"ds_read_b64_tr_b16 a[80:81], v98 offset:33280\n" \
"v_mfma_f32_16x16x32_bf16 a[184:187], a[28:31], a[116:119], a[184:187]\n" \
"v_mfma_f32_16x16x32_bf16 a[180:183], a[24:27], a[104:107], a[180:183]\n" \
"ds_read_b128 a[20:23], v73 offset:36864\n" \
"v_mfma_f32_16x16x32_bf16 a[180:183], a[28:31], a[108:111], a[180:183]\n" \
"buffer_load_dwordx4 v227, s[48:51], s77 offen offset:3072 lds\n" \
"v_mfma_f32_16x16x32_bf16 a[176:179], a[24:27], a[96:99], a[176:179]\n" \
"s_mov_b64 s[64:65], s[12:13]\n" \
"s_add_u32 s68, s67, 0x700000\n" \
"ds_read_b64_tr_b16 a[82:83], v98 offset:37376\n" \
"v_mfma_f32_16x16x32_bf16 a[176:179], a[28:31], a[100:103], a[176:179]\n" \
"s_add_u32 s64, s64, s68\n" \
"s_addc_u32 s65, s65, 0\n" \
"v_mfma_f32_16x16x32_bf16 a[192:195], a[32:35], a[96:99], a[192:195]\n" \
"s_xor_b32 s77, s77, 0x180\n" \
"ds_read_b128 a[24:27], v72 offset:38912\n" \
"v_mfma_f32_16x16x32_bf16 a[192:195], a[36:39], a[100:103], a[192:195]\n" \
"global_load_dword v238, v234, s[64:65]\n" \
"v_mfma_f32_16x16x32_bf16 a[196:199], a[32:35], a[104:107], a[196:199]\n" \
"ds_read_b64_tr_b16 a[84:85], v98 offset:49664\n" \
"v_mfma_f32_16x16x32_bf16 a[196:199], a[36:39], a[108:111], a[196:199]\n" \
"v_mfma_f32_16x16x32_bf16 a[200:203], a[32:35], a[112:115], a[200:203]\n" \
"s_lshr_b32 s68, s66, 2\n" \
"s_and_b32 s68, s68, 0xffff\n" \
"s_add_u32 s68, s68, 65585152\n" \
"ds_read_b128 a[28:31], v73 offset:38912\n" \
"v_mfma_f32_16x16x32_bf16 a[200:203], a[36:39], a[116:119], a[200:203]\n" \
"buffer_load_dword v238, v242, s[56:59], s68 offen sc1\n" \
"v_mfma_f32_16x16x32_bf16 a[204:207], a[32:35], a[120:123], a[204:207]\n" \
"ds_read_b64_tr_b16 a[86:87], v98 offset:53760\n" \
"v_mfma_f32_16x16x32_bf16 a[204:207], a[36:39], a[124:127], a[204:207]\n" \
"v_mfma_f32_16x16x32_bf16 a[220:223], a[40:43], a[120:123], a[220:223]\n" \
"ds_read_b128 a[32:35], v72 offset:40960\n" \
"v_mfma_f32_16x16x32_bf16 a[220:223], a[44:47], a[124:127], a[220:223]\n" \
"v_mfma_f32_16x16x32_bf16 a[216:219], a[40:43], a[112:115], a[216:219]\n" \
"ds_read_b64_tr_b16 a[88:89], v99 offset:33280\n" \
"v_mfma_f32_16x16x32_bf16 a[216:219], a[44:47], a[116:119], a[216:219]\n" \
"v_mfma_f32_16x16x32_bf16 a[212:215], a[40:43], a[104:107], a[212:215]\n" \
"ds_read_b128 a[36:39], v73 offset:40960\n" \
"v_mfma_f32_16x16x32_bf16 a[212:215], a[44:47], a[108:111], a[212:215]\n" \
"v_mfma_f32_16x16x32_bf16 a[208:211], a[40:43], a[96:99], a[208:211]\n" \
"ds_read_b64_tr_b16 a[90:91], v99 offset:37376\n" \
"v_mfma_f32_16x16x32_bf16 a[208:211], a[44:47], a[100:103], a[208:211]\n" \
"v_mfma_f32_16x16x32_bf16 a[224:227], a[48:51], a[96:99], a[224:227]\n" \
"ds_read_b128 a[40:43], v72 offset:43008\n" \
"v_mfma_f32_16x16x32_bf16 a[224:227], a[52:55], a[100:103], a[224:227]\n" \
"v_mfma_f32_16x16x32_bf16 a[228:231], a[48:51], a[104:107], a[228:231]\n" \
"ds_read_b64_tr_b16 a[92:93], v99 offset:49664\n" \
"v_mfma_f32_16x16x32_bf16 a[228:231], a[52:55], a[108:111], a[228:231]\n" \
"v_mfma_f32_16x16x32_bf16 a[232:235], a[48:51], a[112:115], a[232:235]\n" \
"ds_read_b128 a[44:47], v73 offset:43008\n" \
"v_mfma_f32_16x16x32_bf16 a[232:235], a[52:55], a[116:119], a[232:235]\n" \
"v_mfma_f32_16x16x32_bf16 a[236:239], a[48:51], a[120:123], a[236:239]\n" \
"ds_read_b64_tr_b16 a[94:95], v99 offset:53760\n" \
"v_mfma_f32_16x16x32_bf16 a[236:239], a[52:55], a[124:127], a[236:239]\n" \
"v_mfma_f32_16x16x32_bf16 a[252:255], a[56:59], a[120:123], a[252:255]\n" \
"ds_read_b128 a[48:51], v72 offset:45056\n" \
"v_mfma_f32_16x16x32_bf16 a[252:255], a[60:63], a[124:127], a[252:255]\n" \
"v_mfma_f32_16x16x32_bf16 a[248:251], a[56:59], a[112:115], a[248:251]\n" \
"v_mfma_f32_16x16x32_bf16 a[248:251], a[60:63], a[116:119], a[248:251]\n" \
"v_mfma_f32_16x16x32_bf16 a[244:247], a[56:59], a[104:107], a[244:247]\n" \
"ds_read_b128 a[52:55], v73 offset:45056\n" \
"v_mfma_f32_16x16x32_bf16 a[244:247], a[60:63], a[108:111], a[244:247]\n" \
"v_mfma_f32_16x16x32_bf16 a[240:243], a[56:59], a[96:99], a[240:243]\n" \
"v_mfma_f32_16x16x32_bf16 a[240:243], a[60:63], a[100:103], a[240:243]\n" \
"s_waitcnt lgkmcnt(1)\n" \
"v_mfma_f32_16x16x32_bf16 v[0:3], a[64:67], a[0:3], v[0:3]\n" \
"ds_read_b128 a[56:59], v72 offset:47104\n" \
"v_mfma_f32_16x16x32_bf16 v[0:3], a[68:71], a[4:7], v[0:3]\n" \
"v_mfma_f32_16x16x32_bf16 v[4:7], a[72:75], a[0:3], v[4:7]\n" \
"s_add_u32 m0, s74, 65536\n" \
"ds_read_b128 a[60:63], v73 offset:47104\n" \
"v_mfma_f32_16x16x32_bf16 v[4:7], a[76:79], a[4:7], v[4:7]\n" \
"buffer_load_dwordx4 v224, s[48:51], s75 offen offset:0 lds\n" \
"v_mfma_f32_16x16x32_bf16 v[8:11], a[80:83], a[0:3], v[8:11]\n" \
"ds_read_b64_tr_b16 a[96:97], v98 offset:33792\n" \
"v_mfma_f32_16x16x32_bf16 v[8:11], a[84:87], a[4:7], v[8:11]\n" \
"v_mfma_f32_16x16x32_bf16 v[12:15], a[88:91], a[0:3], v[12:15]\n" \
"ds_read_b64_tr_b16 a[98:99], v98 offset:37888\n" \
"v_mfma_f32_16x16x32_bf16 v[12:15], a[92:95], a[4:7], v[12:15]\n" \
"buffer_load_dwordx4 v225, s[48:51], s75 offen offset:1024 lds\n" \
"v_mfma_f32_16x16x32_bf16 v[28:31], a[88:91], a[8:11], v[28:31]\n" \
"ds_read_b64_tr_b16 a[100:101], v98 offset:50176\n" \
"v_mfma_f32_16x16x32_bf16 v[28:31], a[92:95], a[12:15], v[28:31]\n" \
"v_mfma_f32_16x16x32_bf16 v[24:27], a[80:83], a[8:11], v[24:27]\n" \
"ds_read_b64_tr_b16 a[102:103], v98 offset:54272\n" \
"v_mfma_f32_16x16x32_bf16 v[24:27], a[84:87], a[12:15], v[24:27]\n" \
"buffer_load_dwordx4 v226, s[48:51], s75 offen offset:2048 lds\n" \
"v_mfma_f32_16x16x32_bf16 v[20:23], a[72:75], a[8:11], v[20:23]\n" \
"ds_read_b64_tr_b16 a[104:105], v99 offset:33792\n" \
"v_mfma_f32_16x16x32_bf16 v[20:23], a[76:79], a[12:15], v[20:23]\n" \
"v_mfma_f32_16x16x32_bf16 v[16:19], a[64:67], a[8:11], v[16:19]\n" \
"ds_read_b64_tr_b16 a[106:107], v99 offset:37888\n" \
"v_mfma_f32_16x16x32_bf16 v[16:19], a[68:71], a[12:15], v[16:19]\n" \
"buffer_load_dwordx4 v227, s[48:51], s75 offen offset:3072 lds\n" \
"v_mfma_f32_16x16x32_bf16 v[32:35], a[64:67], a[16:19], v[32:35]\n" \
"ds_read_b64_tr_b16 a[108:109], v99 offset:50176\n" \
"v_mfma_f32_16x16x32_bf16 v[32:35], a[68:71], a[20:23], v[32:35]\n" \
"s_xor_b32 s75, s75, 0x400\n" \
"v_mfma_f32_16x16x32_bf16 v[36:39], a[72:75], a[16:19], v[36:39]\n" \
"s_add_u32 m0, s74, 69632\n" \
"ds_read_b64_tr_b16 a[110:111], v99 offset:54272\n" \
"v_mfma_f32_16x16x32_bf16 v[36:39], a[76:79], a[20:23], v[36:39]\n" \
"buffer_load_dwordx4 v224, s[48:51], s77 offen offset:0 lds\n" \
"v_mfma_f32_16x16x32_bf16 v[40:43], a[80:83], a[16:19], v[40:43]\n" \
"ds_read_b64_tr_b16 a[112:113], v98 offset:34304\n" \
"v_mfma_f32_16x16x32_bf16 v[40:43], a[84:87], a[20:23], v[40:43]\n" \
"v_mfma_f32_16x16x32_bf16 v[44:47], a[88:91], a[16:19], v[44:47]\n" \
"ds_read_b64_tr_b16 a[114:115], v98 offset:38400\n" \
"v_mfma_f32_16x16x32_bf16 v[44:47], a[92:95], a[20:23], v[44:47]\n" \
"buffer_load_dwordx4 v225, s[48:51], s77 offen offset:1024 lds\n" \
"v_mfma_f32_16x16x32_bf16 v[60:63], a[88:91], a[24:27], v[60:63]\n" \
"ds_read_b64_tr_b16 a[116:117], v98 offset:50688\n" \
"v_mfma_f32_16x16x32_bf16 v[60:63], a[92:95], a[28:31], v[60:63]\n" \
"v_mfma_f32_16x16x32_bf16 v[56:59], a[80:83], a[24:27], v[56:59]\n" \
"ds_read_b64_tr_b16 a[118:119], v98 offset:54784\n" \
"v_mfma_f32_16x16x32_bf16 v[56:59], a[84:87], a[28:31], v[56:59]\n" \
"buffer_load_dwordx4 v226, s[48:51], s77 offen offset:2048 lds\n" \
"v_mfma_f32_16x16x32_bf16 v[52:55], a[72:75], a[24:27], v[52:55]\n" \
"ds_read_b64_tr_b16 a[120:121], v99 offset:34304\n" \
"v_mfma_f32_16x16x32_bf16 v[52:55], a[76:79], a[28:31], v[52:55]\n" \
"v_mfma_f32_16x16x32_bf16 v[48:51], a[64:67], a[24:27], v[48:51]\n" \
"ds_read_b64_tr_b16 a[122:123], v99 offset:38400\n" \
"v_mfma_f32_16x16x32_bf16 v[48:51], a[68:71], a[28:31], v[48:51]\n" \
"buffer_load_dwordx4 v227, s[48:51], s77 offen offset:3072 lds\n" \
"v_mfma_f32_16x16x32_bf16 v[144:147], a[64:67], a[32:35], v[144:147]\n" \
"ds_read_b64_tr_b16 a[124:125], v99 offset:50688\n" \
"v_mfma_f32_16x16x32_bf16 v[144:147], a[68:71], a[36:39], v[144:147]\n" \
"v_mfma_f32_16x16x32_bf16 v[148:151], a[72:75], a[32:35], v[148:151]\n" \
"s_xor_b32 s77, s77, 0x400\n" \
"ds_read_b64_tr_b16 a[126:127], v99 offset:54784\n" \
"v_mfma_f32_16x16x32_bf16 v[148:151], a[76:79], a[36:39], v[148:151]\n" \
"v_mfma_f32_16x16x32_bf16 v[152:155], a[80:83], a[32:35], v[152:155]\n" \
"v_mfma_f32_16x16x32_bf16 v[152:155], a[84:87], a[36:39], v[152:155]\n" \
"v_mfma_f32_16x16x32_bf16 v[156:159], a[88:91], a[32:35], v[156:159]\n" \
"v_mfma_f32_16x16x32_bf16 v[156:159], a[92:95], a[36:39], v[156:159]\n" \
"v_mfma_f32_16x16x32_bf16 v[188:191], a[88:91], a[40:43], v[188:191]\n" \
"v_mfma_f32_16x16x32_bf16 v[188:191], a[92:95], a[44:47], v[188:191]\n" \
"v_mfma_f32_16x16x32_bf16 v[184:187], a[80:83], a[40:43], v[184:187]\n" \
"v_mfma_f32_16x16x32_bf16 v[184:187], a[84:87], a[44:47], v[184:187]\n" \
"v_mfma_f32_16x16x32_bf16 v[180:183], a[72:75], a[40:43], v[180:183]\n" \
"v_mfma_f32_16x16x32_bf16 v[180:183], a[76:79], a[44:47], v[180:183]\n" \
"v_mfma_f32_16x16x32_bf16 v[176:179], a[64:67], a[40:43], v[176:179]\n" \
"v_mfma_f32_16x16x32_bf16 v[176:179], a[68:71], a[44:47], v[176:179]\n" \
"s_waitcnt lgkmcnt(0)\n" \
"v_mfma_f32_16x16x32_bf16 v[192:195], a[64:67], a[48:51], v[192:195]\n" \
"v_mfma_f32_16x16x32_bf16 v[192:195], a[68:71], a[52:55], v[192:195]\n" \
"v_mfma_f32_16x16x32_bf16 v[196:199], a[72:75], a[48:51], v[196:199]\n" \
"v_mfma_f32_16x16x32_bf16 v[196:199], a[76:79], a[52:55], v[196:199]\n" \
"v_mfma_f32_16x16x32_bf16 v[200:203], a[80:83], a[48:51], v[200:203]\n" \
"v_mfma_f32_16x16x32_bf16 v[200:203], a[84:87], a[52:55], v[200:203]\n" \
"v_mfma_f32_16x16x32_bf16 v[204:207], a[88:91], a[48:51], v[204:207]\n" \
"v_mfma_f32_16x16x32_bf16 v[204:207], a[92:95], a[52:55], v[204:207]\n" \
"v_mfma_f32_16x16x32_bf16 v[220:223], a[88:91], a[56:59], v[220:223]\n" \
"v_mfma_f32_16x16x32_bf16 v[220:223], a[92:95], a[60:63], v[220:223]\n" \
"v_mfma_f32_16x16x32_bf16 v[216:219], a[80:83], a[56:59], v[216:219]\n" \
"v_mfma_f32_16x16x32_bf16 v[216:219], a[84:87], a[60:63], v[216:219]\n" \
"v_mfma_f32_16x16x32_bf16 v[212:215], a[72:75], a[56:59], v[212:215]\n" \
"v_mfma_f32_16x16x32_bf16 v[212:215], a[76:79], a[60:63], v[212:215]\n" \
"v_mfma_f32_16x16x32_bf16 v[208:211], a[64:67], a[56:59], v[208:211]\n" \
"v_mfma_f32_16x16x32_bf16 v[208:211], a[68:71], a[60:63], v[208:211]\n" \
"s_waitcnt vmcnt(10) lgkmcnt(0)\n" \
"s_barrier\n" \
"v_mfma_f32_16x16x32_bf16 a[128:131], a[0:3], a[96:99], a[128:131]\n" \
"s_add_u32 m0, s74, 98304\n" \
"v_mfma_f32_16x16x32_bf16 a[128:131], a[4:7], a[100:103], a[128:131]\n" \
"buffer_load_dwordx4 v224, s[48:51], s75 offen offset:0 lds\n" \
"v_mfma_f32_16x16x32_bf16 a[132:135], a[0:3], a[104:107], a[132:135]\n" \
"ds_read_b64_tr_b16 a[64:65], v232 offset:0\n" \
"ds_read_b64_tr_b16 a[66:67], v232 offset:2048\n" \
"v_mfma_f32_16x16x32_bf16 a[132:135], a[4:7], a[108:111], a[132:135]\n" \
"v_mfma_f32_16x16x32_bf16 a[136:139], a[0:3], a[112:115], a[136:139]\n" \
"v_mfma_f32_16x16x32_bf16 a[136:139], a[4:7], a[116:119], a[136:139]\n" \
"buffer_load_dwordx4 v225, s[48:51], s75 offen offset:1024 lds\n" \
"v_mfma_f32_16x16x32_bf16 a[140:143], a[0:3], a[120:123], a[140:143]\n" \
"ds_read_b64_tr_b16 a[68:69], v232 offset:8192\n" \
"ds_read_b64_tr_b16 a[70:71], v232 offset:10240\n" \
"v_mfma_f32_16x16x32_bf16 a[140:143], a[4:7], a[124:127], a[140:143]\n" \
"v_mfma_f32_16x16x32_bf16 a[156:159], a[8:11], a[120:123], a[156:159]\n" \
"v_mul_f32 v192, s76, v192\n" \
"v_mul_f32 v193, s76, v193\n" \
"v_mfma_f32_16x16x32_bf16 a[156:159], a[12:15], a[124:127], a[156:159]\n" \
"buffer_load_dwordx4 v226, s[48:51], s75 offen offset:2048 lds\n" \
"v_mfma_f32_16x16x32_bf16 a[152:155], a[8:11], a[112:115], a[152:155]\n" \
"ds_read_b64_tr_b16 a[72:73], v233 offset:0\n" \
"ds_read_b64_tr_b16 a[74:75], v233 offset:2048\n" \
"v_mfma_f32_16x16x32_bf16 a[152:155], a[12:15], a[116:119], a[152:155]\n" \
"v_mul_f32 v194, s76, v194\n" \
"v_mul_f32 v195, s76, v195\n" \
"v_mfma_f32_16x16x32_bf16 a[148:151], a[8:11], a[104:107], a[148:151]\n" \
"v_cvt_pk_bf16_f32 v192, v192, v193\n" \
"v_cvt_pk_bf16_f32 v193, v194, v195\n" \
"v_mfma_f32_16x16x32_bf16 a[148:151], a[12:15], a[108:111], a[148:151]\n" \
"buffer_load_dwordx4 v227, s[48:51], s75 offen offset:3072 lds\n" \
"v_mfma_f32_16x16x32_bf16 a[144:147], a[8:11], a[96:99], a[144:147]\n" \
"ds_read_b64_tr_b16 a[76:77], v233 offset:8192\n" \
"ds_read_b64_tr_b16 a[78:79], v233 offset:10240\n" \
"v_mfma_f32_16x16x32_bf16 a[144:147], a[12:15], a[100:103], a[144:147]\n" \
"s_xor_b32 s75, s75, 0x500\n" \
"v_mul_f32 v196, s76, v196\n" \
"v_mul_f32 v197, s76, v197\n" \
"v_mfma_f32_16x16x32_bf16 a[160:163], a[16:19], a[96:99], a[160:163]\n" \
"s_add_u32 m0, s74, 102400\n" \
"v_mul_f32 v198, s76, v198\n" \
"v_mul_f32 v199, s76, v199\n" \
"v_mfma_f32_16x16x32_bf16 a[160:163], a[20:23], a[100:103], a[160:163]\n" \
"buffer_load_dwordx4 v224, s[48:51], s77 offen offset:0 lds\n" \
"v_mfma_f32_16x16x32_bf16 a[164:167], a[16:19], a[104:107], a[164:167]\n" \
"ds_read_b64_tr_b16 a[80:81], v232 offset:512\n" \
"ds_read_b64_tr_b16 a[82:83], v232 offset:2560\n" \
"v_mfma_f32_16x16x32_bf16 a[164:167], a[20:23], a[108:111], a[164:167]\n" \
"v_cvt_pk_bf16_f32 v194, v196, v197\n" \
"v_cvt_pk_bf16_f32 v195, v198, v199\n" \
"v_mfma_f32_16x16x32_bf16 a[168:171], a[16:19], a[112:115], a[168:171]\n" \
"v_mul_f32 v208, s76, v208\n" \
"v_mul_f32 v209, s76, v209\n" \
"v_mfma_f32_16x16x32_bf16 a[168:171], a[20:23], a[116:119], a[168:171]\n" \
"buffer_load_dwordx4 v225, s[48:51], s77 offen offset:1024 lds\n" \
"v_mfma_f32_16x16x32_bf16 a[172:175], a[16:19], a[120:123], a[172:175]\n" \
"ds_read_b64_tr_b16 a[84:85], v232 offset:8704\n" \
"ds_read_b64_tr_b16 a[86:87], v232 offset:10752\n" \
"v_mfma_f32_16x16x32_bf16 a[172:175], a[20:23], a[124:127], a[172:175]\n" \
"v_mul_f32 v210, s76, v210\n" \
"v_mul_f32 v211, s76, v211\n" \
"v_mfma_f32_16x16x32_bf16 a[188:191], a[24:27], a[120:123], a[188:191]\n" \
"v_cvt_pk_bf16_f32 v208, v208, v209\n" \
"v_cvt_pk_bf16_f32 v209, v210, v211\n" \
"v_mfma_f32_16x16x32_bf16 a[188:191], a[28:31], a[124:127], a[188:191]\n" \
"buffer_load_dwordx4 v226, s[48:51], s77 offen offset:2048 lds\n" \
"v_mfma_f32_16x16x32_bf16 a[184:187], a[24:27], a[112:115], a[184:187]\n" \
"ds_read_b64_tr_b16 a[88:89], v233 offset:512\n" \
"ds_read_b64_tr_b16 a[90:91], v233 offset:2560\n" \
"v_mfma_f32_16x16x32_bf16 a[184:187], a[28:31], a[116:119], a[184:187]\n" \
"v_mul_f32 v212, s76, v212\n" \
"v_mul_f32 v213, s76, v213\n" \
"v_mfma_f32_16x16x32_bf16 a[180:183], a[24:27], a[104:107], a[180:183]\n" \
"v_mul_f32 v214, s76, v214\n" \
"v_mul_f32 v215, s76, v215\n" \
"v_mfma_f32_16x16x32_bf16 a[180:183], a[28:31], a[108:111], a[180:183]\n" \
"buffer_load_dwordx4 v227, s[48:51], s77 offen offset:3072 lds\n" \
"v_mfma_f32_16x16x32_bf16 a[176:179], a[24:27], a[96:99], a[176:179]\n" \
"ds_read_b64_tr_b16 a[92:93], v233 offset:8704\n" \
"ds_read_b64_tr_b16 a[94:95], v233 offset:10752\n" \
"v_mfma_f32_16x16x32_bf16 a[176:179], a[28:31], a[100:103], a[176:179]\n" \
"s_xor_b32 s77, s77, 0x500\n" \
"v_cvt_pk_bf16_f32 v210, v212, v213\n" \
"v_cvt_pk_bf16_f32 v211, v214, v215\n" \
"v_mfma_f32_16x16x32_bf16 a[192:195], a[32:35], a[96:99], a[192:195]\n" \
"s_add_u32 m0, s79, 32768\n" \
"v_mul_f32 v200, s76, v200\n" \
"v_mul_f32 v201, s76, v201\n" \
"v_mfma_f32_16x16x32_bf16 a[192:195], a[36:39], a[100:103], a[192:195]\n" \
"buffer_load_dwordx4 v228, s[52:55], s78 offen offset:0 lds\n" \
"v_mfma_f32_16x16x32_bf16 a[196:199], a[32:35], a[104:107], a[196:199]\n" \
"ds_read_b128 a[0:3], v250 offset:0\n" \
"ds_read_b128 a[4:7], v251 offset:0\n" \
"v_mfma_f32_16x16x32_bf16 a[196:199], a[36:39], a[108:111], a[196:199]\n" \
"v_mul_f32 v202, s76, v202\n" \
"v_mul_f32 v203, s76, v203\n" \
"v_mfma_f32_16x16x32_bf16 a[200:203], a[32:35], a[112:115], a[200:203]\n" \
"v_cvt_pk_bf16_f32 v200, v200, v201\n" \
"v_cvt_pk_bf16_f32 v201, v202, v203\n" \
"v_mfma_f32_16x16x32_bf16 a[200:203], a[36:39], a[116:119], a[200:203]\n" \
"buffer_load_dwordx4 v229, s[52:55], s78 offen offset:1024 lds\n" \
"v_mfma_f32_16x16x32_bf16 a[204:207], a[32:35], a[120:123], a[204:207]\n" \
"ds_read_b128 a[8:11], v250 offset:2048\n" \
"ds_read_b128 a[12:15], v251 offset:2048\n" \
"v_mfma_f32_16x16x32_bf16 a[204:207], a[36:39], a[124:127], a[204:207]\n" \
"v_mul_f32 v204, s76, v204\n" \
"v_mul_f32 v205, s76, v205\n" \
"v_mfma_f32_16x16x32_bf16 a[220:223], a[40:43], a[120:123], a[220:223]\n" \
"s_xor_b32 s78, s78, 0x500000\n" \
"v_mul_f32 v206, s76, v206\n" \
"v_mul_f32 v207, s76, v207\n" \
"v_mfma_f32_16x16x32_bf16 a[220:223], a[44:47], a[124:127], a[220:223]\n" \
"buffer_load_dwordx4 v228, s[52:55], s89 offen offset:2048 lds\n" \
"v_mfma_f32_16x16x32_bf16 a[216:219], a[40:43], a[112:115], a[216:219]\n" \
"ds_read_b128 a[16:19], v250 offset:4096\n" \
"ds_read_b128 a[20:23], v251 offset:4096\n" \
"v_mfma_f32_16x16x32_bf16 a[216:219], a[44:47], a[116:119], a[216:219]\n" \
"v_cvt_pk_bf16_f32 v202, v204, v205\n" \
"v_cvt_pk_bf16_f32 v203, v206, v207\n" \
"v_mfma_f32_16x16x32_bf16 a[212:215], a[40:43], a[104:107], a[212:215]\n" \
"v_mul_f32 v216, s76, v216\n" \
"v_mul_f32 v217, s76, v217\n" \
"v_mfma_f32_16x16x32_bf16 a[212:215], a[44:47], a[108:111], a[212:215]\n" \
"buffer_load_dwordx4 v229, s[52:55], s89 offen offset:3072 lds\n" \
"v_mfma_f32_16x16x32_bf16 a[208:211], a[40:43], a[96:99], a[208:211]\n" \
"v_mul_f32 v218, s76, v218\n" \
"v_mul_f32 v219, s76, v219\n" \
"v_mfma_f32_16x16x32_bf16 a[208:211], a[44:47], a[100:103], a[208:211]\n" \
"s_xor_b32 s89, s89, 0x500000\n" \
"v_cvt_pk_bf16_f32 v216, v216, v217\n" \
"v_cvt_pk_bf16_f32 v217, v218, v219\n" \
"v_mfma_f32_16x16x32_bf16 a[224:227], a[48:51], a[96:99], a[224:227]\n" \
"s_add_u32 m0, s79, 49152\n" \
"v_mul_f32 v220, s76, v220\n" \
"v_mul_f32 v221, s76, v221\n" \
"v_mfma_f32_16x16x32_bf16 a[224:227], a[52:55], a[100:103], a[224:227]\n" \
"buffer_load_dwordx4 v228, s[52:55], s78 offen offset:0 lds\n" \
"v_mfma_f32_16x16x32_bf16 a[228:231], a[48:51], a[104:107], a[228:231]\n" \
"v_mul_f32 v222, s76, v222\n" \
"v_mul_f32 v223, s76, v223\n" \
"v_mfma_f32_16x16x32_bf16 a[228:231], a[52:55], a[108:111], a[228:231]\n" \
"v_cvt_pk_bf16_f32 v218, v220, v221\n" \
"v_cvt_pk_bf16_f32 v219, v222, v223\n" \
"v_mfma_f32_16x16x32_bf16 a[232:235], a[48:51], a[112:115], a[232:235]\n" \
"v_mul_f32 v144, s76, v144\n" \
"v_mul_f32 v145, s76, v145\n" \
"v_mfma_f32_16x16x32_bf16 a[232:235], a[52:55], a[116:119], a[232:235]\n" \
"buffer_load_dwordx4 v229, s[52:55], s78 offen offset:1024 lds\n" \
"v_mfma_f32_16x16x32_bf16 a[236:239], a[48:51], a[120:123], a[236:239]\n" \
"v_mul_f32 v146, s76, v146\n" \
"v_mul_f32 v147, s76, v147\n" \
"v_mfma_f32_16x16x32_bf16 a[236:239], a[52:55], a[124:127], a[236:239]\n" \
"v_cvt_pk_bf16_f32 v196, v144, v145\n" \
"v_cvt_pk_bf16_f32 v197, v146, v147\n" \
"v_mfma_f32_16x16x32_bf16 a[252:255], a[56:59], a[120:123], a[252:255]\n" \
"s_xor_b32 s78, s78, 0x180000\n" \
"v_mul_f32 v148, s76, v148\n" \
"v_mul_f32 v149, s76, v149\n" \
"v_mfma_f32_16x16x32_bf16 a[252:255], a[60:63], a[124:127], a[252:255]\n" \
"buffer_load_dwordx4 v228, s[52:55], s89 offen offset:2048 lds\n" \
"v_mfma_f32_16x16x32_bf16 a[248:251], a[56:59], a[112:115], a[248:251]\n" \
"v_mul_f32 v150, s76, v150\n" \
"v_mul_f32 v151, s76, v151\n" \
"v_mfma_f32_16x16x32_bf16 a[248:251], a[60:63], a[116:119], a[248:251]\n" \
"v_cvt_pk_bf16_f32 v198, v148, v149\n" \
"v_cvt_pk_bf16_f32 v199, v150, v151\n" \
"v_mfma_f32_16x16x32_bf16 a[244:247], a[56:59], a[104:107], a[244:247]\n" \
"v_mul_f32 v176, s76, v176\n" \
"v_mul_f32 v177, s76, v177\n" \
"v_mfma_f32_16x16x32_bf16 a[244:247], a[60:63], a[108:111], a[244:247]\n" \
"buffer_load_dwordx4 v229, s[52:55], s89 offen offset:3072 lds\n" \
"v_mfma_f32_16x16x32_bf16 a[240:243], a[56:59], a[96:99], a[240:243]\n" \
"s_waitcnt lgkmcnt(0)\n" \
"v_mul_f32 v178, s76, v178\n" \
"v_mul_f32 v179, s76, v179\n" \
"v_mfma_f32_16x16x32_bf16 a[240:243], a[60:63], a[100:103], a[240:243]\n" \
"s_xor_b32 s89, s89, 0x180000\n" \
"v_cvt_pk_bf16_f32 v212, v176, v177\n" \
"v_cvt_pk_bf16_f32 v213, v178, v179\n" \
"v_mfma_f32_16x16x32_bf16 v[64:67], a[64:67], a[0:3], 0\n" \
"ds_read_b128 a[24:27], v250 offset:6144\n" \
"v_mul_f32 v180, s76, v180\n" \
"v_mfma_f32_16x16x32_bf16 v[64:67], a[68:71], a[4:7], v[64:67]\n" \
"s_add_u32 m0, s74, 131072\n" \
"ds_read_b128 a[28:31], v251 offset:6144\n" \
"v_mul_f32 v181, s76, v181\n" \
"v_mfma_f32_16x16x32_bf16 v[68:71], a[72:75], a[0:3], 0\n" \
"ds_read_b64_tr_b16 a[96:97], v232 offset:1024\n" \
"v_mul_f32 v182, s76, v182\n" \
"v_mfma_f32_16x16x32_bf16 v[68:71], a[76:79], a[4:7], v[68:71]\n" \
"buffer_load_dwordx4 v224, s[48:51], s75 offen offset:0 lds\n" \
"v_mfma_f32_16x16x32_bf16 v[72:75], a[80:83], a[0:3], 0\n" \
"ds_read_b64_tr_b16 a[98:99], v232 offset:3072\n" \
"v_mul_f32 v183, s76, v183\n" \
"v_mfma_f32_16x16x32_bf16 v[72:75], a[84:87], a[4:7], v[72:75]\n" \
"ds_read_b64_tr_b16 a[100:101], v232 offset:9216\n" \
"v_cvt_pk_bf16_f32 v214, v180, v181\n" \
"v_mfma_f32_16x16x32_bf16 v[76:79], a[88:91], a[0:3], 0\n" \
"ds_read_b64_tr_b16 a[102:103], v232 offset:11264\n" \
"v_cvt_pk_bf16_f32 v215, v182, v183\n" \
"v_mfma_f32_16x16x32_bf16 v[76:79], a[92:95], a[4:7], v[76:79]\n" \
"buffer_load_dwordx4 v225, s[48:51], s75 offen offset:1024 lds\n" \
"v_mfma_f32_16x16x32_bf16 v[108:111], a[88:91], a[8:11], 0\n" \
"ds_read_b64_tr_b16 a[104:105], v233 offset:1024\n" \
"v_mul_f32 v152, s76, v152\n" \
"v_mfma_f32_16x16x32_bf16 v[108:111], a[92:95], a[12:15], v[108:111]\n" \
"ds_read_b64_tr_b16 a[106:107], v233 offset:3072\n" \
"v_mul_f32 v153, s76, v153\n" \
"v_mfma_f32_16x16x32_bf16 v[104:107], a[80:83], a[8:11], 0\n" \
"ds_read_b64_tr_b16 a[108:109], v233 offset:9216\n" \
"v_mul_f32 v154, s76, v154\n" \
"v_mfma_f32_16x16x32_bf16 v[104:107], a[84:87], a[12:15], v[104:107]\n" \
"buffer_load_dwordx4 v226, s[48:51], s75 offen offset:2048 lds\n" \
"v_mfma_f32_16x16x32_bf16 v[100:103], a[72:75], a[8:11], 0\n" \
"ds_read_b64_tr_b16 a[110:111], v233 offset:11264\n" \
"v_mul_f32 v155, s76, v155\n" \
"v_mfma_f32_16x16x32_bf16 v[100:103], a[76:79], a[12:15], v[100:103]\n" \
"ds_read_b64_tr_b16 a[112:113], v232 offset:1536\n" \
"v_cvt_pk_bf16_f32 v204, v152, v153\n" \
"v_mfma_f32_16x16x32_bf16 v[96:99], a[64:67], a[8:11], 0\n" \
"ds_read_b64_tr_b16 a[114:115], v232 offset:3584\n" \
"v_cvt_pk_bf16_f32 v205, v154, v155\n" \
"v_mfma_f32_16x16x32_bf16 v[96:99], a[68:71], a[12:15], v[96:99]\n" \
"buffer_load_dwordx4 v227, s[48:51], s75 offen offset:3072 lds\n" \
"v_mfma_f32_16x16x32_bf16 v[128:131], a[64:67], a[16:19], 0\n" \
"s_xor_b32 s75, s75, 0x180\n" \
"ds_read_b64_tr_b16 a[116:117], v232 offset:9728\n" \
"v_mul_f32 v156, s76, v156\n" \
"v_mfma_f32_16x16x32_bf16 v[128:131], a[68:71], a[20:23], v[128:131]\n" \
"s_add_u32 m0, s74, 135168\n" \
"ds_read_b64_tr_b16 a[118:119], v232 offset:11776\n" \
"v_mul_f32 v157, s76, v157\n" \
"v_mfma_f32_16x16x32_bf16 v[132:135], a[72:75], a[16:19], 0\n" \
"ds_read_b64_tr_b16 a[120:121], v233 offset:1536\n" \
"v_mul_f32 v158, s76, v158\n" \
"Lpg2209_%=:\n" \
"Lst0x3_%=:\n" \
"s_cbranch_execz Lpg4447_%=\n" \
"v_mfma_f32_16x16x32_bf16 v[132:135], a[76:79], a[20:23], v[132:135]\n" \
"buffer_load_dwordx4 v224, s[48:51], s77 offen offset:0 lds\n" \
"v_mfma_f32_16x16x32_bf16 v[136:139], a[80:83], a[16:19], 0\n" \
"ds_read_b64_tr_b16 a[122:123], v233 offset:3584\n" \
"v_mul_f32 v159, s76, v159\n" \
"v_mfma_f32_16x16x32_bf16 v[136:139], a[84:87], a[20:23], v[136:139]\n" \
"ds_read_b64_tr_b16 a[124:125], v233 offset:9728\n" \
"v_cvt_pk_bf16_f32 v206, v156, v157\n" \
"v_mfma_f32_16x16x32_bf16 v[140:143], a[88:91], a[16:19], 0\n" \
"s_waitcnt lgkmcnt(3)\n" \
"ds_read_b64_tr_b16 a[126:127], v233 offset:11776\n" \
"v_cvt_pk_bf16_f32 v207, v158, v159\n" \
"v_mfma_f32_16x16x32_bf16 v[140:143], a[92:95], a[20:23], v[140:143]\n" \
"buffer_load_dwordx4 v225, s[48:51], s77 offen offset:1024 lds\n" \
"v_mfma_f32_16x16x32_bf16 v[172:175], a[88:91], a[24:27], 0\n" \
"s_add_u32 s93, s87, 8192\n" \
"v_mul_f32 v184, s76, v184\n" \
"v_mul_f32 v185, s76, v185\n" \
"v_mfma_f32_16x16x32_bf16 v[172:175], a[92:95], a[28:31], v[172:175]\n" \
"buffer_store_dwordx4 v[192:195], v236, s[56:59], s93 offen offset:0 sc1\n" \
"v_mfma_f32_16x16x32_bf16 v[168:171], a[80:83], a[24:27], 0\n" \
"v_mul_f32 v186, s76, v186\n" \
"v_mul_f32 v187, s76, v187\n" \
"v_mfma_f32_16x16x32_bf16 v[168:171], a[84:87], a[28:31], v[168:171]\n" \
"buffer_load_dwordx4 v226, s[48:51], s77 offen offset:2048 lds\n" \
"v_mfma_f32_16x16x32_bf16 v[164:167], a[72:75], a[24:27], 0\n" \
"v_cvt_pk_bf16_f32 v220, v184, v185\n" \
"v_cvt_pk_bf16_f32 v221, v186, v187\n" \
"v_mfma_f32_16x16x32_bf16 v[164:167], a[76:79], a[28:31], v[164:167]\n" \
"buffer_load_dwordx4 v227, s[48:51], s77 offen offset:3072 lds\n" \
"v_mfma_f32_16x16x32_bf16 v[160:163], a[64:67], a[24:27], 0\n" \
"v_mul_f32 v188, s76, v188\n" \
"v_mul_f32 v189, s76, v189\n" \
"v_mfma_f32_16x16x32_bf16 v[160:163], a[68:71], a[28:31], v[160:163]\n" \
"s_waitcnt vmcnt(43) lgkmcnt(0)\n" \
"s_barrier\n" \
"v_mfma_f32_16x16x32_bf16 v[80:83], a[96:99], a[0:3], 0\n" \
"s_xor_b32 s77, s77, 0x180\n" \
"ds_read_b64_tr_b16 a[64:65], v232 offset:16384\n" \
"v_mul_f32 v190, s76, v190\n" \
"v_mfma_f32_16x16x32_bf16 v[80:83], a[100:103], a[4:7], v[80:83]\n" \
"ds_read_b64_tr_b16 a[66:67], v232 offset:18432\n" \
"v_mul_f32 v191, s76, v191\n" \
"v_mfma_f32_16x16x32_bf16 v[84:87], a[104:107], a[0:3], 0\n" \
"s_add_u32 m0, s79, 0\n" \
"ds_read_b64_tr_b16 a[68:69], v232 offset:24576\n" \
"v_cvt_pk_bf16_f32 v222, v188, v189\n" \
"v_mfma_f32_16x16x32_bf16 v[84:87], a[108:111], a[4:7], v[84:87]\n" \
"buffer_load_dwordx4 v228, s[52:55], s78 offen offset:0 lds\n" \
"v_mfma_f32_16x16x32_bf16 v[88:91], a[112:115], a[0:3], 0\n" \
"ds_read_b64_tr_b16 a[70:71], v232 offset:26624\n" \
"v_cvt_pk_bf16_f32 v223, v190, v191\n" \
"v_mfma_f32_16x16x32_bf16 v[88:91], a[116:119], a[4:7], v[88:91]\n" \
"ds_read_b64_tr_b16 a[72:73], v233 offset:16384\n" \
"v_accvgpr_read_b32 v243, a224\n" \
"v_mfma_f32_16x16x32_bf16 v[92:95], a[120:123], a[0:3], 0\n" \
"ds_read_b64_tr_b16 a[74:75], v233 offset:18432\n" \
"v_accvgpr_read_b32 v252, a225\n" \
"v_mfma_f32_16x16x32_bf16 v[92:95], a[124:127], a[4:7], v[92:95]\n" \
"buffer_load_dwordx4 v229, s[52:55], s78 offen offset:1024 lds\n" \
"v_mfma_f32_16x16x32_bf16 v[124:127], a[120:123], a[8:11], 0\n" \
"s_waitcnt vmcnt(33)\n" \
"ds_read_b128 a[0:3], v230 offset:0\n" \
"v_accvgpr_read_b32 v253, a226\n" \
"v_mfma_f32_16x16x32_bf16 v[124:127], a[124:127], a[12:15], v[124:127]\n" \
"ds_read_b128 a[4:7], v231 offset:0\n" \
"v_accvgpr_read_b32 v254, a227\n" \
"v_mfma_f32_16x16x32_bf16 v[120:123], a[112:115], a[8:11], 0\n" \
"s_xor_b32 s78, s78, 0x300000\n" \
"ds_read_b64_tr_b16 a[76:77], v233 offset:24576\n" \
"v_mul_f32 v243, s31, v243\n" \
"v_mfma_f32_16x16x32_bf16 v[120:123], a[116:119], a[12:15], v[120:123]\n" \
"buffer_load_dwordx4 v228, s[52:55], s89 offen offset:2048 lds\n" \
"v_mfma_f32_16x16x32_bf16 v[116:119], a[104:107], a[8:11], 0\n" \
"ds_read_b64_tr_b16 a[78:79], v233 offset:26624\n" \
"v_mul_f32 v252, s31, v252\n" \
"v_mfma_f32_16x16x32_bf16 v[116:119], a[108:111], a[12:15], v[116:119]\n" \
"ds_read_b64_tr_b16 a[80:81], v232 offset:16896\n" \
"v_mul_f32 v253, s31, v253\n" \
"v_mfma_f32_16x16x32_bf16 v[112:115], a[96:99], a[8:11], 0\n" \
"ds_read_b64_tr_b16 a[82:83], v232 offset:18944\n" \
"v_mul_f32 v254, s31, v254\n" \
"v_mfma_f32_16x16x32_bf16 v[112:115], a[100:103], a[12:15], v[112:115]\n" \
"buffer_load_dwordx4 v229, s[52:55], s89 offen offset:3072 lds\n" \
"v_mfma_f32_16x16x32_bf16 v[144:147], a[96:99], a[16:19], 0\n" \
"s_waitcnt vmcnt(33)\n" \
"s_xor_b32 s89, s89, 0x300000\n" \
"ds_read_b128 a[8:11], v230 offset:2048\n" \
"v_mfma_f32_16x16x32_bf16 v[144:147], a[100:103], a[20:23], v[144:147]\n" \
"s_add_u32 m0, s74, 65536\n" \
"ds_read_b128 a[12:15], v231 offset:2048\n" \
"v_cvt_pk_bf16_f32 v243, v243, v252\n" \
"v_mfma_f32_16x16x32_bf16 v[148:151], a[104:107], a[16:19], 0\n" \
"s_waitcnt lgkmcnt(6)\n" \
"ds_read_b64_tr_b16 a[84:85], v232 offset:25088\n" \
"v_cvt_pk_bf16_f32 v253, v253, v254\n" \
"v_mfma_f32_16x16x32_bf16 v[148:151], a[108:111], a[20:23], v[148:151]\n" \
"buffer_load_dwordx4 v224, s[48:51], s75 offen offset:0 lds\n" \
"v_mfma_f32_16x16x32_bf16 v[152:155], a[112:115], a[16:19], 0\n" \
"ds_read_b64_tr_b16 a[86:87], v232 offset:27136\n" \
"v_accvgpr_write_b32 a224, v243\n" \
"v_mfma_f32_16x16x32_bf16 v[152:155], a[116:119], a[20:23], v[152:155]\n" \
"ds_read_b64_tr_b16 a[88:89], v233 offset:16896\n" \
"v_accvgpr_write_b32 a225, v253\n" \
"v_mfma_f32_16x16x32_bf16 v[156:159], a[120:123], a[16:19], 0\n" \
"ds_read_b64_tr_b16 a[90:91], v233 offset:18944\n" \
"v_accvgpr_read_b32 v243, a240\n" \
"v_mfma_f32_16x16x32_bf16 v[156:159], a[124:127], a[20:23], v[156:159]\n" \
"buffer_load_dwordx4 v225, s[48:51], s75 offen offset:1024 lds\n" \
"v_mfma_f32_16x16x32_bf16 v[188:191], a[120:123], a[24:27], 0\n" \
"s_waitcnt vmcnt(33)\n" \
"ds_read_b128 a[16:19], v230 offset:4096\n" \
"v_accvgpr_read_b32 v252, a241\n" \
"v_mfma_f32_16x16x32_bf16 v[188:191], a[124:127], a[28:31], v[188:191]\n" \
"ds_read_b128 a[20:23], v231 offset:4096\n" \
"v_accvgpr_read_b32 v253, a242\n" \
"v_mfma_f32_16x16x32_bf16 v[184:187], a[112:115], a[24:27], 0\n" \
"s_waitcnt lgkmcnt(6)\n" \
"ds_read_b64_tr_b16 a[92:93], v233 offset:25088\n" \
"v_accvgpr_read_b32 v254, a243\n" \
"v_mfma_f32_16x16x32_bf16 v[184:187], a[116:119], a[28:31], v[184:187]\n" \
"buffer_load_dwordx4 v226, s[48:51], s75 offen offset:2048 lds\n" \
"v_mfma_f32_16x16x32_bf16 v[180:183], a[104:107], a[24:27], 0\n" \
"ds_read_b64_tr_b16 a[94:95], v233 offset:27136\n" \
"v_mul_f32 v243, s31, v243\n" \
"v_mfma_f32_16x16x32_bf16 v[180:183], a[108:111], a[28:31], v[180:183]\n" \
"v_mul_f32 v252, s31, v252\n" \
"v_mul_f32 v253, s31, v253\n" \
"v_mfma_f32_16x16x32_bf16 v[176:179], a[96:99], a[24:27], 0\n" \
"v_mul_f32 v254, s31, v254\n" \
"v_cvt_pk_bf16_f32 v243, v243, v252\n" \
"v_mfma_f32_16x16x32_bf16 v[176:179], a[100:103], a[28:31], v[176:179]\n" \
"buffer_load_dwordx4 v227, s[48:51], s75 offen offset:3072 lds\n" \
"v_mfma_f32_16x16x32_bf16 v[64:67], a[64:67], a[0:3], v[64:67]\n" \
"s_waitcnt vmcnt(33)\n" \
"s_xor_b32 s75, s75, 0x300\n" \
"ds_read_b128 a[24:27], v230 offset:6144\n" \
"v_mfma_f32_16x16x32_bf16 v[64:67], a[68:71], a[4:7], v[64:67]\n" \
"s_add_u32 m0, s74, 69632\n" \
"ds_read_b128 a[28:31], v231 offset:6144\n" \
"v_cvt_pk_bf16_f32 v253, v253, v254\n" \
"v_mfma_f32_16x16x32_bf16 v[68:71], a[72:75], a[0:3], v[68:71]\n" \
"s_waitcnt lgkmcnt(4)\n" \
"ds_read_b64_tr_b16 a[96:97], v232 offset:17408\n" \
"v_accvgpr_write_b32 a226, v243\n" \
"v_mfma_f32_16x16x32_bf16 v[68:71], a[76:79], a[4:7], v[68:71]\n" \
"buffer_load_dwordx4 v224, s[48:51], s77 offen offset:0 lds\n" \
"v_mfma_f32_16x16x32_bf16 v[72:75], a[80:83], a[0:3], v[72:75]\n" \
"ds_read_b64_tr_b16 a[98:99], v232 offset:19456\n" \
"v_accvgpr_write_b32 a227, v253\n" \
"v_mfma_f32_16x16x32_bf16 v[72:75], a[84:87], a[4:7], v[72:75]\n" \
"s_waitcnt lgkmcnt(3)\n" \
"ds_read_b64_tr_b16 a[100:101], v232 offset:25600\n" \
"v_accvgpr_read_b32 v243, a228\n" \
"v_mfma_f32_16x16x32_bf16 v[76:79], a[88:91], a[0:3], v[76:79]\n" \
"ds_read_b64_tr_b16 a[102:103], v232 offset:27648\n" \
"v_accvgpr_read_b32 v252, a229\n" \
"v_mfma_f32_16x16x32_bf16 v[76:79], a[92:95], a[4:7], v[76:79]\n" \
"buffer_load_dwordx4 v225, s[48:51], s77 offen offset:1024 lds\n" \
"v_mfma_f32_16x16x32_bf16 v[108:111], a[88:91], a[8:11], v[108:111]\n" \
"ds_read_b64_tr_b16 a[104:105], v233 offset:17408\n" \
"v_accvgpr_read_b32 v253, a230\n" \
"v_mfma_f32_16x16x32_bf16 v[108:111], a[92:95], a[12:15], v[108:111]\n" \
"ds_read_b64_tr_b16 a[106:107], v233 offset:19456\n" \
"v_accvgpr_read_b32 v254, a231\n" \
"v_mfma_f32_16x16x32_bf16 v[104:107], a[80:83], a[8:11], v[104:107]\n" \
"s_waitcnt lgkmcnt(6)\n" \
"ds_read_b64_tr_b16 a[108:109], v233 offset:25600\n" \
"v_mul_f32 v243, s31, v243\n" \
"v_mfma_f32_16x16x32_bf16 v[104:107], a[84:87], a[12:15], v[104:107]\n" \
"buffer_load_dwordx4 v226, s[48:51], s77 offen offset:2048 lds\n" \
"v_mfma_f32_16x16x32_bf16 v[100:103], a[72:75], a[8:11], v[100:103]\n" \
"ds_read_b64_tr_b16 a[110:111], v233 offset:27648\n" \
"v_mul_f32 v252, s31, v252\n" \
"v_mfma_f32_16x16x32_bf16 v[100:103], a[76:79], a[12:15], v[100:103]\n" \
"ds_read_b64_tr_b16 a[112:113], v232 offset:17920\n" \
"v_mul_f32 v253, s31, v253\n" \
"v_mfma_f32_16x16x32_bf16 v[96:99], a[64:67], a[8:11], v[96:99]\n" \
"ds_read_b64_tr_b16 a[114:115], v232 offset:19968\n" \
"v_mul_f32 v254, s31, v254\n" \
"v_mfma_f32_16x16x32_bf16 v[96:99], a[68:71], a[12:15], v[96:99]\n" \
"buffer_load_dwordx4 v227, s[48:51], s77 offen offset:3072 lds\n" \
"v_mfma_f32_16x16x32_bf16 v[128:131], a[64:67], a[16:19], v[128:131]\n" \
"ds_read_b64_tr_b16 a[116:117], v232 offset:26112\n" \
"v_cvt_pk_bf16_f32 v243, v243, v252\n" \
"v_mfma_f32_16x16x32_bf16 v[128:131], a[68:71], a[20:23], v[128:131]\n" \
"ds_read_b64_tr_b16 a[118:119], v232 offset:28160\n" \
"v_cvt_pk_bf16_f32 v253, v253, v254\n" \
"v_mfma_f32_16x16x32_bf16 v[132:135], a[72:75], a[16:19], v[132:135]\n" \
"s_xor_b32 s77, s77, 0x300\n" \
"ds_read_b64_tr_b16 a[120:121], v233 offset:17920\n" \
"v_accvgpr_write_b32 a228, v243\n" \
"v_mfma_f32_16x16x32_bf16 v[132:135], a[76:79], a[20:23], v[132:135]\n" \
"v_accvgpr_write_b32 a229, v253\n" \
"v_accvgpr_read_b32 v243, a244\n" \
"v_mfma_f32_16x16x32_bf16 v[136:139], a[80:83], a[16:19], v[136:139]\n" \
"ds_read_b64_tr_b16 a[122:123], v233 offset:19968\n" \
"v_accvgpr_read_b32 v252, a245\n" \
"v_mfma_f32_16x16x32_bf16 v[136:139], a[84:87], a[20:23], v[136:139]\n" \
"ds_read_b64_tr_b16 a[124:125], v233 offset:26112\n" \
"v_accvgpr_read_b32 v253, a246\n" \
"v_mfma_f32_16x16x32_bf16 v[140:143], a[88:91], a[16:19], v[140:143]\n" \
"ds_read_b64_tr_b16 a[126:127], v233 offset:28160\n" \
"v_accvgpr_read_b32 v254, a247\n" \
"v_mfma_f32_16x16x32_bf16 v[140:143], a[92:95], a[20:23], v[140:143]\n" \
"buffer_store_dwordx4 v[208:211], v236, s[56:59], s93 offen offset:256 sc1\n" \
"v_mfma_f32_16x16x32_bf16 v[172:175], a[88:91], a[24:27], v[172:175]\n" \
"v_mul_f32 v243, s31, v243\n" \
"v_mul_f32 v252, s31, v252\n" \
"v_mfma_f32_16x16x32_bf16 v[172:175], a[92:95], a[28:31], v[172:175]\n" \
"v_mul_f32 v253, s31, v253\n" \
"v_mul_f32 v254, s31, v254\n" \
"v_mfma_f32_16x16x32_bf16 v[168:171], a[80:83], a[24:27], v[168:171]\n" \
"v_cvt_pk_bf16_f32 v243, v243, v252\n" \
"v_cvt_pk_bf16_f32 v253, v253, v254\n" \
"v_mfma_f32_16x16x32_bf16 v[168:171], a[84:87], a[28:31], v[168:171]\n" \
"v_accvgpr_write_b32 a230, v243\n" \
"v_accvgpr_write_b32 a231, v253\n" \
"v_mfma_f32_16x16x32_bf16 v[164:167], a[72:75], a[24:27], v[164:167]\n" \
"v_accvgpr_read_b32 v243, a232\n" \
"v_accvgpr_read_b32 v252, a233\n" \
"v_mfma_f32_16x16x32_bf16 v[164:167], a[76:79], a[28:31], v[164:167]\n" \
"v_accvgpr_read_b32 v253, a234\n" \
"v_accvgpr_read_b32 v254, a235\n" \
"v_mfma_f32_16x16x32_bf16 v[160:163], a[64:67], a[24:27], v[160:163]\n" \
"v_mul_f32 v243, s31, v243\n" \
"v_mul_f32 v252, s31, v252\n" \
"v_mfma_f32_16x16x32_bf16 v[160:163], a[68:71], a[28:31], v[160:163]\n" \
"s_waitcnt vmcnt(26) lgkmcnt(0)\n" \
"s_barrier\n" \
"v_mfma_f32_16x16x32_bf16 v[80:83], a[96:99], a[0:3], v[80:83]\n" \
"ds_read_b64_tr_b16 a[64:65], v232 offset:32768\n" \
"v_mul_f32 v253, s31, v253\n" \
"v_mfma_f32_16x16x32_bf16 v[80:83], a[100:103], a[4:7], v[80:83]\n" \
"ds_read_b64_tr_b16 a[66:67], v232 offset:34816\n" \
"v_mul_f32 v254, s31, v254\n" \
"v_mfma_f32_16x16x32_bf16 v[84:87], a[104:107], a[0:3], v[84:87]\n" \
"s_add_u32 m0, s79, 16384\n" \
"ds_read_b64_tr_b16 a[68:69], v232 offset:40960\n" \
"v_cvt_pk_bf16_f32 v243, v243, v252\n" \
"v_mfma_f32_16x16x32_bf16 v[84:87], a[108:111], a[4:7], v[84:87]\n" \
"buffer_load_dwordx4 v228, s[52:55], s78 offen offset:0 lds\n" \
"v_mfma_f32_16x16x32_bf16 v[88:91], a[112:115], a[0:3], v[88:91]\n" \
"ds_read_b64_tr_b16 a[70:71], v232 offset:43008\n" \
"v_cvt_pk_bf16_f32 v253, v253, v254\n" \
"v_mfma_f32_16x16x32_bf16 v[88:91], a[116:119], a[4:7], v[88:91]\n" \
"ds_read_b64_tr_b16 a[72:73], v233 offset:32768\n" \
"v_accvgpr_write_b32 a232, v243\n" \
"v_mfma_f32_16x16x32_bf16 v[92:95], a[120:123], a[0:3], v[92:95]\n" \
"ds_read_b64_tr_b16 a[74:75], v233 offset:34816\n" \
"v_accvgpr_write_b32 a233, v253\n" \
"v_mfma_f32_16x16x32_bf16 v[92:95], a[124:127], a[4:7], v[92:95]\n" \
"buffer_load_dwordx4 v229, s[52:55], s78 offen offset:1024 lds\n" \
"v_mfma_f32_16x16x32_bf16 v[124:127], a[120:123], a[8:11], v[124:127]\n" \
"ds_read_b128 a[0:3], v230 offset:32768\n" \
"v_accvgpr_read_b32 v243, a248\n" \
"v_mfma_f32_16x16x32_bf16 v[124:127], a[124:127], a[12:15], v[124:127]\n" \
"ds_read_b128 a[4:7], v231 offset:32768\n" \
"v_accvgpr_read_b32 v252, a249\n" \
"v_mfma_f32_16x16x32_bf16 v[120:123], a[112:115], a[8:11], v[120:123]\n" \
"s_xor_b32 s78, s78, 0x80000\n" \
"ds_read_b64_tr_b16 a[76:77], v233 offset:40960\n" \
"v_accvgpr_read_b32 v253, a250\n" \
"v_mfma_f32_16x16x32_bf16 v[120:123], a[116:119], a[12:15], v[120:123]\n" \
"buffer_load_dwordx4 v228, s[52:55], s89 offen offset:2048 lds\n" \
"v_mfma_f32_16x16x32_bf16 v[116:119], a[104:107], a[8:11], v[116:119]\n" \
"ds_read_b64_tr_b16 a[78:79], v233 offset:43008\n" \
"v_accvgpr_read_b32 v254, a251\n" \
"v_mfma_f32_16x16x32_bf16 v[116:119], a[108:111], a[12:15], v[116:119]\n" \
"ds_read_b64_tr_b16 a[80:81], v232 offset:33280\n" \
"v_mul_f32 v243, s31, v243\n" \
"v_mfma_f32_16x16x32_bf16 v[112:115], a[96:99], a[8:11], v[112:115]\n" \
"ds_read_b64_tr_b16 a[82:83], v232 offset:35328\n" \
"v_mul_f32 v252, s31, v252\n" \
"v_mfma_f32_16x16x32_bf16 v[112:115], a[100:103], a[12:15], v[112:115]\n" \
"buffer_load_dwordx4 v229, s[52:55], s89 offen offset:3072 lds\n" \
"v_mfma_f32_16x16x32_bf16 v[144:147], a[96:99], a[16:19], v[144:147]\n" \
"s_xor_b32 s89, s89, 0x80000\n" \
"ds_read_b128 a[8:11], v230 offset:34816\n" \
"v_mfma_f32_16x16x32_bf16 v[144:147], a[100:103], a[20:23], v[144:147]\n" \
"s_add_u32 m0, s74, 98304\n" \
"ds_read_b128 a[12:15], v231 offset:34816\n" \
"v_mul_f32 v253, s31, v253\n" \
"v_mfma_f32_16x16x32_bf16 v[148:151], a[104:107], a[16:19], v[148:151]\n" \
"s_waitcnt lgkmcnt(6)\n" \
"ds_read_b64_tr_b16 a[84:85], v232 offset:41472\n" \
"v_mul_f32 v254, s31, v254\n" \
"v_mfma_f32_16x16x32_bf16 v[148:151], a[108:111], a[20:23], v[148:151]\n" \
"buffer_load_dwordx4 v224, s[48:51], s75 offen offset:0 lds\n" \
"v_mfma_f32_16x16x32_bf16 v[152:155], a[112:115], a[16:19], v[152:155]\n" \
"ds_read_b64_tr_b16 a[86:87], v232 offset:43520\n" \
"v_cvt_pk_bf16_f32 v243, v243, v252\n" \
"v_mfma_f32_16x16x32_bf16 v[152:155], a[116:119], a[20:23], v[152:155]\n" \
"ds_read_b64_tr_b16 a[88:89], v233 offset:33280\n" \
"v_cvt_pk_bf16_f32 v253, v253, v254\n" \
"v_mfma_f32_16x16x32_bf16 v[156:159], a[120:123], a[16:19], v[156:159]\n" \
"ds_read_b64_tr_b16 a[90:91], v233 offset:35328\n" \
"v_accvgpr_write_b32 a234, v243\n" \
"v_mfma_f32_16x16x32_bf16 v[156:159], a[124:127], a[20:23], v[156:159]\n" \
"buffer_load_dwordx4 v225, s[48:51], s75 offen offset:1024 lds\n" \
"v_mfma_f32_16x16x32_bf16 v[188:191], a[120:123], a[24:27], v[188:191]\n" \
"ds_read_b128 a[16:19], v230 offset:36864\n" \
"v_accvgpr_write_b32 a235, v253\n" \
"v_mfma_f32_16x16x32_bf16 v[188:191], a[124:127], a[28:31], v[188:191]\n" \
"ds_read_b128 a[20:23], v231 offset:36864\n" \
"v_accvgpr_read_b32 v243, a236\n" \
"v_mfma_f32_16x16x32_bf16 v[184:187], a[112:115], a[24:27], v[184:187]\n" \
"s_waitcnt lgkmcnt(6)\n" \
"ds_read_b64_tr_b16 a[92:93], v233 offset:41472\n" \
"v_accvgpr_read_b32 v252, a237\n" \
"v_mfma_f32_16x16x32_bf16 v[184:187], a[116:119], a[28:31], v[184:187]\n" \
"buffer_load_dwordx4 v226, s[48:51], s75 offen offset:2048 lds\n" \
"v_mfma_f32_16x16x32_bf16 v[180:183], a[104:107], a[24:27], v[180:183]\n" \
"ds_read_b64_tr_b16 a[94:95], v233 offset:43520\n" \
"v_accvgpr_read_b32 v253, a238\n" \
"v_mfma_f32_16x16x32_bf16 v[180:183], a[108:111], a[28:31], v[180:183]\n" \
"v_accvgpr_read_b32 v254, a239\n" \
"v_mul_f32 v243, s31, v243\n" \
"v_mfma_f32_16x16x32_bf16 v[176:179], a[96:99], a[24:27], v[176:179]\n" \
"v_mul_f32 v252, s31, v252\n" \
"v_mul_f32 v253, s31, v253\n" \
"v_mfma_f32_16x16x32_bf16 v[176:179], a[100:103], a[28:31], v[176:179]\n" \
"buffer_load_dwordx4 v227, s[48:51], s75 offen offset:3072 lds\n" \
"v_mfma_f32_16x16x32_bf16 v[64:67], a[64:67], a[0:3], v[64:67]\n" \
"s_xor_b32 s75, s75, 0x80\n" \
"ds_read_b128 a[24:27], v230 offset:38912\n" \
"v_mfma_f32_16x16x32_bf16 v[64:67], a[68:71], a[4:7], v[64:67]\n" \
"s_add_u32 m0, s74, 102400\n" \
"ds_read_b128 a[28:31], v231 offset:38912\n" \
"v_mul_f32 v254, s31, v254\n" \
"v_mfma_f32_16x16x32_bf16 v[68:71], a[72:75], a[0:3], v[68:71]\n" \
"s_waitcnt lgkmcnt(4)\n" \
"ds_read_b64_tr_b16 a[96:97], v232 offset:33792\n" \
"v_cvt_pk_bf16_f32 v243, v243, v252\n" \
"v_mfma_f32_16x16x32_bf16 v[68:71], a[76:79], a[4:7], v[68:71]\n" \
"buffer_load_dwordx4 v224, s[48:51], s77 offen offset:0 lds\n" \
"v_mfma_f32_16x16x32_bf16 v[72:75], a[80:83], a[0:3], v[72:75]\n" \
"ds_read_b64_tr_b16 a[98:99], v232 offset:35840\n" \
"v_cvt_pk_bf16_f32 v253, v253, v254\n" \
"v_mfma_f32_16x16x32_bf16 v[72:75], a[84:87], a[4:7], v[72:75]\n" \
"s_waitcnt lgkmcnt(3)\n" \
"ds_read_b64_tr_b16 a[100:101], v232 offset:41984\n" \
"v_accvgpr_write_b32 a236, v243\n" \
"v_mfma_f32_16x16x32_bf16 v[76:79], a[88:91], a[0:3], v[76:79]\n" \
"ds_read_b64_tr_b16 a[102:103], v232 offset:44032\n" \
"v_accvgpr_write_b32 a237, v253\n" \
"v_mfma_f32_16x16x32_bf16 v[76:79], a[92:95], a[4:7], v[76:79]\n" \
"buffer_load_dwordx4 v225, s[48:51], s77 offen offset:1024 lds\n" \
"v_mfma_f32_16x16x32_bf16 v[108:111], a[88:91], a[8:11], v[108:111]\n" \
"ds_read_b64_tr_b16 a[104:105], v233 offset:33792\n" \
"v_accvgpr_read_b32 v243, a252\n" \
"v_mfma_f32_16x16x32_bf16 v[108:111], a[92:95], a[12:15], v[108:111]\n" \
"ds_read_b64_tr_b16 a[106:107], v233 offset:35840\n" \
"v_accvgpr_read_b32 v252, a253\n" \
"v_mfma_f32_16x16x32_bf16 v[104:107], a[80:83], a[8:11], v[104:107]\n" \
"s_waitcnt lgkmcnt(6)\n" \
"ds_read_b64_tr_b16 a[108:109], v233 offset:41984\n" \
"v_accvgpr_read_b32 v253, a254\n" \
"v_mfma_f32_16x16x32_bf16 v[104:107], a[84:87], a[12:15], v[104:107]\n" \
"buffer_load_dwordx4 v226, s[48:51], s77 offen offset:2048 lds\n" \
"v_mfma_f32_16x16x32_bf16 v[100:103], a[72:75], a[8:11], v[100:103]\n" \
"ds_read_b64_tr_b16 a[110:111], v233 offset:44032\n" \
"v_accvgpr_read_b32 v254, a255\n" \
"v_mfma_f32_16x16x32_bf16 v[100:103], a[76:79], a[12:15], v[100:103]\n" \
"ds_read_b64_tr_b16 a[112:113], v232 offset:34304\n" \
"v_mul_f32 v243, s31, v243\n" \
"v_mfma_f32_16x16x32_bf16 v[96:99], a[64:67], a[8:11], v[96:99]\n" \
"ds_read_b64_tr_b16 a[114:115], v232 offset:36352\n" \
"v_mul_f32 v252, s31, v252\n" \
"v_mfma_f32_16x16x32_bf16 v[96:99], a[68:71], a[12:15], v[96:99]\n" \
"buffer_load_dwordx4 v227, s[48:51], s77 offen offset:3072 lds\n" \
"v_mfma_f32_16x16x32_bf16 v[128:131], a[64:67], a[16:19], v[128:131]\n" \
"ds_read_b64_tr_b16 a[116:117], v232 offset:42496\n" \
"v_mul_f32 v253, s31, v253\n" \
"v_mfma_f32_16x16x32_bf16 v[128:131], a[68:71], a[20:23], v[128:131]\n" \
"ds_read_b64_tr_b16 a[118:119], v232 offset:44544\n" \
"v_mul_f32 v254, s31, v254\n" \
"v_mfma_f32_16x16x32_bf16 v[132:135], a[72:75], a[16:19], v[132:135]\n" \
"s_xor_b32 s77, s77, 0x80\n" \
"ds_read_b64_tr_b16 a[120:121], v233 offset:34304\n" \
"v_cvt_pk_bf16_f32 v243, v243, v252\n" \
"v_mfma_f32_16x16x32_bf16 v[132:135], a[76:79], a[20:23], v[132:135]\n" \
"v_cvt_pk_bf16_f32 v253, v253, v254\n" \
"v_accvgpr_write_b32 a238, v243\n" \
"v_mfma_f32_16x16x32_bf16 v[136:139], a[80:83], a[16:19], v[136:139]\n" \
"ds_read_b64_tr_b16 a[122:123], v233 offset:36352\n" \
"v_accvgpr_write_b32 a239, v253\n" \
"v_mfma_f32_16x16x32_bf16 v[136:139], a[84:87], a[20:23], v[136:139]\n" \
"ds_read_b64_tr_b16 a[124:125], v233 offset:42496\n" \
"v_accvgpr_read_b32 v243, a192\n" \
"v_mfma_f32_16x16x32_bf16 v[140:143], a[88:91], a[16:19], v[140:143]\n" \
"ds_read_b64_tr_b16 a[126:127], v233 offset:44544\n" \
"v_accvgpr_read_b32 v252, a193\n" \
"v_mfma_f32_16x16x32_bf16 v[140:143], a[92:95], a[20:23], v[140:143]\n" \
"buffer_store_dwordx4 v[200:203], v236, s[56:59], s93 offen offset:2048 sc1\n" \
"v_mfma_f32_16x16x32_bf16 v[172:175], a[88:91], a[24:27], v[172:175]\n" \
"v_accvgpr_read_b32 v253, a194\n" \
"v_accvgpr_read_b32 v254, a195\n" \
"v_mfma_f32_16x16x32_bf16 v[172:175], a[92:95], a[28:31], v[172:175]\n" \
"v_mul_f32 v243, s31, v243\n" \
"v_mul_f32 v252, s31, v252\n" \
"v_mfma_f32_16x16x32_bf16 v[168:171], a[80:83], a[24:27], v[168:171]\n" \
"v_mul_f32 v253, s31, v253\n" \
"v_mul_f32 v254, s31, v254\n" \
"v_mfma_f32_16x16x32_bf16 v[168:171], a[84:87], a[28:31], v[168:171]\n" \
"v_cvt_pk_bf16_f32 v243, v243, v252\n" \
"v_cvt_pk_bf16_f32 v253, v253, v254\n" \
"v_mfma_f32_16x16x32_bf16 v[164:167], a[72:75], a[24:27], v[164:167]\n" \
"v_accvgpr_write_b32 a192, v243\n" \
"v_accvgpr_write_b32 a193, v253\n" \
"v_mfma_f32_16x16x32_bf16 v[164:167], a[76:79], a[28:31], v[164:167]\n" \
"v_accvgpr_read_b32 v243, a208\n" \
"v_accvgpr_read_b32 v252, a209\n" \
"v_mfma_f32_16x16x32_bf16 v[160:163], a[64:67], a[24:27], v[160:163]\n" \
"v_accvgpr_read_b32 v253, a210\n" \
"v_accvgpr_read_b32 v254, a211\n" \
"v_mfma_f32_16x16x32_bf16 v[160:163], a[68:71], a[28:31], v[160:163]\n" \
"s_waitcnt vmcnt(35) lgkmcnt(0)\n" \
"s_barrier\n" \
"v_mfma_f32_16x16x32_bf16 v[80:83], a[96:99], a[0:3], v[80:83]\n" \
"ds_read_b64_tr_b16 a[64:65], v232 offset:49152\n" \
"v_mul_f32 v243, s31, v243\n" \
"v_mfma_f32_16x16x32_bf16 v[80:83], a[100:103], a[4:7], v[80:83]\n" \
"ds_read_b64_tr_b16 a[66:67], v232 offset:51200\n" \
"v_mul_f32 v252, s31, v252\n" \
"v_mfma_f32_16x16x32_bf16 v[84:87], a[104:107], a[0:3], v[84:87]\n" \
"s_add_u32 m0, s79, 32768\n" \
"ds_read_b64_tr_b16 a[68:69], v232 offset:57344\n" \
"v_mul_f32 v253, s31, v253\n" \
"v_mfma_f32_16x16x32_bf16 v[84:87], a[108:111], a[4:7], v[84:87]\n" \
"buffer_load_dwordx4 v228, s[52:55], s78 offen offset:0 lds\n" \
"v_mfma_f32_16x16x32_bf16 v[88:91], a[112:115], a[0:3], v[88:91]\n" \
"ds_read_b64_tr_b16 a[70:71], v232 offset:59392\n" \
"v_mul_f32 v254, s31, v254\n" \
"v_mfma_f32_16x16x32_bf16 v[88:91], a[116:119], a[4:7], v[88:91]\n" \
"ds_read_b64_tr_b16 a[72:73], v233 offset:49152\n" \
"v_cvt_pk_bf16_f32 v243, v243, v252\n" \
"v_mfma_f32_16x16x32_bf16 v[92:95], a[120:123], a[0:3], v[92:95]\n" \
"ds_read_b64_tr_b16 a[74:75], v233 offset:51200\n" \
"v_cvt_pk_bf16_f32 v253, v253, v254\n" \
"v_mfma_f32_16x16x32_bf16 v[92:95], a[124:127], a[4:7], v[92:95]\n" \
"buffer_load_dwordx4 v229, s[52:55], s78 offen offset:1024 lds\n" \
"v_mfma_f32_16x16x32_bf16 v[124:127], a[120:123], a[8:11], v[124:127]\n" \
"s_waitcnt vmcnt(35)\n" \
"ds_read_b128 a[0:3], v250 offset:0\n" \
"v_accvgpr_write_b32 a194, v243\n" \
"v_mfma_f32_16x16x32_bf16 v[124:127], a[124:127], a[12:15], v[124:127]\n" \
"ds_read_b128 a[4:7], v251 offset:0\n" \
"v_accvgpr_write_b32 a195, v253\n" \
"v_mfma_f32_16x16x32_bf16 v[120:123], a[112:115], a[8:11], v[120:123]\n" \
"s_xor_b32 s78, s78, 0x180000\n" \
"ds_read_b64_tr_b16 a[76:77], v233 offset:57344\n" \
"v_accvgpr_read_b32 v243, a196\n" \
"v_mfma_f32_16x16x32_bf16 v[120:123], a[116:119], a[12:15], v[120:123]\n" \
"buffer_load_dwordx4 v228, s[52:55], s89 offen offset:2048 lds\n" \
"v_mfma_f32_16x16x32_bf16 v[116:119], a[104:107], a[8:11], v[116:119]\n" \
"ds_read_b64_tr_b16 a[78:79], v233 offset:59392\n" \
"v_accvgpr_read_b32 v252, a197\n" \
"v_mfma_f32_16x16x32_bf16 v[116:119], a[108:111], a[12:15], v[116:119]\n" \
"ds_read_b64_tr_b16 a[80:81], v232 offset:49664\n" \
"v_accvgpr_read_b32 v253, a198\n" \
"v_mfma_f32_16x16x32_bf16 v[112:115], a[96:99], a[8:11], v[112:115]\n" \
"ds_read_b64_tr_b16 a[82:83], v232 offset:51712\n" \
"v_accvgpr_read_b32 v254, a199\n" \
"v_mfma_f32_16x16x32_bf16 v[112:115], a[100:103], a[12:15], v[112:115]\n" \
"buffer_load_dwordx4 v229, s[52:55], s89 offen offset:3072 lds\n" \
"v_mfma_f32_16x16x32_bf16 v[144:147], a[96:99], a[16:19], v[144:147]\n" \
"s_waitcnt vmcnt(35)\n" \
"s_xor_b32 s89, s89, 0x180000\n" \
"ds_read_b128 a[8:11], v250 offset:2048\n" \
"v_mfma_f32_16x16x32_bf16 v[144:147], a[100:103], a[20:23], v[144:147]\n" \
"s_add_u32 m0, s74, 131072\n" \
"ds_read_b128 a[12:15], v251 offset:2048\n" \
"v_mul_f32 v243, s31, v243\n" \
"v_mfma_f32_16x16x32_bf16 v[148:151], a[104:107], a[16:19], v[148:151]\n" \
"s_waitcnt lgkmcnt(6)\n" \
"ds_read_b64_tr_b16 a[84:85], v232 offset:57856\n" \
"v_mul_f32 v252, s31, v252\n" \
"v_mfma_f32_16x16x32_bf16 v[148:151], a[108:111], a[20:23], v[148:151]\n" \
"buffer_load_dwordx4 v224, s[48:51], s75 offen offset:0 lds\n" \
"v_mfma_f32_16x16x32_bf16 v[152:155], a[112:115], a[16:19], v[152:155]\n" \
"ds_read_b64_tr_b16 a[86:87], v232 offset:59904\n" \
"v_mul_f32 v253, s31, v253\n" \
"v_mfma_f32_16x16x32_bf16 v[152:155], a[116:119], a[20:23], v[152:155]\n" \
"ds_read_b64_tr_b16 a[88:89], v233 offset:49664\n" \
"v_mul_f32 v254, s31, v254\n" \
"v_mfma_f32_16x16x32_bf16 v[156:159], a[120:123], a[16:19], v[156:159]\n" \
"ds_read_b64_tr_b16 a[90:91], v233 offset:51712\n" \
"v_cvt_pk_bf16_f32 v243, v243, v252\n" \
"v_mfma_f32_16x16x32_bf16 v[156:159], a[124:127], a[20:23], v[156:159]\n" \
"buffer_load_dwordx4 v225, s[48:51], s75 offen offset:1024 lds\n" \
"v_mfma_f32_16x16x32_bf16 v[188:191], a[120:123], a[24:27], v[188:191]\n" \
"s_waitcnt vmcnt(35)\n" \
"ds_read_b128 a[16:19], v250 offset:4096\n" \
"v_cvt_pk_bf16_f32 v253, v253, v254\n" \
"v_mfma_f32_16x16x32_bf16 v[188:191], a[124:127], a[28:31], v[188:191]\n" \
"ds_read_b128 a[20:23], v251 offset:4096\n" \
"v_accvgpr_write_b32 a196, v243\n" \
"v_mfma_f32_16x16x32_bf16 v[184:187], a[112:115], a[24:27], v[184:187]\n" \
"s_waitcnt lgkmcnt(6)\n" \
"ds_read_b64_tr_b16 a[92:93], v233 offset:57856\n" \
"v_accvgpr_write_b32 a197, v253\n" \
"v_mfma_f32_16x16x32_bf16 v[184:187], a[116:119], a[28:31], v[184:187]\n" \
"buffer_load_dwordx4 v226, s[48:51], s75 offen offset:2048 lds\n" \
"v_mfma_f32_16x16x32_bf16 v[180:183], a[104:107], a[24:27], v[180:183]\n" \
"ds_read_b64_tr_b16 a[94:95], v233 offset:59904\n" \
"v_accvgpr_read_b32 v243, a212\n" \
"v_mfma_f32_16x16x32_bf16 v[180:183], a[108:111], a[28:31], v[180:183]\n" \
"v_accvgpr_read_b32 v252, a213\n" \
"v_accvgpr_read_b32 v253, a214\n" \
"v_mfma_f32_16x16x32_bf16 v[176:179], a[96:99], a[24:27], v[176:179]\n" \
"v_accvgpr_read_b32 v254, a215\n" \
"v_mul_f32 v243, s31, v243\n" \
"v_mfma_f32_16x16x32_bf16 v[176:179], a[100:103], a[28:31], v[176:179]\n" \
"buffer_load_dwordx4 v227, s[48:51], s75 offen offset:3072 lds\n" \
"v_mfma_f32_16x16x32_bf16 v[64:67], a[64:67], a[0:3], v[64:67]\n" \
"s_waitcnt vmcnt(34)\n" \
"s_xor_b32 s75, s75, 0x180\n" \
"ds_read_b128 a[24:27], v250 offset:6144\n" \
"v_mfma_f32_16x16x32_bf16 v[64:67], a[68:71], a[4:7], v[64:67]\n" \
"s_add_u32 m0, s74, 135168\n" \
"ds_read_b128 a[28:31], v251 offset:6144\n" \
"v_mul_f32 v252, s31, v252\n" \
"v_mfma_f32_16x16x32_bf16 v[68:71], a[72:75], a[0:3], v[68:71]\n" \
"s_waitcnt lgkmcnt(4)\n" \
"ds_read_b64_tr_b16 a[96:97], v232 offset:50176\n" \
"v_mul_f32 v253, s31, v253\n" \
"v_mfma_f32_16x16x32_bf16 v[68:71], a[76:79], a[4:7], v[68:71]\n" \
"buffer_load_dwordx4 v224, s[48:51], s77 offen offset:0 lds\n" \
"v_mfma_f32_16x16x32_bf16 v[72:75], a[80:83], a[0:3], v[72:75]\n" \
"ds_read_b64_tr_b16 a[98:99], v232 offset:52224\n" \
"v_mul_f32 v254, s31, v254\n" \
"v_mfma_f32_16x16x32_bf16 v[72:75], a[84:87], a[4:7], v[72:75]\n" \
"s_waitcnt lgkmcnt(3)\n" \
"ds_read_b64_tr_b16 a[100:101], v232 offset:58368\n" \
"v_cvt_pk_bf16_f32 v243, v243, v252\n" \
"v_mfma_f32_16x16x32_bf16 v[76:79], a[88:91], a[0:3], v[76:79]\n" \
"ds_read_b64_tr_b16 a[102:103], v232 offset:60416\n" \
"v_cvt_pk_bf16_f32 v253, v253, v254\n" \
"v_mfma_f32_16x16x32_bf16 v[76:79], a[92:95], a[4:7], v[76:79]\n" \
"buffer_load_dwordx4 v225, s[48:51], s77 offen offset:1024 lds\n" \
"v_mfma_f32_16x16x32_bf16 v[108:111], a[88:91], a[8:11], v[108:111]\n" \
"ds_read_b64_tr_b16 a[104:105], v233 offset:50176\n" \
"v_accvgpr_write_b32 a198, v243\n" \
"v_mfma_f32_16x16x32_bf16 v[108:111], a[92:95], a[12:15], v[108:111]\n" \
"ds_read_b64_tr_b16 a[106:107], v233 offset:52224\n" \
"v_accvgpr_write_b32 a199, v253\n" \
"v_mfma_f32_16x16x32_bf16 v[104:107], a[80:83], a[8:11], v[104:107]\n" \
"s_waitcnt lgkmcnt(6)\n" \
"ds_read_b64_tr_b16 a[108:109], v233 offset:58368\n" \
"v_accvgpr_read_b32 v243, a200\n" \
"v_mfma_f32_16x16x32_bf16 v[104:107], a[84:87], a[12:15], v[104:107]\n" \
"buffer_load_dwordx4 v226, s[48:51], s77 offen offset:2048 lds\n" \
"v_mfma_f32_16x16x32_bf16 v[100:103], a[72:75], a[8:11], v[100:103]\n" \
"ds_read_b64_tr_b16 a[110:111], v233 offset:60416\n" \
"v_accvgpr_read_b32 v252, a201\n" \
"Lpg2766_%=:\n" \
"s_cbranch_execz Lpg5012_%=\n" \
"v_mfma_f32_16x16x32_bf16 v[100:103], a[76:79], a[12:15], v[100:103]\n" \
"ds_read_b64_tr_b16 a[112:113], v232 offset:50688\n" \
"v_accvgpr_read_b32 v253, a202\n" \
"v_mfma_f32_16x16x32_bf16 v[96:99], a[64:67], a[8:11], v[96:99]\n" \
"ds_read_b64_tr_b16 a[114:115], v232 offset:52736\n" \
"v_accvgpr_read_b32 v254, a203\n" \
"v_mfma_f32_16x16x32_bf16 v[96:99], a[68:71], a[12:15], v[96:99]\n" \
"buffer_load_dwordx4 v227, s[48:51], s77 offen offset:3072 lds\n" \
"v_mfma_f32_16x16x32_bf16 v[128:131], a[64:67], a[16:19], v[128:131]\n" \
"ds_read_b64_tr_b16 a[116:117], v232 offset:58880\n" \
"v_mul_f32 v243, s31, v243\n" \
"v_mfma_f32_16x16x32_bf16 v[128:131], a[68:71], a[20:23], v[128:131]\n" \
"ds_read_b64_tr_b16 a[118:119], v232 offset:60928\n" \
"v_mul_f32 v252, s31, v252\n" \
"v_mfma_f32_16x16x32_bf16 v[132:135], a[72:75], a[16:19], v[132:135]\n" \
"s_xor_b32 s77, s77, 0x180\n" \
"ds_read_b64_tr_b16 a[120:121], v233 offset:50688\n" \
"v_mul_f32 v253, s31, v253\n" \
"v_mfma_f32_16x16x32_bf16 v[132:135], a[76:79], a[20:23], v[132:135]\n" \
"v_mul_f32 v254, s31, v254\n" \
"v_cvt_pk_bf16_f32 v243, v243, v252\n" \
"v_mfma_f32_16x16x32_bf16 v[136:139], a[80:83], a[16:19], v[136:139]\n" \
"ds_read_b64_tr_b16 a[122:123], v233 offset:52736\n" \
"v_cvt_pk_bf16_f32 v253, v253, v254\n" \
"v_mfma_f32_16x16x32_bf16 v[136:139], a[84:87], a[20:23], v[136:139]\n" \
"ds_read_b64_tr_b16 a[124:125], v233 offset:58880\n" \
"v_accvgpr_write_b32 a200, v243\n" \
"v_mfma_f32_16x16x32_bf16 v[140:143], a[88:91], a[16:19], v[140:143]\n" \
"ds_read_b64_tr_b16 a[126:127], v233 offset:60928\n" \
"v_accvgpr_write_b32 a201, v253\n" \
"v_mfma_f32_16x16x32_bf16 v[140:143], a[92:95], a[20:23], v[140:143]\n" \
"buffer_store_dwordx4 v[216:219], v236, s[56:59], s93 offen offset:2304 sc1\n" \
"v_mfma_f32_16x16x32_bf16 v[172:175], a[88:91], a[24:27], v[172:175]\n" \
"v_accvgpr_read_b32 v243, a216\n" \
"v_accvgpr_read_b32 v252, a217\n" \
"v_mfma_f32_16x16x32_bf16 v[172:175], a[92:95], a[28:31], v[172:175]\n" \
"v_accvgpr_read_b32 v253, a218\n" \
"v_accvgpr_read_b32 v254, a219\n" \
"v_mfma_f32_16x16x32_bf16 v[168:171], a[80:83], a[24:27], v[168:171]\n" \
"v_mul_f32 v243, s31, v243\n" \
"v_mul_f32 v252, s31, v252\n" \
"v_mfma_f32_16x16x32_bf16 v[168:171], a[84:87], a[28:31], v[168:171]\n" \
"v_mul_f32 v253, s31, v253\n" \
"v_mul_f32 v254, s31, v254\n" \
"v_mfma_f32_16x16x32_bf16 v[164:167], a[72:75], a[24:27], v[164:167]\n" \
"v_cvt_pk_bf16_f32 v243, v243, v252\n" \
"v_cvt_pk_bf16_f32 v253, v253, v254\n" \
"v_mfma_f32_16x16x32_bf16 v[164:167], a[76:79], a[28:31], v[164:167]\n" \
"v_accvgpr_write_b32 a202, v243\n" \
"v_accvgpr_write_b32 a203, v253\n" \
"v_mfma_f32_16x16x32_bf16 v[160:163], a[64:67], a[24:27], v[160:163]\n" \
"v_accvgpr_read_b32 v243, a204\n" \
"v_accvgpr_read_b32 v252, a205\n" \
"v_mfma_f32_16x16x32_bf16 v[160:163], a[68:71], a[28:31], v[160:163]\n" \
"s_waitcnt vmcnt(35) lgkmcnt(0)\n" \
"s_barrier\n" \
"v_mfma_f32_16x16x32_bf16 v[80:83], a[96:99], a[0:3], v[80:83]\n" \
"ds_read_b64_tr_b16 a[64:65], v232 offset:0\n" \
"v_accvgpr_read_b32 v253, a206\n" \
"v_mfma_f32_16x16x32_bf16 v[80:83], a[100:103], a[4:7], v[80:83]\n" \
"ds_read_b64_tr_b16 a[66:67], v232 offset:2048\n" \
"v_accvgpr_read_b32 v254, a207\n" \
"v_mfma_f32_16x16x32_bf16 v[84:87], a[104:107], a[0:3], v[84:87]\n" \
"s_add_u32 m0, s79, 49152\n" \
"ds_read_b64_tr_b16 a[68:69], v232 offset:8192\n" \
"v_mul_f32 v243, s31, v243\n" \
"v_mfma_f32_16x16x32_bf16 v[84:87], a[108:111], a[4:7], v[84:87]\n" \
"buffer_load_dwordx4 v228, s[52:55], s78 offen offset:0 lds\n" \
"v_mfma_f32_16x16x32_bf16 v[88:91], a[112:115], a[0:3], v[88:91]\n" \
"ds_read_b64_tr_b16 a[70:71], v232 offset:10240\n" \
"v_mul_f32 v252, s31, v252\n" \
"v_mfma_f32_16x16x32_bf16 v[88:91], a[116:119], a[4:7], v[88:91]\n" \
"ds_read_b64_tr_b16 a[72:73], v233 offset:0\n" \
"v_mul_f32 v253, s31, v253\n" \
"v_mfma_f32_16x16x32_bf16 v[92:95], a[120:123], a[0:3], v[92:95]\n" \
"ds_read_b64_tr_b16 a[74:75], v233 offset:2048\n" \
"v_mul_f32 v254, s31, v254\n" \
"v_mfma_f32_16x16x32_bf16 v[92:95], a[124:127], a[4:7], v[92:95]\n" \
"buffer_load_dwordx4 v229, s[52:55], s78 offen offset:1024 lds\n" \
"v_mfma_f32_16x16x32_bf16 v[124:127], a[120:123], a[8:11], v[124:127]\n" \
"s_waitcnt vmcnt(35)\n" \
"ds_read_b128 a[0:3], v230 offset:0\n" \
"v_cvt_pk_bf16_f32 v243, v243, v252\n" \
"v_mfma_f32_16x16x32_bf16 v[124:127], a[124:127], a[12:15], v[124:127]\n" \
"ds_read_b128 a[4:7], v231 offset:0\n" \
"v_cvt_pk_bf16_f32 v253, v253, v254\n" \
"v_mfma_f32_16x16x32_bf16 v[120:123], a[112:115], a[8:11], v[120:123]\n" \
"s_xor_b32 s78, s78, 0x80000\n" \
"ds_read_b64_tr_b16 a[76:77], v233 offset:8192\n" \
"v_accvgpr_write_b32 a204, v243\n" \
"v_mfma_f32_16x16x32_bf16 v[120:123], a[116:119], a[12:15], v[120:123]\n" \
"buffer_load_dwordx4 v228, s[52:55], s89 offen offset:2048 lds\n" \
"v_mfma_f32_16x16x32_bf16 v[116:119], a[104:107], a[8:11], v[116:119]\n" \
"ds_read_b64_tr_b16 a[78:79], v233 offset:10240\n" \
"v_accvgpr_write_b32 a205, v253\n" \
"v_mfma_f32_16x16x32_bf16 v[116:119], a[108:111], a[12:15], v[116:119]\n" \
"ds_read_b64_tr_b16 a[80:81], v232 offset:512\n" \
"v_accvgpr_read_b32 v243, a220\n" \
"v_mfma_f32_16x16x32_bf16 v[112:115], a[96:99], a[8:11], v[112:115]\n" \
"ds_read_b64_tr_b16 a[82:83], v232 offset:2560\n" \
"v_accvgpr_read_b32 v252, a221\n" \
"v_mfma_f32_16x16x32_bf16 v[112:115], a[100:103], a[12:15], v[112:115]\n" \
"buffer_load_dwordx4 v229, s[52:55], s89 offen offset:3072 lds\n" \
"v_mfma_f32_16x16x32_bf16 v[144:147], a[96:99], a[16:19], v[144:147]\n" \
"s_waitcnt vmcnt(35)\n" \
"s_xor_b32 s89, s89, 0x80000\n" \
"ds_read_b128 a[8:11], v230 offset:2048\n" \
"v_mfma_f32_16x16x32_bf16 v[144:147], a[100:103], a[20:23], v[144:147]\n" \
"s_add_u32 m0, s74, 65536\n" \
"ds_read_b128 a[12:15], v231 offset:2048\n" \
"v_accvgpr_read_b32 v253, a222\n" \
"v_mfma_f32_16x16x32_bf16 v[148:151], a[104:107], a[16:19], v[148:151]\n" \
"s_waitcnt lgkmcnt(6)\n" \
"ds_read_b64_tr_b16 a[84:85], v232 offset:8704\n" \
"v_accvgpr_read_b32 v254, a223\n" \
"v_mfma_f32_16x16x32_bf16 v[148:151], a[108:111], a[20:23], v[148:151]\n" \
"buffer_load_dwordx4 v224, s[48:51], s75 offen offset:0 lds\n" \
"v_mfma_f32_16x16x32_bf16 v[152:155], a[112:115], a[16:19], v[152:155]\n" \
"ds_read_b64_tr_b16 a[86:87], v232 offset:10752\n" \
"v_mul_f32 v243, s31, v243\n" \
"v_mfma_f32_16x16x32_bf16 v[152:155], a[116:119], a[20:23], v[152:155]\n" \
"ds_read_b64_tr_b16 a[88:89], v233 offset:512\n" \
"v_mul_f32 v252, s31, v252\n" \
"v_mfma_f32_16x16x32_bf16 v[156:159], a[120:123], a[16:19], v[156:159]\n" \
"ds_read_b64_tr_b16 a[90:91], v233 offset:2560\n" \
"v_mul_f32 v253, s31, v253\n" \
"v_mfma_f32_16x16x32_bf16 v[156:159], a[124:127], a[20:23], v[156:159]\n" \
"buffer_load_dwordx4 v225, s[48:51], s75 offen offset:1024 lds\n" \
"v_mfma_f32_16x16x32_bf16 v[188:191], a[120:123], a[24:27], v[188:191]\n" \
"s_waitcnt vmcnt(35)\n" \
"ds_read_b128 a[16:19], v230 offset:4096\n" \
"v_mul_f32 v254, s31, v254\n" \
"v_mfma_f32_16x16x32_bf16 v[188:191], a[124:127], a[28:31], v[188:191]\n" \
"ds_read_b128 a[20:23], v231 offset:4096\n" \
"v_cvt_pk_bf16_f32 v243, v243, v252\n" \
"v_mfma_f32_16x16x32_bf16 v[184:187], a[112:115], a[24:27], v[184:187]\n" \
"s_waitcnt lgkmcnt(6)\n" \
"ds_read_b64_tr_b16 a[92:93], v233 offset:8704\n" \
"v_cvt_pk_bf16_f32 v253, v253, v254\n" \
"v_mfma_f32_16x16x32_bf16 v[184:187], a[116:119], a[28:31], v[184:187]\n" \
"buffer_load_dwordx4 v226, s[48:51], s75 offen offset:2048 lds\n" \
"v_mfma_f32_16x16x32_bf16 v[180:183], a[104:107], a[24:27], v[180:183]\n" \
"ds_read_b64_tr_b16 a[94:95], v233 offset:10752\n" \
"v_accvgpr_write_b32 a206, v243\n" \
"v_mfma_f32_16x16x32_bf16 v[180:183], a[108:111], a[28:31], v[180:183]\n" \
"v_accvgpr_write_b32 a207, v253\n" \
"v_mul_f32 v32, s76, v32\n" \
"v_mfma_f32_16x16x32_bf16 v[176:179], a[96:99], a[24:27], v[176:179]\n" \
"v_mul_f32 v33, s76, v33\n" \
"v_mul_f32 v34, s76, v34\n" \
"v_mfma_f32_16x16x32_bf16 v[176:179], a[100:103], a[28:31], v[176:179]\n" \
"buffer_load_dwordx4 v227, s[48:51], s75 offen offset:3072 lds\n" \
"v_mfma_f32_16x16x32_bf16 v[64:67], a[64:67], a[0:3], v[64:67]\n" \
"s_waitcnt vmcnt(35)\n" \
"s_xor_b32 s75, s75, 0x80\n" \
"ds_read_b128 a[24:27], v230 offset:6144\n" \
"v_mfma_f32_16x16x32_bf16 v[64:67], a[68:71], a[4:7], v[64:67]\n" \
"s_add_u32 m0, s74, 69632\n" \
"ds_read_b128 a[28:31], v231 offset:6144\n" \
"v_mul_f32 v35, s76, v35\n" \
"v_mfma_f32_16x16x32_bf16 v[68:71], a[72:75], a[0:3], v[68:71]\n" \
"s_waitcnt lgkmcnt(4)\n" \
"ds_read_b64_tr_b16 a[96:97], v232 offset:1024\n" \
"v_cvt_pk_bf16_f32 v32, v32, v33\n" \
"v_mfma_f32_16x16x32_bf16 v[68:71], a[76:79], a[4:7], v[68:71]\n" \
"buffer_load_dwordx4 v224, s[48:51], s77 offen offset:0 lds\n" \
"v_mfma_f32_16x16x32_bf16 v[72:75], a[80:83], a[0:3], v[72:75]\n" \
"ds_read_b64_tr_b16 a[98:99], v232 offset:3072\n" \
"v_cvt_pk_bf16_f32 v33, v34, v35\n" \
"v_mfma_f32_16x16x32_bf16 v[72:75], a[84:87], a[4:7], v[72:75]\n" \
"s_waitcnt lgkmcnt(3)\n" \
"ds_read_b64_tr_b16 a[100:101], v232 offset:9216\n" \
"v_mul_f32 v36, s76, v36\n" \
"v_mfma_f32_16x16x32_bf16 v[76:79], a[88:91], a[0:3], v[76:79]\n" \
"ds_read_b64_tr_b16 a[102:103], v232 offset:11264\n" \
"v_mul_f32 v37, s76, v37\n" \
"v_mfma_f32_16x16x32_bf16 v[76:79], a[92:95], a[4:7], v[76:79]\n" \
"buffer_load_dwordx4 v225, s[48:51], s77 offen offset:1024 lds\n" \
"v_mfma_f32_16x16x32_bf16 v[108:111], a[88:91], a[8:11], v[108:111]\n" \
"ds_read_b64_tr_b16 a[104:105], v233 offset:1024\n" \
"v_mul_f32 v38, s76, v38\n" \
"v_mfma_f32_16x16x32_bf16 v[108:111], a[92:95], a[12:15], v[108:111]\n" \
"ds_read_b64_tr_b16 a[106:107], v233 offset:3072\n" \
"v_mul_f32 v39, s76, v39\n" \
"v_mfma_f32_16x16x32_bf16 v[104:107], a[80:83], a[8:11], v[104:107]\n" \
"s_waitcnt lgkmcnt(6)\n" \
"ds_read_b64_tr_b16 a[108:109], v233 offset:9216\n" \
"v_cvt_pk_bf16_f32 v34, v36, v37\n" \
"v_mfma_f32_16x16x32_bf16 v[104:107], a[84:87], a[12:15], v[104:107]\n" \
"buffer_load_dwordx4 v226, s[48:51], s77 offen offset:2048 lds\n" \
"v_mfma_f32_16x16x32_bf16 v[100:103], a[72:75], a[8:11], v[100:103]\n" \
"ds_read_b64_tr_b16 a[110:111], v233 offset:11264\n" \
"v_cvt_pk_bf16_f32 v35, v38, v39\n" \
"v_mfma_f32_16x16x32_bf16 v[100:103], a[76:79], a[12:15], v[100:103]\n" \
"ds_read_b64_tr_b16 a[112:113], v232 offset:1536\n" \
"v_mul_f32 v48, s76, v48\n" \
"v_mfma_f32_16x16x32_bf16 v[96:99], a[64:67], a[8:11], v[96:99]\n" \
"ds_read_b64_tr_b16 a[114:115], v232 offset:3584\n" \
"v_mul_f32 v49, s76, v49\n" \
"v_mfma_f32_16x16x32_bf16 v[96:99], a[68:71], a[12:15], v[96:99]\n" \
"buffer_load_dwordx4 v227, s[48:51], s77 offen offset:3072 lds\n" \
"v_mfma_f32_16x16x32_bf16 v[128:131], a[64:67], a[16:19], v[128:131]\n" \
"ds_read_b64_tr_b16 a[116:117], v232 offset:9728\n" \
"v_mul_f32 v50, s76, v50\n" \
"v_mfma_f32_16x16x32_bf16 v[128:131], a[68:71], a[20:23], v[128:131]\n" \
"ds_read_b64_tr_b16 a[118:119], v232 offset:11776\n" \
"v_mul_f32 v51, s76, v51\n" \
"v_mfma_f32_16x16x32_bf16 v[132:135], a[72:75], a[16:19], v[132:135]\n" \
"s_xor_b32 s77, s77, 0x80\n" \
"ds_read_b64_tr_b16 a[120:121], v233 offset:1536\n" \
"v_cvt_pk_bf16_f32 v48, v48, v49\n" \
"v_mfma_f32_16x16x32_bf16 v[132:135], a[76:79], a[20:23], v[132:135]\n" \
"v_cvt_pk_bf16_f32 v49, v50, v51\n" \
"v_mul_f32 v52, s76, v52\n" \
"v_mfma_f32_16x16x32_bf16 v[136:139], a[80:83], a[16:19], v[136:139]\n" \
"ds_read_b64_tr_b16 a[122:123], v233 offset:3584\n" \
"v_mul_f32 v53, s76, v53\n" \
"v_mfma_f32_16x16x32_bf16 v[136:139], a[84:87], a[20:23], v[136:139]\n" \
"ds_read_b64_tr_b16 a[124:125], v233 offset:9728\n" \
"v_mul_f32 v54, s76, v54\n" \
"v_mfma_f32_16x16x32_bf16 v[140:143], a[88:91], a[16:19], v[140:143]\n" \
"s_add_u32 s93, s87, 0\n" \
"ds_read_b64_tr_b16 a[126:127], v233 offset:11776\n" \
"v_mul_f32 v55, s76, v55\n" \
"v_mfma_f32_16x16x32_bf16 v[140:143], a[92:95], a[20:23], v[140:143]\n" \
"buffer_store_dwordx4 v[196:199], v236, s[56:59], s93 offen offset:0 sc1\n" \
"v_mfma_f32_16x16x32_bf16 v[172:175], a[88:91], a[24:27], v[172:175]\n" \
"v_cvt_pk_bf16_f32 v50, v52, v53\n" \
"v_cvt_pk_bf16_f32 v51, v54, v55\n" \
"v_mfma_f32_16x16x32_bf16 v[172:175], a[92:95], a[28:31], v[172:175]\n" \
"v_mul_f32 v40, s76, v40\n" \
"v_mul_f32 v41, s76, v41\n" \
"v_mfma_f32_16x16x32_bf16 v[168:171], a[80:83], a[24:27], v[168:171]\n" \
"v_mul_f32 v42, s76, v42\n" \
"v_mul_f32 v43, s76, v43\n" \
"v_mfma_f32_16x16x32_bf16 v[168:171], a[84:87], a[28:31], v[168:171]\n" \
"v_cvt_pk_bf16_f32 v40, v40, v41\n" \
"v_cvt_pk_bf16_f32 v41, v42, v43\n" \
"v_mfma_f32_16x16x32_bf16 v[164:167], a[72:75], a[24:27], v[164:167]\n" \
"v_mul_f32 v44, s76, v44\n" \
"v_mul_f32 v45, s76, v45\n" \
"v_mfma_f32_16x16x32_bf16 v[164:167], a[76:79], a[28:31], v[164:167]\n" \
"v_mul_f32 v46, s76, v46\n" \
"v_mul_f32 v47, s76, v47\n" \
"v_mfma_f32_16x16x32_bf16 v[160:163], a[64:67], a[24:27], v[160:163]\n" \
"v_cvt_pk_bf16_f32 v42, v44, v45\n" \
"v_cvt_pk_bf16_f32 v43, v46, v47\n" \
"v_mfma_f32_16x16x32_bf16 v[160:163], a[68:71], a[28:31], v[160:163]\n" \
"s_waitcnt vmcnt(35) lgkmcnt(0)\n" \
"s_barrier\n" \
"v_mfma_f32_16x16x32_bf16 v[80:83], a[96:99], a[0:3], v[80:83]\n" \
"ds_read_b64_tr_b16 a[64:65], v232 offset:16384\n" \
"v_mul_f32 v56, s76, v56\n" \
"v_mfma_f32_16x16x32_bf16 v[80:83], a[100:103], a[4:7], v[80:83]\n" \
"ds_read_b64_tr_b16 a[66:67], v232 offset:18432\n" \
"v_mul_f32 v57, s76, v57\n" \
"v_mfma_f32_16x16x32_bf16 v[84:87], a[104:107], a[0:3], v[84:87]\n" \
"s_add_u32 m0, s79, 0\n" \
"ds_read_b64_tr_b16 a[68:69], v232 offset:24576\n" \
"v_mul_f32 v58, s76, v58\n" \
"v_mfma_f32_16x16x32_bf16 v[84:87], a[108:111], a[4:7], v[84:87]\n" \
"buffer_load_dwordx4 v228, s[52:55], s78 offen offset:0 lds\n" \
"v_mfma_f32_16x16x32_bf16 v[88:91], a[112:115], a[0:3], v[88:91]\n" \
"ds_read_b64_tr_b16 a[70:71], v232 offset:26624\n" \
"v_mul_f32 v59, s76, v59\n" \
"v_mfma_f32_16x16x32_bf16 v[88:91], a[116:119], a[4:7], v[88:91]\n" \
"ds_read_b64_tr_b16 a[72:73], v233 offset:16384\n" \
"v_cvt_pk_bf16_f32 v56, v56, v57\n" \
"v_mfma_f32_16x16x32_bf16 v[92:95], a[120:123], a[0:3], v[92:95]\n" \
"ds_read_b64_tr_b16 a[74:75], v233 offset:18432\n" \
"v_cvt_pk_bf16_f32 v57, v58, v59\n" \
"v_mfma_f32_16x16x32_bf16 v[92:95], a[124:127], a[4:7], v[92:95]\n" \
"buffer_load_dwordx4 v229, s[52:55], s78 offen offset:1024 lds\n" \
"v_mfma_f32_16x16x32_bf16 v[124:127], a[120:123], a[8:11], v[124:127]\n" \
"s_waitcnt vmcnt(35)\n" \
"ds_read_b128 a[0:3], v230 offset:32768\n" \
"v_mul_f32 v60, s76, v60\n" \
"v_mfma_f32_16x16x32_bf16 v[124:127], a[124:127], a[12:15], v[124:127]\n" \
"ds_read_b128 a[4:7], v231 offset:32768\n" \
"v_mul_f32 v61, s76, v61\n" \
"v_mfma_f32_16x16x32_bf16 v[120:123], a[112:115], a[8:11], v[120:123]\n" \
"s_xor_b32 s78, s78, 0x780000\n" \
"ds_read_b64_tr_b16 a[76:77], v233 offset:24576\n" \
"v_mul_f32 v62, s76, v62\n" \
"v_mfma_f32_16x16x32_bf16 v[120:123], a[116:119], a[12:15], v[120:123]\n" \
"buffer_load_dwordx4 v228, s[52:55], s89 offen offset:2048 lds\n" \
"v_mfma_f32_16x16x32_bf16 v[116:119], a[104:107], a[8:11], v[116:119]\n" \
"ds_read_b64_tr_b16 a[78:79], v233 offset:26624\n" \
"v_mul_f32 v63, s76, v63\n" \
"v_mfma_f32_16x16x32_bf16 v[116:119], a[108:111], a[12:15], v[116:119]\n" \
"ds_read_b64_tr_b16 a[80:81], v232 offset:16896\n" \
"v_cvt_pk_bf16_f32 v58, v60, v61\n" \
"v_mfma_f32_16x16x32_bf16 v[112:115], a[96:99], a[8:11], v[112:115]\n" \
"ds_read_b64_tr_b16 a[82:83], v232 offset:18944\n" \
"v_cvt_pk_bf16_f32 v59, v62, v63\n" \
"v_mfma_f32_16x16x32_bf16 v[112:115], a[100:103], a[12:15], v[112:115]\n" \
"buffer_load_dwordx4 v229, s[52:55], s89 offen offset:3072 lds\n" \
"v_mfma_f32_16x16x32_bf16 v[144:147], a[96:99], a[16:19], v[144:147]\n" \
"s_waitcnt vmcnt(35)\n" \
"s_xor_b32 s89, s89, 0x780000\n" \
"ds_read_b128 a[8:11], v230 offset:34816\n" \
"v_mfma_f32_16x16x32_bf16 v[144:147], a[100:103], a[20:23], v[144:147]\n" \
"s_add_u32 m0, s74, 98304\n" \
"ds_read_b128 a[12:15], v231 offset:34816\n" \
"v_mul_f32 v0, s76, v0\n" \
"v_mfma_f32_16x16x32_bf16 v[148:151], a[104:107], a[16:19], v[148:151]\n" \
"s_waitcnt lgkmcnt(6)\n" \
"ds_read_b64_tr_b16 a[84:85], v232 offset:25088\n" \
"v_mul_f32 v1, s76, v1\n" \
"v_mfma_f32_16x16x32_bf16 v[148:151], a[108:111], a[20:23], v[148:151]\n" \
"buffer_load_dwordx4 v224, s[48:51], s75 offen offset:0 lds\n" \
"v_mfma_f32_16x16x32_bf16 v[152:155], a[112:115], a[16:19], v[152:155]\n" \
"ds_read_b64_tr_b16 a[86:87], v232 offset:27136\n" \
"v_mul_f32 v2, s76, v2\n" \
"v_mfma_f32_16x16x32_bf16 v[152:155], a[116:119], a[20:23], v[152:155]\n" \
"ds_read_b64_tr_b16 a[88:89], v233 offset:16896\n" \
"v_mul_f32 v3, s76, v3\n" \
"v_mfma_f32_16x16x32_bf16 v[156:159], a[120:123], a[16:19], v[156:159]\n" \
"ds_read_b64_tr_b16 a[90:91], v233 offset:18944\n" \
"v_cvt_pk_bf16_f32 v36, v0, v1\n" \
"v_mfma_f32_16x16x32_bf16 v[156:159], a[124:127], a[20:23], v[156:159]\n" \
"buffer_load_dwordx4 v225, s[48:51], s75 offen offset:1024 lds\n" \
"v_mfma_f32_16x16x32_bf16 v[188:191], a[120:123], a[24:27], v[188:191]\n" \
"s_waitcnt vmcnt(35)\n" \
"ds_read_b128 a[16:19], v230 offset:36864\n" \
"v_cvt_pk_bf16_f32 v37, v2, v3\n" \
"v_mfma_f32_16x16x32_bf16 v[188:191], a[124:127], a[28:31], v[188:191]\n" \
"ds_read_b128 a[20:23], v231 offset:36864\n" \
"v_mul_f32 v4, s76, v4\n" \
"v_mfma_f32_16x16x32_bf16 v[184:187], a[112:115], a[24:27], v[184:187]\n" \
"s_waitcnt lgkmcnt(6)\n" \
"ds_read_b64_tr_b16 a[92:93], v233 offset:25088\n" \
"v_mul_f32 v5, s76, v5\n" \
"v_mfma_f32_16x16x32_bf16 v[184:187], a[116:119], a[28:31], v[184:187]\n" \
"buffer_load_dwordx4 v226, s[48:51], s75 offen offset:2048 lds\n" \
"v_mfma_f32_16x16x32_bf16 v[180:183], a[104:107], a[24:27], v[180:183]\n" \
"ds_read_b64_tr_b16 a[94:95], v233 offset:27136\n" \
"v_mul_f32 v6, s76, v6\n" \
"v_mfma_f32_16x16x32_bf16 v[180:183], a[108:111], a[28:31], v[180:183]\n" \
"v_mul_f32 v7, s76, v7\n" \
"v_cvt_pk_bf16_f32 v38, v4, v5\n" \
"v_mfma_f32_16x16x32_bf16 v[176:179], a[96:99], a[24:27], v[176:179]\n" \
"v_cvt_pk_bf16_f32 v39, v6, v7\n" \
"v_mul_f32 v16, s76, v16\n" \
"v_mfma_f32_16x16x32_bf16 v[176:179], a[100:103], a[28:31], v[176:179]\n" \
"buffer_load_dwordx4 v227, s[48:51], s75 offen offset:3072 lds\n" \
"v_mfma_f32_16x16x32_bf16 v[64:67], a[64:67], a[0:3], v[64:67]\n" \
"s_waitcnt vmcnt(35)\n" \
"s_xor_b32 s75, s75, 0x780\n" \
"ds_read_b128 a[24:27], v230 offset:38912\n" \
"v_mfma_f32_16x16x32_bf16 v[64:67], a[68:71], a[4:7], v[64:67]\n" \
"s_add_u32 m0, s74, 102400\n" \
"ds_read_b128 a[28:31], v231 offset:38912\n" \
"v_mul_f32 v17, s76, v17\n" \
"v_mfma_f32_16x16x32_bf16 v[68:71], a[72:75], a[0:3], v[68:71]\n" \
"s_waitcnt lgkmcnt(4)\n" \
"ds_read_b64_tr_b16 a[96:97], v232 offset:17408\n" \
"v_mul_f32 v18, s76, v18\n" \
"v_mfma_f32_16x16x32_bf16 v[68:71], a[76:79], a[4:7], v[68:71]\n" \
"buffer_load_dwordx4 v224, s[48:51], s77 offen offset:0 lds\n" \
"v_mfma_f32_16x16x32_bf16 v[72:75], a[80:83], a[0:3], v[72:75]\n" \
"ds_read_b64_tr_b16 a[98:99], v232 offset:19456\n" \
"v_mul_f32 v19, s76, v19\n" \
"v_mfma_f32_16x16x32_bf16 v[72:75], a[84:87], a[4:7], v[72:75]\n" \
"s_waitcnt lgkmcnt(3)\n" \
"ds_read_b64_tr_b16 a[100:101], v232 offset:25600\n" \
"v_cvt_pk_bf16_f32 v16, v16, v17\n" \
"v_mfma_f32_16x16x32_bf16 v[76:79], a[88:91], a[0:3], v[76:79]\n" \
"ds_read_b64_tr_b16 a[102:103], v232 offset:27648\n" \
"v_cvt_pk_bf16_f32 v17, v18, v19\n" \
"v_mfma_f32_16x16x32_bf16 v[76:79], a[92:95], a[4:7], v[76:79]\n" \
"buffer_load_dwordx4 v225, s[48:51], s77 offen offset:1024 lds\n" \
"v_mfma_f32_16x16x32_bf16 v[108:111], a[88:91], a[8:11], v[108:111]\n" \
"ds_read_b64_tr_b16 a[104:105], v233 offset:17408\n" \
"v_mul_f32 v20, s76, v20\n" \
"v_mfma_f32_16x16x32_bf16 v[108:111], a[92:95], a[12:15], v[108:111]\n" \
"ds_read_b64_tr_b16 a[106:107], v233 offset:19456\n" \
"v_mul_f32 v21, s76, v21\n" \
"v_mfma_f32_16x16x32_bf16 v[104:107], a[80:83], a[8:11], v[104:107]\n" \
"s_waitcnt lgkmcnt(6)\n" \
"ds_read_b64_tr_b16 a[108:109], v233 offset:25600\n" \
"v_mul_f32 v22, s76, v22\n" \
"v_mfma_f32_16x16x32_bf16 v[104:107], a[84:87], a[12:15], v[104:107]\n" \
"buffer_load_dwordx4 v226, s[48:51], s77 offen offset:2048 lds\n" \
"v_mfma_f32_16x16x32_bf16 v[100:103], a[72:75], a[8:11], v[100:103]\n" \
"ds_read_b64_tr_b16 a[110:111], v233 offset:27648\n" \
"v_mul_f32 v23, s76, v23\n" \
"v_mfma_f32_16x16x32_bf16 v[100:103], a[76:79], a[12:15], v[100:103]\n" \
"ds_read_b64_tr_b16 a[112:113], v232 offset:17920\n" \
"v_cvt_pk_bf16_f32 v18, v20, v21\n" \
"v_mfma_f32_16x16x32_bf16 v[96:99], a[64:67], a[8:11], v[96:99]\n" \
"ds_read_b64_tr_b16 a[114:115], v232 offset:19968\n" \
"v_cvt_pk_bf16_f32 v19, v22, v23\n" \
"v_mfma_f32_16x16x32_bf16 v[96:99], a[68:71], a[12:15], v[96:99]\n" \
"buffer_load_dwordx4 v227, s[48:51], s77 offen offset:3072 lds\n" \
"v_mfma_f32_16x16x32_bf16 v[128:131], a[64:67], a[16:19], v[128:131]\n" \
"ds_read_b64_tr_b16 a[116:117], v232 offset:26112\n" \
"v_mul_f32 v8, s76, v8\n" \
"v_mfma_f32_16x16x32_bf16 v[128:131], a[68:71], a[20:23], v[128:131]\n" \
"ds_read_b64_tr_b16 a[118:119], v232 offset:28160\n" \
"v_mul_f32 v9, s76, v9\n" \
"v_mfma_f32_16x16x32_bf16 v[132:135], a[72:75], a[16:19], v[132:135]\n" \
"s_xor_b32 s77, s77, 0x780\n" \
"ds_read_b64_tr_b16 a[120:121], v233 offset:17920\n" \
"v_mul_f32 v10, s76, v10\n" \
"v_mfma_f32_16x16x32_bf16 v[132:135], a[76:79], a[20:23], v[132:135]\n" \
"v_mul_f32 v11, s76, v11\n" \
"v_cvt_pk_bf16_f32 v52, v8, v9\n" \
"v_mfma_f32_16x16x32_bf16 v[136:139], a[80:83], a[16:19], v[136:139]\n" \
"ds_read_b64_tr_b16 a[122:123], v233 offset:19968\n" \
"v_cvt_pk_bf16_f32 v53, v10, v11\n" \
"v_mfma_f32_16x16x32_bf16 v[136:139], a[84:87], a[20:23], v[136:139]\n" \
"ds_read_b64_tr_b16 a[124:125], v233 offset:26112\n" \
"v_mul_f32 v12, s76, v12\n" \
"v_mfma_f32_16x16x32_bf16 v[140:143], a[88:91], a[16:19], v[140:143]\n" \
"ds_read_b64_tr_b16 a[126:127], v233 offset:28160\n" \
"v_mul_f32 v13, s76, v13\n" \
"v_mfma_f32_16x16x32_bf16 v[140:143], a[92:95], a[20:23], v[140:143]\n" \
"buffer_store_dwordx4 v[212:215], v236, s[56:59], s93 offen offset:256 sc1\n" \
"v_mfma_f32_16x16x32_bf16 v[172:175], a[88:91], a[24:27], v[172:175]\n" \
"v_mul_f32 v14, s76, v14\n" \
"v_mul_f32 v15, s76, v15\n" \
"v_mfma_f32_16x16x32_bf16 v[172:175], a[92:95], a[28:31], v[172:175]\n" \
"v_cvt_pk_bf16_f32 v54, v12, v13\n" \
"v_cvt_pk_bf16_f32 v55, v14, v15\n" \
"v_mfma_f32_16x16x32_bf16 v[168:171], a[80:83], a[24:27], v[168:171]\n" \
"v_mul_f32 v24, s76, v24\n" \
"v_mul_f32 v25, s76, v25\n" \
"v_mfma_f32_16x16x32_bf16 v[168:171], a[84:87], a[28:31], v[168:171]\n" \
"v_mul_f32 v26, s76, v26\n" \
"v_mul_f32 v27, s76, v27\n" \
"v_mfma_f32_16x16x32_bf16 v[164:167], a[72:75], a[24:27], v[164:167]\n" \
"v_cvt_pk_bf16_f32 v24, v24, v25\n" \
"v_cvt_pk_bf16_f32 v25, v26, v27\n" \
"v_mfma_f32_16x16x32_bf16 v[164:167], a[76:79], a[28:31], v[164:167]\n" \
"v_mul_f32 v28, s76, v28\n" \
"v_mul_f32 v29, s76, v29\n" \
"v_mfma_f32_16x16x32_bf16 v[160:163], a[64:67], a[24:27], v[160:163]\n" \
"v_mul_f32 v30, s76, v30\n" \
"v_mul_f32 v31, s76, v31\n" \
"v_mfma_f32_16x16x32_bf16 v[160:163], a[68:71], a[28:31], v[160:163]\n" \
"s_waitcnt vmcnt(35) lgkmcnt(0)\n" \
"s_barrier\n" \
"v_mfma_f32_16x16x32_bf16 v[80:83], a[96:99], a[0:3], v[80:83]\n" \
"ds_read_b64_tr_b16 a[64:65], v232 offset:32768\n" \
"v_cvt_pk_bf16_f32 v26, v28, v29\n" \
"v_mfma_f32_16x16x32_bf16 v[80:83], a[100:103], a[4:7], v[80:83]\n" \
"ds_read_b64_tr_b16 a[66:67], v232 offset:34816\n" \
"v_cvt_pk_bf16_f32 v27, v30, v31\n" \
"v_mfma_f32_16x16x32_bf16 v[84:87], a[104:107], a[0:3], v[84:87]\n" \
"s_add_u32 m0, s79, 16384\n" \
"ds_read_b64_tr_b16 a[68:69], v232 offset:40960\n" \
"v_accvgpr_read_b32 v243, a160\n" \
"v_mfma_f32_16x16x32_bf16 v[84:87], a[108:111], a[4:7], v[84:87]\n" \
"buffer_load_dwordx4 v228, s[52:55], s78 offen offset:0 lds\n" \
"v_mfma_f32_16x16x32_bf16 v[88:91], a[112:115], a[0:3], v[88:91]\n" \
"ds_read_b64_tr_b16 a[70:71], v232 offset:43008\n" \
"v_accvgpr_read_b32 v252, a161\n" \
"v_mfma_f32_16x16x32_bf16 v[88:91], a[116:119], a[4:7], v[88:91]\n" \
"ds_read_b64_tr_b16 a[72:73], v233 offset:32768\n" \
"v_accvgpr_read_b32 v253, a162\n" \
"v_mfma_f32_16x16x32_bf16 v[92:95], a[120:123], a[0:3], v[92:95]\n" \
"ds_read_b64_tr_b16 a[74:75], v233 offset:34816\n" \
"v_accvgpr_read_b32 v254, a163\n" \
"v_mfma_f32_16x16x32_bf16 v[92:95], a[124:127], a[4:7], v[92:95]\n" \
"buffer_load_dwordx4 v229, s[52:55], s78 offen offset:1024 lds\n" \
"v_mfma_f32_16x16x32_bf16 v[124:127], a[120:123], a[8:11], v[124:127]\n" \
"s_waitcnt vmcnt(35)\n" \
"ds_read_b128 a[0:3], v250 offset:0\n" \
"v_mul_f32 v243, s31, v243\n" \
"v_mfma_f32_16x16x32_bf16 v[124:127], a[124:127], a[12:15], v[124:127]\n" \
"ds_read_b128 a[4:7], v251 offset:0\n" \
"v_mul_f32 v252, s31, v252\n" \
"v_mfma_f32_16x16x32_bf16 v[120:123], a[112:115], a[8:11], v[120:123]\n" \
"s_xor_b32 s78, s78, 0x80000\n" \
"ds_read_b64_tr_b16 a[76:77], v233 offset:40960\n" \
"v_mul_f32 v253, s31, v253\n" \
"v_mfma_f32_16x16x32_bf16 v[120:123], a[116:119], a[12:15], v[120:123]\n" \
"buffer_load_dwordx4 v228, s[52:55], s89 offen offset:2048 lds\n" \
"v_mfma_f32_16x16x32_bf16 v[116:119], a[104:107], a[8:11], v[116:119]\n" \
"ds_read_b64_tr_b16 a[78:79], v233 offset:43008\n" \
"v_mul_f32 v254, s31, v254\n" \
"v_mfma_f32_16x16x32_bf16 v[116:119], a[108:111], a[12:15], v[116:119]\n" \
"ds_read_b64_tr_b16 a[80:81], v232 offset:33280\n" \
"v_cvt_pk_bf16_f32 v44, v243, v252\n" \
"v_mfma_f32_16x16x32_bf16 v[112:115], a[96:99], a[8:11], v[112:115]\n" \
"ds_read_b64_tr_b16 a[82:83], v232 offset:35328\n" \
"v_cvt_pk_bf16_f32 v45, v253, v254\n" \
"v_mfma_f32_16x16x32_bf16 v[112:115], a[100:103], a[12:15], v[112:115]\n" \
"buffer_load_dwordx4 v229, s[52:55], s89 offen offset:3072 lds\n" \
"v_mfma_f32_16x16x32_bf16 v[144:147], a[96:99], a[16:19], v[144:147]\n" \
"s_waitcnt vmcnt(35)\n" \
"s_xor_b32 s89, s89, 0x80000\n" \
"ds_read_b128 a[8:11], v250 offset:2048\n" \
"v_mfma_f32_16x16x32_bf16 v[144:147], a[100:103], a[20:23], v[144:147]\n" \
"s_add_u32 m0, s74, 131072\n" \
"ds_read_b128 a[12:15], v251 offset:2048\n" \
"v_accvgpr_read_b32 v243, a176\n" \
"v_mfma_f32_16x16x32_bf16 v[148:151], a[104:107], a[16:19], v[148:151]\n" \
"s_waitcnt lgkmcnt(6)\n" \
"ds_read_b64_tr_b16 a[84:85], v232 offset:41472\n" \
"v_accvgpr_read_b32 v252, a177\n" \
"v_mfma_f32_16x16x32_bf16 v[148:151], a[108:111], a[20:23], v[148:151]\n" \
"buffer_load_dwordx4 v224, s[48:51], s75 offen offset:0 lds\n" \
"v_mfma_f32_16x16x32_bf16 v[152:155], a[112:115], a[16:19], v[152:155]\n" \
"ds_read_b64_tr_b16 a[86:87], v232 offset:43520\n" \
"v_accvgpr_read_b32 v253, a178\n" \
"v_mfma_f32_16x16x32_bf16 v[152:155], a[116:119], a[20:23], v[152:155]\n" \
"ds_read_b64_tr_b16 a[88:89], v233 offset:33280\n" \
"v_accvgpr_read_b32 v254, a179\n" \
"v_mfma_f32_16x16x32_bf16 v[156:159], a[120:123], a[16:19], v[156:159]\n" \
"ds_read_b64_tr_b16 a[90:91], v233 offset:35328\n" \
"v_mul_f32 v243, s31, v243\n" \
"v_mfma_f32_16x16x32_bf16 v[156:159], a[124:127], a[20:23], v[156:159]\n" \
"buffer_load_dwordx4 v225, s[48:51], s75 offen offset:1024 lds\n" \
"v_mfma_f32_16x16x32_bf16 v[188:191], a[120:123], a[24:27], v[188:191]\n" \
"s_waitcnt vmcnt(35)\n" \
"ds_read_b128 a[16:19], v250 offset:4096\n" \
"v_mul_f32 v252, s31, v252\n" \
"v_mfma_f32_16x16x32_bf16 v[188:191], a[124:127], a[28:31], v[188:191]\n" \
"ds_read_b128 a[20:23], v251 offset:4096\n" \
"v_mul_f32 v253, s31, v253\n" \
"v_mfma_f32_16x16x32_bf16 v[184:187], a[112:115], a[24:27], v[184:187]\n" \
"s_waitcnt lgkmcnt(6)\n" \
"ds_read_b64_tr_b16 a[92:93], v233 offset:41472\n" \
"v_mul_f32 v254, s31, v254\n" \
"v_mfma_f32_16x16x32_bf16 v[184:187], a[116:119], a[28:31], v[184:187]\n" \
"buffer_load_dwordx4 v226, s[48:51], s75 offen offset:2048 lds\n" \
"v_mfma_f32_16x16x32_bf16 v[180:183], a[104:107], a[24:27], v[180:183]\n" \
"ds_read_b64_tr_b16 a[94:95], v233 offset:43520\n" \
"v_cvt_pk_bf16_f32 v46, v243, v252\n" \
"v_mfma_f32_16x16x32_bf16 v[180:183], a[108:111], a[28:31], v[180:183]\n" \
"v_cvt_pk_bf16_f32 v47, v253, v254\n" \
"v_accvgpr_read_b32 v243, a164\n" \
"v_mfma_f32_16x16x32_bf16 v[176:179], a[96:99], a[24:27], v[176:179]\n" \
"v_accvgpr_read_b32 v252, a165\n" \
"v_accvgpr_read_b32 v253, a166\n" \
"v_mfma_f32_16x16x32_bf16 v[176:179], a[100:103], a[28:31], v[176:179]\n" \
"buffer_load_dwordx4 v227, s[48:51], s75 offen offset:3072 lds\n" \
"v_mfma_f32_16x16x32_bf16 v[64:67], a[64:67], a[0:3], v[64:67]\n" \
"s_waitcnt vmcnt(35)\n" \
"s_xor_b32 s75, s75, 0x80\n" \
"ds_read_b128 a[24:27], v250 offset:6144\n" \
"v_mfma_f32_16x16x32_bf16 v[64:67], a[68:71], a[4:7], v[64:67]\n" \
"s_add_u32 m0, s74, 135168\n" \
"ds_read_b128 a[28:31], v251 offset:6144\n" \
"v_accvgpr_read_b32 v254, a167\n" \
"v_mfma_f32_16x16x32_bf16 v[68:71], a[72:75], a[0:3], v[68:71]\n" \
"s_waitcnt lgkmcnt(4)\n" \
"ds_read_b64_tr_b16 a[96:97], v232 offset:33792\n" \
"v_mul_f32 v243, s31, v243\n" \
"v_mfma_f32_16x16x32_bf16 v[68:71], a[76:79], a[4:7], v[68:71]\n" \
"buffer_load_dwordx4 v224, s[48:51], s77 offen offset:0 lds\n" \
"v_mfma_f32_16x16x32_bf16 v[72:75], a[80:83], a[0:3], v[72:75]\n" \
"ds_read_b64_tr_b16 a[98:99], v232 offset:35840\n" \
"v_mul_f32 v252, s31, v252\n" \
"v_mfma_f32_16x16x32_bf16 v[72:75], a[84:87], a[4:7], v[72:75]\n" \
"s_waitcnt lgkmcnt(3)\n" \
"ds_read_b64_tr_b16 a[100:101], v232 offset:41984\n" \
"v_mul_f32 v253, s31, v253\n" \
"v_mfma_f32_16x16x32_bf16 v[76:79], a[88:91], a[0:3], v[76:79]\n" \
"ds_read_b64_tr_b16 a[102:103], v232 offset:44032\n" \
"v_mul_f32 v254, s31, v254\n" \
"v_mfma_f32_16x16x32_bf16 v[76:79], a[92:95], a[4:7], v[76:79]\n" \
"buffer_load_dwordx4 v225, s[48:51], s77 offen offset:1024 lds\n" \
"v_mfma_f32_16x16x32_bf16 v[108:111], a[88:91], a[8:11], v[108:111]\n" \
"ds_read_b64_tr_b16 a[104:105], v233 offset:33792\n" \
"v_cvt_pk_bf16_f32 v60, v243, v252\n" \
"v_mfma_f32_16x16x32_bf16 v[108:111], a[92:95], a[12:15], v[108:111]\n" \
"ds_read_b64_tr_b16 a[106:107], v233 offset:35840\n" \
"v_cvt_pk_bf16_f32 v61, v253, v254\n" \
"v_mfma_f32_16x16x32_bf16 v[104:107], a[80:83], a[8:11], v[104:107]\n" \
"s_waitcnt lgkmcnt(6)\n" \
"ds_read_b64_tr_b16 a[108:109], v233 offset:41984\n" \
"v_accvgpr_read_b32 v243, a180\n" \
"Lpg3342_%=:\n" \
"s_cbranch_execz Lpg5598_%=\n" \
"v_mfma_f32_16x16x32_bf16 v[104:107], a[84:87], a[12:15], v[104:107]\n" \
"buffer_load_dwordx4 v226, s[48:51], s77 offen offset:2048 lds\n" \
"v_mfma_f32_16x16x32_bf16 v[100:103], a[72:75], a[8:11], v[100:103]\n" \
"ds_read_b64_tr_b16 a[110:111], v233 offset:44032\n" \
"v_accvgpr_read_b32 v252, a181\n" \
"v_mfma_f32_16x16x32_bf16 v[100:103], a[76:79], a[12:15], v[100:103]\n" \
"ds_read_b64_tr_b16 a[112:113], v232 offset:34304\n" \
"v_accvgpr_read_b32 v253, a182\n" \
"v_mfma_f32_16x16x32_bf16 v[96:99], a[64:67], a[8:11], v[96:99]\n" \
"ds_read_b64_tr_b16 a[114:115], v232 offset:36352\n" \
"v_accvgpr_read_b32 v254, a183\n" \
"v_mfma_f32_16x16x32_bf16 v[96:99], a[68:71], a[12:15], v[96:99]\n" \
"buffer_load_dwordx4 v227, s[48:51], s77 offen offset:3072 lds\n" \
"v_mfma_f32_16x16x32_bf16 v[128:131], a[64:67], a[16:19], v[128:131]\n" \
"ds_read_b64_tr_b16 a[116:117], v232 offset:42496\n" \
"v_mul_f32 v243, s31, v243\n" \
"v_mfma_f32_16x16x32_bf16 v[128:131], a[68:71], a[20:23], v[128:131]\n" \
"ds_read_b64_tr_b16 a[118:119], v232 offset:44544\n" \
"v_mul_f32 v252, s31, v252\n" \
"v_mfma_f32_16x16x32_bf16 v[132:135], a[72:75], a[16:19], v[132:135]\n" \
"s_xor_b32 s77, s77, 0x80\n" \
"ds_read_b64_tr_b16 a[120:121], v233 offset:34304\n" \
"v_mul_f32 v253, s31, v253\n" \
"v_mfma_f32_16x16x32_bf16 v[132:135], a[76:79], a[20:23], v[132:135]\n" \
"v_mul_f32 v254, s31, v254\n" \
"v_cvt_pk_bf16_f32 v62, v243, v252\n" \
"v_mfma_f32_16x16x32_bf16 v[136:139], a[80:83], a[16:19], v[136:139]\n" \
"ds_read_b64_tr_b16 a[122:123], v233 offset:36352\n" \
"v_cvt_pk_bf16_f32 v63, v253, v254\n" \
"v_mfma_f32_16x16x32_bf16 v[136:139], a[84:87], a[20:23], v[136:139]\n" \
"ds_read_b64_tr_b16 a[124:125], v233 offset:42496\n" \
"v_accvgpr_read_b32 v243, a168\n" \
"v_mfma_f32_16x16x32_bf16 v[140:143], a[88:91], a[16:19], v[140:143]\n" \
"ds_read_b64_tr_b16 a[126:127], v233 offset:44544\n" \
"v_accvgpr_read_b32 v252, a169\n" \
"v_mfma_f32_16x16x32_bf16 v[140:143], a[92:95], a[20:23], v[140:143]\n" \
"buffer_store_dwordx4 v[204:207], v236, s[56:59], s93 offen offset:2048 sc1\n" \
"v_mfma_f32_16x16x32_bf16 v[172:175], a[88:91], a[24:27], v[172:175]\n" \
"v_accvgpr_read_b32 v253, a170\n" \
"v_accvgpr_read_b32 v254, a171\n" \
"v_mfma_f32_16x16x32_bf16 v[172:175], a[92:95], a[28:31], v[172:175]\n" \
"v_mul_f32 v243, s31, v243\n" \
"v_mul_f32 v252, s31, v252\n" \
"v_mfma_f32_16x16x32_bf16 v[168:171], a[80:83], a[24:27], v[168:171]\n" \
"v_mul_f32 v253, s31, v253\n" \
"v_mul_f32 v254, s31, v254\n" \
"v_mfma_f32_16x16x32_bf16 v[168:171], a[84:87], a[28:31], v[168:171]\n" \
"v_cvt_pk_bf16_f32 v20, v243, v252\n" \
"v_cvt_pk_bf16_f32 v21, v253, v254\n" \
"v_mfma_f32_16x16x32_bf16 v[164:167], a[72:75], a[24:27], v[164:167]\n" \
"v_accvgpr_read_b32 v243, a184\n" \
"v_accvgpr_read_b32 v252, a185\n" \
"v_mfma_f32_16x16x32_bf16 v[164:167], a[76:79], a[28:31], v[164:167]\n" \
"v_accvgpr_read_b32 v253, a186\n" \
"v_accvgpr_read_b32 v254, a187\n" \
"v_mfma_f32_16x16x32_bf16 v[160:163], a[64:67], a[24:27], v[160:163]\n" \
"v_mul_f32 v243, s31, v243\n" \
"v_mul_f32 v252, s31, v252\n" \
"v_mfma_f32_16x16x32_bf16 v[160:163], a[68:71], a[28:31], v[160:163]\n" \
"s_waitcnt vmcnt(35) lgkmcnt(0)\n" \
"s_barrier\n" \
"v_mfma_f32_16x16x32_bf16 v[80:83], a[96:99], a[0:3], v[80:83]\n" \
"ds_read_b64_tr_b16 a[64:65], v232 offset:49152\n" \
"v_mul_f32 v253, s31, v253\n" \
"v_mfma_f32_16x16x32_bf16 v[80:83], a[100:103], a[4:7], v[80:83]\n" \
"ds_read_b64_tr_b16 a[66:67], v232 offset:51200\n" \
"v_mul_f32 v254, s31, v254\n" \
"v_mfma_f32_16x16x32_bf16 v[84:87], a[104:107], a[0:3], v[84:87]\n" \
"s_add_u32 m0, s79, 32768\n" \
"ds_read_b64_tr_b16 a[68:69], v232 offset:57344\n" \
"v_cvt_pk_bf16_f32 v22, v243, v252\n" \
"v_mfma_f32_16x16x32_bf16 v[84:87], a[108:111], a[4:7], v[84:87]\n" \
"buffer_load_dwordx4 v228, s[52:55], s78 offen offset:0 lds\n" \
"v_mfma_f32_16x16x32_bf16 v[88:91], a[112:115], a[0:3], v[88:91]\n" \
"ds_read_b64_tr_b16 a[70:71], v232 offset:59392\n" \
"v_cvt_pk_bf16_f32 v23, v253, v254\n" \
"v_mfma_f32_16x16x32_bf16 v[88:91], a[116:119], a[4:7], v[88:91]\n" \
"ds_read_b64_tr_b16 a[72:73], v233 offset:49152\n" \
"v_accvgpr_read_b32 v243, a172\n" \
"v_mfma_f32_16x16x32_bf16 v[92:95], a[120:123], a[0:3], v[92:95]\n" \
"ds_read_b64_tr_b16 a[74:75], v233 offset:51200\n" \
"v_accvgpr_read_b32 v252, a173\n" \
"v_mfma_f32_16x16x32_bf16 v[92:95], a[124:127], a[4:7], v[92:95]\n" \
"buffer_load_dwordx4 v229, s[52:55], s78 offen offset:1024 lds\n" \
"v_mfma_f32_16x16x32_bf16 v[124:127], a[120:123], a[8:11], v[124:127]\n" \
"s_waitcnt vmcnt(35)\n" \
"ds_read_b128 a[0:3], v230 offset:0\n" \
"v_accvgpr_read_b32 v253, a174\n" \
"v_mfma_f32_16x16x32_bf16 v[124:127], a[124:127], a[12:15], v[124:127]\n" \
"ds_read_b128 a[4:7], v231 offset:0\n" \
"v_accvgpr_read_b32 v254, a175\n" \
"v_mfma_f32_16x16x32_bf16 v[120:123], a[112:115], a[8:11], v[120:123]\n" \
"s_xor_b32 s78, s78, 0x180000\n" \
"ds_read_b64_tr_b16 a[76:77], v233 offset:57344\n" \
"v_mul_f32 v243, s31, v243\n" \
"v_mfma_f32_16x16x32_bf16 v[120:123], a[116:119], a[12:15], v[120:123]\n" \
"buffer_load_dwordx4 v228, s[52:55], s89 offen offset:2048 lds\n" \
"v_mfma_f32_16x16x32_bf16 v[116:119], a[104:107], a[8:11], v[116:119]\n" \
"ds_read_b64_tr_b16 a[78:79], v233 offset:59392\n" \
"v_mul_f32 v252, s31, v252\n" \
"v_mfma_f32_16x16x32_bf16 v[116:119], a[108:111], a[12:15], v[116:119]\n" \
"ds_read_b64_tr_b16 a[80:81], v232 offset:49664\n" \
"v_mul_f32 v253, s31, v253\n" \
"v_mfma_f32_16x16x32_bf16 v[112:115], a[96:99], a[8:11], v[112:115]\n" \
"ds_read_b64_tr_b16 a[82:83], v232 offset:51712\n" \
"v_mul_f32 v254, s31, v254\n" \
"v_mfma_f32_16x16x32_bf16 v[112:115], a[100:103], a[12:15], v[112:115]\n" \
"buffer_load_dwordx4 v229, s[52:55], s89 offen offset:3072 lds\n" \
"v_mfma_f32_16x16x32_bf16 v[144:147], a[96:99], a[16:19], v[144:147]\n" \
"s_waitcnt vmcnt(35)\n" \
"s_xor_b32 s89, s89, 0x180000\n" \
"ds_read_b128 a[8:11], v230 offset:2048\n" \
"v_mfma_f32_16x16x32_bf16 v[144:147], a[100:103], a[20:23], v[144:147]\n" \
"s_add_u32 m0, s74, 65536\n" \
"ds_read_b128 a[12:15], v231 offset:2048\n" \
"v_cvt_pk_bf16_f32 v28, v243, v252\n" \
"v_mfma_f32_16x16x32_bf16 v[148:151], a[104:107], a[16:19], v[148:151]\n" \
"s_waitcnt lgkmcnt(6)\n" \
"ds_read_b64_tr_b16 a[84:85], v232 offset:57856\n" \
"v_cvt_pk_bf16_f32 v29, v253, v254\n" \
"v_mfma_f32_16x16x32_bf16 v[148:151], a[108:111], a[20:23], v[148:151]\n" \
"buffer_load_dwordx4 v224, s[48:51], s75 offen offset:0 lds\n" \
"v_mfma_f32_16x16x32_bf16 v[152:155], a[112:115], a[16:19], v[152:155]\n" \
"ds_read_b64_tr_b16 a[86:87], v232 offset:59904\n" \
"v_accvgpr_read_b32 v243, a188\n" \
"v_mfma_f32_16x16x32_bf16 v[152:155], a[116:119], a[20:23], v[152:155]\n" \
"ds_read_b64_tr_b16 a[88:89], v233 offset:49664\n" \
"v_accvgpr_read_b32 v252, a189\n" \
"v_mfma_f32_16x16x32_bf16 v[156:159], a[120:123], a[16:19], v[156:159]\n" \
"ds_read_b64_tr_b16 a[90:91], v233 offset:51712\n" \
"v_accvgpr_read_b32 v253, a190\n" \
"v_mfma_f32_16x16x32_bf16 v[156:159], a[124:127], a[20:23], v[156:159]\n" \
"buffer_load_dwordx4 v225, s[48:51], s75 offen offset:1024 lds\n" \
"v_mfma_f32_16x16x32_bf16 v[188:191], a[120:123], a[24:27], v[188:191]\n" \
"s_waitcnt vmcnt(35)\n" \
"ds_read_b128 a[16:19], v230 offset:4096\n" \
"v_accvgpr_read_b32 v254, a191\n" \
"v_mfma_f32_16x16x32_bf16 v[188:191], a[124:127], a[28:31], v[188:191]\n" \
"ds_read_b128 a[20:23], v231 offset:4096\n" \
"v_mul_f32 v243, s31, v243\n" \
"v_mfma_f32_16x16x32_bf16 v[184:187], a[112:115], a[24:27], v[184:187]\n" \
"s_waitcnt lgkmcnt(6)\n" \
"ds_read_b64_tr_b16 a[92:93], v233 offset:57856\n" \
"v_mul_f32 v252, s31, v252\n" \
"v_mfma_f32_16x16x32_bf16 v[184:187], a[116:119], a[28:31], v[184:187]\n" \
"buffer_load_dwordx4 v226, s[48:51], s75 offen offset:2048 lds\n" \
"v_mfma_f32_16x16x32_bf16 v[180:183], a[104:107], a[24:27], v[180:183]\n" \
"ds_read_b64_tr_b16 a[94:95], v233 offset:59904\n" \
"v_mul_f32 v253, s31, v253\n" \
"v_mfma_f32_16x16x32_bf16 v[180:183], a[108:111], a[28:31], v[180:183]\n" \
"v_mul_f32 v254, s31, v254\n" \
"v_cvt_pk_bf16_f32 v30, v243, v252\n" \
"v_mfma_f32_16x16x32_bf16 v[176:179], a[96:99], a[24:27], v[176:179]\n" \
"v_cvt_pk_bf16_f32 v31, v253, v254\n" \
"v_accvgpr_read_b32 v243, a128\n" \
"v_mfma_f32_16x16x32_bf16 v[176:179], a[100:103], a[28:31], v[176:179]\n" \
"buffer_load_dwordx4 v227, s[48:51], s75 offen offset:3072 lds\n" \
"v_mfma_f32_16x16x32_bf16 v[64:67], a[64:67], a[0:3], v[64:67]\n" \
"s_waitcnt vmcnt(35)\n" \
"s_xor_b32 s75, s75, 0x180\n" \
"ds_read_b128 a[24:27], v230 offset:6144\n" \
"v_mfma_f32_16x16x32_bf16 v[64:67], a[68:71], a[4:7], v[64:67]\n" \
"s_add_u32 m0, s74, 69632\n" \
"ds_read_b128 a[28:31], v231 offset:6144\n" \
"v_accvgpr_read_b32 v252, a129\n" \
"v_mfma_f32_16x16x32_bf16 v[68:71], a[72:75], a[0:3], v[68:71]\n" \
"s_waitcnt lgkmcnt(4)\n" \
"ds_read_b64_tr_b16 a[96:97], v232 offset:50176\n" \
"v_accvgpr_read_b32 v253, a130\n" \
"v_mfma_f32_16x16x32_bf16 v[68:71], a[76:79], a[4:7], v[68:71]\n" \
"buffer_load_dwordx4 v224, s[48:51], s77 offen offset:0 lds\n" \
"v_mfma_f32_16x16x32_bf16 v[72:75], a[80:83], a[0:3], v[72:75]\n" \
"ds_read_b64_tr_b16 a[98:99], v232 offset:52224\n" \
"v_accvgpr_read_b32 v254, a131\n" \
"v_mfma_f32_16x16x32_bf16 v[72:75], a[84:87], a[4:7], v[72:75]\n" \
"s_waitcnt lgkmcnt(3)\n" \
"ds_read_b64_tr_b16 a[100:101], v232 offset:58368\n" \
"v_mul_f32 v243, s31, v243\n" \
"v_mfma_f32_16x16x32_bf16 v[76:79], a[88:91], a[0:3], v[76:79]\n" \
"ds_read_b64_tr_b16 a[102:103], v232 offset:60416\n" \
"v_mul_f32 v252, s31, v252\n" \
"v_mfma_f32_16x16x32_bf16 v[76:79], a[92:95], a[4:7], v[76:79]\n" \
"buffer_load_dwordx4 v225, s[48:51], s77 offen offset:1024 lds\n" \
"v_mfma_f32_16x16x32_bf16 v[108:111], a[88:91], a[8:11], v[108:111]\n" \
"ds_read_b64_tr_b16 a[104:105], v233 offset:50176\n" \
"v_mul_f32 v253, s31, v253\n" \
"v_mfma_f32_16x16x32_bf16 v[108:111], a[92:95], a[12:15], v[108:111]\n" \
"ds_read_b64_tr_b16 a[106:107], v233 offset:52224\n" \
"v_mul_f32 v254, s31, v254\n" \
"v_mfma_f32_16x16x32_bf16 v[104:107], a[80:83], a[8:11], v[104:107]\n" \
"s_waitcnt lgkmcnt(6)\n" \
"ds_read_b64_tr_b16 a[108:109], v233 offset:58368\n" \
"v_cvt_pk_bf16_f32 v0, v243, v252\n" \
"v_mfma_f32_16x16x32_bf16 v[104:107], a[84:87], a[12:15], v[104:107]\n" \
"buffer_load_dwordx4 v226, s[48:51], s77 offen offset:2048 lds\n" \
"v_mfma_f32_16x16x32_bf16 v[100:103], a[72:75], a[8:11], v[100:103]\n" \
"ds_read_b64_tr_b16 a[110:111], v233 offset:60416\n" \
"v_cvt_pk_bf16_f32 v1, v253, v254\n" \
"v_mfma_f32_16x16x32_bf16 v[100:103], a[76:79], a[12:15], v[100:103]\n" \
"ds_read_b64_tr_b16 a[112:113], v232 offset:50688\n" \
"v_accvgpr_read_b32 v243, a144\n" \
"v_mfma_f32_16x16x32_bf16 v[96:99], a[64:67], a[8:11], v[96:99]\n" \
"ds_read_b64_tr_b16 a[114:115], v232 offset:52736\n" \
"v_accvgpr_read_b32 v252, a145\n" \
"v_mfma_f32_16x16x32_bf16 v[96:99], a[68:71], a[12:15], v[96:99]\n" \
"buffer_load_dwordx4 v227, s[48:51], s77 offen offset:3072 lds\n" \
"v_mfma_f32_16x16x32_bf16 v[128:131], a[64:67], a[16:19], v[128:131]\n" \
"ds_read_b64_tr_b16 a[116:117], v232 offset:58880\n" \
"v_accvgpr_read_b32 v253, a146\n" \
"v_mfma_f32_16x16x32_bf16 v[128:131], a[68:71], a[20:23], v[128:131]\n" \
"ds_read_b64_tr_b16 a[118:119], v232 offset:60928\n" \
"v_accvgpr_read_b32 v254, a147\n" \
"v_mfma_f32_16x16x32_bf16 v[132:135], a[72:75], a[16:19], v[132:135]\n" \
"s_xor_b32 s77, s77, 0x180\n" \
"ds_read_b64_tr_b16 a[120:121], v233 offset:50688\n" \
"v_mul_f32 v243, s31, v243\n" \
"v_mfma_f32_16x16x32_bf16 v[132:135], a[76:79], a[20:23], v[132:135]\n" \
"v_mul_f32 v252, s31, v252\n" \
"v_mul_f32 v253, s31, v253\n" \
"v_mfma_f32_16x16x32_bf16 v[136:139], a[80:83], a[16:19], v[136:139]\n" \
"ds_read_b64_tr_b16 a[122:123], v233 offset:52736\n" \
"v_mul_f32 v254, s31, v254\n" \
"v_mfma_f32_16x16x32_bf16 v[136:139], a[84:87], a[20:23], v[136:139]\n" \
"ds_read_b64_tr_b16 a[124:125], v233 offset:58880\n" \
"v_cvt_pk_bf16_f32 v2, v243, v252\n" \
"v_mfma_f32_16x16x32_bf16 v[140:143], a[88:91], a[16:19], v[140:143]\n" \
"ds_read_b64_tr_b16 a[126:127], v233 offset:60928\n" \
"v_cvt_pk_bf16_f32 v3, v253, v254\n" \
"v_mfma_f32_16x16x32_bf16 v[140:143], a[92:95], a[20:23], v[140:143]\n" \
"buffer_store_dwordx4 v[220:223], v236, s[56:59], s93 offen offset:2304 sc1\n" \
"v_mfma_f32_16x16x32_bf16 v[172:175], a[88:91], a[24:27], v[172:175]\n" \
"v_accvgpr_read_b32 v243, a132\n" \
"v_accvgpr_read_b32 v252, a133\n" \
"v_mfma_f32_16x16x32_bf16 v[172:175], a[92:95], a[28:31], v[172:175]\n" \
"v_accvgpr_read_b32 v253, a134\n" \
"v_accvgpr_read_b32 v254, a135\n" \
"v_mfma_f32_16x16x32_bf16 v[168:171], a[80:83], a[24:27], v[168:171]\n" \
"v_mul_f32 v243, s31, v243\n" \
"v_mul_f32 v252, s31, v252\n" \
"v_mfma_f32_16x16x32_bf16 v[168:171], a[84:87], a[28:31], v[168:171]\n" \
"v_mul_f32 v253, s31, v253\n" \
"v_mul_f32 v254, s31, v254\n" \
"v_mfma_f32_16x16x32_bf16 v[164:167], a[72:75], a[24:27], v[164:167]\n" \
"v_cvt_pk_bf16_f32 v4, v243, v252\n" \
"v_cvt_pk_bf16_f32 v5, v253, v254\n" \
"v_mfma_f32_16x16x32_bf16 v[164:167], a[76:79], a[28:31], v[164:167]\n" \
"v_accvgpr_read_b32 v243, a148\n" \
"v_accvgpr_read_b32 v252, a149\n" \
"v_mfma_f32_16x16x32_bf16 v[160:163], a[64:67], a[24:27], v[160:163]\n" \
"v_accvgpr_read_b32 v253, a150\n" \
"v_accvgpr_read_b32 v254, a151\n" \
"v_mfma_f32_16x16x32_bf16 v[160:163], a[68:71], a[28:31], v[160:163]\n" \
"s_waitcnt vmcnt(35) lgkmcnt(0)\n" \
"s_barrier\n" \
"v_mfma_f32_16x16x32_bf16 v[80:83], a[96:99], a[0:3], v[80:83]\n" \
"ds_read_b64_tr_b16 a[64:65], v232 offset:0\n" \
"v_mul_f32 v243, s31, v243\n" \
"v_mfma_f32_16x16x32_bf16 v[80:83], a[100:103], a[4:7], v[80:83]\n" \
"ds_read_b64_tr_b16 a[66:67], v232 offset:2048\n" \
"v_mul_f32 v252, s31, v252\n" \
"v_mfma_f32_16x16x32_bf16 v[84:87], a[104:107], a[0:3], v[84:87]\n" \
"s_add_u32 m0, s79, 49152\n" \
"ds_read_b64_tr_b16 a[68:69], v232 offset:8192\n" \
"v_mul_f32 v253, s31, v253\n" \
"v_mfma_f32_16x16x32_bf16 v[84:87], a[108:111], a[4:7], v[84:87]\n" \
"buffer_load_dwordx4 v228, s[52:55], s78 offen offset:0 lds\n" \
"v_mfma_f32_16x16x32_bf16 v[88:91], a[112:115], a[0:3], v[88:91]\n" \
"ds_read_b64_tr_b16 a[70:71], v232 offset:10240\n" \
"v_mul_f32 v254, s31, v254\n" \
"v_mfma_f32_16x16x32_bf16 v[88:91], a[116:119], a[4:7], v[88:91]\n" \
"ds_read_b64_tr_b16 a[72:73], v233 offset:0\n" \
"v_cvt_pk_bf16_f32 v6, v243, v252\n" \
"v_mfma_f32_16x16x32_bf16 v[92:95], a[120:123], a[0:3], v[92:95]\n" \
"ds_read_b64_tr_b16 a[74:75], v233 offset:2048\n" \
"v_cvt_pk_bf16_f32 v7, v253, v254\n" \
"v_mfma_f32_16x16x32_bf16 v[92:95], a[124:127], a[4:7], v[92:95]\n" \
"buffer_load_dwordx4 v229, s[52:55], s78 offen offset:1024 lds\n" \
"v_mfma_f32_16x16x32_bf16 v[124:127], a[120:123], a[8:11], v[124:127]\n" \
"s_waitcnt vmcnt(35)\n" \
"ds_read_b128 a[0:3], v230 offset:32768\n" \
"v_accvgpr_read_b32 v243, a136\n" \
"v_mfma_f32_16x16x32_bf16 v[124:127], a[124:127], a[12:15], v[124:127]\n" \
"ds_read_b128 a[4:7], v231 offset:32768\n" \
"v_accvgpr_read_b32 v252, a137\n" \
"v_mfma_f32_16x16x32_bf16 v[120:123], a[112:115], a[8:11], v[120:123]\n" \
"s_xor_b32 s78, s78, 0x300000\n" \
"ds_read_b64_tr_b16 a[76:77], v233 offset:8192\n" \
"v_accvgpr_read_b32 v253, a138\n" \
"v_mfma_f32_16x16x32_bf16 v[120:123], a[116:119], a[12:15], v[120:123]\n" \
"buffer_load_dwordx4 v228, s[52:55], s89 offen offset:2048 lds\n" \
"v_mfma_f32_16x16x32_bf16 v[116:119], a[104:107], a[8:11], v[116:119]\n" \
"ds_read_b64_tr_b16 a[78:79], v233 offset:10240\n" \
"v_accvgpr_read_b32 v254, a139\n" \
"v_mfma_f32_16x16x32_bf16 v[116:119], a[108:111], a[12:15], v[116:119]\n" \
"ds_read_b64_tr_b16 a[80:81], v232 offset:512\n" \
"v_mul_f32 v243, s31, v243\n" \
"v_mfma_f32_16x16x32_bf16 v[112:115], a[96:99], a[8:11], v[112:115]\n" \
"ds_read_b64_tr_b16 a[82:83], v232 offset:2560\n" \
"v_mul_f32 v252, s31, v252\n" \
"v_mfma_f32_16x16x32_bf16 v[112:115], a[100:103], a[12:15], v[112:115]\n" \
"buffer_load_dwordx4 v229, s[52:55], s89 offen offset:3072 lds\n" \
"v_mfma_f32_16x16x32_bf16 v[144:147], a[96:99], a[16:19], v[144:147]\n" \
"s_waitcnt vmcnt(35)\n" \
"s_xor_b32 s89, s89, 0x300000\n" \
"ds_read_b128 a[8:11], v230 offset:34816\n" \
"v_mfma_f32_16x16x32_bf16 v[144:147], a[100:103], a[20:23], v[144:147]\n" \
"s_add_u32 m0, s74, 98304\n" \
"ds_read_b128 a[12:15], v231 offset:34816\n" \
"v_mul_f32 v253, s31, v253\n" \
"v_mfma_f32_16x16x32_bf16 v[148:151], a[104:107], a[16:19], v[148:151]\n" \
"s_waitcnt lgkmcnt(6)\n" \
"ds_read_b64_tr_b16 a[84:85], v232 offset:8704\n" \
"v_mul_f32 v254, s31, v254\n" \
"v_mfma_f32_16x16x32_bf16 v[148:151], a[108:111], a[20:23], v[148:151]\n" \
"buffer_load_dwordx4 v224, s[48:51], s75 offen offset:0 lds\n" \
"v_mfma_f32_16x16x32_bf16 v[152:155], a[112:115], a[16:19], v[152:155]\n" \
"ds_read_b64_tr_b16 a[86:87], v232 offset:10752\n" \
"v_cvt_pk_bf16_f32 v8, v243, v252\n" \
"v_mfma_f32_16x16x32_bf16 v[152:155], a[116:119], a[20:23], v[152:155]\n" \
"ds_read_b64_tr_b16 a[88:89], v233 offset:512\n" \
"v_cvt_pk_bf16_f32 v9, v253, v254\n" \
"v_mfma_f32_16x16x32_bf16 v[156:159], a[120:123], a[16:19], v[156:159]\n" \
"ds_read_b64_tr_b16 a[90:91], v233 offset:2560\n" \
"v_accvgpr_read_b32 v243, a152\n" \
"v_mfma_f32_16x16x32_bf16 v[156:159], a[124:127], a[20:23], v[156:159]\n" \
"buffer_load_dwordx4 v225, s[48:51], s75 offen offset:1024 lds\n" \
"v_mfma_f32_16x16x32_bf16 v[188:191], a[120:123], a[24:27], v[188:191]\n" \
"s_waitcnt vmcnt(35)\n" \
"ds_read_b128 a[16:19], v230 offset:36864\n" \
"v_accvgpr_read_b32 v252, a153\n" \
"v_mfma_f32_16x16x32_bf16 v[188:191], a[124:127], a[28:31], v[188:191]\n" \
"ds_read_b128 a[20:23], v231 offset:36864\n" \
"v_accvgpr_read_b32 v253, a154\n" \
"v_mfma_f32_16x16x32_bf16 v[184:187], a[112:115], a[24:27], v[184:187]\n" \
"s_waitcnt lgkmcnt(6)\n" \
"ds_read_b64_tr_b16 a[92:93], v233 offset:8704\n" \
"v_accvgpr_read_b32 v254, a155\n" \
"v_mfma_f32_16x16x32_bf16 v[184:187], a[116:119], a[28:31], v[184:187]\n" \
"buffer_load_dwordx4 v226, s[48:51], s75 offen offset:2048 lds\n" \
"v_mfma_f32_16x16x32_bf16 v[180:183], a[104:107], a[24:27], v[180:183]\n" \
"ds_read_b64_tr_b16 a[94:95], v233 offset:10752\n" \
"v_mul_f32 v243, s31, v243\n" \
"v_mfma_f32_16x16x32_bf16 v[180:183], a[108:111], a[28:31], v[180:183]\n" \
"v_mul_f32 v252, s31, v252\n" \
"v_mul_f32 v253, s31, v253\n" \
"v_mfma_f32_16x16x32_bf16 v[176:179], a[96:99], a[24:27], v[176:179]\n" \
"v_mul_f32 v254, s31, v254\n" \
"v_cvt_pk_bf16_f32 v10, v243, v252\n" \
"v_mfma_f32_16x16x32_bf16 v[176:179], a[100:103], a[28:31], v[176:179]\n" \
"buffer_load_dwordx4 v227, s[48:51], s75 offen offset:3072 lds\n" \
"v_mfma_f32_16x16x32_bf16 v[64:67], a[64:67], a[0:3], v[64:67]\n" \
"s_waitcnt vmcnt(35)\n" \
"s_xor_b32 s75, s75, 0x300\n" \
"ds_read_b128 a[24:27], v230 offset:38912\n" \
"v_mfma_f32_16x16x32_bf16 v[64:67], a[68:71], a[4:7], v[64:67]\n" \
"s_add_u32 m0, s74, 102400\n" \
"ds_read_b128 a[28:31], v231 offset:38912\n" \
"v_cvt_pk_bf16_f32 v11, v253, v254\n" \
"v_mfma_f32_16x16x32_bf16 v[68:71], a[72:75], a[0:3], v[68:71]\n" \
"s_waitcnt lgkmcnt(4)\n" \
"ds_read_b64_tr_b16 a[96:97], v232 offset:1024\n" \
"v_accvgpr_read_b32 v243, a140\n" \
"v_mfma_f32_16x16x32_bf16 v[68:71], a[76:79], a[4:7], v[68:71]\n" \
"buffer_load_dwordx4 v224, s[48:51], s77 offen offset:0 lds\n" \
"v_mfma_f32_16x16x32_bf16 v[72:75], a[80:83], a[0:3], v[72:75]\n" \
"ds_read_b64_tr_b16 a[98:99], v232 offset:3072\n" \
"v_accvgpr_read_b32 v252, a141\n" \
"v_mfma_f32_16x16x32_bf16 v[72:75], a[84:87], a[4:7], v[72:75]\n" \
"s_waitcnt lgkmcnt(3)\n" \
"ds_read_b64_tr_b16 a[100:101], v232 offset:9216\n" \
"v_accvgpr_read_b32 v253, a142\n" \
"v_mfma_f32_16x16x32_bf16 v[76:79], a[88:91], a[0:3], v[76:79]\n" \
"ds_read_b64_tr_b16 a[102:103], v232 offset:11264\n" \
"v_accvgpr_read_b32 v254, a143\n" \
"v_mfma_f32_16x16x32_bf16 v[76:79], a[92:95], a[4:7], v[76:79]\n" \
"buffer_load_dwordx4 v225, s[48:51], s77 offen offset:1024 lds\n" \
"v_mfma_f32_16x16x32_bf16 v[108:111], a[88:91], a[8:11], v[108:111]\n" \
"ds_read_b64_tr_b16 a[104:105], v233 offset:1024\n" \
"v_mul_f32 v243, s31, v243\n" \
"v_mfma_f32_16x16x32_bf16 v[108:111], a[92:95], a[12:15], v[108:111]\n" \
"ds_read_b64_tr_b16 a[106:107], v233 offset:3072\n" \
"v_mul_f32 v252, s31, v252\n" \
"v_mfma_f32_16x16x32_bf16 v[104:107], a[80:83], a[8:11], v[104:107]\n" \
"s_waitcnt lgkmcnt(6)\n" \
"ds_read_b64_tr_b16 a[108:109], v233 offset:9216\n" \
"v_mul_f32 v253, s31, v253\n" \
"v_mfma_f32_16x16x32_bf16 v[104:107], a[84:87], a[12:15], v[104:107]\n" \
"buffer_load_dwordx4 v226, s[48:51], s77 offen offset:2048 lds\n" \
"v_mfma_f32_16x16x32_bf16 v[100:103], a[72:75], a[8:11], v[100:103]\n" \
"ds_read_b64_tr_b16 a[110:111], v233 offset:11264\n" \
"v_mul_f32 v254, s31, v254\n" \
"v_mfma_f32_16x16x32_bf16 v[100:103], a[76:79], a[12:15], v[100:103]\n" \
"ds_read_b64_tr_b16 a[112:113], v232 offset:1536\n" \
"v_cvt_pk_bf16_f32 v12, v243, v252\n" \
"v_mfma_f32_16x16x32_bf16 v[96:99], a[64:67], a[8:11], v[96:99]\n" \
"ds_read_b64_tr_b16 a[114:115], v232 offset:3584\n" \
"v_cvt_pk_bf16_f32 v13, v253, v254\n" \
"v_mfma_f32_16x16x32_bf16 v[96:99], a[68:71], a[12:15], v[96:99]\n" \
"buffer_load_dwordx4 v227, s[48:51], s77 offen offset:3072 lds\n" \
"v_mfma_f32_16x16x32_bf16 v[128:131], a[64:67], a[16:19], v[128:131]\n" \
"ds_read_b64_tr_b16 a[116:117], v232 offset:9728\n" \
"v_accvgpr_read_b32 v243, a156\n" \
"v_mfma_f32_16x16x32_bf16 v[128:131], a[68:71], a[20:23], v[128:131]\n" \
"ds_read_b64_tr_b16 a[118:119], v232 offset:11776\n" \
"v_accvgpr_read_b32 v252, a157\n" \
"v_mfma_f32_16x16x32_bf16 v[132:135], a[72:75], a[16:19], v[132:135]\n" \
"s_xor_b32 s77, s77, 0x300\n" \
"ds_read_b64_tr_b16 a[120:121], v233 offset:1536\n" \
"v_accvgpr_read_b32 v253, a158\n" \
"v_mfma_f32_16x16x32_bf16 v[132:135], a[76:79], a[20:23], v[132:135]\n" \
"v_accvgpr_read_b32 v254, a159\n" \
"v_mul_f32 v243, s31, v243\n" \
"v_mfma_f32_16x16x32_bf16 v[136:139], a[80:83], a[16:19], v[136:139]\n" \
"ds_read_b64_tr_b16 a[122:123], v233 offset:3584\n" \
"v_mul_f32 v252, s31, v252\n" \
"v_mfma_f32_16x16x32_bf16 v[136:139], a[84:87], a[20:23], v[136:139]\n" \
"ds_read_b64_tr_b16 a[124:125], v233 offset:9728\n" \
"v_mul_f32 v253, s31, v253\n" \
"v_mfma_f32_16x16x32_bf16 v[140:143], a[88:91], a[16:19], v[140:143]\n" \
"s_add_u32 s93, s87, 17858560\n" \
"ds_read_b64_tr_b16 a[126:127], v233 offset:11776\n" \
"v_mul_f32 v254, s31, v254\n" \
"v_mfma_f32_16x16x32_bf16 v[140:143], a[92:95], a[20:23], v[140:143]\n" \
"buffer_store_dwordx4 a[224:227], v234, s[56:59], s93 offen offset:0 sc1\n" \
"v_mfma_f32_16x16x32_bf16 v[172:175], a[88:91], a[24:27], v[172:175]\n" \
"v_cvt_pk_bf16_f32 v14, v243, v252\n" \
"v_cvt_pk_bf16_f32 v15, v253, v254\n" \
"v_mfma_f32_16x16x32_bf16 v[172:175], a[92:95], a[28:31], v[172:175]\n" \
"v_accvgpr_write_b32 a56, v244\n" \
"v_accvgpr_write_b32 a57, v245\n" \
"v_mfma_f32_16x16x32_bf16 v[168:171], a[80:83], a[24:27], v[168:171]\n" \
"v_accvgpr_write_b32 a58, v246\n" \
"v_accvgpr_write_b32 a59, v247\n" \
"v_mfma_f32_16x16x32_bf16 v[168:171], a[84:87], a[28:31], v[168:171]\n" \
"v_accvgpr_write_b32 a60, v248\n" \
"v_accvgpr_write_b32 a61, v249\n" \
"v_mfma_f32_16x16x32_bf16 v[164:167], a[72:75], a[24:27], v[164:167]\n" \
"v_accvgpr_write_b32 a48, 0\n" \
"v_accvgpr_write_b32 a49, 0\n" \
"v_mfma_f32_16x16x32_bf16 v[164:167], a[76:79], a[28:31], v[164:167]\n" \
"v_accvgpr_write_b32 a50, 0\n" \
"v_accvgpr_write_b32 a51, 0\n" \
"v_mfma_f32_16x16x32_bf16 v[160:163], a[64:67], a[24:27], v[160:163]\n" \
"v_accvgpr_write_b32 a52, 0\n" \
"v_accvgpr_write_b32 a53, 0\n" \
"v_mfma_f32_16x16x32_bf16 v[160:163], a[68:71], a[28:31], v[160:163]\n" \
"s_waitcnt vmcnt(35) lgkmcnt(0)\n" \
"s_barrier\n" \
"v_mfma_f32_16x16x32_bf16 v[80:83], a[96:99], a[0:3], v[80:83]\n" \
"ds_read_b64_tr_b16 a[64:65], v232 offset:16384\n" \
"v_mfma_f32_16x16x32_bf16 v[80:83], a[100:103], a[4:7], v[80:83]\n" \
"ds_read_b64_tr_b16 a[66:67], v232 offset:18432\n" \
"v_mfma_f32_16x16x32_bf16 v[84:87], a[104:107], a[0:3], v[84:87]\n" \
"s_add_u32 m0, s79, 0\n" \
"ds_read_b64_tr_b16 a[68:69], v232 offset:24576\n" \
"v_mfma_f32_16x16x32_bf16 v[84:87], a[108:111], a[4:7], v[84:87]\n" \
"buffer_load_dwordx4 v228, s[52:55], s78 offen offset:0 lds\n" \
"v_mfma_f32_16x16x32_bf16 v[88:91], a[112:115], a[0:3], v[88:91]\n" \
"ds_read_b64_tr_b16 a[70:71], v232 offset:26624\n" \
"v_mfma_f32_16x16x32_bf16 v[88:91], a[116:119], a[4:7], v[88:91]\n" \
"ds_read_b64_tr_b16 a[72:73], v233 offset:16384\n" \
"v_mfma_f32_16x16x32_bf16 v[92:95], a[120:123], a[0:3], v[92:95]\n" \
"ds_read_b64_tr_b16 a[74:75], v233 offset:18432\n" \
"v_mfma_f32_16x16x32_bf16 v[92:95], a[124:127], a[4:7], v[92:95]\n" \
"buffer_load_dwordx4 v229, s[52:55], s78 offen offset:1024 lds\n" \
"v_mfma_f32_16x16x32_bf16 v[124:127], a[120:123], a[8:11], v[124:127]\n" \
"s_waitcnt vmcnt(35)\n" \
"ds_read_b128 a[0:3], v250 offset:0\n" \
"v_mfma_f32_16x16x32_bf16 v[124:127], a[124:127], a[12:15], v[124:127]\n" \
"ds_read_b128 a[4:7], v251 offset:0\n" \
"v_mfma_f32_16x16x32_bf16 v[120:123], a[112:115], a[8:11], v[120:123]\n" \
"s_xor_b32 s78, s78, 0x80000\n" \
"ds_read_b64_tr_b16 a[76:77], v233 offset:24576\n" \
"v_mfma_f32_16x16x32_bf16 v[120:123], a[116:119], a[12:15], v[120:123]\n" \
"buffer_load_dwordx4 v228, s[52:55], s89 offen offset:2048 lds\n" \
"v_mfma_f32_16x16x32_bf16 v[116:119], a[104:107], a[8:11], v[116:119]\n" \
"ds_read_b64_tr_b16 a[78:79], v233 offset:26624\n" \
"v_mfma_f32_16x16x32_bf16 v[116:119], a[108:111], a[12:15], v[116:119]\n" \
"ds_read_b64_tr_b16 a[80:81], v232 offset:16896\n" \
"v_mfma_f32_16x16x32_bf16 v[112:115], a[96:99], a[8:11], v[112:115]\n" \
"ds_read_b64_tr_b16 a[82:83], v232 offset:18944\n" \
"v_mfma_f32_16x16x32_bf16 v[112:115], a[100:103], a[12:15], v[112:115]\n" \
"buffer_load_dwordx4 v229, s[52:55], s89 offen offset:3072 lds\n" \
"v_mfma_f32_16x16x32_bf16 v[144:147], a[96:99], a[16:19], v[144:147]\n" \
"s_waitcnt vmcnt(35)\n" \
"s_xor_b32 s89, s89, 0x80000\n" \
"ds_read_b128 a[8:11], v250 offset:2048\n" \
"v_mfma_f32_16x16x32_bf16 v[144:147], a[100:103], a[20:23], v[144:147]\n" \
"s_add_u32 m0, s74, 131072\n" \
"ds_read_b128 a[12:15], v251 offset:2048\n" \
"v_mfma_f32_16x16x32_bf16 v[148:151], a[104:107], a[16:19], v[148:151]\n" \
"s_waitcnt lgkmcnt(6)\n" \
"ds_read_b64_tr_b16 a[84:85], v232 offset:25088\n" \
"v_mfma_f32_16x16x32_bf16 v[148:151], a[108:111], a[20:23], v[148:151]\n" \
"buffer_load_dwordx4 v224, s[48:51], s75 offen offset:0 lds\n" \
"v_mfma_f32_16x16x32_bf16 v[152:155], a[112:115], a[16:19], v[152:155]\n" \
"ds_read_b64_tr_b16 a[86:87], v232 offset:27136\n" \
"v_mfma_f32_16x16x32_bf16 v[152:155], a[116:119], a[20:23], v[152:155]\n" \
"ds_read_b64_tr_b16 a[88:89], v233 offset:16896\n" \
"v_mfma_f32_16x16x32_bf16 v[156:159], a[120:123], a[16:19], v[156:159]\n" \
"ds_read_b64_tr_b16 a[90:91], v233 offset:18944\n" \
"v_mfma_f32_16x16x32_bf16 v[156:159], a[124:127], a[20:23], v[156:159]\n" \
"buffer_load_dwordx4 v225, s[48:51], s75 offen offset:1024 lds\n" \
"v_mfma_f32_16x16x32_bf16 v[188:191], a[120:123], a[24:27], v[188:191]\n" \
"s_waitcnt vmcnt(35)\n" \
"ds_read_b128 a[16:19], v250 offset:4096\n" \
"v_mfma_f32_16x16x32_bf16 v[188:191], a[124:127], a[28:31], v[188:191]\n" \
"ds_read_b128 a[20:23], v251 offset:4096\n" \
"v_mfma_f32_16x16x32_bf16 v[184:187], a[112:115], a[24:27], v[184:187]\n" \
"s_waitcnt lgkmcnt(6)\n" \
"ds_read_b64_tr_b16 a[92:93], v233 offset:25088\n" \
"v_mfma_f32_16x16x32_bf16 v[184:187], a[116:119], a[28:31], v[184:187]\n" \
"buffer_load_dwordx4 v226, s[48:51], s75 offen offset:2048 lds\n" \
"v_mfma_f32_16x16x32_bf16 v[180:183], a[104:107], a[24:27], v[180:183]\n" \
"ds_read_b64_tr_b16 a[94:95], v233 offset:27136\n" \
"v_mfma_f32_16x16x32_bf16 v[180:183], a[108:111], a[28:31], v[180:183]\n" \
"v_mfma_f32_16x16x32_bf16 v[176:179], a[96:99], a[24:27], v[176:179]\n" \
"v_mfma_f32_16x16x32_bf16 v[176:179], a[100:103], a[28:31], v[176:179]\n" \
"buffer_load_dwordx4 v227, s[48:51], s75 offen offset:3072 lds\n" \
"v_mfma_f32_16x16x32_bf16 v[64:67], a[64:67], a[0:3], v[64:67]\n" \
"s_waitcnt vmcnt(35)\n" \
"s_xor_b32 s75, s75, 0x80\n" \
"ds_read_b128 a[24:27], v250 offset:6144\n" \
"v_mfma_f32_16x16x32_bf16 v[64:67], a[68:71], a[4:7], v[64:67]\n" \
"s_add_u32 m0, s74, 135168\n" \
"ds_read_b128 a[28:31], v251 offset:6144\n" \
"v_mfma_f32_16x16x32_bf16 v[68:71], a[72:75], a[0:3], v[68:71]\n" \
"s_waitcnt lgkmcnt(2)\n" \
"ds_read_b64_tr_b16 a[96:97], v232 offset:17408\n" \
"v_mfma_f32_16x16x32_bf16 v[68:71], a[76:79], a[4:7], v[68:71]\n" \
"buffer_load_dwordx4 v224, s[48:51], s77 offen offset:0 lds\n" \
"v_mfma_f32_16x16x32_bf16 v[72:75], a[80:83], a[0:3], v[72:75]\n" \
"ds_read_b64_tr_b16 a[98:99], v232 offset:19456\n" \
"v_mfma_f32_16x16x32_bf16 v[72:75], a[84:87], a[4:7], v[72:75]\n" \
"ds_read_b64_tr_b16 a[100:101], v232 offset:25600\n" \
"v_mfma_f32_16x16x32_bf16 v[76:79], a[88:91], a[0:3], v[76:79]\n" \
"ds_read_b64_tr_b16 a[102:103], v232 offset:27648\n" \
"v_mfma_f32_16x16x32_bf16 v[76:79], a[92:95], a[4:7], v[76:79]\n" \
"buffer_load_dwordx4 v225, s[48:51], s77 offen offset:1024 lds\n" \
"v_mfma_f32_16x16x32_bf16 v[108:111], a[88:91], a[8:11], v[108:111]\n" \
"ds_read_b64_tr_b16 a[104:105], v233 offset:17408\n" \
"v_mfma_f32_16x16x32_bf16 v[108:111], a[92:95], a[12:15], v[108:111]\n" \
"ds_read_b64_tr_b16 a[106:107], v233 offset:19456\n" \
"v_mfma_f32_16x16x32_bf16 v[104:107], a[80:83], a[8:11], v[104:107]\n" \
"s_waitcnt lgkmcnt(6)\n" \
"ds_read_b64_tr_b16 a[108:109], v233 offset:25600\n" \
"v_mfma_f32_16x16x32_bf16 v[104:107], a[84:87], a[12:15], v[104:107]\n" \
"buffer_load_dwordx4 v226, s[48:51], s77 offen offset:2048 lds\n" \
"v_mfma_f32_16x16x32_bf16 v[100:103], a[72:75], a[8:11], v[100:103]\n" \
"ds_read_b64_tr_b16 a[110:111], v233 offset:27648\n" \
"v_mfma_f32_16x16x32_bf16 v[100:103], a[76:79], a[12:15], v[100:103]\n" \
"ds_read_b64_tr_b16 a[112:113], v232 offset:17920\n" \
"v_mfma_f32_16x16x32_bf16 v[96:99], a[64:67], a[8:11], v[96:99]\n" \
"ds_read_b64_tr_b16 a[114:115], v232 offset:19968\n" \
"Lpg3897_%=:\n" \
"s_cbranch_execz Lpg6052_%=\n" \
"v_mfma_f32_16x16x32_bf16 v[96:99], a[68:71], a[12:15], v[96:99]\n" \
"buffer_load_dwordx4 v227, s[48:51], s77 offen offset:3072 lds\n" \
"v_mfma_f32_16x16x32_bf16 v[128:131], a[64:67], a[16:19], v[128:131]\n" \
"ds_read_b64_tr_b16 a[116:117], v232 offset:26112\n" \
"v_mfma_f32_16x16x32_bf16 v[128:131], a[68:71], a[20:23], v[128:131]\n" \
"ds_read_b64_tr_b16 a[118:119], v232 offset:28160\n" \
"v_mfma_f32_16x16x32_bf16 v[132:135], a[72:75], a[16:19], v[132:135]\n" \
"s_xor_b32 s77, s77, 0x80\n" \
"ds_read_b64_tr_b16 a[120:121], v233 offset:17920\n" \
"v_mfma_f32_16x16x32_bf16 v[132:135], a[76:79], a[20:23], v[132:135]\n" \
"v_mfma_f32_16x16x32_bf16 v[136:139], a[80:83], a[16:19], v[136:139]\n" \
"ds_read_b64_tr_b16 a[122:123], v233 offset:19968\n" \
"v_mfma_f32_16x16x32_bf16 v[136:139], a[84:87], a[20:23], v[136:139]\n" \
"ds_read_b64_tr_b16 a[124:125], v233 offset:26112\n" \
"v_mfma_f32_16x16x32_bf16 v[140:143], a[88:91], a[16:19], v[140:143]\n" \
"ds_read_b64_tr_b16 a[126:127], v233 offset:28160\n" \
"v_mfma_f32_16x16x32_bf16 v[140:143], a[92:95], a[20:23], v[140:143]\n" \
"buffer_store_dwordx4 a[228:231], v234, s[56:59], s93 offen offset:1024 sc1\n" \
"v_mfma_f32_16x16x32_bf16 v[172:175], a[88:91], a[24:27], v[172:175]\n" \
"v_mfma_f32_16x16x32_bf16 v[172:175], a[92:95], a[28:31], v[172:175]\n" \
"v_mfma_f32_16x16x32_bf16 v[168:171], a[80:83], a[24:27], v[168:171]\n" \
"v_mfma_f32_16x16x32_bf16 v[168:171], a[84:87], a[28:31], v[168:171]\n" \
"v_mfma_f32_16x16x32_bf16 v[164:167], a[72:75], a[24:27], v[164:167]\n" \
"v_mfma_f32_16x16x32_bf16 v[164:167], a[76:79], a[28:31], v[164:167]\n" \
"v_mfma_f32_16x16x32_bf16 v[160:163], a[64:67], a[24:27], v[160:163]\n" \
"v_mfma_f32_16x16x32_bf16 v[160:163], a[68:71], a[28:31], v[160:163]\n" \
"s_waitcnt vmcnt(35) lgkmcnt(0)\n" \
"s_barrier\n" \
"v_mfma_f32_16x16x32_bf16 v[80:83], a[96:99], a[0:3], v[80:83]\n" \
"ds_read_b64_tr_b16 a[64:65], v232 offset:32768\n" \
"v_mfma_f32_16x16x32_bf16 v[80:83], a[100:103], a[4:7], v[80:83]\n" \
"ds_read_b64_tr_b16 a[66:67], v232 offset:34816\n" \
"v_mfma_f32_16x16x32_bf16 v[84:87], a[104:107], a[0:3], v[84:87]\n" \
"s_add_u32 m0, s79, 16384\n" \
"ds_read_b64_tr_b16 a[68:69], v232 offset:40960\n" \
"v_mfma_f32_16x16x32_bf16 v[84:87], a[108:111], a[4:7], v[84:87]\n" \
"buffer_load_dwordx4 v228, s[52:55], s78 offen offset:0 lds\n" \
"v_mfma_f32_16x16x32_bf16 v[88:91], a[112:115], a[0:3], v[88:91]\n" \
"ds_read_b64_tr_b16 a[70:71], v232 offset:43008\n" \
"v_mfma_f32_16x16x32_bf16 v[88:91], a[116:119], a[4:7], v[88:91]\n" \
"ds_read_b64_tr_b16 a[72:73], v233 offset:32768\n" \
"v_mfma_f32_16x16x32_bf16 v[92:95], a[120:123], a[0:3], v[92:95]\n" \
"ds_read_b64_tr_b16 a[74:75], v233 offset:34816\n" \
"v_mfma_f32_16x16x32_bf16 v[92:95], a[124:127], a[4:7], v[92:95]\n" \
"buffer_load_dwordx4 v229, s[52:55], s78 offen offset:1024 lds\n" \
"v_mfma_f32_16x16x32_bf16 v[124:127], a[120:123], a[8:11], v[124:127]\n" \
"s_waitcnt vmcnt(35)\n" \
"ds_read_b128 a[0:3], v230 offset:0\n" \
"v_mfma_f32_16x16x32_bf16 v[124:127], a[124:127], a[12:15], v[124:127]\n" \
"ds_read_b128 a[4:7], v231 offset:0\n" \
"v_mfma_f32_16x16x32_bf16 v[120:123], a[112:115], a[8:11], v[120:123]\n" \
"s_xor_b32 s78, s78, 0x180000\n" \
"ds_read_b64_tr_b16 a[76:77], v233 offset:40960\n" \
"v_mfma_f32_16x16x32_bf16 v[120:123], a[116:119], a[12:15], v[120:123]\n" \
"buffer_load_dwordx4 v228, s[52:55], s89 offen offset:2048 lds\n" \
"v_mfma_f32_16x16x32_bf16 v[116:119], a[104:107], a[8:11], v[116:119]\n" \
"ds_read_b64_tr_b16 a[78:79], v233 offset:43008\n" \
"v_mfma_f32_16x16x32_bf16 v[116:119], a[108:111], a[12:15], v[116:119]\n" \
"ds_read_b64_tr_b16 a[80:81], v232 offset:33280\n" \
"v_mfma_f32_16x16x32_bf16 v[112:115], a[96:99], a[8:11], v[112:115]\n" \
"ds_read_b64_tr_b16 a[82:83], v232 offset:35328\n" \
"v_mfma_f32_16x16x32_bf16 v[112:115], a[100:103], a[12:15], v[112:115]\n" \
"buffer_load_dwordx4 v229, s[52:55], s89 offen offset:3072 lds\n" \
"v_mfma_f32_16x16x32_bf16 v[144:147], a[96:99], a[16:19], v[144:147]\n" \
"s_waitcnt vmcnt(35)\n" \
"s_xor_b32 s89, s89, 0x180000\n" \
"ds_read_b128 a[8:11], v230 offset:2048\n" \
"v_mfma_f32_16x16x32_bf16 v[144:147], a[100:103], a[20:23], v[144:147]\n" \
"s_add_u32 m0, s74, 65536\n" \
"ds_read_b128 a[12:15], v231 offset:2048\n" \
"v_mfma_f32_16x16x32_bf16 v[148:151], a[104:107], a[16:19], v[148:151]\n" \
"s_waitcnt lgkmcnt(6)\n" \
"ds_read_b64_tr_b16 a[84:85], v232 offset:41472\n" \
"v_mfma_f32_16x16x32_bf16 v[148:151], a[108:111], a[20:23], v[148:151]\n" \
"buffer_load_dwordx4 v224, s[48:51], s75 offen offset:0 lds\n" \
"v_mfma_f32_16x16x32_bf16 v[152:155], a[112:115], a[16:19], v[152:155]\n" \
"ds_read_b64_tr_b16 a[86:87], v232 offset:43520\n" \
"v_mfma_f32_16x16x32_bf16 v[152:155], a[116:119], a[20:23], v[152:155]\n" \
"ds_read_b64_tr_b16 a[88:89], v233 offset:33280\n" \
"v_mfma_f32_16x16x32_bf16 v[156:159], a[120:123], a[16:19], v[156:159]\n" \
"ds_read_b64_tr_b16 a[90:91], v233 offset:35328\n" \
"v_mfma_f32_16x16x32_bf16 v[156:159], a[124:127], a[20:23], v[156:159]\n" \
"buffer_load_dwordx4 v225, s[48:51], s75 offen offset:1024 lds\n" \
"v_mfma_f32_16x16x32_bf16 v[188:191], a[120:123], a[24:27], v[188:191]\n" \
"s_waitcnt vmcnt(35)\n" \
"ds_read_b128 a[16:19], v230 offset:4096\n" \
"v_mfma_f32_16x16x32_bf16 v[188:191], a[124:127], a[28:31], v[188:191]\n" \
"ds_read_b128 a[20:23], v231 offset:4096\n" \
"v_mfma_f32_16x16x32_bf16 v[184:187], a[112:115], a[24:27], v[184:187]\n" \
"s_waitcnt lgkmcnt(6)\n" \
"ds_read_b64_tr_b16 a[92:93], v233 offset:41472\n" \
"v_mfma_f32_16x16x32_bf16 v[184:187], a[116:119], a[28:31], v[184:187]\n" \
"buffer_load_dwordx4 v226, s[48:51], s75 offen offset:2048 lds\n" \
"v_mfma_f32_16x16x32_bf16 v[180:183], a[104:107], a[24:27], v[180:183]\n" \
"ds_read_b64_tr_b16 a[94:95], v233 offset:43520\n" \
"v_mfma_f32_16x16x32_bf16 v[180:183], a[108:111], a[28:31], v[180:183]\n" \
"v_mfma_f32_16x16x32_bf16 v[176:179], a[96:99], a[24:27], v[176:179]\n" \
"v_mfma_f32_16x16x32_bf16 v[176:179], a[100:103], a[28:31], v[176:179]\n" \
"buffer_load_dwordx4 v227, s[48:51], s75 offen offset:3072 lds\n" \
"v_mfma_f32_16x16x32_bf16 v[64:67], a[64:67], a[0:3], v[64:67]\n" \
"s_waitcnt vmcnt(35)\n" \
"s_xor_b32 s75, s75, 0x180\n" \
"ds_read_b128 a[24:27], v230 offset:6144\n" \
"v_mfma_f32_16x16x32_bf16 v[64:67], a[68:71], a[4:7], v[64:67]\n" \
"s_add_u32 m0, s74, 69632\n" \
"ds_read_b128 a[28:31], v231 offset:6144\n" \
"v_mfma_f32_16x16x32_bf16 v[68:71], a[72:75], a[0:3], v[68:71]\n" \
"s_waitcnt lgkmcnt(2)\n" \
"ds_read_b64_tr_b16 a[96:97], v232 offset:33792\n" \
"v_mfma_f32_16x16x32_bf16 v[68:71], a[76:79], a[4:7], v[68:71]\n" \
"buffer_load_dwordx4 v224, s[48:51], s77 offen offset:0 lds\n" \
"v_mfma_f32_16x16x32_bf16 v[72:75], a[80:83], a[0:3], v[72:75]\n" \
"ds_read_b64_tr_b16 a[98:99], v232 offset:35840\n" \
"v_mfma_f32_16x16x32_bf16 v[72:75], a[84:87], a[4:7], v[72:75]\n" \
"ds_read_b64_tr_b16 a[100:101], v232 offset:41984\n" \
"v_mfma_f32_16x16x32_bf16 v[76:79], a[88:91], a[0:3], v[76:79]\n" \
"ds_read_b64_tr_b16 a[102:103], v232 offset:44032\n" \
"v_mfma_f32_16x16x32_bf16 v[76:79], a[92:95], a[4:7], v[76:79]\n" \
"buffer_load_dwordx4 v225, s[48:51], s77 offen offset:1024 lds\n" \
"v_mfma_f32_16x16x32_bf16 v[108:111], a[88:91], a[8:11], v[108:111]\n" \
"ds_read_b64_tr_b16 a[104:105], v233 offset:33792\n" \
"v_mfma_f32_16x16x32_bf16 v[108:111], a[92:95], a[12:15], v[108:111]\n" \
"ds_read_b64_tr_b16 a[106:107], v233 offset:35840\n" \
"v_mfma_f32_16x16x32_bf16 v[104:107], a[80:83], a[8:11], v[104:107]\n" \
"s_waitcnt lgkmcnt(6)\n" \
"ds_read_b64_tr_b16 a[108:109], v233 offset:41984\n" \
"v_mfma_f32_16x16x32_bf16 v[104:107], a[84:87], a[12:15], v[104:107]\n" \
"buffer_load_dwordx4 v226, s[48:51], s77 offen offset:2048 lds\n" \
"v_mfma_f32_16x16x32_bf16 v[100:103], a[72:75], a[8:11], v[100:103]\n" \
"ds_read_b64_tr_b16 a[110:111], v233 offset:44032\n" \
"v_mfma_f32_16x16x32_bf16 v[100:103], a[76:79], a[12:15], v[100:103]\n" \
"ds_read_b64_tr_b16 a[112:113], v232 offset:34304\n" \
"v_mfma_f32_16x16x32_bf16 v[96:99], a[64:67], a[8:11], v[96:99]\n" \
"ds_read_b64_tr_b16 a[114:115], v232 offset:36352\n" \
"v_mfma_f32_16x16x32_bf16 v[96:99], a[68:71], a[12:15], v[96:99]\n" \
"buffer_load_dwordx4 v227, s[48:51], s77 offen offset:3072 lds\n" \
"v_mfma_f32_16x16x32_bf16 v[128:131], a[64:67], a[16:19], v[128:131]\n" \
"ds_read_b64_tr_b16 a[116:117], v232 offset:42496\n" \
"v_mfma_f32_16x16x32_bf16 v[128:131], a[68:71], a[20:23], v[128:131]\n" \
"ds_read_b64_tr_b16 a[118:119], v232 offset:44544\n" \
"v_mfma_f32_16x16x32_bf16 v[132:135], a[72:75], a[16:19], v[132:135]\n" \
"s_xor_b32 s77, s77, 0x180\n" \
"ds_read_b64_tr_b16 a[120:121], v233 offset:34304\n" \
"v_mfma_f32_16x16x32_bf16 v[132:135], a[76:79], a[20:23], v[132:135]\n" \
"buffer_store_dwordx4 a[232:235], v234, s[56:59], s93 offen offset:2048 sc1\n" \
"v_mfma_f32_16x16x32_bf16 v[136:139], a[80:83], a[16:19], v[136:139]\n" \
"ds_read_b64_tr_b16 a[122:123], v233 offset:36352\n" \
"v_mfma_f32_16x16x32_bf16 v[136:139], a[84:87], a[20:23], v[136:139]\n" \
"ds_read_b64_tr_b16 a[124:125], v233 offset:42496\n" \
"v_mfma_f32_16x16x32_bf16 v[140:143], a[88:91], a[16:19], v[140:143]\n" \
"ds_read_b64_tr_b16 a[126:127], v233 offset:44544\n" \
"v_mfma_f32_16x16x32_bf16 v[140:143], a[92:95], a[20:23], v[140:143]\n" \
"v_mfma_f32_16x16x32_bf16 v[172:175], a[88:91], a[24:27], v[172:175]\n" \
"v_mfma_f32_16x16x32_bf16 v[172:175], a[92:95], a[28:31], v[172:175]\n" \
"v_mfma_f32_16x16x32_bf16 v[168:171], a[80:83], a[24:27], v[168:171]\n" \
"v_mfma_f32_16x16x32_bf16 v[168:171], a[84:87], a[28:31], v[168:171]\n" \
"buffer_store_dwordx4 a[236:239], v234, s[56:59], s93 offen offset:3072 sc1\n" \
"v_mfma_f32_16x16x32_bf16 v[164:167], a[72:75], a[24:27], v[164:167]\n" \
"v_mfma_f32_16x16x32_bf16 v[164:167], a[76:79], a[28:31], v[164:167]\n" \
"v_mfma_f32_16x16x32_bf16 v[160:163], a[64:67], a[24:27], v[160:163]\n" \
"v_mfma_f32_16x16x32_bf16 v[160:163], a[68:71], a[28:31], v[160:163]\n" \
"s_waitcnt vmcnt(36) lgkmcnt(0)\n" \
"s_barrier\n" \
"v_mfma_f32_16x16x32_bf16 v[80:83], a[96:99], a[0:3], v[80:83]\n" \
"ds_read_b64_tr_b16 a[64:65], v232 offset:49152\n" \
"v_mfma_f32_16x16x32_bf16 v[80:83], a[100:103], a[4:7], v[80:83]\n" \
"ds_read_b64_tr_b16 a[66:67], v232 offset:51200\n" \
"v_mfma_f32_16x16x32_bf16 v[84:87], a[104:107], a[0:3], v[84:87]\n" \
"s_add_u32 m0, s79, 32768\n" \
"ds_read_b64_tr_b16 a[68:69], v232 offset:57344\n" \
"v_mfma_f32_16x16x32_bf16 v[84:87], a[108:111], a[4:7], v[84:87]\n" \
"buffer_load_dwordx4 v228, s[52:55], s78 offen offset:0 lds\n" \
"v_mfma_f32_16x16x32_bf16 v[88:91], a[112:115], a[0:3], v[88:91]\n" \
"ds_read_b64_tr_b16 a[70:71], v232 offset:59392\n" \
"v_mfma_f32_16x16x32_bf16 v[88:91], a[116:119], a[4:7], v[88:91]\n" \
"ds_read_b64_tr_b16 a[72:73], v233 offset:49152\n" \
"v_mfma_f32_16x16x32_bf16 v[92:95], a[120:123], a[0:3], v[92:95]\n" \
"ds_read_b64_tr_b16 a[74:75], v233 offset:51200\n" \
"v_mfma_f32_16x16x32_bf16 v[92:95], a[124:127], a[4:7], v[92:95]\n" \
"buffer_load_dwordx4 v229, s[52:55], s78 offen offset:1024 lds\n" \
"v_mfma_f32_16x16x32_bf16 v[124:127], a[120:123], a[8:11], v[124:127]\n" \
"s_waitcnt vmcnt(36)\n" \
"ds_read_b128 a[0:3], v230 offset:32768\n" \
"v_mfma_f32_16x16x32_bf16 v[124:127], a[124:127], a[12:15], v[124:127]\n" \
"ds_read_b128 a[4:7], v231 offset:32768\n" \
"v_mfma_f32_16x16x32_bf16 v[120:123], a[112:115], a[8:11], v[120:123]\n" \
"s_xor_b32 s78, s78, 0x80000\n" \
"ds_read_b64_tr_b16 a[76:77], v233 offset:57344\n" \
"v_mfma_f32_16x16x32_bf16 v[120:123], a[116:119], a[12:15], v[120:123]\n" \
"buffer_load_dwordx4 v228, s[52:55], s89 offen offset:2048 lds\n" \
"v_mfma_f32_16x16x32_bf16 v[116:119], a[104:107], a[8:11], v[116:119]\n" \
"ds_read_b64_tr_b16 a[78:79], v233 offset:59392\n" \
"v_mfma_f32_16x16x32_bf16 v[116:119], a[108:111], a[12:15], v[116:119]\n" \
"ds_read_b64_tr_b16 a[80:81], v232 offset:49664\n" \
"v_mfma_f32_16x16x32_bf16 v[112:115], a[96:99], a[8:11], v[112:115]\n" \
"ds_read_b64_tr_b16 a[82:83], v232 offset:51712\n" \
"v_mfma_f32_16x16x32_bf16 v[112:115], a[100:103], a[12:15], v[112:115]\n" \
"buffer_load_dwordx4 v229, s[52:55], s89 offen offset:3072 lds\n" \
"v_mfma_f32_16x16x32_bf16 v[144:147], a[96:99], a[16:19], v[144:147]\n" \
"s_waitcnt vmcnt(36)\n" \
"s_xor_b32 s89, s89, 0x80000\n" \
"ds_read_b128 a[8:11], v230 offset:34816\n" \
"v_mfma_f32_16x16x32_bf16 v[144:147], a[100:103], a[20:23], v[144:147]\n" \
"s_add_u32 m0, s74, 98304\n" \
"ds_read_b128 a[12:15], v231 offset:34816\n" \
"v_mfma_f32_16x16x32_bf16 v[148:151], a[104:107], a[16:19], v[148:151]\n" \
"s_waitcnt lgkmcnt(6)\n" \
"ds_read_b64_tr_b16 a[84:85], v232 offset:57856\n" \
"v_mfma_f32_16x16x32_bf16 v[148:151], a[108:111], a[20:23], v[148:151]\n" \
"buffer_load_dwordx4 v224, s[48:51], s75 offen offset:0 lds\n" \
"v_mfma_f32_16x16x32_bf16 v[152:155], a[112:115], a[16:19], v[152:155]\n" \
"ds_read_b64_tr_b16 a[86:87], v232 offset:59904\n" \
"v_mfma_f32_16x16x32_bf16 v[152:155], a[116:119], a[20:23], v[152:155]\n" \
"ds_read_b64_tr_b16 a[88:89], v233 offset:49664\n" \
"v_mfma_f32_16x16x32_bf16 v[156:159], a[120:123], a[16:19], v[156:159]\n" \
"ds_read_b64_tr_b16 a[90:91], v233 offset:51712\n" \
"v_mfma_f32_16x16x32_bf16 v[156:159], a[124:127], a[20:23], v[156:159]\n" \
"buffer_load_dwordx4 v225, s[48:51], s75 offen offset:1024 lds\n" \
"v_mfma_f32_16x16x32_bf16 v[188:191], a[120:123], a[24:27], v[188:191]\n" \
"s_waitcnt vmcnt(36)\n" \
"ds_read_b128 a[16:19], v230 offset:36864\n" \
"v_mfma_f32_16x16x32_bf16 v[188:191], a[124:127], a[28:31], v[188:191]\n" \
"ds_read_b128 a[20:23], v231 offset:36864\n" \
"v_mfma_f32_16x16x32_bf16 v[184:187], a[112:115], a[24:27], v[184:187]\n" \
"s_waitcnt lgkmcnt(6)\n" \
"ds_read_b64_tr_b16 a[92:93], v233 offset:57856\n" \
"v_mfma_f32_16x16x32_bf16 v[184:187], a[116:119], a[28:31], v[184:187]\n" \
"buffer_load_dwordx4 v226, s[48:51], s75 offen offset:2048 lds\n" \
"v_mfma_f32_16x16x32_bf16 v[180:183], a[104:107], a[24:27], v[180:183]\n" \
"ds_read_b64_tr_b16 a[94:95], v233 offset:59904\n" \
"v_mfma_f32_16x16x32_bf16 v[180:183], a[108:111], a[28:31], v[180:183]\n" \
"v_mfma_f32_16x16x32_bf16 v[176:179], a[96:99], a[24:27], v[176:179]\n" \
"v_mfma_f32_16x16x32_bf16 v[176:179], a[100:103], a[28:31], v[176:179]\n" \
"buffer_load_dwordx4 v227, s[48:51], s75 offen offset:3072 lds\n" \
"v_mfma_f32_16x16x32_bf16 v[64:67], a[64:67], a[0:3], v[64:67]\n" \
"s_waitcnt vmcnt(36)\n" \
"s_xor_b32 s75, s75, 0x80\n" \
"ds_read_b128 a[24:27], v230 offset:38912\n" \
"v_mfma_f32_16x16x32_bf16 v[64:67], a[68:71], a[4:7], v[64:67]\n" \
"s_add_u32 m0, s74, 102400\n" \
"ds_read_b128 a[28:31], v231 offset:38912\n" \
"v_mfma_f32_16x16x32_bf16 v[68:71], a[72:75], a[0:3], v[68:71]\n" \
"s_waitcnt lgkmcnt(2)\n" \
"ds_read_b64_tr_b16 a[96:97], v232 offset:50176\n" \
"v_mfma_f32_16x16x32_bf16 v[68:71], a[76:79], a[4:7], v[68:71]\n" \
"buffer_load_dwordx4 v224, s[48:51], s77 offen offset:0 lds\n" \
"v_mfma_f32_16x16x32_bf16 v[72:75], a[80:83], a[0:3], v[72:75]\n" \
"ds_read_b64_tr_b16 a[98:99], v232 offset:52224\n" \
"v_mfma_f32_16x16x32_bf16 v[72:75], a[84:87], a[4:7], v[72:75]\n" \
"ds_read_b64_tr_b16 a[100:101], v232 offset:58368\n" \
"v_mfma_f32_16x16x32_bf16 v[76:79], a[88:91], a[0:3], v[76:79]\n" \
"ds_read_b64_tr_b16 a[102:103], v232 offset:60416\n" \
"v_mfma_f32_16x16x32_bf16 v[76:79], a[92:95], a[4:7], v[76:79]\n" \
"buffer_load_dwordx4 v225, s[48:51], s77 offen offset:1024 lds\n" \
"v_mfma_f32_16x16x32_bf16 v[108:111], a[88:91], a[8:11], v[108:111]\n" \
"ds_read_b64_tr_b16 a[104:105], v233 offset:50176\n" \
"v_mfma_f32_16x16x32_bf16 v[108:111], a[92:95], a[12:15], v[108:111]\n" \
"ds_read_b64_tr_b16 a[106:107], v233 offset:52224\n" \
"v_mfma_f32_16x16x32_bf16 v[104:107], a[80:83], a[8:11], v[104:107]\n" \
"s_waitcnt lgkmcnt(6)\n" \
"ds_read_b64_tr_b16 a[108:109], v233 offset:58368\n" \
"v_mfma_f32_16x16x32_bf16 v[104:107], a[84:87], a[12:15], v[104:107]\n" \
"buffer_load_dwordx4 v226, s[48:51], s77 offen offset:2048 lds\n" \
"v_mfma_f32_16x16x32_bf16 v[100:103], a[72:75], a[8:11], v[100:103]\n" \
"ds_read_b64_tr_b16 a[110:111], v233 offset:60416\n" \
"v_mfma_f32_16x16x32_bf16 v[100:103], a[76:79], a[12:15], v[100:103]\n" \
"ds_read_b64_tr_b16 a[112:113], v232 offset:50688\n" \
"v_mfma_f32_16x16x32_bf16 v[96:99], a[64:67], a[8:11], v[96:99]\n" \
"ds_read_b64_tr_b16 a[114:115], v232 offset:52736\n" \
"v_mfma_f32_16x16x32_bf16 v[96:99], a[68:71], a[12:15], v[96:99]\n" \
"buffer_load_dwordx4 v227, s[48:51], s77 offen offset:3072 lds\n" \
"v_mfma_f32_16x16x32_bf16 v[128:131], a[64:67], a[16:19], v[128:131]\n" \
"ds_read_b64_tr_b16 a[116:117], v232 offset:58880\n" \
"v_mfma_f32_16x16x32_bf16 v[128:131], a[68:71], a[20:23], v[128:131]\n" \
"ds_read_b64_tr_b16 a[118:119], v232 offset:60928\n" \
"v_mfma_f32_16x16x32_bf16 v[132:135], a[72:75], a[16:19], v[132:135]\n" \
"s_xor_b32 s77, s77, 0x80\n" \
"s_add_u32 s93, s87, 17850368\n" \
"ds_read_b64_tr_b16 a[120:121], v233 offset:50688\n" \
"v_mfma_f32_16x16x32_bf16 v[132:135], a[76:79], a[20:23], v[132:135]\n" \
"buffer_store_dwordx4 a[192:195], v234, s[56:59], s93 offen offset:0 sc1\n" \
"v_mfma_f32_16x16x32_bf16 v[136:139], a[80:83], a[16:19], v[136:139]\n" \
"ds_read_b64_tr_b16 a[122:123], v233 offset:52736\n" \
"v_mfma_f32_16x16x32_bf16 v[136:139], a[84:87], a[20:23], v[136:139]\n" \
"ds_read_b64_tr_b16 a[124:125], v233 offset:58880\n" \
"v_mfma_f32_16x16x32_bf16 v[140:143], a[88:91], a[16:19], v[140:143]\n" \
"ds_read_b64_tr_b16 a[126:127], v233 offset:60928\n" \
"v_mfma_f32_16x16x32_bf16 v[140:143], a[92:95], a[20:23], v[140:143]\n" \
"v_mfma_f32_16x16x32_bf16 v[172:175], a[88:91], a[24:27], v[172:175]\n" \
"v_mfma_f32_16x16x32_bf16 v[172:175], a[92:95], a[28:31], v[172:175]\n" \
"v_mfma_f32_16x16x32_bf16 v[168:171], a[80:83], a[24:27], v[168:171]\n" \
"v_mfma_f32_16x16x32_bf16 v[168:171], a[84:87], a[28:31], v[168:171]\n" \
"buffer_store_dwordx4 a[196:199], v234, s[56:59], s93 offen offset:1024 sc1\n" \
"v_mfma_f32_16x16x32_bf16 v[164:167], a[72:75], a[24:27], v[164:167]\n" \
"v_mfma_f32_16x16x32_bf16 v[164:167], a[76:79], a[28:31], v[164:167]\n" \
"v_mfma_f32_16x16x32_bf16 v[160:163], a[64:67], a[24:27], v[160:163]\n" \
"v_mfma_f32_16x16x32_bf16 v[160:163], a[68:71], a[28:31], v[160:163]\n" \
"s_waitcnt vmcnt(37) lgkmcnt(0)\n" \
"s_barrier\n" \
"v_mfma_f32_16x16x32_bf16 v[80:83], a[96:99], a[0:3], v[80:83]\n" \
"ds_read_b64_tr_b16 a[64:65], v232 offset:0\n" \
"v_mfma_f32_16x16x32_bf16 v[80:83], a[100:103], a[4:7], v[80:83]\n" \
"ds_read_b64_tr_b16 a[66:67], v232 offset:2048\n" \
"v_mfma_f32_16x16x32_bf16 v[84:87], a[104:107], a[0:3], v[84:87]\n" \
"s_add_u32 m0, s79, 49152\n" \
"ds_read_b64_tr_b16 a[68:69], v232 offset:8192\n" \
"v_mfma_f32_16x16x32_bf16 v[84:87], a[108:111], a[4:7], v[84:87]\n" \
"buffer_load_dwordx4 v228, s[52:55], s78 offen offset:0 lds\n" \
"v_mfma_f32_16x16x32_bf16 v[88:91], a[112:115], a[0:3], v[88:91]\n" \
"s_mul_i32 s73, s62, 557824\n" \
"s_add_u32 s85, s73, 17850368\n" \
"ds_read_b64_tr_b16 a[70:71], v232 offset:10240\n" \
"v_mfma_f32_16x16x32_bf16 v[88:91], a[116:119], a[4:7], v[88:91]\n" \
"s_mul_i32 s83, s63, 34816\n" \
"s_sub_u32 s86, 557056, s83\n" \
"ds_read_b64_tr_b16 a[72:73], v233 offset:0\n" \
"v_mfma_f32_16x16x32_bf16 v[92:95], a[120:123], a[0:3], v[92:95]\n" \
"ds_read_b64_tr_b16 a[74:75], v233 offset:2048\n" \
"v_add_u32 v237, %3, v236\n" \
"v_mfma_f32_16x16x32_bf16 v[92:95], a[124:127], a[4:7], v[92:95]\n" \
"buffer_load_dwordx4 v229, s[52:55], s78 offen offset:1024 lds\n" \
"v_mfma_f32_16x16x32_bf16 v[124:127], a[120:123], a[8:11], v[124:127]\n" \
"s_waitcnt vmcnt(37)\n" \
"ds_read_b128 a[0:3], v250 offset:0\n" \
"v_mfma_f32_16x16x32_bf16 v[124:127], a[124:127], a[12:15], v[124:127]\n" \
"s_add_u32 s36, s56, s73\n" \
"s_addc_u32 s37, s57, 0\n" \
"ds_read_b128 a[4:7], v251 offset:0\n" \
"v_mfma_f32_16x16x32_bf16 v[120:123], a[112:115], a[8:11], v[120:123]\n" \
"s_mov_b32 s38, s86\n" \
"s_mov_b32 s39, 0x20000\n" \
"ds_read_b64_tr_b16 a[76:77], v233 offset:8192\n" \
"v_mfma_f32_16x16x32_bf16 v[120:123], a[116:119], a[12:15], v[120:123]\n" \
"buffer_load_dwordx4 v228, s[52:55], s89 offen offset:2048 lds\n" \
"v_mfma_f32_16x16x32_bf16 v[116:119], a[104:107], a[8:11], v[116:119]\n" \
"ds_read_b64_tr_b16 a[78:79], v233 offset:10240\n" \
"v_add_u32 v235, %3, v234\n" \
"v_mfma_f32_16x16x32_bf16 v[116:119], a[108:111], a[12:15], v[116:119]\n" \
"s_add_u32 s40, s56, s85\n" \
"s_addc_u32 s41, s57, 0\n" \
"ds_read_b64_tr_b16 a[80:81], v232 offset:512\n" \
"v_mfma_f32_16x16x32_bf16 v[112:115], a[96:99], a[8:11], v[112:115]\n" \
"s_mov_b32 s42, s86\n" \
"s_mov_b32 s43, 0x20000\n" \
"ds_read_b64_tr_b16 a[82:83], v232 offset:2560\n" \
"v_mfma_f32_16x16x32_bf16 v[112:115], a[100:103], a[12:15], v[112:115]\n" \
"buffer_load_dwordx4 v229, s[52:55], s89 offen offset:3072 lds\n" \
"v_mfma_f32_16x16x32_bf16 v[144:147], a[96:99], a[16:19], v[144:147]\n" \
"s_waitcnt vmcnt(37)\n" \
"s_add_u32 s85, s74, 131072\n" \
"ds_read_b128 a[8:11], v250 offset:2048\n" \
"v_mfma_f32_16x16x32_bf16 v[144:147], a[100:103], a[20:23], v[144:147]\n" \
"s_add_u32 m0, s74, 131072\n" \
"ds_read_b128 a[12:15], v251 offset:2048\n" \
"v_add_u32 v239, s85, v234\n" \
"v_mfma_f32_16x16x32_bf16 v[148:151], a[104:107], a[16:19], v[148:151]\n" \
"s_waitcnt lgkmcnt(6)\n" \
"s_lshl_b32 s73, s61, 12\n" \
"ds_read_b64_tr_b16 a[84:85], v232 offset:8704\n" \
"v_mfma_f32_16x16x32_bf16 v[148:151], a[108:111], a[20:23], v[148:151]\n" \
"buffer_load_dwordx4 v224, s[48:51], s75 offen offset:0 lds\n" \
"v_mfma_f32_16x16x32_bf16 v[152:155], a[112:115], a[16:19], v[152:155]\n" \
"s_add_u32 s80, s79, 0x10000\n" \
"s_add_u32 s83, s83, s73\n" \
"ds_read_b64_tr_b16 a[86:87], v232 offset:10752\n" \
"v_mfma_f32_16x16x32_bf16 v[152:155], a[116:119], a[20:23], v[152:155]\n" \
"s_cmp_eq_u32 s63, 0\n" \
"s_cselect_b32 s85, 34816, 0\n" \
"ds_read_b64_tr_b16 a[88:89], v233 offset:512\n" \
"v_mfma_f32_16x16x32_bf16 v[156:159], a[120:123], a[16:19], v[156:159]\n" \
"s_add_u32 s73, s73, s85\n" \
"ds_read_b64_tr_b16 a[90:91], v233 offset:2560\n" \
"v_mfma_f32_16x16x32_bf16 v[156:159], a[124:127], a[20:23], v[156:159]\n" \
"buffer_load_dwordx4 v225, s[48:51], s75 offen offset:1024 lds\n" \
"v_mfma_f32_16x16x32_bf16 v[188:191], a[120:123], a[24:27], v[188:191]\n" \
"s_waitcnt vmcnt(37)\n" \
"ds_read_b128 a[16:19], v250 offset:4096\n" \
"v_mfma_f32_16x16x32_bf16 v[188:191], a[124:127], a[28:31], v[188:191]\n" \
"ds_read_b128 a[20:23], v251 offset:4096\n" \
"v_mfma_f32_16x16x32_bf16 v[184:187], a[112:115], a[24:27], v[184:187]\n" \
"s_waitcnt lgkmcnt(6)\n" \
"ds_read_b64_tr_b16 a[92:93], v233 offset:8704\n" \
"v_mfma_f32_16x16x32_bf16 v[184:187], a[116:119], a[28:31], v[184:187]\n" \
"buffer_load_dwordx4 v226, s[48:51], s75 offen offset:2048 lds\n" \
"v_mfma_f32_16x16x32_bf16 v[180:183], a[104:107], a[24:27], v[180:183]\n" \
"ds_read_b64_tr_b16 a[94:95], v233 offset:10752\n" \
"v_mfma_f32_16x16x32_bf16 v[180:183], a[108:111], a[28:31], v[180:183]\n" \
"v_mfma_f32_16x16x32_bf16 v[176:179], a[96:99], a[24:27], v[176:179]\n" \
"v_mfma_f32_16x16x32_bf16 v[176:179], a[100:103], a[28:31], v[176:179]\n" \
"buffer_load_dwordx4 v227, s[48:51], s75 offen offset:3072 lds\n" \
"v_mfma_f32_16x16x32_bf16 v[64:67], a[64:67], a[0:3], v[64:67]\n" \
"s_waitcnt vmcnt(37)\n" \
"ds_read_b128 a[24:27], v250 offset:6144\n" \
"v_mfma_f32_16x16x32_bf16 v[64:67], a[68:71], a[4:7], v[64:67]\n" \
"s_add_u32 m0, s74, 135168\n" \
"ds_read_b128 a[28:31], v251 offset:6144\n" \
"v_mfma_f32_16x16x32_bf16 v[68:71], a[72:75], a[0:3], v[68:71]\n" \
"s_waitcnt lgkmcnt(2)\n" \
"ds_read_b64_tr_b16 a[96:97], v232 offset:1024\n" \
"v_mfma_f32_16x16x32_bf16 v[68:71], a[76:79], a[4:7], v[68:71]\n" \
"buffer_load_dwordx4 v224, s[48:51], s77 offen offset:0 lds\n" \
"v_mfma_f32_16x16x32_bf16 v[72:75], a[80:83], a[0:3], v[72:75]\n" \
"ds_read_b64_tr_b16 a[98:99], v232 offset:3072\n" \
"v_mfma_f32_16x16x32_bf16 v[72:75], a[84:87], a[4:7], v[72:75]\n" \
"ds_read_b64_tr_b16 a[100:101], v232 offset:9216\n" \
"v_mfma_f32_16x16x32_bf16 v[76:79], a[88:91], a[0:3], v[76:79]\n" \
"ds_read_b64_tr_b16 a[102:103], v232 offset:11264\n" \
"v_mfma_f32_16x16x32_bf16 v[76:79], a[92:95], a[4:7], v[76:79]\n" \
"buffer_load_dwordx4 v225, s[48:51], s77 offen offset:1024 lds\n" \
"v_mfma_f32_16x16x32_bf16 v[108:111], a[88:91], a[8:11], v[108:111]\n" \
"ds_read_b64_tr_b16 a[104:105], v233 offset:1024\n" \
"v_mfma_f32_16x16x32_bf16 v[108:111], a[92:95], a[12:15], v[108:111]\n" \
"ds_read_b64_tr_b16 a[106:107], v233 offset:3072\n" \
"v_mfma_f32_16x16x32_bf16 v[104:107], a[80:83], a[8:11], v[104:107]\n" \
"s_waitcnt lgkmcnt(6)\n" \
"ds_read_b64_tr_b16 a[108:109], v233 offset:9216\n" \
"v_mfma_f32_16x16x32_bf16 v[104:107], a[84:87], a[12:15], v[104:107]\n" \
"buffer_load_dwordx4 v226, s[48:51], s77 offen offset:2048 lds\n" \
"v_mfma_f32_16x16x32_bf16 v[100:103], a[72:75], a[8:11], v[100:103]\n" \
"ds_read_b64_tr_b16 a[110:111], v233 offset:11264\n" \
"v_mfma_f32_16x16x32_bf16 v[100:103], a[76:79], a[12:15], v[100:103]\n" \
"ds_read_b64_tr_b16 a[112:113], v232 offset:1536\n" \
"v_mfma_f32_16x16x32_bf16 v[96:99], a[64:67], a[8:11], v[96:99]\n" \
"ds_read_b64_tr_b16 a[114:115], v232 offset:3584\n" \
"v_mfma_f32_16x16x32_bf16 v[96:99], a[68:71], a[12:15], v[96:99]\n" \
"buffer_load_dwordx4 v227, s[48:51], s77 offen offset:3072 lds\n" \
"v_mfma_f32_16x16x32_bf16 v[128:131], a[64:67], a[16:19], v[128:131]\n" \
"ds_read_b64_tr_b16 a[116:117], v232 offset:9728\n" \
"v_mfma_f32_16x16x32_bf16 v[128:131], a[68:71], a[20:23], v[128:131]\n" \
"ds_read_b64_tr_b16 a[118:119], v232 offset:11776\n" \
"v_mfma_f32_16x16x32_bf16 v[132:135], a[72:75], a[16:19], v[132:135]\n" \
"ds_read_b64_tr_b16 a[120:121], v233 offset:1536\n" \
"v_mfma_f32_16x16x32_bf16 v[132:135], a[76:79], a[20:23], v[132:135]\n" \
"buffer_store_dwordx4 a[200:203], v234, s[56:59], s93 offen offset:2048 sc1\n" \
"v_mfma_f32_16x16x32_bf16 v[136:139], a[80:83], a[16:19], v[136:139]\n" \
"ds_read_b64_tr_b16 a[122:123], v233 offset:3584\n" \
"v_mfma_f32_16x16x32_bf16 v[136:139], a[84:87], a[20:23], v[136:139]\n" \
"ds_read_b64_tr_b16 a[124:125], v233 offset:9728\n" \
"v_mfma_f32_16x16x32_bf16 v[140:143], a[88:91], a[16:19], v[140:143]\n" \
"ds_read_b64_tr_b16 a[126:127], v233 offset:11776\n" \
"v_mfma_f32_16x16x32_bf16 v[140:143], a[92:95], a[20:23], v[140:143]\n" \
"v_mfma_f32_16x16x32_bf16 v[172:175], a[88:91], a[24:27], v[172:175]\n" \
"v_mfma_f32_16x16x32_bf16 v[172:175], a[92:95], a[28:31], v[172:175]\n" \
"v_mfma_f32_16x16x32_bf16 v[168:171], a[80:83], a[24:27], v[168:171]\n" \
"v_mfma_f32_16x16x32_bf16 v[168:171], a[84:87], a[28:31], v[168:171]\n" \
"buffer_store_dwordx4 a[204:207], v234, s[56:59], s93 offen offset:3072 sc1\n" \
"v_mfma_f32_16x16x32_bf16 v[164:167], a[72:75], a[24:27], v[164:167]\n" \
"v_mfma_f32_16x16x32_bf16 v[164:167], a[76:79], a[28:31], v[164:167]\n" \
"v_mfma_f32_16x16x32_bf16 v[160:163], a[64:67], a[24:27], v[160:163]\n" \
"v_mfma_f32_16x16x32_bf16 v[160:163], a[68:71], a[28:31], v[160:163]\n" \
"s_waitcnt vmcnt(38) lgkmcnt(0)\n" \
"s_barrier\n" \
"v_mfma_f32_16x16x32_bf16 v[80:83], a[96:99], a[0:3], v[80:83]\n" \
"s_lshr_b32 s71, s61, 1\n" \
"s_lshl_b32 s71, s71, 14\n" \
"ds_read_b64_tr_b16 a[64:65], v232 offset:16384\n" \
"v_mfma_f32_16x16x32_bf16 v[80:83], a[100:103], a[4:7], v[80:83]\n" \
"s_xor_b32 s71, s71, 0x4000\n" \
"s_and_b32 s70, s61, 1\n" \
"ds_read_b64_tr_b16 a[66:67], v232 offset:18432\n" \
"v_mfma_f32_16x16x32_bf16 v[84:87], a[104:107], a[0:3], v[84:87]\n" \
"s_lshl_b32 s70, s70, 12\n" \
"s_add_u32 s71, s71, s70\n" \
"ds_read_b64_tr_b16 a[68:69], v232 offset:24576\n" \
"v_mfma_f32_16x16x32_bf16 v[84:87], a[108:111], a[4:7], v[84:87]\n" \
"v_add_u32 v224, s71, v237\n" \
"s_and_b32 s70, s63, 1\n" \
"v_mfma_f32_16x16x32_bf16 v[88:91], a[112:115], a[0:3], v[88:91]\n" \
"s_lshl_b32 s70, s70, 15\n" \
"s_xor_b32 s70, s70, 0x18000\n" \
"ds_read_b64_tr_b16 a[70:71], v232 offset:26624\n" \
"v_mfma_f32_16x16x32_bf16 v[88:91], a[116:119], a[4:7], v[88:91]\n" \
"s_add_u32 s71, s71, s70\n" \
"ds_read_b64_tr_b16 a[72:73], v233 offset:16384\n" \
"v_add_u32 v225, s71, v235\n" \
"v_mfma_f32_16x16x32_bf16 v[92:95], a[120:123], a[0:3], v[92:95]\n" \
"s_lshr_b32 s64, s22, 7\n" \
"s_sub_u32 s64, 3, s64\n" \
"ds_read_b64_tr_b16 a[74:75], v233 offset:18432\n" \
"v_mfma_f32_16x16x32_bf16 v[92:95], a[124:127], a[4:7], v[92:95]\n" \
"s_and_b32 s64, s64, 7\n" \
"s_and_b32 s65, %1, 1\n" \
"s_lshl_b32 s65, s65, 3\n" \
"v_mfma_f32_16x16x32_bf16 v[124:127], a[120:123], a[8:11], v[124:127]\n" \
"s_waitcnt vmcnt(36)\n" \
"s_add_u32 s64, s64, s65\n" \
"ds_read_b128 a[0:3], v230 offset:0\n" \
"v_mfma_f32_16x16x32_bf16 v[124:127], a[124:127], a[12:15], v[124:127]\n" \
"s_lshl_b32 s64, s64, 7\n" \
"s_lshr_b32 s65, %1, 3\n" \
"ds_read_b128 a[4:7], v231 offset:0\n" \
"v_mfma_f32_16x16x32_bf16 v[120:123], a[112:115], a[8:11], v[120:123]\n" \
"s_lshl_b32 s65, s65, 2\n" \
"s_add_u32 s65, s65, s61\n" \
"ds_read_b64_tr_b16 a[76:77], v233 offset:24576\n" \
"v_mfma_f32_16x16x32_bf16 v[120:123], a[116:119], a[12:15], v[120:123]\n" \
"s_mul_i32 s65, s65, 24576\n" \
"s_add_u32 s64, s64, s65\n" \
"v_bfe_u32 v254, v234, 7, 2\n" \
"v_mfma_f32_16x16x32_bf16 v[116:119], a[104:107], a[8:11], v[116:119]\n" \
"ds_read_b64_tr_b16 a[78:79], v233 offset:26624\n" \
"v_mul_u32_u24 v254, 3, v254\n" \
"v_mfma_f32_16x16x32_bf16 v[116:119], a[108:111], a[12:15], v[116:119]\n" \
"ds_read_b64_tr_b16 a[80:81], v232 offset:16896\n" \
"v_bfe_u32 v243, v234, 5, 2\n" \
"v_mfma_f32_16x16x32_bf16 v[112:115], a[96:99], a[8:11], v[112:115]\n" \
"s_add_u32 s93, s94, 8192\n" \
"ds_read_b64_tr_b16 a[82:83], v232 offset:18944\n" \
"v_min_u32 v243, 2, v243\n" \
"v_mfma_f32_16x16x32_bf16 v[112:115], a[100:103], a[12:15], v[112:115]\n" \
"buffer_store_dwordx4 v[32:35], v236, s[56:59], s93 offen offset:0 sc1\n" \
"v_mfma_f32_16x16x32_bf16 v[144:147], a[96:99], a[16:19], v[144:147]\n" \
"s_waitcnt vmcnt(35)\n" \
"ds_read_b128 a[8:11], v230 offset:2048\n" \
"v_add_u32 v254, v254, v243\n" \
"v_mfma_f32_16x16x32_bf16 v[144:147], a[100:103], a[20:23], v[144:147]\n" \
"ds_read_b128 a[12:15], v231 offset:2048\n" \
"v_and_b32 v243, 16, v234\n" \
"v_mfma_f32_16x16x32_bf16 v[148:151], a[104:107], a[16:19], v[148:151]\n" \
"ds_read_b64_tr_b16 a[84:85], v232 offset:25088\n" \
"v_lshl_add_u32 v243, v243, 2, s64\n" \
"v_mfma_f32_16x16x32_bf16 v[148:151], a[108:111], a[20:23], v[148:151]\n" \
"v_lshl_add_u32 v254, v254, 11, v243\n" \
"v_mfma_f32_16x16x32_bf16 v[152:155], a[112:115], a[16:19], v[152:155]\n" \
"ds_read_b64_tr_b16 a[86:87], v232 offset:27136\n" \
"v_mfma_f32_16x16x32_bf16 v[152:155], a[116:119], a[20:23], v[152:155]\n" \
"ds_read_b64_tr_b16 a[88:89], v233 offset:16896\n" \
"v_mfma_f32_16x16x32_bf16 v[156:159], a[120:123], a[16:19], v[156:159]\n" \
"ds_read_b64_tr_b16 a[90:91], v233 offset:18944\n" \
"v_mfma_f32_16x16x32_bf16 v[156:159], a[124:127], a[20:23], v[156:159]\n" \
"v_mfma_f32_16x16x32_bf16 v[188:191], a[120:123], a[24:27], v[188:191]\n" \
"s_waitcnt vmcnt(33)\n" \
"ds_read_b128 a[16:19], v230 offset:4096\n" \
"v_mfma_f32_16x16x32_bf16 v[188:191], a[124:127], a[28:31], v[188:191]\n" \
"ds_read_b128 a[20:23], v231 offset:4096\n" \
"v_mfma_f32_16x16x32_bf16 v[184:187], a[112:115], a[24:27], v[184:187]\n" \
"ds_read_b64_tr_b16 a[92:93], v233 offset:25088\n" \
"v_mfma_f32_16x16x32_bf16 v[184:187], a[116:119], a[28:31], v[184:187]\n" \
"v_mfma_f32_16x16x32_bf16 v[180:183], a[104:107], a[24:27], v[180:183]\n" \
"ds_read_b64_tr_b16 a[94:95], v233 offset:27136\n" \
"v_mfma_f32_16x16x32_bf16 v[180:183], a[108:111], a[28:31], v[180:183]\n" \
"v_mfma_f32_16x16x32_bf16 v[176:179], a[96:99], a[24:27], v[176:179]\n" \
"buffer_store_dwordx4 v[48:51], v236, s[56:59], s93 offen offset:256 sc1\n" \
"v_mfma_f32_16x16x32_bf16 v[176:179], a[100:103], a[28:31], v[176:179]\n" \
"s_waitcnt lgkmcnt(1)\n" \
"v_mfma_f32_16x16x32_bf16 v[64:67], a[64:67], a[0:3], v[64:67]\n" \
"s_waitcnt vmcnt(32)\n" \
"s_lshl_b32 s68, s63, 2\n" \
"ds_read_b128 a[24:27], v230 offset:6144\n" \
"Lpg4447_%=:\n" \
"s_cbranch_execz Lpg6183_%=\n" \
"v_mfma_f32_16x16x32_bf16 v[64:67], a[68:71], a[4:7], v[64:67]\n" \
"s_add_u32 s68, s68, s90\n" \
"ds_read_b128 a[28:31], v231 offset:6144\n" \
"v_mfma_f32_16x16x32_bf16 v[68:71], a[72:75], a[0:3], v[68:71]\n" \
"ds_read_b64_tr_b16 a[96:97], v232 offset:17408\n" \
"v_mfma_f32_16x16x32_bf16 v[68:71], a[76:79], a[4:7], v[68:71]\n" \
"v_mov_b32 v228, s60\n" \
"v_lshlrev_b32 v229, 28, s61\n" \
"v_mfma_f32_16x16x32_bf16 v[72:75], a[80:83], a[0:3], v[72:75]\n" \
"ds_read_b64_tr_b16 a[98:99], v232 offset:19456\n" \
"v_mfma_f32_16x16x32_bf16 v[72:75], a[84:87], a[4:7], v[72:75]\n" \
"s_waitcnt lgkmcnt(3)\n" \
"ds_read_b64_tr_b16 a[100:101], v232 offset:25600\n" \
"v_mfma_f32_16x16x32_bf16 v[76:79], a[88:91], a[0:3], v[76:79]\n" \
"ds_read_b64_tr_b16 a[102:103], v232 offset:27648\n" \
"v_mfma_f32_16x16x32_bf16 v[76:79], a[92:95], a[4:7], v[76:79]\n" \
"v_mfma_f32_16x16x32_bf16 v[108:111], a[88:91], a[8:11], v[108:111]\n" \
"ds_read_b64_tr_b16 a[104:105], v233 offset:17408\n" \
"v_mfma_f32_16x16x32_bf16 v[108:111], a[92:95], a[12:15], v[108:111]\n" \
"ds_read_b64_tr_b16 a[106:107], v233 offset:19456\n" \
"v_mfma_f32_16x16x32_bf16 v[104:107], a[80:83], a[8:11], v[104:107]\n" \
"ds_read_b64_tr_b16 a[108:109], v233 offset:25600\n" \
"v_mfma_f32_16x16x32_bf16 v[104:107], a[84:87], a[12:15], v[104:107]\n" \
"v_mfma_f32_16x16x32_bf16 v[100:103], a[72:75], a[8:11], v[100:103]\n" \
"ds_read_b64_tr_b16 a[110:111], v233 offset:27648\n" \
"v_mfma_f32_16x16x32_bf16 v[100:103], a[76:79], a[12:15], v[100:103]\n" \
"ds_read_b64_tr_b16 a[112:113], v232 offset:17920\n" \
"v_mfma_f32_16x16x32_bf16 v[96:99], a[64:67], a[8:11], v[96:99]\n" \
"ds_read_b64_tr_b16 a[114:115], v232 offset:19968\n" \
"v_mfma_f32_16x16x32_bf16 v[96:99], a[68:71], a[12:15], v[96:99]\n" \
"v_mfma_f32_16x16x32_bf16 v[128:131], a[64:67], a[16:19], v[128:131]\n" \
"ds_read_b64_tr_b16 a[116:117], v232 offset:26112\n" \
"v_mfma_f32_16x16x32_bf16 v[128:131], a[68:71], a[20:23], v[128:131]\n" \
"ds_read_b64_tr_b16 a[118:119], v232 offset:28160\n" \
"v_mfma_f32_16x16x32_bf16 v[132:135], a[72:75], a[16:19], v[132:135]\n" \
"ds_read_b64_tr_b16 a[120:121], v233 offset:17920\n" \
"v_mfma_f32_16x16x32_bf16 v[132:135], a[76:79], a[20:23], v[132:135]\n" \
"buffer_store_dwordx4 v[40:43], v236, s[56:59], s93 offen offset:2048 sc1\n" \
"v_mfma_f32_16x16x32_bf16 v[136:139], a[80:83], a[16:19], v[136:139]\n" \
"ds_read_b64_tr_b16 a[122:123], v233 offset:19968\n" \
"v_mfma_f32_16x16x32_bf16 v[136:139], a[84:87], a[20:23], v[136:139]\n" \
"ds_read_b64_tr_b16 a[124:125], v233 offset:26112\n" \
"v_mfma_f32_16x16x32_bf16 v[140:143], a[88:91], a[16:19], v[140:143]\n" \
"ds_read_b64_tr_b16 a[126:127], v233 offset:28160\n" \
"v_mfma_f32_16x16x32_bf16 v[140:143], a[92:95], a[20:23], v[140:143]\n" \
"s_waitcnt lgkmcnt(3)\n" \
"v_mfma_f32_16x16x32_bf16 v[172:175], a[88:91], a[24:27], v[172:175]\n" \
"v_mfma_f32_16x16x32_bf16 v[172:175], a[92:95], a[28:31], v[172:175]\n" \
"v_mfma_f32_16x16x32_bf16 v[168:171], a[80:83], a[24:27], v[168:171]\n" \
"v_mfma_f32_16x16x32_bf16 v[168:171], a[84:87], a[28:31], v[168:171]\n" \
"v_mfma_f32_16x16x32_bf16 v[164:167], a[72:75], a[24:27], v[164:167]\n" \
"v_mfma_f32_16x16x32_bf16 v[164:167], a[76:79], a[28:31], v[164:167]\n" \
"v_mfma_f32_16x16x32_bf16 v[160:163], a[64:67], a[24:27], v[160:163]\n" \
"v_mfma_f32_16x16x32_bf16 v[160:163], a[68:71], a[28:31], v[160:163]\n" \
"s_waitcnt vmcnt(3) lgkmcnt(0)\n" \
"s_barrier\n" \
"v_mfma_f32_16x16x32_bf16 v[80:83], a[96:99], a[0:3], v[80:83]\n" \
"ds_read_b64_tr_b16 a[64:65], v232 offset:32768\n" \
"v_mfma_f32_16x16x32_bf16 v[80:83], a[100:103], a[4:7], v[80:83]\n" \
"ds_read_b64_tr_b16 a[66:67], v232 offset:34816\n" \
"buffer_store_dword v228, v229, s[56:59], s68 offen sc1\n" \
"v_mfma_f32_16x16x32_bf16 v[84:87], a[104:107], a[0:3], v[84:87]\n" \
"ds_read_b64_tr_b16 a[68:69], v232 offset:40960\n" \
"v_lshrrev_b32 v238, 4, v234\n" \
"v_mfma_f32_16x16x32_bf16 v[84:87], a[108:111], a[4:7], v[84:87]\n" \
"buffer_store_dwordx4 v[56:59], v236, s[56:59], s93 offen offset:2304 sc1\n" \
"v_mfma_f32_16x16x32_bf16 v[88:91], a[112:115], a[0:3], v[88:91]\n" \
"ds_read_b64_tr_b16 a[70:71], v232 offset:43008\n" \
"v_lshrrev_b32 v240, 4, v238\n" \
"v_mfma_f32_16x16x32_bf16 v[88:91], a[116:119], a[4:7], v[88:91]\n" \
"ds_read_b64_tr_b16 a[72:73], v233 offset:32768\n" \
"v_lshlrev_b32 v240, 2, v240\n" \
"v_mfma_f32_16x16x32_bf16 v[92:95], a[120:123], a[0:3], v[92:95]\n" \
"ds_read_b64_tr_b16 a[74:75], v233 offset:34816\n" \
"v_and_b32 v238, 15, v238\n" \
"v_mfma_f32_16x16x32_bf16 v[92:95], a[124:127], a[4:7], v[92:95]\n" \
"ds_write_b128 v224, v[192:195] offset:8192\n" \
"v_mfma_f32_16x16x32_bf16 v[124:127], a[120:123], a[8:11], v[124:127]\n" \
"ds_read_b128 a[0:3], v230 offset:32768\n" \
"v_sub_u32 v238, v238, v240\n" \
"v_mfma_f32_16x16x32_bf16 v[124:127], a[124:127], a[12:15], v[124:127]\n" \
"ds_read_b128 a[4:7], v231 offset:32768\n" \
"v_subrev_u32 v248, 0, v238\n" \
"v_mfma_f32_16x16x32_bf16 v[120:123], a[112:115], a[8:11], v[120:123]\n" \
"ds_read_b64_tr_b16 a[76:77], v233 offset:40960\n" \
"v_subrev_u32 v249, 1, v238\n" \
"v_mfma_f32_16x16x32_bf16 v[120:123], a[116:119], a[12:15], v[120:123]\n" \
"ds_write_b128 v224, v[208:211] offset:8448\n" \
"v_mfma_f32_16x16x32_bf16 v[116:119], a[104:107], a[8:11], v[116:119]\n" \
"ds_read_b64_tr_b16 a[78:79], v233 offset:43008\n" \
"v_subrev_u32 v250, 2, v238\n" \
"v_mfma_f32_16x16x32_bf16 v[116:119], a[108:111], a[12:15], v[116:119]\n" \
"ds_read_b64_tr_b16 a[80:81], v232 offset:33280\n" \
"v_subrev_u32 v251, 3, v238\n" \
"v_mfma_f32_16x16x32_bf16 v[112:115], a[96:99], a[8:11], v[112:115]\n" \
"ds_read_b64_tr_b16 a[82:83], v232 offset:35328\n" \
"v_ashrrev_i32 v248, 31, v248\n" \
"v_mfma_f32_16x16x32_bf16 v[112:115], a[100:103], a[12:15], v[112:115]\n" \
"v_ashrrev_i32 v249, 31, v249\n" \
"v_ashrrev_i32 v250, 31, v250\n" \
"v_mfma_f32_16x16x32_bf16 v[144:147], a[96:99], a[16:19], v[144:147]\n" \
"ds_read_b128 a[8:11], v230 offset:34816\n" \
"v_ashrrev_i32 v251, 31, v251\n" \
"v_mfma_f32_16x16x32_bf16 v[144:147], a[100:103], a[20:23], v[144:147]\n" \
"ds_read_b128 a[12:15], v231 offset:34816\n" \
"v_lshlrev_b32 v248, 23, v248\n" \
"v_mfma_f32_16x16x32_bf16 v[148:151], a[104:107], a[16:19], v[148:151]\n" \
"ds_read_b64_tr_b16 a[84:85], v232 offset:41472\n" \
"v_lshlrev_b32 v249, 23, v249\n" \
"v_mfma_f32_16x16x32_bf16 v[148:151], a[108:111], a[20:23], v[148:151]\n" \
"ds_write_b128 v224, v[200:203] offset:10240\n" \
"v_mfma_f32_16x16x32_bf16 v[152:155], a[112:115], a[16:19], v[152:155]\n" \
"ds_read_b64_tr_b16 a[86:87], v232 offset:43520\n" \
"v_lshlrev_b32 v250, 23, v250\n" \
"v_mfma_f32_16x16x32_bf16 v[152:155], a[116:119], a[20:23], v[152:155]\n" \
"ds_read_b64_tr_b16 a[88:89], v233 offset:33280\n" \
"v_lshlrev_b32 v251, 23, v251\n" \
"v_mfma_f32_16x16x32_bf16 v[156:159], a[120:123], a[16:19], v[156:159]\n" \
"s_add_u32 s93, s94, 0\n" \
"ds_read_b64_tr_b16 a[90:91], v233 offset:35328\n" \
"v_mov_b32 v240, 0xff800000\n" \
"v_mfma_f32_16x16x32_bf16 v[156:159], a[124:127], a[20:23], v[156:159]\n" \
"buffer_store_dwordx4 v[36:39], v236, s[56:59], s93 offen offset:0 sc1\n" \
"v_mfma_f32_16x16x32_bf16 v[188:191], a[120:123], a[24:27], v[188:191]\n" \
"ds_read_b128 a[16:19], v230 offset:36864\n" \
"v_add_f32 v248, 0x42400000, v248\n" \
"v_mfma_f32_16x16x32_bf16 v[188:191], a[124:127], a[28:31], v[188:191]\n" \
"ds_read_b128 a[20:23], v231 offset:36864\n" \
"v_add_f32 v249, 0x42400000, v249\n" \
"v_mfma_f32_16x16x32_bf16 v[184:187], a[112:115], a[24:27], v[184:187]\n" \
"ds_read_b64_tr_b16 a[92:93], v233 offset:41472\n" \
"v_add_f32 v250, 0x42400000, v250\n" \
"v_mfma_f32_16x16x32_bf16 v[184:187], a[116:119], a[28:31], v[184:187]\n" \
"ds_write_b128 v224, v[216:219] offset:10496\n" \
"v_mfma_f32_16x16x32_bf16 v[180:183], a[104:107], a[24:27], v[180:183]\n" \
"ds_read_b64_tr_b16 a[94:95], v233 offset:43520\n" \
"v_add_f32 v251, 0x42400000, v251\n" \
"v_mfma_f32_16x16x32_bf16 v[180:183], a[108:111], a[28:31], v[180:183]\n" \
"s_mov_b32 s72, 0x42400000\n" \
"s_cmp_gt_u32 s61, 0\n" \
"s_cselect_b32 s70, s72, 0xff800000\n" \
"v_mfma_f32_16x16x32_bf16 v[176:179], a[96:99], a[24:27], v[176:179]\n" \
"ds_write_b128 v224, v[196:199] offset:0\n" \
"v_mfma_f32_16x16x32_bf16 v[176:179], a[100:103], a[28:31], v[176:179]\n" \
"s_waitcnt lgkmcnt(3)\n" \
"v_mov_b32 v252, s70\n" \
"v_mfma_f32_16x16x32_bf16 v[64:67], a[64:67], a[0:3], v[64:67]\n" \
"ds_read_b128 a[24:27], v230 offset:38912\n" \
"v_mfma_f32_16x16x32_bf16 v[64:67], a[68:71], a[4:7], v[64:67]\n" \
"s_cmp_gt_u32 s61, 1\n" \
"s_cselect_b32 s70, s72, 0xff800000\n" \
"ds_read_b128 a[28:31], v231 offset:38912\n" \
"v_mfma_f32_16x16x32_bf16 v[68:71], a[72:75], a[0:3], v[68:71]\n" \
"ds_read_b64_tr_b16 a[96:97], v232 offset:33792\n" \
"v_mov_b32 v253, s70\n" \
"v_mfma_f32_16x16x32_bf16 v[68:71], a[76:79], a[4:7], v[68:71]\n" \
"s_cmp_gt_u32 s61, 2\n" \
"s_cselect_b32 s70, s72, 0xff800000\n" \
"v_mov_b32 v226, s70\n" \
"v_mfma_f32_16x16x32_bf16 v[72:75], a[80:83], a[0:3], v[72:75]\n" \
"ds_read_b64_tr_b16 a[98:99], v232 offset:35840\n" \
"v_bfe_i32 v243, v234, 4, 1\n" \
"v_mfma_f32_16x16x32_bf16 v[72:75], a[84:87], a[4:7], v[72:75]\n" \
"s_waitcnt lgkmcnt(3)\n" \
"ds_read_b64_tr_b16 a[100:101], v232 offset:41984\n" \
"v_and_b32 v243, 0xaaaa, v243\n" \
"v_mfma_f32_16x16x32_bf16 v[76:79], a[88:91], a[0:3], v[76:79]\n" \
"ds_read_b64_tr_b16 a[102:103], v232 offset:44032\n" \
"v_or_b32 v243, 0x4444, v243\n" \
"v_mfma_f32_16x16x32_bf16 v[76:79], a[92:95], a[4:7], v[76:79]\n" \
"ds_write_b128 v224, v[212:215] offset:256\n" \
"v_mfma_f32_16x16x32_bf16 v[108:111], a[88:91], a[8:11], v[108:111]\n" \
"ds_read_b64_tr_b16 a[104:105], v233 offset:33792\n" \
"v_bfe_i32 v244, v234, 5, 1\n" \
"v_mfma_f32_16x16x32_bf16 v[108:111], a[92:95], a[12:15], v[108:111]\n" \
"ds_read_b64_tr_b16 a[106:107], v233 offset:35840\n" \
"v_not_b32 v244, v244\n" \
"v_mfma_f32_16x16x32_bf16 v[104:107], a[80:83], a[8:11], v[104:107]\n" \
"ds_read_b64_tr_b16 a[108:109], v233 offset:41984\n" \
"v_and_b32 v244, 0x3f803f80, v244\n" \
"v_mfma_f32_16x16x32_bf16 v[104:107], a[84:87], a[12:15], v[104:107]\n" \
"buffer_store_dwordx4 v[16:19], v236, s[56:59], s93 offen offset:256 sc1\n" \
"v_mfma_f32_16x16x32_bf16 v[100:103], a[72:75], a[8:11], v[100:103]\n" \
"ds_read_b64_tr_b16 a[110:111], v233 offset:44032\n" \
"v_mov_b32 v245, v244\n" \
"v_mfma_f32_16x16x32_bf16 v[100:103], a[76:79], a[12:15], v[100:103]\n" \
"ds_read_b64_tr_b16 a[112:113], v232 offset:34304\n" \
"v_mov_b32 v246, v244\n" \
"v_mfma_f32_16x16x32_bf16 v[96:99], a[64:67], a[8:11], v[96:99]\n" \
"ds_read_b64_tr_b16 a[114:115], v232 offset:36352\n" \
"v_mov_b32 v247, v244\n" \
"v_mfma_f32_16x16x32_bf16 v[96:99], a[68:71], a[12:15], v[96:99]\n" \
"ds_write_b128 v224, v[204:207] offset:2048\n" \
"v_mfma_f32_16x16x32_bf16 v[128:131], a[64:67], a[16:19], v[128:131]\n" \
"s_mov_b32 s76, s73\n" \
"s_and_b32 s77, s63, 1\n" \
"ds_read_b64_tr_b16 a[116:117], v232 offset:42496\n" \
"v_mfma_f32_16x16x32_bf16 v[128:131], a[68:71], a[20:23], v[128:131]\n" \
"s_lshl_b32 s77, s77, 15\n" \
"s_xor_b32 s89, s77, 0x8000\n" \
"ds_read_b64_tr_b16 a[118:119], v232 offset:44544\n" \
"v_mfma_f32_16x16x32_bf16 v[132:135], a[72:75], a[16:19], v[132:135]\n" \
"s_add_u32 s82, s89, 0x10000\n" \
"s_mov_b32 s74, 0\n" \
"ds_read_b64_tr_b16 a[120:121], v233 offset:34304\n" \
"v_mfma_f32_16x16x32_bf16 v[132:135], a[76:79], a[20:23], v[132:135]\n" \
"ds_write_b128 v224, v[220:223] offset:2304\n" \
"v_mfma_f32_16x16x32_bf16 v[136:139], a[80:83], a[16:19], v[136:139]\n" \
"s_waitcnt lgkmcnt(4)\n" \
"s_add_u32 s73, s73, 34816\n" \
"ds_read_b64_tr_b16 a[122:123], v233 offset:36352\n" \
"v_mfma_f32_16x16x32_bf16 v[136:139], a[84:87], a[20:23], v[136:139]\n" \
"s_cmp_eq_u32 s73, s83\n" \
"s_cselect_b32 s70, 34816, 0\n" \
"ds_read_b64_tr_b16 a[124:125], v233 offset:42496\n" \
"v_mfma_f32_16x16x32_bf16 v[140:143], a[88:91], a[16:19], v[140:143]\n" \
"s_add_u32 s73, s73, s70\n" \
"s_lshr_b32 s70, s61, 1\n" \
"ds_read_b64_tr_b16 a[126:127], v233 offset:44544\n" \
"v_mfma_f32_16x16x32_bf16 v[140:143], a[92:95], a[20:23], v[140:143]\n" \
"s_lshl_b32 s85, s63, 2\n" \
"s_sub_u32 s86, 60, s85\n" \
"s_add_u32 s85, s85, s70\n" \
"v_mfma_f32_16x16x32_bf16 v[172:175], a[88:91], a[24:27], v[172:175]\n" \
"s_add_u32 s86, s86, s70\n" \
"s_mul_i32 s87, s85, 266240\n" \
"v_add_u32 v230, 0x10000, v230\n" \
"v_mfma_f32_16x16x32_bf16 v[172:175], a[92:95], a[28:31], v[172:175]\n" \
"s_mul_i32 s88, s86, 266240\n" \
"s_and_b32 s75, s61, 1\n" \
"v_add_u32 v231, 0x10000, v231\n" \
"v_mfma_f32_16x16x32_bf16 v[168:171], a[80:83], a[24:27], v[168:171]\n" \
"s_lshl_b32 s70, s75, 8\n" \
"s_lshl_b32 s91, s62, 13\n" \
"s_add_u32 s91, s91, s70\n" \
"v_mfma_f32_16x16x32_bf16 v[168:171], a[84:87], a[28:31], v[168:171]\n" \
"buffer_store_dwordx4 v[52:55], v236, s[56:59], s93 offen offset:2048 sc1\n" \
"v_mfma_f32_16x16x32_bf16 v[164:167], a[72:75], a[24:27], v[164:167]\n" \
"s_add_u32 s91, s91, 35700736\n" \
"s_add_u32 s87, s87, s91\n" \
"s_add_u32 s88, s88, s91\n" \
"v_mfma_f32_16x16x32_bf16 v[164:167], a[76:79], a[28:31], v[164:167]\n" \
"s_lshl_b32 s75, s75, 14\n" \
"s_cmp_eq_u32 s63, 7\n" \
"s_cselect_b32 s75, 0, s75\n" \
"s_xor_b32 s78, s75, 0xc000\n" \
"s_mov_b32 s81, s78\n" \
"s_cmp_eq_u32 s63, 0\n" \
"s_cselect_b32 s91, s75, 0\n" \
"s_add_u32 s91, s91, 0x8000\n" \
"s_xor_b32 s75, s75, 0x8000\n" \
"s_sub_u32 s84, 7, s63\n" \
"v_mfma_f32_16x16x32_bf16 v[160:163], a[64:67], a[24:27], v[160:163]\n" \
"v_mfma_f32_16x16x32_bf16 v[160:163], a[68:71], a[28:31], v[160:163]\n" \
"s_waitcnt vmcnt(18) lgkmcnt(0)\n" \
"s_barrier\n" \
"v_mfma_f32_16x16x32_bf16 v[144:147], a[96:99], a[16:19], v[144:147]\n" \
"s_mov_b64 s[44:45], s[10:11]\n" \
"s_mov_b32 s46, 0\n" \
"ds_read_b64_tr_b16 a[64:65], v232 offset:49152\n" \
"v_mfma_f32_16x16x32_bf16 v[144:147], a[100:103], a[20:23], v[144:147]\n" \
"s_mov_b32 s47, 0\n" \
"s_cmp_eq_u32 s63, 7\n" \
"s_cselect_b64 s[44:45], s[18:19], s[44:45]\n" \
"s_cselect_b32 s46, 5, s46\n" \
"s_cmp_eq_u32 s63, 3\n" \
"s_cselect_b64 s[44:45], s[18:19], s[44:45]\n" \
"s_cselect_b32 s46, 3, s46\n" \
"ds_read_b64_tr_b16 a[66:67], v232 offset:51200\n" \
"v_mfma_f32_16x16x32_bf16 v[148:151], a[104:107], a[16:19], v[148:151]\n" \
"s_cmp_eq_u32 s63, 3\n" \
"s_cselect_b32 s47, 640, s47\n" \
"ds_read_b64_tr_b16 a[68:69], v232 offset:57344\n" \
"v_mfma_f32_16x16x32_bf16 v[148:151], a[108:111], a[20:23], v[148:151]\n" \
"ds_write_b128 v225, a[192:195] offset:0\n" \
"v_mfma_f32_16x16x32_bf16 v[152:155], a[112:115], a[16:19], v[152:155]\n" \
"ds_read_b64_tr_b16 a[70:71], v232 offset:59392\n" \
"v_mfma_f32_16x16x32_bf16 v[152:155], a[116:119], a[20:23], v[152:155]\n" \
"s_cmp_eq_u32 s63, 6\n" \
"s_cselect_b64 s[44:45], s[22:23], s[44:45]\n" \
"s_cselect_b32 s46, 6, s46\n" \
"ds_read_b64_tr_b16 a[72:73], v233 offset:49152\n" \
"v_mfma_f32_16x16x32_bf16 v[156:159], a[120:123], a[16:19], v[156:159]\n" \
"ds_read_b64_tr_b16 a[74:75], v233 offset:51200\n" \
"v_mfma_f32_16x16x32_bf16 v[156:159], a[124:127], a[20:23], v[156:159]\n" \
"s_cmp_eq_u32 s63, 4\n" \
"s_cselect_b64 s[44:45], s[20:21], s[44:45]\n" \
"s_cselect_b32 s46, 4, s46\n" \
"v_mfma_f32_16x16x32_bf16 v[188:191], a[120:123], a[24:27], v[188:191]\n" \
"ds_read_b128 a[16:19], v230 offset:0\n" \
"v_mfma_f32_16x16x32_bf16 v[188:191], a[124:127], a[28:31], v[188:191]\n" \
"s_cmp_eq_u32 s63, 2\n" \
"s_cselect_b64 s[44:45], s[24:25], s[44:45]\n" \
"s_cselect_b32 s46, 2, s46\n" \
"ds_read_b128 a[20:23], v231 offset:0\n" \
"v_mfma_f32_16x16x32_bf16 v[184:187], a[112:115], a[24:27], v[184:187]\n" \
"s_lshl_b32 s48, s62, 2\n" \
"s_add_u32 s48, s48, s61\n" \
"ds_read_b64_tr_b16 a[76:77], v233 offset:57344\n" \
"v_mfma_f32_16x16x32_bf16 v[184:187], a[116:119], a[28:31], v[184:187]\n" \
"ds_write_b128 v225, a[196:199] offset:1024\n" \
"v_mfma_f32_16x16x32_bf16 v[180:183], a[104:107], a[24:27], v[180:183]\n" \
"s_add_u32 s48, s48, s47\n" \
"s_lshl_b32 s48, s48, 13\n" \
"ds_read_b64_tr_b16 a[78:79], v233 offset:59392\n" \
"v_mfma_f32_16x16x32_bf16 v[180:183], a[108:111], a[28:31], v[180:183]\n" \
"s_add_u32 s44, s44, s48\n" \
"s_addc_u32 s45, s45, 0\n" \
"ds_read_b64_tr_b16 a[80:81], v232 offset:49664\n" \
"v_mfma_f32_16x16x32_bf16 v[176:179], a[96:99], a[24:27], v[176:179]\n" \
"s_cmp_eq_u32 s46, 0\n" \
"s_cselect_b32 s48, 28, 3\n" \
"ds_read_b64_tr_b16 a[82:83], v232 offset:51712\n" \
"v_mfma_f32_16x16x32_bf16 v[176:179], a[100:103], a[28:31], v[176:179]\n" \
"buffer_store_dwordx4 v[24:27], v236, s[56:59], s93 offen offset:2304 sc1\n" \
"v_mfma_f32_16x16x32_bf16 v[80:83], a[96:99], a[0:3], v[80:83]\n" \
"ds_read_b128 a[24:27], v230 offset:2048\n" \
"v_mfma_f32_16x16x32_bf16 v[80:83], a[100:103], a[4:7], v[80:83]\n" \
"ds_read_b128 a[28:31], v231 offset:2048\n" \
"v_mfma_f32_16x16x32_bf16 v[84:87], a[104:107], a[0:3], v[84:87]\n" \
"ds_read_b64_tr_b16 a[84:85], v232 offset:57856\n" \
"v_mfma_f32_16x16x32_bf16 v[84:87], a[108:111], a[4:7], v[84:87]\n" \
"ds_write_b128 v225, a[200:203] offset:2048\n" \
"v_mfma_f32_16x16x32_bf16 v[88:91], a[112:115], a[0:3], v[88:91]\n" \
"ds_read_b64_tr_b16 a[86:87], v232 offset:59904\n" \
"v_mfma_f32_16x16x32_bf16 v[88:91], a[116:119], a[4:7], v[88:91]\n" \
"ds_read_b64_tr_b16 a[88:89], v233 offset:49664\n" \
"v_mfma_f32_16x16x32_bf16 v[92:95], a[120:123], a[0:3], v[92:95]\n" \
"ds_read_b64_tr_b16 a[90:91], v233 offset:51712\n" \
"v_mfma_f32_16x16x32_bf16 v[92:95], a[124:127], a[4:7], v[92:95]\n" \
"ds_write_b128 v225, a[204:207] offset:3072\n" \
"v_mfma_f32_16x16x32_bf16 v[124:127], a[120:123], a[8:11], v[124:127]\n" \
"ds_read_b128 a[0:3], v230 offset:4096\n" \
"v_mfma_f32_16x16x32_bf16 v[124:127], a[124:127], a[12:15], v[124:127]\n" \
"ds_read_b128 a[4:7], v231 offset:4096\n" \
"v_mfma_f32_16x16x32_bf16 v[120:123], a[112:115], a[8:11], v[120:123]\n" \
"ds_read_b64_tr_b16 a[92:93], v233 offset:57856\n" \
"v_mfma_f32_16x16x32_bf16 v[120:123], a[116:119], a[12:15], v[120:123]\n" \
"v_mfma_f32_16x16x32_bf16 v[116:119], a[104:107], a[8:11], v[116:119]\n" \
"ds_read_b64_tr_b16 a[94:95], v233 offset:59904\n" \
"v_mfma_f32_16x16x32_bf16 v[116:119], a[108:111], a[12:15], v[116:119]\n" \
"v_mfma_f32_16x16x32_bf16 v[112:115], a[96:99], a[8:11], v[112:115]\n" \
"ds_write_b128 v225, a[224:227] offset:8192\n" \
"v_mfma_f32_16x16x32_bf16 v[112:115], a[100:103], a[12:15], v[112:115]\n" \
"s_waitcnt lgkmcnt(2)\n" \
"v_mfma_f32_16x16x32_bf16 v[64:67], a[64:67], a[16:19], v[64:67]\n" \
"ds_read_b128 a[8:11], v230 offset:6144\n" \
"ds_read_b128 a[12:15], v231 offset:6144\n" \
"v_mfma_f32_16x16x32_bf16 v[64:67], a[68:71], a[20:23], v[64:67]\n" \
"ds_read_b64_tr_b16 a[96:97], v232 offset:50176\n" \
"ds_read_b64_tr_b16 a[98:99], v232 offset:52224\n" \
"v_mfma_f32_16x16x32_bf16 v[68:71], a[72:75], a[16:19], v[68:71]\n" \
"s_waitcnt lgkmcnt(5)\n" \
"ds_read_b64_tr_b16 a[100:101], v232 offset:58368\n" \
"ds_read_b64_tr_b16 a[102:103], v232 offset:60416\n" \
"v_mfma_f32_16x16x32_bf16 v[68:71], a[76:79], a[20:23], v[68:71]\n" \
"s_getpc_b64 s[64:65]\n" \
"Lpd1_%=:\n" \
"s_lshl_b32 s66, s62, 2\n" \
"ds_read_b64_tr_b16 a[104:105], v233 offset:50176\n" \
"ds_read_b64_tr_b16 a[106:107], v233 offset:52224\n" \
"v_mfma_f32_16x16x32_bf16 v[72:75], a[80:83], a[16:19], v[72:75]\n" \
"s_add_u32 s66, s66, s61\n" \
"ds_read_b64_tr_b16 a[108:109], v233 offset:58368\n" \
"ds_read_b64_tr_b16 a[110:111], v233 offset:60416\n" \
"v_mfma_f32_16x16x32_bf16 v[72:75], a[84:87], a[20:23], v[72:75]\n" \
"s_and_b32 s66, s66, 15\n" \
"ds_read_b64_tr_b16 a[112:113], v232 offset:50688\n" \
"ds_read_b64_tr_b16 a[114:115], v232 offset:52736\n" \
"v_mfma_f32_16x16x32_bf16 v[76:79], a[88:91], a[16:19], v[76:79]\n" \
"s_lshl_b32 s66, s66, 12\n" \
"ds_read_b64_tr_b16 a[116:117], v232 offset:58880\n" \
"ds_read_b64_tr_b16 a[118:119], v232 offset:60928\n" \
"v_mfma_f32_16x16x32_bf16 v[76:79], a[92:95], a[20:23], v[76:79]\n" \
"s_add_u32 s64, s64, Lloop2_%=-Lpd1_%=\n" \
"s_addc_u32 s65, s65, 0\n" \
"s_mov_b32 s67, Lcode_end_%=-Lloop2_%=-4\n" \
"ds_read_b64_tr_b16 a[120:121], v233 offset:50688\n" \
"ds_read_b64_tr_b16 a[122:123], v233 offset:52736\n" \
"v_mfma_f32_16x16x32_bf16 v[96:99], a[64:67], a[24:27], v[96:99]\n" \
"s_add_u32 s93, s94, 17858560\n" \
"ds_read_b64_tr_b16 a[124:125], v233 offset:58880\n" \
"ds_read_b64_tr_b16 a[126:127], v233 offset:60928\n" \
"v_mfma_f32_16x16x32_bf16 v[96:99], a[68:71], a[28:31], v[96:99]\n" \
"buffer_store_dwordx4 v[44:47], v234, s[56:59], s93 offen offset:0 sc1\n" \
"v_mfma_f32_16x16x32_bf16 v[100:103], a[72:75], a[24:27], v[100:103]\n" \
"ds_write_b128 v225, a[228:231] offset:9216\n" \
"v_mfma_f32_16x16x32_bf16 v[100:103], a[76:79], a[28:31], v[100:103]\n" \
"v_lshl_add_u32 v230, v234, 2, s66\n" \
"v_min_u32 v230, s67, v230\n" \
"v_mfma_f32_16x16x32_bf16 v[104:107], a[80:83], a[24:27], v[104:107]\n" \
"buffer_load_dword v227, v242, s[56:59], s90 offen sc1\n" \
"v_mfma_f32_16x16x32_bf16 v[104:107], a[84:87], a[28:31], v[104:107]\n" \
"ds_write_b128 v225, a[232:235] offset:10240\n" \
"v_mfma_f32_16x16x32_bf16 v[108:111], a[88:91], a[24:27], v[108:111]\n" \
"global_load_dword v230, v230, s[64:65]\n" \
"v_mfma_f32_16x16x32_bf16 v[108:111], a[92:95], a[28:31], v[108:111]\n" \
"s_waitcnt lgkmcnt(1)\n" \
"v_cvt_pk_bf16_f32 v64, v64, v65\n" \
"v_cvt_pk_bf16_f32 v65, v66, v67\n" \
"v_mfma_f32_16x16x32_bf16 v[80:83], a[96:99], a[16:19], v[80:83]\n" \
"v_cvt_pk_bf16_f32 v66, v68, v69\n" \
"v_cvt_pk_bf16_f32 v67, v70, v71\n" \
"v_mfma_f32_16x16x32_bf16 v[80:83], a[100:103], a[20:23], v[80:83]\n" \
"v_accvgpr_write_b32 a128, v64\n" \
"v_accvgpr_write_b32 a129, v65\n" \
"v_accvgpr_write_b32 a130, v66\n" \
"v_mfma_f32_16x16x32_bf16 v[84:87], a[104:107], a[16:19], v[84:87]\n" \
"v_accvgpr_write_b32 a131, v67\n" \
"v_cvt_pk_bf16_f32 v72, v72, v73\n" \
"v_mfma_f32_16x16x32_bf16 v[84:87], a[108:111], a[20:23], v[84:87]\n" \
"buffer_store_dwordx4 v[60:63], v234, s[56:59], s93 offen offset:1024 sc1\n" \
"v_mfma_f32_16x16x32_bf16 v[88:91], a[112:115], a[16:19], v[88:91]\n" \
"ds_write_b128 v225, a[236:239] offset:11264\n" \
"v_mfma_f32_16x16x32_bf16 v[88:91], a[116:119], a[20:23], v[88:91]\n" \
"v_cvt_pk_bf16_f32 v73, v74, v75\n" \
"v_cvt_pk_bf16_f32 v74, v76, v77\n" \
"v_cvt_pk_bf16_f32 v75, v78, v79\n" \
"v_mfma_f32_16x16x32_bf16 v[92:95], a[120:123], a[16:19], v[92:95]\n" \
"v_accvgpr_write_b32 a132, v72\n" \
"v_accvgpr_write_b32 a133, v73\n" \
"v_mfma_f32_16x16x32_bf16 v[92:95], a[124:127], a[20:23], v[92:95]\n" \
"v_accvgpr_write_b32 a134, v74\n" \
"v_accvgpr_write_b32 a135, v75\n" \
"v_mfma_f32_16x16x32_bf16 v[112:115], a[96:99], a[24:27], v[112:115]\n" \
"v_cvt_pk_bf16_f32 v96, v96, v97\n" \
"v_cvt_pk_bf16_f32 v97, v98, v99\n" \
"v_mfma_f32_16x16x32_bf16 v[112:115], a[100:103], a[28:31], v[112:115]\n" \
"v_cvt_pk_bf16_f32 v98, v100, v101\n" \
"v_cvt_pk_bf16_f32 v99, v102, v103\n" \
"v_mfma_f32_16x16x32_bf16 v[116:119], a[104:107], a[24:27], v[116:119]\n" \
"v_cvt_pk_bf16_f32 v104, v104, v105\n" \
"v_cvt_pk_bf16_f32 v105, v106, v107\n" \
"v_mfma_f32_16x16x32_bf16 v[116:119], a[108:111], a[28:31], v[116:119]\n" \
"v_cvt_pk_bf16_f32 v106, v108, v109\n" \
"v_cvt_pk_bf16_f32 v107, v110, v111\n" \
"v_mfma_f32_16x16x32_bf16 v[120:123], a[112:115], a[24:27], v[120:123]\n" \
"v_mfma_f32_16x16x32_bf16 v[120:123], a[116:119], a[28:31], v[120:123]\n" \
"v_cvt_pk_bf16_f32 v80, v80, v81\n" \
"v_cvt_pk_bf16_f32 v81, v82, v83\n" \
"v_cvt_pk_bf16_f32 v82, v84, v85\n" \
"v_mfma_f32_16x16x32_bf16 v[124:127], a[120:123], a[24:27], v[124:127]\n" \
"v_cvt_pk_bf16_f32 v83, v86, v87\n" \
"v_accvgpr_write_b32 a136, v80\n" \
"v_mfma_f32_16x16x32_bf16 v[124:127], a[124:127], a[28:31], v[124:127]\n" \
"v_accvgpr_write_b32 a137, v81\n" \
"s_waitcnt lgkmcnt(0)\n" \
"s_barrier\n" \
"v_mfma_f32_16x16x32_bf16 v[128:131], a[64:67], a[0:3], v[128:131]\n" \
"v_accvgpr_write_b32 a138, v82\n" \
"v_accvgpr_write_b32 a139, v83\n" \
"v_mfma_f32_16x16x32_bf16 v[128:131], a[68:71], a[4:7], v[128:131]\n" \
"ds_read_b128 a[192:195], v237 offset:0\n" \
"v_cvt_pk_bf16_f32 v88, v88, v89\n" \
"v_mfma_f32_16x16x32_bf16 v[132:135], a[72:75], a[0:3], v[132:135]\n" \
"v_cvt_pk_bf16_f32 v89, v90, v91\n" \
"v_cvt_pk_bf16_f32 v90, v92, v93\n" \
"v_mfma_f32_16x16x32_bf16 v[132:135], a[76:79], a[4:7], v[132:135]\n" \
"ds_read_b128 a[196:199], v237 offset:2048\n" \
"v_cvt_pk_bf16_f32 v91, v94, v95\n" \
"v_mfma_f32_16x16x32_bf16 v[136:139], a[80:83], a[0:3], v[136:139]\n" \
"v_med3_f32 v208, v248, v252, s72\n" \
"v_med3_f32 v209, v249, v252, s72\n" \
"v_mfma_f32_16x16x32_bf16 v[136:139], a[84:87], a[4:7], v[136:139]\n" \
"ds_read_b128 a[200:203], v237 offset:4096\n" \
"v_med3_f32 v210, v250, v252, s72\n" \
"v_med3_f32 v211, v251, v252, s72\n" \
"v_mfma_f32_16x16x32_bf16 v[140:143], a[88:91], a[0:3], v[140:143]\n" \
"buffer_store_dwordx4 v[20:23], v234, s[56:59], s93 offen offset:2048 sc1\n" \
"v_mfma_f32_16x16x32_bf16 v[140:143], a[92:95], a[4:7], v[140:143]\n" \
"ds_read_b128 a[204:207], v237 offset:6144\n" \
"v_cvt_pk_bf16_f32 v112, v112, v113\n" \
"v_mfma_f32_16x16x32_bf16 v[144:147], a[96:99], a[0:3], v[144:147]\n" \
"v_cvt_pk_bf16_f32 v113, v114, v115\n" \
"v_cvt_pk_bf16_f32 v114, v116, v117\n" \
"v_mfma_f32_16x16x32_bf16 v[144:147], a[100:103], a[4:7], v[144:147]\n" \
"ds_read_b128 a[208:211], v237 offset:256\n" \
"v_cvt_pk_bf16_f32 v115, v118, v119\n" \
"v_cvt_pk_bf16_f32 v120, v120, v121\n" \
"v_mfma_f32_16x16x32_bf16 v[148:151], a[104:107], a[0:3], v[148:151]\n" \
"v_cvt_pk_bf16_f32 v121, v122, v123\n" \
"v_cvt_pk_bf16_f32 v122, v124, v125\n" \
"v_mfma_f32_16x16x32_bf16 v[148:151], a[108:111], a[4:7], v[148:151]\n" \
"ds_read_b128 a[212:215], v237 offset:2304\n" \
"v_cvt_pk_bf16_f32 v123, v126, v127\n" \
"v_mfma_f32_16x16x32_bf16 v[152:155], a[112:115], a[0:3], v[152:155]\n" \
"v_mfma_f32_16x16x32_bf16 v[152:155], a[116:119], a[4:7], v[152:155]\n" \
"ds_read_b128 a[216:219], v237 offset:4352\n" \
"v_cvt_pk_bf16_f32 v128, v128, v129\n" \
"v_mfma_f32_16x16x32_bf16 v[156:159], a[120:123], a[0:3], v[156:159]\n" \
"v_cvt_pk_bf16_f32 v129, v130, v131\n" \
"v_cvt_pk_bf16_f32 v130, v132, v133\n" \
"v_mfma_f32_16x16x32_bf16 v[156:159], a[124:127], a[4:7], v[156:159]\n" \
"ds_read_b128 a[220:223], v237 offset:6400\n" \
"v_cvt_pk_bf16_f32 v131, v134, v135\n" \
"v_cvt_pk_bf16_f32 v136, v136, v137\n" \
"v_mfma_f32_16x16x32_bf16 v[160:163], a[64:67], a[8:11], v[160:163]\n" \
"v_cvt_pk_bf16_f32 v137, v138, v139\n" \
"v_cvt_pk_bf16_f32 v138, v140, v141\n" \
"v_mfma_f32_16x16x32_bf16 v[160:163], a[68:71], a[12:15], v[160:163]\n" \
"ds_read_b128 a[224:227], v237 offset:8192\n" \
"v_cvt_pk_bf16_f32 v139, v142, v143\n" \
"v_mfma_f32_16x16x32_bf16 v[164:167], a[72:75], a[8:11], v[164:167]\n" \
"v_mfma_f32_16x16x32_bf16 v[164:167], a[76:79], a[12:15], v[164:167]\n" \
"ds_read_b128 a[228:231], v237 offset:10240\n" \
"v_mfma_f32_16x16x32_bf16 v[168:171], a[80:83], a[8:11], v[168:171]\n" \
"v_mfma_f32_16x16x32_bf16 v[168:171], a[84:87], a[12:15], v[168:171]\n" \
"ds_read_b128 a[232:235], v237 offset:12288\n" \
"v_cvt_pk_bf16_f32 v144, v144, v145\n" \
"v_cvt_pk_bf16_f32 v145, v146, v147\n" \
"v_mfma_f32_16x16x32_bf16 v[172:175], a[88:91], a[8:11], v[172:175]\n" \
"v_cvt_pk_bf16_f32 v146, v148, v149\n" \
"v_cvt_pk_bf16_f32 v147, v150, v151\n" \
"v_mfma_f32_16x16x32_bf16 v[172:175], a[92:95], a[12:15], v[172:175]\n" \
"ds_read_b128 a[236:239], v237 offset:14336\n" \
"v_cvt_pk_bf16_f32 v152, v152, v153\n" \
"v_mfma_f32_16x16x32_bf16 v[176:179], a[96:99], a[8:11], v[176:179]\n" \
"v_cvt_pk_bf16_f32 v153, v154, v155\n" \
"v_cvt_pk_bf16_f32 v154, v156, v157\n" \
"v_mfma_f32_16x16x32_bf16 v[176:179], a[100:103], a[12:15], v[176:179]\n" \
"ds_read_b128 a[240:243], v237 offset:8448\n" \
"v_cvt_pk_bf16_f32 v155, v158, v159\n" \
"v_mfma_f32_16x16x32_bf16 v[180:183], a[104:107], a[8:11], v[180:183]\n" \
"buffer_store_dwordx4 v[28:31], v234, s[56:59], s93 offen offset:3072 sc1\n" \
"v_mfma_f32_16x16x32_bf16 v[180:183], a[108:111], a[12:15], v[180:183]\n" \
"ds_read_b128 a[244:247], v237 offset:10496\n" \
"ds_write_b128 v239, v[128:131] offset:0\n" \
"v_mov_b32 v196, 0x42400000\n" \
"v_mfma_f32_16x16x32_bf16 v[184:187], a[112:115], a[8:11], v[184:187]\n" \
"v_mov_b32 v197, 0x42400000\n" \
"v_mov_b32 v198, 0x42400000\n" \
"v_mfma_f32_16x16x32_bf16 v[184:187], a[116:119], a[12:15], v[184:187]\n" \
"ds_read_b128 a[248:251], v237 offset:12544\n" \
"v_mov_b32 v199, 0x42400000\n" \
"v_cvt_pk_bf16_f32 v160, v160, v161\n" \
"v_mfma_f32_16x16x32_bf16 v[188:191], a[120:123], a[8:11], v[188:191]\n" \
"v_cvt_pk_bf16_f32 v161, v162, v163\n" \
"v_cvt_pk_bf16_f32 v162, v164, v165\n" \
"v_mfma_f32_16x16x32_bf16 v[188:191], a[124:127], a[12:15], v[188:191]\n" \
"s_waitcnt lgkmcnt(3)\n" \
"ds_read_b128 a[252:255], v237 offset:14592\n" \
"v_cvt_pk_bf16_f32 v163, v166, v167\n" \
"v_mfma_f32_16x16x32_bf16 v[16:19], a[192:195], v[96:99], v[208:211]\n" \
"v_cvt_pk_bf16_f32 v168, v168, v169\n" \
"v_cvt_pk_bf16_f32 v169, v170, v171\n" \
"v_mfma_f32_16x16x32_bf16 v[16:19], a[196:199], v[104:107], v[16:19]\n" \
"v_cvt_pk_bf16_f32 v170, v172, v173\n" \
"v_cvt_pk_bf16_f32 v171, v174, v175\n" \
"v_mfma_f32_16x16x32_bf16 v[16:19], a[200:203], v[112:115], v[16:19]\n" \
"v_cvt_pk_bf16_f32 v176, v176, v177\n" \
"v_cvt_pk_bf16_f32 v177, v178, v179\n" \
"v_mfma_f32_16x16x32_bf16 v[16:19], a[204:207], v[120:123], v[16:19]\n" \
"v_cvt_pk_bf16_f32 v178, v180, v181\n" \
"v_cvt_pk_bf16_f32 v179, v182, v183\n" \
"v_mfma_f32_16x16x32_bf16 v[32:35], a[204:207], v[152:155], v[196:199]\n" \
"v_cvt_pk_bf16_f32 v184, v184, v185\n" \
"v_cvt_pk_bf16_f32 v185, v186, v187\n" \
"v_mfma_f32_16x16x32_bf16 v[32:35], a[200:203], v[144:147], v[32:35]\n" \
"v_cvt_pk_bf16_f32 v186, v188, v189\n" \
"v_cvt_pk_bf16_f32 v187, v190, v191\n" \
"v_mfma_f32_16x16x32_bf16 v[32:35], a[196:199], v[136:139], v[32:35]\n" \
"s_add_u32 s93, s94, 17850368\n" \
"v_med3_f32 v212, v248, v253, v252\n" \
"v_med3_f32 v213, v249, v253, v252\n" \
"Lpg5012_%=:\n" \
"s_cbranch_execz Lpg6775_%=\n" \
"v_mfma_f32_16x16x32_bf16 v[32:35], a[192:195], v[128:131], v[32:35]\n" \
"buffer_store_dwordx4 v[0:3], v234, s[56:59], s93 offen offset:0 sc1\n" \
"v_mfma_f32_16x16x32_bf16 v[48:51], a[192:195], v[160:163], v[196:199]\n" \
"v_med3_f32 v214, v250, v253, v252\n" \
"v_med3_f32 v215, v251, v253, v252\n" \
"v_mfma_f32_16x16x32_bf16 v[48:51], a[196:199], v[168:171], v[48:51]\n" \
"ds_read_b128 a[192:195], v237 offset:16384\n" \
"v_med3_f32 v216, v248, v226, v253\n" \
"v_mfma_f32_16x16x32_bf16 v[48:51], a[200:203], v[176:179], v[48:51]\n" \
"ds_read_b128 a[196:199], v237 offset:18432\n" \
"v_med3_f32 v217, v249, v226, v253\n" \
"v_mfma_f32_16x16x32_bf16 v[48:51], a[204:207], v[184:187], v[48:51]\n" \
"ds_read_b128 a[200:203], v237 offset:20480\n" \
"ds_write_b128 v239, v[136:139] offset:1024\n" \
"v_mfma_f32_16x16x32_bf16 v[52:55], a[220:223], v[184:187], v[196:199]\n" \
"ds_read_b128 a[204:207], v237 offset:22528\n" \
"v_med3_f32 v218, v250, v226, v253\n" \
"v_mfma_f32_16x16x32_bf16 v[52:55], a[216:219], v[176:179], v[52:55]\n" \
"v_med3_f32 v219, v251, v226, v253\n" \
"v_med3_f32 v220, v248, v240, v226\n" \
"v_mfma_f32_16x16x32_bf16 v[52:55], a[212:215], v[168:171], v[52:55]\n" \
"ds_write_b128 v239, v[144:147] offset:2048\n" \
"v_mfma_f32_16x16x32_bf16 v[52:55], a[208:211], v[160:163], v[52:55]\n" \
"buffer_store_dwordx4 v[4:7], v234, s[56:59], s93 offen offset:1024 sc1\n" \
"v_mfma_f32_16x16x32_bf16 v[36:39], a[208:211], v[128:131], v[196:199]\n" \
"v_med3_f32 v221, v249, v240, v226\n" \
"v_med3_f32 v222, v250, v240, v226\n" \
"v_mfma_f32_16x16x32_bf16 v[36:39], a[212:215], v[136:139], v[36:39]\n" \
"v_med3_f32 v223, v251, v240, v226\n" \
"v_accvgpr_write_b32 a144, v96\n" \
"v_mfma_f32_16x16x32_bf16 v[36:39], a[216:219], v[144:147], v[36:39]\n" \
"v_accvgpr_write_b32 a145, v97\n" \
"v_accvgpr_write_b32 a146, v98\n" \
"v_mfma_f32_16x16x32_bf16 v[36:39], a[220:223], v[152:155], v[36:39]\n" \
"ds_write_b128 v239, v[152:155] offset:3072\n" \
"v_mfma_f32_16x16x32_bf16 v[20:23], a[220:223], v[120:123], v[212:215]\n" \
"v_accvgpr_write_b32 a147, v99\n" \
"v_accvgpr_write_b32 a148, v104\n" \
"v_mfma_f32_16x16x32_bf16 v[20:23], a[216:219], v[112:115], v[20:23]\n" \
"ds_read_b128 a[220:223], v237 offset:22784\n" \
"v_accvgpr_write_b32 a149, v105\n" \
"v_mfma_f32_16x16x32_bf16 v[20:23], a[212:215], v[104:107], v[20:23]\n" \
"ds_read_b128 a[216:219], v237 offset:20736\n" \
"v_accvgpr_write_b32 a150, v106\n" \
"v_mfma_f32_16x16x32_bf16 v[20:23], a[208:211], v[96:99], v[20:23]\n" \
"ds_read_b128 a[212:215], v237 offset:18688\n" \
"ds_write_b128 v239, v[160:163] offset:4096\n" \
"v_mfma_f32_16x16x32_bf16 v[24:27], a[224:227], v[96:99], v[216:219]\n" \
"ds_read_b128 a[208:211], v237 offset:16640\n" \
"v_accvgpr_write_b32 a151, v107\n" \
"v_mfma_f32_16x16x32_bf16 v[24:27], a[228:231], v[104:107], v[24:27]\n" \
"v_accvgpr_write_b32 a152, v112\n" \
"v_accvgpr_write_b32 a153, v113\n" \
"v_mfma_f32_16x16x32_bf16 v[24:27], a[232:235], v[112:115], v[24:27]\n" \
"ds_write_b128 v239, v[168:171] offset:5120\n" \
"v_mfma_f32_16x16x32_bf16 v[24:27], a[236:239], v[120:123], v[24:27]\n" \
"buffer_store_dwordx4 v[8:11], v234, s[56:59], s93 offen offset:2048 sc1\n" \
"v_mfma_f32_16x16x32_bf16 v[40:43], a[236:239], v[152:155], v[196:199]\n" \
"v_accvgpr_write_b32 a154, v114\n" \
"v_accvgpr_write_b32 a155, v115\n" \
"v_mfma_f32_16x16x32_bf16 v[40:43], a[232:235], v[144:147], v[40:43]\n" \
"v_accvgpr_write_b32 a156, v120\n" \
"v_accvgpr_write_b32 a157, v121\n" \
"v_mfma_f32_16x16x32_bf16 v[40:43], a[228:231], v[136:139], v[40:43]\n" \
"v_accvgpr_write_b32 a158, v122\n" \
"v_accvgpr_write_b32 a159, v123\n" \
"v_mfma_f32_16x16x32_bf16 v[40:43], a[224:227], v[128:131], v[40:43]\n" \
"ds_write_b128 v239, v[176:179] offset:6144\n" \
"v_mfma_f32_16x16x32_bf16 v[56:59], a[224:227], v[160:163], v[196:199]\n" \
"v_accvgpr_write_b32 a140, v88\n" \
"v_accvgpr_write_b32 a141, v89\n" \
"v_mfma_f32_16x16x32_bf16 v[56:59], a[228:231], v[168:171], v[56:59]\n" \
"ds_read_b128 a[224:227], v237 offset:24576\n" \
"v_accvgpr_write_b32 a142, v90\n" \
"v_mfma_f32_16x16x32_bf16 v[56:59], a[232:235], v[176:179], v[56:59]\n" \
"s_waitcnt lgkmcnt(2)\n" \
"ds_read_b128 a[228:231], v237 offset:26624\n" \
"v_accvgpr_write_b32 a143, v91\n" \
"v_mfma_f32_16x16x32_bf16 v[56:59], a[236:239], v[184:187], v[56:59]\n" \
"buffer_store_dwordx4 v[12:15], v234, s[56:59], s93 offen offset:3072 sc1\n" \
"ds_write_b128 v239, v[184:187] offset:7168\n" \
"v_mfma_f32_16x16x32_bf16 v[60:63], a[252:255], v[184:187], v[196:199]\n" \
"ds_read_b128 a[232:235], v237 offset:28672\n" \
"v_add_u32_e64 v233, s82, v235\n" \
"v_mfma_f32_16x16x32_bf16 v[60:63], a[248:251], v[176:179], v[60:63]\n" \
"s_waitcnt vmcnt(8)\n" \
"ds_read_b128 a[236:239], v237 offset:30720\n" \
"v_cmp_eq_u32 vcc, s60, v227\n" \
"v_mfma_f32_16x16x32_bf16 v[60:63], a[244:247], v[168:171], v[60:63]\n" \
"v_exp_f32_e64 v16, v16\n" \
"v_mfma_f32_16x16x32_bf16 v[60:63], a[240:243], v[160:163], v[60:63]\n" \
"s_andn2_b32 s70, 0xff, vcc_lo\n" \
"s_cbranch_scc1 Lpl1_%=\n" \
"Lpr1_%=:\n" \
"v_exp_f32 v17, v17\n" \
"v_exp_f32 v18, v18\n" \
"v_mfma_f32_16x16x32_bf16 v[44:47], a[240:243], v[128:131], v[196:199]\n" \
"v_exp_f32 v19, v19\n" \
"v_mfma_f32_16x16x32_bf16 v[44:47], a[244:247], v[136:139], v[44:47]\n" \
"s_add_u32 m0, s79, 32768\n" \
"v_exp_f32 v20, v20\n" \
"v_mfma_f32_16x16x32_bf16 v[44:47], a[248:251], v[144:147], v[44:47]\n" \
"s_add_u32 s71, s76, 0\n" \
"v_cvt_pk_bf16_f32 v16, v16, v17\n" \
"v_cvt_pk_bf16_f32 v17, v18, v19\n" \
"v_mfma_f32_16x16x32_bf16 v[44:47], a[252:255], v[152:155], v[44:47]\n" \
"buffer_load_dwordx4 v234, s[36:39], s71 offen offset:0 sc1 lds\n" \
"v_exp_f32 v21, v21\n" \
"v_exp_f32 v22, v22\n" \
"v_mfma_f32_16x16x32_bf16 v[28:31], a[252:255], v[120:123], v[220:223]\n" \
"v_exp_f32 v23, v23\n" \
"v_mfma_f32_16x16x32_bf16 v[28:31], a[248:251], v[112:115], v[28:31]\n" \
"ds_read_b128 a[252:255], v237 offset:30976\n" \
"v_cvt_pk_bf16_f32 v18, v20, v21\n" \
"v_mfma_f32_16x16x32_bf16 v[28:31], a[244:247], v[104:107], v[28:31]\n" \
"buffer_load_dwordx4 v234, s[36:39], s71 offen offset:1024 sc1 lds\n" \
"v_mfma_f32_16x16x32_bf16 v[28:31], a[240:243], v[96:99], v[28:31]\n" \
"ds_read_b128 a[244:247], v237 offset:26880\n" \
"ds_read_b128 a[248:251], v237 offset:28928\n" \
"v_exp_f32 v32, v32\n" \
"v_exp_f32 v33, v33\n" \
"v_mfma_f32_16x16x32_bf16 v[80:83], a[192:195], a[144:147], v[196:199]\n" \
"ds_read_b128 a[240:243], v237 offset:24832\n" \
"v_cvt_pk_bf16_f32 v19, v22, v23\n" \
"v_mfma_f32_16x16x32_bf16 v[80:83], a[196:199], a[148:151], v[80:83]\n" \
"buffer_load_dwordx4 v234, s[36:39], s71 offen offset:2048 sc1 lds\n" \
"v_mfma_f32_16x16x32_bf16 v[80:83], a[200:203], a[152:155], v[80:83]\n" \
"s_lshl_b32 s68, s63, 2\n" \
"s_add_u32 s68, s68, s95\n" \
"s_mov_b32 s69, 0\n" \
"v_mfma_f32_16x16x32_bf16 v[80:83], a[204:207], a[156:159], v[80:83]\n" \
"s_mov_b32 s89, s63\n" \
"ds_read_b128 a[44:47], v233 offset:15360\n" \
"v_mov_b32 v192, s60\n" \
"v_lshlrev_b32 v193, 28, s61\n" \
"v_exp_f32 v34, v34\n" \
"v_exp_f32 v35, v35\n" \
"v_mfma_f32_16x16x32_bf16 v[96:99], a[204:207], v[152:155], v[196:199]\n" \
"buffer_load_dwordx4 v234, s[36:39], s71 offen offset:3072 sc1 lds\n" \
"v_mfma_f32_16x16x32_bf16 v[96:99], a[200:203], v[144:147], v[96:99]\n" \
"s_add_u32 m0, s79, 49152\n" \
"s_mov_b32 s90, 0\n" \
"ds_read_b128 a[40:43], v233 offset:7168\n" \
"v_mfma_f32_16x16x32_bf16 v[96:99], a[196:199], v[136:139], v[96:99]\n" \
"s_add_u32 s71, s76, 17408\n" \
"v_exp_f32 v36, v36\n" \
"v_cvt_pk_bf16_f32 v32, v32, v33\n" \
"v_mfma_f32_16x16x32_bf16 v[96:99], a[192:195], v[128:131], v[96:99]\n" \
"buffer_load_dwordx4 v234, s[36:39], s71 offen offset:0 sc1 lds\n" \
"v_mfma_f32_16x16x32_bf16 v[112:115], a[192:195], v[160:163], v[196:199]\n" \
"ds_read_b128 a[36:39], v233 offset:14336\n" \
"v_cvt_pk_bf16_f32 v33, v34, v35\n" \
"v_mfma_f32_16x16x32_bf16 v[112:115], a[196:199], v[168:171], v[112:115]\n" \
"v_exp_f32 v37, v37\n" \
"v_mfma_f32_16x16x32_bf16 v[112:115], a[200:203], v[176:179], v[112:115]\n" \
"buffer_load_dwordx4 v234, s[36:39], s71 offen offset:1024 sc1 lds\n" \
"v_mfma_f32_16x16x32_bf16 v[112:115], a[204:207], v[184:187], v[112:115]\n" \
"ds_read_b128 a[32:35], v233 offset:6144\n" \
"v_exp_f32 v38, v38\n" \
"v_exp_f32 v39, v39\n" \
"v_mfma_f32_16x16x32_bf16 v[64:67], a[204:207], a[140:143], v[208:211]\n" \
"v_exp_f32 v48, v48\n" \
"v_mfma_f32_16x16x32_bf16 v[64:67], a[200:203], a[136:139], v[64:67]\n" \
"buffer_load_dwordx4 v234, s[36:39], s71 offen offset:2048 sc1 lds\n" \
"v_cvt_pk_bf16_f32 v34, v36, v37\n" \
"v_mfma_f32_16x16x32_bf16 v[64:67], a[196:199], a[132:135], v[64:67]\n" \
"ds_read_b128 a[204:207], v233 offset:9216\n" \
"v_cvt_pk_bf16_f32 v35, v38, v39\n" \
"v_mfma_f32_16x16x32_bf16 v[64:67], a[192:195], a[128:131], v[64:67]\n" \
"v_exp_f32 v49, v49\n" \
"v_exp_f32 v50, v50\n" \
"v_mfma_f32_16x16x32_bf16 v[68:71], a[208:211], a[128:131], v[212:215]\n" \
"buffer_load_dwordx4 v234, s[36:39], s71 offen offset:3072 sc1 lds\n" \
"v_mfma_f32_16x16x32_bf16 v[68:71], a[212:215], a[132:135], v[68:71]\n" \
"ds_read_b128 a[200:203], v233 offset:1024\n" \
"v_cvt_pk_bf16_f32 v48, v48, v49\n" \
"v_mfma_f32_16x16x32_bf16 v[68:71], a[216:219], a[136:139], v[68:71]\n" \
"s_add_u32 m0, s77, s80\n" \
"v_exp_f32 v51, v51\n" \
"v_mfma_f32_16x16x32_bf16 v[68:71], a[220:223], a[140:143], v[68:71]\n" \
"buffer_load_dwordx4 v234, s[40:43], s76 offen offset:0 sc1 lds\n" \
"v_exp_f32 v52, v52\n" \
"v_exp_f32 v53, v53\n" \
"v_mfma_f32_16x16x32_bf16 v[116:119], a[220:223], v[184:187], v[196:199]\n" \
"ds_read_b128 a[196:199], v233 offset:8192\n" \
"v_cvt_pk_bf16_f32 v49, v50, v51\n" \
"v_mfma_f32_16x16x32_bf16 v[116:119], a[216:219], v[176:179], v[116:119]\n" \
"ds_read_b128 a[160:163], v239 offset:0\n" \
"v_mfma_f32_16x16x32_bf16 v[116:119], a[212:215], v[168:171], v[116:119]\n" \
"s_add_u32 s71, s76, 17408\n" \
"ds_read_b128 a[192:195], v233 offset:0\n" \
"v_exp_f32 v54, v54\n" \
"v_mfma_f32_16x16x32_bf16 v[116:119], a[208:211], v[160:163], v[116:119]\n" \
"buffer_load_dwordx4 v234, s[40:43], s76 offen offset:1024 sc1 lds\n" \
"v_mfma_f32_16x16x32_bf16 v[100:103], a[208:211], v[128:131], v[196:199]\n" \
"ds_read_b128 a[164:167], v239 offset:1024\n" \
"v_cvt_pk_bf16_f32 v50, v52, v53\n" \
"v_mfma_f32_16x16x32_bf16 v[100:103], a[212:215], v[136:139], v[100:103]\n" \
"v_exp_f32 v55, v55\n" \
"v_mfma_f32_16x16x32_bf16 v[100:103], a[216:219], v[144:147], v[100:103]\n" \
"v_cvt_pk_bf16_f32 v51, v54, v55\n" \
"v_exp_f32 v24, v24\n" \
"v_mfma_f32_16x16x32_bf16 v[100:103], a[220:223], v[152:155], v[100:103]\n" \
"buffer_load_dwordx4 v234, s[40:43], s76 offen offset:2048 sc1 lds\n" \
"v_exp_f32 v25, v25\n" \
"v_mfma_f32_16x16x32_bf16 v[84:87], a[220:223], a[156:159], v[196:199]\n" \
"ds_read_b128 a[168:171], v239 offset:2048\n" \
"v_mfma_f32_16x16x32_bf16 v[84:87], a[216:219], a[152:155], v[84:87]\n" \
"ds_read_b128 a[220:223], v233 offset:11264\n" \
"v_mfma_f32_16x16x32_bf16 v[84:87], a[212:215], a[148:151], v[84:87]\n" \
"s_waitcnt lgkmcnt(2)\n" \
"v_exp_f32 v26, v26\n" \
"v_mfma_f32_16x16x32_bf16 v[84:87], a[208:211], a[144:147], v[84:87]\n" \
"buffer_load_dwordx4 v234, s[40:43], s76 offen offset:3072 sc1 lds\n" \
"v_exp_f32 v27, v27\n" \
"v_mfma_f32_16x16x32_bf16 v[88:91], a[224:227], a[144:147], v[196:199]\n" \
"ds_read_b128 a[216:219], v233 offset:3072\n" \
"v_cvt_pk_bf16_f32 v20, v24, v25\n" \
"v_mfma_f32_16x16x32_bf16 v[88:91], a[228:231], a[148:151], v[88:91]\n" \
"ds_read_b128 a[172:175], v239 offset:3072\n" \
"v_exp_f32 v28, v28\n" \
"v_mfma_f32_16x16x32_bf16 v[88:91], a[232:235], a[152:155], v[88:91]\n" \
"s_add_u32 m0, m0, 0x4000\n" \
"ds_read_b128 a[212:215], v233 offset:10240\n" \
"v_xor_b32 v248, 0x2000, v239\n" \
"v_mfma_f32_16x16x32_bf16 v[88:91], a[236:239], a[156:159], v[88:91]\n" \
"buffer_load_dwordx4 v234, s[40:43], s71 offen offset:0 sc1 lds\n" \
"v_exp_f32 v29, v29\n" \
"v_mfma_f32_16x16x32_bf16 v[104:107], a[236:239], v[152:155], v[196:199]\n" \
"ds_read_b128 a[208:211], v233 offset:2048\n" \
"v_lshlrev_b32 v252, s48, v234\n" \
"v_mfma_f32_16x16x32_bf16 v[104:107], a[232:235], v[144:147], v[104:107]\n" \
"ds_read_b128 a[176:179], v239 offset:4096\n" \
"v_mov_b32 v224, 0\n" \
"v_mfma_f32_16x16x32_bf16 v[104:107], a[228:231], v[136:139], v[104:107]\n" \
"v_mov_b32 v225, 0\n" \
"v_mov_b32 v228, 0\n" \
"v_mfma_f32_16x16x32_bf16 v[104:107], a[224:227], v[128:131], v[104:107]\n" \
"buffer_load_dwordx4 v234, s[40:43], s71 offen offset:1024 sc1 lds\n" \
"v_exp_f32 v30, v30\n" \
"v_cvt_pk_bf16_f32 v21, v26, v27\n" \
"v_mfma_f32_16x16x32_bf16 v[120:123], a[224:227], v[160:163], v[196:199]\n" \
"ds_read_b128 a[180:183], v239 offset:5120\n" \
"v_mov_b32 v229, 0\n" \
"v_mfma_f32_16x16x32_bf16 v[120:123], a[228:231], v[168:171], v[120:123]\n" \
"v_exp_f32 v31, v31\n" \
"v_mfma_f32_16x16x32_bf16 v[120:123], a[232:235], v[176:179], v[120:123]\n" \
"v_exp_f32 v40, v40\n" \
"v_cvt_pk_bf16_f32 v22, v28, v29\n" \
"v_mfma_f32_16x16x32_bf16 v[120:123], a[236:239], v[184:187], v[120:123]\n" \
"buffer_load_dwordx4 v234, s[40:43], s71 offen offset:2048 sc1 lds\n" \
"v_exp_f32 v41, v41\n" \
"v_mfma_f32_16x16x32_bf16 v[72:75], a[236:239], a[140:143], v[216:219]\n" \
"ds_read_b128 a[184:187], v239 offset:6144\n" \
"v_cvt_pk_bf16_f32 v23, v30, v31\n" \
"v_mfma_f32_16x16x32_bf16 v[72:75], a[232:235], a[136:139], v[72:75]\n" \
"ds_read_b128 a[236:239], v233 offset:13312\n" \
"v_mfma_f32_16x16x32_bf16 v[72:75], a[228:231], a[132:135], v[72:75]\n" \
"v_exp_f32 v42, v42\n" \
"v_mfma_f32_16x16x32_bf16 v[72:75], a[224:227], a[128:131], v[72:75]\n" \
"buffer_load_dwordx4 v234, s[40:43], s71 offen offset:3072 sc1 lds\n" \
"v_exp_f32 v43, v43\n" \
"v_exp_f32 v44, v44\n" \
"v_mfma_f32_16x16x32_bf16 v[76:79], a[240:243], a[128:131], v[220:223]\n" \
"ds_read_b128 a[232:235], v233 offset:5120\n" \
"v_cvt_pk_bf16_f32 v36, v40, v41\n" \
"v_mfma_f32_16x16x32_bf16 v[76:79], a[244:247], a[132:135], v[76:79]\n" \
"ds_read_b128 a[188:191], v239 offset:7168\n" \
"v_cvt_pk_bf16_f32 v37, v42, v43\n" \
"v_mfma_f32_16x16x32_bf16 v[76:79], a[248:251], a[136:139], v[76:79]\n" \
"ds_read_b128 a[228:231], v233 offset:12288\n" \
"v_mfma_f32_16x16x32_bf16 v[76:79], a[252:255], a[140:143], v[76:79]\n" \
"v_exp_f32 v45, v45\n" \
"v_exp_f32 v46, v46\n" \
"v_mfma_f32_16x16x32_bf16 v[124:127], a[252:255], v[184:187], v[196:199]\n" \
"ds_read_b128 a[224:227], v233 offset:4096\n" \
"v_cvt_pk_bf16_f32 v38, v44, v45\n" \
"v_mfma_f32_16x16x32_bf16 v[124:127], a[248:251], v[176:179], v[124:127]\n" \
"v_exp_f32 v47, v47\n" \
"v_mfma_f32_16x16x32_bf16 v[124:127], a[244:247], v[168:171], v[124:127]\n" \
"v_exp_f32 v56, v56\n" \
"v_exp_f32 v57, v57\n" \
"v_mfma_f32_16x16x32_bf16 v[124:127], a[240:243], v[160:163], v[124:127]\n" \
"v_exp_f32 v58, v58\n" \
"v_cvt_pk_bf16_f32 v39, v46, v47\n" \
"v_mfma_f32_16x16x32_bf16 v[108:111], a[240:243], v[128:131], v[196:199]\n" \
"v_exp_f32 v59, v59\n" \
"v_mfma_f32_16x16x32_bf16 v[108:111], a[244:247], v[136:139], v[108:111]\n" \
"v_exp_f32 v60, v60\n" \
"v_mfma_f32_16x16x32_bf16 v[108:111], a[248:251], v[144:147], v[108:111]\n" \
"v_cvt_pk_bf16_f32 v52, v56, v57\n" \
"v_cvt_pk_bf16_f32 v53, v58, v59\n" \
"v_mfma_f32_16x16x32_bf16 v[108:111], a[252:255], v[152:155], v[108:111]\n" \
"v_exp_f32 v61, v61\n" \
"v_exp_f32 v62, v62\n" \
"v_exp_f32 v63, v63\n" \
"v_mfma_f32_16x16x32_bf16 v[92:95], a[252:255], a[156:159], v[196:199]\n" \
"v_add_u32 v232, s91, v237\n" \
"v_cvt_pk_bf16_f32 v54, v60, v61\n" \
"v_mfma_f32_16x16x32_bf16 v[92:95], a[248:251], a[152:155], v[92:95]\n" \
"v_cvt_pk_bf16_f32 v55, v62, v63\n" \
"v_mfma_f32_16x16x32_bf16 v[92:95], a[244:247], a[148:151], v[92:95]\n" \
"s_waitcnt vmcnt(8) lgkmcnt(0)\n" \
"s_barrier\n" \
"v_mfma_f32_16x16x32_bf16 v[92:95], a[240:243], a[144:147], v[92:95]\n" \
"buffer_store_dword v192, v193, s[56:59], s68 offen sc1\n" \
"v_mfma_f32_16x16x32_bf16 v[160:163], a[192:195], v[16:19], 0\n" \
"s_add_u32 m0, s74, s79\n" \
"ds_read_b128 a[240:243], v232 offset:8448\n" \
"v_xor_b32 v233, 0x4000, v233\n" \
"v_mfma_f32_16x16x32_bf16 v[160:163], a[196:199], v[20:23], v[160:163]\n" \
"buffer_load_dwordx4 v234, s[36:39], s73 offen offset:0 sc1 lds\n" \
"v_exp_f32 v80, v80\n" \
"v_mfma_f32_16x16x32_bf16 a[64:67], a[192:195], v[32:35], 0\n" \
"ds_read_b128 a[244:247], v232 offset:10496\n" \
"v_mfma_f32_16x16x32_bf16 a[64:67], a[196:199], v[36:39], a[64:67]\n" \
"ds_read_b128 a[0:3], v248 offset:0\n" \
"v_exp_f32 v81, v81\n" \
"v_mfma_f32_16x16x32_bf16 a[96:99], a[192:195], v[48:51], 0\n" \
"v_exp_f32 v82, v82\n" \
"v_mfma_f32_16x16x32_bf16 a[96:99], a[196:199], v[52:55], a[96:99]\n" \
"s_add_u32 s71, s73, 17408\n" \
"ds_read_b128 a[192:195], v232 offset:0\n" \
"v_cvt_pk_bf16_f32 v80, v80, v81\n" \
"v_mfma_f32_16x16x32_bf16 a[100:103], a[200:203], v[48:51], 0\n" \
"buffer_load_dwordx4 v234, s[36:39], s73 offen offset:1024 sc1 lds\n" \
"v_mfma_f32_16x16x32_bf16 a[100:103], a[204:207], v[52:55], a[100:103]\n" \
"ds_read_b128 a[196:199], v232 offset:2048\n" \
"v_exp_f32 v83, v83\n" \
"v_mfma_f32_16x16x32_bf16 a[68:71], a[200:203], v[32:35], 0\n" \
"ds_read_b128 a[4:7], v248 offset:1024\n" \
"v_mfma_f32_16x16x32_bf16 a[68:71], a[204:207], v[36:39], a[68:71]\n" \
"v_exp_f32 v84, v84\n" \
"v_exp_f32 v85, v85\n" \
"v_mfma_f32_16x16x32_bf16 v[164:167], a[200:203], v[16:19], 0\n" \
"ds_read_b128 a[8:11], v248 offset:2048\n" \
"v_cvt_pk_bf16_f32 v81, v82, v83\n" \
"v_mfma_f32_16x16x32_bf16 v[164:167], a[204:207], v[20:23], v[164:167]\n" \
"buffer_load_dwordx4 v234, s[36:39], s73 offen offset:2048 sc1 lds\n" \
"v_mfma_f32_16x16x32_bf16 v[168:171], a[208:211], v[16:19], 0\n" \
"ds_read_b128 a[200:203], v232 offset:4096\n" \
"v_mfma_f32_16x16x32_bf16 v[168:171], a[212:215], v[20:23], v[168:171]\n" \
"ds_read_b128 a[204:207], v232 offset:6144\n" \
"v_exp_f32 v86, v86\n" \
"v_mfma_f32_16x16x32_bf16 a[72:75], a[208:211], v[32:35], 0\n" \
"ds_read_b128 a[12:15], v248 offset:3072\n" \
"v_cvt_pk_bf16_f32 v82, v84, v85\n" \
"v_mfma_f32_16x16x32_bf16 a[72:75], a[212:215], v[36:39], a[72:75]\n" \
"v_exp_f32 v87, v87\n" \
"v_exp_f32 v96, v96\n" \
"v_mfma_f32_16x16x32_bf16 a[104:107], a[208:211], v[48:51], 0\n" \
"buffer_load_dwordx4 v234, s[36:39], s73 offen offset:3072 sc1 lds\n" \
"v_mfma_f32_16x16x32_bf16 a[104:107], a[212:215], v[52:55], a[104:107]\n" \
"ds_read_b128 a[208:211], v232 offset:256\n" \
"v_cvt_pk_bf16_f32 v83, v86, v87\n" \
"v_smfmac_f32_16x16x64_bf16 a[52:55], v[244:247], v[16:23], v243\n" \
"ds_read_b128 a[212:215], v232 offset:2304\n" \
"v_exp_f32 v97, v97\n" \
"v_mfma_f32_16x16x32_bf16 a[108:111], a[216:219], v[48:51], 0\n" \
"ds_read_b128 a[16:19], v248 offset:4096\n" \
"v_mfma_f32_16x16x32_bf16 a[108:111], a[220:223], v[52:55], a[108:111]\n" \
"s_add_u32 m0, m0, 0x4000\n" \
"v_exp_f32 v98, v98\n" \
"v_mfma_f32_16x16x32_bf16 a[76:79], a[216:219], v[32:35], 0\n" \
"buffer_load_dwordx4 v234, s[36:39], s71 offen offset:0 sc1 lds\n" \
"v_mfma_f32_16x16x32_bf16 a[76:79], a[220:223], v[36:39], a[76:79]\n" \
"ds_read_b128 a[20:23], v248 offset:5120\n" \
"v_cvt_pk_bf16_f32 v96, v96, v97\n" \
"v_exp_f32 v99, v99\n" \
"v_mfma_f32_16x16x32_bf16 v[172:175], a[216:219], v[16:19], 0\n" \
"v_exp_f32 v100, v100\n" \
"v_mfma_f32_16x16x32_bf16 v[172:175], a[220:223], v[20:23], v[172:175]\n" \
"ds_read_b128 a[216:219], v232 offset:4352\n" \
"v_cvt_pk_bf16_f32 v97, v98, v99\n" \
"v_mfma_f32_32x32x16_bf16 v[128:143], a[48:51], a[48:51], 0\n" \
"ds_read_b128 a[220:223], v232 offset:6400\n" \
"v_exp_f32 v101, v101\n" \
"v_mfma_f32_16x16x32_bf16 v[176:179], a[224:227], v[16:19], 0\n" \
"buffer_load_dwordx4 v234, s[36:39], s71 offen offset:1024 sc1 lds\n" \
"v_mfma_f32_16x16x32_bf16 v[176:179], a[228:231], v[20:23], v[176:179]\n" \
"ds_read_b128 a[24:27], v248 offset:6144\n" \
"v_exp_f32 v102, v102\n" \
"v_mfma_f32_16x16x32_bf16 a[80:83], a[224:227], v[32:35], 0\n" \
"v_exp_f32 v103, v103\n" \
"v_mfma_f32_16x16x32_bf16 a[80:83], a[228:231], v[36:39], a[80:83]\n" \
"ds_read_b128 a[28:31], v248 offset:7168\n" \
"v_cvt_pk_bf16_f32 v98, v100, v101\n" \
"v_exp_f32 v112, v112\n" \
"v_mfma_f32_16x16x32_bf16 a[112:115], a[224:227], v[48:51], 0\n" \
"v_exp_f32 v113, v113\n" \
"v_mfma_f32_16x16x32_bf16 a[112:115], a[228:231], v[52:55], a[112:115]\n" \
"buffer_load_dwordx4 v234, s[36:39], s71 offen offset:2048 sc1 lds\n" \
"v_smfmac_f32_16x16x64_bf16 v[224:227], v[244:247], v[32:39], v243\n" \
"ds_read_b128 a[224:227], v232 offset:8192\n" \
"v_cvt_pk_bf16_f32 v99, v102, v103\n" \
"v_mfma_f32_16x16x32_bf16 a[116:119], a[232:235], v[48:51], 0\n" \
"ds_read_b128 a[228:231], v232 offset:10240\n" \
"v_mfma_f32_16x16x32_bf16 a[116:119], a[236:239], v[52:55], a[116:119]\n" \
"v_exp_f32 v114, v114\n" \
"v_exp_f32 v115, v115\n" \
"v_mfma_f32_16x16x32_bf16 a[84:87], a[232:235], v[32:35], 0\n" \
"v_exp_f32 v116, v116\n" \
"v_mfma_f32_16x16x32_bf16 a[84:87], a[236:239], v[36:39], a[84:87]\n" \
"buffer_load_dwordx4 v234, s[36:39], s71 offen offset:3072 sc1 lds\n" \
"v_cvt_pk_bf16_f32 v112, v112, v113\n" \
"v_mfma_f32_16x16x32_bf16 v[180:183], a[232:235], v[16:19], 0\n" \
"v_exp_f32 v117, v117\n" \
"v_mfma_f32_16x16x32_bf16 v[180:183], a[236:239], v[20:23], v[180:183]\n" \
"s_mov_b32 s76, s73\n" \
"s_add_u32 s73, s73, 34816\n" \
"ds_read_b128 a[232:235], v232 offset:12288\n" \
"v_mfma_f32_32x32x16_bf16 v[144:159], a[48:51], a[48:51], 0\n" \
"s_cmp_eq_u32 s73, s83\n" \
"s_cselect_b32 s70, 34816, 0\n" \
"ds_read_b128 a[236:239], v232 offset:14336\n" \
"v_exp_f32 v118, v118\n" \
"v_mfma_f32_16x16x32_bf16 v[184:187], a[32:35], v[16:19], 0\n" \
"s_add_u32 s73, s73, s70\n" \
"s_xor_b32 s74, s74, 0x8000\n" \
"ds_read_b128 a[248:251], v232 offset:12544\n" \
"v_mfma_f32_16x16x32_bf16 v[184:187], a[36:39], v[20:23], v[184:187]\n" \
"s_xor_b32 s77, s77, 0x8000\n" \
"ds_read_b128 a[252:255], v232 offset:14592\n" \
"v_cvt_pk_bf16_f32 v113, v114, v115\n" \
"v_mfma_f32_16x16x32_bf16 a[88:91], a[32:35], v[32:35], 0\n" \
"v_exp_f32 v119, v119\n" \
"v_mfma_f32_16x16x32_bf16 a[88:91], a[36:39], v[36:39], a[88:91]\n" \
"v_exp_f32 v64, v64\n" \
"v_cvt_pk_bf16_f32 v114, v116, v117\n" \
"v_mfma_f32_16x16x32_bf16 a[120:123], a[32:35], v[48:51], 0\n" \
"v_exp_f32 v65, v65\n" \
"v_mfma_f32_16x16x32_bf16 a[120:123], a[36:39], v[52:55], a[120:123]\n" \
"v_exp_f32 v66, v66\n" \
"v_cvt_pk_bf16_f32 v115, v118, v119\n" \
"v_smfmac_f32_16x16x64_bf16 v[228:231], v[244:247], v[48:55], v243\n" \
"v_cvt_pk_bf16_f32 v64, v64, v65\n" \
"v_mfma_f32_16x16x32_bf16 a[124:127], a[40:43], v[48:51], 0\n" \
"v_exp_f32 v67, v67\n" \
"v_mfma_f32_16x16x32_bf16 a[124:127], a[44:47], v[52:55], a[124:127]\n" \
"v_exp_f32 v68, v68\n" \
"v_exp_f32 v69, v69\n" \
"v_mfma_f32_16x16x32_bf16 a[92:95], a[40:43], v[32:35], 0\n" \
"v_exp_f32 v70, v70\n" \
"v_mfma_f32_16x16x32_bf16 a[92:95], a[44:47], v[36:39], a[92:95]\n" \
"v_cvt_pk_bf16_f32 v65, v66, v67\n" \
"v_cvt_pk_bf16_f32 v66, v68, v69\n" \
"v_mfma_f32_16x16x32_bf16 v[188:191], a[40:43], v[16:19], 0\n" \
"s_cmp_eq_u32 s63, 0\n" \
"v_exp_f32_e64 v71, v71\n" \
"v_mfma_f32_16x16x32_bf16 v[188:191], a[44:47], v[20:23], v[188:191]\n" \
"s_waitcnt lgkmcnt(0)\n" \
"v_cvt_pk_bf16_f32 v67, v70, v71\n" \
"s_cbranch_scc1 Lloop2_%=\n" \
"Lloop_%=:\n" \
"v_mfma_f32_16x16x32_bf16 v[0:3], a[192:195], a[128:131], v[196:199]\n" \
"v_exp_f32 v72, v72\n" \
"v_mfma_f32_16x16x32_bf16 v[0:3], a[196:199], a[132:135], v[0:3]\n" \
"v_mfma_f32_16x16x32_bf16 v[0:3], a[200:203], a[136:139], v[0:3]\n" \
"v_exp_f32 v73, v73\n" \
"v_mfma_f32_16x16x32_bf16 v[0:3], a[204:207], a[140:143], v[0:3]\n" \
"ds_read_b128 a[32:35], v233 offset:6144\n" \
"v_cvt_pk_bf16_f32 v68, v72, v73\n" \
"v_mfma_f32_16x16x32_bf16 v[16:19], a[204:207], a[156:159], v[196:199]\n" \
"v_exp_f32 v74, v74\n" \
"v_mfma_f32_16x16x32_bf16 v[16:19], a[200:203], a[152:155], v[16:19]\n" \
"global_load_dword v253, v252, s[44:45]\n" \
"v_mfma_f32_16x16x32_bf16 v[16:19], a[196:199], a[148:151], v[16:19]\n" \
"s_sub_u32 s46, s46, 1\n" \
"v_exp_f32 v75, v75\n" \
"v_mfma_f32_16x16x32_bf16 v[16:19], a[192:195], a[144:147], v[16:19]\n" \
"ds_read_b128 a[36:39], v233 offset:14336\n" \
"v_cvt_pk_bf16_f32 v69, v74, v75\n" \
"v_mfma_f32_16x16x32_bf16 v[32:35], a[192:195], a[160:163], v[196:199]\n" \
"v_exp_f32 v76, v76\n" \
"v_mfma_f32_16x16x32_bf16 v[32:35], a[196:199], a[164:167], v[32:35]\n" \
"s_cmp_gt_i32 s46, 0\n" \
"s_cselect_b32 s47, 0x100000, 0\n" \
"v_mfma_f32_16x16x32_bf16 v[32:35], a[200:203], a[168:171], v[32:35]\n" \
"v_exp_f32 v77, v77\n" \
"v_mfma_f32_16x16x32_bf16 v[32:35], a[204:207], a[172:175], v[32:35]\n" \
"s_add_u32 s44, s44, s47\n" \
"s_addc_u32 s45, s45, 0\n" \
"ds_read_b128 a[40:43], v233 offset:7168\n" \
"v_cvt_pk_bf16_f32 v70, v76, v77\n" \
"v_mfma_f32_16x16x32_bf16 v[48:51], a[204:207], a[188:191], v[196:199]\n" \
"v_exp_f32 v78, v78\n" \
"v_mfma_f32_16x16x32_bf16 v[48:51], a[200:203], a[184:187], v[48:51]\n" \
"v_xor_b32 v232, 0x4000, v232\n" \
"v_mfma_f32_16x16x32_bf16 v[48:51], a[196:199], a[180:183], v[48:51]\n" \
"v_exp_f32 v79, v79\n" \
"v_mfma_f32_16x16x32_bf16 v[48:51], a[192:195], a[176:179], v[48:51]\n" \
"ds_read_b128 a[204:207], v233 offset:9216\n" \
"v_cvt_pk_bf16_f32 v71, v78, v79\n" \
"v_mfma_f32_16x16x32_bf16 v[52:55], a[208:211], a[176:179], v[196:199]\n" \
"v_exp_f32 v88, v88\n" \
"v_mfma_f32_16x16x32_bf16 v[52:55], a[212:215], a[180:183], v[52:55]\n" \
"v_mfma_f32_16x16x32_bf16 v[52:55], a[216:219], a[184:187], v[52:55]\n" \
"v_exp_f32 v89, v89\n" \
"v_mfma_f32_16x16x32_bf16 v[52:55], a[220:223], a[188:191], v[52:55]\n" \
"ds_read_b128 a[200:203], v233 offset:1024\n" \
"v_cvt_pk_bf16_f32 v84, v88, v89\n" \
"v_mfma_f32_16x16x32_bf16 v[36:39], a[220:223], a[172:175], v[196:199]\n" \
"v_exp_f32 v90, v90\n" \
"v_mfma_f32_16x16x32_bf16 v[36:39], a[216:219], a[168:171], v[36:39]\n" \
"v_mfma_f32_16x16x32_bf16 v[36:39], a[212:215], a[164:167], v[36:39]\n" \
"v_exp_f32 v91, v91\n" \
"v_mfma_f32_16x16x32_bf16 v[36:39], a[208:211], a[160:163], v[36:39]\n" \
"ds_read_b128 a[196:199], v233 offset:8192\n" \
"v_cvt_pk_bf16_f32 v85, v90, v91\n" \
"v_mfma_f32_16x16x32_bf16 v[20:23], a[208:211], a[144:147], v[196:199]\n" \
"v_exp_f32 v92, v92\n" \
"v_mfma_f32_16x16x32_bf16 v[20:23], a[212:215], a[148:151], v[20:23]\n" \
"v_mfma_f32_16x16x32_bf16 v[20:23], a[216:219], a[152:155], v[20:23]\n" \
"v_exp_f32 v93, v93\n" \
"v_mfma_f32_16x16x32_bf16 v[20:23], a[220:223], a[156:159], v[20:23]\n" \
"ds_read_b128 a[192:195], v233 offset:0\n" \
"v_cvt_pk_bf16_f32 v86, v92, v93\n" \
"v_mfma_f32_16x16x32_bf16 v[4:7], a[220:223], a[140:143], v[196:199]\n" \
"v_exp_f32 v94, v94\n" \
"v_mfma_f32_16x16x32_bf16 v[4:7], a[216:219], a[136:139], v[4:7]\n" \
"v_mfma_f32_16x16x32_bf16 v[4:7], a[212:215], a[132:135], v[4:7]\n" \
"s_waitcnt lgkmcnt(7)\n" \
"v_exp_f32 v95, v95\n" \
"v_mfma_f32_16x16x32_bf16 v[4:7], a[208:211], a[128:131], v[4:7]\n" \
"ds_read_b128 a[220:223], v233 offset:11264\n" \
"v_cvt_pk_bf16_f32 v87, v94, v95\n" \
"v_mfma_f32_16x16x32_bf16 v[8:11], a[224:227], a[128:131], v[196:199]\n" \
"v_exp_f32 v104, v104\n" \
"v_mfma_f32_16x16x32_bf16 v[8:11], a[228:231], a[132:135], v[8:11]\n" \
"v_mfma_f32_16x16x32_bf16 v[8:11], a[232:235], a[136:139], v[8:11]\n" \
"v_exp_f32 v105, v105\n" \
"v_mfma_f32_16x16x32_bf16 v[8:11], a[236:239], a[140:143], v[8:11]\n" \
"ds_read_b128 a[216:219], v233 offset:3072\n" \
"v_cvt_pk_bf16_f32 v100, v104, v105\n" \
"v_mfma_f32_16x16x32_bf16 v[24:27], a[236:239], a[156:159], v[196:199]\n" \
"v_exp_f32 v106, v106\n" \
"v_mfma_f32_16x16x32_bf16 v[24:27], a[232:235], a[152:155], v[24:27]\n" \
"v_mfma_f32_16x16x32_bf16 v[24:27], a[228:231], a[148:151], v[24:27]\n" \
"v_exp_f32 v107, v107\n" \
"v_mfma_f32_16x16x32_bf16 v[24:27], a[224:227], a[144:147], v[24:27]\n" \
"ds_read_b128 a[212:215], v233 offset:10240\n" \
"v_cvt_pk_bf16_f32 v101, v106, v107\n" \
"v_mfma_f32_16x16x32_bf16 v[40:43], a[224:227], a[160:163], v[196:199]\n" \
"v_exp_f32 v108, v108\n" \
"v_mfma_f32_16x16x32_bf16 v[40:43], a[228:231], a[164:167], v[40:43]\n" \
"v_mfma_f32_16x16x32_bf16 v[40:43], a[232:235], a[168:171], v[40:43]\n" \
"v_exp_f32 v109, v109\n" \
"v_mfma_f32_16x16x32_bf16 v[40:43], a[236:239], a[172:175], v[40:43]\n" \
"ds_read_b128 a[208:211], v233 offset:2048\n" \
"v_cvt_pk_bf16_f32 v102, v108, v109\n" \
"v_mfma_f32_16x16x32_bf16 v[56:59], a[236:239], a[188:191], v[196:199]\n" \
"v_exp_f32 v110, v110\n" \
"v_mfma_f32_16x16x32_bf16 v[56:59], a[232:235], a[184:187], v[56:59]\n" \
"v_mfma_f32_16x16x32_bf16 v[56:59], a[228:231], a[180:183], v[56:59]\n" \
"v_exp_f32 v111, v111\n" \
"v_mfma_f32_16x16x32_bf16 v[56:59], a[224:227], a[176:179], v[56:59]\n" \
"ds_read_b128 a[236:239], v233 offset:13312\n" \
"v_cvt_pk_bf16_f32 v103, v110, v111\n" \
"v_mfma_f32_16x16x32_bf16 v[60:63], a[240:243], a[176:179], v[196:199]\n" \
"v_exp_f32 v120, v120\n" \
"v_mfma_f32_16x16x32_bf16 v[60:63], a[244:247], a[180:183], v[60:63]\n" \
"v_mfma_f32_16x16x32_bf16 v[60:63], a[248:251], a[184:187], v[60:63]\n" \
"v_exp_f32 v121, v121\n" \
"v_mfma_f32_16x16x32_bf16 v[60:63], a[252:255], a[188:191], v[60:63]\n" \
"ds_read_b128 a[232:235], v233 offset:5120\n" \
"v_cvt_pk_bf16_f32 v116, v120, v121\n" \
"v_mfma_f32_16x16x32_bf16 v[44:47], a[252:255], a[172:175], v[196:199]\n" \
"v_exp_f32 v122, v122\n" \
"v_mfma_f32_16x16x32_bf16 v[44:47], a[248:251], a[168:171], v[44:47]\n" \
"v_mfma_f32_16x16x32_bf16 v[44:47], a[244:247], a[164:167], v[44:47]\n" \
"v_exp_f32 v123, v123\n" \
"v_mfma_f32_16x16x32_bf16 v[44:47], a[240:243], a[160:163], v[44:47]\n" \
"ds_read_b128 a[228:231], v233 offset:12288\n" \
"v_cvt_pk_bf16_f32 v117, v122, v123\n" \
"v_mfma_f32_16x16x32_bf16 v[28:31], a[240:243], a[144:147], v[196:199]\n" \
"v_exp_f32 v124, v124\n" \
"v_mfma_f32_16x16x32_bf16 v[28:31], a[244:247], a[148:151], v[28:31]\n" \
"v_mfma_f32_16x16x32_bf16 v[28:31], a[248:251], a[152:155], v[28:31]\n" \
"v_exp_f32 v125, v125\n" \
"v_mfma_f32_16x16x32_bf16 v[28:31], a[252:255], a[156:159], v[28:31]\n" \
"ds_read_b128 a[224:227], v233 offset:4096\n" \
"v_cvt_pk_bf16_f32 v118, v124, v125\n" \
"v_mfma_f32_16x16x32_bf16 v[12:15], a[252:255], a[140:143], v[196:199]\n" \
"v_exp_f32 v126, v126\n" \
"v_mfma_f32_16x16x32_bf16 v[12:15], a[248:251], a[136:139], v[12:15]\n" \
"Lpg5598_%=:\n" \
"s_cbranch_execz Lpg6789_%=\n" \
"v_mfma_f32_16x16x32_bf16 v[12:15], a[244:247], a[132:135], v[12:15]\n" \
"s_waitcnt lgkmcnt(2)\n" \
"v_exp_f32 v127, v127\n" \
"v_mfma_f32_16x16x32_bf16 v[12:15], a[240:243], a[128:131], v[12:15]\n" \
"ds_read_b128 a[44:47], v233 offset:15360\n" \
"v_cvt_pk_bf16_f32 v119, v126, v127\n" \
"v_mfma_f32_16x16x32_bf16 v[128:131], a[192:195], v[64:67], v[128:131]\n" \
"v_exp_f32 v0, v0\n" \
"v_mfma_f32_16x16x32_bf16 v[128:131], a[196:199], v[68:71], v[128:131]\n" \
"ds_read_b128 a[244:247], v232 offset:10496\n" \
"v_mfma_f32_16x16x32_bf16 v[160:163], a[192:195], v[80:83], v[160:163]\n" \
"v_exp_f32 v1, v1\n" \
"v_mfma_f32_16x16x32_bf16 v[160:163], a[196:199], v[84:87], v[160:163]\n" \
"ds_read_b128 a[240:243], v232 offset:8448\n" \
"v_cvt_pk_bf16_f32 v0, v0, v1\n" \
"v_mfma_f32_16x16x32_bf16 a[64:67], a[192:195], v[96:99], a[64:67]\n" \
"v_exp_f32 v2, v2\n" \
"v_mfma_f32_16x16x32_bf16 a[64:67], a[196:199], v[100:103], a[64:67]\n" \
"ds_read_b128 a[248:251], v232 offset:12544\n" \
"v_xor_b32 v233, 0xc000, v233\n" \
"v_mfma_f32_16x16x32_bf16 a[96:99], a[192:195], v[112:115], a[96:99]\n" \
"v_exp_f32 v3, v3\n" \
"v_mfma_f32_16x16x32_bf16 a[96:99], a[196:199], v[116:119], a[96:99]\n" \
"ds_read_b128 a[192:195], v232 offset:0\n" \
"v_cvt_pk_bf16_f32 v1, v2, v3\n" \
"v_mfma_f32_16x16x32_bf16 a[100:103], a[200:203], v[112:115], a[100:103]\n" \
"v_exp_f32 v4, v4\n" \
"v_mfma_f32_16x16x32_bf16 a[100:103], a[204:207], v[116:119], a[100:103]\n" \
"ds_read_b128 a[252:255], v232 offset:14592\n" \
"v_mfma_f32_16x16x32_bf16 a[68:71], a[200:203], v[96:99], a[68:71]\n" \
"v_exp_f32 v5, v5\n" \
"v_mfma_f32_16x16x32_bf16 a[68:71], a[204:207], v[100:103], a[68:71]\n" \
"ds_read_b128 a[196:199], v232 offset:2048\n" \
"v_cvt_pk_bf16_f32 v2, v4, v5\n" \
"v_smfmac_f32_16x16x64_bf16 a[48:51], v[244:247], v[64:71], v243\n" \
"v_mfma_f32_16x16x32_bf16 v[164:167], a[200:203], v[80:83], v[164:167]\n" \
"v_exp_f32 v6, v6\n" \
"v_mfma_f32_16x16x32_bf16 v[164:167], a[204:207], v[84:87], v[164:167]\n" \
"v_mfma_f32_16x16x32_bf16 v[132:135], a[200:203], v[64:67], v[132:135]\n" \
"v_exp_f32 v7, v7\n" \
"v_mfma_f32_16x16x32_bf16 v[132:135], a[204:207], v[68:71], v[132:135]\n" \
"ds_read_b128 a[200:203], v232 offset:4096\n" \
"v_cvt_pk_bf16_f32 v3, v6, v7\n" \
"v_mfma_f32_16x16x32_bf16 v[136:139], a[208:211], v[64:67], v[136:139]\n" \
"v_exp_f32 v16, v16\n" \
"v_mfma_f32_16x16x32_bf16 v[136:139], a[212:215], v[68:71], v[136:139]\n" \
"v_mfma_f32_16x16x32_bf16 v[168:171], a[208:211], v[80:83], v[168:171]\n" \
"v_exp_f32 v17, v17\n" \
"v_mfma_f32_16x16x32_bf16 v[168:171], a[212:215], v[84:87], v[168:171]\n" \
"ds_read_b128 a[204:207], v232 offset:6144\n" \
"v_cvt_pk_bf16_f32 v16, v16, v17\n" \
"v_mfma_f32_16x16x32_bf16 a[72:75], a[208:211], v[96:99], a[72:75]\n" \
"v_exp_f32 v18, v18\n" \
"v_mfma_f32_16x16x32_bf16 a[72:75], a[212:215], v[100:103], a[72:75]\n" \
"v_mfma_f32_16x16x32_bf16 a[104:107], a[208:211], v[112:115], a[104:107]\n" \
"v_exp_f32 v19, v19\n" \
"v_mfma_f32_16x16x32_bf16 a[104:107], a[212:215], v[116:119], a[104:107]\n" \
"ds_read_b128 a[208:211], v232 offset:256\n" \
"v_cvt_pk_bf16_f32 v17, v18, v19\n" \
"v_mfma_f32_16x16x32_bf16 a[108:111], a[216:219], v[112:115], a[108:111]\n" \
"v_exp_f32 v20, v20\n" \
"v_mfma_f32_16x16x32_bf16 a[108:111], a[220:223], v[116:119], a[108:111]\n" \
"v_mfma_f32_16x16x32_bf16 a[76:79], a[216:219], v[96:99], a[76:79]\n" \
"v_exp_f32 v21, v21\n" \
"v_mfma_f32_16x16x32_bf16 a[76:79], a[220:223], v[100:103], a[76:79]\n" \
"ds_read_b128 a[212:215], v232 offset:2304\n" \
"v_cvt_pk_bf16_f32 v18, v20, v21\n" \
"v_smfmac_f32_16x16x64_bf16 a[52:55], v[244:247], v[80:87], v243\n" \
"v_mfma_f32_16x16x32_bf16 v[172:175], a[216:219], v[80:83], v[172:175]\n" \
"v_exp_f32 v22, v22\n" \
"v_mfma_f32_16x16x32_bf16 v[172:175], a[220:223], v[84:87], v[172:175]\n" \
"v_mfma_f32_16x16x32_bf16 v[140:143], a[216:219], v[64:67], v[140:143]\n" \
"s_waitcnt lgkmcnt(10)\n" \
"v_exp_f32 v23, v23\n" \
"v_mfma_f32_16x16x32_bf16 v[140:143], a[220:223], v[68:71], v[140:143]\n" \
"ds_read_b128 a[216:219], v232 offset:4352\n" \
"v_cvt_pk_bf16_f32 v19, v22, v23\n" \
"v_mfma_f32_16x16x32_bf16 v[144:147], a[224:227], v[64:67], v[144:147]\n" \
"v_exp_f32 v32, v32\n" \
"v_mfma_f32_16x16x32_bf16 v[144:147], a[228:231], v[68:71], v[144:147]\n" \
"v_mfma_f32_16x16x32_bf16 v[176:179], a[224:227], v[80:83], v[176:179]\n" \
"v_exp_f32 v33, v33\n" \
"v_mfma_f32_16x16x32_bf16 v[176:179], a[228:231], v[84:87], v[176:179]\n" \
"ds_read_b128 a[220:223], v232 offset:6400\n" \
"v_cvt_pk_bf16_f32 v32, v32, v33\n" \
"v_mfma_f32_16x16x32_bf16 a[80:83], a[224:227], v[96:99], a[80:83]\n" \
"v_exp_f32 v34, v34\n" \
"v_mfma_f32_16x16x32_bf16 a[80:83], a[228:231], v[100:103], a[80:83]\n" \
"v_mfma_f32_16x16x32_bf16 a[112:115], a[224:227], v[112:115], a[112:115]\n" \
"v_exp_f32 v35, v35\n" \
"v_mfma_f32_16x16x32_bf16 a[112:115], a[228:231], v[116:119], a[112:115]\n" \
"ds_read_b128 a[224:227], v232 offset:8192\n" \
"v_cvt_pk_bf16_f32 v33, v34, v35\n" \
"v_mfma_f32_16x16x32_bf16 a[116:119], a[232:235], v[112:115], a[116:119]\n" \
"v_exp_f32 v36, v36\n" \
"v_mfma_f32_16x16x32_bf16 a[116:119], a[236:239], v[116:119], a[116:119]\n" \
"v_mfma_f32_16x16x32_bf16 a[84:87], a[232:235], v[96:99], a[84:87]\n" \
"v_exp_f32 v37, v37\n" \
"v_mfma_f32_16x16x32_bf16 a[84:87], a[236:239], v[100:103], a[84:87]\n" \
"ds_read_b128 a[228:231], v232 offset:10240\n" \
"v_cvt_pk_bf16_f32 v34, v36, v37\n" \
"v_smfmac_f32_16x16x64_bf16 v[224:227], v[244:247], v[96:103], v243\n" \
"v_mfma_f32_16x16x32_bf16 v[180:183], a[232:235], v[80:83], v[180:183]\n" \
"v_exp_f32 v38, v38\n" \
"v_mfma_f32_16x16x32_bf16 v[180:183], a[236:239], v[84:87], v[180:183]\n" \
"v_mfma_f32_16x16x32_bf16 v[148:151], a[232:235], v[64:67], v[148:151]\n" \
"v_exp_f32 v39, v39\n" \
"v_mfma_f32_16x16x32_bf16 v[148:151], a[236:239], v[68:71], v[148:151]\n" \
"ds_read_b128 a[232:235], v232 offset:12288\n" \
"v_cvt_pk_bf16_f32 v35, v38, v39\n" \
"v_mfma_f32_16x16x32_bf16 v[152:155], a[32:35], v[64:67], v[152:155]\n" \
"v_exp_f32 v48, v48\n" \
"v_mfma_f32_16x16x32_bf16 v[152:155], a[36:39], v[68:71], v[152:155]\n" \
"v_mfma_f32_16x16x32_bf16 v[184:187], a[32:35], v[80:83], v[184:187]\n" \
"v_exp_f32 v49, v49\n" \
"v_mfma_f32_16x16x32_bf16 v[184:187], a[36:39], v[84:87], v[184:187]\n" \
"ds_read_b128 a[236:239], v232 offset:14336\n" \
"v_cvt_pk_bf16_f32 v48, v48, v49\n" \
"v_mfma_f32_16x16x32_bf16 a[88:91], a[32:35], v[96:99], a[88:91]\n" \
"v_exp_f32 v50, v50\n" \
"v_mfma_f32_16x16x32_bf16 a[88:91], a[36:39], v[100:103], a[88:91]\n" \
"v_mfma_f32_16x16x32_bf16 a[120:123], a[32:35], v[112:115], a[120:123]\n" \
"v_exp_f32 v51, v51\n" \
"v_mfma_f32_16x16x32_bf16 a[120:123], a[36:39], v[116:119], a[120:123]\n" \
"v_cvt_pk_bf16_f32 v49, v50, v51\n" \
"v_mfma_f32_16x16x32_bf16 a[124:127], a[40:43], v[112:115], a[124:127]\n" \
"v_exp_f32 v52, v52\n" \
"v_mfma_f32_16x16x32_bf16 a[124:127], a[44:47], v[116:119], a[124:127]\n" \
"v_mfma_f32_16x16x32_bf16 a[92:95], a[40:43], v[96:99], a[92:95]\n" \
"v_exp_f32 v53, v53\n" \
"v_mfma_f32_16x16x32_bf16 a[92:95], a[44:47], v[100:103], a[92:95]\n" \
"v_cvt_pk_bf16_f32 v50, v52, v53\n" \
"v_smfmac_f32_16x16x64_bf16 v[228:231], v[244:247], v[112:119], v243\n" \
"v_mfma_f32_16x16x32_bf16 v[188:191], a[40:43], v[80:83], v[188:191]\n" \
"v_exp_f32 v54, v54\n" \
"v_mfma_f32_16x16x32_bf16 v[188:191], a[44:47], v[84:87], v[188:191]\n" \
"v_mfma_f32_16x16x32_bf16 v[156:159], a[40:43], v[64:67], v[156:159]\n" \
"s_waitcnt vmcnt(1) lgkmcnt(0)\n" \
"v_exp_f32 v55, v55\n" \
"v_mfma_f32_16x16x32_bf16 v[156:159], a[44:47], v[68:71], v[156:159]\n" \
"s_barrier\n" \
"v_cvt_pk_bf16_f32 v51, v54, v55\n" \
"v_mfma_f32_16x16x32_bf16 v[64:67], a[192:195], a[128:131], v[196:199]\n" \
"v_exp_f32 v8, v8\n" \
"v_mfma_f32_16x16x32_bf16 v[64:67], a[196:199], a[132:135], v[64:67]\n" \
"v_cmp_eq_u32 vcc, s60, v239\n" \
"v_mfma_f32_16x16x32_bf16 v[64:67], a[200:203], a[136:139], v[64:67]\n" \
"v_exp_f32_e64 v9, v9\n" \
"v_mfma_f32_16x16x32_bf16 v[64:67], a[204:207], a[140:143], v[64:67]\n" \
"s_andn2_b32 s70, s69, vcc_lo\n" \
"s_cbranch_scc1 Lpl2_%=\n" \
"Lpr2_%=:\n" \
"ds_read_b128 a[32:35], v233 offset:6144\n" \
"v_cvt_pk_bf16_f32 v4, v8, v9\n" \
"v_mfma_f32_16x16x32_bf16 v[80:83], a[204:207], a[156:159], v[196:199]\n" \
"s_add_u32 m0, s77, s80\n" \
"v_exp_f32 v10, v10\n" \
"v_mfma_f32_16x16x32_bf16 v[80:83], a[200:203], a[152:155], v[80:83]\n" \
"buffer_load_dwordx4 v234, s[40:43], s76 offen offset:0 sc1 lds\n" \
"v_mfma_f32_16x16x32_bf16 v[80:83], a[196:199], a[148:151], v[80:83]\n" \
"s_sub_u32 s89, s89, 1\n" \
"v_exp_f32 v11, v11\n" \
"v_mfma_f32_16x16x32_bf16 v[80:83], a[192:195], a[144:147], v[80:83]\n" \
"s_cmp_eq_u32 s89, 0\n" \
"s_cselect_b32 s70, s78, 0xc000\n" \
"ds_read_b128 a[36:39], v233 offset:14336\n" \
"v_cvt_pk_bf16_f32 v5, v10, v11\n" \
"v_mfma_f32_16x16x32_bf16 v[96:99], a[192:195], a[160:163], v[196:199]\n" \
"s_add_u32 s71, s76, 17408\n" \
"v_exp_f32 v12, v12\n" \
"v_mfma_f32_16x16x32_bf16 v[96:99], a[196:199], a[164:167], v[96:99]\n" \
"buffer_load_dwordx4 v234, s[40:43], s76 offen offset:1024 sc1 lds\n" \
"v_mfma_f32_16x16x32_bf16 v[96:99], a[200:203], a[168:171], v[96:99]\n" \
"v_exp_f32 v13, v13\n" \
"v_mfma_f32_16x16x32_bf16 v[96:99], a[204:207], a[172:175], v[96:99]\n" \
"ds_read_b128 a[40:43], v233 offset:7168\n" \
"v_cvt_pk_bf16_f32 v6, v12, v13\n" \
"v_mfma_f32_16x16x32_bf16 v[112:115], a[204:207], a[188:191], v[196:199]\n" \
"v_exp_f32 v14, v14\n" \
"v_mfma_f32_16x16x32_bf16 v[112:115], a[200:203], a[184:187], v[112:115]\n" \
"v_xor_b32 v232, s70, v232\n" \
"v_mfma_f32_16x16x32_bf16 v[112:115], a[196:199], a[180:183], v[112:115]\n" \
"v_exp_f32 v15, v15\n" \
"v_mfma_f32_16x16x32_bf16 v[112:115], a[192:195], a[176:179], v[112:115]\n" \
"ds_read_b128 a[204:207], v233 offset:9216\n" \
"v_cvt_pk_bf16_f32 v7, v14, v15\n" \
"v_mfma_f32_16x16x32_bf16 v[116:119], a[208:211], a[176:179], v[196:199]\n" \
"v_exp_f32 v24, v24\n" \
"v_mfma_f32_16x16x32_bf16 v[116:119], a[212:215], a[180:183], v[116:119]\n" \
"buffer_load_dwordx4 v234, s[40:43], s76 offen offset:2048 sc1 lds\n" \
"v_mfma_f32_16x16x32_bf16 v[116:119], a[216:219], a[184:187], v[116:119]\n" \
"v_exp_f32 v25, v25\n" \
"v_mfma_f32_16x16x32_bf16 v[116:119], a[220:223], a[188:191], v[116:119]\n" \
"ds_read_b128 a[200:203], v233 offset:1024\n" \
"v_cvt_pk_bf16_f32 v20, v24, v25\n" \
"v_mfma_f32_16x16x32_bf16 v[100:103], a[220:223], a[172:175], v[196:199]\n" \
"v_exp_f32 v26, v26\n" \
"v_mfma_f32_16x16x32_bf16 v[100:103], a[216:219], a[168:171], v[100:103]\n" \
"buffer_load_dwordx4 v234, s[40:43], s76 offen offset:3072 sc1 lds\n" \
"v_mfma_f32_16x16x32_bf16 v[100:103], a[212:215], a[164:167], v[100:103]\n" \
"v_exp_f32 v27, v27\n" \
"v_mfma_f32_16x16x32_bf16 v[100:103], a[208:211], a[160:163], v[100:103]\n" \
"ds_read_b128 a[196:199], v233 offset:8192\n" \
"v_cvt_pk_bf16_f32 v21, v26, v27\n" \
"v_mfma_f32_16x16x32_bf16 v[84:87], a[208:211], a[144:147], v[196:199]\n" \
"v_exp_f32 v28, v28\n" \
"v_mfma_f32_16x16x32_bf16 v[84:87], a[212:215], a[148:151], v[84:87]\n" \
"v_mfma_f32_16x16x32_bf16 v[84:87], a[216:219], a[152:155], v[84:87]\n" \
"v_exp_f32 v29, v29\n" \
"v_mfma_f32_16x16x32_bf16 v[84:87], a[220:223], a[156:159], v[84:87]\n" \
"ds_read_b128 a[192:195], v233 offset:0\n" \
"v_cvt_pk_bf16_f32 v22, v28, v29\n" \
"v_mfma_f32_16x16x32_bf16 v[68:71], a[220:223], a[140:143], v[196:199]\n" \
"s_add_u32 m0, m0, 0x4000\n" \
"v_exp_f32 v30, v30\n" \
"v_mfma_f32_16x16x32_bf16 v[68:71], a[216:219], a[136:139], v[68:71]\n" \
"buffer_load_dwordx4 v234, s[40:43], s71 offen offset:0 sc1 lds\n" \
"v_mfma_f32_16x16x32_bf16 v[68:71], a[212:215], a[132:135], v[68:71]\n" \
"v_exp_f32 v31, v31\n" \
"v_mfma_f32_16x16x32_bf16 v[68:71], a[208:211], a[128:131], v[68:71]\n" \
"ds_read_b128 a[220:223], v233 offset:11264\n" \
"v_cvt_pk_bf16_f32 v23, v30, v31\n" \
"v_mfma_f32_16x16x32_bf16 v[72:75], a[224:227], a[128:131], v[196:199]\n" \
"v_exp_f32 v40, v40\n" \
"v_mfma_f32_16x16x32_bf16 v[72:75], a[228:231], a[132:135], v[72:75]\n" \
"buffer_load_dwordx4 v234, s[40:43], s71 offen offset:1024 sc1 lds\n" \
"v_mfma_f32_16x16x32_bf16 v[72:75], a[232:235], a[136:139], v[72:75]\n" \
"v_exp_f32 v41, v41\n" \
"v_mfma_f32_16x16x32_bf16 v[72:75], a[236:239], a[140:143], v[72:75]\n" \
"ds_read_b128 a[216:219], v233 offset:3072\n" \
"v_cvt_pk_bf16_f32 v36, v40, v41\n" \
"v_mfma_f32_16x16x32_bf16 v[88:91], a[236:239], a[156:159], v[196:199]\n" \
"v_exp_f32 v42, v42\n" \
"v_mfma_f32_16x16x32_bf16 v[88:91], a[232:235], a[152:155], v[88:91]\n" \
"v_mfma_f32_16x16x32_bf16 v[88:91], a[228:231], a[148:151], v[88:91]\n" \
"v_exp_f32 v43, v43\n" \
"v_mfma_f32_16x16x32_bf16 v[88:91], a[224:227], a[144:147], v[88:91]\n" \
"ds_read_b128 a[212:215], v233 offset:10240\n" \
"v_cvt_pk_bf16_f32 v37, v42, v43\n" \
"v_mfma_f32_16x16x32_bf16 v[104:107], a[224:227], a[160:163], v[196:199]\n" \
"v_exp_f32 v44, v44\n" \
"v_mfma_f32_16x16x32_bf16 v[104:107], a[228:231], a[164:167], v[104:107]\n" \
"buffer_load_dwordx4 v234, s[40:43], s71 offen offset:2048 sc1 lds\n" \
"v_mfma_f32_16x16x32_bf16 v[104:107], a[232:235], a[168:171], v[104:107]\n" \
"v_exp_f32 v45, v45\n" \
"v_mfma_f32_16x16x32_bf16 v[104:107], a[236:239], a[172:175], v[104:107]\n" \
"ds_read_b128 a[208:211], v233 offset:2048\n" \
"v_cvt_pk_bf16_f32 v38, v44, v45\n" \
"v_mfma_f32_16x16x32_bf16 v[120:123], a[236:239], a[188:191], v[196:199]\n" \
"v_exp_f32 v46, v46\n" \
"v_mfma_f32_16x16x32_bf16 v[120:123], a[232:235], a[184:187], v[120:123]\n" \
"buffer_load_dwordx4 v234, s[40:43], s71 offen offset:3072 sc1 lds\n" \
"v_mfma_f32_16x16x32_bf16 v[120:123], a[228:231], a[180:183], v[120:123]\n" \
"v_exp_f32 v47, v47\n" \
"v_mfma_f32_16x16x32_bf16 v[120:123], a[224:227], a[176:179], v[120:123]\n" \
"ds_read_b128 a[236:239], v233 offset:13312\n" \
"v_cvt_pk_bf16_f32 v39, v46, v47\n" \
"v_mfma_f32_16x16x32_bf16 v[124:127], a[240:243], a[176:179], v[196:199]\n" \
"v_exp_f32 v56, v56\n" \
"v_mfma_f32_16x16x32_bf16 v[124:127], a[244:247], a[180:183], v[124:127]\n" \
"v_mfma_f32_16x16x32_bf16 v[124:127], a[248:251], a[184:187], v[124:127]\n" \
"v_exp_f32 v57, v57\n" \
"v_mfma_f32_16x16x32_bf16 v[124:127], a[252:255], a[188:191], v[124:127]\n" \
"ds_read_b128 a[232:235], v233 offset:5120\n" \
"v_cvt_pk_bf16_f32 v52, v56, v57\n" \
"v_mfma_f32_16x16x32_bf16 v[108:111], a[252:255], a[172:175], v[196:199]\n" \
"v_exp_f32 v58, v58\n" \
"v_mfma_f32_16x16x32_bf16 v[108:111], a[248:251], a[168:171], v[108:111]\n" \
"v_mfma_f32_16x16x32_bf16 v[108:111], a[244:247], a[164:167], v[108:111]\n" \
"v_exp_f32 v59, v59\n" \
"v_mfma_f32_16x16x32_bf16 v[108:111], a[240:243], a[160:163], v[108:111]\n" \
"ds_read_b128 a[228:231], v233 offset:12288\n" \
"v_cvt_pk_bf16_f32 v53, v58, v59\n" \
"v_mfma_f32_16x16x32_bf16 v[92:95], a[240:243], a[144:147], v[196:199]\n" \
"v_exp_f32 v60, v60\n" \
"v_mfma_f32_16x16x32_bf16 v[92:95], a[244:247], a[148:151], v[92:95]\n" \
"v_mfma_f32_16x16x32_bf16 v[92:95], a[248:251], a[152:155], v[92:95]\n" \
"v_exp_f32 v61, v61\n" \
"v_mfma_f32_16x16x32_bf16 v[92:95], a[252:255], a[156:159], v[92:95]\n" \
"ds_read_b128 a[224:227], v233 offset:4096\n" \
"v_cvt_pk_bf16_f32 v54, v60, v61\n" \
"v_mfma_f32_16x16x32_bf16 v[76:79], a[252:255], a[140:143], v[196:199]\n" \
"v_exp_f32 v62, v62\n" \
"v_mfma_f32_16x16x32_bf16 v[76:79], a[248:251], a[136:139], v[76:79]\n" \
"v_mfma_f32_16x16x32_bf16 v[76:79], a[244:247], a[132:135], v[76:79]\n" \
"s_waitcnt lgkmcnt(2)\n" \
"v_exp_f32 v63, v63\n" \
"v_mfma_f32_16x16x32_bf16 v[76:79], a[240:243], a[128:131], v[76:79]\n" \
"ds_read_b128 a[44:47], v233 offset:15360\n" \
"v_cvt_pk_bf16_f32 v55, v62, v63\n" \
"v_mfma_f32_16x16x32_bf16 v[128:131], a[192:195], v[0:3], v[128:131]\n" \
"s_add_u32 m0, s74, s79\n" \
"v_exp_f32 v64, v64\n" \
"v_mfma_f32_16x16x32_bf16 v[128:131], a[196:199], v[4:7], v[128:131]\n" \
"buffer_load_dwordx4 v234, s[36:39], s73 offen offset:0 sc1 lds\n" \
"v_mfma_f32_16x16x32_bf16 v[160:163], a[192:195], v[16:19], v[160:163]\n" \
"v_exp_f32 v65, v65\n" \
"v_mfma_f32_16x16x32_bf16 v[160:163], a[196:199], v[20:23], v[160:163]\n" \
"ds_read_b128 a[240:243], v232 offset:8448\n" \
"v_cvt_pk_bf16_f32 v64, v64, v65\n" \
"v_mfma_f32_16x16x32_bf16 a[64:67], a[192:195], v[32:35], a[64:67]\n" \
"s_add_u32 s71, s73, 17408\n" \
"v_exp_f32 v66, v66\n" \
"v_mfma_f32_16x16x32_bf16 a[64:67], a[196:199], v[36:39], a[64:67]\n" \
"buffer_load_dwordx4 v234, s[36:39], s73 offen offset:1024 sc1 lds\n" \
"v_mfma_f32_16x16x32_bf16 a[96:99], a[192:195], v[48:51], a[96:99]\n" \
"v_exp_f32 v67, v67\n" \
"v_mfma_f32_16x16x32_bf16 a[96:99], a[196:199], v[52:55], a[96:99]\n" \
"ds_read_b128 a[192:195], v232 offset:0\n" \
"v_cvt_pk_bf16_f32 v65, v66, v67\n" \
"v_mfma_f32_16x16x32_bf16 a[100:103], a[200:203], v[48:51], a[100:103]\n" \
"v_exp_f32 v68, v68\n" \
"v_mfma_f32_16x16x32_bf16 a[100:103], a[204:207], v[52:55], a[100:103]\n" \
"v_xor_b32 v233, 0x4000, v233\n" \
"v_mfma_f32_16x16x32_bf16 a[68:71], a[200:203], v[32:35], a[68:71]\n" \
"v_exp_f32 v69, v69\n" \
"v_mfma_f32_16x16x32_bf16 a[68:71], a[204:207], v[36:39], a[68:71]\n" \
"ds_read_b128 a[196:199], v232 offset:2048\n" \
"v_cvt_pk_bf16_f32 v66, v68, v69\n" \
"v_smfmac_f32_16x16x64_bf16 a[48:51], v[244:247], v[0:7], v243\n" \
"buffer_load_dwordx4 v234, s[36:39], s73 offen offset:2048 sc1 lds\n" \
"v_mfma_f32_16x16x32_bf16 v[164:167], a[200:203], v[16:19], v[164:167]\n" \
"v_exp_f32 v70, v70\n" \
"v_mfma_f32_16x16x32_bf16 v[164:167], a[204:207], v[20:23], v[164:167]\n" \
"v_mfma_f32_16x16x32_bf16 v[132:135], a[200:203], v[0:3], v[132:135]\n" \
"v_exp_f32 v71, v71\n" \
"v_mfma_f32_16x16x32_bf16 v[132:135], a[204:207], v[4:7], v[132:135]\n" \
"ds_read_b128 a[200:203], v232 offset:4096\n" \
"v_cvt_pk_bf16_f32 v67, v70, v71\n" \
"v_mfma_f32_16x16x32_bf16 v[136:139], a[208:211], v[0:3], v[136:139]\n" \
"v_exp_f32 v80, v80\n" \
"v_mfma_f32_16x16x32_bf16 v[136:139], a[212:215], v[4:7], v[136:139]\n" \
"buffer_load_dwordx4 v234, s[36:39], s73 offen offset:3072 sc1 lds\n" \
"v_mfma_f32_16x16x32_bf16 v[168:171], a[208:211], v[16:19], v[168:171]\n" \
"v_exp_f32 v81, v81\n" \
"v_mfma_f32_16x16x32_bf16 v[168:171], a[212:215], v[20:23], v[168:171]\n" \
"ds_read_b128 a[204:207], v232 offset:6144\n" \
"v_cvt_pk_bf16_f32 v80, v80, v81\n" \
"v_mfma_f32_16x16x32_bf16 a[72:75], a[208:211], v[32:35], a[72:75]\n" \
"v_exp_f32 v82, v82\n" \
"v_mfma_f32_16x16x32_bf16 a[72:75], a[212:215], v[36:39], a[72:75]\n" \
"v_mfma_f32_16x16x32_bf16 a[104:107], a[208:211], v[48:51], a[104:107]\n" \
"v_exp_f32 v83, v83\n" \
"v_mfma_f32_16x16x32_bf16 a[104:107], a[212:215], v[52:55], a[104:107]\n" \
"ds_read_b128 a[208:211], v232 offset:256\n" \
"v_cvt_pk_bf16_f32 v81, v82, v83\n" \
"v_mfma_f32_16x16x32_bf16 a[108:111], a[216:219], v[48:51], a[108:111]\n" \
"s_add_u32 m0, m0, 0x4000\n" \
"v_exp_f32 v84, v84\n" \
"v_mfma_f32_16x16x32_bf16 a[108:111], a[220:223], v[52:55], a[108:111]\n" \
"buffer_load_dwordx4 v234, s[36:39], s71 offen offset:0 sc1 lds\n" \
"v_mfma_f32_16x16x32_bf16 a[76:79], a[216:219], v[32:35], a[76:79]\n" \
"v_exp_f32 v85, v85\n" \
"v_mfma_f32_16x16x32_bf16 a[76:79], a[220:223], v[36:39], a[76:79]\n" \
"ds_read_b128 a[212:215], v232 offset:2304\n" \
"v_cvt_pk_bf16_f32 v82, v84, v85\n" \
"v_smfmac_f32_16x16x64_bf16 a[52:55], v[244:247], v[16:23], v243\n" \
"v_mfma_f32_16x16x32_bf16 v[172:175], a[216:219], v[16:19], v[172:175]\n" \
"v_exp_f32 v86, v86\n" \
"v_mfma_f32_16x16x32_bf16 v[172:175], a[220:223], v[20:23], v[172:175]\n" \
"buffer_load_dwordx4 v234, s[36:39], s71 offen offset:1024 sc1 lds\n" \
"v_mfma_f32_16x16x32_bf16 v[140:143], a[216:219], v[0:3], v[140:143]\n" \
"s_waitcnt lgkmcnt(7)\n" \
"v_exp_f32 v87, v87\n" \
"v_mfma_f32_16x16x32_bf16 v[140:143], a[220:223], v[4:7], v[140:143]\n" \
"ds_read_b128 a[216:219], v232 offset:4352\n" \
"v_cvt_pk_bf16_f32 v83, v86, v87\n" \
"v_mfma_f32_16x16x32_bf16 v[144:147], a[224:227], v[0:3], v[144:147]\n" \
"v_exp_f32 v96, v96\n" \
"v_mfma_f32_16x16x32_bf16 v[144:147], a[228:231], v[4:7], v[144:147]\n" \
"buffer_load_dwordx4 v234, s[36:39], s71 offen offset:2048 sc1 lds\n" \
"v_mfma_f32_16x16x32_bf16 v[176:179], a[224:227], v[16:19], v[176:179]\n" \
"v_exp_f32 v97, v97\n" \
"v_mfma_f32_16x16x32_bf16 v[176:179], a[228:231], v[20:23], v[176:179]\n" \
"ds_read_b128 a[220:223], v232 offset:6400\n" \
"v_cvt_pk_bf16_f32 v96, v96, v97\n" \
"v_mfma_f32_16x16x32_bf16 a[80:83], a[224:227], v[32:35], a[80:83]\n" \
"v_exp_f32 v98, v98\n" \
"v_mfma_f32_16x16x32_bf16 a[80:83], a[228:231], v[36:39], a[80:83]\n" \
"v_mfma_f32_16x16x32_bf16 a[112:115], a[224:227], v[48:51], a[112:115]\n" \
"v_exp_f32 v99, v99\n" \
"v_mfma_f32_16x16x32_bf16 a[112:115], a[228:231], v[52:55], a[112:115]\n" \
"ds_read_b128 a[224:227], v232 offset:8192\n" \
"v_cvt_pk_bf16_f32 v97, v98, v99\n" \
"v_mfma_f32_16x16x32_bf16 a[116:119], a[232:235], v[48:51], a[116:119]\n" \
"v_exp_f32 v100, v100\n" \
"v_mfma_f32_16x16x32_bf16 a[116:119], a[236:239], v[52:55], a[116:119]\n" \
"buffer_load_dwordx4 v234, s[36:39], s71 offen offset:3072 sc1 lds\n" \
"v_mfma_f32_16x16x32_bf16 a[84:87], a[232:235], v[32:35], a[84:87]\n" \
"v_exp_f32 v101, v101\n" \
"v_mfma_f32_16x16x32_bf16 a[84:87], a[236:239], v[36:39], a[84:87]\n" \
"ds_read_b128 a[228:231], v232 offset:10240\n" \
"v_cvt_pk_bf16_f32 v98, v100, v101\n" \
"v_smfmac_f32_16x16x64_bf16 v[224:227], v[244:247], v[32:39], v243\n" \
"v_mfma_f32_16x16x32_bf16 v[180:183], a[232:235], v[16:19], v[180:183]\n" \
"v_exp_f32 v102, v102\n" \
"v_mfma_f32_16x16x32_bf16 v[180:183], a[236:239], v[20:23], v[180:183]\n" \
"buffer_load_dword v239, v242, s[56:59], s95 offen sc1\n" \
"v_mfma_f32_16x16x32_bf16 v[148:151], a[232:235], v[0:3], v[148:151]\n" \
"s_mov_b32 s69, s92\n" \
"v_exp_f32 v103, v103\n" \
"v_mfma_f32_16x16x32_bf16 v[148:151], a[236:239], v[4:7], v[148:151]\n" \
"s_mov_b32 s76, s73\n" \
"ds_read_b128 a[232:235], v232 offset:12288\n" \
"v_cvt_pk_bf16_f32 v99, v102, v103\n" \
"v_mfma_f32_16x16x32_bf16 v[152:155], a[32:35], v[0:3], v[152:155]\n" \
"s_add_u32 s73, s73, 34816\n" \
"v_exp_f32 v112, v112\n" \
"v_mfma_f32_16x16x32_bf16 v[152:155], a[36:39], v[4:7], v[152:155]\n" \
"s_cmp_eq_u32 s73, s83\n" \
"s_cselect_b32 s70, 34816, 0\n" \
"s_add_u32 s73, s73, s70\n" \
"v_mfma_f32_16x16x32_bf16 v[184:187], a[32:35], v[16:19], v[184:187]\n" \
"s_xor_b32 s74, s74, 0x8000\n" \
"v_exp_f32 v113, v113\n" \
"v_mfma_f32_16x16x32_bf16 v[184:187], a[36:39], v[20:23], v[184:187]\n" \
"s_xor_b32 s77, s77, 0x8000\n" \
"ds_read_b128 a[236:239], v232 offset:14336\n" \
"v_cvt_pk_bf16_f32 v112, v112, v113\n" \
"v_mfma_f32_16x16x32_bf16 a[88:91], a[32:35], v[32:35], a[88:91]\n" \
"v_exp_f32 v114, v114\n" \
"v_mfma_f32_16x16x32_bf16 a[88:91], a[36:39], v[36:39], a[88:91]\n" \
"v_mfma_f32_16x16x32_bf16 a[120:123], a[32:35], v[48:51], a[120:123]\n" \
"v_exp_f32 v115, v115\n" \
"v_mfma_f32_16x16x32_bf16 a[120:123], a[36:39], v[52:55], a[120:123]\n" \
"ds_read_b128 a[244:247], v232 offset:10496\n" \
"v_cvt_pk_bf16_f32 v113, v114, v115\n" \
"v_mfma_f32_16x16x32_bf16 a[124:127], a[40:43], v[48:51], a[124:127]\n" \
"v_exp_f32 v116, v116\n" \
"v_mfma_f32_16x16x32_bf16 a[124:127], a[44:47], v[52:55], a[124:127]\n" \
"v_mfma_f32_16x16x32_bf16 a[92:95], a[40:43], v[32:35], a[92:95]\n" \
"v_exp_f32 v117, v117\n" \
"v_mfma_f32_16x16x32_bf16 a[92:95], a[44:47], v[36:39], a[92:95]\n" \
"ds_read_b128 a[248:251], v232 offset:12544\n" \
"v_cvt_pk_bf16_f32 v114, v116, v117\n" \
"v_smfmac_f32_16x16x64_bf16 v[228:231], v[244:247], v[48:55], v243\n" \
"v_mfma_f32_16x16x32_bf16 v[188:191], a[40:43], v[16:19], v[188:191]\n" \
"v_exp_f32 v118, v118\n" \
"v_mfma_f32_16x16x32_bf16 v[188:191], a[44:47], v[20:23], v[188:191]\n" \
"s_cmp_lg_u32 s89, 0\n" \
"v_exp_f32 v119, v119\n" \
"v_mfma_f32_16x16x32_bf16 v[156:159], a[40:43], v[0:3], v[156:159]\n" \
"s_waitcnt lgkmcnt(2)\n" \
"ds_read_b128 a[252:255], v232 offset:14592\n" \
"v_cvt_pk_bf16_f32 v115, v118, v119\n" \
"v_mfma_f32_16x16x32_bf16 v[156:159], a[44:47], v[4:7], v[156:159]\n" \
"s_cbranch_scc1 Lloop_%=\n" \
"s_cmp_eq_u32 s63, 7\n" \
"s_cbranch_scc0 Lloop2_%=\n" \
"s_branch Lg_%=\n" \
"s_nop 0\n" \
"s_nop 0\n" \
"Lep_%=:\n" \
"Lpg6052_%=:\n" \
"s_cbranch_execz Lpg7367_%=\n" \
"s_nop 4\n" \
"v_accvgpr_read_b32 v92, a48\n" \
"v_accvgpr_read_b32 v93, a49\n" \
"v_accvgpr_read_b32 v94, a52\n" \
"v_accvgpr_read_b32 v95, a53\n" \
"v_accvgpr_write_b32 a48, 0\n" \
"v_accvgpr_write_b32 a49, 0\n" \
"v_accvgpr_write_b32 a52, 0\n" \
"v_accvgpr_write_b32 a53, 0\n" \
"v_add_f32 v86, v92, v93\n" \
"v_add_f32 v87, v94, v95\n" \
"v_rcp_f32 v84, v86\n" \
"v_rcp_f32 v85, v87\n" \
"v_add_u32 v240, 0x1000, v236\n" \
"v_add_f32 v80, v86, v87\n" \
"v_mul_f32 v80, 0x43800000, v80\n" \
"v_add_f32 v86, v84, v85\n" \
"v_fmac_f32 v80, 0x5f800000, v86\n" \
"v_mov_b32 v81, 0\n" \
"v_mov_b32 v82, 0\n" \
"v_mov_b32 v83, 0\n" \
"v_mov_b32 v88, 0\n" \
"v_mov_b32 v89, 0\n" \
"v_mov_b32 v90, 0\n" \
"v_mov_b32 v91, 0\n" \
"v_pk_mul_f32 v[128:129], v[128:129], v[84:85] op_sel:[0,0] op_sel_hi:[1,0]\n" \
"v_pk_mul_f32 v[130:131], v[130:131], v[84:85] op_sel:[0,0] op_sel_hi:[1,0]\n" \
"v_pk_mul_f32 v[132:133], v[132:133], v[84:85] op_sel:[0,0] op_sel_hi:[1,0]\n" \
"v_pk_mul_f32 v[134:135], v[134:135], v[84:85] op_sel:[0,0] op_sel_hi:[1,0]\n" \
"v_cvt_pk_bf16_f32 v64, v128, v129\n" \
"v_cvt_pk_bf16_f32 v65, v130, v131\n" \
"v_cvt_pk_bf16_f32 v66, v132, v133\n" \
"v_cvt_pk_bf16_f32 v67, v134, v135\n" \
"buffer_store_dwordx4 v[64:67], v236, s[56:59], s87 offen offset:0 sc1\n" \
"v_pk_mul_f32 v[136:137], v[136:137], v[84:85] op_sel:[0,0] op_sel_hi:[1,0]\n" \
"v_pk_mul_f32 v[138:139], v[138:139], v[84:85] op_sel:[0,0] op_sel_hi:[1,0]\n" \
"v_pk_mul_f32 v[140:141], v[140:141], v[84:85] op_sel:[0,0] op_sel_hi:[1,0]\n" \
"v_pk_mul_f32 v[142:143], v[142:143], v[84:85] op_sel:[0,0] op_sel_hi:[1,0]\n" \
"v_cvt_pk_bf16_f32 v68, v136, v137\n" \
"v_cvt_pk_bf16_f32 v69, v138, v139\n" \
"v_cvt_pk_bf16_f32 v70, v140, v141\n" \
"v_cvt_pk_bf16_f32 v71, v142, v143\n" \
"buffer_store_dwordx4 v[68:71], v236, s[56:59], s87 offen offset:2048 sc1\n" \
"v_pk_mul_f32 v[144:145], v[144:145], v[84:85] op_sel:[0,0] op_sel_hi:[1,0]\n" \
"v_pk_mul_f32 v[146:147], v[146:147], v[84:85] op_sel:[0,0] op_sel_hi:[1,0]\n" \
"v_pk_mul_f32 v[148:149], v[148:149], v[84:85] op_sel:[0,0] op_sel_hi:[1,0]\n" \
"v_pk_mul_f32 v[150:151], v[150:151], v[84:85] op_sel:[0,0] op_sel_hi:[1,0]\n" \
"v_cvt_pk_bf16_f32 v72, v144, v145\n" \
"v_cvt_pk_bf16_f32 v73, v146, v147\n" \
"v_cvt_pk_bf16_f32 v74, v148, v149\n" \
"v_cvt_pk_bf16_f32 v75, v150, v151\n" \
"buffer_store_dwordx4 v[72:75], v240, s[56:59], s87 offen offset:0 sc1\n" \
"v_pk_mul_f32 v[152:153], v[152:153], v[84:85] op_sel:[0,0] op_sel_hi:[1,0]\n" \
"v_pk_mul_f32 v[154:155], v[154:155], v[84:85] op_sel:[0,0] op_sel_hi:[1,0]\n" \
"v_pk_mul_f32 v[156:157], v[156:157], v[84:85] op_sel:[0,0] op_sel_hi:[1,0]\n" \
"v_pk_mul_f32 v[158:159], v[158:159], v[84:85] op_sel:[0,0] op_sel_hi:[1,0]\n" \
"v_cvt_pk_bf16_f32 v76, v152, v153\n" \
"v_cvt_pk_bf16_f32 v77, v154, v155\n" \
"v_cvt_pk_bf16_f32 v78, v156, v157\n" \
"v_cvt_pk_bf16_f32 v79, v158, v159\n" \
"buffer_store_dwordx4 v[76:79], v240, s[56:59], s87 offen offset:2048 sc1\n" \
"v_smfmac_f32_16x16x64_bf16 v[80:83], v[244:247], v[64:71], v243\n" \
"v_smfmac_f32_16x16x64_bf16 v[80:83], v[244:247], v[72:79], v243\n" \
"v_mfma_f32_32x32x16_bf16 v[128:143], v[88:91], v[88:91], 0\n" \
"v_mfma_f32_32x32x16_bf16 v[144:159], v[88:91], v[88:91], 0\n" \
"s_mov_b64 exec, 0\n" \
"s_cmp_eq_u32 s61, 0\n" \
"s_cbranch_scc1 Lst1x0_%=\n" \
"s_cmp_eq_u32 s61, 1\n" \
"s_cbranch_scc1 Lst1x1_%=\n" \
"s_cmp_eq_u32 s61, 2\n" \
"s_cbranch_scc1 Lst1x2_%=\n" \
"s_branch Lst1x3_%=\n" \
"Lcb1_%=:\n" \
"s_mov_b64 exec, -1\n" \
"s_add_u32 s72, s87, 532480\n" \
"v_pk_mul_f32 v[160:161], v[160:161], v[84:85] op_sel:[0,1] op_sel_hi:[1,1]\n" \
"v_pk_mul_f32 v[162:163], v[162:163], v[84:85] op_sel:[0,1] op_sel_hi:[1,1]\n" \
"v_pk_mul_f32 v[164:165], v[164:165], v[84:85] op_sel:[0,1] op_sel_hi:[1,1]\n" \
"v_pk_mul_f32 v[166:167], v[166:167], v[84:85] op_sel:[0,1] op_sel_hi:[1,1]\n" \
"v_cvt_pk_bf16_f32 v64, v160, v161\n" \
"v_cvt_pk_bf16_f32 v65, v162, v163\n" \
"v_cvt_pk_bf16_f32 v66, v164, v165\n" \
"v_cvt_pk_bf16_f32 v67, v166, v167\n" \
"buffer_store_dwordx4 v[64:67], v236, s[56:59], s72 offen offset:0 sc1\n" \
"v_pk_mul_f32 v[168:169], v[168:169], v[84:85] op_sel:[0,1] op_sel_hi:[1,1]\n" \
"v_pk_mul_f32 v[170:171], v[170:171], v[84:85] op_sel:[0,1] op_sel_hi:[1,1]\n" \
"v_pk_mul_f32 v[172:173], v[172:173], v[84:85] op_sel:[0,1] op_sel_hi:[1,1]\n" \
"v_pk_mul_f32 v[174:175], v[174:175], v[84:85] op_sel:[0,1] op_sel_hi:[1,1]\n" \
"v_cvt_pk_bf16_f32 v68, v168, v169\n" \
"v_cvt_pk_bf16_f32 v69, v170, v171\n" \
"v_cvt_pk_bf16_f32 v70, v172, v173\n" \
"v_cvt_pk_bf16_f32 v71, v174, v175\n" \
"buffer_store_dwordx4 v[68:71], v236, s[56:59], s72 offen offset:2048 sc1\n" \
"v_pk_mul_f32 v[176:177], v[176:177], v[84:85] op_sel:[0,1] op_sel_hi:[1,1]\n" \
"v_pk_mul_f32 v[178:179], v[178:179], v[84:85] op_sel:[0,1] op_sel_hi:[1,1]\n" \
"v_pk_mul_f32 v[180:181], v[180:181], v[84:85] op_sel:[0,1] op_sel_hi:[1,1]\n" \
"v_pk_mul_f32 v[182:183], v[182:183], v[84:85] op_sel:[0,1] op_sel_hi:[1,1]\n" \
"v_cvt_pk_bf16_f32 v72, v176, v177\n" \
"v_cvt_pk_bf16_f32 v73, v178, v179\n" \
"v_cvt_pk_bf16_f32 v74, v180, v181\n" \
"v_cvt_pk_bf16_f32 v75, v182, v183\n" \
"buffer_store_dwordx4 v[72:75], v240, s[56:59], s72 offen offset:0 sc1\n" \
"v_pk_mul_f32 v[184:185], v[184:185], v[84:85] op_sel:[0,1] op_sel_hi:[1,1]\n" \
"v_pk_mul_f32 v[186:187], v[186:187], v[84:85] op_sel:[0,1] op_sel_hi:[1,1]\n" \
"v_pk_mul_f32 v[188:189], v[188:189], v[84:85] op_sel:[0,1] op_sel_hi:[1,1]\n" \
"v_pk_mul_f32 v[190:191], v[190:191], v[84:85] op_sel:[0,1] op_sel_hi:[1,1]\n" \
"v_cvt_pk_bf16_f32 v76, v184, v185\n" \
"v_cvt_pk_bf16_f32 v77, v186, v187\n" \
"v_cvt_pk_bf16_f32 v78, v188, v189\n" \
"v_cvt_pk_bf16_f32 v79, v190, v191\n" \
"buffer_store_dwordx4 v[76:79], v240, s[56:59], s72 offen offset:2048 sc1\n" \
"v_smfmac_f32_16x16x64_bf16 v[80:83], v[244:247], v[64:71], v243\n" \
"v_smfmac_f32_16x16x64_bf16 v[80:83], v[244:247], v[72:79], v243\n" \
"v_mfma_f32_32x32x16_bf16 v[160:175], v[88:91], v[88:91], 0\n" \
"v_mfma_f32_32x32x16_bf16 v[176:191], v[88:91], v[88:91], 0\n" \
"s_nop 5\n" \
"v_add_f32 v80, v80, v81\n" \
"s_movk_i32 s64, 0x207\n" \
"v_cmp_class_f32 vcc, v80, s64\n" \
"s_nop 1\n" \
"s_cmp_lg_u64 vcc, 0\n" \
"s_addc_u32 s90, s90, 0\n" \
"s_waitcnt vmcnt(8)\n" \
"s_cmp_eq_u32 s63, 7\n" \
"s_cbranch_scc0 Lepr_%=\n" \
"s_branch Lepg_%=\n" \
".p2align 12\n" \
"Lloop2_%=:\n" \
"v_mfma_f32_16x16x32_bf16 v[0:3], a[192:195], a[0:3], v[196:199]\n" \
"Lpg6183_%=:\n" \
"s_cbranch_execz Lcb0_%=\n" \
"v_exp_f32 v72, v72\n" \
"v_mfma_f32_16x16x32_bf16 v[0:3], a[196:199], a[4:7], v[0:3]\n" \
"ds_read_b128 a[44:47], v233 offset:15360\n" \
"v_mfma_f32_16x16x32_bf16 v[0:3], a[200:203], a[8:11], v[0:3]\n" \
"v_exp_f32 v73, v73\n" \
"v_mfma_f32_16x16x32_bf16 v[0:3], a[204:207], a[12:15], v[0:3]\n" \
"ds_read_b128 a[32:35], v233 offset:6144\n" \
"v_cvt_pk_bf16_f32 v68, v72, v73\n" \
"v_mfma_f32_16x16x32_bf16 v[16:19], a[204:207], a[28:31], v[196:199]\n" \
"v_exp_f32 v74, v74\n" \
"v_mfma_f32_16x16x32_bf16 v[16:19], a[200:203], a[24:27], v[16:19]\n" \
"v_xor_b32 v232, 0x8000, v232\n" \
"v_mfma_f32_16x16x32_bf16 v[16:19], a[196:199], a[20:23], v[16:19]\n" \
"v_exp_f32 v75, v75\n" \
"v_mfma_f32_16x16x32_bf16 v[16:19], a[192:195], a[16:19], v[16:19]\n" \
"ds_read_b128 a[36:39], v233 offset:14336\n" \
"v_cvt_pk_bf16_f32 v69, v74, v75\n" \
"v_mfma_f32_16x16x32_bf16 v[32:35], a[192:195], a[160:163], v[196:199]\n" \
"v_exp_f32 v76, v76\n" \
"v_mfma_f32_16x16x32_bf16 v[32:35], a[196:199], a[164:167], v[32:35]\n" \
"v_mfma_f32_16x16x32_bf16 v[32:35], a[200:203], a[168:171], v[32:35]\n" \
"v_exp_f32 v77, v77\n" \
"v_mfma_f32_16x16x32_bf16 v[32:35], a[204:207], a[172:175], v[32:35]\n" \
"ds_read_b128 a[40:43], v233 offset:7168\n" \
"v_cvt_pk_bf16_f32 v70, v76, v77\n" \
"v_mfma_f32_16x16x32_bf16 v[48:51], a[204:207], a[188:191], v[196:199]\n" \
"v_exp_f32 v78, v78\n" \
"v_mfma_f32_16x16x32_bf16 v[48:51], a[200:203], a[184:187], v[48:51]\n" \
"v_mfma_f32_16x16x32_bf16 v[48:51], a[196:199], a[180:183], v[48:51]\n" \
"v_exp_f32 v79, v79\n" \
"v_mfma_f32_16x16x32_bf16 v[48:51], a[192:195], a[176:179], v[48:51]\n" \
"ds_read_b128 a[204:207], v233 offset:9216\n" \
"v_cvt_pk_bf16_f32 v71, v78, v79\n" \
"v_mfma_f32_16x16x32_bf16 v[52:55], a[208:211], a[176:179], v[196:199]\n" \
"v_exp_f32 v88, v88\n" \
"v_mfma_f32_16x16x32_bf16 v[52:55], a[212:215], a[180:183], v[52:55]\n" \
"v_mfma_f32_16x16x32_bf16 v[52:55], a[216:219], a[184:187], v[52:55]\n" \
"v_exp_f32 v89, v89\n" \
"v_mfma_f32_16x16x32_bf16 v[52:55], a[220:223], a[188:191], v[52:55]\n" \
"ds_read_b128 a[200:203], v233 offset:1024\n" \
"v_cvt_pk_bf16_f32 v84, v88, v89\n" \
"v_mfma_f32_16x16x32_bf16 v[36:39], a[220:223], a[172:175], v[196:199]\n" \
"v_exp_f32 v90, v90\n" \
"v_mfma_f32_16x16x32_bf16 v[36:39], a[216:219], a[168:171], v[36:39]\n" \
"v_mfma_f32_16x16x32_bf16 v[36:39], a[212:215], a[164:167], v[36:39]\n" \
"v_exp_f32 v91, v91\n" \
"v_mfma_f32_16x16x32_bf16 v[36:39], a[208:211], a[160:163], v[36:39]\n" \
"ds_read_b128 a[196:199], v233 offset:8192\n" \
"v_cvt_pk_bf16_f32 v85, v90, v91\n" \
"v_mfma_f32_16x16x32_bf16 v[20:23], a[208:211], a[16:19], v[196:199]\n" \
"v_exp_f32 v92, v92\n" \
"v_mfma_f32_16x16x32_bf16 v[20:23], a[212:215], a[20:23], v[20:23]\n" \
"v_mfma_f32_16x16x32_bf16 v[20:23], a[216:219], a[24:27], v[20:23]\n" \
"v_exp_f32 v93, v93\n" \
"v_mfma_f32_16x16x32_bf16 v[20:23], a[220:223], a[28:31], v[20:23]\n" \
"ds_read_b128 a[192:195], v233 offset:0\n" \
"v_cvt_pk_bf16_f32 v86, v92, v93\n" \
"v_mfma_f32_16x16x32_bf16 v[4:7], a[220:223], a[12:15], v[196:199]\n" \
"v_exp_f32 v94, v94\n" \
"v_mfma_f32_16x16x32_bf16 v[4:7], a[216:219], a[8:11], v[4:7]\n" \
"v_mfma_f32_16x16x32_bf16 v[4:7], a[212:215], a[4:7], v[4:7]\n" \
"s_waitcnt lgkmcnt(8)\n" \
"v_exp_f32 v95, v95\n" \
"v_mfma_f32_16x16x32_bf16 v[4:7], a[208:211], a[0:3], v[4:7]\n" \
"ds_read_b128 a[220:223], v233 offset:11264\n" \
"v_cvt_pk_bf16_f32 v87, v94, v95\n" \
"v_mfma_f32_16x16x32_bf16 v[8:11], a[224:227], a[0:3], v[196:199]\n" \
"v_exp_f32 v104, v104\n" \
"v_mfma_f32_16x16x32_bf16 v[8:11], a[228:231], a[4:7], v[8:11]\n" \
"v_mfma_f32_16x16x32_bf16 v[8:11], a[232:235], a[8:11], v[8:11]\n" \
"v_exp_f32 v105, v105\n" \
"v_mfma_f32_16x16x32_bf16 v[8:11], a[236:239], a[12:15], v[8:11]\n" \
"ds_read_b128 a[216:219], v233 offset:3072\n" \
"v_cvt_pk_bf16_f32 v100, v104, v105\n" \
"v_mfma_f32_16x16x32_bf16 v[24:27], a[236:239], a[28:31], v[196:199]\n" \
"v_exp_f32 v106, v106\n" \
"v_mfma_f32_16x16x32_bf16 v[24:27], a[232:235], a[24:27], v[24:27]\n" \
"v_mfma_f32_16x16x32_bf16 v[24:27], a[228:231], a[20:23], v[24:27]\n" \
"v_exp_f32 v107, v107\n" \
"v_mfma_f32_16x16x32_bf16 v[24:27], a[224:227], a[16:19], v[24:27]\n" \
"ds_read_b128 a[212:215], v233 offset:10240\n" \
"v_cvt_pk_bf16_f32 v101, v106, v107\n" \
"v_mfma_f32_16x16x32_bf16 v[40:43], a[224:227], a[160:163], v[196:199]\n" \
"v_exp_f32 v108, v108\n" \
"v_mfma_f32_16x16x32_bf16 v[40:43], a[228:231], a[164:167], v[40:43]\n" \
"v_mfma_f32_16x16x32_bf16 v[40:43], a[232:235], a[168:171], v[40:43]\n" \
"v_exp_f32 v109, v109\n" \
"v_mfma_f32_16x16x32_bf16 v[40:43], a[236:239], a[172:175], v[40:43]\n" \
"ds_read_b128 a[208:211], v233 offset:2048\n" \
"v_cvt_pk_bf16_f32 v102, v108, v109\n" \
"v_mfma_f32_16x16x32_bf16 v[56:59], a[236:239], a[188:191], v[196:199]\n" \
"v_exp_f32 v110, v110\n" \
"v_mfma_f32_16x16x32_bf16 v[56:59], a[232:235], a[184:187], v[56:59]\n" \
"v_mfma_f32_16x16x32_bf16 v[56:59], a[228:231], a[180:183], v[56:59]\n" \
"v_exp_f32 v111, v111\n" \
"v_mfma_f32_16x16x32_bf16 v[56:59], a[224:227], a[176:179], v[56:59]\n" \
"ds_read_b128 a[236:239], v233 offset:13312\n" \
"v_cvt_pk_bf16_f32 v103, v110, v111\n" \
"v_mfma_f32_16x16x32_bf16 v[60:63], a[240:243], a[176:179], v[196:199]\n" \
"v_exp_f32 v120, v120\n" \
"v_mfma_f32_16x16x32_bf16 v[60:63], a[244:247], a[180:183], v[60:63]\n" \
"ds_read_b128 a[232:235], v233 offset:5120\n" \
"v_mfma_f32_16x16x32_bf16 v[60:63], a[248:251], a[184:187], v[60:63]\n" \
"v_exp_f32 v121, v121\n" \
"v_mfma_f32_16x16x32_bf16 v[60:63], a[252:255], a[188:191], v[60:63]\n" \
"ds_read_b128 a[228:231], v233 offset:12288\n" \
"v_cvt_pk_bf16_f32 v116, v120, v121\n" \
"v_mfma_f32_16x16x32_bf16 v[44:47], a[252:255], a[172:175], v[196:199]\n" \
"v_exp_f32 v122, v122\n" \
"v_mfma_f32_16x16x32_bf16 v[44:47], a[248:251], a[168:171], v[44:47]\n" \
"ds_read_b128 a[224:227], v233 offset:4096\n" \
"v_mfma_f32_16x16x32_bf16 v[44:47], a[244:247], a[164:167], v[44:47]\n" \
"v_exp_f32 v123, v123\n" \
"v_mfma_f32_16x16x32_bf16 v[44:47], a[240:243], a[160:163], v[44:47]\n" \
"v_cvt_pk_bf16_f32 v117, v122, v123\n" \
"v_mfma_f32_16x16x32_bf16 v[28:31], a[240:243], a[16:19], v[196:199]\n" \
"v_exp_f32 v124, v124\n" \
"v_mfma_f32_16x16x32_bf16 v[28:31], a[244:247], a[20:23], v[28:31]\n" \
"v_mfma_f32_16x16x32_bf16 v[28:31], a[248:251], a[24:27], v[28:31]\n" \
"v_exp_f32 v125, v125\n" \
"v_mfma_f32_16x16x32_bf16 v[28:31], a[252:255], a[28:31], v[28:31]\n" \
"v_cvt_pk_bf16_f32 v118, v124, v125\n" \
"v_mfma_f32_16x16x32_bf16 v[12:15], a[252:255], a[12:15], v[196:199]\n" \
"v_exp_f32 v126, v126\n" \
"v_mfma_f32_16x16x32_bf16 v[12:15], a[248:251], a[8:11], v[12:15]\n" \
"v_mfma_f32_16x16x32_bf16 v[12:15], a[244:247], a[4:7], v[12:15]\n" \
"s_waitcnt vmcnt(0) lgkmcnt(0)\n" \
"v_exp_f32 v127, v127\n" \
"v_mfma_f32_16x16x32_bf16 v[12:15], a[240:243], a[0:3], v[12:15]\n" \
"s_barrier\n" \
"v_cvt_pk_bf16_f32 v119, v126, v127\n" \
"v_mfma_f32_16x16x32_bf16 v[128:131], a[192:195], v[64:67], v[128:131]\n" \
"v_exp_f32 v0, v0\n" \
"v_mfma_f32_16x16x32_bf16 v[128:131], a[196:199], v[68:71], v[128:131]\n" \
"v_cmp_eq_u32 vcc, s60, v239\n" \
"v_mfma_f32_16x16x32_bf16 v[160:163], a[192:195], v[80:83], v[160:163]\n" \
"v_exp_f32 v1, v1\n" \
"v_mfma_f32_16x16x32_bf16 v[160:163], a[196:199], v[84:87], v[160:163]\n" \
"s_andn2_b32 s70, s69, vcc_lo\n" \
"s_cbranch_scc1 Lpl3_%=\n" \
"Lpr3_%=:\n" \
"ds_read_b128 a[240:243], v232 offset:8448\n" \
"v_cvt_pk_bf16_f32 v0, v0, v1\n" \
"v_mfma_f32_16x16x32_bf16 a[64:67], a[192:195], v[96:99], a[64:67]\n" \
"s_add_u32 m0, s74, s79\n" \
"v_exp_f32 v2, v2\n" \
"v_mfma_f32_16x16x32_bf16 a[64:67], a[196:199], v[100:103], a[64:67]\n" \
"buffer_load_dwordx4 v234, s[36:39], s73 offen offset:0 sc1 lds\n" \
"v_mfma_f32_16x16x32_bf16 a[96:99], a[192:195], v[112:115], a[96:99]\n" \
"v_exp_f32 v3, v3\n" \
"v_mfma_f32_16x16x32_bf16 a[96:99], a[196:199], v[116:119], a[96:99]\n" \
"ds_read_b128 a[192:195], v232 offset:0\n" \
"v_cvt_pk_bf16_f32 v1, v2, v3\n" \
"v_mfma_f32_16x16x32_bf16 a[100:103], a[200:203], v[112:115], a[100:103]\n" \
"s_add_u32 s71, s73, 17408\n" \
"v_exp_f32 v4, v4\n" \
"v_mfma_f32_16x16x32_bf16 a[100:103], a[204:207], v[116:119], a[100:103]\n" \
"buffer_load_dwordx4 v234, s[36:39], s73 offen offset:1024 sc1 lds\n" \
"v_mfma_f32_16x16x32_bf16 a[68:71], a[200:203], v[96:99], a[68:71]\n" \
"v_exp_f32 v5, v5\n" \
"v_mfma_f32_16x16x32_bf16 a[68:71], a[204:207], v[100:103], a[68:71]\n" \
"ds_read_b128 a[196:199], v232 offset:2048\n" \
"v_cvt_pk_bf16_f32 v2, v4, v5\n" \
"v_smfmac_f32_16x16x64_bf16 a[48:51], v[244:247], v[64:71], v243\n" \
"buffer_load_dwordx4 v234, s[36:39], s73 offen offset:2048 sc1 lds\n" \
"v_mfma_f32_16x16x32_bf16 v[164:167], a[200:203], v[80:83], v[164:167]\n" \
"v_exp_f32 v6, v6\n" \
"v_mfma_f32_16x16x32_bf16 v[164:167], a[204:207], v[84:87], v[164:167]\n" \
"buffer_load_dwordx4 v234, s[36:39], s73 offen offset:3072 sc1 lds\n" \
"v_mfma_f32_16x16x32_bf16 v[132:135], a[200:203], v[64:67], v[132:135]\n" \
"v_exp_f32 v7, v7\n" \
"v_mfma_f32_16x16x32_bf16 v[132:135], a[204:207], v[68:71], v[132:135]\n" \
"ds_read_b128 a[200:203], v232 offset:4096\n" \
"v_cvt_pk_bf16_f32 v3, v6, v7\n" \
"v_mfma_f32_16x16x32_bf16 v[136:139], a[208:211], v[64:67], v[136:139]\n" \
"s_add_u32 m0, m0, 0x4000\n" \
"v_exp_f32 v16, v16\n" \
"v_mfma_f32_16x16x32_bf16 v[136:139], a[212:215], v[68:71], v[136:139]\n" \
"buffer_load_dwordx4 v234, s[36:39], s71 offen offset:0 sc1 lds\n" \
"v_mfma_f32_16x16x32_bf16 v[168:171], a[208:211], v[80:83], v[168:171]\n" \
"v_exp_f32 v17, v17\n" \
"v_mfma_f32_16x16x32_bf16 v[168:171], a[212:215], v[84:87], v[168:171]\n" \
"ds_read_b128 a[204:207], v232 offset:6144\n" \
"v_cvt_pk_bf16_f32 v16, v16, v17\n" \
"v_mfma_f32_16x16x32_bf16 a[72:75], a[208:211], v[96:99], a[72:75]\n" \
"v_exp_f32 v18, v18\n" \
"v_mfma_f32_16x16x32_bf16 a[72:75], a[212:215], v[100:103], a[72:75]\n" \
"buffer_load_dwordx4 v234, s[36:39], s71 offen offset:1024 sc1 lds\n" \
"v_mfma_f32_16x16x32_bf16 a[104:107], a[208:211], v[112:115], a[104:107]\n" \
"v_exp_f32 v19, v19\n" \
"v_mfma_f32_16x16x32_bf16 a[104:107], a[212:215], v[116:119], a[104:107]\n" \
"ds_read_b128 a[208:211], v232 offset:256\n" \
"v_cvt_pk_bf16_f32 v17, v18, v19\n" \
"v_mfma_f32_16x16x32_bf16 a[108:111], a[216:219], v[112:115], a[108:111]\n" \
"v_exp_f32 v20, v20\n" \
"v_mfma_f32_16x16x32_bf16 a[108:111], a[220:223], v[116:119], a[108:111]\n" \
"buffer_load_dwordx4 v234, s[36:39], s71 offen offset:2048 sc1 lds\n" \
"v_mfma_f32_16x16x32_bf16 a[76:79], a[216:219], v[96:99], a[76:79]\n" \
"v_exp_f32 v21, v21\n" \
"v_mfma_f32_16x16x32_bf16 a[76:79], a[220:223], v[100:103], a[76:79]\n" \
"ds_read_b128 a[212:215], v232 offset:2304\n" \
"v_cvt_pk_bf16_f32 v18, v20, v21\n" \
"v_smfmac_f32_16x16x64_bf16 a[52:55], v[244:247], v[80:87], v243\n" \
"buffer_load_dwordx4 v234, s[36:39], s71 offen offset:3072 sc1 lds\n" \
"v_mfma_f32_16x16x32_bf16 v[172:175], a[216:219], v[80:83], v[172:175]\n" \
"s_add_u32 m0, s77, s80\n" \
"v_exp_f32 v22, v22\n" \
"v_mfma_f32_16x16x32_bf16 v[172:175], a[220:223], v[84:87], v[172:175]\n" \
"buffer_load_dwordx4 v234, s[40:43], s76 offen offset:0 sc1 lds\n" \
"v_mfma_f32_16x16x32_bf16 v[140:143], a[216:219], v[64:67], v[140:143]\n" \
"v_exp_f32 v23, v23\n" \
"v_mfma_f32_16x16x32_bf16 v[140:143], a[220:223], v[68:71], v[140:143]\n" \
"ds_read_b128 a[216:219], v232 offset:4352\n" \
"v_cvt_pk_bf16_f32 v19, v22, v23\n" \
"v_mfma_f32_16x16x32_bf16 v[144:147], a[224:227], v[64:67], v[144:147]\n" \
"s_add_u32 s71, s76, 17408\n" \
"v_exp_f32 v32, v32\n" \
"v_mfma_f32_16x16x32_bf16 v[144:147], a[228:231], v[68:71], v[144:147]\n" \
"buffer_load_dwordx4 v234, s[40:43], s76 offen offset:1024 sc1 lds\n" \
"v_mfma_f32_16x16x32_bf16 v[176:179], a[224:227], v[80:83], v[176:179]\n" \
"v_exp_f32 v33, v33\n" \
"v_mfma_f32_16x16x32_bf16 v[176:179], a[228:231], v[84:87], v[176:179]\n" \
"ds_read_b128 a[220:223], v232 offset:6400\n" \
"v_cvt_pk_bf16_f32 v32, v32, v33\n" \
"v_mfma_f32_16x16x32_bf16 a[80:83], a[224:227], v[96:99], a[80:83]\n" \
"v_exp_f32 v34, v34\n" \
"v_mfma_f32_16x16x32_bf16 a[80:83], a[228:231], v[100:103], a[80:83]\n" \
"buffer_load_dwordx4 v234, s[40:43], s76 offen offset:2048 sc1 lds\n" \
"v_mfma_f32_16x16x32_bf16 a[112:115], a[224:227], v[112:115], a[112:115]\n" \
"v_exp_f32 v35, v35\n" \
"v_mfma_f32_16x16x32_bf16 a[112:115], a[228:231], v[116:119], a[112:115]\n" \
"ds_read_b128 a[224:227], v232 offset:8192\n" \
"v_cvt_pk_bf16_f32 v33, v34, v35\n" \
"v_mfma_f32_16x16x32_bf16 a[116:119], a[232:235], v[112:115], a[116:119]\n" \
"v_exp_f32 v36, v36\n" \
"v_mfma_f32_16x16x32_bf16 a[116:119], a[236:239], v[116:119], a[116:119]\n" \
"buffer_load_dwordx4 v234, s[40:43], s76 offen offset:3072 sc1 lds\n" \
"v_mfma_f32_16x16x32_bf16 a[84:87], a[232:235], v[96:99], a[84:87]\n" \
"v_exp_f32 v37, v37\n" \
"v_mfma_f32_16x16x32_bf16 a[84:87], a[236:239], v[100:103], a[84:87]\n" \
"s_add_u32 m0, m0, 0x4000\n" \
"ds_read_b128 a[228:231], v232 offset:10240\n" \
"v_cvt_pk_bf16_f32 v34, v36, v37\n" \
"v_smfmac_f32_16x16x64_bf16 v[224:227], v[244:247], v[96:103], v243\n" \
"buffer_load_dwordx4 v234, s[40:43], s71 offen offset:0 sc1 lds\n" \
"v_mfma_f32_16x16x32_bf16 v[180:183], a[232:235], v[80:83], v[180:183]\n" \
"v_exp_f32 v38, v38\n" \
"v_mfma_f32_16x16x32_bf16 v[180:183], a[236:239], v[84:87], v[180:183]\n" \
"buffer_load_dwordx4 v234, s[40:43], s71 offen offset:1024 sc1 lds\n" \
"v_mfma_f32_16x16x32_bf16 v[148:151], a[232:235], v[64:67], v[148:151]\n" \
"v_exp_f32 v39, v39\n" \
"v_mfma_f32_16x16x32_bf16 v[148:151], a[236:239], v[68:71], v[148:151]\n" \
"ds_read_b128 a[232:235], v232 offset:12288\n" \
"v_cvt_pk_bf16_f32 v35, v38, v39\n" \
"v_mfma_f32_16x16x32_bf16 v[152:155], a[32:35], v[64:67], v[152:155]\n" \
"v_exp_f32 v48, v48\n" \
"v_mfma_f32_16x16x32_bf16 v[152:155], a[36:39], v[68:71], v[152:155]\n" \
"buffer_load_dwordx4 v234, s[40:43], s71 offen offset:2048 sc1 lds\n" \
"v_mfma_f32_16x16x32_bf16 v[184:187], a[32:35], v[80:83], v[184:187]\n" \
"v_exp_f32 v49, v49\n" \
"v_mfma_f32_16x16x32_bf16 v[184:187], a[36:39], v[84:87], v[184:187]\n" \
"ds_read_b128 a[236:239], v232 offset:14336\n" \
"v_cvt_pk_bf16_f32 v48, v48, v49\n" \
"v_mfma_f32_16x16x32_bf16 a[88:91], a[32:35], v[96:99], a[88:91]\n" \
"v_exp_f32 v50, v50\n" \
"v_mfma_f32_16x16x32_bf16 a[88:91], a[36:39], v[100:103], a[88:91]\n" \
"buffer_load_dwordx4 v234, s[40:43], s71 offen offset:3072 sc1 lds\n" \
"v_mfma_f32_16x16x32_bf16 a[120:123], a[32:35], v[112:115], a[120:123]\n" \
"s_mov_b32 s76, s73\n" \
"v_exp_f32 v51, v51\n" \
"v_mfma_f32_16x16x32_bf16 a[120:123], a[36:39], v[116:119], a[120:123]\n" \
"s_add_u32 s73, s73, 34816\n" \
"ds_read_b128 a[244:247], v232 offset:10496\n" \
"v_cvt_pk_bf16_f32 v49, v50, v51\n" \
"v_mfma_f32_16x16x32_bf16 a[124:127], a[40:43], v[112:115], a[124:127]\n" \
"s_xor_b32 s74, s74, 0x8000\n" \
"v_exp_f32 v52, v52\n" \
"v_mfma_f32_16x16x32_bf16 a[124:127], a[44:47], v[116:119], a[124:127]\n" \
"s_xor_b32 s77, s77, 0x8000\n" \
"v_xor_b32 v233, s81, v233\n" \
"s_mov_b32 s81, 0x8000\n" \
"v_mfma_f32_16x16x32_bf16 a[92:95], a[40:43], v[96:99], a[92:95]\n" \
"v_exp_f32 v53, v53\n" \
"v_mfma_f32_16x16x32_bf16 a[92:95], a[44:47], v[100:103], a[92:95]\n" \
"ds_read_b128 a[248:251], v232 offset:12544\n" \
"v_cvt_pk_bf16_f32 v50, v52, v53\n" \
"v_smfmac_f32_16x16x64_bf16 v[228:231], v[244:247], v[112:119], v243\n" \
"v_mfma_f32_16x16x32_bf16 v[188:191], a[40:43], v[80:83], v[188:191]\n" \
"v_exp_f32 v54, v54\n" \
"v_mfma_f32_16x16x32_bf16 v[188:191], a[44:47], v[84:87], v[188:191]\n" \
"s_cmp_lg_u32 s82, 0\n" \
"v_exp_f32_e64 v55, v55\n" \
"v_mfma_f32_16x16x32_bf16 v[156:159], a[40:43], v[64:67], v[156:159]\n" \
"s_waitcnt lgkmcnt(2)\n" \
"ds_read_b128 a[252:255], v232 offset:14592\n" \
"v_cvt_pk_bf16_f32 v51, v54, v55\n" \
"v_mfma_f32_16x16x32_bf16 v[156:159], a[44:47], v[68:71], v[156:159]\n" \
"s_cbranch_scc1 Lep_%=\n" \
"Lepr_%=:\n" \
"v_mfma_f32_16x16x32_bf16 v[64:67], a[192:195], a[0:3], v[196:199]\n" \
"v_exp_f32 v8, v8\n" \
"v_mfma_f32_16x16x32_bf16 v[64:67], a[196:199], a[4:7], v[64:67]\n" \
"ds_read_b128 a[44:47], v233 offset:15360\n" \
"v_mfma_f32_16x16x32_bf16 v[64:67], a[200:203], a[8:11], v[64:67]\n" \
"v_exp_f32 v9, v9\n" \
"v_mfma_f32_16x16x32_bf16 v[64:67], a[204:207], a[12:15], v[64:67]\n" \
"ds_read_b128 a[32:35], v233 offset:6144\n" \
"v_cvt_pk_bf16_f32 v4, v8, v9\n" \
"v_mfma_f32_16x16x32_bf16 v[80:83], a[204:207], a[28:31], v[196:199]\n" \
"s_sub_u32 s84, s84, 1\n" \
"v_exp_f32 v10, v10\n" \
"v_mfma_f32_16x16x32_bf16 v[80:83], a[200:203], a[24:27], v[80:83]\n" \
"s_cmp_eq_u32 s84, 0\n" \
"s_cselect_b32 s70, s75, 0x8000\n" \
"v_xor_b32 v232, s70, v232\n" \
"v_mfma_f32_16x16x32_bf16 v[80:83], a[196:199], a[20:23], v[80:83]\n" \
"v_exp_f32 v11, v11\n" \
"v_mfma_f32_16x16x32_bf16 v[80:83], a[192:195], a[16:19], v[80:83]\n" \
"ds_read_b128 a[36:39], v233 offset:14336\n" \
"v_cvt_pk_bf16_f32 v5, v10, v11\n" \
"v_mfma_f32_16x16x32_bf16 v[96:99], a[192:195], a[160:163], v[196:199]\n" \
"v_exp_f32 v12, v12\n" \
"v_mfma_f32_16x16x32_bf16 v[96:99], a[196:199], a[164:167], v[96:99]\n" \
"v_mfma_f32_16x16x32_bf16 v[96:99], a[200:203], a[168:171], v[96:99]\n" \
"v_exp_f32 v13, v13\n" \
"v_mfma_f32_16x16x32_bf16 v[96:99], a[204:207], a[172:175], v[96:99]\n" \
"ds_read_b128 a[40:43], v233 offset:7168\n" \
"v_cvt_pk_bf16_f32 v6, v12, v13\n" \
"v_mfma_f32_16x16x32_bf16 v[112:115], a[204:207], a[188:191], v[196:199]\n" \
"v_exp_f32 v14, v14\n" \
"v_mfma_f32_16x16x32_bf16 v[112:115], a[200:203], a[184:187], v[112:115]\n" \
"v_mfma_f32_16x16x32_bf16 v[112:115], a[196:199], a[180:183], v[112:115]\n" \
"v_exp_f32 v15, v15\n" \
"v_mfma_f32_16x16x32_bf16 v[112:115], a[192:195], a[176:179], v[112:115]\n" \
"ds_read_b128 a[204:207], v233 offset:9216\n" \
"v_cvt_pk_bf16_f32 v7, v14, v15\n" \
"v_mfma_f32_16x16x32_bf16 v[116:119], a[208:211], a[176:179], v[196:199]\n" \
"v_exp_f32 v24, v24\n" \
"v_mfma_f32_16x16x32_bf16 v[116:119], a[212:215], a[180:183], v[116:119]\n" \
"v_mfma_f32_16x16x32_bf16 v[116:119], a[216:219], a[184:187], v[116:119]\n" \
"v_exp_f32 v25, v25\n" \
"v_mfma_f32_16x16x32_bf16 v[116:119], a[220:223], a[188:191], v[116:119]\n" \
"ds_read_b128 a[200:203], v233 offset:1024\n" \
"v_cvt_pk_bf16_f32 v20, v24, v25\n" \
"v_mfma_f32_16x16x32_bf16 v[100:103], a[220:223], a[172:175], v[196:199]\n" \
"v_exp_f32 v26, v26\n" \
"v_mfma_f32_16x16x32_bf16 v[100:103], a[216:219], a[168:171], v[100:103]\n" \
"v_mfma_f32_16x16x32_bf16 v[100:103], a[212:215], a[164:167], v[100:103]\n" \
"v_exp_f32 v27, v27\n" \
"v_mfma_f32_16x16x32_bf16 v[100:103], a[208:211], a[160:163], v[100:103]\n" \
"ds_read_b128 a[196:199], v233 offset:8192\n" \
"v_cvt_pk_bf16_f32 v21, v26, v27\n" \
"v_mfma_f32_16x16x32_bf16 v[84:87], a[208:211], a[16:19], v[196:199]\n" \
"v_exp_f32 v28, v28\n" \
"v_mfma_f32_16x16x32_bf16 v[84:87], a[212:215], a[20:23], v[84:87]\n" \
"v_mfma_f32_16x16x32_bf16 v[84:87], a[216:219], a[24:27], v[84:87]\n" \
"v_exp_f32 v29, v29\n" \
"v_mfma_f32_16x16x32_bf16 v[84:87], a[220:223], a[28:31], v[84:87]\n" \
"ds_read_b128 a[192:195], v233 offset:0\n" \
"v_cvt_pk_bf16_f32 v22, v28, v29\n" \
"v_mfma_f32_16x16x32_bf16 v[68:71], a[220:223], a[12:15], v[196:199]\n" \
"v_exp_f32 v30, v30\n" \
"v_mfma_f32_16x16x32_bf16 v[68:71], a[216:219], a[8:11], v[68:71]\n" \
"v_mfma_f32_16x16x32_bf16 v[68:71], a[212:215], a[4:7], v[68:71]\n" \
"s_waitcnt lgkmcnt(8)\n" \
"v_exp_f32 v31, v31\n" \
"v_mfma_f32_16x16x32_bf16 v[68:71], a[208:211], a[0:3], v[68:71]\n" \
"ds_read_b128 a[220:223], v233 offset:11264\n" \
"v_cvt_pk_bf16_f32 v23, v30, v31\n" \
"v_mfma_f32_16x16x32_bf16 v[72:75], a[224:227], a[0:3], v[196:199]\n" \
"v_exp_f32 v40, v40\n" \
"v_mfma_f32_16x16x32_bf16 v[72:75], a[228:231], a[4:7], v[72:75]\n" \
"v_mfma_f32_16x16x32_bf16 v[72:75], a[232:235], a[8:11], v[72:75]\n" \
"v_exp_f32 v41, v41\n" \
"v_mfma_f32_16x16x32_bf16 v[72:75], a[236:239], a[12:15], v[72:75]\n" \
"ds_read_b128 a[216:219], v233 offset:3072\n" \
"v_cvt_pk_bf16_f32 v36, v40, v41\n" \
"v_mfma_f32_16x16x32_bf16 v[88:91], a[236:239], a[28:31], v[196:199]\n" \
"v_exp_f32 v42, v42\n" \
"v_mfma_f32_16x16x32_bf16 v[88:91], a[232:235], a[24:27], v[88:91]\n" \
"v_mfma_f32_16x16x32_bf16 v[88:91], a[228:231], a[20:23], v[88:91]\n" \
"v_exp_f32 v43, v43\n" \
"v_mfma_f32_16x16x32_bf16 v[88:91], a[224:227], a[16:19], v[88:91]\n" \
"ds_read_b128 a[212:215], v233 offset:10240\n" \
"v_cvt_pk_bf16_f32 v37, v42, v43\n" \
"v_mfma_f32_16x16x32_bf16 v[104:107], a[224:227], a[160:163], v[196:199]\n" \
"v_exp_f32 v44, v44\n" \
"v_mfma_f32_16x16x32_bf16 v[104:107], a[228:231], a[164:167], v[104:107]\n" \
"v_mfma_f32_16x16x32_bf16 v[104:107], a[232:235], a[168:171], v[104:107]\n" \
"v_exp_f32 v45, v45\n" \
"v_mfma_f32_16x16x32_bf16 v[104:107], a[236:239], a[172:175], v[104:107]\n" \
"ds_read_b128 a[208:211], v233 offset:2048\n" \
"v_cvt_pk_bf16_f32 v38, v44, v45\n" \
"v_mfma_f32_16x16x32_bf16 v[120:123], a[236:239], a[188:191], v[196:199]\n" \
"v_exp_f32 v46, v46\n" \
"v_mfma_f32_16x16x32_bf16 v[120:123], a[232:235], a[184:187], v[120:123]\n" \
"v_mfma_f32_16x16x32_bf16 v[120:123], a[228:231], a[180:183], v[120:123]\n" \
"v_exp_f32 v47, v47\n" \
"v_mfma_f32_16x16x32_bf16 v[120:123], a[224:227], a[176:179], v[120:123]\n" \
"ds_read_b128 a[236:239], v233 offset:13312\n" \
"v_cvt_pk_bf16_f32 v39, v46, v47\n" \
"v_mfma_f32_16x16x32_bf16 v[124:127], a[240:243], a[176:179], v[196:199]\n" \
"v_exp_f32 v56, v56\n" \
"v_mfma_f32_16x16x32_bf16 v[124:127], a[244:247], a[180:183], v[124:127]\n" \
"ds_read_b128 a[232:235], v233 offset:5120\n" \
"v_mfma_f32_16x16x32_bf16 v[124:127], a[248:251], a[184:187], v[124:127]\n" \
"v_exp_f32 v57, v57\n" \
"v_mfma_f32_16x16x32_bf16 v[124:127], a[252:255], a[188:191], v[124:127]\n" \
"ds_read_b128 a[228:231], v233 offset:12288\n" \
"v_cvt_pk_bf16_f32 v52, v56, v57\n" \
"v_mfma_f32_16x16x32_bf16 v[108:111], a[252:255], a[172:175], v[196:199]\n" \
"v_exp_f32 v58, v58\n" \
"v_mfma_f32_16x16x32_bf16 v[108:111], a[248:251], a[168:171], v[108:111]\n" \
"ds_read_b128 a[224:227], v233 offset:4096\n" \
"v_mfma_f32_16x16x32_bf16 v[108:111], a[244:247], a[164:167], v[108:111]\n" \
"v_exp_f32 v59, v59\n" \
"v_mfma_f32_16x16x32_bf16 v[108:111], a[240:243], a[160:163], v[108:111]\n" \
"v_cvt_pk_bf16_f32 v53, v58, v59\n" \
"v_mfma_f32_16x16x32_bf16 v[92:95], a[240:243], a[16:19], v[196:199]\n" \
"v_exp_f32 v60, v60\n" \
"v_mfma_f32_16x16x32_bf16 v[92:95], a[244:247], a[20:23], v[92:95]\n" \
"v_mfma_f32_16x16x32_bf16 v[92:95], a[248:251], a[24:27], v[92:95]\n" \
"v_exp_f32 v61, v61\n" \
"v_mfma_f32_16x16x32_bf16 v[92:95], a[252:255], a[28:31], v[92:95]\n" \
"v_cvt_pk_bf16_f32 v54, v60, v61\n" \
"v_mfma_f32_16x16x32_bf16 v[76:79], a[252:255], a[12:15], v[196:199]\n" \
"v_exp_f32 v62, v62\n" \
"v_mfma_f32_16x16x32_bf16 v[76:79], a[248:251], a[8:11], v[76:79]\n" \
"s_cmp_lg_u32 s82, 0\n" \
"s_cbranch_scc1 Lnw_%=\n" \
"s_waitcnt vmcnt(0)\n" \
"Lnw_%=:\n" \
"v_mfma_f32_16x16x32_bf16 v[76:79], a[244:247], a[4:7], v[76:79]\n" \
"s_waitcnt lgkmcnt(0)\n" \
"v_exp_f32 v63, v63\n" \
"v_mfma_f32_16x16x32_bf16 v[76:79], a[240:243], a[0:3], v[76:79]\n" \
"s_barrier\n" \
"v_cvt_pk_bf16_f32 v55, v62, v63\n" \
"v_mfma_f32_16x16x32_bf16 v[128:131], a[192:195], v[0:3], v[128:131]\n" \
"s_add_u32 m0, s74, s79\n" \
"v_exp_f32 v64, v64\n" \
"v_mfma_f32_16x16x32_bf16 v[128:131], a[196:199], v[4:7], v[128:131]\n" \
"buffer_load_dwordx4 v234, s[36:39], s73 offen offset:0 sc1 lds\n" \
"v_mfma_f32_16x16x32_bf16 v[160:163], a[192:195], v[16:19], v[160:163]\n" \
"v_exp_f32 v65, v65\n" \
"v_mfma_f32_16x16x32_bf16 v[160:163], a[196:199], v[20:23], v[160:163]\n" \
"ds_read_b128 a[240:243], v232 offset:8448\n" \
"v_cvt_pk_bf16_f32 v64, v64, v65\n" \
"v_mfma_f32_16x16x32_bf16 a[64:67], a[192:195], v[32:35], a[64:67]\n" \
"s_add_u32 s71, s73, 17408\n" \
"v_exp_f32 v66, v66\n" \
"v_mfma_f32_16x16x32_bf16 a[64:67], a[196:199], v[36:39], a[64:67]\n" \
"buffer_load_dwordx4 v234, s[36:39], s73 offen offset:1024 sc1 lds\n" \
"v_mfma_f32_16x16x32_bf16 a[96:99], a[192:195], v[48:51], a[96:99]\n" \
"v_exp_f32 v67, v67\n" \
"v_mfma_f32_16x16x32_bf16 a[96:99], a[196:199], v[52:55], a[96:99]\n" \
"ds_read_b128 a[192:195], v232 offset:0\n" \
"v_cvt_pk_bf16_f32 v65, v66, v67\n" \
"v_mfma_f32_16x16x32_bf16 a[100:103], a[200:203], v[48:51], a[100:103]\n" \
"v_exp_f32 v68, v68\n" \
"v_mfma_f32_16x16x32_bf16 a[100:103], a[204:207], v[52:55], a[100:103]\n" \
"buffer_load_dwordx4 v234, s[36:39], s73 offen offset:2048 sc1 lds\n" \
"v_mfma_f32_16x16x32_bf16 a[68:71], a[200:203], v[32:35], a[68:71]\n" \
"v_exp_f32 v69, v69\n" \
"v_mfma_f32_16x16x32_bf16 a[68:71], a[204:207], v[36:39], a[68:71]\n" \
"ds_read_b128 a[196:199], v232 offset:2048\n" \
"v_cvt_pk_bf16_f32 v66, v68, v69\n" \
"v_smfmac_f32_16x16x64_bf16 a[48:51], v[244:247], v[0:7], v243\n" \
"buffer_load_dwordx4 v234, s[36:39], s73 offen offset:3072 sc1 lds\n" \
"v_mfma_f32_16x16x32_bf16 v[164:167], a[200:203], v[16:19], v[164:167]\n" \
"s_add_u32 m0, m0, 0x4000\n" \
"v_exp_f32 v70, v70\n" \
"v_mfma_f32_16x16x32_bf16 v[164:167], a[204:207], v[20:23], v[164:167]\n" \
"buffer_load_dwordx4 v234, s[36:39], s71 offen offset:0 sc1 lds\n" \
"v_mfma_f32_16x16x32_bf16 v[132:135], a[200:203], v[0:3], v[132:135]\n" \
"v_exp_f32 v71, v71\n" \
"v_mfma_f32_16x16x32_bf16 v[132:135], a[204:207], v[4:7], v[132:135]\n" \
"ds_read_b128 a[200:203], v232 offset:4096\n" \
"v_cvt_pk_bf16_f32 v67, v70, v71\n" \
"v_mfma_f32_16x16x32_bf16 v[136:139], a[208:211], v[0:3], v[136:139]\n" \
"v_exp_f32 v80, v80\n" \
"v_mfma_f32_16x16x32_bf16 v[136:139], a[212:215], v[4:7], v[136:139]\n" \
"buffer_load_dwordx4 v234, s[36:39], s71 offen offset:1024 sc1 lds\n" \
"v_mfma_f32_16x16x32_bf16 v[168:171], a[208:211], v[16:19], v[168:171]\n" \
"v_exp_f32 v81, v81\n" \
"v_mfma_f32_16x16x32_bf16 v[168:171], a[212:215], v[20:23], v[168:171]\n" \
"ds_read_b128 a[204:207], v232 offset:6144\n" \
"v_cvt_pk_bf16_f32 v80, v80, v81\n" \
"v_mfma_f32_16x16x32_bf16 a[72:75], a[208:211], v[32:35], a[72:75]\n" \
"v_exp_f32 v82, v82\n" \
"v_mfma_f32_16x16x32_bf16 a[72:75], a[212:215], v[36:39], a[72:75]\n" \
"buffer_load_dwordx4 v234, s[36:39], s71 offen offset:2048 sc1 lds\n" \
"v_mfma_f32_16x16x32_bf16 a[104:107], a[208:211], v[48:51], a[104:107]\n" \
"v_exp_f32 v83, v83\n" \
"v_mfma_f32_16x16x32_bf16 a[104:107], a[212:215], v[52:55], a[104:107]\n" \
"ds_read_b128 a[208:211], v232 offset:256\n" \
"v_cvt_pk_bf16_f32 v81, v82, v83\n" \
"v_mfma_f32_16x16x32_bf16 a[108:111], a[216:219], v[48:51], a[108:111]\n" \
"v_exp_f32 v84, v84\n" \
"v_mfma_f32_16x16x32_bf16 a[108:111], a[220:223], v[52:55], a[108:111]\n" \
"buffer_load_dwordx4 v234, s[36:39], s71 offen offset:3072 sc1 lds\n" \
"v_mfma_f32_16x16x32_bf16 a[76:79], a[216:219], v[32:35], a[76:79]\n" \
"v_exp_f32 v85, v85\n" \
"v_mfma_f32_16x16x32_bf16 a[76:79], a[220:223], v[36:39], a[76:79]\n" \
"s_add_u32 m0, s77, s80\n" \
"ds_read_b128 a[212:215], v232 offset:2304\n" \
"v_cvt_pk_bf16_f32 v82, v84, v85\n" \
"v_smfmac_f32_16x16x64_bf16 a[52:55], v[244:247], v[16:23], v243\n" \
"buffer_load_dwordx4 v234, s[40:43], s76 offen offset:0 sc1 lds\n" \
"v_mfma_f32_16x16x32_bf16 v[172:175], a[216:219], v[16:19], v[172:175]\n" \
"s_add_u32 s71, s76, 17408\n" \
"v_exp_f32 v86, v86\n" \
"v_mfma_f32_16x16x32_bf16 v[172:175], a[220:223], v[20:23], v[172:175]\n" \
"buffer_load_dwordx4 v234, s[40:43], s76 offen offset:1024 sc1 lds\n" \
"v_mfma_f32_16x16x32_bf16 v[140:143], a[216:219], v[0:3], v[140:143]\n" \
"v_exp_f32 v87, v87\n" \
"v_mfma_f32_16x16x32_bf16 v[140:143], a[220:223], v[4:7], v[140:143]\n" \
"ds_read_b128 a[216:219], v232 offset:4352\n" \
"v_cvt_pk_bf16_f32 v83, v86, v87\n" \
"v_mfma_f32_16x16x32_bf16 v[144:147], a[224:227], v[0:3], v[144:147]\n" \
"v_exp_f32 v96, v96\n" \
"v_mfma_f32_16x16x32_bf16 v[144:147], a[228:231], v[4:7], v[144:147]\n" \
"buffer_load_dwordx4 v234, s[40:43], s76 offen offset:2048 sc1 lds\n" \
"v_mfma_f32_16x16x32_bf16 v[176:179], a[224:227], v[16:19], v[176:179]\n" \
"v_exp_f32 v97, v97\n" \
"v_mfma_f32_16x16x32_bf16 v[176:179], a[228:231], v[20:23], v[176:179]\n" \
"ds_read_b128 a[220:223], v232 offset:6400\n" \
"v_cvt_pk_bf16_f32 v96, v96, v97\n" \
"v_mfma_f32_16x16x32_bf16 a[80:83], a[224:227], v[32:35], a[80:83]\n" \
"v_exp_f32 v98, v98\n" \
"v_mfma_f32_16x16x32_bf16 a[80:83], a[228:231], v[36:39], a[80:83]\n" \
"buffer_load_dwordx4 v234, s[40:43], s76 offen offset:3072 sc1 lds\n" \
"v_mfma_f32_16x16x32_bf16 a[112:115], a[224:227], v[48:51], a[112:115]\n" \
"v_exp_f32 v99, v99\n" \
"v_mfma_f32_16x16x32_bf16 a[112:115], a[228:231], v[52:55], a[112:115]\n" \
"ds_read_b128 a[224:227], v232 offset:8192\n" \
"v_cvt_pk_bf16_f32 v97, v98, v99\n" \
"v_mfma_f32_16x16x32_bf16 a[116:119], a[232:235], v[48:51], a[116:119]\n" \
"s_add_u32 m0, m0, 0x4000\n" \
"v_exp_f32 v100, v100\n" \
"v_mfma_f32_16x16x32_bf16 a[116:119], a[236:239], v[52:55], a[116:119]\n" \
"buffer_load_dwordx4 v234, s[40:43], s71 offen offset:0 sc1 lds\n" \
"v_mfma_f32_16x16x32_bf16 a[84:87], a[232:235], v[32:35], a[84:87]\n" \
"v_exp_f32 v101, v101\n" \
"v_mfma_f32_16x16x32_bf16 a[84:87], a[236:239], v[36:39], a[84:87]\n" \
"ds_read_b128 a[228:231], v232 offset:10240\n" \
"v_cvt_pk_bf16_f32 v98, v100, v101\n" \
"v_smfmac_f32_16x16x64_bf16 v[224:227], v[244:247], v[32:39], v243\n" \
"buffer_load_dwordx4 v234, s[40:43], s71 offen offset:1024 sc1 lds\n" \
"v_mfma_f32_16x16x32_bf16 v[180:183], a[232:235], v[16:19], v[180:183]\n" \
"v_exp_f32 v102, v102\n" \
"v_mfma_f32_16x16x32_bf16 v[180:183], a[236:239], v[20:23], v[180:183]\n" \
"buffer_load_dwordx4 v234, s[40:43], s71 offen offset:2048 sc1 lds\n" \
"v_mfma_f32_16x16x32_bf16 v[148:151], a[232:235], v[0:3], v[148:151]\n" \
"v_exp_f32 v103, v103\n" \
"v_mfma_f32_16x16x32_bf16 v[148:151], a[236:239], v[4:7], v[148:151]\n" \
"ds_read_b128 a[232:235], v232 offset:12288\n" \
"v_cvt_pk_bf16_f32 v99, v102, v103\n" \
"v_mfma_f32_16x16x32_bf16 v[152:155], a[32:35], v[0:3], v[152:155]\n" \
"v_exp_f32 v112, v112\n" \
"v_mfma_f32_16x16x32_bf16 v[152:155], a[36:39], v[4:7], v[152:155]\n" \
"buffer_load_dwordx4 v234, s[40:43], s71 offen offset:3072 sc1 lds\n" \
"v_mfma_f32_16x16x32_bf16 v[184:187], a[32:35], v[16:19], v[184:187]\n" \
"v_exp_f32 v113, v113\n" \
"v_mfma_f32_16x16x32_bf16 v[184:187], a[36:39], v[20:23], v[184:187]\n" \
"ds_read_b128 a[236:239], v232 offset:14336\n" \
"v_cvt_pk_bf16_f32 v112, v112, v113\n" \
"v_mfma_f32_16x16x32_bf16 a[88:91], a[32:35], v[32:35], a[88:91]\n" \
"v_exp_f32 v114, v114\n" \
"v_mfma_f32_16x16x32_bf16 a[88:91], a[36:39], v[36:39], a[88:91]\n" \
"buffer_load_dword v239, v242, s[56:59], s95 offen sc1\n" \
"v_mfma_f32_16x16x32_bf16 a[120:123], a[32:35], v[48:51], a[120:123]\n" \
"s_mov_b32 s69, s92\n" \
"v_exp_f32 v115, v115\n" \
"v_mfma_f32_16x16x32_bf16 a[120:123], a[36:39], v[52:55], a[120:123]\n" \
"s_mov_b32 s76, s73\n" \
"ds_read_b128 a[244:247], v232 offset:10496\n" \
"v_cvt_pk_bf16_f32 v113, v114, v115\n" \
"v_mfma_f32_16x16x32_bf16 a[124:127], a[40:43], v[48:51], a[124:127]\n" \
"s_add_u32 s73, s73, 34816\n" \
"v_exp_f32 v116, v116\n" \
"v_mfma_f32_16x16x32_bf16 a[124:127], a[44:47], v[52:55], a[124:127]\n" \
"s_xor_b32 s74, s74, 0x8000\n" \
"s_xor_b32 s77, s77, 0x8000\n" \
"v_xor_b32 v233, 0x8000, v233\n" \
"v_mfma_f32_16x16x32_bf16 a[92:95], a[40:43], v[32:35], a[92:95]\n" \
"s_mov_b32 s82, 0\n" \
"v_exp_f32 v117, v117\n" \
"v_mfma_f32_16x16x32_bf16 a[92:95], a[44:47], v[36:39], a[92:95]\n" \
"ds_read_b128 a[248:251], v232 offset:12544\n" \
"v_cvt_pk_bf16_f32 v114, v116, v117\n" \
"Lpg6775_%=:\n" \
"s_cbranch_execz Lcb0_%=\n" \
"v_smfmac_f32_16x16x64_bf16 v[228:231], v[244:247], v[48:55], v243\n" \
"v_mfma_f32_16x16x32_bf16 v[188:191], a[40:43], v[16:19], v[188:191]\n" \
"v_exp_f32_e64 v118, v118\n" \
"v_mfma_f32_16x16x32_bf16 v[188:191], a[44:47], v[20:23], v[188:191]\n" \
"s_cmp_lg_u32 s84, 0\n" \
"v_exp_f32_e64 v119, v119\n" \
"v_mfma_f32_16x16x32_bf16 v[156:159], a[40:43], v[0:3], v[156:159]\n" \
"s_waitcnt lgkmcnt(2)\n" \
"ds_read_b128 a[252:255], v232 offset:14592\n" \
"v_cvt_pk_bf16_f32 v115, v118, v119\n" \
"v_mfma_f32_16x16x32_bf16 v[156:159], a[44:47], v[4:7], v[156:159]\n" \
"s_cbranch_scc1 Lloop2_%=\n" \
"Lg_%=:\n" \
"Lpg6789_%=:\n" \
"s_cbranch_execz Lcb0_%=\n" \
"v_mfma_f32_16x16x32_bf16 v[32:35], a[192:195], a[160:163], v[208:211]\n" \
"s_mov_b64 s[44:45], s[20:21]\n" \
"s_memrealtime s[14:15]\n" \
"v_exp_f32 v72, v72\n" \
"v_mfma_f32_16x16x32_bf16 v[32:35], a[196:199], a[164:167], v[32:35]\n" \
"s_and_b32 s45, s45, 0xffff\n" \
"ds_read_b128 a[32:35], v233 offset:6144\n" \
"ds_read_b128 a[36:39], v233 offset:14336\n" \
"v_mfma_f32_16x16x32_bf16 v[32:35], a[200:203], a[168:171], v[32:35]\n" \
"s_mov_b32 s46, 4194304\n" \
"v_exp_f32 v73, v73\n" \
"v_mfma_f32_16x16x32_bf16 v[32:35], a[204:207], a[172:175], v[32:35]\n" \
"ds_read_b128 a[40:43], v233 offset:7168\n" \
"ds_read_b128 a[44:47], v233 offset:15360\n" \
"global_load_dword v253, v254, s[22:23]\n" \
"v_mfma_f32_16x16x32_bf16 v[48:51], a[204:207], a[188:191], v[196:199]\n" \
"s_mov_b32 s47, 0x20000\n" \
"v_exp_f32 v74, v74\n" \
"v_mfma_f32_16x16x32_bf16 v[48:51], a[200:203], a[184:187], v[48:51]\n" \
"s_mov_b64 s[48:49], s[22:23]\n" \
"v_exp_f32 v75, v75\n" \
"v_mfma_f32_16x16x32_bf16 v[48:51], a[196:199], a[180:183], v[48:51]\n" \
"s_and_b32 s49, s49, 0xffff\n" \
"v_exp_f32 v76, v76\n" \
"v_mfma_f32_16x16x32_bf16 v[48:51], a[192:195], a[176:179], v[48:51]\n" \
"s_mov_b32 s50, 6291456\n" \
"ds_read_b128 a[200:203], v233 offset:1024\n" \
"ds_read_b128 a[204:207], v233 offset:9216\n" \
"v_mfma_f32_16x16x32_bf16 v[52:55], a[208:211], a[176:179], v[196:199]\n" \
"s_mov_b32 s51, 0x20000\n" \
"v_cvt_pk_bf16_f32 v68, v72, v73\n" \
"v_cvt_pk_bf16_f32 v69, v74, v75\n" \
"v_mfma_f32_16x16x32_bf16 v[52:55], a[212:215], a[180:183], v[52:55]\n" \
"s_bfe_u32 s94, %1, 0x20001\n" \
"v_exp_f32 v77, v77\n" \
"v_mfma_f32_16x16x32_bf16 v[52:55], a[216:219], a[184:187], v[52:55]\n" \
"s_lshl_b32 s94, s94, 2\n" \
"v_exp_f32 v78, v78\n" \
"v_mfma_f32_16x16x32_bf16 v[52:55], a[220:223], a[188:191], v[52:55]\n" \
"s_lshr_b32 s70, %1, 6\n" \
"ds_read_b128 a[192:195], v233 offset:0\n" \
"ds_read_b128 a[196:199], v233 offset:8192\n" \
"v_mfma_f32_16x16x32_bf16 v[36:39], a[220:223], a[172:175], v[212:215]\n" \
"s_add_u32 s94, s94, s70\n" \
"v_exp_f32 v79, v79\n" \
"v_mfma_f32_16x16x32_bf16 v[36:39], a[216:219], a[168:171], v[36:39]\n" \
"s_lshr_b32 s71, s22, 7\n" \
"v_exp_f32 v88, v88\n" \
"v_mfma_f32_16x16x32_bf16 v[36:39], a[212:215], a[164:167], v[36:39]\n" \
"s_sub_u32 s71, s63, s71\n" \
"v_cvt_pk_bf16_f32 v70, v76, v77\n" \
"v_cvt_pk_bf16_f32 v71, v78, v79\n" \
"v_mfma_f32_16x16x32_bf16 v[36:39], a[208:211], a[160:163], v[36:39]\n" \
"s_add_u32 s71, s71, 6\n" \
"ds_read_b128 a[216:219], v233 offset:3072\n" \
"ds_read_b128 a[220:223], v233 offset:11264\n" \
"v_mfma_f32_16x16x32_bf16 v[40:43], a[224:227], a[160:163], v[216:219]\n" \
"s_and_b32 s71, s71, 7\n" \
"v_exp_f32 v89, v89\n" \
"v_mfma_f32_16x16x32_bf16 v[40:43], a[228:231], a[164:167], v[40:43]\n" \
"s_and_b32 s70, %1, 1\n" \
"v_exp_f32 v90, v90\n" \
"v_mfma_f32_16x16x32_bf16 v[40:43], a[232:235], a[168:171], v[40:43]\n" \
"s_lshl_b32 s70, s70, 3\n" \
"v_exp_f32 v91, v91\n" \
"v_mfma_f32_16x16x32_bf16 v[40:43], a[236:239], a[172:175], v[40:43]\n" \
"s_add_u32 s70, s70, s71\n" \
"ds_read_b128 a[208:211], v233 offset:2048\n" \
"ds_read_b128 a[212:215], v233 offset:10240\n" \
"v_mfma_f32_16x16x32_bf16 v[56:59], a[236:239], a[188:191], v[196:199]\n" \
"s_lshl_b32 s78, s70, 7\n" \
"v_exp_f32 v92, v92\n" \
"v_mfma_f32_16x16x32_bf16 v[56:59], a[232:235], a[184:187], v[56:59]\n" \
"s_lshr_b32 s80, %1, 6\n" \
"v_cvt_pk_bf16_f32 v84, v88, v89\n" \
"v_cvt_pk_bf16_f32 v85, v90, v91\n" \
"v_mfma_f32_16x16x32_bf16 v[56:59], a[228:231], a[180:183], v[56:59]\n" \
"s_xor_b32 s80, s80, s63\n" \
"v_exp_f32 v93, v93\n" \
"v_mfma_f32_16x16x32_bf16 v[56:59], a[224:227], a[176:179], v[56:59]\n" \
"s_and_b32 s80, s80, 0\n" \
"ds_read_b128 a[232:235], v233 offset:5120\n" \
"ds_read_b128 a[236:239], v233 offset:13312\n" \
"v_mfma_f32_16x16x32_bf16 v[60:63], a[240:243], a[176:179], v[196:199]\n" \
"s_waitcnt lgkmcnt(2)\n" \
"s_lshl_b32 s93, s70, 12\n" \
"v_exp_f32 v94, v94\n" \
"v_mfma_f32_16x16x32_bf16 v[60:63], a[244:247], a[180:183], v[60:63]\n" \
"s_mul_i32 s91, s61, 6144\n" \
"v_exp_f32 v95, v95\n" \
"v_mfma_f32_16x16x32_bf16 v[60:63], a[248:251], a[184:187], v[60:63]\n" \
"s_add_u32 s91, s91, %3\n" \
"v_cvt_pk_bf16_f32 v86, v92, v93\n" \
"v_mfma_f32_16x16x32_bf16 v[60:63], a[252:255], a[188:191], v[60:63]\n" \
"s_lshl_b32 s71, s80, 7\n" \
"ds_read_b128 a[224:227], v233 offset:4096\n" \
"ds_read_b128 a[228:231], v233 offset:12288\n" \
"v_mfma_f32_16x16x32_bf16 v[44:47], a[252:255], a[172:175], v[220:223]\n" \
"s_lshl_b32 s75, s94, 18\n" \
"v_cvt_pk_bf16_f32 v87, v94, v95\n" \
"v_mfma_f32_16x16x32_bf16 v[44:47], a[248:251], a[168:171], v[44:47]\n" \
"s_lshl_b32 s70, s61, 16\n" \
"v_exp_f32 v104, v104\n" \
"v_mfma_f32_16x16x32_bf16 v[44:47], a[244:247], a[164:167], v[44:47]\n" \
"s_add_u32 s73, s75, s70\n" \
"v_accvgpr_read_b32 v197, a56\n" \
"v_accvgpr_read_b32 v192, a57\n" \
"v_mfma_f32_16x16x32_bf16 v[44:47], a[240:243], a[160:163], v[44:47]\n" \
"s_add_u32 s73, s73, s71\n" \
"v_accvgpr_read_b32 v196, a58\n" \
"v_add_u32 v198, 1024, v197\n" \
"v_mfma_f32_16x16x32_bf16 v[128:131], a[192:195], v[64:67], v[128:131]\n" \
"s_max_u32 s75, s75, 32768\n" \
"v_add_u32 v199, 2048, v197\n" \
"v_add_u32 v193, 31744, v192\n" \
"v_mfma_f32_16x16x32_bf16 v[128:131], a[196:199], v[68:71], v[128:131]\n" \
"s_sub_u32 s75, s75, 32768\n" \
"ds_read_b128 a[0:3], v232 offset:16384\n" \
"ds_read_b128 a[4:7], v232 offset:18432\n" \
"v_mfma_f32_16x16x32_bf16 v[160:163], a[192:195], v[80:83], v[160:163]\n" \
"s_add_u32 s75, s75, s71\n" \
"s_lshl_b32 s89, s61, 10\n" \
"v_mfma_f32_16x16x32_bf16 v[160:163], a[196:199], v[84:87], v[160:163]\n" \
"s_mul_i32 s76, s80, 393216\n" \
"s_add_u32 s89, s89, %3\n" \
"v_exp_f32 v105, v105\n" \
"v_mfma_f32_16x16x32_bf16 v[164:167], a[200:203], v[80:83], v[164:167]\n" \
"s_add_u32 s76, s76, s78\n" \
"ds_read_b128 a[8:11], v232 offset:20480\n" \
"ds_read_b128 a[12:15], v232 offset:22528\n" \
"v_mfma_f32_16x16x32_bf16 v[164:167], a[204:207], v[84:87], v[164:167]\n" \
"s_add_u32 m0, s91, 139264\n" \
"s_add_u32 s89, s89, 16384\n" \
"v_exp_f32 v106, v106\n" \
"v_mfma_f32_16x16x32_bf16 v[132:135], a[200:203], v[64:67], v[132:135]\n" \
"buffer_load_dwordx4 v197, s[48:51], s76 offen offset:0 lds\n" \
"v_mfma_f32_16x16x32_bf16 v[132:135], a[204:207], v[68:71], v[132:135]\n" \
"s_and_b32 s70, s61, 1\n" \
"ds_read_b128 a[16:19], v232 offset:16640\n" \
"ds_read_b128 a[20:23], v232 offset:18688\n" \
"v_smfmac_f32_16x16x64_bf16 a[48:51], v[244:247], v[64:71], v243\n" \
"s_add_u32 s77, s76, 24576\n" \
"v_cvt_pk_bf16_f32 v100, v104, v105\n" \
"v_mfma_f32_16x16x32_bf16 v[136:139], a[208:211], v[64:67], v[136:139]\n" \
"buffer_load_dwordx4 v198, s[48:51], s76 offen offset:1024 lds\n" \
"v_mfma_f32_16x16x32_bf16 v[136:139], a[212:215], v[68:71], v[136:139]\n" \
"s_lshl_b32 s70, s70, 11\n" \
"ds_read_b128 a[24:27], v232 offset:20736\n" \
"ds_read_b128 a[28:31], v232 offset:22784\n" \
"v_mfma_f32_16x16x32_bf16 v[168:171], a[208:211], v[80:83], v[168:171]\n" \
"s_xor_b32 s80, s80, 0x3\n" \
"v_exp_f32 v107, v107\n" \
"v_mfma_f32_16x16x32_bf16 v[168:171], a[212:215], v[84:87], v[168:171]\n" \
"buffer_load_dwordx4 v199, s[48:51], s76 offen offset:2048 lds\n" \
"v_exp_f32 v108, v108\n" \
"v_mfma_f32_16x16x32_bf16 v[172:175], a[216:219], v[80:83], v[172:175]\n" \
"s_add_u32 s93, s93, s70\n" \
"ds_read_b128 v[0:3], v232 offset:24576\n" \
"ds_read_b128 v[4:7], v232 offset:26624\n" \
"v_mfma_f32_16x16x32_bf16 v[172:175], a[220:223], v[84:87], v[172:175]\n" \
"s_add_u32 m0, m0, 3072\n" \
"v_exp_f32 v109, v109\n" \
"v_mfma_f32_16x16x32_bf16 v[140:143], a[216:219], v[64:67], v[140:143]\n" \
"buffer_load_dwordx4 v197, s[48:51], s77 offen offset:0 lds\n" \
"v_mfma_f32_16x16x32_bf16 v[140:143], a[220:223], v[68:71], v[140:143]\n" \
"s_add_u32 s93, s93, 52740096\n" \
"ds_read_b128 v[8:11], v232 offset:28672\n" \
"ds_read_b128 v[12:15], v232 offset:30720\n" \
"v_smfmac_f32_16x16x64_bf16 a[52:55], v[244:247], v[80:87], v243\n" \
"s_waitcnt lgkmcnt(4)\n" \
"s_cmp_eq_u32 s94, 0\n" \
"s_cselect_b32 s92, -1, 0\n" \
"v_cvt_pk_bf16_f32 v101, v106, v107\n" \
"v_mfma_f32_16x16x32_bf16 v[144:147], a[224:227], v[64:67], v[144:147]\n" \
"buffer_load_dwordx4 v198, s[48:51], s77 offen offset:1024 lds\n" \
"v_mfma_f32_16x16x32_bf16 v[144:147], a[228:231], v[68:71], v[144:147]\n" \
"s_mul_i32 s70, s94, 278528\n" \
"ds_read_b128 v[200:203], v232 offset:24832\n" \
"ds_read_b128 v[204:207], v232 offset:26880\n" \
"v_exp_f32 v110, v110\n" \
"v_mfma_f32_16x16x32_bf16 v[176:179], a[224:227], v[80:83], v[176:179]\n" \
"s_add_u32 s93, s93, s70\n" \
"v_exp_f32 v111, v111\n" \
"v_mfma_f32_16x16x32_bf16 v[176:179], a[228:231], v[84:87], v[176:179]\n" \
"buffer_load_dwordx4 v199, s[48:51], s77 offen offset:2048 lds\n" \
"v_mfma_f32_16x16x32_bf16 v[180:183], a[232:235], v[80:83], v[180:183]\n" \
"s_lshr_b32 s70, s61, 1\n" \
"ds_read_b128 a[240:243], v232 offset:28928\n" \
"ds_read_b128 a[244:247], v232 offset:30976\n" \
"v_mfma_f32_16x16x32_bf16 v[180:183], a[236:239], v[84:87], v[180:183]\n" \
"s_lshl_b32 s70, s70, 8\n" \
"v_exp_f32 v120, v120\n" \
"v_cvt_pk_bf16_f32 v102, v108, v109\n" \
"v_mfma_f32_16x16x32_bf16 v[148:151], a[232:235], v[64:67], v[148:151]\n" \
"s_add_u32 s93, s93, s70\n" \
"v_exp_f32 v121, v121\n" \
"v_mfma_f32_16x16x32_bf16 v[148:151], a[236:239], v[68:71], v[148:151]\n" \
"v_exp_f32 v122, v122\n" \
"v_mfma_f32_16x16x32_bf16 v[152:155], a[32:35], v[64:67], v[152:155]\n" \
"v_add_u32 v233, 0x10000, v235\n" \
"s_lshl_b32 s72, s61, 14\n" \
"v_mfma_f32_16x16x32_bf16 v[152:155], a[36:39], v[68:71], v[152:155]\n" \
"v_add_u32 v232, s72, v235\n" \
"v_cvt_pk_bf16_f32 v103, v110, v111\n" \
"v_mfma_f32_16x16x32_bf16 v[184:187], a[32:35], v[80:83], v[184:187]\n" \
"v_exp_f32 v123, v123\n" \
"v_mfma_f32_16x16x32_bf16 v[184:187], a[36:39], v[84:87], v[184:187]\n" \
"v_exp_f32 v124, v124\n" \
"v_cvt_pk_bf16_f32 v116, v120, v121\n" \
"v_mfma_f32_16x16x32_bf16 v[188:191], a[40:43], v[80:83], v[188:191]\n" \
"v_exp_f32 v125, v125\n" \
"v_mfma_f32_16x16x32_bf16 v[188:191], a[44:47], v[84:87], v[188:191]\n" \
"v_exp_f32_e64 v126, v126\n" \
"v_cvt_pk_bf16_f32 v117, v122, v123\n" \
"v_mfma_f32_16x16x32_bf16 v[156:159], a[40:43], v[64:67], v[156:159]\n" \
"v_exp_f32_e64 v127, v127\n" \
"v_mfma_f32_16x16x32_bf16 v[156:159], a[44:47], v[68:71], v[156:159]\n" \
"s_cmp_eq_u32 s63, 7\n" \
"s_waitcnt vmcnt(7) lgkmcnt(0)\n" \
"s_barrier\n" \
"s_cbranch_scc1 Lep_%=\n" \
"Lepg_%=:\n" \
"v_mfma_f32_16x16x32_bf16 v[16:19], a[0:3], a[176:179], v[208:211]\n" \
"s_add_u32 m0, s79, 110592\n" \
"ds_read_b128 v[64:67], v233 offset:16384\n" \
"ds_read_b128 v[68:71], v233 offset:24576\n" \
"v_mfma_f32_16x16x32_bf16 v[16:19], a[4:7], a[180:183], v[16:19]\n" \
"buffer_load_dwordx4 v192, s[44:47], s73 offen offset:0 lds\n" \
"v_mfma_f32_16x16x32_bf16 v[16:19], a[8:11], a[184:187], v[16:19]\n" \
"ds_read_b128 v[72:75], v233 offset:17408\n" \
"ds_read_b128 v[76:79], v233 offset:25600\n" \
"v_mfma_f32_16x16x32_bf16 v[16:19], a[12:15], a[188:191], v[16:19]\n" \
"ds_read_b128 v[80:83], v233 offset:18432\n" \
"ds_read_b128 v[84:87], v233 offset:26624\n" \
"v_cvt_pk_bf16_f32 v128, v128, v129\n" \
"v_cvt_pk_bf16_f32 v129, v130, v131\n" \
"v_cvt_pk_bf16_f32 v130, v132, v133\n" \
"v_cvt_pk_bf16_f32 v118, v124, v125\n" \
"v_mfma_f32_16x16x32_bf16 v[20:23], a[28:31], a[188:191], v[212:215]\n" \
"ds_read_b128 v[88:91], v233 offset:19456\n" \
"ds_read_b128 v[92:95], v233 offset:27648\n" \
"v_mfma_f32_16x16x32_bf16 v[20:23], a[24:27], a[184:187], v[20:23]\n" \
"buffer_load_dwordx4 v193, s[44:47], s73 offen offset:1024 lds\n" \
"v_cvt_pk_bf16_f32 v131, v134, v135\n" \
"v_cvt_pk_bf16_f32 v119, v126, v127\n" \
"v_xor_b32 v248, 16384, v232\n" \
"v_mfma_f32_16x16x32_bf16 v[20:23], a[20:23], a[180:183], v[20:23]\n" \
"ds_write_b128 v232, v[128:131] offset:0\n" \
"v_exp_f32 v32, v32\n" \
"v_mfma_f32_16x16x32_bf16 v[20:23], a[16:19], a[176:179], v[20:23]\n" \
"ds_read_b128 a[248:251], v233 offset:7168\n" \
"ds_read_b128 a[252:255], v233 offset:15360\n" \
"v_exp_f32 v33, v33\n" \
"v_mfma_f32_16x16x32_bf16 v[24:27], v[0:3], a[176:179], v[216:219]\n" \
"v_cvt_pk_bf16_f32 v132, v136, v137\n" \
"v_cvt_pk_bf16_f32 v133, v138, v139\n" \
"v_mfma_f32_16x16x32_bf16 v[24:27], v[4:7], a[180:183], v[24:27]\n" \
"buffer_load_dwordx4 v192, s[44:47], s73 offen offset:2048 lds\n" \
"v_exp_f32 v34, v34\n" \
"v_lshrrev_b32 v249, 1, v234\n" \
"v_mfma_f32_16x16x32_bf16 v[24:27], v[8:11], a[184:187], v[24:27]\n" \
"s_lshl_b32 s72, s61, 9\n" \
"v_cvt_pk_bf16_f32 v134, v140, v141\n" \
"v_cvt_pk_bf16_f32 v135, v142, v143\n" \
"v_mfma_f32_16x16x32_bf16 v[24:27], v[12:15], a[188:191], v[24:27]\n" \
"s_add_u32 s72, s72, 0x20000\n" \
"v_cvt_pk_bf16_f32 v136, v144, v145\n" \
"v_cvt_pk_bf16_f32 v137, v146, v147\n" \
"v_exp_f32 v35, v35\n" \
"v_exp_f32 v36, v36\n" \
"v_mfma_f32_16x16x32_bf16 v[28:31], a[244:247], a[188:191], v[220:223]\n" \
"ds_write_b128 v232, v[132:135] offset:1024\n" \
"s_add_u32 s72, s72, %3\n" \
"v_cvt_pk_bf16_f32 v32, v32, v33\n" \
"v_mfma_f32_16x16x32_bf16 v[28:31], a[240:243], a[184:187], v[28:31]\n" \
"buffer_load_dwordx4 v193, s[44:47], s73 offen offset:3072 lds\n" \
"v_mfma_f32_16x16x32_bf16 v[28:31], v[204:207], a[180:183], v[28:31]\n" \
"v_cvt_pk_bf16_f32 v138, v148, v149\n" \
"v_cvt_pk_bf16_f32 v139, v150, v151\n" \
"v_exp_f32 v37, v37\n" \
"v_add_u32 v249, s72, v249\n" \
"v_mfma_f32_16x16x32_bf16 v[28:31], v[200:203], a[176:179], v[28:31]\n" \
"s_add_u32 m0, s89, 110592\n" \
"v_cvt_pk_bf16_f32 v140, v152, v153\n" \
"v_cvt_pk_bf16_f32 v141, v154, v155\n" \
"v_exp_f32 v38, v38\n" \
"v_mfma_f32_16x16x32_bf16 a[64:67], a[192:195], v[96:99], a[64:67]\n" \
"s_xor_b32 s73, s73, 0x180\n" \
"v_cvt_pk_bf16_f32 v142, v156, v157\n" \
"v_cvt_pk_bf16_f32 v143, v158, v159\n" \
"v_mfma_f32_16x16x32_bf16 a[64:67], a[196:199], v[100:103], a[64:67]\n" \
"buffer_load_dwordx4 v196, s[44:47], s75 offen lds\n" \
"v_cvt_pk_bf16_f32 v33, v34, v35\n" \
"v_exp_f32 v39, v39\n" \
"v_mfma_f32_16x16x32_bf16 a[96:99], a[192:195], v[112:115], a[96:99]\n" \
"v_cvt_pk_bf16_f32 v144, v160, v161\n" \
"v_cvt_pk_bf16_f32 v145, v162, v163\n" \
"v_mfma_f32_16x16x32_bf16 a[96:99], a[196:199], v[116:119], a[96:99]\n" \
"ds_write_b128 v232, v[136:139] offset:2048\n" \
"v_exp_f32 v48, v48\n" \
"v_cvt_pk_bf16_f32 v34, v36, v37\n" \
"v_accvgpr_read_b32 v250, a48\n" \
"v_mfma_f32_16x16x32_bf16 a[100:103], a[200:203], v[112:115], a[100:103]\n" \
"s_xor_b32 s75, s75, 0x180\n" \
"ds_read_b128 a[192:195], v233 offset:0\n" \
"ds_read_b128 a[196:199], v233 offset:8192\n" \
"v_mfma_f32_16x16x32_bf16 a[100:103], a[204:207], v[116:119], a[100:103]\n" \
"v_cvt_pk_bf16_f32 v146, v164, v165\n" \
"v_cvt_pk_bf16_f32 v147, v166, v167\n" \
"v_exp_f32 v49, v49\n" \
"v_exp_f32 v50, v50\n" \
"v_mfma_f32_16x16x32_bf16 a[68:71], a[200:203], v[96:99], a[68:71]\n" \
"v_cvt_pk_bf16_f32 v148, v168, v169\n" \
"v_cvt_pk_bf16_f32 v149, v170, v171\n" \
"v_mfma_f32_16x16x32_bf16 a[68:71], a[204:207], v[100:103], a[68:71]\n" \
"ds_read_b128 a[200:203], v233 offset:1024\n" \
"ds_read_b128 a[204:207], v233 offset:9216\n" \
"v_cvt_pk_bf16_f32 v35, v38, v39\n" \
"v_exp_f32 v51, v51\n" \
"v_smfmac_f32_16x16x64_bf16 v[224:227], v[244:247], v[96:103], v243\n" \
"v_cvt_pk_bf16_f32 v150, v172, v173\n" \
"v_exp_f32 v52, v52\n" \
"v_cvt_pk_bf16_f32 v48, v48, v49\n" \
"v_accvgpr_read_b32 v240, a49\n" \
"v_mfma_f32_16x16x32_bf16 a[72:75], a[208:211], v[96:99], a[72:75]\n" \
"v_cvt_pk_bf16_f32 v151, v174, v175\n" \
"v_cvt_pk_bf16_f32 v152, v176, v177\n" \
"v_mfma_f32_16x16x32_bf16 a[72:75], a[212:215], v[100:103], a[72:75]\n" \
"ds_write_b128 v232, v[140:143] offset:3072\n" \
"v_exp_f32 v53, v53\n" \
"v_mfma_f32_16x16x32_bf16 a[104:107], a[208:211], v[112:115], a[104:107]\n" \
"v_cvt_pk_bf16_f32 v153, v178, v179\n" \
"v_cvt_pk_bf16_f32 v154, v180, v181\n" \
"v_mfma_f32_16x16x32_bf16 a[104:107], a[212:215], v[116:119], a[104:107]\n" \
"ds_read_b128 a[208:211], v233 offset:2048\n" \
"ds_read_b128 a[212:215], v233 offset:10240\n" \
"v_exp_f32 v54, v54\n" \
"v_cvt_pk_bf16_f32 v49, v50, v51\n" \
"v_exp_f32 v55, v55\n" \
"v_mfma_f32_16x16x32_bf16 a[108:111], a[216:219], v[112:115], a[108:111]\n" \
"v_cvt_pk_bf16_f32 v155, v182, v183\n" \
"v_cvt_pk_bf16_f32 v156, v184, v185\n" \
"v_mfma_f32_16x16x32_bf16 a[108:111], a[220:223], v[116:119], a[108:111]\n" \
"v_cvt_pk_bf16_f32 v157, v186, v187\n" \
"v_cvt_pk_bf16_f32 v158, v188, v189\n" \
"v_exp_f32 v40, v40\n" \
"v_cvt_pk_bf16_f32 v50, v52, v53\n" \
"v_add_f32 v250, v250, v240\n" \
"v_mfma_f32_16x16x32_bf16 a[76:79], a[216:219], v[96:99], a[76:79]\n" \
"v_cvt_pk_bf16_f32 v159, v190, v191\n" \
"v_cvt_pk_bf16_f32 v51, v54, v55\n" \
"v_mfma_f32_16x16x32_bf16 a[76:79], a[220:223], v[100:103], a[76:79]\n" \
"ds_read_b128 a[216:219], v233 offset:3072\n" \
"ds_read_b128 a[220:223], v233 offset:11264\n" \
"v_exp_f32 v41, v41\n" \
"v_exp_f32 v42, v42\n" \
"v_smfmac_f32_16x16x64_bf16 v[228:231], v[244:247], v[112:119], v243\n" \
"ds_write_b128 v232, v[144:147] offset:4096\n" \
"v_mfma_f32_16x16x32_bf16 a[80:83], a[224:227], v[96:99], a[80:83]\n" \
"v_exp_f32 v43, v43\n" \
"v_mfma_f32_16x16x32_bf16 a[80:83], a[228:231], v[100:103], a[80:83]\n" \
"v_exp_f32 v44, v44\n" \
"v_cvt_pk_bf16_f32 v36, v40, v41\n" \
"v_exp_f32 v45, v45\n" \
"v_exp_f32 v46, v46\n" \
"v_accvgpr_read_b32 v251, a52\n" \
"v_mfma_f32_16x16x32_bf16 a[112:115], a[224:227], v[112:115], a[112:115]\n" \
"v_cvt_pk_bf16_f32 v37, v42, v43\n" \
"v_cvt_pk_bf16_f32 v38, v44, v45\n" \
"v_mfma_f32_16x16x32_bf16 a[112:115], a[228:231], v[116:119], a[112:115]\n" \
"ds_read_b128 a[224:227], v233 offset:4096\n" \
"ds_read_b128 a[228:231], v233 offset:12288\n" \
"v_exp_f32 v47, v47\n" \
"v_mfma_f32_16x16x32_bf16 a[116:119], a[232:235], v[112:115], a[116:119]\n" \
"v_exp_f32 v56, v56\n" \
"v_mfma_f32_16x16x32_bf16 a[116:119], a[236:239], v[116:119], a[116:119]\n" \
"v_exp_f32 v57, v57\n" \
"v_exp_f32 v58, v58\n" \
"v_cvt_pk_bf16_f32 v39, v46, v47\n" \
"v_exp_f32 v59, v59\n" \
"v_accvgpr_read_b32 v240, a53\n" \
"v_mfma_f32_16x16x32_bf16 a[84:87], a[232:235], v[96:99], a[84:87]\n" \
"ds_write_b128 v232, v[148:151] offset:5120\n" \
"v_exp_f32 v60, v60\n" \
"v_mfma_f32_16x16x32_bf16 a[84:87], a[236:239], v[100:103], a[84:87]\n" \
"ds_read_b128 a[232:235], v233 offset:5120\n" \
"ds_read_b128 a[236:239], v233 offset:13312\n" \
"v_cvt_pk_bf16_f32 v52, v56, v57\n" \
"v_mfma_f32_16x16x32_bf16 a[88:91], a[32:35], v[96:99], a[88:91]\n" \
"v_exp_f32 v61, v61\n" \
"v_mfma_f32_16x16x32_bf16 a[88:91], a[36:39], v[100:103], a[88:91]\n" \
"ds_read_b128 a[240:243], v233 offset:6144\n" \
"ds_read_b128 a[244:247], v233 offset:14336\n" \
"v_exp_f32 v62, v62\n" \
"v_cvt_pk_bf16_f32 v53, v58, v59\n" \
"v_mfma_f32_16x16x32_bf16 a[120:123], a[32:35], v[112:115], a[120:123]\n" \
"ds_read_b128 v[0:3], v233 offset:20480\n" \
"ds_read_b128 v[4:7], v233 offset:28672\n" \
"v_mfma_f32_16x16x32_bf16 a[120:123], a[36:39], v[116:119], a[120:123]\n" \
"ds_read_b128 v[8:11], v233 offset:21504\n" \
"ds_read_b128 v[12:15], v233 offset:29696\n" \
"v_exp_f32 v63, v63\n" \
"v_cvt_pk_bf16_f32 v54, v60, v61\n" \
"v_add_f32 v251, v251, v240\n" \
"v_mfma_f32_16x16x32_bf16 a[124:127], a[40:43], v[112:115], a[124:127]\n" \
"v_cvt_pk_bf16_f32 v55, v62, v63\n" \
"v_mfma_f32_16x16x32_bf16 a[124:127], a[44:47], v[116:119], a[124:127]\n" \
"ds_write_b128 v232, v[152:155] offset:6144\n" \
"ds_write_b64 v249, v[250:251]\n" \
"v_mfma_f32_16x16x32_bf16 a[92:95], a[40:43], v[96:99], a[92:95]\n" \
"v_exp_f32 v16, v16\n" \
"v_mfma_f32_16x16x32_bf16 a[92:95], a[44:47], v[100:103], a[92:95]\n" \
"s_waitcnt lgkmcnt(4)\n" \
"ds_read_b128 v[96:99], v233 offset:22528\n" \
"ds_read_b128 v[100:103], v233 offset:30720\n" \
"v_exp_f32 v17, v17\n" \
"v_xor_b32 v249, 0x200, v249\n" \
"v_accvgpr_read_b32 v200, a59\n" \
"v_mfma_f32_16x16x32_bf16 a[64:67], a[192:195], v[32:35], a[64:67]\n" \
"ds_read_b128 v[104:107], v233 offset:23552\n" \
"ds_read_b128 v[108:111], v233 offset:31744\n" \
"v_mfma_f32_16x16x32_bf16 a[64:67], a[196:199], v[36:39], a[64:67]\n" \
"v_exp_f32 v18, v18\n" \
"v_exp_f32 v19, v19\n" \
"v_accvgpr_read_b32 v202, a60\n" \
"v_accvgpr_read_b32 v204, a61\n" \
"v_xor_b32 v201, 64, v200\n" \
"v_mfma_f32_16x16x32_bf16 a[96:99], a[192:195], v[48:51], a[96:99]\n" \
"v_exp_f32 v20, v20\n" \
"v_mfma_f32_16x16x32_bf16 a[96:99], a[196:199], v[52:55], a[96:99]\n" \
"v_cvt_pk_bf16_f32 v16, v16, v17\n" \
"v_cvt_pk_bf16_f32 v17, v18, v19\n" \
"v_xor_b32 v203, 64, v202\n" \
"v_xor_b32 v222, 32, v204\n" \
"v_add_u32 v205, 0x10000, v200\n" \
"v_mfma_f32_16x16x32_bf16 a[100:103], a[200:203], v[48:51], a[100:103]\n" \
"v_exp_f32 v21, v21\n" \
"v_mfma_f32_16x16x32_bf16 a[100:103], a[204:207], v[52:55], a[100:103]\n" \
"ds_write_b128 v232, v[156:159] offset:7168\n" \
"v_exp_f32 v22, v22\n" \
"v_add_u32 v207, 0x10000, v202\n" \
"v_add_u32 v194, 139264, v204\n" \
"v_add_u32 v206, 0x10000, v201\n" \
"v_mfma_f32_16x16x32_bf16 a[68:71], a[200:203], v[32:35], a[68:71]\n" \
"v_exp_f32 v23, v23\n" \
"v_mfma_f32_16x16x32_bf16 a[68:71], a[204:207], v[36:39], a[68:71]\n" \
"s_mul_i32 s76, s80, 393216\n" \
"s_add_u32 s76, s76, s78\n" \
"v_cvt_pk_bf16_f32 v18, v20, v21\n" \
"v_exp_f32 v24, v24\n" \
"v_add_u32 v208, 0x10000, v203\n" \
"v_add_u32 v195, 139264, v222\n" \
"v_smfmac_f32_16x16x64_bf16 v[224:227], v[244:247], v[32:39], v243\n" \
"s_waitcnt vmcnt(0) lgkmcnt(0)\n" \
"s_barrier\n" \
"v_mfma_f32_16x16x32_bf16 a[72:75], a[208:211], v[32:35], a[72:75]\n" \
"s_add_u32 m0, s91, 86016\n" \
"ds_read_b64_tr_b16 a[32:33], v194 offset:1024\n" \
"ds_read_b64_tr_b16 a[34:35], v194 offset:4096\n" \
"v_mfma_f32_16x16x32_bf16 a[72:75], a[212:215], v[36:39], a[72:75]\n" \
"buffer_load_dwordx4 v197, s[48:51], s76 offen offset:0 lds\n" \
"v_exp_f32 v25, v25\n" \
"v_mfma_f32_16x16x32_bf16 a[104:107], a[208:211], v[48:51], a[104:107]\n" \
"s_and_b32 s15, %1, 7\n" \
"ds_read_b64_tr_b16 a[36:37], v194 offset:13312\n" \
"ds_read_b64_tr_b16 a[38:39], v194 offset:16384\n" \
"v_mfma_f32_16x16x32_bf16 a[104:107], a[212:215], v[52:55], a[104:107]\n" \
"s_add_u32 s77, s76, 24576\n" \
"ds_read_b64_tr_b16 a[40:41], v194 offset:0\n" \
"ds_read_b64_tr_b16 a[42:43], v194 offset:3072\n" \
"v_mfma_f32_16x16x32_bf16 a[108:111], a[216:219], v[48:51], a[108:111]\n" \
"buffer_load_dwordx4 v198, s[48:51], s76 offen offset:1024 lds\n" \
"v_mfma_f32_16x16x32_bf16 a[108:111], a[220:223], v[52:55], a[108:111]\n" \
"s_lshl3_add_u32 s14, s14, s15\n" \
"ds_read_b64_tr_b16 a[44:45], v194 offset:12288\n" \
"ds_read_b64_tr_b16 a[46:47], v194 offset:15360\n" \
"v_exp_f32 v26, v26\n" \
"v_cvt_pk_bf16_f32 v19, v22, v23\n" \
"v_mfma_f32_16x16x32_bf16 a[76:79], a[216:219], v[32:35], a[76:79]\n" \
"s_xor_b32 s80, s80, 0x8\n" \
"ds_read_b64_tr_b16 a[48:49], v195 offset:0\n" \
"ds_read_b64_tr_b16 a[50:51], v195 offset:3072\n" \
"v_mfma_f32_16x16x32_bf16 a[76:79], a[220:223], v[36:39], a[76:79]\n" \
"buffer_load_dwordx4 v199, s[48:51], s76 offen offset:2048 lds\n" \
"v_smfmac_f32_16x16x64_bf16 v[228:231], v[244:247], v[48:55], v243\n" \
"ds_read_b64_tr_b16 a[52:53], v195 offset:12288\n" \
"ds_read_b64_tr_b16 a[54:55], v195 offset:15360\n" \
"v_exp_f32 v27, v27\n" \
"v_mfma_f32_16x16x32_bf16 a[80:83], a[224:227], v[32:35], a[80:83]\n" \
"s_add_u32 m0, m0, 3072\n" \
"ds_read_b128 a[0:3], v207 offset:45056\n" \
"ds_read_b128 a[4:7], v208 offset:45056\n" \
"v_mfma_f32_16x16x32_bf16 a[80:83], a[228:231], v[36:39], a[80:83]\n" \
"buffer_load_dwordx4 v197, s[48:51], s77 offen offset:0 lds\n" \
"v_mfma_f32_16x16x32_bf16 a[112:115], a[224:227], v[48:51], a[112:115]\n" \
"ds_read_b128 a[8:11], v205 offset:45056\n" \
"ds_read_b128 a[12:15], v206 offset:45056\n" \
"v_mfma_f32_16x16x32_bf16 a[112:115], a[228:231], v[52:55], a[112:115]\n" \
"ds_read_b128 a[16:19], v205 offset:49152\n" \
"ds_read_b128 a[20:23], v206 offset:49152\n" \
"v_exp_f32 v28, v28\n" \
"v_cvt_pk_bf16_f32 v20, v24, v25\n" \
"v_mfma_f32_16x16x32_bf16 a[116:119], a[232:235], v[48:51], a[116:119]\n" \
"buffer_load_dwordx4 v198, s[48:51], s77 offen offset:1024 lds\n" \
"v_mfma_f32_16x16x32_bf16 a[116:119], a[236:239], v[52:55], a[116:119]\n" \
"ds_read_b128 a[24:27], v205 offset:53248\n" \
"ds_read_b128 a[28:31], v206 offset:53248\n" \
"v_mfma_f32_16x16x32_bf16 a[84:87], a[232:235], v[32:35], a[84:87]\n" \
"ds_read_b64 v[250:251], v249\n" \
"ds_read_b128 v[128:131], v248 offset:0\n" \
"v_mfma_f32_16x16x32_bf16 a[84:87], a[236:239], v[36:39], a[84:87]\n" \
"buffer_load_dwordx4 v199, s[48:51], s77 offen offset:2048 lds\n" \
"v_exp_f32 v29, v29\n" \
"v_mfma_f32_16x16x32_bf16 a[88:91], a[240:243], v[32:35], a[88:91]\n" \
"ds_read_b128 v[132:135], v248 offset:1024\n" \
"ds_read_b128 v[136:139], v248 offset:2048\n" \
"v_mfma_f32_16x16x32_bf16 a[88:91], a[244:247], v[36:39], a[88:91]\n" \
"s_add_u32 m0, s79, 65536\n" \
"ds_read_b128 v[140:143], v248 offset:3072\n" \
"ds_read_b128 v[144:147], v248 offset:4096\n" \
"v_exp_f32 v30, v30\n" \
"v_mfma_f32_16x16x32_bf16 a[120:123], a[240:243], v[48:51], a[120:123]\n" \
"buffer_load_dwordx4 v192, s[44:47], s73 offen offset:0 lds\n" \
"v_mfma_f32_16x16x32_bf16 a[120:123], a[244:247], v[52:55], a[120:123]\n" \
"ds_read_b128 v[148:151], v248 offset:5120\n" \
"ds_read_b128 v[152:155], v248 offset:6144\n" \
"v_cvt_pk_bf16_f32 v21, v26, v27\n" \
"v_mfma_f32_16x16x32_bf16 a[124:127], a[248:251], v[48:51], a[124:127]\n" \
"ds_read_b128 v[156:159], v248 offset:7168\n" \
"v_cvt_pk_bf16_f32 v22, v28, v29\n" \
"v_mfma_f32_16x16x32_bf16 a[124:127], a[252:255], v[52:55], a[124:127]\n" \
"buffer_load_dwordx4 v193, s[44:47], s73 offen offset:1024 lds\n" \
"v_exp_f32 v31, v31\n" \
"v_mfma_f32_16x16x32_bf16 a[92:95], a[248:251], v[32:35], a[92:95]\n" \
"v_cvt_pk_bf16_f32 v23, v30, v31\n" \
"v_mov_b32 v218, s60\n" \
"v_mfma_f32_16x16x32_bf16 a[92:95], a[252:255], v[36:39], a[92:95]\n" \
"v_mov_b32 v219, s14\n" \
"v_lshlrev_b32 v220, 28, s61\n" \
"v_mfma_f32_16x16x32_bf16 a[96:99], v[64:67], v[16:19], a[96:99]\n" \
"buffer_load_dwordx4 v192, s[44:47], s73 offen offset:2048 lds\n" \
"v_mfma_f32_16x16x32_bf16 a[96:99], v[68:71], v[20:23], a[96:99]\n" \
"s_lshl3_add_u32 s15, s15, 65593856\n" \
"v_mov_b32 v170, 0x3f80\n" \
"v_mov_b32 v160, 0\n" \
"v_mfma_f32_16x16x32_bf16 a[100:103], v[72:75], v[16:19], a[100:103]\n" \
"s_lshr_b32 s14, %1, 3\n" \
"s_cmp_eq_u32 s14, 3\n" \
"s_cselect_b32 s15, s15, 0x20000000\n" \
"v_mfma_f32_16x16x32_bf16 a[100:103], v[76:79], v[20:23], a[100:103]\n" \
"buffer_load_dwordx4 v193, s[44:47], s73 offen offset:3072 lds\n" \
"v_smfmac_f32_16x16x64_bf16 v[228:231], v[244:247], v[16:23], v243\n" \
"s_add_u32 m0, s89, 65536\n" \
"buffer_store_dwordx2 v[218:219], v220, s[56:59], s15 offen sc1\n" \
"v_mfma_f32_16x16x32_bf16 a[104:107], v[80:83], v[16:19], a[104:107]\n" \
"s_xor_b32 s73, s73, 0x400\n" \
"v_mov_b32 v161, 0\n" \
"v_mov_b32 v164, 0\n" \
"v_mfma_f32_16x16x32_bf16 a[104:107], v[84:87], v[20:23], a[104:107]\n" \
"buffer_load_dwordx4 v196, s[44:47], s75 offen lds\n" \
"v_mfma_f32_16x16x32_bf16 a[108:111], v[88:91], v[16:19], a[108:111]\n" \
"v_mov_b32 v165, 0\n" \
"v_lshrrev_b32 v168, 8, v234\n" \
"v_mfma_f32_16x16x32_bf16 a[108:111], v[92:95], v[20:23], a[108:111]\n" \
"v_bfe_u32 v169, v234, 6, 2\n" \
"v_cmp_eq_u32 s[16:17], v168, v169\n" \
"v_mfma_f32_16x16x32_bf16 a[112:115], v[0:3], v[16:19], a[112:115]\n" \
"s_xor_b32 s75, s75, 0x400\n" \
"v_and_b32 v168, 16, v234\n" \
"v_lshlrev_b32 v168, v168, v170\n" \
"v_mfma_f32_16x16x32_bf16 a[112:115], v[4:7], v[20:23], a[112:115]\n" \
"v_cndmask_b32 v168, 0, v168, s[16:17]\n" \
"v_bfe_i32 v169, v234, 5, 1\n" \
"v_mfma_f32_16x16x32_bf16 a[116:119], v[8:11], v[16:19], a[116:119]\n" \
"v_and_b32 v163, v168, v169\n" \
"v_xor_b32 v162, v168, v163\n" \
"v_mfma_f32_16x16x32_bf16 a[116:119], v[12:15], v[20:23], a[116:119]\n" \
"Lpg7367_%=:\n" \
"s_cbranch_execz Lcb0_%=\n" \
"v_mfma_f32_16x16x32_bf16 a[120:123], v[96:99], v[16:19], a[120:123]\n" \
"v_mfma_f32_16x16x32_bf16 a[120:123], v[100:103], v[20:23], a[120:123]\n" \
"v_mfma_f32_16x16x32_bf16 a[124:127], v[104:107], v[16:19], a[124:127]\n" \
"v_mfma_f32_16x16x32_bf16 a[124:127], v[108:111], v[20:23], a[124:127]\n" \
"s_waitcnt lgkmcnt(0)\n" \
"s_barrier\n" \
"v_mfma_f32_16x16x32_bf16 v[32:35], a[32:35], a[0:3], 0\n" \
"s_add_u32 m0, s79, 0\n" \
"ds_read_b128 a[192:195], v205 offset:57344\n" \
"v_mfma_f32_16x16x32_bf16 v[32:35], a[36:39], a[4:7], v[32:35]\n" \
"buffer_load_dwordx4 v192, s[44:47], s73 offen offset:0 lds\n" \
"v_mfma_f32_16x16x32_bf16 v[40:43], a[32:35], a[8:11], 0\n" \
"ds_read_b128 a[196:199], v206 offset:57344\n" \
"v_add_u32 v209, 86016, v204\n" \
"v_mfma_f32_16x16x32_bf16 v[40:43], a[36:39], a[12:15], v[40:43]\n" \
"buffer_load_dwordx4 v193, s[44:47], s73 offen offset:1024 lds\n" \
"v_mfma_f32_16x16x32_bf16 a[64:67], v[162:165], v[128:131], a[64:67]\n" \
"v_mfma_f32_16x16x32_bf16 a[68:71], v[160:163], v[128:131], a[68:71]\n" \
"v_mfma_f32_16x16x32_bf16 a[72:75], v[162:165], v[132:135], a[72:75]\n" \
"v_mfma_f32_16x16x32_bf16 a[76:79], v[160:163], v[132:135], a[76:79]\n" \
"v_add_f32 v248, v224, v225\n" \
"v_add_f32 v249, v228, v229\n" \
"v_mfma_f32_16x16x32_bf16 v[0:3], a[40:43], a[8:11], 0\n" \
"ds_read_b64_tr_b16 a[200:201], v195 offset:1024\n" \
"ds_read_b64_tr_b16 a[202:203], v195 offset:4096\n" \
"v_mfma_f32_16x16x32_bf16 v[0:3], a[44:47], a[12:15], v[0:3]\n" \
"buffer_load_dwordx4 v192, s[44:47], s73 offen offset:2048 lds\n" \
"v_mfma_f32_16x16x32_bf16 v[4:7], a[48:51], a[8:11], 0\n" \
"ds_read_b64_tr_b16 a[204:205], v195 offset:13312\n" \
"ds_read_b64_tr_b16 a[206:207], v195 offset:16384\n" \
"v_mfma_f32_16x16x32_bf16 v[4:7], a[52:55], a[12:15], v[4:7]\n" \
"buffer_load_dwordx4 v193, s[44:47], s73 offen offset:3072 lds\n" \
"v_mfma_f32_16x16x32_bf16 v[12:15], a[48:51], a[16:19], 0\n" \
"s_add_u32 m0, s89, 0\n" \
"s_xor_b32 s73, s73, 0x500\n" \
"ds_read_b64_tr_b16 a[208:209], v194 offset:2048\n" \
"ds_read_b64_tr_b16 a[210:211], v194 offset:5120\n" \
"v_add_f32 v248, v248, v250\n" \
"v_mfma_f32_16x16x32_bf16 v[12:15], a[52:55], a[20:23], v[12:15]\n" \
"buffer_load_dwordx4 v196, s[44:47], s75 offen lds\n" \
"v_mfma_f32_16x16x32_bf16 v[8:11], a[40:43], a[16:19], 0\n" \
"s_xor_b32 s75, s75, 0x500\n" \
"s_mul_i32 s76, s80, 393216\n" \
"s_add_u32 s76, s76, s78\n" \
"s_add_u32 m0, s91, 20480\n" \
"ds_read_b64_tr_b16 a[212:213], v194 offset:14336\n" \
"ds_read_b64_tr_b16 a[214:215], v194 offset:17408\n" \
"v_mfma_f32_16x16x32_bf16 v[8:11], a[44:47], a[20:23], v[8:11]\n" \
"buffer_load_dwordx4 v197, s[48:51], s76 offen offset:0 lds\n" \
"v_mfma_f32_16x16x32_bf16 v[48:51], a[32:35], a[16:19], 0\n" \
"s_add_u32 s77, s76, 24576\n" \
"ds_read_b64_tr_b16 a[216:217], v195 offset:2048\n" \
"v_add_u32 v210, 86016, v222\n" \
"v_mfma_f32_16x16x32_bf16 v[48:51], a[36:39], a[20:23], v[48:51]\n" \
"buffer_load_dwordx4 v198, s[48:51], s76 offen offset:1024 lds\n" \
"v_mfma_f32_16x16x32_bf16 v[56:59], a[32:35], a[24:27], 0\n" \
"s_xor_b32 s80, s80, 0xa\n" \
"ds_read_b64_tr_b16 a[218:219], v195 offset:5120\n" \
"v_add_f32 v249, v249, v251\n" \
"v_mfma_f32_16x16x32_bf16 v[56:59], a[36:39], a[28:31], v[56:59]\n" \
"buffer_load_dwordx4 v199, s[48:51], s76 offen offset:2048 lds\n" \
"v_mfma_f32_16x16x32_bf16 v[16:19], a[40:43], a[24:27], 0\n" \
"s_add_u32 m0, m0, 3072\n" \
"ds_read_b64_tr_b16 a[220:221], v195 offset:14336\n" \
"v_mfma_f32_16x16x32_bf16 v[16:19], a[44:47], a[28:31], v[16:19]\n" \
"buffer_load_dwordx4 v197, s[48:51], s77 offen offset:0 lds\n" \
"v_mfma_f32_16x16x32_bf16 v[20:23], a[48:51], a[24:27], 0\n" \
"s_waitcnt lgkmcnt(2)\n" \
"ds_read_b64_tr_b16 a[222:223], v195 offset:17408\n" \
"v_mfma_f32_16x16x32_bf16 v[20:23], a[52:55], a[28:31], v[20:23]\n" \
"buffer_load_dwordx4 v198, s[48:51], s77 offen offset:1024 lds\n" \
"v_mfma_f32_16x16x32_bf16 v[28:31], a[48:51], a[192:195], 0\n" \
"v_rcp_f32 v232, v248\n" \
"v_mfma_f32_16x16x32_bf16 v[28:31], a[52:55], a[196:199], v[28:31]\n" \
"buffer_load_dwordx4 v199, s[48:51], s77 offen offset:2048 lds\n" \
"v_rcp_f32 v233, v249\n" \
"v_mfma_f32_16x16x32_bf16 v[24:27], a[40:43], a[192:195], 0\n" \
"v_add_u32 v240, 0x1000, v236\n" \
"v_add_f32 v224, v248, v249\n" \
"v_mfma_f32_16x16x32_bf16 v[24:27], a[44:47], a[196:199], v[24:27]\n" \
"v_mul_f32 v224, 0x43800000, v224\n" \
"v_add_f32 v248, v232, v233\n" \
"v_mfma_f32_16x16x32_bf16 v[64:67], a[32:35], a[192:195], 0\n" \
"v_fmac_f32 v224, 0x5f800000, v248\n" \
"v_mov_b32 v225, 0\n" \
"v_mfma_f32_16x16x32_bf16 v[64:67], a[36:39], a[196:199], v[64:67]\n" \
"s_waitcnt vmcnt(11) lgkmcnt(0)\n" \
"s_barrier\n" \
"v_mfma_f32_16x16x32_bf16 v[68:71], a[200:203], a[192:195], 0\n" \
"ds_read_b64_tr_b16 a[32:33], v209 offset:1024\n" \
"ds_read_b64_tr_b16 a[34:35], v209 offset:4096\n" \
"v_mfma_f32_16x16x32_bf16 v[68:71], a[204:207], a[196:199], v[68:71]\n" \
"ds_read_b64_tr_b16 a[36:37], v209 offset:13312\n" \
"ds_read_b64_tr_b16 a[38:39], v209 offset:16384\n" \
"v_mfma_f32_16x16x32_bf16 v[60:63], a[200:203], a[24:27], 0\n" \
"s_add_u32 m0, s79, 110592\n" \
"ds_read_b64_tr_b16 a[40:41], v209 offset:0\n" \
"v_mov_b32 v226, 0\n" \
"v_mfma_f32_16x16x32_bf16 v[60:63], a[204:207], a[28:31], v[60:63]\n" \
"buffer_load_dwordx4 v192, s[44:47], s73 offen offset:0 lds\n" \
"v_mov_b32 v227, 0\n" \
"v_mfma_f32_16x16x32_bf16 v[52:55], a[200:203], a[16:19], 0\n" \
"ds_read_b64_tr_b16 a[42:43], v209 offset:3072\n" \
"v_accvgpr_read_b32 v112, a64\n" \
"v_mfma_f32_16x16x32_bf16 v[52:55], a[204:207], a[20:23], v[52:55]\n" \
"ds_read_b64_tr_b16 a[44:45], v209 offset:12288\n" \
"v_accvgpr_read_b32 v113, a65\n" \
"v_mfma_f32_16x16x32_bf16 v[44:47], a[200:203], a[8:11], 0\n" \
"ds_read_b64_tr_b16 a[46:47], v209 offset:15360\n" \
"v_accvgpr_read_b32 v114, a66\n" \
"v_mfma_f32_16x16x32_bf16 v[44:47], a[204:207], a[12:15], v[44:47]\n" \
"buffer_load_dwordx4 v193, s[44:47], s73 offen offset:1024 lds\n" \
"v_mfma_f32_16x16x32_bf16 v[72:75], a[0:3], a[208:211], 0\n" \
"ds_read_b64_tr_b16 a[48:49], v210 offset:0\n" \
"v_accvgpr_read_b32 v115, a67\n" \
"v_mfma_f32_16x16x32_bf16 v[72:75], a[4:7], a[212:215], v[72:75]\n" \
"ds_read_b128 a[0:3], v207 offset:0\n" \
"v_accvgpr_read_b32 v116, a68\n" \
"v_accvgpr_read_b32 v117, a69\n" \
"v_mfma_f32_16x16x32_bf16 v[80:83], a[8:11], a[208:211], 0\n" \
"ds_read_b128 a[4:7], v208 offset:0\n" \
"v_accvgpr_read_b32 v118, a70\n" \
"v_mfma_f32_16x16x32_bf16 v[80:83], a[12:15], a[212:215], v[80:83]\n" \
"buffer_load_dwordx4 v192, s[44:47], s73 offen offset:2048 lds\n" \
"v_mfma_f32_16x16x32_bf16 v[84:87], a[8:11], a[216:219], 0\n" \
"ds_read_b64_tr_b16 a[50:51], v210 offset:3072\n" \
"v_accvgpr_read_b32 v119, a71\n" \
"v_mfma_f32_16x16x32_bf16 v[84:87], a[12:15], a[220:223], v[84:87]\n" \
"ds_read_b128 a[8:11], v205 offset:0\n" \
"v_mfma_f32_16x16x32_bf16 a[80:83], v[162:165], v[136:139], a[80:83]\n" \
"v_mul_f32 v112, v112, v232\n" \
"v_mul_f32 v113, v113, v232\n" \
"v_mfma_f32_16x16x32_bf16 v[92:95], a[16:19], a[216:219], 0\n" \
"ds_read_b128 a[12:15], v206 offset:0\n" \
"v_mul_f32 v114, v114, v232\n" \
"v_mfma_f32_16x16x32_bf16 v[92:95], a[20:23], a[220:223], v[92:95]\n" \
"buffer_load_dwordx4 v193, s[44:47], s73 offen offset:3072 lds\n" \
"v_mul_f32 v115, v115, v232\n" \
"v_mfma_f32_16x16x32_bf16 a[84:87], v[160:163], v[136:139], a[84:87]\n" \
"v_mul_f32 v116, v116, v232\n" \
"v_mul_f32 v117, v117, v232\n" \
"v_mfma_f32_16x16x32_bf16 v[88:91], a[16:19], a[208:211], 0\n" \
"ds_read_b64_tr_b16 a[52:53], v210 offset:12288\n" \
"v_mul_f32 v118, v118, v232\n" \
"v_mfma_f32_16x16x32_bf16 v[88:91], a[20:23], a[212:215], v[88:91]\n" \
"s_add_u32 m0, s89, 110592\n" \
"ds_read_b128 a[16:19], v205 offset:4096\n" \
"v_mul_f32 v119, v119, v232\n" \
"v_mfma_f32_16x16x32_bf16 v[96:99], a[24:27], a[208:211], 0\n" \
"s_xor_b32 s73, s73, 0x180\n" \
"ds_read_b128 a[20:23], v206 offset:4096\n" \
"v_cvt_pk_bf16_f32 v120, v112, v113\n" \
"v_mfma_f32_16x16x32_bf16 v[96:99], a[28:31], a[212:215], v[96:99]\n" \
"buffer_load_dwordx4 v196, s[44:47], s75 offen lds\n" \
"v_mfma_f32_16x16x32_bf16 v[100:103], a[24:27], a[216:219], 0\n" \
"s_xor_b32 s75, s75, 0x180\n" \
"s_mul_i32 s76, s80, 393216\n" \
"ds_read_b64_tr_b16 a[54:55], v210 offset:15360\n" \
"v_mfma_f32_16x16x32_bf16 v[100:103], a[28:31], a[220:223], v[100:103]\n" \
"s_add_u32 s76, s76, s78\n" \
"ds_read_b128 a[24:27], v205 offset:8192\n" \
"v_cvt_pk_bf16_f32 v121, v114, v115\n" \
"v_mfma_f32_16x16x32_bf16 v[108:111], a[192:195], a[216:219], 0\n" \
"s_add_u32 m0, s91, 139264\n" \
"ds_read_b128 a[28:31], v206 offset:8192\n" \
"v_cvt_pk_bf16_f32 v122, v116, v117\n" \
"v_mfma_f32_16x16x32_bf16 v[108:111], a[196:199], a[220:223], v[108:111]\n" \
"buffer_load_dwordx4 v197, s[48:51], s76 offen offset:0 lds\n" \
"v_cvt_pk_bf16_f32 v123, v118, v119\n" \
"v_mfma_f32_16x16x32_bf16 v[104:107], a[192:195], a[208:211], 0\n" \
"s_waitcnt lgkmcnt(3)\n" \
"v_mfma_f32_16x16x32_bf16 v[104:107], a[196:199], a[212:215], v[104:107]\n" \
"buffer_store_dwordx4 v[120:123], v236, s[56:59], s88 offen offset:0 sc1\n" \
"v_mfma_f32_16x16x32_bf16 v[32:35], a[32:35], a[0:3], v[32:35]\n" \
"s_add_u32 s77, s76, 24576\n" \
"ds_read_b128 a[192:195], v205 offset:12288\n" \
"v_mfma_f32_16x16x32_bf16 v[32:35], a[36:39], a[4:7], v[32:35]\n" \
"buffer_load_dwordx4 v198, s[48:51], s76 offen offset:1024 lds\n" \
"v_mfma_f32_16x16x32_bf16 v[224:227], v[244:247], v[120:123], v[224:227]\n" \
"v_accvgpr_read_b32 v112, a72\n" \
"v_accvgpr_read_b32 v113, a73\n" \
"v_mfma_f32_16x16x32_bf16 v[40:43], a[32:35], a[8:11], v[40:43]\n" \
"ds_read_b128 a[196:199], v206 offset:12288\n" \
"v_accvgpr_read_b32 v114, a74\n" \
"v_mfma_f32_16x16x32_bf16 v[40:43], a[36:39], a[12:15], v[40:43]\n" \
"s_waitcnt lgkmcnt(2)\n" \
"ds_read_b64_tr_b16 a[200:201], v210 offset:1024\n" \
"v_accvgpr_read_b32 v115, a75\n" \
"v_accvgpr_read_b32 v116, a76\n" \
"v_mfma_f32_16x16x32_bf16 v[0:3], a[40:43], a[8:11], v[0:3]\n" \
"s_xor_b32 s80, s80, 0x3\n" \
"ds_read_b64_tr_b16 a[202:203], v210 offset:4096\n" \
"v_accvgpr_read_b32 v117, a77\n" \
"v_mfma_f32_16x16x32_bf16 v[0:3], a[44:47], a[12:15], v[0:3]\n" \
"buffer_load_dwordx4 v199, s[48:51], s76 offen offset:2048 lds\n" \
"v_mfma_f32_16x16x32_bf16 v[4:7], a[48:51], a[8:11], v[4:7]\n" \
"ds_read_b64_tr_b16 a[204:205], v210 offset:13312\n" \
"v_accvgpr_read_b32 v118, a78\n" \
"v_mfma_f32_16x16x32_bf16 v[4:7], a[52:55], a[12:15], v[4:7]\n" \
"ds_read_b64_tr_b16 a[206:207], v210 offset:16384\n" \
"v_accvgpr_read_b32 v119, a79\n" \
"v_mfma_f32_16x16x32_bf16 a[88:91], v[162:165], v[140:143], a[88:91]\n" \
"v_mul_f32 v112, v112, v232\n" \
"v_mul_f32 v113, v113, v232\n" \
"v_mfma_f32_16x16x32_bf16 v[12:15], a[48:51], a[16:19], v[12:15]\n" \
"s_add_u32 m0, m0, 3072\n" \
"ds_read_b64_tr_b16 a[208:209], v209 offset:2048\n" \
"v_mul_f32 v114, v114, v232\n" \
"v_mfma_f32_16x16x32_bf16 v[12:15], a[52:55], a[20:23], v[12:15]\n" \
"buffer_load_dwordx4 v197, s[48:51], s77 offen offset:0 lds\n" \
"v_mul_f32 v115, v115, v232\n" \
"v_mfma_f32_16x16x32_bf16 a[92:95], v[160:163], v[140:143], a[92:95]\n" \
"v_mul_f32 v116, v116, v232\n" \
"v_mul_f32 v117, v117, v232\n" \
"v_mfma_f32_16x16x32_bf16 v[8:11], a[40:43], a[16:19], v[8:11]\n" \
"ds_read_b64_tr_b16 a[210:211], v209 offset:5120\n" \
"v_mul_f32 v118, v118, v232\n" \
"v_mfma_f32_16x16x32_bf16 v[8:11], a[44:47], a[20:23], v[8:11]\n" \
"ds_read_b64_tr_b16 a[212:213], v209 offset:14336\n" \
"v_mul_f32 v119, v119, v232\n" \
"v_mfma_f32_16x16x32_bf16 v[48:51], a[32:35], a[16:19], v[48:51]\n" \
"ds_read_b64_tr_b16 a[214:215], v209 offset:17408\n" \
"v_cvt_pk_bf16_f32 v124, v112, v113\n" \
"v_mfma_f32_16x16x32_bf16 v[48:51], a[36:39], a[20:23], v[48:51]\n" \
"buffer_load_dwordx4 v198, s[48:51], s77 offen offset:1024 lds\n" \
"v_mfma_f32_16x16x32_bf16 v[56:59], a[32:35], a[24:27], v[56:59]\n" \
"ds_read_b64_tr_b16 a[216:217], v210 offset:2048\n" \
"v_cvt_pk_bf16_f32 v125, v114, v115\n" \
"v_mfma_f32_16x16x32_bf16 v[56:59], a[36:39], a[28:31], v[56:59]\n" \
"ds_read_b64_tr_b16 a[218:219], v210 offset:5120\n" \
"v_cvt_pk_bf16_f32 v126, v116, v117\n" \
"v_mfma_f32_16x16x32_bf16 v[16:19], a[40:43], a[24:27], v[16:19]\n" \
"ds_read_b64_tr_b16 a[220:221], v210 offset:14336\n" \
"v_cvt_pk_bf16_f32 v127, v118, v119\n" \
"v_mfma_f32_16x16x32_bf16 v[16:19], a[44:47], a[28:31], v[16:19]\n" \
"buffer_load_dwordx4 v199, s[48:51], s77 offen offset:2048 lds\n" \
"v_mfma_f32_16x16x32_bf16 v[20:23], a[48:51], a[24:27], v[20:23]\n" \
"s_waitcnt lgkmcnt(3)\n" \
"ds_read_b64_tr_b16 a[222:223], v210 offset:17408\n" \
"v_mfma_f32_16x16x32_bf16 v[20:23], a[52:55], a[28:31], v[20:23]\n" \
"buffer_store_dwordx4 v[124:127], v236, s[56:59], s88 offen offset:2048 sc1\n" \
"v_mfma_f32_16x16x32_bf16 v[28:31], a[48:51], a[192:195], v[28:31]\n" \
"v_mfma_f32_16x16x32_bf16 v[28:31], a[52:55], a[196:199], v[28:31]\n" \
"v_mfma_f32_16x16x32_bf16 v[224:227], v[244:247], v[124:127], v[224:227]\n" \
"v_accvgpr_read_b32 v112, a80\n" \
"v_accvgpr_read_b32 v113, a81\n" \
"v_accvgpr_read_b32 v114, a82\n" \
"v_mfma_f32_16x16x32_bf16 v[24:27], a[40:43], a[192:195], v[24:27]\n" \
"v_accvgpr_read_b32 v115, a83\n" \
"v_accvgpr_read_b32 v116, a84\n" \
"v_mfma_f32_16x16x32_bf16 v[24:27], a[44:47], a[196:199], v[24:27]\n" \
"v_accvgpr_read_b32 v117, a85\n" \
"v_accvgpr_read_b32 v118, a86\n" \
"v_accvgpr_read_b32 v119, a87\n" \
"v_mfma_f32_16x16x32_bf16 a[96:99], v[162:165], v[144:147], a[96:99]\n" \
"v_mul_f32 v112, v112, v232\n" \
"v_mul_f32 v113, v113, v232\n" \
"v_mfma_f32_16x16x32_bf16 v[64:67], a[32:35], a[192:195], v[64:67]\n" \
"v_mul_f32 v114, v114, v232\n" \
"v_mul_f32 v115, v115, v232\n" \
"v_mfma_f32_16x16x32_bf16 v[64:67], a[36:39], a[196:199], v[64:67]\n" \
"v_mfma_f32_16x16x32_bf16 a[100:103], v[160:163], v[144:147], a[100:103]\n" \
"s_waitcnt vmcnt(13) lgkmcnt(0)\n" \
"s_barrier\n" \
"v_mfma_f32_16x16x32_bf16 v[68:71], a[200:203], a[192:195], v[68:71]\n" \
"ds_read_b64_tr_b16 a[32:33], v204 offset:21504\n" \
"ds_read_b64_tr_b16 a[34:35], v204 offset:24576\n" \
"v_mfma_f32_16x16x32_bf16 v[68:71], a[204:207], a[196:199], v[68:71]\n" \
"ds_read_b64_tr_b16 a[36:37], v204 offset:33792\n" \
"ds_read_b64_tr_b16 a[38:39], v204 offset:36864\n" \
"v_mfma_f32_16x16x32_bf16 v[60:63], a[200:203], a[24:27], v[60:63]\n" \
"s_add_u32 m0, s79, 65536\n" \
"ds_read_b64_tr_b16 a[40:41], v204 offset:20480\n" \
"v_mul_f32 v116, v116, v232\n" \
"v_mfma_f32_16x16x32_bf16 v[60:63], a[204:207], a[28:31], v[60:63]\n" \
"buffer_load_dwordx4 v192, s[44:47], s73 offen offset:0 lds\n" \
"v_mul_f32 v117, v117, v232\n" \
"v_mfma_f32_16x16x32_bf16 v[52:55], a[200:203], a[16:19], v[52:55]\n" \
"ds_read_b64_tr_b16 a[42:43], v204 offset:23552\n" \
"v_mul_f32 v118, v118, v232\n" \
"v_mfma_f32_16x16x32_bf16 v[52:55], a[204:207], a[20:23], v[52:55]\n" \
"ds_read_b64_tr_b16 a[44:45], v204 offset:32768\n" \
"v_mul_f32 v119, v119, v232\n" \
"v_mfma_f32_16x16x32_bf16 v[44:47], a[200:203], a[8:11], v[44:47]\n" \
"ds_read_b64_tr_b16 a[46:47], v204 offset:35840\n" \
"v_cvt_pk_bf16_f32 v120, v112, v113\n" \
"v_mfma_f32_16x16x32_bf16 v[44:47], a[204:207], a[12:15], v[44:47]\n" \
"buffer_load_dwordx4 v193, s[44:47], s73 offen offset:1024 lds\n" \
"v_mfma_f32_16x16x32_bf16 v[72:75], a[0:3], a[208:211], v[72:75]\n" \
"ds_read_b64_tr_b16 a[48:49], v222 offset:20480\n" \
"v_cvt_pk_bf16_f32 v121, v114, v115\n" \
"v_mfma_f32_16x16x32_bf16 v[72:75], a[4:7], a[212:215], v[72:75]\n" \
"ds_read_b128 a[0:3], v202 offset:0\n" \
"v_cvt_pk_bf16_f32 v122, v116, v117\n" \
"v_mfma_f32_16x16x32_bf16 v[80:83], a[8:11], a[208:211], v[80:83]\n" \
"ds_read_b128 a[4:7], v203 offset:0\n" \
"v_cvt_pk_bf16_f32 v123, v118, v119\n" \
"v_mfma_f32_16x16x32_bf16 v[80:83], a[12:15], a[212:215], v[80:83]\n" \
"buffer_load_dwordx4 v192, s[44:47], s73 offen offset:2048 lds\n" \
"v_mfma_f32_16x16x32_bf16 v[84:87], a[8:11], a[216:219], v[84:87]\n" \
"ds_read_b64_tr_b16 a[50:51], v222 offset:23552\n" \
"v_mfma_f32_16x16x32_bf16 v[84:87], a[12:15], a[220:223], v[84:87]\n" \
"ds_read_b128 a[8:11], v200 offset:0\n" \
"buffer_store_dwordx4 v[120:123], v240, s[56:59], s88 offen offset:0 sc1\n" \
"v_mfma_f32_16x16x32_bf16 v[92:95], a[16:19], a[216:219], v[92:95]\n" \
"ds_read_b128 a[12:15], v201 offset:0\n" \
"v_mfma_f32_16x16x32_bf16 v[92:95], a[20:23], a[220:223], v[92:95]\n" \
"buffer_load_dwordx4 v193, s[44:47], s73 offen offset:3072 lds\n" \
"v_mfma_f32_16x16x32_bf16 v[224:227], v[244:247], v[120:123], v[224:227]\n" \
"v_accvgpr_read_b32 v112, a88\n" \
"v_accvgpr_read_b32 v113, a89\n" \
"v_accvgpr_read_b32 v114, a90\n" \
"v_mfma_f32_16x16x32_bf16 v[88:91], a[16:19], a[208:211], v[88:91]\n" \
"ds_read_b64_tr_b16 a[52:53], v222 offset:32768\n" \
"v_accvgpr_read_b32 v115, a91\n" \
"v_mfma_f32_16x16x32_bf16 v[88:91], a[20:23], a[212:215], v[88:91]\n" \
"s_add_u32 m0, s89, 65536\n" \
"ds_read_b128 a[16:19], v200 offset:4096\n" \
"v_accvgpr_read_b32 v116, a92\n" \
"v_accvgpr_read_b32 v117, a93\n" \
"v_mfma_f32_16x16x32_bf16 v[96:99], a[24:27], a[208:211], v[96:99]\n" \
"s_xor_b32 s73, s73, 0x300\n" \
"ds_read_b128 a[20:23], v201 offset:4096\n" \
"v_accvgpr_read_b32 v118, a94\n" \
"v_mfma_f32_16x16x32_bf16 v[96:99], a[28:31], a[212:215], v[96:99]\n" \
"buffer_load_dwordx4 v196, s[44:47], s75 offen lds\n" \
"v_mfma_f32_16x16x32_bf16 v[100:103], a[24:27], a[216:219], v[100:103]\n" \
"s_xor_b32 s75, s75, 0x300\n" \
"s_mul_i32 s76, s80, 393216\n" \
"ds_read_b64_tr_b16 a[54:55], v222 offset:35840\n" \
"v_mfma_f32_16x16x32_bf16 v[100:103], a[28:31], a[220:223], v[100:103]\n" \
"s_add_u32 s76, s76, s78\n" \
"ds_read_b128 a[24:27], v200 offset:8192\n" \
"v_accvgpr_read_b32 v119, a95\n" \
"v_mfma_f32_16x16x32_bf16 a[104:107], v[162:165], v[148:151], a[104:107]\n" \
"v_mul_f32 v112, v112, v232\n" \
"v_mul_f32 v113, v113, v232\n" \
"v_mfma_f32_16x16x32_bf16 v[108:111], a[192:195], a[216:219], v[108:111]\n" \
"s_add_u32 m0, s91, 86016\n" \
"ds_read_b128 a[28:31], v201 offset:8192\n" \
"v_mul_f32 v114, v114, v232\n" \
"v_mfma_f32_16x16x32_bf16 v[108:111], a[196:199], a[220:223], v[108:111]\n" \
"buffer_load_dwordx4 v197, s[48:51], s76 offen offset:0 lds\n" \
"v_mul_f32 v115, v115, v232\n" \
"v_mfma_f32_16x16x32_bf16 a[108:111], v[160:163], v[148:151], a[108:111]\n" \
"v_mul_f32 v116, v116, v232\n" \
"v_mul_f32 v117, v117, v232\n" \
"v_mfma_f32_16x16x32_bf16 v[104:107], a[192:195], a[208:211], v[104:107]\n" \
"v_mul_f32 v118, v118, v232\n" \
"v_mul_f32 v119, v119, v232\n" \
"v_mfma_f32_16x16x32_bf16 v[104:107], a[196:199], a[212:215], v[104:107]\n" \
"s_waitcnt lgkmcnt(2)\n" \
"v_cvt_pk_bf16_f32 v124, v112, v113\n" \
"v_cvt_pk_bf16_f32 v125, v114, v115\n" \
"v_mfma_f32_16x16x32_bf16 v[32:35], a[32:35], a[0:3], v[32:35]\n" \
"s_add_u32 s77, s76, 24576\n" \
"ds_read_b128 a[192:195], v200 offset:12288\n" \
"v_cvt_pk_bf16_f32 v126, v116, v117\n" \
"v_mfma_f32_16x16x32_bf16 v[32:35], a[36:39], a[4:7], v[32:35]\n" \
"buffer_load_dwordx4 v198, s[48:51], s76 offen offset:1024 lds\n" \
"v_mfma_f32_16x16x32_bf16 v[40:43], a[32:35], a[8:11], v[40:43]\n" \
"ds_read_b128 a[196:199], v201 offset:12288\n" \
"v_cvt_pk_bf16_f32 v127, v118, v119\n" \
"v_mfma_f32_16x16x32_bf16 v[40:43], a[36:39], a[12:15], v[40:43]\n" \
"ds_read_b64_tr_b16 a[200:201], v222 offset:21504\n" \
"buffer_store_dwordx4 v[124:127], v240, s[56:59], s88 offen offset:2048 sc1\n" \
"v_mfma_f32_16x16x32_bf16 v[0:3], a[40:43], a[8:11], v[0:3]\n" \
"s_xor_b32 s80, s80, 0x6\n" \
"ds_read_b64_tr_b16 a[202:203], v222 offset:24576\n" \
"v_mfma_f32_16x16x32_bf16 v[0:3], a[44:47], a[12:15], v[0:3]\n" \
"buffer_load_dwordx4 v199, s[48:51], s76 offen offset:2048 lds\n" \
"v_mfma_f32_16x16x32_bf16 v[224:227], v[244:247], v[124:127], v[224:227]\n" \
"s_add_u32 s82, s88, 532480\n" \
"v_accvgpr_read_b32 v112, a96\n" \
"v_accvgpr_read_b32 v113, a97\n" \
"v_accvgpr_read_b32 v114, a98\n" \
"v_mfma_f32_16x16x32_bf16 v[4:7], a[48:51], a[8:11], v[4:7]\n" \
"ds_read_b64_tr_b16 a[204:205], v222 offset:33792\n" \
"v_accvgpr_read_b32 v115, a99\n" \
"v_mfma_f32_16x16x32_bf16 v[4:7], a[52:55], a[12:15], v[4:7]\n" \
"ds_read_b64_tr_b16 a[206:207], v222 offset:36864\n" \
"v_accvgpr_read_b32 v116, a100\n" \
"v_mfma_f32_16x16x32_bf16 v[12:15], a[48:51], a[16:19], v[12:15]\n" \
"s_add_u32 m0, m0, 3072\n" \
"ds_read_b64_tr_b16 a[208:209], v204 offset:22528\n" \
"v_accvgpr_read_b32 v117, a101\n" \
"v_mfma_f32_16x16x32_bf16 v[12:15], a[52:55], a[20:23], v[12:15]\n" \
"buffer_load_dwordx4 v197, s[48:51], s77 offen offset:0 lds\n" \
"v_mfma_f32_16x16x32_bf16 v[8:11], a[40:43], a[16:19], v[8:11]\n" \
"ds_read_b64_tr_b16 a[210:211], v204 offset:25600\n" \
"v_accvgpr_read_b32 v118, a102\n" \
"v_mfma_f32_16x16x32_bf16 v[8:11], a[44:47], a[20:23], v[8:11]\n" \
"ds_read_b64_tr_b16 a[212:213], v204 offset:34816\n" \
"v_accvgpr_read_b32 v119, a103\n" \
"v_mfma_f32_16x16x32_bf16 a[112:115], v[162:165], v[152:155], a[112:115]\n" \
"v_mul_f32 v112, v112, v233\n" \
"v_mul_f32 v113, v113, v233\n" \
"v_mul_f32 v114, v114, v233\n" \
"v_mfma_f32_16x16x32_bf16 v[48:51], a[32:35], a[16:19], v[48:51]\n" \
"s_waitcnt lgkmcnt(3)\n" \
"ds_read_b64_tr_b16 a[214:215], v204 offset:37888\n" \
"v_mul_f32 v115, v115, v233\n" \
"v_mfma_f32_16x16x32_bf16 v[48:51], a[36:39], a[20:23], v[48:51]\n" \
"buffer_load_dwordx4 v198, s[48:51], s77 offen offset:1024 lds\n" \
"v_mfma_f32_16x16x32_bf16 a[116:119], v[160:163], v[152:155], a[116:119]\n" \
"v_mul_f32 v116, v116, v233\n" \
"v_mul_f32 v117, v117, v233\n" \
"v_mfma_f32_16x16x32_bf16 v[56:59], a[32:35], a[24:27], v[56:59]\n" \
"ds_read_b64_tr_b16 a[216:217], v222 offset:22528\n" \
"v_mul_f32 v118, v118, v233\n" \
"v_mfma_f32_16x16x32_bf16 v[56:59], a[36:39], a[28:31], v[56:59]\n" \
"ds_read_b64_tr_b16 a[218:219], v222 offset:25600\n" \
"v_mul_f32 v119, v119, v233\n" \
"v_mfma_f32_16x16x32_bf16 v[16:19], a[40:43], a[24:27], v[16:19]\n" \
"ds_read_b64_tr_b16 a[220:221], v222 offset:34816\n" \
"v_cvt_pk_bf16_f32 v120, v112, v113\n" \
"v_mfma_f32_16x16x32_bf16 v[16:19], a[44:47], a[28:31], v[16:19]\n" \
"buffer_load_dwordx4 v199, s[48:51], s77 offen offset:2048 lds\n" \
"v_cvt_pk_bf16_f32 v121, v114, v115\n" \
"v_mfma_f32_16x16x32_bf16 v[20:23], a[48:51], a[24:27], v[20:23]\n" \
"ds_read_b64_tr_b16 a[222:223], v222 offset:37888\n" \
"v_cvt_pk_bf16_f32 v122, v116, v117\n" \
"v_mfma_f32_16x16x32_bf16 v[20:23], a[52:55], a[28:31], v[20:23]\n" \
"v_cvt_pk_bf16_f32 v123, v118, v119\n" \
"v_mfma_f32_16x16x32_bf16 v[28:31], a[48:51], a[192:195], v[28:31]\n" \
"v_mfma_f32_16x16x32_bf16 v[28:31], a[52:55], a[196:199], v[28:31]\n" \
"buffer_store_dwordx4 v[120:123], v236, s[56:59], s82 offen offset:0 sc1\n" \
"v_mfma_f32_16x16x32_bf16 v[24:27], a[40:43], a[192:195], v[24:27]\n" \
"v_mfma_f32_16x16x32_bf16 v[24:27], a[44:47], a[196:199], v[24:27]\n" \
"v_mfma_f32_16x16x32_bf16 v[224:227], v[244:247], v[120:123], v[224:227]\n" \
"v_accvgpr_read_b32 v112, a104\n" \
"v_accvgpr_read_b32 v113, a105\n" \
"v_mfma_f32_16x16x32_bf16 v[64:67], a[32:35], a[192:195], v[64:67]\n" \
"v_accvgpr_read_b32 v114, a106\n" \
"v_accvgpr_read_b32 v115, a107\n" \
"v_mfma_f32_16x16x32_bf16 v[64:67], a[36:39], a[196:199], v[64:67]\n" \
"v_accvgpr_read_b32 v116, a108\n" \
"s_waitcnt vmcnt(15) lgkmcnt(0)\n" \
"s_barrier\n" \
"v_mfma_f32_16x16x32_bf16 v[68:71], a[200:203], a[192:195], v[68:71]\n" \
"ds_read_b64_tr_b16 a[32:33], v194 offset:1024\n" \
"ds_read_b64_tr_b16 a[34:35], v194 offset:4096\n" \
"v_mfma_f32_16x16x32_bf16 v[68:71], a[204:207], a[196:199], v[68:71]\n" \
"ds_read_b64_tr_b16 a[36:37], v194 offset:13312\n" \
"ds_read_b64_tr_b16 a[38:39], v194 offset:16384\n" \
"v_mfma_f32_16x16x32_bf16 v[60:63], a[200:203], a[24:27], v[60:63]\n" \
"s_add_u32 m0, s79, 0\n" \
"ds_read_b64_tr_b16 a[40:41], v194 offset:0\n" \
"v_accvgpr_read_b32 v117, a109\n" \
"v_mfma_f32_16x16x32_bf16 v[60:63], a[204:207], a[28:31], v[60:63]\n" \
"buffer_load_dwordx4 v192, s[44:47], s73 offen offset:0 lds\n" \
"v_mfma_f32_16x16x32_bf16 v[52:55], a[200:203], a[16:19], v[52:55]\n" \
"ds_read_b64_tr_b16 a[42:43], v194 offset:3072\n" \
"v_accvgpr_read_b32 v118, a110\n" \
"v_mfma_f32_16x16x32_bf16 v[52:55], a[204:207], a[20:23], v[52:55]\n" \
"ds_read_b64_tr_b16 a[44:45], v194 offset:12288\n" \
"v_accvgpr_read_b32 v119, a111\n" \
"v_mfma_f32_16x16x32_bf16 a[120:123], v[162:165], v[156:159], a[120:123]\n" \
"v_mul_f32 v112, v112, v233\n" \
"v_mul_f32 v113, v113, v233\n" \
"v_mul_f32 v114, v114, v233\n" \
"v_mfma_f32_16x16x32_bf16 v[44:47], a[200:203], a[8:11], v[44:47]\n" \
"ds_read_b64_tr_b16 a[46:47], v194 offset:15360\n" \
"v_mul_f32 v115, v115, v233\n" \
"v_mfma_f32_16x16x32_bf16 v[44:47], a[204:207], a[12:15], v[44:47]\n" \
"buffer_load_dwordx4 v193, s[44:47], s73 offen offset:1024 lds\n" \
"v_mfma_f32_16x16x32_bf16 a[124:127], v[160:163], v[156:159], a[124:127]\n" \
"v_mul_f32 v116, v116, v233\n" \
"v_mul_f32 v117, v117, v233\n" \
"v_mfma_f32_16x16x32_bf16 v[72:75], a[0:3], a[208:211], v[72:75]\n" \
"ds_read_b64_tr_b16 a[48:49], v195 offset:0\n" \
"v_mul_f32 v118, v118, v233\n" \
"v_mfma_f32_16x16x32_bf16 v[72:75], a[4:7], a[212:215], v[72:75]\n" \
"ds_read_b128 a[0:3], v207 offset:45056\n" \
"v_mul_f32 v119, v119, v233\n" \
"v_mfma_f32_16x16x32_bf16 v[80:83], a[8:11], a[208:211], v[80:83]\n" \
"ds_read_b128 a[4:7], v208 offset:45056\n" \
"v_cvt_pk_bf16_f32 v124, v112, v113\n" \
"v_mfma_f32_16x16x32_bf16 v[80:83], a[12:15], a[212:215], v[80:83]\n" \
"buffer_load_dwordx4 v192, s[44:47], s73 offen offset:2048 lds\n" \
"v_cvt_pk_bf16_f32 v125, v114, v115\n" \
"v_mfma_f32_16x16x32_bf16 v[84:87], a[8:11], a[216:219], v[84:87]\n" \
"ds_read_b64_tr_b16 a[50:51], v195 offset:3072\n" \
"v_cvt_pk_bf16_f32 v126, v116, v117\n" \
"v_mfma_f32_16x16x32_bf16 v[84:87], a[12:15], a[220:223], v[84:87]\n" \
"ds_read_b128 a[8:11], v205 offset:45056\n" \
"v_cvt_pk_bf16_f32 v127, v118, v119\n" \
"v_mfma_f32_16x16x32_bf16 v[92:95], a[16:19], a[216:219], v[92:95]\n" \
"ds_read_b128 a[12:15], v206 offset:45056\n" \
"v_mfma_f32_16x16x32_bf16 v[92:95], a[20:23], a[220:223], v[92:95]\n" \
"buffer_load_dwordx4 v193, s[44:47], s73 offen offset:3072 lds\n" \
"v_mfma_f32_16x16x32_bf16 v[88:91], a[16:19], a[208:211], v[88:91]\n" \
"ds_read_b64_tr_b16 a[52:53], v195 offset:12288\n" \
"v_mfma_f32_16x16x32_bf16 v[88:91], a[20:23], a[212:215], v[88:91]\n" \
"s_add_u32 m0, s89, 0\n" \
"ds_read_b128 a[16:19], v205 offset:49152\n" \
"buffer_store_dwordx4 v[124:127], v236, s[56:59], s82 offen offset:2048 sc1\n" \
"v_mfma_f32_16x16x32_bf16 v[96:99], a[24:27], a[208:211], v[96:99]\n" \
"s_xor_b32 s73, s73, 0x80\n" \
"ds_read_b128 a[20:23], v206 offset:49152\n" \
"v_mfma_f32_16x16x32_bf16 v[96:99], a[28:31], a[212:215], v[96:99]\n" \
"buffer_load_dwordx4 v196, s[44:47], s75 offen lds\n" \
"v_mfma_f32_16x16x32_bf16 v[224:227], v[244:247], v[124:127], v[224:227]\n" \
"v_accvgpr_read_b32 v112, a112\n" \
"v_accvgpr_read_b32 v113, a113\n" \
"v_accvgpr_read_b32 v114, a114\n" \
"v_mfma_f32_16x16x32_bf16 v[100:103], a[24:27], a[216:219], v[100:103]\n" \
"s_xor_b32 s75, s75, 0x80\n" \
"s_mul_i32 s76, s80, 393216\n" \
"ds_read_b64_tr_b16 a[54:55], v195 offset:15360\n" \
"v_mfma_f32_16x16x32_bf16 v[100:103], a[28:31], a[220:223], v[100:103]\n" \
"s_add_u32 s76, s76, s78\n" \
"ds_read_b128 a[24:27], v205 offset:53248\n" \
"v_accvgpr_read_b32 v115, a115\n" \
"v_mfma_f32_16x16x32_bf16 v[108:111], a[192:195], a[216:219], v[108:111]\n" \
"s_add_u32 m0, s91, 20480\n" \
"ds_read_b128 a[28:31], v206 offset:53248\n" \
"v_accvgpr_read_b32 v116, a116\n" \
"v_mfma_f32_16x16x32_bf16 v[108:111], a[196:199], a[220:223], v[108:111]\n" \
"buffer_load_dwordx4 v197, s[48:51], s76 offen offset:0 lds\n" \
"v_mfma_f32_16x16x32_bf16 v[104:107], a[192:195], a[208:211], v[104:107]\n" \
"v_accvgpr_read_b32 v117, a117\n" \
"v_accvgpr_read_b32 v118, a118\n" \
"v_mfma_f32_16x16x32_bf16 v[104:107], a[196:199], a[212:215], v[104:107]\n" \
"s_waitcnt lgkmcnt(2)\n" \
"v_accvgpr_read_b32 v119, a119\n" \
"v_mul_f32 v112, v112, v233\n" \
"v_mul_f32 v113, v113, v233\n" \
"v_mfma_f32_16x16x32_bf16 v[32:35], a[32:35], a[0:3], v[32:35]\n" \
"s_add_u32 s77, s76, 24576\n" \
"ds_read_b128 a[192:195], v205 offset:57344\n" \
"v_mul_f32 v114, v114, v233\n" \
"v_mfma_f32_16x16x32_bf16 v[32:35], a[36:39], a[4:7], v[32:35]\n" \
"buffer_load_dwordx4 v198, s[48:51], s76 offen offset:1024 lds\n" \
"v_mfma_f32_16x16x32_bf16 v[40:43], a[32:35], a[8:11], v[40:43]\n" \
"ds_read_b128 a[196:199], v206 offset:57344\n" \
"v_mul_f32 v115, v115, v233\n" \
"v_mfma_f32_16x16x32_bf16 v[40:43], a[36:39], a[12:15], v[40:43]\n" \
"ds_read_b64_tr_b16 a[200:201], v195 offset:1024\n" \
"v_mul_f32 v116, v116, v233\n" \
"v_mfma_f32_16x16x32_bf16 v[0:3], a[40:43], a[8:11], v[0:3]\n" \
"s_xor_b32 s80, s80, 0x1\n" \
"ds_read_b64_tr_b16 a[202:203], v195 offset:4096\n" \
"v_mul_f32 v117, v117, v233\n" \
"v_mfma_f32_16x16x32_bf16 v[0:3], a[44:47], a[12:15], v[0:3]\n" \
"buffer_load_dwordx4 v199, s[48:51], s76 offen offset:2048 lds\n" \
"v_mul_f32 v118, v118, v233\n" \
"v_mfma_f32_16x16x32_bf16 v[4:7], a[48:51], a[8:11], v[4:7]\n" \
"ds_read_b64_tr_b16 a[204:205], v195 offset:13312\n" \
"v_mul_f32 v119, v119, v233\n" \
"v_mfma_f32_16x16x32_bf16 v[4:7], a[52:55], a[12:15], v[4:7]\n" \
"ds_read_b64_tr_b16 a[206:207], v195 offset:16384\n" \
"v_cvt_pk_bf16_f32 v120, v112, v113\n" \
"v_mfma_f32_16x16x32_bf16 v[12:15], a[48:51], a[16:19], v[12:15]\n" \
"s_add_u32 m0, m0, 3072\n" \
"ds_read_b64_tr_b16 a[208:209], v194 offset:2048\n" \
"v_cvt_pk_bf16_f32 v121, v114, v115\n" \
"v_mfma_f32_16x16x32_bf16 v[12:15], a[52:55], a[20:23], v[12:15]\n" \
"buffer_load_dwordx4 v197, s[48:51], s77 offen offset:0 lds\n" \
"Lpg7927_%=:\n" \
"Lst1x0_%=:\n" \
"s_cbranch_execz Lpg10211_%=\n" \
"v_mfma_f32_16x16x32_bf16 v[8:11], a[40:43], a[16:19], v[8:11]\n" \
"ds_read_b64_tr_b16 a[210:211], v194 offset:5120\n" \
"v_cvt_pk_bf16_f32 v122, v116, v117\n" \
"v_mfma_f32_16x16x32_bf16 v[8:11], a[44:47], a[20:23], v[8:11]\n" \
"ds_read_b64_tr_b16 a[212:213], v194 offset:14336\n" \
"v_cvt_pk_bf16_f32 v123, v118, v119\n" \
"v_mfma_f32_16x16x32_bf16 v[48:51], a[32:35], a[16:19], v[48:51]\n" \
"s_waitcnt lgkmcnt(3)\n" \
"ds_read_b64_tr_b16 a[214:215], v194 offset:17408\n" \
"v_mfma_f32_16x16x32_bf16 v[48:51], a[36:39], a[20:23], v[48:51]\n" \
"buffer_load_dwordx4 v198, s[48:51], s77 offen offset:1024 lds\n" \
"v_mfma_f32_16x16x32_bf16 v[56:59], a[32:35], a[24:27], v[56:59]\n" \
"ds_read_b64_tr_b16 a[216:217], v195 offset:2048\n" \
"v_mfma_f32_16x16x32_bf16 v[56:59], a[36:39], a[28:31], v[56:59]\n" \
"ds_read_b64_tr_b16 a[218:219], v195 offset:5120\n" \
"buffer_store_dwordx4 v[120:123], v240, s[56:59], s82 offen offset:0 sc1\n" \
"v_mfma_f32_16x16x32_bf16 v[16:19], a[40:43], a[24:27], v[16:19]\n" \
"ds_read_b64_tr_b16 a[220:221], v195 offset:14336\n" \
"v_mfma_f32_16x16x32_bf16 v[16:19], a[44:47], a[28:31], v[16:19]\n" \
"buffer_load_dwordx4 v199, s[48:51], s77 offen offset:2048 lds\n" \
"v_mfma_f32_16x16x32_bf16 v[224:227], v[244:247], v[120:123], v[224:227]\n" \
"v_accvgpr_read_b32 v112, a120\n" \
"v_accvgpr_read_b32 v113, a121\n" \
"v_accvgpr_read_b32 v114, a122\n" \
"v_mfma_f32_16x16x32_bf16 v[20:23], a[48:51], a[24:27], v[20:23]\n" \
"ds_read_b64_tr_b16 a[222:223], v195 offset:17408\n" \
"v_accvgpr_read_b32 v115, a123\n" \
"v_mfma_f32_16x16x32_bf16 v[20:23], a[52:55], a[28:31], v[20:23]\n" \
"v_accvgpr_read_b32 v116, a124\n" \
"v_accvgpr_read_b32 v117, a125\n" \
"v_accvgpr_read_b32 v118, a126\n" \
"v_mfma_f32_16x16x32_bf16 v[28:31], a[48:51], a[192:195], v[28:31]\n" \
"v_accvgpr_read_b32 v119, a127\n" \
"v_mul_f32 v112, v112, v233\n" \
"v_mfma_f32_16x16x32_bf16 v[28:31], a[52:55], a[196:199], v[28:31]\n" \
"v_mul_f32 v113, v113, v233\n" \
"v_mul_f32 v114, v114, v233\n" \
"v_mfma_f32_16x16x32_bf16 v[24:27], a[40:43], a[192:195], v[24:27]\n" \
"v_mul_f32 v115, v115, v233\n" \
"v_mul_f32 v116, v116, v233\n" \
"v_mfma_f32_16x16x32_bf16 v[24:27], a[44:47], a[196:199], v[24:27]\n" \
"v_mul_f32 v117, v117, v233\n" \
"v_mul_f32 v118, v118, v233\n" \
"v_mfma_f32_16x16x32_bf16 v[64:67], a[32:35], a[192:195], v[64:67]\n" \
"v_mul_f32 v119, v119, v233\n" \
"v_cvt_pk_bf16_f32 v124, v112, v113\n" \
"v_mfma_f32_16x16x32_bf16 v[64:67], a[36:39], a[196:199], v[64:67]\n" \
"v_cvt_pk_bf16_f32 v125, v114, v115\n" \
"s_waitcnt vmcnt(14) lgkmcnt(0)\n" \
"s_barrier\n" \
"v_mfma_f32_16x16x32_bf16 v[68:71], a[200:203], a[192:195], v[68:71]\n" \
"ds_read_b64_tr_b16 a[32:33], v209 offset:1024\n" \
"ds_read_b64_tr_b16 a[34:35], v209 offset:4096\n" \
"v_mfma_f32_16x16x32_bf16 v[68:71], a[204:207], a[196:199], v[68:71]\n" \
"ds_read_b64_tr_b16 a[36:37], v209 offset:13312\n" \
"ds_read_b64_tr_b16 a[38:39], v209 offset:16384\n" \
"v_mfma_f32_16x16x32_bf16 v[60:63], a[200:203], a[24:27], v[60:63]\n" \
"s_add_u32 m0, s79, 110592\n" \
"ds_read_b64_tr_b16 a[40:41], v209 offset:0\n" \
"v_cvt_pk_bf16_f32 v126, v116, v117\n" \
"v_mfma_f32_16x16x32_bf16 v[60:63], a[204:207], a[28:31], v[60:63]\n" \
"buffer_load_dwordx4 v192, s[44:47], s73 offen offset:0 lds\n" \
"v_mfma_f32_16x16x32_bf16 v[52:55], a[200:203], a[16:19], v[52:55]\n" \
"ds_read_b64_tr_b16 a[42:43], v209 offset:3072\n" \
"v_cvt_pk_bf16_f32 v127, v118, v119\n" \
"v_mfma_f32_16x16x32_bf16 v[52:55], a[204:207], a[20:23], v[52:55]\n" \
"ds_read_b64_tr_b16 a[44:45], v209 offset:12288\n" \
"buffer_store_dwordx4 v[124:127], v240, s[56:59], s82 offen offset:2048 sc1\n" \
"v_mfma_f32_16x16x32_bf16 v[44:47], a[200:203], a[8:11], v[44:47]\n" \
"ds_read_b64_tr_b16 a[46:47], v209 offset:15360\n" \
"v_mfma_f32_16x16x32_bf16 v[44:47], a[204:207], a[12:15], v[44:47]\n" \
"buffer_load_dwordx4 v193, s[44:47], s73 offen offset:1024 lds\n" \
"v_mfma_f32_16x16x32_bf16 v[224:227], v[244:247], v[124:127], v[224:227]\n" \
"v_mfma_f32_16x16x32_bf16 v[72:75], a[0:3], a[208:211], v[72:75]\n" \
"ds_read_b64_tr_b16 a[48:49], v210 offset:0\n" \
"v_mfma_f32_16x16x32_bf16 v[72:75], a[4:7], a[212:215], v[72:75]\n" \
"ds_read_b128 a[0:3], v207 offset:0\n" \
"v_mfma_f32_16x16x32_bf16 v[80:83], a[8:11], a[208:211], v[80:83]\n" \
"ds_read_b128 a[4:7], v208 offset:0\n" \
"v_mfma_f32_16x16x32_bf16 v[80:83], a[12:15], a[212:215], v[80:83]\n" \
"buffer_load_dwordx4 v192, s[44:47], s73 offen offset:2048 lds\n" \
"v_mfma_f32_16x16x32_bf16 v[84:87], a[8:11], a[216:219], v[84:87]\n" \
"ds_read_b64_tr_b16 a[50:51], v210 offset:3072\n" \
"v_mfma_f32_16x16x32_bf16 v[84:87], a[12:15], a[220:223], v[84:87]\n" \
"ds_read_b128 a[8:11], v205 offset:0\n" \
"v_mfma_f32_16x16x32_bf16 v[92:95], a[16:19], a[216:219], v[92:95]\n" \
"ds_read_b128 a[12:15], v206 offset:0\n" \
"v_mfma_f32_16x16x32_bf16 v[92:95], a[20:23], a[220:223], v[92:95]\n" \
"buffer_load_dwordx4 v193, s[44:47], s73 offen offset:3072 lds\n" \
"v_mfma_f32_16x16x32_bf16 v[88:91], a[16:19], a[208:211], v[88:91]\n" \
"ds_read_b64_tr_b16 a[52:53], v210 offset:12288\n" \
"v_mfma_f32_16x16x32_bf16 v[88:91], a[20:23], a[212:215], v[88:91]\n" \
"s_add_u32 m0, s89, 110592\n" \
"ds_read_b128 a[16:19], v205 offset:4096\n" \
"v_mfma_f32_16x16x32_bf16 v[96:99], a[24:27], a[208:211], v[96:99]\n" \
"s_xor_b32 s73, s73, 0x180\n" \
"ds_read_b128 a[20:23], v206 offset:4096\n" \
"v_mfma_f32_16x16x32_bf16 v[96:99], a[28:31], a[212:215], v[96:99]\n" \
"buffer_load_dwordx4 v196, s[44:47], s75 offen lds\n" \
"v_mfma_f32_16x16x32_bf16 v[100:103], a[24:27], a[216:219], v[100:103]\n" \
"s_xor_b32 s75, s75, 0x180\n" \
"s_mul_i32 s76, s80, 393216\n" \
"ds_read_b64_tr_b16 a[54:55], v210 offset:15360\n" \
"v_mfma_f32_16x16x32_bf16 v[100:103], a[28:31], a[220:223], v[100:103]\n" \
"s_add_u32 s76, s76, s78\n" \
"ds_read_b128 a[24:27], v205 offset:8192\n" \
"v_mfma_f32_16x16x32_bf16 v[108:111], a[192:195], a[216:219], v[108:111]\n" \
"s_add_u32 m0, s91, 139264\n" \
"ds_read_b128 a[28:31], v206 offset:8192\n" \
"v_mfma_f32_16x16x32_bf16 v[108:111], a[196:199], a[220:223], v[108:111]\n" \
"buffer_load_dwordx4 v197, s[48:51], s76 offen offset:0 lds\n" \
"v_mfma_f32_16x16x32_bf16 v[104:107], a[192:195], a[208:211], v[104:107]\n" \
"v_mfma_f32_16x16x32_bf16 v[104:107], a[196:199], a[212:215], v[104:107]\n" \
"s_waitcnt lgkmcnt(2)\n" \
"v_mfma_f32_16x16x32_bf16 v[32:35], a[32:35], a[0:3], v[32:35]\n" \
"s_add_u32 s77, s76, 24576\n" \
"ds_read_b128 a[192:195], v205 offset:12288\n" \
"v_mfma_f32_16x16x32_bf16 v[32:35], a[36:39], a[4:7], v[32:35]\n" \
"buffer_load_dwordx4 v198, s[48:51], s76 offen offset:1024 lds\n" \
"v_mfma_f32_16x16x32_bf16 v[40:43], a[32:35], a[8:11], v[40:43]\n" \
"ds_read_b128 a[196:199], v206 offset:12288\n" \
"v_mfma_f32_16x16x32_bf16 v[40:43], a[36:39], a[12:15], v[40:43]\n" \
"ds_read_b64_tr_b16 a[200:201], v210 offset:1024\n" \
"v_mfma_f32_16x16x32_bf16 v[0:3], a[40:43], a[8:11], v[0:3]\n" \
"s_xor_b32 s80, s80, 0x3\n" \
"ds_read_b64_tr_b16 a[202:203], v210 offset:4096\n" \
"v_mfma_f32_16x16x32_bf16 v[0:3], a[44:47], a[12:15], v[0:3]\n" \
"buffer_load_dwordx4 v199, s[48:51], s76 offen offset:2048 lds\n" \
"v_mfma_f32_16x16x32_bf16 v[4:7], a[48:51], a[8:11], v[4:7]\n" \
"ds_read_b64_tr_b16 a[204:205], v210 offset:13312\n" \
"v_mfma_f32_16x16x32_bf16 v[4:7], a[52:55], a[12:15], v[4:7]\n" \
"ds_read_b64_tr_b16 a[206:207], v210 offset:16384\n" \
"v_mfma_f32_16x16x32_bf16 v[12:15], a[48:51], a[16:19], v[12:15]\n" \
"s_add_u32 m0, m0, 3072\n" \
"ds_read_b64_tr_b16 a[208:209], v209 offset:2048\n" \
"v_mfma_f32_16x16x32_bf16 v[12:15], a[52:55], a[20:23], v[12:15]\n" \
"buffer_load_dwordx4 v197, s[48:51], s77 offen offset:0 lds\n" \
"v_mfma_f32_16x16x32_bf16 v[8:11], a[40:43], a[16:19], v[8:11]\n" \
"ds_read_b64_tr_b16 a[210:211], v209 offset:5120\n" \
"v_mfma_f32_16x16x32_bf16 v[8:11], a[44:47], a[20:23], v[8:11]\n" \
"ds_read_b64_tr_b16 a[212:213], v209 offset:14336\n" \
"v_mfma_f32_16x16x32_bf16 v[48:51], a[32:35], a[16:19], v[48:51]\n" \
"s_waitcnt lgkmcnt(3)\n" \
"ds_read_b64_tr_b16 a[214:215], v209 offset:17408\n" \
"v_mfma_f32_16x16x32_bf16 v[48:51], a[36:39], a[20:23], v[48:51]\n" \
"buffer_load_dwordx4 v198, s[48:51], s77 offen offset:1024 lds\n" \
"v_mfma_f32_16x16x32_bf16 v[56:59], a[32:35], a[24:27], v[56:59]\n" \
"ds_read_b64_tr_b16 a[216:217], v210 offset:2048\n" \
"v_mfma_f32_16x16x32_bf16 v[56:59], a[36:39], a[28:31], v[56:59]\n" \
"ds_read_b64_tr_b16 a[218:219], v210 offset:5120\n" \
"v_mfma_f32_16x16x32_bf16 v[16:19], a[40:43], a[24:27], v[16:19]\n" \
"ds_read_b64_tr_b16 a[220:221], v210 offset:14336\n" \
"v_mfma_f32_16x16x32_bf16 v[16:19], a[44:47], a[28:31], v[16:19]\n" \
"buffer_load_dwordx4 v199, s[48:51], s77 offen offset:2048 lds\n" \
"v_mfma_f32_16x16x32_bf16 v[20:23], a[48:51], a[24:27], v[20:23]\n" \
"ds_read_b64_tr_b16 a[222:223], v210 offset:17408\n" \
"v_mfma_f32_16x16x32_bf16 v[20:23], a[52:55], a[28:31], v[20:23]\n" \
"v_mfma_f32_16x16x32_bf16 v[28:31], a[48:51], a[192:195], v[28:31]\n" \
"v_mfma_f32_16x16x32_bf16 v[28:31], a[52:55], a[196:199], v[28:31]\n" \
"v_mfma_f32_16x16x32_bf16 v[24:27], a[40:43], a[192:195], v[24:27]\n" \
"v_mfma_f32_16x16x32_bf16 v[24:27], a[44:47], a[196:199], v[24:27]\n" \
"v_mfma_f32_16x16x32_bf16 v[64:67], a[32:35], a[192:195], v[64:67]\n" \
"v_mfma_f32_16x16x32_bf16 v[64:67], a[36:39], a[196:199], v[64:67]\n" \
"s_waitcnt vmcnt(12) lgkmcnt(0)\n" \
"s_barrier\n" \
"s_movk_i32 s64, 0x207\n" \
"v_cmp_class_f32 vcc, v224, s64\n" \
"s_nop 1\n" \
"s_cmp_lg_u64 vcc, 0\n" \
"s_addc_u32 s90, s90, 0\n" \
"s_cmp_lg_u32 s90, 0\n" \
"s_cbranch_scc1 Lexact_%=\n" \
"Lexr_%=:\n" \
"v_mfma_f32_16x16x32_bf16 v[68:71], a[200:203], a[192:195], v[68:71]\n" \
"s_lshl_b32 s15, s63, 7\n" \
"ds_read_b64_tr_b16 a[32:33], v204 offset:21504\n" \
"ds_read_b64_tr_b16 a[34:35], v204 offset:24576\n" \
"v_mfma_f32_16x16x32_bf16 v[68:71], a[204:207], a[196:199], v[68:71]\n" \
"s_lshl2_add_u32 s16, s62, 65589248\n" \
"ds_read_b64_tr_b16 a[36:37], v204 offset:33792\n" \
"ds_read_b64_tr_b16 a[38:39], v204 offset:36864\n" \
"v_mfma_f32_16x16x32_bf16 v[60:63], a[200:203], a[24:27], v[60:63]\n" \
"s_add_u32 m0, s79, 65536\n" \
"s_sub_u32 s17, s16, s15\n" \
"ds_read_b64_tr_b16 a[40:41], v204 offset:20480\n" \
"v_mfma_f32_16x16x32_bf16 v[60:63], a[204:207], a[28:31], v[60:63]\n" \
"buffer_load_dwordx4 v192, s[44:47], s73 offen offset:0 lds\n" \
"v_mfma_f32_16x16x32_bf16 v[52:55], a[200:203], a[16:19], v[52:55]\n" \
"s_add_u32 s15, s15, s16\n" \
"s_add_u32 s17, s17, 1920\n" \
"ds_read_b64_tr_b16 a[42:43], v204 offset:23552\n" \
"v_mfma_f32_16x16x32_bf16 v[52:55], a[204:207], a[20:23], v[52:55]\n" \
"ds_read_b64_tr_b16 a[44:45], v204 offset:32768\n" \
"v_mov_b32 v146, s60\n" \
"v_mfma_f32_16x16x32_bf16 v[44:47], a[200:203], a[8:11], v[44:47]\n" \
"ds_read_b64_tr_b16 a[46:47], v204 offset:35840\n" \
"v_lshlrev_b32 v147, 28, s61\n" \
"v_mfma_f32_16x16x32_bf16 v[44:47], a[204:207], a[12:15], v[44:47]\n" \
"buffer_load_dwordx4 v193, s[44:47], s73 offen offset:1024 lds\n" \
"v_mfma_f32_16x16x32_bf16 v[72:75], a[0:3], a[208:211], v[72:75]\n" \
"ds_read_b64_tr_b16 a[48:49], v222 offset:20480\n" \
"v_mfma_f32_16x16x32_bf16 v[72:75], a[4:7], a[212:215], v[72:75]\n" \
"ds_read_b128 a[0:3], v202 offset:0\n" \
"v_mfma_f32_16x16x32_bf16 v[80:83], a[8:11], a[208:211], v[80:83]\n" \
"ds_read_b128 a[4:7], v203 offset:0\n" \
"v_mfma_f32_16x16x32_bf16 v[80:83], a[12:15], a[212:215], v[80:83]\n" \
"buffer_load_dwordx4 v192, s[44:47], s73 offen offset:2048 lds\n" \
"v_mfma_f32_16x16x32_bf16 v[84:87], a[8:11], a[216:219], v[84:87]\n" \
"ds_read_b64_tr_b16 a[50:51], v222 offset:23552\n" \
"v_mfma_f32_16x16x32_bf16 v[84:87], a[12:15], a[220:223], v[84:87]\n" \
"ds_read_b128 a[8:11], v200 offset:0\n" \
"v_mfma_f32_16x16x32_bf16 v[92:95], a[16:19], a[216:219], v[92:95]\n" \
"ds_read_b128 a[12:15], v201 offset:0\n" \
"v_mfma_f32_16x16x32_bf16 v[92:95], a[20:23], a[220:223], v[92:95]\n" \
"buffer_load_dwordx4 v193, s[44:47], s73 offen offset:3072 lds\n" \
"v_mfma_f32_16x16x32_bf16 v[88:91], a[16:19], a[208:211], v[88:91]\n" \
"ds_read_b64_tr_b16 a[52:53], v222 offset:32768\n" \
"v_mfma_f32_16x16x32_bf16 v[88:91], a[20:23], a[212:215], v[88:91]\n" \
"s_add_u32 m0, s89, 65536\n" \
"ds_read_b128 a[16:19], v200 offset:4096\n" \
"v_mfma_f32_16x16x32_bf16 v[96:99], a[24:27], a[208:211], v[96:99]\n" \
"s_xor_b32 s73, s73, 0x80\n" \
"ds_read_b128 a[20:23], v201 offset:4096\n" \
"v_mfma_f32_16x16x32_bf16 v[96:99], a[28:31], a[212:215], v[96:99]\n" \
"buffer_load_dwordx4 v196, s[44:47], s75 offen lds\n" \
"v_mfma_f32_16x16x32_bf16 v[100:103], a[24:27], a[216:219], v[100:103]\n" \
"s_xor_b32 s75, s75, 0x80\n" \
"s_mul_i32 s76, s80, 393216\n" \
"ds_read_b64_tr_b16 a[54:55], v222 offset:35840\n" \
"v_mfma_f32_16x16x32_bf16 v[100:103], a[28:31], a[220:223], v[100:103]\n" \
"s_add_u32 s76, s76, s78\n" \
"ds_read_b128 a[24:27], v200 offset:8192\n" \
"v_mfma_f32_16x16x32_bf16 v[108:111], a[192:195], a[216:219], v[108:111]\n" \
"s_add_u32 m0, s91, 86016\n" \
"ds_read_b128 a[28:31], v201 offset:8192\n" \
"v_mfma_f32_16x16x32_bf16 v[108:111], a[196:199], a[220:223], v[108:111]\n" \
"buffer_load_dwordx4 v197, s[48:51], s76 offen offset:0 lds\n" \
"v_mfma_f32_16x16x32_bf16 v[104:107], a[192:195], a[208:211], v[104:107]\n" \
"v_mfma_f32_16x16x32_bf16 v[104:107], a[196:199], a[212:215], v[104:107]\n" \
"s_waitcnt lgkmcnt(2)\n" \
"v_mfma_f32_16x16x32_bf16 v[32:35], a[32:35], a[0:3], v[32:35]\n" \
"s_add_u32 s77, s76, 24576\n" \
"ds_read_b128 a[192:195], v200 offset:12288\n" \
"v_mfma_f32_16x16x32_bf16 v[32:35], a[36:39], a[4:7], v[32:35]\n" \
"buffer_load_dwordx4 v198, s[48:51], s76 offen offset:1024 lds\n" \
"v_mfma_f32_16x16x32_bf16 v[40:43], a[32:35], a[8:11], v[40:43]\n" \
"ds_read_b128 a[196:199], v201 offset:12288\n" \
"v_mfma_f32_16x16x32_bf16 v[40:43], a[36:39], a[12:15], v[40:43]\n" \
"ds_read_b64_tr_b16 a[200:201], v222 offset:21504\n" \
"v_mfma_f32_16x16x32_bf16 v[0:3], a[40:43], a[8:11], v[0:3]\n" \
"s_xor_b32 s80, s80, 0x1\n" \
"ds_read_b64_tr_b16 a[202:203], v222 offset:24576\n" \
"v_mfma_f32_16x16x32_bf16 v[0:3], a[44:47], a[12:15], v[0:3]\n" \
"buffer_load_dwordx4 v199, s[48:51], s76 offen offset:2048 lds\n" \
"v_mfma_f32_16x16x32_bf16 v[4:7], a[48:51], a[8:11], v[4:7]\n" \
"ds_read_b64_tr_b16 a[204:205], v222 offset:33792\n" \
"v_mfma_f32_16x16x32_bf16 v[4:7], a[52:55], a[12:15], v[4:7]\n" \
"ds_read_b64_tr_b16 a[206:207], v222 offset:36864\n" \
"v_mfma_f32_16x16x32_bf16 v[12:15], a[48:51], a[16:19], v[12:15]\n" \
"s_add_u32 m0, m0, 3072\n" \
"ds_read_b64_tr_b16 a[208:209], v204 offset:22528\n" \
"v_mfma_f32_16x16x32_bf16 v[12:15], a[52:55], a[20:23], v[12:15]\n" \
"buffer_load_dwordx4 v197, s[48:51], s77 offen offset:0 lds\n" \
"v_mfma_f32_16x16x32_bf16 v[8:11], a[40:43], a[16:19], v[8:11]\n" \
"ds_read_b64_tr_b16 a[210:211], v204 offset:25600\n" \
"v_mfma_f32_16x16x32_bf16 v[8:11], a[44:47], a[20:23], v[8:11]\n" \
"ds_read_b64_tr_b16 a[212:213], v204 offset:34816\n" \
"v_mfma_f32_16x16x32_bf16 v[48:51], a[32:35], a[16:19], v[48:51]\n" \
"s_waitcnt lgkmcnt(3)\n" \
"ds_read_b64_tr_b16 a[214:215], v204 offset:37888\n" \
"v_mfma_f32_16x16x32_bf16 v[48:51], a[36:39], a[20:23], v[48:51]\n" \
"buffer_load_dwordx4 v198, s[48:51], s77 offen offset:1024 lds\n" \
"v_mfma_f32_16x16x32_bf16 v[56:59], a[32:35], a[24:27], v[56:59]\n" \
"ds_read_b64_tr_b16 a[216:217], v222 offset:22528\n" \
"v_mfma_f32_16x16x32_bf16 v[56:59], a[36:39], a[28:31], v[56:59]\n" \
"ds_read_b64_tr_b16 a[218:219], v222 offset:25600\n" \
"v_mfma_f32_16x16x32_bf16 v[16:19], a[40:43], a[24:27], v[16:19]\n" \
"ds_read_b64_tr_b16 a[220:221], v222 offset:34816\n" \
"v_mfma_f32_16x16x32_bf16 v[16:19], a[44:47], a[28:31], v[16:19]\n" \
"buffer_load_dwordx4 v199, s[48:51], s77 offen offset:2048 lds\n" \
"v_mfma_f32_16x16x32_bf16 v[20:23], a[48:51], a[24:27], v[20:23]\n" \
"ds_read_b64_tr_b16 a[222:223], v222 offset:37888\n" \
"v_mfma_f32_16x16x32_bf16 v[20:23], a[52:55], a[28:31], v[20:23]\n" \
"v_mfma_f32_16x16x32_bf16 v[28:31], a[48:51], a[192:195], v[28:31]\n" \
"v_mfma_f32_16x16x32_bf16 v[28:31], a[52:55], a[196:199], v[28:31]\n" \
"v_mfma_f32_16x16x32_bf16 v[24:27], a[40:43], a[192:195], v[24:27]\n" \
"v_mfma_f32_16x16x32_bf16 v[24:27], a[44:47], a[196:199], v[24:27]\n" \
"v_mfma_f32_16x16x32_bf16 v[64:67], a[32:35], a[192:195], v[64:67]\n" \
"v_mfma_f32_16x16x32_bf16 v[64:67], a[36:39], a[196:199], v[64:67]\n" \
"s_waitcnt vmcnt(11) lgkmcnt(0)\n" \
"s_barrier\n" \
"v_mfma_f32_16x16x32_bf16 v[68:71], a[200:203], a[192:195], v[68:71]\n" \
"ds_read_b64_tr_b16 a[32:33], v194 offset:1024\n" \
"ds_read_b64_tr_b16 a[34:35], v194 offset:4096\n" \
"v_mfma_f32_16x16x32_bf16 v[68:71], a[204:207], a[196:199], v[68:71]\n" \
"ds_read_b64_tr_b16 a[36:37], v194 offset:13312\n" \
"ds_read_b64_tr_b16 a[38:39], v194 offset:16384\n" \
"v_mfma_f32_16x16x32_bf16 v[60:63], a[200:203], a[24:27], v[60:63]\n" \
"s_add_u32 m0, s79, 0\n" \
"ds_read_b64_tr_b16 a[40:41], v194 offset:0\n" \
"buffer_store_dword v146, v147, s[56:59], s15 offen sc1\n" \
"v_mfma_f32_16x16x32_bf16 v[60:63], a[204:207], a[28:31], v[60:63]\n" \
"buffer_load_dwordx4 v192, s[44:47], s73 offen offset:0 lds\n" \
"v_mfma_f32_16x16x32_bf16 v[52:55], a[200:203], a[16:19], v[52:55]\n" \
"ds_read_b64_tr_b16 a[42:43], v194 offset:3072\n" \
"v_mfma_f32_16x16x32_bf16 v[52:55], a[204:207], a[20:23], v[52:55]\n" \
"ds_read_b64_tr_b16 a[44:45], v194 offset:12288\n" \
"buffer_store_dword v146, v147, s[56:59], s17 offen sc1\n" \
"v_mfma_f32_16x16x32_bf16 v[44:47], a[200:203], a[8:11], v[44:47]\n" \
"s_mov_b64 s[36:37], s[18:19]\n" \
"ds_read_b64_tr_b16 a[46:47], v194 offset:15360\n" \
"v_mfma_f32_16x16x32_bf16 v[44:47], a[204:207], a[12:15], v[44:47]\n" \
"buffer_load_dwordx4 v193, s[44:47], s73 offen offset:1024 lds\n" \
"v_mfma_f32_16x16x32_bf16 v[72:75], a[0:3], a[208:211], v[72:75]\n" \
"s_sub_u32 s36, s36, 4096\n" \
"s_subb_u32 s37, s37, 0\n" \
"ds_read_b64_tr_b16 a[48:49], v195 offset:0\n" \
"v_mfma_f32_16x16x32_bf16 v[72:75], a[4:7], a[212:215], v[72:75]\n" \
"s_and_b32 s37, s37, 0xffff\n" \
"s_mov_b32 s38, 8392704\n" \
"ds_read_b128 a[0:3], v207 offset:45056\n" \
"v_mfma_f32_16x16x32_bf16 v[80:83], a[8:11], a[208:211], v[80:83]\n" \
"s_mov_b32 s39, 0x20000\n" \
"s_mov_b64 s[40:41], s[24:25]\n" \
"ds_read_b128 a[4:7], v208 offset:45056\n" \
"v_mfma_f32_16x16x32_bf16 v[80:83], a[12:15], a[212:215], v[80:83]\n" \
"buffer_load_dwordx4 v192, s[44:47], s73 offen offset:2048 lds\n" \
"v_mfma_f32_16x16x32_bf16 v[84:87], a[8:11], a[216:219], v[84:87]\n" \
"s_sub_u32 s40, s40, 4096\n" \
"s_subb_u32 s41, s41, 0\n" \
"ds_read_b64_tr_b16 a[50:51], v195 offset:3072\n" \
"v_mfma_f32_16x16x32_bf16 v[84:87], a[12:15], a[220:223], v[84:87]\n" \
"s_and_b32 s41, s41, 0xffff\n" \
"s_mov_b32 s42, 2101248\n" \
"ds_read_b128 a[8:11], v205 offset:45056\n" \
"v_mfma_f32_16x16x32_bf16 v[92:95], a[16:19], a[216:219], v[92:95]\n" \
"s_mov_b32 s43, 0x20000\n" \
"s_mov_b64 s[52:53], s[26:27]\n" \
"ds_read_b128 a[12:15], v206 offset:45056\n" \
"v_mfma_f32_16x16x32_bf16 v[92:95], a[20:23], a[220:223], v[92:95]\n" \
"buffer_load_dwordx4 v193, s[44:47], s73 offen offset:3072 lds\n" \
"v_mfma_f32_16x16x32_bf16 v[88:91], a[16:19], a[208:211], v[88:91]\n" \
"s_and_b32 s53, s53, 0xffff\n" \
"s_mov_b32 s54, 8388608\n" \
"ds_read_b64_tr_b16 a[52:53], v195 offset:12288\n" \
"v_mfma_f32_16x16x32_bf16 v[88:91], a[20:23], a[212:215], v[88:91]\n" \
"s_add_u32 m0, s89, 0\n" \
"s_mov_b32 s55, 0x20000\n" \
"ds_read_b128 a[16:19], v205 offset:49152\n" \
"v_mfma_f32_16x16x32_bf16 v[96:99], a[24:27], a[208:211], v[96:99]\n" \
"s_xor_b32 s73, s73, 0x780\n" \
"ds_read_b128 a[20:23], v206 offset:49152\n" \
"v_lshrrev_b32 v221, 2, v234\n" \
"v_mfma_f32_16x16x32_bf16 v[96:99], a[28:31], a[212:215], v[96:99]\n" \
"buffer_load_dwordx4 v196, s[44:47], s75 offen lds\n" \
"v_mfma_f32_16x16x32_bf16 v[100:103], a[24:27], a[216:219], v[100:103]\n" \
"s_xor_b32 s75, s75, 0x780\n" \
"s_mul_i32 s76, s80, 393216\n" \
"ds_read_b64_tr_b16 a[54:55], v195 offset:15360\n" \
"v_mfma_f32_16x16x32_bf16 v[100:103], a[28:31], a[220:223], v[100:103]\n" \
"s_add_u32 s76, s76, s78\n" \
"ds_read_b128 a[24:27], v205 offset:53248\n" \
"v_lshrrev_b32 v244, 4, v234\n" \
"v_mfma_f32_16x16x32_bf16 v[108:111], a[192:195], a[216:219], v[108:111]\n" \
"s_add_u32 m0, s91, 20480\n" \
"s_lshl_b32 s12, s94, 7\n" \
"ds_read_b128 a[28:31], v206 offset:53248\n" \
"v_mfma_f32_16x16x32_bf16 v[108:111], a[196:199], a[220:223], v[108:111]\n" \
"buffer_load_dwordx4 v197, s[48:51], s76 offen offset:0 lds\n" \
"v_mfma_f32_16x16x32_bf16 v[104:107], a[192:195], a[208:211], v[104:107]\n" \
"s_add_u32 s12, s12, 65591296\n" \
"s_and_b32 s70, %1, 1\n" \
"s_lshl3_add_u32 s70, s70, s63\n" \
"v_mfma_f32_16x16x32_bf16 v[104:107], a[196:199], a[212:215], v[104:107]\n" \
"s_waitcnt lgkmcnt(2)\n" \
"s_lshl2_add_u32 s12, s70, s12\n" \
"s_lshr_b32 s68, s63, 1\n" \
"s_add_u32 s68, s68, 2\n" \
"v_mfma_f32_16x16x32_bf16 v[32:35], a[32:35], a[0:3], v[32:35]\n" \
"s_add_u32 s77, s76, 24576\n" \
"s_lshr_b32 s70, s18, 8\n" \
"ds_read_b128 a[192:195], v205 offset:57344\n" \
"v_mfma_f32_16x16x32_bf16 v[32:35], a[36:39], a[4:7], v[32:35]\n" \
"buffer_load_dwordx4 v198, s[48:51], s76 offen offset:1024 lds\n" \
"v_mfma_f32_16x16x32_bf16 v[40:43], a[32:35], a[8:11], v[40:43]\n" \
"s_sub_u32 s68, s68, s70\n" \
"s_and_b32 s68, s68, 3\n" \
"ds_read_b128 a[196:199], v206 offset:57344\n" \
"v_mfma_f32_16x16x32_bf16 v[40:43], a[36:39], a[12:15], v[40:43]\n" \
"s_and_b32 s70, s63, 1\n" \
"s_lshl_b32 s70, s70, 2\n" \
"ds_read_b64_tr_b16 a[200:201], v195 offset:1024\n" \
"v_mfma_f32_16x16x32_bf16 v[0:3], a[40:43], a[8:11], v[0:3]\n" \
"s_xor_b32 s80, s80, 0xf\n" \
"s_or_b32 s68, s68, s70\n" \
"ds_read_b64_tr_b16 a[202:203], v195 offset:4096\n" \
"v_mfma_f32_16x16x32_bf16 v[0:3], a[44:47], a[12:15], v[0:3]\n" \
"buffer_load_dwordx4 v199, s[48:51], s76 offen offset:2048 lds\n" \
"v_mfma_f32_16x16x32_bf16 v[4:7], a[48:51], a[8:11], v[4:7]\n" \
"s_lshr_b32 s84, s61, 1\n" \
"s_lshl_b32 s84, s84, 16\n" \
"ds_read_b64_tr_b16 a[204:205], v195 offset:13312\n" \
"v_mfma_f32_16x16x32_bf16 v[4:7], a[52:55], a[12:15], v[4:7]\n" \
"s_and_b32 s70, s61, 1\n" \
"s_lshl_b32 s70, s70, 14\n" \
"ds_read_b64_tr_b16 a[206:207], v195 offset:16384\n" \
"v_mfma_f32_16x16x32_bf16 v[12:15], a[48:51], a[16:19], v[12:15]\n" \
"s_add_u32 m0, m0, 3072\n" \
"s_add_u32 s84, s84, s70\n" \
"ds_read_b64_tr_b16 a[208:209], v194 offset:2048\n" \
"v_mfma_f32_16x16x32_bf16 v[12:15], a[52:55], a[20:23], v[12:15]\n" \
"buffer_load_dwordx4 v197, s[48:51], s77 offen offset:0 lds\n" \
"v_mfma_f32_16x16x32_bf16 v[8:11], a[40:43], a[16:19], v[8:11]\n" \
"s_lshl_b32 s88, s68, 8\n" \
"s_add_u32 s84, s84, s88\n" \
"ds_read_b64_tr_b16 a[210:211], v194 offset:5120\n" \
"v_mfma_f32_16x16x32_bf16 v[8:11], a[44:47], a[20:23], v[8:11]\n" \
"ds_read_b64_tr_b16 a[212:213], v194 offset:14336\n" \
"v_bfe_u32 v245, v244, 2, 3\n" \
"v_mfma_f32_16x16x32_bf16 v[48:51], a[32:35], a[16:19], v[48:51]\n" \
"s_waitcnt lgkmcnt(3)\n" \
"ds_read_b64_tr_b16 a[214:215], v194 offset:17408\n" \
"v_lshlrev_b32 v245, 11, v245\n" \
"v_mfma_f32_16x16x32_bf16 v[48:51], a[36:39], a[20:23], v[48:51]\n" \
"buffer_load_dwordx4 v198, s[48:51], s77 offen offset:1024 lds\n" \
"v_mfma_f32_16x16x32_bf16 v[56:59], a[32:35], a[24:27], v[56:59]\n" \
"ds_read_b64_tr_b16 a[216:217], v195 offset:2048\n" \
"v_lshrrev_b32 v246, 5, v244\n" \
"v_mfma_f32_16x16x32_bf16 v[56:59], a[36:39], a[28:31], v[56:59]\n" \
"ds_read_b64_tr_b16 a[218:219], v195 offset:5120\n" \
"v_lshl_add_u32 v245, v246, 6, v245\n" \
"v_mfma_f32_16x16x32_bf16 v[16:19], a[40:43], a[24:27], v[16:19]\n" \
"ds_read_b64_tr_b16 a[220:221], v195 offset:14336\n" \
"v_and_b32 v246, 3, v244\n" \
"v_mfma_f32_16x16x32_bf16 v[16:19], a[44:47], a[28:31], v[16:19]\n" \
"buffer_load_dwordx4 v199, s[48:51], s77 offen offset:2048 lds\n" \
"v_mfma_f32_16x16x32_bf16 v[20:23], a[48:51], a[24:27], v[20:23]\n" \
"ds_read_b64_tr_b16 a[222:223], v195 offset:17408\n" \
"v_bfe_u32 v247, v244, 4, 1\n" \
"v_mfma_f32_16x16x32_bf16 v[20:23], a[52:55], a[28:31], v[20:23]\n" \
"v_lshlrev_b32 v247, 1, v247\n" \
"v_xor_b32 v246, v246, v247\n" \
"v_mfma_f32_16x16x32_bf16 v[28:31], a[48:51], a[192:195], v[28:31]\n" \
"v_add_u32 v254, 3145728, v254\n" \
"v_lshl_add_u32 v245, v246, 4, v245\n" \
"v_mfma_f32_16x16x32_bf16 v[28:31], a[52:55], a[196:199], v[28:31]\n" \
"global_load_dword v253, v254, s[22:23]\n" \
"v_mfma_f32_16x16x32_bf16 v[24:27], a[40:43], a[192:195], v[24:27]\n" \
"v_add_u32 v216, 4096, v245\n" \
"v_add_u32 v217, 3200, v245\n" \
"v_mfma_f32_16x16x32_bf16 v[24:27], a[44:47], a[196:199], v[24:27]\n" \
"v_add_u32 v218, 34816, v245\n" \
"v_add_u32 v219, 33920, v245\n" \
"v_mfma_f32_16x16x32_bf16 v[64:67], a[32:35], a[192:195], v[64:67]\n" \
"s_mov_b32 s86, 65536\n" \
"s_lshr_b32 s70, s61, 1\n" \
"s_lshl_b32 s71, s70, 13\n" \
"v_mfma_f32_16x16x32_bf16 v[64:67], a[36:39], a[196:199], v[64:67]\n" \
"s_add_u32 s71, s71, %3\n" \
"s_waitcnt vmcnt(14) lgkmcnt(0)\n" \
"s_barrier\n" \
"v_mfma_f32_16x16x32_bf16 v[68:71], a[200:203], a[192:195], v[68:71]\n" \
"ds_read_b64_tr_b16 a[32:33], v209 offset:1024\n" \
"ds_read_b64_tr_b16 a[34:35], v209 offset:4096\n" \
"v_mfma_f32_16x16x32_bf16 v[68:71], a[204:207], a[196:199], v[68:71]\n" \
"ds_read_b64_tr_b16 a[36:37], v209 offset:13312\n" \
"ds_read_b64_tr_b16 a[38:39], v209 offset:16384\n" \
"v_mfma_f32_16x16x32_bf16 v[60:63], a[200:203], a[24:27], v[60:63]\n" \
"s_add_u32 m0, s79, 110592\n" \
"ds_read_b64_tr_b16 a[40:41], v209 offset:0\n" \
"v_add_u32 v211, s71, v236\n" \
"v_mfma_f32_16x16x32_bf16 v[60:63], a[204:207], a[28:31], v[60:63]\n" \
"buffer_load_dwordx4 v192, s[44:47], s73 offen offset:0 lds\n" \
"v_mfma_f32_16x16x32_bf16 v[52:55], a[200:203], a[16:19], v[52:55]\n" \
"s_lshl_b32 s70, s70, 18\n" \
"s_add_u32 s88, s88, s70\n" \
"ds_read_b64_tr_b16 a[42:43], v209 offset:3072\n" \
"v_mfma_f32_16x16x32_bf16 v[52:55], a[204:207], a[20:23], v[52:55]\n" \
"s_and_b32 s70, s61, 1\n" \
"s_lshl_b32 s71, s70, 7\n" \
"ds_read_b64_tr_b16 a[44:45], v209 offset:12288\n" \
"v_mfma_f32_16x16x32_bf16 v[44:47], a[200:203], a[8:11], v[44:47]\n" \
"s_add_u32 s88, s88, s71\n" \
"ds_read_b64_tr_b16 a[46:47], v209 offset:15360\n" \
"v_and_b32 v245, 3, v244\n" \
"v_mfma_f32_16x16x32_bf16 v[44:47], a[204:207], a[12:15], v[44:47]\n" \
"buffer_load_dwordx4 v193, s[44:47], s73 offen offset:1024 lds\n" \
"v_mfma_f32_16x16x32_bf16 v[72:75], a[0:3], a[208:211], v[72:75]\n" \
"ds_read_b64_tr_b16 a[48:49], v210 offset:0\n" \
"v_lshlrev_b32 v245, 3, v245\n" \
"v_mfma_f32_16x16x32_bf16 v[72:75], a[4:7], a[212:215], v[72:75]\n" \
"ds_read_b128 a[0:3], v207 offset:0\n" \
"v_bfe_u32 v246, v244, 2, 2\n" \
"v_mfma_f32_16x16x32_bf16 v[80:83], a[8:11], a[208:211], v[80:83]\n" \
"ds_read_b128 a[4:7], v208 offset:0\n" \
"v_lshl_add_u32 v245, v246, 6, v245\n" \
"v_mfma_f32_16x16x32_bf16 v[80:83], a[12:15], a[212:215], v[80:83]\n" \
"buffer_load_dwordx4 v192, s[44:47], s73 offen offset:2048 lds\n" \
"v_mfma_f32_16x16x32_bf16 v[84:87], a[8:11], a[216:219], v[84:87]\n" \
"ds_read_b64_tr_b16 a[50:51], v210 offset:3072\n" \
"v_bfe_u32 v246, v244, 4, 1\n" \
"v_mfma_f32_16x16x32_bf16 v[84:87], a[12:15], a[220:223], v[84:87]\n" \
"ds_read_b128 a[8:11], v205 offset:0\n" \
"v_lshl_add_u32 v245, v246, 5, v245\n" \
"v_mfma_f32_16x16x32_bf16 v[92:95], a[16:19], a[216:219], v[92:95]\n" \
"ds_read_b128 a[12:15], v206 offset:0\n" \
"v_lshl_add_u32 v245, v246, 8, v245\n" \
"v_mfma_f32_16x16x32_bf16 v[92:95], a[20:23], a[220:223], v[92:95]\n" \
"buffer_load_dwordx4 v193, s[44:47], s73 offen offset:3072 lds\n" \
"v_mfma_f32_16x16x32_bf16 v[88:91], a[16:19], a[208:211], v[88:91]\n" \
"ds_read_b64_tr_b16 a[52:53], v210 offset:12288\n" \
"v_lshrrev_b32 v246, 5, v244\n" \
"v_mfma_f32_16x16x32_bf16 v[88:91], a[20:23], a[212:215], v[88:91]\n" \
"s_add_u32 m0, s89, 110592\n" \
"ds_read_b128 a[16:19], v205 offset:4096\n" \
"v_lshl_add_u32 v245, v246, 12, v245\n" \
"v_mfma_f32_16x16x32_bf16 v[96:99], a[24:27], a[208:211], v[96:99]\n" \
"s_xor_b32 s73, s73, 0x80\n" \
"s_lshl_b32 s71, s70, 10\n" \
"ds_read_b128 a[20:23], v206 offset:4096\n" \
"v_mfma_f32_16x16x32_bf16 v[96:99], a[28:31], a[212:215], v[96:99]\n" \
"buffer_load_dwordx4 v196, s[44:47], s75 offen lds\n" \
"v_mfma_f32_16x16x32_bf16 v[100:103], a[24:27], a[216:219], v[100:103]\n" \
"s_xor_b32 s75, s75, 0x80\n" \
"s_mul_i32 s76, s80, 393216\n" \
"ds_read_b64_tr_b16 a[54:55], v210 offset:15360\n" \
"v_mfma_f32_16x16x32_bf16 v[100:103], a[28:31], a[220:223], v[100:103]\n" \
"s_add_u32 s76, s76, s78\n" \
"s_add_u32 s71, s71, %3\n" \
"ds_read_b128 a[24:27], v205 offset:8192\n" \
"v_mfma_f32_16x16x32_bf16 v[108:111], a[192:195], a[216:219], v[108:111]\n" \
"s_add_u32 m0, s91, 139264\n" \
"ds_read_b128 a[28:31], v206 offset:8192\n" \
"v_add_u32 v212, s71, v245\n" \
"v_mfma_f32_16x16x32_bf16 v[108:111], a[196:199], a[220:223], v[108:111]\n" \
"buffer_load_dwordx4 v197, s[48:51], s76 offen offset:0 lds\n" \
"v_mfma_f32_16x16x32_bf16 v[104:107], a[192:195], a[208:211], v[104:107]\n" \
"s_mov_b32 s69, 1\n" \
"s_mov_b32 s30, 0x0\n" \
"s_mov_b32 s85, 98304\n" \
"v_mfma_f32_16x16x32_bf16 v[104:107], a[196:199], a[212:215], v[104:107]\n" \
"s_waitcnt lgkmcnt(2)\n" \
"s_sub_u32 s85, s85, 32768\n" \
"s_cselect_b32 s85, 131072, s85\n" \
"v_add_u32 v213, s85, v211\n" \
"v_mfma_f32_16x16x32_bf16 v[32:35], a[32:35], a[0:3], v[32:35]\n" \
"s_add_u32 s77, s76, 24576\n" \
"s_mov_b32 s13, 65593856\n" \
"ds_read_b128 a[192:195], v205 offset:12288\n" \
"v_mfma_f32_16x16x32_bf16 v[32:35], a[36:39], a[4:7], v[32:35]\n" \
"buffer_load_dwordx4 v198, s[48:51], s76 offen offset:1024 lds\n" \
"v_mfma_f32_16x16x32_bf16 v[40:43], a[32:35], a[8:11], v[40:43]\n" \
"ds_read_b128 a[196:199], v206 offset:12288\n" \
"v_lshrrev_b32 v228, 1, v234\n" \
"v_mfma_f32_16x16x32_bf16 v[40:43], a[36:39], a[12:15], v[40:43]\n" \
"ds_read_b64_tr_b16 a[200:201], v210 offset:1024\n" \
"v_add_u32 v214, s85, v212\n" \
"v_mfma_f32_16x16x32_bf16 v[0:3], a[40:43], a[8:11], v[0:3]\n" \
"s_xor_b32 s80, s80, 0x1\n" \
"ds_read_b64_tr_b16 a[202:203], v210 offset:4096\n" \
"Lpg8488_%=:\n" \
"Lst1x1_%=:\n" \
"s_cbranch_execz Lpg10801_%=\n" \
"v_xor_b32_e64 v215, 32, v214\n" \
"v_mfma_f32_16x16x32_bf16 v[0:3], a[44:47], a[12:15], v[0:3]\n" \
"buffer_load_dwordx4 v199, s[48:51], s76 offen offset:2048 lds\n" \
"v_mfma_f32_16x16x32_bf16 v[4:7], a[48:51], a[8:11], v[4:7]\n" \
"ds_read_b64_tr_b16 a[204:205], v210 offset:13312\n" \
"v_mov_b32_e64 v230, s60\n" \
"v_mfma_f32_16x16x32_bf16 v[4:7], a[52:55], a[12:15], v[4:7]\n" \
"ds_read_b64_tr_b16 a[206:207], v210 offset:16384\n" \
"v_lshlrev_b32 v231, 28, s61\n" \
"v_mfma_f32_16x16x32_bf16 v[12:15], a[48:51], a[16:19], v[12:15]\n" \
"s_add_u32 m0, m0, 3072\n" \
"ds_read_b64_tr_b16 a[208:209], v209 offset:2048\n" \
"v_mfma_f32_16x16x32_bf16 v[12:15], a[52:55], a[20:23], v[12:15]\n" \
"buffer_load_dwordx4 v197, s[48:51], s77 offen offset:0 lds\n" \
"v_mfma_f32_16x16x32_bf16 v[8:11], a[40:43], a[16:19], v[8:11]\n" \
"ds_read_b64_tr_b16 a[210:211], v209 offset:5120\n" \
"v_mfma_f32_16x16x32_bf16 v[8:11], a[44:47], a[20:23], v[8:11]\n" \
"ds_read_b64_tr_b16 a[212:213], v209 offset:14336\n" \
"v_mfma_f32_16x16x32_bf16 v[48:51], a[32:35], a[16:19], v[48:51]\n" \
"s_waitcnt lgkmcnt(3)\n" \
"ds_read_b64_tr_b16 a[214:215], v209 offset:17408\n" \
"v_mfma_f32_16x16x32_bf16 v[48:51], a[36:39], a[20:23], v[48:51]\n" \
"buffer_load_dwordx4 v198, s[48:51], s77 offen offset:1024 lds\n" \
"v_mfma_f32_16x16x32_bf16 v[56:59], a[32:35], a[24:27], v[56:59]\n" \
"ds_read_b64_tr_b16 a[216:217], v210 offset:2048\n" \
"v_mfma_f32_16x16x32_bf16 v[56:59], a[36:39], a[28:31], v[56:59]\n" \
"ds_read_b64_tr_b16 a[218:219], v210 offset:5120\n" \
"v_mfma_f32_16x16x32_bf16 v[16:19], a[40:43], a[24:27], v[16:19]\n" \
"ds_read_b64_tr_b16 a[220:221], v210 offset:14336\n" \
"v_mfma_f32_16x16x32_bf16 v[16:19], a[44:47], a[28:31], v[16:19]\n" \
"buffer_load_dwordx4 v199, s[48:51], s77 offen offset:2048 lds\n" \
"v_mfma_f32_16x16x32_bf16 v[20:23], a[48:51], a[24:27], v[20:23]\n" \
"ds_read_b64_tr_b16 a[222:223], v210 offset:17408\n" \
"v_mfma_f32_16x16x32_bf16 v[20:23], a[52:55], a[28:31], v[20:23]\n" \
"v_mfma_f32_16x16x32_bf16 v[28:31], a[48:51], a[192:195], v[28:31]\n" \
"v_mfma_f32_16x16x32_bf16 v[28:31], a[52:55], a[196:199], v[28:31]\n" \
"buffer_load_dwordx2 v[226:227], v228, s[56:59], s13 offen sc1\n" \
"v_mfma_f32_16x16x32_bf16 v[24:27], a[40:43], a[192:195], v[24:27]\n" \
"v_mfma_f32_16x16x32_bf16 v[24:27], a[44:47], a[196:199], v[24:27]\n" \
"v_mfma_f32_16x16x32_bf16 v[64:67], a[32:35], a[192:195], v[64:67]\n" \
"v_mfma_f32_16x16x32_bf16 v[64:67], a[36:39], a[196:199], v[64:67]\n" \
"s_waitcnt vmcnt(13) lgkmcnt(0)\n" \
"s_barrier\n" \
"v_mfma_f32_16x16x32_bf16 v[68:71], a[200:203], a[192:195], v[68:71]\n" \
"ds_read_b64_tr_b16 a[32:33], v204 offset:21504\n" \
"ds_read_b64_tr_b16 a[34:35], v204 offset:24576\n" \
"v_mfma_f32_16x16x32_bf16 v[68:71], a[204:207], a[196:199], v[68:71]\n" \
"ds_read_b64_tr_b16 a[36:37], v204 offset:33792\n" \
"ds_read_b64_tr_b16 a[38:39], v204 offset:36864\n" \
"v_mfma_f32_16x16x32_bf16 v[60:63], a[200:203], a[24:27], v[60:63]\n" \
"s_add_u32 m0, s79, 65536\n" \
"ds_read_b64_tr_b16 a[40:41], v204 offset:20480\n" \
"v_mfma_f32_16x16x32_bf16 v[60:63], a[204:207], a[28:31], v[60:63]\n" \
"buffer_load_dwordx4 v192, s[44:47], s73 offen offset:0 lds\n" \
"v_mfma_f32_16x16x32_bf16 v[52:55], a[200:203], a[16:19], v[52:55]\n" \
"ds_read_b64_tr_b16 a[42:43], v204 offset:23552\n" \
"v_mfma_f32_16x16x32_bf16 v[52:55], a[204:207], a[20:23], v[52:55]\n" \
"ds_read_b64_tr_b16 a[44:45], v204 offset:32768\n" \
"v_mfma_f32_16x16x32_bf16 v[44:47], a[200:203], a[8:11], v[44:47]\n" \
"ds_read_b64_tr_b16 a[46:47], v204 offset:35840\n" \
"v_mfma_f32_16x16x32_bf16 v[44:47], a[204:207], a[12:15], v[44:47]\n" \
"buffer_load_dwordx4 v193, s[44:47], s73 offen offset:1024 lds\n" \
"v_mfma_f32_16x16x32_bf16 v[72:75], a[0:3], a[208:211], v[72:75]\n" \
"ds_read_b64_tr_b16 a[48:49], v222 offset:20480\n" \
"v_mfma_f32_16x16x32_bf16 v[72:75], a[4:7], a[212:215], v[72:75]\n" \
"ds_read_b128 a[0:3], v202 offset:0\n" \
"v_mfma_f32_16x16x32_bf16 v[80:83], a[8:11], a[208:211], v[80:83]\n" \
"ds_read_b128 a[4:7], v203 offset:0\n" \
"v_mfma_f32_16x16x32_bf16 v[80:83], a[12:15], a[212:215], v[80:83]\n" \
"buffer_load_dwordx4 v192, s[44:47], s73 offen offset:2048 lds\n" \
"v_mfma_f32_16x16x32_bf16 v[84:87], a[8:11], a[216:219], v[84:87]\n" \
"ds_read_b64_tr_b16 a[50:51], v222 offset:23552\n" \
"v_mfma_f32_16x16x32_bf16 v[84:87], a[12:15], a[220:223], v[84:87]\n" \
"ds_read_b128 a[8:11], v200 offset:0\n" \
"v_mfma_f32_16x16x32_bf16 v[92:95], a[16:19], a[216:219], v[92:95]\n" \
"ds_read_b128 a[12:15], v201 offset:0\n" \
"v_mfma_f32_16x16x32_bf16 v[92:95], a[20:23], a[220:223], v[92:95]\n" \
"buffer_load_dwordx4 v193, s[44:47], s73 offen offset:3072 lds\n" \
"v_mfma_f32_16x16x32_bf16 v[88:91], a[16:19], a[208:211], v[88:91]\n" \
"ds_read_b64_tr_b16 a[52:53], v222 offset:32768\n" \
"v_mfma_f32_16x16x32_bf16 v[88:91], a[20:23], a[212:215], v[88:91]\n" \
"s_add_u32 m0, s89, 65536\n" \
"ds_read_b128 a[16:19], v200 offset:4096\n" \
"v_mfma_f32_16x16x32_bf16 v[96:99], a[24:27], a[208:211], v[96:99]\n" \
"s_xor_b32 s73, s73, 0x180\n" \
"ds_read_b128 a[20:23], v201 offset:4096\n" \
"v_mfma_f32_16x16x32_bf16 v[96:99], a[28:31], a[212:215], v[96:99]\n" \
"buffer_load_dwordx4 v196, s[44:47], s75 offen lds\n" \
"v_mfma_f32_16x16x32_bf16 v[100:103], a[24:27], a[216:219], v[100:103]\n" \
"s_xor_b32 s75, s75, 0x180\n" \
"s_mul_i32 s76, s80, 393216\n" \
"ds_read_b64_tr_b16 a[54:55], v222 offset:35840\n" \
"v_mfma_f32_16x16x32_bf16 v[100:103], a[28:31], a[220:223], v[100:103]\n" \
"s_add_u32 s76, s76, s78\n" \
"ds_read_b128 a[24:27], v200 offset:8192\n" \
"v_mfma_f32_16x16x32_bf16 v[108:111], a[192:195], a[216:219], v[108:111]\n" \
"s_add_u32 m0, s91, 86016\n" \
"ds_read_b128 a[28:31], v201 offset:8192\n" \
"v_mfma_f32_16x16x32_bf16 v[108:111], a[196:199], a[220:223], v[108:111]\n" \
"buffer_load_dwordx4 v197, s[48:51], s76 offen offset:0 lds\n" \
"v_mfma_f32_16x16x32_bf16 v[104:107], a[192:195], a[208:211], v[104:107]\n" \
"v_mfma_f32_16x16x32_bf16 v[104:107], a[196:199], a[212:215], v[104:107]\n" \
"s_waitcnt lgkmcnt(2)\n" \
"v_mfma_f32_16x16x32_bf16 v[32:35], a[32:35], a[0:3], v[32:35]\n" \
"s_add_u32 s77, s76, 24576\n" \
"ds_read_b128 a[192:195], v200 offset:12288\n" \
"v_mfma_f32_16x16x32_bf16 v[32:35], a[36:39], a[4:7], v[32:35]\n" \
"buffer_load_dwordx4 v198, s[48:51], s76 offen offset:1024 lds\n" \
"v_mfma_f32_16x16x32_bf16 v[40:43], a[32:35], a[8:11], v[40:43]\n" \
"ds_read_b128 a[196:199], v201 offset:12288\n" \
"v_mfma_f32_16x16x32_bf16 v[40:43], a[36:39], a[12:15], v[40:43]\n" \
"ds_read_b64_tr_b16 a[200:201], v222 offset:21504\n" \
"v_mfma_f32_16x16x32_bf16 v[0:3], a[40:43], a[8:11], v[0:3]\n" \
"s_xor_b32 s80, s80, 0x3\n" \
"ds_read_b64_tr_b16 a[202:203], v222 offset:24576\n" \
"v_mfma_f32_16x16x32_bf16 v[0:3], a[44:47], a[12:15], v[0:3]\n" \
"buffer_load_dwordx4 v199, s[48:51], s76 offen offset:2048 lds\n" \
"v_mfma_f32_16x16x32_bf16 v[4:7], a[48:51], a[8:11], v[4:7]\n" \
"ds_read_b64_tr_b16 a[204:205], v222 offset:33792\n" \
"v_mfma_f32_16x16x32_bf16 v[4:7], a[52:55], a[12:15], v[4:7]\n" \
"ds_read_b64_tr_b16 a[206:207], v222 offset:36864\n" \
"v_mfma_f32_16x16x32_bf16 v[12:15], a[48:51], a[16:19], v[12:15]\n" \
"s_add_u32 m0, m0, 3072\n" \
"ds_read_b64_tr_b16 a[208:209], v204 offset:22528\n" \
"v_mfma_f32_16x16x32_bf16 v[12:15], a[52:55], a[20:23], v[12:15]\n" \
"buffer_load_dwordx4 v197, s[48:51], s77 offen offset:0 lds\n" \
"v_mfma_f32_16x16x32_bf16 v[8:11], a[40:43], a[16:19], v[8:11]\n" \
"ds_read_b64_tr_b16 a[210:211], v204 offset:25600\n" \
"v_mfma_f32_16x16x32_bf16 v[8:11], a[44:47], a[20:23], v[8:11]\n" \
"ds_read_b64_tr_b16 a[212:213], v204 offset:34816\n" \
"v_mfma_f32_16x16x32_bf16 v[48:51], a[32:35], a[16:19], v[48:51]\n" \
"s_waitcnt lgkmcnt(3)\n" \
"ds_read_b64_tr_b16 a[214:215], v204 offset:37888\n" \
"v_mfma_f32_16x16x32_bf16 v[48:51], a[36:39], a[20:23], v[48:51]\n" \
"buffer_load_dwordx4 v198, s[48:51], s77 offen offset:1024 lds\n" \
"v_mfma_f32_16x16x32_bf16 v[56:59], a[32:35], a[24:27], v[56:59]\n" \
"ds_read_b64_tr_b16 a[216:217], v222 offset:22528\n" \
"v_mfma_f32_16x16x32_bf16 v[56:59], a[36:39], a[28:31], v[56:59]\n" \
"ds_read_b64_tr_b16 a[218:219], v222 offset:25600\n" \
"v_mfma_f32_16x16x32_bf16 v[16:19], a[40:43], a[24:27], v[16:19]\n" \
"ds_read_b64_tr_b16 a[220:221], v222 offset:34816\n" \
"v_mfma_f32_16x16x32_bf16 v[16:19], a[44:47], a[28:31], v[16:19]\n" \
"buffer_load_dwordx4 v199, s[48:51], s77 offen offset:2048 lds\n" \
"v_mfma_f32_16x16x32_bf16 v[20:23], a[48:51], a[24:27], v[20:23]\n" \
"ds_read_b64_tr_b16 a[222:223], v222 offset:37888\n" \
"v_mfma_f32_16x16x32_bf16 v[20:23], a[52:55], a[28:31], v[20:23]\n" \
"v_mfma_f32_16x16x32_bf16 v[28:31], a[48:51], a[192:195], v[28:31]\n" \
"v_mfma_f32_16x16x32_bf16 v[28:31], a[52:55], a[196:199], v[28:31]\n" \
"v_mfma_f32_16x16x32_bf16 v[24:27], a[40:43], a[192:195], v[24:27]\n" \
"v_mfma_f32_16x16x32_bf16 v[24:27], a[44:47], a[196:199], v[24:27]\n" \
"v_mfma_f32_16x16x32_bf16 v[64:67], a[32:35], a[192:195], v[64:67]\n" \
"v_mfma_f32_16x16x32_bf16 v[64:67], a[36:39], a[196:199], v[64:67]\n" \
"s_waitcnt vmcnt(12) lgkmcnt(0)\n" \
"s_barrier\n" \
"v_mfma_f32_16x16x32_bf16 v[68:71], a[200:203], a[192:195], v[68:71]\n" \
"ds_read_b64_tr_b16 a[32:33], v194 offset:1024\n" \
"ds_read_b64_tr_b16 a[34:35], v194 offset:4096\n" \
"v_mfma_f32_16x16x32_bf16 v[68:71], a[204:207], a[196:199], v[68:71]\n" \
"ds_read_b64_tr_b16 a[36:37], v194 offset:13312\n" \
"ds_read_b64_tr_b16 a[38:39], v194 offset:16384\n" \
"v_mfma_f32_16x16x32_bf16 v[60:63], a[200:203], a[24:27], v[60:63]\n" \
"s_add_u32 m0, s79, 0\n" \
"ds_read_b64_tr_b16 a[40:41], v194 offset:0\n" \
"v_mfma_f32_16x16x32_bf16 v[60:63], a[204:207], a[28:31], v[60:63]\n" \
"buffer_load_dwordx4 v192, s[44:47], s73 offen offset:0 lds\n" \
"v_mfma_f32_16x16x32_bf16 v[52:55], a[200:203], a[16:19], v[52:55]\n" \
"ds_read_b64_tr_b16 a[42:43], v194 offset:3072\n" \
"v_mfma_f32_16x16x32_bf16 v[52:55], a[204:207], a[20:23], v[52:55]\n" \
"ds_read_b64_tr_b16 a[44:45], v194 offset:12288\n" \
"v_mfma_f32_16x16x32_bf16 v[44:47], a[200:203], a[8:11], v[44:47]\n" \
"ds_read_b64_tr_b16 a[46:47], v194 offset:15360\n" \
"v_mfma_f32_16x16x32_bf16 v[44:47], a[204:207], a[12:15], v[44:47]\n" \
"buffer_load_dwordx4 v193, s[44:47], s73 offen offset:1024 lds\n" \
"v_mfma_f32_16x16x32_bf16 v[72:75], a[0:3], a[208:211], v[72:75]\n" \
"ds_read_b64_tr_b16 a[48:49], v195 offset:0\n" \
"v_mfma_f32_16x16x32_bf16 v[72:75], a[4:7], a[212:215], v[72:75]\n" \
"ds_read_b128 a[0:3], v207 offset:45056\n" \
"v_mfma_f32_16x16x32_bf16 v[80:83], a[8:11], a[208:211], v[80:83]\n" \
"ds_read_b128 a[4:7], v208 offset:45056\n" \
"v_mfma_f32_16x16x32_bf16 v[80:83], a[12:15], a[212:215], v[80:83]\n" \
"buffer_load_dwordx4 v192, s[44:47], s73 offen offset:2048 lds\n" \
"v_mfma_f32_16x16x32_bf16 v[84:87], a[8:11], a[216:219], v[84:87]\n" \
"ds_read_b64_tr_b16 a[50:51], v195 offset:3072\n" \
"v_mfma_f32_16x16x32_bf16 v[84:87], a[12:15], a[220:223], v[84:87]\n" \
"ds_read_b128 a[8:11], v205 offset:45056\n" \
"v_mfma_f32_16x16x32_bf16 v[92:95], a[16:19], a[216:219], v[92:95]\n" \
"ds_read_b128 a[12:15], v206 offset:45056\n" \
"v_mfma_f32_16x16x32_bf16 v[92:95], a[20:23], a[220:223], v[92:95]\n" \
"buffer_load_dwordx4 v193, s[44:47], s73 offen offset:3072 lds\n" \
"v_mfma_f32_16x16x32_bf16 v[88:91], a[16:19], a[208:211], v[88:91]\n" \
"ds_read_b64_tr_b16 a[52:53], v195 offset:12288\n" \
"v_mfma_f32_16x16x32_bf16 v[88:91], a[20:23], a[212:215], v[88:91]\n" \
"s_add_u32 m0, s89, 0\n" \
"ds_read_b128 a[16:19], v205 offset:49152\n" \
"v_mfma_f32_16x16x32_bf16 v[96:99], a[24:27], a[208:211], v[96:99]\n" \
"s_xor_b32 s73, s73, 0x300\n" \
"ds_read_b128 a[20:23], v206 offset:49152\n" \
"v_mfma_f32_16x16x32_bf16 v[96:99], a[28:31], a[212:215], v[96:99]\n" \
"buffer_load_dwordx4 v196, s[44:47], s75 offen lds\n" \
"v_mfma_f32_16x16x32_bf16 v[100:103], a[24:27], a[216:219], v[100:103]\n" \
"s_xor_b32 s75, s75, 0x300\n" \
"s_mul_i32 s76, s80, 393216\n" \
"ds_read_b64_tr_b16 a[54:55], v195 offset:15360\n" \
"v_mfma_f32_16x16x32_bf16 v[100:103], a[28:31], a[220:223], v[100:103]\n" \
"s_add_u32 s76, s76, s78\n" \
"ds_read_b128 a[24:27], v205 offset:53248\n" \
"v_mfma_f32_16x16x32_bf16 v[108:111], a[192:195], a[216:219], v[108:111]\n" \
"s_add_u32 m0, s91, 20480\n" \
"ds_read_b128 a[28:31], v206 offset:53248\n" \
"v_mfma_f32_16x16x32_bf16 v[108:111], a[196:199], a[220:223], v[108:111]\n" \
"buffer_load_dwordx4 v197, s[48:51], s76 offen offset:0 lds\n" \
"v_mfma_f32_16x16x32_bf16 v[104:107], a[192:195], a[208:211], v[104:107]\n" \
"v_mfma_f32_16x16x32_bf16 v[104:107], a[196:199], a[212:215], v[104:107]\n" \
"s_waitcnt lgkmcnt(2)\n" \
"v_mfma_f32_16x16x32_bf16 v[32:35], a[32:35], a[0:3], v[32:35]\n" \
"s_add_u32 s77, s76, 24576\n" \
"ds_read_b128 a[192:195], v205 offset:57344\n" \
"v_mfma_f32_16x16x32_bf16 v[32:35], a[36:39], a[4:7], v[32:35]\n" \
"buffer_load_dwordx4 v198, s[48:51], s76 offen offset:1024 lds\n" \
"v_mfma_f32_16x16x32_bf16 v[40:43], a[32:35], a[8:11], v[40:43]\n" \
"ds_read_b128 a[196:199], v206 offset:57344\n" \
"v_mfma_f32_16x16x32_bf16 v[40:43], a[36:39], a[12:15], v[40:43]\n" \
"ds_read_b64_tr_b16 a[200:201], v195 offset:1024\n" \
"v_mfma_f32_16x16x32_bf16 v[0:3], a[40:43], a[8:11], v[0:3]\n" \
"s_xor_b32 s80, s80, 0x6\n" \
"ds_read_b64_tr_b16 a[202:203], v195 offset:4096\n" \
"v_mfma_f32_16x16x32_bf16 v[0:3], a[44:47], a[12:15], v[0:3]\n" \
"buffer_load_dwordx4 v199, s[48:51], s76 offen offset:2048 lds\n" \
"v_mfma_f32_16x16x32_bf16 v[4:7], a[48:51], a[8:11], v[4:7]\n" \
"ds_read_b64_tr_b16 a[204:205], v195 offset:13312\n" \
"v_mfma_f32_16x16x32_bf16 v[4:7], a[52:55], a[12:15], v[4:7]\n" \
"ds_read_b64_tr_b16 a[206:207], v195 offset:16384\n" \
"v_mfma_f32_16x16x32_bf16 v[12:15], a[48:51], a[16:19], v[12:15]\n" \
"s_add_u32 m0, m0, 3072\n" \
"ds_read_b64_tr_b16 a[208:209], v194 offset:2048\n" \
"v_mfma_f32_16x16x32_bf16 v[12:15], a[52:55], a[20:23], v[12:15]\n" \
"buffer_load_dwordx4 v197, s[48:51], s77 offen offset:0 lds\n" \
"v_mfma_f32_16x16x32_bf16 v[8:11], a[40:43], a[16:19], v[8:11]\n" \
"ds_read_b64_tr_b16 a[210:211], v194 offset:5120\n" \
"v_mfma_f32_16x16x32_bf16 v[8:11], a[44:47], a[20:23], v[8:11]\n" \
"ds_read_b64_tr_b16 a[212:213], v194 offset:14336\n" \
"v_mfma_f32_16x16x32_bf16 v[48:51], a[32:35], a[16:19], v[48:51]\n" \
"s_waitcnt lgkmcnt(3)\n" \
"ds_read_b64_tr_b16 a[214:215], v194 offset:17408\n" \
"v_mfma_f32_16x16x32_bf16 v[48:51], a[36:39], a[20:23], v[48:51]\n" \
"buffer_load_dwordx4 v198, s[48:51], s77 offen offset:1024 lds\n" \
"v_mfma_f32_16x16x32_bf16 v[56:59], a[32:35], a[24:27], v[56:59]\n" \
"ds_read_b64_tr_b16 a[216:217], v195 offset:2048\n" \
"v_mfma_f32_16x16x32_bf16 v[56:59], a[36:39], a[28:31], v[56:59]\n" \
"ds_read_b64_tr_b16 a[218:219], v195 offset:5120\n" \
"v_mfma_f32_16x16x32_bf16 v[16:19], a[40:43], a[24:27], v[16:19]\n" \
"ds_read_b64_tr_b16 a[220:221], v195 offset:14336\n" \
"v_mfma_f32_16x16x32_bf16 v[16:19], a[44:47], a[28:31], v[16:19]\n" \
"buffer_load_dwordx4 v199, s[48:51], s77 offen offset:2048 lds\n" \
"v_mfma_f32_16x16x32_bf16 v[20:23], a[48:51], a[24:27], v[20:23]\n" \
"ds_read_b64_tr_b16 a[222:223], v195 offset:17408\n" \
"v_mfma_f32_16x16x32_bf16 v[20:23], a[52:55], a[28:31], v[20:23]\n" \
"v_mfma_f32_16x16x32_bf16 v[28:31], a[48:51], a[192:195], v[28:31]\n" \
"v_mfma_f32_16x16x32_bf16 v[28:31], a[52:55], a[196:199], v[28:31]\n" \
"v_mfma_f32_16x16x32_bf16 v[24:27], a[40:43], a[192:195], v[24:27]\n" \
"v_mfma_f32_16x16x32_bf16 v[24:27], a[44:47], a[196:199], v[24:27]\n" \
"v_mfma_f32_16x16x32_bf16 v[64:67], a[32:35], a[192:195], v[64:67]\n" \
"v_mfma_f32_16x16x32_bf16 v[64:67], a[36:39], a[196:199], v[64:67]\n" \
"s_waitcnt vmcnt(11) lgkmcnt(0)\n" \
"s_barrier\n" \
"v_mfma_f32_16x16x32_bf16 v[68:71], a[200:203], a[192:195], v[68:71]\n" \
"ds_read_b64_tr_b16 a[32:33], v209 offset:1024\n" \
"ds_read_b64_tr_b16 a[34:35], v209 offset:4096\n" \
"v_mfma_f32_16x16x32_bf16 v[68:71], a[204:207], a[196:199], v[68:71]\n" \
"ds_read_b64_tr_b16 a[36:37], v209 offset:13312\n" \
"ds_read_b64_tr_b16 a[38:39], v209 offset:16384\n" \
"v_mfma_f32_16x16x32_bf16 v[60:63], a[200:203], a[24:27], v[60:63]\n" \
"s_add_u32 m0, s79, 110592\n" \
"ds_read_b64_tr_b16 a[40:41], v209 offset:0\n" \
"v_cmp_eq_u32 vcc, s60, v226\n" \
"v_mfma_f32_16x16x32_bf16 v[60:63], a[204:207], a[28:31], v[60:63]\n" \
"buffer_load_dwordx4 v192, s[44:47], s73 offen offset:0 lds\n" \
"v_mfma_f32_16x16x32_bf16 v[52:55], a[200:203], a[16:19], v[52:55]\n" \
"s_andn2_b32 s70, 0xff, vcc_lo\n" \
"s_cbranch_scc1 Lpl4_%=\n" \
"Lpr4_%=:\n" \
"ds_read_b64_tr_b16 a[42:43], v209 offset:3072\n" \
"v_mfma_f32_16x16x32_bf16 v[52:55], a[204:207], a[20:23], v[52:55]\n" \
"s_and_b32 s70, %1, 7\n" \
"ds_read_b64_tr_b16 a[44:45], v209 offset:12288\n" \
"v_readlane_b32 s70, v227, s70\n" \
"v_mfma_f32_16x16x32_bf16 v[44:47], a[200:203], a[8:11], v[44:47]\n" \
"ds_read_b64_tr_b16 a[46:47], v209 offset:15360\n" \
"v_mfma_f32_16x16x32_bf16 v[44:47], a[204:207], a[12:15], v[44:47]\n" \
"buffer_load_dwordx4 v193, s[44:47], s73 offen offset:1024 lds\n" \
"v_mfma_f32_16x16x32_bf16 v[72:75], a[0:3], a[208:211], v[72:75]\n" \
"ds_read_b64_tr_b16 a[48:49], v210 offset:0\n" \
"v_subrev_u32 v227, s70, v227\n" \
"v_mfma_f32_16x16x32_bf16 v[72:75], a[4:7], a[212:215], v[72:75]\n" \
"ds_read_b128 a[0:3], v207 offset:0\n" \
"v_cmp_gt_i32 vcc, 0, v227\n" \
"v_mfma_f32_16x16x32_bf16 v[80:83], a[8:11], a[208:211], v[80:83]\n" \
"ds_read_b128 a[4:7], v208 offset:0\n" \
"v_mfma_f32_16x16x32_bf16 v[80:83], a[12:15], a[212:215], v[80:83]\n" \
"buffer_load_dwordx4 v192, s[44:47], s73 offen offset:2048 lds\n" \
"v_mfma_f32_16x16x32_bf16 v[84:87], a[8:11], a[216:219], v[84:87]\n" \
"s_and_b32 s13, vcc_lo, 0xff\n" \
"s_bcnt1_i32_b32 s13, s13\n" \
"ds_read_b64_tr_b16 a[50:51], v210 offset:3072\n" \
"v_mfma_f32_16x16x32_bf16 v[84:87], a[12:15], a[220:223], v[84:87]\n" \
"s_lshr_b32 s90, s13, 2\n" \
"s_mul_i32 s70, s90, 7\n" \
"ds_read_b128 a[8:11], v205 offset:0\n" \
"v_mfma_f32_16x16x32_bf16 v[92:95], a[16:19], a[216:219], v[92:95]\n" \
"s_xor_b32 s13, s13, s70\n" \
"s_lshl_b32 s70, s13, 3\n" \
"ds_read_b128 a[12:15], v206 offset:0\n" \
"v_mfma_f32_16x16x32_bf16 v[92:95], a[20:23], a[220:223], v[92:95]\n" \
"buffer_load_dwordx4 v193, s[44:47], s73 offen offset:3072 lds\n" \
"v_mfma_f32_16x16x32_bf16 v[88:91], a[16:19], a[208:211], v[88:91]\n" \
"s_lshr_b32 s9, 0x2a2b2d2f, s70\n" \
"s_and_b32 s9, s9, 0xff\n" \
"ds_read_b64_tr_b16 a[52:53], v210 offset:12288\n" \
"v_mfma_f32_16x16x32_bf16 v[88:91], a[20:23], a[212:215], v[88:91]\n" \
"s_add_u32 m0, s89, 110592\n" \
"s_lshr_b32 s8, %1, 6\n" \
"ds_read_b128 a[16:19], v205 offset:4096\n" \
"v_mfma_f32_16x16x32_bf16 v[96:99], a[24:27], a[208:211], v[96:99]\n" \
"s_xor_b32 s73, s73, 0x80\n" \
"s_lshl2_add_u32 s8, s13, s8\n" \
"ds_read_b128 a[20:23], v206 offset:4096\n" \
"v_mfma_f32_16x16x32_bf16 v[96:99], a[28:31], a[212:215], v[96:99]\n" \
"buffer_load_dwordx4 v196, s[44:47], s75 offen lds\n" \
"v_mfma_f32_16x16x32_bf16 v[100:103], a[24:27], a[216:219], v[100:103]\n" \
"s_xor_b32 s75, s75, 0x80\n" \
"s_mul_i32 s76, s80, 393216\n" \
"ds_read_b64_tr_b16 a[54:55], v210 offset:15360\n" \
"v_mfma_f32_16x16x32_bf16 v[100:103], a[28:31], a[220:223], v[100:103]\n" \
"s_add_u32 s76, s76, s78\n" \
"ds_read_b128 a[24:27], v205 offset:8192\n" \
"v_max_i32_dpp v226, v227, v227 quad_perm:[1,0,3,2] row_mask:0xf bank_mask:0xf\n" \
"v_mfma_f32_16x16x32_bf16 v[108:111], a[192:195], a[216:219], v[108:111]\n" \
"s_add_u32 m0, s91, 139264\n" \
"s_mov_b32 s14, 0\n" \
"ds_read_b128 a[28:31], v206 offset:8192\n" \
"v_mfma_f32_16x16x32_bf16 v[108:111], a[196:199], a[220:223], v[108:111]\n" \
"buffer_load_dwordx4 v197, s[48:51], s76 offen offset:0 lds\n" \
"v_mfma_f32_16x16x32_bf16 v[104:107], a[192:195], a[208:211], v[104:107]\n" \
"v_readlane_b32 s15, v226, 2\n" \
"v_readlane_b32 s16, v226, 4\n" \
"v_mfma_f32_16x16x32_bf16 v[104:107], a[196:199], a[212:215], v[104:107]\n" \
"s_waitcnt lgkmcnt(2)\n" \
"v_readlane_b32 s17, v226, 6\n" \
"v_cmp_gt_i32 vcc, s15, v226\n" \
"v_mfma_f32_16x16x32_bf16 v[32:35], a[32:35], a[0:3], v[32:35]\n" \
"s_add_u32 s77, s76, 24576\n" \
"ds_read_b128 a[192:195], v205 offset:12288\n" \
"v_mfma_f32_16x16x32_bf16 v[32:35], a[36:39], a[4:7], v[32:35]\n" \
"buffer_load_dwordx4 v198, s[48:51], s76 offen offset:1024 lds\n" \
"v_mfma_f32_16x16x32_bf16 v[40:43], a[32:35], a[8:11], v[40:43]\n" \
"s_and_b32 s70, vcc_lo, 0x55\n" \
"s_bcnt1_i32_b32 s70, s70\n" \
"ds_read_b128 a[196:199], v206 offset:12288\n" \
"v_mfma_f32_16x16x32_bf16 v[40:43], a[36:39], a[12:15], v[40:43]\n" \
"s_cmp_eq_u32 s70, s13\n" \
"s_cselect_b32 s14, 4, s14\n" \
"ds_read_b64_tr_b16 a[200:201], v210 offset:1024\n" \
"v_mfma_f32_16x16x32_bf16 v[0:3], a[40:43], a[8:11], v[0:3]\n" \
"s_xor_b32 s80, s80, 0x1\n" \
"ds_read_b64_tr_b16 a[202:203], v210 offset:4096\n" \
"v_cmp_gt_i32 vcc, s16, v226\n" \
"v_mfma_f32_16x16x32_bf16 v[0:3], a[44:47], a[12:15], v[0:3]\n" \
"buffer_load_dwordx4 v199, s[48:51], s76 offen offset:2048 lds\n" \
"v_mfma_f32_16x16x32_bf16 v[4:7], a[48:51], a[8:11], v[4:7]\n" \
"s_and_b32 s70, vcc_lo, 0x55\n" \
"s_bcnt1_i32_b32 s70, s70\n" \
"ds_read_b64_tr_b16 a[204:205], v210 offset:13312\n" \
"v_mfma_f32_16x16x32_bf16 v[4:7], a[52:55], a[12:15], v[4:7]\n" \
"s_cmp_eq_u32 s70, s13\n" \
"s_cselect_b32 s14, 8, s14\n" \
"ds_read_b64_tr_b16 a[206:207], v210 offset:16384\n" \
"v_mfma_f32_16x16x32_bf16 v[12:15], a[48:51], a[16:19], v[12:15]\n" \
"s_add_u32 m0, m0, 3072\n" \
"ds_read_b64_tr_b16 a[208:209], v209 offset:2048\n" \
"v_cmp_gt_i32 vcc, s17, v226\n" \
"v_mfma_f32_16x16x32_bf16 v[12:15], a[52:55], a[20:23], v[12:15]\n" \
"buffer_load_dwordx4 v197, s[48:51], s77 offen offset:0 lds\n" \
"v_mfma_f32_16x16x32_bf16 v[8:11], a[40:43], a[16:19], v[8:11]\n" \
"s_and_b32 s70, vcc_lo, 0x55\n" \
"s_bcnt1_i32_b32 s70, s70\n" \
"ds_read_b64_tr_b16 a[210:211], v209 offset:5120\n" \
"v_mfma_f32_16x16x32_bf16 v[8:11], a[44:47], a[20:23], v[8:11]\n" \
"s_cmp_eq_u32 s70, s13\n" \
"s_cselect_b32 s14, 12, s14\n" \
"ds_read_b64_tr_b16 a[212:213], v209 offset:14336\n" \
"v_mfma_f32_16x16x32_bf16 v[48:51], a[32:35], a[16:19], v[48:51]\n" \
"s_lshr_b32 s70, %1, 6\n" \
"s_add_u32 s14, s14, s70\n" \
"ds_read_b64_tr_b16 a[214:215], v209 offset:17408\n" \
"v_mfma_f32_16x16x32_bf16 v[48:51], a[36:39], a[20:23], v[48:51]\n" \
"s_waitcnt lgkmcnt(3)\n" \
"buffer_load_dwordx4 v198, s[48:51], s77 offen offset:1024 lds\n" \
"v_mfma_f32_16x16x32_bf16 v[56:59], a[32:35], a[24:27], v[56:59]\n" \
"s_cmp_eq_u32 s90, 0\n" \
"s_cselect_b32 s14, s8, s14\n" \
"ds_read_b64_tr_b16 a[216:217], v210 offset:2048\n" \
"v_mfma_f32_16x16x32_bf16 v[56:59], a[36:39], a[28:31], v[56:59]\n" \
"s_sub_u32 s10, 49, s9\n" \
"s_lshl_b32 s11, s9, 1\n" \
"ds_read_b64_tr_b16 a[218:219], v210 offset:5120\n" \
"v_mfma_f32_16x16x32_bf16 v[16:19], a[40:43], a[24:27], v[16:19]\n" \
"s_sub_u32 s11, s11, 71\n" \
"s_lshl2_add_u32 s70, s8, s61\n" \
"ds_read_b64_tr_b16 a[220:221], v210 offset:14336\n" \
"v_mfma_f32_16x16x32_bf16 v[16:19], a[44:47], a[28:31], v[16:19]\n" \
"buffer_load_dwordx4 v199, s[48:51], s77 offen offset:2048 lds\n" \
"v_mfma_f32_16x16x32_bf16 v[20:23], a[48:51], a[24:27], v[20:23]\n" \
"s_mul_i32 s81, s70, 266240\n" \
"s_mul_i32 s82, s90, s9\n" \
"ds_read_b64_tr_b16 a[222:223], v210 offset:17408\n" \
"v_mfma_f32_16x16x32_bf16 v[20:23], a[52:55], a[28:31], v[20:23]\n" \
"s_lshl_b32 s71, s82, 12\n" \
"s_add_u32 s81, s81, s71\n" \
"s_add_u32 s81, s81, 35700736\n" \
"v_mfma_f32_16x16x32_bf16 v[28:31], a[48:51], a[192:195], v[28:31]\n" \
"s_lshl2_add_u32 s70, s14, s61\n" \
"s_mul_i32 s83, s70, 69632\n" \
"s_add_u32 s83, s83, 52740096\n" \
"v_mfma_f32_16x16x32_bf16 v[28:31], a[52:55], a[196:199], v[28:31]\n" \
"s_lshl_b32 s82, s82, 17\n" \
"s_add_u32 s82, s82, s84\n" \
"s_lshl_b32 s70, s14, 19\n" \
"v_mfma_f32_16x16x32_bf16 v[24:27], a[40:43], a[192:195], v[24:27]\n" \
"s_add_u32 s88, s88, s70\n" \
"s_lshl_b32 s71, s90, 11\n" \
"s_add_u32 s88, s88, s71\n" \
"v_mfma_f32_16x16x32_bf16 v[24:27], a[44:47], a[196:199], v[24:27]\n" \
"s_lshl3_add_u32 s70, s8, s68\n" \
"s_lshl2_add_u32 s87, s70, s61\n" \
"s_lshl_b32 s87, s87, 14\n" \
"v_mfma_f32_16x16x32_bf16 v[64:67], a[32:35], a[192:195], v[64:67]\n" \
"s_add_u32 s87, s87, 57196544\n" \
"s_lshl_b32 s64, s70, 7\n" \
"s_add_u32 s64, s64, 65601536\n" \
"v_mfma_f32_16x16x32_bf16 v[64:67], a[36:39], a[196:199], v[64:67]\n" \
"s_lshl_b32 s66, s8, 7\n" \
"s_waitcnt vmcnt(11) lgkmcnt(0)\n" \
"s_barrier\n" \
"v_mfma_f32_16x16x32_bf16 v[68:71], a[200:203], a[192:195], v[68:71]\n" \
"s_lshl_b32 s67, s14, 7\n" \
"ds_read_b64_tr_b16 a[32:33], v204 offset:21504\n" \
"ds_read_b64_tr_b16 a[34:35], v204 offset:24576\n" \
"v_mfma_f32_16x16x32_bf16 v[68:71], a[204:207], a[196:199], v[68:71]\n" \
"s_add_u32 s67, s67, 65591296\n" \
"ds_read_b64_tr_b16 a[36:37], v204 offset:33792\n" \
"ds_read_b64_tr_b16 a[38:39], v204 offset:36864\n" \
"v_mfma_f32_16x16x32_bf16 v[60:63], a[200:203], a[24:27], v[60:63]\n" \
"s_add_u32 m0, s79, 65536\n" \
"s_add_u32 s66, s66, 65589248\n" \
"ds_read_b64_tr_b16 a[40:41], v204 offset:20480\n" \
"v_mfma_f32_16x16x32_bf16 v[60:63], a[204:207], a[28:31], v[60:63]\n" \
"buffer_load_dwordx4 v192, s[44:47], s73 offen offset:0 lds\n" \
"v_mfma_f32_16x16x32_bf16 v[52:55], a[200:203], a[16:19], v[52:55]\n" \
"s_add_u32 s70, s9, 1\n" \
"s_sub_u32 s70, s70, s90\n" \
"ds_read_b64_tr_b16 a[42:43], v204 offset:23552\n" \
"v_mfma_f32_16x16x32_bf16 v[52:55], a[204:207], a[20:23], v[52:55]\n" \
"s_lshr_b32 s70, s70, 1\n" \
"s_bfm_b32 s65, s70, 0\n" \
"ds_read_b64_tr_b16 a[44:45], v204 offset:32768\n" \
"v_mfma_f32_16x16x32_bf16 v[44:47], a[200:203], a[8:11], v[44:47]\n" \
"s_sub_u32 s70, 0, s90\n" \
"s_xor_b32 s65, s65, s70\n" \
"ds_read_b64_tr_b16 a[46:47], v204 offset:35840\n" \
"v_mfma_f32_16x16x32_bf16 v[44:47], a[204:207], a[12:15], v[44:47]\n" \
"buffer_load_dwordx4 v193, s[44:47], s73 offen offset:1024 lds\n" \
"v_mfma_f32_16x16x32_bf16 v[72:75], a[0:3], a[208:211], v[72:75]\n" \
"s_and_b32 s74, s70, 0xffff\n" \
"s_xor_b32 s68, s68, s8\n" \
"ds_read_b64_tr_b16 a[48:49], v222 offset:20480\n" \
"v_mfma_f32_16x16x32_bf16 v[72:75], a[4:7], a[212:215], v[72:75]\n" \
"s_and_b32 s68, s68, 3\n" \
"s_lshl_b32 s68, s68, 12\n" \
"ds_read_b128 a[0:3], v202 offset:0\n" \
"v_mfma_f32_16x16x32_bf16 v[80:83], a[8:11], a[208:211], v[80:83]\n" \
"ds_read_b128 a[4:7], v203 offset:0\n" \
"v_mfma_f32_16x16x32_bf16 v[80:83], a[12:15], a[212:215], v[80:83]\n" \
"buffer_load_dwordx4 v192, s[44:47], s73 offen offset:2048 lds\n" \
"v_mfma_f32_16x16x32_bf16 v[84:87], a[8:11], a[216:219], v[84:87]\n" \
"ds_read_b64_tr_b16 a[50:51], v222 offset:23552\n" \
"v_mfma_f32_16x16x32_bf16 v[84:87], a[12:15], a[220:223], v[84:87]\n" \
"ds_read_b128 a[8:11], v200 offset:0\n" \
"v_mfma_f32_16x16x32_bf16 v[92:95], a[16:19], a[216:219], v[92:95]\n" \
"ds_read_b128 a[12:15], v201 offset:0\n" \
"v_mfma_f32_16x16x32_bf16 v[92:95], a[20:23], a[220:223], v[92:95]\n" \
"buffer_load_dwordx4 v193, s[44:47], s73 offen offset:3072 lds\n" \
"v_mfma_f32_16x16x32_bf16 v[88:91], a[16:19], a[208:211], v[88:91]\n" \
"ds_read_b64_tr_b16 a[52:53], v222 offset:32768\n" \
"v_mfma_f32_16x16x32_bf16 v[88:91], a[20:23], a[212:215], v[88:91]\n" \
"s_add_u32 m0, s89, 65536\n" \
"ds_read_b128 a[16:19], v200 offset:4096\n" \
"v_mfma_f32_16x16x32_bf16 v[96:99], a[24:27], a[208:211], v[96:99]\n" \
"s_xor_b32 s73, s73, 0x180\n" \
"ds_read_b128 a[20:23], v201 offset:4096\n" \
"v_mfma_f32_16x16x32_bf16 v[96:99], a[28:31], a[212:215], v[96:99]\n" \
"buffer_load_dwordx4 v196, s[44:47], s75 offen lds\n" \
"v_mfma_f32_16x16x32_bf16 v[100:103], a[24:27], a[216:219], v[100:103]\n" \
"s_xor_b32 s75, s75, 0x180\n" \
"s_mul_i32 s76, s80, 393216\n" \
"ds_read_b64_tr_b16 a[54:55], v222 offset:35840\n" \
"v_mfma_f32_16x16x32_bf16 v[100:103], a[28:31], a[220:223], v[100:103]\n" \
"s_add_u32 s76, s76, s78\n" \
"ds_read_b128 a[24:27], v200 offset:8192\n" \
"v_mfma_f32_16x16x32_bf16 v[108:111], a[192:195], a[216:219], v[108:111]\n" \
"s_add_u32 m0, s91, 86016\n" \
"ds_read_b128 a[28:31], v201 offset:8192\n" \
"v_mfma_f32_16x16x32_bf16 v[108:111], a[196:199], a[220:223], v[108:111]\n" \
"buffer_load_dwordx4 v197, s[48:51], s76 offen offset:0 lds\n" \
"v_mfma_f32_16x16x32_bf16 v[104:107], a[192:195], a[208:211], v[104:107]\n" \
"v_mfma_f32_16x16x32_bf16 v[104:107], a[196:199], a[212:215], v[104:107]\n" \
"s_waitcnt lgkmcnt(2)\n" \
"v_mfma_f32_16x16x32_bf16 v[32:35], a[32:35], a[0:3], v[32:35]\n" \
"s_add_u32 s77, s76, 24576\n" \
"ds_read_b128 a[192:195], v200 offset:12288\n" \
"v_mfma_f32_16x16x32_bf16 v[32:35], a[36:39], a[4:7], v[32:35]\n" \
"buffer_load_dwordx4 v198, s[48:51], s76 offen offset:1024 lds\n" \
"v_mfma_f32_16x16x32_bf16 v[40:43], a[32:35], a[8:11], v[40:43]\n" \
"ds_read_b128 a[196:199], v201 offset:12288\n" \
"v_mfma_f32_16x16x32_bf16 v[40:43], a[36:39], a[12:15], v[40:43]\n" \
"ds_read_b64_tr_b16 a[200:201], v222 offset:21504\n" \
"v_mfma_f32_16x16x32_bf16 v[0:3], a[40:43], a[8:11], v[0:3]\n" \
"s_xor_b32 s80, s80, 0x3\n" \
"ds_read_b64_tr_b16 a[202:203], v222 offset:24576\n" \
"v_mfma_f32_16x16x32_bf16 v[0:3], a[44:47], a[12:15], v[0:3]\n" \
"buffer_load_dwordx4 v199, s[48:51], s76 offen offset:2048 lds\n" \
"v_mfma_f32_16x16x32_bf16 v[4:7], a[48:51], a[8:11], v[4:7]\n" \
"ds_read_b64_tr_b16 a[204:205], v222 offset:33792\n" \
"v_mfma_f32_16x16x32_bf16 v[4:7], a[52:55], a[12:15], v[4:7]\n" \
"ds_read_b64_tr_b16 a[206:207], v222 offset:36864\n" \
"v_mfma_f32_16x16x32_bf16 v[12:15], a[48:51], a[16:19], v[12:15]\n" \
"s_add_u32 m0, m0, 3072\n" \
"ds_read_b64_tr_b16 a[208:209], v204 offset:22528\n" \
"v_mfma_f32_16x16x32_bf16 v[12:15], a[52:55], a[20:23], v[12:15]\n" \
"buffer_load_dwordx4 v197, s[48:51], s77 offen offset:0 lds\n" \
"v_mfma_f32_16x16x32_bf16 v[8:11], a[40:43], a[16:19], v[8:11]\n" \
"ds_read_b64_tr_b16 a[210:211], v204 offset:25600\n" \
"v_mfma_f32_16x16x32_bf16 v[8:11], a[44:47], a[20:23], v[8:11]\n" \
"ds_read_b64_tr_b16 a[212:213], v204 offset:34816\n" \
"v_mfma_f32_16x16x32_bf16 v[48:51], a[32:35], a[16:19], v[48:51]\n" \
"s_waitcnt lgkmcnt(3)\n" \
"ds_read_b64_tr_b16 a[214:215], v204 offset:37888\n" \
"v_mfma_f32_16x16x32_bf16 v[48:51], a[36:39], a[20:23], v[48:51]\n" \
"buffer_load_dwordx4 v198, s[48:51], s77 offen offset:1024 lds\n" \
"v_mfma_f32_16x16x32_bf16 v[56:59], a[32:35], a[24:27], v[56:59]\n" \
"ds_read_b64_tr_b16 a[216:217], v222 offset:22528\n" \
"v_mfma_f32_16x16x32_bf16 v[56:59], a[36:39], a[28:31], v[56:59]\n" \
"ds_read_b64_tr_b16 a[218:219], v222 offset:25600\n" \
"v_mfma_f32_16x16x32_bf16 v[16:19], a[40:43], a[24:27], v[16:19]\n" \
"ds_read_b64_tr_b16 a[220:221], v222 offset:34816\n" \
"Lpg9043_%=:\n" \
"Lst1x2_%=:\n" \
"s_cbranch_execz Lpg10924_%=\n" \
"v_mfma_f32_16x16x32_bf16 v[16:19], a[44:47], a[28:31], v[16:19]\n" \
"buffer_load_dwordx4 v199, s[48:51], s77 offen offset:2048 lds\n" \
"v_mfma_f32_16x16x32_bf16 v[20:23], a[48:51], a[24:27], v[20:23]\n" \
"ds_read_b64_tr_b16 a[222:223], v222 offset:37888\n" \
"v_mfma_f32_16x16x32_bf16 v[20:23], a[52:55], a[28:31], v[20:23]\n" \
"v_mfma_f32_16x16x32_bf16 v[28:31], a[48:51], a[192:195], v[28:31]\n" \
"v_mfma_f32_16x16x32_bf16 v[28:31], a[52:55], a[196:199], v[28:31]\n" \
"v_mfma_f32_16x16x32_bf16 v[24:27], a[40:43], a[192:195], v[24:27]\n" \
"v_mfma_f32_16x16x32_bf16 v[24:27], a[44:47], a[196:199], v[24:27]\n" \
"v_mfma_f32_16x16x32_bf16 v[64:67], a[32:35], a[192:195], v[64:67]\n" \
"v_mfma_f32_16x16x32_bf16 v[64:67], a[36:39], a[196:199], v[64:67]\n" \
"s_waitcnt vmcnt(11) lgkmcnt(0)\n" \
"s_barrier\n" \
"v_mfma_f32_16x16x32_bf16 v[68:71], a[200:203], a[192:195], v[68:71]\n" \
"ds_read_b64_tr_b16 a[32:33], v194 offset:1024\n" \
"ds_read_b64_tr_b16 a[34:35], v194 offset:4096\n" \
"v_mfma_f32_16x16x32_bf16 v[68:71], a[204:207], a[196:199], v[68:71]\n" \
"ds_read_b64_tr_b16 a[36:37], v194 offset:13312\n" \
"ds_read_b64_tr_b16 a[38:39], v194 offset:16384\n" \
"v_mfma_f32_16x16x32_bf16 v[60:63], a[200:203], a[24:27], v[60:63]\n" \
"s_add_u32 m0, s79, 0\n" \
"ds_read_b64_tr_b16 a[40:41], v194 offset:0\n" \
"v_mfma_f32_16x16x32_bf16 v[60:63], a[204:207], a[28:31], v[60:63]\n" \
"buffer_load_dwordx4 v192, s[44:47], s73 offen offset:0 lds\n" \
"v_mfma_f32_16x16x32_bf16 v[52:55], a[200:203], a[16:19], v[52:55]\n" \
"ds_read_b64_tr_b16 a[42:43], v194 offset:3072\n" \
"v_mfma_f32_16x16x32_bf16 v[52:55], a[204:207], a[20:23], v[52:55]\n" \
"ds_read_b64_tr_b16 a[44:45], v194 offset:12288\n" \
"v_mfma_f32_16x16x32_bf16 v[44:47], a[200:203], a[8:11], v[44:47]\n" \
"ds_read_b64_tr_b16 a[46:47], v194 offset:15360\n" \
"v_mfma_f32_16x16x32_bf16 v[44:47], a[204:207], a[12:15], v[44:47]\n" \
"buffer_load_dwordx4 v193, s[44:47], s73 offen offset:1024 lds\n" \
"v_mfma_f32_16x16x32_bf16 v[72:75], a[0:3], a[208:211], v[72:75]\n" \
"ds_read_b64_tr_b16 a[48:49], v195 offset:0\n" \
"v_mfma_f32_16x16x32_bf16 v[72:75], a[4:7], a[212:215], v[72:75]\n" \
"ds_read_b128 a[0:3], v207 offset:45056\n" \
"v_mfma_f32_16x16x32_bf16 v[80:83], a[8:11], a[208:211], v[80:83]\n" \
"ds_read_b128 a[4:7], v208 offset:45056\n" \
"v_mfma_f32_16x16x32_bf16 v[80:83], a[12:15], a[212:215], v[80:83]\n" \
"buffer_load_dwordx4 v192, s[44:47], s73 offen offset:2048 lds\n" \
"v_mfma_f32_16x16x32_bf16 v[84:87], a[8:11], a[216:219], v[84:87]\n" \
"ds_read_b64_tr_b16 a[50:51], v195 offset:3072\n" \
"v_mfma_f32_16x16x32_bf16 v[84:87], a[12:15], a[220:223], v[84:87]\n" \
"ds_read_b128 a[8:11], v205 offset:45056\n" \
"v_mfma_f32_16x16x32_bf16 v[92:95], a[16:19], a[216:219], v[92:95]\n" \
"ds_read_b128 a[12:15], v206 offset:45056\n" \
"v_mfma_f32_16x16x32_bf16 v[92:95], a[20:23], a[220:223], v[92:95]\n" \
"buffer_load_dwordx4 v193, s[44:47], s73 offen offset:3072 lds\n" \
"v_mfma_f32_16x16x32_bf16 v[88:91], a[16:19], a[208:211], v[88:91]\n" \
"ds_read_b64_tr_b16 a[52:53], v195 offset:12288\n" \
"v_mfma_f32_16x16x32_bf16 v[88:91], a[20:23], a[212:215], v[88:91]\n" \
"s_add_u32 m0, s89, 0\n" \
"ds_read_b128 a[16:19], v205 offset:49152\n" \
"v_mfma_f32_16x16x32_bf16 v[96:99], a[24:27], a[208:211], v[96:99]\n" \
"s_xor_b32 s73, s73, 0x80\n" \
"ds_read_b128 a[20:23], v206 offset:49152\n" \
"v_mfma_f32_16x16x32_bf16 v[96:99], a[28:31], a[212:215], v[96:99]\n" \
"buffer_load_dwordx4 v196, s[44:47], s75 offen lds\n" \
"v_mfma_f32_16x16x32_bf16 v[100:103], a[24:27], a[216:219], v[100:103]\n" \
"s_xor_b32 s75, s75, 0x80\n" \
"s_mul_i32 s76, s80, 393216\n" \
"ds_read_b64_tr_b16 a[54:55], v195 offset:15360\n" \
"v_mfma_f32_16x16x32_bf16 v[100:103], a[28:31], a[220:223], v[100:103]\n" \
"s_add_u32 s76, s76, s78\n" \
"ds_read_b128 a[24:27], v205 offset:53248\n" \
"v_mfma_f32_16x16x32_bf16 v[108:111], a[192:195], a[216:219], v[108:111]\n" \
"s_add_u32 m0, s91, 20480\n" \
"ds_read_b128 a[28:31], v206 offset:53248\n" \
"v_mfma_f32_16x16x32_bf16 v[108:111], a[196:199], a[220:223], v[108:111]\n" \
"buffer_load_dwordx4 v197, s[48:51], s76 offen offset:0 lds\n" \
"v_mfma_f32_16x16x32_bf16 v[104:107], a[192:195], a[208:211], v[104:107]\n" \
"v_mfma_f32_16x16x32_bf16 v[104:107], a[196:199], a[212:215], v[104:107]\n" \
"s_waitcnt lgkmcnt(2)\n" \
"v_mfma_f32_16x16x32_bf16 v[32:35], a[32:35], a[0:3], v[32:35]\n" \
"s_add_u32 s77, s76, 24576\n" \
"ds_read_b128 a[192:195], v205 offset:57344\n" \
"v_mfma_f32_16x16x32_bf16 v[32:35], a[36:39], a[4:7], v[32:35]\n" \
"buffer_load_dwordx4 v198, s[48:51], s76 offen offset:1024 lds\n" \
"v_mfma_f32_16x16x32_bf16 v[40:43], a[32:35], a[8:11], v[40:43]\n" \
"ds_read_b128 a[196:199], v206 offset:57344\n" \
"v_mfma_f32_16x16x32_bf16 v[40:43], a[36:39], a[12:15], v[40:43]\n" \
"ds_read_b64_tr_b16 a[200:201], v195 offset:1024\n" \
"v_mfma_f32_16x16x32_bf16 v[0:3], a[40:43], a[8:11], v[0:3]\n" \
"s_xor_b32 s80, s80, 0x1\n" \
"ds_read_b64_tr_b16 a[202:203], v195 offset:4096\n" \
"v_mfma_f32_16x16x32_bf16 v[0:3], a[44:47], a[12:15], v[0:3]\n" \
"buffer_load_dwordx4 v199, s[48:51], s76 offen offset:2048 lds\n" \
"v_mfma_f32_16x16x32_bf16 v[4:7], a[48:51], a[8:11], v[4:7]\n" \
"ds_read_b64_tr_b16 a[204:205], v195 offset:13312\n" \
"v_mfma_f32_16x16x32_bf16 v[4:7], a[52:55], a[12:15], v[4:7]\n" \
"ds_read_b64_tr_b16 a[206:207], v195 offset:16384\n" \
"v_mfma_f32_16x16x32_bf16 v[12:15], a[48:51], a[16:19], v[12:15]\n" \
"s_add_u32 m0, m0, 3072\n" \
"ds_read_b64_tr_b16 a[208:209], v194 offset:2048\n" \
"v_mfma_f32_16x16x32_bf16 v[12:15], a[52:55], a[20:23], v[12:15]\n" \
"buffer_load_dwordx4 v197, s[48:51], s77 offen offset:0 lds\n" \
"v_mfma_f32_16x16x32_bf16 v[8:11], a[40:43], a[16:19], v[8:11]\n" \
"ds_read_b64_tr_b16 a[210:211], v194 offset:5120\n" \
"v_mfma_f32_16x16x32_bf16 v[8:11], a[44:47], a[20:23], v[8:11]\n" \
"ds_read_b64_tr_b16 a[212:213], v194 offset:14336\n" \
"v_mfma_f32_16x16x32_bf16 v[48:51], a[32:35], a[16:19], v[48:51]\n" \
"s_waitcnt lgkmcnt(3)\n" \
"ds_read_b64_tr_b16 a[214:215], v194 offset:17408\n" \
"v_mfma_f32_16x16x32_bf16 v[48:51], a[36:39], a[20:23], v[48:51]\n" \
"buffer_load_dwordx4 v198, s[48:51], s77 offen offset:1024 lds\n" \
"v_mfma_f32_16x16x32_bf16 v[56:59], a[32:35], a[24:27], v[56:59]\n" \
"ds_read_b64_tr_b16 a[216:217], v195 offset:2048\n" \
"v_mfma_f32_16x16x32_bf16 v[56:59], a[36:39], a[28:31], v[56:59]\n" \
"ds_read_b64_tr_b16 a[218:219], v195 offset:5120\n" \
"v_mfma_f32_16x16x32_bf16 v[16:19], a[40:43], a[24:27], v[16:19]\n" \
"ds_read_b64_tr_b16 a[220:221], v195 offset:14336\n" \
"v_mfma_f32_16x16x32_bf16 v[16:19], a[44:47], a[28:31], v[16:19]\n" \
"buffer_load_dwordx4 v199, s[48:51], s77 offen offset:2048 lds\n" \
"v_mfma_f32_16x16x32_bf16 v[20:23], a[48:51], a[24:27], v[20:23]\n" \
"ds_read_b64_tr_b16 a[222:223], v195 offset:17408\n" \
"v_mfma_f32_16x16x32_bf16 v[20:23], a[52:55], a[28:31], v[20:23]\n" \
"v_mfma_f32_16x16x32_bf16 v[28:31], a[48:51], a[192:195], v[28:31]\n" \
"v_mfma_f32_16x16x32_bf16 v[28:31], a[52:55], a[196:199], v[28:31]\n" \
"buffer_load_dword v220, v221, s[56:59], s66 offen sc1\n" \
"v_mfma_f32_16x16x32_bf16 v[24:27], a[40:43], a[192:195], v[24:27]\n" \
"v_mfma_f32_16x16x32_bf16 v[24:27], a[44:47], a[196:199], v[24:27]\n" \
"v_mfma_f32_16x16x32_bf16 v[64:67], a[32:35], a[192:195], v[64:67]\n" \
"v_mfma_f32_16x16x32_bf16 v[64:67], a[36:39], a[196:199], v[64:67]\n" \
"s_waitcnt vmcnt(12) lgkmcnt(0)\n" \
"s_barrier\n" \
"v_mfma_f32_16x16x32_bf16 v[68:71], a[200:203], a[192:195], v[68:71]\n" \
"ds_read_b64_tr_b16 a[32:33], v209 offset:1024\n" \
"ds_read_b64_tr_b16 a[34:35], v209 offset:4096\n" \
"v_mfma_f32_16x16x32_bf16 v[68:71], a[204:207], a[196:199], v[68:71]\n" \
"ds_read_b64_tr_b16 a[36:37], v209 offset:13312\n" \
"ds_read_b64_tr_b16 a[38:39], v209 offset:16384\n" \
"v_mfma_f32_16x16x32_bf16 v[60:63], a[200:203], a[24:27], v[60:63]\n" \
"s_add_u32 m0, s79, 110592\n" \
"ds_read_b64_tr_b16 a[40:41], v209 offset:0\n" \
"v_mfma_f32_16x16x32_bf16 v[60:63], a[204:207], a[28:31], v[60:63]\n" \
"buffer_load_dwordx4 v192, s[44:47], s73 offen offset:0 lds\n" \
"v_mfma_f32_16x16x32_bf16 v[52:55], a[200:203], a[16:19], v[52:55]\n" \
"ds_read_b64_tr_b16 a[42:43], v209 offset:3072\n" \
"v_mfma_f32_16x16x32_bf16 v[52:55], a[204:207], a[20:23], v[52:55]\n" \
"ds_read_b64_tr_b16 a[44:45], v209 offset:12288\n" \
"v_mfma_f32_16x16x32_bf16 v[44:47], a[200:203], a[8:11], v[44:47]\n" \
"ds_read_b64_tr_b16 a[46:47], v209 offset:15360\n" \
"v_mfma_f32_16x16x32_bf16 v[44:47], a[204:207], a[12:15], v[44:47]\n" \
"buffer_load_dwordx4 v193, s[44:47], s73 offen offset:1024 lds\n" \
"v_mfma_f32_16x16x32_bf16 v[72:75], a[0:3], a[208:211], v[72:75]\n" \
"ds_read_b64_tr_b16 a[48:49], v210 offset:0\n" \
"v_mfma_f32_16x16x32_bf16 v[72:75], a[4:7], a[212:215], v[72:75]\n" \
"ds_read_b128 a[0:3], v207 offset:0\n" \
"v_mfma_f32_16x16x32_bf16 v[80:83], a[8:11], a[208:211], v[80:83]\n" \
"ds_read_b128 a[4:7], v208 offset:0\n" \
"v_mfma_f32_16x16x32_bf16 v[80:83], a[12:15], a[212:215], v[80:83]\n" \
"buffer_load_dwordx4 v192, s[44:47], s73 offen offset:2048 lds\n" \
"v_mfma_f32_16x16x32_bf16 v[84:87], a[8:11], a[216:219], v[84:87]\n" \
"ds_read_b64_tr_b16 a[50:51], v210 offset:3072\n" \
"v_mfma_f32_16x16x32_bf16 v[84:87], a[12:15], a[220:223], v[84:87]\n" \
"ds_read_b128 a[8:11], v205 offset:0\n" \
"v_mfma_f32_16x16x32_bf16 v[92:95], a[16:19], a[216:219], v[92:95]\n" \
"ds_read_b128 a[12:15], v206 offset:0\n" \
"v_mfma_f32_16x16x32_bf16 v[92:95], a[20:23], a[220:223], v[92:95]\n" \
"buffer_load_dwordx4 v193, s[44:47], s73 offen offset:3072 lds\n" \
"v_mfma_f32_16x16x32_bf16 v[88:91], a[16:19], a[208:211], v[88:91]\n" \
"ds_read_b64_tr_b16 a[52:53], v210 offset:12288\n" \
"v_mfma_f32_16x16x32_bf16 v[88:91], a[20:23], a[212:215], v[88:91]\n" \
"s_add_u32 m0, s89, 110592\n" \
"ds_read_b128 a[16:19], v205 offset:4096\n" \
"v_mfma_f32_16x16x32_bf16 v[96:99], a[24:27], a[208:211], v[96:99]\n" \
"s_xor_b32 s73, s73, 0x80\n" \
"ds_read_b128 a[20:23], v206 offset:4096\n" \
"v_mfma_f32_16x16x32_bf16 v[96:99], a[28:31], a[212:215], v[96:99]\n" \
"buffer_load_dwordx4 v196, s[44:47], s75 offen lds\n" \
"v_mfma_f32_16x16x32_bf16 v[100:103], a[24:27], a[216:219], v[100:103]\n" \
"s_xor_b32 s75, s75, 0x80\n" \
"s_mul_i32 s76, s80, 393216\n" \
"ds_read_b64_tr_b16 a[54:55], v210 offset:15360\n" \
"v_mfma_f32_16x16x32_bf16 v[100:103], a[28:31], a[220:223], v[100:103]\n" \
"s_add_u32 s76, s76, s78\n" \
"ds_read_b128 a[24:27], v205 offset:8192\n" \
"v_mfma_f32_16x16x32_bf16 v[108:111], a[192:195], a[216:219], v[108:111]\n" \
"s_add_u32 m0, s91, 139264\n" \
"ds_read_b128 a[28:31], v206 offset:8192\n" \
"v_mfma_f32_16x16x32_bf16 v[108:111], a[196:199], a[220:223], v[108:111]\n" \
"buffer_load_dwordx4 v197, s[48:51], s76 offen offset:0 lds\n" \
"v_mfma_f32_16x16x32_bf16 v[104:107], a[192:195], a[208:211], v[104:107]\n" \
"v_mfma_f32_16x16x32_bf16 v[104:107], a[196:199], a[212:215], v[104:107]\n" \
"s_waitcnt lgkmcnt(2)\n" \
"v_mfma_f32_16x16x32_bf16 v[32:35], a[32:35], a[0:3], v[32:35]\n" \
"s_add_u32 s77, s76, 24576\n" \
"ds_read_b128 a[192:195], v205 offset:12288\n" \
"v_mfma_f32_16x16x32_bf16 v[32:35], a[36:39], a[4:7], v[32:35]\n" \
"buffer_load_dwordx4 v198, s[48:51], s76 offen offset:1024 lds\n" \
"v_mfma_f32_16x16x32_bf16 v[40:43], a[32:35], a[8:11], v[40:43]\n" \
"ds_read_b128 a[196:199], v206 offset:12288\n" \
"v_mfma_f32_16x16x32_bf16 v[40:43], a[36:39], a[12:15], v[40:43]\n" \
"ds_read_b64_tr_b16 a[200:201], v210 offset:1024\n" \
"v_mfma_f32_16x16x32_bf16 v[0:3], a[40:43], a[8:11], v[0:3]\n" \
"s_xor_b32 s80, s80, 0x1\n" \
"ds_read_b64_tr_b16 a[202:203], v210 offset:4096\n" \
"v_mfma_f32_16x16x32_bf16 v[0:3], a[44:47], a[12:15], v[0:3]\n" \
"buffer_load_dwordx4 v199, s[48:51], s76 offen offset:2048 lds\n" \
"v_mfma_f32_16x16x32_bf16 v[4:7], a[48:51], a[8:11], v[4:7]\n" \
"ds_read_b64_tr_b16 a[204:205], v210 offset:13312\n" \
"v_mfma_f32_16x16x32_bf16 v[4:7], a[52:55], a[12:15], v[4:7]\n" \
"ds_read_b64_tr_b16 a[206:207], v210 offset:16384\n" \
"v_mfma_f32_16x16x32_bf16 v[12:15], a[48:51], a[16:19], v[12:15]\n" \
"s_add_u32 m0, m0, 3072\n" \
"ds_read_b64_tr_b16 a[208:209], v209 offset:2048\n" \
"v_mfma_f32_16x16x32_bf16 v[12:15], a[52:55], a[20:23], v[12:15]\n" \
"buffer_load_dwordx4 v197, s[48:51], s77 offen offset:0 lds\n" \
"v_mfma_f32_16x16x32_bf16 v[8:11], a[40:43], a[16:19], v[8:11]\n" \
"ds_read_b64_tr_b16 a[210:211], v209 offset:5120\n" \
"v_mfma_f32_16x16x32_bf16 v[8:11], a[44:47], a[20:23], v[8:11]\n" \
"ds_read_b64_tr_b16 a[212:213], v209 offset:14336\n" \
"v_mfma_f32_16x16x32_bf16 v[48:51], a[32:35], a[16:19], v[48:51]\n" \
"s_waitcnt lgkmcnt(3)\n" \
"ds_read_b64_tr_b16 a[214:215], v209 offset:17408\n" \
"v_mfma_f32_16x16x32_bf16 v[48:51], a[36:39], a[20:23], v[48:51]\n" \
"buffer_load_dwordx4 v198, s[48:51], s77 offen offset:1024 lds\n" \
"v_mfma_f32_16x16x32_bf16 v[56:59], a[32:35], a[24:27], v[56:59]\n" \
"ds_read_b64_tr_b16 a[216:217], v210 offset:2048\n" \
"v_mfma_f32_16x16x32_bf16 v[56:59], a[36:39], a[28:31], v[56:59]\n" \
"ds_read_b64_tr_b16 a[218:219], v210 offset:5120\n" \
"v_mfma_f32_16x16x32_bf16 v[16:19], a[40:43], a[24:27], v[16:19]\n" \
"ds_read_b64_tr_b16 a[220:221], v210 offset:14336\n" \
"v_mfma_f32_16x16x32_bf16 v[16:19], a[44:47], a[28:31], v[16:19]\n" \
"buffer_load_dwordx4 v199, s[48:51], s77 offen offset:2048 lds\n" \
"v_mfma_f32_16x16x32_bf16 v[20:23], a[48:51], a[24:27], v[20:23]\n" \
"s_mov_b32 s46, s81\n" \
"s_mov_b32 s47, s82\n" \
"ds_read_b64_tr_b16 a[222:223], v210 offset:17408\n" \
"v_mfma_f32_16x16x32_bf16 v[20:23], a[52:55], a[28:31], v[20:23]\n" \
"s_mov_b32 s48, 0\n" \
"s_mov_b32 s49, s68\n" \
"s_sub_u32 s50, 64, s9\n" \
"v_mfma_f32_16x16x32_bf16 v[28:31], a[48:51], a[192:195], v[28:31]\n" \
"s_cmp_eq_u32 s90, 0\n" \
"s_cselect_b32 s50, s9, s50\n" \
"s_andn2_b32 s50, s50, 3\n" \
"v_mfma_f32_16x16x32_bf16 v[28:31], a[52:55], a[196:199], v[28:31]\n" \
"s_lshl_b32 s50, s50, 12\n" \
"s_waitcnt vmcnt(11)\n" \
"v_cmp_eq_u32 vcc, s60, v220\n" \
"v_mfma_f32_16x16x32_bf16 v[24:27], a[40:43], a[192:195], v[24:27]\n" \
"v_mfma_f32_16x16x32_bf16 v[24:27], a[44:47], a[196:199], v[24:27]\n" \
"s_andn2_b32 s70, s65, vcc_lo\n" \
"s_cbranch_scc1 Lpl5_%=\n" \
"Lpr5_%=:\n" \
"v_mfma_f32_16x16x32_bf16 v[64:67], a[32:35], a[192:195], v[64:67]\n" \
"v_mfma_f32_16x16x32_bf16 v[64:67], a[36:39], a[196:199], v[64:67]\n" \
"s_xor_b32 s82, s48, s49\n" \
"s_cmp_lt_u32 s48, s50\n" \
"s_cselect_b32 s82, s82, s48\n" \
"s_waitcnt vmcnt(12) lgkmcnt(0)\n" \
"s_barrier\n" \
"v_mfma_f32_16x16x32_bf16 v[68:71], a[200:203], a[192:195], v[68:71]\n" \
"ds_read_b64_tr_b16 a[32:33], v204 offset:21504\n" \
"ds_read_b64_tr_b16 a[34:35], v204 offset:24576\n" \
"v_mfma_f32_16x16x32_bf16 v[68:71], a[204:207], a[196:199], v[68:71]\n" \
"s_add_u32 s81, s82, s46\n" \
"s_lshr_b32 s78, s61, 1\n" \
"s_lshl_b32 s80, s78, 9\n" \
"s_add_u32 s80, s80, 511\n" \
"ds_read_b64_tr_b16 a[36:37], v204 offset:33792\n" \
"ds_read_b64_tr_b16 a[38:39], v204 offset:36864\n" \
"v_mfma_f32_16x16x32_bf16 v[60:63], a[200:203], a[24:27], v[60:63]\n" \
"s_add_u32 m0, s86, s79\n" \
"ds_read_b64_tr_b16 a[40:41], v204 offset:20480\n" \
"v_sub_u32 v145, s80, v234\n" \
"v_mfma_f32_16x16x32_bf16 v[60:63], a[204:207], a[28:31], v[60:63]\n" \
"buffer_load_dwordx4 v234, s[56:59], s81 offen offset:0 sc1 lds\n" \
"v_mfma_f32_16x16x32_bf16 v[52:55], a[200:203], a[16:19], v[52:55]\n" \
"s_lshl_b32 s80, s78, 3\n" \
"s_not_b32 s80, s80\n" \
"ds_read_b64_tr_b16 a[42:43], v204 offset:23552\n" \
"v_mfma_f32_16x16x32_bf16 v[52:55], a[204:207], a[20:23], v[52:55]\n" \
"s_lshl_b32 s82, s82, 5\n" \
"ds_read_b64_tr_b16 a[44:45], v204 offset:32768\n" \
"v_sub_u32 v146, s80, v238\n" \
"v_mfma_f32_16x16x32_bf16 v[44:47], a[200:203], a[8:11], v[44:47]\n" \
"s_add_u32 s82, s82, s47\n" \
"ds_read_b64_tr_b16 a[46:47], v204 offset:35840\n" \
"v_subrev_u32 v152, 0, v238\n" \
"v_mfma_f32_16x16x32_bf16 v[44:47], a[204:207], a[12:15], v[44:47]\n" \
"buffer_load_dwordx4 v234, s[56:59], s81 offen offset:1024 sc1 lds\n" \
"v_mfma_f32_16x16x32_bf16 v[72:75], a[0:3], a[208:211], v[72:75]\n" \
"ds_read_b64_tr_b16 a[48:49], v222 offset:20480\n" \
"v_sub_u32 v144, 7, v238\n" \
"v_mfma_f32_16x16x32_bf16 v[72:75], a[4:7], a[212:215], v[72:75]\n" \
"s_add_u32 s48, s48, 0x1000\n" \
"ds_read_b128 a[0:3], v202 offset:0\n" \
"v_or_b32 v152, v152, v144\n" \
"v_mfma_f32_16x16x32_bf16 v[80:83], a[8:11], a[208:211], v[80:83]\n" \
"s_sub_u32 s86, s86, 32768\n" \
"s_cselect_b32 s86, 131072, s86\n" \
"ds_read_b128 a[4:7], v203 offset:0\n" \
"v_mfma_f32_16x16x32_bf16 v[80:83], a[12:15], a[212:215], v[80:83]\n" \
"buffer_load_dwordx4 v234, s[56:59], s81 offen offset:2048 sc1 lds\n" \
"v_mfma_f32_16x16x32_bf16 v[84:87], a[8:11], a[216:219], v[84:87]\n" \
"ds_read_b64_tr_b16 a[50:51], v222 offset:23552\n" \
"v_ashrrev_i32 v152, 31, v152\n" \
"v_mfma_f32_16x16x32_bf16 v[84:87], a[12:15], a[220:223], v[84:87]\n" \
"ds_read_b128 a[8:11], v200 offset:0\n" \
"v_and_b32 v152, 0xff800000, v152\n" \
"v_mfma_f32_16x16x32_bf16 v[92:95], a[16:19], a[216:219], v[92:95]\n" \
"ds_read_b128 a[12:15], v201 offset:0\n" \
"v_sub_u32 v156, -9, v238\n" \
"v_mfma_f32_16x16x32_bf16 v[92:95], a[20:23], a[220:223], v[92:95]\n" \
"buffer_load_dwordx4 v234, s[56:59], s81 offen offset:3072 sc1 lds\n" \
"v_mfma_f32_16x16x32_bf16 v[88:91], a[16:19], a[208:211], v[88:91]\n" \
"ds_read_b64_tr_b16 a[52:53], v222 offset:32768\n" \
"v_ashrrev_i32 v156, 31, v156\n" \
"v_mfma_f32_16x16x32_bf16 v[88:91], a[20:23], a[212:215], v[88:91]\n" \
"ds_read_b128 a[16:19], v200 offset:4096\n" \
"v_and_b32 v156, 0xff800000, v156\n" \
"v_mfma_f32_16x16x32_bf16 v[96:99], a[24:27], a[208:211], v[96:99]\n" \
"s_add_u32 m0, m0, 0x4000\n" \
"ds_read_b128 a[20:23], v201 offset:4096\n" \
"v_add_u32 v148, 0, v146\n" \
"v_mfma_f32_16x16x32_bf16 v[96:99], a[28:31], a[212:215], v[96:99]\n" \
"buffer_load_dwordx4 v216, s[36:39], s82 offen offset:0 lds\n" \
"v_mfma_f32_16x16x32_bf16 v[100:103], a[24:27], a[216:219], v[100:103]\n" \
"ds_read_b64_tr_b16 a[54:55], v222 offset:35840\n" \
"v_or3_b32 v148, v148, v145, s92\n" \
"v_mfma_f32_16x16x32_bf16 v[100:103], a[28:31], a[220:223], v[100:103]\n" \
"ds_read_b128 a[24:27], v200 offset:8192\n" \
"v_ashrrev_i32 v148, 31, v148\n" \
"v_mfma_f32_16x16x32_bf16 v[108:111], a[192:195], a[216:219], v[108:111]\n" \
"ds_read_b128 a[28:31], v201 offset:8192\n" \
"v_and_b32 v148, 0xff800000, v148\n" \
"v_mfma_f32_16x16x32_bf16 v[108:111], a[196:199], a[220:223], v[108:111]\n" \
"buffer_load_dwordx4 v217, s[36:39], s82 offen offset:1024 lds\n" \
"v_mfma_f32_16x16x32_bf16 v[104:107], a[192:195], a[208:211], v[104:107]\n" \
"v_subrev_u32 v153, 1, v238\n" \
"v_sub_u32 v144, 8, v238\n" \
"v_mfma_f32_16x16x32_bf16 v[104:107], a[196:199], a[212:215], v[104:107]\n" \
"s_waitcnt lgkmcnt(2)\n" \
"v_or_b32 v153, v153, v144\n" \
"v_ashrrev_i32 v153, 31, v153\n" \
"v_mfma_f32_16x16x32_bf16 v[32:35], a[32:35], a[0:3], v[32:35]\n" \
"ds_read_b128 a[192:195], v200 offset:12288\n" \
"v_and_b32 v153, 0xff800000, v153\n" \
"v_mfma_f32_16x16x32_bf16 v[32:35], a[36:39], a[4:7], v[32:35]\n" \
"buffer_load_dwordx4 v218, s[36:39], s82 offen offset:2048 lds\n" \
"v_mfma_f32_16x16x32_bf16 v[40:43], a[32:35], a[8:11], v[40:43]\n" \
"ds_read_b128 a[196:199], v201 offset:12288\n" \
"v_sub_u32 v157, -8, v238\n" \
"v_mfma_f32_16x16x32_bf16 v[40:43], a[36:39], a[12:15], v[40:43]\n" \
"ds_read_b64_tr_b16 a[200:201], v222 offset:21504\n" \
"v_ashrrev_i32 v157, 31, v157\n" \
"v_mfma_f32_16x16x32_bf16 v[0:3], a[40:43], a[8:11], v[0:3]\n" \
"ds_read_b64_tr_b16 a[202:203], v222 offset:24576\n" \
"v_and_b32 v157, 0xff800000, v157\n" \
"v_mfma_f32_16x16x32_bf16 v[0:3], a[44:47], a[12:15], v[0:3]\n" \
"buffer_load_dwordx4 v219, s[36:39], s82 offen offset:3072 lds\n" \
"v_mfma_f32_16x16x32_bf16 v[4:7], a[48:51], a[8:11], v[4:7]\n" \
"ds_read_b64_tr_b16 a[204:205], v222 offset:33792\n" \
"v_add_u32 v149, 1, v146\n" \
"v_mfma_f32_16x16x32_bf16 v[4:7], a[52:55], a[12:15], v[4:7]\n" \
"ds_read_b64_tr_b16 a[206:207], v222 offset:36864\n" \
"v_or3_b32 v149, v149, v145, s92\n" \
"v_mfma_f32_16x16x32_bf16 v[12:15], a[48:51], a[16:19], v[12:15]\n" \
"ds_read_b64_tr_b16 a[208:209], v204 offset:22528\n" \
"v_ashrrev_i32 v149, 31, v149\n" \
"v_mfma_f32_16x16x32_bf16 v[12:15], a[52:55], a[20:23], v[12:15]\n" \
"v_and_b32 v149, 0xff800000, v149\n" \
"v_subrev_u32 v154, 2, v238\n" \
"v_mfma_f32_16x16x32_bf16 v[8:11], a[40:43], a[16:19], v[8:11]\n" \
"ds_read_b64_tr_b16 a[210:211], v204 offset:25600\n" \
"v_sub_u32 v144, 9, v238\n" \
"v_mfma_f32_16x16x32_bf16 v[8:11], a[44:47], a[20:23], v[8:11]\n" \
"ds_read_b64_tr_b16 a[212:213], v204 offset:34816\n" \
"v_or_b32 v154, v154, v144\n" \
"v_mfma_f32_16x16x32_bf16 v[48:51], a[32:35], a[16:19], v[48:51]\n" \
"ds_read_b64_tr_b16 a[214:215], v204 offset:37888\n" \
"v_ashrrev_i32 v154, 31, v154\n" \
"v_mfma_f32_16x16x32_bf16 v[48:51], a[36:39], a[20:23], v[48:51]\n" \
"s_waitcnt lgkmcnt(3)\n" \
"v_and_b32 v154, 0xff800000, v154\n" \
"v_sub_u32 v158, -7, v238\n" \
"v_mfma_f32_16x16x32_bf16 v[56:59], a[32:35], a[24:27], v[56:59]\n" \
"ds_read_b64_tr_b16 a[216:217], v222 offset:22528\n" \
"v_ashrrev_i32 v158, 31, v158\n" \
"v_mfma_f32_16x16x32_bf16 v[56:59], a[36:39], a[28:31], v[56:59]\n" \
"ds_read_b64_tr_b16 a[218:219], v222 offset:25600\n" \
"v_and_b32 v158, 0xff800000, v158\n" \
"v_mfma_f32_16x16x32_bf16 v[16:19], a[40:43], a[24:27], v[16:19]\n" \
"ds_read_b64_tr_b16 a[220:221], v222 offset:34816\n" \
"v_add_u32 v150, 2, v146\n" \
"v_mfma_f32_16x16x32_bf16 v[16:19], a[44:47], a[28:31], v[16:19]\n" \
"v_or3_b32 v150, v150, v145, s92\n" \
"v_ashrrev_i32 v150, 31, v150\n" \
"v_mfma_f32_16x16x32_bf16 v[20:23], a[48:51], a[24:27], v[20:23]\n" \
"ds_read_b64_tr_b16 a[222:223], v222 offset:37888\n" \
"v_and_b32 v150, 0xff800000, v150\n" \
"v_mfma_f32_16x16x32_bf16 v[20:23], a[52:55], a[28:31], v[20:23]\n" \
"v_subrev_u32 v155, 3, v238\n" \
"v_sub_u32 v144, 10, v238\n" \
"v_mfma_f32_16x16x32_bf16 v[28:31], a[48:51], a[192:195], v[28:31]\n" \
"v_or_b32 v155, v155, v144\n" \
"v_ashrrev_i32 v155, 31, v155\n" \
"v_mfma_f32_16x16x32_bf16 v[28:31], a[52:55], a[196:199], v[28:31]\n" \
"v_and_b32 v155, 0xff800000, v155\n" \
"v_sub_u32 v159, -6, v238\n" \
"v_mfma_f32_16x16x32_bf16 v[24:27], a[40:43], a[192:195], v[24:27]\n" \
"v_ashrrev_i32 v159, 31, v159\n" \
"v_and_b32 v159, 0xff800000, v159\n" \
"v_mfma_f32_16x16x32_bf16 v[24:27], a[44:47], a[196:199], v[24:27]\n" \
"v_add_u32 v151, 3, v146\n" \
"v_or3_b32 v151, v151, v145, s92\n" \
"v_mfma_f32_16x16x32_bf16 v[64:67], a[32:35], a[192:195], v[64:67]\n" \
"v_ashrrev_i32 v151, 31, v151\n" \
"v_and_b32 v151, 0xff800000, v151\n" \
"v_mfma_f32_16x16x32_bf16 v[64:67], a[36:39], a[196:199], v[64:67]\n" \
"s_xor_b32 s82, s48, s49\n" \
"s_cmp_lt_u32 s48, s50\n" \
"s_cselect_b32 s82, s82, s48\n" \
"s_waitcnt vmcnt(8) lgkmcnt(0)\n" \
"s_barrier\n" \
"v_mfma_f32_16x16x32_bf16 v[68:71], a[200:203], a[192:195], v[68:71]\n" \
"ds_read_b64_tr_b16 a[32:33], v194 offset:1024\n" \
"ds_read_b64_tr_b16 a[34:35], v194 offset:4096\n" \
"v_mfma_f32_16x16x32_bf16 v[68:71], a[204:207], a[196:199], v[68:71]\n" \
"s_add_u32 s81, s82, s46\n" \
"ds_read_b64_tr_b16 a[36:37], v194 offset:13312\n" \
"ds_read_b64_tr_b16 a[38:39], v194 offset:16384\n" \
"v_mfma_f32_16x16x32_bf16 v[60:63], a[200:203], a[24:27], v[60:63]\n" \
"s_add_u32 m0, s86, s79\n" \
"ds_read_b64_tr_b16 a[40:41], v194 offset:0\n" \
"v_mov_b32 v186, 0xffff0000\n" \
"v_mfma_f32_16x16x32_bf16 v[60:63], a[204:207], a[28:31], v[60:63]\n" \
"buffer_load_dwordx4 v234, s[56:59], s81 offen offset:0 sc1 lds\n" \
"v_mfma_f32_16x16x32_bf16 v[52:55], a[200:203], a[16:19], v[52:55]\n" \
"ds_read_b64_tr_b16 a[42:43], v194 offset:3072\n" \
"v_subrev_u32 v144, 0, v238\n" \
"v_mfma_f32_16x16x32_bf16 v[52:55], a[204:207], a[20:23], v[52:55]\n" \
"s_lshl_b32 s82, s82, 5\n" \
"ds_read_b64_tr_b16 a[44:45], v194 offset:12288\n" \
"v_subrev_u32 v184, 1, v238\n" \
"v_mfma_f32_16x16x32_bf16 v[44:47], a[200:203], a[8:11], v[44:47]\n" \
"s_add_u32 s82, s82, s47\n" \
"ds_read_b64_tr_b16 a[46:47], v194 offset:15360\n" \
"v_lshrrev_b32 v144, 16, v144\n" \
"v_mfma_f32_16x16x32_bf16 v[44:47], a[204:207], a[12:15], v[44:47]\n" \
"buffer_load_dwordx4 v234, s[56:59], s81 offen offset:1024 sc1 lds\n" \
"v_mfma_f32_16x16x32_bf16 v[72:75], a[0:3], a[208:211], v[72:75]\n" \
"ds_read_b64_tr_b16 a[48:49], v195 offset:0\n" \
"v_and_or_b32 v184, v184, v186, v144\n" \
"v_mfma_f32_16x16x32_bf16 v[72:75], a[4:7], a[212:215], v[72:75]\n" \
"s_add_u32 s48, s48, 0x1000\n" \
"ds_read_b128 a[0:3], v207 offset:45056\n" \
"v_subrev_u32 v144, 2, v238\n" \
"v_mfma_f32_16x16x32_bf16 v[80:83], a[8:11], a[208:211], v[80:83]\n" \
"s_sub_u32 s86, s86, 32768\n" \
"s_cselect_b32 s86, 131072, s86\n" \
"ds_read_b128 a[4:7], v208 offset:45056\n" \
"v_mfma_f32_16x16x32_bf16 v[80:83], a[12:15], a[212:215], v[80:83]\n" \
"buffer_load_dwordx4 v234, s[56:59], s81 offen offset:2048 sc1 lds\n" \
"v_mfma_f32_16x16x32_bf16 v[84:87], a[8:11], a[216:219], v[84:87]\n" \
"ds_read_b64_tr_b16 a[50:51], v195 offset:3072\n" \
"v_subrev_u32 v185, 3, v238\n" \
"v_mfma_f32_16x16x32_bf16 v[84:87], a[12:15], a[220:223], v[84:87]\n" \
"ds_read_b128 a[8:11], v205 offset:45056\n" \
"v_lshrrev_b32 v144, 16, v144\n" \
"v_mfma_f32_16x16x32_bf16 v[92:95], a[16:19], a[216:219], v[92:95]\n" \
"ds_read_b128 a[12:15], v206 offset:45056\n" \
"v_and_or_b32 v185, v185, v186, v144\n" \
"v_mfma_f32_16x16x32_bf16 v[92:95], a[20:23], a[220:223], v[92:95]\n" \
"buffer_load_dwordx4 v234, s[56:59], s81 offen offset:3072 sc1 lds\n" \
"v_mfma_f32_16x16x32_bf16 v[88:91], a[16:19], a[208:211], v[88:91]\n" \
"ds_read_b64_tr_b16 a[52:53], v195 offset:12288\n" \
"v_mov_b32 v144, 0x3e8293ee\n" \
"v_mfma_f32_16x16x32_bf16 v[88:91], a[20:23], a[212:215], v[88:91]\n" \
"s_add_u32 s80, s79, 117760\n" \
"ds_read_b128 a[16:19], v205 offset:49152\n" \
"v_add_u32 v223, s80, v234\n" \
"v_mfma_f32_16x16x32_bf16 v[96:99], a[24:27], a[208:211], v[96:99]\n" \
"s_add_u32 m0, m0, 0x4000\n" \
"ds_read_b128 a[20:23], v206 offset:49152\n" \
"v_mfma_f32_16x16x32_bf16 v[96:99], a[28:31], a[212:215], v[96:99]\n" \
"buffer_load_dwordx4 v216, s[36:39], s82 offen offset:0 lds\n" \
"v_mfma_f32_16x16x32_bf16 v[100:103], a[24:27], a[216:219], v[100:103]\n" \
"s_cmp_lg_u32 s78, 0\n" \
"s_cselect_b64 s[76:77], -1, 0\n" \
"ds_read_b64_tr_b16 a[54:55], v195 offset:15360\n" \
"v_mfma_f32_16x16x32_bf16 v[100:103], a[28:31], a[220:223], v[100:103]\n" \
"ds_read_b128 a[24:27], v205 offset:53248\n" \
"v_mfma_f32_16x16x32_bf16 v[108:111], a[192:195], a[216:219], v[108:111]\n" \
"ds_read_b128 a[28:31], v206 offset:53248\n" \
"v_mfma_f32_16x16x32_bf16 v[108:111], a[196:199], a[220:223], v[108:111]\n" \
"buffer_load_dwordx4 v217, s[36:39], s82 offen offset:1024 lds\n" \
"v_mfma_f32_16x16x32_bf16 v[104:107], a[192:195], a[208:211], v[104:107]\n" \
"v_mfma_f32_16x16x32_bf16 v[104:107], a[196:199], a[212:215], v[104:107]\n" \
"s_waitcnt lgkmcnt(2)\n" \
"v_mfma_f32_16x16x32_bf16 v[32:35], a[32:35], a[0:3], v[32:35]\n" \
"ds_read_b128 a[192:195], v205 offset:57344\n" \
"v_mfma_f32_16x16x32_bf16 v[32:35], a[36:39], a[4:7], v[32:35]\n" \
"buffer_load_dwordx4 v218, s[36:39], s82 offen offset:2048 lds\n" \
"v_mfma_f32_16x16x32_bf16 v[40:43], a[32:35], a[8:11], v[40:43]\n" \
"ds_read_b128 a[196:199], v206 offset:57344\n" \
"v_mfma_f32_16x16x32_bf16 v[40:43], a[36:39], a[12:15], v[40:43]\n" \
"ds_read_b64_tr_b16 a[200:201], v195 offset:1024\n" \
"v_mfma_f32_16x16x32_bf16 v[0:3], a[40:43], a[8:11], v[0:3]\n" \
"ds_read_b64_tr_b16 a[202:203], v195 offset:4096\n" \
"v_mfma_f32_16x16x32_bf16 v[0:3], a[44:47], a[12:15], v[0:3]\n" \
"buffer_load_dwordx4 v219, s[36:39], s82 offen offset:3072 lds\n" \
"v_mfma_f32_16x16x32_bf16 v[4:7], a[48:51], a[8:11], v[4:7]\n" \
"s_xor_b32 s82, s48, s49\n" \
"s_cmp_lt_u32 s48, s50\n" \
"s_cselect_b32 s82, s82, s48\n" \
"ds_read_b64_tr_b16 a[204:205], v195 offset:13312\n" \
"v_mfma_f32_16x16x32_bf16 v[4:7], a[52:55], a[12:15], v[4:7]\n" \
"s_add_u32 s81, s82, s46\n" \
"ds_read_b64_tr_b16 a[206:207], v195 offset:16384\n" \
"v_mfma_f32_16x16x32_bf16 v[12:15], a[48:51], a[16:19], v[12:15]\n" \
"s_add_u32 m0, s86, s79\n" \
"ds_read_b64_tr_b16 a[208:209], v194 offset:2048\n" \
"v_mfma_f32_16x16x32_bf16 v[12:15], a[52:55], a[20:23], v[12:15]\n" \
"buffer_load_dwordx4 v234, s[56:59], s81 offen offset:0 sc1 lds\n" \
"v_mfma_f32_16x16x32_bf16 v[8:11], a[40:43], a[16:19], v[8:11]\n" \
"ds_read_b64_tr_b16 a[210:211], v194 offset:5120\n" \
"v_mfma_f32_16x16x32_bf16 v[8:11], a[44:47], a[20:23], v[8:11]\n" \
"s_lshl_b32 s82, s82, 5\n" \
"ds_read_b64_tr_b16 a[212:213], v194 offset:14336\n" \
"v_mfma_f32_16x16x32_bf16 v[48:51], a[32:35], a[16:19], v[48:51]\n" \
"s_waitcnt lgkmcnt(3)\n" \
"s_add_u32 s82, s82, s47\n" \
"ds_read_b64_tr_b16 a[214:215], v194 offset:17408\n" \
"v_mfma_f32_16x16x32_bf16 v[48:51], a[36:39], a[20:23], v[48:51]\n" \
"buffer_load_dwordx4 v234, s[56:59], s81 offen offset:1024 sc1 lds\n" \
"v_mfma_f32_16x16x32_bf16 v[56:59], a[32:35], a[24:27], v[56:59]\n" \
"ds_read_b64_tr_b16 a[216:217], v195 offset:2048\n" \
"v_mfma_f32_16x16x32_bf16 v[56:59], a[36:39], a[28:31], v[56:59]\n" \
"s_add_u32 s48, s48, 0x1000\n" \
"ds_read_b64_tr_b16 a[218:219], v195 offset:5120\n" \
"v_mfma_f32_16x16x32_bf16 v[16:19], a[40:43], a[24:27], v[16:19]\n" \
"s_sub_u32 s86, s86, 32768\n" \
"s_cselect_b32 s86, 131072, s86\n" \
"ds_read_b64_tr_b16 a[220:221], v195 offset:14336\n" \
"v_mfma_f32_16x16x32_bf16 v[16:19], a[44:47], a[28:31], v[16:19]\n" \
"buffer_load_dwordx4 v234, s[56:59], s81 offen offset:2048 sc1 lds\n" \
"v_mfma_f32_16x16x32_bf16 v[20:23], a[48:51], a[24:27], v[20:23]\n" \
"ds_read_b64_tr_b16 a[222:223], v195 offset:17408\n" \
"v_mfma_f32_16x16x32_bf16 v[20:23], a[52:55], a[28:31], v[20:23]\n" \
"v_mfma_f32_16x16x32_bf16 v[28:31], a[48:51], a[192:195], v[28:31]\n" \
"v_mfma_f32_16x16x32_bf16 v[28:31], a[52:55], a[196:199], v[28:31]\n" \
"buffer_load_dwordx4 v234, s[56:59], s81 offen offset:3072 sc1 lds\n" \
"v_mfma_f32_16x16x32_bf16 v[24:27], a[40:43], a[192:195], v[24:27]\n" \
"v_mfma_f32_16x16x32_bf16 v[24:27], a[44:47], a[196:199], v[24:27]\n" \
"v_mfma_f32_16x16x32_bf16 v[64:67], a[32:35], a[192:195], v[64:67]\n" \
"s_add_u32 m0, m0, 0x4000\n" \
"v_mfma_f32_16x16x32_bf16 v[64:67], a[36:39], a[196:199], v[64:67]\n" \
"buffer_load_dwordx4 v216, s[36:39], s82 offen offset:0 lds\n" \
"s_waitcnt vmcnt(13) lgkmcnt(0)\n" \
"s_barrier\n" \
"v_mfma_f32_16x16x32_bf16 v[44:47], a[200:203], a[8:11], v[44:47]\n" \
"ds_read_b64_tr_b16 a[96:97], v214 offset:16384\n" \
"v_mfma_f32_16x16x32_bf16 v[44:47], a[204:207], a[12:15], v[44:47]\n" \
"ds_read_b64_tr_b16 a[98:99], v214 offset:18432\n" \
"v_mfma_f32_16x16x32_bf16 v[52:55], a[200:203], a[16:19], v[52:55]\n" \
"ds_read_b64_tr_b16 a[100:101], v214 offset:24576\n" \
"v_mfma_f32_16x16x32_bf16 v[52:55], a[204:207], a[20:23], v[52:55]\n" \
"buffer_load_dwordx4 v217, s[36:39], s82 offen offset:1024 lds\n" \
"v_mfma_f32_16x16x32_bf16 v[60:63], a[200:203], a[24:27], v[60:63]\n" \
"ds_read_b64_tr_b16 a[102:103], v214 offset:26624\n" \
"v_cvt_pk_bf16_f32 v112, v0, v1\n" \
"v_mfma_f32_16x16x32_bf16 v[60:63], a[204:207], a[28:31], v[60:63]\n" \
"Lpg9609_%=:\n" \
"Lst1x3_%=:\n" \
"s_cbranch_execz Lpg11486_%=\n" \
"ds_read_b128 a[64:67], v213 offset:0\n" \
"v_cvt_pk_bf16_f32 v113, v2, v3\n" \
"v_mfma_f32_16x16x32_bf16 v[68:71], a[200:203], a[192:195], v[68:71]\n" \
"ds_read_b128 a[68:71], v213 offset:2048\n" \
"v_cvt_pk_bf16_f32 v114, v4, v5\n" \
"v_mfma_f32_16x16x32_bf16 v[68:71], a[204:207], a[196:199], v[68:71]\n" \
"buffer_load_dwordx4 v218, s[36:39], s82 offen offset:2048 lds\n" \
"v_mfma_f32_16x16x32_bf16 v[72:75], a[0:3], a[208:211], v[72:75]\n" \
"ds_read_b64_tr_b16 a[104:105], v215 offset:16384\n" \
"v_cvt_pk_bf16_f32 v115, v6, v7\n" \
"v_mfma_f32_16x16x32_bf16 v[72:75], a[4:7], a[212:215], v[72:75]\n" \
"ds_read_b64_tr_b16 a[106:107], v215 offset:18432\n" \
"v_cvt_pk_bf16_f32 v116, v8, v9\n" \
"v_mfma_f32_16x16x32_bf16 v[80:83], a[8:11], a[208:211], v[80:83]\n" \
"ds_read_b64_tr_b16 a[108:109], v215 offset:24576\n" \
"v_cvt_pk_bf16_f32 v117, v10, v11\n" \
"v_mfma_f32_16x16x32_bf16 v[80:83], a[12:15], a[212:215], v[80:83]\n" \
"buffer_load_dwordx4 v219, s[36:39], s82 offen offset:3072 lds\n" \
"v_mfma_f32_16x16x32_bf16 v[84:87], a[8:11], a[216:219], v[84:87]\n" \
"s_xor_b32 s82, s48, s49\n" \
"s_cmp_lt_u32 s48, s50\n" \
"s_cselect_b32 s82, s82, s48\n" \
"ds_read_b64_tr_b16 a[110:111], v215 offset:26624\n" \
"v_mfma_f32_16x16x32_bf16 v[84:87], a[12:15], a[220:223], v[84:87]\n" \
"s_add_u32 s81, s82, s46\n" \
"ds_read_b128 a[72:75], v213 offset:256\n" \
"v_cvt_pk_bf16_f32 v118, v12, v13\n" \
"v_mfma_f32_16x16x32_bf16 v[92:95], a[16:19], a[216:219], v[92:95]\n" \
"s_add_u32 m0, s86, s79\n" \
"ds_read_b128 a[76:79], v213 offset:2304\n" \
"v_cvt_pk_bf16_f32 v119, v14, v15\n" \
"v_mfma_f32_16x16x32_bf16 v[92:95], a[20:23], a[220:223], v[92:95]\n" \
"buffer_load_dwordx4 v234, s[56:59], s81 offen offset:0 sc1 lds\n" \
"v_mfma_f32_16x16x32_bf16 v[88:91], a[16:19], a[208:211], v[88:91]\n" \
"ds_read_b128 a[80:83], v213 offset:4096\n" \
"v_cvt_pk_bf16_f32 v120, v16, v17\n" \
"v_mfma_f32_16x16x32_bf16 v[88:91], a[20:23], a[212:215], v[88:91]\n" \
"s_lshl_b32 s82, s82, 5\n" \
"ds_read_b128 a[84:87], v213 offset:6144\n" \
"v_cvt_pk_bf16_f32 v121, v18, v19\n" \
"v_mfma_f32_16x16x32_bf16 v[96:99], a[24:27], a[208:211], v[96:99]\n" \
"s_add_u32 s82, s82, s47\n" \
"v_cvt_pk_bf16_f32 v122, v20, v21\n" \
"v_cvt_pk_bf16_f32 v123, v22, v23\n" \
"v_mfma_f32_16x16x32_bf16 v[96:99], a[28:31], a[212:215], v[96:99]\n" \
"buffer_load_dwordx4 v234, s[56:59], s81 offen offset:1024 sc1 lds\n" \
"v_mfma_f32_16x16x32_bf16 v[100:103], a[24:27], a[216:219], v[100:103]\n" \
"v_cvt_pk_bf16_f32 v124, v24, v25\n" \
"v_cvt_pk_bf16_f32 v125, v26, v27\n" \
"v_mfma_f32_16x16x32_bf16 v[100:103], a[28:31], a[220:223], v[100:103]\n" \
"s_add_u32 s48, s48, 0x1000\n" \
"v_cvt_pk_bf16_f32 v126, v28, v29\n" \
"v_cvt_pk_bf16_f32 v127, v30, v31\n" \
"v_mfma_f32_16x16x32_bf16 v[108:111], a[192:195], a[216:219], v[108:111]\n" \
"s_sub_u32 s86, s86, 32768\n" \
"s_cselect_b32 s86, 131072, s86\n" \
"v_cvt_pk_bf16_f32 v128, v32, v33\n" \
"v_mfma_f32_16x16x32_bf16 v[108:111], a[196:199], a[220:223], v[108:111]\n" \
"buffer_load_dwordx4 v234, s[56:59], s81 offen offset:2048 sc1 lds\n" \
"v_mfma_f32_16x16x32_bf16 v[104:107], a[192:195], a[208:211], v[104:107]\n" \
"v_cvt_pk_bf16_f32 v129, v34, v35\n" \
"v_xor_b32 v145, 0x2000, v223\n" \
"v_mfma_f32_16x16x32_bf16 v[104:107], a[196:199], a[212:215], v[104:107]\n" \
"s_waitcnt lgkmcnt(0)\n" \
"v_cvt_pk_bf16_f32 v192, v72, v73\n" \
"v_cvt_pk_bf16_f32 v193, v74, v75\n" \
"ds_write_b64 v145, v[128:129]\n" \
"s_nop 0\n" \
"ds_write_b64 v145, v[192:193] offset:8\n" \
"v_cvt_pk_bf16_f32 v132, v40, v41\n" \
"v_cvt_pk_bf16_f32 v133, v42, v43\n" \
"v_cvt_pk_bf16_f32 v134, v44, v45\n" \
"v_cvt_pk_bf16_f32 v135, v46, v47\n" \
"v_cvt_pk_bf16_f32 v136, v48, v49\n" \
"v_cvt_pk_bf16_f32 v137, v50, v51\n" \
"v_cvt_pk_bf16_f32 v138, v52, v53\n" \
"v_cvt_pk_bf16_f32 v139, v54, v55\n" \
"v_cvt_pk_bf16_f32 v140, v56, v57\n" \
"v_cvt_pk_bf16_f32 v141, v58, v59\n" \
"v_cvt_pk_bf16_f32 v142, v60, v61\n" \
"v_cvt_pk_bf16_f32 v143, v62, v63\n" \
"v_mfma_f32_16x16x32_bf16 v[0:3], a[96:99], a[64:67], 0\n" \
"ds_read_b128 a[88:91], v213 offset:4352\n" \
"v_mfma_f32_16x16x32_bf16 v[0:3], a[100:103], a[68:71], v[0:3]\n" \
"ds_read_b128 a[92:95], v213 offset:6400\n" \
"v_mfma_f32_16x16x32_bf16 v[4:7], a[104:107], a[64:67], 0\n" \
"ds_read_b64_tr_b16 a[112:113], v214 offset:16896\n" \
"v_mfma_f32_16x16x32_bf16 v[4:7], a[108:111], a[68:71], v[4:7]\n" \
"ds_read_b64_tr_b16 a[114:115], v214 offset:18944\n" \
"v_mfma_f32_16x16x32_bf16 v[20:23], a[104:107], a[72:75], 0\n" \
"buffer_load_dwordx4 v234, s[56:59], s81 offen offset:3072 sc1 lds\n" \
"v_mfma_f32_16x16x32_bf16 v[20:23], a[108:111], a[76:79], v[20:23]\n" \
"ds_read_b64_tr_b16 a[116:117], v214 offset:25088\n" \
"v_cvt_pk_bf16_f32 v64, v64, v65\n" \
"v_cvt_pk_bf16_f32 v65, v66, v67\n" \
"v_cvt_pk_bf16_f32 v66, v68, v69\n" \
"v_mfma_f32_16x16x32_bf16 v[16:19], a[96:99], a[72:75], 0\n" \
"ds_read_b64_tr_b16 a[118:119], v214 offset:27136\n" \
"v_cvt_pk_bf16_f32 v67, v70, v71\n" \
"v_mfma_f32_16x16x32_bf16 v[16:19], a[100:103], a[76:79], v[16:19]\n" \
"s_add_u32 m0, m0, 0x4000\n" \
"ds_read_b64_tr_b16 a[120:121], v215 offset:16896\n" \
"v_cvt_pk_bf16_f32 v194, v80, v81\n" \
"v_cvt_pk_bf16_f32 v195, v82, v83\n" \
"v_cvt_pk_bf16_f32 v74, v84, v85\n" \
"v_cvt_pk_bf16_f32 v75, v86, v87\n" \
"v_mfma_f32_16x16x32_bf16 v[32:35], a[96:99], a[80:83], 0\n" \
"buffer_load_dwordx4 v216, s[36:39], s82 offen offset:0 lds\n" \
"v_mfma_f32_16x16x32_bf16 v[32:35], a[100:103], a[84:87], v[32:35]\n" \
"s_sub_u32 s85, s85, 32768\n" \
"s_cselect_b32 s85, 131072, s85\n" \
"ds_read_b64_tr_b16 a[122:123], v215 offset:18944\n" \
"v_cvt_pk_bf16_f32 v196, v88, v89\n" \
"v_cvt_pk_bf16_f32 v197, v90, v91\n" \
"v_mfma_f32_16x16x32_bf16 v[176:179], v[64:67], v[124:127], v[152:155]\n" \
"v_mfma_f32_16x16x32_bf16 v[172:175], v[140:143], v[124:127], v[156:159]\n" \
"v_mfma_f32_16x16x32_bf16 v[168:171], v[140:143], v[120:123], v[152:155]\n" \
"v_mfma_f32_16x16x32_bf16 v[164:167], v[136:139], v[120:123], v[156:159]\n" \
"v_mfma_f32_16x16x32_bf16 v[160:163], v[136:139], v[116:119], v[152:155]\n" \
"v_mfma_f32_16x16x32_bf16 v[156:159], v[132:135], v[116:119], v[156:159]\n" \
"v_mfma_f32_16x16x32_bf16 v[152:155], v[132:135], v[112:115], v[152:155]\n" \
"v_mfma_f32_16x16x32_bf16 v[36:39], a[104:107], a[80:83], 0\n" \
"ds_read_b64_tr_b16 a[124:125], v215 offset:25088\n" \
"v_add_u32 v213, s85, v211\n" \
"v_mfma_f32_16x16x32_bf16 v[36:39], a[108:111], a[84:87], v[36:39]\n" \
"s_waitcnt lgkmcnt(3)\n" \
"ds_read_b64_tr_b16 a[126:127], v215 offset:27136\n" \
"v_mfma_f32_16x16x32_bf16 v[52:55], a[104:107], a[88:91], 0\n" \
"buffer_load_dwordx4 v217, s[36:39], s82 offen offset:1024 lds\n" \
"v_mfma_f32_16x16x32_bf16 v[52:55], a[108:111], a[92:95], v[52:55]\n" \
"v_add_u32 v214, s85, v212\n" \
"v_xor_b32 v215, 32, v214\n" \
"v_mfma_f32_16x16x32_bf16 v[48:51], a[96:99], a[88:91], 0\n" \
"buffer_load_dwordx4 v218, s[36:39], s82 offen offset:2048 lds\n" \
"v_mfma_f32_16x16x32_bf16 v[48:51], a[100:103], a[92:95], v[48:51]\n" \
"s_waitcnt vmcnt(15) lgkmcnt(0)\n" \
"s_barrier\n" \
"v_mfma_f32_16x16x32_bf16 v[8:11], a[112:115], a[64:67], 0\n" \
"ds_read_b64 v[130:131], v223\n" \
"ds_read_b64_tr_b16 a[96:97], v214 offset:16384\n" \
"ds_read_b64_tr_b16 a[98:99], v214 offset:18432\n" \
"v_mfma_f32_16x16x32_bf16 v[8:11], a[116:119], a[68:71], v[8:11]\n" \
"ds_read_b64 v[72:73], v223 offset:8\n" \
"ds_read_b64_tr_b16 a[100:101], v214 offset:24576\n" \
"ds_read_b64_tr_b16 a[102:103], v214 offset:26624\n" \
"v_mfma_f32_16x16x32_bf16 v[12:15], a[120:123], a[64:67], 0\n" \
"buffer_load_dwordx4 v219, s[36:39], s82 offen offset:3072 lds\n" \
"v_mfma_f32_16x16x32_bf16 v[12:15], a[124:127], a[68:71], v[12:15]\n" \
"s_xor_b32 s82, s48, s49\n" \
"s_cmp_lt_u32 s48, s50\n" \
"s_cselect_b32 s82, s82, s48\n" \
"ds_read_b128 a[64:67], v213 offset:0\n" \
"v_mfma_f32_16x16x32_bf16 v[28:31], a[120:123], a[72:75], 0\n" \
"s_add_u32 s81, s82, s46\n" \
"ds_read_b128 a[68:71], v213 offset:2048\n" \
"v_mfma_f32_16x16x32_bf16 v[28:31], a[124:127], a[76:79], v[28:31]\n" \
"s_add_u32 m0, s86, s79\n" \
"ds_read_b64_tr_b16 a[104:105], v215 offset:16384\n" \
"ds_read_b64_tr_b16 a[106:107], v215 offset:18432\n" \
"v_mfma_f32_16x16x32_bf16 v[24:27], a[112:115], a[72:75], 0\n" \
"buffer_load_dwordx4 v234, s[56:59], s81 offen offset:0 sc1 lds\n" \
"v_mfma_f32_16x16x32_bf16 v[24:27], a[116:119], a[76:79], v[24:27]\n" \
"s_waitcnt lgkmcnt(4)\n" \
"ds_read_b128 a[72:75], v213 offset:256\n" \
"v_mfma_f32_16x16x32_bf16 v[40:43], a[112:115], a[80:83], 0\n" \
"s_lshl_b32 s82, s82, 5\n" \
"ds_read_b128 a[76:79], v213 offset:2304\n" \
"v_mfma_f32_16x16x32_bf16 v[148:151], v[128:131], v[112:115], v[148:151]\n" \
"v_max_f32 v172, v172, v176\n" \
"v_max_f32 v173, v173, v177\n" \
"v_max_f32 v174, v174, v178\n" \
"v_max_f32 v175, v175, v179\n" \
"v_max_f32 v164, v164, v168\n" \
"v_max_f32 v165, v165, v169\n" \
"v_max_f32 v166, v166, v170\n" \
"v_max_f32 v167, v167, v171\n" \
"v_max_f32 v156, v156, v160\n" \
"v_max_f32 v157, v157, v161\n" \
"v_max_f32 v158, v158, v162\n" \
"v_max_f32 v159, v159, v163\n" \
"v_max_f32 v148, v148, v152\n" \
"v_max_f32 v149, v149, v153\n" \
"v_max_f32 v150, v150, v154\n" \
"v_max_f32 v151, v151, v155\n" \
"v_max3_f32 v183, v172, v173, v174\n" \
"v_max3_f32 v182, v164, v165, v166\n" \
"v_max3_f32 v181, v156, v157, v158\n" \
"v_mfma_f32_16x16x32_bf16 v[40:43], a[116:119], a[84:87], v[40:43]\n" \
"s_add_u32 s82, s82, s47\n" \
"ds_read_b64_tr_b16 a[108:109], v215 offset:24576\n" \
"ds_read_b64_tr_b16 a[110:111], v215 offset:26624\n" \
"v_max3_f32 v180, v148, v149, v150\n" \
"v_mfma_f32_16x16x32_bf16 v[44:47], a[120:123], a[80:83], 0\n" \
"buffer_load_dwordx4 v234, s[56:59], s81 offen offset:1024 sc1 lds\n" \
"v_mfma_f32_16x16x32_bf16 v[44:47], a[124:127], a[84:87], v[44:47]\n" \
"s_add_u32 s48, s48, 0x1000\n" \
"s_sub_u32 s86, s86, 32768\n" \
"s_cselect_b32 s86, 131072, s86\n" \
"ds_read_b128 a[80:83], v213 offset:4096\n" \
"v_max_f32 v183, v183, v175\n" \
"v_max_f32 v182, v182, v167\n" \
"v_max_f32 v181, v181, v159\n" \
"v_mfma_f32_16x16x32_bf16 v[60:63], a[120:123], a[88:91], 0\n" \
"s_waitcnt lgkmcnt(4)\n" \
"buffer_load_dwordx4 v234, s[56:59], s81 offen offset:2048 sc1 lds\n" \
"v_max_f32 v180, v180, v151\n" \
"v_mfma_f32_16x16x32_bf16 v[60:63], a[124:127], a[92:95], v[60:63]\n" \
"ds_read_b128 a[84:87], v213 offset:6144\n" \
"v_cvt_pk_bf16_f32 v76, v92, v93\n" \
"v_cvt_pk_bf16_f32 v77, v94, v95\n" \
"v_mfma_f32_16x16x32_bf16 v[56:59], a[112:115], a[88:91], 0\n" \
"buffer_load_dwordx4 v234, s[56:59], s81 offen offset:3072 sc1 lds\n" \
"v_mfma_f32_16x16x32_bf16 v[56:59], a[116:119], a[92:95], v[56:59]\n" \
"s_getpc_b64 s[44:45]\n" \
"Lop_%=:\n" \
"s_add_u32 s44, s44, Lor_%=-Lop_%=\n" \
"s_addc_u32 s45, s45, 0\n" \
"v_permlane32_swap_b32 v180, v181\n" \
"v_permlane32_swap_b32 v182, v183\n" \
"v_max_f32 v180, v180, v181\n" \
"v_mfma_f32_16x16x32_bf16 v[0:3], a[96:99], a[64:67], v[0:3]\n" \
"ds_read_b128 a[88:91], v213 offset:4352\n" \
"v_max_f32 v182, v182, v183\n" \
"v_mfma_f32_16x16x32_bf16 v[0:3], a[100:103], a[68:71], v[0:3]\n" \
"s_waitcnt lgkmcnt(2)\n" \
"ds_read_b128 a[92:95], v213 offset:6400\n" \
"v_cvt_pk_bf16_f32 v198, v96, v97\n" \
"v_cvt_pk_bf16_f32 v199, v98, v99\n" \
"v_permlane16_swap_b32 v180, v182\n" \
"v_mfma_f32_16x16x32_bf16 v[4:7], a[104:107], a[64:67], v[4:7]\n" \
"ds_read_b64_tr_b16 a[112:113], v214 offset:16896\n" \
"v_max_f32 v180, v180, v182\n" \
"v_mfma_f32_16x16x32_bf16 v[4:7], a[108:111], a[68:71], v[4:7]\n" \
"s_add_u32 m0, m0, 0x4000\n" \
"ds_read_b64_tr_b16 a[114:115], v214 offset:18944\n" \
"v_mul_f32 v180, v180, v144\n" \
"v_cvt_pk_bf16_f32 v78, v100, v101\n" \
"v_cvt_pk_bf16_f32 v79, v102, v103\n" \
"v_mov_b32 v182, v180\n" \
"v_mfma_f32_16x16x32_bf16 v[20:23], a[104:107], a[72:75], v[20:23]\n" \
"buffer_load_dwordx4 v216, s[36:39], s82 offen offset:0 lds\n" \
"v_mfma_f32_16x16x32_bf16 v[20:23], a[108:111], a[76:79], v[20:23]\n" \
"ds_read_b64_tr_b16 a[116:117], v214 offset:25088\n" \
"v_cvt_pk_bf16_f32 v200, v104, v105\n" \
"v_cvt_pk_bf16_f32 v201, v106, v107\n" \
"v_permlane16_swap_b32 v180, v182\n" \
"v_mfma_f32_16x16x32_bf16 v[16:19], a[96:99], a[72:75], v[16:19]\n" \
"ds_read_b64_tr_b16 a[118:119], v214 offset:27136\n" \
"v_mov_b32 v181, v180\n" \
"v_mfma_f32_16x16x32_bf16 v[16:19], a[100:103], a[76:79], v[16:19]\n" \
"s_waitcnt lgkmcnt(3)\n" \
"ds_read_b64_tr_b16 a[120:121], v215 offset:16896\n" \
"v_mov_b32 v183, v182\n" \
"v_cvt_pk_bf16_f32 v80, v108, v109\n" \
"v_cvt_pk_bf16_f32 v81, v110, v111\n" \
"v_permlane32_swap_b32 v180, v181\n" \
"v_mfma_f32_16x16x32_bf16 v[32:35], a[96:99], a[80:83], v[32:35]\n" \
"buffer_load_dwordx4 v217, s[36:39], s82 offen offset:1024 lds\n" \
"v_mfma_f32_16x16x32_bf16 v[32:35], a[100:103], a[84:87], v[32:35]\n" \
"s_sub_u32 s85, s85, 32768\n" \
"s_cselect_b32 s85, 131072, s85\n" \
"ds_read_b64_tr_b16 a[122:123], v215 offset:18944\n" \
"v_permlane32_swap_b32 v182, v183\n" \
"v_fma_f32 v172, v172, v144, -v183\n" \
"v_mfma_f32_16x16x32_bf16 v[36:39], a[104:107], a[80:83], v[36:39]\n" \
"ds_read_b64_tr_b16 a[124:125], v215 offset:25088\n" \
"v_add_u32 v213, s85, v211\n" \
"v_mfma_f32_16x16x32_bf16 v[36:39], a[108:111], a[84:87], v[36:39]\n" \
"ds_read_b64_tr_b16 a[126:127], v215 offset:27136\n" \
"v_fma_f32 v164, v164, v144, -v182\n" \
"v_fma_f32 v156, v156, v144, -v181\n" \
"v_fma_f32 v148, v148, v144, -v180\n" \
"v_fma_f32 v173, v173, v144, -v183\n" \
"v_mfma_f32_16x16x32_bf16 v[52:55], a[104:107], a[88:91], v[52:55]\n" \
"buffer_load_dwordx4 v218, s[36:39], s82 offen offset:2048 lds\n" \
"v_mfma_f32_16x16x32_bf16 v[52:55], a[108:111], a[92:95], v[52:55]\n" \
"v_add_u32 v214, s85, v212\n" \
"v_xor_b32 v215, 32, v214\n" \
"v_fma_f32 v165, v165, v144, -v182\n" \
"v_fma_f32 v157, v157, v144, -v181\n" \
"v_mfma_f32_16x16x32_bf16 v[48:51], a[96:99], a[88:91], v[48:51]\n" \
"buffer_load_dwordx4 v219, s[36:39], s82 offen offset:3072 lds\n" \
"v_mfma_f32_16x16x32_bf16 v[48:51], a[100:103], a[92:95], v[48:51]\n" \
"s_xor_b32 s82, s48, s49\n" \
"s_cmp_lt_u32 s48, s50\n" \
"s_cselect_b32 s82, s82, s48\n" \
"v_fma_f32 v149, v149, v144, -v180\n" \
"v_fma_f32 v174, v174, v144, -v183\n" \
"v_fma_f32 v166, v166, v144, -v182\n" \
"s_waitcnt vmcnt(16) lgkmcnt(0)\n" \
"s_barrier\n" \
"v_mfma_f32_16x16x32_bf16 v[8:11], a[112:115], a[64:67], v[8:11]\n" \
"s_add_u32 s81, s82, s46\n" \
"ds_read_b64_tr_b16 a[96:97], v214 offset:16384\n" \
"ds_read_b64_tr_b16 a[98:99], v214 offset:18432\n" \
"v_mfma_f32_16x16x32_bf16 v[8:11], a[116:119], a[68:71], v[8:11]\n" \
"s_add_u32 m0, s86, s79\n" \
"ds_read_b64_tr_b16 a[100:101], v214 offset:24576\n" \
"ds_read_b64_tr_b16 a[102:103], v214 offset:26624\n" \
"v_fma_f32 v158, v158, v144, -v181\n" \
"v_fma_f32 v150, v150, v144, -v180\n" \
"v_mfma_f32_16x16x32_bf16 v[12:15], a[120:123], a[64:67], v[12:15]\n" \
"buffer_load_dwordx4 v234, s[56:59], s81 offen offset:0 sc1 lds\n" \
"v_mfma_f32_16x16x32_bf16 v[12:15], a[124:127], a[68:71], v[12:15]\n" \
"ds_read_b128 a[64:67], v213 offset:0\n" \
"v_fma_f32 v175, v175, v144, -v183\n" \
"v_fma_f32 v167, v167, v144, -v182\n" \
"v_fma_f32 v159, v159, v144, -v181\n" \
"v_fma_f32 v151, v151, v144, -v180\n" \
"v_mfma_f32_16x16x32_bf16 v[28:31], a[120:123], a[72:75], v[28:31]\n" \
"s_lshl_b32 s82, s82, 5\n" \
"ds_read_b128 a[68:71], v213 offset:2048\n" \
"v_mfma_f32_16x16x32_bf16 v[28:31], a[124:127], a[76:79], v[28:31]\n" \
"s_add_u32 s82, s82, s47\n" \
"ds_read_b64_tr_b16 a[104:105], v215 offset:16384\n" \
"ds_read_b64_tr_b16 a[106:107], v215 offset:18432\n" \
"v_exp_f32 v172, v172\n" \
"v_mfma_f32_16x16x32_bf16 v[24:27], a[112:115], a[72:75], v[24:27]\n" \
"buffer_load_dwordx4 v234, s[56:59], s81 offen offset:1024 sc1 lds\n" \
"v_mfma_f32_16x16x32_bf16 v[24:27], a[116:119], a[76:79], v[24:27]\n" \
"ds_read_b128 a[72:75], v213 offset:256\n" \
"v_exp_f32 v164, v164\n" \
"v_exp_f32 v156, v156\n" \
"v_mfma_f32_16x16x32_bf16 v[40:43], a[112:115], a[80:83], v[40:43]\n" \
"s_add_u32 s48, s48, 0x1000\n" \
"ds_read_b128 a[76:79], v213 offset:2304\n" \
"v_mfma_f32_16x16x32_bf16 v[40:43], a[116:119], a[84:87], v[40:43]\n" \
"s_sub_u32 s86, s86, 32768\n" \
"s_cselect_b32 s86, 131072, s86\n" \
"ds_read_b64_tr_b16 a[108:109], v215 offset:24576\n" \
"ds_read_b64_tr_b16 a[110:111], v215 offset:26624\n" \
"v_exp_f32 v148, v148\n" \
"v_mfma_f32_16x16x32_bf16 v[44:47], a[120:123], a[80:83], v[44:47]\n" \
"buffer_load_dwordx4 v234, s[56:59], s81 offen offset:2048 sc1 lds\n" \
"v_mfma_f32_16x16x32_bf16 v[44:47], a[124:127], a[84:87], v[44:47]\n" \
"ds_read_b128 a[80:83], v213 offset:4096\n" \
"v_exp_f32 v173, v173\n" \
"v_exp_f32 v165, v165\n" \
"v_mfma_f32_16x16x32_bf16 v[60:63], a[120:123], a[88:91], v[60:63]\n" \
"s_waitcnt lgkmcnt(4)\n" \
"buffer_load_dwordx4 v234, s[56:59], s81 offen offset:3072 sc1 lds\n" \
"v_mfma_f32_16x16x32_bf16 v[60:63], a[124:127], a[92:95], v[60:63]\n" \
"s_add_u32 m0, m0, 0x4000\n" \
"ds_read_b128 a[84:87], v213 offset:6144\n" \
"v_exp_f32 v157, v157\n" \
"v_mfma_f32_16x16x32_bf16 v[56:59], a[112:115], a[88:91], v[56:59]\n" \
"buffer_load_dwordx4 v216, s[36:39], s82 offen offset:0 lds\n" \
"v_mfma_f32_16x16x32_bf16 v[56:59], a[116:119], a[92:95], v[56:59]\n" \
"v_exp_f32 v149, v149\n" \
"v_exp_f32 v174, v174\n" \
"v_exp_f32 v166, v166\n" \
"v_mfma_f32_16x16x32_bf16 v[0:3], a[96:99], a[64:67], v[0:3]\n" \
"ds_read_b128 a[88:91], v213 offset:4352\n" \
"v_mfma_f32_16x16x32_bf16 v[0:3], a[100:103], a[68:71], v[0:3]\n" \
"s_waitcnt lgkmcnt(2)\n" \
"ds_read_b128 a[92:95], v213 offset:6400\n" \
"v_exp_f32 v158, v158\n" \
"v_mfma_f32_16x16x32_bf16 v[4:7], a[104:107], a[64:67], v[4:7]\n" \
"ds_read_b64_tr_b16 a[112:113], v214 offset:16896\n" \
"v_mfma_f32_16x16x32_bf16 v[4:7], a[108:111], a[68:71], v[4:7]\n" \
"ds_read_b64_tr_b16 a[114:115], v214 offset:18944\n" \
"v_exp_f32 v150, v150\n" \
"v_exp_f32 v175, v175\n" \
"v_mfma_f32_16x16x32_bf16 v[20:23], a[104:107], a[72:75], v[20:23]\n" \
"buffer_load_dwordx4 v217, s[36:39], s82 offen offset:1024 lds\n" \
"v_mfma_f32_16x16x32_bf16 v[20:23], a[108:111], a[76:79], v[20:23]\n" \
"ds_read_b64_tr_b16 a[116:117], v214 offset:25088\n" \
"v_exp_f32 v167, v167\n" \
"v_exp_f32 v159, v159\n" \
"v_mfma_f32_16x16x32_bf16 v[16:19], a[96:99], a[72:75], v[16:19]\n" \
"ds_read_b64_tr_b16 a[118:119], v214 offset:27136\n" \
"v_mfma_f32_16x16x32_bf16 v[16:19], a[100:103], a[76:79], v[16:19]\n" \
"s_waitcnt lgkmcnt(3)\n" \
"ds_read_b64_tr_b16 a[120:121], v215 offset:16896\n" \
"v_exp_f32 v151, v151\n" \
"v_add_f32 v191, v172, v173\n" \
"v_add_f32 v190, v164, v165\n" \
"v_mfma_f32_16x16x32_bf16 v[32:35], a[96:99], a[80:83], v[32:35]\n" \
"buffer_load_dwordx4 v218, s[36:39], s82 offen offset:2048 lds\n" \
"v_mfma_f32_16x16x32_bf16 v[32:35], a[100:103], a[84:87], v[32:35]\n" \
"s_sub_u32 s85, s85, 32768\n" \
"s_cselect_b32 s85, 131072, s85\n" \
"ds_read_b64_tr_b16 a[122:123], v215 offset:18944\n" \
"v_add_f32 v189, v156, v157\n" \
"v_add_f32 v188, v148, v149\n" \
"v_mfma_f32_16x16x32_bf16 v[36:39], a[104:107], a[80:83], v[36:39]\n" \
"ds_read_b64_tr_b16 a[124:125], v215 offset:25088\n" \
"v_add_u32 v213, s85, v211\n" \
"v_mfma_f32_16x16x32_bf16 v[36:39], a[108:111], a[84:87], v[36:39]\n" \
"ds_read_b64_tr_b16 a[126:127], v215 offset:27136\n" \
"v_add_f32 v191, v191, v174\n" \
"v_add_f32 v190, v190, v166\n" \
"v_add_f32 v189, v189, v158\n" \
"v_add_f32 v188, v188, v150\n" \
"v_mfma_f32_16x16x32_bf16 v[52:55], a[104:107], a[88:91], v[52:55]\n" \
"buffer_load_dwordx4 v219, s[36:39], s82 offen offset:3072 lds\n" \
"v_mfma_f32_16x16x32_bf16 v[52:55], a[108:111], a[92:95], v[52:55]\n" \
"s_xor_b32 s82, s48, s49\n" \
"s_cmp_lt_u32 s48, s50\n" \
"s_cselect_b32 s82, s82, s48\n" \
"s_add_u32 s81, s82, s46\n" \
"s_add_u32 m0, s86, s79\n" \
"v_add_u32 v214, s85, v212\n" \
"v_xor_b32 v215, 32, v214\n" \
"v_add_f32 v191, v191, v175\n" \
"v_add_f32 v190, v190, v167\n" \
"v_mfma_f32_16x16x32_bf16 v[48:51], a[96:99], a[88:91], v[48:51]\n" \
"buffer_load_dwordx4 v234, s[56:59], s81 offen offset:0 sc1 lds\n" \
"v_mfma_f32_16x16x32_bf16 v[48:51], a[100:103], a[92:95], v[48:51]\n" \
"v_add_f32 v189, v189, v159\n" \
"v_add_f32 v188, v188, v151\n" \
"s_waitcnt vmcnt(17) lgkmcnt(0)\n" \
"s_barrier\n" \
"v_mfma_f32_16x16x32_bf16 v[8:11], a[112:115], a[64:67], v[8:11]\n" \
"s_lshl_b32 s82, s82, 5\n" \
"ds_read_b64_tr_b16 a[96:97], v214 offset:16384\n" \
"ds_read_b64_tr_b16 a[98:99], v214 offset:18432\n" \
"v_mfma_f32_16x16x32_bf16 v[8:11], a[116:119], a[68:71], v[8:11]\n" \
"s_add_u32 s82, s82, s47\n" \
"ds_read_b64_tr_b16 a[100:101], v214 offset:24576\n" \
"ds_read_b64_tr_b16 a[102:103], v214 offset:26624\n" \
"v_permlane32_swap_b32 v188, v189\n" \
"v_permlane32_swap_b32 v190, v191\n" \
"v_add_f32 v188, v188, v189\n" \
"v_mfma_f32_16x16x32_bf16 v[12:15], a[120:123], a[64:67], v[12:15]\n" \
"buffer_load_dwordx4 v234, s[56:59], s81 offen offset:1024 sc1 lds\n" \
"v_mfma_f32_16x16x32_bf16 v[12:15], a[124:127], a[68:71], v[12:15]\n" \
"ds_read_b128 a[64:67], v213 offset:0\n" \
"v_add_f32 v190, v190, v191\n" \
"v_mfma_f32_16x16x32_bf16 v[28:31], a[120:123], a[72:75], v[28:31]\n" \
"s_add_u32 s48, s48, 0x1000\n" \
"ds_read_b128 a[68:71], v213 offset:2048\n" \
"v_mfma_f32_16x16x32_bf16 v[28:31], a[124:127], a[76:79], v[28:31]\n" \
"s_sub_u32 s86, s86, 32768\n" \
"s_cselect_b32 s86, 131072, s86\n" \
"ds_read_b64_tr_b16 a[104:105], v215 offset:16384\n" \
"ds_read_b64_tr_b16 a[106:107], v215 offset:18432\n" \
"v_permlane16_swap_b32 v188, v190\n" \
"v_add_f32 v188, v188, v190\n" \
"v_rcp_f32 v188, v188\n" \
"v_mfma_f32_16x16x32_bf16 v[24:27], a[112:115], a[72:75], v[24:27]\n" \
"buffer_load_dwordx4 v234, s[56:59], s81 offen offset:2048 sc1 lds\n" \
"v_mfma_f32_16x16x32_bf16 v[24:27], a[116:119], a[76:79], v[24:27]\n" \
"ds_read_b128 a[72:75], v213 offset:256\n" \
"v_mov_b32 v190, v188\n" \
"v_mfma_f32_16x16x32_bf16 v[40:43], a[112:115], a[80:83], v[40:43]\n" \
"ds_read_b128 a[76:79], v213 offset:2304\n" \
"v_mfma_f32_16x16x32_bf16 v[40:43], a[116:119], a[84:87], v[40:43]\n" \
"ds_read_b64_tr_b16 a[108:109], v215 offset:24576\n" \
"ds_read_b64_tr_b16 a[110:111], v215 offset:26624\n" \
"v_permlane16_swap_b32 v188, v190\n" \
"v_mov_b32 v189, v188\n" \
"v_mov_b32 v191, v190\n" \
"v_mfma_f32_16x16x32_bf16 v[44:47], a[120:123], a[80:83], v[44:47]\n" \
"buffer_load_dwordx4 v234, s[56:59], s81 offen offset:3072 sc1 lds\n" \
"v_mfma_f32_16x16x32_bf16 v[44:47], a[124:127], a[84:87], v[44:47]\n" \
"s_add_u32 m0, m0, 0x4000\n" \
"ds_read_b128 a[80:83], v213 offset:4096\n" \
"v_permlane32_swap_b32 v188, v189\n" \
"v_permlane32_swap_b32 v190, v191\n" \
"v_mul_f32 v172, v172, v191\n" \
"v_mul_f32 v164, v164, v190\n" \
"v_mul_f32 v156, v156, v189\n" \
"v_mul_f32 v148, v148, v188\n" \
"v_mul_f32 v173, v173, v191\n" \
"v_mfma_f32_16x16x32_bf16 v[60:63], a[120:123], a[88:91], v[60:63]\n" \
"s_waitcnt lgkmcnt(4)\n" \
"buffer_load_dwordx4 v216, s[36:39], s82 offen offset:0 lds\n" \
"v_mul_f32 v165, v165, v190\n" \
"v_mfma_f32_16x16x32_bf16 v[60:63], a[124:127], a[92:95], v[60:63]\n" \
"ds_read_b128 a[84:87], v213 offset:6144\n" \
"v_mul_f32 v157, v157, v189\n" \
"v_mul_f32 v149, v149, v188\n" \
"v_mfma_f32_16x16x32_bf16 v[56:59], a[112:115], a[88:91], v[56:59]\n" \
"buffer_load_dwordx4 v217, s[36:39], s82 offen offset:1024 lds\n" \
"v_mfma_f32_16x16x32_bf16 v[56:59], a[116:119], a[92:95], v[56:59]\n" \
"v_mul_f32 v174, v174, v191\n" \
"v_mul_f32 v166, v166, v190\n" \
"v_mul_f32 v158, v158, v189\n" \
"v_mul_f32 v150, v150, v188\n" \
"v_mul_f32 v175, v175, v191\n" \
"v_mfma_f32_16x16x32_bf16 v[0:3], a[96:99], a[64:67], v[0:3]\n" \
"ds_read_b128 a[88:91], v213 offset:4352\n" \
"v_mul_f32 v167, v167, v190\n" \
"v_mfma_f32_16x16x32_bf16 v[0:3], a[100:103], a[68:71], v[0:3]\n" \
"s_waitcnt lgkmcnt(2)\n" \
"ds_read_b128 a[92:95], v213 offset:6400\n" \
"v_mul_f32 v159, v159, v189\n" \
"v_mul_f32 v151, v151, v188\n" \
"v_cvt_pk_bf16_f32 v178, v172, v173\n" \
"v_mfma_f32_16x16x32_bf16 v[4:7], a[104:107], a[64:67], v[4:7]\n" \
"ds_read_b64_tr_b16 a[112:113], v214 offset:16896\n" \
"v_cvt_pk_bf16_f32 v179, v174, v175\n" \
"v_mfma_f32_16x16x32_bf16 v[4:7], a[108:111], a[68:71], v[4:7]\n" \
"ds_read_b64_tr_b16 a[114:115], v214 offset:18944\n" \
"v_cvt_pk_bf16_f32 v170, v164, v165\n" \
"v_cvt_pk_bf16_f32 v171, v166, v167\n" \
"v_cvt_pk_bf16_f32 v162, v156, v157\n" \
"v_cvt_pk_bf16_f32 v163, v158, v159\n" \
"v_mfma_f32_16x16x32_bf16 v[20:23], a[104:107], a[72:75], v[20:23]\n" \
"buffer_load_dwordx4 v218, s[36:39], s82 offen offset:2048 lds\n" \
"v_mfma_f32_16x16x32_bf16 v[20:23], a[108:111], a[76:79], v[20:23]\n" \
"ds_read_b64_tr_b16 a[116:117], v214 offset:25088\n" \
"v_cvt_pk_bf16_f32 v154, v148, v149\n" \
"v_cvt_pk_bf16_f32 v155, v150, v151\n" \
"v_and_b32 v176, v178, v184\n" \
"v_mfma_f32_16x16x32_bf16 v[16:19], a[96:99], a[72:75], v[16:19]\n" \
"ds_read_b64_tr_b16 a[118:119], v214 offset:27136\n" \
"v_and_b32 v177, v179, v185\n" \
"v_mfma_f32_16x16x32_bf16 v[16:19], a[100:103], a[76:79], v[16:19]\n" \
"s_waitcnt lgkmcnt(3)\n" \
"ds_read_b64_tr_b16 a[120:121], v215 offset:16896\n" \
"v_xor_b32 v178, v178, v176\n" \
"v_xor_b32 v179, v179, v177\n" \
"v_and_b32 v168, v170, v184\n" \
"v_and_b32 v169, v171, v185\n" \
"v_mfma_f32_16x16x32_bf16 v[32:35], a[96:99], a[80:83], v[32:35]\n" \
"buffer_load_dwordx4 v219, s[36:39], s82 offen offset:3072 lds\n" \
"v_mfma_f32_16x16x32_bf16 v[32:35], a[100:103], a[84:87], v[32:35]\n" \
"s_sub_u32 s85, s85, 32768\n" \
"s_cselect_b32 s85, 131072, s85\n" \
"s_xor_b32 s82, s48, s49\n" \
"s_cmp_lt_u32 s48, s50\n" \
"s_cselect_b32 s82, s82, s48\n" \
"ds_read_b64_tr_b16 a[122:123], v215 offset:18944\n" \
"v_xor_b32 v170, v170, v168\n" \
"v_xor_b32 v171, v171, v169\n" \
"v_mfma_f32_16x16x32_bf16 v[36:39], a[104:107], a[80:83], v[36:39]\n" \
"s_add_u32 s81, s82, s46\n" \
"ds_read_b64_tr_b16 a[124:125], v215 offset:25088\n" \
"v_add_u32 v213, s85, v211\n" \
"v_mfma_f32_16x16x32_bf16 v[36:39], a[108:111], a[84:87], v[36:39]\n" \
"s_add_u32 m0, s86, s79\n" \
"ds_read_b64_tr_b16 a[126:127], v215 offset:27136\n" \
"v_and_b32 v160, v162, v184\n" \
"v_and_b32 v161, v163, v185\n" \
"v_xor_b32 v162, v162, v160\n" \
"v_xor_b32 v163, v163, v161\n" \
"v_mfma_f32_16x16x32_bf16 v[52:55], a[104:107], a[88:91], v[52:55]\n" \
"buffer_load_dwordx4 v234, s[56:59], s81 offen offset:0 sc1 lds\n" \
"v_mfma_f32_16x16x32_bf16 v[52:55], a[108:111], a[92:95], v[52:55]\n" \
"s_lshl_b32 s82, s82, 5\n" \
"s_add_u32 s82, s82, s47\n" \
"v_add_u32 v214, s85, v212\n" \
"v_xor_b32 v215, 32, v214\n" \
"v_and_b32 v152, v154, v184\n" \
"v_and_b32 v153, v155, v185\n" \
"v_mfma_f32_16x16x32_bf16 v[48:51], a[96:99], a[88:91], v[48:51]\n" \
"buffer_load_dwordx4 v234, s[56:59], s81 offen offset:1024 sc1 lds\n" \
"v_mfma_f32_16x16x32_bf16 v[48:51], a[100:103], a[92:95], v[48:51]\n" \
"v_xor_b32 v154, v154, v152\n" \
"v_xor_b32 v155, v155, v153\n" \
"s_waitcnt vmcnt(18) lgkmcnt(0)\n" \
"s_barrier\n" \
"v_mfma_f32_16x16x32_bf16 v[8:11], a[112:115], a[64:67], v[8:11]\n" \
"s_add_u32 s48, s48, 0x1000\n" \
"ds_read_b64_tr_b16 a[96:97], v214 offset:16384\n" \
"ds_read_b64_tr_b16 a[98:99], v214 offset:18432\n" \
"v_mfma_f32_16x16x32_bf16 v[136:139], v[198:201], v[176:179], 0\n" \
"v_mfma_f32_16x16x32_bf16 v[140:143], v[78:81], v[176:179], 0\n" \
"v_mfma_f32_16x16x32_bf16 v[128:131], v[196:199], v[168:171], 0\n" \
"v_mfma_f32_16x16x32_bf16 v[132:135], v[76:79], v[168:171], 0\n" \
"v_mfma_f32_16x16x32_bf16 v[120:123], v[194:197], v[160:163], 0\n" \
"v_mfma_f32_16x16x32_bf16 v[124:127], v[74:77], v[160:163], 0\n" \
"v_mfma_f32_16x16x32_bf16 v[112:115], v[192:195], v[152:155], 0\n" \
"v_mfma_f32_16x16x32_bf16 v[116:119], v[72:75], v[152:155], 0\n" \
"v_mfma_f32_16x16x32_bf16 v[8:11], a[116:119], a[68:71], v[8:11]\n" \
"s_sub_u32 s86, s86, 32768\n" \
"s_cselect_b32 s86, 131072, s86\n" \
"ds_read_b64_tr_b16 a[100:101], v214 offset:24576\n" \
"ds_read_b64_tr_b16 a[102:103], v214 offset:26624\n" \
"v_mfma_f32_16x16x32_bf16 v[12:15], a[120:123], a[64:67], v[12:15]\n" \
"buffer_load_dwordx4 v234, s[56:59], s81 offen offset:2048 sc1 lds\n" \
"v_mfma_f32_16x16x32_bf16 v[12:15], a[124:127], a[68:71], v[12:15]\n" \
"ds_read_b128 a[64:67], v213 offset:0\n" \
"v_cvt_pk_bf16_f32 v136, v136, v137\n" \
"v_cvt_pk_bf16_f32 v137, v138, v139\n" \
"v_cvt_pk_bf16_f32 v138, v140, v141\n" \
"v_cvt_pk_bf16_f32 v139, v142, v143\n" \
"v_cndmask_b32 v140, v136, v138, s[76:77]\n" \
"v_mfma_f32_16x16x32_bf16 v[28:31], a[120:123], a[72:75], v[28:31]\n" \
"ds_read_b128 a[68:71], v213 offset:2048\n" \
"v_cndmask_b32 v141, v137, v139, s[76:77]\n" \
"v_mfma_f32_16x16x32_bf16 v[28:31], a[124:127], a[76:79], v[28:31]\n" \
"ds_read_b64_tr_b16 a[104:105], v215 offset:16384\n" \
"ds_read_b64_tr_b16 a[106:107], v215 offset:18432\n" \
"v_cndmask_b32 v142, v138, v136, s[76:77]\n" \
"v_cndmask_b32 v143, v139, v137, s[76:77]\n" \
"v_mfma_f32_16x16x32_bf16 v[24:27], a[112:115], a[72:75], v[24:27]\n" \
"buffer_load_dwordx4 v234, s[56:59], s81 offen offset:3072 sc1 lds\n" \
"v_mfma_f32_16x16x32_bf16 v[24:27], a[116:119], a[76:79], v[24:27]\n" \
"ds_read_b128 a[72:75], v213 offset:256\n" \
"v_cvt_pk_bf16_f32 v128, v128, v129\n" \
"v_cvt_pk_bf16_f32 v129, v130, v131\n" \
"v_cvt_pk_bf16_f32 v130, v132, v133\n" \
"v_cvt_pk_bf16_f32 v131, v134, v135\n" \
"v_mfma_f32_16x16x32_bf16 v[40:43], a[112:115], a[80:83], v[40:43]\n" \
"ds_read_b128 a[76:79], v213 offset:2304\n" \
"v_cndmask_b32 v132, v128, v130, s[76:77]\n" \
"v_mfma_f32_16x16x32_bf16 v[40:43], a[116:119], a[84:87], v[40:43]\n" \
"s_add_u32 m0, m0, 0x4000\n" \
"Lpg10211_%=:\n" \
"s_cbranch_execz Lcb1_%=\n" \
"ds_read_b64_tr_b16 a[108:109], v215 offset:24576\n" \
"ds_read_b64_tr_b16 a[110:111], v215 offset:26624\n" \
"v_cndmask_b32 v133, v129, v131, s[76:77]\n" \
"v_cndmask_b32 v134, v130, v128, s[76:77]\n" \
"v_mfma_f32_16x16x32_bf16 v[44:47], a[120:123], a[80:83], v[44:47]\n" \
"buffer_load_dwordx4 v216, s[36:39], s82 offen offset:0 lds\n" \
"v_mfma_f32_16x16x32_bf16 v[44:47], a[124:127], a[84:87], v[44:47]\n" \
"ds_read_b128 a[80:83], v213 offset:4096\n" \
"v_cndmask_b32 v135, v131, v129, s[76:77]\n" \
"v_cvt_pk_bf16_f32 v120, v120, v121\n" \
"v_cvt_pk_bf16_f32 v121, v122, v123\n" \
"v_cvt_pk_bf16_f32 v122, v124, v125\n" \
"v_mfma_f32_16x16x32_bf16 v[60:63], a[120:123], a[88:91], v[60:63]\n" \
"s_waitcnt lgkmcnt(4)\n" \
"buffer_load_dwordx4 v217, s[36:39], s82 offen offset:1024 lds\n" \
"v_cvt_pk_bf16_f32 v123, v126, v127\n" \
"v_mfma_f32_16x16x32_bf16 v[60:63], a[124:127], a[92:95], v[60:63]\n" \
"ds_read_b128 a[84:87], v213 offset:6144\n" \
"v_cndmask_b32 v124, v120, v122, s[76:77]\n" \
"v_cndmask_b32 v125, v121, v123, s[76:77]\n" \
"v_mfma_f32_16x16x32_bf16 v[56:59], a[112:115], a[88:91], v[56:59]\n" \
"buffer_load_dwordx4 v218, s[36:39], s82 offen offset:2048 lds\n" \
"v_mfma_f32_16x16x32_bf16 v[56:59], a[116:119], a[92:95], v[56:59]\n" \
"v_cndmask_b32 v126, v122, v120, s[76:77]\n" \
"v_cndmask_b32 v127, v123, v121, s[76:77]\n" \
"v_cvt_pk_bf16_f32 v112, v112, v113\n" \
"v_cvt_pk_bf16_f32 v113, v114, v115\n" \
"v_cvt_pk_bf16_f32 v114, v116, v117\n" \
"v_mfma_f32_16x16x32_bf16 v[0:3], a[96:99], a[64:67], v[0:3]\n" \
"ds_read_b128 a[88:91], v213 offset:4352\n" \
"v_cvt_pk_bf16_f32 v115, v118, v119\n" \
"v_mfma_f32_16x16x32_bf16 v[0:3], a[100:103], a[68:71], v[0:3]\n" \
"s_waitcnt lgkmcnt(2)\n" \
"ds_read_b128 a[92:95], v213 offset:6400\n" \
"v_cndmask_b32 v116, v112, v114, s[76:77]\n" \
"v_cndmask_b32 v117, v113, v115, s[76:77]\n" \
"v_cndmask_b32 v118, v114, v112, s[76:77]\n" \
"v_mfma_f32_16x16x32_bf16 v[4:7], a[104:107], a[64:67], v[4:7]\n" \
"ds_read_b64_tr_b16 a[112:113], v214 offset:16896\n" \
"v_cndmask_b32 v119, v115, v113, s[76:77]\n" \
"v_mfma_f32_16x16x32_bf16 v[4:7], a[108:111], a[68:71], v[4:7]\n" \
"s_add_u32 s71, s93, 208896\n" \
"ds_read_b64_tr_b16 a[114:115], v214 offset:18944\n" \
"buffer_store_dwordx4 v[140:143], v236, s[56:59], s71 offen sc1\n" \
"s_add_u32 s71, s93, 139264\n" \
"v_mfma_f32_16x16x32_bf16 v[20:23], a[104:107], a[72:75], v[20:23]\n" \
"buffer_load_dwordx4 v219, s[36:39], s82 offen offset:3072 lds\n" \
"v_mfma_f32_16x16x32_bf16 v[20:23], a[108:111], a[76:79], v[20:23]\n" \
"s_xor_b32 s82, s48, s49\n" \
"s_cmp_lt_u32 s48, s50\n" \
"s_cselect_b32 s82, s82, s48\n" \
"ds_read_b64_tr_b16 a[116:117], v214 offset:25088\n" \
"buffer_store_dwordx4 v[132:135], v236, s[56:59], s71 offen sc1\n" \
"s_add_u32 s71, s93, 69632\n" \
"v_mfma_f32_16x16x32_bf16 v[16:19], a[96:99], a[72:75], v[16:19]\n" \
"s_waitcnt lgkmcnt(3)\n" \
"s_add_u32 s81, s82, s46\n" \
"ds_read_b64_tr_b16 a[118:119], v214 offset:27136\n" \
"v_mfma_f32_16x16x32_bf16 v[16:19], a[100:103], a[76:79], v[16:19]\n" \
"s_add_u32 m0, s86, s79\n" \
"ds_read_b64_tr_b16 a[120:121], v215 offset:16896\n" \
"buffer_store_dwordx4 v[124:127], v236, s[56:59], s71 offen sc1\n" \
"s_add_u32 s71, s93, 0\n" \
"v_mfma_f32_16x16x32_bf16 v[32:35], a[96:99], a[80:83], v[32:35]\n" \
"buffer_load_dwordx4 v234, s[56:59], s81 offen offset:0 sc1 lds\n" \
"v_mfma_f32_16x16x32_bf16 v[32:35], a[100:103], a[84:87], v[32:35]\n" \
"s_sub_u32 s85, s85, 32768\n" \
"s_cselect_b32 s85, 131072, s85\n" \
"ds_read_b64_tr_b16 a[122:123], v215 offset:18944\n" \
"buffer_store_dwordx4 v[116:119], v236, s[56:59], s71 offen sc1\n" \
"v_mfma_f32_16x16x32_bf16 v[36:39], a[104:107], a[80:83], v[36:39]\n" \
"s_lshl_b32 s82, s82, 5\n" \
"ds_read_b64_tr_b16 a[124:125], v215 offset:25088\n" \
"v_add_u32_e64 v213, s85, v211\n" \
"v_mfma_f32_16x16x32_bf16 v[36:39], a[108:111], a[84:87], v[36:39]\n" \
"s_add_u32 s82, s82, s47\n" \
"ds_read_b64_tr_b16 a[126:127], v215 offset:27136\n" \
"v_mfma_f32_16x16x32_bf16 v[52:55], a[104:107], a[88:91], v[52:55]\n" \
"buffer_load_dwordx4 v234, s[56:59], s81 offen offset:1024 sc1 lds\n" \
"v_mfma_f32_16x16x32_bf16 v[52:55], a[108:111], a[92:95], v[52:55]\n" \
"s_add_u32 s48, s48, 0x1000\n" \
"s_sub_u32 s86, s86, 32768\n" \
"s_cselect_b32 s86, 131072, s86\n" \
"v_add_u32_e64 v214, s85, v212\n" \
"v_xor_b32_e64 v215, 32, v214\n" \
"v_mfma_f32_16x16x32_bf16 v[48:51], a[96:99], a[88:91], v[48:51]\n" \
"buffer_load_dwordx4 v234, s[56:59], s81 offen offset:2048 sc1 lds\n" \
"v_mfma_f32_16x16x32_bf16 v[48:51], a[100:103], a[92:95], v[48:51]\n" \
"s_waitcnt vmcnt(23) lgkmcnt(0)\n" \
"s_barrier\n" \
"v_mfma_f32_16x16x32_bf16 v[8:11], a[112:115], a[64:67], v[8:11]\n" \
"ds_read_b64_tr_b16 a[96:97], v214 offset:16384\n" \
"ds_read_b64_tr_b16 a[98:99], v214 offset:18432\n" \
"v_mfma_f32_16x16x32_bf16 v[8:11], a[116:119], a[68:71], v[8:11]\n" \
"ds_read_b64_tr_b16 a[100:101], v214 offset:24576\n" \
"ds_read_b64_tr_b16 a[102:103], v214 offset:26624\n" \
"v_mfma_f32_16x16x32_bf16 v[12:15], a[120:123], a[64:67], v[12:15]\n" \
"buffer_load_dwordx4 v234, s[56:59], s81 offen offset:3072 sc1 lds\n" \
"v_mfma_f32_16x16x32_bf16 v[12:15], a[124:127], a[68:71], v[12:15]\n" \
"ds_read_b128 a[64:67], v213 offset:0\n" \
"v_mfma_f32_16x16x32_bf16 v[28:31], a[120:123], a[72:75], v[28:31]\n" \
"ds_read_b128 a[68:71], v213 offset:2048\n" \
"v_mfma_f32_16x16x32_bf16 v[28:31], a[124:127], a[76:79], v[28:31]\n" \
"s_add_u32 m0, m0, 0x4000\n" \
"ds_read_b64_tr_b16 a[104:105], v215 offset:16384\n" \
"ds_read_b64_tr_b16 a[106:107], v215 offset:18432\n" \
"v_mfma_f32_16x16x32_bf16 v[24:27], a[112:115], a[72:75], v[24:27]\n" \
"buffer_load_dwordx4 v216, s[36:39], s82 offen offset:0 lds\n" \
"v_mfma_f32_16x16x32_bf16 v[24:27], a[116:119], a[76:79], v[24:27]\n" \
"ds_read_b128 a[72:75], v213 offset:256\n" \
"v_mfma_f32_16x16x32_bf16 v[40:43], a[112:115], a[80:83], v[40:43]\n" \
"ds_read_b128 a[76:79], v213 offset:2304\n" \
"v_mfma_f32_16x16x32_bf16 v[40:43], a[116:119], a[84:87], v[40:43]\n" \
"ds_read_b64_tr_b16 a[108:109], v215 offset:24576\n" \
"ds_read_b64_tr_b16 a[110:111], v215 offset:26624\n" \
"v_mfma_f32_16x16x32_bf16 v[44:47], a[120:123], a[80:83], v[44:47]\n" \
"buffer_load_dwordx4 v217, s[36:39], s82 offen offset:1024 lds\n" \
"v_mfma_f32_16x16x32_bf16 v[44:47], a[124:127], a[84:87], v[44:47]\n" \
"ds_read_b128 a[80:83], v213 offset:4096\n" \
"v_mfma_f32_16x16x32_bf16 v[60:63], a[120:123], a[88:91], v[60:63]\n" \
"s_waitcnt lgkmcnt(4)\n" \
"buffer_load_dwordx4 v218, s[36:39], s82 offen offset:2048 lds\n" \
"v_mfma_f32_16x16x32_bf16 v[60:63], a[124:127], a[92:95], v[60:63]\n" \
"ds_read_b128 a[84:87], v213 offset:6144\n" \
"v_mfma_f32_16x16x32_bf16 v[56:59], a[112:115], a[88:91], v[56:59]\n" \
"buffer_load_dwordx4 v219, s[36:39], s82 offen offset:3072 lds\n" \
"v_mfma_f32_16x16x32_bf16 v[56:59], a[116:119], a[92:95], v[56:59]\n" \
"s_mov_b32 s72, 2\n" \
"s_nop 0\n" \
"s_nop 0\n" \
"s_nop 0\n" \
"Lo0_%=:\n" \
"v_mfma_f32_16x16x32_bf16 v[0:3], a[96:99], a[64:67], v[0:3]\n" \
"s_waitcnt lgkmcnt(1)\n" \
"s_xor_b32 s82, s48, s49\n" \
"ds_read_b128 a[88:91], v213 offset:4352\n" \
"v_mfma_f32_16x16x32_bf16 v[0:3], a[100:103], a[68:71], v[0:3]\n" \
"s_cmp_lt_u32 s48, s50\n" \
"s_cselect_b32 s82, s82, s48\n" \
"ds_read_b128 a[92:95], v213 offset:6400\n" \
"v_mfma_f32_16x16x32_bf16 v[4:7], a[104:107], a[64:67], v[4:7]\n" \
"s_add_u32 s81, s82, s46\n" \
"ds_read_b64_tr_b16 a[112:113], v214 offset:16896\n" \
"v_mfma_f32_16x16x32_bf16 v[4:7], a[108:111], a[68:71], v[4:7]\n" \
"s_add_u32 m0, s86, s79\n" \
"ds_read_b64_tr_b16 a[114:115], v214 offset:18944\n" \
"v_mfma_f32_16x16x32_bf16 v[20:23], a[104:107], a[72:75], v[20:23]\n" \
"buffer_load_dwordx4 v234, s[56:59], s81 offen offset:0 sc1 lds\n" \
"v_mfma_f32_16x16x32_bf16 v[20:23], a[108:111], a[76:79], v[20:23]\n" \
"ds_read_b64_tr_b16 a[116:117], v214 offset:25088\n" \
"v_mfma_f32_16x16x32_bf16 v[16:19], a[96:99], a[72:75], v[16:19]\n" \
"s_lshl_b32 s82, s82, 5\n" \
"ds_read_b64_tr_b16 a[118:119], v214 offset:27136\n" \
"v_mfma_f32_16x16x32_bf16 v[16:19], a[100:103], a[76:79], v[16:19]\n" \
"s_waitcnt lgkmcnt(3)\n" \
"s_add_u32 s82, s82, s47\n" \
"ds_read_b64_tr_b16 a[120:121], v215 offset:16896\n" \
"v_mfma_f32_16x16x32_bf16 v[32:35], a[96:99], a[80:83], v[32:35]\n" \
"buffer_load_dwordx4 v234, s[56:59], s81 offen offset:1024 sc1 lds\n" \
"v_mfma_f32_16x16x32_bf16 v[32:35], a[100:103], a[84:87], v[32:35]\n" \
"s_sub_u32 s85, s85, 32768\n" \
"s_cselect_b32 s85, 131072, s85\n" \
"ds_read_b64_tr_b16 a[122:123], v215 offset:18944\n" \
"v_mfma_f32_16x16x32_bf16 v[36:39], a[104:107], a[80:83], v[36:39]\n" \
"s_add_u32 s48, s48, 0x1000\n" \
"ds_read_b64_tr_b16 a[124:125], v215 offset:25088\n" \
"v_add_u32 v213, s85, v211\n" \
"v_mfma_f32_16x16x32_bf16 v[36:39], a[108:111], a[84:87], v[36:39]\n" \
"s_sub_u32 s86, s86, 32768\n" \
"s_cselect_b32 s86, 131072, s86\n" \
"ds_read_b64_tr_b16 a[126:127], v215 offset:27136\n" \
"v_mfma_f32_16x16x32_bf16 v[52:55], a[104:107], a[88:91], v[52:55]\n" \
"buffer_load_dwordx4 v234, s[56:59], s81 offen offset:2048 sc1 lds\n" \
"v_mfma_f32_16x16x32_bf16 v[52:55], a[108:111], a[92:95], v[52:55]\n" \
"v_add_u32 v214, s85, v212\n" \
"v_xor_b32 v215, 32, v214\n" \
"v_mfma_f32_16x16x32_bf16 v[48:51], a[96:99], a[88:91], v[48:51]\n" \
"buffer_load_dwordx4 v234, s[56:59], s81 offen offset:3072 sc1 lds\n" \
"v_mfma_f32_16x16x32_bf16 v[48:51], a[100:103], a[92:95], v[48:51]\n" \
"s_waitcnt vmcnt(20) lgkmcnt(0)\n" \
"s_barrier\n" \
"v_mfma_f32_16x16x32_bf16 v[8:11], a[112:115], a[64:67], v[8:11]\n" \
"ds_read_b64_tr_b16 a[96:97], v214 offset:16384\n" \
"ds_read_b64_tr_b16 a[98:99], v214 offset:18432\n" \
"v_mfma_f32_16x16x32_bf16 v[8:11], a[116:119], a[68:71], v[8:11]\n" \
"s_add_u32 m0, m0, 0x4000\n" \
"ds_read_b64_tr_b16 a[100:101], v214 offset:24576\n" \
"ds_read_b64_tr_b16 a[102:103], v214 offset:26624\n" \
"v_mfma_f32_16x16x32_bf16 v[12:15], a[120:123], a[64:67], v[12:15]\n" \
"buffer_load_dwordx4 v216, s[36:39], s82 offen offset:0 lds\n" \
"v_mfma_f32_16x16x32_bf16 v[12:15], a[124:127], a[68:71], v[12:15]\n" \
"ds_read_b128 a[64:67], v213 offset:0\n" \
"v_mfma_f32_16x16x32_bf16 v[28:31], a[120:123], a[72:75], v[28:31]\n" \
"ds_read_b128 a[68:71], v213 offset:2048\n" \
"v_mfma_f32_16x16x32_bf16 v[28:31], a[124:127], a[76:79], v[28:31]\n" \
"ds_read_b64_tr_b16 a[104:105], v215 offset:16384\n" \
"ds_read_b64_tr_b16 a[106:107], v215 offset:18432\n" \
"v_mfma_f32_16x16x32_bf16 v[24:27], a[112:115], a[72:75], v[24:27]\n" \
"buffer_load_dwordx4 v217, s[36:39], s82 offen offset:1024 lds\n" \
"v_mfma_f32_16x16x32_bf16 v[24:27], a[116:119], a[76:79], v[24:27]\n" \
"ds_read_b128 a[72:75], v213 offset:256\n" \
"v_mfma_f32_16x16x32_bf16 v[40:43], a[112:115], a[80:83], v[40:43]\n" \
"ds_read_b128 a[76:79], v213 offset:2304\n" \
"v_mfma_f32_16x16x32_bf16 v[40:43], a[116:119], a[84:87], v[40:43]\n" \
"ds_read_b64_tr_b16 a[108:109], v215 offset:24576\n" \
"ds_read_b64_tr_b16 a[110:111], v215 offset:26624\n" \
"v_mfma_f32_16x16x32_bf16 v[44:47], a[120:123], a[80:83], v[44:47]\n" \
"buffer_load_dwordx4 v218, s[36:39], s82 offen offset:2048 lds\n" \
"v_mfma_f32_16x16x32_bf16 v[44:47], a[124:127], a[84:87], v[44:47]\n" \
"ds_read_b128 a[80:83], v213 offset:4096\n" \
"v_mfma_f32_16x16x32_bf16 v[60:63], a[120:123], a[88:91], v[60:63]\n" \
"s_waitcnt lgkmcnt(4)\n" \
"v_mfma_f32_16x16x32_bf16 v[60:63], a[124:127], a[92:95], v[60:63]\n" \
"s_sub_u32 s72, s72, 1\n" \
"ds_read_b128 a[84:87], v213 offset:6144\n" \
"v_mfma_f32_16x16x32_bf16 v[56:59], a[112:115], a[88:91], v[56:59]\n" \
"buffer_load_dwordx4 v219, s[36:39], s82 offen offset:3072 lds\n" \
"v_mfma_f32_16x16x32_bf16 v[56:59], a[116:119], a[92:95], v[56:59]\n" \
"s_cbranch_scc0 Lo0_%=\n" \
"s_setpc_b64 s[44:45]\n" \
"Lor_%=:\n" \
"buffer_store_dword v230, v231, s[56:59], s12 offen sc1\n" \
"s_mov_b32 s72, s10\n" \
"s_call_b64 s[44:45], Lo0_%=\n" \
"buffer_load_dword v220, v221, s[56:59], s67 offen sc1\n" \
"s_mov_b32 s72, 1\n" \
"s_call_b64 s[44:45], Lo0_%=\n" \
"s_waitcnt vmcnt(16)\n" \
"v_cmp_eq_u32 vcc, s60, v220\n" \
"s_nop 1\n" \
"s_andn2_b32 s70, s74, vcc_lo\n" \
"s_cbranch_scc1 Lpl6_%=\n" \
"Lpr6_%=:\n" \
"s_cmp_eq_u32 s90, 0\n" \
"s_cselect_b32 s46, s46, s83\n" \
"s_cselect_b32 s47, s47, s84\n" \
"s_cselect_b32 s48, s48, 0\n" \
"s_cselect_b64 s[36:37], s[36:37], s[40:41]\n" \
"s_cselect_b32 s38, s38, s42\n" \
"s_mov_b32 s72, 3\n" \
"s_call_b64 s[44:45], Lo0_%=\n" \
"s_cmp_eq_u32 s90, 0\n" \
"s_cbranch_scc0 Lrole1_%=\n" \
"s_mov_b32 s72, s11\n" \
"s_call_b64 s[44:45], Lo0_%=\n" \
"buffer_load_dword v220, v241, s[56:59], s64 offen sc1\n" \
"s_mov_b32 s72, 1\n" \
"s_call_b64 s[44:45], Lo0_%=\n" \
"v_mfma_f32_16x16x32_bf16 v[0:3], a[96:99], a[64:67], v[0:3]\n" \
"s_waitcnt lgkmcnt(1)\n" \
"ds_read_b128 a[88:91], v213 offset:4352\n" \
"v_mov_b32 v74, 0x3f80\n" \
"v_mfma_f32_16x16x32_bf16 v[0:3], a[100:103], a[68:71], v[0:3]\n" \
"s_add_u32 s48, s86, 32768\n" \
"s_cmp_eq_u32 s48, 163840\n" \
"s_cselect_b32 s48, 0, s48\n" \
"ds_read_b128 a[92:95], v213 offset:6400\n" \
"v_mfma_f32_16x16x32_bf16 v[4:7], a[104:107], a[64:67], v[4:7]\n" \
"ds_read_b64_tr_b16 a[112:113], v214 offset:16896\n" \
"v_mov_b32 v64, 0\n" \
"v_mfma_f32_16x16x32_bf16 v[4:7], a[108:111], a[68:71], v[4:7]\n" \
"ds_read_b64_tr_b16 a[114:115], v214 offset:18944\n" \
"v_mov_b32 v65, 0\n" \
"v_mfma_f32_16x16x32_bf16 v[20:23], a[104:107], a[72:75], v[20:23]\n" \
"s_add_u32 s49, s48, 32768\n" \
"s_cmp_eq_u32 s49, 163840\n" \
"s_cselect_b32 s49, 0, s49\n" \
"v_mfma_f32_16x16x32_bf16 v[20:23], a[108:111], a[76:79], v[20:23]\n" \
"ds_read_b64_tr_b16 a[116:117], v214 offset:25088\n" \
"v_add_u32 v224, s49, v211\n" \
"v_mfma_f32_16x16x32_bf16 v[16:19], a[96:99], a[72:75], v[16:19]\n" \
"ds_read_b64_tr_b16 a[118:119], v214 offset:27136\n" \
"v_add_u32 v225, s49, v212\n" \
"v_mfma_f32_16x16x32_bf16 v[16:19], a[100:103], a[76:79], v[16:19]\n" \
"s_waitcnt lgkmcnt(3)\n" \
"ds_read_b64_tr_b16 a[120:121], v215 offset:16896\n" \
"v_xor_b32 v226, 32, v225\n" \
"v_mfma_f32_16x16x32_bf16 v[32:35], a[96:99], a[80:83], v[32:35]\n" \
"v_add_u32 v227, s48, v211\n" \
"v_add_u32 v228, s48, v212\n" \
"v_mfma_f32_16x16x32_bf16 v[32:35], a[100:103], a[84:87], v[32:35]\n" \
"s_sub_u32 s85, s85, 32768\n" \
"s_cselect_b32 s85, 131072, s85\n" \
"ds_read_b64_tr_b16 a[122:123], v215 offset:18944\n" \
"v_mfma_f32_16x16x32_bf16 v[36:39], a[104:107], a[80:83], v[36:39]\n" \
"ds_read_b64_tr_b16 a[124:125], v215 offset:25088\n" \
"v_add_u32 v213, s85, v211\n" \
"v_mfma_f32_16x16x32_bf16 v[36:39], a[108:111], a[84:87], v[36:39]\n" \
"ds_read_b64_tr_b16 a[126:127], v215 offset:27136\n" \
"v_xor_b32 v229, 32, v228\n" \
"v_mfma_f32_16x16x32_bf16 v[52:55], a[104:107], a[88:91], v[52:55]\n" \
"v_mov_b32 v68, 0\n" \
"v_mov_b32 v69, 0\n" \
"v_mfma_f32_16x16x32_bf16 v[52:55], a[108:111], a[92:95], v[52:55]\n" \
"v_add_u32 v214, s85, v212\n" \
"v_xor_b32 v215, 32, v214\n" \
"v_mfma_f32_16x16x32_bf16 v[48:51], a[96:99], a[88:91], v[48:51]\n" \
"v_lshrrev_b32_e64 v72, 8, v234\n" \
"v_bfe_u32 v73, v234, 6, 2\n" \
"v_mfma_f32_16x16x32_bf16 v[48:51], a[100:103], a[92:95], v[48:51]\n" \
"s_waitcnt vmcnt(8) lgkmcnt(0)\n" \
"s_barrier\n" \
"s_waitcnt vmcnt(16)\n" \
"v_cmp_eq_u32 vcc, s60, v220\n" \
"s_nop 1\n" \
"s_andn2_b32 s70, s69, vcc_lo\n" \
"s_cbranch_scc1 Lpl7_%=\n" \
"Lpr7_%=:\n" \
"v_mfma_f32_16x16x32_bf16 v[8:11], a[112:115], a[64:67], v[8:11]\n" \
"s_add_u32 s71, s87, 0\n" \
"ds_read_b128 a[0:3], v213 offset:0\n" \
"ds_read_b128 a[4:7], v213 offset:2048\n" \
"v_mfma_f32_16x16x32_bf16 v[8:11], a[116:119], a[68:71], v[8:11]\n" \
"buffer_load_dwordx4 v[176:179], v234, s[56:59], s71 offen offset:0 sc1\n" \
"v_mfma_f32_16x16x32_bf16 v[12:15], a[120:123], a[64:67], v[12:15]\n" \
"ds_read_b64_tr_b16 a[128:129], v214 offset:16384\n" \
"ds_read_b64_tr_b16 a[130:131], v214 offset:18432\n" \
"v_mfma_f32_16x16x32_bf16 v[12:15], a[124:127], a[68:71], v[12:15]\n" \
"buffer_load_dwordx4 v[180:183], v234, s[56:59], s71 offen offset:1024 sc1\n" \
"v_mfma_f32_16x16x32_bf16 v[28:31], a[120:123], a[72:75], v[28:31]\n" \
"ds_read_b64_tr_b16 a[132:133], v214 offset:24576\n" \
"ds_read_b64_tr_b16 a[134:135], v214 offset:26624\n" \
"v_mfma_f32_16x16x32_bf16 v[28:31], a[124:127], a[76:79], v[28:31]\n" \
"buffer_load_dwordx4 v[184:187], v234, s[56:59], s71 offen offset:2048 sc1\n" \
"v_mfma_f32_16x16x32_bf16 v[24:27], a[112:115], a[72:75], v[24:27]\n" \
"ds_read_b64_tr_b16 a[136:137], v215 offset:16384\n" \
"ds_read_b64_tr_b16 a[138:139], v215 offset:18432\n" \
"v_mfma_f32_16x16x32_bf16 v[24:27], a[116:119], a[76:79], v[24:27]\n" \
"buffer_load_dwordx4 v[188:191], v234, s[56:59], s71 offen offset:3072 sc1\n" \
"v_mfma_f32_16x16x32_bf16 v[40:43], a[112:115], a[80:83], v[40:43]\n" \
"ds_read_b64_tr_b16 a[140:141], v215 offset:24576\n" \
"ds_read_b64_tr_b16 a[142:143], v215 offset:26624\n" \
"v_mfma_f32_16x16x32_bf16 v[40:43], a[116:119], a[84:87], v[40:43]\n" \
"ds_read_b64_tr_b16 a[144:145], v214 offset:16896\n" \
"ds_read_b64_tr_b16 a[146:147], v214 offset:18944\n" \
"v_mfma_f32_16x16x32_bf16 v[44:47], a[120:123], a[80:83], v[44:47]\n" \
"ds_read_b64_tr_b16 a[148:149], v214 offset:25088\n" \
"ds_read_b64_tr_b16 a[150:151], v214 offset:27136\n" \
"v_mfma_f32_16x16x32_bf16 v[44:47], a[124:127], a[84:87], v[44:47]\n" \
"ds_read_b64_tr_b16 a[152:153], v215 offset:16896\n" \
"ds_read_b64_tr_b16 a[154:155], v215 offset:18944\n" \
"v_mfma_f32_16x16x32_bf16 v[60:63], a[120:123], a[88:91], v[60:63]\n" \
"ds_read_b64_tr_b16 a[156:157], v215 offset:25088\n" \
"ds_read_b64_tr_b16 a[158:159], v215 offset:27136\n" \
"v_mfma_f32_16x16x32_bf16 v[60:63], a[124:127], a[92:95], v[60:63]\n" \
"ds_read_b128 a[24:27], v213 offset:256\n" \
"ds_read_b128 a[28:31], v213 offset:2304\n" \
"v_mfma_f32_16x16x32_bf16 v[56:59], a[112:115], a[88:91], v[56:59]\n" \
"ds_read_b128 a[8:11], v224 offset:0\n" \
"ds_read_b128 a[12:15], v224 offset:2048\n" \
"v_mfma_f32_16x16x32_bf16 v[56:59], a[116:119], a[92:95], v[56:59]\n" \
"s_waitcnt lgkmcnt(8)\n" \
"ds_read_b64_tr_b16 a[160:161], v225 offset:16384\n" \
"ds_read_b64_tr_b16 a[162:163], v225 offset:18432\n" \
"v_mfma_f32_16x16x32_bf16 v[0:3], a[128:131], a[0:3], v[0:3]\n" \
"ds_read_b64_tr_b16 a[164:165], v225 offset:24576\n" \
"ds_read_b64_tr_b16 a[166:167], v225 offset:26624\n" \
"v_mfma_f32_16x16x32_bf16 v[0:3], a[132:135], a[4:7], v[0:3]\n" \
"ds_read_b64_tr_b16 a[168:169], v226 offset:16384\n" \
"ds_read_b64_tr_b16 a[170:171], v226 offset:18432\n" \
"v_mfma_f32_16x16x32_bf16 v[4:7], a[136:139], a[0:3], v[4:7]\n" \
"s_mov_b32 s70, 0x20000000\n" \
"ds_read_b64_tr_b16 a[172:173], v226 offset:24576\n" \
"ds_read_b64_tr_b16 a[174:175], v226 offset:26624\n" \
"v_mfma_f32_16x16x32_bf16 v[4:7], a[140:143], a[4:7], v[4:7]\n" \
"s_cmp_eq_u32 s64, 65616896\n" \
"s_cselect_b32 s70, 65593344, s70\n" \
"ds_read_b64_tr_b16 a[176:177], v225 offset:16896\n" \
"ds_read_b64_tr_b16 a[178:179], v225 offset:18944\n" \
"buffer_store_dword v230, v231, s[56:59], s70 offen sc1\n" \
"v_mfma_f32_16x16x32_bf16 v[8:11], a[144:147], a[0:3], v[8:11]\n" \
"s_waitcnt lgkmcnt(8)\n" \
"ds_read_b64_tr_b16 a[180:181], v225 offset:25088\n" \
"ds_read_b64_tr_b16 a[182:183], v225 offset:27136\n" \
"v_mfma_f32_16x16x32_bf16 v[8:11], a[148:151], a[4:7], v[8:11]\n" \
"s_sub_u32 s68, s86, 32768\n" \
"s_cselect_b32 s68, 131072, s68\n" \
"ds_read_b64_tr_b16 a[184:185], v226 offset:16896\n" \
"ds_read_b64_tr_b16 a[186:187], v226 offset:18944\n" \
"v_mfma_f32_16x16x32_bf16 v[12:15], a[152:155], a[0:3], v[12:15]\n" \
"ds_read_b64_tr_b16 a[188:189], v226 offset:25088\n" \
"ds_read_b64_tr_b16 a[190:191], v226 offset:27136\n" \
"v_mfma_f32_16x16x32_bf16 v[12:15], a[156:159], a[4:7], v[12:15]\n" \
"s_cmp_lt_u32 s61, 2\n" \
"s_cselect_b32 s68, s86, s68\n" \
"v_cmp_eq_u32 s[16:17], v72, v73\n" \
"v_and_b32 v72, 16, v234\n" \
"v_mfma_f32_16x16x32_bf16 v[28:31], a[152:155], a[24:27], v[28:31]\n" \
"s_and_b32 s50, s61, 1\n" \
"v_lshlrev_b32 v72, v72, v74\n" \
"v_cndmask_b32 v72, 0, v72, s[16:17]\n" \
"v_mfma_f32_16x16x32_bf16 v[28:31], a[156:159], a[28:31], v[28:31]\n" \
"s_mul_i32 s50, s50, 9216\n" \
"v_bfe_i32 v73, v234, 5, 1\n" \
"v_and_b32 v67, v72, v73\n" \
"v_mfma_f32_16x16x32_bf16 v[24:27], a[144:147], a[24:27], v[24:27]\n" \
"s_add_u32 s68, s68, s50\n" \
"s_add_u32 s68, s68, %3\n" \
"v_xor_b32 v66, v72, v67\n" \
"v_mfma_f32_16x16x32_bf16 v[24:27], a[148:151], a[28:31], v[24:27]\n" \
"v_lshrrev_b32 v132, 4, v234\n" \
"v_and_b32 v133, 15, v132\n" \
"v_mul_u32_u24 v128, 144, v133\n" \
"v_mfma_f32_16x16x32_bf16 v[20:23], a[136:139], a[24:27], v[20:23]\n" \
"s_waitcnt vmcnt(5)\n" \
"s_barrier\n" \
"v_mfma_f32_16x16x32_bf16 v[20:23], a[140:143], a[28:31], v[20:23]\n" \
"s_add_u32 s71, s87, 4096\n" \
"ds_read_b128 a[32:35], v224 offset:256\n" \
"ds_read_b128 a[36:39], v224 offset:2304\n" \
"v_mfma_f32_16x16x32_bf16 v[16:19], a[128:131], a[24:27], v[16:19]\n" \
"buffer_load_dwordx4 v[192:195], v234, s[56:59], s71 offen offset:0 sc1\n" \
"v_mfma_f32_16x16x32_bf16 v[16:19], a[132:135], a[28:31], v[16:19]\n" \
"s_waitcnt lgkmcnt(2)\n" \
"ds_read_b128 a[16:19], v227 offset:0\n" \
"ds_read_b128 a[20:23], v227 offset:2048\n" \
"v_mfma_f32_16x16x32_bf16 v[0:3], a[160:163], a[8:11], v[0:3]\n" \
"ds_read_b64_tr_b16 a[192:193], v228 offset:16384\n" \
"ds_read_b64_tr_b16 a[194:195], v228 offset:18432\n" \
"v_mfma_f32_16x16x32_bf16 v[0:3], a[164:167], a[12:15], v[0:3]\n" \
"buffer_load_dwordx4 v[196:199], v234, s[56:59], s71 offen offset:1024 sc1\n" \
"v_mfma_f32_16x16x32_bf16 v[4:7], a[168:171], a[8:11], v[4:7]\n" \
"ds_read_b64_tr_b16 a[196:197], v228 offset:24576\n" \
"ds_read_b64_tr_b16 a[198:199], v228 offset:26624\n" \
"v_mfma_f32_16x16x32_bf16 v[4:7], a[172:175], a[12:15], v[4:7]\n" \
"ds_read_b64_tr_b16 a[200:201], v229 offset:16384\n" \
"ds_read_b64_tr_b16 a[202:203], v229 offset:18432\n" \
"v_mfma_f32_16x16x32_bf16 v[8:11], a[176:179], a[8:11], v[8:11]\n" \
"buffer_load_dwordx4 v[200:203], v234, s[56:59], s71 offen offset:2048 sc1\n" \
"v_mfma_f32_16x16x32_bf16 v[8:11], a[180:183], a[12:15], v[8:11]\n" \
"ds_read_b64_tr_b16 a[204:205], v229 offset:24576\n" \
"ds_read_b64_tr_b16 a[206:207], v229 offset:26624\n" \
"v_lshrrev_b32 v133, 4, v132\n" \
"v_mfma_f32_16x16x32_bf16 v[12:15], a[184:187], a[8:11], v[12:15]\n" \
"s_waitcnt lgkmcnt(6)\n" \
"ds_read_b64_tr_b16 a[208:209], v228 offset:16896\n" \
"ds_read_b64_tr_b16 a[210:211], v228 offset:18944\n" \
"v_mfma_f32_16x16x32_bf16 v[12:15], a[188:191], a[12:15], v[12:15]\n" \
"buffer_load_dwordx4 v[204:207], v234, s[56:59], s71 offen offset:3072 sc1\n" \
"v_mfma_f32_16x16x32_bf16 v[28:31], a[184:187], a[32:35], v[28:31]\n" \
"ds_read_b64_tr_b16 a[212:213], v228 offset:25088\n" \
"ds_read_b64_tr_b16 a[214:215], v228 offset:27136\n" \
"v_mfma_f32_16x16x32_bf16 v[28:31], a[188:191], a[36:39], v[28:31]\n" \
"ds_read_b64_tr_b16 a[216:217], v229 offset:16896\n" \
"ds_read_b64_tr_b16 a[218:219], v229 offset:18944\n" \
"v_mfma_f32_16x16x32_bf16 v[24:27], a[176:179], a[32:35], v[24:27]\n" \
"ds_read_b64_tr_b16 a[220:221], v229 offset:25088\n" \
"ds_read_b64_tr_b16 a[222:223], v229 offset:27136\n" \
"v_mfma_f32_16x16x32_bf16 v[24:27], a[180:183], a[36:39], v[24:27]\n" \
"ds_read_b128 a[40:43], v227 offset:256\n" \
"ds_read_b128 a[44:47], v227 offset:2304\n" \
"v_mfma_f32_16x16x32_bf16 v[20:23], a[168:171], a[32:35], v[20:23]\n" \
"ds_read_b128 a[248:251], v213 offset:4096\n" \
"ds_read_b128 a[252:255], v213 offset:6144\n" \
"v_mfma_f32_16x16x32_bf16 v[20:23], a[172:175], a[36:39], v[20:23]\n" \
"ds_read_b128 a[48:51], v224 offset:4096\n" \
"ds_read_b128 a[52:55], v224 offset:6144\n" \
"v_mfma_f32_16x16x32_bf16 v[16:19], a[160:163], a[32:35], v[16:19]\n" \
"ds_read_b128 a[56:59], v227 offset:4096\n" \
"ds_read_b128 a[60:63], v227 offset:6144\n" \
"v_mfma_f32_16x16x32_bf16 v[16:19], a[164:167], a[36:39], v[16:19]\n" \
"s_waitcnt lgkmcnt(8)\n" \
"ds_read_b128 a[224:227], v213 offset:4352\n" \
"ds_read_b128 a[228:231], v213 offset:6400\n" \
"v_lshl_add_u32 v128, v133, 3, v128\n" \
"v_mfma_f32_16x16x32_bf16 v[0:3], a[192:195], a[16:19], v[0:3]\n" \
"s_waitcnt vmcnt(7)\n" \
"ds_read_b128 a[232:235], v224 offset:4352\n" \
"ds_read_b128 a[236:239], v224 offset:6400\n" \
"v_mfma_f32_16x16x32_bf16 v[0:3], a[196:199], a[20:23], v[0:3]\n" \
"ds_read_b128 a[240:243], v227 offset:4352\n" \
"ds_read_b128 a[244:247], v227 offset:6400\n" \
"v_mfma_f32_16x16x32_bf16 v[0:3], v[66:69], v[176:179], v[0:3]\n" \
"v_add_u32 v128, s68, v128\n" \
"v_lshrrev_b32 v133, 3, v132\n" \
"v_mfma_f32_16x16x32_bf16 v[4:7], a[200:203], a[16:19], v[4:7]\n" \
"v_and_b32 v134, 7, v132\n" \
"v_mul_u32_u24 v129, 144, v133\n" \
"v_mfma_f32_16x16x32_bf16 v[4:7], a[204:207], a[20:23], v[4:7]\n" \
"v_lshl_add_u32 v129, v134, 4, v129\n" \
"v_add_u32 v129, s68, v129\n" \
"v_mfma_f32_16x16x32_bf16 v[4:7], v[64:67], v[176:179], v[4:7]\n" \
"v_lshlrev_b32 v134, 4, v134\n" \
"v_lshl_add_u32 v130, v133, 12, v134\n" \
"v_mfma_f32_16x16x32_bf16 v[8:11], a[208:211], a[16:19], v[8:11]\n" \
"v_mul_f32 v0, 4.0, v0\n" \
"v_mul_f32 v1, 4.0, v1\n" \
"v_mul_f32 v2, 4.0, v2\n" \
"v_mul_f32 v3, 4.0, v3\n" \
"v_cvt_pk_bf16_f32 v136, v0, v1\n" \
"v_cvt_pk_bf16_f32 v137, v2, v3\n" \
"v_mfma_f32_16x16x32_bf16 v[8:11], a[212:215], a[20:23], v[8:11]\n" \
"v_mfma_f32_16x16x32_bf16 v[8:11], v[66:69], v[180:183], v[8:11]\n" \
"ds_write_b64 v128, v[136:137] offset:0\n" \
"v_mfma_f32_16x16x32_bf16 v[12:15], a[216:219], a[16:19], v[12:15]\n" \
"v_mul_f32 v4, 4.0, v4\n" \
"v_mul_f32 v5, 4.0, v5\n" \
"v_mfma_f32_16x16x32_bf16 v[12:15], a[220:223], a[20:23], v[12:15]\n" \
"v_mul_f32 v6, 4.0, v6\n" \
"v_mul_f32 v7, 4.0, v7\n" \
"v_mfma_f32_16x16x32_bf16 v[12:15], v[64:67], v[180:183], v[12:15]\n" \
"s_waitcnt lgkmcnt(1)\n" \
"v_cvt_pk_bf16_f32 v138, v4, v5\n" \
"v_cvt_pk_bf16_f32 v139, v6, v7\n" \
"v_mfma_f32_16x16x32_bf16 v[16:19], a[192:195], a[40:43], v[16:19]\n" \
"s_waitcnt vmcnt(5)\n" \
"v_mfma_f32_16x16x32_bf16 v[16:19], a[196:199], a[44:47], v[16:19]\n" \
"v_mfma_f32_16x16x32_bf16 v[16:19], v[66:69], v[184:187], v[16:19]\n" \
"ds_write_b64 v128, v[138:139] offset:32\n" \
"v_mul_f32 v8, 4.0, v8\n" \
"v_mfma_f32_16x16x32_bf16 v[20:23], a[200:203], a[40:43], v[20:23]\n" \
"v_mul_f32 v9, 4.0, v9\n" \
"v_mul_f32 v10, 4.0, v10\n" \
"v_mfma_f32_16x16x32_bf16 v[20:23], a[204:207], a[44:47], v[20:23]\n" \
"v_mul_f32 v11, 4.0, v11\n" \
"v_cvt_pk_bf16_f32 v136, v8, v9\n" \
"v_mfma_f32_16x16x32_bf16 v[20:23], v[64:67], v[184:187], v[20:23]\n" \
"v_cvt_pk_bf16_f32 v137, v10, v11\n" \
"v_mfma_f32_16x16x32_bf16 v[24:27], a[208:211], a[40:43], v[24:27]\n" \
"v_mfma_f32_16x16x32_bf16 v[24:27], a[212:215], a[44:47], v[24:27]\n" \
"v_mfma_f32_16x16x32_bf16 v[24:27], v[66:69], v[188:191], v[24:27]\n" \
"ds_write_b64 v128, v[136:137] offset:64\n" \
"v_mul_f32 v12, 4.0, v12\n" \
"v_mfma_f32_16x16x32_bf16 v[28:31], a[216:219], a[40:43], v[28:31]\n" \
"v_mul_f32 v13, 4.0, v13\n" \
"v_mul_f32 v14, 4.0, v14\n" \
"v_mfma_f32_16x16x32_bf16 v[28:31], a[220:223], a[44:47], v[28:31]\n" \
"v_mul_f32 v15, 4.0, v15\n" \
"v_cvt_pk_bf16_f32 v138, v12, v13\n" \
"v_mfma_f32_16x16x32_bf16 v[28:31], v[64:67], v[188:191], v[28:31]\n" \
"v_cvt_pk_bf16_f32 v139, v14, v15\n" \
"v_mfma_f32_16x16x32_bf16 v[32:35], a[128:131], a[248:251], v[32:35]\n" \
"ds_write_b64 v128, v[138:139] offset:96\n" \
"v_mfma_f32_16x16x32_bf16 v[32:35], a[132:135], a[252:255], v[32:35]\n" \
"ds_read_b128 v[140:143], v129 offset:0\n" \
"ds_read_b128 v[144:147], v129 offset:1152\n" \
"v_mfma_f32_16x16x32_bf16 v[32:35], a[160:163], a[48:51], v[32:35]\n" \
"s_waitcnt vmcnt(2)\n" \
"v_mul_f32 v16, 4.0, v16\n" \
"v_mul_f32 v17, 4.0, v17\n" \
"v_mfma_f32_16x16x32_bf16 v[32:35], a[164:167], a[52:55], v[32:35]\n" \
"v_mul_f32 v18, 4.0, v18\n" \
"v_mul_f32 v19, 4.0, v19\n" \
"v_mfma_f32_16x16x32_bf16 v[32:35], v[66:69], v[192:195], v[32:35]\n" \
"s_add_u32 s70, s88, 0\n" \
"s_waitcnt lgkmcnt(1)\n" \
"v_cvt_pk_bf16_f32 v136, v16, v17\n" \
"v_mfma_f32_16x16x32_bf16 v[32:35], a[192:195], a[56:59], v[32:35]\n" \
"buffer_store_dwordx4 v[140:143], v130, s[52:55], s70 offen sc1\n" \
"v_cvt_pk_bf16_f32 v137, v18, v19\n" \
"v_mfma_f32_16x16x32_bf16 v[32:35], a[196:199], a[60:63], v[32:35]\n" \
"ds_write_b64 v128, v[136:137] offset:2304\n" \
"v_mfma_f32_16x16x32_bf16 v[36:39], a[204:207], a[60:63], v[36:39]\n" \
"s_add_u32 s70, s88, 32768\n" \
"s_waitcnt lgkmcnt(1)\n" \
"v_mul_f32 v20, 4.0, v20\n" \
"v_mfma_f32_16x16x32_bf16 v[36:39], a[200:203], a[56:59], v[36:39]\n" \
"buffer_store_dwordx4 v[144:147], v130, s[52:55], s70 offen sc1\n" \
"v_mfma_f32_16x16x32_bf16 v[36:39], a[172:175], a[52:55], v[36:39]\n" \
"v_mul_f32 v21, 4.0, v21\n" \
"v_mul_f32 v22, 4.0, v22\n" \
"v_mfma_f32_16x16x32_bf16 v[36:39], a[168:171], a[48:51], v[36:39]\n" \
"v_mul_f32 v23, 4.0, v23\n" \
"v_cvt_pk_bf16_f32 v138, v20, v21\n" \
"v_mfma_f32_16x16x32_bf16 v[36:39], v[64:67], v[192:195], v[36:39]\n" \
"v_cvt_pk_bf16_f32 v139, v22, v23\n" \
"v_mfma_f32_16x16x32_bf16 v[36:39], a[140:143], a[252:255], v[36:39]\n" \
"v_mfma_f32_16x16x32_bf16 v[36:39], a[136:139], a[248:251], v[36:39]\n" \
"ds_write_b64 v128, v[138:139] offset:2336\n" \
"v_mul_f32 v24, 4.0, v24\n" \
"v_mul_f32 v25, 4.0, v25\n" \
"v_mfma_f32_16x16x32_bf16 v[40:43], a[144:147], a[248:251], v[40:43]\n" \
"v_mul_f32 v26, 4.0, v26\n" \
"v_mul_f32 v27, 4.0, v27\n" \
"v_mfma_f32_16x16x32_bf16 v[40:43], a[148:151], a[252:255], v[40:43]\n" \
"v_cvt_pk_bf16_f32 v136, v24, v25\n" \
"v_cvt_pk_bf16_f32 v137, v26, v27\n" \
"v_mfma_f32_16x16x32_bf16 v[40:43], a[176:179], a[48:51], v[40:43]\n" \
"ds_write_b64 v128, v[136:137] offset:2368\n" \
"v_mfma_f32_16x16x32_bf16 v[40:43], a[180:183], a[52:55], v[40:43]\n" \
"v_mul_f32 v28, 4.0, v28\n" \
"v_mul_f32 v29, 4.0, v29\n" \
"v_mfma_f32_16x16x32_bf16 v[40:43], v[66:69], v[196:199], v[40:43]\n" \
"v_mul_f32 v30, 4.0, v30\n" \
"v_mul_f32 v31, 4.0, v31\n" \
"v_mfma_f32_16x16x32_bf16 v[40:43], a[208:211], a[56:59], v[40:43]\n" \
"v_cvt_pk_bf16_f32 v138, v28, v29\n" \
"v_cvt_pk_bf16_f32 v139, v30, v31\n" \
"v_mfma_f32_16x16x32_bf16 v[40:43], a[212:215], a[60:63], v[40:43]\n" \
"Lpg10801_%=:\n" \
"s_cbranch_execz Lcb1_%=\n" \
"ds_write_b64 v128, v[138:139] offset:2400\n" \
"v_mfma_f32_16x16x32_bf16 v[44:47], a[220:223], a[60:63], v[44:47]\n" \
"ds_read_b128 v[148:151], v129 offset:2304\n" \
"ds_read_b128 v[152:155], v129 offset:3456\n" \
"v_mfma_f32_16x16x32_bf16 v[44:47], a[216:219], a[56:59], v[44:47]\n" \
"v_mul_f32 v32, 4.0, v32\n" \
"v_mul_f32 v33, 4.0, v33\n" \
"v_mfma_f32_16x16x32_bf16 v[44:47], a[188:191], a[52:55], v[44:47]\n" \
"v_mul_f32 v34, 4.0, v34\n" \
"v_mul_f32 v35, 4.0, v35\n" \
"v_mfma_f32_16x16x32_bf16 v[44:47], a[184:187], a[48:51], v[44:47]\n" \
"s_add_u32 s70, s88, 65536\n" \
"s_waitcnt lgkmcnt(1)\n" \
"v_cvt_pk_bf16_f32 v136, v32, v33\n" \
"v_mfma_f32_16x16x32_bf16 v[44:47], v[64:67], v[196:199], v[44:47]\n" \
"buffer_store_dwordx4 v[148:151], v130, s[52:55], s70 offen sc1\n" \
"v_cvt_pk_bf16_f32 v137, v34, v35\n" \
"v_mfma_f32_16x16x32_bf16 v[44:47], a[156:159], a[252:255], v[44:47]\n" \
"v_mfma_f32_16x16x32_bf16 v[44:47], a[152:155], a[248:251], v[44:47]\n" \
"s_add_u32 s70, s88, 98304\n" \
"s_waitcnt lgkmcnt(0)\n" \
"ds_write_b64 v128, v[136:137] offset:4608\n" \
"v_mul_f32 v36, 4.0, v36\n" \
"v_mfma_f32_16x16x32_bf16 v[48:51], a[128:131], a[224:227], v[48:51]\n" \
"buffer_store_dwordx4 v[152:155], v130, s[52:55], s70 offen sc1\n" \
"v_mul_f32 v37, 4.0, v37\n" \
"v_mfma_f32_16x16x32_bf16 v[48:51], a[132:135], a[228:231], v[48:51]\n" \
"v_mul_f32 v38, 4.0, v38\n" \
"v_mul_f32 v39, 4.0, v39\n" \
"v_mfma_f32_16x16x32_bf16 v[48:51], a[160:163], a[232:235], v[48:51]\n" \
"s_waitcnt vmcnt(4)\n" \
"v_cvt_pk_bf16_f32 v138, v36, v37\n" \
"v_cvt_pk_bf16_f32 v139, v38, v39\n" \
"v_mfma_f32_16x16x32_bf16 v[48:51], a[164:167], a[236:239], v[48:51]\n" \
"ds_write_b64 v128, v[138:139] offset:4640\n" \
"v_mul_f32 v40, 4.0, v40\n" \
"v_mfma_f32_16x16x32_bf16 v[48:51], v[66:69], v[200:203], v[48:51]\n" \
"v_mul_f32 v41, 4.0, v41\n" \
"v_mul_f32 v42, 4.0, v42\n" \
"v_mfma_f32_16x16x32_bf16 v[48:51], a[192:195], a[240:243], v[48:51]\n" \
"v_mul_f32 v43, 4.0, v43\n" \
"v_cvt_pk_bf16_f32 v136, v40, v41\n" \
"v_mfma_f32_16x16x32_bf16 v[48:51], a[196:199], a[244:247], v[48:51]\n" \
"v_cvt_pk_bf16_f32 v137, v42, v43\n" \
"v_mfma_f32_16x16x32_bf16 v[52:55], a[204:207], a[244:247], v[52:55]\n" \
"ds_write_b64 v128, v[136:137] offset:4672\n" \
"v_mul_f32 v44, 4.0, v44\n" \
"v_mul_f32 v45, 4.0, v45\n" \
"v_mfma_f32_16x16x32_bf16 v[52:55], a[200:203], a[240:243], v[52:55]\n" \
"v_mul_f32 v46, 4.0, v46\n" \
"v_mul_f32 v47, 4.0, v47\n" \
"v_mfma_f32_16x16x32_bf16 v[52:55], a[172:175], a[236:239], v[52:55]\n" \
"v_cvt_pk_bf16_f32 v138, v44, v45\n" \
"v_cvt_pk_bf16_f32 v139, v46, v47\n" \
"v_mfma_f32_16x16x32_bf16 v[52:55], a[168:171], a[232:235], v[52:55]\n" \
"ds_write_b64 v128, v[138:139] offset:4704\n" \
"v_mfma_f32_16x16x32_bf16 v[52:55], v[64:67], v[200:203], v[52:55]\n" \
"ds_read_b128 v[156:159], v129 offset:4608\n" \
"ds_read_b128 v[160:163], v129 offset:5760\n" \
"v_mfma_f32_16x16x32_bf16 v[52:55], a[140:143], a[228:231], v[52:55]\n" \
"v_mul_f32 v48, 4.0, v48\n" \
"v_mul_f32 v49, 4.0, v49\n" \
"v_mfma_f32_16x16x32_bf16 v[52:55], a[136:139], a[224:227], v[52:55]\n" \
"v_mul_f32 v50, 4.0, v50\n" \
"v_mul_f32 v51, 4.0, v51\n" \
"v_mfma_f32_16x16x32_bf16 v[56:59], a[144:147], a[224:227], v[56:59]\n" \
"s_add_u32 s70, s88, 131072\n" \
"s_waitcnt lgkmcnt(1)\n" \
"v_cvt_pk_bf16_f32 v136, v48, v49\n" \
"v_mfma_f32_16x16x32_bf16 v[56:59], a[148:151], a[228:231], v[56:59]\n" \
"buffer_store_dwordx4 v[156:159], v130, s[52:55], s70 offen sc1\n" \
"v_cvt_pk_bf16_f32 v137, v50, v51\n" \
"v_mfma_f32_16x16x32_bf16 v[56:59], a[176:179], a[232:235], v[56:59]\n" \
"ds_write_b64 v128, v[136:137] offset:6912\n" \
"v_mfma_f32_16x16x32_bf16 v[56:59], a[180:183], a[236:239], v[56:59]\n" \
"s_add_u32 s70, s88, 163840\n" \
"s_waitcnt lgkmcnt(1)\n" \
"v_mul_f32 v52, 4.0, v52\n" \
"v_mfma_f32_16x16x32_bf16 v[56:59], v[66:69], v[204:207], v[56:59]\n" \
"buffer_store_dwordx4 v[160:163], v130, s[52:55], s70 offen sc1\n" \
"v_mfma_f32_16x16x32_bf16 v[56:59], a[208:211], a[240:243], v[56:59]\n" \
"v_mul_f32 v53, 4.0, v53\n" \
"v_mul_f32 v54, 4.0, v54\n" \
"v_mfma_f32_16x16x32_bf16 v[56:59], a[212:215], a[244:247], v[56:59]\n" \
"v_mul_f32 v55, 4.0, v55\n" \
"v_cvt_pk_bf16_f32 v138, v52, v53\n" \
"v_cvt_pk_bf16_f32 v139, v54, v55\n" \
"v_mfma_f32_16x16x32_bf16 v[60:63], a[220:223], a[244:247], v[60:63]\n" \
"ds_write_b64 v128, v[138:139] offset:6944\n" \
"v_mfma_f32_16x16x32_bf16 v[60:63], a[216:219], a[240:243], v[60:63]\n" \
"v_mfma_f32_16x16x32_bf16 v[60:63], a[188:191], a[236:239], v[60:63]\n" \
"v_mfma_f32_16x16x32_bf16 v[60:63], a[184:187], a[232:235], v[60:63]\n" \
"v_mul_f32 v56, 4.0, v56\n" \
"v_mul_f32 v57, 4.0, v57\n" \
"v_mfma_f32_16x16x32_bf16 v[60:63], v[64:67], v[204:207], v[60:63]\n" \
"v_mul_f32 v58, 4.0, v58\n" \
"v_mul_f32 v59, 4.0, v59\n" \
"v_mfma_f32_16x16x32_bf16 v[60:63], a[156:159], a[228:231], v[60:63]\n" \
"v_cvt_pk_bf16_f32 v136, v56, v57\n" \
"v_cvt_pk_bf16_f32 v137, v58, v59\n" \
"v_mfma_f32_16x16x32_bf16 v[60:63], a[152:155], a[224:227], v[60:63]\n" \
"ds_write_b64 v128, v[136:137] offset:6976\n" \
"s_nop 7\n" \
"v_mul_f32 v60, 4.0, v60\n" \
"v_mul_f32 v61, 4.0, v61\n" \
"v_mul_f32 v62, 4.0, v62\n" \
"v_mul_f32 v63, 4.0, v63\n" \
"v_cvt_pk_bf16_f32 v138, v60, v61\n" \
"v_cvt_pk_bf16_f32 v139, v62, v63\n" \
"ds_write_b64 v128, v[138:139] offset:7008\n" \
"ds_read_b128 v[164:167], v129 offset:6912\n" \
"ds_read_b128 v[168:171], v129 offset:8064\n" \
"s_add_u32 s70, s88, 196608\n" \
"s_waitcnt lgkmcnt(1)\n" \
"buffer_store_dwordx4 v[164:167], v130, s[52:55], s70 offen sc1\n" \
"s_add_u32 s70, s88, 229376\n" \
"s_waitcnt lgkmcnt(0)\n" \
"buffer_store_dwordx4 v[168:171], v130, s[52:55], s70 offen sc1\n" \
"s_endpgm\n" \
".p2align 12\n" \
"Lrole1_%=:\n" \
"v_mfma_f32_16x16x32_bf16 v[64:67], a[96:99], a[64:67], 0\n" \
"Lpg10924_%=:\n" \
"s_cbranch_execz Lcb1_%=\n" \
"s_waitcnt lgkmcnt(1)\n" \
"s_xor_b32 s82, s48, s49\n" \
"ds_read_b128 a[88:91], v213 offset:4352\n" \
"v_mfma_f32_16x16x32_bf16 v[64:67], a[100:103], a[68:71], v[64:67]\n" \
"s_cmp_lt_u32 s48, s50\n" \
"s_cselect_b32 s82, s82, s48\n" \
"ds_read_b128 a[92:95], v213 offset:6400\n" \
"v_mfma_f32_16x16x32_bf16 v[68:71], a[104:107], a[64:67], 0\n" \
"s_add_u32 s81, s82, s46\n" \
"ds_read_b64_tr_b16 a[112:113], v214 offset:16896\n" \
"v_mfma_f32_16x16x32_bf16 v[68:71], a[108:111], a[68:71], v[68:71]\n" \
"s_add_u32 m0, s86, s79\n" \
"ds_read_b64_tr_b16 a[114:115], v214 offset:18944\n" \
"v_mfma_f32_16x16x32_bf16 v[84:87], a[104:107], a[72:75], 0\n" \
"buffer_load_dwordx4 v234, s[56:59], s81 offen offset:0 sc1 lds\n" \
"v_mfma_f32_16x16x32_bf16 v[84:87], a[108:111], a[76:79], v[84:87]\n" \
"s_waitcnt lgkmcnt(3)\n" \
"ds_read_b64_tr_b16 a[116:117], v214 offset:25088\n" \
"v_mfma_f32_16x16x32_bf16 v[80:83], a[96:99], a[72:75], 0\n" \
"s_lshl_b32 s82, s82, 5\n" \
"ds_read_b64_tr_b16 a[118:119], v214 offset:27136\n" \
"v_cvt_pk_bf16_f32 v0, v0, v1\n" \
"v_mfma_f32_16x16x32_bf16 v[80:83], a[100:103], a[76:79], v[80:83]\n" \
"s_add_u32 s82, s82, s47\n" \
"ds_read_b64_tr_b16 a[120:121], v215 offset:16896\n" \
"v_cvt_pk_bf16_f32 v1, v2, v3\n" \
"v_mfma_f32_16x16x32_bf16 v[96:99], a[96:99], a[80:83], 0\n" \
"buffer_load_dwordx4 v234, s[56:59], s81 offen offset:1024 sc1 lds\n" \
"v_mfma_f32_16x16x32_bf16 v[96:99], a[100:103], a[84:87], v[96:99]\n" \
"s_sub_u32 s85, s85, 32768\n" \
"s_cselect_b32 s85, 131072, s85\n" \
"ds_read_b64_tr_b16 a[122:123], v215 offset:18944\n" \
"v_cvt_pk_bf16_f32 v2, v4, v5\n" \
"v_mfma_f32_16x16x32_bf16 v[100:103], a[104:107], a[80:83], 0\n" \
"s_add_u32 s48, s48, 0x1000\n" \
"ds_read_b64_tr_b16 a[124:125], v215 offset:25088\n" \
"v_add_u32 v213, s85, v211\n" \
"v_mfma_f32_16x16x32_bf16 v[100:103], a[108:111], a[84:87], v[100:103]\n" \
"s_waitcnt lgkmcnt(3)\n" \
"s_sub_u32 s86, s86, 32768\n" \
"s_cselect_b32 s86, 131072, s86\n" \
"ds_read_b64_tr_b16 a[126:127], v215 offset:27136\n" \
"v_mfma_f32_16x16x32_bf16 v[116:119], a[104:107], a[88:91], 0\n" \
"buffer_load_dwordx4 v234, s[56:59], s81 offen offset:2048 sc1 lds\n" \
"v_mfma_f32_16x16x32_bf16 v[116:119], a[108:111], a[92:95], v[116:119]\n" \
"v_add_u32 v214, s85, v212\n" \
"v_xor_b32 v215, 32, v214\n" \
"v_cvt_pk_bf16_f32 v3, v6, v7\n" \
"v_mfma_f32_16x16x32_bf16 v[112:115], a[96:99], a[88:91], 0\n" \
"buffer_load_dwordx4 v234, s[56:59], s81 offen offset:3072 sc1 lds\n" \
"v_mfma_f32_16x16x32_bf16 v[112:115], a[100:103], a[92:95], v[112:115]\n" \
"s_waitcnt vmcnt(20) lgkmcnt(0)\n" \
"s_barrier\n" \
"v_mfma_f32_16x16x32_bf16 v[72:75], a[112:115], a[64:67], 0\n" \
"ds_read_b64_tr_b16 a[96:97], v214 offset:16384\n" \
"ds_read_b64_tr_b16 a[98:99], v214 offset:18432\n" \
"v_mfma_f32_16x16x32_bf16 v[72:75], a[116:119], a[68:71], v[72:75]\n" \
"s_add_u32 m0, m0, 0x4000\n" \
"ds_read_b64_tr_b16 a[100:101], v214 offset:24576\n" \
"ds_read_b64_tr_b16 a[102:103], v214 offset:26624\n" \
"v_cvt_pk_bf16_f32 v4, v8, v9\n" \
"v_mfma_f32_16x16x32_bf16 v[76:79], a[120:123], a[64:67], 0\n" \
"buffer_load_dwordx4 v216, s[36:39], s82 offen offset:0 lds\n" \
"v_mfma_f32_16x16x32_bf16 v[76:79], a[124:127], a[68:71], v[76:79]\n" \
"ds_read_b128 a[64:67], v213 offset:0\n" \
"v_cvt_pk_bf16_f32 v5, v10, v11\n" \
"v_mfma_f32_16x16x32_bf16 v[92:95], a[120:123], a[72:75], 0\n" \
"ds_read_b128 a[68:71], v213 offset:2048\n" \
"v_cvt_pk_bf16_f32 v6, v12, v13\n" \
"v_mfma_f32_16x16x32_bf16 v[92:95], a[124:127], a[76:79], v[92:95]\n" \
"ds_read_b64_tr_b16 a[104:105], v215 offset:16384\n" \
"ds_read_b64_tr_b16 a[106:107], v215 offset:18432\n" \
"v_cvt_pk_bf16_f32 v7, v14, v15\n" \
"v_mfma_f32_16x16x32_bf16 v[88:91], a[112:115], a[72:75], 0\n" \
"buffer_load_dwordx4 v217, s[36:39], s82 offen offset:1024 lds\n" \
"v_mfma_f32_16x16x32_bf16 v[88:91], a[116:119], a[76:79], v[88:91]\n" \
"ds_read_b128 a[72:75], v213 offset:256\n" \
"v_cvt_pk_bf16_f32 v8, v16, v17\n" \
"v_mfma_f32_16x16x32_bf16 v[104:107], a[112:115], a[80:83], 0\n" \
"ds_read_b128 a[76:79], v213 offset:2304\n" \
"v_cvt_pk_bf16_f32 v9, v18, v19\n" \
"v_mfma_f32_16x16x32_bf16 v[104:107], a[116:119], a[84:87], v[104:107]\n" \
"ds_read_b64_tr_b16 a[108:109], v215 offset:24576\n" \
"ds_read_b64_tr_b16 a[110:111], v215 offset:26624\n" \
"v_cvt_pk_bf16_f32 v10, v20, v21\n" \
"v_mfma_f32_16x16x32_bf16 v[108:111], a[120:123], a[80:83], 0\n" \
"buffer_load_dwordx4 v218, s[36:39], s82 offen offset:2048 lds\n" \
"v_mfma_f32_16x16x32_bf16 v[108:111], a[124:127], a[84:87], v[108:111]\n" \
"s_add_u32 s71, s87, 0\n" \
"ds_read_b128 a[80:83], v213 offset:4096\n" \
"v_cvt_pk_bf16_f32 v11, v22, v23\n" \
"v_mfma_f32_16x16x32_bf16 v[124:127], a[120:123], a[88:91], 0\n" \
"s_waitcnt lgkmcnt(4)\n" \
"buffer_store_dwordx4 v[0:3], v234, s[56:59], s71 offen offset:0 sc1\n" \
"v_mfma_f32_16x16x32_bf16 v[124:127], a[124:127], a[92:95], v[124:127]\n" \
"ds_read_b128 a[84:87], v213 offset:6144\n" \
"v_cvt_pk_bf16_f32 v12, v24, v25\n" \
"v_mfma_f32_16x16x32_bf16 v[120:123], a[112:115], a[88:91], 0\n" \
"buffer_load_dwordx4 v219, s[36:39], s82 offen offset:3072 lds\n" \
"v_mfma_f32_16x16x32_bf16 v[120:123], a[116:119], a[92:95], v[120:123]\n" \
"buffer_store_dwordx4 v[4:7], v234, s[56:59], s71 offen offset:1024 sc1\n" \
"buffer_store_dwordx4 v[8:11], v234, s[56:59], s71 offen offset:2048 sc1\n" \
"v_mfma_f32_16x16x32_bf16 v[64:67], a[96:99], a[64:67], v[64:67]\n" \
"s_xor_b32 s82, s48, s49\n" \
"ds_read_b128 a[88:91], v213 offset:4352\n" \
"v_cvt_pk_bf16_f32 v13, v26, v27\n" \
"v_mfma_f32_16x16x32_bf16 v[64:67], a[100:103], a[68:71], v[64:67]\n" \
"s_waitcnt lgkmcnt(2)\n" \
"s_cmp_lt_u32 s48, s50\n" \
"s_cselect_b32 s82, s82, s48\n" \
"ds_read_b128 a[92:95], v213 offset:6400\n" \
"v_mfma_f32_16x16x32_bf16 v[68:71], a[104:107], a[64:67], v[68:71]\n" \
"s_add_u32 s81, s82, s46\n" \
"ds_read_b64_tr_b16 a[112:113], v214 offset:16896\n" \
"v_cvt_pk_bf16_f32 v14, v28, v29\n" \
"v_mfma_f32_16x16x32_bf16 v[68:71], a[108:111], a[68:71], v[68:71]\n" \
"s_add_u32 m0, s86, s79\n" \
"ds_read_b64_tr_b16 a[114:115], v214 offset:18944\n" \
"v_cvt_pk_bf16_f32 v15, v30, v31\n" \
"v_mfma_f32_16x16x32_bf16 v[84:87], a[104:107], a[72:75], v[84:87]\n" \
"buffer_load_dwordx4 v234, s[56:59], s81 offen offset:0 sc1 lds\n" \
"v_mfma_f32_16x16x32_bf16 v[84:87], a[108:111], a[76:79], v[84:87]\n" \
"s_waitcnt lgkmcnt(3)\n" \
"ds_read_b64_tr_b16 a[116:117], v214 offset:25088\n" \
"v_cvt_pk_bf16_f32 v16, v32, v33\n" \
"v_mfma_f32_16x16x32_bf16 v[80:83], a[96:99], a[72:75], v[80:83]\n" \
"s_lshl_b32 s82, s82, 5\n" \
"ds_read_b64_tr_b16 a[118:119], v214 offset:27136\n" \
"v_cvt_pk_bf16_f32 v17, v34, v35\n" \
"v_mfma_f32_16x16x32_bf16 v[80:83], a[100:103], a[76:79], v[80:83]\n" \
"s_add_u32 s82, s82, s47\n" \
"ds_read_b64_tr_b16 a[120:121], v215 offset:16896\n" \
"v_cvt_pk_bf16_f32 v18, v36, v37\n" \
"v_mfma_f32_16x16x32_bf16 v[96:99], a[96:99], a[80:83], v[96:99]\n" \
"buffer_load_dwordx4 v234, s[56:59], s81 offen offset:1024 sc1 lds\n" \
"v_mfma_f32_16x16x32_bf16 v[96:99], a[100:103], a[84:87], v[96:99]\n" \
"s_sub_u32 s85, s85, 32768\n" \
"s_cselect_b32 s85, 131072, s85\n" \
"ds_read_b64_tr_b16 a[122:123], v215 offset:18944\n" \
"v_mfma_f32_16x16x32_bf16 v[100:103], a[104:107], a[80:83], v[100:103]\n" \
"s_add_u32 s48, s48, 0x1000\n" \
"ds_read_b64_tr_b16 a[124:125], v215 offset:25088\n" \
"v_add_u32 v213, s85, v211\n" \
"v_mfma_f32_16x16x32_bf16 v[100:103], a[108:111], a[84:87], v[100:103]\n" \
"s_waitcnt lgkmcnt(3)\n" \
"s_sub_u32 s86, s86, 32768\n" \
"s_cselect_b32 s86, 131072, s86\n" \
"ds_read_b64_tr_b16 a[126:127], v215 offset:27136\n" \
"v_mfma_f32_16x16x32_bf16 v[116:119], a[104:107], a[88:91], v[116:119]\n" \
"buffer_load_dwordx4 v234, s[56:59], s81 offen offset:2048 sc1 lds\n" \
"v_mfma_f32_16x16x32_bf16 v[116:119], a[108:111], a[92:95], v[116:119]\n" \
"v_add_u32 v214, s85, v212\n" \
"v_xor_b32 v215, 32, v214\n" \
"v_mfma_f32_16x16x32_bf16 v[112:115], a[96:99], a[88:91], v[112:115]\n" \
"buffer_load_dwordx4 v234, s[56:59], s81 offen offset:3072 sc1 lds\n" \
"v_mfma_f32_16x16x32_bf16 v[112:115], a[100:103], a[92:95], v[112:115]\n" \
"s_waitcnt vmcnt(23) lgkmcnt(0)\n" \
"s_barrier\n" \
"v_mfma_f32_16x16x32_bf16 v[72:75], a[112:115], a[64:67], v[72:75]\n" \
"ds_read_b64_tr_b16 a[96:97], v214 offset:16384\n" \
"ds_read_b64_tr_b16 a[98:99], v214 offset:18432\n" \
"v_mfma_f32_16x16x32_bf16 v[72:75], a[116:119], a[68:71], v[72:75]\n" \
"s_add_u32 m0, m0, 0x4000\n" \
"ds_read_b64_tr_b16 a[100:101], v214 offset:24576\n" \
"ds_read_b64_tr_b16 a[102:103], v214 offset:26624\n" \
"v_mfma_f32_16x16x32_bf16 v[76:79], a[120:123], a[64:67], v[76:79]\n" \
"buffer_load_dwordx4 v216, s[36:39], s82 offen offset:0 lds\n" \
"v_mfma_f32_16x16x32_bf16 v[76:79], a[124:127], a[68:71], v[76:79]\n" \
"ds_read_b128 a[64:67], v213 offset:0\n" \
"v_cvt_pk_bf16_f32 v19, v38, v39\n" \
"v_mfma_f32_16x16x32_bf16 v[92:95], a[120:123], a[72:75], v[92:95]\n" \
"ds_read_b128 a[68:71], v213 offset:2048\n" \
"v_cvt_pk_bf16_f32 v20, v40, v41\n" \
"v_mfma_f32_16x16x32_bf16 v[92:95], a[124:127], a[76:79], v[92:95]\n" \
"ds_read_b64_tr_b16 a[104:105], v215 offset:16384\n" \
"ds_read_b64_tr_b16 a[106:107], v215 offset:18432\n" \
"v_mfma_f32_16x16x32_bf16 v[88:91], a[112:115], a[72:75], v[88:91]\n" \
"buffer_load_dwordx4 v217, s[36:39], s82 offen offset:1024 lds\n" \
"v_mfma_f32_16x16x32_bf16 v[88:91], a[116:119], a[76:79], v[88:91]\n" \
"ds_read_b128 a[72:75], v213 offset:256\n" \
"v_cvt_pk_bf16_f32 v21, v42, v43\n" \
"v_mfma_f32_16x16x32_bf16 v[104:107], a[112:115], a[80:83], v[104:107]\n" \
"ds_read_b128 a[76:79], v213 offset:2304\n" \
"v_cvt_pk_bf16_f32 v22, v44, v45\n" \
"v_mfma_f32_16x16x32_bf16 v[104:107], a[116:119], a[84:87], v[104:107]\n" \
"ds_read_b64_tr_b16 a[108:109], v215 offset:24576\n" \
"ds_read_b64_tr_b16 a[110:111], v215 offset:26624\n" \
"v_mfma_f32_16x16x32_bf16 v[108:111], a[120:123], a[80:83], v[108:111]\n" \
"buffer_load_dwordx4 v218, s[36:39], s82 offen offset:2048 lds\n" \
"v_mfma_f32_16x16x32_bf16 v[108:111], a[124:127], a[84:87], v[108:111]\n" \
"ds_read_b128 a[80:83], v213 offset:4096\n" \
"v_cvt_pk_bf16_f32 v23, v46, v47\n" \
"v_mfma_f32_16x16x32_bf16 v[124:127], a[120:123], a[88:91], v[124:127]\n" \
"s_waitcnt lgkmcnt(4)\n" \
"buffer_store_dwordx4 v[12:15], v234, s[56:59], s71 offen offset:3072 sc1\n" \
"v_mfma_f32_16x16x32_bf16 v[124:127], a[124:127], a[92:95], v[124:127]\n" \
"s_add_u32 s71, s87, 4096\n" \
"ds_read_b128 a[84:87], v213 offset:6144\n" \
"v_cvt_pk_bf16_f32 v24, v48, v49\n" \
"v_mfma_f32_16x16x32_bf16 v[120:123], a[112:115], a[88:91], v[120:123]\n" \
"buffer_load_dwordx4 v219, s[36:39], s82 offen offset:3072 lds\n" \
"v_mfma_f32_16x16x32_bf16 v[120:123], a[116:119], a[92:95], v[120:123]\n" \
"buffer_store_dwordx4 v[16:19], v234, s[56:59], s71 offen offset:0 sc1\n" \
"buffer_store_dwordx4 v[20:23], v234, s[56:59], s71 offen offset:1024 sc1\n" \
"v_mfma_f32_16x16x32_bf16 v[64:67], a[96:99], a[64:67], v[64:67]\n" \
"s_xor_b32 s82, s48, s49\n" \
"ds_read_b128 a[88:91], v213 offset:4352\n" \
"v_cvt_pk_bf16_f32 v25, v50, v51\n" \
"v_mfma_f32_16x16x32_bf16 v[64:67], a[100:103], a[68:71], v[64:67]\n" \
"s_waitcnt lgkmcnt(2)\n" \
"s_cmp_lt_u32 s48, s50\n" \
"s_cselect_b32 s82, s82, s48\n" \
"ds_read_b128 a[92:95], v213 offset:6400\n" \
"v_mfma_f32_16x16x32_bf16 v[68:71], a[104:107], a[64:67], v[68:71]\n" \
"s_add_u32 s81, s82, s46\n" \
"ds_read_b64_tr_b16 a[112:113], v214 offset:16896\n" \
"v_cvt_pk_bf16_f32 v26, v52, v53\n" \
"v_mfma_f32_16x16x32_bf16 v[68:71], a[108:111], a[68:71], v[68:71]\n" \
"s_add_u32 m0, s86, s79\n" \
"ds_read_b64_tr_b16 a[114:115], v214 offset:18944\n" \
"v_cvt_pk_bf16_f32 v27, v54, v55\n" \
"v_mfma_f32_16x16x32_bf16 v[84:87], a[104:107], a[72:75], v[84:87]\n" \
"buffer_load_dwordx4 v234, s[56:59], s81 offen offset:0 sc1 lds\n" \
"v_mfma_f32_16x16x32_bf16 v[84:87], a[108:111], a[76:79], v[84:87]\n" \
"s_waitcnt lgkmcnt(3)\n" \
"ds_read_b64_tr_b16 a[116:117], v214 offset:25088\n" \
"v_cvt_pk_bf16_f32 v28, v56, v57\n" \
"v_mfma_f32_16x16x32_bf16 v[80:83], a[96:99], a[72:75], v[80:83]\n" \
"s_lshl_b32 s82, s82, 5\n" \
"ds_read_b64_tr_b16 a[118:119], v214 offset:27136\n" \
"v_cvt_pk_bf16_f32 v29, v58, v59\n" \
"v_mfma_f32_16x16x32_bf16 v[80:83], a[100:103], a[76:79], v[80:83]\n" \
"s_add_u32 s82, s82, s47\n" \
"ds_read_b64_tr_b16 a[120:121], v215 offset:16896\n" \
"v_cvt_pk_bf16_f32 v30, v60, v61\n" \
"v_mfma_f32_16x16x32_bf16 v[96:99], a[96:99], a[80:83], v[96:99]\n" \
"buffer_load_dwordx4 v234, s[56:59], s81 offen offset:1024 sc1 lds\n" \
"v_mfma_f32_16x16x32_bf16 v[96:99], a[100:103], a[84:87], v[96:99]\n" \
"s_sub_u32 s85, s85, 32768\n" \
"s_cselect_b32 s85, 131072, s85\n" \
"ds_read_b64_tr_b16 a[122:123], v215 offset:18944\n" \
"v_mfma_f32_16x16x32_bf16 v[100:103], a[104:107], a[80:83], v[100:103]\n" \
"s_add_u32 s48, s48, 0x1000\n" \
"ds_read_b64_tr_b16 a[124:125], v215 offset:25088\n" \
"v_add_u32 v213, s85, v211\n" \
"v_mfma_f32_16x16x32_bf16 v[100:103], a[108:111], a[84:87], v[100:103]\n" \
"s_waitcnt lgkmcnt(3)\n" \
"s_sub_u32 s86, s86, 32768\n" \
"s_cselect_b32 s86, 131072, s86\n" \
"ds_read_b64_tr_b16 a[126:127], v215 offset:27136\n" \
"v_mfma_f32_16x16x32_bf16 v[116:119], a[104:107], a[88:91], v[116:119]\n" \
"buffer_load_dwordx4 v234, s[56:59], s81 offen offset:2048 sc1 lds\n" \
"v_mfma_f32_16x16x32_bf16 v[116:119], a[108:111], a[92:95], v[116:119]\n" \
"v_add_u32 v214, s85, v212\n" \
"v_xor_b32 v215, 32, v214\n" \
"v_mfma_f32_16x16x32_bf16 v[112:115], a[96:99], a[88:91], v[112:115]\n" \
"buffer_load_dwordx4 v234, s[56:59], s81 offen offset:3072 sc1 lds\n" \
"v_mfma_f32_16x16x32_bf16 v[112:115], a[100:103], a[92:95], v[112:115]\n" \
"s_waitcnt vmcnt(26) lgkmcnt(0)\n" \
"s_barrier\n" \
"v_mfma_f32_16x16x32_bf16 v[72:75], a[112:115], a[64:67], v[72:75]\n" \
"ds_read_b64_tr_b16 a[96:97], v214 offset:16384\n" \
"ds_read_b64_tr_b16 a[98:99], v214 offset:18432\n" \
"v_mfma_f32_16x16x32_bf16 v[72:75], a[116:119], a[68:71], v[72:75]\n" \
"s_add_u32 m0, m0, 0x4000\n" \
"ds_read_b64_tr_b16 a[100:101], v214 offset:24576\n" \
"ds_read_b64_tr_b16 a[102:103], v214 offset:26624\n" \
"v_mfma_f32_16x16x32_bf16 v[76:79], a[120:123], a[64:67], v[76:79]\n" \
"buffer_load_dwordx4 v216, s[36:39], s82 offen offset:0 lds\n" \
"v_mfma_f32_16x16x32_bf16 v[76:79], a[124:127], a[68:71], v[76:79]\n" \
"ds_read_b128 a[64:67], v213 offset:0\n" \
"v_cvt_pk_bf16_f32 v31, v62, v63\n" \
"v_mfma_f32_16x16x32_bf16 v[92:95], a[120:123], a[72:75], v[92:95]\n" \
"ds_read_b128 a[68:71], v213 offset:2048\n" \
"v_mfma_f32_16x16x32_bf16 v[92:95], a[124:127], a[76:79], v[92:95]\n" \
"ds_read_b64_tr_b16 a[104:105], v215 offset:16384\n" \
"ds_read_b64_tr_b16 a[106:107], v215 offset:18432\n" \
"v_mfma_f32_16x16x32_bf16 v[88:91], a[112:115], a[72:75], v[88:91]\n" \
"buffer_load_dwordx4 v217, s[36:39], s82 offen offset:1024 lds\n" \
"v_mfma_f32_16x16x32_bf16 v[88:91], a[116:119], a[76:79], v[88:91]\n" \
"ds_read_b128 a[72:75], v213 offset:256\n" \
"v_mfma_f32_16x16x32_bf16 v[104:107], a[112:115], a[80:83], v[104:107]\n" \
"ds_read_b128 a[76:79], v213 offset:2304\n" \
"v_mfma_f32_16x16x32_bf16 v[104:107], a[116:119], a[84:87], v[104:107]\n" \
"ds_read_b64_tr_b16 a[108:109], v215 offset:24576\n" \
"ds_read_b64_tr_b16 a[110:111], v215 offset:26624\n" \
"v_mfma_f32_16x16x32_bf16 v[108:111], a[120:123], a[80:83], v[108:111]\n" \
"buffer_load_dwordx4 v218, s[36:39], s82 offen offset:2048 lds\n" \
"v_mfma_f32_16x16x32_bf16 v[108:111], a[124:127], a[84:87], v[108:111]\n" \
"ds_read_b128 a[80:83], v213 offset:4096\n" \
"v_mfma_f32_16x16x32_bf16 v[124:127], a[120:123], a[88:91], v[124:127]\n" \
"s_waitcnt lgkmcnt(4)\n" \
"buffer_store_dwordx4 v[24:27], v234, s[56:59], s71 offen offset:2048 sc1\n" \
"v_mfma_f32_16x16x32_bf16 v[124:127], a[124:127], a[92:95], v[124:127]\n" \
"ds_read_b128 a[84:87], v213 offset:6144\n" \
"v_mfma_f32_16x16x32_bf16 v[120:123], a[112:115], a[88:91], v[120:123]\n" \
"buffer_load_dwordx4 v219, s[36:39], s82 offen offset:3072 lds\n" \
"v_mfma_f32_16x16x32_bf16 v[120:123], a[116:119], a[92:95], v[120:123]\n" \
"buffer_store_dwordx4 v[28:31], v234, s[56:59], s71 offen offset:3072 sc1\n" \
"v_mfma_f32_16x16x32_bf16 v[64:67], a[96:99], a[64:67], v[64:67]\n" \
"s_waitcnt lgkmcnt(1)\n" \
"s_xor_b32 s82, s48, s49\n" \
"ds_read_b128 a[88:91], v213 offset:4352\n" \
"v_mfma_f32_16x16x32_bf16 v[64:67], a[100:103], a[68:71], v[64:67]\n" \
"s_cmp_lt_u32 s48, s50\n" \
"s_cselect_b32 s82, s82, s48\n" \
"ds_read_b128 a[92:95], v213 offset:6400\n" \
"v_mfma_f32_16x16x32_bf16 v[68:71], a[104:107], a[64:67], v[68:71]\n" \
"s_add_u32 s81, s82, s46\n" \
"ds_read_b64_tr_b16 a[112:113], v214 offset:16896\n" \
"v_mfma_f32_16x16x32_bf16 v[68:71], a[108:111], a[68:71], v[68:71]\n" \
"s_add_u32 m0, s86, s79\n" \
"ds_read_b64_tr_b16 a[114:115], v214 offset:18944\n" \
"v_mfma_f32_16x16x32_bf16 v[84:87], a[104:107], a[72:75], v[84:87]\n" \
"buffer_load_dwordx4 v234, s[56:59], s81 offen offset:0 sc1 lds\n" \
"v_mfma_f32_16x16x32_bf16 v[84:87], a[108:111], a[76:79], v[84:87]\n" \
"ds_read_b64_tr_b16 a[116:117], v214 offset:25088\n" \
"v_mfma_f32_16x16x32_bf16 v[80:83], a[96:99], a[72:75], v[80:83]\n" \
"s_lshl_b32 s82, s82, 5\n" \
"ds_read_b64_tr_b16 a[118:119], v214 offset:27136\n" \
"v_mfma_f32_16x16x32_bf16 v[80:83], a[100:103], a[76:79], v[80:83]\n" \
"s_waitcnt lgkmcnt(3)\n" \
"s_add_u32 s82, s82, s47\n" \
"ds_read_b64_tr_b16 a[120:121], v215 offset:16896\n" \
"v_mfma_f32_16x16x32_bf16 v[96:99], a[96:99], a[80:83], v[96:99]\n" \
"buffer_load_dwordx4 v234, s[56:59], s81 offen offset:1024 sc1 lds\n" \
"v_mfma_f32_16x16x32_bf16 v[96:99], a[100:103], a[84:87], v[96:99]\n" \
"s_sub_u32 s85, s85, 32768\n" \
"s_cselect_b32 s85, 131072, s85\n" \
"ds_read_b64_tr_b16 a[122:123], v215 offset:18944\n" \
"v_mfma_f32_16x16x32_bf16 v[100:103], a[104:107], a[80:83], v[100:103]\n" \
"s_add_u32 s48, s48, 0x1000\n" \
"ds_read_b64_tr_b16 a[124:125], v215 offset:25088\n" \
"v_add_u32 v213, s85, v211\n" \
"v_mfma_f32_16x16x32_bf16 v[100:103], a[108:111], a[84:87], v[100:103]\n" \
"s_sub_u32 s86, s86, 32768\n" \
"s_cselect_b32 s86, 131072, s86\n" \
"ds_read_b64_tr_b16 a[126:127], v215 offset:27136\n" \
"v_mfma_f32_16x16x32_bf16 v[116:119], a[104:107], a[88:91], v[116:119]\n" \
"buffer_load_dwordx4 v234, s[56:59], s81 offen offset:2048 sc1 lds\n" \
"v_mfma_f32_16x16x32_bf16 v[116:119], a[108:111], a[92:95], v[116:119]\n" \
"v_add_u32 v214, s85, v212\n" \
"v_xor_b32 v215, 32, v214\n" \
"v_mfma_f32_16x16x32_bf16 v[112:115], a[96:99], a[88:91], v[112:115]\n" \
"buffer_load_dwordx4 v234, s[56:59], s81 offen offset:3072 sc1 lds\n" \
"v_mfma_f32_16x16x32_bf16 v[112:115], a[100:103], a[92:95], v[112:115]\n" \
"s_waitcnt vmcnt(27) lgkmcnt(0)\n" \
"s_barrier\n" \
"v_mfma_f32_16x16x32_bf16 v[72:75], a[112:115], a[64:67], v[72:75]\n" \
"ds_read_b64_tr_b16 a[96:97], v214 offset:16384\n" \
"ds_read_b64_tr_b16 a[98:99], v214 offset:18432\n" \
"v_mfma_f32_16x16x32_bf16 v[72:75], a[116:119], a[68:71], v[72:75]\n" \
"s_add_u32 m0, m0, 0x4000\n" \
"ds_read_b64_tr_b16 a[100:101], v214 offset:24576\n" \
"ds_read_b64_tr_b16 a[102:103], v214 offset:26624\n" \
"v_mfma_f32_16x16x32_bf16 v[76:79], a[120:123], a[64:67], v[76:79]\n" \
"buffer_load_dwordx4 v216, s[36:39], s82 offen offset:0 lds\n" \
"v_mfma_f32_16x16x32_bf16 v[76:79], a[124:127], a[68:71], v[76:79]\n" \
"ds_read_b128 a[64:67], v213 offset:0\n" \
"v_mfma_f32_16x16x32_bf16 v[92:95], a[120:123], a[72:75], v[92:95]\n" \
"ds_read_b128 a[68:71], v213 offset:2048\n" \
"v_mfma_f32_16x16x32_bf16 v[92:95], a[124:127], a[76:79], v[92:95]\n" \
"ds_read_b64_tr_b16 a[104:105], v215 offset:16384\n" \
"ds_read_b64_tr_b16 a[106:107], v215 offset:18432\n" \
"v_mfma_f32_16x16x32_bf16 v[88:91], a[112:115], a[72:75], v[88:91]\n" \
"buffer_load_dwordx4 v217, s[36:39], s82 offen offset:1024 lds\n" \
"v_mfma_f32_16x16x32_bf16 v[88:91], a[116:119], a[76:79], v[88:91]\n" \
"ds_read_b128 a[72:75], v213 offset:256\n" \
"v_mfma_f32_16x16x32_bf16 v[104:107], a[112:115], a[80:83], v[104:107]\n" \
"ds_read_b128 a[76:79], v213 offset:2304\n" \
"v_mfma_f32_16x16x32_bf16 v[104:107], a[116:119], a[84:87], v[104:107]\n" \
"ds_read_b64_tr_b16 a[108:109], v215 offset:24576\n" \
"ds_read_b64_tr_b16 a[110:111], v215 offset:26624\n" \
"v_mfma_f32_16x16x32_bf16 v[108:111], a[120:123], a[80:83], v[108:111]\n" \
"buffer_load_dwordx4 v218, s[36:39], s82 offen offset:2048 lds\n" \
"v_mfma_f32_16x16x32_bf16 v[108:111], a[124:127], a[84:87], v[108:111]\n" \
"ds_read_b128 a[80:83], v213 offset:4096\n" \
"v_mfma_f32_16x16x32_bf16 v[124:127], a[120:123], a[88:91], v[124:127]\n" \
"s_waitcnt lgkmcnt(4)\n" \
"v_mfma_f32_16x16x32_bf16 v[124:127], a[124:127], a[92:95], v[124:127]\n" \
"ds_read_b128 a[84:87], v213 offset:6144\n" \
"v_mfma_f32_16x16x32_bf16 v[120:123], a[112:115], a[88:91], v[120:123]\n" \
"buffer_load_dwordx4 v219, s[36:39], s82 offen offset:3072 lds\n" \
"v_mfma_f32_16x16x32_bf16 v[120:123], a[116:119], a[92:95], v[120:123]\n" \
"v_mfma_f32_16x16x32_bf16 v[64:67], a[96:99], a[64:67], v[64:67]\n" \
"s_waitcnt lgkmcnt(1)\n" \
"s_xor_b32 s82, s48, s49\n" \
"ds_read_b128 a[88:91], v213 offset:4352\n" \
"v_mfma_f32_16x16x32_bf16 v[64:67], a[100:103], a[68:71], v[64:67]\n" \
"s_cmp_lt_u32 s48, s50\n" \
"s_cselect_b32 s82, s82, s48\n" \
"ds_read_b128 a[92:95], v213 offset:6400\n" \
"v_mfma_f32_16x16x32_bf16 v[68:71], a[104:107], a[64:67], v[68:71]\n" \
"s_add_u32 s81, s82, s46\n" \
"ds_read_b64_tr_b16 a[112:113], v214 offset:16896\n" \
"v_mfma_f32_16x16x32_bf16 v[68:71], a[108:111], a[68:71], v[68:71]\n" \
"s_add_u32 m0, s86, s79\n" \
"ds_read_b64_tr_b16 a[114:115], v214 offset:18944\n" \
"v_mfma_f32_16x16x32_bf16 v[84:87], a[104:107], a[72:75], v[84:87]\n" \
"buffer_load_dwordx4 v234, s[56:59], s81 offen offset:0 sc1 lds\n" \
"v_mfma_f32_16x16x32_bf16 v[84:87], a[108:111], a[76:79], v[84:87]\n" \
"ds_read_b64_tr_b16 a[116:117], v214 offset:25088\n" \
"v_mfma_f32_16x16x32_bf16 v[80:83], a[96:99], a[72:75], v[80:83]\n" \
"s_lshl_b32 s82, s82, 5\n" \
"ds_read_b64_tr_b16 a[118:119], v214 offset:27136\n" \
"v_mfma_f32_16x16x32_bf16 v[80:83], a[100:103], a[76:79], v[80:83]\n" \
"s_waitcnt lgkmcnt(3)\n" \
"s_add_u32 s82, s82, s47\n" \
"ds_read_b64_tr_b16 a[120:121], v215 offset:16896\n" \
"v_mfma_f32_16x16x32_bf16 v[96:99], a[96:99], a[80:83], v[96:99]\n" \
"buffer_load_dwordx4 v234, s[56:59], s81 offen offset:1024 sc1 lds\n" \
"v_mfma_f32_16x16x32_bf16 v[96:99], a[100:103], a[84:87], v[96:99]\n" \
"s_sub_u32 s85, s85, 32768\n" \
"s_cselect_b32 s85, 131072, s85\n" \
"ds_read_b64_tr_b16 a[122:123], v215 offset:18944\n" \
"v_mfma_f32_16x16x32_bf16 v[100:103], a[104:107], a[80:83], v[100:103]\n" \
"s_add_u32 s48, s48, 0x1000\n" \
"ds_read_b64_tr_b16 a[124:125], v215 offset:25088\n" \
"v_add_u32 v213, s85, v211\n" \
"v_mfma_f32_16x16x32_bf16 v[100:103], a[108:111], a[84:87], v[100:103]\n" \
"s_sub_u32 s86, s86, 32768\n" \
"s_cselect_b32 s86, 131072, s86\n" \
"ds_read_b64_tr_b16 a[126:127], v215 offset:27136\n" \
"v_mfma_f32_16x16x32_bf16 v[116:119], a[104:107], a[88:91], v[116:119]\n" \
"buffer_load_dwordx4 v234, s[56:59], s81 offen offset:2048 sc1 lds\n" \
"v_mfma_f32_16x16x32_bf16 v[116:119], a[108:111], a[92:95], v[116:119]\n" \
"v_add_u32 v214, s85, v212\n" \
"v_xor_b32 v215, 32, v214\n" \
"v_mfma_f32_16x16x32_bf16 v[112:115], a[96:99], a[88:91], v[112:115]\n" \
"buffer_load_dwordx4 v234, s[56:59], s81 offen offset:3072 sc1 lds\n" \
"v_mfma_f32_16x16x32_bf16 v[112:115], a[100:103], a[92:95], v[112:115]\n" \
"s_waitcnt vmcnt(24) lgkmcnt(0)\n" \
"s_barrier\n" \
"v_mfma_f32_16x16x32_bf16 v[72:75], a[112:115], a[64:67], v[72:75]\n" \
"ds_read_b64_tr_b16 a[96:97], v214 offset:16384\n" \
"ds_read_b64_tr_b16 a[98:99], v214 offset:18432\n" \
"v_mfma_f32_16x16x32_bf16 v[72:75], a[116:119], a[68:71], v[72:75]\n" \
"s_add_u32 m0, m0, 0x4000\n" \
"ds_read_b64_tr_b16 a[100:101], v214 offset:24576\n" \
"ds_read_b64_tr_b16 a[102:103], v214 offset:26624\n" \
"v_mfma_f32_16x16x32_bf16 v[76:79], a[120:123], a[64:67], v[76:79]\n" \
"buffer_load_dwordx4 v216, s[36:39], s82 offen offset:0 lds\n" \
"v_mfma_f32_16x16x32_bf16 v[76:79], a[124:127], a[68:71], v[76:79]\n" \
"ds_read_b128 a[64:67], v213 offset:0\n" \
"v_mfma_f32_16x16x32_bf16 v[92:95], a[120:123], a[72:75], v[92:95]\n" \
"ds_read_b128 a[68:71], v213 offset:2048\n" \
"v_mfma_f32_16x16x32_bf16 v[92:95], a[124:127], a[76:79], v[92:95]\n" \
"ds_read_b64_tr_b16 a[104:105], v215 offset:16384\n" \
"ds_read_b64_tr_b16 a[106:107], v215 offset:18432\n" \
"v_mfma_f32_16x16x32_bf16 v[88:91], a[112:115], a[72:75], v[88:91]\n" \
"buffer_load_dwordx4 v217, s[36:39], s82 offen offset:1024 lds\n" \
"v_mfma_f32_16x16x32_bf16 v[88:91], a[116:119], a[76:79], v[88:91]\n" \
"ds_read_b128 a[72:75], v213 offset:256\n" \
"v_mfma_f32_16x16x32_bf16 v[104:107], a[112:115], a[80:83], v[104:107]\n" \
"ds_read_b128 a[76:79], v213 offset:2304\n" \
"v_mfma_f32_16x16x32_bf16 v[104:107], a[116:119], a[84:87], v[104:107]\n" \
"ds_read_b64_tr_b16 a[108:109], v215 offset:24576\n" \
"ds_read_b64_tr_b16 a[110:111], v215 offset:26624\n" \
"v_mfma_f32_16x16x32_bf16 v[108:111], a[120:123], a[80:83], v[108:111]\n" \
"buffer_load_dwordx4 v218, s[36:39], s82 offen offset:2048 lds\n" \
"v_mfma_f32_16x16x32_bf16 v[108:111], a[124:127], a[84:87], v[108:111]\n" \
"ds_read_b128 a[80:83], v213 offset:4096\n" \
"v_mfma_f32_16x16x32_bf16 v[124:127], a[120:123], a[88:91], v[124:127]\n" \
"s_waitcnt lgkmcnt(4)\n" \
"v_mfma_f32_16x16x32_bf16 v[124:127], a[124:127], a[92:95], v[124:127]\n" \
"ds_read_b128 a[84:87], v213 offset:6144\n" \
"v_mfma_f32_16x16x32_bf16 v[120:123], a[112:115], a[88:91], v[120:123]\n" \
"buffer_load_dwordx4 v219, s[36:39], s82 offen offset:3072 lds\n" \
"v_mfma_f32_16x16x32_bf16 v[120:123], a[116:119], a[92:95], v[120:123]\n" \
"v_mfma_f32_16x16x32_bf16 v[64:67], a[96:99], a[64:67], v[64:67]\n" \
"s_waitcnt lgkmcnt(1)\n" \
"s_xor_b32 s82, s48, s49\n" \
"ds_read_b128 a[88:91], v213 offset:4352\n" \
"v_mfma_f32_16x16x32_bf16 v[64:67], a[100:103], a[68:71], v[64:67]\n" \
"s_cmp_lt_u32 s48, s50\n" \
"s_cselect_b32 s82, s82, s48\n" \
"ds_read_b128 a[92:95], v213 offset:6400\n" \
"v_mfma_f32_16x16x32_bf16 v[68:71], a[104:107], a[64:67], v[68:71]\n" \
"s_add_u32 s81, s82, s46\n" \
"ds_read_b64_tr_b16 a[112:113], v214 offset:16896\n" \
"v_mfma_f32_16x16x32_bf16 v[68:71], a[108:111], a[68:71], v[68:71]\n" \
"s_add_u32 m0, s86, s79\n" \
"ds_read_b64_tr_b16 a[114:115], v214 offset:18944\n" \
"v_mfma_f32_16x16x32_bf16 v[84:87], a[104:107], a[72:75], v[84:87]\n" \
"buffer_load_dwordx4 v234, s[56:59], s81 offen offset:0 sc1 lds\n" \
"v_mfma_f32_16x16x32_bf16 v[84:87], a[108:111], a[76:79], v[84:87]\n" \
"ds_read_b64_tr_b16 a[116:117], v214 offset:25088\n" \
"v_mfma_f32_16x16x32_bf16 v[80:83], a[96:99], a[72:75], v[80:83]\n" \
"s_lshl_b32 s82, s82, 5\n" \
"ds_read_b64_tr_b16 a[118:119], v214 offset:27136\n" \
"v_mfma_f32_16x16x32_bf16 v[80:83], a[100:103], a[76:79], v[80:83]\n" \
"s_waitcnt lgkmcnt(3)\n" \
"s_add_u32 s82, s82, s47\n" \
"ds_read_b64_tr_b16 a[120:121], v215 offset:16896\n" \
"v_mfma_f32_16x16x32_bf16 v[96:99], a[96:99], a[80:83], v[96:99]\n" \
"buffer_load_dwordx4 v234, s[56:59], s81 offen offset:1024 sc1 lds\n" \
"v_mfma_f32_16x16x32_bf16 v[96:99], a[100:103], a[84:87], v[96:99]\n" \
"s_sub_u32 s85, s85, 32768\n" \
"s_cselect_b32 s85, 131072, s85\n" \
"ds_read_b64_tr_b16 a[122:123], v215 offset:18944\n" \
"v_mfma_f32_16x16x32_bf16 v[100:103], a[104:107], a[80:83], v[100:103]\n" \
"s_add_u32 s48, s48, 0x1000\n" \
"ds_read_b64_tr_b16 a[124:125], v215 offset:25088\n" \
"v_add_u32 v213, s85, v211\n" \
"v_mfma_f32_16x16x32_bf16 v[100:103], a[108:111], a[84:87], v[100:103]\n" \
"s_sub_u32 s86, s86, 32768\n" \
"s_cselect_b32 s86, 131072, s86\n" \
"ds_read_b64_tr_b16 a[126:127], v215 offset:27136\n" \
"v_mfma_f32_16x16x32_bf16 v[116:119], a[104:107], a[88:91], v[116:119]\n" \
"buffer_load_dwordx4 v234, s[56:59], s81 offen offset:2048 sc1 lds\n" \
"v_mfma_f32_16x16x32_bf16 v[116:119], a[108:111], a[92:95], v[116:119]\n" \
"v_add_u32 v214, s85, v212\n" \
"v_xor_b32 v215, 32, v214\n" \
"v_mfma_f32_16x16x32_bf16 v[112:115], a[96:99], a[88:91], v[112:115]\n" \
"buffer_load_dwordx4 v234, s[56:59], s81 offen offset:3072 sc1 lds\n" \
"v_mfma_f32_16x16x32_bf16 v[112:115], a[100:103], a[92:95], v[112:115]\n" \
"s_waitcnt vmcnt(20) lgkmcnt(0)\n" \
"s_barrier\n" \
"buffer_store_dword v230, v231, s[56:59], s64 offen sc1\n" \
"v_mfma_f32_16x16x32_bf16 v[72:75], a[112:115], a[64:67], v[72:75]\n" \
"ds_read_b64_tr_b16 a[96:97], v214 offset:16384\n" \
"ds_read_b64_tr_b16 a[98:99], v214 offset:18432\n" \
"v_mfma_f32_16x16x32_bf16 v[72:75], a[116:119], a[68:71], v[72:75]\n" \
"s_add_u32 m0, m0, 0x4000\n" \
"ds_read_b64_tr_b16 a[100:101], v214 offset:24576\n" \
"ds_read_b64_tr_b16 a[102:103], v214 offset:26624\n" \
"v_mfma_f32_16x16x32_bf16 v[76:79], a[120:123], a[64:67], v[76:79]\n" \
"buffer_load_dwordx4 v216, s[36:39], s82 offen offset:0 lds\n" \
"v_mfma_f32_16x16x32_bf16 v[76:79], a[124:127], a[68:71], v[76:79]\n" \
"ds_read_b128 a[64:67], v213 offset:0\n" \
"v_mfma_f32_16x16x32_bf16 v[92:95], a[120:123], a[72:75], v[92:95]\n" \
"ds_read_b128 a[68:71], v213 offset:2048\n" \
"v_mfma_f32_16x16x32_bf16 v[92:95], a[124:127], a[76:79], v[92:95]\n" \
"ds_read_b64_tr_b16 a[104:105], v215 offset:16384\n" \
"ds_read_b64_tr_b16 a[106:107], v215 offset:18432\n" \
"v_mfma_f32_16x16x32_bf16 v[88:91], a[112:115], a[72:75], v[88:91]\n" \
"buffer_load_dwordx4 v217, s[36:39], s82 offen offset:1024 lds\n" \
"v_mfma_f32_16x16x32_bf16 v[88:91], a[116:119], a[76:79], v[88:91]\n" \
"ds_read_b128 a[72:75], v213 offset:256\n" \
"v_mfma_f32_16x16x32_bf16 v[104:107], a[112:115], a[80:83], v[104:107]\n" \
"ds_read_b128 a[76:79], v213 offset:2304\n" \
"v_mfma_f32_16x16x32_bf16 v[104:107], a[116:119], a[84:87], v[104:107]\n" \
"ds_read_b64_tr_b16 a[108:109], v215 offset:24576\n" \
"ds_read_b64_tr_b16 a[110:111], v215 offset:26624\n" \
"v_mfma_f32_16x16x32_bf16 v[108:111], a[120:123], a[80:83], v[108:111]\n" \
"buffer_load_dwordx4 v218, s[36:39], s82 offen offset:2048 lds\n" \
"v_mfma_f32_16x16x32_bf16 v[108:111], a[124:127], a[84:87], v[108:111]\n" \
"ds_read_b128 a[80:83], v213 offset:4096\n" \
"v_mfma_f32_16x16x32_bf16 v[124:127], a[120:123], a[88:91], v[124:127]\n" \
"s_waitcnt lgkmcnt(4)\n" \
"v_mfma_f32_16x16x32_bf16 v[124:127], a[124:127], a[92:95], v[124:127]\n" \
"ds_read_b128 a[84:87], v213 offset:6144\n" \
"v_mfma_f32_16x16x32_bf16 v[120:123], a[112:115], a[88:91], v[120:123]\n" \
"buffer_load_dwordx4 v219, s[36:39], s82 offen offset:3072 lds\n" \
"v_mfma_f32_16x16x32_bf16 v[120:123], a[116:119], a[92:95], v[120:123]\n" \
"s_mov_b32 s72, 5\n" \
"Lo64_%=:\n" \
"v_mfma_f32_16x16x32_bf16 v[64:67], a[96:99], a[64:67], v[64:67]\n" \
"s_waitcnt lgkmcnt(1)\n" \
"s_xor_b32 s82, s48, s49\n" \
"ds_read_b128 a[88:91], v213 offset:4352\n" \
"Lpg11486_%=:\n" \
"s_cbranch_execz Lcb1_%=\n" \
"v_mfma_f32_16x16x32_bf16 v[64:67], a[100:103], a[68:71], v[64:67]\n" \
"s_cmp_lt_u32 s48, s50\n" \
"s_cselect_b32 s82, s82, s48\n" \
"ds_read_b128 a[92:95], v213 offset:6400\n" \
"v_mfma_f32_16x16x32_bf16 v[68:71], a[104:107], a[64:67], v[68:71]\n" \
"s_add_u32 s81, s82, s46\n" \
"ds_read_b64_tr_b16 a[112:113], v214 offset:16896\n" \
"v_mfma_f32_16x16x32_bf16 v[68:71], a[108:111], a[68:71], v[68:71]\n" \
"s_add_u32 m0, s86, s79\n" \
"ds_read_b64_tr_b16 a[114:115], v214 offset:18944\n" \
"v_mfma_f32_16x16x32_bf16 v[84:87], a[104:107], a[72:75], v[84:87]\n" \
"buffer_load_dwordx4 v234, s[56:59], s81 offen offset:0 sc1 lds\n" \
"v_mfma_f32_16x16x32_bf16 v[84:87], a[108:111], a[76:79], v[84:87]\n" \
"ds_read_b64_tr_b16 a[116:117], v214 offset:25088\n" \
"v_mfma_f32_16x16x32_bf16 v[80:83], a[96:99], a[72:75], v[80:83]\n" \
"s_lshl_b32 s82, s82, 5\n" \
"ds_read_b64_tr_b16 a[118:119], v214 offset:27136\n" \
"v_mfma_f32_16x16x32_bf16 v[80:83], a[100:103], a[76:79], v[80:83]\n" \
"s_waitcnt lgkmcnt(3)\n" \
"s_add_u32 s82, s82, s47\n" \
"ds_read_b64_tr_b16 a[120:121], v215 offset:16896\n" \
"v_mfma_f32_16x16x32_bf16 v[96:99], a[96:99], a[80:83], v[96:99]\n" \
"buffer_load_dwordx4 v234, s[56:59], s81 offen offset:1024 sc1 lds\n" \
"v_mfma_f32_16x16x32_bf16 v[96:99], a[100:103], a[84:87], v[96:99]\n" \
"s_sub_u32 s85, s85, 32768\n" \
"s_cselect_b32 s85, 131072, s85\n" \
"ds_read_b64_tr_b16 a[122:123], v215 offset:18944\n" \
"v_mfma_f32_16x16x32_bf16 v[100:103], a[104:107], a[80:83], v[100:103]\n" \
"s_add_u32 s48, s48, 0x1000\n" \
"ds_read_b64_tr_b16 a[124:125], v215 offset:25088\n" \
"v_add_u32 v213, s85, v211\n" \
"v_mfma_f32_16x16x32_bf16 v[100:103], a[108:111], a[84:87], v[100:103]\n" \
"s_sub_u32 s86, s86, 32768\n" \
"s_cselect_b32 s86, 131072, s86\n" \
"ds_read_b64_tr_b16 a[126:127], v215 offset:27136\n" \
"v_mfma_f32_16x16x32_bf16 v[116:119], a[104:107], a[88:91], v[116:119]\n" \
"buffer_load_dwordx4 v234, s[56:59], s81 offen offset:2048 sc1 lds\n" \
"v_mfma_f32_16x16x32_bf16 v[116:119], a[108:111], a[92:95], v[116:119]\n" \
"v_add_u32 v214, s85, v212\n" \
"v_xor_b32 v215, 32, v214\n" \
"v_mfma_f32_16x16x32_bf16 v[112:115], a[96:99], a[88:91], v[112:115]\n" \
"buffer_load_dwordx4 v234, s[56:59], s81 offen offset:3072 sc1 lds\n" \
"v_mfma_f32_16x16x32_bf16 v[112:115], a[100:103], a[92:95], v[112:115]\n" \
"s_waitcnt vmcnt(20) lgkmcnt(0)\n" \
"s_barrier\n" \
"v_mfma_f32_16x16x32_bf16 v[72:75], a[112:115], a[64:67], v[72:75]\n" \
"ds_read_b64_tr_b16 a[96:97], v214 offset:16384\n" \
"ds_read_b64_tr_b16 a[98:99], v214 offset:18432\n" \
"v_mfma_f32_16x16x32_bf16 v[72:75], a[116:119], a[68:71], v[72:75]\n" \
"s_add_u32 m0, m0, 0x4000\n" \
"ds_read_b64_tr_b16 a[100:101], v214 offset:24576\n" \
"ds_read_b64_tr_b16 a[102:103], v214 offset:26624\n" \
"v_mfma_f32_16x16x32_bf16 v[76:79], a[120:123], a[64:67], v[76:79]\n" \
"buffer_load_dwordx4 v216, s[36:39], s82 offen offset:0 lds\n" \
"v_mfma_f32_16x16x32_bf16 v[76:79], a[124:127], a[68:71], v[76:79]\n" \
"ds_read_b128 a[64:67], v213 offset:0\n" \
"v_mfma_f32_16x16x32_bf16 v[92:95], a[120:123], a[72:75], v[92:95]\n" \
"ds_read_b128 a[68:71], v213 offset:2048\n" \
"v_mfma_f32_16x16x32_bf16 v[92:95], a[124:127], a[76:79], v[92:95]\n" \
"ds_read_b64_tr_b16 a[104:105], v215 offset:16384\n" \
"ds_read_b64_tr_b16 a[106:107], v215 offset:18432\n" \
"v_mfma_f32_16x16x32_bf16 v[88:91], a[112:115], a[72:75], v[88:91]\n" \
"buffer_load_dwordx4 v217, s[36:39], s82 offen offset:1024 lds\n" \
"v_mfma_f32_16x16x32_bf16 v[88:91], a[116:119], a[76:79], v[88:91]\n" \
"ds_read_b128 a[72:75], v213 offset:256\n" \
"v_mfma_f32_16x16x32_bf16 v[104:107], a[112:115], a[80:83], v[104:107]\n" \
"ds_read_b128 a[76:79], v213 offset:2304\n" \
"v_mfma_f32_16x16x32_bf16 v[104:107], a[116:119], a[84:87], v[104:107]\n" \
"ds_read_b64_tr_b16 a[108:109], v215 offset:24576\n" \
"ds_read_b64_tr_b16 a[110:111], v215 offset:26624\n" \
"v_mfma_f32_16x16x32_bf16 v[108:111], a[120:123], a[80:83], v[108:111]\n" \
"buffer_load_dwordx4 v218, s[36:39], s82 offen offset:2048 lds\n" \
"v_mfma_f32_16x16x32_bf16 v[108:111], a[124:127], a[84:87], v[108:111]\n" \
"ds_read_b128 a[80:83], v213 offset:4096\n" \
"v_mfma_f32_16x16x32_bf16 v[124:127], a[120:123], a[88:91], v[124:127]\n" \
"s_waitcnt lgkmcnt(4)\n" \
"v_mfma_f32_16x16x32_bf16 v[124:127], a[124:127], a[92:95], v[124:127]\n" \
"s_sub_u32 s72, s72, 1\n" \
"ds_read_b128 a[84:87], v213 offset:6144\n" \
"v_mfma_f32_16x16x32_bf16 v[120:123], a[112:115], a[88:91], v[120:123]\n" \
"buffer_load_dwordx4 v219, s[36:39], s82 offen offset:3072 lds\n" \
"v_mfma_f32_16x16x32_bf16 v[120:123], a[116:119], a[92:95], v[120:123]\n" \
"s_cbranch_scc0 Lo64_%=\n" \
"v_mfma_f32_16x16x32_bf16 v[64:67], a[96:99], a[64:67], v[64:67]\n" \
"s_waitcnt lgkmcnt(1)\n" \
"ds_read_b128 a[88:91], v213 offset:4352\n" \
"v_mfma_f32_16x16x32_bf16 v[64:67], a[100:103], a[68:71], v[64:67]\n" \
"s_add_u32 s48, s86, 32768\n" \
"s_cmp_eq_u32 s48, 163840\n" \
"s_cselect_b32 s48, 0, s48\n" \
"ds_read_b128 a[92:95], v213 offset:6400\n" \
"v_mfma_f32_16x16x32_bf16 v[68:71], a[104:107], a[64:67], v[68:71]\n" \
"ds_read_b64_tr_b16 a[112:113], v214 offset:16896\n" \
"v_mfma_f32_16x16x32_bf16 v[68:71], a[108:111], a[68:71], v[68:71]\n" \
"ds_read_b64_tr_b16 a[114:115], v214 offset:18944\n" \
"v_mfma_f32_16x16x32_bf16 v[84:87], a[104:107], a[72:75], v[84:87]\n" \
"s_add_u32 s49, s48, 32768\n" \
"s_cmp_eq_u32 s49, 163840\n" \
"s_cselect_b32 s49, 0, s49\n" \
"v_mfma_f32_16x16x32_bf16 v[84:87], a[108:111], a[76:79], v[84:87]\n" \
"ds_read_b64_tr_b16 a[116:117], v214 offset:25088\n" \
"v_add_u32 v224, s49, v211\n" \
"v_mfma_f32_16x16x32_bf16 v[80:83], a[96:99], a[72:75], v[80:83]\n" \
"ds_read_b64_tr_b16 a[118:119], v214 offset:27136\n" \
"v_add_u32 v225, s49, v212\n" \
"v_mfma_f32_16x16x32_bf16 v[80:83], a[100:103], a[76:79], v[80:83]\n" \
"s_waitcnt lgkmcnt(3)\n" \
"ds_read_b64_tr_b16 a[120:121], v215 offset:16896\n" \
"v_xor_b32 v226, 32, v225\n" \
"v_mfma_f32_16x16x32_bf16 v[96:99], a[96:99], a[80:83], v[96:99]\n" \
"v_add_u32 v227, s48, v211\n" \
"v_add_u32 v228, s48, v212\n" \
"v_mfma_f32_16x16x32_bf16 v[96:99], a[100:103], a[84:87], v[96:99]\n" \
"s_sub_u32 s85, s85, 32768\n" \
"s_cselect_b32 s85, 131072, s85\n" \
"ds_read_b64_tr_b16 a[122:123], v215 offset:18944\n" \
"v_mfma_f32_16x16x32_bf16 v[100:103], a[104:107], a[80:83], v[100:103]\n" \
"ds_read_b64_tr_b16 a[124:125], v215 offset:25088\n" \
"v_add_u32 v213, s85, v211\n" \
"v_mfma_f32_16x16x32_bf16 v[100:103], a[108:111], a[84:87], v[100:103]\n" \
"ds_read_b64_tr_b16 a[126:127], v215 offset:27136\n" \
"v_xor_b32 v229, 32, v228\n" \
"v_mfma_f32_16x16x32_bf16 v[116:119], a[104:107], a[88:91], v[116:119]\n" \
"v_mfma_f32_16x16x32_bf16 v[116:119], a[108:111], a[92:95], v[116:119]\n" \
"v_add_u32 v214, s85, v212\n" \
"v_xor_b32 v215, 32, v214\n" \
"v_mfma_f32_16x16x32_bf16 v[112:115], a[96:99], a[88:91], v[112:115]\n" \
"v_mfma_f32_16x16x32_bf16 v[112:115], a[100:103], a[92:95], v[112:115]\n" \
"s_waitcnt vmcnt(8) lgkmcnt(0)\n" \
"s_barrier\n" \
"v_mfma_f32_16x16x32_bf16 v[72:75], a[112:115], a[64:67], v[72:75]\n" \
"ds_read_b128 a[0:3], v213 offset:0\n" \
"ds_read_b128 a[4:7], v213 offset:2048\n" \
"v_mfma_f32_16x16x32_bf16 v[72:75], a[116:119], a[68:71], v[72:75]\n" \
"s_sub_u32 s68, s86, 32768\n" \
"s_cselect_b32 s68, 131072, s68\n" \
"ds_read_b64_tr_b16 a[128:129], v214 offset:16384\n" \
"ds_read_b64_tr_b16 a[130:131], v214 offset:18432\n" \
"v_mfma_f32_16x16x32_bf16 v[76:79], a[120:123], a[64:67], v[76:79]\n" \
"ds_read_b64_tr_b16 a[132:133], v214 offset:24576\n" \
"ds_read_b64_tr_b16 a[134:135], v214 offset:26624\n" \
"v_mfma_f32_16x16x32_bf16 v[76:79], a[124:127], a[68:71], v[76:79]\n" \
"s_cmp_lt_u32 s61, 2\n" \
"s_cselect_b32 s68, s86, s68\n" \
"ds_read_b64_tr_b16 a[136:137], v215 offset:16384\n" \
"ds_read_b64_tr_b16 a[138:139], v215 offset:18432\n" \
"v_mfma_f32_16x16x32_bf16 v[92:95], a[120:123], a[72:75], v[92:95]\n" \
"s_and_b32 s50, s61, 1\n" \
"ds_read_b64_tr_b16 a[140:141], v215 offset:24576\n" \
"ds_read_b64_tr_b16 a[142:143], v215 offset:26624\n" \
"v_mfma_f32_16x16x32_bf16 v[92:95], a[124:127], a[76:79], v[92:95]\n" \
"s_mul_i32 s50, s50, 9216\n" \
"ds_read_b64_tr_b16 a[144:145], v214 offset:16896\n" \
"ds_read_b64_tr_b16 a[146:147], v214 offset:18944\n" \
"v_mfma_f32_16x16x32_bf16 v[88:91], a[112:115], a[72:75], v[88:91]\n" \
"s_add_u32 s68, s68, s50\n" \
"ds_read_b64_tr_b16 a[148:149], v214 offset:25088\n" \
"ds_read_b64_tr_b16 a[150:151], v214 offset:27136\n" \
"v_mfma_f32_16x16x32_bf16 v[88:91], a[116:119], a[76:79], v[88:91]\n" \
"s_add_u32 s68, s68, %3\n" \
"ds_read_b64_tr_b16 a[152:153], v215 offset:16896\n" \
"ds_read_b64_tr_b16 a[154:155], v215 offset:18944\n" \
"v_mfma_f32_16x16x32_bf16 v[104:107], a[112:115], a[80:83], v[104:107]\n" \
"ds_read_b64_tr_b16 a[156:157], v215 offset:25088\n" \
"ds_read_b64_tr_b16 a[158:159], v215 offset:27136\n" \
"v_mfma_f32_16x16x32_bf16 v[104:107], a[116:119], a[84:87], v[104:107]\n" \
"ds_read_b128 a[24:27], v213 offset:256\n" \
"ds_read_b128 a[28:31], v213 offset:2304\n" \
"v_mfma_f32_16x16x32_bf16 v[108:111], a[120:123], a[80:83], v[108:111]\n" \
"ds_read_b128 a[8:11], v224 offset:0\n" \
"ds_read_b128 a[12:15], v224 offset:2048\n" \
"v_mfma_f32_16x16x32_bf16 v[108:111], a[124:127], a[84:87], v[108:111]\n" \
"ds_read_b64_tr_b16 a[160:161], v225 offset:16384\n" \
"ds_read_b64_tr_b16 a[162:163], v225 offset:18432\n" \
"v_mfma_f32_16x16x32_bf16 v[124:127], a[120:123], a[88:91], v[124:127]\n" \
"ds_read_b64_tr_b16 a[164:165], v225 offset:24576\n" \
"ds_read_b64_tr_b16 a[166:167], v225 offset:26624\n" \
"v_mfma_f32_16x16x32_bf16 v[124:127], a[124:127], a[92:95], v[124:127]\n" \
"ds_read_b64_tr_b16 a[168:169], v226 offset:16384\n" \
"ds_read_b64_tr_b16 a[170:171], v226 offset:18432\n" \
"v_mfma_f32_16x16x32_bf16 v[120:123], a[112:115], a[88:91], v[120:123]\n" \
"ds_read_b64_tr_b16 a[172:173], v226 offset:24576\n" \
"ds_read_b64_tr_b16 a[174:175], v226 offset:26624\n" \
"v_mfma_f32_16x16x32_bf16 v[120:123], a[116:119], a[92:95], v[120:123]\n" \
"s_waitcnt lgkmcnt(8)\n" \
"ds_read_b64_tr_b16 a[176:177], v225 offset:16896\n" \
"ds_read_b64_tr_b16 a[178:179], v225 offset:18944\n" \
"v_mfma_f32_16x16x32_bf16 v[64:67], a[128:131], a[0:3], v[64:67]\n" \
"ds_read_b64_tr_b16 a[180:181], v225 offset:25088\n" \
"ds_read_b64_tr_b16 a[182:183], v225 offset:27136\n" \
"v_mfma_f32_16x16x32_bf16 v[64:67], a[132:135], a[4:7], v[64:67]\n" \
"ds_read_b64_tr_b16 a[184:185], v226 offset:16896\n" \
"ds_read_b64_tr_b16 a[186:187], v226 offset:18944\n" \
"v_mfma_f32_16x16x32_bf16 v[68:71], a[136:139], a[0:3], v[68:71]\n" \
"ds_read_b64_tr_b16 a[188:189], v226 offset:25088\n" \
"ds_read_b64_tr_b16 a[190:191], v226 offset:27136\n" \
"v_mfma_f32_16x16x32_bf16 v[68:71], a[140:143], a[4:7], v[68:71]\n" \
"v_lshrrev_b32 v132, 4, v234\n" \
"v_and_b32 v133, 15, v132\n" \
"v_mfma_f32_16x16x32_bf16 v[72:75], a[144:147], a[0:3], v[72:75]\n" \
"v_mul_u32_u24 v128, 144, v133\n" \
"v_lshrrev_b32 v133, 4, v132\n" \
"v_mfma_f32_16x16x32_bf16 v[72:75], a[148:151], a[4:7], v[72:75]\n" \
"v_lshl_add_u32 v128, v133, 3, v128\n" \
"v_add_u32 v128, s68, v128\n" \
"v_mfma_f32_16x16x32_bf16 v[76:79], a[152:155], a[0:3], v[76:79]\n" \
"v_lshrrev_b32 v133, 3, v132\n" \
"v_and_b32 v134, 7, v132\n" \
"v_mfma_f32_16x16x32_bf16 v[76:79], a[156:159], a[4:7], v[76:79]\n" \
"v_mul_u32_u24 v129, 144, v133\n" \
"v_lshl_add_u32 v129, v134, 4, v129\n" \
"v_mfma_f32_16x16x32_bf16 v[92:95], a[152:155], a[24:27], v[92:95]\n" \
"v_add_u32 v129, s68, v129\n" \
"v_lshlrev_b32 v134, 4, v134\n" \
"v_mfma_f32_16x16x32_bf16 v[92:95], a[156:159], a[28:31], v[92:95]\n" \
"v_lshl_add_u32 v130, v133, 13, v134\n" \
"v_mfma_f32_16x16x32_bf16 v[88:91], a[144:147], a[24:27], v[88:91]\n" \
"v_mfma_f32_16x16x32_bf16 v[88:91], a[148:151], a[28:31], v[88:91]\n" \
"v_mfma_f32_16x16x32_bf16 v[84:87], a[136:139], a[24:27], v[84:87]\n" \
"s_waitcnt vmcnt(0)\n" \
"s_barrier\n" \
"v_mfma_f32_16x16x32_bf16 v[84:87], a[140:143], a[28:31], v[84:87]\n" \
"ds_read_b128 a[32:35], v224 offset:256\n" \
"ds_read_b128 a[36:39], v224 offset:2304\n" \
"v_mfma_f32_16x16x32_bf16 v[80:83], a[128:131], a[24:27], v[80:83]\n" \
"ds_read_b128 a[16:19], v227 offset:0\n" \
"ds_read_b128 a[20:23], v227 offset:2048\n" \
"v_mfma_f32_16x16x32_bf16 v[80:83], a[132:135], a[28:31], v[80:83]\n" \
"s_waitcnt lgkmcnt(4)\n" \
"ds_read_b64_tr_b16 a[192:193], v228 offset:16384\n" \
"ds_read_b64_tr_b16 a[194:195], v228 offset:18432\n" \
"v_mfma_f32_16x16x32_bf16 v[64:67], a[160:163], a[8:11], v[64:67]\n" \
"ds_read_b64_tr_b16 a[196:197], v228 offset:24576\n" \
"ds_read_b64_tr_b16 a[198:199], v228 offset:26624\n" \
"v_mfma_f32_16x16x32_bf16 v[64:67], a[164:167], a[12:15], v[64:67]\n" \
"ds_read_b64_tr_b16 a[200:201], v229 offset:16384\n" \
"ds_read_b64_tr_b16 a[202:203], v229 offset:18432\n" \
"v_mfma_f32_16x16x32_bf16 v[68:71], a[168:171], a[8:11], v[68:71]\n" \
"ds_read_b64_tr_b16 a[204:205], v229 offset:24576\n" \
"ds_read_b64_tr_b16 a[206:207], v229 offset:26624\n" \
"v_mfma_f32_16x16x32_bf16 v[68:71], a[172:175], a[12:15], v[68:71]\n" \
"ds_read_b64_tr_b16 a[208:209], v228 offset:16896\n" \
"ds_read_b64_tr_b16 a[210:211], v228 offset:18944\n" \
"v_mfma_f32_16x16x32_bf16 v[72:75], a[176:179], a[8:11], v[72:75]\n" \
"ds_read_b64_tr_b16 a[212:213], v228 offset:25088\n" \
"ds_read_b64_tr_b16 a[214:215], v228 offset:27136\n" \
"v_mfma_f32_16x16x32_bf16 v[72:75], a[180:183], a[12:15], v[72:75]\n" \
"ds_read_b64_tr_b16 a[216:217], v229 offset:16896\n" \
"ds_read_b64_tr_b16 a[218:219], v229 offset:18944\n" \
"v_mfma_f32_16x16x32_bf16 v[76:79], a[184:187], a[8:11], v[76:79]\n" \
"ds_read_b64_tr_b16 a[220:221], v229 offset:25088\n" \
"ds_read_b64_tr_b16 a[222:223], v229 offset:27136\n" \
"v_mfma_f32_16x16x32_bf16 v[76:79], a[188:191], a[12:15], v[76:79]\n" \
"s_waitcnt lgkmcnt(8)\n" \
"ds_read_b128 a[40:43], v227 offset:256\n" \
"ds_read_b128 a[44:47], v227 offset:2304\n" \
"v_mfma_f32_16x16x32_bf16 v[92:95], a[184:187], a[32:35], v[92:95]\n" \
"ds_read_b128 a[248:251], v213 offset:4096\n" \
"ds_read_b128 a[252:255], v213 offset:6144\n" \
"v_mfma_f32_16x16x32_bf16 v[92:95], a[188:191], a[36:39], v[92:95]\n" \
"ds_read_b128 a[48:51], v224 offset:4096\n" \
"ds_read_b128 a[52:55], v224 offset:6144\n" \
"v_mfma_f32_16x16x32_bf16 v[88:91], a[176:179], a[32:35], v[88:91]\n" \
"ds_read_b128 a[56:59], v227 offset:4096\n" \
"ds_read_b128 a[60:63], v227 offset:6144\n" \
"v_mfma_f32_16x16x32_bf16 v[88:91], a[180:183], a[36:39], v[88:91]\n" \
"ds_read_b128 a[224:227], v213 offset:4352\n" \
"ds_read_b128 a[228:231], v213 offset:6400\n" \
"v_mfma_f32_16x16x32_bf16 v[84:87], a[168:171], a[32:35], v[84:87]\n" \
"ds_read_b128 a[232:235], v224 offset:4352\n" \
"ds_read_b128 a[236:239], v224 offset:6400\n" \
"v_mfma_f32_16x16x32_bf16 v[84:87], a[172:175], a[36:39], v[84:87]\n" \
"ds_read_b128 a[240:243], v227 offset:4352\n" \
"ds_read_b128 a[244:247], v227 offset:6400\n" \
"v_mfma_f32_16x16x32_bf16 v[80:83], a[160:163], a[32:35], v[80:83]\n" \
"v_mfma_f32_16x16x32_bf16 v[80:83], a[164:167], a[36:39], v[80:83]\n" \
"v_mfma_f32_16x16x32_bf16 v[64:67], a[192:195], a[16:19], v[64:67]\n" \
"v_mfma_f32_16x16x32_bf16 v[64:67], a[196:199], a[20:23], v[64:67]\n" \
"v_mfma_f32_16x16x32_bf16 v[68:71], a[200:203], a[16:19], v[68:71]\n" \
"v_mfma_f32_16x16x32_bf16 v[68:71], a[204:207], a[20:23], v[68:71]\n" \
"s_waitcnt lgkmcnt(0)\n" \
"v_mfma_f32_16x16x32_bf16 v[72:75], a[208:211], a[16:19], v[72:75]\n" \
"v_mfma_f32_16x16x32_bf16 v[72:75], a[212:215], a[20:23], v[72:75]\n" \
"v_cvt_pk_bf16_f32 v136, v64, v65\n" \
"v_cvt_pk_bf16_f32 v137, v66, v67\n" \
"v_mfma_f32_16x16x32_bf16 v[76:79], a[216:219], a[16:19], v[76:79]\n" \
"v_mfma_f32_16x16x32_bf16 v[76:79], a[220:223], a[20:23], v[76:79]\n" \
"ds_write_b64 v128, v[136:137] offset:0\n" \
"v_mfma_f32_16x16x32_bf16 v[80:83], a[192:195], a[40:43], v[80:83]\n" \
"v_cvt_pk_bf16_f32 v138, v68, v69\n" \
"v_cvt_pk_bf16_f32 v139, v70, v71\n" \
"v_mfma_f32_16x16x32_bf16 v[80:83], a[196:199], a[44:47], v[80:83]\n" \
"ds_write_b64 v128, v[138:139] offset:32\n" \
"v_mfma_f32_16x16x32_bf16 v[84:87], a[200:203], a[40:43], v[84:87]\n" \
"v_cvt_pk_bf16_f32 v136, v72, v73\n" \
"v_cvt_pk_bf16_f32 v137, v74, v75\n" \
"v_mfma_f32_16x16x32_bf16 v[84:87], a[204:207], a[44:47], v[84:87]\n" \
"ds_write_b64 v128, v[136:137] offset:64\n" \
"v_mfma_f32_16x16x32_bf16 v[88:91], a[208:211], a[40:43], v[88:91]\n" \
"v_cvt_pk_bf16_f32 v138, v76, v77\n" \
"v_cvt_pk_bf16_f32 v139, v78, v79\n" \
"v_mfma_f32_16x16x32_bf16 v[88:91], a[212:215], a[44:47], v[88:91]\n" \
"ds_write_b64 v128, v[138:139] offset:96\n" \
"v_mfma_f32_16x16x32_bf16 v[92:95], a[216:219], a[40:43], v[92:95]\n" \
"ds_read_b128 v[140:143], v129 offset:0\n" \
"ds_read_b128 v[144:147], v129 offset:1152\n" \
"v_mfma_f32_16x16x32_bf16 v[92:95], a[220:223], a[44:47], v[92:95]\n" \
"v_cvt_pk_bf16_f32 v136, v80, v81\n" \
"v_cvt_pk_bf16_f32 v137, v82, v83\n" \
"v_mfma_f32_16x16x32_bf16 v[96:99], a[128:131], a[248:251], v[96:99]\n" \
"ds_write_b64 v128, v[136:137] offset:2304\n" \
"v_mfma_f32_16x16x32_bf16 v[96:99], a[132:135], a[252:255], v[96:99]\n" \
"s_add_u32 s70, s88, 0\n" \
"s_waitcnt lgkmcnt(2)\n" \
"v_cvt_pk_bf16_f32 v138, v84, v85\n" \
"v_mfma_f32_16x16x32_bf16 v[96:99], a[160:163], a[48:51], v[96:99]\n" \
"buffer_store_dwordx4 v[140:143], v130, s[52:55], s70 offen sc1\n" \
"v_mfma_f32_16x16x32_bf16 v[96:99], a[164:167], a[52:55], v[96:99]\n" \
"v_cvt_pk_bf16_f32 v139, v86, v87\n" \
"v_mfma_f32_16x16x32_bf16 v[96:99], a[192:195], a[56:59], v[96:99]\n" \
"s_add_u32 s70, s88, 65536\n" \
"s_waitcnt lgkmcnt(1)\n" \
"v_mfma_f32_16x16x32_bf16 v[96:99], a[196:199], a[60:63], v[96:99]\n" \
"buffer_store_dwordx4 v[144:147], v130, s[52:55], s70 offen sc1\n" \
"ds_write_b64 v128, v[138:139] offset:2336\n" \
"v_mfma_f32_16x16x32_bf16 v[100:103], a[204:207], a[60:63], v[100:103]\n" \
"v_cvt_pk_bf16_f32 v136, v88, v89\n" \
"v_cvt_pk_bf16_f32 v137, v90, v91\n" \
"v_mfma_f32_16x16x32_bf16 v[100:103], a[200:203], a[56:59], v[100:103]\n" \
"ds_write_b64 v128, v[136:137] offset:2368\n" \
"v_mfma_f32_16x16x32_bf16 v[100:103], a[172:175], a[52:55], v[100:103]\n" \
"v_cvt_pk_bf16_f32 v138, v92, v93\n" \
"v_cvt_pk_bf16_f32 v139, v94, v95\n" \
"v_mfma_f32_16x16x32_bf16 v[100:103], a[168:171], a[48:51], v[100:103]\n" \
"v_mfma_f32_16x16x32_bf16 v[100:103], a[140:143], a[252:255], v[100:103]\n" \
"v_mfma_f32_16x16x32_bf16 v[100:103], a[136:139], a[248:251], v[100:103]\n" \
"ds_write_b64 v128, v[138:139] offset:2400\n" \
"v_mfma_f32_16x16x32_bf16 v[104:107], a[144:147], a[248:251], v[104:107]\n" \
"ds_read_b128 v[148:151], v129 offset:2304\n" \
"ds_read_b128 v[152:155], v129 offset:3456\n" \
"v_mfma_f32_16x16x32_bf16 v[104:107], a[148:151], a[252:255], v[104:107]\n" \
"v_cvt_pk_bf16_f32 v136, v96, v97\n" \
"v_cvt_pk_bf16_f32 v137, v98, v99\n" \
"v_mfma_f32_16x16x32_bf16 v[104:107], a[176:179], a[48:51], v[104:107]\n" \
"ds_write_b64 v128, v[136:137] offset:4608\n" \
"v_mfma_f32_16x16x32_bf16 v[104:107], a[180:183], a[52:55], v[104:107]\n" \
"v_cvt_pk_bf16_f32 v138, v100, v101\n" \
"v_cvt_pk_bf16_f32 v139, v102, v103\n" \
"v_mfma_f32_16x16x32_bf16 v[104:107], a[208:211], a[56:59], v[104:107]\n" \
"s_add_u32 s70, s88, 4096\n" \
"s_waitcnt lgkmcnt(2)\n" \
"v_mfma_f32_16x16x32_bf16 v[104:107], a[212:215], a[60:63], v[104:107]\n" \
"buffer_store_dwordx4 v[148:151], v130, s[52:55], s70 offen sc1\n" \
"ds_write_b64 v128, v[138:139] offset:4640\n" \
"v_mfma_f32_16x16x32_bf16 v[108:111], a[220:223], a[60:63], v[108:111]\n" \
"s_add_u32 s70, s88, 69632\n" \
"s_waitcnt lgkmcnt(2)\n" \
"v_mfma_f32_16x16x32_bf16 v[108:111], a[216:219], a[56:59], v[108:111]\n" \
"buffer_store_dwordx4 v[152:155], v130, s[52:55], s70 offen sc1\n" \
"v_mfma_f32_16x16x32_bf16 v[108:111], a[188:191], a[52:55], v[108:111]\n" \
"v_mfma_f32_16x16x32_bf16 v[108:111], a[184:187], a[48:51], v[108:111]\n" \
"v_cvt_pk_bf16_f32 v136, v104, v105\n" \
"v_cvt_pk_bf16_f32 v137, v106, v107\n" \
"v_mfma_f32_16x16x32_bf16 v[108:111], a[156:159], a[252:255], v[108:111]\n" \
"v_mfma_f32_16x16x32_bf16 v[108:111], a[152:155], a[248:251], v[108:111]\n" \
"ds_write_b64 v128, v[136:137] offset:4672\n" \
"v_mfma_f32_16x16x32_bf16 v[112:115], a[128:131], a[224:227], v[112:115]\n" \
"v_mfma_f32_16x16x32_bf16 v[112:115], a[132:135], a[228:231], v[112:115]\n" \
"v_mfma_f32_16x16x32_bf16 v[112:115], a[160:163], a[232:235], v[112:115]\n" \
"v_mfma_f32_16x16x32_bf16 v[112:115], a[164:167], a[236:239], v[112:115]\n" \
"v_cvt_pk_bf16_f32 v138, v108, v109\n" \
"v_cvt_pk_bf16_f32 v139, v110, v111\n" \
"v_mfma_f32_16x16x32_bf16 v[112:115], a[192:195], a[240:243], v[112:115]\n" \
"v_mfma_f32_16x16x32_bf16 v[112:115], a[196:199], a[244:247], v[112:115]\n" \
"ds_write_b64 v128, v[138:139] offset:4704\n" \
"v_mfma_f32_16x16x32_bf16 v[116:119], a[204:207], a[244:247], v[116:119]\n" \
"ds_read_b128 v[156:159], v129 offset:4608\n" \
"ds_read_b128 v[160:163], v129 offset:5760\n" \
"v_mfma_f32_16x16x32_bf16 v[116:119], a[200:203], a[240:243], v[116:119]\n" \
"v_mfma_f32_16x16x32_bf16 v[116:119], a[172:175], a[236:239], v[116:119]\n" \
"v_mfma_f32_16x16x32_bf16 v[116:119], a[168:171], a[232:235], v[116:119]\n" \
"v_cvt_pk_bf16_f32 v136, v112, v113\n" \
"v_cvt_pk_bf16_f32 v137, v114, v115\n" \
"v_mfma_f32_16x16x32_bf16 v[116:119], a[140:143], a[228:231], v[116:119]\n" \
"s_add_u32 s70, s88, 131072\n" \
"s_waitcnt lgkmcnt(1)\n" \
"v_mfma_f32_16x16x32_bf16 v[116:119], a[136:139], a[224:227], v[116:119]\n" \
"buffer_store_dwordx4 v[156:159], v130, s[52:55], s70 offen sc1\n" \
"ds_write_b64 v128, v[136:137] offset:6912\n" \
"v_mfma_f32_16x16x32_bf16 v[120:123], a[144:147], a[224:227], v[120:123]\n" \
"s_add_u32 s70, s88, 196608\n" \
"s_waitcnt lgkmcnt(1)\n" \
"v_mfma_f32_16x16x32_bf16 v[120:123], a[148:151], a[228:231], v[120:123]\n" \
"buffer_store_dwordx4 v[160:163], v130, s[52:55], s70 offen sc1\n" \
"v_mfma_f32_16x16x32_bf16 v[120:123], a[176:179], a[232:235], v[120:123]\n" \
"v_mfma_f32_16x16x32_bf16 v[120:123], a[180:183], a[236:239], v[120:123]\n" \
"v_cvt_pk_bf16_f32 v138, v116, v117\n" \
"v_cvt_pk_bf16_f32 v139, v118, v119\n" \
"v_mfma_f32_16x16x32_bf16 v[120:123], a[208:211], a[240:243], v[120:123]\n" \
"v_mfma_f32_16x16x32_bf16 v[120:123], a[212:215], a[244:247], v[120:123]\n" \
"ds_write_b64 v128, v[138:139] offset:6944\n" \
"v_mfma_f32_16x16x32_bf16 v[124:127], a[220:223], a[244:247], v[124:127]\n" \
"v_mfma_f32_16x16x32_bf16 v[124:127], a[216:219], a[240:243], v[124:127]\n" \
"v_mfma_f32_16x16x32_bf16 v[124:127], a[188:191], a[236:239], v[124:127]\n" \
"v_mfma_f32_16x16x32_bf16 v[124:127], a[184:187], a[232:235], v[124:127]\n" \
"v_cvt_pk_bf16_f32 v136, v120, v121\n" \
"v_cvt_pk_bf16_f32 v137, v122, v123\n" \
"v_mfma_f32_16x16x32_bf16 v[124:127], a[156:159], a[228:231], v[124:127]\n" \
"v_mfma_f32_16x16x32_bf16 v[124:127], a[152:155], a[224:227], v[124:127]\n" \
"ds_write_b64 v128, v[136:137] offset:6976\n" \
"s_nop 7\n" \
"v_cvt_pk_bf16_f32 v138, v124, v125\n" \
"v_cvt_pk_bf16_f32 v139, v126, v127\n" \
"ds_write_b64 v128, v[138:139] offset:7008\n" \
"ds_read_b128 v[164:167], v129 offset:6912\n" \
"ds_read_b128 v[168:171], v129 offset:8064\n" \
"s_add_u32 s70, s88, 135168\n" \
"s_waitcnt lgkmcnt(1)\n" \
"buffer_store_dwordx4 v[164:167], v130, s[52:55], s70 offen sc1\n" \
"s_add_u32 s70, s88, 200704\n" \
"s_waitcnt lgkmcnt(0)\n" \
"buffer_store_dwordx4 v[168:171], v130, s[52:55], s70 offen sc1\n" \
"s_endpgm\n" \
"Lhot_end_%=:\n" \
"Lpl1_%=:\n" \
"s_mov_b32 s72, 0\n" \
"Lpk1_%=:\n" \
"s_sleep 2\n" \
"buffer_load_dword v227, v242, s[56:59], s90 offen sc1\n" \
"s_waitcnt vmcnt(0)\n" \
"v_cmp_eq_u32 vcc, s60, v227\n" \
"s_nop 1\n" \
"s_andn2_b32 s70, 0xff, vcc_lo\n" \
"s_cbranch_scc0 Lpr1_%=\n" \
"s_add_u32 s72, s72, 1\n" \
"s_cmp_lt_u32 s72, 0x40000\n" \
"s_cbranch_scc1 Lpk1_%=\n" \
"s_branch Lpr1_%=\n" \
"Lpl2_%=:\n" \
"s_mov_b32 s72, 0\n" \
"Lpk2_%=:\n" \
"s_sleep 2\n" \
"buffer_load_dword v239, v242, s[56:59], s95 offen sc1\n" \
"s_waitcnt vmcnt(0)\n" \
"v_cmp_eq_u32 vcc, s60, v239\n" \
"s_nop 1\n" \
"s_andn2_b32 s70, s69, vcc_lo\n" \
"s_cbranch_scc0 Lpr2_%=\n" \
"s_add_u32 s72, s72, 1\n" \
"s_cmp_lt_u32 s72, 0x40000\n" \
"s_cbranch_scc1 Lpk2_%=\n" \
"s_branch Lpr2_%=\n" \
"Lpl3_%=:\n" \
"s_mov_b32 s72, 0\n" \
"Lpk3_%=:\n" \
"s_sleep 2\n" \
"buffer_load_dword v239, v242, s[56:59], s95 offen sc1\n" \
"s_waitcnt vmcnt(0)\n" \
"v_cmp_eq_u32 vcc, s60, v239\n" \
"s_nop 1\n" \
"s_andn2_b32 s70, s69, vcc_lo\n" \
"s_cbranch_scc0 Lpr3_%=\n" \
"s_add_u32 s72, s72, 1\n" \
"s_cmp_lt_u32 s72, 0x40000\n" \
"s_cbranch_scc1 Lpk3_%=\n" \
"s_branch Lpr3_%=\n" \
"Lexact_%=:\n" \
"v_mov_b32 v239, 0xff800000\n" \
"s_and_b32 s70, s61, 1\n" \
"s_lshl_b32 s70, s70, 4\n" \
"v_add_u32 v186, s70, v238\n" \
"v_accvgpr_read_b32 v160, a128\n" \
"v_accvgpr_read_b32 v161, a129\n" \
"v_accvgpr_read_b32 v162, a130\n" \
"v_accvgpr_read_b32 v163, a131\n" \
"v_accvgpr_read_b32 v164, a132\n" \
"v_accvgpr_read_b32 v165, a133\n" \
"v_accvgpr_read_b32 v166, a134\n" \
"v_accvgpr_read_b32 v167, a135\n" \
"v_accvgpr_read_b32 v168, a136\n" \
"v_accvgpr_read_b32 v169, a137\n" \
"v_accvgpr_read_b32 v170, a138\n" \
"v_accvgpr_read_b32 v171, a139\n" \
"v_accvgpr_read_b32 v172, a140\n" \
"v_accvgpr_read_b32 v173, a141\n" \
"v_accvgpr_read_b32 v174, a142\n" \
"v_accvgpr_read_b32 v175, a143\n" \
"s_add_u32 s83, s85, 0\n" \
"s_add_u32 s84, s87, 0\n" \
"s_call_b64 s[64:65], Lex1_%=\n" \
"v_accvgpr_read_b32 v160, a144\n" \
"v_accvgpr_read_b32 v161, a145\n" \
"v_accvgpr_read_b32 v162, a146\n" \
"v_accvgpr_read_b32 v163, a147\n" \
"v_accvgpr_read_b32 v164, a148\n" \
"v_accvgpr_read_b32 v165, a149\n" \
"v_accvgpr_read_b32 v166, a150\n" \
"v_accvgpr_read_b32 v167, a151\n" \
"v_accvgpr_read_b32 v168, a152\n" \
"v_accvgpr_read_b32 v169, a153\n" \
"v_accvgpr_read_b32 v170, a154\n" \
"v_accvgpr_read_b32 v171, a155\n" \
"v_accvgpr_read_b32 v172, a156\n" \
"v_accvgpr_read_b32 v173, a157\n" \
"v_accvgpr_read_b32 v174, a158\n" \
"v_accvgpr_read_b32 v175, a159\n" \
"s_add_u32 s83, s85, 2\n" \
"s_add_u32 s84, s87, 532480\n" \
"s_call_b64 s[64:65], Lex1_%=\n" \
"v_accvgpr_read_b32 v160, a160\n" \
"v_accvgpr_read_b32 v161, a161\n" \
"v_accvgpr_read_b32 v162, a162\n" \
"v_accvgpr_read_b32 v163, a163\n" \
"v_accvgpr_read_b32 v164, a164\n" \
"v_accvgpr_read_b32 v165, a165\n" \
"v_accvgpr_read_b32 v166, a166\n" \
"v_accvgpr_read_b32 v167, a167\n" \
"v_accvgpr_read_b32 v168, a168\n" \
"v_accvgpr_read_b32 v169, a169\n" \
"v_accvgpr_read_b32 v170, a170\n" \
"v_accvgpr_read_b32 v171, a171\n" \
"v_accvgpr_read_b32 v172, a172\n" \
"v_accvgpr_read_b32 v173, a173\n" \
"v_accvgpr_read_b32 v174, a174\n" \
"v_accvgpr_read_b32 v175, a175\n" \
"s_add_u32 s83, s86, 0\n" \
"s_add_u32 s84, s88, 0\n" \
"s_call_b64 s[64:65], Lex1_%=\n" \
"v_accvgpr_read_b32 v160, a176\n" \
"v_accvgpr_read_b32 v161, a177\n" \
"v_accvgpr_read_b32 v162, a178\n" \
"v_accvgpr_read_b32 v163, a179\n" \
"v_accvgpr_read_b32 v164, a180\n" \
"v_accvgpr_read_b32 v165, a181\n" \
"v_accvgpr_read_b32 v166, a182\n" \
"v_accvgpr_read_b32 v167, a183\n" \
"v_accvgpr_read_b32 v168, a184\n" \
"v_accvgpr_read_b32 v169, a185\n" \
"v_accvgpr_read_b32 v170, a186\n" \
"v_accvgpr_read_b32 v171, a187\n" \
"v_accvgpr_read_b32 v172, a188\n" \
"v_accvgpr_read_b32 v173, a189\n" \
"v_accvgpr_read_b32 v174, a190\n" \
"v_accvgpr_read_b32 v175, a191\n" \
"s_add_u32 s83, s86, 2\n" \
"s_add_u32 s84, s88, 532480\n" \
"s_call_b64 s[64:65], Lex1_%=\n" \
"s_waitcnt vmcnt(0)\n" \
"s_branch Lexr_%=\n" \
"Lex1_%=:\n" \
"v_mov_b32 v128, 0\n" \
"v_mov_b32 v129, 0\n" \
"v_mov_b32 v130, 0\n" \
"v_mov_b32 v131, 0\n" \
"v_mov_b32 v132, 0\n" \
"v_mov_b32 v133, 0\n" \
"v_mov_b32 v134, 0\n" \
"v_mov_b32 v135, 0\n" \
"v_mov_b32 v136, 0\n" \
"v_mov_b32 v137, 0\n" \
"v_mov_b32 v138, 0\n" \
"v_mov_b32 v139, 0\n" \
"v_mov_b32 v140, 0\n" \
"v_mov_b32 v141, 0\n" \
"v_mov_b32 v142, 0\n" \
"v_mov_b32 v143, 0\n" \
"v_mov_b32 v144, 0\n" \
"v_mov_b32 v145, 0\n" \
"v_mov_b32 v146, 0\n" \
"v_mov_b32 v147, 0\n" \
"v_mov_b32 v148, 0\n" \
"v_mov_b32 v149, 0\n" \
"v_mov_b32 v150, 0\n" \
"v_mov_b32 v151, 0\n" \
"v_mov_b32 v152, 0\n" \
"v_mov_b32 v153, 0\n" \
"v_mov_b32 v154, 0\n" \
"v_mov_b32 v155, 0\n" \
"v_mov_b32 v156, 0\n" \
"v_mov_b32 v157, 0\n" \
"v_mov_b32 v158, 0\n" \
"v_mov_b32 v159, 0\n" \
"v_mov_b32 v184, 0xf149f2ca\n" \
"v_mov_b32 v185, 0\n" \
"s_mov_b32 s81, 0\n" \
"s_mul_i32 s82, s62, 557824\n" \
"Lex2_%=:\n" \
"s_add_u32 s70, s82, 4096\n" \
"buffer_load_dwordx4 a[64:67], v236, s[56:59], s82 offen offset:0 sc1\n" \
"buffer_load_dwordx4 a[68:71], v236, s[56:59], s82 offen offset:2048 sc1\n" \
"buffer_load_dwordx4 a[72:75], v236, s[56:59], s70 offen offset:0 sc1\n" \
"buffer_load_dwordx4 a[76:79], v236, s[56:59], s70 offen offset:2048 sc1\n" \
"buffer_load_dwordx4 a[80:83], v236, s[56:59], s82 offen offset:256 sc1\n" \
"buffer_load_dwordx4 a[84:87], v236, s[56:59], s82 offen offset:2304 sc1\n" \
"buffer_load_dwordx4 a[88:91], v236, s[56:59], s70 offen offset:256 sc1\n" \
"buffer_load_dwordx4 a[92:95], v236, s[56:59], s70 offen offset:2304 sc1\n" \
"s_add_u32 s70, s82, 17850368\n" \
"s_add_u32 s71, s70, 4096\n" \
"buffer_load_dwordx4 a[96:99], v234, s[56:59], s70 offen offset:0 sc1\n" \
"buffer_load_dwordx4 a[100:103], v234, s[56:59], s70 offen offset:1024 sc1\n" \
"buffer_load_dwordx4 a[104:107], v234, s[56:59], s70 offen offset:2048 sc1\n" \
"buffer_load_dwordx4 a[108:111], v234, s[56:59], s70 offen offset:3072 sc1\n" \
"buffer_load_dwordx4 a[112:115], v234, s[56:59], s71 offen offset:0 sc1\n" \
"buffer_load_dwordx4 a[116:119], v234, s[56:59], s71 offen offset:1024 sc1\n" \
"buffer_load_dwordx4 a[120:123], v234, s[56:59], s71 offen offset:2048 sc1\n" \
"buffer_load_dwordx4 a[124:127], v234, s[56:59], s71 offen offset:3072 sc1\n" \
"s_waitcnt vmcnt(0)\n" \
"v_mfma_f32_16x16x32_bf16 v[176:179], a[64:67], v[160:163], 0\n" \
"v_mfma_f32_16x16x32_bf16 v[176:179], a[68:71], v[164:167], v[176:179]\n" \
"v_mfma_f32_16x16x32_bf16 v[176:179], a[72:75], v[168:171], v[176:179]\n" \
"v_mfma_f32_16x16x32_bf16 v[176:179], a[76:79], v[172:175], v[176:179]\n" \
"v_mfma_f32_16x16x32_bf16 v[180:183], a[80:83], v[160:163], 0\n" \
"v_mfma_f32_16x16x32_bf16 v[180:183], a[84:87], v[164:167], v[180:183]\n" \
"v_mfma_f32_16x16x32_bf16 v[180:183], a[88:91], v[168:171], v[180:183]\n" \
"v_mfma_f32_16x16x32_bf16 v[180:183], a[92:95], v[172:175], v[180:183]\n" \
"s_nop 7\n" \
"s_nop 1\n" \
"s_cmp_eq_u32 s81, s83\n" \
"s_cbranch_scc0 Lex3_%=\n" \
"v_cmp_gt_i32 vcc, 0, v186\n" \
"s_nop 1\n" \
"v_cndmask_b32 v176, v176, v239, vcc\n" \
"v_cmp_gt_i32 vcc, 1, v186\n" \
"s_nop 1\n" \
"v_cndmask_b32 v177, v177, v239, vcc\n" \
"v_cmp_gt_i32 vcc, 2, v186\n" \
"s_nop 1\n" \
"v_cndmask_b32 v178, v178, v239, vcc\n" \
"v_cmp_gt_i32 vcc, 3, v186\n" \
"s_nop 1\n" \
"v_cndmask_b32 v179, v179, v239, vcc\n" \
"v_cmp_gt_i32 vcc, 16, v186\n" \
"s_nop 1\n" \
"v_cndmask_b32 v180, v180, v239, vcc\n" \
"v_cmp_gt_i32 vcc, 17, v186\n" \
"s_nop 1\n" \
"v_cndmask_b32 v181, v181, v239, vcc\n" \
"v_cmp_gt_i32 vcc, 18, v186\n" \
"s_nop 1\n" \
"v_cndmask_b32 v182, v182, v239, vcc\n" \
"v_cmp_gt_i32 vcc, 19, v186\n" \
"s_nop 1\n" \
"v_cndmask_b32 v183, v183, v239, vcc\n" \
"Lex3_%=:\n" \
"v_max3_f32 v187, v176, v177, v178\n" \
"v_max3_f32 v187, v187, v179, v180\n" \
"v_max3_f32 v187, v187, v181, v182\n" \
"v_max3_f32 v187, v187, v183, v184\n" \
"v_mov_b32 v188, v187\n" \
"s_nop 1\n" \
"v_permlane16_swap_b32 v188, v187\n" \
"v_max_f32 v187, v187, v188\n" \
"v_mov_b32 v188, v187\n" \
"s_nop 1\n" \
"v_permlane32_swap_b32 v188, v187\n" \
"v_max_f32 v187, v187, v188\n" \
"v_sub_f32 v189, v184, v187\n" \
"v_exp_f32 v189, v189\n" \
"v_mov_b32 v184, v187\n" \
"v_sub_f32 v176, v176, v187\n" \
"v_sub_f32 v177, v177, v187\n" \
"v_sub_f32 v178, v178, v187\n" \
"v_sub_f32 v179, v179, v187\n" \
"v_sub_f32 v180, v180, v187\n" \
"v_sub_f32 v181, v181, v187\n" \
"v_sub_f32 v182, v182, v187\n" \
"v_sub_f32 v183, v183, v187\n" \
"v_exp_f32 v176, v176\n" \
"v_exp_f32 v177, v177\n" \
"v_exp_f32 v178, v178\n" \
"v_exp_f32 v179, v179\n" \
"v_exp_f32 v180, v180\n" \
"v_exp_f32 v181, v181\n" \
"v_exp_f32 v182, v182\n" \
"v_exp_f32 v183, v183\n" \
"v_mul_f32 v185, v185, v189\n" \
"v_add_f32 v185, v185, v176\n" \
"v_add_f32 v185, v185, v177\n" \
"v_add_f32 v185, v185, v178\n" \
"v_add_f32 v185, v185, v179\n" \
"v_add_f32 v185, v185, v180\n" \
"v_add_f32 v185, v185, v181\n" \
"v_add_f32 v185, v185, v182\n" \
"v_add_f32 v185, v185, v183\n" \
"v_cvt_pk_bf16_f32 v112, v176, v177\n" \
"v_cvt_pk_bf16_f32 v113, v178, v179\n" \
"v_cvt_pk_bf16_f32 v114, v180, v181\n" \
"v_cvt_pk_bf16_f32 v115, v182, v183\n" \
"v_mul_f32 v128, v128, v189\n" \
"v_mul_f32 v129, v129, v189\n" \
"v_mul_f32 v130, v130, v189\n" \
"v_mul_f32 v131, v131, v189\n" \
"v_mul_f32 v132, v132, v189\n" \
"v_mul_f32 v133, v133, v189\n" \
"v_mul_f32 v134, v134, v189\n" \
"v_mul_f32 v135, v135, v189\n" \
"v_mul_f32 v136, v136, v189\n" \
"v_mul_f32 v137, v137, v189\n" \
"v_mul_f32 v138, v138, v189\n" \
"v_mul_f32 v139, v139, v189\n" \
"v_mul_f32 v140, v140, v189\n" \
"v_mul_f32 v141, v141, v189\n" \
"v_mul_f32 v142, v142, v189\n" \
"v_mul_f32 v143, v143, v189\n" \
"v_mul_f32 v144, v144, v189\n" \
"v_mul_f32 v145, v145, v189\n" \
"v_mul_f32 v146, v146, v189\n" \
"v_mul_f32 v147, v147, v189\n" \
"v_mul_f32 v148, v148, v189\n" \
"v_mul_f32 v149, v149, v189\n" \
"v_mul_f32 v150, v150, v189\n" \
"v_mul_f32 v151, v151, v189\n" \
"v_mul_f32 v152, v152, v189\n" \
"v_mul_f32 v153, v153, v189\n" \
"v_mul_f32 v154, v154, v189\n" \
"v_mul_f32 v155, v155, v189\n" \
"v_mul_f32 v156, v156, v189\n" \
"v_mul_f32 v157, v157, v189\n" \
"v_mul_f32 v158, v158, v189\n" \
"v_mul_f32 v159, v159, v189\n" \
"s_nop 1\n" \
"v_mfma_f32_16x16x32_bf16 v[128:131], a[96:99], v[112:115], v[128:131]\n" \
"v_mfma_f32_16x16x32_bf16 v[132:135], a[100:103], v[112:115], v[132:135]\n" \
"v_mfma_f32_16x16x32_bf16 v[136:139], a[104:107], v[112:115], v[136:139]\n" \
"v_mfma_f32_16x16x32_bf16 v[140:143], a[108:111], v[112:115], v[140:143]\n" \
"v_mfma_f32_16x16x32_bf16 v[144:147], a[112:115], v[112:115], v[144:147]\n" \
"v_mfma_f32_16x16x32_bf16 v[148:151], a[116:119], v[112:115], v[148:151]\n" \
"v_mfma_f32_16x16x32_bf16 v[152:155], a[120:123], v[112:115], v[152:155]\n" \
"v_mfma_f32_16x16x32_bf16 v[156:159], a[124:127], v[112:115], v[156:159]\n" \
"s_nop 7\n" \
"s_nop 1\n" \
"s_and_b32 s70, s81, 1\n" \
"s_mul_i32 s70, s70, 1024\n" \
"s_add_u32 s82, s82, s70\n" \
"s_add_u32 s82, s82, 8192\n" \
"s_add_u32 s81, s81, 1\n" \
"s_cmp_le_u32 s81, s83\n" \
"s_cbranch_scc1 Lex2_%=\n" \
"v_mov_b32 v187, v185\n" \
"s_nop 1\n" \
"v_permlane16_swap_b32 v187, v185\n" \
"v_add_f32 v185, v185, v187\n" \
"v_mov_b32 v187, v185\n" \
"s_nop 1\n" \
"v_permlane32_swap_b32 v187, v185\n" \
"v_add_f32 v185, v185, v187\n" \
"v_rcp_f32 v185, v185\n" \
"s_nop 0\n" \
"v_mul_f32 v128, v128, v185\n" \
"v_mul_f32 v129, v129, v185\n" \
"v_mul_f32 v130, v130, v185\n" \
"v_mul_f32 v131, v131, v185\n" \
"v_mul_f32 v132, v132, v185\n" \
"v_mul_f32 v133, v133, v185\n" \
"v_mul_f32 v134, v134, v185\n" \
"v_mul_f32 v135, v135, v185\n" \
"v_mul_f32 v136, v136, v185\n" \
"v_mul_f32 v137, v137, v185\n" \
"v_mul_f32 v138, v138, v185\n" \
"v_mul_f32 v139, v139, v185\n" \
"v_mul_f32 v140, v140, v185\n" \
"v_mul_f32 v141, v141, v185\n" \
"v_mul_f32 v142, v142, v185\n" \
"v_mul_f32 v143, v143, v185\n" \
"v_mul_f32 v144, v144, v185\n" \
"v_mul_f32 v145, v145, v185\n" \
"v_mul_f32 v146, v146, v185\n" \
"v_mul_f32 v147, v147, v185\n" \
"v_mul_f32 v148, v148, v185\n" \
"v_mul_f32 v149, v149, v185\n" \
"v_mul_f32 v150, v150, v185\n" \
"v_mul_f32 v151, v151, v185\n" \
"v_mul_f32 v152, v152, v185\n" \
"v_mul_f32 v153, v153, v185\n" \
"v_mul_f32 v154, v154, v185\n" \
"v_mul_f32 v155, v155, v185\n" \
"v_mul_f32 v156, v156, v185\n" \
"v_mul_f32 v157, v157, v185\n" \
"v_mul_f32 v158, v158, v185\n" \
"v_mul_f32 v159, v159, v185\n" \
"s_add_u32 s70, s84, 4096\n" \
"v_cvt_pk_bf16_f32 v112, v128, v129\n" \
"v_cvt_pk_bf16_f32 v113, v130, v131\n" \
"v_cvt_pk_bf16_f32 v114, v132, v133\n" \
"v_cvt_pk_bf16_f32 v115, v134, v135\n" \
"buffer_store_dwordx4 v[112:115], v236, s[56:59], s84 offen offset:0 sc1\n" \
"v_cvt_pk_bf16_f32 v116, v136, v137\n" \
"v_cvt_pk_bf16_f32 v117, v138, v139\n" \
"v_cvt_pk_bf16_f32 v118, v140, v141\n" \
"v_cvt_pk_bf16_f32 v119, v142, v143\n" \
"buffer_store_dwordx4 v[116:119], v236, s[56:59], s84 offen offset:2048 sc1\n" \
"v_cvt_pk_bf16_f32 v120, v144, v145\n" \
"v_cvt_pk_bf16_f32 v121, v146, v147\n" \
"v_cvt_pk_bf16_f32 v122, v148, v149\n" \
"v_cvt_pk_bf16_f32 v123, v150, v151\n" \
"buffer_store_dwordx4 v[120:123], v236, s[56:59], s70 offen offset:0 sc1\n" \
"v_cvt_pk_bf16_f32 v124, v152, v153\n" \
"v_cvt_pk_bf16_f32 v125, v154, v155\n" \
"v_cvt_pk_bf16_f32 v126, v156, v157\n" \
"v_cvt_pk_bf16_f32 v127, v158, v159\n" \
"buffer_store_dwordx4 v[124:127], v236, s[56:59], s70 offen offset:2048 sc1\n" \
"s_waitcnt vmcnt(0)\n" \
"s_setpc_b64 s[64:65]\n" \
"Lpl4_%=:\n" \
"s_mov_b32 s72, 0\n" \
"Lpk4_%=:\n" \
"s_sleep 2\n" \
"buffer_load_dwordx2 v[226:227], v228, s[56:59], s13 offen sc1\n" \
"s_waitcnt vmcnt(0)\n" \
"v_cmp_eq_u32 vcc, s60, v226\n" \
"s_nop 1\n" \
"s_andn2_b32 s70, 0xff, vcc_lo\n" \
"s_cbranch_scc0 Lpr4_%=\n" \
"s_add_u32 s72, s72, 1\n" \
"s_cmp_lt_u32 s72, 0x40000\n" \
"s_cbranch_scc1 Lpk4_%=\n" \
"s_branch Lpr4_%=\n" \
"Lpl5_%=:\n" \
"s_mov_b32 s72, 0\n" \
"Lpk5_%=:\n" \
"s_sleep 2\n" \
"buffer_load_dword v220, v221, s[56:59], s66 offen sc1\n" \
"s_waitcnt vmcnt(0)\n" \
"v_cmp_eq_u32 vcc, s60, v220\n" \
"s_nop 1\n" \
"s_andn2_b32 s70, s65, vcc_lo\n" \
"s_cbranch_scc0 Lpr5_%=\n" \
"s_add_u32 s72, s72, 1\n" \
"s_cmp_lt_u32 s72, 0x40000\n" \
"s_cbranch_scc1 Lpk5_%=\n" \
"s_branch Lpr5_%=\n" \
"Lpl6_%=:\n" \
"s_mov_b32 s72, 0\n" \
"Lpk6_%=:\n" \
"s_sleep 2\n" \
"buffer_load_dword v220, v221, s[56:59], s67 offen sc1\n" \
"s_waitcnt vmcnt(0)\n" \
"v_cmp_eq_u32 vcc, s60, v220\n" \
"s_nop 1\n" \
"s_andn2_b32 s70, s74, vcc_lo\n" \
"s_cbranch_scc0 Lpr6_%=\n" \
"s_add_u32 s72, s72, 1\n" \
"s_cmp_lt_u32 s72, 0x40000\n" \
"s_cbranch_scc1 Lpk6_%=\n" \
"s_branch Lpr6_%=\n" \
"Lpl7_%=:\n" \
"s_mov_b32 s72, 0\n" \
"Lpk7_%=:\n" \
"s_sleep 2\n" \
"buffer_load_dword v220, v241, s[56:59], s64 offen sc1\n" \
"s_waitcnt vmcnt(0)\n" \
"v_cmp_eq_u32 vcc, s60, v220\n" \
"s_nop 1\n" \
"s_andn2_b32 s70, s69, vcc_lo\n" \
"s_cbranch_scc0 Lpr7_%=\n" \
"s_add_u32 s72, s72, 1\n" \
"s_cmp_lt_u32 s72, 0x40000\n" \
"s_cbranch_scc1 Lpk7_%=\n" \
"s_branch Lpr7_%=\n" \
"Lcode_end_%=:\n" \
".p2align 12\n" \
""
#define MK_ASM_CLOBBERS "v1", "v2", "v3", "v4", "v5", "v6", "v7", "v8", "v9", "v10", "v11", "v12", "v13", "v14", "v15", "v16", "v17", "v18", "v19", "v20", "v21", "v22", "v23", "v24", "v25", "v26", "v27", "v28", "v29", "v30", "v31", "v32", "v33", "v34", "v35", "v36", "v37", "v38", "v39", "v40", "v41", "v42", "v43", "v44", "v45", "v46", "v47", "v48", "v49", "v50", "v51", "v52", "v53", "v54", "v55", "v56", "v57", "v58", "v59", "v60", "v61", "v62", "v63", "v64", "v65", "v66", "v67", "v68", "v69", "v70", "v71", "v72", "v73", "v74", "v75", "v76", "v77", "v78", "v79", "v80", "v81", "v82", "v83", "v84", "v85", "v86", "v87", "v88", "v89", "v90", "v91", "v92", "v93", "v94", "v95", "v96", "v97", "v98", "v99", "v100", "v101", "v102", "v103", "v104", "v105", "v106", "v107", "v108", "v109", "v110", "v111", "v112", "v113", "v114", "v115", "v116", "v117", "v118", "v119", "v120", "v121", "v122", "v123", "v124", "v125", "v126", "v127", "v128", "v129", "v130", "v131", "v132", "v133", "v134", "v135", "v136", "v137", "v138", "v139", "v140", "v141", "v142", "v143", "v144", "v145", "v146", "v147", "v148", "v149", "v150", "v151", "v152", "v153", "v154", "v155", "v156", "v157", "v158", "v159", "v160", "v161", "v162", "v163", "v164", "v165", "v166", "v167", "v168", "v169", "v170", "v171", "v172", "v173", "v174", "v175", "v176", "v177", "v178", "v179", "v180", "v181", "v182", "v183", "v184", "v185", "v186", "v187", "v188", "v189", "v190", "v191", "v192", "v193", "v194", "v195", "v196", "v197", "v198", "v199", "v200", "v201", "v202", "v203", "v204", "v205", "v206", "v207", "v208", "v209", "v210", "v211", "v212", "v213", "v214", "v215", "v216", "v217", "v218", "v219", "v220", "v221", "v222", "v223", "v224", "v225", "v226", "v227", "v228", "v229", "v230", "v231", "v232", "v233", "v234", "v235", "v236", "v237", "v238", "v239", "v240", "v241", "v242", "v243", "v244", "v245", "v246", "v247", "v248", "v249", "v250", "v251", "v252", "v253", "v254", "v255", "a0", "a1", "a2", "a3", "a4", "a5", "a6", "a7", "a8", "a9", "a10", "a11", "a12", "a13", "a14", "a15", "a16", "a17", "a18", "a19", "a20", "a21", "a22", "a23", "a24", "a25", "a26", "a27", "a28", "a29", "a30", "a31", "a32", "a33", "a34", "a35", "a36", "a37", "a38", "a39", "a40", "a41", "a42", "a43", "a44", "a45", "a46", "a47", "a48", "a49", "a50", "a51", "a52", "a53", "a54", "a55", "a56", "a57", "a58", "a59", "a60", "a61", "a62", "a63", "a64", "a65", "a66", "a67", "a68", "a69", "a70", "a71", "a72", "a73", "a74", "a75", "a76", "a77", "a78", "a79", "a80", "a81", "a82", "a83", "a84", "a85", "a86", "a87", "a88", "a89", "a90", "a91", "a92", "a93", "a94", "a95", "a96", "a97", "a98", "a99", "a100", "a101", "a102", "a103", "a104", "a105", "a106", "a107", "a108", "a109", "a110", "a111", "a112", "a113", "a114", "a115", "a116", "a117", "a118", "a119", "a120", "a121", "a122", "a123", "a124", "a125", "a126", "a127", "a128", "a129", "a130", "a131", "a132", "a133", "a134", "a135", "a136", "a137", "a138", "a139", "a140", "a141", "a142", "a143", "a144", "a145", "a146", "a147", "a148", "a149", "a150", "a151", "a152", "a153", "a154", "a155", "a156", "a157", "a158", "a159", "a160", "a161", "a162", "a163", "a164", "a165", "a166", "a167", "a168", "a169", "a170", "a171", "a172", "a173", "a174", "a175", "a176", "a177", "a178", "a179", "a180", "a181", "a182", "a183", "a184", "a185", "a186", "a187", "a188", "a189", "a190", "a191", "a192", "a193", "a194", "a195", "a196", "a197", "a198", "a199", "a200", "a201", "a202", "a203", "a204", "a205", "a206", "a207", "a208", "a209", "a210", "a211", "a212", "a213", "a214", "a215", "a216", "a217", "a218", "a219", "a220", "a221", "a222", "a223", "a224", "a225", "a226", "a227", "a228", "a229", "a230", "a231", "a232", "a233", "a234", "a235", "a236", "a237", "a238", "a239", "a240", "a241", "a242", "a243", "a244", "a245", "a246", "a247", "a248", "a249", "a250", "a251", "a252", "a253", "a254", "a255", "s8", "s9", "s10", "s11", "s12", "s13", "s14", "s15", "s16", "s17", "s18", "s19", "s20", "s21", "s22", "s23", "s24", "s25", "s26", "s27", "s28", "s29", "s30", "s31", "s36", "s37", "s38", "s39", "s40", "s41", "s42", "s43", "s44", "s45", "s46", "s47", "s48", "s49", "s50", "s51", "s52", "s53", "s54", "s55", "s56", "s57", "s58", "s59", "s60", "s61", "s62", "s63", "s64", "s65", "s66", "s67", "s68", "s69", "s70", "s71", "s72", "s73", "s74", "s75", "s76", "s77", "s78", "s79", "s80", "s81", "s82", "s83", "s84", "s85", "s86", "s87", "s88", "s89", "s90", "s91", "s92", "s93", "s94", "s95", "vcc", "scc", "memory"
namespace mk {
constexpr int LDS_BYTES = 163840;
struct Args { const void* in[9]; void* out; void* ws; };
__global__ __launch_bounds__(256, 1) void mega_k(const Args a) {
    extern __shared__ __attribute__((aligned(16))) unsigned char shm[];
    const unsigned ldsb = (unsigned)(size_t)(LAS unsigned char*)shm;
    asm volatile(MK_ASM_BODY
                 :
                 : "s"(__builtin_amdgcn_kernarg_segment_ptr()), "s"(blockIdx.x), "v"(threadIdx.x), "s"(ldsb)
                 : MK_ASM_CLOBBERS);
}
}
extern "C" void kernel_launch(void* const* d_in, const int* in_sizes, int n_in, void* d_out, int out_size, void* d_ws, size_t ws_size, hipStream_t stream) {
    (void)hipFuncSetAttribute((const void*)mk::mega_k, hipFuncAttributeMaxDynamicSharedMemorySize, mk::LDS_BYTES);
    mk::Args a;
    for (int i = 0; i < 9; ++i) a.in[i] = d_in[i];
    a.out = d_out; a.ws = (char*)d_ws + ((ws_size - WS_END) & ~(size_t)0x1fffff);
    mk::mega_k<<<256, 256, mk::LDS_BYTES, stream>>>(a);
}
```

```cpp
#include <hip/hip_runtime.h>
#include <stdint.h>
#define LAS __attribute__((address_space(3)))
constexpr size_t LAY_TS = 17408, LAY_HS = 557824, LAY_CRS = 266240, LAY_ORS = 69632, WS_K = 0, WS_V = 17850368, WS_CTX = 35700736, WS_OB = 52740096, WS_FL = 65585152, WS_END = 65634304, LAY_VS = 2;
#define MK_ASM_BODY \
"s_load_dwordx16 s[8:23], %0, 0x0\n" \
"s_load_dwordx4 s[24:27], %0, 0x40\n" \
"s_load_dwordx2 s[28:29], %0, 0x50\n" \
"v_readfirstlane_b32 s61, %2\n" \
"s_lshr_b32 s61, s61, 6\n" \
"v_mbcnt_lo_u32_b32 v78, -1, 0\n" \
"v_mbcnt_hi_u32_b32 v78, -1, v78\n" \
"v_lshlrev_b32 v234, 4, v78\n" \
"s_getpc_b64 s[64:65]\n" \
"Lpc_%=:\n" \
"s_lshr_b32 s70, %1, 3\n" \
"s_lshl_b32 s70, s70, 2\n" \
"s_add_u32 s70, s70, s61\n" \
"s_and_b32 s71, s70, 31\n" \
"s_lshl_b32 s71, s71, 12\n" \
"v_lshl_add_u32 v237, v234, 2, s71\n" \
"s_mov_b32 s71, Lcode_end_%=-Lpc_%=-4\n" \
"v_min_u32 v237, s71, v237\n" \
"global_load_dword v238, v237, s[64:65]\n" \
"s_and_b32 s62, %1, 7\n" \
"s_lshl_b32 s62, s62, 2\n" \
"s_lshr_b32 s70, %1, 6\n" \
"s_add_u32 s62, s62, s70\n" \
"s_bfe_u32 s63, %1, 0x30003\n" \
"s_lshl_b32 s74, s61, 13\n" \
"s_add_u32 s74, s74, %3\n" \
"v_bfe_u32 v79, v78, 4, 1\n" \
"v_bfe_u32 v80, v78, 2, 2\n" \
"v_lshl_add_u32 v80, v79, 3, v80\n" \
"s_lshl_b32 s71, s61, 4\n" \
"v_add_u32 v80, s71, v80\n" \
"v_lshlrev_b32 v80, 13, v80\n" \
"v_lshlrev_b32 v79, 1, v79\n" \
"v_and_b32 v81, 3, v78\n" \
"v_xor_b32 v79, v79, v81\n" \
"v_lshl_add_u32 v80, v79, 4, v80\n" \
"v_lshrrev_b32 v90, 5, v78\n" \
"v_lshl_add_u32 v84, v90, 6, v80\n" \
"v_add_u32 v68, 4096, v84\n" \
"v_add_u32 v69, 3072, v84\n" \
"v_add_u32 v70, 2176, v84\n" \
"v_add_u32 v71, 1152, v84\n" \
"s_lshl_b32 s83, s62, 8\n" \
"s_add_u32 s84, s83, 32768\n" \
"v_lshrrev_b32 v85, 3, v78\n" \
"v_and_b32 v86, 7, v78\n" \
"v_xor_b32 v86, v86, v85\n" \
"v_lshlrev_b32 v86, 4, v86\n" \
"v_lshl_add_u32 v64, v85, 10, v86\n" \
"v_add_u32 v65, 7168, v64\n" \
"v_add_u32 v66, 14336, v64\n" \
"v_add_u32 v67, 21504, v64\n" \
"s_bitcmp1_b32 s61, 0\n" \
"s_cselect_b32 s70, 0, 15\n" \
"s_xor_b32 s81, s63, s70\n" \
"s_lshl_b32 s81, s81, 17\n" \
"s_lshr_b32 s70, s61, 1\n" \
"s_lshl_b32 s71, s70, 16\n" \
"s_add_u32 s81, s81, s71\n" \
"s_add_u32 s82, s81, 32768\n" \
"s_mov_b32 s42, 4198400\n" \
"s_mov_b32 s43, 0x20000\n" \
"s_mov_b32 s46, 4198400\n" \
"s_mov_b32 s47, 0x20000\n" \
"s_mov_b32 s38, 2097152\n" \
"s_mov_b32 s39, 0x20000\n" \
"s_waitcnt lgkmcnt(0)\n" \
"s_mov_b64 s[40:41], s[14:15]\n" \
"s_sub_u32 s40, s40, 4096\n" \
"s_subb_u32 s41, s41, 0\n" \
"s_and_b32 s41, s41, 0xffff\n" \
"s_mov_b64 s[44:45], s[16:17]\n" \
"s_sub_u32 s44, s44, 4096\n" \
"s_subb_u32 s45, s45, 0\n" \
"s_and_b32 s45, s45, 0xffff\n" \
"s_mov_b64 s[36:37], s[10:11]\n" \
"s_and_b32 s37, s37, 0xffff\n" \
"s_add_u32 m0, s74, 32768\n" \
"s_nop 0\n" \
"buffer_load_dwordx4 v68, s[40:43], s83 offen offset:0 lds\n" \
"buffer_load_dwordx4 v69, s[44:47], s83 offen offset:1024 lds\n" \
"buffer_load_dwordx4 v70, s[40:43], s83 offen offset:2048 lds\n" \
"buffer_load_dwordx4 v71, s[44:47], s83 offen offset:3072 lds\n" \
"s_xor_b32 s83, s83, 0x80000\n" \
"s_add_u32 m0, s74, 36864\n" \
"s_nop 0\n" \
"buffer_load_dwordx4 v68, s[40:43], s84 offen offset:0 lds\n" \
"buffer_load_dwordx4 v69, s[44:47], s84 offen offset:1024 lds\n" \
"buffer_load_dwordx4 v70, s[40:43], s84 offen offset:2048 lds\n" \
"buffer_load_dwordx4 v71, s[44:47], s84 offen offset:3072 lds\n" \
"s_xor_b32 s84, s84, 0x80000\n" \
"s_add_u32 m0, s74, 0\n" \
"s_nop 0\n" \
"buffer_load_dwordx4 v64, s[36:39], s81 offen offset:0 lds\n" \
"buffer_load_dwordx4 v65, s[36:39], s81 offen offset:1024 lds\n" \
"buffer_load_dwordx4 v66, s[36:39], s81 offen offset:2048 lds\n" \
"buffer_load_dwordx4 v67, s[36:39], s81 offen offset:3072 lds\n" \
"s_xor_b32 s81, s81, 0x80\n" \
"s_add_u32 m0, s74, 4096\n" \
"s_nop 0\n" \
"buffer_load_dwordx4 v64, s[36:39], s82 offen offset:0 lds\n" \
"buffer_load_dwordx4 v65, s[36:39], s82 offen offset:1024 lds\n" \
"buffer_load_dwordx4 v66, s[36:39], s82 offen offset:2048 lds\n" \
"buffer_load_dwordx4 v67, s[36:39], s82 offen offset:3072 lds\n" \
"s_xor_b32 s82, s82, 0x80\n" \
"v_lshrrev_b32 v87, 4, v78\n" \
"v_and_b32 v79, 15, v78\n" \
"v_and_b32 v80, 7, v78\n" \
"v_xor_b32 v80, v80, v87\n" \
"v_lshlrev_b32 v79, 7, v79\n" \
"s_lshl_b32 s70, s70, 14\n" \
"s_add_u32 s70, s70, %3\n" \
"v_lshl_add_u32 v88, v80, 4, v79\n" \
"v_add_u32 v72, s70, v88\n" \
"v_xor_b32 v73, 64, v72\n" \
"v_lshlrev_b32 v79, 3, v81\n" \
"v_bfe_u32 v80, v78, 2, 2\n" \
"v_lshl_add_u32 v79, v80, 6, v79\n" \
"v_bfe_u32 v80, v78, 4, 1\n" \
"v_lshl_add_u32 v79, v80, 8, v79\n" \
"v_lshl_add_u32 v89, v80, 5, v79\n" \
"s_and_b32 s70, s61, 1\n" \
"s_lshl_b32 s70, s70, 11\n" \
"s_add_u32 s70, s70, %3\n" \
"v_lshl_add_u32 v82, v90, 13, v89\n" \
"v_add_u32 v74, s70, v82\n" \
"v_xor_b32 v75, 32, v74\n" \
"s_mov_b64 s[56:57], s[28:29]\n" \
"s_and_b32 s57, s57, 0xffff\n" \
"s_mov_b32 s58, 65634304\n" \
"s_mov_b32 s59, 0x20000\n" \
"v_mov_b32 v241, 0\n" \
"s_lshl_b32 s66, %1, 2\n" \
"s_add_u32 s66, s66, s61\n" \
"s_lshl_b32 s66, s66, 10\n" \
"s_lshr_b32 s67, %1, 3\n" \
"s_lshl_b32 s67, s67, 2\n" \
"s_add_u32 s67, s67, s61\n" \
"s_lshl_b32 s67, s67, 13\n" \
"s_and_b32 s64, %1, 7\n" \
"s_lshl_b32 s64, s64, 10\n" \
"s_add_u32 s67, s67, s64\n" \
"s_mov_b64 exec, 0\n" \
"s_cmp_eq_u32 s61, 0\n" \
"s_cbranch_scc1 Lst0x0_%=\n" \
"s_cmp_eq_u32 s61, 1\n" \
"s_cbranch_scc1 Lst0x1_%=\n" \
"s_cmp_eq_u32 s61, 2\n" \
"s_cbranch_scc1 Lst0x2_%=\n" \
"s_branch Lst0x3_%=\n" \
"Lcb0_%=:\n" \
"s_mov_b64 exec, -1\n" \
"s_add_u32 m0, s74, 131072\n" \
"s_nop 0\n" \
"buffer_load_dwordx4 v68, s[40:43], s83 offen offset:0 lds\n" \
"buffer_load_dwordx4 v69, s[44:47], s83 offen offset:1024 lds\n" \
"buffer_load_dwordx4 v70, s[40:43], s83 offen offset:2048 lds\n" \
"buffer_load_dwordx4 v71, s[44:47], s83 offen offset:3072 lds\n" \
"s_xor_b32 s83, s83, 0x180000\n" \
"s_add_u32 m0, s74, 135168\n" \
"s_nop 0\n" \
"buffer_load_dwordx4 v68, s[40:43], s84 offen offset:0 lds\n" \
"buffer_load_dwordx4 v69, s[44:47], s84 offen offset:1024 lds\n" \
"buffer_load_dwordx4 v70, s[40:43], s84 offen offset:2048 lds\n" \
"buffer_load_dwordx4 v71, s[44:47], s84 offen offset:3072 lds\n" \
"s_xor_b32 s84, s84, 0x180000\n" \
"s_mov_b32 s93, 65593344\n" \
"buffer_load_dword v240, v241, s[56:59], s93 offen sc1\n" \
"s_mov_b64 s[64:65], s[10:11]\n" \
"s_add_u32 s68, s66, 0x0\n" \
"s_add_u32 s64, s64, s68\n" \
"s_addc_u32 s65, s65, 0\n" \
"global_load_dword v238, v234, s[64:65]\n" \
"s_mov_b64 s[64:65], s[10:11]\n" \
"s_add_u32 s68, s66, 0x100000\n" \
"s_add_u32 s64, s64, s68\n" \
"s_addc_u32 s65, s65, 0\n" \
"global_load_dword v238, v234, s[64:65]\n" \
"s_mov_b64 s[64:65], s[14:15]\n" \
"s_add_u32 s68, s67, 0x100000\n" \
"s_add_u32 s64, s64, s68\n" \
"s_addc_u32 s65, s65, 0\n" \
"global_load_dword v238, v234, s[64:65]\n" \
"s_mov_b64 s[64:65], s[16:17]\n" \
"s_add_u32 s68, s67, 0x100000\n" \
"s_add_u32 s64, s64, s68\n" \
"s_addc_u32 s65, s65, 0\n" \
"global_load_dword v238, v234, s[64:65]\n" \
"s_waitcnt vmcnt(13)\n" \
"s_barrier\n" \
"ds_read_b64_tr_b16 a[64:65], v74 offset:32768\n" \
"ds_read_b64_tr_b16 a[66:67], v74 offset:36864\n" \
"ds_read_b64_tr_b16 a[68:69], v74 offset:49152\n" \
"ds_read_b64_tr_b16 a[70:71], v74 offset:53248\n" \
"ds_read_b128 a[0:3], v72 offset:0\n" \
"ds_read_b128 a[4:7], v73 offset:0\n" \
"ds_read_b64_tr_b16 a[72:73], v75 offset:32768\n" \
"ds_read_b64_tr_b16 a[74:75], v75 offset:36864\n" \
"ds_read_b64_tr_b16 a[76:77], v75 offset:49152\n" \
"ds_read_b64_tr_b16 a[78:79], v75 offset:53248\n" \
"ds_read_b64_tr_b16 a[80:81], v74 offset:33280\n" \
"ds_read_b64_tr_b16 a[82:83], v74 offset:37376\n" \
"ds_read_b64_tr_b16 a[84:85], v74 offset:49664\n" \
"ds_read_b64_tr_b16 a[86:87], v74 offset:53760\n" \
"ds_read_b64_tr_b16 a[88:89], v75 offset:33280\n" \
"ds_read_b64_tr_b16 a[90:91], v75 offset:37376\n" \
"ds_read_b64_tr_b16 a[92:93], v75 offset:49664\n" \
"ds_read_b64_tr_b16 a[94:95], v75 offset:53760\n" \
"ds_read_b128 a[8:11], v72 offset:2048\n" \
"ds_read_b128 a[12:15], v73 offset:2048\n" \
"s_waitcnt lgkmcnt(14)\n" \
"v_mfma_f32_16x16x32_bf16 v[0:3], a[64:67], a[0:3], 0\n" \
"s_add_u32 m0, s74, 65536\n" \
"ds_read_b128 a[16:19], v72 offset:4096\n" \
"v_mfma_f32_16x16x32_bf16 v[0:3], a[68:71], a[4:7], v[0:3]\n" \
"s_waitcnt lgkmcnt(11)\n" \
"buffer_load_dwordx4 v64, s[36:39], s81 offen offset:0 lds\n" \
"v_mfma_f32_16x16x32_bf16 v[4:7], a[72:75], a[0:3], 0\n" \
"ds_read_b128 a[20:23], v73 offset:4096\n" \
"v_mfma_f32_16x16x32_bf16 v[4:7], a[76:79], a[4:7], v[4:7]\n" \
"s_waitcnt lgkmcnt(8)\n" \
"v_mfma_f32_16x16x32_bf16 v[8:11], a[80:83], a[0:3], 0\n" \
"s_waitcnt lgkmcnt(2)\n" \
"ds_read_b128 a[24:27], v72 offset:6144\n" \
"v_mfma_f32_16x16x32_bf16 v[8:11], a[84:87], a[4:7], v[8:11]\n" \
"buffer_load_dwordx4 v65, s[36:39], s81 offen offset:1024 lds\n" \
"v_mfma_f32_16x16x32_bf16 v[12:15], a[88:91], a[0:3], 0\n" \
"ds_read_b128 a[28:31], v73 offset:6144\n" \
"v_mfma_f32_16x16x32_bf16 v[12:15], a[92:95], a[4:7], v[12:15]\n" \
"v_mfma_f32_16x16x32_bf16 v[28:31], a[88:91], a[8:11], 0\n" \
"ds_read_b128 a[32:35], v72 offset:8192\n" \
"v_mfma_f32_16x16x32_bf16 v[28:31], a[92:95], a[12:15], v[28:31]\n" \
"buffer_load_dwordx4 v66, s[36:39], s81 offen offset:2048 lds\n" \
"v_mfma_f32_16x16x32_bf16 v[24:27], a[80:83], a[8:11], 0\n" \
"ds_read_b128 a[36:39], v73 offset:8192\n" \
"v_mfma_f32_16x16x32_bf16 v[24:27], a[84:87], a[12:15], v[24:27]\n" \
"v_mfma_f32_16x16x32_bf16 v[20:23], a[72:75], a[8:11], 0\n" \
"ds_read_b128 a[40:43], v72 offset:10240\n" \
"v_mfma_f32_16x16x32_bf16 v[20:23], a[76:79], a[12:15], v[20:23]\n" \
"buffer_load_dwordx4 v67, s[36:39], s81 offen offset:3072 lds\n" \
"v_mfma_f32_16x16x32_bf16 v[16:19], a[64:67], a[8:11], 0\n" \
"ds_read_b128 a[44:47], v73 offset:10240\n" \
"v_mfma_f32_16x16x32_bf16 v[16:19], a[68:71], a[12:15], v[16:19]\n" \
"s_waitcnt lgkmcnt(2)\n" \
"s_xor_b32 s81, s81, 0x180\n" \
"v_mfma_f32_16x16x32_bf16 v[32:35], a[64:67], a[16:19], 0\n" \
"s_add_u32 m0, s74, 69632\n" \
"ds_read_b128 a[48:51], v72 offset:12288\n" \
"v_mfma_f32_16x16x32_bf16 v[32:35], a[68:71], a[20:23], v[32:35]\n" \
"buffer_load_dwordx4 v64, s[36:39], s82 offen offset:0 lds\n" \
"v_mfma_f32_16x16x32_bf16 v[36:39], a[72:75], a[16:19], 0\n" \
"ds_read_b128 a[52:55], v73 offset:12288\n" \
"v_mfma_f32_16x16x32_bf16 v[36:39], a[76:79], a[20:23], v[36:39]\n" \
"v_mfma_f32_16x16x32_bf16 v[40:43], a[80:83], a[16:19], 0\n" \
"ds_read_b128 a[56:59], v72 offset:14336\n" \
"v_mfma_f32_16x16x32_bf16 v[40:43], a[84:87], a[20:23], v[40:43]\n" \
"buffer_load_dwordx4 v65, s[36:39], s82 offen offset:1024 lds\n" \
"v_mfma_f32_16x16x32_bf16 v[44:47], a[88:91], a[16:19], 0\n" \
"ds_read_b128 a[60:63], v73 offset:14336\n" \
"v_mfma_f32_16x16x32_bf16 v[44:47], a[92:95], a[20:23], v[44:47]\n" \
"v_mfma_f32_16x16x32_bf16 v[60:63], a[88:91], a[24:27], 0\n" \
"ds_read_b64_tr_b16 a[96:97], v74 offset:33792\n" \
"v_mfma_f32_16x16x32_bf16 v[60:63], a[92:95], a[28:31], v[60:63]\n" \
"buffer_load_dwordx4 v66, s[36:39], s82 offen offset:2048 lds\n" \
"v_mfma_f32_16x16x32_bf16 v[56:59], a[80:83], a[24:27], 0\n" \
"ds_read_b64_tr_b16 a[98:99], v74 offset:37888\n" \
"v_mfma_f32_16x16x32_bf16 v[56:59], a[84:87], a[28:31], v[56:59]\n" \
"v_mfma_f32_16x16x32_bf16 v[52:55], a[72:75], a[24:27], 0\n" \
"ds_read_b64_tr_b16 a[100:101], v74 offset:50176\n" \
"v_mfma_f32_16x16x32_bf16 v[52:55], a[76:79], a[28:31], v[52:55]\n" \
"buffer_load_dwordx4 v67, s[36:39], s82 offen offset:3072 lds\n" \
"v_mfma_f32_16x16x32_bf16 v[48:51], a[64:67], a[24:27], 0\n" \
"ds_read_b64_tr_b16 a[102:103], v74 offset:54272\n" \
"v_mfma_f32_16x16x32_bf16 v[48:51], a[68:71], a[28:31], v[48:51]\n" \
"v_mfma_f32_16x16x32_bf16 v[144:147], a[64:67], a[32:35], 0\n" \
"s_xor_b32 s82, s82, 0x180\n" \
"ds_read_b64_tr_b16 a[104:105], v75 offset:33792\n" \
"v_mfma_f32_16x16x32_bf16 v[144:147], a[68:71], a[36:39], v[144:147]\n" \
"v_mfma_f32_16x16x32_bf16 v[148:151], a[72:75], a[32:35], 0\n" \
"ds_read_b64_tr_b16 a[106:107], v75 offset:37888\n" \
"v_mfma_f32_16x16x32_bf16 v[148:151], a[76:79], a[36:39], v[148:151]\n" \
"v_mfma_f32_16x16x32_bf16 v[152:155], a[80:83], a[32:35], 0\n" \
"ds_read_b64_tr_b16 a[108:109], v75 offset:50176\n" \
"v_mfma_f32_16x16x32_bf16 v[152:155], a[84:87], a[36:39], v[152:155]\n" \
"v_mfma_f32_16x16x32_bf16 v[156:159], a[88:91], a[32:35], 0\n" \
"ds_read_b64_tr_b16 a[110:111], v75 offset:54272\n" \
"v_mfma_f32_16x16x32_bf16 v[156:159], a[92:95], a[36:39], v[156:159]\n" \
"s_waitcnt lgkmcnt(2)\n" \
"v_mfma_f32_16x16x32_bf16 v[188:191], a[88:91], a[40:43], 0\n" \
"ds_read_b64_tr_b16 a[112:113], v74 offset:34304\n" \
"v_add_u32 v96, 0x10000, v72\n" \
"v_mfma_f32_16x16x32_bf16 v[188:191], a[92:95], a[44:47], v[188:191]\n" \
"v_mfma_f32_16x16x32_bf16 v[184:187], a[80:83], a[40:43], 0\n" \
"ds_read_b64_tr_b16 a[114:115], v74 offset:38400\n" \
"v_add_u32 v97, 0x10000, v73\n" \
"v_mfma_f32_16x16x32_bf16 v[184:187], a[84:87], a[44:47], v[184:187]\n" \
"v_mfma_f32_16x16x32_bf16 v[180:183], a[72:75], a[40:43], 0\n" \
"ds_read_b64_tr_b16 a[116:117], v74 offset:50688\n" \
"v_add_u32 v98, 0x10000, v74\n" \
"v_mfma_f32_16x16x32_bf16 v[180:183], a[76:79], a[44:47], v[180:183]\n" \
"v_mfma_f32_16x16x32_bf16 v[176:179], a[64:67], a[40:43], 0\n" \
"ds_read_b64_tr_b16 a[118:119], v74 offset:54784\n" \
"v_add_u32 v99, 0x10000, v75\n" \
"v_mfma_f32_16x16x32_bf16 v[176:179], a[68:71], a[44:47], v[176:179]\n" \
"v_mfma_f32_16x16x32_bf16 v[192:195], a[64:67], a[48:51], 0\n" \
"s_add_u32 m0, s74, 98304\n" \
"ds_read_b64_tr_b16 a[120:121], v75 offset:34304\n" \
"v_add_u32 v100, 0x20000, v72\n" \
"v_mfma_f32_16x16x32_bf16 v[192:195], a[68:71], a[52:55], v[192:195]\n" \
"buffer_load_dwordx4 v68, s[40:43], s83 offen offset:0 lds\n" \
"v_mfma_f32_16x16x32_bf16 v[196:199], a[72:75], a[48:51], 0\n" \
"ds_read_b64_tr_b16 a[122:123], v75 offset:38400\n" \
"v_add_u32 v101, 0x20000, v73\n" \
"v_mfma_f32_16x16x32_bf16 v[196:199], a[76:79], a[52:55], v[196:199]\n" \
"v_mfma_f32_16x16x32_bf16 v[200:203], a[80:83], a[48:51], 0\n" \
"ds_read_b64_tr_b16 a[124:125], v75 offset:50688\n" \
"v_add_u32 v102, 0x20000, v74\n" \
"v_mfma_f32_16x16x32_bf16 v[200:203], a[84:87], a[52:55], v[200:203]\n" \
"buffer_load_dwordx4 v69, s[44:47], s83 offen offset:1024 lds\n" \
"v_mfma_f32_16x16x32_bf16 v[204:207], a[88:91], a[48:51], 0\n" \
"ds_read_b64_tr_b16 a[126:127], v75 offset:54784\n" \
"v_add_u32 v103, 0x20000, v75\n" \
"v_mfma_f32_16x16x32_bf16 v[204:207], a[92:95], a[52:55], v[204:207]\n" \
"v_mfma_f32_16x16x32_bf16 v[220:223], a[88:91], a[56:59], 0\n" \
"v_mfma_f32_16x16x32_bf16 v[220:223], a[92:95], a[60:63], v[220:223]\n" \
"buffer_load_dwordx4 v70, s[40:43], s83 offen offset:2048 lds\n" \
"v_mfma_f32_16x16x32_bf16 v[216:219], a[80:83], a[56:59], 0\n" \
"v_mfma_f32_16x16x32_bf16 v[216:219], a[84:87], a[60:63], v[216:219]\n" \
"v_mfma_f32_16x16x32_bf16 v[212:215], a[72:75], a[56:59], 0\n" \
"v_mfma_f32_16x16x32_bf16 v[212:215], a[76:79], a[60:63], v[212:215]\n" \
"buffer_load_dwordx4 v71, s[44:47], s83 offen offset:3072 lds\n" \
"v_mfma_f32_16x16x32_bf16 v[208:211], a[64:67], a[56:59], 0\n" \
"v_mfma_f32_16x16x32_bf16 v[208:211], a[68:71], a[60:63], v[208:211]\n" \
"s_xor_b32 s83, s83, 0x80000\n" \
"s_waitcnt lgkmcnt(0)\n" \
"s_barrier\n" \
"v_mfma_f32_16x16x32_bf16 a[128:131], a[0:3], a[96:99], 0\n" \
"s_add_u32 m0, s74, 102400\n" \
"v_mfma_f32_16x16x32_bf16 a[128:131], a[4:7], a[100:103], a[128:131]\n" \
"buffer_load_dwordx4 v68, s[40:43], s84 offen offset:0 lds\n" \
"v_mfma_f32_16x16x32_bf16 a[132:135], a[0:3], a[104:107], 0\n" \
"v_mfma_f32_16x16x32_bf16 a[132:135], a[4:7], a[108:111], a[132:135]\n" \
"v_mfma_f32_16x16x32_bf16 a[136:139], a[0:3], a[112:115], 0\n" \
"v_mfma_f32_16x16x32_bf16 a[136:139], a[4:7], a[116:119], a[136:139]\n" \
"buffer_load_dwordx4 v69, s[44:47], s84 offen offset:1024 lds\n" \
"v_mfma_f32_16x16x32_bf16 a[140:143], a[0:3], a[120:123], 0\n" \
"v_mfma_f32_16x16x32_bf16 a[140:143], a[4:7], a[124:127], a[140:143]\n" \
"v_mfma_f32_16x16x32_bf16 a[156:159], a[8:11], a[120:123], 0\n" \
"v_mfma_f32_16x16x32_bf16 a[156:159], a[12:15], a[124:127], a[156:159]\n" \
"buffer_load_dwordx4 v70, s[40:43], s84 offen offset:2048 lds\n" \
"v_mfma_f32_16x16x32_bf16 a[152:155], a[8:11], a[112:115], 0\n" \
"v_mfma_f32_16x16x32_bf16 a[152:155], a[12:15], a[116:119], a[152:155]\n" \
"v_mfma_f32_16x16x32_bf16 a[148:151], a[8:11], a[104:107], 0\n" \
"v_mfma_f32_16x16x32_bf16 a[148:151], a[12:15], a[108:111], a[148:151]\n" \
"buffer_load_dwordx4 v71, s[44:47], s84 offen offset:3072 lds\n" \
"v_mfma_f32_16x16x32_bf16 a[144:147], a[8:11], a[96:99], 0\n" \
"v_mfma_f32_16x16x32_bf16 a[144:147], a[12:15], a[100:103], a[144:147]\n" \
"v_mfma_f32_16x16x32_bf16 a[160:163], a[16:19], a[96:99], 0\n" \
"s_xor_b32 s84, s84, 0x80000\n" \
"v_mfma_f32_16x16x32_bf16 a[160:163], a[20:23], a[100:103], a[160:163]\n" \
"v_mfma_f32_16x16x32_bf16 a[164:167], a[16:19], a[104:107], 0\n" \
"v_mfma_f32_16x16x32_bf16 a[164:167], a[20:23], a[108:111], a[164:167]\n" \
"v_mfma_f32_16x16x32_bf16 a[168:171], a[16:19], a[112:115], 0\n" \
"s_add_u32 m0, s74, 32768\n" \
"v_mfma_f32_16x16x32_bf16 a[168:171], a[20:23], a[116:119], a[168:171]\n" \
"buffer_load_dwordx4 v64, s[36:39], s81 offen offset:0 lds\n" \
"v_mfma_f32_16x16x32_bf16 a[172:175], a[16:19], a[120:123], 0\n" \
"v_mfma_f32_16x16x32_bf16 a[172:175], a[20:23], a[124:127], a[172:175]\n" \
"v_mfma_f32_16x16x32_bf16 a[188:191], a[24:27], a[120:123], 0\n" \
"v_mfma_f32_16x16x32_bf16 a[188:191], a[28:31], a[124:127], a[188:191]\n" \
"buffer_load_dwordx4 v65, s[36:39], s81 offen offset:1024 lds\n" \
"v_mfma_f32_16x16x32_bf16 a[184:187], a[24:27], a[112:115], 0\n" \
"v_mfma_f32_16x16x32_bf16 a[184:187], a[28:31], a[116:119], a[184:187]\n" \
"v_mfma_f32_16x16x32_bf16 a[180:183], a[24:27], a[104:107], 0\n" \
"v_mfma_f32_16x16x32_bf16 a[180:183], a[28:31], a[108:111], a[180:183]\n" \
"buffer_load_dwordx4 v66, s[36:39], s81 offen offset:2048 lds\n" \
"v_mfma_f32_16x16x32_bf16 a[176:179], a[24:27], a[96:99], 0\n" \
"v_mfma_f32_16x16x32_bf16 a[176:179], a[28:31], a[100:103], a[176:179]\n" \
"v_mfma_f32_16x16x32_bf16 a[192:195], a[32:35], a[96:99], 0\n" \
"v_mfma_f32_16x16x32_bf16 a[192:195], a[36:39], a[100:103], a[192:195]\n" \
"buffer_load_dwordx4 v67, s[36:39], s81 offen offset:3072 lds\n" \
"v_mfma_f32_16x16x32_bf16 a[196:199], a[32:35], a[104:107], 0\n" \
"v_mfma_f32_16x16x32_bf16 a[196:199], a[36:39], a[108:111], a[196:199]\n" \
"s_xor_b32 s81, s81, 0x80\n" \
"v_mfma_f32_16x16x32_bf16 a[200:203], a[32:35], a[112:115], 0\n" \
"s_add_u32 m0, s74, 36864\n" \
"v_mfma_f32_16x16x32_bf16 a[200:203], a[36:39], a[116:119], a[200:203]\n" \
"buffer_load_dwordx4 v64, s[36:39], s82 offen offset:0 lds\n" \
"v_mfma_f32_16x16x32_bf16 a[204:207], a[32:35], a[120:123], 0\n" \
"v_mfma_f32_16x16x32_bf16 a[204:207], a[36:39], a[124:127], a[204:207]\n" \
"s_waitcnt vmcnt(13) lgkmcnt(0)\n" \
"s_barrier\n" \
"v_mfma_f32_16x16x32_bf16 a[220:223], a[40:43], a[120:123], 0\n" \
"ds_read_b64_tr_b16 a[64:65], v102 offset:0\n" \
"ds_read_b64_tr_b16 a[66:67], v102 offset:4096\n" \
"v_mfma_f32_16x16x32_bf16 a[220:223], a[44:47], a[124:127], a[220:223]\n" \
"buffer_load_dwordx4 v65, s[36:39], s82 offen offset:1024 lds\n" \
"v_mfma_f32_16x16x32_bf16 a[216:219], a[40:43], a[112:115], 0\n" \
"ds_read_b64_tr_b16 a[68:69], v102 offset:16384\n" \
"ds_read_b64_tr_b16 a[70:71], v102 offset:20480\n" \
"v_mfma_f32_16x16x32_bf16 a[216:219], a[44:47], a[116:119], a[216:219]\n" \
"ds_read_b128 a[0:3], v96 offset:0\n" \
"ds_read_b128 a[4:7], v97 offset:0\n" \
"v_mfma_f32_16x16x32_bf16 a[212:215], a[40:43], a[104:107], 0\n" \
"ds_read_b64_tr_b16 a[72:73], v103 offset:0\n" \
"ds_read_b64_tr_b16 a[74:75], v103 offset:4096\n" \
"v_mfma_f32_16x16x32_bf16 a[212:215], a[44:47], a[108:111], a[212:215]\n" \
"buffer_load_dwordx4 v66, s[36:39], s82 offen offset:2048 lds\n" \
"v_mfma_f32_16x16x32_bf16 a[208:211], a[40:43], a[96:99], 0\n" \
"ds_read_b64_tr_b16 a[76:77], v103 offset:16384\n" \
"ds_read_b64_tr_b16 a[78:79], v103 offset:20480\n" \
"v_mfma_f32_16x16x32_bf16 a[208:211], a[44:47], a[100:103], a[208:211]\n" \
"ds_read_b64_tr_b16 a[80:81], v102 offset:512\n" \
"ds_read_b64_tr_b16 a[82:83], v102 offset:4608\n" \
"v_mfma_f32_16x16x32_bf16 a[224:227], a[48:51], a[96:99], 0\n" \
"ds_read_b64_tr_b16 a[84:85], v102 offset:16896\n" \
"ds_read_b64_tr_b16 a[86:87], v102 offset:20992\n" \
"v_mfma_f32_16x16x32_bf16 a[224:227], a[52:55], a[100:103], a[224:227]\n" \
"buffer_load_dwordx4 v67, s[36:39], s82 offen offset:3072 lds\n" \
"v_mfma_f32_16x16x32_bf16 a[228:231], a[48:51], a[104:107], 0\n" \
"s_mov_b64 s[64:65], s[14:15]\n" \
"s_add_u32 s68, s67, 0x200000\n" \
"ds_read_b64_tr_b16 a[88:89], v103 offset:512\n" \
"ds_read_b64_tr_b16 a[90:91], v103 offset:4608\n" \
"v_mfma_f32_16x16x32_bf16 a[228:231], a[52:55], a[108:111], a[228:231]\n" \
"s_add_u32 s64, s64, s68\n" \
"s_addc_u32 s65, s65, 0\n" \
"ds_read_b64_tr_b16 a[92:93], v103 offset:16896\n" \
"ds_read_b64_tr_b16 a[94:95], v103 offset:20992\n" \
"v_mfma_f32_16x16x32_bf16 a[232:235], a[48:51], a[112:115], 0\n" \
"s_xor_b32 s82, s82, 0x80\n" \
"ds_read_b128 a[8:11], v96 offset:2048\n" \
"ds_read_b128 a[12:15], v97 offset:2048\n" \
"v_mfma_f32_16x16x32_bf16 a[232:235], a[52:55], a[116:119], a[232:235]\n" \
"global_load_dword v238, v234, s[64:65]\n" \
"v_mfma_f32_16x16x32_bf16 a[236:239], a[48:51], a[120:123], 0\n" \
"s_mov_b64 s[64:65], s[16:17]\n" \
"s_add_u32 s68, s67, 0x200000\n" \
"ds_read_b128 a[16:19], v96 offset:4096\n" \
"ds_read_b128 a[20:23], v97 offset:4096\n" \
"v_mfma_f32_16x16x32_bf16 a[236:239], a[52:55], a[124:127], a[236:239]\n" \
"s_add_u32 s64, s64, s68\n" \
"s_addc_u32 s65, s65, 0\n" \
"ds_read_b128 a[24:27], v96 offset:6144\n" \
"ds_read_b128 a[28:31], v97 offset:6144\n" \
"v_mfma_f32_16x16x32_bf16 a[252:255], a[56:59], a[120:123], 0\n" \
"ds_read_b128 a[32:35], v96 offset:8192\n" \
"ds_read_b128 a[36:39], v97 offset:8192\n" \
"v_mfma_f32_16x16x32_bf16 a[252:255], a[60:63], a[124:127], a[252:255]\n" \
"global_load_dword v238, v234, s[64:65]\n" \
"v_mfma_f32_16x16x32_bf16 a[248:251], a[56:59], a[112:115], 0\n" \
"ds_read_b128 a[40:43], v96 offset:10240\n" \
"ds_read_b128 a[44:47], v97 offset:10240\n" \
"v_mfma_f32_16x16x32_bf16 a[248:251], a[60:63], a[116:119], a[248:251]\n" \
"ds_read_b128 a[48:51], v96 offset:12288\n" \
"ds_read_b128 a[52:55], v97 offset:12288\n" \
"v_mfma_f32_16x16x32_bf16 a[244:247], a[56:59], a[104:107], 0\n" \
"v_mfma_f32_16x16x32_bf16 a[244:247], a[60:63], a[108:111], a[244:247]\n" \
"v_mfma_f32_16x16x32_bf16 a[240:243], a[56:59], a[96:99], 0\n" \
"v_mfma_f32_16x16x32_bf16 a[240:243], a[60:63], a[100:103], a[240:243]\n" \
"s_waitcnt lgkmcnt(2)\n" \
"v_mfma_f32_16x16x32_bf16 v[0:3], a[64:67], a[0:3], v[0:3]\n" \
"s_lshl_b32 s79, s61, 12\n" \
"ds_read_b128 a[56:59], v96 offset:14336\n" \
"v_mfma_f32_16x16x32_bf16 v[0:3], a[68:71], a[4:7], v[0:3]\n" \
"s_add_u32 s79, s79, %3\n" \
"v_add_u32 v228, 4096, v84\n" \
"v_add_u32 v229, 3200, v84\n" \
"v_mfma_f32_16x16x32_bf16 v[4:7], a[72:75], a[0:3], v[4:7]\n" \
"s_add_u32 m0, s74, 0\n" \
"ds_read_b128 a[60:63], v97 offset:14336\n" \
"v_mfma_f32_16x16x32_bf16 v[4:7], a[76:79], a[4:7], v[4:7]\n" \
"buffer_load_dwordx4 v68, s[40:43], s83 offen offset:0 lds\n" \
"v_mfma_f32_16x16x32_bf16 v[8:11], a[80:83], a[0:3], v[8:11]\n" \
"s_lshl_b32 s78, s62, 8\n" \
"ds_read_b64_tr_b16 a[96:97], v102 offset:1024\n" \
"v_mfma_f32_16x16x32_bf16 v[8:11], a[84:87], a[4:7], v[8:11]\n" \
"s_add_u32 s89, s78, 30720\n" \
"v_mfma_f32_16x16x32_bf16 v[12:15], a[88:91], a[0:3], v[12:15]\n" \
"s_mov_b64 s[48:49], s[8:9]\n" \
"ds_read_b64_tr_b16 a[98:99], v102 offset:5120\n" \
"v_mfma_f32_16x16x32_bf16 v[12:15], a[92:95], a[4:7], v[12:15]\n" \
"buffer_load_dwordx4 v69, s[44:47], s83 offen offset:1024 lds\n" \
"v_mfma_f32_16x16x32_bf16 v[28:31], a[88:91], a[8:11], v[28:31]\n" \
"s_and_b32 s49, s49, 0xffff\n" \
"ds_read_b64_tr_b16 a[100:101], v102 offset:17408\n" \
"v_mfma_f32_16x16x32_bf16 v[28:31], a[92:95], a[12:15], v[28:31]\n" \
"s_mov_b32 s50, 4194304\n" \
"v_mfma_f32_16x16x32_bf16 v[24:27], a[80:83], a[8:11], v[24:27]\n" \
"s_mov_b32 s51, 0x20000\n" \
"ds_read_b64_tr_b16 a[102:103], v102 offset:21504\n" \
"v_mfma_f32_16x16x32_bf16 v[24:27], a[84:87], a[12:15], v[24:27]\n" \
"buffer_load_dwordx4 v70, s[40:43], s83 offen offset:2048 lds\n" \
"v_mfma_f32_16x16x32_bf16 v[20:23], a[72:75], a[8:11], v[20:23]\n" \
"s_mov_b64 s[52:53], s[12:13]\n" \
"ds_read_b64_tr_b16 a[104:105], v103 offset:1024\n" \
"v_mfma_f32_16x16x32_bf16 v[20:23], a[76:79], a[12:15], v[20:23]\n" \
"s_sub_u32 s52, s52, 4096\n" \
"s_subb_u32 s53, s53, 0\n" \
"v_mfma_f32_16x16x32_bf16 v[16:19], a[64:67], a[8:11], v[16:19]\n" \
"s_and_b32 s53, s53, 0xffff\n" \
"ds_read_b64_tr_b16 a[106:107], v103 offset:5120\n" \
"v_mfma_f32_16x16x32_bf16 v[16:19], a[68:71], a[12:15], v[16:19]\n" \
"buffer_load_dwordx4 v71, s[44:47], s83 offen offset:3072 lds\n" \
"v_mfma_f32_16x16x32_bf16 v[32:35], a[64:67], a[16:19], v[32:35]\n" \
"s_mov_b32 s54, 8392704\n" \
"s_mov_b32 s55, 0x20000\n" \
"ds_read_b64_tr_b16 a[108:109], v103 offset:17408\n" \
"v_mfma_f32_16x16x32_bf16 v[32:35], a[68:71], a[20:23], v[32:35]\n" \
"s_xor_b32 s83, s83, 0x380000\n" \
"v_lshl_add_u32 v224, v85, 11, v86\n" \
"v_add_u32 v225, 15360, v224\n" \
"v_mfma_f32_16x16x32_bf16 v[36:39], a[72:75], a[16:19], v[36:39]\n" \
"s_add_u32 m0, s74, 4096\n" \
"ds_read_b64_tr_b16 a[110:111], v103 offset:21504\n" \
"v_add_u32 v226, 129024, v224\n" \
"v_mfma_f32_16x16x32_bf16 v[36:39], a[76:79], a[20:23], v[36:39]\n" \
"buffer_load_dwordx4 v68, s[40:43], s84 offen offset:0 lds\n" \
"v_mfma_f32_16x16x32_bf16 v[40:43], a[80:83], a[16:19], v[40:43]\n" \
"ds_read_b64_tr_b16 a[112:113], v102 offset:1536\n" \
"v_add_u32 v227, 144384, v224\n" \
"v_mfma_f32_16x16x32_bf16 v[40:43], a[84:87], a[20:23], v[40:43]\n" \
"s_lshl_b32 s70, s61, 15\n" \
"v_mfma_f32_16x16x32_bf16 v[44:47], a[88:91], a[16:19], v[44:47]\n" \
"s_lshl_b32 s75, s63, 18\n" \
"ds_read_b64_tr_b16 a[114:115], v102 offset:5632\n" \
"v_mfma_f32_16x16x32_bf16 v[44:47], a[92:95], a[20:23], v[44:47]\n" \
"buffer_load_dwordx4 v69, s[44:47], s84 offen offset:1024 lds\n" \
"v_mfma_f32_16x16x32_bf16 v[60:63], a[88:91], a[24:27], v[60:63]\n" \
"s_sub_u32 s77, 3932160, s75\n" \
"ds_read_b64_tr_b16 a[116:117], v102 offset:17920\n" \
"v_mfma_f32_16x16x32_bf16 v[60:63], a[92:95], a[28:31], v[60:63]\n" \
"s_lshr_b32 s71, %1, 6\n" \
"v_mfma_f32_16x16x32_bf16 v[56:59], a[80:83], a[24:27], v[56:59]\n" \
"s_xor_b32 s71, s71, s63\n" \
"ds_read_b64_tr_b16 a[118:119], v102 offset:22016\n" \
"v_mfma_f32_16x16x32_bf16 v[56:59], a[84:87], a[28:31], v[56:59]\n" \
"buffer_load_dwordx4 v70, s[40:43], s84 offen offset:2048 lds\n" \
"v_mfma_f32_16x16x32_bf16 v[52:55], a[72:75], a[24:27], v[52:55]\n" \
"s_and_b32 s71, s71, 0\n" \
"ds_read_b64_tr_b16 a[120:121], v103 offset:1536\n" \
"v_mfma_f32_16x16x32_bf16 v[52:55], a[76:79], a[28:31], v[52:55]\n" \
"s_lshl_b32 s71, s71, 7\n" \
"v_mfma_f32_16x16x32_bf16 v[48:51], a[64:67], a[24:27], v[48:51]\n" \
"s_add_u32 s70, s70, s71\n" \
"ds_read_b64_tr_b16 a[122:123], v103 offset:5632\n" \
"v_mfma_f32_16x16x32_bf16 v[48:51], a[68:71], a[28:31], v[48:51]\n" \
"buffer_load_dwordx4 v71, s[44:47], s84 offen offset:3072 lds\n" \
"v_mfma_f32_16x16x32_bf16 v[144:147], a[64:67], a[32:35], v[144:147]\n" \
"s_add_u32 s75, s75, s70\n" \
"ds_read_b64_tr_b16 a[124:125], v103 offset:17920\n" \
"v_mfma_f32_16x16x32_bf16 v[144:147], a[68:71], a[36:39], v[144:147]\n" \
"s_add_u32 s77, s77, s70\n" \
"v_mfma_f32_16x16x32_bf16 v[148:151], a[72:75], a[32:35], v[148:151]\n" \
"s_xor_b32 s84, s84, 0x380000\n" \
"ds_read_b64_tr_b16 a[126:127], v103 offset:22016\n" \
"v_mfma_f32_16x16x32_bf16 v[148:151], a[76:79], a[36:39], v[148:151]\n" \
"s_lshl_b32 s71, s71, 12\n" \
"v_mfma_f32_16x16x32_bf16 v[152:155], a[80:83], a[32:35], v[152:155]\n" \
"s_add_u32 s78, s78, s71\n" \
"v_mfma_f32_16x16x32_bf16 v[152:155], a[84:87], a[36:39], v[152:155]\n" \
"s_add_u32 s89, s89, s71\n" \
"v_lshlrev_b32 v242, 2, v78\n" \
"v_and_b32 v236, 0xf0, v234\n" \
"v_mfma_f32_16x16x32_bf16 v[156:159], a[88:91], a[32:35], v[156:159]\n" \
"v_lshl_add_u32 v236, v87, 9, v236\n" \
"v_mfma_f32_16x16x32_bf16 v[156:159], a[92:95], a[36:39], v[156:159]\n" \
"s_add_u32 s71, s74, 0x10000\n" \
"v_add_u32 v230, s71, v88\n" \
"v_xor_b32 v231, 64, v230\n" \
"v_mfma_f32_16x16x32_bf16 v[188:191], a[88:91], a[40:43], v[188:191]\n" \
"v_add_u32 v250, 0x10000, v230\n" \
"v_add_u32 v251, 0x10000, v231\n" \
"v_mfma_f32_16x16x32_bf16 v[188:191], a[92:95], a[44:47], v[188:191]\n" \
"v_lshl_add_u32 v82, v90, 12, v89\n" \
"v_add_u32 v232, %3, v82\n" \
"v_mfma_f32_16x16x32_bf16 v[184:187], a[80:83], a[40:43], v[184:187]\n" \
"v_xor_b32 v233, 32, v232\n" \
"v_mfma_f32_16x16x32_bf16 v[184:187], a[84:87], a[44:47], v[184:187]\n" \
"s_mul_i32 s87, s62, 557824\n" \
"v_mfma_f32_16x16x32_bf16 v[180:183], a[72:75], a[40:43], v[180:183]\n" \
"s_lshr_b32 s70, s61, 1\n" \
"v_mfma_f32_16x16x32_bf16 v[180:183], a[76:79], a[44:47], v[180:183]\n" \
"s_mul_i32 s70, s70, 17408\n" \
"v_mfma_f32_16x16x32_bf16 v[176:179], a[64:67], a[40:43], v[176:179]\n" \
"Lpg585_%=:\n" \
"Lst0x0_%=:\n" \
"s_cbranch_execz Lpg2766_%=\n" \
"s_add_u32 s87, s87, s70\n" \
"v_mfma_f32_16x16x32_bf16 v[176:179], a[68:71], a[44:47], v[176:179]\n" \
"s_waitcnt lgkmcnt(0)\n" \
"s_and_b32 s70, s61, 1\n" \
"v_mfma_f32_16x16x32_bf16 v[192:195], a[64:67], a[48:51], v[192:195]\n" \
"s_lshl_b32 s70, s70, 12\n" \
"v_mfma_f32_16x16x32_bf16 v[192:195], a[68:71], a[52:55], v[192:195]\n" \
"s_add_u32 s87, s87, s70\n" \
"v_mfma_f32_16x16x32_bf16 v[196:199], a[72:75], a[48:51], v[196:199]\n" \
"s_mul_i32 s70, s63, 34816\n" \
"v_mfma_f32_16x16x32_bf16 v[196:199], a[76:79], a[52:55], v[196:199]\n" \
"s_add_u32 s94, s87, 522240\n" \
"v_mfma_f32_16x16x32_bf16 v[200:203], a[80:83], a[48:51], v[200:203]\n" \
"s_sub_u32 s94, s94, s70\n" \
"v_mfma_f32_16x16x32_bf16 v[200:203], a[84:87], a[52:55], v[200:203]\n" \
"s_add_u32 s87, s87, s70\n" \
"v_mfma_f32_16x16x32_bf16 v[204:207], a[88:91], a[48:51], v[204:207]\n" \
"s_lshl_b32 s90, s62, 7\n" \
"v_mfma_f32_16x16x32_bf16 v[204:207], a[92:95], a[52:55], v[204:207]\n" \
"s_add_u32 s95, s90, 65597440\n" \
"v_mfma_f32_16x16x32_bf16 v[220:223], a[88:91], a[56:59], v[220:223]\n" \
"s_add_u32 s90, s90, 65585152\n" \
"v_mfma_f32_16x16x32_bf16 v[220:223], a[92:95], a[60:63], v[220:223]\n" \
"s_lshl_b32 s92, 0xff, s63\n" \
"v_mfma_f32_16x16x32_bf16 v[216:219], a[80:83], a[56:59], v[216:219]\n" \
"s_and_b32 s92, s92, 0xff\n" \
"v_mfma_f32_16x16x32_bf16 v[216:219], a[84:87], a[60:63], v[216:219]\n" \
"s_mov_b32 s76, 0x3e0293ee\n" \
"v_mfma_f32_16x16x32_bf16 v[212:215], a[72:75], a[56:59], v[212:215]\n" \
"s_mov_b32 s31, 0x3e800000\n" \
"v_mfma_f32_16x16x32_bf16 v[212:215], a[76:79], a[60:63], v[212:215]\n" \
"v_mfma_f32_16x16x32_bf16 v[208:211], a[64:67], a[56:59], v[208:211]\n" \
"v_mfma_f32_16x16x32_bf16 v[208:211], a[68:71], a[60:63], v[208:211]\n" \
"s_waitcnt vmcnt(10) lgkmcnt(0)\n" \
"s_barrier\n" \
"v_readfirstlane_b32 s60, v240\n" \
"s_add_u32 s60, s60, 0x9e3779b9\n" \
"v_mfma_f32_16x16x32_bf16 a[128:131], a[0:3], a[96:99], a[128:131]\n" \
"s_add_u32 m0, s74, 131072\n" \
"ds_read_b64_tr_b16 a[64:65], v98 offset:32768\n" \
"v_mfma_f32_16x16x32_bf16 a[128:131], a[4:7], a[100:103], a[128:131]\n" \
"buffer_load_dwordx4 v64, s[36:39], s81 offen offset:0 lds\n" \
"v_mfma_f32_16x16x32_bf16 a[132:135], a[0:3], a[104:107], a[132:135]\n" \
"ds_read_b64_tr_b16 a[66:67], v98 offset:36864\n" \
"v_mfma_f32_16x16x32_bf16 a[132:135], a[4:7], a[108:111], a[132:135]\n" \
"v_mfma_f32_16x16x32_bf16 a[136:139], a[0:3], a[112:115], a[136:139]\n" \
"ds_read_b64_tr_b16 a[68:69], v98 offset:49152\n" \
"v_mfma_f32_16x16x32_bf16 a[136:139], a[4:7], a[116:119], a[136:139]\n" \
"buffer_load_dwordx4 v65, s[36:39], s81 offen offset:1024 lds\n" \
"v_mfma_f32_16x16x32_bf16 a[140:143], a[0:3], a[120:123], a[140:143]\n" \
"ds_read_b64_tr_b16 a[70:71], v98 offset:53248\n" \
"v_mfma_f32_16x16x32_bf16 a[140:143], a[4:7], a[124:127], a[140:143]\n" \
"v_mfma_f32_16x16x32_bf16 a[156:159], a[8:11], a[120:123], a[156:159]\n" \
"ds_read_b128 a[0:3], v72 offset:32768\n" \
"v_mfma_f32_16x16x32_bf16 a[156:159], a[12:15], a[124:127], a[156:159]\n" \
"buffer_load_dwordx4 v66, s[36:39], s81 offen offset:2048 lds\n" \
"v_mfma_f32_16x16x32_bf16 a[152:155], a[8:11], a[112:115], a[152:155]\n" \
"ds_read_b64_tr_b16 a[72:73], v99 offset:32768\n" \
"v_mfma_f32_16x16x32_bf16 a[152:155], a[12:15], a[116:119], a[152:155]\n" \
"v_mfma_f32_16x16x32_bf16 a[148:151], a[8:11], a[104:107], a[148:151]\n" \
"ds_read_b128 a[4:7], v73 offset:32768\n" \
"v_mfma_f32_16x16x32_bf16 a[148:151], a[12:15], a[108:111], a[148:151]\n" \
"buffer_load_dwordx4 v67, s[36:39], s81 offen offset:3072 lds\n" \
"v_mfma_f32_16x16x32_bf16 a[144:147], a[8:11], a[96:99], a[144:147]\n" \
"ds_read_b64_tr_b16 a[74:75], v99 offset:36864\n" \
"v_mfma_f32_16x16x32_bf16 a[144:147], a[12:15], a[100:103], a[144:147]\n" \
"s_xor_b32 s81, s81, 0x380\n" \
"v_mfma_f32_16x16x32_bf16 a[160:163], a[16:19], a[96:99], a[160:163]\n" \
"s_add_u32 m0, s74, 135168\n" \
"ds_read_b128 a[8:11], v72 offset:34816\n" \
"v_mfma_f32_16x16x32_bf16 a[160:163], a[20:23], a[100:103], a[160:163]\n" \
"buffer_load_dwordx4 v64, s[36:39], s82 offen offset:0 lds\n" \
"v_mfma_f32_16x16x32_bf16 a[164:167], a[16:19], a[104:107], a[164:167]\n" \
"ds_read_b64_tr_b16 a[76:77], v99 offset:49152\n" \
"v_mfma_f32_16x16x32_bf16 a[164:167], a[20:23], a[108:111], a[164:167]\n" \
"v_mfma_f32_16x16x32_bf16 a[168:171], a[16:19], a[112:115], a[168:171]\n" \
"ds_read_b128 a[12:15], v73 offset:34816\n" \
"v_mfma_f32_16x16x32_bf16 a[168:171], a[20:23], a[116:119], a[168:171]\n" \
"buffer_load_dwordx4 v65, s[36:39], s82 offen offset:1024 lds\n" \
"v_mfma_f32_16x16x32_bf16 a[172:175], a[16:19], a[120:123], a[172:175]\n" \
"ds_read_b64_tr_b16 a[78:79], v99 offset:53248\n" \
"v_mfma_f32_16x16x32_bf16 a[172:175], a[20:23], a[124:127], a[172:175]\n" \
"v_mfma_f32_16x16x32_bf16 a[188:191], a[24:27], a[120:123], a[188:191]\n" \
"ds_read_b128 a[16:19], v72 offset:36864\n" \
"v_mfma_f32_16x16x32_bf16 a[188:191], a[28:31], a[124:127], a[188:191]\n" \
"buffer_load_dwordx4 v66, s[36:39], s82 offen offset:2048 lds\n" \
"v_mfma_f32_16x16x32_bf16 a[184:187], a[24:27], a[112:115], a[184:187]\n" \
"ds_read_b64_tr_b16 a[80:81], v98 offset:33280\n" \
"v_mfma_f32_16x16x32_bf16 a[184:187], a[28:31], a[116:119], a[184:187]\n" \
"v_mfma_f32_16x16x32_bf16 a[180:183], a[24:27], a[104:107], a[180:183]\n" \
"ds_read_b128 a[20:23], v73 offset:36864\n" \
"v_mfma_f32_16x16x32_bf16 a[180:183], a[28:31], a[108:111], a[180:183]\n" \
"buffer_load_dwordx4 v67, s[36:39], s82 offen offset:3072 lds\n" \
"v_mfma_f32_16x16x32_bf16 a[176:179], a[24:27], a[96:99], a[176:179]\n" \
"s_mov_b64 s[64:65], s[14:15]\n" \
"s_add_u32 s68, s67, 0x300000\n" \
"ds_read_b64_tr_b16 a[82:83], v98 offset:37376\n" \
"v_mfma_f32_16x16x32_bf16 a[176:179], a[28:31], a[100:103], a[176:179]\n" \
"s_add_u32 s64, s64, s68\n" \
"s_addc_u32 s65, s65, 0\n" \
"v_mfma_f32_16x16x32_bf16 a[192:195], a[32:35], a[96:99], a[192:195]\n" \
"s_xor_b32 s82, s82, 0x380\n" \
"ds_read_b128 a[24:27], v72 offset:38912\n" \
"v_mfma_f32_16x16x32_bf16 a[192:195], a[36:39], a[100:103], a[192:195]\n" \
"global_load_dword v238, v234, s[64:65]\n" \
"v_mfma_f32_16x16x32_bf16 a[196:199], a[32:35], a[104:107], a[196:199]\n" \
"s_mov_b64 s[64:65], s[16:17]\n" \
"s_add_u32 s68, s67, 0x300000\n" \
"ds_read_b64_tr_b16 a[84:85], v98 offset:49664\n" \
"v_mfma_f32_16x16x32_bf16 a[196:199], a[36:39], a[108:111], a[196:199]\n" \
"s_add_u32 s64, s64, s68\n" \
"s_addc_u32 s65, s65, 0\n" \
"v_mfma_f32_16x16x32_bf16 a[200:203], a[32:35], a[112:115], a[200:203]\n" \
"ds_read_b128 a[28:31], v73 offset:38912\n" \
"v_mfma_f32_16x16x32_bf16 a[200:203], a[36:39], a[116:119], a[200:203]\n" \
"global_load_dword v238, v234, s[64:65]\n" \
"v_mfma_f32_16x16x32_bf16 a[204:207], a[32:35], a[120:123], a[204:207]\n" \
"ds_read_b64_tr_b16 a[86:87], v98 offset:53760\n" \
"v_mfma_f32_16x16x32_bf16 a[204:207], a[36:39], a[124:127], a[204:207]\n" \
"v_mfma_f32_16x16x32_bf16 a[220:223], a[40:43], a[120:123], a[220:223]\n" \
"ds_read_b128 a[32:35], v72 offset:40960\n" \
"v_mfma_f32_16x16x32_bf16 a[220:223], a[44:47], a[124:127], a[220:223]\n" \
"v_mfma_f32_16x16x32_bf16 a[216:219], a[40:43], a[112:115], a[216:219]\n" \
"ds_read_b64_tr_b16 a[88:89], v99 offset:33280\n" \
"v_mfma_f32_16x16x32_bf16 a[216:219], a[44:47], a[116:119], a[216:219]\n" \
"v_mfma_f32_16x16x32_bf16 a[212:215], a[40:43], a[104:107], a[212:215]\n" \
"ds_read_b128 a[36:39], v73 offset:40960\n" \
"v_mfma_f32_16x16x32_bf16 a[212:215], a[44:47], a[108:111], a[212:215]\n" \
"v_mfma_f32_16x16x32_bf16 a[208:211], a[40:43], a[96:99], a[208:211]\n" \
"ds_read_b64_tr_b16 a[90:91], v99 offset:37376\n" \
"v_mfma_f32_16x16x32_bf16 a[208:211], a[44:47], a[100:103], a[208:211]\n" \
"v_mfma_f32_16x16x32_bf16 a[224:227], a[48:51], a[96:99], a[224:227]\n" \
"ds_read_b128 a[40:43], v72 offset:43008\n" \
"v_mfma_f32_16x16x32_bf16 a[224:227], a[52:55], a[100:103], a[224:227]\n" \
"v_mfma_f32_16x16x32_bf16 a[228:231], a[48:51], a[104:107], a[228:231]\n" \
"ds_read_b64_tr_b16 a[92:93], v99 offset:49664\n" \
"v_mfma_f32_16x16x32_bf16 a[228:231], a[52:55], a[108:111], a[228:231]\n" \
"v_mfma_f32_16x16x32_bf16 a[232:235], a[48:51], a[112:115], a[232:235]\n" \
"ds_read_b128 a[44:47], v73 offset:43008\n" \
"v_mfma_f32_16x16x32_bf16 a[232:235], a[52:55], a[116:119], a[232:235]\n" \
"v_mfma_f32_16x16x32_bf16 a[236:239], a[48:51], a[120:123], a[236:239]\n" \
"ds_read_b64_tr_b16 a[94:95], v99 offset:53760\n" \
"v_mfma_f32_16x16x32_bf16 a[236:239], a[52:55], a[124:127], a[236:239]\n" \
"v_mfma_f32_16x16x32_bf16 a[252:255], a[56:59], a[120:123], a[252:255]\n" \
"ds_read_b128 a[48:51], v72 offset:45056\n" \
"v_mfma_f32_16x16x32_bf16 a[252:255], a[60:63], a[124:127], a[252:255]\n" \
"v_mfma_f32_16x16x32_bf16 a[248:251], a[56:59], a[112:115], a[248:251]\n" \
"v_mfma_f32_16x16x32_bf16 a[248:251], a[60:63], a[116:119], a[248:251]\n" \
"v_mfma_f32_16x16x32_bf16 a[244:247], a[56:59], a[104:107], a[244:247]\n" \
"ds_read_b128 a[52:55], v73 offset:45056\n" \
"v_mfma_f32_16x16x32_bf16 a[244:247], a[60:63], a[108:111], a[244:247]\n" \
"v_mfma_f32_16x16x32_bf16 a[240:243], a[56:59], a[96:99], a[240:243]\n" \
"v_mfma_f32_16x16x32_bf16 a[240:243], a[60:63], a[100:103], a[240:243]\n" \
"s_waitcnt lgkmcnt(1)\n" \
"v_mfma_f32_16x16x32_bf16 v[0:3], a[64:67], a[0:3], v[0:3]\n" \
"ds_read_b128 a[56:59], v72 offset:47104\n" \
"v_lshrrev_b32 v104, 4, v234\n" \
"v_mfma_f32_16x16x32_bf16 v[0:3], a[68:71], a[4:7], v[0:3]\n" \
"v_bfe_u32 v105, v104, 4, 1\n" \
"v_bfe_u32 v106, v104, 2, 2\n" \
"v_mfma_f32_16x16x32_bf16 v[4:7], a[72:75], a[0:3], v[4:7]\n" \
"s_add_u32 m0, s74, 65536\n" \
"ds_read_b128 a[60:63], v73 offset:47104\n" \
"v_lshl_add_u32 v106, v105, 3, v106\n" \
"v_mfma_f32_16x16x32_bf16 v[4:7], a[76:79], a[4:7], v[4:7]\n" \
"buffer_load_dwordx4 v68, s[40:43], s83 offen offset:0 lds\n" \
"v_mfma_f32_16x16x32_bf16 v[8:11], a[80:83], a[0:3], v[8:11]\n" \
"s_lshl_b32 s71, s61, 4\n" \
"ds_read_b64_tr_b16 a[96:97], v98 offset:33792\n" \
"v_add_u32 v106, s71, v106\n" \
"v_mfma_f32_16x16x32_bf16 v[8:11], a[84:87], a[4:7], v[8:11]\n" \
"v_mul_u32_u24 v106, 6144, v106\n" \
"v_lshlrev_b32 v105, 1, v105\n" \
"v_mfma_f32_16x16x32_bf16 v[12:15], a[88:91], a[0:3], v[12:15]\n" \
"ds_read_b64_tr_b16 a[98:99], v98 offset:37888\n" \
"v_and_b32 v107, 3, v104\n" \
"v_mfma_f32_16x16x32_bf16 v[12:15], a[92:95], a[4:7], v[12:15]\n" \
"buffer_load_dwordx4 v69, s[44:47], s83 offen offset:1024 lds\n" \
"v_mfma_f32_16x16x32_bf16 v[28:31], a[88:91], a[8:11], v[28:31]\n" \
"ds_read_b64_tr_b16 a[100:101], v98 offset:50176\n" \
"v_xor_b32 v105, v105, v107\n" \
"v_mfma_f32_16x16x32_bf16 v[28:31], a[92:95], a[12:15], v[28:31]\n" \
"v_lshl_add_u32 v106, v105, 4, v106\n" \
"v_lshrrev_b32 v105, 5, v104\n" \
"v_mfma_f32_16x16x32_bf16 v[24:27], a[80:83], a[8:11], v[24:27]\n" \
"ds_read_b64_tr_b16 a[102:103], v98 offset:54272\n" \
"v_lshl_add_u32 v244, v105, 6, v106\n" \
"v_mfma_f32_16x16x32_bf16 v[24:27], a[84:87], a[12:15], v[24:27]\n" \
"buffer_load_dwordx4 v70, s[40:43], s83 offen offset:2048 lds\n" \
"v_mfma_f32_16x16x32_bf16 v[20:23], a[72:75], a[8:11], v[20:23]\n" \
"ds_read_b64_tr_b16 a[104:105], v99 offset:33792\n" \
"v_lshrrev_b32 v107, 3, v104\n" \
"v_mfma_f32_16x16x32_bf16 v[20:23], a[76:79], a[12:15], v[20:23]\n" \
"v_and_b32 v108, 7, v104\n" \
"v_xor_b32 v109, v108, v107\n" \
"v_mfma_f32_16x16x32_bf16 v[16:19], a[64:67], a[8:11], v[16:19]\n" \
"ds_read_b64_tr_b16 a[106:107], v99 offset:37888\n" \
"v_lshlrev_b32 v109, 4, v109\n" \
"v_mfma_f32_16x16x32_bf16 v[16:19], a[68:71], a[12:15], v[16:19]\n" \
"buffer_load_dwordx4 v71, s[44:47], s83 offen offset:3072 lds\n" \
"v_mfma_f32_16x16x32_bf16 v[32:35], a[64:67], a[16:19], v[32:35]\n" \
"ds_read_b64_tr_b16 a[108:109], v99 offset:50176\n" \
"v_lshl_add_u32 v245, v107, 12, v109\n" \
"v_mfma_f32_16x16x32_bf16 v[32:35], a[68:71], a[20:23], v[32:35]\n" \
"s_xor_b32 s83, s83, 0x80000\n" \
"v_mfma_f32_16x16x32_bf16 v[36:39], a[72:75], a[16:19], v[36:39]\n" \
"s_add_u32 m0, s74, 69632\n" \
"ds_read_b64_tr_b16 a[110:111], v99 offset:54272\n" \
"v_mfma_f32_16x16x32_bf16 v[36:39], a[76:79], a[20:23], v[36:39]\n" \
"buffer_load_dwordx4 v68, s[40:43], s84 offen offset:0 lds\n" \
"v_mfma_f32_16x16x32_bf16 v[40:43], a[80:83], a[16:19], v[40:43]\n" \
"s_and_b32 s70, s61, 1\n" \
"ds_read_b64_tr_b16 a[112:113], v98 offset:34304\n" \
"v_mfma_f32_16x16x32_bf16 v[40:43], a[84:87], a[20:23], v[40:43]\n" \
"s_lshl_b32 s71, s70, 2\n" \
"v_add_u32 v107, s71, v107\n" \
"v_xor_b32 v109, v108, v107\n" \
"v_mfma_f32_16x16x32_bf16 v[44:47], a[88:91], a[16:19], v[44:47]\n" \
"ds_read_b64_tr_b16 a[114:115], v98 offset:38400\n" \
"v_lshlrev_b32 v109, 4, v109\n" \
"v_mfma_f32_16x16x32_bf16 v[44:47], a[92:95], a[20:23], v[44:47]\n" \
"buffer_load_dwordx4 v69, s[44:47], s84 offen offset:1024 lds\n" \
"v_mfma_f32_16x16x32_bf16 v[60:63], a[88:91], a[24:27], v[60:63]\n" \
"ds_read_b64_tr_b16 a[116:117], v98 offset:50688\n" \
"v_lshl_add_u32 v246, v107, 12, v109\n" \
"v_mfma_f32_16x16x32_bf16 v[60:63], a[92:95], a[28:31], v[60:63]\n" \
"s_lshr_b32 s71, s61, 1\n" \
"v_mfma_f32_16x16x32_bf16 v[56:59], a[80:83], a[24:27], v[56:59]\n" \
"s_lshl_b32 s70, s71, 11\n" \
"ds_read_b64_tr_b16 a[118:119], v98 offset:54784\n" \
"v_lshl_add_u32 v246, v105, 30, v246\n" \
"v_mfma_f32_16x16x32_bf16 v[56:59], a[84:87], a[28:31], v[56:59]\n" \
"buffer_load_dwordx4 v70, s[40:43], s84 offen offset:2048 lds\n" \
"v_mfma_f32_16x16x32_bf16 v[52:55], a[72:75], a[24:27], v[52:55]\n" \
"ds_read_b64_tr_b16 a[120:121], v99 offset:34304\n" \
"v_add_u32 v246, s70, v246\n" \
"v_mfma_f32_16x16x32_bf16 v[52:55], a[76:79], a[28:31], v[52:55]\n" \
"v_lshrrev_b32 v107, 4, v104\n" \
"v_xor_b32 v107, v107, v108\n" \
"v_mfma_f32_16x16x32_bf16 v[48:51], a[64:67], a[24:27], v[48:51]\n" \
"ds_read_b64_tr_b16 a[122:123], v99 offset:38400\n" \
"v_and_b32 v109, 15, v104\n" \
"v_mfma_f32_16x16x32_bf16 v[48:51], a[68:71], a[28:31], v[48:51]\n" \
"buffer_load_dwordx4 v71, s[44:47], s84 offen offset:3072 lds\n" \
"v_mfma_f32_16x16x32_bf16 v[144:147], a[64:67], a[32:35], v[144:147]\n" \
"ds_read_b64_tr_b16 a[124:125], v99 offset:50688\n" \
"v_lshlrev_b32 v106, 7, v109\n" \
"v_mfma_f32_16x16x32_bf16 v[144:147], a[68:71], a[36:39], v[144:147]\n" \
"v_lshl_add_u32 v106, v107, 4, v106\n" \
"v_mfma_f32_16x16x32_bf16 v[148:151], a[72:75], a[32:35], v[148:151]\n" \
"s_xor_b32 s84, s84, 0x80000\n" \
"ds_read_b64_tr_b16 a[126:127], v99 offset:54784\n" \
"v_mfma_f32_16x16x32_bf16 v[148:151], a[76:79], a[36:39], v[148:151]\n" \
"s_add_u32 s70, s70, %3\n" \
"v_add_u32 v247, s70, v106\n" \
"v_lshrrev_b32 v109, 2, v109\n" \
"v_mfma_f32_16x16x32_bf16 v[152:155], a[80:83], a[32:35], v[152:155]\n" \
"v_lshl_add_u32 v109, v109, 9, v106\n" \
"v_mfma_f32_16x16x32_bf16 v[152:155], a[84:87], a[36:39], v[152:155]\n" \
"s_add_u32 s70, %3, 16384\n" \
"v_add_u32 v248, s70, v109\n" \
"v_and_b32 v106, 3, v104\n" \
"v_mfma_f32_16x16x32_bf16 v[156:159], a[88:91], a[32:35], v[156:159]\n" \
"v_lshlrev_b32 v106, 3, v106\n" \
"v_bfe_u32 v107, v104, 2, 2\n" \
"v_mfma_f32_16x16x32_bf16 v[156:159], a[92:95], a[36:39], v[156:159]\n" \
"v_lshl_add_u32 v106, v107, 6, v106\n" \
"v_bfe_u32 v107, v104, 4, 1\n" \
"v_mfma_f32_16x16x32_bf16 v[188:191], a[88:91], a[40:43], v[188:191]\n" \
"v_mul_u32_u24 v107, 288, v107\n" \
"v_mul_u32_u24 v108, 6144, v105\n" \
"v_mfma_f32_16x16x32_bf16 v[188:191], a[92:95], a[44:47], v[188:191]\n" \
"v_add3_u32 v106, v106, v107, v108\n" \
"v_mfma_f32_16x16x32_bf16 v[184:187], a[80:83], a[40:43], v[184:187]\n" \
"s_and_b32 s70, s61, 1\n" \
"v_mfma_f32_16x16x32_bf16 v[184:187], a[84:87], a[44:47], v[184:187]\n" \
"s_lshl_b32 s70, s70, 9\n" \
"v_mfma_f32_16x16x32_bf16 v[180:183], a[72:75], a[40:43], v[180:183]\n" \
"s_add_u32 s70, s70, %3\n" \
"v_add_u32 v249, s70, v106\n" \
"v_mfma_f32_16x16x32_bf16 v[180:183], a[76:79], a[44:47], v[180:183]\n" \
"s_lshl_b32 s70, s71, 5\n" \
"v_xor_b32 v249, s70, v249\n" \
"v_mfma_f32_16x16x32_bf16 v[176:179], a[64:67], a[40:43], v[176:179]\n" \
"v_mfma_f32_16x16x32_bf16 v[176:179], a[68:71], a[44:47], v[176:179]\n" \
"s_waitcnt lgkmcnt(0)\n" \
"v_mfma_f32_16x16x32_bf16 v[192:195], a[64:67], a[48:51], v[192:195]\n" \
"v_mfma_f32_16x16x32_bf16 v[192:195], a[68:71], a[52:55], v[192:195]\n" \
"v_mfma_f32_16x16x32_bf16 v[196:199], a[72:75], a[48:51], v[196:199]\n" \
"v_mfma_f32_16x16x32_bf16 v[196:199], a[76:79], a[52:55], v[196:199]\n" \
"v_mfma_f32_16x16x32_bf16 v[200:203], a[80:83], a[48:51], v[200:203]\n" \
"v_mfma_f32_16x16x32_bf16 v[200:203], a[84:87], a[52:55], v[200:203]\n" \
"v_mfma_f32_16x16x32_bf16 v[204:207], a[88:91], a[48:51], v[204:207]\n" \
"v_mfma_f32_16x16x32_bf16 v[204:207], a[92:95], a[52:55], v[204:207]\n" \
"v_mfma_f32_16x16x32_bf16 v[220:223], a[88:91], a[56:59], v[220:223]\n" \
"v_mfma_f32_16x16x32_bf16 v[220:223], a[92:95], a[60:63], v[220:223]\n" \
"v_mfma_f32_16x16x32_bf16 v[216:219], a[80:83], a[56:59], v[216:219]\n" \
"v_mfma_f32_16x16x32_bf16 v[216:219], a[84:87], a[60:63], v[216:219]\n" \
"v_mfma_f32_16x16x32_bf16 v[212:215], a[72:75], a[56:59], v[212:215]\n" \
"v_mfma_f32_16x16x32_bf16 v[212:215], a[76:79], a[60:63], v[212:215]\n" \
"v_mfma_f32_16x16x32_bf16 v[208:211], a[64:67], a[56:59], v[208:211]\n" \
"v_mfma_f32_16x16x32_bf16 v[208:211], a[68:71], a[60:63], v[208:211]\n" \
"s_waitcnt vmcnt(10) lgkmcnt(0)\n" \
"s_barrier\n" \
"v_mfma_f32_16x16x32_bf16 a[128:131], a[0:3], a[96:99], a[128:131]\n" \
"s_add_u32 m0, s74, 98304\n" \
"ds_read_b64_tr_b16 a[64:65], v74 offset:0\n" \
"v_mfma_f32_16x16x32_bf16 a[128:131], a[4:7], a[100:103], a[128:131]\n" \
"buffer_load_dwordx4 v64, s[36:39], s81 offen offset:0 lds\n" \
"v_mfma_f32_16x16x32_bf16 a[132:135], a[0:3], a[104:107], a[132:135]\n" \
"ds_read_b64_tr_b16 a[66:67], v74 offset:4096\n" \
"v_mfma_f32_16x16x32_bf16 a[132:135], a[4:7], a[108:111], a[132:135]\n" \
"v_mfma_f32_16x16x32_bf16 a[136:139], a[0:3], a[112:115], a[136:139]\n" \
"ds_read_b64_tr_b16 a[68:69], v74 offset:16384\n" \
"v_mfma_f32_16x16x32_bf16 a[136:139], a[4:7], a[116:119], a[136:139]\n" \
"buffer_load_dwordx4 v65, s[36:39], s81 offen offset:1024 lds\n" \
"v_mfma_f32_16x16x32_bf16 a[140:143], a[0:3], a[120:123], a[140:143]\n" \
"ds_read_b64_tr_b16 a[70:71], v74 offset:20480\n" \
"v_mfma_f32_16x16x32_bf16 a[140:143], a[4:7], a[124:127], a[140:143]\n" \
"v_mfma_f32_16x16x32_bf16 a[156:159], a[8:11], a[120:123], a[156:159]\n" \
"ds_read_b128 a[0:3], v100 offset:0\n" \
"v_mfma_f32_16x16x32_bf16 a[156:159], a[12:15], a[124:127], a[156:159]\n" \
"buffer_load_dwordx4 v66, s[36:39], s81 offen offset:2048 lds\n" \
"v_mfma_f32_16x16x32_bf16 a[152:155], a[8:11], a[112:115], a[152:155]\n" \
"ds_read_b64_tr_b16 a[72:73], v75 offset:0\n" \
"v_mfma_f32_16x16x32_bf16 a[152:155], a[12:15], a[116:119], a[152:155]\n" \
"v_mfma_f32_16x16x32_bf16 a[148:151], a[8:11], a[104:107], a[148:151]\n" \
"ds_read_b128 a[4:7], v101 offset:0\n" \
"v_mfma_f32_16x16x32_bf16 a[148:151], a[12:15], a[108:111], a[148:151]\n" \
"buffer_load_dwordx4 v67, s[36:39], s81 offen offset:3072 lds\n" \
"v_mfma_f32_16x16x32_bf16 a[144:147], a[8:11], a[96:99], a[144:147]\n" \
"ds_read_b64_tr_b16 a[74:75], v75 offset:4096\n" \
"v_mfma_f32_16x16x32_bf16 a[144:147], a[12:15], a[100:103], a[144:147]\n" \
"s_xor_b32 s81, s81, 0x80\n" \
"v_mfma_f32_16x16x32_bf16 a[160:163], a[16:19], a[96:99], a[160:163]\n" \
"s_add_u32 m0, s74, 102400\n" \
"ds_read_b128 a[8:11], v100 offset:2048\n" \
"v_mfma_f32_16x16x32_bf16 a[160:163], a[20:23], a[100:103], a[160:163]\n" \
"buffer_load_dwordx4 v64, s[36:39], s82 offen offset:0 lds\n" \
"v_mfma_f32_16x16x32_bf16 a[164:167], a[16:19], a[104:107], a[164:167]\n" \
"ds_read_b64_tr_b16 a[76:77], v75 offset:16384\n" \
"v_mfma_f32_16x16x32_bf16 a[164:167], a[20:23], a[108:111], a[164:167]\n" \
"v_mfma_f32_16x16x32_bf16 a[168:171], a[16:19], a[112:115], a[168:171]\n" \
"ds_read_b128 a[12:15], v101 offset:2048\n" \
"v_mfma_f32_16x16x32_bf16 a[168:171], a[20:23], a[116:119], a[168:171]\n" \
"buffer_load_dwordx4 v65, s[36:39], s82 offen offset:1024 lds\n" \
"v_mfma_f32_16x16x32_bf16 a[172:175], a[16:19], a[120:123], a[172:175]\n" \
"ds_read_b64_tr_b16 a[78:79], v75 offset:20480\n" \
"v_mfma_f32_16x16x32_bf16 a[172:175], a[20:23], a[124:127], a[172:175]\n" \
"v_mfma_f32_16x16x32_bf16 a[188:191], a[24:27], a[120:123], a[188:191]\n" \
"ds_read_b128 a[16:19], v100 offset:4096\n" \
"v_mfma_f32_16x16x32_bf16 a[188:191], a[28:31], a[124:127], a[188:191]\n" \
"buffer_load_dwordx4 v66, s[36:39], s82 offen offset:2048 lds\n" \
"v_mfma_f32_16x16x32_bf16 a[184:187], a[24:27], a[112:115], a[184:187]\n" \
"ds_read_b64_tr_b16 a[80:81], v74 offset:512\n" \
"v_mfma_f32_16x16x32_bf16 a[184:187], a[28:31], a[116:119], a[184:187]\n" \
"v_mfma_f32_16x16x32_bf16 a[180:183], a[24:27], a[104:107], a[180:183]\n" \
"ds_read_b128 a[20:23], v101 offset:4096\n" \
"v_mfma_f32_16x16x32_bf16 a[180:183], a[28:31], a[108:111], a[180:183]\n" \
"buffer_load_dwordx4 v67, s[36:39], s82 offen offset:3072 lds\n" \
"v_mfma_f32_16x16x32_bf16 a[176:179], a[24:27], a[96:99], a[176:179]\n" \
"s_mov_b64 s[64:65], s[8:9]\n" \
"s_add_u32 s68, s66, 0x0\n" \
"ds_read_b64_tr_b16 a[82:83], v74 offset:4608\n" \
"v_mfma_f32_16x16x32_bf16 a[176:179], a[28:31], a[100:103], a[176:179]\n" \
"s_add_u32 s64, s64, s68\n" \
"s_addc_u32 s65, s65, 0\n" \
"v_mfma_f32_16x16x32_bf16 a[192:195], a[32:35], a[96:99], a[192:195]\n" \
"s_xor_b32 s82, s82, 0x80\n" \
"ds_read_b128 a[24:27], v100 offset:6144\n" \
"v_mfma_f32_16x16x32_bf16 a[192:195], a[36:39], a[100:103], a[192:195]\n" \
"global_load_dword v238, v234, s[64:65]\n" \
"v_mfma_f32_16x16x32_bf16 a[196:199], a[32:35], a[104:107], a[196:199]\n" \
"s_mov_b64 s[64:65], s[8:9]\n" \
"s_add_u32 s68, s66, 0x100000\n" \
"ds_read_b64_tr_b16 a[84:85], v74 offset:16896\n" \
"v_mfma_f32_16x16x32_bf16 a[196:199], a[36:39], a[108:111], a[196:199]\n" \
"s_add_u32 s64, s64, s68\n" \
"s_addc_u32 s65, s65, 0\n" \
"v_mfma_f32_16x16x32_bf16 a[200:203], a[32:35], a[112:115], a[200:203]\n" \
"ds_read_b128 a[28:31], v101 offset:6144\n" \
"v_mfma_f32_16x16x32_bf16 a[200:203], a[36:39], a[116:119], a[200:203]\n" \
"global_load_dword v238, v234, s[64:65]\n" \
"v_mfma_f32_16x16x32_bf16 a[204:207], a[32:35], a[120:123], a[204:207]\n" \
"ds_read_b64_tr_b16 a[86:87], v74 offset:20992\n" \
"v_mfma_f32_16x16x32_bf16 a[204:207], a[36:39], a[124:127], a[204:207]\n" \
"v_mfma_f32_16x16x32_bf16 a[220:223], a[40:43], a[120:123], a[220:223]\n" \
"ds_read_b128 a[32:35], v100 offset:8192\n" \
"v_mfma_f32_16x16x32_bf16 a[220:223], a[44:47], a[124:127], a[220:223]\n" \
"v_mfma_f32_16x16x32_bf16 a[216:219], a[40:43], a[112:115], a[216:219]\n" \
"ds_read_b64_tr_b16 a[88:89], v75 offset:512\n" \
"v_mfma_f32_16x16x32_bf16 a[216:219], a[44:47], a[116:119], a[216:219]\n" \
"v_mfma_f32_16x16x32_bf16 a[212:215], a[40:43], a[104:107], a[212:215]\n" \
"ds_read_b128 a[36:39], v101 offset:8192\n" \
"v_mfma_f32_16x16x32_bf16 a[212:215], a[44:47], a[108:111], a[212:215]\n" \
"v_mfma_f32_16x16x32_bf16 a[208:211], a[40:43], a[96:99], a[208:211]\n" \
"ds_read_b64_tr_b16 a[90:91], v75 offset:4608\n" \
"v_mfma_f32_16x16x32_bf16 a[208:211], a[44:47], a[100:103], a[208:211]\n" \
"v_mfma_f32_16x16x32_bf16 a[224:227], a[48:51], a[96:99], a[224:227]\n" \
"ds_read_b128 a[40:43], v100 offset:10240\n" \
"v_mfma_f32_16x16x32_bf16 a[224:227], a[52:55], a[100:103], a[224:227]\n" \
"v_mfma_f32_16x16x32_bf16 a[228:231], a[48:51], a[104:107], a[228:231]\n" \
"ds_read_b64_tr_b16 a[92:93], v75 offset:16896\n" \
"v_mfma_f32_16x16x32_bf16 a[228:231], a[52:55], a[108:111], a[228:231]\n" \
"v_mfma_f32_16x16x32_bf16 a[232:235], a[48:51], a[112:115], a[232:235]\n" \
"ds_read_b128 a[44:47], v101 offset:10240\n" \
"v_mfma_f32_16x16x32_bf16 a[232:235], a[52:55], a[116:119], a[232:235]\n" \
"v_mfma_f32_16x16x32_bf16 a[236:239], a[48:51], a[120:123], a[236:239]\n" \
"ds_read_b64_tr_b16 a[94:95], v75 offset:20992\n" \
"v_mfma_f32_16x16x32_bf16 a[236:239], a[52:55], a[124:127], a[236:239]\n" \
"v_mfma_f32_16x16x32_bf16 a[252:255], a[56:59], a[120:123], a[252:255]\n" \
"ds_read_b128 a[48:51], v100 offset:12288\n" \
"v_mfma_f32_16x16x32_bf16 a[252:255], a[60:63], a[124:127], a[252:255]\n" \
"v_mfma_f32_16x16x32_bf16 a[248:251], a[56:59], a[112:115], a[248:251]\n" \
"v_mfma_f32_16x16x32_bf16 a[248:251], a[60:63], a[116:119], a[248:251]\n" \
"v_mfma_f32_16x16x32_bf16 a[244:247], a[56:59], a[104:107], a[244:247]\n" \
"ds_read_b128 a[52:55], v101 offset:12288\n" \
"v_mfma_f32_16x16x32_bf16 a[244:247], a[60:63], a[108:111], a[244:247]\n" \
"v_mfma_f32_16x16x32_bf16 a[240:243], a[56:59], a[96:99], a[240:243]\n" \
"v_mfma_f32_16x16x32_bf16 a[240:243], a[60:63], a[100:103], a[240:243]\n" \
"s_waitcnt lgkmcnt(1)\n" \
"v_mfma_f32_16x16x32_bf16 v[0:3], a[64:67], a[0:3], v[0:3]\n" \
"ds_read_b128 a[56:59], v100 offset:14336\n" \
"v_mfma_f32_16x16x32_bf16 v[0:3], a[68:71], a[4:7], v[0:3]\n" \
"v_mfma_f32_16x16x32_bf16 v[4:7], a[72:75], a[0:3], v[4:7]\n" \
"s_add_u32 m0, s74, 32768\n" \
"ds_read_b128 a[60:63], v101 offset:14336\n" \
"v_mfma_f32_16x16x32_bf16 v[4:7], a[76:79], a[4:7], v[4:7]\n" \
"buffer_load_dwordx4 v68, s[40:43], s83 offen offset:0 lds\n" \
"v_mfma_f32_16x16x32_bf16 v[8:11], a[80:83], a[0:3], v[8:11]\n" \
"ds_read_b64_tr_b16 a[96:97], v74 offset:1024\n" \
"v_mfma_f32_16x16x32_bf16 v[8:11], a[84:87], a[4:7], v[8:11]\n" \
"v_mfma_f32_16x16x32_bf16 v[12:15], a[88:91], a[0:3], v[12:15]\n" \
"ds_read_b64_tr_b16 a[98:99], v74 offset:5120\n" \
"v_mfma_f32_16x16x32_bf16 v[12:15], a[92:95], a[4:7], v[12:15]\n" \
"buffer_load_dwordx4 v69, s[44:47], s83 offen offset:1024 lds\n" \
"v_mfma_f32_16x16x32_bf16 v[28:31], a[88:91], a[8:11], v[28:31]\n" \
"ds_read_b64_tr_b16 a[100:101], v74 offset:17408\n" \
"v_mfma_f32_16x16x32_bf16 v[28:31], a[92:95], a[12:15], v[28:31]\n" \
"v_mfma_f32_16x16x32_bf16 v[24:27], a[80:83], a[8:11], v[24:27]\n" \
"ds_read_b64_tr_b16 a[102:103], v74 offset:21504\n" \
"v_mfma_f32_16x16x32_bf16 v[24:27], a[84:87], a[12:15], v[24:27]\n" \
"buffer_load_dwordx4 v70, s[40:43], s83 offen offset:2048 lds\n" \
"v_mfma_f32_16x16x32_bf16 v[20:23], a[72:75], a[8:11], v[20:23]\n" \
"ds_read_b64_tr_b16 a[104:105], v75 offset:1024\n" \
"v_mfma_f32_16x16x32_bf16 v[20:23], a[76:79], a[12:15], v[20:23]\n" \
"v_mfma_f32_16x16x32_bf16 v[16:19], a[64:67], a[8:11], v[16:19]\n" \
"ds_read_b64_tr_b16 a[106:107], v75 offset:5120\n" \
"v_mfma_f32_16x16x32_bf16 v[16:19], a[68:71], a[12:15], v[16:19]\n" \
"buffer_load_dwordx4 v71, s[44:47], s83 offen offset:3072 lds\n" \
"v_mfma_f32_16x16x32_bf16 v[32:35], a[64:67], a[16:19], v[32:35]\n" \
"ds_read_b64_tr_b16 a[108:109], v75 offset:17408\n" \
"v_mfma_f32_16x16x32_bf16 v[32:35], a[68:71], a[20:23], v[32:35]\n" \
"s_xor_b32 s83, s83, 0x180000\n" \
"v_mfma_f32_16x16x32_bf16 v[36:39], a[72:75], a[16:19], v[36:39]\n" \
"s_add_u32 m0, s74, 36864\n" \
"ds_read_b64_tr_b16 a[110:111], v75 offset:21504\n" \
"v_mfma_f32_16x16x32_bf16 v[36:39], a[76:79], a[20:23], v[36:39]\n" \
"buffer_load_dwordx4 v68, s[40:43], s84 offen offset:0 lds\n" \
"v_mfma_f32_16x16x32_bf16 v[40:43], a[80:83], a[16:19], v[40:43]\n" \
"ds_read_b64_tr_b16 a[112:113], v74 offset:1536\n" \
"v_mfma_f32_16x16x32_bf16 v[40:43], a[84:87], a[20:23], v[40:43]\n" \
"v_mfma_f32_16x16x32_bf16 v[44:47], a[88:91], a[16:19], v[44:47]\n" \
"ds_read_b64_tr_b16 a[114:115], v74 offset:5632\n" \
"v_mfma_f32_16x16x32_bf16 v[44:47], a[92:95], a[20:23], v[44:47]\n" \
"buffer_load_dwordx4 v69, s[44:47], s84 offen offset:1024 lds\n" \
"v_mfma_f32_16x16x32_bf16 v[60:63], a[88:91], a[24:27], v[60:63]\n" \
"ds_read_b64_tr_b16 a[116:117], v74 offset:17920\n" \
"v_mfma_f32_16x16x32_bf16 v[60:63], a[92:95], a[28:31], v[60:63]\n" \
"v_mfma_f32_16x16x32_bf16 v[56:59], a[80:83], a[24:27], v[56:59]\n" \
"ds_read_b64_tr_b16 a[118:119], v74 offset:22016\n" \
"v_mfma_f32_16x16x32_bf16 v[56:59], a[84:87], a[28:31], v[56:59]\n" \
"buffer_load_dwordx4 v70, s[40:43], s84 offen offset:2048 lds\n" \
"v_mfma_f32_16x16x32_bf16 v[52:55], a[72:75], a[24:27], v[52:55]\n" \
"ds_read_b64_tr_b16 a[120:121], v75 offset:1536\n" \
"v_mfma_f32_16x16x32_bf16 v[52:55], a[76:79], a[28:31], v[52:55]\n" \
"v_mfma_f32_16x16x32_bf16 v[48:51], a[64:67], a[24:27], v[48:51]\n" \
"ds_read_b64_tr_b16 a[122:123], v75 offset:5632\n" \
"v_mfma_f32_16x16x32_bf16 v[48:51], a[68:71], a[28:31], v[48:51]\n" \
"buffer_load_dwordx4 v71, s[44:47], s84 offen offset:3072 lds\n" \
"v_mfma_f32_16x16x32_bf16 v[144:147], a[64:67], a[32:35], v[144:147]\n" \
"ds_read_b64_tr_b16 a[124:125], v75 offset:17920\n" \
"v_mfma_f32_16x16x32_bf16 v[144:147], a[68:71], a[36:39], v[144:147]\n" \
"v_mfma_f32_16x16x32_bf16 v[148:151], a[72:75], a[32:35], v[148:151]\n" \
"s_xor_b32 s84, s84, 0x180000\n" \
"ds_read_b64_tr_b16 a[126:127], v75 offset:22016\n" \
"v_mfma_f32_16x16x32_bf16 v[148:151], a[76:79], a[36:39], v[148:151]\n" \
"v_mfma_f32_16x16x32_bf16 v[152:155], a[80:83], a[32:35], v[152:155]\n" \
"v_mfma_f32_16x16x32_bf16 v[152:155], a[84:87], a[36:39], v[152:155]\n" \
"v_mfma_f32_16x16x32_bf16 v[156:159], a[88:91], a[32:35], v[156:159]\n" \
"v_mfma_f32_16x16x32_bf16 v[156:159], a[92:95], a[36:39], v[156:159]\n" \
"v_mfma_f32_16x16x32_bf16 v[188:191], a[88:91], a[40:43], v[188:191]\n" \
"v_mfma_f32_16x16x32_bf16 v[188:191], a[92:95], a[44:47], v[188:191]\n" \
"v_mfma_f32_16x16x32_bf16 v[184:187], a[80:83], a[40:43], v[184:187]\n" \
"v_mfma_f32_16x16x32_bf16 v[184:187], a[84:87], a[44:47], v[184:187]\n" \
"v_mfma_f32_16x16x32_bf16 v[180:183], a[72:75], a[40:43], v[180:183]\n" \
"v_mfma_f32_16x16x32_bf16 v[180:183], a[76:79], a[44:47], v[180:183]\n" \
"v_mfma_f32_16x16x32_bf16 v[176:179], a[64:67], a[40:43], v[176:179]\n" \
"v_mfma_f32_16x16x32_bf16 v[176:179], a[68:71], a[44:47], v[176:179]\n" \
"s_waitcnt lgkmcnt(0)\n" \
"v_mfma_f32_16x16x32_bf16 v[192:195], a[64:67], a[48:51], v[192:195]\n" \
"v_mfma_f32_16x16x32_bf16 v[192:195], a[68:71], a[52:55], v[192:195]\n" \
"v_mfma_f32_16x16x32_bf16 v[196:199], a[72:75], a[48:51], v[196:199]\n" \
"v_mfma_f32_16x16x32_bf16 v[196:199], a[76:79], a[52:55], v[196:199]\n" \
"v_mfma_f32_16x16x32_bf16 v[200:203], a[80:83], a[48:51], v[200:203]\n" \
"v_mfma_f32_16x16x32_bf16 v[200:203], a[84:87], a[52:55], v[200:203]\n" \
"v_mfma_f32_16x16x32_bf16 v[204:207], a[88:91], a[48:51], v[204:207]\n" \
"v_mfma_f32_16x16x32_bf16 v[204:207], a[92:95], a[52:55], v[204:207]\n" \
"v_mfma_f32_16x16x32_bf16 v[220:223], a[88:91], a[56:59], v[220:223]\n" \
"v_mfma_f32_16x16x32_bf16 v[220:223], a[92:95], a[60:63], v[220:223]\n" \
"v_mfma_f32_16x16x32_bf16 v[216:219], a[80:83], a[56:59], v[216:219]\n" \
"v_mfma_f32_16x16x32_bf16 v[216:219], a[84:87], a[60:63], v[216:219]\n" \
"v_mfma_f32_16x16x32_bf16 v[212:215], a[72:75], a[56:59], v[212:215]\n" \
"v_mfma_f32_16x16x32_bf16 v[212:215], a[76:79], a[60:63], v[212:215]\n" \
"v_mfma_f32_16x16x32_bf16 v[208:211], a[64:67], a[56:59], v[208:211]\n" \
"v_mfma_f32_16x16x32_bf16 v[208:211], a[68:71], a[60:63], v[208:211]\n" \
"s_waitcnt vmcnt(10) lgkmcnt(0)\n" \
"s_barrier\n" \
"v_mfma_f32_16x16x32_bf16 a[128:131], a[0:3], a[96:99], a[128:131]\n" \
"s_add_u32 m0, s74, 0\n" \
"ds_read_b64_tr_b16 a[64:65], v98 offset:0\n" \
"v_mfma_f32_16x16x32_bf16 a[128:131], a[4:7], a[100:103], a[128:131]\n" \
"buffer_load_dwordx4 v64, s[36:39], s81 offen offset:0 lds\n" \
"v_mfma_f32_16x16x32_bf16 a[132:135], a[0:3], a[104:107], a[132:135]\n" \
"ds_read_b64_tr_b16 a[66:67], v98 offset:4096\n" \
"v_mfma_f32_16x16x32_bf16 a[132:135], a[4:7], a[108:111], a[132:135]\n" \
"v_mfma_f32_16x16x32_bf16 a[136:139], a[0:3], a[112:115], a[136:139]\n" \
"ds_read_b64_tr_b16 a[68:69], v98 offset:16384\n" \
"v_mfma_f32_16x16x32_bf16 a[136:139], a[4:7], a[116:119], a[136:139]\n" \
"buffer_load_dwordx4 v65, s[36:39], s81 offen offset:1024 lds\n" \
"v_mfma_f32_16x16x32_bf16 a[140:143], a[0:3], a[120:123], a[140:143]\n" \
"ds_read_b64_tr_b16 a[70:71], v98 offset:20480\n" \
"v_mfma_f32_16x16x32_bf16 a[140:143], a[4:7], a[124:127], a[140:143]\n" \
"v_mfma_f32_16x16x32_bf16 a[156:159], a[8:11], a[120:123], a[156:159]\n" \
"ds_read_b128 a[0:3], v96 offset:32768\n" \
"v_mfma_f32_16x16x32_bf16 a[156:159], a[12:15], a[124:127], a[156:159]\n" \
"buffer_load_dwordx4 v66, s[36:39], s81 offen offset:2048 lds\n" \
"v_mfma_f32_16x16x32_bf16 a[152:155], a[8:11], a[112:115], a[152:155]\n" \
"ds_read_b64_tr_b16 a[72:73], v99 offset:0\n" \
"v_mfma_f32_16x16x32_bf16 a[152:155], a[12:15], a[116:119], a[152:155]\n" \
"v_mfma_f32_16x16x32_bf16 a[148:151], a[8:11], a[104:107], a[148:151]\n" \
"ds_read_b128 a[4:7], v97 offset:32768\n" \
"v_mfma_f32_16x16x32_bf16 a[148:151], a[12:15], a[108:111], a[148:151]\n" \
"buffer_load_dwordx4 v67, s[36:39], s81 offen offset:3072 lds\n" \
"v_mfma_f32_16x16x32_bf16 a[144:147], a[8:11], a[96:99], a[144:147]\n" \
"ds_read_b64_tr_b16 a[74:75], v99 offset:4096\n" \
"Lpg1132_%=:\n" \
"Lst0x1_%=:\n" \
"s_cbranch_execz Lpg3342_%=\n" \
"v_mfma_f32_16x16x32_bf16 a[144:147], a[12:15], a[100:103], a[144:147]\n" \
"s_xor_b32 s81, s81, 0x180\n" \
"v_mfma_f32_16x16x32_bf16 a[160:163], a[16:19], a[96:99], a[160:163]\n" \
"s_add_u32 m0, s74, 4096\n" \
"ds_read_b128 a[8:11], v96 offset:34816\n" \
"v_mfma_f32_16x16x32_bf16 a[160:163], a[20:23], a[100:103], a[160:163]\n" \
"buffer_load_dwordx4 v64, s[36:39], s82 offen offset:0 lds\n" \
"v_mfma_f32_16x16x32_bf16 a[164:167], a[16:19], a[104:107], a[164:167]\n" \
"ds_read_b64_tr_b16 a[76:77], v99 offset:16384\n" \
"v_mfma_f32_16x16x32_bf16 a[164:167], a[20:23], a[108:111], a[164:167]\n" \
"v_mfma_f32_16x16x32_bf16 a[168:171], a[16:19], a[112:115], a[168:171]\n" \
"ds_read_b128 a[12:15], v97 offset:34816\n" \
"v_mfma_f32_16x16x32_bf16 a[168:171], a[20:23], a[116:119], a[168:171]\n" \
"buffer_load_dwordx4 v65, s[36:39], s82 offen offset:1024 lds\n" \
"v_mfma_f32_16x16x32_bf16 a[172:175], a[16:19], a[120:123], a[172:175]\n" \
"ds_read_b64_tr_b16 a[78:79], v99 offset:20480\n" \
"v_mfma_f32_16x16x32_bf16 a[172:175], a[20:23], a[124:127], a[172:175]\n" \
"v_mfma_f32_16x16x32_bf16 a[188:191], a[24:27], a[120:123], a[188:191]\n" \
"ds_read_b128 a[16:19], v96 offset:36864\n" \
"v_mfma_f32_16x16x32_bf16 a[188:191], a[28:31], a[124:127], a[188:191]\n" \
"buffer_load_dwordx4 v66, s[36:39], s82 offen offset:2048 lds\n" \
"v_mfma_f32_16x16x32_bf16 a[184:187], a[24:27], a[112:115], a[184:187]\n" \
"ds_read_b64_tr_b16 a[80:81], v98 offset:512\n" \
"v_mfma_f32_16x16x32_bf16 a[184:187], a[28:31], a[116:119], a[184:187]\n" \
"v_mfma_f32_16x16x32_bf16 a[180:183], a[24:27], a[104:107], a[180:183]\n" \
"ds_read_b128 a[20:23], v97 offset:36864\n" \
"v_mfma_f32_16x16x32_bf16 a[180:183], a[28:31], a[108:111], a[180:183]\n" \
"buffer_load_dwordx4 v67, s[36:39], s82 offen offset:3072 lds\n" \
"v_mfma_f32_16x16x32_bf16 a[176:179], a[24:27], a[96:99], a[176:179]\n" \
"s_mov_b64 s[64:65], s[8:9]\n" \
"s_add_u32 s68, s66, 0x200000\n" \
"ds_read_b64_tr_b16 a[82:83], v98 offset:4608\n" \
"v_mfma_f32_16x16x32_bf16 a[176:179], a[28:31], a[100:103], a[176:179]\n" \
"s_add_u32 s64, s64, s68\n" \
"s_addc_u32 s65, s65, 0\n" \
"v_mfma_f32_16x16x32_bf16 a[192:195], a[32:35], a[96:99], a[192:195]\n" \
"s_xor_b32 s82, s82, 0x180\n" \
"ds_read_b128 a[24:27], v96 offset:38912\n" \
"v_mfma_f32_16x16x32_bf16 a[192:195], a[36:39], a[100:103], a[192:195]\n" \
"global_load_dword v238, v234, s[64:65]\n" \
"v_mfma_f32_16x16x32_bf16 a[196:199], a[32:35], a[104:107], a[196:199]\n" \
"s_mov_b64 s[64:65], s[8:9]\n" \
"s_add_u32 s68, s66, 0x300000\n" \
"ds_read_b64_tr_b16 a[84:85], v98 offset:16896\n" \
"v_mfma_f32_16x16x32_bf16 a[196:199], a[36:39], a[108:111], a[196:199]\n" \
"s_add_u32 s64, s64, s68\n" \
"s_addc_u32 s65, s65, 0\n" \
"v_mfma_f32_16x16x32_bf16 a[200:203], a[32:35], a[112:115], a[200:203]\n" \
"ds_read_b128 a[28:31], v97 offset:38912\n" \
"v_mfma_f32_16x16x32_bf16 a[200:203], a[36:39], a[116:119], a[200:203]\n" \
"global_load_dword v238, v234, s[64:65]\n" \
"v_mfma_f32_16x16x32_bf16 a[204:207], a[32:35], a[120:123], a[204:207]\n" \
"s_mov_b64 s[64:65], s[12:13]\n" \
"s_add_u32 s68, s67, 0x0\n" \
"ds_read_b64_tr_b16 a[86:87], v98 offset:20992\n" \
"v_mfma_f32_16x16x32_bf16 a[204:207], a[36:39], a[124:127], a[204:207]\n" \
"s_add_u32 s64, s64, s68\n" \
"s_addc_u32 s65, s65, 0\n" \
"v_mfma_f32_16x16x32_bf16 a[220:223], a[40:43], a[120:123], a[220:223]\n" \
"ds_read_b128 a[32:35], v96 offset:40960\n" \
"v_mfma_f32_16x16x32_bf16 a[220:223], a[44:47], a[124:127], a[220:223]\n" \
"global_load_dword v238, v234, s[64:65]\n" \
"v_mfma_f32_16x16x32_bf16 a[216:219], a[40:43], a[112:115], a[216:219]\n" \
"ds_read_b64_tr_b16 a[88:89], v99 offset:512\n" \
"v_mfma_f32_16x16x32_bf16 a[216:219], a[44:47], a[116:119], a[216:219]\n" \
"v_mfma_f32_16x16x32_bf16 a[212:215], a[40:43], a[104:107], a[212:215]\n" \
"ds_read_b128 a[36:39], v97 offset:40960\n" \
"v_mfma_f32_16x16x32_bf16 a[212:215], a[44:47], a[108:111], a[212:215]\n" \
"v_mfma_f32_16x16x32_bf16 a[208:211], a[40:43], a[96:99], a[208:211]\n" \
"ds_read_b64_tr_b16 a[90:91], v99 offset:4608\n" \
"v_mfma_f32_16x16x32_bf16 a[208:211], a[44:47], a[100:103], a[208:211]\n" \
"v_mfma_f32_16x16x32_bf16 a[224:227], a[48:51], a[96:99], a[224:227]\n" \
"ds_read_b128 a[40:43], v96 offset:43008\n" \
"v_mfma_f32_16x16x32_bf16 a[224:227], a[52:55], a[100:103], a[224:227]\n" \
"v_mfma_f32_16x16x32_bf16 a[228:231], a[48:51], a[104:107], a[228:231]\n" \
"ds_read_b64_tr_b16 a[92:93], v99 offset:16896\n" \
"v_mfma_f32_16x16x32_bf16 a[228:231], a[52:55], a[108:111], a[228:231]\n" \
"v_mfma_f32_16x16x32_bf16 a[232:235], a[48:51], a[112:115], a[232:235]\n" \
"ds_read_b128 a[44:47], v97 offset:43008\n" \
"v_mfma_f32_16x16x32_bf16 a[232:235], a[52:55], a[116:119], a[232:235]\n" \
"v_mfma_f32_16x16x32_bf16 a[236:239], a[48:51], a[120:123], a[236:239]\n" \
"ds_read_b64_tr_b16 a[94:95], v99 offset:20992\n" \
"v_mfma_f32_16x16x32_bf16 a[236:239], a[52:55], a[124:127], a[236:239]\n" \
"v_mfma_f32_16x16x32_bf16 a[252:255], a[56:59], a[120:123], a[252:255]\n" \
"ds_read_b128 a[48:51], v96 offset:45056\n" \
"v_mfma_f32_16x16x32_bf16 a[252:255], a[60:63], a[124:127], a[252:255]\n" \
"v_mfma_f32_16x16x32_bf16 a[248:251], a[56:59], a[112:115], a[248:251]\n" \
"v_mfma_f32_16x16x32_bf16 a[248:251], a[60:63], a[116:119], a[248:251]\n" \
"v_mfma_f32_16x16x32_bf16 a[244:247], a[56:59], a[104:107], a[244:247]\n" \
"ds_read_b128 a[52:55], v97 offset:45056\n" \
"v_mfma_f32_16x16x32_bf16 a[244:247], a[60:63], a[108:111], a[244:247]\n" \
"v_mfma_f32_16x16x32_bf16 a[240:243], a[56:59], a[96:99], a[240:243]\n" \
"v_mfma_f32_16x16x32_bf16 a[240:243], a[60:63], a[100:103], a[240:243]\n" \
"s_waitcnt lgkmcnt(1)\n" \
"v_mfma_f32_16x16x32_bf16 v[0:3], a[64:67], a[0:3], v[0:3]\n" \
"ds_read_b128 a[56:59], v96 offset:47104\n" \
"v_mfma_f32_16x16x32_bf16 v[0:3], a[68:71], a[4:7], v[0:3]\n" \
"v_mfma_f32_16x16x32_bf16 v[4:7], a[72:75], a[0:3], v[4:7]\n" \
"s_add_u32 m0, s74, 131072\n" \
"ds_read_b128 a[60:63], v97 offset:47104\n" \
"v_mfma_f32_16x16x32_bf16 v[4:7], a[76:79], a[4:7], v[4:7]\n" \
"buffer_load_dwordx4 v68, s[40:43], s83 offen offset:0 lds\n" \
"v_mfma_f32_16x16x32_bf16 v[8:11], a[80:83], a[0:3], v[8:11]\n" \
"ds_read_b64_tr_b16 a[96:97], v98 offset:1024\n" \
"v_mfma_f32_16x16x32_bf16 v[8:11], a[84:87], a[4:7], v[8:11]\n" \
"v_mfma_f32_16x16x32_bf16 v[12:15], a[88:91], a[0:3], v[12:15]\n" \
"ds_read_b64_tr_b16 a[98:99], v98 offset:5120\n" \
"v_mfma_f32_16x16x32_bf16 v[12:15], a[92:95], a[4:7], v[12:15]\n" \
"buffer_load_dwordx4 v69, s[44:47], s83 offen offset:1024 lds\n" \
"v_mfma_f32_16x16x32_bf16 v[28:31], a[88:91], a[8:11], v[28:31]\n" \
"ds_read_b64_tr_b16 a[100:101], v98 offset:17408\n" \
"v_mfma_f32_16x16x32_bf16 v[28:31], a[92:95], a[12:15], v[28:31]\n" \
"v_mfma_f32_16x16x32_bf16 v[24:27], a[80:83], a[8:11], v[24:27]\n" \
"ds_read_b64_tr_b16 a[102:103], v98 offset:21504\n" \
"v_mfma_f32_16x16x32_bf16 v[24:27], a[84:87], a[12:15], v[24:27]\n" \
"buffer_load_dwordx4 v70, s[40:43], s83 offen offset:2048 lds\n" \
"v_mfma_f32_16x16x32_bf16 v[20:23], a[72:75], a[8:11], v[20:23]\n" \
"ds_read_b64_tr_b16 a[104:105], v99 offset:1024\n" \
"v_mfma_f32_16x16x32_bf16 v[20:23], a[76:79], a[12:15], v[20:23]\n" \
"v_mfma_f32_16x16x32_bf16 v[16:19], a[64:67], a[8:11], v[16:19]\n" \
"ds_read_b64_tr_b16 a[106:107], v99 offset:5120\n" \
"v_mfma_f32_16x16x32_bf16 v[16:19], a[68:71], a[12:15], v[16:19]\n" \
"buffer_load_dwordx4 v71, s[44:47], s83 offen offset:3072 lds\n" \
"v_mfma_f32_16x16x32_bf16 v[32:35], a[64:67], a[16:19], v[32:35]\n" \
"ds_read_b64_tr_b16 a[108:109], v99 offset:17408\n" \
"v_mfma_f32_16x16x32_bf16 v[32:35], a[68:71], a[20:23], v[32:35]\n" \
"s_xor_b32 s83, s83, 0x80000\n" \
"v_mfma_f32_16x16x32_bf16 v[36:39], a[72:75], a[16:19], v[36:39]\n" \
"s_add_u32 m0, s74, 135168\n" \
"ds_read_b64_tr_b16 a[110:111], v99 offset:21504\n" \
"v_mfma_f32_16x16x32_bf16 v[36:39], a[76:79], a[20:23], v[36:39]\n" \
"buffer_load_dwordx4 v68, s[40:43], s84 offen offset:0 lds\n" \
"v_mfma_f32_16x16x32_bf16 v[40:43], a[80:83], a[16:19], v[40:43]\n" \
"ds_read_b64_tr_b16 a[112:113], v98 offset:1536\n" \
"v_mfma_f32_16x16x32_bf16 v[40:43], a[84:87], a[20:23], v[40:43]\n" \
"v_mfma_f32_16x16x32_bf16 v[44:47], a[88:91], a[16:19], v[44:47]\n" \
"ds_read_b64_tr_b16 a[114:115], v98 offset:5632\n" \
"v_mfma_f32_16x16x32_bf16 v[44:47], a[92:95], a[20:23], v[44:47]\n" \
"buffer_load_dwordx4 v69, s[44:47], s84 offen offset:1024 lds\n" \
"v_mfma_f32_16x16x32_bf16 v[60:63], a[88:91], a[24:27], v[60:63]\n" \
"ds_read_b64_tr_b16 a[116:117], v98 offset:17920\n" \
"v_mfma_f32_16x16x32_bf16 v[60:63], a[92:95], a[28:31], v[60:63]\n" \
"v_mfma_f32_16x16x32_bf16 v[56:59], a[80:83], a[24:27], v[56:59]\n" \
"ds_read_b64_tr_b16 a[118:119], v98 offset:22016\n" \
"v_mfma_f32_16x16x32_bf16 v[56:59], a[84:87], a[28:31], v[56:59]\n" \
"buffer_load_dwordx4 v70, s[40:43], s84 offen offset:2048 lds\n" \
"v_mfma_f32_16x16x32_bf16 v[52:55], a[72:75], a[24:27], v[52:55]\n" \
"ds_read_b64_tr_b16 a[120:121], v99 offset:1536\n" \
"v_mfma_f32_16x16x32_bf16 v[52:55], a[76:79], a[28:31], v[52:55]\n" \
"v_mfma_f32_16x16x32_bf16 v[48:51], a[64:67], a[24:27], v[48:51]\n" \
"ds_read_b64_tr_b16 a[122:123], v99 offset:5632\n" \
"v_mfma_f32_16x16x32_bf16 v[48:51], a[68:71], a[28:31], v[48:51]\n" \
"buffer_load_dwordx4 v71, s[44:47], s84 offen offset:3072 lds\n" \
"v_mfma_f32_16x16x32_bf16 v[144:147], a[64:67], a[32:35], v[144:147]\n" \
"ds_read_b64_tr_b16 a[124:125], v99 offset:17920\n" \
"v_mfma_f32_16x16x32_bf16 v[144:147], a[68:71], a[36:39], v[144:147]\n" \
"v_mfma_f32_16x16x32_bf16 v[148:151], a[72:75], a[32:35], v[148:151]\n" \
"s_xor_b32 s84, s84, 0x80000\n" \
"ds_read_b64_tr_b16 a[126:127], v99 offset:22016\n" \
"v_mfma_f32_16x16x32_bf16 v[148:151], a[76:79], a[36:39], v[148:151]\n" \
"v_mfma_f32_16x16x32_bf16 v[152:155], a[80:83], a[32:35], v[152:155]\n" \
"v_mfma_f32_16x16x32_bf16 v[152:155], a[84:87], a[36:39], v[152:155]\n" \
"v_mfma_f32_16x16x32_bf16 v[156:159], a[88:91], a[32:35], v[156:159]\n" \
"v_mfma_f32_16x16x32_bf16 v[156:159], a[92:95], a[36:39], v[156:159]\n" \
"v_mfma_f32_16x16x32_bf16 v[188:191], a[88:91], a[40:43], v[188:191]\n" \
"v_mfma_f32_16x16x32_bf16 v[188:191], a[92:95], a[44:47], v[188:191]\n" \
"v_mfma_f32_16x16x32_bf16 v[184:187], a[80:83], a[40:43], v[184:187]\n" \
"v_mfma_f32_16x16x32_bf16 v[184:187], a[84:87], a[44:47], v[184:187]\n" \
"v_mfma_f32_16x16x32_bf16 v[180:183], a[72:75], a[40:43], v[180:183]\n" \
"v_mfma_f32_16x16x32_bf16 v[180:183], a[76:79], a[44:47], v[180:183]\n" \
"v_mfma_f32_16x16x32_bf16 v[176:179], a[64:67], a[40:43], v[176:179]\n" \
"v_mfma_f32_16x16x32_bf16 v[176:179], a[68:71], a[44:47], v[176:179]\n" \
"s_waitcnt lgkmcnt(0)\n" \
"v_mfma_f32_16x16x32_bf16 v[192:195], a[64:67], a[48:51], v[192:195]\n" \
"v_mfma_f32_16x16x32_bf16 v[192:195], a[68:71], a[52:55], v[192:195]\n" \
"v_mfma_f32_16x16x32_bf16 v[196:199], a[72:75], a[48:51], v[196:199]\n" \
"v_mfma_f32_16x16x32_bf16 v[196:199], a[76:79], a[52:55], v[196:199]\n" \
"v_mfma_f32_16x16x32_bf16 v[200:203], a[80:83], a[48:51], v[200:203]\n" \
"v_mfma_f32_16x16x32_bf16 v[200:203], a[84:87], a[52:55], v[200:203]\n" \
"v_mfma_f32_16x16x32_bf16 v[204:207], a[88:91], a[48:51], v[204:207]\n" \
"v_mfma_f32_16x16x32_bf16 v[204:207], a[92:95], a[52:55], v[204:207]\n" \
"v_mfma_f32_16x16x32_bf16 v[220:223], a[88:91], a[56:59], v[220:223]\n" \
"v_mfma_f32_16x16x32_bf16 v[220:223], a[92:95], a[60:63], v[220:223]\n" \
"v_mfma_f32_16x16x32_bf16 v[216:219], a[80:83], a[56:59], v[216:219]\n" \
"v_mfma_f32_16x16x32_bf16 v[216:219], a[84:87], a[60:63], v[216:219]\n" \
"v_mfma_f32_16x16x32_bf16 v[212:215], a[72:75], a[56:59], v[212:215]\n" \
"v_mfma_f32_16x16x32_bf16 v[212:215], a[76:79], a[60:63], v[212:215]\n" \
"v_mfma_f32_16x16x32_bf16 v[208:211], a[64:67], a[56:59], v[208:211]\n" \
"v_mfma_f32_16x16x32_bf16 v[208:211], a[68:71], a[60:63], v[208:211]\n" \
"s_waitcnt vmcnt(11) lgkmcnt(0)\n" \
"s_barrier\n" \
"v_mfma_f32_16x16x32_bf16 a[128:131], a[0:3], a[96:99], a[128:131]\n" \
"s_add_u32 m0, s74, 65536\n" \
"ds_read_b64_tr_b16 a[64:65], v74 offset:32768\n" \
"v_mfma_f32_16x16x32_bf16 a[128:131], a[4:7], a[100:103], a[128:131]\n" \
"buffer_load_dwordx4 v64, s[36:39], s81 offen offset:0 lds\n" \
"v_mfma_f32_16x16x32_bf16 a[132:135], a[0:3], a[104:107], a[132:135]\n" \
"ds_read_b64_tr_b16 a[66:67], v74 offset:36864\n" \
"v_mfma_f32_16x16x32_bf16 a[132:135], a[4:7], a[108:111], a[132:135]\n" \
"v_mfma_f32_16x16x32_bf16 a[136:139], a[0:3], a[112:115], a[136:139]\n" \
"ds_read_b64_tr_b16 a[68:69], v74 offset:49152\n" \
"v_mfma_f32_16x16x32_bf16 a[136:139], a[4:7], a[116:119], a[136:139]\n" \
"buffer_load_dwordx4 v65, s[36:39], s81 offen offset:1024 lds\n" \
"v_mfma_f32_16x16x32_bf16 a[140:143], a[0:3], a[120:123], a[140:143]\n" \
"ds_read_b64_tr_b16 a[70:71], v74 offset:53248\n" \
"v_mfma_f32_16x16x32_bf16 a[140:143], a[4:7], a[124:127], a[140:143]\n" \
"v_mfma_f32_16x16x32_bf16 a[156:159], a[8:11], a[120:123], a[156:159]\n" \
"ds_read_b128 a[0:3], v72 offset:0\n" \
"v_mfma_f32_16x16x32_bf16 a[156:159], a[12:15], a[124:127], a[156:159]\n" \
"buffer_load_dwordx4 v66, s[36:39], s81 offen offset:2048 lds\n" \
"v_mfma_f32_16x16x32_bf16 a[152:155], a[8:11], a[112:115], a[152:155]\n" \
"ds_read_b64_tr_b16 a[72:73], v75 offset:32768\n" \
"v_mfma_f32_16x16x32_bf16 a[152:155], a[12:15], a[116:119], a[152:155]\n" \
"v_mfma_f32_16x16x32_bf16 a[148:151], a[8:11], a[104:107], a[148:151]\n" \
"ds_read_b128 a[4:7], v73 offset:0\n" \
"v_mfma_f32_16x16x32_bf16 a[148:151], a[12:15], a[108:111], a[148:151]\n" \
"buffer_load_dwordx4 v67, s[36:39], s81 offen offset:3072 lds\n" \
"v_mfma_f32_16x16x32_bf16 a[144:147], a[8:11], a[96:99], a[144:147]\n" \
"ds_read_b64_tr_b16 a[74:75], v75 offset:36864\n" \
"v_mfma_f32_16x16x32_bf16 a[144:147], a[12:15], a[100:103], a[144:147]\n" \
"s_xor_b32 s81, s81, 0x80\n" \
"v_mfma_f32_16x16x32_bf16 a[160:163], a[16:19], a[96:99], a[160:163]\n" \
"s_add_u32 m0, s74, 69632\n" \
"ds_read_b128 a[8:11], v72 offset:2048\n" \
"v_mfma_f32_16x16x32_bf16 a[160:163], a[20:23], a[100:103], a[160:163]\n" \
"buffer_load_dwordx4 v64, s[36:39], s82 offen offset:0 lds\n" \
"v_mfma_f32_16x16x32_bf16 a[164:167], a[16:19], a[104:107], a[164:167]\n" \
"ds_read_b64_tr_b16 a[76:77], v75 offset:49152\n" \
"v_mfma_f32_16x16x32_bf16 a[164:167], a[20:23], a[108:111], a[164:167]\n" \
"v_mfma_f32_16x16x32_bf16 a[168:171], a[16:19], a[112:115], a[168:171]\n" \
"ds_read_b128 a[12:15], v73 offset:2048\n" \
"v_mfma_f32_16x16x32_bf16 a[168:171], a[20:23], a[116:119], a[168:171]\n" \
"buffer_load_dwordx4 v65, s[36:39], s82 offen offset:1024 lds\n" \
"v_mfma_f32_16x16x32_bf16 a[172:175], a[16:19], a[120:123], a[172:175]\n" \
"ds_read_b64_tr_b16 a[78:79], v75 offset:53248\n" \
"v_mfma_f32_16x16x32_bf16 a[172:175], a[20:23], a[124:127], a[172:175]\n" \
"v_mfma_f32_16x16x32_bf16 a[188:191], a[24:27], a[120:123], a[188:191]\n" \
"ds_read_b128 a[16:19], v72 offset:4096\n" \
"v_mfma_f32_16x16x32_bf16 a[188:191], a[28:31], a[124:127], a[188:191]\n" \
"buffer_load_dwordx4 v66, s[36:39], s82 offen offset:2048 lds\n" \
"v_mfma_f32_16x16x32_bf16 a[184:187], a[24:27], a[112:115], a[184:187]\n" \
"ds_read_b64_tr_b16 a[80:81], v74 offset:33280\n" \
"v_mfma_f32_16x16x32_bf16 a[184:187], a[28:31], a[116:119], a[184:187]\n" \
"v_mfma_f32_16x16x32_bf16 a[180:183], a[24:27], a[104:107], a[180:183]\n" \
"ds_read_b128 a[20:23], v73 offset:4096\n" \
"v_mfma_f32_16x16x32_bf16 a[180:183], a[28:31], a[108:111], a[180:183]\n" \
"buffer_load_dwordx4 v67, s[36:39], s82 offen offset:3072 lds\n" \
"v_mfma_f32_16x16x32_bf16 a[176:179], a[24:27], a[96:99], a[176:179]\n" \
"s_mov_b64 s[64:65], s[12:13]\n" \
"s_add_u32 s68, s67, 0x100000\n" \
"ds_read_b64_tr_b16 a[82:83], v74 offset:37376\n" \
"v_mfma_f32_16x16x32_bf16 a[176:179], a[28:31], a[100:103], a[176:179]\n" \
"s_add_u32 s64, s64, s68\n" \
"s_addc_u32 s65, s65, 0\n" \
"v_mfma_f32_16x16x32_bf16 a[192:195], a[32:35], a[96:99], a[192:195]\n" \
"s_xor_b32 s82, s82, 0x80\n" \
"ds_read_b128 a[24:27], v72 offset:6144\n" \
"v_mfma_f32_16x16x32_bf16 a[192:195], a[36:39], a[100:103], a[192:195]\n" \
"global_load_dword v238, v234, s[64:65]\n" \
"v_mfma_f32_16x16x32_bf16 a[196:199], a[32:35], a[104:107], a[196:199]\n" \
"s_mov_b64 s[64:65], s[12:13]\n" \
"s_add_u32 s68, s67, 0x500000\n" \
"ds_read_b64_tr_b16 a[84:85], v74 offset:49664\n" \
"v_mfma_f32_16x16x32_bf16 a[196:199], a[36:39], a[108:111], a[196:199]\n" \
"s_add_u32 s64, s64, s68\n" \
"s_addc_u32 s65, s65, 0\n" \
"v_mfma_f32_16x16x32_bf16 a[200:203], a[32:35], a[112:115], a[200:203]\n" \
"ds_read_b128 a[28:31], v73 offset:6144\n" \
"v_mfma_f32_16x16x32_bf16 a[200:203], a[36:39], a[116:119], a[200:203]\n" \
"global_load_dword v238, v234, s[64:65]\n" \
"v_mfma_f32_16x16x32_bf16 a[204:207], a[32:35], a[120:123], a[204:207]\n" \
"s_mov_b64 s[64:65], s[12:13]\n" \
"s_add_u32 s68, s67, 0x200000\n" \
"ds_read_b64_tr_b16 a[86:87], v74 offset:53760\n" \
"v_mfma_f32_16x16x32_bf16 a[204:207], a[36:39], a[124:127], a[204:207]\n" \
"s_add_u32 s64, s64, s68\n" \
"s_addc_u32 s65, s65, 0\n" \
"v_mfma_f32_16x16x32_bf16 a[220:223], a[40:43], a[120:123], a[220:223]\n" \
"ds_read_b128 a[32:35], v72 offset:8192\n" \
"v_mfma_f32_16x16x32_bf16 a[220:223], a[44:47], a[124:127], a[220:223]\n" \
"global_load_dword v238, v234, s[64:65]\n" \
"v_mfma_f32_16x16x32_bf16 a[216:219], a[40:43], a[112:115], a[216:219]\n" \
"ds_read_b64_tr_b16 a[88:89], v75 offset:33280\n" \
"v_mfma_f32_16x16x32_bf16 a[216:219], a[44:47], a[116:119], a[216:219]\n" \
"v_mfma_f32_16x16x32_bf16 a[212:215], a[40:43], a[104:107], a[212:215]\n" \
"ds_read_b128 a[36:39], v73 offset:8192\n" \
"v_mfma_f32_16x16x32_bf16 a[212:215], a[44:47], a[108:111], a[212:215]\n" \
"v_mfma_f32_16x16x32_bf16 a[208:211], a[40:43], a[96:99], a[208:211]\n" \
"ds_read_b64_tr_b16 a[90:91], v75 offset:37376\n" \
"v_mfma_f32_16x16x32_bf16 a[208:211], a[44:47], a[100:103], a[208:211]\n" \
"v_mfma_f32_16x16x32_bf16 a[224:227], a[48:51], a[96:99], a[224:227]\n" \
"ds_read_b128 a[40:43], v72 offset:10240\n" \
"v_mfma_f32_16x16x32_bf16 a[224:227], a[52:55], a[100:103], a[224:227]\n" \
"v_mfma_f32_16x16x32_bf16 a[228:231], a[48:51], a[104:107], a[228:231]\n" \
"ds_read_b64_tr_b16 a[92:93], v75 offset:49664\n" \
"v_mfma_f32_16x16x32_bf16 a[228:231], a[52:55], a[108:111], a[228:231]\n" \
"v_mfma_f32_16x16x32_bf16 a[232:235], a[48:51], a[112:115], a[232:235]\n" \
"ds_read_b128 a[44:47], v73 offset:10240\n" \
"v_mfma_f32_16x16x32_bf16 a[232:235], a[52:55], a[116:119], a[232:235]\n" \
"v_mfma_f32_16x16x32_bf16 a[236:239], a[48:51], a[120:123], a[236:239]\n" \
"ds_read_b64_tr_b16 a[94:95], v75 offset:53760\n" \
"v_mfma_f32_16x16x32_bf16 a[236:239], a[52:55], a[124:127], a[236:239]\n" \
"v_mfma_f32_16x16x32_bf16 a[252:255], a[56:59], a[120:123], a[252:255]\n" \
"ds_read_b128 a[48:51], v72 offset:12288\n" \
"v_mfma_f32_16x16x32_bf16 a[252:255], a[60:63], a[124:127], a[252:255]\n" \
"v_mfma_f32_16x16x32_bf16 a[248:251], a[56:59], a[112:115], a[248:251]\n" \
"v_mfma_f32_16x16x32_bf16 a[248:251], a[60:63], a[116:119], a[248:251]\n" \
"v_mfma_f32_16x16x32_bf16 a[244:247], a[56:59], a[104:107], a[244:247]\n" \
"ds_read_b128 a[52:55], v73 offset:12288\n" \
"v_mfma_f32_16x16x32_bf16 a[244:247], a[60:63], a[108:111], a[244:247]\n" \
"v_mfma_f32_16x16x32_bf16 a[240:243], a[56:59], a[96:99], a[240:243]\n" \
"v_mfma_f32_16x16x32_bf16 a[240:243], a[60:63], a[100:103], a[240:243]\n" \
"s_waitcnt lgkmcnt(1)\n" \
"v_mfma_f32_16x16x32_bf16 v[0:3], a[64:67], a[0:3], v[0:3]\n" \
"ds_read_b128 a[56:59], v72 offset:14336\n" \
"v_mfma_f32_16x16x32_bf16 v[0:3], a[68:71], a[4:7], v[0:3]\n" \
"v_mfma_f32_16x16x32_bf16 v[4:7], a[72:75], a[0:3], v[4:7]\n" \
"s_add_u32 m0, s74, 98304\n" \
"ds_read_b128 a[60:63], v73 offset:14336\n" \
"v_mfma_f32_16x16x32_bf16 v[4:7], a[76:79], a[4:7], v[4:7]\n" \
"buffer_load_dwordx4 v68, s[40:43], s83 offen offset:0 lds\n" \
"v_mfma_f32_16x16x32_bf16 v[8:11], a[80:83], a[0:3], v[8:11]\n" \
"ds_read_b64_tr_b16 a[96:97], v74 offset:33792\n" \
"v_mfma_f32_16x16x32_bf16 v[8:11], a[84:87], a[4:7], v[8:11]\n" \
"v_mfma_f32_16x16x32_bf16 v[12:15], a[88:91], a[0:3], v[12:15]\n" \
"ds_read_b64_tr_b16 a[98:99], v74 offset:37888\n" \
"v_mfma_f32_16x16x32_bf16 v[12:15], a[92:95], a[4:7], v[12:15]\n" \
"buffer_load_dwordx4 v69, s[44:47], s83 offen offset:1024 lds\n" \
"v_mfma_f32_16x16x32_bf16 v[28:31], a[88:91], a[8:11], v[28:31]\n" \
"ds_read_b64_tr_b16 a[100:101], v74 offset:50176\n" \
"v_mfma_f32_16x16x32_bf16 v[28:31], a[92:95], a[12:15], v[28:31]\n" \
"v_mfma_f32_16x16x32_bf16 v[24:27], a[80:83], a[8:11], v[24:27]\n" \
"ds_read_b64_tr_b16 a[102:103], v74 offset:54272\n" \
"v_mfma_f32_16x16x32_bf16 v[24:27], a[84:87], a[12:15], v[24:27]\n" \
"buffer_load_dwordx4 v70, s[40:43], s83 offen offset:2048 lds\n" \
"v_mfma_f32_16x16x32_bf16 v[20:23], a[72:75], a[8:11], v[20:23]\n" \
"ds_read_b64_tr_b16 a[104:105], v75 offset:33792\n" \
"v_mfma_f32_16x16x32_bf16 v[20:23], a[76:79], a[12:15], v[20:23]\n" \
"v_mfma_f32_16x16x32_bf16 v[16:19], a[64:67], a[8:11], v[16:19]\n" \
"ds_read_b64_tr_b16 a[106:107], v75 offset:37888\n" \
"v_mfma_f32_16x16x32_bf16 v[16:19], a[68:71], a[12:15], v[16:19]\n" \
"buffer_load_dwordx4 v71, s[44:47], s83 offen offset:3072 lds\n" \
"v_mfma_f32_16x16x32_bf16 v[32:35], a[64:67], a[16:19], v[32:35]\n" \
"ds_read_b64_tr_b16 a[108:109], v75 offset:50176\n" \
"v_mfma_f32_16x16x32_bf16 v[32:35], a[68:71], a[20:23], v[32:35]\n" \
"v_mfma_f32_16x16x32_bf16 v[36:39], a[72:75], a[16:19], v[36:39]\n" \
"s_add_u32 m0, s74, 102400\n" \
"ds_read_b64_tr_b16 a[110:111], v75 offset:54272\n" \
"v_mfma_f32_16x16x32_bf16 v[36:39], a[76:79], a[20:23], v[36:39]\n" \
"buffer_load_dwordx4 v68, s[40:43], s84 offen offset:0 lds\n" \
"v_mfma_f32_16x16x32_bf16 v[40:43], a[80:83], a[16:19], v[40:43]\n" \
"ds_read_b64_tr_b16 a[112:113], v74 offset:34304\n" \
"v_mfma_f32_16x16x32_bf16 v[40:43], a[84:87], a[20:23], v[40:43]\n" \
"v_mfma_f32_16x16x32_bf16 v[44:47], a[88:91], a[16:19], v[44:47]\n" \
"ds_read_b64_tr_b16 a[114:115], v74 offset:38400\n" \
"v_mfma_f32_16x16x32_bf16 v[44:47], a[92:95], a[20:23], v[44:47]\n" \
"buffer_load_dwordx4 v69, s[44:47], s84 offen offset:1024 lds\n" \
"v_mfma_f32_16x16x32_bf16 v[60:63], a[88:91], a[24:27], v[60:63]\n" \
"ds_read_b64_tr_b16 a[116:117], v74 offset:50688\n" \
"v_mfma_f32_16x16x32_bf16 v[60:63], a[92:95], a[28:31], v[60:63]\n" \
"v_mfma_f32_16x16x32_bf16 v[56:59], a[80:83], a[24:27], v[56:59]\n" \
"ds_read_b64_tr_b16 a[118:119], v74 offset:54784\n" \
"v_mfma_f32_16x16x32_bf16 v[56:59], a[84:87], a[28:31], v[56:59]\n" \
"buffer_load_dwordx4 v70, s[40:43], s84 offen offset:2048 lds\n" \
"v_mfma_f32_16x16x32_bf16 v[52:55], a[72:75], a[24:27], v[52:55]\n" \
"ds_read_b64_tr_b16 a[120:121], v75 offset:34304\n" \
"v_mfma_f32_16x16x32_bf16 v[52:55], a[76:79], a[28:31], v[52:55]\n" \
"v_mfma_f32_16x16x32_bf16 v[48:51], a[64:67], a[24:27], v[48:51]\n" \
"ds_read_b64_tr_b16 a[122:123], v75 offset:38400\n" \
"v_mfma_f32_16x16x32_bf16 v[48:51], a[68:71], a[28:31], v[48:51]\n" \
"buffer_load_dwordx4 v71, s[44:47], s84 offen offset:3072 lds\n" \
"v_mfma_f32_16x16x32_bf16 v[144:147], a[64:67], a[32:35], v[144:147]\n" \
"ds_read_b64_tr_b16 a[124:125], v75 offset:50688\n" \
"v_mfma_f32_16x16x32_bf16 v[144:147], a[68:71], a[36:39], v[144:147]\n" \
"v_mfma_f32_16x16x32_bf16 v[148:151], a[72:75], a[32:35], v[148:151]\n" \
"ds_read_b64_tr_b16 a[126:127], v75 offset:54784\n" \
"v_mfma_f32_16x16x32_bf16 v[148:151], a[76:79], a[36:39], v[148:151]\n" \
"v_mfma_f32_16x16x32_bf16 v[152:155], a[80:83], a[32:35], v[152:155]\n" \
"v_mfma_f32_16x16x32_bf16 v[152:155], a[84:87], a[36:39], v[152:155]\n" \
"v_mfma_f32_16x16x32_bf16 v[156:159], a[88:91], a[32:35], v[156:159]\n" \
"v_mfma_f32_16x16x32_bf16 v[156:159], a[92:95], a[36:39], v[156:159]\n" \
"v_mfma_f32_16x16x32_bf16 v[188:191], a[88:91], a[40:43], v[188:191]\n" \
"v_mfma_f32_16x16x32_bf16 v[188:191], a[92:95], a[44:47], v[188:191]\n" \
"v_mfma_f32_16x16x32_bf16 v[184:187], a[80:83], a[40:43], v[184:187]\n" \
"v_mfma_f32_16x16x32_bf16 v[184:187], a[84:87], a[44:47], v[184:187]\n" \
"v_mfma_f32_16x16x32_bf16 v[180:183], a[72:75], a[40:43], v[180:183]\n" \
"v_mfma_f32_16x16x32_bf16 v[180:183], a[76:79], a[44:47], v[180:183]\n" \
"v_mfma_f32_16x16x32_bf16 v[176:179], a[64:67], a[40:43], v[176:179]\n" \
"v_mfma_f32_16x16x32_bf16 v[176:179], a[68:71], a[44:47], v[176:179]\n" \
"s_waitcnt lgkmcnt(0)\n" \
"v_mfma_f32_16x16x32_bf16 v[192:195], a[64:67], a[48:51], v[192:195]\n" \
"v_mfma_f32_16x16x32_bf16 v[192:195], a[68:71], a[52:55], v[192:195]\n" \
"v_mfma_f32_16x16x32_bf16 v[196:199], a[72:75], a[48:51], v[196:199]\n" \
"v_mfma_f32_16x16x32_bf16 v[196:199], a[76:79], a[52:55], v[196:199]\n" \
"v_mfma_f32_16x16x32_bf16 v[200:203], a[80:83], a[48:51], v[200:203]\n" \
"v_mfma_f32_16x16x32_bf16 v[200:203], a[84:87], a[52:55], v[200:203]\n" \
"v_mfma_f32_16x16x32_bf16 v[204:207], a[88:91], a[48:51], v[204:207]\n" \
"v_mfma_f32_16x16x32_bf16 v[204:207], a[92:95], a[52:55], v[204:207]\n" \
"v_mfma_f32_16x16x32_bf16 v[220:223], a[88:91], a[56:59], v[220:223]\n" \
"v_mfma_f32_16x16x32_bf16 v[220:223], a[92:95], a[60:63], v[220:223]\n" \
"v_mfma_f32_16x16x32_bf16 v[216:219], a[80:83], a[56:59], v[216:219]\n" \
"v_mfma_f32_16x16x32_bf16 v[216:219], a[84:87], a[60:63], v[216:219]\n" \
"v_mfma_f32_16x16x32_bf16 v[212:215], a[72:75], a[56:59], v[212:215]\n" \
"v_mfma_f32_16x16x32_bf16 v[212:215], a[76:79], a[60:63], v[212:215]\n" \
"v_mfma_f32_16x16x32_bf16 v[208:211], a[64:67], a[56:59], v[208:211]\n" \
"v_mfma_f32_16x16x32_bf16 v[208:211], a[68:71], a[60:63], v[208:211]\n" \
"s_waitcnt vmcnt(11) lgkmcnt(0)\n" \
"s_barrier\n" \
"v_mfma_f32_16x16x32_bf16 a[128:131], a[0:3], a[96:99], a[128:131]\n" \
"s_add_u32 m0, s74, 32768\n" \
"ds_read_b64_tr_b16 a[64:65], v102 offset:0\n" \
"v_mfma_f32_16x16x32_bf16 a[128:131], a[4:7], a[100:103], a[128:131]\n" \
"buffer_load_dwordx4 v64, s[36:39], s81 offen offset:0 lds\n" \
"v_mfma_f32_16x16x32_bf16 a[132:135], a[0:3], a[104:107], a[132:135]\n" \
"ds_read_b64_tr_b16 a[66:67], v102 offset:4096\n" \
"v_mfma_f32_16x16x32_bf16 a[132:135], a[4:7], a[108:111], a[132:135]\n" \
"v_mfma_f32_16x16x32_bf16 a[136:139], a[0:3], a[112:115], a[136:139]\n" \
"ds_read_b64_tr_b16 a[68:69], v102 offset:16384\n" \
"v_mfma_f32_16x16x32_bf16 a[136:139], a[4:7], a[116:119], a[136:139]\n" \
"buffer_load_dwordx4 v65, s[36:39], s81 offen offset:1024 lds\n" \
"v_mfma_f32_16x16x32_bf16 a[140:143], a[0:3], a[120:123], a[140:143]\n" \
"ds_read_b64_tr_b16 a[70:71], v102 offset:20480\n" \
"v_mfma_f32_16x16x32_bf16 a[140:143], a[4:7], a[124:127], a[140:143]\n" \
"v_mfma_f32_16x16x32_bf16 a[156:159], a[8:11], a[120:123], a[156:159]\n" \
"ds_read_b128 a[0:3], v96 offset:0\n" \
"v_mfma_f32_16x16x32_bf16 a[156:159], a[12:15], a[124:127], a[156:159]\n" \
"buffer_load_dwordx4 v66, s[36:39], s81 offen offset:2048 lds\n" \
"v_mfma_f32_16x16x32_bf16 a[152:155], a[8:11], a[112:115], a[152:155]\n" \
"ds_read_b64_tr_b16 a[72:73], v103 offset:0\n" \
"v_mfma_f32_16x16x32_bf16 a[152:155], a[12:15], a[116:119], a[152:155]\n" \
"v_mfma_f32_16x16x32_bf16 a[148:151], a[8:11], a[104:107], a[148:151]\n" \
"ds_read_b128 a[4:7], v97 offset:0\n" \
"v_mfma_f32_16x16x32_bf16 a[148:151], a[12:15], a[108:111], a[148:151]\n" \
"buffer_load_dwordx4 v67, s[36:39], s81 offen offset:3072 lds\n" \
"v_mfma_f32_16x16x32_bf16 a[144:147], a[8:11], a[96:99], a[144:147]\n" \
"ds_read_b64_tr_b16 a[74:75], v103 offset:4096\n" \
"v_mfma_f32_16x16x32_bf16 a[144:147], a[12:15], a[100:103], a[144:147]\n" \
"v_mfma_f32_16x16x32_bf16 a[160:163], a[16:19], a[96:99], a[160:163]\n" \
"s_add_u32 m0, s74, 36864\n" \
"ds_read_b128 a[8:11], v96 offset:2048\n" \
"v_mfma_f32_16x16x32_bf16 a[160:163], a[20:23], a[100:103], a[160:163]\n" \
"buffer_load_dwordx4 v64, s[36:39], s82 offen offset:0 lds\n" \
"v_mfma_f32_16x16x32_bf16 a[164:167], a[16:19], a[104:107], a[164:167]\n" \
"ds_read_b64_tr_b16 a[76:77], v103 offset:16384\n" \
"v_mfma_f32_16x16x32_bf16 a[164:167], a[20:23], a[108:111], a[164:167]\n" \
"v_mfma_f32_16x16x32_bf16 a[168:171], a[16:19], a[112:115], a[168:171]\n" \
"ds_read_b128 a[12:15], v97 offset:2048\n" \
"v_mfma_f32_16x16x32_bf16 a[168:171], a[20:23], a[116:119], a[168:171]\n" \
"buffer_load_dwordx4 v65, s[36:39], s82 offen offset:1024 lds\n" \
"v_mfma_f32_16x16x32_bf16 a[172:175], a[16:19], a[120:123], a[172:175]\n" \
"ds_read_b64_tr_b16 a[78:79], v103 offset:20480\n" \
"v_mfma_f32_16x16x32_bf16 a[172:175], a[20:23], a[124:127], a[172:175]\n" \
"v_mfma_f32_16x16x32_bf16 a[188:191], a[24:27], a[120:123], a[188:191]\n" \
"ds_read_b128 a[16:19], v96 offset:4096\n" \
"v_mfma_f32_16x16x32_bf16 a[188:191], a[28:31], a[124:127], a[188:191]\n" \
"buffer_load_dwordx4 v66, s[36:39], s82 offen offset:2048 lds\n" \
"v_mfma_f32_16x16x32_bf16 a[184:187], a[24:27], a[112:115], a[184:187]\n" \
"ds_read_b64_tr_b16 a[80:81], v102 offset:512\n" \
"v_mfma_f32_16x16x32_bf16 a[184:187], a[28:31], a[116:119], a[184:187]\n" \
"v_mfma_f32_16x16x32_bf16 a[180:183], a[24:27], a[104:107], a[180:183]\n" \
"ds_read_b128 a[20:23], v97 offset:4096\n" \
"v_mfma_f32_16x16x32_bf16 a[180:183], a[28:31], a[108:111], a[180:183]\n" \
"buffer_load_dwordx4 v67, s[36:39], s82 offen offset:3072 lds\n" \
"v_mfma_f32_16x16x32_bf16 a[176:179], a[24:27], a[96:99], a[176:179]\n" \
"s_mov_b64 s[64:65], s[12:13]\n" \
"s_add_u32 s68, s67, 0x300000\n" \
"ds_read_b64_tr_b16 a[82:83], v102 offset:4608\n" \
"v_mfma_f32_16x16x32_bf16 a[176:179], a[28:31], a[100:103], a[176:179]\n" \
"s_add_u32 s64, s64, s68\n" \
"s_addc_u32 s65, s65, 0\n" \
"v_mfma_f32_16x16x32_bf16 a[192:195], a[32:35], a[96:99], a[192:195]\n" \
"ds_read_b128 a[24:27], v96 offset:6144\n" \
"v_mfma_f32_16x16x32_bf16 a[192:195], a[36:39], a[100:103], a[192:195]\n" \
"global_load_dword v238, v234, s[64:65]\n" \
"v_mfma_f32_16x16x32_bf16 a[196:199], a[32:35], a[104:107], a[196:199]\n" \
"s_mov_b64 s[64:65], s[12:13]\n" \
"s_add_u32 s68, s67, 0x400000\n" \
"ds_read_b64_tr_b16 a[84:85], v102 offset:16896\n" \
"v_mfma_f32_16x16x32_bf16 a[196:199], a[36:39], a[108:111], a[196:199]\n" \
"s_add_u32 s64, s64, s68\n" \
"s_addc_u32 s65, s65, 0\n" \
"v_mfma_f32_16x16x32_bf16 a[200:203], a[32:35], a[112:115], a[200:203]\n" \
"ds_read_b128 a[28:31], v97 offset:6144\n" \
"v_mfma_f32_16x16x32_bf16 a[200:203], a[36:39], a[116:119], a[200:203]\n" \
"global_load_dword v238, v234, s[64:65]\n" \
"v_mfma_f32_16x16x32_bf16 a[204:207], a[32:35], a[120:123], a[204:207]\n" \
"s_mov_b64 s[64:65], s[12:13]\n" \
"s_add_u32 s68, s67, 0x600000\n" \
"ds_read_b64_tr_b16 a[86:87], v102 offset:20992\n" \
"v_mfma_f32_16x16x32_bf16 a[204:207], a[36:39], a[124:127], a[204:207]\n" \
"s_add_u32 s64, s64, s68\n" \
"s_addc_u32 s65, s65, 0\n" \
"v_mfma_f32_16x16x32_bf16 a[220:223], a[40:43], a[120:123], a[220:223]\n" \
"ds_read_b128 a[32:35], v96 offset:8192\n" \
"v_mfma_f32_16x16x32_bf16 a[220:223], a[44:47], a[124:127], a[220:223]\n" \
"global_load_dword v238, v234, s[64:65]\n" \
"v_mfma_f32_16x16x32_bf16 a[216:219], a[40:43], a[112:115], a[216:219]\n" \
"ds_read_b64_tr_b16 a[88:89], v103 offset:512\n" \
"v_mfma_f32_16x16x32_bf16 a[216:219], a[44:47], a[116:119], a[216:219]\n" \
"v_mfma_f32_16x16x32_bf16 a[212:215], a[40:43], a[104:107], a[212:215]\n" \
"ds_read_b128 a[36:39], v97 offset:8192\n" \
"v_mfma_f32_16x16x32_bf16 a[212:215], a[44:47], a[108:111], a[212:215]\n" \
"v_mfma_f32_16x16x32_bf16 a[208:211], a[40:43], a[96:99], a[208:211]\n" \
"ds_read_b64_tr_b16 a[90:91], v103 offset:4608\n" \
"v_mfma_f32_16x16x32_bf16 a[208:211], a[44:47], a[100:103], a[208:211]\n" \
"v_mfma_f32_16x16x32_bf16 a[224:227], a[48:51], a[96:99], a[224:227]\n" \
"ds_read_b128 a[40:43], v96 offset:10240\n" \
"v_mfma_f32_16x16x32_bf16 a[224:227], a[52:55], a[100:103], a[224:227]\n" \
"v_mfma_f32_16x16x32_bf16 a[228:231], a[48:51], a[104:107], a[228:231]\n" \
"ds_read_b64_tr_b16 a[92:93], v103 offset:16896\n" \
"v_mfma_f32_16x16x32_bf16 a[228:231], a[52:55], a[108:111], a[228:231]\n" \
"v_mfma_f32_16x16x32_bf16 a[232:235], a[48:51], a[112:115], a[232:235]\n" \
"ds_read_b128 a[44:47], v97 offset:10240\n" \
"v_mfma_f32_16x16x32_bf16 a[232:235], a[52:55], a[116:119], a[232:235]\n" \
"v_mfma_f32_16x16x32_bf16 a[236:239], a[48:51], a[120:123], a[236:239]\n" \
"ds_read_b64_tr_b16 a[94:95], v103 offset:20992\n" \
"v_mfma_f32_16x16x32_bf16 a[236:239], a[52:55], a[124:127], a[236:239]\n" \
"v_mfma_f32_16x16x32_bf16 a[252:255], a[56:59], a[120:123], a[252:255]\n" \
"ds_read_b128 a[48:51], v96 offset:12288\n" \
"v_mfma_f32_16x16x32_bf16 a[252:255], a[60:63], a[124:127], a[252:255]\n" \
"v_mfma_f32_16x16x32_bf16 a[248:251], a[56:59], a[112:115], a[248:251]\n" \
"v_mfma_f32_16x16x32_bf16 a[248:251], a[60:63], a[116:119], a[248:251]\n" \
"v_mfma_f32_16x16x32_bf16 a[244:247], a[56:59], a[104:107], a[244:247]\n" \
"ds_read_b128 a[52:55], v97 offset:12288\n" \
"v_mfma_f32_16x16x32_bf16 a[244:247], a[60:63], a[108:111], a[244:247]\n" \
"v_mfma_f32_16x16x32_bf16 a[240:243], a[56:59], a[96:99], a[240:243]\n" \
"v_mfma_f32_16x16x32_bf16 a[240:243], a[60:63], a[100:103], a[240:243]\n" \
"s_waitcnt lgkmcnt(1)\n" \
"v_mfma_f32_16x16x32_bf16 v[0:3], a[64:67], a[0:3], v[0:3]\n" \
"ds_read_b128 a[56:59], v96 offset:14336\n" \
"Lpg1663_%=:\n" \
"Lst0x2_%=:\n" \
"s_cbranch_execz Lpg3897_%=\n" \
"v_mfma_f32_16x16x32_bf16 v[0:3], a[68:71], a[4:7], v[0:3]\n" \
"v_mfma_f32_16x16x32_bf16 v[4:7], a[72:75], a[0:3], v[4:7]\n" \
"s_add_u32 m0, s79, 0\n" \
"ds_read_b128 a[60:63], v97 offset:14336\n" \
"v_mfma_f32_16x16x32_bf16 v[4:7], a[76:79], a[4:7], v[4:7]\n" \
"buffer_load_dwordx4 v228, s[52:55], s78 offen offset:0 lds\n" \
"v_mfma_f32_16x16x32_bf16 v[8:11], a[80:83], a[0:3], v[8:11]\n" \
"ds_read_b64_tr_b16 a[96:97], v102 offset:1024\n" \
"v_mfma_f32_16x16x32_bf16 v[8:11], a[84:87], a[4:7], v[8:11]\n" \
"v_mfma_f32_16x16x32_bf16 v[12:15], a[88:91], a[0:3], v[12:15]\n" \
"ds_read_b64_tr_b16 a[98:99], v102 offset:5120\n" \
"v_mfma_f32_16x16x32_bf16 v[12:15], a[92:95], a[4:7], v[12:15]\n" \
"buffer_load_dwordx4 v229, s[52:55], s78 offen offset:1024 lds\n" \
"v_mfma_f32_16x16x32_bf16 v[28:31], a[88:91], a[8:11], v[28:31]\n" \
"ds_read_b64_tr_b16 a[100:101], v102 offset:17408\n" \
"v_mfma_f32_16x16x32_bf16 v[28:31], a[92:95], a[12:15], v[28:31]\n" \
"v_mfma_f32_16x16x32_bf16 v[24:27], a[80:83], a[8:11], v[24:27]\n" \
"s_xor_b32 s78, s78, 0x180000\n" \
"ds_read_b64_tr_b16 a[102:103], v102 offset:21504\n" \
"v_mfma_f32_16x16x32_bf16 v[24:27], a[84:87], a[12:15], v[24:27]\n" \
"buffer_load_dwordx4 v228, s[52:55], s89 offen offset:2048 lds\n" \
"v_mfma_f32_16x16x32_bf16 v[20:23], a[72:75], a[8:11], v[20:23]\n" \
"ds_read_b64_tr_b16 a[104:105], v103 offset:1024\n" \
"v_mfma_f32_16x16x32_bf16 v[20:23], a[76:79], a[12:15], v[20:23]\n" \
"v_mfma_f32_16x16x32_bf16 v[16:19], a[64:67], a[8:11], v[16:19]\n" \
"ds_read_b64_tr_b16 a[106:107], v103 offset:5120\n" \
"v_mfma_f32_16x16x32_bf16 v[16:19], a[68:71], a[12:15], v[16:19]\n" \
"buffer_load_dwordx4 v229, s[52:55], s89 offen offset:3072 lds\n" \
"v_mfma_f32_16x16x32_bf16 v[32:35], a[64:67], a[16:19], v[32:35]\n" \
"ds_read_b64_tr_b16 a[108:109], v103 offset:17408\n" \
"v_mfma_f32_16x16x32_bf16 v[32:35], a[68:71], a[20:23], v[32:35]\n" \
"s_xor_b32 s89, s89, 0x180000\n" \
"v_mfma_f32_16x16x32_bf16 v[36:39], a[72:75], a[16:19], v[36:39]\n" \
"s_add_u32 m0, s79, 16384\n" \
"ds_read_b64_tr_b16 a[110:111], v103 offset:21504\n" \
"v_mfma_f32_16x16x32_bf16 v[36:39], a[76:79], a[20:23], v[36:39]\n" \
"buffer_load_dwordx4 v228, s[52:55], s78 offen offset:0 lds\n" \
"v_mfma_f32_16x16x32_bf16 v[40:43], a[80:83], a[16:19], v[40:43]\n" \
"ds_read_b64_tr_b16 a[112:113], v102 offset:1536\n" \
"v_mfma_f32_16x16x32_bf16 v[40:43], a[84:87], a[20:23], v[40:43]\n" \
"v_mfma_f32_16x16x32_bf16 v[44:47], a[88:91], a[16:19], v[44:47]\n" \
"ds_read_b64_tr_b16 a[114:115], v102 offset:5632\n" \
"v_mfma_f32_16x16x32_bf16 v[44:47], a[92:95], a[20:23], v[44:47]\n" \
"buffer_load_dwordx4 v229, s[52:55], s78 offen offset:1024 lds\n" \
"v_mfma_f32_16x16x32_bf16 v[60:63], a[88:91], a[24:27], v[60:63]\n" \
"ds_read_b64_tr_b16 a[116:117], v102 offset:17920\n" \
"v_mfma_f32_16x16x32_bf16 v[60:63], a[92:95], a[28:31], v[60:63]\n" \
"v_mfma_f32_16x16x32_bf16 v[56:59], a[80:83], a[24:27], v[56:59]\n" \
"s_xor_b32 s78, s78, 0x400000\n" \
"ds_read_b64_tr_b16 a[118:119], v102 offset:22016\n" \
"v_mfma_f32_16x16x32_bf16 v[56:59], a[84:87], a[28:31], v[56:59]\n" \
"buffer_load_dwordx4 v228, s[52:55], s89 offen offset:2048 lds\n" \
"v_mfma_f32_16x16x32_bf16 v[52:55], a[72:75], a[24:27], v[52:55]\n" \
"ds_read_b64_tr_b16 a[120:121], v103 offset:1536\n" \
"v_mfma_f32_16x16x32_bf16 v[52:55], a[76:79], a[28:31], v[52:55]\n" \
"v_mfma_f32_16x16x32_bf16 v[48:51], a[64:67], a[24:27], v[48:51]\n" \
"ds_read_b64_tr_b16 a[122:123], v103 offset:5632\n" \
"v_mfma_f32_16x16x32_bf16 v[48:51], a[68:71], a[28:31], v[48:51]\n" \
"buffer_load_dwordx4 v229, s[52:55], s89 offen offset:3072 lds\n" \
"v_mfma_f32_16x16x32_bf16 v[144:147], a[64:67], a[32:35], v[144:147]\n" \
"ds_read_b64_tr_b16 a[124:125], v103 offset:17920\n" \
"v_mfma_f32_16x16x32_bf16 v[144:147], a[68:71], a[36:39], v[144:147]\n" \
"v_mfma_f32_16x16x32_bf16 v[148:151], a[72:75], a[32:35], v[148:151]\n" \
"s_xor_b32 s89, s89, 0x400000\n" \
"ds_read_b64_tr_b16 a[126:127], v103 offset:22016\n" \
"v_mfma_f32_16x16x32_bf16 v[148:151], a[76:79], a[36:39], v[148:151]\n" \
"v_mfma_f32_16x16x32_bf16 v[152:155], a[80:83], a[32:35], v[152:155]\n" \
"v_mfma_f32_16x16x32_bf16 v[152:155], a[84:87], a[36:39], v[152:155]\n" \
"v_mfma_f32_16x16x32_bf16 v[156:159], a[88:91], a[32:35], v[156:159]\n" \
"v_mfma_f32_16x16x32_bf16 v[156:159], a[92:95], a[36:39], v[156:159]\n" \
"v_mfma_f32_16x16x32_bf16 v[188:191], a[88:91], a[40:43], v[188:191]\n" \
"v_mfma_f32_16x16x32_bf16 v[188:191], a[92:95], a[44:47], v[188:191]\n" \
"v_mfma_f32_16x16x32_bf16 v[184:187], a[80:83], a[40:43], v[184:187]\n" \
"v_mfma_f32_16x16x32_bf16 v[184:187], a[84:87], a[44:47], v[184:187]\n" \
"v_mfma_f32_16x16x32_bf16 v[180:183], a[72:75], a[40:43], v[180:183]\n" \
"v_mfma_f32_16x16x32_bf16 v[180:183], a[76:79], a[44:47], v[180:183]\n" \
"v_mfma_f32_16x16x32_bf16 v[176:179], a[64:67], a[40:43], v[176:179]\n" \
"v_mfma_f32_16x16x32_bf16 v[176:179], a[68:71], a[44:47], v[176:179]\n" \
"s_waitcnt lgkmcnt(0)\n" \
"v_mfma_f32_16x16x32_bf16 v[192:195], a[64:67], a[48:51], v[192:195]\n" \
"v_mfma_f32_16x16x32_bf16 v[192:195], a[68:71], a[52:55], v[192:195]\n" \
"v_mfma_f32_16x16x32_bf16 v[196:199], a[72:75], a[48:51], v[196:199]\n" \
"v_mfma_f32_16x16x32_bf16 v[196:199], a[76:79], a[52:55], v[196:199]\n" \
"v_mfma_f32_16x16x32_bf16 v[200:203], a[80:83], a[48:51], v[200:203]\n" \
"v_mfma_f32_16x16x32_bf16 v[200:203], a[84:87], a[52:55], v[200:203]\n" \
"v_mfma_f32_16x16x32_bf16 v[204:207], a[88:91], a[48:51], v[204:207]\n" \
"v_mfma_f32_16x16x32_bf16 v[204:207], a[92:95], a[52:55], v[204:207]\n" \
"v_mfma_f32_16x16x32_bf16 v[220:223], a[88:91], a[56:59], v[220:223]\n" \
"v_mfma_f32_16x16x32_bf16 v[220:223], a[92:95], a[60:63], v[220:223]\n" \
"v_mfma_f32_16x16x32_bf16 v[216:219], a[80:83], a[56:59], v[216:219]\n" \
"v_mfma_f32_16x16x32_bf16 v[216:219], a[84:87], a[60:63], v[216:219]\n" \
"v_mfma_f32_16x16x32_bf16 v[212:215], a[72:75], a[56:59], v[212:215]\n" \
"v_mfma_f32_16x16x32_bf16 v[212:215], a[76:79], a[60:63], v[212:215]\n" \
"v_mfma_f32_16x16x32_bf16 v[208:211], a[64:67], a[56:59], v[208:211]\n" \
"v_mfma_f32_16x16x32_bf16 v[208:211], a[68:71], a[60:63], v[208:211]\n" \
"s_waitcnt vmcnt(11) lgkmcnt(0)\n" \
"s_barrier\n" \
"v_mfma_f32_16x16x32_bf16 a[128:131], a[0:3], a[96:99], a[128:131]\n" \
"s_add_u32 m0, s74, 131072\n" \
"ds_read_b64_tr_b16 a[64:65], v98 offset:32768\n" \
"v_mfma_f32_16x16x32_bf16 a[128:131], a[4:7], a[100:103], a[128:131]\n" \
"buffer_load_dwordx4 v224, s[48:51], s75 offen offset:0 lds\n" \
"v_mfma_f32_16x16x32_bf16 a[132:135], a[0:3], a[104:107], a[132:135]\n" \
"ds_read_b64_tr_b16 a[66:67], v98 offset:36864\n" \
"v_mfma_f32_16x16x32_bf16 a[132:135], a[4:7], a[108:111], a[132:135]\n" \
"v_mfma_f32_16x16x32_bf16 a[136:139], a[0:3], a[112:115], a[136:139]\n" \
"ds_read_b64_tr_b16 a[68:69], v98 offset:49152\n" \
"v_mfma_f32_16x16x32_bf16 a[136:139], a[4:7], a[116:119], a[136:139]\n" \
"buffer_load_dwordx4 v225, s[48:51], s75 offen offset:1024 lds\n" \
"v_mfma_f32_16x16x32_bf16 a[140:143], a[0:3], a[120:123], a[140:143]\n" \
"ds_read_b64_tr_b16 a[70:71], v98 offset:53248\n" \
"v_mfma_f32_16x16x32_bf16 a[140:143], a[4:7], a[124:127], a[140:143]\n" \
"v_mfma_f32_16x16x32_bf16 a[156:159], a[8:11], a[120:123], a[156:159]\n" \
"ds_read_b128 a[0:3], v72 offset:32768\n" \
"v_mfma_f32_16x16x32_bf16 a[156:159], a[12:15], a[124:127], a[156:159]\n" \
"buffer_load_dwordx4 v226, s[48:51], s75 offen offset:2048 lds\n" \
"v_mfma_f32_16x16x32_bf16 a[152:155], a[8:11], a[112:115], a[152:155]\n" \
"ds_read_b64_tr_b16 a[72:73], v99 offset:32768\n" \
"v_mfma_f32_16x16x32_bf16 a[152:155], a[12:15], a[116:119], a[152:155]\n" \
"v_mfma_f32_16x16x32_bf16 a[148:151], a[8:11], a[104:107], a[148:151]\n" \
"ds_read_b128 a[4:7], v73 offset:32768\n" \
"v_mfma_f32_16x16x32_bf16 a[148:151], a[12:15], a[108:111], a[148:151]\n" \
"buffer_load_dwordx4 v227, s[48:51], s75 offen offset:3072 lds\n" \
"v_mfma_f32_16x16x32_bf16 a[144:147], a[8:11], a[96:99], a[144:147]\n" \
"ds_read_b64_tr_b16 a[74:75], v99 offset:36864\n" \
"v_mfma_f32_16x16x32_bf16 a[144:147], a[12:15], a[100:103], a[144:147]\n" \
"s_xor_b32 s75, s75, 0x180\n" \
"v_mfma_f32_16x16x32_bf16 a[160:163], a[16:19], a[96:99], a[160:163]\n" \
"s_add_u32 m0, s74, 135168\n" \
"ds_read_b128 a[8:11], v72 offset:34816\n" \
"v_mfma_f32_16x16x32_bf16 a[160:163], a[20:23], a[100:103], a[160:163]\n" \
"buffer_load_dwordx4 v224, s[48:51], s77 offen offset:0 lds\n" \
"v_mfma_f32_16x16x32_bf16 a[164:167], a[16:19], a[104:107], a[164:167]\n" \
"ds_read_b64_tr_b16 a[76:77], v99 offset:49152\n" \
"v_mfma_f32_16x16x32_bf16 a[164:167], a[20:23], a[108:111], a[164:167]\n" \
"v_mfma_f32_16x16x32_bf16 a[168:171], a[16:19], a[112:115], a[168:171]\n" \
"ds_read_b128 a[12:15], v73 offset:34816\n" \
"v_mfma_f32_16x16x32_bf16 a[168:171], a[20:23], a[116:119], a[168:171]\n" \
"buffer_load_dwordx4 v225, s[48:51], s77 offen offset:1024 lds\n" \
"v_mfma_f32_16x16x32_bf16 a[172:175], a[16:19], a[120:123], a[172:175]\n" \
"ds_read_b64_tr_b16 a[78:79], v99 offset:53248\n" \
"v_mfma_f32_16x16x32_bf16 a[172:175], a[20:23], a[124:127], a[172:175]\n" \
"v_mfma_f32_16x16x32_bf16 a[188:191], a[24:27], a[120:123], a[188:191]\n" \
"ds_read_b128 a[16:19], v72 offset:36864\n" \
"v_mfma_f32_16x16x32_bf16 a[188:191], a[28:31], a[124:127], a[188:191]\n" \
"buffer_load_dwordx4 v226, s[48:51], s77 offen offset:2048 lds\n" \
"v_mfma_f32_16x16x32_bf16 a[184:187], a[24:27], a[112:115], a[184:187]\n" \
"ds_read_b64_tr_b16 a[80:81], v98 offset:33280\n" \
"v_mfma_f32_16x16x32_bf16 a[184:187], a[28:31], a[116:119], a[184:187]\n" \
"v_mfma_f32_16x16x32_bf16 a[180:183], a[24:27], a[104:107], a[180:183]\n" \
"ds_read_b128 a[20:23], v73 offset:36864\n" \
"v_mfma_f32_16x16x32_bf16 a[180:183], a[28:31], a[108:111], a[180:183]\n" \
"buffer_load_dwordx4 v227, s[48:51], s77 offen offset:3072 lds\n" \
"v_mfma_f32_16x16x32_bf16 a[176:179], a[24:27], a[96:99], a[176:179]\n" \
"s_mov_b64 s[64:65], s[12:13]\n" \
"s_add_u32 s68, s67, 0x700000\n" \
"ds_read_b64_tr_b16 a[82:83], v98 offset:37376\n" \
"v_mfma_f32_16x16x32_bf16 a[176:179], a[28:31], a[100:103], a[176:179]\n" \
"s_add_u32 s64, s64, s68\n" \
"s_addc_u32 s65, s65, 0\n" \
"v_mfma_f32_16x16x32_bf16 a[192:195], a[32:35], a[96:99], a[192:195]\n" \
"s_xor_b32 s77, s77, 0x180\n" \
"ds_read_b128 a[24:27], v72 offset:38912\n" \
"v_mfma_f32_16x16x32_bf16 a[192:195], a[36:39], a[100:103], a[192:195]\n" \
"global_load_dword v238, v234, s[64:65]\n" \
"v_mfma_f32_16x16x32_bf16 a[196:199], a[32:35], a[104:107], a[196:199]\n" \
"ds_read_b64_tr_b16 a[84:85], v98 offset:49664\n" \
"v_mfma_f32_16x16x32_bf16 a[196:199], a[36:39], a[108:111], a[196:199]\n" \
"v_mfma_f32_16x16x32_bf16 a[200:203], a[32:35], a[112:115], a[200:203]\n" \
"s_lshr_b32 s68, s66, 2\n" \
"s_and_b32 s68, s68, 0xffff\n" \
"s_add_u32 s68, s68, 65585152\n" \
"ds_read_b128 a[28:31], v73 offset:38912\n" \
"v_mfma_f32_16x16x32_bf16 a[200:203], a[36:39], a[116:119], a[200:203]\n" \
"buffer_load_dword v238, v242, s[56:59], s68 offen sc1\n" \
"v_mfma_f32_16x16x32_bf16 a[204:207], a[32:35], a[120:123], a[204:207]\n" \
"ds_read_b64_tr_b16 a[86:87], v98 offset:53760\n" \
"v_mfma_f32_16x16x32_bf16 a[204:207], a[36:39], a[124:127], a[204:207]\n" \
"v_mfma_f32_16x16x32_bf16 a[220:223], a[40:43], a[120:123], a[220:223]\n" \
"ds_read_b128 a[32:35], v72 offset:40960\n" \
"v_mfma_f32_16x16x32_bf16 a[220:223], a[44:47], a[124:127], a[220:223]\n" \
"v_mfma_f32_16x16x32_bf16 a[216:219], a[40:43], a[112:115], a[216:219]\n" \
"ds_read_b64_tr_b16 a[88:89], v99 offset:33280\n" \
"v_mfma_f32_16x16x32_bf16 a[216:219], a[44:47], a[116:119], a[216:219]\n" \
"v_mfma_f32_16x16x32_bf16 a[212:215], a[40:43], a[104:107], a[212:215]\n" \
"ds_read_b128 a[36:39], v73 offset:40960\n" \
"v_mfma_f32_16x16x32_bf16 a[212:215], a[44:47], a[108:111], a[212:215]\n" \
"v_mfma_f32_16x16x32_bf16 a[208:211], a[40:43], a[96:99], a[208:211]\n" \
"ds_read_b64_tr_b16 a[90:91], v99 offset:37376\n" \
"v_mfma_f32_16x16x32_bf16 a[208:211], a[44:47], a[100:103], a[208:211]\n" \
"v_mfma_f32_16x16x32_bf16 a[224:227], a[48:51], a[96:99], a[224:227]\n" \
"ds_read_b128 a[40:43], v72 offset:43008\n" \
"v_mfma_f32_16x16x32_bf16 a[224:227], a[52:55], a[100:103], a[224:227]\n" \
"v_mfma_f32_16x16x32_bf16 a[228:231], a[48:51], a[104:107], a[228:231]\n" \
"ds_read_b64_tr_b16 a[92:93], v99 offset:49664\n" \
"v_mfma_f32_16x16x32_bf16 a[228:231], a[52:55], a[108:111], a[228:231]\n" \
"v_mfma_f32_16x16x32_bf16 a[232:235], a[48:51], a[112:115], a[232:235]\n" \
"ds_read_b128 a[44:47], v73 offset:43008\n" \
"v_mfma_f32_16x16x32_bf16 a[232:235], a[52:55], a[116:119], a[232:235]\n" \
"v_mfma_f32_16x16x32_bf16 a[236:239], a[48:51], a[120:123], a[236:239]\n" \
"ds_read_b64_tr_b16 a[94:95], v99 offset:53760\n" \
"v_mfma_f32_16x16x32_bf16 a[236:239], a[52:55], a[124:127], a[236:239]\n" \
"v_mfma_f32_16x16x32_bf16 a[252:255], a[56:59], a[120:123], a[252:255]\n" \
"ds_read_b128 a[48:51], v72 offset:45056\n" \
"v_mfma_f32_16x16x32_bf16 a[252:255], a[60:63], a[124:127], a[252:255]\n" \
"v_mfma_f32_16x16x32_bf16 a[248:251], a[56:59], a[112:115], a[248:251]\n" \
"v_mfma_f32_16x16x32_bf16 a[248:251], a[60:63], a[116:119], a[248:251]\n" \
"v_mfma_f32_16x16x32_bf16 a[244:247], a[56:59], a[104:107], a[244:247]\n" \
"ds_read_b128 a[52:55], v73 offset:45056\n" \
"v_mfma_f32_16x16x32_bf16 a[244:247], a[60:63], a[108:111], a[244:247]\n" \
"v_mfma_f32_16x16x32_bf16 a[240:243], a[56:59], a[96:99], a[240:243]\n" \
"v_mfma_f32_16x16x32_bf16 a[240:243], a[60:63], a[100:103], a[240:243]\n" \
"s_waitcnt lgkmcnt(1)\n" \
"v_mfma_f32_16x16x32_bf16 v[0:3], a[64:67], a[0:3], v[0:3]\n" \
"ds_read_b128 a[56:59], v72 offset:47104\n" \
"v_mfma_f32_16x16x32_bf16 v[0:3], a[68:71], a[4:7], v[0:3]\n" \
"v_mfma_f32_16x16x32_bf16 v[4:7], a[72:75], a[0:3], v[4:7]\n" \
"s_add_u32 m0, s74, 65536\n" \
"ds_read_b128 a[60:63], v73 offset:47104\n" \
"v_mfma_f32_16x16x32_bf16 v[4:7], a[76:79], a[4:7], v[4:7]\n" \
"buffer_load_dwordx4 v224, s[48:51], s75 offen offset:0 lds\n" \
"v_mfma_f32_16x16x32_bf16 v[8:11], a[80:83], a[0:3], v[8:11]\n" \
"ds_read_b64_tr_b16 a[96:97], v98 offset:33792\n" \
"v_mfma_f32_16x16x32_bf16 v[8:11], a[84:87], a[4:7], v[8:11]\n" \
"v_mfma_f32_16x16x32_bf16 v[12:15], a[88:91], a[0:3], v[12:15]\n" \
"ds_read_b64_tr_b16 a[98:99], v98 offset:37888\n" \
"v_mfma_f32_16x16x32_bf16 v[12:15], a[92:95], a[4:7], v[12:15]\n" \
"buffer_load_dwordx4 v225, s[48:51], s75 offen offset:1024 lds\n" \
"v_mfma_f32_16x16x32_bf16 v[28:31], a[88:91], a[8:11], v[28:31]\n" \
"ds_read_b64_tr_b16 a[100:101], v98 offset:50176\n" \
"v_mfma_f32_16x16x32_bf16 v[28:31], a[92:95], a[12:15], v[28:31]\n" \
"v_mfma_f32_16x16x32_bf16 v[24:27], a[80:83], a[8:11], v[24:27]\n" \
"ds_read_b64_tr_b16 a[102:103], v98 offset:54272\n" \
"v_mfma_f32_16x16x32_bf16 v[24:27], a[84:87], a[12:15], v[24:27]\n" \
"buffer_load_dwordx4 v226, s[48:51], s75 offen offset:2048 lds\n" \
"v_mfma_f32_16x16x32_bf16 v[20:23], a[72:75], a[8:11], v[20:23]\n" \
"ds_read_b64_tr_b16 a[104:105], v99 offset:33792\n" \
"v_mfma_f32_16x16x32_bf16 v[20:23], a[76:79], a[12:15], v[20:23]\n" \
"v_mfma_f32_16x16x32_bf16 v[16:19], a[64:67], a[8:11], v[16:19]\n" \
"ds_read_b64_tr_b16 a[106:107], v99 offset:37888\n" \
"v_mfma_f32_16x16x32_bf16 v[16:19], a[68:71], a[12:15], v[16:19]\n" \
"buffer_load_dwordx4 v227, s[48:51], s75 offen offset:3072 lds\n" \
"v_mfma_f32_16x16x32_bf16 v[32:35], a[64:67], a[16:19], v[32:35]\n" \
"ds_read_b64_tr_b16 a[108:109], v99 offset:50176\n" \
"v_mfma_f32_16x16x32_bf16 v[32:35], a[68:71], a[20:23], v[32:35]\n" \
"s_xor_b32 s75, s75, 0x400\n" \
"v_mfma_f32_16x16x32_bf16 v[36:39], a[72:75], a[16:19], v[36:39]\n" \
"s_add_u32 m0, s74, 69632\n" \
"ds_read_b64_tr_b16 a[110:111], v99 offset:54272\n" \
"v_mfma_f32_16x16x32_bf16 v[36:39], a[76:79], a[20:23], v[36:39]\n" \
"buffer_load_dwordx4 v224, s[48:51], s77 offen offset:0 lds\n" \
"v_mfma_f32_16x16x32_bf16 v[40:43], a[80:83], a[16:19], v[40:43]\n" \
"ds_read_b64_tr_b16 a[112:113], v98 offset:34304\n" \
"v_mfma_f32_16x16x32_bf16 v[40:43], a[84:87], a[20:23], v[40:43]\n" \
"v_mfma_f32_16x16x32_bf16 v[44:47], a[88:91], a[16:19], v[44:47]\n" \
"ds_read_b64_tr_b16 a[114:115], v98 offset:38400\n" \
"v_mfma_f32_16x16x32_bf16 v[44:47], a[92:95], a[20:23], v[44:47]\n" \
"buffer_load_dwordx4 v225, s[48:51], s77 offen offset:1024 lds\n" \
"v_mfma_f32_16x16x32_bf16 v[60:63], a[88:91], a[24:27], v[60:63]\n" \
"ds_read_b64_tr_b16 a[116:117], v98 offset:50688\n" \
"v_mfma_f32_16x16x32_bf16 v[60:63], a[92:95], a[28:31], v[60:63]\n" \
"v_mfma_f32_16x16x32_bf16 v[56:59], a[80:83], a[24:27], v[56:59]\n" \
"ds_read_b64_tr_b16 a[118:119], v98 offset:54784\n" \
"v_mfma_f32_16x16x32_bf16 v[56:59], a[84:87], a[28:31], v[56:59]\n" \
"buffer_load_dwordx4 v226, s[48:51], s77 offen offset:2048 lds\n" \
"v_mfma_f32_16x16x32_bf16 v[52:55], a[72:75], a[24:27], v[52:55]\n" \
"ds_read_b64_tr_b16 a[120:121], v99 offset:34304\n" \
"v_mfma_f32_16x16x32_bf16 v[52:55], a[76:79], a[28:31], v[52:55]\n" \
"v_mfma_f32_16x16x32_bf16 v[48:51], a[64:67], a[24:27], v[48:51]\n" \
"ds_read_b64_tr_b16 a[122:123], v99 offset:38400\n" \
"v_mfma_f32_16x16x32_bf16 v[48:51], a[68:71], a[28:31], v[48:51]\n" \
"buffer_load_dwordx4 v227, s[48:51], s77 offen offset:3072 lds\n" \
"v_mfma_f32_16x16x32_bf16 v[144:147], a[64:67], a[32:35], v[144:147]\n" \
"ds_read_b64_tr_b16 a[124:125], v99 offset:50688\n" \
"v_mfma_f32_16x16x32_bf16 v[144:147], a[68:71], a[36:39], v[144:147]\n" \
"v_mfma_f32_16x16x32_bf16 v[148:151], a[72:75], a[32:35], v[148:151]\n" \
"s_xor_b32 s77, s77, 0x400\n" \
"ds_read_b64_tr_b16 a[126:127], v99 offset:54784\n" \
"v_mfma_f32_16x16x32_bf16 v[148:151], a[76:79], a[36:39], v[148:151]\n" \
"v_mfma_f32_16x16x32_bf16 v[152:155], a[80:83], a[32:35], v[152:155]\n" \
"v_mfma_f32_16x16x32_bf16 v[152:155], a[84:87], a[36:39], v[152:155]\n" \
"v_mfma_f32_16x16x32_bf16 v[156:159], a[88:91], a[32:35], v[156:159]\n" \
"v_mfma_f32_16x16x32_bf16 v[156:159], a[92:95], a[36:39], v[156:159]\n" \
"v_mfma_f32_16x16x32_bf16 v[188:191], a[88:91], a[40:43], v[188:191]\n" \
"v_mfma_f32_16x16x32_bf16 v[188:191], a[92:95], a[44:47], v[188:191]\n" \
"v_mfma_f32_16x16x32_bf16 v[184:187], a[80:83], a[40:43], v[184:187]\n" \
"v_mfma_f32_16x16x32_bf16 v[184:187], a[84:87], a[44:47], v[184:187]\n" \
"v_mfma_f32_16x16x32_bf16 v[180:183], a[72:75], a[40:43], v[180:183]\n" \
"v_mfma_f32_16x16x32_bf16 v[180:183], a[76:79], a[44:47], v[180:183]\n" \
"v_mfma_f32_16x16x32_bf16 v[176:179], a[64:67], a[40:43], v[176:179]\n" \
"v_mfma_f32_16x16x32_bf16 v[176:179], a[68:71], a[44:47], v[176:179]\n" \
"s_waitcnt lgkmcnt(0)\n" \
"v_mfma_f32_16x16x32_bf16 v[192:195], a[64:67], a[48:51], v[192:195]\n" \
"v_mfma_f32_16x16x32_bf16 v[192:195], a[68:71], a[52:55], v[192:195]\n" \
"v_mfma_f32_16x16x32_bf16 v[196:199], a[72:75], a[48:51], v[196:199]\n" \
"v_mfma_f32_16x16x32_bf16 v[196:199], a[76:79], a[52:55], v[196:199]\n" \
"v_mfma_f32_16x16x32_bf16 v[200:203], a[80:83], a[48:51], v[200:203]\n" \
"v_mfma_f32_16x16x32_bf16 v[200:203], a[84:87], a[52:55], v[200:203]\n" \
"v_mfma_f32_16x16x32_bf16 v[204:207], a[88:91], a[48:51], v[204:207]\n" \
"v_mfma_f32_16x16x32_bf16 v[204:207], a[92:95], a[52:55], v[204:207]\n" \
"v_mfma_f32_16x16x32_bf16 v[220:223], a[88:91], a[56:59], v[220:223]\n" \
"v_mfma_f32_16x16x32_bf16 v[220:223], a[92:95], a[60:63], v[220:223]\n" \
"v_mfma_f32_16x16x32_bf16 v[216:219], a[80:83], a[56:59], v[216:219]\n" \
"v_mfma_f32_16x16x32_bf16 v[216:219], a[84:87], a[60:63], v[216:219]\n" \
"v_mfma_f32_16x16x32_bf16 v[212:215], a[72:75], a[56:59], v[212:215]\n" \
"v_mfma_f32_16x16x32_bf16 v[212:215], a[76:79], a[60:63], v[212:215]\n" \
"v_mfma_f32_16x16x32_bf16 v[208:211], a[64:67], a[56:59], v[208:211]\n" \
"v_mfma_f32_16x16x32_bf16 v[208:211], a[68:71], a[60:63], v[208:211]\n" \
"s_waitcnt vmcnt(10) lgkmcnt(0)\n" \
"s_barrier\n" \
"v_mfma_f32_16x16x32_bf16 a[128:131], a[0:3], a[96:99], a[128:131]\n" \
"s_add_u32 m0, s74, 98304\n" \
"v_mfma_f32_16x16x32_bf16 a[128:131], a[4:7], a[100:103], a[128:131]\n" \
"buffer_load_dwordx4 v224, s[48:51], s75 offen offset:0 lds\n" \
"v_mfma_f32_16x16x32_bf16 a[132:135], a[0:3], a[104:107], a[132:135]\n" \
"ds_read_b64_tr_b16 a[64:65], v232 offset:0\n" \
"ds_read_b64_tr_b16 a[66:67], v232 offset:2048\n" \
"v_mfma_f32_16x16x32_bf16 a[132:135], a[4:7], a[108:111], a[132:135]\n" \
"v_mfma_f32_16x16x32_bf16 a[136:139], a[0:3], a[112:115], a[136:139]\n" \
"v_mfma_f32_16x16x32_bf16 a[136:139], a[4:7], a[116:119], a[136:139]\n" \
"buffer_load_dwordx4 v225, s[48:51], s75 offen offset:1024 lds\n" \
"v_mfma_f32_16x16x32_bf16 a[140:143], a[0:3], a[120:123], a[140:143]\n" \
"ds_read_b64_tr_b16 a[68:69], v232 offset:8192\n" \
"ds_read_b64_tr_b16 a[70:71], v232 offset:10240\n" \
"v_mfma_f32_16x16x32_bf16 a[140:143], a[4:7], a[124:127], a[140:143]\n" \
"v_mfma_f32_16x16x32_bf16 a[156:159], a[8:11], a[120:123], a[156:159]\n" \
"v_mul_f32 v192, s76, v192\n" \
"v_mul_f32 v193, s76, v193\n" \
"v_mfma_f32_16x16x32_bf16 a[156:159], a[12:15], a[124:127], a[156:159]\n" \
"buffer_load_dwordx4 v226, s[48:51], s75 offen offset:2048 lds\n" \
"v_mfma_f32_16x16x32_bf16 a[152:155], a[8:11], a[112:115], a[152:155]\n" \
"ds_read_b64_tr_b16 a[72:73], v233 offset:0\n" \
"ds_read_b64_tr_b16 a[74:75], v233 offset:2048\n" \
"v_mfma_f32_16x16x32_bf16 a[152:155], a[12:15], a[116:119], a[152:155]\n" \
"v_mul_f32 v194, s76, v194\n" \
"v_mul_f32 v195, s76, v195\n" \
"v_mfma_f32_16x16x32_bf16 a[148:151], a[8:11], a[104:107], a[148:151]\n" \
"v_cvt_pk_bf16_f32 v192, v192, v193\n" \
"v_cvt_pk_bf16_f32 v193, v194, v195\n" \
"v_mfma_f32_16x16x32_bf16 a[148:151], a[12:15], a[108:111], a[148:151]\n" \
"buffer_load_dwordx4 v227, s[48:51], s75 offen offset:3072 lds\n" \
"v_mfma_f32_16x16x32_bf16 a[144:147], a[8:11], a[96:99], a[144:147]\n" \
"ds_read_b64_tr_b16 a[76:77], v233 offset:8192\n" \
"ds_read_b64_tr_b16 a[78:79], v233 offset:10240\n" \
"v_mfma_f32_16x16x32_bf16 a[144:147], a[12:15], a[100:103], a[144:147]\n" \
"s_xor_b32 s75, s75, 0x500\n" \
"v_mul_f32 v196, s76, v196\n" \
"v_mul_f32 v197, s76, v197\n" \
"v_mfma_f32_16x16x32_bf16 a[160:163], a[16:19], a[96:99], a[160:163]\n" \
"s_add_u32 m0, s74, 102400\n" \
"v_mul_f32 v198, s76, v198\n" \
"v_mul_f32 v199, s76, v199\n" \
"v_mfma_f32_16x16x32_bf16 a[160:163], a[20:23], a[100:103], a[160:163]\n" \
"buffer_load_dwordx4 v224, s[48:51], s77 offen offset:0 lds\n" \
"v_mfma_f32_16x16x32_bf16 a[164:167], a[16:19], a[104:107], a[164:167]\n" \
"ds_read_b64_tr_b16 a[80:81], v232 offset:512\n" \
"ds_read_b64_tr_b16 a[82:83], v232 offset:2560\n" \
"v_mfma_f32_16x16x32_bf16 a[164:167], a[20:23], a[108:111], a[164:167]\n" \
"v_cvt_pk_bf16_f32 v194, v196, v197\n" \
"v_cvt_pk_bf16_f32 v195, v198, v199\n" \
"v_mfma_f32_16x16x32_bf16 a[168:171], a[16:19], a[112:115], a[168:171]\n" \
"v_mul_f32 v208, s76, v208\n" \
"v_mul_f32 v209, s76, v209\n" \
"v_mfma_f32_16x16x32_bf16 a[168:171], a[20:23], a[116:119], a[168:171]\n" \
"buffer_load_dwordx4 v225, s[48:51], s77 offen offset:1024 lds\n" \
"v_mfma_f32_16x16x32_bf16 a[172:175], a[16:19], a[120:123], a[172:175]\n" \
"ds_read_b64_tr_b16 a[84:85], v232 offset:8704\n" \
"ds_read_b64_tr_b16 a[86:87], v232 offset:10752\n" \
"v_mfma_f32_16x16x32_bf16 a[172:175], a[20:23], a[124:127], a[172:175]\n" \
"v_mul_f32 v210, s76, v210\n" \
"v_mul_f32 v211, s76, v211\n" \
"v_mfma_f32_16x16x32_bf16 a[188:191], a[24:27], a[120:123], a[188:191]\n" \
"v_cvt_pk_bf16_f32 v208, v208, v209\n" \
"v_cvt_pk_bf16_f32 v209, v210, v211\n" \
"v_mfma_f32_16x16x32_bf16 a[188:191], a[28:31], a[124:127], a[188:191]\n" \
"buffer_load_dwordx4 v226, s[48:51], s77 offen offset:2048 lds\n" \
"v_mfma_f32_16x16x32_bf16 a[184:187], a[24:27], a[112:115], a[184:187]\n" \
"ds_read_b64_tr_b16 a[88:89], v233 offset:512\n" \
"ds_read_b64_tr_b16 a[90:91], v233 offset:2560\n" \
"v_mfma_f32_16x16x32_bf16 a[184:187], a[28:31], a[116:119], a[184:187]\n" \
"v_mul_f32 v212, s76, v212\n" \
"v_mul_f32 v213, s76, v213\n" \
"v_mfma_f32_16x16x32_bf16 a[180:183], a[24:27], a[104:107], a[180:183]\n" \
"v_mul_f32 v214, s76, v214\n" \
"v_mul_f32 v215, s76, v215\n" \
"v_mfma_f32_16x16x32_bf16 a[180:183], a[28:31], a[108:111], a[180:183]\n" \
"buffer_load_dwordx4 v227, s[48:51], s77 offen offset:3072 lds\n" \
"v_mfma_f32_16x16x32_bf16 a[176:179], a[24:27], a[96:99], a[176:179]\n" \
"ds_read_b64_tr_b16 a[92:93], v233 offset:8704\n" \
"ds_read_b64_tr_b16 a[94:95], v233 offset:10752\n" \
"v_mfma_f32_16x16x32_bf16 a[176:179], a[28:31], a[100:103], a[176:179]\n" \
"s_xor_b32 s77, s77, 0x500\n" \
"v_cvt_pk_bf16_f32 v210, v212, v213\n" \
"v_cvt_pk_bf16_f32 v211, v214, v215\n" \
"v_mfma_f32_16x16x32_bf16 a[192:195], a[32:35], a[96:99], a[192:195]\n" \
"s_add_u32 m0, s79, 32768\n" \
"v_mul_f32 v200, s76, v200\n" \
"v_mul_f32 v201, s76, v201\n" \
"v_mfma_f32_16x16x32_bf16 a[192:195], a[36:39], a[100:103], a[192:195]\n" \
"buffer_load_dwordx4 v228, s[52:55], s78 offen offset:0 lds\n" \
"v_mfma_f32_16x16x32_bf16 a[196:199], a[32:35], a[104:107], a[196:199]\n" \
"ds_read_b128 a[0:3], v250 offset:0\n" \
"ds_read_b128 a[4:7], v251 offset:0\n" \
"v_mfma_f32_16x16x32_bf16 a[196:199], a[36:39], a[108:111], a[196:199]\n" \
"v_mul_f32 v202, s76, v202\n" \
"v_mul_f32 v203, s76, v203\n" \
"v_mfma_f32_16x16x32_bf16 a[200:203], a[32:35], a[112:115], a[200:203]\n" \
"v_cvt_pk_bf16_f32 v200, v200, v201\n" \
"v_cvt_pk_bf16_f32 v201, v202, v203\n" \
"v_mfma_f32_16x16x32_bf16 a[200:203], a[36:39], a[116:119], a[200:203]\n" \
"buffer_load_dwordx4 v229, s[52:55], s78 offen offset:1024 lds\n" \
"v_mfma_f32_16x16x32_bf16 a[204:207], a[32:35], a[120:123], a[204:207]\n" \
"ds_read_b128 a[8:11], v250 offset:2048\n" \
"ds_read_b128 a[12:15], v251 offset:2048\n" \
"v_mfma_f32_16x16x32_bf16 a[204:207], a[36:39], a[124:127], a[204:207]\n" \
"v_mul_f32 v204, s76, v204\n" \
"v_mul_f32 v205, s76, v205\n" \
"v_mfma_f32_16x16x32_bf16 a[220:223], a[40:43], a[120:123], a[220:223]\n" \
"s_xor_b32 s78, s78, 0x500000\n" \
"v_mul_f32 v206, s76, v206\n" \
"v_mul_f32 v207, s76, v207\n" \
"v_mfma_f32_16x16x32_bf16 a[220:223], a[44:47], a[124:127], a[220:223]\n" \
"buffer_load_dwordx4 v228, s[52:55], s89 offen offset:2048 lds\n" \
"v_mfma_f32_16x16x32_bf16 a[216:219], a[40:43], a[112:115], a[216:219]\n" \
"ds_read_b128 a[16:19], v250 offset:4096\n" \
"ds_read_b128 a[20:23], v251 offset:4096\n" \
"v_mfma_f32_16x16x32_bf16 a[216:219], a[44:47], a[116:119], a[216:219]\n" \
"v_cvt_pk_bf16_f32 v202, v204, v205\n" \
"v_cvt_pk_bf16_f32 v203, v206, v207\n" \
"v_mfma_f32_16x16x32_bf16 a[212:215], a[40:43], a[104:107], a[212:215]\n" \
"v_mul_f32 v216, s76, v216\n" \
"v_mul_f32 v217, s76, v217\n" \
"v_mfma_f32_16x16x32_bf16 a[212:215], a[44:47], a[108:111], a[212:215]\n" \
"buffer_load_dwordx4 v229, s[52:55], s89 offen offset:3072 lds\n" \
"v_mfma_f32_16x16x32_bf16 a[208:211], a[40:43], a[96:99], a[208:211]\n" \
"v_mul_f32 v218, s76, v218\n" \
"v_mul_f32 v219, s76, v219\n" \
"v_mfma_f32_16x16x32_bf16 a[208:211], a[44:47], a[100:103], a[208:211]\n" \
"s_xor_b32 s89, s89, 0x500000\n" \
"v_cvt_pk_bf16_f32 v216, v216, v217\n" \
"v_cvt_pk_bf16_f32 v217, v218, v219\n" \
"v_mfma_f32_16x16x32_bf16 a[224:227], a[48:51], a[96:99], a[224:227]\n" \
"s_add_u32 m0, s79, 49152\n" \
"v_mul_f32 v220, s76, v220\n" \
"v_mul_f32 v221, s76, v221\n" \
"v_mfma_f32_16x16x32_bf16 a[224:227], a[52:55], a[100:103], a[224:227]\n" \
"buffer_load_dwordx4 v228, s[52:55], s78 offen offset:0 lds\n" \
"v_mfma_f32_16x16x32_bf16 a[228:231], a[48:51], a[104:107], a[228:231]\n" \
"v_mul_f32 v222, s76, v222\n" \
"v_mul_f32 v223, s76, v223\n" \
"v_mfma_f32_16x16x32_bf16 a[228:231], a[52:55], a[108:111], a[228:231]\n" \
"v_cvt_pk_bf16_f32 v218, v220, v221\n" \
"v_cvt_pk_bf16_f32 v219, v222, v223\n" \
"v_mfma_f32_16x16x32_bf16 a[232:235], a[48:51], a[112:115], a[232:235]\n" \
"v_mul_f32 v144, s76, v144\n" \
"v_mul_f32 v145, s76, v145\n" \
"v_mfma_f32_16x16x32_bf16 a[232:235], a[52:55], a[116:119], a[232:235]\n" \
"buffer_load_dwordx4 v229, s[52:55], s78 offen offset:1024 lds\n" \
"v_mfma_f32_16x16x32_bf16 a[236:239], a[48:51], a[120:123], a[236:239]\n" \
"v_mul_f32 v146, s76, v146\n" \
"v_mul_f32 v147, s76, v147\n" \
"v_mfma_f32_16x16x32_bf16 a[236:239], a[52:55], a[124:127], a[236:239]\n" \
"v_cvt_pk_bf16_f32 v196, v144, v145\n" \
"v_cvt_pk_bf16_f32 v197, v146, v147\n" \
"v_mfma_f32_16x16x32_bf16 a[252:255], a[56:59], a[120:123], a[252:255]\n" \
"s_xor_b32 s78, s78, 0x180000\n" \
"v_mul_f32 v148, s76, v148\n" \
"v_mul_f32 v149, s76, v149\n" \
"v_mfma_f32_16x16x32_bf16 a[252:255], a[60:63], a[124:127], a[252:255]\n" \
"buffer_load_dwordx4 v228, s[52:55], s89 offen offset:2048 lds\n" \
"v_mfma_f32_16x16x32_bf16 a[248:251], a[56:59], a[112:115], a[248:251]\n" \
"v_mul_f32 v150, s76, v150\n" \
"v_mul_f32 v151, s76, v151\n" \
"v_mfma_f32_16x16x32_bf16 a[248:251], a[60:63], a[116:119], a[248:251]\n" \
"v_cvt_pk_bf16_f32 v198, v148, v149\n" \
"v_cvt_pk_bf16_f32 v199, v150, v151\n" \
"v_mfma_f32_16x16x32_bf16 a[244:247], a[56:59], a[104:107], a[244:247]\n" \
"v_mul_f32 v176, s76, v176\n" \
"v_mul_f32 v177, s76, v177\n" \
"v_mfma_f32_16x16x32_bf16 a[244:247], a[60:63], a[108:111], a[244:247]\n" \
"buffer_load_dwordx4 v229, s[52:55], s89 offen offset:3072 lds\n" \
"v_mfma_f32_16x16x32_bf16 a[240:243], a[56:59], a[96:99], a[240:243]\n" \
"s_waitcnt lgkmcnt(0)\n" \
"v_mul_f32 v178, s76, v178\n" \
"v_mul_f32 v179, s76, v179\n" \
"v_mfma_f32_16x16x32_bf16 a[240:243], a[60:63], a[100:103], a[240:243]\n" \
"s_xor_b32 s89, s89, 0x180000\n" \
"v_cvt_pk_bf16_f32 v212, v176, v177\n" \
"v_cvt_pk_bf16_f32 v213, v178, v179\n" \
"v_mfma_f32_16x16x32_bf16 v[64:67], a[64:67], a[0:3], 0\n" \
"ds_read_b128 a[24:27], v250 offset:6144\n" \
"v_mul_f32 v180, s76, v180\n" \
"v_mfma_f32_16x16x32_bf16 v[64:67], a[68:71], a[4:7], v[64:67]\n" \
"s_add_u32 m0, s74, 131072\n" \
"ds_read_b128 a[28:31], v251 offset:6144\n" \
"v_mul_f32 v181, s76, v181\n" \
"v_mfma_f32_16x16x32_bf16 v[68:71], a[72:75], a[0:3], 0\n" \
"ds_read_b64_tr_b16 a[96:97], v232 offset:1024\n" \
"v_mul_f32 v182, s76, v182\n" \
"v_mfma_f32_16x16x32_bf16 v[68:71], a[76:79], a[4:7], v[68:71]\n" \
"buffer_load_dwordx4 v224, s[48:51], s75 offen offset:0 lds\n" \
"v_mfma_f32_16x16x32_bf16 v[72:75], a[80:83], a[0:3], 0\n" \
"ds_read_b64_tr_b16 a[98:99], v232 offset:3072\n" \
"v_mul_f32 v183, s76, v183\n" \
"v_mfma_f32_16x16x32_bf16 v[72:75], a[84:87], a[4:7], v[72:75]\n" \
"ds_read_b64_tr_b16 a[100:101], v232 offset:9216\n" \
"v_cvt_pk_bf16_f32 v214, v180, v181\n" \
"v_mfma_f32_16x16x32_bf16 v[76:79], a[88:91], a[0:3], 0\n" \
"ds_read_b64_tr_b16 a[102:103], v232 offset:11264\n" \
"v_cvt_pk_bf16_f32 v215, v182, v183\n" \
"v_mfma_f32_16x16x32_bf16 v[76:79], a[92:95], a[4:7], v[76:79]\n" \
"buffer_load_dwordx4 v225, s[48:51], s75 offen offset:1024 lds\n" \
"v_mfma_f32_16x16x32_bf16 v[108:111], a[88:91], a[8:11], 0\n" \
"ds_read_b64_tr_b16 a[104:105], v233 offset:1024\n" \
"v_mul_f32 v152, s76, v152\n" \
"v_mfma_f32_16x16x32_bf16 v[108:111], a[92:95], a[12:15], v[108:111]\n" \
"ds_read_b64_tr_b16 a[106:107], v233 offset:3072\n" \
"v_mul_f32 v153, s76, v153\n" \
"v_mfma_f32_16x16x32_bf16 v[104:107], a[80:83], a[8:11], 0\n" \
"ds_read_b64_tr_b16 a[108:109], v233 offset:9216\n" \
"v_mul_f32 v154, s76, v154\n" \
"v_mfma_f32_16x16x32_bf16 v[104:107], a[84:87], a[12:15], v[104:107]\n" \
"buffer_load_dwordx4 v226, s[48:51], s75 offen offset:2048 lds\n" \
"v_mfma_f32_16x16x32_bf16 v[100:103], a[72:75], a[8:11], 0\n" \
"ds_read_b64_tr_b16 a[110:111], v233 offset:11264\n" \
"v_mul_f32 v155, s76, v155\n" \
"v_mfma_f32_16x16x32_bf16 v[100:103], a[76:79], a[12:15], v[100:103]\n" \
"ds_read_b64_tr_b16 a[112:113], v232 offset:1536\n" \
"v_cvt_pk_bf16_f32 v204, v152, v153\n" \
"v_mfma_f32_16x16x32_bf16 v[96:99], a[64:67], a[8:11], 0\n" \
"ds_read_b64_tr_b16 a[114:115], v232 offset:3584\n" \
"v_cvt_pk_bf16_f32 v205, v154, v155\n" \
"v_mfma_f32_16x16x32_bf16 v[96:99], a[68:71], a[12:15], v[96:99]\n" \
"buffer_load_dwordx4 v227, s[48:51], s75 offen offset:3072 lds\n" \
"v_mfma_f32_16x16x32_bf16 v[128:131], a[64:67], a[16:19], 0\n" \
"s_xor_b32 s75, s75, 0x180\n" \
"ds_read_b64_tr_b16 a[116:117], v232 offset:9728\n" \
"v_mul_f32 v156, s76, v156\n" \
"v_mfma_f32_16x16x32_bf16 v[128:131], a[68:71], a[20:23], v[128:131]\n" \
"s_add_u32 m0, s74, 135168\n" \
"ds_read_b64_tr_b16 a[118:119], v232 offset:11776\n" \
"v_mul_f32 v157, s76, v157\n" \
"v_mfma_f32_16x16x32_bf16 v[132:135], a[72:75], a[16:19], 0\n" \
"ds_read_b64_tr_b16 a[120:121], v233 offset:1536\n" \
"v_mul_f32 v158, s76, v158\n" \
"Lpg2209_%=:\n" \
"Lst0x3_%=:\n" \
"s_cbranch_execz Lpg4447_%=\n" \
"v_mfma_f32_16x16x32_bf16 v[132:135], a[76:79], a[20:23], v[132:135]\n" \
"buffer_load_dwordx4 v224, s[48:51], s77 offen offset:0 lds\n" \
"v_mfma_f32_16x16x32_bf16 v[136:139], a[80:83], a[16:19], 0\n" \
"ds_read_b64_tr_b16 a[122:123], v233 offset:3584\n" \
"v_mul_f32 v159, s76, v159\n" \
"v_mfma_f32_16x16x32_bf16 v[136:139], a[84:87], a[20:23], v[136:139]\n" \
"ds_read_b64_tr_b16 a[124:125], v233 offset:9728\n" \
"v_cvt_pk_bf16_f32 v206, v156, v157\n" \
"v_mfma_f32_16x16x32_bf16 v[140:143], a[88:91], a[16:19], 0\n" \
"s_waitcnt lgkmcnt(3)\n" \
"ds_read_b64_tr_b16 a[126:127], v233 offset:11776\n" \
"v_cvt_pk_bf16_f32 v207, v158, v159\n" \
"v_mfma_f32_16x16x32_bf16 v[140:143], a[92:95], a[20:23], v[140:143]\n" \
"buffer_load_dwordx4 v225, s[48:51], s77 offen offset:1024 lds\n" \
"v_mfma_f32_16x16x32_bf16 v[172:175], a[88:91], a[24:27], 0\n" \
"s_add_u32 s93, s87, 8192\n" \
"v_mul_f32 v184, s76, v184\n" \
"v_mul_f32 v185, s76, v185\n" \
"v_mfma_f32_16x16x32_bf16 v[172:175], a[92:95], a[28:31], v[172:175]\n" \
"buffer_store_dwordx4 v[192:195], v236, s[56:59], s93 offen offset:0 sc1\n" \
"v_mfma_f32_16x16x32_bf16 v[168:171], a[80:83], a[24:27], 0\n" \
"v_mul_f32 v186, s76, v186\n" \
"v_mul_f32 v187, s76, v187\n" \
"v_mfma_f32_16x16x32_bf16 v[168:171], a[84:87], a[28:31], v[168:171]\n" \
"buffer_load_dwordx4 v226, s[48:51], s77 offen offset:2048 lds\n" \
"v_mfma_f32_16x16x32_bf16 v[164:167], a[72:75], a[24:27], 0\n" \
"v_cvt_pk_bf16_f32 v220, v184, v185\n" \
"v_cvt_pk_bf16_f32 v221, v186, v187\n" \
"v_mfma_f32_16x16x32_bf16 v[164:167], a[76:79], a[28:31], v[164:167]\n" \
"buffer_load_dwordx4 v227, s[48:51], s77 offen offset:3072 lds\n" \
"v_mfma_f32_16x16x32_bf16 v[160:163], a[64:67], a[24:27], 0\n" \
"v_mul_f32 v188, s76, v188\n" \
"v_mul_f32 v189, s76, v189\n" \
"v_mfma_f32_16x16x32_bf16 v[160:163], a[68:71], a[28:31], v[160:163]\n" \
"s_waitcnt vmcnt(43) lgkmcnt(0)\n" \
"s_barrier\n" \
"v_mfma_f32_16x16x32_bf16 v[80:83], a[96:99], a[0:3], 0\n" \
"s_xor_b32 s77, s77, 0x180\n" \
"ds_read_b64_tr_b16 a[64:65], v232 offset:16384\n" \
"v_mul_f32 v190, s76, v190\n" \
"v_mfma_f32_16x16x32_bf16 v[80:83], a[100:103], a[4:7], v[80:83]\n" \
"ds_read_b64_tr_b16 a[66:67], v232 offset:18432\n" \
"v_mul_f32 v191, s76, v191\n" \
"v_mfma_f32_16x16x32_bf16 v[84:87], a[104:107], a[0:3], 0\n" \
"s_add_u32 m0, s79, 0\n" \
"ds_read_b64_tr_b16 a[68:69], v232 offset:24576\n" \
"v_cvt_pk_bf16_f32 v222, v188, v189\n" \
"v_mfma_f32_16x16x32_bf16 v[84:87], a[108:111], a[4:7], v[84:87]\n" \
"buffer_load_dwordx4 v228, s[52:55], s78 offen offset:0 lds\n" \
"v_mfma_f32_16x16x32_bf16 v[88:91], a[112:115], a[0:3], 0\n" \
"ds_read_b64_tr_b16 a[70:71], v232 offset:26624\n" \
"v_cvt_pk_bf16_f32 v223, v190, v191\n" \
"v_mfma_f32_16x16x32_bf16 v[88:91], a[116:119], a[4:7], v[88:91]\n" \
"ds_read_b64_tr_b16 a[72:73], v233 offset:16384\n" \
"v_accvgpr_read_b32 v243, a224\n" \
"v_mfma_f32_16x16x32_bf16 v[92:95], a[120:123], a[0:3], 0\n" \
"ds_read_b64_tr_b16 a[74:75], v233 offset:18432\n" \
"v_accvgpr_read_b32 v252, a225\n" \
"v_mfma_f32_16x16x32_bf16 v[92:95], a[124:127], a[4:7], v[92:95]\n" \
"buffer_load_dwordx4 v229, s[52:55], s78 offen offset:1024 lds\n" \
"v_mfma_f32_16x16x32_bf16 v[124:127], a[120:123], a[8:11], 0\n" \
"s_waitcnt vmcnt(33)\n" \
"ds_read_b128 a[0:3], v230 offset:0\n" \
"v_accvgpr_read_b32 v253, a226\n" \
"v_mfma_f32_16x16x32_bf16 v[124:127], a[124:127], a[12:15], v[124:127]\n" \
"ds_read_b128 a[4:7], v231 offset:0\n" \
"v_accvgpr_read_b32 v254, a227\n" \
"v_mfma_f32_16x16x32_bf16 v[120:123], a[112:115], a[8:11], 0\n" \
"s_xor_b32 s78, s78, 0x300000\n" \
"ds_read_b64_tr_b16 a[76:77], v233 offset:24576\n" \
"v_mul_f32 v243, s31, v243\n" \
"v_mfma_f32_16x16x32_bf16 v[120:123], a[116:119], a[12:15], v[120:123]\n" \
"buffer_load_dwordx4 v228, s[52:55], s89 offen offset:2048 lds\n" \
"v_mfma_f32_16x16x32_bf16 v[116:119], a[104:107], a[8:11], 0\n" \
"ds_read_b64_tr_b16 a[78:79], v233 offset:26624\n" \
"v_mul_f32 v252, s31, v252\n" \
"v_mfma_f32_16x16x32_bf16 v[116:119], a[108:111], a[12:15], v[116:119]\n" \
"ds_read_b64_tr_b16 a[80:81], v232 offset:16896\n" \
"v_mul_f32 v253, s31, v253\n" \
"v_mfma_f32_16x16x32_bf16 v[112:115], a[96:99], a[8:11], 0\n" \
"ds_read_b64_tr_b16 a[82:83], v232 offset:18944\n" \
"v_mul_f32 v254, s31, v254\n" \
"v_mfma_f32_16x16x32_bf16 v[112:115], a[100:103], a[12:15], v[112:115]\n" \
"buffer_load_dwordx4 v229, s[52:55], s89 offen offset:3072 lds\n" \
"v_mfma_f32_16x16x32_bf16 v[144:147], a[96:99], a[16:19], 0\n" \
"s_waitcnt vmcnt(33)\n" \
"s_xor_b32 s89, s89, 0x300000\n" \
"ds_read_b128 a[8:11], v230 offset:2048\n" \
"v_mfma_f32_16x16x32_bf16 v[144:147], a[100:103], a[20:23], v[144:147]\n" \
"s_add_u32 m0, s74, 65536\n" \
"ds_read_b128 a[12:15], v231 offset:2048\n" \
"v_cvt_pk_bf16_f32 v243, v243, v252\n" \
"v_mfma_f32_16x16x32_bf16 v[148:151], a[104:107], a[16:19], 0\n" \
"s_waitcnt lgkmcnt(6)\n" \
"ds_read_b64_tr_b16 a[84:85], v232 offset:25088\n" \
"v_cvt_pk_bf16_f32 v253, v253, v254\n" \
"v_mfma_f32_16x16x32_bf16 v[148:151], a[108:111], a[20:23], v[148:151]\n" \
"buffer_load_dwordx4 v224, s[48:51], s75 offen offset:0 lds\n" \
"v_mfma_f32_16x16x32_bf16 v[152:155], a[112:115], a[16:19], 0\n" \
"ds_read_b64_tr_b16 a[86:87], v232 offset:27136\n" \
"v_accvgpr_write_b32 a224, v243\n" \
"v_mfma_f32_16x16x32_bf16 v[152:155], a[116:119], a[20:23], v[152:155]\n" \
"ds_read_b64_tr_b16 a[88:89], v233 offset:16896\n" \
"v_accvgpr_write_b32 a225, v253\n" \
"v_mfma_f32_16x16x32_bf16 v[156:159], a[120:123], a[16:19], 0\n" \
"ds_read_b64_tr_b16 a[90:91], v233 offset:18944\n" \
"v_accvgpr_read_b32 v243, a240\n" \
"v_mfma_f32_16x16x32_bf16 v[156:159], a[124:127], a[20:23], v[156:159]\n" \
"buffer_load_dwordx4 v225, s[48:51], s75 offen offset:1024 lds\n" \
"v_mfma_f32_16x16x32_bf16 v[188:191], a[120:123], a[24:27], 0\n" \
"s_waitcnt vmcnt(33)\n" \
"ds_read_b128 a[16:19], v230 offset:4096\n" \
"v_accvgpr_read_b32 v252, a241\n" \
"v_mfma_f32_16x16x32_bf16 v[188:191], a[124:127], a[28:31], v[188:191]\n" \
"ds_read_b128 a[20:23], v231 offset:4096\n" \
"v_accvgpr_read_b32 v253, a242\n" \
"v_mfma_f32_16x16x32_bf16 v[184:187], a[112:115], a[24:27], 0\n" \
"s_waitcnt lgkmcnt(6)\n" \
"ds_read_b64_tr_b16 a[92:93], v233 offset:25088\n" \
"v_accvgpr_read_b32 v254, a243\n" \
"v_mfma_f32_16x16x32_bf16 v[184:187], a[116:119], a[28:31], v[184:187]\n" \
"buffer_load_dwordx4 v226, s[48:51], s75 offen offset:2048 lds\n" \
"v_mfma_f32_16x16x32_bf16 v[180:183], a[104:107], a[24:27], 0\n" \
"ds_read_b64_tr_b16 a[94:95], v233 offset:27136\n" \
"v_mul_f32 v243, s31, v243\n" \
"v_mfma_f32_16x16x32_bf16 v[180:183], a[108:111], a[28:31], v[180:183]\n" \
"v_mul_f32 v252, s31, v252\n" \
"v_mul_f32 v253, s31, v253\n" \
"v_mfma_f32_16x16x32_bf16 v[176:179], a[96:99], a[24:27], 0\n" \
"v_mul_f32 v254, s31, v254\n" \
"v_cvt_pk_bf16_f32 v243, v243, v252\n" \
"v_mfma_f32_16x16x32_bf16 v[176:179], a[100:103], a[28:31], v[176:179]\n" \
"buffer_load_dwordx4 v227, s[48:51], s75 offen offset:3072 lds\n" \
"v_mfma_f32_16x16x32_bf16 v[64:67], a[64:67], a[0:3], v[64:67]\n" \
"s_waitcnt vmcnt(33)\n" \
"s_xor_b32 s75, s75, 0x300\n" \
"ds_read_b128 a[24:27], v230 offset:6144\n" \
"v_mfma_f32_16x16x32_bf16 v[64:67], a[68:71], a[4:7], v[64:67]\n" \
"s_add_u32 m0, s74, 69632\n" \
"ds_read_b128 a[28:31], v231 offset:6144\n" \
"v_cvt_pk_bf16_f32 v253, v253, v254\n" \
"v_mfma_f32_16x16x32_bf16 v[68:71], a[72:75], a[0:3], v[68:71]\n" \
"s_waitcnt lgkmcnt(4)\n" \
"ds_read_b64_tr_b16 a[96:97], v232 offset:17408\n" \
"v_accvgpr_write_b32 a226, v243\n" \
"v_mfma_f32_16x16x32_bf16 v[68:71], a[76:79], a[4:7], v[68:71]\n" \
"buffer_load_dwordx4 v224, s[48:51], s77 offen offset:0 lds\n" \
"v_mfma_f32_16x16x32_bf16 v[72:75], a[80:83], a[0:3], v[72:75]\n" \
"ds_read_b64_tr_b16 a[98:99], v232 offset:19456\n" \
"v_accvgpr_write_b32 a227, v253\n" \
"v_mfma_f32_16x16x32_bf16 v[72:75], a[84:87], a[4:7], v[72:75]\n" \
"s_waitcnt lgkmcnt(3)\n" \
"ds_read_b64_tr_b16 a[100:101], v232 offset:25600\n" \
"v_accvgpr_read_b32 v243, a228\n" \
"v_mfma_f32_16x16x32_bf16 v[76:79], a[88:91], a[0:3], v[76:79]\n" \
"ds_read_b64_tr_b16 a[102:103], v232 offset:27648\n" \
"v_accvgpr_read_b32 v252, a229\n" \
"v_mfma_f32_16x16x32_bf16 v[76:79], a[92:95], a[4:7], v[76:79]\n" \
"buffer_load_dwordx4 v225, s[48:51], s77 offen offset:1024 lds\n" \
"v_mfma_f32_16x16x32_bf16 v[108:111], a[88:91], a[8:11], v[108:111]\n" \
"ds_read_b64_tr_b16 a[104:105], v233 offset:17408\n" \
"v_accvgpr_read_b32 v253, a230\n" \
"v_mfma_f32_16x16x32_bf16 v[108:111], a[92:95], a[12:15], v[108:111]\n" \
"ds_read_b64_tr_b16 a[106:107], v233 offset:19456\n" \
"v_accvgpr_read_b32 v254, a231\n" \
"v_mfma_f32_16x16x32_bf16 v[104:107], a[80:83], a[8:11], v[104:107]\n" \
"s_waitcnt lgkmcnt(6)\n" \
"ds_read_b64_tr_b16 a[108:109], v233 offset:25600\n" \
"v_mul_f32 v243, s31, v243\n" \
"v_mfma_f32_16x16x32_bf16 v[104:107], a[84:87], a[12:15], v[104:107]\n" \
"buffer_load_dwordx4 v226, s[48:51], s77 offen offset:2048 lds\n" \
"v_mfma_f32_16x16x32_bf16 v[100:103], a[72:75], a[8:11], v[100:103]\n" \
"ds_read_b64_tr_b16 a[110:111], v233 offset:27648\n" \
"v_mul_f32 v252, s31, v252\n" \
"v_mfma_f32_16x16x32_bf16 v[100:103], a[76:79], a[12:15], v[100:103]\n" \
"ds_read_b64_tr_b16 a[112:113], v232 offset:17920\n" \
"v_mul_f32 v253, s31, v253\n" \
"v_mfma_f32_16x16x32_bf16 v[96:99], a[64:67], a[8:11], v[96:99]\n" \
"ds_read_b64_tr_b16 a[114:115], v232 offset:19968\n" \
"v_mul_f32 v254, s31, v254\n" \
"v_mfma_f32_16x16x32_bf16 v[96:99], a[68:71], a[12:15], v[96:99]\n" \
"buffer_load_dwordx4 v227, s[48:51], s77 offen offset:3072 lds\n" \
"v_mfma_f32_16x16x32_bf16 v[128:131], a[64:67], a[16:19], v[128:131]\n" \
"ds_read_b64_tr_b16 a[116:117], v232 offset:26112\n" \
"v_cvt_pk_bf16_f32 v243, v243, v252\n" \
"v_mfma_f32_16x16x32_bf16 v[128:131], a[68:71], a[20:23], v[128:131]\n" \
"ds_read_b64_tr_b16 a[118:119], v232 offset:28160\n" \
"v_cvt_pk_bf16_f32 v253, v253, v254\n" \
"v_mfma_f32_16x16x32_bf16 v[132:135], a[72:75], a[16:19], v[132:135]\n" \
"s_xor_b32 s77, s77, 0x300\n" \
"ds_read_b64_tr_b16 a[120:121], v233 offset:17920\n" \
"v_accvgpr_write_b32 a228, v243\n" \
"v_mfma_f32_16x16x32_bf16 v[132:135], a[76:79], a[20:23], v[132:135]\n" \
"v_accvgpr_write_b32 a229, v253\n" \
"v_accvgpr_read_b32 v243, a244\n" \
"v_mfma_f32_16x16x32_bf16 v[136:139], a[80:83], a[16:19], v[136:139]\n" \
"ds_read_b64_tr_b16 a[122:123], v233 offset:19968\n" \
"v_accvgpr_read_b32 v252, a245\n" \
"v_mfma_f32_16x16x32_bf16 v[136:139], a[84:87], a[20:23], v[136:139]\n" \
"ds_read_b64_tr_b16 a[124:125], v233 offset:26112\n" \
"v_accvgpr_read_b32 v253, a246\n" \
"v_mfma_f32_16x16x32_bf16 v[140:143], a[88:91], a[16:19], v[140:143]\n" \
"ds_read_b64_tr_b16 a[126:127], v233 offset:28160\n" \
"v_accvgpr_read_b32 v254, a247\n" \
"v_mfma_f32_16x16x32_bf16 v[140:143], a[92:95], a[20:23], v[140:143]\n" \
"buffer_store_dwordx4 v[208:211], v236, s[56:59], s93 offen offset:256 sc1\n" \
"v_mfma_f32_16x16x32_bf16 v[172:175], a[88:91], a[24:27], v[172:175]\n" \
"v_mul_f32 v243, s31, v243\n" \
"v_mul_f32 v252, s31, v252\n" \
"v_mfma_f32_16x16x32_bf16 v[172:175], a[92:95], a[28:31], v[172:175]\n" \
"v_mul_f32 v253, s31, v253\n" \
"v_mul_f32 v254, s31, v254\n" \
"v_mfma_f32_16x16x32_bf16 v[168:171], a[80:83], a[24:27], v[168:171]\n" \
"v_cvt_pk_bf16_f32 v243, v243, v252\n" \
"v_cvt_pk_bf16_f32 v253, v253, v254\n" \
"v_mfma_f32_16x16x32_bf16 v[168:171], a[84:87], a[28:31], v[168:171]\n" \
"v_accvgpr_write_b32 a230, v243\n" \
"v_accvgpr_write_b32 a231, v253\n" \
"v_mfma_f32_16x16x32_bf16 v[164:167], a[72:75], a[24:27], v[164:167]\n" \
"v_accvgpr_read_b32 v243, a232\n" \
"v_accvgpr_read_b32 v252, a233\n" \
"v_mfma_f32_16x16x32_bf16 v[164:167], a[76:79], a[28:31], v[164:167]\n" \
"v_accvgpr_read_b32 v253, a234\n" \
"v_accvgpr_read_b32 v254, a235\n" \
"v_mfma_f32_16x16x32_bf16 v[160:163], a[64:67], a[24:27], v[160:163]\n" \
"v_mul_f32 v243, s31, v243\n" \
"v_mul_f32 v252, s31, v252\n" \
"v_mfma_f32_16x16x32_bf16 v[160:163], a[68:71], a[28:31], v[160:163]\n" \
"s_waitcnt vmcnt(26) lgkmcnt(0)\n" \
"s_barrier\n" \
"v_mfma_f32_16x16x32_bf16 v[80:83], a[96:99], a[0:3], v[80:83]\n" \
"ds_read_b64_tr_b16 a[64:65], v232 offset:32768\n" \
"v_mul_f32 v253, s31, v253\n" \
"v_mfma_f32_16x16x32_bf16 v[80:83], a[100:103], a[4:7], v[80:83]\n" \
"ds_read_b64_tr_b16 a[66:67], v232 offset:34816\n" \
"v_mul_f32 v254, s31, v254\n" \
"v_mfma_f32_16x16x32_bf16 v[84:87], a[104:107], a[0:3], v[84:87]\n" \
"s_add_u32 m0, s79, 16384\n" \
"ds_read_b64_tr_b16 a[68:69], v232 offset:40960\n" \
"v_cvt_pk_bf16_f32 v243, v243, v252\n" \
"v_mfma_f32_16x16x32_bf16 v[84:87], a[108:111], a[4:7], v[84:87]\n" \
"buffer_load_dwordx4 v228, s[52:55], s78 offen offset:0 lds\n" \
"v_mfma_f32_16x16x32_bf16 v[88:91], a[112:115], a[0:3], v[88:91]\n" \
"ds_read_b64_tr_b16 a[70:71], v232 offset:43008\n" \
"v_cvt_pk_bf16_f32 v253, v253, v254\n" \
"v_mfma_f32_16x16x32_bf16 v[88:91], a[116:119], a[4:7], v[88:91]\n" \
"ds_read_b64_tr_b16 a[72:73], v233 offset:32768\n" \
"v_accvgpr_write_b32 a232, v243\n" \
"v_mfma_f32_16x16x32_bf16 v[92:95], a[120:123], a[0:3], v[92:95]\n" \
"ds_read_b64_tr_b16 a[74:75], v233 offset:34816\n" \
"v_accvgpr_write_b32 a233, v253\n" \
"v_mfma_f32_16x16x32_bf16 v[92:95], a[124:127], a[4:7], v[92:95]\n" \
"buffer_load_dwordx4 v229, s[52:55], s78 offen offset:1024 lds\n" \
"v_mfma_f32_16x16x32_bf16 v[124:127], a[120:123], a[8:11], v[124:127]\n" \
"ds_read_b128 a[0:3], v230 offset:32768\n" \
"v_accvgpr_read_b32 v243, a248\n" \
"v_mfma_f32_16x16x32_bf16 v[124:127], a[124:127], a[12:15], v[124:127]\n" \
"ds_read_b128 a[4:7], v231 offset:32768\n" \
"v_accvgpr_read_b32 v252, a249\n" \
"v_mfma_f32_16x16x32_bf16 v[120:123], a[112:115], a[8:11], v[120:123]\n" \
"s_xor_b32 s78, s78, 0x80000\n" \
"ds_read_b64_tr_b16 a[76:77], v233 offset:40960\n" \
"v_accvgpr_read_b32 v253, a250\n" \
"v_mfma_f32_16x16x32_bf16 v[120:123], a[116:119], a[12:15], v[120:123]\n" \
"buffer_load_dwordx4 v228, s[52:55], s89 offen offset:2048 lds\n" \
"v_mfma_f32_16x16x32_bf16 v[116:119], a[104:107], a[8:11], v[116:119]\n" \
"ds_read_b64_tr_b16 a[78:79], v233 offset:43008\n" \
"v_accvgpr_read_b32 v254, a251\n" \
"v_mfma_f32_16x16x32_bf16 v[116:119], a[108:111], a[12:15], v[116:119]\n" \
"ds_read_b64_tr_b16 a[80:81], v232 offset:33280\n" \
"v_mul_f32 v243, s31, v243\n" \
"v_mfma_f32_16x16x32_bf16 v[112:115], a[96:99], a[8:11], v[112:115]\n" \
"ds_read_b64_tr_b16 a[82:83], v232 offset:35328\n" \
"v_mul_f32 v252, s31, v252\n" \
"v_mfma_f32_16x16x32_bf16 v[112:115], a[100:103], a[12:15], v[112:115]\n" \
"buffer_load_dwordx4 v229, s[52:55], s89 offen offset:3072 lds\n" \
"v_mfma_f32_16x16x32_bf16 v[144:147], a[96:99], a[16:19], v[144:147]\n" \
"s_xor_b32 s89, s89, 0x80000\n" \
"ds_read_b128 a[8:11], v230 offset:34816\n" \
"v_mfma_f32_16x16x32_bf16 v[144:147], a[100:103], a[20:23], v[144:147]\n" \
"s_add_u32 m0, s74, 98304\n" \
"ds_read_b128 a[12:15], v231 offset:34816\n" \
"v_mul_f32 v253, s31, v253\n" \
"v_mfma_f32_16x16x32_bf16 v[148:151], a[104:107], a[16:19], v[148:151]\n" \
"s_waitcnt lgkmcnt(6)\n" \
"ds_read_b64_tr_b16 a[84:85], v232 offset:41472\n" \
"v_mul_f32 v254, s31, v254\n" \
"v_mfma_f32_16x16x32_bf16 v[148:151], a[108:111], a[20:23], v[148:151]\n" \
"buffer_load_dwordx4 v224, s[48:51], s75 offen offset:0 lds\n" \
"v_mfma_f32_16x16x32_bf16 v[152:155], a[112:115], a[16:19], v[152:155]\n" \
"ds_read_b64_tr_b16 a[86:87], v232 offset:43520\n" \
"v_cvt_pk_bf16_f32 v243, v243, v252\n" \
"v_mfma_f32_16x16x32_bf16 v[152:155], a[116:119], a[20:23], v[152:155]\n" \
"ds_read_b64_tr_b16 a[88:89], v233 offset:33280\n" \
"v_cvt_pk_bf16_f32 v253, v253, v254\n" \
"v_mfma_f32_16x16x32_bf16 v[156:159], a[120:123], a[16:19], v[156:159]\n" \
"ds_read_b64_tr_b16 a[90:91], v233 offset:35328\n" \
"v_accvgpr_write_b32 a234, v243\n" \
"v_mfma_f32_16x16x32_bf16 v[156:159], a[124:127], a[20:23], v[156:159]\n" \
"buffer_load_dwordx4 v225, s[48:51], s75 offen offset:1024 lds\n" \
"v_mfma_f32_16x16x32_bf16 v[188:191], a[120:123], a[24:27], v[188:191]\n" \
"ds_read_b128 a[16:19], v230 offset:36864\n" \
"v_accvgpr_write_b32 a235, v253\n" \
"v_mfma_f32_16x16x32_bf16 v[188:191], a[124:127], a[28:31], v[188:191]\n" \
"ds_read_b128 a[20:23], v231 offset:36864\n" \
"v_accvgpr_read_b32 v243, a236\n" \
"v_mfma_f32_16x16x32_bf16 v[184:187], a[112:115], a[24:27], v[184:187]\n" \
"s_waitcnt lgkmcnt(6)\n" \
"ds_read_b64_tr_b16 a[92:93], v233 offset:41472\n" \
"v_accvgpr_read_b32 v252, a237\n" \
"v_mfma_f32_16x16x32_bf16 v[184:187], a[116:119], a[28:31], v[184:187]\n" \
"buffer_load_dwordx4 v226, s[48:51], s75 offen offset:2048 lds\n" \
"v_mfma_f32_16x16x32_bf16 v[180:183], a[104:107], a[24:27], v[180:183]\n" \
"ds_read_b64_tr_b16 a[94:95], v233 offset:43520\n" \
"v_accvgpr_read_b32 v253, a238\n" \
"v_mfma_f32_16x16x32_bf16 v[180:183], a[108:111], a[28:31], v[180:183]\n" \
"v_accvgpr_read_b32 v254, a239\n" \
"v_mul_f32 v243, s31, v243\n" \
"v_mfma_f32_16x16x32_bf16 v[176:179], a[96:99], a[24:27], v[176:179]\n" \
"v_mul_f32 v252, s31, v252\n" \
"v_mul_f32 v253, s31, v253\n" \
"v_mfma_f32_16x16x32_bf16 v[176:179], a[100:103], a[28:31], v[176:179]\n" \
"buffer_load_dwordx4 v227, s[48:51], s75 offen offset:3072 lds\n" \
"v_mfma_f32_16x16x32_bf16 v[64:67], a[64:67], a[0:3], v[64:67]\n" \
"s_xor_b32 s75, s75, 0x80\n" \
"ds_read_b128 a[24:27], v230 offset:38912\n" \
"v_mfma_f32_16x16x32_bf16 v[64:67], a[68:71], a[4:7], v[64:67]\n" \
"s_add_u32 m0, s74, 102400\n" \
"ds_read_b128 a[28:31], v231 offset:38912\n" \
"v_mul_f32 v254, s31, v254\n" \
"v_mfma_f32_16x16x32_bf16 v[68:71], a[72:75], a[0:3], v[68:71]\n" \
"s_waitcnt lgkmcnt(4)\n" \
"ds_read_b64_tr_b16 a[96:97], v232 offset:33792\n" \
"v_cvt_pk_bf16_f32 v243, v243, v252\n" \
"v_mfma_f32_16x16x32_bf16 v[68:71], a[76:79], a[4:7], v[68:71]\n" \
"buffer_load_dwordx4 v224, s[48:51], s77 offen offset:0 lds\n" \
"v_mfma_f32_16x16x32_bf16 v[72:75], a[80:83], a[0:3], v[72:75]\n" \
"ds_read_b64_tr_b16 a[98:99], v232 offset:35840\n" \
"v_cvt_pk_bf16_f32 v253, v253, v254\n" \
"v_mfma_f32_16x16x32_bf16 v[72:75], a[84:87], a[4:7], v[72:75]\n" \
"s_waitcnt lgkmcnt(3)\n" \
"ds_read_b64_tr_b16 a[100:101], v232 offset:41984\n" \
"v_accvgpr_write_b32 a236, v243\n" \
"v_mfma_f32_16x16x32_bf16 v[76:79], a[88:91], a[0:3], v[76:79]\n" \
"ds_read_b64_tr_b16 a[102:103], v232 offset:44032\n" \
"v_accvgpr_write_b32 a237, v253\n" \
"v_mfma_f32_16x16x32_bf16 v[76:79], a[92:95], a[4:7], v[76:79]\n" \
"buffer_load_dwordx4 v225, s[48:51], s77 offen offset:1024 lds\n" \
"v_mfma_f32_16x16x32_bf16 v[108:111], a[88:91], a[8:11], v[108:111]\n" \
"ds_read_b64_tr_b16 a[104:105], v233 offset:33792\n" \
"v_accvgpr_read_b32 v243, a252\n" \
"v_mfma_f32_16x16x32_bf16 v[108:111], a[92:95], a[12:15], v[108:111]\n" \
"ds_read_b64_tr_b16 a[106:107], v233 offset:35840\n" \
"v_accvgpr_read_b32 v252, a253\n" \
"v_mfma_f32_16x16x32_bf16 v[104:107], a[80:83], a[8:11], v[104:107]\n" \
"s_waitcnt lgkmcnt(6)\n" \
"ds_read_b64_tr_b16 a[108:109], v233 offset:41984\n" \
"v_accvgpr_read_b32 v253, a254\n" \
"v_mfma_f32_16x16x32_bf16 v[104:107], a[84:87], a[12:15], v[104:107]\n" \
"buffer_load_dwordx4 v226, s[48:51], s77 offen offset:2048 lds\n" \
"v_mfma_f32_16x16x32_bf16 v[100:103], a[72:75], a[8:11], v[100:103]\n" \
"ds_read_b64_tr_b16 a[110:111], v233 offset:44032\n" \
"v_accvgpr_read_b32 v254, a255\n" \
"v_mfma_f32_16x16x32_bf16 v[100:103], a[76:79], a[12:15], v[100:103]\n" \
"ds_read_b64_tr_b16 a[112:113], v232 offset:34304\n" \
"v_mul_f32 v243, s31, v243\n" \
"v_mfma_f32_16x16x32_bf16 v[96:99], a[64:67], a[8:11], v[96:99]\n" \
"ds_read_b64_tr_b16 a[114:115], v232 offset:36352\n" \
"v_mul_f32 v252, s31, v252\n" \
"v_mfma_f32_16x16x32_bf16 v[96:99], a[68:71], a[12:15], v[96:99]\n" \
"buffer_load_dwordx4 v227, s[48:51], s77 offen offset:3072 lds\n" \
"v_mfma_f32_16x16x32_bf16 v[128:131], a[64:67], a[16:19], v[128:131]\n" \
"ds_read_b64_tr_b16 a[116:117], v232 offset:42496\n" \
"v_mul_f32 v253, s31, v253\n" \
"v_mfma_f32_16x16x32_bf16 v[128:131], a[68:71], a[20:23], v[128:131]\n" \
"ds_read_b64_tr_b16 a[118:119], v232 offset:44544\n" \
"v_mul_f32 v254, s31, v254\n" \
"v_mfma_f32_16x16x32_bf16 v[132:135], a[72:75], a[16:19], v[132:135]\n" \
"s_xor_b32 s77, s77, 0x80\n" \
"ds_read_b64_tr_b16 a[120:121], v233 offset:34304\n" \
"v_cvt_pk_bf16_f32 v243, v243, v252\n" \
"v_mfma_f32_16x16x32_bf16 v[132:135], a[76:79], a[20:23], v[132:135]\n" \
"v_cvt_pk_bf16_f32 v253, v253, v254\n" \
"v_accvgpr_write_b32 a238, v243\n" \
"v_mfma_f32_16x16x32_bf16 v[136:139], a[80:83], a[16:19], v[136:139]\n" \
"ds_read_b64_tr_b16 a[122:123], v233 offset:36352\n" \
"v_accvgpr_write_b32 a239, v253\n" \
"v_mfma_f32_16x16x32_bf16 v[136:139], a[84:87], a[20:23], v[136:139]\n" \
"ds_read_b64_tr_b16 a[124:125], v233 offset:42496\n" \
"v_accvgpr_read_b32 v243, a192\n" \
"v_mfma_f32_16x16x32_bf16 v[140:143], a[88:91], a[16:19], v[140:143]\n" \
"ds_read_b64_tr_b16 a[126:127], v233 offset:44544\n" \
"v_accvgpr_read_b32 v252, a193\n" \
"v_mfma_f32_16x16x32_bf16 v[140:143], a[92:95], a[20:23], v[140:143]\n" \
"buffer_store_dwordx4 v[200:203], v236, s[56:59], s93 offen offset:2048 sc1\n" \
"v_mfma_f32_16x16x32_bf16 v[172:175], a[88:91], a[24:27], v[172:175]\n" \
"v_accvgpr_read_b32 v253, a194\n" \
"v_accvgpr_read_b32 v254, a195\n" \
"v_mfma_f32_16x16x32_bf16 v[172:175], a[92:95], a[28:31], v[172:175]\n" \
"v_mul_f32 v243, s31, v243\n" \
"v_mul_f32 v252, s31, v252\n" \
"v_mfma_f32_16x16x32_bf16 v[168:171], a[80:83], a[24:27], v[168:171]\n" \
"v_mul_f32 v253, s31, v253\n" \
"v_mul_f32 v254, s31, v254\n" \
"v_mfma_f32_16x16x32_bf16 v[168:171], a[84:87], a[28:31], v[168:171]\n" \
"v_cvt_pk_bf16_f32 v243, v243, v252\n" \
"v_cvt_pk_bf16_f32 v253, v253, v254\n" \
"v_mfma_f32_16x16x32_bf16 v[164:167], a[72:75], a[24:27], v[164:167]\n" \
"v_accvgpr_write_b32 a192, v243\n" \
"v_accvgpr_write_b32 a193, v253\n" \
"v_mfma_f32_16x16x32_bf16 v[164:167], a[76:79], a[28:31], v[164:167]\n" \
"v_accvgpr_read_b32 v243, a208\n" \
"v_accvgpr_read_b32 v252, a209\n" \
"v_mfma_f32_16x16x32_bf16 v[160:163], a[64:67], a[24:27], v[160:163]\n" \
"v_accvgpr_read_b32 v253, a210\n" \
"v_accvgpr_read_b32 v254, a211\n" \
"v_mfma_f32_16x16x32_bf16 v[160:163], a[68:71], a[28:31], v[160:163]\n" \
"s_waitcnt vmcnt(35) lgkmcnt(0)\n" \
"s_barrier\n" \
"v_mfma_f32_16x16x32_bf16 v[80:83], a[96:99], a[0:3], v[80:83]\n" \
"ds_read_b64_tr_b16 a[64:65], v232 offset:49152\n" \
"v_mul_f32 v243, s31, v243\n" \
"v_mfma_f32_16x16x32_bf16 v[80:83], a[100:103], a[4:7], v[80:83]\n" \
"ds_read_b64_tr_b16 a[66:67], v232 offset:51200\n" \
"v_mul_f32 v252, s31, v252\n" \
"v_mfma_f32_16x16x32_bf16 v[84:87], a[104:107], a[0:3], v[84:87]\n" \
"s_add_u32 m0, s79, 32768\n" \
"ds_read_b64_tr_b16 a[68:69], v232 offset:57344\n" \
"v_mul_f32 v253, s31, v253\n" \
"v_mfma_f32_16x16x32_bf16 v[84:87], a[108:111], a[4:7], v[84:87]\n" \
"buffer_load_dwordx4 v228, s[52:55], s78 offen offset:0 lds\n" \
"v_mfma_f32_16x16x32_bf16 v[88:91], a[112:115], a[0:3], v[88:91]\n" \
"ds_read_b64_tr_b16 a[70:71], v232 offset:59392\n" \
"v_mul_f32 v254, s31, v254\n" \
"v_mfma_f32_16x16x32_bf16 v[88:91], a[116:119], a[4:7], v[88:91]\n" \
"ds_read_b64_tr_b16 a[72:73], v233 offset:49152\n" \
"v_cvt_pk_bf16_f32 v243, v243, v252\n" \
"v_mfma_f32_16x16x32_bf16 v[92:95], a[120:123], a[0:3], v[92:95]\n" \
"ds_read_b64_tr_b16 a[74:75], v233 offset:51200\n" \
"v_cvt_pk_bf16_f32 v253, v253, v254\n" \
"v_mfma_f32_16x16x32_bf16 v[92:95], a[124:127], a[4:7], v[92:95]\n" \
"buffer_load_dwordx4 v229, s[52:55], s78 offen offset:1024 lds\n" \
"v_mfma_f32_16x16x32_bf16 v[124:127], a[120:123], a[8:11], v[124:127]\n" \
"s_waitcnt vmcnt(35)\n" \
"ds_read_b128 a[0:3], v250 offset:0\n" \
"v_accvgpr_write_b32 a194, v243\n" \
"v_mfma_f32_16x16x32_bf16 v[124:127], a[124:127], a[12:15], v[124:127]\n" \
"ds_read_b128 a[4:7], v251 offset:0\n" \
"v_accvgpr_write_b32 a195, v253\n" \
"v_mfma_f32_16x16x32_bf16 v[120:123], a[112:115], a[8:11], v[120:123]\n" \
"s_xor_b32 s78, s78, 0x180000\n" \
"ds_read_b64_tr_b16 a[76:77], v233 offset:57344\n" \
"v_accvgpr_read_b32 v243, a196\n" \
"v_mfma_f32_16x16x32_bf16 v[120:123], a[116:119], a[12:15], v[120:123]\n" \
"buffer_load_dwordx4 v228, s[52:55], s89 offen offset:2048 lds\n" \
"v_mfma_f32_16x16x32_bf16 v[116:119], a[104:107], a[8:11], v[116:119]\n" \
"ds_read_b64_tr_b16 a[78:79], v233 offset:59392\n" \
"v_accvgpr_read_b32 v252, a197\n" \
"v_mfma_f32_16x16x32_bf16 v[116:119], a[108:111], a[12:15], v[116:119]\n" \
"ds_read_b64_tr_b16 a[80:81], v232 offset:49664\n" \
"v_accvgpr_read_b32 v253, a198\n" \
"v_mfma_f32_16x16x32_bf16 v[112:115], a[96:99], a[8:11], v[112:115]\n" \
"ds_read_b64_tr_b16 a[82:83], v232 offset:51712\n" \
"v_accvgpr_read_b32 v254, a199\n" \
"v_mfma_f32_16x16x32_bf16 v[112:115], a[100:103], a[12:15], v[112:115]\n" \
"buffer_load_dwordx4 v229, s[52:55], s89 offen offset:3072 lds\n" \
"v_mfma_f32_16x16x32_bf16 v[144:147], a[96:99], a[16:19], v[144:147]\n" \
"s_waitcnt vmcnt(35)\n" \
"s_xor_b32 s89, s89, 0x180000\n" \
"ds_read_b128 a[8:11], v250 offset:2048\n" \
"v_mfma_f32_16x16x32_bf16 v[144:147], a[100:103], a[20:23], v[144:147]\n" \
"s_add_u32 m0, s74, 131072\n" \
"ds_read_b128 a[12:15], v251 offset:2048\n" \
"v_mul_f32 v243, s31, v243\n" \
"v_mfma_f32_16x16x32_bf16 v[148:151], a[104:107], a[16:19], v[148:151]\n" \
"s_waitcnt lgkmcnt(6)\n" \
"ds_read_b64_tr_b16 a[84:85], v232 offset:57856\n" \
"v_mul_f32 v252, s31, v252\n" \
"v_mfma_f32_16x16x32_bf16 v[148:151], a[108:111], a[20:23], v[148:151]\n" \
"buffer_load_dwordx4 v224, s[48:51], s75 offen offset:0 lds\n" \
"v_mfma_f32_16x16x32_bf16 v[152:155], a[112:115], a[16:19], v[152:155]\n" \
"ds_read_b64_tr_b16 a[86:87], v232 offset:59904\n" \
"v_mul_f32 v253, s31, v253\n" \
"v_mfma_f32_16x16x32_bf16 v[152:155], a[116:119], a[20:23], v[152:155]\n" \
"ds_read_b64_tr_b16 a[88:89], v233 offset:49664\n" \
"v_mul_f32 v254, s31, v254\n" \
"v_mfma_f32_16x16x32_bf16 v[156:159], a[120:123], a[16:19], v[156:159]\n" \
"ds_read_b64_tr_b16 a[90:91], v233 offset:51712\n" \
"v_cvt_pk_bf16_f32 v243, v243, v252\n" \
"v_mfma_f32_16x16x32_bf16 v[156:159], a[124:127], a[20:23], v[156:159]\n" \
"buffer_load_dwordx4 v225, s[48:51], s75 offen offset:1024 lds\n" \
"v_mfma_f32_16x16x32_bf16 v[188:191], a[120:123], a[24:27], v[188:191]\n" \
"s_waitcnt vmcnt(35)\n" \
"ds_read_b128 a[16:19], v250 offset:4096\n" \
"v_cvt_pk_bf16_f32 v253, v253, v254\n" \
"v_mfma_f32_16x16x32_bf16 v[188:191], a[124:127], a[28:31], v[188:191]\n" \
"ds_read_b128 a[20:23], v251 offset:4096\n" \
"v_accvgpr_write_b32 a196, v243\n" \
"v_mfma_f32_16x16x32_bf16 v[184:187], a[112:115], a[24:27], v[184:187]\n" \
"s_waitcnt lgkmcnt(6)\n" \
"ds_read_b64_tr_b16 a[92:93], v233 offset:57856\n" \
"v_accvgpr_write_b32 a197, v253\n" \
"v_mfma_f32_16x16x32_bf16 v[184:187], a[116:119], a[28:31], v[184:187]\n" \
"buffer_load_dwordx4 v226, s[48:51], s75 offen offset:2048 lds\n" \
"v_mfma_f32_16x16x32_bf16 v[180:183], a[104:107], a[24:27], v[180:183]\n" \
"ds_read_b64_tr_b16 a[94:95], v233 offset:59904\n" \
"v_accvgpr_read_b32 v243, a212\n" \
"v_mfma_f32_16x16x32_bf16 v[180:183], a[108:111], a[28:31], v[180:183]\n" \
"v_accvgpr_read_b32 v252, a213\n" \
"v_accvgpr_read_b32 v253, a214\n" \
"v_mfma_f32_16x16x32_bf16 v[176:179], a[96:99], a[24:27], v[176:179]\n" \
"v_accvgpr_read_b32 v254, a215\n" \
"v_mul_f32 v243, s31, v243\n" \
"v_mfma_f32_16x16x32_bf16 v[176:179], a[100:103], a[28:31], v[176:179]\n" \
"buffer_load_dwordx4 v227, s[48:51], s75 offen offset:3072 lds\n" \
"v_mfma_f32_16x16x32_bf16 v[64:67], a[64:67], a[0:3], v[64:67]\n" \
"s_waitcnt vmcnt(34)\n" \
"s_xor_b32 s75, s75, 0x180\n" \
"ds_read_b128 a[24:27], v250 offset:6144\n" \
"v_mfma_f32_16x16x32_bf16 v[64:67], a[68:71], a[4:7], v[64:67]\n" \
"s_add_u32 m0, s74, 135168\n" \
"ds_read_b128 a[28:31], v251 offset:6144\n" \
"v_mul_f32 v252, s31, v252\n" \
"v_mfma_f32_16x16x32_bf16 v[68:71], a[72:75], a[0:3], v[68:71]\n" \
"s_waitcnt lgkmcnt(4)\n" \
"ds_read_b64_tr_b16 a[96:97], v232 offset:50176\n" \
"v_mul_f32 v253, s31, v253\n" \
"v_mfma_f32_16x16x32_bf16 v[68:71], a[76:79], a[4:7], v[68:71]\n" \
"buffer_load_dwordx4 v224, s[48:51], s77 offen offset:0 lds\n" \
"v_mfma_f32_16x16x32_bf16 v[72:75], a[80:83], a[0:3], v[72:75]\n" \
"ds_read_b64_tr_b16 a[98:99], v232 offset:52224\n" \
"v_mul_f32 v254, s31, v254\n" \
"v_mfma_f32_16x16x32_bf16 v[72:75], a[84:87], a[4:7], v[72:75]\n" \
"s_waitcnt lgkmcnt(3)\n" \
"ds_read_b64_tr_b16 a[100:101], v232 offset:58368\n" \
"v_cvt_pk_bf16_f32 v243, v243, v252\n" \
"v_mfma_f32_16x16x32_bf16 v[76:79], a[88:91], a[0:3], v[76:79]\n" \
"ds_read_b64_tr_b16 a[102:103], v232 offset:60416\n" \
"v_cvt_pk_bf16_f32 v253, v253, v254\n" \
"v_mfma_f32_16x16x32_bf16 v[76:79], a[92:95], a[4:7], v[76:79]\n" \
"buffer_load_dwordx4 v225, s[48:51], s77 offen offset:1024 lds\n" \
"v_mfma_f32_16x16x32_bf16 v[108:111], a[88:91], a[8:11], v[108:111]\n" \
"ds_read_b64_tr_b16 a[104:105], v233 offset:50176\n" \
"v_accvgpr_write_b32 a198, v243\n" \
"v_mfma_f32_16x16x32_bf16 v[108:111], a[92:95], a[12:15], v[108:111]\n" \
"ds_read_b64_tr_b16 a[106:107], v233 offset:52224\n" \
"v_accvgpr_write_b32 a199, v253\n" \
"v_mfma_f32_16x16x32_bf16 v[104:107], a[80:83], a[8:11], v[104:107]\n" \
"s_waitcnt lgkmcnt(6)\n" \
"ds_read_b64_tr_b16 a[108:109], v233 offset:58368\n" \
"v_accvgpr_read_b32 v243, a200\n" \
"v_mfma_f32_16x16x32_bf16 v[104:107], a[84:87], a[12:15], v[104:107]\n" \
"buffer_load_dwordx4 v226, s[48:51], s77 offen offset:2048 lds\n" \
"v_mfma_f32_16x16x32_bf16 v[100:103], a[72:75], a[8:11], v[100:103]\n" \
"ds_read_b64_tr_b16 a[110:111], v233 offset:60416\n" \
"v_accvgpr_read_b32 v252, a201\n" \
"Lpg2766_%=:\n" \
"s_cbranch_execz Lpg5012_%=\n" \
"v_mfma_f32_16x16x32_bf16 v[100:103], a[76:79], a[12:15], v[100:103]\n" \
"ds_read_b64_tr_b16 a[112:113], v232 offset:50688\n" \
"v_accvgpr_read_b32 v253, a202\n" \
"v_mfma_f32_16x16x32_bf16 v[96:99], a[64:67], a[8:11], v[96:99]\n" \
"ds_read_b64_tr_b16 a[114:115], v232 offset:52736\n" \
"v_accvgpr_read_b32 v254, a203\n" \
"v_mfma_f32_16x16x32_bf16 v[96:99], a[68:71], a[12:15], v[96:99]\n" \
"buffer_load_dwordx4 v227, s[48:51], s77 offen offset:3072 lds\n" \
"v_mfma_f32_16x16x32_bf16 v[128:131], a[64:67], a[16:19], v[128:131]\n" \
"ds_read_b64_tr_b16 a[116:117], v232 offset:58880\n" \
"v_mul_f32 v243, s31, v243\n" \
"v_mfma_f32_16x16x32_bf16 v[128:131], a[68:71], a[20:23], v[128:131]\n" \
"ds_read_b64_tr_b16 a[118:119], v232 offset:60928\n" \
"v_mul_f32 v252, s31, v252\n" \
"v_mfma_f32_16x16x32_bf16 v[132:135], a[72:75], a[16:19], v[132:135]\n" \
"s_xor_b32 s77, s77, 0x180\n" \
"ds_read_b64_tr_b16 a[120:121], v233 offset:50688\n" \
"v_mul_f32 v253, s31, v253\n" \
"v_mfma_f32_16x16x32_bf16 v[132:135], a[76:79], a[20:23], v[132:135]\n" \
"v_mul_f32 v254, s31, v254\n" \
"v_cvt_pk_bf16_f32 v243, v243, v252\n" \
"v_mfma_f32_16x16x32_bf16 v[136:139], a[80:83], a[16:19], v[136:139]\n" \
"ds_read_b64_tr_b16 a[122:123], v233 offset:52736\n" \
"v_cvt_pk_bf16_f32 v253, v253, v254\n" \
"v_mfma_f32_16x16x32_bf16 v[136:139], a[84:87], a[20:23], v[136:139]\n" \
"ds_read_b64_tr_b16 a[124:125], v233 offset:58880\n" \
"v_accvgpr_write_b32 a200, v243\n" \
"v_mfma_f32_16x16x32_bf16 v[140:143], a[88:91], a[16:19], v[140:143]\n" \
"ds_read_b64_tr_b16 a[126:127], v233 offset:60928\n" \
"v_accvgpr_write_b32 a201, v253\n" \
"v_mfma_f32_16x16x32_bf16 v[140:143], a[92:95], a[20:23], v[140:143]\n" \
"buffer_store_dwordx4 v[216:219], v236, s[56:59], s93 offen offset:2304 sc1\n" \
"v_mfma_f32_16x16x32_bf16 v[172:175], a[88:91], a[24:27], v[172:175]\n" \
"v_accvgpr_read_b32 v243, a216\n" \
"v_accvgpr_read_b32 v252, a217\n" \
"v_mfma_f32_16x16x32_bf16 v[172:175], a[92:95], a[28:31], v[172:175]\n" \
"v_accvgpr_read_b32 v253, a218\n" \
"v_accvgpr_read_b32 v254, a219\n" \
"v_mfma_f32_16x16x32_bf16 v[168:171], a[80:83], a[24:27], v[168:171]\n" \
"v_mul_f32 v243, s31, v243\n" \
"v_mul_f32 v252, s31, v252\n" \
"v_mfma_f32_16x16x32_bf16 v[168:171], a[84:87], a[28:31], v[168:171]\n" \
"v_mul_f32 v253, s31, v253\n" \
"v_mul_f32 v254, s31, v254\n" \
"v_mfma_f32_16x16x32_bf16 v[164:167], a[72:75], a[24:27], v[164:167]\n" \
"v_cvt_pk_bf16_f32 v243, v243, v252\n" \
"v_cvt_pk_bf16_f32 v253, v253, v254\n" \
"v_mfma_f32_16x16x32_bf16 v[164:167], a[76:79], a[28:31], v[164:167]\n" \
"v_accvgpr_write_b32 a202, v243\n" \
"v_accvgpr_write_b32 a203, v253\n" \
"v_mfma_f32_16x16x32_bf16 v[160:163], a[64:67], a[24:27], v[160:163]\n" \
"v_accvgpr_read_b32 v243, a204\n" \
"v_accvgpr_read_b32 v252, a205\n" \
"v_mfma_f32_16x16x32_bf16 v[160:163], a[68:71], a[28:31], v[160:163]\n" \
"s_waitcnt vmcnt(35) lgkmcnt(0)\n" \
"s_barrier\n" \
"v_mfma_f32_16x16x32_bf16 v[80:83], a[96:99], a[0:3], v[80:83]\n" \
"ds_read_b64_tr_b16 a[64:65], v232 offset:0\n" \
"v_accvgpr_read_b32 v253, a206\n" \
"v_mfma_f32_16x16x32_bf16 v[80:83], a[100:103], a[4:7], v[80:83]\n" \
"ds_read_b64_tr_b16 a[66:67], v232 offset:2048\n" \
"v_accvgpr_read_b32 v254, a207\n" \
"v_mfma_f32_16x16x32_bf16 v[84:87], a[104:107], a[0:3], v[84:87]\n" \
"s_add_u32 m0, s79, 49152\n" \
"ds_read_b64_tr_b16 a[68:69], v232 offset:8192\n" \
"v_mul_f32 v243, s31, v243\n" \
"v_mfma_f32_16x16x32_bf16 v[84:87], a[108:111], a[4:7], v[84:87]\n" \
"buffer_load_dwordx4 v228, s[52:55], s78 offen offset:0 lds\n" \
"v_mfma_f32_16x16x32_bf16 v[88:91], a[112:115], a[0:3], v[88:91]\n" \
"ds_read_b64_tr_b16 a[70:71], v232 offset:10240\n" \
"v_mul_f32 v252, s31, v252\n" \
"v_mfma_f32_16x16x32_bf16 v[88:91], a[116:119], a[4:7], v[88:91]\n" \
"ds_read_b64_tr_b16 a[72:73], v233 offset:0\n" \
"v_mul_f32 v253, s31, v253\n" \
"v_mfma_f32_16x16x32_bf16 v[92:95], a[120:123], a[0:3], v[92:95]\n" \
"ds_read_b64_tr_b16 a[74:75], v233 offset:2048\n" \
"v_mul_f32 v254, s31, v254\n" \
"v_mfma_f32_16x16x32_bf16 v[92:95], a[124:127], a[4:7], v[92:95]\n" \
"buffer_load_dwordx4 v229, s[52:55], s78 offen offset:1024 lds\n" \
"v_mfma_f32_16x16x32_bf16 v[124:127], a[120:123], a[8:11], v[124:127]\n" \
"s_waitcnt vmcnt(35)\n" \
"ds_read_b128 a[0:3], v230 offset:0\n" \
"v_cvt_pk_bf16_f32 v243, v243, v252\n" \
"v_mfma_f32_16x16x32_bf16 v[124:127], a[124:127], a[12:15], v[124:127]\n" \
"ds_read_b128 a[4:7], v231 offset:0\n" \
"v_cvt_pk_bf16_f32 v253, v253, v254\n" \
"v_mfma_f32_16x16x32_bf16 v[120:123], a[112:115], a[8:11], v[120:123]\n" \
"s_xor_b32 s78, s78, 0x80000\n" \
"ds_read_b64_tr_b16 a[76:77], v233 offset:8192\n" \
"v_accvgpr_write_b32 a204, v243\n" \
"v_mfma_f32_16x16x32_bf16 v[120:123], a[116:119], a[12:15], v[120:123]\n" \
"buffer_load_dwordx4 v228, s[52:55], s89 offen offset:2048 lds\n" \
"v_mfma_f32_16x16x32_bf16 v[116:119], a[104:107], a[8:11], v[116:119]\n" \
"ds_read_b64_tr_b16 a[78:79], v233 offset:10240\n" \
"v_accvgpr_write_b32 a205, v253\n" \
"v_mfma_f32_16x16x32_bf16 v[116:119], a[108:111], a[12:15], v[116:119]\n" \
"ds_read_b64_tr_b16 a[80:81], v232 offset:512\n" \
"v_accvgpr_read_b32 v243, a220\n" \
"v_mfma_f32_16x16x32_bf16 v[112:115], a[96:99], a[8:11], v[112:115]\n" \
"ds_read_b64_tr_b16 a[82:83], v232 offset:2560\n" \
"v_accvgpr_read_b32 v252, a221\n" \
"v_mfma_f32_16x16x32_bf16 v[112:115], a[100:103], a[12:15], v[112:115]\n" \
"buffer_load_dwordx4 v229, s[52:55], s89 offen offset:3072 lds\n" \
"v_mfma_f32_16x16x32_bf16 v[144:147], a[96:99], a[16:19], v[144:147]\n" \
"s_waitcnt vmcnt(35)\n" \
"s_xor_b32 s89, s89, 0x80000\n" \
"ds_read_b128 a[8:11], v230 offset:2048\n" \
"v_mfma_f32_16x16x32_bf16 v[144:147], a[100:103], a[20:23], v[144:147]\n" \
"s_add_u32 m0, s74, 65536\n" \
"ds_read_b128 a[12:15], v231 offset:2048\n" \
"v_accvgpr_read_b32 v253, a222\n" \
"v_mfma_f32_16x16x32_bf16 v[148:151], a[104:107], a[16:19], v[148:151]\n" \
"s_waitcnt lgkmcnt(6)\n" \
"ds_read_b64_tr_b16 a[84:85], v232 offset:8704\n" \
"v_accvgpr_read_b32 v254, a223\n" \
"v_mfma_f32_16x16x32_bf16 v[148:151], a[108:111], a[20:23], v[148:151]\n" \
"buffer_load_dwordx4 v224, s[48:51], s75 offen offset:0 lds\n" \
"v_mfma_f32_16x16x32_bf16 v[152:155], a[112:115], a[16:19], v[152:155]\n" \
"ds_read_b64_tr_b16 a[86:87], v232 offset:10752\n" \
"v_mul_f32 v243, s31, v243\n" \
"v_mfma_f32_16x16x32_bf16 v[152:155], a[116:119], a[20:23], v[152:155]\n" \
"ds_read_b64_tr_b16 a[88:89], v233 offset:512\n" \
"v_mul_f32 v252, s31, v252\n" \
"v_mfma_f32_16x16x32_bf16 v[156:159], a[120:123], a[16:19], v[156:159]\n" \
"ds_read_b64_tr_b16 a[90:91], v233 offset:2560\n" \
"v_mul_f32 v253, s31, v253\n" \
"v_mfma_f32_16x16x32_bf16 v[156:159], a[124:127], a[20:23], v[156:159]\n" \
"buffer_load_dwordx4 v225, s[48:51], s75 offen offset:1024 lds\n" \
"v_mfma_f32_16x16x32_bf16 v[188:191], a[120:123], a[24:27], v[188:191]\n" \
"s_waitcnt vmcnt(35)\n" \
"ds_read_b128 a[16:19], v230 offset:4096\n" \
"v_mul_f32 v254, s31, v254\n" \
"v_mfma_f32_16x16x32_bf16 v[188:191], a[124:127], a[28:31], v[188:191]\n" \
"ds_read_b128 a[20:23], v231 offset:4096\n" \
"v_cvt_pk_bf16_f32 v243, v243, v252\n" \
"v_mfma_f32_16x16x32_bf16 v[184:187], a[112:115], a[24:27], v[184:187]\n" \
"s_waitcnt lgkmcnt(6)\n" \
"ds_read_b64_tr_b16 a[92:93], v233 offset:8704\n" \
"v_cvt_pk_bf16_f32 v253, v253, v254\n" \
"v_mfma_f32_16x16x32_bf16 v[184:187], a[116:119], a[28:31], v[184:187]\n" \
"buffer_load_dwordx4 v226, s[48:51], s75 offen offset:2048 lds\n" \
"v_mfma_f32_16x16x32_bf16 v[180:183], a[104:107], a[24:27], v[180:183]\n" \
"ds_read_b64_tr_b16 a[94:95], v233 offset:10752\n" \
"v_accvgpr_write_b32 a206, v243\n" \
"v_mfma_f32_16x16x32_bf16 v[180:183], a[108:111], a[28:31], v[180:183]\n" \
"v_accvgpr_write_b32 a207, v253\n" \
"v_mul_f32 v32, s76, v32\n" \
"v_mfma_f32_16x16x32_bf16 v[176:179], a[96:99], a[24:27], v[176:179]\n" \
"v_mul_f32 v33, s76, v33\n" \
"v_mul_f32 v34, s76, v34\n" \
"v_mfma_f32_16x16x32_bf16 v[176:179], a[100:103], a[28:31], v[176:179]\n" \
"buffer_load_dwordx4 v227, s[48:51], s75 offen offset:3072 lds\n" \
"v_mfma_f32_16x16x32_bf16 v[64:67], a[64:67], a[0:3], v[64:67]\n" \
"s_waitcnt vmcnt(35)\n" \
"s_xor_b32 s75, s75, 0x80\n" \
"ds_read_b128 a[24:27], v230 offset:6144\n" \
"v_mfma_f32_16x16x32_bf16 v[64:67], a[68:71], a[4:7], v[64:67]\n" \
"s_add_u32 m0, s74, 69632\n" \
"ds_read_b128 a[28:31], v231 offset:6144\n" \
"v_mul_f32 v35, s76, v35\n" \
"v_mfma_f32_16x16x32_bf16 v[68:71], a[72:75], a[0:3], v[68:71]\n" \
"s_waitcnt lgkmcnt(4)\n" \
"ds_read_b64_tr_b16 a[96:97], v232 offset:1024\n" \
"v_cvt_pk_bf16_f32 v32, v32, v33\n" \
"v_mfma_f32_16x16x32_bf16 v[68:71], a[76:79], a[4:7], v[68:71]\n" \
"buffer_load_dwordx4 v224, s[48:51], s77 offen offset:0 lds\n" \
"v_mfma_f32_16x16x32_bf16 v[72:75], a[80:83], a[0:3], v[72:75]\n" \
"ds_read_b64_tr_b16 a[98:99], v232 offset:3072\n" \
"v_cvt_pk_bf16_f32 v33, v34, v35\n" \
"v_mfma_f32_16x16x32_bf16 v[72:75], a[84:87], a[4:7], v[72:75]\n" \
"s_waitcnt lgkmcnt(3)\n" \
"ds_read_b64_tr_b16 a[100:101], v232 offset:9216\n" \
"v_mul_f32 v36, s76, v36\n" \
"v_mfma_f32_16x16x32_bf16 v[76:79], a[88:91], a[0:3], v[76:79]\n" \
"ds_read_b64_tr_b16 a[102:103], v232 offset:11264\n" \
"v_mul_f32 v37, s76, v37\n" \
"v_mfma_f32_16x16x32_bf16 v[76:79], a[92:95], a[4:7], v[76:79]\n" \
"buffer_load_dwordx4 v225, s[48:51], s77 offen offset:1024 lds\n" \
"v_mfma_f32_16x16x32_bf16 v[108:111], a[88:91], a[8:11], v[108:111]\n" \
"ds_read_b64_tr_b16 a[104:105], v233 offset:1024\n" \
"v_mul_f32 v38, s76, v38\n" \
"v_mfma_f32_16x16x32_bf16 v[108:111], a[92:95], a[12:15], v[108:111]\n" \
"ds_read_b64_tr_b16 a[106:107], v233 offset:3072\n" \
"v_mul_f32 v39, s76, v39\n" \
"v_mfma_f32_16x16x32_bf16 v[104:107], a[80:83], a[8:11], v[104:107]\n" \
"s_waitcnt lgkmcnt(6)\n" \
"ds_read_b64_tr_b16 a[108:109], v233 offset:9216\n" \
"v_cvt_pk_bf16_f32 v34, v36, v37\n" \
"v_mfma_f32_16x16x32_bf16 v[104:107], a[84:87], a[12:15], v[104:107]\n" \
"buffer_load_dwordx4 v226, s[48:51], s77 offen offset:2048 lds\n" \
"v_mfma_f32_16x16x32_bf16 v[100:103], a[72:75], a[8:11], v[100:103]\n" \
"ds_read_b64_tr_b16 a[110:111], v233 offset:11264\n" \
"v_cvt_pk_bf16_f32 v35, v38, v39\n" \
"v_mfma_f32_16x16x32_bf16 v[100:103], a[76:79], a[12:15], v[100:103]\n" \
"ds_read_b64_tr_b16 a[112:113], v232 offset:1536\n" \
"v_mul_f32 v48, s76, v48\n" \
"v_mfma_f32_16x16x32_bf16 v[96:99], a[64:67], a[8:11], v[96:99]\n" \
"ds_read_b64_tr_b16 a[114:115], v232 offset:3584\n" \
"v_mul_f32 v49, s76, v49\n" \
"v_mfma_f32_16x16x32_bf16 v[96:99], a[68:71], a[12:15], v[96:99]\n" \
"buffer_load_dwordx4 v227, s[48:51], s77 offen offset:3072 lds\n" \
"v_mfma_f32_16x16x32_bf16 v[128:131], a[64:67], a[16:19], v[128:131]\n" \
"ds_read_b64_tr_b16 a[116:117], v232 offset:9728\n" \
"v_mul_f32 v50, s76, v50\n" \
"v_mfma_f32_16x16x32_bf16 v[128:131], a[68:71], a[20:23], v[128:131]\n" \
"ds_read_b64_tr_b16 a[118:119], v232 offset:11776\n" \
"v_mul_f32 v51, s76, v51\n" \
"v_mfma_f32_16x16x32_bf16 v[132:135], a[72:75], a[16:19], v[132:135]\n" \
"s_xor_b32 s77, s77, 0x80\n" \
"ds_read_b64_tr_b16 a[120:121], v233 offset:1536\n" \
"v_cvt_pk_bf16_f32 v48, v48, v49\n" \
"v_mfma_f32_16x16x32_bf16 v[132:135], a[76:79], a[20:23], v[132:135]\n" \
"v_cvt_pk_bf16_f32 v49, v50, v51\n" \
"v_mul_f32 v52, s76, v52\n" \
"v_mfma_f32_16x16x32_bf16 v[136:139], a[80:83], a[16:19], v[136:139]\n" \
"ds_read_b64_tr_b16 a[122:123], v233 offset:3584\n" \
"v_mul_f32 v53, s76, v53\n" \
"v_mfma_f32_16x16x32_bf16 v[136:139], a[84:87], a[20:23], v[136:139]\n" \
"ds_read_b64_tr_b16 a[124:125], v233 offset:9728\n" \
"v_mul_f32 v54, s76, v54\n" \
"v_mfma_f32_16x16x32_bf16 v[140:143], a[88:91], a[16:19], v[140:143]\n" \
"s_add_u32 s93, s87, 0\n" \
"ds_read_b64_tr_b16 a[126:127], v233 offset:11776\n" \
"v_mul_f32 v55, s76, v55\n" \
"v_mfma_f32_16x16x32_bf16 v[140:143], a[92:95], a[20:23], v[140:143]\n" \
"buffer_store_dwordx4 v[196:199], v236, s[56:59], s93 offen offset:0 sc1\n" \
"v_mfma_f32_16x16x32_bf16 v[172:175], a[88:91], a[24:27], v[172:175]\n" \
"v_cvt_pk_bf16_f32 v50, v52, v53\n" \
"v_cvt_pk_bf16_f32 v51, v54, v55\n" \
"v_mfma_f32_16x16x32_bf16 v[172:175], a[92:95], a[28:31], v[172:175]\n" \
"v_mul_f32 v40, s76, v40\n" \
"v_mul_f32 v41, s76, v41\n" \
"v_mfma_f32_16x16x32_bf16 v[168:171], a[80:83], a[24:27], v[168:171]\n" \
"v_mul_f32 v42, s76, v42\n" \
"v_mul_f32 v43, s76, v43\n" \
"v_mfma_f32_16x16x32_bf16 v[168:171], a[84:87], a[28:31], v[168:171]\n" \
"v_cvt_pk_bf16_f32 v40, v40, v41\n" \
"v_cvt_pk_bf16_f32 v41, v42, v43\n" \
"v_mfma_f32_16x16x32_bf16 v[164:167], a[72:75], a[24:27], v[164:167]\n" \
"v_mul_f32 v44, s76, v44\n" \
"v_mul_f32 v45, s76, v45\n" \
"v_mfma_f32_16x16x32_bf16 v[164:167], a[76:79], a[28:31], v[164:167]\n" \
"v_mul_f32 v46, s76, v46\n" \
"v_mul_f32 v47, s76, v47\n" \
"v_mfma_f32_16x16x32_bf16 v[160:163], a[64:67], a[24:27], v[160:163]\n" \
"v_cvt_pk_bf16_f32 v42, v44, v45\n" \
"v_cvt_pk_bf16_f32 v43, v46, v47\n" \
"v_mfma_f32_16x16x32_bf16 v[160:163], a[68:71], a[28:31], v[160:163]\n" \
"s_waitcnt vmcnt(35) lgkmcnt(0)\n" \
"s_barrier\n" \
"v_mfma_f32_16x16x32_bf16 v[80:83], a[96:99], a[0:3], v[80:83]\n" \
"ds_read_b64_tr_b16 a[64:65], v232 offset:16384\n" \
"v_mul_f32 v56, s76, v56\n" \
"v_mfma_f32_16x16x32_bf16 v[80:83], a[100:103], a[4:7], v[80:83]\n" \
"ds_read_b64_tr_b16 a[66:67], v232 offset:18432\n" \
"v_mul_f32 v57, s76, v57\n" \
"v_mfma_f32_16x16x32_bf16 v[84:87], a[104:107], a[0:3], v[84:87]\n" \
"s_add_u32 m0, s79, 0\n" \
"ds_read_b64_tr_b16 a[68:69], v232 offset:24576\n" \
"v_mul_f32 v58, s76, v58\n" \
"v_mfma_f32_16x16x32_bf16 v[84:87], a[108:111], a[4:7], v[84:87]\n" \
"buffer_load_dwordx4 v228, s[52:55], s78 offen offset:0 lds\n" \
"v_mfma_f32_16x16x32_bf16 v[88:91], a[112:115], a[0:3], v[88:91]\n" \
"ds_read_b64_tr_b16 a[70:71], v232 offset:26624\n" \
"v_mul_f32 v59, s76, v59\n" \
"v_mfma_f32_16x16x32_bf16 v[88:91], a[116:119], a[4:7], v[88:91]\n" \
"ds_read_b64_tr_b16 a[72:73], v233 offset:16384\n" \
"v_cvt_pk_bf16_f32 v56, v56, v57\n" \
"v_mfma_f32_16x16x32_bf16 v[92:95], a[120:123], a[0:3], v[92:95]\n" \
"ds_read_b64_tr_b16 a[74:75], v233 offset:18432\n" \
"v_cvt_pk_bf16_f32 v57, v58, v59\n" \
"v_mfma_f32_16x16x32_bf16 v[92:95], a[124:127], a[4:7], v[92:95]\n" \
"buffer_load_dwordx4 v229, s[52:55], s78 offen offset:1024 lds\n" \
"v_mfma_f32_16x16x32_bf16 v[124:127], a[120:123], a[8:11], v[124:127]\n" \
"s_waitcnt vmcnt(35)\n" \
"ds_read_b128 a[0:3], v230 offset:32768\n" \
"v_mul_f32 v60, s76, v60\n" \
"v_mfma_f32_16x16x32_bf16 v[124:127], a[124:127], a[12:15], v[124:127]\n" \
"ds_read_b128 a[4:7], v231 offset:32768\n" \
"v_mul_f32 v61, s76, v61\n" \
"v_mfma_f32_16x16x32_bf16 v[120:123], a[112:115], a[8:11], v[120:123]\n" \
"s_xor_b32 s78, s78, 0x780000\n" \
"ds_read_b64_tr_b16 a[76:77], v233 offset:24576\n" \
"v_mul_f32 v62, s76, v62\n" \
"v_mfma_f32_16x16x32_bf16 v[120:123], a[116:119], a[12:15], v[120:123]\n" \
"buffer_load_dwordx4 v228, s[52:55], s89 offen offset:2048 lds\n" \
"v_mfma_f32_16x16x32_bf16 v[116:119], a[104:107], a[8:11], v[116:119]\n" \
"ds_read_b64_tr_b16 a[78:79], v233 offset:26624\n" \
"v_mul_f32 v63, s76, v63\n" \
"v_mfma_f32_16x16x32_bf16 v[116:119], a[108:111], a[12:15], v[116:119]\n" \
"ds_read_b64_tr_b16 a[80:81], v232 offset:16896\n" \
"v_cvt_pk_bf16_f32 v58, v60, v61\n" \
"v_mfma_f32_16x16x32_bf16 v[112:115], a[96:99], a[8:11], v[112:115]\n" \
"ds_read_b64_tr_b16 a[82:83], v232 offset:18944\n" \
"v_cvt_pk_bf16_f32 v59, v62, v63\n" \
"v_mfma_f32_16x16x32_bf16 v[112:115], a[100:103], a[12:15], v[112:115]\n" \
"buffer_load_dwordx4 v229, s[52:55], s89 offen offset:3072 lds\n" \
"v_mfma_f32_16x16x32_bf16 v[144:147], a[96:99], a[16:19], v[144:147]\n" \
"s_waitcnt vmcnt(35)\n" \
"s_xor_b32 s89, s89, 0x780000\n" \
"ds_read_b128 a[8:11], v230 offset:34816\n" \
"v_mfma_f32_16x16x32_bf16 v[144:147], a[100:103], a[20:23], v[144:147]\n" \
"s_add_u32 m0, s74, 98304\n" \
"ds_read_b128 a[12:15], v231 offset:34816\n" \
"v_mul_f32 v0, s76, v0\n" \
"v_mfma_f32_16x16x32_bf16 v[148:151], a[104:107], a[16:19], v[148:151]\n" \
"s_waitcnt lgkmcnt(6)\n" \
"ds_read_b64_tr_b16 a[84:85], v232 offset:25088\n" \
"v_mul_f32 v1, s76, v1\n" \
"v_mfma_f32_16x16x32_bf16 v[148:151], a[108:111], a[20:23], v[148:151]\n" \
"buffer_load_dwordx4 v224, s[48:51], s75 offen offset:0 lds\n" \
"v_mfma_f32_16x16x32_bf16 v[152:155], a[112:115], a[16:19], v[152:155]\n" \
"ds_read_b64_tr_b16 a[86:87], v232 offset:27136\n" \
"v_mul_f32 v2, s76, v2\n" \
"v_mfma_f32_16x16x32_bf16 v[152:155], a[116:119], a[20:23], v[152:155]\n" \
"ds_read_b64_tr_b16 a[88:89], v233 offset:16896\n" \
"v_mul_f32 v3, s76, v3\n" \
"v_mfma_f32_16x16x32_bf16 v[156:159], a[120:123], a[16:19], v[156:159]\n" \
"ds_read_b64_tr_b16 a[90:91], v233 offset:18944\n" \
"v_cvt_pk_bf16_f32 v36, v0, v1\n" \
"v_mfma_f32_16x16x32_bf16 v[156:159], a[124:127], a[20:23], v[156:159]\n" \
"buffer_load_dwordx4 v225, s[48:51], s75 offen offset:1024 lds\n" \
"v_mfma_f32_16x16x32_bf16 v[188:191], a[120:123], a[24:27], v[188:191]\n" \
"s_waitcnt vmcnt(35)\n" \
"ds_read_b128 a[16:19], v230 offset:36864\n" \
"v_cvt_pk_bf16_f32 v37, v2, v3\n" \
"v_mfma_f32_16x16x32_bf16 v[188:191], a[124:127], a[28:31], v[188:191]\n" \
"ds_read_b128 a[20:23], v231 offset:36864\n" \
"v_mul_f32 v4, s76, v4\n" \
"v_mfma_f32_16x16x32_bf16 v[184:187], a[112:115], a[24:27], v[184:187]\n" \
"s_waitcnt lgkmcnt(6)\n" \
"ds_read_b64_tr_b16 a[92:93], v233 offset:25088\n" \
"v_mul_f32 v5, s76, v5\n" \
"v_mfma_f32_16x16x32_bf16 v[184:187], a[116:119], a[28:31], v[184:187]\n" \
"buffer_load_dwordx4 v226, s[48:51], s75 offen offset:2048 lds\n" \
"v_mfma_f32_16x16x32_bf16 v[180:183], a[104:107], a[24:27], v[180:183]\n" \
"ds_read_b64_tr_b16 a[94:95], v233 offset:27136\n" \
"v_mul_f32 v6, s76, v6\n" \
"v_mfma_f32_16x16x32_bf16 v[180:183], a[108:111], a[28:31], v[180:183]\n" \
"v_mul_f32 v7, s76, v7\n" \
"v_cvt_pk_bf16_f32 v38, v4, v5\n" \
"v_mfma_f32_16x16x32_bf16 v[176:179], a[96:99], a[24:27], v[176:179]\n" \
"v_cvt_pk_bf16_f32 v39, v6, v7\n" \
"v_mul_f32 v16, s76, v16\n" \
"v_mfma_f32_16x16x32_bf16 v[176:179], a[100:103], a[28:31], v[176:179]\n" \
"buffer_load_dwordx4 v227, s[48:51], s75 offen offset:3072 lds\n" \
"v_mfma_f32_16x16x32_bf16 v[64:67], a[64:67], a[0:3], v[64:67]\n" \
"s_waitcnt vmcnt(35)\n" \
"s_xor_b32 s75, s75, 0x780\n" \
"ds_read_b128 a[24:27], v230 offset:38912\n" \
"v_mfma_f32_16x16x32_bf16 v[64:67], a[68:71], a[4:7], v[64:67]\n" \
"s_add_u32 m0, s74, 102400\n" \
"ds_read_b128 a[28:31], v231 offset:38912\n" \
"v_mul_f32 v17, s76, v17\n" \
"v_mfma_f32_16x16x32_bf16 v[68:71], a[72:75], a[0:3], v[68:71]\n" \
"s_waitcnt lgkmcnt(4)\n" \
"ds_read_b64_tr_b16 a[96:97], v232 offset:17408\n" \
"v_mul_f32 v18, s76, v18\n" \
"v_mfma_f32_16x16x32_bf16 v[68:71], a[76:79], a[4:7], v[68:71]\n" \
"buffer_load_dwordx4 v224, s[48:51], s77 offen offset:0 lds\n" \
"v_mfma_f32_16x16x32_bf16 v[72:75], a[80:83], a[0:3], v[72:75]\n" \
"ds_read_b64_tr_b16 a[98:99], v232 offset:19456\n" \
"v_mul_f32 v19, s76, v19\n" \
"v_mfma_f32_16x16x32_bf16 v[72:75], a[84:87], a[4:7], v[72:75]\n" \
"s_waitcnt lgkmcnt(3)\n" \
"ds_read_b64_tr_b16 a[100:101], v232 offset:25600\n" \
"v_cvt_pk_bf16_f32 v16, v16, v17\n" \
"v_mfma_f32_16x16x32_bf16 v[76:79], a[88:91], a[0:3], v[76:79]\n" \
"ds_read_b64_tr_b16 a[102:103], v232 offset:27648\n" \
"v_cvt_pk_bf16_f32 v17, v18, v19\n" \
"v_mfma_f32_16x16x32_bf16 v[76:79], a[92:95], a[4:7], v[76:79]\n" \
"buffer_load_dwordx4 v225, s[48:51], s77 offen offset:1024 lds\n" \
"v_mfma_f32_16x16x32_bf16 v[108:111], a[88:91], a[8:11], v[108:111]\n" \
"ds_read_b64_tr_b16 a[104:105], v233 offset:17408\n" \
"v_mul_f32 v20, s76, v20\n" \
"v_mfma_f32_16x16x32_bf16 v[108:111], a[92:95], a[12:15], v[108:111]\n" \
"ds_read_b64_tr_b16 a[106:107], v233 offset:19456\n" \
"v_mul_f32 v21, s76, v21\n" \
"v_mfma_f32_16x16x32_bf16 v[104:107], a[80:83], a[8:11], v[104:107]\n" \
"s_waitcnt lgkmcnt(6)\n" \
"ds_read_b64_tr_b16 a[108:109], v233 offset:25600\n" \
"v_mul_f32 v22, s76, v22\n" \
"v_mfma_f32_16x16x32_bf16 v[104:107], a[84:87], a[12:15], v[104:107]\n" \
"buffer_load_dwordx4 v226, s[48:51], s77 offen offset:2048 lds\n" \
"v_mfma_f32_16x16x32_bf16 v[100:103], a[72:75], a[8:11], v[100:103]\n" \
"ds_read_b64_tr_b16 a[110:111], v233 offset:27648\n" \
"v_mul_f32 v23, s76, v23\n" \
"v_mfma_f32_16x16x32_bf16 v[100:103], a[76:79], a[12:15], v[100:103]\n" \
"ds_read_b64_tr_b16 a[112:113], v232 offset:17920\n" \
"v_cvt_pk_bf16_f32 v18, v20, v21\n" \
"v_mfma_f32_16x16x32_bf16 v[96:99], a[64:67], a[8:11], v[96:99]\n" \
"ds_read_b64_tr_b16 a[114:115], v232 offset:19968\n" \
"v_cvt_pk_bf16_f32 v19, v22, v23\n" \
"v_mfma_f32_16x16x32_bf16 v[96:99], a[68:71], a[12:15], v[96:99]\n" \
"buffer_load_dwordx4 v227, s[48:51], s77 offen offset:3072 lds\n" \
"v_mfma_f32_16x16x32_bf16 v[128:131], a[64:67], a[16:19], v[128:131]\n" \
"ds_read_b64_tr_b16 a[116:117], v232 offset:26112\n" \
"v_mul_f32 v8, s76, v8\n" \
"v_mfma_f32_16x16x32_bf16 v[128:131], a[68:71], a[20:23], v[128:131]\n" \
"ds_read_b64_tr_b16 a[118:119], v232 offset:28160\n" \
"v_mul_f32 v9, s76, v9\n" \
"v_mfma_f32_16x16x32_bf16 v[132:135], a[72:75], a[16:19], v[132:135]\n" \
"s_xor_b32 s77, s77, 0x780\n" \
"ds_read_b64_tr_b16 a[120:121], v233 offset:17920\n" \
"v_mul_f32 v10, s76, v10\n" \
"v_mfma_f32_16x16x32_bf16 v[132:135], a[76:79], a[20:23], v[132:135]\n" \
"v_mul_f32 v11, s76, v11\n" \
"v_cvt_pk_bf16_f32 v52, v8, v9\n" \
"v_mfma_f32_16x16x32_bf16 v[136:139], a[80:83], a[16:19], v[136:139]\n" \
"ds_read_b64_tr_b16 a[122:123], v233 offset:19968\n" \
"v_cvt_pk_bf16_f32 v53, v10, v11\n" \
"v_mfma_f32_16x16x32_bf16 v[136:139], a[84:87], a[20:23], v[136:139]\n" \
"ds_read_b64_tr_b16 a[124:125], v233 offset:26112\n" \
"v_mul_f32 v12, s76, v12\n" \
"v_mfma_f32_16x16x32_bf16 v[140:143], a[88:91], a[16:19], v[140:143]\n" \
"ds_read_b64_tr_b16 a[126:127], v233 offset:28160\n" \
"v_mul_f32 v13, s76, v13\n" \
"v_mfma_f32_16x16x32_bf16 v[140:143], a[92:95], a[20:23], v[140:143]\n" \
"buffer_store_dwordx4 v[212:215], v236, s[56:59], s93 offen offset:256 sc1\n" \
"v_mfma_f32_16x16x32_bf16 v[172:175], a[88:91], a[24:27], v[172:175]\n" \
"v_mul_f32 v14, s76, v14\n" \
"v_mul_f32 v15, s76, v15\n" \
"v_mfma_f32_16x16x32_bf16 v[172:175], a[92:95], a[28:31], v[172:175]\n" \
"v_cvt_pk_bf16_f32 v54, v12, v13\n" \
"v_cvt_pk_bf16_f32 v55, v14, v15\n" \
"v_mfma_f32_16x16x32_bf16 v[168:171], a[80:83], a[24:27], v[168:171]\n" \
"v_mul_f32 v24, s76, v24\n" \
"v_mul_f32 v25, s76, v25\n" \
"v_mfma_f32_16x16x32_bf16 v[168:171], a[84:87], a[28:31], v[168:171]\n" \
"v_mul_f32 v26, s76, v26\n" \
"v_mul_f32 v27, s76, v27\n" \
"v_mfma_f32_16x16x32_bf16 v[164:167], a[72:75], a[24:27], v[164:167]\n" \
"v_cvt_pk_bf16_f32 v24, v24, v25\n" \
"v_cvt_pk_bf16_f32 v25, v26, v27\n" \
"v_mfma_f32_16x16x32_bf16 v[164:167], a[76:79], a[28:31], v[164:167]\n" \
"v_mul_f32 v28, s76, v28\n" \
"v_mul_f32 v29, s76, v29\n" \
"v_mfma_f32_16x16x32_bf16 v[160:163], a[64:67], a[24:27], v[160:163]\n" \
"v_mul_f32 v30, s76, v30\n" \
"v_mul_f32 v31, s76, v31\n" \
"v_mfma_f32_16x16x32_bf16 v[160:163], a[68:71], a[28:31], v[160:163]\n" \
"s_waitcnt vmcnt(35) lgkmcnt(0)\n" \
"s_barrier\n" \
"v_mfma_f32_16x16x32_bf16 v[80:83], a[96:99], a[0:3], v[80:83]\n" \
"ds_read_b64_tr_b16 a[64:65], v232 offset:32768\n" \
"v_cvt_pk_bf16_f32 v26, v28, v29\n" \
"v_mfma_f32_16x16x32_bf16 v[80:83], a[100:103], a[4:7], v[80:83]\n" \
"ds_read_b64_tr_b16 a[66:67], v232 offset:34816\n" \
"v_cvt_pk_bf16_f32 v27, v30, v31\n" \
"v_mfma_f32_16x16x32_bf16 v[84:87], a[104:107], a[0:3], v[84:87]\n" \
"s_add_u32 m0, s79, 16384\n" \
"ds_read_b64_tr_b16 a[68:69], v232 offset:40960\n" \
"v_accvgpr_read_b32 v243, a160\n" \
"v_mfma_f32_16x16x32_bf16 v[84:87], a[108:111], a[4:7], v[84:87]\n" \
"buffer_load_dwordx4 v228, s[52:55], s78 offen offset:0 lds\n" \
"v_mfma_f32_16x16x32_bf16 v[88:91], a[112:115], a[0:3], v[88:91]\n" \
"ds_read_b64_tr_b16 a[70:71], v232 offset:43008\n" \
"v_accvgpr_read_b32 v252, a161\n" \
"v_mfma_f32_16x16x32_bf16 v[88:91], a[116:119], a[4:7], v[88:91]\n" \
"ds_read_b64_tr_b16 a[72:73], v233 offset:32768\n" \
"v_accvgpr_read_b32 v253, a162\n" \
"v_mfma_f32_16x16x32_bf16 v[92:95], a[120:123], a[0:3], v[92:95]\n" \
"ds_read_b64_tr_b16 a[74:75], v233 offset:34816\n" \
"v_accvgpr_read_b32 v254, a163\n" \
"v_mfma_f32_16x16x32_bf16 v[92:95], a[124:127], a[4:7], v[92:95]\n" \
"buffer_load_dwordx4 v229, s[52:55], s78 offen offset:1024 lds\n" \
"v_mfma_f32_16x16x32_bf16 v[124:127], a[120:123], a[8:11], v[124:127]\n" \
"s_waitcnt vmcnt(35)\n" \
"ds_read_b128 a[0:3], v250 offset:0\n" \
"v_mul_f32 v243, s31, v243\n" \
"v_mfma_f32_16x16x32_bf16 v[124:127], a[124:127], a[12:15], v[124:127]\n" \
"ds_read_b128 a[4:7], v251 offset:0\n" \
"v_mul_f32 v252, s31, v252\n" \
"v_mfma_f32_16x16x32_bf16 v[120:123], a[112:115], a[8:11], v[120:123]\n" \
"s_xor_b32 s78, s78, 0x80000\n" \
"ds_read_b64_tr_b16 a[76:77], v233 offset:40960\n" \
"v_mul_f32 v253, s31, v253\n" \
"v_mfma_f32_16x16x32_bf16 v[120:123], a[116:119], a[12:15], v[120:123]\n" \
"buffer_load_dwordx4 v228, s[52:55], s89 offen offset:2048 lds\n" \
"v_mfma_f32_16x16x32_bf16 v[116:119], a[104:107], a[8:11], v[116:119]\n" \
"ds_read_b64_tr_b16 a[78:79], v233 offset:43008\n" \
"v_mul_f32 v254, s31, v254\n" \
"v_mfma_f32_16x16x32_bf16 v[116:119], a[108:111], a[12:15], v[116:119]\n" \
"ds_read_b64_tr_b16 a[80:81], v232 offset:33280\n" \
"v_cvt_pk_bf16_f32 v44, v243, v252\n" \
"v_mfma_f32_16x16x32_bf16 v[112:115], a[96:99], a[8:11], v[112:115]\n" \
"ds_read_b64_tr_b16 a[82:83], v232 offset:35328\n" \
"v_cvt_pk_bf16_f32 v45, v253, v254\n" \
"v_mfma_f32_16x16x32_bf16 v[112:115], a[100:103], a[12:15], v[112:115]\n" \
"buffer_load_dwordx4 v229, s[52:55], s89 offen offset:3072 lds\n" \
"v_mfma_f32_16x16x32_bf16 v[144:147], a[96:99], a[16:19], v[144:147]\n" \
"s_waitcnt vmcnt(35)\n" \
"s_xor_b32 s89, s89, 0x80000\n" \
"ds_read_b128 a[8:11], v250 offset:2048\n" \
"v_mfma_f32_16x16x32_bf16 v[144:147], a[100:103], a[20:23], v[144:147]\n" \
"s_add_u32 m0, s74, 131072\n" \
"ds_read_b128 a[12:15], v251 offset:2048\n" \
"v_accvgpr_read_b32 v243, a176\n" \
"v_mfma_f32_16x16x32_bf16 v[148:151], a[104:107], a[16:19], v[148:151]\n" \
"s_waitcnt lgkmcnt(6)\n" \
"ds_read_b64_tr_b16 a[84:85], v232 offset:41472\n" \
"v_accvgpr_read_b32 v252, a177\n" \
"v_mfma_f32_16x16x32_bf16 v[148:151], a[108:111], a[20:23], v[148:151]\n" \
"buffer_load_dwordx4 v224, s[48:51], s75 offen offset:0 lds\n" \
"v_mfma_f32_16x16x32_bf16 v[152:155], a[112:115], a[16:19], v[152:155]\n" \
"ds_read_b64_tr_b16 a[86:87], v232 offset:43520\n" \
"v_accvgpr_read_b32 v253, a178\n" \
"v_mfma_f32_16x16x32_bf16 v[152:155], a[116:119], a[20:23], v[152:155]\n" \
"ds_read_b64_tr_b16 a[88:89], v233 offset:33280\n" \
"v_accvgpr_read_b32 v254, a179\n" \
"v_mfma_f32_16x16x32_bf16 v[156:159], a[120:123], a[16:19], v[156:159]\n" \
"ds_read_b64_tr_b16 a[90:91], v233 offset:35328\n" \
"v_mul_f32 v243, s31, v243\n" \
"v_mfma_f32_16x16x32_bf16 v[156:159], a[124:127], a[20:23], v[156:159]\n" \
"buffer_load_dwordx4 v225, s[48:51], s75 offen offset:1024 lds\n" \
"v_mfma_f32_16x16x32_bf16 v[188:191], a[120:123], a[24:27], v[188:191]\n" \
"s_waitcnt vmcnt(35)\n" \
"ds_read_b128 a[16:19], v250 offset:4096\n" \
"v_mul_f32 v252, s31, v252\n" \
"v_mfma_f32_16x16x32_bf16 v[188:191], a[124:127], a[28:31], v[188:191]\n" \
"ds_read_b128 a[20:23], v251 offset:4096\n" \
"v_mul_f32 v253, s31, v253\n" \
"v_mfma_f32_16x16x32_bf16 v[184:187], a[112:115], a[24:27], v[184:187]\n" \
"s_waitcnt lgkmcnt(6)\n" \
"ds_read_b64_tr_b16 a[92:93], v233 offset:41472\n" \
"v_mul_f32 v254, s31, v254\n" \
"v_mfma_f32_16x16x32_bf16 v[184:187], a[116:119], a[28:31], v[184:187]\n" \
"buffer_load_dwordx4 v226, s[48:51], s75 offen offset:2048 lds\n" \
"v_mfma_f32_16x16x32_bf16 v[180:183], a[104:107], a[24:27], v[180:183]\n" \
"ds_read_b64_tr_b16 a[94:95], v233 offset:43520\n" \
"v_cvt_pk_bf16_f32 v46, v243, v252\n" \
"v_mfma_f32_16x16x32_bf16 v[180:183], a[108:111], a[28:31], v[180:183]\n" \
"v_cvt_pk_bf16_f32 v47, v253, v254\n" \
"v_accvgpr_read_b32 v243, a164\n" \
"v_mfma_f32_16x16x32_bf16 v[176:179], a[96:99], a[24:27], v[176:179]\n" \
"v_accvgpr_read_b32 v252, a165\n" \
"v_accvgpr_read_b32 v253, a166\n" \
"v_mfma_f32_16x16x32_bf16 v[176:179], a[100:103], a[28:31], v[176:179]\n" \
"buffer_load_dwordx4 v227, s[48:51], s75 offen offset:3072 lds\n" \
"v_mfma_f32_16x16x32_bf16 v[64:67], a[64:67], a[0:3], v[64:67]\n" \
"s_waitcnt vmcnt(35)\n" \
"s_xor_b32 s75, s75, 0x80\n" \
"ds_read_b128 a[24:27], v250 offset:6144\n" \
"v_mfma_f32_16x16x32_bf16 v[64:67], a[68:71], a[4:7], v[64:67]\n" \
"s_add_u32 m0, s74, 135168\n" \
"ds_read_b128 a[28:31], v251 offset:6144\n" \
"v_accvgpr_read_b32 v254, a167\n" \
"v_mfma_f32_16x16x32_bf16 v[68:71], a[72:75], a[0:3], v[68:71]\n" \
"s_waitcnt lgkmcnt(4)\n" \
"ds_read_b64_tr_b16 a[96:97], v232 offset:33792\n" \
"v_mul_f32 v243, s31, v243\n" \
"v_mfma_f32_16x16x32_bf16 v[68:71], a[76:79], a[4:7], v[68:71]\n" \
"buffer_load_dwordx4 v224, s[48:51], s77 offen offset:0 lds\n" \
"v_mfma_f32_16x16x32_bf16 v[72:75], a[80:83], a[0:3], v[72:75]\n" \
"ds_read_b64_tr_b16 a[98:99], v232 offset:35840\n" \
"v_mul_f32 v252, s31, v252\n" \
"v_mfma_f32_16x16x32_bf16 v[72:75], a[84:87], a[4:7], v[72:75]\n" \
"s_waitcnt lgkmcnt(3)\n" \
"ds_read_b64_tr_b16 a[100:101], v232 offset:41984\n" \
"v_mul_f32 v253, s31, v253\n" \
"v_mfma_f32_16x16x32_bf16 v[76:79], a[88:91], a[0:3], v[76:79]\n" \
"ds_read_b64_tr_b16 a[102:103], v232 offset:44032\n" \
"v_mul_f32 v254, s31, v254\n" \
"v_mfma_f32_16x16x32_bf16 v[76:79], a[92:95], a[4:7], v[76:79]\n" \
"buffer_load_dwordx4 v225, s[48:51], s77 offen offset:1024 lds\n" \
"v_mfma_f32_16x16x32_bf16 v[108:111], a[88:91], a[8:11], v[108:111]\n" \
"ds_read_b64_tr_b16 a[104:105], v233 offset:33792\n" \
"v_cvt_pk_bf16_f32 v60, v243, v252\n" \
"v_mfma_f32_16x16x32_bf16 v[108:111], a[92:95], a[12:15], v[108:111]\n" \
"ds_read_b64_tr_b16 a[106:107], v233 offset:35840\n" \
"v_cvt_pk_bf16_f32 v61, v253, v254\n" \
"v_mfma_f32_16x16x32_bf16 v[104:107], a[80:83], a[8:11], v[104:107]\n" \
"s_waitcnt lgkmcnt(6)\n" \
"ds_read_b64_tr_b16 a[108:109], v233 offset:41984\n" \
"v_accvgpr_read_b32 v243, a180\n" \
"Lpg3342_%=:\n" \
"s_cbranch_execz Lpg5598_%=\n" \
"v_mfma_f32_16x16x32_bf16 v[104:107], a[84:87], a[12:15], v[104:107]\n" \
"buffer_load_dwordx4 v226, s[48:51], s77 offen offset:2048 lds\n" \
"v_mfma_f32_16x16x32_bf16 v[100:103], a[72:75], a[8:11], v[100:103]\n" \
"ds_read_b64_tr_b16 a[110:111], v233 offset:44032\n" \
"v_accvgpr_read_b32 v252, a181\n" \
"v_mfma_f32_16x16x32_bf16 v[100:103], a[76:79], a[12:15], v[100:103]\n" \
"ds_read_b64_tr_b16 a[112:113], v232 offset:34304\n" \
"v_accvgpr_read_b32 v253, a182\n" \
"v_mfma_f32_16x16x32_bf16 v[96:99], a[64:67], a[8:11], v[96:99]\n" \
"ds_read_b64_tr_b16 a[114:115], v232 offset:36352\n" \
"v_accvgpr_read_b32 v254, a183\n" \
"v_mfma_f32_16x16x32_bf16 v[96:99], a[68:71], a[12:15], v[96:99]\n" \
"buffer_load_dwordx4 v227, s[48:51], s77 offen offset:3072 lds\n" \
"v_mfma_f32_16x16x32_bf16 v[128:131], a[64:67], a[16:19], v[128:131]\n" \
"ds_read_b64_tr_b16 a[116:117], v232 offset:42496\n" \
"v_mul_f32 v243, s31, v243\n" \
"v_mfma_f32_16x16x32_bf16 v[128:131], a[68:71], a[20:23], v[128:131]\n" \
"ds_read_b64_tr_b16 a[118:119], v232 offset:44544\n" \
"v_mul_f32 v252, s31, v252\n" \
"v_mfma_f32_16x16x32_bf16 v[132:135], a[72:75], a[16:19], v[132:135]\n" \
"s_xor_b32 s77, s77, 0x80\n" \
"ds_read_b64_tr_b16 a[120:121], v233 offset:34304\n" \
"v_mul_f32 v253, s31, v253\n" \
"v_mfma_f32_16x16x32_bf16 v[132:135], a[76:79], a[20:23], v[132:135]\n" \
"v_mul_f32 v254, s31, v254\n" \
"v_cvt_pk_bf16_f32 v62, v243, v252\n" \
"v_mfma_f32_16x16x32_bf16 v[136:139], a[80:83], a[16:19], v[136:139]\n" \
"ds_read_b64_tr_b16 a[122:123], v233 offset:36352\n" \
"v_cvt_pk_bf16_f32 v63, v253, v254\n" \
"v_mfma_f32_16x16x32_bf16 v[136:139], a[84:87], a[20:23], v[136:139]\n" \
"ds_read_b64_tr_b16 a[124:125], v233 offset:42496\n" \
"v_accvgpr_read_b32 v243, a168\n" \
"v_mfma_f32_16x16x32_bf16 v[140:143], a[88:91], a[16:19], v[140:143]\n" \
"ds_read_b64_tr_b16 a[126:127], v233 offset:44544\n" \
"v_accvgpr_read_b32 v252, a169\n" \
"v_mfma_f32_16x16x32_bf16 v[140:143], a[92:95], a[20:23], v[140:143]\n" \
"buffer_store_dwordx4 v[204:207], v236, s[56:59], s93 offen offset:2048 sc1\n" \
"v_mfma_f32_16x16x32_bf16 v[172:175], a[88:91], a[24:27], v[172:175]\n" \
"v_accvgpr_read_b32 v253, a170\n" \
"v_accvgpr_read_b32 v254, a171\n" \
"v_mfma_f32_16x16x32_bf16 v[172:175], a[92:95], a[28:31], v[172:175]\n" \
"v_mul_f32 v243, s31, v243\n" \
"v_mul_f32 v252, s31, v252\n" \
"v_mfma_f32_16x16x32_bf16 v[168:171], a[80:83], a[24:27], v[168:171]\n" \
"v_mul_f32 v253, s31, v253\n" \
"v_mul_f32 v254, s31, v254\n" \
"v_mfma_f32_16x16x32_bf16 v[168:171], a[84:87], a[28:31], v[168:171]\n" \
"v_cvt_pk_bf16_f32 v20, v243, v252\n" \
"v_cvt_pk_bf16_f32 v21, v253, v254\n" \
"v_mfma_f32_16x16x32_bf16 v[164:167], a[72:75], a[24:27], v[164:167]\n" \
"v_accvgpr_read_b32 v243, a184\n" \
"v_accvgpr_read_b32 v252, a185\n" \
"v_mfma_f32_16x16x32_bf16 v[164:167], a[76:79], a[28:31], v[164:167]\n" \
"v_accvgpr_read_b32 v253, a186\n" \
"v_accvgpr_read_b32 v254, a187\n" \
"v_mfma_f32_16x16x32_bf16 v[160:163], a[64:67], a[24:27], v[160:163]\n" \
"v_mul_f32 v243, s31, v243\n" \
"v_mul_f32 v252, s31, v252\n" \
"v_mfma_f32_16x16x32_bf16 v[160:163], a[68:71], a[28:31], v[160:163]\n" \
"s_waitcnt vmcnt(35) lgkmcnt(0)\n" \
"s_barrier\n" \
"v_mfma_f32_16x16x32_bf16 v[80:83], a[96:99], a[0:3], v[80:83]\n" \
"ds_read_b64_tr_b16 a[64:65], v232 offset:49152\n" \
"v_mul_f32 v253, s31, v253\n" \
"v_mfma_f32_16x16x32_bf16 v[80:83], a[100:103], a[4:7], v[80:83]\n" \
"ds_read_b64_tr_b16 a[66:67], v232 offset:51200\n" \
"v_mul_f32 v254, s31, v254\n" \
"v_mfma_f32_16x16x32_bf16 v[84:87], a[104:107], a[0:3], v[84:87]\n" \
"s_add_u32 m0, s79, 32768\n" \
"ds_read_b64_tr_b16 a[68:69], v232 offset:57344\n" \
"v_cvt_pk_bf16_f32 v22, v243, v252\n" \
"v_mfma_f32_16x16x32_bf16 v[84:87], a[108:111], a[4:7], v[84:87]\n" \
"buffer_load_dwordx4 v228, s[52:55], s78 offen offset:0 lds\n" \
"v_mfma_f32_16x16x32_bf16 v[88:91], a[112:115], a[0:3], v[88:91]\n" \
"ds_read_b64_tr_b16 a[70:71], v232 offset:59392\n" \
"v_cvt_pk_bf16_f32 v23, v253, v254\n" \
"v_mfma_f32_16x16x32_bf16 v[88:91], a[116:119], a[4:7], v[88:91]\n" \
"ds_read_b64_tr_b16 a[72:73], v233 offset:49152\n" \
"v_accvgpr_read_b32 v243, a172\n" \
"v_mfma_f32_16x16x32_bf16 v[92:95], a[120:123], a[0:3], v[92:95]\n" \
"ds_read_b64_tr_b16 a[74:75], v233 offset:51200\n" \
"v_accvgpr_read_b32 v252, a173\n" \
"v_mfma_f32_16x16x32_bf16 v[92:95], a[124:127], a[4:7], v[92:95]\n" \
"buffer_load_dwordx4 v229, s[52:55], s78 offen offset:1024 lds\n" \
"v_mfma_f32_16x16x32_bf16 v[124:127], a[120:123], a[8:11], v[124:127]\n" \
"s_waitcnt vmcnt(35)\n" \
"ds_read_b128 a[0:3], v230 offset:0\n" \
"v_accvgpr_read_b32 v253, a174\n" \
"v_mfma_f32_16x16x32_bf16 v[124:127], a[124:127], a[12:15], v[124:127]\n" \
"ds_read_b128 a[4:7], v231 offset:0\n" \
"v_accvgpr_read_b32 v254, a175\n" \
"v_mfma_f32_16x16x32_bf16 v[120:123], a[112:115], a[8:11], v[120:123]\n" \
"s_xor_b32 s78, s78, 0x180000\n" \
"ds_read_b64_tr_b16 a[76:77], v233 offset:57344\n" \
"v_mul_f32 v243, s31, v243\n" \
"v_mfma_f32_16x16x32_bf16 v[120:123], a[116:119], a[12:15], v[120:123]\n" \
"buffer_load_dwordx4 v228, s[52:55], s89 offen offset:2048 lds\n" \
"v_mfma_f32_16x16x32_bf16 v[116:119], a[104:107], a[8:11], v[116:119]\n" \
"ds_read_b64_tr_b16 a[78:79], v233 offset:59392\n" \
"v_mul_f32 v252, s31, v252\n" \
"v_mfma_f32_16x16x32_bf16 v[116:119], a[108:111], a[12:15], v[116:119]\n" \
"ds_read_b64_tr_b16 a[80:81], v232 offset:49664\n" \
"v_mul_f32 v253, s31, v253\n" \
"v_mfma_f32_16x16x32_bf16 v[112:115], a[96:99], a[8:11], v[112:115]\n" \
"ds_read_b64_tr_b16 a[82:83], v232 offset:51712\n" \
"v_mul_f32 v254, s31, v254\n" \
"v_mfma_f32_16x16x32_bf16 v[112:115], a[100:103], a[12:15], v[112:115]\n" \
"buffer_load_dwordx4 v229, s[52:55], s89 offen offset:3072 lds\n" \
"v_mfma_f32_16x16x32_bf16 v[144:147], a[96:99], a[16:19], v[144:147]\n" \
"s_waitcnt vmcnt(35)\n" \
"s_xor_b32 s89, s89, 0x180000\n" \
"ds_read_b128 a[8:11], v230 offset:2048\n" \
"v_mfma_f32_16x16x32_bf16 v[144:147], a[100:103], a[20:23], v[144:147]\n" \
"s_add_u32 m0, s74, 65536\n" \
"ds_read_b128 a[12:15], v231 offset:2048\n" \
"v_cvt_pk_bf16_f32 v28, v243, v252\n" \
"v_mfma_f32_16x16x32_bf16 v[148:151], a[104:107], a[16:19], v[148:151]\n" \
"s_waitcnt lgkmcnt(6)\n" \
"ds_read_b64_tr_b16 a[84:85], v232 offset:57856\n" \
"v_cvt_pk_bf16_f32 v29, v253, v254\n" \
"v_mfma_f32_16x16x32_bf16 v[148:151], a[108:111], a[20:23], v[148:151]\n" \
"buffer_load_dwordx4 v224, s[48:51], s75 offen offset:0 lds\n" \
"v_mfma_f32_16x16x32_bf16 v[152:155], a[112:115], a[16:19], v[152:155]\n" \
"ds_read_b64_tr_b16 a[86:87], v232 offset:59904\n" \
"v_accvgpr_read_b32 v243, a188\n" \
"v_mfma_f32_16x16x32_bf16 v[152:155], a[116:119], a[20:23], v[152:155]\n" \
"ds_read_b64_tr_b16 a[88:89], v233 offset:49664\n" \
"v_accvgpr_read_b32 v252, a189\n" \
"v_mfma_f32_16x16x32_bf16 v[156:159], a[120:123], a[16:19], v[156:159]\n" \
"ds_read_b64_tr_b16 a[90:91], v233 offset:51712\n" \
"v_accvgpr_read_b32 v253, a190\n" \
"v_mfma_f32_16x16x32_bf16 v[156:159], a[124:127], a[20:23], v[156:159]\n" \
"buffer_load_dwordx4 v225, s[48:51], s75 offen offset:1024 lds\n" \
"v_mfma_f32_16x16x32_bf16 v[188:191], a[120:123], a[24:27], v[188:191]\n" \
"s_waitcnt vmcnt(35)\n" \
"ds_read_b128 a[16:19], v230 offset:4096\n" \
"v_accvgpr_read_b32 v254, a191\n" \
"v_mfma_f32_16x16x32_bf16 v[188:191], a[124:127], a[28:31], v[188:191]\n" \
"ds_read_b128 a[20:23], v231 offset:4096\n" \
"v_mul_f32 v243, s31, v243\n" \
"v_mfma_f32_16x16x32_bf16 v[184:187], a[112:115], a[24:27], v[184:187]\n" \
"s_waitcnt lgkmcnt(6)\n" \
"ds_read_b64_tr_b16 a[92:93], v233 offset:57856\n" \
"v_mul_f32 v252, s31, v252\n" \
"v_mfma_f32_16x16x32_bf16 v[184:187], a[116:119], a[28:31], v[184:187]\n" \
"buffer_load_dwordx4 v226, s[48:51], s75 offen offset:2048 lds\n" \
"v_mfma_f32_16x16x32_bf16 v[180:183], a[104:107], a[24:27], v[180:183]\n" \
"ds_read_b64_tr_b16 a[94:95], v233 offset:59904\n" \
"v_mul_f32 v253, s31, v253\n" \
"v_mfma_f32_16x16x32_bf16 v[180:183], a[108:111], a[28:31], v[180:183]\n" \
"v_mul_f32 v254, s31, v254\n" \
"v_cvt_pk_bf16_f32 v30, v243, v252\n" \
"v_mfma_f32_16x16x32_bf16 v[176:179], a[96:99], a[24:27], v[176:179]\n" \
"v_cvt_pk_bf16_f32 v31, v253, v254\n" \
"v_accvgpr_read_b32 v243, a128\n" \
"v_mfma_f32_16x16x32_bf16 v[176:179], a[100:103], a[28:31], v[176:179]\n" \
"buffer_load_dwordx4 v227, s[48:51], s75 offen offset:3072 lds\n" \
"v_mfma_f32_16x16x32_bf16 v[64:67], a[64:67], a[0:3], v[64:67]\n" \
"s_waitcnt vmcnt(35)\n" \
"s_xor_b32 s75, s75, 0x180\n" \
"ds_read_b128 a[24:27], v230 offset:6144\n" \
"v_mfma_f32_16x16x32_bf16 v[64:67], a[68:71], a[4:7], v[64:67]\n" \
"s_add_u32 m0, s74, 69632\n" \
"ds_read_b128 a[28:31], v231 offset:6144\n" \
"v_accvgpr_read_b32 v252, a129\n" \
"v_mfma_f32_16x16x32_bf16 v[68:71], a[72:75], a[0:3], v[68:71]\n" \
"s_waitcnt lgkmcnt(4)\n" \
"ds_read_b64_tr_b16 a[96:97], v232 offset:50176\n" \
"v_accvgpr_read_b32 v253, a130\n" \
"v_mfma_f32_16x16x32_bf16 v[68:71], a[76:79], a[4:7], v[68:71]\n" \
"buffer_load_dwordx4 v224, s[48:51], s77 offen offset:0 lds\n" \
"v_mfma_f32_16x16x32_bf16 v[72:75], a[80:83], a[0:3], v[72:75]\n" \
"ds_read_b64_tr_b16 a[98:99], v232 offset:52224\n" \
"v_accvgpr_read_b32 v254, a131\n" \
"v_mfma_f32_16x16x32_bf16 v[72:75], a[84:87], a[4:7], v[72:75]\n" \
"s_waitcnt lgkmcnt(3)\n" \
"ds_read_b64_tr_b16 a[100:101], v232 offset:58368\n" \
"v_mul_f32 v243, s31, v243\n" \
"v_mfma_f32_16x16x32_bf16 v[76:79], a[88:91], a[0:3], v[76:79]\n" \
"ds_read_b64_tr_b16 a[102:103], v232 offset:60416\n" \
"v_mul_f32 v252, s31, v252\n" \
"v_mfma_f32_16x16x32_bf16 v[76:79], a[92:95], a[4:7], v[76:79]\n" \
"buffer_load_dwordx4 v225, s[48:51], s77 offen offset:1024 lds\n" \
"v_mfma_f32_16x16x32_bf16 v[108:111], a[88:91], a[8:11], v[108:111]\n" \
"ds_read_b64_tr_b16 a[104:105], v233 offset:50176\n" \
"v_mul_f32 v253, s31, v253\n" \
"v_mfma_f32_16x16x32_bf16 v[108:111], a[92:95], a[12:15], v[108:111]\n" \
"ds_read_b64_tr_b16 a[106:107], v233 offset:52224\n" \
"v_mul_f32 v254, s31, v254\n" \
"v_mfma_f32_16x16x32_bf16 v[104:107], a[80:83], a[8:11], v[104:107]\n" \
"s_waitcnt lgkmcnt(6)\n" \
"ds_read_b64_tr_b16 a[108:109], v233 offset:58368\n" \
"v_cvt_pk_bf16_f32 v0, v243, v252\n" \
"v_mfma_f32_16x16x32_bf16 v[104:107], a[84:87], a[12:15], v[104:107]\n" \
"buffer_load_dwordx4 v226, s[48:51], s77 offen offset:2048 lds\n" \
"v_mfma_f32_16x16x32_bf16 v[100:103], a[72:75], a[8:11], v[100:103]\n" \
"ds_read_b64_tr_b16 a[110:111], v233 offset:60416\n" \
"v_cvt_pk_bf16_f32 v1, v253, v254\n" \
"v_mfma_f32_16x16x32_bf16 v[100:103], a[76:79], a[12:15], v[100:103]\n" \
"ds_read_b64_tr_b16 a[112:113], v232 offset:50688\n" \
"v_accvgpr_read_b32 v243, a144\n" \
"v_mfma_f32_16x16x32_bf16 v[96:99], a[64:67], a[8:11], v[96:99]\n" \
"ds_read_b64_tr_b16 a[114:115], v232 offset:52736\n" \
"v_accvgpr_read_b32 v252, a145\n" \
"v_mfma_f32_16x16x32_bf16 v[96:99], a[68:71], a[12:15], v[96:99]\n" \
"buffer_load_dwordx4 v227, s[48:51], s77 offen offset:3072 lds\n" \
"v_mfma_f32_16x16x32_bf16 v[128:131], a[64:67], a[16:19], v[128:131]\n" \
"ds_read_b64_tr_b16 a[116:117], v232 offset:58880\n" \
"v_accvgpr_read_b32 v253, a146\n" \
"v_mfma_f32_16x16x32_bf16 v[128:131], a[68:71], a[20:23], v[128:131]\n" \
"ds_read_b64_tr_b16 a[118:119], v232 offset:60928\n" \
"v_accvgpr_read_b32 v254, a147\n" \
"v_mfma_f32_16x16x32_bf16 v[132:135], a[72:75], a[16:19], v[132:135]\n" \
"s_xor_b32 s77, s77, 0x180\n" \
"ds_read_b64_tr_b16 a[120:121], v233 offset:50688\n" \
"v_mul_f32 v243, s31, v243\n" \
"v_mfma_f32_16x16x32_bf16 v[132:135], a[76:79], a[20:23], v[132:135]\n" \
"v_mul_f32 v252, s31, v252\n" \
"v_mul_f32 v253, s31, v253\n" \
"v_mfma_f32_16x16x32_bf16 v[136:139], a[80:83], a[16:19], v[136:139]\n" \
"ds_read_b64_tr_b16 a[122:123], v233 offset:52736\n" \
"v_mul_f32 v254, s31, v254\n" \
"v_mfma_f32_16x16x32_bf16 v[136:139], a[84:87], a[20:23], v[136:139]\n" \
"ds_read_b64_tr_b16 a[124:125], v233 offset:58880\n" \
"v_cvt_pk_bf16_f32 v2, v243, v252\n" \
"v_mfma_f32_16x16x32_bf16 v[140:143], a[88:91], a[16:19], v[140:143]\n" \
"ds_read_b64_tr_b16 a[126:127], v233 offset:60928\n" \
"v_cvt_pk_bf16_f32 v3, v253, v254\n" \
"v_mfma_f32_16x16x32_bf16 v[140:143], a[92:95], a[20:23], v[140:143]\n" \
"buffer_store_dwordx4 v[220:223], v236, s[56:59], s93 offen offset:2304 sc1\n" \
"v_mfma_f32_16x16x32_bf16 v[172:175], a[88:91], a[24:27], v[172:175]\n" \
"v_accvgpr_read_b32 v243, a132\n" \
"v_accvgpr_read_b32 v252, a133\n" \
"v_mfma_f32_16x16x32_bf16 v[172:175], a[92:95], a[28:31], v[172:175]\n" \
"v_accvgpr_read_b32 v253, a134\n" \
"v_accvgpr_read_b32 v254, a135\n" \
"v_mfma_f32_16x16x32_bf16 v[168:171], a[80:83], a[24:27], v[168:171]\n" \
"v_mul_f32 v243, s31, v243\n" \
"v_mul_f32 v252, s31, v252\n" \
"v_mfma_f32_16x16x32_bf16 v[168:171], a[84:87], a[28:31], v[168:171]\n" \
"v_mul_f32 v253, s31, v253\n" \
"v_mul_f32 v254, s31, v254\n" \
"v_mfma_f32_16x16x32_bf16 v[164:167], a[72:75], a[24:27], v[164:167]\n" \
"v_cvt_pk_bf16_f32 v4, v243, v252\n" \
"v_cvt_pk_bf16_f32 v5, v253, v254\n" \
"v_mfma_f32_16x16x32_bf16 v[164:167], a[76:79], a[28:31], v[164:167]\n" \
"v_accvgpr_read_b32 v243, a148\n" \
"v_accvgpr_read_b32 v252, a149\n" \
"v_mfma_f32_16x16x32_bf16 v[160:163], a[64:67], a[24:27], v[160:163]\n" \
"v_accvgpr_read_b32 v253, a150\n" \
"v_accvgpr_read_b32 v254, a151\n" \
"v_mfma_f32_16x16x32_bf16 v[160:163], a[68:71], a[28:31], v[160:163]\n" \
"s_waitcnt vmcnt(35) lgkmcnt(0)\n" \
"s_barrier\n" \
"v_mfma_f32_16x16x32_bf16 v[80:83], a[96:99], a[0:3], v[80:83]\n" \
"ds_read_b64_tr_b16 a[64:65], v232 offset:0\n" \
"v_mul_f32 v243, s31, v243\n" \
"v_mfma_f32_16x16x32_bf16 v[80:83], a[100:103], a[4:7], v[80:83]\n" \
"ds_read_b64_tr_b16 a[66:67], v232 offset:2048\n" \
"v_mul_f32 v252, s31, v252\n" \
"v_mfma_f32_16x16x32_bf16 v[84:87], a[104:107], a[0:3], v[84:87]\n" \
"s_add_u32 m0, s79, 49152\n" \
"ds_read_b64_tr_b16 a[68:69], v232 offset:8192\n" \
"v_mul_f32 v253, s31, v253\n" \
"v_mfma_f32_16x16x32_bf16 v[84:87], a[108:111], a[4:7], v[84:87]\n" \
"buffer_load_dwordx4 v228, s[52:55], s78 offen offset:0 lds\n" \
"v_mfma_f32_16x16x32_bf16 v[88:91], a[112:115], a[0:3], v[88:91]\n" \
"ds_read_b64_tr_b16 a[70:71], v232 offset:10240\n" \
"v_mul_f32 v254, s31, v254\n" \
"v_mfma_f32_16x16x32_bf16 v[88:91], a[116:119], a[4:7], v[88:91]\n" \
"ds_read_b64_tr_b16 a[72:73], v233 offset:0\n" \
"v_cvt_pk_bf16_f32 v6, v243, v252\n" \
"v_mfma_f32_16x16x32_bf16 v[92:95], a[120:123], a[0:3], v[92:95]\n" \
"ds_read_b64_tr_b16 a[74:75], v233 offset:2048\n" \
"v_cvt_pk_bf16_f32 v7, v253, v254\n" \
"v_mfma_f32_16x16x32_bf16 v[92:95], a[124:127], a[4:7], v[92:95]\n" \
"buffer_load_dwordx4 v229, s[52:55], s78 offen offset:1024 lds\n" \
"v_mfma_f32_16x16x32_bf16 v[124:127], a[120:123], a[8:11], v[124:127]\n" \
"s_waitcnt vmcnt(35)\n" \
"ds_read_b128 a[0:3], v230 offset:32768\n" \
"v_accvgpr_read_b32 v243, a136\n" \
"v_mfma_f32_16x16x32_bf16 v[124:127], a[124:127], a[12:15], v[124:127]\n" \
"ds_read_b128 a[4:7], v231 offset:32768\n" \
"v_accvgpr_read_b32 v252, a137\n" \
"v_mfma_f32_16x16x32_bf16 v[120:123], a[112:115], a[8:11], v[120:123]\n" \
"s_xor_b32 s78, s78, 0x300000\n" \
"ds_read_b64_tr_b16 a[76:77], v233 offset:8192\n" \
"v_accvgpr_read_b32 v253, a138\n" \
"v_mfma_f32_16x16x32_bf16 v[120:123], a[116:119], a[12:15], v[120:123]\n" \
"buffer_load_dwordx4 v228, s[52:55], s89 offen offset:2048 lds\n" \
"v_mfma_f32_16x16x32_bf16 v[116:119], a[104:107], a[8:11], v[116:119]\n" \
"ds_read_b64_tr_b16 a[78:79], v233 offset:10240\n" \
"v_accvgpr_read_b32 v254, a139\n" \
"v_mfma_f32_16x16x32_bf16 v[116:119], a[108:111], a[12:15], v[116:119]\n" \
"ds_read_b64_tr_b16 a[80:81], v232 offset:512\n" \
"v_mul_f32 v243, s31, v243\n" \
"v_mfma_f32_16x16x32_bf16 v[112:115], a[96:99], a[8:11], v[112:115]\n" \
"ds_read_b64_tr_b16 a[82:83], v232 offset:2560\n" \
"v_mul_f32 v252, s31, v252\n" \
"v_mfma_f32_16x16x32_bf16 v[112:115], a[100:103], a[12:15], v[112:115]\n" \
"buffer_load_dwordx4 v229, s[52:55], s89 offen offset:3072 lds\n" \
"v_mfma_f32_16x16x32_bf16 v[144:147], a[96:99], a[16:19], v[144:147]\n" \
"s_waitcnt vmcnt(35)\n" \
"s_xor_b32 s89, s89, 0x300000\n" \
"ds_read_b128 a[8:11], v230 offset:34816\n" \
"v_mfma_f32_16x16x32_bf16 v[144:147], a[100:103], a[20:23], v[144:147]\n" \
"s_add_u32 m0, s74, 98304\n" \
"ds_read_b128 a[12:15], v231 offset:34816\n" \
"v_mul_f32 v253, s31, v253\n" \
"v_mfma_f32_16x16x32_bf16 v[148:151], a[104:107], a[16:19], v[148:151]\n" \
"s_waitcnt lgkmcnt(6)\n" \
"ds_read_b64_tr_b16 a[84:85], v232 offset:8704\n" \
"v_mul_f32 v254, s31, v254\n" \
"v_mfma_f32_16x16x32_bf16 v[148:151], a[108:111], a[20:23], v[148:151]\n" \
"buffer_load_dwordx4 v224, s[48:51], s75 offen offset:0 lds\n" \
"v_mfma_f32_16x16x32_bf16 v[152:155], a[112:115], a[16:19], v[152:155]\n" \
"ds_read_b64_tr_b16 a[86:87], v232 offset:10752\n" \
"v_cvt_pk_bf16_f32 v8, v243, v252\n" \
"v_mfma_f32_16x16x32_bf16 v[152:155], a[116:119], a[20:23], v[152:155]\n" \
"ds_read_b64_tr_b16 a[88:89], v233 offset:512\n" \
"v_cvt_pk_bf16_f32 v9, v253, v254\n" \
"v_mfma_f32_16x16x32_bf16 v[156:159], a[120:123], a[16:19], v[156:159]\n" \
"ds_read_b64_tr_b16 a[90:91], v233 offset:2560\n" \
"v_accvgpr_read_b32 v243, a152\n" \
"v_mfma_f32_16x16x32_bf16 v[156:159], a[124:127], a[20:23], v[156:159]\n" \
"buffer_load_dwordx4 v225, s[48:51], s75 offen offset:1024 lds\n" \
"v_mfma_f32_16x16x32_bf16 v[188:191], a[120:123], a[24:27], v[188:191]\n" \
"s_waitcnt vmcnt(35)\n" \
"ds_read_b128 a[16:19], v230 offset:36864\n" \
"v_accvgpr_read_b32 v252, a153\n" \
"v_mfma_f32_16x16x32_bf16 v[188:191], a[124:127], a[28:31], v[188:191]\n" \
"ds_read_b128 a[20:23], v231 offset:36864\n" \
"v_accvgpr_read_b32 v253, a154\n" \
"v_mfma_f32_16x16x32_bf16 v[184:187], a[112:115], a[24:27], v[184:187]\n" \
"s_waitcnt lgkmcnt(6)\n" \
"ds_read_b64_tr_b16 a[92:93], v233 offset:8704\n" \
"v_accvgpr_read_b32 v254, a155\n" \
"v_mfma_f32_16x16x32_bf16 v[184:187], a[116:119], a[28:31], v[184:187]\n" \
"buffer_load_dwordx4 v226, s[48:51], s75 offen offset:2048 lds\n" \
"v_mfma_f32_16x16x32_bf16 v[180:183], a[104:107], a[24:27], v[180:183]\n" \
"ds_read_b64_tr_b16 a[94:95], v233 offset:10752\n" \
"v_mul_f32 v243, s31, v243\n" \
"v_mfma_f32_16x16x32_bf16 v[180:183], a[108:111], a[28:31], v[180:183]\n" \
"v_mul_f32 v252, s31, v252\n" \
"v_mul_f32 v253, s31, v253\n" \
"v_mfma_f32_16x16x32_bf16 v[176:179], a[96:99], a[24:27], v[176:179]\n" \
"v_mul_f32 v254, s31, v254\n" \
"v_cvt_pk_bf16_f32 v10, v243, v252\n" \
"v_mfma_f32_16x16x32_bf16 v[176:179], a[100:103], a[28:31], v[176:179]\n" \
"buffer_load_dwordx4 v227, s[48:51], s75 offen offset:3072 lds\n" \
"v_mfma_f32_16x16x32_bf16 v[64:67], a[64:67], a[0:3], v[64:67]\n" \
"s_waitcnt vmcnt(35)\n" \
"s_xor_b32 s75, s75, 0x300\n" \
"ds_read_b128 a[24:27], v230 offset:38912\n" \
"v_mfma_f32_16x16x32_bf16 v[64:67], a[68:71], a[4:7], v[64:67]\n" \
"s_add_u32 m0, s74, 102400\n" \
"ds_read_b128 a[28:31], v231 offset:38912\n" \
"v_cvt_pk_bf16_f32 v11, v253, v254\n" \
"v_mfma_f32_16x16x32_bf16 v[68:71], a[72:75], a[0:3], v[68:71]\n" \
"s_waitcnt lgkmcnt(4)\n" \
"ds_read_b64_tr_b16 a[96:97], v232 offset:1024\n" \
"v_accvgpr_read_b32 v243, a140\n" \
"v_mfma_f32_16x16x32_bf16 v[68:71], a[76:79], a[4:7], v[68:71]\n" \
"buffer_load_dwordx4 v224, s[48:51], s77 offen offset:0 lds\n" \
"v_mfma_f32_16x16x32_bf16 v[72:75], a[80:83], a[0:3], v[72:75]\n" \
"ds_read_b64_tr_b16 a[98:99], v232 offset:3072\n" \
"v_accvgpr_read_b32 v252, a141\n" \
"v_mfma_f32_16x16x32_bf16 v[72:75], a[84:87], a[4:7], v[72:75]\n" \
"s_waitcnt lgkmcnt(3)\n" \
"ds_read_b64_tr_b16 a[100:101], v232 offset:9216\n" \
"v_accvgpr_read_b32 v253, a142\n" \
"v_mfma_f32_16x16x32_bf16 v[76:79], a[88:91], a[0:3], v[76:79]\n" \
"ds_read_b64_tr_b16 a[102:103], v232 offset:11264\n" \
"v_accvgpr_read_b32 v254, a143\n" \
"v_mfma_f32_16x16x32_bf16 v[76:79], a[92:95], a[4:7], v[76:79]\n" \
"buffer_load_dwordx4 v225, s[48:51], s77 offen offset:1024 lds\n" \
"v_mfma_f32_16x16x32_bf16 v[108:111], a[88:91], a[8:11], v[108:111]\n" \
"ds_read_b64_tr_b16 a[104:105], v233 offset:1024\n" \
"v_mul_f32 v243, s31, v243\n" \
"v_mfma_f32_16x16x32_bf16 v[108:111], a[92:95], a[12:15], v[108:111]\n" \
"ds_read_b64_tr_b16 a[106:107], v233 offset:3072\n" \
"v_mul_f32 v252, s31, v252\n" \
"v_mfma_f32_16x16x32_bf16 v[104:107], a[80:83], a[8:11], v[104:107]\n" \
"s_waitcnt lgkmcnt(6)\n" \
"ds_read_b64_tr_b16 a[108:109], v233 offset:9216\n" \
"v_mul_f32 v253, s31, v253\n" \
"v_mfma_f32_16x16x32_bf16 v[104:107], a[84:87], a[12:15], v[104:107]\n" \
"buffer_load_dwordx4 v226, s[48:51], s77 offen offset:2048 lds\n" \
"v_mfma_f32_16x16x32_bf16 v[100:103], a[72:75], a[8:11], v[100:103]\n" \
"ds_read_b64_tr_b16 a[110:111], v233 offset:11264\n" \
"v_mul_f32 v254, s31, v254\n" \
"v_mfma_f32_16x16x32_bf16 v[100:103], a[76:79], a[12:15], v[100:103]\n" \
"ds_read_b64_tr_b16 a[112:113], v232 offset:1536\n" \
"v_cvt_pk_bf16_f32 v12, v243, v252\n" \
"v_mfma_f32_16x16x32_bf16 v[96:99], a[64:67], a[8:11], v[96:99]\n" \
"ds_read_b64_tr_b16 a[114:115], v232 offset:3584\n" \
"v_cvt_pk_bf16_f32 v13, v253, v254\n" \
"v_mfma_f32_16x16x32_bf16 v[96:99], a[68:71], a[12:15], v[96:99]\n" \
"buffer_load_dwordx4 v227, s[48:51], s77 offen offset:3072 lds\n" \
"v_mfma_f32_16x16x32_bf16 v[128:131], a[64:67], a[16:19], v[128:131]\n" \
"ds_read_b64_tr_b16 a[116:117], v232 offset:9728\n" \
"v_accvgpr_read_b32 v243, a156\n" \
"v_mfma_f32_16x16x32_bf16 v[128:131], a[68:71], a[20:23], v[128:131]\n" \
"ds_read_b64_tr_b16 a[118:119], v232 offset:11776\n" \
"v_accvgpr_read_b32 v252, a157\n" \
"v_mfma_f32_16x16x32_bf16 v[132:135], a[72:75], a[16:19], v[132:135]\n" \
"s_xor_b32 s77, s77, 0x300\n" \
"ds_read_b64_tr_b16 a[120:121], v233 offset:1536\n" \
"v_accvgpr_read_b32 v253, a158\n" \
"v_mfma_f32_16x16x32_bf16 v[132:135], a[76:79], a[20:23], v[132:135]\n" \
"v_accvgpr_read_b32 v254, a159\n" \
"v_mul_f32 v243, s31, v243\n" \
"v_mfma_f32_16x16x32_bf16 v[136:139], a[80:83], a[16:19], v[136:139]\n" \
"ds_read_b64_tr_b16 a[122:123], v233 offset:3584\n" \
"v_mul_f32 v252, s31, v252\n" \
"v_mfma_f32_16x16x32_bf16 v[136:139], a[84:87], a[20:23], v[136:139]\n" \
"ds_read_b64_tr_b16 a[124:125], v233 offset:9728\n" \
"v_mul_f32 v253, s31, v253\n" \
"v_mfma_f32_16x16x32_bf16 v[140:143], a[88:91], a[16:19], v[140:143]\n" \
"s_add_u32 s93, s87, 17858560\n" \
"ds_read_b64_tr_b16 a[126:127], v233 offset:11776\n" \
"v_mul_f32 v254, s31, v254\n" \
"v_mfma_f32_16x16x32_bf16 v[140:143], a[92:95], a[20:23], v[140:143]\n" \
"buffer_store_dwordx4 a[224:227], v234, s[56:59], s93 offen offset:0 sc1\n" \
"v_mfma_f32_16x16x32_bf16 v[172:175], a[88:91], a[24:27], v[172:175]\n" \
"v_cvt_pk_bf16_f32 v14, v243, v252\n" \
"v_cvt_pk_bf16_f32 v15, v253, v254\n" \
"v_mfma_f32_16x16x32_bf16 v[172:175], a[92:95], a[28:31], v[172:175]\n" \
"v_accvgpr_write_b32 a56, v244\n" \
"v_accvgpr_write_b32 a57, v245\n" \
"v_mfma_f32_16x16x32_bf16 v[168:171], a[80:83], a[24:27], v[168:171]\n" \
"v_accvgpr_write_b32 a58, v246\n" \
"v_accvgpr_write_b32 a59, v247\n" \
"v_mfma_f32_16x16x32_bf16 v[168:171], a[84:87], a[28:31], v[168:171]\n" \
"v_accvgpr_write_b32 a60, v248\n" \
"v_accvgpr_write_b32 a61, v249\n" \
"v_mfma_f32_16x16x32_bf16 v[164:167], a[72:75], a[24:27], v[164:167]\n" \
"v_accvgpr_write_b32 a48, 0\n" \
"v_accvgpr_write_b32 a49, 0\n" \
"v_mfma_f32_16x16x32_bf16 v[164:167], a[76:79], a[28:31], v[164:167]\n" \
"v_accvgpr_write_b32 a50, 0\n" \
"v_accvgpr_write_b32 a51, 0\n" \
"v_mfma_f32_16x16x32_bf16 v[160:163], a[64:67], a[24:27], v[160:163]\n" \
"v_accvgpr_write_b32 a52, 0\n" \
"v_accvgpr_write_b32 a53, 0\n" \
"v_mfma_f32_16x16x32_bf16 v[160:163], a[68:71], a[28:31], v[160:163]\n" \
"s_waitcnt vmcnt(35) lgkmcnt(0)\n" \
"s_barrier\n" \
"v_mfma_f32_16x16x32_bf16 v[80:83], a[96:99], a[0:3], v[80:83]\n" \
"ds_read_b64_tr_b16 a[64:65], v232 offset:16384\n" \
"v_mfma_f32_16x16x32_bf16 v[80:83], a[100:103], a[4:7], v[80:83]\n" \
"ds_read_b64_tr_b16 a[66:67], v232 offset:18432\n" \
"v_mfma_f32_16x16x32_bf16 v[84:87], a[104:107], a[0:3], v[84:87]\n" \
"s_add_u32 m0, s79, 0\n" \
"ds_read_b64_tr_b16 a[68:69], v232 offset:24576\n" \
"v_mfma_f32_16x16x32_bf16 v[84:87], a[108:111], a[4:7], v[84:87]\n" \
"buffer_load_dwordx4 v228, s[52:55], s78 offen offset:0 lds\n" \
"v_mfma_f32_16x16x32_bf16 v[88:91], a[112:115], a[0:3], v[88:91]\n" \
"ds_read_b64_tr_b16 a[70:71], v232 offset:26624\n" \
"v_mfma_f32_16x16x32_bf16 v[88:91], a[116:119], a[4:7], v[88:91]\n" \
"ds_read_b64_tr_b16 a[72:73], v233 offset:16384\n" \
"v_mfma_f32_16x16x32_bf16 v[92:95], a[120:123], a[0:3], v[92:95]\n" \
"ds_read_b64_tr_b16 a[74:75], v233 offset:18432\n" \
"v_mfma_f32_16x16x32_bf16 v[92:95], a[124:127], a[4:7], v[92:95]\n" \
"buffer_load_dwordx4 v229, s[52:55], s78 offen offset:1024 lds\n" \
"v_mfma_f32_16x16x32_bf16 v[124:127], a[120:123], a[8:11], v[124:127]\n" \
"s_waitcnt vmcnt(35)\n" \
"ds_read_b128 a[0:3], v250 offset:0\n" \
"v_mfma_f32_16x16x32_bf16 v[124:127], a[124:127], a[12:15], v[124:127]\n" \
"ds_read_b128 a[4:7], v251 offset:0\n" \
"v_mfma_f32_16x16x32_bf16 v[120:123], a[112:115], a[8:11], v[120:123]\n" \
"s_xor_b32 s78, s78, 0x80000\n" \
"ds_read_b64_tr_b16 a[76:77], v233 offset:24576\n" \
"v_mfma_f32_16x16x32_bf16 v[120:123], a[116:119], a[12:15], v[120:123]\n" \
"buffer_load_dwordx4 v228, s[52:55], s89 offen offset:2048 lds\n" \
"v_mfma_f32_16x16x32_bf16 v[116:119], a[104:107], a[8:11], v[116:119]\n" \
"ds_read_b64_tr_b16 a[78:79], v233 offset:26624\n" \
"v_mfma_f32_16x16x32_bf16 v[116:119], a[108:111], a[12:15], v[116:119]\n" \
"ds_read_b64_tr_b16 a[80:81], v232 offset:16896\n" \
"v_mfma_f32_16x16x32_bf16 v[112:115], a[96:99], a[8:11], v[112:115]\n" \
"ds_read_b64_tr_b16 a[82:83], v232 offset:18944\n" \
"v_mfma_f32_16x16x32_bf16 v[112:115], a[100:103], a[12:15], v[112:115]\n" \
"buffer_load_dwordx4 v229, s[52:55], s89 offen offset:3072 lds\n" \
"v_mfma_f32_16x16x32_bf16 v[144:147], a[96:99], a[16:19], v[144:147]\n" \
"s_waitcnt vmcnt(35)\n" \
"s_xor_b32 s89, s89, 0x80000\n" \
"ds_read_b128 a[8:11], v250 offset:2048\n" \
"v_mfma_f32_16x16x32_bf16 v[144:147], a[100:103], a[20:23], v[144:147]\n" \
"s_add_u32 m0, s74, 131072\n" \
"ds_read_b128 a[12:15], v251 offset:2048\n" \
"v_mfma_f32_16x16x32_bf16 v[148:151], a[104:107], a[16:19], v[148:151]\n" \
"s_waitcnt lgkmcnt(6)\n" \
"ds_read_b64_tr_b16 a[84:85], v232 offset:25088\n" \
"v_mfma_f32_16x16x32_bf16 v[148:151], a[108:111], a[20:23], v[148:151]\n" \
"buffer_load_dwordx4 v224, s[48:51], s75 offen offset:0 lds\n" \
"v_mfma_f32_16x16x32_bf16 v[152:155], a[112:115], a[16:19], v[152:155]\n" \
"ds_read_b64_tr_b16 a[86:87], v232 offset:27136\n" \
"v_mfma_f32_16x16x32_bf16 v[152:155], a[116:119], a[20:23], v[152:155]\n" \
"ds_read_b64_tr_b16 a[88:89], v233 offset:16896\n" \
"v_mfma_f32_16x16x32_bf16 v[156:159], a[120:123], a[16:19], v[156:159]\n" \
"ds_read_b64_tr_b16 a[90:91], v233 offset:18944\n" \
"v_mfma_f32_16x16x32_bf16 v[156:159], a[124:127], a[20:23], v[156:159]\n" \
"buffer_load_dwordx4 v225, s[48:51], s75 offen offset:1024 lds\n" \
"v_mfma_f32_16x16x32_bf16 v[188:191], a[120:123], a[24:27], v[188:191]\n" \
"s_waitcnt vmcnt(35)\n" \
"ds_read_b128 a[16:19], v250 offset:4096\n" \
"v_mfma_f32_16x16x32_bf16 v[188:191], a[124:127], a[28:31], v[188:191]\n" \
"ds_read_b128 a[20:23], v251 offset:4096\n" \
"v_mfma_f32_16x16x32_bf16 v[184:187], a[112:115], a[24:27], v[184:187]\n" \
"s_waitcnt lgkmcnt(6)\n" \
"ds_read_b64_tr_b16 a[92:93], v233 offset:25088\n" \
"v_mfma_f32_16x16x32_bf16 v[184:187], a[116:119], a[28:31], v[184:187]\n" \
"buffer_load_dwordx4 v226, s[48:51], s75 offen offset:2048 lds\n" \
"v_mfma_f32_16x16x32_bf16 v[180:183], a[104:107], a[24:27], v[180:183]\n" \
"ds_read_b64_tr_b16 a[94:95], v233 offset:27136\n" \
"v_mfma_f32_16x16x32_bf16 v[180:183], a[108:111], a[28:31], v[180:183]\n" \
"v_mfma_f32_16x16x32_bf16 v[176:179], a[96:99], a[24:27], v[176:179]\n" \
"v_mfma_f32_16x16x32_bf16 v[176:179], a[100:103], a[28:31], v[176:179]\n" \
"buffer_load_dwordx4 v227, s[48:51], s75 offen offset:3072 lds\n" \
"v_mfma_f32_16x16x32_bf16 v[64:67], a[64:67], a[0:3], v[64:67]\n" \
"s_waitcnt vmcnt(35)\n" \
"s_xor_b32 s75, s75, 0x80\n" \
"ds_read_b128 a[24:27], v250 offset:6144\n" \
"v_mfma_f32_16x16x32_bf16 v[64:67], a[68:71], a[4:7], v[64:67]\n" \
"s_add_u32 m0, s74, 135168\n" \
"ds_read_b128 a[28:31], v251 offset:6144\n" \
"v_mfma_f32_16x16x32_bf16 v[68:71], a[72:75], a[0:3], v[68:71]\n" \
"s_waitcnt lgkmcnt(2)\n" \
"ds_read_b64_tr_b16 a[96:97], v232 offset:17408\n" \
"v_mfma_f32_16x16x32_bf16 v[68:71], a[76:79], a[4:7], v[68:71]\n" \
"buffer_load_dwordx4 v224, s[48:51], s77 offen offset:0 lds\n" \
"v_mfma_f32_16x16x32_bf16 v[72:75], a[80:83], a[0:3], v[72:75]\n" \
"ds_read_b64_tr_b16 a[98:99], v232 offset:19456\n" \
"v_mfma_f32_16x16x32_bf16 v[72:75], a[84:87], a[4:7], v[72:75]\n" \
"ds_read_b64_tr_b16 a[100:101], v232 offset:25600\n" \
"v_mfma_f32_16x16x32_bf16 v[76:79], a[88:91], a[0:3], v[76:79]\n" \
"ds_read_b64_tr_b16 a[102:103], v232 offset:27648\n" \
"v_mfma_f32_16x16x32_bf16 v[76:79], a[92:95], a[4:7], v[76:79]\n" \
"buffer_load_dwordx4 v225, s[48:51], s77 offen offset:1024 lds\n" \
"v_mfma_f32_16x16x32_bf16 v[108:111], a[88:91], a[8:11], v[108:111]\n" \
"ds_read_b64_tr_b16 a[104:105], v233 offset:17408\n" \
"v_mfma_f32_16x16x32_bf16 v[108:111], a[92:95], a[12:15], v[108:111]\n" \
"ds_read_b64_tr_b16 a[106:107], v233 offset:19456\n" \
"v_mfma_f32_16x16x32_bf16 v[104:107], a[80:83], a[8:11], v[104:107]\n" \
"s_waitcnt lgkmcnt(6)\n" \
"ds_read_b64_tr_b16 a[108:109], v233 offset:25600\n" \
"v_mfma_f32_16x16x32_bf16 v[104:107], a[84:87], a[12:15], v[104:107]\n" \
"buffer_load_dwordx4 v226, s[48:51], s77 offen offset:2048 lds\n" \
"v_mfma_f32_16x16x32_bf16 v[100:103], a[72:75], a[8:11], v[100:103]\n" \
"ds_read_b64_tr_b16 a[110:111], v233 offset:27648\n" \
"v_mfma_f32_16x16x32_bf16 v[100:103], a[76:79], a[12:15], v[100:103]\n" \
"ds_read_b64_tr_b16 a[112:113], v232 offset:17920\n" \
"v_mfma_f32_16x16x32_bf16 v[96:99], a[64:67], a[8:11], v[96:99]\n" \
"ds_read_b64_tr_b16 a[114:115], v232 offset:19968\n" \
"Lpg3897_%=:\n" \
"s_cbranch_execz Lpg6052_%=\n" \
"v_mfma_f32_16x16x32_bf16 v[96:99], a[68:71], a[12:15], v[96:99]\n" \
"buffer_load_dwordx4 v227, s[48:51], s77 offen offset:3072 lds\n" \
"v_mfma_f32_16x16x32_bf16 v[128:131], a[64:67], a[16:19], v[128:131]\n" \
"ds_read_b64_tr_b16 a[116:117], v232 offset:26112\n" \
"v_mfma_f32_16x16x32_bf16 v[128:131], a[68:71], a[20:23], v[128:131]\n" \
"ds_read_b64_tr_b16 a[118:119], v232 offset:28160\n" \
"v_mfma_f32_16x16x32_bf16 v[132:135], a[72:75], a[16:19], v[132:135]\n" \
"s_xor_b32 s77, s77, 0x80\n" \
"ds_read_b64_tr_b16 a[120:121], v233 offset:17920\n" \
"v_mfma_f32_16x16x32_bf16 v[132:135], a[76:79], a[20:23], v[132:135]\n" \
"v_mfma_f32_16x16x32_bf16 v[136:139], a[80:83], a[16:19], v[136:139]\n" \
"ds_read_b64_tr_b16 a[122:123], v233 offset:19968\n" \
"v_mfma_f32_16x16x32_bf16 v[136:139], a[84:87], a[20:23], v[136:139]\n" \
"ds_read_b64_tr_b16 a[124:125], v233 offset:26112\n" \
"v_mfma_f32_16x16x32_bf16 v[140:143], a[88:91], a[16:19], v[140:143]\n" \
"ds_read_b64_tr_b16 a[126:127], v233 offset:28160\n" \
"v_mfma_f32_16x16x32_bf16 v[140:143], a[92:95], a[20:23], v[140:143]\n" \
"buffer_store_dwordx4 a[228:231], v234, s[56:59], s93 offen offset:1024 sc1\n" \
"v_mfma_f32_16x16x32_bf16 v[172:175], a[88:91], a[24:27], v[172:175]\n" \
"v_mfma_f32_16x16x32_bf16 v[172:175], a[92:95], a[28:31], v[172:175]\n" \
"v_mfma_f32_16x16x32_bf16 v[168:171], a[80:83], a[24:27], v[168:171]\n" \
"v_mfma_f32_16x16x32_bf16 v[168:171], a[84:87], a[28:31], v[168:171]\n" \
"v_mfma_f32_16x16x32_bf16 v[164:167], a[72:75], a[24:27], v[164:167]\n" \
"v_mfma_f32_16x16x32_bf16 v[164:167], a[76:79], a[28:31], v[164:167]\n" \
"v_mfma_f32_16x16x32_bf16 v[160:163], a[64:67], a[24:27], v[160:163]\n" \
"v_mfma_f32_16x16x32_bf16 v[160:163], a[68:71], a[28:31], v[160:163]\n" \
"s_waitcnt vmcnt(35) lgkmcnt(0)\n" \
"s_barrier\n" \
"v_mfma_f32_16x16x32_bf16 v[80:83], a[96:99], a[0:3], v[80:83]\n" \
"ds_read_b64_tr_b16 a[64:65], v232 offset:32768\n" \
"v_mfma_f32_16x16x32_bf16 v[80:83], a[100:103], a[4:7], v[80:83]\n" \
"ds_read_b64_tr_b16 a[66:67], v232 offset:34816\n" \
"v_mfma_f32_16x16x32_bf16 v[84:87], a[104:107], a[0:3], v[84:87]\n" \
"s_add_u32 m0, s79, 16384\n" \
"ds_read_b64_tr_b16 a[68:69], v232 offset:40960\n" \
"v_mfma_f32_16x16x32_bf16 v[84:87], a[108:111], a[4:7], v[84:87]\n" \
"buffer_load_dwordx4 v228, s[52:55], s78 offen offset:0 lds\n" \
"v_mfma_f32_16x16x32_bf16 v[88:91], a[112:115], a[0:3], v[88:91]\n" \
"ds_read_b64_tr_b16 a[70:71], v232 offset:43008\n" \
"v_mfma_f32_16x16x32_bf16 v[88:91], a[116:119], a[4:7], v[88:91]\n" \
"ds_read_b64_tr_b16 a[72:73], v233 offset:32768\n" \
"v_mfma_f32_16x16x32_bf16 v[92:95], a[120:123], a[0:3], v[92:95]\n" \
"ds_read_b64_tr_b16 a[74:75], v233 offset:34816\n" \
"v_mfma_f32_16x16x32_bf16 v[92:95], a[124:127], a[4:7], v[92:95]\n" \
"buffer_load_dwordx4 v229, s[52:55], s78 offen offset:1024 lds\n" \
"v_mfma_f32_16x16x32_bf16 v[124:127], a[120:123], a[8:11], v[124:127]\n" \
"s_waitcnt vmcnt(35)\n" \
"ds_read_b128 a[0:3], v230 offset:0\n" \
"v_mfma_f32_16x16x32_bf16 v[124:127], a[124:127], a[12:15], v[124:127]\n" \
"ds_read_b128 a[4:7], v231 offset:0\n" \
"v_mfma_f32_16x16x32_bf16 v[120:123], a[112:115], a[8:11], v[120:123]\n" \
"s_xor_b32 s78, s78, 0x180000\n" \
"ds_read_b64_tr_b16 a[76:77], v233 offset:40960\n" \
"v_mfma_f32_16x16x32_bf16 v[120:123], a[116:119], a[12:15], v[120:123]\n" \
"buffer_load_dwordx4 v228, s[52:55], s89 offen offset:2048 lds\n" \
"v_mfma_f32_16x16x32_bf16 v[116:119], a[104:107], a[8:11], v[116:119]\n" \
"ds_read_b64_tr_b16 a[78:79], v233 offset:43008\n" \
"v_mfma_f32_16x16x32_bf16 v[116:119], a[108:111], a[12:15], v[116:119]\n" \
"ds_read_b64_tr_b16 a[80:81], v232 offset:33280\n" \
"v_mfma_f32_16x16x32_bf16 v[112:115], a[96:99], a[8:11], v[112:115]\n" \
"ds_read_b64_tr_b16 a[82:83], v232 offset:35328\n" \
"v_mfma_f32_16x16x32_bf16 v[112:115], a[100:103], a[12:15], v[112:115]\n" \
"buffer_load_dwordx4 v229, s[52:55], s89 offen offset:3072 lds\n" \
"v_mfma_f32_16x16x32_bf16 v[144:147], a[96:99], a[16:19], v[144:147]\n" \
"s_waitcnt vmcnt(35)\n" \
"s_xor_b32 s89, s89, 0x180000\n" \
"ds_read_b128 a[8:11], v230 offset:2048\n" \
"v_mfma_f32_16x16x32_bf16 v[144:147], a[100:103], a[20:23], v[144:147]\n" \
"s_add_u32 m0, s74, 65536\n" \
"ds_read_b128 a[12:15], v231 offset:2048\n" \
"v_mfma_f32_16x16x32_bf16 v[148:151], a[104:107], a[16:19], v[148:151]\n" \
"s_waitcnt lgkmcnt(6)\n" \
"ds_read_b64_tr_b16 a[84:85], v232 offset:41472\n" \
"v_mfma_f32_16x16x32_bf16 v[148:151], a[108:111], a[20:23], v[148:151]\n" \
"buffer_load_dwordx4 v224, s[48:51], s75 offen offset:0 lds\n" \
"v_mfma_f32_16x16x32_bf16 v[152:155], a[112:115], a[16:19], v[152:155]\n" \
"ds_read_b64_tr_b16 a[86:87], v232 offset:43520\n" \
"v_mfma_f32_16x16x32_bf16 v[152:155], a[116:119], a[20:23], v[152:155]\n" \
"ds_read_b64_tr_b16 a[88:89], v233 offset:33280\n" \
"v_mfma_f32_16x16x32_bf16 v[156:159], a[120:123], a[16:19], v[156:159]\n" \
"ds_read_b64_tr_b16 a[90:91], v233 offset:35328\n" \
"v_mfma_f32_16x16x32_bf16 v[156:159], a[124:127], a[20:23], v[156:159]\n" \
"buffer_load_dwordx4 v225, s[48:51], s75 offen offset:1024 lds\n" \
"v_mfma_f32_16x16x32_bf16 v[188:191], a[120:123], a[24:27], v[188:191]\n" \
"s_waitcnt vmcnt(35)\n" \
"ds_read_b128 a[16:19], v230 offset:4096\n" \
"v_mfma_f32_16x16x32_bf16 v[188:191], a[124:127], a[28:31], v[188:191]\n" \
"ds_read_b128 a[20:23], v231 offset:4096\n" \
"v_mfma_f32_16x16x32_bf16 v[184:187], a[112:115], a[24:27], v[184:187]\n" \
"s_waitcnt lgkmcnt(6)\n" \
"ds_read_b64_tr_b16 a[92:93], v233 offset:41472\n" \
"v_mfma_f32_16x16x32_bf16 v[184:187], a[116:119], a[28:31], v[184:187]\n" \
"buffer_load_dwordx4 v226, s[48:51], s75 offen offset:2048 lds\n" \
"v_mfma_f32_16x16x32_bf16 v[180:183], a[104:107], a[24:27], v[180:183]\n" \
"ds_read_b64_tr_b16 a[94:95], v233 offset:43520\n" \
"v_mfma_f32_16x16x32_bf16 v[180:183], a[108:111], a[28:31], v[180:183]\n" \
"v_mfma_f32_16x16x32_bf16 v[176:179], a[96:99], a[24:27], v[176:179]\n" \
"v_mfma_f32_16x16x32_bf16 v[176:179], a[100:103], a[28:31], v[176:179]\n" \
"buffer_load_dwordx4 v227, s[48:51], s75 offen offset:3072 lds\n" \
"v_mfma_f32_16x16x32_bf16 v[64:67], a[64:67], a[0:3], v[64:67]\n" \
"s_waitcnt vmcnt(35)\n" \
"s_xor_b32 s75, s75, 0x180\n" \
"ds_read_b128 a[24:27], v230 offset:6144\n" \
"v_mfma_f32_16x16x32_bf16 v[64:67], a[68:71], a[4:7], v[64:67]\n" \
"s_add_u32 m0, s74, 69632\n" \
"ds_read_b128 a[28:31], v231 offset:6144\n" \
"v_mfma_f32_16x16x32_bf16 v[68:71], a[72:75], a[0:3], v[68:71]\n" \
"s_waitcnt lgkmcnt(2)\n" \
"ds_read_b64_tr_b16 a[96:97], v232 offset:33792\n" \
"v_mfma_f32_16x16x32_bf16 v[68:71], a[76:79], a[4:7], v[68:71]\n" \
"buffer_load_dwordx4 v224, s[48:51], s77 offen offset:0 lds\n" \
"v_mfma_f32_16x16x32_bf16 v[72:75], a[80:83], a[0:3], v[72:75]\n" \
"ds_read_b64_tr_b16 a[98:99], v232 offset:35840\n" \
"v_mfma_f32_16x16x32_bf16 v[72:75], a[84:87], a[4:7], v[72:75]\n" \
"ds_read_b64_tr_b16 a[100:101], v232 offset:41984\n" \
"v_mfma_f32_16x16x32_bf16 v[76:79], a[88:91], a[0:3], v[76:79]\n" \
"ds_read_b64_tr_b16 a[102:103], v232 offset:44032\n" \
"v_mfma_f32_16x16x32_bf16 v[76:79], a[92:95], a[4:7], v[76:79]\n" \
"buffer_load_dwordx4 v225, s[48:51], s77 offen offset:1024 lds\n" \
"v_mfma_f32_16x16x32_bf16 v[108:111], a[88:91], a[8:11], v[108:111]\n" \
"ds_read_b64_tr_b16 a[104:105], v233 offset:33792\n" \
"v_mfma_f32_16x16x32_bf16 v[108:111], a[92:95], a[12:15], v[108:111]\n" \
"ds_read_b64_tr_b16 a[106:107], v233 offset:35840\n" \
"v_mfma_f32_16x16x32_bf16 v[104:107], a[80:83], a[8:11], v[104:107]\n" \
"s_waitcnt lgkmcnt(6)\n" \
"ds_read_b64_tr_b16 a[108:109], v233 offset:41984\n" \
"v_mfma_f32_16x16x32_bf16 v[104:107], a[84:87], a[12:15], v[104:107]\n" \
"buffer_load_dwordx4 v226, s[48:51], s77 offen offset:2048 lds\n" \
"v_mfma_f32_16x16x32_bf16 v[100:103], a[72:75], a[8:11], v[100:103]\n" \
"ds_read_b64_tr_b16 a[110:111], v233 offset:44032\n" \
"v_mfma_f32_16x16x32_bf16 v[100:103], a[76:79], a[12:15], v[100:103]\n" \
"ds_read_b64_tr_b16 a[112:113], v232 offset:34304\n" \
"v_mfma_f32_16x16x32_bf16 v[96:99], a[64:67], a[8:11], v[96:99]\n" \
"ds_read_b64_tr_b16 a[114:115], v232 offset:36352\n" \
"v_mfma_f32_16x16x32_bf16 v[96:99], a[68:71], a[12:15], v[96:99]\n" \
"buffer_load_dwordx4 v227, s[48:51], s77 offen offset:3072 lds\n" \
"v_mfma_f32_16x16x32_bf16 v[128:131], a[64:67], a[16:19], v[128:131]\n" \
"ds_read_b64_tr_b16 a[116:117], v232 offset:42496\n" \
"v_mfma_f32_16x16x32_bf16 v[128:131], a[68:71], a[20:23], v[128:131]\n" \
"ds_read_b64_tr_b16 a[118:119], v232 offset:44544\n" \
"v_mfma_f32_16x16x32_bf16 v[132:135], a[72:75], a[16:19], v[132:135]\n" \
"s_xor_b32 s77, s77, 0x180\n" \
"ds_read_b64_tr_b16 a[120:121], v233 offset:34304\n" \
"v_mfma_f32_16x16x32_bf16 v[132:135], a[76:79], a[20:23], v[132:135]\n" \
"buffer_store_dwordx4 a[232:235], v234, s[56:59], s93 offen offset:2048 sc1\n" \
"v_mfma_f32_16x16x32_bf16 v[136:139], a[80:83], a[16:19], v[136:139]\n" \
"ds_read_b64_tr_b16 a[122:123], v233 offset:36352\n" \
"v_mfma_f32_16x16x32_bf16 v[136:139], a[84:87], a[20:23], v[136:139]\n" \
"ds_read_b64_tr_b16 a[124:125], v233 offset:42496\n" \
"v_mfma_f32_16x16x32_bf16 v[140:143], a[88:91], a[16:19], v[140:143]\n" \
"ds_read_b64_tr_b16 a[126:127], v233 offset:44544\n" \
"v_mfma_f32_16x16x32_bf16 v[140:143], a[92:95], a[20:23], v[140:143]\n" \
"v_mfma_f32_16x16x32_bf16 v[172:175], a[88:91], a[24:27], v[172:175]\n" \
"v_mfma_f32_16x16x32_bf16 v[172:175], a[92:95], a[28:31], v[172:175]\n" \
"v_mfma_f32_16x16x32_bf16 v[168:171], a[80:83], a[24:27], v[168:171]\n" \
"v_mfma_f32_16x16x32_bf16 v[168:171], a[84:87], a[28:31], v[168:171]\n" \
"buffer_store_dwordx4 a[236:239], v234, s[56:59], s93 offen offset:3072 sc1\n" \
"v_mfma_f32_16x16x32_bf16 v[164:167], a[72:75], a[24:27], v[164:167]\n" \
"v_mfma_f32_16x16x32_bf16 v[164:167], a[76:79], a[28:31], v[164:167]\n" \
"v_mfma_f32_16x16x32_bf16 v[160:163], a[64:67], a[24:27], v[160:163]\n" \
"v_mfma_f32_16x16x32_bf16 v[160:163], a[68:71], a[28:31], v[160:163]\n" \
"s_waitcnt vmcnt(36) lgkmcnt(0)\n" \
"s_barrier\n" \
"v_mfma_f32_16x16x32_bf16 v[80:83], a[96:99], a[0:3], v[80:83]\n" \
"ds_read_b64_tr_b16 a[64:65], v232 offset:49152\n" \
"v_mfma_f32_16x16x32_bf16 v[80:83], a[100:103], a[4:7], v[80:83]\n" \
"ds_read_b64_tr_b16 a[66:67], v232 offset:51200\n" \
"v_mfma_f32_16x16x32_bf16 v[84:87], a[104:107], a[0:3], v[84:87]\n" \
"s_add_u32 m0, s79, 32768\n" \
"ds_read_b64_tr_b16 a[68:69], v232 offset:57344\n" \
"v_mfma_f32_16x16x32_bf16 v[84:87], a[108:111], a[4:7], v[84:87]\n" \
"buffer_load_dwordx4 v228, s[52:55], s78 offen offset:0 lds\n" \
"v_mfma_f32_16x16x32_bf16 v[88:91], a[112:115], a[0:3], v[88:91]\n" \
"ds_read_b64_tr_b16 a[70:71], v232 offset:59392\n" \
"v_mfma_f32_16x16x32_bf16 v[88:91], a[116:119], a[4:7], v[88:91]\n" \
"ds_read_b64_tr_b16 a[72:73], v233 offset:49152\n" \
"v_mfma_f32_16x16x32_bf16 v[92:95], a[120:123], a[0:3], v[92:95]\n" \
"ds_read_b64_tr_b16 a[74:75], v233 offset:51200\n" \
"v_mfma_f32_16x16x32_bf16 v[92:95], a[124:127], a[4:7], v[92:95]\n" \
"buffer_load_dwordx4 v229, s[52:55], s78 offen offset:1024 lds\n" \
"v_mfma_f32_16x16x32_bf16 v[124:127], a[120:123], a[8:11], v[124:127]\n" \
"s_waitcnt vmcnt(36)\n" \
"ds_read_b128 a[0:3], v230 offset:32768\n" \
"v_mfma_f32_16x16x32_bf16 v[124:127], a[124:127], a[12:15], v[124:127]\n" \
"ds_read_b128 a[4:7], v231 offset:32768\n" \
"v_mfma_f32_16x16x32_bf16 v[120:123], a[112:115], a[8:11], v[120:123]\n" \
"s_xor_b32 s78, s78, 0x80000\n" \
"ds_read_b64_tr_b16 a[76:77], v233 offset:57344\n" \
"v_mfma_f32_16x16x32_bf16 v[120:123], a[116:119], a[12:15], v[120:123]\n" \
"buffer_load_dwordx4 v228, s[52:55], s89 offen offset:2048 lds\n" \
"v_mfma_f32_16x16x32_bf16 v[116:119], a[104:107], a[8:11], v[116:119]\n" \
"ds_read_b64_tr_b16 a[78:79], v233 offset:59392\n" \
"v_mfma_f32_16x16x32_bf16 v[116:119], a[108:111], a[12:15], v[116:119]\n" \
"ds_read_b64_tr_b16 a[80:81], v232 offset:49664\n" \
"v_mfma_f32_16x16x32_bf16 v[112:115], a[96:99], a[8:11], v[112:115]\n" \
"ds_read_b64_tr_b16 a[82:83], v232 offset:51712\n" \
"v_mfma_f32_16x16x32_bf16 v[112:115], a[100:103], a[12:15], v[112:115]\n" \
"buffer_load_dwordx4 v229, s[52:55], s89 offen offset:3072 lds\n" \
"v_mfma_f32_16x16x32_bf16 v[144:147], a[96:99], a[16:19], v[144:147]\n" \
"s_waitcnt vmcnt(36)\n" \
"s_xor_b32 s89, s89, 0x80000\n" \
"ds_read_b128 a[8:11], v230 offset:34816\n" \
"v_mfma_f32_16x16x32_bf16 v[144:147], a[100:103], a[20:23], v[144:147]\n" \
"s_add_u32 m0, s74, 98304\n" \
"ds_read_b128 a[12:15], v231 offset:34816\n" \
"v_mfma_f32_16x16x32_bf16 v[148:151], a[104:107], a[16:19], v[148:151]\n" \
"s_waitcnt lgkmcnt(6)\n" \
"ds_read_b64_tr_b16 a[84:85], v232 offset:57856\n" \
"v_mfma_f32_16x16x32_bf16 v[148:151], a[108:111], a[20:23], v[148:151]\n" \
"buffer_load_dwordx4 v224, s[48:51], s75 offen offset:0 lds\n" \
"v_mfma_f32_16x16x32_bf16 v[152:155], a[112:115], a[16:19], v[152:155]\n" \
"ds_read_b64_tr_b16 a[86:87], v232 offset:59904\n" \
"v_mfma_f32_16x16x32_bf16 v[152:155], a[116:119], a[20:23], v[152:155]\n" \
"ds_read_b64_tr_b16 a[88:89], v233 offset:49664\n" \
"v_mfma_f32_16x16x32_bf16 v[156:159], a[120:123], a[16:19], v[156:159]\n" \
"ds_read_b64_tr_b16 a[90:91], v233 offset:51712\n" \
"v_mfma_f32_16x16x32_bf16 v[156:159], a[124:127], a[20:23], v[156:159]\n" \
"buffer_load_dwordx4 v225, s[48:51], s75 offen offset:1024 lds\n" \
"v_mfma_f32_16x16x32_bf16 v[188:191], a[120:123], a[24:27], v[188:191]\n" \
"s_waitcnt vmcnt(36)\n" \
"ds_read_b128 a[16:19], v230 offset:36864\n" \
"v_mfma_f32_16x16x32_bf16 v[188:191], a[124:127], a[28:31], v[188:191]\n" \
"ds_read_b128 a[20:23], v231 offset:36864\n" \
"v_mfma_f32_16x16x32_bf16 v[184:187], a[112:115], a[24:27], v[184:187]\n" \
"s_waitcnt lgkmcnt(6)\n" \
"ds_read_b64_tr_b16 a[92:93], v233 offset:57856\n" \
"v_mfma_f32_16x16x32_bf16 v[184:187], a[116:119], a[28:31], v[184:187]\n" \
"buffer_load_dwordx4 v226, s[48:51], s75 offen offset:2048 lds\n" \
"v_mfma_f32_16x16x32_bf16 v[180:183], a[104:107], a[24:27], v[180:183]\n" \
"ds_read_b64_tr_b16 a[94:95], v233 offset:59904\n" \
"v_mfma_f32_16x16x32_bf16 v[180:183], a[108:111], a[28:31], v[180:183]\n" \
"v_mfma_f32_16x16x32_bf16 v[176:179], a[96:99], a[24:27], v[176:179]\n" \
"v_mfma_f32_16x16x32_bf16 v[176:179], a[100:103], a[28:31], v[176:179]\n" \
"buffer_load_dwordx4 v227, s[48:51], s75 offen offset:3072 lds\n" \
"v_mfma_f32_16x16x32_bf16 v[64:67], a[64:67], a[0:3], v[64:67]\n" \
"s_waitcnt vmcnt(36)\n" \
"s_xor_b32 s75, s75, 0x80\n" \
"ds_read_b128 a[24:27], v230 offset:38912\n" \
"v_mfma_f32_16x16x32_bf16 v[64:67], a[68:71], a[4:7], v[64:67]\n" \
"s_add_u32 m0, s74, 102400\n" \
"ds_read_b128 a[28:31], v231 offset:38912\n" \
"v_mfma_f32_16x16x32_bf16 v[68:71], a[72:75], a[0:3], v[68:71]\n" \
"s_waitcnt lgkmcnt(2)\n" \
"ds_read_b64_tr_b16 a[96:97], v232 offset:50176\n" \
"v_mfma_f32_16x16x32_bf16 v[68:71], a[76:79], a[4:7], v[68:71]\n" \
"buffer_load_dwordx4 v224, s[48:51], s77 offen offset:0 lds\n" \
"v_mfma_f32_16x16x32_bf16 v[72:75], a[80:83], a[0:3], v[72:75]\n" \
"ds_read_b64_tr_b16 a[98:99], v232 offset:52224\n" \
"v_mfma_f32_16x16x32_bf16 v[72:75], a[84:87], a[4:7], v[72:75]\n" \
"ds_read_b64_tr_b16 a[100:101], v232 offset:58368\n" \
"v_mfma_f32_16x16x32_bf16 v[76:79], a[88:91], a[0:3], v[76:79]\n" \
"ds_read_b64_tr_b16 a[102:103], v232 offset:60416\n" \
"v_mfma_f32_16x16x32_bf16 v[76:79], a[92:95], a[4:7], v[76:79]\n" \
"buffer_load_dwordx4 v225, s[48:51], s77 offen offset:1024 lds\n" \
"v_mfma_f32_16x16x32_bf16 v[108:111], a[88:91], a[8:11], v[108:111]\n" \
"ds_read_b64_tr_b16 a[104:105], v233 offset:50176\n" \
"v_mfma_f32_16x16x32_bf16 v[108:111], a[92:95], a[12:15], v[108:111]\n" \
"ds_read_b64_tr_b16 a[106:107], v233 offset:52224\n" \
"v_mfma_f32_16x16x32_bf16 v[104:107], a[80:83], a[8:11], v[104:107]\n" \
"s_waitcnt lgkmcnt(6)\n" \
"ds_read_b64_tr_b16 a[108:109], v233 offset:58368\n" \
"v_mfma_f32_16x16x32_bf16 v[104:107], a[84:87], a[12:15], v[104:107]\n" \
"buffer_load_dwordx4 v226, s[48:51], s77 offen offset:2048 lds\n" \
"v_mfma_f32_16x16x32_bf16 v[100:103], a[72:75], a[8:11], v[100:103]\n" \
"ds_read_b64_tr_b16 a[110:111], v233 offset:60416\n" \
"v_mfma_f32_16x16x32_bf16 v[100:103], a[76:79], a[12:15], v[100:103]\n" \
"ds_read_b64_tr_b16 a[112:113], v232 offset:50688\n" \
"v_mfma_f32_16x16x32_bf16 v[96:99], a[64:67], a[8:11], v[96:99]\n" \
"ds_read_b64_tr_b16 a[114:115], v232 offset:52736\n" \
"v_mfma_f32_16x16x32_bf16 v[96:99], a[68:71], a[12:15], v[96:99]\n" \
"buffer_load_dwordx4 v227, s[48:51], s77 offen offset:3072 lds\n" \
"v_mfma_f32_16x16x32_bf16 v[128:131], a[64:67], a[16:19], v[128:131]\n" \
"ds_read_b64_tr_b16 a[116:117], v232 offset:58880\n" \
"v_mfma_f32_16x16x32_bf16 v[128:131], a[68:71], a[20:23], v[128:131]\n" \
"ds_read_b64_tr_b16 a[118:119], v232 offset:60928\n" \
"v_mfma_f32_16x16x32_bf16 v[132:135], a[72:75], a[16:19], v[132:135]\n" \
"s_xor_b32 s77, s77, 0x80\n" \
"s_add_u32 s93, s87, 17850368\n" \
"ds_read_b64_tr_b16 a[120:121], v233 offset:50688\n" \
"v_mfma_f32_16x16x32_bf16 v[132:135], a[76:79], a[20:23], v[132:135]\n" \
"buffer_store_dwordx4 a[192:195], v234, s[56:59], s93 offen offset:0 sc1\n" \
"v_mfma_f32_16x16x32_bf16 v[136:139], a[80:83], a[16:19], v[136:139]\n" \
"ds_read_b64_tr_b16 a[122:123], v233 offset:52736\n" \
"v_mfma_f32_16x16x32_bf16 v[136:139], a[84:87], a[20:23], v[136:139]\n" \
"ds_read_b64_tr_b16 a[124:125], v233 offset:58880\n" \
"v_mfma_f32_16x16x32_bf16 v[140:143], a[88:91], a[16:19], v[140:143]\n" \
"ds_read_b64_tr_b16 a[126:127], v233 offset:60928\n" \
"v_mfma_f32_16x16x32_bf16 v[140:143], a[92:95], a[20:23], v[140:143]\n" \
"v_mfma_f32_16x16x32_bf16 v[172:175], a[88:91], a[24:27], v[172:175]\n" \
"v_mfma_f32_16x16x32_bf16 v[172:175], a[92:95], a[28:31], v[172:175]\n" \
"v_mfma_f32_16x16x32_bf16 v[168:171], a[80:83], a[24:27], v[168:171]\n" \
"v_mfma_f32_16x16x32_bf16 v[168:171], a[84:87], a[28:31], v[168:171]\n" \
"buffer_store_dwordx4 a[196:199], v234, s[56:59], s93 offen offset:1024 sc1\n" \
"v_mfma_f32_16x16x32_bf16 v[164:167], a[72:75], a[24:27], v[164:167]\n" \
"v_mfma_f32_16x16x32_bf16 v[164:167], a[76:79], a[28:31], v[164:167]\n" \
"v_mfma_f32_16x16x32_bf16 v[160:163], a[64:67], a[24:27], v[160:163]\n" \
"v_mfma_f32_16x16x32_bf16 v[160:163], a[68:71], a[28:31], v[160:163]\n" \
"s_waitcnt vmcnt(37) lgkmcnt(0)\n" \
"s_barrier\n" \
"v_mfma_f32_16x16x32_bf16 v[80:83], a[96:99], a[0:3], v[80:83]\n" \
"ds_read_b64_tr_b16 a[64:65], v232 offset:0\n" \
"v_mfma_f32_16x16x32_bf16 v[80:83], a[100:103], a[4:7], v[80:83]\n" \
"ds_read_b64_tr_b16 a[66:67], v232 offset:2048\n" \
"v_mfma_f32_16x16x32_bf16 v[84:87], a[104:107], a[0:3], v[84:87]\n" \
"s_add_u32 m0, s79, 49152\n" \
"ds_read_b64_tr_b16 a[68:69], v232 offset:8192\n" \
"v_mfma_f32_16x16x32_bf16 v[84:87], a[108:111], a[4:7], v[84:87]\n" \
"buffer_load_dwordx4 v228, s[52:55], s78 offen offset:0 lds\n" \
"v_mfma_f32_16x16x32_bf16 v[88:91], a[112:115], a[0:3], v[88:91]\n" \
"s_mul_i32 s73, s62, 557824\n" \
"s_add_u32 s85, s73, 17850368\n" \
"ds_read_b64_tr_b16 a[70:71], v232 offset:10240\n" \
"v_mfma_f32_16x16x32_bf16 v[88:91], a[116:119], a[4:7], v[88:91]\n" \
"s_mul_i32 s83, s63, 34816\n" \
"s_sub_u32 s86, 557056, s83\n" \
"ds_read_b64_tr_b16 a[72:73], v233 offset:0\n" \
"v_mfma_f32_16x16x32_bf16 v[92:95], a[120:123], a[0:3], v[92:95]\n" \
"ds_read_b64_tr_b16 a[74:75], v233 offset:2048\n" \
"v_add_u32 v237, %3, v236\n" \
"v_mfma_f32_16x16x32_bf16 v[92:95], a[124:127], a[4:7], v[92:95]\n" \
"buffer_load_dwordx4 v229, s[52:55], s78 offen offset:1024 lds\n" \
"v_mfma_f32_16x16x32_bf16 v[124:127], a[120:123], a[8:11], v[124:127]\n" \
"s_waitcnt vmcnt(37)\n" \
"ds_read_b128 a[0:3], v250 offset:0\n" \
"v_mfma_f32_16x16x32_bf16 v[124:127], a[124:127], a[12:15], v[124:127]\n" \
"s_add_u32 s36, s56, s73\n" \
"s_addc_u32 s37, s57, 0\n" \
"ds_read_b128 a[4:7], v251 offset:0\n" \
"v_mfma_f32_16x16x32_bf16 v[120:123], a[112:115], a[8:11], v[120:123]\n" \
"s_mov_b32 s38, s86\n" \
"s_mov_b32 s39, 0x20000\n" \
"ds_read_b64_tr_b16 a[76:77], v233 offset:8192\n" \
"v_mfma_f32_16x16x32_bf16 v[120:123], a[116:119], a[12:15], v[120:123]\n" \
"buffer_load_dwordx4 v228, s[52:55], s89 offen offset:2048 lds\n" \
"v_mfma_f32_16x16x32_bf16 v[116:119], a[104:107], a[8:11], v[116:119]\n" \
"ds_read_b64_tr_b16 a[78:79], v233 offset:10240\n" \
"v_add_u32 v235, %3, v234\n" \
"v_mfma_f32_16x16x32_bf16 v[116:119], a[108:111], a[12:15], v[116:119]\n" \
"s_add_u32 s40, s56, s85\n" \
"s_addc_u32 s41, s57, 0\n" \
"ds_read_b64_tr_b16 a[80:81], v232 offset:512\n" \
"v_mfma_f32_16x16x32_bf16 v[112:115], a[96:99], a[8:11], v[112:115]\n" \
"s_mov_b32 s42, s86\n" \
"s_mov_b32 s43, 0x20000\n" \
"ds_read_b64_tr_b16 a[82:83], v232 offset:2560\n" \
"v_mfma_f32_16x16x32_bf16 v[112:115], a[100:103], a[12:15], v[112:115]\n" \
"buffer_load_dwordx4 v229, s[52:55], s89 offen offset:3072 lds\n" \
"v_mfma_f32_16x16x32_bf16 v[144:147], a[96:99], a[16:19], v[144:147]\n" \
"s_waitcnt vmcnt(37)\n" \
"s_add_u32 s85, s74, 131072\n" \
"ds_read_b128 a[8:11], v250 offset:2048\n" \
"v_mfma_f32_16x16x32_bf16 v[144:147], a[100:103], a[20:23], v[144:147]\n" \
"s_add_u32 m0, s74, 131072\n" \
"ds_read_b128 a[12:15], v251 offset:2048\n" \
"v_add_u32 v239, s85, v234\n" \
"v_mfma_f32_16x16x32_bf16 v[148:151], a[104:107], a[16:19], v[148:151]\n" \
"s_waitcnt lgkmcnt(6)\n" \
"s_lshl_b32 s73, s61, 12\n" \
"ds_read_b64_tr_b16 a[84:85], v232 offset:8704\n" \
"v_mfma_f32_16x16x32_bf16 v[148:151], a[108:111], a[20:23], v[148:151]\n" \
"buffer_load_dwordx4 v224, s[48:51], s75 offen offset:0 lds\n" \
"v_mfma_f32_16x16x32_bf16 v[152:155], a[112:115], a[16:19], v[152:155]\n" \
"s_add_u32 s80, s79, 0x10000\n" \
"s_add_u32 s83, s83, s73\n" \
"ds_read_b64_tr_b16 a[86:87], v232 offset:10752\n" \
"v_mfma_f32_16x16x32_bf16 v[152:155], a[116:119], a[20:23], v[152:155]\n" \
"s_cmp_eq_u32 s63, 0\n" \
"s_cselect_b32 s85, 34816, 0\n" \
"ds_read_b64_tr_b16 a[88:89], v233 offset:512\n" \
"v_mfma_f32_16x16x32_bf16 v[156:159], a[120:123], a[16:19], v[156:159]\n" \
"s_add_u32 s73, s73, s85\n" \
"ds_read_b64_tr_b16 a[90:91], v233 offset:2560\n" \
"v_mfma_f32_16x16x32_bf16 v[156:159], a[124:127], a[20:23], v[156:159]\n" \
"buffer_load_dwordx4 v225, s[48:51], s75 offen offset:1024 lds\n" \
"v_mfma_f32_16x16x32_bf16 v[188:191], a[120:123], a[24:27], v[188:191]\n" \
"s_waitcnt vmcnt(37)\n" \
"ds_read_b128 a[16:19], v250 offset:4096\n" \
"v_mfma_f32_16x16x32_bf16 v[188:191], a[124:127], a[28:31], v[188:191]\n" \
"ds_read_b128 a[20:23], v251 offset:4096\n" \
"v_mfma_f32_16x16x32_bf16 v[184:187], a[112:115], a[24:27], v[184:187]\n" \
"s_waitcnt lgkmcnt(6)\n" \
"ds_read_b64_tr_b16 a[92:93], v233 offset:8704\n" \
"v_mfma_f32_16x16x32_bf16 v[184:187], a[116:119], a[28:31], v[184:187]\n" \
"buffer_load_dwordx4 v226, s[48:51], s75 offen offset:2048 lds\n" \
"v_mfma_f32_16x16x32_bf16 v[180:183], a[104:107], a[24:27], v[180:183]\n" \
"ds_read_b64_tr_b16 a[94:95], v233 offset:10752\n" \
"v_mfma_f32_16x16x32_bf16 v[180:183], a[108:111], a[28:31], v[180:183]\n" \
"v_mfma_f32_16x16x32_bf16 v[176:179], a[96:99], a[24:27], v[176:179]\n" \
"v_mfma_f32_16x16x32_bf16 v[176:179], a[100:103], a[28:31], v[176:179]\n" \
"buffer_load_dwordx4 v227, s[48:51], s75 offen offset:3072 lds\n" \
"v_mfma_f32_16x16x32_bf16 v[64:67], a[64:67], a[0:3], v[64:67]\n" \
"s_waitcnt vmcnt(37)\n" \
"ds_read_b128 a[24:27], v250 offset:6144\n" \
"v_mfma_f32_16x16x32_bf16 v[64:67], a[68:71], a[4:7], v[64:67]\n" \
"s_add_u32 m0, s74, 135168\n" \
"ds_read_b128 a[28:31], v251 offset:6144\n" \
"v_mfma_f32_16x16x32_bf16 v[68:71], a[72:75], a[0:3], v[68:71]\n" \
"s_waitcnt lgkmcnt(2)\n" \
"ds_read_b64_tr_b16 a[96:97], v232 offset:1024\n" \
"v_mfma_f32_16x16x32_bf16 v[68:71], a[76:79], a[4:7], v[68:71]\n" \
"buffer_load_dwordx4 v224, s[48:51], s77 offen offset:0 lds\n" \
"v_mfma_f32_16x16x32_bf16 v[72:75], a[80:83], a[0:3], v[72:75]\n" \
"ds_read_b64_tr_b16 a[98:99], v232 offset:3072\n" \
"v_mfma_f32_16x16x32_bf16 v[72:75], a[84:87], a[4:7], v[72:75]\n" \
"ds_read_b64_tr_b16 a[100:101], v232 offset:9216\n" \
"v_mfma_f32_16x16x32_bf16 v[76:79], a[88:91], a[0:3], v[76:79]\n" \
"ds_read_b64_tr_b16 a[102:103], v232 offset:11264\n" \
"v_mfma_f32_16x16x32_bf16 v[76:79], a[92:95], a[4:7], v[76:79]\n" \
"buffer_load_dwordx4 v225, s[48:51], s77 offen offset:1024 lds\n" \
"v_mfma_f32_16x16x32_bf16 v[108:111], a[88:91], a[8:11], v[108:111]\n" \
"ds_read_b64_tr_b16 a[104:105], v233 offset:1024\n" \
"v_mfma_f32_16x16x32_bf16 v[108:111], a[92:95], a[12:15], v[108:111]\n" \
"ds_read_b64_tr_b16 a[106:107], v233 offset:3072\n" \
"v_mfma_f32_16x16x32_bf16 v[104:107], a[80:83], a[8:11], v[104:107]\n" \
"s_waitcnt lgkmcnt(6)\n" \
"ds_read_b64_tr_b16 a[108:109], v233 offset:9216\n" \
"v_mfma_f32_16x16x32_bf16 v[104:107], a[84:87], a[12:15], v[104:107]\n" \
"buffer_load_dwordx4 v226, s[48:51], s77 offen offset:2048 lds\n" \
"v_mfma_f32_16x16x32_bf16 v[100:103], a[72:75], a[8:11], v[100:103]\n" \
"ds_read_b64_tr_b16 a[110:111], v233 offset:11264\n" \
"v_mfma_f32_16x16x32_bf16 v[100:103], a[76:79], a[12:15], v[100:103]\n" \
"ds_read_b64_tr_b16 a[112:113], v232 offset:1536\n" \
"v_mfma_f32_16x16x32_bf16 v[96:99], a[64:67], a[8:11], v[96:99]\n" \
"ds_read_b64_tr_b16 a[114:115], v232 offset:3584\n" \
"v_mfma_f32_16x16x32_bf16 v[96:99], a[68:71], a[12:15], v[96:99]\n" \
"buffer_load_dwordx4 v227, s[48:51], s77 offen offset:3072 lds\n" \
"v_mfma_f32_16x16x32_bf16 v[128:131], a[64:67], a[16:19], v[128:131]\n" \
"ds_read_b64_tr_b16 a[116:117], v232 offset:9728\n" \
"v_mfma_f32_16x16x32_bf16 v[128:131], a[68:71], a[20:23], v[128:131]\n" \
"ds_read_b64_tr_b16 a[118:119], v232 offset:11776\n" \
"v_mfma_f32_16x16x32_bf16 v[132:135], a[72:75], a[16:19], v[132:135]\n" \
"ds_read_b64_tr_b16 a[120:121], v233 offset:1536\n" \
"v_mfma_f32_16x16x32_bf16 v[132:135], a[76:79], a[20:23], v[132:135]\n" \
"buffer_store_dwordx4 a[200:203], v234, s[56:59], s93 offen offset:2048 sc1\n" \
"v_mfma_f32_16x16x32_bf16 v[136:139], a[80:83], a[16:19], v[136:139]\n" \
"ds_read_b64_tr_b16 a[122:123], v233 offset:3584\n" \
"v_mfma_f32_16x16x32_bf16 v[136:139], a[84:87], a[20:23], v[136:139]\n" \
"ds_read_b64_tr_b16 a[124:125], v233 offset:9728\n" \
"v_mfma_f32_16x16x32_bf16 v[140:143], a[88:91], a[16:19], v[140:143]\n" \
"ds_read_b64_tr_b16 a[126:127], v233 offset:11776\n" \
"v_mfma_f32_16x16x32_bf16 v[140:143], a[92:95], a[20:23], v[140:143]\n" \
"v_mfma_f32_16x16x32_bf16 v[172:175], a[88:91], a[24:27], v[172:175]\n" \
"v_mfma_f32_16x16x32_bf16 v[172:175], a[92:95], a[28:31], v[172:175]\n" \
"v_mfma_f32_16x16x32_bf16 v[168:171], a[80:83], a[24:27], v[168:171]\n" \
"v_mfma_f32_16x16x32_bf16 v[168:171], a[84:87], a[28:31], v[168:171]\n" \
"buffer_store_dwordx4 a[204:207], v234, s[56:59], s93 offen offset:3072 sc1\n" \
"v_mfma_f32_16x16x32_bf16 v[164:167], a[72:75], a[24:27], v[164:167]\n" \
"v_mfma_f32_16x16x32_bf16 v[164:167], a[76:79], a[28:31], v[164:167]\n" \
"v_mfma_f32_16x16x32_bf16 v[160:163], a[64:67], a[24:27], v[160:163]\n" \
"v_mfma_f32_16x16x32_bf16 v[160:163], a[68:71], a[28:31], v[160:163]\n" \
"s_waitcnt vmcnt(38) lgkmcnt(0)\n" \
"s_barrier\n" \
"v_mfma_f32_16x16x32_bf16 v[80:83], a[96:99], a[0:3], v[80:83]\n" \
"s_lshr_b32 s71, s61, 1\n" \
"s_lshl_b32 s71, s71, 14\n" \
"ds_read_b64_tr_b16 a[64:65], v232 offset:16384\n" \
"v_mfma_f32_16x16x32_bf16 v[80:83], a[100:103], a[4:7], v[80:83]\n" \
"s_xor_b32 s71, s71, 0x4000\n" \
"s_and_b32 s70, s61, 1\n" \
"ds_read_b64_tr_b16 a[66:67], v232 offset:18432\n" \
"v_mfma_f32_16x16x32_bf16 v[84:87], a[104:107], a[0:3], v[84:87]\n" \
"s_lshl_b32 s70, s70, 12\n" \
"s_add_u32 s71, s71, s70\n" \
"ds_read_b64_tr_b16 a[68:69], v232 offset:24576\n" \
"v_mfma_f32_16x16x32_bf16 v[84:87], a[108:111], a[4:7], v[84:87]\n" \
"v_add_u32 v224, s71, v237\n" \
"s_and_b32 s70, s63, 1\n" \
"v_mfma_f32_16x16x32_bf16 v[88:91], a[112:115], a[0:3], v[88:91]\n" \
"s_lshl_b32 s70, s70, 15\n" \
"s_xor_b32 s70, s70, 0x18000\n" \
"ds_read_b64_tr_b16 a[70:71], v232 offset:26624\n" \
"v_mfma_f32_16x16x32_bf16 v[88:91], a[116:119], a[4:7], v[88:91]\n" \
"s_add_u32 s71, s71, s70\n" \
"ds_read_b64_tr_b16 a[72:73], v233 offset:16384\n" \
"v_add_u32 v225, s71, v235\n" \
"v_mfma_f32_16x16x32_bf16 v[92:95], a[120:123], a[0:3], v[92:95]\n" \
"s_lshr_b32 s64, s22, 7\n" \
"s_sub_u32 s64, 3, s64\n" \
"ds_read_b64_tr_b16 a[74:75], v233 offset:18432\n" \
"v_mfma_f32_16x16x32_bf16 v[92:95], a[124:127], a[4:7], v[92:95]\n" \
"s_and_b32 s64, s64, 7\n" \
"s_and_b32 s65, %1, 1\n" \
"s_lshl_b32 s65, s65, 3\n" \
"v_mfma_f32_16x16x32_bf16 v[124:127], a[120:123], a[8:11], v[124:127]\n" \
"s_waitcnt vmcnt(36)\n" \
"s_add_u32 s64, s64, s65\n" \
"ds_read_b128 a[0:3], v230 offset:0\n" \
"v_mfma_f32_16x16x32_bf16 v[124:127], a[124:127], a[12:15], v[124:127]\n" \
"s_lshl_b32 s64, s64, 7\n" \
"s_lshr_b32 s65, %1, 3\n" \
"ds_read_b128 a[4:7], v231 offset:0\n" \
"v_mfma_f32_16x16x32_bf16 v[120:123], a[112:115], a[8:11], v[120:123]\n" \
"s_lshl_b32 s65, s65, 2\n" \
"s_add_u32 s65, s65, s61\n" \
"ds_read_b64_tr_b16 a[76:77], v233 offset:24576\n" \
"v_mfma_f32_16x16x32_bf16 v[120:123], a[116:119], a[12:15], v[120:123]\n" \
"s_mul_i32 s65, s65, 24576\n" \
"s_add_u32 s64, s64, s65\n" \
"v_bfe_u32 v254, v234, 7, 2\n" \
"v_mfma_f32_16x16x32_bf16 v[116:119], a[104:107], a[8:11], v[116:119]\n" \
"ds_read_b64_tr_b16 a[78:79], v233 offset:26624\n" \
"v_mul_u32_u24 v254, 3, v254\n" \
"v_mfma_f32_16x16x32_bf16 v[116:119], a[108:111], a[12:15], v[116:119]\n" \
"ds_read_b64_tr_b16 a[80:81], v232 offset:16896\n" \
"v_bfe_u32 v243, v234, 5, 2\n" \
"v_mfma_f32_16x16x32_bf16 v[112:115], a[96:99], a[8:11], v[112:115]\n" \
"s_add_u32 s93, s94, 8192\n" \
"ds_read_b64_tr_b16 a[82:83], v232 offset:18944\n" \
"v_min_u32 v243, 2, v243\n" \
"v_mfma_f32_16x16x32_bf16 v[112:115], a[100:103], a[12:15], v[112:115]\n" \
"buffer_store_dwordx4 v[32:35], v236, s[56:59], s93 offen offset:0 sc1\n" \
"v_mfma_f32_16x16x32_bf16 v[144:147], a[96:99], a[16:19], v[144:147]\n" \
"s_waitcnt vmcnt(35)\n" \
"ds_read_b128 a[8:11], v230 offset:2048\n" \
"v_add_u32 v254, v254, v243\n" \
"v_mfma_f32_16x16x32_bf16 v[144:147], a[100:103], a[20:23], v[144:147]\n" \
"ds_read_b128 a[12:15], v231 offset:2048\n" \
"v_and_b32 v243, 16, v234\n" \
"v_mfma_f32_16x16x32_bf16 v[148:151], a[104:107], a[16:19], v[148:151]\n" \
"ds_read_b64_tr_b16 a[84:85], v232 offset:25088\n" \
"v_lshl_add_u32 v243, v243, 2, s64\n" \
"v_mfma_f32_16x16x32_bf16 v[148:151], a[108:111], a[20:23], v[148:151]\n" \
"v_lshl_add_u32 v254, v254, 11, v243\n" \
"v_mfma_f32_16x16x32_bf16 v[152:155], a[112:115], a[16:19], v[152:155]\n" \
"ds_read_b64_tr_b16 a[86:87], v232 offset:27136\n" \
"v_mfma_f32_16x16x32_bf16 v[152:155], a[116:119], a[20:23], v[152:155]\n" \
"ds_read_b64_tr_b16 a[88:89], v233 offset:16896\n" \
"v_mfma_f32_16x16x32_bf16 v[156:159], a[120:123], a[16:19], v[156:159]\n" \
"ds_read_b64_tr_b16 a[90:91], v233 offset:18944\n" \
"v_mfma_f32_16x16x32_bf16 v[156:159], a[124:127], a[20:23], v[156:159]\n" \
"v_mfma_f32_16x16x32_bf16 v[188:191], a[120:123], a[24:27], v[188:191]\n" \
"s_waitcnt vmcnt(33)\n" \
"ds_read_b128 a[16:19], v230 offset:4096\n" \
"v_mfma_f32_16x16x32_bf16 v[188:191], a[124:127], a[28:31], v[188:191]\n" \
"ds_read_b128 a[20:23], v231 offset:4096\n" \
"v_mfma_f32_16x16x32_bf16 v[184:187], a[112:115], a[24:27], v[184:187]\n" \
"ds_read_b64_tr_b16 a[92:93], v233 offset:25088\n" \
"v_mfma_f32_16x16x32_bf16 v[184:187], a[116:119], a[28:31], v[184:187]\n" \
"v_mfma_f32_16x16x32_bf16 v[180:183], a[104:107], a[24:27], v[180:183]\n" \
"ds_read_b64_tr_b16 a[94:95], v233 offset:27136\n" \
"v_mfma_f32_16x16x32_bf16 v[180:183], a[108:111], a[28:31], v[180:183]\n" \
"v_mfma_f32_16x16x32_bf16 v[176:179], a[96:99], a[24:27], v[176:179]\n" \
"buffer_store_dwordx4 v[48:51], v236, s[56:59], s93 offen offset:256 sc1\n" \
"v_mfma_f32_16x16x32_bf16 v[176:179], a[100:103], a[28:31], v[176:179]\n" \
"s_waitcnt lgkmcnt(1)\n" \
"v_mfma_f32_16x16x32_bf16 v[64:67], a[64:67], a[0:3], v[64:67]\n" \
"s_waitcnt vmcnt(32)\n" \
"s_lshl_b32 s68, s63, 2\n" \
"ds_read_b128 a[24:27], v230 offset:6144\n" \
"Lpg4447_%=:\n" \
"s_cbranch_execz Lpg6183_%=\n" \
"v_mfma_f32_16x16x32_bf16 v[64:67], a[68:71], a[4:7], v[64:67]\n" \
"s_add_u32 s68, s68, s90\n" \
"ds_read_b128 a[28:31], v231 offset:6144\n" \
"v_mfma_f32_16x16x32_bf16 v[68:71], a[72:75], a[0:3], v[68:71]\n" \
"ds_read_b64_tr_b16 a[96:97], v232 offset:17408\n" \
"v_mfma_f32_16x16x32_bf16 v[68:71], a[76:79], a[4:7], v[68:71]\n" \
"v_mov_b32 v228, s60\n" \
"v_lshlrev_b32 v229, 28, s61\n" \
"v_mfma_f32_16x16x32_bf16 v[72:75], a[80:83], a[0:3], v[72:75]\n" \
"ds_read_b64_tr_b16 a[98:99], v232 offset:19456\n" \
"v_mfma_f32_16x16x32_bf16 v[72:75], a[84:87], a[4:7], v[72:75]\n" \
"s_waitcnt lgkmcnt(3)\n" \
"ds_read_b64_tr_b16 a[100:101], v232 offset:25600\n" \
"v_mfma_f32_16x16x32_bf16 v[76:79], a[88:91], a[0:3], v[76:79]\n" \
"ds_read_b64_tr_b16 a[102:103], v232 offset:27648\n" \
"v_mfma_f32_16x16x32_bf16 v[76:79], a[92:95], a[4:7], v[76:79]\n" \
"v_mfma_f32_16x16x32_bf16 v[108:111], a[88:91], a[8:11], v[108:111]\n" \
"ds_read_b64_tr_b16 a[104:105], v233 offset:17408\n" \
"v_mfma_f32_16x16x32_bf16 v[108:111], a[92:95], a[12:15], v[108:111]\n" \
"ds_read_b64_tr_b16 a[106:107], v233 offset:19456\n" \
"v_mfma_f32_16x16x32_bf16 v[104:107], a[80:83], a[8:11], v[104:107]\n" \
"ds_read_b64_tr_b16 a[108:109], v233 offset:25600\n" \
"v_mfma_f32_16x16x32_bf16 v[104:107], a[84:87], a[12:15], v[104:107]\n" \
"v_mfma_f32_16x16x32_bf16 v[100:103], a[72:75], a[8:11], v[100:103]\n" \
"ds_read_b64_tr_b16 a[110:111], v233 offset:27648\n" \
"v_mfma_f32_16x16x32_bf16 v[100:103], a[76:79], a[12:15], v[100:103]\n" \
"ds_read_b64_tr_b16 a[112:113], v232 offset:17920\n" \
"v_mfma_f32_16x16x32_bf16 v[96:99], a[64:67], a[8:11], v[96:99]\n" \
"ds_read_b64_tr_b16 a[114:115], v232 offset:19968\n" \
"v_mfma_f32_16x16x32_bf16 v[96:99], a[68:71], a[12:15], v[96:99]\n" \
"v_mfma_f32_16x16x32_bf16 v[128:131], a[64:67], a[16:19], v[128:131]\n" \
"ds_read_b64_tr_b16 a[116:117], v232 offset:26112\n" \
"v_mfma_f32_16x16x32_bf16 v[128:131], a[68:71], a[20:23], v[128:131]\n" \
"ds_read_b64_tr_b16 a[118:119], v232 offset:28160\n" \
"v_mfma_f32_16x16x32_bf16 v[132:135], a[72:75], a[16:19], v[132:135]\n" \
"ds_read_b64_tr_b16 a[120:121], v233 offset:17920\n" \
"v_mfma_f32_16x16x32_bf16 v[132:135], a[76:79], a[20:23], v[132:135]\n" \
"buffer_store_dwordx4 v[40:43], v236, s[56:59], s93 offen offset:2048 sc1\n" \
"v_mfma_f32_16x16x32_bf16 v[136:139], a[80:83], a[16:19], v[136:139]\n" \
"ds_read_b64_tr_b16 a[122:123], v233 offset:19968\n" \
"v_mfma_f32_16x16x32_bf16 v[136:139], a[84:87], a[20:23], v[136:139]\n" \
"ds_read_b64_tr_b16 a[124:125], v233 offset:26112\n" \
"v_mfma_f32_16x16x32_bf16 v[140:143], a[88:91], a[16:19], v[140:143]\n" \
"ds_read_b64_tr_b16 a[126:127], v233 offset:28160\n" \
"v_mfma_f32_16x16x32_bf16 v[140:143], a[92:95], a[20:23], v[140:143]\n" \
"s_waitcnt lgkmcnt(3)\n" \
"v_mfma_f32_16x16x32_bf16 v[172:175], a[88:91], a[24:27], v[172:175]\n" \
"v_mfma_f32_16x16x32_bf16 v[172:175], a[92:95], a[28:31], v[172:175]\n" \
"v_mfma_f32_16x16x32_bf16 v[168:171], a[80:83], a[24:27], v[168:171]\n" \
"v_mfma_f32_16x16x32_bf16 v[168:171], a[84:87], a[28:31], v[168:171]\n" \
"v_mfma_f32_16x16x32_bf16 v[164:167], a[72:75], a[24:27], v[164:167]\n" \
"v_mfma_f32_16x16x32_bf16 v[164:167], a[76:79], a[28:31], v[164:167]\n" \
"v_mfma_f32_16x16x32_bf16 v[160:163], a[64:67], a[24:27], v[160:163]\n" \
"v_mfma_f32_16x16x32_bf16 v[160:163], a[68:71], a[28:31], v[160:163]\n" \
"s_waitcnt vmcnt(3) lgkmcnt(0)\n" \
"s_barrier\n" \
"v_mfma_f32_16x16x32_bf16 v[80:83], a[96:99], a[0:3], v[80:83]\n" \
"ds_read_b64_tr_b16 a[64:65], v232 offset:32768\n" \
"v_mfma_f32_16x16x32_bf16 v[80:83], a[100:103], a[4:7], v[80:83]\n" \
"ds_read_b64_tr_b16 a[66:67], v232 offset:34816\n" \
"buffer_store_dword v228, v229, s[56:59], s68 offen sc1\n" \
"v_mfma_f32_16x16x32_bf16 v[84:87], a[104:107], a[0:3], v[84:87]\n" \
"ds_read_b64_tr_b16 a[68:69], v232 offset:40960\n" \
"v_lshrrev_b32 v238, 4, v234\n" \
"v_mfma_f32_16x16x32_bf16 v[84:87], a[108:111], a[4:7], v[84:87]\n" \
"buffer_store_dwordx4 v[56:59], v236, s[56:59], s93 offen offset:2304 sc1\n" \
"v_mfma_f32_16x16x32_bf16 v[88:91], a[112:115], a[0:3], v[88:91]\n" \
"ds_read_b64_tr_b16 a[70:71], v232 offset:43008\n" \
"v_lshrrev_b32 v240, 4, v238\n" \
"v_mfma_f32_16x16x32_bf16 v[88:91], a[116:119], a[4:7], v[88:91]\n" \
"ds_read_b64_tr_b16 a[72:73], v233 offset:32768\n" \
"v_lshlrev_b32 v240, 2, v240\n" \
"v_mfma_f32_16x16x32_bf16 v[92:95], a[120:123], a[0:3], v[92:95]\n" \
"ds_read_b64_tr_b16 a[74:75], v233 offset:34816\n" \
"v_and_b32 v238, 15, v238\n" \
"v_mfma_f32_16x16x32_bf16 v[92:95], a[124:127], a[4:7], v[92:95]\n" \
"ds_write_b128 v224, v[192:195] offset:8192\n" \
"v_mfma_f32_16x16x32_bf16 v[124:127], a[120:123], a[8:11], v[124:127]\n" \
"ds_read_b128 a[0:3], v230 offset:32768\n" \
"v_sub_u32 v238, v238, v240\n" \
"v_mfma_f32_16x16x32_bf16 v[124:127], a[124:127], a[12:15], v[124:127]\n" \
"ds_read_b128 a[4:7], v231 offset:32768\n" \
"v_subrev_u32 v248, 0, v238\n" \
"v_mfma_f32_16x16x32_bf16 v[120:123], a[112:115], a[8:11], v[120:123]\n" \
"ds_read_b64_tr_b16 a[76:77], v233 offset:40960\n" \
"v_subrev_u32 v249, 1, v238\n" \
"v_mfma_f32_16x16x32_bf16 v[120:123], a[116:119], a[12:15], v[120:123]\n" \
"ds_write_b128 v224, v[208:211] offset:8448\n" \
"v_mfma_f32_16x16x32_bf16 v[116:119], a[104:107], a[8:11], v[116:119]\n" \
"ds_read_b64_tr_b16 a[78:79], v233 offset:43008\n" \
"v_subrev_u32 v250, 2, v238\n" \
"v_mfma_f32_16x16x32_bf16 v[116:119], a[108:111], a[12:15], v[116:119]\n" \
"ds_read_b64_tr_b16 a[80:81], v232 offset:33280\n" \
"v_subrev_u32 v251, 3, v238\n" \
"v_mfma_f32_16x16x32_bf16 v[112:115], a[96:99], a[8:11], v[112:115]\n" \
"ds_read_b64_tr_b16 a[82:83], v232 offset:35328\n" \
"v_ashrrev_i32 v248, 31, v248\n" \
"v_mfma_f32_16x16x32_bf16 v[112:115], a[100:103], a[12:15], v[112:115]\n" \
"v_ashrrev_i32 v249, 31, v249\n" \
"v_ashrrev_i32 v250, 31, v250\n" \
"v_mfma_f32_16x16x32_bf16 v[144:147], a[96:99], a[16:19], v[144:147]\n" \
"ds_read_b128 a[8:11], v230 offset:34816\n" \
"v_ashrrev_i32 v251, 31, v251\n" \
"v_mfma_f32_16x16x32_bf16 v[144:147], a[100:103], a[20:23], v[144:147]\n" \
"ds_read_b128 a[12:15], v231 offset:34816\n" \
"v_lshlrev_b32 v248, 23, v248\n" \
"v_mfma_f32_16x16x32_bf16 v[148:151], a[104:107], a[16:19], v[148:151]\n" \
"ds_read_b64_tr_b16 a[84:85], v232 offset:41472\n" \
"v_lshlrev_b32 v249, 23, v249\n" \
"v_mfma_f32_16x16x32_bf16 v[148:151], a[108:111], a[20:23], v[148:151]\n" \
"ds_write_b128 v224, v[200:203] offset:10240\n" \
"v_mfma_f32_16x16x32_bf16 v[152:155], a[112:115], a[16:19], v[152:155]\n" \
"ds_read_b64_tr_b16 a[86:87], v232 offset:43520\n" \
"v_lshlrev_b32 v250, 23, v250\n" \
"v_mfma_f32_16x16x32_bf16 v[152:155], a[116:119], a[20:23], v[152:155]\n" \
"ds_read_b64_tr_b16 a[88:89], v233 offset:33280\n" \
"v_lshlrev_b32 v251, 23, v251\n" \
"v_mfma_f32_16x16x32_bf16 v[156:159], a[120:123], a[16:19], v[156:159]\n" \
"s_add_u32 s93, s94, 0\n" \
"ds_read_b64_tr_b16 a[90:91], v233 offset:35328\n" \
"v_mov_b32 v240, 0xff800000\n" \
"v_mfma_f32_16x16x32_bf16 v[156:159], a[124:127], a[20:23], v[156:159]\n" \
"buffer_store_dwordx4 v[36:39], v236, s[56:59], s93 offen offset:0 sc1\n" \
"v_mfma_f32_16x16x32_bf16 v[188:191], a[120:123], a[24:27], v[188:191]\n" \
"ds_read_b128 a[16:19], v230 offset:36864\n" \
"v_add_f32 v248, 0x42400000, v248\n" \
"v_mfma_f32_16x16x32_bf16 v[188:191], a[124:127], a[28:31], v[188:191]\n" \
"ds_read_b128 a[20:23], v231 offset:36864\n" \
"v_add_f32 v249, 0x42400000, v249\n" \
"v_mfma_f32_16x16x32_bf16 v[184:187], a[112:115], a[24:27], v[184:187]\n" \
"ds_read_b64_tr_b16 a[92:93], v233 offset:41472\n" \
"v_add_f32 v250, 0x42400000, v250\n" \
"v_mfma_f32_16x16x32_bf16 v[184:187], a[116:119], a[28:31], v[184:187]\n" \
"ds_write_b128 v224, v[216:219] offset:10496\n" \
"v_mfma_f32_16x16x32_bf16 v[180:183], a[104:107], a[24:27], v[180:183]\n" \
"ds_read_b64_tr_b16 a[94:95], v233 offset:43520\n" \
"v_add_f32 v251, 0x42400000, v251\n" \
"v_mfma_f32_16x16x32_bf16 v[180:183], a[108:111], a[28:31], v[180:183]\n" \
"s_mov_b32 s72, 0x42400000\n" \
"s_cmp_gt_u32 s61, 0\n" \
"s_cselect_b32 s70, s72, 0xff800000\n" \
"v_mfma_f32_16x16x32_bf16 v[176:179], a[96:99], a[24:27], v[176:179]\n" \
"ds_write_b128 v224, v[196:199] offset:0\n" \
"v_mfma_f32_16x16x32_bf16 v[176:179], a[100:103], a[28:31], v[176:179]\n" \
"s_waitcnt lgkmcnt(3)\n" \
"v_mov_b32 v252, s70\n" \
"v_mfma_f32_16x16x32_bf16 v[64:67], a[64:67], a[0:3], v[64:67]\n" \
"ds_read_b128 a[24:27], v230 offset:38912\n" \
"v_mfma_f32_16x16x32_bf16 v[64:67], a[68:71], a[4:7], v[64:67]\n" \
"s_cmp_gt_u32 s61, 1\n" \
"s_cselect_b32 s70, s72, 0xff800000\n" \
"ds_read_b128 a[28:31], v231 offset:38912\n" \
"v_mfma_f32_16x16x32_bf16 v[68:71], a[72:75], a[0:3], v[68:71]\n" \
"ds_read_b64_tr_b16 a[96:97], v232 offset:33792\n" \
"v_mov_b32 v253, s70\n" \
"v_mfma_f32_16x16x32_bf16 v[68:71], a[76:79], a[4:7], v[68:71]\n" \
"s_cmp_gt_u32 s61, 2\n" \
"s_cselect_b32 s70, s72, 0xff800000\n" \
"v_mov_b32 v226, s70\n" \
"v_mfma_f32_16x16x32_bf16 v[72:75], a[80:83], a[0:3], v[72:75]\n" \
"ds_read_b64_tr_b16 a[98:99], v232 offset:35840\n" \
"v_bfe_i32 v243, v234, 4, 1\n" \
"v_mfma_f32_16x16x32_bf16 v[72:75], a[84:87], a[4:7], v[72:75]\n" \
"s_waitcnt lgkmcnt(3)\n" \
"ds_read_b64_tr_b16 a[100:101], v232 offset:41984\n" \
"v_and_b32 v243, 0xaaaa, v243\n" \
"v_mfma_f32_16x16x32_bf16 v[76:79], a[88:91], a[0:3], v[76:79]\n" \
"ds_read_b64_tr_b16 a[102:103], v232 offset:44032\n" \
"v_or_b32 v243, 0x4444, v243\n" \
"v_mfma_f32_16x16x32_bf16 v[76:79], a[92:95], a[4:7], v[76:79]\n" \
"ds_write_b128 v224, v[212:215] offset:256\n" \
"v_mfma_f32_16x16x32_bf16 v[108:111], a[88:91], a[8:11], v[108:111]\n" \
"ds_read_b64_tr_b16 a[104:105], v233 offset:33792\n" \
"v_bfe_i32 v244, v234, 5, 1\n" \
"v_mfma_f32_16x16x32_bf16 v[108:111], a[92:95], a[12:15], v[108:111]\n" \
"ds_read_b64_tr_b16 a[106:107], v233 offset:35840\n" \
"v_not_b32 v244, v244\n" \
"v_mfma_f32_16x16x32_bf16 v[104:107], a[80:83], a[8:11], v[104:107]\n" \
"ds_read_b64_tr_b16 a[108:109], v233 offset:41984\n" \
"v_and_b32 v244, 0x3f803f80, v244\n" \
"v_mfma_f32_16x16x32_bf16 v[104:107], a[84:87], a[12:15], v[104:107]\n" \
"buffer_store_dwordx4 v[16:19], v236, s[56:59], s93 offen offset:256 sc1\n" \
"v_mfma_f32_16x16x32_bf16 v[100:103], a[72:75], a[8:11], v[100:103]\n" \
"ds_read_b64_tr_b16 a[110:111], v233 offset:44032\n" \
"v_mov_b32 v245, v244\n" \
"v_mfma_f32_16x16x32_bf16 v[100:103], a[76:79], a[12:15], v[100:103]\n" \
"ds_read_b64_tr_b16 a[112:113], v232 offset:34304\n" \
"v_mov_b32 v246, v244\n" \
"v_mfma_f32_16x16x32_bf16 v[96:99], a[64:67], a[8:11], v[96:99]\n" \
"ds_read_b64_tr_b16 a[114:115], v232 offset:36352\n" \
"v_mov_b32 v247, v244\n" \
"v_mfma_f32_16x16x32_bf16 v[96:99], a[68:71], a[12:15], v[96:99]\n" \
"ds_write_b128 v224, v[204:207] offset:2048\n" \
"v_mfma_f32_16x16x32_bf16 v[128:131], a[64:67], a[16:19], v[128:131]\n" \
"s_mov_b32 s76, s73\n" \
"s_and_b32 s77, s63, 1\n" \
"ds_read_b64_tr_b16 a[116:117], v232 offset:42496\n" \
"v_mfma_f32_16x16x32_bf16 v[128:131], a[68:71], a[20:23], v[128:131]\n" \
"s_lshl_b32 s77, s77, 15\n" \
"s_xor_b32 s89, s77, 0x8000\n" \
"ds_read_b64_tr_b16 a[118:119], v232 offset:44544\n" \
"v_mfma_f32_16x16x32_bf16 v[132:135], a[72:75], a[16:19], v[132:135]\n" \
"s_add_u32 s82, s89, 0x10000\n" \
"s_mov_b32 s74, 0\n" \
"ds_read_b64_tr_b16 a[120:121], v233 offset:34304\n" \
"v_mfma_f32_16x16x32_bf16 v[132:135], a[76:79], a[20:23], v[132:135]\n" \
"ds_write_b128 v224, v[220:223] offset:2304\n" \
"v_mfma_f32_16x16x32_bf16 v[136:139], a[80:83], a[16:19], v[136:139]\n" \
"s_waitcnt lgkmcnt(4)\n" \
"s_add_u32 s73, s73, 34816\n" \
"ds_read_b64_tr_b16 a[122:123], v233 offset:36352\n" \
"v_mfma_f32_16x16x32_bf16 v[136:139], a[84:87], a[20:23], v[136:139]\n" \
"s_cmp_eq_u32 s73, s83\n" \
"s_cselect_b32 s70, 34816, 0\n" \
"ds_read_b64_tr_b16 a[124:125], v233 offset:42496\n" \
"v_mfma_f32_16x16x32_bf16 v[140:143], a[88:91], a[16:19], v[140:143]\n" \
"s_add_u32 s73, s73, s70\n" \
"s_lshr_b32 s70, s61, 1\n" \
"ds_read_b64_tr_b16 a[126:127], v233 offset:44544\n" \
"v_mfma_f32_16x16x32_bf16 v[140:143], a[92:95], a[20:23], v[140:143]\n" \
"s_lshl_b32 s85, s63, 2\n" \
"s_sub_u32 s86, 60, s85\n" \
"s_add_u32 s85, s85, s70\n" \
"v_mfma_f32_16x16x32_bf16 v[172:175], a[88:91], a[24:27], v[172:175]\n" \
"s_add_u32 s86, s86, s70\n" \
"s_mul_i32 s87, s85, 266240\n" \
"v_add_u32 v230, 0x10000, v230\n" \
"v_mfma_f32_16x16x32_bf16 v[172:175], a[92:95], a[28:31], v[172:175]\n" \
"s_mul_i32 s88, s86, 266240\n" \
"s_and_b32 s75, s61, 1\n" \
"v_add_u32 v231, 0x10000, v231\n" \
"v_mfma_f32_16x16x32_bf16 v[168:171], a[80:83], a[24:27], v[168:171]\n" \
"s_lshl_b32 s70, s75, 8\n" \
"s_lshl_b32 s91, s62, 13\n" \
"s_add_u32 s91, s91, s70\n" \
"v_mfma_f32_16x16x32_bf16 v[168:171], a[84:87], a[28:31], v[168:171]\n" \
"buffer_store_dwordx4 v[52:55], v236, s[56:59], s93 offen offset:2048 sc1\n" \
"v_mfma_f32_16x16x32_bf16 v[164:167], a[72:75], a[24:27], v[164:167]\n" \
"s_add_u32 s91, s91, 35700736\n" \
"s_add_u32 s87, s87, s91\n" \
"s_add_u32 s88, s88, s91\n" \
"v_mfma_f32_16x16x32_bf16 v[164:167], a[76:79], a[28:31], v[164:167]\n" \
"s_lshl_b32 s75, s75, 14\n" \
"s_cmp_eq_u32 s63, 7\n" \
"s_cselect_b32 s75, 0, s75\n" \
"s_xor_b32 s78, s75, 0xc000\n" \
"s_mov_b32 s81, s78\n" \
"s_cmp_eq_u32 s63, 0\n" \
"s_cselect_b32 s91, s75, 0\n" \
"s_add_u32 s91, s91, 0x8000\n" \
"s_xor_b32 s75, s75, 0x8000\n" \
"s_sub_u32 s84, 7, s63\n" \
"v_mfma_f32_16x16x32_bf16 v[160:163], a[64:67], a[24:27], v[160:163]\n" \
"v_mfma_f32_16x16x32_bf16 v[160:163], a[68:71], a[28:31], v[160:163]\n" \
"s_waitcnt vmcnt(18) lgkmcnt(0)\n" \
"s_barrier\n" \
"v_mfma_f32_16x16x32_bf16 v[144:147], a[96:99], a[16:19], v[144:147]\n" \
"s_mov_b64 s[44:45], s[10:11]\n" \
"s_mov_b32 s46, 0\n" \
"ds_read_b64_tr_b16 a[64:65], v232 offset:49152\n" \
"v_mfma_f32_16x16x32_bf16 v[144:147], a[100:103], a[20:23], v[144:147]\n" \
"s_mov_b32 s47, 0\n" \
"s_cmp_eq_u32 s63, 7\n" \
"s_cselect_b64 s[44:45], s[18:19], s[44:45]\n" \
"s_cselect_b32 s46, 5, s46\n" \
"s_cmp_eq_u32 s63, 3\n" \
"s_cselect_b64 s[44:45], s[18:19], s[44:45]\n" \
"s_cselect_b32 s46, 3, s46\n" \
"ds_read_b64_tr_b16 a[66:67], v232 offset:51200\n" \
"v_mfma_f32_16x16x32_bf16 v[148:151], a[104:107], a[16:19], v[148:151]\n" \
"s_cmp_eq_u32 s63, 3\n" \
"s_cselect_b32 s47, 640, s47\n" \
"ds_read_b64_tr_b16 a[68:69], v232 offset:57344\n" \
"v_mfma_f32_16x16x32_bf16 v[148:151], a[108:111], a[20:23], v[148:151]\n" \
"ds_write_b128 v225, a[192:195] offset:0\n" \
"v_mfma_f32_16x16x32_bf16 v[152:155], a[112:115], a[16:19], v[152:155]\n" \
"ds_read_b64_tr_b16 a[70:71], v232 offset:59392\n" \
"v_mfma_f32_16x16x32_bf16 v[152:155], a[116:119], a[20:23], v[152:155]\n" \
"s_cmp_eq_u32 s63, 6\n" \
"s_cselect_b64 s[44:45], s[22:23], s[44:45]\n" \
"s_cselect_b32 s46, 6, s46\n" \
"ds_read_b64_tr_b16 a[72:73], v233 offset:49152\n" \
"v_mfma_f32_16x16x32_bf16 v[156:159], a[120:123], a[16:19], v[156:159]\n" \
"ds_read_b64_tr_b16 a[74:75], v233 offset:51200\n" \
"v_mfma_f32_16x16x32_bf16 v[156:159], a[124:127], a[20:23], v[156:159]\n" \
"s_cmp_eq_u32 s63, 4\n" \
"s_cselect_b64 s[44:45], s[20:21], s[44:45]\n" \
"s_cselect_b32 s46, 4, s46\n" \
"v_mfma_f32_16x16x32_bf16 v[188:191], a[120:123], a[24:27], v[188:191]\n" \
"ds_read_b128 a[16:19], v230 offset:0\n" \
"v_mfma_f32_16x16x32_bf16 v[188:191], a[124:127], a[28:31], v[188:191]\n" \
"s_cmp_eq_u32 s63, 2\n" \
"s_cselect_b64 s[44:45], s[24:25], s[44:45]\n" \
"s_cselect_b32 s46, 2, s46\n" \
"ds_read_b128 a[20:23], v231 offset:0\n" \
"v_mfma_f32_16x16x32_bf16 v[184:187], a[112:115], a[24:27], v[184:187]\n" \
"s_lshl_b32 s48, s62, 2\n" \
"s_add_u32 s48, s48, s61\n" \
"ds_read_b64_tr_b16 a[76:77], v233 offset:57344\n" \
"v_mfma_f32_16x16x32_bf16 v[184:187], a[116:119], a[28:31], v[184:187]\n" \
"ds_write_b128 v225, a[196:199] offset:1024\n" \
"v_mfma_f32_16x16x32_bf16 v[180:183], a[104:107], a[24:27], v[180:183]\n" \
"s_add_u32 s48, s48, s47\n" \
"s_lshl_b32 s48, s48, 13\n" \
"ds_read_b64_tr_b16 a[78:79], v233 offset:59392\n" \
"v_mfma_f32_16x16x32_bf16 v[180:183], a[108:111], a[28:31], v[180:183]\n" \
"s_add_u32 s44, s44, s48\n" \
"s_addc_u32 s45, s45, 0\n" \
"ds_read_b64_tr_b16 a[80:81], v232 offset:49664\n" \
"v_mfma_f32_16x16x32_bf16 v[176:179], a[96:99], a[24:27], v[176:179]\n" \
"s_cmp_eq_u32 s46, 0\n" \
"s_cselect_b32 s48, 28, 3\n" \
"ds_read_b64_tr_b16 a[82:83], v232 offset:51712\n" \
"v_mfma_f32_16x16x32_bf16 v[176:179], a[100:103], a[28:31], v[176:179]\n" \
"buffer_store_dwordx4 v[24:27], v236, s[56:59], s93 offen offset:2304 sc1\n" \
"v_mfma_f32_16x16x32_bf16 v[80:83], a[96:99], a[0:3], v[80:83]\n" \
"ds_read_b128 a[24:27], v230 offset:2048\n" \
"v_mfma_f32_16x16x32_bf16 v[80:83], a[100:103], a[4:7], v[80:83]\n" \
"ds_read_b128 a[28:31], v231 offset:2048\n" \
"v_mfma_f32_16x16x32_bf16 v[84:87], a[104:107], a[0:3], v[84:87]\n" \
"ds_read_b64_tr_b16 a[84:85], v232 offset:57856\n" \
"v_mfma_f32_16x16x32_bf16 v[84:87], a[108:111], a[4:7], v[84:87]\n" \
"ds_write_b128 v225, a[200:203] offset:2048\n" \
"v_mfma_f32_16x16x32_bf16 v[88:91], a[112:115], a[0:3], v[88:91]\n" \
"ds_read_b64_tr_b16 a[86:87], v232 offset:59904\n" \
"v_mfma_f32_16x16x32_bf16 v[88:91], a[116:119], a[4:7], v[88:91]\n" \
"ds_read_b64_tr_b16 a[88:89], v233 offset:49664\n" \
"v_mfma_f32_16x16x32_bf16 v[92:95], a[120:123], a[0:3], v[92:95]\n" \
"ds_read_b64_tr_b16 a[90:91], v233 offset:51712\n" \
"v_mfma_f32_16x16x32_bf16 v[92:95], a[124:127], a[4:7], v[92:95]\n" \
"ds_write_b128 v225, a[204:207] offset:3072\n" \
"v_mfma_f32_16x16x32_bf16 v[124:127], a[120:123], a[8:11], v[124:127]\n" \
"ds_read_b128 a[0:3], v230 offset:4096\n" \
"v_mfma_f32_16x16x32_bf16 v[124:127], a[124:127], a[12:15], v[124:127]\n" \
"ds_read_b128 a[4:7], v231 offset:4096\n" \
"v_mfma_f32_16x16x32_bf16 v[120:123], a[112:115], a[8:11], v[120:123]\n" \
"ds_read_b64_tr_b16 a[92:93], v233 offset:57856\n" \
"v_mfma_f32_16x16x32_bf16 v[120:123], a[116:119], a[12:15], v[120:123]\n" \
"v_mfma_f32_16x16x32_bf16 v[116:119], a[104:107], a[8:11], v[116:119]\n" \
"ds_read_b64_tr_b16 a[94:95], v233 offset:59904\n" \
"v_mfma_f32_16x16x32_bf16 v[116:119], a[108:111], a[12:15], v[116:119]\n" \
"v_mfma_f32_16x16x32_bf16 v[112:115], a[96:99], a[8:11], v[112:115]\n" \
"ds_write_b128 v225, a[224:227] offset:8192\n" \
"v_mfma_f32_16x16x32_bf16 v[112:115], a[100:103], a[12:15], v[112:115]\n" \
"s_waitcnt lgkmcnt(2)\n" \
"v_mfma_f32_16x16x32_bf16 v[64:67], a[64:67], a[16:19], v[64:67]\n" \
"ds_read_b128 a[8:11], v230 offset:6144\n" \
"ds_read_b128 a[12:15], v231 offset:6144\n" \
"v_mfma_f32_16x16x32_bf16 v[64:67], a[68:71], a[20:23], v[64:67]\n" \
"ds_read_b64_tr_b16 a[96:97], v232 offset:50176\n" \
"ds_read_b64_tr_b16 a[98:99], v232 offset:52224\n" \
"v_mfma_f32_16x16x32_bf16 v[68:71], a[72:75], a[16:19], v[68:71]\n" \
"s_waitcnt lgkmcnt(5)\n" \
"ds_read_b64_tr_b16 a[100:101], v232 offset:58368\n" \
"ds_read_b64_tr_b16 a[102:103], v232 offset:60416\n" \
"v_mfma_f32_16x16x32_bf16 v[68:71], a[76:79], a[20:23], v[68:71]\n" \
"s_getpc_b64 s[64:65]\n" \
"Lpd1_%=:\n" \
"s_lshl_b32 s66, s62, 2\n" \
"ds_read_b64_tr_b16 a[104:105], v233 offset:50176\n" \
"ds_read_b64_tr_b16 a[106:107], v233 offset:52224\n" \
"v_mfma_f32_16x16x32_bf16 v[72:75], a[80:83], a[16:19], v[72:75]\n" \
"s_add_u32 s66, s66, s61\n" \
"ds_read_b64_tr_b16 a[108:109], v233 offset:58368\n" \
"ds_read_b64_tr_b16 a[110:111], v233 offset:60416\n" \
"v_mfma_f32_16x16x32_bf16 v[72:75], a[84:87], a[20:23], v[72:75]\n" \
"s_and_b32 s66, s66, 15\n" \
"ds_read_b64_tr_b16 a[112:113], v232 offset:50688\n" \
"ds_read_b64_tr_b16 a[114:115], v232 offset:52736\n" \
"v_mfma_f32_16x16x32_bf16 v[76:79], a[88:91], a[16:19], v[76:79]\n" \
"s_lshl_b32 s66, s66, 12\n" \
"ds_read_b64_tr_b16 a[116:117], v232 offset:58880\n" \
"ds_read_b64_tr_b16 a[118:119], v232 offset:60928\n" \
"v_mfma_f32_16x16x32_bf16 v[76:79], a[92:95], a[20:23], v[76:79]\n" \
"s_add_u32 s64, s64, Lloop2_%=-Lpd1_%=\n" \
"s_addc_u32 s65, s65, 0\n" \
"s_mov_b32 s67, Lcode_end_%=-Lloop2_%=-4\n" \
"ds_read_b64_tr_b16 a[120:121], v233 offset:50688\n" \
"ds_read_b64_tr_b16 a[122:123], v233 offset:52736\n" \
"v_mfma_f32_16x16x32_bf16 v[96:99], a[64:67], a[24:27], v[96:99]\n" \
"s_add_u32 s93, s94, 17858560\n" \
"ds_read_b64_tr_b16 a[124:125], v233 offset:58880\n" \
"ds_read_b64_tr_b16 a[126:127], v233 offset:60928\n" \
"v_mfma_f32_16x16x32_bf16 v[96:99], a[68:71], a[28:31], v[96:99]\n" \
"buffer_store_dwordx4 v[44:47], v234, s[56:59], s93 offen offset:0 sc1\n" \
"v_mfma_f32_16x16x32_bf16 v[100:103], a[72:75], a[24:27], v[100:103]\n" \
"ds_write_b128 v225, a[228:231] offset:9216\n" \
"v_mfma_f32_16x16x32_bf16 v[100:103], a[76:79], a[28:31], v[100:103]\n" \
"v_lshl_add_u32 v230, v234, 2, s66\n" \
"v_min_u32 v230, s67, v230\n" \
"v_mfma_f32_16x16x32_bf16 v[104:107], a[80:83], a[24:27], v[104:107]\n" \
"buffer_load_dword v227, v242, s[56:59], s90 offen sc1\n" \
"v_mfma_f32_16x16x32_bf16 v[104:107], a[84:87], a[28:31], v[104:107]\n" \
"ds_write_b128 v225, a[232:235] offset:10240\n" \
"v_mfma_f32_16x16x32_bf16 v[108:111], a[88:91], a[24:27], v[108:111]\n" \
"global_load_dword v230, v230, s[64:65]\n" \
"v_mfma_f32_16x16x32_bf16 v[108:111], a[92:95], a[28:31], v[108:111]\n" \
"s_waitcnt lgkmcnt(1)\n" \
"v_cvt_pk_bf16_f32 v64, v64, v65\n" \
"v_cvt_pk_bf16_f32 v65, v66, v67\n" \
"v_mfma_f32_16x16x32_bf16 v[80:83], a[96:99], a[16:19], v[80:83]\n" \
"v_cvt_pk_bf16_f32 v66, v68, v69\n" \
"v_cvt_pk_bf16_f32 v67, v70, v71\n" \
"v_mfma_f32_16x16x32_bf16 v[80:83], a[100:103], a[20:23], v[80:83]\n" \
"v_accvgpr_write_b32 a128, v64\n" \
"v_accvgpr_write_b32 a129, v65\n" \
"v_accvgpr_write_b32 a130, v66\n" \
"v_mfma_f32_16x16x32_bf16 v[84:87], a[104:107], a[16:19], v[84:87]\n" \
"v_accvgpr_write_b32 a131, v67\n" \
"v_cvt_pk_bf16_f32 v72, v72, v73\n" \
"v_mfma_f32_16x16x32_bf16 v[84:87], a[108:111], a[20:23], v[84:87]\n" \
"buffer_store_dwordx4 v[60:63], v234, s[56:59], s93 offen offset:1024 sc1\n" \
"v_mfma_f32_16x16x32_bf16 v[88:91], a[112:115], a[16:19], v[88:91]\n" \
"ds_write_b128 v225, a[236:239] offset:11264\n" \
"v_mfma_f32_16x16x32_bf16 v[88:91], a[116:119], a[20:23], v[88:91]\n" \
"v_cvt_pk_bf16_f32 v73, v74, v75\n" \
"v_cvt_pk_bf16_f32 v74, v76, v77\n" \
"v_cvt_pk_bf16_f32 v75, v78, v79\n" \
"v_mfma_f32_16x16x32_bf16 v[92:95], a[120:123], a[16:19], v[92:95]\n" \
"v_accvgpr_write_b32 a132, v72\n" \
"v_accvgpr_write_b32 a133, v73\n" \
"v_mfma_f32_16x16x32_bf16 v[92:95], a[124:127], a[20:23], v[92:95]\n" \
"v_accvgpr_write_b32 a134, v74\n" \
"v_accvgpr_write_b32 a135, v75\n" \
"v_mfma_f32_16x16x32_bf16 v[112:115], a[96:99], a[24:27], v[112:115]\n" \
"v_cvt_pk_bf16_f32 v96, v96, v97\n" \
"v_cvt_pk_bf16_f32 v97, v98, v99\n" \
"v_mfma_f32_16x16x32_bf16 v[112:115], a[100:103], a[28:31], v[112:115]\n" \
"v_cvt_pk_bf16_f32 v98, v100, v101\n" \
"v_cvt_pk_bf16_f32 v99, v102, v103\n" \
"v_mfma_f32_16x16x32_bf16 v[116:119], a[104:107], a[24:27], v[116:119]\n" \
"v_cvt_pk_bf16_f32 v104, v104, v105\n" \
"v_cvt_pk_bf16_f32 v105, v106, v107\n" \
"v_mfma_f32_16x16x32_bf16 v[116:119], a[108:111], a[28:31], v[116:119]\n" \
"v_cvt_pk_bf16_f32 v106, v108, v109\n" \
"v_cvt_pk_bf16_f32 v107, v110, v111\n" \
"v_mfma_f32_16x16x32_bf16 v[120:123], a[112:115], a[24:27], v[120:123]\n" \
"v_mfma_f32_16x16x32_bf16 v[120:123], a[116:119], a[28:31], v[120:123]\n" \
"v_cvt_pk_bf16_f32 v80, v80, v81\n" \
"v_cvt_pk_bf16_f32 v81, v82, v83\n" \
"v_cvt_pk_bf16_f32 v82, v84, v85\n" \
"v_mfma_f32_16x16x32_bf16 v[124:127], a[120:123], a[24:27], v[124:127]\n" \
"v_cvt_pk_bf16_f32 v83, v86, v87\n" \
"v_accvgpr_write_b32 a136, v80\n" \
"v_mfma_f32_16x16x32_bf16 v[124:127], a[124:127], a[28:31], v[124:127]\n" \
"v_accvgpr_write_b32 a137, v81\n" \
"s_waitcnt lgkmcnt(0)\n" \
"s_barrier\n" \
"v_mfma_f32_16x16x32_bf16 v[128:131], a[64:67], a[0:3], v[128:131]\n" \
"v_accvgpr_write_b32 a138, v82\n" \
"v_accvgpr_write_b32 a139, v83\n" \
"v_mfma_f32_16x16x32_bf16 v[128:131], a[68:71], a[4:7], v[128:131]\n" \
"ds_read_b128 a[192:195], v237 offset:0\n" \
"v_cvt_pk_bf16_f32 v88, v88, v89\n" \
"v_mfma_f32_16x16x32_bf16 v[132:135], a[72:75], a[0:3], v[132:135]\n" \
"v_cvt_pk_bf16_f32 v89, v90, v91\n" \
"v_cvt_pk_bf16_f32 v90, v92, v93\n" \
"v_mfma_f32_16x16x32_bf16 v[132:135], a[76:79], a[4:7], v[132:135]\n" \
"ds_read_b128 a[196:199], v237 offset:2048\n" \
"v_cvt_pk_bf16_f32 v91, v94, v95\n" \
"v_mfma_f32_16x16x32_bf16 v[136:139], a[80:83], a[0:3], v[136:139]\n" \
"v_med3_f32 v208, v248, v252, s72\n" \
"v_med3_f32 v209, v249, v252, s72\n" \
"v_mfma_f32_16x16x32_bf16 v[136:139], a[84:87], a[4:7], v[136:139]\n" \
"ds_read_b128 a[200:203], v237 offset:4096\n" \
"v_med3_f32 v210, v250, v252, s72\n" \
"v_med3_f32 v211, v251, v252, s72\n" \
"v_mfma_f32_16x16x32_bf16 v[140:143], a[88:91], a[0:3], v[140:143]\n" \
"buffer_store_dwordx4 v[20:23], v234, s[56:59], s93 offen offset:2048 sc1\n" \
"v_mfma_f32_16x16x32_bf16 v[140:143], a[92:95], a[4:7], v[140:143]\n" \
"ds_read_b128 a[204:207], v237 offset:6144\n" \
"v_cvt_pk_bf16_f32 v112, v112, v113\n" \
"v_mfma_f32_16x16x32_bf16 v[144:147], a[96:99], a[0:3], v[144:147]\n" \
"v_cvt_pk_bf16_f32 v113, v114, v115\n" \
"v_cvt_pk_bf16_f32 v114, v116, v117\n" \
"v_mfma_f32_16x16x32_bf16 v[144:147], a[100:103], a[4:7], v[144:147]\n" \
"ds_read_b128 a[208:211], v237 offset:256\n" \
"v_cvt_pk_bf16_f32 v115, v118, v119\n" \
"v_cvt_pk_bf16_f32 v120, v120, v121\n" \
"v_mfma_f32_16x16x32_bf16 v[148:151], a[104:107], a[0:3], v[148:151]\n" \
"v_cvt_pk_bf16_f32 v121, v122, v123\n" \
"v_cvt_pk_bf16_f32 v122, v124, v125\n" \
"v_mfma_f32_16x16x32_bf16 v[148:151], a[108:111], a[4:7], v[148:151]\n" \
"ds_read_b128 a[212:215], v237 offset:2304\n" \
"v_cvt_pk_bf16_f32 v123, v126, v127\n" \
"v_mfma_f32_16x16x32_bf16 v[152:155], a[112:115], a[0:3], v[152:155]\n" \
"v_mfma_f32_16x16x32_bf16 v[152:155], a[116:119], a[4:7], v[152:155]\n" \
"ds_read_b128 a[216:219], v237 offset:4352\n" \
"v_cvt_pk_bf16_f32 v128, v128, v129\n" \
"v_mfma_f32_16x16x32_bf16 v[156:159], a[120:123], a[0:3], v[156:159]\n" \
"v_cvt_pk_bf16_f32 v129, v130, v131\n" \
"v_cvt_pk_bf16_f32 v130, v132, v133\n" \
"v_mfma_f32_16x16x32_bf16 v[156:159], a[124:127], a[4:7], v[156:159]\n" \
"ds_read_b128 a[220:223], v237 offset:6400\n" \
"v_cvt_pk_bf16_f32 v131, v134, v135\n" \
"v_cvt_pk_bf16_f32 v136, v136, v137\n" \
"v_mfma_f32_16x16x32_bf16 v[160:163], a[64:67], a[8:11], v[160:163]\n" \
"v_cvt_pk_bf16_f32 v137, v138, v139\n" \
"v_cvt_pk_bf16_f32 v138, v140, v141\n" \
"v_mfma_f32_16x16x32_bf16 v[160:163], a[68:71], a[12:15], v[160:163]\n" \
"ds_read_b128 a[224:227], v237 offset:8192\n" \
"v_cvt_pk_bf16_f32 v139, v142, v143\n" \
"v_mfma_f32_16x16x32_bf16 v[164:167], a[72:75], a[8:11], v[164:167]\n" \
"v_mfma_f32_16x16x32_bf16 v[164:167], a[76:79], a[12:15], v[164:167]\n" \
"ds_read_b128 a[228:231], v237 offset:10240\n" \
"v_mfma_f32_16x16x32_bf16 v[168:171], a[80:83], a[8:11], v[168:171]\n" \
"v_mfma_f32_16x16x32_bf16 v[168:171], a[84:87], a[12:15], v[168:171]\n" \
"ds_read_b128 a[232:235], v237 offset:12288\n" \
"v_cvt_pk_bf16_f32 v144, v144, v145\n" \
"v_cvt_pk_bf16_f32 v145, v146, v147\n" \
"v_mfma_f32_16x16x32_bf16 v[172:175], a[88:91], a[8:11], v[172:175]\n" \
"v_cvt_pk_bf16_f32 v146, v148, v149\n" \
"v_cvt_pk_bf16_f32 v147, v150, v151\n" \
"v_mfma_f32_16x16x32_bf16 v[172:175], a[92:95], a[12:15], v[172:175]\n" \
"ds_read_b128 a[236:239], v237 offset:14336\n" \
"v_cvt_pk_bf16_f32 v152, v152, v153\n" \
"v_mfma_f32_16x16x32_bf16 v[176:179], a[96:99], a[8:11], v[176:179]\n" \
"v_cvt_pk_bf16_f32 v153, v154, v155\n" \
"v_cvt_pk_bf16_f32 v154, v156, v157\n" \
"v_mfma_f32_16x16x32_bf16 v[176:179], a[100:103], a[12:15], v[176:179]\n" \
"ds_read_b128 a[240:243], v237 offset:8448\n" \
"v_cvt_pk_bf16_f32 v155, v158, v159\n" \
"v_mfma_f32_16x16x32_bf16 v[180:183], a[104:107], a[8:11], v[180:183]\n" \
"buffer_store_dwordx4 v[28:31], v234, s[56:59], s93 offen offset:3072 sc1\n" \
"v_mfma_f32_16x16x32_bf16 v[180:183], a[108:111], a[12:15], v[180:183]\n" \
"ds_read_b128 a[244:247], v237 offset:10496\n" \
"ds_write_b128 v239, v[128:131] offset:0\n" \
"v_mov_b32 v196, 0x42400000\n" \
"v_mfma_f32_16x16x32_bf16 v[184:187], a[112:115], a[8:11], v[184:187]\n" \
"v_mov_b32 v197, 0x42400000\n" \
"v_mov_b32 v198, 0x42400000\n" \
"v_mfma_f32_16x16x32_bf16 v[184:187], a[116:119], a[12:15], v[184:187]\n" \
"ds_read_b128 a[248:251], v237 offset:12544\n" \
"v_mov_b32 v199, 0x42400000\n" \
"v_cvt_pk_bf16_f32 v160, v160, v161\n" \
"v_mfma_f32_16x16x32_bf16 v[188:191], a[120:123], a[8:11], v[188:191]\n" \
"v_cvt_pk_bf16_f32 v161, v162, v163\n" \
"v_cvt_pk_bf16_f32 v162, v164, v165\n" \
"v_mfma_f32_16x16x32_bf16 v[188:191], a[124:127], a[12:15], v[188:191]\n" \
"s_waitcnt lgkmcnt(3)\n" \
"ds_read_b128 a[252:255], v237 offset:14592\n" \
"v_cvt_pk_bf16_f32 v163, v166, v167\n" \
"v_mfma_f32_16x16x32_bf16 v[16:19], a[192:195], v[96:99], v[208:211]\n" \
"v_cvt_pk_bf16_f32 v168, v168, v169\n" \
"v_cvt_pk_bf16_f32 v169, v170, v171\n" \
"v_mfma_f32_16x16x32_bf16 v[16:19], a[196:199], v[104:107], v[16:19]\n" \
"v_cvt_pk_bf16_f32 v170, v172, v173\n" \
"v_cvt_pk_bf16_f32 v171, v174, v175\n" \
"v_mfma_f32_16x16x32_bf16 v[16:19], a[200:203], v[112:115], v[16:19]\n" \
"v_cvt_pk_bf16_f32 v176, v176, v177\n" \
"v_cvt_pk_bf16_f32 v177, v178, v179\n" \
"v_mfma_f32_16x16x32_bf16 v[16:19], a[204:207], v[120:123], v[16:19]\n" \
"v_cvt_pk_bf16_f32 v178, v180, v181\n" \
"v_cvt_pk_bf16_f32 v179, v182, v183\n" \
"v_mfma_f32_16x16x32_bf16 v[32:35], a[204:207], v[152:155], v[196:199]\n" \
"v_cvt_pk_bf16_f32 v184, v184, v185\n" \
"v_cvt_pk_bf16_f32 v185, v186, v187\n" \
"v_mfma_f32_16x16x32_bf16 v[32:35], a[200:203], v[144:147], v[32:35]\n" \
"v_cvt_pk_bf16_f32 v186, v188, v189\n" \
"v_cvt_pk_bf16_f32 v187, v190, v191\n" \
"v_mfma_f32_16x16x32_bf16 v[32:35], a[196:199], v[136:139], v[32:35]\n" \
"s_add_u32 s93, s94, 17850368\n" \
"v_med3_f32 v212, v248, v253, v252\n" \
"v_med3_f32 v213, v249, v253, v252\n" \
"Lpg5012_%=:\n" \
"s_cbranch_execz Lpg6775_%=\n" \
"v_mfma_f32_16x16x32_bf16 v[32:35], a[192:195], v[128:131], v[32:35]\n" \
"buffer_store_dwordx4 v[0:3], v234, s[56:59], s93 offen offset:0 sc1\n" \
"v_mfma_f32_16x16x32_bf16 v[48:51], a[192:195], v[160:163], v[196:199]\n" \
"v_med3_f32 v214, v250, v253, v252\n" \
"v_med3_f32 v215, v251, v253, v252\n" \
"v_mfma_f32_16x16x32_bf16 v[48:51], a[196:199], v[168:171], v[48:51]\n" \
"ds_read_b128 a[192:195], v237 offset:16384\n" \
"v_med3_f32 v216, v248, v226, v253\n" \
"v_mfma_f32_16x16x32_bf16 v[48:51], a[200:203], v[176:179], v[48:51]\n" \
"ds_read_b128 a[196:199], v237 offset:18432\n" \
"v_med3_f32 v217, v249, v226, v253\n" \
"v_mfma_f32_16x16x32_bf16 v[48:51], a[204:207], v[184:187], v[48:51]\n" \
"ds_read_b128 a[200:203], v237 offset:20480\n" \
"ds_write_b128 v239, v[136:139] offset:1024\n" \
"v_mfma_f32_16x16x32_bf16 v[52:55], a[220:223], v[184:187], v[196:199]\n" \
"ds_read_b128 a[204:207], v237 offset:22528\n" \
"v_med3_f32 v218, v250, v226, v253\n" \
"v_mfma_f32_16x16x32_bf16 v[52:55], a[216:219], v[176:179], v[52:55]\n" \
"v_med3_f32 v219, v251, v226, v253\n" \
"v_med3_f32 v220, v248, v240, v226\n" \
"v_mfma_f32_16x16x32_bf16 v[52:55], a[212:215], v[168:171], v[52:55]\n" \
"ds_write_b128 v239, v[144:147] offset:2048\n" \
"v_mfma_f32_16x16x32_bf16 v[52:55], a[208:211], v[160:163], v[52:55]\n" \
"buffer_store_dwordx4 v[4:7], v234, s[56:59], s93 offen offset:1024 sc1\n" \
"v_mfma_f32_16x16x32_bf16 v[36:39], a[208:211], v[128:131], v[196:199]\n" \
"v_med3_f32 v221, v249, v240, v226\n" \
"v_med3_f32 v222, v250, v240, v226\n" \
"v_mfma_f32_16x16x32_bf16 v[36:39], a[212:215], v[136:139], v[36:39]\n" \
"v_med3_f32 v223, v251, v240, v226\n" \
"v_accvgpr_write_b32 a144, v96\n" \
"v_mfma_f32_16x16x32_bf16 v[36:39], a[216:219], v[144:147], v[36:39]\n" \
"v_accvgpr_write_b32 a145, v97\n" \
"v_accvgpr_write_b32 a146, v98\n" \
"v_mfma_f32_16x16x32_bf16 v[36:39], a[220:223], v[152:155], v[36:39]\n" \
"ds_write_b128 v239, v[152:155] offset:3072\n" \
"v_mfma_f32_16x16x32_bf16 v[20:23], a[220:223], v[120:123], v[212:215]\n" \
"v_accvgpr_write_b32 a147, v99\n" \
"v_accvgpr_write_b32 a148, v104\n" \
"v_mfma_f32_16x16x32_bf16 v[20:23], a[216:219], v[112:115], v[20:23]\n" \
"ds_read_b128 a[220:223], v237 offset:22784\n" \
"v_accvgpr_write_b32 a149, v105\n" \
"v_mfma_f32_16x16x32_bf16 v[20:23], a[212:215], v[104:107], v[20:23]\n" \
"ds_read_b128 a[216:219], v237 offset:20736\n" \
"v_accvgpr_write_b32 a150, v106\n" \
"v_mfma_f32_16x16x32_bf16 v[20:23], a[208:211], v[96:99], v[20:23]\n" \
"ds_read_b128 a[212:215], v237 offset:18688\n" \
"ds_write_b128 v239, v[160:163] offset:4096\n" \
"v_mfma_f32_16x16x32_bf16 v[24:27], a[224:227], v[96:99], v[216:219]\n" \
"ds_read_b128 a[208:211], v237 offset:16640\n" \
"v_accvgpr_write_b32 a151, v107\n" \
"v_mfma_f32_16x16x32_bf16 v[24:27], a[228:231], v[104:107], v[24:27]\n" \
"v_accvgpr_write_b32 a152, v112\n" \
"v_accvgpr_write_b32 a153, v113\n" \
"v_mfma_f32_16x16x32_bf16 v[24:27], a[232:235], v[112:115], v[24:27]\n" \
"ds_write_b128 v239, v[168:171] offset:5120\n" \
"v_mfma_f32_16x16x32_bf16 v[24:27], a[236:239], v[120:123], v[24:27]\n" \
"buffer_store_dwordx4 v[8:11], v234, s[56:59], s93 offen offset:2048 sc1\n" \
"v_mfma_f32_16x16x32_bf16 v[40:43], a[236:239], v[152:155], v[196:199]\n" \
"v_accvgpr_write_b32 a154, v114\n" \
"v_accvgpr_write_b32 a155, v115\n" \
"v_mfma_f32_16x16x32_bf16 v[40:43], a[232:235], v[144:147], v[40:43]\n" \
"v_accvgpr_write_b32 a156, v120\n" \
"v_accvgpr_write_b32 a157, v121\n" \
"v_mfma_f32_16x16x32_bf16 v[40:43], a[228:231], v[136:139], v[40:43]\n" \
"v_accvgpr_write_b32 a158, v122\n" \
"v_accvgpr_write_b32 a159, v123\n" \
"v_mfma_f32_16x16x32_bf16 v[40:43], a[224:227], v[128:131], v[40:43]\n" \
"ds_write_b128 v239, v[176:179] offset:6144\n" \
"v_mfma_f32_16x16x32_bf16 v[56:59], a[224:227], v[160:163], v[196:199]\n" \
"v_accvgpr_write_b32 a140, v88\n" \
"v_accvgpr_write_b32 a141, v89\n" \
"v_mfma_f32_16x16x32_bf16 v[56:59], a[228:231], v[168:171], v[56:59]\n" \
"ds_read_b128 a[224:227], v237 offset:24576\n" \
"v_accvgpr_write_b32 a142, v90\n" \
"v_mfma_f32_16x16x32_bf16 v[56:59], a[232:235], v[176:179], v[56:59]\n" \
"s_waitcnt lgkmcnt(2)\n" \
"ds_read_b128 a[228:231], v237 offset:26624\n" \
"v_accvgpr_write_b32 a143, v91\n" \
"v_mfma_f32_16x16x32_bf16 v[56:59], a[236:239], v[184:187], v[56:59]\n" \
"buffer_store_dwordx4 v[12:15], v234, s[56:59], s93 offen offset:3072 sc1\n" \
"ds_write_b128 v239, v[184:187] offset:7168\n" \
"v_mfma_f32_16x16x32_bf16 v[60:63], a[252:255], v[184:187], v[196:199]\n" \
"ds_read_b128 a[232:235], v237 offset:28672\n" \
"v_add_u32_e64 v233, s82, v235\n" \
"v_mfma_f32_16x16x32_bf16 v[60:63], a[248:251], v[176:179], v[60:63]\n" \
"s_waitcnt vmcnt(8)\n" \
"ds_read_b128 a[236:239], v237 offset:30720\n" \
"v_cmp_eq_u32 vcc, s60, v227\n" \
"v_mfma_f32_16x16x32_bf16 v[60:63], a[244:247], v[168:171], v[60:63]\n" \
"v_exp_f32_e64 v16, v16\n" \
"v_mfma_f32_16x16x32_bf16 v[60:63], a[240:243], v[160:163], v[60:63]\n" \
"s_andn2_b32 s70, 0xff, vcc_lo\n" \
"s_cbranch_scc1 Lpl1_%=\n" \
"Lpr1_%=:\n" \
"v_exp_f32 v17, v17\n" \
"v_exp_f32 v18, v18\n" \
"v_mfma_f32_16x16x32_bf16 v[44:47], a[240:243], v[128:131], v[196:199]\n" \
"v_exp_f32 v19, v19\n" \
"v_mfma_f32_16x16x32_bf16 v[44:47], a[244:247], v[136:139], v[44:47]\n" \
"s_add_u32 m0, s79, 32768\n" \
"v_exp_f32 v20, v20\n" \
"v_mfma_f32_16x16x32_bf16 v[44:47], a[248:251], v[144:147], v[44:47]\n" \
"s_add_u32 s71, s76, 0\n" \
"v_cvt_pk_bf16_f32 v16, v16, v17\n" \
"v_cvt_pk_bf16_f32 v17, v18, v19\n" \
"v_mfma_f32_16x16x32_bf16 v[44:47], a[252:255], v[152:155], v[44:47]\n" \
"buffer_load_dwordx4 v234, s[36:39], s71 offen offset:0 sc1 lds\n" \
"v_exp_f32 v21, v21\n" \
"v_exp_f32 v22, v22\n" \
"v_mfma_f32_16x16x32_bf16 v[28:31], a[252:255], v[120:123], v[220:223]\n" \
"v_exp_f32 v23, v23\n" \
"v_mfma_f32_16x16x32_bf16 v[28:31], a[248:251], v[112:115], v[28:31]\n" \
"ds_read_b128 a[252:255], v237 offset:30976\n" \
"v_cvt_pk_bf16_f32 v18, v20, v21\n" \
"v_mfma_f32_16x16x32_bf16 v[28:31], a[244:247], v[104:107], v[28:31]\n" \
"buffer_load_dwordx4 v234, s[36:39], s71 offen offset:1024 sc1 lds\n" \
"v_mfma_f32_16x16x32_bf16 v[28:31], a[240:243], v[96:99], v[28:31]\n" \
"ds_read_b128 a[244:247], v237 offset:26880\n" \
"ds_read_b128 a[248:251], v237 offset:28928\n" \
"v_exp_f32 v32, v32\n" \
"v_exp_f32 v33, v33\n" \
"v_mfma_f32_16x16x32_bf16 v[80:83], a[192:195], a[144:147], v[196:199]\n" \
"ds_read_b128 a[240:243], v237 offset:24832\n" \
"v_cvt_pk_bf16_f32 v19, v22, v23\n" \
"v_mfma_f32_16x16x32_bf16 v[80:83], a[196:199], a[148:151], v[80:83]\n" \
"buffer_load_dwordx4 v234, s[36:39], s71 offen offset:2048 sc1 lds\n" \
"v_mfma_f32_16x16x32_bf16 v[80:83], a[200:203], a[152:155], v[80:83]\n" \
"s_lshl_b32 s68, s63, 2\n" \
"s_add_u32 s68, s68, s95\n" \
"s_mov_b32 s69, 0\n" \
"v_mfma_f32_16x16x32_bf16 v[80:83], a[204:207], a[156:159], v[80:83]\n" \
"s_mov_b32 s89, s63\n" \
"ds_read_b128 a[44:47], v233 offset:15360\n" \
"v_mov_b32 v192, s60\n" \
"v_lshlrev_b32 v193, 28, s61\n" \
"v_exp_f32 v34, v34\n" \
"v_exp_f32 v35, v35\n" \
"v_mfma_f32_16x16x32_bf16 v[96:99], a[204:207], v[152:155], v[196:199]\n" \
"buffer_load_dwordx4 v234, s[36:39], s71 offen offset:3072 sc1 lds\n" \
"v_mfma_f32_16x16x32_bf16 v[96:99], a[200:203], v[144:147], v[96:99]\n" \
"s_add_u32 m0, s79, 49152\n" \
"s_mov_b32 s90, 0\n" \
"ds_read_b128 a[40:43], v233 offset:7168\n" \
"v_mfma_f32_16x16x32_bf16 v[96:99], a[196:199], v[136:139], v[96:99]\n" \
"s_add_u32 s71, s76, 17408\n" \
"v_exp_f32 v36, v36\n" \
"v_cvt_pk_bf16_f32 v32, v32, v33\n" \
"v_mfma_f32_16x16x32_bf16 v[96:99], a[192:195], v[128:131], v[96:99]\n" \
"buffer_load_dwordx4 v234, s[36:39], s71 offen offset:0 sc1 lds\n" \
"v_mfma_f32_16x16x32_bf16 v[112:115], a[192:195], v[160:163], v[196:199]\n" \
"ds_read_b128 a[36:39], v233 offset:14336\n" \
"v_cvt_pk_bf16_f32 v33, v34, v35\n" \
"v_mfma_f32_16x16x32_bf16 v[112:115], a[196:199], v[168:171], v[112:115]\n" \
"v_exp_f32 v37, v37\n" \
"v_mfma_f32_16x16x32_bf16 v[112:115], a[200:203], v[176:179], v[112:115]\n" \
"buffer_load_dwordx4 v234, s[36:39], s71 offen offset:1024 sc1 lds\n" \
"v_mfma_f32_16x16x32_bf16 v[112:115], a[204:207], v[184:187], v[112:115]\n" \
"ds_read_b128 a[32:35], v233 offset:6144\n" \
"v_exp_f32 v38, v38\n" \
"v_exp_f32 v39, v39\n" \
"v_mfma_f32_16x16x32_bf16 v[64:67], a[204:207], a[140:143], v[208:211]\n" \
"v_exp_f32 v48, v48\n" \
"v_mfma_f32_16x16x32_bf16 v[64:67], a[200:203], a[136:139], v[64:67]\n" \
"buffer_load_dwordx4 v234, s[36:39], s71 offen offset:2048 sc1 lds\n" \
"v_cvt_pk_bf16_f32 v34, v36, v37\n" \
"v_mfma_f32_16x16x32_bf16 v[64:67], a[196:199], a[132:135], v[64:67]\n" \
"ds_read_b128 a[204:207], v233 offset:9216\n" \
"v_cvt_pk_bf16_f32 v35, v38, v39\n" \
"v_mfma_f32_16x16x32_bf16 v[64:67], a[192:195], a[128:131], v[64:67]\n" \
"v_exp_f32 v49, v49\n" \
"v_exp_f32 v50, v50\n" \
"v_mfma_f32_16x16x32_bf16 v[68:71], a[208:211], a[128:131], v[212:215]\n" \
"buffer_load_dwordx4 v234, s[36:39], s71 offen offset:3072 sc1 lds\n" \
"v_mfma_f32_16x16x32_bf16 v[68:71], a[212:215], a[132:135], v[68:71]\n" \
"ds_read_b128 a[200:203], v233 offset:1024\n" \
"v_cvt_pk_bf16_f32 v48, v48, v49\n" \
"v_mfma_f32_16x16x32_bf16 v[68:71], a[216:219], a[136:139], v[68:71]\n" \
"s_add_u32 m0, s77, s80\n" \
"v_exp_f32 v51, v51\n" \
"v_mfma_f32_16x16x32_bf16 v[68:71], a[220:223], a[140:143], v[68:71]\n" \
"buffer_load_dwordx4 v234, s[40:43], s76 offen offset:0 sc1 lds\n" \
"v_exp_f32 v52, v52\n" \
"v_exp_f32 v53, v53\n" \
"v_mfma_f32_16x16x32_bf16 v[116:119], a[220:223], v[184:187], v[196:199]\n" \
"ds_read_b128 a[196:199], v233 offset:8192\n" \
"v_cvt_pk_bf16_f32 v49, v50, v51\n" \
"v_mfma_f32_16x16x32_bf16 v[116:119], a[216:219], v[176:179], v[116:119]\n" \
"ds_read_b128 a[160:163], v239 offset:0\n" \
"v_mfma_f32_16x16x32_bf16 v[116:119], a[212:215], v[168:171], v[116:119]\n" \
"s_add_u32 s71, s76, 17408\n" \
"ds_read_b128 a[192:195], v233 offset:0\n" \
"v_exp_f32 v54, v54\n" \
"v_mfma_f32_16x16x32_bf16 v[116:119], a[208:211], v[160:163], v[116:119]\n" \
"buffer_load_dwordx4 v234, s[40:43], s76 offen offset:1024 sc1 lds\n" \
"v_mfma_f32_16x16x32_bf16 v[100:103], a[208:211], v[128:131], v[196:199]\n" \
"ds_read_b128 a[164:167], v239 offset:1024\n" \
"v_cvt_pk_bf16_f32 v50, v52, v53\n" \
"v_mfma_f32_16x16x32_bf16 v[100:103], a[212:215], v[136:139], v[100:103]\n" \
"v_exp_f32 v55, v55\n" \
"v_mfma_f32_16x16x32_bf16 v[100:103], a[216:219], v[144:147], v[100:103]\n" \
"v_cvt_pk_bf16_f32 v51, v54, v55\n" \
"v_exp_f32 v24, v24\n" \
"v_mfma_f32_16x16x32_bf16 v[100:103], a[220:223], v[152:155], v[100:103]\n" \
"buffer_load_dwordx4 v234, s[40:43], s76 offen offset:2048 sc1 lds\n" \
"v_exp_f32 v25, v25\n" \
"v_mfma_f32_16x16x32_bf16 v[84:87], a[220:223], a[156:159], v[196:199]\n" \
"ds_read_b128 a[168:171], v239 offset:2048\n" \
"v_mfma_f32_16x16x32_bf16 v[84:87], a[216:219], a[152:155], v[84:87]\n" \
"ds_read_b128 a[220:223], v233 offset:11264\n" \
"v_mfma_f32_16x16x32_bf16 v[84:87], a[212:215], a[148:151], v[84:87]\n" \
"s_waitcnt lgkmcnt(2)\n" \
"v_exp_f32 v26, v26\n" \
"v_mfma_f32_16x16x32_bf16 v[84:87], a[208:211], a[144:147], v[84:87]\n" \
"buffer_load_dwordx4 v234, s[40:43], s76 offen offset:3072 sc1 lds\n" \
"v_exp_f32 v27, v27\n" \
"v_mfma_f32_16x16x32_bf16 v[88:91], a[224:227], a[144:147], v[196:199]\n" \
"ds_read_b128 a[216:219], v233 offset:3072\n" \
"v_cvt_pk_bf16_f32 v20, v24, v25\n" \
"v_mfma_f32_16x16x32_bf16 v[88:91], a[228:231], a[148:151], v[88:91]\n" \
"ds_read_b128 a[172:175], v239 offset:3072\n" \
"v_exp_f32 v28, v28\n" \
"v_mfma_f32_16x16x32_bf16 v[88:91], a[232:235], a[152:155], v[88:91]\n" \
"s_add_u32 m0, m0, 0x4000\n" \
"ds_read_b128 a[212:215], v233 offset:10240\n" \
"v_xor_b32 v248, 0x2000, v239\n" \
"v_mfma_f32_16x16x32_bf16 v[88:91], a[236:239], a[156:159], v[88:91]\n" \
"buffer_load_dwordx4 v234, s[40:43], s71 offen offset:0 sc1 lds\n" \
"v_exp_f32 v29, v29\n" \
"v_mfma_f32_16x16x32_bf16 v[104:107], a[236:239], v[152:155], v[196:199]\n" \
"ds_read_b128 a[208:211], v233 offset:2048\n" \
"v_lshlrev_b32 v252, s48, v234\n" \
"v_mfma_f32_16x16x32_bf16 v[104:107], a[232:235], v[144:147], v[104:107]\n" \
"ds_read_b128 a[176:179], v239 offset:4096\n" \
"v_mov_b32 v224, 0\n" \
"v_mfma_f32_16x16x32_bf16 v[104:107], a[228:231], v[136:139], v[104:107]\n" \
"v_mov_b32 v225, 0\n" \
"v_mov_b32 v228, 0\n" \
"v_mfma_f32_16x16x32_bf16 v[104:107], a[224:227], v[128:131], v[104:107]\n" \
"buffer_load_dwordx4 v234, s[40:43], s71 offen offset:1024 sc1 lds\n" \
"v_exp_f32 v30, v30\n" \
"v_cvt_pk_bf16_f32 v21, v26, v27\n" \
"v_mfma_f32_16x16x32_bf16 v[120:123], a[224:227], v[160:163], v[196:199]\n" \
"ds_read_b128 a[180:183], v239 offset:5120\n" \
"v_mov_b32 v229, 0\n" \
"v_mfma_f32_16x16x32_bf16 v[120:123], a[228:231], v[168:171], v[120:123]\n" \
"v_exp_f32 v31, v31\n" \
"v_mfma_f32_16x16x32_bf16 v[120:123], a[232:235], v[176:179], v[120:123]\n" \
"v_exp_f32 v40, v40\n" \
"v_cvt_pk_bf16_f32 v22, v28, v29\n" \
"v_mfma_f32_16x16x32_bf16 v[120:123], a[236:239], v[184:187], v[120:123]\n" \
"buffer_load_dwordx4 v234, s[40:43], s71 offen offset:2048 sc1 lds\n" \
"v_exp_f32 v41, v41\n" \
"v_mfma_f32_16x16x32_bf16 v[72:75], a[236:239], a[140:143], v[216:219]\n" \
"ds_read_b128 a[184:187], v239 offset:6144\n" \
"v_cvt_pk_bf16_f32 v23, v30, v31\n" \
"v_mfma_f32_16x16x32_bf16 v[72:75], a[232:235], a[136:139], v[72:75]\n" \
"ds_read_b128 a[236:239], v233 offset:13312\n" \
"v_mfma_f32_16x16x32_bf16 v[72:75], a[228:231], a[132:135], v[72:75]\n" \
"v_exp_f32 v42, v42\n" \
"v_mfma_f32_16x16x32_bf16 v[72:75], a[224:227], a[128:131], v[72:75]\n" \
"buffer_load_dwordx4 v234, s[40:43], s71 offen offset:3072 sc1 lds\n" \
"v_exp_f32 v43, v43\n" \
"v_exp_f32 v44, v44\n" \
"v_mfma_f32_16x16x32_bf16 v[76:79], a[240:243], a[128:131], v[220:223]\n" \
"ds_read_b128 a[232:235], v233 offset:5120\n" \
"v_cvt_pk_bf16_f32 v36, v40, v41\n" \
"v_mfma_f32_16x16x32_bf16 v[76:79], a[244:247], a[132:135], v[76:79]\n" \
"ds_read_b128 a[188:191], v239 offset:7168\n" \
"v_cvt_pk_bf16_f32 v37, v42, v43\n" \
"v_mfma_f32_16x16x32_bf16 v[76:79], a[248:251], a[136:139], v[76:79]\n" \
"ds_read_b128 a[228:231], v233 offset:12288\n" \
"v_mfma_f32_16x16x32_bf16 v[76:79], a[252:255], a[140:143], v[76:79]\n" \
"v_exp_f32 v45, v45\n" \
"v_exp_f32 v46, v46\n" \
"v_mfma_f32_16x16x32_bf16 v[124:127], a[252:255], v[184:187], v[196:199]\n" \
"ds_read_b128 a[224:227], v233 offset:4096\n" \
"v_cvt_pk_bf16_f32 v38, v44, v45\n" \
"v_mfma_f32_16x16x32_bf16 v[124:127], a[248:251], v[176:179], v[124:127]\n" \
"v_exp_f32 v47, v47\n" \
"v_mfma_f32_16x16x32_bf16 v[124:127], a[244:247], v[168:171], v[124:127]\n" \
"v_exp_f32 v56, v56\n" \
"v_exp_f32 v57, v57\n" \
"v_mfma_f32_16x16x32_bf16 v[124:127], a[240:243], v[160:163], v[124:127]\n" \
"v_exp_f32 v58, v58\n" \
"v_cvt_pk_bf16_f32 v39, v46, v47\n" \
"v_mfma_f32_16x16x32_bf16 v[108:111], a[240:243], v[128:131], v[196:199]\n" \
"v_exp_f32 v59, v59\n" \
"v_mfma_f32_16x16x32_bf16 v[108:111], a[244:247], v[136:139], v[108:111]\n" \
"v_exp_f32 v60, v60\n" \
"v_mfma_f32_16x16x32_bf16 v[108:111], a[248:251], v[144:147], v[108:111]\n" \
"v_cvt_pk_bf16_f32 v52, v56, v57\n" \
"v_cvt_pk_bf16_f32 v53, v58, v59\n" \
"v_mfma_f32_16x16x32_bf16 v[108:111], a[252:255], v[152:155], v[108:111]\n" \
"v_exp_f32 v61, v61\n" \
"v_exp_f32 v62, v62\n" \
"v_exp_f32 v63, v63\n" \
"v_mfma_f32_16x16x32_bf16 v[92:95], a[252:255], a[156:159], v[196:199]\n" \
"v_add_u32 v232, s91, v237\n" \
"v_cvt_pk_bf16_f32 v54, v60, v61\n" \
"v_mfma_f32_16x16x32_bf16 v[92:95], a[248:251], a[152:155], v[92:95]\n" \
"v_cvt_pk_bf16_f32 v55, v62, v63\n" \
"v_mfma_f32_16x16x32_bf16 v[92:95], a[244:247], a[148:151], v[92:95]\n" \
"s_waitcnt vmcnt(8) lgkmcnt(0)\n" \
"s_barrier\n" \
"v_mfma_f32_16x16x32_bf16 v[92:95], a[240:243], a[144:147], v[92:95]\n" \
"buffer_store_dword v192, v193, s[56:59], s68 offen sc1\n" \
"v_mfma_f32_16x16x32_bf16 v[160:163], a[192:195], v[16:19], 0\n" \
"s_add_u32 m0, s74, s79\n" \
"ds_read_b128 a[240:243], v232 offset:8448\n" \
"v_xor_b32 v233, 0x4000, v233\n" \
"v_mfma_f32_16x16x32_bf16 v[160:163], a[196:199], v[20:23], v[160:163]\n" \
"buffer_load_dwordx4 v234, s[36:39], s73 offen offset:0 sc1 lds\n" \
"v_exp_f32 v80, v80\n" \
"v_mfma_f32_16x16x32_bf16 a[64:67], a[192:195], v[32:35], 0\n" \
"ds_read_b128 a[244:247], v232 offset:10496\n" \
"v_mfma_f32_16x16x32_bf16 a[64:67], a[196:199], v[36:39], a[64:67]\n" \
"ds_read_b128 a[0:3], v248 offset:0\n" \
"v_exp_f32 v81, v81\n" \
"v_mfma_f32_16x16x32_bf16 a[96:99], a[192:195], v[48:51], 0\n" \
"v_exp_f32 v82, v82\n" \
"v_mfma_f32_16x16x32_bf16 a[96:99], a[196:199], v[52:55], a[96:99]\n" \
"s_add_u32 s71, s73, 17408\n" \
"ds_read_b128 a[192:195], v232 offset:0\n" \
"v_cvt_pk_bf16_f32 v80, v80, v81\n" \
"v_mfma_f32_16x16x32_bf16 a[100:103], a[200:203], v[48:51], 0\n" \
"buffer_load_dwordx4 v234, s[36:39], s73 offen offset:1024 sc1 lds\n" \
"v_mfma_f32_16x16x32_bf16 a[100:103], a[204:207], v[52:55], a[100:103]\n" \
"ds_read_b128 a[196:199], v232 offset:2048\n" \
"v_exp_f32 v83, v83\n" \
"v_mfma_f32_16x16x32_bf16 a[68:71], a[200:203], v[32:35], 0\n" \
"ds_read_b128 a[4:7], v248 offset:1024\n" \
"v_mfma_f32_16x16x32_bf16 a[68:71], a[204:207], v[36:39], a[68:71]\n" \
"v_exp_f32 v84, v84\n" \
"v_exp_f32 v85, v85\n" \
"v_mfma_f32_16x16x32_bf16 v[164:167], a[200:203], v[16:19], 0\n" \
"ds_read_b128 a[8:11], v248 offset:2048\n" \
"v_cvt_pk_bf16_f32 v81, v82, v83\n" \
"v_mfma_f32_16x16x32_bf16 v[164:167], a[204:207], v[20:23], v[164:167]\n" \
"buffer_load_dwordx4 v234, s[36:39], s73 offen offset:2048 sc1 lds\n" \
"v_mfma_f32_16x16x32_bf16 v[168:171], a[208:211], v[16:19], 0\n" \
"ds_read_b128 a[200:203], v232 offset:4096\n" \
"v_mfma_f32_16x16x32_bf16 v[168:171], a[212:215], v[20:23], v[168:171]\n" \
"ds_read_b128 a[204:207], v232 offset:6144\n" \
"v_exp_f32 v86, v86\n" \
"v_mfma_f32_16x16x32_bf16 a[72:75], a[208:211], v[32:35], 0\n" \
"ds_read_b128 a[12:15], v248 offset:3072\n" \
"v_cvt_pk_bf16_f32 v82, v84, v85\n" \
"v_mfma_f32_16x16x32_bf16 a[72:75], a[212:215], v[36:39], a[72:75]\n" \
"v_exp_f32 v87, v87\n" \
"v_exp_f32 v96, v96\n" \
"v_mfma_f32_16x16x32_bf16 a[104:107], a[208:211], v[48:51], 0\n" \
"buffer_load_dwordx4 v234, s[36:39], s73 offen offset:3072 sc1 lds\n" \
"v_mfma_f32_16x16x32_bf16 a[104:107], a[212:215], v[52:55], a[104:107]\n" \
"ds_read_b128 a[208:211], v232 offset:256\n" \
"v_cvt_pk_bf16_f32 v83, v86, v87\n" \
"v_smfmac_f32_16x16x64_bf16 a[52:55], v[244:247], v[16:23], v243\n" \
"ds_read_b128 a[212:215], v232 offset:2304\n" \
"v_exp_f32 v97, v97\n" \
"v_mfma_f32_16x16x32_bf16 a[108:111], a[216:219], v[48:51], 0\n" \
"ds_read_b128 a[16:19], v248 offset:4096\n" \
"v_mfma_f32_16x16x32_bf16 a[108:111], a[220:223], v[52:55], a[108:111]\n" \
"s_add_u32 m0, m0, 0x4000\n" \
"v_exp_f32 v98, v98\n" \
"v_mfma_f32_16x16x32_bf16 a[76:79], a[216:219], v[32:35], 0\n" \
"buffer_load_dwordx4 v234, s[36:39], s71 offen offset:0 sc1 lds\n" \
"v_mfma_f32_16x16x32_bf16 a[76:79], a[220:223], v[36:39], a[76:79]\n" \
"ds_read_b128 a[20:23], v248 offset:5120\n" \
"v_cvt_pk_bf16_f32 v96, v96, v97\n" \
"v_exp_f32 v99, v99\n" \
"v_mfma_f32_16x16x32_bf16 v[172:175], a[216:219], v[16:19], 0\n" \
"v_exp_f32 v100, v100\n" \
"v_mfma_f32_16x16x32_bf16 v[172:175], a[220:223], v[20:23], v[172:175]\n" \
"ds_read_b128 a[216:219], v232 offset:4352\n" \
"v_cvt_pk_bf16_f32 v97, v98, v99\n" \
"v_mfma_f32_32x32x16_bf16 v[128:143], a[48:51], a[48:51], 0\n" \
"ds_read_b128 a[220:223], v232 offset:6400\n" \
"v_exp_f32 v101, v101\n" \
"v_mfma_f32_16x16x32_bf16 v[176:179], a[224:227], v[16:19], 0\n" \
"buffer_load_dwordx4 v234, s[36:39], s71 offen offset:1024 sc1 lds\n" \
"v_mfma_f32_16x16x32_bf16 v[176:179], a[228:231], v[20:23], v[176:179]\n" \
"ds_read_b128 a[24:27], v248 offset:6144\n" \
"v_exp_f32 v102, v102\n" \
"v_mfma_f32_16x16x32_bf16 a[80:83], a[224:227], v[32:35], 0\n" \
"v_exp_f32 v103, v103\n" \
"v_mfma_f32_16x16x32_bf16 a[80:83], a[228:231], v[36:39], a[80:83]\n" \
"ds_read_b128 a[28:31], v248 offset:7168\n" \
"v_cvt_pk_bf16_f32 v98, v100, v101\n" \
"v_exp_f32 v112, v112\n" \
"v_mfma_f32_16x16x32_bf16 a[112:115], a[224:227], v[48:51], 0\n" \
"v_exp_f32 v113, v113\n" \
"v_mfma_f32_16x16x32_bf16 a[112:115], a[228:231], v[52:55], a[112:115]\n" \
"buffer_load_dwordx4 v234, s[36:39], s71 offen offset:2048 sc1 lds\n" \
"v_smfmac_f32_16x16x64_bf16 v[224:227], v[244:247], v[32:39], v243\n" \
"ds_read_b128 a[224:227], v232 offset:8192\n" \
"v_cvt_pk_bf16_f32 v99, v102, v103\n" \
"v_mfma_f32_16x16x32_bf16 a[116:119], a[232:235], v[48:51], 0\n" \
"ds_read_b128 a[228:231], v232 offset:10240\n" \
"v_mfma_f32_16x16x32_bf16 a[116:119], a[236:239], v[52:55], a[116:119]\n" \
"v_exp_f32 v114, v114\n" \
"v_exp_f32 v115, v115\n" \
"v_mfma_f32_16x16x32_bf16 a[84:87], a[232:235], v[32:35], 0\n" \
"v_exp_f32 v116, v116\n" \
"v_mfma_f32_16x16x32_bf16 a[84:87], a[236:239], v[36:39], a[84:87]\n" \
"buffer_load_dwordx4 v234, s[36:39], s71 offen offset:3072 sc1 lds\n" \
"v_cvt_pk_bf16_f32 v112, v112, v113\n" \
"v_mfma_f32_16x16x32_bf16 v[180:183], a[232:235], v[16:19], 0\n" \
"v_exp_f32 v117, v117\n" \
"v_mfma_f32_16x16x32_bf16 v[180:183], a[236:239], v[20:23], v[180:183]\n" \
"s_mov_b32 s76, s73\n" \
"s_add_u32 s73, s73, 34816\n" \
"ds_read_b128 a[232:235], v232 offset:12288\n" \
"v_mfma_f32_32x32x16_bf16 v[144:159], a[48:51], a[48:51], 0\n" \
"s_cmp_eq_u32 s73, s83\n" \
"s_cselect_b32 s70, 34816, 0\n" \
"ds_read_b128 a[236:239], v232 offset:14336\n" \
"v_exp_f32 v118, v118\n" \
"v_mfma_f32_16x16x32_bf16 v[184:187], a[32:35], v[16:19], 0\n" \
"s_add_u32 s73, s73, s70\n" \
"s_xor_b32 s74, s74, 0x8000\n" \
"ds_read_b128 a[248:251], v232 offset:12544\n" \
"v_mfma_f32_16x16x32_bf16 v[184:187], a[36:39], v[20:23], v[184:187]\n" \
"s_xor_b32 s77, s77, 0x8000\n" \
"ds_read_b128 a[252:255], v232 offset:14592\n" \
"v_cvt_pk_bf16_f32 v113, v114, v115\n" \
"v_mfma_f32_16x16x32_bf16 a[88:91], a[32:35], v[32:35], 0\n" \
"v_exp_f32 v119, v119\n" \
"v_mfma_f32_16x16x32_bf16 a[88:91], a[36:39], v[36:39], a[88:91]\n" \
"v_exp_f32 v64, v64\n" \
"v_cvt_pk_bf16_f32 v114, v116, v117\n" \
"v_mfma_f32_16x16x32_bf16 a[120:123], a[32:35], v[48:51], 0\n" \
"v_exp_f32 v65, v65\n" \
"v_mfma_f32_16x16x32_bf16 a[120:123], a[36:39], v[52:55], a[120:123]\n" \
"v_exp_f32 v66, v66\n" \
"v_cvt_pk_bf16_f32 v115, v118, v119\n" \
"v_smfmac_f32_16x16x64_bf16 v[228:231], v[244:247], v[48:55], v243\n" \
"v_cvt_pk_bf16_f32 v64, v64, v65\n" \
"v_mfma_f32_16x16x32_bf16 a[124:127], a[40:43], v[48:51], 0\n" \
"v_exp_f32 v67, v67\n" \
"v_mfma_f32_16x16x32_bf16 a[124:127], a[44:47], v[52:55], a[124:127]\n" \
"v_exp_f32 v68, v68\n" \
"v_exp_f32 v69, v69\n" \
"v_mfma_f32_16x16x32_bf16 a[92:95], a[40:43], v[32:35], 0\n" \
"v_exp_f32 v70, v70\n" \
"v_mfma_f32_16x16x32_bf16 a[92:95], a[44:47], v[36:39], a[92:95]\n" \
"v_cvt_pk_bf16_f32 v65, v66, v67\n" \
"v_cvt_pk_bf16_f32 v66, v68, v69\n" \
"v_mfma_f32_16x16x32_bf16 v[188:191], a[40:43], v[16:19], 0\n" \
"s_cmp_eq_u32 s63, 0\n" \
"v_exp_f32_e64 v71, v71\n" \
"v_mfma_f32_16x16x32_bf16 v[188:191], a[44:47], v[20:23], v[188:191]\n" \
"s_waitcnt lgkmcnt(0)\n" \
"v_cvt_pk_bf16_f32 v67, v70, v71\n" \
"s_cbranch_scc1 Lloop2_%=\n" \
"Lloop_%=:\n" \
"v_mfma_f32_16x16x32_bf16 v[0:3], a[192:195], a[128:131], v[196:199]\n" \
"v_exp_f32 v72, v72\n" \
"v_mfma_f32_16x16x32_bf16 v[0:3], a[196:199], a[132:135], v[0:3]\n" \
"v_mfma_f32_16x16x32_bf16 v[0:3], a[200:203], a[136:139], v[0:3]\n" \
"v_exp_f32 v73, v73\n" \
"v_mfma_f32_16x16x32_bf16 v[0:3], a[204:207], a[140:143], v[0:3]\n" \
"ds_read_b128 a[32:35], v233 offset:6144\n" \
"v_cvt_pk_bf16_f32 v68, v72, v73\n" \
"v_mfma_f32_16x16x32_bf16 v[16:19], a[204:207], a[156:159], v[196:199]\n" \
"v_exp_f32 v74, v74\n" \
"v_mfma_f32_16x16x32_bf16 v[16:19], a[200:203], a[152:155], v[16:19]\n" \
"global_load_dword v253, v252, s[44:45]\n" \
"v_mfma_f32_16x16x32_bf16 v[16:19], a[196:199], a[148:151], v[16:19]\n" \
"s_sub_u32 s46, s46, 1\n" \
"v_exp_f32 v75, v75\n" \
"v_mfma_f32_16x16x32_bf16 v[16:19], a[192:195], a[144:147], v[16:19]\n" \
"ds_read_b128 a[36:39], v233 offset:14336\n" \
"v_cvt_pk_bf16_f32 v69, v74, v75\n" \
"v_mfma_f32_16x16x32_bf16 v[32:35], a[192:195], a[160:163], v[196:199]\n" \
"v_exp_f32 v76, v76\n" \
"v_mfma_f32_16x16x32_bf16 v[32:35], a[196:199], a[164:167], v[32:35]\n" \
"s_cmp_gt_i32 s46, 0\n" \
"s_cselect_b32 s47, 0x100000, 0\n" \
"v_mfma_f32_16x16x32_bf16 v[32:35], a[200:203], a[168:171], v[32:35]\n" \
"v_exp_f32 v77, v77\n" \
"v_mfma_f32_16x16x32_bf16 v[32:35], a[204:207], a[172:175], v[32:35]\n" \
"s_add_u32 s44, s44, s47\n" \
"s_addc_u32 s45, s45, 0\n" \
"ds_read_b128 a[40:43], v233 offset:7168\n" \
"v_cvt_pk_bf16_f32 v70, v76, v77\n" \
"v_mfma_f32_16x16x32_bf16 v[48:51], a[204:207], a[188:191], v[196:199]\n" \
"v_exp_f32 v78, v78\n" \
"v_mfma_f32_16x16x32_bf16 v[48:51], a[200:203], a[184:187], v[48:51]\n" \
"v_xor_b32 v232, 0x4000, v232\n" \
"v_mfma_f32_16x16x32_bf16 v[48:51], a[196:199], a[180:183], v[48:51]\n" \
"v_exp_f32 v79, v79\n" \
"v_mfma_f32_16x16x32_bf16 v[48:51], a[192:195], a[176:179], v[48:51]\n" \
"ds_read_b128 a[204:207], v233 offset:9216\n" \
"v_cvt_pk_bf16_f32 v71, v78, v79\n" \
"v_mfma_f32_16x16x32_bf16 v[52:55], a[208:211], a[176:179], v[196:199]\n" \
"v_exp_f32 v88, v88\n" \
"v_mfma_f32_16x16x32_bf16 v[52:55], a[212:215], a[180:183], v[52:55]\n" \
"v_mfma_f32_16x16x32_bf16 v[52:55], a[216:219], a[184:187], v[52:55]\n" \
"v_exp_f32 v89, v89\n" \
"v_mfma_f32_16x16x32_bf16 v[52:55], a[220:223], a[188:191], v[52:55]\n" \
"ds_read_b128 a[200:203], v233 offset:1024\n" \
"v_cvt_pk_bf16_f32 v84, v88, v89\n" \
"v_mfma_f32_16x16x32_bf16 v[36:39], a[220:223], a[172:175], v[196:199]\n" \
"v_exp_f32 v90, v90\n" \
"v_mfma_f32_16x16x32_bf16 v[36:39], a[216:219], a[168:171], v[36:39]\n" \
"v_mfma_f32_16x16x32_bf16 v[36:39], a[212:215], a[164:167], v[36:39]\n" \
"v_exp_f32 v91, v91\n" \
"v_mfma_f32_16x16x32_bf16 v[36:39], a[208:211], a[160:163], v[36:39]\n" \
"ds_read_b128 a[196:199], v233 offset:8192\n" \
"v_cvt_pk_bf16_f32 v85, v90, v91\n" \
"v_mfma_f32_16x16x32_bf16 v[20:23], a[208:211], a[144:147], v[196:199]\n" \
"v_exp_f32 v92, v92\n" \
"v_mfma_f32_16x16x32_bf16 v[20:23], a[212:215], a[148:151], v[20:23]\n" \
"v_mfma_f32_16x16x32_bf16 v[20:23], a[216:219], a[152:155], v[20:23]\n" \
"v_exp_f32 v93, v93\n" \
"v_mfma_f32_16x16x32_bf16 v[20:23], a[220:223], a[156:159], v[20:23]\n" \
"ds_read_b128 a[192:195], v233 offset:0\n" \
"v_cvt_pk_bf16_f32 v86, v92, v93\n" \
"v_mfma_f32_16x16x32_bf16 v[4:7], a[220:223], a[140:143], v[196:199]\n" \
"v_exp_f32 v94, v94\n" \
"v_mfma_f32_16x16x32_bf16 v[4:7], a[216:219], a[136:139], v[4:7]\n" \
"v_mfma_f32_16x16x32_bf16 v[4:7], a[212:215], a[132:135], v[4:7]\n" \
"s_waitcnt lgkmcnt(7)\n" \
"v_exp_f32 v95, v95\n" \
"v_mfma_f32_16x16x32_bf16 v[4:7], a[208:211], a[128:131], v[4:7]\n" \
"ds_read_b128 a[220:223], v233 offset:11264\n" \
"v_cvt_pk_bf16_f32 v87, v94, v95\n" \
"v_mfma_f32_16x16x32_bf16 v[8:11], a[224:227], a[128:131], v[196:199]\n" \
"v_exp_f32 v104, v104\n" \
"v_mfma_f32_16x16x32_bf16 v[8:11], a[228:231], a[132:135], v[8:11]\n" \
"v_mfma_f32_16x16x32_bf16 v[8:11], a[232:235], a[136:139], v[8:11]\n" \
"v_exp_f32 v105, v105\n" \
"v_mfma_f32_16x16x32_bf16 v[8:11], a[236:239], a[140:143], v[8:11]\n" \
"ds_read_b128 a[216:219], v233 offset:3072\n" \
"v_cvt_pk_bf16_f32 v100, v104, v105\n" \
"v_mfma_f32_16x16x32_bf16 v[24:27], a[236:239], a[156:159], v[196:199]\n" \
"v_exp_f32 v106, v106\n" \
"v_mfma_f32_16x16x32_bf16 v[24:27], a[232:235], a[152:155], v[24:27]\n" \
"v_mfma_f32_16x16x32_bf16 v[24:27], a[228:231], a[148:151], v[24:27]\n" \
"v_exp_f32 v107, v107\n" \
"v_mfma_f32_16x16x32_bf16 v[24:27], a[224:227], a[144:147], v[24:27]\n" \
"ds_read_b128 a[212:215], v233 offset:10240\n" \
"v_cvt_pk_bf16_f32 v101, v106, v107\n" \
"v_mfma_f32_16x16x32_bf16 v[40:43], a[224:227], a[160:163], v[196:199]\n" \
"v_exp_f32 v108, v108\n" \
"v_mfma_f32_16x16x32_bf16 v[40:43], a[228:231], a[164:167], v[40:43]\n" \
"v_mfma_f32_16x16x32_bf16 v[40:43], a[232:235], a[168:171], v[40:43]\n" \
"v_exp_f32 v109, v109\n" \
"v_mfma_f32_16x16x32_bf16 v[40:43], a[236:239], a[172:175], v[40:43]\n" \
"ds_read_b128 a[208:211], v233 offset:2048\n" \
"v_cvt_pk_bf16_f32 v102, v108, v109\n" \
"v_mfma_f32_16x16x32_bf16 v[56:59], a[236:239], a[188:191], v[196:199]\n" \
"v_exp_f32 v110, v110\n" \
"v_mfma_f32_16x16x32_bf16 v[56:59], a[232:235], a[184:187], v[56:59]\n" \
"v_mfma_f32_16x16x32_bf16 v[56:59], a[228:231], a[180:183], v[56:59]\n" \
"v_exp_f32 v111, v111\n" \
"v_mfma_f32_16x16x32_bf16 v[56:59], a[224:227], a[176:179], v[56:59]\n" \
"ds_read_b128 a[236:239], v233 offset:13312\n" \
"v_cvt_pk_bf16_f32 v103, v110, v111\n" \
"v_mfma_f32_16x16x32_bf16 v[60:63], a[240:243], a[176:179], v[196:199]\n" \
"v_exp_f32 v120, v120\n" \
"v_mfma_f32_16x16x32_bf16 v[60:63], a[244:247], a[180:183], v[60:63]\n" \
"v_mfma_f32_16x16x32_bf16 v[60:63], a[248:251], a[184:187], v[60:63]\n" \
"v_exp_f32 v121, v121\n" \
"v_mfma_f32_16x16x32_bf16 v[60:63], a[252:255], a[188:191], v[60:63]\n" \
"ds_read_b128 a[232:235], v233 offset:5120\n" \
"v_cvt_pk_bf16_f32 v116, v120, v121\n" \
"v_mfma_f32_16x16x32_bf16 v[44:47], a[252:255], a[172:175], v[196:199]\n" \
"v_exp_f32 v122, v122\n" \
"v_mfma_f32_16x16x32_bf16 v[44:47], a[248:251], a[168:171], v[44:47]\n" \
"v_mfma_f32_16x16x32_bf16 v[44:47], a[244:247], a[164:167], v[44:47]\n" \
"v_exp_f32 v123, v123\n" \
"v_mfma_f32_16x16x32_bf16 v[44:47], a[240:243], a[160:163], v[44:47]\n" \
"ds_read_b128 a[228:231], v233 offset:12288\n" \
"v_cvt_pk_bf16_f32 v117, v122, v123\n" \
"v_mfma_f32_16x16x32_bf16 v[28:31], a[240:243], a[144:147], v[196:199]\n" \
"v_exp_f32 v124, v124\n" \
"v_mfma_f32_16x16x32_bf16 v[28:31], a[244:247], a[148:151], v[28:31]\n" \
"v_mfma_f32_16x16x32_bf16 v[28:31], a[248:251], a[152:155], v[28:31]\n" \
"v_exp_f32 v125, v125\n" \
"v_mfma_f32_16x16x32_bf16 v[28:31], a[252:255], a[156:159], v[28:31]\n" \
"ds_read_b128 a[224:227], v233 offset:4096\n" \
"v_cvt_pk_bf16_f32 v118, v124, v125\n" \
"v_mfma_f32_16x16x32_bf16 v[12:15], a[252:255], a[140:143], v[196:199]\n" \
"v_exp_f32 v126, v126\n" \
"v_mfma_f32_16x16x32_bf16 v[12:15], a[248:251], a[136:139], v[12:15]\n" \
"Lpg5598_%=:\n" \
"s_cbranch_execz Lpg6789_%=\n" \
"v_mfma_f32_16x16x32_bf16 v[12:15], a[244:247], a[132:135], v[12:15]\n" \
"s_waitcnt lgkmcnt(2)\n" \
"v_exp_f32 v127, v127\n" \
"v_mfma_f32_16x16x32_bf16 v[12:15], a[240:243], a[128:131], v[12:15]\n" \
"ds_read_b128 a[44:47], v233 offset:15360\n" \
"v_cvt_pk_bf16_f32 v119, v126, v127\n" \
"v_mfma_f32_16x16x32_bf16 v[128:131], a[192:195], v[64:67], v[128:131]\n" \
"v_exp_f32 v0, v0\n" \
"v_mfma_f32_16x16x32_bf16 v[128:131], a[196:199], v[68:71], v[128:131]\n" \
"ds_read_b128 a[244:247], v232 offset:10496\n" \
"v_mfma_f32_16x16x32_bf16 v[160:163], a[192:195], v[80:83], v[160:163]\n" \
"v_exp_f32 v1, v1\n" \
"v_mfma_f32_16x16x32_bf16 v[160:163], a[196:199], v[84:87], v[160:163]\n" \
"ds_read_b128 a[240:243], v232 offset:8448\n" \
"v_cvt_pk_bf16_f32 v0, v0, v1\n" \
"v_mfma_f32_16x16x32_bf16 a[64:67], a[192:195], v[96:99], a[64:67]\n" \
"v_exp_f32 v2, v2\n" \
"v_mfma_f32_16x16x32_bf16 a[64:67], a[196:199], v[100:103], a[64:67]\n" \
"ds_read_b128 a[248:251], v232 offset:12544\n" \
"v_xor_b32 v233, 0xc000, v233\n" \
"v_mfma_f32_16x16x32_bf16 a[96:99], a[192:195], v[112:115], a[96:99]\n" \
"v_exp_f32 v3, v3\n" \
"v_mfma_f32_16x16x32_bf16 a[96:99], a[196:199], v[116:119], a[96:99]\n" \
"ds_read_b128 a[192:195], v232 offset:0\n" \
"v_cvt_pk_bf16_f32 v1, v2, v3\n" \
"v_mfma_f32_16x16x32_bf16 a[100:103], a[200:203], v[112:115], a[100:103]\n" \
"v_exp_f32 v4, v4\n" \
"v_mfma_f32_16x16x32_bf16 a[100:103], a[204:207], v[116:119], a[100:103]\n" \
"ds_read_b128 a[252:255], v232 offset:14592\n" \
"v_mfma_f32_16x16x32_bf16 a[68:71], a[200:203], v[96:99], a[68:71]\n" \
"v_exp_f32 v5, v5\n" \
"v_mfma_f32_16x16x32_bf16 a[68:71], a[204:207], v[100:103], a[68:71]\n" \
"ds_read_b128 a[196:199], v232 offset:2048\n" \
"v_cvt_pk_bf16_f32 v2, v4, v5\n" \
"v_smfmac_f32_16x16x64_bf16 a[48:51], v[244:247], v[64:71], v243\n" \
"v_mfma_f32_16x16x32_bf16 v[164:167], a[200:203], v[80:83], v[164:167]\n" \
"v_exp_f32 v6, v6\n" \
"v_mfma_f32_16x16x32_bf16 v[164:167], a[204:207], v[84:87], v[164:167]\n" \
"v_mfma_f32_16x16x32_bf16 v[132:135], a[200:203], v[64:67], v[132:135]\n" \
"v_exp_f32 v7, v7\n" \
"v_mfma_f32_16x16x32_bf16 v[132:135], a[204:207], v[68:71], v[132:135]\n" \
"ds_read_b128 a[200:203], v232 offset:4096\n" \
"v_cvt_pk_bf16_f32 v3, v6, v7\n" \
"v_mfma_f32_16x16x32_bf16 v[136:139], a[208:211], v[64:67], v[136:139]\n" \
"v_exp_f32 v16, v16\n" \
"v_mfma_f32_16x16x32_bf16 v[136:139], a[212:215], v[68:71], v[136:139]\n" \
"v_mfma_f32_16x16x32_bf16 v[168:171], a[208:211], v[80:83], v[168:171]\n" \
"v_exp_f32 v17, v17\n" \
"v_mfma_f32_16x16x32_bf16 v[168:171], a[212:215], v[84:87], v[168:171]\n" \
"ds_read_b128 a[204:207], v232 offset:6144\n" \
"v_cvt_pk_bf16_f32 v16, v16, v17\n" \
"v_mfma_f32_16x16x32_bf16 a[72:75], a[208:211], v[96:99], a[72:75]\n" \
"v_exp_f32 v18, v18\n" \
"v_mfma_f32_16x16x32_bf16 a[72:75], a[212:215], v[100:103], a[72:75]\n" \
"v_mfma_f32_16x16x32_bf16 a[104:107], a[208:211], v[112:115], a[104:107]\n" \
"v_exp_f32 v19, v19\n" \
"v_mfma_f32_16x16x32_bf16 a[104:107], a[212:215], v[116:119], a[104:107]\n" \
"ds_read_b128 a[208:211], v232 offset:256\n" \
"v_cvt_pk_bf16_f32 v17, v18, v19\n" \
"v_mfma_f32_16x16x32_bf16 a[108:111], a[216:219], v[112:115], a[108:111]\n" \
"v_exp_f32 v20, v20\n" \
"v_mfma_f32_16x16x32_bf16 a[108:111], a[220:223], v[116:119], a[108:111]\n" \
"v_mfma_f32_16x16x32_bf16 a[76:79], a[216:219], v[96:99], a[76:79]\n" \
"v_exp_f32 v21, v21\n" \
"v_mfma_f32_16x16x32_bf16 a[76:79], a[220:223], v[100:103], a[76:79]\n" \
"ds_read_b128 a[212:215], v232 offset:2304\n" \
"v_cvt_pk_bf16_f32 v18, v20, v21\n" \
"v_smfmac_f32_16x16x64_bf16 a[52:55], v[244:247], v[80:87], v243\n" \
"v_mfma_f32_16x16x32_bf16 v[172:175], a[216:219], v[80:83], v[172:175]\n" \
"v_exp_f32 v22, v22\n" \
"v_mfma_f32_16x16x32_bf16 v[172:175], a[220:223], v[84:87], v[172:175]\n" \
"v_mfma_f32_16x16x32_bf16 v[140:143], a[216:219], v[64:67], v[140:143]\n" \
"s_waitcnt lgkmcnt(10)\n" \
"v_exp_f32 v23, v23\n" \
"v_mfma_f32_16x16x32_bf16 v[140:143], a[220:223], v[68:71], v[140:143]\n" \
"ds_read_b128 a[216:219], v232 offset:4352\n" \
"v_cvt_pk_bf16_f32 v19, v22, v23\n" \
"v_mfma_f32_16x16x32_bf16 v[144:147], a[224:227], v[64:67], v[144:147]\n" \
"v_exp_f32 v32, v32\n" \
"v_mfma_f32_16x16x32_bf16 v[144:147], a[228:231], v[68:71], v[144:147]\n" \
"v_mfma_f32_16x16x32_bf16 v[176:179], a[224:227], v[80:83], v[176:179]\n" \
"v_exp_f32 v33, v33\n" \
"v_mfma_f32_16x16x32_bf16 v[176:179], a[228:231], v[84:87], v[176:179]\n" \
"ds_read_b128 a[220:223], v232 offset:6400\n" \
"v_cvt_pk_bf16_f32 v32, v32, v33\n" \
"v_mfma_f32_16x16x32_bf16 a[80:83], a[224:227], v[96:99], a[80:83]\n" \
"v_exp_f32 v34, v34\n" \
"v_mfma_f32_16x16x32_bf16 a[80:83], a[228:231], v[100:103], a[80:83]\n" \
"v_mfma_f32_16x16x32_bf16 a[112:115], a[224:227], v[112:115], a[112:115]\n" \
"v_exp_f32 v35, v35\n" \
"v_mfma_f32_16x16x32_bf16 a[112:115], a[228:231], v[116:119], a[112:115]\n" \
"ds_read_b128 a[224:227], v232 offset:8192\n" \
"v_cvt_pk_bf16_f32 v33, v34, v35\n" \
"v_mfma_f32_16x16x32_bf16 a[116:119], a[232:235], v[112:115], a[116:119]\n" \
"v_exp_f32 v36, v36\n" \
"v_mfma_f32_16x16x32_bf16 a[116:119], a[236:239], v[116:119], a[116:119]\n" \
"v_mfma_f32_16x16x32_bf16 a[84:87], a[232:235], v[96:99], a[84:87]\n" \
"v_exp_f32 v37, v37\n" \
"v_mfma_f32_16x16x32_bf16 a[84:87], a[236:239], v[100:103], a[84:87]\n" \
"ds_read_b128 a[228:231], v232 offset:10240\n" \
"v_cvt_pk_bf16_f32 v34, v36, v37\n" \
"v_smfmac_f32_16x16x64_bf16 v[224:227], v[244:247], v[96:103], v243\n" \
"v_mfma_f32_16x16x32_bf16 v[180:183], a[232:235], v[80:83], v[180:183]\n" \
"v_exp_f32 v38, v38\n" \
"v_mfma_f32_16x16x32_bf16 v[180:183], a[236:239], v[84:87], v[180:183]\n" \
"v_mfma_f32_16x16x32_bf16 v[148:151], a[232:235], v[64:67], v[148:151]\n" \
"v_exp_f32 v39, v39\n" \
"v_mfma_f32_16x16x32_bf16 v[148:151], a[236:239], v[68:71], v[148:151]\n" \
"ds_read_b128 a[232:235], v232 offset:12288\n" \
"v_cvt_pk_bf16_f32 v35, v38, v39\n" \
"v_mfma_f32_16x16x32_bf16 v[152:155], a[32:35], v[64:67], v[152:155]\n" \
"v_exp_f32 v48, v48\n" \
"v_mfma_f32_16x16x32_bf16 v[152:155], a[36:39], v[68:71], v[152:155]\n" \
"v_mfma_f32_16x16x32_bf16 v[184:187], a[32:35], v[80:83], v[184:187]\n" \
"v_exp_f32 v49, v49\n" \
"v_mfma_f32_16x16x32_bf16 v[184:187], a[36:39], v[84:87], v[184:187]\n" \
"ds_read_b128 a[236:239], v232 offset:14336\n" \
"v_cvt_pk_bf16_f32 v48, v48, v49\n" \
"v_mfma_f32_16x16x32_bf16 a[88:91], a[32:35], v[96:99], a[88:91]\n" \
"v_exp_f32 v50, v50\n" \
"v_mfma_f32_16x16x32_bf16 a[88:91], a[36:39], v[100:103], a[88:91]\n" \
"v_mfma_f32_16x16x32_bf16 a[120:123], a[32:35], v[112:115], a[120:123]\n" \
"v_exp_f32 v51, v51\n" \
"v_mfma_f32_16x16x32_bf16 a[120:123], a[36:39], v[116:119], a[120:123]\n" \
"v_cvt_pk_bf16_f32 v49, v50, v51\n" \
"v_mfma_f32_16x16x32_bf16 a[124:127], a[40:43], v[112:115], a[124:127]\n" \
"v_exp_f32 v52, v52\n" \
"v_mfma_f32_16x16x32_bf16 a[124:127], a[44:47], v[116:119], a[124:127]\n" \
"v_mfma_f32_16x16x32_bf16 a[92:95], a[40:43], v[96:99], a[92:95]\n" \
"v_exp_f32 v53, v53\n" \
"v_mfma_f32_16x16x32_bf16 a[92:95], a[44:47], v[100:103], a[92:95]\n" \
"v_cvt_pk_bf16_f32 v50, v52, v53\n" \
"v_smfmac_f32_16x16x64_bf16 v[228:231], v[244:247], v[112:119], v243\n" \
"v_mfma_f32_16x16x32_bf16 v[188:191], a[40:43], v[80:83], v[188:191]\n" \
"v_exp_f32 v54, v54\n" \
"v_mfma_f32_16x16x32_bf16 v[188:191], a[44:47], v[84:87], v[188:191]\n" \
"v_mfma_f32_16x16x32_bf16 v[156:159], a[40:43], v[64:67], v[156:159]\n" \
"s_waitcnt vmcnt(1) lgkmcnt(0)\n" \
"v_exp_f32 v55, v55\n" \
"v_mfma_f32_16x16x32_bf16 v[156:159], a[44:47], v[68:71], v[156:159]\n" \
"s_barrier\n" \
"v_cvt_pk_bf16_f32 v51, v54, v55\n" \
"v_mfma_f32_16x16x32_bf16 v[64:67], a[192:195], a[128:131], v[196:199]\n" \
"v_exp_f32 v8, v8\n" \
"v_mfma_f32_16x16x32_bf16 v[64:67], a[196:199], a[132:135], v[64:67]\n" \
"v_cmp_eq_u32 vcc, s60, v239\n" \
"v_mfma_f32_16x16x32_bf16 v[64:67], a[200:203], a[136:139], v[64:67]\n" \
"v_exp_f32_e64 v9, v9\n" \
"v_mfma_f32_16x16x32_bf16 v[64:67], a[204:207], a[140:143], v[64:67]\n" \
"s_andn2_b32 s70, s69, vcc_lo\n" \
"s_cbranch_scc1 Lpl2_%=\n" \
"Lpr2_%=:\n" \
"ds_read_b128 a[32:35], v233 offset:6144\n" \
"v_cvt_pk_bf16_f32 v4, v8, v9\n" \
"v_mfma_f32_16x16x32_bf16 v[80:83], a[204:207], a[156:159], v[196:199]\n" \
"s_add_u32 m0, s77, s80\n" \
"v_exp_f32 v10, v10\n" \
"v_mfma_f32_16x16x32_bf16 v[80:83], a[200:203], a[152:155], v[80:83]\n" \
"buffer_load_dwordx4 v234, s[40:43], s76 offen offset:0 sc1 lds\n" \
"v_mfma_f32_16x16x32_bf16 v[80:83], a[196:199], a[148:151], v[80:83]\n" \
"s_sub_u32 s89, s89, 1\n" \
"v_exp_f32 v11, v11\n" \
"v_mfma_f32_16x16x32_bf16 v[80:83], a[192:195], a[144:147], v[80:83]\n" \
"s_cmp_eq_u32 s89, 0\n" \
"s_cselect_b32 s70, s78, 0xc000\n" \
"ds_read_b128 a[36:39], v233 offset:14336\n" \
"v_cvt_pk_bf16_f32 v5, v10, v11\n" \
"v_mfma_f32_16x16x32_bf16 v[96:99], a[192:195], a[160:163], v[196:199]\n" \
"s_add_u32 s71, s76, 17408\n" \
"v_exp_f32 v12, v12\n" \
"v_mfma_f32_16x16x32_bf16 v[96:99], a[196:199], a[164:167], v[96:99]\n" \
"buffer_load_dwordx4 v234, s[40:43], s76 offen offset:1024 sc1 lds\n" \
"v_mfma_f32_16x16x32_bf16 v[96:99], a[200:203], a[168:171], v[96:99]\n" \
"v_exp_f32 v13, v13\n" \
"v_mfma_f32_16x16x32_bf16 v[96:99], a[204:207], a[172:175], v[96:99]\n" \
"ds_read_b128 a[40:43], v233 offset:7168\n" \
"v_cvt_pk_bf16_f32 v6, v12, v13\n" \
"v_mfma_f32_16x16x32_bf16 v[112:115], a[204:207], a[188:191], v[196:199]\n" \
"v_exp_f32 v14, v14\n" \
"v_mfma_f32_16x16x32_bf16 v[112:115], a[200:203], a[184:187], v[112:115]\n" \
"v_xor_b32 v232, s70, v232\n" \
"v_mfma_f32_16x16x32_bf16 v[112:115], a[196:199], a[180:183], v[112:115]\n" \
"v_exp_f32 v15, v15\n" \
"v_mfma_f32_16x16x32_bf16 v[112:115], a[192:195], a[176:179], v[112:115]\n" \
"ds_read_b128 a[204:207], v233 offset:9216\n" \
"v_cvt_pk_bf16_f32 v7, v14, v15\n" \
"v_mfma_f32_16x16x32_bf16 v[116:119], a[208:211], a[176:179], v[196:199]\n" \
"v_exp_f32 v24, v24\n" \
"v_mfma_f32_16x16x32_bf16 v[116:119], a[212:215], a[180:183], v[116:119]\n" \
"buffer_load_dwordx4 v234, s[40:43], s76 offen offset:2048 sc1 lds\n" \
"v_mfma_f32_16x16x32_bf16 v[116:119], a[216:219], a[184:187], v[116:119]\n" \
"v_exp_f32 v25, v25\n" \
"v_mfma_f32_16x16x32_bf16 v[116:119], a[220:223], a[188:191], v[116:119]\n" \
"ds_read_b128 a[200:203], v233 offset:1024\n" \
"v_cvt_pk_bf16_f32 v20, v24, v25\n" \
"v_mfma_f32_16x16x32_bf16 v[100:103], a[220:223], a[172:175], v[196:199]\n" \
"v_exp_f32 v26, v26\n" \
"v_mfma_f32_16x16x32_bf16 v[100:103], a[216:219], a[168:171], v[100:103]\n" \
"buffer_load_dwordx4 v234, s[40:43], s76 offen offset:3072 sc1 lds\n" \
"v_mfma_f32_16x16x32_bf16 v[100:103], a[212:215], a[164:167], v[100:103]\n" \
"v_exp_f32 v27, v27\n" \
"v_mfma_f32_16x16x32_bf16 v[100:103], a[208:211], a[160:163], v[100:103]\n" \
"ds_read_b128 a[196:199], v233 offset:8192\n" \
"v_cvt_pk_bf16_f32 v21, v26, v27\n" \
"v_mfma_f32_16x16x32_bf16 v[84:87], a[208:211], a[144:147], v[196:199]\n" \
"v_exp_f32 v28, v28\n" \
"v_mfma_f32_16x16x32_bf16 v[84:87], a[212:215], a[148:151], v[84:87]\n" \
"v_mfma_f32_16x16x32_bf16 v[84:87], a[216:219], a[152:155], v[84:87]\n" \
"v_exp_f32 v29, v29\n" \
"v_mfma_f32_16x16x32_bf16 v[84:87], a[220:223], a[156:159], v[84:87]\n" \
"ds_read_b128 a[192:195], v233 offset:0\n" \
"v_cvt_pk_bf16_f32 v22, v28, v29\n" \
"v_mfma_f32_16x16x32_bf16 v[68:71], a[220:223], a[140:143], v[196:199]\n" \
"s_add_u32 m0, m0, 0x4000\n" \
"v_exp_f32 v30, v30\n" \
"v_mfma_f32_16x16x32_bf16 v[68:71], a[216:219], a[136:139], v[68:71]\n" \
"buffer_load_dwordx4 v234, s[40:43], s71 offen offset:0 sc1 lds\n" \
"v_mfma_f32_16x16x32_bf16 v[68:71], a[212:215], a[132:135], v[68:71]\n" \
"v_exp_f32 v31, v31\n" \
"v_mfma_f32_16x16x32_bf16 v[68:71], a[208:211], a[128:131], v[68:71]\n" \
"ds_read_b128 a[220:223], v233 offset:11264\n" \
"v_cvt_pk_bf16_f32 v23, v30, v31\n" \
"v_mfma_f32_16x16x32_bf16 v[72:75], a[224:227], a[128:131], v[196:199]\n" \
"v_exp_f32 v40, v40\n" \
"v_mfma_f32_16x16x32_bf16 v[72:75], a[228:231], a[132:135], v[72:75]\n" \
"buffer_load_dwordx4 v234, s[40:43], s71 offen offset:1024 sc1 lds\n" \
"v_mfma_f32_16x16x32_bf16 v[72:75], a[232:235], a[136:139], v[72:75]\n" \
"v_exp_f32 v41, v41\n" \
"v_mfma_f32_16x16x32_bf16 v[72:75], a[236:239], a[140:143], v[72:75]\n" \
"ds_read_b128 a[216:219], v233 offset:3072\n" \
"v_cvt_pk_bf16_f32 v36, v40, v41\n" \
"v_mfma_f32_16x16x32_bf16 v[88:91], a[236:239], a[156:159], v[196:199]\n" \
"v_exp_f32 v42, v42\n" \
"v_mfma_f32_16x16x32_bf16 v[88:91], a[232:235], a[152:155], v[88:91]\n" \
"v_mfma_f32_16x16x32_bf16 v[88:91], a[228:231], a[148:151], v[88:91]\n" \
"v_exp_f32 v43, v43\n" \
"v_mfma_f32_16x16x32_bf16 v[88:91], a[224:227], a[144:147], v[88:91]\n" \
"ds_read_b128 a[212:215], v233 offset:10240\n" \
"v_cvt_pk_bf16_f32 v37, v42, v43\n" \
"v_mfma_f32_16x16x32_bf16 v[104:107], a[224:227], a[160:163], v[196:199]\n" \
"v_exp_f32 v44, v44\n" \
"v_mfma_f32_16x16x32_bf16 v[104:107], a[228:231], a[164:167], v[104:107]\n" \
"buffer_load_dwordx4 v234, s[40:43], s71 offen offset:2048 sc1 lds\n" \
"v_mfma_f32_16x16x32_bf16 v[104:107], a[232:235], a[168:171], v[104:107]\n" \
"v_exp_f32 v45, v45\n" \
"v_mfma_f32_16x16x32_bf16 v[104:107], a[236:239], a[172:175], v[104:107]\n" \
"ds_read_b128 a[208:211], v233 offset:2048\n" \
"v_cvt_pk_bf16_f32 v38, v44, v45\n" \
"v_mfma_f32_16x16x32_bf16 v[120:123], a[236:239], a[188:191], v[196:199]\n" \
"v_exp_f32 v46, v46\n" \
"v_mfma_f32_16x16x32_bf16 v[120:123], a[232:235], a[184:187], v[120:123]\n" \
"buffer_load_dwordx4 v234, s[40:43], s71 offen offset:3072 sc1 lds\n" \
"v_mfma_f32_16x16x32_bf16 v[120:123], a[228:231], a[180:183], v[120:123]\n" \
"v_exp_f32 v47, v47\n" \
"v_mfma_f32_16x16x32_bf16 v[120:123], a[224:227], a[176:179], v[120:123]\n" \
"ds_read_b128 a[236:239], v233 offset:13312\n" \
"v_cvt_pk_bf16_f32 v39, v46, v47\n" \
"v_mfma_f32_16x16x32_bf16 v[124:127], a[240:243], a[176:179], v[196:199]\n" \
"v_exp_f32 v56, v56\n" \
"v_mfma_f32_16x16x32_bf16 v[124:127], a[244:247], a[180:183], v[124:127]\n" \
"v_mfma_f32_16x16x32_bf16 v[124:127], a[248:251], a[184:187], v[124:127]\n" \
"v_exp_f32 v57, v57\n" \
"v_mfma_f32_16x16x32_bf16 v[124:127], a[252:255], a[188:191], v[124:127]\n" \
"ds_read_b128 a[232:235], v233 offset:5120\n" \
"v_cvt_pk_bf16_f32 v52, v56, v57\n" \
"v_mfma_f32_16x16x32_bf16 v[108:111], a[252:255], a[172:175], v[196:199]\n" \
"v_exp_f32 v58, v58\n" \
"v_mfma_f32_16x16x32_bf16 v[108:111], a[248:251], a[168:171], v[108:111]\n" \
"v_mfma_f32_16x16x32_bf16 v[108:111], a[244:247], a[164:167], v[108:111]\n" \
"v_exp_f32 v59, v59\n" \
"v_mfma_f32_16x16x32_bf16 v[108:111], a[240:243], a[160:163], v[108:111]\n" \
"ds_read_b128 a[228:231], v233 offset:12288\n" \
"v_cvt_pk_bf16_f32 v53, v58, v59\n" \
"v_mfma_f32_16x16x32_bf16 v[92:95], a[240:243], a[144:147], v[196:199]\n" \
"v_exp_f32 v60, v60\n" \
"v_mfma_f32_16x16x32_bf16 v[92:95], a[244:247], a[148:151], v[92:95]\n" \
"v_mfma_f32_16x16x32_bf16 v[92:95], a[248:251], a[152:155], v[92:95]\n" \
"v_exp_f32 v61, v61\n" \
"v_mfma_f32_16x16x32_bf16 v[92:95], a[252:255], a[156:159], v[92:95]\n" \
"ds_read_b128 a[224:227], v233 offset:4096\n" \
"v_cvt_pk_bf16_f32 v54, v60, v61\n" \
"v_mfma_f32_16x16x32_bf16 v[76:79], a[252:255], a[140:143], v[196:199]\n" \
"v_exp_f32 v62, v62\n" \
"v_mfma_f32_16x16x32_bf16 v[76:79], a[248:251], a[136:139], v[76:79]\n" \
"v_mfma_f32_16x16x32_bf16 v[76:79], a[244:247], a[132:135], v[76:79]\n" \
"s_waitcnt lgkmcnt(2)\n" \
"v_exp_f32 v63, v63\n" \
"v_mfma_f32_16x16x32_bf16 v[76:79], a[240:243], a[128:131], v[76:79]\n" \
"ds_read_b128 a[44:47], v233 offset:15360\n" \
"v_cvt_pk_bf16_f32 v55, v62, v63\n" \
"v_mfma_f32_16x16x32_bf16 v[128:131], a[192:195], v[0:3], v[128:131]\n" \
"s_add_u32 m0, s74, s79\n" \
"v_exp_f32 v64, v64\n" \
"v_mfma_f32_16x16x32_bf16 v[128:131], a[196:199], v[4:7], v[128:131]\n" \
"buffer_load_dwordx4 v234, s[36:39], s73 offen offset:0 sc1 lds\n" \
"v_mfma_f32_16x16x32_bf16 v[160:163], a[192:195], v[16:19], v[160:163]\n" \
"v_exp_f32 v65, v65\n" \
"v_mfma_f32_16x16x32_bf16 v[160:163], a[196:199], v[20:23], v[160:163]\n" \
"ds_read_b128 a[240:243], v232 offset:8448\n" \
"v_cvt_pk_bf16_f32 v64, v64, v65\n" \
"v_mfma_f32_16x16x32_bf16 a[64:67], a[192:195], v[32:35], a[64:67]\n" \
"s_add_u32 s71, s73, 17408\n" \
"v_exp_f32 v66, v66\n" \
"v_mfma_f32_16x16x32_bf16 a[64:67], a[196:199], v[36:39], a[64:67]\n" \
"buffer_load_dwordx4 v234, s[36:39], s73 offen offset:1024 sc1 lds\n" \
"v_mfma_f32_16x16x32_bf16 a[96:99], a[192:195], v[48:51], a[96:99]\n" \
"v_exp_f32 v67, v67\n" \
"v_mfma_f32_16x16x32_bf16 a[96:99], a[196:199], v[52:55], a[96:99]\n" \
"ds_read_b128 a[192:195], v232 offset:0\n" \
"v_cvt_pk_bf16_f32 v65, v66, v67\n" \
"v_mfma_f32_16x16x32_bf16 a[100:103], a[200:203], v[48:51], a[100:103]\n" \
"v_exp_f32 v68, v68\n" \
"v_mfma_f32_16x16x32_bf16 a[100:103], a[204:207], v[52:55], a[100:103]\n" \
"v_xor_b32 v233, 0x4000, v233\n" \
"v_mfma_f32_16x16x32_bf16 a[68:71], a[200:203], v[32:35], a[68:71]\n" \
"v_exp_f32 v69, v69\n" \
"v_mfma_f32_16x16x32_bf16 a[68:71], a[204:207], v[36:39], a[68:71]\n" \
"ds_read_b128 a[196:199], v232 offset:2048\n" \
"v_cvt_pk_bf16_f32 v66, v68, v69\n" \
"v_smfmac_f32_16x16x64_bf16 a[48:51], v[244:247], v[0:7], v243\n" \
"buffer_load_dwordx4 v234, s[36:39], s73 offen offset:2048 sc1 lds\n" \
"v_mfma_f32_16x16x32_bf16 v[164:167], a[200:203], v[16:19], v[164:167]\n" \
"v_exp_f32 v70, v70\n" \
"v_mfma_f32_16x16x32_bf16 v[164:167], a[204:207], v[20:23], v[164:167]\n" \
"v_mfma_f32_16x16x32_bf16 v[132:135], a[200:203], v[0:3], v[132:135]\n" \
"v_exp_f32 v71, v71\n" \
"v_mfma_f32_16x16x32_bf16 v[132:135], a[204:207], v[4:7], v[132:135]\n" \
"ds_read_b128 a[200:203], v232 offset:4096\n" \
"v_cvt_pk_bf16_f32 v67, v70, v71\n" \
"v_mfma_f32_16x16x32_bf16 v[136:139], a[208:211], v[0:3], v[136:139]\n" \
"v_exp_f32 v80, v80\n" \
"v_mfma_f32_16x16x32_bf16 v[136:139], a[212:215], v[4:7], v[136:139]\n" \
"buffer_load_dwordx4 v234, s[36:39], s73 offen offset:3072 sc1 lds\n" \
"v_mfma_f32_16x16x32_bf16 v[168:171], a[208:211], v[16:19], v[168:171]\n" \
"v_exp_f32 v81, v81\n" \
"v_mfma_f32_16x16x32_bf16 v[168:171], a[212:215], v[20:23], v[168:171]\n" \
"ds_read_b128 a[204:207], v232 offset:6144\n" \
"v_cvt_pk_bf16_f32 v80, v80, v81\n" \
"v_mfma_f32_16x16x32_bf16 a[72:75], a[208:211], v[32:35], a[72:75]\n" \
"v_exp_f32 v82, v82\n" \
"v_mfma_f32_16x16x32_bf16 a[72:75], a[212:215], v[36:39], a[72:75]\n" \
"v_mfma_f32_16x16x32_bf16 a[104:107], a[208:211], v[48:51], a[104:107]\n" \
"v_exp_f32 v83, v83\n" \
"v_mfma_f32_16x16x32_bf16 a[104:107], a[212:215], v[52:55], a[104:107]\n" \
"ds_read_b128 a[208:211], v232 offset:256\n" \
"v_cvt_pk_bf16_f32 v81, v82, v83\n" \
"v_mfma_f32_16x16x32_bf16 a[108:111], a[216:219], v[48:51], a[108:111]\n" \
"s_add_u32 m0, m0, 0x4000\n" \
"v_exp_f32 v84, v84\n" \
"v_mfma_f32_16x16x32_bf16 a[108:111], a[220:223], v[52:55], a[108:111]\n" \
"buffer_load_dwordx4 v234, s[36:39], s71 offen offset:0 sc1 lds\n" \
"v_mfma_f32_16x16x32_bf16 a[76:79], a[216:219], v[32:35], a[76:79]\n" \
"v_exp_f32 v85, v85\n" \
"v_mfma_f32_16x16x32_bf16 a[76:79], a[220:223], v[36:39], a[76:79]\n" \
"ds_read_b128 a[212:215], v232 offset:2304\n" \
"v_cvt_pk_bf16_f32 v82, v84, v85\n" \
"v_smfmac_f32_16x16x64_bf16 a[52:55], v[244:247], v[16:23], v243\n" \
"v_mfma_f32_16x16x32_bf16 v[172:175], a[216:219], v[16:19], v[172:175]\n" \
"v_exp_f32 v86, v86\n" \
"v_mfma_f32_16x16x32_bf16 v[172:175], a[220:223], v[20:23], v[172:175]\n" \
"buffer_load_dwordx4 v234, s[36:39], s71 offen offset:1024 sc1 lds\n" \
"v_mfma_f32_16x16x32_bf16 v[140:143], a[216:219], v[0:3], v[140:143]\n" \
"s_waitcnt lgkmcnt(7)\n" \
"v_exp_f32 v87, v87\n" \
"v_mfma_f32_16x16x32_bf16 v[140:143], a[220:223], v[4:7], v[140:143]\n" \
"ds_read_b128 a[216:219], v232 offset:4352\n" \
"v_cvt_pk_bf16_f32 v83, v86, v87\n" \
"v_mfma_f32_16x16x32_bf16 v[144:147], a[224:227], v[0:3], v[144:147]\n" \
"v_exp_f32 v96, v96\n" \
"v_mfma_f32_16x16x32_bf16 v[144:147], a[228:231], v[4:7], v[144:147]\n" \
"buffer_load_dwordx4 v234, s[36:39], s71 offen offset:2048 sc1 lds\n" \
"v_mfma_f32_16x16x32_bf16 v[176:179], a[224:227], v[16:19], v[176:179]\n" \
"v_exp_f32 v97, v97\n" \
"v_mfma_f32_16x16x32_bf16 v[176:179], a[228:231], v[20:23], v[176:179]\n" \
"ds_read_b128 a[220:223], v232 offset:6400\n" \
"v_cvt_pk_bf16_f32 v96, v96, v97\n" \
"v_mfma_f32_16x16x32_bf16 a[80:83], a[224:227], v[32:35], a[80:83]\n" \
"v_exp_f32 v98, v98\n" \
"v_mfma_f32_16x16x32_bf16 a[80:83], a[228:231], v[36:39], a[80:83]\n" \
"v_mfma_f32_16x16x32_bf16 a[112:115], a[224:227], v[48:51], a[112:115]\n" \
"v_exp_f32 v99, v99\n" \
"v_mfma_f32_16x16x32_bf16 a[112:115], a[228:231], v[52:55], a[112:115]\n" \
"ds_read_b128 a[224:227], v232 offset:8192\n" \
"v_cvt_pk_bf16_f32 v97, v98, v99\n" \
"v_mfma_f32_16x16x32_bf16 a[116:119], a[232:235], v[48:51], a[116:119]\n" \
"v_exp_f32 v100, v100\n" \
"v_mfma_f32_16x16x32_bf16 a[116:119], a[236:239], v[52:55], a[116:119]\n" \
"buffer_load_dwordx4 v234, s[36:39], s71 offen offset:3072 sc1 lds\n" \
"v_mfma_f32_16x16x32_bf16 a[84:87], a[232:235], v[32:35], a[84:87]\n" \
"v_exp_f32 v101, v101\n" \
"v_mfma_f32_16x16x32_bf16 a[84:87], a[236:239], v[36:39], a[84:87]\n" \
"ds_read_b128 a[228:231], v232 offset:10240\n" \
"v_cvt_pk_bf16_f32 v98, v100, v101\n" \
"v_smfmac_f32_16x16x64_bf16 v[224:227], v[244:247], v[32:39], v243\n" \
"v_mfma_f32_16x16x32_bf16 v[180:183], a[232:235], v[16:19], v[180:183]\n" \
"v_exp_f32 v102, v102\n" \
"v_mfma_f32_16x16x32_bf16 v[180:183], a[236:239], v[20:23], v[180:183]\n" \
"buffer_load_dword v239, v242, s[56:59], s95 offen sc1\n" \
"v_mfma_f32_16x16x32_bf16 v[148:151], a[232:235], v[0:3], v[148:151]\n" \
"s_mov_b32 s69, s92\n" \
"v_exp_f32 v103, v103\n" \
"v_mfma_f32_16x16x32_bf16 v[148:151], a[236:239], v[4:7], v[148:151]\n" \
"s_mov_b32 s76, s73\n" \
"ds_read_b128 a[232:235], v232 offset:12288\n" \
"v_cvt_pk_bf16_f32 v99, v102, v103\n" \
"v_mfma_f32_16x16x32_bf16 v[152:155], a[32:35], v[0:3], v[152:155]\n" \
"s_add_u32 s73, s73, 34816\n" \
"v_exp_f32 v112, v112\n" \
"v_mfma_f32_16x16x32_bf16 v[152:155], a[36:39], v[4:7], v[152:155]\n" \
"s_cmp_eq_u32 s73, s83\n" \
"s_cselect_b32 s70, 34816, 0\n" \
"s_add_u32 s73, s73, s70\n" \
"v_mfma_f32_16x16x32_bf16 v[184:187], a[32:35], v[16:19], v[184:187]\n" \
"s_xor_b32 s74, s74, 0x8000\n" \
"v_exp_f32 v113, v113\n" \
"v_mfma_f32_16x16x32_bf16 v[184:187], a[36:39], v[20:23], v[184:187]\n" \
"s_xor_b32 s77, s77, 0x8000\n" \
"ds_read_b128 a[236:239], v232 offset:14336\n" \
"v_cvt_pk_bf16_f32 v112, v112, v113\n" \
"v_mfma_f32_16x16x32_bf16 a[88:91], a[32:35], v[32:35], a[88:91]\n" \
"v_exp_f32 v114, v114\n" \
"v_mfma_f32_16x16x32_bf16 a[88:91], a[36:39], v[36:39], a[88:91]\n" \
"v_mfma_f32_16x16x32_bf16 a[120:123], a[32:35], v[48:51], a[120:123]\n" \
"v_exp_f32 v115, v115\n" \
"v_mfma_f32_16x16x32_bf16 a[120:123], a[36:39], v[52:55], a[120:123]\n" \
"ds_read_b128 a[244:247], v232 offset:10496\n" \
"v_cvt_pk_bf16_f32 v113, v114, v115\n" \
"v_mfma_f32_16x16x32_bf16 a[124:127], a[40:43], v[48:51], a[124:127]\n" \
"v_exp_f32 v116, v116\n" \
"v_mfma_f32_16x16x32_bf16 a[124:127], a[44:47], v[52:55], a[124:127]\n" \
"v_mfma_f32_16x16x32_bf16 a[92:95], a[40:43], v[32:35], a[92:95]\n" \
"v_exp_f32 v117, v117\n" \
"v_mfma_f32_16x16x32_bf16 a[92:95], a[44:47], v[36:39], a[92:95]\n" \
"ds_read_b128 a[248:251], v232 offset:12544\n" \
"v_cvt_pk_bf16_f32 v114, v116, v117\n" \
"v_smfmac_f32_16x16x64_bf16 v[228:231], v[244:247], v[48:55], v243\n" \
"v_mfma_f32_16x16x32_bf16 v[188:191], a[40:43], v[16:19], v[188:191]\n" \
"v_exp_f32 v118, v118\n" \
"v_mfma_f32_16x16x32_bf16 v[188:191], a[44:47], v[20:23], v[188:191]\n" \
"s_cmp_lg_u32 s89, 0\n" \
"v_exp_f32 v119, v119\n" \
"v_mfma_f32_16x16x32_bf16 v[156:159], a[40:43], v[0:3], v[156:159]\n" \
"s_waitcnt lgkmcnt(2)\n" \
"ds_read_b128 a[252:255], v232 offset:14592\n" \
"v_cvt_pk_bf16_f32 v115, v118, v119\n" \
"v_mfma_f32_16x16x32_bf16 v[156:159], a[44:47], v[4:7], v[156:159]\n" \
"s_cbranch_scc1 Lloop_%=\n" \
"s_cmp_eq_u32 s63, 7\n" \
"s_cbranch_scc0 Lloop2_%=\n" \
"s_branch Lg_%=\n" \
"s_nop 0\n" \
"s_nop 0\n" \
"Lep_%=:\n" \
"Lpg6052_%=:\n" \
"s_cbranch_execz Lpg7367_%=\n" \
"s_nop 4\n" \
"v_accvgpr_read_b32 v92, a48\n" \
"v_accvgpr_read_b32 v93, a49\n" \
"v_accvgpr_read_b32 v94, a52\n" \
"v_accvgpr_read_b32 v95, a53\n" \
"v_accvgpr_write_b32 a48, 0\n" \
"v_accvgpr_write_b32 a49, 0\n" \
"v_accvgpr_write_b32 a52, 0\n" \
"v_accvgpr_write_b32 a53, 0\n" \
"v_add_f32 v86, v92, v93\n" \
"v_add_f32 v87, v94, v95\n" \
"v_rcp_f32 v84, v86\n" \
"v_rcp_f32 v85, v87\n" \
"v_add_u32 v240, 0x1000, v236\n" \
"v_add_f32 v80, v86, v87\n" \
"v_mul_f32 v80, 0x43800000, v80\n" \
"v_add_f32 v86, v84, v85\n" \
"v_fmac_f32 v80, 0x5f800000, v86\n" \
"v_mov_b32 v81, 0\n" \
"v_mov_b32 v82, 0\n" \
"v_mov_b32 v83, 0\n" \
"v_mov_b32 v88, 0\n" \
"v_mov_b32 v89, 0\n" \
"v_mov_b32 v90, 0\n" \
"v_mov_b32 v91, 0\n" \
"v_pk_mul_f32 v[128:129], v[128:129], v[84:85] op_sel:[0,0] op_sel_hi:[1,0]\n" \
"v_pk_mul_f32 v[130:131], v[130:131], v[84:85] op_sel:[0,0] op_sel_hi:[1,0]\n" \
"v_pk_mul_f32 v[132:133], v[132:133], v[84:85] op_sel:[0,0] op_sel_hi:[1,0]\n" \
"v_pk_mul_f32 v[134:135], v[134:135], v[84:85] op_sel:[0,0] op_sel_hi:[1,0]\n" \
"v_cvt_pk_bf16_f32 v64, v128, v129\n" \
"v_cvt_pk_bf16_f32 v65, v130, v131\n" \
"v_cvt_pk_bf16_f32 v66, v132, v133\n" \
"v_cvt_pk_bf16_f32 v67, v134, v135\n" \
"buffer_store_dwordx4 v[64:67], v236, s[56:59], s87 offen offset:0 sc1\n" \
"v_pk_mul_f32 v[136:137], v[136:137], v[84:85] op_sel:[0,0] op_sel_hi:[1,0]\n" \
"v_pk_mul_f32 v[138:139], v[138:139], v[84:85] op_sel:[0,0] op_sel_hi:[1,0]\n" \
"v_pk_mul_f32 v[140:141], v[140:141], v[84:85] op_sel:[0,0] op_sel_hi:[1,0]\n" \
"v_pk_mul_f32 v[142:143], v[142:143], v[84:85] op_sel:[0,0] op_sel_hi:[1,0]\n" \
"v_cvt_pk_bf16_f32 v68, v136, v137\n" \
"v_cvt_pk_bf16_f32 v69, v138, v139\n" \
"v_cvt_pk_bf16_f32 v70, v140, v141\n" \
"v_cvt_pk_bf16_f32 v71, v142, v143\n" \
"buffer_store_dwordx4 v[68:71], v236, s[56:59], s87 offen offset:2048 sc1\n" \
"v_pk_mul_f32 v[144:145], v[144:145], v[84:85] op_sel:[0,0] op_sel_hi:[1,0]\n" \
"v_pk_mul_f32 v[146:147], v[146:147], v[84:85] op_sel:[0,0] op_sel_hi:[1,0]\n" \
"v_pk_mul_f32 v[148:149], v[148:149], v[84:85] op_sel:[0,0] op_sel_hi:[1,0]\n" \
"v_pk_mul_f32 v[150:151], v[150:151], v[84:85] op_sel:[0,0] op_sel_hi:[1,0]\n" \
"v_cvt_pk_bf16_f32 v72, v144, v145\n" \
"v_cvt_pk_bf16_f32 v73, v146, v147\n" \
"v_cvt_pk_bf16_f32 v74, v148, v149\n" \
"v_cvt_pk_bf16_f32 v75, v150, v151\n" \
"buffer_store_dwordx4 v[72:75], v240, s[56:59], s87 offen offset:0 sc1\n" \
"v_pk_mul_f32 v[152:153], v[152:153], v[84:85] op_sel:[0,0] op_sel_hi:[1,0]\n" \
"v_pk_mul_f32 v[154:155], v[154:155], v[84:85] op_sel:[0,0] op_sel_hi:[1,0]\n" \
"v_pk_mul_f32 v[156:157], v[156:157], v[84:85] op_sel:[0,0] op_sel_hi:[1,0]\n" \
"v_pk_mul_f32 v[158:159], v[158:159], v[84:85] op_sel:[0,0] op_sel_hi:[1,0]\n" \
"v_cvt_pk_bf16_f32 v76, v152, v153\n" \
"v_cvt_pk_bf16_f32 v77, v154, v155\n" \
"v_cvt_pk_bf16_f32 v78, v156, v157\n" \
"v_cvt_pk_bf16_f32 v79, v158, v159\n" \
"buffer_store_dwordx4 v[76:79], v240, s[56:59], s87 offen offset:2048 sc1\n" \
"v_smfmac_f32_16x16x64_bf16 v[80:83], v[244:247], v[64:71], v243\n" \
"v_smfmac_f32_16x16x64_bf16 v[80:83], v[244:247], v[72:79], v243\n" \
"v_mfma_f32_32x32x16_bf16 v[128:143], v[88:91], v[88:91], 0\n" \
"v_mfma_f32_32x32x16_bf16 v[144:159], v[88:91], v[88:91], 0\n" \
"s_mov_b64 exec, 0\n" \
"s_cmp_eq_u32 s61, 0\n" \
"s_cbranch_scc1 Lst1x0_%=\n" \
"s_cmp_eq_u32 s61, 1\n" \
"s_cbranch_scc1 Lst1x1_%=\n" \
"s_cmp_eq_u32 s61, 2\n" \
"s_cbranch_scc1 Lst1x2_%=\n" \
"s_branch Lst1x3_%=\n" \
"Lcb1_%=:\n" \
"s_mov_b64 exec, -1\n" \
"s_add_u32 s72, s87, 532480\n" \
"v_pk_mul_f32 v[160:161], v[160:161], v[84:85] op_sel:[0,1] op_sel_hi:[1,1]\n" \
"v_pk_mul_f32 v[162:163], v[162:163], v[84:85] op_sel:[0,1] op_sel_hi:[1,1]\n" \
"v_pk_mul_f32 v[164:165], v[164:165], v[84:85] op_sel:[0,1] op_sel_hi:[1,1]\n" \
"v_pk_mul_f32 v[166:167], v[166:167], v[84:85] op_sel:[0,1] op_sel_hi:[1,1]\n" \
"v_cvt_pk_bf16_f32 v64, v160, v161\n" \
"v_cvt_pk_bf16_f32 v65, v162, v163\n" \
"v_cvt_pk_bf16_f32 v66, v164, v165\n" \
"v_cvt_pk_bf16_f32 v67, v166, v167\n" \
"buffer_store_dwordx4 v[64:67], v236, s[56:59], s72 offen offset:0 sc1\n" \
"v_pk_mul_f32 v[168:169], v[168:169], v[84:85] op_sel:[0,1] op_sel_hi:[1,1]\n" \
"v_pk_mul_f32 v[170:171], v[170:171], v[84:85] op_sel:[0,1] op_sel_hi:[1,1]\n" \
"v_pk_mul_f32 v[172:173], v[172:173], v[84:85] op_sel:[0,1] op_sel_hi:[1,1]\n" \
"v_pk_mul_f32 v[174:175], v[174:175], v[84:85] op_sel:[0,1] op_sel_hi:[1,1]\n" \
"v_cvt_pk_bf16_f32 v68, v168, v169\n" \
"v_cvt_pk_bf16_f32 v69, v170, v171\n" \
"v_cvt_pk_bf16_f32 v70, v172, v173\n" \
"v_cvt_pk_bf16_f32 v71, v174, v175\n" \
"buffer_store_dwordx4 v[68:71], v236, s[56:59], s72 offen offset:2048 sc1\n" \
"v_pk_mul_f32 v[176:177], v[176:177], v[84:85] op_sel:[0,1] op_sel_hi:[1,1]\n" \
"v_pk_mul_f32 v[178:179], v[178:179], v[84:85] op_sel:[0,1] op_sel_hi:[1,1]\n" \
"v_pk_mul_f32 v[180:181], v[180:181], v[84:85] op_sel:[0,1] op_sel_hi:[1,1]\n" \
"v_pk_mul_f32 v[182:183], v[182:183], v[84:85] op_sel:[0,1] op_sel_hi:[1,1]\n" \
"v_cvt_pk_bf16_f32 v72, v176, v177\n" \
"v_cvt_pk_bf16_f32 v73, v178, v179\n" \
"v_cvt_pk_bf16_f32 v74, v180, v181\n" \
"v_cvt_pk_bf16_f32 v75, v182, v183\n" \
"buffer_store_dwordx4 v[72:75], v240, s[56:59], s72 offen offset:0 sc1\n" \
"v_pk_mul_f32 v[184:185], v[184:185], v[84:85] op_sel:[0,1] op_sel_hi:[1,1]\n" \
"v_pk_mul_f32 v[186:187], v[186:187], v[84:85] op_sel:[0,1] op_sel_hi:[1,1]\n" \
"v_pk_mul_f32 v[188:189], v[188:189], v[84:85] op_sel:[0,1] op_sel_hi:[1,1]\n" \
"v_pk_mul_f32 v[190:191], v[190:191], v[84:85] op_sel:[0,1] op_sel_hi:[1,1]\n" \
"v_cvt_pk_bf16_f32 v76, v184, v185\n" \
"v_cvt_pk_bf16_f32 v77, v186, v187\n" \
"v_cvt_pk_bf16_f32 v78, v188, v189\n" \
"v_cvt_pk_bf16_f32 v79, v190, v191\n" \
"buffer_store_dwordx4 v[76:79], v240, s[56:59], s72 offen offset:2048 sc1\n" \
"v_smfmac_f32_16x16x64_bf16 v[80:83], v[244:247], v[64:71], v243\n" \
"v_smfmac_f32_16x16x64_bf16 v[80:83], v[244:247], v[72:79], v243\n" \
"v_mfma_f32_32x32x16_bf16 v[160:175], v[88:91], v[88:91], 0\n" \
"v_mfma_f32_32x32x16_bf16 v[176:191], v[88:91], v[88:91], 0\n" \
"s_nop 5\n" \
"v_add_f32 v80, v80, v81\n" \
"s_movk_i32 s64, 0x207\n" \
"v_cmp_class_f32 vcc, v80, s64\n" \
"s_nop 1\n" \
"s_cmp_lg_u64 vcc, 0\n" \
"s_addc_u32 s90, s90, 0\n" \
"s_waitcnt vmcnt(8)\n" \
"s_cmp_eq_u32 s63, 7\n" \
"s_cbranch_scc0 Lepr_%=\n" \
"s_branch Lepg_%=\n" \
".p2align 12\n" \
"Lloop2_%=:\n" \
"v_mfma_f32_16x16x32_bf16 v[0:3], a[192:195], a[0:3], v[196:199]\n" \
"Lpg6183_%=:\n" \
"s_cbranch_execz Lcb0_%=\n" \
"v_exp_f32 v72, v72\n" \
"v_mfma_f32_16x16x32_bf16 v[0:3], a[196:199], a[4:7], v[0:3]\n" \
"ds_read_b128 a[44:47], v233 offset:15360\n" \
"v_mfma_f32_16x16x32_bf16 v[0:3], a[200:203], a[8:11], v[0:3]\n" \
"v_exp_f32 v73, v73\n" \
"v_mfma_f32_16x16x32_bf16 v[0:3], a[204:207], a[12:15], v[0:3]\n" \
"ds_read_b128 a[32:35], v233 offset:6144\n" \
"v_cvt_pk_bf16_f32 v68, v72, v73\n" \
"v_mfma_f32_16x16x32_bf16 v[16:19], a[204:207], a[28:31], v[196:199]\n" \
"v_exp_f32 v74, v74\n" \
"v_mfma_f32_16x16x32_bf16 v[16:19], a[200:203], a[24:27], v[16:19]\n" \
"v_xor_b32 v232, 0x8000, v232\n" \
"v_mfma_f32_16x16x32_bf16 v[16:19], a[196:199], a[20:23], v[16:19]\n" \
"v_exp_f32 v75, v75\n" \
"v_mfma_f32_16x16x32_bf16 v[16:19], a[192:195], a[16:19], v[16:19]\n" \
"ds_read_b128 a[36:39], v233 offset:14336\n" \
"v_cvt_pk_bf16_f32 v69, v74, v75\n" \
"v_mfma_f32_16x16x32_bf16 v[32:35], a[192:195], a[160:163], v[196:199]\n" \
"v_exp_f32 v76, v76\n" \
"v_mfma_f32_16x16x32_bf16 v[32:35], a[196:199], a[164:167], v[32:35]\n" \
"v_mfma_f32_16x16x32_bf16 v[32:35], a[200:203], a[168:171], v[32:35]\n" \
"v_exp_f32 v77, v77\n" \
"v_mfma_f32_16x16x32_bf16 v[32:35], a[204:207], a[172:175], v[32:35]\n" \
"ds_read_b128 a[40:43], v233 offset:7168\n" \
"v_cvt_pk_bf16_f32 v70, v76, v77\n" \
"v_mfma_f32_16x16x32_bf16 v[48:51], a[204:207], a[188:191], v[196:199]\n" \
"v_exp_f32 v78, v78\n" \
"v_mfma_f32_16x16x32_bf16 v[48:51], a[200:203], a[184:187], v[48:51]\n" \
"v_mfma_f32_16x16x32_bf16 v[48:51], a[196:199], a[180:183], v[48:51]\n" \
"v_exp_f32 v79, v79\n" \
"v_mfma_f32_16x16x32_bf16 v[48:51], a[192:195], a[176:179], v[48:51]\n" \
"ds_read_b128 a[204:207], v233 offset:9216\n" \
"v_cvt_pk_bf16_f32 v71, v78, v79\n" \
"v_mfma_f32_16x16x32_bf16 v[52:55], a[208:211], a[176:179], v[196:199]\n" \
"v_exp_f32 v88, v88\n" \
"v_mfma_f32_16x16x32_bf16 v[52:55], a[212:215], a[180:183], v[52:55]\n" \
"v_mfma_f32_16x16x32_bf16 v[52:55], a[216:219], a[184:187], v[52:55]\n" \
"v_exp_f32 v89, v89\n" \
"v_mfma_f32_16x16x32_bf16 v[52:55], a[220:223], a[188:191], v[52:55]\n" \
"ds_read_b128 a[200:203], v233 offset:1024\n" \
"v_cvt_pk_bf16_f32 v84, v88, v89\n" \
"v_mfma_f32_16x16x32_bf16 v[36:39], a[220:223], a[172:175], v[196:199]\n" \
"v_exp_f32 v90, v90\n" \
"v_mfma_f32_16x16x32_bf16 v[36:39], a[216:219], a[168:171], v[36:39]\n" \
"v_mfma_f32_16x16x32_bf16 v[36:39], a[212:215], a[164:167], v[36:39]\n" \
"v_exp_f32 v91, v91\n" \
"v_mfma_f32_16x16x32_bf16 v[36:39], a[208:211], a[160:163], v[36:39]\n" \
"ds_read_b128 a[196:199], v233 offset:8192\n" \
"v_cvt_pk_bf16_f32 v85, v90, v91\n" \
"v_mfma_f32_16x16x32_bf16 v[20:23], a[208:211], a[16:19], v[196:199]\n" \
"v_exp_f32 v92, v92\n" \
"v_mfma_f32_16x16x32_bf16 v[20:23], a[212:215], a[20:23], v[20:23]\n" \
"v_mfma_f32_16x16x32_bf16 v[20:23], a[216:219], a[24:27], v[20:23]\n" \
"v_exp_f32 v93, v93\n" \
"v_mfma_f32_16x16x32_bf16 v[20:23], a[220:223], a[28:31], v[20:23]\n" \
"ds_read_b128 a[192:195], v233 offset:0\n" \
"v_cvt_pk_bf16_f32 v86, v92, v93\n" \
"v_mfma_f32_16x16x32_bf16 v[4:7], a[220:223], a[12:15], v[196:199]\n" \
"v_exp_f32 v94, v94\n" \
"v_mfma_f32_16x16x32_bf16 v[4:7], a[216:219], a[8:11], v[4:7]\n" \
"v_mfma_f32_16x16x32_bf16 v[4:7], a[212:215], a[4:7], v[4:7]\n" \
"s_waitcnt lgkmcnt(8)\n" \
"v_exp_f32 v95, v95\n" \
"v_mfma_f32_16x16x32_bf16 v[4:7], a[208:211], a[0:3], v[4:7]\n" \
"ds_read_b128 a[220:223], v233 offset:11264\n" \
"v_cvt_pk_bf16_f32 v87, v94, v95\n" \
"v_mfma_f32_16x16x32_bf16 v[8:11], a[224:227], a[0:3], v[196:199]\n" \
"v_exp_f32 v104, v104\n" \
"v_mfma_f32_16x16x32_bf16 v[8:11], a[228:231], a[4:7], v[8:11]\n" \
"v_mfma_f32_16x16x32_bf16 v[8:11], a[232:235], a[8:11], v[8:11]\n" \
"v_exp_f32 v105, v105\n" \
"v_mfma_f32_16x16x32_bf16 v[8:11], a[236:239], a[12:15], v[8:11]\n" \
"ds_read_b128 a[216:219], v233 offset:3072\n" \
"v_cvt_pk_bf16_f32 v100, v104, v105\n" \
"v_mfma_f32_16x16x32_bf16 v[24:27], a[236:239], a[28:31], v[196:199]\n" \
"v_exp_f32 v106, v106\n" \
"v_mfma_f32_16x16x32_bf16 v[24:27], a[232:235], a[24:27], v[24:27]\n" \
"v_mfma_f32_16x16x32_bf16 v[24:27], a[228:231], a[20:23], v[24:27]\n" \
"v_exp_f32 v107, v107\n" \
"v_mfma_f32_16x16x32_bf16 v[24:27], a[224:227], a[16:19], v[24:27]\n" \
"ds_read_b128 a[212:215], v233 offset:10240\n" \
"v_cvt_pk_bf16_f32 v101, v106, v107\n" \
"v_mfma_f32_16x16x32_bf16 v[40:43], a[224:227], a[160:163], v[196:199]\n" \
"v_exp_f32 v108, v108\n" \
"v_mfma_f32_16x16x32_bf16 v[40:43], a[228:231], a[164:167], v[40:43]\n" \
"v_mfma_f32_16x16x32_bf16 v[40:43], a[232:235], a[168:171], v[40:43]\n" \
"v_exp_f32 v109, v109\n" \
"v_mfma_f32_16x16x32_bf16 v[40:43], a[236:239], a[172:175], v[40:43]\n" \
"ds_read_b128 a[208:211], v233 offset:2048\n" \
"v_cvt_pk_bf16_f32 v102, v108, v109\n" \
"v_mfma_f32_16x16x32_bf16 v[56:59], a[236:239], a[188:191], v[196:199]\n" \
"v_exp_f32 v110, v110\n" \
"v_mfma_f32_16x16x32_bf16 v[56:59], a[232:235], a[184:187], v[56:59]\n" \
"v_mfma_f32_16x16x32_bf16 v[56:59], a[228:231], a[180:183], v[56:59]\n" \
"v_exp_f32 v111, v111\n" \
"v_mfma_f32_16x16x32_bf16 v[56:59], a[224:227], a[176:179], v[56:59]\n" \
"ds_read_b128 a[236:239], v233 offset:13312\n" \
"v_cvt_pk_bf16_f32 v103, v110, v111\n" \
"v_mfma_f32_16x16x32_bf16 v[60:63], a[240:243], a[176:179], v[196:199]\n" \
"v_exp_f32 v120, v120\n" \
"v_mfma_f32_16x16x32_bf16 v[60:63], a[244:247], a[180:183], v[60:63]\n" \
"ds_read_b128 a[232:235], v233 offset:5120\n" \
"v_mfma_f32_16x16x32_bf16 v[60:63], a[248:251], a[184:187], v[60:63]\n" \
"v_exp_f32 v121, v121\n" \
"v_mfma_f32_16x16x32_bf16 v[60:63], a[252:255], a[188:191], v[60:63]\n" \
"ds_read_b128 a[228:231], v233 offset:12288\n" \
"v_cvt_pk_bf16_f32 v116, v120, v121\n" \
"v_mfma_f32_16x16x32_bf16 v[44:47], a[252:255], a[172:175], v[196:199]\n" \
"v_exp_f32 v122, v122\n" \
"v_mfma_f32_16x16x32_bf16 v[44:47], a[248:251], a[168:171], v[44:47]\n" \
"ds_read_b128 a[224:227], v233 offset:4096\n" \
"v_mfma_f32_16x16x32_bf16 v[44:47], a[244:247], a[164:167], v[44:47]\n" \
"v_exp_f32 v123, v123\n" \
"v_mfma_f32_16x16x32_bf16 v[44:47], a[240:243], a[160:163], v[44:47]\n" \
"v_cvt_pk_bf16_f32 v117, v122, v123\n" \
"v_mfma_f32_16x16x32_bf16 v[28:31], a[240:243], a[16:19], v[196:199]\n" \
"v_exp_f32 v124, v124\n" \
"v_mfma_f32_16x16x32_bf16 v[28:31], a[244:247], a[20:23], v[28:31]\n" \
"v_mfma_f32_16x16x32_bf16 v[28:31], a[248:251], a[24:27], v[28:31]\n" \
"v_exp_f32 v125, v125\n" \
"v_mfma_f32_16x16x32_bf16 v[28:31], a[252:255], a[28:31], v[28:31]\n" \
"v_cvt_pk_bf16_f32 v118, v124, v125\n" \
"v_mfma_f32_16x16x32_bf16 v[12:15], a[252:255], a[12:15], v[196:199]\n" \
"v_exp_f32 v126, v126\n" \
"v_mfma_f32_16x16x32_bf16 v[12:15], a[248:251], a[8:11], v[12:15]\n" \
"v_mfma_f32_16x16x32_bf16 v[12:15], a[244:247], a[4:7], v[12:15]\n" \
"s_waitcnt vmcnt(0) lgkmcnt(0)\n" \
"v_exp_f32 v127, v127\n" \
"v_mfma_f32_16x16x32_bf16 v[12:15], a[240:243], a[0:3], v[12:15]\n" \
"s_barrier\n" \
"v_cvt_pk_bf16_f32 v119, v126, v127\n" \
"v_mfma_f32_16x16x32_bf16 v[128:131], a[192:195], v[64:67], v[128:131]\n" \
"v_exp_f32 v0, v0\n" \
"v_mfma_f32_16x16x32_bf16 v[128:131], a[196:199], v[68:71], v[128:131]\n" \
"v_cmp_eq_u32 vcc, s60, v239\n" \
"v_mfma_f32_16x16x32_bf16 v[160:163], a[192:195], v[80:83], v[160:163]\n" \
"v_exp_f32 v1, v1\n" \
"v_mfma_f32_16x16x32_bf16 v[160:163], a[196:199], v[84:87], v[160:163]\n" \
"s_andn2_b32 s70, s69, vcc_lo\n" \
"s_cbranch_scc1 Lpl3_%=\n" \
"Lpr3_%=:\n" \
"ds_read_b128 a[240:243], v232 offset:8448\n" \
"v_cvt_pk_bf16_f32 v0, v0, v1\n" \
"v_mfma_f32_16x16x32_bf16 a[64:67], a[192:195], v[96:99], a[64:67]\n" \
"s_add_u32 m0, s74, s79\n" \
"v_exp_f32 v2, v2\n" \
"v_mfma_f32_16x16x32_bf16 a[64:67], a[196:199], v[100:103], a[64:67]\n" \
"buffer_load_dwordx4 v234, s[36:39], s73 offen offset:0 sc1 lds\n" \
"v_mfma_f32_16x16x32_bf16 a[96:99], a[192:195], v[112:115], a[96:99]\n" \
"v_exp_f32 v3, v3\n" \
"v_mfma_f32_16x16x32_bf16 a[96:99], a[196:199], v[116:119], a[96:99]\n" \
"ds_read_b128 a[192:195], v232 offset:0\n" \
"v_cvt_pk_bf16_f32 v1, v2, v3\n" \
"v_mfma_f32_16x16x32_bf16 a[100:103], a[200:203], v[112:115], a[100:103]\n" \
"s_add_u32 s71, s73, 17408\n" \
"v_exp_f32 v4, v4\n" \
"v_mfma_f32_16x16x32_bf16 a[100:103], a[204:207], v[116:119], a[100:103]\n" \
"buffer_load_dwordx4 v234, s[36:39], s73 offen offset:1024 sc1 lds\n" \
"v_mfma_f32_16x16x32_bf16 a[68:71], a[200:203], v[96:99], a[68:71]\n" \
"v_exp_f32 v5, v5\n" \
"v_mfma_f32_16x16x32_bf16 a[68:71], a[204:207], v[100:103], a[68:71]\n" \
"ds_read_b128 a[196:199], v232 offset:2048\n" \
"v_cvt_pk_bf16_f32 v2, v4, v5\n" \
"v_smfmac_f32_16x16x64_bf16 a[48:51], v[244:247], v[64:71], v243\n" \
"buffer_load_dwordx4 v234, s[36:39], s73 offen offset:2048 sc1 lds\n" \
"v_mfma_f32_16x16x32_bf16 v[164:167], a[200:203], v[80:83], v[164:167]\n" \
"v_exp_f32 v6, v6\n" \
"v_mfma_f32_16x16x32_bf16 v[164:167], a[204:207], v[84:87], v[164:167]\n" \
"buffer_load_dwordx4 v234, s[36:39], s73 offen offset:3072 sc1 lds\n" \
"v_mfma_f32_16x16x32_bf16 v[132:135], a[200:203], v[64:67], v[132:135]\n" \
"v_exp_f32 v7, v7\n" \
"v_mfma_f32_16x16x32_bf16 v[132:135], a[204:207], v[68:71], v[132:135]\n" \
"ds_read_b128 a[200:203], v232 offset:4096\n" \
"v_cvt_pk_bf16_f32 v3, v6, v7\n" \
"v_mfma_f32_16x16x32_bf16 v[136:139], a[208:211], v[64:67], v[136:139]\n" \
"s_add_u32 m0, m0, 0x4000\n" \
"v_exp_f32 v16, v16\n" \
"v_mfma_f32_16x16x32_bf16 v[136:139], a[212:215], v[68:71], v[136:139]\n" \
"buffer_load_dwordx4 v234, s[36:39], s71 offen offset:0 sc1 lds\n" \
"v_mfma_f32_16x16x32_bf16 v[168:171], a[208:211], v[80:83], v[168:171]\n" \
"v_exp_f32 v17, v17\n" \
"v_mfma_f32_16x16x32_bf16 v[168:171], a[212:215], v[84:87], v[168:171]\n" \
"ds_read_b128 a[204:207], v232 offset:6144\n" \
"v_cvt_pk_bf16_f32 v16, v16, v17\n" \
"v_mfma_f32_16x16x32_bf16 a[72:75], a[208:211], v[96:99], a[72:75]\n" \
"v_exp_f32 v18, v18\n" \
"v_mfma_f32_16x16x32_bf16 a[72:75], a[212:215], v[100:103], a[72:75]\n" \
"buffer_load_dwordx4 v234, s[36:39], s71 offen offset:1024 sc1 lds\n" \
"v_mfma_f32_16x16x32_bf16 a[104:107], a[208:211], v[112:115], a[104:107]\n" \
"v_exp_f32 v19, v19\n" \
"v_mfma_f32_16x16x32_bf16 a[104:107], a[212:215], v[116:119], a[104:107]\n" \
"ds_read_b128 a[208:211], v232 offset:256\n" \
"v_cvt_pk_bf16_f32 v17, v18, v19\n" \
"v_mfma_f32_16x16x32_bf16 a[108:111], a[216:219], v[112:115], a[108:111]\n" \
"v_exp_f32 v20, v20\n" \
"v_mfma_f32_16x16x32_bf16 a[108:111], a[220:223], v[116:119], a[108:111]\n" \
"buffer_load_dwordx4 v234, s[36:39], s71 offen offset:2048 sc1 lds\n" \
"v_mfma_f32_16x16x32_bf16 a[76:79], a[216:219], v[96:99], a[76:79]\n" \
"v_exp_f32 v21, v21\n" \
"v_mfma_f32_16x16x32_bf16 a[76:79], a[220:223], v[100:103], a[76:79]\n" \
"ds_read_b128 a[212:215], v232 offset:2304\n" \
"v_cvt_pk_bf16_f32 v18, v20, v21\n" \
"v_smfmac_f32_16x16x64_bf16 a[52:55], v[244:247], v[80:87], v243\n" \
"buffer_load_dwordx4 v234, s[36:39], s71 offen offset:3072 sc1 lds\n" \
"v_mfma_f32_16x16x32_bf16 v[172:175], a[216:219], v[80:83], v[172:175]\n" \
"s_add_u32 m0, s77, s80\n" \
"v_exp_f32 v22, v22\n" \
"v_mfma_f32_16x16x32_bf16 v[172:175], a[220:223], v[84:87], v[172:175]\n" \
"buffer_load_dwordx4 v234, s[40:43], s76 offen offset:0 sc1 lds\n" \
"v_mfma_f32_16x16x32_bf16 v[140:143], a[216:219], v[64:67], v[140:143]\n" \
"v_exp_f32 v23, v23\n" \
"v_mfma_f32_16x16x32_bf16 v[140:143], a[220:223], v[68:71], v[140:143]\n" \
"ds_read_b128 a[216:219], v232 offset:4352\n" \
"v_cvt_pk_bf16_f32 v19, v22, v23\n" \
"v_mfma_f32_16x16x32_bf16 v[144:147], a[224:227], v[64:67], v[144:147]\n" \
"s_add_u32 s71, s76, 17408\n" \
"v_exp_f32 v32, v32\n" \
"v_mfma_f32_16x16x32_bf16 v[144:147], a[228:231], v[68:71], v[144:147]\n" \
"buffer_load_dwordx4 v234, s[40:43], s76 offen offset:1024 sc1 lds\n" \
"v_mfma_f32_16x16x32_bf16 v[176:179], a[224:227], v[80:83], v[176:179]\n" \
"v_exp_f32 v33, v33\n" \
"v_mfma_f32_16x16x32_bf16 v[176:179], a[228:231], v[84:87], v[176:179]\n" \
"ds_read_b128 a[220:223], v232 offset:6400\n" \
"v_cvt_pk_bf16_f32 v32, v32, v33\n" \
"v_mfma_f32_16x16x32_bf16 a[80:83], a[224:227], v[96:99], a[80:83]\n" \
"v_exp_f32 v34, v34\n" \
"v_mfma_f32_16x16x32_bf16 a[80:83], a[228:231], v[100:103], a[80:83]\n" \
"buffer_load_dwordx4 v234, s[40:43], s76 offen offset:2048 sc1 lds\n" \
"v_mfma_f32_16x16x32_bf16 a[112:115], a[224:227], v[112:115], a[112:115]\n" \
"v_exp_f32 v35, v35\n" \
"v_mfma_f32_16x16x32_bf16 a[112:115], a[228:231], v[116:119], a[112:115]\n" \
"ds_read_b128 a[224:227], v232 offset:8192\n" \
"v_cvt_pk_bf16_f32 v33, v34, v35\n" \
"v_mfma_f32_16x16x32_bf16 a[116:119], a[232:235], v[112:115], a[116:119]\n" \
"v_exp_f32 v36, v36\n" \
"v_mfma_f32_16x16x32_bf16 a[116:119], a[236:239], v[116:119], a[116:119]\n" \
"buffer_load_dwordx4 v234, s[40:43], s76 offen offset:3072 sc1 lds\n" \
"v_mfma_f32_16x16x32_bf16 a[84:87], a[232:235], v[96:99], a[84:87]\n" \
"v_exp_f32 v37, v37\n" \
"v_mfma_f32_16x16x32_bf16 a[84:87], a[236:239], v[100:103], a[84:87]\n" \
"s_add_u32 m0, m0, 0x4000\n" \
"ds_read_b128 a[228:231], v232 offset:10240\n" \
"v_cvt_pk_bf16_f32 v34, v36, v37\n" \
"v_smfmac_f32_16x16x64_bf16 v[224:227], v[244:247], v[96:103], v243\n" \
"buffer_load_dwordx4 v234, s[40:43], s71 offen offset:0 sc1 lds\n" \
"v_mfma_f32_16x16x32_bf16 v[180:183], a[232:235], v[80:83], v[180:183]\n" \
"v_exp_f32 v38, v38\n" \
"v_mfma_f32_16x16x32_bf16 v[180:183], a[236:239], v[84:87], v[180:183]\n" \
"buffer_load_dwordx4 v234, s[40:43], s71 offen offset:1024 sc1 lds\n" \
"v_mfma_f32_16x16x32_bf16 v[148:151], a[232:235], v[64:67], v[148:151]\n" \
"v_exp_f32 v39, v39\n" \
"v_mfma_f32_16x16x32_bf16 v[148:151], a[236:239], v[68:71], v[148:151]\n" \
"ds_read_b128 a[232:235], v232 offset:12288\n" \
"v_cvt_pk_bf16_f32 v35, v38, v39\n" \
"v_mfma_f32_16x16x32_bf16 v[152:155], a[32:35], v[64:67], v[152:155]\n" \
"v_exp_f32 v48, v48\n" \
"v_mfma_f32_16x16x32_bf16 v[152:155], a[36:39], v[68:71], v[152:155]\n" \
"buffer_load_dwordx4 v234, s[40:43], s71 offen offset:2048 sc1 lds\n" \
"v_mfma_f32_16x16x32_bf16 v[184:187], a[32:35], v[80:83], v[184:187]\n" \
"v_exp_f32 v49, v49\n" \
"v_mfma_f32_16x16x32_bf16 v[184:187], a[36:39], v[84:87], v[184:187]\n" \
"ds_read_b128 a[236:239], v232 offset:14336\n" \
"v_cvt_pk_bf16_f32 v48, v48, v49\n" \
"v_mfma_f32_16x16x32_bf16 a[88:91], a[32:35], v[96:99], a[88:91]\n" \
"v_exp_f32 v50, v50\n" \
"v_mfma_f32_16x16x32_bf16 a[88:91], a[36:39], v[100:103], a[88:91]\n" \
"buffer_load_dwordx4 v234, s[40:43], s71 offen offset:3072 sc1 lds\n" \
"v_mfma_f32_16x16x32_bf16 a[120:123], a[32:35], v[112:115], a[120:123]\n" \
"s_mov_b32 s76, s73\n" \
"v_exp_f32 v51, v51\n" \
"v_mfma_f32_16x16x32_bf16 a[120:123], a[36:39], v[116:119], a[120:123]\n" \
"s_add_u32 s73, s73, 34816\n" \
"ds_read_b128 a[244:247], v232 offset:10496\n" \
"v_cvt_pk_bf16_f32 v49, v50, v51\n" \
"v_mfma_f32_16x16x32_bf16 a[124:127], a[40:43], v[112:115], a[124:127]\n" \
"s_xor_b32 s74, s74, 0x8000\n" \
"v_exp_f32 v52, v52\n" \
"v_mfma_f32_16x16x32_bf16 a[124:127], a[44:47], v[116:119], a[124:127]\n" \
"s_xor_b32 s77, s77, 0x8000\n" \
"v_xor_b32 v233, s81, v233\n" \
"s_mov_b32 s81, 0x8000\n" \
"v_mfma_f32_16x16x32_bf16 a[92:95], a[40:43], v[96:99], a[92:95]\n" \
"v_exp_f32 v53, v53\n" \
"v_mfma_f32_16x16x32_bf16 a[92:95], a[44:47], v[100:103], a[92:95]\n" \
"ds_read_b128 a[248:251], v232 offset:12544\n" \
"v_cvt_pk_bf16_f32 v50, v52, v53\n" \
"v_smfmac_f32_16x16x64_bf16 v[228:231], v[244:247], v[112:119], v243\n" \
"v_mfma_f32_16x16x32_bf16 v[188:191], a[40:43], v[80:83], v[188:191]\n" \
"v_exp_f32 v54, v54\n" \
"v_mfma_f32_16x16x32_bf16 v[188:191], a[44:47], v[84:87], v[188:191]\n" \
"s_cmp_lg_u32 s82, 0\n" \
"v_exp_f32_e64 v55, v55\n" \
"v_mfma_f32_16x16x32_bf16 v[156:159], a[40:43], v[64:67], v[156:159]\n" \
"s_waitcnt lgkmcnt(2)\n" \
"ds_read_b128 a[252:255], v232 offset:14592\n" \
"v_cvt_pk_bf16_f32 v51, v54, v55\n" \
"v_mfma_f32_16x16x32_bf16 v[156:159], a[44:47], v[68:71], v[156:159]\n" \
"s_cbranch_scc1 Lep_%=\n" \
"Lepr_%=:\n" \
"v_mfma_f32_16x16x32_bf16 v[64:67], a[192:195], a[0:3], v[196:199]\n" \
"v_exp_f32 v8, v8\n" \
"v_mfma_f32_16x16x32_bf16 v[64:67], a[196:199], a[4:7], v[64:67]\n" \
"ds_read_b128 a[44:47], v233 offset:15360\n" \
"v_mfma_f32_16x16x32_bf16 v[64:67], a[200:203], a[8:11], v[64:67]\n" \
"v_exp_f32 v9, v9\n" \
"v_mfma_f32_16x16x32_bf16 v[64:67], a[204:207], a[12:15], v[64:67]\n" \
"ds_read_b128 a[32:35], v233 offset:6144\n" \
"v_cvt_pk_bf16_f32 v4, v8, v9\n" \
"v_mfma_f32_16x16x32_bf16 v[80:83], a[204:207], a[28:31], v[196:199]\n" \
"s_sub_u32 s84, s84, 1\n" \
"v_exp_f32 v10, v10\n" \
"v_mfma_f32_16x16x32_bf16 v[80:83], a[200:203], a[24:27], v[80:83]\n" \
"s_cmp_eq_u32 s84, 0\n" \
"s_cselect_b32 s70, s75, 0x8000\n" \
"v_xor_b32 v232, s70, v232\n" \
"v_mfma_f32_16x16x32_bf16 v[80:83], a[196:199], a[20:23], v[80:83]\n" \
"v_exp_f32 v11, v11\n" \
"v_mfma_f32_16x16x32_bf16 v[80:83], a[192:195], a[16:19], v[80:83]\n" \
"ds_read_b128 a[36:39], v233 offset:14336\n" \
"v_cvt_pk_bf16_f32 v5, v10, v11\n" \
"v_mfma_f32_16x16x32_bf16 v[96:99], a[192:195], a[160:163], v[196:199]\n" \
"v_exp_f32 v12, v12\n" \
"v_mfma_f32_16x16x32_bf16 v[96:99], a[196:199], a[164:167], v[96:99]\n" \
"v_mfma_f32_16x16x32_bf16 v[96:99], a[200:203], a[168:171], v[96:99]\n" \
"v_exp_f32 v13, v13\n" \
"v_mfma_f32_16x16x32_bf16 v[96:99], a[204:207], a[172:175], v[96:99]\n" \
"ds_read_b128 a[40:43], v233 offset:7168\n" \
"v_cvt_pk_bf16_f32 v6, v12, v13\n" \
"v_mfma_f32_16x16x32_bf16 v[112:115], a[204:207], a[188:191], v[196:199]\n" \
"v_exp_f32 v14, v14\n" \
"v_mfma_f32_16x16x32_bf16 v[112:115], a[200:203], a[184:187], v[112:115]\n" \
"v_mfma_f32_16x16x32_bf16 v[112:115], a[196:199], a[180:183], v[112:115]\n" \
"v_exp_f32 v15, v15\n" \
"v_mfma_f32_16x16x32_bf16 v[112:115], a[192:195], a[176:179], v[112:115]\n" \
"ds_read_b128 a[204:207], v233 offset:9216\n" \
"v_cvt_pk_bf16_f32 v7, v14, v15\n" \
"v_mfma_f32_16x16x32_bf16 v[116:119], a[208:211], a[176:179], v[196:199]\n" \
"v_exp_f32 v24, v24\n" \
"v_mfma_f32_16x16x32_bf16 v[116:119], a[212:215], a[180:183], v[116:119]\n" \
"v_mfma_f32_16x16x32_bf16 v[116:119], a[216:219], a[184:187], v[116:119]\n" \
"v_exp_f32 v25, v25\n" \
"v_mfma_f32_16x16x32_bf16 v[116:119], a[220:223], a[188:191], v[116:119]\n" \
"ds_read_b128 a[200:203], v233 offset:1024\n" \
"v_cvt_pk_bf16_f32 v20, v24, v25\n" \
"v_mfma_f32_16x16x32_bf16 v[100:103], a[220:223], a[172:175], v[196:199]\n" \
"v_exp_f32 v26, v26\n" \
"v_mfma_f32_16x16x32_bf16 v[100:103], a[216:219], a[168:171], v[100:103]\n" \
"v_mfma_f32_16x16x32_bf16 v[100:103], a[212:215], a[164:167], v[100:103]\n" \
"v_exp_f32 v27, v27\n" \
"v_mfma_f32_16x16x32_bf16 v[100:103], a[208:211], a[160:163], v[100:103]\n" \
"ds_read_b128 a[196:199], v233 offset:8192\n" \
"v_cvt_pk_bf16_f32 v21, v26, v27\n" \
"v_mfma_f32_16x16x32_bf16 v[84:87], a[208:211], a[16:19], v[196:199]\n" \
"v_exp_f32 v28, v28\n" \
"v_mfma_f32_16x16x32_bf16 v[84:87], a[212:215], a[20:23], v[84:87]\n" \
"v_mfma_f32_16x16x32_bf16 v[84:87], a[216:219], a[24:27], v[84:87]\n" \
"v_exp_f32 v29, v29\n" \
"v_mfma_f32_16x16x32_bf16 v[84:87], a[220:223], a[28:31], v[84:87]\n" \
"ds_read_b128 a[192:195], v233 offset:0\n" \
"v_cvt_pk_bf16_f32 v22, v28, v29\n" \
"v_mfma_f32_16x16x32_bf16 v[68:71], a[220:223], a[12:15], v[196:199]\n" \
"v_exp_f32 v30, v30\n" \
"v_mfma_f32_16x16x32_bf16 v[68:71], a[216:219], a[8:11], v[68:71]\n" \
"v_mfma_f32_16x16x32_bf16 v[68:71], a[212:215], a[4:7], v[68:71]\n" \
"s_waitcnt lgkmcnt(8)\n" \
"v_exp_f32 v31, v31\n" \
"v_mfma_f32_16x16x32_bf16 v[68:71], a[208:211], a[0:3], v[68:71]\n" \
"ds_read_b128 a[220:223], v233 offset:11264\n" \
"v_cvt_pk_bf16_f32 v23, v30, v31\n" \
"v_mfma_f32_16x16x32_bf16 v[72:75], a[224:227], a[0:3], v[196:199]\n" \
"v_exp_f32 v40, v40\n" \
"v_mfma_f32_16x16x32_bf16 v[72:75], a[228:231], a[4:7], v[72:75]\n" \
"v_mfma_f32_16x16x32_bf16 v[72:75], a[232:235], a[8:11], v[72:75]\n" \
"v_exp_f32 v41, v41\n" \
"v_mfma_f32_16x16x32_bf16 v[72:75], a[236:239], a[12:15], v[72:75]\n" \
"ds_read_b128 a[216:219], v233 offset:3072\n" \
"v_cvt_pk_bf16_f32 v36, v40, v41\n" \
"v_mfma_f32_16x16x32_bf16 v[88:91], a[236:239], a[28:31], v[196:199]\n" \
"v_exp_f32 v42, v42\n" \
"v_mfma_f32_16x16x32_bf16 v[88:91], a[232:235], a[24:27], v[88:91]\n" \
"v_mfma_f32_16x16x32_bf16 v[88:91], a[228:231], a[20:23], v[88:91]\n" \
"v_exp_f32 v43, v43\n" \
"v_mfma_f32_16x16x32_bf16 v[88:91], a[224:227], a[16:19], v[88:91]\n" \
"ds_read_b128 a[212:215], v233 offset:10240\n" \
"v_cvt_pk_bf16_f32 v37, v42, v43\n" \
"v_mfma_f32_16x16x32_bf16 v[104:107], a[224:227], a[160:163], v[196:199]\n" \
"v_exp_f32 v44, v44\n" \
"v_mfma_f32_16x16x32_bf16 v[104:107], a[228:231], a[164:167], v[104:107]\n" \
"v_mfma_f32_16x16x32_bf16 v[104:107], a[232:235], a[168:171], v[104:107]\n" \
"v_exp_f32 v45, v45\n" \
"v_mfma_f32_16x16x32_bf16 v[104:107], a[236:239], a[172:175], v[104:107]\n" \
"ds_read_b128 a[208:211], v233 offset:2048\n" \
"v_cvt_pk_bf16_f32 v38, v44, v45\n" \
"v_mfma_f32_16x16x32_bf16 v[120:123], a[236:239], a[188:191], v[196:199]\n" \
"v_exp_f32 v46, v46\n" \
"v_mfma_f32_16x16x32_bf16 v[120:123], a[232:235], a[184:187], v[120:123]\n" \
"v_mfma_f32_16x16x32_bf16 v[120:123], a[228:231], a[180:183], v[120:123]\n" \
"v_exp_f32 v47, v47\n" \
"v_mfma_f32_16x16x32_bf16 v[120:123], a[224:227], a[176:179], v[120:123]\n" \
"ds_read_b128 a[236:239], v233 offset:13312\n" \
"v_cvt_pk_bf16_f32 v39, v46, v47\n" \
"v_mfma_f32_16x16x32_bf16 v[124:127], a[240:243], a[176:179], v[196:199]\n" \
"v_exp_f32 v56, v56\n" \
"v_mfma_f32_16x16x32_bf16 v[124:127], a[244:247], a[180:183], v[124:127]\n" \
"ds_read_b128 a[232:235], v233 offset:5120\n" \
"v_mfma_f32_16x16x32_bf16 v[124:127], a[248:251], a[184:187], v[124:127]\n" \
"v_exp_f32 v57, v57\n" \
"v_mfma_f32_16x16x32_bf16 v[124:127], a[252:255], a[188:191], v[124:127]\n" \
"ds_read_b128 a[228:231], v233 offset:12288\n" \
"v_cvt_pk_bf16_f32 v52, v56, v57\n" \
"v_mfma_f32_16x16x32_bf16 v[108:111], a[252:255], a[172:175], v[196:199]\n" \
"v_exp_f32 v58, v58\n" \
"v_mfma_f32_16x16x32_bf16 v[108:111], a[248:251], a[168:171], v[108:111]\n" \
"ds_read_b128 a[224:227], v233 offset:4096\n" \
"v_mfma_f32_16x16x32_bf16 v[108:111], a[244:247], a[164:167], v[108:111]\n" \
"v_exp_f32 v59, v59\n" \
"v_mfma_f32_16x16x32_bf16 v[108:111], a[240:243], a[160:163], v[108:111]\n" \
"v_cvt_pk_bf16_f32 v53, v58, v59\n" \
"v_mfma_f32_16x16x32_bf16 v[92:95], a[240:243], a[16:19], v[196:199]\n" \
"v_exp_f32 v60, v60\n" \
"v_mfma_f32_16x16x32_bf16 v[92:95], a[244:247], a[20:23], v[92:95]\n" \
"v_mfma_f32_16x16x32_bf16 v[92:95], a[248:251], a[24:27], v[92:95]\n" \
"v_exp_f32 v61, v61\n" \
"v_mfma_f32_16x16x32_bf16 v[92:95], a[252:255], a[28:31], v[92:95]\n" \
"v_cvt_pk_bf16_f32 v54, v60, v61\n" \
"v_mfma_f32_16x16x32_bf16 v[76:79], a[252:255], a[12:15], v[196:199]\n" \
"v_exp_f32 v62, v62\n" \
"v_mfma_f32_16x16x32_bf16 v[76:79], a[248:251], a[8:11], v[76:79]\n" \
"s_cmp_lg_u32 s82, 0\n" \
"s_cbranch_scc1 Lnw_%=\n" \
"s_waitcnt vmcnt(0)\n" \
"Lnw_%=:\n" \
"v_mfma_f32_16x16x32_bf16 v[76:79], a[244:247], a[4:7], v[76:79]\n" \
"s_waitcnt lgkmcnt(0)\n" \
"v_exp_f32 v63, v63\n" \
"v_mfma_f32_16x16x32_bf16 v[76:79], a[240:243], a[0:3], v[76:79]\n" \
"s_barrier\n" \
"v_cvt_pk_bf16_f32 v55, v62, v63\n" \
"v_mfma_f32_16x16x32_bf16 v[128:131], a[192:195], v[0:3], v[128:131]\n" \
"s_add_u32 m0, s74, s79\n" \
"v_exp_f32 v64, v64\n" \
"v_mfma_f32_16x16x32_bf16 v[128:131], a[196:199], v[4:7], v[128:131]\n" \
"buffer_load_dwordx4 v234, s[36:39], s73 offen offset:0 sc1 lds\n" \
"v_mfma_f32_16x16x32_bf16 v[160:163], a[192:195], v[16:19], v[160:163]\n" \
"v_exp_f32 v65, v65\n" \
"v_mfma_f32_16x16x32_bf16 v[160:163], a[196:199], v[20:23], v[160:163]\n" \
"ds_read_b128 a[240:243], v232 offset:8448\n" \
"v_cvt_pk_bf16_f32 v64, v64, v65\n" \
"v_mfma_f32_16x16x32_bf16 a[64:67], a[192:195], v[32:35], a[64:67]\n" \
"s_add_u32 s71, s73, 17408\n" \
"v_exp_f32 v66, v66\n" \
"v_mfma_f32_16x16x32_bf16 a[64:67], a[196:199], v[36:39], a[64:67]\n" \
"buffer_load_dwordx4 v234, s[36:39], s73 offen offset:1024 sc1 lds\n" \
"v_mfma_f32_16x16x32_bf16 a[96:99], a[192:195], v[48:51], a[96:99]\n" \
"v_exp_f32 v67, v67\n" \
"v_mfma_f32_16x16x32_bf16 a[96:99], a[196:199], v[52:55], a[96:99]\n" \
"ds_read_b128 a[192:195], v232 offset:0\n" \
"v_cvt_pk_bf16_f32 v65, v66, v67\n" \
"v_mfma_f32_16x16x32_bf16 a[100:103], a[200:203], v[48:51], a[100:103]\n" \
"v_exp_f32 v68, v68\n" \
"v_mfma_f32_16x16x32_bf16 a[100:103], a[204:207], v[52:55], a[100:103]\n" \
"buffer_load_dwordx4 v234, s[36:39], s73 offen offset:2048 sc1 lds\n" \
"v_mfma_f32_16x16x32_bf16 a[68:71], a[200:203], v[32:35], a[68:71]\n" \
"v_exp_f32 v69, v69\n" \
"v_mfma_f32_16x16x32_bf16 a[68:71], a[204:207], v[36:39], a[68:71]\n" \
"ds_read_b128 a[196:199], v232 offset:2048\n" \
"v_cvt_pk_bf16_f32 v66, v68, v69\n" \
"v_smfmac_f32_16x16x64_bf16 a[48:51], v[244:247], v[0:7], v243\n" \
"buffer_load_dwordx4 v234, s[36:39], s73 offen offset:3072 sc1 lds\n" \
"v_mfma_f32_16x16x32_bf16 v[164:167], a[200:203], v[16:19], v[164:167]\n" \
"s_add_u32 m0, m0, 0x4000\n" \
"v_exp_f32 v70, v70\n" \
"v_mfma_f32_16x16x32_bf16 v[164:167], a[204:207], v[20:23], v[164:167]\n" \
"buffer_load_dwordx4 v234, s[36:39], s71 offen offset:0 sc1 lds\n" \
"v_mfma_f32_16x16x32_bf16 v[132:135], a[200:203], v[0:3], v[132:135]\n" \
"v_exp_f32 v71, v71\n" \
"v_mfma_f32_16x16x32_bf16 v[132:135], a[204:207], v[4:7], v[132:135]\n" \
"ds_read_b128 a[200:203], v232 offset:4096\n" \
"v_cvt_pk_bf16_f32 v67, v70, v71\n" \
"v_mfma_f32_16x16x32_bf16 v[136:139], a[208:211], v[0:3], v[136:139]\n" \
"v_exp_f32 v80, v80\n" \
"v_mfma_f32_16x16x32_bf16 v[136:139], a[212:215], v[4:7], v[136:139]\n" \
"buffer_load_dwordx4 v234, s[36:39], s71 offen offset:1024 sc1 lds\n" \
"v_mfma_f32_16x16x32_bf16 v[168:171], a[208:211], v[16:19], v[168:171]\n" \
"v_exp_f32 v81, v81\n" \
"v_mfma_f32_16x16x32_bf16 v[168:171], a[212:215], v[20:23], v[168:171]\n" \
"ds_read_b128 a[204:207], v232 offset:6144\n" \
"v_cvt_pk_bf16_f32 v80, v80, v81\n" \
"v_mfma_f32_16x16x32_bf16 a[72:75], a[208:211], v[32:35], a[72:75]\n" \
"v_exp_f32 v82, v82\n" \
"v_mfma_f32_16x16x32_bf16 a[72:75], a[212:215], v[36:39], a[72:75]\n" \
"buffer_load_dwordx4 v234, s[36:39], s71 offen offset:2048 sc1 lds\n" \
"v_mfma_f32_16x16x32_bf16 a[104:107], a[208:211], v[48:51], a[104:107]\n" \
"v_exp_f32 v83, v83\n" \
"v_mfma_f32_16x16x32_bf16 a[104:107], a[212:215], v[52:55], a[104:107]\n" \
"ds_read_b128 a[208:211], v232 offset:256\n" \
"v_cvt_pk_bf16_f32 v81, v82, v83\n" \
"v_mfma_f32_16x16x32_bf16 a[108:111], a[216:219], v[48:51], a[108:111]\n" \
"v_exp_f32 v84, v84\n" \
"v_mfma_f32_16x16x32_bf16 a[108:111], a[220:223], v[52:55], a[108:111]\n" \
"buffer_load_dwordx4 v234, s[36:39], s71 offen offset:3072 sc1 lds\n" \
"v_mfma_f32_16x16x32_bf16 a[76:79], a[216:219], v[32:35], a[76:79]\n" \
"v_exp_f32 v85, v85\n" \
"v_mfma_f32_16x16x32_bf16 a[76:79], a[220:223], v[36:39], a[76:79]\n" \
"s_add_u32 m0, s77, s80\n" \
"ds_read_b128 a[212:215], v232 offset:2304\n" \
"v_cvt_pk_bf16_f32 v82, v84, v85\n" \
"v_smfmac_f32_16x16x64_bf16 a[52:55], v[244:247], v[16:23], v243\n" \
"buffer_load_dwordx4 v234, s[40:43], s76 offen offset:0 sc1 lds\n" \
"v_mfma_f32_16x16x32_bf16 v[172:175], a[216:219], v[16:19], v[172:175]\n" \
"s_add_u32 s71, s76, 17408\n" \
"v_exp_f32 v86, v86\n" \
"v_mfma_f32_16x16x32_bf16 v[172:175], a[220:223], v[20:23], v[172:175]\n" \
"buffer_load_dwordx4 v234, s[40:43], s76 offen offset:1024 sc1 lds\n" \
"v_mfma_f32_16x16x32_bf16 v[140:143], a[216:219], v[0:3], v[140:143]\n" \
"v_exp_f32 v87, v87\n" \
"v_mfma_f32_16x16x32_bf16 v[140:143], a[220:223], v[4:7], v[140:143]\n" \
"ds_read_b128 a[216:219], v232 offset:4352\n" \
"v_cvt_pk_bf16_f32 v83, v86, v87\n" \
"v_mfma_f32_16x16x32_bf16 v[144:147], a[224:227], v[0:3], v[144:147]\n" \
"v_exp_f32 v96, v96\n" \
"v_mfma_f32_16x16x32_bf16 v[144:147], a[228:231], v[4:7], v[144:147]\n" \
"buffer_load_dwordx4 v234, s[40:43], s76 offen offset:2048 sc1 lds\n" \
"v_mfma_f32_16x16x32_bf16 v[176:179], a[224:227], v[16:19], v[176:179]\n" \
"v_exp_f32 v97, v97\n" \
"v_mfma_f32_16x16x32_bf16 v[176:179], a[228:231], v[20:23], v[176:179]\n" \
"ds_read_b128 a[220:223], v232 offset:6400\n" \
"v_cvt_pk_bf16_f32 v96, v96, v97\n" \
"v_mfma_f32_16x16x32_bf16 a[80:83], a[224:227], v[32:35], a[80:83]\n" \
"v_exp_f32 v98, v98\n" \
"v_mfma_f32_16x16x32_bf16 a[80:83], a[228:231], v[36:39], a[80:83]\n" \
"buffer_load_dwordx4 v234, s[40:43], s76 offen offset:3072 sc1 lds\n" \
"v_mfma_f32_16x16x32_bf16 a[112:115], a[224:227], v[48:51], a[112:115]\n" \
"v_exp_f32 v99, v99\n" \
"v_mfma_f32_16x16x32_bf16 a[112:115], a[228:231], v[52:55], a[112:115]\n" \
"ds_read_b128 a[224:227], v232 offset:8192\n" \
"v_cvt_pk_bf16_f32 v97, v98, v99\n" \
"v_mfma_f32_16x16x32_bf16 a[116:119], a[232:235], v[48:51], a[116:119]\n" \
"s_add_u32 m0, m0, 0x4000\n" \
"v_exp_f32 v100, v100\n" \
"v_mfma_f32_16x16x32_bf16 a[116:119], a[236:239], v[52:55], a[116:119]\n" \
"buffer_load_dwordx4 v234, s[40:43], s71 offen offset:0 sc1 lds\n" \
"v_mfma_f32_16x16x32_bf16 a[84:87], a[232:235], v[32:35], a[84:87]\n" \
"v_exp_f32 v101, v101\n" \
"v_mfma_f32_16x16x32_bf16 a[84:87], a[236:239], v[36:39], a[84:87]\n" \
"ds_read_b128 a[228:231], v232 offset:10240\n" \
"v_cvt_pk_bf16_f32 v98, v100, v101\n" \
"v_smfmac_f32_16x16x64_bf16 v[224:227], v[244:247], v[32:39], v243\n" \
"buffer_load_dwordx4 v234, s[40:43], s71 offen offset:1024 sc1 lds\n" \
"v_mfma_f32_16x16x32_bf16 v[180:183], a[232:235], v[16:19], v[180:183]\n" \
"v_exp_f32 v102, v102\n" \
"v_mfma_f32_16x16x32_bf16 v[180:183], a[236:239], v[20:23], v[180:183]\n" \
"buffer_load_dwordx4 v234, s[40:43], s71 offen offset:2048 sc1 lds\n" \
"v_mfma_f32_16x16x32_bf16 v[148:151], a[232:235], v[0:3], v[148:151]\n" \
"v_exp_f32 v103, v103\n" \
"v_mfma_f32_16x16x32_bf16 v[148:151], a[236:239], v[4:7], v[148:151]\n" \
"ds_read_b128 a[232:235], v232 offset:12288\n" \
"v_cvt_pk_bf16_f32 v99, v102, v103\n" \
"v_mfma_f32_16x16x32_bf16 v[152:155], a[32:35], v[0:3], v[152:155]\n" \
"v_exp_f32 v112, v112\n" \
"v_mfma_f32_16x16x32_bf16 v[152:155], a[36:39], v[4:7], v[152:155]\n" \
"buffer_load_dwordx4 v234, s[40:43], s71 offen offset:3072 sc1 lds\n" \
"v_mfma_f32_16x16x32_bf16 v[184:187], a[32:35], v[16:19], v[184:187]\n" \
"v_exp_f32 v113, v113\n" \
"v_mfma_f32_16x16x32_bf16 v[184:187], a[36:39], v[20:23], v[184:187]\n" \
"ds_read_b128 a[236:239], v232 offset:14336\n" \
"v_cvt_pk_bf16_f32 v112, v112, v113\n" \
"v_mfma_f32_16x16x32_bf16 a[88:91], a[32:35], v[32:35], a[88:91]\n" \
"v_exp_f32 v114, v114\n" \
"v_mfma_f32_16x16x32_bf16 a[88:91], a[36:39], v[36:39], a[88:91]\n" \
"buffer_load_dword v239, v242, s[56:59], s95 offen sc1\n" \
"v_mfma_f32_16x16x32_bf16 a[120:123], a[32:35], v[48:51], a[120:123]\n" \
"s_mov_b32 s69, s92\n" \
"v_exp_f32 v115, v115\n" \
"v_mfma_f32_16x16x32_bf16 a[120:123], a[36:39], v[52:55], a[120:123]\n" \
"s_mov_b32 s76, s73\n" \
"ds_read_b128 a[244:247], v232 offset:10496\n" \
"v_cvt_pk_bf16_f32 v113, v114, v115\n" \
"v_mfma_f32_16x16x32_bf16 a[124:127], a[40:43], v[48:51], a[124:127]\n" \
"s_add_u32 s73, s73, 34816\n" \
"v_exp_f32 v116, v116\n" \
"v_mfma_f32_16x16x32_bf16 a[124:127], a[44:47], v[52:55], a[124:127]\n" \
"s_xor_b32 s74, s74, 0x8000\n" \
"s_xor_b32 s77, s77, 0x8000\n" \
"v_xor_b32 v233, 0x8000, v233\n" \
"v_mfma_f32_16x16x32_bf16 a[92:95], a[40:43], v[32:35], a[92:95]\n" \
"s_mov_b32 s82, 0\n" \
"v_exp_f32 v117, v117\n" \
"v_mfma_f32_16x16x32_bf16 a[92:95], a[44:47], v[36:39], a[92:95]\n" \
"ds_read_b128 a[248:251], v232 offset:12544\n" \
"v_cvt_pk_bf16_f32 v114, v116, v117\n" \
"Lpg6775_%=:\n" \
"s_cbranch_execz Lcb0_%=\n" \
"v_smfmac_f32_16x16x64_bf16 v[228:231], v[244:247], v[48:55], v243\n" \
"v_mfma_f32_16x16x32_bf16 v[188:191], a[40:43], v[16:19], v[188:191]\n" \
"v_exp_f32_e64 v118, v118\n" \
"v_mfma_f32_16x16x32_bf16 v[188:191], a[44:47], v[20:23], v[188:191]\n" \
"s_cmp_lg_u32 s84, 0\n" \
"v_exp_f32_e64 v119, v119\n" \
"v_mfma_f32_16x16x32_bf16 v[156:159], a[40:43], v[0:3], v[156:159]\n" \
"s_waitcnt lgkmcnt(2)\n" \
"ds_read_b128 a[252:255], v232 offset:14592\n" \
"v_cvt_pk_bf16_f32 v115, v118, v119\n" \
"v_mfma_f32_16x16x32_bf16 v[156:159], a[44:47], v[4:7], v[156:159]\n" \
"s_cbranch_scc1 Lloop2_%=\n" \
"Lg_%=:\n" \
"Lpg6789_%=:\n" \
"s_cbranch_execz Lcb0_%=\n" \
"v_mfma_f32_16x16x32_bf16 v[32:35], a[192:195], a[160:163], v[208:211]\n" \
"s_mov_b64 s[44:45], s[20:21]\n" \
"s_memrealtime s[14:15]\n" \
"v_exp_f32 v72, v72\n" \
"v_mfma_f32_16x16x32_bf16 v[32:35], a[196:199], a[164:167], v[32:35]\n" \
"s_and_b32 s45, s45, 0xffff\n" \
"ds_read_b128 a[32:35], v233 offset:6144\n" \
"ds_read_b128 a[36:39], v233 offset:14336\n" \
"v_mfma_f32_16x16x32_bf16 v[32:35], a[200:203], a[168:171], v[32:35]\n" \
"s_mov_b32 s46, 4194304\n" \
"v_exp_f32 v73, v73\n" \
"v_mfma_f32_16x16x32_bf16 v[32:35], a[204:207], a[172:175], v[32:35]\n" \
"ds_read_b128 a[40:43], v233 offset:7168\n" \
"ds_read_b128 a[44:47], v233 offset:15360\n" \
"global_load_dword v253, v254, s[22:23]\n" \
"v_mfma_f32_16x16x32_bf16 v[48:51], a[204:207], a[188:191], v[196:199]\n" \
"s_mov_b32 s47, 0x20000\n" \
"v_exp_f32 v74, v74\n" \
"v_mfma_f32_16x16x32_bf16 v[48:51], a[200:203], a[184:187], v[48:51]\n" \
"s_mov_b64 s[48:49], s[22:23]\n" \
"v_exp_f32 v75, v75\n" \
"v_mfma_f32_16x16x32_bf16 v[48:51], a[196:199], a[180:183], v[48:51]\n" \
"s_and_b32 s49, s49, 0xffff\n" \
"v_exp_f32 v76, v76\n" \
"v_mfma_f32_16x16x32_bf16 v[48:51], a[192:195], a[176:179], v[48:51]\n" \
"s_mov_b32 s50, 6291456\n" \
"ds_read_b128 a[200:203], v233 offset:1024\n" \
"ds_read_b128 a[204:207], v233 offset:9216\n" \
"v_mfma_f32_16x16x32_bf16 v[52:55], a[208:211], a[176:179], v[196:199]\n" \
"s_mov_b32 s51, 0x20000\n" \
"v_cvt_pk_bf16_f32 v68, v72, v73\n" \
"v_cvt_pk_bf16_f32 v69, v74, v75\n" \
"v_mfma_f32_16x16x32_bf16 v[52:55], a[212:215], a[180:183], v[52:55]\n" \
"s_bfe_u32 s94, %1, 0x20001\n" \
"v_exp_f32 v77, v77\n" \
"v_mfma_f32_16x16x32_bf16 v[52:55], a[216:219], a[184:187], v[52:55]\n" \
"s_lshl_b32 s94, s94, 2\n" \
"v_exp_f32 v78, v78\n" \
"v_mfma_f32_16x16x32_bf16 v[52:55], a[220:223], a[188:191], v[52:55]\n" \
"s_lshr_b32 s70, %1, 6\n" \
"ds_read_b128 a[192:195], v233 offset:0\n" \
"ds_read_b128 a[196:199], v233 offset:8192\n" \
"v_mfma_f32_16x16x32_bf16 v[36:39], a[220:223], a[172:175], v[212:215]\n" \
"s_add_u32 s94, s94, s70\n" \
"v_exp_f32 v79, v79\n" \
"v_mfma_f32_16x16x32_bf16 v[36:39], a[216:219], a[168:171], v[36:39]\n" \
"s_lshr_b32 s71, s22, 7\n" \
"v_exp_f32 v88, v88\n" \
"v_mfma_f32_16x16x32_bf16 v[36:39], a[212:215], a[164:167], v[36:39]\n" \
"s_sub_u32 s71, s63, s71\n" \
"v_cvt_pk_bf16_f32 v70, v76, v77\n" \
"v_cvt_pk_bf16_f32 v71, v78, v79\n" \
"v_mfma_f32_16x16x32_bf16 v[36:39], a[208:211], a[160:163], v[36:39]\n" \
"s_add_u32 s71, s71, 6\n" \
"ds_read_b128 a[216:219], v233 offset:3072\n" \
"ds_read_b128 a[220:223], v233 offset:11264\n" \
"v_mfma_f32_16x16x32_bf16 v[40:43], a[224:227], a[160:163], v[216:219]\n" \
"s_and_b32 s71, s71, 7\n" \
"v_exp_f32 v89, v89\n" \
"v_mfma_f32_16x16x32_bf16 v[40:43], a[228:231], a[164:167], v[40:43]\n" \
"s_and_b32 s70, %1, 1\n" \
"v_exp_f32 v90, v90\n" \
"v_mfma_f32_16x16x32_bf16 v[40:43], a[232:235], a[168:171], v[40:43]\n" \
"s_lshl_b32 s70, s70, 3\n" \
"v_exp_f32 v91, v91\n" \
"v_mfma_f32_16x16x32_bf16 v[40:43], a[236:239], a[172:175], v[40:43]\n" \
"s_add_u32 s70, s70, s71\n" \
"ds_read_b128 a[208:211], v233 offset:2048\n" \
"ds_read_b128 a[212:215], v233 offset:10240\n" \
"v_mfma_f32_16x16x32_bf16 v[56:59], a[236:239], a[188:191], v[196:199]\n" \
"s_lshl_b32 s78, s70, 7\n" \
"v_exp_f32 v92, v92\n" \
"v_mfma_f32_16x16x32_bf16 v[56:59], a[232:235], a[184:187], v[56:59]\n" \
"s_lshr_b32 s80, %1, 6\n" \
"v_cvt_pk_bf16_f32 v84, v88, v89\n" \
"v_cvt_pk_bf16_f32 v85, v90, v91\n" \
"v_mfma_f32_16x16x32_bf16 v[56:59], a[228:231], a[180:183], v[56:59]\n" \
"s_xor_b32 s80, s80, s63\n" \
"v_exp_f32 v93, v93\n" \
"v_mfma_f32_16x16x32_bf16 v[56:59], a[224:227], a[176:179], v[56:59]\n" \
"s_and_b32 s80, s80, 0\n" \
"ds_read_b128 a[232:235], v233 offset:5120\n" \
"ds_read_b128 a[236:239], v233 offset:13312\n" \
"v_mfma_f32_16x16x32_bf16 v[60:63], a[240:243], a[176:179], v[196:199]\n" \
"s_waitcnt lgkmcnt(2)\n" \
"s_lshl_b32 s93, s70, 12\n" \
"v_exp_f32 v94, v94\n" \
"v_mfma_f32_16x16x32_bf16 v[60:63], a[244:247], a[180:183], v[60:63]\n" \
"s_mul_i32 s91, s61, 6144\n" \
"v_exp_f32 v95, v95\n" \
"v_mfma_f32_16x16x32_bf16 v[60:63], a[248:251], a[184:187], v[60:63]\n" \
"s_add_u32 s91, s91, %3\n" \
"v_cvt_pk_bf16_f32 v86, v92, v93\n" \
"v_mfma_f32_16x16x32_bf16 v[60:63], a[252:255], a[188:191], v[60:63]\n" \
"s_lshl_b32 s71, s80, 7\n" \
"ds_read_b128 a[224:227], v233 offset:4096\n" \
"ds_read_b128 a[228:231], v233 offset:12288\n" \
"v_mfma_f32_16x16x32_bf16 v[44:47], a[252:255], a[172:175], v[220:223]\n" \
"s_lshl_b32 s75, s94, 18\n" \
"v_cvt_pk_bf16_f32 v87, v94, v95\n" \
"v_mfma_f32_16x16x32_bf16 v[44:47], a[248:251], a[168:171], v[44:47]\n" \
"s_lshl_b32 s70, s61, 16\n" \
"v_exp_f32 v104, v104\n" \
"v_mfma_f32_16x16x32_bf16 v[44:47], a[244:247], a[164:167], v[44:47]\n" \
"s_add_u32 s73, s75, s70\n" \
"v_accvgpr_read_b32 v197, a56\n" \
"v_accvgpr_read_b32 v192, a57\n" \
"v_mfma_f32_16x16x32_bf16 v[44:47], a[240:243], a[160:163], v[44:47]\n" \
"s_add_u32 s73, s73, s71\n" \
"v_accvgpr_read_b32 v196, a58\n" \
"v_add_u32 v198, 1024, v197\n" \
"v_mfma_f32_16x16x32_bf16 v[128:131], a[192:195], v[64:67], v[128:131]\n" \
"s_max_u32 s75, s75, 32768\n" \
"v_add_u32 v199, 2048, v197\n" \
"v_add_u32 v193, 31744, v192\n" \
"v_mfma_f32_16x16x32_bf16 v[128:131], a[196:199], v[68:71], v[128:131]\n" \
"s_sub_u32 s75, s75, 32768\n" \
"ds_read_b128 a[0:3], v232 offset:16384\n" \
"ds_read_b128 a[4:7], v232 offset:18432\n" \
"v_mfma_f32_16x16x32_bf16 v[160:163], a[192:195], v[80:83], v[160:163]\n" \
"s_add_u32 s75, s75, s71\n" \
"s_lshl_b32 s89, s61, 10\n" \
"v_mfma_f32_16x16x32_bf16 v[160:163], a[196:199], v[84:87], v[160:163]\n" \
"s_mul_i32 s76, s80, 393216\n" \
"s_add_u32 s89, s89, %3\n" \
"v_exp_f32 v105, v105\n" \
"v_mfma_f32_16x16x32_bf16 v[164:167], a[200:203], v[80:83], v[164:167]\n" \
"s_add_u32 s76, s76, s78\n" \
"ds_read_b128 a[8:11], v232 offset:20480\n" \
"ds_read_b128 a[12:15], v232 offset:22528\n" \
"v_mfma_f32_16x16x32_bf16 v[164:167], a[204:207], v[84:87], v[164:167]\n" \
"s_add_u32 m0, s91, 139264\n" \
"s_add_u32 s89, s89, 16384\n" \
"v_exp_f32 v106, v106\n" \
"v_mfma_f32_16x16x32_bf16 v[132:135], a[200:203], v[64:67], v[132:135]\n" \
"buffer_load_dwordx4 v197, s[48:51], s76 offen offset:0 lds\n" \
"v_mfma_f32_16x16x32_bf16 v[132:135], a[204:207], v[68:71], v[132:135]\n" \
"s_and_b32 s70, s61, 1\n" \
"ds_read_b128 a[16:19], v232 offset:16640\n" \
"ds_read_b128 a[20:23], v232 offset:18688\n" \
"v_smfmac_f32_16x16x64_bf16 a[48:51], v[244:247], v[64:71], v243\n" \
"s_add_u32 s77, s76, 24576\n" \
"v_cvt_pk_bf16_f32 v100, v104, v105\n" \
"v_mfma_f32_16x16x32_bf16 v[136:139], a[208:211], v[64:67], v[136:139]\n" \
"buffer_load_dwordx4 v198, s[48:51], s76 offen offset:1024 lds\n" \
"v_mfma_f32_16x16x32_bf16 v[136:139], a[212:215], v[68:71], v[136:139]\n" \
"s_lshl_b32 s70, s70, 11\n" \
"ds_read_b128 a[24:27], v232 offset:20736\n" \
"ds_read_b128 a[28:31], v232 offset:22784\n" \
"v_mfma_f32_16x16x32_bf16 v[168:171], a[208:211], v[80:83], v[168:171]\n" \
"s_xor_b32 s80, s80, 0x3\n" \
"v_exp_f32 v107, v107\n" \
"v_mfma_f32_16x16x32_bf16 v[168:171], a[212:215], v[84:87], v[168:171]\n" \
"buffer_load_dwordx4 v199, s[48:51], s76 offen offset:2048 lds\n" \
"v_exp_f32 v108, v108\n" \
"v_mfma_f32_16x16x32_bf16 v[172:175], a[216:219], v[80:83], v[172:175]\n" \
"s_add_u32 s93, s93, s70\n" \
"ds_read_b128 v[0:3], v232 offset:24576\n" \
"ds_read_b128 v[4:7], v232 offset:26624\n" \
"v_mfma_f32_16x16x32_bf16 v[172:175], a[220:223], v[84:87], v[172:175]\n" \
"s_add_u32 m0, m0, 3072\n" \
"v_exp_f32 v109, v109\n" \
"v_mfma_f32_16x16x32_bf16 v[140:143], a[216:219], v[64:67], v[140:143]\n" \
"buffer_load_dwordx4 v197, s[48:51], s77 offen offset:0 lds\n" \
"v_mfma_f32_16x16x32_bf16 v[140:143], a[220:223], v[68:71], v[140:143]\n" \
"s_add_u32 s93, s93, 52740096\n" \
"ds_read_b128 v[8:11], v232 offset:28672\n" \
"ds_read_b128 v[12:15], v232 offset:30720\n" \
"v_smfmac_f32_16x16x64_bf16 a[52:55], v[244:247], v[80:87], v243\n" \
"s_waitcnt lgkmcnt(4)\n" \
"s_cmp_eq_u32 s94, 0\n" \
"s_cselect_b32 s92, -1, 0\n" \
"v_cvt_pk_bf16_f32 v101, v106, v107\n" \
"v_mfma_f32_16x16x32_bf16 v[144:147], a[224:227], v[64:67], v[144:147]\n" \
"buffer_load_dwordx4 v198, s[48:51], s77 offen offset:1024 lds\n" \
"v_mfma_f32_16x16x32_bf16 v[144:147], a[228:231], v[68:71], v[144:147]\n" \
"s_mul_i32 s70, s94, 278528\n" \
"ds_read_b128 v[200:203], v232 offset:24832\n" \
"ds_read_b128 v[204:207], v232 offset:26880\n" \
"v_exp_f32 v110, v110\n" \
"v_mfma_f32_16x16x32_bf16 v[176:179], a[224:227], v[80:83], v[176:179]\n" \
"s_add_u32 s93, s93, s70\n" \
"v_exp_f32 v111, v111\n" \
"v_mfma_f32_16x16x32_bf16 v[176:179], a[228:231], v[84:87], v[176:179]\n" \
"buffer_load_dwordx4 v199, s[48:51], s77 offen offset:2048 lds\n" \
"v_mfma_f32_16x16x32_bf16 v[180:183], a[232:235], v[80:83], v[180:183]\n" \
"s_lshr_b32 s70, s61, 1\n" \
"ds_read_b128 a[240:243], v232 offset:28928\n" \
"ds_read_b128 a[244:247], v232 offset:30976\n" \
"v_mfma_f32_16x16x32_bf16 v[180:183], a[236:239], v[84:87], v[180:183]\n" \
"s_lshl_b32 s70, s70, 8\n" \
"v_exp_f32 v120, v120\n" \
"v_cvt_pk_bf16_f32 v102, v108, v109\n" \
"v_mfma_f32_16x16x32_bf16 v[148:151], a[232:235], v[64:67], v[148:151]\n" \
"s_add_u32 s93, s93, s70\n" \
"v_exp_f32 v121, v121\n" \
"v_mfma_f32_16x16x32_bf16 v[148:151], a[236:239], v[68:71], v[148:151]\n" \
"v_exp_f32 v122, v122\n" \
"v_mfma_f32_16x16x32_bf16 v[152:155], a[32:35], v[64:67], v[152:155]\n" \
"v_add_u32 v233, 0x10000, v235\n" \
"s_lshl_b32 s72, s61, 14\n" \
"v_mfma_f32_16x16x32_bf16 v[152:155], a[36:39], v[68:71], v[152:155]\n" \
"v_add_u32 v232, s72, v235\n" \
"v_cvt_pk_bf16_f32 v103, v110, v111\n" \
"v_mfma_f32_16x16x32_bf16 v[184:187], a[32:35], v[80:83], v[184:187]\n" \
"v_exp_f32 v123, v123\n" \
"v_mfma_f32_16x16x32_bf16 v[184:187], a[36:39], v[84:87], v[184:187]\n" \
"v_exp_f32 v124, v124\n" \
"v_cvt_pk_bf16_f32 v116, v120, v121\n" \
"v_mfma_f32_16x16x32_bf16 v[188:191], a[40:43], v[80:83], v[188:191]\n" \
"v_exp_f32 v125, v125\n" \
"v_mfma_f32_16x16x32_bf16 v[188:191], a[44:47], v[84:87], v[188:191]\n" \
"v_exp_f32_e64 v126, v126\n" \
"v_cvt_pk_bf16_f32 v117, v122, v123\n" \
"v_mfma_f32_16x16x32_bf16 v[156:159], a[40:43], v[64:67], v[156:159]\n" \
"v_exp_f32_e64 v127, v127\n" \
"v_mfma_f32_16x16x32_bf16 v[156:159], a[44:47], v[68:71], v[156:159]\n" \
"s_cmp_eq_u32 s63, 7\n" \
"s_waitcnt vmcnt(7) lgkmcnt(0)\n" \
"s_barrier\n" \
"s_cbranch_scc1 Lep_%=\n" \
"Lepg_%=:\n" \
"v_mfma_f32_16x16x32_bf16 v[16:19], a[0:3], a[176:179], v[208:211]\n" \
"s_add_u32 m0, s79, 110592\n" \
"ds_read_b128 v[64:67], v233 offset:16384\n" \
"ds_read_b128 v[68:71], v233 offset:24576\n" \
"v_mfma_f32_16x16x32_bf16 v[16:19], a[4:7], a[180:183], v[16:19]\n" \
"buffer_load_dwordx4 v192, s[44:47], s73 offen offset:0 lds\n" \
"v_mfma_f32_16x16x32_bf16 v[16:19], a[8:11], a[184:187], v[16:19]\n" \
"ds_read_b128 v[72:75], v233 offset:17408\n" \
"ds_read_b128 v[76:79], v233 offset:25600\n" \
"v_mfma_f32_16x16x32_bf16 v[16:19], a[12:15], a[188:191], v[16:19]\n" \
"ds_read_b128 v[80:83], v233 offset:18432\n" \
"ds_read_b128 v[84:87], v233 offset:26624\n" \
"v_cvt_pk_bf16_f32 v128, v128, v129\n" \
"v_cvt_pk_bf16_f32 v129, v130, v131\n" \
"v_cvt_pk_bf16_f32 v130, v132, v133\n" \
"v_cvt_pk_bf16_f32 v118, v124, v125\n" \
"v_mfma_f32_16x16x32_bf16 v[20:23], a[28:31], a[188:191], v[212:215]\n" \
"ds_read_b128 v[88:91], v233 offset:19456\n" \
"ds_read_b128 v[92:95], v233 offset:27648\n" \
"v_mfma_f32_16x16x32_bf16 v[20:23], a[24:27], a[184:187], v[20:23]\n" \
"buffer_load_dwordx4 v193, s[44:47], s73 offen offset:1024 lds\n" \
"v_cvt_pk_bf16_f32 v131, v134, v135\n" \
"v_cvt_pk_bf16_f32 v119, v126, v127\n" \
"v_xor_b32 v248, 16384, v232\n" \
"v_mfma_f32_16x16x32_bf16 v[20:23], a[20:23], a[180:183], v[20:23]\n" \
"ds_write_b128 v232, v[128:131] offset:0\n" \
"v_exp_f32 v32, v32\n" \
"v_mfma_f32_16x16x32_bf16 v[20:23], a[16:19], a[176:179], v[20:23]\n" \
"ds_read_b128 a[248:251], v233 offset:7168\n" \
"ds_read_b128 a[252:255], v233 offset:15360\n" \
"v_exp_f32 v33, v33\n" \
"v_mfma_f32_16x16x32_bf16 v[24:27], v[0:3], a[176:179], v[216:219]\n" \
"v_cvt_pk_bf16_f32 v132, v136, v137\n" \
"v_cvt_pk_bf16_f32 v133, v138, v139\n" \
"v_mfma_f32_16x16x32_bf16 v[24:27], v[4:7], a[180:183], v[24:27]\n" \
"buffer_load_dwordx4 v192, s[44:47], s73 offen offset:2048 lds\n" \
"v_exp_f32 v34, v34\n" \
"v_lshrrev_b32 v249, 1, v234\n" \
"v_mfma_f32_16x16x32_bf16 v[24:27], v[8:11], a[184:187], v[24:27]\n" \
"s_lshl_b32 s72, s61, 9\n" \
"v_cvt_pk_bf16_f32 v134, v140, v141\n" \
"v_cvt_pk_bf16_f32 v135, v142, v143\n" \
"v_mfma_f32_16x16x32_bf16 v[24:27], v[12:15], a[188:191], v[24:27]\n" \
"s_add_u32 s72, s72, 0x20000\n" \
"v_cvt_pk_bf16_f32 v136, v144, v145\n" \
"v_cvt_pk_bf16_f32 v137, v146, v147\n" \
"v_exp_f32 v35, v35\n" \
"v_exp_f32 v36, v36\n" \
"v_mfma_f32_16x16x32_bf16 v[28:31], a[244:247], a[188:191], v[220:223]\n" \
"ds_write_b128 v232, v[132:135] offset:1024\n" \
"s_add_u32 s72, s72, %3\n" \
"v_cvt_pk_bf16_f32 v32, v32, v33\n" \
"v_mfma_f32_16x16x32_bf16 v[28:31], a[240:243], a[184:187], v[28:31]\n" \
"buffer_load_dwordx4 v193, s[44:47], s73 offen offset:3072 lds\n" \
"v_mfma_f32_16x16x32_bf16 v[28:31], v[204:207], a[180:183], v[28:31]\n" \
"v_cvt_pk_bf16_f32 v138, v148, v149\n" \
"v_cvt_pk_bf16_f32 v139, v150, v151\n" \
"v_exp_f32 v37, v37\n" \
"v_add_u32 v249, s72, v249\n" \
"v_mfma_f32_16x16x32_bf16 v[28:31], v[200:203], a[176:179], v[28:31]\n" \
"s_add_u32 m0, s89, 110592\n" \
"v_cvt_pk_bf16_f32 v140, v152, v153\n" \
"v_cvt_pk_bf16_f32 v141, v154, v155\n" \
"v_exp_f32 v38, v38\n" \
"v_mfma_f32_16x16x32_bf16 a[64:67], a[192:195], v[96:99], a[64:67]\n" \
"s_xor_b32 s73, s73, 0x180\n" \
"v_cvt_pk_bf16_f32 v142, v156, v157\n" \
"v_cvt_pk_bf16_f32 v143, v158, v159\n" \
"v_mfma_f32_16x16x32_bf16 a[64:67], a[196:199], v[100:103], a[64:67]\n" \
"buffer_load_dwordx4 v196, s[44:47], s75 offen lds\n" \
"v_cvt_pk_bf16_f32 v33, v34, v35\n" \
"v_exp_f32 v39, v39\n" \
"v_mfma_f32_16x16x32_bf16 a[96:99], a[192:195], v[112:115], a[96:99]\n" \
"v_cvt_pk_bf16_f32 v144, v160, v161\n" \
"v_cvt_pk_bf16_f32 v145, v162, v163\n" \
"v_mfma_f32_16x16x32_bf16 a[96:99], a[196:199], v[116:119], a[96:99]\n" \
"ds_write_b128 v232, v[136:139] offset:2048\n" \
"v_exp_f32 v48, v48\n" \
"v_cvt_pk_bf16_f32 v34, v36, v37\n" \
"v_accvgpr_read_b32 v250, a48\n" \
"v_mfma_f32_16x16x32_bf16 a[100:103], a[200:203], v[112:115], a[100:103]\n" \
"s_xor_b32 s75, s75, 0x180\n" \
"ds_read_b128 a[192:195], v233 offset:0\n" \
"ds_read_b128 a[196:199], v233 offset:8192\n" \
"v_mfma_f32_16x16x32_bf16 a[100:103], a[204:207], v[116:119], a[100:103]\n" \
"v_cvt_pk_bf16_f32 v146, v164, v165\n" \
"v_cvt_pk_bf16_f32 v147, v166, v167\n" \
"v_exp_f32 v49, v49\n" \
"v_exp_f32 v50, v50\n" \
"v_mfma_f32_16x16x32_bf16 a[68:71], a[200:203], v[96:99], a[68:71]\n" \
"v_cvt_pk_bf16_f32 v148, v168, v169\n" \
"v_cvt_pk_bf16_f32 v149, v170, v171\n" \
"v_mfma_f32_16x16x32_bf16 a[68:71], a[204:207], v[100:103], a[68:71]\n" \
"ds_read_b128 a[200:203], v233 offset:1024\n" \
"ds_read_b128 a[204:207], v233 offset:9216\n" \
"v_cvt_pk_bf16_f32 v35, v38, v39\n" \
"v_exp_f32 v51, v51\n" \
"v_smfmac_f32_16x16x64_bf16 v[224:227], v[244:247], v[96:103], v243\n" \
"v_cvt_pk_bf16_f32 v150, v172, v173\n" \
"v_exp_f32 v52, v52\n" \
"v_cvt_pk_bf16_f32 v48, v48, v49\n" \
"v_accvgpr_read_b32 v240, a49\n" \
"v_mfma_f32_16x16x32_bf16 a[72:75], a[208:211], v[96:99], a[72:75]\n" \
"v_cvt_pk_bf16_f32 v151, v174, v175\n" \
"v_cvt_pk_bf16_f32 v152, v176, v177\n" \
"v_mfma_f32_16x16x32_bf16 a[72:75], a[212:215], v[100:103], a[72:75]\n" \
"ds_write_b128 v232, v[140:143] offset:3072\n" \
"v_exp_f32 v53, v53\n" \
"v_mfma_f32_16x16x32_bf16 a[104:107], a[208:211], v[112:115], a[104:107]\n" \
"v_cvt_pk_bf16_f32 v153, v178, v179\n" \
"v_cvt_pk_bf16_f32 v154, v180, v181\n" \
"v_mfma_f32_16x16x32_bf16 a[104:107], a[212:215], v[116:119], a[104:107]\n" \
"ds_read_b128 a[208:211], v233 offset:2048\n" \
"ds_read_b128 a[212:215], v233 offset:10240\n" \
"v_exp_f32 v54, v54\n" \
"v_cvt_pk_bf16_f32 v49, v50, v51\n" \
"v_exp_f32 v55, v55\n" \
"v_mfma_f32_16x16x32_bf16 a[108:111], a[216:219], v[112:115], a[108:111]\n" \
"v_cvt_pk_bf16_f32 v155, v182, v183\n" \
"v_cvt_pk_bf16_f32 v156, v184, v185\n" \
"v_mfma_f32_16x16x32_bf16 a[108:111], a[220:223], v[116:119], a[108:111]\n" \
"v_cvt_pk_bf16_f32 v157, v186, v187\n" \
"v_cvt_pk_bf16_f32 v158, v188, v189\n" \
"v_exp_f32 v40, v40\n" \
"v_cvt_pk_bf16_f32 v50, v52, v53\n" \
"v_add_f32 v250, v250, v240\n" \
"v_mfma_f32_16x16x32_bf16 a[76:79], a[216:219], v[96:99], a[76:79]\n" \
"v_cvt_pk_bf16_f32 v159, v190, v191\n" \
"v_cvt_pk_bf16_f32 v51, v54, v55\n" \
"v_mfma_f32_16x16x32_bf16 a[76:79], a[220:223], v[100:103], a[76:79]\n" \
"ds_read_b128 a[216:219], v233 offset:3072\n" \
"ds_read_b128 a[220:223], v233 offset:11264\n" \
"v_exp_f32 v41, v41\n" \
"v_exp_f32 v42, v42\n" \
"v_smfmac_f32_16x16x64_bf16 v[228:231], v[244:247], v[112:119], v243\n" \
"ds_write_b128 v232, v[144:147] offset:4096\n" \
"v_mfma_f32_16x16x32_bf16 a[80:83], a[224:227], v[96:99], a[80:83]\n" \
"v_exp_f32 v43, v43\n" \
"v_mfma_f32_16x16x32_bf16 a[80:83], a[228:231], v[100:103], a[80:83]\n" \
"v_exp_f32 v44, v44\n" \
"v_cvt_pk_bf16_f32 v36, v40, v41\n" \
"v_exp_f32 v45, v45\n" \
"v_exp_f32 v46, v46\n" \
"v_accvgpr_read_b32 v251, a52\n" \
"v_mfma_f32_16x16x32_bf16 a[112:115], a[224:227], v[112:115], a[112:115]\n" \
"v_cvt_pk_bf16_f32 v37, v42, v43\n" \
"v_cvt_pk_bf16_f32 v38, v44, v45\n" \
"v_mfma_f32_16x16x32_bf16 a[112:115], a[228:231], v[116:119], a[112:115]\n" \
"ds_read_b128 a[224:227], v233 offset:4096\n" \
"ds_read_b128 a[228:231], v233 offset:12288\n" \
"v_exp_f32 v47, v47\n" \
"v_mfma_f32_16x16x32_bf16 a[116:119], a[232:235], v[112:115], a[116:119]\n" \
"v_exp_f32 v56, v56\n" \
"v_mfma_f32_16x16x32_bf16 a[116:119], a[236:239], v[116:119], a[116:119]\n" \
"v_exp_f32 v57, v57\n" \
"v_exp_f32 v58, v58\n" \
"v_cvt_pk_bf16_f32 v39, v46, v47\n" \
"v_exp_f32 v59, v59\n" \
"v_accvgpr_read_b32 v240, a53\n" \
"v_mfma_f32_16x16x32_bf16 a[84:87], a[232:235], v[96:99], a[84:87]\n" \
"ds_write_b128 v232, v[148:151] offset:5120\n" \
"v_exp_f32 v60, v60\n" \
"v_mfma_f32_16x16x32_bf16 a[84:87], a[236:239], v[100:103], a[84:87]\n" \
"ds_read_b128 a[232:235], v233 offset:5120\n" \
"ds_read_b128 a[236:239], v233 offset:13312\n" \
"v_cvt_pk_bf16_f32 v52, v56, v57\n" \
"v_mfma_f32_16x16x32_bf16 a[88:91], a[32:35], v[96:99], a[88:91]\n" \
"v_exp_f32 v61, v61\n" \
"v_mfma_f32_16x16x32_bf16 a[88:91], a[36:39], v[100:103], a[88:91]\n" \
"ds_read_b128 a[240:243], v233 offset:6144\n" \
"ds_read_b128 a[244:247], v233 offset:14336\n" \
"v_exp_f32 v62, v62\n" \
"v_cvt_pk_bf16_f32 v53, v58, v59\n" \
"v_mfma_f32_16x16x32_bf16 a[120:123], a[32:35], v[112:115], a[120:123]\n" \
"ds_read_b128 v[0:3], v233 offset:20480\n" \
"ds_read_b128 v[4:7], v233 offset:28672\n" \
"v_mfma_f32_16x16x32_bf16 a[120:123], a[36:39], v[116:119], a[120:123]\n" \
"ds_read_b128 v[8:11], v233 offset:21504\n" \
"ds_read_b128 v[12:15], v233 offset:29696\n" \
"v_exp_f32 v63, v63\n" \
"v_cvt_pk_bf16_f32 v54, v60, v61\n" \
"v_add_f32 v251, v251, v240\n" \
"v_mfma_f32_16x16x32_bf16 a[124:127], a[40:43], v[112:115], a[124:127]\n" \
"v_cvt_pk_bf16_f32 v55, v62, v63\n" \
"v_mfma_f32_16x16x32_bf16 a[124:127], a[44:47], v[116:119], a[124:127]\n" \
"ds_write_b128 v232, v[152:155] offset:6144\n" \
"ds_write_b64 v249, v[250:251]\n" \
"v_mfma_f32_16x16x32_bf16 a[92:95], a[40:43], v[96:99], a[92:95]\n" \
"v_exp_f32 v16, v16\n" \
"v_mfma_f32_16x16x32_bf16 a[92:95], a[44:47], v[100:103], a[92:95]\n" \
"s_waitcnt lgkmcnt(4)\n" \
"ds_read_b128 v[96:99], v233 offset:22528\n" \
"ds_read_b128 v[100:103], v233 offset:30720\n" \
"v_exp_f32 v17, v17\n" \
"v_xor_b32 v249, 0x200, v249\n" \
"v_accvgpr_read_b32 v200, a59\n" \
"v_mfma_f32_16x16x32_bf16 a[64:67], a[192:195], v[32:35], a[64:67]\n" \
"ds_read_b128 v[104:107], v233 offset:23552\n" \
"ds_read_b128 v[108:111], v233 offset:31744\n" \
"v_mfma_f32_16x16x32_bf16 a[64:67], a[196:199], v[36:39], a[64:67]\n" \
"v_exp_f32 v18, v18\n" \
"v_exp_f32 v19, v19\n" \
"v_accvgpr_read_b32 v202, a60\n" \
"v_accvgpr_read_b32 v204, a61\n" \
"v_xor_b32 v201, 64, v200\n" \
"v_mfma_f32_16x16x32_bf16 a[96:99], a[192:195], v[48:51], a[96:99]\n" \
"v_exp_f32 v20, v20\n" \
"v_mfma_f32_16x16x32_bf16 a[96:99], a[196:199], v[52:55], a[96:99]\n" \
"v_cvt_pk_bf16_f32 v16, v16, v17\n" \
"v_cvt_pk_bf16_f32 v17, v18, v19\n" \
"v_xor_b32 v203, 64, v202\n" \
"v_xor_b32 v222, 32, v204\n" \
"v_add_u32 v205, 0x10000, v200\n" \
"v_mfma_f32_16x16x32_bf16 a[100:103], a[200:203], v[48:51], a[100:103]\n" \
"v_exp_f32 v21, v21\n" \
"v_mfma_f32_16x16x32_bf16 a[100:103], a[204:207], v[52:55], a[100:103]\n" \
"ds_write_b128 v232, v[156:159] offset:7168\n" \
"v_exp_f32 v22, v22\n" \
"v_add_u32 v207, 0x10000, v202\n" \
"v_add_u32 v194, 139264, v204\n" \
"v_add_u32 v206, 0x10000, v201\n" \
"v_mfma_f32_16x16x32_bf16 a[68:71], a[200:203], v[32:35], a[68:71]\n" \
"v_exp_f32 v23, v23\n" \
"v_mfma_f32_16x16x32_bf16 a[68:71], a[204:207], v[36:39], a[68:71]\n" \
"s_mul_i32 s76, s80, 393216\n" \
"s_add_u32 s76, s76, s78\n" \
"v_cvt_pk_bf16_f32 v18, v20, v21\n" \
"v_exp_f32 v24, v24\n" \
"v_add_u32 v208, 0x10000, v203\n" \
"v_add_u32 v195, 139264, v222\n" \
"v_smfmac_f32_16x16x64_bf16 v[224:227], v[244:247], v[32:39], v243\n" \
"s_waitcnt vmcnt(0) lgkmcnt(0)\n" \
"s_barrier\n" \
"v_mfma_f32_16x16x32_bf16 a[72:75], a[208:211], v[32:35], a[72:75]\n" \
"s_add_u32 m0, s91, 86016\n" \
"ds_read_b64_tr_b16 a[32:33], v194 offset:1024\n" \
"ds_read_b64_tr_b16 a[34:35], v194 offset:4096\n" \
"v_mfma_f32_16x16x32_bf16 a[72:75], a[212:215], v[36:39], a[72:75]\n" \
"buffer_load_dwordx4 v197, s[48:51], s76 offen offset:0 lds\n" \
"v_exp_f32 v25, v25\n" \
"v_mfma_f32_16x16x32_bf16 a[104:107], a[208:211], v[48:51], a[104:107]\n" \
"s_and_b32 s15, %1, 7\n" \
"ds_read_b64_tr_b16 a[36:37], v194 offset:13312\n" \
"ds_read_b64_tr_b16 a[38:39], v194 offset:16384\n" \
"v_mfma_f32_16x16x32_bf16 a[104:107], a[212:215], v[52:55], a[104:107]\n" \
"s_add_u32 s77, s76, 24576\n" \
"ds_read_b64_tr_b16 a[40:41], v194 offset:0\n" \
"ds_read_b64_tr_b16 a[42:43], v194 offset:3072\n" \
"v_mfma_f32_16x16x32_bf16 a[108:111], a[216:219], v[48:51], a[108:111]\n" \
"buffer_load_dwordx4 v198, s[48:51], s76 offen offset:1024 lds\n" \
"v_mfma_f32_16x16x32_bf16 a[108:111], a[220:223], v[52:55], a[108:111]\n" \
"s_lshl3_add_u32 s14, s14, s15\n" \
"ds_read_b64_tr_b16 a[44:45], v194 offset:12288\n" \
"ds_read_b64_tr_b16 a[46:47], v194 offset:15360\n" \
"v_exp_f32 v26, v26\n" \
"v_cvt_pk_bf16_f32 v19, v22, v23\n" \
"v_mfma_f32_16x16x32_bf16 a[76:79], a[216:219], v[32:35], a[76:79]\n" \
"s_xor_b32 s80, s80, 0x8\n" \
"ds_read_b64_tr_b16 a[48:49], v195 offset:0\n" \
"ds_read_b64_tr_b16 a[50:51], v195 offset:3072\n" \
"v_mfma_f32_16x16x32_bf16 a[76:79], a[220:223], v[36:39], a[76:79]\n" \
"buffer_load_dwordx4 v199, s[48:51], s76 offen offset:2048 lds\n" \
"v_smfmac_f32_16x16x64_bf16 v[228:231], v[244:247], v[48:55], v243\n" \
"ds_read_b64_tr_b16 a[52:53], v195 offset:12288\n" \
"ds_read_b64_tr_b16 a[54:55], v195 offset:15360\n" \
"v_exp_f32 v27, v27\n" \
"v_mfma_f32_16x16x32_bf16 a[80:83], a[224:227], v[32:35], a[80:83]\n" \
"s_add_u32 m0, m0, 3072\n" \
"ds_read_b128 a[0:3], v207 offset:45056\n" \
"ds_read_b128 a[4:7], v208 offset:45056\n" \
"v_mfma_f32_16x16x32_bf16 a[80:83], a[228:231], v[36:39], a[80:83]\n" \
"buffer_load_dwordx4 v197, s[48:51], s77 offen offset:0 lds\n" \
"v_mfma_f32_16x16x32_bf16 a[112:115], a[224:227], v[48:51], a[112:115]\n" \
"ds_read_b128 a[8:11], v205 offset:45056\n" \
"ds_read_b128 a[12:15], v206 offset:45056\n" \
"v_mfma_f32_16x16x32_bf16 a[112:115], a[228:231], v[52:55], a[112:115]\n" \
"ds_read_b128 a[16:19], v205 offset:49152\n" \
"ds_read_b128 a[20:23], v206 offset:49152\n" \
"v_exp_f32 v28, v28\n" \
"v_cvt_pk_bf16_f32 v20, v24, v25\n" \
"v_mfma_f32_16x16x32_bf16 a[116:119], a[232:235], v[48:51], a[116:119]\n" \
"buffer_load_dwordx4 v198, s[48:51], s77 offen offset:1024 lds\n" \
"v_mfma_f32_16x16x32_bf16 a[116:119], a[236:239], v[52:55], a[116:119]\n" \
"ds_read_b128 a[24:27], v205 offset:53248\n" \
"ds_read_b128 a[28:31], v206 offset:53248\n" \
"v_mfma_f32_16x16x32_bf16 a[84:87], a[232:235], v[32:35], a[84:87]\n" \
"ds_read_b64 v[250:251], v249\n" \
"ds_read_b128 v[128:131], v248 offset:0\n" \
"v_mfma_f32_16x16x32_bf16 a[84:87], a[236:239], v[36:39], a[84:87]\n" \
"buffer_load_dwordx4 v199, s[48:51], s77 offen offset:2048 lds\n" \
"v_exp_f32 v29, v29\n" \
"v_mfma_f32_16x16x32_bf16 a[88:91], a[240:243], v[32:35], a[88:91]\n" \
"ds_read_b128 v[132:135], v248 offset:1024\n" \
"ds_read_b128 v[136:139], v248 offset:2048\n" \
"v_mfma_f32_16x16x32_bf16 a[88:91], a[244:247], v[36:39], a[88:91]\n" \
"s_add_u32 m0, s79, 65536\n" \
"ds_read_b128 v[140:143], v248 offset:3072\n" \
"ds_read_b128 v[144:147], v248 offset:4096\n" \
"v_exp_f32 v30, v30\n" \
"v_mfma_f32_16x16x32_bf16 a[120:123], a[240:243], v[48:51], a[120:123]\n" \
"buffer_load_dwordx4 v192, s[44:47], s73 offen offset:0 lds\n" \
"v_mfma_f32_16x16x32_bf16 a[120:123], a[244:247], v[52:55], a[120:123]\n" \
"ds_read_b128 v[148:151], v248 offset:5120\n" \
"ds_read_b128 v[152:155], v248 offset:6144\n" \
"v_cvt_pk_bf16_f32 v21, v26, v27\n" \
"v_mfma_f32_16x16x32_bf16 a[124:127], a[248:251], v[48:51], a[124:127]\n" \
"ds_read_b128 v[156:159], v248 offset:7168\n" \
"v_cvt_pk_bf16_f32 v22, v28, v29\n" \
"v_mfma_f32_16x16x32_bf16 a[124:127], a[252:255], v[52:55], a[124:127]\n" \
"buffer_load_dwordx4 v193, s[44:47], s73 offen offset:1024 lds\n" \
"v_exp_f32 v31, v31\n" \
"v_mfma_f32_16x16x32_bf16 a[92:95], a[248:251], v[32:35], a[92:95]\n" \
"v_cvt_pk_bf16_f32 v23, v30, v31\n" \
"v_mov_b32 v218, s60\n" \
"v_mfma_f32_16x16x32_bf16 a[92:95], a[252:255], v[36:39], a[92:95]\n" \
"v_mov_b32 v219, s14\n" \
"v_lshlrev_b32 v220, 28, s61\n" \
"v_mfma_f32_16x16x32_bf16 a[96:99], v[64:67], v[16:19], a[96:99]\n" \
"buffer_load_dwordx4 v192, s[44:47], s73 offen offset:2048 lds\n" \
"v_mfma_f32_16x16x32_bf16 a[96:99], v[68:71], v[20:23], a[96:99]\n" \
"s_lshl3_add_u32 s15, s15, 65593856\n" \
"v_mov_b32 v170, 0x3f80\n" \
"v_mov_b32 v160, 0\n" \
"v_mfma_f32_16x16x32_bf16 a[100:103], v[72:75], v[16:19], a[100:103]\n" \
"s_lshr_b32 s14, %1, 3\n" \
"s_cmp_eq_u32 s14, 3\n" \
"s_cselect_b32 s15, s15, 0x20000000\n" \
"v_mfma_f32_16x16x32_bf16 a[100:103], v[76:79], v[20:23], a[100:103]\n" \
"buffer_load_dwordx4 v193, s[44:47], s73 offen offset:3072 lds\n" \
"v_smfmac_f32_16x16x64_bf16 v[228:231], v[244:247], v[16:23], v243\n" \
"s_add_u32 m0, s89, 65536\n" \
"buffer_store_dwordx2 v[218:219], v220, s[56:59], s15 offen sc1\n" \
"v_mfma_f32_16x16x32_bf16 a[104:107], v[80:83], v[16:19], a[104:107]\n" \
"s_xor_b32 s73, s73, 0x400\n" \
"v_mov_b32 v161, 0\n" \
"v_mov_b32 v164, 0\n" \
"v_mfma_f32_16x16x32_bf16 a[104:107], v[84:87], v[20:23], a[104:107]\n" \
"buffer_load_dwordx4 v196, s[44:47], s75 offen lds\n" \
"v_mfma_f32_16x16x32_bf16 a[108:111], v[88:91], v[16:19], a[108:111]\n" \
"v_mov_b32 v165, 0\n" \
"v_lshrrev_b32 v168, 8, v234\n" \
"v_mfma_f32_16x16x32_bf16 a[108:111], v[92:95], v[20:23], a[108:111]\n" \
"v_bfe_u32 v169, v234, 6, 2\n" \
"v_cmp_eq_u32 s[16:17], v168, v169\n" \
"v_mfma_f32_16x16x32_bf16 a[112:115], v[0:3], v[16:19], a[112:115]\n" \
"s_xor_b32 s75, s75, 0x400\n" \
"v_and_b32 v168, 16, v234\n" \
"v_lshlrev_b32 v168, v168, v170\n" \
"v_mfma_f32_16x16x32_bf16 a[112:115], v[4:7], v[20:23], a[112:115]\n" \
"v_cndmask_b32 v168, 0, v168, s[16:17]\n" \
"v_bfe_i32 v169, v234, 5, 1\n" \
"v_mfma_f32_16x16x32_bf16 a[116:119], v[8:11], v[16:19], a[116:119]\n" \
"v_and_b32 v163, v168, v169\n" \
"v_xor_b32 v162, v168, v163\n" \
"v_mfma_f32_16x16x32_bf16 a[116:119], v[12:15], v[20:23], a[116:119]\n" \
"Lpg7367_%=:\n" \
"s_cbranch_execz Lcb0_%=\n" \
"v_mfma_f32_16x16x32_bf16 a[120:123], v[96:99], v[16:19], a[120:123]\n" \
"v_mfma_f32_16x16x32_bf16 a[120:123], v[100:103], v[20:23], a[120:123]\n" \
"v_mfma_f32_16x16x32_bf16 a[124:127], v[104:107], v[16:19], a[124:127]\n" \
"v_mfma_f32_16x16x32_bf16 a[124:127], v[108:111], v[20:23], a[124:127]\n" \
"s_waitcnt lgkmcnt(0)\n" \
"s_barrier\n" \
"v_mfma_f32_16x16x32_bf16 v[32:35], a[32:35], a[0:3], 0\n" \
"s_add_u32 m0, s79, 0\n" \
"ds_read_b128 a[192:195], v205 offset:57344\n" \
"v_mfma_f32_16x16x32_bf16 v[32:35], a[36:39], a[4:7], v[32:35]\n" \
"buffer_load_dwordx4 v192, s[44:47], s73 offen offset:0 lds\n" \
"v_mfma_f32_16x16x32_bf16 v[40:43], a[32:35], a[8:11], 0\n" \
"ds_read_b128 a[196:199], v206 offset:57344\n" \
"v_add_u32 v209, 86016, v204\n" \
"v_mfma_f32_16x16x32_bf16 v[40:43], a[36:39], a[12:15], v[40:43]\n" \
"buffer_load_dwordx4 v193, s[44:47], s73 offen offset:1024 lds\n" \
"v_mfma_f32_16x16x32_bf16 a[64:67], v[162:165], v[128:131], a[64:67]\n" \
"v_mfma_f32_16x16x32_bf16 a[68:71], v[160:163], v[128:131], a[68:71]\n" \
"v_mfma_f32_16x16x32_bf16 a[72:75], v[162:165], v[132:135], a[72:75]\n" \
"v_mfma_f32_16x16x32_bf16 a[76:79], v[160:163], v[132:135], a[76:79]\n" \
"v_add_f32 v248, v224, v225\n" \
"v_add_f32 v249, v228, v229\n" \
"v_mfma_f32_16x16x32_bf16 v[0:3], a[40:43], a[8:11], 0\n" \
"ds_read_b64_tr_b16 a[200:201], v195 offset:1024\n" \
"ds_read_b64_tr_b16 a[202:203], v195 offset:4096\n" \
"v_mfma_f32_16x16x32_bf16 v[0:3], a[44:47], a[12:15], v[0:3]\n" \
"buffer_load_dwordx4 v192, s[44:47], s73 offen offset:2048 lds\n" \
"v_mfma_f32_16x16x32_bf16 v[4:7], a[48:51], a[8:11], 0\n" \
"ds_read_b64_tr_b16 a[204:205], v195 offset:13312\n" \
"ds_read_b64_tr_b16 a[206:207], v195 offset:16384\n" \
"v_mfma_f32_16x16x32_bf16 v[4:7], a[52:55], a[12:15], v[4:7]\n" \
"buffer_load_dwordx4 v193, s[44:47], s73 offen offset:3072 lds\n" \
"v_mfma_f32_16x16x32_bf16 v[12:15], a[48:51], a[16:19], 0\n" \
"s_add_u32 m0, s89, 0\n" \
"s_xor_b32 s73, s73, 0x500\n" \
"ds_read_b64_tr_b16 a[208:209], v194 offset:2048\n" \
"ds_read_b64_tr_b16 a[210:211], v194 offset:5120\n" \
"v_add_f32 v248, v248, v250\n" \
"v_mfma_f32_16x16x32_bf16 v[12:15], a[52:55], a[20:23], v[12:15]\n" \
"buffer_load_dwordx4 v196, s[44:47], s75 offen lds\n" \
"v_mfma_f32_16x16x32_bf16 v[8:11], a[40:43], a[16:19], 0\n" \
"s_xor_b32 s75, s75, 0x500\n" \
"s_mul_i32 s76, s80, 393216\n" \
"s_add_u32 s76, s76, s78\n" \
"s_add_u32 m0, s91, 20480\n" \
"ds_read_b64_tr_b16 a[212:213], v194 offset:14336\n" \
"ds_read_b64_tr_b16 a[214:215], v194 offset:17408\n" \
"v_mfma_f32_16x16x32_bf16 v[8:11], a[44:47], a[20:23], v[8:11]\n" \
"buffer_load_dwordx4 v197, s[48:51], s76 offen offset:0 lds\n" \
"v_mfma_f32_16x16x32_bf16 v[48:51], a[32:35], a[16:19], 0\n" \
"s_add_u32 s77, s76, 24576\n" \
"ds_read_b64_tr_b16 a[216:217], v195 offset:2048\n" \
"v_add_u32 v210, 86016, v222\n" \
"v_mfma_f32_16x16x32_bf16 v[48:51], a[36:39], a[20:23], v[48:51]\n" \
"buffer_load_dwordx4 v198, s[48:51], s76 offen offset:1024 lds\n" \
"v_mfma_f32_16x16x32_bf16 v[56:59], a[32:35], a[24:27], 0\n" \
"s_xor_b32 s80, s80, 0xa\n" \
"ds_read_b64_tr_b16 a[218:219], v195 offset:5120\n" \
"v_add_f32 v249, v249, v251\n" \
"v_mfma_f32_16x16x32_bf16 v[56:59], a[36:39], a[28:31], v[56:59]\n" \
"buffer_load_dwordx4 v199, s[48:51], s76 offen offset:2048 lds\n" \
"v_mfma_f32_16x16x32_bf16 v[16:19], a[40:43], a[24:27], 0\n" \
"s_add_u32 m0, m0, 3072\n" \
"ds_read_b64_tr_b16 a[220:221], v195 offset:14336\n" \
"v_mfma_f32_16x16x32_bf16 v[16:19], a[44:47], a[28:31], v[16:19]\n" \
"buffer_load_dwordx4 v197, s[48:51], s77 offen offset:0 lds\n" \
"v_mfma_f32_16x16x32_bf16 v[20:23], a[48:51], a[24:27], 0\n" \
"s_waitcnt lgkmcnt(2)\n" \
"ds_read_b64_tr_b16 a[222:223], v195 offset:17408\n" \
"v_mfma_f32_16x16x32_bf16 v[20:23], a[52:55], a[28:31], v[20:23]\n" \
"buffer_load_dwordx4 v198, s[48:51], s77 offen offset:1024 lds\n" \
"v_mfma_f32_16x16x32_bf16 v[28:31], a[48:51], a[192:195], 0\n" \
"v_rcp_f32 v232, v248\n" \
"v_mfma_f32_16x16x32_bf16 v[28:31], a[52:55], a[196:199], v[28:31]\n" \
"buffer_load_dwordx4 v199, s[48:51], s77 offen offset:2048 lds\n" \
"v_rcp_f32 v233, v249\n" \
"v_mfma_f32_16x16x32_bf16 v[24:27], a[40:43], a[192:195], 0\n" \
"v_add_u32 v240, 0x1000, v236\n" \
"v_add_f32 v224, v248, v249\n" \
"v_mfma_f32_16x16x32_bf16 v[24:27], a[44:47], a[196:199], v[24:27]\n" \
"v_mul_f32 v224, 0x43800000, v224\n" \
"v_add_f32 v248, v232, v233\n" \
"v_mfma_f32_16x16x32_bf16 v[64:67], a[32:35], a[192:195], 0\n" \
"v_fmac_f32 v224, 0x5f800000, v248\n" \
"v_mov_b32 v225, 0\n" \
"v_mfma_f32_16x16x32_bf16 v[64:67], a[36:39], a[196:199], v[64:67]\n" \
"s_waitcnt vmcnt(11) lgkmcnt(0)\n" \
"s_barrier\n" \
"v_mfma_f32_16x16x32_bf16 v[68:71], a[200:203], a[192:195], 0\n" \
"ds_read_b64_tr_b16 a[32:33], v209 offset:1024\n" \
"ds_read_b64_tr_b16 a[34:35], v209 offset:4096\n" \
"v_mfma_f32_16x16x32_bf16 v[68:71], a[204:207], a[196:199], v[68:71]\n" \
"ds_read_b64_tr_b16 a[36:37], v209 offset:13312\n" \
"ds_read_b64_tr_b16 a[38:39], v209 offset:16384\n" \
"v_mfma_f32_16x16x32_bf16 v[60:63], a[200:203], a[24:27], 0\n" \
"s_add_u32 m0, s79, 110592\n" \
"ds_read_b64_tr_b16 a[40:41], v209 offset:0\n" \
"v_mov_b32 v226, 0\n" \
"v_mfma_f32_16x16x32_bf16 v[60:63], a[204:207], a[28:31], v[60:63]\n" \
"buffer_load_dwordx4 v192, s[44:47], s73 offen offset:0 lds\n" \
"v_mov_b32 v227, 0\n" \
"v_mfma_f32_16x16x32_bf16 v[52:55], a[200:203], a[16:19], 0\n" \
"ds_read_b64_tr_b16 a[42:43], v209 offset:3072\n" \
"v_accvgpr_read_b32 v112, a64\n" \
"v_mfma_f32_16x16x32_bf16 v[52:55], a[204:207], a[20:23], v[52:55]\n" \
"ds_read_b64_tr_b16 a[44:45], v209 offset:12288\n" \
"v_accvgpr_read_b32 v113, a65\n" \
"v_mfma_f32_16x16x32_bf16 v[44:47], a[200:203], a[8:11], 0\n" \
"ds_read_b64_tr_b16 a[46:47], v209 offset:15360\n" \
"v_accvgpr_read_b32 v114, a66\n" \
"v_mfma_f32_16x16x32_bf16 v[44:47], a[204:207], a[12:15], v[44:47]\n" \
"buffer_load_dwordx4 v193, s[44:47], s73 offen offset:1024 lds\n" \
"v_mfma_f32_16x16x32_bf16 v[72:75], a[0:3], a[208:211], 0\n" \
"ds_read_b64_tr_b16 a[48:49], v210 offset:0\n" \
"v_accvgpr_read_b32 v115, a67\n" \
"v_mfma_f32_16x16x32_bf16 v[72:75], a[4:7], a[212:215], v[72:75]\n" \
"ds_read_b128 a[0:3], v207 offset:0\n" \
"v_accvgpr_read_b32 v116, a68\n" \
"v_accvgpr_read_b32 v117, a69\n" \
"v_mfma_f32_16x16x32_bf16 v[80:83], a[8:11], a[208:211], 0\n" \
"ds_read_b128 a[4:7], v208 offset:0\n" \
"v_accvgpr_read_b32 v118, a70\n" \
"v_mfma_f32_16x16x32_bf16 v[80:83], a[12:15], a[212:215], v[80:83]\n" \
"buffer_load_dwordx4 v192, s[44:47], s73 offen offset:2048 lds\n" \
"v_mfma_f32_16x16x32_bf16 v[84:87], a[8:11], a[216:219], 0\n" \
"ds_read_b64_tr_b16 a[50:51], v210 offset:3072\n" \
"v_accvgpr_read_b32 v119, a71\n" \
"v_mfma_f32_16x16x32_bf16 v[84:87], a[12:15], a[220:223], v[84:87]\n" \
"ds_read_b128 a[8:11], v205 offset:0\n" \
"v_mfma_f32_16x16x32_bf16 a[80:83], v[162:165], v[136:139], a[80:83]\n" \
"v_mul_f32 v112, v112, v232\n" \
"v_mul_f32 v113, v113, v232\n" \
"v_mfma_f32_16x16x32_bf16 v[92:95], a[16:19], a[216:219], 0\n" \
"ds_read_b128 a[12:15], v206 offset:0\n" \
"v_mul_f32 v114, v114, v232\n" \
"v_mfma_f32_16x16x32_bf16 v[92:95], a[20:23], a[220:223], v[92:95]\n" \
"buffer_load_dwordx4 v193, s[44:47], s73 offen offset:3072 lds\n" \
"v_mul_f32 v115, v115, v232\n" \
"v_mfma_f32_16x16x32_bf16 a[84:87], v[160:163], v[136:139], a[84:87]\n" \
"v_mul_f32 v116, v116, v232\n" \
"v_mul_f32 v117, v117, v232\n" \
"v_mfma_f32_16x16x32_bf16 v[88:91], a[16:19], a[208:211], 0\n" \
"ds_read_b64_tr_b16 a[52:53], v210 offset:12288\n" \
"v_mul_f32 v118, v118, v232\n" \
"v_mfma_f32_16x16x32_bf16 v[88:91], a[20:23], a[212:215], v[88:91]\n" \
"s_add_u32 m0, s89, 110592\n" \
"ds_read_b128 a[16:19], v205 offset:4096\n" \
"v_mul_f32 v119, v119, v232\n" \
"v_mfma_f32_16x16x32_bf16 v[96:99], a[24:27], a[208:211], 0\n" \
"s_xor_b32 s73, s73, 0x180\n" \
"ds_read_b128 a[20:23], v206 offset:4096\n" \
"v_cvt_pk_bf16_f32 v120, v112, v113\n" \
"v_mfma_f32_16x16x32_bf16 v[96:99], a[28:31], a[212:215], v[96:99]\n" \
"buffer_load_dwordx4 v196, s[44:47], s75 offen lds\n" \
"v_mfma_f32_16x16x32_bf16 v[100:103], a[24:27], a[216:219], 0\n" \
"s_xor_b32 s75, s75, 0x180\n" \
"s_mul_i32 s76, s80, 393216\n" \
"ds_read_b64_tr_b16 a[54:55], v210 offset:15360\n" \
"v_mfma_f32_16x16x32_bf16 v[100:103], a[28:31], a[220:223], v[100:103]\n" \
"s_add_u32 s76, s76, s78\n" \
"ds_read_b128 a[24:27], v205 offset:8192\n" \
"v_cvt_pk_bf16_f32 v121, v114, v115\n" \
"v_mfma_f32_16x16x32_bf16 v[108:111], a[192:195], a[216:219], 0\n" \
"s_add_u32 m0, s91, 139264\n" \
"ds_read_b128 a[28:31], v206 offset:8192\n" \
"v_cvt_pk_bf16_f32 v122, v116, v117\n" \
"v_mfma_f32_16x16x32_bf16 v[108:111], a[196:199], a[220:223], v[108:111]\n" \
"buffer_load_dwordx4 v197, s[48:51], s76 offen offset:0 lds\n" \
"v_cvt_pk_bf16_f32 v123, v118, v119\n" \
"v_mfma_f32_16x16x32_bf16 v[104:107], a[192:195], a[208:211], 0\n" \
"s_waitcnt lgkmcnt(3)\n" \
"v_mfma_f32_16x16x32_bf16 v[104:107], a[196:199], a[212:215], v[104:107]\n" \
"buffer_store_dwordx4 v[120:123], v236, s[56:59], s88 offen offset:0 sc1\n" \
"v_mfma_f32_16x16x32_bf16 v[32:35], a[32:35], a[0:3], v[32:35]\n" \
"s_add_u32 s77, s76, 24576\n" \
"ds_read_b128 a[192:195], v205 offset:12288\n" \
"v_mfma_f32_16x16x32_bf16 v[32:35], a[36:39], a[4:7], v[32:35]\n" \
"buffer_load_dwordx4 v198, s[48:51], s76 offen offset:1024 lds\n" \
"v_mfma_f32_16x16x32_bf16 v[224:227], v[244:247], v[120:123], v[224:227]\n" \
"v_accvgpr_read_b32 v112, a72\n" \
"v_accvgpr_read_b32 v113, a73\n" \
"v_mfma_f32_16x16x32_bf16 v[40:43], a[32:35], a[8:11], v[40:43]\n" \
"ds_read_b128 a[196:199], v206 offset:12288\n" \
"v_accvgpr_read_b32 v114, a74\n" \
"v_mfma_f32_16x16x32_bf16 v[40:43], a[36:39], a[12:15], v[40:43]\n" \
"s_waitcnt lgkmcnt(2)\n" \
"ds_read_b64_tr_b16 a[200:201], v210 offset:1024\n" \
"v_accvgpr_read_b32 v115, a75\n" \
"v_accvgpr_read_b32 v116, a76\n" \
"v_mfma_f32_16x16x32_bf16 v[0:3], a[40:43], a[8:11], v[0:3]\n" \
"s_xor_b32 s80, s80, 0x3\n" \
"ds_read_b64_tr_b16 a[202:203], v210 offset:4096\n" \
"v_accvgpr_read_b32 v117, a77\n" \
"v_mfma_f32_16x16x32_bf16 v[0:3], a[44:47], a[12:15], v[0:3]\n" \
"buffer_load_dwordx4 v199, s[48:51], s76 offen offset:2048 lds\n" \
"v_mfma_f32_16x16x32_bf16 v[4:7], a[48:51], a[8:11], v[4:7]\n" \
"ds_read_b64_tr_b16 a[204:205], v210 offset:13312\n" \
"v_accvgpr_read_b32 v118, a78\n" \
"v_mfma_f32_16x16x32_bf16 v[4:7], a[52:55], a[12:15], v[4:7]\n" \
"ds_read_b64_tr_b16 a[206:207], v210 offset:16384\n" \
"v_accvgpr_read_b32 v119, a79\n" \
"v_mfma_f32_16x16x32_bf16 a[88:91], v[162:165], v[140:143], a[88:91]\n" \
"v_mul_f32 v112, v112, v232\n" \
"v_mul_f32 v113, v113, v232\n" \
"v_mfma_f32_16x16x32_bf16 v[12:15], a[48:51], a[16:19], v[12:15]\n" \
"s_add_u32 m0, m0, 3072\n" \
"ds_read_b64_tr_b16 a[208:209], v209 offset:2048\n" \
"v_mul_f32 v114, v114, v232\n" \
"v_mfma_f32_16x16x32_bf16 v[12:15], a[52:55], a[20:23], v[12:15]\n" \
"buffer_load_dwordx4 v197, s[48:51], s77 offen offset:0 lds\n" \
"v_mul_f32 v115, v115, v232\n" \
"v_mfma_f32_16x16x32_bf16 a[92:95], v[160:163], v[140:143], a[92:95]\n" \
"v_mul_f32 v116, v116, v232\n" \
"v_mul_f32 v117, v117, v232\n" \
"v_mfma_f32_16x16x32_bf16 v[8:11], a[40:43], a[16:19], v[8:11]\n" \
"ds_read_b64_tr_b16 a[210:211], v209 offset:5120\n" \
"v_mul_f32 v118, v118, v232\n" \
"v_mfma_f32_16x16x32_bf16 v[8:11], a[44:47], a[20:23], v[8:11]\n" \
"ds_read_b64_tr_b16 a[212:213], v209 offset:14336\n" \
"v_mul_f32 v119, v119, v232\n" \
"v_mfma_f32_16x16x32_bf16 v[48:51], a[32:35], a[16:19], v[48:51]\n" \
"ds_read_b64_tr_b16 a[214:215], v209 offset:17408\n" \
"v_cvt_pk_bf16_f32 v124, v112, v113\n" \
"v_mfma_f32_16x16x32_bf16 v[48:51], a[36:39], a[20:23], v[48:51]\n" \
"buffer_load_dwordx4 v198, s[48:51], s77 offen offset:1024 lds\n" \
"v_mfma_f32_16x16x32_bf16 v[56:59], a[32:35], a[24:27], v[56:59]\n" \
"ds_read_b64_tr_b16 a[216:217], v210 offset:2048\n" \
"v_cvt_pk_bf16_f32 v125, v114, v115\n" \
"v_mfma_f32_16x16x32_bf16 v[56:59], a[36:39], a[28:31], v[56:59]\n" \
"ds_read_b64_tr_b16 a[218:219], v210 offset:5120\n" \
"v_cvt_pk_bf16_f32 v126, v116, v117\n" \
"v_mfma_f32_16x16x32_bf16 v[16:19], a[40:43], a[24:27], v[16:19]\n" \
"ds_read_b64_tr_b16 a[220:221], v210 offset:14336\n" \
"v_cvt_pk_bf16_f32 v127, v118, v119\n" \
"v_mfma_f32_16x16x32_bf16 v[16:19], a[44:47], a[28:31], v[16:19]\n" \
"buffer_load_dwordx4 v199, s[48:51], s77 offen offset:2048 lds\n" \
"v_mfma_f32_16x16x32_bf16 v[20:23], a[48:51], a[24:27], v[20:23]\n" \
"s_waitcnt lgkmcnt(3)\n" \
"ds_read_b64_tr_b16 a[222:223], v210 offset:17408\n" \
"v_mfma_f32_16x16x32_bf16 v[20:23], a[52:55], a[28:31], v[20:23]\n" \
"buffer_store_dwordx4 v[124:127], v236, s[56:59], s88 offen offset:2048 sc1\n" \
"v_mfma_f32_16x16x32_bf16 v[28:31], a[48:51], a[192:195], v[28:31]\n" \
"v_mfma_f32_16x16x32_bf16 v[28:31], a[52:55], a[196:199], v[28:31]\n" \
"v_mfma_f32_16x16x32_bf16 v[224:227], v[244:247], v[124:127], v[224:227]\n" \
"v_accvgpr_read_b32 v112, a80\n" \
"v_accvgpr_read_b32 v113, a81\n" \
"v_accvgpr_read_b32 v114, a82\n" \
"v_mfma_f32_16x16x32_bf16 v[24:27], a[40:43], a[192:195], v[24:27]\n" \
"v_accvgpr_read_b32 v115, a83\n" \
"v_accvgpr_read_b32 v116, a84\n" \
"v_mfma_f32_16x16x32_bf16 v[24:27], a[44:47], a[196:199], v[24:27]\n" \
"v_accvgpr_read_b32 v117, a85\n" \
"v_accvgpr_read_b32 v118, a86\n" \
"v_accvgpr_read_b32 v119, a87\n" \
"v_mfma_f32_16x16x32_bf16 a[96:99], v[162:165], v[144:147], a[96:99]\n" \
"v_mul_f32 v112, v112, v232\n" \
"v_mul_f32 v113, v113, v232\n" \
"v_mfma_f32_16x16x32_bf16 v[64:67], a[32:35], a[192:195], v[64:67]\n" \
"v_mul_f32 v114, v114, v232\n" \
"v_mul_f32 v115, v115, v232\n" \
"v_mfma_f32_16x16x32_bf16 v[64:67], a[36:39], a[196:199], v[64:67]\n" \
"v_mfma_f32_16x16x32_bf16 a[100:103], v[160:163], v[144:147], a[100:103]\n" \
"s_waitcnt vmcnt(13) lgkmcnt(0)\n" \
"s_barrier\n" \
"v_mfma_f32_16x16x32_bf16 v[68:71], a[200:203], a[192:195], v[68:71]\n" \
"ds_read_b64_tr_b16 a[32:33], v204 offset:21504\n" \
"ds_read_b64_tr_b16 a[34:35], v204 offset:24576\n" \
"v_mfma_f32_16x16x32_bf16 v[68:71], a[204:207], a[196:199], v[68:71]\n" \
"ds_read_b64_tr_b16 a[36:37], v204 offset:33792\n" \
"ds_read_b64_tr_b16 a[38:39], v204 offset:36864\n" \
"v_mfma_f32_16x16x32_bf16 v[60:63], a[200:203], a[24:27], v[60:63]\n" \
"s_add_u32 m0, s79, 65536\n" \
"ds_read_b64_tr_b16 a[40:41], v204 offset:20480\n" \
"v_mul_f32 v116, v116, v232\n" \
"v_mfma_f32_16x16x32_bf16 v[60:63], a[204:207], a[28:31], v[60:63]\n" \
"buffer_load_dwordx4 v192, s[44:47], s73 offen offset:0 lds\n" \
"v_mul_f32 v117, v117, v232\n" \
"v_mfma_f32_16x16x32_bf16 v[52:55], a[200:203], a[16:19], v[52:55]\n" \
"ds_read_b64_tr_b16 a[42:43], v204 offset:23552\n" \
"v_mul_f32 v118, v118, v232\n" \
"v_mfma_f32_16x16x32_bf16 v[52:55], a[204:207], a[20:23], v[52:55]\n" \
"ds_read_b64_tr_b16 a[44:45], v204 offset:32768\n" \
"v_mul_f32 v119, v119, v232\n" \
"v_mfma_f32_16x16x32_bf16 v[44:47], a[200:203], a[8:11], v[44:47]\n" \
"ds_read_b64_tr_b16 a[46:47], v204 offset:35840\n" \
"v_cvt_pk_bf16_f32 v120, v112, v113\n" \
"v_mfma_f32_16x16x32_bf16 v[44:47], a[204:207], a[12:15], v[44:47]\n" \
"buffer_load_dwordx4 v193, s[44:47], s73 offen offset:1024 lds\n" \
"v_mfma_f32_16x16x32_bf16 v[72:75], a[0:3], a[208:211], v[72:75]\n" \
"ds_read_b64_tr_b16 a[48:49], v222 offset:20480\n" \
"v_cvt_pk_bf16_f32 v121, v114, v115\n" \
"v_mfma_f32_16x16x32_bf16 v[72:75], a[4:7], a[212:215], v[72:75]\n" \
"ds_read_b128 a[0:3], v202 offset:0\n" \
"v_cvt_pk_bf16_f32 v122, v116, v117\n" \
"v_mfma_f32_16x16x32_bf16 v[80:83], a[8:11], a[208:211], v[80:83]\n" \
"ds_read_b128 a[4:7], v203 offset:0\n" \
"v_cvt_pk_bf16_f32 v123, v118, v119\n" \
"v_mfma_f32_16x16x32_bf16 v[80:83], a[12:15], a[212:215], v[80:83]\n" \
"buffer_load_dwordx4 v192, s[44:47], s73 offen offset:2048 lds\n" \
"v_mfma_f32_16x16x32_bf16 v[84:87], a[8:11], a[216:219], v[84:87]\n" \
"ds_read_b64_tr_b16 a[50:51], v222 offset:23552\n" \
"v_mfma_f32_16x16x32_bf16 v[84:87], a[12:15], a[220:223], v[84:87]\n" \
"ds_read_b128 a[8:11], v200 offset:0\n" \
"buffer_store_dwordx4 v[120:123], v240, s[56:59], s88 offen offset:0 sc1\n" \
"v_mfma_f32_16x16x32_bf16 v[92:95], a[16:19], a[216:219], v[92:95]\n" \
"ds_read_b128 a[12:15], v201 offset:0\n" \
"v_mfma_f32_16x16x32_bf16 v[92:95], a[20:23], a[220:223], v[92:95]\n" \
"buffer_load_dwordx4 v193, s[44:47], s73 offen offset:3072 lds\n" \
"v_mfma_f32_16x16x32_bf16 v[224:227], v[244:247], v[120:123], v[224:227]\n" \
"v_accvgpr_read_b32 v112, a88\n" \
"v_accvgpr_read_b32 v113, a89\n" \
"v_accvgpr_read_b32 v114, a90\n" \
"v_mfma_f32_16x16x32_bf16 v[88:91], a[16:19], a[208:211], v[88:91]\n" \
"ds_read_b64_tr_b16 a[52:53], v222 offset:32768\n" \
"v_accvgpr_read_b32 v115, a91\n" \
"v_mfma_f32_16x16x32_bf16 v[88:91], a[20:23], a[212:215], v[88:91]\n" \
"s_add_u32 m0, s89, 65536\n" \
"ds_read_b128 a[16:19], v200 offset:4096\n" \
"v_accvgpr_read_b32 v116, a92\n" \
"v_accvgpr_read_b32 v117, a93\n" \
"v_mfma_f32_16x16x32_bf16 v[96:99], a[24:27], a[208:211], v[96:99]\n" \
"s_xor_b32 s73, s73, 0x300\n" \
"ds_read_b128 a[20:23], v201 offset:4096\n" \
"v_accvgpr_read_b32 v118, a94\n" \
"v_mfma_f32_16x16x32_bf16 v[96:99], a[28:31], a[212:215], v[96:99]\n" \
"buffer_load_dwordx4 v196, s[44:47], s75 offen lds\n" \
"v_mfma_f32_16x16x32_bf16 v[100:103], a[24:27], a[216:219], v[100:103]\n" \
"s_xor_b32 s75, s75, 0x300\n" \
"s_mul_i32 s76, s80, 393216\n" \
"ds_read_b64_tr_b16 a[54:55], v222 offset:35840\n" \
"v_mfma_f32_16x16x32_bf16 v[100:103], a[28:31], a[220:223], v[100:103]\n" \
"s_add_u32 s76, s76, s78\n" \
"ds_read_b128 a[24:27], v200 offset:8192\n" \
"v_accvgpr_read_b32 v119, a95\n" \
"v_mfma_f32_16x16x32_bf16 a[104:107], v[162:165], v[148:151], a[104:107]\n" \
"v_mul_f32 v112, v112, v232\n" \
"v_mul_f32 v113, v113, v232\n" \
"v_mfma_f32_16x16x32_bf16 v[108:111], a[192:195], a[216:219], v[108:111]\n" \
"s_add_u32 m0, s91, 86016\n" \
"ds_read_b128 a[28:31], v201 offset:8192\n" \
"v_mul_f32 v114, v114, v232\n" \
"v_mfma_f32_16x16x32_bf16 v[108:111], a[196:199], a[220:223], v[108:111]\n" \
"buffer_load_dwordx4 v197, s[48:51], s76 offen offset:0 lds\n" \
"v_mul_f32 v115, v115, v232\n" \
"v_mfma_f32_16x16x32_bf16 a[108:111], v[160:163], v[148:151], a[108:111]\n" \
"v_mul_f32 v116, v116, v232\n" \
"v_mul_f32 v117, v117, v232\n" \
"v_mfma_f32_16x16x32_bf16 v[104:107], a[192:195], a[208:211], v[104:107]\n" \
"v_mul_f32 v118, v118, v232\n" \
"v_mul_f32 v119, v119, v232\n" \
"v_mfma_f32_16x16x32_bf16 v[104:107], a[196:199], a[212:215], v[104:107]\n" \
"s_waitcnt lgkmcnt(2)\n" \
"v_cvt_pk_bf16_f32 v124, v112, v113\n" \
"v_cvt_pk_bf16_f32 v125, v114, v115\n" \
"v_mfma_f32_16x16x32_bf16 v[32:35], a[32:35], a[0:3], v[32:35]\n" \
"s_add_u32 s77, s76, 24576\n" \
"ds_read_b128 a[192:195], v200 offset:12288\n" \
"v_cvt_pk_bf16_f32 v126, v116, v117\n" \
"v_mfma_f32_16x16x32_bf16 v[32:35], a[36:39], a[4:7], v[32:35]\n" \
"buffer_load_dwordx4 v198, s[48:51], s76 offen offset:1024 lds\n" \
"v_mfma_f32_16x16x32_bf16 v[40:43], a[32:35], a[8:11], v[40:43]\n" \
"ds_read_b128 a[196:199], v201 offset:12288\n" \
"v_cvt_pk_bf16_f32 v127, v118, v119\n" \
"v_mfma_f32_16x16x32_bf16 v[40:43], a[36:39], a[12:15], v[40:43]\n" \
"ds_read_b64_tr_b16 a[200:201], v222 offset:21504\n" \
"buffer_store_dwordx4 v[124:127], v240, s[56:59], s88 offen offset:2048 sc1\n" \
"v_mfma_f32_16x16x32_bf16 v[0:3], a[40:43], a[8:11], v[0:3]\n" \
"s_xor_b32 s80, s80, 0x6\n" \
"ds_read_b64_tr_b16 a[202:203], v222 offset:24576\n" \
"v_mfma_f32_16x16x32_bf16 v[0:3], a[44:47], a[12:15], v[0:3]\n" \
"buffer_load_dwordx4 v199, s[48:51], s76 offen offset:2048 lds\n" \
"v_mfma_f32_16x16x32_bf16 v[224:227], v[244:247], v[124:127], v[224:227]\n" \
"s_add_u32 s82, s88, 532480\n" \
"v_accvgpr_read_b32 v112, a96\n" \
"v_accvgpr_read_b32 v113, a97\n" \
"v_accvgpr_read_b32 v114, a98\n" \
"v_mfma_f32_16x16x32_bf16 v[4:7], a[48:51], a[8:11], v[4:7]\n" \
"ds_read_b64_tr_b16 a[204:205], v222 offset:33792\n" \
"v_accvgpr_read_b32 v115, a99\n" \
"v_mfma_f32_16x16x32_bf16 v[4:7], a[52:55], a[12:15], v[4:7]\n" \
"ds_read_b64_tr_b16 a[206:207], v222 offset:36864\n" \
"v_accvgpr_read_b32 v116, a100\n" \
"v_mfma_f32_16x16x32_bf16 v[12:15], a[48:51], a[16:19], v[12:15]\n" \
"s_add_u32 m0, m0, 3072\n" \
"ds_read_b64_tr_b16 a[208:209], v204 offset:22528\n" \
"v_accvgpr_read_b32 v117, a101\n" \
"v_mfma_f32_16x16x32_bf16 v[12:15], a[52:55], a[20:23], v[12:15]\n" \
"buffer_load_dwordx4 v197, s[48:51], s77 offen offset:0 lds\n" \
"v_mfma_f32_16x16x32_bf16 v[8:11], a[40:43], a[16:19], v[8:11]\n" \
"ds_read_b64_tr_b16 a[210:211], v204 offset:25600\n" \
"v_accvgpr_read_b32 v118, a102\n" \
"v_mfma_f32_16x16x32_bf16 v[8:11], a[44:47], a[20:23], v[8:11]\n" \
"ds_read_b64_tr_b16 a[212:213], v204 offset:34816\n" \
"v_accvgpr_read_b32 v119, a103\n" \
"v_mfma_f32_16x16x32_bf16 a[112:115], v[162:165], v[152:155], a[112:115]\n" \
"v_mul_f32 v112, v112, v233\n" \
"v_mul_f32 v113, v113, v233\n" \
"v_mul_f32 v114, v114, v233\n" \
"v_mfma_f32_16x16x32_bf16 v[48:51], a[32:35], a[16:19], v[48:51]\n" \
"s_waitcnt lgkmcnt(3)\n" \
"ds_read_b64_tr_b16 a[214:215], v204 offset:37888\n" \
"v_mul_f32 v115, v115, v233\n" \
"v_mfma_f32_16x16x32_bf16 v[48:51], a[36:39], a[20:23], v[48:51]\n" \
"buffer_load_dwordx4 v198, s[48:51], s77 offen offset:1024 lds\n" \
"v_mfma_f32_16x16x32_bf16 a[116:119], v[160:163], v[152:155], a[116:119]\n" \
"v_mul_f32 v116, v116, v233\n" \
"v_mul_f32 v117, v117, v233\n" \
"v_mfma_f32_16x16x32_bf16 v[56:59], a[32:35], a[24:27], v[56:59]\n" \
"ds_read_b64_tr_b16 a[216:217], v222 offset:22528\n" \
"v_mul_f32 v118, v118, v233\n" \
"v_mfma_f32_16x16x32_bf16 v[56:59], a[36:39], a[28:31], v[56:59]\n" \
"ds_read_b64_tr_b16 a[218:219], v222 offset:25600\n" \
"v_mul_f32 v119, v119, v233\n" \
"v_mfma_f32_16x16x32_bf16 v[16:19], a[40:43], a[24:27], v[16:19]\n" \
"ds_read_b64_tr_b16 a[220:221], v222 offset:34816\n" \
"v_cvt_pk_bf16_f32 v120, v112, v113\n" \
"v_mfma_f32_16x16x32_bf16 v[16:19], a[44:47], a[28:31], v[16:19]\n" \
"buffer_load_dwordx4 v199, s[48:51], s77 offen offset:2048 lds\n" \
"v_cvt_pk_bf16_f32 v121, v114, v115\n" \
"v_mfma_f32_16x16x32_bf16 v[20:23], a[48:51], a[24:27], v[20:23]\n" \
"ds_read_b64_tr_b16 a[222:223], v222 offset:37888\n" \
"v_cvt_pk_bf16_f32 v122, v116, v117\n" \
"v_mfma_f32_16x16x32_bf16 v[20:23], a[52:55], a[28:31], v[20:23]\n" \
"v_cvt_pk_bf16_f32 v123, v118, v119\n" \
"v_mfma_f32_16x16x32_bf16 v[28:31], a[48:51], a[192:195], v[28:31]\n" \
"v_mfma_f32_16x16x32_bf16 v[28:31], a[52:55], a[196:199], v[28:31]\n" \
"buffer_store_dwordx4 v[120:123], v236, s[56:59], s82 offen offset:0 sc1\n" \
"v_mfma_f32_16x16x32_bf16 v[24:27], a[40:43], a[192:195], v[24:27]\n" \
"v_mfma_f32_16x16x32_bf16 v[24:27], a[44:47], a[196:199], v[24:27]\n" \
"v_mfma_f32_16x16x32_bf16 v[224:227], v[244:247], v[120:123], v[224:227]\n" \
"v_accvgpr_read_b32 v112, a104\n" \
"v_accvgpr_read_b32 v113, a105\n" \
"v_mfma_f32_16x16x32_bf16 v[64:67], a[32:35], a[192:195], v[64:67]\n" \
"v_accvgpr_read_b32 v114, a106\n" \
"v_accvgpr_read_b32 v115, a107\n" \
"v_mfma_f32_16x16x32_bf16 v[64:67], a[36:39], a[196:199], v[64:67]\n" \
"v_accvgpr_read_b32 v116, a108\n" \
"s_waitcnt vmcnt(15) lgkmcnt(0)\n" \
"s_barrier\n" \
"v_mfma_f32_16x16x32_bf16 v[68:71], a[200:203], a[192:195], v[68:71]\n" \
"ds_read_b64_tr_b16 a[32:33], v194 offset:1024\n" \
"ds_read_b64_tr_b16 a[34:35], v194 offset:4096\n" \
"v_mfma_f32_16x16x32_bf16 v[68:71], a[204:207], a[196:199], v[68:71]\n" \
"ds_read_b64_tr_b16 a[36:37], v194 offset:13312\n" \
"ds_read_b64_tr_b16 a[38:39], v194 offset:16384\n" \
"v_mfma_f32_16x16x32_bf16 v[60:63], a[200:203], a[24:27], v[60:63]\n" \
"s_add_u32 m0, s79, 0\n" \
"ds_read_b64_tr_b16 a[40:41], v194 offset:0\n" \
"v_accvgpr_read_b32 v117, a109\n" \
"v_mfma_f32_16x16x32_bf16 v[60:63], a[204:207], a[28:31], v[60:63]\n" \
"buffer_load_dwordx4 v192, s[44:47], s73 offen offset:0 lds\n" \
"v_mfma_f32_16x16x32_bf16 v[52:55], a[200:203], a[16:19], v[52:55]\n" \
"ds_read_b64_tr_b16 a[42:43], v194 offset:3072\n" \
"v_accvgpr_read_b32 v118, a110\n" \
"v_mfma_f32_16x16x32_bf16 v[52:55], a[204:207], a[20:23], v[52:55]\n" \
"ds_read_b64_tr_b16 a[44:45], v194 offset:12288\n" \
"v_accvgpr_read_b32 v119, a111\n" \
"v_mfma_f32_16x16x32_bf16 a[120:123], v[162:165], v[156:159], a[120:123]\n" \
"v_mul_f32 v112, v112, v233\n" \
"v_mul_f32 v113, v113, v233\n" \
"v_mul_f32 v114, v114, v233\n" \
"v_mfma_f32_16x16x32_bf16 v[44:47], a[200:203], a[8:11], v[44:47]\n" \
"ds_read_b64_tr_b16 a[46:47], v194 offset:15360\n" \
"v_mul_f32 v115, v115, v233\n" \
"v_mfma_f32_16x16x32_bf16 v[44:47], a[204:207], a[12:15], v[44:47]\n" \
"buffer_load_dwordx4 v193, s[44:47], s73 offen offset:1024 lds\n" \
"v_mfma_f32_16x16x32_bf16 a[124:127], v[160:163], v[156:159], a[124:127]\n" \
"v_mul_f32 v116, v116, v233\n" \
"v_mul_f32 v117, v117, v233\n" \
"v_mfma_f32_16x16x32_bf16 v[72:75], a[0:3], a[208:211], v[72:75]\n" \
"ds_read_b64_tr_b16 a[48:49], v195 offset:0\n" \
"v_mul_f32 v118, v118, v233\n" \
"v_mfma_f32_16x16x32_bf16 v[72:75], a[4:7], a[212:215], v[72:75]\n" \
"ds_read_b128 a[0:3], v207 offset:45056\n" \
"v_mul_f32 v119, v119, v233\n" \
"v_mfma_f32_16x16x32_bf16 v[80:83], a[8:11], a[208:211], v[80:83]\n" \
"ds_read_b128 a[4:7], v208 offset:45056\n" \
"v_cvt_pk_bf16_f32 v124, v112, v113\n" \
"v_mfma_f32_16x16x32_bf16 v[80:83], a[12:15], a[212:215], v[80:83]\n" \
"buffer_load_dwordx4 v192, s[44:47], s73 offen offset:2048 lds\n" \
"v_cvt_pk_bf16_f32 v125, v114, v115\n" \
"v_mfma_f32_16x16x32_bf16 v[84:87], a[8:11], a[216:219], v[84:87]\n" \
"ds_read_b64_tr_b16 a[50:51], v195 offset:3072\n" \
"v_cvt_pk_bf16_f32 v126, v116, v117\n" \
"v_mfma_f32_16x16x32_bf16 v[84:87], a[12:15], a[220:223], v[84:87]\n" \
"ds_read_b128 a[8:11], v205 offset:45056\n" \
"v_cvt_pk_bf16_f32 v127, v118, v119\n" \
"v_mfma_f32_16x16x32_bf16 v[92:95], a[16:19], a[216:219], v[92:95]\n" \
"ds_read_b128 a[12:15], v206 offset:45056\n" \
"v_mfma_f32_16x16x32_bf16 v[92:95], a[20:23], a[220:223], v[92:95]\n" \
"buffer_load_dwordx4 v193, s[44:47], s73 offen offset:3072 lds\n" \
"v_mfma_f32_16x16x32_bf16 v[88:91], a[16:19], a[208:211], v[88:91]\n" \
"ds_read_b64_tr_b16 a[52:53], v195 offset:12288\n" \
"v_mfma_f32_16x16x32_bf16 v[88:91], a[20:23], a[212:215], v[88:91]\n" \
"s_add_u32 m0, s89, 0\n" \
"ds_read_b128 a[16:19], v205 offset:49152\n" \
"buffer_store_dwordx4 v[124:127], v236, s[56:59], s82 offen offset:2048 sc1\n" \
"v_mfma_f32_16x16x32_bf16 v[96:99], a[24:27], a[208:211], v[96:99]\n" \
"s_xor_b32 s73, s73, 0x80\n" \
"ds_read_b128 a[20:23], v206 offset:49152\n" \
"v_mfma_f32_16x16x32_bf16 v[96:99], a[28:31], a[212:215], v[96:99]\n" \
"buffer_load_dwordx4 v196, s[44:47], s75 offen lds\n" \
"v_mfma_f32_16x16x32_bf16 v[224:227], v[244:247], v[124:127], v[224:227]\n" \
"v_accvgpr_read_b32 v112, a112\n" \
"v_accvgpr_read_b32 v113, a113\n" \
"v_accvgpr_read_b32 v114, a114\n" \
"v_mfma_f32_16x16x32_bf16 v[100:103], a[24:27], a[216:219], v[100:103]\n" \
"s_xor_b32 s75, s75, 0x80\n" \
"s_mul_i32 s76, s80, 393216\n" \
"ds_read_b64_tr_b16 a[54:55], v195 offset:15360\n" \
"v_mfma_f32_16x16x32_bf16 v[100:103], a[28:31], a[220:223], v[100:103]\n" \
"s_add_u32 s76, s76, s78\n" \
"ds_read_b128 a[24:27], v205 offset:53248\n" \
"v_accvgpr_read_b32 v115, a115\n" \
"v_mfma_f32_16x16x32_bf16 v[108:111], a[192:195], a[216:219], v[108:111]\n" \
"s_add_u32 m0, s91, 20480\n" \
"ds_read_b128 a[28:31], v206 offset:53248\n" \
"v_accvgpr_read_b32 v116, a116\n" \
"v_mfma_f32_16x16x32_bf16 v[108:111], a[196:199], a[220:223], v[108:111]\n" \
"buffer_load_dwordx4 v197, s[48:51], s76 offen offset:0 lds\n" \
"v_mfma_f32_16x16x32_bf16 v[104:107], a[192:195], a[208:211], v[104:107]\n" \
"v_accvgpr_read_b32 v117, a117\n" \
"v_accvgpr_read_b32 v118, a118\n" \
"v_mfma_f32_16x16x32_bf16 v[104:107], a[196:199], a[212:215], v[104:107]\n" \
"s_waitcnt lgkmcnt(2)\n" \
"v_accvgpr_read_b32 v119, a119\n" \
"v_mul_f32 v112, v112, v233\n" \
"v_mul_f32 v113, v113, v233\n" \
"v_mfma_f32_16x16x32_bf16 v[32:35], a[32:35], a[0:3], v[32:35]\n" \
"s_add_u32 s77, s76, 24576\n" \
"ds_read_b128 a[192:195], v205 offset:57344\n" \
"v_mul_f32 v114, v114, v233\n" \
"v_mfma_f32_16x16x32_bf16 v[32:35], a[36:39], a[4:7], v[32:35]\n" \
"buffer_load_dwordx4 v198, s[48:51], s76 offen offset:1024 lds\n" \
"v_mfma_f32_16x16x32_bf16 v[40:43], a[32:35], a[8:11], v[40:43]\n" \
"ds_read_b128 a[196:199], v206 offset:57344\n" \
"v_mul_f32 v115, v115, v233\n" \
"v_mfma_f32_16x16x32_bf16 v[40:43], a[36:39], a[12:15], v[40:43]\n" \
"ds_read_b64_tr_b16 a[200:201], v195 offset:1024\n" \
"v_mul_f32 v116, v116, v233\n" \
"v_mfma_f32_16x16x32_bf16 v[0:3], a[40:43], a[8:11], v[0:3]\n" \
"s_xor_b32 s80, s80, 0x1\n" \
"ds_read_b64_tr_b16 a[202:203], v195 offset:4096\n" \
"v_mul_f32 v117, v117, v233\n" \
"v_mfma_f32_16x16x32_bf16 v[0:3], a[44:47], a[12:15], v[0:3]\n" \
"buffer_load_dwordx4 v199, s[48:51], s76 offen offset:2048 lds\n" \
"v_mul_f32 v118, v118, v233\n" \
"v_mfma_f32_16x16x32_bf16 v[4:7], a[48:51], a[8:11], v[4:7]\n" \
"ds_read_b64_tr_b16 a[204:205], v195 offset:13312\n" \
"v_mul_f32 v119, v119, v233\n" \
"v_mfma_f32_16x16x32_bf16 v[4:7], a[52:55], a[12:15], v[4:7]\n" \
"ds_read_b64_tr_b16 a[206:207], v195 offset:16384\n" \
"v_cvt_pk_bf16_f32 v120, v112, v113\n" \
"v_mfma_f32_16x16x32_bf16 v[12:15], a[48:51], a[16:19], v[12:15]\n" \
"s_add_u32 m0, m0, 3072\n" \
"ds_read_b64_tr_b16 a[208:209], v194 offset:2048\n" \
"v_cvt_pk_bf16_f32 v121, v114, v115\n" \
"v_mfma_f32_16x16x32_bf16 v[12:15], a[52:55], a[20:23], v[12:15]\n" \
"buffer_load_dwordx4 v197, s[48:51], s77 offen offset:0 lds\n" \
"Lpg7927_%=:\n" \
"Lst1x0_%=:\n" \
"s_cbranch_execz Lpg10211_%=\n" \
"v_mfma_f32_16x16x32_bf16 v[8:11], a[40:43], a[16:19], v[8:11]\n" \
"ds_read_b64_tr_b16 a[210:211], v194 offset:5120\n" \
"v_cvt_pk_bf16_f32 v122, v116, v117\n" \
"v_mfma_f32_16x16x32_bf16 v[8:11], a[44:47], a[20:23], v[8:11]\n" \
"ds_read_b64_tr_b16 a[212:213], v194 offset:14336\n" \
"v_cvt_pk_bf16_f32 v123, v118, v119\n" \
"v_mfma_f32_16x16x32_bf16 v[48:51], a[32:35], a[16:19], v[48:51]\n" \
"s_waitcnt lgkmcnt(3)\n" \
"ds_read_b64_tr_b16 a[214:215], v194 offset:17408\n" \
"v_mfma_f32_16x16x32_bf16 v[48:51], a[36:39], a[20:23], v[48:51]\n" \
"buffer_load_dwordx4 v198, s[48:51], s77 offen offset:1024 lds\n" \
"v_mfma_f32_16x16x32_bf16 v[56:59], a[32:35], a[24:27], v[56:59]\n" \
"ds_read_b64_tr_b16 a[216:217], v195 offset:2048\n" \
"v_mfma_f32_16x16x32_bf16 v[56:59], a[36:39], a[28:31], v[56:59]\n" \
"ds_read_b64_tr_b16 a[218:219], v195 offset:5120\n" \
"buffer_store_dwordx4 v[120:123], v240, s[56:59], s82 offen offset:0 sc1\n" \
"v_mfma_f32_16x16x32_bf16 v[16:19], a[40:43], a[24:27], v[16:19]\n" \
"ds_read_b64_tr_b16 a[220:221], v195 offset:14336\n" \
"v_mfma_f32_16x16x32_bf16 v[16:19], a[44:47], a[28:31], v[16:19]\n" \
"buffer_load_dwordx4 v199, s[48:51], s77 offen offset:2048 lds\n" \
"v_mfma_f32_16x16x32_bf16 v[224:227], v[244:247], v[120:123], v[224:227]\n" \
"v_accvgpr_read_b32 v112, a120\n" \
"v_accvgpr_read_b32 v113, a121\n" \
"v_accvgpr_read_b32 v114, a122\n" \
"v_mfma_f32_16x16x32_bf16 v[20:23], a[48:51], a[24:27], v[20:23]\n" \
"ds_read_b64_tr_b16 a[222:223], v195 offset:17408\n" \
"v_accvgpr_read_b32 v115, a123\n" \
"v_mfma_f32_16x16x32_bf16 v[20:23], a[52:55], a[28:31], v[20:23]\n" \
"v_accvgpr_read_b32 v116, a124\n" \
"v_accvgpr_read_b32 v117, a125\n" \
"v_accvgpr_read_b32 v118, a126\n" \
"v_mfma_f32_16x16x32_bf16 v[28:31], a[48:51], a[192:195], v[28:31]\n" \
"v_accvgpr_read_b32 v119, a127\n" \
"v_mul_f32 v112, v112, v233\n" \
"v_mfma_f32_16x16x32_bf16 v[28:31], a[52:55], a[196:199], v[28:31]\n" \
"v_mul_f32 v113, v113, v233\n" \
"v_mul_f32 v114, v114, v233\n" \
"v_mfma_f32_16x16x32_bf16 v[24:27], a[40:43], a[192:195], v[24:27]\n" \
"v_mul_f32 v115, v115, v233\n" \
"v_mul_f32 v116, v116, v233\n" \
"v_mfma_f32_16x16x32_bf16 v[24:27], a[44:47], a[196:199], v[24:27]\n" \
"v_mul_f32 v117, v117, v233\n" \
"v_mul_f32 v118, v118, v233\n" \
"v_mfma_f32_16x16x32_bf16 v[64:67], a[32:35], a[192:195], v[64:67]\n" \
"v_mul_f32 v119, v119, v233\n" \
"v_cvt_pk_bf16_f32 v124, v112, v113\n" \
"v_mfma_f32_16x16x32_bf16 v[64:67], a[36:39], a[196:199], v[64:67]\n" \
"v_cvt_pk_bf16_f32 v125, v114, v115\n" \
"s_waitcnt vmcnt(14) lgkmcnt(0)\n" \
"s_barrier\n" \
"v_mfma_f32_16x16x32_bf16 v[68:71], a[200:203], a[192:195], v[68:71]\n" \
"ds_read_b64_tr_b16 a[32:33], v209 offset:1024\n" \
"ds_read_b64_tr_b16 a[34:35], v209 offset:4096\n" \
"v_mfma_f32_16x16x32_bf16 v[68:71], a[204:207], a[196:199], v[68:71]\n" \
"ds_read_b64_tr_b16 a[36:37], v209 offset:13312\n" \
"ds_read_b64_tr_b16 a[38:39], v209 offset:16384\n" \
"v_mfma_f32_16x16x32_bf16 v[60:63], a[200:203], a[24:27], v[60:63]\n" \
"s_add_u32 m0, s79, 110592\n" \
"ds_read_b64_tr_b16 a[40:41], v209 offset:0\n" \
"v_cvt_pk_bf16_f32 v126, v116, v117\n" \
"v_mfma_f32_16x16x32_bf16 v[60:63], a[204:207], a[28:31], v[60:63]\n" \
"buffer_load_dwordx4 v192, s[44:47], s73 offen offset:0 lds\n" \
"v_mfma_f32_16x16x32_bf16 v[52:55], a[200:203], a[16:19], v[52:55]\n" \
"ds_read_b64_tr_b16 a[42:43], v209 offset:3072\n" \
"v_cvt_pk_bf16_f32 v127, v118, v119\n" \
"v_mfma_f32_16x16x32_bf16 v[52:55], a[204:207], a[20:23], v[52:55]\n" \
"ds_read_b64_tr_b16 a[44:45], v209 offset:12288\n" \
"buffer_store_dwordx4 v[124:127], v240, s[56:59], s82 offen offset:2048 sc1\n" \
"v_mfma_f32_16x16x32_bf16 v[44:47], a[200:203], a[8:11], v[44:47]\n" \
"ds_read_b64_tr_b16 a[46:47], v209 offset:15360\n" \
"v_mfma_f32_16x16x32_bf16 v[44:47], a[204:207], a[12:15], v[44:47]\n" \
"buffer_load_dwordx4 v193, s[44:47], s73 offen offset:1024 lds\n" \
"v_mfma_f32_16x16x32_bf16 v[224:227], v[244:247], v[124:127], v[224:227]\n" \
"v_mfma_f32_16x16x32_bf16 v[72:75], a[0:3], a[208:211], v[72:75]\n" \
"ds_read_b64_tr_b16 a[48:49], v210 offset:0\n" \
"v_mfma_f32_16x16x32_bf16 v[72:75], a[4:7], a[212:215], v[72:75]\n" \
"ds_read_b128 a[0:3], v207 offset:0\n" \
"v_mfma_f32_16x16x32_bf16 v[80:83], a[8:11], a[208:211], v[80:83]\n" \
"ds_read_b128 a[4:7], v208 offset:0\n" \
"v_mfma_f32_16x16x32_bf16 v[80:83], a[12:15], a[212:215], v[80:83]\n" \
"buffer_load_dwordx4 v192, s[44:47], s73 offen offset:2048 lds\n" \
"v_mfma_f32_16x16x32_bf16 v[84:87], a[8:11], a[216:219], v[84:87]\n" \
"ds_read_b64_tr_b16 a[50:51], v210 offset:3072\n" \
"v_mfma_f32_16x16x32_bf16 v[84:87], a[12:15], a[220:223], v[84:87]\n" \
"ds_read_b128 a[8:11], v205 offset:0\n" \
"v_mfma_f32_16x16x32_bf16 v[92:95], a[16:19], a[216:219], v[92:95]\n" \
"ds_read_b128 a[12:15], v206 offset:0\n" \
"v_mfma_f32_16x16x32_bf16 v[92:95], a[20:23], a[220:223], v[92:95]\n" \
"buffer_load_dwordx4 v193, s[44:47], s73 offen offset:3072 lds\n" \
"v_mfma_f32_16x16x32_bf16 v[88:91], a[16:19], a[208:211], v[88:91]\n" \
"ds_read_b64_tr_b16 a[52:53], v210 offset:12288\n" \
"v_mfma_f32_16x16x32_bf16 v[88:91], a[20:23], a[212:215], v[88:91]\n" \
"s_add_u32 m0, s89, 110592\n" \
"ds_read_b128 a[16:19], v205 offset:4096\n" \
"v_mfma_f32_16x16x32_bf16 v[96:99], a[24:27], a[208:211], v[96:99]\n" \
"s_xor_b32 s73, s73, 0x180\n" \
"ds_read_b128 a[20:23], v206 offset:4096\n" \
"v_mfma_f32_16x16x32_bf16 v[96:99], a[28:31], a[212:215], v[96:99]\n" \
"buffer_load_dwordx4 v196, s[44:47], s75 offen lds\n" \
"v_mfma_f32_16x16x32_bf16 v[100:103], a[24:27], a[216:219], v[100:103]\n" \
"s_xor_b32 s75, s75, 0x180\n" \
"s_mul_i32 s76, s80, 393216\n" \
"ds_read_b64_tr_b16 a[54:55], v210 offset:15360\n" \
"v_mfma_f32_16x16x32_bf16 v[100:103], a[28:31], a[220:223], v[100:103]\n" \
"s_add_u32 s76, s76, s78\n" \
"ds_read_b128 a[24:27], v205 offset:8192\n" \
"v_mfma_f32_16x16x32_bf16 v[108:111], a[192:195], a[216:219], v[108:111]\n" \
"s_add_u32 m0, s91, 139264\n" \
"ds_read_b128 a[28:31], v206 offset:8192\n" \
"v_mfma_f32_16x16x32_bf16 v[108:111], a[196:199], a[220:223], v[108:111]\n" \
"buffer_load_dwordx4 v197, s[48:51], s76 offen offset:0 lds\n" \
"v_mfma_f32_16x16x32_bf16 v[104:107], a[192:195], a[208:211], v[104:107]\n" \
"v_mfma_f32_16x16x32_bf16 v[104:107], a[196:199], a[212:215], v[104:107]\n" \
"s_waitcnt lgkmcnt(2)\n" \
"v_mfma_f32_16x16x32_bf16 v[32:35], a[32:35], a[0:3], v[32:35]\n" \
"s_add_u32 s77, s76, 24576\n" \
"ds_read_b128 a[192:195], v205 offset:12288\n" \
"v_mfma_f32_16x16x32_bf16 v[32:35], a[36:39], a[4:7], v[32:35]\n" \
"buffer_load_dwordx4 v198, s[48:51], s76 offen offset:1024 lds\n" \
"v_mfma_f32_16x16x32_bf16 v[40:43], a[32:35], a[8:11], v[40:43]\n" \
"ds_read_b128 a[196:199], v206 offset:12288\n" \
"v_mfma_f32_16x16x32_bf16 v[40:43], a[36:39], a[12:15], v[40:43]\n" \
"ds_read_b64_tr_b16 a[200:201], v210 offset:1024\n" \
"v_mfma_f32_16x16x32_bf16 v[0:3], a[40:43], a[8:11], v[0:3]\n" \
"s_xor_b32 s80, s80, 0x3\n" \
"ds_read_b64_tr_b16 a[202:203], v210 offset:4096\n" \
"v_mfma_f32_16x16x32_bf16 v[0:3], a[44:47], a[12:15], v[0:3]\n" \
"buffer_load_dwordx4 v199, s[48:51], s76 offen offset:2048 lds\n" \
"v_mfma_f32_16x16x32_bf16 v[4:7], a[48:51], a[8:11], v[4:7]\n" \
"ds_read_b64_tr_b16 a[204:205], v210 offset:13312\n" \
"v_mfma_f32_16x16x32_bf16 v[4:7], a[52:55], a[12:15], v[4:7]\n" \
"ds_read_b64_tr_b16 a[206:207], v210 offset:16384\n" \
"v_mfma_f32_16x16x32_bf16 v[12:15], a[48:51], a[16:19], v[12:15]\n" \
"s_add_u32 m0, m0, 3072\n" \
"ds_read_b64_tr_b16 a[208:209], v209 offset:2048\n" \
"v_mfma_f32_16x16x32_bf16 v[12:15], a[52:55], a[20:23], v[12:15]\n" \
"buffer_load_dwordx4 v197, s[48:51], s77 offen offset:0 lds\n" \
"v_mfma_f32_16x16x32_bf16 v[8:11], a[40:43], a[16:19], v[8:11]\n" \
"ds_read_b64_tr_b16 a[210:211], v209 offset:5120\n" \
"v_mfma_f32_16x16x32_bf16 v[8:11], a[44:47], a[20:23], v[8:11]\n" \
"ds_read_b64_tr_b16 a[212:213], v209 offset:14336\n" \
"v_mfma_f32_16x16x32_bf16 v[48:51], a[32:35], a[16:19], v[48:51]\n" \
"s_waitcnt lgkmcnt(3)\n" \
"ds_read_b64_tr_b16 a[214:215], v209 offset:17408\n" \
"v_mfma_f32_16x16x32_bf16 v[48:51], a[36:39], a[20:23], v[48:51]\n" \
"buffer_load_dwordx4 v198, s[48:51], s77 offen offset:1024 lds\n" \
"v_mfma_f32_16x16x32_bf16 v[56:59], a[32:35], a[24:27], v[56:59]\n" \
"ds_read_b64_tr_b16 a[216:217], v210 offset:2048\n" \
"v_mfma_f32_16x16x32_bf16 v[56:59], a[36:39], a[28:31], v[56:59]\n" \
"ds_read_b64_tr_b16 a[218:219], v210 offset:5120\n" \
"v_mfma_f32_16x16x32_bf16 v[16:19], a[40:43], a[24:27], v[16:19]\n" \
"ds_read_b64_tr_b16 a[220:221], v210 offset:14336\n" \
"v_mfma_f32_16x16x32_bf16 v[16:19], a[44:47], a[28:31], v[16:19]\n" \
"buffer_load_dwordx4 v199, s[48:51], s77 offen offset:2048 lds\n" \
"v_mfma_f32_16x16x32_bf16 v[20:23], a[48:51], a[24:27], v[20:23]\n" \
"ds_read_b64_tr_b16 a[222:223], v210 offset:17408\n" \
"v_mfma_f32_16x16x32_bf16 v[20:23], a[52:55], a[28:31], v[20:23]\n" \
"v_mfma_f32_16x16x32_bf16 v[28:31], a[48:51], a[192:195], v[28:31]\n" \
"v_mfma_f32_16x16x32_bf16 v[28:31], a[52:55], a[196:199], v[28:31]\n" \
"v_mfma_f32_16x16x32_bf16 v[24:27], a[40:43], a[192:195], v[24:27]\n" \
"v_mfma_f32_16x16x32_bf16 v[24:27], a[44:47], a[196:199], v[24:27]\n" \
"v_mfma_f32_16x16x32_bf16 v[64:67], a[32:35], a[192:195], v[64:67]\n" \
"v_mfma_f32_16x16x32_bf16 v[64:67], a[36:39], a[196:199], v[64:67]\n" \
"s_waitcnt vmcnt(12) lgkmcnt(0)\n" \
"s_barrier\n" \
"s_movk_i32 s64, 0x207\n" \
"v_cmp_class_f32 vcc, v224, s64\n" \
"s_nop 1\n" \
"s_cmp_lg_u64 vcc, 0\n" \
"s_addc_u32 s90, s90, 0\n" \
"s_cmp_lg_u32 s90, 0\n" \
"s_cbranch_scc1 Lexact_%=\n" \
"Lexr_%=:\n" \
"v_mfma_f32_16x16x32_bf16 v[68:71], a[200:203], a[192:195], v[68:71]\n" \
"s_lshl_b32 s15, s63, 7\n" \
"ds_read_b64_tr_b16 a[32:33], v204 offset:21504\n" \
"ds_read_b64_tr_b16 a[34:35], v204 offset:24576\n" \
"v_mfma_f32_16x16x32_bf16 v[68:71], a[204:207], a[196:199], v[68:71]\n" \
"s_lshl2_add_u32 s16, s62, 65589248\n" \
"ds_read_b64_tr_b16 a[36:37], v204 offset:33792\n" \
"ds_read_b64_tr_b16 a[38:39], v204 offset:36864\n" \
"v_mfma_f32_16x16x32_bf16 v[60:63], a[200:203], a[24:27], v[60:63]\n" \
"s_add_u32 m0, s79, 65536\n" \
"s_sub_u32 s17, s16, s15\n" \
"ds_read_b64_tr_b16 a[40:41], v204 offset:20480\n" \
"v_mfma_f32_16x16x32_bf16 v[60:63], a[204:207], a[28:31], v[60:63]\n" \
"buffer_load_dwordx4 v192, s[44:47], s73 offen offset:0 lds\n" \
"v_mfma_f32_16x16x32_bf16 v[52:55], a[200:203], a[16:19], v[52:55]\n" \
"s_add_u32 s15, s15, s16\n" \
"s_add_u32 s17, s17, 1920\n" \
"ds_read_b64_tr_b16 a[42:43], v204 offset:23552\n" \
"v_mfma_f32_16x16x32_bf16 v[52:55], a[204:207], a[20:23], v[52:55]\n" \
"ds_read_b64_tr_b16 a[44:45], v204 offset:32768\n" \
"v_mov_b32 v146, s60\n" \
"v_mfma_f32_16x16x32_bf16 v[44:47], a[200:203], a[8:11], v[44:47]\n" \
"ds_read_b64_tr_b16 a[46:47], v204 offset:35840\n" \
"v_lshlrev_b32 v147, 28, s61\n" \
"v_mfma_f32_16x16x32_bf16 v[44:47], a[204:207], a[12:15], v[44:47]\n" \
"buffer_load_dwordx4 v193, s[44:47], s73 offen offset:1024 lds\n" \
"v_mfma_f32_16x16x32_bf16 v[72:75], a[0:3], a[208:211], v[72:75]\n" \
"ds_read_b64_tr_b16 a[48:49], v222 offset:20480\n" \
"v_mfma_f32_16x16x32_bf16 v[72:75], a[4:7], a[212:215], v[72:75]\n" \
"ds_read_b128 a[0:3], v202 offset:0\n" \
"v_mfma_f32_16x16x32_bf16 v[80:83], a[8:11], a[208:211], v[80:83]\n" \
"ds_read_b128 a[4:7], v203 offset:0\n" \
"v_mfma_f32_16x16x32_bf16 v[80:83], a[12:15], a[212:215], v[80:83]\n" \
"buffer_load_dwordx4 v192, s[44:47], s73 offen offset:2048 lds\n" \
"v_mfma_f32_16x16x32_bf16 v[84:87], a[8:11], a[216:219], v[84:87]\n" \
"ds_read_b64_tr_b16 a[50:51], v222 offset:23552\n" \
"v_mfma_f32_16x16x32_bf16 v[84:87], a[12:15], a[220:223], v[84:87]\n" \
"ds_read_b128 a[8:11], v200 offset:0\n" \
"v_mfma_f32_16x16x32_bf16 v[92:95], a[16:19], a[216:219], v[92:95]\n" \
"ds_read_b128 a[12:15], v201 offset:0\n" \
"v_mfma_f32_16x16x32_bf16 v[92:95], a[20:23], a[220:223], v[92:95]\n" \
"buffer_load_dwordx4 v193, s[44:47], s73 offen offset:3072 lds\n" \
"v_mfma_f32_16x16x32_bf16 v[88:91], a[16:19], a[208:211], v[88:91]\n" \
"ds_read_b64_tr_b16 a[52:53], v222 offset:32768\n" \
"v_mfma_f32_16x16x32_bf16 v[88:91], a[20:23], a[212:215], v[88:91]\n" \
"s_add_u32 m0, s89, 65536\n" \
"ds_read_b128 a[16:19], v200 offset:4096\n" \
"v_mfma_f32_16x16x32_bf16 v[96:99], a[24:27], a[208:211], v[96:99]\n" \
"s_xor_b32 s73, s73, 0x80\n" \
"ds_read_b128 a[20:23], v201 offset:4096\n" \
"v_mfma_f32_16x16x32_bf16 v[96:99], a[28:31], a[212:215], v[96:99]\n" \
"buffer_load_dwordx4 v196, s[44:47], s75 offen lds\n" \
"v_mfma_f32_16x16x32_bf16 v[100:103], a[24:27], a[216:219], v[100:103]\n" \
"s_xor_b32 s75, s75, 0x80\n" \
"s_mul_i32 s76, s80, 393216\n" \
"ds_read_b64_tr_b16 a[54:55], v222 offset:35840\n" \
"v_mfma_f32_16x16x32_bf16 v[100:103], a[28:31], a[220:223], v[100:103]\n" \
"s_add_u32 s76, s76, s78\n" \
"ds_read_b128 a[24:27], v200 offset:8192\n" \
"v_mfma_f32_16x16x32_bf16 v[108:111], a[192:195], a[216:219], v[108:111]\n" \
"s_add_u32 m0, s91, 86016\n" \
"ds_read_b128 a[28:31], v201 offset:8192\n" \
"v_mfma_f32_16x16x32_bf16 v[108:111], a[196:199], a[220:223], v[108:111]\n" \
"buffer_load_dwordx4 v197, s[48:51], s76 offen offset:0 lds\n" \
"v_mfma_f32_16x16x32_bf16 v[104:107], a[192:195], a[208:211], v[104:107]\n" \
"v_mfma_f32_16x16x32_bf16 v[104:107], a[196:199], a[212:215], v[104:107]\n" \
"s_waitcnt lgkmcnt(2)\n" \
"v_mfma_f32_16x16x32_bf16 v[32:35], a[32:35], a[0:3], v[32:35]\n" \
"s_add_u32 s77, s76, 24576\n" \
"ds_read_b128 a[192:195], v200 offset:12288\n" \
"v_mfma_f32_16x16x32_bf16 v[32:35], a[36:39], a[4:7], v[32:35]\n" \
"buffer_load_dwordx4 v198, s[48:51], s76 offen offset:1024 lds\n" \
"v_mfma_f32_16x16x32_bf16 v[40:43], a[32:35], a[8:11], v[40:43]\n" \
"ds_read_b128 a[196:199], v201 offset:12288\n" \
"v_mfma_f32_16x16x32_bf16 v[40:43], a[36:39], a[12:15], v[40:43]\n" \
"ds_read_b64_tr_b16 a[200:201], v222 offset:21504\n" \
"v_mfma_f32_16x16x32_bf16 v[0:3], a[40:43], a[8:11], v[0:3]\n" \
"s_xor_b32 s80, s80, 0x1\n" \
"ds_read_b64_tr_b16 a[202:203], v222 offset:24576\n" \
"v_mfma_f32_16x16x32_bf16 v[0:3], a[44:47], a[12:15], v[0:3]\n" \
"buffer_load_dwordx4 v199, s[48:51], s76 offen offset:2048 lds\n" \
"v_mfma_f32_16x16x32_bf16 v[4:7], a[48:51], a[8:11], v[4:7]\n" \
"ds_read_b64_tr_b16 a[204:205], v222 offset:33792\n" \
"v_mfma_f32_16x16x32_bf16 v[4:7], a[52:55], a[12:15], v[4:7]\n" \
"ds_read_b64_tr_b16 a[206:207], v222 offset:36864\n" \
"v_mfma_f32_16x16x32_bf16 v[12:15], a[48:51], a[16:19], v[12:15]\n" \
"s_add_u32 m0, m0, 3072\n" \
"ds_read_b64_tr_b16 a[208:209], v204 offset:22528\n" \
"v_mfma_f32_16x16x32_bf16 v[12:15], a[52:55], a[20:23], v[12:15]\n" \
"buffer_load_dwordx4 v197, s[48:51], s77 offen offset:0 lds\n" \
"v_mfma_f32_16x16x32_bf16 v[8:11], a[40:43], a[16:19], v[8:11]\n" \
"ds_read_b64_tr_b16 a[210:211], v204 offset:25600\n" \
"v_mfma_f32_16x16x32_bf16 v[8:11], a[44:47], a[20:23], v[8:11]\n" \
"ds_read_b64_tr_b16 a[212:213], v204 offset:34816\n" \
"v_mfma_f32_16x16x32_bf16 v[48:51], a[32:35], a[16:19], v[48:51]\n" \
"s_waitcnt lgkmcnt(3)\n" \
"ds_read_b64_tr_b16 a[214:215], v204 offset:37888\n" \
"v_mfma_f32_16x16x32_bf16 v[48:51], a[36:39], a[20:23], v[48:51]\n" \
"buffer_load_dwordx4 v198, s[48:51], s77 offen offset:1024 lds\n" \
"v_mfma_f32_16x16x32_bf16 v[56:59], a[32:35], a[24:27], v[56:59]\n" \
"ds_read_b64_tr_b16 a[216:217], v222 offset:22528\n" \
"v_mfma_f32_16x16x32_bf16 v[56:59], a[36:39], a[28:31], v[56:59]\n" \
"ds_read_b64_tr_b16 a[218:219], v222 offset:25600\n" \
"v_mfma_f32_16x16x32_bf16 v[16:19], a[40:43], a[24:27], v[16:19]\n" \
"ds_read_b64_tr_b16 a[220:221], v222 offset:34816\n" \
"v_mfma_f32_16x16x32_bf16 v[16:19], a[44:47], a[28:31], v[16:19]\n" \
"buffer_load_dwordx4 v199, s[48:51], s77 offen offset:2048 lds\n" \
"v_mfma_f32_16x16x32_bf16 v[20:23], a[48:51], a[24:27], v[20:23]\n" \
"ds_read_b64_tr_b16 a[222:223], v222 offset:37888\n" \
"v_mfma_f32_16x16x32_bf16 v[20:23], a[52:55], a[28:31], v[20:23]\n" \
"v_mfma_f32_16x16x32_bf16 v[28:31], a[48:51], a[192:195], v[28:31]\n" \
"v_mfma_f32_16x16x32_bf16 v[28:31], a[52:55], a[196:199], v[28:31]\n" \
"v_mfma_f32_16x16x32_bf16 v[24:27], a[40:43], a[192:195], v[24:27]\n" \
"v_mfma_f32_16x16x32_bf16 v[24:27], a[44:47], a[196:199], v[24:27]\n" \
"v_mfma_f32_16x16x32_bf16 v[64:67], a[32:35], a[192:195], v[64:67]\n" \
"v_mfma_f32_16x16x32_bf16 v[64:67], a[36:39], a[196:199], v[64:67]\n" \
"s_waitcnt vmcnt(11) lgkmcnt(0)\n" \
"s_barrier\n" \
"v_mfma_f32_16x16x32_bf16 v[68:71], a[200:203], a[192:195], v[68:71]\n" \
"ds_read_b64_tr_b16 a[32:33], v194 offset:1024\n" \
"ds_read_b64_tr_b16 a[34:35], v194 offset:4096\n" \
"v_mfma_f32_16x16x32_bf16 v[68:71], a[204:207], a[196:199], v[68:71]\n" \
"ds_read_b64_tr_b16 a[36:37], v194 offset:13312\n" \
"ds_read_b64_tr_b16 a[38:39], v194 offset:16384\n" \
"v_mfma_f32_16x16x32_bf16 v[60:63], a[200:203], a[24:27], v[60:63]\n" \
"s_add_u32 m0, s79, 0\n" \
"ds_read_b64_tr_b16 a[40:41], v194 offset:0\n" \
"buffer_store_dword v146, v147, s[56:59], s15 offen sc1\n" \
"v_mfma_f32_16x16x32_bf16 v[60:63], a[204:207], a[28:31], v[60:63]\n" \
"buffer_load_dwordx4 v192, s[44:47], s73 offen offset:0 lds\n" \
"v_mfma_f32_16x16x32_bf16 v[52:55], a[200:203], a[16:19], v[52:55]\n" \
"ds_read_b64_tr_b16 a[42:43], v194 offset:3072\n" \
"v_mfma_f32_16x16x32_bf16 v[52:55], a[204:207], a[20:23], v[52:55]\n" \
"ds_read_b64_tr_b16 a[44:45], v194 offset:12288\n" \
"buffer_store_dword v146, v147, s[56:59], s17 offen sc1\n" \
"v_mfma_f32_16x16x32_bf16 v[44:47], a[200:203], a[8:11], v[44:47]\n" \
"s_mov_b64 s[36:37], s[18:19]\n" \
"ds_read_b64_tr_b16 a[46:47], v194 offset:15360\n" \
"v_mfma_f32_16x16x32_bf16 v[44:47], a[204:207], a[12:15], v[44:47]\n" \
"buffer_load_dwordx4 v193, s[44:47], s73 offen offset:1024 lds\n" \
"v_mfma_f32_16x16x32_bf16 v[72:75], a[0:3], a[208:211], v[72:75]\n" \
"s_sub_u32 s36, s36, 4096\n" \
"s_subb_u32 s37, s37, 0\n" \
"ds_read_b64_tr_b16 a[48:49], v195 offset:0\n" \
"v_mfma_f32_16x16x32_bf16 v[72:75], a[4:7], a[212:215], v[72:75]\n" \
"s_and_b32 s37, s37, 0xffff\n" \
"s_mov_b32 s38, 8392704\n" \
"ds_read_b128 a[0:3], v207 offset:45056\n" \
"v_mfma_f32_16x16x32_bf16 v[80:83], a[8:11], a[208:211], v[80:83]\n" \
"s_mov_b32 s39, 0x20000\n" \
"s_mov_b64 s[40:41], s[24:25]\n" \
"ds_read_b128 a[4:7], v208 offset:45056\n" \
"v_mfma_f32_16x16x32_bf16 v[80:83], a[12:15], a[212:215], v[80:83]\n" \
"buffer_load_dwordx4 v192, s[44:47], s73 offen offset:2048 lds\n" \
"v_mfma_f32_16x16x32_bf16 v[84:87], a[8:11], a[216:219], v[84:87]\n" \
"s_sub_u32 s40, s40, 4096\n" \
"s_subb_u32 s41, s41, 0\n" \
"ds_read_b64_tr_b16 a[50:51], v195 offset:3072\n" \
"v_mfma_f32_16x16x32_bf16 v[84:87], a[12:15], a[220:223], v[84:87]\n" \
"s_and_b32 s41, s41, 0xffff\n" \
"s_mov_b32 s42, 2101248\n" \
"ds_read_b128 a[8:11], v205 offset:45056\n" \
"v_mfma_f32_16x16x32_bf16 v[92:95], a[16:19], a[216:219], v[92:95]\n" \
"s_mov_b32 s43, 0x20000\n" \
"s_mov_b64 s[52:53], s[26:27]\n" \
"ds_read_b128 a[12:15], v206 offset:45056\n" \
"v_mfma_f32_16x16x32_bf16 v[92:95], a[20:23], a[220:223], v[92:95]\n" \
"buffer_load_dwordx4 v193, s[44:47], s73 offen offset:3072 lds\n" \
"v_mfma_f32_16x16x32_bf16 v[88:91], a[16:19], a[208:211], v[88:91]\n" \
"s_and_b32 s53, s53, 0xffff\n" \
"s_mov_b32 s54, 8388608\n" \
"ds_read_b64_tr_b16 a[52:53], v195 offset:12288\n" \
"v_mfma_f32_16x16x32_bf16 v[88:91], a[20:23], a[212:215], v[88:91]\n" \
"s_add_u32 m0, s89, 0\n" \
"s_mov_b32 s55, 0x20000\n" \
"ds_read_b128 a[16:19], v205 offset:49152\n" \
"v_mfma_f32_16x16x32_bf16 v[96:99], a[24:27], a[208:211], v[96:99]\n" \
"s_xor_b32 s73, s73, 0x780\n" \
"ds_read_b128 a[20:23], v206 offset:49152\n" \
"v_lshrrev_b32 v221, 2, v234\n" \
"v_mfma_f32_16x16x32_bf16 v[96:99], a[28:31], a[212:215], v[96:99]\n" \
"buffer_load_dwordx4 v196, s[44:47], s75 offen lds\n" \
"v_mfma_f32_16x16x32_bf16 v[100:103], a[24:27], a[216:219], v[100:103]\n" \
"s_xor_b32 s75, s75, 0x780\n" \
"s_mul_i32 s76, s80, 393216\n" \
"ds_read_b64_tr_b16 a[54:55], v195 offset:15360\n" \
"v_mfma_f32_16x16x32_bf16 v[100:103], a[28:31], a[220:223], v[100:103]\n" \
"s_add_u32 s76, s76, s78\n" \
"ds_read_b128 a[24:27], v205 offset:53248\n" \
"v_lshrrev_b32 v244, 4, v234\n" \
"v_mfma_f32_16x16x32_bf16 v[108:111], a[192:195], a[216:219], v[108:111]\n" \
"s_add_u32 m0, s91, 20480\n" \
"s_lshl_b32 s12, s94, 7\n" \
"ds_read_b128 a[28:31], v206 offset:53248\n" \
"v_mfma_f32_16x16x32_bf16 v[108:111], a[196:199], a[220:223], v[108:111]\n" \
"buffer_load_dwordx4 v197, s[48:51], s76 offen offset:0 lds\n" \
"v_mfma_f32_16x16x32_bf16 v[104:107], a[192:195], a[208:211], v[104:107]\n" \
"s_add_u32 s12, s12, 65591296\n" \
"s_and_b32 s70, %1, 1\n" \
"s_lshl3_add_u32 s70, s70, s63\n" \
"v_mfma_f32_16x16x32_bf16 v[104:107], a[196:199], a[212:215], v[104:107]\n" \
"s_waitcnt lgkmcnt(2)\n" \
"s_lshl2_add_u32 s12, s70, s12\n" \
"s_lshr_b32 s68, s63, 1\n" \
"s_add_u32 s68, s68, 2\n" \
"v_mfma_f32_16x16x32_bf16 v[32:35], a[32:35], a[0:3], v[32:35]\n" \
"s_add_u32 s77, s76, 24576\n" \
"s_lshr_b32 s70, s18, 8\n" \
"ds_read_b128 a[192:195], v205 offset:57344\n" \
"v_mfma_f32_16x16x32_bf16 v[32:35], a[36:39], a[4:7], v[32:35]\n" \
"buffer_load_dwordx4 v198, s[48:51], s76 offen offset:1024 lds\n" \
"v_mfma_f32_16x16x32_bf16 v[40:43], a[32:35], a[8:11], v[40:43]\n" \
"s_sub_u32 s68, s68, s70\n" \
"s_and_b32 s68, s68, 3\n" \
"ds_read_b128 a[196:199], v206 offset:57344\n" \
"v_mfma_f32_16x16x32_bf16 v[40:43], a[36:39], a[12:15], v[40:43]\n" \
"s_and_b32 s70, s63, 1\n" \
"s_lshl_b32 s70, s70, 2\n" \
"ds_read_b64_tr_b16 a[200:201], v195 offset:1024\n" \
"v_mfma_f32_16x16x32_bf16 v[0:3], a[40:43], a[8:11], v[0:3]\n" \
"s_xor_b32 s80, s80, 0xf\n" \
"s_or_b32 s68, s68, s70\n" \
"ds_read_b64_tr_b16 a[202:203], v195 offset:4096\n" \
"v_mfma_f32_16x16x32_bf16 v[0:3], a[44:47], a[12:15], v[0:3]\n" \
"buffer_load_dwordx4 v199, s[48:51], s76 offen offset:2048 lds\n" \
"v_mfma_f32_16x16x32_bf16 v[4:7], a[48:51], a[8:11], v[4:7]\n" \
"s_lshr_b32 s84, s61, 1\n" \
"s_lshl_b32 s84, s84, 16\n" \
"ds_read_b64_tr_b16 a[204:205], v195 offset:13312\n" \
"v_mfma_f32_16x16x32_bf16 v[4:7], a[52:55], a[12:15], v[4:7]\n" \
"s_and_b32 s70, s61, 1\n" \
"s_lshl_b32 s70, s70, 14\n" \
"ds_read_b64_tr_b16 a[206:207], v195 offset:16384\n" \
"v_mfma_f32_16x16x32_bf16 v[12:15], a[48:51], a[16:19], v[12:15]\n" \
"s_add_u32 m0, m0, 3072\n" \
"s_add_u32 s84, s84, s70\n" \
"ds_read_b64_tr_b16 a[208:209], v194 offset:2048\n" \
"v_mfma_f32_16x16x32_bf16 v[12:15], a[52:55], a[20:23], v[12:15]\n" \
"buffer_load_dwordx4 v197, s[48:51], s77 offen offset:0 lds\n" \
"v_mfma_f32_16x16x32_bf16 v[8:11], a[40:43], a[16:19], v[8:11]\n" \
"s_lshl_b32 s88, s68, 8\n" \
"s_add_u32 s84, s84, s88\n" \
"ds_read_b64_tr_b16 a[210:211], v194 offset:5120\n" \
"v_mfma_f32_16x16x32_bf16 v[8:11], a[44:47], a[20:23], v[8:11]\n" \
"ds_read_b64_tr_b16 a[212:213], v194 offset:14336\n" \
"v_bfe_u32 v245, v244, 2, 3\n" \
"v_mfma_f32_16x16x32_bf16 v[48:51], a[32:35], a[16:19], v[48:51]\n" \
"s_waitcnt lgkmcnt(3)\n" \
"ds_read_b64_tr_b16 a[214:215], v194 offset:17408\n" \
"v_lshlrev_b32 v245, 11, v245\n" \
"v_mfma_f32_16x16x32_bf16 v[48:51], a[36:39], a[20:23], v[48:51]\n" \
"buffer_load_dwordx4 v198, s[48:51], s77 offen offset:1024 lds\n" \
"v_mfma_f32_16x16x32_bf16 v[56:59], a[32:35], a[24:27], v[56:59]\n" \
"ds_read_b64_tr_b16 a[216:217], v195 offset:2048\n" \
"v_lshrrev_b32 v246, 5, v244\n" \
"v_mfma_f32_16x16x32_bf16 v[56:59], a[36:39], a[28:31], v[56:59]\n" \
"ds_read_b64_tr_b16 a[218:219], v195 offset:5120\n" \
"v_lshl_add_u32 v245, v246, 6, v245\n" \
"v_mfma_f32_16x16x32_bf16 v[16:19], a[40:43], a[24:27], v[16:19]\n" \
"ds_read_b64_tr_b16 a[220:221], v195 offset:14336\n" \
"v_and_b32 v246, 3, v244\n" \
"v_mfma_f32_16x16x32_bf16 v[16:19], a[44:47], a[28:31], v[16:19]\n" \
"buffer_load_dwordx4 v199, s[48:51], s77 offen offset:2048 lds\n" \
"v_mfma_f32_16x16x32_bf16 v[20:23], a[48:51], a[24:27], v[20:23]\n" \
"ds_read_b64_tr_b16 a[222:223], v195 offset:17408\n" \
"v_bfe_u32 v247, v244, 4, 1\n" \
"v_mfma_f32_16x16x32_bf16 v[20:23], a[52:55], a[28:31], v[20:23]\n" \
"v_lshlrev_b32 v247, 1, v247\n" \
"v_xor_b32 v246, v246, v247\n" \
"v_mfma_f32_16x16x32_bf16 v[28:31], a[48:51], a[192:195], v[28:31]\n" \
"v_add_u32 v254, 3145728, v254\n" \
"v_lshl_add_u32 v245, v246, 4, v245\n" \
"v_mfma_f32_16x16x32_bf16 v[28:31], a[52:55], a[196:199], v[28:31]\n" \
"global_load_dword v253, v254, s[22:23]\n" \
"v_mfma_f32_16x16x32_bf16 v[24:27], a[40:43], a[192:195], v[24:27]\n" \
"v_add_u32 v216, 4096, v245\n" \
"v_add_u32 v217, 3200, v245\n" \
"v_mfma_f32_16x16x32_bf16 v[24:27], a[44:47], a[196:199], v[24:27]\n" \
"v_add_u32 v218, 34816, v245\n" \
"v_add_u32 v219, 33920, v245\n" \
"v_mfma_f32_16x16x32_bf16 v[64:67], a[32:35], a[192:195], v[64:67]\n" \
"s_mov_b32 s86, 65536\n" \
"s_lshr_b32 s70, s61, 1\n" \
"s_lshl_b32 s71, s70, 13\n" \
"v_mfma_f32_16x16x32_bf16 v[64:67], a[36:39], a[196:199], v[64:67]\n" \
"s_add_u32 s71, s71, %3\n" \
"s_waitcnt vmcnt(14) lgkmcnt(0)\n" \
"s_barrier\n" \
"v_mfma_f32_16x16x32_bf16 v[68:71], a[200:203], a[192:195], v[68:71]\n" \
"ds_read_b64_tr_b16 a[32:33], v209 offset:1024\n" \
"ds_read_b64_tr_b16 a[34:35], v209 offset:4096\n" \
"v_mfma_f32_16x16x32_bf16 v[68:71], a[204:207], a[196:199], v[68:71]\n" \
"ds_read_b64_tr_b16 a[36:37], v209 offset:13312\n" \
"ds_read_b64_tr_b16 a[38:39], v209 offset:16384\n" \
"v_mfma_f32_16x16x32_bf16 v[60:63], a[200:203], a[24:27], v[60:63]\n" \
"s_add_u32 m0, s79, 110592\n" \
"ds_read_b64_tr_b16 a[40:41], v209 offset:0\n" \
"v_add_u32 v211, s71, v236\n" \
"v_mfma_f32_16x16x32_bf16 v[60:63], a[204:207], a[28:31], v[60:63]\n" \
"buffer_load_dwordx4 v192, s[44:47], s73 offen offset:0 lds\n" \
"v_mfma_f32_16x16x32_bf16 v[52:55], a[200:203], a[16:19], v[52:55]\n" \
"s_lshl_b32 s70, s70, 18\n" \
"s_add_u32 s88, s88, s70\n" \
"ds_read_b64_tr_b16 a[42:43], v209 offset:3072\n" \
"v_mfma_f32_16x16x32_bf16 v[52:55], a[204:207], a[20:23], v[52:55]\n" \
"s_and_b32 s70, s61, 1\n" \
"s_lshl_b32 s71, s70, 7\n" \
"ds_read_b64_tr_b16 a[44:45], v209 offset:12288\n" \
"v_mfma_f32_16x16x32_bf16 v[44:47], a[200:203], a[8:11], v[44:47]\n" \
"s_add_u32 s88, s88, s71\n" \
"ds_read_b64_tr_b16 a[46:47], v209 offset:15360\n" \
"v_and_b32 v245, 3, v244\n" \
"v_mfma_f32_16x16x32_bf16 v[44:47], a[204:207], a[12:15], v[44:47]\n" \
"buffer_load_dwordx4 v193, s[44:47], s73 offen offset:1024 lds\n" \
"v_mfma_f32_16x16x32_bf16 v[72:75], a[0:3], a[208:211], v[72:75]\n" \
"ds_read_b64_tr_b16 a[48:49], v210 offset:0\n" \
"v_lshlrev_b32 v245, 3, v245\n" \
"v_mfma_f32_16x16x32_bf16 v[72:75], a[4:7], a[212:215], v[72:75]\n" \
"ds_read_b128 a[0:3], v207 offset:0\n" \
"v_bfe_u32 v246, v244, 2, 2\n" \
"v_mfma_f32_16x16x32_bf16 v[80:83], a[8:11], a[208:211], v[80:83]\n" \
"ds_read_b128 a[4:7], v208 offset:0\n" \
"v_lshl_add_u32 v245, v246, 6, v245\n" \
"v_mfma_f32_16x16x32_bf16 v[80:83], a[12:15], a[212:215], v[80:83]\n" \
"buffer_load_dwordx4 v192, s[44:47], s73 offen offset:2048 lds\n" \
"v_mfma_f32_16x16x32_bf16 v[84:87], a[8:11], a[216:219], v[84:87]\n" \
"ds_read_b64_tr_b16 a[50:51], v210 offset:3072\n" \
"v_bfe_u32 v246, v244, 4, 1\n" \
"v_mfma_f32_16x16x32_bf16 v[84:87], a[12:15], a[220:223], v[84:87]\n" \
"ds_read_b128 a[8:11], v205 offset:0\n" \
"v_lshl_add_u32 v245, v246, 5, v245\n" \
"v_mfma_f32_16x16x32_bf16 v[92:95], a[16:19], a[216:219], v[92:95]\n" \
"ds_read_b128 a[12:15], v206 offset:0\n" \
"v_lshl_add_u32 v245, v246, 8, v245\n" \
"v_mfma_f32_16x16x32_bf16 v[92:95], a[20:23], a[220:223], v[92:95]\n" \
"buffer_load_dwordx4 v193, s[44:47], s73 offen offset:3072 lds\n" \
"v_mfma_f32_16x16x32_bf16 v[88:91], a[16:19], a[208:211], v[88:91]\n" \
"ds_read_b64_tr_b16 a[52:53], v210 offset:12288\n" \
"v_lshrrev_b32 v246, 5, v244\n" \
"v_mfma_f32_16x16x32_bf16 v[88:91], a[20:23], a[212:215], v[88:91]\n" \
"s_add_u32 m0, s89, 110592\n" \
"ds_read_b128 a[16:19], v205 offset:4096\n" \
"v_lshl_add_u32 v245, v246, 12, v245\n" \
"v_mfma_f32_16x16x32_bf16 v[96:99], a[24:27], a[208:211], v[96:99]\n" \
"s_xor_b32 s73, s73, 0x80\n" \
"s_lshl_b32 s71, s70, 10\n" \
"ds_read_b128 a[20:23], v206 offset:4096\n" \
"v_mfma_f32_16x16x32_bf16 v[96:99], a[28:31], a[212:215], v[96:99]\n" \
"buffer_load_dwordx4 v196, s[44:47], s75 offen lds\n" \
"v_mfma_f32_16x16x32_bf16 v[100:103], a[24:27], a[216:219], v[100:103]\n" \
"s_xor_b32 s75, s75, 0x80\n" \
"s_mul_i32 s76, s80, 393216\n" \
"ds_read_b64_tr_b16 a[54:55], v210 offset:15360\n" \
"v_mfma_f32_16x16x32_bf16 v[100:103], a[28:31], a[220:223], v[100:103]\n" \
"s_add_u32 s76, s76, s78\n" \
"s_add_u32 s71, s71, %3\n" \
"ds_read_b128 a[24:27], v205 offset:8192\n" \
"v_mfma_f32_16x16x32_bf16 v[108:111], a[192:195], a[216:219], v[108:111]\n" \
"s_add_u32 m0, s91, 139264\n" \
"ds_read_b128 a[28:31], v206 offset:8192\n" \
"v_add_u32 v212, s71, v245\n" \
"v_mfma_f32_16x16x32_bf16 v[108:111], a[196:199], a[220:223], v[108:111]\n" \
"buffer_load_dwordx4 v197, s[48:51], s76 offen offset:0 lds\n" \
"v_mfma_f32_16x16x32_bf16 v[104:107], a[192:195], a[208:211], v[104:107]\n" \
"s_mov_b32 s69, 1\n" \
"s_mov_b32 s30, 0x0\n" \
"s_mov_b32 s85, 98304\n" \
"v_mfma_f32_16x16x32_bf16 v[104:107], a[196:199], a[212:215], v[104:107]\n" \
"s_waitcnt lgkmcnt(2)\n" \
"s_sub_u32 s85, s85, 32768\n" \
"s_cselect_b32 s85, 131072, s85\n" \
"v_add_u32 v213, s85, v211\n" \
"v_mfma_f32_16x16x32_bf16 v[32:35], a[32:35], a[0:3], v[32:35]\n" \
"s_add_u32 s77, s76, 24576\n" \
"s_mov_b32 s13, 65593856\n" \
"ds_read_b128 a[192:195], v205 offset:12288\n" \
"v_mfma_f32_16x16x32_bf16 v[32:35], a[36:39], a[4:7], v[32:35]\n" \
"buffer_load_dwordx4 v198, s[48:51], s76 offen offset:1024 lds\n" \
"v_mfma_f32_16x16x32_bf16 v[40:43], a[32:35], a[8:11], v[40:43]\n" \
"ds_read_b128 a[196:199], v206 offset:12288\n" \
"v_lshrrev_b32 v228, 1, v234\n" \
"v_mfma_f32_16x16x32_bf16 v[40:43], a[36:39], a[12:15], v[40:43]\n" \
"ds_read_b64_tr_b16 a[200:201], v210 offset:1024\n" \
"v_add_u32 v214, s85, v212\n" \
"v_mfma_f32_16x16x32_bf16 v[0:3], a[40:43], a[8:11], v[0:3]\n" \
"s_xor_b32 s80, s80, 0x1\n" \
"ds_read_b64_tr_b16 a[202:203], v210 offset:4096\n" \
"Lpg8488_%=:\n" \
"Lst1x1_%=:\n" \
"s_cbranch_execz Lpg10801_%=\n" \
"v_xor_b32_e64 v215, 32, v214\n" \
"v_mfma_f32_16x16x32_bf16 v[0:3], a[44:47], a[12:15], v[0:3]\n" \
"buffer_load_dwordx4 v199, s[48:51], s76 offen offset:2048 lds\n" \
"v_mfma_f32_16x16x32_bf16 v[4:7], a[48:51], a[8:11], v[4:7]\n" \
"ds_read_b64_tr_b16 a[204:205], v210 offset:13312\n" \
"v_mov_b32_e64 v230, s60\n" \
"v_mfma_f32_16x16x32_bf16 v[4:7], a[52:55], a[12:15], v[4:7]\n" \
"ds_read_b64_tr_b16 a[206:207], v210 offset:16384\n" \
"v_lshlrev_b32 v231, 28, s61\n" \
"v_mfma_f32_16x16x32_bf16 v[12:15], a[48:51], a[16:19], v[12:15]\n" \
"s_add_u32 m0, m0, 3072\n" \
"ds_read_b64_tr_b16 a[208:209], v209 offset:2048\n" \
"v_mfma_f32_16x16x32_bf16 v[12:15], a[52:55], a[20:23], v[12:15]\n" \
"buffer_load_dwordx4 v197, s[48:51], s77 offen offset:0 lds\n" \
"v_mfma_f32_16x16x32_bf16 v[8:11], a[40:43], a[16:19], v[8:11]\n" \
"ds_read_b64_tr_b16 a[210:211], v209 offset:5120\n" \
"v_mfma_f32_16x16x32_bf16 v[8:11], a[44:47], a[20:23], v[8:11]\n" \
"ds_read_b64_tr_b16 a[212:213], v209 offset:14336\n" \
"v_mfma_f32_16x16x32_bf16 v[48:51], a[32:35], a[16:19], v[48:51]\n" \
"s_waitcnt lgkmcnt(3)\n" \
"ds_read_b64_tr_b16 a[214:215], v209 offset:17408\n" \
"v_mfma_f32_16x16x32_bf16 v[48:51], a[36:39], a[20:23], v[48:51]\n" \
"buffer_load_dwordx4 v198, s[48:51], s77 offen offset:1024 lds\n" \
"v_mfma_f32_16x16x32_bf16 v[56:59], a[32:35], a[24:27], v[56:59]\n" \
"ds_read_b64_tr_b16 a[216:217], v210 offset:2048\n" \
"v_mfma_f32_16x16x32_bf16 v[56:59], a[36:39], a[28:31], v[56:59]\n" \
"ds_read_b64_tr_b16 a[218:219], v210 offset:5120\n" \
"v_mfma_f32_16x16x32_bf16 v[16:19], a[40:43], a[24:27], v[16:19]\n" \
"ds_read_b64_tr_b16 a[220:221], v210 offset:14336\n" \
"v_mfma_f32_16x16x32_bf16 v[16:19], a[44:47], a[28:31], v[16:19]\n" \
"buffer_load_dwordx4 v199, s[48:51], s77 offen offset:2048 lds\n" \
"v_mfma_f32_16x16x32_bf16 v[20:23], a[48:51], a[24:27], v[20:23]\n" \
"ds_read_b64_tr_b16 a[222:223], v210 offset:17408\n" \
"v_mfma_f32_16x16x32_bf16 v[20:23], a[52:55], a[28:31], v[20:23]\n" \
"v_mfma_f32_16x16x32_bf16 v[28:31], a[48:51], a[192:195], v[28:31]\n" \
"v_mfma_f32_16x16x32_bf16 v[28:31], a[52:55], a[196:199], v[28:31]\n" \
"buffer_load_dwordx2 v[226:227], v228, s[56:59], s13 offen sc1\n" \
"v_mfma_f32_16x16x32_bf16 v[24:27], a[40:43], a[192:195], v[24:27]\n" \
"v_mfma_f32_16x16x32_bf16 v[24:27], a[44:47], a[196:199], v[24:27]\n" \
"v_mfma_f32_16x16x32_bf16 v[64:67], a[32:35], a[192:195], v[64:67]\n" \
"v_mfma_f32_16x16x32_bf16 v[64:67], a[36:39], a[196:199], v[64:67]\n" \
"s_waitcnt vmcnt(13) lgkmcnt(0)\n" \
"s_barrier\n" \
"v_mfma_f32_16x16x32_bf16 v[68:71], a[200:203], a[192:195], v[68:71]\n" \
"ds_read_b64_tr_b16 a[32:33], v204 offset:21504\n" \
"ds_read_b64_tr_b16 a[34:35], v204 offset:24576\n" \
"v_mfma_f32_16x16x32_bf16 v[68:71], a[204:207], a[196:199], v[68:71]\n" \
"ds_read_b64_tr_b16 a[36:37], v204 offset:33792\n" \
"ds_read_b64_tr_b16 a[38:39], v204 offset:36864\n" \
"v_mfma_f32_16x16x32_bf16 v[60:63], a[200:203], a[24:27], v[60:63]\n" \
"s_add_u32 m0, s79, 65536\n" \
"ds_read_b64_tr_b16 a[40:41], v204 offset:20480\n" \
"v_mfma_f32_16x16x32_bf16 v[60:63], a[204:207], a[28:31], v[60:63]\n" \
"buffer_load_dwordx4 v192, s[44:47], s73 offen offset:0 lds\n" \
"v_mfma_f32_16x16x32_bf16 v[52:55], a[200:203], a[16:19], v[52:55]\n" \
"ds_read_b64_tr_b16 a[42:43], v204 offset:23552\n" \
"v_mfma_f32_16x16x32_bf16 v[52:55], a[204:207], a[20:23], v[52:55]\n" \
"ds_read_b64_tr_b16 a[44:45], v204 offset:32768\n" \
"v_mfma_f32_16x16x32_bf16 v[44:47], a[200:203], a[8:11], v[44:47]\n" \
"ds_read_b64_tr_b16 a[46:47], v204 offset:35840\n" \
"v_mfma_f32_16x16x32_bf16 v[44:47], a[204:207], a[12:15], v[44:47]\n" \
"buffer_load_dwordx4 v193, s[44:47], s73 offen offset:1024 lds\n" \
"v_mfma_f32_16x16x32_bf16 v[72:75], a[0:3], a[208:211], v[72:75]\n" \
"ds_read_b64_tr_b16 a[48:49], v222 offset:20480\n" \
"v_mfma_f32_16x16x32_bf16 v[72:75], a[4:7], a[212:215], v[72:75]\n" \
"ds_read_b128 a[0:3], v202 offset:0\n" \
"v_mfma_f32_16x16x32_bf16 v[80:83], a[8:11], a[208:211], v[80:83]\n" \
"ds_read_b128 a[4:7], v203 offset:0\n" \
"v_mfma_f32_16x16x32_bf16 v[80:83], a[12:15], a[212:215], v[80:83]\n" \
"buffer_load_dwordx4 v192, s[44:47], s73 offen offset:2048 lds\n" \
"v_mfma_f32_16x16x32_bf16 v[84:87], a[8:11], a[216:219], v[84:87]\n" \
"ds_read_b64_tr_b16 a[50:51], v222 offset:23552\n" \
"v_mfma_f32_16x16x32_bf16 v[84:87], a[12:15], a[220:223], v[84:87]\n" \
"ds_read_b128 a[8:11], v200 offset:0\n" \
"v_mfma_f32_16x16x32_bf16 v[92:95], a[16:19], a[216:219], v[92:95]\n" \
"ds_read_b128 a[12:15], v201 offset:0\n" \
"v_mfma_f32_16x16x32_bf16 v[92:95], a[20:23], a[220:223], v[92:95]\n" \
"buffer_load_dwordx4 v193, s[44:47], s73 offen offset:3072 lds\n" \
"v_mfma_f32_16x16x32_bf16 v[88:91], a[16:19], a[208:211], v[88:91]\n" \
"ds_read_b64_tr_b16 a[52:53], v222 offset:32768\n" \
"v_mfma_f32_16x16x32_bf16 v[88:91], a[20:23], a[212:215], v[88:91]\n" \
"s_add_u32 m0, s89, 65536\n" \
"ds_read_b128 a[16:19], v200 offset:4096\n" \
"v_mfma_f32_16x16x32_bf16 v[96:99], a[24:27], a[208:211], v[96:99]\n" \
"s_xor_b32 s73, s73, 0x180\n" \
"ds_read_b128 a[20:23], v201 offset:4096\n" \
"v_mfma_f32_16x16x32_bf16 v[96:99], a[28:31], a[212:215], v[96:99]\n" \
"buffer_load_dwordx4 v196, s[44:47], s75 offen lds\n" \
"v_mfma_f32_16x16x32_bf16 v[100:103], a[24:27], a[216:219], v[100:103]\n" \
"s_xor_b32 s75, s75, 0x180\n" \
"s_mul_i32 s76, s80, 393216\n" \
"ds_read_b64_tr_b16 a[54:55], v222 offset:35840\n" \
"v_mfma_f32_16x16x32_bf16 v[100:103], a[28:31], a[220:223], v[100:103]\n" \
"s_add_u32 s76, s76, s78\n" \
"ds_read_b128 a[24:27], v200 offset:8192\n" \
"v_mfma_f32_16x16x32_bf16 v[108:111], a[192:195], a[216:219], v[108:111]\n" \
"s_add_u32 m0, s91, 86016\n" \
"ds_read_b128 a[28:31], v201 offset:8192\n" \
"v_mfma_f32_16x16x32_bf16 v[108:111], a[196:199], a[220:223], v[108:111]\n" \
"buffer_load_dwordx4 v197, s[48:51], s76 offen offset:0 lds\n" \
"v_mfma_f32_16x16x32_bf16 v[104:107], a[192:195], a[208:211], v[104:107]\n" \
"v_mfma_f32_16x16x32_bf16 v[104:107], a[196:199], a[212:215], v[104:107]\n" \
"s_waitcnt lgkmcnt(2)\n" \
"v_mfma_f32_16x16x32_bf16 v[32:35], a[32:35], a[0:3], v[32:35]\n" \
"s_add_u32 s77, s76, 24576\n" \
"ds_read_b128 a[192:195], v200 offset:12288\n" \
"v_mfma_f32_16x16x32_bf16 v[32:35], a[36:39], a[4:7], v[32:35]\n" \
"buffer_load_dwordx4 v198, s[48:51], s76 offen offset:1024 lds\n" \
"v_mfma_f32_16x16x32_bf16 v[40:43], a[32:35], a[8:11], v[40:43]\n" \
"ds_read_b128 a[196:199], v201 offset:12288\n" \
"v_mfma_f32_16x16x32_bf16 v[40:43], a[36:39], a[12:15], v[40:43]\n" \
"ds_read_b64_tr_b16 a[200:201], v222 offset:21504\n" \
"v_mfma_f32_16x16x32_bf16 v[0:3], a[40:43], a[8:11], v[0:3]\n" \
"s_xor_b32 s80, s80, 0x3\n" \
"ds_read_b64_tr_b16 a[202:203], v222 offset:24576\n" \
"v_mfma_f32_16x16x32_bf16 v[0:3], a[44:47], a[12:15], v[0:3]\n" \
"buffer_load_dwordx4 v199, s[48:51], s76 offen offset:2048 lds\n" \
"v_mfma_f32_16x16x32_bf16 v[4:7], a[48:51], a[8:11], v[4:7]\n" \
"ds_read_b64_tr_b16 a[204:205], v222 offset:33792\n" \
"v_mfma_f32_16x16x32_bf16 v[4:7], a[52:55], a[12:15], v[4:7]\n" \
"ds_read_b64_tr_b16 a[206:207], v222 offset:36864\n" \
"v_mfma_f32_16x16x32_bf16 v[12:15], a[48:51], a[16:19], v[12:15]\n" \
"s_add_u32 m0, m0, 3072\n" \
"ds_read_b64_tr_b16 a[208:209], v204 offset:22528\n" \
"v_mfma_f32_16x16x32_bf16 v[12:15], a[52:55], a[20:23], v[12:15]\n" \
"buffer_load_dwordx4 v197, s[48:51], s77 offen offset:0 lds\n" \
"v_mfma_f32_16x16x32_bf16 v[8:11], a[40:43], a[16:19], v[8:11]\n" \
"ds_read_b64_tr_b16 a[210:211], v204 offset:25600\n" \
"v_mfma_f32_16x16x32_bf16 v[8:11], a[44:47], a[20:23], v[8:11]\n" \
"ds_read_b64_tr_b16 a[212:213], v204 offset:34816\n" \
"v_mfma_f32_16x16x32_bf16 v[48:51], a[32:35], a[16:19], v[48:51]\n" \
"s_waitcnt lgkmcnt(3)\n" \
"ds_read_b64_tr_b16 a[214:215], v204 offset:37888\n" \
"v_mfma_f32_16x16x32_bf16 v[48:51], a[36:39], a[20:23], v[48:51]\n" \
"buffer_load_dwordx4 v198, s[48:51], s77 offen offset:1024 lds\n" \
"v_mfma_f32_16x16x32_bf16 v[56:59], a[32:35], a[24:27], v[56:59]\n" \
"ds_read_b64_tr_b16 a[216:217], v222 offset:22528\n" \
"v_mfma_f32_16x16x32_bf16 v[56:59], a[36:39], a[28:31], v[56:59]\n" \
"ds_read_b64_tr_b16 a[218:219], v222 offset:25600\n" \
"v_mfma_f32_16x16x32_bf16 v[16:19], a[40:43], a[24:27], v[16:19]\n" \
"ds_read_b64_tr_b16 a[220:221], v222 offset:34816\n" \
"v_mfma_f32_16x16x32_bf16 v[16:19], a[44:47], a[28:31], v[16:19]\n" \
"buffer_load_dwordx4 v199, s[48:51], s77 offen offset:2048 lds\n" \
"v_mfma_f32_16x16x32_bf16 v[20:23], a[48:51], a[24:27], v[20:23]\n" \
"ds_read_b64_tr_b16 a[222:223], v222 offset:37888\n" \
"v_mfma_f32_16x16x32_bf16 v[20:23], a[52:55], a[28:31], v[20:23]\n" \
"v_mfma_f32_16x16x32_bf16 v[28:31], a[48:51], a[192:195], v[28:31]\n" \
"v_mfma_f32_16x16x32_bf16 v[28:31], a[52:55], a[196:199], v[28:31]\n" \
"v_mfma_f32_16x16x32_bf16 v[24:27], a[40:43], a[192:195], v[24:27]\n" \
"v_mfma_f32_16x16x32_bf16 v[24:27], a[44:47], a[196:199], v[24:27]\n" \
"v_mfma_f32_16x16x32_bf16 v[64:67], a[32:35], a[192:195], v[64:67]\n" \
"v_mfma_f32_16x16x32_bf16 v[64:67], a[36:39], a[196:199], v[64:67]\n" \
"s_waitcnt vmcnt(12) lgkmcnt(0)\n" \
"s_barrier\n" \
"v_mfma_f32_16x16x32_bf16 v[68:71], a[200:203], a[192:195], v[68:71]\n" \
"ds_read_b64_tr_b16 a[32:33], v194 offset:1024\n" \
"ds_read_b64_tr_b16 a[34:35], v194 offset:4096\n" \
"v_mfma_f32_16x16x32_bf16 v[68:71], a[204:207], a[196:199], v[68:71]\n" \
"ds_read_b64_tr_b16 a[36:37], v194 offset:13312\n" \
"ds_read_b64_tr_b16 a[38:39], v194 offset:16384\n" \
"v_mfma_f32_16x16x32_bf16 v[60:63], a[200:203], a[24:27], v[60:63]\n" \
"s_add_u32 m0, s79, 0\n" \
"ds_read_b64_tr_b16 a[40:41], v194 offset:0\n" \
"v_mfma_f32_16x16x32_bf16 v[60:63], a[204:207], a[28:31], v[60:63]\n" \
"buffer_load_dwordx4 v192, s[44:47], s73 offen offset:0 lds\n" \
"v_mfma_f32_16x16x32_bf16 v[52:55], a[200:203], a[16:19], v[52:55]\n" \
"ds_read_b64_tr_b16 a[42:43], v194 offset:3072\n" \
"v_mfma_f32_16x16x32_bf16 v[52:55], a[204:207], a[20:23], v[52:55]\n" \
"ds_read_b64_tr_b16 a[44:45], v194 offset:12288\n" \
"v_mfma_f32_16x16x32_bf16 v[44:47], a[200:203], a[8:11], v[44:47]\n" \
"ds_read_b64_tr_b16 a[46:47], v194 offset:15360\n" \
"v_mfma_f32_16x16x32_bf16 v[44:47], a[204:207], a[12:15], v[44:47]\n" \
"buffer_load_dwordx4 v193, s[44:47], s73 offen offset:1024 lds\n" \
"v_mfma_f32_16x16x32_bf16 v[72:75], a[0:3], a[208:211], v[72:75]\n" \
"ds_read_b64_tr_b16 a[48:49], v195 offset:0\n" \
"v_mfma_f32_16x16x32_bf16 v[72:75], a[4:7], a[212:215], v[72:75]\n" \
"ds_read_b128 a[0:3], v207 offset:45056\n" \
"v_mfma_f32_16x16x32_bf16 v[80:83], a[8:11], a[208:211], v[80:83]\n" \
"ds_read_b128 a[4:7], v208 offset:45056\n" \
"v_mfma_f32_16x16x32_bf16 v[80:83], a[12:15], a[212:215], v[80:83]\n" \
"buffer_load_dwordx4 v192, s[44:47], s73 offen offset:2048 lds\n" \
"v_mfma_f32_16x16x32_bf16 v[84:87], a[8:11], a[216:219], v[84:87]\n" \
"ds_read_b64_tr_b16 a[50:51], v195 offset:3072\n" \
"v_mfma_f32_16x16x32_bf16 v[84:87], a[12:15], a[220:223], v[84:87]\n" \
"ds_read_b128 a[8:11], v205 offset:45056\n" \
"v_mfma_f32_16x16x32_bf16 v[92:95], a[16:19], a[216:219], v[92:95]\n" \
"ds_read_b128 a[12:15], v206 offset:45056\n" \
"v_mfma_f32_16x16x32_bf16 v[92:95], a[20:23], a[220:223], v[92:95]\n" \
"buffer_load_dwordx4 v193, s[44:47], s73 offen offset:3072 lds\n" \
"v_mfma_f32_16x16x32_bf16 v[88:91], a[16:19], a[208:211], v[88:91]\n" \
"ds_read_b64_tr_b16 a[52:53], v195 offset:12288\n" \
"v_mfma_f32_16x16x32_bf16 v[88:91], a[20:23], a[212:215], v[88:91]\n" \
"s_add_u32 m0, s89, 0\n" \
"ds_read_b128 a[16:19], v205 offset:49152\n" \
"v_mfma_f32_16x16x32_bf16 v[96:99], a[24:27], a[208:211], v[96:99]\n" \
"s_xor_b32 s73, s73, 0x300\n" \
"ds_read_b128 a[20:23], v206 offset:49152\n" \
"v_mfma_f32_16x16x32_bf16 v[96:99], a[28:31], a[212:215], v[96:99]\n" \
"buffer_load_dwordx4 v196, s[44:47], s75 offen lds\n" \
"v_mfma_f32_16x16x32_bf16 v[100:103], a[24:27], a[216:219], v[100:103]\n" \
"s_xor_b32 s75, s75, 0x300\n" \
"s_mul_i32 s76, s80, 393216\n" \
"ds_read_b64_tr_b16 a[54:55], v195 offset:15360\n" \
"v_mfma_f32_16x16x32_bf16 v[100:103], a[28:31], a[220:223], v[100:103]\n" \
"s_add_u32 s76, s76, s78\n" \
"ds_read_b128 a[24:27], v205 offset:53248\n" \
"v_mfma_f32_16x16x32_bf16 v[108:111], a[192:195], a[216:219], v[108:111]\n" \
"s_add_u32 m0, s91, 20480\n" \
"ds_read_b128 a[28:31], v206 offset:53248\n" \
"v_mfma_f32_16x16x32_bf16 v[108:111], a[196:199], a[220:223], v[108:111]\n" \
"buffer_load_dwordx4 v197, s[48:51], s76 offen offset:0 lds\n" \
"v_mfma_f32_16x16x32_bf16 v[104:107], a[192:195], a[208:211], v[104:107]\n" \
"v_mfma_f32_16x16x32_bf16 v[104:107], a[196:199], a[212:215], v[104:107]\n" \
"s_waitcnt lgkmcnt(2)\n" \
"v_mfma_f32_16x16x32_bf16 v[32:35], a[32:35], a[0:3], v[32:35]\n" \
"s_add_u32 s77, s76, 24576\n" \
"ds_read_b128 a[192:195], v205 offset:57344\n" \
"v_mfma_f32_16x16x32_bf16 v[32:35], a[36:39], a[4:7], v[32:35]\n" \
"buffer_load_dwordx4 v198, s[48:51], s76 offen offset:1024 lds\n" \
"v_mfma_f32_16x16x32_bf16 v[40:43], a[32:35], a[8:11], v[40:43]\n" \
"ds_read_b128 a[196:199], v206 offset:57344\n" \
"v_mfma_f32_16x16x32_bf16 v[40:43], a[36:39], a[12:15], v[40:43]\n" \
"ds_read_b64_tr_b16 a[200:201], v195 offset:1024\n" \
"v_mfma_f32_16x16x32_bf16 v[0:3], a[40:43], a[8:11], v[0:3]\n" \
"s_xor_b32 s80, s80, 0x6\n" \
"ds_read_b64_tr_b16 a[202:203], v195 offset:4096\n" \
"v_mfma_f32_16x16x32_bf16 v[0:3], a[44:47], a[12:15], v[0:3]\n" \
"buffer_load_dwordx4 v199, s[48:51], s76 offen offset:2048 lds\n" \
"v_mfma_f32_16x16x32_bf16 v[4:7], a[48:51], a[8:11], v[4:7]\n" \
"ds_read_b64_tr_b16 a[204:205], v195 offset:13312\n" \
"v_mfma_f32_16x16x32_bf16 v[4:7], a[52:55], a[12:15], v[4:7]\n" \
"ds_read_b64_tr_b16 a[206:207], v195 offset:16384\n" \
"v_mfma_f32_16x16x32_bf16 v[12:15], a[48:51], a[16:19], v[12:15]\n" \
"s_add_u32 m0, m0, 3072\n" \
"ds_read_b64_tr_b16 a[208:209], v194 offset:2048\n" \
"v_mfma_f32_16x16x32_bf16 v[12:15], a[52:55], a[20:23], v[12:15]\n" \
"buffer_load_dwordx4 v197, s[48:51], s77 offen offset:0 lds\n" \
"v_mfma_f32_16x16x32_bf16 v[8:11], a[40:43], a[16:19], v[8:11]\n" \
"ds_read_b64_tr_b16 a[210:211], v194 offset:5120\n" \
"v_mfma_f32_16x16x32_bf16 v[8:11], a[44:47], a[20:23], v[8:11]\n" \
"ds_read_b64_tr_b16 a[212:213], v194 offset:14336\n" \
"v_mfma_f32_16x16x32_bf16 v[48:51], a[32:35], a[16:19], v[48:51]\n" \
"s_waitcnt lgkmcnt(3)\n" \
"ds_read_b64_tr_b16 a[214:215], v194 offset:17408\n" \
"v_mfma_f32_16x16x32_bf16 v[48:51], a[36:39], a[20:23], v[48:51]\n" \
"buffer_load_dwordx4 v198, s[48:51], s77 offen offset:1024 lds\n" \
"v_mfma_f32_16x16x32_bf16 v[56:59], a[32:35], a[24:27], v[56:59]\n" \
"ds_read_b64_tr_b16 a[216:217], v195 offset:2048\n" \
"v_mfma_f32_16x16x32_bf16 v[56:59], a[36:39], a[28:31], v[56:59]\n" \
"ds_read_b64_tr_b16 a[218:219], v195 offset:5120\n" \
"v_mfma_f32_16x16x32_bf16 v[16:19], a[40:43], a[24:27], v[16:19]\n" \
"ds_read_b64_tr_b16 a[220:221], v195 offset:14336\n" \
"v_mfma_f32_16x16x32_bf16 v[16:19], a[44:47], a[28:31], v[16:19]\n" \
"buffer_load_dwordx4 v199, s[48:51], s77 offen offset:2048 lds\n" \
"v_mfma_f32_16x16x32_bf16 v[20:23], a[48:51], a[24:27], v[20:23]\n" \
"ds_read_b64_tr_b16 a[222:223], v195 offset:17408\n" \
"v_mfma_f32_16x16x32_bf16 v[20:23], a[52:55], a[28:31], v[20:23]\n" \
"v_mfma_f32_16x16x32_bf16 v[28:31], a[48:51], a[192:195], v[28:31]\n" \
"v_mfma_f32_16x16x32_bf16 v[28:31], a[52:55], a[196:199], v[28:31]\n" \
"v_mfma_f32_16x16x32_bf16 v[24:27], a[40:43], a[192:195], v[24:27]\n" \
"v_mfma_f32_16x16x32_bf16 v[24:27], a[44:47], a[196:199], v[24:27]\n" \
"v_mfma_f32_16x16x32_bf16 v[64:67], a[32:35], a[192:195], v[64:67]\n" \
"v_mfma_f32_16x16x32_bf16 v[64:67], a[36:39], a[196:199], v[64:67]\n" \
"s_waitcnt vmcnt(11) lgkmcnt(0)\n" \
"s_barrier\n" \
"v_mfma_f32_16x16x32_bf16 v[68:71], a[200:203], a[192:195], v[68:71]\n" \
"ds_read_b64_tr_b16 a[32:33], v209 offset:1024\n" \
"ds_read_b64_tr_b16 a[34:35], v209 offset:4096\n" \
"v_mfma_f32_16x16x32_bf16 v[68:71], a[204:207], a[196:199], v[68:71]\n" \
"ds_read_b64_tr_b16 a[36:37], v209 offset:13312\n" \
"ds_read_b64_tr_b16 a[38:39], v209 offset:16384\n" \
"v_mfma_f32_16x16x32_bf16 v[60:63], a[200:203], a[24:27], v[60:63]\n" \
"s_add_u32 m0, s79, 110592\n" \
"ds_read_b64_tr_b16 a[40:41], v209 offset:0\n" \
"v_cmp_eq_u32 vcc, s60, v226\n" \
"v_mfma_f32_16x16x32_bf16 v[60:63], a[204:207], a[28:31], v[60:63]\n" \
"buffer_load_dwordx4 v192, s[44:47], s73 offen offset:0 lds\n" \
"v_mfma_f32_16x16x32_bf16 v[52:55], a[200:203], a[16:19], v[52:55]\n" \
"s_andn2_b32 s70, 0xff, vcc_lo\n" \
"s_cbranch_scc1 Lpl4_%=\n" \
"Lpr4_%=:\n" \
"ds_read_b64_tr_b16 a[42:43], v209 offset:3072\n" \
"v_mfma_f32_16x16x32_bf16 v[52:55], a[204:207], a[20:23], v[52:55]\n" \
"s_and_b32 s70, %1, 7\n" \
"ds_read_b64_tr_b16 a[44:45], v209 offset:12288\n" \
"v_readlane_b32 s70, v227, s70\n" \
"v_mfma_f32_16x16x32_bf16 v[44:47], a[200:203], a[8:11], v[44:47]\n" \
"ds_read_b64_tr_b16 a[46:47], v209 offset:15360\n" \
"v_mfma_f32_16x16x32_bf16 v[44:47], a[204:207], a[12:15], v[44:47]\n" \
"buffer_load_dwordx4 v193, s[44:47], s73 offen offset:1024 lds\n" \
"v_mfma_f32_16x16x32_bf16 v[72:75], a[0:3], a[208:211], v[72:75]\n" \
"ds_read_b64_tr_b16 a[48:49], v210 offset:0\n" \
"v_subrev_u32 v227, s70, v227\n" \
"v_mfma_f32_16x16x32_bf16 v[72:75], a[4:7], a[212:215], v[72:75]\n" \
"ds_read_b128 a[0:3], v207 offset:0\n" \
"v_cmp_gt_i32 vcc, 0, v227\n" \
"v_mfma_f32_16x16x32_bf16 v[80:83], a[8:11], a[208:211], v[80:83]\n" \
"ds_read_b128 a[4:7], v208 offset:0\n" \
"v_mfma_f32_16x16x32_bf16 v[80:83], a[12:15], a[212:215], v[80:83]\n" \
"buffer_load_dwordx4 v192, s[44:47], s73 offen offset:2048 lds\n" \
"v_mfma_f32_16x16x32_bf16 v[84:87], a[8:11], a[216:219], v[84:87]\n" \
"s_and_b32 s13, vcc_lo, 0xff\n" \
"s_bcnt1_i32_b32 s13, s13\n" \
"ds_read_b64_tr_b16 a[50:51], v210 offset:3072\n" \
"v_mfma_f32_16x16x32_bf16 v[84:87], a[12:15], a[220:223], v[84:87]\n" \
"s_lshr_b32 s90, s13, 2\n" \
"s_mul_i32 s70, s90, 7\n" \
"ds_read_b128 a[8:11], v205 offset:0\n" \
"v_mfma_f32_16x16x32_bf16 v[92:95], a[16:19], a[216:219], v[92:95]\n" \
"s_xor_b32 s13, s13, s70\n" \
"s_lshl_b32 s70, s13, 3\n" \
"ds_read_b128 a[12:15], v206 offset:0\n" \
"v_mfma_f32_16x16x32_bf16 v[92:95], a[20:23], a[220:223], v[92:95]\n" \
"buffer_load_dwordx4 v193, s[44:47], s73 offen offset:3072 lds\n" \
"v_mfma_f32_16x16x32_bf16 v[88:91], a[16:19], a[208:211], v[88:91]\n" \
"s_lshr_b32 s9, 0x2a2b2d2f, s70\n" \
"s_and_b32 s9, s9, 0xff\n" \
"ds_read_b64_tr_b16 a[52:53], v210 offset:12288\n" \
"v_mfma_f32_16x16x32_bf16 v[88:91], a[20:23], a[212:215], v[88:91]\n" \
"s_add_u32 m0, s89, 110592\n" \
"s_lshr_b32 s8, %1, 6\n" \
"ds_read_b128 a[16:19], v205 offset:4096\n" \
"v_mfma_f32_16x16x32_bf16 v[96:99], a[24:27], a[208:211], v[96:99]\n" \
"s_xor_b32 s73, s73, 0x80\n" \
"s_lshl2_add_u32 s8, s13, s8\n" \
"ds_read_b128 a[20:23], v206 offset:4096\n" \
"v_mfma_f32_16x16x32_bf16 v[96:99], a[28:31], a[212:215], v[96:99]\n" \
"buffer_load_dwordx4 v196, s[44:47], s75 offen lds\n" \
"v_mfma_f32_16x16x32_bf16 v[100:103], a[24:27], a[216:219], v[100:103]\n" \
"s_xor_b32 s75, s75, 0x80\n" \
"s_mul_i32 s76, s80, 393216\n" \
"ds_read_b64_tr_b16 a[54:55], v210 offset:15360\n" \
"v_mfma_f32_16x16x32_bf16 v[100:103], a[28:31], a[220:223], v[100:103]\n" \
"s_add_u32 s76, s76, s78\n" \
"ds_read_b128 a[24:27], v205 offset:8192\n" \
"v_max_i32_dpp v226, v227, v227 quad_perm:[1,0,3,2] row_mask:0xf bank_mask:0xf\n" \
"v_mfma_f32_16x16x32_bf16 v[108:111], a[192:195], a[216:219], v[108:111]\n" \
"s_add_u32 m0, s91, 139264\n" \
"s_mov_b32 s14, 0\n" \
"ds_read_b128 a[28:31], v206 offset:8192\n" \
"v_mfma_f32_16x16x32_bf16 v[108:111], a[196:199], a[220:223], v[108:111]\n" \
"buffer_load_dwordx4 v197, s[48:51], s76 offen offset:0 lds\n" \
"v_mfma_f32_16x16x32_bf16 v[104:107], a[192:195], a[208:211], v[104:107]\n" \
"v_readlane_b32 s15, v226, 2\n" \
"v_readlane_b32 s16, v226, 4\n" \
"v_mfma_f32_16x16x32_bf16 v[104:107], a[196:199], a[212:215], v[104:107]\n" \
"s_waitcnt lgkmcnt(2)\n" \
"v_readlane_b32 s17, v226, 6\n" \
"v_cmp_gt_i32 vcc, s15, v226\n" \
"v_mfma_f32_16x16x32_bf16 v[32:35], a[32:35], a[0:3], v[32:35]\n" \
"s_add_u32 s77, s76, 24576\n" \
"ds_read_b128 a[192:195], v205 offset:12288\n" \
"v_mfma_f32_16x16x32_bf16 v[32:35], a[36:39], a[4:7], v[32:35]\n" \
"buffer_load_dwordx4 v198, s[48:51], s76 offen offset:1024 lds\n" \
"v_mfma_f32_16x16x32_bf16 v[40:43], a[32:35], a[8:11], v[40:43]\n" \
"s_and_b32 s70, vcc_lo, 0x55\n" \
"s_bcnt1_i32_b32 s70, s70\n" \
"ds_read_b128 a[196:199], v206 offset:12288\n" \
"v_mfma_f32_16x16x32_bf16 v[40:43], a[36:39], a[12:15], v[40:43]\n" \
"s_cmp_eq_u32 s70, s13\n" \
"s_cselect_b32 s14, 4, s14\n" \
"ds_read_b64_tr_b16 a[200:201], v210 offset:1024\n" \
"v_mfma_f32_16x16x32_bf16 v[0:3], a[40:43], a[8:11], v[0:3]\n" \
"s_xor_b32 s80, s80, 0x1\n" \
"ds_read_b64_tr_b16 a[202:203], v210 offset:4096\n" \
"v_cmp_gt_i32 vcc, s16, v226\n" \
"v_mfma_f32_16x16x32_bf16 v[0:3], a[44:47], a[12:15], v[0:3]\n" \
"buffer_load_dwordx4 v199, s[48:51], s76 offen offset:2048 lds\n" \
"v_mfma_f32_16x16x32_bf16 v[4:7], a[48:51], a[8:11], v[4:7]\n" \
"s_and_b32 s70, vcc_lo, 0x55\n" \
"s_bcnt1_i32_b32 s70, s70\n" \
"ds_read_b64_tr_b16 a[204:205], v210 offset:13312\n" \
"v_mfma_f32_16x16x32_bf16 v[4:7], a[52:55], a[12:15], v[4:7]\n" \
"s_cmp_eq_u32 s70, s13\n" \
"s_cselect_b32 s14, 8, s14\n" \
"ds_read_b64_tr_b16 a[206:207], v210 offset:16384\n" \
"v_mfma_f32_16x16x32_bf16 v[12:15], a[48:51], a[16:19], v[12:15]\n" \
"s_add_u32 m0, m0, 3072\n" \
"ds_read_b64_tr_b16 a[208:209], v209 offset:2048\n" \
"v_cmp_gt_i32 vcc, s17, v226\n" \
"v_mfma_f32_16x16x32_bf16 v[12:15], a[52:55], a[20:23], v[12:15]\n" \
"buffer_load_dwordx4 v197, s[48:51], s77 offen offset:0 lds\n" \
"v_mfma_f32_16x16x32_bf16 v[8:11], a[40:43], a[16:19], v[8:11]\n" \
"s_and_b32 s70, vcc_lo, 0x55\n" \
"s_bcnt1_i32_b32 s70, s70\n" \
"ds_read_b64_tr_b16 a[210:211], v209 offset:5120\n" \
"v_mfma_f32_16x16x32_bf16 v[8:11], a[44:47], a[20:23], v[8:11]\n" \
"s_cmp_eq_u32 s70, s13\n" \
"s_cselect_b32 s14, 12, s14\n" \
"ds_read_b64_tr_b16 a[212:213], v209 offset:14336\n" \
"v_mfma_f32_16x16x32_bf16 v[48:51], a[32:35], a[16:19], v[48:51]\n" \
"s_lshr_b32 s70, %1, 6\n" \
"s_add_u32 s14, s14, s70\n" \
"ds_read_b64_tr_b16 a[214:215], v209 offset:17408\n" \
"v_mfma_f32_16x16x32_bf16 v[48:51], a[36:39], a[20:23], v[48:51]\n" \
"s_waitcnt lgkmcnt(3)\n" \
"buffer_load_dwordx4 v198, s[48:51], s77 offen offset:1024 lds\n" \
"v_mfma_f32_16x16x32_bf16 v[56:59], a[32:35], a[24:27], v[56:59]\n" \
"s_cmp_eq_u32 s90, 0\n" \
"s_cselect_b32 s14, s8, s14\n" \
"ds_read_b64_tr_b16 a[216:217], v210 offset:2048\n" \
"v_mfma_f32_16x16x32_bf16 v[56:59], a[36:39], a[28:31], v[56:59]\n" \
"s_sub_u32 s10, 49, s9\n" \
"s_lshl_b32 s11, s9, 1\n" \
"ds_read_b64_tr_b16 a[218:219], v210 offset:5120\n" \
"v_mfma_f32_16x16x32_bf16 v[16:19], a[40:43], a[24:27], v[16:19]\n" \
"s_sub_u32 s11, s11, 71\n" \
"s_lshl2_add_u32 s70, s8, s61\n" \
"ds_read_b64_tr_b16 a[220:221], v210 offset:14336\n" \
"v_mfma_f32_16x16x32_bf16 v[16:19], a[44:47], a[28:31], v[16:19]\n" \
"buffer_load_dwordx4 v199, s[48:51], s77 offen offset:2048 lds\n" \
"v_mfma_f32_16x16x32_bf16 v[20:23], a[48:51], a[24:27], v[20:23]\n" \
"s_mul_i32 s81, s70, 266240\n" \
"s_mul_i32 s82, s90, s9\n" \
"ds_read_b64_tr_b16 a[222:223], v210 offset:17408\n" \
"v_mfma_f32_16x16x32_bf16 v[20:23], a[52:55], a[28:31], v[20:23]\n" \
"s_lshl_b32 s71, s82, 12\n" \
"s_add_u32 s81, s81, s71\n" \
"s_add_u32 s81, s81, 35700736\n" \
"v_mfma_f32_16x16x32_bf16 v[28:31], a[48:51], a[192:195], v[28:31]\n" \
"s_lshl2_add_u32 s70, s14, s61\n" \
"s_mul_i32 s83, s70, 69632\n" \
"s_add_u32 s83, s83, 52740096\n" \
"v_mfma_f32_16x16x32_bf16 v[28:31], a[52:55], a[196:199], v[28:31]\n" \
"s_lshl_b32 s82, s82, 17\n" \
"s_add_u32 s82, s82, s84\n" \
"s_lshl_b32 s70, s14, 19\n" \
"v_mfma_f32_16x16x32_bf16 v[24:27], a[40:43], a[192:195], v[24:27]\n" \
"s_add_u32 s88, s88, s70\n" \
"s_lshl_b32 s71, s90, 11\n" \
"s_add_u32 s88, s88, s71\n" \
"v_mfma_f32_16x16x32_bf16 v[24:27], a[44:47], a[196:199], v[24:27]\n" \
"s_lshl3_add_u32 s70, s8, s68\n" \
"s_lshl2_add_u32 s87, s70, s61\n" \
"s_lshl_b32 s87, s87, 14\n" \
"v_mfma_f32_16x16x32_bf16 v[64:67], a[32:35], a[192:195], v[64:67]\n" \
"s_add_u32 s87, s87, 57196544\n" \
"s_lshl_b32 s64, s70, 7\n" \
"s_add_u32 s64, s64, 65601536\n" \
"v_mfma_f32_16x16x32_bf16 v[64:67], a[36:39], a[196:199], v[64:67]\n" \
"s_lshl_b32 s66, s8, 7\n" \
"s_waitcnt vmcnt(11) lgkmcnt(0)\n" \
"s_barrier\n" \
"v_mfma_f32_16x16x32_bf16 v[68:71], a[200:203], a[192:195], v[68:71]\n" \
"s_lshl_b32 s67, s14, 7\n" \
"ds_read_b64_tr_b16 a[32:33], v204 offset:21504\n" \
"ds_read_b64_tr_b16 a[34:35], v204 offset:24576\n" \
"v_mfma_f32_16x16x32_bf16 v[68:71], a[204:207], a[196:199], v[68:71]\n" \
"s_add_u32 s67, s67, 65591296\n" \
"ds_read_b64_tr_b16 a[36:37], v204 offset:33792\n" \
"ds_read_b64_tr_b16 a[38:39], v204 offset:36864\n" \
"v_mfma_f32_16x16x32_bf16 v[60:63], a[200:203], a[24:27], v[60:63]\n" \
"s_add_u32 m0, s79, 65536\n" \
"s_add_u32 s66, s66, 65589248\n" \
"ds_read_b64_tr_b16 a[40:41], v204 offset:20480\n" \
"v_mfma_f32_16x16x32_bf16 v[60:63], a[204:207], a[28:31], v[60:63]\n" \
"buffer_load_dwordx4 v192, s[44:47], s73 offen offset:0 lds\n" \
"v_mfma_f32_16x16x32_bf16 v[52:55], a[200:203], a[16:19], v[52:55]\n" \
"s_add_u32 s70, s9, 1\n" \
"s_sub_u32 s70, s70, s90\n" \
"ds_read_b64_tr_b16 a[42:43], v204 offset:23552\n" \
"v_mfma_f32_16x16x32_bf16 v[52:55], a[204:207], a[20:23], v[52:55]\n" \
"s_lshr_b32 s70, s70, 1\n" \
"s_bfm_b32 s65, s70, 0\n" \
"ds_read_b64_tr_b16 a[44:45], v204 offset:32768\n" \
"v_mfma_f32_16x16x32_bf16 v[44:47], a[200:203], a[8:11], v[44:47]\n" \
"s_sub_u32 s70, 0, s90\n" \
"s_xor_b32 s65, s65, s70\n" \
"ds_read_b64_tr_b16 a[46:47], v204 offset:35840\n" \
"v_mfma_f32_16x16x32_bf16 v[44:47], a[204:207], a[12:15], v[44:47]\n" \
"buffer_load_dwordx4 v193, s[44:47], s73 offen offset:1024 lds\n" \
"v_mfma_f32_16x16x32_bf16 v[72:75], a[0:3], a[208:211], v[72:75]\n" \
"s_and_b32 s74, s70, 0xffff\n" \
"s_xor_b32 s68, s68, s8\n" \
"ds_read_b64_tr_b16 a[48:49], v222 offset:20480\n" \
"v_mfma_f32_16x16x32_bf16 v[72:75], a[4:7], a[212:215], v[72:75]\n" \
"s_and_b32 s68, s68, 3\n" \
"s_lshl_b32 s68, s68, 12\n" \
"ds_read_b128 a[0:3], v202 offset:0\n" \
"v_mfma_f32_16x16x32_bf16 v[80:83], a[8:11], a[208:211], v[80:83]\n" \
"ds_read_b128 a[4:7], v203 offset:0\n" \
"v_mfma_f32_16x16x32_bf16 v[80:83], a[12:15], a[212:215], v[80:83]\n" \
"buffer_load_dwordx4 v192, s[44:47], s73 offen offset:2048 lds\n" \
"v_mfma_f32_16x16x32_bf16 v[84:87], a[8:11], a[216:219], v[84:87]\n" \
"ds_read_b64_tr_b16 a[50:51], v222 offset:23552\n" \
"v_mfma_f32_16x16x32_bf16 v[84:87], a[12:15], a[220:223], v[84:87]\n" \
"ds_read_b128 a[8:11], v200 offset:0\n" \
"v_mfma_f32_16x16x32_bf16 v[92:95], a[16:19], a[216:219], v[92:95]\n" \
"ds_read_b128 a[12:15], v201 offset:0\n" \
"v_mfma_f32_16x16x32_bf16 v[92:95], a[20:23], a[220:223], v[92:95]\n" \
"buffer_load_dwordx4 v193, s[44:47], s73 offen offset:3072 lds\n" \
"v_mfma_f32_16x16x32_bf16 v[88:91], a[16:19], a[208:211], v[88:91]\n" \
"ds_read_b64_tr_b16 a[52:53], v222 offset:32768\n" \
"v_mfma_f32_16x16x32_bf16 v[88:91], a[20:23], a[212:215], v[88:91]\n" \
"s_add_u32 m0, s89, 65536\n" \
"ds_read_b128 a[16:19], v200 offset:4096\n" \
"v_mfma_f32_16x16x32_bf16 v[96:99], a[24:27], a[208:211], v[96:99]\n" \
"s_xor_b32 s73, s73, 0x180\n" \
"ds_read_b128 a[20:23], v201 offset:4096\n" \
"v_mfma_f32_16x16x32_bf16 v[96:99], a[28:31], a[212:215], v[96:99]\n" \
"buffer_load_dwordx4 v196, s[44:47], s75 offen lds\n" \
"v_mfma_f32_16x16x32_bf16 v[100:103], a[24:27], a[216:219], v[100:103]\n" \
"s_xor_b32 s75, s75, 0x180\n" \
"s_mul_i32 s76, s80, 393216\n" \
"ds_read_b64_tr_b16 a[54:55], v222 offset:35840\n" \
"v_mfma_f32_16x16x32_bf16 v[100:103], a[28:31], a[220:223], v[100:103]\n" \
"s_add_u32 s76, s76, s78\n" \
"ds_read_b128 a[24:27], v200 offset:8192\n" \
"v_mfma_f32_16x16x32_bf16 v[108:111], a[192:195], a[216:219], v[108:111]\n" \
"s_add_u32 m0, s91, 86016\n" \
"ds_read_b128 a[28:31], v201 offset:8192\n" \
"v_mfma_f32_16x16x32_bf16 v[108:111], a[196:199], a[220:223], v[108:111]\n" \
"buffer_load_dwordx4 v197, s[48:51], s76 offen offset:0 lds\n" \
"v_mfma_f32_16x16x32_bf16 v[104:107], a[192:195], a[208:211], v[104:107]\n" \
"v_mfma_f32_16x16x32_bf16 v[104:107], a[196:199], a[212:215], v[104:107]\n" \
"s_waitcnt lgkmcnt(2)\n" \
"v_mfma_f32_16x16x32_bf16 v[32:35], a[32:35], a[0:3], v[32:35]\n" \
"s_add_u32 s77, s76, 24576\n" \
"ds_read_b128 a[192:195], v200 offset:12288\n" \
"v_mfma_f32_16x16x32_bf16 v[32:35], a[36:39], a[4:7], v[32:35]\n" \
"buffer_load_dwordx4 v198, s[48:51], s76 offen offset:1024 lds\n" \
"v_mfma_f32_16x16x32_bf16 v[40:43], a[32:35], a[8:11], v[40:43]\n" \
"ds_read_b128 a[196:199], v201 offset:12288\n" \
"v_mfma_f32_16x16x32_bf16 v[40:43], a[36:39], a[12:15], v[40:43]\n" \
"ds_read_b64_tr_b16 a[200:201], v222 offset:21504\n" \
"v_mfma_f32_16x16x32_bf16 v[0:3], a[40:43], a[8:11], v[0:3]\n" \
"s_xor_b32 s80, s80, 0x3\n" \
"ds_read_b64_tr_b16 a[202:203], v222 offset:24576\n" \
"v_mfma_f32_16x16x32_bf16 v[0:3], a[44:47], a[12:15], v[0:3]\n" \
"buffer_load_dwordx4 v199, s[48:51], s76 offen offset:2048 lds\n" \
"v_mfma_f32_16x16x32_bf16 v[4:7], a[48:51], a[8:11], v[4:7]\n" \
"ds_read_b64_tr_b16 a[204:205], v222 offset:33792\n" \
"v_mfma_f32_16x16x32_bf16 v[4:7], a[52:55], a[12:15], v[4:7]\n" \
"ds_read_b64_tr_b16 a[206:207], v222 offset:36864\n" \
"v_mfma_f32_16x16x32_bf16 v[12:15], a[48:51], a[16:19], v[12:15]\n" \
"s_add_u32 m0, m0, 3072\n" \
"ds_read_b64_tr_b16 a[208:209], v204 offset:22528\n" \
"v_mfma_f32_16x16x32_bf16 v[12:15], a[52:55], a[20:23], v[12:15]\n" \
"buffer_load_dwordx4 v197, s[48:51], s77 offen offset:0 lds\n" \
"v_mfma_f32_16x16x32_bf16 v[8:11], a[40:43], a[16:19], v[8:11]\n" \
"ds_read_b64_tr_b16 a[210:211], v204 offset:25600\n" \
"v_mfma_f32_16x16x32_bf16 v[8:11], a[44:47], a[20:23], v[8:11]\n" \
"ds_read_b64_tr_b16 a[212:213], v204 offset:34816\n" \
"v_mfma_f32_16x16x32_bf16 v[48:51], a[32:35], a[16:19], v[48:51]\n" \
"s_waitcnt lgkmcnt(3)\n" \
"ds_read_b64_tr_b16 a[214:215], v204 offset:37888\n" \
"v_mfma_f32_16x16x32_bf16 v[48:51], a[36:39], a[20:23], v[48:51]\n" \
"buffer_load_dwordx4 v198, s[48:51], s77 offen offset:1024 lds\n" \
"v_mfma_f32_16x16x32_bf16 v[56:59], a[32:35], a[24:27], v[56:59]\n" \
"ds_read_b64_tr_b16 a[216:217], v222 offset:22528\n" \
"v_mfma_f32_16x16x32_bf16 v[56:59], a[36:39], a[28:31], v[56:59]\n" \
"ds_read_b64_tr_b16 a[218:219], v222 offset:25600\n" \
"v_mfma_f32_16x16x32_bf16 v[16:19], a[40:43], a[24:27], v[16:19]\n" \
"ds_read_b64_tr_b16 a[220:221], v222 offset:34816\n" \
"Lpg9043_%=:\n" \
"Lst1x2_%=:\n" \
"s_cbranch_execz Lpg10924_%=\n" \
"v_mfma_f32_16x16x32_bf16 v[16:19], a[44:47], a[28:31], v[16:19]\n" \
"buffer_load_dwordx4 v199, s[48:51], s77 offen offset:2048 lds\n" \
"v_mfma_f32_16x16x32_bf16 v[20:23], a[48:51], a[24:27], v[20:23]\n" \
"ds_read_b64_tr_b16 a[222:223], v222 offset:37888\n" \
"v_mfma_f32_16x16x32_bf16 v[20:23], a[52:55], a[28:31], v[20:23]\n" \
"v_mfma_f32_16x16x32_bf16 v[28:31], a[48:51], a[192:195], v[28:31]\n" \
"v_mfma_f32_16x16x32_bf16 v[28:31], a[52:55], a[196:199], v[28:31]\n" \
"v_mfma_f32_16x16x32_bf16 v[24:27], a[40:43], a[192:195], v[24:27]\n" \
"v_mfma_f32_16x16x32_bf16 v[24:27], a[44:47], a[196:199], v[24:27]\n" \
"v_mfma_f32_16x16x32_bf16 v[64:67], a[32:35], a[192:195], v[64:67]\n" \
"v_mfma_f32_16x16x32_bf16 v[64:67], a[36:39], a[196:199], v[64:67]\n" \
"s_waitcnt vmcnt(11) lgkmcnt(0)\n" \
"s_barrier\n" \
"v_mfma_f32_16x16x32_bf16 v[68:71], a[200:203], a[192:195], v[68:71]\n" \
"ds_read_b64_tr_b16 a[32:33], v194 offset:1024\n" \
"ds_read_b64_tr_b16 a[34:35], v194 offset:4096\n" \
"v_mfma_f32_16x16x32_bf16 v[68:71], a[204:207], a[196:199], v[68:71]\n" \
"ds_read_b64_tr_b16 a[36:37], v194 offset:13312\n" \
"ds_read_b64_tr_b16 a[38:39], v194 offset:16384\n" \
"v_mfma_f32_16x16x32_bf16 v[60:63], a[200:203], a[24:27], v[60:63]\n" \
"s_add_u32 m0, s79, 0\n" \
"ds_read_b64_tr_b16 a[40:41], v194 offset:0\n" \
"v_mfma_f32_16x16x32_bf16 v[60:63], a[204:207], a[28:31], v[60:63]\n" \
"buffer_load_dwordx4 v192, s[44:47], s73 offen offset:0 lds\n" \
"v_mfma_f32_16x16x32_bf16 v[52:55], a[200:203], a[16:19], v[52:55]\n" \
"ds_read_b64_tr_b16 a[42:43], v194 offset:3072\n" \
"v_mfma_f32_16x16x32_bf16 v[52:55], a[204:207], a[20:23], v[52:55]\n" \
"ds_read_b64_tr_b16 a[44:45], v194 offset:12288\n" \
"v_mfma_f32_16x16x32_bf16 v[44:47], a[200:203], a[8:11], v[44:47]\n" \
"ds_read_b64_tr_b16 a[46:47], v194 offset:15360\n" \
"v_mfma_f32_16x16x32_bf16 v[44:47], a[204:207], a[12:15], v[44:47]\n" \
"buffer_load_dwordx4 v193, s[44:47], s73 offen offset:1024 lds\n" \
"v_mfma_f32_16x16x32_bf16 v[72:75], a[0:3], a[208:211], v[72:75]\n" \
"ds_read_b64_tr_b16 a[48:49], v195 offset:0\n" \
"v_mfma_f32_16x16x32_bf16 v[72:75], a[4:7], a[212:215], v[72:75]\n" \
"ds_read_b128 a[0:3], v207 offset:45056\n" \
"v_mfma_f32_16x16x32_bf16 v[80:83], a[8:11], a[208:211], v[80:83]\n" \
"ds_read_b128 a[4:7], v208 offset:45056\n" \
"v_mfma_f32_16x16x32_bf16 v[80:83], a[12:15], a[212:215], v[80:83]\n" \
"buffer_load_dwordx4 v192, s[44:47], s73 offen offset:2048 lds\n" \
"v_mfma_f32_16x16x32_bf16 v[84:87], a[8:11], a[216:219], v[84:87]\n" \
"ds_read_b64_tr_b16 a[50:51], v195 offset:3072\n" \
"v_mfma_f32_16x16x32_bf16 v[84:87], a[12:15], a[220:223], v[84:87]\n" \
"ds_read_b128 a[8:11], v205 offset:45056\n" \
"v_mfma_f32_16x16x32_bf16 v[92:95], a[16:19], a[216:219], v[92:95]\n" \
"ds_read_b128 a[12:15], v206 offset:45056\n" \
"v_mfma_f32_16x16x32_bf16 v[92:95], a[20:23], a[220:223], v[92:95]\n" \
"buffer_load_dwordx4 v193, s[44:47], s73 offen offset:3072 lds\n" \
"v_mfma_f32_16x16x32_bf16 v[88:91], a[16:19], a[208:211], v[88:91]\n" \
"ds_read_b64_tr_b16 a[52:53], v195 offset:12288\n" \
"v_mfma_f32_16x16x32_bf16 v[88:91], a[20:23], a[212:215], v[88:91]\n" \
"s_add_u32 m0, s89, 0\n" \
"ds_read_b128 a[16:19], v205 offset:49152\n" \
"v_mfma_f32_16x16x32_bf16 v[96:99], a[24:27], a[208:211], v[96:99]\n" \
"s_xor_b32 s73, s73, 0x80\n" \
"ds_read_b128 a[20:23], v206 offset:49152\n" \
"v_mfma_f32_16x16x32_bf16 v[96:99], a[28:31], a[212:215], v[96:99]\n" \
"buffer_load_dwordx4 v196, s[44:47], s75 offen lds\n" \
"v_mfma_f32_16x16x32_bf16 v[100:103], a[24:27], a[216:219], v[100:103]\n" \
"s_xor_b32 s75, s75, 0x80\n" \
"s_mul_i32 s76, s80, 393216\n" \
"ds_read_b64_tr_b16 a[54:55], v195 offset:15360\n" \
"v_mfma_f32_16x16x32_bf16 v[100:103], a[28:31], a[220:223], v[100:103]\n" \
"s_add_u32 s76, s76, s78\n" \
"ds_read_b128 a[24:27], v205 offset:53248\n" \
"v_mfma_f32_16x16x32_bf16 v[108:111], a[192:195], a[216:219], v[108:111]\n" \
"s_add_u32 m0, s91, 20480\n" \
"ds_read_b128 a[28:31], v206 offset:53248\n" \
"v_mfma_f32_16x16x32_bf16 v[108:111], a[196:199], a[220:223], v[108:111]\n" \
"buffer_load_dwordx4 v197, s[48:51], s76 offen offset:0 lds\n" \
"v_mfma_f32_16x16x32_bf16 v[104:107], a[192:195], a[208:211], v[104:107]\n" \
"v_mfma_f32_16x16x32_bf16 v[104:107], a[196:199], a[212:215], v[104:107]\n" \
"s_waitcnt lgkmcnt(2)\n" \
"v_mfma_f32_16x16x32_bf16 v[32:35], a[32:35], a[0:3], v[32:35]\n" \
"s_add_u32 s77, s76, 24576\n" \
"ds_read_b128 a[192:195], v205 offset:57344\n" \
"v_mfma_f32_16x16x32_bf16 v[32:35], a[36:39], a[4:7], v[32:35]\n" \
"buffer_load_dwordx4 v198, s[48:51], s76 offen offset:1024 lds\n" \
"v_mfma_f32_16x16x32_bf16 v[40:43], a[32:35], a[8:11], v[40:43]\n" \
"ds_read_b128 a[196:199], v206 offset:57344\n" \
"v_mfma_f32_16x16x32_bf16 v[40:43], a[36:39], a[12:15], v[40:43]\n" \
"ds_read_b64_tr_b16 a[200:201], v195 offset:1024\n" \
"v_mfma_f32_16x16x32_bf16 v[0:3], a[40:43], a[8:11], v[0:3]\n" \
"s_xor_b32 s80, s80, 0x1\n" \
"ds_read_b64_tr_b16 a[202:203], v195 offset:4096\n" \
"v_mfma_f32_16x16x32_bf16 v[0:3], a[44:47], a[12:15], v[0:3]\n" \
"buffer_load_dwordx4 v199, s[48:51], s76 offen offset:2048 lds\n" \
"v_mfma_f32_16x16x32_bf16 v[4:7], a[48:51], a[8:11], v[4:7]\n" \
"ds_read_b64_tr_b16 a[204:205], v195 offset:13312\n" \
"v_mfma_f32_16x16x32_bf16 v[4:7], a[52:55], a[12:15], v[4:7]\n" \
"ds_read_b64_tr_b16 a[206:207], v195 offset:16384\n" \
"v_mfma_f32_16x16x32_bf16 v[12:15], a[48:51], a[16:19], v[12:15]\n" \
"s_add_u32 m0, m0, 3072\n" \
"ds_read_b64_tr_b16 a[208:209], v194 offset:2048\n" \
"v_mfma_f32_16x16x32_bf16 v[12:15], a[52:55], a[20:23], v[12:15]\n" \
"buffer_load_dwordx4 v197, s[48:51], s77 offen offset:0 lds\n" \
"v_mfma_f32_16x16x32_bf16 v[8:11], a[40:43], a[16:19], v[8:11]\n" \
"ds_read_b64_tr_b16 a[210:211], v194 offset:5120\n" \
"v_mfma_f32_16x16x32_bf16 v[8:11], a[44:47], a[20:23], v[8:11]\n" \
"ds_read_b64_tr_b16 a[212:213], v194 offset:14336\n" \
"v_mfma_f32_16x16x32_bf16 v[48:51], a[32:35], a[16:19], v[48:51]\n" \
"s_waitcnt lgkmcnt(3)\n" \
"ds_read_b64_tr_b16 a[214:215], v194 offset:17408\n" \
"v_mfma_f32_16x16x32_bf16 v[48:51], a[36:39], a[20:23], v[48:51]\n" \
"buffer_load_dwordx4 v198, s[48:51], s77 offen offset:1024 lds\n" \
"v_mfma_f32_16x16x32_bf16 v[56:59], a[32:35], a[24:27], v[56:59]\n" \
"ds_read_b64_tr_b16 a[216:217], v195 offset:2048\n" \
"v_mfma_f32_16x16x32_bf16 v[56:59], a[36:39], a[28:31], v[56:59]\n" \
"ds_read_b64_tr_b16 a[218:219], v195 offset:5120\n" \
"v_mfma_f32_16x16x32_bf16 v[16:19], a[40:43], a[24:27], v[16:19]\n" \
"ds_read_b64_tr_b16 a[220:221], v195 offset:14336\n" \
"v_mfma_f32_16x16x32_bf16 v[16:19], a[44:47], a[28:31], v[16:19]\n" \
"buffer_load_dwordx4 v199, s[48:51], s77 offen offset:2048 lds\n" \
"v_mfma_f32_16x16x32_bf16 v[20:23], a[48:51], a[24:27], v[20:23]\n" \
"ds_read_b64_tr_b16 a[222:223], v195 offset:17408\n" \
"v_mfma_f32_16x16x32_bf16 v[20:23], a[52:55], a[28:31], v[20:23]\n" \
"v_mfma_f32_16x16x32_bf16 v[28:31], a[48:51], a[192:195], v[28:31]\n" \
"v_mfma_f32_16x16x32_bf16 v[28:31], a[52:55], a[196:199], v[28:31]\n" \
"buffer_load_dword v220, v221, s[56:59], s66 offen sc1\n" \
"v_mfma_f32_16x16x32_bf16 v[24:27], a[40:43], a[192:195], v[24:27]\n" \
"v_mfma_f32_16x16x32_bf16 v[24:27], a[44:47], a[196:199], v[24:27]\n" \
"v_mfma_f32_16x16x32_bf16 v[64:67], a[32:35], a[192:195], v[64:67]\n" \
"v_mfma_f32_16x16x32_bf16 v[64:67], a[36:39], a[196:199], v[64:67]\n" \
"s_waitcnt vmcnt(12) lgkmcnt(0)\n" \
"s_barrier\n" \
"v_mfma_f32_16x16x32_bf16 v[68:71], a[200:203], a[192:195], v[68:71]\n" \
"ds_read_b64_tr_b16 a[32:33], v209 offset:1024\n" \
"ds_read_b64_tr_b16 a[34:35], v209 offset:4096\n" \
"v_mfma_f32_16x16x32_bf16 v[68:71], a[204:207], a[196:199], v[68:71]\n" \
"ds_read_b64_tr_b16 a[36:37], v209 offset:13312\n" \
"ds_read_b64_tr_b16 a[38:39], v209 offset:16384\n" \
"v_mfma_f32_16x16x32_bf16 v[60:63], a[200:203], a[24:27], v[60:63]\n" \
"s_add_u32 m0, s79, 110592\n" \
"ds_read_b64_tr_b16 a[40:41], v209 offset:0\n" \
"v_mfma_f32_16x16x32_bf16 v[60:63], a[204:207], a[28:31], v[60:63]\n" \
"buffer_load_dwordx4 v192, s[44:47], s73 offen offset:0 lds\n" \
"v_mfma_f32_16x16x32_bf16 v[52:55], a[200:203], a[16:19], v[52:55]\n" \
"ds_read_b64_tr_b16 a[42:43], v209 offset:3072\n" \
"v_mfma_f32_16x16x32_bf16 v[52:55], a[204:207], a[20:23], v[52:55]\n" \
"ds_read_b64_tr_b16 a[44:45], v209 offset:12288\n" \
"v_mfma_f32_16x16x32_bf16 v[44:47], a[200:203], a[8:11], v[44:47]\n" \
"ds_read_b64_tr_b16 a[46:47], v209 offset:15360\n" \
"v_mfma_f32_16x16x32_bf16 v[44:47], a[204:207], a[12:15], v[44:47]\n" \
"buffer_load_dwordx4 v193, s[44:47], s73 offen offset:1024 lds\n" \
"v_mfma_f32_16x16x32_bf16 v[72:75], a[0:3], a[208:211], v[72:75]\n" \
"ds_read_b64_tr_b16 a[48:49], v210 offset:0\n" \
"v_mfma_f32_16x16x32_bf16 v[72:75], a[4:7], a[212:215], v[72:75]\n" \
"ds_read_b128 a[0:3], v207 offset:0\n" \
"v_mfma_f32_16x16x32_bf16 v[80:83], a[8:11], a[208:211], v[80:83]\n" \
"ds_read_b128 a[4:7], v208 offset:0\n" \
"v_mfma_f32_16x16x32_bf16 v[80:83], a[12:15], a[212:215], v[80:83]\n" \
"buffer_load_dwordx4 v192, s[44:47], s73 offen offset:2048 lds\n" \
"v_mfma_f32_16x16x32_bf16 v[84:87], a[8:11], a[216:219], v[84:87]\n" \
"ds_read_b64_tr_b16 a[50:51], v210 offset:3072\n" \
"v_mfma_f32_16x16x32_bf16 v[84:87], a[12:15], a[220:223], v[84:87]\n" \
"ds_read_b128 a[8:11], v205 offset:0\n" \
"v_mfma_f32_16x16x32_bf16 v[92:95], a[16:19], a[216:219], v[92:95]\n" \
"ds_read_b128 a[12:15], v206 offset:0\n" \
"v_mfma_f32_16x16x32_bf16 v[92:95], a[20:23], a[220:223], v[92:95]\n" \
"buffer_load_dwordx4 v193, s[44:47], s73 offen offset:3072 lds\n" \
"v_mfma_f32_16x16x32_bf16 v[88:91], a[16:19], a[208:211], v[88:91]\n" \
"ds_read_b64_tr_b16 a[52:53], v210 offset:12288\n" \
"v_mfma_f32_16x16x32_bf16 v[88:91], a[20:23], a[212:215], v[88:91]\n" \
"s_add_u32 m0, s89, 110592\n" \
"ds_read_b128 a[16:19], v205 offset:4096\n" \
"v_mfma_f32_16x16x32_bf16 v[96:99], a[24:27], a[208:211], v[96:99]\n" \
"s_xor_b32 s73, s73, 0x80\n" \
"ds_read_b128 a[20:23], v206 offset:4096\n" \
"v_mfma_f32_16x16x32_bf16 v[96:99], a[28:31], a[212:215], v[96:99]\n" \
"buffer_load_dwordx4 v196, s[44:47], s75 offen lds\n" \
"v_mfma_f32_16x16x32_bf16 v[100:103], a[24:27], a[216:219], v[100:103]\n" \
"s_xor_b32 s75, s75, 0x80\n" \
"s_mul_i32 s76, s80, 393216\n" \
"ds_read_b64_tr_b16 a[54:55], v210 offset:15360\n" \
"v_mfma_f32_16x16x32_bf16 v[100:103], a[28:31], a[220:223], v[100:103]\n" \
"s_add_u32 s76, s76, s78\n" \
"ds_read_b128 a[24:27], v205 offset:8192\n" \
"v_mfma_f32_16x16x32_bf16 v[108:111], a[192:195], a[216:219], v[108:111]\n" \
"s_add_u32 m0, s91, 139264\n" \
"ds_read_b128 a[28:31], v206 offset:8192\n" \
"v_mfma_f32_16x16x32_bf16 v[108:111], a[196:199], a[220:223], v[108:111]\n" \
"buffer_load_dwordx4 v197, s[48:51], s76 offen offset:0 lds\n" \
"v_mfma_f32_16x16x32_bf16 v[104:107], a[192:195], a[208:211], v[104:107]\n" \
"v_mfma_f32_16x16x32_bf16 v[104:107], a[196:199], a[212:215], v[104:107]\n" \
"s_waitcnt lgkmcnt(2)\n" \
"v_mfma_f32_16x16x32_bf16 v[32:35], a[32:35], a[0:3], v[32:35]\n" \
"s_add_u32 s77, s76, 24576\n" \
"ds_read_b128 a[192:195], v205 offset:12288\n" \
"v_mfma_f32_16x16x32_bf16 v[32:35], a[36:39], a[4:7], v[32:35]\n" \
"buffer_load_dwordx4 v198, s[48:51], s76 offen offset:1024 lds\n" \
"v_mfma_f32_16x16x32_bf16 v[40:43], a[32:35], a[8:11], v[40:43]\n" \
"ds_read_b128 a[196:199], v206 offset:12288\n" \
"v_mfma_f32_16x16x32_bf16 v[40:43], a[36:39], a[12:15], v[40:43]\n" \
"ds_read_b64_tr_b16 a[200:201], v210 offset:1024\n" \
"v_mfma_f32_16x16x32_bf16 v[0:3], a[40:43], a[8:11], v[0:3]\n" \
"s_xor_b32 s80, s80, 0x1\n" \
"ds_read_b64_tr_b16 a[202:203], v210 offset:4096\n" \
"v_mfma_f32_16x16x32_bf16 v[0:3], a[44:47], a[12:15], v[0:3]\n" \
"buffer_load_dwordx4 v199, s[48:51], s76 offen offset:2048 lds\n" \
"v_mfma_f32_16x16x32_bf16 v[4:7], a[48:51], a[8:11], v[4:7]\n" \
"ds_read_b64_tr_b16 a[204:205], v210 offset:13312\n" \
"v_mfma_f32_16x16x32_bf16 v[4:7], a[52:55], a[12:15], v[4:7]\n" \
"ds_read_b64_tr_b16 a[206:207], v210 offset:16384\n" \
"v_mfma_f32_16x16x32_bf16 v[12:15], a[48:51], a[16:19], v[12:15]\n" \
"s_add_u32 m0, m0, 3072\n" \
"ds_read_b64_tr_b16 a[208:209], v209 offset:2048\n" \
"v_mfma_f32_16x16x32_bf16 v[12:15], a[52:55], a[20:23], v[12:15]\n" \
"buffer_load_dwordx4 v197, s[48:51], s77 offen offset:0 lds\n" \
"v_mfma_f32_16x16x32_bf16 v[8:11], a[40:43], a[16:19], v[8:11]\n" \
"ds_read_b64_tr_b16 a[210:211], v209 offset:5120\n" \
"v_mfma_f32_16x16x32_bf16 v[8:11], a[44:47], a[20:23], v[8:11]\n" \
"ds_read_b64_tr_b16 a[212:213], v209 offset:14336\n" \
"v_mfma_f32_16x16x32_bf16 v[48:51], a[32:35], a[16:19], v[48:51]\n" \
"s_waitcnt lgkmcnt(3)\n" \
"ds_read_b64_tr_b16 a[214:215], v209 offset:17408\n" \
"v_mfma_f32_16x16x32_bf16 v[48:51], a[36:39], a[20:23], v[48:51]\n" \
"buffer_load_dwordx4 v198, s[48:51], s77 offen offset:1024 lds\n" \
"v_mfma_f32_16x16x32_bf16 v[56:59], a[32:35], a[24:27], v[56:59]\n" \
"ds_read_b64_tr_b16 a[216:217], v210 offset:2048\n" \
"v_mfma_f32_16x16x32_bf16 v[56:59], a[36:39], a[28:31], v[56:59]\n" \
"ds_read_b64_tr_b16 a[218:219], v210 offset:5120\n" \
"v_mfma_f32_16x16x32_bf16 v[16:19], a[40:43], a[24:27], v[16:19]\n" \
"ds_read_b64_tr_b16 a[220:221], v210 offset:14336\n" \
"v_mfma_f32_16x16x32_bf16 v[16:19], a[44:47], a[28:31], v[16:19]\n" \
"buffer_load_dwordx4 v199, s[48:51], s77 offen offset:2048 lds\n" \
"v_mfma_f32_16x16x32_bf16 v[20:23], a[48:51], a[24:27], v[20:23]\n" \
"s_mov_b32 s46, s81\n" \
"s_mov_b32 s47, s82\n" \
"ds_read_b64_tr_b16 a[222:223], v210 offset:17408\n" \
"v_mfma_f32_16x16x32_bf16 v[20:23], a[52:55], a[28:31], v[20:23]\n" \
"s_mov_b32 s48, 0\n" \
"s_mov_b32 s49, s68\n" \
"s_sub_u32 s50, 64, s9\n" \
"v_mfma_f32_16x16x32_bf16 v[28:31], a[48:51], a[192:195], v[28:31]\n" \
"s_cmp_eq_u32 s90, 0\n" \
"s_cselect_b32 s50, s9, s50\n" \
"s_andn2_b32 s50, s50, 3\n" \
"v_mfma_f32_16x16x32_bf16 v[28:31], a[52:55], a[196:199], v[28:31]\n" \
"s_lshl_b32 s50, s50, 12\n" \
"s_waitcnt vmcnt(11)\n" \
"v_cmp_eq_u32 vcc, s60, v220\n" \
"v_mfma_f32_16x16x32_bf16 v[24:27], a[40:43], a[192:195], v[24:27]\n" \
"v_mfma_f32_16x16x32_bf16 v[24:27], a[44:47], a[196:199], v[24:27]\n" \
"s_andn2_b32 s70, s65, vcc_lo\n" \
"s_cbranch_scc1 Lpl5_%=\n" \
"Lpr5_%=:\n" \
"v_mfma_f32_16x16x32_bf16 v[64:67], a[32:35], a[192:195], v[64:67]\n" \
"v_mfma_f32_16x16x32_bf16 v[64:67], a[36:39], a[196:199], v[64:67]\n" \
"s_xor_b32 s82, s48, s49\n" \
"s_cmp_lt_u32 s48, s50\n" \
"s_cselect_b32 s82, s82, s48\n" \
"s_waitcnt vmcnt(12) lgkmcnt(0)\n" \
"s_barrier\n" \
"v_mfma_f32_16x16x32_bf16 v[68:71], a[200:203], a[192:195], v[68:71]\n" \
"ds_read_b64_tr_b16 a[32:33], v204 offset:21504\n" \
"ds_read_b64_tr_b16 a[34:35], v204 offset:24576\n" \
"v_mfma_f32_16x16x32_bf16 v[68:71], a[204:207], a[196:199], v[68:71]\n" \
"s_add_u32 s81, s82, s46\n" \
"s_lshr_b32 s78, s61, 1\n" \
"s_lshl_b32 s80, s78, 9\n" \
"s_add_u32 s80, s80, 511\n" \
"ds_read_b64_tr_b16 a[36:37], v204 offset:33792\n" \
"ds_read_b64_tr_b16 a[38:39], v204 offset:36864\n" \
"v_mfma_f32_16x16x32_bf16 v[60:63], a[200:203], a[24:27], v[60:63]\n" \
"s_add_u32 m0, s86, s79\n" \
"ds_read_b64_tr_b16 a[40:41], v204 offset:20480\n" \
"v_sub_u32 v145, s80, v234\n" \
"v_mfma_f32_16x16x32_bf16 v[60:63], a[204:207], a[28:31], v[60:63]\n" \
"buffer_load_dwordx4 v234, s[56:59], s81 offen offset:0 sc1 lds\n" \
"v_mfma_f32_16x16x32_bf16 v[52:55], a[200:203], a[16:19], v[52:55]\n" \
"s_lshl_b32 s80, s78, 3\n" \
"s_not_b32 s80, s80\n" \
"ds_read_b64_tr_b16 a[42:43], v204 offset:23552\n" \
"v_mfma_f32_16x16x32_bf16 v[52:55], a[204:207], a[20:23], v[52:55]\n" \
"s_lshl_b32 s82, s82, 5\n" \
"ds_read_b64_tr_b16 a[44:45], v204 offset:32768\n" \
"v_sub_u32 v146, s80, v238\n" \
"v_mfma_f32_16x16x32_bf16 v[44:47], a[200:203], a[8:11], v[44:47]\n" \
"s_add_u32 s82, s82, s47\n" \
"ds_read_b64_tr_b16 a[46:47], v204 offset:35840\n" \
"v_subrev_u32 v152, 0, v238\n" \
"v_mfma_f32_16x16x32_bf16 v[44:47], a[204:207], a[12:15], v[44:47]\n" \
"buffer_load_dwordx4 v234, s[56:59], s81 offen offset:1024 sc1 lds\n" \
"v_mfma_f32_16x16x32_bf16 v[72:75], a[0:3], a[208:211], v[72:75]\n" \
"ds_read_b64_tr_b16 a[48:49], v222 offset:20480\n" \
"v_sub_u32 v144, 7, v238\n" \
"v_mfma_f32_16x16x32_bf16 v[72:75], a[4:7], a[212:215], v[72:75]\n" \
"ds_read_b128 a[0:3], v202 offset:0\n" \
"v_or_b32 v152, v152, v144\n" \
"v_mfma_f32_16x16x32_bf16 v[80:83], a[8:11], a[208:211], v[80:83]\n" \
"ds_read_b128 a[4:7], v203 offset:0\n" \
"v_ashrrev_i32 v152, 31, v152\n" \
"v_mfma_f32_16x16x32_bf16 v[80:83], a[12:15], a[212:215], v[80:83]\n" \
"buffer_load_dwordx4 v234, s[56:59], s81 offen offset:2048 sc1 lds\n" \
"v_mfma_f32_16x16x32_bf16 v[84:87], a[8:11], a[216:219], v[84:87]\n" \
"ds_read_b64_tr_b16 a[50:51], v222 offset:23552\n" \
"v_and_b32 v152, 0xff800000, v152\n" \
"v_mfma_f32_16x16x32_bf16 v[84:87], a[12:15], a[220:223], v[84:87]\n" \
"ds_read_b128 a[8:11], v200 offset:0\n" \
"v_sub_u32 v156, -9, v238\n" \
"v_mfma_f32_16x16x32_bf16 v[92:95], a[16:19], a[216:219], v[92:95]\n" \
"ds_read_b128 a[12:15], v201 offset:0\n" \
"v_ashrrev_i32 v156, 31, v156\n" \
"v_mfma_f32_16x16x32_bf16 v[92:95], a[20:23], a[220:223], v[92:95]\n" \
"buffer_load_dwordx4 v234, s[56:59], s81 offen offset:3072 sc1 lds\n" \
"v_mfma_f32_16x16x32_bf16 v[88:91], a[16:19], a[208:211], v[88:91]\n" \
"ds_read_b64_tr_b16 a[52:53], v222 offset:32768\n" \
"v_and_b32 v156, 0xff800000, v156\n" \
"v_mfma_f32_16x16x32_bf16 v[88:91], a[20:23], a[212:215], v[88:91]\n" \
"ds_read_b128 a[16:19], v200 offset:4096\n" \
"v_add_u32 v148, 0, v146\n" \
"v_mfma_f32_16x16x32_bf16 v[96:99], a[24:27], a[208:211], v[96:99]\n" \
"s_add_u32 m0, m0, 0x4000\n" \
"ds_read_b128 a[20:23], v201 offset:4096\n" \
"v_or3_b32 v148, v148, v145, s92\n" \
"v_mfma_f32_16x16x32_bf16 v[96:99], a[28:31], a[212:215], v[96:99]\n" \
"buffer_load_dwordx4 v216, s[36:39], s82 offen offset:0 lds\n" \
"v_mfma_f32_16x16x32_bf16 v[100:103], a[24:27], a[216:219], v[100:103]\n" \
"ds_read_b64_tr_b16 a[54:55], v222 offset:35840\n" \
"v_ashrrev_i32 v148, 31, v148\n" \
"v_mfma_f32_16x16x32_bf16 v[100:103], a[28:31], a[220:223], v[100:103]\n" \
"ds_read_b128 a[24:27], v200 offset:8192\n" \
"v_and_b32 v148, 0xff800000, v148\n" \
"v_mfma_f32_16x16x32_bf16 v[108:111], a[192:195], a[216:219], v[108:111]\n" \
"s_add_u32 s48, s48, 0x1000\n" \
"ds_read_b128 a[28:31], v201 offset:8192\n" \
"v_subrev_u32 v153, 1, v238\n" \
"v_mfma_f32_16x16x32_bf16 v[108:111], a[196:199], a[220:223], v[108:111]\n" \
"buffer_load_dwordx4 v217, s[36:39], s82 offen offset:1024 lds\n" \
"v_mfma_f32_16x16x32_bf16 v[104:107], a[192:195], a[208:211], v[104:107]\n" \
"v_sub_u32 v144, 8, v238\n" \
"v_or_b32 v153, v153, v144\n" \
"v_mfma_f32_16x16x32_bf16 v[104:107], a[196:199], a[212:215], v[104:107]\n" \
"s_waitcnt lgkmcnt(2)\n" \
"v_ashrrev_i32 v153, 31, v153\n" \
"v_and_b32 v153, 0xff800000, v153\n" \
"v_mfma_f32_16x16x32_bf16 v[32:35], a[32:35], a[0:3], v[32:35]\n" \
"s_sub_u32 s86, s86, 32768\n" \
"s_cselect_b32 s86, 131072, s86\n" \
"ds_read_b128 a[192:195], v200 offset:12288\n" \
"v_mfma_f32_16x16x32_bf16 v[32:35], a[36:39], a[4:7], v[32:35]\n" \
"buffer_load_dwordx4 v218, s[36:39], s82 offen offset:2048 lds\n" \
"v_mfma_f32_16x16x32_bf16 v[40:43], a[32:35], a[8:11], v[40:43]\n" \
"ds_read_b128 a[196:199], v201 offset:12288\n" \
"v_sub_u32 v157, -8, v238\n" \
"v_mfma_f32_16x16x32_bf16 v[40:43], a[36:39], a[12:15], v[40:43]\n" \
"ds_read_b64_tr_b16 a[200:201], v222 offset:21504\n" \
"v_ashrrev_i32 v157, 31, v157\n" \
"v_mfma_f32_16x16x32_bf16 v[0:3], a[40:43], a[8:11], v[0:3]\n" \
"ds_read_b64_tr_b16 a[202:203], v222 offset:24576\n" \
"v_and_b32 v157, 0xff800000, v157\n" \
"v_mfma_f32_16x16x32_bf16 v[0:3], a[44:47], a[12:15], v[0:3]\n" \
"buffer_load_dwordx4 v219, s[36:39], s82 offen offset:3072 lds\n" \
"v_mfma_f32_16x16x32_bf16 v[4:7], a[48:51], a[8:11], v[4:7]\n" \
"ds_read_b64_tr_b16 a[204:205], v222 offset:33792\n" \
"v_add_u32 v149, 1, v146\n" \
"v_mfma_f32_16x16x32_bf16 v[4:7], a[52:55], a[12:15], v[4:7]\n" \
"ds_read_b64_tr_b16 a[206:207], v222 offset:36864\n" \
"v_or3_b32 v149, v149, v145, s92\n" \
"v_mfma_f32_16x16x32_bf16 v[12:15], a[48:51], a[16:19], v[12:15]\n" \
"ds_read_b64_tr_b16 a[208:209], v204 offset:22528\n" \
"v_ashrrev_i32 v149, 31, v149\n" \
"v_mfma_f32_16x16x32_bf16 v[12:15], a[52:55], a[20:23], v[12:15]\n" \
"v_and_b32 v149, 0xff800000, v149\n" \
"v_subrev_u32 v154, 2, v238\n" \
"v_mfma_f32_16x16x32_bf16 v[8:11], a[40:43], a[16:19], v[8:11]\n" \
"ds_read_b64_tr_b16 a[210:211], v204 offset:25600\n" \
"v_sub_u32 v144, 9, v238\n" \
"v_mfma_f32_16x16x32_bf16 v[8:11], a[44:47], a[20:23], v[8:11]\n" \
"ds_read_b64_tr_b16 a[212:213], v204 offset:34816\n" \
"v_or_b32 v154, v154, v144\n" \
"v_mfma_f32_16x16x32_bf16 v[48:51], a[32:35], a[16:19], v[48:51]\n" \
"ds_read_b64_tr_b16 a[214:215], v204 offset:37888\n" \
"v_ashrrev_i32 v154, 31, v154\n" \
"v_mfma_f32_16x16x32_bf16 v[48:51], a[36:39], a[20:23], v[48:51]\n" \
"s_waitcnt lgkmcnt(3)\n" \
"v_and_b32 v154, 0xff800000, v154\n" \
"v_sub_u32 v158, -7, v238\n" \
"v_mfma_f32_16x16x32_bf16 v[56:59], a[32:35], a[24:27], v[56:59]\n" \
"ds_read_b64_tr_b16 a[216:217], v222 offset:22528\n" \
"v_ashrrev_i32 v158, 31, v158\n" \
"v_mfma_f32_16x16x32_bf16 v[56:59], a[36:39], a[28:31], v[56:59]\n" \
"ds_read_b64_tr_b16 a[218:219], v222 offset:25600\n" \
"v_and_b32 v158, 0xff800000, v158\n" \
"v_mfma_f32_16x16x32_bf16 v[16:19], a[40:43], a[24:27], v[16:19]\n" \
"ds_read_b64_tr_b16 a[220:221], v222 offset:34816\n" \
"v_add_u32 v150, 2, v146\n" \
"v_mfma_f32_16x16x32_bf16 v[16:19], a[44:47], a[28:31], v[16:19]\n" \
"v_or3_b32 v150, v150, v145, s92\n" \
"v_ashrrev_i32 v150, 31, v150\n" \
"v_mfma_f32_16x16x32_bf16 v[20:23], a[48:51], a[24:27], v[20:23]\n" \
"ds_read_b64_tr_b16 a[222:223], v222 offset:37888\n" \
"v_and_b32 v150, 0xff800000, v150\n" \
"v_mfma_f32_16x16x32_bf16 v[20:23], a[52:55], a[28:31], v[20:23]\n" \
"v_subrev_u32 v155, 3, v238\n" \
"v_sub_u32 v144, 10, v238\n" \
"v_mfma_f32_16x16x32_bf16 v[28:31], a[48:51], a[192:195], v[28:31]\n" \
"v_or_b32 v155, v155, v144\n" \
"v_ashrrev_i32 v155, 31, v155\n" \
"v_mfma_f32_16x16x32_bf16 v[28:31], a[52:55], a[196:199], v[28:31]\n" \
"v_and_b32 v155, 0xff800000, v155\n" \
"v_sub_u32 v159, -6, v238\n" \
"v_mfma_f32_16x16x32_bf16 v[24:27], a[40:43], a[192:195], v[24:27]\n" \
"v_ashrrev_i32 v159, 31, v159\n" \
"v_and_b32 v159, 0xff800000, v159\n" \
"v_mfma_f32_16x16x32_bf16 v[24:27], a[44:47], a[196:199], v[24:27]\n" \
"v_add_u32 v151, 3, v146\n" \
"v_or3_b32 v151, v151, v145, s92\n" \
"v_mfma_f32_16x16x32_bf16 v[64:67], a[32:35], a[192:195], v[64:67]\n" \
"v_ashrrev_i32 v151, 31, v151\n" \
"v_and_b32 v151, 0xff800000, v151\n" \
"v_mfma_f32_16x16x32_bf16 v[64:67], a[36:39], a[196:199], v[64:67]\n" \
"s_xor_b32 s82, s48, s49\n" \
"s_cmp_lt_u32 s48, s50\n" \
"s_cselect_b32 s82, s82, s48\n" \
"s_waitcnt vmcnt(8) lgkmcnt(0)\n" \
"s_barrier\n" \
"v_mfma_f32_16x16x32_bf16 v[68:71], a[200:203], a[192:195], v[68:71]\n" \
"ds_read_b64_tr_b16 a[32:33], v194 offset:1024\n" \
"ds_read_b64_tr_b16 a[34:35], v194 offset:4096\n" \
"v_mfma_f32_16x16x32_bf16 v[68:71], a[204:207], a[196:199], v[68:71]\n" \
"s_add_u32 s81, s82, s46\n" \
"ds_read_b64_tr_b16 a[36:37], v194 offset:13312\n" \
"ds_read_b64_tr_b16 a[38:39], v194 offset:16384\n" \
"v_mfma_f32_16x16x32_bf16 v[60:63], a[200:203], a[24:27], v[60:63]\n" \
"s_add_u32 m0, s86, s79\n" \
"ds_read_b64_tr_b16 a[40:41], v194 offset:0\n" \
"v_mov_b32 v186, 0xffff0000\n" \
"v_mfma_f32_16x16x32_bf16 v[60:63], a[204:207], a[28:31], v[60:63]\n" \
"buffer_load_dwordx4 v234, s[56:59], s81 offen offset:0 sc1 lds\n" \
"v_mfma_f32_16x16x32_bf16 v[52:55], a[200:203], a[16:19], v[52:55]\n" \
"ds_read_b64_tr_b16 a[42:43], v194 offset:3072\n" \
"v_subrev_u32 v144, 0, v238\n" \
"v_mfma_f32_16x16x32_bf16 v[52:55], a[204:207], a[20:23], v[52:55]\n" \
"s_lshl_b32 s82, s82, 5\n" \
"ds_read_b64_tr_b16 a[44:45], v194 offset:12288\n" \
"v_subrev_u32 v184, 1, v238\n" \
"v_mfma_f32_16x16x32_bf16 v[44:47], a[200:203], a[8:11], v[44:47]\n" \
"s_add_u32 s82, s82, s47\n" \
"ds_read_b64_tr_b16 a[46:47], v194 offset:15360\n" \
"v_lshrrev_b32 v144, 16, v144\n" \
"v_mfma_f32_16x16x32_bf16 v[44:47], a[204:207], a[12:15], v[44:47]\n" \
"buffer_load_dwordx4 v234, s[56:59], s81 offen offset:1024 sc1 lds\n" \
"v_mfma_f32_16x16x32_bf16 v[72:75], a[0:3], a[208:211], v[72:75]\n" \
"ds_read_b64_tr_b16 a[48:49], v195 offset:0\n" \
"v_and_or_b32 v184, v184, v186, v144\n" \
"v_mfma_f32_16x16x32_bf16 v[72:75], a[4:7], a[212:215], v[72:75]\n" \
"ds_read_b128 a[0:3], v207 offset:45056\n" \
"v_subrev_u32 v144, 2, v238\n" \
"v_mfma_f32_16x16x32_bf16 v[80:83], a[8:11], a[208:211], v[80:83]\n" \
"ds_read_b128 a[4:7], v208 offset:45056\n" \
"v_subrev_u32 v185, 3, v238\n" \
"v_mfma_f32_16x16x32_bf16 v[80:83], a[12:15], a[212:215], v[80:83]\n" \
"buffer_load_dwordx4 v234, s[56:59], s81 offen offset:2048 sc1 lds\n" \
"v_mfma_f32_16x16x32_bf16 v[84:87], a[8:11], a[216:219], v[84:87]\n" \
"ds_read_b64_tr_b16 a[50:51], v195 offset:3072\n" \
"v_lshrrev_b32 v144, 16, v144\n" \
"v_mfma_f32_16x16x32_bf16 v[84:87], a[12:15], a[220:223], v[84:87]\n" \
"ds_read_b128 a[8:11], v205 offset:45056\n" \
"v_and_or_b32 v185, v185, v186, v144\n" \
"v_mfma_f32_16x16x32_bf16 v[92:95], a[16:19], a[216:219], v[92:95]\n" \
"ds_read_b128 a[12:15], v206 offset:45056\n" \
"v_mov_b32 v144, 0x3e8293ee\n" \
"v_mfma_f32_16x16x32_bf16 v[92:95], a[20:23], a[220:223], v[92:95]\n" \
"buffer_load_dwordx4 v234, s[56:59], s81 offen offset:3072 sc1 lds\n" \
"v_mfma_f32_16x16x32_bf16 v[88:91], a[16:19], a[208:211], v[88:91]\n" \
"s_add_u32 s80, s79, 117760\n" \
"ds_read_b64_tr_b16 a[52:53], v195 offset:12288\n" \
"v_add_u32 v223, s80, v234\n" \
"v_mfma_f32_16x16x32_bf16 v[88:91], a[20:23], a[212:215], v[88:91]\n" \
"s_cmp_lg_u32 s78, 0\n" \
"s_cselect_b64 s[76:77], -1, 0\n" \
"ds_read_b128 a[16:19], v205 offset:49152\n" \
"v_mfma_f32_16x16x32_bf16 v[96:99], a[24:27], a[208:211], v[96:99]\n" \
"s_add_u32 m0, m0, 0x4000\n" \
"ds_read_b128 a[20:23], v206 offset:49152\n" \
"v_mfma_f32_16x16x32_bf16 v[96:99], a[28:31], a[212:215], v[96:99]\n" \
"buffer_load_dwordx4 v216, s[36:39], s82 offen offset:0 lds\n" \
"v_mfma_f32_16x16x32_bf16 v[100:103], a[24:27], a[216:219], v[100:103]\n" \
"ds_read_b64_tr_b16 a[54:55], v195 offset:15360\n" \
"v_mfma_f32_16x16x32_bf16 v[100:103], a[28:31], a[220:223], v[100:103]\n" \
"ds_read_b128 a[24:27], v205 offset:53248\n" \
"v_mfma_f32_16x16x32_bf16 v[108:111], a[192:195], a[216:219], v[108:111]\n" \
"s_add_u32 s48, s48, 0x1000\n" \
"ds_read_b128 a[28:31], v206 offset:53248\n" \
"v_mfma_f32_16x16x32_bf16 v[108:111], a[196:199], a[220:223], v[108:111]\n" \
"buffer_load_dwordx4 v217, s[36:39], s82 offen offset:1024 lds\n" \
"v_mfma_f32_16x16x32_bf16 v[104:107], a[192:195], a[208:211], v[104:107]\n" \
"v_mfma_f32_16x16x32_bf16 v[104:107], a[196:199], a[212:215], v[104:107]\n" \
"s_waitcnt lgkmcnt(2)\n" \
"v_mfma_f32_16x16x32_bf16 v[32:35], a[32:35], a[0:3], v[32:35]\n" \
"s_sub_u32 s86, s86, 32768\n" \
"s_cselect_b32 s86, 131072, s86\n" \
"ds_read_b128 a[192:195], v205 offset:57344\n" \
"v_mfma_f32_16x16x32_bf16 v[32:35], a[36:39], a[4:7], v[32:35]\n" \
"buffer_load_dwordx4 v218, s[36:39], s82 offen offset:2048 lds\n" \
"v_mfma_f32_16x16x32_bf16 v[40:43], a[32:35], a[8:11], v[40:43]\n" \
"ds_read_b128 a[196:199], v206 offset:57344\n" \
"v_mfma_f32_16x16x32_bf16 v[40:43], a[36:39], a[12:15], v[40:43]\n" \
"ds_read_b64_tr_b16 a[200:201], v195 offset:1024\n" \
"v_mfma_f32_16x16x32_bf16 v[0:3], a[40:43], a[8:11], v[0:3]\n" \
"ds_read_b64_tr_b16 a[202:203], v195 offset:4096\n" \
"v_mfma_f32_16x16x32_bf16 v[0:3], a[44:47], a[12:15], v[0:3]\n" \
"buffer_load_dwordx4 v219, s[36:39], s82 offen offset:3072 lds\n" \
"v_mfma_f32_16x16x32_bf16 v[4:7], a[48:51], a[8:11], v[4:7]\n" \
"s_xor_b32 s82, s48, s49\n" \
"s_cmp_lt_u32 s48, s50\n" \
"s_cselect_b32 s82, s82, s48\n" \
"ds_read_b64_tr_b16 a[204:205], v195 offset:13312\n" \
"v_mfma_f32_16x16x32_bf16 v[4:7], a[52:55], a[12:15], v[4:7]\n" \
"s_add_u32 s81, s82, s46\n" \
"ds_read_b64_tr_b16 a[206:207], v195 offset:16384\n" \
"v_mfma_f32_16x16x32_bf16 v[12:15], a[48:51], a[16:19], v[12:15]\n" \
"s_add_u32 m0, s86, s79\n" \
"ds_read_b64_tr_b16 a[208:209], v194 offset:2048\n" \
"v_mfma_f32_16x16x32_bf16 v[12:15], a[52:55], a[20:23], v[12:15]\n" \
"buffer_load_dwordx4 v234, s[56:59], s81 offen offset:0 sc1 lds\n" \
"v_mfma_f32_16x16x32_bf16 v[8:11], a[40:43], a[16:19], v[8:11]\n" \
"ds_read_b64_tr_b16 a[210:211], v194 offset:5120\n" \
"v_mfma_f32_16x16x32_bf16 v[8:11], a[44:47], a[20:23], v[8:11]\n" \
"s_lshl_b32 s82, s82, 5\n" \
"ds_read_b64_tr_b16 a[212:213], v194 offset:14336\n" \
"v_mfma_f32_16x16x32_bf16 v[48:51], a[32:35], a[16:19], v[48:51]\n" \
"s_waitcnt lgkmcnt(3)\n" \
"s_add_u32 s82, s82, s47\n" \
"ds_read_b64_tr_b16 a[214:215], v194 offset:17408\n" \
"v_mfma_f32_16x16x32_bf16 v[48:51], a[36:39], a[20:23], v[48:51]\n" \
"buffer_load_dwordx4 v234, s[56:59], s81 offen offset:1024 sc1 lds\n" \
"v_mfma_f32_16x16x32_bf16 v[56:59], a[32:35], a[24:27], v[56:59]\n" \
"ds_read_b64_tr_b16 a[216:217], v195 offset:2048\n" \
"v_mfma_f32_16x16x32_bf16 v[56:59], a[36:39], a[28:31], v[56:59]\n" \
"ds_read_b64_tr_b16 a[218:219], v195 offset:5120\n" \
"v_mfma_f32_16x16x32_bf16 v[16:19], a[40:43], a[24:27], v[16:19]\n" \
"ds_read_b64_tr_b16 a[220:221], v195 offset:14336\n" \
"v_mfma_f32_16x16x32_bf16 v[16:19], a[44:47], a[28:31], v[16:19]\n" \
"buffer_load_dwordx4 v234, s[56:59], s81 offen offset:2048 sc1 lds\n" \
"v_mfma_f32_16x16x32_bf16 v[20:23], a[48:51], a[24:27], v[20:23]\n" \
"ds_read_b64_tr_b16 a[222:223], v195 offset:17408\n" \
"v_mfma_f32_16x16x32_bf16 v[20:23], a[52:55], a[28:31], v[20:23]\n" \
"v_mfma_f32_16x16x32_bf16 v[28:31], a[48:51], a[192:195], v[28:31]\n" \
"v_mfma_f32_16x16x32_bf16 v[28:31], a[52:55], a[196:199], v[28:31]\n" \
"buffer_load_dwordx4 v234, s[56:59], s81 offen offset:3072 sc1 lds\n" \
"v_mfma_f32_16x16x32_bf16 v[24:27], a[40:43], a[192:195], v[24:27]\n" \
"v_mfma_f32_16x16x32_bf16 v[24:27], a[44:47], a[196:199], v[24:27]\n" \
"v_mfma_f32_16x16x32_bf16 v[64:67], a[32:35], a[192:195], v[64:67]\n" \
"s_add_u32 m0, m0, 0x4000\n" \
"v_mfma_f32_16x16x32_bf16 v[64:67], a[36:39], a[196:199], v[64:67]\n" \
"buffer_load_dwordx4 v216, s[36:39], s82 offen offset:0 lds\n" \
"s_waitcnt vmcnt(13) lgkmcnt(0)\n" \
"s_barrier\n" \
"v_mfma_f32_16x16x32_bf16 v[44:47], a[200:203], a[8:11], v[44:47]\n" \
"ds_read_b64_tr_b16 a[96:97], v214 offset:16384\n" \
"v_mfma_f32_16x16x32_bf16 v[44:47], a[204:207], a[12:15], v[44:47]\n" \
"ds_read_b64_tr_b16 a[98:99], v214 offset:18432\n" \
"v_mfma_f32_16x16x32_bf16 v[52:55], a[200:203], a[16:19], v[52:55]\n" \
"s_add_u32 s48, s48, 0x1000\n" \
"ds_read_b64_tr_b16 a[100:101], v214 offset:24576\n" \
"v_mfma_f32_16x16x32_bf16 v[52:55], a[204:207], a[20:23], v[52:55]\n" \
"buffer_load_dwordx4 v217, s[36:39], s82 offen offset:1024 lds\n" \
"v_mfma_f32_16x16x32_bf16 v[60:63], a[200:203], a[24:27], v[60:63]\n" \
"ds_read_b64_tr_b16 a[102:103], v214 offset:26624\n" \
"v_cvt_pk_bf16_f32 v112, v0, v1\n" \
"v_mfma_f32_16x16x32_bf16 v[60:63], a[204:207], a[28:31], v[60:63]\n" \
"ds_read_b128 a[64:67], v213 offset:0\n" \
"v_cvt_pk_bf16_f32 v113, v2, v3\n" \
"Lpg9609_%=:\n" \
"Lst1x3_%=:\n" \
"s_cbranch_execz Lpg11485_%=\n" \
"v_mfma_f32_16x16x32_bf16 v[68:71], a[200:203], a[192:195], v[68:71]\n" \
"s_sub_u32 s86, s86, 32768\n" \
"s_cselect_b32 s86, 131072, s86\n" \
"ds_read_b128 a[68:71], v213 offset:2048\n" \
"v_mfma_f32_16x16x32_bf16 v[68:71], a[204:207], a[196:199], v[68:71]\n" \
"buffer_load_dwordx4 v218, s[36:39], s82 offen offset:2048 lds\n" \
"v_mfma_f32_16x16x32_bf16 v[72:75], a[0:3], a[208:211], v[72:75]\n" \
"ds_read_b64_tr_b16 a[104:105], v215 offset:16384\n" \
"v_cvt_pk_bf16_f32 v114, v4, v5\n" \
"v_mfma_f32_16x16x32_bf16 v[72:75], a[4:7], a[212:215], v[72:75]\n" \
"ds_read_b64_tr_b16 a[106:107], v215 offset:18432\n" \
"v_cvt_pk_bf16_f32 v115, v6, v7\n" \
"v_mfma_f32_16x16x32_bf16 v[80:83], a[8:11], a[208:211], v[80:83]\n" \
"ds_read_b64_tr_b16 a[108:109], v215 offset:24576\n" \
"v_cvt_pk_bf16_f32 v116, v8, v9\n" \
"v_mfma_f32_16x16x32_bf16 v[80:83], a[12:15], a[212:215], v[80:83]\n" \
"buffer_load_dwordx4 v219, s[36:39], s82 offen offset:3072 lds\n" \
"v_mfma_f32_16x16x32_bf16 v[84:87], a[8:11], a[216:219], v[84:87]\n" \
"s_xor_b32 s82, s48, s49\n" \
"s_cmp_lt_u32 s48, s50\n" \
"s_cselect_b32 s82, s82, s48\n" \
"ds_read_b64_tr_b16 a[110:111], v215 offset:26624\n" \
"v_mfma_f32_16x16x32_bf16 v[84:87], a[12:15], a[220:223], v[84:87]\n" \
"s_add_u32 s81, s82, s46\n" \
"ds_read_b128 a[72:75], v213 offset:256\n" \
"v_cvt_pk_bf16_f32 v117, v10, v11\n" \
"v_mfma_f32_16x16x32_bf16 v[92:95], a[16:19], a[216:219], v[92:95]\n" \
"s_add_u32 m0, s86, s79\n" \
"ds_read_b128 a[76:79], v213 offset:2304\n" \
"v_cvt_pk_bf16_f32 v118, v12, v13\n" \
"v_mfma_f32_16x16x32_bf16 v[92:95], a[20:23], a[220:223], v[92:95]\n" \
"buffer_load_dwordx4 v234, s[56:59], s81 offen offset:0 sc1 lds\n" \
"v_mfma_f32_16x16x32_bf16 v[88:91], a[16:19], a[208:211], v[88:91]\n" \
"ds_read_b128 a[80:83], v213 offset:4096\n" \
"v_cvt_pk_bf16_f32 v119, v14, v15\n" \
"v_mfma_f32_16x16x32_bf16 v[88:91], a[20:23], a[212:215], v[88:91]\n" \
"s_lshl_b32 s82, s82, 5\n" \
"ds_read_b128 a[84:87], v213 offset:6144\n" \
"v_cvt_pk_bf16_f32 v120, v16, v17\n" \
"v_mfma_f32_16x16x32_bf16 v[96:99], a[24:27], a[208:211], v[96:99]\n" \
"s_add_u32 s82, s82, s47\n" \
"v_cvt_pk_bf16_f32 v121, v18, v19\n" \
"v_cvt_pk_bf16_f32 v122, v20, v21\n" \
"v_mfma_f32_16x16x32_bf16 v[96:99], a[28:31], a[212:215], v[96:99]\n" \
"buffer_load_dwordx4 v234, s[56:59], s81 offen offset:1024 sc1 lds\n" \
"v_mfma_f32_16x16x32_bf16 v[100:103], a[24:27], a[216:219], v[100:103]\n" \
"v_cvt_pk_bf16_f32 v123, v22, v23\n" \
"v_cvt_pk_bf16_f32 v124, v24, v25\n" \
"v_mfma_f32_16x16x32_bf16 v[100:103], a[28:31], a[220:223], v[100:103]\n" \
"v_cvt_pk_bf16_f32 v125, v26, v27\n" \
"v_cvt_pk_bf16_f32 v126, v28, v29\n" \
"v_mfma_f32_16x16x32_bf16 v[108:111], a[192:195], a[216:219], v[108:111]\n" \
"v_cvt_pk_bf16_f32 v127, v30, v31\n" \
"v_cvt_pk_bf16_f32 v128, v32, v33\n" \
"v_mfma_f32_16x16x32_bf16 v[108:111], a[196:199], a[220:223], v[108:111]\n" \
"buffer_load_dwordx4 v234, s[56:59], s81 offen offset:2048 sc1 lds\n" \
"v_mfma_f32_16x16x32_bf16 v[104:107], a[192:195], a[208:211], v[104:107]\n" \
"v_cvt_pk_bf16_f32 v129, v34, v35\n" \
"v_xor_b32 v145, 0x2000, v223\n" \
"v_mfma_f32_16x16x32_bf16 v[104:107], a[196:199], a[212:215], v[104:107]\n" \
"s_waitcnt lgkmcnt(0)\n" \
"v_cvt_pk_bf16_f32 v192, v72, v73\n" \
"v_cvt_pk_bf16_f32 v193, v74, v75\n" \
"ds_write_b64 v145, v[128:129]\n" \
"s_nop 0\n" \
"ds_write_b64 v145, v[192:193] offset:8\n" \
"v_cvt_pk_bf16_f32 v132, v40, v41\n" \
"v_cvt_pk_bf16_f32 v133, v42, v43\n" \
"v_cvt_pk_bf16_f32 v134, v44, v45\n" \
"v_cvt_pk_bf16_f32 v135, v46, v47\n" \
"v_cvt_pk_bf16_f32 v136, v48, v49\n" \
"v_cvt_pk_bf16_f32 v137, v50, v51\n" \
"v_cvt_pk_bf16_f32 v138, v52, v53\n" \
"v_cvt_pk_bf16_f32 v139, v54, v55\n" \
"v_cvt_pk_bf16_f32 v140, v56, v57\n" \
"v_cvt_pk_bf16_f32 v141, v58, v59\n" \
"v_cvt_pk_bf16_f32 v142, v60, v61\n" \
"v_cvt_pk_bf16_f32 v143, v62, v63\n" \
"v_mfma_f32_16x16x32_bf16 v[0:3], a[96:99], a[64:67], 0\n" \
"ds_read_b128 a[88:91], v213 offset:4352\n" \
"v_mfma_f32_16x16x32_bf16 v[0:3], a[100:103], a[68:71], v[0:3]\n" \
"ds_read_b128 a[92:95], v213 offset:6400\n" \
"v_mfma_f32_16x16x32_bf16 v[4:7], a[104:107], a[64:67], 0\n" \
"ds_read_b64_tr_b16 a[112:113], v214 offset:16896\n" \
"v_mfma_f32_16x16x32_bf16 v[4:7], a[108:111], a[68:71], v[4:7]\n" \
"ds_read_b64_tr_b16 a[114:115], v214 offset:18944\n" \
"v_mfma_f32_16x16x32_bf16 v[20:23], a[104:107], a[72:75], 0\n" \
"ds_read_b64_tr_b16 a[116:117], v214 offset:25088\n" \
"v_cvt_pk_bf16_f32 v64, v64, v65\n" \
"v_mfma_f32_16x16x32_bf16 v[20:23], a[108:111], a[76:79], v[20:23]\n" \
"buffer_load_dwordx4 v234, s[56:59], s81 offen offset:3072 sc1 lds\n" \
"v_cvt_pk_bf16_f32 v65, v66, v67\n" \
"v_cvt_pk_bf16_f32 v66, v68, v69\n" \
"v_mfma_f32_16x16x32_bf16 v[16:19], a[96:99], a[72:75], 0\n" \
"s_sub_u32 s85, s85, 32768\n" \
"s_cselect_b32 s85, 131072, s85\n" \
"ds_read_b64_tr_b16 a[118:119], v214 offset:27136\n" \
"v_mfma_f32_16x16x32_bf16 v[16:19], a[100:103], a[76:79], v[16:19]\n" \
"s_waitcnt lgkmcnt(3)\n" \
"ds_read_b64_tr_b16 a[120:121], v215 offset:16896\n" \
"v_cvt_pk_bf16_f32 v67, v70, v71\n" \
"v_cvt_pk_bf16_f32 v194, v80, v81\n" \
"v_cvt_pk_bf16_f32 v195, v82, v83\n" \
"v_cvt_pk_bf16_f32 v74, v84, v85\n" \
"v_mfma_f32_16x16x32_bf16 v[32:35], a[96:99], a[80:83], 0\n" \
"s_add_u32 m0, m0, 0x4000\n" \
"ds_read_b64_tr_b16 a[122:123], v215 offset:18944\n" \
"v_cvt_pk_bf16_f32 v75, v86, v87\n" \
"v_mfma_f32_16x16x32_bf16 v[32:35], a[100:103], a[84:87], v[32:35]\n" \
"buffer_load_dwordx4 v216, s[36:39], s82 offen offset:0 lds\n" \
"v_cvt_pk_bf16_f32 v196, v88, v89\n" \
"v_cvt_pk_bf16_f32 v197, v90, v91\n" \
"v_mfma_f32_16x16x32_bf16 v[176:179], v[64:67], v[124:127], v[152:155]\n" \
"v_mfma_f32_16x16x32_bf16 v[172:175], v[140:143], v[124:127], v[156:159]\n" \
"v_mfma_f32_16x16x32_bf16 v[168:171], v[140:143], v[120:123], v[152:155]\n" \
"v_mfma_f32_16x16x32_bf16 v[164:167], v[136:139], v[120:123], v[156:159]\n" \
"v_mfma_f32_16x16x32_bf16 v[160:163], v[136:139], v[116:119], v[152:155]\n" \
"v_mfma_f32_16x16x32_bf16 v[156:159], v[132:135], v[116:119], v[156:159]\n" \
"v_mfma_f32_16x16x32_bf16 v[152:155], v[132:135], v[112:115], v[152:155]\n" \
"v_mfma_f32_16x16x32_bf16 v[36:39], a[104:107], a[80:83], 0\n" \
"s_add_u32 s48, s48, 0x1000\n" \
"ds_read_b64_tr_b16 a[124:125], v215 offset:25088\n" \
"v_add_u32 v213, s85, v211\n" \
"v_mfma_f32_16x16x32_bf16 v[36:39], a[108:111], a[84:87], v[36:39]\n" \
"buffer_load_dwordx4 v217, s[36:39], s82 offen offset:1024 lds\n" \
"v_mfma_f32_16x16x32_bf16 v[52:55], a[104:107], a[88:91], 0\n" \
"s_sub_u32 s86, s86, 32768\n" \
"s_cselect_b32 s86, 131072, s86\n" \
"ds_read_b64_tr_b16 a[126:127], v215 offset:27136\n" \
"v_mfma_f32_16x16x32_bf16 v[52:55], a[108:111], a[92:95], v[52:55]\n" \
"buffer_load_dwordx4 v218, s[36:39], s82 offen offset:2048 lds\n" \
"v_mfma_f32_16x16x32_bf16 v[48:51], a[96:99], a[88:91], 0\n" \
"v_add_u32 v214, s85, v212\n" \
"v_xor_b32 v215, 32, v214\n" \
"v_mfma_f32_16x16x32_bf16 v[48:51], a[100:103], a[92:95], v[48:51]\n" \
"s_waitcnt vmcnt(15) lgkmcnt(0)\n" \
"s_barrier\n" \
"v_mfma_f32_16x16x32_bf16 v[8:11], a[112:115], a[64:67], 0\n" \
"ds_read_b64 v[130:131], v223\n" \
"ds_read_b64_tr_b16 a[96:97], v214 offset:16384\n" \
"ds_read_b64_tr_b16 a[98:99], v214 offset:18432\n" \
"v_mfma_f32_16x16x32_bf16 v[8:11], a[116:119], a[68:71], v[8:11]\n" \
"ds_read_b64 v[72:73], v223 offset:8\n" \
"buffer_load_dwordx4 v219, s[36:39], s82 offen offset:3072 lds\n" \
"v_mfma_f32_16x16x32_bf16 v[12:15], a[120:123], a[64:67], 0\n" \
"s_xor_b32 s82, s48, s49\n" \
"s_cmp_lt_u32 s48, s50\n" \
"s_cselect_b32 s82, s82, s48\n" \
"ds_read_b64_tr_b16 a[100:101], v214 offset:24576\n" \
"ds_read_b64_tr_b16 a[102:103], v214 offset:26624\n" \
"v_mfma_f32_16x16x32_bf16 v[12:15], a[124:127], a[68:71], v[12:15]\n" \
"s_add_u32 s81, s82, s46\n" \
"ds_read_b128 a[64:67], v213 offset:0\n" \
"v_mfma_f32_16x16x32_bf16 v[28:31], a[120:123], a[72:75], 0\n" \
"s_add_u32 m0, s86, s79\n" \
"ds_read_b128 a[68:71], v213 offset:2048\n" \
"v_mfma_f32_16x16x32_bf16 v[28:31], a[124:127], a[76:79], v[28:31]\n" \
"buffer_load_dwordx4 v234, s[56:59], s81 offen offset:0 sc1 lds\n" \
"v_mfma_f32_16x16x32_bf16 v[24:27], a[112:115], a[72:75], 0\n" \
"ds_read_b64_tr_b16 a[104:105], v215 offset:16384\n" \
"ds_read_b64_tr_b16 a[106:107], v215 offset:18432\n" \
"v_mfma_f32_16x16x32_bf16 v[24:27], a[116:119], a[76:79], v[24:27]\n" \
"s_waitcnt lgkmcnt(4)\n" \
"s_lshl_b32 s82, s82, 5\n" \
"ds_read_b128 a[72:75], v213 offset:256\n" \
"v_mfma_f32_16x16x32_bf16 v[40:43], a[112:115], a[80:83], 0\n" \
"s_add_u32 s82, s82, s47\n" \
"ds_read_b128 a[76:79], v213 offset:2304\n" \
"v_mfma_f32_16x16x32_bf16 v[148:151], v[128:131], v[112:115], v[148:151]\n" \
"v_max_f32 v172, v172, v176\n" \
"v_max_f32 v173, v173, v177\n" \
"v_max_f32 v174, v174, v178\n" \
"v_max_f32 v175, v175, v179\n" \
"v_max_f32 v164, v164, v168\n" \
"v_max_f32 v165, v165, v169\n" \
"v_max_f32 v166, v166, v170\n" \
"v_max_f32 v167, v167, v171\n" \
"v_max_f32 v156, v156, v160\n" \
"v_max_f32 v157, v157, v161\n" \
"v_max_f32 v158, v158, v162\n" \
"v_max_f32 v159, v159, v163\n" \
"v_max_f32 v148, v148, v152\n" \
"v_max_f32 v149, v149, v153\n" \
"v_max_f32 v150, v150, v154\n" \
"v_max_f32 v151, v151, v155\n" \
"v_max3_f32 v183, v172, v173, v174\n" \
"v_max3_f32 v182, v164, v165, v166\n" \
"v_max3_f32 v181, v156, v157, v158\n" \
"v_mfma_f32_16x16x32_bf16 v[40:43], a[116:119], a[84:87], v[40:43]\n" \
"buffer_load_dwordx4 v234, s[56:59], s81 offen offset:1024 sc1 lds\n" \
"v_max3_f32 v180, v148, v149, v150\n" \
"v_mfma_f32_16x16x32_bf16 v[44:47], a[120:123], a[80:83], 0\n" \
"ds_read_b64_tr_b16 a[108:109], v215 offset:24576\n" \
"ds_read_b64_tr_b16 a[110:111], v215 offset:26624\n" \
"v_mfma_f32_16x16x32_bf16 v[44:47], a[124:127], a[84:87], v[44:47]\n" \
"buffer_load_dwordx4 v234, s[56:59], s81 offen offset:2048 sc1 lds\n" \
"v_max_f32 v183, v183, v175\n" \
"v_max_f32 v182, v182, v167\n" \
"v_max_f32 v181, v181, v159\n" \
"v_mfma_f32_16x16x32_bf16 v[60:63], a[120:123], a[88:91], 0\n" \
"ds_read_b128 a[80:83], v213 offset:4096\n" \
"s_waitcnt lgkmcnt(4)\n" \
"v_mfma_f32_16x16x32_bf16 v[60:63], a[124:127], a[92:95], v[60:63]\n" \
"ds_read_b128 a[84:87], v213 offset:6144\n" \
"v_max_f32 v180, v180, v151\n" \
"v_cvt_pk_bf16_f32 v76, v92, v93\n" \
"v_cvt_pk_bf16_f32 v77, v94, v95\n" \
"v_mfma_f32_16x16x32_bf16 v[56:59], a[112:115], a[88:91], 0\n" \
"buffer_load_dwordx4 v234, s[56:59], s81 offen offset:3072 sc1 lds\n" \
"v_mfma_f32_16x16x32_bf16 v[56:59], a[116:119], a[92:95], v[56:59]\n" \
"s_getpc_b64 s[44:45]\n" \
"Lop_%=:\n" \
"s_add_u32 s44, s44, Lor_%=-Lop_%=\n" \
"s_addc_u32 s45, s45, 0\n" \
"v_permlane32_swap_b32 v180, v181\n" \
"v_permlane32_swap_b32 v182, v183\n" \
"v_max_f32 v180, v180, v181\n" \
"v_mfma_f32_16x16x32_bf16 v[0:3], a[96:99], a[64:67], v[0:3]\n" \
"ds_read_b128 a[88:91], v213 offset:4352\n" \
"v_max_f32 v182, v182, v183\n" \
"v_mfma_f32_16x16x32_bf16 v[0:3], a[100:103], a[68:71], v[0:3]\n" \
"s_waitcnt lgkmcnt(2)\n" \
"ds_read_b128 a[92:95], v213 offset:6400\n" \
"v_cvt_pk_bf16_f32 v198, v96, v97\n" \
"v_cvt_pk_bf16_f32 v199, v98, v99\n" \
"v_permlane16_swap_b32 v180, v182\n" \
"v_mfma_f32_16x16x32_bf16 v[4:7], a[104:107], a[64:67], v[4:7]\n" \
"ds_read_b64_tr_b16 a[112:113], v214 offset:16896\n" \
"v_max_f32 v180, v180, v182\n" \
"v_mfma_f32_16x16x32_bf16 v[4:7], a[108:111], a[68:71], v[4:7]\n" \
"ds_read_b64_tr_b16 a[114:115], v214 offset:18944\n" \
"v_mul_f32 v180, v180, v144\n" \
"v_cvt_pk_bf16_f32 v78, v100, v101\n" \
"v_cvt_pk_bf16_f32 v79, v102, v103\n" \
"v_mov_b32 v182, v180\n" \
"v_mfma_f32_16x16x32_bf16 v[20:23], a[104:107], a[72:75], v[20:23]\n" \
"s_add_u32 m0, m0, 0x4000\n" \
"ds_read_b64_tr_b16 a[116:117], v214 offset:25088\n" \
"v_cvt_pk_bf16_f32 v200, v104, v105\n" \
"v_mfma_f32_16x16x32_bf16 v[20:23], a[108:111], a[76:79], v[20:23]\n" \
"buffer_load_dwordx4 v216, s[36:39], s82 offen offset:0 lds\n" \
"v_cvt_pk_bf16_f32 v201, v106, v107\n" \
"v_permlane16_swap_b32 v180, v182\n" \
"v_mfma_f32_16x16x32_bf16 v[16:19], a[96:99], a[72:75], v[16:19]\n" \
"s_sub_u32 s85, s85, 32768\n" \
"s_cselect_b32 s85, 131072, s85\n" \
"ds_read_b64_tr_b16 a[118:119], v214 offset:27136\n" \
"v_mfma_f32_16x16x32_bf16 v[16:19], a[100:103], a[76:79], v[16:19]\n" \
"s_waitcnt lgkmcnt(3)\n" \
"ds_read_b64_tr_b16 a[120:121], v215 offset:16896\n" \
"v_mov_b32 v181, v180\n" \
"v_mov_b32 v183, v182\n" \
"v_cvt_pk_bf16_f32 v80, v108, v109\n" \
"v_cvt_pk_bf16_f32 v81, v110, v111\n" \
"v_mfma_f32_16x16x32_bf16 v[32:35], a[96:99], a[80:83], v[32:35]\n" \
"s_add_u32 s48, s48, 0x1000\n" \
"ds_read_b64_tr_b16 a[122:123], v215 offset:18944\n" \
"v_permlane32_swap_b32 v180, v181\n" \
"v_mfma_f32_16x16x32_bf16 v[32:35], a[100:103], a[84:87], v[32:35]\n" \
"buffer_load_dwordx4 v217, s[36:39], s82 offen offset:1024 lds\n" \
"v_permlane32_swap_b32 v182, v183\n" \
"v_fma_f32 v172, v172, v144, -v183\n" \
"v_mfma_f32_16x16x32_bf16 v[36:39], a[104:107], a[80:83], v[36:39]\n" \
"s_sub_u32 s86, s86, 32768\n" \
"s_cselect_b32 s86, 131072, s86\n" \
"ds_read_b64_tr_b16 a[124:125], v215 offset:25088\n" \
"v_add_u32 v213, s85, v211\n" \
"v_fma_f32 v164, v164, v144, -v182\n" \
"v_mfma_f32_16x16x32_bf16 v[36:39], a[108:111], a[84:87], v[36:39]\n" \
"buffer_load_dwordx4 v218, s[36:39], s82 offen offset:2048 lds\n" \
"v_fma_f32 v156, v156, v144, -v181\n" \
"v_fma_f32 v148, v148, v144, -v180\n" \
"v_mfma_f32_16x16x32_bf16 v[52:55], a[104:107], a[88:91], v[52:55]\n" \
"ds_read_b64_tr_b16 a[126:127], v215 offset:27136\n" \
"v_fma_f32 v173, v173, v144, -v183\n" \
"v_mfma_f32_16x16x32_bf16 v[52:55], a[108:111], a[92:95], v[52:55]\n" \
"buffer_load_dwordx4 v219, s[36:39], s82 offen offset:3072 lds\n" \
"v_fma_f32 v165, v165, v144, -v182\n" \
"v_fma_f32 v157, v157, v144, -v181\n" \
"v_mfma_f32_16x16x32_bf16 v[48:51], a[96:99], a[88:91], v[48:51]\n" \
"s_xor_b32 s82, s48, s49\n" \
"s_cmp_lt_u32 s48, s50\n" \
"s_cselect_b32 s82, s82, s48\n" \
"v_add_u32 v214, s85, v212\n" \
"v_xor_b32 v215, 32, v214\n" \
"v_fma_f32 v149, v149, v144, -v180\n" \
"v_mfma_f32_16x16x32_bf16 v[48:51], a[100:103], a[92:95], v[48:51]\n" \
"s_add_u32 s81, s82, s46\n" \
"v_fma_f32 v174, v174, v144, -v183\n" \
"v_fma_f32 v166, v166, v144, -v182\n" \
"s_waitcnt vmcnt(16) lgkmcnt(0)\n" \
"s_barrier\n" \
"v_mfma_f32_16x16x32_bf16 v[8:11], a[112:115], a[64:67], v[8:11]\n" \
"s_add_u32 m0, s86, s79\n" \
"ds_read_b64_tr_b16 a[96:97], v214 offset:16384\n" \
"ds_read_b64_tr_b16 a[98:99], v214 offset:18432\n" \
"v_mfma_f32_16x16x32_bf16 v[8:11], a[116:119], a[68:71], v[8:11]\n" \
"buffer_load_dwordx4 v234, s[56:59], s81 offen offset:0 sc1 lds\n" \
"v_fma_f32 v158, v158, v144, -v181\n" \
"v_fma_f32 v150, v150, v144, -v180\n" \
"v_mfma_f32_16x16x32_bf16 v[12:15], a[120:123], a[64:67], v[12:15]\n" \
"ds_read_b64_tr_b16 a[100:101], v214 offset:24576\n" \
"ds_read_b64_tr_b16 a[102:103], v214 offset:26624\n" \
"v_mfma_f32_16x16x32_bf16 v[12:15], a[124:127], a[68:71], v[12:15]\n" \
"s_lshl_b32 s82, s82, 5\n" \
"ds_read_b128 a[64:67], v213 offset:0\n" \
"v_fma_f32 v175, v175, v144, -v183\n" \
"v_fma_f32 v167, v167, v144, -v182\n" \
"v_fma_f32 v159, v159, v144, -v181\n" \
"v_fma_f32 v151, v151, v144, -v180\n" \
"v_mfma_f32_16x16x32_bf16 v[28:31], a[120:123], a[72:75], v[28:31]\n" \
"s_add_u32 s82, s82, s47\n" \
"ds_read_b128 a[68:71], v213 offset:2048\n" \
"v_mfma_f32_16x16x32_bf16 v[28:31], a[124:127], a[76:79], v[28:31]\n" \
"buffer_load_dwordx4 v234, s[56:59], s81 offen offset:1024 sc1 lds\n" \
"v_exp_f32 v172, v172\n" \
"v_mfma_f32_16x16x32_bf16 v[24:27], a[112:115], a[72:75], v[24:27]\n" \
"ds_read_b64_tr_b16 a[104:105], v215 offset:16384\n" \
"ds_read_b64_tr_b16 a[106:107], v215 offset:18432\n" \
"v_mfma_f32_16x16x32_bf16 v[24:27], a[116:119], a[76:79], v[24:27]\n" \
"ds_read_b128 a[72:75], v213 offset:256\n" \
"v_exp_f32 v164, v164\n" \
"v_exp_f32 v156, v156\n" \
"v_mfma_f32_16x16x32_bf16 v[40:43], a[112:115], a[80:83], v[40:43]\n" \
"ds_read_b128 a[76:79], v213 offset:2304\n" \
"v_mfma_f32_16x16x32_bf16 v[40:43], a[116:119], a[84:87], v[40:43]\n" \
"buffer_load_dwordx4 v234, s[56:59], s81 offen offset:2048 sc1 lds\n" \
"v_exp_f32 v148, v148\n" \
"v_mfma_f32_16x16x32_bf16 v[44:47], a[120:123], a[80:83], v[44:47]\n" \
"ds_read_b64_tr_b16 a[108:109], v215 offset:24576\n" \
"ds_read_b64_tr_b16 a[110:111], v215 offset:26624\n" \
"v_mfma_f32_16x16x32_bf16 v[44:47], a[124:127], a[84:87], v[44:47]\n" \
"buffer_load_dwordx4 v234, s[56:59], s81 offen offset:3072 sc1 lds\n" \
"v_exp_f32 v173, v173\n" \
"v_mfma_f32_16x16x32_bf16 v[60:63], a[120:123], a[88:91], v[60:63]\n" \
"ds_read_b128 a[80:83], v213 offset:4096\n" \
"s_waitcnt lgkmcnt(4)\n" \
"v_mfma_f32_16x16x32_bf16 v[60:63], a[124:127], a[92:95], v[60:63]\n" \
"s_add_u32 m0, m0, 0x4000\n" \
"ds_read_b128 a[84:87], v213 offset:6144\n" \
"v_exp_f32 v165, v165\n" \
"v_exp_f32 v157, v157\n" \
"v_mfma_f32_16x16x32_bf16 v[56:59], a[112:115], a[88:91], v[56:59]\n" \
"buffer_load_dwordx4 v216, s[36:39], s82 offen offset:0 lds\n" \
"v_mfma_f32_16x16x32_bf16 v[56:59], a[116:119], a[92:95], v[56:59]\n" \
"v_exp_f32 v149, v149\n" \
"v_exp_f32 v174, v174\n" \
"v_mfma_f32_16x16x32_bf16 v[0:3], a[96:99], a[64:67], v[0:3]\n" \
"ds_read_b128 a[88:91], v213 offset:4352\n" \
"v_mfma_f32_16x16x32_bf16 v[0:3], a[100:103], a[68:71], v[0:3]\n" \
"s_waitcnt lgkmcnt(2)\n" \
"ds_read_b128 a[92:95], v213 offset:6400\n" \
"v_exp_f32 v166, v166\n" \
"v_exp_f32 v158, v158\n" \
"v_mfma_f32_16x16x32_bf16 v[4:7], a[104:107], a[64:67], v[4:7]\n" \
"ds_read_b64_tr_b16 a[112:113], v214 offset:16896\n" \
"v_mfma_f32_16x16x32_bf16 v[4:7], a[108:111], a[68:71], v[4:7]\n" \
"ds_read_b64_tr_b16 a[114:115], v214 offset:18944\n" \
"v_exp_f32 v150, v150\n" \
"v_exp_f32 v175, v175\n" \
"v_mfma_f32_16x16x32_bf16 v[20:23], a[104:107], a[72:75], v[20:23]\n" \
"s_add_u32 s48, s48, 0x1000\n" \
"ds_read_b64_tr_b16 a[116:117], v214 offset:25088\n" \
"v_mfma_f32_16x16x32_bf16 v[20:23], a[108:111], a[76:79], v[20:23]\n" \
"buffer_load_dwordx4 v217, s[36:39], s82 offen offset:1024 lds\n" \
"v_exp_f32 v167, v167\n" \
"v_mfma_f32_16x16x32_bf16 v[16:19], a[96:99], a[72:75], v[16:19]\n" \
"s_sub_u32 s85, s85, 32768\n" \
"s_cselect_b32 s85, 131072, s85\n" \
"ds_read_b64_tr_b16 a[118:119], v214 offset:27136\n" \
"v_mfma_f32_16x16x32_bf16 v[16:19], a[100:103], a[76:79], v[16:19]\n" \
"s_waitcnt lgkmcnt(3)\n" \
"ds_read_b64_tr_b16 a[120:121], v215 offset:16896\n" \
"v_exp_f32 v159, v159\n" \
"v_exp_f32 v151, v151\n" \
"v_mfma_f32_16x16x32_bf16 v[32:35], a[96:99], a[80:83], v[32:35]\n" \
"s_sub_u32 s86, s86, 32768\n" \
"s_cselect_b32 s86, 131072, s86\n" \
"ds_read_b64_tr_b16 a[122:123], v215 offset:18944\n" \
"v_mfma_f32_16x16x32_bf16 v[32:35], a[100:103], a[84:87], v[32:35]\n" \
"buffer_load_dwordx4 v218, s[36:39], s82 offen offset:2048 lds\n" \
"v_add_f32 v191, v172, v173\n" \
"v_add_f32 v190, v164, v165\n" \
"v_mfma_f32_16x16x32_bf16 v[36:39], a[104:107], a[80:83], v[36:39]\n" \
"ds_read_b64_tr_b16 a[124:125], v215 offset:25088\n" \
"v_add_u32 v213, s85, v211\n" \
"v_mfma_f32_16x16x32_bf16 v[36:39], a[108:111], a[84:87], v[36:39]\n" \
"buffer_load_dwordx4 v219, s[36:39], s82 offen offset:3072 lds\n" \
"v_add_f32 v189, v156, v157\n" \
"v_add_f32 v188, v148, v149\n" \
"v_add_f32 v191, v191, v174\n" \
"v_mfma_f32_16x16x32_bf16 v[52:55], a[104:107], a[88:91], v[52:55]\n" \
"s_xor_b32 s82, s48, s49\n" \
"s_cmp_lt_u32 s48, s50\n" \
"s_cselect_b32 s82, s82, s48\n" \
"s_add_u32 s81, s82, s46\n" \
"s_add_u32 m0, s86, s79\n" \
"ds_read_b64_tr_b16 a[126:127], v215 offset:27136\n" \
"v_add_f32 v190, v190, v166\n" \
"v_mfma_f32_16x16x32_bf16 v[52:55], a[108:111], a[92:95], v[52:55]\n" \
"buffer_load_dwordx4 v234, s[56:59], s81 offen offset:0 sc1 lds\n" \
"v_add_f32 v189, v189, v158\n" \
"v_mfma_f32_16x16x32_bf16 v[48:51], a[96:99], a[88:91], v[48:51]\n" \
"v_add_u32 v214, s85, v212\n" \
"v_xor_b32 v215, 32, v214\n" \
"v_mfma_f32_16x16x32_bf16 v[48:51], a[100:103], a[92:95], v[48:51]\n" \
"s_lshl_b32 s82, s82, 5\n" \
"v_add_f32 v188, v188, v150\n" \
"v_add_f32 v191, v191, v175\n" \
"v_add_f32 v190, v190, v167\n" \
"s_waitcnt vmcnt(17) lgkmcnt(0)\n" \
"s_barrier\n" \
"v_mfma_f32_16x16x32_bf16 v[8:11], a[112:115], a[64:67], v[8:11]\n" \
"s_add_u32 s82, s82, s47\n" \
"ds_read_b64_tr_b16 a[96:97], v214 offset:16384\n" \
"ds_read_b64_tr_b16 a[98:99], v214 offset:18432\n" \
"v_mfma_f32_16x16x32_bf16 v[8:11], a[116:119], a[68:71], v[8:11]\n" \
"buffer_load_dwordx4 v234, s[56:59], s81 offen offset:1024 sc1 lds\n" \
"v_add_f32 v189, v189, v159\n" \
"v_add_f32 v188, v188, v151\n" \
"v_mfma_f32_16x16x32_bf16 v[12:15], a[120:123], a[64:67], v[12:15]\n" \
"ds_read_b64_tr_b16 a[100:101], v214 offset:24576\n" \
"ds_read_b64_tr_b16 a[102:103], v214 offset:26624\n" \
"v_mfma_f32_16x16x32_bf16 v[12:15], a[124:127], a[68:71], v[12:15]\n" \
"ds_read_b128 a[64:67], v213 offset:0\n" \
"v_permlane32_swap_b32 v188, v189\n" \
"v_permlane32_swap_b32 v190, v191\n" \
"v_add_f32 v188, v188, v189\n" \
"v_add_f32 v190, v190, v191\n" \
"v_mfma_f32_16x16x32_bf16 v[28:31], a[120:123], a[72:75], v[28:31]\n" \
"ds_read_b128 a[68:71], v213 offset:2048\n" \
"v_mfma_f32_16x16x32_bf16 v[28:31], a[124:127], a[76:79], v[28:31]\n" \
"buffer_load_dwordx4 v234, s[56:59], s81 offen offset:2048 sc1 lds\n" \
"v_permlane16_swap_b32 v188, v190\n" \
"v_add_f32 v188, v188, v190\n" \
"v_mfma_f32_16x16x32_bf16 v[24:27], a[112:115], a[72:75], v[24:27]\n" \
"ds_read_b64_tr_b16 a[104:105], v215 offset:16384\n" \
"ds_read_b64_tr_b16 a[106:107], v215 offset:18432\n" \
"v_mfma_f32_16x16x32_bf16 v[24:27], a[116:119], a[76:79], v[24:27]\n" \
"ds_read_b128 a[72:75], v213 offset:256\n" \
"v_rcp_f32 v188, v188\n" \
"v_mfma_f32_16x16x32_bf16 v[40:43], a[112:115], a[80:83], v[40:43]\n" \
"ds_read_b128 a[76:79], v213 offset:2304\n" \
"v_mfma_f32_16x16x32_bf16 v[40:43], a[116:119], a[84:87], v[40:43]\n" \
"buffer_load_dwordx4 v234, s[56:59], s81 offen offset:3072 sc1 lds\n" \
"v_mov_b32 v190, v188\n" \
"v_mfma_f32_16x16x32_bf16 v[44:47], a[120:123], a[80:83], v[44:47]\n" \
"s_add_u32 m0, m0, 0x4000\n" \
"ds_read_b64_tr_b16 a[108:109], v215 offset:24576\n" \
"ds_read_b64_tr_b16 a[110:111], v215 offset:26624\n" \
"v_mfma_f32_16x16x32_bf16 v[44:47], a[124:127], a[84:87], v[44:47]\n" \
"buffer_load_dwordx4 v216, s[36:39], s82 offen offset:0 lds\n" \
"v_permlane16_swap_b32 v188, v190\n" \
"v_mov_b32 v189, v188\n" \
"v_mov_b32 v191, v190\n" \
"v_mfma_f32_16x16x32_bf16 v[60:63], a[120:123], a[88:91], v[60:63]\n" \
"ds_read_b128 a[80:83], v213 offset:4096\n" \
"s_waitcnt lgkmcnt(4)\n" \
"v_mfma_f32_16x16x32_bf16 v[60:63], a[124:127], a[92:95], v[60:63]\n" \
"s_add_u32 s48, s48, 0x1000\n" \
"ds_read_b128 a[84:87], v213 offset:6144\n" \
"v_permlane32_swap_b32 v188, v189\n" \
"v_permlane32_swap_b32 v190, v191\n" \
"v_mul_f32 v172, v172, v191\n" \
"v_mul_f32 v164, v164, v190\n" \
"v_mul_f32 v156, v156, v189\n" \
"v_mul_f32 v148, v148, v188\n" \
"v_mfma_f32_16x16x32_bf16 v[56:59], a[112:115], a[88:91], v[56:59]\n" \
"buffer_load_dwordx4 v217, s[36:39], s82 offen offset:1024 lds\n" \
"v_mfma_f32_16x16x32_bf16 v[56:59], a[116:119], a[92:95], v[56:59]\n" \
"v_mul_f32 v173, v173, v191\n" \
"v_mul_f32 v165, v165, v190\n" \
"v_mul_f32 v157, v157, v189\n" \
"v_mul_f32 v149, v149, v188\n" \
"v_mul_f32 v174, v174, v191\n" \
"v_mfma_f32_16x16x32_bf16 v[0:3], a[96:99], a[64:67], v[0:3]\n" \
"ds_read_b128 a[88:91], v213 offset:4352\n" \
"v_mul_f32 v166, v166, v190\n" \
"v_mfma_f32_16x16x32_bf16 v[0:3], a[100:103], a[68:71], v[0:3]\n" \
"s_waitcnt lgkmcnt(2)\n" \
"ds_read_b128 a[92:95], v213 offset:6400\n" \
"v_mul_f32 v158, v158, v189\n" \
"v_mul_f32 v150, v150, v188\n" \
"v_mul_f32 v175, v175, v191\n" \
"v_mfma_f32_16x16x32_bf16 v[4:7], a[104:107], a[64:67], v[4:7]\n" \
"ds_read_b64_tr_b16 a[112:113], v214 offset:16896\n" \
"v_mul_f32 v167, v167, v190\n" \
"v_mfma_f32_16x16x32_bf16 v[4:7], a[108:111], a[68:71], v[4:7]\n" \
"ds_read_b64_tr_b16 a[114:115], v214 offset:18944\n" \
"v_mul_f32 v159, v159, v189\n" \
"v_mul_f32 v151, v151, v188\n" \
"v_cvt_pk_bf16_f32 v178, v172, v173\n" \
"v_cvt_pk_bf16_f32 v179, v174, v175\n" \
"v_mfma_f32_16x16x32_bf16 v[20:23], a[104:107], a[72:75], v[20:23]\n" \
"s_sub_u32 s86, s86, 32768\n" \
"s_cselect_b32 s86, 131072, s86\n" \
"ds_read_b64_tr_b16 a[116:117], v214 offset:25088\n" \
"v_mfma_f32_16x16x32_bf16 v[20:23], a[108:111], a[76:79], v[20:23]\n" \
"buffer_load_dwordx4 v218, s[36:39], s82 offen offset:2048 lds\n" \
"v_cvt_pk_bf16_f32 v170, v164, v165\n" \
"v_cvt_pk_bf16_f32 v171, v166, v167\n" \
"v_mfma_f32_16x16x32_bf16 v[16:19], a[96:99], a[72:75], v[16:19]\n" \
"s_sub_u32 s85, s85, 32768\n" \
"s_cselect_b32 s85, 131072, s85\n" \
"ds_read_b64_tr_b16 a[118:119], v214 offset:27136\n" \
"v_mfma_f32_16x16x32_bf16 v[16:19], a[100:103], a[76:79], v[16:19]\n" \
"s_waitcnt lgkmcnt(3)\n" \
"ds_read_b64_tr_b16 a[120:121], v215 offset:16896\n" \
"v_cvt_pk_bf16_f32 v162, v156, v157\n" \
"v_cvt_pk_bf16_f32 v163, v158, v159\n" \
"v_cvt_pk_bf16_f32 v154, v148, v149\n" \
"v_cvt_pk_bf16_f32 v155, v150, v151\n" \
"v_mfma_f32_16x16x32_bf16 v[32:35], a[96:99], a[80:83], v[32:35]\n" \
"ds_read_b64_tr_b16 a[122:123], v215 offset:18944\n" \
"v_and_b32 v176, v178, v184\n" \
"v_mfma_f32_16x16x32_bf16 v[32:35], a[100:103], a[84:87], v[32:35]\n" \
"buffer_load_dwordx4 v219, s[36:39], s82 offen offset:3072 lds\n" \
"v_and_b32 v177, v179, v185\n" \
"v_xor_b32 v178, v178, v176\n" \
"v_mfma_f32_16x16x32_bf16 v[36:39], a[104:107], a[80:83], v[36:39]\n" \
"s_xor_b32 s82, s48, s49\n" \
"s_cmp_lt_u32 s48, s50\n" \
"s_cselect_b32 s82, s82, s48\n" \
"s_add_u32 s81, s82, s46\n" \
"s_add_u32 m0, s86, s79\n" \
"ds_read_b64_tr_b16 a[124:125], v215 offset:25088\n" \
"v_add_u32 v213, s85, v211\n" \
"v_xor_b32 v179, v179, v177\n" \
"v_mfma_f32_16x16x32_bf16 v[36:39], a[108:111], a[84:87], v[36:39]\n" \
"buffer_load_dwordx4 v234, s[56:59], s81 offen offset:0 sc1 lds\n" \
"v_and_b32 v168, v170, v184\n" \
"v_and_b32 v169, v171, v185\n" \
"v_mfma_f32_16x16x32_bf16 v[52:55], a[104:107], a[88:91], v[52:55]\n" \
"s_lshl_b32 s82, s82, 5\n" \
"s_add_u32 s82, s82, s47\n" \
"ds_read_b64_tr_b16 a[126:127], v215 offset:27136\n" \
"v_mfma_f32_16x16x32_bf16 v[52:55], a[108:111], a[92:95], v[52:55]\n" \
"buffer_load_dwordx4 v234, s[56:59], s81 offen offset:1024 sc1 lds\n" \
"v_xor_b32 v170, v170, v168\n" \
"v_xor_b32 v171, v171, v169\n" \
"v_mfma_f32_16x16x32_bf16 v[48:51], a[96:99], a[88:91], v[48:51]\n" \
"v_add_u32 v214, s85, v212\n" \
"v_xor_b32 v215, 32, v214\n" \
"v_mfma_f32_16x16x32_bf16 v[48:51], a[100:103], a[92:95], v[48:51]\n" \
"v_and_b32 v160, v162, v184\n" \
"v_and_b32 v161, v163, v185\n" \
"v_xor_b32 v162, v162, v160\n" \
"s_waitcnt vmcnt(18) lgkmcnt(0)\n" \
"s_barrier\n" \
"v_mfma_f32_16x16x32_bf16 v[8:11], a[112:115], a[64:67], v[8:11]\n" \
"ds_read_b64_tr_b16 a[96:97], v214 offset:16384\n" \
"ds_read_b64_tr_b16 a[98:99], v214 offset:18432\n" \
"v_mfma_f32_16x16x32_bf16 v[8:11], a[116:119], a[68:71], v[8:11]\n" \
"buffer_load_dwordx4 v234, s[56:59], s81 offen offset:2048 sc1 lds\n" \
"v_xor_b32 v163, v163, v161\n" \
"v_and_b32 v152, v154, v184\n" \
"v_mfma_f32_16x16x32_bf16 v[12:15], a[120:123], a[64:67], v[12:15]\n" \
"ds_read_b64_tr_b16 a[100:101], v214 offset:24576\n" \
"ds_read_b64_tr_b16 a[102:103], v214 offset:26624\n" \
"v_mfma_f32_16x16x32_bf16 v[12:15], a[124:127], a[68:71], v[12:15]\n" \
"ds_read_b128 a[64:67], v213 offset:0\n" \
"v_and_b32 v153, v155, v185\n" \
"v_xor_b32 v154, v154, v152\n" \
"v_xor_b32 v155, v155, v153\n" \
"v_mfma_f32_16x16x32_bf16 v[28:31], a[120:123], a[72:75], v[28:31]\n" \
"ds_read_b128 a[68:71], v213 offset:2048\n" \
"v_mfma_f32_16x16x32_bf16 v[136:139], v[198:201], v[176:179], 0\n" \
"v_mfma_f32_16x16x32_bf16 v[140:143], v[78:81], v[176:179], 0\n" \
"v_mfma_f32_16x16x32_bf16 v[128:131], v[196:199], v[168:171], 0\n" \
"v_mfma_f32_16x16x32_bf16 v[132:135], v[76:79], v[168:171], 0\n" \
"v_mfma_f32_16x16x32_bf16 v[120:123], v[194:197], v[160:163], 0\n" \
"v_mfma_f32_16x16x32_bf16 v[124:127], v[74:77], v[160:163], 0\n" \
"v_mfma_f32_16x16x32_bf16 v[112:115], v[192:195], v[152:155], 0\n" \
"v_mfma_f32_16x16x32_bf16 v[116:119], v[72:75], v[152:155], 0\n" \
"v_mfma_f32_16x16x32_bf16 v[28:31], a[124:127], a[76:79], v[28:31]\n" \
"buffer_load_dwordx4 v234, s[56:59], s81 offen offset:3072 sc1 lds\n" \
"v_mfma_f32_16x16x32_bf16 v[24:27], a[112:115], a[72:75], v[24:27]\n" \
"ds_read_b64_tr_b16 a[104:105], v215 offset:16384\n" \
"ds_read_b64_tr_b16 a[106:107], v215 offset:18432\n" \
"v_mfma_f32_16x16x32_bf16 v[24:27], a[116:119], a[76:79], v[24:27]\n" \
"ds_read_b128 a[72:75], v213 offset:256\n" \
"v_cvt_pk_bf16_f32 v136, v136, v137\n" \
"v_cvt_pk_bf16_f32 v137, v138, v139\n" \
"v_cvt_pk_bf16_f32 v138, v140, v141\n" \
"v_cvt_pk_bf16_f32 v139, v142, v143\n" \
"v_cndmask_b32 v140, v136, v138, s[76:77]\n" \
"v_mfma_f32_16x16x32_bf16 v[40:43], a[112:115], a[80:83], v[40:43]\n" \
"s_add_u32 m0, m0, 0x4000\n" \
"ds_read_b128 a[76:79], v213 offset:2304\n" \
"v_cndmask_b32 v141, v137, v139, s[76:77]\n" \
"v_mfma_f32_16x16x32_bf16 v[40:43], a[116:119], a[84:87], v[40:43]\n" \
"buffer_load_dwordx4 v216, s[36:39], s82 offen offset:0 lds\n" \
"v_cndmask_b32 v142, v138, v136, s[76:77]\n" \
"v_cndmask_b32 v143, v139, v137, s[76:77]\n" \
"v_mfma_f32_16x16x32_bf16 v[44:47], a[120:123], a[80:83], v[44:47]\n" \
"s_add_u32 s48, s48, 0x1000\n" \
"ds_read_b64_tr_b16 a[108:109], v215 offset:24576\n" \
"ds_read_b64_tr_b16 a[110:111], v215 offset:26624\n" \
"v_mfma_f32_16x16x32_bf16 v[44:47], a[124:127], a[84:87], v[44:47]\n" \
"buffer_load_dwordx4 v217, s[36:39], s82 offen offset:1024 lds\n" \
"v_cvt_pk_bf16_f32 v128, v128, v129\n" \
"Lpg10211_%=:\n" \
"s_cbranch_execz Lcb1_%=\n" \
"v_cvt_pk_bf16_f32 v129, v130, v131\n" \
"v_cvt_pk_bf16_f32 v130, v132, v133\n" \
"v_mfma_f32_16x16x32_bf16 v[60:63], a[120:123], a[88:91], v[60:63]\n" \
"ds_read_b128 a[80:83], v213 offset:4096\n" \
"s_waitcnt lgkmcnt(4)\n" \
"v_mfma_f32_16x16x32_bf16 v[60:63], a[124:127], a[92:95], v[60:63]\n" \
"s_sub_u32 s86, s86, 32768\n" \
"s_cselect_b32 s86, 131072, s86\n" \
"ds_read_b128 a[84:87], v213 offset:6144\n" \
"v_cvt_pk_bf16_f32 v131, v134, v135\n" \
"v_cndmask_b32 v132, v128, v130, s[76:77]\n" \
"v_mfma_f32_16x16x32_bf16 v[56:59], a[112:115], a[88:91], v[56:59]\n" \
"buffer_load_dwordx4 v218, s[36:39], s82 offen offset:2048 lds\n" \
"v_mfma_f32_16x16x32_bf16 v[56:59], a[116:119], a[92:95], v[56:59]\n" \
"v_cndmask_b32 v133, v129, v131, s[76:77]\n" \
"v_cndmask_b32 v134, v130, v128, s[76:77]\n" \
"v_cndmask_b32 v135, v131, v129, s[76:77]\n" \
"v_cvt_pk_bf16_f32 v120, v120, v121\n" \
"v_cvt_pk_bf16_f32 v121, v122, v123\n" \
"v_mfma_f32_16x16x32_bf16 v[0:3], a[96:99], a[64:67], v[0:3]\n" \
"ds_read_b128 a[88:91], v213 offset:4352\n" \
"v_cvt_pk_bf16_f32 v122, v124, v125\n" \
"v_mfma_f32_16x16x32_bf16 v[0:3], a[100:103], a[68:71], v[0:3]\n" \
"s_waitcnt lgkmcnt(2)\n" \
"ds_read_b128 a[92:95], v213 offset:6400\n" \
"v_cvt_pk_bf16_f32 v123, v126, v127\n" \
"v_cndmask_b32 v124, v120, v122, s[76:77]\n" \
"v_cndmask_b32 v125, v121, v123, s[76:77]\n" \
"v_mfma_f32_16x16x32_bf16 v[4:7], a[104:107], a[64:67], v[4:7]\n" \
"ds_read_b64_tr_b16 a[112:113], v214 offset:16896\n" \
"v_cndmask_b32 v126, v122, v120, s[76:77]\n" \
"v_mfma_f32_16x16x32_bf16 v[4:7], a[108:111], a[68:71], v[4:7]\n" \
"ds_read_b64_tr_b16 a[114:115], v214 offset:18944\n" \
"v_cndmask_b32 v127, v123, v121, s[76:77]\n" \
"v_cvt_pk_bf16_f32 v112, v112, v113\n" \
"v_cvt_pk_bf16_f32 v113, v114, v115\n" \
"v_cvt_pk_bf16_f32 v114, v116, v117\n" \
"v_mfma_f32_16x16x32_bf16 v[20:23], a[104:107], a[72:75], v[20:23]\n" \
"s_waitcnt lgkmcnt(4)\n" \
"ds_read_b64_tr_b16 a[116:117], v214 offset:25088\n" \
"v_cvt_pk_bf16_f32 v115, v118, v119\n" \
"v_mfma_f32_16x16x32_bf16 v[20:23], a[108:111], a[76:79], v[20:23]\n" \
"buffer_load_dwordx4 v219, s[36:39], s82 offen offset:3072 lds\n" \
"v_cndmask_b32 v116, v112, v114, s[76:77]\n" \
"v_cndmask_b32 v117, v113, v115, s[76:77]\n" \
"v_mfma_f32_16x16x32_bf16 v[16:19], a[96:99], a[72:75], v[16:19]\n" \
"s_sub_u32 s85, s85, 32768\n" \
"s_cselect_b32 s85, 131072, s85\n" \
"s_xor_b32 s82, s48, s49\n" \
"s_cmp_lt_u32 s48, s50\n" \
"s_cselect_b32 s82, s82, s48\n" \
"ds_read_b64_tr_b16 a[118:119], v214 offset:27136\n" \
"v_cndmask_b32 v118, v114, v112, s[76:77]\n" \
"v_mfma_f32_16x16x32_bf16 v[16:19], a[100:103], a[76:79], v[16:19]\n" \
"s_add_u32 s81, s82, s46\n" \
"ds_read_b64_tr_b16 a[120:121], v215 offset:16896\n" \
"v_cndmask_b32 v119, v115, v113, s[76:77]\n" \
"s_add_u32 s71, s93, 208896\n" \
"v_mfma_f32_16x16x32_bf16 v[32:35], a[96:99], a[80:83], v[32:35]\n" \
"s_add_u32 m0, s86, s79\n" \
"ds_read_b64_tr_b16 a[122:123], v215 offset:18944\n" \
"buffer_store_dwordx4 v[140:143], v236, s[56:59], s71 offen sc1\n" \
"s_add_u32 s71, s93, 139264\n" \
"v_mfma_f32_16x16x32_bf16 v[32:35], a[100:103], a[84:87], v[32:35]\n" \
"buffer_load_dwordx4 v234, s[56:59], s81 offen offset:0 sc1 lds\n" \
"v_mfma_f32_16x16x32_bf16 v[36:39], a[104:107], a[80:83], v[36:39]\n" \
"s_lshl_b32 s82, s82, 5\n" \
"s_add_u32 s82, s82, s47\n" \
"ds_read_b64_tr_b16 a[124:125], v215 offset:25088\n" \
"buffer_store_dwordx4 v[132:135], v236, s[56:59], s71 offen sc1\n" \
"s_add_u32 s71, s93, 69632\n" \
"v_add_u32_e64 v213, s85, v211\n" \
"v_mfma_f32_16x16x32_bf16 v[36:39], a[108:111], a[84:87], v[36:39]\n" \
"s_waitcnt lgkmcnt(3)\n" \
"buffer_load_dwordx4 v234, s[56:59], s81 offen offset:1024 sc1 lds\n" \
"v_mfma_f32_16x16x32_bf16 v[52:55], a[104:107], a[88:91], v[52:55]\n" \
"ds_read_b64_tr_b16 a[126:127], v215 offset:27136\n" \
"buffer_store_dwordx4 v[124:127], v236, s[56:59], s71 offen sc1\n" \
"s_add_u32 s71, s93, 0\n" \
"v_mfma_f32_16x16x32_bf16 v[52:55], a[108:111], a[92:95], v[52:55]\n" \
"buffer_load_dwordx4 v234, s[56:59], s81 offen offset:2048 sc1 lds\n" \
"v_mfma_f32_16x16x32_bf16 v[48:51], a[96:99], a[88:91], v[48:51]\n" \
"v_add_u32_e64 v214, s85, v212\n" \
"v_xor_b32_e64 v215, 32, v214\n" \
"v_mfma_f32_16x16x32_bf16 v[48:51], a[100:103], a[92:95], v[48:51]\n" \
"buffer_store_dwordx4 v[116:119], v236, s[56:59], s71 offen sc1\n" \
"s_waitcnt vmcnt(23) lgkmcnt(0)\n" \
"s_barrier\n" \
"v_mfma_f32_16x16x32_bf16 v[8:11], a[112:115], a[64:67], v[8:11]\n" \
"ds_read_b64_tr_b16 a[96:97], v214 offset:16384\n" \
"ds_read_b64_tr_b16 a[98:99], v214 offset:18432\n" \
"v_mfma_f32_16x16x32_bf16 v[8:11], a[116:119], a[68:71], v[8:11]\n" \
"buffer_load_dwordx4 v234, s[56:59], s81 offen offset:3072 sc1 lds\n" \
"v_mfma_f32_16x16x32_bf16 v[12:15], a[120:123], a[64:67], v[12:15]\n" \
"ds_read_b64_tr_b16 a[100:101], v214 offset:24576\n" \
"ds_read_b64_tr_b16 a[102:103], v214 offset:26624\n" \
"v_mfma_f32_16x16x32_bf16 v[12:15], a[124:127], a[68:71], v[12:15]\n" \
"ds_read_b128 a[64:67], v213 offset:0\n" \
"v_mfma_f32_16x16x32_bf16 v[28:31], a[120:123], a[72:75], v[28:31]\n" \
"s_add_u32 m0, m0, 0x4000\n" \
"ds_read_b128 a[68:71], v213 offset:2048\n" \
"v_mfma_f32_16x16x32_bf16 v[28:31], a[124:127], a[76:79], v[28:31]\n" \
"buffer_load_dwordx4 v216, s[36:39], s82 offen offset:0 lds\n" \
"v_mfma_f32_16x16x32_bf16 v[24:27], a[112:115], a[72:75], v[24:27]\n" \
"ds_read_b64_tr_b16 a[104:105], v215 offset:16384\n" \
"ds_read_b64_tr_b16 a[106:107], v215 offset:18432\n" \
"v_mfma_f32_16x16x32_bf16 v[24:27], a[116:119], a[76:79], v[24:27]\n" \
"ds_read_b128 a[72:75], v213 offset:256\n" \
"v_mfma_f32_16x16x32_bf16 v[40:43], a[112:115], a[80:83], v[40:43]\n" \
"s_add_u32 s48, s48, 0x1000\n" \
"ds_read_b128 a[76:79], v213 offset:2304\n" \
"v_mfma_f32_16x16x32_bf16 v[40:43], a[116:119], a[84:87], v[40:43]\n" \
"buffer_load_dwordx4 v217, s[36:39], s82 offen offset:1024 lds\n" \
"v_mfma_f32_16x16x32_bf16 v[44:47], a[120:123], a[80:83], v[44:47]\n" \
"s_sub_u32 s86, s86, 32768\n" \
"s_cselect_b32 s86, 131072, s86\n" \
"ds_read_b64_tr_b16 a[108:109], v215 offset:24576\n" \
"ds_read_b64_tr_b16 a[110:111], v215 offset:26624\n" \
"v_mfma_f32_16x16x32_bf16 v[44:47], a[124:127], a[84:87], v[44:47]\n" \
"buffer_load_dwordx4 v218, s[36:39], s82 offen offset:2048 lds\n" \
"v_mfma_f32_16x16x32_bf16 v[60:63], a[120:123], a[88:91], v[60:63]\n" \
"ds_read_b128 a[80:83], v213 offset:4096\n" \
"s_waitcnt lgkmcnt(4)\n" \
"v_mfma_f32_16x16x32_bf16 v[60:63], a[124:127], a[92:95], v[60:63]\n" \
"ds_read_b128 a[84:87], v213 offset:6144\n" \
"v_mfma_f32_16x16x32_bf16 v[56:59], a[112:115], a[88:91], v[56:59]\n" \
"buffer_load_dwordx4 v219, s[36:39], s82 offen offset:3072 lds\n" \
"v_mfma_f32_16x16x32_bf16 v[56:59], a[116:119], a[92:95], v[56:59]\n" \
"s_mov_b32 s72, 2\n" \
"s_nop 0\n" \
"s_nop 0\n" \
"Lo0_%=:\n" \
"v_mfma_f32_16x16x32_bf16 v[0:3], a[96:99], a[64:67], v[0:3]\n" \
"ds_read_b128 a[88:91], v213 offset:4352\n" \
"v_mfma_f32_16x16x32_bf16 v[0:3], a[100:103], a[68:71], v[0:3]\n" \
"s_waitcnt lgkmcnt(2)\n" \
"s_xor_b32 s82, s48, s49\n" \
"ds_read_b128 a[92:95], v213 offset:6400\n" \
"v_mfma_f32_16x16x32_bf16 v[4:7], a[104:107], a[64:67], v[4:7]\n" \
"s_cmp_lt_u32 s48, s50\n" \
"s_cselect_b32 s82, s82, s48\n" \
"ds_read_b64_tr_b16 a[112:113], v214 offset:16896\n" \
"v_mfma_f32_16x16x32_bf16 v[4:7], a[108:111], a[68:71], v[4:7]\n" \
"s_add_u32 s81, s82, s46\n" \
"ds_read_b64_tr_b16 a[114:115], v214 offset:18944\n" \
"v_mfma_f32_16x16x32_bf16 v[20:23], a[104:107], a[72:75], v[20:23]\n" \
"s_add_u32 m0, s86, s79\n" \
"ds_read_b64_tr_b16 a[116:117], v214 offset:25088\n" \
"v_mfma_f32_16x16x32_bf16 v[20:23], a[108:111], a[76:79], v[20:23]\n" \
"buffer_load_dwordx4 v234, s[56:59], s81 offen offset:0 sc1 lds\n" \
"v_mfma_f32_16x16x32_bf16 v[16:19], a[96:99], a[72:75], v[16:19]\n" \
"s_sub_u32 s85, s85, 32768\n" \
"s_cselect_b32 s85, 131072, s85\n" \
"ds_read_b64_tr_b16 a[118:119], v214 offset:27136\n" \
"v_mfma_f32_16x16x32_bf16 v[16:19], a[100:103], a[76:79], v[16:19]\n" \
"s_waitcnt lgkmcnt(3)\n" \
"s_lshl_b32 s82, s82, 5\n" \
"ds_read_b64_tr_b16 a[120:121], v215 offset:16896\n" \
"v_mfma_f32_16x16x32_bf16 v[32:35], a[96:99], a[80:83], v[32:35]\n" \
"s_add_u32 s82, s82, s47\n" \
"ds_read_b64_tr_b16 a[122:123], v215 offset:18944\n" \
"v_mfma_f32_16x16x32_bf16 v[32:35], a[100:103], a[84:87], v[32:35]\n" \
"buffer_load_dwordx4 v234, s[56:59], s81 offen offset:1024 sc1 lds\n" \
"v_mfma_f32_16x16x32_bf16 v[36:39], a[104:107], a[80:83], v[36:39]\n" \
"ds_read_b64_tr_b16 a[124:125], v215 offset:25088\n" \
"v_add_u32 v213, s85, v211\n" \
"v_mfma_f32_16x16x32_bf16 v[36:39], a[108:111], a[84:87], v[36:39]\n" \
"buffer_load_dwordx4 v234, s[56:59], s81 offen offset:2048 sc1 lds\n" \
"v_mfma_f32_16x16x32_bf16 v[52:55], a[104:107], a[88:91], v[52:55]\n" \
"ds_read_b64_tr_b16 a[126:127], v215 offset:27136\n" \
"v_mfma_f32_16x16x32_bf16 v[52:55], a[108:111], a[92:95], v[52:55]\n" \
"buffer_load_dwordx4 v234, s[56:59], s81 offen offset:3072 sc1 lds\n" \
"v_mfma_f32_16x16x32_bf16 v[48:51], a[96:99], a[88:91], v[48:51]\n" \
"v_add_u32 v214, s85, v212\n" \
"v_xor_b32 v215, 32, v214\n" \
"v_mfma_f32_16x16x32_bf16 v[48:51], a[100:103], a[92:95], v[48:51]\n" \
"s_waitcnt vmcnt(20) lgkmcnt(0)\n" \
"s_barrier\n" \
"v_mfma_f32_16x16x32_bf16 v[8:11], a[112:115], a[64:67], v[8:11]\n" \
"s_add_u32 m0, m0, 0x4000\n" \
"ds_read_b64_tr_b16 a[96:97], v214 offset:16384\n" \
"ds_read_b64_tr_b16 a[98:99], v214 offset:18432\n" \
"v_mfma_f32_16x16x32_bf16 v[8:11], a[116:119], a[68:71], v[8:11]\n" \
"buffer_load_dwordx4 v216, s[36:39], s82 offen offset:0 lds\n" \
"v_mfma_f32_16x16x32_bf16 v[12:15], a[120:123], a[64:67], v[12:15]\n" \
"ds_read_b64_tr_b16 a[100:101], v214 offset:24576\n" \
"ds_read_b64_tr_b16 a[102:103], v214 offset:26624\n" \
"v_mfma_f32_16x16x32_bf16 v[12:15], a[124:127], a[68:71], v[12:15]\n" \
"ds_read_b128 a[64:67], v213 offset:0\n" \
"v_mfma_f32_16x16x32_bf16 v[28:31], a[120:123], a[72:75], v[28:31]\n" \
"s_add_u32 s48, s48, 0x1000\n" \
"ds_read_b128 a[68:71], v213 offset:2048\n" \
"v_mfma_f32_16x16x32_bf16 v[28:31], a[124:127], a[76:79], v[28:31]\n" \
"buffer_load_dwordx4 v217, s[36:39], s82 offen offset:1024 lds\n" \
"v_mfma_f32_16x16x32_bf16 v[24:27], a[112:115], a[72:75], v[24:27]\n" \
"ds_read_b64_tr_b16 a[104:105], v215 offset:16384\n" \
"ds_read_b64_tr_b16 a[106:107], v215 offset:18432\n" \
"v_mfma_f32_16x16x32_bf16 v[24:27], a[116:119], a[76:79], v[24:27]\n" \
"ds_read_b128 a[72:75], v213 offset:256\n" \
"v_mfma_f32_16x16x32_bf16 v[40:43], a[112:115], a[80:83], v[40:43]\n" \
"s_sub_u32 s86, s86, 32768\n" \
"s_cselect_b32 s86, 131072, s86\n" \
"ds_read_b128 a[76:79], v213 offset:2304\n" \
"v_mfma_f32_16x16x32_bf16 v[40:43], a[116:119], a[84:87], v[40:43]\n" \
"buffer_load_dwordx4 v218, s[36:39], s82 offen offset:2048 lds\n" \
"v_mfma_f32_16x16x32_bf16 v[44:47], a[120:123], a[80:83], v[44:47]\n" \
"ds_read_b64_tr_b16 a[108:109], v215 offset:24576\n" \
"ds_read_b64_tr_b16 a[110:111], v215 offset:26624\n" \
"v_mfma_f32_16x16x32_bf16 v[44:47], a[124:127], a[84:87], v[44:47]\n" \
"buffer_load_dwordx4 v219, s[36:39], s82 offen offset:3072 lds\n" \
"v_mfma_f32_16x16x32_bf16 v[60:63], a[120:123], a[88:91], v[60:63]\n" \
"ds_read_b128 a[80:83], v213 offset:4096\n" \
"s_waitcnt lgkmcnt(4)\n" \
"v_mfma_f32_16x16x32_bf16 v[60:63], a[124:127], a[92:95], v[60:63]\n" \
"s_sub_u32 s72, s72, 1\n" \
"ds_read_b128 a[84:87], v213 offset:6144\n" \
"v_mfma_f32_16x16x32_bf16 v[56:59], a[112:115], a[88:91], v[56:59]\n" \
"v_mfma_f32_16x16x32_bf16 v[56:59], a[116:119], a[92:95], v[56:59]\n" \
"s_cbranch_scc0 Lo0_%=\n" \
"s_setpc_b64 s[44:45]\n" \
"Lor_%=:\n" \
"buffer_store_dword v230, v231, s[56:59], s12 offen sc1\n" \
"s_mov_b32 s72, s10\n" \
"s_call_b64 s[44:45], Lo0_%=\n" \
"buffer_load_dword v220, v221, s[56:59], s67 offen sc1\n" \
"s_mov_b32 s72, 1\n" \
"s_call_b64 s[44:45], Lo0_%=\n" \
"s_waitcnt vmcnt(16)\n" \
"v_cmp_eq_u32 vcc, s60, v220\n" \
"s_nop 1\n" \
"s_andn2_b32 s70, s74, vcc_lo\n" \
"s_cbranch_scc1 Lpl6_%=\n" \
"Lpr6_%=:\n" \
"s_cmp_eq_u32 s90, 0\n" \
"s_cselect_b32 s46, s46, s83\n" \
"s_cselect_b32 s47, s47, s84\n" \
"s_cselect_b32 s48, s48, 0\n" \
"s_cselect_b64 s[36:37], s[36:37], s[40:41]\n" \
"s_cselect_b32 s38, s38, s42\n" \
"s_mov_b32 s72, 3\n" \
"s_call_b64 s[44:45], Lo0_%=\n" \
"s_cmp_eq_u32 s90, 0\n" \
"s_cbranch_scc0 Lrole1_%=\n" \
"s_mov_b32 s72, s11\n" \
"s_call_b64 s[44:45], Lo0_%=\n" \
"buffer_load_dword v220, v241, s[56:59], s64 offen sc1\n" \
"s_mov_b32 s72, 1\n" \
"s_call_b64 s[44:45], Lo0_%=\n" \
"v_mfma_f32_16x16x32_bf16 v[0:3], a[96:99], a[64:67], v[0:3]\n" \
"s_waitcnt lgkmcnt(2)\n" \
"ds_read_b128 a[88:91], v213 offset:4352\n" \
"v_mov_b32 v74, 0x3f80\n" \
"v_mfma_f32_16x16x32_bf16 v[0:3], a[100:103], a[68:71], v[0:3]\n" \
"s_add_u32 s48, s86, 32768\n" \
"s_cmp_eq_u32 s48, 163840\n" \
"s_cselect_b32 s48, 0, s48\n" \
"ds_read_b128 a[92:95], v213 offset:6400\n" \
"v_mfma_f32_16x16x32_bf16 v[4:7], a[104:107], a[64:67], v[4:7]\n" \
"ds_read_b64_tr_b16 a[112:113], v214 offset:16896\n" \
"v_mov_b32 v64, 0\n" \
"v_mfma_f32_16x16x32_bf16 v[4:7], a[108:111], a[68:71], v[4:7]\n" \
"s_add_u32 s49, s48, 32768\n" \
"s_cmp_eq_u32 s49, 163840\n" \
"s_cselect_b32 s49, 0, s49\n" \
"ds_read_b64_tr_b16 a[114:115], v214 offset:18944\n" \
"v_mfma_f32_16x16x32_bf16 v[20:23], a[104:107], a[72:75], v[20:23]\n" \
"ds_read_b64_tr_b16 a[116:117], v214 offset:25088\n" \
"v_add_u32 v224, s49, v211\n" \
"v_mfma_f32_16x16x32_bf16 v[20:23], a[108:111], a[76:79], v[20:23]\n" \
"v_add_u32 v225, s49, v212\n" \
"v_xor_b32 v226, 32, v225\n" \
"v_mfma_f32_16x16x32_bf16 v[16:19], a[96:99], a[72:75], v[16:19]\n" \
"s_sub_u32 s85, s85, 32768\n" \
"s_cselect_b32 s85, 131072, s85\n" \
"ds_read_b64_tr_b16 a[118:119], v214 offset:27136\n" \
"v_mfma_f32_16x16x32_bf16 v[16:19], a[100:103], a[76:79], v[16:19]\n" \
"s_waitcnt lgkmcnt(3)\n" \
"ds_read_b64_tr_b16 a[120:121], v215 offset:16896\n" \
"v_add_u32 v227, s48, v211\n" \
"v_mfma_f32_16x16x32_bf16 v[32:35], a[96:99], a[80:83], v[32:35]\n" \
"ds_read_b64_tr_b16 a[122:123], v215 offset:18944\n" \
"v_add_u32 v228, s48, v212\n" \
"v_mfma_f32_16x16x32_bf16 v[32:35], a[100:103], a[84:87], v[32:35]\n" \
"v_xor_b32 v229, 32, v228\n" \
"v_mov_b32 v65, 0\n" \
"v_mfma_f32_16x16x32_bf16 v[36:39], a[104:107], a[80:83], v[36:39]\n" \
"ds_read_b64_tr_b16 a[124:125], v215 offset:25088\n" \
"v_add_u32 v213, s85, v211\n" \
"v_mfma_f32_16x16x32_bf16 v[36:39], a[108:111], a[84:87], v[36:39]\n" \
"v_mov_b32 v68, 0\n" \
"v_mov_b32 v69, 0\n" \
"v_mfma_f32_16x16x32_bf16 v[52:55], a[104:107], a[88:91], v[52:55]\n" \
"ds_read_b64_tr_b16 a[126:127], v215 offset:27136\n" \
"v_lshrrev_b32 v72, 8, v234\n" \
"v_mfma_f32_16x16x32_bf16 v[52:55], a[108:111], a[92:95], v[52:55]\n" \
"v_bfe_u32 v73, v234, 6, 2\n" \
"v_cmp_eq_u32 s[16:17], v72, v73\n" \
"v_mfma_f32_16x16x32_bf16 v[48:51], a[96:99], a[88:91], v[48:51]\n" \
"v_add_u32 v214, s85, v212\n" \
"v_xor_b32 v215, 32, v214\n" \
"v_mfma_f32_16x16x32_bf16 v[48:51], a[100:103], a[92:95], v[48:51]\n" \
"s_waitcnt vmcnt(8) lgkmcnt(0)\n" \
"s_barrier\n" \
"s_waitcnt vmcnt(16)\n" \
"v_cmp_eq_u32 vcc, s60, v220\n" \
"s_nop 1\n" \
"s_andn2_b32 s70, s69, vcc_lo\n" \
"s_cbranch_scc1 Lpl7_%=\n" \
"Lpr7_%=:\n" \
"v_mfma_f32_16x16x32_bf16 v[8:11], a[112:115], a[64:67], v[8:11]\n" \
"s_add_u32 s71, s87, 0\n" \
"ds_read_b128 a[0:3], v213 offset:0\n" \
"ds_read_b128 a[4:7], v213 offset:2048\n" \
"v_mfma_f32_16x16x32_bf16 v[8:11], a[116:119], a[68:71], v[8:11]\n" \
"buffer_load_dwordx4 v[176:179], v234, s[56:59], s71 offen offset:0 sc1\n" \
"v_mfma_f32_16x16x32_bf16 v[12:15], a[120:123], a[64:67], v[12:15]\n" \
"ds_read_b64_tr_b16 a[128:129], v214 offset:16384\n" \
"ds_read_b64_tr_b16 a[130:131], v214 offset:18432\n" \
"v_mfma_f32_16x16x32_bf16 v[12:15], a[124:127], a[68:71], v[12:15]\n" \
"buffer_load_dwordx4 v[180:183], v234, s[56:59], s71 offen offset:1024 sc1\n" \
"v_mfma_f32_16x16x32_bf16 v[28:31], a[120:123], a[72:75], v[28:31]\n" \
"ds_read_b64_tr_b16 a[132:133], v214 offset:24576\n" \
"ds_read_b64_tr_b16 a[134:135], v214 offset:26624\n" \
"v_mfma_f32_16x16x32_bf16 v[28:31], a[124:127], a[76:79], v[28:31]\n" \
"buffer_load_dwordx4 v[184:187], v234, s[56:59], s71 offen offset:2048 sc1\n" \
"v_mfma_f32_16x16x32_bf16 v[24:27], a[112:115], a[72:75], v[24:27]\n" \
"ds_read_b64_tr_b16 a[136:137], v215 offset:16384\n" \
"ds_read_b64_tr_b16 a[138:139], v215 offset:18432\n" \
"v_mfma_f32_16x16x32_bf16 v[24:27], a[116:119], a[76:79], v[24:27]\n" \
"buffer_load_dwordx4 v[188:191], v234, s[56:59], s71 offen offset:3072 sc1\n" \
"v_mfma_f32_16x16x32_bf16 v[40:43], a[112:115], a[80:83], v[40:43]\n" \
"ds_read_b64_tr_b16 a[140:141], v215 offset:24576\n" \
"ds_read_b64_tr_b16 a[142:143], v215 offset:26624\n" \
"v_mfma_f32_16x16x32_bf16 v[40:43], a[116:119], a[84:87], v[40:43]\n" \
"ds_read_b64_tr_b16 a[144:145], v214 offset:16896\n" \
"ds_read_b64_tr_b16 a[146:147], v214 offset:18944\n" \
"v_mfma_f32_16x16x32_bf16 v[44:47], a[120:123], a[80:83], v[44:47]\n" \
"ds_read_b64_tr_b16 a[148:149], v214 offset:25088\n" \
"ds_read_b64_tr_b16 a[150:151], v214 offset:27136\n" \
"v_mfma_f32_16x16x32_bf16 v[44:47], a[124:127], a[84:87], v[44:47]\n" \
"ds_read_b64_tr_b16 a[152:153], v215 offset:16896\n" \
"ds_read_b64_tr_b16 a[154:155], v215 offset:18944\n" \
"v_mfma_f32_16x16x32_bf16 v[60:63], a[120:123], a[88:91], v[60:63]\n" \
"ds_read_b64_tr_b16 a[156:157], v215 offset:25088\n" \
"ds_read_b64_tr_b16 a[158:159], v215 offset:27136\n" \
"v_mfma_f32_16x16x32_bf16 v[60:63], a[124:127], a[92:95], v[60:63]\n" \
"ds_read_b128 a[24:27], v213 offset:256\n" \
"ds_read_b128 a[28:31], v213 offset:2304\n" \
"v_mfma_f32_16x16x32_bf16 v[56:59], a[112:115], a[88:91], v[56:59]\n" \
"ds_read_b128 a[8:11], v224 offset:0\n" \
"ds_read_b128 a[12:15], v224 offset:2048\n" \
"v_mfma_f32_16x16x32_bf16 v[56:59], a[116:119], a[92:95], v[56:59]\n" \
"s_waitcnt lgkmcnt(8)\n" \
"ds_read_b64_tr_b16 a[160:161], v225 offset:16384\n" \
"ds_read_b64_tr_b16 a[162:163], v225 offset:18432\n" \
"v_mfma_f32_16x16x32_bf16 v[0:3], a[128:131], a[0:3], v[0:3]\n" \
"ds_read_b64_tr_b16 a[164:165], v225 offset:24576\n" \
"ds_read_b64_tr_b16 a[166:167], v225 offset:26624\n" \
"v_mfma_f32_16x16x32_bf16 v[0:3], a[132:135], a[4:7], v[0:3]\n" \
"ds_read_b64_tr_b16 a[168:169], v226 offset:16384\n" \
"ds_read_b64_tr_b16 a[170:171], v226 offset:18432\n" \
"v_mfma_f32_16x16x32_bf16 v[4:7], a[136:139], a[0:3], v[4:7]\n" \
"s_mov_b32 s70, 0x20000000\n" \
"ds_read_b64_tr_b16 a[172:173], v226 offset:24576\n" \
"ds_read_b64_tr_b16 a[174:175], v226 offset:26624\n" \
"v_mfma_f32_16x16x32_bf16 v[4:7], a[140:143], a[4:7], v[4:7]\n" \
"s_cmp_eq_u32 s64, 65616896\n" \
"s_cselect_b32 s70, 65593344, s70\n" \
"ds_read_b64_tr_b16 a[176:177], v225 offset:16896\n" \
"ds_read_b64_tr_b16 a[178:179], v225 offset:18944\n" \
"buffer_store_dword v230, v231, s[56:59], s70 offen sc1\n" \
"v_mfma_f32_16x16x32_bf16 v[8:11], a[144:147], a[0:3], v[8:11]\n" \
"s_waitcnt lgkmcnt(8)\n" \
"ds_read_b64_tr_b16 a[180:181], v225 offset:25088\n" \
"ds_read_b64_tr_b16 a[182:183], v225 offset:27136\n" \
"v_mfma_f32_16x16x32_bf16 v[8:11], a[148:151], a[4:7], v[8:11]\n" \
"s_sub_u32 s68, s86, 32768\n" \
"s_cselect_b32 s68, 131072, s68\n" \
"ds_read_b64_tr_b16 a[184:185], v226 offset:16896\n" \
"ds_read_b64_tr_b16 a[186:187], v226 offset:18944\n" \
"v_mfma_f32_16x16x32_bf16 v[12:15], a[152:155], a[0:3], v[12:15]\n" \
"ds_read_b64_tr_b16 a[188:189], v226 offset:25088\n" \
"ds_read_b64_tr_b16 a[190:191], v226 offset:27136\n" \
"v_mfma_f32_16x16x32_bf16 v[12:15], a[156:159], a[4:7], v[12:15]\n" \
"s_cmp_lt_u32 s61, 2\n" \
"s_cselect_b32 s68, s86, s68\n" \
"v_and_b32 v72, 16, v234\n" \
"v_lshlrev_b32 v72, v72, v74\n" \
"v_mfma_f32_16x16x32_bf16 v[28:31], a[152:155], a[24:27], v[28:31]\n" \
"s_and_b32 s50, s61, 1\n" \
"v_cndmask_b32 v72, 0, v72, s[16:17]\n" \
"v_bfe_i32 v73, v234, 5, 1\n" \
"v_mfma_f32_16x16x32_bf16 v[28:31], a[156:159], a[28:31], v[28:31]\n" \
"s_mul_i32 s50, s50, 9216\n" \
"v_and_b32 v67, v72, v73\n" \
"v_xor_b32 v66, v72, v67\n" \
"v_mfma_f32_16x16x32_bf16 v[24:27], a[144:147], a[24:27], v[24:27]\n" \
"s_add_u32 s68, s68, s50\n" \
"s_add_u32 s68, s68, %3\n" \
"v_lshrrev_b32 v132, 4, v234\n" \
"v_mfma_f32_16x16x32_bf16 v[24:27], a[148:151], a[28:31], v[24:27]\n" \
"v_and_b32 v133, 15, v132\n" \
"v_mul_u32_u24 v128, 144, v133\n" \
"v_lshrrev_b32 v133, 4, v132\n" \
"v_mfma_f32_16x16x32_bf16 v[20:23], a[136:139], a[24:27], v[20:23]\n" \
"s_waitcnt vmcnt(5)\n" \
"s_barrier\n" \
"v_mfma_f32_16x16x32_bf16 v[20:23], a[140:143], a[28:31], v[20:23]\n" \
"s_add_u32 s71, s87, 4096\n" \
"ds_read_b128 a[32:35], v224 offset:256\n" \
"ds_read_b128 a[36:39], v224 offset:2304\n" \
"v_mfma_f32_16x16x32_bf16 v[16:19], a[128:131], a[24:27], v[16:19]\n" \
"buffer_load_dwordx4 v[192:195], v234, s[56:59], s71 offen offset:0 sc1\n" \
"v_mfma_f32_16x16x32_bf16 v[16:19], a[132:135], a[28:31], v[16:19]\n" \
"s_waitcnt lgkmcnt(2)\n" \
"ds_read_b128 a[16:19], v227 offset:0\n" \
"ds_read_b128 a[20:23], v227 offset:2048\n" \
"v_mfma_f32_16x16x32_bf16 v[0:3], a[160:163], a[8:11], v[0:3]\n" \
"ds_read_b64_tr_b16 a[192:193], v228 offset:16384\n" \
"ds_read_b64_tr_b16 a[194:195], v228 offset:18432\n" \
"v_mfma_f32_16x16x32_bf16 v[0:3], a[164:167], a[12:15], v[0:3]\n" \
"buffer_load_dwordx4 v[196:199], v234, s[56:59], s71 offen offset:1024 sc1\n" \
"v_mfma_f32_16x16x32_bf16 v[4:7], a[168:171], a[8:11], v[4:7]\n" \
"ds_read_b64_tr_b16 a[196:197], v228 offset:24576\n" \
"ds_read_b64_tr_b16 a[198:199], v228 offset:26624\n" \
"v_mfma_f32_16x16x32_bf16 v[4:7], a[172:175], a[12:15], v[4:7]\n" \
"ds_read_b64_tr_b16 a[200:201], v229 offset:16384\n" \
"ds_read_b64_tr_b16 a[202:203], v229 offset:18432\n" \
"v_mfma_f32_16x16x32_bf16 v[8:11], a[176:179], a[8:11], v[8:11]\n" \
"buffer_load_dwordx4 v[200:203], v234, s[56:59], s71 offen offset:2048 sc1\n" \
"v_mfma_f32_16x16x32_bf16 v[8:11], a[180:183], a[12:15], v[8:11]\n" \
"ds_read_b64_tr_b16 a[204:205], v229 offset:24576\n" \
"ds_read_b64_tr_b16 a[206:207], v229 offset:26624\n" \
"v_lshl_add_u32 v128, v133, 3, v128\n" \
"v_mfma_f32_16x16x32_bf16 v[12:15], a[184:187], a[8:11], v[12:15]\n" \
"s_waitcnt lgkmcnt(6)\n" \
"ds_read_b64_tr_b16 a[208:209], v228 offset:16896\n" \
"ds_read_b64_tr_b16 a[210:211], v228 offset:18944\n" \
"v_mfma_f32_16x16x32_bf16 v[12:15], a[188:191], a[12:15], v[12:15]\n" \
"buffer_load_dwordx4 v[204:207], v234, s[56:59], s71 offen offset:3072 sc1\n" \
"v_mfma_f32_16x16x32_bf16 v[28:31], a[184:187], a[32:35], v[28:31]\n" \
"ds_read_b64_tr_b16 a[212:213], v228 offset:25088\n" \
"ds_read_b64_tr_b16 a[214:215], v228 offset:27136\n" \
"v_mfma_f32_16x16x32_bf16 v[28:31], a[188:191], a[36:39], v[28:31]\n" \
"ds_read_b64_tr_b16 a[216:217], v229 offset:16896\n" \
"ds_read_b64_tr_b16 a[218:219], v229 offset:18944\n" \
"v_mfma_f32_16x16x32_bf16 v[24:27], a[176:179], a[32:35], v[24:27]\n" \
"ds_read_b64_tr_b16 a[220:221], v229 offset:25088\n" \
"ds_read_b64_tr_b16 a[222:223], v229 offset:27136\n" \
"v_mfma_f32_16x16x32_bf16 v[24:27], a[180:183], a[36:39], v[24:27]\n" \
"ds_read_b128 a[40:43], v227 offset:256\n" \
"ds_read_b128 a[44:47], v227 offset:2304\n" \
"v_mfma_f32_16x16x32_bf16 v[20:23], a[168:171], a[32:35], v[20:23]\n" \
"ds_read_b128 a[248:251], v213 offset:4096\n" \
"ds_read_b128 a[252:255], v213 offset:6144\n" \
"v_mfma_f32_16x16x32_bf16 v[20:23], a[172:175], a[36:39], v[20:23]\n" \
"ds_read_b128 a[48:51], v224 offset:4096\n" \
"ds_read_b128 a[52:55], v224 offset:6144\n" \
"v_mfma_f32_16x16x32_bf16 v[16:19], a[160:163], a[32:35], v[16:19]\n" \
"ds_read_b128 a[56:59], v227 offset:4096\n" \
"ds_read_b128 a[60:63], v227 offset:6144\n" \
"v_mfma_f32_16x16x32_bf16 v[16:19], a[164:167], a[36:39], v[16:19]\n" \
"s_waitcnt lgkmcnt(8)\n" \
"ds_read_b128 a[224:227], v213 offset:4352\n" \
"ds_read_b128 a[228:231], v213 offset:6400\n" \
"v_add_u32 v128, s68, v128\n" \
"v_mfma_f32_16x16x32_bf16 v[0:3], a[192:195], a[16:19], v[0:3]\n" \
"s_waitcnt vmcnt(7)\n" \
"ds_read_b128 a[232:235], v224 offset:4352\n" \
"ds_read_b128 a[236:239], v224 offset:6400\n" \
"v_mfma_f32_16x16x32_bf16 v[0:3], a[196:199], a[20:23], v[0:3]\n" \
"ds_read_b128 a[240:243], v227 offset:4352\n" \
"ds_read_b128 a[244:247], v227 offset:6400\n" \
"v_mfma_f32_16x16x32_bf16 v[0:3], v[66:69], v[176:179], v[0:3]\n" \
"v_lshrrev_b32 v133, 3, v132\n" \
"v_and_b32 v134, 7, v132\n" \
"v_mfma_f32_16x16x32_bf16 v[4:7], a[200:203], a[16:19], v[4:7]\n" \
"v_mul_u32_u24 v129, 144, v133\n" \
"v_lshl_add_u32 v129, v134, 4, v129\n" \
"v_mfma_f32_16x16x32_bf16 v[4:7], a[204:207], a[20:23], v[4:7]\n" \
"v_add_u32 v129, s68, v129\n" \
"v_lshlrev_b32 v134, 4, v134\n" \
"v_mfma_f32_16x16x32_bf16 v[4:7], v[64:67], v[176:179], v[4:7]\n" \
"v_lshl_add_u32 v130, v133, 12, v134\n" \
"v_mfma_f32_16x16x32_bf16 v[8:11], a[208:211], a[16:19], v[8:11]\n" \
"v_mul_f32 v0, 4.0, v0\n" \
"v_mul_f32 v1, 4.0, v1\n" \
"v_mul_f32 v2, 4.0, v2\n" \
"v_mul_f32 v3, 4.0, v3\n" \
"v_cvt_pk_bf16_f32 v136, v0, v1\n" \
"v_cvt_pk_bf16_f32 v137, v2, v3\n" \
"v_mfma_f32_16x16x32_bf16 v[8:11], a[212:215], a[20:23], v[8:11]\n" \
"v_mfma_f32_16x16x32_bf16 v[8:11], v[66:69], v[180:183], v[8:11]\n" \
"ds_write_b64 v128, v[136:137] offset:0\n" \
"v_mfma_f32_16x16x32_bf16 v[12:15], a[216:219], a[16:19], v[12:15]\n" \
"v_mul_f32 v4, 4.0, v4\n" \
"v_mul_f32 v5, 4.0, v5\n" \
"v_mfma_f32_16x16x32_bf16 v[12:15], a[220:223], a[20:23], v[12:15]\n" \
"v_mul_f32 v6, 4.0, v6\n" \
"v_mul_f32 v7, 4.0, v7\n" \
"v_mfma_f32_16x16x32_bf16 v[12:15], v[64:67], v[180:183], v[12:15]\n" \
"s_waitcnt lgkmcnt(1)\n" \
"v_cvt_pk_bf16_f32 v138, v4, v5\n" \
"v_cvt_pk_bf16_f32 v139, v6, v7\n" \
"v_mfma_f32_16x16x32_bf16 v[16:19], a[192:195], a[40:43], v[16:19]\n" \
"s_waitcnt vmcnt(5)\n" \
"v_mfma_f32_16x16x32_bf16 v[16:19], a[196:199], a[44:47], v[16:19]\n" \
"v_mfma_f32_16x16x32_bf16 v[16:19], v[66:69], v[184:187], v[16:19]\n" \
"ds_write_b64 v128, v[138:139] offset:32\n" \
"v_mul_f32 v8, 4.0, v8\n" \
"v_mfma_f32_16x16x32_bf16 v[20:23], a[200:203], a[40:43], v[20:23]\n" \
"v_mul_f32 v9, 4.0, v9\n" \
"v_mul_f32 v10, 4.0, v10\n" \
"v_mfma_f32_16x16x32_bf16 v[20:23], a[204:207], a[44:47], v[20:23]\n" \
"v_mul_f32 v11, 4.0, v11\n" \
"v_cvt_pk_bf16_f32 v136, v8, v9\n" \
"v_mfma_f32_16x16x32_bf16 v[20:23], v[64:67], v[184:187], v[20:23]\n" \
"v_cvt_pk_bf16_f32 v137, v10, v11\n" \
"v_mfma_f32_16x16x32_bf16 v[24:27], a[208:211], a[40:43], v[24:27]\n" \
"v_mfma_f32_16x16x32_bf16 v[24:27], a[212:215], a[44:47], v[24:27]\n" \
"v_mfma_f32_16x16x32_bf16 v[24:27], v[66:69], v[188:191], v[24:27]\n" \
"ds_write_b64 v128, v[136:137] offset:64\n" \
"v_mul_f32 v12, 4.0, v12\n" \
"v_mfma_f32_16x16x32_bf16 v[28:31], a[216:219], a[40:43], v[28:31]\n" \
"v_mul_f32 v13, 4.0, v13\n" \
"v_mul_f32 v14, 4.0, v14\n" \
"v_mfma_f32_16x16x32_bf16 v[28:31], a[220:223], a[44:47], v[28:31]\n" \
"v_mul_f32 v15, 4.0, v15\n" \
"v_cvt_pk_bf16_f32 v138, v12, v13\n" \
"v_mfma_f32_16x16x32_bf16 v[28:31], v[64:67], v[188:191], v[28:31]\n" \
"v_cvt_pk_bf16_f32 v139, v14, v15\n" \
"v_mfma_f32_16x16x32_bf16 v[32:35], a[128:131], a[248:251], v[32:35]\n" \
"ds_write_b64 v128, v[138:139] offset:96\n" \
"v_mfma_f32_16x16x32_bf16 v[32:35], a[132:135], a[252:255], v[32:35]\n" \
"ds_read_b128 v[140:143], v129 offset:0\n" \
"ds_read_b128 v[144:147], v129 offset:1152\n" \
"v_mfma_f32_16x16x32_bf16 v[32:35], a[160:163], a[48:51], v[32:35]\n" \
"s_waitcnt vmcnt(2)\n" \
"v_mul_f32 v16, 4.0, v16\n" \
"v_mul_f32 v17, 4.0, v17\n" \
"v_mfma_f32_16x16x32_bf16 v[32:35], a[164:167], a[52:55], v[32:35]\n" \
"v_mul_f32 v18, 4.0, v18\n" \
"v_mul_f32 v19, 4.0, v19\n" \
"v_mfma_f32_16x16x32_bf16 v[32:35], v[66:69], v[192:195], v[32:35]\n" \
"s_add_u32 s70, s88, 0\n" \
"s_waitcnt lgkmcnt(1)\n" \
"v_cvt_pk_bf16_f32 v136, v16, v17\n" \
"v_mfma_f32_16x16x32_bf16 v[32:35], a[192:195], a[56:59], v[32:35]\n" \
"buffer_store_dwordx4 v[140:143], v130, s[52:55], s70 offen sc1\n" \
"v_cvt_pk_bf16_f32 v137, v18, v19\n" \
"v_mfma_f32_16x16x32_bf16 v[32:35], a[196:199], a[60:63], v[32:35]\n" \
"ds_write_b64 v128, v[136:137] offset:2304\n" \
"v_mfma_f32_16x16x32_bf16 v[36:39], a[204:207], a[60:63], v[36:39]\n" \
"s_add_u32 s70, s88, 32768\n" \
"s_waitcnt lgkmcnt(1)\n" \
"v_mul_f32 v20, 4.0, v20\n" \
"v_mfma_f32_16x16x32_bf16 v[36:39], a[200:203], a[56:59], v[36:39]\n" \
"buffer_store_dwordx4 v[144:147], v130, s[52:55], s70 offen sc1\n" \
"v_mfma_f32_16x16x32_bf16 v[36:39], a[172:175], a[52:55], v[36:39]\n" \
"v_mul_f32 v21, 4.0, v21\n" \
"v_mul_f32 v22, 4.0, v22\n" \
"v_mfma_f32_16x16x32_bf16 v[36:39], a[168:171], a[48:51], v[36:39]\n" \
"v_mul_f32 v23, 4.0, v23\n" \
"v_cvt_pk_bf16_f32 v138, v20, v21\n" \
"v_mfma_f32_16x16x32_bf16 v[36:39], v[64:67], v[192:195], v[36:39]\n" \
"v_cvt_pk_bf16_f32 v139, v22, v23\n" \
"v_mfma_f32_16x16x32_bf16 v[36:39], a[140:143], a[252:255], v[36:39]\n" \
"v_mfma_f32_16x16x32_bf16 v[36:39], a[136:139], a[248:251], v[36:39]\n" \
"ds_write_b64 v128, v[138:139] offset:2336\n" \
"v_mul_f32 v24, 4.0, v24\n" \
"v_mul_f32 v25, 4.0, v25\n" \
"v_mfma_f32_16x16x32_bf16 v[40:43], a[144:147], a[248:251], v[40:43]\n" \
"v_mul_f32 v26, 4.0, v26\n" \
"v_mul_f32 v27, 4.0, v27\n" \
"v_mfma_f32_16x16x32_bf16 v[40:43], a[148:151], a[252:255], v[40:43]\n" \
"v_cvt_pk_bf16_f32 v136, v24, v25\n" \
"v_cvt_pk_bf16_f32 v137, v26, v27\n" \
"v_mfma_f32_16x16x32_bf16 v[40:43], a[176:179], a[48:51], v[40:43]\n" \
"ds_write_b64 v128, v[136:137] offset:2368\n" \
"v_mfma_f32_16x16x32_bf16 v[40:43], a[180:183], a[52:55], v[40:43]\n" \
"v_mul_f32 v28, 4.0, v28\n" \
"v_mul_f32 v29, 4.0, v29\n" \
"v_mfma_f32_16x16x32_bf16 v[40:43], v[66:69], v[196:199], v[40:43]\n" \
"v_mul_f32 v30, 4.0, v30\n" \
"v_mul_f32 v31, 4.0, v31\n" \
"v_mfma_f32_16x16x32_bf16 v[40:43], a[208:211], a[56:59], v[40:43]\n" \
"v_cvt_pk_bf16_f32 v138, v28, v29\n" \
"v_cvt_pk_bf16_f32 v139, v30, v31\n" \
"v_mfma_f32_16x16x32_bf16 v[40:43], a[212:215], a[60:63], v[40:43]\n" \
"Lpg10801_%=:\n" \
"s_cbranch_execz Lcb1_%=\n" \
"ds_write_b64 v128, v[138:139] offset:2400\n" \
"v_mfma_f32_16x16x32_bf16 v[44:47], a[220:223], a[60:63], v[44:47]\n" \
"ds_read_b128 v[148:151], v129 offset:2304\n" \
"ds_read_b128 v[152:155], v129 offset:3456\n" \
"v_mfma_f32_16x16x32_bf16 v[44:47], a[216:219], a[56:59], v[44:47]\n" \
"v_mul_f32 v32, 4.0, v32\n" \
"v_mul_f32 v33, 4.0, v33\n" \
"v_mfma_f32_16x16x32_bf16 v[44:47], a[188:191], a[52:55], v[44:47]\n" \
"v_mul_f32 v34, 4.0, v34\n" \
"v_mul_f32 v35, 4.0, v35\n" \
"v_mfma_f32_16x16x32_bf16 v[44:47], a[184:187], a[48:51], v[44:47]\n" \
"s_add_u32 s70, s88, 65536\n" \
"s_waitcnt lgkmcnt(1)\n" \
"v_cvt_pk_bf16_f32 v136, v32, v33\n" \
"v_mfma_f32_16x16x32_bf16 v[44:47], v[64:67], v[196:199], v[44:47]\n" \
"buffer_store_dwordx4 v[148:151], v130, s[52:55], s70 offen sc1\n" \
"v_cvt_pk_bf16_f32 v137, v34, v35\n" \
"v_mfma_f32_16x16x32_bf16 v[44:47], a[156:159], a[252:255], v[44:47]\n" \
"v_mfma_f32_16x16x32_bf16 v[44:47], a[152:155], a[248:251], v[44:47]\n" \
"s_add_u32 s70, s88, 98304\n" \
"s_waitcnt lgkmcnt(0)\n" \
"ds_write_b64 v128, v[136:137] offset:4608\n" \
"v_mul_f32 v36, 4.0, v36\n" \
"v_mfma_f32_16x16x32_bf16 v[48:51], a[128:131], a[224:227], v[48:51]\n" \
"buffer_store_dwordx4 v[152:155], v130, s[52:55], s70 offen sc1\n" \
"v_mul_f32 v37, 4.0, v37\n" \
"v_mfma_f32_16x16x32_bf16 v[48:51], a[132:135], a[228:231], v[48:51]\n" \
"v_mul_f32 v38, 4.0, v38\n" \
"v_mul_f32 v39, 4.0, v39\n" \
"v_mfma_f32_16x16x32_bf16 v[48:51], a[160:163], a[232:235], v[48:51]\n" \
"s_waitcnt vmcnt(4)\n" \
"v_cvt_pk_bf16_f32 v138, v36, v37\n" \
"v_cvt_pk_bf16_f32 v139, v38, v39\n" \
"v_mfma_f32_16x16x32_bf16 v[48:51], a[164:167], a[236:239], v[48:51]\n" \
"ds_write_b64 v128, v[138:139] offset:4640\n" \
"v_mul_f32 v40, 4.0, v40\n" \
"v_mfma_f32_16x16x32_bf16 v[48:51], v[66:69], v[200:203], v[48:51]\n" \
"v_mul_f32 v41, 4.0, v41\n" \
"v_mul_f32 v42, 4.0, v42\n" \
"v_mfma_f32_16x16x32_bf16 v[48:51], a[192:195], a[240:243], v[48:51]\n" \
"v_mul_f32 v43, 4.0, v43\n" \
"v_cvt_pk_bf16_f32 v136, v40, v41\n" \
"v_mfma_f32_16x16x32_bf16 v[48:51], a[196:199], a[244:247], v[48:51]\n" \
"v_cvt_pk_bf16_f32 v137, v42, v43\n" \
"v_mfma_f32_16x16x32_bf16 v[52:55], a[204:207], a[244:247], v[52:55]\n" \
"ds_write_b64 v128, v[136:137] offset:4672\n" \
"v_mul_f32 v44, 4.0, v44\n" \
"v_mul_f32 v45, 4.0, v45\n" \
"v_mfma_f32_16x16x32_bf16 v[52:55], a[200:203], a[240:243], v[52:55]\n" \
"v_mul_f32 v46, 4.0, v46\n" \
"v_mul_f32 v47, 4.0, v47\n" \
"v_mfma_f32_16x16x32_bf16 v[52:55], a[172:175], a[236:239], v[52:55]\n" \
"v_cvt_pk_bf16_f32 v138, v44, v45\n" \
"v_cvt_pk_bf16_f32 v139, v46, v47\n" \
"v_mfma_f32_16x16x32_bf16 v[52:55], a[168:171], a[232:235], v[52:55]\n" \
"ds_write_b64 v128, v[138:139] offset:4704\n" \
"v_mfma_f32_16x16x32_bf16 v[52:55], v[64:67], v[200:203], v[52:55]\n" \
"ds_read_b128 v[156:159], v129 offset:4608\n" \
"ds_read_b128 v[160:163], v129 offset:5760\n" \
"v_mfma_f32_16x16x32_bf16 v[52:55], a[140:143], a[228:231], v[52:55]\n" \
"v_mul_f32 v48, 4.0, v48\n" \
"v_mul_f32 v49, 4.0, v49\n" \
"v_mfma_f32_16x16x32_bf16 v[52:55], a[136:139], a[224:227], v[52:55]\n" \
"v_mul_f32 v50, 4.0, v50\n" \
"v_mul_f32 v51, 4.0, v51\n" \
"v_mfma_f32_16x16x32_bf16 v[56:59], a[144:147], a[224:227], v[56:59]\n" \
"s_add_u32 s70, s88, 131072\n" \
"s_waitcnt lgkmcnt(1)\n" \
"v_cvt_pk_bf16_f32 v136, v48, v49\n" \
"v_mfma_f32_16x16x32_bf16 v[56:59], a[148:151], a[228:231], v[56:59]\n" \
"buffer_store_dwordx4 v[156:159], v130, s[52:55], s70 offen sc1\n" \
"v_cvt_pk_bf16_f32 v137, v50, v51\n" \
"v_mfma_f32_16x16x32_bf16 v[56:59], a[176:179], a[232:235], v[56:59]\n" \
"ds_write_b64 v128, v[136:137] offset:6912\n" \
"v_mfma_f32_16x16x32_bf16 v[56:59], a[180:183], a[236:239], v[56:59]\n" \
"s_add_u32 s70, s88, 163840\n" \
"s_waitcnt lgkmcnt(1)\n" \
"v_mul_f32 v52, 4.0, v52\n" \
"v_mfma_f32_16x16x32_bf16 v[56:59], v[66:69], v[204:207], v[56:59]\n" \
"buffer_store_dwordx4 v[160:163], v130, s[52:55], s70 offen sc1\n" \
"v_mfma_f32_16x16x32_bf16 v[56:59], a[208:211], a[240:243], v[56:59]\n" \
"v_mul_f32 v53, 4.0, v53\n" \
"v_mul_f32 v54, 4.0, v54\n" \
"v_mfma_f32_16x16x32_bf16 v[56:59], a[212:215], a[244:247], v[56:59]\n" \
"v_mul_f32 v55, 4.0, v55\n" \
"v_cvt_pk_bf16_f32 v138, v52, v53\n" \
"v_cvt_pk_bf16_f32 v139, v54, v55\n" \
"v_mfma_f32_16x16x32_bf16 v[60:63], a[220:223], a[244:247], v[60:63]\n" \
"ds_write_b64 v128, v[138:139] offset:6944\n" \
"v_mfma_f32_16x16x32_bf16 v[60:63], a[216:219], a[240:243], v[60:63]\n" \
"v_mfma_f32_16x16x32_bf16 v[60:63], a[188:191], a[236:239], v[60:63]\n" \
"v_mfma_f32_16x16x32_bf16 v[60:63], a[184:187], a[232:235], v[60:63]\n" \
"v_mul_f32 v56, 4.0, v56\n" \
"v_mul_f32 v57, 4.0, v57\n" \
"v_mfma_f32_16x16x32_bf16 v[60:63], v[64:67], v[204:207], v[60:63]\n" \
"v_mul_f32 v58, 4.0, v58\n" \
"v_mul_f32 v59, 4.0, v59\n" \
"v_mfma_f32_16x16x32_bf16 v[60:63], a[156:159], a[228:231], v[60:63]\n" \
"v_cvt_pk_bf16_f32 v136, v56, v57\n" \
"v_cvt_pk_bf16_f32 v137, v58, v59\n" \
"v_mfma_f32_16x16x32_bf16 v[60:63], a[152:155], a[224:227], v[60:63]\n" \
"ds_write_b64 v128, v[136:137] offset:6976\n" \
"s_nop 7\n" \
"v_mul_f32 v60, 4.0, v60\n" \
"v_mul_f32 v61, 4.0, v61\n" \
"v_mul_f32 v62, 4.0, v62\n" \
"v_mul_f32 v63, 4.0, v63\n" \
"v_cvt_pk_bf16_f32 v138, v60, v61\n" \
"v_cvt_pk_bf16_f32 v139, v62, v63\n" \
"ds_write_b64 v128, v[138:139] offset:7008\n" \
"ds_read_b128 v[164:167], v129 offset:6912\n" \
"ds_read_b128 v[168:171], v129 offset:8064\n" \
"s_add_u32 s70, s88, 196608\n" \
"s_waitcnt lgkmcnt(1)\n" \
"buffer_store_dwordx4 v[164:167], v130, s[52:55], s70 offen sc1\n" \
"s_add_u32 s70, s88, 229376\n" \
"s_waitcnt lgkmcnt(0)\n" \
"buffer_store_dwordx4 v[168:171], v130, s[52:55], s70 offen sc1\n" \
"s_endpgm\n" \
".p2align 12\n" \
"Lrole1_%=:\n" \
"v_mfma_f32_16x16x32_bf16 v[64:67], a[96:99], a[64:67], 0\n" \
"Lpg10924_%=:\n" \
"s_cbranch_execz Lcb1_%=\n" \
"ds_read_b128 a[88:91], v213 offset:4352\n" \
"v_mfma_f32_16x16x32_bf16 v[64:67], a[100:103], a[68:71], v[64:67]\n" \
"s_waitcnt lgkmcnt(2)\n" \
"s_xor_b32 s82, s48, s49\n" \
"ds_read_b128 a[92:95], v213 offset:6400\n" \
"v_mfma_f32_16x16x32_bf16 v[68:71], a[104:107], a[64:67], 0\n" \
"s_cmp_lt_u32 s48, s50\n" \
"s_cselect_b32 s82, s82, s48\n" \
"ds_read_b64_tr_b16 a[112:113], v214 offset:16896\n" \
"v_mfma_f32_16x16x32_bf16 v[68:71], a[108:111], a[68:71], v[68:71]\n" \
"s_add_u32 s81, s82, s46\n" \
"ds_read_b64_tr_b16 a[114:115], v214 offset:18944\n" \
"v_mfma_f32_16x16x32_bf16 v[84:87], a[104:107], a[72:75], 0\n" \
"s_waitcnt lgkmcnt(4)\n" \
"s_add_u32 m0, s86, s79\n" \
"ds_read_b64_tr_b16 a[116:117], v214 offset:25088\n" \
"v_mfma_f32_16x16x32_bf16 v[84:87], a[108:111], a[76:79], v[84:87]\n" \
"buffer_load_dwordx4 v234, s[56:59], s81 offen offset:0 sc1 lds\n" \
"v_mfma_f32_16x16x32_bf16 v[80:83], a[96:99], a[72:75], 0\n" \
"s_sub_u32 s85, s85, 32768\n" \
"s_cselect_b32 s85, 131072, s85\n" \
"ds_read_b64_tr_b16 a[118:119], v214 offset:27136\n" \
"v_mfma_f32_16x16x32_bf16 v[80:83], a[100:103], a[76:79], v[80:83]\n" \
"s_lshl_b32 s82, s82, 5\n" \
"ds_read_b64_tr_b16 a[120:121], v215 offset:16896\n" \
"v_cvt_pk_bf16_f32 v0, v0, v1\n" \
"v_mfma_f32_16x16x32_bf16 v[96:99], a[96:99], a[80:83], 0\n" \
"s_add_u32 s82, s82, s47\n" \
"ds_read_b64_tr_b16 a[122:123], v215 offset:18944\n" \
"v_cvt_pk_bf16_f32 v1, v2, v3\n" \
"v_mfma_f32_16x16x32_bf16 v[96:99], a[100:103], a[84:87], v[96:99]\n" \
"buffer_load_dwordx4 v234, s[56:59], s81 offen offset:1024 sc1 lds\n" \
"v_cvt_pk_bf16_f32 v2, v4, v5\n" \
"v_mfma_f32_16x16x32_bf16 v[100:103], a[104:107], a[80:83], 0\n" \
"s_waitcnt lgkmcnt(3)\n" \
"ds_read_b64_tr_b16 a[124:125], v215 offset:25088\n" \
"v_add_u32 v213, s85, v211\n" \
"v_mfma_f32_16x16x32_bf16 v[100:103], a[108:111], a[84:87], v[100:103]\n" \
"buffer_load_dwordx4 v234, s[56:59], s81 offen offset:2048 sc1 lds\n" \
"v_mfma_f32_16x16x32_bf16 v[116:119], a[104:107], a[88:91], 0\n" \
"ds_read_b64_tr_b16 a[126:127], v215 offset:27136\n" \
"v_cvt_pk_bf16_f32 v3, v6, v7\n" \
"v_mfma_f32_16x16x32_bf16 v[116:119], a[108:111], a[92:95], v[116:119]\n" \
"buffer_load_dwordx4 v234, s[56:59], s81 offen offset:3072 sc1 lds\n" \
"v_cvt_pk_bf16_f32 v4, v8, v9\n" \
"v_mfma_f32_16x16x32_bf16 v[112:115], a[96:99], a[88:91], 0\n" \
"v_add_u32 v214, s85, v212\n" \
"v_xor_b32 v215, 32, v214\n" \
"v_mfma_f32_16x16x32_bf16 v[112:115], a[100:103], a[92:95], v[112:115]\n" \
"v_cvt_pk_bf16_f32 v5, v10, v11\n" \
"s_waitcnt vmcnt(20) lgkmcnt(0)\n" \
"s_barrier\n" \
"v_mfma_f32_16x16x32_bf16 v[72:75], a[112:115], a[64:67], 0\n" \
"s_add_u32 m0, m0, 0x4000\n" \
"ds_read_b64_tr_b16 a[96:97], v214 offset:16384\n" \
"ds_read_b64_tr_b16 a[98:99], v214 offset:18432\n" \
"v_mfma_f32_16x16x32_bf16 v[72:75], a[116:119], a[68:71], v[72:75]\n" \
"buffer_load_dwordx4 v216, s[36:39], s82 offen offset:0 lds\n" \
"v_mfma_f32_16x16x32_bf16 v[76:79], a[120:123], a[64:67], 0\n" \
"ds_read_b64_tr_b16 a[100:101], v214 offset:24576\n" \
"ds_read_b64_tr_b16 a[102:103], v214 offset:26624\n" \
"v_mfma_f32_16x16x32_bf16 v[76:79], a[124:127], a[68:71], v[76:79]\n" \
"ds_read_b128 a[64:67], v213 offset:0\n" \
"v_cvt_pk_bf16_f32 v6, v12, v13\n" \
"v_cvt_pk_bf16_f32 v7, v14, v15\n" \
"v_mfma_f32_16x16x32_bf16 v[92:95], a[120:123], a[72:75], 0\n" \
"s_add_u32 s48, s48, 0x1000\n" \
"ds_read_b128 a[68:71], v213 offset:2048\n" \
"v_cvt_pk_bf16_f32 v8, v16, v17\n" \
"v_mfma_f32_16x16x32_bf16 v[92:95], a[124:127], a[76:79], v[92:95]\n" \
"buffer_load_dwordx4 v217, s[36:39], s82 offen offset:1024 lds\n" \
"v_mfma_f32_16x16x32_bf16 v[88:91], a[112:115], a[72:75], 0\n" \
"ds_read_b64_tr_b16 a[104:105], v215 offset:16384\n" \
"ds_read_b64_tr_b16 a[106:107], v215 offset:18432\n" \
"v_mfma_f32_16x16x32_bf16 v[88:91], a[116:119], a[76:79], v[88:91]\n" \
"ds_read_b128 a[72:75], v213 offset:256\n" \
"v_cvt_pk_bf16_f32 v9, v18, v19\n" \
"v_cvt_pk_bf16_f32 v10, v20, v21\n" \
"v_mfma_f32_16x16x32_bf16 v[104:107], a[112:115], a[80:83], 0\n" \
"s_sub_u32 s86, s86, 32768\n" \
"s_cselect_b32 s86, 131072, s86\n" \
"ds_read_b128 a[76:79], v213 offset:2304\n" \
"v_mfma_f32_16x16x32_bf16 v[104:107], a[116:119], a[84:87], v[104:107]\n" \
"buffer_load_dwordx4 v218, s[36:39], s82 offen offset:2048 lds\n" \
"v_cvt_pk_bf16_f32 v11, v22, v23\n" \
"v_mfma_f32_16x16x32_bf16 v[108:111], a[120:123], a[80:83], 0\n" \
"ds_read_b64_tr_b16 a[108:109], v215 offset:24576\n" \
"ds_read_b64_tr_b16 a[110:111], v215 offset:26624\n" \
"v_mfma_f32_16x16x32_bf16 v[108:111], a[124:127], a[84:87], v[108:111]\n" \
"buffer_load_dwordx4 v219, s[36:39], s82 offen offset:3072 lds\n" \
"v_mfma_f32_16x16x32_bf16 v[124:127], a[120:123], a[88:91], 0\n" \
"s_waitcnt lgkmcnt(3)\n" \
"ds_read_b128 a[80:83], v213 offset:4096\n" \
"v_mfma_f32_16x16x32_bf16 v[124:127], a[124:127], a[92:95], v[124:127]\n" \
"s_add_u32 s71, s87, 0\n" \
"ds_read_b128 a[84:87], v213 offset:6144\n" \
"v_cvt_pk_bf16_f32 v12, v24, v25\n" \
"v_mfma_f32_16x16x32_bf16 v[120:123], a[112:115], a[88:91], 0\n" \
"buffer_store_dwordx4 v[0:3], v234, s[56:59], s71 offen offset:0 sc1\n" \
"v_mfma_f32_16x16x32_bf16 v[120:123], a[116:119], a[92:95], v[120:123]\n" \
"buffer_store_dwordx4 v[4:7], v234, s[56:59], s71 offen offset:1024 sc1\n" \
"buffer_store_dwordx4 v[8:11], v234, s[56:59], s71 offen offset:2048 sc1\n" \
"v_mfma_f32_16x16x32_bf16 v[64:67], a[96:99], a[64:67], v[64:67]\n" \
"s_waitcnt lgkmcnt(2)\n" \
"ds_read_b128 a[88:91], v213 offset:4352\n" \
"v_cvt_pk_bf16_f32 v13, v26, v27\n" \
"v_mfma_f32_16x16x32_bf16 v[64:67], a[100:103], a[68:71], v[64:67]\n" \
"s_xor_b32 s82, s48, s49\n" \
"ds_read_b128 a[92:95], v213 offset:6400\n" \
"v_cvt_pk_bf16_f32 v14, v28, v29\n" \
"v_mfma_f32_16x16x32_bf16 v[68:71], a[104:107], a[64:67], v[68:71]\n" \
"s_cmp_lt_u32 s48, s50\n" \
"s_cselect_b32 s82, s82, s48\n" \
"ds_read_b64_tr_b16 a[112:113], v214 offset:16896\n" \
"v_mfma_f32_16x16x32_bf16 v[68:71], a[108:111], a[68:71], v[68:71]\n" \
"s_add_u32 s81, s82, s46\n" \
"ds_read_b64_tr_b16 a[114:115], v214 offset:18944\n" \
"v_cvt_pk_bf16_f32 v15, v30, v31\n" \
"v_mfma_f32_16x16x32_bf16 v[84:87], a[104:107], a[72:75], v[84:87]\n" \
"s_add_u32 m0, s86, s79\n" \
"ds_read_b64_tr_b16 a[116:117], v214 offset:25088\n" \
"v_cvt_pk_bf16_f32 v16, v32, v33\n" \
"v_mfma_f32_16x16x32_bf16 v[84:87], a[108:111], a[76:79], v[84:87]\n" \
"buffer_load_dwordx4 v234, s[56:59], s81 offen offset:0 sc1 lds\n" \
"v_mfma_f32_16x16x32_bf16 v[80:83], a[96:99], a[72:75], v[80:83]\n" \
"s_sub_u32 s85, s85, 32768\n" \
"s_cselect_b32 s85, 131072, s85\n" \
"ds_read_b64_tr_b16 a[118:119], v214 offset:27136\n" \
"v_mfma_f32_16x16x32_bf16 v[80:83], a[100:103], a[76:79], v[80:83]\n" \
"s_waitcnt lgkmcnt(3)\n" \
"s_lshl_b32 s82, s82, 5\n" \
"ds_read_b64_tr_b16 a[120:121], v215 offset:16896\n" \
"v_cvt_pk_bf16_f32 v17, v34, v35\n" \
"v_mfma_f32_16x16x32_bf16 v[96:99], a[96:99], a[80:83], v[96:99]\n" \
"s_add_u32 s82, s82, s47\n" \
"ds_read_b64_tr_b16 a[122:123], v215 offset:18944\n" \
"v_cvt_pk_bf16_f32 v18, v36, v37\n" \
"v_mfma_f32_16x16x32_bf16 v[96:99], a[100:103], a[84:87], v[96:99]\n" \
"buffer_load_dwordx4 v234, s[56:59], s81 offen offset:1024 sc1 lds\n" \
"v_mfma_f32_16x16x32_bf16 v[100:103], a[104:107], a[80:83], v[100:103]\n" \
"ds_read_b64_tr_b16 a[124:125], v215 offset:25088\n" \
"v_add_u32 v213, s85, v211\n" \
"v_mfma_f32_16x16x32_bf16 v[100:103], a[108:111], a[84:87], v[100:103]\n" \
"buffer_load_dwordx4 v234, s[56:59], s81 offen offset:2048 sc1 lds\n" \
"v_mfma_f32_16x16x32_bf16 v[116:119], a[104:107], a[88:91], v[116:119]\n" \
"ds_read_b64_tr_b16 a[126:127], v215 offset:27136\n" \
"v_cvt_pk_bf16_f32 v19, v38, v39\n" \
"v_mfma_f32_16x16x32_bf16 v[116:119], a[108:111], a[92:95], v[116:119]\n" \
"buffer_load_dwordx4 v234, s[56:59], s81 offen offset:3072 sc1 lds\n" \
"v_cvt_pk_bf16_f32 v20, v40, v41\n" \
"v_mfma_f32_16x16x32_bf16 v[112:115], a[96:99], a[88:91], v[112:115]\n" \
"v_add_u32 v214, s85, v212\n" \
"v_xor_b32 v215, 32, v214\n" \
"v_mfma_f32_16x16x32_bf16 v[112:115], a[100:103], a[92:95], v[112:115]\n" \
"s_waitcnt vmcnt(23) lgkmcnt(0)\n" \
"s_barrier\n" \
"v_mfma_f32_16x16x32_bf16 v[72:75], a[112:115], a[64:67], v[72:75]\n" \
"s_add_u32 m0, m0, 0x4000\n" \
"ds_read_b64_tr_b16 a[96:97], v214 offset:16384\n" \
"ds_read_b64_tr_b16 a[98:99], v214 offset:18432\n" \
"v_mfma_f32_16x16x32_bf16 v[72:75], a[116:119], a[68:71], v[72:75]\n" \
"buffer_load_dwordx4 v216, s[36:39], s82 offen offset:0 lds\n" \
"v_mfma_f32_16x16x32_bf16 v[76:79], a[120:123], a[64:67], v[76:79]\n" \
"ds_read_b64_tr_b16 a[100:101], v214 offset:24576\n" \
"ds_read_b64_tr_b16 a[102:103], v214 offset:26624\n" \
"v_mfma_f32_16x16x32_bf16 v[76:79], a[124:127], a[68:71], v[76:79]\n" \
"ds_read_b128 a[64:67], v213 offset:0\n" \
"v_cvt_pk_bf16_f32 v21, v42, v43\n" \
"v_mfma_f32_16x16x32_bf16 v[92:95], a[120:123], a[72:75], v[92:95]\n" \
"s_add_u32 s48, s48, 0x1000\n" \
"ds_read_b128 a[68:71], v213 offset:2048\n" \
"v_cvt_pk_bf16_f32 v22, v44, v45\n" \
"v_mfma_f32_16x16x32_bf16 v[92:95], a[124:127], a[76:79], v[92:95]\n" \
"buffer_load_dwordx4 v217, s[36:39], s82 offen offset:1024 lds\n" \
"v_mfma_f32_16x16x32_bf16 v[88:91], a[112:115], a[72:75], v[88:91]\n" \
"ds_read_b64_tr_b16 a[104:105], v215 offset:16384\n" \
"ds_read_b64_tr_b16 a[106:107], v215 offset:18432\n" \
"v_mfma_f32_16x16x32_bf16 v[88:91], a[116:119], a[76:79], v[88:91]\n" \
"ds_read_b128 a[72:75], v213 offset:256\n" \
"v_cvt_pk_bf16_f32 v23, v46, v47\n" \
"v_mfma_f32_16x16x32_bf16 v[104:107], a[112:115], a[80:83], v[104:107]\n" \
"s_sub_u32 s86, s86, 32768\n" \
"s_cselect_b32 s86, 131072, s86\n" \
"ds_read_b128 a[76:79], v213 offset:2304\n" \
"v_mfma_f32_16x16x32_bf16 v[104:107], a[116:119], a[84:87], v[104:107]\n" \
"buffer_load_dwordx4 v218, s[36:39], s82 offen offset:2048 lds\n" \
"v_mfma_f32_16x16x32_bf16 v[108:111], a[120:123], a[80:83], v[108:111]\n" \
"ds_read_b64_tr_b16 a[108:109], v215 offset:24576\n" \
"ds_read_b64_tr_b16 a[110:111], v215 offset:26624\n" \
"v_mfma_f32_16x16x32_bf16 v[108:111], a[124:127], a[84:87], v[108:111]\n" \
"buffer_load_dwordx4 v219, s[36:39], s82 offen offset:3072 lds\n" \
"v_mfma_f32_16x16x32_bf16 v[124:127], a[120:123], a[88:91], v[124:127]\n" \
"ds_read_b128 a[80:83], v213 offset:4096\n" \
"s_waitcnt lgkmcnt(4)\n" \
"v_mfma_f32_16x16x32_bf16 v[124:127], a[124:127], a[92:95], v[124:127]\n" \
"ds_read_b128 a[84:87], v213 offset:6144\n" \
"v_cvt_pk_bf16_f32 v24, v48, v49\n" \
"v_mfma_f32_16x16x32_bf16 v[120:123], a[112:115], a[88:91], v[120:123]\n" \
"buffer_store_dwordx4 v[12:15], v234, s[56:59], s71 offen offset:3072 sc1\n" \
"v_mfma_f32_16x16x32_bf16 v[120:123], a[116:119], a[92:95], v[120:123]\n" \
"s_add_u32 s71, s87, 4096\n" \
"buffer_store_dwordx4 v[16:19], v234, s[56:59], s71 offen offset:0 sc1\n" \
"buffer_store_dwordx4 v[20:23], v234, s[56:59], s71 offen offset:1024 sc1\n" \
"v_mfma_f32_16x16x32_bf16 v[64:67], a[96:99], a[64:67], v[64:67]\n" \
"s_waitcnt lgkmcnt(2)\n" \
"ds_read_b128 a[88:91], v213 offset:4352\n" \
"v_cvt_pk_bf16_f32 v25, v50, v51\n" \
"v_mfma_f32_16x16x32_bf16 v[64:67], a[100:103], a[68:71], v[64:67]\n" \
"s_xor_b32 s82, s48, s49\n" \
"ds_read_b128 a[92:95], v213 offset:6400\n" \
"v_cvt_pk_bf16_f32 v26, v52, v53\n" \
"v_mfma_f32_16x16x32_bf16 v[68:71], a[104:107], a[64:67], v[68:71]\n" \
"s_cmp_lt_u32 s48, s50\n" \
"s_cselect_b32 s82, s82, s48\n" \
"ds_read_b64_tr_b16 a[112:113], v214 offset:16896\n" \
"v_mfma_f32_16x16x32_bf16 v[68:71], a[108:111], a[68:71], v[68:71]\n" \
"s_add_u32 s81, s82, s46\n" \
"ds_read_b64_tr_b16 a[114:115], v214 offset:18944\n" \
"v_cvt_pk_bf16_f32 v27, v54, v55\n" \
"v_mfma_f32_16x16x32_bf16 v[84:87], a[104:107], a[72:75], v[84:87]\n" \
"s_add_u32 m0, s86, s79\n" \
"ds_read_b64_tr_b16 a[116:117], v214 offset:25088\n" \
"v_cvt_pk_bf16_f32 v28, v56, v57\n" \
"v_mfma_f32_16x16x32_bf16 v[84:87], a[108:111], a[76:79], v[84:87]\n" \
"buffer_load_dwordx4 v234, s[56:59], s81 offen offset:0 sc1 lds\n" \
"v_mfma_f32_16x16x32_bf16 v[80:83], a[96:99], a[72:75], v[80:83]\n" \
"s_sub_u32 s85, s85, 32768\n" \
"s_cselect_b32 s85, 131072, s85\n" \
"ds_read_b64_tr_b16 a[118:119], v214 offset:27136\n" \
"v_mfma_f32_16x16x32_bf16 v[80:83], a[100:103], a[76:79], v[80:83]\n" \
"s_waitcnt lgkmcnt(3)\n" \
"s_lshl_b32 s82, s82, 5\n" \
"ds_read_b64_tr_b16 a[120:121], v215 offset:16896\n" \
"v_cvt_pk_bf16_f32 v29, v58, v59\n" \
"v_mfma_f32_16x16x32_bf16 v[96:99], a[96:99], a[80:83], v[96:99]\n" \
"s_add_u32 s82, s82, s47\n" \
"ds_read_b64_tr_b16 a[122:123], v215 offset:18944\n" \
"v_cvt_pk_bf16_f32 v30, v60, v61\n" \
"v_mfma_f32_16x16x32_bf16 v[96:99], a[100:103], a[84:87], v[96:99]\n" \
"buffer_load_dwordx4 v234, s[56:59], s81 offen offset:1024 sc1 lds\n" \
"v_mfma_f32_16x16x32_bf16 v[100:103], a[104:107], a[80:83], v[100:103]\n" \
"ds_read_b64_tr_b16 a[124:125], v215 offset:25088\n" \
"v_add_u32 v213, s85, v211\n" \
"v_mfma_f32_16x16x32_bf16 v[100:103], a[108:111], a[84:87], v[100:103]\n" \
"buffer_load_dwordx4 v234, s[56:59], s81 offen offset:2048 sc1 lds\n" \
"v_mfma_f32_16x16x32_bf16 v[116:119], a[104:107], a[88:91], v[116:119]\n" \
"ds_read_b64_tr_b16 a[126:127], v215 offset:27136\n" \
"v_cvt_pk_bf16_f32 v31, v62, v63\n" \
"v_mfma_f32_16x16x32_bf16 v[116:119], a[108:111], a[92:95], v[116:119]\n" \
"buffer_load_dwordx4 v234, s[56:59], s81 offen offset:3072 sc1 lds\n" \
"v_mfma_f32_16x16x32_bf16 v[112:115], a[96:99], a[88:91], v[112:115]\n" \
"v_add_u32 v214, s85, v212\n" \
"v_xor_b32 v215, 32, v214\n" \
"v_mfma_f32_16x16x32_bf16 v[112:115], a[100:103], a[92:95], v[112:115]\n" \
"s_waitcnt vmcnt(26) lgkmcnt(0)\n" \
"s_barrier\n" \
"v_mfma_f32_16x16x32_bf16 v[72:75], a[112:115], a[64:67], v[72:75]\n" \
"s_add_u32 m0, m0, 0x4000\n" \
"ds_read_b64_tr_b16 a[96:97], v214 offset:16384\n" \
"ds_read_b64_tr_b16 a[98:99], v214 offset:18432\n" \
"v_mfma_f32_16x16x32_bf16 v[72:75], a[116:119], a[68:71], v[72:75]\n" \
"buffer_load_dwordx4 v216, s[36:39], s82 offen offset:0 lds\n" \
"v_mfma_f32_16x16x32_bf16 v[76:79], a[120:123], a[64:67], v[76:79]\n" \
"ds_read_b64_tr_b16 a[100:101], v214 offset:24576\n" \
"ds_read_b64_tr_b16 a[102:103], v214 offset:26624\n" \
"v_mfma_f32_16x16x32_bf16 v[76:79], a[124:127], a[68:71], v[76:79]\n" \
"ds_read_b128 a[64:67], v213 offset:0\n" \
"v_mfma_f32_16x16x32_bf16 v[92:95], a[120:123], a[72:75], v[92:95]\n" \
"s_add_u32 s48, s48, 0x1000\n" \
"ds_read_b128 a[68:71], v213 offset:2048\n" \
"v_mfma_f32_16x16x32_bf16 v[92:95], a[124:127], a[76:79], v[92:95]\n" \
"buffer_load_dwordx4 v217, s[36:39], s82 offen offset:1024 lds\n" \
"v_mfma_f32_16x16x32_bf16 v[88:91], a[112:115], a[72:75], v[88:91]\n" \
"ds_read_b64_tr_b16 a[104:105], v215 offset:16384\n" \
"ds_read_b64_tr_b16 a[106:107], v215 offset:18432\n" \
"v_mfma_f32_16x16x32_bf16 v[88:91], a[116:119], a[76:79], v[88:91]\n" \
"ds_read_b128 a[72:75], v213 offset:256\n" \
"v_mfma_f32_16x16x32_bf16 v[104:107], a[112:115], a[80:83], v[104:107]\n" \
"s_sub_u32 s86, s86, 32768\n" \
"s_cselect_b32 s86, 131072, s86\n" \
"ds_read_b128 a[76:79], v213 offset:2304\n" \
"v_mfma_f32_16x16x32_bf16 v[104:107], a[116:119], a[84:87], v[104:107]\n" \
"buffer_load_dwordx4 v218, s[36:39], s82 offen offset:2048 lds\n" \
"v_mfma_f32_16x16x32_bf16 v[108:111], a[120:123], a[80:83], v[108:111]\n" \
"ds_read_b64_tr_b16 a[108:109], v215 offset:24576\n" \
"ds_read_b64_tr_b16 a[110:111], v215 offset:26624\n" \
"v_mfma_f32_16x16x32_bf16 v[108:111], a[124:127], a[84:87], v[108:111]\n" \
"buffer_load_dwordx4 v219, s[36:39], s82 offen offset:3072 lds\n" \
"v_mfma_f32_16x16x32_bf16 v[124:127], a[120:123], a[88:91], v[124:127]\n" \
"ds_read_b128 a[80:83], v213 offset:4096\n" \
"s_waitcnt lgkmcnt(4)\n" \
"v_mfma_f32_16x16x32_bf16 v[124:127], a[124:127], a[92:95], v[124:127]\n" \
"ds_read_b128 a[84:87], v213 offset:6144\n" \
"v_mfma_f32_16x16x32_bf16 v[120:123], a[112:115], a[88:91], v[120:123]\n" \
"buffer_store_dwordx4 v[24:27], v234, s[56:59], s71 offen offset:2048 sc1\n" \
"v_mfma_f32_16x16x32_bf16 v[120:123], a[116:119], a[92:95], v[120:123]\n" \
"buffer_store_dwordx4 v[28:31], v234, s[56:59], s71 offen offset:3072 sc1\n" \
"v_mfma_f32_16x16x32_bf16 v[64:67], a[96:99], a[64:67], v[64:67]\n" \
"ds_read_b128 a[88:91], v213 offset:4352\n" \
"v_mfma_f32_16x16x32_bf16 v[64:67], a[100:103], a[68:71], v[64:67]\n" \
"s_waitcnt lgkmcnt(2)\n" \
"s_xor_b32 s82, s48, s49\n" \
"ds_read_b128 a[92:95], v213 offset:6400\n" \
"v_mfma_f32_16x16x32_bf16 v[68:71], a[104:107], a[64:67], v[68:71]\n" \
"s_cmp_lt_u32 s48, s50\n" \
"s_cselect_b32 s82, s82, s48\n" \
"ds_read_b64_tr_b16 a[112:113], v214 offset:16896\n" \
"v_mfma_f32_16x16x32_bf16 v[68:71], a[108:111], a[68:71], v[68:71]\n" \
"s_add_u32 s81, s82, s46\n" \
"ds_read_b64_tr_b16 a[114:115], v214 offset:18944\n" \
"v_mfma_f32_16x16x32_bf16 v[84:87], a[104:107], a[72:75], v[84:87]\n" \
"s_add_u32 m0, s86, s79\n" \
"ds_read_b64_tr_b16 a[116:117], v214 offset:25088\n" \
"v_mfma_f32_16x16x32_bf16 v[84:87], a[108:111], a[76:79], v[84:87]\n" \
"buffer_load_dwordx4 v234, s[56:59], s81 offen offset:0 sc1 lds\n" \
"v_mfma_f32_16x16x32_bf16 v[80:83], a[96:99], a[72:75], v[80:83]\n" \
"s_sub_u32 s85, s85, 32768\n" \
"s_cselect_b32 s85, 131072, s85\n" \
"ds_read_b64_tr_b16 a[118:119], v214 offset:27136\n" \
"v_mfma_f32_16x16x32_bf16 v[80:83], a[100:103], a[76:79], v[80:83]\n" \
"s_waitcnt lgkmcnt(3)\n" \
"s_lshl_b32 s82, s82, 5\n" \
"ds_read_b64_tr_b16 a[120:121], v215 offset:16896\n" \
"v_mfma_f32_16x16x32_bf16 v[96:99], a[96:99], a[80:83], v[96:99]\n" \
"s_add_u32 s82, s82, s47\n" \
"ds_read_b64_tr_b16 a[122:123], v215 offset:18944\n" \
"v_mfma_f32_16x16x32_bf16 v[96:99], a[100:103], a[84:87], v[96:99]\n" \
"buffer_load_dwordx4 v234, s[56:59], s81 offen offset:1024 sc1 lds\n" \
"v_mfma_f32_16x16x32_bf16 v[100:103], a[104:107], a[80:83], v[100:103]\n" \
"ds_read_b64_tr_b16 a[124:125], v215 offset:25088\n" \
"v_add_u32 v213, s85, v211\n" \
"v_mfma_f32_16x16x32_bf16 v[100:103], a[108:111], a[84:87], v[100:103]\n" \
"buffer_load_dwordx4 v234, s[56:59], s81 offen offset:2048 sc1 lds\n" \
"v_mfma_f32_16x16x32_bf16 v[116:119], a[104:107], a[88:91], v[116:119]\n" \
"ds_read_b64_tr_b16 a[126:127], v215 offset:27136\n" \
"v_mfma_f32_16x16x32_bf16 v[116:119], a[108:111], a[92:95], v[116:119]\n" \
"buffer_load_dwordx4 v234, s[56:59], s81 offen offset:3072 sc1 lds\n" \
"v_mfma_f32_16x16x32_bf16 v[112:115], a[96:99], a[88:91], v[112:115]\n" \
"v_add_u32 v214, s85, v212\n" \
"v_xor_b32 v215, 32, v214\n" \
"v_mfma_f32_16x16x32_bf16 v[112:115], a[100:103], a[92:95], v[112:115]\n" \
"s_waitcnt vmcnt(28) lgkmcnt(0)\n" \
"s_barrier\n" \
"v_mfma_f32_16x16x32_bf16 v[72:75], a[112:115], a[64:67], v[72:75]\n" \
"s_add_u32 m0, m0, 0x4000\n" \
"ds_read_b64_tr_b16 a[96:97], v214 offset:16384\n" \
"ds_read_b64_tr_b16 a[98:99], v214 offset:18432\n" \
"v_mfma_f32_16x16x32_bf16 v[72:75], a[116:119], a[68:71], v[72:75]\n" \
"buffer_load_dwordx4 v216, s[36:39], s82 offen offset:0 lds\n" \
"v_mfma_f32_16x16x32_bf16 v[76:79], a[120:123], a[64:67], v[76:79]\n" \
"ds_read_b64_tr_b16 a[100:101], v214 offset:24576\n" \
"ds_read_b64_tr_b16 a[102:103], v214 offset:26624\n" \
"v_mfma_f32_16x16x32_bf16 v[76:79], a[124:127], a[68:71], v[76:79]\n" \
"ds_read_b128 a[64:67], v213 offset:0\n" \
"v_mfma_f32_16x16x32_bf16 v[92:95], a[120:123], a[72:75], v[92:95]\n" \
"s_add_u32 s48, s48, 0x1000\n" \
"ds_read_b128 a[68:71], v213 offset:2048\n" \
"v_mfma_f32_16x16x32_bf16 v[92:95], a[124:127], a[76:79], v[92:95]\n" \
"buffer_load_dwordx4 v217, s[36:39], s82 offen offset:1024 lds\n" \
"v_mfma_f32_16x16x32_bf16 v[88:91], a[112:115], a[72:75], v[88:91]\n" \
"ds_read_b64_tr_b16 a[104:105], v215 offset:16384\n" \
"ds_read_b64_tr_b16 a[106:107], v215 offset:18432\n" \
"v_mfma_f32_16x16x32_bf16 v[88:91], a[116:119], a[76:79], v[88:91]\n" \
"ds_read_b128 a[72:75], v213 offset:256\n" \
"v_mfma_f32_16x16x32_bf16 v[104:107], a[112:115], a[80:83], v[104:107]\n" \
"s_sub_u32 s86, s86, 32768\n" \
"s_cselect_b32 s86, 131072, s86\n" \
"ds_read_b128 a[76:79], v213 offset:2304\n" \
"v_mfma_f32_16x16x32_bf16 v[104:107], a[116:119], a[84:87], v[104:107]\n" \
"buffer_load_dwordx4 v218, s[36:39], s82 offen offset:2048 lds\n" \
"v_mfma_f32_16x16x32_bf16 v[108:111], a[120:123], a[80:83], v[108:111]\n" \
"ds_read_b64_tr_b16 a[108:109], v215 offset:24576\n" \
"ds_read_b64_tr_b16 a[110:111], v215 offset:26624\n" \
"v_mfma_f32_16x16x32_bf16 v[108:111], a[124:127], a[84:87], v[108:111]\n" \
"buffer_load_dwordx4 v219, s[36:39], s82 offen offset:3072 lds\n" \
"v_mfma_f32_16x16x32_bf16 v[124:127], a[120:123], a[88:91], v[124:127]\n" \
"ds_read_b128 a[80:83], v213 offset:4096\n" \
"s_waitcnt lgkmcnt(4)\n" \
"v_mfma_f32_16x16x32_bf16 v[124:127], a[124:127], a[92:95], v[124:127]\n" \
"ds_read_b128 a[84:87], v213 offset:6144\n" \
"v_mfma_f32_16x16x32_bf16 v[120:123], a[112:115], a[88:91], v[120:123]\n" \
"v_mfma_f32_16x16x32_bf16 v[120:123], a[116:119], a[92:95], v[120:123]\n" \
"v_mfma_f32_16x16x32_bf16 v[64:67], a[96:99], a[64:67], v[64:67]\n" \
"ds_read_b128 a[88:91], v213 offset:4352\n" \
"v_mfma_f32_16x16x32_bf16 v[64:67], a[100:103], a[68:71], v[64:67]\n" \
"s_waitcnt lgkmcnt(2)\n" \
"s_xor_b32 s82, s48, s49\n" \
"ds_read_b128 a[92:95], v213 offset:6400\n" \
"v_mfma_f32_16x16x32_bf16 v[68:71], a[104:107], a[64:67], v[68:71]\n" \
"s_cmp_lt_u32 s48, s50\n" \
"s_cselect_b32 s82, s82, s48\n" \
"ds_read_b64_tr_b16 a[112:113], v214 offset:16896\n" \
"v_mfma_f32_16x16x32_bf16 v[68:71], a[108:111], a[68:71], v[68:71]\n" \
"s_add_u32 s81, s82, s46\n" \
"ds_read_b64_tr_b16 a[114:115], v214 offset:18944\n" \
"v_mfma_f32_16x16x32_bf16 v[84:87], a[104:107], a[72:75], v[84:87]\n" \
"s_add_u32 m0, s86, s79\n" \
"ds_read_b64_tr_b16 a[116:117], v214 offset:25088\n" \
"v_mfma_f32_16x16x32_bf16 v[84:87], a[108:111], a[76:79], v[84:87]\n" \
"buffer_load_dwordx4 v234, s[56:59], s81 offen offset:0 sc1 lds\n" \
"v_mfma_f32_16x16x32_bf16 v[80:83], a[96:99], a[72:75], v[80:83]\n" \
"s_sub_u32 s85, s85, 32768\n" \
"s_cselect_b32 s85, 131072, s85\n" \
"ds_read_b64_tr_b16 a[118:119], v214 offset:27136\n" \
"v_mfma_f32_16x16x32_bf16 v[80:83], a[100:103], a[76:79], v[80:83]\n" \
"s_waitcnt lgkmcnt(3)\n" \
"s_lshl_b32 s82, s82, 5\n" \
"ds_read_b64_tr_b16 a[120:121], v215 offset:16896\n" \
"v_mfma_f32_16x16x32_bf16 v[96:99], a[96:99], a[80:83], v[96:99]\n" \
"s_add_u32 s82, s82, s47\n" \
"ds_read_b64_tr_b16 a[122:123], v215 offset:18944\n" \
"v_mfma_f32_16x16x32_bf16 v[96:99], a[100:103], a[84:87], v[96:99]\n" \
"buffer_load_dwordx4 v234, s[56:59], s81 offen offset:1024 sc1 lds\n" \
"v_mfma_f32_16x16x32_bf16 v[100:103], a[104:107], a[80:83], v[100:103]\n" \
"ds_read_b64_tr_b16 a[124:125], v215 offset:25088\n" \
"v_add_u32 v213, s85, v211\n" \
"v_mfma_f32_16x16x32_bf16 v[100:103], a[108:111], a[84:87], v[100:103]\n" \
"buffer_load_dwordx4 v234, s[56:59], s81 offen offset:2048 sc1 lds\n" \
"v_mfma_f32_16x16x32_bf16 v[116:119], a[104:107], a[88:91], v[116:119]\n" \
"ds_read_b64_tr_b16 a[126:127], v215 offset:27136\n" \
"v_mfma_f32_16x16x32_bf16 v[116:119], a[108:111], a[92:95], v[116:119]\n" \
"buffer_load_dwordx4 v234, s[56:59], s81 offen offset:3072 sc1 lds\n" \
"v_mfma_f32_16x16x32_bf16 v[112:115], a[96:99], a[88:91], v[112:115]\n" \
"v_add_u32 v214, s85, v212\n" \
"v_xor_b32 v215, 32, v214\n" \
"v_mfma_f32_16x16x32_bf16 v[112:115], a[100:103], a[92:95], v[112:115]\n" \
"s_waitcnt vmcnt(25) lgkmcnt(0)\n" \
"s_barrier\n" \
"v_mfma_f32_16x16x32_bf16 v[72:75], a[112:115], a[64:67], v[72:75]\n" \
"s_add_u32 m0, m0, 0x4000\n" \
"ds_read_b64_tr_b16 a[96:97], v214 offset:16384\n" \
"ds_read_b64_tr_b16 a[98:99], v214 offset:18432\n" \
"v_mfma_f32_16x16x32_bf16 v[72:75], a[116:119], a[68:71], v[72:75]\n" \
"buffer_load_dwordx4 v216, s[36:39], s82 offen offset:0 lds\n" \
"v_mfma_f32_16x16x32_bf16 v[76:79], a[120:123], a[64:67], v[76:79]\n" \
"ds_read_b64_tr_b16 a[100:101], v214 offset:24576\n" \
"ds_read_b64_tr_b16 a[102:103], v214 offset:26624\n" \
"v_mfma_f32_16x16x32_bf16 v[76:79], a[124:127], a[68:71], v[76:79]\n" \
"ds_read_b128 a[64:67], v213 offset:0\n" \
"v_mfma_f32_16x16x32_bf16 v[92:95], a[120:123], a[72:75], v[92:95]\n" \
"s_add_u32 s48, s48, 0x1000\n" \
"ds_read_b128 a[68:71], v213 offset:2048\n" \
"v_mfma_f32_16x16x32_bf16 v[92:95], a[124:127], a[76:79], v[92:95]\n" \
"buffer_load_dwordx4 v217, s[36:39], s82 offen offset:1024 lds\n" \
"v_mfma_f32_16x16x32_bf16 v[88:91], a[112:115], a[72:75], v[88:91]\n" \
"ds_read_b64_tr_b16 a[104:105], v215 offset:16384\n" \
"ds_read_b64_tr_b16 a[106:107], v215 offset:18432\n" \
"v_mfma_f32_16x16x32_bf16 v[88:91], a[116:119], a[76:79], v[88:91]\n" \
"ds_read_b128 a[72:75], v213 offset:256\n" \
"v_mfma_f32_16x16x32_bf16 v[104:107], a[112:115], a[80:83], v[104:107]\n" \
"s_sub_u32 s86, s86, 32768\n" \
"s_cselect_b32 s86, 131072, s86\n" \
"ds_read_b128 a[76:79], v213 offset:2304\n" \
"v_mfma_f32_16x16x32_bf16 v[104:107], a[116:119], a[84:87], v[104:107]\n" \
"buffer_load_dwordx4 v218, s[36:39], s82 offen offset:2048 lds\n" \
"v_mfma_f32_16x16x32_bf16 v[108:111], a[120:123], a[80:83], v[108:111]\n" \
"ds_read_b64_tr_b16 a[108:109], v215 offset:24576\n" \
"ds_read_b64_tr_b16 a[110:111], v215 offset:26624\n" \
"v_mfma_f32_16x16x32_bf16 v[108:111], a[124:127], a[84:87], v[108:111]\n" \
"buffer_load_dwordx4 v219, s[36:39], s82 offen offset:3072 lds\n" \
"v_mfma_f32_16x16x32_bf16 v[124:127], a[120:123], a[88:91], v[124:127]\n" \
"ds_read_b128 a[80:83], v213 offset:4096\n" \
"s_waitcnt lgkmcnt(4)\n" \
"v_mfma_f32_16x16x32_bf16 v[124:127], a[124:127], a[92:95], v[124:127]\n" \
"ds_read_b128 a[84:87], v213 offset:6144\n" \
"v_mfma_f32_16x16x32_bf16 v[120:123], a[112:115], a[88:91], v[120:123]\n" \
"v_mfma_f32_16x16x32_bf16 v[120:123], a[116:119], a[92:95], v[120:123]\n" \
"v_mfma_f32_16x16x32_bf16 v[64:67], a[96:99], a[64:67], v[64:67]\n" \
"ds_read_b128 a[88:91], v213 offset:4352\n" \
"v_mfma_f32_16x16x32_bf16 v[64:67], a[100:103], a[68:71], v[64:67]\n" \
"s_waitcnt lgkmcnt(2)\n" \
"s_xor_b32 s82, s48, s49\n" \
"ds_read_b128 a[92:95], v213 offset:6400\n" \
"v_mfma_f32_16x16x32_bf16 v[68:71], a[104:107], a[64:67], v[68:71]\n" \
"s_cmp_lt_u32 s48, s50\n" \
"s_cselect_b32 s82, s82, s48\n" \
"ds_read_b64_tr_b16 a[112:113], v214 offset:16896\n" \
"v_mfma_f32_16x16x32_bf16 v[68:71], a[108:111], a[68:71], v[68:71]\n" \
"s_add_u32 s81, s82, s46\n" \
"ds_read_b64_tr_b16 a[114:115], v214 offset:18944\n" \
"v_mfma_f32_16x16x32_bf16 v[84:87], a[104:107], a[72:75], v[84:87]\n" \
"s_add_u32 m0, s86, s79\n" \
"ds_read_b64_tr_b16 a[116:117], v214 offset:25088\n" \
"v_mfma_f32_16x16x32_bf16 v[84:87], a[108:111], a[76:79], v[84:87]\n" \
"buffer_load_dwordx4 v234, s[56:59], s81 offen offset:0 sc1 lds\n" \
"v_mfma_f32_16x16x32_bf16 v[80:83], a[96:99], a[72:75], v[80:83]\n" \
"s_sub_u32 s85, s85, 32768\n" \
"s_cselect_b32 s85, 131072, s85\n" \
"ds_read_b64_tr_b16 a[118:119], v214 offset:27136\n" \
"v_mfma_f32_16x16x32_bf16 v[80:83], a[100:103], a[76:79], v[80:83]\n" \
"s_waitcnt lgkmcnt(3)\n" \
"s_lshl_b32 s82, s82, 5\n" \
"ds_read_b64_tr_b16 a[120:121], v215 offset:16896\n" \
"v_mfma_f32_16x16x32_bf16 v[96:99], a[96:99], a[80:83], v[96:99]\n" \
"s_add_u32 s82, s82, s47\n" \
"ds_read_b64_tr_b16 a[122:123], v215 offset:18944\n" \
"v_mfma_f32_16x16x32_bf16 v[96:99], a[100:103], a[84:87], v[96:99]\n" \
"buffer_load_dwordx4 v234, s[56:59], s81 offen offset:1024 sc1 lds\n" \
"v_mfma_f32_16x16x32_bf16 v[100:103], a[104:107], a[80:83], v[100:103]\n" \
"ds_read_b64_tr_b16 a[124:125], v215 offset:25088\n" \
"v_add_u32 v213, s85, v211\n" \
"v_mfma_f32_16x16x32_bf16 v[100:103], a[108:111], a[84:87], v[100:103]\n" \
"buffer_load_dwordx4 v234, s[56:59], s81 offen offset:2048 sc1 lds\n" \
"v_mfma_f32_16x16x32_bf16 v[116:119], a[104:107], a[88:91], v[116:119]\n" \
"ds_read_b64_tr_b16 a[126:127], v215 offset:27136\n" \
"v_mfma_f32_16x16x32_bf16 v[116:119], a[108:111], a[92:95], v[116:119]\n" \
"buffer_load_dwordx4 v234, s[56:59], s81 offen offset:3072 sc1 lds\n" \
"v_mfma_f32_16x16x32_bf16 v[112:115], a[96:99], a[88:91], v[112:115]\n" \
"v_add_u32 v214, s85, v212\n" \
"v_xor_b32 v215, 32, v214\n" \
"v_mfma_f32_16x16x32_bf16 v[112:115], a[100:103], a[92:95], v[112:115]\n" \
"s_waitcnt vmcnt(20) lgkmcnt(0)\n" \
"s_barrier\n" \
"buffer_store_dword v230, v231, s[56:59], s64 offen sc1\n" \
"v_mfma_f32_16x16x32_bf16 v[72:75], a[112:115], a[64:67], v[72:75]\n" \
"s_add_u32 m0, m0, 0x4000\n" \
"ds_read_b64_tr_b16 a[96:97], v214 offset:16384\n" \
"ds_read_b64_tr_b16 a[98:99], v214 offset:18432\n" \
"v_mfma_f32_16x16x32_bf16 v[72:75], a[116:119], a[68:71], v[72:75]\n" \
"buffer_load_dwordx4 v216, s[36:39], s82 offen offset:0 lds\n" \
"v_mfma_f32_16x16x32_bf16 v[76:79], a[120:123], a[64:67], v[76:79]\n" \
"ds_read_b64_tr_b16 a[100:101], v214 offset:24576\n" \
"ds_read_b64_tr_b16 a[102:103], v214 offset:26624\n" \
"v_mfma_f32_16x16x32_bf16 v[76:79], a[124:127], a[68:71], v[76:79]\n" \
"ds_read_b128 a[64:67], v213 offset:0\n" \
"v_mfma_f32_16x16x32_bf16 v[92:95], a[120:123], a[72:75], v[92:95]\n" \
"s_add_u32 s48, s48, 0x1000\n" \
"ds_read_b128 a[68:71], v213 offset:2048\n" \
"v_mfma_f32_16x16x32_bf16 v[92:95], a[124:127], a[76:79], v[92:95]\n" \
"buffer_load_dwordx4 v217, s[36:39], s82 offen offset:1024 lds\n" \
"v_mfma_f32_16x16x32_bf16 v[88:91], a[112:115], a[72:75], v[88:91]\n" \
"ds_read_b64_tr_b16 a[104:105], v215 offset:16384\n" \
"ds_read_b64_tr_b16 a[106:107], v215 offset:18432\n" \
"v_mfma_f32_16x16x32_bf16 v[88:91], a[116:119], a[76:79], v[88:91]\n" \
"ds_read_b128 a[72:75], v213 offset:256\n" \
"v_mfma_f32_16x16x32_bf16 v[104:107], a[112:115], a[80:83], v[104:107]\n" \
"s_sub_u32 s86, s86, 32768\n" \
"s_cselect_b32 s86, 131072, s86\n" \
"ds_read_b128 a[76:79], v213 offset:2304\n" \
"v_mfma_f32_16x16x32_bf16 v[104:107], a[116:119], a[84:87], v[104:107]\n" \
"buffer_load_dwordx4 v218, s[36:39], s82 offen offset:2048 lds\n" \
"v_mfma_f32_16x16x32_bf16 v[108:111], a[120:123], a[80:83], v[108:111]\n" \
"ds_read_b64_tr_b16 a[108:109], v215 offset:24576\n" \
"ds_read_b64_tr_b16 a[110:111], v215 offset:26624\n" \
"v_mfma_f32_16x16x32_bf16 v[108:111], a[124:127], a[84:87], v[108:111]\n" \
"buffer_load_dwordx4 v219, s[36:39], s82 offen offset:3072 lds\n" \
"v_mfma_f32_16x16x32_bf16 v[124:127], a[120:123], a[88:91], v[124:127]\n" \
"ds_read_b128 a[80:83], v213 offset:4096\n" \
"s_waitcnt lgkmcnt(4)\n" \
"v_mfma_f32_16x16x32_bf16 v[124:127], a[124:127], a[92:95], v[124:127]\n" \
"ds_read_b128 a[84:87], v213 offset:6144\n" \
"v_mfma_f32_16x16x32_bf16 v[120:123], a[112:115], a[88:91], v[120:123]\n" \
"v_mfma_f32_16x16x32_bf16 v[120:123], a[116:119], a[92:95], v[120:123]\n" \
"s_mov_b32 s72, 5\n" \
"s_nop 0\n" \
"s_nop 0\n" \
"Lo64_%=:\n" \
"v_mfma_f32_16x16x32_bf16 v[64:67], a[96:99], a[64:67], v[64:67]\n" \
"ds_read_b128 a[88:91], v213 offset:4352\n" \
"v_mfma_f32_16x16x32_bf16 v[64:67], a[100:103], a[68:71], v[64:67]\n" \
"Lpg11485_%=:\n" \
"s_cbranch_execz Lcb1_%=\n" \
"s_waitcnt lgkmcnt(2)\n" \
"s_xor_b32 s82, s48, s49\n" \
"ds_read_b128 a[92:95], v213 offset:6400\n" \
"v_mfma_f32_16x16x32_bf16 v[68:71], a[104:107], a[64:67], v[68:71]\n" \
"s_cmp_lt_u32 s48, s50\n" \
"s_cselect_b32 s82, s82, s48\n" \
"ds_read_b64_tr_b16 a[112:113], v214 offset:16896\n" \
"v_mfma_f32_16x16x32_bf16 v[68:71], a[108:111], a[68:71], v[68:71]\n" \
"s_add_u32 s81, s82, s46\n" \
"ds_read_b64_tr_b16 a[114:115], v214 offset:18944\n" \
"v_mfma_f32_16x16x32_bf16 v[84:87], a[104:107], a[72:75], v[84:87]\n" \
"s_add_u32 m0, s86, s79\n" \
"ds_read_b64_tr_b16 a[116:117], v214 offset:25088\n" \
"v_mfma_f32_16x16x32_bf16 v[84:87], a[108:111], a[76:79], v[84:87]\n" \
"buffer_load_dwordx4 v234, s[56:59], s81 offen offset:0 sc1 lds\n" \
"v_mfma_f32_16x16x32_bf16 v[80:83], a[96:99], a[72:75], v[80:83]\n" \
"s_sub_u32 s85, s85, 32768\n" \
"s_cselect_b32 s85, 131072, s85\n" \
"ds_read_b64_tr_b16 a[118:119], v214 offset:27136\n" \
"v_mfma_f32_16x16x32_bf16 v[80:83], a[100:103], a[76:79], v[80:83]\n" \
"s_waitcnt lgkmcnt(3)\n" \
"s_lshl_b32 s82, s82, 5\n" \
"ds_read_b64_tr_b16 a[120:121], v215 offset:16896\n" \
"v_mfma_f32_16x16x32_bf16 v[96:99], a[96:99], a[80:83], v[96:99]\n" \
"s_add_u32 s82, s82, s47\n" \
"ds_read_b64_tr_b16 a[122:123], v215 offset:18944\n" \
"v_mfma_f32_16x16x32_bf16 v[96:99], a[100:103], a[84:87], v[96:99]\n" \
"buffer_load_dwordx4 v234, s[56:59], s81 offen offset:1024 sc1 lds\n" \
"v_mfma_f32_16x16x32_bf16 v[100:103], a[104:107], a[80:83], v[100:103]\n" \
"ds_read_b64_tr_b16 a[124:125], v215 offset:25088\n" \
"v_add_u32 v213, s85, v211\n" \
"v_mfma_f32_16x16x32_bf16 v[100:103], a[108:111], a[84:87], v[100:103]\n" \
"buffer_load_dwordx4 v234, s[56:59], s81 offen offset:2048 sc1 lds\n" \
"v_mfma_f32_16x16x32_bf16 v[116:119], a[104:107], a[88:91], v[116:119]\n" \
"ds_read_b64_tr_b16 a[126:127], v215 offset:27136\n" \
"v_mfma_f32_16x16x32_bf16 v[116:119], a[108:111], a[92:95], v[116:119]\n" \
"buffer_load_dwordx4 v234, s[56:59], s81 offen offset:3072 sc1 lds\n" \
"v_mfma_f32_16x16x32_bf16 v[112:115], a[96:99], a[88:91], v[112:115]\n" \
"v_add_u32 v214, s85, v212\n" \
"v_xor_b32 v215, 32, v214\n" \
"v_mfma_f32_16x16x32_bf16 v[112:115], a[100:103], a[92:95], v[112:115]\n" \
"s_waitcnt vmcnt(20) lgkmcnt(0)\n" \
"s_barrier\n" \
"v_mfma_f32_16x16x32_bf16 v[72:75], a[112:115], a[64:67], v[72:75]\n" \
"s_add_u32 m0, m0, 0x4000\n" \
"ds_read_b64_tr_b16 a[96:97], v214 offset:16384\n" \
"ds_read_b64_tr_b16 a[98:99], v214 offset:18432\n" \
"v_mfma_f32_16x16x32_bf16 v[72:75], a[116:119], a[68:71], v[72:75]\n" \
"buffer_load_dwordx4 v216, s[36:39], s82 offen offset:0 lds\n" \
"v_mfma_f32_16x16x32_bf16 v[76:79], a[120:123], a[64:67], v[76:79]\n" \
"ds_read_b64_tr_b16 a[100:101], v214 offset:24576\n" \
"ds_read_b64_tr_b16 a[102:103], v214 offset:26624\n" \
"v_mfma_f32_16x16x32_bf16 v[76:79], a[124:127], a[68:71], v[76:79]\n" \
"ds_read_b128 a[64:67], v213 offset:0\n" \
"v_mfma_f32_16x16x32_bf16 v[92:95], a[120:123], a[72:75], v[92:95]\n" \
"s_add_u32 s48, s48, 0x1000\n" \
"ds_read_b128 a[68:71], v213 offset:2048\n" \
"v_mfma_f32_16x16x32_bf16 v[92:95], a[124:127], a[76:79], v[92:95]\n" \
"buffer_load_dwordx4 v217, s[36:39], s82 offen offset:1024 lds\n" \
"v_mfma_f32_16x16x32_bf16 v[88:91], a[112:115], a[72:75], v[88:91]\n" \
"ds_read_b64_tr_b16 a[104:105], v215 offset:16384\n" \
"ds_read_b64_tr_b16 a[106:107], v215 offset:18432\n" \
"v_mfma_f32_16x16x32_bf16 v[88:91], a[116:119], a[76:79], v[88:91]\n" \
"ds_read_b128 a[72:75], v213 offset:256\n" \
"v_mfma_f32_16x16x32_bf16 v[104:107], a[112:115], a[80:83], v[104:107]\n" \
"s_sub_u32 s86, s86, 32768\n" \
"s_cselect_b32 s86, 131072, s86\n" \
"ds_read_b128 a[76:79], v213 offset:2304\n" \
"v_mfma_f32_16x16x32_bf16 v[104:107], a[116:119], a[84:87], v[104:107]\n" \
"buffer_load_dwordx4 v218, s[36:39], s82 offen offset:2048 lds\n" \
"v_mfma_f32_16x16x32_bf16 v[108:111], a[120:123], a[80:83], v[108:111]\n" \
"ds_read_b64_tr_b16 a[108:109], v215 offset:24576\n" \
"ds_read_b64_tr_b16 a[110:111], v215 offset:26624\n" \
"v_mfma_f32_16x16x32_bf16 v[108:111], a[124:127], a[84:87], v[108:111]\n" \
"buffer_load_dwordx4 v219, s[36:39], s82 offen offset:3072 lds\n" \
"v_mfma_f32_16x16x32_bf16 v[124:127], a[120:123], a[88:91], v[124:127]\n" \
"ds_read_b128 a[80:83], v213 offset:4096\n" \
"s_waitcnt lgkmcnt(4)\n" \
"v_mfma_f32_16x16x32_bf16 v[124:127], a[124:127], a[92:95], v[124:127]\n" \
"s_sub_u32 s72, s72, 1\n" \
"ds_read_b128 a[84:87], v213 offset:6144\n" \
"v_mfma_f32_16x16x32_bf16 v[120:123], a[112:115], a[88:91], v[120:123]\n" \
"v_mfma_f32_16x16x32_bf16 v[120:123], a[116:119], a[92:95], v[120:123]\n" \
"s_cbranch_scc0 Lo64_%=\n" \
"v_mfma_f32_16x16x32_bf16 v[64:67], a[96:99], a[64:67], v[64:67]\n" \
"s_waitcnt lgkmcnt(2)\n" \
"ds_read_b128 a[88:91], v213 offset:4352\n" \
"v_mfma_f32_16x16x32_bf16 v[64:67], a[100:103], a[68:71], v[64:67]\n" \
"s_add_u32 s48, s86, 32768\n" \
"s_cmp_eq_u32 s48, 163840\n" \
"s_cselect_b32 s48, 0, s48\n" \
"ds_read_b128 a[92:95], v213 offset:6400\n" \
"v_mfma_f32_16x16x32_bf16 v[68:71], a[104:107], a[64:67], v[68:71]\n" \
"ds_read_b64_tr_b16 a[112:113], v214 offset:16896\n" \
"v_mfma_f32_16x16x32_bf16 v[68:71], a[108:111], a[68:71], v[68:71]\n" \
"s_add_u32 s49, s48, 32768\n" \
"s_cmp_eq_u32 s49, 163840\n" \
"s_cselect_b32 s49, 0, s49\n" \
"ds_read_b64_tr_b16 a[114:115], v214 offset:18944\n" \
"v_mfma_f32_16x16x32_bf16 v[84:87], a[104:107], a[72:75], v[84:87]\n" \
"ds_read_b64_tr_b16 a[116:117], v214 offset:25088\n" \
"v_add_u32 v224, s49, v211\n" \
"v_mfma_f32_16x16x32_bf16 v[84:87], a[108:111], a[76:79], v[84:87]\n" \
"v_add_u32 v225, s49, v212\n" \
"v_xor_b32 v226, 32, v225\n" \
"v_mfma_f32_16x16x32_bf16 v[80:83], a[96:99], a[72:75], v[80:83]\n" \
"s_sub_u32 s85, s85, 32768\n" \
"s_cselect_b32 s85, 131072, s85\n" \
"ds_read_b64_tr_b16 a[118:119], v214 offset:27136\n" \
"v_mfma_f32_16x16x32_bf16 v[80:83], a[100:103], a[76:79], v[80:83]\n" \
"s_waitcnt lgkmcnt(3)\n" \
"ds_read_b64_tr_b16 a[120:121], v215 offset:16896\n" \
"v_add_u32 v227, s48, v211\n" \
"v_mfma_f32_16x16x32_bf16 v[96:99], a[96:99], a[80:83], v[96:99]\n" \
"ds_read_b64_tr_b16 a[122:123], v215 offset:18944\n" \
"v_add_u32 v228, s48, v212\n" \
"v_mfma_f32_16x16x32_bf16 v[96:99], a[100:103], a[84:87], v[96:99]\n" \
"v_xor_b32 v229, 32, v228\n" \
"v_mfma_f32_16x16x32_bf16 v[100:103], a[104:107], a[80:83], v[100:103]\n" \
"ds_read_b64_tr_b16 a[124:125], v215 offset:25088\n" \
"v_add_u32 v213, s85, v211\n" \
"v_mfma_f32_16x16x32_bf16 v[100:103], a[108:111], a[84:87], v[100:103]\n" \
"v_mfma_f32_16x16x32_bf16 v[116:119], a[104:107], a[88:91], v[116:119]\n" \
"ds_read_b64_tr_b16 a[126:127], v215 offset:27136\n" \
"v_mfma_f32_16x16x32_bf16 v[116:119], a[108:111], a[92:95], v[116:119]\n" \
"v_mfma_f32_16x16x32_bf16 v[112:115], a[96:99], a[88:91], v[112:115]\n" \
"v_add_u32 v214, s85, v212\n" \
"v_xor_b32 v215, 32, v214\n" \
"v_mfma_f32_16x16x32_bf16 v[112:115], a[100:103], a[92:95], v[112:115]\n" \
"s_waitcnt vmcnt(8) lgkmcnt(0)\n" \
"s_barrier\n" \
"v_mfma_f32_16x16x32_bf16 v[72:75], a[112:115], a[64:67], v[72:75]\n" \
"ds_read_b128 a[0:3], v213 offset:0\n" \
"ds_read_b128 a[4:7], v213 offset:2048\n" \
"v_mfma_f32_16x16x32_bf16 v[72:75], a[116:119], a[68:71], v[72:75]\n" \
"s_sub_u32 s68, s86, 32768\n" \
"s_cselect_b32 s68, 131072, s68\n" \
"ds_read_b64_tr_b16 a[128:129], v214 offset:16384\n" \
"ds_read_b64_tr_b16 a[130:131], v214 offset:18432\n" \
"v_mfma_f32_16x16x32_bf16 v[76:79], a[120:123], a[64:67], v[76:79]\n" \
"ds_read_b64_tr_b16 a[132:133], v214 offset:24576\n" \
"ds_read_b64_tr_b16 a[134:135], v214 offset:26624\n" \
"v_mfma_f32_16x16x32_bf16 v[76:79], a[124:127], a[68:71], v[76:79]\n" \
"s_cmp_lt_u32 s61, 2\n" \
"s_cselect_b32 s68, s86, s68\n" \
"ds_read_b64_tr_b16 a[136:137], v215 offset:16384\n" \
"ds_read_b64_tr_b16 a[138:139], v215 offset:18432\n" \
"v_mfma_f32_16x16x32_bf16 v[92:95], a[120:123], a[72:75], v[92:95]\n" \
"s_and_b32 s50, s61, 1\n" \
"ds_read_b64_tr_b16 a[140:141], v215 offset:24576\n" \
"ds_read_b64_tr_b16 a[142:143], v215 offset:26624\n" \
"v_mfma_f32_16x16x32_bf16 v[92:95], a[124:127], a[76:79], v[92:95]\n" \
"s_mul_i32 s50, s50, 9216\n" \
"ds_read_b64_tr_b16 a[144:145], v214 offset:16896\n" \
"ds_read_b64_tr_b16 a[146:147], v214 offset:18944\n" \
"v_mfma_f32_16x16x32_bf16 v[88:91], a[112:115], a[72:75], v[88:91]\n" \
"s_add_u32 s68, s68, s50\n" \
"ds_read_b64_tr_b16 a[148:149], v214 offset:25088\n" \
"ds_read_b64_tr_b16 a[150:151], v214 offset:27136\n" \
"v_mfma_f32_16x16x32_bf16 v[88:91], a[116:119], a[76:79], v[88:91]\n" \
"s_add_u32 s68, s68, %3\n" \
"ds_read_b64_tr_b16 a[152:153], v215 offset:16896\n" \
"ds_read_b64_tr_b16 a[154:155], v215 offset:18944\n" \
"v_mfma_f32_16x16x32_bf16 v[104:107], a[112:115], a[80:83], v[104:107]\n" \
"ds_read_b64_tr_b16 a[156:157], v215 offset:25088\n" \
"ds_read_b64_tr_b16 a[158:159], v215 offset:27136\n" \
"v_mfma_f32_16x16x32_bf16 v[104:107], a[116:119], a[84:87], v[104:107]\n" \
"ds_read_b128 a[24:27], v213 offset:256\n" \
"ds_read_b128 a[28:31], v213 offset:2304\n" \
"v_mfma_f32_16x16x32_bf16 v[108:111], a[120:123], a[80:83], v[108:111]\n" \
"ds_read_b128 a[8:11], v224 offset:0\n" \
"ds_read_b128 a[12:15], v224 offset:2048\n" \
"v_mfma_f32_16x16x32_bf16 v[108:111], a[124:127], a[84:87], v[108:111]\n" \
"ds_read_b64_tr_b16 a[160:161], v225 offset:16384\n" \
"ds_read_b64_tr_b16 a[162:163], v225 offset:18432\n" \
"v_mfma_f32_16x16x32_bf16 v[124:127], a[120:123], a[88:91], v[124:127]\n" \
"ds_read_b64_tr_b16 a[164:165], v225 offset:24576\n" \
"ds_read_b64_tr_b16 a[166:167], v225 offset:26624\n" \
"v_mfma_f32_16x16x32_bf16 v[124:127], a[124:127], a[92:95], v[124:127]\n" \
"ds_read_b64_tr_b16 a[168:169], v226 offset:16384\n" \
"ds_read_b64_tr_b16 a[170:171], v226 offset:18432\n" \
"v_mfma_f32_16x16x32_bf16 v[120:123], a[112:115], a[88:91], v[120:123]\n" \
"ds_read_b64_tr_b16 a[172:173], v226 offset:24576\n" \
"ds_read_b64_tr_b16 a[174:175], v226 offset:26624\n" \
"v_mfma_f32_16x16x32_bf16 v[120:123], a[116:119], a[92:95], v[120:123]\n" \
"s_waitcnt lgkmcnt(8)\n" \
"ds_read_b64_tr_b16 a[176:177], v225 offset:16896\n" \
"ds_read_b64_tr_b16 a[178:179], v225 offset:18944\n" \
"v_mfma_f32_16x16x32_bf16 v[64:67], a[128:131], a[0:3], v[64:67]\n" \
"ds_read_b64_tr_b16 a[180:181], v225 offset:25088\n" \
"ds_read_b64_tr_b16 a[182:183], v225 offset:27136\n" \
"v_mfma_f32_16x16x32_bf16 v[64:67], a[132:135], a[4:7], v[64:67]\n" \
"ds_read_b64_tr_b16 a[184:185], v226 offset:16896\n" \
"ds_read_b64_tr_b16 a[186:187], v226 offset:18944\n" \
"v_mfma_f32_16x16x32_bf16 v[68:71], a[136:139], a[0:3], v[68:71]\n" \
"ds_read_b64_tr_b16 a[188:189], v226 offset:25088\n" \
"ds_read_b64_tr_b16 a[190:191], v226 offset:27136\n" \
"v_mfma_f32_16x16x32_bf16 v[68:71], a[140:143], a[4:7], v[68:71]\n" \
"v_lshrrev_b32 v132, 4, v234\n" \
"v_and_b32 v133, 15, v132\n" \
"v_mfma_f32_16x16x32_bf16 v[72:75], a[144:147], a[0:3], v[72:75]\n" \
"v_mul_u32_u24 v128, 144, v133\n" \
"v_lshrrev_b32 v133, 4, v132\n" \
"v_mfma_f32_16x16x32_bf16 v[72:75], a[148:151], a[4:7], v[72:75]\n" \
"v_lshl_add_u32 v128, v133, 3, v128\n" \
"v_add_u32 v128, s68, v128\n" \
"v_mfma_f32_16x16x32_bf16 v[76:79], a[152:155], a[0:3], v[76:79]\n" \
"v_lshrrev_b32 v133, 3, v132\n" \
"v_and_b32 v134, 7, v132\n" \
"v_mfma_f32_16x16x32_bf16 v[76:79], a[156:159], a[4:7], v[76:79]\n" \
"v_mul_u32_u24 v129, 144, v133\n" \
"v_lshl_add_u32 v129, v134, 4, v129\n" \
"v_mfma_f32_16x16x32_bf16 v[92:95], a[152:155], a[24:27], v[92:95]\n" \
"v_add_u32 v129, s68, v129\n" \
"v_lshlrev_b32 v134, 4, v134\n" \
"v_mfma_f32_16x16x32_bf16 v[92:95], a[156:159], a[28:31], v[92:95]\n" \
"v_lshl_add_u32 v130, v133, 13, v134\n" \
"v_mfma_f32_16x16x32_bf16 v[88:91], a[144:147], a[24:27], v[88:91]\n" \
"v_mfma_f32_16x16x32_bf16 v[88:91], a[148:151], a[28:31], v[88:91]\n" \
"v_mfma_f32_16x16x32_bf16 v[84:87], a[136:139], a[24:27], v[84:87]\n" \
"s_waitcnt vmcnt(0)\n" \
"s_barrier\n" \
"v_mfma_f32_16x16x32_bf16 v[84:87], a[140:143], a[28:31], v[84:87]\n" \
"ds_read_b128 a[32:35], v224 offset:256\n" \
"ds_read_b128 a[36:39], v224 offset:2304\n" \
"v_mfma_f32_16x16x32_bf16 v[80:83], a[128:131], a[24:27], v[80:83]\n" \
"ds_read_b128 a[16:19], v227 offset:0\n" \
"ds_read_b128 a[20:23], v227 offset:2048\n" \
"v_mfma_f32_16x16x32_bf16 v[80:83], a[132:135], a[28:31], v[80:83]\n" \
"s_waitcnt lgkmcnt(4)\n" \
"ds_read_b64_tr_b16 a[192:193], v228 offset:16384\n" \
"ds_read_b64_tr_b16 a[194:195], v228 offset:18432\n" \
"v_mfma_f32_16x16x32_bf16 v[64:67], a[160:163], a[8:11], v[64:67]\n" \
"ds_read_b64_tr_b16 a[196:197], v228 offset:24576\n" \
"ds_read_b64_tr_b16 a[198:199], v228 offset:26624\n" \
"v_mfma_f32_16x16x32_bf16 v[64:67], a[164:167], a[12:15], v[64:67]\n" \
"ds_read_b64_tr_b16 a[200:201], v229 offset:16384\n" \
"ds_read_b64_tr_b16 a[202:203], v229 offset:18432\n" \
"v_mfma_f32_16x16x32_bf16 v[68:71], a[168:171], a[8:11], v[68:71]\n" \
"ds_read_b64_tr_b16 a[204:205], v229 offset:24576\n" \
"ds_read_b64_tr_b16 a[206:207], v229 offset:26624\n" \
"v_mfma_f32_16x16x32_bf16 v[68:71], a[172:175], a[12:15], v[68:71]\n" \
"ds_read_b64_tr_b16 a[208:209], v228 offset:16896\n" \
"ds_read_b64_tr_b16 a[210:211], v228 offset:18944\n" \
"v_mfma_f32_16x16x32_bf16 v[72:75], a[176:179], a[8:11], v[72:75]\n" \
"ds_read_b64_tr_b16 a[212:213], v228 offset:25088\n" \
"ds_read_b64_tr_b16 a[214:215], v228 offset:27136\n" \
"v_mfma_f32_16x16x32_bf16 v[72:75], a[180:183], a[12:15], v[72:75]\n" \
"ds_read_b64_tr_b16 a[216:217], v229 offset:16896\n" \
"ds_read_b64_tr_b16 a[218:219], v229 offset:18944\n" \
"v_mfma_f32_16x16x32_bf16 v[76:79], a[184:187], a[8:11], v[76:79]\n" \
"ds_read_b64_tr_b16 a[220:221], v229 offset:25088\n" \
"ds_read_b64_tr_b16 a[222:223], v229 offset:27136\n" \
"v_mfma_f32_16x16x32_bf16 v[76:79], a[188:191], a[12:15], v[76:79]\n" \
"s_waitcnt lgkmcnt(8)\n" \
"ds_read_b128 a[40:43], v227 offset:256\n" \
"ds_read_b128 a[44:47], v227 offset:2304\n" \
"v_mfma_f32_16x16x32_bf16 v[92:95], a[184:187], a[32:35], v[92:95]\n" \
"ds_read_b128 a[248:251], v213 offset:4096\n" \
"ds_read_b128 a[252:255], v213 offset:6144\n" \
"v_mfma_f32_16x16x32_bf16 v[92:95], a[188:191], a[36:39], v[92:95]\n" \
"ds_read_b128 a[48:51], v224 offset:4096\n" \
"ds_read_b128 a[52:55], v224 offset:6144\n" \
"v_mfma_f32_16x16x32_bf16 v[88:91], a[176:179], a[32:35], v[88:91]\n" \
"ds_read_b128 a[56:59], v227 offset:4096\n" \
"ds_read_b128 a[60:63], v227 offset:6144\n" \
"v_mfma_f32_16x16x32_bf16 v[88:91], a[180:183], a[36:39], v[88:91]\n" \
"ds_read_b128 a[224:227], v213 offset:4352\n" \
"ds_read_b128 a[228:231], v213 offset:6400\n" \
"v_mfma_f32_16x16x32_bf16 v[84:87], a[168:171], a[32:35], v[84:87]\n" \
"ds_read_b128 a[232:235], v224 offset:4352\n" \
"ds_read_b128 a[236:239], v224 offset:6400\n" \
"v_mfma_f32_16x16x32_bf16 v[84:87], a[172:175], a[36:39], v[84:87]\n" \
"ds_read_b128 a[240:243], v227 offset:4352\n" \
"ds_read_b128 a[244:247], v227 offset:6400\n" \
"v_mfma_f32_16x16x32_bf16 v[80:83], a[160:163], a[32:35], v[80:83]\n" \
"v_mfma_f32_16x16x32_bf16 v[80:83], a[164:167], a[36:39], v[80:83]\n" \
"v_mfma_f32_16x16x32_bf16 v[64:67], a[192:195], a[16:19], v[64:67]\n" \
"v_mfma_f32_16x16x32_bf16 v[64:67], a[196:199], a[20:23], v[64:67]\n" \
"v_mfma_f32_16x16x32_bf16 v[68:71], a[200:203], a[16:19], v[68:71]\n" \
"v_mfma_f32_16x16x32_bf16 v[68:71], a[204:207], a[20:23], v[68:71]\n" \
"s_waitcnt lgkmcnt(0)\n" \
"v_mfma_f32_16x16x32_bf16 v[72:75], a[208:211], a[16:19], v[72:75]\n" \
"v_mfma_f32_16x16x32_bf16 v[72:75], a[212:215], a[20:23], v[72:75]\n" \
"v_cvt_pk_bf16_f32 v136, v64, v65\n" \
"v_cvt_pk_bf16_f32 v137, v66, v67\n" \
"v_mfma_f32_16x16x32_bf16 v[76:79], a[216:219], a[16:19], v[76:79]\n" \
"v_mfma_f32_16x16x32_bf16 v[76:79], a[220:223], a[20:23], v[76:79]\n" \
"ds_write_b64 v128, v[136:137] offset:0\n" \
"v_mfma_f32_16x16x32_bf16 v[80:83], a[192:195], a[40:43], v[80:83]\n" \
"v_cvt_pk_bf16_f32 v138, v68, v69\n" \
"v_cvt_pk_bf16_f32 v139, v70, v71\n" \
"v_mfma_f32_16x16x32_bf16 v[80:83], a[196:199], a[44:47], v[80:83]\n" \
"ds_write_b64 v128, v[138:139] offset:32\n" \
"v_mfma_f32_16x16x32_bf16 v[84:87], a[200:203], a[40:43], v[84:87]\n" \
"v_cvt_pk_bf16_f32 v136, v72, v73\n" \
"v_cvt_pk_bf16_f32 v137, v74, v75\n" \
"v_mfma_f32_16x16x32_bf16 v[84:87], a[204:207], a[44:47], v[84:87]\n" \
"ds_write_b64 v128, v[136:137] offset:64\n" \
"v_mfma_f32_16x16x32_bf16 v[88:91], a[208:211], a[40:43], v[88:91]\n" \
"v_cvt_pk_bf16_f32 v138, v76, v77\n" \
"v_cvt_pk_bf16_f32 v139, v78, v79\n" \
"v_mfma_f32_16x16x32_bf16 v[88:91], a[212:215], a[44:47], v[88:91]\n" \
"ds_write_b64 v128, v[138:139] offset:96\n" \
"v_mfma_f32_16x16x32_bf16 v[92:95], a[216:219], a[40:43], v[92:95]\n" \
"ds_read_b128 v[140:143], v129 offset:0\n" \
"ds_read_b128 v[144:147], v129 offset:1152\n" \
"v_mfma_f32_16x16x32_bf16 v[92:95], a[220:223], a[44:47], v[92:95]\n" \
"v_cvt_pk_bf16_f32 v136, v80, v81\n" \
"v_cvt_pk_bf16_f32 v137, v82, v83\n" \
"v_mfma_f32_16x16x32_bf16 v[96:99], a[128:131], a[248:251], v[96:99]\n" \
"ds_write_b64 v128, v[136:137] offset:2304\n" \
"v_mfma_f32_16x16x32_bf16 v[96:99], a[132:135], a[252:255], v[96:99]\n" \
"s_add_u32 s70, s88, 0\n" \
"s_waitcnt lgkmcnt(2)\n" \
"v_cvt_pk_bf16_f32 v138, v84, v85\n" \
"v_mfma_f32_16x16x32_bf16 v[96:99], a[160:163], a[48:51], v[96:99]\n" \
"buffer_store_dwordx4 v[140:143], v130, s[52:55], s70 offen sc1\n" \
"v_mfma_f32_16x16x32_bf16 v[96:99], a[164:167], a[52:55], v[96:99]\n" \
"v_cvt_pk_bf16_f32 v139, v86, v87\n" \
"v_mfma_f32_16x16x32_bf16 v[96:99], a[192:195], a[56:59], v[96:99]\n" \
"s_add_u32 s70, s88, 65536\n" \
"s_waitcnt lgkmcnt(1)\n" \
"v_mfma_f32_16x16x32_bf16 v[96:99], a[196:199], a[60:63], v[96:99]\n" \
"buffer_store_dwordx4 v[144:147], v130, s[52:55], s70 offen sc1\n" \
"ds_write_b64 v128, v[138:139] offset:2336\n" \
"v_mfma_f32_16x16x32_bf16 v[100:103], a[204:207], a[60:63], v[100:103]\n" \
"v_cvt_pk_bf16_f32 v136, v88, v89\n" \
"v_cvt_pk_bf16_f32 v137, v90, v91\n" \
"v_mfma_f32_16x16x32_bf16 v[100:103], a[200:203], a[56:59], v[100:103]\n" \
"ds_write_b64 v128, v[136:137] offset:2368\n" \
"v_mfma_f32_16x16x32_bf16 v[100:103], a[172:175], a[52:55], v[100:103]\n" \
"v_cvt_pk_bf16_f32 v138, v92, v93\n" \
"v_cvt_pk_bf16_f32 v139, v94, v95\n" \
"v_mfma_f32_16x16x32_bf16 v[100:103], a[168:171], a[48:51], v[100:103]\n" \
"v_mfma_f32_16x16x32_bf16 v[100:103], a[140:143], a[252:255], v[100:103]\n" \
"v_mfma_f32_16x16x32_bf16 v[100:103], a[136:139], a[248:251], v[100:103]\n" \
"ds_write_b64 v128, v[138:139] offset:2400\n" \
"v_mfma_f32_16x16x32_bf16 v[104:107], a[144:147], a[248:251], v[104:107]\n" \
"ds_read_b128 v[148:151], v129 offset:2304\n" \
"ds_read_b128 v[152:155], v129 offset:3456\n" \
"v_mfma_f32_16x16x32_bf16 v[104:107], a[148:151], a[252:255], v[104:107]\n" \
"v_cvt_pk_bf16_f32 v136, v96, v97\n" \
"v_cvt_pk_bf16_f32 v137, v98, v99\n" \
"v_mfma_f32_16x16x32_bf16 v[104:107], a[176:179], a[48:51], v[104:107]\n" \
"ds_write_b64 v128, v[136:137] offset:4608\n" \
"v_mfma_f32_16x16x32_bf16 v[104:107], a[180:183], a[52:55], v[104:107]\n" \
"v_cvt_pk_bf16_f32 v138, v100, v101\n" \
"v_cvt_pk_bf16_f32 v139, v102, v103\n" \
"v_mfma_f32_16x16x32_bf16 v[104:107], a[208:211], a[56:59], v[104:107]\n" \
"s_add_u32 s70, s88, 4096\n" \
"s_waitcnt lgkmcnt(2)\n" \
"v_mfma_f32_16x16x32_bf16 v[104:107], a[212:215], a[60:63], v[104:107]\n" \
"buffer_store_dwordx4 v[148:151], v130, s[52:55], s70 offen sc1\n" \
"ds_write_b64 v128, v[138:139] offset:4640\n" \
"v_mfma_f32_16x16x32_bf16 v[108:111], a[220:223], a[60:63], v[108:111]\n" \
"s_add_u32 s70, s88, 69632\n" \
"s_waitcnt lgkmcnt(2)\n" \
"v_mfma_f32_16x16x32_bf16 v[108:111], a[216:219], a[56:59], v[108:111]\n" \
"buffer_store_dwordx4 v[152:155], v130, s[52:55], s70 offen sc1\n" \
"v_mfma_f32_16x16x32_bf16 v[108:111], a[188:191], a[52:55], v[108:111]\n" \
"v_mfma_f32_16x16x32_bf16 v[108:111], a[184:187], a[48:51], v[108:111]\n" \
"v_cvt_pk_bf16_f32 v136, v104, v105\n" \
"v_cvt_pk_bf16_f32 v137, v106, v107\n" \
"v_mfma_f32_16x16x32_bf16 v[108:111], a[156:159], a[252:255], v[108:111]\n" \
"v_mfma_f32_16x16x32_bf16 v[108:111], a[152:155], a[248:251], v[108:111]\n" \
"ds_write_b64 v128, v[136:137] offset:4672\n" \
"v_mfma_f32_16x16x32_bf16 v[112:115], a[128:131], a[224:227], v[112:115]\n" \
"v_mfma_f32_16x16x32_bf16 v[112:115], a[132:135], a[228:231], v[112:115]\n" \
"v_mfma_f32_16x16x32_bf16 v[112:115], a[160:163], a[232:235], v[112:115]\n" \
"v_mfma_f32_16x16x32_bf16 v[112:115], a[164:167], a[236:239], v[112:115]\n" \
"v_cvt_pk_bf16_f32 v138, v108, v109\n" \
"v_cvt_pk_bf16_f32 v139, v110, v111\n" \
"v_mfma_f32_16x16x32_bf16 v[112:115], a[192:195], a[240:243], v[112:115]\n" \
"v_mfma_f32_16x16x32_bf16 v[112:115], a[196:199], a[244:247], v[112:115]\n" \
"ds_write_b64 v128, v[138:139] offset:4704\n" \
"v_mfma_f32_16x16x32_bf16 v[116:119], a[204:207], a[244:247], v[116:119]\n" \
"ds_read_b128 v[156:159], v129 offset:4608\n" \
"ds_read_b128 v[160:163], v129 offset:5760\n" \
"v_mfma_f32_16x16x32_bf16 v[116:119], a[200:203], a[240:243], v[116:119]\n" \
"v_mfma_f32_16x16x32_bf16 v[116:119], a[172:175], a[236:239], v[116:119]\n" \
"v_mfma_f32_16x16x32_bf16 v[116:119], a[168:171], a[232:235], v[116:119]\n" \
"v_cvt_pk_bf16_f32 v136, v112, v113\n" \
"v_cvt_pk_bf16_f32 v137, v114, v115\n" \
"v_mfma_f32_16x16x32_bf16 v[116:119], a[140:143], a[228:231], v[116:119]\n" \
"s_add_u32 s70, s88, 131072\n" \
"s_waitcnt lgkmcnt(1)\n" \
"v_mfma_f32_16x16x32_bf16 v[116:119], a[136:139], a[224:227], v[116:119]\n" \
"buffer_store_dwordx4 v[156:159], v130, s[52:55], s70 offen sc1\n" \
"ds_write_b64 v128, v[136:137] offset:6912\n" \
"v_mfma_f32_16x16x32_bf16 v[120:123], a[144:147], a[224:227], v[120:123]\n" \
"s_add_u32 s70, s88, 196608\n" \
"s_waitcnt lgkmcnt(1)\n" \
"v_mfma_f32_16x16x32_bf16 v[120:123], a[148:151], a[228:231], v[120:123]\n" \
"buffer_store_dwordx4 v[160:163], v130, s[52:55], s70 offen sc1\n" \
"v_mfma_f32_16x16x32_bf16 v[120:123], a[176:179], a[232:235], v[120:123]\n" \
"v_mfma_f32_16x16x32_bf16 v[120:123], a[180:183], a[236:239], v[120:123]\n" \
"v_cvt_pk_bf16_f32 v138, v116, v117\n" \
"v_cvt_pk_bf16_f32 v139, v118, v119\n" \
"v_mfma_f32_16x16x32_bf16 v[120:123], a[208:211], a[240:243], v[120:123]\n" \
"v_mfma_f32_16x16x32_bf16 v[120:123], a[212:215], a[244:247], v[120:123]\n" \
"ds_write_b64 v128, v[138:139] offset:6944\n" \
"v_mfma_f32_16x16x32_bf16 v[124:127], a[220:223], a[244:247], v[124:127]\n" \
"v_mfma_f32_16x16x32_bf16 v[124:127], a[216:219], a[240:243], v[124:127]\n" \
"v_mfma_f32_16x16x32_bf16 v[124:127], a[188:191], a[236:239], v[124:127]\n" \
"v_mfma_f32_16x16x32_bf16 v[124:127], a[184:187], a[232:235], v[124:127]\n" \
"v_cvt_pk_bf16_f32 v136, v120, v121\n" \
"v_cvt_pk_bf16_f32 v137, v122, v123\n" \
"v_mfma_f32_16x16x32_bf16 v[124:127], a[156:159], a[228:231], v[124:127]\n" \
"v_mfma_f32_16x16x32_bf16 v[124:127], a[152:155], a[224:227], v[124:127]\n" \
"ds_write_b64 v128, v[136:137] offset:6976\n" \
"s_nop 7\n" \
"v_cvt_pk_bf16_f32 v138, v124, v125\n" \
"v_cvt_pk_bf16_f32 v139, v126, v127\n" \
"ds_write_b64 v128, v[138:139] offset:7008\n" \
"ds_read_b128 v[164:167], v129 offset:6912\n" \
"ds_read_b128 v[168:171], v129 offset:8064\n" \
"s_add_u32 s70, s88, 135168\n" \
"s_waitcnt lgkmcnt(1)\n" \
"buffer_store_dwordx4 v[164:167], v130, s[52:55], s70 offen sc1\n" \
"s_add_u32 s70, s88, 200704\n" \
"s_waitcnt lgkmcnt(0)\n" \
"buffer_store_dwordx4 v[168:171], v130, s[52:55], s70 offen sc1\n" \
"s_endpgm\n" \
"Lhot_end_%=:\n" \
"Lpl1_%=:\n" \
"s_mov_b32 s72, 0\n" \
"Lpk1_%=:\n" \
"s_sleep 2\n" \
"buffer_load_dword v227, v242, s[56:59], s90 offen sc1\n" \
"s_waitcnt vmcnt(0)\n" \
"v_cmp_eq_u32 vcc, s60, v227\n" \
"s_nop 1\n" \
"s_andn2_b32 s70, 0xff, vcc_lo\n" \
"s_cbranch_scc0 Lpr1_%=\n" \
"s_add_u32 s72, s72, 1\n" \
"s_cmp_lt_u32 s72, 0x40000\n" \
"s_cbranch_scc1 Lpk1_%=\n" \
"s_branch Lpr1_%=\n" \
"Lpl2_%=:\n" \
"s_mov_b32 s72, 0\n" \
"Lpk2_%=:\n" \
"s_sleep 2\n" \
"buffer_load_dword v239, v242, s[56:59], s95 offen sc1\n" \
"s_waitcnt vmcnt(0)\n" \
"v_cmp_eq_u32 vcc, s60, v239\n" \
"s_nop 1\n" \
"s_andn2_b32 s70, s69, vcc_lo\n" \
"s_cbranch_scc0 Lpr2_%=\n" \
"s_add_u32 s72, s72, 1\n" \
"s_cmp_lt_u32 s72, 0x40000\n" \
"s_cbranch_scc1 Lpk2_%=\n" \
"s_branch Lpr2_%=\n" \
"Lpl3_%=:\n" \
"s_mov_b32 s72, 0\n" \
"Lpk3_%=:\n" \
"s_sleep 2\n" \
"buffer_load_dword v239, v242, s[56:59], s95 offen sc1\n" \
"s_waitcnt vmcnt(0)\n" \
"v_cmp_eq_u32 vcc, s60, v239\n" \
"s_nop 1\n" \
"s_andn2_b32 s70, s69, vcc_lo\n" \
"s_cbranch_scc0 Lpr3_%=\n" \
"s_add_u32 s72, s72, 1\n" \
"s_cmp_lt_u32 s72, 0x40000\n" \
"s_cbranch_scc1 Lpk3_%=\n" \
"s_branch Lpr3_%=\n" \
"Lexact_%=:\n" \
"v_mov_b32 v239, 0xff800000\n" \
"s_and_b32 s70, s61, 1\n" \
"s_lshl_b32 s70, s70, 4\n" \
"v_add_u32 v186, s70, v238\n" \
"v_accvgpr_read_b32 v160, a128\n" \
"v_accvgpr_read_b32 v161, a129\n" \
"v_accvgpr_read_b32 v162, a130\n" \
"v_accvgpr_read_b32 v163, a131\n" \
"v_accvgpr_read_b32 v164, a132\n" \
"v_accvgpr_read_b32 v165, a133\n" \
"v_accvgpr_read_b32 v166, a134\n" \
"v_accvgpr_read_b32 v167, a135\n" \
"v_accvgpr_read_b32 v168, a136\n" \
"v_accvgpr_read_b32 v169, a137\n" \
"v_accvgpr_read_b32 v170, a138\n" \
"v_accvgpr_read_b32 v171, a139\n" \
"v_accvgpr_read_b32 v172, a140\n" \
"v_accvgpr_read_b32 v173, a141\n" \
"v_accvgpr_read_b32 v174, a142\n" \
"v_accvgpr_read_b32 v175, a143\n" \
"s_add_u32 s83, s85, 0\n" \
"s_add_u32 s84, s87, 0\n" \
"s_call_b64 s[64:65], Lex1_%=\n" \
"v_accvgpr_read_b32 v160, a144\n" \
"v_accvgpr_read_b32 v161, a145\n" \
"v_accvgpr_read_b32 v162, a146\n" \
"v_accvgpr_read_b32 v163, a147\n" \
"v_accvgpr_read_b32 v164, a148\n" \
"v_accvgpr_read_b32 v165, a149\n" \
"v_accvgpr_read_b32 v166, a150\n" \
"v_accvgpr_read_b32 v167, a151\n" \
"v_accvgpr_read_b32 v168, a152\n" \
"v_accvgpr_read_b32 v169, a153\n" \
"v_accvgpr_read_b32 v170, a154\n" \
"v_accvgpr_read_b32 v171, a155\n" \
"v_accvgpr_read_b32 v172, a156\n" \
"v_accvgpr_read_b32 v173, a157\n" \
"v_accvgpr_read_b32 v174, a158\n" \
"v_accvgpr_read_b32 v175, a159\n" \
"s_add_u32 s83, s85, 2\n" \
"s_add_u32 s84, s87, 532480\n" \
"s_call_b64 s[64:65], Lex1_%=\n" \
"v_accvgpr_read_b32 v160, a160\n" \
"v_accvgpr_read_b32 v161, a161\n" \
"v_accvgpr_read_b32 v162, a162\n" \
"v_accvgpr_read_b32 v163, a163\n" \
"v_accvgpr_read_b32 v164, a164\n" \
"v_accvgpr_read_b32 v165, a165\n" \
"v_accvgpr_read_b32 v166, a166\n" \
"v_accvgpr_read_b32 v167, a167\n" \
"v_accvgpr_read_b32 v168, a168\n" \
"v_accvgpr_read_b32 v169, a169\n" \
"v_accvgpr_read_b32 v170, a170\n" \
"v_accvgpr_read_b32 v171, a171\n" \
"v_accvgpr_read_b32 v172, a172\n" \
"v_accvgpr_read_b32 v173, a173\n" \
"v_accvgpr_read_b32 v174, a174\n" \
"v_accvgpr_read_b32 v175, a175\n" \
"s_add_u32 s83, s86, 0\n" \
"s_add_u32 s84, s88, 0\n" \
"s_call_b64 s[64:65], Lex1_%=\n" \
"v_accvgpr_read_b32 v160, a176\n" \
"v_accvgpr_read_b32 v161, a177\n" \
"v_accvgpr_read_b32 v162, a178\n" \
"v_accvgpr_read_b32 v163, a179\n" \
"v_accvgpr_read_b32 v164, a180\n" \
"v_accvgpr_read_b32 v165, a181\n" \
"v_accvgpr_read_b32 v166, a182\n" \
"v_accvgpr_read_b32 v167, a183\n" \
"v_accvgpr_read_b32 v168, a184\n" \
"v_accvgpr_read_b32 v169, a185\n" \
"v_accvgpr_read_b32 v170, a186\n" \
"v_accvgpr_read_b32 v171, a187\n" \
"v_accvgpr_read_b32 v172, a188\n" \
"v_accvgpr_read_b32 v173, a189\n" \
"v_accvgpr_read_b32 v174, a190\n" \
"v_accvgpr_read_b32 v175, a191\n" \
"s_add_u32 s83, s86, 2\n" \
"s_add_u32 s84, s88, 532480\n" \
"s_call_b64 s[64:65], Lex1_%=\n" \
"s_waitcnt vmcnt(0)\n" \
"s_branch Lexr_%=\n" \
"Lex1_%=:\n" \
"v_mov_b32 v128, 0\n" \
"v_mov_b32 v129, 0\n" \
"v_mov_b32 v130, 0\n" \
"v_mov_b32 v131, 0\n" \
"v_mov_b32 v132, 0\n" \
"v_mov_b32 v133, 0\n" \
"v_mov_b32 v134, 0\n" \
"v_mov_b32 v135, 0\n" \
"v_mov_b32 v136, 0\n" \
"v_mov_b32 v137, 0\n" \
"v_mov_b32 v138, 0\n" \
"v_mov_b32 v139, 0\n" \
"v_mov_b32 v140, 0\n" \
"v_mov_b32 v141, 0\n" \
"v_mov_b32 v142, 0\n" \
"v_mov_b32 v143, 0\n" \
"v_mov_b32 v144, 0\n" \
"v_mov_b32 v145, 0\n" \
"v_mov_b32 v146, 0\n" \
"v_mov_b32 v147, 0\n" \
"v_mov_b32 v148, 0\n" \
"v_mov_b32 v149, 0\n" \
"v_mov_b32 v150, 0\n" \
"v_mov_b32 v151, 0\n" \
"v_mov_b32 v152, 0\n" \
"v_mov_b32 v153, 0\n" \
"v_mov_b32 v154, 0\n" \
"v_mov_b32 v155, 0\n" \
"v_mov_b32 v156, 0\n" \
"v_mov_b32 v157, 0\n" \
"v_mov_b32 v158, 0\n" \
"v_mov_b32 v159, 0\n" \
"v_mov_b32 v184, 0xf149f2ca\n" \
"v_mov_b32 v185, 0\n" \
"s_mov_b32 s81, 0\n" \
"s_mul_i32 s82, s62, 557824\n" \
"Lex2_%=:\n" \
"s_add_u32 s70, s82, 4096\n" \
"buffer_load_dwordx4 a[64:67], v236, s[56:59], s82 offen offset:0 sc1\n" \
"buffer_load_dwordx4 a[68:71], v236, s[56:59], s82 offen offset:2048 sc1\n" \
"buffer_load_dwordx4 a[72:75], v236, s[56:59], s70 offen offset:0 sc1\n" \
"buffer_load_dwordx4 a[76:79], v236, s[56:59], s70 offen offset:2048 sc1\n" \
"buffer_load_dwordx4 a[80:83], v236, s[56:59], s82 offen offset:256 sc1\n" \
"buffer_load_dwordx4 a[84:87], v236, s[56:59], s82 offen offset:2304 sc1\n" \
"buffer_load_dwordx4 a[88:91], v236, s[56:59], s70 offen offset:256 sc1\n" \
"buffer_load_dwordx4 a[92:95], v236, s[56:59], s70 offen offset:2304 sc1\n" \
"s_add_u32 s70, s82, 17850368\n" \
"s_add_u32 s71, s70, 4096\n" \
"buffer_load_dwordx4 a[96:99], v234, s[56:59], s70 offen offset:0 sc1\n" \
"buffer_load_dwordx4 a[100:103], v234, s[56:59], s70 offen offset:1024 sc1\n" \
"buffer_load_dwordx4 a[104:107], v234, s[56:59], s70 offen offset:2048 sc1\n" \
"buffer_load_dwordx4 a[108:111], v234, s[56:59], s70 offen offset:3072 sc1\n" \
"buffer_load_dwordx4 a[112:115], v234, s[56:59], s71 offen offset:0 sc1\n" \
"buffer_load_dwordx4 a[116:119], v234, s[56:59], s71 offen offset:1024 sc1\n" \
"buffer_load_dwordx4 a[120:123], v234, s[56:59], s71 offen offset:2048 sc1\n" \
"buffer_load_dwordx4 a[124:127], v234, s[56:59], s71 offen offset:3072 sc1\n" \
"s_waitcnt vmcnt(0)\n" \
"v_mfma_f32_16x16x32_bf16 v[176:179], a[64:67], v[160:163], 0\n" \
"v_mfma_f32_16x16x32_bf16 v[176:179], a[68:71], v[164:167], v[176:179]\n" \
"v_mfma_f32_16x16x32_bf16 v[176:179], a[72:75], v[168:171], v[176:179]\n" \
"v_mfma_f32_16x16x32_bf16 v[176:179], a[76:79], v[172:175], v[176:179]\n" \
"v_mfma_f32_16x16x32_bf16 v[180:183], a[80:83], v[160:163], 0\n" \
"v_mfma_f32_16x16x32_bf16 v[180:183], a[84:87], v[164:167], v[180:183]\n" \
"v_mfma_f32_16x16x32_bf16 v[180:183], a[88:91], v[168:171], v[180:183]\n" \
"v_mfma_f32_16x16x32_bf16 v[180:183], a[92:95], v[172:175], v[180:183]\n" \
"s_nop 7\n" \
"s_nop 1\n" \
"s_cmp_eq_u32 s81, s83\n" \
"s_cbranch_scc0 Lex3_%=\n" \
"v_cmp_gt_i32 vcc, 0, v186\n" \
"s_nop 1\n" \
"v_cndmask_b32 v176, v176, v239, vcc\n" \
"v_cmp_gt_i32 vcc, 1, v186\n" \
"s_nop 1\n" \
"v_cndmask_b32 v177, v177, v239, vcc\n" \
"v_cmp_gt_i32 vcc, 2, v186\n" \
"s_nop 1\n" \
"v_cndmask_b32 v178, v178, v239, vcc\n" \
"v_cmp_gt_i32 vcc, 3, v186\n" \
"s_nop 1\n" \
"v_cndmask_b32 v179, v179, v239, vcc\n" \
"v_cmp_gt_i32 vcc, 16, v186\n" \
"s_nop 1\n" \
"v_cndmask_b32 v180, v180, v239, vcc\n" \
"v_cmp_gt_i32 vcc, 17, v186\n" \
"s_nop 1\n" \
"v_cndmask_b32 v181, v181, v239, vcc\n" \
"v_cmp_gt_i32 vcc, 18, v186\n" \
"s_nop 1\n" \
"v_cndmask_b32 v182, v182, v239, vcc\n" \
"v_cmp_gt_i32 vcc, 19, v186\n" \
"s_nop 1\n" \
"v_cndmask_b32 v183, v183, v239, vcc\n" \
"Lex3_%=:\n" \
"v_max3_f32 v187, v176, v177, v178\n" \
"v_max3_f32 v187, v187, v179, v180\n" \
"v_max3_f32 v187, v187, v181, v182\n" \
"v_max3_f32 v187, v187, v183, v184\n" \
"v_mov_b32 v188, v187\n" \
"s_nop 1\n" \
"v_permlane16_swap_b32 v188, v187\n" \
"v_max_f32 v187, v187, v188\n" \
"v_mov_b32 v188, v187\n" \
"s_nop 1\n" \
"v_permlane32_swap_b32 v188, v187\n" \
"v_max_f32 v187, v187, v188\n" \
"v_sub_f32 v189, v184, v187\n" \
"v_exp_f32 v189, v189\n" \
"v_mov_b32 v184, v187\n" \
"v_sub_f32 v176, v176, v187\n" \
"v_sub_f32 v177, v177, v187\n" \
"v_sub_f32 v178, v178, v187\n" \
"v_sub_f32 v179, v179, v187\n" \
"v_sub_f32 v180, v180, v187\n" \
"v_sub_f32 v181, v181, v187\n" \
"v_sub_f32 v182, v182, v187\n" \
"v_sub_f32 v183, v183, v187\n" \
"v_exp_f32 v176, v176\n" \
"v_exp_f32 v177, v177\n" \
"v_exp_f32 v178, v178\n" \
"v_exp_f32 v179, v179\n" \
"v_exp_f32 v180, v180\n" \
"v_exp_f32 v181, v181\n" \
"v_exp_f32 v182, v182\n" \
"v_exp_f32 v183, v183\n" \
"v_mul_f32 v185, v185, v189\n" \
"v_add_f32 v185, v185, v176\n" \
"v_add_f32 v185, v185, v177\n" \
"v_add_f32 v185, v185, v178\n" \
"v_add_f32 v185, v185, v179\n" \
"v_add_f32 v185, v185, v180\n" \
"v_add_f32 v185, v185, v181\n" \
"v_add_f32 v185, v185, v182\n" \
"v_add_f32 v185, v185, v183\n" \
"v_cvt_pk_bf16_f32 v112, v176, v177\n" \
"v_cvt_pk_bf16_f32 v113, v178, v179\n" \
"v_cvt_pk_bf16_f32 v114, v180, v181\n" \
"v_cvt_pk_bf16_f32 v115, v182, v183\n" \
"v_mul_f32 v128, v128, v189\n" \
"v_mul_f32 v129, v129, v189\n" \
"v_mul_f32 v130, v130, v189\n" \
"v_mul_f32 v131, v131, v189\n" \
"v_mul_f32 v132, v132, v189\n" \
"v_mul_f32 v133, v133, v189\n" \
"v_mul_f32 v134, v134, v189\n" \
"v_mul_f32 v135, v135, v189\n" \
"v_mul_f32 v136, v136, v189\n" \
"v_mul_f32 v137, v137, v189\n" \
"v_mul_f32 v138, v138, v189\n" \
"v_mul_f32 v139, v139, v189\n" \
"v_mul_f32 v140, v140, v189\n" \
"v_mul_f32 v141, v141, v189\n" \
"v_mul_f32 v142, v142, v189\n" \
"v_mul_f32 v143, v143, v189\n" \
"v_mul_f32 v144, v144, v189\n" \
"v_mul_f32 v145, v145, v189\n" \
"v_mul_f32 v146, v146, v189\n" \
"v_mul_f32 v147, v147, v189\n" \
"v_mul_f32 v148, v148, v189\n" \
"v_mul_f32 v149, v149, v189\n" \
"v_mul_f32 v150, v150, v189\n" \
"v_mul_f32 v151, v151, v189\n" \
"v_mul_f32 v152, v152, v189\n" \
"v_mul_f32 v153, v153, v189\n" \
"v_mul_f32 v154, v154, v189\n" \
"v_mul_f32 v155, v155, v189\n" \
"v_mul_f32 v156, v156, v189\n" \
"v_mul_f32 v157, v157, v189\n" \
"v_mul_f32 v158, v158, v189\n" \
"v_mul_f32 v159, v159, v189\n" \
"s_nop 1\n" \
"v_mfma_f32_16x16x32_bf16 v[128:131], a[96:99], v[112:115], v[128:131]\n" \
"v_mfma_f32_16x16x32_bf16 v[132:135], a[100:103], v[112:115], v[132:135]\n" \
"v_mfma_f32_16x16x32_bf16 v[136:139], a[104:107], v[112:115], v[136:139]\n" \
"v_mfma_f32_16x16x32_bf16 v[140:143], a[108:111], v[112:115], v[140:143]\n" \
"v_mfma_f32_16x16x32_bf16 v[144:147], a[112:115], v[112:115], v[144:147]\n" \
"v_mfma_f32_16x16x32_bf16 v[148:151], a[116:119], v[112:115], v[148:151]\n" \
"v_mfma_f32_16x16x32_bf16 v[152:155], a[120:123], v[112:115], v[152:155]\n" \
"v_mfma_f32_16x16x32_bf16 v[156:159], a[124:127], v[112:115], v[156:159]\n" \
"s_nop 7\n" \
"s_nop 1\n" \
"s_and_b32 s70, s81, 1\n" \
"s_mul_i32 s70, s70, 1024\n" \
"s_add_u32 s82, s82, s70\n" \
"s_add_u32 s82, s82, 8192\n" \
"s_add_u32 s81, s81, 1\n" \
"s_cmp_le_u32 s81, s83\n" \
"s_cbranch_scc1 Lex2_%=\n" \
"v_mov_b32 v187, v185\n" \
"s_nop 1\n" \
"v_permlane16_swap_b32 v187, v185\n" \
"v_add_f32 v185, v185, v187\n" \
"v_mov_b32 v187, v185\n" \
"s_nop 1\n" \
"v_permlane32_swap_b32 v187, v185\n" \
"v_add_f32 v185, v185, v187\n" \
"v_rcp_f32 v185, v185\n" \
"s_nop 0\n" \
"v_mul_f32 v128, v128, v185\n" \
"v_mul_f32 v129, v129, v185\n" \
"v_mul_f32 v130, v130, v185\n" \
"v_mul_f32 v131, v131, v185\n" \
"v_mul_f32 v132, v132, v185\n" \
"v_mul_f32 v133, v133, v185\n" \
"v_mul_f32 v134, v134, v185\n" \
"v_mul_f32 v135, v135, v185\n" \
"v_mul_f32 v136, v136, v185\n" \
"v_mul_f32 v137, v137, v185\n" \
"v_mul_f32 v138, v138, v185\n" \
"v_mul_f32 v139, v139, v185\n" \
"v_mul_f32 v140, v140, v185\n" \
"v_mul_f32 v141, v141, v185\n" \
"v_mul_f32 v142, v142, v185\n" \
"v_mul_f32 v143, v143, v185\n" \
"v_mul_f32 v144, v144, v185\n" \
"v_mul_f32 v145, v145, v185\n" \
"v_mul_f32 v146, v146, v185\n" \
"v_mul_f32 v147, v147, v185\n" \
"v_mul_f32 v148, v148, v185\n" \
"v_mul_f32 v149, v149, v185\n" \
"v_mul_f32 v150, v150, v185\n" \
"v_mul_f32 v151, v151, v185\n" \
"v_mul_f32 v152, v152, v185\n" \
"v_mul_f32 v153, v153, v185\n" \
"v_mul_f32 v154, v154, v185\n" \
"v_mul_f32 v155, v155, v185\n" \
"v_mul_f32 v156, v156, v185\n" \
"v_mul_f32 v157, v157, v185\n" \
"v_mul_f32 v158, v158, v185\n" \
"v_mul_f32 v159, v159, v185\n" \
"s_add_u32 s70, s84, 4096\n" \
"v_cvt_pk_bf16_f32 v112, v128, v129\n" \
"v_cvt_pk_bf16_f32 v113, v130, v131\n" \
"v_cvt_pk_bf16_f32 v114, v132, v133\n" \
"v_cvt_pk_bf16_f32 v115, v134, v135\n" \
"buffer_store_dwordx4 v[112:115], v236, s[56:59], s84 offen offset:0 sc1\n" \
"v_cvt_pk_bf16_f32 v116, v136, v137\n" \
"v_cvt_pk_bf16_f32 v117, v138, v139\n" \
"v_cvt_pk_bf16_f32 v118, v140, v141\n" \
"v_cvt_pk_bf16_f32 v119, v142, v143\n" \
"buffer_store_dwordx4 v[116:119], v236, s[56:59], s84 offen offset:2048 sc1\n" \
"v_cvt_pk_bf16_f32 v120, v144, v145\n" \
"v_cvt_pk_bf16_f32 v121, v146, v147\n" \
"v_cvt_pk_bf16_f32 v122, v148, v149\n" \
"v_cvt_pk_bf16_f32 v123, v150, v151\n" \
"buffer_store_dwordx4 v[120:123], v236, s[56:59], s70 offen offset:0 sc1\n" \
"v_cvt_pk_bf16_f32 v124, v152, v153\n" \
"v_cvt_pk_bf16_f32 v125, v154, v155\n" \
"v_cvt_pk_bf16_f32 v126, v156, v157\n" \
"v_cvt_pk_bf16_f32 v127, v158, v159\n" \
"buffer_store_dwordx4 v[124:127], v236, s[56:59], s70 offen offset:2048 sc1\n" \
"s_waitcnt vmcnt(0)\n" \
"s_setpc_b64 s[64:65]\n" \
"Lpl4_%=:\n" \
"s_mov_b32 s72, 0\n" \
"Lpk4_%=:\n" \
"s_sleep 2\n" \
"buffer_load_dwordx2 v[226:227], v228, s[56:59], s13 offen sc1\n" \
"s_waitcnt vmcnt(0)\n" \
"v_cmp_eq_u32 vcc, s60, v226\n" \
"s_nop 1\n" \
"s_andn2_b32 s70, 0xff, vcc_lo\n" \
"s_cbranch_scc0 Lpr4_%=\n" \
"s_add_u32 s72, s72, 1\n" \
"s_cmp_lt_u32 s72, 0x40000\n" \
"s_cbranch_scc1 Lpk4_%=\n" \
"s_branch Lpr4_%=\n" \
"Lpl5_%=:\n" \
"s_mov_b32 s72, 0\n" \
"Lpk5_%=:\n" \
"s_sleep 2\n" \
"buffer_load_dword v220, v221, s[56:59], s66 offen sc1\n" \
"s_waitcnt vmcnt(0)\n" \
"v_cmp_eq_u32 vcc, s60, v220\n" \
"s_nop 1\n" \
"s_andn2_b32 s70, s65, vcc_lo\n" \
"s_cbranch_scc0 Lpr5_%=\n" \
"s_add_u32 s72, s72, 1\n" \
"s_cmp_lt_u32 s72, 0x40000\n" \
"s_cbranch_scc1 Lpk5_%=\n" \
"s_branch Lpr5_%=\n" \
"Lpl6_%=:\n" \
"s_mov_b32 s72, 0\n" \
"Lpk6_%=:\n" \
"s_sleep 2\n" \
"buffer_load_dword v220, v221, s[56:59], s67 offen sc1\n" \
"s_waitcnt vmcnt(0)\n" \
"v_cmp_eq_u32 vcc, s60, v220\n" \
"s_nop 1\n" \
"s_andn2_b32 s70, s74, vcc_lo\n" \
"s_cbranch_scc0 Lpr6_%=\n" \
"s_add_u32 s72, s72, 1\n" \
"s_cmp_lt_u32 s72, 0x40000\n" \
"s_cbranch_scc1 Lpk6_%=\n" \
"s_branch Lpr6_%=\n" \
"Lpl7_%=:\n" \
"s_mov_b32 s72, 0\n" \
"Lpk7_%=:\n" \
"s_sleep 2\n" \
"buffer_load_dword v220, v241, s[56:59], s64 offen sc1\n" \
"s_waitcnt vmcnt(0)\n" \
"v_cmp_eq_u32 vcc, s60, v220\n" \
"s_nop 1\n" \
"s_andn2_b32 s70, s69, vcc_lo\n" \
"s_cbranch_scc0 Lpr7_%=\n" \
"s_add_u32 s72, s72, 1\n" \
"s_cmp_lt_u32 s72, 0x40000\n" \
"s_cbranch_scc1 Lpk7_%=\n" \
"s_branch Lpr7_%=\n" \
"Lcode_end_%=:\n" \
".p2align 12\n" \
""
#define MK_ASM_CLOBBERS "v1", "v2", "v3", "v4", "v5", "v6", "v7", "v8", "v9", "v10", "v11", "v12", "v13", "v14", "v15", "v16", "v17", "v18", "v19", "v20", "v21", "v22", "v23", "v24", "v25", "v26", "v27", "v28", "v29", "v30", "v31", "v32", "v33", "v34", "v35", "v36", "v37", "v38", "v39", "v40", "v41", "v42", "v43", "v44", "v45", "v46", "v47", "v48", "v49", "v50", "v51", "v52", "v53", "v54", "v55", "v56", "v57", "v58", "v59", "v60", "v61", "v62", "v63", "v64", "v65", "v66", "v67", "v68", "v69", "v70", "v71", "v72", "v73", "v74", "v75", "v76", "v77", "v78", "v79", "v80", "v81", "v82", "v83", "v84", "v85", "v86", "v87", "v88", "v89", "v90", "v91", "v92", "v93", "v94", "v95", "v96", "v97", "v98", "v99", "v100", "v101", "v102", "v103", "v104", "v105", "v106", "v107", "v108", "v109", "v110", "v111", "v112", "v113", "v114", "v115", "v116", "v117", "v118", "v119", "v120", "v121", "v122", "v123", "v124", "v125", "v126", "v127", "v128", "v129", "v130", "v131", "v132", "v133", "v134", "v135", "v136", "v137", "v138", "v139", "v140", "v141", "v142", "v143", "v144", "v145", "v146", "v147", "v148", "v149", "v150", "v151", "v152", "v153", "v154", "v155", "v156", "v157", "v158", "v159", "v160", "v161", "v162", "v163", "v164", "v165", "v166", "v167", "v168", "v169", "v170", "v171", "v172", "v173", "v174", "v175", "v176", "v177", "v178", "v179", "v180", "v181", "v182", "v183", "v184", "v185", "v186", "v187", "v188", "v189", "v190", "v191", "v192", "v193", "v194", "v195", "v196", "v197", "v198", "v199", "v200", "v201", "v202", "v203", "v204", "v205", "v206", "v207", "v208", "v209", "v210", "v211", "v212", "v213", "v214", "v215", "v216", "v217", "v218", "v219", "v220", "v221", "v222", "v223", "v224", "v225", "v226", "v227", "v228", "v229", "v230", "v231", "v232", "v233", "v234", "v235", "v236", "v237", "v238", "v239", "v240", "v241", "v242", "v243", "v244", "v245", "v246", "v247", "v248", "v249", "v250", "v251", "v252", "v253", "v254", "v255", "a0", "a1", "a2", "a3", "a4", "a5", "a6", "a7", "a8", "a9", "a10", "a11", "a12", "a13", "a14", "a15", "a16", "a17", "a18", "a19", "a20", "a21", "a22", "a23", "a24", "a25", "a26", "a27", "a28", "a29", "a30", "a31", "a32", "a33", "a34", "a35", "a36", "a37", "a38", "a39", "a40", "a41", "a42", "a43", "a44", "a45", "a46", "a47", "a48", "a49", "a50", "a51", "a52", "a53", "a54", "a55", "a56", "a57", "a58", "a59", "a60", "a61", "a62", "a63", "a64", "a65", "a66", "a67", "a68", "a69", "a70", "a71", "a72", "a73", "a74", "a75", "a76", "a77", "a78", "a79", "a80", "a81", "a82", "a83", "a84", "a85", "a86", "a87", "a88", "a89", "a90", "a91", "a92", "a93", "a94", "a95", "a96", "a97", "a98", "a99", "a100", "a101", "a102", "a103", "a104", "a105", "a106", "a107", "a108", "a109", "a110", "a111", "a112", "a113", "a114", "a115", "a116", "a117", "a118", "a119", "a120", "a121", "a122", "a123", "a124", "a125", "a126", "a127", "a128", "a129", "a130", "a131", "a132", "a133", "a134", "a135", "a136", "a137", "a138", "a139", "a140", "a141", "a142", "a143", "a144", "a145", "a146", "a147", "a148", "a149", "a150", "a151", "a152", "a153", "a154", "a155", "a156", "a157", "a158", "a159", "a160", "a161", "a162", "a163", "a164", "a165", "a166", "a167", "a168", "a169", "a170", "a171", "a172", "a173", "a174", "a175", "a176", "a177", "a178", "a179", "a180", "a181", "a182", "a183", "a184", "a185", "a186", "a187", "a188", "a189", "a190", "a191", "a192", "a193", "a194", "a195", "a196", "a197", "a198", "a199", "a200", "a201", "a202", "a203", "a204", "a205", "a206", "a207", "a208", "a209", "a210", "a211", "a212", "a213", "a214", "a215", "a216", "a217", "a218", "a219", "a220", "a221", "a222", "a223", "a224", "a225", "a226", "a227", "a228", "a229", "a230", "a231", "a232", "a233", "a234", "a235", "a236", "a237", "a238", "a239", "a240", "a241", "a242", "a243", "a244", "a245", "a246", "a247", "a248", "a249", "a250", "a251", "a252", "a253", "a254", "a255", "s8", "s9", "s10", "s11", "s12", "s13", "s14", "s15", "s16", "s17", "s18", "s19", "s20", "s21", "s22", "s23", "s24", "s25", "s26", "s27", "s28", "s29", "s30", "s31", "s36", "s37", "s38", "s39", "s40", "s41", "s42", "s43", "s44", "s45", "s46", "s47", "s48", "s49", "s50", "s51", "s52", "s53", "s54", "s55", "s56", "s57", "s58", "s59", "s60", "s61", "s62", "s63", "s64", "s65", "s66", "s67", "s68", "s69", "s70", "s71", "s72", "s73", "s74", "s75", "s76", "s77", "s78", "s79", "s80", "s81", "s82", "s83", "s84", "s85", "s86", "s87", "s88", "s89", "s90", "s91", "s92", "s93", "s94", "s95", "vcc", "scc", "memory"
namespace mk {
constexpr int LDS_BYTES = 163840;
struct Args { const void* in[9]; void* out; void* ws; };
__global__ __launch_bounds__(256, 1) void mega_k(const Args a) {
    extern __shared__ __attribute__((aligned(16))) unsigned char shm[];
    const unsigned ldsb = (unsigned)(size_t)(LAS unsigned char*)shm;
    asm volatile(MK_ASM_BODY
                 :
                 : "s"(__builtin_amdgcn_kernarg_segment_ptr()), "s"(blockIdx.x), "v"(threadIdx.x), "s"(ldsb)
                 : MK_ASM_CLOBBERS);
}
}
extern "C" void kernel_launch(void* const* d_in, const int* in_sizes, int n_in, void* d_out, int out_size, void* d_ws, size_t ws_size, hipStream_t stream) {
    (void)hipFuncSetAttribute((const void*)mk::mega_k, hipFuncAttributeMaxDynamicSharedMemorySize, mk::LDS_BYTES);
    mk::Args a;
    for (int i = 0; i < 9; ++i) a.in[i] = d_in[i];
    a.out = d_out; a.ws = (char*)d_ws + ((ws_size - WS_END) & ~(size_t)0x1fffff);
    mk::mega_k<<<256, 256, mk::LDS_BYTES, stream>>>(a);
}
```

```cpp
#include <hip/hip_runtime.h>
#include <stdint.h>
#define LAS __attribute__((address_space(3)))
constexpr size_t LAY_TS = 17408, LAY_HS = 557824, LAY_CRS = 266240, LAY_ORS = 69632, WS_K = 0, WS_V = 17850368, WS_CTX = 35700736, WS_OB = 52740096, WS_FL = 65585152, WS_END = 65634304, LAY_VS = 2;
#define MK_ASM_BODY \
"s_load_dwordx16 s[8:23], %0, 0x0\n" \
"s_load_dwordx4 s[24:27], %0, 0x40\n" \
"s_load_dwordx2 s[28:29], %0, 0x50\n" \
"v_readfirstlane_b32 s61, %2\n" \
"s_lshr_b32 s61, s61, 6\n" \
"v_mbcnt_lo_u32_b32 v78, -1, 0\n" \
"v_mbcnt_hi_u32_b32 v78, -1, v78\n" \
"v_lshlrev_b32 v234, 4, v78\n" \
"s_getpc_b64 s[64:65]\n" \
"Lpc_%=:\n" \
"s_lshr_b32 s70, %1, 3\n" \
"s_lshl_b32 s70, s70, 2\n" \
"s_add_u32 s70, s70, s61\n" \
"s_and_b32 s71, s70, 31\n" \
"s_lshl_b32 s71, s71, 12\n" \
"v_lshl_add_u32 v237, v234, 2, s71\n" \
"s_mov_b32 s71, Lcode_end_%=-Lpc_%=-4\n" \
"v_min_u32 v237, s71, v237\n" \
"global_load_dword v238, v237, s[64:65]\n" \
"s_and_b32 s62, %1, 7\n" \
"s_lshl_b32 s62, s62, 2\n" \
"s_lshr_b32 s70, %1, 6\n" \
"s_add_u32 s62, s62, s70\n" \
"s_bfe_u32 s63, %1, 0x30003\n" \
"s_lshl_b32 s74, s61, 13\n" \
"s_add_u32 s74, s74, %3\n" \
"v_bfe_u32 v79, v78, 4, 1\n" \
"v_bfe_u32 v80, v78, 2, 2\n" \
"v_lshl_add_u32 v80, v79, 3, v80\n" \
"s_lshl_b32 s71, s61, 4\n" \
"v_add_u32 v80, s71, v80\n" \
"v_lshlrev_b32 v80, 13, v80\n" \
"v_lshlrev_b32 v79, 1, v79\n" \
"v_and_b32 v81, 3, v78\n" \
"v_xor_b32 v79, v79, v81\n" \
"v_lshl_add_u32 v80, v79, 4, v80\n" \
"v_lshrrev_b32 v90, 5, v78\n" \
"v_lshl_add_u32 v84, v90, 6, v80\n" \
"v_add_u32 v68, 4096, v84\n" \
"v_add_u32 v69, 3072, v84\n" \
"v_add_u32 v70, 2176, v84\n" \
"v_add_u32 v71, 1152, v84\n" \
"s_lshl_b32 s83, s62, 8\n" \
"s_add_u32 s84, s83, 32768\n" \
"v_lshrrev_b32 v85, 3, v78\n" \
"v_and_b32 v86, 7, v78\n" \
"v_xor_b32 v86, v86, v85\n" \
"v_lshlrev_b32 v86, 4, v86\n" \
"v_lshl_add_u32 v64, v85, 10, v86\n" \
"v_add_u32 v65, 7168, v64\n" \
"v_add_u32 v66, 14336, v64\n" \
"v_add_u32 v67, 21504, v64\n" \
"s_bitcmp1_b32 s61, 0\n" \
"s_cselect_b32 s70, 0, 15\n" \
"s_xor_b32 s81, s63, s70\n" \
"s_lshl_b32 s81, s81, 17\n" \
"s_lshr_b32 s70, s61, 1\n" \
"s_lshl_b32 s71, s70, 16\n" \
"s_add_u32 s81, s81, s71\n" \
"s_add_u32 s82, s81, 32768\n" \
"s_mov_b32 s42, 4198400\n" \
"s_mov_b32 s43, 0x20000\n" \
"s_mov_b32 s46, 4198400\n" \
"s_mov_b32 s47, 0x20000\n" \
"s_mov_b32 s38, 2097152\n" \
"s_mov_b32 s39, 0x20000\n" \
"s_waitcnt lgkmcnt(0)\n" \
"s_mov_b64 s[40:41], s[14:15]\n" \
"s_sub_u32 s40, s40, 4096\n" \
"s_subb_u32 s41, s41, 0\n" \
"s_and_b32 s41, s41, 0xffff\n" \
"s_mov_b64 s[44:45], s[16:17]\n" \
"s_sub_u32 s44, s44, 4096\n" \
"s_subb_u32 s45, s45, 0\n" \
"s_and_b32 s45, s45, 0xffff\n" \
"s_mov_b64 s[36:37], s[10:11]\n" \
"s_and_b32 s37, s37, 0xffff\n" \
"s_add_u32 m0, s74, 32768\n" \
"s_nop 0\n" \
"buffer_load_dwordx4 v68, s[40:43], s83 offen offset:0 lds\n" \
"buffer_load_dwordx4 v69, s[44:47], s83 offen offset:1024 lds\n" \
"buffer_load_dwordx4 v70, s[40:43], s83 offen offset:2048 lds\n" \
"buffer_load_dwordx4 v71, s[44:47], s83 offen offset:3072 lds\n" \
"s_xor_b32 s83, s83, 0x80000\n" \
"s_add_u32 m0, s74, 36864\n" \
"s_nop 0\n" \
"buffer_load_dwordx4 v68, s[40:43], s84 offen offset:0 lds\n" \
"buffer_load_dwordx4 v69, s[44:47], s84 offen offset:1024 lds\n" \
"buffer_load_dwordx4 v70, s[40:43], s84 offen offset:2048 lds\n" \
"buffer_load_dwordx4 v71, s[44:47], s84 offen offset:3072 lds\n" \
"s_xor_b32 s84, s84, 0x80000\n" \
"s_add_u32 m0, s74, 0\n" \
"s_nop 0\n" \
"buffer_load_dwordx4 v64, s[36:39], s81 offen offset:0 lds\n" \
"buffer_load_dwordx4 v65, s[36:39], s81 offen offset:1024 lds\n" \
"buffer_load_dwordx4 v66, s[36:39], s81 offen offset:2048 lds\n" \
"buffer_load_dwordx4 v67, s[36:39], s81 offen offset:3072 lds\n" \
"s_xor_b32 s81, s81, 0x80\n" \
"s_add_u32 m0, s74, 4096\n" \
"s_nop 0\n" \
"buffer_load_dwordx4 v64, s[36:39], s82 offen offset:0 lds\n" \
"buffer_load_dwordx4 v65, s[36:39], s82 offen offset:1024 lds\n" \
"buffer_load_dwordx4 v66, s[36:39], s82 offen offset:2048 lds\n" \
"buffer_load_dwordx4 v67, s[36:39], s82 offen offset:3072 lds\n" \
"s_xor_b32 s82, s82, 0x80\n" \
"v_lshrrev_b32 v87, 4, v78\n" \
"v_and_b32 v79, 15, v78\n" \
"v_and_b32 v80, 7, v78\n" \
"v_xor_b32 v80, v80, v87\n" \
"v_lshlrev_b32 v79, 7, v79\n" \
"s_lshl_b32 s70, s70, 14\n" \
"s_add_u32 s70, s70, %3\n" \
"v_lshl_add_u32 v88, v80, 4, v79\n" \
"v_add_u32 v72, s70, v88\n" \
"v_xor_b32 v73, 64, v72\n" \
"v_lshlrev_b32 v79, 3, v81\n" \
"v_bfe_u32 v80, v78, 2, 2\n" \
"v_lshl_add_u32 v79, v80, 6, v79\n" \
"v_bfe_u32 v80, v78, 4, 1\n" \
"v_lshl_add_u32 v79, v80, 8, v79\n" \
"v_lshl_add_u32 v89, v80, 5, v79\n" \
"s_and_b32 s70, s61, 1\n" \
"s_lshl_b32 s70, s70, 11\n" \
"s_add_u32 s70, s70, %3\n" \
"v_lshl_add_u32 v82, v90, 13, v89\n" \
"v_add_u32 v74, s70, v82\n" \
"v_xor_b32 v75, 32, v74\n" \
"s_mov_b64 s[56:57], s[28:29]\n" \
"s_and_b32 s57, s57, 0xffff\n" \
"s_mov_b32 s58, 65634304\n" \
"s_mov_b32 s59, 0x20000\n" \
"v_mov_b32 v241, 0\n" \
"s_lshl_b32 s66, %1, 2\n" \
"s_add_u32 s66, s66, s61\n" \
"s_lshl_b32 s66, s66, 10\n" \
"s_lshr_b32 s67, %1, 3\n" \
"s_lshl_b32 s67, s67, 2\n" \
"s_add_u32 s67, s67, s61\n" \
"s_lshl_b32 s67, s67, 13\n" \
"s_and_b32 s64, %1, 7\n" \
"s_lshl_b32 s64, s64, 10\n" \
"s_add_u32 s67, s67, s64\n" \
"s_mov_b64 exec, 0\n" \
"s_cmp_eq_u32 s61, 0\n" \
"s_cbranch_scc1 Lst0x0_%=\n" \
"s_cmp_eq_u32 s61, 1\n" \
"s_cbranch_scc1 Lst0x1_%=\n" \
"s_cmp_eq_u32 s61, 2\n" \
"s_cbranch_scc1 Lst0x2_%=\n" \
"s_branch Lst0x3_%=\n" \
"Lcb0_%=:\n" \
"s_mov_b64 exec, -1\n" \
"s_add_u32 m0, s74, 131072\n" \
"s_nop 0\n" \
"buffer_load_dwordx4 v68, s[40:43], s83 offen offset:0 lds\n" \
"buffer_load_dwordx4 v69, s[44:47], s83 offen offset:1024 lds\n" \
"buffer_load_dwordx4 v70, s[40:43], s83 offen offset:2048 lds\n" \
"buffer_load_dwordx4 v71, s[44:47], s83 offen offset:3072 lds\n" \
"s_xor_b32 s83, s83, 0x180000\n" \
"s_add_u32 m0, s74, 135168\n" \
"s_nop 0\n" \
"buffer_load_dwordx4 v68, s[40:43], s84 offen offset:0 lds\n" \
"buffer_load_dwordx4 v69, s[44:47], s84 offen offset:1024 lds\n" \
"buffer_load_dwordx4 v70, s[40:43], s84 offen offset:2048 lds\n" \
"buffer_load_dwordx4 v71, s[44:47], s84 offen offset:3072 lds\n" \
"s_xor_b32 s84, s84, 0x180000\n" \
"s_mov_b32 s93, 65593344\n" \
"buffer_load_dword v240, v241, s[56:59], s93 offen sc1\n" \
"s_mov_b64 s[64:65], s[10:11]\n" \
"s_add_u32 s68, s66, 0x0\n" \
"s_add_u32 s64, s64, s68\n" \
"s_addc_u32 s65, s65, 0\n" \
"global_load_dword v238, v234, s[64:65]\n" \
"s_mov_b64 s[64:65], s[10:11]\n" \
"s_add_u32 s68, s66, 0x100000\n" \
"s_add_u32 s64, s64, s68\n" \
"s_addc_u32 s65, s65, 0\n" \
"global_load_dword v238, v234, s[64:65]\n" \
"s_mov_b64 s[64:65], s[14:15]\n" \
"s_add_u32 s68, s67, 0x100000\n" \
"s_add_u32 s64, s64, s68\n" \
"s_addc_u32 s65, s65, 0\n" \
"global_load_dword v238, v234, s[64:65]\n" \
"s_mov_b64 s[64:65], s[16:17]\n" \
"s_add_u32 s68, s67, 0x100000\n" \
"s_add_u32 s64, s64, s68\n" \
"s_addc_u32 s65, s65, 0\n" \
"global_load_dword v238, v234, s[64:65]\n" \
"s_waitcnt vmcnt(13)\n" \
"s_barrier\n" \
"ds_read_b64_tr_b16 a[64:65], v74 offset:32768\n" \
"ds_read_b64_tr_b16 a[66:67], v74 offset:36864\n" \
"ds_read_b64_tr_b16 a[68:69], v74 offset:49152\n" \
"ds_read_b64_tr_b16 a[70:71], v74 offset:53248\n" \
"ds_read_b128 a[0:3], v72 offset:0\n" \
"ds_read_b128 a[4:7], v73 offset:0\n" \
"ds_read_b64_tr_b16 a[72:73], v75 offset:32768\n" \
"ds_read_b64_tr_b16 a[74:75], v75 offset:36864\n" \
"ds_read_b64_tr_b16 a[76:77], v75 offset:49152\n" \
"ds_read_b64_tr_b16 a[78:79], v75 offset:53248\n" \
"ds_read_b64_tr_b16 a[80:81], v74 offset:33280\n" \
"ds_read_b64_tr_b16 a[82:83], v74 offset:37376\n" \
"ds_read_b64_tr_b16 a[84:85], v74 offset:49664\n" \
"ds_read_b64_tr_b16 a[86:87], v74 offset:53760\n" \
"ds_read_b64_tr_b16 a[88:89], v75 offset:33280\n" \
"ds_read_b64_tr_b16 a[90:91], v75 offset:37376\n" \
"ds_read_b64_tr_b16 a[92:93], v75 offset:49664\n" \
"ds_read_b64_tr_b16 a[94:95], v75 offset:53760\n" \
"ds_read_b128 a[8:11], v72 offset:2048\n" \
"ds_read_b128 a[12:15], v73 offset:2048\n" \
"s_waitcnt lgkmcnt(14)\n" \
"v_mfma_f32_16x16x32_bf16 v[0:3], a[64:67], a[0:3], 0\n" \
"s_add_u32 m0, s74, 65536\n" \
"ds_read_b128 a[16:19], v72 offset:4096\n" \
"v_mfma_f32_16x16x32_bf16 v[0:3], a[68:71], a[4:7], v[0:3]\n" \
"s_waitcnt lgkmcnt(11)\n" \
"buffer_load_dwordx4 v64, s[36:39], s81 offen offset:0 lds\n" \
"v_mfma_f32_16x16x32_bf16 v[4:7], a[72:75], a[0:3], 0\n" \
"ds_read_b128 a[20:23], v73 offset:4096\n" \
"v_mfma_f32_16x16x32_bf16 v[4:7], a[76:79], a[4:7], v[4:7]\n" \
"s_waitcnt lgkmcnt(8)\n" \
"v_mfma_f32_16x16x32_bf16 v[8:11], a[80:83], a[0:3], 0\n" \
"s_waitcnt lgkmcnt(2)\n" \
"ds_read_b128 a[24:27], v72 offset:6144\n" \
"v_mfma_f32_16x16x32_bf16 v[8:11], a[84:87], a[4:7], v[8:11]\n" \
"buffer_load_dwordx4 v65, s[36:39], s81 offen offset:1024 lds\n" \
"v_mfma_f32_16x16x32_bf16 v[12:15], a[88:91], a[0:3], 0\n" \
"ds_read_b128 a[28:31], v73 offset:6144\n" \
"v_mfma_f32_16x16x32_bf16 v[12:15], a[92:95], a[4:7], v[12:15]\n" \
"v_mfma_f32_16x16x32_bf16 v[28:31], a[88:91], a[8:11], 0\n" \
"ds_read_b128 a[32:35], v72 offset:8192\n" \
"v_mfma_f32_16x16x32_bf16 v[28:31], a[92:95], a[12:15], v[28:31]\n" \
"buffer_load_dwordx4 v66, s[36:39], s81 offen offset:2048 lds\n" \
"v_mfma_f32_16x16x32_bf16 v[24:27], a[80:83], a[8:11], 0\n" \
"ds_read_b128 a[36:39], v73 offset:8192\n" \
"v_mfma_f32_16x16x32_bf16 v[24:27], a[84:87], a[12:15], v[24:27]\n" \
"v_mfma_f32_16x16x32_bf16 v[20:23], a[72:75], a[8:11], 0\n" \
"ds_read_b128 a[40:43], v72 offset:10240\n" \
"v_mfma_f32_16x16x32_bf16 v[20:23], a[76:79], a[12:15], v[20:23]\n" \
"buffer_load_dwordx4 v67, s[36:39], s81 offen offset:3072 lds\n" \
"v_mfma_f32_16x16x32_bf16 v[16:19], a[64:67], a[8:11], 0\n" \
"ds_read_b128 a[44:47], v73 offset:10240\n" \
"v_mfma_f32_16x16x32_bf16 v[16:19], a[68:71], a[12:15], v[16:19]\n" \
"s_waitcnt lgkmcnt(2)\n" \
"s_xor_b32 s81, s81, 0x180\n" \
"v_mfma_f32_16x16x32_bf16 v[32:35], a[64:67], a[16:19], 0\n" \
"s_add_u32 m0, s74, 69632\n" \
"ds_read_b128 a[48:51], v72 offset:12288\n" \
"v_mfma_f32_16x16x32_bf16 v[32:35], a[68:71], a[20:23], v[32:35]\n" \
"buffer_load_dwordx4 v64, s[36:39], s82 offen offset:0 lds\n" \
"v_mfma_f32_16x16x32_bf16 v[36:39], a[72:75], a[16:19], 0\n" \
"ds_read_b128 a[52:55], v73 offset:12288\n" \
"v_mfma_f32_16x16x32_bf16 v[36:39], a[76:79], a[20:23], v[36:39]\n" \
"v_mfma_f32_16x16x32_bf16 v[40:43], a[80:83], a[16:19], 0\n" \
"ds_read_b128 a[56:59], v72 offset:14336\n" \
"v_mfma_f32_16x16x32_bf16 v[40:43], a[84:87], a[20:23], v[40:43]\n" \
"buffer_load_dwordx4 v65, s[36:39], s82 offen offset:1024 lds\n" \
"v_mfma_f32_16x16x32_bf16 v[44:47], a[88:91], a[16:19], 0\n" \
"ds_read_b128 a[60:63], v73 offset:14336\n" \
"v_mfma_f32_16x16x32_bf16 v[44:47], a[92:95], a[20:23], v[44:47]\n" \
"v_mfma_f32_16x16x32_bf16 v[60:63], a[88:91], a[24:27], 0\n" \
"ds_read_b64_tr_b16 a[96:97], v74 offset:33792\n" \
"v_mfma_f32_16x16x32_bf16 v[60:63], a[92:95], a[28:31], v[60:63]\n" \
"buffer_load_dwordx4 v66, s[36:39], s82 offen offset:2048 lds\n" \
"v_mfma_f32_16x16x32_bf16 v[56:59], a[80:83], a[24:27], 0\n" \
"ds_read_b64_tr_b16 a[98:99], v74 offset:37888\n" \
"v_mfma_f32_16x16x32_bf16 v[56:59], a[84:87], a[28:31], v[56:59]\n" \
"v_mfma_f32_16x16x32_bf16 v[52:55], a[72:75], a[24:27], 0\n" \
"ds_read_b64_tr_b16 a[100:101], v74 offset:50176\n" \
"v_mfma_f32_16x16x32_bf16 v[52:55], a[76:79], a[28:31], v[52:55]\n" \
"buffer_load_dwordx4 v67, s[36:39], s82 offen offset:3072 lds\n" \
"v_mfma_f32_16x16x32_bf16 v[48:51], a[64:67], a[24:27], 0\n" \
"ds_read_b64_tr_b16 a[102:103], v74 offset:54272\n" \
"v_mfma_f32_16x16x32_bf16 v[48:51], a[68:71], a[28:31], v[48:51]\n" \
"v_mfma_f32_16x16x32_bf16 v[144:147], a[64:67], a[32:35], 0\n" \
"s_xor_b32 s82, s82, 0x180\n" \
"ds_read_b64_tr_b16 a[104:105], v75 offset:33792\n" \
"v_mfma_f32_16x16x32_bf16 v[144:147], a[68:71], a[36:39], v[144:147]\n" \
"v_mfma_f32_16x16x32_bf16 v[148:151], a[72:75], a[32:35], 0\n" \
"ds_read_b64_tr_b16 a[106:107], v75 offset:37888\n" \
"v_mfma_f32_16x16x32_bf16 v[148:151], a[76:79], a[36:39], v[148:151]\n" \
"v_mfma_f32_16x16x32_bf16 v[152:155], a[80:83], a[32:35], 0\n" \
"ds_read_b64_tr_b16 a[108:109], v75 offset:50176\n" \
"v_mfma_f32_16x16x32_bf16 v[152:155], a[84:87], a[36:39], v[152:155]\n" \
"v_mfma_f32_16x16x32_bf16 v[156:159], a[88:91], a[32:35], 0\n" \
"ds_read_b64_tr_b16 a[110:111], v75 offset:54272\n" \
"v_mfma_f32_16x16x32_bf16 v[156:159], a[92:95], a[36:39], v[156:159]\n" \
"s_waitcnt lgkmcnt(2)\n" \
"v_mfma_f32_16x16x32_bf16 v[188:191], a[88:91], a[40:43], 0\n" \
"ds_read_b64_tr_b16 a[112:113], v74 offset:34304\n" \
"v_add_u32 v96, 0x10000, v72\n" \
"v_mfma_f32_16x16x32_bf16 v[188:191], a[92:95], a[44:47], v[188:191]\n" \
"v_mfma_f32_16x16x32_bf16 v[184:187], a[80:83], a[40:43], 0\n" \
"ds_read_b64_tr_b16 a[114:115], v74 offset:38400\n" \
"v_add_u32 v97, 0x10000, v73\n" \
"v_mfma_f32_16x16x32_bf16 v[184:187], a[84:87], a[44:47], v[184:187]\n" \
"v_mfma_f32_16x16x32_bf16 v[180:183], a[72:75], a[40:43], 0\n" \
"ds_read_b64_tr_b16 a[116:117], v74 offset:50688\n" \
"v_add_u32 v98, 0x10000, v74\n" \
"v_mfma_f32_16x16x32_bf16 v[180:183], a[76:79], a[44:47], v[180:183]\n" \
"v_mfma_f32_16x16x32_bf16 v[176:179], a[64:67], a[40:43], 0\n" \
"ds_read_b64_tr_b16 a[118:119], v74 offset:54784\n" \
"v_add_u32 v99, 0x10000, v75\n" \
"v_mfma_f32_16x16x32_bf16 v[176:179], a[68:71], a[44:47], v[176:179]\n" \
"v_mfma_f32_16x16x32_bf16 v[192:195], a[64:67], a[48:51], 0\n" \
"s_add_u32 m0, s74, 98304\n" \
"ds_read_b64_tr_b16 a[120:121], v75 offset:34304\n" \
"v_add_u32 v100, 0x20000, v72\n" \
"v_mfma_f32_16x16x32_bf16 v[192:195], a[68:71], a[52:55], v[192:195]\n" \
"buffer_load_dwordx4 v68, s[40:43], s83 offen offset:0 lds\n" \
"v_mfma_f32_16x16x32_bf16 v[196:199], a[72:75], a[48:51], 0\n" \
"ds_read_b64_tr_b16 a[122:123], v75 offset:38400\n" \
"v_add_u32 v101, 0x20000, v73\n" \
"v_mfma_f32_16x16x32_bf16 v[196:199], a[76:79], a[52:55], v[196:199]\n" \
"v_mfma_f32_16x16x32_bf16 v[200:203], a[80:83], a[48:51], 0\n" \
"ds_read_b64_tr_b16 a[124:125], v75 offset:50688\n" \
"v_add_u32 v102, 0x20000, v74\n" \
"v_mfma_f32_16x16x32_bf16 v[200:203], a[84:87], a[52:55], v[200:203]\n" \
"buffer_load_dwordx4 v69, s[44:47], s83 offen offset:1024 lds\n" \
"v_mfma_f32_16x16x32_bf16 v[204:207], a[88:91], a[48:51], 0\n" \
"ds_read_b64_tr_b16 a[126:127], v75 offset:54784\n" \
"v_add_u32 v103, 0x20000, v75\n" \
"v_mfma_f32_16x16x32_bf16 v[204:207], a[92:95], a[52:55], v[204:207]\n" \
"v_mfma_f32_16x16x32_bf16 v[220:223], a[88:91], a[56:59], 0\n" \
"v_mfma_f32_16x16x32_bf16 v[220:223], a[92:95], a[60:63], v[220:223]\n" \
"buffer_load_dwordx4 v70, s[40:43], s83 offen offset:2048 lds\n" \
"v_mfma_f32_16x16x32_bf16 v[216:219], a[80:83], a[56:59], 0\n" \
"v_mfma_f32_16x16x32_bf16 v[216:219], a[84:87], a[60:63], v[216:219]\n" \
"v_mfma_f32_16x16x32_bf16 v[212:215], a[72:75], a[56:59], 0\n" \
"v_mfma_f32_16x16x32_bf16 v[212:215], a[76:79], a[60:63], v[212:215]\n" \
"buffer_load_dwordx4 v71, s[44:47], s83 offen offset:3072 lds\n" \
"v_mfma_f32_16x16x32_bf16 v[208:211], a[64:67], a[56:59], 0\n" \
"v_mfma_f32_16x16x32_bf16 v[208:211], a[68:71], a[60:63], v[208:211]\n" \
"s_xor_b32 s83, s83, 0x80000\n" \
"s_waitcnt lgkmcnt(0)\n" \
"s_barrier\n" \
"v_mfma_f32_16x16x32_bf16 a[128:131], a[0:3], a[96:99], 0\n" \
"s_add_u32 m0, s74, 102400\n" \
"v_mfma_f32_16x16x32_bf16 a[128:131], a[4:7], a[100:103], a[128:131]\n" \
"buffer_load_dwordx4 v68, s[40:43], s84 offen offset:0 lds\n" \
"v_mfma_f32_16x16x32_bf16 a[132:135], a[0:3], a[104:107], 0\n" \
"v_mfma_f32_16x16x32_bf16 a[132:135], a[4:7], a[108:111], a[132:135]\n" \
"v_mfma_f32_16x16x32_bf16 a[136:139], a[0:3], a[112:115], 0\n" \
"v_mfma_f32_16x16x32_bf16 a[136:139], a[4:7], a[116:119], a[136:139]\n" \
"buffer_load_dwordx4 v69, s[44:47], s84 offen offset:1024 lds\n" \
"v_mfma_f32_16x16x32_bf16 a[140:143], a[0:3], a[120:123], 0\n" \
"v_mfma_f32_16x16x32_bf16 a[140:143], a[4:7], a[124:127], a[140:143]\n" \
"v_mfma_f32_16x16x32_bf16 a[156:159], a[8:11], a[120:123], 0\n" \
"v_mfma_f32_16x16x32_bf16 a[156:159], a[12:15], a[124:127], a[156:159]\n" \
"buffer_load_dwordx4 v70, s[40:43], s84 offen offset:2048 lds\n" \
"v_mfma_f32_16x16x32_bf16 a[152:155], a[8:11], a[112:115], 0\n" \
"v_mfma_f32_16x16x32_bf16 a[152:155], a[12:15], a[116:119], a[152:155]\n" \
"v_mfma_f32_16x16x32_bf16 a[148:151], a[8:11], a[104:107], 0\n" \
"v_mfma_f32_16x16x32_bf16 a[148:151], a[12:15], a[108:111], a[148:151]\n" \
"buffer_load_dwordx4 v71, s[44:47], s84 offen offset:3072 lds\n" \
"v_mfma_f32_16x16x32_bf16 a[144:147], a[8:11], a[96:99], 0\n" \
"v_mfma_f32_16x16x32_bf16 a[144:147], a[12:15], a[100:103], a[144:147]\n" \
"v_mfma_f32_16x16x32_bf16 a[160:163], a[16:19], a[96:99], 0\n" \
"s_xor_b32 s84, s84, 0x80000\n" \
"v_mfma_f32_16x16x32_bf16 a[160:163], a[20:23], a[100:103], a[160:163]\n" \
"v_mfma_f32_16x16x32_bf16 a[164:167], a[16:19], a[104:107], 0\n" \
"v_mfma_f32_16x16x32_bf16 a[164:167], a[20:23], a[108:111], a[164:167]\n" \
"v_mfma_f32_16x16x32_bf16 a[168:171], a[16:19], a[112:115], 0\n" \
"s_add_u32 m0, s74, 32768\n" \
"v_mfma_f32_16x16x32_bf16 a[168:171], a[20:23], a[116:119], a[168:171]\n" \
"buffer_load_dwordx4 v64, s[36:39], s81 offen offset:0 lds\n" \
"v_mfma_f32_16x16x32_bf16 a[172:175], a[16:19], a[120:123], 0\n" \
"v_mfma_f32_16x16x32_bf16 a[172:175], a[20:23], a[124:127], a[172:175]\n" \
"v_mfma_f32_16x16x32_bf16 a[188:191], a[24:27], a[120:123], 0\n" \
"v_mfma_f32_16x16x32_bf16 a[188:191], a[28:31], a[124:127], a[188:191]\n" \
"buffer_load_dwordx4 v65, s[36:39], s81 offen offset:1024 lds\n" \
"v_mfma_f32_16x16x32_bf16 a[184:187], a[24:27], a[112:115], 0\n" \
"v_mfma_f32_16x16x32_bf16 a[184:187], a[28:31], a[116:119], a[184:187]\n" \
"v_mfma_f32_16x16x32_bf16 a[180:183], a[24:27], a[104:107], 0\n" \
"v_mfma_f32_16x16x32_bf16 a[180:183], a[28:31], a[108:111], a[180:183]\n" \
"buffer_load_dwordx4 v66, s[36:39], s81 offen offset:2048 lds\n" \
"v_mfma_f32_16x16x32_bf16 a[176:179], a[24:27], a[96:99], 0\n" \
"v_mfma_f32_16x16x32_bf16 a[176:179], a[28:31], a[100:103], a[176:179]\n" \
"v_mfma_f32_16x16x32_bf16 a[192:195], a[32:35], a[96:99], 0\n" \
"v_mfma_f32_16x16x32_bf16 a[192:195], a[36:39], a[100:103], a[192:195]\n" \
"buffer_load_dwordx4 v67, s[36:39], s81 offen offset:3072 lds\n" \
"v_mfma_f32_16x16x32_bf16 a[196:199], a[32:35], a[104:107], 0\n" \
"v_mfma_f32_16x16x32_bf16 a[196:199], a[36:39], a[108:111], a[196:199]\n" \
"s_xor_b32 s81, s81, 0x80\n" \
"v_mfma_f32_16x16x32_bf16 a[200:203], a[32:35], a[112:115], 0\n" \
"s_add_u32 m0, s74, 36864\n" \
"v_mfma_f32_16x16x32_bf16 a[200:203], a[36:39], a[116:119], a[200:203]\n" \
"buffer_load_dwordx4 v64, s[36:39], s82 offen offset:0 lds\n" \
"v_mfma_f32_16x16x32_bf16 a[204:207], a[32:35], a[120:123], 0\n" \
"v_mfma_f32_16x16x32_bf16 a[204:207], a[36:39], a[124:127], a[204:207]\n" \
"s_waitcnt vmcnt(13) lgkmcnt(0)\n" \
"s_barrier\n" \
"v_mfma_f32_16x16x32_bf16 a[220:223], a[40:43], a[120:123], 0\n" \
"ds_read_b64_tr_b16 a[64:65], v102 offset:0\n" \
"ds_read_b64_tr_b16 a[66:67], v102 offset:4096\n" \
"v_mfma_f32_16x16x32_bf16 a[220:223], a[44:47], a[124:127], a[220:223]\n" \
"buffer_load_dwordx4 v65, s[36:39], s82 offen offset:1024 lds\n" \
"v_mfma_f32_16x16x32_bf16 a[216:219], a[40:43], a[112:115], 0\n" \
"ds_read_b64_tr_b16 a[68:69], v102 offset:16384\n" \
"ds_read_b64_tr_b16 a[70:71], v102 offset:20480\n" \
"v_mfma_f32_16x16x32_bf16 a[216:219], a[44:47], a[116:119], a[216:219]\n" \
"ds_read_b128 a[0:3], v96 offset:0\n" \
"ds_read_b128 a[4:7], v97 offset:0\n" \
"v_mfma_f32_16x16x32_bf16 a[212:215], a[40:43], a[104:107], 0\n" \
"ds_read_b64_tr_b16 a[72:73], v103 offset:0\n" \
"ds_read_b64_tr_b16 a[74:75], v103 offset:4096\n" \
"v_mfma_f32_16x16x32_bf16 a[212:215], a[44:47], a[108:111], a[212:215]\n" \
"buffer_load_dwordx4 v66, s[36:39], s82 offen offset:2048 lds\n" \
"v_mfma_f32_16x16x32_bf16 a[208:211], a[40:43], a[96:99], 0\n" \
"ds_read_b64_tr_b16 a[76:77], v103 offset:16384\n" \
"ds_read_b64_tr_b16 a[78:79], v103 offset:20480\n" \
"v_mfma_f32_16x16x32_bf16 a[208:211], a[44:47], a[100:103], a[208:211]\n" \
"ds_read_b64_tr_b16 a[80:81], v102 offset:512\n" \
"ds_read_b64_tr_b16 a[82:83], v102 offset:4608\n" \
"v_mfma_f32_16x16x32_bf16 a[224:227], a[48:51], a[96:99], 0\n" \
"ds_read_b64_tr_b16 a[84:85], v102 offset:16896\n" \
"ds_read_b64_tr_b16 a[86:87], v102 offset:20992\n" \
"v_mfma_f32_16x16x32_bf16 a[224:227], a[52:55], a[100:103], a[224:227]\n" \
"buffer_load_dwordx4 v67, s[36:39], s82 offen offset:3072 lds\n" \
"v_mfma_f32_16x16x32_bf16 a[228:231], a[48:51], a[104:107], 0\n" \
"s_mov_b64 s[64:65], s[14:15]\n" \
"s_add_u32 s68, s67, 0x200000\n" \
"ds_read_b64_tr_b16 a[88:89], v103 offset:512\n" \
"ds_read_b64_tr_b16 a[90:91], v103 offset:4608\n" \
"v_mfma_f32_16x16x32_bf16 a[228:231], a[52:55], a[108:111], a[228:231]\n" \
"s_add_u32 s64, s64, s68\n" \
"s_addc_u32 s65, s65, 0\n" \
"ds_read_b64_tr_b16 a[92:93], v103 offset:16896\n" \
"ds_read_b64_tr_b16 a[94:95], v103 offset:20992\n" \
"v_mfma_f32_16x16x32_bf16 a[232:235], a[48:51], a[112:115], 0\n" \
"s_xor_b32 s82, s82, 0x80\n" \
"ds_read_b128 a[8:11], v96 offset:2048\n" \
"ds_read_b128 a[12:15], v97 offset:2048\n" \
"v_mfma_f32_16x16x32_bf16 a[232:235], a[52:55], a[116:119], a[232:235]\n" \
"global_load_dword v238, v234, s[64:65]\n" \
"v_mfma_f32_16x16x32_bf16 a[236:239], a[48:51], a[120:123], 0\n" \
"s_mov_b64 s[64:65], s[16:17]\n" \
"s_add_u32 s68, s67, 0x200000\n" \
"ds_read_b128 a[16:19], v96 offset:4096\n" \
"ds_read_b128 a[20:23], v97 offset:4096\n" \
"v_mfma_f32_16x16x32_bf16 a[236:239], a[52:55], a[124:127], a[236:239]\n" \
"s_add_u32 s64, s64, s68\n" \
"s_addc_u32 s65, s65, 0\n" \
"ds_read_b128 a[24:27], v96 offset:6144\n" \
"ds_read_b128 a[28:31], v97 offset:6144\n" \
"v_mfma_f32_16x16x32_bf16 a[252:255], a[56:59], a[120:123], 0\n" \
"ds_read_b128 a[32:35], v96 offset:8192\n" \
"ds_read_b128 a[36:39], v97 offset:8192\n" \
"v_mfma_f32_16x16x32_bf16 a[252:255], a[60:63], a[124:127], a[252:255]\n" \
"global_load_dword v238, v234, s[64:65]\n" \
"v_mfma_f32_16x16x32_bf16 a[248:251], a[56:59], a[112:115], 0\n" \
"ds_read_b128 a[40:43], v96 offset:10240\n" \
"ds_read_b128 a[44:47], v97 offset:10240\n" \
"v_mfma_f32_16x16x32_bf16 a[248:251], a[60:63], a[116:119], a[248:251]\n" \
"ds_read_b128 a[48:51], v96 offset:12288\n" \
"ds_read_b128 a[52:55], v97 offset:12288\n" \
"v_mfma_f32_16x16x32_bf16 a[244:247], a[56:59], a[104:107], 0\n" \
"v_mfma_f32_16x16x32_bf16 a[244:247], a[60:63], a[108:111], a[244:247]\n" \
"v_mfma_f32_16x16x32_bf16 a[240:243], a[56:59], a[96:99], 0\n" \
"v_mfma_f32_16x16x32_bf16 a[240:243], a[60:63], a[100:103], a[240:243]\n" \
"s_waitcnt lgkmcnt(2)\n" \
"v_mfma_f32_16x16x32_bf16 v[0:3], a[64:67], a[0:3], v[0:3]\n" \
"s_lshl_b32 s79, s61, 12\n" \
"ds_read_b128 a[56:59], v96 offset:14336\n" \
"v_mfma_f32_16x16x32_bf16 v[0:3], a[68:71], a[4:7], v[0:3]\n" \
"s_add_u32 s79, s79, %3\n" \
"v_add_u32 v228, 4096, v84\n" \
"v_add_u32 v229, 3200, v84\n" \
"v_mfma_f32_16x16x32_bf16 v[4:7], a[72:75], a[0:3], v[4:7]\n" \
"s_add_u32 m0, s74, 0\n" \
"ds_read_b128 a[60:63], v97 offset:14336\n" \
"v_mfma_f32_16x16x32_bf16 v[4:7], a[76:79], a[4:7], v[4:7]\n" \
"buffer_load_dwordx4 v68, s[40:43], s83 offen offset:0 lds\n" \
"v_mfma_f32_16x16x32_bf16 v[8:11], a[80:83], a[0:3], v[8:11]\n" \
"s_lshl_b32 s78, s62, 8\n" \
"ds_read_b64_tr_b16 a[96:97], v102 offset:1024\n" \
"v_mfma_f32_16x16x32_bf16 v[8:11], a[84:87], a[4:7], v[8:11]\n" \
"s_add_u32 s89, s78, 30720\n" \
"v_mfma_f32_16x16x32_bf16 v[12:15], a[88:91], a[0:3], v[12:15]\n" \
"s_mov_b64 s[48:49], s[8:9]\n" \
"ds_read_b64_tr_b16 a[98:99], v102 offset:5120\n" \
"v_mfma_f32_16x16x32_bf16 v[12:15], a[92:95], a[4:7], v[12:15]\n" \
"buffer_load_dwordx4 v69, s[44:47], s83 offen offset:1024 lds\n" \
"v_mfma_f32_16x16x32_bf16 v[28:31], a[88:91], a[8:11], v[28:31]\n" \
"s_and_b32 s49, s49, 0xffff\n" \
"ds_read_b64_tr_b16 a[100:101], v102 offset:17408\n" \
"v_mfma_f32_16x16x32_bf16 v[28:31], a[92:95], a[12:15], v[28:31]\n" \
"s_mov_b32 s50, 4194304\n" \
"v_mfma_f32_16x16x32_bf16 v[24:27], a[80:83], a[8:11], v[24:27]\n" \
"s_mov_b32 s51, 0x20000\n" \
"ds_read_b64_tr_b16 a[102:103], v102 offset:21504\n" \
"v_mfma_f32_16x16x32_bf16 v[24:27], a[84:87], a[12:15], v[24:27]\n" \
"buffer_load_dwordx4 v70, s[40:43], s83 offen offset:2048 lds\n" \
"v_mfma_f32_16x16x32_bf16 v[20:23], a[72:75], a[8:11], v[20:23]\n" \
"s_mov_b64 s[52:53], s[12:13]\n" \
"ds_read_b64_tr_b16 a[104:105], v103 offset:1024\n" \
"v_mfma_f32_16x16x32_bf16 v[20:23], a[76:79], a[12:15], v[20:23]\n" \
"s_sub_u32 s52, s52, 4096\n" \
"s_subb_u32 s53, s53, 0\n" \
"v_mfma_f32_16x16x32_bf16 v[16:19], a[64:67], a[8:11], v[16:19]\n" \
"s_and_b32 s53, s53, 0xffff\n" \
"ds_read_b64_tr_b16 a[106:107], v103 offset:5120\n" \
"v_mfma_f32_16x16x32_bf16 v[16:19], a[68:71], a[12:15], v[16:19]\n" \
"buffer_load_dwordx4 v71, s[44:47], s83 offen offset:3072 lds\n" \
"v_mfma_f32_16x16x32_bf16 v[32:35], a[64:67], a[16:19], v[32:35]\n" \
"s_mov_b32 s54, 8392704\n" \
"s_mov_b32 s55, 0x20000\n" \
"ds_read_b64_tr_b16 a[108:109], v103 offset:17408\n" \
"v_mfma_f32_16x16x32_bf16 v[32:35], a[68:71], a[20:23], v[32:35]\n" \
"s_xor_b32 s83, s83, 0x380000\n" \
"v_lshl_add_u32 v224, v85, 11, v86\n" \
"v_add_u32 v225, 15360, v224\n" \
"v_mfma_f32_16x16x32_bf16 v[36:39], a[72:75], a[16:19], v[36:39]\n" \
"s_add_u32 m0, s74, 4096\n" \
"ds_read_b64_tr_b16 a[110:111], v103 offset:21504\n" \
"v_add_u32 v226, 129024, v224\n" \
"v_mfma_f32_16x16x32_bf16 v[36:39], a[76:79], a[20:23], v[36:39]\n" \
"buffer_load_dwordx4 v68, s[40:43], s84 offen offset:0 lds\n" \
"v_mfma_f32_16x16x32_bf16 v[40:43], a[80:83], a[16:19], v[40:43]\n" \
"ds_read_b64_tr_b16 a[112:113], v102 offset:1536\n" \
"v_add_u32 v227, 144384, v224\n" \
"v_mfma_f32_16x16x32_bf16 v[40:43], a[84:87], a[20:23], v[40:43]\n" \
"s_lshl_b32 s70, s61, 15\n" \
"v_mfma_f32_16x16x32_bf16 v[44:47], a[88:91], a[16:19], v[44:47]\n" \
"s_lshl_b32 s75, s63, 18\n" \
"ds_read_b64_tr_b16 a[114:115], v102 offset:5632\n" \
"v_mfma_f32_16x16x32_bf16 v[44:47], a[92:95], a[20:23], v[44:47]\n" \
"buffer_load_dwordx4 v69, s[44:47], s84 offen offset:1024 lds\n" \
"v_mfma_f32_16x16x32_bf16 v[60:63], a[88:91], a[24:27], v[60:63]\n" \
"s_sub_u32 s77, 3932160, s75\n" \
"ds_read_b64_tr_b16 a[116:117], v102 offset:17920\n" \
"v_mfma_f32_16x16x32_bf16 v[60:63], a[92:95], a[28:31], v[60:63]\n" \
"s_lshr_b32 s71, %1, 6\n" \
"v_mfma_f32_16x16x32_bf16 v[56:59], a[80:83], a[24:27], v[56:59]\n" \
"s_xor_b32 s71, s71, s63\n" \
"ds_read_b64_tr_b16 a[118:119], v102 offset:22016\n" \
"v_mfma_f32_16x16x32_bf16 v[56:59], a[84:87], a[28:31], v[56:59]\n" \
"buffer_load_dwordx4 v70, s[40:43], s84 offen offset:2048 lds\n" \
"v_mfma_f32_16x16x32_bf16 v[52:55], a[72:75], a[24:27], v[52:55]\n" \
"s_and_b32 s71, s71, 0\n" \
"ds_read_b64_tr_b16 a[120:121], v103 offset:1536\n" \
"v_mfma_f32_16x16x32_bf16 v[52:55], a[76:79], a[28:31], v[52:55]\n" \
"s_lshl_b32 s71, s71, 7\n" \
"v_mfma_f32_16x16x32_bf16 v[48:51], a[64:67], a[24:27], v[48:51]\n" \
"s_add_u32 s70, s70, s71\n" \
"ds_read_b64_tr_b16 a[122:123], v103 offset:5632\n" \
"v_mfma_f32_16x16x32_bf16 v[48:51], a[68:71], a[28:31], v[48:51]\n" \
"buffer_load_dwordx4 v71, s[44:47], s84 offen offset:3072 lds\n" \
"v_mfma_f32_16x16x32_bf16 v[144:147], a[64:67], a[32:35], v[144:147]\n" \
"s_add_u32 s75, s75, s70\n" \
"ds_read_b64_tr_b16 a[124:125], v103 offset:17920\n" \
"v_mfma_f32_16x16x32_bf16 v[144:147], a[68:71], a[36:39], v[144:147]\n" \
"s_add_u32 s77, s77, s70\n" \
"v_mfma_f32_16x16x32_bf16 v[148:151], a[72:75], a[32:35], v[148:151]\n" \
"s_xor_b32 s84, s84, 0x380000\n" \
"ds_read_b64_tr_b16 a[126:127], v103 offset:22016\n" \
"v_mfma_f32_16x16x32_bf16 v[148:151], a[76:79], a[36:39], v[148:151]\n" \
"s_lshl_b32 s71, s71, 12\n" \
"v_mfma_f32_16x16x32_bf16 v[152:155], a[80:83], a[32:35], v[152:155]\n" \
"s_add_u32 s78, s78, s71\n" \
"v_mfma_f32_16x16x32_bf16 v[152:155], a[84:87], a[36:39], v[152:155]\n" \
"s_add_u32 s89, s89, s71\n" \
"v_lshlrev_b32 v242, 2, v78\n" \
"v_and_b32 v236, 0xf0, v234\n" \
"v_mfma_f32_16x16x32_bf16 v[156:159], a[88:91], a[32:35], v[156:159]\n" \
"v_lshl_add_u32 v236, v87, 9, v236\n" \
"v_mfma_f32_16x16x32_bf16 v[156:159], a[92:95], a[36:39], v[156:159]\n" \
"s_add_u32 s71, s74, 0x10000\n" \
"v_add_u32 v230, s71, v88\n" \
"v_xor_b32 v231, 64, v230\n" \
"v_mfma_f32_16x16x32_bf16 v[188:191], a[88:91], a[40:43], v[188:191]\n" \
"v_add_u32 v250, 0x10000, v230\n" \
"v_add_u32 v251, 0x10000, v231\n" \
"v_mfma_f32_16x16x32_bf16 v[188:191], a[92:95], a[44:47], v[188:191]\n" \
"v_lshl_add_u32 v82, v90, 12, v89\n" \
"v_add_u32 v232, %3, v82\n" \
"v_mfma_f32_16x16x32_bf16 v[184:187], a[80:83], a[40:43], v[184:187]\n" \
"v_xor_b32 v233, 32, v232\n" \
"v_mfma_f32_16x16x32_bf16 v[184:187], a[84:87], a[44:47], v[184:187]\n" \
"s_mul_i32 s87, s62, 557824\n" \
"v_mfma_f32_16x16x32_bf16 v[180:183], a[72:75], a[40:43], v[180:183]\n" \
"s_lshr_b32 s70, s61, 1\n" \
"v_mfma_f32_16x16x32_bf16 v[180:183], a[76:79], a[44:47], v[180:183]\n" \
"s_mul_i32 s70, s70, 17408\n" \
"v_mfma_f32_16x16x32_bf16 v[176:179], a[64:67], a[40:43], v[176:179]\n" \
"Lpg585_%=:\n" \
"Lst0x0_%=:\n" \
"s_cbranch_execz Lpg2763_%=\n" \
"s_add_u32 s87, s87, s70\n" \
"v_mfma_f32_16x16x32_bf16 v[176:179], a[68:71], a[44:47], v[176:179]\n" \
"s_waitcnt lgkmcnt(0)\n" \
"s_and_b32 s70, s61, 1\n" \
"v_mfma_f32_16x16x32_bf16 v[192:195], a[64:67], a[48:51], v[192:195]\n" \
"s_lshl_b32 s70, s70, 12\n" \
"v_mfma_f32_16x16x32_bf16 v[192:195], a[68:71], a[52:55], v[192:195]\n" \
"s_add_u32 s87, s87, s70\n" \
"v_mfma_f32_16x16x32_bf16 v[196:199], a[72:75], a[48:51], v[196:199]\n" \
"s_mul_i32 s70, s63, 34816\n" \
"v_mfma_f32_16x16x32_bf16 v[196:199], a[76:79], a[52:55], v[196:199]\n" \
"s_add_u32 s94, s87, 522240\n" \
"v_mfma_f32_16x16x32_bf16 v[200:203], a[80:83], a[48:51], v[200:203]\n" \
"s_sub_u32 s94, s94, s70\n" \
"v_mfma_f32_16x16x32_bf16 v[200:203], a[84:87], a[52:55], v[200:203]\n" \
"s_add_u32 s87, s87, s70\n" \
"v_mfma_f32_16x16x32_bf16 v[204:207], a[88:91], a[48:51], v[204:207]\n" \
"s_lshl_b32 s90, s62, 7\n" \
"v_mfma_f32_16x16x32_bf16 v[204:207], a[92:95], a[52:55], v[204:207]\n" \
"s_add_u32 s95, s90, 65597440\n" \
"v_mfma_f32_16x16x32_bf16 v[220:223], a[88:91], a[56:59], v[220:223]\n" \
"s_add_u32 s90, s90, 65585152\n" \
"v_mfma_f32_16x16x32_bf16 v[220:223], a[92:95], a[60:63], v[220:223]\n" \
"s_lshl_b32 s92, 0xff, s63\n" \
"v_mfma_f32_16x16x32_bf16 v[216:219], a[80:83], a[56:59], v[216:219]\n" \
"s_and_b32 s92, s92, 0xff\n" \
"v_mfma_f32_16x16x32_bf16 v[216:219], a[84:87], a[60:63], v[216:219]\n" \
"s_mov_b32 s76, 0x3e0293ee\n" \
"v_mfma_f32_16x16x32_bf16 v[212:215], a[72:75], a[56:59], v[212:215]\n" \
"s_mov_b32 s31, 0x3e800000\n" \
"v_mfma_f32_16x16x32_bf16 v[212:215], a[76:79], a[60:63], v[212:215]\n" \
"v_mfma_f32_16x16x32_bf16 v[208:211], a[64:67], a[56:59], v[208:211]\n" \
"v_mfma_f32_16x16x32_bf16 v[208:211], a[68:71], a[60:63], v[208:211]\n" \
"s_waitcnt vmcnt(10) lgkmcnt(0)\n" \
"s_barrier\n" \
"v_readfirstlane_b32 s60, v240\n" \
"s_add_u32 s60, s60, 0x9e3779b9\n" \
"v_mfma_f32_16x16x32_bf16 a[128:131], a[0:3], a[96:99], a[128:131]\n" \
"s_add_u32 m0, s74, 131072\n" \
"ds_read_b64_tr_b16 a[64:65], v98 offset:32768\n" \
"v_mfma_f32_16x16x32_bf16 a[128:131], a[4:7], a[100:103], a[128:131]\n" \
"buffer_load_dwordx4 v64, s[36:39], s81 offen offset:0 lds\n" \
"v_mfma_f32_16x16x32_bf16 a[132:135], a[0:3], a[104:107], a[132:135]\n" \
"ds_read_b64_tr_b16 a[66:67], v98 offset:36864\n" \
"v_mfma_f32_16x16x32_bf16 a[132:135], a[4:7], a[108:111], a[132:135]\n" \
"v_mfma_f32_16x16x32_bf16 a[136:139], a[0:3], a[112:115], a[136:139]\n" \
"ds_read_b64_tr_b16 a[68:69], v98 offset:49152\n" \
"v_mfma_f32_16x16x32_bf16 a[136:139], a[4:7], a[116:119], a[136:139]\n" \
"buffer_load_dwordx4 v65, s[36:39], s81 offen offset:1024 lds\n" \
"v_mfma_f32_16x16x32_bf16 a[140:143], a[0:3], a[120:123], a[140:143]\n" \
"ds_read_b64_tr_b16 a[70:71], v98 offset:53248\n" \
"v_mfma_f32_16x16x32_bf16 a[140:143], a[4:7], a[124:127], a[140:143]\n" \
"v_mfma_f32_16x16x32_bf16 a[156:159], a[8:11], a[120:123], a[156:159]\n" \
"ds_read_b128 a[0:3], v72 offset:32768\n" \
"v_mfma_f32_16x16x32_bf16 a[156:159], a[12:15], a[124:127], a[156:159]\n" \
"buffer_load_dwordx4 v66, s[36:39], s81 offen offset:2048 lds\n" \
"v_mfma_f32_16x16x32_bf16 a[152:155], a[8:11], a[112:115], a[152:155]\n" \
"ds_read_b64_tr_b16 a[72:73], v99 offset:32768\n" \
"v_mfma_f32_16x16x32_bf16 a[152:155], a[12:15], a[116:119], a[152:155]\n" \
"v_mfma_f32_16x16x32_bf16 a[148:151], a[8:11], a[104:107], a[148:151]\n" \
"ds_read_b128 a[4:7], v73 offset:32768\n" \
"v_mfma_f32_16x16x32_bf16 a[148:151], a[12:15], a[108:111], a[148:151]\n" \
"buffer_load_dwordx4 v67, s[36:39], s81 offen offset:3072 lds\n" \
"v_mfma_f32_16x16x32_bf16 a[144:147], a[8:11], a[96:99], a[144:147]\n" \
"ds_read_b64_tr_b16 a[74:75], v99 offset:36864\n" \
"v_mfma_f32_16x16x32_bf16 a[144:147], a[12:15], a[100:103], a[144:147]\n" \
"s_xor_b32 s81, s81, 0x380\n" \
"v_mfma_f32_16x16x32_bf16 a[160:163], a[16:19], a[96:99], a[160:163]\n" \
"s_add_u32 m0, s74, 135168\n" \
"ds_read_b128 a[8:11], v72 offset:34816\n" \
"v_mfma_f32_16x16x32_bf16 a[160:163], a[20:23], a[100:103], a[160:163]\n" \
"buffer_load_dwordx4 v64, s[36:39], s82 offen offset:0 lds\n" \
"v_mfma_f32_16x16x32_bf16 a[164:167], a[16:19], a[104:107], a[164:167]\n" \
"ds_read_b64_tr_b16 a[76:77], v99 offset:49152\n" \
"v_mfma_f32_16x16x32_bf16 a[164:167], a[20:23], a[108:111], a[164:167]\n" \
"v_mfma_f32_16x16x32_bf16 a[168:171], a[16:19], a[112:115], a[168:171]\n" \
"ds_read_b128 a[12:15], v73 offset:34816\n" \
"v_mfma_f32_16x16x32_bf16 a[168:171], a[20:23], a[116:119], a[168:171]\n" \
"buffer_load_dwordx4 v65, s[36:39], s82 offen offset:1024 lds\n" \
"v_mfma_f32_16x16x32_bf16 a[172:175], a[16:19], a[120:123], a[172:175]\n" \
"ds_read_b64_tr_b16 a[78:79], v99 offset:53248\n" \
"v_mfma_f32_16x16x32_bf16 a[172:175], a[20:23], a[124:127], a[172:175]\n" \
"v_mfma_f32_16x16x32_bf16 a[188:191], a[24:27], a[120:123], a[188:191]\n" \
"ds_read_b128 a[16:19], v72 offset:36864\n" \
"v_mfma_f32_16x16x32_bf16 a[188:191], a[28:31], a[124:127], a[188:191]\n" \
"buffer_load_dwordx4 v66, s[36:39], s82 offen offset:2048 lds\n" \
"v_mfma_f32_16x16x32_bf16 a[184:187], a[24:27], a[112:115], a[184:187]\n" \
"ds_read_b64_tr_b16 a[80:81], v98 offset:33280\n" \
"v_mfma_f32_16x16x32_bf16 a[184:187], a[28:31], a[116:119], a[184:187]\n" \
"v_mfma_f32_16x16x32_bf16 a[180:183], a[24:27], a[104:107], a[180:183]\n" \
"ds_read_b128 a[20:23], v73 offset:36864\n" \
"v_mfma_f32_16x16x32_bf16 a[180:183], a[28:31], a[108:111], a[180:183]\n" \
"buffer_load_dwordx4 v67, s[36:39], s82 offen offset:3072 lds\n" \
"v_mfma_f32_16x16x32_bf16 a[176:179], a[24:27], a[96:99], a[176:179]\n" \
"s_mov_b64 s[64:65], s[14:15]\n" \
"s_add_u32 s68, s67, 0x300000\n" \
"ds_read_b64_tr_b16 a[82:83], v98 offset:37376\n" \
"v_mfma_f32_16x16x32_bf16 a[176:179], a[28:31], a[100:103], a[176:179]\n" \
"s_add_u32 s64, s64, s68\n" \
"s_addc_u32 s65, s65, 0\n" \
"v_mfma_f32_16x16x32_bf16 a[192:195], a[32:35], a[96:99], a[192:195]\n" \
"s_xor_b32 s82, s82, 0x380\n" \
"ds_read_b128 a[24:27], v72 offset:38912\n" \
"v_mfma_f32_16x16x32_bf16 a[192:195], a[36:39], a[100:103], a[192:195]\n" \
"global_load_dword v238, v234, s[64:65]\n" \
"v_mfma_f32_16x16x32_bf16 a[196:199], a[32:35], a[104:107], a[196:199]\n" \
"s_mov_b64 s[64:65], s[16:17]\n" \
"s_add_u32 s68, s67, 0x300000\n" \
"ds_read_b64_tr_b16 a[84:85], v98 offset:49664\n" \
"v_mfma_f32_16x16x32_bf16 a[196:199], a[36:39], a[108:111], a[196:199]\n" \
"s_add_u32 s64, s64, s68\n" \
"s_addc_u32 s65, s65, 0\n" \
"v_mfma_f32_16x16x32_bf16 a[200:203], a[32:35], a[112:115], a[200:203]\n" \
"ds_read_b128 a[28:31], v73 offset:38912\n" \
"v_mfma_f32_16x16x32_bf16 a[200:203], a[36:39], a[116:119], a[200:203]\n" \
"global_load_dword v238, v234, s[64:65]\n" \
"v_mfma_f32_16x16x32_bf16 a[204:207], a[32:35], a[120:123], a[204:207]\n" \
"ds_read_b64_tr_b16 a[86:87], v98 offset:53760\n" \
"v_mfma_f32_16x16x32_bf16 a[204:207], a[36:39], a[124:127], a[204:207]\n" \
"v_mfma_f32_16x16x32_bf16 a[220:223], a[40:43], a[120:123], a[220:223]\n" \
"ds_read_b128 a[32:35], v72 offset:40960\n" \
"v_mfma_f32_16x16x32_bf16 a[220:223], a[44:47], a[124:127], a[220:223]\n" \
"v_mfma_f32_16x16x32_bf16 a[216:219], a[40:43], a[112:115], a[216:219]\n" \
"ds_read_b64_tr_b16 a[88:89], v99 offset:33280\n" \
"v_mfma_f32_16x16x32_bf16 a[216:219], a[44:47], a[116:119], a[216:219]\n" \
"v_mfma_f32_16x16x32_bf16 a[212:215], a[40:43], a[104:107], a[212:215]\n" \
"ds_read_b128 a[36:39], v73 offset:40960\n" \
"v_mfma_f32_16x16x32_bf16 a[212:215], a[44:47], a[108:111], a[212:215]\n" \
"v_mfma_f32_16x16x32_bf16 a[208:211], a[40:43], a[96:99], a[208:211]\n" \
"ds_read_b64_tr_b16 a[90:91], v99 offset:37376\n" \
"v_mfma_f32_16x16x32_bf16 a[208:211], a[44:47], a[100:103], a[208:211]\n" \
"v_mfma_f32_16x16x32_bf16 a[224:227], a[48:51], a[96:99], a[224:227]\n" \
"ds_read_b128 a[40:43], v72 offset:43008\n" \
"v_mfma_f32_16x16x32_bf16 a[224:227], a[52:55], a[100:103], a[224:227]\n" \
"v_mfma_f32_16x16x32_bf16 a[228:231], a[48:51], a[104:107], a[228:231]\n" \
"ds_read_b64_tr_b16 a[92:93], v99 offset:49664\n" \
"v_mfma_f32_16x16x32_bf16 a[228:231], a[52:55], a[108:111], a[228:231]\n" \
"v_mfma_f32_16x16x32_bf16 a[232:235], a[48:51], a[112:115], a[232:235]\n" \
"ds_read_b128 a[44:47], v73 offset:43008\n" \
"v_mfma_f32_16x16x32_bf16 a[232:235], a[52:55], a[116:119], a[232:235]\n" \
"v_mfma_f32_16x16x32_bf16 a[236:239], a[48:51], a[120:123], a[236:239]\n" \
"ds_read_b64_tr_b16 a[94:95], v99 offset:53760\n" \
"v_mfma_f32_16x16x32_bf16 a[236:239], a[52:55], a[124:127], a[236:239]\n" \
"v_mfma_f32_16x16x32_bf16 a[252:255], a[56:59], a[120:123], a[252:255]\n" \
"ds_read_b128 a[48:51], v72 offset:45056\n" \
"v_mfma_f32_16x16x32_bf16 a[252:255], a[60:63], a[124:127], a[252:255]\n" \
"v_mfma_f32_16x16x32_bf16 a[248:251], a[56:59], a[112:115], a[248:251]\n" \
"v_mfma_f32_16x16x32_bf16 a[248:251], a[60:63], a[116:119], a[248:251]\n" \
"v_mfma_f32_16x16x32_bf16 a[244:247], a[56:59], a[104:107], a[244:247]\n" \
"ds_read_b128 a[52:55], v73 offset:45056\n" \
"v_mfma_f32_16x16x32_bf16 a[244:247], a[60:63], a[108:111], a[244:247]\n" \
"v_mfma_f32_16x16x32_bf16 a[240:243], a[56:59], a[96:99], a[240:243]\n" \
"v_mfma_f32_16x16x32_bf16 a[240:243], a[60:63], a[100:103], a[240:243]\n" \
"s_waitcnt lgkmcnt(1)\n" \
"v_mfma_f32_16x16x32_bf16 v[0:3], a[64:67], a[0:3], v[0:3]\n" \
"ds_read_b128 a[56:59], v72 offset:47104\n" \
"v_lshrrev_b32 v104, 4, v234\n" \
"v_mfma_f32_16x16x32_bf16 v[0:3], a[68:71], a[4:7], v[0:3]\n" \
"v_bfe_u32 v105, v104, 4, 1\n" \
"v_bfe_u32 v106, v104, 2, 2\n" \
"v_mfma_f32_16x16x32_bf16 v[4:7], a[72:75], a[0:3], v[4:7]\n" \
"s_add_u32 m0, s74, 65536\n" \
"ds_read_b128 a[60:63], v73 offset:47104\n" \
"v_lshl_add_u32 v106, v105, 3, v106\n" \
"v_mfma_f32_16x16x32_bf16 v[4:7], a[76:79], a[4:7], v[4:7]\n" \
"buffer_load_dwordx4 v68, s[40:43], s83 offen offset:0 lds\n" \
"v_mfma_f32_16x16x32_bf16 v[8:11], a[80:83], a[0:3], v[8:11]\n" \
"s_lshl_b32 s71, s61, 4\n" \
"ds_read_b64_tr_b16 a[96:97], v98 offset:33792\n" \
"v_add_u32 v106, s71, v106\n" \
"v_mfma_f32_16x16x32_bf16 v[8:11], a[84:87], a[4:7], v[8:11]\n" \
"v_mul_u32_u24 v106, 6144, v106\n" \
"v_lshlrev_b32 v105, 1, v105\n" \
"v_mfma_f32_16x16x32_bf16 v[12:15], a[88:91], a[0:3], v[12:15]\n" \
"ds_read_b64_tr_b16 a[98:99], v98 offset:37888\n" \
"v_and_b32 v107, 3, v104\n" \
"v_mfma_f32_16x16x32_bf16 v[12:15], a[92:95], a[4:7], v[12:15]\n" \
"buffer_load_dwordx4 v69, s[44:47], s83 offen offset:1024 lds\n" \
"v_mfma_f32_16x16x32_bf16 v[28:31], a[88:91], a[8:11], v[28:31]\n" \
"ds_read_b64_tr_b16 a[100:101], v98 offset:50176\n" \
"v_xor_b32 v105, v105, v107\n" \
"v_mfma_f32_16x16x32_bf16 v[28:31], a[92:95], a[12:15], v[28:31]\n" \
"v_lshl_add_u32 v106, v105, 4, v106\n" \
"v_lshrrev_b32 v105, 5, v104\n" \
"v_mfma_f32_16x16x32_bf16 v[24:27], a[80:83], a[8:11], v[24:27]\n" \
"ds_read_b64_tr_b16 a[102:103], v98 offset:54272\n" \
"v_lshl_add_u32 v244, v105, 6, v106\n" \
"v_mfma_f32_16x16x32_bf16 v[24:27], a[84:87], a[12:15], v[24:27]\n" \
"buffer_load_dwordx4 v70, s[40:43], s83 offen offset:2048 lds\n" \
"v_mfma_f32_16x16x32_bf16 v[20:23], a[72:75], a[8:11], v[20:23]\n" \
"ds_read_b64_tr_b16 a[104:105], v99 offset:33792\n" \
"v_lshrrev_b32 v107, 3, v104\n" \
"v_mfma_f32_16x16x32_bf16 v[20:23], a[76:79], a[12:15], v[20:23]\n" \
"v_and_b32 v108, 7, v104\n" \
"v_xor_b32 v109, v108, v107\n" \
"v_mfma_f32_16x16x32_bf16 v[16:19], a[64:67], a[8:11], v[16:19]\n" \
"ds_read_b64_tr_b16 a[106:107], v99 offset:37888\n" \
"v_lshlrev_b32 v109, 4, v109\n" \
"v_mfma_f32_16x16x32_bf16 v[16:19], a[68:71], a[12:15], v[16:19]\n" \
"buffer_load_dwordx4 v71, s[44:47], s83 offen offset:3072 lds\n" \
"v_mfma_f32_16x16x32_bf16 v[32:35], a[64:67], a[16:19], v[32:35]\n" \
"ds_read_b64_tr_b16 a[108:109], v99 offset:50176\n" \
"v_lshl_add_u32 v245, v107, 12, v109\n" \
"v_mfma_f32_16x16x32_bf16 v[32:35], a[68:71], a[20:23], v[32:35]\n" \
"s_xor_b32 s83, s83, 0x80000\n" \
"v_mfma_f32_16x16x32_bf16 v[36:39], a[72:75], a[16:19], v[36:39]\n" \
"s_add_u32 m0, s74, 69632\n" \
"ds_read_b64_tr_b16 a[110:111], v99 offset:54272\n" \
"v_mfma_f32_16x16x32_bf16 v[36:39], a[76:79], a[20:23], v[36:39]\n" \
"buffer_load_dwordx4 v68, s[40:43], s84 offen offset:0 lds\n" \
"v_mfma_f32_16x16x32_bf16 v[40:43], a[80:83], a[16:19], v[40:43]\n" \
"s_lshr_b32 s71, s61, 1\n" \
"ds_read_b64_tr_b16 a[112:113], v98 offset:34304\n" \
"v_mfma_f32_16x16x32_bf16 v[40:43], a[84:87], a[20:23], v[40:43]\n" \
"s_lshl_b32 s70, s71, 11\n" \
"v_lshrrev_b32 v107, 4, v104\n" \
"v_xor_b32 v107, v107, v108\n" \
"v_mfma_f32_16x16x32_bf16 v[44:47], a[88:91], a[16:19], v[44:47]\n" \
"ds_read_b64_tr_b16 a[114:115], v98 offset:38400\n" \
"v_and_b32 v109, 15, v104\n" \
"v_mfma_f32_16x16x32_bf16 v[44:47], a[92:95], a[20:23], v[44:47]\n" \
"buffer_load_dwordx4 v69, s[44:47], s84 offen offset:1024 lds\n" \
"v_mfma_f32_16x16x32_bf16 v[60:63], a[88:91], a[24:27], v[60:63]\n" \
"ds_read_b64_tr_b16 a[116:117], v98 offset:50688\n" \
"v_lshlrev_b32 v106, 7, v109\n" \
"v_mfma_f32_16x16x32_bf16 v[60:63], a[92:95], a[28:31], v[60:63]\n" \
"v_lshl_add_u32 v106, v107, 4, v106\n" \
"v_mfma_f32_16x16x32_bf16 v[56:59], a[80:83], a[24:27], v[56:59]\n" \
"s_add_u32 s70, s70, %3\n" \
"ds_read_b64_tr_b16 a[118:119], v98 offset:54784\n" \
"v_add_u32 v247, s70, v106\n" \
"v_mfma_f32_16x16x32_bf16 v[56:59], a[84:87], a[28:31], v[56:59]\n" \
"buffer_load_dwordx4 v70, s[40:43], s84 offen offset:2048 lds\n" \
"v_mfma_f32_16x16x32_bf16 v[52:55], a[72:75], a[24:27], v[52:55]\n" \
"ds_read_b64_tr_b16 a[120:121], v99 offset:34304\n" \
"v_lshrrev_b32 v109, 3, v109\n" \
"v_mfma_f32_16x16x32_bf16 v[52:55], a[76:79], a[28:31], v[52:55]\n" \
"v_mul_u32_u24 v109, 7168, v109\n" \
"v_add_u32 v109, v109, v106\n" \
"v_mfma_f32_16x16x32_bf16 v[48:51], a[64:67], a[24:27], v[48:51]\n" \
"s_add_u32 s70, %3, 131072\n" \
"ds_read_b64_tr_b16 a[122:123], v99 offset:38400\n" \
"v_add_u32 v248, s70, v109\n" \
"v_mfma_f32_16x16x32_bf16 v[48:51], a[68:71], a[28:31], v[48:51]\n" \
"buffer_load_dwordx4 v71, s[44:47], s84 offen offset:3072 lds\n" \
"v_mfma_f32_16x16x32_bf16 v[144:147], a[64:67], a[32:35], v[144:147]\n" \
"ds_read_b64_tr_b16 a[124:125], v99 offset:50688\n" \
"v_and_b32 v106, 3, v104\n" \
"v_mfma_f32_16x16x32_bf16 v[144:147], a[68:71], a[36:39], v[144:147]\n" \
"v_lshlrev_b32 v106, 3, v106\n" \
"v_bfe_u32 v107, v104, 2, 2\n" \
"v_mfma_f32_16x16x32_bf16 v[148:151], a[72:75], a[32:35], v[148:151]\n" \
"s_xor_b32 s84, s84, 0x80000\n" \
"ds_read_b64_tr_b16 a[126:127], v99 offset:54784\n" \
"v_lshl_add_u32 v106, v107, 6, v106\n" \
"v_mfma_f32_16x16x32_bf16 v[148:151], a[76:79], a[36:39], v[148:151]\n" \
"v_bfe_u32 v107, v104, 4, 1\n" \
"v_mul_u32_u24 v107, 288, v107\n" \
"v_mfma_f32_16x16x32_bf16 v[152:155], a[80:83], a[32:35], v[152:155]\n" \
"v_mul_u32_u24 v108, 6144, v105\n" \
"v_add3_u32 v106, v106, v107, v108\n" \
"v_mfma_f32_16x16x32_bf16 v[152:155], a[84:87], a[36:39], v[152:155]\n" \
"s_and_b32 s70, s61, 1\n" \
"v_mfma_f32_16x16x32_bf16 v[156:159], a[88:91], a[32:35], v[156:159]\n" \
"s_lshl_b32 s70, s70, 9\n" \
"v_mfma_f32_16x16x32_bf16 v[156:159], a[92:95], a[36:39], v[156:159]\n" \
"s_add_u32 s70, s70, %3\n" \
"v_add_u32 v249, s70, v106\n" \
"v_mfma_f32_16x16x32_bf16 v[188:191], a[88:91], a[40:43], v[188:191]\n" \
"s_lshl_b32 s70, s71, 5\n" \
"v_xor_b32 v249, s70, v249\n" \
"v_mfma_f32_16x16x32_bf16 v[188:191], a[92:95], a[44:47], v[188:191]\n" \
"v_mfma_f32_16x16x32_bf16 v[184:187], a[80:83], a[40:43], v[184:187]\n" \
"v_mfma_f32_16x16x32_bf16 v[184:187], a[84:87], a[44:47], v[184:187]\n" \
"v_mfma_f32_16x16x32_bf16 v[180:183], a[72:75], a[40:43], v[180:183]\n" \
"v_mfma_f32_16x16x32_bf16 v[180:183], a[76:79], a[44:47], v[180:183]\n" \
"v_mfma_f32_16x16x32_bf16 v[176:179], a[64:67], a[40:43], v[176:179]\n" \
"v_mfma_f32_16x16x32_bf16 v[176:179], a[68:71], a[44:47], v[176:179]\n" \
"s_waitcnt lgkmcnt(0)\n" \
"v_mfma_f32_16x16x32_bf16 v[192:195], a[64:67], a[48:51], v[192:195]\n" \
"v_mfma_f32_16x16x32_bf16 v[192:195], a[68:71], a[52:55], v[192:195]\n" \
"v_mfma_f32_16x16x32_bf16 v[196:199], a[72:75], a[48:51], v[196:199]\n" \
"v_mfma_f32_16x16x32_bf16 v[196:199], a[76:79], a[52:55], v[196:199]\n" \
"v_mfma_f32_16x16x32_bf16 v[200:203], a[80:83], a[48:51], v[200:203]\n" \
"v_mfma_f32_16x16x32_bf16 v[200:203], a[84:87], a[52:55], v[200:203]\n" \
"v_mfma_f32_16x16x32_bf16 v[204:207], a[88:91], a[48:51], v[204:207]\n" \
"v_mfma_f32_16x16x32_bf16 v[204:207], a[92:95], a[52:55], v[204:207]\n" \
"v_mfma_f32_16x16x32_bf16 v[220:223], a[88:91], a[56:59], v[220:223]\n" \
"v_mfma_f32_16x16x32_bf16 v[220:223], a[92:95], a[60:63], v[220:223]\n" \
"v_mfma_f32_16x16x32_bf16 v[216:219], a[80:83], a[56:59], v[216:219]\n" \
"v_mfma_f32_16x16x32_bf16 v[216:219], a[84:87], a[60:63], v[216:219]\n" \
"v_mfma_f32_16x16x32_bf16 v[212:215], a[72:75], a[56:59], v[212:215]\n" \
"v_mfma_f32_16x16x32_bf16 v[212:215], a[76:79], a[60:63], v[212:215]\n" \
"v_mfma_f32_16x16x32_bf16 v[208:211], a[64:67], a[56:59], v[208:211]\n" \
"v_mfma_f32_16x16x32_bf16 v[208:211], a[68:71], a[60:63], v[208:211]\n" \
"s_waitcnt vmcnt(10) lgkmcnt(0)\n" \
"s_barrier\n" \
"v_mfma_f32_16x16x32_bf16 a[128:131], a[0:3], a[96:99], a[128:131]\n" \
"s_add_u32 m0, s74, 98304\n" \
"ds_read_b64_tr_b16 a[64:65], v74 offset:0\n" \
"v_mfma_f32_16x16x32_bf16 a[128:131], a[4:7], a[100:103], a[128:131]\n" \
"buffer_load_dwordx4 v64, s[36:39], s81 offen offset:0 lds\n" \
"v_mfma_f32_16x16x32_bf16 a[132:135], a[0:3], a[104:107], a[132:135]\n" \
"ds_read_b64_tr_b16 a[66:67], v74 offset:4096\n" \
"v_mfma_f32_16x16x32_bf16 a[132:135], a[4:7], a[108:111], a[132:135]\n" \
"v_mfma_f32_16x16x32_bf16 a[136:139], a[0:3], a[112:115], a[136:139]\n" \
"ds_read_b64_tr_b16 a[68:69], v74 offset:16384\n" \
"v_mfma_f32_16x16x32_bf16 a[136:139], a[4:7], a[116:119], a[136:139]\n" \
"buffer_load_dwordx4 v65, s[36:39], s81 offen offset:1024 lds\n" \
"v_mfma_f32_16x16x32_bf16 a[140:143], a[0:3], a[120:123], a[140:143]\n" \
"ds_read_b64_tr_b16 a[70:71], v74 offset:20480\n" \
"v_mfma_f32_16x16x32_bf16 a[140:143], a[4:7], a[124:127], a[140:143]\n" \
"v_mfma_f32_16x16x32_bf16 a[156:159], a[8:11], a[120:123], a[156:159]\n" \
"ds_read_b128 a[0:3], v100 offset:0\n" \
"v_mfma_f32_16x16x32_bf16 a[156:159], a[12:15], a[124:127], a[156:159]\n" \
"buffer_load_dwordx4 v66, s[36:39], s81 offen offset:2048 lds\n" \
"v_mfma_f32_16x16x32_bf16 a[152:155], a[8:11], a[112:115], a[152:155]\n" \
"ds_read_b64_tr_b16 a[72:73], v75 offset:0\n" \
"v_mfma_f32_16x16x32_bf16 a[152:155], a[12:15], a[116:119], a[152:155]\n" \
"v_mfma_f32_16x16x32_bf16 a[148:151], a[8:11], a[104:107], a[148:151]\n" \
"ds_read_b128 a[4:7], v101 offset:0\n" \
"v_mfma_f32_16x16x32_bf16 a[148:151], a[12:15], a[108:111], a[148:151]\n" \
"buffer_load_dwordx4 v67, s[36:39], s81 offen offset:3072 lds\n" \
"v_mfma_f32_16x16x32_bf16 a[144:147], a[8:11], a[96:99], a[144:147]\n" \
"ds_read_b64_tr_b16 a[74:75], v75 offset:4096\n" \
"v_mfma_f32_16x16x32_bf16 a[144:147], a[12:15], a[100:103], a[144:147]\n" \
"s_xor_b32 s81, s81, 0x80\n" \
"v_mfma_f32_16x16x32_bf16 a[160:163], a[16:19], a[96:99], a[160:163]\n" \
"s_add_u32 m0, s74, 102400\n" \
"ds_read_b128 a[8:11], v100 offset:2048\n" \
"v_mfma_f32_16x16x32_bf16 a[160:163], a[20:23], a[100:103], a[160:163]\n" \
"buffer_load_dwordx4 v64, s[36:39], s82 offen offset:0 lds\n" \
"v_mfma_f32_16x16x32_bf16 a[164:167], a[16:19], a[104:107], a[164:167]\n" \
"ds_read_b64_tr_b16 a[76:77], v75 offset:16384\n" \
"v_mfma_f32_16x16x32_bf16 a[164:167], a[20:23], a[108:111], a[164:167]\n" \
"v_mfma_f32_16x16x32_bf16 a[168:171], a[16:19], a[112:115], a[168:171]\n" \
"ds_read_b128 a[12:15], v101 offset:2048\n" \
"v_mfma_f32_16x16x32_bf16 a[168:171], a[20:23], a[116:119], a[168:171]\n" \
"buffer_load_dwordx4 v65, s[36:39], s82 offen offset:1024 lds\n" \
"v_mfma_f32_16x16x32_bf16 a[172:175], a[16:19], a[120:123], a[172:175]\n" \
"ds_read_b64_tr_b16 a[78:79], v75 offset:20480\n" \
"v_mfma_f32_16x16x32_bf16 a[172:175], a[20:23], a[124:127], a[172:175]\n" \
"v_mfma_f32_16x16x32_bf16 a[188:191], a[24:27], a[120:123], a[188:191]\n" \
"ds_read_b128 a[16:19], v100 offset:4096\n" \
"v_mfma_f32_16x16x32_bf16 a[188:191], a[28:31], a[124:127], a[188:191]\n" \
"buffer_load_dwordx4 v66, s[36:39], s82 offen offset:2048 lds\n" \
"v_mfma_f32_16x16x32_bf16 a[184:187], a[24:27], a[112:115], a[184:187]\n" \
"ds_read_b64_tr_b16 a[80:81], v74 offset:512\n" \
"v_mfma_f32_16x16x32_bf16 a[184:187], a[28:31], a[116:119], a[184:187]\n" \
"v_mfma_f32_16x16x32_bf16 a[180:183], a[24:27], a[104:107], a[180:183]\n" \
"ds_read_b128 a[20:23], v101 offset:4096\n" \
"v_mfma_f32_16x16x32_bf16 a[180:183], a[28:31], a[108:111], a[180:183]\n" \
"buffer_load_dwordx4 v67, s[36:39], s82 offen offset:3072 lds\n" \
"v_mfma_f32_16x16x32_bf16 a[176:179], a[24:27], a[96:99], a[176:179]\n" \
"s_mov_b64 s[64:65], s[8:9]\n" \
"s_add_u32 s68, s66, 0x0\n" \
"ds_read_b64_tr_b16 a[82:83], v74 offset:4608\n" \
"v_mfma_f32_16x16x32_bf16 a[176:179], a[28:31], a[100:103], a[176:179]\n" \
"s_add_u32 s64, s64, s68\n" \
"s_addc_u32 s65, s65, 0\n" \
"v_mfma_f32_16x16x32_bf16 a[192:195], a[32:35], a[96:99], a[192:195]\n" \
"s_xor_b32 s82, s82, 0x80\n" \
"ds_read_b128 a[24:27], v100 offset:6144\n" \
"v_mfma_f32_16x16x32_bf16 a[192:195], a[36:39], a[100:103], a[192:195]\n" \
"global_load_dword v238, v234, s[64:65]\n" \
"v_mfma_f32_16x16x32_bf16 a[196:199], a[32:35], a[104:107], a[196:199]\n" \
"s_mov_b64 s[64:65], s[8:9]\n" \
"s_add_u32 s68, s66, 0x100000\n" \
"ds_read_b64_tr_b16 a[84:85], v74 offset:16896\n" \
"v_mfma_f32_16x16x32_bf16 a[196:199], a[36:39], a[108:111], a[196:199]\n" \
"s_add_u32 s64, s64, s68\n" \
"s_addc_u32 s65, s65, 0\n" \
"v_mfma_f32_16x16x32_bf16 a[200:203], a[32:35], a[112:115], a[200:203]\n" \
"ds_read_b128 a[28:31], v101 offset:6144\n" \
"v_mfma_f32_16x16x32_bf16 a[200:203], a[36:39], a[116:119], a[200:203]\n" \
"global_load_dword v238, v234, s[64:65]\n" \
"v_mfma_f32_16x16x32_bf16 a[204:207], a[32:35], a[120:123], a[204:207]\n" \
"ds_read_b64_tr_b16 a[86:87], v74 offset:20992\n" \
"v_mfma_f32_16x16x32_bf16 a[204:207], a[36:39], a[124:127], a[204:207]\n" \
"v_mfma_f32_16x16x32_bf16 a[220:223], a[40:43], a[120:123], a[220:223]\n" \
"ds_read_b128 a[32:35], v100 offset:8192\n" \
"v_mfma_f32_16x16x32_bf16 a[220:223], a[44:47], a[124:127], a[220:223]\n" \
"v_mfma_f32_16x16x32_bf16 a[216:219], a[40:43], a[112:115], a[216:219]\n" \
"ds_read_b64_tr_b16 a[88:89], v75 offset:512\n" \
"v_mfma_f32_16x16x32_bf16 a[216:219], a[44:47], a[116:119], a[216:219]\n" \
"v_mfma_f32_16x16x32_bf16 a[212:215], a[40:43], a[104:107], a[212:215]\n" \
"ds_read_b128 a[36:39], v101 offset:8192\n" \
"v_mfma_f32_16x16x32_bf16 a[212:215], a[44:47], a[108:111], a[212:215]\n" \
"v_mfma_f32_16x16x32_bf16 a[208:211], a[40:43], a[96:99], a[208:211]\n" \
"ds_read_b64_tr_b16 a[90:91], v75 offset:4608\n" \
"v_mfma_f32_16x16x32_bf16 a[208:211], a[44:47], a[100:103], a[208:211]\n" \
"v_mfma_f32_16x16x32_bf16 a[224:227], a[48:51], a[96:99], a[224:227]\n" \
"ds_read_b128 a[40:43], v100 offset:10240\n" \
"v_mfma_f32_16x16x32_bf16 a[224:227], a[52:55], a[100:103], a[224:227]\n" \
"v_mfma_f32_16x16x32_bf16 a[228:231], a[48:51], a[104:107], a[228:231]\n" \
"ds_read_b64_tr_b16 a[92:93], v75 offset:16896\n" \
"v_mfma_f32_16x16x32_bf16 a[228:231], a[52:55], a[108:111], a[228:231]\n" \
"v_mfma_f32_16x16x32_bf16 a[232:235], a[48:51], a[112:115], a[232:235]\n" \
"ds_read_b128 a[44:47], v101 offset:10240\n" \
"v_mfma_f32_16x16x32_bf16 a[232:235], a[52:55], a[116:119], a[232:235]\n" \
"v_mfma_f32_16x16x32_bf16 a[236:239], a[48:51], a[120:123], a[236:239]\n" \
"ds_read_b64_tr_b16 a[94:95], v75 offset:20992\n" \
"v_mfma_f32_16x16x32_bf16 a[236:239], a[52:55], a[124:127], a[236:239]\n" \
"v_mfma_f32_16x16x32_bf16 a[252:255], a[56:59], a[120:123], a[252:255]\n" \
"ds_read_b128 a[48:51], v100 offset:12288\n" \
"v_mfma_f32_16x16x32_bf16 a[252:255], a[60:63], a[124:127], a[252:255]\n" \
"v_mfma_f32_16x16x32_bf16 a[248:251], a[56:59], a[112:115], a[248:251]\n" \
"v_mfma_f32_16x16x32_bf16 a[248:251], a[60:63], a[116:119], a[248:251]\n" \
"v_mfma_f32_16x16x32_bf16 a[244:247], a[56:59], a[104:107], a[244:247]\n" \
"ds_read_b128 a[52:55], v101 offset:12288\n" \
"v_mfma_f32_16x16x32_bf16 a[244:247], a[60:63], a[108:111], a[244:247]\n" \
"v_mfma_f32_16x16x32_bf16 a[240:243], a[56:59], a[96:99], a[240:243]\n" \
"v_mfma_f32_16x16x32_bf16 a[240:243], a[60:63], a[100:103], a[240:243]\n" \
"s_waitcnt lgkmcnt(1)\n" \
"v_mfma_f32_16x16x32_bf16 v[0:3], a[64:67], a[0:3], v[0:3]\n" \
"ds_read_b128 a[56:59], v100 offset:14336\n" \
"v_mfma_f32_16x16x32_bf16 v[0:3], a[68:71], a[4:7], v[0:3]\n" \
"v_mfma_f32_16x16x32_bf16 v[4:7], a[72:75], a[0:3], v[4:7]\n" \
"s_add_u32 m0, s74, 32768\n" \
"ds_read_b128 a[60:63], v101 offset:14336\n" \
"v_mfma_f32_16x16x32_bf16 v[4:7], a[76:79], a[4:7], v[4:7]\n" \
"buffer_load_dwordx4 v68, s[40:43], s83 offen offset:0 lds\n" \
"v_mfma_f32_16x16x32_bf16 v[8:11], a[80:83], a[0:3], v[8:11]\n" \
"ds_read_b64_tr_b16 a[96:97], v74 offset:1024\n" \
"v_mfma_f32_16x16x32_bf16 v[8:11], a[84:87], a[4:7], v[8:11]\n" \
"v_mfma_f32_16x16x32_bf16 v[12:15], a[88:91], a[0:3], v[12:15]\n" \
"ds_read_b64_tr_b16 a[98:99], v74 offset:5120\n" \
"v_mfma_f32_16x16x32_bf16 v[12:15], a[92:95], a[4:7], v[12:15]\n" \
"buffer_load_dwordx4 v69, s[44:47], s83 offen offset:1024 lds\n" \
"v_mfma_f32_16x16x32_bf16 v[28:31], a[88:91], a[8:11], v[28:31]\n" \
"ds_read_b64_tr_b16 a[100:101], v74 offset:17408\n" \
"v_mfma_f32_16x16x32_bf16 v[28:31], a[92:95], a[12:15], v[28:31]\n" \
"v_mfma_f32_16x16x32_bf16 v[24:27], a[80:83], a[8:11], v[24:27]\n" \
"ds_read_b64_tr_b16 a[102:103], v74 offset:21504\n" \
"v_mfma_f32_16x16x32_bf16 v[24:27], a[84:87], a[12:15], v[24:27]\n" \
"buffer_load_dwordx4 v70, s[40:43], s83 offen offset:2048 lds\n" \
"v_mfma_f32_16x16x32_bf16 v[20:23], a[72:75], a[8:11], v[20:23]\n" \
"ds_read_b64_tr_b16 a[104:105], v75 offset:1024\n" \
"v_mfma_f32_16x16x32_bf16 v[20:23], a[76:79], a[12:15], v[20:23]\n" \
"v_mfma_f32_16x16x32_bf16 v[16:19], a[64:67], a[8:11], v[16:19]\n" \
"ds_read_b64_tr_b16 a[106:107], v75 offset:5120\n" \
"v_mfma_f32_16x16x32_bf16 v[16:19], a[68:71], a[12:15], v[16:19]\n" \
"buffer_load_dwordx4 v71, s[44:47], s83 offen offset:3072 lds\n" \
"v_mfma_f32_16x16x32_bf16 v[32:35], a[64:67], a[16:19], v[32:35]\n" \
"ds_read_b64_tr_b16 a[108:109], v75 offset:17408\n" \
"v_mfma_f32_16x16x32_bf16 v[32:35], a[68:71], a[20:23], v[32:35]\n" \
"s_xor_b32 s83, s83, 0x180000\n" \
"v_mfma_f32_16x16x32_bf16 v[36:39], a[72:75], a[16:19], v[36:39]\n" \
"s_add_u32 m0, s74, 36864\n" \
"ds_read_b64_tr_b16 a[110:111], v75 offset:21504\n" \
"v_mfma_f32_16x16x32_bf16 v[36:39], a[76:79], a[20:23], v[36:39]\n" \
"buffer_load_dwordx4 v68, s[40:43], s84 offen offset:0 lds\n" \
"v_mfma_f32_16x16x32_bf16 v[40:43], a[80:83], a[16:19], v[40:43]\n" \
"ds_read_b64_tr_b16 a[112:113], v74 offset:1536\n" \
"v_mfma_f32_16x16x32_bf16 v[40:43], a[84:87], a[20:23], v[40:43]\n" \
"v_mfma_f32_16x16x32_bf16 v[44:47], a[88:91], a[16:19], v[44:47]\n" \
"ds_read_b64_tr_b16 a[114:115], v74 offset:5632\n" \
"v_mfma_f32_16x16x32_bf16 v[44:47], a[92:95], a[20:23], v[44:47]\n" \
"buffer_load_dwordx4 v69, s[44:47], s84 offen offset:1024 lds\n" \
"v_mfma_f32_16x16x32_bf16 v[60:63], a[88:91], a[24:27], v[60:63]\n" \
"ds_read_b64_tr_b16 a[116:117], v74 offset:17920\n" \
"v_mfma_f32_16x16x32_bf16 v[60:63], a[92:95], a[28:31], v[60:63]\n" \
"v_mfma_f32_16x16x32_bf16 v[56:59], a[80:83], a[24:27], v[56:59]\n" \
"ds_read_b64_tr_b16 a[118:119], v74 offset:22016\n" \
"v_mfma_f32_16x16x32_bf16 v[56:59], a[84:87], a[28:31], v[56:59]\n" \
"buffer_load_dwordx4 v70, s[40:43], s84 offen offset:2048 lds\n" \
"v_mfma_f32_16x16x32_bf16 v[52:55], a[72:75], a[24:27], v[52:55]\n" \
"ds_read_b64_tr_b16 a[120:121], v75 offset:1536\n" \
"v_mfma_f32_16x16x32_bf16 v[52:55], a[76:79], a[28:31], v[52:55]\n" \
"v_mfma_f32_16x16x32_bf16 v[48:51], a[64:67], a[24:27], v[48:51]\n" \
"ds_read_b64_tr_b16 a[122:123], v75 offset:5632\n" \
"v_mfma_f32_16x16x32_bf16 v[48:51], a[68:71], a[28:31], v[48:51]\n" \
"buffer_load_dwordx4 v71, s[44:47], s84 offen offset:3072 lds\n" \
"v_mfma_f32_16x16x32_bf16 v[144:147], a[64:67], a[32:35], v[144:147]\n" \
"ds_read_b64_tr_b16 a[124:125], v75 offset:17920\n" \
"v_mfma_f32_16x16x32_bf16 v[144:147], a[68:71], a[36:39], v[144:147]\n" \
"v_mfma_f32_16x16x32_bf16 v[148:151], a[72:75], a[32:35], v[148:151]\n" \
"s_xor_b32 s84, s84, 0x180000\n" \
"ds_read_b64_tr_b16 a[126:127], v75 offset:22016\n" \
"v_mfma_f32_16x16x32_bf16 v[148:151], a[76:79], a[36:39], v[148:151]\n" \
"v_mfma_f32_16x16x32_bf16 v[152:155], a[80:83], a[32:35], v[152:155]\n" \
"v_mfma_f32_16x16x32_bf16 v[152:155], a[84:87], a[36:39], v[152:155]\n" \
"v_mfma_f32_16x16x32_bf16 v[156:159], a[88:91], a[32:35], v[156:159]\n" \
"v_mfma_f32_16x16x32_bf16 v[156:159], a[92:95], a[36:39], v[156:159]\n" \
"v_mfma_f32_16x16x32_bf16 v[188:191], a[88:91], a[40:43], v[188:191]\n" \
"v_mfma_f32_16x16x32_bf16 v[188:191], a[92:95], a[44:47], v[188:191]\n" \
"v_mfma_f32_16x16x32_bf16 v[184:187], a[80:83], a[40:43], v[184:187]\n" \
"v_mfma_f32_16x16x32_bf16 v[184:187], a[84:87], a[44:47], v[184:187]\n" \
"v_mfma_f32_16x16x32_bf16 v[180:183], a[72:75], a[40:43], v[180:183]\n" \
"v_mfma_f32_16x16x32_bf16 v[180:183], a[76:79], a[44:47], v[180:183]\n" \
"v_mfma_f32_16x16x32_bf16 v[176:179], a[64:67], a[40:43], v[176:179]\n" \
"v_mfma_f32_16x16x32_bf16 v[176:179], a[68:71], a[44:47], v[176:179]\n" \
"s_waitcnt lgkmcnt(0)\n" \
"v_mfma_f32_16x16x32_bf16 v[192:195], a[64:67], a[48:51], v[192:195]\n" \
"v_mfma_f32_16x16x32_bf16 v[192:195], a[68:71], a[52:55], v[192:195]\n" \
"v_mfma_f32_16x16x32_bf16 v[196:199], a[72:75], a[48:51], v[196:199]\n" \
"v_mfma_f32_16x16x32_bf16 v[196:199], a[76:79], a[52:55], v[196:199]\n" \
"v_mfma_f32_16x16x32_bf16 v[200:203], a[80:83], a[48:51], v[200:203]\n" \
"v_mfma_f32_16x16x32_bf16 v[200:203], a[84:87], a[52:55], v[200:203]\n" \
"v_mfma_f32_16x16x32_bf16 v[204:207], a[88:91], a[48:51], v[204:207]\n" \
"v_mfma_f32_16x16x32_bf16 v[204:207], a[92:95], a[52:55], v[204:207]\n" \
"v_mfma_f32_16x16x32_bf16 v[220:223], a[88:91], a[56:59], v[220:223]\n" \
"v_mfma_f32_16x16x32_bf16 v[220:223], a[92:95], a[60:63], v[220:223]\n" \
"v_mfma_f32_16x16x32_bf16 v[216:219], a[80:83], a[56:59], v[216:219]\n" \
"v_mfma_f32_16x16x32_bf16 v[216:219], a[84:87], a[60:63], v[216:219]\n" \
"v_mfma_f32_16x16x32_bf16 v[212:215], a[72:75], a[56:59], v[212:215]\n" \
"v_mfma_f32_16x16x32_bf16 v[212:215], a[76:79], a[60:63], v[212:215]\n" \
"v_mfma_f32_16x16x32_bf16 v[208:211], a[64:67], a[56:59], v[208:211]\n" \
"v_mfma_f32_16x16x32_bf16 v[208:211], a[68:71], a[60:63], v[208:211]\n" \
"s_waitcnt vmcnt(10) lgkmcnt(0)\n" \
"s_barrier\n" \
"v_mfma_f32_16x16x32_bf16 a[128:131], a[0:3], a[96:99], a[128:131]\n" \
"s_add_u32 m0, s74, 0\n" \
"ds_read_b64_tr_b16 a[64:65], v98 offset:0\n" \
"v_mfma_f32_16x16x32_bf16 a[128:131], a[4:7], a[100:103], a[128:131]\n" \
"buffer_load_dwordx4 v64, s[36:39], s81 offen offset:0 lds\n" \
"v_mfma_f32_16x16x32_bf16 a[132:135], a[0:3], a[104:107], a[132:135]\n" \
"ds_read_b64_tr_b16 a[66:67], v98 offset:4096\n" \
"v_mfma_f32_16x16x32_bf16 a[132:135], a[4:7], a[108:111], a[132:135]\n" \
"v_mfma_f32_16x16x32_bf16 a[136:139], a[0:3], a[112:115], a[136:139]\n" \
"ds_read_b64_tr_b16 a[68:69], v98 offset:16384\n" \
"v_mfma_f32_16x16x32_bf16 a[136:139], a[4:7], a[116:119], a[136:139]\n" \
"buffer_load_dwordx4 v65, s[36:39], s81 offen offset:1024 lds\n" \
"v_mfma_f32_16x16x32_bf16 a[140:143], a[0:3], a[120:123], a[140:143]\n" \
"ds_read_b64_tr_b16 a[70:71], v98 offset:20480\n" \
"v_mfma_f32_16x16x32_bf16 a[140:143], a[4:7], a[124:127], a[140:143]\n" \
"v_mfma_f32_16x16x32_bf16 a[156:159], a[8:11], a[120:123], a[156:159]\n" \
"ds_read_b128 a[0:3], v96 offset:32768\n" \
"v_mfma_f32_16x16x32_bf16 a[156:159], a[12:15], a[124:127], a[156:159]\n" \
"buffer_load_dwordx4 v66, s[36:39], s81 offen offset:2048 lds\n" \
"v_mfma_f32_16x16x32_bf16 a[152:155], a[8:11], a[112:115], a[152:155]\n" \
"ds_read_b64_tr_b16 a[72:73], v99 offset:0\n" \
"v_mfma_f32_16x16x32_bf16 a[152:155], a[12:15], a[116:119], a[152:155]\n" \
"v_mfma_f32_16x16x32_bf16 a[148:151], a[8:11], a[104:107], a[148:151]\n" \
"ds_read_b128 a[4:7], v97 offset:32768\n" \
"v_mfma_f32_16x16x32_bf16 a[148:151], a[12:15], a[108:111], a[148:151]\n" \
"buffer_load_dwordx4 v67, s[36:39], s81 offen offset:3072 lds\n" \
"v_mfma_f32_16x16x32_bf16 a[144:147], a[8:11], a[96:99], a[144:147]\n" \
"ds_read_b64_tr_b16 a[74:75], v99 offset:4096\n" \
"v_mfma_f32_16x16x32_bf16 a[144:147], a[12:15], a[100:103], a[144:147]\n" \
"s_xor_b32 s81, s81, 0x180\n" \
"v_mfma_f32_16x16x32_bf16 a[160:163], a[16:19], a[96:99], a[160:163]\n" \
"s_add_u32 m0, s74, 4096\n" \
"Lpg1129_%=:\n" \
"Lst0x1_%=:\n" \
"s_cbranch_execz Lpg3339_%=\n" \
"ds_read_b128 a[8:11], v96 offset:34816\n" \
"v_mfma_f32_16x16x32_bf16 a[160:163], a[20:23], a[100:103], a[160:163]\n" \
"buffer_load_dwordx4 v64, s[36:39], s82 offen offset:0 lds\n" \
"v_mfma_f32_16x16x32_bf16 a[164:167], a[16:19], a[104:107], a[164:167]\n" \
"ds_read_b64_tr_b16 a[76:77], v99 offset:16384\n" \
"v_mfma_f32_16x16x32_bf16 a[164:167], a[20:23], a[108:111], a[164:167]\n" \
"v_mfma_f32_16x16x32_bf16 a[168:171], a[16:19], a[112:115], a[168:171]\n" \
"ds_read_b128 a[12:15], v97 offset:34816\n" \
"v_mfma_f32_16x16x32_bf16 a[168:171], a[20:23], a[116:119], a[168:171]\n" \
"buffer_load_dwordx4 v65, s[36:39], s82 offen offset:1024 lds\n" \
"v_mfma_f32_16x16x32_bf16 a[172:175], a[16:19], a[120:123], a[172:175]\n" \
"ds_read_b64_tr_b16 a[78:79], v99 offset:20480\n" \
"v_mfma_f32_16x16x32_bf16 a[172:175], a[20:23], a[124:127], a[172:175]\n" \
"v_mfma_f32_16x16x32_bf16 a[188:191], a[24:27], a[120:123], a[188:191]\n" \
"ds_read_b128 a[16:19], v96 offset:36864\n" \
"v_mfma_f32_16x16x32_bf16 a[188:191], a[28:31], a[124:127], a[188:191]\n" \
"buffer_load_dwordx4 v66, s[36:39], s82 offen offset:2048 lds\n" \
"v_mfma_f32_16x16x32_bf16 a[184:187], a[24:27], a[112:115], a[184:187]\n" \
"ds_read_b64_tr_b16 a[80:81], v98 offset:512\n" \
"v_mfma_f32_16x16x32_bf16 a[184:187], a[28:31], a[116:119], a[184:187]\n" \
"v_mfma_f32_16x16x32_bf16 a[180:183], a[24:27], a[104:107], a[180:183]\n" \
"ds_read_b128 a[20:23], v97 offset:36864\n" \
"v_mfma_f32_16x16x32_bf16 a[180:183], a[28:31], a[108:111], a[180:183]\n" \
"buffer_load_dwordx4 v67, s[36:39], s82 offen offset:3072 lds\n" \
"v_mfma_f32_16x16x32_bf16 a[176:179], a[24:27], a[96:99], a[176:179]\n" \
"s_mov_b64 s[64:65], s[8:9]\n" \
"s_add_u32 s68, s66, 0x200000\n" \
"ds_read_b64_tr_b16 a[82:83], v98 offset:4608\n" \
"v_mfma_f32_16x16x32_bf16 a[176:179], a[28:31], a[100:103], a[176:179]\n" \
"s_add_u32 s64, s64, s68\n" \
"s_addc_u32 s65, s65, 0\n" \
"v_mfma_f32_16x16x32_bf16 a[192:195], a[32:35], a[96:99], a[192:195]\n" \
"s_xor_b32 s82, s82, 0x180\n" \
"ds_read_b128 a[24:27], v96 offset:38912\n" \
"v_mfma_f32_16x16x32_bf16 a[192:195], a[36:39], a[100:103], a[192:195]\n" \
"global_load_dword v238, v234, s[64:65]\n" \
"v_mfma_f32_16x16x32_bf16 a[196:199], a[32:35], a[104:107], a[196:199]\n" \
"s_mov_b64 s[64:65], s[8:9]\n" \
"s_add_u32 s68, s66, 0x300000\n" \
"ds_read_b64_tr_b16 a[84:85], v98 offset:16896\n" \
"v_mfma_f32_16x16x32_bf16 a[196:199], a[36:39], a[108:111], a[196:199]\n" \
"s_add_u32 s64, s64, s68\n" \
"s_addc_u32 s65, s65, 0\n" \
"v_mfma_f32_16x16x32_bf16 a[200:203], a[32:35], a[112:115], a[200:203]\n" \
"ds_read_b128 a[28:31], v97 offset:38912\n" \
"v_mfma_f32_16x16x32_bf16 a[200:203], a[36:39], a[116:119], a[200:203]\n" \
"global_load_dword v238, v234, s[64:65]\n" \
"v_mfma_f32_16x16x32_bf16 a[204:207], a[32:35], a[120:123], a[204:207]\n" \
"s_mov_b64 s[64:65], s[12:13]\n" \
"s_add_u32 s68, s67, 0x0\n" \
"ds_read_b64_tr_b16 a[86:87], v98 offset:20992\n" \
"v_mfma_f32_16x16x32_bf16 a[204:207], a[36:39], a[124:127], a[204:207]\n" \
"s_add_u32 s64, s64, s68\n" \
"s_addc_u32 s65, s65, 0\n" \
"v_mfma_f32_16x16x32_bf16 a[220:223], a[40:43], a[120:123], a[220:223]\n" \
"ds_read_b128 a[32:35], v96 offset:40960\n" \
"v_mfma_f32_16x16x32_bf16 a[220:223], a[44:47], a[124:127], a[220:223]\n" \
"global_load_dword v238, v234, s[64:65]\n" \
"v_mfma_f32_16x16x32_bf16 a[216:219], a[40:43], a[112:115], a[216:219]\n" \
"ds_read_b64_tr_b16 a[88:89], v99 offset:512\n" \
"v_mfma_f32_16x16x32_bf16 a[216:219], a[44:47], a[116:119], a[216:219]\n" \
"v_mfma_f32_16x16x32_bf16 a[212:215], a[40:43], a[104:107], a[212:215]\n" \
"ds_read_b128 a[36:39], v97 offset:40960\n" \
"v_mfma_f32_16x16x32_bf16 a[212:215], a[44:47], a[108:111], a[212:215]\n" \
"v_mfma_f32_16x16x32_bf16 a[208:211], a[40:43], a[96:99], a[208:211]\n" \
"ds_read_b64_tr_b16 a[90:91], v99 offset:4608\n" \
"v_mfma_f32_16x16x32_bf16 a[208:211], a[44:47], a[100:103], a[208:211]\n" \
"v_mfma_f32_16x16x32_bf16 a[224:227], a[48:51], a[96:99], a[224:227]\n" \
"ds_read_b128 a[40:43], v96 offset:43008\n" \
"v_mfma_f32_16x16x32_bf16 a[224:227], a[52:55], a[100:103], a[224:227]\n" \
"v_mfma_f32_16x16x32_bf16 a[228:231], a[48:51], a[104:107], a[228:231]\n" \
"ds_read_b64_tr_b16 a[92:93], v99 offset:16896\n" \
"v_mfma_f32_16x16x32_bf16 a[228:231], a[52:55], a[108:111], a[228:231]\n" \
"v_mfma_f32_16x16x32_bf16 a[232:235], a[48:51], a[112:115], a[232:235]\n" \
"ds_read_b128 a[44:47], v97 offset:43008\n" \
"v_mfma_f32_16x16x32_bf16 a[232:235], a[52:55], a[116:119], a[232:235]\n" \
"v_mfma_f32_16x16x32_bf16 a[236:239], a[48:51], a[120:123], a[236:239]\n" \
"ds_read_b64_tr_b16 a[94:95], v99 offset:20992\n" \
"v_mfma_f32_16x16x32_bf16 a[236:239], a[52:55], a[124:127], a[236:239]\n" \
"v_mfma_f32_16x16x32_bf16 a[252:255], a[56:59], a[120:123], a[252:255]\n" \
"ds_read_b128 a[48:51], v96 offset:45056\n" \
"v_mfma_f32_16x16x32_bf16 a[252:255], a[60:63], a[124:127], a[252:255]\n" \
"v_mfma_f32_16x16x32_bf16 a[248:251], a[56:59], a[112:115], a[248:251]\n" \
"v_mfma_f32_16x16x32_bf16 a[248:251], a[60:63], a[116:119], a[248:251]\n" \
"v_mfma_f32_16x16x32_bf16 a[244:247], a[56:59], a[104:107], a[244:247]\n" \
"ds_read_b128 a[52:55], v97 offset:45056\n" \
"v_mfma_f32_16x16x32_bf16 a[244:247], a[60:63], a[108:111], a[244:247]\n" \
"v_mfma_f32_16x16x32_bf16 a[240:243], a[56:59], a[96:99], a[240:243]\n" \
"v_mfma_f32_16x16x32_bf16 a[240:243], a[60:63], a[100:103], a[240:243]\n" \
"s_waitcnt lgkmcnt(1)\n" \
"v_mfma_f32_16x16x32_bf16 v[0:3], a[64:67], a[0:3], v[0:3]\n" \
"ds_read_b128 a[56:59], v96 offset:47104\n" \
"v_mfma_f32_16x16x32_bf16 v[0:3], a[68:71], a[4:7], v[0:3]\n" \
"v_mfma_f32_16x16x32_bf16 v[4:7], a[72:75], a[0:3], v[4:7]\n" \
"s_add_u32 m0, s74, 131072\n" \
"ds_read_b128 a[60:63], v97 offset:47104\n" \
"v_mfma_f32_16x16x32_bf16 v[4:7], a[76:79], a[4:7], v[4:7]\n" \
"buffer_load_dwordx4 v68, s[40:43], s83 offen offset:0 lds\n" \
"v_mfma_f32_16x16x32_bf16 v[8:11], a[80:83], a[0:3], v[8:11]\n" \
"ds_read_b64_tr_b16 a[96:97], v98 offset:1024\n" \
"v_mfma_f32_16x16x32_bf16 v[8:11], a[84:87], a[4:7], v[8:11]\n" \
"v_mfma_f32_16x16x32_bf16 v[12:15], a[88:91], a[0:3], v[12:15]\n" \
"ds_read_b64_tr_b16 a[98:99], v98 offset:5120\n" \
"v_mfma_f32_16x16x32_bf16 v[12:15], a[92:95], a[4:7], v[12:15]\n" \
"buffer_load_dwordx4 v69, s[44:47], s83 offen offset:1024 lds\n" \
"v_mfma_f32_16x16x32_bf16 v[28:31], a[88:91], a[8:11], v[28:31]\n" \
"ds_read_b64_tr_b16 a[100:101], v98 offset:17408\n" \
"v_mfma_f32_16x16x32_bf16 v[28:31], a[92:95], a[12:15], v[28:31]\n" \
"v_mfma_f32_16x16x32_bf16 v[24:27], a[80:83], a[8:11], v[24:27]\n" \
"ds_read_b64_tr_b16 a[102:103], v98 offset:21504\n" \
"v_mfma_f32_16x16x32_bf16 v[24:27], a[84:87], a[12:15], v[24:27]\n" \
"buffer_load_dwordx4 v70, s[40:43], s83 offen offset:2048 lds\n" \
"v_mfma_f32_16x16x32_bf16 v[20:23], a[72:75], a[8:11], v[20:23]\n" \
"ds_read_b64_tr_b16 a[104:105], v99 offset:1024\n" \
"v_mfma_f32_16x16x32_bf16 v[20:23], a[76:79], a[12:15], v[20:23]\n" \
"v_mfma_f32_16x16x32_bf16 v[16:19], a[64:67], a[8:11], v[16:19]\n" \
"ds_read_b64_tr_b16 a[106:107], v99 offset:5120\n" \
"v_mfma_f32_16x16x32_bf16 v[16:19], a[68:71], a[12:15], v[16:19]\n" \
"buffer_load_dwordx4 v71, s[44:47], s83 offen offset:3072 lds\n" \
"v_mfma_f32_16x16x32_bf16 v[32:35], a[64:67], a[16:19], v[32:35]\n" \
"ds_read_b64_tr_b16 a[108:109], v99 offset:17408\n" \
"v_mfma_f32_16x16x32_bf16 v[32:35], a[68:71], a[20:23], v[32:35]\n" \
"s_xor_b32 s83, s83, 0x80000\n" \
"v_mfma_f32_16x16x32_bf16 v[36:39], a[72:75], a[16:19], v[36:39]\n" \
"s_add_u32 m0, s74, 135168\n" \
"ds_read_b64_tr_b16 a[110:111], v99 offset:21504\n" \
"v_mfma_f32_16x16x32_bf16 v[36:39], a[76:79], a[20:23], v[36:39]\n" \
"buffer_load_dwordx4 v68, s[40:43], s84 offen offset:0 lds\n" \
"v_mfma_f32_16x16x32_bf16 v[40:43], a[80:83], a[16:19], v[40:43]\n" \
"ds_read_b64_tr_b16 a[112:113], v98 offset:1536\n" \
"v_mfma_f32_16x16x32_bf16 v[40:43], a[84:87], a[20:23], v[40:43]\n" \
"v_mfma_f32_16x16x32_bf16 v[44:47], a[88:91], a[16:19], v[44:47]\n" \
"ds_read_b64_tr_b16 a[114:115], v98 offset:5632\n" \
"v_mfma_f32_16x16x32_bf16 v[44:47], a[92:95], a[20:23], v[44:47]\n" \
"buffer_load_dwordx4 v69, s[44:47], s84 offen offset:1024 lds\n" \
"v_mfma_f32_16x16x32_bf16 v[60:63], a[88:91], a[24:27], v[60:63]\n" \
"ds_read_b64_tr_b16 a[116:117], v98 offset:17920\n" \
"v_mfma_f32_16x16x32_bf16 v[60:63], a[92:95], a[28:31], v[60:63]\n" \
"v_mfma_f32_16x16x32_bf16 v[56:59], a[80:83], a[24:27], v[56:59]\n" \
"ds_read_b64_tr_b16 a[118:119], v98 offset:22016\n" \
"v_mfma_f32_16x16x32_bf16 v[56:59], a[84:87], a[28:31], v[56:59]\n" \
"buffer_load_dwordx4 v70, s[40:43], s84 offen offset:2048 lds\n" \
"v_mfma_f32_16x16x32_bf16 v[52:55], a[72:75], a[24:27], v[52:55]\n" \
"ds_read_b64_tr_b16 a[120:121], v99 offset:1536\n" \
"v_mfma_f32_16x16x32_bf16 v[52:55], a[76:79], a[28:31], v[52:55]\n" \
"v_mfma_f32_16x16x32_bf16 v[48:51], a[64:67], a[24:27], v[48:51]\n" \
"ds_read_b64_tr_b16 a[122:123], v99 offset:5632\n" \
"v_mfma_f32_16x16x32_bf16 v[48:51], a[68:71], a[28:31], v[48:51]\n" \
"buffer_load_dwordx4 v71, s[44:47], s84 offen offset:3072 lds\n" \
"v_mfma_f32_16x16x32_bf16 v[144:147], a[64:67], a[32:35], v[144:147]\n" \
"ds_read_b64_tr_b16 a[124:125], v99 offset:17920\n" \
"v_mfma_f32_16x16x32_bf16 v[144:147], a[68:71], a[36:39], v[144:147]\n" \
"v_mfma_f32_16x16x32_bf16 v[148:151], a[72:75], a[32:35], v[148:151]\n" \
"s_xor_b32 s84, s84, 0x80000\n" \
"ds_read_b64_tr_b16 a[126:127], v99 offset:22016\n" \
"v_mfma_f32_16x16x32_bf16 v[148:151], a[76:79], a[36:39], v[148:151]\n" \
"v_mfma_f32_16x16x32_bf16 v[152:155], a[80:83], a[32:35], v[152:155]\n" \
"v_mfma_f32_16x16x32_bf16 v[152:155], a[84:87], a[36:39], v[152:155]\n" \
"v_mfma_f32_16x16x32_bf16 v[156:159], a[88:91], a[32:35], v[156:159]\n" \
"v_mfma_f32_16x16x32_bf16 v[156:159], a[92:95], a[36:39], v[156:159]\n" \
"v_mfma_f32_16x16x32_bf16 v[188:191], a[88:91], a[40:43], v[188:191]\n" \
"v_mfma_f32_16x16x32_bf16 v[188:191], a[92:95], a[44:47], v[188:191]\n" \
"v_mfma_f32_16x16x32_bf16 v[184:187], a[80:83], a[40:43], v[184:187]\n" \
"v_mfma_f32_16x16x32_bf16 v[184:187], a[84:87], a[44:47], v[184:187]\n" \
"v_mfma_f32_16x16x32_bf16 v[180:183], a[72:75], a[40:43], v[180:183]\n" \
"v_mfma_f32_16x16x32_bf16 v[180:183], a[76:79], a[44:47], v[180:183]\n" \
"v_mfma_f32_16x16x32_bf16 v[176:179], a[64:67], a[40:43], v[176:179]\n" \
"v_mfma_f32_16x16x32_bf16 v[176:179], a[68:71], a[44:47], v[176:179]\n" \
"s_waitcnt lgkmcnt(0)\n" \
"v_mfma_f32_16x16x32_bf16 v[192:195], a[64:67], a[48:51], v[192:195]\n" \
"v_mfma_f32_16x16x32_bf16 v[192:195], a[68:71], a[52:55], v[192:195]\n" \
"v_mfma_f32_16x16x32_bf16 v[196:199], a[72:75], a[48:51], v[196:199]\n" \
"v_mfma_f32_16x16x32_bf16 v[196:199], a[76:79], a[52:55], v[196:199]\n" \
"v_mfma_f32_16x16x32_bf16 v[200:203], a[80:83], a[48:51], v[200:203]\n" \
"v_mfma_f32_16x16x32_bf16 v[200:203], a[84:87], a[52:55], v[200:203]\n" \
"v_mfma_f32_16x16x32_bf16 v[204:207], a[88:91], a[48:51], v[204:207]\n" \
"v_mfma_f32_16x16x32_bf16 v[204:207], a[92:95], a[52:55], v[204:207]\n" \
"v_mfma_f32_16x16x32_bf16 v[220:223], a[88:91], a[56:59], v[220:223]\n" \
"v_mfma_f32_16x16x32_bf16 v[220:223], a[92:95], a[60:63], v[220:223]\n" \
"v_mfma_f32_16x16x32_bf16 v[216:219], a[80:83], a[56:59], v[216:219]\n" \
"v_mfma_f32_16x16x32_bf16 v[216:219], a[84:87], a[60:63], v[216:219]\n" \
"v_mfma_f32_16x16x32_bf16 v[212:215], a[72:75], a[56:59], v[212:215]\n" \
"v_mfma_f32_16x16x32_bf16 v[212:215], a[76:79], a[60:63], v[212:215]\n" \
"v_mfma_f32_16x16x32_bf16 v[208:211], a[64:67], a[56:59], v[208:211]\n" \
"v_mfma_f32_16x16x32_bf16 v[208:211], a[68:71], a[60:63], v[208:211]\n" \
"s_waitcnt vmcnt(11) lgkmcnt(0)\n" \
"s_barrier\n" \
"v_mfma_f32_16x16x32_bf16 a[128:131], a[0:3], a[96:99], a[128:131]\n" \
"s_add_u32 m0, s74, 65536\n" \
"ds_read_b64_tr_b16 a[64:65], v74 offset:32768\n" \
"v_mfma_f32_16x16x32_bf16 a[128:131], a[4:7], a[100:103], a[128:131]\n" \
"buffer_load_dwordx4 v64, s[36:39], s81 offen offset:0 lds\n" \
"v_mfma_f32_16x16x32_bf16 a[132:135], a[0:3], a[104:107], a[132:135]\n" \
"ds_read_b64_tr_b16 a[66:67], v74 offset:36864\n" \
"v_mfma_f32_16x16x32_bf16 a[132:135], a[4:7], a[108:111], a[132:135]\n" \
"v_mfma_f32_16x16x32_bf16 a[136:139], a[0:3], a[112:115], a[136:139]\n" \
"ds_read_b64_tr_b16 a[68:69], v74 offset:49152\n" \
"v_mfma_f32_16x16x32_bf16 a[136:139], a[4:7], a[116:119], a[136:139]\n" \
"buffer_load_dwordx4 v65, s[36:39], s81 offen offset:1024 lds\n" \
"v_mfma_f32_16x16x32_bf16 a[140:143], a[0:3], a[120:123], a[140:143]\n" \
"ds_read_b64_tr_b16 a[70:71], v74 offset:53248\n" \
"v_mfma_f32_16x16x32_bf16 a[140:143], a[4:7], a[124:127], a[140:143]\n" \
"v_mfma_f32_16x16x32_bf16 a[156:159], a[8:11], a[120:123], a[156:159]\n" \
"ds_read_b128 a[0:3], v72 offset:0\n" \
"v_mfma_f32_16x16x32_bf16 a[156:159], a[12:15], a[124:127], a[156:159]\n" \
"buffer_load_dwordx4 v66, s[36:39], s81 offen offset:2048 lds\n" \
"v_mfma_f32_16x16x32_bf16 a[152:155], a[8:11], a[112:115], a[152:155]\n" \
"ds_read_b64_tr_b16 a[72:73], v75 offset:32768\n" \
"v_mfma_f32_16x16x32_bf16 a[152:155], a[12:15], a[116:119], a[152:155]\n" \
"v_mfma_f32_16x16x32_bf16 a[148:151], a[8:11], a[104:107], a[148:151]\n" \
"ds_read_b128 a[4:7], v73 offset:0\n" \
"v_mfma_f32_16x16x32_bf16 a[148:151], a[12:15], a[108:111], a[148:151]\n" \
"buffer_load_dwordx4 v67, s[36:39], s81 offen offset:3072 lds\n" \
"v_mfma_f32_16x16x32_bf16 a[144:147], a[8:11], a[96:99], a[144:147]\n" \
"ds_read_b64_tr_b16 a[74:75], v75 offset:36864\n" \
"v_mfma_f32_16x16x32_bf16 a[144:147], a[12:15], a[100:103], a[144:147]\n" \
"s_xor_b32 s81, s81, 0x80\n" \
"v_mfma_f32_16x16x32_bf16 a[160:163], a[16:19], a[96:99], a[160:163]\n" \
"s_add_u32 m0, s74, 69632\n" \
"ds_read_b128 a[8:11], v72 offset:2048\n" \
"v_mfma_f32_16x16x32_bf16 a[160:163], a[20:23], a[100:103], a[160:163]\n" \
"buffer_load_dwordx4 v64, s[36:39], s82 offen offset:0 lds\n" \
"v_mfma_f32_16x16x32_bf16 a[164:167], a[16:19], a[104:107], a[164:167]\n" \
"ds_read_b64_tr_b16 a[76:77], v75 offset:49152\n" \
"v_mfma_f32_16x16x32_bf16 a[164:167], a[20:23], a[108:111], a[164:167]\n" \
"v_mfma_f32_16x16x32_bf16 a[168:171], a[16:19], a[112:115], a[168:171]\n" \
"ds_read_b128 a[12:15], v73 offset:2048\n" \
"v_mfma_f32_16x16x32_bf16 a[168:171], a[20:23], a[116:119], a[168:171]\n" \
"buffer_load_dwordx4 v65, s[36:39], s82 offen offset:1024 lds\n" \
"v_mfma_f32_16x16x32_bf16 a[172:175], a[16:19], a[120:123], a[172:175]\n" \
"ds_read_b64_tr_b16 a[78:79], v75 offset:53248\n" \
"v_mfma_f32_16x16x32_bf16 a[172:175], a[20:23], a[124:127], a[172:175]\n" \
"v_mfma_f32_16x16x32_bf16 a[188:191], a[24:27], a[120:123], a[188:191]\n" \
"ds_read_b128 a[16:19], v72 offset:4096\n" \
"v_mfma_f32_16x16x32_bf16 a[188:191], a[28:31], a[124:127], a[188:191]\n" \
"buffer_load_dwordx4 v66, s[36:39], s82 offen offset:2048 lds\n" \
"v_mfma_f32_16x16x32_bf16 a[184:187], a[24:27], a[112:115], a[184:187]\n" \
"ds_read_b64_tr_b16 a[80:81], v74 offset:33280\n" \
"v_mfma_f32_16x16x32_bf16 a[184:187], a[28:31], a[116:119], a[184:187]\n" \
"v_mfma_f32_16x16x32_bf16 a[180:183], a[24:27], a[104:107], a[180:183]\n" \
"ds_read_b128 a[20:23], v73 offset:4096\n" \
"v_mfma_f32_16x16x32_bf16 a[180:183], a[28:31], a[108:111], a[180:183]\n" \
"buffer_load_dwordx4 v67, s[36:39], s82 offen offset:3072 lds\n" \
"v_mfma_f32_16x16x32_bf16 a[176:179], a[24:27], a[96:99], a[176:179]\n" \
"s_mov_b64 s[64:65], s[12:13]\n" \
"s_add_u32 s68, s67, 0x100000\n" \
"ds_read_b64_tr_b16 a[82:83], v74 offset:37376\n" \
"v_mfma_f32_16x16x32_bf16 a[176:179], a[28:31], a[100:103], a[176:179]\n" \
"s_add_u32 s64, s64, s68\n" \
"s_addc_u32 s65, s65, 0\n" \
"v_mfma_f32_16x16x32_bf16 a[192:195], a[32:35], a[96:99], a[192:195]\n" \
"s_xor_b32 s82, s82, 0x80\n" \
"ds_read_b128 a[24:27], v72 offset:6144\n" \
"v_mfma_f32_16x16x32_bf16 a[192:195], a[36:39], a[100:103], a[192:195]\n" \
"global_load_dword v238, v234, s[64:65]\n" \
"v_mfma_f32_16x16x32_bf16 a[196:199], a[32:35], a[104:107], a[196:199]\n" \
"s_mov_b64 s[64:65], s[12:13]\n" \
"s_add_u32 s68, s67, 0x500000\n" \
"ds_read_b64_tr_b16 a[84:85], v74 offset:49664\n" \
"v_mfma_f32_16x16x32_bf16 a[196:199], a[36:39], a[108:111], a[196:199]\n" \
"s_add_u32 s64, s64, s68\n" \
"s_addc_u32 s65, s65, 0\n" \
"v_mfma_f32_16x16x32_bf16 a[200:203], a[32:35], a[112:115], a[200:203]\n" \
"ds_read_b128 a[28:31], v73 offset:6144\n" \
"v_mfma_f32_16x16x32_bf16 a[200:203], a[36:39], a[116:119], a[200:203]\n" \
"global_load_dword v238, v234, s[64:65]\n" \
"v_mfma_f32_16x16x32_bf16 a[204:207], a[32:35], a[120:123], a[204:207]\n" \
"s_mov_b64 s[64:65], s[12:13]\n" \
"s_add_u32 s68, s67, 0x200000\n" \
"ds_read_b64_tr_b16 a[86:87], v74 offset:53760\n" \
"v_mfma_f32_16x16x32_bf16 a[204:207], a[36:39], a[124:127], a[204:207]\n" \
"s_add_u32 s64, s64, s68\n" \
"s_addc_u32 s65, s65, 0\n" \
"v_mfma_f32_16x16x32_bf16 a[220:223], a[40:43], a[120:123], a[220:223]\n" \
"ds_read_b128 a[32:35], v72 offset:8192\n" \
"v_mfma_f32_16x16x32_bf16 a[220:223], a[44:47], a[124:127], a[220:223]\n" \
"global_load_dword v238, v234, s[64:65]\n" \
"v_mfma_f32_16x16x32_bf16 a[216:219], a[40:43], a[112:115], a[216:219]\n" \
"ds_read_b64_tr_b16 a[88:89], v75 offset:33280\n" \
"v_mfma_f32_16x16x32_bf16 a[216:219], a[44:47], a[116:119], a[216:219]\n" \
"v_mfma_f32_16x16x32_bf16 a[212:215], a[40:43], a[104:107], a[212:215]\n" \
"ds_read_b128 a[36:39], v73 offset:8192\n" \
"v_mfma_f32_16x16x32_bf16 a[212:215], a[44:47], a[108:111], a[212:215]\n" \
"v_mfma_f32_16x16x32_bf16 a[208:211], a[40:43], a[96:99], a[208:211]\n" \
"ds_read_b64_tr_b16 a[90:91], v75 offset:37376\n" \
"v_mfma_f32_16x16x32_bf16 a[208:211], a[44:47], a[100:103], a[208:211]\n" \
"v_mfma_f32_16x16x32_bf16 a[224:227], a[48:51], a[96:99], a[224:227]\n" \
"ds_read_b128 a[40:43], v72 offset:10240\n" \
"v_mfma_f32_16x16x32_bf16 a[224:227], a[52:55], a[100:103], a[224:227]\n" \
"v_mfma_f32_16x16x32_bf16 a[228:231], a[48:51], a[104:107], a[228:231]\n" \
"ds_read_b64_tr_b16 a[92:93], v75 offset:49664\n" \
"v_mfma_f32_16x16x32_bf16 a[228:231], a[52:55], a[108:111], a[228:231]\n" \
"v_mfma_f32_16x16x32_bf16 a[232:235], a[48:51], a[112:115], a[232:235]\n" \
"ds_read_b128 a[44:47], v73 offset:10240\n" \
"v_mfma_f32_16x16x32_bf16 a[232:235], a[52:55], a[116:119], a[232:235]\n" \
"v_mfma_f32_16x16x32_bf16 a[236:239], a[48:51], a[120:123], a[236:239]\n" \
"ds_read_b64_tr_b16 a[94:95], v75 offset:53760\n" \
"v_mfma_f32_16x16x32_bf16 a[236:239], a[52:55], a[124:127], a[236:239]\n" \
"v_mfma_f32_16x16x32_bf16 a[252:255], a[56:59], a[120:123], a[252:255]\n" \
"ds_read_b128 a[48:51], v72 offset:12288\n" \
"v_mfma_f32_16x16x32_bf16 a[252:255], a[60:63], a[124:127], a[252:255]\n" \
"v_mfma_f32_16x16x32_bf16 a[248:251], a[56:59], a[112:115], a[248:251]\n" \
"v_mfma_f32_16x16x32_bf16 a[248:251], a[60:63], a[116:119], a[248:251]\n" \
"v_mfma_f32_16x16x32_bf16 a[244:247], a[56:59], a[104:107], a[244:247]\n" \
"ds_read_b128 a[52:55], v73 offset:12288\n" \
"v_mfma_f32_16x16x32_bf16 a[244:247], a[60:63], a[108:111], a[244:247]\n" \
"v_mfma_f32_16x16x32_bf16 a[240:243], a[56:59], a[96:99], a[240:243]\n" \
"v_mfma_f32_16x16x32_bf16 a[240:243], a[60:63], a[100:103], a[240:243]\n" \
"s_waitcnt lgkmcnt(1)\n" \
"v_mfma_f32_16x16x32_bf16 v[0:3], a[64:67], a[0:3], v[0:3]\n" \
"ds_read_b128 a[56:59], v72 offset:14336\n" \
"v_mfma_f32_16x16x32_bf16 v[0:3], a[68:71], a[4:7], v[0:3]\n" \
"v_mfma_f32_16x16x32_bf16 v[4:7], a[72:75], a[0:3], v[4:7]\n" \
"s_add_u32 m0, s74, 98304\n" \
"ds_read_b128 a[60:63], v73 offset:14336\n" \
"v_mfma_f32_16x16x32_bf16 v[4:7], a[76:79], a[4:7], v[4:7]\n" \
"buffer_load_dwordx4 v68, s[40:43], s83 offen offset:0 lds\n" \
"v_mfma_f32_16x16x32_bf16 v[8:11], a[80:83], a[0:3], v[8:11]\n" \
"ds_read_b64_tr_b16 a[96:97], v74 offset:33792\n" \
"v_mfma_f32_16x16x32_bf16 v[8:11], a[84:87], a[4:7], v[8:11]\n" \
"v_mfma_f32_16x16x32_bf16 v[12:15], a[88:91], a[0:3], v[12:15]\n" \
"ds_read_b64_tr_b16 a[98:99], v74 offset:37888\n" \
"v_mfma_f32_16x16x32_bf16 v[12:15], a[92:95], a[4:7], v[12:15]\n" \
"buffer_load_dwordx4 v69, s[44:47], s83 offen offset:1024 lds\n" \
"v_mfma_f32_16x16x32_bf16 v[28:31], a[88:91], a[8:11], v[28:31]\n" \
"ds_read_b64_tr_b16 a[100:101], v74 offset:50176\n" \
"v_mfma_f32_16x16x32_bf16 v[28:31], a[92:95], a[12:15], v[28:31]\n" \
"v_mfma_f32_16x16x32_bf16 v[24:27], a[80:83], a[8:11], v[24:27]\n" \
"ds_read_b64_tr_b16 a[102:103], v74 offset:54272\n" \
"v_mfma_f32_16x16x32_bf16 v[24:27], a[84:87], a[12:15], v[24:27]\n" \
"buffer_load_dwordx4 v70, s[40:43], s83 offen offset:2048 lds\n" \
"v_mfma_f32_16x16x32_bf16 v[20:23], a[72:75], a[8:11], v[20:23]\n" \
"ds_read_b64_tr_b16 a[104:105], v75 offset:33792\n" \
"v_mfma_f32_16x16x32_bf16 v[20:23], a[76:79], a[12:15], v[20:23]\n" \
"v_mfma_f32_16x16x32_bf16 v[16:19], a[64:67], a[8:11], v[16:19]\n" \
"ds_read_b64_tr_b16 a[106:107], v75 offset:37888\n" \
"v_mfma_f32_16x16x32_bf16 v[16:19], a[68:71], a[12:15], v[16:19]\n" \
"buffer_load_dwordx4 v71, s[44:47], s83 offen offset:3072 lds\n" \
"v_mfma_f32_16x16x32_bf16 v[32:35], a[64:67], a[16:19], v[32:35]\n" \
"ds_read_b64_tr_b16 a[108:109], v75 offset:50176\n" \
"v_mfma_f32_16x16x32_bf16 v[32:35], a[68:71], a[20:23], v[32:35]\n" \
"v_mfma_f32_16x16x32_bf16 v[36:39], a[72:75], a[16:19], v[36:39]\n" \
"s_add_u32 m0, s74, 102400\n" \
"ds_read_b64_tr_b16 a[110:111], v75 offset:54272\n" \
"v_mfma_f32_16x16x32_bf16 v[36:39], a[76:79], a[20:23], v[36:39]\n" \
"buffer_load_dwordx4 v68, s[40:43], s84 offen offset:0 lds\n" \
"v_mfma_f32_16x16x32_bf16 v[40:43], a[80:83], a[16:19], v[40:43]\n" \
"ds_read_b64_tr_b16 a[112:113], v74 offset:34304\n" \
"v_mfma_f32_16x16x32_bf16 v[40:43], a[84:87], a[20:23], v[40:43]\n" \
"v_mfma_f32_16x16x32_bf16 v[44:47], a[88:91], a[16:19], v[44:47]\n" \
"ds_read_b64_tr_b16 a[114:115], v74 offset:38400\n" \
"v_mfma_f32_16x16x32_bf16 v[44:47], a[92:95], a[20:23], v[44:47]\n" \
"buffer_load_dwordx4 v69, s[44:47], s84 offen offset:1024 lds\n" \
"v_mfma_f32_16x16x32_bf16 v[60:63], a[88:91], a[24:27], v[60:63]\n" \
"ds_read_b64_tr_b16 a[116:117], v74 offset:50688\n" \
"v_mfma_f32_16x16x32_bf16 v[60:63], a[92:95], a[28:31], v[60:63]\n" \
"v_mfma_f32_16x16x32_bf16 v[56:59], a[80:83], a[24:27], v[56:59]\n" \
"ds_read_b64_tr_b16 a[118:119], v74 offset:54784\n" \
"v_mfma_f32_16x16x32_bf16 v[56:59], a[84:87], a[28:31], v[56:59]\n" \
"buffer_load_dwordx4 v70, s[40:43], s84 offen offset:2048 lds\n" \
"v_mfma_f32_16x16x32_bf16 v[52:55], a[72:75], a[24:27], v[52:55]\n" \
"ds_read_b64_tr_b16 a[120:121], v75 offset:34304\n" \
"v_mfma_f32_16x16x32_bf16 v[52:55], a[76:79], a[28:31], v[52:55]\n" \
"v_mfma_f32_16x16x32_bf16 v[48:51], a[64:67], a[24:27], v[48:51]\n" \
"ds_read_b64_tr_b16 a[122:123], v75 offset:38400\n" \
"v_mfma_f32_16x16x32_bf16 v[48:51], a[68:71], a[28:31], v[48:51]\n" \
"buffer_load_dwordx4 v71, s[44:47], s84 offen offset:3072 lds\n" \
"v_mfma_f32_16x16x32_bf16 v[144:147], a[64:67], a[32:35], v[144:147]\n" \
"ds_read_b64_tr_b16 a[124:125], v75 offset:50688\n" \
"v_mfma_f32_16x16x32_bf16 v[144:147], a[68:71], a[36:39], v[144:147]\n" \
"v_mfma_f32_16x16x32_bf16 v[148:151], a[72:75], a[32:35], v[148:151]\n" \
"ds_read_b64_tr_b16 a[126:127], v75 offset:54784\n" \
"v_mfma_f32_16x16x32_bf16 v[148:151], a[76:79], a[36:39], v[148:151]\n" \
"v_mfma_f32_16x16x32_bf16 v[152:155], a[80:83], a[32:35], v[152:155]\n" \
"v_mfma_f32_16x16x32_bf16 v[152:155], a[84:87], a[36:39], v[152:155]\n" \
"v_mfma_f32_16x16x32_bf16 v[156:159], a[88:91], a[32:35], v[156:159]\n" \
"v_mfma_f32_16x16x32_bf16 v[156:159], a[92:95], a[36:39], v[156:159]\n" \
"v_mfma_f32_16x16x32_bf16 v[188:191], a[88:91], a[40:43], v[188:191]\n" \
"v_mfma_f32_16x16x32_bf16 v[188:191], a[92:95], a[44:47], v[188:191]\n" \
"v_mfma_f32_16x16x32_bf16 v[184:187], a[80:83], a[40:43], v[184:187]\n" \
"v_mfma_f32_16x16x32_bf16 v[184:187], a[84:87], a[44:47], v[184:187]\n" \
"v_mfma_f32_16x16x32_bf16 v[180:183], a[72:75], a[40:43], v[180:183]\n" \
"v_mfma_f32_16x16x32_bf16 v[180:183], a[76:79], a[44:47], v[180:183]\n" \
"v_mfma_f32_16x16x32_bf16 v[176:179], a[64:67], a[40:43], v[176:179]\n" \
"v_mfma_f32_16x16x32_bf16 v[176:179], a[68:71], a[44:47], v[176:179]\n" \
"s_waitcnt lgkmcnt(0)\n" \
"v_mfma_f32_16x16x32_bf16 v[192:195], a[64:67], a[48:51], v[192:195]\n" \
"v_mfma_f32_16x16x32_bf16 v[192:195], a[68:71], a[52:55], v[192:195]\n" \
"v_mfma_f32_16x16x32_bf16 v[196:199], a[72:75], a[48:51], v[196:199]\n" \
"v_mfma_f32_16x16x32_bf16 v[196:199], a[76:79], a[52:55], v[196:199]\n" \
"v_mfma_f32_16x16x32_bf16 v[200:203], a[80:83], a[48:51], v[200:203]\n" \
"v_mfma_f32_16x16x32_bf16 v[200:203], a[84:87], a[52:55], v[200:203]\n" \
"v_mfma_f32_16x16x32_bf16 v[204:207], a[88:91], a[48:51], v[204:207]\n" \
"v_mfma_f32_16x16x32_bf16 v[204:207], a[92:95], a[52:55], v[204:207]\n" \
"v_mfma_f32_16x16x32_bf16 v[220:223], a[88:91], a[56:59], v[220:223]\n" \
"v_mfma_f32_16x16x32_bf16 v[220:223], a[92:95], a[60:63], v[220:223]\n" \
"v_mfma_f32_16x16x32_bf16 v[216:219], a[80:83], a[56:59], v[216:219]\n" \
"v_mfma_f32_16x16x32_bf16 v[216:219], a[84:87], a[60:63], v[216:219]\n" \
"v_mfma_f32_16x16x32_bf16 v[212:215], a[72:75], a[56:59], v[212:215]\n" \
"v_mfma_f32_16x16x32_bf16 v[212:215], a[76:79], a[60:63], v[212:215]\n" \
"v_mfma_f32_16x16x32_bf16 v[208:211], a[64:67], a[56:59], v[208:211]\n" \
"v_mfma_f32_16x16x32_bf16 v[208:211], a[68:71], a[60:63], v[208:211]\n" \
"s_waitcnt vmcnt(11) lgkmcnt(0)\n" \
"s_barrier\n" \
"v_mfma_f32_16x16x32_bf16 a[128:131], a[0:3], a[96:99], a[128:131]\n" \
"s_add_u32 m0, s74, 32768\n" \
"ds_read_b64_tr_b16 a[64:65], v102 offset:0\n" \
"v_mfma_f32_16x16x32_bf16 a[128:131], a[4:7], a[100:103], a[128:131]\n" \
"buffer_load_dwordx4 v64, s[36:39], s81 offen offset:0 lds\n" \
"v_mfma_f32_16x16x32_bf16 a[132:135], a[0:3], a[104:107], a[132:135]\n" \
"ds_read_b64_tr_b16 a[66:67], v102 offset:4096\n" \
"v_mfma_f32_16x16x32_bf16 a[132:135], a[4:7], a[108:111], a[132:135]\n" \
"v_mfma_f32_16x16x32_bf16 a[136:139], a[0:3], a[112:115], a[136:139]\n" \
"ds_read_b64_tr_b16 a[68:69], v102 offset:16384\n" \
"v_mfma_f32_16x16x32_bf16 a[136:139], a[4:7], a[116:119], a[136:139]\n" \
"buffer_load_dwordx4 v65, s[36:39], s81 offen offset:1024 lds\n" \
"v_mfma_f32_16x16x32_bf16 a[140:143], a[0:3], a[120:123], a[140:143]\n" \
"ds_read_b64_tr_b16 a[70:71], v102 offset:20480\n" \
"v_mfma_f32_16x16x32_bf16 a[140:143], a[4:7], a[124:127], a[140:143]\n" \
"v_mfma_f32_16x16x32_bf16 a[156:159], a[8:11], a[120:123], a[156:159]\n" \
"ds_read_b128 a[0:3], v96 offset:0\n" \
"v_mfma_f32_16x16x32_bf16 a[156:159], a[12:15], a[124:127], a[156:159]\n" \
"buffer_load_dwordx4 v66, s[36:39], s81 offen offset:2048 lds\n" \
"v_mfma_f32_16x16x32_bf16 a[152:155], a[8:11], a[112:115], a[152:155]\n" \
"ds_read_b64_tr_b16 a[72:73], v103 offset:0\n" \
"v_mfma_f32_16x16x32_bf16 a[152:155], a[12:15], a[116:119], a[152:155]\n" \
"v_mfma_f32_16x16x32_bf16 a[148:151], a[8:11], a[104:107], a[148:151]\n" \
"ds_read_b128 a[4:7], v97 offset:0\n" \
"v_mfma_f32_16x16x32_bf16 a[148:151], a[12:15], a[108:111], a[148:151]\n" \
"buffer_load_dwordx4 v67, s[36:39], s81 offen offset:3072 lds\n" \
"v_mfma_f32_16x16x32_bf16 a[144:147], a[8:11], a[96:99], a[144:147]\n" \
"ds_read_b64_tr_b16 a[74:75], v103 offset:4096\n" \
"v_mfma_f32_16x16x32_bf16 a[144:147], a[12:15], a[100:103], a[144:147]\n" \
"v_mfma_f32_16x16x32_bf16 a[160:163], a[16:19], a[96:99], a[160:163]\n" \
"s_add_u32 m0, s74, 36864\n" \
"ds_read_b128 a[8:11], v96 offset:2048\n" \
"v_mfma_f32_16x16x32_bf16 a[160:163], a[20:23], a[100:103], a[160:163]\n" \
"buffer_load_dwordx4 v64, s[36:39], s82 offen offset:0 lds\n" \
"v_mfma_f32_16x16x32_bf16 a[164:167], a[16:19], a[104:107], a[164:167]\n" \
"ds_read_b64_tr_b16 a[76:77], v103 offset:16384\n" \
"v_mfma_f32_16x16x32_bf16 a[164:167], a[20:23], a[108:111], a[164:167]\n" \
"v_mfma_f32_16x16x32_bf16 a[168:171], a[16:19], a[112:115], a[168:171]\n" \
"ds_read_b128 a[12:15], v97 offset:2048\n" \
"v_mfma_f32_16x16x32_bf16 a[168:171], a[20:23], a[116:119], a[168:171]\n" \
"buffer_load_dwordx4 v65, s[36:39], s82 offen offset:1024 lds\n" \
"v_mfma_f32_16x16x32_bf16 a[172:175], a[16:19], a[120:123], a[172:175]\n" \
"ds_read_b64_tr_b16 a[78:79], v103 offset:20480\n" \
"v_mfma_f32_16x16x32_bf16 a[172:175], a[20:23], a[124:127], a[172:175]\n" \
"v_mfma_f32_16x16x32_bf16 a[188:191], a[24:27], a[120:123], a[188:191]\n" \
"ds_read_b128 a[16:19], v96 offset:4096\n" \
"v_mfma_f32_16x16x32_bf16 a[188:191], a[28:31], a[124:127], a[188:191]\n" \
"buffer_load_dwordx4 v66, s[36:39], s82 offen offset:2048 lds\n" \
"v_mfma_f32_16x16x32_bf16 a[184:187], a[24:27], a[112:115], a[184:187]\n" \
"ds_read_b64_tr_b16 a[80:81], v102 offset:512\n" \
"v_mfma_f32_16x16x32_bf16 a[184:187], a[28:31], a[116:119], a[184:187]\n" \
"v_mfma_f32_16x16x32_bf16 a[180:183], a[24:27], a[104:107], a[180:183]\n" \
"ds_read_b128 a[20:23], v97 offset:4096\n" \
"v_mfma_f32_16x16x32_bf16 a[180:183], a[28:31], a[108:111], a[180:183]\n" \
"buffer_load_dwordx4 v67, s[36:39], s82 offen offset:3072 lds\n" \
"v_mfma_f32_16x16x32_bf16 a[176:179], a[24:27], a[96:99], a[176:179]\n" \
"s_mov_b64 s[64:65], s[12:13]\n" \
"s_add_u32 s68, s67, 0x300000\n" \
"ds_read_b64_tr_b16 a[82:83], v102 offset:4608\n" \
"v_mfma_f32_16x16x32_bf16 a[176:179], a[28:31], a[100:103], a[176:179]\n" \
"s_add_u32 s64, s64, s68\n" \
"s_addc_u32 s65, s65, 0\n" \
"v_mfma_f32_16x16x32_bf16 a[192:195], a[32:35], a[96:99], a[192:195]\n" \
"ds_read_b128 a[24:27], v96 offset:6144\n" \
"v_mfma_f32_16x16x32_bf16 a[192:195], a[36:39], a[100:103], a[192:195]\n" \
"global_load_dword v238, v234, s[64:65]\n" \
"v_mfma_f32_16x16x32_bf16 a[196:199], a[32:35], a[104:107], a[196:199]\n" \
"s_mov_b64 s[64:65], s[12:13]\n" \
"s_add_u32 s68, s67, 0x400000\n" \
"ds_read_b64_tr_b16 a[84:85], v102 offset:16896\n" \
"v_mfma_f32_16x16x32_bf16 a[196:199], a[36:39], a[108:111], a[196:199]\n" \
"s_add_u32 s64, s64, s68\n" \
"s_addc_u32 s65, s65, 0\n" \
"v_mfma_f32_16x16x32_bf16 a[200:203], a[32:35], a[112:115], a[200:203]\n" \
"ds_read_b128 a[28:31], v97 offset:6144\n" \
"v_mfma_f32_16x16x32_bf16 a[200:203], a[36:39], a[116:119], a[200:203]\n" \
"global_load_dword v238, v234, s[64:65]\n" \
"v_mfma_f32_16x16x32_bf16 a[204:207], a[32:35], a[120:123], a[204:207]\n" \
"s_mov_b64 s[64:65], s[12:13]\n" \
"s_add_u32 s68, s67, 0x600000\n" \
"ds_read_b64_tr_b16 a[86:87], v102 offset:20992\n" \
"v_mfma_f32_16x16x32_bf16 a[204:207], a[36:39], a[124:127], a[204:207]\n" \
"s_add_u32 s64, s64, s68\n" \
"s_addc_u32 s65, s65, 0\n" \
"v_mfma_f32_16x16x32_bf16 a[220:223], a[40:43], a[120:123], a[220:223]\n" \
"ds_read_b128 a[32:35], v96 offset:8192\n" \
"v_mfma_f32_16x16x32_bf16 a[220:223], a[44:47], a[124:127], a[220:223]\n" \
"global_load_dword v238, v234, s[64:65]\n" \
"v_mfma_f32_16x16x32_bf16 a[216:219], a[40:43], a[112:115], a[216:219]\n" \
"ds_read_b64_tr_b16 a[88:89], v103 offset:512\n" \
"v_mfma_f32_16x16x32_bf16 a[216:219], a[44:47], a[116:119], a[216:219]\n" \
"v_mfma_f32_16x16x32_bf16 a[212:215], a[40:43], a[104:107], a[212:215]\n" \
"ds_read_b128 a[36:39], v97 offset:8192\n" \
"v_mfma_f32_16x16x32_bf16 a[212:215], a[44:47], a[108:111], a[212:215]\n" \
"v_mfma_f32_16x16x32_bf16 a[208:211], a[40:43], a[96:99], a[208:211]\n" \
"ds_read_b64_tr_b16 a[90:91], v103 offset:4608\n" \
"v_mfma_f32_16x16x32_bf16 a[208:211], a[44:47], a[100:103], a[208:211]\n" \
"v_mfma_f32_16x16x32_bf16 a[224:227], a[48:51], a[96:99], a[224:227]\n" \
"ds_read_b128 a[40:43], v96 offset:10240\n" \
"v_mfma_f32_16x16x32_bf16 a[224:227], a[52:55], a[100:103], a[224:227]\n" \
"v_mfma_f32_16x16x32_bf16 a[228:231], a[48:51], a[104:107], a[228:231]\n" \
"ds_read_b64_tr_b16 a[92:93], v103 offset:16896\n" \
"v_mfma_f32_16x16x32_bf16 a[228:231], a[52:55], a[108:111], a[228:231]\n" \
"v_mfma_f32_16x16x32_bf16 a[232:235], a[48:51], a[112:115], a[232:235]\n" \
"ds_read_b128 a[44:47], v97 offset:10240\n" \
"v_mfma_f32_16x16x32_bf16 a[232:235], a[52:55], a[116:119], a[232:235]\n" \
"v_mfma_f32_16x16x32_bf16 a[236:239], a[48:51], a[120:123], a[236:239]\n" \
"ds_read_b64_tr_b16 a[94:95], v103 offset:20992\n" \
"v_mfma_f32_16x16x32_bf16 a[236:239], a[52:55], a[124:127], a[236:239]\n" \
"v_mfma_f32_16x16x32_bf16 a[252:255], a[56:59], a[120:123], a[252:255]\n" \
"ds_read_b128 a[48:51], v96 offset:12288\n" \
"v_mfma_f32_16x16x32_bf16 a[252:255], a[60:63], a[124:127], a[252:255]\n" \
"v_mfma_f32_16x16x32_bf16 a[248:251], a[56:59], a[112:115], a[248:251]\n" \
"v_mfma_f32_16x16x32_bf16 a[248:251], a[60:63], a[116:119], a[248:251]\n" \
"v_mfma_f32_16x16x32_bf16 a[244:247], a[56:59], a[104:107], a[244:247]\n" \
"ds_read_b128 a[52:55], v97 offset:12288\n" \
"v_mfma_f32_16x16x32_bf16 a[244:247], a[60:63], a[108:111], a[244:247]\n" \
"v_mfma_f32_16x16x32_bf16 a[240:243], a[56:59], a[96:99], a[240:243]\n" \
"v_mfma_f32_16x16x32_bf16 a[240:243], a[60:63], a[100:103], a[240:243]\n" \
"s_waitcnt lgkmcnt(1)\n" \
"v_mfma_f32_16x16x32_bf16 v[0:3], a[64:67], a[0:3], v[0:3]\n" \
"ds_read_b128 a[56:59], v96 offset:14336\n" \
"v_mfma_f32_16x16x32_bf16 v[0:3], a[68:71], a[4:7], v[0:3]\n" \
"v_mfma_f32_16x16x32_bf16 v[4:7], a[72:75], a[0:3], v[4:7]\n" \
"s_add_u32 m0, s79, 0\n" \
"ds_read_b128 a[60:63], v97 offset:14336\n" \
"v_mfma_f32_16x16x32_bf16 v[4:7], a[76:79], a[4:7], v[4:7]\n" \
"Lpg1661_%=:\n" \
"Lst0x2_%=:\n" \
"s_cbranch_execz Lpg3894_%=\n" \
"buffer_load_dwordx4 v228, s[52:55], s78 offen offset:0 lds\n" \
"v_mfma_f32_16x16x32_bf16 v[8:11], a[80:83], a[0:3], v[8:11]\n" \
"ds_read_b64_tr_b16 a[96:97], v102 offset:1024\n" \
"v_mfma_f32_16x16x32_bf16 v[8:11], a[84:87], a[4:7], v[8:11]\n" \
"v_mfma_f32_16x16x32_bf16 v[12:15], a[88:91], a[0:3], v[12:15]\n" \
"ds_read_b64_tr_b16 a[98:99], v102 offset:5120\n" \
"v_mfma_f32_16x16x32_bf16 v[12:15], a[92:95], a[4:7], v[12:15]\n" \
"buffer_load_dwordx4 v229, s[52:55], s78 offen offset:1024 lds\n" \
"v_mfma_f32_16x16x32_bf16 v[28:31], a[88:91], a[8:11], v[28:31]\n" \
"ds_read_b64_tr_b16 a[100:101], v102 offset:17408\n" \
"v_mfma_f32_16x16x32_bf16 v[28:31], a[92:95], a[12:15], v[28:31]\n" \
"v_mfma_f32_16x16x32_bf16 v[24:27], a[80:83], a[8:11], v[24:27]\n" \
"s_xor_b32 s78, s78, 0x180000\n" \
"ds_read_b64_tr_b16 a[102:103], v102 offset:21504\n" \
"v_mfma_f32_16x16x32_bf16 v[24:27], a[84:87], a[12:15], v[24:27]\n" \
"buffer_load_dwordx4 v228, s[52:55], s89 offen offset:2048 lds\n" \
"v_mfma_f32_16x16x32_bf16 v[20:23], a[72:75], a[8:11], v[20:23]\n" \
"ds_read_b64_tr_b16 a[104:105], v103 offset:1024\n" \
"v_mfma_f32_16x16x32_bf16 v[20:23], a[76:79], a[12:15], v[20:23]\n" \
"v_mfma_f32_16x16x32_bf16 v[16:19], a[64:67], a[8:11], v[16:19]\n" \
"ds_read_b64_tr_b16 a[106:107], v103 offset:5120\n" \
"v_mfma_f32_16x16x32_bf16 v[16:19], a[68:71], a[12:15], v[16:19]\n" \
"buffer_load_dwordx4 v229, s[52:55], s89 offen offset:3072 lds\n" \
"v_mfma_f32_16x16x32_bf16 v[32:35], a[64:67], a[16:19], v[32:35]\n" \
"ds_read_b64_tr_b16 a[108:109], v103 offset:17408\n" \
"v_mfma_f32_16x16x32_bf16 v[32:35], a[68:71], a[20:23], v[32:35]\n" \
"s_xor_b32 s89, s89, 0x180000\n" \
"v_mfma_f32_16x16x32_bf16 v[36:39], a[72:75], a[16:19], v[36:39]\n" \
"s_add_u32 m0, s79, 16384\n" \
"ds_read_b64_tr_b16 a[110:111], v103 offset:21504\n" \
"v_mfma_f32_16x16x32_bf16 v[36:39], a[76:79], a[20:23], v[36:39]\n" \
"buffer_load_dwordx4 v228, s[52:55], s78 offen offset:0 lds\n" \
"v_mfma_f32_16x16x32_bf16 v[40:43], a[80:83], a[16:19], v[40:43]\n" \
"ds_read_b64_tr_b16 a[112:113], v102 offset:1536\n" \
"v_mfma_f32_16x16x32_bf16 v[40:43], a[84:87], a[20:23], v[40:43]\n" \
"v_mfma_f32_16x16x32_bf16 v[44:47], a[88:91], a[16:19], v[44:47]\n" \
"ds_read_b64_tr_b16 a[114:115], v102 offset:5632\n" \
"v_mfma_f32_16x16x32_bf16 v[44:47], a[92:95], a[20:23], v[44:47]\n" \
"buffer_load_dwordx4 v229, s[52:55], s78 offen offset:1024 lds\n" \
"v_mfma_f32_16x16x32_bf16 v[60:63], a[88:91], a[24:27], v[60:63]\n" \
"ds_read_b64_tr_b16 a[116:117], v102 offset:17920\n" \
"v_mfma_f32_16x16x32_bf16 v[60:63], a[92:95], a[28:31], v[60:63]\n" \
"v_mfma_f32_16x16x32_bf16 v[56:59], a[80:83], a[24:27], v[56:59]\n" \
"s_xor_b32 s78, s78, 0x400000\n" \
"ds_read_b64_tr_b16 a[118:119], v102 offset:22016\n" \
"v_mfma_f32_16x16x32_bf16 v[56:59], a[84:87], a[28:31], v[56:59]\n" \
"buffer_load_dwordx4 v228, s[52:55], s89 offen offset:2048 lds\n" \
"v_mfma_f32_16x16x32_bf16 v[52:55], a[72:75], a[24:27], v[52:55]\n" \
"ds_read_b64_tr_b16 a[120:121], v103 offset:1536\n" \
"v_mfma_f32_16x16x32_bf16 v[52:55], a[76:79], a[28:31], v[52:55]\n" \
"v_mfma_f32_16x16x32_bf16 v[48:51], a[64:67], a[24:27], v[48:51]\n" \
"ds_read_b64_tr_b16 a[122:123], v103 offset:5632\n" \
"v_mfma_f32_16x16x32_bf16 v[48:51], a[68:71], a[28:31], v[48:51]\n" \
"buffer_load_dwordx4 v229, s[52:55], s89 offen offset:3072 lds\n" \
"v_mfma_f32_16x16x32_bf16 v[144:147], a[64:67], a[32:35], v[144:147]\n" \
"ds_read_b64_tr_b16 a[124:125], v103 offset:17920\n" \
"v_mfma_f32_16x16x32_bf16 v[144:147], a[68:71], a[36:39], v[144:147]\n" \
"v_mfma_f32_16x16x32_bf16 v[148:151], a[72:75], a[32:35], v[148:151]\n" \
"s_xor_b32 s89, s89, 0x400000\n" \
"ds_read_b64_tr_b16 a[126:127], v103 offset:22016\n" \
"v_mfma_f32_16x16x32_bf16 v[148:151], a[76:79], a[36:39], v[148:151]\n" \
"v_mfma_f32_16x16x32_bf16 v[152:155], a[80:83], a[32:35], v[152:155]\n" \
"v_mfma_f32_16x16x32_bf16 v[152:155], a[84:87], a[36:39], v[152:155]\n" \
"v_mfma_f32_16x16x32_bf16 v[156:159], a[88:91], a[32:35], v[156:159]\n" \
"v_mfma_f32_16x16x32_bf16 v[156:159], a[92:95], a[36:39], v[156:159]\n" \
"v_mfma_f32_16x16x32_bf16 v[188:191], a[88:91], a[40:43], v[188:191]\n" \
"v_mfma_f32_16x16x32_bf16 v[188:191], a[92:95], a[44:47], v[188:191]\n" \
"v_mfma_f32_16x16x32_bf16 v[184:187], a[80:83], a[40:43], v[184:187]\n" \
"v_mfma_f32_16x16x32_bf16 v[184:187], a[84:87], a[44:47], v[184:187]\n" \
"v_mfma_f32_16x16x32_bf16 v[180:183], a[72:75], a[40:43], v[180:183]\n" \
"v_mfma_f32_16x16x32_bf16 v[180:183], a[76:79], a[44:47], v[180:183]\n" \
"v_mfma_f32_16x16x32_bf16 v[176:179], a[64:67], a[40:43], v[176:179]\n" \
"v_mfma_f32_16x16x32_bf16 v[176:179], a[68:71], a[44:47], v[176:179]\n" \
"s_waitcnt lgkmcnt(0)\n" \
"v_mfma_f32_16x16x32_bf16 v[192:195], a[64:67], a[48:51], v[192:195]\n" \
"v_mfma_f32_16x16x32_bf16 v[192:195], a[68:71], a[52:55], v[192:195]\n" \
"v_mfma_f32_16x16x32_bf16 v[196:199], a[72:75], a[48:51], v[196:199]\n" \
"v_mfma_f32_16x16x32_bf16 v[196:199], a[76:79], a[52:55], v[196:199]\n" \
"v_mfma_f32_16x16x32_bf16 v[200:203], a[80:83], a[48:51], v[200:203]\n" \
"v_mfma_f32_16x16x32_bf16 v[200:203], a[84:87], a[52:55], v[200:203]\n" \
"v_mfma_f32_16x16x32_bf16 v[204:207], a[88:91], a[48:51], v[204:207]\n" \
"v_mfma_f32_16x16x32_bf16 v[204:207], a[92:95], a[52:55], v[204:207]\n" \
"v_mfma_f32_16x16x32_bf16 v[220:223], a[88:91], a[56:59], v[220:223]\n" \
"v_mfma_f32_16x16x32_bf16 v[220:223], a[92:95], a[60:63], v[220:223]\n" \
"v_mfma_f32_16x16x32_bf16 v[216:219], a[80:83], a[56:59], v[216:219]\n" \
"v_mfma_f32_16x16x32_bf16 v[216:219], a[84:87], a[60:63], v[216:219]\n" \
"v_mfma_f32_16x16x32_bf16 v[212:215], a[72:75], a[56:59], v[212:215]\n" \
"v_mfma_f32_16x16x32_bf16 v[212:215], a[76:79], a[60:63], v[212:215]\n" \
"v_mfma_f32_16x16x32_bf16 v[208:211], a[64:67], a[56:59], v[208:211]\n" \
"v_mfma_f32_16x16x32_bf16 v[208:211], a[68:71], a[60:63], v[208:211]\n" \
"s_waitcnt vmcnt(11) lgkmcnt(0)\n" \
"s_barrier\n" \
"v_mfma_f32_16x16x32_bf16 a[128:131], a[0:3], a[96:99], a[128:131]\n" \
"s_add_u32 m0, s74, 131072\n" \
"ds_read_b64_tr_b16 a[64:65], v98 offset:32768\n" \
"v_mfma_f32_16x16x32_bf16 a[128:131], a[4:7], a[100:103], a[128:131]\n" \
"buffer_load_dwordx4 v224, s[48:51], s75 offen offset:0 lds\n" \
"v_mfma_f32_16x16x32_bf16 a[132:135], a[0:3], a[104:107], a[132:135]\n" \
"ds_read_b64_tr_b16 a[66:67], v98 offset:36864\n" \
"v_mfma_f32_16x16x32_bf16 a[132:135], a[4:7], a[108:111], a[132:135]\n" \
"v_mfma_f32_16x16x32_bf16 a[136:139], a[0:3], a[112:115], a[136:139]\n" \
"ds_read_b64_tr_b16 a[68:69], v98 offset:49152\n" \
"v_mfma_f32_16x16x32_bf16 a[136:139], a[4:7], a[116:119], a[136:139]\n" \
"buffer_load_dwordx4 v225, s[48:51], s75 offen offset:1024 lds\n" \
"v_mfma_f32_16x16x32_bf16 a[140:143], a[0:3], a[120:123], a[140:143]\n" \
"ds_read_b64_tr_b16 a[70:71], v98 offset:53248\n" \
"v_mfma_f32_16x16x32_bf16 a[140:143], a[4:7], a[124:127], a[140:143]\n" \
"v_mfma_f32_16x16x32_bf16 a[156:159], a[8:11], a[120:123], a[156:159]\n" \
"ds_read_b128 a[0:3], v72 offset:32768\n" \
"v_mfma_f32_16x16x32_bf16 a[156:159], a[12:15], a[124:127], a[156:159]\n" \
"buffer_load_dwordx4 v226, s[48:51], s75 offen offset:2048 lds\n" \
"v_mfma_f32_16x16x32_bf16 a[152:155], a[8:11], a[112:115], a[152:155]\n" \
"ds_read_b64_tr_b16 a[72:73], v99 offset:32768\n" \
"v_mfma_f32_16x16x32_bf16 a[152:155], a[12:15], a[116:119], a[152:155]\n" \
"v_mfma_f32_16x16x32_bf16 a[148:151], a[8:11], a[104:107], a[148:151]\n" \
"ds_read_b128 a[4:7], v73 offset:32768\n" \
"v_mfma_f32_16x16x32_bf16 a[148:151], a[12:15], a[108:111], a[148:151]\n" \
"buffer_load_dwordx4 v227, s[48:51], s75 offen offset:3072 lds\n" \
"v_mfma_f32_16x16x32_bf16 a[144:147], a[8:11], a[96:99], a[144:147]\n" \
"ds_read_b64_tr_b16 a[74:75], v99 offset:36864\n" \
"v_mfma_f32_16x16x32_bf16 a[144:147], a[12:15], a[100:103], a[144:147]\n" \
"s_xor_b32 s75, s75, 0x180\n" \
"v_mfma_f32_16x16x32_bf16 a[160:163], a[16:19], a[96:99], a[160:163]\n" \
"s_add_u32 m0, s74, 135168\n" \
"ds_read_b128 a[8:11], v72 offset:34816\n" \
"v_mfma_f32_16x16x32_bf16 a[160:163], a[20:23], a[100:103], a[160:163]\n" \
"buffer_load_dwordx4 v224, s[48:51], s77 offen offset:0 lds\n" \
"v_mfma_f32_16x16x32_bf16 a[164:167], a[16:19], a[104:107], a[164:167]\n" \
"ds_read_b64_tr_b16 a[76:77], v99 offset:49152\n" \
"v_mfma_f32_16x16x32_bf16 a[164:167], a[20:23], a[108:111], a[164:167]\n" \
"v_mfma_f32_16x16x32_bf16 a[168:171], a[16:19], a[112:115], a[168:171]\n" \
"ds_read_b128 a[12:15], v73 offset:34816\n" \
"v_mfma_f32_16x16x32_bf16 a[168:171], a[20:23], a[116:119], a[168:171]\n" \
"buffer_load_dwordx4 v225, s[48:51], s77 offen offset:1024 lds\n" \
"v_mfma_f32_16x16x32_bf16 a[172:175], a[16:19], a[120:123], a[172:175]\n" \
"ds_read_b64_tr_b16 a[78:79], v99 offset:53248\n" \
"v_mfma_f32_16x16x32_bf16 a[172:175], a[20:23], a[124:127], a[172:175]\n" \
"v_mfma_f32_16x16x32_bf16 a[188:191], a[24:27], a[120:123], a[188:191]\n" \
"ds_read_b128 a[16:19], v72 offset:36864\n" \
"v_mfma_f32_16x16x32_bf16 a[188:191], a[28:31], a[124:127], a[188:191]\n" \
"buffer_load_dwordx4 v226, s[48:51], s77 offen offset:2048 lds\n" \
"v_mfma_f32_16x16x32_bf16 a[184:187], a[24:27], a[112:115], a[184:187]\n" \
"ds_read_b64_tr_b16 a[80:81], v98 offset:33280\n" \
"v_mfma_f32_16x16x32_bf16 a[184:187], a[28:31], a[116:119], a[184:187]\n" \
"v_mfma_f32_16x16x32_bf16 a[180:183], a[24:27], a[104:107], a[180:183]\n" \
"ds_read_b128 a[20:23], v73 offset:36864\n" \
"v_mfma_f32_16x16x32_bf16 a[180:183], a[28:31], a[108:111], a[180:183]\n" \
"buffer_load_dwordx4 v227, s[48:51], s77 offen offset:3072 lds\n" \
"v_mfma_f32_16x16x32_bf16 a[176:179], a[24:27], a[96:99], a[176:179]\n" \
"s_mov_b64 s[64:65], s[12:13]\n" \
"s_add_u32 s68, s67, 0x700000\n" \
"ds_read_b64_tr_b16 a[82:83], v98 offset:37376\n" \
"v_mfma_f32_16x16x32_bf16 a[176:179], a[28:31], a[100:103], a[176:179]\n" \
"s_add_u32 s64, s64, s68\n" \
"s_addc_u32 s65, s65, 0\n" \
"v_mfma_f32_16x16x32_bf16 a[192:195], a[32:35], a[96:99], a[192:195]\n" \
"s_xor_b32 s77, s77, 0x180\n" \
"ds_read_b128 a[24:27], v72 offset:38912\n" \
"v_mfma_f32_16x16x32_bf16 a[192:195], a[36:39], a[100:103], a[192:195]\n" \
"global_load_dword v238, v234, s[64:65]\n" \
"v_mfma_f32_16x16x32_bf16 a[196:199], a[32:35], a[104:107], a[196:199]\n" \
"ds_read_b64_tr_b16 a[84:85], v98 offset:49664\n" \
"v_mfma_f32_16x16x32_bf16 a[196:199], a[36:39], a[108:111], a[196:199]\n" \
"v_mfma_f32_16x16x32_bf16 a[200:203], a[32:35], a[112:115], a[200:203]\n" \
"s_lshr_b32 s68, s66, 2\n" \
"s_and_b32 s68, s68, 0xffff\n" \
"s_add_u32 s68, s68, 65585152\n" \
"ds_read_b128 a[28:31], v73 offset:38912\n" \
"v_mfma_f32_16x16x32_bf16 a[200:203], a[36:39], a[116:119], a[200:203]\n" \
"buffer_load_dword v238, v242, s[56:59], s68 offen sc1\n" \
"v_mfma_f32_16x16x32_bf16 a[204:207], a[32:35], a[120:123], a[204:207]\n" \
"ds_read_b64_tr_b16 a[86:87], v98 offset:53760\n" \
"v_mfma_f32_16x16x32_bf16 a[204:207], a[36:39], a[124:127], a[204:207]\n" \
"v_mfma_f32_16x16x32_bf16 a[220:223], a[40:43], a[120:123], a[220:223]\n" \
"ds_read_b128 a[32:35], v72 offset:40960\n" \
"v_mfma_f32_16x16x32_bf16 a[220:223], a[44:47], a[124:127], a[220:223]\n" \
"v_mfma_f32_16x16x32_bf16 a[216:219], a[40:43], a[112:115], a[216:219]\n" \
"ds_read_b64_tr_b16 a[88:89], v99 offset:33280\n" \
"v_mfma_f32_16x16x32_bf16 a[216:219], a[44:47], a[116:119], a[216:219]\n" \
"v_mfma_f32_16x16x32_bf16 a[212:215], a[40:43], a[104:107], a[212:215]\n" \
"ds_read_b128 a[36:39], v73 offset:40960\n" \
"v_mfma_f32_16x16x32_bf16 a[212:215], a[44:47], a[108:111], a[212:215]\n" \
"v_mfma_f32_16x16x32_bf16 a[208:211], a[40:43], a[96:99], a[208:211]\n" \
"ds_read_b64_tr_b16 a[90:91], v99 offset:37376\n" \
"v_mfma_f32_16x16x32_bf16 a[208:211], a[44:47], a[100:103], a[208:211]\n" \
"v_mfma_f32_16x16x32_bf16 a[224:227], a[48:51], a[96:99], a[224:227]\n" \
"ds_read_b128 a[40:43], v72 offset:43008\n" \
"v_mfma_f32_16x16x32_bf16 a[224:227], a[52:55], a[100:103], a[224:227]\n" \
"v_mfma_f32_16x16x32_bf16 a[228:231], a[48:51], a[104:107], a[228:231]\n" \
"ds_read_b64_tr_b16 a[92:93], v99 offset:49664\n" \
"v_mfma_f32_16x16x32_bf16 a[228:231], a[52:55], a[108:111], a[228:231]\n" \
"v_mfma_f32_16x16x32_bf16 a[232:235], a[48:51], a[112:115], a[232:235]\n" \
"ds_read_b128 a[44:47], v73 offset:43008\n" \
"v_mfma_f32_16x16x32_bf16 a[232:235], a[52:55], a[116:119], a[232:235]\n" \
"v_mfma_f32_16x16x32_bf16 a[236:239], a[48:51], a[120:123], a[236:239]\n" \
"ds_read_b64_tr_b16 a[94:95], v99 offset:53760\n" \
"v_mfma_f32_16x16x32_bf16 a[236:239], a[52:55], a[124:127], a[236:239]\n" \
"v_mfma_f32_16x16x32_bf16 a[252:255], a[56:59], a[120:123], a[252:255]\n" \
"ds_read_b128 a[48:51], v72 offset:45056\n" \
"v_mfma_f32_16x16x32_bf16 a[252:255], a[60:63], a[124:127], a[252:255]\n" \
"v_mfma_f32_16x16x32_bf16 a[248:251], a[56:59], a[112:115], a[248:251]\n" \
"v_mfma_f32_16x16x32_bf16 a[248:251], a[60:63], a[116:119], a[248:251]\n" \
"v_mfma_f32_16x16x32_bf16 a[244:247], a[56:59], a[104:107], a[244:247]\n" \
"ds_read_b128 a[52:55], v73 offset:45056\n" \
"v_mfma_f32_16x16x32_bf16 a[244:247], a[60:63], a[108:111], a[244:247]\n" \
"v_mfma_f32_16x16x32_bf16 a[240:243], a[56:59], a[96:99], a[240:243]\n" \
"v_mfma_f32_16x16x32_bf16 a[240:243], a[60:63], a[100:103], a[240:243]\n" \
"s_waitcnt lgkmcnt(1)\n" \
"v_mfma_f32_16x16x32_bf16 v[0:3], a[64:67], a[0:3], v[0:3]\n" \
"ds_read_b128 a[56:59], v72 offset:47104\n" \
"v_mfma_f32_16x16x32_bf16 v[0:3], a[68:71], a[4:7], v[0:3]\n" \
"v_mfma_f32_16x16x32_bf16 v[4:7], a[72:75], a[0:3], v[4:7]\n" \
"s_add_u32 m0, s74, 65536\n" \
"ds_read_b128 a[60:63], v73 offset:47104\n" \
"v_mfma_f32_16x16x32_bf16 v[4:7], a[76:79], a[4:7], v[4:7]\n" \
"buffer_load_dwordx4 v224, s[48:51], s75 offen offset:0 lds\n" \
"v_mfma_f32_16x16x32_bf16 v[8:11], a[80:83], a[0:3], v[8:11]\n" \
"ds_read_b64_tr_b16 a[96:97], v98 offset:33792\n" \
"v_mfma_f32_16x16x32_bf16 v[8:11], a[84:87], a[4:7], v[8:11]\n" \
"v_mfma_f32_16x16x32_bf16 v[12:15], a[88:91], a[0:3], v[12:15]\n" \
"ds_read_b64_tr_b16 a[98:99], v98 offset:37888\n" \
"v_mfma_f32_16x16x32_bf16 v[12:15], a[92:95], a[4:7], v[12:15]\n" \
"buffer_load_dwordx4 v225, s[48:51], s75 offen offset:1024 lds\n" \
"v_mfma_f32_16x16x32_bf16 v[28:31], a[88:91], a[8:11], v[28:31]\n" \
"ds_read_b64_tr_b16 a[100:101], v98 offset:50176\n" \
"v_mfma_f32_16x16x32_bf16 v[28:31], a[92:95], a[12:15], v[28:31]\n" \
"v_mfma_f32_16x16x32_bf16 v[24:27], a[80:83], a[8:11], v[24:27]\n" \
"ds_read_b64_tr_b16 a[102:103], v98 offset:54272\n" \
"v_mfma_f32_16x16x32_bf16 v[24:27], a[84:87], a[12:15], v[24:27]\n" \
"buffer_load_dwordx4 v226, s[48:51], s75 offen offset:2048 lds\n" \
"v_mfma_f32_16x16x32_bf16 v[20:23], a[72:75], a[8:11], v[20:23]\n" \
"ds_read_b64_tr_b16 a[104:105], v99 offset:33792\n" \
"v_mfma_f32_16x16x32_bf16 v[20:23], a[76:79], a[12:15], v[20:23]\n" \
"v_mfma_f32_16x16x32_bf16 v[16:19], a[64:67], a[8:11], v[16:19]\n" \
"ds_read_b64_tr_b16 a[106:107], v99 offset:37888\n" \
"v_mfma_f32_16x16x32_bf16 v[16:19], a[68:71], a[12:15], v[16:19]\n" \
"buffer_load_dwordx4 v227, s[48:51], s75 offen offset:3072 lds\n" \
"v_mfma_f32_16x16x32_bf16 v[32:35], a[64:67], a[16:19], v[32:35]\n" \
"ds_read_b64_tr_b16 a[108:109], v99 offset:50176\n" \
"v_mfma_f32_16x16x32_bf16 v[32:35], a[68:71], a[20:23], v[32:35]\n" \
"s_xor_b32 s75, s75, 0x400\n" \
"v_mfma_f32_16x16x32_bf16 v[36:39], a[72:75], a[16:19], v[36:39]\n" \
"s_add_u32 m0, s74, 69632\n" \
"ds_read_b64_tr_b16 a[110:111], v99 offset:54272\n" \
"v_mfma_f32_16x16x32_bf16 v[36:39], a[76:79], a[20:23], v[36:39]\n" \
"buffer_load_dwordx4 v224, s[48:51], s77 offen offset:0 lds\n" \
"v_mfma_f32_16x16x32_bf16 v[40:43], a[80:83], a[16:19], v[40:43]\n" \
"ds_read_b64_tr_b16 a[112:113], v98 offset:34304\n" \
"v_mfma_f32_16x16x32_bf16 v[40:43], a[84:87], a[20:23], v[40:43]\n" \
"v_mfma_f32_16x16x32_bf16 v[44:47], a[88:91], a[16:19], v[44:47]\n" \
"ds_read_b64_tr_b16 a[114:115], v98 offset:38400\n" \
"v_mfma_f32_16x16x32_bf16 v[44:47], a[92:95], a[20:23], v[44:47]\n" \
"buffer_load_dwordx4 v225, s[48:51], s77 offen offset:1024 lds\n" \
"v_mfma_f32_16x16x32_bf16 v[60:63], a[88:91], a[24:27], v[60:63]\n" \
"ds_read_b64_tr_b16 a[116:117], v98 offset:50688\n" \
"v_mfma_f32_16x16x32_bf16 v[60:63], a[92:95], a[28:31], v[60:63]\n" \
"v_mfma_f32_16x16x32_bf16 v[56:59], a[80:83], a[24:27], v[56:59]\n" \
"ds_read_b64_tr_b16 a[118:119], v98 offset:54784\n" \
"v_mfma_f32_16x16x32_bf16 v[56:59], a[84:87], a[28:31], v[56:59]\n" \
"buffer_load_dwordx4 v226, s[48:51], s77 offen offset:2048 lds\n" \
"v_mfma_f32_16x16x32_bf16 v[52:55], a[72:75], a[24:27], v[52:55]\n" \
"ds_read_b64_tr_b16 a[120:121], v99 offset:34304\n" \
"v_mfma_f32_16x16x32_bf16 v[52:55], a[76:79], a[28:31], v[52:55]\n" \
"v_mfma_f32_16x16x32_bf16 v[48:51], a[64:67], a[24:27], v[48:51]\n" \
"ds_read_b64_tr_b16 a[122:123], v99 offset:38400\n" \
"v_mfma_f32_16x16x32_bf16 v[48:51], a[68:71], a[28:31], v[48:51]\n" \
"buffer_load_dwordx4 v227, s[48:51], s77 offen offset:3072 lds\n" \
"v_mfma_f32_16x16x32_bf16 v[144:147], a[64:67], a[32:35], v[144:147]\n" \
"ds_read_b64_tr_b16 a[124:125], v99 offset:50688\n" \
"v_mfma_f32_16x16x32_bf16 v[144:147], a[68:71], a[36:39], v[144:147]\n" \
"v_mfma_f32_16x16x32_bf16 v[148:151], a[72:75], a[32:35], v[148:151]\n" \
"s_xor_b32 s77, s77, 0x400\n" \
"ds_read_b64_tr_b16 a[126:127], v99 offset:54784\n" \
"v_mfma_f32_16x16x32_bf16 v[148:151], a[76:79], a[36:39], v[148:151]\n" \
"v_mfma_f32_16x16x32_bf16 v[152:155], a[80:83], a[32:35], v[152:155]\n" \
"v_mfma_f32_16x16x32_bf16 v[152:155], a[84:87], a[36:39], v[152:155]\n" \
"v_mfma_f32_16x16x32_bf16 v[156:159], a[88:91], a[32:35], v[156:159]\n" \
"v_mfma_f32_16x16x32_bf16 v[156:159], a[92:95], a[36:39], v[156:159]\n" \
"v_mfma_f32_16x16x32_bf16 v[188:191], a[88:91], a[40:43], v[188:191]\n" \
"v_mfma_f32_16x16x32_bf16 v[188:191], a[92:95], a[44:47], v[188:191]\n" \
"v_mfma_f32_16x16x32_bf16 v[184:187], a[80:83], a[40:43], v[184:187]\n" \
"v_mfma_f32_16x16x32_bf16 v[184:187], a[84:87], a[44:47], v[184:187]\n" \
"v_mfma_f32_16x16x32_bf16 v[180:183], a[72:75], a[40:43], v[180:183]\n" \
"v_mfma_f32_16x16x32_bf16 v[180:183], a[76:79], a[44:47], v[180:183]\n" \
"v_mfma_f32_16x16x32_bf16 v[176:179], a[64:67], a[40:43], v[176:179]\n" \
"v_mfma_f32_16x16x32_bf16 v[176:179], a[68:71], a[44:47], v[176:179]\n" \
"s_waitcnt lgkmcnt(0)\n" \
"v_mfma_f32_16x16x32_bf16 v[192:195], a[64:67], a[48:51], v[192:195]\n" \
"v_mfma_f32_16x16x32_bf16 v[192:195], a[68:71], a[52:55], v[192:195]\n" \
"v_mfma_f32_16x16x32_bf16 v[196:199], a[72:75], a[48:51], v[196:199]\n" \
"v_mfma_f32_16x16x32_bf16 v[196:199], a[76:79], a[52:55], v[196:199]\n" \
"v_mfma_f32_16x16x32_bf16 v[200:203], a[80:83], a[48:51], v[200:203]\n" \
"v_mfma_f32_16x16x32_bf16 v[200:203], a[84:87], a[52:55], v[200:203]\n" \
"v_mfma_f32_16x16x32_bf16 v[204:207], a[88:91], a[48:51], v[204:207]\n" \
"v_mfma_f32_16x16x32_bf16 v[204:207], a[92:95], a[52:55], v[204:207]\n" \
"v_mfma_f32_16x16x32_bf16 v[220:223], a[88:91], a[56:59], v[220:223]\n" \
"v_mfma_f32_16x16x32_bf16 v[220:223], a[92:95], a[60:63], v[220:223]\n" \
"v_mfma_f32_16x16x32_bf16 v[216:219], a[80:83], a[56:59], v[216:219]\n" \
"v_mfma_f32_16x16x32_bf16 v[216:219], a[84:87], a[60:63], v[216:219]\n" \
"v_mfma_f32_16x16x32_bf16 v[212:215], a[72:75], a[56:59], v[212:215]\n" \
"v_mfma_f32_16x16x32_bf16 v[212:215], a[76:79], a[60:63], v[212:215]\n" \
"v_mfma_f32_16x16x32_bf16 v[208:211], a[64:67], a[56:59], v[208:211]\n" \
"v_mfma_f32_16x16x32_bf16 v[208:211], a[68:71], a[60:63], v[208:211]\n" \
"s_waitcnt vmcnt(10) lgkmcnt(0)\n" \
"s_barrier\n" \
"v_mfma_f32_16x16x32_bf16 a[128:131], a[0:3], a[96:99], a[128:131]\n" \
"s_add_u32 m0, s74, 98304\n" \
"v_mfma_f32_16x16x32_bf16 a[128:131], a[4:7], a[100:103], a[128:131]\n" \
"buffer_load_dwordx4 v224, s[48:51], s75 offen offset:0 lds\n" \
"v_mfma_f32_16x16x32_bf16 a[132:135], a[0:3], a[104:107], a[132:135]\n" \
"ds_read_b64_tr_b16 a[64:65], v232 offset:0\n" \
"ds_read_b64_tr_b16 a[66:67], v232 offset:2048\n" \
"v_mfma_f32_16x16x32_bf16 a[132:135], a[4:7], a[108:111], a[132:135]\n" \
"v_mfma_f32_16x16x32_bf16 a[136:139], a[0:3], a[112:115], a[136:139]\n" \
"v_mfma_f32_16x16x32_bf16 a[136:139], a[4:7], a[116:119], a[136:139]\n" \
"buffer_load_dwordx4 v225, s[48:51], s75 offen offset:1024 lds\n" \
"v_mfma_f32_16x16x32_bf16 a[140:143], a[0:3], a[120:123], a[140:143]\n" \
"ds_read_b64_tr_b16 a[68:69], v232 offset:8192\n" \
"ds_read_b64_tr_b16 a[70:71], v232 offset:10240\n" \
"v_mfma_f32_16x16x32_bf16 a[140:143], a[4:7], a[124:127], a[140:143]\n" \
"v_mfma_f32_16x16x32_bf16 a[156:159], a[8:11], a[120:123], a[156:159]\n" \
"v_mul_f32 v192, s76, v192\n" \
"v_mul_f32 v193, s76, v193\n" \
"v_mfma_f32_16x16x32_bf16 a[156:159], a[12:15], a[124:127], a[156:159]\n" \
"buffer_load_dwordx4 v226, s[48:51], s75 offen offset:2048 lds\n" \
"v_mfma_f32_16x16x32_bf16 a[152:155], a[8:11], a[112:115], a[152:155]\n" \
"ds_read_b64_tr_b16 a[72:73], v233 offset:0\n" \
"ds_read_b64_tr_b16 a[74:75], v233 offset:2048\n" \
"v_mfma_f32_16x16x32_bf16 a[152:155], a[12:15], a[116:119], a[152:155]\n" \
"v_mul_f32 v194, s76, v194\n" \
"v_mul_f32 v195, s76, v195\n" \
"v_mfma_f32_16x16x32_bf16 a[148:151], a[8:11], a[104:107], a[148:151]\n" \
"v_cvt_pk_bf16_f32 v192, v192, v193\n" \
"v_cvt_pk_bf16_f32 v193, v194, v195\n" \
"v_mfma_f32_16x16x32_bf16 a[148:151], a[12:15], a[108:111], a[148:151]\n" \
"buffer_load_dwordx4 v227, s[48:51], s75 offen offset:3072 lds\n" \
"v_mfma_f32_16x16x32_bf16 a[144:147], a[8:11], a[96:99], a[144:147]\n" \
"ds_read_b64_tr_b16 a[76:77], v233 offset:8192\n" \
"ds_read_b64_tr_b16 a[78:79], v233 offset:10240\n" \
"v_mfma_f32_16x16x32_bf16 a[144:147], a[12:15], a[100:103], a[144:147]\n" \
"s_xor_b32 s75, s75, 0x500\n" \
"v_mul_f32 v196, s76, v196\n" \
"v_mul_f32 v197, s76, v197\n" \
"v_mfma_f32_16x16x32_bf16 a[160:163], a[16:19], a[96:99], a[160:163]\n" \
"s_add_u32 m0, s74, 102400\n" \
"v_mul_f32 v198, s76, v198\n" \
"v_mul_f32 v199, s76, v199\n" \
"v_mfma_f32_16x16x32_bf16 a[160:163], a[20:23], a[100:103], a[160:163]\n" \
"buffer_load_dwordx4 v224, s[48:51], s77 offen offset:0 lds\n" \
"v_mfma_f32_16x16x32_bf16 a[164:167], a[16:19], a[104:107], a[164:167]\n" \
"ds_read_b64_tr_b16 a[80:81], v232 offset:512\n" \
"ds_read_b64_tr_b16 a[82:83], v232 offset:2560\n" \
"v_mfma_f32_16x16x32_bf16 a[164:167], a[20:23], a[108:111], a[164:167]\n" \
"v_cvt_pk_bf16_f32 v194, v196, v197\n" \
"v_cvt_pk_bf16_f32 v195, v198, v199\n" \
"v_mfma_f32_16x16x32_bf16 a[168:171], a[16:19], a[112:115], a[168:171]\n" \
"v_mul_f32 v208, s76, v208\n" \
"v_mul_f32 v209, s76, v209\n" \
"v_mfma_f32_16x16x32_bf16 a[168:171], a[20:23], a[116:119], a[168:171]\n" \
"buffer_load_dwordx4 v225, s[48:51], s77 offen offset:1024 lds\n" \
"v_mfma_f32_16x16x32_bf16 a[172:175], a[16:19], a[120:123], a[172:175]\n" \
"ds_read_b64_tr_b16 a[84:85], v232 offset:8704\n" \
"ds_read_b64_tr_b16 a[86:87], v232 offset:10752\n" \
"v_mfma_f32_16x16x32_bf16 a[172:175], a[20:23], a[124:127], a[172:175]\n" \
"v_mul_f32 v210, s76, v210\n" \
"v_mul_f32 v211, s76, v211\n" \
"v_mfma_f32_16x16x32_bf16 a[188:191], a[24:27], a[120:123], a[188:191]\n" \
"v_cvt_pk_bf16_f32 v208, v208, v209\n" \
"v_cvt_pk_bf16_f32 v209, v210, v211\n" \
"v_mfma_f32_16x16x32_bf16 a[188:191], a[28:31], a[124:127], a[188:191]\n" \
"buffer_load_dwordx4 v226, s[48:51], s77 offen offset:2048 lds\n" \
"v_mfma_f32_16x16x32_bf16 a[184:187], a[24:27], a[112:115], a[184:187]\n" \
"ds_read_b64_tr_b16 a[88:89], v233 offset:512\n" \
"ds_read_b64_tr_b16 a[90:91], v233 offset:2560\n" \
"v_mfma_f32_16x16x32_bf16 a[184:187], a[28:31], a[116:119], a[184:187]\n" \
"v_mul_f32 v212, s76, v212\n" \
"v_mul_f32 v213, s76, v213\n" \
"v_mfma_f32_16x16x32_bf16 a[180:183], a[24:27], a[104:107], a[180:183]\n" \
"v_mul_f32 v214, s76, v214\n" \
"v_mul_f32 v215, s76, v215\n" \
"v_mfma_f32_16x16x32_bf16 a[180:183], a[28:31], a[108:111], a[180:183]\n" \
"buffer_load_dwordx4 v227, s[48:51], s77 offen offset:3072 lds\n" \
"v_mfma_f32_16x16x32_bf16 a[176:179], a[24:27], a[96:99], a[176:179]\n" \
"ds_read_b64_tr_b16 a[92:93], v233 offset:8704\n" \
"ds_read_b64_tr_b16 a[94:95], v233 offset:10752\n" \
"v_mfma_f32_16x16x32_bf16 a[176:179], a[28:31], a[100:103], a[176:179]\n" \
"s_xor_b32 s77, s77, 0x500\n" \
"v_cvt_pk_bf16_f32 v210, v212, v213\n" \
"v_cvt_pk_bf16_f32 v211, v214, v215\n" \
"v_mfma_f32_16x16x32_bf16 a[192:195], a[32:35], a[96:99], a[192:195]\n" \
"s_add_u32 m0, s79, 32768\n" \
"v_mul_f32 v200, s76, v200\n" \
"v_mul_f32 v201, s76, v201\n" \
"v_mfma_f32_16x16x32_bf16 a[192:195], a[36:39], a[100:103], a[192:195]\n" \
"buffer_load_dwordx4 v228, s[52:55], s78 offen offset:0 lds\n" \
"v_mfma_f32_16x16x32_bf16 a[196:199], a[32:35], a[104:107], a[196:199]\n" \
"ds_read_b128 a[0:3], v250 offset:0\n" \
"ds_read_b128 a[4:7], v251 offset:0\n" \
"v_mfma_f32_16x16x32_bf16 a[196:199], a[36:39], a[108:111], a[196:199]\n" \
"v_mul_f32 v202, s76, v202\n" \
"v_mul_f32 v203, s76, v203\n" \
"v_mfma_f32_16x16x32_bf16 a[200:203], a[32:35], a[112:115], a[200:203]\n" \
"v_cvt_pk_bf16_f32 v200, v200, v201\n" \
"v_cvt_pk_bf16_f32 v201, v202, v203\n" \
"v_mfma_f32_16x16x32_bf16 a[200:203], a[36:39], a[116:119], a[200:203]\n" \
"buffer_load_dwordx4 v229, s[52:55], s78 offen offset:1024 lds\n" \
"v_mfma_f32_16x16x32_bf16 a[204:207], a[32:35], a[120:123], a[204:207]\n" \
"ds_read_b128 a[8:11], v250 offset:2048\n" \
"ds_read_b128 a[12:15], v251 offset:2048\n" \
"v_mfma_f32_16x16x32_bf16 a[204:207], a[36:39], a[124:127], a[204:207]\n" \
"v_mul_f32 v204, s76, v204\n" \
"v_mul_f32 v205, s76, v205\n" \
"v_mfma_f32_16x16x32_bf16 a[220:223], a[40:43], a[120:123], a[220:223]\n" \
"s_xor_b32 s78, s78, 0x500000\n" \
"v_mul_f32 v206, s76, v206\n" \
"v_mul_f32 v207, s76, v207\n" \
"v_mfma_f32_16x16x32_bf16 a[220:223], a[44:47], a[124:127], a[220:223]\n" \
"buffer_load_dwordx4 v228, s[52:55], s89 offen offset:2048 lds\n" \
"v_mfma_f32_16x16x32_bf16 a[216:219], a[40:43], a[112:115], a[216:219]\n" \
"ds_read_b128 a[16:19], v250 offset:4096\n" \
"ds_read_b128 a[20:23], v251 offset:4096\n" \
"v_mfma_f32_16x16x32_bf16 a[216:219], a[44:47], a[116:119], a[216:219]\n" \
"v_cvt_pk_bf16_f32 v202, v204, v205\n" \
"v_cvt_pk_bf16_f32 v203, v206, v207\n" \
"v_mfma_f32_16x16x32_bf16 a[212:215], a[40:43], a[104:107], a[212:215]\n" \
"v_mul_f32 v216, s76, v216\n" \
"v_mul_f32 v217, s76, v217\n" \
"v_mfma_f32_16x16x32_bf16 a[212:215], a[44:47], a[108:111], a[212:215]\n" \
"buffer_load_dwordx4 v229, s[52:55], s89 offen offset:3072 lds\n" \
"v_mfma_f32_16x16x32_bf16 a[208:211], a[40:43], a[96:99], a[208:211]\n" \
"v_mul_f32 v218, s76, v218\n" \
"v_mul_f32 v219, s76, v219\n" \
"v_mfma_f32_16x16x32_bf16 a[208:211], a[44:47], a[100:103], a[208:211]\n" \
"s_xor_b32 s89, s89, 0x500000\n" \
"v_cvt_pk_bf16_f32 v216, v216, v217\n" \
"v_cvt_pk_bf16_f32 v217, v218, v219\n" \
"v_mfma_f32_16x16x32_bf16 a[224:227], a[48:51], a[96:99], a[224:227]\n" \
"s_add_u32 m0, s79, 49152\n" \
"v_mul_f32 v220, s76, v220\n" \
"v_mul_f32 v221, s76, v221\n" \
"v_mfma_f32_16x16x32_bf16 a[224:227], a[52:55], a[100:103], a[224:227]\n" \
"buffer_load_dwordx4 v228, s[52:55], s78 offen offset:0 lds\n" \
"v_mfma_f32_16x16x32_bf16 a[228:231], a[48:51], a[104:107], a[228:231]\n" \
"v_mul_f32 v222, s76, v222\n" \
"v_mul_f32 v223, s76, v223\n" \
"v_mfma_f32_16x16x32_bf16 a[228:231], a[52:55], a[108:111], a[228:231]\n" \
"v_cvt_pk_bf16_f32 v218, v220, v221\n" \
"v_cvt_pk_bf16_f32 v219, v222, v223\n" \
"v_mfma_f32_16x16x32_bf16 a[232:235], a[48:51], a[112:115], a[232:235]\n" \
"v_mul_f32 v144, s76, v144\n" \
"v_mul_f32 v145, s76, v145\n" \
"v_mfma_f32_16x16x32_bf16 a[232:235], a[52:55], a[116:119], a[232:235]\n" \
"buffer_load_dwordx4 v229, s[52:55], s78 offen offset:1024 lds\n" \
"v_mfma_f32_16x16x32_bf16 a[236:239], a[48:51], a[120:123], a[236:239]\n" \
"v_mul_f32 v146, s76, v146\n" \
"v_mul_f32 v147, s76, v147\n" \
"v_mfma_f32_16x16x32_bf16 a[236:239], a[52:55], a[124:127], a[236:239]\n" \
"v_cvt_pk_bf16_f32 v196, v144, v145\n" \
"v_cvt_pk_bf16_f32 v197, v146, v147\n" \
"v_mfma_f32_16x16x32_bf16 a[252:255], a[56:59], a[120:123], a[252:255]\n" \
"s_xor_b32 s78, s78, 0x180000\n" \
"v_mul_f32 v148, s76, v148\n" \
"v_mul_f32 v149, s76, v149\n" \
"v_mfma_f32_16x16x32_bf16 a[252:255], a[60:63], a[124:127], a[252:255]\n" \
"buffer_load_dwordx4 v228, s[52:55], s89 offen offset:2048 lds\n" \
"v_mfma_f32_16x16x32_bf16 a[248:251], a[56:59], a[112:115], a[248:251]\n" \
"v_mul_f32 v150, s76, v150\n" \
"v_mul_f32 v151, s76, v151\n" \
"v_mfma_f32_16x16x32_bf16 a[248:251], a[60:63], a[116:119], a[248:251]\n" \
"v_cvt_pk_bf16_f32 v198, v148, v149\n" \
"v_cvt_pk_bf16_f32 v199, v150, v151\n" \
"v_mfma_f32_16x16x32_bf16 a[244:247], a[56:59], a[104:107], a[244:247]\n" \
"v_mul_f32 v176, s76, v176\n" \
"v_mul_f32 v177, s76, v177\n" \
"v_mfma_f32_16x16x32_bf16 a[244:247], a[60:63], a[108:111], a[244:247]\n" \
"buffer_load_dwordx4 v229, s[52:55], s89 offen offset:3072 lds\n" \
"v_mfma_f32_16x16x32_bf16 a[240:243], a[56:59], a[96:99], a[240:243]\n" \
"s_waitcnt lgkmcnt(0)\n" \
"v_mul_f32 v178, s76, v178\n" \
"v_mul_f32 v179, s76, v179\n" \
"v_mfma_f32_16x16x32_bf16 a[240:243], a[60:63], a[100:103], a[240:243]\n" \
"s_xor_b32 s89, s89, 0x180000\n" \
"v_cvt_pk_bf16_f32 v212, v176, v177\n" \
"v_cvt_pk_bf16_f32 v213, v178, v179\n" \
"v_mfma_f32_16x16x32_bf16 v[64:67], a[64:67], a[0:3], 0\n" \
"ds_read_b128 a[24:27], v250 offset:6144\n" \
"v_mul_f32 v180, s76, v180\n" \
"v_mfma_f32_16x16x32_bf16 v[64:67], a[68:71], a[4:7], v[64:67]\n" \
"s_add_u32 m0, s74, 131072\n" \
"ds_read_b128 a[28:31], v251 offset:6144\n" \
"v_mul_f32 v181, s76, v181\n" \
"v_mfma_f32_16x16x32_bf16 v[68:71], a[72:75], a[0:3], 0\n" \
"ds_read_b64_tr_b16 a[96:97], v232 offset:1024\n" \
"v_mul_f32 v182, s76, v182\n" \
"v_mfma_f32_16x16x32_bf16 v[68:71], a[76:79], a[4:7], v[68:71]\n" \
"buffer_load_dwordx4 v224, s[48:51], s75 offen offset:0 lds\n" \
"v_mfma_f32_16x16x32_bf16 v[72:75], a[80:83], a[0:3], 0\n" \
"ds_read_b64_tr_b16 a[98:99], v232 offset:3072\n" \
"v_mul_f32 v183, s76, v183\n" \
"v_mfma_f32_16x16x32_bf16 v[72:75], a[84:87], a[4:7], v[72:75]\n" \
"ds_read_b64_tr_b16 a[100:101], v232 offset:9216\n" \
"v_cvt_pk_bf16_f32 v214, v180, v181\n" \
"v_mfma_f32_16x16x32_bf16 v[76:79], a[88:91], a[0:3], 0\n" \
"ds_read_b64_tr_b16 a[102:103], v232 offset:11264\n" \
"v_cvt_pk_bf16_f32 v215, v182, v183\n" \
"v_mfma_f32_16x16x32_bf16 v[76:79], a[92:95], a[4:7], v[76:79]\n" \
"buffer_load_dwordx4 v225, s[48:51], s75 offen offset:1024 lds\n" \
"v_mfma_f32_16x16x32_bf16 v[108:111], a[88:91], a[8:11], 0\n" \
"ds_read_b64_tr_b16 a[104:105], v233 offset:1024\n" \
"v_mul_f32 v152, s76, v152\n" \
"v_mfma_f32_16x16x32_bf16 v[108:111], a[92:95], a[12:15], v[108:111]\n" \
"ds_read_b64_tr_b16 a[106:107], v233 offset:3072\n" \
"v_mul_f32 v153, s76, v153\n" \
"v_mfma_f32_16x16x32_bf16 v[104:107], a[80:83], a[8:11], 0\n" \
"ds_read_b64_tr_b16 a[108:109], v233 offset:9216\n" \
"v_mul_f32 v154, s76, v154\n" \
"v_mfma_f32_16x16x32_bf16 v[104:107], a[84:87], a[12:15], v[104:107]\n" \
"buffer_load_dwordx4 v226, s[48:51], s75 offen offset:2048 lds\n" \
"v_mfma_f32_16x16x32_bf16 v[100:103], a[72:75], a[8:11], 0\n" \
"ds_read_b64_tr_b16 a[110:111], v233 offset:11264\n" \
"v_mul_f32 v155, s76, v155\n" \
"v_mfma_f32_16x16x32_bf16 v[100:103], a[76:79], a[12:15], v[100:103]\n" \
"ds_read_b64_tr_b16 a[112:113], v232 offset:1536\n" \
"v_cvt_pk_bf16_f32 v204, v152, v153\n" \
"v_mfma_f32_16x16x32_bf16 v[96:99], a[64:67], a[8:11], 0\n" \
"ds_read_b64_tr_b16 a[114:115], v232 offset:3584\n" \
"v_cvt_pk_bf16_f32 v205, v154, v155\n" \
"v_mfma_f32_16x16x32_bf16 v[96:99], a[68:71], a[12:15], v[96:99]\n" \
"buffer_load_dwordx4 v227, s[48:51], s75 offen offset:3072 lds\n" \
"v_mfma_f32_16x16x32_bf16 v[128:131], a[64:67], a[16:19], 0\n" \
"s_xor_b32 s75, s75, 0x180\n" \
"ds_read_b64_tr_b16 a[116:117], v232 offset:9728\n" \
"v_mul_f32 v156, s76, v156\n" \
"v_mfma_f32_16x16x32_bf16 v[128:131], a[68:71], a[20:23], v[128:131]\n" \
"s_add_u32 m0, s74, 135168\n" \
"ds_read_b64_tr_b16 a[118:119], v232 offset:11776\n" \
"v_mul_f32 v157, s76, v157\n" \
"v_mfma_f32_16x16x32_bf16 v[132:135], a[72:75], a[16:19], 0\n" \
"ds_read_b64_tr_b16 a[120:121], v233 offset:1536\n" \
"v_mul_f32 v158, s76, v158\n" \
"v_mfma_f32_16x16x32_bf16 v[132:135], a[76:79], a[20:23], v[132:135]\n" \
"buffer_load_dwordx4 v224, s[48:51], s77 offen offset:0 lds\n" \
"v_mfma_f32_16x16x32_bf16 v[136:139], a[80:83], a[16:19], 0\n" \
"ds_read_b64_tr_b16 a[122:123], v233 offset:3584\n" \
"v_mul_f32 v159, s76, v159\n" \
"Lpg2207_%=:\n" \
"Lst0x3_%=:\n" \
"s_cbranch_execz Lpg4445_%=\n" \
"v_mfma_f32_16x16x32_bf16 v[136:139], a[84:87], a[20:23], v[136:139]\n" \
"ds_read_b64_tr_b16 a[124:125], v233 offset:9728\n" \
"v_cvt_pk_bf16_f32 v206, v156, v157\n" \
"v_mfma_f32_16x16x32_bf16 v[140:143], a[88:91], a[16:19], 0\n" \
"s_waitcnt lgkmcnt(3)\n" \
"ds_read_b64_tr_b16 a[126:127], v233 offset:11776\n" \
"v_cvt_pk_bf16_f32 v207, v158, v159\n" \
"v_mfma_f32_16x16x32_bf16 v[140:143], a[92:95], a[20:23], v[140:143]\n" \
"buffer_load_dwordx4 v225, s[48:51], s77 offen offset:1024 lds\n" \
"v_mfma_f32_16x16x32_bf16 v[172:175], a[88:91], a[24:27], 0\n" \
"s_add_u32 s93, s87, 8192\n" \
"v_mul_f32 v184, s76, v184\n" \
"v_mul_f32 v185, s76, v185\n" \
"v_mfma_f32_16x16x32_bf16 v[172:175], a[92:95], a[28:31], v[172:175]\n" \
"buffer_store_dwordx4 v[192:195], v236, s[56:59], s93 offen offset:0 sc1\n" \
"v_mfma_f32_16x16x32_bf16 v[168:171], a[80:83], a[24:27], 0\n" \
"v_mul_f32 v186, s76, v186\n" \
"v_mul_f32 v187, s76, v187\n" \
"v_mfma_f32_16x16x32_bf16 v[168:171], a[84:87], a[28:31], v[168:171]\n" \
"buffer_load_dwordx4 v226, s[48:51], s77 offen offset:2048 lds\n" \
"v_mfma_f32_16x16x32_bf16 v[164:167], a[72:75], a[24:27], 0\n" \
"v_cvt_pk_bf16_f32 v220, v184, v185\n" \
"v_cvt_pk_bf16_f32 v221, v186, v187\n" \
"v_mfma_f32_16x16x32_bf16 v[164:167], a[76:79], a[28:31], v[164:167]\n" \
"buffer_load_dwordx4 v227, s[48:51], s77 offen offset:3072 lds\n" \
"v_mfma_f32_16x16x32_bf16 v[160:163], a[64:67], a[24:27], 0\n" \
"v_mul_f32 v188, s76, v188\n" \
"v_mul_f32 v189, s76, v189\n" \
"v_mfma_f32_16x16x32_bf16 v[160:163], a[68:71], a[28:31], v[160:163]\n" \
"s_waitcnt vmcnt(43) lgkmcnt(0)\n" \
"s_barrier\n" \
"v_mfma_f32_16x16x32_bf16 v[80:83], a[96:99], a[0:3], 0\n" \
"s_xor_b32 s77, s77, 0x180\n" \
"ds_read_b64_tr_b16 a[64:65], v232 offset:16384\n" \
"v_mul_f32 v190, s76, v190\n" \
"v_mfma_f32_16x16x32_bf16 v[80:83], a[100:103], a[4:7], v[80:83]\n" \
"ds_read_b64_tr_b16 a[66:67], v232 offset:18432\n" \
"v_mul_f32 v191, s76, v191\n" \
"v_mfma_f32_16x16x32_bf16 v[84:87], a[104:107], a[0:3], 0\n" \
"s_add_u32 m0, s79, 0\n" \
"ds_read_b64_tr_b16 a[68:69], v232 offset:24576\n" \
"v_cvt_pk_bf16_f32 v222, v188, v189\n" \
"v_mfma_f32_16x16x32_bf16 v[84:87], a[108:111], a[4:7], v[84:87]\n" \
"buffer_load_dwordx4 v228, s[52:55], s78 offen offset:0 lds\n" \
"v_mfma_f32_16x16x32_bf16 v[88:91], a[112:115], a[0:3], 0\n" \
"ds_read_b64_tr_b16 a[70:71], v232 offset:26624\n" \
"v_cvt_pk_bf16_f32 v223, v190, v191\n" \
"v_mfma_f32_16x16x32_bf16 v[88:91], a[116:119], a[4:7], v[88:91]\n" \
"ds_read_b64_tr_b16 a[72:73], v233 offset:16384\n" \
"v_accvgpr_read_b32 v243, a224\n" \
"v_mfma_f32_16x16x32_bf16 v[92:95], a[120:123], a[0:3], 0\n" \
"ds_read_b64_tr_b16 a[74:75], v233 offset:18432\n" \
"v_accvgpr_read_b32 v252, a225\n" \
"v_mfma_f32_16x16x32_bf16 v[92:95], a[124:127], a[4:7], v[92:95]\n" \
"buffer_load_dwordx4 v229, s[52:55], s78 offen offset:1024 lds\n" \
"v_mfma_f32_16x16x32_bf16 v[124:127], a[120:123], a[8:11], 0\n" \
"s_waitcnt vmcnt(33)\n" \
"ds_read_b128 a[0:3], v230 offset:0\n" \
"v_accvgpr_read_b32 v253, a226\n" \
"v_mfma_f32_16x16x32_bf16 v[124:127], a[124:127], a[12:15], v[124:127]\n" \
"ds_read_b128 a[4:7], v231 offset:0\n" \
"v_accvgpr_read_b32 v254, a227\n" \
"v_mfma_f32_16x16x32_bf16 v[120:123], a[112:115], a[8:11], 0\n" \
"s_xor_b32 s78, s78, 0x300000\n" \
"ds_read_b64_tr_b16 a[76:77], v233 offset:24576\n" \
"v_mul_f32 v243, s31, v243\n" \
"v_mfma_f32_16x16x32_bf16 v[120:123], a[116:119], a[12:15], v[120:123]\n" \
"buffer_load_dwordx4 v228, s[52:55], s89 offen offset:2048 lds\n" \
"v_mfma_f32_16x16x32_bf16 v[116:119], a[104:107], a[8:11], 0\n" \
"ds_read_b64_tr_b16 a[78:79], v233 offset:26624\n" \
"v_mul_f32 v252, s31, v252\n" \
"v_mfma_f32_16x16x32_bf16 v[116:119], a[108:111], a[12:15], v[116:119]\n" \
"ds_read_b64_tr_b16 a[80:81], v232 offset:16896\n" \
"v_mul_f32 v253, s31, v253\n" \
"v_mfma_f32_16x16x32_bf16 v[112:115], a[96:99], a[8:11], 0\n" \
"ds_read_b64_tr_b16 a[82:83], v232 offset:18944\n" \
"v_mul_f32 v254, s31, v254\n" \
"v_mfma_f32_16x16x32_bf16 v[112:115], a[100:103], a[12:15], v[112:115]\n" \
"buffer_load_dwordx4 v229, s[52:55], s89 offen offset:3072 lds\n" \
"v_mfma_f32_16x16x32_bf16 v[144:147], a[96:99], a[16:19], 0\n" \
"s_waitcnt vmcnt(33)\n" \
"s_xor_b32 s89, s89, 0x300000\n" \
"ds_read_b128 a[8:11], v230 offset:2048\n" \
"v_mfma_f32_16x16x32_bf16 v[144:147], a[100:103], a[20:23], v[144:147]\n" \
"s_add_u32 m0, s74, 65536\n" \
"ds_read_b128 a[12:15], v231 offset:2048\n" \
"v_cvt_pk_bf16_f32 v243, v243, v252\n" \
"v_mfma_f32_16x16x32_bf16 v[148:151], a[104:107], a[16:19], 0\n" \
"s_waitcnt lgkmcnt(6)\n" \
"ds_read_b64_tr_b16 a[84:85], v232 offset:25088\n" \
"v_cvt_pk_bf16_f32 v253, v253, v254\n" \
"v_mfma_f32_16x16x32_bf16 v[148:151], a[108:111], a[20:23], v[148:151]\n" \
"buffer_load_dwordx4 v224, s[48:51], s75 offen offset:0 lds\n" \
"v_mfma_f32_16x16x32_bf16 v[152:155], a[112:115], a[16:19], 0\n" \
"ds_read_b64_tr_b16 a[86:87], v232 offset:27136\n" \
"v_accvgpr_write_b32 a224, v243\n" \
"v_mfma_f32_16x16x32_bf16 v[152:155], a[116:119], a[20:23], v[152:155]\n" \
"ds_read_b64_tr_b16 a[88:89], v233 offset:16896\n" \
"v_accvgpr_write_b32 a225, v253\n" \
"v_mfma_f32_16x16x32_bf16 v[156:159], a[120:123], a[16:19], 0\n" \
"ds_read_b64_tr_b16 a[90:91], v233 offset:18944\n" \
"v_accvgpr_read_b32 v243, a240\n" \
"v_mfma_f32_16x16x32_bf16 v[156:159], a[124:127], a[20:23], v[156:159]\n" \
"buffer_load_dwordx4 v225, s[48:51], s75 offen offset:1024 lds\n" \
"v_mfma_f32_16x16x32_bf16 v[188:191], a[120:123], a[24:27], 0\n" \
"s_waitcnt vmcnt(33)\n" \
"ds_read_b128 a[16:19], v230 offset:4096\n" \
"v_accvgpr_read_b32 v252, a241\n" \
"v_mfma_f32_16x16x32_bf16 v[188:191], a[124:127], a[28:31], v[188:191]\n" \
"ds_read_b128 a[20:23], v231 offset:4096\n" \
"v_accvgpr_read_b32 v253, a242\n" \
"v_mfma_f32_16x16x32_bf16 v[184:187], a[112:115], a[24:27], 0\n" \
"s_waitcnt lgkmcnt(6)\n" \
"ds_read_b64_tr_b16 a[92:93], v233 offset:25088\n" \
"v_accvgpr_read_b32 v254, a243\n" \
"v_mfma_f32_16x16x32_bf16 v[184:187], a[116:119], a[28:31], v[184:187]\n" \
"buffer_load_dwordx4 v226, s[48:51], s75 offen offset:2048 lds\n" \
"v_mfma_f32_16x16x32_bf16 v[180:183], a[104:107], a[24:27], 0\n" \
"ds_read_b64_tr_b16 a[94:95], v233 offset:27136\n" \
"v_mul_f32 v243, s31, v243\n" \
"v_mfma_f32_16x16x32_bf16 v[180:183], a[108:111], a[28:31], v[180:183]\n" \
"v_mul_f32 v252, s31, v252\n" \
"v_mul_f32 v253, s31, v253\n" \
"v_mfma_f32_16x16x32_bf16 v[176:179], a[96:99], a[24:27], 0\n" \
"v_mul_f32 v254, s31, v254\n" \
"v_cvt_pk_bf16_f32 v243, v243, v252\n" \
"v_mfma_f32_16x16x32_bf16 v[176:179], a[100:103], a[28:31], v[176:179]\n" \
"buffer_load_dwordx4 v227, s[48:51], s75 offen offset:3072 lds\n" \
"v_mfma_f32_16x16x32_bf16 v[64:67], a[64:67], a[0:3], v[64:67]\n" \
"s_waitcnt vmcnt(33)\n" \
"s_xor_b32 s75, s75, 0x300\n" \
"ds_read_b128 a[24:27], v230 offset:6144\n" \
"v_mfma_f32_16x16x32_bf16 v[64:67], a[68:71], a[4:7], v[64:67]\n" \
"s_add_u32 m0, s74, 69632\n" \
"ds_read_b128 a[28:31], v231 offset:6144\n" \
"v_cvt_pk_bf16_f32 v253, v253, v254\n" \
"v_mfma_f32_16x16x32_bf16 v[68:71], a[72:75], a[0:3], v[68:71]\n" \
"s_waitcnt lgkmcnt(4)\n" \
"ds_read_b64_tr_b16 a[96:97], v232 offset:17408\n" \
"v_accvgpr_write_b32 a226, v243\n" \
"v_mfma_f32_16x16x32_bf16 v[68:71], a[76:79], a[4:7], v[68:71]\n" \
"buffer_load_dwordx4 v224, s[48:51], s77 offen offset:0 lds\n" \
"v_mfma_f32_16x16x32_bf16 v[72:75], a[80:83], a[0:3], v[72:75]\n" \
"ds_read_b64_tr_b16 a[98:99], v232 offset:19456\n" \
"v_accvgpr_write_b32 a227, v253\n" \
"v_mfma_f32_16x16x32_bf16 v[72:75], a[84:87], a[4:7], v[72:75]\n" \
"s_waitcnt lgkmcnt(3)\n" \
"ds_read_b64_tr_b16 a[100:101], v232 offset:25600\n" \
"v_accvgpr_read_b32 v243, a228\n" \
"v_mfma_f32_16x16x32_bf16 v[76:79], a[88:91], a[0:3], v[76:79]\n" \
"ds_read_b64_tr_b16 a[102:103], v232 offset:27648\n" \
"v_accvgpr_read_b32 v252, a229\n" \
"v_mfma_f32_16x16x32_bf16 v[76:79], a[92:95], a[4:7], v[76:79]\n" \
"buffer_load_dwordx4 v225, s[48:51], s77 offen offset:1024 lds\n" \
"v_mfma_f32_16x16x32_bf16 v[108:111], a[88:91], a[8:11], v[108:111]\n" \
"ds_read_b64_tr_b16 a[104:105], v233 offset:17408\n" \
"v_accvgpr_read_b32 v253, a230\n" \
"v_mfma_f32_16x16x32_bf16 v[108:111], a[92:95], a[12:15], v[108:111]\n" \
"ds_read_b64_tr_b16 a[106:107], v233 offset:19456\n" \
"v_accvgpr_read_b32 v254, a231\n" \
"v_mfma_f32_16x16x32_bf16 v[104:107], a[80:83], a[8:11], v[104:107]\n" \
"s_waitcnt lgkmcnt(6)\n" \
"ds_read_b64_tr_b16 a[108:109], v233 offset:25600\n" \
"v_mul_f32 v243, s31, v243\n" \
"v_mfma_f32_16x16x32_bf16 v[104:107], a[84:87], a[12:15], v[104:107]\n" \
"buffer_load_dwordx4 v226, s[48:51], s77 offen offset:2048 lds\n" \
"v_mfma_f32_16x16x32_bf16 v[100:103], a[72:75], a[8:11], v[100:103]\n" \
"ds_read_b64_tr_b16 a[110:111], v233 offset:27648\n" \
"v_mul_f32 v252, s31, v252\n" \
"v_mfma_f32_16x16x32_bf16 v[100:103], a[76:79], a[12:15], v[100:103]\n" \
"ds_read_b64_tr_b16 a[112:113], v232 offset:17920\n" \
"v_mul_f32 v253, s31, v253\n" \
"v_mfma_f32_16x16x32_bf16 v[96:99], a[64:67], a[8:11], v[96:99]\n" \
"ds_read_b64_tr_b16 a[114:115], v232 offset:19968\n" \
"v_mul_f32 v254, s31, v254\n" \
"v_mfma_f32_16x16x32_bf16 v[96:99], a[68:71], a[12:15], v[96:99]\n" \
"buffer_load_dwordx4 v227, s[48:51], s77 offen offset:3072 lds\n" \
"v_mfma_f32_16x16x32_bf16 v[128:131], a[64:67], a[16:19], v[128:131]\n" \
"ds_read_b64_tr_b16 a[116:117], v232 offset:26112\n" \
"v_cvt_pk_bf16_f32 v243, v243, v252\n" \
"v_mfma_f32_16x16x32_bf16 v[128:131], a[68:71], a[20:23], v[128:131]\n" \
"ds_read_b64_tr_b16 a[118:119], v232 offset:28160\n" \
"v_cvt_pk_bf16_f32 v253, v253, v254\n" \
"v_mfma_f32_16x16x32_bf16 v[132:135], a[72:75], a[16:19], v[132:135]\n" \
"s_xor_b32 s77, s77, 0x300\n" \
"ds_read_b64_tr_b16 a[120:121], v233 offset:17920\n" \
"v_accvgpr_write_b32 a228, v243\n" \
"v_mfma_f32_16x16x32_bf16 v[132:135], a[76:79], a[20:23], v[132:135]\n" \
"v_accvgpr_write_b32 a229, v253\n" \
"v_accvgpr_read_b32 v243, a244\n" \
"v_mfma_f32_16x16x32_bf16 v[136:139], a[80:83], a[16:19], v[136:139]\n" \
"ds_read_b64_tr_b16 a[122:123], v233 offset:19968\n" \
"v_accvgpr_read_b32 v252, a245\n" \
"v_mfma_f32_16x16x32_bf16 v[136:139], a[84:87], a[20:23], v[136:139]\n" \
"ds_read_b64_tr_b16 a[124:125], v233 offset:26112\n" \
"v_accvgpr_read_b32 v253, a246\n" \
"v_mfma_f32_16x16x32_bf16 v[140:143], a[88:91], a[16:19], v[140:143]\n" \
"ds_read_b64_tr_b16 a[126:127], v233 offset:28160\n" \
"v_accvgpr_read_b32 v254, a247\n" \
"v_mfma_f32_16x16x32_bf16 v[140:143], a[92:95], a[20:23], v[140:143]\n" \
"buffer_store_dwordx4 v[208:211], v236, s[56:59], s93 offen offset:256 sc1\n" \
"v_mfma_f32_16x16x32_bf16 v[172:175], a[88:91], a[24:27], v[172:175]\n" \
"v_mul_f32 v243, s31, v243\n" \
"v_mul_f32 v252, s31, v252\n" \
"v_mfma_f32_16x16x32_bf16 v[172:175], a[92:95], a[28:31], v[172:175]\n" \
"v_mul_f32 v253, s31, v253\n" \
"v_mul_f32 v254, s31, v254\n" \
"v_mfma_f32_16x16x32_bf16 v[168:171], a[80:83], a[24:27], v[168:171]\n" \
"v_cvt_pk_bf16_f32 v243, v243, v252\n" \
"v_cvt_pk_bf16_f32 v253, v253, v254\n" \
"v_mfma_f32_16x16x32_bf16 v[168:171], a[84:87], a[28:31], v[168:171]\n" \
"v_accvgpr_write_b32 a230, v243\n" \
"v_accvgpr_write_b32 a231, v253\n" \
"v_mfma_f32_16x16x32_bf16 v[164:167], a[72:75], a[24:27], v[164:167]\n" \
"v_accvgpr_read_b32 v243, a232\n" \
"v_accvgpr_read_b32 v252, a233\n" \
"v_mfma_f32_16x16x32_bf16 v[164:167], a[76:79], a[28:31], v[164:167]\n" \
"v_accvgpr_read_b32 v253, a234\n" \
"v_accvgpr_read_b32 v254, a235\n" \
"v_mfma_f32_16x16x32_bf16 v[160:163], a[64:67], a[24:27], v[160:163]\n" \
"v_mul_f32 v243, s31, v243\n" \
"v_mul_f32 v252, s31, v252\n" \
"v_mfma_f32_16x16x32_bf16 v[160:163], a[68:71], a[28:31], v[160:163]\n" \
"s_waitcnt vmcnt(26) lgkmcnt(0)\n" \
"s_barrier\n" \
"v_mfma_f32_16x16x32_bf16 v[80:83], a[96:99], a[0:3], v[80:83]\n" \
"ds_read_b64_tr_b16 a[64:65], v232 offset:32768\n" \
"v_mul_f32 v253, s31, v253\n" \
"v_mfma_f32_16x16x32_bf16 v[80:83], a[100:103], a[4:7], v[80:83]\n" \
"ds_read_b64_tr_b16 a[66:67], v232 offset:34816\n" \
"v_mul_f32 v254, s31, v254\n" \
"v_mfma_f32_16x16x32_bf16 v[84:87], a[104:107], a[0:3], v[84:87]\n" \
"s_add_u32 m0, s79, 16384\n" \
"ds_read_b64_tr_b16 a[68:69], v232 offset:40960\n" \
"v_cvt_pk_bf16_f32 v243, v243, v252\n" \
"v_mfma_f32_16x16x32_bf16 v[84:87], a[108:111], a[4:7], v[84:87]\n" \
"buffer_load_dwordx4 v228, s[52:55], s78 offen offset:0 lds\n" \
"v_mfma_f32_16x16x32_bf16 v[88:91], a[112:115], a[0:3], v[88:91]\n" \
"ds_read_b64_tr_b16 a[70:71], v232 offset:43008\n" \
"v_cvt_pk_bf16_f32 v253, v253, v254\n" \
"v_mfma_f32_16x16x32_bf16 v[88:91], a[116:119], a[4:7], v[88:91]\n" \
"ds_read_b64_tr_b16 a[72:73], v233 offset:32768\n" \
"v_accvgpr_write_b32 a232, v243\n" \
"v_mfma_f32_16x16x32_bf16 v[92:95], a[120:123], a[0:3], v[92:95]\n" \
"ds_read_b64_tr_b16 a[74:75], v233 offset:34816\n" \
"v_accvgpr_write_b32 a233, v253\n" \
"v_mfma_f32_16x16x32_bf16 v[92:95], a[124:127], a[4:7], v[92:95]\n" \
"buffer_load_dwordx4 v229, s[52:55], s78 offen offset:1024 lds\n" \
"v_mfma_f32_16x16x32_bf16 v[124:127], a[120:123], a[8:11], v[124:127]\n" \
"ds_read_b128 a[0:3], v230 offset:32768\n" \
"v_accvgpr_read_b32 v243, a248\n" \
"v_mfma_f32_16x16x32_bf16 v[124:127], a[124:127], a[12:15], v[124:127]\n" \
"ds_read_b128 a[4:7], v231 offset:32768\n" \
"v_accvgpr_read_b32 v252, a249\n" \
"v_mfma_f32_16x16x32_bf16 v[120:123], a[112:115], a[8:11], v[120:123]\n" \
"s_xor_b32 s78, s78, 0x80000\n" \
"ds_read_b64_tr_b16 a[76:77], v233 offset:40960\n" \
"v_accvgpr_read_b32 v253, a250\n" \
"v_mfma_f32_16x16x32_bf16 v[120:123], a[116:119], a[12:15], v[120:123]\n" \
"buffer_load_dwordx4 v228, s[52:55], s89 offen offset:2048 lds\n" \
"v_mfma_f32_16x16x32_bf16 v[116:119], a[104:107], a[8:11], v[116:119]\n" \
"ds_read_b64_tr_b16 a[78:79], v233 offset:43008\n" \
"v_accvgpr_read_b32 v254, a251\n" \
"v_mfma_f32_16x16x32_bf16 v[116:119], a[108:111], a[12:15], v[116:119]\n" \
"ds_read_b64_tr_b16 a[80:81], v232 offset:33280\n" \
"v_mul_f32 v243, s31, v243\n" \
"v_mfma_f32_16x16x32_bf16 v[112:115], a[96:99], a[8:11], v[112:115]\n" \
"ds_read_b64_tr_b16 a[82:83], v232 offset:35328\n" \
"v_mul_f32 v252, s31, v252\n" \
"v_mfma_f32_16x16x32_bf16 v[112:115], a[100:103], a[12:15], v[112:115]\n" \
"buffer_load_dwordx4 v229, s[52:55], s89 offen offset:3072 lds\n" \
"v_mfma_f32_16x16x32_bf16 v[144:147], a[96:99], a[16:19], v[144:147]\n" \
"s_xor_b32 s89, s89, 0x80000\n" \
"ds_read_b128 a[8:11], v230 offset:34816\n" \
"v_mfma_f32_16x16x32_bf16 v[144:147], a[100:103], a[20:23], v[144:147]\n" \
"s_add_u32 m0, s74, 98304\n" \
"ds_read_b128 a[12:15], v231 offset:34816\n" \
"v_mul_f32 v253, s31, v253\n" \
"v_mfma_f32_16x16x32_bf16 v[148:151], a[104:107], a[16:19], v[148:151]\n" \
"s_waitcnt lgkmcnt(6)\n" \
"ds_read_b64_tr_b16 a[84:85], v232 offset:41472\n" \
"v_mul_f32 v254, s31, v254\n" \
"v_mfma_f32_16x16x32_bf16 v[148:151], a[108:111], a[20:23], v[148:151]\n" \
"buffer_load_dwordx4 v224, s[48:51], s75 offen offset:0 lds\n" \
"v_mfma_f32_16x16x32_bf16 v[152:155], a[112:115], a[16:19], v[152:155]\n" \
"ds_read_b64_tr_b16 a[86:87], v232 offset:43520\n" \
"v_cvt_pk_bf16_f32 v243, v243, v252\n" \
"v_mfma_f32_16x16x32_bf16 v[152:155], a[116:119], a[20:23], v[152:155]\n" \
"ds_read_b64_tr_b16 a[88:89], v233 offset:33280\n" \
"v_cvt_pk_bf16_f32 v253, v253, v254\n" \
"v_mfma_f32_16x16x32_bf16 v[156:159], a[120:123], a[16:19], v[156:159]\n" \
"ds_read_b64_tr_b16 a[90:91], v233 offset:35328\n" \
"v_accvgpr_write_b32 a234, v243\n" \
"v_mfma_f32_16x16x32_bf16 v[156:159], a[124:127], a[20:23], v[156:159]\n" \
"buffer_load_dwordx4 v225, s[48:51], s75 offen offset:1024 lds\n" \
"v_mfma_f32_16x16x32_bf16 v[188:191], a[120:123], a[24:27], v[188:191]\n" \
"ds_read_b128 a[16:19], v230 offset:36864\n" \
"v_accvgpr_write_b32 a235, v253\n" \
"v_mfma_f32_16x16x32_bf16 v[188:191], a[124:127], a[28:31], v[188:191]\n" \
"ds_read_b128 a[20:23], v231 offset:36864\n" \
"v_accvgpr_read_b32 v243, a236\n" \
"v_mfma_f32_16x16x32_bf16 v[184:187], a[112:115], a[24:27], v[184:187]\n" \
"s_waitcnt lgkmcnt(6)\n" \
"ds_read_b64_tr_b16 a[92:93], v233 offset:41472\n" \
"v_accvgpr_read_b32 v252, a237\n" \
"v_mfma_f32_16x16x32_bf16 v[184:187], a[116:119], a[28:31], v[184:187]\n" \
"buffer_load_dwordx4 v226, s[48:51], s75 offen offset:2048 lds\n" \
"v_mfma_f32_16x16x32_bf16 v[180:183], a[104:107], a[24:27], v[180:183]\n" \
"ds_read_b64_tr_b16 a[94:95], v233 offset:43520\n" \
"v_accvgpr_read_b32 v253, a238\n" \
"v_mfma_f32_16x16x32_bf16 v[180:183], a[108:111], a[28:31], v[180:183]\n" \
"v_accvgpr_read_b32 v254, a239\n" \
"v_mul_f32 v243, s31, v243\n" \
"v_mfma_f32_16x16x32_bf16 v[176:179], a[96:99], a[24:27], v[176:179]\n" \
"v_mul_f32 v252, s31, v252\n" \
"v_mul_f32 v253, s31, v253\n" \
"v_mfma_f32_16x16x32_bf16 v[176:179], a[100:103], a[28:31], v[176:179]\n" \
"buffer_load_dwordx4 v227, s[48:51], s75 offen offset:3072 lds\n" \
"v_mfma_f32_16x16x32_bf16 v[64:67], a[64:67], a[0:3], v[64:67]\n" \
"s_xor_b32 s75, s75, 0x80\n" \
"ds_read_b128 a[24:27], v230 offset:38912\n" \
"v_mfma_f32_16x16x32_bf16 v[64:67], a[68:71], a[4:7], v[64:67]\n" \
"s_add_u32 m0, s74, 102400\n" \
"ds_read_b128 a[28:31], v231 offset:38912\n" \
"v_mul_f32 v254, s31, v254\n" \
"v_mfma_f32_16x16x32_bf16 v[68:71], a[72:75], a[0:3], v[68:71]\n" \
"s_waitcnt lgkmcnt(4)\n" \
"ds_read_b64_tr_b16 a[96:97], v232 offset:33792\n" \
"v_cvt_pk_bf16_f32 v243, v243, v252\n" \
"v_mfma_f32_16x16x32_bf16 v[68:71], a[76:79], a[4:7], v[68:71]\n" \
"buffer_load_dwordx4 v224, s[48:51], s77 offen offset:0 lds\n" \
"v_mfma_f32_16x16x32_bf16 v[72:75], a[80:83], a[0:3], v[72:75]\n" \
"ds_read_b64_tr_b16 a[98:99], v232 offset:35840\n" \
"v_cvt_pk_bf16_f32 v253, v253, v254\n" \
"v_mfma_f32_16x16x32_bf16 v[72:75], a[84:87], a[4:7], v[72:75]\n" \
"s_waitcnt lgkmcnt(3)\n" \
"ds_read_b64_tr_b16 a[100:101], v232 offset:41984\n" \
"v_accvgpr_write_b32 a236, v243\n" \
"v_mfma_f32_16x16x32_bf16 v[76:79], a[88:91], a[0:3], v[76:79]\n" \
"ds_read_b64_tr_b16 a[102:103], v232 offset:44032\n" \
"v_accvgpr_write_b32 a237, v253\n" \
"v_mfma_f32_16x16x32_bf16 v[76:79], a[92:95], a[4:7], v[76:79]\n" \
"buffer_load_dwordx4 v225, s[48:51], s77 offen offset:1024 lds\n" \
"v_mfma_f32_16x16x32_bf16 v[108:111], a[88:91], a[8:11], v[108:111]\n" \
"ds_read_b64_tr_b16 a[104:105], v233 offset:33792\n" \
"v_accvgpr_read_b32 v243, a252\n" \
"v_mfma_f32_16x16x32_bf16 v[108:111], a[92:95], a[12:15], v[108:111]\n" \
"ds_read_b64_tr_b16 a[106:107], v233 offset:35840\n" \
"v_accvgpr_read_b32 v252, a253\n" \
"v_mfma_f32_16x16x32_bf16 v[104:107], a[80:83], a[8:11], v[104:107]\n" \
"s_waitcnt lgkmcnt(6)\n" \
"ds_read_b64_tr_b16 a[108:109], v233 offset:41984\n" \
"v_accvgpr_read_b32 v253, a254\n" \
"v_mfma_f32_16x16x32_bf16 v[104:107], a[84:87], a[12:15], v[104:107]\n" \
"buffer_load_dwordx4 v226, s[48:51], s77 offen offset:2048 lds\n" \
"v_mfma_f32_16x16x32_bf16 v[100:103], a[72:75], a[8:11], v[100:103]\n" \
"ds_read_b64_tr_b16 a[110:111], v233 offset:44032\n" \
"v_accvgpr_read_b32 v254, a255\n" \
"v_mfma_f32_16x16x32_bf16 v[100:103], a[76:79], a[12:15], v[100:103]\n" \
"ds_read_b64_tr_b16 a[112:113], v232 offset:34304\n" \
"v_mul_f32 v243, s31, v243\n" \
"v_mfma_f32_16x16x32_bf16 v[96:99], a[64:67], a[8:11], v[96:99]\n" \
"ds_read_b64_tr_b16 a[114:115], v232 offset:36352\n" \
"v_mul_f32 v252, s31, v252\n" \
"v_mfma_f32_16x16x32_bf16 v[96:99], a[68:71], a[12:15], v[96:99]\n" \
"buffer_load_dwordx4 v227, s[48:51], s77 offen offset:3072 lds\n" \
"v_mfma_f32_16x16x32_bf16 v[128:131], a[64:67], a[16:19], v[128:131]\n" \
"ds_read_b64_tr_b16 a[116:117], v232 offset:42496\n" \
"v_mul_f32 v253, s31, v253\n" \
"v_mfma_f32_16x16x32_bf16 v[128:131], a[68:71], a[20:23], v[128:131]\n" \
"ds_read_b64_tr_b16 a[118:119], v232 offset:44544\n" \
"v_mul_f32 v254, s31, v254\n" \
"v_mfma_f32_16x16x32_bf16 v[132:135], a[72:75], a[16:19], v[132:135]\n" \
"s_xor_b32 s77, s77, 0x80\n" \
"ds_read_b64_tr_b16 a[120:121], v233 offset:34304\n" \
"v_cvt_pk_bf16_f32 v243, v243, v252\n" \
"v_mfma_f32_16x16x32_bf16 v[132:135], a[76:79], a[20:23], v[132:135]\n" \
"v_cvt_pk_bf16_f32 v253, v253, v254\n" \
"v_accvgpr_write_b32 a238, v243\n" \
"v_mfma_f32_16x16x32_bf16 v[136:139], a[80:83], a[16:19], v[136:139]\n" \
"ds_read_b64_tr_b16 a[122:123], v233 offset:36352\n" \
"v_accvgpr_write_b32 a239, v253\n" \
"v_mfma_f32_16x16x32_bf16 v[136:139], a[84:87], a[20:23], v[136:139]\n" \
"ds_read_b64_tr_b16 a[124:125], v233 offset:42496\n" \
"v_accvgpr_read_b32 v243, a192\n" \
"v_mfma_f32_16x16x32_bf16 v[140:143], a[88:91], a[16:19], v[140:143]\n" \
"ds_read_b64_tr_b16 a[126:127], v233 offset:44544\n" \
"v_accvgpr_read_b32 v252, a193\n" \
"v_mfma_f32_16x16x32_bf16 v[140:143], a[92:95], a[20:23], v[140:143]\n" \
"buffer_store_dwordx4 v[200:203], v236, s[56:59], s93 offen offset:2048 sc1\n" \
"v_mfma_f32_16x16x32_bf16 v[172:175], a[88:91], a[24:27], v[172:175]\n" \
"v_accvgpr_read_b32 v253, a194\n" \
"v_accvgpr_read_b32 v254, a195\n" \
"v_mfma_f32_16x16x32_bf16 v[172:175], a[92:95], a[28:31], v[172:175]\n" \
"v_mul_f32 v243, s31, v243\n" \
"v_mul_f32 v252, s31, v252\n" \
"v_mfma_f32_16x16x32_bf16 v[168:171], a[80:83], a[24:27], v[168:171]\n" \
"v_mul_f32 v253, s31, v253\n" \
"v_mul_f32 v254, s31, v254\n" \
"v_mfma_f32_16x16x32_bf16 v[168:171], a[84:87], a[28:31], v[168:171]\n" \
"v_cvt_pk_bf16_f32 v243, v243, v252\n" \
"v_cvt_pk_bf16_f32 v253, v253, v254\n" \
"v_mfma_f32_16x16x32_bf16 v[164:167], a[72:75], a[24:27], v[164:167]\n" \
"v_accvgpr_write_b32 a192, v243\n" \
"v_accvgpr_write_b32 a193, v253\n" \
"v_mfma_f32_16x16x32_bf16 v[164:167], a[76:79], a[28:31], v[164:167]\n" \
"v_accvgpr_read_b32 v243, a208\n" \
"v_accvgpr_read_b32 v252, a209\n" \
"v_mfma_f32_16x16x32_bf16 v[160:163], a[64:67], a[24:27], v[160:163]\n" \
"v_accvgpr_read_b32 v253, a210\n" \
"v_accvgpr_read_b32 v254, a211\n" \
"v_mfma_f32_16x16x32_bf16 v[160:163], a[68:71], a[28:31], v[160:163]\n" \
"s_waitcnt vmcnt(35) lgkmcnt(0)\n" \
"s_barrier\n" \
"v_mfma_f32_16x16x32_bf16 v[80:83], a[96:99], a[0:3], v[80:83]\n" \
"ds_read_b64_tr_b16 a[64:65], v232 offset:49152\n" \
"v_mul_f32 v243, s31, v243\n" \
"v_mfma_f32_16x16x32_bf16 v[80:83], a[100:103], a[4:7], v[80:83]\n" \
"ds_read_b64_tr_b16 a[66:67], v232 offset:51200\n" \
"v_mul_f32 v252, s31, v252\n" \
"v_mfma_f32_16x16x32_bf16 v[84:87], a[104:107], a[0:3], v[84:87]\n" \
"s_add_u32 m0, s79, 32768\n" \
"ds_read_b64_tr_b16 a[68:69], v232 offset:57344\n" \
"v_mul_f32 v253, s31, v253\n" \
"v_mfma_f32_16x16x32_bf16 v[84:87], a[108:111], a[4:7], v[84:87]\n" \
"buffer_load_dwordx4 v228, s[52:55], s78 offen offset:0 lds\n" \
"v_mfma_f32_16x16x32_bf16 v[88:91], a[112:115], a[0:3], v[88:91]\n" \
"ds_read_b64_tr_b16 a[70:71], v232 offset:59392\n" \
"v_mul_f32 v254, s31, v254\n" \
"v_mfma_f32_16x16x32_bf16 v[88:91], a[116:119], a[4:7], v[88:91]\n" \
"ds_read_b64_tr_b16 a[72:73], v233 offset:49152\n" \
"v_cvt_pk_bf16_f32 v243, v243, v252\n" \
"v_mfma_f32_16x16x32_bf16 v[92:95], a[120:123], a[0:3], v[92:95]\n" \
"ds_read_b64_tr_b16 a[74:75], v233 offset:51200\n" \
"v_cvt_pk_bf16_f32 v253, v253, v254\n" \
"v_mfma_f32_16x16x32_bf16 v[92:95], a[124:127], a[4:7], v[92:95]\n" \
"buffer_load_dwordx4 v229, s[52:55], s78 offen offset:1024 lds\n" \
"v_mfma_f32_16x16x32_bf16 v[124:127], a[120:123], a[8:11], v[124:127]\n" \
"s_waitcnt vmcnt(35)\n" \
"ds_read_b128 a[0:3], v250 offset:0\n" \
"v_accvgpr_write_b32 a194, v243\n" \
"v_mfma_f32_16x16x32_bf16 v[124:127], a[124:127], a[12:15], v[124:127]\n" \
"ds_read_b128 a[4:7], v251 offset:0\n" \
"v_accvgpr_write_b32 a195, v253\n" \
"v_mfma_f32_16x16x32_bf16 v[120:123], a[112:115], a[8:11], v[120:123]\n" \
"s_xor_b32 s78, s78, 0x180000\n" \
"ds_read_b64_tr_b16 a[76:77], v233 offset:57344\n" \
"v_accvgpr_read_b32 v243, a196\n" \
"v_mfma_f32_16x16x32_bf16 v[120:123], a[116:119], a[12:15], v[120:123]\n" \
"buffer_load_dwordx4 v228, s[52:55], s89 offen offset:2048 lds\n" \
"v_mfma_f32_16x16x32_bf16 v[116:119], a[104:107], a[8:11], v[116:119]\n" \
"ds_read_b64_tr_b16 a[78:79], v233 offset:59392\n" \
"v_accvgpr_read_b32 v252, a197\n" \
"v_mfma_f32_16x16x32_bf16 v[116:119], a[108:111], a[12:15], v[116:119]\n" \
"ds_read_b64_tr_b16 a[80:81], v232 offset:49664\n" \
"v_accvgpr_read_b32 v253, a198\n" \
"v_mfma_f32_16x16x32_bf16 v[112:115], a[96:99], a[8:11], v[112:115]\n" \
"ds_read_b64_tr_b16 a[82:83], v232 offset:51712\n" \
"v_accvgpr_read_b32 v254, a199\n" \
"v_mfma_f32_16x16x32_bf16 v[112:115], a[100:103], a[12:15], v[112:115]\n" \
"buffer_load_dwordx4 v229, s[52:55], s89 offen offset:3072 lds\n" \
"v_mfma_f32_16x16x32_bf16 v[144:147], a[96:99], a[16:19], v[144:147]\n" \
"s_waitcnt vmcnt(35)\n" \
"s_xor_b32 s89, s89, 0x180000\n" \
"ds_read_b128 a[8:11], v250 offset:2048\n" \
"v_mfma_f32_16x16x32_bf16 v[144:147], a[100:103], a[20:23], v[144:147]\n" \
"s_add_u32 m0, s74, 131072\n" \
"ds_read_b128 a[12:15], v251 offset:2048\n" \
"v_mul_f32 v243, s31, v243\n" \
"v_mfma_f32_16x16x32_bf16 v[148:151], a[104:107], a[16:19], v[148:151]\n" \
"s_waitcnt lgkmcnt(6)\n" \
"ds_read_b64_tr_b16 a[84:85], v232 offset:57856\n" \
"v_mul_f32 v252, s31, v252\n" \
"v_mfma_f32_16x16x32_bf16 v[148:151], a[108:111], a[20:23], v[148:151]\n" \
"buffer_load_dwordx4 v224, s[48:51], s75 offen offset:0 lds\n" \
"v_mfma_f32_16x16x32_bf16 v[152:155], a[112:115], a[16:19], v[152:155]\n" \
"ds_read_b64_tr_b16 a[86:87], v232 offset:59904\n" \
"v_mul_f32 v253, s31, v253\n" \
"v_mfma_f32_16x16x32_bf16 v[152:155], a[116:119], a[20:23], v[152:155]\n" \
"ds_read_b64_tr_b16 a[88:89], v233 offset:49664\n" \
"v_mul_f32 v254, s31, v254\n" \
"v_mfma_f32_16x16x32_bf16 v[156:159], a[120:123], a[16:19], v[156:159]\n" \
"ds_read_b64_tr_b16 a[90:91], v233 offset:51712\n" \
"v_cvt_pk_bf16_f32 v243, v243, v252\n" \
"v_mfma_f32_16x16x32_bf16 v[156:159], a[124:127], a[20:23], v[156:159]\n" \
"buffer_load_dwordx4 v225, s[48:51], s75 offen offset:1024 lds\n" \
"v_mfma_f32_16x16x32_bf16 v[188:191], a[120:123], a[24:27], v[188:191]\n" \
"s_waitcnt vmcnt(35)\n" \
"ds_read_b128 a[16:19], v250 offset:4096\n" \
"v_cvt_pk_bf16_f32 v253, v253, v254\n" \
"v_mfma_f32_16x16x32_bf16 v[188:191], a[124:127], a[28:31], v[188:191]\n" \
"ds_read_b128 a[20:23], v251 offset:4096\n" \
"v_accvgpr_write_b32 a196, v243\n" \
"v_mfma_f32_16x16x32_bf16 v[184:187], a[112:115], a[24:27], v[184:187]\n" \
"s_waitcnt lgkmcnt(6)\n" \
"ds_read_b64_tr_b16 a[92:93], v233 offset:57856\n" \
"v_accvgpr_write_b32 a197, v253\n" \
"v_mfma_f32_16x16x32_bf16 v[184:187], a[116:119], a[28:31], v[184:187]\n" \
"buffer_load_dwordx4 v226, s[48:51], s75 offen offset:2048 lds\n" \
"v_mfma_f32_16x16x32_bf16 v[180:183], a[104:107], a[24:27], v[180:183]\n" \
"ds_read_b64_tr_b16 a[94:95], v233 offset:59904\n" \
"v_accvgpr_read_b32 v243, a212\n" \
"v_mfma_f32_16x16x32_bf16 v[180:183], a[108:111], a[28:31], v[180:183]\n" \
"v_accvgpr_read_b32 v252, a213\n" \
"v_accvgpr_read_b32 v253, a214\n" \
"v_mfma_f32_16x16x32_bf16 v[176:179], a[96:99], a[24:27], v[176:179]\n" \
"v_accvgpr_read_b32 v254, a215\n" \
"v_mul_f32 v243, s31, v243\n" \
"v_mfma_f32_16x16x32_bf16 v[176:179], a[100:103], a[28:31], v[176:179]\n" \
"buffer_load_dwordx4 v227, s[48:51], s75 offen offset:3072 lds\n" \
"v_mfma_f32_16x16x32_bf16 v[64:67], a[64:67], a[0:3], v[64:67]\n" \
"s_waitcnt vmcnt(34)\n" \
"s_xor_b32 s75, s75, 0x180\n" \
"ds_read_b128 a[24:27], v250 offset:6144\n" \
"v_mfma_f32_16x16x32_bf16 v[64:67], a[68:71], a[4:7], v[64:67]\n" \
"s_add_u32 m0, s74, 135168\n" \
"ds_read_b128 a[28:31], v251 offset:6144\n" \
"v_mul_f32 v252, s31, v252\n" \
"v_mfma_f32_16x16x32_bf16 v[68:71], a[72:75], a[0:3], v[68:71]\n" \
"s_waitcnt lgkmcnt(4)\n" \
"ds_read_b64_tr_b16 a[96:97], v232 offset:50176\n" \
"v_mul_f32 v253, s31, v253\n" \
"v_mfma_f32_16x16x32_bf16 v[68:71], a[76:79], a[4:7], v[68:71]\n" \
"buffer_load_dwordx4 v224, s[48:51], s77 offen offset:0 lds\n" \
"v_mfma_f32_16x16x32_bf16 v[72:75], a[80:83], a[0:3], v[72:75]\n" \
"ds_read_b64_tr_b16 a[98:99], v232 offset:52224\n" \
"v_mul_f32 v254, s31, v254\n" \
"v_mfma_f32_16x16x32_bf16 v[72:75], a[84:87], a[4:7], v[72:75]\n" \
"s_waitcnt lgkmcnt(3)\n" \
"ds_read_b64_tr_b16 a[100:101], v232 offset:58368\n" \
"v_cvt_pk_bf16_f32 v243, v243, v252\n" \
"v_mfma_f32_16x16x32_bf16 v[76:79], a[88:91], a[0:3], v[76:79]\n" \
"ds_read_b64_tr_b16 a[102:103], v232 offset:60416\n" \
"v_cvt_pk_bf16_f32 v253, v253, v254\n" \
"v_mfma_f32_16x16x32_bf16 v[76:79], a[92:95], a[4:7], v[76:79]\n" \
"buffer_load_dwordx4 v225, s[48:51], s77 offen offset:1024 lds\n" \
"v_mfma_f32_16x16x32_bf16 v[108:111], a[88:91], a[8:11], v[108:111]\n" \
"ds_read_b64_tr_b16 a[104:105], v233 offset:50176\n" \
"v_accvgpr_write_b32 a198, v243\n" \
"v_mfma_f32_16x16x32_bf16 v[108:111], a[92:95], a[12:15], v[108:111]\n" \
"ds_read_b64_tr_b16 a[106:107], v233 offset:52224\n" \
"v_accvgpr_write_b32 a199, v253\n" \
"v_mfma_f32_16x16x32_bf16 v[104:107], a[80:83], a[8:11], v[104:107]\n" \
"s_waitcnt lgkmcnt(6)\n" \
"ds_read_b64_tr_b16 a[108:109], v233 offset:58368\n" \
"v_accvgpr_read_b32 v243, a200\n" \
"v_mfma_f32_16x16x32_bf16 v[104:107], a[84:87], a[12:15], v[104:107]\n" \
"buffer_load_dwordx4 v226, s[48:51], s77 offen offset:2048 lds\n" \
"v_mfma_f32_16x16x32_bf16 v[100:103], a[72:75], a[8:11], v[100:103]\n" \
"ds_read_b64_tr_b16 a[110:111], v233 offset:60416\n" \
"v_accvgpr_read_b32 v252, a201\n" \
"v_mfma_f32_16x16x32_bf16 v[100:103], a[76:79], a[12:15], v[100:103]\n" \
"ds_read_b64_tr_b16 a[112:113], v232 offset:50688\n" \
"v_accvgpr_read_b32 v253, a202\n" \
"v_mfma_f32_16x16x32_bf16 v[96:99], a[64:67], a[8:11], v[96:99]\n" \
"Lpg2763_%=:\n" \
"s_cbranch_execz Lpg5010_%=\n" \
"ds_read_b64_tr_b16 a[114:115], v232 offset:52736\n" \
"v_accvgpr_read_b32 v254, a203\n" \
"v_mfma_f32_16x16x32_bf16 v[96:99], a[68:71], a[12:15], v[96:99]\n" \
"buffer_load_dwordx4 v227, s[48:51], s77 offen offset:3072 lds\n" \
"v_mfma_f32_16x16x32_bf16 v[128:131], a[64:67], a[16:19], v[128:131]\n" \
"ds_read_b64_tr_b16 a[116:117], v232 offset:58880\n" \
"v_mul_f32 v243, s31, v243\n" \
"v_mfma_f32_16x16x32_bf16 v[128:131], a[68:71], a[20:23], v[128:131]\n" \
"ds_read_b64_tr_b16 a[118:119], v232 offset:60928\n" \
"v_mul_f32 v252, s31, v252\n" \
"v_mfma_f32_16x16x32_bf16 v[132:135], a[72:75], a[16:19], v[132:135]\n" \
"s_xor_b32 s77, s77, 0x180\n" \
"ds_read_b64_tr_b16 a[120:121], v233 offset:50688\n" \
"v_mul_f32 v253, s31, v253\n" \
"v_mfma_f32_16x16x32_bf16 v[132:135], a[76:79], a[20:23], v[132:135]\n" \
"v_mul_f32 v254, s31, v254\n" \
"v_cvt_pk_bf16_f32 v243, v243, v252\n" \
"v_mfma_f32_16x16x32_bf16 v[136:139], a[80:83], a[16:19], v[136:139]\n" \
"ds_read_b64_tr_b16 a[122:123], v233 offset:52736\n" \
"v_cvt_pk_bf16_f32 v253, v253, v254\n" \
"v_mfma_f32_16x16x32_bf16 v[136:139], a[84:87], a[20:23], v[136:139]\n" \
"ds_read_b64_tr_b16 a[124:125], v233 offset:58880\n" \
"v_accvgpr_write_b32 a200, v243\n" \
"v_mfma_f32_16x16x32_bf16 v[140:143], a[88:91], a[16:19], v[140:143]\n" \
"ds_read_b64_tr_b16 a[126:127], v233 offset:60928\n" \
"v_accvgpr_write_b32 a201, v253\n" \
"v_mfma_f32_16x16x32_bf16 v[140:143], a[92:95], a[20:23], v[140:143]\n" \
"buffer_store_dwordx4 v[216:219], v236, s[56:59], s93 offen offset:2304 sc1\n" \
"v_mfma_f32_16x16x32_bf16 v[172:175], a[88:91], a[24:27], v[172:175]\n" \
"v_accvgpr_read_b32 v243, a216\n" \
"v_accvgpr_read_b32 v252, a217\n" \
"v_mfma_f32_16x16x32_bf16 v[172:175], a[92:95], a[28:31], v[172:175]\n" \
"v_accvgpr_read_b32 v253, a218\n" \
"v_accvgpr_read_b32 v254, a219\n" \
"v_mfma_f32_16x16x32_bf16 v[168:171], a[80:83], a[24:27], v[168:171]\n" \
"v_mul_f32 v243, s31, v243\n" \
"v_mul_f32 v252, s31, v252\n" \
"v_mfma_f32_16x16x32_bf16 v[168:171], a[84:87], a[28:31], v[168:171]\n" \
"v_mul_f32 v253, s31, v253\n" \
"v_mul_f32 v254, s31, v254\n" \
"v_mfma_f32_16x16x32_bf16 v[164:167], a[72:75], a[24:27], v[164:167]\n" \
"v_cvt_pk_bf16_f32 v243, v243, v252\n" \
"v_cvt_pk_bf16_f32 v253, v253, v254\n" \
"v_mfma_f32_16x16x32_bf16 v[164:167], a[76:79], a[28:31], v[164:167]\n" \
"v_accvgpr_write_b32 a202, v243\n" \
"v_accvgpr_write_b32 a203, v253\n" \
"v_mfma_f32_16x16x32_bf16 v[160:163], a[64:67], a[24:27], v[160:163]\n" \
"v_accvgpr_read_b32 v243, a204\n" \
"v_accvgpr_read_b32 v252, a205\n" \
"v_mfma_f32_16x16x32_bf16 v[160:163], a[68:71], a[28:31], v[160:163]\n" \
"s_waitcnt vmcnt(35) lgkmcnt(0)\n" \
"s_barrier\n" \
"v_mfma_f32_16x16x32_bf16 v[80:83], a[96:99], a[0:3], v[80:83]\n" \
"ds_read_b64_tr_b16 a[64:65], v232 offset:0\n" \
"v_accvgpr_read_b32 v253, a206\n" \
"v_mfma_f32_16x16x32_bf16 v[80:83], a[100:103], a[4:7], v[80:83]\n" \
"ds_read_b64_tr_b16 a[66:67], v232 offset:2048\n" \
"v_accvgpr_read_b32 v254, a207\n" \
"v_mfma_f32_16x16x32_bf16 v[84:87], a[104:107], a[0:3], v[84:87]\n" \
"s_add_u32 m0, s79, 49152\n" \
"ds_read_b64_tr_b16 a[68:69], v232 offset:8192\n" \
"v_mul_f32 v243, s31, v243\n" \
"v_mfma_f32_16x16x32_bf16 v[84:87], a[108:111], a[4:7], v[84:87]\n" \
"buffer_load_dwordx4 v228, s[52:55], s78 offen offset:0 lds\n" \
"v_mfma_f32_16x16x32_bf16 v[88:91], a[112:115], a[0:3], v[88:91]\n" \
"ds_read_b64_tr_b16 a[70:71], v232 offset:10240\n" \
"v_mul_f32 v252, s31, v252\n" \
"v_mfma_f32_16x16x32_bf16 v[88:91], a[116:119], a[4:7], v[88:91]\n" \
"ds_read_b64_tr_b16 a[72:73], v233 offset:0\n" \
"v_mul_f32 v253, s31, v253\n" \
"v_mfma_f32_16x16x32_bf16 v[92:95], a[120:123], a[0:3], v[92:95]\n" \
"ds_read_b64_tr_b16 a[74:75], v233 offset:2048\n" \
"v_mul_f32 v254, s31, v254\n" \
"v_mfma_f32_16x16x32_bf16 v[92:95], a[124:127], a[4:7], v[92:95]\n" \
"buffer_load_dwordx4 v229, s[52:55], s78 offen offset:1024 lds\n" \
"v_mfma_f32_16x16x32_bf16 v[124:127], a[120:123], a[8:11], v[124:127]\n" \
"s_waitcnt vmcnt(35)\n" \
"ds_read_b128 a[0:3], v230 offset:0\n" \
"v_cvt_pk_bf16_f32 v243, v243, v252\n" \
"v_mfma_f32_16x16x32_bf16 v[124:127], a[124:127], a[12:15], v[124:127]\n" \
"ds_read_b128 a[4:7], v231 offset:0\n" \
"v_cvt_pk_bf16_f32 v253, v253, v254\n" \
"v_mfma_f32_16x16x32_bf16 v[120:123], a[112:115], a[8:11], v[120:123]\n" \
"s_xor_b32 s78, s78, 0x80000\n" \
"ds_read_b64_tr_b16 a[76:77], v233 offset:8192\n" \
"v_accvgpr_write_b32 a204, v243\n" \
"v_mfma_f32_16x16x32_bf16 v[120:123], a[116:119], a[12:15], v[120:123]\n" \
"buffer_load_dwordx4 v228, s[52:55], s89 offen offset:2048 lds\n" \
"v_mfma_f32_16x16x32_bf16 v[116:119], a[104:107], a[8:11], v[116:119]\n" \
"ds_read_b64_tr_b16 a[78:79], v233 offset:10240\n" \
"v_accvgpr_write_b32 a205, v253\n" \
"v_mfma_f32_16x16x32_bf16 v[116:119], a[108:111], a[12:15], v[116:119]\n" \
"ds_read_b64_tr_b16 a[80:81], v232 offset:512\n" \
"v_accvgpr_read_b32 v243, a220\n" \
"v_mfma_f32_16x16x32_bf16 v[112:115], a[96:99], a[8:11], v[112:115]\n" \
"ds_read_b64_tr_b16 a[82:83], v232 offset:2560\n" \
"v_accvgpr_read_b32 v252, a221\n" \
"v_mfma_f32_16x16x32_bf16 v[112:115], a[100:103], a[12:15], v[112:115]\n" \
"buffer_load_dwordx4 v229, s[52:55], s89 offen offset:3072 lds\n" \
"v_mfma_f32_16x16x32_bf16 v[144:147], a[96:99], a[16:19], v[144:147]\n" \
"s_waitcnt vmcnt(35)\n" \
"s_xor_b32 s89, s89, 0x80000\n" \
"ds_read_b128 a[8:11], v230 offset:2048\n" \
"v_mfma_f32_16x16x32_bf16 v[144:147], a[100:103], a[20:23], v[144:147]\n" \
"s_add_u32 m0, s74, 65536\n" \
"ds_read_b128 a[12:15], v231 offset:2048\n" \
"v_accvgpr_read_b32 v253, a222\n" \
"v_mfma_f32_16x16x32_bf16 v[148:151], a[104:107], a[16:19], v[148:151]\n" \
"s_waitcnt lgkmcnt(6)\n" \
"ds_read_b64_tr_b16 a[84:85], v232 offset:8704\n" \
"v_accvgpr_read_b32 v254, a223\n" \
"v_mfma_f32_16x16x32_bf16 v[148:151], a[108:111], a[20:23], v[148:151]\n" \
"buffer_load_dwordx4 v224, s[48:51], s75 offen offset:0 lds\n" \
"v_mfma_f32_16x16x32_bf16 v[152:155], a[112:115], a[16:19], v[152:155]\n" \
"ds_read_b64_tr_b16 a[86:87], v232 offset:10752\n" \
"v_mul_f32 v243, s31, v243\n" \
"v_mfma_f32_16x16x32_bf16 v[152:155], a[116:119], a[20:23], v[152:155]\n" \
"ds_read_b64_tr_b16 a[88:89], v233 offset:512\n" \
"v_mul_f32 v252, s31, v252\n" \
"v_mfma_f32_16x16x32_bf16 v[156:159], a[120:123], a[16:19], v[156:159]\n" \
"ds_read_b64_tr_b16 a[90:91], v233 offset:2560\n" \
"v_mul_f32 v253, s31, v253\n" \
"v_mfma_f32_16x16x32_bf16 v[156:159], a[124:127], a[20:23], v[156:159]\n" \
"buffer_load_dwordx4 v225, s[48:51], s75 offen offset:1024 lds\n" \
"v_mfma_f32_16x16x32_bf16 v[188:191], a[120:123], a[24:27], v[188:191]\n" \
"s_waitcnt vmcnt(35)\n" \
"ds_read_b128 a[16:19], v230 offset:4096\n" \
"v_mul_f32 v254, s31, v254\n" \
"v_mfma_f32_16x16x32_bf16 v[188:191], a[124:127], a[28:31], v[188:191]\n" \
"ds_read_b128 a[20:23], v231 offset:4096\n" \
"v_cvt_pk_bf16_f32 v243, v243, v252\n" \
"v_mfma_f32_16x16x32_bf16 v[184:187], a[112:115], a[24:27], v[184:187]\n" \
"s_waitcnt lgkmcnt(6)\n" \
"ds_read_b64_tr_b16 a[92:93], v233 offset:8704\n" \
"v_cvt_pk_bf16_f32 v253, v253, v254\n" \
"v_mfma_f32_16x16x32_bf16 v[184:187], a[116:119], a[28:31], v[184:187]\n" \
"buffer_load_dwordx4 v226, s[48:51], s75 offen offset:2048 lds\n" \
"v_mfma_f32_16x16x32_bf16 v[180:183], a[104:107], a[24:27], v[180:183]\n" \
"ds_read_b64_tr_b16 a[94:95], v233 offset:10752\n" \
"v_accvgpr_write_b32 a206, v243\n" \
"v_mfma_f32_16x16x32_bf16 v[180:183], a[108:111], a[28:31], v[180:183]\n" \
"v_accvgpr_write_b32 a207, v253\n" \
"v_mul_f32 v32, s76, v32\n" \
"v_mfma_f32_16x16x32_bf16 v[176:179], a[96:99], a[24:27], v[176:179]\n" \
"v_mul_f32 v33, s76, v33\n" \
"v_mul_f32 v34, s76, v34\n" \
"v_mfma_f32_16x16x32_bf16 v[176:179], a[100:103], a[28:31], v[176:179]\n" \
"buffer_load_dwordx4 v227, s[48:51], s75 offen offset:3072 lds\n" \
"v_mfma_f32_16x16x32_bf16 v[64:67], a[64:67], a[0:3], v[64:67]\n" \
"s_waitcnt vmcnt(35)\n" \
"s_xor_b32 s75, s75, 0x80\n" \
"ds_read_b128 a[24:27], v230 offset:6144\n" \
"v_mfma_f32_16x16x32_bf16 v[64:67], a[68:71], a[4:7], v[64:67]\n" \
"s_add_u32 m0, s74, 69632\n" \
"ds_read_b128 a[28:31], v231 offset:6144\n" \
"v_mul_f32 v35, s76, v35\n" \
"v_mfma_f32_16x16x32_bf16 v[68:71], a[72:75], a[0:3], v[68:71]\n" \
"s_waitcnt lgkmcnt(4)\n" \
"ds_read_b64_tr_b16 a[96:97], v232 offset:1024\n" \
"v_cvt_pk_bf16_f32 v32, v32, v33\n" \
"v_mfma_f32_16x16x32_bf16 v[68:71], a[76:79], a[4:7], v[68:71]\n" \
"buffer_load_dwordx4 v224, s[48:51], s77 offen offset:0 lds\n" \
"v_mfma_f32_16x16x32_bf16 v[72:75], a[80:83], a[0:3], v[72:75]\n" \
"ds_read_b64_tr_b16 a[98:99], v232 offset:3072\n" \
"v_cvt_pk_bf16_f32 v33, v34, v35\n" \
"v_mfma_f32_16x16x32_bf16 v[72:75], a[84:87], a[4:7], v[72:75]\n" \
"s_waitcnt lgkmcnt(3)\n" \
"ds_read_b64_tr_b16 a[100:101], v232 offset:9216\n" \
"v_mul_f32 v36, s76, v36\n" \
"v_mfma_f32_16x16x32_bf16 v[76:79], a[88:91], a[0:3], v[76:79]\n" \
"ds_read_b64_tr_b16 a[102:103], v232 offset:11264\n" \
"v_mul_f32 v37, s76, v37\n" \
"v_mfma_f32_16x16x32_bf16 v[76:79], a[92:95], a[4:7], v[76:79]\n" \
"buffer_load_dwordx4 v225, s[48:51], s77 offen offset:1024 lds\n" \
"v_mfma_f32_16x16x32_bf16 v[108:111], a[88:91], a[8:11], v[108:111]\n" \
"ds_read_b64_tr_b16 a[104:105], v233 offset:1024\n" \
"v_mul_f32 v38, s76, v38\n" \
"v_mfma_f32_16x16x32_bf16 v[108:111], a[92:95], a[12:15], v[108:111]\n" \
"ds_read_b64_tr_b16 a[106:107], v233 offset:3072\n" \
"v_mul_f32 v39, s76, v39\n" \
"v_mfma_f32_16x16x32_bf16 v[104:107], a[80:83], a[8:11], v[104:107]\n" \
"s_waitcnt lgkmcnt(6)\n" \
"ds_read_b64_tr_b16 a[108:109], v233 offset:9216\n" \
"v_cvt_pk_bf16_f32 v34, v36, v37\n" \
"v_mfma_f32_16x16x32_bf16 v[104:107], a[84:87], a[12:15], v[104:107]\n" \
"buffer_load_dwordx4 v226, s[48:51], s77 offen offset:2048 lds\n" \
"v_mfma_f32_16x16x32_bf16 v[100:103], a[72:75], a[8:11], v[100:103]\n" \
"ds_read_b64_tr_b16 a[110:111], v233 offset:11264\n" \
"v_cvt_pk_bf16_f32 v35, v38, v39\n" \
"v_mfma_f32_16x16x32_bf16 v[100:103], a[76:79], a[12:15], v[100:103]\n" \
"ds_read_b64_tr_b16 a[112:113], v232 offset:1536\n" \
"v_mul_f32 v48, s76, v48\n" \
"v_mfma_f32_16x16x32_bf16 v[96:99], a[64:67], a[8:11], v[96:99]\n" \
"ds_read_b64_tr_b16 a[114:115], v232 offset:3584\n" \
"v_mul_f32 v49, s76, v49\n" \
"v_mfma_f32_16x16x32_bf16 v[96:99], a[68:71], a[12:15], v[96:99]\n" \
"buffer_load_dwordx4 v227, s[48:51], s77 offen offset:3072 lds\n" \
"v_mfma_f32_16x16x32_bf16 v[128:131], a[64:67], a[16:19], v[128:131]\n" \
"ds_read_b64_tr_b16 a[116:117], v232 offset:9728\n" \
"v_mul_f32 v50, s76, v50\n" \
"v_mfma_f32_16x16x32_bf16 v[128:131], a[68:71], a[20:23], v[128:131]\n" \
"ds_read_b64_tr_b16 a[118:119], v232 offset:11776\n" \
"v_mul_f32 v51, s76, v51\n" \
"v_mfma_f32_16x16x32_bf16 v[132:135], a[72:75], a[16:19], v[132:135]\n" \
"s_xor_b32 s77, s77, 0x80\n" \
"ds_read_b64_tr_b16 a[120:121], v233 offset:1536\n" \
"v_cvt_pk_bf16_f32 v48, v48, v49\n" \
"v_mfma_f32_16x16x32_bf16 v[132:135], a[76:79], a[20:23], v[132:135]\n" \
"v_cvt_pk_bf16_f32 v49, v50, v51\n" \
"v_mul_f32 v52, s76, v52\n" \
"v_mfma_f32_16x16x32_bf16 v[136:139], a[80:83], a[16:19], v[136:139]\n" \
"ds_read_b64_tr_b16 a[122:123], v233 offset:3584\n" \
"v_mul_f32 v53, s76, v53\n" \
"v_mfma_f32_16x16x32_bf16 v[136:139], a[84:87], a[20:23], v[136:139]\n" \
"ds_read_b64_tr_b16 a[124:125], v233 offset:9728\n" \
"v_mul_f32 v54, s76, v54\n" \
"v_mfma_f32_16x16x32_bf16 v[140:143], a[88:91], a[16:19], v[140:143]\n" \
"s_add_u32 s93, s87, 0\n" \
"ds_read_b64_tr_b16 a[126:127], v233 offset:11776\n" \
"v_mul_f32 v55, s76, v55\n" \
"v_mfma_f32_16x16x32_bf16 v[140:143], a[92:95], a[20:23], v[140:143]\n" \
"buffer_store_dwordx4 v[196:199], v236, s[56:59], s93 offen offset:0 sc1\n" \
"v_mfma_f32_16x16x32_bf16 v[172:175], a[88:91], a[24:27], v[172:175]\n" \
"v_cvt_pk_bf16_f32 v50, v52, v53\n" \
"v_cvt_pk_bf16_f32 v51, v54, v55\n" \
"v_mfma_f32_16x16x32_bf16 v[172:175], a[92:95], a[28:31], v[172:175]\n" \
"v_mul_f32 v40, s76, v40\n" \
"v_mul_f32 v41, s76, v41\n" \
"v_mfma_f32_16x16x32_bf16 v[168:171], a[80:83], a[24:27], v[168:171]\n" \
"v_mul_f32 v42, s76, v42\n" \
"v_mul_f32 v43, s76, v43\n" \
"v_mfma_f32_16x16x32_bf16 v[168:171], a[84:87], a[28:31], v[168:171]\n" \
"v_cvt_pk_bf16_f32 v40, v40, v41\n" \
"v_cvt_pk_bf16_f32 v41, v42, v43\n" \
"v_mfma_f32_16x16x32_bf16 v[164:167], a[72:75], a[24:27], v[164:167]\n" \
"v_mul_f32 v44, s76, v44\n" \
"v_mul_f32 v45, s76, v45\n" \
"v_mfma_f32_16x16x32_bf16 v[164:167], a[76:79], a[28:31], v[164:167]\n" \
"v_mul_f32 v46, s76, v46\n" \
"v_mul_f32 v47, s76, v47\n" \
"v_mfma_f32_16x16x32_bf16 v[160:163], a[64:67], a[24:27], v[160:163]\n" \
"v_cvt_pk_bf16_f32 v42, v44, v45\n" \
"v_cvt_pk_bf16_f32 v43, v46, v47\n" \
"v_mfma_f32_16x16x32_bf16 v[160:163], a[68:71], a[28:31], v[160:163]\n" \
"s_waitcnt vmcnt(35) lgkmcnt(0)\n" \
"s_barrier\n" \
"v_mfma_f32_16x16x32_bf16 v[80:83], a[96:99], a[0:3], v[80:83]\n" \
"ds_read_b64_tr_b16 a[64:65], v232 offset:16384\n" \
"v_mul_f32 v56, s76, v56\n" \
"v_mfma_f32_16x16x32_bf16 v[80:83], a[100:103], a[4:7], v[80:83]\n" \
"ds_read_b64_tr_b16 a[66:67], v232 offset:18432\n" \
"v_mul_f32 v57, s76, v57\n" \
"v_mfma_f32_16x16x32_bf16 v[84:87], a[104:107], a[0:3], v[84:87]\n" \
"s_add_u32 m0, s79, 0\n" \
"ds_read_b64_tr_b16 a[68:69], v232 offset:24576\n" \
"v_mul_f32 v58, s76, v58\n" \
"v_mfma_f32_16x16x32_bf16 v[84:87], a[108:111], a[4:7], v[84:87]\n" \
"buffer_load_dwordx4 v228, s[52:55], s78 offen offset:0 lds\n" \
"v_mfma_f32_16x16x32_bf16 v[88:91], a[112:115], a[0:3], v[88:91]\n" \
"ds_read_b64_tr_b16 a[70:71], v232 offset:26624\n" \
"v_mul_f32 v59, s76, v59\n" \
"v_mfma_f32_16x16x32_bf16 v[88:91], a[116:119], a[4:7], v[88:91]\n" \
"ds_read_b64_tr_b16 a[72:73], v233 offset:16384\n" \
"v_cvt_pk_bf16_f32 v56, v56, v57\n" \
"v_mfma_f32_16x16x32_bf16 v[92:95], a[120:123], a[0:3], v[92:95]\n" \
"ds_read_b64_tr_b16 a[74:75], v233 offset:18432\n" \
"v_cvt_pk_bf16_f32 v57, v58, v59\n" \
"v_mfma_f32_16x16x32_bf16 v[92:95], a[124:127], a[4:7], v[92:95]\n" \
"buffer_load_dwordx4 v229, s[52:55], s78 offen offset:1024 lds\n" \
"v_mfma_f32_16x16x32_bf16 v[124:127], a[120:123], a[8:11], v[124:127]\n" \
"s_waitcnt vmcnt(35)\n" \
"ds_read_b128 a[0:3], v230 offset:32768\n" \
"v_mul_f32 v60, s76, v60\n" \
"v_mfma_f32_16x16x32_bf16 v[124:127], a[124:127], a[12:15], v[124:127]\n" \
"ds_read_b128 a[4:7], v231 offset:32768\n" \
"v_mul_f32 v61, s76, v61\n" \
"v_mfma_f32_16x16x32_bf16 v[120:123], a[112:115], a[8:11], v[120:123]\n" \
"s_xor_b32 s78, s78, 0x780000\n" \
"ds_read_b64_tr_b16 a[76:77], v233 offset:24576\n" \
"v_mul_f32 v62, s76, v62\n" \
"v_mfma_f32_16x16x32_bf16 v[120:123], a[116:119], a[12:15], v[120:123]\n" \
"buffer_load_dwordx4 v228, s[52:55], s89 offen offset:2048 lds\n" \
"v_mfma_f32_16x16x32_bf16 v[116:119], a[104:107], a[8:11], v[116:119]\n" \
"ds_read_b64_tr_b16 a[78:79], v233 offset:26624\n" \
"v_mul_f32 v63, s76, v63\n" \
"v_mfma_f32_16x16x32_bf16 v[116:119], a[108:111], a[12:15], v[116:119]\n" \
"ds_read_b64_tr_b16 a[80:81], v232 offset:16896\n" \
"v_cvt_pk_bf16_f32 v58, v60, v61\n" \
"v_mfma_f32_16x16x32_bf16 v[112:115], a[96:99], a[8:11], v[112:115]\n" \
"ds_read_b64_tr_b16 a[82:83], v232 offset:18944\n" \
"v_cvt_pk_bf16_f32 v59, v62, v63\n" \
"v_mfma_f32_16x16x32_bf16 v[112:115], a[100:103], a[12:15], v[112:115]\n" \
"buffer_load_dwordx4 v229, s[52:55], s89 offen offset:3072 lds\n" \
"v_mfma_f32_16x16x32_bf16 v[144:147], a[96:99], a[16:19], v[144:147]\n" \
"s_waitcnt vmcnt(35)\n" \
"s_xor_b32 s89, s89, 0x780000\n" \
"ds_read_b128 a[8:11], v230 offset:34816\n" \
"v_mfma_f32_16x16x32_bf16 v[144:147], a[100:103], a[20:23], v[144:147]\n" \
"s_add_u32 m0, s74, 98304\n" \
"ds_read_b128 a[12:15], v231 offset:34816\n" \
"v_mul_f32 v0, s76, v0\n" \
"v_mfma_f32_16x16x32_bf16 v[148:151], a[104:107], a[16:19], v[148:151]\n" \
"s_waitcnt lgkmcnt(6)\n" \
"ds_read_b64_tr_b16 a[84:85], v232 offset:25088\n" \
"v_mul_f32 v1, s76, v1\n" \
"v_mfma_f32_16x16x32_bf16 v[148:151], a[108:111], a[20:23], v[148:151]\n" \
"buffer_load_dwordx4 v224, s[48:51], s75 offen offset:0 lds\n" \
"v_mfma_f32_16x16x32_bf16 v[152:155], a[112:115], a[16:19], v[152:155]\n" \
"ds_read_b64_tr_b16 a[86:87], v232 offset:27136\n" \
"v_mul_f32 v2, s76, v2\n" \
"v_mfma_f32_16x16x32_bf16 v[152:155], a[116:119], a[20:23], v[152:155]\n" \
"ds_read_b64_tr_b16 a[88:89], v233 offset:16896\n" \
"v_mul_f32 v3, s76, v3\n" \
"v_mfma_f32_16x16x32_bf16 v[156:159], a[120:123], a[16:19], v[156:159]\n" \
"ds_read_b64_tr_b16 a[90:91], v233 offset:18944\n" \
"v_cvt_pk_bf16_f32 v36, v0, v1\n" \
"v_mfma_f32_16x16x32_bf16 v[156:159], a[124:127], a[20:23], v[156:159]\n" \
"buffer_load_dwordx4 v225, s[48:51], s75 offen offset:1024 lds\n" \
"v_mfma_f32_16x16x32_bf16 v[188:191], a[120:123], a[24:27], v[188:191]\n" \
"s_waitcnt vmcnt(35)\n" \
"ds_read_b128 a[16:19], v230 offset:36864\n" \
"v_cvt_pk_bf16_f32 v37, v2, v3\n" \
"v_mfma_f32_16x16x32_bf16 v[188:191], a[124:127], a[28:31], v[188:191]\n" \
"ds_read_b128 a[20:23], v231 offset:36864\n" \
"v_mul_f32 v4, s76, v4\n" \
"v_mfma_f32_16x16x32_bf16 v[184:187], a[112:115], a[24:27], v[184:187]\n" \
"s_waitcnt lgkmcnt(6)\n" \
"ds_read_b64_tr_b16 a[92:93], v233 offset:25088\n" \
"v_mul_f32 v5, s76, v5\n" \
"v_mfma_f32_16x16x32_bf16 v[184:187], a[116:119], a[28:31], v[184:187]\n" \
"buffer_load_dwordx4 v226, s[48:51], s75 offen offset:2048 lds\n" \
"v_mfma_f32_16x16x32_bf16 v[180:183], a[104:107], a[24:27], v[180:183]\n" \
"ds_read_b64_tr_b16 a[94:95], v233 offset:27136\n" \
"v_mul_f32 v6, s76, v6\n" \
"v_mfma_f32_16x16x32_bf16 v[180:183], a[108:111], a[28:31], v[180:183]\n" \
"v_mul_f32 v7, s76, v7\n" \
"v_cvt_pk_bf16_f32 v38, v4, v5\n" \
"v_mfma_f32_16x16x32_bf16 v[176:179], a[96:99], a[24:27], v[176:179]\n" \
"v_cvt_pk_bf16_f32 v39, v6, v7\n" \
"v_mul_f32 v16, s76, v16\n" \
"v_mfma_f32_16x16x32_bf16 v[176:179], a[100:103], a[28:31], v[176:179]\n" \
"buffer_load_dwordx4 v227, s[48:51], s75 offen offset:3072 lds\n" \
"v_mfma_f32_16x16x32_bf16 v[64:67], a[64:67], a[0:3], v[64:67]\n" \
"s_waitcnt vmcnt(35)\n" \
"s_xor_b32 s75, s75, 0x780\n" \
"ds_read_b128 a[24:27], v230 offset:38912\n" \
"v_mfma_f32_16x16x32_bf16 v[64:67], a[68:71], a[4:7], v[64:67]\n" \
"s_add_u32 m0, s74, 102400\n" \
"ds_read_b128 a[28:31], v231 offset:38912\n" \
"v_mul_f32 v17, s76, v17\n" \
"v_mfma_f32_16x16x32_bf16 v[68:71], a[72:75], a[0:3], v[68:71]\n" \
"s_waitcnt lgkmcnt(4)\n" \
"ds_read_b64_tr_b16 a[96:97], v232 offset:17408\n" \
"v_mul_f32 v18, s76, v18\n" \
"v_mfma_f32_16x16x32_bf16 v[68:71], a[76:79], a[4:7], v[68:71]\n" \
"buffer_load_dwordx4 v224, s[48:51], s77 offen offset:0 lds\n" \
"v_mfma_f32_16x16x32_bf16 v[72:75], a[80:83], a[0:3], v[72:75]\n" \
"ds_read_b64_tr_b16 a[98:99], v232 offset:19456\n" \
"v_mul_f32 v19, s76, v19\n" \
"v_mfma_f32_16x16x32_bf16 v[72:75], a[84:87], a[4:7], v[72:75]\n" \
"s_waitcnt lgkmcnt(3)\n" \
"ds_read_b64_tr_b16 a[100:101], v232 offset:25600\n" \
"v_cvt_pk_bf16_f32 v16, v16, v17\n" \
"v_mfma_f32_16x16x32_bf16 v[76:79], a[88:91], a[0:3], v[76:79]\n" \
"ds_read_b64_tr_b16 a[102:103], v232 offset:27648\n" \
"v_cvt_pk_bf16_f32 v17, v18, v19\n" \
"v_mfma_f32_16x16x32_bf16 v[76:79], a[92:95], a[4:7], v[76:79]\n" \
"buffer_load_dwordx4 v225, s[48:51], s77 offen offset:1024 lds\n" \
"v_mfma_f32_16x16x32_bf16 v[108:111], a[88:91], a[8:11], v[108:111]\n" \
"ds_read_b64_tr_b16 a[104:105], v233 offset:17408\n" \
"v_mul_f32 v20, s76, v20\n" \
"v_mfma_f32_16x16x32_bf16 v[108:111], a[92:95], a[12:15], v[108:111]\n" \
"ds_read_b64_tr_b16 a[106:107], v233 offset:19456\n" \
"v_mul_f32 v21, s76, v21\n" \
"v_mfma_f32_16x16x32_bf16 v[104:107], a[80:83], a[8:11], v[104:107]\n" \
"s_waitcnt lgkmcnt(6)\n" \
"ds_read_b64_tr_b16 a[108:109], v233 offset:25600\n" \
"v_mul_f32 v22, s76, v22\n" \
"v_mfma_f32_16x16x32_bf16 v[104:107], a[84:87], a[12:15], v[104:107]\n" \
"buffer_load_dwordx4 v226, s[48:51], s77 offen offset:2048 lds\n" \
"v_mfma_f32_16x16x32_bf16 v[100:103], a[72:75], a[8:11], v[100:103]\n" \
"ds_read_b64_tr_b16 a[110:111], v233 offset:27648\n" \
"v_mul_f32 v23, s76, v23\n" \
"v_mfma_f32_16x16x32_bf16 v[100:103], a[76:79], a[12:15], v[100:103]\n" \
"ds_read_b64_tr_b16 a[112:113], v232 offset:17920\n" \
"v_cvt_pk_bf16_f32 v18, v20, v21\n" \
"v_mfma_f32_16x16x32_bf16 v[96:99], a[64:67], a[8:11], v[96:99]\n" \
"ds_read_b64_tr_b16 a[114:115], v232 offset:19968\n" \
"v_cvt_pk_bf16_f32 v19, v22, v23\n" \
"v_mfma_f32_16x16x32_bf16 v[96:99], a[68:71], a[12:15], v[96:99]\n" \
"buffer_load_dwordx4 v227, s[48:51], s77 offen offset:3072 lds\n" \
"v_mfma_f32_16x16x32_bf16 v[128:131], a[64:67], a[16:19], v[128:131]\n" \
"ds_read_b64_tr_b16 a[116:117], v232 offset:26112\n" \
"v_mul_f32 v8, s76, v8\n" \
"v_mfma_f32_16x16x32_bf16 v[128:131], a[68:71], a[20:23], v[128:131]\n" \
"ds_read_b64_tr_b16 a[118:119], v232 offset:28160\n" \
"v_mul_f32 v9, s76, v9\n" \
"v_mfma_f32_16x16x32_bf16 v[132:135], a[72:75], a[16:19], v[132:135]\n" \
"s_xor_b32 s77, s77, 0x780\n" \
"ds_read_b64_tr_b16 a[120:121], v233 offset:17920\n" \
"v_mul_f32 v10, s76, v10\n" \
"v_mfma_f32_16x16x32_bf16 v[132:135], a[76:79], a[20:23], v[132:135]\n" \
"v_mul_f32 v11, s76, v11\n" \
"v_cvt_pk_bf16_f32 v52, v8, v9\n" \
"v_mfma_f32_16x16x32_bf16 v[136:139], a[80:83], a[16:19], v[136:139]\n" \
"ds_read_b64_tr_b16 a[122:123], v233 offset:19968\n" \
"v_cvt_pk_bf16_f32 v53, v10, v11\n" \
"v_mfma_f32_16x16x32_bf16 v[136:139], a[84:87], a[20:23], v[136:139]\n" \
"ds_read_b64_tr_b16 a[124:125], v233 offset:26112\n" \
"v_mul_f32 v12, s76, v12\n" \
"v_mfma_f32_16x16x32_bf16 v[140:143], a[88:91], a[16:19], v[140:143]\n" \
"ds_read_b64_tr_b16 a[126:127], v233 offset:28160\n" \
"v_mul_f32 v13, s76, v13\n" \
"v_mfma_f32_16x16x32_bf16 v[140:143], a[92:95], a[20:23], v[140:143]\n" \
"buffer_store_dwordx4 v[212:215], v236, s[56:59], s93 offen offset:256 sc1\n" \
"v_mfma_f32_16x16x32_bf16 v[172:175], a[88:91], a[24:27], v[172:175]\n" \
"v_mul_f32 v14, s76, v14\n" \
"v_mul_f32 v15, s76, v15\n" \
"v_mfma_f32_16x16x32_bf16 v[172:175], a[92:95], a[28:31], v[172:175]\n" \
"v_cvt_pk_bf16_f32 v54, v12, v13\n" \
"v_cvt_pk_bf16_f32 v55, v14, v15\n" \
"v_mfma_f32_16x16x32_bf16 v[168:171], a[80:83], a[24:27], v[168:171]\n" \
"v_mul_f32 v24, s76, v24\n" \
"v_mul_f32 v25, s76, v25\n" \
"v_mfma_f32_16x16x32_bf16 v[168:171], a[84:87], a[28:31], v[168:171]\n" \
"v_mul_f32 v26, s76, v26\n" \
"v_mul_f32 v27, s76, v27\n" \
"v_mfma_f32_16x16x32_bf16 v[164:167], a[72:75], a[24:27], v[164:167]\n" \
"v_cvt_pk_bf16_f32 v24, v24, v25\n" \
"v_cvt_pk_bf16_f32 v25, v26, v27\n" \
"v_mfma_f32_16x16x32_bf16 v[164:167], a[76:79], a[28:31], v[164:167]\n" \
"v_mul_f32 v28, s76, v28\n" \
"v_mul_f32 v29, s76, v29\n" \
"v_mfma_f32_16x16x32_bf16 v[160:163], a[64:67], a[24:27], v[160:163]\n" \
"v_mul_f32 v30, s76, v30\n" \
"v_mul_f32 v31, s76, v31\n" \
"v_mfma_f32_16x16x32_bf16 v[160:163], a[68:71], a[28:31], v[160:163]\n" \
"s_waitcnt vmcnt(35) lgkmcnt(0)\n" \
"s_barrier\n" \
"v_mfma_f32_16x16x32_bf16 v[80:83], a[96:99], a[0:3], v[80:83]\n" \
"ds_read_b64_tr_b16 a[64:65], v232 offset:32768\n" \
"v_cvt_pk_bf16_f32 v26, v28, v29\n" \
"v_mfma_f32_16x16x32_bf16 v[80:83], a[100:103], a[4:7], v[80:83]\n" \
"ds_read_b64_tr_b16 a[66:67], v232 offset:34816\n" \
"v_cvt_pk_bf16_f32 v27, v30, v31\n" \
"v_mfma_f32_16x16x32_bf16 v[84:87], a[104:107], a[0:3], v[84:87]\n" \
"s_add_u32 m0, s79, 16384\n" \
"ds_read_b64_tr_b16 a[68:69], v232 offset:40960\n" \
"v_accvgpr_read_b32 v243, a160\n" \
"v_mfma_f32_16x16x32_bf16 v[84:87], a[108:111], a[4:7], v[84:87]\n" \
"buffer_load_dwordx4 v228, s[52:55], s78 offen offset:0 lds\n" \
"v_mfma_f32_16x16x32_bf16 v[88:91], a[112:115], a[0:3], v[88:91]\n" \
"ds_read_b64_tr_b16 a[70:71], v232 offset:43008\n" \
"v_accvgpr_read_b32 v252, a161\n" \
"v_mfma_f32_16x16x32_bf16 v[88:91], a[116:119], a[4:7], v[88:91]\n" \
"ds_read_b64_tr_b16 a[72:73], v233 offset:32768\n" \
"v_accvgpr_read_b32 v253, a162\n" \
"v_mfma_f32_16x16x32_bf16 v[92:95], a[120:123], a[0:3], v[92:95]\n" \
"ds_read_b64_tr_b16 a[74:75], v233 offset:34816\n" \
"v_accvgpr_read_b32 v254, a163\n" \
"v_mfma_f32_16x16x32_bf16 v[92:95], a[124:127], a[4:7], v[92:95]\n" \
"buffer_load_dwordx4 v229, s[52:55], s78 offen offset:1024 lds\n" \
"v_mfma_f32_16x16x32_bf16 v[124:127], a[120:123], a[8:11], v[124:127]\n" \
"s_waitcnt vmcnt(35)\n" \
"ds_read_b128 a[0:3], v250 offset:0\n" \
"v_mul_f32 v243, s31, v243\n" \
"v_mfma_f32_16x16x32_bf16 v[124:127], a[124:127], a[12:15], v[124:127]\n" \
"ds_read_b128 a[4:7], v251 offset:0\n" \
"v_mul_f32 v252, s31, v252\n" \
"v_mfma_f32_16x16x32_bf16 v[120:123], a[112:115], a[8:11], v[120:123]\n" \
"s_xor_b32 s78, s78, 0x80000\n" \
"ds_read_b64_tr_b16 a[76:77], v233 offset:40960\n" \
"v_mul_f32 v253, s31, v253\n" \
"v_mfma_f32_16x16x32_bf16 v[120:123], a[116:119], a[12:15], v[120:123]\n" \
"buffer_load_dwordx4 v228, s[52:55], s89 offen offset:2048 lds\n" \
"v_mfma_f32_16x16x32_bf16 v[116:119], a[104:107], a[8:11], v[116:119]\n" \
"ds_read_b64_tr_b16 a[78:79], v233 offset:43008\n" \
"v_mul_f32 v254, s31, v254\n" \
"v_mfma_f32_16x16x32_bf16 v[116:119], a[108:111], a[12:15], v[116:119]\n" \
"ds_read_b64_tr_b16 a[80:81], v232 offset:33280\n" \
"v_cvt_pk_bf16_f32 v44, v243, v252\n" \
"v_mfma_f32_16x16x32_bf16 v[112:115], a[96:99], a[8:11], v[112:115]\n" \
"ds_read_b64_tr_b16 a[82:83], v232 offset:35328\n" \
"v_cvt_pk_bf16_f32 v45, v253, v254\n" \
"v_mfma_f32_16x16x32_bf16 v[112:115], a[100:103], a[12:15], v[112:115]\n" \
"buffer_load_dwordx4 v229, s[52:55], s89 offen offset:3072 lds\n" \
"v_mfma_f32_16x16x32_bf16 v[144:147], a[96:99], a[16:19], v[144:147]\n" \
"s_waitcnt vmcnt(35)\n" \
"s_xor_b32 s89, s89, 0x80000\n" \
"ds_read_b128 a[8:11], v250 offset:2048\n" \
"v_mfma_f32_16x16x32_bf16 v[144:147], a[100:103], a[20:23], v[144:147]\n" \
"s_add_u32 m0, s74, 131072\n" \
"ds_read_b128 a[12:15], v251 offset:2048\n" \
"v_accvgpr_read_b32 v243, a176\n" \
"v_mfma_f32_16x16x32_bf16 v[148:151], a[104:107], a[16:19], v[148:151]\n" \
"s_waitcnt lgkmcnt(6)\n" \
"ds_read_b64_tr_b16 a[84:85], v232 offset:41472\n" \
"v_accvgpr_read_b32 v252, a177\n" \
"v_mfma_f32_16x16x32_bf16 v[148:151], a[108:111], a[20:23], v[148:151]\n" \
"buffer_load_dwordx4 v224, s[48:51], s75 offen offset:0 lds\n" \
"v_mfma_f32_16x16x32_bf16 v[152:155], a[112:115], a[16:19], v[152:155]\n" \
"ds_read_b64_tr_b16 a[86:87], v232 offset:43520\n" \
"v_accvgpr_read_b32 v253, a178\n" \
"v_mfma_f32_16x16x32_bf16 v[152:155], a[116:119], a[20:23], v[152:155]\n" \
"ds_read_b64_tr_b16 a[88:89], v233 offset:33280\n" \
"v_accvgpr_read_b32 v254, a179\n" \
"v_mfma_f32_16x16x32_bf16 v[156:159], a[120:123], a[16:19], v[156:159]\n" \
"ds_read_b64_tr_b16 a[90:91], v233 offset:35328\n" \
"v_mul_f32 v243, s31, v243\n" \
"v_mfma_f32_16x16x32_bf16 v[156:159], a[124:127], a[20:23], v[156:159]\n" \
"buffer_load_dwordx4 v225, s[48:51], s75 offen offset:1024 lds\n" \
"v_mfma_f32_16x16x32_bf16 v[188:191], a[120:123], a[24:27], v[188:191]\n" \
"s_waitcnt vmcnt(35)\n" \
"ds_read_b128 a[16:19], v250 offset:4096\n" \
"v_mul_f32 v252, s31, v252\n" \
"v_mfma_f32_16x16x32_bf16 v[188:191], a[124:127], a[28:31], v[188:191]\n" \
"ds_read_b128 a[20:23], v251 offset:4096\n" \
"v_mul_f32 v253, s31, v253\n" \
"v_mfma_f32_16x16x32_bf16 v[184:187], a[112:115], a[24:27], v[184:187]\n" \
"s_waitcnt lgkmcnt(6)\n" \
"ds_read_b64_tr_b16 a[92:93], v233 offset:41472\n" \
"v_mul_f32 v254, s31, v254\n" \
"v_mfma_f32_16x16x32_bf16 v[184:187], a[116:119], a[28:31], v[184:187]\n" \
"buffer_load_dwordx4 v226, s[48:51], s75 offen offset:2048 lds\n" \
"v_mfma_f32_16x16x32_bf16 v[180:183], a[104:107], a[24:27], v[180:183]\n" \
"ds_read_b64_tr_b16 a[94:95], v233 offset:43520\n" \
"v_cvt_pk_bf16_f32 v46, v243, v252\n" \
"v_mfma_f32_16x16x32_bf16 v[180:183], a[108:111], a[28:31], v[180:183]\n" \
"v_cvt_pk_bf16_f32 v47, v253, v254\n" \
"v_accvgpr_read_b32 v243, a164\n" \
"v_mfma_f32_16x16x32_bf16 v[176:179], a[96:99], a[24:27], v[176:179]\n" \
"v_accvgpr_read_b32 v252, a165\n" \
"v_accvgpr_read_b32 v253, a166\n" \
"v_mfma_f32_16x16x32_bf16 v[176:179], a[100:103], a[28:31], v[176:179]\n" \
"buffer_load_dwordx4 v227, s[48:51], s75 offen offset:3072 lds\n" \
"v_mfma_f32_16x16x32_bf16 v[64:67], a[64:67], a[0:3], v[64:67]\n" \
"s_waitcnt vmcnt(35)\n" \
"s_xor_b32 s75, s75, 0x80\n" \
"ds_read_b128 a[24:27], v250 offset:6144\n" \
"v_mfma_f32_16x16x32_bf16 v[64:67], a[68:71], a[4:7], v[64:67]\n" \
"s_add_u32 m0, s74, 135168\n" \
"ds_read_b128 a[28:31], v251 offset:6144\n" \
"v_accvgpr_read_b32 v254, a167\n" \
"v_mfma_f32_16x16x32_bf16 v[68:71], a[72:75], a[0:3], v[68:71]\n" \
"s_waitcnt lgkmcnt(4)\n" \
"ds_read_b64_tr_b16 a[96:97], v232 offset:33792\n" \
"v_mul_f32 v243, s31, v243\n" \
"v_mfma_f32_16x16x32_bf16 v[68:71], a[76:79], a[4:7], v[68:71]\n" \
"buffer_load_dwordx4 v224, s[48:51], s77 offen offset:0 lds\n" \
"v_mfma_f32_16x16x32_bf16 v[72:75], a[80:83], a[0:3], v[72:75]\n" \
"ds_read_b64_tr_b16 a[98:99], v232 offset:35840\n" \
"v_mul_f32 v252, s31, v252\n" \
"v_mfma_f32_16x16x32_bf16 v[72:75], a[84:87], a[4:7], v[72:75]\n" \
"s_waitcnt lgkmcnt(3)\n" \
"ds_read_b64_tr_b16 a[100:101], v232 offset:41984\n" \
"v_mul_f32 v253, s31, v253\n" \
"v_mfma_f32_16x16x32_bf16 v[76:79], a[88:91], a[0:3], v[76:79]\n" \
"ds_read_b64_tr_b16 a[102:103], v232 offset:44032\n" \
"v_mul_f32 v254, s31, v254\n" \
"v_mfma_f32_16x16x32_bf16 v[76:79], a[92:95], a[4:7], v[76:79]\n" \
"buffer_load_dwordx4 v225, s[48:51], s77 offen offset:1024 lds\n" \
"v_mfma_f32_16x16x32_bf16 v[108:111], a[88:91], a[8:11], v[108:111]\n" \
"ds_read_b64_tr_b16 a[104:105], v233 offset:33792\n" \
"v_cvt_pk_bf16_f32 v60, v243, v252\n" \
"v_mfma_f32_16x16x32_bf16 v[108:111], a[92:95], a[12:15], v[108:111]\n" \
"ds_read_b64_tr_b16 a[106:107], v233 offset:35840\n" \
"v_cvt_pk_bf16_f32 v61, v253, v254\n" \
"v_mfma_f32_16x16x32_bf16 v[104:107], a[80:83], a[8:11], v[104:107]\n" \
"s_waitcnt lgkmcnt(6)\n" \
"ds_read_b64_tr_b16 a[108:109], v233 offset:41984\n" \
"v_accvgpr_read_b32 v243, a180\n" \
"v_mfma_f32_16x16x32_bf16 v[104:107], a[84:87], a[12:15], v[104:107]\n" \
"buffer_load_dwordx4 v226, s[48:51], s77 offen offset:2048 lds\n" \
"v_mfma_f32_16x16x32_bf16 v[100:103], a[72:75], a[8:11], v[100:103]\n" \
"ds_read_b64_tr_b16 a[110:111], v233 offset:44032\n" \
"Lpg3339_%=:\n" \
"s_cbranch_execz Lpg5598_%=\n" \
"v_accvgpr_read_b32 v252, a181\n" \
"v_mfma_f32_16x16x32_bf16 v[100:103], a[76:79], a[12:15], v[100:103]\n" \
"ds_read_b64_tr_b16 a[112:113], v232 offset:34304\n" \
"v_accvgpr_read_b32 v253, a182\n" \
"v_mfma_f32_16x16x32_bf16 v[96:99], a[64:67], a[8:11], v[96:99]\n" \
"ds_read_b64_tr_b16 a[114:115], v232 offset:36352\n" \
"v_accvgpr_read_b32 v254, a183\n" \
"v_mfma_f32_16x16x32_bf16 v[96:99], a[68:71], a[12:15], v[96:99]\n" \
"buffer_load_dwordx4 v227, s[48:51], s77 offen offset:3072 lds\n" \
"v_mfma_f32_16x16x32_bf16 v[128:131], a[64:67], a[16:19], v[128:131]\n" \
"ds_read_b64_tr_b16 a[116:117], v232 offset:42496\n" \
"v_mul_f32 v243, s31, v243\n" \
"v_mfma_f32_16x16x32_bf16 v[128:131], a[68:71], a[20:23], v[128:131]\n" \
"ds_read_b64_tr_b16 a[118:119], v232 offset:44544\n" \
"v_mul_f32 v252, s31, v252\n" \
"v_mfma_f32_16x16x32_bf16 v[132:135], a[72:75], a[16:19], v[132:135]\n" \
"s_xor_b32 s77, s77, 0x80\n" \
"ds_read_b64_tr_b16 a[120:121], v233 offset:34304\n" \
"v_mul_f32 v253, s31, v253\n" \
"v_mfma_f32_16x16x32_bf16 v[132:135], a[76:79], a[20:23], v[132:135]\n" \
"v_mul_f32 v254, s31, v254\n" \
"v_cvt_pk_bf16_f32 v62, v243, v252\n" \
"v_mfma_f32_16x16x32_bf16 v[136:139], a[80:83], a[16:19], v[136:139]\n" \
"ds_read_b64_tr_b16 a[122:123], v233 offset:36352\n" \
"v_cvt_pk_bf16_f32 v63, v253, v254\n" \
"v_mfma_f32_16x16x32_bf16 v[136:139], a[84:87], a[20:23], v[136:139]\n" \
"ds_read_b64_tr_b16 a[124:125], v233 offset:42496\n" \
"v_accvgpr_read_b32 v243, a168\n" \
"v_mfma_f32_16x16x32_bf16 v[140:143], a[88:91], a[16:19], v[140:143]\n" \
"ds_read_b64_tr_b16 a[126:127], v233 offset:44544\n" \
"v_accvgpr_read_b32 v252, a169\n" \
"v_mfma_f32_16x16x32_bf16 v[140:143], a[92:95], a[20:23], v[140:143]\n" \
"buffer_store_dwordx4 v[204:207], v236, s[56:59], s93 offen offset:2048 sc1\n" \
"v_mfma_f32_16x16x32_bf16 v[172:175], a[88:91], a[24:27], v[172:175]\n" \
"v_accvgpr_read_b32 v253, a170\n" \
"v_accvgpr_read_b32 v254, a171\n" \
"v_mfma_f32_16x16x32_bf16 v[172:175], a[92:95], a[28:31], v[172:175]\n" \
"v_mul_f32 v243, s31, v243\n" \
"v_mul_f32 v252, s31, v252\n" \
"v_mfma_f32_16x16x32_bf16 v[168:171], a[80:83], a[24:27], v[168:171]\n" \
"v_mul_f32 v253, s31, v253\n" \
"v_mul_f32 v254, s31, v254\n" \
"v_mfma_f32_16x16x32_bf16 v[168:171], a[84:87], a[28:31], v[168:171]\n" \
"v_cvt_pk_bf16_f32 v20, v243, v252\n" \
"v_cvt_pk_bf16_f32 v21, v253, v254\n" \
"v_mfma_f32_16x16x32_bf16 v[164:167], a[72:75], a[24:27], v[164:167]\n" \
"v_accvgpr_read_b32 v243, a184\n" \
"v_accvgpr_read_b32 v252, a185\n" \
"v_mfma_f32_16x16x32_bf16 v[164:167], a[76:79], a[28:31], v[164:167]\n" \
"v_accvgpr_read_b32 v253, a186\n" \
"v_accvgpr_read_b32 v254, a187\n" \
"v_mfma_f32_16x16x32_bf16 v[160:163], a[64:67], a[24:27], v[160:163]\n" \
"v_mul_f32 v243, s31, v243\n" \
"v_mul_f32 v252, s31, v252\n" \
"v_mfma_f32_16x16x32_bf16 v[160:163], a[68:71], a[28:31], v[160:163]\n" \
"s_waitcnt vmcnt(35) lgkmcnt(0)\n" \
"s_barrier\n" \
"v_mfma_f32_16x16x32_bf16 v[80:83], a[96:99], a[0:3], v[80:83]\n" \
"ds_read_b64_tr_b16 a[64:65], v232 offset:49152\n" \
"v_mul_f32 v253, s31, v253\n" \
"v_mfma_f32_16x16x32_bf16 v[80:83], a[100:103], a[4:7], v[80:83]\n" \
"ds_read_b64_tr_b16 a[66:67], v232 offset:51200\n" \
"v_mul_f32 v254, s31, v254\n" \
"v_mfma_f32_16x16x32_bf16 v[84:87], a[104:107], a[0:3], v[84:87]\n" \
"s_add_u32 m0, s79, 32768\n" \
"ds_read_b64_tr_b16 a[68:69], v232 offset:57344\n" \
"v_cvt_pk_bf16_f32 v22, v243, v252\n" \
"v_mfma_f32_16x16x32_bf16 v[84:87], a[108:111], a[4:7], v[84:87]\n" \
"buffer_load_dwordx4 v228, s[52:55], s78 offen offset:0 lds\n" \
"v_mfma_f32_16x16x32_bf16 v[88:91], a[112:115], a[0:3], v[88:91]\n" \
"ds_read_b64_tr_b16 a[70:71], v232 offset:59392\n" \
"v_cvt_pk_bf16_f32 v23, v253, v254\n" \
"v_mfma_f32_16x16x32_bf16 v[88:91], a[116:119], a[4:7], v[88:91]\n" \
"ds_read_b64_tr_b16 a[72:73], v233 offset:49152\n" \
"v_accvgpr_read_b32 v243, a172\n" \
"v_mfma_f32_16x16x32_bf16 v[92:95], a[120:123], a[0:3], v[92:95]\n" \
"ds_read_b64_tr_b16 a[74:75], v233 offset:51200\n" \
"v_accvgpr_read_b32 v252, a173\n" \
"v_mfma_f32_16x16x32_bf16 v[92:95], a[124:127], a[4:7], v[92:95]\n" \
"buffer_load_dwordx4 v229, s[52:55], s78 offen offset:1024 lds\n" \
"v_mfma_f32_16x16x32_bf16 v[124:127], a[120:123], a[8:11], v[124:127]\n" \
"s_waitcnt vmcnt(35)\n" \
"ds_read_b128 a[0:3], v230 offset:0\n" \
"v_accvgpr_read_b32 v253, a174\n" \
"v_mfma_f32_16x16x32_bf16 v[124:127], a[124:127], a[12:15], v[124:127]\n" \
"ds_read_b128 a[4:7], v231 offset:0\n" \
"v_accvgpr_read_b32 v254, a175\n" \
"v_mfma_f32_16x16x32_bf16 v[120:123], a[112:115], a[8:11], v[120:123]\n" \
"s_xor_b32 s78, s78, 0x180000\n" \
"ds_read_b64_tr_b16 a[76:77], v233 offset:57344\n" \
"v_mul_f32 v243, s31, v243\n" \
"v_mfma_f32_16x16x32_bf16 v[120:123], a[116:119], a[12:15], v[120:123]\n" \
"buffer_load_dwordx4 v228, s[52:55], s89 offen offset:2048 lds\n" \
"v_mfma_f32_16x16x32_bf16 v[116:119], a[104:107], a[8:11], v[116:119]\n" \
"ds_read_b64_tr_b16 a[78:79], v233 offset:59392\n" \
"v_mul_f32 v252, s31, v252\n" \
"v_mfma_f32_16x16x32_bf16 v[116:119], a[108:111], a[12:15], v[116:119]\n" \
"ds_read_b64_tr_b16 a[80:81], v232 offset:49664\n" \
"v_mul_f32 v253, s31, v253\n" \
"v_mfma_f32_16x16x32_bf16 v[112:115], a[96:99], a[8:11], v[112:115]\n" \
"ds_read_b64_tr_b16 a[82:83], v232 offset:51712\n" \
"v_mul_f32 v254, s31, v254\n" \
"v_mfma_f32_16x16x32_bf16 v[112:115], a[100:103], a[12:15], v[112:115]\n" \
"buffer_load_dwordx4 v229, s[52:55], s89 offen offset:3072 lds\n" \
"v_mfma_f32_16x16x32_bf16 v[144:147], a[96:99], a[16:19], v[144:147]\n" \
"s_waitcnt vmcnt(35)\n" \
"s_xor_b32 s89, s89, 0x180000\n" \
"ds_read_b128 a[8:11], v230 offset:2048\n" \
"v_mfma_f32_16x16x32_bf16 v[144:147], a[100:103], a[20:23], v[144:147]\n" \
"s_add_u32 m0, s74, 65536\n" \
"ds_read_b128 a[12:15], v231 offset:2048\n" \
"v_cvt_pk_bf16_f32 v28, v243, v252\n" \
"v_mfma_f32_16x16x32_bf16 v[148:151], a[104:107], a[16:19], v[148:151]\n" \
"s_waitcnt lgkmcnt(6)\n" \
"ds_read_b64_tr_b16 a[84:85], v232 offset:57856\n" \
"v_cvt_pk_bf16_f32 v29, v253, v254\n" \
"v_mfma_f32_16x16x32_bf16 v[148:151], a[108:111], a[20:23], v[148:151]\n" \
"buffer_load_dwordx4 v224, s[48:51], s75 offen offset:0 lds\n" \
"v_mfma_f32_16x16x32_bf16 v[152:155], a[112:115], a[16:19], v[152:155]\n" \
"ds_read_b64_tr_b16 a[86:87], v232 offset:59904\n" \
"v_accvgpr_read_b32 v243, a188\n" \
"v_mfma_f32_16x16x32_bf16 v[152:155], a[116:119], a[20:23], v[152:155]\n" \
"ds_read_b64_tr_b16 a[88:89], v233 offset:49664\n" \
"v_accvgpr_read_b32 v252, a189\n" \
"v_mfma_f32_16x16x32_bf16 v[156:159], a[120:123], a[16:19], v[156:159]\n" \
"ds_read_b64_tr_b16 a[90:91], v233 offset:51712\n" \
"v_accvgpr_read_b32 v253, a190\n" \
"v_mfma_f32_16x16x32_bf16 v[156:159], a[124:127], a[20:23], v[156:159]\n" \
"buffer_load_dwordx4 v225, s[48:51], s75 offen offset:1024 lds\n" \
"v_mfma_f32_16x16x32_bf16 v[188:191], a[120:123], a[24:27], v[188:191]\n" \
"s_waitcnt vmcnt(35)\n" \
"ds_read_b128 a[16:19], v230 offset:4096\n" \
"v_accvgpr_read_b32 v254, a191\n" \
"v_mfma_f32_16x16x32_bf16 v[188:191], a[124:127], a[28:31], v[188:191]\n" \
"ds_read_b128 a[20:23], v231 offset:4096\n" \
"v_mul_f32 v243, s31, v243\n" \
"v_mfma_f32_16x16x32_bf16 v[184:187], a[112:115], a[24:27], v[184:187]\n" \
"s_waitcnt lgkmcnt(6)\n" \
"ds_read_b64_tr_b16 a[92:93], v233 offset:57856\n" \
"v_mul_f32 v252, s31, v252\n" \
"v_mfma_f32_16x16x32_bf16 v[184:187], a[116:119], a[28:31], v[184:187]\n" \
"buffer_load_dwordx4 v226, s[48:51], s75 offen offset:2048 lds\n" \
"v_mfma_f32_16x16x32_bf16 v[180:183], a[104:107], a[24:27], v[180:183]\n" \
"ds_read_b64_tr_b16 a[94:95], v233 offset:59904\n" \
"v_mul_f32 v253, s31, v253\n" \
"v_mfma_f32_16x16x32_bf16 v[180:183], a[108:111], a[28:31], v[180:183]\n" \
"v_mul_f32 v254, s31, v254\n" \
"v_cvt_pk_bf16_f32 v30, v243, v252\n" \
"v_mfma_f32_16x16x32_bf16 v[176:179], a[96:99], a[24:27], v[176:179]\n" \
"v_cvt_pk_bf16_f32 v31, v253, v254\n" \
"v_accvgpr_read_b32 v243, a128\n" \
"v_mfma_f32_16x16x32_bf16 v[176:179], a[100:103], a[28:31], v[176:179]\n" \
"buffer_load_dwordx4 v227, s[48:51], s75 offen offset:3072 lds\n" \
"v_mfma_f32_16x16x32_bf16 v[64:67], a[64:67], a[0:3], v[64:67]\n" \
"s_waitcnt vmcnt(35)\n" \
"s_xor_b32 s75, s75, 0x180\n" \
"ds_read_b128 a[24:27], v230 offset:6144\n" \
"v_mfma_f32_16x16x32_bf16 v[64:67], a[68:71], a[4:7], v[64:67]\n" \
"s_add_u32 m0, s74, 69632\n" \
"ds_read_b128 a[28:31], v231 offset:6144\n" \
"v_accvgpr_read_b32 v252, a129\n" \
"v_mfma_f32_16x16x32_bf16 v[68:71], a[72:75], a[0:3], v[68:71]\n" \
"s_waitcnt lgkmcnt(4)\n" \
"ds_read_b64_tr_b16 a[96:97], v232 offset:50176\n" \
"v_accvgpr_read_b32 v253, a130\n" \
"v_mfma_f32_16x16x32_bf16 v[68:71], a[76:79], a[4:7], v[68:71]\n" \
"buffer_load_dwordx4 v224, s[48:51], s77 offen offset:0 lds\n" \
"v_mfma_f32_16x16x32_bf16 v[72:75], a[80:83], a[0:3], v[72:75]\n" \
"ds_read_b64_tr_b16 a[98:99], v232 offset:52224\n" \
"v_accvgpr_read_b32 v254, a131\n" \
"v_mfma_f32_16x16x32_bf16 v[72:75], a[84:87], a[4:7], v[72:75]\n" \
"s_waitcnt lgkmcnt(3)\n" \
"ds_read_b64_tr_b16 a[100:101], v232 offset:58368\n" \
"v_mul_f32 v243, s31, v243\n" \
"v_mfma_f32_16x16x32_bf16 v[76:79], a[88:91], a[0:3], v[76:79]\n" \
"ds_read_b64_tr_b16 a[102:103], v232 offset:60416\n" \
"v_mul_f32 v252, s31, v252\n" \
"v_mfma_f32_16x16x32_bf16 v[76:79], a[92:95], a[4:7], v[76:79]\n" \
"buffer_load_dwordx4 v225, s[48:51], s77 offen offset:1024 lds\n" \
"v_mfma_f32_16x16x32_bf16 v[108:111], a[88:91], a[8:11], v[108:111]\n" \
"ds_read_b64_tr_b16 a[104:105], v233 offset:50176\n" \
"v_mul_f32 v253, s31, v253\n" \
"v_mfma_f32_16x16x32_bf16 v[108:111], a[92:95], a[12:15], v[108:111]\n" \
"ds_read_b64_tr_b16 a[106:107], v233 offset:52224\n" \
"v_mul_f32 v254, s31, v254\n" \
"v_mfma_f32_16x16x32_bf16 v[104:107], a[80:83], a[8:11], v[104:107]\n" \
"s_waitcnt lgkmcnt(6)\n" \
"ds_read_b64_tr_b16 a[108:109], v233 offset:58368\n" \
"v_cvt_pk_bf16_f32 v0, v243, v252\n" \
"v_mfma_f32_16x16x32_bf16 v[104:107], a[84:87], a[12:15], v[104:107]\n" \
"buffer_load_dwordx4 v226, s[48:51], s77 offen offset:2048 lds\n" \
"v_mfma_f32_16x16x32_bf16 v[100:103], a[72:75], a[8:11], v[100:103]\n" \
"ds_read_b64_tr_b16 a[110:111], v233 offset:60416\n" \
"v_cvt_pk_bf16_f32 v1, v253, v254\n" \
"v_mfma_f32_16x16x32_bf16 v[100:103], a[76:79], a[12:15], v[100:103]\n" \
"ds_read_b64_tr_b16 a[112:113], v232 offset:50688\n" \
"v_accvgpr_read_b32 v243, a144\n" \
"v_mfma_f32_16x16x32_bf16 v[96:99], a[64:67], a[8:11], v[96:99]\n" \
"ds_read_b64_tr_b16 a[114:115], v232 offset:52736\n" \
"v_accvgpr_read_b32 v252, a145\n" \
"v_mfma_f32_16x16x32_bf16 v[96:99], a[68:71], a[12:15], v[96:99]\n" \
"buffer_load_dwordx4 v227, s[48:51], s77 offen offset:3072 lds\n" \
"v_mfma_f32_16x16x32_bf16 v[128:131], a[64:67], a[16:19], v[128:131]\n" \
"ds_read_b64_tr_b16 a[116:117], v232 offset:58880\n" \
"v_accvgpr_read_b32 v253, a146\n" \
"v_mfma_f32_16x16x32_bf16 v[128:131], a[68:71], a[20:23], v[128:131]\n" \
"ds_read_b64_tr_b16 a[118:119], v232 offset:60928\n" \
"v_accvgpr_read_b32 v254, a147\n" \
"v_mfma_f32_16x16x32_bf16 v[132:135], a[72:75], a[16:19], v[132:135]\n" \
"s_xor_b32 s77, s77, 0x180\n" \
"ds_read_b64_tr_b16 a[120:121], v233 offset:50688\n" \
"v_mul_f32 v243, s31, v243\n" \
"v_mfma_f32_16x16x32_bf16 v[132:135], a[76:79], a[20:23], v[132:135]\n" \
"v_mul_f32 v252, s31, v252\n" \
"v_mul_f32 v253, s31, v253\n" \
"v_mfma_f32_16x16x32_bf16 v[136:139], a[80:83], a[16:19], v[136:139]\n" \
"ds_read_b64_tr_b16 a[122:123], v233 offset:52736\n" \
"v_mul_f32 v254, s31, v254\n" \
"v_mfma_f32_16x16x32_bf16 v[136:139], a[84:87], a[20:23], v[136:139]\n" \
"ds_read_b64_tr_b16 a[124:125], v233 offset:58880\n" \
"v_cvt_pk_bf16_f32 v2, v243, v252\n" \
"v_mfma_f32_16x16x32_bf16 v[140:143], a[88:91], a[16:19], v[140:143]\n" \
"ds_read_b64_tr_b16 a[126:127], v233 offset:60928\n" \
"v_cvt_pk_bf16_f32 v3, v253, v254\n" \
"v_mfma_f32_16x16x32_bf16 v[140:143], a[92:95], a[20:23], v[140:143]\n" \
"buffer_store_dwordx4 v[220:223], v236, s[56:59], s93 offen offset:2304 sc1\n" \
"v_mfma_f32_16x16x32_bf16 v[172:175], a[88:91], a[24:27], v[172:175]\n" \
"v_accvgpr_read_b32 v243, a132\n" \
"v_accvgpr_read_b32 v252, a133\n" \
"v_mfma_f32_16x16x32_bf16 v[172:175], a[92:95], a[28:31], v[172:175]\n" \
"v_accvgpr_read_b32 v253, a134\n" \
"v_accvgpr_read_b32 v254, a135\n" \
"v_mfma_f32_16x16x32_bf16 v[168:171], a[80:83], a[24:27], v[168:171]\n" \
"v_mul_f32 v243, s31, v243\n" \
"v_mul_f32 v252, s31, v252\n" \
"v_mfma_f32_16x16x32_bf16 v[168:171], a[84:87], a[28:31], v[168:171]\n" \
"v_mul_f32 v253, s31, v253\n" \
"v_mul_f32 v254, s31, v254\n" \
"v_mfma_f32_16x16x32_bf16 v[164:167], a[72:75], a[24:27], v[164:167]\n" \
"v_cvt_pk_bf16_f32 v4, v243, v252\n" \
"v_cvt_pk_bf16_f32 v5, v253, v254\n" \
"v_mfma_f32_16x16x32_bf16 v[164:167], a[76:79], a[28:31], v[164:167]\n" \
"v_accvgpr_read_b32 v243, a148\n" \
"v_accvgpr_read_b32 v252, a149\n" \
"v_mfma_f32_16x16x32_bf16 v[160:163], a[64:67], a[24:27], v[160:163]\n" \
"v_accvgpr_read_b32 v253, a150\n" \
"v_accvgpr_read_b32 v254, a151\n" \
"v_mfma_f32_16x16x32_bf16 v[160:163], a[68:71], a[28:31], v[160:163]\n" \
"s_waitcnt vmcnt(35) lgkmcnt(0)\n" \
"s_barrier\n" \
"v_mfma_f32_16x16x32_bf16 v[80:83], a[96:99], a[0:3], v[80:83]\n" \
"ds_read_b64_tr_b16 a[64:65], v232 offset:0\n" \
"v_mul_f32 v243, s31, v243\n" \
"v_mfma_f32_16x16x32_bf16 v[80:83], a[100:103], a[4:7], v[80:83]\n" \
"ds_read_b64_tr_b16 a[66:67], v232 offset:2048\n" \
"v_mul_f32 v252, s31, v252\n" \
"v_mfma_f32_16x16x32_bf16 v[84:87], a[104:107], a[0:3], v[84:87]\n" \
"s_add_u32 m0, s79, 49152\n" \
"ds_read_b64_tr_b16 a[68:69], v232 offset:8192\n" \
"v_mul_f32 v253, s31, v253\n" \
"v_mfma_f32_16x16x32_bf16 v[84:87], a[108:111], a[4:7], v[84:87]\n" \
"buffer_load_dwordx4 v228, s[52:55], s78 offen offset:0 lds\n" \
"v_mfma_f32_16x16x32_bf16 v[88:91], a[112:115], a[0:3], v[88:91]\n" \
"ds_read_b64_tr_b16 a[70:71], v232 offset:10240\n" \
"v_mul_f32 v254, s31, v254\n" \
"v_mfma_f32_16x16x32_bf16 v[88:91], a[116:119], a[4:7], v[88:91]\n" \
"ds_read_b64_tr_b16 a[72:73], v233 offset:0\n" \
"v_cvt_pk_bf16_f32 v6, v243, v252\n" \
"v_mfma_f32_16x16x32_bf16 v[92:95], a[120:123], a[0:3], v[92:95]\n" \
"ds_read_b64_tr_b16 a[74:75], v233 offset:2048\n" \
"v_cvt_pk_bf16_f32 v7, v253, v254\n" \
"v_mfma_f32_16x16x32_bf16 v[92:95], a[124:127], a[4:7], v[92:95]\n" \
"buffer_load_dwordx4 v229, s[52:55], s78 offen offset:1024 lds\n" \
"v_mfma_f32_16x16x32_bf16 v[124:127], a[120:123], a[8:11], v[124:127]\n" \
"s_waitcnt vmcnt(35)\n" \
"ds_read_b128 a[0:3], v230 offset:32768\n" \
"v_accvgpr_read_b32 v243, a136\n" \
"v_mfma_f32_16x16x32_bf16 v[124:127], a[124:127], a[12:15], v[124:127]\n" \
"ds_read_b128 a[4:7], v231 offset:32768\n" \
"v_accvgpr_read_b32 v252, a137\n" \
"v_mfma_f32_16x16x32_bf16 v[120:123], a[112:115], a[8:11], v[120:123]\n" \
"s_xor_b32 s78, s78, 0x300000\n" \
"ds_read_b64_tr_b16 a[76:77], v233 offset:8192\n" \
"v_accvgpr_read_b32 v253, a138\n" \
"v_mfma_f32_16x16x32_bf16 v[120:123], a[116:119], a[12:15], v[120:123]\n" \
"buffer_load_dwordx4 v228, s[52:55], s89 offen offset:2048 lds\n" \
"v_mfma_f32_16x16x32_bf16 v[116:119], a[104:107], a[8:11], v[116:119]\n" \
"ds_read_b64_tr_b16 a[78:79], v233 offset:10240\n" \
"v_accvgpr_read_b32 v254, a139\n" \
"v_mfma_f32_16x16x32_bf16 v[116:119], a[108:111], a[12:15], v[116:119]\n" \
"ds_read_b64_tr_b16 a[80:81], v232 offset:512\n" \
"v_mul_f32 v243, s31, v243\n" \
"v_mfma_f32_16x16x32_bf16 v[112:115], a[96:99], a[8:11], v[112:115]\n" \
"ds_read_b64_tr_b16 a[82:83], v232 offset:2560\n" \
"v_mul_f32 v252, s31, v252\n" \
"v_mfma_f32_16x16x32_bf16 v[112:115], a[100:103], a[12:15], v[112:115]\n" \
"buffer_load_dwordx4 v229, s[52:55], s89 offen offset:3072 lds\n" \
"v_mfma_f32_16x16x32_bf16 v[144:147], a[96:99], a[16:19], v[144:147]\n" \
"s_waitcnt vmcnt(35)\n" \
"s_xor_b32 s89, s89, 0x300000\n" \
"ds_read_b128 a[8:11], v230 offset:34816\n" \
"v_mfma_f32_16x16x32_bf16 v[144:147], a[100:103], a[20:23], v[144:147]\n" \
"s_add_u32 m0, s74, 98304\n" \
"ds_read_b128 a[12:15], v231 offset:34816\n" \
"v_mul_f32 v253, s31, v253\n" \
"v_mfma_f32_16x16x32_bf16 v[148:151], a[104:107], a[16:19], v[148:151]\n" \
"s_waitcnt lgkmcnt(6)\n" \
"ds_read_b64_tr_b16 a[84:85], v232 offset:8704\n" \
"v_mul_f32 v254, s31, v254\n" \
"v_mfma_f32_16x16x32_bf16 v[148:151], a[108:111], a[20:23], v[148:151]\n" \
"buffer_load_dwordx4 v224, s[48:51], s75 offen offset:0 lds\n" \
"v_mfma_f32_16x16x32_bf16 v[152:155], a[112:115], a[16:19], v[152:155]\n" \
"ds_read_b64_tr_b16 a[86:87], v232 offset:10752\n" \
"v_cvt_pk_bf16_f32 v8, v243, v252\n" \
"v_mfma_f32_16x16x32_bf16 v[152:155], a[116:119], a[20:23], v[152:155]\n" \
"ds_read_b64_tr_b16 a[88:89], v233 offset:512\n" \
"v_cvt_pk_bf16_f32 v9, v253, v254\n" \
"v_mfma_f32_16x16x32_bf16 v[156:159], a[120:123], a[16:19], v[156:159]\n" \
"ds_read_b64_tr_b16 a[90:91], v233 offset:2560\n" \
"v_accvgpr_read_b32 v243, a152\n" \
"v_mfma_f32_16x16x32_bf16 v[156:159], a[124:127], a[20:23], v[156:159]\n" \
"buffer_load_dwordx4 v225, s[48:51], s75 offen offset:1024 lds\n" \
"v_mfma_f32_16x16x32_bf16 v[188:191], a[120:123], a[24:27], v[188:191]\n" \
"s_waitcnt vmcnt(35)\n" \
"ds_read_b128 a[16:19], v230 offset:36864\n" \
"v_accvgpr_read_b32 v252, a153\n" \
"v_mfma_f32_16x16x32_bf16 v[188:191], a[124:127], a[28:31], v[188:191]\n" \
"ds_read_b128 a[20:23], v231 offset:36864\n" \
"v_accvgpr_read_b32 v253, a154\n" \
"v_mfma_f32_16x16x32_bf16 v[184:187], a[112:115], a[24:27], v[184:187]\n" \
"s_waitcnt lgkmcnt(6)\n" \
"ds_read_b64_tr_b16 a[92:93], v233 offset:8704\n" \
"v_accvgpr_read_b32 v254, a155\n" \
"v_mfma_f32_16x16x32_bf16 v[184:187], a[116:119], a[28:31], v[184:187]\n" \
"buffer_load_dwordx4 v226, s[48:51], s75 offen offset:2048 lds\n" \
"v_mfma_f32_16x16x32_bf16 v[180:183], a[104:107], a[24:27], v[180:183]\n" \
"ds_read_b64_tr_b16 a[94:95], v233 offset:10752\n" \
"v_mul_f32 v243, s31, v243\n" \
"v_mfma_f32_16x16x32_bf16 v[180:183], a[108:111], a[28:31], v[180:183]\n" \
"v_mul_f32 v252, s31, v252\n" \
"v_mul_f32 v253, s31, v253\n" \
"v_mfma_f32_16x16x32_bf16 v[176:179], a[96:99], a[24:27], v[176:179]\n" \
"v_mul_f32 v254, s31, v254\n" \
"v_cvt_pk_bf16_f32 v10, v243, v252\n" \
"v_mfma_f32_16x16x32_bf16 v[176:179], a[100:103], a[28:31], v[176:179]\n" \
"buffer_load_dwordx4 v227, s[48:51], s75 offen offset:3072 lds\n" \
"v_mfma_f32_16x16x32_bf16 v[64:67], a[64:67], a[0:3], v[64:67]\n" \
"s_waitcnt vmcnt(35)\n" \
"s_xor_b32 s75, s75, 0x300\n" \
"ds_read_b128 a[24:27], v230 offset:38912\n" \
"v_mfma_f32_16x16x32_bf16 v[64:67], a[68:71], a[4:7], v[64:67]\n" \
"s_add_u32 m0, s74, 102400\n" \
"ds_read_b128 a[28:31], v231 offset:38912\n" \
"v_cvt_pk_bf16_f32 v11, v253, v254\n" \
"v_mfma_f32_16x16x32_bf16 v[68:71], a[72:75], a[0:3], v[68:71]\n" \
"s_waitcnt lgkmcnt(4)\n" \
"ds_read_b64_tr_b16 a[96:97], v232 offset:1024\n" \
"v_accvgpr_read_b32 v243, a140\n" \
"v_mfma_f32_16x16x32_bf16 v[68:71], a[76:79], a[4:7], v[68:71]\n" \
"buffer_load_dwordx4 v224, s[48:51], s77 offen offset:0 lds\n" \
"v_mfma_f32_16x16x32_bf16 v[72:75], a[80:83], a[0:3], v[72:75]\n" \
"ds_read_b64_tr_b16 a[98:99], v232 offset:3072\n" \
"v_accvgpr_read_b32 v252, a141\n" \
"v_mfma_f32_16x16x32_bf16 v[72:75], a[84:87], a[4:7], v[72:75]\n" \
"s_waitcnt lgkmcnt(3)\n" \
"ds_read_b64_tr_b16 a[100:101], v232 offset:9216\n" \
"v_accvgpr_read_b32 v253, a142\n" \
"v_mfma_f32_16x16x32_bf16 v[76:79], a[88:91], a[0:3], v[76:79]\n" \
"ds_read_b64_tr_b16 a[102:103], v232 offset:11264\n" \
"v_accvgpr_read_b32 v254, a143\n" \
"v_mfma_f32_16x16x32_bf16 v[76:79], a[92:95], a[4:7], v[76:79]\n" \
"buffer_load_dwordx4 v225, s[48:51], s77 offen offset:1024 lds\n" \
"v_mfma_f32_16x16x32_bf16 v[108:111], a[88:91], a[8:11], v[108:111]\n" \
"ds_read_b64_tr_b16 a[104:105], v233 offset:1024\n" \
"v_mul_f32 v243, s31, v243\n" \
"v_mfma_f32_16x16x32_bf16 v[108:111], a[92:95], a[12:15], v[108:111]\n" \
"ds_read_b64_tr_b16 a[106:107], v233 offset:3072\n" \
"v_mul_f32 v252, s31, v252\n" \
"v_mfma_f32_16x16x32_bf16 v[104:107], a[80:83], a[8:11], v[104:107]\n" \
"s_waitcnt lgkmcnt(6)\n" \
"ds_read_b64_tr_b16 a[108:109], v233 offset:9216\n" \
"v_mul_f32 v253, s31, v253\n" \
"v_mfma_f32_16x16x32_bf16 v[104:107], a[84:87], a[12:15], v[104:107]\n" \
"buffer_load_dwordx4 v226, s[48:51], s77 offen offset:2048 lds\n" \
"v_mfma_f32_16x16x32_bf16 v[100:103], a[72:75], a[8:11], v[100:103]\n" \
"ds_read_b64_tr_b16 a[110:111], v233 offset:11264\n" \
"v_mul_f32 v254, s31, v254\n" \
"v_mfma_f32_16x16x32_bf16 v[100:103], a[76:79], a[12:15], v[100:103]\n" \
"ds_read_b64_tr_b16 a[112:113], v232 offset:1536\n" \
"v_cvt_pk_bf16_f32 v12, v243, v252\n" \
"v_mfma_f32_16x16x32_bf16 v[96:99], a[64:67], a[8:11], v[96:99]\n" \
"ds_read_b64_tr_b16 a[114:115], v232 offset:3584\n" \
"v_cvt_pk_bf16_f32 v13, v253, v254\n" \
"v_mfma_f32_16x16x32_bf16 v[96:99], a[68:71], a[12:15], v[96:99]\n" \
"buffer_load_dwordx4 v227, s[48:51], s77 offen offset:3072 lds\n" \
"v_mfma_f32_16x16x32_bf16 v[128:131], a[64:67], a[16:19], v[128:131]\n" \
"ds_read_b64_tr_b16 a[116:117], v232 offset:9728\n" \
"v_accvgpr_read_b32 v243, a156\n" \
"v_mfma_f32_16x16x32_bf16 v[128:131], a[68:71], a[20:23], v[128:131]\n" \
"ds_read_b64_tr_b16 a[118:119], v232 offset:11776\n" \
"v_accvgpr_read_b32 v252, a157\n" \
"v_mfma_f32_16x16x32_bf16 v[132:135], a[72:75], a[16:19], v[132:135]\n" \
"s_xor_b32 s77, s77, 0x300\n" \
"ds_read_b64_tr_b16 a[120:121], v233 offset:1536\n" \
"v_accvgpr_read_b32 v253, a158\n" \
"v_mfma_f32_16x16x32_bf16 v[132:135], a[76:79], a[20:23], v[132:135]\n" \
"v_accvgpr_read_b32 v254, a159\n" \
"v_mul_f32 v243, s31, v243\n" \
"v_mfma_f32_16x16x32_bf16 v[136:139], a[80:83], a[16:19], v[136:139]\n" \
"ds_read_b64_tr_b16 a[122:123], v233 offset:3584\n" \
"v_mul_f32 v252, s31, v252\n" \
"v_mfma_f32_16x16x32_bf16 v[136:139], a[84:87], a[20:23], v[136:139]\n" \
"ds_read_b64_tr_b16 a[124:125], v233 offset:9728\n" \
"v_mul_f32 v253, s31, v253\n" \
"v_mfma_f32_16x16x32_bf16 v[140:143], a[88:91], a[16:19], v[140:143]\n" \
"s_add_u32 s93, s87, 17858560\n" \
"ds_read_b64_tr_b16 a[126:127], v233 offset:11776\n" \
"v_mul_f32 v254, s31, v254\n" \
"v_mfma_f32_16x16x32_bf16 v[140:143], a[92:95], a[20:23], v[140:143]\n" \
"buffer_store_dwordx4 a[224:227], v234, s[56:59], s93 offen offset:0 sc1\n" \
"v_mfma_f32_16x16x32_bf16 v[172:175], a[88:91], a[24:27], v[172:175]\n" \
"v_cvt_pk_bf16_f32 v14, v243, v252\n" \
"v_cvt_pk_bf16_f32 v15, v253, v254\n" \
"v_mfma_f32_16x16x32_bf16 v[172:175], a[92:95], a[28:31], v[172:175]\n" \
"v_accvgpr_write_b32 a56, v244\n" \
"v_accvgpr_write_b32 a57, v245\n" \
"v_mfma_f32_16x16x32_bf16 v[168:171], a[80:83], a[24:27], v[168:171]\n" \
"v_accvgpr_write_b32 a59, v247\n" \
"v_accvgpr_write_b32 a60, v248\n" \
"v_mfma_f32_16x16x32_bf16 v[168:171], a[84:87], a[28:31], v[168:171]\n" \
"v_accvgpr_write_b32 a61, v249\n" \
"v_accvgpr_write_b32 a48, 0\n" \
"v_mfma_f32_16x16x32_bf16 v[164:167], a[72:75], a[24:27], v[164:167]\n" \
"v_accvgpr_write_b32 a49, 0\n" \
"v_accvgpr_write_b32 a50, 0\n" \
"v_mfma_f32_16x16x32_bf16 v[164:167], a[76:79], a[28:31], v[164:167]\n" \
"v_accvgpr_write_b32 a51, 0\n" \
"v_accvgpr_write_b32 a52, 0\n" \
"v_mfma_f32_16x16x32_bf16 v[160:163], a[64:67], a[24:27], v[160:163]\n" \
"v_accvgpr_write_b32 a53, 0\n" \
"v_mfma_f32_16x16x32_bf16 v[160:163], a[68:71], a[28:31], v[160:163]\n" \
"s_waitcnt vmcnt(35) lgkmcnt(0)\n" \
"s_barrier\n" \
"v_mfma_f32_16x16x32_bf16 v[80:83], a[96:99], a[0:3], v[80:83]\n" \
"ds_read_b64_tr_b16 a[64:65], v232 offset:16384\n" \
"v_mfma_f32_16x16x32_bf16 v[80:83], a[100:103], a[4:7], v[80:83]\n" \
"ds_read_b64_tr_b16 a[66:67], v232 offset:18432\n" \
"v_mfma_f32_16x16x32_bf16 v[84:87], a[104:107], a[0:3], v[84:87]\n" \
"s_add_u32 m0, s79, 0\n" \
"ds_read_b64_tr_b16 a[68:69], v232 offset:24576\n" \
"v_mfma_f32_16x16x32_bf16 v[84:87], a[108:111], a[4:7], v[84:87]\n" \
"buffer_load_dwordx4 v228, s[52:55], s78 offen offset:0 lds\n" \
"v_mfma_f32_16x16x32_bf16 v[88:91], a[112:115], a[0:3], v[88:91]\n" \
"ds_read_b64_tr_b16 a[70:71], v232 offset:26624\n" \
"v_mfma_f32_16x16x32_bf16 v[88:91], a[116:119], a[4:7], v[88:91]\n" \
"ds_read_b64_tr_b16 a[72:73], v233 offset:16384\n" \
"v_mfma_f32_16x16x32_bf16 v[92:95], a[120:123], a[0:3], v[92:95]\n" \
"ds_read_b64_tr_b16 a[74:75], v233 offset:18432\n" \
"v_mfma_f32_16x16x32_bf16 v[92:95], a[124:127], a[4:7], v[92:95]\n" \
"buffer_load_dwordx4 v229, s[52:55], s78 offen offset:1024 lds\n" \
"v_mfma_f32_16x16x32_bf16 v[124:127], a[120:123], a[8:11], v[124:127]\n" \
"s_waitcnt vmcnt(35)\n" \
"ds_read_b128 a[0:3], v250 offset:0\n" \
"v_mfma_f32_16x16x32_bf16 v[124:127], a[124:127], a[12:15], v[124:127]\n" \
"ds_read_b128 a[4:7], v251 offset:0\n" \
"v_mfma_f32_16x16x32_bf16 v[120:123], a[112:115], a[8:11], v[120:123]\n" \
"s_xor_b32 s78, s78, 0x80000\n" \
"ds_read_b64_tr_b16 a[76:77], v233 offset:24576\n" \
"v_mfma_f32_16x16x32_bf16 v[120:123], a[116:119], a[12:15], v[120:123]\n" \
"buffer_load_dwordx4 v228, s[52:55], s89 offen offset:2048 lds\n" \
"v_mfma_f32_16x16x32_bf16 v[116:119], a[104:107], a[8:11], v[116:119]\n" \
"ds_read_b64_tr_b16 a[78:79], v233 offset:26624\n" \
"v_mfma_f32_16x16x32_bf16 v[116:119], a[108:111], a[12:15], v[116:119]\n" \
"ds_read_b64_tr_b16 a[80:81], v232 offset:16896\n" \
"v_mfma_f32_16x16x32_bf16 v[112:115], a[96:99], a[8:11], v[112:115]\n" \
"ds_read_b64_tr_b16 a[82:83], v232 offset:18944\n" \
"v_mfma_f32_16x16x32_bf16 v[112:115], a[100:103], a[12:15], v[112:115]\n" \
"buffer_load_dwordx4 v229, s[52:55], s89 offen offset:3072 lds\n" \
"v_mfma_f32_16x16x32_bf16 v[144:147], a[96:99], a[16:19], v[144:147]\n" \
"s_waitcnt vmcnt(35)\n" \
"s_xor_b32 s89, s89, 0x80000\n" \
"ds_read_b128 a[8:11], v250 offset:2048\n" \
"v_mfma_f32_16x16x32_bf16 v[144:147], a[100:103], a[20:23], v[144:147]\n" \
"s_add_u32 m0, s74, 131072\n" \
"ds_read_b128 a[12:15], v251 offset:2048\n" \
"v_mfma_f32_16x16x32_bf16 v[148:151], a[104:107], a[16:19], v[148:151]\n" \
"s_waitcnt lgkmcnt(6)\n" \
"ds_read_b64_tr_b16 a[84:85], v232 offset:25088\n" \
"v_mfma_f32_16x16x32_bf16 v[148:151], a[108:111], a[20:23], v[148:151]\n" \
"buffer_load_dwordx4 v224, s[48:51], s75 offen offset:0 lds\n" \
"v_mfma_f32_16x16x32_bf16 v[152:155], a[112:115], a[16:19], v[152:155]\n" \
"ds_read_b64_tr_b16 a[86:87], v232 offset:27136\n" \
"v_mfma_f32_16x16x32_bf16 v[152:155], a[116:119], a[20:23], v[152:155]\n" \
"ds_read_b64_tr_b16 a[88:89], v233 offset:16896\n" \
"v_mfma_f32_16x16x32_bf16 v[156:159], a[120:123], a[16:19], v[156:159]\n" \
"ds_read_b64_tr_b16 a[90:91], v233 offset:18944\n" \
"v_mfma_f32_16x16x32_bf16 v[156:159], a[124:127], a[20:23], v[156:159]\n" \
"buffer_load_dwordx4 v225, s[48:51], s75 offen offset:1024 lds\n" \
"v_mfma_f32_16x16x32_bf16 v[188:191], a[120:123], a[24:27], v[188:191]\n" \
"s_waitcnt vmcnt(35)\n" \
"ds_read_b128 a[16:19], v250 offset:4096\n" \
"v_mfma_f32_16x16x32_bf16 v[188:191], a[124:127], a[28:31], v[188:191]\n" \
"ds_read_b128 a[20:23], v251 offset:4096\n" \
"v_mfma_f32_16x16x32_bf16 v[184:187], a[112:115], a[24:27], v[184:187]\n" \
"s_waitcnt lgkmcnt(6)\n" \
"ds_read_b64_tr_b16 a[92:93], v233 offset:25088\n" \
"v_mfma_f32_16x16x32_bf16 v[184:187], a[116:119], a[28:31], v[184:187]\n" \
"buffer_load_dwordx4 v226, s[48:51], s75 offen offset:2048 lds\n" \
"v_mfma_f32_16x16x32_bf16 v[180:183], a[104:107], a[24:27], v[180:183]\n" \
"ds_read_b64_tr_b16 a[94:95], v233 offset:27136\n" \
"v_mfma_f32_16x16x32_bf16 v[180:183], a[108:111], a[28:31], v[180:183]\n" \
"v_mfma_f32_16x16x32_bf16 v[176:179], a[96:99], a[24:27], v[176:179]\n" \
"v_mfma_f32_16x16x32_bf16 v[176:179], a[100:103], a[28:31], v[176:179]\n" \
"buffer_load_dwordx4 v227, s[48:51], s75 offen offset:3072 lds\n" \
"v_mfma_f32_16x16x32_bf16 v[64:67], a[64:67], a[0:3], v[64:67]\n" \
"s_waitcnt vmcnt(35)\n" \
"s_xor_b32 s75, s75, 0x80\n" \
"ds_read_b128 a[24:27], v250 offset:6144\n" \
"v_mfma_f32_16x16x32_bf16 v[64:67], a[68:71], a[4:7], v[64:67]\n" \
"s_add_u32 m0, s74, 135168\n" \
"ds_read_b128 a[28:31], v251 offset:6144\n" \
"v_mfma_f32_16x16x32_bf16 v[68:71], a[72:75], a[0:3], v[68:71]\n" \
"s_waitcnt lgkmcnt(2)\n" \
"ds_read_b64_tr_b16 a[96:97], v232 offset:17408\n" \
"v_mfma_f32_16x16x32_bf16 v[68:71], a[76:79], a[4:7], v[68:71]\n" \
"buffer_load_dwordx4 v224, s[48:51], s77 offen offset:0 lds\n" \
"v_mfma_f32_16x16x32_bf16 v[72:75], a[80:83], a[0:3], v[72:75]\n" \
"ds_read_b64_tr_b16 a[98:99], v232 offset:19456\n" \
"v_mfma_f32_16x16x32_bf16 v[72:75], a[84:87], a[4:7], v[72:75]\n" \
"ds_read_b64_tr_b16 a[100:101], v232 offset:25600\n" \
"v_mfma_f32_16x16x32_bf16 v[76:79], a[88:91], a[0:3], v[76:79]\n" \
"ds_read_b64_tr_b16 a[102:103], v232 offset:27648\n" \
"v_mfma_f32_16x16x32_bf16 v[76:79], a[92:95], a[4:7], v[76:79]\n" \
"buffer_load_dwordx4 v225, s[48:51], s77 offen offset:1024 lds\n" \
"v_mfma_f32_16x16x32_bf16 v[108:111], a[88:91], a[8:11], v[108:111]\n" \
"ds_read_b64_tr_b16 a[104:105], v233 offset:17408\n" \
"v_mfma_f32_16x16x32_bf16 v[108:111], a[92:95], a[12:15], v[108:111]\n" \
"ds_read_b64_tr_b16 a[106:107], v233 offset:19456\n" \
"v_mfma_f32_16x16x32_bf16 v[104:107], a[80:83], a[8:11], v[104:107]\n" \
"s_waitcnt lgkmcnt(6)\n" \
"ds_read_b64_tr_b16 a[108:109], v233 offset:25600\n" \
"v_mfma_f32_16x16x32_bf16 v[104:107], a[84:87], a[12:15], v[104:107]\n" \
"buffer_load_dwordx4 v226, s[48:51], s77 offen offset:2048 lds\n" \
"v_mfma_f32_16x16x32_bf16 v[100:103], a[72:75], a[8:11], v[100:103]\n" \
"ds_read_b64_tr_b16 a[110:111], v233 offset:27648\n" \
"v_mfma_f32_16x16x32_bf16 v[100:103], a[76:79], a[12:15], v[100:103]\n" \
"ds_read_b64_tr_b16 a[112:113], v232 offset:17920\n" \
"v_mfma_f32_16x16x32_bf16 v[96:99], a[64:67], a[8:11], v[96:99]\n" \
"ds_read_b64_tr_b16 a[114:115], v232 offset:19968\n" \
"v_mfma_f32_16x16x32_bf16 v[96:99], a[68:71], a[12:15], v[96:99]\n" \
"buffer_load_dwordx4 v227, s[48:51], s77 offen offset:3072 lds\n" \
"v_mfma_f32_16x16x32_bf16 v[128:131], a[64:67], a[16:19], v[128:131]\n" \
"ds_read_b64_tr_b16 a[116:117], v232 offset:26112\n" \
"v_mfma_f32_16x16x32_bf16 v[128:131], a[68:71], a[20:23], v[128:131]\n" \
"Lpg3894_%=:\n" \
"s_cbranch_execz Lpg6043_%=\n" \
"ds_read_b64_tr_b16 a[118:119], v232 offset:28160\n" \
"v_mfma_f32_16x16x32_bf16 v[132:135], a[72:75], a[16:19], v[132:135]\n" \
"s_xor_b32 s77, s77, 0x80\n" \
"ds_read_b64_tr_b16 a[120:121], v233 offset:17920\n" \
"v_mfma_f32_16x16x32_bf16 v[132:135], a[76:79], a[20:23], v[132:135]\n" \
"v_mfma_f32_16x16x32_bf16 v[136:139], a[80:83], a[16:19], v[136:139]\n" \
"ds_read_b64_tr_b16 a[122:123], v233 offset:19968\n" \
"v_mfma_f32_16x16x32_bf16 v[136:139], a[84:87], a[20:23], v[136:139]\n" \
"ds_read_b64_tr_b16 a[124:125], v233 offset:26112\n" \
"v_mfma_f32_16x16x32_bf16 v[140:143], a[88:91], a[16:19], v[140:143]\n" \
"ds_read_b64_tr_b16 a[126:127], v233 offset:28160\n" \
"v_mfma_f32_16x16x32_bf16 v[140:143], a[92:95], a[20:23], v[140:143]\n" \
"buffer_store_dwordx4 a[228:231], v234, s[56:59], s93 offen offset:1024 sc1\n" \
"v_mfma_f32_16x16x32_bf16 v[172:175], a[88:91], a[24:27], v[172:175]\n" \
"v_mfma_f32_16x16x32_bf16 v[172:175], a[92:95], a[28:31], v[172:175]\n" \
"v_mfma_f32_16x16x32_bf16 v[168:171], a[80:83], a[24:27], v[168:171]\n" \
"v_mfma_f32_16x16x32_bf16 v[168:171], a[84:87], a[28:31], v[168:171]\n" \
"v_mfma_f32_16x16x32_bf16 v[164:167], a[72:75], a[24:27], v[164:167]\n" \
"v_mfma_f32_16x16x32_bf16 v[164:167], a[76:79], a[28:31], v[164:167]\n" \
"v_mfma_f32_16x16x32_bf16 v[160:163], a[64:67], a[24:27], v[160:163]\n" \
"v_mfma_f32_16x16x32_bf16 v[160:163], a[68:71], a[28:31], v[160:163]\n" \
"s_waitcnt vmcnt(35) lgkmcnt(0)\n" \
"s_barrier\n" \
"v_mfma_f32_16x16x32_bf16 v[80:83], a[96:99], a[0:3], v[80:83]\n" \
"ds_read_b64_tr_b16 a[64:65], v232 offset:32768\n" \
"v_mfma_f32_16x16x32_bf16 v[80:83], a[100:103], a[4:7], v[80:83]\n" \
"ds_read_b64_tr_b16 a[66:67], v232 offset:34816\n" \
"v_mfma_f32_16x16x32_bf16 v[84:87], a[104:107], a[0:3], v[84:87]\n" \
"s_add_u32 m0, s79, 16384\n" \
"ds_read_b64_tr_b16 a[68:69], v232 offset:40960\n" \
"v_mfma_f32_16x16x32_bf16 v[84:87], a[108:111], a[4:7], v[84:87]\n" \
"buffer_load_dwordx4 v228, s[52:55], s78 offen offset:0 lds\n" \
"v_mfma_f32_16x16x32_bf16 v[88:91], a[112:115], a[0:3], v[88:91]\n" \
"ds_read_b64_tr_b16 a[70:71], v232 offset:43008\n" \
"v_mfma_f32_16x16x32_bf16 v[88:91], a[116:119], a[4:7], v[88:91]\n" \
"ds_read_b64_tr_b16 a[72:73], v233 offset:32768\n" \
"v_mfma_f32_16x16x32_bf16 v[92:95], a[120:123], a[0:3], v[92:95]\n" \
"ds_read_b64_tr_b16 a[74:75], v233 offset:34816\n" \
"v_mfma_f32_16x16x32_bf16 v[92:95], a[124:127], a[4:7], v[92:95]\n" \
"buffer_load_dwordx4 v229, s[52:55], s78 offen offset:1024 lds\n" \
"v_mfma_f32_16x16x32_bf16 v[124:127], a[120:123], a[8:11], v[124:127]\n" \
"s_waitcnt vmcnt(35)\n" \
"ds_read_b128 a[0:3], v230 offset:0\n" \
"v_mfma_f32_16x16x32_bf16 v[124:127], a[124:127], a[12:15], v[124:127]\n" \
"ds_read_b128 a[4:7], v231 offset:0\n" \
"v_mfma_f32_16x16x32_bf16 v[120:123], a[112:115], a[8:11], v[120:123]\n" \
"s_xor_b32 s78, s78, 0x180000\n" \
"ds_read_b64_tr_b16 a[76:77], v233 offset:40960\n" \
"v_mfma_f32_16x16x32_bf16 v[120:123], a[116:119], a[12:15], v[120:123]\n" \
"buffer_load_dwordx4 v228, s[52:55], s89 offen offset:2048 lds\n" \
"v_mfma_f32_16x16x32_bf16 v[116:119], a[104:107], a[8:11], v[116:119]\n" \
"ds_read_b64_tr_b16 a[78:79], v233 offset:43008\n" \
"v_mfma_f32_16x16x32_bf16 v[116:119], a[108:111], a[12:15], v[116:119]\n" \
"ds_read_b64_tr_b16 a[80:81], v232 offset:33280\n" \
"v_mfma_f32_16x16x32_bf16 v[112:115], a[96:99], a[8:11], v[112:115]\n" \
"ds_read_b64_tr_b16 a[82:83], v232 offset:35328\n" \
"v_mfma_f32_16x16x32_bf16 v[112:115], a[100:103], a[12:15], v[112:115]\n" \
"buffer_load_dwordx4 v229, s[52:55], s89 offen offset:3072 lds\n" \
"v_mfma_f32_16x16x32_bf16 v[144:147], a[96:99], a[16:19], v[144:147]\n" \
"s_waitcnt vmcnt(35)\n" \
"s_xor_b32 s89, s89, 0x180000\n" \
"ds_read_b128 a[8:11], v230 offset:2048\n" \
"v_mfma_f32_16x16x32_bf16 v[144:147], a[100:103], a[20:23], v[144:147]\n" \
"s_add_u32 m0, s74, 65536\n" \
"ds_read_b128 a[12:15], v231 offset:2048\n" \
"v_mfma_f32_16x16x32_bf16 v[148:151], a[104:107], a[16:19], v[148:151]\n" \
"s_waitcnt lgkmcnt(6)\n" \
"ds_read_b64_tr_b16 a[84:85], v232 offset:41472\n" \
"v_mfma_f32_16x16x32_bf16 v[148:151], a[108:111], a[20:23], v[148:151]\n" \
"buffer_load_dwordx4 v224, s[48:51], s75 offen offset:0 lds\n" \
"v_mfma_f32_16x16x32_bf16 v[152:155], a[112:115], a[16:19], v[152:155]\n" \
"ds_read_b64_tr_b16 a[86:87], v232 offset:43520\n" \
"v_mfma_f32_16x16x32_bf16 v[152:155], a[116:119], a[20:23], v[152:155]\n" \
"ds_read_b64_tr_b16 a[88:89], v233 offset:33280\n" \
"v_mfma_f32_16x16x32_bf16 v[156:159], a[120:123], a[16:19], v[156:159]\n" \
"ds_read_b64_tr_b16 a[90:91], v233 offset:35328\n" \
"v_mfma_f32_16x16x32_bf16 v[156:159], a[124:127], a[20:23], v[156:159]\n" \
"buffer_load_dwordx4 v225, s[48:51], s75 offen offset:1024 lds\n" \
"v_mfma_f32_16x16x32_bf16 v[188:191], a[120:123], a[24:27], v[188:191]\n" \
"s_waitcnt vmcnt(35)\n" \
"ds_read_b128 a[16:19], v230 offset:4096\n" \
"v_mfma_f32_16x16x32_bf16 v[188:191], a[124:127], a[28:31], v[188:191]\n" \
"ds_read_b128 a[20:23], v231 offset:4096\n" \
"v_mfma_f32_16x16x32_bf16 v[184:187], a[112:115], a[24:27], v[184:187]\n" \
"s_waitcnt lgkmcnt(6)\n" \
"ds_read_b64_tr_b16 a[92:93], v233 offset:41472\n" \
"v_mfma_f32_16x16x32_bf16 v[184:187], a[116:119], a[28:31], v[184:187]\n" \
"buffer_load_dwordx4 v226, s[48:51], s75 offen offset:2048 lds\n" \
"v_mfma_f32_16x16x32_bf16 v[180:183], a[104:107], a[24:27], v[180:183]\n" \
"ds_read_b64_tr_b16 a[94:95], v233 offset:43520\n" \
"v_mfma_f32_16x16x32_bf16 v[180:183], a[108:111], a[28:31], v[180:183]\n" \
"v_mfma_f32_16x16x32_bf16 v[176:179], a[96:99], a[24:27], v[176:179]\n" \
"v_mfma_f32_16x16x32_bf16 v[176:179], a[100:103], a[28:31], v[176:179]\n" \
"buffer_load_dwordx4 v227, s[48:51], s75 offen offset:3072 lds\n" \
"v_mfma_f32_16x16x32_bf16 v[64:67], a[64:67], a[0:3], v[64:67]\n" \
"s_waitcnt vmcnt(35)\n" \
"s_xor_b32 s75, s75, 0x180\n" \
"ds_read_b128 a[24:27], v230 offset:6144\n" \
"v_mfma_f32_16x16x32_bf16 v[64:67], a[68:71], a[4:7], v[64:67]\n" \
"s_add_u32 m0, s74, 69632\n" \
"ds_read_b128 a[28:31], v231 offset:6144\n" \
"v_mfma_f32_16x16x32_bf16 v[68:71], a[72:75], a[0:3], v[68:71]\n" \
"s_waitcnt lgkmcnt(2)\n" \
"ds_read_b64_tr_b16 a[96:97], v232 offset:33792\n" \
"v_mfma_f32_16x16x32_bf16 v[68:71], a[76:79], a[4:7], v[68:71]\n" \
"buffer_load_dwordx4 v224, s[48:51], s77 offen offset:0 lds\n" \
"v_mfma_f32_16x16x32_bf16 v[72:75], a[80:83], a[0:3], v[72:75]\n" \
"ds_read_b64_tr_b16 a[98:99], v232 offset:35840\n" \
"v_mfma_f32_16x16x32_bf16 v[72:75], a[84:87], a[4:7], v[72:75]\n" \
"ds_read_b64_tr_b16 a[100:101], v232 offset:41984\n" \
"v_mfma_f32_16x16x32_bf16 v[76:79], a[88:91], a[0:3], v[76:79]\n" \
"ds_read_b64_tr_b16 a[102:103], v232 offset:44032\n" \
"v_mfma_f32_16x16x32_bf16 v[76:79], a[92:95], a[4:7], v[76:79]\n" \
"buffer_load_dwordx4 v225, s[48:51], s77 offen offset:1024 lds\n" \
"v_mfma_f32_16x16x32_bf16 v[108:111], a[88:91], a[8:11], v[108:111]\n" \
"ds_read_b64_tr_b16 a[104:105], v233 offset:33792\n" \
"v_mfma_f32_16x16x32_bf16 v[108:111], a[92:95], a[12:15], v[108:111]\n" \
"ds_read_b64_tr_b16 a[106:107], v233 offset:35840\n" \
"v_mfma_f32_16x16x32_bf16 v[104:107], a[80:83], a[8:11], v[104:107]\n" \
"s_waitcnt lgkmcnt(6)\n" \
"ds_read_b64_tr_b16 a[108:109], v233 offset:41984\n" \
"v_mfma_f32_16x16x32_bf16 v[104:107], a[84:87], a[12:15], v[104:107]\n" \
"buffer_load_dwordx4 v226, s[48:51], s77 offen offset:2048 lds\n" \
"v_mfma_f32_16x16x32_bf16 v[100:103], a[72:75], a[8:11], v[100:103]\n" \
"ds_read_b64_tr_b16 a[110:111], v233 offset:44032\n" \
"v_mfma_f32_16x16x32_bf16 v[100:103], a[76:79], a[12:15], v[100:103]\n" \
"ds_read_b64_tr_b16 a[112:113], v232 offset:34304\n" \
"v_mfma_f32_16x16x32_bf16 v[96:99], a[64:67], a[8:11], v[96:99]\n" \
"ds_read_b64_tr_b16 a[114:115], v232 offset:36352\n" \
"v_mfma_f32_16x16x32_bf16 v[96:99], a[68:71], a[12:15], v[96:99]\n" \
"buffer_load_dwordx4 v227, s[48:51], s77 offen offset:3072 lds\n" \
"v_mfma_f32_16x16x32_bf16 v[128:131], a[64:67], a[16:19], v[128:131]\n" \
"ds_read_b64_tr_b16 a[116:117], v232 offset:42496\n" \
"v_mfma_f32_16x16x32_bf16 v[128:131], a[68:71], a[20:23], v[128:131]\n" \
"ds_read_b64_tr_b16 a[118:119], v232 offset:44544\n" \
"v_mfma_f32_16x16x32_bf16 v[132:135], a[72:75], a[16:19], v[132:135]\n" \
"s_xor_b32 s77, s77, 0x180\n" \
"ds_read_b64_tr_b16 a[120:121], v233 offset:34304\n" \
"v_mfma_f32_16x16x32_bf16 v[132:135], a[76:79], a[20:23], v[132:135]\n" \
"buffer_store_dwordx4 a[232:235], v234, s[56:59], s93 offen offset:2048 sc1\n" \
"v_mfma_f32_16x16x32_bf16 v[136:139], a[80:83], a[16:19], v[136:139]\n" \
"ds_read_b64_tr_b16 a[122:123], v233 offset:36352\n" \
"v_mfma_f32_16x16x32_bf16 v[136:139], a[84:87], a[20:23], v[136:139]\n" \
"ds_read_b64_tr_b16 a[124:125], v233 offset:42496\n" \
"v_mfma_f32_16x16x32_bf16 v[140:143], a[88:91], a[16:19], v[140:143]\n" \
"ds_read_b64_tr_b16 a[126:127], v233 offset:44544\n" \
"v_mfma_f32_16x16x32_bf16 v[140:143], a[92:95], a[20:23], v[140:143]\n" \
"v_mfma_f32_16x16x32_bf16 v[172:175], a[88:91], a[24:27], v[172:175]\n" \
"v_mfma_f32_16x16x32_bf16 v[172:175], a[92:95], a[28:31], v[172:175]\n" \
"v_mfma_f32_16x16x32_bf16 v[168:171], a[80:83], a[24:27], v[168:171]\n" \
"v_mfma_f32_16x16x32_bf16 v[168:171], a[84:87], a[28:31], v[168:171]\n" \
"buffer_store_dwordx4 a[236:239], v234, s[56:59], s93 offen offset:3072 sc1\n" \
"v_mfma_f32_16x16x32_bf16 v[164:167], a[72:75], a[24:27], v[164:167]\n" \
"v_mfma_f32_16x16x32_bf16 v[164:167], a[76:79], a[28:31], v[164:167]\n" \
"v_mfma_f32_16x16x32_bf16 v[160:163], a[64:67], a[24:27], v[160:163]\n" \
"v_mfma_f32_16x16x32_bf16 v[160:163], a[68:71], a[28:31], v[160:163]\n" \
"s_waitcnt vmcnt(36) lgkmcnt(0)\n" \
"s_barrier\n" \
"v_mfma_f32_16x16x32_bf16 v[80:83], a[96:99], a[0:3], v[80:83]\n" \
"ds_read_b64_tr_b16 a[64:65], v232 offset:49152\n" \
"v_mfma_f32_16x16x32_bf16 v[80:83], a[100:103], a[4:7], v[80:83]\n" \
"ds_read_b64_tr_b16 a[66:67], v232 offset:51200\n" \
"v_mfma_f32_16x16x32_bf16 v[84:87], a[104:107], a[0:3], v[84:87]\n" \
"s_add_u32 m0, s79, 32768\n" \
"ds_read_b64_tr_b16 a[68:69], v232 offset:57344\n" \
"v_mfma_f32_16x16x32_bf16 v[84:87], a[108:111], a[4:7], v[84:87]\n" \
"buffer_load_dwordx4 v228, s[52:55], s78 offen offset:0 lds\n" \
"v_mfma_f32_16x16x32_bf16 v[88:91], a[112:115], a[0:3], v[88:91]\n" \
"ds_read_b64_tr_b16 a[70:71], v232 offset:59392\n" \
"v_mfma_f32_16x16x32_bf16 v[88:91], a[116:119], a[4:7], v[88:91]\n" \
"ds_read_b64_tr_b16 a[72:73], v233 offset:49152\n" \
"v_mfma_f32_16x16x32_bf16 v[92:95], a[120:123], a[0:3], v[92:95]\n" \
"ds_read_b64_tr_b16 a[74:75], v233 offset:51200\n" \
"v_mfma_f32_16x16x32_bf16 v[92:95], a[124:127], a[4:7], v[92:95]\n" \
"buffer_load_dwordx4 v229, s[52:55], s78 offen offset:1024 lds\n" \
"v_mfma_f32_16x16x32_bf16 v[124:127], a[120:123], a[8:11], v[124:127]\n" \
"s_waitcnt vmcnt(36)\n" \
"ds_read_b128 a[0:3], v230 offset:32768\n" \
"v_mfma_f32_16x16x32_bf16 v[124:127], a[124:127], a[12:15], v[124:127]\n" \
"ds_read_b128 a[4:7], v231 offset:32768\n" \
"v_mfma_f32_16x16x32_bf16 v[120:123], a[112:115], a[8:11], v[120:123]\n" \
"s_xor_b32 s78, s78, 0x80000\n" \
"ds_read_b64_tr_b16 a[76:77], v233 offset:57344\n" \
"v_mfma_f32_16x16x32_bf16 v[120:123], a[116:119], a[12:15], v[120:123]\n" \
"buffer_load_dwordx4 v228, s[52:55], s89 offen offset:2048 lds\n" \
"v_mfma_f32_16x16x32_bf16 v[116:119], a[104:107], a[8:11], v[116:119]\n" \
"ds_read_b64_tr_b16 a[78:79], v233 offset:59392\n" \
"v_mfma_f32_16x16x32_bf16 v[116:119], a[108:111], a[12:15], v[116:119]\n" \
"ds_read_b64_tr_b16 a[80:81], v232 offset:49664\n" \
"v_mfma_f32_16x16x32_bf16 v[112:115], a[96:99], a[8:11], v[112:115]\n" \
"ds_read_b64_tr_b16 a[82:83], v232 offset:51712\n" \
"v_mfma_f32_16x16x32_bf16 v[112:115], a[100:103], a[12:15], v[112:115]\n" \
"buffer_load_dwordx4 v229, s[52:55], s89 offen offset:3072 lds\n" \
"v_mfma_f32_16x16x32_bf16 v[144:147], a[96:99], a[16:19], v[144:147]\n" \
"s_waitcnt vmcnt(36)\n" \
"s_xor_b32 s89, s89, 0x80000\n" \
"ds_read_b128 a[8:11], v230 offset:34816\n" \
"v_mfma_f32_16x16x32_bf16 v[144:147], a[100:103], a[20:23], v[144:147]\n" \
"s_add_u32 m0, s74, 98304\n" \
"ds_read_b128 a[12:15], v231 offset:34816\n" \
"v_mfma_f32_16x16x32_bf16 v[148:151], a[104:107], a[16:19], v[148:151]\n" \
"s_waitcnt lgkmcnt(6)\n" \
"ds_read_b64_tr_b16 a[84:85], v232 offset:57856\n" \
"v_mfma_f32_16x16x32_bf16 v[148:151], a[108:111], a[20:23], v[148:151]\n" \
"buffer_load_dwordx4 v224, s[48:51], s75 offen offset:0 lds\n" \
"v_mfma_f32_16x16x32_bf16 v[152:155], a[112:115], a[16:19], v[152:155]\n" \
"ds_read_b64_tr_b16 a[86:87], v232 offset:59904\n" \
"v_mfma_f32_16x16x32_bf16 v[152:155], a[116:119], a[20:23], v[152:155]\n" \
"ds_read_b64_tr_b16 a[88:89], v233 offset:49664\n" \
"v_mfma_f32_16x16x32_bf16 v[156:159], a[120:123], a[16:19], v[156:159]\n" \
"ds_read_b64_tr_b16 a[90:91], v233 offset:51712\n" \
"v_mfma_f32_16x16x32_bf16 v[156:159], a[124:127], a[20:23], v[156:159]\n" \
"buffer_load_dwordx4 v225, s[48:51], s75 offen offset:1024 lds\n" \
"v_mfma_f32_16x16x32_bf16 v[188:191], a[120:123], a[24:27], v[188:191]\n" \
"s_waitcnt vmcnt(36)\n" \
"ds_read_b128 a[16:19], v230 offset:36864\n" \
"v_mfma_f32_16x16x32_bf16 v[188:191], a[124:127], a[28:31], v[188:191]\n" \
"ds_read_b128 a[20:23], v231 offset:36864\n" \
"v_mfma_f32_16x16x32_bf16 v[184:187], a[112:115], a[24:27], v[184:187]\n" \
"s_waitcnt lgkmcnt(6)\n" \
"ds_read_b64_tr_b16 a[92:93], v233 offset:57856\n" \
"v_mfma_f32_16x16x32_bf16 v[184:187], a[116:119], a[28:31], v[184:187]\n" \
"buffer_load_dwordx4 v226, s[48:51], s75 offen offset:2048 lds\n" \
"v_mfma_f32_16x16x32_bf16 v[180:183], a[104:107], a[24:27], v[180:183]\n" \
"ds_read_b64_tr_b16 a[94:95], v233 offset:59904\n" \
"v_mfma_f32_16x16x32_bf16 v[180:183], a[108:111], a[28:31], v[180:183]\n" \
"v_mfma_f32_16x16x32_bf16 v[176:179], a[96:99], a[24:27], v[176:179]\n" \
"v_mfma_f32_16x16x32_bf16 v[176:179], a[100:103], a[28:31], v[176:179]\n" \
"buffer_load_dwordx4 v227, s[48:51], s75 offen offset:3072 lds\n" \
"v_mfma_f32_16x16x32_bf16 v[64:67], a[64:67], a[0:3], v[64:67]\n" \
"s_waitcnt vmcnt(36)\n" \
"s_xor_b32 s75, s75, 0x80\n" \
"ds_read_b128 a[24:27], v230 offset:38912\n" \
"v_mfma_f32_16x16x32_bf16 v[64:67], a[68:71], a[4:7], v[64:67]\n" \
"s_add_u32 m0, s74, 102400\n" \
"ds_read_b128 a[28:31], v231 offset:38912\n" \
"v_mfma_f32_16x16x32_bf16 v[68:71], a[72:75], a[0:3], v[68:71]\n" \
"s_waitcnt lgkmcnt(2)\n" \
"ds_read_b64_tr_b16 a[96:97], v232 offset:50176\n" \
"v_mfma_f32_16x16x32_bf16 v[68:71], a[76:79], a[4:7], v[68:71]\n" \
"buffer_load_dwordx4 v224, s[48:51], s77 offen offset:0 lds\n" \
"v_mfma_f32_16x16x32_bf16 v[72:75], a[80:83], a[0:3], v[72:75]\n" \
"ds_read_b64_tr_b16 a[98:99], v232 offset:52224\n" \
"v_mfma_f32_16x16x32_bf16 v[72:75], a[84:87], a[4:7], v[72:75]\n" \
"ds_read_b64_tr_b16 a[100:101], v232 offset:58368\n" \
"v_mfma_f32_16x16x32_bf16 v[76:79], a[88:91], a[0:3], v[76:79]\n" \
"ds_read_b64_tr_b16 a[102:103], v232 offset:60416\n" \
"v_mfma_f32_16x16x32_bf16 v[76:79], a[92:95], a[4:7], v[76:79]\n" \
"buffer_load_dwordx4 v225, s[48:51], s77 offen offset:1024 lds\n" \
"v_mfma_f32_16x16x32_bf16 v[108:111], a[88:91], a[8:11], v[108:111]\n" \
"ds_read_b64_tr_b16 a[104:105], v233 offset:50176\n" \
"v_mfma_f32_16x16x32_bf16 v[108:111], a[92:95], a[12:15], v[108:111]\n" \
"ds_read_b64_tr_b16 a[106:107], v233 offset:52224\n" \
"v_mfma_f32_16x16x32_bf16 v[104:107], a[80:83], a[8:11], v[104:107]\n" \
"s_waitcnt lgkmcnt(6)\n" \
"ds_read_b64_tr_b16 a[108:109], v233 offset:58368\n" \
"v_mfma_f32_16x16x32_bf16 v[104:107], a[84:87], a[12:15], v[104:107]\n" \
"buffer_load_dwordx4 v226, s[48:51], s77 offen offset:2048 lds\n" \
"v_mfma_f32_16x16x32_bf16 v[100:103], a[72:75], a[8:11], v[100:103]\n" \
"ds_read_b64_tr_b16 a[110:111], v233 offset:60416\n" \
"v_mfma_f32_16x16x32_bf16 v[100:103], a[76:79], a[12:15], v[100:103]\n" \
"ds_read_b64_tr_b16 a[112:113], v232 offset:50688\n" \
"v_mfma_f32_16x16x32_bf16 v[96:99], a[64:67], a[8:11], v[96:99]\n" \
"ds_read_b64_tr_b16 a[114:115], v232 offset:52736\n" \
"v_mfma_f32_16x16x32_bf16 v[96:99], a[68:71], a[12:15], v[96:99]\n" \
"buffer_load_dwordx4 v227, s[48:51], s77 offen offset:3072 lds\n" \
"v_mfma_f32_16x16x32_bf16 v[128:131], a[64:67], a[16:19], v[128:131]\n" \
"ds_read_b64_tr_b16 a[116:117], v232 offset:58880\n" \
"v_mfma_f32_16x16x32_bf16 v[128:131], a[68:71], a[20:23], v[128:131]\n" \
"ds_read_b64_tr_b16 a[118:119], v232 offset:60928\n" \
"v_mfma_f32_16x16x32_bf16 v[132:135], a[72:75], a[16:19], v[132:135]\n" \
"s_xor_b32 s77, s77, 0x80\n" \
"s_add_u32 s93, s87, 17850368\n" \
"ds_read_b64_tr_b16 a[120:121], v233 offset:50688\n" \
"v_mfma_f32_16x16x32_bf16 v[132:135], a[76:79], a[20:23], v[132:135]\n" \
"buffer_store_dwordx4 a[192:195], v234, s[56:59], s93 offen offset:0 sc1\n" \
"v_mfma_f32_16x16x32_bf16 v[136:139], a[80:83], a[16:19], v[136:139]\n" \
"ds_read_b64_tr_b16 a[122:123], v233 offset:52736\n" \
"v_mfma_f32_16x16x32_bf16 v[136:139], a[84:87], a[20:23], v[136:139]\n" \
"ds_read_b64_tr_b16 a[124:125], v233 offset:58880\n" \
"v_mfma_f32_16x16x32_bf16 v[140:143], a[88:91], a[16:19], v[140:143]\n" \
"ds_read_b64_tr_b16 a[126:127], v233 offset:60928\n" \
"v_mfma_f32_16x16x32_bf16 v[140:143], a[92:95], a[20:23], v[140:143]\n" \
"v_mfma_f32_16x16x32_bf16 v[172:175], a[88:91], a[24:27], v[172:175]\n" \
"v_mfma_f32_16x16x32_bf16 v[172:175], a[92:95], a[28:31], v[172:175]\n" \
"v_mfma_f32_16x16x32_bf16 v[168:171], a[80:83], a[24:27], v[168:171]\n" \
"v_mfma_f32_16x16x32_bf16 v[168:171], a[84:87], a[28:31], v[168:171]\n" \
"buffer_store_dwordx4 a[196:199], v234, s[56:59], s93 offen offset:1024 sc1\n" \
"v_mfma_f32_16x16x32_bf16 v[164:167], a[72:75], a[24:27], v[164:167]\n" \
"v_mfma_f32_16x16x32_bf16 v[164:167], a[76:79], a[28:31], v[164:167]\n" \
"v_mfma_f32_16x16x32_bf16 v[160:163], a[64:67], a[24:27], v[160:163]\n" \
"v_mfma_f32_16x16x32_bf16 v[160:163], a[68:71], a[28:31], v[160:163]\n" \
"s_waitcnt vmcnt(37) lgkmcnt(0)\n" \
"s_barrier\n" \
"v_mfma_f32_16x16x32_bf16 v[80:83], a[96:99], a[0:3], v[80:83]\n" \
"ds_read_b64_tr_b16 a[64:65], v232 offset:0\n" \
"v_mfma_f32_16x16x32_bf16 v[80:83], a[100:103], a[4:7], v[80:83]\n" \
"ds_read_b64_tr_b16 a[66:67], v232 offset:2048\n" \
"v_mfma_f32_16x16x32_bf16 v[84:87], a[104:107], a[0:3], v[84:87]\n" \
"s_add_u32 m0, s79, 49152\n" \
"ds_read_b64_tr_b16 a[68:69], v232 offset:8192\n" \
"v_mfma_f32_16x16x32_bf16 v[84:87], a[108:111], a[4:7], v[84:87]\n" \
"buffer_load_dwordx4 v228, s[52:55], s78 offen offset:0 lds\n" \
"v_mfma_f32_16x16x32_bf16 v[88:91], a[112:115], a[0:3], v[88:91]\n" \
"s_mul_i32 s73, s62, 557824\n" \
"s_add_u32 s85, s73, 17850368\n" \
"ds_read_b64_tr_b16 a[70:71], v232 offset:10240\n" \
"v_mfma_f32_16x16x32_bf16 v[88:91], a[116:119], a[4:7], v[88:91]\n" \
"s_mul_i32 s83, s63, 34816\n" \
"s_sub_u32 s86, 557056, s83\n" \
"ds_read_b64_tr_b16 a[72:73], v233 offset:0\n" \
"v_mfma_f32_16x16x32_bf16 v[92:95], a[120:123], a[0:3], v[92:95]\n" \
"ds_read_b64_tr_b16 a[74:75], v233 offset:2048\n" \
"v_add_u32 v237, %3, v236\n" \
"v_mfma_f32_16x16x32_bf16 v[92:95], a[124:127], a[4:7], v[92:95]\n" \
"buffer_load_dwordx4 v229, s[52:55], s78 offen offset:1024 lds\n" \
"v_mfma_f32_16x16x32_bf16 v[124:127], a[120:123], a[8:11], v[124:127]\n" \
"s_waitcnt vmcnt(37)\n" \
"ds_read_b128 a[0:3], v250 offset:0\n" \
"v_mfma_f32_16x16x32_bf16 v[124:127], a[124:127], a[12:15], v[124:127]\n" \
"s_add_u32 s36, s56, s73\n" \
"s_addc_u32 s37, s57, 0\n" \
"ds_read_b128 a[4:7], v251 offset:0\n" \
"v_mfma_f32_16x16x32_bf16 v[120:123], a[112:115], a[8:11], v[120:123]\n" \
"s_mov_b32 s38, s86\n" \
"s_mov_b32 s39, 0x20000\n" \
"ds_read_b64_tr_b16 a[76:77], v233 offset:8192\n" \
"v_mfma_f32_16x16x32_bf16 v[120:123], a[116:119], a[12:15], v[120:123]\n" \
"buffer_load_dwordx4 v228, s[52:55], s89 offen offset:2048 lds\n" \
"v_mfma_f32_16x16x32_bf16 v[116:119], a[104:107], a[8:11], v[116:119]\n" \
"ds_read_b64_tr_b16 a[78:79], v233 offset:10240\n" \
"v_add_u32 v235, %3, v234\n" \
"v_mfma_f32_16x16x32_bf16 v[116:119], a[108:111], a[12:15], v[116:119]\n" \
"s_add_u32 s40, s56, s85\n" \
"s_addc_u32 s41, s57, 0\n" \
"ds_read_b64_tr_b16 a[80:81], v232 offset:512\n" \
"v_mfma_f32_16x16x32_bf16 v[112:115], a[96:99], a[8:11], v[112:115]\n" \
"s_mov_b32 s42, s86\n" \
"s_mov_b32 s43, 0x20000\n" \
"ds_read_b64_tr_b16 a[82:83], v232 offset:2560\n" \
"v_mfma_f32_16x16x32_bf16 v[112:115], a[100:103], a[12:15], v[112:115]\n" \
"buffer_load_dwordx4 v229, s[52:55], s89 offen offset:3072 lds\n" \
"v_mfma_f32_16x16x32_bf16 v[144:147], a[96:99], a[16:19], v[144:147]\n" \
"s_waitcnt vmcnt(37)\n" \
"s_add_u32 s85, s74, 131072\n" \
"ds_read_b128 a[8:11], v250 offset:2048\n" \
"v_mfma_f32_16x16x32_bf16 v[144:147], a[100:103], a[20:23], v[144:147]\n" \
"s_add_u32 m0, s74, 131072\n" \
"ds_read_b128 a[12:15], v251 offset:2048\n" \
"v_add_u32 v239, s85, v234\n" \
"v_mfma_f32_16x16x32_bf16 v[148:151], a[104:107], a[16:19], v[148:151]\n" \
"s_waitcnt lgkmcnt(6)\n" \
"s_lshl_b32 s73, s61, 12\n" \
"ds_read_b64_tr_b16 a[84:85], v232 offset:8704\n" \
"v_mfma_f32_16x16x32_bf16 v[148:151], a[108:111], a[20:23], v[148:151]\n" \
"buffer_load_dwordx4 v224, s[48:51], s75 offen offset:0 lds\n" \
"v_mfma_f32_16x16x32_bf16 v[152:155], a[112:115], a[16:19], v[152:155]\n" \
"s_add_u32 s80, s79, 0x10000\n" \
"s_add_u32 s83, s83, s73\n" \
"ds_read_b64_tr_b16 a[86:87], v232 offset:10752\n" \
"v_mfma_f32_16x16x32_bf16 v[152:155], a[116:119], a[20:23], v[152:155]\n" \
"s_cmp_eq_u32 s63, 0\n" \
"s_cselect_b32 s85, 34816, 0\n" \
"ds_read_b64_tr_b16 a[88:89], v233 offset:512\n" \
"v_mfma_f32_16x16x32_bf16 v[156:159], a[120:123], a[16:19], v[156:159]\n" \
"s_add_u32 s73, s73, s85\n" \
"ds_read_b64_tr_b16 a[90:91], v233 offset:2560\n" \
"v_mfma_f32_16x16x32_bf16 v[156:159], a[124:127], a[20:23], v[156:159]\n" \
"buffer_load_dwordx4 v225, s[48:51], s75 offen offset:1024 lds\n" \
"v_mfma_f32_16x16x32_bf16 v[188:191], a[120:123], a[24:27], v[188:191]\n" \
"s_waitcnt vmcnt(37)\n" \
"ds_read_b128 a[16:19], v250 offset:4096\n" \
"v_mfma_f32_16x16x32_bf16 v[188:191], a[124:127], a[28:31], v[188:191]\n" \
"ds_read_b128 a[20:23], v251 offset:4096\n" \
"v_mfma_f32_16x16x32_bf16 v[184:187], a[112:115], a[24:27], v[184:187]\n" \
"s_waitcnt lgkmcnt(6)\n" \
"ds_read_b64_tr_b16 a[92:93], v233 offset:8704\n" \
"v_mfma_f32_16x16x32_bf16 v[184:187], a[116:119], a[28:31], v[184:187]\n" \
"buffer_load_dwordx4 v226, s[48:51], s75 offen offset:2048 lds\n" \
"v_mfma_f32_16x16x32_bf16 v[180:183], a[104:107], a[24:27], v[180:183]\n" \
"ds_read_b64_tr_b16 a[94:95], v233 offset:10752\n" \
"v_mfma_f32_16x16x32_bf16 v[180:183], a[108:111], a[28:31], v[180:183]\n" \
"v_mfma_f32_16x16x32_bf16 v[176:179], a[96:99], a[24:27], v[176:179]\n" \
"v_mfma_f32_16x16x32_bf16 v[176:179], a[100:103], a[28:31], v[176:179]\n" \
"buffer_load_dwordx4 v227, s[48:51], s75 offen offset:3072 lds\n" \
"v_mfma_f32_16x16x32_bf16 v[64:67], a[64:67], a[0:3], v[64:67]\n" \
"s_waitcnt vmcnt(37)\n" \
"ds_read_b128 a[24:27], v250 offset:6144\n" \
"v_mfma_f32_16x16x32_bf16 v[64:67], a[68:71], a[4:7], v[64:67]\n" \
"s_add_u32 m0, s74, 135168\n" \
"ds_read_b128 a[28:31], v251 offset:6144\n" \
"v_mfma_f32_16x16x32_bf16 v[68:71], a[72:75], a[0:3], v[68:71]\n" \
"s_waitcnt lgkmcnt(2)\n" \
"ds_read_b64_tr_b16 a[96:97], v232 offset:1024\n" \
"v_mfma_f32_16x16x32_bf16 v[68:71], a[76:79], a[4:7], v[68:71]\n" \
"buffer_load_dwordx4 v224, s[48:51], s77 offen offset:0 lds\n" \
"v_mfma_f32_16x16x32_bf16 v[72:75], a[80:83], a[0:3], v[72:75]\n" \
"ds_read_b64_tr_b16 a[98:99], v232 offset:3072\n" \
"v_mfma_f32_16x16x32_bf16 v[72:75], a[84:87], a[4:7], v[72:75]\n" \
"ds_read_b64_tr_b16 a[100:101], v232 offset:9216\n" \
"v_mfma_f32_16x16x32_bf16 v[76:79], a[88:91], a[0:3], v[76:79]\n" \
"ds_read_b64_tr_b16 a[102:103], v232 offset:11264\n" \
"v_mfma_f32_16x16x32_bf16 v[76:79], a[92:95], a[4:7], v[76:79]\n" \
"buffer_load_dwordx4 v225, s[48:51], s77 offen offset:1024 lds\n" \
"v_mfma_f32_16x16x32_bf16 v[108:111], a[88:91], a[8:11], v[108:111]\n" \
"ds_read_b64_tr_b16 a[104:105], v233 offset:1024\n" \
"v_mfma_f32_16x16x32_bf16 v[108:111], a[92:95], a[12:15], v[108:111]\n" \
"ds_read_b64_tr_b16 a[106:107], v233 offset:3072\n" \
"v_mfma_f32_16x16x32_bf16 v[104:107], a[80:83], a[8:11], v[104:107]\n" \
"s_waitcnt lgkmcnt(6)\n" \
"ds_read_b64_tr_b16 a[108:109], v233 offset:9216\n" \
"v_mfma_f32_16x16x32_bf16 v[104:107], a[84:87], a[12:15], v[104:107]\n" \
"buffer_load_dwordx4 v226, s[48:51], s77 offen offset:2048 lds\n" \
"v_mfma_f32_16x16x32_bf16 v[100:103], a[72:75], a[8:11], v[100:103]\n" \
"ds_read_b64_tr_b16 a[110:111], v233 offset:11264\n" \
"v_mfma_f32_16x16x32_bf16 v[100:103], a[76:79], a[12:15], v[100:103]\n" \
"ds_read_b64_tr_b16 a[112:113], v232 offset:1536\n" \
"v_mfma_f32_16x16x32_bf16 v[96:99], a[64:67], a[8:11], v[96:99]\n" \
"ds_read_b64_tr_b16 a[114:115], v232 offset:3584\n" \
"v_mfma_f32_16x16x32_bf16 v[96:99], a[68:71], a[12:15], v[96:99]\n" \
"buffer_load_dwordx4 v227, s[48:51], s77 offen offset:3072 lds\n" \
"v_mfma_f32_16x16x32_bf16 v[128:131], a[64:67], a[16:19], v[128:131]\n" \
"ds_read_b64_tr_b16 a[116:117], v232 offset:9728\n" \
"v_mfma_f32_16x16x32_bf16 v[128:131], a[68:71], a[20:23], v[128:131]\n" \
"ds_read_b64_tr_b16 a[118:119], v232 offset:11776\n" \
"v_mfma_f32_16x16x32_bf16 v[132:135], a[72:75], a[16:19], v[132:135]\n" \
"ds_read_b64_tr_b16 a[120:121], v233 offset:1536\n" \
"v_mfma_f32_16x16x32_bf16 v[132:135], a[76:79], a[20:23], v[132:135]\n" \
"buffer_store_dwordx4 a[200:203], v234, s[56:59], s93 offen offset:2048 sc1\n" \
"v_mfma_f32_16x16x32_bf16 v[136:139], a[80:83], a[16:19], v[136:139]\n" \
"ds_read_b64_tr_b16 a[122:123], v233 offset:3584\n" \
"v_mfma_f32_16x16x32_bf16 v[136:139], a[84:87], a[20:23], v[136:139]\n" \
"ds_read_b64_tr_b16 a[124:125], v233 offset:9728\n" \
"v_mfma_f32_16x16x32_bf16 v[140:143], a[88:91], a[16:19], v[140:143]\n" \
"ds_read_b64_tr_b16 a[126:127], v233 offset:11776\n" \
"v_mfma_f32_16x16x32_bf16 v[140:143], a[92:95], a[20:23], v[140:143]\n" \
"v_mfma_f32_16x16x32_bf16 v[172:175], a[88:91], a[24:27], v[172:175]\n" \
"v_mfma_f32_16x16x32_bf16 v[172:175], a[92:95], a[28:31], v[172:175]\n" \
"v_mfma_f32_16x16x32_bf16 v[168:171], a[80:83], a[24:27], v[168:171]\n" \
"v_mfma_f32_16x16x32_bf16 v[168:171], a[84:87], a[28:31], v[168:171]\n" \
"buffer_store_dwordx4 a[204:207], v234, s[56:59], s93 offen offset:3072 sc1\n" \
"v_mfma_f32_16x16x32_bf16 v[164:167], a[72:75], a[24:27], v[164:167]\n" \
"v_mfma_f32_16x16x32_bf16 v[164:167], a[76:79], a[28:31], v[164:167]\n" \
"v_mfma_f32_16x16x32_bf16 v[160:163], a[64:67], a[24:27], v[160:163]\n" \
"v_mfma_f32_16x16x32_bf16 v[160:163], a[68:71], a[28:31], v[160:163]\n" \
"s_waitcnt vmcnt(38) lgkmcnt(0)\n" \
"s_barrier\n" \
"v_mfma_f32_16x16x32_bf16 v[80:83], a[96:99], a[0:3], v[80:83]\n" \
"s_lshr_b32 s71, s61, 1\n" \
"s_lshl_b32 s71, s71, 14\n" \
"ds_read_b64_tr_b16 a[64:65], v232 offset:16384\n" \
"v_mfma_f32_16x16x32_bf16 v[80:83], a[100:103], a[4:7], v[80:83]\n" \
"s_xor_b32 s71, s71, 0x4000\n" \
"s_and_b32 s70, s61, 1\n" \
"ds_read_b64_tr_b16 a[66:67], v232 offset:18432\n" \
"v_mfma_f32_16x16x32_bf16 v[84:87], a[104:107], a[0:3], v[84:87]\n" \
"s_lshl_b32 s70, s70, 12\n" \
"s_add_u32 s71, s71, s70\n" \
"ds_read_b64_tr_b16 a[68:69], v232 offset:24576\n" \
"v_mfma_f32_16x16x32_bf16 v[84:87], a[108:111], a[4:7], v[84:87]\n" \
"v_add_u32 v224, s71, v237\n" \
"s_and_b32 s70, s63, 1\n" \
"v_mfma_f32_16x16x32_bf16 v[88:91], a[112:115], a[0:3], v[88:91]\n" \
"s_lshl_b32 s70, s70, 15\n" \
"s_xor_b32 s70, s70, 0x18000\n" \
"ds_read_b64_tr_b16 a[70:71], v232 offset:26624\n" \
"v_mfma_f32_16x16x32_bf16 v[88:91], a[116:119], a[4:7], v[88:91]\n" \
"s_add_u32 s71, s71, s70\n" \
"ds_read_b64_tr_b16 a[72:73], v233 offset:16384\n" \
"v_add_u32 v225, s71, v235\n" \
"v_mfma_f32_16x16x32_bf16 v[92:95], a[120:123], a[0:3], v[92:95]\n" \
"s_lshr_b32 s64, s22, 7\n" \
"s_sub_u32 s64, 3, s64\n" \
"ds_read_b64_tr_b16 a[74:75], v233 offset:18432\n" \
"v_mfma_f32_16x16x32_bf16 v[92:95], a[124:127], a[4:7], v[92:95]\n" \
"s_and_b32 s64, s64, 7\n" \
"s_and_b32 s65, %1, 1\n" \
"s_lshl_b32 s65, s65, 3\n" \
"v_mfma_f32_16x16x32_bf16 v[124:127], a[120:123], a[8:11], v[124:127]\n" \
"s_waitcnt vmcnt(36)\n" \
"s_add_u32 s64, s64, s65\n" \
"ds_read_b128 a[0:3], v230 offset:0\n" \
"v_mfma_f32_16x16x32_bf16 v[124:127], a[124:127], a[12:15], v[124:127]\n" \
"s_lshl_b32 s64, s64, 7\n" \
"s_lshr_b32 s65, %1, 3\n" \
"ds_read_b128 a[4:7], v231 offset:0\n" \
"v_mfma_f32_16x16x32_bf16 v[120:123], a[112:115], a[8:11], v[120:123]\n" \
"s_lshl_b32 s65, s65, 2\n" \
"s_add_u32 s65, s65, s61\n" \
"ds_read_b64_tr_b16 a[76:77], v233 offset:24576\n" \
"v_mfma_f32_16x16x32_bf16 v[120:123], a[116:119], a[12:15], v[120:123]\n" \
"s_mul_i32 s65, s65, 24576\n" \
"s_add_u32 s64, s64, s65\n" \
"v_bfe_u32 v254, v234, 7, 2\n" \
"v_mfma_f32_16x16x32_bf16 v[116:119], a[104:107], a[8:11], v[116:119]\n" \
"ds_read_b64_tr_b16 a[78:79], v233 offset:26624\n" \
"v_mul_u32_u24 v254, 3, v254\n" \
"v_mfma_f32_16x16x32_bf16 v[116:119], a[108:111], a[12:15], v[116:119]\n" \
"ds_read_b64_tr_b16 a[80:81], v232 offset:16896\n" \
"v_bfe_u32 v243, v234, 5, 2\n" \
"v_mfma_f32_16x16x32_bf16 v[112:115], a[96:99], a[8:11], v[112:115]\n" \
"s_add_u32 s93, s94, 8192\n" \
"ds_read_b64_tr_b16 a[82:83], v232 offset:18944\n" \
"v_min_u32 v243, 2, v243\n" \
"v_mfma_f32_16x16x32_bf16 v[112:115], a[100:103], a[12:15], v[112:115]\n" \
"buffer_store_dwordx4 v[32:35], v236, s[56:59], s93 offen offset:0 sc1\n" \
"v_mfma_f32_16x16x32_bf16 v[144:147], a[96:99], a[16:19], v[144:147]\n" \
"s_waitcnt vmcnt(35)\n" \
"ds_read_b128 a[8:11], v230 offset:2048\n" \
"v_add_u32 v254, v254, v243\n" \
"v_mfma_f32_16x16x32_bf16 v[144:147], a[100:103], a[20:23], v[144:147]\n" \
"ds_read_b128 a[12:15], v231 offset:2048\n" \
"v_and_b32 v243, 16, v234\n" \
"v_mfma_f32_16x16x32_bf16 v[148:151], a[104:107], a[16:19], v[148:151]\n" \
"ds_read_b64_tr_b16 a[84:85], v232 offset:25088\n" \
"v_lshl_add_u32 v243, v243, 2, s64\n" \
"v_mfma_f32_16x16x32_bf16 v[148:151], a[108:111], a[20:23], v[148:151]\n" \
"v_lshl_add_u32 v254, v254, 11, v243\n" \
"v_mfma_f32_16x16x32_bf16 v[152:155], a[112:115], a[16:19], v[152:155]\n" \
"ds_read_b64_tr_b16 a[86:87], v232 offset:27136\n" \
"v_mfma_f32_16x16x32_bf16 v[152:155], a[116:119], a[20:23], v[152:155]\n" \
"ds_read_b64_tr_b16 a[88:89], v233 offset:16896\n" \
"v_mfma_f32_16x16x32_bf16 v[156:159], a[120:123], a[16:19], v[156:159]\n" \
"ds_read_b64_tr_b16 a[90:91], v233 offset:18944\n" \
"v_mfma_f32_16x16x32_bf16 v[156:159], a[124:127], a[20:23], v[156:159]\n" \
"v_mfma_f32_16x16x32_bf16 v[188:191], a[120:123], a[24:27], v[188:191]\n" \
"s_waitcnt vmcnt(33)\n" \
"ds_read_b128 a[16:19], v230 offset:4096\n" \
"v_mfma_f32_16x16x32_bf16 v[188:191], a[124:127], a[28:31], v[188:191]\n" \
"ds_read_b128 a[20:23], v231 offset:4096\n" \
"v_mfma_f32_16x16x32_bf16 v[184:187], a[112:115], a[24:27], v[184:187]\n" \
"ds_read_b64_tr_b16 a[92:93], v233 offset:25088\n" \
"v_mfma_f32_16x16x32_bf16 v[184:187], a[116:119], a[28:31], v[184:187]\n" \
"v_mfma_f32_16x16x32_bf16 v[180:183], a[104:107], a[24:27], v[180:183]\n" \
"ds_read_b64_tr_b16 a[94:95], v233 offset:27136\n" \
"v_mfma_f32_16x16x32_bf16 v[180:183], a[108:111], a[28:31], v[180:183]\n" \
"v_mfma_f32_16x16x32_bf16 v[176:179], a[96:99], a[24:27], v[176:179]\n" \
"buffer_store_dwordx4 v[48:51], v236, s[56:59], s93 offen offset:256 sc1\n" \
"v_mfma_f32_16x16x32_bf16 v[176:179], a[100:103], a[28:31], v[176:179]\n" \
"s_waitcnt lgkmcnt(1)\n" \
"v_mfma_f32_16x16x32_bf16 v[64:67], a[64:67], a[0:3], v[64:67]\n" \
"s_waitcnt vmcnt(32)\n" \
"s_lshl_b32 s68, s63, 2\n" \
"ds_read_b128 a[24:27], v230 offset:6144\n" \
"v_mfma_f32_16x16x32_bf16 v[64:67], a[68:71], a[4:7], v[64:67]\n" \
"s_add_u32 s68, s68, s90\n" \
"ds_read_b128 a[28:31], v231 offset:6144\n" \
"v_mfma_f32_16x16x32_bf16 v[68:71], a[72:75], a[0:3], v[68:71]\n" \
"ds_read_b64_tr_b16 a[96:97], v232 offset:17408\n" \
"v_mfma_f32_16x16x32_bf16 v[68:71], a[76:79], a[4:7], v[68:71]\n" \
"Lpg4445_%=:\n" \
"s_cbranch_execz Lpg6174_%=\n" \
"v_mov_b32 v228, s60\n" \
"v_lshlrev_b32 v229, 28, s61\n" \
"v_mfma_f32_16x16x32_bf16 v[72:75], a[80:83], a[0:3], v[72:75]\n" \
"ds_read_b64_tr_b16 a[98:99], v232 offset:19456\n" \
"v_mfma_f32_16x16x32_bf16 v[72:75], a[84:87], a[4:7], v[72:75]\n" \
"s_waitcnt lgkmcnt(3)\n" \
"ds_read_b64_tr_b16 a[100:101], v232 offset:25600\n" \
"v_mfma_f32_16x16x32_bf16 v[76:79], a[88:91], a[0:3], v[76:79]\n" \
"ds_read_b64_tr_b16 a[102:103], v232 offset:27648\n" \
"v_mfma_f32_16x16x32_bf16 v[76:79], a[92:95], a[4:7], v[76:79]\n" \
"v_mfma_f32_16x16x32_bf16 v[108:111], a[88:91], a[8:11], v[108:111]\n" \
"ds_read_b64_tr_b16 a[104:105], v233 offset:17408\n" \
"v_mfma_f32_16x16x32_bf16 v[108:111], a[92:95], a[12:15], v[108:111]\n" \
"ds_read_b64_tr_b16 a[106:107], v233 offset:19456\n" \
"v_mfma_f32_16x16x32_bf16 v[104:107], a[80:83], a[8:11], v[104:107]\n" \
"ds_read_b64_tr_b16 a[108:109], v233 offset:25600\n" \
"v_mfma_f32_16x16x32_bf16 v[104:107], a[84:87], a[12:15], v[104:107]\n" \
"v_mfma_f32_16x16x32_bf16 v[100:103], a[72:75], a[8:11], v[100:103]\n" \
"ds_read_b64_tr_b16 a[110:111], v233 offset:27648\n" \
"v_mfma_f32_16x16x32_bf16 v[100:103], a[76:79], a[12:15], v[100:103]\n" \
"ds_read_b64_tr_b16 a[112:113], v232 offset:17920\n" \
"v_mfma_f32_16x16x32_bf16 v[96:99], a[64:67], a[8:11], v[96:99]\n" \
"ds_read_b64_tr_b16 a[114:115], v232 offset:19968\n" \
"v_mfma_f32_16x16x32_bf16 v[96:99], a[68:71], a[12:15], v[96:99]\n" \
"v_mfma_f32_16x16x32_bf16 v[128:131], a[64:67], a[16:19], v[128:131]\n" \
"ds_read_b64_tr_b16 a[116:117], v232 offset:26112\n" \
"v_mfma_f32_16x16x32_bf16 v[128:131], a[68:71], a[20:23], v[128:131]\n" \
"ds_read_b64_tr_b16 a[118:119], v232 offset:28160\n" \
"v_mfma_f32_16x16x32_bf16 v[132:135], a[72:75], a[16:19], v[132:135]\n" \
"ds_read_b64_tr_b16 a[120:121], v233 offset:17920\n" \
"v_mfma_f32_16x16x32_bf16 v[132:135], a[76:79], a[20:23], v[132:135]\n" \
"buffer_store_dwordx4 v[40:43], v236, s[56:59], s93 offen offset:2048 sc1\n" \
"v_mfma_f32_16x16x32_bf16 v[136:139], a[80:83], a[16:19], v[136:139]\n" \
"ds_read_b64_tr_b16 a[122:123], v233 offset:19968\n" \
"v_mfma_f32_16x16x32_bf16 v[136:139], a[84:87], a[20:23], v[136:139]\n" \
"ds_read_b64_tr_b16 a[124:125], v233 offset:26112\n" \
"v_mfma_f32_16x16x32_bf16 v[140:143], a[88:91], a[16:19], v[140:143]\n" \
"ds_read_b64_tr_b16 a[126:127], v233 offset:28160\n" \
"v_mfma_f32_16x16x32_bf16 v[140:143], a[92:95], a[20:23], v[140:143]\n" \
"s_waitcnt lgkmcnt(3)\n" \
"v_mfma_f32_16x16x32_bf16 v[172:175], a[88:91], a[24:27], v[172:175]\n" \
"v_mfma_f32_16x16x32_bf16 v[172:175], a[92:95], a[28:31], v[172:175]\n" \
"v_mfma_f32_16x16x32_bf16 v[168:171], a[80:83], a[24:27], v[168:171]\n" \
"v_mfma_f32_16x16x32_bf16 v[168:171], a[84:87], a[28:31], v[168:171]\n" \
"v_mfma_f32_16x16x32_bf16 v[164:167], a[72:75], a[24:27], v[164:167]\n" \
"v_mfma_f32_16x16x32_bf16 v[164:167], a[76:79], a[28:31], v[164:167]\n" \
"v_mfma_f32_16x16x32_bf16 v[160:163], a[64:67], a[24:27], v[160:163]\n" \
"v_mfma_f32_16x16x32_bf16 v[160:163], a[68:71], a[28:31], v[160:163]\n" \
"s_waitcnt vmcnt(3) lgkmcnt(0)\n" \
"s_barrier\n" \
"v_mfma_f32_16x16x32_bf16 v[80:83], a[96:99], a[0:3], v[80:83]\n" \
"ds_read_b64_tr_b16 a[64:65], v232 offset:32768\n" \
"v_mfma_f32_16x16x32_bf16 v[80:83], a[100:103], a[4:7], v[80:83]\n" \
"ds_read_b64_tr_b16 a[66:67], v232 offset:34816\n" \
"buffer_store_dword v228, v229, s[56:59], s68 offen sc1\n" \
"v_mfma_f32_16x16x32_bf16 v[84:87], a[104:107], a[0:3], v[84:87]\n" \
"ds_read_b64_tr_b16 a[68:69], v232 offset:40960\n" \
"v_lshrrev_b32 v238, 4, v234\n" \
"v_mfma_f32_16x16x32_bf16 v[84:87], a[108:111], a[4:7], v[84:87]\n" \
"buffer_store_dwordx4 v[56:59], v236, s[56:59], s93 offen offset:2304 sc1\n" \
"v_mfma_f32_16x16x32_bf16 v[88:91], a[112:115], a[0:3], v[88:91]\n" \
"ds_read_b64_tr_b16 a[70:71], v232 offset:43008\n" \
"v_lshrrev_b32 v240, 4, v238\n" \
"v_mfma_f32_16x16x32_bf16 v[88:91], a[116:119], a[4:7], v[88:91]\n" \
"ds_read_b64_tr_b16 a[72:73], v233 offset:32768\n" \
"v_lshlrev_b32 v240, 2, v240\n" \
"v_mfma_f32_16x16x32_bf16 v[92:95], a[120:123], a[0:3], v[92:95]\n" \
"ds_read_b64_tr_b16 a[74:75], v233 offset:34816\n" \
"v_and_b32 v238, 15, v238\n" \
"v_mfma_f32_16x16x32_bf16 v[92:95], a[124:127], a[4:7], v[92:95]\n" \
"ds_write_b128 v224, v[192:195] offset:8192\n" \
"v_mfma_f32_16x16x32_bf16 v[124:127], a[120:123], a[8:11], v[124:127]\n" \
"ds_read_b128 a[0:3], v230 offset:32768\n" \
"v_sub_u32 v238, v238, v240\n" \
"v_mfma_f32_16x16x32_bf16 v[124:127], a[124:127], a[12:15], v[124:127]\n" \
"ds_read_b128 a[4:7], v231 offset:32768\n" \
"v_subrev_u32 v248, 0, v238\n" \
"v_mfma_f32_16x16x32_bf16 v[120:123], a[112:115], a[8:11], v[120:123]\n" \
"ds_read_b64_tr_b16 a[76:77], v233 offset:40960\n" \
"v_subrev_u32 v249, 1, v238\n" \
"v_mfma_f32_16x16x32_bf16 v[120:123], a[116:119], a[12:15], v[120:123]\n" \
"ds_write_b128 v224, v[208:211] offset:8448\n" \
"v_mfma_f32_16x16x32_bf16 v[116:119], a[104:107], a[8:11], v[116:119]\n" \
"ds_read_b64_tr_b16 a[78:79], v233 offset:43008\n" \
"v_subrev_u32 v250, 2, v238\n" \
"v_mfma_f32_16x16x32_bf16 v[116:119], a[108:111], a[12:15], v[116:119]\n" \
"ds_read_b64_tr_b16 a[80:81], v232 offset:33280\n" \
"v_subrev_u32 v251, 3, v238\n" \
"v_mfma_f32_16x16x32_bf16 v[112:115], a[96:99], a[8:11], v[112:115]\n" \
"ds_read_b64_tr_b16 a[82:83], v232 offset:35328\n" \
"v_ashrrev_i32 v248, 31, v248\n" \
"v_mfma_f32_16x16x32_bf16 v[112:115], a[100:103], a[12:15], v[112:115]\n" \
"v_ashrrev_i32 v249, 31, v249\n" \
"v_ashrrev_i32 v250, 31, v250\n" \
"v_mfma_f32_16x16x32_bf16 v[144:147], a[96:99], a[16:19], v[144:147]\n" \
"ds_read_b128 a[8:11], v230 offset:34816\n" \
"v_ashrrev_i32 v251, 31, v251\n" \
"v_mfma_f32_16x16x32_bf16 v[144:147], a[100:103], a[20:23], v[144:147]\n" \
"ds_read_b128 a[12:15], v231 offset:34816\n" \
"v_lshlrev_b32 v248, 23, v248\n" \
"v_mfma_f32_16x16x32_bf16 v[148:151], a[104:107], a[16:19], v[148:151]\n" \
"ds_read_b64_tr_b16 a[84:85], v232 offset:41472\n" \
"v_lshlrev_b32 v249, 23, v249\n" \
"v_mfma_f32_16x16x32_bf16 v[148:151], a[108:111], a[20:23], v[148:151]\n" \
"ds_write_b128 v224, v[200:203] offset:10240\n" \
"v_mfma_f32_16x16x32_bf16 v[152:155], a[112:115], a[16:19], v[152:155]\n" \
"ds_read_b64_tr_b16 a[86:87], v232 offset:43520\n" \
"v_lshlrev_b32 v250, 23, v250\n" \
"v_mfma_f32_16x16x32_bf16 v[152:155], a[116:119], a[20:23], v[152:155]\n" \
"ds_read_b64_tr_b16 a[88:89], v233 offset:33280\n" \
"v_lshlrev_b32 v251, 23, v251\n" \
"v_mfma_f32_16x16x32_bf16 v[156:159], a[120:123], a[16:19], v[156:159]\n" \
"s_add_u32 s93, s94, 0\n" \
"ds_read_b64_tr_b16 a[90:91], v233 offset:35328\n" \
"v_mov_b32 v240, 0xff800000\n" \
"v_mfma_f32_16x16x32_bf16 v[156:159], a[124:127], a[20:23], v[156:159]\n" \
"buffer_store_dwordx4 v[36:39], v236, s[56:59], s93 offen offset:0 sc1\n" \
"v_mfma_f32_16x16x32_bf16 v[188:191], a[120:123], a[24:27], v[188:191]\n" \
"ds_read_b128 a[16:19], v230 offset:36864\n" \
"v_add_f32 v248, 0x42400000, v248\n" \
"v_mfma_f32_16x16x32_bf16 v[188:191], a[124:127], a[28:31], v[188:191]\n" \
"ds_read_b128 a[20:23], v231 offset:36864\n" \
"v_add_f32 v249, 0x42400000, v249\n" \
"v_mfma_f32_16x16x32_bf16 v[184:187], a[112:115], a[24:27], v[184:187]\n" \
"ds_read_b64_tr_b16 a[92:93], v233 offset:41472\n" \
"v_add_f32 v250, 0x42400000, v250\n" \
"v_mfma_f32_16x16x32_bf16 v[184:187], a[116:119], a[28:31], v[184:187]\n" \
"ds_write_b128 v224, v[216:219] offset:10496\n" \
"v_mfma_f32_16x16x32_bf16 v[180:183], a[104:107], a[24:27], v[180:183]\n" \
"ds_read_b64_tr_b16 a[94:95], v233 offset:43520\n" \
"v_add_f32 v251, 0x42400000, v251\n" \
"v_mfma_f32_16x16x32_bf16 v[180:183], a[108:111], a[28:31], v[180:183]\n" \
"s_mov_b32 s72, 0x42400000\n" \
"s_cmp_gt_u32 s61, 0\n" \
"s_cselect_b32 s70, s72, 0xff800000\n" \
"v_mfma_f32_16x16x32_bf16 v[176:179], a[96:99], a[24:27], v[176:179]\n" \
"ds_write_b128 v224, v[196:199] offset:0\n" \
"v_mfma_f32_16x16x32_bf16 v[176:179], a[100:103], a[28:31], v[176:179]\n" \
"s_waitcnt lgkmcnt(3)\n" \
"v_mov_b32 v252, s70\n" \
"v_mfma_f32_16x16x32_bf16 v[64:67], a[64:67], a[0:3], v[64:67]\n" \
"ds_read_b128 a[24:27], v230 offset:38912\n" \
"v_mfma_f32_16x16x32_bf16 v[64:67], a[68:71], a[4:7], v[64:67]\n" \
"s_cmp_gt_u32 s61, 1\n" \
"s_cselect_b32 s70, s72, 0xff800000\n" \
"ds_read_b128 a[28:31], v231 offset:38912\n" \
"v_mfma_f32_16x16x32_bf16 v[68:71], a[72:75], a[0:3], v[68:71]\n" \
"ds_read_b64_tr_b16 a[96:97], v232 offset:33792\n" \
"v_mov_b32 v253, s70\n" \
"v_mfma_f32_16x16x32_bf16 v[68:71], a[76:79], a[4:7], v[68:71]\n" \
"s_cmp_gt_u32 s61, 2\n" \
"s_cselect_b32 s70, s72, 0xff800000\n" \
"v_mov_b32 v226, s70\n" \
"v_mfma_f32_16x16x32_bf16 v[72:75], a[80:83], a[0:3], v[72:75]\n" \
"ds_read_b64_tr_b16 a[98:99], v232 offset:35840\n" \
"v_bfe_i32 v243, v234, 4, 1\n" \
"v_mfma_f32_16x16x32_bf16 v[72:75], a[84:87], a[4:7], v[72:75]\n" \
"s_waitcnt lgkmcnt(3)\n" \
"ds_read_b64_tr_b16 a[100:101], v232 offset:41984\n" \
"v_and_b32 v243, 0xaaaa, v243\n" \
"v_mfma_f32_16x16x32_bf16 v[76:79], a[88:91], a[0:3], v[76:79]\n" \
"ds_read_b64_tr_b16 a[102:103], v232 offset:44032\n" \
"v_or_b32 v243, 0x4444, v243\n" \
"v_mfma_f32_16x16x32_bf16 v[76:79], a[92:95], a[4:7], v[76:79]\n" \
"ds_write_b128 v224, v[212:215] offset:256\n" \
"v_mfma_f32_16x16x32_bf16 v[108:111], a[88:91], a[8:11], v[108:111]\n" \
"ds_read_b64_tr_b16 a[104:105], v233 offset:33792\n" \
"v_bfe_i32 v244, v234, 5, 1\n" \
"v_mfma_f32_16x16x32_bf16 v[108:111], a[92:95], a[12:15], v[108:111]\n" \
"ds_read_b64_tr_b16 a[106:107], v233 offset:35840\n" \
"v_not_b32 v244, v244\n" \
"v_mfma_f32_16x16x32_bf16 v[104:107], a[80:83], a[8:11], v[104:107]\n" \
"ds_read_b64_tr_b16 a[108:109], v233 offset:41984\n" \
"v_and_b32 v244, 0x3f803f80, v244\n" \
"v_mfma_f32_16x16x32_bf16 v[104:107], a[84:87], a[12:15], v[104:107]\n" \
"buffer_store_dwordx4 v[16:19], v236, s[56:59], s93 offen offset:256 sc1\n" \
"v_mfma_f32_16x16x32_bf16 v[100:103], a[72:75], a[8:11], v[100:103]\n" \
"ds_read_b64_tr_b16 a[110:111], v233 offset:44032\n" \
"v_mov_b32 v245, v244\n" \
"v_mfma_f32_16x16x32_bf16 v[100:103], a[76:79], a[12:15], v[100:103]\n" \
"ds_read_b64_tr_b16 a[112:113], v232 offset:34304\n" \
"v_mov_b32 v246, v244\n" \
"v_mfma_f32_16x16x32_bf16 v[96:99], a[64:67], a[8:11], v[96:99]\n" \
"ds_read_b64_tr_b16 a[114:115], v232 offset:36352\n" \
"v_mov_b32 v247, v244\n" \
"v_mfma_f32_16x16x32_bf16 v[96:99], a[68:71], a[12:15], v[96:99]\n" \
"ds_write_b128 v224, v[204:207] offset:2048\n" \
"v_mfma_f32_16x16x32_bf16 v[128:131], a[64:67], a[16:19], v[128:131]\n" \
"s_mov_b32 s76, s73\n" \
"s_and_b32 s77, s63, 1\n" \
"ds_read_b64_tr_b16 a[116:117], v232 offset:42496\n" \
"v_mfma_f32_16x16x32_bf16 v[128:131], a[68:71], a[20:23], v[128:131]\n" \
"s_lshl_b32 s77, s77, 15\n" \
"s_xor_b32 s89, s77, 0x8000\n" \
"ds_read_b64_tr_b16 a[118:119], v232 offset:44544\n" \
"v_mfma_f32_16x16x32_bf16 v[132:135], a[72:75], a[16:19], v[132:135]\n" \
"s_add_u32 s82, s89, 0x10000\n" \
"s_mov_b32 s74, 0\n" \
"ds_read_b64_tr_b16 a[120:121], v233 offset:34304\n" \
"v_mfma_f32_16x16x32_bf16 v[132:135], a[76:79], a[20:23], v[132:135]\n" \
"ds_write_b128 v224, v[220:223] offset:2304\n" \
"v_mfma_f32_16x16x32_bf16 v[136:139], a[80:83], a[16:19], v[136:139]\n" \
"s_waitcnt lgkmcnt(4)\n" \
"s_add_u32 s73, s73, 34816\n" \
"ds_read_b64_tr_b16 a[122:123], v233 offset:36352\n" \
"v_mfma_f32_16x16x32_bf16 v[136:139], a[84:87], a[20:23], v[136:139]\n" \
"s_cmp_eq_u32 s73, s83\n" \
"s_cselect_b32 s70, 34816, 0\n" \
"ds_read_b64_tr_b16 a[124:125], v233 offset:42496\n" \
"v_mfma_f32_16x16x32_bf16 v[140:143], a[88:91], a[16:19], v[140:143]\n" \
"s_add_u32 s73, s73, s70\n" \
"s_lshr_b32 s70, s61, 1\n" \
"ds_read_b64_tr_b16 a[126:127], v233 offset:44544\n" \
"v_mfma_f32_16x16x32_bf16 v[140:143], a[92:95], a[20:23], v[140:143]\n" \
"s_lshl_b32 s85, s63, 2\n" \
"s_sub_u32 s86, 60, s85\n" \
"s_add_u32 s85, s85, s70\n" \
"v_mfma_f32_16x16x32_bf16 v[172:175], a[88:91], a[24:27], v[172:175]\n" \
"s_add_u32 s86, s86, s70\n" \
"s_mul_i32 s87, s85, 266240\n" \
"v_add_u32 v230, 0x10000, v230\n" \
"v_mfma_f32_16x16x32_bf16 v[172:175], a[92:95], a[28:31], v[172:175]\n" \
"s_mul_i32 s88, s86, 266240\n" \
"s_and_b32 s75, s61, 1\n" \
"v_add_u32 v231, 0x10000, v231\n" \
"v_mfma_f32_16x16x32_bf16 v[168:171], a[80:83], a[24:27], v[168:171]\n" \
"s_lshl_b32 s70, s75, 8\n" \
"s_lshl_b32 s91, s62, 13\n" \
"s_add_u32 s91, s91, s70\n" \
"v_mfma_f32_16x16x32_bf16 v[168:171], a[84:87], a[28:31], v[168:171]\n" \
"buffer_store_dwordx4 v[52:55], v236, s[56:59], s93 offen offset:2048 sc1\n" \
"v_mfma_f32_16x16x32_bf16 v[164:167], a[72:75], a[24:27], v[164:167]\n" \
"s_add_u32 s91, s91, 35700736\n" \
"s_add_u32 s87, s87, s91\n" \
"s_add_u32 s88, s88, s91\n" \
"v_mfma_f32_16x16x32_bf16 v[164:167], a[76:79], a[28:31], v[164:167]\n" \
"s_lshl_b32 s75, s75, 14\n" \
"s_cmp_eq_u32 s63, 7\n" \
"s_cselect_b32 s75, 0, s75\n" \
"s_xor_b32 s78, s75, 0xc000\n" \
"s_mov_b32 s81, s78\n" \
"s_cmp_eq_u32 s63, 0\n" \
"s_cselect_b32 s91, s75, 0\n" \
"s_add_u32 s91, s91, 0x8000\n" \
"s_xor_b32 s75, s75, 0x8000\n" \
"s_sub_u32 s84, 7, s63\n" \
"v_mfma_f32_16x16x32_bf16 v[160:163], a[64:67], a[24:27], v[160:163]\n" \
"v_mfma_f32_16x16x32_bf16 v[160:163], a[68:71], a[28:31], v[160:163]\n" \
"s_waitcnt vmcnt(18) lgkmcnt(0)\n" \
"s_barrier\n" \
"v_mfma_f32_16x16x32_bf16 v[144:147], a[96:99], a[16:19], v[144:147]\n" \
"s_mov_b64 s[44:45], s[10:11]\n" \
"s_mov_b32 s46, 0\n" \
"ds_read_b64_tr_b16 a[64:65], v232 offset:49152\n" \
"v_mfma_f32_16x16x32_bf16 v[144:147], a[100:103], a[20:23], v[144:147]\n" \
"s_mov_b32 s47, 0\n" \
"s_cmp_eq_u32 s63, 7\n" \
"s_cselect_b64 s[44:45], s[18:19], s[44:45]\n" \
"s_cselect_b32 s46, 5, s46\n" \
"s_cmp_eq_u32 s63, 3\n" \
"s_cselect_b64 s[44:45], s[18:19], s[44:45]\n" \
"s_cselect_b32 s46, 3, s46\n" \
"ds_read_b64_tr_b16 a[66:67], v232 offset:51200\n" \
"v_mfma_f32_16x16x32_bf16 v[148:151], a[104:107], a[16:19], v[148:151]\n" \
"s_cmp_eq_u32 s63, 3\n" \
"s_cselect_b32 s47, 640, s47\n" \
"ds_read_b64_tr_b16 a[68:69], v232 offset:57344\n" \
"v_mfma_f32_16x16x32_bf16 v[148:151], a[108:111], a[20:23], v[148:151]\n" \
"ds_write_b128 v225, a[192:195] offset:0\n" \
"v_mfma_f32_16x16x32_bf16 v[152:155], a[112:115], a[16:19], v[152:155]\n" \
"ds_read_b64_tr_b16 a[70:71], v232 offset:59392\n" \
"v_mfma_f32_16x16x32_bf16 v[152:155], a[116:119], a[20:23], v[152:155]\n" \
"s_cmp_eq_u32 s63, 6\n" \
"s_cselect_b64 s[44:45], s[22:23], s[44:45]\n" \
"s_cselect_b32 s46, 6, s46\n" \
"ds_read_b64_tr_b16 a[72:73], v233 offset:49152\n" \
"v_mfma_f32_16x16x32_bf16 v[156:159], a[120:123], a[16:19], v[156:159]\n" \
"ds_read_b64_tr_b16 a[74:75], v233 offset:51200\n" \
"v_mfma_f32_16x16x32_bf16 v[156:159], a[124:127], a[20:23], v[156:159]\n" \
"s_cmp_eq_u32 s63, 4\n" \
"s_cselect_b64 s[44:45], s[20:21], s[44:45]\n" \
"s_cselect_b32 s46, 4, s46\n" \
"v_mfma_f32_16x16x32_bf16 v[188:191], a[120:123], a[24:27], v[188:191]\n" \
"ds_read_b128 a[16:19], v230 offset:0\n" \
"v_mfma_f32_16x16x32_bf16 v[188:191], a[124:127], a[28:31], v[188:191]\n" \
"s_cmp_eq_u32 s63, 2\n" \
"s_cselect_b64 s[44:45], s[24:25], s[44:45]\n" \
"s_cselect_b32 s46, 2, s46\n" \
"ds_read_b128 a[20:23], v231 offset:0\n" \
"v_mfma_f32_16x16x32_bf16 v[184:187], a[112:115], a[24:27], v[184:187]\n" \
"s_lshl_b32 s48, s62, 2\n" \
"s_add_u32 s48, s48, s61\n" \
"ds_read_b64_tr_b16 a[76:77], v233 offset:57344\n" \
"v_mfma_f32_16x16x32_bf16 v[184:187], a[116:119], a[28:31], v[184:187]\n" \
"ds_write_b128 v225, a[196:199] offset:1024\n" \
"v_mfma_f32_16x16x32_bf16 v[180:183], a[104:107], a[24:27], v[180:183]\n" \
"s_add_u32 s48, s48, s47\n" \
"s_lshl_b32 s48, s48, 13\n" \
"ds_read_b64_tr_b16 a[78:79], v233 offset:59392\n" \
"v_mfma_f32_16x16x32_bf16 v[180:183], a[108:111], a[28:31], v[180:183]\n" \
"s_add_u32 s44, s44, s48\n" \
"s_addc_u32 s45, s45, 0\n" \
"ds_read_b64_tr_b16 a[80:81], v232 offset:49664\n" \
"v_mfma_f32_16x16x32_bf16 v[176:179], a[96:99], a[24:27], v[176:179]\n" \
"s_cmp_eq_u32 s46, 0\n" \
"s_cselect_b32 s48, 28, 3\n" \
"ds_read_b64_tr_b16 a[82:83], v232 offset:51712\n" \
"v_mfma_f32_16x16x32_bf16 v[176:179], a[100:103], a[28:31], v[176:179]\n" \
"buffer_store_dwordx4 v[24:27], v236, s[56:59], s93 offen offset:2304 sc1\n" \
"v_mfma_f32_16x16x32_bf16 v[80:83], a[96:99], a[0:3], v[80:83]\n" \
"ds_read_b128 a[24:27], v230 offset:2048\n" \
"v_mfma_f32_16x16x32_bf16 v[80:83], a[100:103], a[4:7], v[80:83]\n" \
"ds_read_b128 a[28:31], v231 offset:2048\n" \
"v_mfma_f32_16x16x32_bf16 v[84:87], a[104:107], a[0:3], v[84:87]\n" \
"ds_read_b64_tr_b16 a[84:85], v232 offset:57856\n" \
"v_mfma_f32_16x16x32_bf16 v[84:87], a[108:111], a[4:7], v[84:87]\n" \
"ds_write_b128 v225, a[200:203] offset:2048\n" \
"v_mfma_f32_16x16x32_bf16 v[88:91], a[112:115], a[0:3], v[88:91]\n" \
"ds_read_b64_tr_b16 a[86:87], v232 offset:59904\n" \
"v_mfma_f32_16x16x32_bf16 v[88:91], a[116:119], a[4:7], v[88:91]\n" \
"ds_read_b64_tr_b16 a[88:89], v233 offset:49664\n" \
"v_mfma_f32_16x16x32_bf16 v[92:95], a[120:123], a[0:3], v[92:95]\n" \
"ds_read_b64_tr_b16 a[90:91], v233 offset:51712\n" \
"v_mfma_f32_16x16x32_bf16 v[92:95], a[124:127], a[4:7], v[92:95]\n" \
"ds_write_b128 v225, a[204:207] offset:3072\n" \
"v_mfma_f32_16x16x32_bf16 v[124:127], a[120:123], a[8:11], v[124:127]\n" \
"ds_read_b128 a[0:3], v230 offset:4096\n" \
"v_mfma_f32_16x16x32_bf16 v[124:127], a[124:127], a[12:15], v[124:127]\n" \
"ds_read_b128 a[4:7], v231 offset:4096\n" \
"v_mfma_f32_16x16x32_bf16 v[120:123], a[112:115], a[8:11], v[120:123]\n" \
"ds_read_b64_tr_b16 a[92:93], v233 offset:57856\n" \
"v_mfma_f32_16x16x32_bf16 v[120:123], a[116:119], a[12:15], v[120:123]\n" \
"v_mfma_f32_16x16x32_bf16 v[116:119], a[104:107], a[8:11], v[116:119]\n" \
"ds_read_b64_tr_b16 a[94:95], v233 offset:59904\n" \
"v_mfma_f32_16x16x32_bf16 v[116:119], a[108:111], a[12:15], v[116:119]\n" \
"v_mfma_f32_16x16x32_bf16 v[112:115], a[96:99], a[8:11], v[112:115]\n" \
"ds_write_b128 v225, a[224:227] offset:8192\n" \
"v_mfma_f32_16x16x32_bf16 v[112:115], a[100:103], a[12:15], v[112:115]\n" \
"s_waitcnt lgkmcnt(2)\n" \
"v_mfma_f32_16x16x32_bf16 v[64:67], a[64:67], a[16:19], v[64:67]\n" \
"ds_read_b128 a[8:11], v230 offset:6144\n" \
"ds_read_b128 a[12:15], v231 offset:6144\n" \
"v_mfma_f32_16x16x32_bf16 v[64:67], a[68:71], a[20:23], v[64:67]\n" \
"ds_read_b64_tr_b16 a[96:97], v232 offset:50176\n" \
"ds_read_b64_tr_b16 a[98:99], v232 offset:52224\n" \
"v_mfma_f32_16x16x32_bf16 v[68:71], a[72:75], a[16:19], v[68:71]\n" \
"s_waitcnt lgkmcnt(5)\n" \
"ds_read_b64_tr_b16 a[100:101], v232 offset:58368\n" \
"ds_read_b64_tr_b16 a[102:103], v232 offset:60416\n" \
"v_mfma_f32_16x16x32_bf16 v[68:71], a[76:79], a[20:23], v[68:71]\n" \
"s_getpc_b64 s[64:65]\n" \
"Lpd1_%=:\n" \
"s_lshl_b32 s66, s62, 2\n" \
"ds_read_b64_tr_b16 a[104:105], v233 offset:50176\n" \
"ds_read_b64_tr_b16 a[106:107], v233 offset:52224\n" \
"v_mfma_f32_16x16x32_bf16 v[72:75], a[80:83], a[16:19], v[72:75]\n" \
"s_add_u32 s66, s66, s61\n" \
"ds_read_b64_tr_b16 a[108:109], v233 offset:58368\n" \
"ds_read_b64_tr_b16 a[110:111], v233 offset:60416\n" \
"v_mfma_f32_16x16x32_bf16 v[72:75], a[84:87], a[20:23], v[72:75]\n" \
"s_and_b32 s66, s66, 15\n" \
"ds_read_b64_tr_b16 a[112:113], v232 offset:50688\n" \
"ds_read_b64_tr_b16 a[114:115], v232 offset:52736\n" \
"v_mfma_f32_16x16x32_bf16 v[76:79], a[88:91], a[16:19], v[76:79]\n" \
"s_lshl_b32 s66, s66, 12\n" \
"ds_read_b64_tr_b16 a[116:117], v232 offset:58880\n" \
"ds_read_b64_tr_b16 a[118:119], v232 offset:60928\n" \
"v_mfma_f32_16x16x32_bf16 v[76:79], a[92:95], a[20:23], v[76:79]\n" \
"s_add_u32 s64, s64, Lloop2_%=-Lpd1_%=\n" \
"s_addc_u32 s65, s65, 0\n" \
"s_mov_b32 s67, Lcode_end_%=-Lloop2_%=-4\n" \
"ds_read_b64_tr_b16 a[120:121], v233 offset:50688\n" \
"ds_read_b64_tr_b16 a[122:123], v233 offset:52736\n" \
"v_mfma_f32_16x16x32_bf16 v[96:99], a[64:67], a[24:27], v[96:99]\n" \
"s_add_u32 s93, s94, 17858560\n" \
"ds_read_b64_tr_b16 a[124:125], v233 offset:58880\n" \
"ds_read_b64_tr_b16 a[126:127], v233 offset:60928\n" \
"v_mfma_f32_16x16x32_bf16 v[96:99], a[68:71], a[28:31], v[96:99]\n" \
"buffer_store_dwordx4 v[44:47], v234, s[56:59], s93 offen offset:0 sc1\n" \
"v_mfma_f32_16x16x32_bf16 v[100:103], a[72:75], a[24:27], v[100:103]\n" \
"ds_write_b128 v225, a[228:231] offset:9216\n" \
"v_mfma_f32_16x16x32_bf16 v[100:103], a[76:79], a[28:31], v[100:103]\n" \
"v_lshl_add_u32 v230, v234, 2, s66\n" \
"v_min_u32 v230, s67, v230\n" \
"v_mfma_f32_16x16x32_bf16 v[104:107], a[80:83], a[24:27], v[104:107]\n" \
"buffer_load_dword v227, v242, s[56:59], s90 offen sc1\n" \
"v_mfma_f32_16x16x32_bf16 v[104:107], a[84:87], a[28:31], v[104:107]\n" \
"ds_write_b128 v225, a[232:235] offset:10240\n" \
"v_mfma_f32_16x16x32_bf16 v[108:111], a[88:91], a[24:27], v[108:111]\n" \
"global_load_dword v230, v230, s[64:65]\n" \
"v_mfma_f32_16x16x32_bf16 v[108:111], a[92:95], a[28:31], v[108:111]\n" \
"s_waitcnt lgkmcnt(1)\n" \
"v_cvt_pk_bf16_f32 v64, v64, v65\n" \
"v_cvt_pk_bf16_f32 v65, v66, v67\n" \
"v_mfma_f32_16x16x32_bf16 v[80:83], a[96:99], a[16:19], v[80:83]\n" \
"v_cvt_pk_bf16_f32 v66, v68, v69\n" \
"v_cvt_pk_bf16_f32 v67, v70, v71\n" \
"v_mfma_f32_16x16x32_bf16 v[80:83], a[100:103], a[20:23], v[80:83]\n" \
"v_accvgpr_write_b32 a128, v64\n" \
"v_accvgpr_write_b32 a129, v65\n" \
"v_accvgpr_write_b32 a130, v66\n" \
"v_mfma_f32_16x16x32_bf16 v[84:87], a[104:107], a[16:19], v[84:87]\n" \
"v_accvgpr_write_b32 a131, v67\n" \
"v_cvt_pk_bf16_f32 v72, v72, v73\n" \
"v_mfma_f32_16x16x32_bf16 v[84:87], a[108:111], a[20:23], v[84:87]\n" \
"buffer_store_dwordx4 v[60:63], v234, s[56:59], s93 offen offset:1024 sc1\n" \
"v_mfma_f32_16x16x32_bf16 v[88:91], a[112:115], a[16:19], v[88:91]\n" \
"ds_write_b128 v225, a[236:239] offset:11264\n" \
"v_mfma_f32_16x16x32_bf16 v[88:91], a[116:119], a[20:23], v[88:91]\n" \
"v_cvt_pk_bf16_f32 v73, v74, v75\n" \
"v_cvt_pk_bf16_f32 v74, v76, v77\n" \
"v_cvt_pk_bf16_f32 v75, v78, v79\n" \
"v_mfma_f32_16x16x32_bf16 v[92:95], a[120:123], a[16:19], v[92:95]\n" \
"v_accvgpr_write_b32 a132, v72\n" \
"v_accvgpr_write_b32 a133, v73\n" \
"v_mfma_f32_16x16x32_bf16 v[92:95], a[124:127], a[20:23], v[92:95]\n" \
"v_accvgpr_write_b32 a134, v74\n" \
"v_accvgpr_write_b32 a135, v75\n" \
"v_mfma_f32_16x16x32_bf16 v[112:115], a[96:99], a[24:27], v[112:115]\n" \
"v_cvt_pk_bf16_f32 v96, v96, v97\n" \
"v_cvt_pk_bf16_f32 v97, v98, v99\n" \
"v_mfma_f32_16x16x32_bf16 v[112:115], a[100:103], a[28:31], v[112:115]\n" \
"v_cvt_pk_bf16_f32 v98, v100, v101\n" \
"v_cvt_pk_bf16_f32 v99, v102, v103\n" \
"v_mfma_f32_16x16x32_bf16 v[116:119], a[104:107], a[24:27], v[116:119]\n" \
"v_cvt_pk_bf16_f32 v104, v104, v105\n" \
"v_cvt_pk_bf16_f32 v105, v106, v107\n" \
"v_mfma_f32_16x16x32_bf16 v[116:119], a[108:111], a[28:31], v[116:119]\n" \
"v_cvt_pk_bf16_f32 v106, v108, v109\n" \
"v_cvt_pk_bf16_f32 v107, v110, v111\n" \
"v_mfma_f32_16x16x32_bf16 v[120:123], a[112:115], a[24:27], v[120:123]\n" \
"v_mfma_f32_16x16x32_bf16 v[120:123], a[116:119], a[28:31], v[120:123]\n" \
"v_cvt_pk_bf16_f32 v80, v80, v81\n" \
"v_cvt_pk_bf16_f32 v81, v82, v83\n" \
"v_cvt_pk_bf16_f32 v82, v84, v85\n" \
"v_mfma_f32_16x16x32_bf16 v[124:127], a[120:123], a[24:27], v[124:127]\n" \
"v_cvt_pk_bf16_f32 v83, v86, v87\n" \
"v_accvgpr_write_b32 a136, v80\n" \
"v_mfma_f32_16x16x32_bf16 v[124:127], a[124:127], a[28:31], v[124:127]\n" \
"v_accvgpr_write_b32 a137, v81\n" \
"s_waitcnt lgkmcnt(0)\n" \
"s_barrier\n" \
"v_mfma_f32_16x16x32_bf16 v[128:131], a[64:67], a[0:3], v[128:131]\n" \
"v_accvgpr_write_b32 a138, v82\n" \
"v_accvgpr_write_b32 a139, v83\n" \
"v_mfma_f32_16x16x32_bf16 v[128:131], a[68:71], a[4:7], v[128:131]\n" \
"ds_read_b128 a[192:195], v237 offset:0\n" \
"v_cvt_pk_bf16_f32 v88, v88, v89\n" \
"v_mfma_f32_16x16x32_bf16 v[132:135], a[72:75], a[0:3], v[132:135]\n" \
"v_cvt_pk_bf16_f32 v89, v90, v91\n" \
"v_cvt_pk_bf16_f32 v90, v92, v93\n" \
"v_mfma_f32_16x16x32_bf16 v[132:135], a[76:79], a[4:7], v[132:135]\n" \
"ds_read_b128 a[196:199], v237 offset:2048\n" \
"v_cvt_pk_bf16_f32 v91, v94, v95\n" \
"v_mfma_f32_16x16x32_bf16 v[136:139], a[80:83], a[0:3], v[136:139]\n" \
"v_med3_f32 v208, v248, v252, s72\n" \
"v_med3_f32 v209, v249, v252, s72\n" \
"v_mfma_f32_16x16x32_bf16 v[136:139], a[84:87], a[4:7], v[136:139]\n" \
"ds_read_b128 a[200:203], v237 offset:4096\n" \
"v_med3_f32 v210, v250, v252, s72\n" \
"v_med3_f32 v211, v251, v252, s72\n" \
"v_mfma_f32_16x16x32_bf16 v[140:143], a[88:91], a[0:3], v[140:143]\n" \
"buffer_store_dwordx4 v[20:23], v234, s[56:59], s93 offen offset:2048 sc1\n" \
"v_mfma_f32_16x16x32_bf16 v[140:143], a[92:95], a[4:7], v[140:143]\n" \
"ds_read_b128 a[204:207], v237 offset:6144\n" \
"v_cvt_pk_bf16_f32 v112, v112, v113\n" \
"v_mfma_f32_16x16x32_bf16 v[144:147], a[96:99], a[0:3], v[144:147]\n" \
"v_cvt_pk_bf16_f32 v113, v114, v115\n" \
"v_cvt_pk_bf16_f32 v114, v116, v117\n" \
"v_mfma_f32_16x16x32_bf16 v[144:147], a[100:103], a[4:7], v[144:147]\n" \
"ds_read_b128 a[208:211], v237 offset:256\n" \
"v_cvt_pk_bf16_f32 v115, v118, v119\n" \
"v_cvt_pk_bf16_f32 v120, v120, v121\n" \
"v_mfma_f32_16x16x32_bf16 v[148:151], a[104:107], a[0:3], v[148:151]\n" \
"v_cvt_pk_bf16_f32 v121, v122, v123\n" \
"v_cvt_pk_bf16_f32 v122, v124, v125\n" \
"v_mfma_f32_16x16x32_bf16 v[148:151], a[108:111], a[4:7], v[148:151]\n" \
"ds_read_b128 a[212:215], v237 offset:2304\n" \
"v_cvt_pk_bf16_f32 v123, v126, v127\n" \
"v_mfma_f32_16x16x32_bf16 v[152:155], a[112:115], a[0:3], v[152:155]\n" \
"v_mfma_f32_16x16x32_bf16 v[152:155], a[116:119], a[4:7], v[152:155]\n" \
"ds_read_b128 a[216:219], v237 offset:4352\n" \
"v_cvt_pk_bf16_f32 v128, v128, v129\n" \
"v_mfma_f32_16x16x32_bf16 v[156:159], a[120:123], a[0:3], v[156:159]\n" \
"v_cvt_pk_bf16_f32 v129, v130, v131\n" \
"v_cvt_pk_bf16_f32 v130, v132, v133\n" \
"v_mfma_f32_16x16x32_bf16 v[156:159], a[124:127], a[4:7], v[156:159]\n" \
"ds_read_b128 a[220:223], v237 offset:6400\n" \
"v_cvt_pk_bf16_f32 v131, v134, v135\n" \
"v_cvt_pk_bf16_f32 v136, v136, v137\n" \
"v_mfma_f32_16x16x32_bf16 v[160:163], a[64:67], a[8:11], v[160:163]\n" \
"v_cvt_pk_bf16_f32 v137, v138, v139\n" \
"v_cvt_pk_bf16_f32 v138, v140, v141\n" \
"v_mfma_f32_16x16x32_bf16 v[160:163], a[68:71], a[12:15], v[160:163]\n" \
"ds_read_b128 a[224:227], v237 offset:8192\n" \
"v_cvt_pk_bf16_f32 v139, v142, v143\n" \
"v_mfma_f32_16x16x32_bf16 v[164:167], a[72:75], a[8:11], v[164:167]\n" \
"v_mfma_f32_16x16x32_bf16 v[164:167], a[76:79], a[12:15], v[164:167]\n" \
"ds_read_b128 a[228:231], v237 offset:10240\n" \
"v_mfma_f32_16x16x32_bf16 v[168:171], a[80:83], a[8:11], v[168:171]\n" \
"v_mfma_f32_16x16x32_bf16 v[168:171], a[84:87], a[12:15], v[168:171]\n" \
"ds_read_b128 a[232:235], v237 offset:12288\n" \
"v_cvt_pk_bf16_f32 v144, v144, v145\n" \
"v_cvt_pk_bf16_f32 v145, v146, v147\n" \
"v_mfma_f32_16x16x32_bf16 v[172:175], a[88:91], a[8:11], v[172:175]\n" \
"v_cvt_pk_bf16_f32 v146, v148, v149\n" \
"v_cvt_pk_bf16_f32 v147, v150, v151\n" \
"v_mfma_f32_16x16x32_bf16 v[172:175], a[92:95], a[12:15], v[172:175]\n" \
"ds_read_b128 a[236:239], v237 offset:14336\n" \
"v_cvt_pk_bf16_f32 v152, v152, v153\n" \
"v_mfma_f32_16x16x32_bf16 v[176:179], a[96:99], a[8:11], v[176:179]\n" \
"v_cvt_pk_bf16_f32 v153, v154, v155\n" \
"v_cvt_pk_bf16_f32 v154, v156, v157\n" \
"v_mfma_f32_16x16x32_bf16 v[176:179], a[100:103], a[12:15], v[176:179]\n" \
"ds_read_b128 a[240:243], v237 offset:8448\n" \
"v_cvt_pk_bf16_f32 v155, v158, v159\n" \
"v_mfma_f32_16x16x32_bf16 v[180:183], a[104:107], a[8:11], v[180:183]\n" \
"buffer_store_dwordx4 v[28:31], v234, s[56:59], s93 offen offset:3072 sc1\n" \
"v_mfma_f32_16x16x32_bf16 v[180:183], a[108:111], a[12:15], v[180:183]\n" \
"ds_read_b128 a[244:247], v237 offset:10496\n" \
"ds_write_b128 v239, v[128:131] offset:0\n" \
"v_mov_b32 v196, 0x42400000\n" \
"v_mfma_f32_16x16x32_bf16 v[184:187], a[112:115], a[8:11], v[184:187]\n" \
"v_mov_b32 v197, 0x42400000\n" \
"v_mov_b32 v198, 0x42400000\n" \
"v_mfma_f32_16x16x32_bf16 v[184:187], a[116:119], a[12:15], v[184:187]\n" \
"ds_read_b128 a[248:251], v237 offset:12544\n" \
"v_mov_b32 v199, 0x42400000\n" \
"v_cvt_pk_bf16_f32 v160, v160, v161\n" \
"v_mfma_f32_16x16x32_bf16 v[188:191], a[120:123], a[8:11], v[188:191]\n" \
"v_cvt_pk_bf16_f32 v161, v162, v163\n" \
"v_cvt_pk_bf16_f32 v162, v164, v165\n" \
"v_mfma_f32_16x16x32_bf16 v[188:191], a[124:127], a[12:15], v[188:191]\n" \
"s_waitcnt lgkmcnt(3)\n" \
"ds_read_b128 a[252:255], v237 offset:14592\n" \
"v_cvt_pk_bf16_f32 v163, v166, v167\n" \
"v_mfma_f32_16x16x32_bf16 v[16:19], a[192:195], v[96:99], v[208:211]\n" \
"v_cvt_pk_bf16_f32 v168, v168, v169\n" \
"v_cvt_pk_bf16_f32 v169, v170, v171\n" \
"v_mfma_f32_16x16x32_bf16 v[16:19], a[196:199], v[104:107], v[16:19]\n" \
"v_cvt_pk_bf16_f32 v170, v172, v173\n" \
"v_cvt_pk_bf16_f32 v171, v174, v175\n" \
"v_mfma_f32_16x16x32_bf16 v[16:19], a[200:203], v[112:115], v[16:19]\n" \
"v_cvt_pk_bf16_f32 v176, v176, v177\n" \
"v_cvt_pk_bf16_f32 v177, v178, v179\n" \
"v_mfma_f32_16x16x32_bf16 v[16:19], a[204:207], v[120:123], v[16:19]\n" \
"v_cvt_pk_bf16_f32 v178, v180, v181\n" \
"v_cvt_pk_bf16_f32 v179, v182, v183\n" \
"v_mfma_f32_16x16x32_bf16 v[32:35], a[204:207], v[152:155], v[196:199]\n" \
"v_cvt_pk_bf16_f32 v184, v184, v185\n" \
"v_cvt_pk_bf16_f32 v185, v186, v187\n" \
"v_mfma_f32_16x16x32_bf16 v[32:35], a[200:203], v[144:147], v[32:35]\n" \
"v_cvt_pk_bf16_f32 v186, v188, v189\n" \
"v_cvt_pk_bf16_f32 v187, v190, v191\n" \
"v_mfma_f32_16x16x32_bf16 v[32:35], a[196:199], v[136:139], v[32:35]\n" \
"s_add_u32 s93, s94, 17850368\n" \
"v_med3_f32 v212, v248, v253, v252\n" \
"v_med3_f32 v213, v249, v253, v252\n" \
"v_mfma_f32_16x16x32_bf16 v[32:35], a[192:195], v[128:131], v[32:35]\n" \
"buffer_store_dwordx4 v[0:3], v234, s[56:59], s93 offen offset:0 sc1\n" \
"v_mfma_f32_16x16x32_bf16 v[48:51], a[192:195], v[160:163], v[196:199]\n" \
"v_med3_f32 v214, v250, v253, v252\n" \
"v_med3_f32 v215, v251, v253, v252\n" \
"v_mfma_f32_16x16x32_bf16 v[48:51], a[196:199], v[168:171], v[48:51]\n" \
"Lpg5010_%=:\n" \
"s_cbranch_execz Lpg6766_%=\n" \
"ds_read_b128 a[192:195], v237 offset:16384\n" \
"v_med3_f32 v216, v248, v226, v253\n" \
"v_mfma_f32_16x16x32_bf16 v[48:51], a[200:203], v[176:179], v[48:51]\n" \
"ds_read_b128 a[196:199], v237 offset:18432\n" \
"v_med3_f32 v217, v249, v226, v253\n" \
"v_mfma_f32_16x16x32_bf16 v[48:51], a[204:207], v[184:187], v[48:51]\n" \
"ds_read_b128 a[200:203], v237 offset:20480\n" \
"ds_write_b128 v239, v[136:139] offset:1024\n" \
"v_mfma_f32_16x16x32_bf16 v[52:55], a[220:223], v[184:187], v[196:199]\n" \
"ds_read_b128 a[204:207], v237 offset:22528\n" \
"v_med3_f32 v218, v250, v226, v253\n" \
"v_mfma_f32_16x16x32_bf16 v[52:55], a[216:219], v[176:179], v[52:55]\n" \
"v_med3_f32 v219, v251, v226, v253\n" \
"v_med3_f32 v220, v248, v240, v226\n" \
"v_mfma_f32_16x16x32_bf16 v[52:55], a[212:215], v[168:171], v[52:55]\n" \
"ds_write_b128 v239, v[144:147] offset:2048\n" \
"v_mfma_f32_16x16x32_bf16 v[52:55], a[208:211], v[160:163], v[52:55]\n" \
"buffer_store_dwordx4 v[4:7], v234, s[56:59], s93 offen offset:1024 sc1\n" \
"v_mfma_f32_16x16x32_bf16 v[36:39], a[208:211], v[128:131], v[196:199]\n" \
"v_med3_f32 v221, v249, v240, v226\n" \
"v_med3_f32 v222, v250, v240, v226\n" \
"v_mfma_f32_16x16x32_bf16 v[36:39], a[212:215], v[136:139], v[36:39]\n" \
"v_med3_f32 v223, v251, v240, v226\n" \
"v_accvgpr_write_b32 a144, v96\n" \
"v_mfma_f32_16x16x32_bf16 v[36:39], a[216:219], v[144:147], v[36:39]\n" \
"v_accvgpr_write_b32 a145, v97\n" \
"v_accvgpr_write_b32 a146, v98\n" \
"v_mfma_f32_16x16x32_bf16 v[36:39], a[220:223], v[152:155], v[36:39]\n" \
"ds_write_b128 v239, v[152:155] offset:3072\n" \
"v_mfma_f32_16x16x32_bf16 v[20:23], a[220:223], v[120:123], v[212:215]\n" \
"v_accvgpr_write_b32 a147, v99\n" \
"v_accvgpr_write_b32 a148, v104\n" \
"v_mfma_f32_16x16x32_bf16 v[20:23], a[216:219], v[112:115], v[20:23]\n" \
"ds_read_b128 a[220:223], v237 offset:22784\n" \
"v_accvgpr_write_b32 a149, v105\n" \
"v_mfma_f32_16x16x32_bf16 v[20:23], a[212:215], v[104:107], v[20:23]\n" \
"ds_read_b128 a[216:219], v237 offset:20736\n" \
"v_accvgpr_write_b32 a150, v106\n" \
"v_mfma_f32_16x16x32_bf16 v[20:23], a[208:211], v[96:99], v[20:23]\n" \
"ds_read_b128 a[212:215], v237 offset:18688\n" \
"ds_write_b128 v239, v[160:163] offset:4096\n" \
"v_mfma_f32_16x16x32_bf16 v[24:27], a[224:227], v[96:99], v[216:219]\n" \
"ds_read_b128 a[208:211], v237 offset:16640\n" \
"v_accvgpr_write_b32 a151, v107\n" \
"v_mfma_f32_16x16x32_bf16 v[24:27], a[228:231], v[104:107], v[24:27]\n" \
"v_accvgpr_write_b32 a152, v112\n" \
"v_accvgpr_write_b32 a153, v113\n" \
"v_mfma_f32_16x16x32_bf16 v[24:27], a[232:235], v[112:115], v[24:27]\n" \
"ds_write_b128 v239, v[168:171] offset:5120\n" \
"v_mfma_f32_16x16x32_bf16 v[24:27], a[236:239], v[120:123], v[24:27]\n" \
"buffer_store_dwordx4 v[8:11], v234, s[56:59], s93 offen offset:2048 sc1\n" \
"v_mfma_f32_16x16x32_bf16 v[40:43], a[236:239], v[152:155], v[196:199]\n" \
"v_accvgpr_write_b32 a154, v114\n" \
"v_accvgpr_write_b32 a155, v115\n" \
"v_mfma_f32_16x16x32_bf16 v[40:43], a[232:235], v[144:147], v[40:43]\n" \
"v_accvgpr_write_b32 a156, v120\n" \
"v_accvgpr_write_b32 a157, v121\n" \
"v_mfma_f32_16x16x32_bf16 v[40:43], a[228:231], v[136:139], v[40:43]\n" \
"v_accvgpr_write_b32 a158, v122\n" \
"v_accvgpr_write_b32 a159, v123\n" \
"v_mfma_f32_16x16x32_bf16 v[40:43], a[224:227], v[128:131], v[40:43]\n" \
"ds_write_b128 v239, v[176:179] offset:6144\n" \
"v_mfma_f32_16x16x32_bf16 v[56:59], a[224:227], v[160:163], v[196:199]\n" \
"v_accvgpr_write_b32 a140, v88\n" \
"v_accvgpr_write_b32 a141, v89\n" \
"v_mfma_f32_16x16x32_bf16 v[56:59], a[228:231], v[168:171], v[56:59]\n" \
"ds_read_b128 a[224:227], v237 offset:24576\n" \
"v_accvgpr_write_b32 a142, v90\n" \
"v_mfma_f32_16x16x32_bf16 v[56:59], a[232:235], v[176:179], v[56:59]\n" \
"s_waitcnt lgkmcnt(2)\n" \
"ds_read_b128 a[228:231], v237 offset:26624\n" \
"v_accvgpr_write_b32 a143, v91\n" \
"v_mfma_f32_16x16x32_bf16 v[56:59], a[236:239], v[184:187], v[56:59]\n" \
"buffer_store_dwordx4 v[12:15], v234, s[56:59], s93 offen offset:3072 sc1\n" \
"ds_write_b128 v239, v[184:187] offset:7168\n" \
"v_mfma_f32_16x16x32_bf16 v[60:63], a[252:255], v[184:187], v[196:199]\n" \
"ds_read_b128 a[232:235], v237 offset:28672\n" \
"v_add_u32 v233, s82, v235\n" \
"v_mfma_f32_16x16x32_bf16 v[60:63], a[248:251], v[176:179], v[60:63]\n" \
"s_waitcnt vmcnt(8)\n" \
"ds_read_b128 a[236:239], v237 offset:30720\n" \
"v_cmp_eq_u32 vcc, s60, v227\n" \
"v_mfma_f32_16x16x32_bf16 v[60:63], a[244:247], v[168:171], v[60:63]\n" \
"v_exp_f32 v16, v16\n" \
"v_mfma_f32_16x16x32_bf16 v[60:63], a[240:243], v[160:163], v[60:63]\n" \
"s_andn2_b32 s70, 0xff, vcc_lo\n" \
"s_cbranch_scc1 Lpl1_%=\n" \
"Lpr1_%=:\n" \
"v_exp_f32 v17, v17\n" \
"v_exp_f32 v18, v18\n" \
"v_mfma_f32_16x16x32_bf16 v[44:47], a[240:243], v[128:131], v[196:199]\n" \
"v_exp_f32 v19, v19\n" \
"v_mfma_f32_16x16x32_bf16 v[44:47], a[244:247], v[136:139], v[44:47]\n" \
"s_add_u32 m0, s79, 32768\n" \
"v_exp_f32 v20, v20\n" \
"v_mfma_f32_16x16x32_bf16 v[44:47], a[248:251], v[144:147], v[44:47]\n" \
"s_add_u32 s71, s76, 0\n" \
"v_cvt_pk_bf16_f32 v16, v16, v17\n" \
"v_cvt_pk_bf16_f32 v17, v18, v19\n" \
"v_mfma_f32_16x16x32_bf16 v[44:47], a[252:255], v[152:155], v[44:47]\n" \
"buffer_load_dwordx4 v234, s[36:39], s71 offen offset:0 sc1 lds\n" \
"v_exp_f32 v21, v21\n" \
"v_exp_f32 v22, v22\n" \
"v_mfma_f32_16x16x32_bf16 v[28:31], a[252:255], v[120:123], v[220:223]\n" \
"v_exp_f32 v23, v23\n" \
"v_mfma_f32_16x16x32_bf16 v[28:31], a[248:251], v[112:115], v[28:31]\n" \
"ds_read_b128 a[252:255], v237 offset:30976\n" \
"v_cvt_pk_bf16_f32 v18, v20, v21\n" \
"v_mfma_f32_16x16x32_bf16 v[28:31], a[244:247], v[104:107], v[28:31]\n" \
"buffer_load_dwordx4 v234, s[36:39], s71 offen offset:1024 sc1 lds\n" \
"v_mfma_f32_16x16x32_bf16 v[28:31], a[240:243], v[96:99], v[28:31]\n" \
"ds_read_b128 a[244:247], v237 offset:26880\n" \
"ds_read_b128 a[248:251], v237 offset:28928\n" \
"v_exp_f32 v32, v32\n" \
"v_exp_f32 v33, v33\n" \
"v_mfma_f32_16x16x32_bf16 v[80:83], a[192:195], a[144:147], v[196:199]\n" \
"ds_read_b128 a[240:243], v237 offset:24832\n" \
"v_cvt_pk_bf16_f32 v19, v22, v23\n" \
"v_mfma_f32_16x16x32_bf16 v[80:83], a[196:199], a[148:151], v[80:83]\n" \
"buffer_load_dwordx4 v234, s[36:39], s71 offen offset:2048 sc1 lds\n" \
"v_mfma_f32_16x16x32_bf16 v[80:83], a[200:203], a[152:155], v[80:83]\n" \
"s_lshl_b32 s68, s63, 2\n" \
"s_add_u32 s68, s68, s95\n" \
"s_mov_b32 s69, 0\n" \
"v_mfma_f32_16x16x32_bf16 v[80:83], a[204:207], a[156:159], v[80:83]\n" \
"s_mov_b32 s89, s63\n" \
"ds_read_b128 a[44:47], v233 offset:15360\n" \
"v_mov_b32 v192, s60\n" \
"v_lshlrev_b32 v193, 28, s61\n" \
"v_exp_f32 v34, v34\n" \
"v_exp_f32 v35, v35\n" \
"v_mfma_f32_16x16x32_bf16 v[96:99], a[204:207], v[152:155], v[196:199]\n" \
"buffer_load_dwordx4 v234, s[36:39], s71 offen offset:3072 sc1 lds\n" \
"v_mfma_f32_16x16x32_bf16 v[96:99], a[200:203], v[144:147], v[96:99]\n" \
"s_add_u32 m0, s79, 49152\n" \
"s_mov_b32 s90, 0\n" \
"ds_read_b128 a[40:43], v233 offset:7168\n" \
"v_mfma_f32_16x16x32_bf16 v[96:99], a[196:199], v[136:139], v[96:99]\n" \
"s_add_u32 s71, s76, 17408\n" \
"v_exp_f32 v36, v36\n" \
"v_cvt_pk_bf16_f32 v32, v32, v33\n" \
"v_mfma_f32_16x16x32_bf16 v[96:99], a[192:195], v[128:131], v[96:99]\n" \
"buffer_load_dwordx4 v234, s[36:39], s71 offen offset:0 sc1 lds\n" \
"v_mfma_f32_16x16x32_bf16 v[112:115], a[192:195], v[160:163], v[196:199]\n" \
"ds_read_b128 a[36:39], v233 offset:14336\n" \
"v_cvt_pk_bf16_f32 v33, v34, v35\n" \
"v_mfma_f32_16x16x32_bf16 v[112:115], a[196:199], v[168:171], v[112:115]\n" \
"v_exp_f32 v37, v37\n" \
"v_mfma_f32_16x16x32_bf16 v[112:115], a[200:203], v[176:179], v[112:115]\n" \
"buffer_load_dwordx4 v234, s[36:39], s71 offen offset:1024 sc1 lds\n" \
"v_mfma_f32_16x16x32_bf16 v[112:115], a[204:207], v[184:187], v[112:115]\n" \
"ds_read_b128 a[32:35], v233 offset:6144\n" \
"v_exp_f32 v38, v38\n" \
"v_exp_f32 v39, v39\n" \
"v_mfma_f32_16x16x32_bf16 v[64:67], a[204:207], a[140:143], v[208:211]\n" \
"v_exp_f32 v48, v48\n" \
"v_mfma_f32_16x16x32_bf16 v[64:67], a[200:203], a[136:139], v[64:67]\n" \
"buffer_load_dwordx4 v234, s[36:39], s71 offen offset:2048 sc1 lds\n" \
"v_cvt_pk_bf16_f32 v34, v36, v37\n" \
"v_mfma_f32_16x16x32_bf16 v[64:67], a[196:199], a[132:135], v[64:67]\n" \
"ds_read_b128 a[204:207], v233 offset:9216\n" \
"v_cvt_pk_bf16_f32 v35, v38, v39\n" \
"v_mfma_f32_16x16x32_bf16 v[64:67], a[192:195], a[128:131], v[64:67]\n" \
"v_exp_f32 v49, v49\n" \
"v_exp_f32 v50, v50\n" \
"v_mfma_f32_16x16x32_bf16 v[68:71], a[208:211], a[128:131], v[212:215]\n" \
"buffer_load_dwordx4 v234, s[36:39], s71 offen offset:3072 sc1 lds\n" \
"v_mfma_f32_16x16x32_bf16 v[68:71], a[212:215], a[132:135], v[68:71]\n" \
"ds_read_b128 a[200:203], v233 offset:1024\n" \
"v_cvt_pk_bf16_f32 v48, v48, v49\n" \
"v_mfma_f32_16x16x32_bf16 v[68:71], a[216:219], a[136:139], v[68:71]\n" \
"s_add_u32 m0, s77, s80\n" \
"v_exp_f32 v51, v51\n" \
"v_mfma_f32_16x16x32_bf16 v[68:71], a[220:223], a[140:143], v[68:71]\n" \
"buffer_load_dwordx4 v234, s[40:43], s76 offen offset:0 sc1 lds\n" \
"v_exp_f32 v52, v52\n" \
"v_exp_f32 v53, v53\n" \
"v_mfma_f32_16x16x32_bf16 v[116:119], a[220:223], v[184:187], v[196:199]\n" \
"ds_read_b128 a[196:199], v233 offset:8192\n" \
"v_cvt_pk_bf16_f32 v49, v50, v51\n" \
"v_mfma_f32_16x16x32_bf16 v[116:119], a[216:219], v[176:179], v[116:119]\n" \
"ds_read_b128 a[160:163], v239 offset:0\n" \
"v_mfma_f32_16x16x32_bf16 v[116:119], a[212:215], v[168:171], v[116:119]\n" \
"s_add_u32 s71, s76, 17408\n" \
"ds_read_b128 a[192:195], v233 offset:0\n" \
"v_exp_f32 v54, v54\n" \
"v_mfma_f32_16x16x32_bf16 v[116:119], a[208:211], v[160:163], v[116:119]\n" \
"buffer_load_dwordx4 v234, s[40:43], s76 offen offset:1024 sc1 lds\n" \
"v_mfma_f32_16x16x32_bf16 v[100:103], a[208:211], v[128:131], v[196:199]\n" \
"ds_read_b128 a[164:167], v239 offset:1024\n" \
"v_cvt_pk_bf16_f32 v50, v52, v53\n" \
"v_mfma_f32_16x16x32_bf16 v[100:103], a[212:215], v[136:139], v[100:103]\n" \
"v_exp_f32 v55, v55\n" \
"v_mfma_f32_16x16x32_bf16 v[100:103], a[216:219], v[144:147], v[100:103]\n" \
"v_cvt_pk_bf16_f32 v51, v54, v55\n" \
"v_exp_f32 v24, v24\n" \
"v_mfma_f32_16x16x32_bf16 v[100:103], a[220:223], v[152:155], v[100:103]\n" \
"buffer_load_dwordx4 v234, s[40:43], s76 offen offset:2048 sc1 lds\n" \
"v_exp_f32 v25, v25\n" \
"v_mfma_f32_16x16x32_bf16 v[84:87], a[220:223], a[156:159], v[196:199]\n" \
"ds_read_b128 a[168:171], v239 offset:2048\n" \
"v_mfma_f32_16x16x32_bf16 v[84:87], a[216:219], a[152:155], v[84:87]\n" \
"ds_read_b128 a[220:223], v233 offset:11264\n" \
"v_mfma_f32_16x16x32_bf16 v[84:87], a[212:215], a[148:151], v[84:87]\n" \
"s_waitcnt lgkmcnt(2)\n" \
"v_exp_f32 v26, v26\n" \
"v_mfma_f32_16x16x32_bf16 v[84:87], a[208:211], a[144:147], v[84:87]\n" \
"buffer_load_dwordx4 v234, s[40:43], s76 offen offset:3072 sc1 lds\n" \
"v_exp_f32 v27, v27\n" \
"v_mfma_f32_16x16x32_bf16 v[88:91], a[224:227], a[144:147], v[196:199]\n" \
"ds_read_b128 a[216:219], v233 offset:3072\n" \
"v_cvt_pk_bf16_f32 v20, v24, v25\n" \
"v_mfma_f32_16x16x32_bf16 v[88:91], a[228:231], a[148:151], v[88:91]\n" \
"ds_read_b128 a[172:175], v239 offset:3072\n" \
"v_exp_f32 v28, v28\n" \
"v_mfma_f32_16x16x32_bf16 v[88:91], a[232:235], a[152:155], v[88:91]\n" \
"s_add_u32 m0, m0, 0x4000\n" \
"ds_read_b128 a[212:215], v233 offset:10240\n" \
"v_xor_b32 v248, 0x2000, v239\n" \
"v_mfma_f32_16x16x32_bf16 v[88:91], a[236:239], a[156:159], v[88:91]\n" \
"buffer_load_dwordx4 v234, s[40:43], s71 offen offset:0 sc1 lds\n" \
"v_exp_f32 v29, v29\n" \
"v_mfma_f32_16x16x32_bf16 v[104:107], a[236:239], v[152:155], v[196:199]\n" \
"ds_read_b128 a[208:211], v233 offset:2048\n" \
"v_lshlrev_b32 v252, s48, v234\n" \
"v_mfma_f32_16x16x32_bf16 v[104:107], a[232:235], v[144:147], v[104:107]\n" \
"ds_read_b128 a[176:179], v239 offset:4096\n" \
"v_mov_b32 v224, 0\n" \
"v_mfma_f32_16x16x32_bf16 v[104:107], a[228:231], v[136:139], v[104:107]\n" \
"v_mov_b32 v225, 0\n" \
"v_mov_b32 v228, 0\n" \
"v_mfma_f32_16x16x32_bf16 v[104:107], a[224:227], v[128:131], v[104:107]\n" \
"buffer_load_dwordx4 v234, s[40:43], s71 offen offset:1024 sc1 lds\n" \
"v_exp_f32 v30, v30\n" \
"v_cvt_pk_bf16_f32 v21, v26, v27\n" \
"v_mfma_f32_16x16x32_bf16 v[120:123], a[224:227], v[160:163], v[196:199]\n" \
"ds_read_b128 a[180:183], v239 offset:5120\n" \
"v_mov_b32 v229, 0\n" \
"v_mfma_f32_16x16x32_bf16 v[120:123], a[228:231], v[168:171], v[120:123]\n" \
"v_exp_f32 v31, v31\n" \
"v_mfma_f32_16x16x32_bf16 v[120:123], a[232:235], v[176:179], v[120:123]\n" \
"v_exp_f32 v40, v40\n" \
"v_cvt_pk_bf16_f32 v22, v28, v29\n" \
"v_mfma_f32_16x16x32_bf16 v[120:123], a[236:239], v[184:187], v[120:123]\n" \
"buffer_load_dwordx4 v234, s[40:43], s71 offen offset:2048 sc1 lds\n" \
"v_exp_f32 v41, v41\n" \
"v_mfma_f32_16x16x32_bf16 v[72:75], a[236:239], a[140:143], v[216:219]\n" \
"ds_read_b128 a[184:187], v239 offset:6144\n" \
"v_cvt_pk_bf16_f32 v23, v30, v31\n" \
"v_mfma_f32_16x16x32_bf16 v[72:75], a[232:235], a[136:139], v[72:75]\n" \
"ds_read_b128 a[236:239], v233 offset:13312\n" \
"v_mfma_f32_16x16x32_bf16 v[72:75], a[228:231], a[132:135], v[72:75]\n" \
"v_exp_f32 v42, v42\n" \
"v_mfma_f32_16x16x32_bf16 v[72:75], a[224:227], a[128:131], v[72:75]\n" \
"buffer_load_dwordx4 v234, s[40:43], s71 offen offset:3072 sc1 lds\n" \
"v_exp_f32 v43, v43\n" \
"v_exp_f32 v44, v44\n" \
"v_mfma_f32_16x16x32_bf16 v[76:79], a[240:243], a[128:131], v[220:223]\n" \
"ds_read_b128 a[232:235], v233 offset:5120\n" \
"v_cvt_pk_bf16_f32 v36, v40, v41\n" \
"v_mfma_f32_16x16x32_bf16 v[76:79], a[244:247], a[132:135], v[76:79]\n" \
"ds_read_b128 a[188:191], v239 offset:7168\n" \
"v_cvt_pk_bf16_f32 v37, v42, v43\n" \
"v_mfma_f32_16x16x32_bf16 v[76:79], a[248:251], a[136:139], v[76:79]\n" \
"ds_read_b128 a[228:231], v233 offset:12288\n" \
"v_mfma_f32_16x16x32_bf16 v[76:79], a[252:255], a[140:143], v[76:79]\n" \
"v_exp_f32 v45, v45\n" \
"v_exp_f32 v46, v46\n" \
"v_mfma_f32_16x16x32_bf16 v[124:127], a[252:255], v[184:187], v[196:199]\n" \
"ds_read_b128 a[224:227], v233 offset:4096\n" \
"v_cvt_pk_bf16_f32 v38, v44, v45\n" \
"v_mfma_f32_16x16x32_bf16 v[124:127], a[248:251], v[176:179], v[124:127]\n" \
"v_exp_f32 v47, v47\n" \
"v_mfma_f32_16x16x32_bf16 v[124:127], a[244:247], v[168:171], v[124:127]\n" \
"v_exp_f32 v56, v56\n" \
"v_exp_f32 v57, v57\n" \
"v_mfma_f32_16x16x32_bf16 v[124:127], a[240:243], v[160:163], v[124:127]\n" \
"v_exp_f32 v58, v58\n" \
"v_cvt_pk_bf16_f32 v39, v46, v47\n" \
"v_mfma_f32_16x16x32_bf16 v[108:111], a[240:243], v[128:131], v[196:199]\n" \
"v_exp_f32 v59, v59\n" \
"v_mfma_f32_16x16x32_bf16 v[108:111], a[244:247], v[136:139], v[108:111]\n" \
"v_exp_f32 v60, v60\n" \
"v_mfma_f32_16x16x32_bf16 v[108:111], a[248:251], v[144:147], v[108:111]\n" \
"v_cvt_pk_bf16_f32 v52, v56, v57\n" \
"v_cvt_pk_bf16_f32 v53, v58, v59\n" \
"v_mfma_f32_16x16x32_bf16 v[108:111], a[252:255], v[152:155], v[108:111]\n" \
"v_exp_f32 v61, v61\n" \
"v_exp_f32 v62, v62\n" \
"v_exp_f32 v63, v63\n" \
"v_mfma_f32_16x16x32_bf16 v[92:95], a[252:255], a[156:159], v[196:199]\n" \
"v_add_u32 v232, s91, v237\n" \
"v_cvt_pk_bf16_f32 v54, v60, v61\n" \
"v_mfma_f32_16x16x32_bf16 v[92:95], a[248:251], a[152:155], v[92:95]\n" \
"v_cvt_pk_bf16_f32 v55, v62, v63\n" \
"v_mfma_f32_16x16x32_bf16 v[92:95], a[244:247], a[148:151], v[92:95]\n" \
"s_waitcnt vmcnt(8) lgkmcnt(0)\n" \
"s_barrier\n" \
"v_mfma_f32_16x16x32_bf16 v[92:95], a[240:243], a[144:147], v[92:95]\n" \
"buffer_store_dword v192, v193, s[56:59], s68 offen sc1\n" \
"v_mfma_f32_16x16x32_bf16 v[160:163], a[192:195], v[16:19], 0\n" \
"s_add_u32 m0, s74, s79\n" \
"ds_read_b128 a[240:243], v232 offset:8448\n" \
"v_xor_b32 v233, 0x4000, v233\n" \
"v_mfma_f32_16x16x32_bf16 v[160:163], a[196:199], v[20:23], v[160:163]\n" \
"buffer_load_dwordx4 v234, s[36:39], s73 offen offset:0 sc1 lds\n" \
"v_exp_f32 v80, v80\n" \
"v_mfma_f32_16x16x32_bf16 a[64:67], a[192:195], v[32:35], 0\n" \
"ds_read_b128 a[244:247], v232 offset:10496\n" \
"v_mfma_f32_16x16x32_bf16 a[64:67], a[196:199], v[36:39], a[64:67]\n" \
"ds_read_b128 a[0:3], v248 offset:0\n" \
"v_exp_f32 v81, v81\n" \
"v_mfma_f32_16x16x32_bf16 a[96:99], a[192:195], v[48:51], 0\n" \
"v_exp_f32 v82, v82\n" \
"v_mfma_f32_16x16x32_bf16 a[96:99], a[196:199], v[52:55], a[96:99]\n" \
"s_add_u32 s71, s73, 17408\n" \
"ds_read_b128 a[192:195], v232 offset:0\n" \
"v_cvt_pk_bf16_f32 v80, v80, v81\n" \
"v_mfma_f32_16x16x32_bf16 a[100:103], a[200:203], v[48:51], 0\n" \
"buffer_load_dwordx4 v234, s[36:39], s73 offen offset:1024 sc1 lds\n" \
"v_mfma_f32_16x16x32_bf16 a[100:103], a[204:207], v[52:55], a[100:103]\n" \
"ds_read_b128 a[196:199], v232 offset:2048\n" \
"v_exp_f32 v83, v83\n" \
"v_mfma_f32_16x16x32_bf16 a[68:71], a[200:203], v[32:35], 0\n" \
"ds_read_b128 a[4:7], v248 offset:1024\n" \
"v_mfma_f32_16x16x32_bf16 a[68:71], a[204:207], v[36:39], a[68:71]\n" \
"v_exp_f32 v84, v84\n" \
"v_exp_f32 v85, v85\n" \
"v_mfma_f32_16x16x32_bf16 v[164:167], a[200:203], v[16:19], 0\n" \
"ds_read_b128 a[8:11], v248 offset:2048\n" \
"v_cvt_pk_bf16_f32 v81, v82, v83\n" \
"v_mfma_f32_16x16x32_bf16 v[164:167], a[204:207], v[20:23], v[164:167]\n" \
"buffer_load_dwordx4 v234, s[36:39], s73 offen offset:2048 sc1 lds\n" \
"v_mfma_f32_16x16x32_bf16 v[168:171], a[208:211], v[16:19], 0\n" \
"ds_read_b128 a[200:203], v232 offset:4096\n" \
"v_mfma_f32_16x16x32_bf16 v[168:171], a[212:215], v[20:23], v[168:171]\n" \
"ds_read_b128 a[204:207], v232 offset:6144\n" \
"v_exp_f32 v86, v86\n" \
"v_mfma_f32_16x16x32_bf16 a[72:75], a[208:211], v[32:35], 0\n" \
"ds_read_b128 a[12:15], v248 offset:3072\n" \
"v_cvt_pk_bf16_f32 v82, v84, v85\n" \
"v_mfma_f32_16x16x32_bf16 a[72:75], a[212:215], v[36:39], a[72:75]\n" \
"v_exp_f32 v87, v87\n" \
"v_exp_f32 v96, v96\n" \
"v_mfma_f32_16x16x32_bf16 a[104:107], a[208:211], v[48:51], 0\n" \
"buffer_load_dwordx4 v234, s[36:39], s73 offen offset:3072 sc1 lds\n" \
"v_mfma_f32_16x16x32_bf16 a[104:107], a[212:215], v[52:55], a[104:107]\n" \
"ds_read_b128 a[208:211], v232 offset:256\n" \
"v_cvt_pk_bf16_f32 v83, v86, v87\n" \
"v_smfmac_f32_16x16x64_bf16 a[52:55], v[244:247], v[16:23], v243\n" \
"ds_read_b128 a[212:215], v232 offset:2304\n" \
"v_exp_f32 v97, v97\n" \
"v_mfma_f32_16x16x32_bf16 a[108:111], a[216:219], v[48:51], 0\n" \
"ds_read_b128 a[16:19], v248 offset:4096\n" \
"v_mfma_f32_16x16x32_bf16 a[108:111], a[220:223], v[52:55], a[108:111]\n" \
"s_add_u32 m0, m0, 0x4000\n" \
"v_exp_f32 v98, v98\n" \
"v_mfma_f32_16x16x32_bf16 a[76:79], a[216:219], v[32:35], 0\n" \
"buffer_load_dwordx4 v234, s[36:39], s71 offen offset:0 sc1 lds\n" \
"v_mfma_f32_16x16x32_bf16 a[76:79], a[220:223], v[36:39], a[76:79]\n" \
"ds_read_b128 a[20:23], v248 offset:5120\n" \
"v_cvt_pk_bf16_f32 v96, v96, v97\n" \
"v_exp_f32 v99, v99\n" \
"v_mfma_f32_16x16x32_bf16 v[172:175], a[216:219], v[16:19], 0\n" \
"v_exp_f32 v100, v100\n" \
"v_mfma_f32_16x16x32_bf16 v[172:175], a[220:223], v[20:23], v[172:175]\n" \
"ds_read_b128 a[216:219], v232 offset:4352\n" \
"v_cvt_pk_bf16_f32 v97, v98, v99\n" \
"v_mfma_f32_32x32x16_bf16 v[128:143], a[48:51], a[48:51], 0\n" \
"ds_read_b128 a[220:223], v232 offset:6400\n" \
"v_exp_f32 v101, v101\n" \
"v_mfma_f32_16x16x32_bf16 v[176:179], a[224:227], v[16:19], 0\n" \
"buffer_load_dwordx4 v234, s[36:39], s71 offen offset:1024 sc1 lds\n" \
"v_mfma_f32_16x16x32_bf16 v[176:179], a[228:231], v[20:23], v[176:179]\n" \
"ds_read_b128 a[24:27], v248 offset:6144\n" \
"v_exp_f32 v102, v102\n" \
"v_mfma_f32_16x16x32_bf16 a[80:83], a[224:227], v[32:35], 0\n" \
"v_exp_f32 v103, v103\n" \
"v_mfma_f32_16x16x32_bf16 a[80:83], a[228:231], v[36:39], a[80:83]\n" \
"ds_read_b128 a[28:31], v248 offset:7168\n" \
"v_cvt_pk_bf16_f32 v98, v100, v101\n" \
"v_exp_f32 v112, v112\n" \
"v_mfma_f32_16x16x32_bf16 a[112:115], a[224:227], v[48:51], 0\n" \
"v_exp_f32 v113, v113\n" \
"v_mfma_f32_16x16x32_bf16 a[112:115], a[228:231], v[52:55], a[112:115]\n" \
"buffer_load_dwordx4 v234, s[36:39], s71 offen offset:2048 sc1 lds\n" \
"v_smfmac_f32_16x16x64_bf16 v[224:227], v[244:247], v[32:39], v243\n" \
"ds_read_b128 a[224:227], v232 offset:8192\n" \
"v_cvt_pk_bf16_f32 v99, v102, v103\n" \
"v_mfma_f32_16x16x32_bf16 a[116:119], a[232:235], v[48:51], 0\n" \
"ds_read_b128 a[228:231], v232 offset:10240\n" \
"v_mfma_f32_16x16x32_bf16 a[116:119], a[236:239], v[52:55], a[116:119]\n" \
"v_exp_f32 v114, v114\n" \
"v_exp_f32 v115, v115\n" \
"v_mfma_f32_16x16x32_bf16 a[84:87], a[232:235], v[32:35], 0\n" \
"v_exp_f32 v116, v116\n" \
"v_mfma_f32_16x16x32_bf16 a[84:87], a[236:239], v[36:39], a[84:87]\n" \
"buffer_load_dwordx4 v234, s[36:39], s71 offen offset:3072 sc1 lds\n" \
"v_cvt_pk_bf16_f32 v112, v112, v113\n" \
"v_mfma_f32_16x16x32_bf16 v[180:183], a[232:235], v[16:19], 0\n" \
"v_exp_f32 v117, v117\n" \
"v_mfma_f32_16x16x32_bf16 v[180:183], a[236:239], v[20:23], v[180:183]\n" \
"s_mov_b32 s76, s73\n" \
"s_add_u32 s73, s73, 34816\n" \
"ds_read_b128 a[232:235], v232 offset:12288\n" \
"v_mfma_f32_32x32x16_bf16 v[144:159], a[48:51], a[48:51], 0\n" \
"s_cmp_eq_u32 s73, s83\n" \
"s_cselect_b32 s70, 34816, 0\n" \
"ds_read_b128 a[236:239], v232 offset:14336\n" \
"v_exp_f32 v118, v118\n" \
"v_mfma_f32_16x16x32_bf16 v[184:187], a[32:35], v[16:19], 0\n" \
"s_add_u32 s73, s73, s70\n" \
"s_xor_b32 s74, s74, 0x8000\n" \
"ds_read_b128 a[248:251], v232 offset:12544\n" \
"v_mfma_f32_16x16x32_bf16 v[184:187], a[36:39], v[20:23], v[184:187]\n" \
"s_xor_b32 s77, s77, 0x8000\n" \
"ds_read_b128 a[252:255], v232 offset:14592\n" \
"v_cvt_pk_bf16_f32 v113, v114, v115\n" \
"v_mfma_f32_16x16x32_bf16 a[88:91], a[32:35], v[32:35], 0\n" \
"v_exp_f32 v119, v119\n" \
"v_mfma_f32_16x16x32_bf16 a[88:91], a[36:39], v[36:39], a[88:91]\n" \
"v_exp_f32 v64, v64\n" \
"v_cvt_pk_bf16_f32 v114, v116, v117\n" \
"v_mfma_f32_16x16x32_bf16 a[120:123], a[32:35], v[48:51], 0\n" \
"v_exp_f32 v65, v65\n" \
"v_mfma_f32_16x16x32_bf16 a[120:123], a[36:39], v[52:55], a[120:123]\n" \
"v_exp_f32 v66, v66\n" \
"v_cvt_pk_bf16_f32 v115, v118, v119\n" \
"v_smfmac_f32_16x16x64_bf16 v[228:231], v[244:247], v[48:55], v243\n" \
"v_cvt_pk_bf16_f32 v64, v64, v65\n" \
"v_mfma_f32_16x16x32_bf16 a[124:127], a[40:43], v[48:51], 0\n" \
"v_exp_f32 v67, v67\n" \
"v_mfma_f32_16x16x32_bf16 a[124:127], a[44:47], v[52:55], a[124:127]\n" \
"v_exp_f32 v68, v68\n" \
"v_exp_f32 v69, v69\n" \
"v_mfma_f32_16x16x32_bf16 a[92:95], a[40:43], v[32:35], 0\n" \
"v_exp_f32 v70, v70\n" \
"v_mfma_f32_16x16x32_bf16 a[92:95], a[44:47], v[36:39], a[92:95]\n" \
"v_cvt_pk_bf16_f32 v65, v66, v67\n" \
"v_cvt_pk_bf16_f32 v66, v68, v69\n" \
"v_mfma_f32_16x16x32_bf16 v[188:191], a[40:43], v[16:19], 0\n" \
"s_cmp_eq_u32 s63, 0\n" \
"v_exp_f32 v71, v71\n" \
"v_mfma_f32_16x16x32_bf16 v[188:191], a[44:47], v[20:23], v[188:191]\n" \
"s_waitcnt lgkmcnt(0)\n" \
"v_cvt_pk_bf16_f32 v67, v70, v71\n" \
"s_cbranch_scc1 Lloop2_%=\n" \
"Lloop_%=:\n" \
"v_mfma_f32_16x16x32_bf16 v[0:3], a[192:195], a[128:131], v[196:199]\n" \
"v_exp_f32 v72, v72\n" \
"v_mfma_f32_16x16x32_bf16 v[0:3], a[196:199], a[132:135], v[0:3]\n" \
"v_mfma_f32_16x16x32_bf16 v[0:3], a[200:203], a[136:139], v[0:3]\n" \
"v_exp_f32 v73, v73\n" \
"v_mfma_f32_16x16x32_bf16 v[0:3], a[204:207], a[140:143], v[0:3]\n" \
"ds_read_b128 a[32:35], v233 offset:6144\n" \
"v_cvt_pk_bf16_f32 v68, v72, v73\n" \
"v_mfma_f32_16x16x32_bf16 v[16:19], a[204:207], a[156:159], v[196:199]\n" \
"v_exp_f32 v74, v74\n" \
"v_mfma_f32_16x16x32_bf16 v[16:19], a[200:203], a[152:155], v[16:19]\n" \
"global_load_dword v253, v252, s[44:45]\n" \
"v_mfma_f32_16x16x32_bf16 v[16:19], a[196:199], a[148:151], v[16:19]\n" \
"s_sub_u32 s46, s46, 1\n" \
"v_exp_f32 v75, v75\n" \
"v_mfma_f32_16x16x32_bf16 v[16:19], a[192:195], a[144:147], v[16:19]\n" \
"ds_read_b128 a[36:39], v233 offset:14336\n" \
"v_cvt_pk_bf16_f32 v69, v74, v75\n" \
"v_mfma_f32_16x16x32_bf16 v[32:35], a[192:195], a[160:163], v[196:199]\n" \
"v_exp_f32 v76, v76\n" \
"v_mfma_f32_16x16x32_bf16 v[32:35], a[196:199], a[164:167], v[32:35]\n" \
"s_cmp_gt_i32 s46, 0\n" \
"s_cselect_b32 s47, 0x100000, 0\n" \
"v_mfma_f32_16x16x32_bf16 v[32:35], a[200:203], a[168:171], v[32:35]\n" \
"v_exp_f32 v77, v77\n" \
"v_mfma_f32_16x16x32_bf16 v[32:35], a[204:207], a[172:175], v[32:35]\n" \
"s_add_u32 s44, s44, s47\n" \
"s_addc_u32 s45, s45, 0\n" \
"ds_read_b128 a[40:43], v233 offset:7168\n" \
"v_cvt_pk_bf16_f32 v70, v76, v77\n" \
"v_mfma_f32_16x16x32_bf16 v[48:51], a[204:207], a[188:191], v[196:199]\n" \
"v_exp_f32 v78, v78\n" \
"v_mfma_f32_16x16x32_bf16 v[48:51], a[200:203], a[184:187], v[48:51]\n" \
"v_xor_b32 v232, 0x4000, v232\n" \
"v_mfma_f32_16x16x32_bf16 v[48:51], a[196:199], a[180:183], v[48:51]\n" \
"v_exp_f32 v79, v79\n" \
"v_mfma_f32_16x16x32_bf16 v[48:51], a[192:195], a[176:179], v[48:51]\n" \
"ds_read_b128 a[204:207], v233 offset:9216\n" \
"v_cvt_pk_bf16_f32 v71, v78, v79\n" \
"v_mfma_f32_16x16x32_bf16 v[52:55], a[208:211], a[176:179], v[196:199]\n" \
"v_exp_f32 v88, v88\n" \
"v_mfma_f32_16x16x32_bf16 v[52:55], a[212:215], a[180:183], v[52:55]\n" \
"v_mfma_f32_16x16x32_bf16 v[52:55], a[216:219], a[184:187], v[52:55]\n" \
"v_exp_f32 v89, v89\n" \
"v_mfma_f32_16x16x32_bf16 v[52:55], a[220:223], a[188:191], v[52:55]\n" \
"ds_read_b128 a[200:203], v233 offset:1024\n" \
"v_cvt_pk_bf16_f32 v84, v88, v89\n" \
"v_mfma_f32_16x16x32_bf16 v[36:39], a[220:223], a[172:175], v[196:199]\n" \
"v_exp_f32 v90, v90\n" \
"v_mfma_f32_16x16x32_bf16 v[36:39], a[216:219], a[168:171], v[36:39]\n" \
"v_mfma_f32_16x16x32_bf16 v[36:39], a[212:215], a[164:167], v[36:39]\n" \
"v_exp_f32 v91, v91\n" \
"v_mfma_f32_16x16x32_bf16 v[36:39], a[208:211], a[160:163], v[36:39]\n" \
"ds_read_b128 a[196:199], v233 offset:8192\n" \
"v_cvt_pk_bf16_f32 v85, v90, v91\n" \
"v_mfma_f32_16x16x32_bf16 v[20:23], a[208:211], a[144:147], v[196:199]\n" \
"v_exp_f32 v92, v92\n" \
"v_mfma_f32_16x16x32_bf16 v[20:23], a[212:215], a[148:151], v[20:23]\n" \
"v_mfma_f32_16x16x32_bf16 v[20:23], a[216:219], a[152:155], v[20:23]\n" \
"v_exp_f32 v93, v93\n" \
"v_mfma_f32_16x16x32_bf16 v[20:23], a[220:223], a[156:159], v[20:23]\n" \
"ds_read_b128 a[192:195], v233 offset:0\n" \
"v_cvt_pk_bf16_f32 v86, v92, v93\n" \
"v_mfma_f32_16x16x32_bf16 v[4:7], a[220:223], a[140:143], v[196:199]\n" \
"v_exp_f32 v94, v94\n" \
"v_mfma_f32_16x16x32_bf16 v[4:7], a[216:219], a[136:139], v[4:7]\n" \
"v_mfma_f32_16x16x32_bf16 v[4:7], a[212:215], a[132:135], v[4:7]\n" \
"s_waitcnt lgkmcnt(7)\n" \
"v_exp_f32 v95, v95\n" \
"v_mfma_f32_16x16x32_bf16 v[4:7], a[208:211], a[128:131], v[4:7]\n" \
"ds_read_b128 a[220:223], v233 offset:11264\n" \
"v_cvt_pk_bf16_f32 v87, v94, v95\n" \
"v_mfma_f32_16x16x32_bf16 v[8:11], a[224:227], a[128:131], v[196:199]\n" \
"v_exp_f32 v104, v104\n" \
"v_mfma_f32_16x16x32_bf16 v[8:11], a[228:231], a[132:135], v[8:11]\n" \
"v_mfma_f32_16x16x32_bf16 v[8:11], a[232:235], a[136:139], v[8:11]\n" \
"v_exp_f32 v105, v105\n" \
"v_mfma_f32_16x16x32_bf16 v[8:11], a[236:239], a[140:143], v[8:11]\n" \
"ds_read_b128 a[216:219], v233 offset:3072\n" \
"v_cvt_pk_bf16_f32 v100, v104, v105\n" \
"v_mfma_f32_16x16x32_bf16 v[24:27], a[236:239], a[156:159], v[196:199]\n" \
"v_exp_f32 v106, v106\n" \
"v_mfma_f32_16x16x32_bf16 v[24:27], a[232:235], a[152:155], v[24:27]\n" \
"v_mfma_f32_16x16x32_bf16 v[24:27], a[228:231], a[148:151], v[24:27]\n" \
"v_exp_f32 v107, v107\n" \
"v_mfma_f32_16x16x32_bf16 v[24:27], a[224:227], a[144:147], v[24:27]\n" \
"ds_read_b128 a[212:215], v233 offset:10240\n" \
"v_cvt_pk_bf16_f32 v101, v106, v107\n" \
"v_mfma_f32_16x16x32_bf16 v[40:43], a[224:227], a[160:163], v[196:199]\n" \
"v_exp_f32 v108, v108\n" \
"v_mfma_f32_16x16x32_bf16 v[40:43], a[228:231], a[164:167], v[40:43]\n" \
"v_mfma_f32_16x16x32_bf16 v[40:43], a[232:235], a[168:171], v[40:43]\n" \
"v_exp_f32 v109, v109\n" \
"v_mfma_f32_16x16x32_bf16 v[40:43], a[236:239], a[172:175], v[40:43]\n" \
"ds_read_b128 a[208:211], v233 offset:2048\n" \
"v_cvt_pk_bf16_f32 v102, v108, v109\n" \
"v_mfma_f32_16x16x32_bf16 v[56:59], a[236:239], a[188:191], v[196:199]\n" \
"v_exp_f32 v110, v110\n" \
"v_mfma_f32_16x16x32_bf16 v[56:59], a[232:235], a[184:187], v[56:59]\n" \
"v_mfma_f32_16x16x32_bf16 v[56:59], a[228:231], a[180:183], v[56:59]\n" \
"v_exp_f32 v111, v111\n" \
"v_mfma_f32_16x16x32_bf16 v[56:59], a[224:227], a[176:179], v[56:59]\n" \
"ds_read_b128 a[236:239], v233 offset:13312\n" \
"v_cvt_pk_bf16_f32 v103, v110, v111\n" \
"v_mfma_f32_16x16x32_bf16 v[60:63], a[240:243], a[176:179], v[196:199]\n" \
"v_exp_f32 v120, v120\n" \
"v_mfma_f32_16x16x32_bf16 v[60:63], a[244:247], a[180:183], v[60:63]\n" \
"v_mfma_f32_16x16x32_bf16 v[60:63], a[248:251], a[184:187], v[60:63]\n" \
"v_exp_f32 v121, v121\n" \
"v_mfma_f32_16x16x32_bf16 v[60:63], a[252:255], a[188:191], v[60:63]\n" \
"ds_read_b128 a[232:235], v233 offset:5120\n" \
"v_cvt_pk_bf16_f32 v116, v120, v121\n" \
"v_mfma_f32_16x16x32_bf16 v[44:47], a[252:255], a[172:175], v[196:199]\n" \
"v_exp_f32 v122, v122\n" \
"v_mfma_f32_16x16x32_bf16 v[44:47], a[248:251], a[168:171], v[44:47]\n" \
"v_mfma_f32_16x16x32_bf16 v[44:47], a[244:247], a[164:167], v[44:47]\n" \
"v_exp_f32 v123, v123\n" \
"v_mfma_f32_16x16x32_bf16 v[44:47], a[240:243], a[160:163], v[44:47]\n" \
"ds_read_b128 a[228:231], v233 offset:12288\n" \
"v_cvt_pk_bf16_f32 v117, v122, v123\n" \
"v_mfma_f32_16x16x32_bf16 v[28:31], a[240:243], a[144:147], v[196:199]\n" \
"v_exp_f32 v124, v124\n" \
"v_mfma_f32_16x16x32_bf16 v[28:31], a[244:247], a[148:151], v[28:31]\n" \
"v_mfma_f32_16x16x32_bf16 v[28:31], a[248:251], a[152:155], v[28:31]\n" \
"v_exp_f32 v125, v125\n" \
"v_mfma_f32_16x16x32_bf16 v[28:31], a[252:255], a[156:159], v[28:31]\n" \
"ds_read_b128 a[224:227], v233 offset:4096\n" \
"v_cvt_pk_bf16_f32 v118, v124, v125\n" \
"v_mfma_f32_16x16x32_bf16 v[12:15], a[252:255], a[140:143], v[196:199]\n" \
"v_exp_f32 v126, v126\n" \
"v_mfma_f32_16x16x32_bf16 v[12:15], a[248:251], a[136:139], v[12:15]\n" \
"v_mfma_f32_16x16x32_bf16 v[12:15], a[244:247], a[132:135], v[12:15]\n" \
"s_waitcnt lgkmcnt(2)\n" \
"v_exp_f32 v127, v127\n" \
"v_mfma_f32_16x16x32_bf16 v[12:15], a[240:243], a[128:131], v[12:15]\n" \
"ds_read_b128 a[44:47], v233 offset:15360\n" \
"v_cvt_pk_bf16_f32 v119, v126, v127\n" \
"v_mfma_f32_16x16x32_bf16 v[128:131], a[192:195], v[64:67], v[128:131]\n" \
"v_exp_f32 v0, v0\n" \
"Lpg5598_%=:\n" \
"s_cbranch_execz Lpg6780_%=\n" \
"v_mfma_f32_16x16x32_bf16 v[128:131], a[196:199], v[68:71], v[128:131]\n" \
"ds_read_b128 a[244:247], v232 offset:10496\n" \
"v_mfma_f32_16x16x32_bf16 v[160:163], a[192:195], v[80:83], v[160:163]\n" \
"v_exp_f32 v1, v1\n" \
"v_mfma_f32_16x16x32_bf16 v[160:163], a[196:199], v[84:87], v[160:163]\n" \
"ds_read_b128 a[240:243], v232 offset:8448\n" \
"v_cvt_pk_bf16_f32 v0, v0, v1\n" \
"v_mfma_f32_16x16x32_bf16 a[64:67], a[192:195], v[96:99], a[64:67]\n" \
"v_exp_f32 v2, v2\n" \
"v_mfma_f32_16x16x32_bf16 a[64:67], a[196:199], v[100:103], a[64:67]\n" \
"ds_read_b128 a[248:251], v232 offset:12544\n" \
"v_xor_b32 v233, 0xc000, v233\n" \
"v_mfma_f32_16x16x32_bf16 a[96:99], a[192:195], v[112:115], a[96:99]\n" \
"v_exp_f32 v3, v3\n" \
"v_mfma_f32_16x16x32_bf16 a[96:99], a[196:199], v[116:119], a[96:99]\n" \
"ds_read_b128 a[192:195], v232 offset:0\n" \
"v_cvt_pk_bf16_f32 v1, v2, v3\n" \
"v_mfma_f32_16x16x32_bf16 a[100:103], a[200:203], v[112:115], a[100:103]\n" \
"v_exp_f32 v4, v4\n" \
"v_mfma_f32_16x16x32_bf16 a[100:103], a[204:207], v[116:119], a[100:103]\n" \
"ds_read_b128 a[252:255], v232 offset:14592\n" \
"v_mfma_f32_16x16x32_bf16 a[68:71], a[200:203], v[96:99], a[68:71]\n" \
"v_exp_f32 v5, v5\n" \
"v_mfma_f32_16x16x32_bf16 a[68:71], a[204:207], v[100:103], a[68:71]\n" \
"ds_read_b128 a[196:199], v232 offset:2048\n" \
"v_cvt_pk_bf16_f32 v2, v4, v5\n" \
"v_smfmac_f32_16x16x64_bf16 a[48:51], v[244:247], v[64:71], v243\n" \
"v_mfma_f32_16x16x32_bf16 v[164:167], a[200:203], v[80:83], v[164:167]\n" \
"v_exp_f32 v6, v6\n" \
"v_mfma_f32_16x16x32_bf16 v[164:167], a[204:207], v[84:87], v[164:167]\n" \
"v_mfma_f32_16x16x32_bf16 v[132:135], a[200:203], v[64:67], v[132:135]\n" \
"v_exp_f32 v7, v7\n" \
"v_mfma_f32_16x16x32_bf16 v[132:135], a[204:207], v[68:71], v[132:135]\n" \
"ds_read_b128 a[200:203], v232 offset:4096\n" \
"v_cvt_pk_bf16_f32 v3, v6, v7\n" \
"v_mfma_f32_16x16x32_bf16 v[136:139], a[208:211], v[64:67], v[136:139]\n" \
"v_exp_f32 v16, v16\n" \
"v_mfma_f32_16x16x32_bf16 v[136:139], a[212:215], v[68:71], v[136:139]\n" \
"v_mfma_f32_16x16x32_bf16 v[168:171], a[208:211], v[80:83], v[168:171]\n" \
"v_exp_f32 v17, v17\n" \
"v_mfma_f32_16x16x32_bf16 v[168:171], a[212:215], v[84:87], v[168:171]\n" \
"ds_read_b128 a[204:207], v232 offset:6144\n" \
"v_cvt_pk_bf16_f32 v16, v16, v17\n" \
"v_mfma_f32_16x16x32_bf16 a[72:75], a[208:211], v[96:99], a[72:75]\n" \
"v_exp_f32 v18, v18\n" \
"v_mfma_f32_16x16x32_bf16 a[72:75], a[212:215], v[100:103], a[72:75]\n" \
"v_mfma_f32_16x16x32_bf16 a[104:107], a[208:211], v[112:115], a[104:107]\n" \
"v_exp_f32 v19, v19\n" \
"v_mfma_f32_16x16x32_bf16 a[104:107], a[212:215], v[116:119], a[104:107]\n" \
"ds_read_b128 a[208:211], v232 offset:256\n" \
"v_cvt_pk_bf16_f32 v17, v18, v19\n" \
"v_mfma_f32_16x16x32_bf16 a[108:111], a[216:219], v[112:115], a[108:111]\n" \
"v_exp_f32 v20, v20\n" \
"v_mfma_f32_16x16x32_bf16 a[108:111], a[220:223], v[116:119], a[108:111]\n" \
"v_mfma_f32_16x16x32_bf16 a[76:79], a[216:219], v[96:99], a[76:79]\n" \
"v_exp_f32 v21, v21\n" \
"v_mfma_f32_16x16x32_bf16 a[76:79], a[220:223], v[100:103], a[76:79]\n" \
"ds_read_b128 a[212:215], v232 offset:2304\n" \
"v_cvt_pk_bf16_f32 v18, v20, v21\n" \
"v_smfmac_f32_16x16x64_bf16 a[52:55], v[244:247], v[80:87], v243\n" \
"v_mfma_f32_16x16x32_bf16 v[172:175], a[216:219], v[80:83], v[172:175]\n" \
"v_exp_f32 v22, v22\n" \
"v_mfma_f32_16x16x32_bf16 v[172:175], a[220:223], v[84:87], v[172:175]\n" \
"v_mfma_f32_16x16x32_bf16 v[140:143], a[216:219], v[64:67], v[140:143]\n" \
"s_waitcnt lgkmcnt(10)\n" \
"v_exp_f32 v23, v23\n" \
"v_mfma_f32_16x16x32_bf16 v[140:143], a[220:223], v[68:71], v[140:143]\n" \
"ds_read_b128 a[216:219], v232 offset:4352\n" \
"v_cvt_pk_bf16_f32 v19, v22, v23\n" \
"v_mfma_f32_16x16x32_bf16 v[144:147], a[224:227], v[64:67], v[144:147]\n" \
"v_exp_f32 v32, v32\n" \
"v_mfma_f32_16x16x32_bf16 v[144:147], a[228:231], v[68:71], v[144:147]\n" \
"v_mfma_f32_16x16x32_bf16 v[176:179], a[224:227], v[80:83], v[176:179]\n" \
"v_exp_f32 v33, v33\n" \
"v_mfma_f32_16x16x32_bf16 v[176:179], a[228:231], v[84:87], v[176:179]\n" \
"ds_read_b128 a[220:223], v232 offset:6400\n" \
"v_cvt_pk_bf16_f32 v32, v32, v33\n" \
"v_mfma_f32_16x16x32_bf16 a[80:83], a[224:227], v[96:99], a[80:83]\n" \
"v_exp_f32 v34, v34\n" \
"v_mfma_f32_16x16x32_bf16 a[80:83], a[228:231], v[100:103], a[80:83]\n" \
"v_mfma_f32_16x16x32_bf16 a[112:115], a[224:227], v[112:115], a[112:115]\n" \
"v_exp_f32 v35, v35\n" \
"v_mfma_f32_16x16x32_bf16 a[112:115], a[228:231], v[116:119], a[112:115]\n" \
"ds_read_b128 a[224:227], v232 offset:8192\n" \
"v_cvt_pk_bf16_f32 v33, v34, v35\n" \
"v_mfma_f32_16x16x32_bf16 a[116:119], a[232:235], v[112:115], a[116:119]\n" \
"v_exp_f32 v36, v36\n" \
"v_mfma_f32_16x16x32_bf16 a[116:119], a[236:239], v[116:119], a[116:119]\n" \
"v_mfma_f32_16x16x32_bf16 a[84:87], a[232:235], v[96:99], a[84:87]\n" \
"v_exp_f32 v37, v37\n" \
"v_mfma_f32_16x16x32_bf16 a[84:87], a[236:239], v[100:103], a[84:87]\n" \
"ds_read_b128 a[228:231], v232 offset:10240\n" \
"v_cvt_pk_bf16_f32 v34, v36, v37\n" \
"v_smfmac_f32_16x16x64_bf16 v[224:227], v[244:247], v[96:103], v243\n" \
"v_mfma_f32_16x16x32_bf16 v[180:183], a[232:235], v[80:83], v[180:183]\n" \
"v_exp_f32 v38, v38\n" \
"v_mfma_f32_16x16x32_bf16 v[180:183], a[236:239], v[84:87], v[180:183]\n" \
"v_mfma_f32_16x16x32_bf16 v[148:151], a[232:235], v[64:67], v[148:151]\n" \
"v_exp_f32 v39, v39\n" \
"v_mfma_f32_16x16x32_bf16 v[148:151], a[236:239], v[68:71], v[148:151]\n" \
"ds_read_b128 a[232:235], v232 offset:12288\n" \
"v_cvt_pk_bf16_f32 v35, v38, v39\n" \
"v_mfma_f32_16x16x32_bf16 v[152:155], a[32:35], v[64:67], v[152:155]\n" \
"v_exp_f32 v48, v48\n" \
"v_mfma_f32_16x16x32_bf16 v[152:155], a[36:39], v[68:71], v[152:155]\n" \
"v_mfma_f32_16x16x32_bf16 v[184:187], a[32:35], v[80:83], v[184:187]\n" \
"v_exp_f32 v49, v49\n" \
"v_mfma_f32_16x16x32_bf16 v[184:187], a[36:39], v[84:87], v[184:187]\n" \
"ds_read_b128 a[236:239], v232 offset:14336\n" \
"v_cvt_pk_bf16_f32 v48, v48, v49\n" \
"v_mfma_f32_16x16x32_bf16 a[88:91], a[32:35], v[96:99], a[88:91]\n" \
"v_exp_f32 v50, v50\n" \
"v_mfma_f32_16x16x32_bf16 a[88:91], a[36:39], v[100:103], a[88:91]\n" \
"v_mfma_f32_16x16x32_bf16 a[120:123], a[32:35], v[112:115], a[120:123]\n" \
"v_exp_f32 v51, v51\n" \
"v_mfma_f32_16x16x32_bf16 a[120:123], a[36:39], v[116:119], a[120:123]\n" \
"v_cvt_pk_bf16_f32 v49, v50, v51\n" \
"v_mfma_f32_16x16x32_bf16 a[124:127], a[40:43], v[112:115], a[124:127]\n" \
"v_exp_f32 v52, v52\n" \
"v_mfma_f32_16x16x32_bf16 a[124:127], a[44:47], v[116:119], a[124:127]\n" \
"v_mfma_f32_16x16x32_bf16 a[92:95], a[40:43], v[96:99], a[92:95]\n" \
"v_exp_f32 v53, v53\n" \
"v_mfma_f32_16x16x32_bf16 a[92:95], a[44:47], v[100:103], a[92:95]\n" \
"v_cvt_pk_bf16_f32 v50, v52, v53\n" \
"v_smfmac_f32_16x16x64_bf16 v[228:231], v[244:247], v[112:119], v243\n" \
"v_mfma_f32_16x16x32_bf16 v[188:191], a[40:43], v[80:83], v[188:191]\n" \
"v_exp_f32 v54, v54\n" \
"v_mfma_f32_16x16x32_bf16 v[188:191], a[44:47], v[84:87], v[188:191]\n" \
"v_mfma_f32_16x16x32_bf16 v[156:159], a[40:43], v[64:67], v[156:159]\n" \
"s_waitcnt vmcnt(1) lgkmcnt(0)\n" \
"v_exp_f32 v55, v55\n" \
"v_mfma_f32_16x16x32_bf16 v[156:159], a[44:47], v[68:71], v[156:159]\n" \
"s_barrier\n" \
"v_cvt_pk_bf16_f32 v51, v54, v55\n" \
"v_mfma_f32_16x16x32_bf16 v[64:67], a[192:195], a[128:131], v[196:199]\n" \
"v_exp_f32 v8, v8\n" \
"v_mfma_f32_16x16x32_bf16 v[64:67], a[196:199], a[132:135], v[64:67]\n" \
"v_cmp_eq_u32 vcc, s60, v239\n" \
"v_mfma_f32_16x16x32_bf16 v[64:67], a[200:203], a[136:139], v[64:67]\n" \
"v_exp_f32 v9, v9\n" \
"v_mfma_f32_16x16x32_bf16 v[64:67], a[204:207], a[140:143], v[64:67]\n" \
"s_andn2_b32 s70, s69, vcc_lo\n" \
"s_cbranch_scc1 Lpl2_%=\n" \
"Lpr2_%=:\n" \
"ds_read_b128 a[32:35], v233 offset:6144\n" \
"v_cvt_pk_bf16_f32 v4, v8, v9\n" \
"v_mfma_f32_16x16x32_bf16 v[80:83], a[204:207], a[156:159], v[196:199]\n" \
"s_add_u32 m0, s77, s80\n" \
"v_exp_f32 v10, v10\n" \
"v_mfma_f32_16x16x32_bf16 v[80:83], a[200:203], a[152:155], v[80:83]\n" \
"buffer_load_dwordx4 v234, s[40:43], s76 offen offset:0 sc1 lds\n" \
"v_mfma_f32_16x16x32_bf16 v[80:83], a[196:199], a[148:151], v[80:83]\n" \
"s_sub_u32 s89, s89, 1\n" \
"v_exp_f32 v11, v11\n" \
"v_mfma_f32_16x16x32_bf16 v[80:83], a[192:195], a[144:147], v[80:83]\n" \
"s_cmp_eq_u32 s89, 0\n" \
"s_cselect_b32 s70, s78, 0xc000\n" \
"ds_read_b128 a[36:39], v233 offset:14336\n" \
"v_cvt_pk_bf16_f32 v5, v10, v11\n" \
"v_mfma_f32_16x16x32_bf16 v[96:99], a[192:195], a[160:163], v[196:199]\n" \
"s_add_u32 s71, s76, 17408\n" \
"v_exp_f32 v12, v12\n" \
"v_mfma_f32_16x16x32_bf16 v[96:99], a[196:199], a[164:167], v[96:99]\n" \
"buffer_load_dwordx4 v234, s[40:43], s76 offen offset:1024 sc1 lds\n" \
"v_mfma_f32_16x16x32_bf16 v[96:99], a[200:203], a[168:171], v[96:99]\n" \
"v_exp_f32 v13, v13\n" \
"v_mfma_f32_16x16x32_bf16 v[96:99], a[204:207], a[172:175], v[96:99]\n" \
"ds_read_b128 a[40:43], v233 offset:7168\n" \
"v_cvt_pk_bf16_f32 v6, v12, v13\n" \
"v_mfma_f32_16x16x32_bf16 v[112:115], a[204:207], a[188:191], v[196:199]\n" \
"v_exp_f32 v14, v14\n" \
"v_mfma_f32_16x16x32_bf16 v[112:115], a[200:203], a[184:187], v[112:115]\n" \
"v_xor_b32 v232, s70, v232\n" \
"v_mfma_f32_16x16x32_bf16 v[112:115], a[196:199], a[180:183], v[112:115]\n" \
"v_exp_f32 v15, v15\n" \
"v_mfma_f32_16x16x32_bf16 v[112:115], a[192:195], a[176:179], v[112:115]\n" \
"ds_read_b128 a[204:207], v233 offset:9216\n" \
"v_cvt_pk_bf16_f32 v7, v14, v15\n" \
"v_mfma_f32_16x16x32_bf16 v[116:119], a[208:211], a[176:179], v[196:199]\n" \
"v_exp_f32 v24, v24\n" \
"v_mfma_f32_16x16x32_bf16 v[116:119], a[212:215], a[180:183], v[116:119]\n" \
"buffer_load_dwordx4 v234, s[40:43], s76 offen offset:2048 sc1 lds\n" \
"v_mfma_f32_16x16x32_bf16 v[116:119], a[216:219], a[184:187], v[116:119]\n" \
"v_exp_f32 v25, v25\n" \
"v_mfma_f32_16x16x32_bf16 v[116:119], a[220:223], a[188:191], v[116:119]\n" \
"ds_read_b128 a[200:203], v233 offset:1024\n" \
"v_cvt_pk_bf16_f32 v20, v24, v25\n" \
"v_mfma_f32_16x16x32_bf16 v[100:103], a[220:223], a[172:175], v[196:199]\n" \
"v_exp_f32 v26, v26\n" \
"v_mfma_f32_16x16x32_bf16 v[100:103], a[216:219], a[168:171], v[100:103]\n" \
"buffer_load_dwordx4 v234, s[40:43], s76 offen offset:3072 sc1 lds\n" \
"v_mfma_f32_16x16x32_bf16 v[100:103], a[212:215], a[164:167], v[100:103]\n" \
"v_exp_f32 v27, v27\n" \
"v_mfma_f32_16x16x32_bf16 v[100:103], a[208:211], a[160:163], v[100:103]\n" \
"ds_read_b128 a[196:199], v233 offset:8192\n" \
"v_cvt_pk_bf16_f32 v21, v26, v27\n" \
"v_mfma_f32_16x16x32_bf16 v[84:87], a[208:211], a[144:147], v[196:199]\n" \
"v_exp_f32 v28, v28\n" \
"v_mfma_f32_16x16x32_bf16 v[84:87], a[212:215], a[148:151], v[84:87]\n" \
"v_mfma_f32_16x16x32_bf16 v[84:87], a[216:219], a[152:155], v[84:87]\n" \
"v_exp_f32 v29, v29\n" \
"v_mfma_f32_16x16x32_bf16 v[84:87], a[220:223], a[156:159], v[84:87]\n" \
"ds_read_b128 a[192:195], v233 offset:0\n" \
"v_cvt_pk_bf16_f32 v22, v28, v29\n" \
"v_mfma_f32_16x16x32_bf16 v[68:71], a[220:223], a[140:143], v[196:199]\n" \
"s_add_u32 m0, m0, 0x4000\n" \
"v_exp_f32 v30, v30\n" \
"v_mfma_f32_16x16x32_bf16 v[68:71], a[216:219], a[136:139], v[68:71]\n" \
"buffer_load_dwordx4 v234, s[40:43], s71 offen offset:0 sc1 lds\n" \
"v_mfma_f32_16x16x32_bf16 v[68:71], a[212:215], a[132:135], v[68:71]\n" \
"v_exp_f32 v31, v31\n" \
"v_mfma_f32_16x16x32_bf16 v[68:71], a[208:211], a[128:131], v[68:71]\n" \
"ds_read_b128 a[220:223], v233 offset:11264\n" \
"v_cvt_pk_bf16_f32 v23, v30, v31\n" \
"v_mfma_f32_16x16x32_bf16 v[72:75], a[224:227], a[128:131], v[196:199]\n" \
"v_exp_f32 v40, v40\n" \
"v_mfma_f32_16x16x32_bf16 v[72:75], a[228:231], a[132:135], v[72:75]\n" \
"buffer_load_dwordx4 v234, s[40:43], s71 offen offset:1024 sc1 lds\n" \
"v_mfma_f32_16x16x32_bf16 v[72:75], a[232:235], a[136:139], v[72:75]\n" \
"v_exp_f32 v41, v41\n" \
"v_mfma_f32_16x16x32_bf16 v[72:75], a[236:239], a[140:143], v[72:75]\n" \
"ds_read_b128 a[216:219], v233 offset:3072\n" \
"v_cvt_pk_bf16_f32 v36, v40, v41\n" \
"v_mfma_f32_16x16x32_bf16 v[88:91], a[236:239], a[156:159], v[196:199]\n" \
"v_exp_f32 v42, v42\n" \
"v_mfma_f32_16x16x32_bf16 v[88:91], a[232:235], a[152:155], v[88:91]\n" \
"v_mfma_f32_16x16x32_bf16 v[88:91], a[228:231], a[148:151], v[88:91]\n" \
"v_exp_f32 v43, v43\n" \
"v_mfma_f32_16x16x32_bf16 v[88:91], a[224:227], a[144:147], v[88:91]\n" \
"ds_read_b128 a[212:215], v233 offset:10240\n" \
"v_cvt_pk_bf16_f32 v37, v42, v43\n" \
"v_mfma_f32_16x16x32_bf16 v[104:107], a[224:227], a[160:163], v[196:199]\n" \
"v_exp_f32 v44, v44\n" \
"v_mfma_f32_16x16x32_bf16 v[104:107], a[228:231], a[164:167], v[104:107]\n" \
"buffer_load_dwordx4 v234, s[40:43], s71 offen offset:2048 sc1 lds\n" \
"v_mfma_f32_16x16x32_bf16 v[104:107], a[232:235], a[168:171], v[104:107]\n" \
"v_exp_f32 v45, v45\n" \
"v_mfma_f32_16x16x32_bf16 v[104:107], a[236:239], a[172:175], v[104:107]\n" \
"ds_read_b128 a[208:211], v233 offset:2048\n" \
"v_cvt_pk_bf16_f32 v38, v44, v45\n" \
"v_mfma_f32_16x16x32_bf16 v[120:123], a[236:239], a[188:191], v[196:199]\n" \
"v_exp_f32 v46, v46\n" \
"v_mfma_f32_16x16x32_bf16 v[120:123], a[232:235], a[184:187], v[120:123]\n" \
"buffer_load_dwordx4 v234, s[40:43], s71 offen offset:3072 sc1 lds\n" \
"v_mfma_f32_16x16x32_bf16 v[120:123], a[228:231], a[180:183], v[120:123]\n" \
"v_exp_f32 v47, v47\n" \
"v_mfma_f32_16x16x32_bf16 v[120:123], a[224:227], a[176:179], v[120:123]\n" \
"ds_read_b128 a[236:239], v233 offset:13312\n" \
"v_cvt_pk_bf16_f32 v39, v46, v47\n" \
"v_mfma_f32_16x16x32_bf16 v[124:127], a[240:243], a[176:179], v[196:199]\n" \
"v_exp_f32 v56, v56\n" \
"v_mfma_f32_16x16x32_bf16 v[124:127], a[244:247], a[180:183], v[124:127]\n" \
"v_mfma_f32_16x16x32_bf16 v[124:127], a[248:251], a[184:187], v[124:127]\n" \
"v_exp_f32 v57, v57\n" \
"v_mfma_f32_16x16x32_bf16 v[124:127], a[252:255], a[188:191], v[124:127]\n" \
"ds_read_b128 a[232:235], v233 offset:5120\n" \
"v_cvt_pk_bf16_f32 v52, v56, v57\n" \
"v_mfma_f32_16x16x32_bf16 v[108:111], a[252:255], a[172:175], v[196:199]\n" \
"v_exp_f32 v58, v58\n" \
"v_mfma_f32_16x16x32_bf16 v[108:111], a[248:251], a[168:171], v[108:111]\n" \
"v_mfma_f32_16x16x32_bf16 v[108:111], a[244:247], a[164:167], v[108:111]\n" \
"v_exp_f32 v59, v59\n" \
"v_mfma_f32_16x16x32_bf16 v[108:111], a[240:243], a[160:163], v[108:111]\n" \
"ds_read_b128 a[228:231], v233 offset:12288\n" \
"v_cvt_pk_bf16_f32 v53, v58, v59\n" \
"v_mfma_f32_16x16x32_bf16 v[92:95], a[240:243], a[144:147], v[196:199]\n" \
"v_exp_f32 v60, v60\n" \
"v_mfma_f32_16x16x32_bf16 v[92:95], a[244:247], a[148:151], v[92:95]\n" \
"v_mfma_f32_16x16x32_bf16 v[92:95], a[248:251], a[152:155], v[92:95]\n" \
"v_exp_f32 v61, v61\n" \
"v_mfma_f32_16x16x32_bf16 v[92:95], a[252:255], a[156:159], v[92:95]\n" \
"ds_read_b128 a[224:227], v233 offset:4096\n" \
"v_cvt_pk_bf16_f32 v54, v60, v61\n" \
"v_mfma_f32_16x16x32_bf16 v[76:79], a[252:255], a[140:143], v[196:199]\n" \
"v_exp_f32 v62, v62\n" \
"v_mfma_f32_16x16x32_bf16 v[76:79], a[248:251], a[136:139], v[76:79]\n" \
"v_mfma_f32_16x16x32_bf16 v[76:79], a[244:247], a[132:135], v[76:79]\n" \
"s_waitcnt lgkmcnt(2)\n" \
"v_exp_f32 v63, v63\n" \
"v_mfma_f32_16x16x32_bf16 v[76:79], a[240:243], a[128:131], v[76:79]\n" \
"ds_read_b128 a[44:47], v233 offset:15360\n" \
"v_cvt_pk_bf16_f32 v55, v62, v63\n" \
"v_mfma_f32_16x16x32_bf16 v[128:131], a[192:195], v[0:3], v[128:131]\n" \
"s_add_u32 m0, s74, s79\n" \
"v_exp_f32 v64, v64\n" \
"v_mfma_f32_16x16x32_bf16 v[128:131], a[196:199], v[4:7], v[128:131]\n" \
"buffer_load_dwordx4 v234, s[36:39], s73 offen offset:0 sc1 lds\n" \
"v_mfma_f32_16x16x32_bf16 v[160:163], a[192:195], v[16:19], v[160:163]\n" \
"v_exp_f32 v65, v65\n" \
"v_mfma_f32_16x16x32_bf16 v[160:163], a[196:199], v[20:23], v[160:163]\n" \
"ds_read_b128 a[240:243], v232 offset:8448\n" \
"v_cvt_pk_bf16_f32 v64, v64, v65\n" \
"v_mfma_f32_16x16x32_bf16 a[64:67], a[192:195], v[32:35], a[64:67]\n" \
"s_add_u32 s71, s73, 17408\n" \
"v_exp_f32 v66, v66\n" \
"v_mfma_f32_16x16x32_bf16 a[64:67], a[196:199], v[36:39], a[64:67]\n" \
"buffer_load_dwordx4 v234, s[36:39], s73 offen offset:1024 sc1 lds\n" \
"v_mfma_f32_16x16x32_bf16 a[96:99], a[192:195], v[48:51], a[96:99]\n" \
"v_exp_f32 v67, v67\n" \
"v_mfma_f32_16x16x32_bf16 a[96:99], a[196:199], v[52:55], a[96:99]\n" \
"ds_read_b128 a[192:195], v232 offset:0\n" \
"v_cvt_pk_bf16_f32 v65, v66, v67\n" \
"v_mfma_f32_16x16x32_bf16 a[100:103], a[200:203], v[48:51], a[100:103]\n" \
"v_exp_f32 v68, v68\n" \
"v_mfma_f32_16x16x32_bf16 a[100:103], a[204:207], v[52:55], a[100:103]\n" \
"v_xor_b32 v233, 0x4000, v233\n" \
"v_mfma_f32_16x16x32_bf16 a[68:71], a[200:203], v[32:35], a[68:71]\n" \
"v_exp_f32 v69, v69\n" \
"v_mfma_f32_16x16x32_bf16 a[68:71], a[204:207], v[36:39], a[68:71]\n" \
"ds_read_b128 a[196:199], v232 offset:2048\n" \
"v_cvt_pk_bf16_f32 v66, v68, v69\n" \
"v_smfmac_f32_16x16x64_bf16 a[48:51], v[244:247], v[0:7], v243\n" \
"buffer_load_dwordx4 v234, s[36:39], s73 offen offset:2048 sc1 lds\n" \
"v_mfma_f32_16x16x32_bf16 v[164:167], a[200:203], v[16:19], v[164:167]\n" \
"v_exp_f32 v70, v70\n" \
"v_mfma_f32_16x16x32_bf16 v[164:167], a[204:207], v[20:23], v[164:167]\n" \
"v_mfma_f32_16x16x32_bf16 v[132:135], a[200:203], v[0:3], v[132:135]\n" \
"v_exp_f32 v71, v71\n" \
"v_mfma_f32_16x16x32_bf16 v[132:135], a[204:207], v[4:7], v[132:135]\n" \
"ds_read_b128 a[200:203], v232 offset:4096\n" \
"v_cvt_pk_bf16_f32 v67, v70, v71\n" \
"v_mfma_f32_16x16x32_bf16 v[136:139], a[208:211], v[0:3], v[136:139]\n" \
"v_exp_f32 v80, v80\n" \
"v_mfma_f32_16x16x32_bf16 v[136:139], a[212:215], v[4:7], v[136:139]\n" \
"buffer_load_dwordx4 v234, s[36:39], s73 offen offset:3072 sc1 lds\n" \
"v_mfma_f32_16x16x32_bf16 v[168:171], a[208:211], v[16:19], v[168:171]\n" \
"v_exp_f32 v81, v81\n" \
"v_mfma_f32_16x16x32_bf16 v[168:171], a[212:215], v[20:23], v[168:171]\n" \
"ds_read_b128 a[204:207], v232 offset:6144\n" \
"v_cvt_pk_bf16_f32 v80, v80, v81\n" \
"v_mfma_f32_16x16x32_bf16 a[72:75], a[208:211], v[32:35], a[72:75]\n" \
"v_exp_f32 v82, v82\n" \
"v_mfma_f32_16x16x32_bf16 a[72:75], a[212:215], v[36:39], a[72:75]\n" \
"v_mfma_f32_16x16x32_bf16 a[104:107], a[208:211], v[48:51], a[104:107]\n" \
"v_exp_f32 v83, v83\n" \
"v_mfma_f32_16x16x32_bf16 a[104:107], a[212:215], v[52:55], a[104:107]\n" \
"ds_read_b128 a[208:211], v232 offset:256\n" \
"v_cvt_pk_bf16_f32 v81, v82, v83\n" \
"v_mfma_f32_16x16x32_bf16 a[108:111], a[216:219], v[48:51], a[108:111]\n" \
"s_add_u32 m0, m0, 0x4000\n" \
"v_exp_f32 v84, v84\n" \
"v_mfma_f32_16x16x32_bf16 a[108:111], a[220:223], v[52:55], a[108:111]\n" \
"buffer_load_dwordx4 v234, s[36:39], s71 offen offset:0 sc1 lds\n" \
"v_mfma_f32_16x16x32_bf16 a[76:79], a[216:219], v[32:35], a[76:79]\n" \
"v_exp_f32 v85, v85\n" \
"v_mfma_f32_16x16x32_bf16 a[76:79], a[220:223], v[36:39], a[76:79]\n" \
"ds_read_b128 a[212:215], v232 offset:2304\n" \
"v_cvt_pk_bf16_f32 v82, v84, v85\n" \
"v_smfmac_f32_16x16x64_bf16 a[52:55], v[244:247], v[16:23], v243\n" \
"v_mfma_f32_16x16x32_bf16 v[172:175], a[216:219], v[16:19], v[172:175]\n" \
"v_exp_f32 v86, v86\n" \
"v_mfma_f32_16x16x32_bf16 v[172:175], a[220:223], v[20:23], v[172:175]\n" \
"buffer_load_dwordx4 v234, s[36:39], s71 offen offset:1024 sc1 lds\n" \
"v_mfma_f32_16x16x32_bf16 v[140:143], a[216:219], v[0:3], v[140:143]\n" \
"s_waitcnt lgkmcnt(7)\n" \
"v_exp_f32 v87, v87\n" \
"v_mfma_f32_16x16x32_bf16 v[140:143], a[220:223], v[4:7], v[140:143]\n" \
"ds_read_b128 a[216:219], v232 offset:4352\n" \
"v_cvt_pk_bf16_f32 v83, v86, v87\n" \
"v_mfma_f32_16x16x32_bf16 v[144:147], a[224:227], v[0:3], v[144:147]\n" \
"v_exp_f32 v96, v96\n" \
"v_mfma_f32_16x16x32_bf16 v[144:147], a[228:231], v[4:7], v[144:147]\n" \
"buffer_load_dwordx4 v234, s[36:39], s71 offen offset:2048 sc1 lds\n" \
"v_mfma_f32_16x16x32_bf16 v[176:179], a[224:227], v[16:19], v[176:179]\n" \
"v_exp_f32 v97, v97\n" \
"v_mfma_f32_16x16x32_bf16 v[176:179], a[228:231], v[20:23], v[176:179]\n" \
"ds_read_b128 a[220:223], v232 offset:6400\n" \
"v_cvt_pk_bf16_f32 v96, v96, v97\n" \
"v_mfma_f32_16x16x32_bf16 a[80:83], a[224:227], v[32:35], a[80:83]\n" \
"v_exp_f32 v98, v98\n" \
"v_mfma_f32_16x16x32_bf16 a[80:83], a[228:231], v[36:39], a[80:83]\n" \
"v_mfma_f32_16x16x32_bf16 a[112:115], a[224:227], v[48:51], a[112:115]\n" \
"v_exp_f32 v99, v99\n" \
"v_mfma_f32_16x16x32_bf16 a[112:115], a[228:231], v[52:55], a[112:115]\n" \
"ds_read_b128 a[224:227], v232 offset:8192\n" \
"v_cvt_pk_bf16_f32 v97, v98, v99\n" \
"v_mfma_f32_16x16x32_bf16 a[116:119], a[232:235], v[48:51], a[116:119]\n" \
"v_exp_f32 v100, v100\n" \
"v_mfma_f32_16x16x32_bf16 a[116:119], a[236:239], v[52:55], a[116:119]\n" \
"buffer_load_dwordx4 v234, s[36:39], s71 offen offset:3072 sc1 lds\n" \
"v_mfma_f32_16x16x32_bf16 a[84:87], a[232:235], v[32:35], a[84:87]\n" \
"v_exp_f32 v101, v101\n" \
"v_mfma_f32_16x16x32_bf16 a[84:87], a[236:239], v[36:39], a[84:87]\n" \
"ds_read_b128 a[228:231], v232 offset:10240\n" \
"v_cvt_pk_bf16_f32 v98, v100, v101\n" \
"v_smfmac_f32_16x16x64_bf16 v[224:227], v[244:247], v[32:39], v243\n" \
"v_mfma_f32_16x16x32_bf16 v[180:183], a[232:235], v[16:19], v[180:183]\n" \
"v_exp_f32 v102, v102\n" \
"v_mfma_f32_16x16x32_bf16 v[180:183], a[236:239], v[20:23], v[180:183]\n" \
"buffer_load_dword v239, v242, s[56:59], s95 offen sc1\n" \
"v_mfma_f32_16x16x32_bf16 v[148:151], a[232:235], v[0:3], v[148:151]\n" \
"s_mov_b32 s69, s92\n" \
"v_exp_f32 v103, v103\n" \
"v_mfma_f32_16x16x32_bf16 v[148:151], a[236:239], v[4:7], v[148:151]\n" \
"s_mov_b32 s76, s73\n" \
"ds_read_b128 a[232:235], v232 offset:12288\n" \
"v_cvt_pk_bf16_f32 v99, v102, v103\n" \
"v_mfma_f32_16x16x32_bf16 v[152:155], a[32:35], v[0:3], v[152:155]\n" \
"s_add_u32 s73, s73, 34816\n" \
"v_exp_f32 v112, v112\n" \
"v_mfma_f32_16x16x32_bf16 v[152:155], a[36:39], v[4:7], v[152:155]\n" \
"s_cmp_eq_u32 s73, s83\n" \
"s_cselect_b32 s70, 34816, 0\n" \
"s_add_u32 s73, s73, s70\n" \
"v_mfma_f32_16x16x32_bf16 v[184:187], a[32:35], v[16:19], v[184:187]\n" \
"s_xor_b32 s74, s74, 0x8000\n" \
"v_exp_f32 v113, v113\n" \
"v_mfma_f32_16x16x32_bf16 v[184:187], a[36:39], v[20:23], v[184:187]\n" \
"s_xor_b32 s77, s77, 0x8000\n" \
"ds_read_b128 a[236:239], v232 offset:14336\n" \
"v_cvt_pk_bf16_f32 v112, v112, v113\n" \
"v_mfma_f32_16x16x32_bf16 a[88:91], a[32:35], v[32:35], a[88:91]\n" \
"v_exp_f32 v114, v114\n" \
"v_mfma_f32_16x16x32_bf16 a[88:91], a[36:39], v[36:39], a[88:91]\n" \
"v_mfma_f32_16x16x32_bf16 a[120:123], a[32:35], v[48:51], a[120:123]\n" \
"v_exp_f32 v115, v115\n" \
"v_mfma_f32_16x16x32_bf16 a[120:123], a[36:39], v[52:55], a[120:123]\n" \
"ds_read_b128 a[244:247], v232 offset:10496\n" \
"v_cvt_pk_bf16_f32 v113, v114, v115\n" \
"v_mfma_f32_16x16x32_bf16 a[124:127], a[40:43], v[48:51], a[124:127]\n" \
"v_exp_f32 v116, v116\n" \
"v_mfma_f32_16x16x32_bf16 a[124:127], a[44:47], v[52:55], a[124:127]\n" \
"v_mfma_f32_16x16x32_bf16 a[92:95], a[40:43], v[32:35], a[92:95]\n" \
"v_exp_f32 v117, v117\n" \
"v_mfma_f32_16x16x32_bf16 a[92:95], a[44:47], v[36:39], a[92:95]\n" \
"ds_read_b128 a[248:251], v232 offset:12544\n" \
"v_cvt_pk_bf16_f32 v114, v116, v117\n" \
"v_smfmac_f32_16x16x64_bf16 v[228:231], v[244:247], v[48:55], v243\n" \
"v_mfma_f32_16x16x32_bf16 v[188:191], a[40:43], v[16:19], v[188:191]\n" \
"v_exp_f32 v118, v118\n" \
"v_mfma_f32_16x16x32_bf16 v[188:191], a[44:47], v[20:23], v[188:191]\n" \
"s_cmp_lg_u32 s89, 0\n" \
"v_exp_f32 v119, v119\n" \
"v_mfma_f32_16x16x32_bf16 v[156:159], a[40:43], v[0:3], v[156:159]\n" \
"s_waitcnt lgkmcnt(2)\n" \
"ds_read_b128 a[252:255], v232 offset:14592\n" \
"v_cvt_pk_bf16_f32 v115, v118, v119\n" \
"v_mfma_f32_16x16x32_bf16 v[156:159], a[44:47], v[4:7], v[156:159]\n" \
"s_cbranch_scc1 Lloop_%=\n" \
"s_cmp_eq_u32 s63, 7\n" \
"s_cbranch_scc0 Lloop2_%=\n" \
"s_branch Lg_%=\n" \
"s_nop 0\n" \
"Lep_%=:\n" \
"Lpg6043_%=:\n" \
"s_cbranch_execz Lpg7360_%=\n" \
"s_nop 4\n" \
"v_accvgpr_read_b32 v92, a48\n" \
"v_accvgpr_read_b32 v93, a49\n" \
"v_accvgpr_read_b32 v94, a52\n" \
"v_accvgpr_read_b32 v95, a53\n" \
"v_accvgpr_write_b32 a48, 0\n" \
"v_accvgpr_write_b32 a49, 0\n" \
"v_accvgpr_write_b32 a52, 0\n" \
"v_accvgpr_write_b32 a53, 0\n" \
"v_add_f32 v86, v92, v93\n" \
"v_add_f32 v87, v94, v95\n" \
"v_rcp_f32 v84, v86\n" \
"v_rcp_f32 v85, v87\n" \
"v_add_u32 v240, 0x1000, v236\n" \
"v_add_f32 v80, v86, v87\n" \
"v_mul_f32 v80, 0x43800000, v80\n" \
"v_add_f32 v86, v84, v85\n" \
"v_fmac_f32 v80, 0x5f800000, v86\n" \
"v_mov_b32 v81, 0\n" \
"v_mov_b32 v82, 0\n" \
"v_mov_b32 v83, 0\n" \
"v_mov_b32 v88, 0\n" \
"v_mov_b32 v89, 0\n" \
"v_mov_b32 v90, 0\n" \
"v_mov_b32 v91, 0\n" \
"v_pk_mul_f32 v[128:129], v[128:129], v[84:85] op_sel:[0,0] op_sel_hi:[1,0]\n" \
"v_pk_mul_f32 v[130:131], v[130:131], v[84:85] op_sel:[0,0] op_sel_hi:[1,0]\n" \
"v_pk_mul_f32 v[132:133], v[132:133], v[84:85] op_sel:[0,0] op_sel_hi:[1,0]\n" \
"v_pk_mul_f32 v[134:135], v[134:135], v[84:85] op_sel:[0,0] op_sel_hi:[1,0]\n" \
"v_cvt_pk_bf16_f32 v64, v128, v129\n" \
"v_cvt_pk_bf16_f32 v65, v130, v131\n" \
"v_cvt_pk_bf16_f32 v66, v132, v133\n" \
"v_cvt_pk_bf16_f32 v67, v134, v135\n" \
"buffer_store_dwordx4 v[64:67], v236, s[56:59], s87 offen offset:0 sc1\n" \
"v_pk_mul_f32 v[136:137], v[136:137], v[84:85] op_sel:[0,0] op_sel_hi:[1,0]\n" \
"v_pk_mul_f32 v[138:139], v[138:139], v[84:85] op_sel:[0,0] op_sel_hi:[1,0]\n" \
"v_pk_mul_f32 v[140:141], v[140:141], v[84:85] op_sel:[0,0] op_sel_hi:[1,0]\n" \
"v_pk_mul_f32 v[142:143], v[142:143], v[84:85] op_sel:[0,0] op_sel_hi:[1,0]\n" \
"v_cvt_pk_bf16_f32 v68, v136, v137\n" \
"v_cvt_pk_bf16_f32 v69, v138, v139\n" \
"v_cvt_pk_bf16_f32 v70, v140, v141\n" \
"v_cvt_pk_bf16_f32 v71, v142, v143\n" \
"buffer_store_dwordx4 v[68:71], v236, s[56:59], s87 offen offset:2048 sc1\n" \
"v_pk_mul_f32 v[144:145], v[144:145], v[84:85] op_sel:[0,0] op_sel_hi:[1,0]\n" \
"v_pk_mul_f32 v[146:147], v[146:147], v[84:85] op_sel:[0,0] op_sel_hi:[1,0]\n" \
"v_pk_mul_f32 v[148:149], v[148:149], v[84:85] op_sel:[0,0] op_sel_hi:[1,0]\n" \
"v_pk_mul_f32 v[150:151], v[150:151], v[84:85] op_sel:[0,0] op_sel_hi:[1,0]\n" \
"v_cvt_pk_bf16_f32 v72, v144, v145\n" \
"v_cvt_pk_bf16_f32 v73, v146, v147\n" \
"v_cvt_pk_bf16_f32 v74, v148, v149\n" \
"v_cvt_pk_bf16_f32 v75, v150, v151\n" \
"buffer_store_dwordx4 v[72:75], v240, s[56:59], s87 offen offset:0 sc1\n" \
"v_pk_mul_f32 v[152:153], v[152:153], v[84:85] op_sel:[0,0] op_sel_hi:[1,0]\n" \
"v_pk_mul_f32 v[154:155], v[154:155], v[84:85] op_sel:[0,0] op_sel_hi:[1,0]\n" \
"v_pk_mul_f32 v[156:157], v[156:157], v[84:85] op_sel:[0,0] op_sel_hi:[1,0]\n" \
"v_pk_mul_f32 v[158:159], v[158:159], v[84:85] op_sel:[0,0] op_sel_hi:[1,0]\n" \
"v_cvt_pk_bf16_f32 v76, v152, v153\n" \
"v_cvt_pk_bf16_f32 v77, v154, v155\n" \
"v_cvt_pk_bf16_f32 v78, v156, v157\n" \
"v_cvt_pk_bf16_f32 v79, v158, v159\n" \
"buffer_store_dwordx4 v[76:79], v240, s[56:59], s87 offen offset:2048 sc1\n" \
"v_smfmac_f32_16x16x64_bf16 v[80:83], v[244:247], v[64:71], v243\n" \
"v_smfmac_f32_16x16x64_bf16 v[80:83], v[244:247], v[72:79], v243\n" \
"v_mfma_f32_32x32x16_bf16 v[128:143], v[88:91], v[88:91], 0\n" \
"v_mfma_f32_32x32x16_bf16 v[144:159], v[88:91], v[88:91], 0\n" \
"s_mov_b64 exec, 0\n" \
"s_cmp_eq_u32 s61, 0\n" \
"s_cbranch_scc1 Lst1x0_%=\n" \
"s_cmp_eq_u32 s61, 1\n" \
"s_cbranch_scc1 Lst1x1_%=\n" \
"s_cmp_eq_u32 s61, 2\n" \
"s_cbranch_scc1 Lst1x2_%=\n" \
"s_branch Lst1x3_%=\n" \
"Lcb1_%=:\n" \
"s_mov_b64 exec, -1\n" \
"s_add_u32 s72, s87, 532480\n" \
"v_pk_mul_f32 v[160:161], v[160:161], v[84:85] op_sel:[0,1] op_sel_hi:[1,1]\n" \
"v_pk_mul_f32 v[162:163], v[162:163], v[84:85] op_sel:[0,1] op_sel_hi:[1,1]\n" \
"v_pk_mul_f32 v[164:165], v[164:165], v[84:85] op_sel:[0,1] op_sel_hi:[1,1]\n" \
"v_pk_mul_f32 v[166:167], v[166:167], v[84:85] op_sel:[0,1] op_sel_hi:[1,1]\n" \
"v_cvt_pk_bf16_f32 v64, v160, v161\n" \
"v_cvt_pk_bf16_f32 v65, v162, v163\n" \
"v_cvt_pk_bf16_f32 v66, v164, v165\n" \
"v_cvt_pk_bf16_f32 v67, v166, v167\n" \
"buffer_store_dwordx4 v[64:67], v236, s[56:59], s72 offen offset:0 sc1\n" \
"v_pk_mul_f32 v[168:169], v[168:169], v[84:85] op_sel:[0,1] op_sel_hi:[1,1]\n" \
"v_pk_mul_f32 v[170:171], v[170:171], v[84:85] op_sel:[0,1] op_sel_hi:[1,1]\n" \
"v_pk_mul_f32 v[172:173], v[172:173], v[84:85] op_sel:[0,1] op_sel_hi:[1,1]\n" \
"v_pk_mul_f32 v[174:175], v[174:175], v[84:85] op_sel:[0,1] op_sel_hi:[1,1]\n" \
"v_cvt_pk_bf16_f32 v68, v168, v169\n" \
"v_cvt_pk_bf16_f32 v69, v170, v171\n" \
"v_cvt_pk_bf16_f32 v70, v172, v173\n" \
"v_cvt_pk_bf16_f32 v71, v174, v175\n" \
"buffer_store_dwordx4 v[68:71], v236, s[56:59], s72 offen offset:2048 sc1\n" \
"v_pk_mul_f32 v[176:177], v[176:177], v[84:85] op_sel:[0,1] op_sel_hi:[1,1]\n" \
"v_pk_mul_f32 v[178:179], v[178:179], v[84:85] op_sel:[0,1] op_sel_hi:[1,1]\n" \
"v_pk_mul_f32 v[180:181], v[180:181], v[84:85] op_sel:[0,1] op_sel_hi:[1,1]\n" \
"v_pk_mul_f32 v[182:183], v[182:183], v[84:85] op_sel:[0,1] op_sel_hi:[1,1]\n" \
"v_cvt_pk_bf16_f32 v72, v176, v177\n" \
"v_cvt_pk_bf16_f32 v73, v178, v179\n" \
"v_cvt_pk_bf16_f32 v74, v180, v181\n" \
"v_cvt_pk_bf16_f32 v75, v182, v183\n" \
"buffer_store_dwordx4 v[72:75], v240, s[56:59], s72 offen offset:0 sc1\n" \
"v_pk_mul_f32 v[184:185], v[184:185], v[84:85] op_sel:[0,1] op_sel_hi:[1,1]\n" \
"v_pk_mul_f32 v[186:187], v[186:187], v[84:85] op_sel:[0,1] op_sel_hi:[1,1]\n" \
"v_pk_mul_f32 v[188:189], v[188:189], v[84:85] op_sel:[0,1] op_sel_hi:[1,1]\n" \
"v_pk_mul_f32 v[190:191], v[190:191], v[84:85] op_sel:[0,1] op_sel_hi:[1,1]\n" \
"v_cvt_pk_bf16_f32 v76, v184, v185\n" \
"v_cvt_pk_bf16_f32 v77, v186, v187\n" \
"v_cvt_pk_bf16_f32 v78, v188, v189\n" \
"v_cvt_pk_bf16_f32 v79, v190, v191\n" \
"buffer_store_dwordx4 v[76:79], v240, s[56:59], s72 offen offset:2048 sc1\n" \
"v_smfmac_f32_16x16x64_bf16 v[80:83], v[244:247], v[64:71], v243\n" \
"v_smfmac_f32_16x16x64_bf16 v[80:83], v[244:247], v[72:79], v243\n" \
"v_mfma_f32_32x32x16_bf16 v[160:175], v[88:91], v[88:91], 0\n" \
"v_mfma_f32_32x32x16_bf16 v[176:191], v[88:91], v[88:91], 0\n" \
"s_nop 5\n" \
"v_add_f32 v80, v80, v81\n" \
"s_movk_i32 s64, 0x207\n" \
"v_cmp_class_f32 vcc, v80, s64\n" \
"s_nop 1\n" \
"s_cmp_lg_u64 vcc, 0\n" \
"s_addc_u32 s90, s90, 0\n" \
"s_waitcnt vmcnt(8)\n" \
"s_cmp_eq_u32 s63, 7\n" \
"s_cbranch_scc0 Lepr_%=\n" \
"s_branch Lepg_%=\n" \
".p2align 12\n" \
"Lloop2_%=:\n" \
"v_mfma_f32_16x16x32_bf16 v[0:3], a[192:195], a[0:3], v[196:199]\n" \
"Lpg6174_%=:\n" \
"s_cbranch_execz Lcb0_%=\n" \
"v_exp_f32 v72, v72\n" \
"v_mfma_f32_16x16x32_bf16 v[0:3], a[196:199], a[4:7], v[0:3]\n" \
"ds_read_b128 a[44:47], v233 offset:15360\n" \
"v_mfma_f32_16x16x32_bf16 v[0:3], a[200:203], a[8:11], v[0:3]\n" \
"v_exp_f32 v73, v73\n" \
"v_mfma_f32_16x16x32_bf16 v[0:3], a[204:207], a[12:15], v[0:3]\n" \
"ds_read_b128 a[32:35], v233 offset:6144\n" \
"v_cvt_pk_bf16_f32 v68, v72, v73\n" \
"v_mfma_f32_16x16x32_bf16 v[16:19], a[204:207], a[28:31], v[196:199]\n" \
"v_exp_f32 v74, v74\n" \
"v_mfma_f32_16x16x32_bf16 v[16:19], a[200:203], a[24:27], v[16:19]\n" \
"v_xor_b32 v232, 0x8000, v232\n" \
"v_mfma_f32_16x16x32_bf16 v[16:19], a[196:199], a[20:23], v[16:19]\n" \
"v_exp_f32 v75, v75\n" \
"v_mfma_f32_16x16x32_bf16 v[16:19], a[192:195], a[16:19], v[16:19]\n" \
"ds_read_b128 a[36:39], v233 offset:14336\n" \
"v_cvt_pk_bf16_f32 v69, v74, v75\n" \
"v_mfma_f32_16x16x32_bf16 v[32:35], a[192:195], a[160:163], v[196:199]\n" \
"v_exp_f32 v76, v76\n" \
"v_mfma_f32_16x16x32_bf16 v[32:35], a[196:199], a[164:167], v[32:35]\n" \
"v_mfma_f32_16x16x32_bf16 v[32:35], a[200:203], a[168:171], v[32:35]\n" \
"v_exp_f32 v77, v77\n" \
"v_mfma_f32_16x16x32_bf16 v[32:35], a[204:207], a[172:175], v[32:35]\n" \
"ds_read_b128 a[40:43], v233 offset:7168\n" \
"v_cvt_pk_bf16_f32 v70, v76, v77\n" \
"v_mfma_f32_16x16x32_bf16 v[48:51], a[204:207], a[188:191], v[196:199]\n" \
"v_exp_f32 v78, v78\n" \
"v_mfma_f32_16x16x32_bf16 v[48:51], a[200:203], a[184:187], v[48:51]\n" \
"v_mfma_f32_16x16x32_bf16 v[48:51], a[196:199], a[180:183], v[48:51]\n" \
"v_exp_f32 v79, v79\n" \
"v_mfma_f32_16x16x32_bf16 v[48:51], a[192:195], a[176:179], v[48:51]\n" \
"ds_read_b128 a[204:207], v233 offset:9216\n" \
"v_cvt_pk_bf16_f32 v71, v78, v79\n" \
"v_mfma_f32_16x16x32_bf16 v[52:55], a[208:211], a[176:179], v[196:199]\n" \
"v_exp_f32 v88, v88\n" \
"v_mfma_f32_16x16x32_bf16 v[52:55], a[212:215], a[180:183], v[52:55]\n" \
"v_mfma_f32_16x16x32_bf16 v[52:55], a[216:219], a[184:187], v[52:55]\n" \
"v_exp_f32 v89, v89\n" \
"v_mfma_f32_16x16x32_bf16 v[52:55], a[220:223], a[188:191], v[52:55]\n" \
"ds_read_b128 a[200:203], v233 offset:1024\n" \
"v_cvt_pk_bf16_f32 v84, v88, v89\n" \
"v_mfma_f32_16x16x32_bf16 v[36:39], a[220:223], a[172:175], v[196:199]\n" \
"v_exp_f32 v90, v90\n" \
"v_mfma_f32_16x16x32_bf16 v[36:39], a[216:219], a[168:171], v[36:39]\n" \
"v_mfma_f32_16x16x32_bf16 v[36:39], a[212:215], a[164:167], v[36:39]\n" \
"v_exp_f32 v91, v91\n" \
"v_mfma_f32_16x16x32_bf16 v[36:39], a[208:211], a[160:163], v[36:39]\n" \
"ds_read_b128 a[196:199], v233 offset:8192\n" \
"v_cvt_pk_bf16_f32 v85, v90, v91\n" \
"v_mfma_f32_16x16x32_bf16 v[20:23], a[208:211], a[16:19], v[196:199]\n" \
"v_exp_f32 v92, v92\n" \
"v_mfma_f32_16x16x32_bf16 v[20:23], a[212:215], a[20:23], v[20:23]\n" \
"v_mfma_f32_16x16x32_bf16 v[20:23], a[216:219], a[24:27], v[20:23]\n" \
"v_exp_f32 v93, v93\n" \
"v_mfma_f32_16x16x32_bf16 v[20:23], a[220:223], a[28:31], v[20:23]\n" \
"ds_read_b128 a[192:195], v233 offset:0\n" \
"v_cvt_pk_bf16_f32 v86, v92, v93\n" \
"v_mfma_f32_16x16x32_bf16 v[4:7], a[220:223], a[12:15], v[196:199]\n" \
"v_exp_f32 v94, v94\n" \
"v_mfma_f32_16x16x32_bf16 v[4:7], a[216:219], a[8:11], v[4:7]\n" \
"v_mfma_f32_16x16x32_bf16 v[4:7], a[212:215], a[4:7], v[4:7]\n" \
"s_waitcnt lgkmcnt(8)\n" \
"v_exp_f32 v95, v95\n" \
"v_mfma_f32_16x16x32_bf16 v[4:7], a[208:211], a[0:3], v[4:7]\n" \
"ds_read_b128 a[220:223], v233 offset:11264\n" \
"v_cvt_pk_bf16_f32 v87, v94, v95\n" \
"v_mfma_f32_16x16x32_bf16 v[8:11], a[224:227], a[0:3], v[196:199]\n" \
"v_exp_f32 v104, v104\n" \
"v_mfma_f32_16x16x32_bf16 v[8:11], a[228:231], a[4:7], v[8:11]\n" \
"v_mfma_f32_16x16x32_bf16 v[8:11], a[232:235], a[8:11], v[8:11]\n" \
"v_exp_f32 v105, v105\n" \
"v_mfma_f32_16x16x32_bf16 v[8:11], a[236:239], a[12:15], v[8:11]\n" \
"ds_read_b128 a[216:219], v233 offset:3072\n" \
"v_cvt_pk_bf16_f32 v100, v104, v105\n" \
"v_mfma_f32_16x16x32_bf16 v[24:27], a[236:239], a[28:31], v[196:199]\n" \
"v_exp_f32 v106, v106\n" \
"v_mfma_f32_16x16x32_bf16 v[24:27], a[232:235], a[24:27], v[24:27]\n" \
"v_mfma_f32_16x16x32_bf16 v[24:27], a[228:231], a[20:23], v[24:27]\n" \
"v_exp_f32 v107, v107\n" \
"v_mfma_f32_16x16x32_bf16 v[24:27], a[224:227], a[16:19], v[24:27]\n" \
"ds_read_b128 a[212:215], v233 offset:10240\n" \
"v_cvt_pk_bf16_f32 v101, v106, v107\n" \
"v_mfma_f32_16x16x32_bf16 v[40:43], a[224:227], a[160:163], v[196:199]\n" \
"v_exp_f32 v108, v108\n" \
"v_mfma_f32_16x16x32_bf16 v[40:43], a[228:231], a[164:167], v[40:43]\n" \
"v_mfma_f32_16x16x32_bf16 v[40:43], a[232:235], a[168:171], v[40:43]\n" \
"v_exp_f32 v109, v109\n" \
"v_mfma_f32_16x16x32_bf16 v[40:43], a[236:239], a[172:175], v[40:43]\n" \
"ds_read_b128 a[208:211], v233 offset:2048\n" \
"v_cvt_pk_bf16_f32 v102, v108, v109\n" \
"v_mfma_f32_16x16x32_bf16 v[56:59], a[236:239], a[188:191], v[196:199]\n" \
"v_exp_f32 v110, v110\n" \
"v_mfma_f32_16x16x32_bf16 v[56:59], a[232:235], a[184:187], v[56:59]\n" \
"v_mfma_f32_16x16x32_bf16 v[56:59], a[228:231], a[180:183], v[56:59]\n" \
"v_exp_f32 v111, v111\n" \
"v_mfma_f32_16x16x32_bf16 v[56:59], a[224:227], a[176:179], v[56:59]\n" \
"ds_read_b128 a[236:239], v233 offset:13312\n" \
"v_cvt_pk_bf16_f32 v103, v110, v111\n" \
"v_mfma_f32_16x16x32_bf16 v[60:63], a[240:243], a[176:179], v[196:199]\n" \
"v_exp_f32 v120, v120\n" \
"v_mfma_f32_16x16x32_bf16 v[60:63], a[244:247], a[180:183], v[60:63]\n" \
"ds_read_b128 a[232:235], v233 offset:5120\n" \
"v_mfma_f32_16x16x32_bf16 v[60:63], a[248:251], a[184:187], v[60:63]\n" \
"v_exp_f32 v121, v121\n" \
"v_mfma_f32_16x16x32_bf16 v[60:63], a[252:255], a[188:191], v[60:63]\n" \
"ds_read_b128 a[228:231], v233 offset:12288\n" \
"v_cvt_pk_bf16_f32 v116, v120, v121\n" \
"v_mfma_f32_16x16x32_bf16 v[44:47], a[252:255], a[172:175], v[196:199]\n" \
"v_exp_f32 v122, v122\n" \
"v_mfma_f32_16x16x32_bf16 v[44:47], a[248:251], a[168:171], v[44:47]\n" \
"ds_read_b128 a[224:227], v233 offset:4096\n" \
"v_mfma_f32_16x16x32_bf16 v[44:47], a[244:247], a[164:167], v[44:47]\n" \
"v_exp_f32 v123, v123\n" \
"v_mfma_f32_16x16x32_bf16 v[44:47], a[240:243], a[160:163], v[44:47]\n" \
"v_cvt_pk_bf16_f32 v117, v122, v123\n" \
"v_mfma_f32_16x16x32_bf16 v[28:31], a[240:243], a[16:19], v[196:199]\n" \
"v_exp_f32 v124, v124\n" \
"v_mfma_f32_16x16x32_bf16 v[28:31], a[244:247], a[20:23], v[28:31]\n" \
"v_mfma_f32_16x16x32_bf16 v[28:31], a[248:251], a[24:27], v[28:31]\n" \
"v_exp_f32 v125, v125\n" \
"v_mfma_f32_16x16x32_bf16 v[28:31], a[252:255], a[28:31], v[28:31]\n" \
"v_cvt_pk_bf16_f32 v118, v124, v125\n" \
"v_mfma_f32_16x16x32_bf16 v[12:15], a[252:255], a[12:15], v[196:199]\n" \
"v_exp_f32 v126, v126\n" \
"v_mfma_f32_16x16x32_bf16 v[12:15], a[248:251], a[8:11], v[12:15]\n" \
"v_mfma_f32_16x16x32_bf16 v[12:15], a[244:247], a[4:7], v[12:15]\n" \
"s_waitcnt vmcnt(0) lgkmcnt(0)\n" \
"v_exp_f32 v127, v127\n" \
"v_mfma_f32_16x16x32_bf16 v[12:15], a[240:243], a[0:3], v[12:15]\n" \
"s_barrier\n" \
"v_cvt_pk_bf16_f32 v119, v126, v127\n" \
"v_mfma_f32_16x16x32_bf16 v[128:131], a[192:195], v[64:67], v[128:131]\n" \
"v_exp_f32 v0, v0\n" \
"v_mfma_f32_16x16x32_bf16 v[128:131], a[196:199], v[68:71], v[128:131]\n" \
"v_cmp_eq_u32 vcc, s60, v239\n" \
"v_mfma_f32_16x16x32_bf16 v[160:163], a[192:195], v[80:83], v[160:163]\n" \
"v_exp_f32 v1, v1\n" \
"v_mfma_f32_16x16x32_bf16 v[160:163], a[196:199], v[84:87], v[160:163]\n" \
"s_andn2_b32 s70, s69, vcc_lo\n" \
"s_cbranch_scc1 Lpl3_%=\n" \
"Lpr3_%=:\n" \
"ds_read_b128 a[240:243], v232 offset:8448\n" \
"v_cvt_pk_bf16_f32 v0, v0, v1\n" \
"v_mfma_f32_16x16x32_bf16 a[64:67], a[192:195], v[96:99], a[64:67]\n" \
"s_add_u32 m0, s74, s79\n" \
"v_exp_f32 v2, v2\n" \
"v_mfma_f32_16x16x32_bf16 a[64:67], a[196:199], v[100:103], a[64:67]\n" \
"buffer_load_dwordx4 v234, s[36:39], s73 offen offset:0 sc1 lds\n" \
"v_mfma_f32_16x16x32_bf16 a[96:99], a[192:195], v[112:115], a[96:99]\n" \
"v_exp_f32 v3, v3\n" \
"v_mfma_f32_16x16x32_bf16 a[96:99], a[196:199], v[116:119], a[96:99]\n" \
"ds_read_b128 a[192:195], v232 offset:0\n" \
"v_cvt_pk_bf16_f32 v1, v2, v3\n" \
"v_mfma_f32_16x16x32_bf16 a[100:103], a[200:203], v[112:115], a[100:103]\n" \
"s_add_u32 s71, s73, 17408\n" \
"v_exp_f32 v4, v4\n" \
"v_mfma_f32_16x16x32_bf16 a[100:103], a[204:207], v[116:119], a[100:103]\n" \
"buffer_load_dwordx4 v234, s[36:39], s73 offen offset:1024 sc1 lds\n" \
"v_mfma_f32_16x16x32_bf16 a[68:71], a[200:203], v[96:99], a[68:71]\n" \
"v_exp_f32 v5, v5\n" \
"v_mfma_f32_16x16x32_bf16 a[68:71], a[204:207], v[100:103], a[68:71]\n" \
"ds_read_b128 a[196:199], v232 offset:2048\n" \
"v_cvt_pk_bf16_f32 v2, v4, v5\n" \
"v_smfmac_f32_16x16x64_bf16 a[48:51], v[244:247], v[64:71], v243\n" \
"buffer_load_dwordx4 v234, s[36:39], s73 offen offset:2048 sc1 lds\n" \
"v_mfma_f32_16x16x32_bf16 v[164:167], a[200:203], v[80:83], v[164:167]\n" \
"v_exp_f32 v6, v6\n" \
"v_mfma_f32_16x16x32_bf16 v[164:167], a[204:207], v[84:87], v[164:167]\n" \
"buffer_load_dwordx4 v234, s[36:39], s73 offen offset:3072 sc1 lds\n" \
"v_mfma_f32_16x16x32_bf16 v[132:135], a[200:203], v[64:67], v[132:135]\n" \
"v_exp_f32 v7, v7\n" \
"v_mfma_f32_16x16x32_bf16 v[132:135], a[204:207], v[68:71], v[132:135]\n" \
"ds_read_b128 a[200:203], v232 offset:4096\n" \
"v_cvt_pk_bf16_f32 v3, v6, v7\n" \
"v_mfma_f32_16x16x32_bf16 v[136:139], a[208:211], v[64:67], v[136:139]\n" \
"s_add_u32 m0, m0, 0x4000\n" \
"v_exp_f32 v16, v16\n" \
"v_mfma_f32_16x16x32_bf16 v[136:139], a[212:215], v[68:71], v[136:139]\n" \
"buffer_load_dwordx4 v234, s[36:39], s71 offen offset:0 sc1 lds\n" \
"v_mfma_f32_16x16x32_bf16 v[168:171], a[208:211], v[80:83], v[168:171]\n" \
"v_exp_f32 v17, v17\n" \
"v_mfma_f32_16x16x32_bf16 v[168:171], a[212:215], v[84:87], v[168:171]\n" \
"ds_read_b128 a[204:207], v232 offset:6144\n" \
"v_cvt_pk_bf16_f32 v16, v16, v17\n" \
"v_mfma_f32_16x16x32_bf16 a[72:75], a[208:211], v[96:99], a[72:75]\n" \
"v_exp_f32 v18, v18\n" \
"v_mfma_f32_16x16x32_bf16 a[72:75], a[212:215], v[100:103], a[72:75]\n" \
"buffer_load_dwordx4 v234, s[36:39], s71 offen offset:1024 sc1 lds\n" \
"v_mfma_f32_16x16x32_bf16 a[104:107], a[208:211], v[112:115], a[104:107]\n" \
"v_exp_f32 v19, v19\n" \
"v_mfma_f32_16x16x32_bf16 a[104:107], a[212:215], v[116:119], a[104:107]\n" \
"ds_read_b128 a[208:211], v232 offset:256\n" \
"v_cvt_pk_bf16_f32 v17, v18, v19\n" \
"v_mfma_f32_16x16x32_bf16 a[108:111], a[216:219], v[112:115], a[108:111]\n" \
"v_exp_f32 v20, v20\n" \
"v_mfma_f32_16x16x32_bf16 a[108:111], a[220:223], v[116:119], a[108:111]\n" \
"buffer_load_dwordx4 v234, s[36:39], s71 offen offset:2048 sc1 lds\n" \
"v_mfma_f32_16x16x32_bf16 a[76:79], a[216:219], v[96:99], a[76:79]\n" \
"v_exp_f32 v21, v21\n" \
"v_mfma_f32_16x16x32_bf16 a[76:79], a[220:223], v[100:103], a[76:79]\n" \
"ds_read_b128 a[212:215], v232 offset:2304\n" \
"v_cvt_pk_bf16_f32 v18, v20, v21\n" \
"v_smfmac_f32_16x16x64_bf16 a[52:55], v[244:247], v[80:87], v243\n" \
"buffer_load_dwordx4 v234, s[36:39], s71 offen offset:3072 sc1 lds\n" \
"v_mfma_f32_16x16x32_bf16 v[172:175], a[216:219], v[80:83], v[172:175]\n" \
"s_add_u32 m0, s77, s80\n" \
"v_exp_f32 v22, v22\n" \
"v_mfma_f32_16x16x32_bf16 v[172:175], a[220:223], v[84:87], v[172:175]\n" \
"buffer_load_dwordx4 v234, s[40:43], s76 offen offset:0 sc1 lds\n" \
"v_mfma_f32_16x16x32_bf16 v[140:143], a[216:219], v[64:67], v[140:143]\n" \
"v_exp_f32 v23, v23\n" \
"v_mfma_f32_16x16x32_bf16 v[140:143], a[220:223], v[68:71], v[140:143]\n" \
"ds_read_b128 a[216:219], v232 offset:4352\n" \
"v_cvt_pk_bf16_f32 v19, v22, v23\n" \
"v_mfma_f32_16x16x32_bf16 v[144:147], a[224:227], v[64:67], v[144:147]\n" \
"s_add_u32 s71, s76, 17408\n" \
"v_exp_f32 v32, v32\n" \
"v_mfma_f32_16x16x32_bf16 v[144:147], a[228:231], v[68:71], v[144:147]\n" \
"buffer_load_dwordx4 v234, s[40:43], s76 offen offset:1024 sc1 lds\n" \
"v_mfma_f32_16x16x32_bf16 v[176:179], a[224:227], v[80:83], v[176:179]\n" \
"v_exp_f32 v33, v33\n" \
"v_mfma_f32_16x16x32_bf16 v[176:179], a[228:231], v[84:87], v[176:179]\n" \
"ds_read_b128 a[220:223], v232 offset:6400\n" \
"v_cvt_pk_bf16_f32 v32, v32, v33\n" \
"v_mfma_f32_16x16x32_bf16 a[80:83], a[224:227], v[96:99], a[80:83]\n" \
"v_exp_f32 v34, v34\n" \
"v_mfma_f32_16x16x32_bf16 a[80:83], a[228:231], v[100:103], a[80:83]\n" \
"buffer_load_dwordx4 v234, s[40:43], s76 offen offset:2048 sc1 lds\n" \
"v_mfma_f32_16x16x32_bf16 a[112:115], a[224:227], v[112:115], a[112:115]\n" \
"v_exp_f32 v35, v35\n" \
"v_mfma_f32_16x16x32_bf16 a[112:115], a[228:231], v[116:119], a[112:115]\n" \
"ds_read_b128 a[224:227], v232 offset:8192\n" \
"v_cvt_pk_bf16_f32 v33, v34, v35\n" \
"v_mfma_f32_16x16x32_bf16 a[116:119], a[232:235], v[112:115], a[116:119]\n" \
"v_exp_f32 v36, v36\n" \
"v_mfma_f32_16x16x32_bf16 a[116:119], a[236:239], v[116:119], a[116:119]\n" \
"buffer_load_dwordx4 v234, s[40:43], s76 offen offset:3072 sc1 lds\n" \
"v_mfma_f32_16x16x32_bf16 a[84:87], a[232:235], v[96:99], a[84:87]\n" \
"v_exp_f32 v37, v37\n" \
"v_mfma_f32_16x16x32_bf16 a[84:87], a[236:239], v[100:103], a[84:87]\n" \
"s_add_u32 m0, m0, 0x4000\n" \
"ds_read_b128 a[228:231], v232 offset:10240\n" \
"v_cvt_pk_bf16_f32 v34, v36, v37\n" \
"v_smfmac_f32_16x16x64_bf16 v[224:227], v[244:247], v[96:103], v243\n" \
"buffer_load_dwordx4 v234, s[40:43], s71 offen offset:0 sc1 lds\n" \
"v_mfma_f32_16x16x32_bf16 v[180:183], a[232:235], v[80:83], v[180:183]\n" \
"v_exp_f32 v38, v38\n" \
"v_mfma_f32_16x16x32_bf16 v[180:183], a[236:239], v[84:87], v[180:183]\n" \
"buffer_load_dwordx4 v234, s[40:43], s71 offen offset:1024 sc1 lds\n" \
"v_mfma_f32_16x16x32_bf16 v[148:151], a[232:235], v[64:67], v[148:151]\n" \
"v_exp_f32 v39, v39\n" \
"v_mfma_f32_16x16x32_bf16 v[148:151], a[236:239], v[68:71], v[148:151]\n" \
"ds_read_b128 a[232:235], v232 offset:12288\n" \
"v_cvt_pk_bf16_f32 v35, v38, v39\n" \
"v_mfma_f32_16x16x32_bf16 v[152:155], a[32:35], v[64:67], v[152:155]\n" \
"v_exp_f32 v48, v48\n" \
"v_mfma_f32_16x16x32_bf16 v[152:155], a[36:39], v[68:71], v[152:155]\n" \
"buffer_load_dwordx4 v234, s[40:43], s71 offen offset:2048 sc1 lds\n" \
"v_mfma_f32_16x16x32_bf16 v[184:187], a[32:35], v[80:83], v[184:187]\n" \
"v_exp_f32 v49, v49\n" \
"v_mfma_f32_16x16x32_bf16 v[184:187], a[36:39], v[84:87], v[184:187]\n" \
"ds_read_b128 a[236:239], v232 offset:14336\n" \
"v_cvt_pk_bf16_f32 v48, v48, v49\n" \
"v_mfma_f32_16x16x32_bf16 a[88:91], a[32:35], v[96:99], a[88:91]\n" \
"v_exp_f32 v50, v50\n" \
"v_mfma_f32_16x16x32_bf16 a[88:91], a[36:39], v[100:103], a[88:91]\n" \
"buffer_load_dwordx4 v234, s[40:43], s71 offen offset:3072 sc1 lds\n" \
"v_mfma_f32_16x16x32_bf16 a[120:123], a[32:35], v[112:115], a[120:123]\n" \
"s_mov_b32 s76, s73\n" \
"v_exp_f32 v51, v51\n" \
"v_mfma_f32_16x16x32_bf16 a[120:123], a[36:39], v[116:119], a[120:123]\n" \
"s_add_u32 s73, s73, 34816\n" \
"ds_read_b128 a[244:247], v232 offset:10496\n" \
"v_cvt_pk_bf16_f32 v49, v50, v51\n" \
"v_mfma_f32_16x16x32_bf16 a[124:127], a[40:43], v[112:115], a[124:127]\n" \
"s_xor_b32 s74, s74, 0x8000\n" \
"v_exp_f32 v52, v52\n" \
"v_mfma_f32_16x16x32_bf16 a[124:127], a[44:47], v[116:119], a[124:127]\n" \
"s_xor_b32 s77, s77, 0x8000\n" \
"v_xor_b32 v233, s81, v233\n" \
"s_mov_b32 s81, 0x8000\n" \
"v_mfma_f32_16x16x32_bf16 a[92:95], a[40:43], v[96:99], a[92:95]\n" \
"v_exp_f32 v53, v53\n" \
"v_mfma_f32_16x16x32_bf16 a[92:95], a[44:47], v[100:103], a[92:95]\n" \
"ds_read_b128 a[248:251], v232 offset:12544\n" \
"v_cvt_pk_bf16_f32 v50, v52, v53\n" \
"v_smfmac_f32_16x16x64_bf16 v[228:231], v[244:247], v[112:119], v243\n" \
"v_mfma_f32_16x16x32_bf16 v[188:191], a[40:43], v[80:83], v[188:191]\n" \
"v_exp_f32 v54, v54\n" \
"v_mfma_f32_16x16x32_bf16 v[188:191], a[44:47], v[84:87], v[188:191]\n" \
"s_cmp_lg_u32 s82, 0\n" \
"v_exp_f32_e64 v55, v55\n" \
"v_mfma_f32_16x16x32_bf16 v[156:159], a[40:43], v[64:67], v[156:159]\n" \
"s_waitcnt lgkmcnt(2)\n" \
"ds_read_b128 a[252:255], v232 offset:14592\n" \
"v_cvt_pk_bf16_f32 v51, v54, v55\n" \
"v_mfma_f32_16x16x32_bf16 v[156:159], a[44:47], v[68:71], v[156:159]\n" \
"s_cbranch_scc1 Lep_%=\n" \
"Lepr_%=:\n" \
"v_mfma_f32_16x16x32_bf16 v[64:67], a[192:195], a[0:3], v[196:199]\n" \
"v_exp_f32 v8, v8\n" \
"v_mfma_f32_16x16x32_bf16 v[64:67], a[196:199], a[4:7], v[64:67]\n" \
"ds_read_b128 a[44:47], v233 offset:15360\n" \
"v_mfma_f32_16x16x32_bf16 v[64:67], a[200:203], a[8:11], v[64:67]\n" \
"v_exp_f32 v9, v9\n" \
"v_mfma_f32_16x16x32_bf16 v[64:67], a[204:207], a[12:15], v[64:67]\n" \
"ds_read_b128 a[32:35], v233 offset:6144\n" \
"v_cvt_pk_bf16_f32 v4, v8, v9\n" \
"v_mfma_f32_16x16x32_bf16 v[80:83], a[204:207], a[28:31], v[196:199]\n" \
"s_sub_u32 s84, s84, 1\n" \
"v_exp_f32 v10, v10\n" \
"v_mfma_f32_16x16x32_bf16 v[80:83], a[200:203], a[24:27], v[80:83]\n" \
"s_cmp_eq_u32 s84, 0\n" \
"s_cselect_b32 s70, s75, 0x8000\n" \
"v_xor_b32 v232, s70, v232\n" \
"v_mfma_f32_16x16x32_bf16 v[80:83], a[196:199], a[20:23], v[80:83]\n" \
"v_exp_f32 v11, v11\n" \
"v_mfma_f32_16x16x32_bf16 v[80:83], a[192:195], a[16:19], v[80:83]\n" \
"ds_read_b128 a[36:39], v233 offset:14336\n" \
"v_cvt_pk_bf16_f32 v5, v10, v11\n" \
"v_mfma_f32_16x16x32_bf16 v[96:99], a[192:195], a[160:163], v[196:199]\n" \
"v_exp_f32 v12, v12\n" \
"v_mfma_f32_16x16x32_bf16 v[96:99], a[196:199], a[164:167], v[96:99]\n" \
"v_mfma_f32_16x16x32_bf16 v[96:99], a[200:203], a[168:171], v[96:99]\n" \
"v_exp_f32 v13, v13\n" \
"v_mfma_f32_16x16x32_bf16 v[96:99], a[204:207], a[172:175], v[96:99]\n" \
"ds_read_b128 a[40:43], v233 offset:7168\n" \
"v_cvt_pk_bf16_f32 v6, v12, v13\n" \
"v_mfma_f32_16x16x32_bf16 v[112:115], a[204:207], a[188:191], v[196:199]\n" \
"v_exp_f32 v14, v14\n" \
"v_mfma_f32_16x16x32_bf16 v[112:115], a[200:203], a[184:187], v[112:115]\n" \
"v_mfma_f32_16x16x32_bf16 v[112:115], a[196:199], a[180:183], v[112:115]\n" \
"v_exp_f32 v15, v15\n" \
"v_mfma_f32_16x16x32_bf16 v[112:115], a[192:195], a[176:179], v[112:115]\n" \
"ds_read_b128 a[204:207], v233 offset:9216\n" \
"v_cvt_pk_bf16_f32 v7, v14, v15\n" \
"v_mfma_f32_16x16x32_bf16 v[116:119], a[208:211], a[176:179], v[196:199]\n" \
"v_exp_f32 v24, v24\n" \
"v_mfma_f32_16x16x32_bf16 v[116:119], a[212:215], a[180:183], v[116:119]\n" \
"v_mfma_f32_16x16x32_bf16 v[116:119], a[216:219], a[184:187], v[116:119]\n" \
"v_exp_f32 v25, v25\n" \
"v_mfma_f32_16x16x32_bf16 v[116:119], a[220:223], a[188:191], v[116:119]\n" \
"ds_read_b128 a[200:203], v233 offset:1024\n" \
"v_cvt_pk_bf16_f32 v20, v24, v25\n" \
"v_mfma_f32_16x16x32_bf16 v[100:103], a[220:223], a[172:175], v[196:199]\n" \
"v_exp_f32 v26, v26\n" \
"v_mfma_f32_16x16x32_bf16 v[100:103], a[216:219], a[168:171], v[100:103]\n" \
"v_mfma_f32_16x16x32_bf16 v[100:103], a[212:215], a[164:167], v[100:103]\n" \
"v_exp_f32 v27, v27\n" \
"v_mfma_f32_16x16x32_bf16 v[100:103], a[208:211], a[160:163], v[100:103]\n" \
"ds_read_b128 a[196:199], v233 offset:8192\n" \
"v_cvt_pk_bf16_f32 v21, v26, v27\n" \
"v_mfma_f32_16x16x32_bf16 v[84:87], a[208:211], a[16:19], v[196:199]\n" \
"v_exp_f32 v28, v28\n" \
"v_mfma_f32_16x16x32_bf16 v[84:87], a[212:215], a[20:23], v[84:87]\n" \
"v_mfma_f32_16x16x32_bf16 v[84:87], a[216:219], a[24:27], v[84:87]\n" \
"v_exp_f32 v29, v29\n" \
"v_mfma_f32_16x16x32_bf16 v[84:87], a[220:223], a[28:31], v[84:87]\n" \
"ds_read_b128 a[192:195], v233 offset:0\n" \
"v_cvt_pk_bf16_f32 v22, v28, v29\n" \
"v_mfma_f32_16x16x32_bf16 v[68:71], a[220:223], a[12:15], v[196:199]\n" \
"v_exp_f32 v30, v30\n" \
"v_mfma_f32_16x16x32_bf16 v[68:71], a[216:219], a[8:11], v[68:71]\n" \
"v_mfma_f32_16x16x32_bf16 v[68:71], a[212:215], a[4:7], v[68:71]\n" \
"s_waitcnt lgkmcnt(8)\n" \
"v_exp_f32 v31, v31\n" \
"v_mfma_f32_16x16x32_bf16 v[68:71], a[208:211], a[0:3], v[68:71]\n" \
"ds_read_b128 a[220:223], v233 offset:11264\n" \
"v_cvt_pk_bf16_f32 v23, v30, v31\n" \
"v_mfma_f32_16x16x32_bf16 v[72:75], a[224:227], a[0:3], v[196:199]\n" \
"v_exp_f32 v40, v40\n" \
"v_mfma_f32_16x16x32_bf16 v[72:75], a[228:231], a[4:7], v[72:75]\n" \
"v_mfma_f32_16x16x32_bf16 v[72:75], a[232:235], a[8:11], v[72:75]\n" \
"v_exp_f32 v41, v41\n" \
"v_mfma_f32_16x16x32_bf16 v[72:75], a[236:239], a[12:15], v[72:75]\n" \
"ds_read_b128 a[216:219], v233 offset:3072\n" \
"v_cvt_pk_bf16_f32 v36, v40, v41\n" \
"v_mfma_f32_16x16x32_bf16 v[88:91], a[236:239], a[28:31], v[196:199]\n" \
"v_exp_f32 v42, v42\n" \
"v_mfma_f32_16x16x32_bf16 v[88:91], a[232:235], a[24:27], v[88:91]\n" \
"v_mfma_f32_16x16x32_bf16 v[88:91], a[228:231], a[20:23], v[88:91]\n" \
"v_exp_f32 v43, v43\n" \
"v_mfma_f32_16x16x32_bf16 v[88:91], a[224:227], a[16:19], v[88:91]\n" \
"ds_read_b128 a[212:215], v233 offset:10240\n" \
"v_cvt_pk_bf16_f32 v37, v42, v43\n" \
"v_mfma_f32_16x16x32_bf16 v[104:107], a[224:227], a[160:163], v[196:199]\n" \
"v_exp_f32 v44, v44\n" \
"v_mfma_f32_16x16x32_bf16 v[104:107], a[228:231], a[164:167], v[104:107]\n" \
"v_mfma_f32_16x16x32_bf16 v[104:107], a[232:235], a[168:171], v[104:107]\n" \
"v_exp_f32 v45, v45\n" \
"v_mfma_f32_16x16x32_bf16 v[104:107], a[236:239], a[172:175], v[104:107]\n" \
"ds_read_b128 a[208:211], v233 offset:2048\n" \
"v_cvt_pk_bf16_f32 v38, v44, v45\n" \
"v_mfma_f32_16x16x32_bf16 v[120:123], a[236:239], a[188:191], v[196:199]\n" \
"v_exp_f32 v46, v46\n" \
"v_mfma_f32_16x16x32_bf16 v[120:123], a[232:235], a[184:187], v[120:123]\n" \
"v_mfma_f32_16x16x32_bf16 v[120:123], a[228:231], a[180:183], v[120:123]\n" \
"v_exp_f32 v47, v47\n" \
"v_mfma_f32_16x16x32_bf16 v[120:123], a[224:227], a[176:179], v[120:123]\n" \
"ds_read_b128 a[236:239], v233 offset:13312\n" \
"v_cvt_pk_bf16_f32 v39, v46, v47\n" \
"v_mfma_f32_16x16x32_bf16 v[124:127], a[240:243], a[176:179], v[196:199]\n" \
"v_exp_f32 v56, v56\n" \
"v_mfma_f32_16x16x32_bf16 v[124:127], a[244:247], a[180:183], v[124:127]\n" \
"ds_read_b128 a[232:235], v233 offset:5120\n" \
"v_mfma_f32_16x16x32_bf16 v[124:127], a[248:251], a[184:187], v[124:127]\n" \
"v_exp_f32 v57, v57\n" \
"v_mfma_f32_16x16x32_bf16 v[124:127], a[252:255], a[188:191], v[124:127]\n" \
"ds_read_b128 a[228:231], v233 offset:12288\n" \
"v_cvt_pk_bf16_f32 v52, v56, v57\n" \
"v_mfma_f32_16x16x32_bf16 v[108:111], a[252:255], a[172:175], v[196:199]\n" \
"v_exp_f32 v58, v58\n" \
"v_mfma_f32_16x16x32_bf16 v[108:111], a[248:251], a[168:171], v[108:111]\n" \
"ds_read_b128 a[224:227], v233 offset:4096\n" \
"v_mfma_f32_16x16x32_bf16 v[108:111], a[244:247], a[164:167], v[108:111]\n" \
"v_exp_f32 v59, v59\n" \
"v_mfma_f32_16x16x32_bf16 v[108:111], a[240:243], a[160:163], v[108:111]\n" \
"v_cvt_pk_bf16_f32 v53, v58, v59\n" \
"v_mfma_f32_16x16x32_bf16 v[92:95], a[240:243], a[16:19], v[196:199]\n" \
"v_exp_f32 v60, v60\n" \
"v_mfma_f32_16x16x32_bf16 v[92:95], a[244:247], a[20:23], v[92:95]\n" \
"v_mfma_f32_16x16x32_bf16 v[92:95], a[248:251], a[24:27], v[92:95]\n" \
"v_exp_f32 v61, v61\n" \
"v_mfma_f32_16x16x32_bf16 v[92:95], a[252:255], a[28:31], v[92:95]\n" \
"v_cvt_pk_bf16_f32 v54, v60, v61\n" \
"v_mfma_f32_16x16x32_bf16 v[76:79], a[252:255], a[12:15], v[196:199]\n" \
"v_exp_f32 v62, v62\n" \
"v_mfma_f32_16x16x32_bf16 v[76:79], a[248:251], a[8:11], v[76:79]\n" \
"s_cmp_lg_u32 s82, 0\n" \
"s_cbranch_scc1 Lnw_%=\n" \
"s_waitcnt vmcnt(0)\n" \
"Lnw_%=:\n" \
"v_mfma_f32_16x16x32_bf16 v[76:79], a[244:247], a[4:7], v[76:79]\n" \
"s_waitcnt lgkmcnt(0)\n" \
"v_exp_f32 v63, v63\n" \
"v_mfma_f32_16x16x32_bf16 v[76:79], a[240:243], a[0:3], v[76:79]\n" \
"s_barrier\n" \
"v_cvt_pk_bf16_f32 v55, v62, v63\n" \
"v_mfma_f32_16x16x32_bf16 v[128:131], a[192:195], v[0:3], v[128:131]\n" \
"s_add_u32 m0, s74, s79\n" \
"v_exp_f32 v64, v64\n" \
"v_mfma_f32_16x16x32_bf16 v[128:131], a[196:199], v[4:7], v[128:131]\n" \
"buffer_load_dwordx4 v234, s[36:39], s73 offen offset:0 sc1 lds\n" \
"v_mfma_f32_16x16x32_bf16 v[160:163], a[192:195], v[16:19], v[160:163]\n" \
"v_exp_f32 v65, v65\n" \
"v_mfma_f32_16x16x32_bf16 v[160:163], a[196:199], v[20:23], v[160:163]\n" \
"ds_read_b128 a[240:243], v232 offset:8448\n" \
"v_cvt_pk_bf16_f32 v64, v64, v65\n" \
"v_mfma_f32_16x16x32_bf16 a[64:67], a[192:195], v[32:35], a[64:67]\n" \
"s_add_u32 s71, s73, 17408\n" \
"v_exp_f32 v66, v66\n" \
"v_mfma_f32_16x16x32_bf16 a[64:67], a[196:199], v[36:39], a[64:67]\n" \
"buffer_load_dwordx4 v234, s[36:39], s73 offen offset:1024 sc1 lds\n" \
"v_mfma_f32_16x16x32_bf16 a[96:99], a[192:195], v[48:51], a[96:99]\n" \
"v_exp_f32 v67, v67\n" \
"v_mfma_f32_16x16x32_bf16 a[96:99], a[196:199], v[52:55], a[96:99]\n" \
"ds_read_b128 a[192:195], v232 offset:0\n" \
"v_cvt_pk_bf16_f32 v65, v66, v67\n" \
"v_mfma_f32_16x16x32_bf16 a[100:103], a[200:203], v[48:51], a[100:103]\n" \
"v_exp_f32 v68, v68\n" \
"v_mfma_f32_16x16x32_bf16 a[100:103], a[204:207], v[52:55], a[100:103]\n" \
"buffer_load_dwordx4 v234, s[36:39], s73 offen offset:2048 sc1 lds\n" \
"v_mfma_f32_16x16x32_bf16 a[68:71], a[200:203], v[32:35], a[68:71]\n" \
"v_exp_f32 v69, v69\n" \
"v_mfma_f32_16x16x32_bf16 a[68:71], a[204:207], v[36:39], a[68:71]\n" \
"ds_read_b128 a[196:199], v232 offset:2048\n" \
"v_cvt_pk_bf16_f32 v66, v68, v69\n" \
"v_smfmac_f32_16x16x64_bf16 a[48:51], v[244:247], v[0:7], v243\n" \
"buffer_load_dwordx4 v234, s[36:39], s73 offen offset:3072 sc1 lds\n" \
"v_mfma_f32_16x16x32_bf16 v[164:167], a[200:203], v[16:19], v[164:167]\n" \
"s_add_u32 m0, m0, 0x4000\n" \
"v_exp_f32 v70, v70\n" \
"v_mfma_f32_16x16x32_bf16 v[164:167], a[204:207], v[20:23], v[164:167]\n" \
"buffer_load_dwordx4 v234, s[36:39], s71 offen offset:0 sc1 lds\n" \
"v_mfma_f32_16x16x32_bf16 v[132:135], a[200:203], v[0:3], v[132:135]\n" \
"v_exp_f32 v71, v71\n" \
"v_mfma_f32_16x16x32_bf16 v[132:135], a[204:207], v[4:7], v[132:135]\n" \
"ds_read_b128 a[200:203], v232 offset:4096\n" \
"v_cvt_pk_bf16_f32 v67, v70, v71\n" \
"v_mfma_f32_16x16x32_bf16 v[136:139], a[208:211], v[0:3], v[136:139]\n" \
"v_exp_f32 v80, v80\n" \
"v_mfma_f32_16x16x32_bf16 v[136:139], a[212:215], v[4:7], v[136:139]\n" \
"buffer_load_dwordx4 v234, s[36:39], s71 offen offset:1024 sc1 lds\n" \
"v_mfma_f32_16x16x32_bf16 v[168:171], a[208:211], v[16:19], v[168:171]\n" \
"v_exp_f32 v81, v81\n" \
"v_mfma_f32_16x16x32_bf16 v[168:171], a[212:215], v[20:23], v[168:171]\n" \
"ds_read_b128 a[204:207], v232 offset:6144\n" \
"v_cvt_pk_bf16_f32 v80, v80, v81\n" \
"v_mfma_f32_16x16x32_bf16 a[72:75], a[208:211], v[32:35], a[72:75]\n" \
"v_exp_f32 v82, v82\n" \
"v_mfma_f32_16x16x32_bf16 a[72:75], a[212:215], v[36:39], a[72:75]\n" \
"buffer_load_dwordx4 v234, s[36:39], s71 offen offset:2048 sc1 lds\n" \
"v_mfma_f32_16x16x32_bf16 a[104:107], a[208:211], v[48:51], a[104:107]\n" \
"v_exp_f32 v83, v83\n" \
"v_mfma_f32_16x16x32_bf16 a[104:107], a[212:215], v[52:55], a[104:107]\n" \
"ds_read_b128 a[208:211], v232 offset:256\n" \
"v_cvt_pk_bf16_f32 v81, v82, v83\n" \
"v_mfma_f32_16x16x32_bf16 a[108:111], a[216:219], v[48:51], a[108:111]\n" \
"v_exp_f32 v84, v84\n" \
"v_mfma_f32_16x16x32_bf16 a[108:111], a[220:223], v[52:55], a[108:111]\n" \
"buffer_load_dwordx4 v234, s[36:39], s71 offen offset:3072 sc1 lds\n" \
"v_mfma_f32_16x16x32_bf16 a[76:79], a[216:219], v[32:35], a[76:79]\n" \
"v_exp_f32 v85, v85\n" \
"v_mfma_f32_16x16x32_bf16 a[76:79], a[220:223], v[36:39], a[76:79]\n" \
"s_add_u32 m0, s77, s80\n" \
"ds_read_b128 a[212:215], v232 offset:2304\n" \
"v_cvt_pk_bf16_f32 v82, v84, v85\n" \
"v_smfmac_f32_16x16x64_bf16 a[52:55], v[244:247], v[16:23], v243\n" \
"buffer_load_dwordx4 v234, s[40:43], s76 offen offset:0 sc1 lds\n" \
"v_mfma_f32_16x16x32_bf16 v[172:175], a[216:219], v[16:19], v[172:175]\n" \
"s_add_u32 s71, s76, 17408\n" \
"v_exp_f32 v86, v86\n" \
"v_mfma_f32_16x16x32_bf16 v[172:175], a[220:223], v[20:23], v[172:175]\n" \
"buffer_load_dwordx4 v234, s[40:43], s76 offen offset:1024 sc1 lds\n" \
"v_mfma_f32_16x16x32_bf16 v[140:143], a[216:219], v[0:3], v[140:143]\n" \
"v_exp_f32 v87, v87\n" \
"v_mfma_f32_16x16x32_bf16 v[140:143], a[220:223], v[4:7], v[140:143]\n" \
"ds_read_b128 a[216:219], v232 offset:4352\n" \
"v_cvt_pk_bf16_f32 v83, v86, v87\n" \
"v_mfma_f32_16x16x32_bf16 v[144:147], a[224:227], v[0:3], v[144:147]\n" \
"v_exp_f32 v96, v96\n" \
"v_mfma_f32_16x16x32_bf16 v[144:147], a[228:231], v[4:7], v[144:147]\n" \
"buffer_load_dwordx4 v234, s[40:43], s76 offen offset:2048 sc1 lds\n" \
"v_mfma_f32_16x16x32_bf16 v[176:179], a[224:227], v[16:19], v[176:179]\n" \
"v_exp_f32 v97, v97\n" \
"v_mfma_f32_16x16x32_bf16 v[176:179], a[228:231], v[20:23], v[176:179]\n" \
"ds_read_b128 a[220:223], v232 offset:6400\n" \
"v_cvt_pk_bf16_f32 v96, v96, v97\n" \
"v_mfma_f32_16x16x32_bf16 a[80:83], a[224:227], v[32:35], a[80:83]\n" \
"v_exp_f32 v98, v98\n" \
"v_mfma_f32_16x16x32_bf16 a[80:83], a[228:231], v[36:39], a[80:83]\n" \
"buffer_load_dwordx4 v234, s[40:43], s76 offen offset:3072 sc1 lds\n" \
"v_mfma_f32_16x16x32_bf16 a[112:115], a[224:227], v[48:51], a[112:115]\n" \
"v_exp_f32 v99, v99\n" \
"v_mfma_f32_16x16x32_bf16 a[112:115], a[228:231], v[52:55], a[112:115]\n" \
"ds_read_b128 a[224:227], v232 offset:8192\n" \
"v_cvt_pk_bf16_f32 v97, v98, v99\n" \
"v_mfma_f32_16x16x32_bf16 a[116:119], a[232:235], v[48:51], a[116:119]\n" \
"s_add_u32 m0, m0, 0x4000\n" \
"v_exp_f32 v100, v100\n" \
"v_mfma_f32_16x16x32_bf16 a[116:119], a[236:239], v[52:55], a[116:119]\n" \
"buffer_load_dwordx4 v234, s[40:43], s71 offen offset:0 sc1 lds\n" \
"v_mfma_f32_16x16x32_bf16 a[84:87], a[232:235], v[32:35], a[84:87]\n" \
"v_exp_f32 v101, v101\n" \
"v_mfma_f32_16x16x32_bf16 a[84:87], a[236:239], v[36:39], a[84:87]\n" \
"ds_read_b128 a[228:231], v232 offset:10240\n" \
"v_cvt_pk_bf16_f32 v98, v100, v101\n" \
"v_smfmac_f32_16x16x64_bf16 v[224:227], v[244:247], v[32:39], v243\n" \
"buffer_load_dwordx4 v234, s[40:43], s71 offen offset:1024 sc1 lds\n" \
"v_mfma_f32_16x16x32_bf16 v[180:183], a[232:235], v[16:19], v[180:183]\n" \
"v_exp_f32 v102, v102\n" \
"v_mfma_f32_16x16x32_bf16 v[180:183], a[236:239], v[20:23], v[180:183]\n" \
"buffer_load_dwordx4 v234, s[40:43], s71 offen offset:2048 sc1 lds\n" \
"v_mfma_f32_16x16x32_bf16 v[148:151], a[232:235], v[0:3], v[148:151]\n" \
"v_exp_f32 v103, v103\n" \
"v_mfma_f32_16x16x32_bf16 v[148:151], a[236:239], v[4:7], v[148:151]\n" \
"ds_read_b128 a[232:235], v232 offset:12288\n" \
"v_cvt_pk_bf16_f32 v99, v102, v103\n" \
"v_mfma_f32_16x16x32_bf16 v[152:155], a[32:35], v[0:3], v[152:155]\n" \
"v_exp_f32 v112, v112\n" \
"v_mfma_f32_16x16x32_bf16 v[152:155], a[36:39], v[4:7], v[152:155]\n" \
"buffer_load_dwordx4 v234, s[40:43], s71 offen offset:3072 sc1 lds\n" \
"v_mfma_f32_16x16x32_bf16 v[184:187], a[32:35], v[16:19], v[184:187]\n" \
"v_exp_f32 v113, v113\n" \
"v_mfma_f32_16x16x32_bf16 v[184:187], a[36:39], v[20:23], v[184:187]\n" \
"ds_read_b128 a[236:239], v232 offset:14336\n" \
"v_cvt_pk_bf16_f32 v112, v112, v113\n" \
"v_mfma_f32_16x16x32_bf16 a[88:91], a[32:35], v[32:35], a[88:91]\n" \
"v_exp_f32 v114, v114\n" \
"v_mfma_f32_16x16x32_bf16 a[88:91], a[36:39], v[36:39], a[88:91]\n" \
"buffer_load_dword v239, v242, s[56:59], s95 offen sc1\n" \
"v_mfma_f32_16x16x32_bf16 a[120:123], a[32:35], v[48:51], a[120:123]\n" \
"s_mov_b32 s69, s92\n" \
"v_exp_f32 v115, v115\n" \
"v_mfma_f32_16x16x32_bf16 a[120:123], a[36:39], v[52:55], a[120:123]\n" \
"s_mov_b32 s76, s73\n" \
"ds_read_b128 a[244:247], v232 offset:10496\n" \
"v_cvt_pk_bf16_f32 v113, v114, v115\n" \
"v_mfma_f32_16x16x32_bf16 a[124:127], a[40:43], v[48:51], a[124:127]\n" \
"s_add_u32 s73, s73, 34816\n" \
"v_exp_f32 v116, v116\n" \
"v_mfma_f32_16x16x32_bf16 a[124:127], a[44:47], v[52:55], a[124:127]\n" \
"s_xor_b32 s74, s74, 0x8000\n" \
"s_xor_b32 s77, s77, 0x8000\n" \
"v_xor_b32 v233, 0x8000, v233\n" \
"v_mfma_f32_16x16x32_bf16 a[92:95], a[40:43], v[32:35], a[92:95]\n" \
"s_mov_b32 s82, 0\n" \
"v_exp_f32 v117, v117\n" \
"v_mfma_f32_16x16x32_bf16 a[92:95], a[44:47], v[36:39], a[92:95]\n" \
"ds_read_b128 a[248:251], v232 offset:12544\n" \
"v_cvt_pk_bf16_f32 v114, v116, v117\n" \
"Lpg6766_%=:\n" \
"s_cbranch_execz Lcb0_%=\n" \
"v_smfmac_f32_16x16x64_bf16 v[228:231], v[244:247], v[48:55], v243\n" \
"v_mfma_f32_16x16x32_bf16 v[188:191], a[40:43], v[16:19], v[188:191]\n" \
"v_exp_f32_e64 v118, v118\n" \
"v_mfma_f32_16x16x32_bf16 v[188:191], a[44:47], v[20:23], v[188:191]\n" \
"s_cmp_lg_u32 s84, 0\n" \
"v_exp_f32_e64 v119, v119\n" \
"v_mfma_f32_16x16x32_bf16 v[156:159], a[40:43], v[0:3], v[156:159]\n" \
"s_waitcnt lgkmcnt(2)\n" \
"ds_read_b128 a[252:255], v232 offset:14592\n" \
"v_cvt_pk_bf16_f32 v115, v118, v119\n" \
"v_mfma_f32_16x16x32_bf16 v[156:159], a[44:47], v[4:7], v[156:159]\n" \
"s_cbranch_scc1 Lloop2_%=\n" \
"Lg_%=:\n" \
"Lpg6780_%=:\n" \
"s_cbranch_execz Lcb0_%=\n" \
"v_mfma_f32_16x16x32_bf16 v[32:35], a[192:195], a[160:163], v[208:211]\n" \
"s_mov_b64 s[44:45], s[20:21]\n" \
"s_memrealtime s[14:15]\n" \
"v_exp_f32 v72, v72\n" \
"v_mfma_f32_16x16x32_bf16 v[32:35], a[196:199], a[164:167], v[32:35]\n" \
"s_and_b32 s45, s45, 0xffff\n" \
"ds_read_b128 a[32:35], v233 offset:6144\n" \
"ds_read_b128 a[36:39], v233 offset:14336\n" \
"v_mfma_f32_16x16x32_bf16 v[32:35], a[200:203], a[168:171], v[32:35]\n" \
"s_mov_b32 s46, 4194304\n" \
"v_exp_f32 v73, v73\n" \
"v_mfma_f32_16x16x32_bf16 v[32:35], a[204:207], a[172:175], v[32:35]\n" \
"ds_read_b128 a[40:43], v233 offset:7168\n" \
"ds_read_b128 a[44:47], v233 offset:15360\n" \
"global_load_dword v253, v254, s[22:23]\n" \
"v_mfma_f32_16x16x32_bf16 v[48:51], a[204:207], a[188:191], v[196:199]\n" \
"s_mov_b32 s47, 0x20000\n" \
"v_exp_f32 v74, v74\n" \
"v_mfma_f32_16x16x32_bf16 v[48:51], a[200:203], a[184:187], v[48:51]\n" \
"s_mov_b64 s[48:49], s[22:23]\n" \
"v_exp_f32 v75, v75\n" \
"v_mfma_f32_16x16x32_bf16 v[48:51], a[196:199], a[180:183], v[48:51]\n" \
"s_and_b32 s49, s49, 0xffff\n" \
"v_exp_f32 v76, v76\n" \
"v_mfma_f32_16x16x32_bf16 v[48:51], a[192:195], a[176:179], v[48:51]\n" \
"s_mov_b32 s50, 6291456\n" \
"ds_read_b128 a[200:203], v233 offset:1024\n" \
"ds_read_b128 a[204:207], v233 offset:9216\n" \
"v_mfma_f32_16x16x32_bf16 v[52:55], a[208:211], a[176:179], v[196:199]\n" \
"s_mov_b32 s51, 0x20000\n" \
"v_cvt_pk_bf16_f32 v68, v72, v73\n" \
"v_cvt_pk_bf16_f32 v69, v74, v75\n" \
"v_mfma_f32_16x16x32_bf16 v[52:55], a[212:215], a[180:183], v[52:55]\n" \
"s_bfe_u32 s94, %1, 0x20001\n" \
"v_exp_f32 v77, v77\n" \
"v_mfma_f32_16x16x32_bf16 v[52:55], a[216:219], a[184:187], v[52:55]\n" \
"s_lshl_b32 s94, s94, 2\n" \
"v_exp_f32 v78, v78\n" \
"v_mfma_f32_16x16x32_bf16 v[52:55], a[220:223], a[188:191], v[52:55]\n" \
"s_lshr_b32 s70, %1, 6\n" \
"ds_read_b128 a[192:195], v233 offset:0\n" \
"ds_read_b128 a[196:199], v233 offset:8192\n" \
"v_mfma_f32_16x16x32_bf16 v[36:39], a[220:223], a[172:175], v[212:215]\n" \
"s_add_u32 s94, s94, s70\n" \
"v_exp_f32 v79, v79\n" \
"v_mfma_f32_16x16x32_bf16 v[36:39], a[216:219], a[168:171], v[36:39]\n" \
"s_lshl_b32 s73, s94, 18\n" \
"v_exp_f32 v88, v88\n" \
"v_mfma_f32_16x16x32_bf16 v[36:39], a[212:215], a[164:167], v[36:39]\n" \
"s_max_u32 s75, s73, 32768\n" \
"v_cvt_pk_bf16_f32 v70, v76, v77\n" \
"v_cvt_pk_bf16_f32 v71, v78, v79\n" \
"v_mfma_f32_16x16x32_bf16 v[36:39], a[208:211], a[160:163], v[36:39]\n" \
"s_sub_u32 s75, s75, 32768\n" \
"ds_read_b128 a[216:219], v233 offset:3072\n" \
"ds_read_b128 a[220:223], v233 offset:11264\n" \
"v_mfma_f32_16x16x32_bf16 v[40:43], a[224:227], a[160:163], v[216:219]\n" \
"s_and_b32 s70, s61, 1\n" \
"v_exp_f32 v89, v89\n" \
"v_mfma_f32_16x16x32_bf16 v[40:43], a[228:231], a[164:167], v[40:43]\n" \
"s_lshr_b32 s71, s61, 1\n" \
"v_exp_f32 v90, v90\n" \
"v_mfma_f32_16x16x32_bf16 v[40:43], a[232:235], a[168:171], v[40:43]\n" \
"s_lshl1_add_u32 s70, s70, s71\n" \
"v_exp_f32 v91, v91\n" \
"v_mfma_f32_16x16x32_bf16 v[40:43], a[236:239], a[172:175], v[40:43]\n" \
"s_lshl_b32 s70, s70, 10\n" \
"ds_read_b128 a[208:211], v233 offset:2048\n" \
"ds_read_b128 a[212:215], v233 offset:10240\n" \
"v_mfma_f32_16x16x32_bf16 v[56:59], a[236:239], a[188:191], v[196:199]\n" \
"s_add_u32 s75, s75, s70\n" \
"v_exp_f32 v92, v92\n" \
"v_mfma_f32_16x16x32_bf16 v[56:59], a[232:235], a[184:187], v[56:59]\n" \
"s_lshl_b32 s89, s61, 13\n" \
"v_cvt_pk_bf16_f32 v84, v88, v89\n" \
"v_cvt_pk_bf16_f32 v85, v90, v91\n" \
"v_mfma_f32_16x16x32_bf16 v[56:59], a[228:231], a[180:183], v[56:59]\n" \
"s_add_u32 s89, s89, %3\n" \
"v_exp_f32 v93, v93\n" \
"v_mfma_f32_16x16x32_bf16 v[56:59], a[224:227], a[176:179], v[56:59]\n" \
"s_lshr_b32 s71, s22, 7\n" \
"ds_read_b128 a[232:235], v233 offset:5120\n" \
"ds_read_b128 a[236:239], v233 offset:13312\n" \
"v_mfma_f32_16x16x32_bf16 v[60:63], a[240:243], a[176:179], v[196:199]\n" \
"s_waitcnt lgkmcnt(2)\n" \
"s_sub_u32 s71, s63, s71\n" \
"v_exp_f32 v94, v94\n" \
"v_mfma_f32_16x16x32_bf16 v[60:63], a[244:247], a[180:183], v[60:63]\n" \
"s_add_u32 s71, s71, 6\n" \
"v_exp_f32 v95, v95\n" \
"v_mfma_f32_16x16x32_bf16 v[60:63], a[248:251], a[184:187], v[60:63]\n" \
"s_and_b32 s71, s71, 7\n" \
"v_cvt_pk_bf16_f32 v86, v92, v93\n" \
"v_accvgpr_read_b32 v192, a57\n" \
"v_mfma_f32_16x16x32_bf16 v[60:63], a[252:255], a[188:191], v[60:63]\n" \
"s_and_b32 s70, %1, 1\n" \
"ds_read_b128 a[224:227], v233 offset:4096\n" \
"ds_read_b128 a[228:231], v233 offset:12288\n" \
"v_mfma_f32_16x16x32_bf16 v[44:47], a[252:255], a[172:175], v[220:223]\n" \
"s_lshl_b32 s70, s70, 3\n" \
"v_cvt_pk_bf16_f32 v87, v94, v95\n" \
"v_mfma_f32_16x16x32_bf16 v[44:47], a[248:251], a[168:171], v[44:47]\n" \
"s_add_u32 s70, s70, s71\n" \
"v_exp_f32 v104, v104\n" \
"v_mfma_f32_16x16x32_bf16 v[44:47], a[244:247], a[164:167], v[44:47]\n" \
"s_lshl_b32 s78, s70, 7\n" \
"v_accvgpr_read_b32 v197, a56\n" \
"v_add_u32 v198, 1024, v197\n" \
"v_mfma_f32_16x16x32_bf16 v[44:47], a[240:243], a[160:163], v[44:47]\n" \
"s_lshr_b32 s80, %1, 6\n" \
"v_add_u32 v199, 2048, v197\n" \
"v_add_u32 v193, 31744, v192\n" \
"v_mfma_f32_16x16x32_bf16 v[128:131], a[192:195], v[64:67], v[128:131]\n" \
"s_xor_b32 s80, s80, s63\n" \
"v_exp_f32 v105, v105\n" \
"v_mfma_f32_16x16x32_bf16 v[128:131], a[196:199], v[68:71], v[128:131]\n" \
"s_and_b32 s80, s80, 0\n" \
"ds_read_b128 a[0:3], v232 offset:16384\n" \
"ds_read_b128 a[4:7], v232 offset:18432\n" \
"v_mfma_f32_16x16x32_bf16 v[160:163], a[192:195], v[80:83], v[160:163]\n" \
"s_add_u32 m0, s89, 131072\n" \
"v_exp_f32 v106, v106\n" \
"v_mfma_f32_16x16x32_bf16 v[160:163], a[196:199], v[84:87], v[160:163]\n" \
"buffer_load_dwordx4 v192, s[44:47], s75 offen offset:0 lds\n" \
"v_exp_f32 v107, v107\n" \
"v_mfma_f32_16x16x32_bf16 v[164:167], a[200:203], v[80:83], v[164:167]\n" \
"s_lshl_b32 s93, s70, 12\n" \
"ds_read_b128 a[8:11], v232 offset:20480\n" \
"ds_read_b128 a[12:15], v232 offset:22528\n" \
"v_mfma_f32_16x16x32_bf16 v[164:167], a[204:207], v[84:87], v[164:167]\n" \
"s_add_u32 m0, s89, 131968\n" \
"v_exp_f32 v108, v108\n" \
"v_mfma_f32_16x16x32_bf16 v[132:135], a[200:203], v[64:67], v[132:135]\n" \
"buffer_load_dwordx4 v192, s[44:47], s75 offen offset:128 lds\n" \
"v_mfma_f32_16x16x32_bf16 v[132:135], a[204:207], v[68:71], v[132:135]\n" \
"s_mul_i32 s91, s61, 6144\n" \
"ds_read_b128 a[16:19], v232 offset:16640\n" \
"ds_read_b128 a[20:23], v232 offset:18688\n" \
"v_cvt_pk_bf16_f32 v100, v104, v105\n" \
"v_smfmac_f32_16x16x64_bf16 a[48:51], v[244:247], v[64:71], v243\n" \
"s_add_u32 m0, s89, 132864\n" \
"v_cvt_pk_bf16_f32 v101, v106, v107\n" \
"v_mfma_f32_16x16x32_bf16 v[136:139], a[208:211], v[64:67], v[136:139]\n" \
"buffer_load_dwordx4 v192, s[44:47], s75 offen offset:256 lds\n" \
"v_mfma_f32_16x16x32_bf16 v[136:139], a[212:215], v[68:71], v[136:139]\n" \
"s_add_u32 s91, s91, %3\n" \
"ds_read_b128 a[24:27], v232 offset:20736\n" \
"ds_read_b128 a[28:31], v232 offset:22784\n" \
"v_mfma_f32_16x16x32_bf16 v[168:171], a[208:211], v[80:83], v[168:171]\n" \
"s_add_u32 m0, s89, 133760\n" \
"v_exp_f32 v109, v109\n" \
"v_mfma_f32_16x16x32_bf16 v[168:171], a[212:215], v[84:87], v[168:171]\n" \
"buffer_load_dwordx4 v192, s[44:47], s75 offen offset:384 lds\n" \
"v_mfma_f32_16x16x32_bf16 v[172:175], a[216:219], v[80:83], v[172:175]\n" \
"s_lshl_b32 s71, s80, 7\n" \
"ds_read_b128 v[0:3], v232 offset:24576\n" \
"ds_read_b128 v[4:7], v232 offset:26624\n" \
"v_mfma_f32_16x16x32_bf16 v[172:175], a[220:223], v[84:87], v[172:175]\n" \
"s_add_u32 m0, s89, 134656\n" \
"v_exp_f32 v110, v110\n" \
"v_mfma_f32_16x16x32_bf16 v[140:143], a[216:219], v[64:67], v[140:143]\n" \
"buffer_load_dwordx4 v192, s[44:47], s75 offen offset:512 lds\n" \
"v_mfma_f32_16x16x32_bf16 v[140:143], a[220:223], v[68:71], v[140:143]\n" \
"s_lshl_b32 s70, s61, 16\n" \
"ds_read_b128 v[8:11], v232 offset:28672\n" \
"ds_read_b128 v[12:15], v232 offset:30720\n" \
"v_exp_f32 v111, v111\n" \
"v_smfmac_f32_16x16x64_bf16 a[52:55], v[244:247], v[80:87], v243\n" \
"s_waitcnt lgkmcnt(4)\n" \
"s_add_u32 m0, s89, 135552\n" \
"v_cvt_pk_bf16_f32 v102, v108, v109\n" \
"v_mfma_f32_16x16x32_bf16 v[144:147], a[224:227], v[64:67], v[144:147]\n" \
"buffer_load_dwordx4 v192, s[44:47], s75 offen offset:640 lds\n" \
"v_mfma_f32_16x16x32_bf16 v[144:147], a[228:231], v[68:71], v[144:147]\n" \
"s_add_u32 s73, s73, s70\n" \
"ds_read_b128 v[200:203], v232 offset:24832\n" \
"ds_read_b128 v[204:207], v232 offset:26880\n" \
"v_mfma_f32_16x16x32_bf16 v[176:179], a[224:227], v[80:83], v[176:179]\n" \
"s_add_u32 m0, s89, 136448\n" \
"v_exp_f32 v120, v120\n" \
"v_mfma_f32_16x16x32_bf16 v[176:179], a[228:231], v[84:87], v[176:179]\n" \
"buffer_load_dwordx4 v192, s[44:47], s75 offen offset:768 lds\n" \
"v_mfma_f32_16x16x32_bf16 v[180:183], a[232:235], v[80:83], v[180:183]\n" \
"s_add_u32 s73, s73, s71\n" \
"ds_read_b128 a[240:243], v232 offset:28928\n" \
"ds_read_b128 a[244:247], v232 offset:30976\n" \
"v_mfma_f32_16x16x32_bf16 v[180:183], a[236:239], v[84:87], v[180:183]\n" \
"s_add_u32 m0, s89, 137344\n" \
"v_exp_f32 v121, v121\n" \
"v_exp_f32 v122, v122\n" \
"v_mfma_f32_16x16x32_bf16 v[148:151], a[232:235], v[64:67], v[148:151]\n" \
"buffer_load_dwordx4 v192, s[44:47], s75 offen offset:896 lds\n" \
"v_mfma_f32_16x16x32_bf16 v[148:151], a[236:239], v[68:71], v[148:151]\n" \
"s_and_b32 s70, s61, 1\n" \
"v_cvt_pk_bf16_f32 v103, v110, v111\n" \
"v_cvt_pk_bf16_f32 v116, v120, v121\n" \
"v_mfma_f32_16x16x32_bf16 v[152:155], a[32:35], v[64:67], v[152:155]\n" \
"s_lshl_b32 s70, s70, 11\n" \
"v_add_u32 v233, 0x10000, v235\n" \
"s_lshl_b32 s72, s61, 13\n" \
"v_mfma_f32_16x16x32_bf16 v[152:155], a[36:39], v[68:71], v[152:155]\n" \
"s_add_u32 s93, s93, s70\n" \
"v_add_u32 v232, s72, v235\n" \
"v_exp_f32 v123, v123\n" \
"v_mfma_f32_16x16x32_bf16 v[184:187], a[32:35], v[80:83], v[184:187]\n" \
"s_add_u32 s93, s93, 52740096\n" \
"s_cmp_eq_u32 s94, 0\n" \
"s_cselect_b32 s92, -1, 0\n" \
"s_mul_i32 s70, s94, 278528\n" \
"s_add_u32 s93, s93, s70\n" \
"s_lshr_b32 s70, s61, 1\n" \
"s_lshl_b32 s70, s70, 8\n" \
"s_add_u32 s93, s93, s70\n" \
"v_mfma_f32_16x16x32_bf16 v[184:187], a[36:39], v[84:87], v[184:187]\n" \
"v_exp_f32 v124, v124\n" \
"v_mfma_f32_16x16x32_bf16 v[188:191], a[40:43], v[80:83], v[188:191]\n" \
"v_exp_f32 v125, v125\n" \
"v_mfma_f32_16x16x32_bf16 v[188:191], a[44:47], v[84:87], v[188:191]\n" \
"v_exp_f32 v126, v126\n" \
"v_cvt_pk_bf16_f32 v117, v122, v123\n" \
"v_mfma_f32_16x16x32_bf16 v[156:159], a[40:43], v[64:67], v[156:159]\n" \
"s_mul_i32 s76, s80, 393216\n" \
"v_exp_f32 v127, v127\n" \
"v_mfma_f32_16x16x32_bf16 v[156:159], a[44:47], v[68:71], v[156:159]\n" \
"s_add_u32 s76, s76, s78\n" \
"s_cmp_eq_u32 s63, 7\n" \
"s_waitcnt vmcnt(9) lgkmcnt(0)\n" \
"s_barrier\n" \
"s_cbranch_scc1 Lep_%=\n" \
"Lepg_%=:\n" \
"v_mfma_f32_16x16x32_bf16 v[16:19], a[0:3], a[176:179], v[208:211]\n" \
"s_add_u32 m0, s91, 106496\n" \
"ds_read_b128 v[64:67], v233 offset:16384\n" \
"ds_read_b128 v[68:71], v233 offset:24576\n" \
"v_mfma_f32_16x16x32_bf16 v[16:19], a[4:7], a[180:183], v[16:19]\n" \
"buffer_load_dwordx4 v197, s[48:51], s76 offen offset:0 lds\n" \
"v_mfma_f32_16x16x32_bf16 v[16:19], a[8:11], a[184:187], v[16:19]\n" \
"ds_read_b128 v[72:75], v233 offset:17408\n" \
"ds_read_b128 v[76:79], v233 offset:25600\n" \
"v_mfma_f32_16x16x32_bf16 v[16:19], a[12:15], a[188:191], v[16:19]\n" \
"ds_read_b128 v[80:83], v233 offset:18432\n" \
"ds_read_b128 v[84:87], v233 offset:26624\n" \
"v_cvt_pk_bf16_f32 v128, v128, v129\n" \
"v_cvt_pk_bf16_f32 v129, v130, v131\n" \
"v_cvt_pk_bf16_f32 v130, v132, v133\n" \
"v_cvt_pk_bf16_f32 v118, v124, v125\n" \
"v_mfma_f32_16x16x32_bf16 v[20:23], a[28:31], a[188:191], v[212:215]\n" \
"s_add_u32 s77, s76, 24576\n" \
"ds_read_b128 v[88:91], v233 offset:19456\n" \
"ds_read_b128 v[92:95], v233 offset:27648\n" \
"v_mfma_f32_16x16x32_bf16 v[20:23], a[24:27], a[184:187], v[20:23]\n" \
"buffer_load_dwordx4 v198, s[48:51], s76 offen offset:1024 lds\n" \
"v_cvt_pk_bf16_f32 v131, v134, v135\n" \
"v_cvt_pk_bf16_f32 v119, v126, v127\n" \
"v_exp_f32 v32, v32\n" \
"v_xor_b32 v248, 8192, v232\n" \
"v_mfma_f32_16x16x32_bf16 v[20:23], a[20:23], a[180:183], v[20:23]\n" \
"ds_write_b128 v232, v[128:131] offset:0\n" \
"v_exp_f32 v33, v33\n" \
"v_mfma_f32_16x16x32_bf16 v[20:23], a[16:19], a[176:179], v[20:23]\n" \
"ds_read_b128 a[248:251], v233 offset:7168\n" \
"ds_read_b128 a[252:255], v233 offset:15360\n" \
"v_exp_f32 v34, v34\n" \
"v_mfma_f32_16x16x32_bf16 v[24:27], v[0:3], a[176:179], v[216:219]\n" \
"s_xor_b32 s80, s80, 0x3\n" \
"v_cvt_pk_bf16_f32 v132, v136, v137\n" \
"v_cvt_pk_bf16_f32 v133, v138, v139\n" \
"v_mfma_f32_16x16x32_bf16 v[24:27], v[4:7], a[180:183], v[24:27]\n" \
"buffer_load_dwordx4 v199, s[48:51], s76 offen offset:2048 lds\n" \
"v_exp_f32 v35, v35\n" \
"v_lshrrev_b32 v249, 1, v234\n" \
"v_mfma_f32_16x16x32_bf16 v[24:27], v[8:11], a[184:187], v[24:27]\n" \
"s_lshl_b32 s72, s61, 9\n" \
"v_cvt_pk_bf16_f32 v134, v140, v141\n" \
"v_cvt_pk_bf16_f32 v135, v142, v143\n" \
"v_mfma_f32_16x16x32_bf16 v[24:27], v[12:15], a[188:191], v[24:27]\n" \
"s_add_u32 s72, s72, 57344\n" \
"s_add_u32 s72, s72, %3\n" \
"v_cvt_pk_bf16_f32 v136, v144, v145\n" \
"v_cvt_pk_bf16_f32 v137, v146, v147\n" \
"v_exp_f32 v36, v36\n" \
"v_cvt_pk_bf16_f32 v32, v32, v33\n" \
"v_mfma_f32_16x16x32_bf16 v[28:31], a[244:247], a[188:191], v[220:223]\n" \
"s_add_u32 m0, m0, 3072\n" \
"v_cvt_pk_bf16_f32 v138, v148, v149\n" \
"v_cvt_pk_bf16_f32 v139, v150, v151\n" \
"v_mfma_f32_16x16x32_bf16 v[28:31], a[240:243], a[184:187], v[28:31]\n" \
"buffer_load_dwordx4 v197, s[48:51], s77 offen offset:0 lds\n" \
"v_exp_f32 v37, v37\n" \
"v_mfma_f32_16x16x32_bf16 v[28:31], v[204:207], a[180:183], v[28:31]\n" \
"ds_write_b128 v232, v[132:135] offset:1024\n" \
"v_exp_f32 v38, v38\n" \
"v_cvt_pk_bf16_f32 v33, v34, v35\n" \
"v_add_u32 v249, s72, v249\n" \
"v_mfma_f32_16x16x32_bf16 v[28:31], v[200:203], a[176:179], v[28:31]\n" \
"v_cvt_pk_bf16_f32 v140, v152, v153\n" \
"v_cvt_pk_bf16_f32 v141, v154, v155\n" \
"v_exp_f32 v39, v39\n" \
"v_mfma_f32_16x16x32_bf16 a[64:67], a[192:195], v[96:99], a[64:67]\n" \
"v_cvt_pk_bf16_f32 v142, v156, v157\n" \
"v_cvt_pk_bf16_f32 v143, v158, v159\n" \
"v_mfma_f32_16x16x32_bf16 a[64:67], a[196:199], v[100:103], a[64:67]\n" \
"buffer_load_dwordx4 v198, s[48:51], s77 offen offset:1024 lds\n" \
"v_exp_f32 v48, v48\n" \
"v_cvt_pk_bf16_f32 v34, v36, v37\n" \
"v_mfma_f32_16x16x32_bf16 a[96:99], a[192:195], v[112:115], a[96:99]\n" \
"v_cvt_pk_bf16_f32 v144, v160, v161\n" \
"v_cvt_pk_bf16_f32 v145, v162, v163\n" \
"v_mfma_f32_16x16x32_bf16 a[96:99], a[196:199], v[116:119], a[96:99]\n" \
"ds_write_b128 v232, v[136:139] offset:2048\n" \
"v_exp_f32 v49, v49\n" \
"v_exp_f32 v50, v50\n" \
"v_cvt_pk_bf16_f32 v35, v38, v39\n" \
"v_accvgpr_read_b32 v250, a48\n" \
"v_mfma_f32_16x16x32_bf16 a[100:103], a[200:203], v[112:115], a[100:103]\n" \
"ds_read_b128 a[192:195], v233 offset:0\n" \
"ds_read_b128 a[196:199], v233 offset:8192\n" \
"v_mfma_f32_16x16x32_bf16 a[100:103], a[204:207], v[116:119], a[100:103]\n" \
"buffer_load_dwordx4 v199, s[48:51], s77 offen offset:2048 lds\n" \
"v_exp_f32 v51, v51\n" \
"v_exp_f32 v52, v52\n" \
"v_mfma_f32_16x16x32_bf16 a[68:71], a[200:203], v[96:99], a[68:71]\n" \
"v_cvt_pk_bf16_f32 v146, v164, v165\n" \
"v_cvt_pk_bf16_f32 v147, v166, v167\n" \
"v_mfma_f32_16x16x32_bf16 a[68:71], a[204:207], v[100:103], a[68:71]\n" \
"ds_read_b128 a[200:203], v233 offset:1024\n" \
"ds_read_b128 a[204:207], v233 offset:9216\n" \
"v_cvt_pk_bf16_f32 v48, v48, v49\n" \
"v_exp_f32 v53, v53\n" \
"v_smfmac_f32_16x16x64_bf16 v[224:227], v[244:247], v[96:103], v243\n" \
"s_add_u32 m0, s79, 40960\n" \
"v_cvt_pk_bf16_f32 v148, v168, v169\n" \
"v_exp_f32 v54, v54\n" \
"v_cvt_pk_bf16_f32 v49, v50, v51\n" \
"v_accvgpr_read_b32 v240, a49\n" \
"v_mfma_f32_16x16x32_bf16 a[72:75], a[208:211], v[96:99], a[72:75]\n" \
"buffer_load_dwordx4 v192, s[44:47], s73 offen offset:0 lds\n" \
"v_mfma_f32_16x16x32_bf16 a[72:75], a[212:215], v[100:103], a[72:75]\n" \
"ds_write_b128 v232, v[140:143] offset:3072\n" \
"v_exp_f32 v55, v55\n" \
"v_exp_f32 v40, v40\n" \
"v_mfma_f32_16x16x32_bf16 a[104:107], a[208:211], v[112:115], a[104:107]\n" \
"v_cvt_pk_bf16_f32 v149, v170, v171\n" \
"v_cvt_pk_bf16_f32 v150, v172, v173\n" \
"v_mfma_f32_16x16x32_bf16 a[104:107], a[212:215], v[116:119], a[104:107]\n" \
"ds_read_b128 a[208:211], v233 offset:2048\n" \
"ds_read_b128 a[212:215], v233 offset:10240\n" \
"v_cvt_pk_bf16_f32 v50, v52, v53\n" \
"v_exp_f32 v41, v41\n" \
"v_mfma_f32_16x16x32_bf16 a[108:111], a[216:219], v[112:115], a[108:111]\n" \
"buffer_load_dwordx4 v193, s[44:47], s73 offen offset:1024 lds\n" \
"v_mfma_f32_16x16x32_bf16 a[108:111], a[220:223], v[116:119], a[108:111]\n" \
"v_cvt_pk_bf16_f32 v151, v174, v175\n" \
"v_cvt_pk_bf16_f32 v152, v176, v177\n" \
"v_exp_f32 v42, v42\n" \
"v_cvt_pk_bf16_f32 v51, v54, v55\n" \
"v_exp_f32 v43, v43\n" \
"v_add_f32 v250, v250, v240\n" \
"v_mfma_f32_16x16x32_bf16 a[76:79], a[216:219], v[96:99], a[76:79]\n" \
"v_cvt_pk_bf16_f32 v153, v178, v179\n" \
"v_cvt_pk_bf16_f32 v154, v180, v181\n" \
"v_mfma_f32_16x16x32_bf16 a[76:79], a[220:223], v[100:103], a[76:79]\n" \
"ds_read_b128 a[216:219], v233 offset:3072\n" \
"ds_read_b128 a[220:223], v233 offset:11264\n" \
"v_exp_f32 v44, v44\n" \
"v_cvt_pk_bf16_f32 v36, v40, v41\n" \
"v_smfmac_f32_16x16x64_bf16 v[228:231], v[244:247], v[112:119], v243\n" \
"buffer_load_dwordx4 v192, s[44:47], s73 offen offset:2048 lds\n" \
"v_exp_f32 v45, v45\n" \
"v_mfma_f32_16x16x32_bf16 a[80:83], a[224:227], v[96:99], a[80:83]\n" \
"ds_write_b128 v232, v[144:147] offset:4096\n" \
"v_exp_f32 v46, v46\n" \
"v_cvt_pk_bf16_f32 v37, v42, v43\n" \
"v_accvgpr_read_b32 v251, a52\n" \
"v_mfma_f32_16x16x32_bf16 a[80:83], a[228:231], v[100:103], a[80:83]\n" \
"v_cvt_pk_bf16_f32 v155, v182, v183\n" \
"v_cvt_pk_bf16_f32 v156, v184, v185\n" \
"v_exp_f32 v47, v47\n" \
"v_mfma_f32_16x16x32_bf16 a[112:115], a[224:227], v[112:115], a[112:115]\n" \
"v_cvt_pk_bf16_f32 v157, v186, v187\n" \
"v_cvt_pk_bf16_f32 v158, v188, v189\n" \
"v_mfma_f32_16x16x32_bf16 a[112:115], a[228:231], v[116:119], a[112:115]\n" \
"buffer_load_dwordx4 v193, s[44:47], s73 offen offset:3072 lds\n" \
"v_exp_f32 v56, v56\n" \
"v_cvt_pk_bf16_f32 v38, v44, v45\n" \
"v_mfma_f32_16x16x32_bf16 a[116:119], a[232:235], v[112:115], a[116:119]\n" \
"ds_read_b128 a[224:227], v233 offset:4096\n" \
"ds_read_b128 a[228:231], v233 offset:12288\n" \
"v_mfma_f32_16x16x32_bf16 a[116:119], a[236:239], v[116:119], a[116:119]\n" \
"v_cvt_pk_bf16_f32 v159, v190, v191\n" \
"v_cvt_pk_bf16_f32 v39, v46, v47\n" \
"v_exp_f32 v57, v57\n" \
"v_exp_f32 v58, v58\n" \
"v_accvgpr_read_b32 v240, a53\n" \
"v_mfma_f32_16x16x32_bf16 a[84:87], a[232:235], v[96:99], a[84:87]\n" \
"s_xor_b32 s73, s73, 0x180\n" \
"v_exp_f32 v59, v59\n" \
"v_mfma_f32_16x16x32_bf16 a[84:87], a[236:239], v[100:103], a[84:87]\n" \
"ds_write_b128 v232, v[148:151] offset:5120\n" \
"v_exp_f32 v60, v60\n" \
"v_cvt_pk_bf16_f32 v52, v56, v57\n" \
"v_exp_f32 v61, v61\n" \
"v_mfma_f32_16x16x32_bf16 a[88:91], a[32:35], v[96:99], a[88:91]\n" \
"ds_read_b128 a[232:235], v233 offset:5120\n" \
"ds_read_b128 a[236:239], v233 offset:13312\n" \
"v_mfma_f32_16x16x32_bf16 a[88:91], a[36:39], v[100:103], a[88:91]\n" \
"ds_read_b128 a[240:243], v233 offset:6144\n" \
"ds_read_b128 a[244:247], v233 offset:14336\n" \
"v_exp_f32 v62, v62\n" \
"v_cvt_pk_bf16_f32 v53, v58, v59\n" \
"v_mfma_f32_16x16x32_bf16 a[120:123], a[32:35], v[112:115], a[120:123]\n" \
"ds_read_b128 v[0:3], v233 offset:20480\n" \
"ds_read_b128 v[4:7], v233 offset:28672\n" \
"v_mfma_f32_16x16x32_bf16 a[120:123], a[36:39], v[116:119], a[120:123]\n" \
"ds_read_b128 v[8:11], v233 offset:21504\n" \
"ds_read_b128 v[12:15], v233 offset:29696\n" \
"v_exp_f32 v63, v63\n" \
"v_cvt_pk_bf16_f32 v54, v60, v61\n" \
"v_add_f32 v251, v251, v240\n" \
"v_mfma_f32_16x16x32_bf16 a[124:127], a[40:43], v[112:115], a[124:127]\n" \
"v_cvt_pk_bf16_f32 v55, v62, v63\n" \
"v_mfma_f32_16x16x32_bf16 a[124:127], a[44:47], v[116:119], a[124:127]\n" \
"ds_write_b128 v232, v[152:155] offset:6144\n" \
"ds_write_b64 v249, v[250:251]\n" \
"v_mfma_f32_16x16x32_bf16 a[92:95], a[40:43], v[96:99], a[92:95]\n" \
"v_exp_f32 v16, v16\n" \
"v_mfma_f32_16x16x32_bf16 a[92:95], a[44:47], v[100:103], a[92:95]\n" \
"s_waitcnt lgkmcnt(4)\n" \
"ds_read_b128 v[96:99], v233 offset:22528\n" \
"ds_read_b128 v[100:103], v233 offset:30720\n" \
"v_exp_f32 v17, v17\n" \
"v_xor_b32 v249, 0x200, v249\n" \
"v_mfma_f32_16x16x32_bf16 a[64:67], a[192:195], v[32:35], a[64:67]\n" \
"ds_read_b128 v[104:107], v233 offset:23552\n" \
"ds_read_b128 v[108:111], v233 offset:31744\n" \
"v_mfma_f32_16x16x32_bf16 a[64:67], a[196:199], v[36:39], a[64:67]\n" \
"v_exp_f32 v18, v18\n" \
"v_exp_f32 v19, v19\n" \
"v_accvgpr_read_b32 v200, a59\n" \
"v_accvgpr_read_b32 v202, a60\n" \
"v_mfma_f32_16x16x32_bf16 a[96:99], a[192:195], v[48:51], a[96:99]\n" \
"v_exp_f32 v20, v20\n" \
"v_mfma_f32_16x16x32_bf16 a[96:99], a[196:199], v[52:55], a[96:99]\n" \
"v_cvt_pk_bf16_f32 v16, v16, v17\n" \
"v_cvt_pk_bf16_f32 v17, v18, v19\n" \
"v_accvgpr_read_b32 v204, a61\n" \
"v_xor_b32 v201, 64, v200\n" \
"v_mfma_f32_16x16x32_bf16 a[100:103], a[200:203], v[48:51], a[100:103]\n" \
"v_exp_f32 v21, v21\n" \
"v_mfma_f32_16x16x32_bf16 a[100:103], a[204:207], v[52:55], a[100:103]\n" \
"ds_write_b128 v232, v[156:159] offset:7168\n" \
"v_exp_f32 v22, v22\n" \
"v_xor_b32 v203, 64, v202\n" \
"v_xor_b32 v222, 32, v204\n" \
"v_mfma_f32_16x16x32_bf16 a[68:71], a[200:203], v[32:35], a[68:71]\n" \
"v_exp_f32 v23, v23\n" \
"v_mfma_f32_16x16x32_bf16 a[68:71], a[204:207], v[36:39], a[68:71]\n" \
"s_mul_i32 s76, s80, 393216\n" \
"s_add_u32 s76, s76, s78\n" \
"v_cvt_pk_bf16_f32 v18, v20, v21\n" \
"v_exp_f32 v24, v24\n" \
"v_add_u32 v209, 81920, v204\n" \
"v_add_u32 v210, 81920, v222\n" \
"v_smfmac_f32_16x16x64_bf16 v[224:227], v[244:247], v[32:39], v243\n" \
"s_waitcnt vmcnt(0) lgkmcnt(0)\n" \
"s_barrier\n" \
"v_mfma_f32_16x16x32_bf16 a[72:75], a[208:211], v[32:35], a[72:75]\n" \
"s_add_u32 m0, s91, 81920\n" \
"ds_read_b64_tr_b16 a[32:33], v209 offset:25600\n" \
"ds_read_b64_tr_b16 a[34:35], v209 offset:28672\n" \
"v_mfma_f32_16x16x32_bf16 a[72:75], a[212:215], v[36:39], a[72:75]\n" \
"buffer_load_dwordx4 v197, s[48:51], s76 offen offset:0 lds\n" \
"v_exp_f32 v25, v25\n" \
"v_mfma_f32_16x16x32_bf16 a[104:107], a[208:211], v[48:51], a[104:107]\n" \
"s_and_b32 s15, %1, 7\n" \
"ds_read_b64_tr_b16 a[36:37], v209 offset:37888\n" \
"ds_read_b64_tr_b16 a[38:39], v209 offset:40960\n" \
"v_mfma_f32_16x16x32_bf16 a[104:107], a[212:215], v[52:55], a[104:107]\n" \
"s_add_u32 s77, s76, 24576\n" \
"ds_read_b64_tr_b16 a[40:41], v209 offset:24576\n" \
"ds_read_b64_tr_b16 a[42:43], v209 offset:27648\n" \
"v_mfma_f32_16x16x32_bf16 a[108:111], a[216:219], v[48:51], a[108:111]\n" \
"buffer_load_dwordx4 v198, s[48:51], s76 offen offset:1024 lds\n" \
"v_mfma_f32_16x16x32_bf16 a[108:111], a[220:223], v[52:55], a[108:111]\n" \
"s_lshl3_add_u32 s14, s14, s15\n" \
"ds_read_b64_tr_b16 a[44:45], v209 offset:36864\n" \
"ds_read_b64_tr_b16 a[46:47], v209 offset:39936\n" \
"v_exp_f32 v26, v26\n" \
"v_cvt_pk_bf16_f32 v19, v22, v23\n" \
"v_mfma_f32_16x16x32_bf16 a[76:79], a[216:219], v[32:35], a[76:79]\n" \
"s_xor_b32 s80, s80, 0x8\n" \
"ds_read_b64_tr_b16 a[48:49], v210 offset:24576\n" \
"ds_read_b64_tr_b16 a[50:51], v210 offset:27648\n" \
"v_mfma_f32_16x16x32_bf16 a[76:79], a[220:223], v[36:39], a[76:79]\n" \
"buffer_load_dwordx4 v199, s[48:51], s76 offen offset:2048 lds\n" \
"v_smfmac_f32_16x16x64_bf16 v[228:231], v[244:247], v[48:55], v243\n" \
"ds_read_b64_tr_b16 a[52:53], v210 offset:36864\n" \
"ds_read_b64_tr_b16 a[54:55], v210 offset:39936\n" \
"v_exp_f32 v27, v27\n" \
"v_mfma_f32_16x16x32_bf16 a[80:83], a[224:227], v[32:35], a[80:83]\n" \
"s_add_u32 m0, m0, 3072\n" \
"ds_read_b128 a[0:3], v202 offset:0\n" \
"ds_read_b128 a[4:7], v203 offset:0\n" \
"v_mfma_f32_16x16x32_bf16 a[80:83], a[228:231], v[36:39], a[80:83]\n" \
"buffer_load_dwordx4 v197, s[48:51], s77 offen offset:0 lds\n" \
"v_mfma_f32_16x16x32_bf16 a[112:115], a[224:227], v[48:51], a[112:115]\n" \
"ds_read_b128 a[8:11], v200 offset:40960\n" \
"ds_read_b128 a[12:15], v201 offset:40960\n" \
"v_mfma_f32_16x16x32_bf16 a[112:115], a[228:231], v[52:55], a[112:115]\n" \
"ds_read_b128 a[16:19], v200 offset:45056\n" \
"ds_read_b128 a[20:23], v201 offset:45056\n" \
"v_exp_f32 v28, v28\n" \
"v_cvt_pk_bf16_f32 v20, v24, v25\n" \
"v_mfma_f32_16x16x32_bf16 a[116:119], a[232:235], v[48:51], a[116:119]\n" \
"buffer_load_dwordx4 v198, s[48:51], s77 offen offset:1024 lds\n" \
"v_mfma_f32_16x16x32_bf16 a[116:119], a[236:239], v[52:55], a[116:119]\n" \
"ds_read_b128 a[24:27], v200 offset:49152\n" \
"ds_read_b128 a[28:31], v201 offset:49152\n" \
"v_mfma_f32_16x16x32_bf16 a[84:87], a[232:235], v[32:35], a[84:87]\n" \
"ds_read_b64 v[250:251], v249\n" \
"ds_read_b128 v[128:131], v248 offset:0\n" \
"v_mfma_f32_16x16x32_bf16 a[84:87], a[236:239], v[36:39], a[84:87]\n" \
"buffer_load_dwordx4 v199, s[48:51], s77 offen offset:2048 lds\n" \
"v_exp_f32 v29, v29\n" \
"v_mfma_f32_16x16x32_bf16 a[88:91], a[240:243], v[32:35], a[88:91]\n" \
"ds_read_b128 v[132:135], v248 offset:1024\n" \
"ds_read_b128 v[136:139], v248 offset:2048\n" \
"v_mfma_f32_16x16x32_bf16 a[88:91], a[244:247], v[36:39], a[88:91]\n" \
"s_add_u32 m0, s79, 65536\n" \
"ds_read_b128 v[140:143], v248 offset:3072\n" \
"ds_read_b128 v[144:147], v248 offset:4096\n" \
"v_exp_f32 v30, v30\n" \
"v_mfma_f32_16x16x32_bf16 a[120:123], a[240:243], v[48:51], a[120:123]\n" \
"buffer_load_dwordx4 v192, s[44:47], s73 offen offset:0 lds\n" \
"v_mfma_f32_16x16x32_bf16 a[120:123], a[244:247], v[52:55], a[120:123]\n" \
"ds_read_b128 v[148:151], v248 offset:5120\n" \
"ds_read_b128 v[152:155], v248 offset:6144\n" \
"v_cvt_pk_bf16_f32 v21, v26, v27\n" \
"v_mfma_f32_16x16x32_bf16 a[124:127], a[248:251], v[48:51], a[124:127]\n" \
"ds_read_b128 v[156:159], v248 offset:7168\n" \
"v_cvt_pk_bf16_f32 v22, v28, v29\n" \
"v_mfma_f32_16x16x32_bf16 a[124:127], a[252:255], v[52:55], a[124:127]\n" \
"buffer_load_dwordx4 v193, s[44:47], s73 offen offset:1024 lds\n" \
"v_exp_f32 v31, v31\n" \
"v_mfma_f32_16x16x32_bf16 a[92:95], a[248:251], v[32:35], a[92:95]\n" \
"v_cvt_pk_bf16_f32 v23, v30, v31\n" \
"v_mov_b32 v218, s60\n" \
"v_mfma_f32_16x16x32_bf16 a[92:95], a[252:255], v[36:39], a[92:95]\n" \
"v_mov_b32 v219, s14\n" \
"v_lshlrev_b32 v220, 28, s61\n" \
"v_mfma_f32_16x16x32_bf16 a[96:99], v[64:67], v[16:19], a[96:99]\n" \
"buffer_load_dwordx4 v192, s[44:47], s73 offen offset:2048 lds\n" \
"v_mfma_f32_16x16x32_bf16 a[96:99], v[68:71], v[20:23], a[96:99]\n" \
"s_lshl3_add_u32 s15, s15, 65593856\n" \
"v_mov_b32 v170, 0x3f80\n" \
"v_mov_b32 v160, 0\n" \
"v_mfma_f32_16x16x32_bf16 a[100:103], v[72:75], v[16:19], a[100:103]\n" \
"s_lshr_b32 s14, %1, 3\n" \
"v_mov_b32 v161, 0\n" \
"v_mov_b32 v164, 0\n" \
"v_mfma_f32_16x16x32_bf16 a[100:103], v[76:79], v[20:23], a[100:103]\n" \
"buffer_load_dwordx4 v193, s[44:47], s73 offen offset:3072 lds\n" \
"v_smfmac_f32_16x16x64_bf16 v[228:231], v[244:247], v[16:23], v243\n" \
"s_cmp_eq_u32 s14, 3\n" \
"s_cselect_b32 s15, s15, 0x20000000\n" \
"v_mov_b32 v165, 0\n" \
"v_mfma_f32_16x16x32_bf16 a[104:107], v[80:83], v[16:19], a[104:107]\n" \
"buffer_store_dwordx2 v[218:219], v220, s[56:59], s15 offen sc1\n" \
"v_mfma_f32_16x16x32_bf16 a[104:107], v[84:87], v[20:23], a[104:107]\n" \
"s_xor_b32 s73, s73, 0x400\n" \
"v_lshrrev_b32 v168, 8, v234\n" \
"v_bfe_u32 v169, v234, 6, 2\n" \
"v_mfma_f32_16x16x32_bf16 a[108:111], v[88:91], v[16:19], a[108:111]\n" \
"v_cmp_eq_u32 s[16:17], v168, v169\n" \
"v_and_b32 v168, 16, v234\n" \
"v_mfma_f32_16x16x32_bf16 a[108:111], v[92:95], v[20:23], a[108:111]\n" \
"v_lshlrev_b32 v168, v168, v170\n" \
"v_cndmask_b32 v168, 0, v168, s[16:17]\n" \
"v_mfma_f32_16x16x32_bf16 a[112:115], v[0:3], v[16:19], a[112:115]\n" \
"Lpg7360_%=:\n" \
"s_cbranch_execz Lcb0_%=\n" \
"v_bfe_i32 v169, v234, 5, 1\n" \
"v_and_b32 v163, v168, v169\n" \
"v_mfma_f32_16x16x32_bf16 a[112:115], v[4:7], v[20:23], a[112:115]\n" \
"v_xor_b32 v162, v168, v163\n" \
"v_mfma_f32_16x16x32_bf16 a[116:119], v[8:11], v[16:19], a[116:119]\n" \
"v_mfma_f32_16x16x32_bf16 a[116:119], v[12:15], v[20:23], a[116:119]\n" \
"v_mfma_f32_16x16x32_bf16 a[120:123], v[96:99], v[16:19], a[120:123]\n" \
"v_mfma_f32_16x16x32_bf16 a[120:123], v[100:103], v[20:23], a[120:123]\n" \
"v_mfma_f32_16x16x32_bf16 a[124:127], v[104:107], v[16:19], a[124:127]\n" \
"v_mfma_f32_16x16x32_bf16 a[124:127], v[108:111], v[20:23], a[124:127]\n" \
"s_waitcnt lgkmcnt(0)\n" \
"s_barrier\n" \
"v_mfma_f32_16x16x32_bf16 v[32:35], a[32:35], a[0:3], 0\n" \
"s_add_u32 m0, s79, 0\n" \
"ds_read_b128 a[192:195], v200 offset:53248\n" \
"v_mfma_f32_16x16x32_bf16 v[32:35], a[36:39], a[4:7], v[32:35]\n" \
"buffer_load_dwordx4 v192, s[44:47], s73 offen offset:0 lds\n" \
"v_mfma_f32_16x16x32_bf16 v[40:43], a[32:35], a[8:11], 0\n" \
"ds_read_b128 a[196:199], v201 offset:53248\n" \
"v_add_u32 v205, 0x10000, v200\n" \
"v_mfma_f32_16x16x32_bf16 v[40:43], a[36:39], a[12:15], v[40:43]\n" \
"buffer_load_dwordx4 v193, s[44:47], s73 offen offset:1024 lds\n" \
"v_mfma_f32_16x16x32_bf16 a[64:67], v[162:165], v[128:131], a[64:67]\n" \
"v_mfma_f32_16x16x32_bf16 a[68:71], v[160:163], v[128:131], a[68:71]\n" \
"v_mfma_f32_16x16x32_bf16 a[72:75], v[162:165], v[132:135], a[72:75]\n" \
"v_mfma_f32_16x16x32_bf16 a[76:79], v[160:163], v[132:135], a[76:79]\n" \
"v_add_f32 v248, v224, v225\n" \
"v_add_f32 v249, v228, v229\n" \
"v_mfma_f32_16x16x32_bf16 v[0:3], a[40:43], a[8:11], 0\n" \
"ds_read_b64_tr_b16 a[200:201], v210 offset:25600\n" \
"ds_read_b64_tr_b16 a[202:203], v210 offset:28672\n" \
"v_mfma_f32_16x16x32_bf16 v[0:3], a[44:47], a[12:15], v[0:3]\n" \
"buffer_load_dwordx4 v192, s[44:47], s73 offen offset:2048 lds\n" \
"v_mfma_f32_16x16x32_bf16 v[4:7], a[48:51], a[8:11], 0\n" \
"ds_read_b64_tr_b16 a[204:205], v210 offset:37888\n" \
"ds_read_b64_tr_b16 a[206:207], v210 offset:40960\n" \
"v_mfma_f32_16x16x32_bf16 v[4:7], a[52:55], a[12:15], v[4:7]\n" \
"buffer_load_dwordx4 v193, s[44:47], s73 offen offset:3072 lds\n" \
"v_mfma_f32_16x16x32_bf16 v[12:15], a[48:51], a[16:19], 0\n" \
"s_xor_b32 s73, s73, 0x500\n" \
"s_mul_i32 s76, s80, 393216\n" \
"s_add_u32 s76, s76, s78\n" \
"s_add_u32 m0, s91, 16384\n" \
"ds_read_b64_tr_b16 a[208:209], v209 offset:26624\n" \
"ds_read_b64_tr_b16 a[210:211], v209 offset:29696\n" \
"v_add_f32 v248, v248, v250\n" \
"v_mfma_f32_16x16x32_bf16 v[12:15], a[52:55], a[20:23], v[12:15]\n" \
"buffer_load_dwordx4 v197, s[48:51], s76 offen offset:0 lds\n" \
"v_mfma_f32_16x16x32_bf16 v[8:11], a[40:43], a[16:19], 0\n" \
"s_add_u32 s77, s76, 24576\n" \
"ds_read_b64_tr_b16 a[212:213], v209 offset:38912\n" \
"ds_read_b64_tr_b16 a[214:215], v209 offset:41984\n" \
"v_mfma_f32_16x16x32_bf16 v[8:11], a[44:47], a[20:23], v[8:11]\n" \
"buffer_load_dwordx4 v198, s[48:51], s76 offen offset:1024 lds\n" \
"v_mfma_f32_16x16x32_bf16 v[48:51], a[32:35], a[16:19], 0\n" \
"s_xor_b32 s80, s80, 0xa\n" \
"ds_read_b64_tr_b16 a[216:217], v210 offset:26624\n" \
"v_add_u32 v206, 0x10000, v201\n" \
"v_mfma_f32_16x16x32_bf16 v[48:51], a[36:39], a[20:23], v[48:51]\n" \
"buffer_load_dwordx4 v199, s[48:51], s76 offen offset:2048 lds\n" \
"v_mfma_f32_16x16x32_bf16 v[56:59], a[32:35], a[24:27], 0\n" \
"s_add_u32 m0, m0, 3072\n" \
"ds_read_b64_tr_b16 a[218:219], v210 offset:29696\n" \
"v_add_u32 v207, 0x20000, v200\n" \
"v_mfma_f32_16x16x32_bf16 v[56:59], a[36:39], a[28:31], v[56:59]\n" \
"buffer_load_dwordx4 v197, s[48:51], s77 offen offset:0 lds\n" \
"v_add_f32 v249, v249, v251\n" \
"v_mfma_f32_16x16x32_bf16 v[16:19], a[40:43], a[24:27], 0\n" \
"ds_read_b64_tr_b16 a[220:221], v210 offset:38912\n" \
"v_add_u32 v208, 0x20000, v201\n" \
"v_mfma_f32_16x16x32_bf16 v[16:19], a[44:47], a[28:31], v[16:19]\n" \
"buffer_load_dwordx4 v198, s[48:51], s77 offen offset:1024 lds\n" \
"v_mfma_f32_16x16x32_bf16 v[20:23], a[48:51], a[24:27], 0\n" \
"s_waitcnt lgkmcnt(2)\n" \
"ds_read_b64_tr_b16 a[222:223], v210 offset:41984\n" \
"v_mfma_f32_16x16x32_bf16 v[20:23], a[52:55], a[28:31], v[20:23]\n" \
"buffer_load_dwordx4 v199, s[48:51], s77 offen offset:2048 lds\n" \
"v_mfma_f32_16x16x32_bf16 v[28:31], a[48:51], a[192:195], 0\n" \
"v_rcp_f32 v232, v248\n" \
"v_mfma_f32_16x16x32_bf16 v[28:31], a[52:55], a[196:199], v[28:31]\n" \
"v_rcp_f32 v233, v249\n" \
"v_add_u32 v240, 0x1000, v236\n" \
"v_mfma_f32_16x16x32_bf16 v[24:27], a[40:43], a[192:195], 0\n" \
"v_add_f32 v224, v248, v249\n" \
"v_mul_f32 v224, 0x43800000, v224\n" \
"v_mfma_f32_16x16x32_bf16 v[24:27], a[44:47], a[196:199], v[24:27]\n" \
"v_add_f32 v248, v232, v233\n" \
"v_fmac_f32 v224, 0x5f800000, v248\n" \
"v_mfma_f32_16x16x32_bf16 v[64:67], a[32:35], a[192:195], 0\n" \
"v_mov_b32 v225, 0\n" \
"v_mov_b32 v226, 0\n" \
"v_mfma_f32_16x16x32_bf16 v[64:67], a[36:39], a[196:199], v[64:67]\n" \
"s_waitcnt vmcnt(11) lgkmcnt(0)\n" \
"s_barrier\n" \
"v_mfma_f32_16x16x32_bf16 v[68:71], a[200:203], a[192:195], 0\n" \
"ds_read_b64_tr_b16 a[32:33], v209 offset:1024\n" \
"ds_read_b64_tr_b16 a[34:35], v209 offset:4096\n" \
"v_mfma_f32_16x16x32_bf16 v[68:71], a[204:207], a[196:199], v[68:71]\n" \
"ds_read_b64_tr_b16 a[36:37], v209 offset:13312\n" \
"ds_read_b64_tr_b16 a[38:39], v209 offset:16384\n" \
"v_mfma_f32_16x16x32_bf16 v[60:63], a[200:203], a[24:27], 0\n" \
"s_add_u32 m0, s79, 40960\n" \
"ds_read_b64_tr_b16 a[40:41], v209 offset:0\n" \
"v_mov_b32 v227, 0\n" \
"v_mfma_f32_16x16x32_bf16 v[60:63], a[204:207], a[28:31], v[60:63]\n" \
"buffer_load_dwordx4 v192, s[44:47], s73 offen offset:0 lds\n" \
"v_accvgpr_read_b32 v112, a64\n" \
"v_mfma_f32_16x16x32_bf16 v[52:55], a[200:203], a[16:19], 0\n" \
"ds_read_b64_tr_b16 a[42:43], v209 offset:3072\n" \
"v_accvgpr_read_b32 v113, a65\n" \
"v_mfma_f32_16x16x32_bf16 v[52:55], a[204:207], a[20:23], v[52:55]\n" \
"ds_read_b64_tr_b16 a[44:45], v209 offset:12288\n" \
"v_accvgpr_read_b32 v114, a66\n" \
"v_mfma_f32_16x16x32_bf16 v[44:47], a[200:203], a[8:11], 0\n" \
"ds_read_b64_tr_b16 a[46:47], v209 offset:15360\n" \
"v_accvgpr_read_b32 v115, a67\n" \
"v_mfma_f32_16x16x32_bf16 v[44:47], a[204:207], a[12:15], v[44:47]\n" \
"buffer_load_dwordx4 v193, s[44:47], s73 offen offset:1024 lds\n" \
"v_mfma_f32_16x16x32_bf16 v[72:75], a[0:3], a[208:211], 0\n" \
"ds_read_b64_tr_b16 a[48:49], v210 offset:0\n" \
"v_accvgpr_read_b32 v116, a68\n" \
"v_mfma_f32_16x16x32_bf16 v[72:75], a[4:7], a[212:215], v[72:75]\n" \
"ds_read_b128 a[0:3], v202 offset:3072\n" \
"v_accvgpr_read_b32 v117, a69\n" \
"v_accvgpr_read_b32 v118, a70\n" \
"v_mfma_f32_16x16x32_bf16 v[80:83], a[8:11], a[208:211], 0\n" \
"ds_read_b128 a[4:7], v203 offset:3072\n" \
"v_accvgpr_read_b32 v119, a71\n" \
"v_mfma_f32_16x16x32_bf16 v[80:83], a[12:15], a[212:215], v[80:83]\n" \
"buffer_load_dwordx4 v192, s[44:47], s73 offen offset:2048 lds\n" \
"v_mfma_f32_16x16x32_bf16 a[80:83], v[162:165], v[136:139], a[80:83]\n" \
"v_mul_f32 v112, v112, v232\n" \
"v_mul_f32 v113, v113, v232\n" \
"v_mfma_f32_16x16x32_bf16 v[84:87], a[8:11], a[216:219], 0\n" \
"ds_read_b64_tr_b16 a[50:51], v210 offset:3072\n" \
"v_mul_f32 v114, v114, v232\n" \
"v_mfma_f32_16x16x32_bf16 v[84:87], a[12:15], a[220:223], v[84:87]\n" \
"ds_read_b128 a[8:11], v205 offset:0\n" \
"v_mul_f32 v115, v115, v232\n" \
"v_mfma_f32_16x16x32_bf16 a[84:87], v[160:163], v[136:139], a[84:87]\n" \
"v_mul_f32 v116, v116, v232\n" \
"v_mul_f32 v117, v117, v232\n" \
"v_mfma_f32_16x16x32_bf16 v[92:95], a[16:19], a[216:219], 0\n" \
"ds_read_b128 a[12:15], v206 offset:0\n" \
"v_mul_f32 v118, v118, v232\n" \
"v_mfma_f32_16x16x32_bf16 v[92:95], a[20:23], a[220:223], v[92:95]\n" \
"buffer_load_dwordx4 v193, s[44:47], s73 offen offset:3072 lds\n" \
"v_mul_f32 v119, v119, v232\n" \
"v_mfma_f32_16x16x32_bf16 v[88:91], a[16:19], a[208:211], 0\n" \
"s_xor_b32 s73, s73, 0x180\n" \
"s_mul_i32 s76, s80, 393216\n" \
"ds_read_b64_tr_b16 a[52:53], v210 offset:12288\n" \
"v_mfma_f32_16x16x32_bf16 v[88:91], a[20:23], a[212:215], v[88:91]\n" \
"s_add_u32 s76, s76, s78\n" \
"ds_read_b128 a[16:19], v205 offset:4096\n" \
"v_cvt_pk_bf16_f32 v120, v112, v113\n" \
"v_mfma_f32_16x16x32_bf16 v[96:99], a[24:27], a[208:211], 0\n" \
"s_add_u32 m0, s91, 106496\n" \
"ds_read_b128 a[20:23], v206 offset:4096\n" \
"v_cvt_pk_bf16_f32 v121, v114, v115\n" \
"v_mfma_f32_16x16x32_bf16 v[96:99], a[28:31], a[212:215], v[96:99]\n" \
"buffer_load_dwordx4 v197, s[48:51], s76 offen offset:0 lds\n" \
"v_mfma_f32_16x16x32_bf16 v[100:103], a[24:27], a[216:219], 0\n" \
"ds_read_b64_tr_b16 a[54:55], v210 offset:15360\n" \
"v_cvt_pk_bf16_f32 v122, v116, v117\n" \
"v_mfma_f32_16x16x32_bf16 v[100:103], a[28:31], a[220:223], v[100:103]\n" \
"ds_read_b128 a[24:27], v205 offset:8192\n" \
"v_cvt_pk_bf16_f32 v123, v118, v119\n" \
"v_mfma_f32_16x16x32_bf16 v[108:111], a[192:195], a[216:219], 0\n" \
"s_add_u32 s77, s76, 24576\n" \
"ds_read_b128 a[28:31], v206 offset:8192\n" \
"v_mfma_f32_16x16x32_bf16 v[108:111], a[196:199], a[220:223], v[108:111]\n" \
"buffer_load_dwordx4 v198, s[48:51], s76 offen offset:1024 lds\n" \
"v_mfma_f32_16x16x32_bf16 v[104:107], a[192:195], a[208:211], 0\n" \
"s_waitcnt lgkmcnt(3)\n" \
"v_mfma_f32_16x16x32_bf16 v[104:107], a[196:199], a[212:215], v[104:107]\n" \
"buffer_store_dwordx4 v[120:123], v236, s[56:59], s88 offen offset:0 sc1\n" \
"v_mfma_f32_16x16x32_bf16 v[32:35], a[32:35], a[0:3], v[32:35]\n" \
"s_xor_b32 s80, s80, 0x3\n" \
"ds_read_b128 a[192:195], v205 offset:12288\n" \
"v_mfma_f32_16x16x32_bf16 v[32:35], a[36:39], a[4:7], v[32:35]\n" \
"buffer_load_dwordx4 v199, s[48:51], s76 offen offset:2048 lds\n" \
"v_mfma_f32_16x16x32_bf16 v[224:227], v[244:247], v[120:123], v[224:227]\n" \
"v_accvgpr_read_b32 v112, a72\n" \
"v_accvgpr_read_b32 v113, a73\n" \
"v_accvgpr_read_b32 v114, a74\n" \
"v_mfma_f32_16x16x32_bf16 v[40:43], a[32:35], a[8:11], v[40:43]\n" \
"ds_read_b128 a[196:199], v206 offset:12288\n" \
"v_accvgpr_read_b32 v115, a75\n" \
"v_mfma_f32_16x16x32_bf16 v[40:43], a[36:39], a[12:15], v[40:43]\n" \
"s_waitcnt lgkmcnt(2)\n" \
"ds_read_b64_tr_b16 a[200:201], v210 offset:1024\n" \
"v_accvgpr_read_b32 v116, a76\n" \
"v_accvgpr_read_b32 v117, a77\n" \
"v_mfma_f32_16x16x32_bf16 v[0:3], a[40:43], a[8:11], v[0:3]\n" \
"s_add_u32 m0, m0, 3072\n" \
"ds_read_b64_tr_b16 a[202:203], v210 offset:4096\n" \
"v_accvgpr_read_b32 v118, a78\n" \
"v_mfma_f32_16x16x32_bf16 v[0:3], a[44:47], a[12:15], v[0:3]\n" \
"buffer_load_dwordx4 v197, s[48:51], s77 offen offset:0 lds\n" \
"v_mfma_f32_16x16x32_bf16 v[4:7], a[48:51], a[8:11], v[4:7]\n" \
"ds_read_b64_tr_b16 a[204:205], v210 offset:13312\n" \
"v_accvgpr_read_b32 v119, a79\n" \
"v_mfma_f32_16x16x32_bf16 v[4:7], a[52:55], a[12:15], v[4:7]\n" \
"ds_read_b64_tr_b16 a[206:207], v210 offset:16384\n" \
"v_mfma_f32_16x16x32_bf16 a[88:91], v[162:165], v[140:143], a[88:91]\n" \
"v_mul_f32 v112, v112, v232\n" \
"v_mul_f32 v113, v113, v232\n" \
"v_mfma_f32_16x16x32_bf16 v[12:15], a[48:51], a[16:19], v[12:15]\n" \
"ds_read_b64_tr_b16 a[208:209], v209 offset:2048\n" \
"v_mul_f32 v114, v114, v232\n" \
"v_mfma_f32_16x16x32_bf16 v[12:15], a[52:55], a[20:23], v[12:15]\n" \
"buffer_load_dwordx4 v198, s[48:51], s77 offen offset:1024 lds\n" \
"v_mul_f32 v115, v115, v232\n" \
"v_mfma_f32_16x16x32_bf16 a[92:95], v[160:163], v[140:143], a[92:95]\n" \
"v_mul_f32 v116, v116, v232\n" \
"v_mul_f32 v117, v117, v232\n" \
"v_mfma_f32_16x16x32_bf16 v[8:11], a[40:43], a[16:19], v[8:11]\n" \
"ds_read_b64_tr_b16 a[210:211], v209 offset:5120\n" \
"v_mul_f32 v118, v118, v232\n" \
"v_mfma_f32_16x16x32_bf16 v[8:11], a[44:47], a[20:23], v[8:11]\n" \
"ds_read_b64_tr_b16 a[212:213], v209 offset:14336\n" \
"v_mul_f32 v119, v119, v232\n" \
"v_mfma_f32_16x16x32_bf16 v[48:51], a[32:35], a[16:19], v[48:51]\n" \
"ds_read_b64_tr_b16 a[214:215], v209 offset:17408\n" \
"v_cvt_pk_bf16_f32 v124, v112, v113\n" \
"v_mfma_f32_16x16x32_bf16 v[48:51], a[36:39], a[20:23], v[48:51]\n" \
"buffer_load_dwordx4 v199, s[48:51], s77 offen offset:2048 lds\n" \
"v_mfma_f32_16x16x32_bf16 v[56:59], a[32:35], a[24:27], v[56:59]\n" \
"ds_read_b64_tr_b16 a[216:217], v210 offset:2048\n" \
"v_cvt_pk_bf16_f32 v125, v114, v115\n" \
"v_mfma_f32_16x16x32_bf16 v[56:59], a[36:39], a[28:31], v[56:59]\n" \
"ds_read_b64_tr_b16 a[218:219], v210 offset:5120\n" \
"v_cvt_pk_bf16_f32 v126, v116, v117\n" \
"v_mfma_f32_16x16x32_bf16 v[16:19], a[40:43], a[24:27], v[16:19]\n" \
"ds_read_b64_tr_b16 a[220:221], v210 offset:14336\n" \
"v_cvt_pk_bf16_f32 v127, v118, v119\n" \
"v_mfma_f32_16x16x32_bf16 v[16:19], a[44:47], a[28:31], v[16:19]\n" \
"buffer_store_dwordx4 v[124:127], v236, s[56:59], s88 offen offset:2048 sc1\n" \
"v_mfma_f32_16x16x32_bf16 v[20:23], a[48:51], a[24:27], v[20:23]\n" \
"ds_read_b64_tr_b16 a[222:223], v210 offset:17408\n" \
"v_mfma_f32_16x16x32_bf16 v[20:23], a[52:55], a[28:31], v[20:23]\n" \
"s_waitcnt lgkmcnt(3)\n" \
"v_mfma_f32_16x16x32_bf16 v[224:227], v[244:247], v[124:127], v[224:227]\n" \
"v_accvgpr_read_b32 v112, a80\n" \
"v_accvgpr_read_b32 v113, a81\n" \
"v_accvgpr_read_b32 v114, a82\n" \
"v_mfma_f32_16x16x32_bf16 v[28:31], a[48:51], a[192:195], v[28:31]\n" \
"v_accvgpr_read_b32 v115, a83\n" \
"v_accvgpr_read_b32 v116, a84\n" \
"v_mfma_f32_16x16x32_bf16 v[28:31], a[52:55], a[196:199], v[28:31]\n" \
"v_accvgpr_read_b32 v117, a85\n" \
"v_accvgpr_read_b32 v118, a86\n" \
"v_mfma_f32_16x16x32_bf16 v[24:27], a[40:43], a[192:195], v[24:27]\n" \
"v_accvgpr_read_b32 v119, a87\n" \
"v_mfma_f32_16x16x32_bf16 v[24:27], a[44:47], a[196:199], v[24:27]\n" \
"v_mfma_f32_16x16x32_bf16 a[96:99], v[162:165], v[144:147], a[96:99]\n" \
"v_mul_f32 v112, v112, v232\n" \
"v_mul_f32 v113, v113, v232\n" \
"v_mul_f32 v114, v114, v232\n" \
"v_mfma_f32_16x16x32_bf16 v[64:67], a[32:35], a[192:195], v[64:67]\n" \
"v_mul_f32 v115, v115, v232\n" \
"v_mfma_f32_16x16x32_bf16 v[64:67], a[36:39], a[196:199], v[64:67]\n" \
"v_mfma_f32_16x16x32_bf16 a[100:103], v[160:163], v[144:147], a[100:103]\n" \
"s_waitcnt vmcnt(12) lgkmcnt(0)\n" \
"s_barrier\n" \
"v_mfma_f32_16x16x32_bf16 v[68:71], a[200:203], a[192:195], v[68:71]\n" \
"ds_read_b64_tr_b16 a[32:33], v204 offset:17408\n" \
"ds_read_b64_tr_b16 a[34:35], v204 offset:20480\n" \
"v_mfma_f32_16x16x32_bf16 v[68:71], a[204:207], a[196:199], v[68:71]\n" \
"ds_read_b64_tr_b16 a[36:37], v204 offset:29696\n" \
"ds_read_b64_tr_b16 a[38:39], v204 offset:32768\n" \
"v_mfma_f32_16x16x32_bf16 v[60:63], a[200:203], a[24:27], v[60:63]\n" \
"s_add_u32 m0, s79, 65536\n" \
"ds_read_b64_tr_b16 a[40:41], v204 offset:16384\n" \
"v_mul_f32 v116, v116, v232\n" \
"v_mfma_f32_16x16x32_bf16 v[60:63], a[204:207], a[28:31], v[60:63]\n" \
"buffer_load_dwordx4 v192, s[44:47], s73 offen offset:0 lds\n" \
"v_mul_f32 v117, v117, v232\n" \
"v_mfma_f32_16x16x32_bf16 v[52:55], a[200:203], a[16:19], v[52:55]\n" \
"ds_read_b64_tr_b16 a[42:43], v204 offset:19456\n" \
"v_mul_f32 v118, v118, v232\n" \
"v_mfma_f32_16x16x32_bf16 v[52:55], a[204:207], a[20:23], v[52:55]\n" \
"ds_read_b64_tr_b16 a[44:45], v204 offset:28672\n" \
"v_mul_f32 v119, v119, v232\n" \
"v_mfma_f32_16x16x32_bf16 v[44:47], a[200:203], a[8:11], v[44:47]\n" \
"ds_read_b64_tr_b16 a[46:47], v204 offset:31744\n" \
"v_cvt_pk_bf16_f32 v120, v112, v113\n" \
"v_mfma_f32_16x16x32_bf16 v[44:47], a[204:207], a[12:15], v[44:47]\n" \
"buffer_load_dwordx4 v193, s[44:47], s73 offen offset:1024 lds\n" \
"v_mfma_f32_16x16x32_bf16 v[72:75], a[0:3], a[208:211], v[72:75]\n" \
"ds_read_b64_tr_b16 a[48:49], v222 offset:16384\n" \
"v_cvt_pk_bf16_f32 v121, v114, v115\n" \
"v_mfma_f32_16x16x32_bf16 v[72:75], a[4:7], a[212:215], v[72:75]\n" \
"ds_read_b128 a[0:3], v202 offset:19456\n" \
"v_cvt_pk_bf16_f32 v122, v116, v117\n" \
"v_mfma_f32_16x16x32_bf16 v[80:83], a[8:11], a[208:211], v[80:83]\n" \
"ds_read_b128 a[4:7], v203 offset:19456\n" \
"v_cvt_pk_bf16_f32 v123, v118, v119\n" \
"v_mfma_f32_16x16x32_bf16 v[80:83], a[12:15], a[212:215], v[80:83]\n" \
"buffer_load_dwordx4 v192, s[44:47], s73 offen offset:2048 lds\n" \
"v_mfma_f32_16x16x32_bf16 v[84:87], a[8:11], a[216:219], v[84:87]\n" \
"ds_read_b64_tr_b16 a[50:51], v222 offset:19456\n" \
"v_mfma_f32_16x16x32_bf16 v[84:87], a[12:15], a[220:223], v[84:87]\n" \
"ds_read_b128 a[8:11], v200 offset:0\n" \
"buffer_store_dwordx4 v[120:123], v240, s[56:59], s88 offen offset:0 sc1\n" \
"v_mfma_f32_16x16x32_bf16 v[92:95], a[16:19], a[216:219], v[92:95]\n" \
"ds_read_b128 a[12:15], v201 offset:0\n" \
"v_mfma_f32_16x16x32_bf16 v[92:95], a[20:23], a[220:223], v[92:95]\n" \
"buffer_load_dwordx4 v193, s[44:47], s73 offen offset:3072 lds\n" \
"v_mfma_f32_16x16x32_bf16 v[224:227], v[244:247], v[120:123], v[224:227]\n" \
"v_accvgpr_read_b32 v112, a88\n" \
"v_accvgpr_read_b32 v113, a89\n" \
"v_accvgpr_read_b32 v114, a90\n" \
"v_mfma_f32_16x16x32_bf16 v[88:91], a[16:19], a[208:211], v[88:91]\n" \
"s_xor_b32 s73, s73, 0x300\n" \
"s_mul_i32 s76, s80, 393216\n" \
"ds_read_b64_tr_b16 a[52:53], v222 offset:28672\n" \
"v_mfma_f32_16x16x32_bf16 v[88:91], a[20:23], a[212:215], v[88:91]\n" \
"s_add_u32 s76, s76, s78\n" \
"ds_read_b128 a[16:19], v200 offset:4096\n" \
"v_accvgpr_read_b32 v115, a91\n" \
"v_accvgpr_read_b32 v116, a92\n" \
"v_mfma_f32_16x16x32_bf16 v[96:99], a[24:27], a[208:211], v[96:99]\n" \
"s_add_u32 m0, s91, 81920\n" \
"ds_read_b128 a[20:23], v201 offset:4096\n" \
"v_accvgpr_read_b32 v117, a93\n" \
"v_mfma_f32_16x16x32_bf16 v[96:99], a[28:31], a[212:215], v[96:99]\n" \
"buffer_load_dwordx4 v197, s[48:51], s76 offen offset:0 lds\n" \
"v_mfma_f32_16x16x32_bf16 v[100:103], a[24:27], a[216:219], v[100:103]\n" \
"ds_read_b64_tr_b16 a[54:55], v222 offset:31744\n" \
"v_accvgpr_read_b32 v118, a94\n" \
"v_mfma_f32_16x16x32_bf16 v[100:103], a[28:31], a[220:223], v[100:103]\n" \
"ds_read_b128 a[24:27], v200 offset:8192\n" \
"v_accvgpr_read_b32 v119, a95\n" \
"v_mfma_f32_16x16x32_bf16 a[104:107], v[162:165], v[148:151], a[104:107]\n" \
"v_mul_f32 v112, v112, v232\n" \
"v_mul_f32 v113, v113, v232\n" \
"v_mfma_f32_16x16x32_bf16 v[108:111], a[192:195], a[216:219], v[108:111]\n" \
"s_add_u32 s77, s76, 24576\n" \
"ds_read_b128 a[28:31], v201 offset:8192\n" \
"v_mul_f32 v114, v114, v232\n" \
"v_mfma_f32_16x16x32_bf16 v[108:111], a[196:199], a[220:223], v[108:111]\n" \
"buffer_load_dwordx4 v198, s[48:51], s76 offen offset:1024 lds\n" \
"v_mul_f32 v115, v115, v232\n" \
"v_mfma_f32_16x16x32_bf16 a[108:111], v[160:163], v[148:151], a[108:111]\n" \
"v_mul_f32 v116, v116, v232\n" \
"v_mul_f32 v117, v117, v232\n" \
"v_mfma_f32_16x16x32_bf16 v[104:107], a[192:195], a[208:211], v[104:107]\n" \
"v_mul_f32 v118, v118, v232\n" \
"v_mul_f32 v119, v119, v232\n" \
"v_mfma_f32_16x16x32_bf16 v[104:107], a[196:199], a[212:215], v[104:107]\n" \
"s_waitcnt lgkmcnt(2)\n" \
"v_cvt_pk_bf16_f32 v124, v112, v113\n" \
"v_cvt_pk_bf16_f32 v125, v114, v115\n" \
"v_mfma_f32_16x16x32_bf16 v[32:35], a[32:35], a[0:3], v[32:35]\n" \
"s_xor_b32 s80, s80, 0x6\n" \
"ds_read_b128 a[192:195], v200 offset:12288\n" \
"v_cvt_pk_bf16_f32 v126, v116, v117\n" \
"v_mfma_f32_16x16x32_bf16 v[32:35], a[36:39], a[4:7], v[32:35]\n" \
"buffer_load_dwordx4 v199, s[48:51], s76 offen offset:2048 lds\n" \
"v_mfma_f32_16x16x32_bf16 v[40:43], a[32:35], a[8:11], v[40:43]\n" \
"ds_read_b128 a[196:199], v201 offset:12288\n" \
"v_cvt_pk_bf16_f32 v127, v118, v119\n" \
"v_mfma_f32_16x16x32_bf16 v[40:43], a[36:39], a[12:15], v[40:43]\n" \
"ds_read_b64_tr_b16 a[200:201], v222 offset:17408\n" \
"buffer_store_dwordx4 v[124:127], v240, s[56:59], s88 offen offset:2048 sc1\n" \
"v_mfma_f32_16x16x32_bf16 v[0:3], a[40:43], a[8:11], v[0:3]\n" \
"s_add_u32 m0, m0, 3072\n" \
"ds_read_b64_tr_b16 a[202:203], v222 offset:20480\n" \
"v_mfma_f32_16x16x32_bf16 v[0:3], a[44:47], a[12:15], v[0:3]\n" \
"buffer_load_dwordx4 v197, s[48:51], s77 offen offset:0 lds\n" \
"v_mfma_f32_16x16x32_bf16 v[224:227], v[244:247], v[124:127], v[224:227]\n" \
"s_add_u32 s82, s88, 532480\n" \
"v_accvgpr_read_b32 v112, a96\n" \
"v_accvgpr_read_b32 v113, a97\n" \
"v_accvgpr_read_b32 v114, a98\n" \
"v_mfma_f32_16x16x32_bf16 v[4:7], a[48:51], a[8:11], v[4:7]\n" \
"ds_read_b64_tr_b16 a[204:205], v222 offset:29696\n" \
"v_accvgpr_read_b32 v115, a99\n" \
"v_mfma_f32_16x16x32_bf16 v[4:7], a[52:55], a[12:15], v[4:7]\n" \
"ds_read_b64_tr_b16 a[206:207], v222 offset:32768\n" \
"v_accvgpr_read_b32 v116, a100\n" \
"v_mfma_f32_16x16x32_bf16 v[12:15], a[48:51], a[16:19], v[12:15]\n" \
"ds_read_b64_tr_b16 a[208:209], v204 offset:18432\n" \
"v_accvgpr_read_b32 v117, a101\n" \
"v_mfma_f32_16x16x32_bf16 v[12:15], a[52:55], a[20:23], v[12:15]\n" \
"buffer_load_dwordx4 v198, s[48:51], s77 offen offset:1024 lds\n" \
"v_mfma_f32_16x16x32_bf16 v[8:11], a[40:43], a[16:19], v[8:11]\n" \
"ds_read_b64_tr_b16 a[210:211], v204 offset:21504\n" \
"v_accvgpr_read_b32 v118, a102\n" \
"v_mfma_f32_16x16x32_bf16 v[8:11], a[44:47], a[20:23], v[8:11]\n" \
"ds_read_b64_tr_b16 a[212:213], v204 offset:30720\n" \
"v_accvgpr_read_b32 v119, a103\n" \
"v_mfma_f32_16x16x32_bf16 a[112:115], v[162:165], v[152:155], a[112:115]\n" \
"v_mul_f32 v112, v112, v233\n" \
"v_mul_f32 v113, v113, v233\n" \
"v_mul_f32 v114, v114, v233\n" \
"v_mfma_f32_16x16x32_bf16 v[48:51], a[32:35], a[16:19], v[48:51]\n" \
"s_waitcnt lgkmcnt(3)\n" \
"ds_read_b64_tr_b16 a[214:215], v204 offset:33792\n" \
"v_mul_f32 v115, v115, v233\n" \
"v_mfma_f32_16x16x32_bf16 v[48:51], a[36:39], a[20:23], v[48:51]\n" \
"buffer_load_dwordx4 v199, s[48:51], s77 offen offset:2048 lds\n" \
"v_mfma_f32_16x16x32_bf16 a[116:119], v[160:163], v[152:155], a[116:119]\n" \
"v_mul_f32 v116, v116, v233\n" \
"v_mul_f32 v117, v117, v233\n" \
"v_mfma_f32_16x16x32_bf16 v[56:59], a[32:35], a[24:27], v[56:59]\n" \
"ds_read_b64_tr_b16 a[216:217], v222 offset:18432\n" \
"v_mul_f32 v118, v118, v233\n" \
"v_mfma_f32_16x16x32_bf16 v[56:59], a[36:39], a[28:31], v[56:59]\n" \
"ds_read_b64_tr_b16 a[218:219], v222 offset:21504\n" \
"v_mul_f32 v119, v119, v233\n" \
"v_mfma_f32_16x16x32_bf16 v[16:19], a[40:43], a[24:27], v[16:19]\n" \
"ds_read_b64_tr_b16 a[220:221], v222 offset:30720\n" \
"v_cvt_pk_bf16_f32 v120, v112, v113\n" \
"v_mfma_f32_16x16x32_bf16 v[16:19], a[44:47], a[28:31], v[16:19]\n" \
"v_cvt_pk_bf16_f32 v121, v114, v115\n" \
"v_cvt_pk_bf16_f32 v122, v116, v117\n" \
"v_cvt_pk_bf16_f32 v123, v118, v119\n" \
"v_mfma_f32_16x16x32_bf16 v[20:23], a[48:51], a[24:27], v[20:23]\n" \
"ds_read_b64_tr_b16 a[222:223], v222 offset:33792\n" \
"v_mfma_f32_16x16x32_bf16 v[20:23], a[52:55], a[28:31], v[20:23]\n" \
"buffer_store_dwordx4 v[120:123], v236, s[56:59], s82 offen offset:0 sc1\n" \
"v_mfma_f32_16x16x32_bf16 v[28:31], a[48:51], a[192:195], v[28:31]\n" \
"v_mfma_f32_16x16x32_bf16 v[28:31], a[52:55], a[196:199], v[28:31]\n" \
"v_mfma_f32_16x16x32_bf16 v[224:227], v[244:247], v[120:123], v[224:227]\n" \
"v_accvgpr_read_b32 v112, a104\n" \
"v_accvgpr_read_b32 v113, a105\n" \
"v_mfma_f32_16x16x32_bf16 v[24:27], a[40:43], a[192:195], v[24:27]\n" \
"v_accvgpr_read_b32 v114, a106\n" \
"v_accvgpr_read_b32 v115, a107\n" \
"v_mfma_f32_16x16x32_bf16 v[24:27], a[44:47], a[196:199], v[24:27]\n" \
"v_accvgpr_read_b32 v116, a108\n" \
"v_accvgpr_read_b32 v117, a109\n" \
"v_mfma_f32_16x16x32_bf16 v[64:67], a[32:35], a[192:195], v[64:67]\n" \
"v_accvgpr_read_b32 v118, a110\n" \
"v_accvgpr_read_b32 v119, a111\n" \
"v_mfma_f32_16x16x32_bf16 v[64:67], a[36:39], a[196:199], v[64:67]\n" \
"v_mfma_f32_16x16x32_bf16 a[120:123], v[162:165], v[156:159], a[120:123]\n" \
"v_mul_f32 v112, v112, v233\n" \
"s_waitcnt vmcnt(14) lgkmcnt(0)\n" \
"s_barrier\n" \
"v_mfma_f32_16x16x32_bf16 v[68:71], a[200:203], a[192:195], v[68:71]\n" \
"ds_read_b64_tr_b16 a[32:33], v209 offset:25600\n" \
"ds_read_b64_tr_b16 a[34:35], v209 offset:28672\n" \
"v_mfma_f32_16x16x32_bf16 v[68:71], a[204:207], a[196:199], v[68:71]\n" \
"ds_read_b64_tr_b16 a[36:37], v209 offset:37888\n" \
"ds_read_b64_tr_b16 a[38:39], v209 offset:40960\n" \
"v_mfma_f32_16x16x32_bf16 v[60:63], a[200:203], a[24:27], v[60:63]\n" \
"s_add_u32 m0, s79, 0\n" \
"ds_read_b64_tr_b16 a[40:41], v209 offset:24576\n" \
"v_mul_f32 v113, v113, v233\n" \
"v_mfma_f32_16x16x32_bf16 v[60:63], a[204:207], a[28:31], v[60:63]\n" \
"buffer_load_dwordx4 v192, s[44:47], s73 offen offset:0 lds\n" \
"v_mfma_f32_16x16x32_bf16 v[52:55], a[200:203], a[16:19], v[52:55]\n" \
"ds_read_b64_tr_b16 a[42:43], v209 offset:27648\n" \
"v_mul_f32 v114, v114, v233\n" \
"v_mfma_f32_16x16x32_bf16 v[52:55], a[204:207], a[20:23], v[52:55]\n" \
"ds_read_b64_tr_b16 a[44:45], v209 offset:36864\n" \
"v_mul_f32 v115, v115, v233\n" \
"v_mfma_f32_16x16x32_bf16 a[124:127], v[160:163], v[156:159], a[124:127]\n" \
"v_mul_f32 v116, v116, v233\n" \
"v_mul_f32 v117, v117, v233\n" \
"v_mul_f32 v118, v118, v233\n" \
"v_mfma_f32_16x16x32_bf16 v[44:47], a[200:203], a[8:11], v[44:47]\n" \
"ds_read_b64_tr_b16 a[46:47], v209 offset:39936\n" \
"v_mul_f32 v119, v119, v233\n" \
"v_mfma_f32_16x16x32_bf16 v[44:47], a[204:207], a[12:15], v[44:47]\n" \
"buffer_load_dwordx4 v193, s[44:47], s73 offen offset:1024 lds\n" \
"v_mfma_f32_16x16x32_bf16 v[72:75], a[0:3], a[208:211], v[72:75]\n" \
"ds_read_b64_tr_b16 a[48:49], v210 offset:24576\n" \
"v_cvt_pk_bf16_f32 v124, v112, v113\n" \
"v_mfma_f32_16x16x32_bf16 v[72:75], a[4:7], a[212:215], v[72:75]\n" \
"ds_read_b128 a[0:3], v202 offset:1024\n" \
"v_cvt_pk_bf16_f32 v125, v114, v115\n" \
"v_mfma_f32_16x16x32_bf16 v[80:83], a[8:11], a[208:211], v[80:83]\n" \
"ds_read_b128 a[4:7], v203 offset:1024\n" \
"v_cvt_pk_bf16_f32 v126, v116, v117\n" \
"v_mfma_f32_16x16x32_bf16 v[80:83], a[12:15], a[212:215], v[80:83]\n" \
"buffer_load_dwordx4 v192, s[44:47], s73 offen offset:2048 lds\n" \
"v_cvt_pk_bf16_f32 v127, v118, v119\n" \
"v_mfma_f32_16x16x32_bf16 v[84:87], a[8:11], a[216:219], v[84:87]\n" \
"ds_read_b64_tr_b16 a[50:51], v210 offset:27648\n" \
"v_mfma_f32_16x16x32_bf16 v[84:87], a[12:15], a[220:223], v[84:87]\n" \
"ds_read_b128 a[8:11], v200 offset:40960\n" \
"buffer_store_dwordx4 v[124:127], v236, s[56:59], s82 offen offset:2048 sc1\n" \
"v_mfma_f32_16x16x32_bf16 v[92:95], a[16:19], a[216:219], v[92:95]\n" \
"ds_read_b128 a[12:15], v201 offset:40960\n" \
"v_mfma_f32_16x16x32_bf16 v[92:95], a[20:23], a[220:223], v[92:95]\n" \
"buffer_load_dwordx4 v193, s[44:47], s73 offen offset:3072 lds\n" \
"v_mfma_f32_16x16x32_bf16 v[224:227], v[244:247], v[124:127], v[224:227]\n" \
"v_accvgpr_read_b32 v112, a112\n" \
"v_accvgpr_read_b32 v113, a113\n" \
"v_mfma_f32_16x16x32_bf16 v[88:91], a[16:19], a[208:211], v[88:91]\n" \
"s_xor_b32 s73, s73, 0x80\n" \
"s_mul_i32 s76, s80, 393216\n" \
"ds_read_b64_tr_b16 a[52:53], v210 offset:36864\n" \
"v_mfma_f32_16x16x32_bf16 v[88:91], a[20:23], a[212:215], v[88:91]\n" \
"s_add_u32 s76, s76, s78\n" \
"ds_read_b128 a[16:19], v200 offset:45056\n" \
"v_accvgpr_read_b32 v114, a114\n" \
"v_mfma_f32_16x16x32_bf16 v[96:99], a[24:27], a[208:211], v[96:99]\n" \
"s_add_u32 m0, s91, 16384\n" \
"ds_read_b128 a[20:23], v201 offset:45056\n" \
"v_accvgpr_read_b32 v115, a115\n" \
"v_mfma_f32_16x16x32_bf16 v[96:99], a[28:31], a[212:215], v[96:99]\n" \
"buffer_load_dwordx4 v197, s[48:51], s76 offen offset:0 lds\n" \
"v_accvgpr_read_b32 v116, a116\n" \
"v_mfma_f32_16x16x32_bf16 v[100:103], a[24:27], a[216:219], v[100:103]\n" \
"ds_read_b64_tr_b16 a[54:55], v210 offset:39936\n" \
"v_accvgpr_read_b32 v117, a117\n" \
"v_mfma_f32_16x16x32_bf16 v[100:103], a[28:31], a[220:223], v[100:103]\n" \
"ds_read_b128 a[24:27], v200 offset:49152\n" \
"v_accvgpr_read_b32 v118, a118\n" \
"v_mfma_f32_16x16x32_bf16 v[108:111], a[192:195], a[216:219], v[108:111]\n" \
"s_add_u32 s77, s76, 24576\n" \
"ds_read_b128 a[28:31], v201 offset:49152\n" \
"v_accvgpr_read_b32 v119, a119\n" \
"v_mfma_f32_16x16x32_bf16 v[108:111], a[196:199], a[220:223], v[108:111]\n" \
"buffer_load_dwordx4 v198, s[48:51], s76 offen offset:1024 lds\n" \
"v_mfma_f32_16x16x32_bf16 v[104:107], a[192:195], a[208:211], v[104:107]\n" \
"v_mul_f32 v112, v112, v233\n" \
"v_mul_f32 v113, v113, v233\n" \
"v_mfma_f32_16x16x32_bf16 v[104:107], a[196:199], a[212:215], v[104:107]\n" \
"s_waitcnt lgkmcnt(2)\n" \
"v_mul_f32 v114, v114, v233\n" \
"v_mul_f32 v115, v115, v233\n" \
"v_mul_f32 v116, v116, v233\n" \
"v_mfma_f32_16x16x32_bf16 v[32:35], a[32:35], a[0:3], v[32:35]\n" \
"s_xor_b32 s80, s80, 0x1\n" \
"ds_read_b128 a[192:195], v200 offset:53248\n" \
"v_mul_f32 v117, v117, v233\n" \
"v_mfma_f32_16x16x32_bf16 v[32:35], a[36:39], a[4:7], v[32:35]\n" \
"buffer_load_dwordx4 v199, s[48:51], s76 offen offset:2048 lds\n" \
"v_mfma_f32_16x16x32_bf16 v[40:43], a[32:35], a[8:11], v[40:43]\n" \
"ds_read_b128 a[196:199], v201 offset:53248\n" \
"v_mul_f32 v118, v118, v233\n" \
"v_mfma_f32_16x16x32_bf16 v[40:43], a[36:39], a[12:15], v[40:43]\n" \
"ds_read_b64_tr_b16 a[200:201], v210 offset:25600\n" \
"v_mul_f32 v119, v119, v233\n" \
"v_mfma_f32_16x16x32_bf16 v[0:3], a[40:43], a[8:11], v[0:3]\n" \
"s_add_u32 m0, m0, 3072\n" \
"ds_read_b64_tr_b16 a[202:203], v210 offset:28672\n" \
"v_cvt_pk_bf16_f32 v120, v112, v113\n" \
"v_mfma_f32_16x16x32_bf16 v[0:3], a[44:47], a[12:15], v[0:3]\n" \
"buffer_load_dwordx4 v197, s[48:51], s77 offen offset:0 lds\n" \
"v_cvt_pk_bf16_f32 v121, v114, v115\n" \
"v_mfma_f32_16x16x32_bf16 v[4:7], a[48:51], a[8:11], v[4:7]\n" \
"ds_read_b64_tr_b16 a[204:205], v210 offset:37888\n" \
"v_cvt_pk_bf16_f32 v122, v116, v117\n" \
"v_mfma_f32_16x16x32_bf16 v[4:7], a[52:55], a[12:15], v[4:7]\n" \
"ds_read_b64_tr_b16 a[206:207], v210 offset:40960\n" \
"v_cvt_pk_bf16_f32 v123, v118, v119\n" \
"v_mfma_f32_16x16x32_bf16 v[12:15], a[48:51], a[16:19], v[12:15]\n" \
"ds_read_b64_tr_b16 a[208:209], v209 offset:26624\n" \
"v_mfma_f32_16x16x32_bf16 v[12:15], a[52:55], a[20:23], v[12:15]\n" \
"buffer_load_dwordx4 v198, s[48:51], s77 offen offset:1024 lds\n" \
"v_mfma_f32_16x16x32_bf16 v[8:11], a[40:43], a[16:19], v[8:11]\n" \
"Lpg7920_%=:\n" \
"Lst1x0_%=:\n" \
"s_cbranch_execz Lpg10204_%=\n" \
"ds_read_b64_tr_b16 a[210:211], v209 offset:29696\n" \
"v_mfma_f32_16x16x32_bf16 v[8:11], a[44:47], a[20:23], v[8:11]\n" \
"ds_read_b64_tr_b16 a[212:213], v209 offset:38912\n" \
"buffer_store_dwordx4 v[120:123], v240, s[56:59], s82 offen offset:0 sc1\n" \
"v_mfma_f32_16x16x32_bf16 v[48:51], a[32:35], a[16:19], v[48:51]\n" \
"s_waitcnt lgkmcnt(3)\n" \
"ds_read_b64_tr_b16 a[214:215], v209 offset:41984\n" \
"v_mfma_f32_16x16x32_bf16 v[48:51], a[36:39], a[20:23], v[48:51]\n" \
"buffer_load_dwordx4 v199, s[48:51], s77 offen offset:2048 lds\n" \
"v_mfma_f32_16x16x32_bf16 v[224:227], v[244:247], v[120:123], v[224:227]\n" \
"v_accvgpr_read_b32 v112, a120\n" \
"v_accvgpr_read_b32 v113, a121\n" \
"v_accvgpr_read_b32 v114, a122\n" \
"v_mfma_f32_16x16x32_bf16 v[56:59], a[32:35], a[24:27], v[56:59]\n" \
"ds_read_b64_tr_b16 a[216:217], v210 offset:26624\n" \
"v_accvgpr_read_b32 v115, a123\n" \
"v_mfma_f32_16x16x32_bf16 v[56:59], a[36:39], a[28:31], v[56:59]\n" \
"ds_read_b64_tr_b16 a[218:219], v210 offset:29696\n" \
"v_accvgpr_read_b32 v116, a124\n" \
"v_mfma_f32_16x16x32_bf16 v[16:19], a[40:43], a[24:27], v[16:19]\n" \
"ds_read_b64_tr_b16 a[220:221], v210 offset:38912\n" \
"v_accvgpr_read_b32 v117, a125\n" \
"v_mfma_f32_16x16x32_bf16 v[16:19], a[44:47], a[28:31], v[16:19]\n" \
"v_accvgpr_read_b32 v118, a126\n" \
"v_accvgpr_read_b32 v119, a127\n" \
"v_mfma_f32_16x16x32_bf16 v[20:23], a[48:51], a[24:27], v[20:23]\n" \
"ds_read_b64_tr_b16 a[222:223], v210 offset:41984\n" \
"v_mul_f32 v112, v112, v233\n" \
"v_mfma_f32_16x16x32_bf16 v[20:23], a[52:55], a[28:31], v[20:23]\n" \
"v_mul_f32 v113, v113, v233\n" \
"v_mul_f32 v114, v114, v233\n" \
"v_mul_f32 v115, v115, v233\n" \
"v_mfma_f32_16x16x32_bf16 v[28:31], a[48:51], a[192:195], v[28:31]\n" \
"v_mul_f32 v116, v116, v233\n" \
"v_mul_f32 v117, v117, v233\n" \
"v_mfma_f32_16x16x32_bf16 v[28:31], a[52:55], a[196:199], v[28:31]\n" \
"v_mul_f32_e64 v118, v118, v233\n" \
"v_mul_f32_e64 v119, v119, v233\n" \
"v_mfma_f32_16x16x32_bf16 v[24:27], a[40:43], a[192:195], v[24:27]\n" \
"v_cvt_pk_bf16_f32 v124, v112, v113\n" \
"v_cvt_pk_bf16_f32 v125, v114, v115\n" \
"v_mfma_f32_16x16x32_bf16 v[24:27], a[44:47], a[196:199], v[24:27]\n" \
"v_cvt_pk_bf16_f32 v126, v116, v117\n" \
"v_cvt_pk_bf16_f32 v127, v118, v119\n" \
"v_mfma_f32_16x16x32_bf16 v[64:67], a[32:35], a[192:195], v[64:67]\n" \
"buffer_store_dwordx4 v[124:127], v240, s[56:59], s82 offen offset:2048 sc1\n" \
"v_mfma_f32_16x16x32_bf16 v[64:67], a[36:39], a[196:199], v[64:67]\n" \
"v_mfma_f32_16x16x32_bf16 v[224:227], v[244:247], v[124:127], v[224:227]\n" \
"s_waitcnt vmcnt(14) lgkmcnt(0)\n" \
"s_barrier\n" \
"v_mfma_f32_16x16x32_bf16 v[68:71], a[200:203], a[192:195], v[68:71]\n" \
"ds_read_b64_tr_b16 a[32:33], v209 offset:1024\n" \
"ds_read_b64_tr_b16 a[34:35], v209 offset:4096\n" \
"v_mfma_f32_16x16x32_bf16 v[68:71], a[204:207], a[196:199], v[68:71]\n" \
"ds_read_b64_tr_b16 a[36:37], v209 offset:13312\n" \
"ds_read_b64_tr_b16 a[38:39], v209 offset:16384\n" \
"v_mfma_f32_16x16x32_bf16 v[60:63], a[200:203], a[24:27], v[60:63]\n" \
"s_add_u32 m0, s79, 40960\n" \
"ds_read_b64_tr_b16 a[40:41], v209 offset:0\n" \
"v_mfma_f32_16x16x32_bf16 v[60:63], a[204:207], a[28:31], v[60:63]\n" \
"buffer_load_dwordx4 v192, s[44:47], s73 offen offset:0 lds\n" \
"v_mfma_f32_16x16x32_bf16 v[52:55], a[200:203], a[16:19], v[52:55]\n" \
"ds_read_b64_tr_b16 a[42:43], v209 offset:3072\n" \
"v_mfma_f32_16x16x32_bf16 v[52:55], a[204:207], a[20:23], v[52:55]\n" \
"ds_read_b64_tr_b16 a[44:45], v209 offset:12288\n" \
"v_mfma_f32_16x16x32_bf16 v[44:47], a[200:203], a[8:11], v[44:47]\n" \
"ds_read_b64_tr_b16 a[46:47], v209 offset:15360\n" \
"v_mfma_f32_16x16x32_bf16 v[44:47], a[204:207], a[12:15], v[44:47]\n" \
"buffer_load_dwordx4 v193, s[44:47], s73 offen offset:1024 lds\n" \
"v_mfma_f32_16x16x32_bf16 v[72:75], a[0:3], a[208:211], v[72:75]\n" \
"ds_read_b64_tr_b16 a[48:49], v210 offset:0\n" \
"v_mfma_f32_16x16x32_bf16 v[72:75], a[4:7], a[212:215], v[72:75]\n" \
"ds_read_b128 a[0:3], v202 offset:2048\n" \
"v_mfma_f32_16x16x32_bf16 v[80:83], a[8:11], a[208:211], v[80:83]\n" \
"ds_read_b128 a[4:7], v203 offset:2048\n" \
"v_mfma_f32_16x16x32_bf16 v[80:83], a[12:15], a[212:215], v[80:83]\n" \
"buffer_load_dwordx4 v192, s[44:47], s73 offen offset:2048 lds\n" \
"v_mfma_f32_16x16x32_bf16 v[84:87], a[8:11], a[216:219], v[84:87]\n" \
"ds_read_b64_tr_b16 a[50:51], v210 offset:3072\n" \
"v_mfma_f32_16x16x32_bf16 v[84:87], a[12:15], a[220:223], v[84:87]\n" \
"ds_read_b128 a[8:11], v205 offset:0\n" \
"v_mfma_f32_16x16x32_bf16 v[92:95], a[16:19], a[216:219], v[92:95]\n" \
"ds_read_b128 a[12:15], v206 offset:0\n" \
"v_mfma_f32_16x16x32_bf16 v[92:95], a[20:23], a[220:223], v[92:95]\n" \
"buffer_load_dwordx4 v193, s[44:47], s73 offen offset:3072 lds\n" \
"v_mfma_f32_16x16x32_bf16 v[88:91], a[16:19], a[208:211], v[88:91]\n" \
"s_xor_b32 s73, s73, 0x180\n" \
"s_mul_i32 s76, s80, 393216\n" \
"ds_read_b64_tr_b16 a[52:53], v210 offset:12288\n" \
"v_mfma_f32_16x16x32_bf16 v[88:91], a[20:23], a[212:215], v[88:91]\n" \
"s_add_u32 s76, s76, s78\n" \
"ds_read_b128 a[16:19], v205 offset:4096\n" \
"v_mfma_f32_16x16x32_bf16 v[96:99], a[24:27], a[208:211], v[96:99]\n" \
"s_add_u32 m0, s91, 106496\n" \
"ds_read_b128 a[20:23], v206 offset:4096\n" \
"v_mfma_f32_16x16x32_bf16 v[96:99], a[28:31], a[212:215], v[96:99]\n" \
"buffer_load_dwordx4 v197, s[48:51], s76 offen offset:0 lds\n" \
"v_mfma_f32_16x16x32_bf16 v[100:103], a[24:27], a[216:219], v[100:103]\n" \
"ds_read_b64_tr_b16 a[54:55], v210 offset:15360\n" \
"v_mfma_f32_16x16x32_bf16 v[100:103], a[28:31], a[220:223], v[100:103]\n" \
"ds_read_b128 a[24:27], v205 offset:8192\n" \
"v_mfma_f32_16x16x32_bf16 v[108:111], a[192:195], a[216:219], v[108:111]\n" \
"s_add_u32 s77, s76, 24576\n" \
"ds_read_b128 a[28:31], v206 offset:8192\n" \
"v_mfma_f32_16x16x32_bf16 v[108:111], a[196:199], a[220:223], v[108:111]\n" \
"buffer_load_dwordx4 v198, s[48:51], s76 offen offset:1024 lds\n" \
"v_mfma_f32_16x16x32_bf16 v[104:107], a[192:195], a[208:211], v[104:107]\n" \
"v_mfma_f32_16x16x32_bf16 v[104:107], a[196:199], a[212:215], v[104:107]\n" \
"s_waitcnt lgkmcnt(2)\n" \
"v_mfma_f32_16x16x32_bf16 v[32:35], a[32:35], a[0:3], v[32:35]\n" \
"s_xor_b32 s80, s80, 0x3\n" \
"ds_read_b128 a[192:195], v205 offset:12288\n" \
"v_mfma_f32_16x16x32_bf16 v[32:35], a[36:39], a[4:7], v[32:35]\n" \
"buffer_load_dwordx4 v199, s[48:51], s76 offen offset:2048 lds\n" \
"v_mfma_f32_16x16x32_bf16 v[40:43], a[32:35], a[8:11], v[40:43]\n" \
"ds_read_b128 a[196:199], v206 offset:12288\n" \
"v_mfma_f32_16x16x32_bf16 v[40:43], a[36:39], a[12:15], v[40:43]\n" \
"ds_read_b64_tr_b16 a[200:201], v210 offset:1024\n" \
"v_mfma_f32_16x16x32_bf16 v[0:3], a[40:43], a[8:11], v[0:3]\n" \
"s_add_u32 m0, m0, 3072\n" \
"ds_read_b64_tr_b16 a[202:203], v210 offset:4096\n" \
"v_mfma_f32_16x16x32_bf16 v[0:3], a[44:47], a[12:15], v[0:3]\n" \
"buffer_load_dwordx4 v197, s[48:51], s77 offen offset:0 lds\n" \
"v_mfma_f32_16x16x32_bf16 v[4:7], a[48:51], a[8:11], v[4:7]\n" \
"ds_read_b64_tr_b16 a[204:205], v210 offset:13312\n" \
"v_mfma_f32_16x16x32_bf16 v[4:7], a[52:55], a[12:15], v[4:7]\n" \
"ds_read_b64_tr_b16 a[206:207], v210 offset:16384\n" \
"v_mfma_f32_16x16x32_bf16 v[12:15], a[48:51], a[16:19], v[12:15]\n" \
"ds_read_b64_tr_b16 a[208:209], v209 offset:2048\n" \
"v_mfma_f32_16x16x32_bf16 v[12:15], a[52:55], a[20:23], v[12:15]\n" \
"buffer_load_dwordx4 v198, s[48:51], s77 offen offset:1024 lds\n" \
"v_mfma_f32_16x16x32_bf16 v[8:11], a[40:43], a[16:19], v[8:11]\n" \
"ds_read_b64_tr_b16 a[210:211], v209 offset:5120\n" \
"v_mfma_f32_16x16x32_bf16 v[8:11], a[44:47], a[20:23], v[8:11]\n" \
"ds_read_b64_tr_b16 a[212:213], v209 offset:14336\n" \
"v_mfma_f32_16x16x32_bf16 v[48:51], a[32:35], a[16:19], v[48:51]\n" \
"s_waitcnt lgkmcnt(3)\n" \
"ds_read_b64_tr_b16 a[214:215], v209 offset:17408\n" \
"v_mfma_f32_16x16x32_bf16 v[48:51], a[36:39], a[20:23], v[48:51]\n" \
"buffer_load_dwordx4 v199, s[48:51], s77 offen offset:2048 lds\n" \
"v_mfma_f32_16x16x32_bf16 v[56:59], a[32:35], a[24:27], v[56:59]\n" \
"ds_read_b64_tr_b16 a[216:217], v210 offset:2048\n" \
"v_mfma_f32_16x16x32_bf16 v[56:59], a[36:39], a[28:31], v[56:59]\n" \
"ds_read_b64_tr_b16 a[218:219], v210 offset:5120\n" \
"v_mfma_f32_16x16x32_bf16 v[16:19], a[40:43], a[24:27], v[16:19]\n" \
"ds_read_b64_tr_b16 a[220:221], v210 offset:14336\n" \
"v_mfma_f32_16x16x32_bf16 v[16:19], a[44:47], a[28:31], v[16:19]\n" \
"v_mfma_f32_16x16x32_bf16 v[20:23], a[48:51], a[24:27], v[20:23]\n" \
"ds_read_b64_tr_b16 a[222:223], v210 offset:17408\n" \
"v_mfma_f32_16x16x32_bf16 v[20:23], a[52:55], a[28:31], v[20:23]\n" \
"v_mfma_f32_16x16x32_bf16 v[28:31], a[48:51], a[192:195], v[28:31]\n" \
"v_mfma_f32_16x16x32_bf16 v[28:31], a[52:55], a[196:199], v[28:31]\n" \
"v_mfma_f32_16x16x32_bf16 v[24:27], a[40:43], a[192:195], v[24:27]\n" \
"v_mfma_f32_16x16x32_bf16 v[24:27], a[44:47], a[196:199], v[24:27]\n" \
"v_mfma_f32_16x16x32_bf16 v[64:67], a[32:35], a[192:195], v[64:67]\n" \
"v_mfma_f32_16x16x32_bf16 v[64:67], a[36:39], a[196:199], v[64:67]\n" \
"s_waitcnt vmcnt(11) lgkmcnt(0)\n" \
"s_barrier\n" \
"s_movk_i32 s64, 0x207\n" \
"v_cmp_class_f32 vcc, v224, s64\n" \
"s_nop 1\n" \
"s_cmp_lg_u64 vcc, 0\n" \
"s_addc_u32 s90, s90, 0\n" \
"s_cmp_lg_u32 s90, 0\n" \
"s_cbranch_scc1 Lexact_%=\n" \
"Lexr_%=:\n" \
"v_mfma_f32_16x16x32_bf16 v[68:71], a[200:203], a[192:195], v[68:71]\n" \
"s_lshl_b32 s15, s63, 7\n" \
"ds_read_b64_tr_b16 a[32:33], v204 offset:17408\n" \
"ds_read_b64_tr_b16 a[34:35], v204 offset:20480\n" \
"v_mfma_f32_16x16x32_bf16 v[68:71], a[204:207], a[196:199], v[68:71]\n" \
"s_lshl2_add_u32 s16, s62, 65589248\n" \
"ds_read_b64_tr_b16 a[36:37], v204 offset:29696\n" \
"ds_read_b64_tr_b16 a[38:39], v204 offset:32768\n" \
"v_mfma_f32_16x16x32_bf16 v[60:63], a[200:203], a[24:27], v[60:63]\n" \
"s_add_u32 m0, s79, 65536\n" \
"s_sub_u32 s17, s16, s15\n" \
"ds_read_b64_tr_b16 a[40:41], v204 offset:16384\n" \
"v_mfma_f32_16x16x32_bf16 v[60:63], a[204:207], a[28:31], v[60:63]\n" \
"buffer_load_dwordx4 v192, s[44:47], s73 offen offset:0 lds\n" \
"v_mfma_f32_16x16x32_bf16 v[52:55], a[200:203], a[16:19], v[52:55]\n" \
"s_add_u32 s15, s15, s16\n" \
"s_add_u32 s17, s17, 1920\n" \
"ds_read_b64_tr_b16 a[42:43], v204 offset:19456\n" \
"v_mfma_f32_16x16x32_bf16 v[52:55], a[204:207], a[20:23], v[52:55]\n" \
"ds_read_b64_tr_b16 a[44:45], v204 offset:28672\n" \
"v_mov_b32 v146, s60\n" \
"v_mfma_f32_16x16x32_bf16 v[44:47], a[200:203], a[8:11], v[44:47]\n" \
"ds_read_b64_tr_b16 a[46:47], v204 offset:31744\n" \
"v_lshlrev_b32 v147, 28, s61\n" \
"v_mfma_f32_16x16x32_bf16 v[44:47], a[204:207], a[12:15], v[44:47]\n" \
"buffer_load_dwordx4 v193, s[44:47], s73 offen offset:1024 lds\n" \
"v_mfma_f32_16x16x32_bf16 v[72:75], a[0:3], a[208:211], v[72:75]\n" \
"ds_read_b64_tr_b16 a[48:49], v222 offset:16384\n" \
"v_mfma_f32_16x16x32_bf16 v[72:75], a[4:7], a[212:215], v[72:75]\n" \
"ds_read_b128 a[0:3], v202 offset:4096\n" \
"v_mfma_f32_16x16x32_bf16 v[80:83], a[8:11], a[208:211], v[80:83]\n" \
"ds_read_b128 a[4:7], v203 offset:4096\n" \
"v_mfma_f32_16x16x32_bf16 v[80:83], a[12:15], a[212:215], v[80:83]\n" \
"buffer_load_dwordx4 v192, s[44:47], s73 offen offset:2048 lds\n" \
"v_mfma_f32_16x16x32_bf16 v[84:87], a[8:11], a[216:219], v[84:87]\n" \
"ds_read_b64_tr_b16 a[50:51], v222 offset:19456\n" \
"v_mfma_f32_16x16x32_bf16 v[84:87], a[12:15], a[220:223], v[84:87]\n" \
"ds_read_b128 a[8:11], v200 offset:0\n" \
"v_mfma_f32_16x16x32_bf16 v[92:95], a[16:19], a[216:219], v[92:95]\n" \
"ds_read_b128 a[12:15], v201 offset:0\n" \
"v_mfma_f32_16x16x32_bf16 v[92:95], a[20:23], a[220:223], v[92:95]\n" \
"buffer_load_dwordx4 v193, s[44:47], s73 offen offset:3072 lds\n" \
"v_mfma_f32_16x16x32_bf16 v[88:91], a[16:19], a[208:211], v[88:91]\n" \
"s_xor_b32 s73, s73, 0x80\n" \
"s_mul_i32 s76, s80, 393216\n" \
"ds_read_b64_tr_b16 a[52:53], v222 offset:28672\n" \
"v_mfma_f32_16x16x32_bf16 v[88:91], a[20:23], a[212:215], v[88:91]\n" \
"s_add_u32 s76, s76, s78\n" \
"ds_read_b128 a[16:19], v200 offset:4096\n" \
"v_mfma_f32_16x16x32_bf16 v[96:99], a[24:27], a[208:211], v[96:99]\n" \
"s_add_u32 m0, s91, 81920\n" \
"ds_read_b128 a[20:23], v201 offset:4096\n" \
"v_mfma_f32_16x16x32_bf16 v[96:99], a[28:31], a[212:215], v[96:99]\n" \
"buffer_load_dwordx4 v197, s[48:51], s76 offen offset:0 lds\n" \
"v_mfma_f32_16x16x32_bf16 v[100:103], a[24:27], a[216:219], v[100:103]\n" \
"ds_read_b64_tr_b16 a[54:55], v222 offset:31744\n" \
"v_mfma_f32_16x16x32_bf16 v[100:103], a[28:31], a[220:223], v[100:103]\n" \
"ds_read_b128 a[24:27], v200 offset:8192\n" \
"v_mfma_f32_16x16x32_bf16 v[108:111], a[192:195], a[216:219], v[108:111]\n" \
"s_add_u32 s77, s76, 24576\n" \
"ds_read_b128 a[28:31], v201 offset:8192\n" \
"v_mfma_f32_16x16x32_bf16 v[108:111], a[196:199], a[220:223], v[108:111]\n" \
"buffer_load_dwordx4 v198, s[48:51], s76 offen offset:1024 lds\n" \
"v_mfma_f32_16x16x32_bf16 v[104:107], a[192:195], a[208:211], v[104:107]\n" \
"v_mfma_f32_16x16x32_bf16 v[104:107], a[196:199], a[212:215], v[104:107]\n" \
"s_waitcnt lgkmcnt(2)\n" \
"v_mfma_f32_16x16x32_bf16 v[32:35], a[32:35], a[0:3], v[32:35]\n" \
"s_xor_b32 s80, s80, 0x1\n" \
"ds_read_b128 a[192:195], v200 offset:12288\n" \
"v_mfma_f32_16x16x32_bf16 v[32:35], a[36:39], a[4:7], v[32:35]\n" \
"buffer_load_dwordx4 v199, s[48:51], s76 offen offset:2048 lds\n" \
"v_mfma_f32_16x16x32_bf16 v[40:43], a[32:35], a[8:11], v[40:43]\n" \
"ds_read_b128 a[196:199], v201 offset:12288\n" \
"v_mfma_f32_16x16x32_bf16 v[40:43], a[36:39], a[12:15], v[40:43]\n" \
"ds_read_b64_tr_b16 a[200:201], v222 offset:17408\n" \
"v_mfma_f32_16x16x32_bf16 v[0:3], a[40:43], a[8:11], v[0:3]\n" \
"s_add_u32 m0, m0, 3072\n" \
"ds_read_b64_tr_b16 a[202:203], v222 offset:20480\n" \
"v_mfma_f32_16x16x32_bf16 v[0:3], a[44:47], a[12:15], v[0:3]\n" \
"buffer_load_dwordx4 v197, s[48:51], s77 offen offset:0 lds\n" \
"v_mfma_f32_16x16x32_bf16 v[4:7], a[48:51], a[8:11], v[4:7]\n" \
"ds_read_b64_tr_b16 a[204:205], v222 offset:29696\n" \
"v_mfma_f32_16x16x32_bf16 v[4:7], a[52:55], a[12:15], v[4:7]\n" \
"ds_read_b64_tr_b16 a[206:207], v222 offset:32768\n" \
"v_mfma_f32_16x16x32_bf16 v[12:15], a[48:51], a[16:19], v[12:15]\n" \
"ds_read_b64_tr_b16 a[208:209], v204 offset:18432\n" \
"v_mfma_f32_16x16x32_bf16 v[12:15], a[52:55], a[20:23], v[12:15]\n" \
"buffer_load_dwordx4 v198, s[48:51], s77 offen offset:1024 lds\n" \
"v_mfma_f32_16x16x32_bf16 v[8:11], a[40:43], a[16:19], v[8:11]\n" \
"ds_read_b64_tr_b16 a[210:211], v204 offset:21504\n" \
"v_mfma_f32_16x16x32_bf16 v[8:11], a[44:47], a[20:23], v[8:11]\n" \
"ds_read_b64_tr_b16 a[212:213], v204 offset:30720\n" \
"v_mfma_f32_16x16x32_bf16 v[48:51], a[32:35], a[16:19], v[48:51]\n" \
"s_waitcnt lgkmcnt(3)\n" \
"ds_read_b64_tr_b16 a[214:215], v204 offset:33792\n" \
"v_mfma_f32_16x16x32_bf16 v[48:51], a[36:39], a[20:23], v[48:51]\n" \
"buffer_load_dwordx4 v199, s[48:51], s77 offen offset:2048 lds\n" \
"v_mfma_f32_16x16x32_bf16 v[56:59], a[32:35], a[24:27], v[56:59]\n" \
"ds_read_b64_tr_b16 a[216:217], v222 offset:18432\n" \
"v_mfma_f32_16x16x32_bf16 v[56:59], a[36:39], a[28:31], v[56:59]\n" \
"ds_read_b64_tr_b16 a[218:219], v222 offset:21504\n" \
"v_mfma_f32_16x16x32_bf16 v[16:19], a[40:43], a[24:27], v[16:19]\n" \
"ds_read_b64_tr_b16 a[220:221], v222 offset:30720\n" \
"v_mfma_f32_16x16x32_bf16 v[16:19], a[44:47], a[28:31], v[16:19]\n" \
"v_mfma_f32_16x16x32_bf16 v[20:23], a[48:51], a[24:27], v[20:23]\n" \
"ds_read_b64_tr_b16 a[222:223], v222 offset:33792\n" \
"v_mfma_f32_16x16x32_bf16 v[20:23], a[52:55], a[28:31], v[20:23]\n" \
"v_mfma_f32_16x16x32_bf16 v[28:31], a[48:51], a[192:195], v[28:31]\n" \
"v_mfma_f32_16x16x32_bf16 v[28:31], a[52:55], a[196:199], v[28:31]\n" \
"v_mfma_f32_16x16x32_bf16 v[24:27], a[40:43], a[192:195], v[24:27]\n" \
"v_mfma_f32_16x16x32_bf16 v[24:27], a[44:47], a[196:199], v[24:27]\n" \
"v_mfma_f32_16x16x32_bf16 v[64:67], a[32:35], a[192:195], v[64:67]\n" \
"v_mfma_f32_16x16x32_bf16 v[64:67], a[36:39], a[196:199], v[64:67]\n" \
"s_waitcnt vmcnt(10) lgkmcnt(0)\n" \
"s_barrier\n" \
"v_mfma_f32_16x16x32_bf16 v[68:71], a[200:203], a[192:195], v[68:71]\n" \
"ds_read_b64_tr_b16 a[32:33], v209 offset:25600\n" \
"ds_read_b64_tr_b16 a[34:35], v209 offset:28672\n" \
"v_mfma_f32_16x16x32_bf16 v[68:71], a[204:207], a[196:199], v[68:71]\n" \
"ds_read_b64_tr_b16 a[36:37], v209 offset:37888\n" \
"ds_read_b64_tr_b16 a[38:39], v209 offset:40960\n" \
"v_mfma_f32_16x16x32_bf16 v[60:63], a[200:203], a[24:27], v[60:63]\n" \
"s_add_u32 m0, s79, 0\n" \
"ds_read_b64_tr_b16 a[40:41], v209 offset:24576\n" \
"buffer_store_dword v146, v147, s[56:59], s15 offen sc1\n" \
"v_mfma_f32_16x16x32_bf16 v[60:63], a[204:207], a[28:31], v[60:63]\n" \
"buffer_load_dwordx4 v192, s[44:47], s73 offen offset:0 lds\n" \
"v_mfma_f32_16x16x32_bf16 v[52:55], a[200:203], a[16:19], v[52:55]\n" \
"ds_read_b64_tr_b16 a[42:43], v209 offset:27648\n" \
"v_mfma_f32_16x16x32_bf16 v[52:55], a[204:207], a[20:23], v[52:55]\n" \
"ds_read_b64_tr_b16 a[44:45], v209 offset:36864\n" \
"buffer_store_dword v146, v147, s[56:59], s17 offen sc1\n" \
"v_mfma_f32_16x16x32_bf16 v[44:47], a[200:203], a[8:11], v[44:47]\n" \
"s_mov_b64 s[36:37], s[18:19]\n" \
"ds_read_b64_tr_b16 a[46:47], v209 offset:39936\n" \
"v_mfma_f32_16x16x32_bf16 v[44:47], a[204:207], a[12:15], v[44:47]\n" \
"buffer_load_dwordx4 v193, s[44:47], s73 offen offset:1024 lds\n" \
"v_mfma_f32_16x16x32_bf16 v[72:75], a[0:3], a[208:211], v[72:75]\n" \
"s_sub_u32 s36, s36, 4096\n" \
"s_subb_u32 s37, s37, 0\n" \
"ds_read_b64_tr_b16 a[48:49], v210 offset:24576\n" \
"v_mfma_f32_16x16x32_bf16 v[72:75], a[4:7], a[212:215], v[72:75]\n" \
"s_and_b32 s37, s37, 0xffff\n" \
"s_mov_b32 s38, 8392704\n" \
"ds_read_b128 a[0:3], v202 offset:5120\n" \
"v_mfma_f32_16x16x32_bf16 v[80:83], a[8:11], a[208:211], v[80:83]\n" \
"s_mov_b32 s39, 0x20000\n" \
"s_mov_b64 s[40:41], s[24:25]\n" \
"ds_read_b128 a[4:7], v203 offset:5120\n" \
"v_mfma_f32_16x16x32_bf16 v[80:83], a[12:15], a[212:215], v[80:83]\n" \
"buffer_load_dwordx4 v192, s[44:47], s73 offen offset:2048 lds\n" \
"v_mfma_f32_16x16x32_bf16 v[84:87], a[8:11], a[216:219], v[84:87]\n" \
"s_sub_u32 s40, s40, 4096\n" \
"s_subb_u32 s41, s41, 0\n" \
"ds_read_b64_tr_b16 a[50:51], v210 offset:27648\n" \
"v_mfma_f32_16x16x32_bf16 v[84:87], a[12:15], a[220:223], v[84:87]\n" \
"s_and_b32 s41, s41, 0xffff\n" \
"s_mov_b32 s42, 2101248\n" \
"ds_read_b128 a[8:11], v200 offset:40960\n" \
"v_mfma_f32_16x16x32_bf16 v[92:95], a[16:19], a[216:219], v[92:95]\n" \
"s_mov_b32 s43, 0x20000\n" \
"s_mov_b64 s[52:53], s[26:27]\n" \
"ds_read_b128 a[12:15], v201 offset:40960\n" \
"v_mfma_f32_16x16x32_bf16 v[92:95], a[20:23], a[220:223], v[92:95]\n" \
"buffer_load_dwordx4 v193, s[44:47], s73 offen offset:3072 lds\n" \
"v_mfma_f32_16x16x32_bf16 v[88:91], a[16:19], a[208:211], v[88:91]\n" \
"s_xor_b32 s73, s73, 0x780\n" \
"s_mul_i32 s76, s80, 393216\n" \
"ds_read_b64_tr_b16 a[52:53], v210 offset:36864\n" \
"v_mfma_f32_16x16x32_bf16 v[88:91], a[20:23], a[212:215], v[88:91]\n" \
"s_add_u32 s76, s76, s78\n" \
"s_and_b32 s53, s53, 0xffff\n" \
"ds_read_b128 a[16:19], v200 offset:45056\n" \
"v_mfma_f32_16x16x32_bf16 v[96:99], a[24:27], a[208:211], v[96:99]\n" \
"s_add_u32 m0, s91, 16384\n" \
"s_mov_b32 s54, 8388608\n" \
"ds_read_b128 a[20:23], v201 offset:45056\n" \
"v_mfma_f32_16x16x32_bf16 v[96:99], a[28:31], a[212:215], v[96:99]\n" \
"buffer_load_dwordx4 v197, s[48:51], s76 offen offset:0 lds\n" \
"v_mfma_f32_16x16x32_bf16 v[100:103], a[24:27], a[216:219], v[100:103]\n" \
"s_mov_b32 s55, 0x20000\n" \
"ds_read_b64_tr_b16 a[54:55], v210 offset:39936\n" \
"v_lshrrev_b32 v221, 2, v234\n" \
"v_mfma_f32_16x16x32_bf16 v[100:103], a[28:31], a[220:223], v[100:103]\n" \
"s_waitcnt lgkmcnt(3)\n" \
"ds_read_b128 a[24:27], v200 offset:49152\n" \
"v_lshrrev_b32 v244, 4, v234\n" \
"v_mfma_f32_16x16x32_bf16 v[108:111], a[192:195], a[216:219], v[108:111]\n" \
"s_add_u32 s77, s76, 24576\n" \
"s_lshl_b32 s12, s94, 7\n" \
"ds_read_b128 a[28:31], v201 offset:49152\n" \
"v_mfma_f32_16x16x32_bf16 v[108:111], a[196:199], a[220:223], v[108:111]\n" \
"buffer_load_dwordx4 v198, s[48:51], s76 offen offset:1024 lds\n" \
"v_mfma_f32_16x16x32_bf16 v[104:107], a[192:195], a[208:211], v[104:107]\n" \
"s_add_u32 s12, s12, 65591296\n" \
"s_and_b32 s70, %1, 1\n" \
"s_lshl3_add_u32 s70, s70, s63\n" \
"v_mfma_f32_16x16x32_bf16 v[104:107], a[196:199], a[212:215], v[104:107]\n" \
"s_lshl2_add_u32 s12, s70, s12\n" \
"s_lshr_b32 s68, s63, 1\n" \
"s_add_u32 s68, s68, 2\n" \
"v_mfma_f32_16x16x32_bf16 v[32:35], a[32:35], a[0:3], v[32:35]\n" \
"s_xor_b32 s80, s80, 0xf\n" \
"s_lshr_b32 s70, s18, 8\n" \
"ds_read_b128 a[192:195], v200 offset:53248\n" \
"v_mfma_f32_16x16x32_bf16 v[32:35], a[36:39], a[4:7], v[32:35]\n" \
"buffer_load_dwordx4 v199, s[48:51], s76 offen offset:2048 lds\n" \
"v_mfma_f32_16x16x32_bf16 v[40:43], a[32:35], a[8:11], v[40:43]\n" \
"s_sub_u32 s68, s68, s70\n" \
"s_and_b32 s68, s68, 3\n" \
"ds_read_b128 a[196:199], v201 offset:53248\n" \
"v_mfma_f32_16x16x32_bf16 v[40:43], a[36:39], a[12:15], v[40:43]\n" \
"s_and_b32 s70, s63, 1\n" \
"s_lshl_b32 s70, s70, 2\n" \
"ds_read_b64_tr_b16 a[200:201], v210 offset:25600\n" \
"v_mfma_f32_16x16x32_bf16 v[0:3], a[40:43], a[8:11], v[0:3]\n" \
"s_add_u32 m0, m0, 3072\n" \
"s_or_b32 s68, s68, s70\n" \
"ds_read_b64_tr_b16 a[202:203], v210 offset:28672\n" \
"v_mfma_f32_16x16x32_bf16 v[0:3], a[44:47], a[12:15], v[0:3]\n" \
"s_waitcnt lgkmcnt(3)\n" \
"buffer_load_dwordx4 v197, s[48:51], s77 offen offset:0 lds\n" \
"v_mfma_f32_16x16x32_bf16 v[4:7], a[48:51], a[8:11], v[4:7]\n" \
"s_lshr_b32 s84, s61, 1\n" \
"s_lshl_b32 s84, s84, 16\n" \
"ds_read_b64_tr_b16 a[204:205], v210 offset:37888\n" \
"v_mfma_f32_16x16x32_bf16 v[4:7], a[52:55], a[12:15], v[4:7]\n" \
"s_and_b32 s70, s61, 1\n" \
"s_lshl_b32 s70, s70, 14\n" \
"ds_read_b64_tr_b16 a[206:207], v210 offset:40960\n" \
"v_mfma_f32_16x16x32_bf16 v[12:15], a[48:51], a[16:19], v[12:15]\n" \
"s_add_u32 s84, s84, s70\n" \
"s_lshl_b32 s88, s68, 8\n" \
"ds_read_b64_tr_b16 a[208:209], v209 offset:26624\n" \
"v_mfma_f32_16x16x32_bf16 v[12:15], a[52:55], a[20:23], v[12:15]\n" \
"buffer_load_dwordx4 v198, s[48:51], s77 offen offset:1024 lds\n" \
"v_mfma_f32_16x16x32_bf16 v[8:11], a[40:43], a[16:19], v[8:11]\n" \
"s_add_u32 s84, s84, s88\n" \
"ds_read_b64_tr_b16 a[210:211], v209 offset:29696\n" \
"v_bfe_u32 v245, v244, 2, 3\n" \
"v_mfma_f32_16x16x32_bf16 v[8:11], a[44:47], a[20:23], v[8:11]\n" \
"ds_read_b64_tr_b16 a[212:213], v209 offset:38912\n" \
"v_lshlrev_b32 v245, 11, v245\n" \
"v_mfma_f32_16x16x32_bf16 v[48:51], a[32:35], a[16:19], v[48:51]\n" \
"ds_read_b64_tr_b16 a[214:215], v209 offset:41984\n" \
"v_lshrrev_b32 v246, 5, v244\n" \
"v_mfma_f32_16x16x32_bf16 v[48:51], a[36:39], a[20:23], v[48:51]\n" \
"buffer_load_dwordx4 v199, s[48:51], s77 offen offset:2048 lds\n" \
"v_mfma_f32_16x16x32_bf16 v[56:59], a[32:35], a[24:27], v[56:59]\n" \
"ds_read_b64_tr_b16 a[216:217], v210 offset:26624\n" \
"v_lshl_add_u32 v245, v246, 6, v245\n" \
"v_mfma_f32_16x16x32_bf16 v[56:59], a[36:39], a[28:31], v[56:59]\n" \
"ds_read_b64_tr_b16 a[218:219], v210 offset:29696\n" \
"v_and_b32 v246, 3, v244\n" \
"v_mfma_f32_16x16x32_bf16 v[16:19], a[40:43], a[24:27], v[16:19]\n" \
"ds_read_b64_tr_b16 a[220:221], v210 offset:38912\n" \
"v_add_u32 v254, 3145728, v254\n" \
"v_mfma_f32_16x16x32_bf16 v[16:19], a[44:47], a[28:31], v[16:19]\n" \
"global_load_dword v253, v254, s[22:23]\n" \
"v_mfma_f32_16x16x32_bf16 v[20:23], a[48:51], a[24:27], v[20:23]\n" \
"ds_read_b64_tr_b16 a[222:223], v210 offset:41984\n" \
"v_bfe_u32 v247, v244, 4, 1\n" \
"v_mfma_f32_16x16x32_bf16 v[20:23], a[52:55], a[28:31], v[20:23]\n" \
"s_waitcnt lgkmcnt(3)\n" \
"v_lshlrev_b32 v247, 1, v247\n" \
"v_xor_b32 v246, v246, v247\n" \
"v_mfma_f32_16x16x32_bf16 v[28:31], a[48:51], a[192:195], v[28:31]\n" \
"v_lshl_add_u32 v245, v246, 4, v245\n" \
"v_add_u32 v216, 4096, v245\n" \
"v_mfma_f32_16x16x32_bf16 v[28:31], a[52:55], a[196:199], v[28:31]\n" \
"v_add_u32 v217, 3200, v245\n" \
"v_add_u32 v218, 34816, v245\n" \
"v_mfma_f32_16x16x32_bf16 v[24:27], a[40:43], a[192:195], v[24:27]\n" \
"v_add_u32 v219, 33920, v245\n" \
"s_mov_b32 s86, 65536\n" \
"v_mfma_f32_16x16x32_bf16 v[24:27], a[44:47], a[196:199], v[24:27]\n" \
"s_lshr_b32 s70, s61, 1\n" \
"s_lshl_b32 s71, s70, 13\n" \
"s_add_u32 s71, s71, %3\n" \
"v_mfma_f32_16x16x32_bf16 v[64:67], a[32:35], a[192:195], v[64:67]\n" \
"v_add_u32 v211, s71, v236\n" \
"s_lshl_b32 s70, s70, 18\n" \
"v_mfma_f32_16x16x32_bf16 v[64:67], a[36:39], a[196:199], v[64:67]\n" \
"s_add_u32 s88, s88, s70\n" \
"s_waitcnt vmcnt(13) lgkmcnt(0)\n" \
"s_barrier\n" \
"v_mfma_f32_16x16x32_bf16 v[68:71], a[200:203], a[192:195], v[68:71]\n" \
"s_and_b32 s70, s61, 1\n" \
"ds_read_b64_tr_b16 a[32:33], v209 offset:1024\n" \
"ds_read_b64_tr_b16 a[34:35], v209 offset:4096\n" \
"v_mfma_f32_16x16x32_bf16 v[68:71], a[204:207], a[196:199], v[68:71]\n" \
"s_lshl_b32 s71, s70, 7\n" \
"ds_read_b64_tr_b16 a[36:37], v209 offset:13312\n" \
"ds_read_b64_tr_b16 a[38:39], v209 offset:16384\n" \
"v_mfma_f32_16x16x32_bf16 v[60:63], a[200:203], a[24:27], v[60:63]\n" \
"s_add_u32 m0, s79, 40960\n" \
"s_add_u32 s88, s88, s71\n" \
"ds_read_b64_tr_b16 a[40:41], v209 offset:0\n" \
"v_mfma_f32_16x16x32_bf16 v[60:63], a[204:207], a[28:31], v[60:63]\n" \
"buffer_load_dwordx4 v192, s[44:47], s73 offen offset:0 lds\n" \
"v_mfma_f32_16x16x32_bf16 v[52:55], a[200:203], a[16:19], v[52:55]\n" \
"ds_read_b64_tr_b16 a[42:43], v209 offset:3072\n" \
"v_and_b32 v245, 3, v244\n" \
"v_mfma_f32_16x16x32_bf16 v[52:55], a[204:207], a[20:23], v[52:55]\n" \
"ds_read_b64_tr_b16 a[44:45], v209 offset:12288\n" \
"v_lshlrev_b32 v245, 3, v245\n" \
"v_mfma_f32_16x16x32_bf16 v[44:47], a[200:203], a[8:11], v[44:47]\n" \
"ds_read_b64_tr_b16 a[46:47], v209 offset:15360\n" \
"v_bfe_u32 v246, v244, 2, 2\n" \
"v_mfma_f32_16x16x32_bf16 v[44:47], a[204:207], a[12:15], v[44:47]\n" \
"buffer_load_dwordx4 v193, s[44:47], s73 offen offset:1024 lds\n" \
"v_mfma_f32_16x16x32_bf16 v[72:75], a[0:3], a[208:211], v[72:75]\n" \
"ds_read_b64_tr_b16 a[48:49], v210 offset:0\n" \
"v_lshl_add_u32 v245, v246, 6, v245\n" \
"v_mfma_f32_16x16x32_bf16 v[72:75], a[4:7], a[212:215], v[72:75]\n" \
"ds_read_b128 a[0:3], v202 offset:6144\n" \
"v_bfe_u32 v246, v244, 4, 1\n" \
"v_mfma_f32_16x16x32_bf16 v[80:83], a[8:11], a[208:211], v[80:83]\n" \
"ds_read_b128 a[4:7], v203 offset:6144\n" \
"v_lshl_add_u32 v245, v246, 5, v245\n" \
"v_mfma_f32_16x16x32_bf16 v[80:83], a[12:15], a[212:215], v[80:83]\n" \
"buffer_load_dwordx4 v192, s[44:47], s73 offen offset:2048 lds\n" \
"v_mfma_f32_16x16x32_bf16 v[84:87], a[8:11], a[216:219], v[84:87]\n" \
"ds_read_b64_tr_b16 a[50:51], v210 offset:3072\n" \
"v_lshl_add_u32 v245, v246, 8, v245\n" \
"v_mfma_f32_16x16x32_bf16 v[84:87], a[12:15], a[220:223], v[84:87]\n" \
"ds_read_b128 a[8:11], v205 offset:0\n" \
"v_lshrrev_b32 v246, 5, v244\n" \
"v_mfma_f32_16x16x32_bf16 v[92:95], a[16:19], a[216:219], v[92:95]\n" \
"ds_read_b128 a[12:15], v206 offset:0\n" \
"v_lshl_add_u32 v245, v246, 12, v245\n" \
"v_mfma_f32_16x16x32_bf16 v[92:95], a[20:23], a[220:223], v[92:95]\n" \
"buffer_load_dwordx4 v193, s[44:47], s73 offen offset:3072 lds\n" \
"v_mfma_f32_16x16x32_bf16 v[88:91], a[16:19], a[208:211], v[88:91]\n" \
"s_xor_b32 s73, s73, 0x80\n" \
"s_mul_i32 s76, s80, 393216\n" \
"ds_read_b64_tr_b16 a[52:53], v210 offset:12288\n" \
"v_mfma_f32_16x16x32_bf16 v[88:91], a[20:23], a[212:215], v[88:91]\n" \
"s_add_u32 s76, s76, s78\n" \
"s_lshl_b32 s71, s70, 10\n" \
"ds_read_b128 a[16:19], v205 offset:4096\n" \
"v_mfma_f32_16x16x32_bf16 v[96:99], a[24:27], a[208:211], v[96:99]\n" \
"s_add_u32 m0, s91, 106496\n" \
"s_add_u32 s71, s71, %3\n" \
"ds_read_b128 a[20:23], v206 offset:4096\n" \
"v_mfma_f32_16x16x32_bf16 v[96:99], a[28:31], a[212:215], v[96:99]\n" \
"buffer_load_dwordx4 v197, s[48:51], s76 offen offset:0 lds\n" \
"v_mfma_f32_16x16x32_bf16 v[100:103], a[24:27], a[216:219], v[100:103]\n" \
"ds_read_b64_tr_b16 a[54:55], v210 offset:15360\n" \
"v_add_u32 v212, s71, v245\n" \
"v_mfma_f32_16x16x32_bf16 v[100:103], a[28:31], a[220:223], v[100:103]\n" \
"s_mov_b32 s69, 1\n" \
"s_mov_b32 s30, 0x0\n" \
"ds_read_b128 a[24:27], v205 offset:8192\n" \
"v_mfma_f32_16x16x32_bf16 v[108:111], a[192:195], a[216:219], v[108:111]\n" \
"s_add_u32 s77, s76, 24576\n" \
"s_mov_b32 s85, 98304\n" \
"ds_read_b128 a[28:31], v206 offset:8192\n" \
"v_mfma_f32_16x16x32_bf16 v[108:111], a[196:199], a[220:223], v[108:111]\n" \
"buffer_load_dwordx4 v198, s[48:51], s76 offen offset:1024 lds\n" \
"v_mfma_f32_16x16x32_bf16 v[104:107], a[192:195], a[208:211], v[104:107]\n" \
"s_sub_u32 s85, s85, 32768\n" \
"s_cselect_b32 s85, 131072, s85\n" \
"v_add_u32 v213, s85, v211\n" \
"v_mfma_f32_16x16x32_bf16 v[104:107], a[196:199], a[212:215], v[104:107]\n" \
"s_waitcnt lgkmcnt(2)\n" \
"v_add_u32 v214, s85, v212\n" \
"v_xor_b32 v215, 32, v214\n" \
"v_mfma_f32_16x16x32_bf16 v[32:35], a[32:35], a[0:3], v[32:35]\n" \
"s_xor_b32 s80, s80, 0x1\n" \
"s_mov_b32 s13, 65593856\n" \
"ds_read_b128 a[192:195], v205 offset:12288\n" \
"v_mfma_f32_16x16x32_bf16 v[32:35], a[36:39], a[4:7], v[32:35]\n" \
"buffer_load_dwordx4 v199, s[48:51], s76 offen offset:2048 lds\n" \
"v_mfma_f32_16x16x32_bf16 v[40:43], a[32:35], a[8:11], v[40:43]\n" \
"ds_read_b128 a[196:199], v206 offset:12288\n" \
"v_lshrrev_b32 v228, 1, v234\n" \
"v_mfma_f32_16x16x32_bf16 v[40:43], a[36:39], a[12:15], v[40:43]\n" \
"ds_read_b64_tr_b16 a[200:201], v210 offset:1024\n" \
"v_mov_b32 v230, s60\n" \
"v_mfma_f32_16x16x32_bf16 v[0:3], a[40:43], a[8:11], v[0:3]\n" \
"s_add_u32 m0, m0, 3072\n" \
"ds_read_b64_tr_b16 a[202:203], v210 offset:4096\n" \
"v_lshlrev_b32 v231, 28, s61\n" \
"v_mfma_f32_16x16x32_bf16 v[0:3], a[44:47], a[12:15], v[0:3]\n" \
"buffer_load_dwordx4 v197, s[48:51], s77 offen offset:0 lds\n" \
"v_mfma_f32_16x16x32_bf16 v[4:7], a[48:51], a[8:11], v[4:7]\n" \
"ds_read_b64_tr_b16 a[204:205], v210 offset:13312\n" \
"v_mfma_f32_16x16x32_bf16 v[4:7], a[52:55], a[12:15], v[4:7]\n" \
"ds_read_b64_tr_b16 a[206:207], v210 offset:16384\n" \
"v_mfma_f32_16x16x32_bf16 v[12:15], a[48:51], a[16:19], v[12:15]\n" \
"ds_read_b64_tr_b16 a[208:209], v209 offset:2048\n" \
"v_mfma_f32_16x16x32_bf16 v[12:15], a[52:55], a[20:23], v[12:15]\n" \
"buffer_load_dwordx4 v198, s[48:51], s77 offen offset:1024 lds\n" \
"Lpg8481_%=:\n" \
"Lst1x1_%=:\n" \
"s_cbranch_execz Lpg10800_%=\n" \
"v_mfma_f32_16x16x32_bf16 v[8:11], a[40:43], a[16:19], v[8:11]\n" \
"ds_read_b64_tr_b16 a[210:211], v209 offset:5120\n" \
"v_mfma_f32_16x16x32_bf16 v[8:11], a[44:47], a[20:23], v[8:11]\n" \
"ds_read_b64_tr_b16 a[212:213], v209 offset:14336\n" \
"v_mfma_f32_16x16x32_bf16 v[48:51], a[32:35], a[16:19], v[48:51]\n" \
"s_waitcnt lgkmcnt(3)\n" \
"ds_read_b64_tr_b16 a[214:215], v209 offset:17408\n" \
"v_mfma_f32_16x16x32_bf16 v[48:51], a[36:39], a[20:23], v[48:51]\n" \
"buffer_load_dwordx4 v199, s[48:51], s77 offen offset:2048 lds\n" \
"v_mfma_f32_16x16x32_bf16 v[56:59], a[32:35], a[24:27], v[56:59]\n" \
"ds_read_b64_tr_b16 a[216:217], v210 offset:2048\n" \
"v_mfma_f32_16x16x32_bf16 v[56:59], a[36:39], a[28:31], v[56:59]\n" \
"ds_read_b64_tr_b16 a[218:219], v210 offset:5120\n" \
"v_mfma_f32_16x16x32_bf16 v[16:19], a[40:43], a[24:27], v[16:19]\n" \
"ds_read_b64_tr_b16 a[220:221], v210 offset:14336\n" \
"v_mfma_f32_16x16x32_bf16 v[16:19], a[44:47], a[28:31], v[16:19]\n" \
"v_mfma_f32_16x16x32_bf16 v[20:23], a[48:51], a[24:27], v[20:23]\n" \
"ds_read_b64_tr_b16 a[222:223], v210 offset:17408\n" \
"v_mfma_f32_16x16x32_bf16 v[20:23], a[52:55], a[28:31], v[20:23]\n" \
"v_mfma_f32_16x16x32_bf16 v[28:31], a[48:51], a[192:195], v[28:31]\n" \
"v_mfma_f32_16x16x32_bf16 v[28:31], a[52:55], a[196:199], v[28:31]\n" \
"buffer_load_dwordx2 v[226:227], v228, s[56:59], s13 offen sc1\n" \
"v_mfma_f32_16x16x32_bf16 v[24:27], a[40:43], a[192:195], v[24:27]\n" \
"v_mfma_f32_16x16x32_bf16 v[24:27], a[44:47], a[196:199], v[24:27]\n" \
"v_mfma_f32_16x16x32_bf16 v[64:67], a[32:35], a[192:195], v[64:67]\n" \
"v_mfma_f32_16x16x32_bf16 v[64:67], a[36:39], a[196:199], v[64:67]\n" \
"s_waitcnt vmcnt(12) lgkmcnt(0)\n" \
"s_barrier\n" \
"v_mfma_f32_16x16x32_bf16 v[68:71], a[200:203], a[192:195], v[68:71]\n" \
"ds_read_b64_tr_b16 a[32:33], v204 offset:17408\n" \
"ds_read_b64_tr_b16 a[34:35], v204 offset:20480\n" \
"v_mfma_f32_16x16x32_bf16 v[68:71], a[204:207], a[196:199], v[68:71]\n" \
"ds_read_b64_tr_b16 a[36:37], v204 offset:29696\n" \
"ds_read_b64_tr_b16 a[38:39], v204 offset:32768\n" \
"v_mfma_f32_16x16x32_bf16 v[60:63], a[200:203], a[24:27], v[60:63]\n" \
"s_add_u32 m0, s79, 65536\n" \
"ds_read_b64_tr_b16 a[40:41], v204 offset:16384\n" \
"v_mfma_f32_16x16x32_bf16 v[60:63], a[204:207], a[28:31], v[60:63]\n" \
"buffer_load_dwordx4 v192, s[44:47], s73 offen offset:0 lds\n" \
"v_mfma_f32_16x16x32_bf16 v[52:55], a[200:203], a[16:19], v[52:55]\n" \
"ds_read_b64_tr_b16 a[42:43], v204 offset:19456\n" \
"v_mfma_f32_16x16x32_bf16 v[52:55], a[204:207], a[20:23], v[52:55]\n" \
"ds_read_b64_tr_b16 a[44:45], v204 offset:28672\n" \
"v_mfma_f32_16x16x32_bf16 v[44:47], a[200:203], a[8:11], v[44:47]\n" \
"ds_read_b64_tr_b16 a[46:47], v204 offset:31744\n" \
"v_mfma_f32_16x16x32_bf16 v[44:47], a[204:207], a[12:15], v[44:47]\n" \
"buffer_load_dwordx4 v193, s[44:47], s73 offen offset:1024 lds\n" \
"v_mfma_f32_16x16x32_bf16 v[72:75], a[0:3], a[208:211], v[72:75]\n" \
"ds_read_b64_tr_b16 a[48:49], v222 offset:16384\n" \
"v_mfma_f32_16x16x32_bf16 v[72:75], a[4:7], a[212:215], v[72:75]\n" \
"ds_read_b128 a[0:3], v202 offset:7168\n" \
"v_mfma_f32_16x16x32_bf16 v[80:83], a[8:11], a[208:211], v[80:83]\n" \
"ds_read_b128 a[4:7], v203 offset:7168\n" \
"v_mfma_f32_16x16x32_bf16 v[80:83], a[12:15], a[212:215], v[80:83]\n" \
"buffer_load_dwordx4 v192, s[44:47], s73 offen offset:2048 lds\n" \
"v_mfma_f32_16x16x32_bf16 v[84:87], a[8:11], a[216:219], v[84:87]\n" \
"ds_read_b64_tr_b16 a[50:51], v222 offset:19456\n" \
"v_mfma_f32_16x16x32_bf16 v[84:87], a[12:15], a[220:223], v[84:87]\n" \
"ds_read_b128 a[8:11], v200 offset:0\n" \
"v_mfma_f32_16x16x32_bf16 v[92:95], a[16:19], a[216:219], v[92:95]\n" \
"ds_read_b128 a[12:15], v201 offset:0\n" \
"v_mfma_f32_16x16x32_bf16 v[92:95], a[20:23], a[220:223], v[92:95]\n" \
"buffer_load_dwordx4 v193, s[44:47], s73 offen offset:3072 lds\n" \
"v_mfma_f32_16x16x32_bf16 v[88:91], a[16:19], a[208:211], v[88:91]\n" \
"s_xor_b32 s73, s73, 0x180\n" \
"s_mul_i32 s76, s80, 393216\n" \
"ds_read_b64_tr_b16 a[52:53], v222 offset:28672\n" \
"v_mfma_f32_16x16x32_bf16 v[88:91], a[20:23], a[212:215], v[88:91]\n" \
"s_add_u32 s76, s76, s78\n" \
"ds_read_b128 a[16:19], v200 offset:4096\n" \
"v_mfma_f32_16x16x32_bf16 v[96:99], a[24:27], a[208:211], v[96:99]\n" \
"s_add_u32 m0, s91, 81920\n" \
"ds_read_b128 a[20:23], v201 offset:4096\n" \
"v_mfma_f32_16x16x32_bf16 v[96:99], a[28:31], a[212:215], v[96:99]\n" \
"buffer_load_dwordx4 v197, s[48:51], s76 offen offset:0 lds\n" \
"v_mfma_f32_16x16x32_bf16 v[100:103], a[24:27], a[216:219], v[100:103]\n" \
"ds_read_b64_tr_b16 a[54:55], v222 offset:31744\n" \
"v_mfma_f32_16x16x32_bf16 v[100:103], a[28:31], a[220:223], v[100:103]\n" \
"ds_read_b128 a[24:27], v200 offset:8192\n" \
"v_mfma_f32_16x16x32_bf16 v[108:111], a[192:195], a[216:219], v[108:111]\n" \
"s_add_u32 s77, s76, 24576\n" \
"ds_read_b128 a[28:31], v201 offset:8192\n" \
"v_mfma_f32_16x16x32_bf16 v[108:111], a[196:199], a[220:223], v[108:111]\n" \
"buffer_load_dwordx4 v198, s[48:51], s76 offen offset:1024 lds\n" \
"v_mfma_f32_16x16x32_bf16 v[104:107], a[192:195], a[208:211], v[104:107]\n" \
"v_mfma_f32_16x16x32_bf16 v[104:107], a[196:199], a[212:215], v[104:107]\n" \
"s_waitcnt lgkmcnt(2)\n" \
"v_mfma_f32_16x16x32_bf16 v[32:35], a[32:35], a[0:3], v[32:35]\n" \
"s_xor_b32 s80, s80, 0x3\n" \
"ds_read_b128 a[192:195], v200 offset:12288\n" \
"v_mfma_f32_16x16x32_bf16 v[32:35], a[36:39], a[4:7], v[32:35]\n" \
"buffer_load_dwordx4 v199, s[48:51], s76 offen offset:2048 lds\n" \
"v_mfma_f32_16x16x32_bf16 v[40:43], a[32:35], a[8:11], v[40:43]\n" \
"ds_read_b128 a[196:199], v201 offset:12288\n" \
"v_mfma_f32_16x16x32_bf16 v[40:43], a[36:39], a[12:15], v[40:43]\n" \
"ds_read_b64_tr_b16 a[200:201], v222 offset:17408\n" \
"v_mfma_f32_16x16x32_bf16 v[0:3], a[40:43], a[8:11], v[0:3]\n" \
"s_add_u32 m0, m0, 3072\n" \
"ds_read_b64_tr_b16 a[202:203], v222 offset:20480\n" \
"v_mfma_f32_16x16x32_bf16 v[0:3], a[44:47], a[12:15], v[0:3]\n" \
"buffer_load_dwordx4 v197, s[48:51], s77 offen offset:0 lds\n" \
"v_mfma_f32_16x16x32_bf16 v[4:7], a[48:51], a[8:11], v[4:7]\n" \
"ds_read_b64_tr_b16 a[204:205], v222 offset:29696\n" \
"v_mfma_f32_16x16x32_bf16 v[4:7], a[52:55], a[12:15], v[4:7]\n" \
"ds_read_b64_tr_b16 a[206:207], v222 offset:32768\n" \
"v_mfma_f32_16x16x32_bf16 v[12:15], a[48:51], a[16:19], v[12:15]\n" \
"ds_read_b64_tr_b16 a[208:209], v204 offset:18432\n" \
"v_mfma_f32_16x16x32_bf16 v[12:15], a[52:55], a[20:23], v[12:15]\n" \
"buffer_load_dwordx4 v198, s[48:51], s77 offen offset:1024 lds\n" \
"v_mfma_f32_16x16x32_bf16 v[8:11], a[40:43], a[16:19], v[8:11]\n" \
"ds_read_b64_tr_b16 a[210:211], v204 offset:21504\n" \
"v_mfma_f32_16x16x32_bf16 v[8:11], a[44:47], a[20:23], v[8:11]\n" \
"ds_read_b64_tr_b16 a[212:213], v204 offset:30720\n" \
"v_mfma_f32_16x16x32_bf16 v[48:51], a[32:35], a[16:19], v[48:51]\n" \
"s_waitcnt lgkmcnt(3)\n" \
"ds_read_b64_tr_b16 a[214:215], v204 offset:33792\n" \
"v_mfma_f32_16x16x32_bf16 v[48:51], a[36:39], a[20:23], v[48:51]\n" \
"buffer_load_dwordx4 v199, s[48:51], s77 offen offset:2048 lds\n" \
"v_mfma_f32_16x16x32_bf16 v[56:59], a[32:35], a[24:27], v[56:59]\n" \
"ds_read_b64_tr_b16 a[216:217], v222 offset:18432\n" \
"v_mfma_f32_16x16x32_bf16 v[56:59], a[36:39], a[28:31], v[56:59]\n" \
"ds_read_b64_tr_b16 a[218:219], v222 offset:21504\n" \
"v_mfma_f32_16x16x32_bf16 v[16:19], a[40:43], a[24:27], v[16:19]\n" \
"ds_read_b64_tr_b16 a[220:221], v222 offset:30720\n" \
"v_mfma_f32_16x16x32_bf16 v[16:19], a[44:47], a[28:31], v[16:19]\n" \
"v_mfma_f32_16x16x32_bf16 v[20:23], a[48:51], a[24:27], v[20:23]\n" \
"ds_read_b64_tr_b16 a[222:223], v222 offset:33792\n" \
"v_mfma_f32_16x16x32_bf16 v[20:23], a[52:55], a[28:31], v[20:23]\n" \
"v_mfma_f32_16x16x32_bf16 v[28:31], a[48:51], a[192:195], v[28:31]\n" \
"v_mfma_f32_16x16x32_bf16 v[28:31], a[52:55], a[196:199], v[28:31]\n" \
"v_mfma_f32_16x16x32_bf16 v[24:27], a[40:43], a[192:195], v[24:27]\n" \
"v_mfma_f32_16x16x32_bf16 v[24:27], a[44:47], a[196:199], v[24:27]\n" \
"v_mfma_f32_16x16x32_bf16 v[64:67], a[32:35], a[192:195], v[64:67]\n" \
"v_mfma_f32_16x16x32_bf16 v[64:67], a[36:39], a[196:199], v[64:67]\n" \
"s_waitcnt vmcnt(11) lgkmcnt(0)\n" \
"s_barrier\n" \
"v_mfma_f32_16x16x32_bf16 v[68:71], a[200:203], a[192:195], v[68:71]\n" \
"ds_read_b64_tr_b16 a[32:33], v209 offset:25600\n" \
"ds_read_b64_tr_b16 a[34:35], v209 offset:28672\n" \
"v_mfma_f32_16x16x32_bf16 v[68:71], a[204:207], a[196:199], v[68:71]\n" \
"ds_read_b64_tr_b16 a[36:37], v209 offset:37888\n" \
"ds_read_b64_tr_b16 a[38:39], v209 offset:40960\n" \
"v_mfma_f32_16x16x32_bf16 v[60:63], a[200:203], a[24:27], v[60:63]\n" \
"s_add_u32 m0, s79, 0\n" \
"ds_read_b64_tr_b16 a[40:41], v209 offset:24576\n" \
"v_mfma_f32_16x16x32_bf16 v[60:63], a[204:207], a[28:31], v[60:63]\n" \
"buffer_load_dwordx4 v192, s[44:47], s73 offen offset:0 lds\n" \
"v_mfma_f32_16x16x32_bf16 v[52:55], a[200:203], a[16:19], v[52:55]\n" \
"ds_read_b64_tr_b16 a[42:43], v209 offset:27648\n" \
"v_mfma_f32_16x16x32_bf16 v[52:55], a[204:207], a[20:23], v[52:55]\n" \
"ds_read_b64_tr_b16 a[44:45], v209 offset:36864\n" \
"v_mfma_f32_16x16x32_bf16 v[44:47], a[200:203], a[8:11], v[44:47]\n" \
"ds_read_b64_tr_b16 a[46:47], v209 offset:39936\n" \
"v_mfma_f32_16x16x32_bf16 v[44:47], a[204:207], a[12:15], v[44:47]\n" \
"buffer_load_dwordx4 v193, s[44:47], s73 offen offset:1024 lds\n" \
"v_mfma_f32_16x16x32_bf16 v[72:75], a[0:3], a[208:211], v[72:75]\n" \
"ds_read_b64_tr_b16 a[48:49], v210 offset:24576\n" \
"v_mfma_f32_16x16x32_bf16 v[72:75], a[4:7], a[212:215], v[72:75]\n" \
"ds_read_b128 a[0:3], v202 offset:16384\n" \
"v_mfma_f32_16x16x32_bf16 v[80:83], a[8:11], a[208:211], v[80:83]\n" \
"ds_read_b128 a[4:7], v203 offset:16384\n" \
"v_mfma_f32_16x16x32_bf16 v[80:83], a[12:15], a[212:215], v[80:83]\n" \
"buffer_load_dwordx4 v192, s[44:47], s73 offen offset:2048 lds\n" \
"v_mfma_f32_16x16x32_bf16 v[84:87], a[8:11], a[216:219], v[84:87]\n" \
"ds_read_b64_tr_b16 a[50:51], v210 offset:27648\n" \
"v_mfma_f32_16x16x32_bf16 v[84:87], a[12:15], a[220:223], v[84:87]\n" \
"ds_read_b128 a[8:11], v200 offset:40960\n" \
"v_mfma_f32_16x16x32_bf16 v[92:95], a[16:19], a[216:219], v[92:95]\n" \
"ds_read_b128 a[12:15], v201 offset:40960\n" \
"v_mfma_f32_16x16x32_bf16 v[92:95], a[20:23], a[220:223], v[92:95]\n" \
"buffer_load_dwordx4 v193, s[44:47], s73 offen offset:3072 lds\n" \
"v_mfma_f32_16x16x32_bf16 v[88:91], a[16:19], a[208:211], v[88:91]\n" \
"s_xor_b32 s73, s73, 0x300\n" \
"s_mul_i32 s76, s80, 393216\n" \
"ds_read_b64_tr_b16 a[52:53], v210 offset:36864\n" \
"v_mfma_f32_16x16x32_bf16 v[88:91], a[20:23], a[212:215], v[88:91]\n" \
"s_add_u32 s76, s76, s78\n" \
"ds_read_b128 a[16:19], v200 offset:45056\n" \
"v_mfma_f32_16x16x32_bf16 v[96:99], a[24:27], a[208:211], v[96:99]\n" \
"s_add_u32 m0, s91, 16384\n" \
"ds_read_b128 a[20:23], v201 offset:45056\n" \
"v_mfma_f32_16x16x32_bf16 v[96:99], a[28:31], a[212:215], v[96:99]\n" \
"buffer_load_dwordx4 v197, s[48:51], s76 offen offset:0 lds\n" \
"v_mfma_f32_16x16x32_bf16 v[100:103], a[24:27], a[216:219], v[100:103]\n" \
"ds_read_b64_tr_b16 a[54:55], v210 offset:39936\n" \
"v_mfma_f32_16x16x32_bf16 v[100:103], a[28:31], a[220:223], v[100:103]\n" \
"ds_read_b128 a[24:27], v200 offset:49152\n" \
"v_mfma_f32_16x16x32_bf16 v[108:111], a[192:195], a[216:219], v[108:111]\n" \
"s_add_u32 s77, s76, 24576\n" \
"ds_read_b128 a[28:31], v201 offset:49152\n" \
"v_mfma_f32_16x16x32_bf16 v[108:111], a[196:199], a[220:223], v[108:111]\n" \
"buffer_load_dwordx4 v198, s[48:51], s76 offen offset:1024 lds\n" \
"v_mfma_f32_16x16x32_bf16 v[104:107], a[192:195], a[208:211], v[104:107]\n" \
"v_mfma_f32_16x16x32_bf16 v[104:107], a[196:199], a[212:215], v[104:107]\n" \
"s_waitcnt lgkmcnt(2)\n" \
"v_mfma_f32_16x16x32_bf16 v[32:35], a[32:35], a[0:3], v[32:35]\n" \
"s_xor_b32 s80, s80, 0x6\n" \
"ds_read_b128 a[192:195], v200 offset:53248\n" \
"v_mfma_f32_16x16x32_bf16 v[32:35], a[36:39], a[4:7], v[32:35]\n" \
"buffer_load_dwordx4 v199, s[48:51], s76 offen offset:2048 lds\n" \
"v_mfma_f32_16x16x32_bf16 v[40:43], a[32:35], a[8:11], v[40:43]\n" \
"ds_read_b128 a[196:199], v201 offset:53248\n" \
"v_mfma_f32_16x16x32_bf16 v[40:43], a[36:39], a[12:15], v[40:43]\n" \
"ds_read_b64_tr_b16 a[200:201], v210 offset:25600\n" \
"v_mfma_f32_16x16x32_bf16 v[0:3], a[40:43], a[8:11], v[0:3]\n" \
"s_add_u32 m0, m0, 3072\n" \
"ds_read_b64_tr_b16 a[202:203], v210 offset:28672\n" \
"v_mfma_f32_16x16x32_bf16 v[0:3], a[44:47], a[12:15], v[0:3]\n" \
"buffer_load_dwordx4 v197, s[48:51], s77 offen offset:0 lds\n" \
"v_mfma_f32_16x16x32_bf16 v[4:7], a[48:51], a[8:11], v[4:7]\n" \
"ds_read_b64_tr_b16 a[204:205], v210 offset:37888\n" \
"v_mfma_f32_16x16x32_bf16 v[4:7], a[52:55], a[12:15], v[4:7]\n" \
"ds_read_b64_tr_b16 a[206:207], v210 offset:40960\n" \
"v_mfma_f32_16x16x32_bf16 v[12:15], a[48:51], a[16:19], v[12:15]\n" \
"ds_read_b64_tr_b16 a[208:209], v209 offset:26624\n" \
"v_mfma_f32_16x16x32_bf16 v[12:15], a[52:55], a[20:23], v[12:15]\n" \
"buffer_load_dwordx4 v198, s[48:51], s77 offen offset:1024 lds\n" \
"v_mfma_f32_16x16x32_bf16 v[8:11], a[40:43], a[16:19], v[8:11]\n" \
"ds_read_b64_tr_b16 a[210:211], v209 offset:29696\n" \
"v_mfma_f32_16x16x32_bf16 v[8:11], a[44:47], a[20:23], v[8:11]\n" \
"ds_read_b64_tr_b16 a[212:213], v209 offset:38912\n" \
"v_mfma_f32_16x16x32_bf16 v[48:51], a[32:35], a[16:19], v[48:51]\n" \
"s_waitcnt lgkmcnt(3)\n" \
"ds_read_b64_tr_b16 a[214:215], v209 offset:41984\n" \
"v_mfma_f32_16x16x32_bf16 v[48:51], a[36:39], a[20:23], v[48:51]\n" \
"buffer_load_dwordx4 v199, s[48:51], s77 offen offset:2048 lds\n" \
"v_mfma_f32_16x16x32_bf16 v[56:59], a[32:35], a[24:27], v[56:59]\n" \
"ds_read_b64_tr_b16 a[216:217], v210 offset:26624\n" \
"v_mfma_f32_16x16x32_bf16 v[56:59], a[36:39], a[28:31], v[56:59]\n" \
"ds_read_b64_tr_b16 a[218:219], v210 offset:29696\n" \
"v_mfma_f32_16x16x32_bf16 v[16:19], a[40:43], a[24:27], v[16:19]\n" \
"ds_read_b64_tr_b16 a[220:221], v210 offset:38912\n" \
"v_mfma_f32_16x16x32_bf16 v[16:19], a[44:47], a[28:31], v[16:19]\n" \
"v_mfma_f32_16x16x32_bf16 v[20:23], a[48:51], a[24:27], v[20:23]\n" \
"ds_read_b64_tr_b16 a[222:223], v210 offset:41984\n" \
"v_mfma_f32_16x16x32_bf16 v[20:23], a[52:55], a[28:31], v[20:23]\n" \
"v_mfma_f32_16x16x32_bf16 v[28:31], a[48:51], a[192:195], v[28:31]\n" \
"v_mfma_f32_16x16x32_bf16 v[28:31], a[52:55], a[196:199], v[28:31]\n" \
"v_mfma_f32_16x16x32_bf16 v[24:27], a[40:43], a[192:195], v[24:27]\n" \
"v_mfma_f32_16x16x32_bf16 v[24:27], a[44:47], a[196:199], v[24:27]\n" \
"v_mfma_f32_16x16x32_bf16 v[64:67], a[32:35], a[192:195], v[64:67]\n" \
"v_mfma_f32_16x16x32_bf16 v[64:67], a[36:39], a[196:199], v[64:67]\n" \
"s_waitcnt vmcnt(10) lgkmcnt(0)\n" \
"s_barrier\n" \
"v_mfma_f32_16x16x32_bf16 v[68:71], a[200:203], a[192:195], v[68:71]\n" \
"ds_read_b64_tr_b16 a[32:33], v209 offset:1024\n" \
"ds_read_b64_tr_b16 a[34:35], v209 offset:4096\n" \
"v_mfma_f32_16x16x32_bf16 v[68:71], a[204:207], a[196:199], v[68:71]\n" \
"ds_read_b64_tr_b16 a[36:37], v209 offset:13312\n" \
"ds_read_b64_tr_b16 a[38:39], v209 offset:16384\n" \
"v_mfma_f32_16x16x32_bf16 v[60:63], a[200:203], a[24:27], v[60:63]\n" \
"s_add_u32 m0, s79, 40960\n" \
"ds_read_b64_tr_b16 a[40:41], v209 offset:0\n" \
"v_cmp_eq_u32 vcc, s60, v226\n" \
"v_mfma_f32_16x16x32_bf16 v[60:63], a[204:207], a[28:31], v[60:63]\n" \
"buffer_load_dwordx4 v192, s[44:47], s73 offen offset:0 lds\n" \
"v_mfma_f32_16x16x32_bf16 v[52:55], a[200:203], a[16:19], v[52:55]\n" \
"s_andn2_b32 s70, 0xff, vcc_lo\n" \
"s_nop 0\n" \
"s_cbranch_scc1 Lpl4_%=\n" \
"Lpr4_%=:\n" \
"ds_read_b64_tr_b16 a[42:43], v209 offset:3072\n" \
"v_mfma_f32_16x16x32_bf16 v[52:55], a[204:207], a[20:23], v[52:55]\n" \
"s_and_b32 s70, %1, 7\n" \
"ds_read_b64_tr_b16 a[44:45], v209 offset:12288\n" \
"v_readlane_b32 s70, v227, s70\n" \
"v_mfma_f32_16x16x32_bf16 v[44:47], a[200:203], a[8:11], v[44:47]\n" \
"ds_read_b64_tr_b16 a[46:47], v209 offset:15360\n" \
"v_mfma_f32_16x16x32_bf16 v[44:47], a[204:207], a[12:15], v[44:47]\n" \
"buffer_load_dwordx4 v193, s[44:47], s73 offen offset:1024 lds\n" \
"v_mfma_f32_16x16x32_bf16 v[72:75], a[0:3], a[208:211], v[72:75]\n" \
"ds_read_b64_tr_b16 a[48:49], v210 offset:0\n" \
"v_subrev_u32 v227, s70, v227\n" \
"v_mfma_f32_16x16x32_bf16 v[72:75], a[4:7], a[212:215], v[72:75]\n" \
"ds_read_b128 a[0:3], v202 offset:17408\n" \
"v_cmp_gt_i32 vcc, 0, v227\n" \
"v_mfma_f32_16x16x32_bf16 v[80:83], a[8:11], a[208:211], v[80:83]\n" \
"ds_read_b128 a[4:7], v203 offset:17408\n" \
"v_mfma_f32_16x16x32_bf16 v[80:83], a[12:15], a[212:215], v[80:83]\n" \
"buffer_load_dwordx4 v192, s[44:47], s73 offen offset:2048 lds\n" \
"v_mfma_f32_16x16x32_bf16 v[84:87], a[8:11], a[216:219], v[84:87]\n" \
"s_and_b32 s13, vcc_lo, 0xff\n" \
"s_bcnt1_i32_b32 s13, s13\n" \
"ds_read_b64_tr_b16 a[50:51], v210 offset:3072\n" \
"v_mfma_f32_16x16x32_bf16 v[84:87], a[12:15], a[220:223], v[84:87]\n" \
"s_lshr_b32 s90, s13, 2\n" \
"s_mul_i32 s70, s90, 7\n" \
"ds_read_b128 a[8:11], v205 offset:0\n" \
"v_mfma_f32_16x16x32_bf16 v[92:95], a[16:19], a[216:219], v[92:95]\n" \
"s_xor_b32 s13, s13, s70\n" \
"s_lshl_b32 s70, s13, 3\n" \
"ds_read_b128 a[12:15], v206 offset:0\n" \
"v_mfma_f32_16x16x32_bf16 v[92:95], a[20:23], a[220:223], v[92:95]\n" \
"buffer_load_dwordx4 v193, s[44:47], s73 offen offset:3072 lds\n" \
"v_mfma_f32_16x16x32_bf16 v[88:91], a[16:19], a[208:211], v[88:91]\n" \
"s_xor_b32 s73, s73, 0x80\n" \
"s_mul_i32 s76, s80, 393216\n" \
"ds_read_b64_tr_b16 a[52:53], v210 offset:12288\n" \
"v_mfma_f32_16x16x32_bf16 v[88:91], a[20:23], a[212:215], v[88:91]\n" \
"s_add_u32 s76, s76, s78\n" \
"s_lshr_b32 s9, 0x2a2b2d2f, s70\n" \
"ds_read_b128 a[16:19], v205 offset:4096\n" \
"v_mfma_f32_16x16x32_bf16 v[96:99], a[24:27], a[208:211], v[96:99]\n" \
"s_add_u32 m0, s91, 106496\n" \
"s_and_b32 s9, s9, 0xff\n" \
"ds_read_b128 a[20:23], v206 offset:4096\n" \
"v_mfma_f32_16x16x32_bf16 v[96:99], a[28:31], a[212:215], v[96:99]\n" \
"buffer_load_dwordx4 v197, s[48:51], s76 offen offset:0 lds\n" \
"v_mfma_f32_16x16x32_bf16 v[100:103], a[24:27], a[216:219], v[100:103]\n" \
"s_lshr_b32 s8, %1, 6\n" \
"s_lshl2_add_u32 s8, s13, s8\n" \
"ds_read_b64_tr_b16 a[54:55], v210 offset:15360\n" \
"v_mfma_f32_16x16x32_bf16 v[100:103], a[28:31], a[220:223], v[100:103]\n" \
"ds_read_b128 a[24:27], v205 offset:8192\n" \
"v_max_i32_dpp v226, v227, v227 quad_perm:[1,0,3,2] row_mask:0xf bank_mask:0xf\n" \
"v_mfma_f32_16x16x32_bf16 v[108:111], a[192:195], a[216:219], v[108:111]\n" \
"s_add_u32 s77, s76, 24576\n" \
"s_mov_b32 s14, 0\n" \
"ds_read_b128 a[28:31], v206 offset:8192\n" \
"v_mfma_f32_16x16x32_bf16 v[108:111], a[196:199], a[220:223], v[108:111]\n" \
"buffer_load_dwordx4 v198, s[48:51], s76 offen offset:1024 lds\n" \
"v_mfma_f32_16x16x32_bf16 v[104:107], a[192:195], a[208:211], v[104:107]\n" \
"v_readlane_b32 s15, v226, 2\n" \
"v_readlane_b32 s16, v226, 4\n" \
"v_mfma_f32_16x16x32_bf16 v[104:107], a[196:199], a[212:215], v[104:107]\n" \
"s_waitcnt lgkmcnt(2)\n" \
"v_readlane_b32 s17, v226, 6\n" \
"v_cmp_gt_i32 vcc, s15, v226\n" \
"v_mfma_f32_16x16x32_bf16 v[32:35], a[32:35], a[0:3], v[32:35]\n" \
"s_xor_b32 s80, s80, 0x1\n" \
"ds_read_b128 a[192:195], v205 offset:12288\n" \
"v_mfma_f32_16x16x32_bf16 v[32:35], a[36:39], a[4:7], v[32:35]\n" \
"buffer_load_dwordx4 v199, s[48:51], s76 offen offset:2048 lds\n" \
"v_mfma_f32_16x16x32_bf16 v[40:43], a[32:35], a[8:11], v[40:43]\n" \
"s_and_b32 s70, vcc_lo, 0x55\n" \
"s_bcnt1_i32_b32 s70, s70\n" \
"ds_read_b128 a[196:199], v206 offset:12288\n" \
"v_mfma_f32_16x16x32_bf16 v[40:43], a[36:39], a[12:15], v[40:43]\n" \
"s_cmp_eq_u32 s70, s13\n" \
"s_cselect_b32 s14, 4, s14\n" \
"ds_read_b64_tr_b16 a[200:201], v210 offset:1024\n" \
"v_mfma_f32_16x16x32_bf16 v[0:3], a[40:43], a[8:11], v[0:3]\n" \
"s_add_u32 m0, m0, 3072\n" \
"ds_read_b64_tr_b16 a[202:203], v210 offset:4096\n" \
"v_cmp_gt_i32 vcc, s16, v226\n" \
"v_mfma_f32_16x16x32_bf16 v[0:3], a[44:47], a[12:15], v[0:3]\n" \
"buffer_load_dwordx4 v197, s[48:51], s77 offen offset:0 lds\n" \
"v_mfma_f32_16x16x32_bf16 v[4:7], a[48:51], a[8:11], v[4:7]\n" \
"s_and_b32 s70, vcc_lo, 0x55\n" \
"s_bcnt1_i32_b32 s70, s70\n" \
"ds_read_b64_tr_b16 a[204:205], v210 offset:13312\n" \
"v_mfma_f32_16x16x32_bf16 v[4:7], a[52:55], a[12:15], v[4:7]\n" \
"s_cmp_eq_u32 s70, s13\n" \
"s_cselect_b32 s14, 8, s14\n" \
"ds_read_b64_tr_b16 a[206:207], v210 offset:16384\n" \
"v_mfma_f32_16x16x32_bf16 v[12:15], a[48:51], a[16:19], v[12:15]\n" \
"ds_read_b64_tr_b16 a[208:209], v209 offset:2048\n" \
"v_cmp_gt_i32 vcc, s17, v226\n" \
"v_mfma_f32_16x16x32_bf16 v[12:15], a[52:55], a[20:23], v[12:15]\n" \
"buffer_load_dwordx4 v198, s[48:51], s77 offen offset:1024 lds\n" \
"v_mfma_f32_16x16x32_bf16 v[8:11], a[40:43], a[16:19], v[8:11]\n" \
"s_and_b32 s70, vcc_lo, 0x55\n" \
"s_bcnt1_i32_b32 s70, s70\n" \
"ds_read_b64_tr_b16 a[210:211], v209 offset:5120\n" \
"v_mfma_f32_16x16x32_bf16 v[8:11], a[44:47], a[20:23], v[8:11]\n" \
"s_cmp_eq_u32 s70, s13\n" \
"s_cselect_b32 s14, 12, s14\n" \
"ds_read_b64_tr_b16 a[212:213], v209 offset:14336\n" \
"v_mfma_f32_16x16x32_bf16 v[48:51], a[32:35], a[16:19], v[48:51]\n" \
"s_lshr_b32 s70, %1, 6\n" \
"s_add_u32 s14, s14, s70\n" \
"ds_read_b64_tr_b16 a[214:215], v209 offset:17408\n" \
"v_mfma_f32_16x16x32_bf16 v[48:51], a[36:39], a[20:23], v[48:51]\n" \
"s_waitcnt lgkmcnt(3)\n" \
"buffer_load_dwordx4 v199, s[48:51], s77 offen offset:2048 lds\n" \
"v_mfma_f32_16x16x32_bf16 v[56:59], a[32:35], a[24:27], v[56:59]\n" \
"s_cmp_eq_u32 s90, 0\n" \
"s_cselect_b32 s14, s8, s14\n" \
"ds_read_b64_tr_b16 a[216:217], v210 offset:2048\n" \
"v_mfma_f32_16x16x32_bf16 v[56:59], a[36:39], a[28:31], v[56:59]\n" \
"s_sub_u32 s10, 49, s9\n" \
"s_lshl_b32 s11, s9, 1\n" \
"ds_read_b64_tr_b16 a[218:219], v210 offset:5120\n" \
"v_mfma_f32_16x16x32_bf16 v[16:19], a[40:43], a[24:27], v[16:19]\n" \
"s_sub_u32 s11, s11, 71\n" \
"s_lshl2_add_u32 s70, s8, s61\n" \
"ds_read_b64_tr_b16 a[220:221], v210 offset:14336\n" \
"v_mfma_f32_16x16x32_bf16 v[16:19], a[44:47], a[28:31], v[16:19]\n" \
"s_mul_i32 s81, s70, 266240\n" \
"s_mul_i32 s82, s90, s9\n" \
"s_lshl_b32 s71, s82, 12\n" \
"v_mfma_f32_16x16x32_bf16 v[20:23], a[48:51], a[24:27], v[20:23]\n" \
"s_add_u32 s81, s81, s71\n" \
"s_add_u32 s81, s81, 35700736\n" \
"ds_read_b64_tr_b16 a[222:223], v210 offset:17408\n" \
"v_mfma_f32_16x16x32_bf16 v[20:23], a[52:55], a[28:31], v[20:23]\n" \
"s_lshl2_add_u32 s70, s14, s61\n" \
"s_mul_i32 s83, s70, 69632\n" \
"s_add_u32 s83, s83, 52740096\n" \
"v_mfma_f32_16x16x32_bf16 v[28:31], a[48:51], a[192:195], v[28:31]\n" \
"s_lshl_b32 s82, s82, 17\n" \
"s_add_u32 s82, s82, s84\n" \
"s_lshl_b32 s70, s14, 19\n" \
"v_mfma_f32_16x16x32_bf16 v[28:31], a[52:55], a[196:199], v[28:31]\n" \
"s_add_u32 s88, s88, s70\n" \
"s_lshl_b32 s71, s90, 11\n" \
"s_add_u32 s88, s88, s71\n" \
"v_mfma_f32_16x16x32_bf16 v[24:27], a[40:43], a[192:195], v[24:27]\n" \
"s_lshl3_add_u32 s70, s8, s68\n" \
"s_lshl2_add_u32 s87, s70, s61\n" \
"s_lshl_b32 s87, s87, 14\n" \
"v_mfma_f32_16x16x32_bf16 v[24:27], a[44:47], a[196:199], v[24:27]\n" \
"s_add_u32 s87, s87, 57196544\n" \
"s_lshl_b32 s64, s70, 7\n" \
"s_add_u32 s64, s64, 65601536\n" \
"v_mfma_f32_16x16x32_bf16 v[64:67], a[32:35], a[192:195], v[64:67]\n" \
"s_lshl_b32 s66, s8, 7\n" \
"s_lshl_b32 s67, s14, 7\n" \
"s_add_u32 s67, s67, 65591296\n" \
"v_mfma_f32_16x16x32_bf16 v[64:67], a[36:39], a[196:199], v[64:67]\n" \
"s_add_u32 s66, s66, 65589248\n" \
"s_waitcnt vmcnt(10) lgkmcnt(0)\n" \
"s_barrier\n" \
"v_mfma_f32_16x16x32_bf16 v[68:71], a[200:203], a[192:195], v[68:71]\n" \
"s_add_u32 s70, s9, 1\n" \
"ds_read_b64_tr_b16 a[32:33], v204 offset:17408\n" \
"ds_read_b64_tr_b16 a[34:35], v204 offset:20480\n" \
"v_mfma_f32_16x16x32_bf16 v[68:71], a[204:207], a[196:199], v[68:71]\n" \
"s_sub_u32 s70, s70, s90\n" \
"ds_read_b64_tr_b16 a[36:37], v204 offset:29696\n" \
"ds_read_b64_tr_b16 a[38:39], v204 offset:32768\n" \
"v_mfma_f32_16x16x32_bf16 v[60:63], a[200:203], a[24:27], v[60:63]\n" \
"s_add_u32 m0, s79, 65536\n" \
"s_lshr_b32 s70, s70, 1\n" \
"ds_read_b64_tr_b16 a[40:41], v204 offset:16384\n" \
"v_mfma_f32_16x16x32_bf16 v[60:63], a[204:207], a[28:31], v[60:63]\n" \
"buffer_load_dwordx4 v192, s[44:47], s73 offen offset:0 lds\n" \
"v_mfma_f32_16x16x32_bf16 v[52:55], a[200:203], a[16:19], v[52:55]\n" \
"s_bfm_b32 s65, s70, 0\n" \
"s_sub_u32 s70, 0, s90\n" \
"ds_read_b64_tr_b16 a[42:43], v204 offset:19456\n" \
"v_mfma_f32_16x16x32_bf16 v[52:55], a[204:207], a[20:23], v[52:55]\n" \
"s_xor_b32 s65, s65, s70\n" \
"s_and_b32 s74, s70, 0xffff\n" \
"ds_read_b64_tr_b16 a[44:45], v204 offset:28672\n" \
"v_mfma_f32_16x16x32_bf16 v[44:47], a[200:203], a[8:11], v[44:47]\n" \
"s_xor_b32 s68, s68, s8\n" \
"s_and_b32 s68, s68, 3\n" \
"ds_read_b64_tr_b16 a[46:47], v204 offset:31744\n" \
"v_mfma_f32_16x16x32_bf16 v[44:47], a[204:207], a[12:15], v[44:47]\n" \
"buffer_load_dwordx4 v193, s[44:47], s73 offen offset:1024 lds\n" \
"v_mfma_f32_16x16x32_bf16 v[72:75], a[0:3], a[208:211], v[72:75]\n" \
"s_lshl_b32 s68, s68, 12\n" \
"ds_read_b64_tr_b16 a[48:49], v222 offset:16384\n" \
"v_mfma_f32_16x16x32_bf16 v[72:75], a[4:7], a[212:215], v[72:75]\n" \
"ds_read_b128 a[0:3], v202 offset:18432\n" \
"v_mfma_f32_16x16x32_bf16 v[80:83], a[8:11], a[208:211], v[80:83]\n" \
"ds_read_b128 a[4:7], v203 offset:18432\n" \
"v_mfma_f32_16x16x32_bf16 v[80:83], a[12:15], a[212:215], v[80:83]\n" \
"buffer_load_dwordx4 v192, s[44:47], s73 offen offset:2048 lds\n" \
"v_mfma_f32_16x16x32_bf16 v[84:87], a[8:11], a[216:219], v[84:87]\n" \
"ds_read_b64_tr_b16 a[50:51], v222 offset:19456\n" \
"v_mfma_f32_16x16x32_bf16 v[84:87], a[12:15], a[220:223], v[84:87]\n" \
"ds_read_b128 a[8:11], v200 offset:0\n" \
"v_mfma_f32_16x16x32_bf16 v[92:95], a[16:19], a[216:219], v[92:95]\n" \
"ds_read_b128 a[12:15], v201 offset:0\n" \
"v_mfma_f32_16x16x32_bf16 v[92:95], a[20:23], a[220:223], v[92:95]\n" \
"buffer_load_dwordx4 v193, s[44:47], s73 offen offset:3072 lds\n" \
"v_mfma_f32_16x16x32_bf16 v[88:91], a[16:19], a[208:211], v[88:91]\n" \
"s_xor_b32 s73, s73, 0x180\n" \
"s_mul_i32 s76, s80, 393216\n" \
"ds_read_b64_tr_b16 a[52:53], v222 offset:28672\n" \
"v_mfma_f32_16x16x32_bf16 v[88:91], a[20:23], a[212:215], v[88:91]\n" \
"s_add_u32 s76, s76, s78\n" \
"ds_read_b128 a[16:19], v200 offset:4096\n" \
"v_mfma_f32_16x16x32_bf16 v[96:99], a[24:27], a[208:211], v[96:99]\n" \
"s_add_u32 m0, s91, 81920\n" \
"ds_read_b128 a[20:23], v201 offset:4096\n" \
"v_mfma_f32_16x16x32_bf16 v[96:99], a[28:31], a[212:215], v[96:99]\n" \
"buffer_load_dwordx4 v197, s[48:51], s76 offen offset:0 lds\n" \
"v_mfma_f32_16x16x32_bf16 v[100:103], a[24:27], a[216:219], v[100:103]\n" \
"ds_read_b64_tr_b16 a[54:55], v222 offset:31744\n" \
"v_mfma_f32_16x16x32_bf16 v[100:103], a[28:31], a[220:223], v[100:103]\n" \
"ds_read_b128 a[24:27], v200 offset:8192\n" \
"v_mfma_f32_16x16x32_bf16 v[108:111], a[192:195], a[216:219], v[108:111]\n" \
"s_add_u32 s77, s76, 24576\n" \
"ds_read_b128 a[28:31], v201 offset:8192\n" \
"v_mfma_f32_16x16x32_bf16 v[108:111], a[196:199], a[220:223], v[108:111]\n" \
"buffer_load_dwordx4 v198, s[48:51], s76 offen offset:1024 lds\n" \
"v_mfma_f32_16x16x32_bf16 v[104:107], a[192:195], a[208:211], v[104:107]\n" \
"v_mfma_f32_16x16x32_bf16 v[104:107], a[196:199], a[212:215], v[104:107]\n" \
"s_waitcnt lgkmcnt(2)\n" \
"v_mfma_f32_16x16x32_bf16 v[32:35], a[32:35], a[0:3], v[32:35]\n" \
"s_xor_b32 s80, s80, 0x3\n" \
"ds_read_b128 a[192:195], v200 offset:12288\n" \
"v_mfma_f32_16x16x32_bf16 v[32:35], a[36:39], a[4:7], v[32:35]\n" \
"buffer_load_dwordx4 v199, s[48:51], s76 offen offset:2048 lds\n" \
"v_mfma_f32_16x16x32_bf16 v[40:43], a[32:35], a[8:11], v[40:43]\n" \
"ds_read_b128 a[196:199], v201 offset:12288\n" \
"v_mfma_f32_16x16x32_bf16 v[40:43], a[36:39], a[12:15], v[40:43]\n" \
"ds_read_b64_tr_b16 a[200:201], v222 offset:17408\n" \
"v_mfma_f32_16x16x32_bf16 v[0:3], a[40:43], a[8:11], v[0:3]\n" \
"s_add_u32 m0, m0, 3072\n" \
"ds_read_b64_tr_b16 a[202:203], v222 offset:20480\n" \
"v_mfma_f32_16x16x32_bf16 v[0:3], a[44:47], a[12:15], v[0:3]\n" \
"buffer_load_dwordx4 v197, s[48:51], s77 offen offset:0 lds\n" \
"v_mfma_f32_16x16x32_bf16 v[4:7], a[48:51], a[8:11], v[4:7]\n" \
"ds_read_b64_tr_b16 a[204:205], v222 offset:29696\n" \
"v_mfma_f32_16x16x32_bf16 v[4:7], a[52:55], a[12:15], v[4:7]\n" \
"ds_read_b64_tr_b16 a[206:207], v222 offset:32768\n" \
"v_mfma_f32_16x16x32_bf16 v[12:15], a[48:51], a[16:19], v[12:15]\n" \
"ds_read_b64_tr_b16 a[208:209], v204 offset:18432\n" \
"v_mfma_f32_16x16x32_bf16 v[12:15], a[52:55], a[20:23], v[12:15]\n" \
"buffer_load_dwordx4 v198, s[48:51], s77 offen offset:1024 lds\n" \
"v_mfma_f32_16x16x32_bf16 v[8:11], a[40:43], a[16:19], v[8:11]\n" \
"ds_read_b64_tr_b16 a[210:211], v204 offset:21504\n" \
"v_mfma_f32_16x16x32_bf16 v[8:11], a[44:47], a[20:23], v[8:11]\n" \
"ds_read_b64_tr_b16 a[212:213], v204 offset:30720\n" \
"v_mfma_f32_16x16x32_bf16 v[48:51], a[32:35], a[16:19], v[48:51]\n" \
"s_waitcnt lgkmcnt(3)\n" \
"ds_read_b64_tr_b16 a[214:215], v204 offset:33792\n" \
"v_mfma_f32_16x16x32_bf16 v[48:51], a[36:39], a[20:23], v[48:51]\n" \
"buffer_load_dwordx4 v199, s[48:51], s77 offen offset:2048 lds\n" \
"v_mfma_f32_16x16x32_bf16 v[56:59], a[32:35], a[24:27], v[56:59]\n" \
"ds_read_b64_tr_b16 a[216:217], v222 offset:18432\n" \
"v_mfma_f32_16x16x32_bf16 v[56:59], a[36:39], a[28:31], v[56:59]\n" \
"ds_read_b64_tr_b16 a[218:219], v222 offset:21504\n" \
"v_mfma_f32_16x16x32_bf16 v[16:19], a[40:43], a[24:27], v[16:19]\n" \
"ds_read_b64_tr_b16 a[220:221], v222 offset:30720\n" \
"v_mfma_f32_16x16x32_bf16 v[16:19], a[44:47], a[28:31], v[16:19]\n" \
"v_mfma_f32_16x16x32_bf16 v[20:23], a[48:51], a[24:27], v[20:23]\n" \
"ds_read_b64_tr_b16 a[222:223], v222 offset:33792\n" \
"v_mfma_f32_16x16x32_bf16 v[20:23], a[52:55], a[28:31], v[20:23]\n" \
"v_mfma_f32_16x16x32_bf16 v[28:31], a[48:51], a[192:195], v[28:31]\n" \
"v_mfma_f32_16x16x32_bf16 v[28:31], a[52:55], a[196:199], v[28:31]\n" \
"v_mfma_f32_16x16x32_bf16 v[24:27], a[40:43], a[192:195], v[24:27]\n" \
"v_mfma_f32_16x16x32_bf16 v[24:27], a[44:47], a[196:199], v[24:27]\n" \
"v_mfma_f32_16x16x32_bf16 v[64:67], a[32:35], a[192:195], v[64:67]\n" \
"v_mfma_f32_16x16x32_bf16 v[64:67], a[36:39], a[196:199], v[64:67]\n" \
"s_waitcnt vmcnt(10) lgkmcnt(0)\n" \
"s_barrier\n" \
"v_mfma_f32_16x16x32_bf16 v[68:71], a[200:203], a[192:195], v[68:71]\n" \
"ds_read_b64_tr_b16 a[32:33], v209 offset:25600\n" \
"ds_read_b64_tr_b16 a[34:35], v209 offset:28672\n" \
"v_mfma_f32_16x16x32_bf16 v[68:71], a[204:207], a[196:199], v[68:71]\n" \
"ds_read_b64_tr_b16 a[36:37], v209 offset:37888\n" \
"ds_read_b64_tr_b16 a[38:39], v209 offset:40960\n" \
"v_mfma_f32_16x16x32_bf16 v[60:63], a[200:203], a[24:27], v[60:63]\n" \
"s_add_u32 m0, s79, 0\n" \
"ds_read_b64_tr_b16 a[40:41], v209 offset:24576\n" \
"v_mfma_f32_16x16x32_bf16 v[60:63], a[204:207], a[28:31], v[60:63]\n" \
"buffer_load_dwordx4 v192, s[44:47], s73 offen offset:0 lds\n" \
"v_mfma_f32_16x16x32_bf16 v[52:55], a[200:203], a[16:19], v[52:55]\n" \
"ds_read_b64_tr_b16 a[42:43], v209 offset:27648\n" \
"v_mfma_f32_16x16x32_bf16 v[52:55], a[204:207], a[20:23], v[52:55]\n" \
"ds_read_b64_tr_b16 a[44:45], v209 offset:36864\n" \
"Lpg9038_%=:\n" \
"Lst1x2_%=:\n" \
"s_cbranch_execz Lpg10884_%=\n" \
"v_mfma_f32_16x16x32_bf16 v[44:47], a[200:203], a[8:11], v[44:47]\n" \
"ds_read_b64_tr_b16 a[46:47], v209 offset:39936\n" \
"v_mfma_f32_16x16x32_bf16 v[44:47], a[204:207], a[12:15], v[44:47]\n" \
"buffer_load_dwordx4 v193, s[44:47], s73 offen offset:1024 lds\n" \
"v_mfma_f32_16x16x32_bf16 v[72:75], a[0:3], a[208:211], v[72:75]\n" \
"ds_read_b64_tr_b16 a[48:49], v210 offset:24576\n" \
"v_mfma_f32_16x16x32_bf16 v[72:75], a[4:7], a[212:215], v[72:75]\n" \
"ds_read_b128 a[0:3], v202 offset:20480\n" \
"v_mfma_f32_16x16x32_bf16 v[80:83], a[8:11], a[208:211], v[80:83]\n" \
"ds_read_b128 a[4:7], v203 offset:20480\n" \
"v_mfma_f32_16x16x32_bf16 v[80:83], a[12:15], a[212:215], v[80:83]\n" \
"buffer_load_dwordx4 v192, s[44:47], s73 offen offset:2048 lds\n" \
"v_mfma_f32_16x16x32_bf16 v[84:87], a[8:11], a[216:219], v[84:87]\n" \
"ds_read_b64_tr_b16 a[50:51], v210 offset:27648\n" \
"v_mfma_f32_16x16x32_bf16 v[84:87], a[12:15], a[220:223], v[84:87]\n" \
"ds_read_b128 a[8:11], v200 offset:40960\n" \
"v_mfma_f32_16x16x32_bf16 v[92:95], a[16:19], a[216:219], v[92:95]\n" \
"ds_read_b128 a[12:15], v201 offset:40960\n" \
"v_mfma_f32_16x16x32_bf16 v[92:95], a[20:23], a[220:223], v[92:95]\n" \
"buffer_load_dwordx4 v193, s[44:47], s73 offen offset:3072 lds\n" \
"v_mfma_f32_16x16x32_bf16 v[88:91], a[16:19], a[208:211], v[88:91]\n" \
"s_xor_b32 s73, s73, 0x80\n" \
"s_mul_i32 s76, s80, 393216\n" \
"ds_read_b64_tr_b16 a[52:53], v210 offset:36864\n" \
"v_mfma_f32_16x16x32_bf16 v[88:91], a[20:23], a[212:215], v[88:91]\n" \
"s_add_u32 s76, s76, s78\n" \
"ds_read_b128 a[16:19], v200 offset:45056\n" \
"v_mfma_f32_16x16x32_bf16 v[96:99], a[24:27], a[208:211], v[96:99]\n" \
"s_add_u32 m0, s91, 16384\n" \
"ds_read_b128 a[20:23], v201 offset:45056\n" \
"v_mfma_f32_16x16x32_bf16 v[96:99], a[28:31], a[212:215], v[96:99]\n" \
"buffer_load_dwordx4 v197, s[48:51], s76 offen offset:0 lds\n" \
"v_mfma_f32_16x16x32_bf16 v[100:103], a[24:27], a[216:219], v[100:103]\n" \
"ds_read_b64_tr_b16 a[54:55], v210 offset:39936\n" \
"v_mfma_f32_16x16x32_bf16 v[100:103], a[28:31], a[220:223], v[100:103]\n" \
"ds_read_b128 a[24:27], v200 offset:49152\n" \
"v_mfma_f32_16x16x32_bf16 v[108:111], a[192:195], a[216:219], v[108:111]\n" \
"s_add_u32 s77, s76, 24576\n" \
"ds_read_b128 a[28:31], v201 offset:49152\n" \
"v_mfma_f32_16x16x32_bf16 v[108:111], a[196:199], a[220:223], v[108:111]\n" \
"buffer_load_dwordx4 v198, s[48:51], s76 offen offset:1024 lds\n" \
"v_mfma_f32_16x16x32_bf16 v[104:107], a[192:195], a[208:211], v[104:107]\n" \
"v_mfma_f32_16x16x32_bf16 v[104:107], a[196:199], a[212:215], v[104:107]\n" \
"s_waitcnt lgkmcnt(2)\n" \
"v_mfma_f32_16x16x32_bf16 v[32:35], a[32:35], a[0:3], v[32:35]\n" \
"s_xor_b32 s80, s80, 0x1\n" \
"ds_read_b128 a[192:195], v200 offset:53248\n" \
"v_mfma_f32_16x16x32_bf16 v[32:35], a[36:39], a[4:7], v[32:35]\n" \
"buffer_load_dwordx4 v199, s[48:51], s76 offen offset:2048 lds\n" \
"v_mfma_f32_16x16x32_bf16 v[40:43], a[32:35], a[8:11], v[40:43]\n" \
"ds_read_b128 a[196:199], v201 offset:53248\n" \
"v_mfma_f32_16x16x32_bf16 v[40:43], a[36:39], a[12:15], v[40:43]\n" \
"ds_read_b64_tr_b16 a[200:201], v210 offset:25600\n" \
"v_mfma_f32_16x16x32_bf16 v[0:3], a[40:43], a[8:11], v[0:3]\n" \
"s_add_u32 m0, m0, 3072\n" \
"ds_read_b64_tr_b16 a[202:203], v210 offset:28672\n" \
"v_mfma_f32_16x16x32_bf16 v[0:3], a[44:47], a[12:15], v[0:3]\n" \
"buffer_load_dwordx4 v197, s[48:51], s77 offen offset:0 lds\n" \
"v_mfma_f32_16x16x32_bf16 v[4:7], a[48:51], a[8:11], v[4:7]\n" \
"ds_read_b64_tr_b16 a[204:205], v210 offset:37888\n" \
"v_mfma_f32_16x16x32_bf16 v[4:7], a[52:55], a[12:15], v[4:7]\n" \
"ds_read_b64_tr_b16 a[206:207], v210 offset:40960\n" \
"v_mfma_f32_16x16x32_bf16 v[12:15], a[48:51], a[16:19], v[12:15]\n" \
"ds_read_b64_tr_b16 a[208:209], v209 offset:26624\n" \
"v_mfma_f32_16x16x32_bf16 v[12:15], a[52:55], a[20:23], v[12:15]\n" \
"buffer_load_dwordx4 v198, s[48:51], s77 offen offset:1024 lds\n" \
"v_mfma_f32_16x16x32_bf16 v[8:11], a[40:43], a[16:19], v[8:11]\n" \
"ds_read_b64_tr_b16 a[210:211], v209 offset:29696\n" \
"v_mfma_f32_16x16x32_bf16 v[8:11], a[44:47], a[20:23], v[8:11]\n" \
"ds_read_b64_tr_b16 a[212:213], v209 offset:38912\n" \
"v_mfma_f32_16x16x32_bf16 v[48:51], a[32:35], a[16:19], v[48:51]\n" \
"s_waitcnt lgkmcnt(3)\n" \
"ds_read_b64_tr_b16 a[214:215], v209 offset:41984\n" \
"v_mfma_f32_16x16x32_bf16 v[48:51], a[36:39], a[20:23], v[48:51]\n" \
"buffer_load_dwordx4 v199, s[48:51], s77 offen offset:2048 lds\n" \
"v_mfma_f32_16x16x32_bf16 v[56:59], a[32:35], a[24:27], v[56:59]\n" \
"ds_read_b64_tr_b16 a[216:217], v210 offset:26624\n" \
"v_mfma_f32_16x16x32_bf16 v[56:59], a[36:39], a[28:31], v[56:59]\n" \
"ds_read_b64_tr_b16 a[218:219], v210 offset:29696\n" \
"v_mfma_f32_16x16x32_bf16 v[16:19], a[40:43], a[24:27], v[16:19]\n" \
"ds_read_b64_tr_b16 a[220:221], v210 offset:38912\n" \
"v_mfma_f32_16x16x32_bf16 v[16:19], a[44:47], a[28:31], v[16:19]\n" \
"v_mfma_f32_16x16x32_bf16 v[20:23], a[48:51], a[24:27], v[20:23]\n" \
"ds_read_b64_tr_b16 a[222:223], v210 offset:41984\n" \
"v_mfma_f32_16x16x32_bf16 v[20:23], a[52:55], a[28:31], v[20:23]\n" \
"v_mfma_f32_16x16x32_bf16 v[28:31], a[48:51], a[192:195], v[28:31]\n" \
"v_mfma_f32_16x16x32_bf16 v[28:31], a[52:55], a[196:199], v[28:31]\n" \
"buffer_load_dword v220, v221, s[56:59], s66 offen sc1\n" \
"v_mfma_f32_16x16x32_bf16 v[24:27], a[40:43], a[192:195], v[24:27]\n" \
"v_mfma_f32_16x16x32_bf16 v[24:27], a[44:47], a[196:199], v[24:27]\n" \
"v_mfma_f32_16x16x32_bf16 v[64:67], a[32:35], a[192:195], v[64:67]\n" \
"v_mfma_f32_16x16x32_bf16 v[64:67], a[36:39], a[196:199], v[64:67]\n" \
"s_waitcnt vmcnt(11) lgkmcnt(0)\n" \
"s_barrier\n" \
"v_mfma_f32_16x16x32_bf16 v[68:71], a[200:203], a[192:195], v[68:71]\n" \
"ds_read_b64_tr_b16 a[32:33], v209 offset:1024\n" \
"ds_read_b64_tr_b16 a[34:35], v209 offset:4096\n" \
"v_mfma_f32_16x16x32_bf16 v[68:71], a[204:207], a[196:199], v[68:71]\n" \
"ds_read_b64_tr_b16 a[36:37], v209 offset:13312\n" \
"ds_read_b64_tr_b16 a[38:39], v209 offset:16384\n" \
"v_mfma_f32_16x16x32_bf16 v[60:63], a[200:203], a[24:27], v[60:63]\n" \
"s_add_u32 m0, s79, 131072\n" \
"ds_read_b64_tr_b16 a[40:41], v209 offset:0\n" \
"v_mfma_f32_16x16x32_bf16 v[60:63], a[204:207], a[28:31], v[60:63]\n" \
"buffer_load_dwordx4 v192, s[44:47], s73 offen offset:0 lds\n" \
"v_mfma_f32_16x16x32_bf16 v[52:55], a[200:203], a[16:19], v[52:55]\n" \
"ds_read_b64_tr_b16 a[42:43], v209 offset:3072\n" \
"v_mfma_f32_16x16x32_bf16 v[52:55], a[204:207], a[20:23], v[52:55]\n" \
"ds_read_b64_tr_b16 a[44:45], v209 offset:12288\n" \
"v_mfma_f32_16x16x32_bf16 v[44:47], a[200:203], a[8:11], v[44:47]\n" \
"ds_read_b64_tr_b16 a[46:47], v209 offset:15360\n" \
"v_mfma_f32_16x16x32_bf16 v[44:47], a[204:207], a[12:15], v[44:47]\n" \
"buffer_load_dwordx4 v193, s[44:47], s73 offen offset:1024 lds\n" \
"v_mfma_f32_16x16x32_bf16 v[72:75], a[0:3], a[208:211], v[72:75]\n" \
"ds_read_b64_tr_b16 a[48:49], v210 offset:0\n" \
"v_mfma_f32_16x16x32_bf16 v[72:75], a[4:7], a[212:215], v[72:75]\n" \
"ds_read_b128 a[0:3], v202 offset:21504\n" \
"v_mfma_f32_16x16x32_bf16 v[80:83], a[8:11], a[208:211], v[80:83]\n" \
"ds_read_b128 a[4:7], v203 offset:21504\n" \
"v_mfma_f32_16x16x32_bf16 v[80:83], a[12:15], a[212:215], v[80:83]\n" \
"buffer_load_dwordx4 v192, s[44:47], s73 offen offset:2048 lds\n" \
"v_mfma_f32_16x16x32_bf16 v[84:87], a[8:11], a[216:219], v[84:87]\n" \
"ds_read_b64_tr_b16 a[50:51], v210 offset:3072\n" \
"v_mfma_f32_16x16x32_bf16 v[84:87], a[12:15], a[220:223], v[84:87]\n" \
"ds_read_b128 a[8:11], v205 offset:0\n" \
"v_mfma_f32_16x16x32_bf16 v[92:95], a[16:19], a[216:219], v[92:95]\n" \
"ds_read_b128 a[12:15], v206 offset:0\n" \
"v_mfma_f32_16x16x32_bf16 v[92:95], a[20:23], a[220:223], v[92:95]\n" \
"buffer_load_dwordx4 v193, s[44:47], s73 offen offset:3072 lds\n" \
"v_mfma_f32_16x16x32_bf16 v[88:91], a[16:19], a[208:211], v[88:91]\n" \
"s_xor_b32 s73, s73, 0x80\n" \
"s_mul_i32 s76, s80, 393216\n" \
"ds_read_b64_tr_b16 a[52:53], v210 offset:12288\n" \
"v_mfma_f32_16x16x32_bf16 v[88:91], a[20:23], a[212:215], v[88:91]\n" \
"s_add_u32 s76, s76, s78\n" \
"ds_read_b128 a[16:19], v205 offset:4096\n" \
"v_mfma_f32_16x16x32_bf16 v[96:99], a[24:27], a[208:211], v[96:99]\n" \
"s_add_u32 m0, s91, 106496\n" \
"ds_read_b128 a[20:23], v206 offset:4096\n" \
"v_mfma_f32_16x16x32_bf16 v[96:99], a[28:31], a[212:215], v[96:99]\n" \
"buffer_load_dwordx4 v197, s[48:51], s76 offen offset:0 lds\n" \
"v_mfma_f32_16x16x32_bf16 v[100:103], a[24:27], a[216:219], v[100:103]\n" \
"ds_read_b64_tr_b16 a[54:55], v210 offset:15360\n" \
"v_mfma_f32_16x16x32_bf16 v[100:103], a[28:31], a[220:223], v[100:103]\n" \
"ds_read_b128 a[24:27], v205 offset:8192\n" \
"v_mfma_f32_16x16x32_bf16 v[108:111], a[192:195], a[216:219], v[108:111]\n" \
"s_add_u32 s77, s76, 24576\n" \
"ds_read_b128 a[28:31], v206 offset:8192\n" \
"v_mfma_f32_16x16x32_bf16 v[108:111], a[196:199], a[220:223], v[108:111]\n" \
"buffer_load_dwordx4 v198, s[48:51], s76 offen offset:1024 lds\n" \
"v_mfma_f32_16x16x32_bf16 v[104:107], a[192:195], a[208:211], v[104:107]\n" \
"v_mfma_f32_16x16x32_bf16 v[104:107], a[196:199], a[212:215], v[104:107]\n" \
"s_waitcnt lgkmcnt(2)\n" \
"v_mfma_f32_16x16x32_bf16 v[32:35], a[32:35], a[0:3], v[32:35]\n" \
"s_xor_b32 s80, s80, 0x1\n" \
"ds_read_b128 a[192:195], v205 offset:12288\n" \
"v_mfma_f32_16x16x32_bf16 v[32:35], a[36:39], a[4:7], v[32:35]\n" \
"buffer_load_dwordx4 v199, s[48:51], s76 offen offset:2048 lds\n" \
"v_mfma_f32_16x16x32_bf16 v[40:43], a[32:35], a[8:11], v[40:43]\n" \
"ds_read_b128 a[196:199], v206 offset:12288\n" \
"v_mfma_f32_16x16x32_bf16 v[40:43], a[36:39], a[12:15], v[40:43]\n" \
"ds_read_b64_tr_b16 a[200:201], v210 offset:1024\n" \
"v_mfma_f32_16x16x32_bf16 v[0:3], a[40:43], a[8:11], v[0:3]\n" \
"s_add_u32 m0, m0, 3072\n" \
"ds_read_b64_tr_b16 a[202:203], v210 offset:4096\n" \
"v_mfma_f32_16x16x32_bf16 v[0:3], a[44:47], a[12:15], v[0:3]\n" \
"buffer_load_dwordx4 v197, s[48:51], s77 offen offset:0 lds\n" \
"v_mfma_f32_16x16x32_bf16 v[4:7], a[48:51], a[8:11], v[4:7]\n" \
"ds_read_b64_tr_b16 a[204:205], v210 offset:13312\n" \
"v_mfma_f32_16x16x32_bf16 v[4:7], a[52:55], a[12:15], v[4:7]\n" \
"ds_read_b64_tr_b16 a[206:207], v210 offset:16384\n" \
"v_mfma_f32_16x16x32_bf16 v[12:15], a[48:51], a[16:19], v[12:15]\n" \
"ds_read_b64_tr_b16 a[208:209], v209 offset:2048\n" \
"v_mfma_f32_16x16x32_bf16 v[12:15], a[52:55], a[20:23], v[12:15]\n" \
"buffer_load_dwordx4 v198, s[48:51], s77 offen offset:1024 lds\n" \
"v_mfma_f32_16x16x32_bf16 v[8:11], a[40:43], a[16:19], v[8:11]\n" \
"ds_read_b64_tr_b16 a[210:211], v209 offset:5120\n" \
"v_mfma_f32_16x16x32_bf16 v[8:11], a[44:47], a[20:23], v[8:11]\n" \
"ds_read_b64_tr_b16 a[212:213], v209 offset:14336\n" \
"v_mfma_f32_16x16x32_bf16 v[48:51], a[32:35], a[16:19], v[48:51]\n" \
"s_waitcnt lgkmcnt(3)\n" \
"ds_read_b64_tr_b16 a[214:215], v209 offset:17408\n" \
"v_mfma_f32_16x16x32_bf16 v[48:51], a[36:39], a[20:23], v[48:51]\n" \
"buffer_load_dwordx4 v199, s[48:51], s77 offen offset:2048 lds\n" \
"v_mfma_f32_16x16x32_bf16 v[56:59], a[32:35], a[24:27], v[56:59]\n" \
"ds_read_b64_tr_b16 a[216:217], v210 offset:2048\n" \
"v_mfma_f32_16x16x32_bf16 v[56:59], a[36:39], a[28:31], v[56:59]\n" \
"ds_read_b64_tr_b16 a[218:219], v210 offset:5120\n" \
"v_mfma_f32_16x16x32_bf16 v[16:19], a[40:43], a[24:27], v[16:19]\n" \
"ds_read_b64_tr_b16 a[220:221], v210 offset:14336\n" \
"v_mfma_f32_16x16x32_bf16 v[16:19], a[44:47], a[28:31], v[16:19]\n" \
"v_mfma_f32_16x16x32_bf16 v[20:23], a[48:51], a[24:27], v[20:23]\n" \
"s_mov_b32 s46, s81\n" \
"s_mov_b32 s47, s82\n" \
"ds_read_b64_tr_b16 a[222:223], v210 offset:17408\n" \
"v_mfma_f32_16x16x32_bf16 v[20:23], a[52:55], a[28:31], v[20:23]\n" \
"s_mov_b32 s48, 0\n" \
"s_mov_b32 s49, s68\n" \
"s_sub_u32 s50, 64, s9\n" \
"v_mfma_f32_16x16x32_bf16 v[28:31], a[48:51], a[192:195], v[28:31]\n" \
"s_cmp_eq_u32 s90, 0\n" \
"s_cselect_b32 s50, s9, s50\n" \
"s_andn2_b32 s50, s50, 3\n" \
"v_mfma_f32_16x16x32_bf16 v[28:31], a[52:55], a[196:199], v[28:31]\n" \
"s_lshl_b32 s50, s50, 12\n" \
"s_waitcnt vmcnt(10)\n" \
"v_cmp_eq_u32 vcc, s60, v220\n" \
"v_mfma_f32_16x16x32_bf16 v[24:27], a[40:43], a[192:195], v[24:27]\n" \
"v_mfma_f32_16x16x32_bf16 v[24:27], a[44:47], a[196:199], v[24:27]\n" \
"s_andn2_b32 s70, s65, vcc_lo\n" \
"s_cbranch_scc1 Lpl5_%=\n" \
"Lpr5_%=:\n" \
"v_mfma_f32_16x16x32_bf16 v[64:67], a[32:35], a[192:195], v[64:67]\n" \
"v_mfma_f32_16x16x32_bf16 v[64:67], a[36:39], a[196:199], v[64:67]\n" \
"s_xor_b32 s82, s48, s49\n" \
"s_cmp_lt_u32 s48, s50\n" \
"s_cselect_b32 s82, s82, s48\n" \
"s_waitcnt vmcnt(11) lgkmcnt(0)\n" \
"s_barrier\n" \
"v_mfma_f32_16x16x32_bf16 v[68:71], a[200:203], a[192:195], v[68:71]\n" \
"ds_read_b64_tr_b16 a[32:33], v204 offset:17408\n" \
"ds_read_b64_tr_b16 a[34:35], v204 offset:20480\n" \
"v_mfma_f32_16x16x32_bf16 v[68:71], a[204:207], a[196:199], v[68:71]\n" \
"s_add_u32 s81, s82, s46\n" \
"s_lshr_b32 s78, s61, 1\n" \
"s_lshl_b32 s80, s78, 9\n" \
"s_add_u32 s80, s80, 511\n" \
"ds_read_b64_tr_b16 a[36:37], v204 offset:29696\n" \
"ds_read_b64_tr_b16 a[38:39], v204 offset:32768\n" \
"v_mfma_f32_16x16x32_bf16 v[60:63], a[200:203], a[24:27], v[60:63]\n" \
"s_add_u32 m0, s86, s79\n" \
"ds_read_b64_tr_b16 a[40:41], v204 offset:16384\n" \
"v_sub_u32 v145, s80, v234\n" \
"v_mfma_f32_16x16x32_bf16 v[60:63], a[204:207], a[28:31], v[60:63]\n" \
"buffer_load_dwordx4 v234, s[56:59], s81 offen offset:0 sc1 lds\n" \
"v_mfma_f32_16x16x32_bf16 v[52:55], a[200:203], a[16:19], v[52:55]\n" \
"s_lshl_b32 s80, s78, 3\n" \
"s_not_b32 s80, s80\n" \
"ds_read_b64_tr_b16 a[42:43], v204 offset:19456\n" \
"v_mfma_f32_16x16x32_bf16 v[52:55], a[204:207], a[20:23], v[52:55]\n" \
"s_lshl_b32 s82, s82, 5\n" \
"ds_read_b64_tr_b16 a[44:45], v204 offset:28672\n" \
"v_sub_u32 v146, s80, v238\n" \
"v_mfma_f32_16x16x32_bf16 v[44:47], a[200:203], a[8:11], v[44:47]\n" \
"s_add_u32 s82, s82, s47\n" \
"ds_read_b64_tr_b16 a[46:47], v204 offset:31744\n" \
"v_subrev_u32 v152, 0, v238\n" \
"v_mfma_f32_16x16x32_bf16 v[44:47], a[204:207], a[12:15], v[44:47]\n" \
"buffer_load_dwordx4 v234, s[56:59], s81 offen offset:1024 sc1 lds\n" \
"v_mfma_f32_16x16x32_bf16 v[72:75], a[0:3], a[208:211], v[72:75]\n" \
"ds_read_b64_tr_b16 a[48:49], v222 offset:16384\n" \
"v_sub_u32 v144, 7, v238\n" \
"v_mfma_f32_16x16x32_bf16 v[72:75], a[4:7], a[212:215], v[72:75]\n" \
"ds_read_b128 a[0:3], v202 offset:22528\n" \
"v_or_b32 v152, v152, v144\n" \
"v_mfma_f32_16x16x32_bf16 v[80:83], a[8:11], a[208:211], v[80:83]\n" \
"ds_read_b128 a[4:7], v203 offset:22528\n" \
"v_ashrrev_i32 v152, 31, v152\n" \
"v_mfma_f32_16x16x32_bf16 v[80:83], a[12:15], a[212:215], v[80:83]\n" \
"buffer_load_dwordx4 v234, s[56:59], s81 offen offset:2048 sc1 lds\n" \
"v_mfma_f32_16x16x32_bf16 v[84:87], a[8:11], a[216:219], v[84:87]\n" \
"ds_read_b64_tr_b16 a[50:51], v222 offset:19456\n" \
"v_and_b32 v152, 0xff800000, v152\n" \
"v_mfma_f32_16x16x32_bf16 v[84:87], a[12:15], a[220:223], v[84:87]\n" \
"ds_read_b128 a[8:11], v200 offset:0\n" \
"v_sub_u32 v156, -9, v238\n" \
"v_mfma_f32_16x16x32_bf16 v[92:95], a[16:19], a[216:219], v[92:95]\n" \
"ds_read_b128 a[12:15], v201 offset:0\n" \
"v_ashrrev_i32 v156, 31, v156\n" \
"v_mfma_f32_16x16x32_bf16 v[92:95], a[20:23], a[220:223], v[92:95]\n" \
"buffer_load_dwordx4 v234, s[56:59], s81 offen offset:3072 sc1 lds\n" \
"v_mfma_f32_16x16x32_bf16 v[88:91], a[16:19], a[208:211], v[88:91]\n" \
"ds_read_b64_tr_b16 a[52:53], v222 offset:28672\n" \
"v_and_b32 v156, 0xff800000, v156\n" \
"v_mfma_f32_16x16x32_bf16 v[88:91], a[20:23], a[212:215], v[88:91]\n" \
"ds_read_b128 a[16:19], v200 offset:4096\n" \
"v_add_u32 v148, 0, v146\n" \
"v_mfma_f32_16x16x32_bf16 v[96:99], a[24:27], a[208:211], v[96:99]\n" \
"s_add_u32 m0, m0, 0x4000\n" \
"ds_read_b128 a[20:23], v201 offset:4096\n" \
"v_or3_b32 v148, v148, v145, s92\n" \
"v_mfma_f32_16x16x32_bf16 v[96:99], a[28:31], a[212:215], v[96:99]\n" \
"buffer_load_dwordx4 v216, s[36:39], s82 offen offset:0 lds\n" \
"v_mfma_f32_16x16x32_bf16 v[100:103], a[24:27], a[216:219], v[100:103]\n" \
"ds_read_b64_tr_b16 a[54:55], v222 offset:31744\n" \
"v_ashrrev_i32 v148, 31, v148\n" \
"v_mfma_f32_16x16x32_bf16 v[100:103], a[28:31], a[220:223], v[100:103]\n" \
"ds_read_b128 a[24:27], v200 offset:8192\n" \
"v_and_b32 v148, 0xff800000, v148\n" \
"v_mfma_f32_16x16x32_bf16 v[108:111], a[192:195], a[216:219], v[108:111]\n" \
"s_add_u32 s48, s48, 0x1000\n" \
"ds_read_b128 a[28:31], v201 offset:8192\n" \
"v_subrev_u32 v153, 1, v238\n" \
"v_mfma_f32_16x16x32_bf16 v[108:111], a[196:199], a[220:223], v[108:111]\n" \
"buffer_load_dwordx4 v217, s[36:39], s82 offen offset:1024 lds\n" \
"v_mfma_f32_16x16x32_bf16 v[104:107], a[192:195], a[208:211], v[104:107]\n" \
"v_sub_u32 v144, 8, v238\n" \
"v_or_b32 v153, v153, v144\n" \
"v_mfma_f32_16x16x32_bf16 v[104:107], a[196:199], a[212:215], v[104:107]\n" \
"s_waitcnt lgkmcnt(2)\n" \
"v_ashrrev_i32 v153, 31, v153\n" \
"v_and_b32 v153, 0xff800000, v153\n" \
"v_mfma_f32_16x16x32_bf16 v[32:35], a[32:35], a[0:3], v[32:35]\n" \
"s_sub_u32 s86, s86, 32768\n" \
"s_cselect_b32 s86, 131072, s86\n" \
"ds_read_b128 a[192:195], v200 offset:12288\n" \
"v_mfma_f32_16x16x32_bf16 v[32:35], a[36:39], a[4:7], v[32:35]\n" \
"buffer_load_dwordx4 v218, s[36:39], s82 offen offset:2048 lds\n" \
"v_mfma_f32_16x16x32_bf16 v[40:43], a[32:35], a[8:11], v[40:43]\n" \
"ds_read_b128 a[196:199], v201 offset:12288\n" \
"v_sub_u32 v157, -8, v238\n" \
"v_mfma_f32_16x16x32_bf16 v[40:43], a[36:39], a[12:15], v[40:43]\n" \
"ds_read_b64_tr_b16 a[200:201], v222 offset:17408\n" \
"v_ashrrev_i32 v157, 31, v157\n" \
"v_mfma_f32_16x16x32_bf16 v[0:3], a[40:43], a[8:11], v[0:3]\n" \
"ds_read_b64_tr_b16 a[202:203], v222 offset:20480\n" \
"v_and_b32 v157, 0xff800000, v157\n" \
"v_mfma_f32_16x16x32_bf16 v[0:3], a[44:47], a[12:15], v[0:3]\n" \
"buffer_load_dwordx4 v219, s[36:39], s82 offen offset:3072 lds\n" \
"v_mfma_f32_16x16x32_bf16 v[4:7], a[48:51], a[8:11], v[4:7]\n" \
"ds_read_b64_tr_b16 a[204:205], v222 offset:29696\n" \
"v_add_u32 v149, 1, v146\n" \
"v_mfma_f32_16x16x32_bf16 v[4:7], a[52:55], a[12:15], v[4:7]\n" \
"ds_read_b64_tr_b16 a[206:207], v222 offset:32768\n" \
"v_or3_b32 v149, v149, v145, s92\n" \
"v_mfma_f32_16x16x32_bf16 v[12:15], a[48:51], a[16:19], v[12:15]\n" \
"ds_read_b64_tr_b16 a[208:209], v204 offset:18432\n" \
"v_ashrrev_i32 v149, 31, v149\n" \
"v_mfma_f32_16x16x32_bf16 v[12:15], a[52:55], a[20:23], v[12:15]\n" \
"v_and_b32 v149, 0xff800000, v149\n" \
"v_subrev_u32 v154, 2, v238\n" \
"v_mfma_f32_16x16x32_bf16 v[8:11], a[40:43], a[16:19], v[8:11]\n" \
"ds_read_b64_tr_b16 a[210:211], v204 offset:21504\n" \
"v_sub_u32 v144, 9, v238\n" \
"v_mfma_f32_16x16x32_bf16 v[8:11], a[44:47], a[20:23], v[8:11]\n" \
"ds_read_b64_tr_b16 a[212:213], v204 offset:30720\n" \
"v_or_b32 v154, v154, v144\n" \
"v_mfma_f32_16x16x32_bf16 v[48:51], a[32:35], a[16:19], v[48:51]\n" \
"ds_read_b64_tr_b16 a[214:215], v204 offset:33792\n" \
"v_ashrrev_i32 v154, 31, v154\n" \
"v_mfma_f32_16x16x32_bf16 v[48:51], a[36:39], a[20:23], v[48:51]\n" \
"s_waitcnt lgkmcnt(3)\n" \
"v_and_b32 v154, 0xff800000, v154\n" \
"v_sub_u32 v158, -7, v238\n" \
"v_mfma_f32_16x16x32_bf16 v[56:59], a[32:35], a[24:27], v[56:59]\n" \
"ds_read_b64_tr_b16 a[216:217], v222 offset:18432\n" \
"v_ashrrev_i32 v158, 31, v158\n" \
"v_mfma_f32_16x16x32_bf16 v[56:59], a[36:39], a[28:31], v[56:59]\n" \
"ds_read_b64_tr_b16 a[218:219], v222 offset:21504\n" \
"v_and_b32 v158, 0xff800000, v158\n" \
"v_mfma_f32_16x16x32_bf16 v[16:19], a[40:43], a[24:27], v[16:19]\n" \
"ds_read_b64_tr_b16 a[220:221], v222 offset:30720\n" \
"v_add_u32 v150, 2, v146\n" \
"v_mfma_f32_16x16x32_bf16 v[16:19], a[44:47], a[28:31], v[16:19]\n" \
"v_or3_b32 v150, v150, v145, s92\n" \
"v_ashrrev_i32 v150, 31, v150\n" \
"v_mfma_f32_16x16x32_bf16 v[20:23], a[48:51], a[24:27], v[20:23]\n" \
"ds_read_b64_tr_b16 a[222:223], v222 offset:33792\n" \
"v_and_b32 v150, 0xff800000, v150\n" \
"v_mfma_f32_16x16x32_bf16 v[20:23], a[52:55], a[28:31], v[20:23]\n" \
"v_subrev_u32 v155, 3, v238\n" \
"v_sub_u32 v144, 10, v238\n" \
"v_mfma_f32_16x16x32_bf16 v[28:31], a[48:51], a[192:195], v[28:31]\n" \
"v_or_b32 v155, v155, v144\n" \
"v_ashrrev_i32 v155, 31, v155\n" \
"v_mfma_f32_16x16x32_bf16 v[28:31], a[52:55], a[196:199], v[28:31]\n" \
"v_and_b32 v155, 0xff800000, v155\n" \
"v_sub_u32 v159, -6, v238\n" \
"v_mfma_f32_16x16x32_bf16 v[24:27], a[40:43], a[192:195], v[24:27]\n" \
"v_ashrrev_i32 v159, 31, v159\n" \
"v_and_b32 v159, 0xff800000, v159\n" \
"v_mfma_f32_16x16x32_bf16 v[24:27], a[44:47], a[196:199], v[24:27]\n" \
"v_add_u32 v151, 3, v146\n" \
"v_or3_b32 v151, v151, v145, s92\n" \
"v_mfma_f32_16x16x32_bf16 v[64:67], a[32:35], a[192:195], v[64:67]\n" \
"v_ashrrev_i32 v151, 31, v151\n" \
"v_and_b32 v151, 0xff800000, v151\n" \
"v_mfma_f32_16x16x32_bf16 v[64:67], a[36:39], a[196:199], v[64:67]\n" \
"s_xor_b32 s82, s48, s49\n" \
"s_cmp_lt_u32 s48, s50\n" \
"s_cselect_b32 s82, s82, s48\n" \
"s_waitcnt vmcnt(8) lgkmcnt(0)\n" \
"s_barrier\n" \
"v_mfma_f32_16x16x32_bf16 v[68:71], a[200:203], a[192:195], v[68:71]\n" \
"ds_read_b64_tr_b16 a[32:33], v209 offset:25600\n" \
"ds_read_b64_tr_b16 a[34:35], v209 offset:28672\n" \
"v_mfma_f32_16x16x32_bf16 v[68:71], a[204:207], a[196:199], v[68:71]\n" \
"s_add_u32 s81, s82, s46\n" \
"ds_read_b64_tr_b16 a[36:37], v209 offset:37888\n" \
"ds_read_b64_tr_b16 a[38:39], v209 offset:40960\n" \
"v_mfma_f32_16x16x32_bf16 v[60:63], a[200:203], a[24:27], v[60:63]\n" \
"s_add_u32 m0, s86, s79\n" \
"ds_read_b64_tr_b16 a[40:41], v209 offset:24576\n" \
"v_mov_b32 v186, 0xffff0000\n" \
"v_mfma_f32_16x16x32_bf16 v[60:63], a[204:207], a[28:31], v[60:63]\n" \
"buffer_load_dwordx4 v234, s[56:59], s81 offen offset:0 sc1 lds\n" \
"v_mfma_f32_16x16x32_bf16 v[52:55], a[200:203], a[16:19], v[52:55]\n" \
"ds_read_b64_tr_b16 a[42:43], v209 offset:27648\n" \
"v_subrev_u32 v144, 0, v238\n" \
"v_mfma_f32_16x16x32_bf16 v[52:55], a[204:207], a[20:23], v[52:55]\n" \
"s_lshl_b32 s82, s82, 5\n" \
"ds_read_b64_tr_b16 a[44:45], v209 offset:36864\n" \
"v_subrev_u32 v184, 1, v238\n" \
"v_mfma_f32_16x16x32_bf16 v[44:47], a[200:203], a[8:11], v[44:47]\n" \
"s_add_u32 s82, s82, s47\n" \
"ds_read_b64_tr_b16 a[46:47], v209 offset:39936\n" \
"v_lshrrev_b32 v144, 16, v144\n" \
"v_mfma_f32_16x16x32_bf16 v[44:47], a[204:207], a[12:15], v[44:47]\n" \
"buffer_load_dwordx4 v234, s[56:59], s81 offen offset:1024 sc1 lds\n" \
"v_mfma_f32_16x16x32_bf16 v[72:75], a[0:3], a[208:211], v[72:75]\n" \
"ds_read_b64_tr_b16 a[48:49], v210 offset:24576\n" \
"v_and_or_b32 v184, v184, v186, v144\n" \
"v_mfma_f32_16x16x32_bf16 v[72:75], a[4:7], a[212:215], v[72:75]\n" \
"ds_read_b128 a[0:3], v202 offset:23552\n" \
"v_subrev_u32 v144, 2, v238\n" \
"v_mfma_f32_16x16x32_bf16 v[80:83], a[8:11], a[208:211], v[80:83]\n" \
"ds_read_b128 a[4:7], v203 offset:23552\n" \
"v_subrev_u32 v185, 3, v238\n" \
"v_mfma_f32_16x16x32_bf16 v[80:83], a[12:15], a[212:215], v[80:83]\n" \
"buffer_load_dwordx4 v234, s[56:59], s81 offen offset:2048 sc1 lds\n" \
"v_mfma_f32_16x16x32_bf16 v[84:87], a[8:11], a[216:219], v[84:87]\n" \
"ds_read_b64_tr_b16 a[50:51], v210 offset:27648\n" \
"v_lshrrev_b32 v144, 16, v144\n" \
"v_mfma_f32_16x16x32_bf16 v[84:87], a[12:15], a[220:223], v[84:87]\n" \
"ds_read_b128 a[8:11], v207 offset:0\n" \
"v_and_or_b32 v185, v185, v186, v144\n" \
"v_mfma_f32_16x16x32_bf16 v[92:95], a[16:19], a[216:219], v[92:95]\n" \
"ds_read_b128 a[12:15], v208 offset:0\n" \
"v_mov_b32 v144, 0x3e8293ee\n" \
"v_mfma_f32_16x16x32_bf16 v[92:95], a[20:23], a[220:223], v[92:95]\n" \
"buffer_load_dwordx4 v234, s[56:59], s81 offen offset:3072 sc1 lds\n" \
"v_mfma_f32_16x16x32_bf16 v[88:91], a[16:19], a[208:211], v[88:91]\n" \
"s_add_u32 s80, s79, 117760\n" \
"ds_read_b64_tr_b16 a[52:53], v210 offset:36864\n" \
"v_add_u32 v223, s80, v234\n" \
"v_mfma_f32_16x16x32_bf16 v[88:91], a[20:23], a[212:215], v[88:91]\n" \
"s_cmp_lg_u32 s78, 0\n" \
"s_cselect_b64 s[76:77], -1, 0\n" \
"ds_read_b128 a[16:19], v207 offset:4096\n" \
"v_mfma_f32_16x16x32_bf16 v[96:99], a[24:27], a[208:211], v[96:99]\n" \
"s_add_u32 m0, m0, 0x4000\n" \
"ds_read_b128 a[20:23], v208 offset:4096\n" \
"v_mfma_f32_16x16x32_bf16 v[96:99], a[28:31], a[212:215], v[96:99]\n" \
"buffer_load_dwordx4 v216, s[36:39], s82 offen offset:0 lds\n" \
"v_mfma_f32_16x16x32_bf16 v[100:103], a[24:27], a[216:219], v[100:103]\n" \
"ds_read_b64_tr_b16 a[54:55], v210 offset:39936\n" \
"v_mfma_f32_16x16x32_bf16 v[100:103], a[28:31], a[220:223], v[100:103]\n" \
"ds_read_b128 a[24:27], v207 offset:8192\n" \
"v_mfma_f32_16x16x32_bf16 v[108:111], a[192:195], a[216:219], v[108:111]\n" \
"s_add_u32 s48, s48, 0x1000\n" \
"ds_read_b128 a[28:31], v208 offset:8192\n" \
"v_mfma_f32_16x16x32_bf16 v[108:111], a[196:199], a[220:223], v[108:111]\n" \
"buffer_load_dwordx4 v217, s[36:39], s82 offen offset:1024 lds\n" \
"v_mfma_f32_16x16x32_bf16 v[104:107], a[192:195], a[208:211], v[104:107]\n" \
"v_mfma_f32_16x16x32_bf16 v[104:107], a[196:199], a[212:215], v[104:107]\n" \
"s_waitcnt lgkmcnt(2)\n" \
"v_mfma_f32_16x16x32_bf16 v[32:35], a[32:35], a[0:3], v[32:35]\n" \
"s_sub_u32 s86, s86, 32768\n" \
"s_cselect_b32 s86, 131072, s86\n" \
"ds_read_b128 a[192:195], v207 offset:12288\n" \
"v_mfma_f32_16x16x32_bf16 v[32:35], a[36:39], a[4:7], v[32:35]\n" \
"buffer_load_dwordx4 v218, s[36:39], s82 offen offset:2048 lds\n" \
"v_mfma_f32_16x16x32_bf16 v[40:43], a[32:35], a[8:11], v[40:43]\n" \
"ds_read_b128 a[196:199], v208 offset:12288\n" \
"v_mfma_f32_16x16x32_bf16 v[40:43], a[36:39], a[12:15], v[40:43]\n" \
"ds_read_b64_tr_b16 a[200:201], v210 offset:25600\n" \
"v_mfma_f32_16x16x32_bf16 v[0:3], a[40:43], a[8:11], v[0:3]\n" \
"ds_read_b64_tr_b16 a[202:203], v210 offset:28672\n" \
"v_mfma_f32_16x16x32_bf16 v[0:3], a[44:47], a[12:15], v[0:3]\n" \
"buffer_load_dwordx4 v219, s[36:39], s82 offen offset:3072 lds\n" \
"v_mfma_f32_16x16x32_bf16 v[4:7], a[48:51], a[8:11], v[4:7]\n" \
"s_xor_b32 s82, s48, s49\n" \
"s_cmp_lt_u32 s48, s50\n" \
"s_cselect_b32 s82, s82, s48\n" \
"ds_read_b64_tr_b16 a[204:205], v210 offset:37888\n" \
"v_mfma_f32_16x16x32_bf16 v[4:7], a[52:55], a[12:15], v[4:7]\n" \
"s_add_u32 s81, s82, s46\n" \
"ds_read_b64_tr_b16 a[206:207], v210 offset:40960\n" \
"v_mfma_f32_16x16x32_bf16 v[12:15], a[48:51], a[16:19], v[12:15]\n" \
"s_add_u32 m0, s86, s79\n" \
"ds_read_b64_tr_b16 a[208:209], v209 offset:26624\n" \
"v_mfma_f32_16x16x32_bf16 v[12:15], a[52:55], a[20:23], v[12:15]\n" \
"buffer_load_dwordx4 v234, s[56:59], s81 offen offset:0 sc1 lds\n" \
"v_mfma_f32_16x16x32_bf16 v[8:11], a[40:43], a[16:19], v[8:11]\n" \
"ds_read_b64_tr_b16 a[210:211], v209 offset:29696\n" \
"v_mfma_f32_16x16x32_bf16 v[8:11], a[44:47], a[20:23], v[8:11]\n" \
"s_lshl_b32 s82, s82, 5\n" \
"ds_read_b64_tr_b16 a[212:213], v209 offset:38912\n" \
"v_mfma_f32_16x16x32_bf16 v[48:51], a[32:35], a[16:19], v[48:51]\n" \
"s_waitcnt lgkmcnt(3)\n" \
"s_add_u32 s82, s82, s47\n" \
"ds_read_b64_tr_b16 a[214:215], v209 offset:41984\n" \
"v_mfma_f32_16x16x32_bf16 v[48:51], a[36:39], a[20:23], v[48:51]\n" \
"buffer_load_dwordx4 v234, s[56:59], s81 offen offset:1024 sc1 lds\n" \
"v_mfma_f32_16x16x32_bf16 v[56:59], a[32:35], a[24:27], v[56:59]\n" \
"ds_read_b64_tr_b16 a[216:217], v210 offset:26624\n" \
"v_mfma_f32_16x16x32_bf16 v[56:59], a[36:39], a[28:31], v[56:59]\n" \
"ds_read_b64_tr_b16 a[218:219], v210 offset:29696\n" \
"v_mfma_f32_16x16x32_bf16 v[16:19], a[40:43], a[24:27], v[16:19]\n" \
"ds_read_b64_tr_b16 a[220:221], v210 offset:38912\n" \
"v_mfma_f32_16x16x32_bf16 v[16:19], a[44:47], a[28:31], v[16:19]\n" \
"buffer_load_dwordx4 v234, s[56:59], s81 offen offset:2048 sc1 lds\n" \
"v_mfma_f32_16x16x32_bf16 v[20:23], a[48:51], a[24:27], v[20:23]\n" \
"ds_read_b64_tr_b16 a[222:223], v210 offset:41984\n" \
"v_mfma_f32_16x16x32_bf16 v[20:23], a[52:55], a[28:31], v[20:23]\n" \
"v_mfma_f32_16x16x32_bf16 v[28:31], a[48:51], a[192:195], v[28:31]\n" \
"v_mfma_f32_16x16x32_bf16 v[28:31], a[52:55], a[196:199], v[28:31]\n" \
"buffer_load_dwordx4 v234, s[56:59], s81 offen offset:3072 sc1 lds\n" \
"v_mfma_f32_16x16x32_bf16 v[24:27], a[40:43], a[192:195], v[24:27]\n" \
"v_mfma_f32_16x16x32_bf16 v[24:27], a[44:47], a[196:199], v[24:27]\n" \
"v_mfma_f32_16x16x32_bf16 v[64:67], a[32:35], a[192:195], v[64:67]\n" \
"s_add_u32 m0, m0, 0x4000\n" \
"v_mfma_f32_16x16x32_bf16 v[64:67], a[36:39], a[196:199], v[64:67]\n" \
"buffer_load_dwordx4 v216, s[36:39], s82 offen offset:0 lds\n" \
"s_waitcnt vmcnt(13) lgkmcnt(0)\n" \
"s_barrier\n" \
"v_mfma_f32_16x16x32_bf16 v[44:47], a[200:203], a[8:11], v[44:47]\n" \
"ds_read_b64_tr_b16 a[96:97], v214 offset:16384\n" \
"v_mfma_f32_16x16x32_bf16 v[44:47], a[204:207], a[12:15], v[44:47]\n" \
"ds_read_b64_tr_b16 a[98:99], v214 offset:18432\n" \
"v_mfma_f32_16x16x32_bf16 v[52:55], a[200:203], a[16:19], v[52:55]\n" \
"s_add_u32 s48, s48, 0x1000\n" \
"ds_read_b64_tr_b16 a[100:101], v214 offset:24576\n" \
"v_mfma_f32_16x16x32_bf16 v[52:55], a[204:207], a[20:23], v[52:55]\n" \
"buffer_load_dwordx4 v217, s[36:39], s82 offen offset:1024 lds\n" \
"v_mfma_f32_16x16x32_bf16 v[60:63], a[200:203], a[24:27], v[60:63]\n" \
"ds_read_b64_tr_b16 a[102:103], v214 offset:26624\n" \
"v_cvt_pk_bf16_f32 v112, v0, v1\n" \
"v_mfma_f32_16x16x32_bf16 v[60:63], a[204:207], a[28:31], v[60:63]\n" \
"ds_read_b128 a[64:67], v213 offset:0\n" \
"v_cvt_pk_bf16_f32 v113, v2, v3\n" \
"v_mfma_f32_16x16x32_bf16 v[68:71], a[200:203], a[192:195], v[68:71]\n" \
"s_sub_u32 s86, s86, 32768\n" \
"s_cselect_b32 s86, 131072, s86\n" \
"ds_read_b128 a[68:71], v213 offset:2048\n" \
"v_mfma_f32_16x16x32_bf16 v[68:71], a[204:207], a[196:199], v[68:71]\n" \
"buffer_load_dwordx4 v218, s[36:39], s82 offen offset:2048 lds\n" \
"v_mfma_f32_16x16x32_bf16 v[72:75], a[0:3], a[208:211], v[72:75]\n" \
"ds_read_b64_tr_b16 a[104:105], v215 offset:16384\n" \
"v_cvt_pk_bf16_f32 v114, v4, v5\n" \
"v_mfma_f32_16x16x32_bf16 v[72:75], a[4:7], a[212:215], v[72:75]\n" \
"ds_read_b64_tr_b16 a[106:107], v215 offset:18432\n" \
"v_cvt_pk_bf16_f32 v115, v6, v7\n" \
"v_mfma_f32_16x16x32_bf16 v[80:83], a[8:11], a[208:211], v[80:83]\n" \
"ds_read_b64_tr_b16 a[108:109], v215 offset:24576\n" \
"v_cvt_pk_bf16_f32 v116, v8, v9\n" \
"v_mfma_f32_16x16x32_bf16 v[80:83], a[12:15], a[212:215], v[80:83]\n" \
"buffer_load_dwordx4 v219, s[36:39], s82 offen offset:3072 lds\n" \
"v_mfma_f32_16x16x32_bf16 v[84:87], a[8:11], a[216:219], v[84:87]\n" \
"s_xor_b32 s82, s48, s49\n" \
"s_cmp_lt_u32 s48, s50\n" \
"s_cselect_b32 s82, s82, s48\n" \
"ds_read_b64_tr_b16 a[110:111], v215 offset:26624\n" \
"v_mfma_f32_16x16x32_bf16 v[84:87], a[12:15], a[220:223], v[84:87]\n" \
"s_add_u32 s81, s82, s46\n" \
"ds_read_b128 a[72:75], v213 offset:256\n" \
"v_cvt_pk_bf16_f32 v117, v10, v11\n" \
"v_mfma_f32_16x16x32_bf16 v[92:95], a[16:19], a[216:219], v[92:95]\n" \
"s_add_u32 m0, s86, s79\n" \
"ds_read_b128 a[76:79], v213 offset:2304\n" \
"v_cvt_pk_bf16_f32 v118, v12, v13\n" \
"v_mfma_f32_16x16x32_bf16 v[92:95], a[20:23], a[220:223], v[92:95]\n" \
"buffer_load_dwordx4 v234, s[56:59], s81 offen offset:0 sc1 lds\n" \
"v_mfma_f32_16x16x32_bf16 v[88:91], a[16:19], a[208:211], v[88:91]\n" \
"ds_read_b128 a[80:83], v213 offset:4096\n" \
"Lpg9604_%=:\n" \
"Lst1x3_%=:\n" \
"s_cbranch_execz Lpg11445_%=\n" \
"v_cvt_pk_bf16_f32 v119, v14, v15\n" \
"v_mfma_f32_16x16x32_bf16 v[88:91], a[20:23], a[212:215], v[88:91]\n" \
"s_lshl_b32 s82, s82, 5\n" \
"ds_read_b128 a[84:87], v213 offset:6144\n" \
"v_cvt_pk_bf16_f32 v120, v16, v17\n" \
"v_mfma_f32_16x16x32_bf16 v[96:99], a[24:27], a[208:211], v[96:99]\n" \
"s_add_u32 s82, s82, s47\n" \
"v_cvt_pk_bf16_f32 v121, v18, v19\n" \
"v_cvt_pk_bf16_f32 v122, v20, v21\n" \
"v_mfma_f32_16x16x32_bf16 v[96:99], a[28:31], a[212:215], v[96:99]\n" \
"buffer_load_dwordx4 v234, s[56:59], s81 offen offset:1024 sc1 lds\n" \
"v_mfma_f32_16x16x32_bf16 v[100:103], a[24:27], a[216:219], v[100:103]\n" \
"v_cvt_pk_bf16_f32 v123, v22, v23\n" \
"v_cvt_pk_bf16_f32 v124, v24, v25\n" \
"v_mfma_f32_16x16x32_bf16 v[100:103], a[28:31], a[220:223], v[100:103]\n" \
"v_cvt_pk_bf16_f32 v125, v26, v27\n" \
"v_cvt_pk_bf16_f32 v126, v28, v29\n" \
"v_mfma_f32_16x16x32_bf16 v[108:111], a[192:195], a[216:219], v[108:111]\n" \
"v_cvt_pk_bf16_f32 v127, v30, v31\n" \
"v_cvt_pk_bf16_f32 v128, v32, v33\n" \
"v_mfma_f32_16x16x32_bf16 v[108:111], a[196:199], a[220:223], v[108:111]\n" \
"buffer_load_dwordx4 v234, s[56:59], s81 offen offset:2048 sc1 lds\n" \
"v_mfma_f32_16x16x32_bf16 v[104:107], a[192:195], a[208:211], v[104:107]\n" \
"v_cvt_pk_bf16_f32 v129, v34, v35\n" \
"v_xor_b32 v145, 0x2000, v223\n" \
"v_mfma_f32_16x16x32_bf16 v[104:107], a[196:199], a[212:215], v[104:107]\n" \
"s_waitcnt lgkmcnt(0)\n" \
"v_cvt_pk_bf16_f32 v192, v72, v73\n" \
"v_cvt_pk_bf16_f32 v193, v74, v75\n" \
"ds_write_b64 v145, v[128:129]\n" \
"s_nop 0\n" \
"ds_write_b64 v145, v[192:193] offset:8\n" \
"v_cvt_pk_bf16_f32 v132, v40, v41\n" \
"v_cvt_pk_bf16_f32 v133, v42, v43\n" \
"v_cvt_pk_bf16_f32 v134, v44, v45\n" \
"v_cvt_pk_bf16_f32 v135, v46, v47\n" \
"v_cvt_pk_bf16_f32 v136, v48, v49\n" \
"v_cvt_pk_bf16_f32 v137, v50, v51\n" \
"v_cvt_pk_bf16_f32 v138, v52, v53\n" \
"v_cvt_pk_bf16_f32 v139, v54, v55\n" \
"v_cvt_pk_bf16_f32 v140, v56, v57\n" \
"v_cvt_pk_bf16_f32 v141, v58, v59\n" \
"v_cvt_pk_bf16_f32 v142, v60, v61\n" \
"v_cvt_pk_bf16_f32 v143, v62, v63\n" \
"v_mfma_f32_16x16x32_bf16 v[0:3], a[96:99], a[64:67], 0\n" \
"ds_read_b128 a[88:91], v213 offset:4352\n" \
"v_mfma_f32_16x16x32_bf16 v[0:3], a[100:103], a[68:71], v[0:3]\n" \
"ds_read_b128 a[92:95], v213 offset:6400\n" \
"v_mfma_f32_16x16x32_bf16 v[4:7], a[104:107], a[64:67], 0\n" \
"ds_read_b64_tr_b16 a[112:113], v214 offset:16896\n" \
"v_mfma_f32_16x16x32_bf16 v[4:7], a[108:111], a[68:71], v[4:7]\n" \
"ds_read_b64_tr_b16 a[114:115], v214 offset:18944\n" \
"v_mfma_f32_16x16x32_bf16 v[20:23], a[104:107], a[72:75], 0\n" \
"ds_read_b64_tr_b16 a[116:117], v214 offset:25088\n" \
"v_cvt_pk_bf16_f32 v64, v64, v65\n" \
"v_mfma_f32_16x16x32_bf16 v[20:23], a[108:111], a[76:79], v[20:23]\n" \
"buffer_load_dwordx4 v234, s[56:59], s81 offen offset:3072 sc1 lds\n" \
"v_cvt_pk_bf16_f32 v65, v66, v67\n" \
"v_cvt_pk_bf16_f32 v66, v68, v69\n" \
"v_mfma_f32_16x16x32_bf16 v[16:19], a[96:99], a[72:75], 0\n" \
"s_sub_u32 s85, s85, 32768\n" \
"s_cselect_b32 s85, 131072, s85\n" \
"ds_read_b64_tr_b16 a[118:119], v214 offset:27136\n" \
"v_mfma_f32_16x16x32_bf16 v[16:19], a[100:103], a[76:79], v[16:19]\n" \
"s_waitcnt lgkmcnt(3)\n" \
"ds_read_b64_tr_b16 a[120:121], v215 offset:16896\n" \
"v_cvt_pk_bf16_f32 v67, v70, v71\n" \
"v_cvt_pk_bf16_f32 v194, v80, v81\n" \
"v_cvt_pk_bf16_f32 v195, v82, v83\n" \
"v_cvt_pk_bf16_f32 v74, v84, v85\n" \
"v_mfma_f32_16x16x32_bf16 v[32:35], a[96:99], a[80:83], 0\n" \
"s_add_u32 m0, m0, 0x4000\n" \
"ds_read_b64_tr_b16 a[122:123], v215 offset:18944\n" \
"v_cvt_pk_bf16_f32 v75, v86, v87\n" \
"v_mfma_f32_16x16x32_bf16 v[32:35], a[100:103], a[84:87], v[32:35]\n" \
"buffer_load_dwordx4 v216, s[36:39], s82 offen offset:0 lds\n" \
"v_cvt_pk_bf16_f32 v196, v88, v89\n" \
"v_cvt_pk_bf16_f32 v197, v90, v91\n" \
"v_mfma_f32_16x16x32_bf16 v[176:179], v[64:67], v[124:127], v[152:155]\n" \
"v_mfma_f32_16x16x32_bf16 v[172:175], v[140:143], v[124:127], v[156:159]\n" \
"v_mfma_f32_16x16x32_bf16 v[168:171], v[140:143], v[120:123], v[152:155]\n" \
"v_mfma_f32_16x16x32_bf16 v[164:167], v[136:139], v[120:123], v[156:159]\n" \
"v_mfma_f32_16x16x32_bf16 v[160:163], v[136:139], v[116:119], v[152:155]\n" \
"v_mfma_f32_16x16x32_bf16 v[156:159], v[132:135], v[116:119], v[156:159]\n" \
"v_mfma_f32_16x16x32_bf16 v[152:155], v[132:135], v[112:115], v[152:155]\n" \
"v_mfma_f32_16x16x32_bf16 v[36:39], a[104:107], a[80:83], 0\n" \
"s_add_u32 s48, s48, 0x1000\n" \
"ds_read_b64_tr_b16 a[124:125], v215 offset:25088\n" \
"v_add_u32 v213, s85, v211\n" \
"v_mfma_f32_16x16x32_bf16 v[36:39], a[108:111], a[84:87], v[36:39]\n" \
"buffer_load_dwordx4 v217, s[36:39], s82 offen offset:1024 lds\n" \
"v_mfma_f32_16x16x32_bf16 v[52:55], a[104:107], a[88:91], 0\n" \
"s_sub_u32 s86, s86, 32768\n" \
"s_cselect_b32 s86, 131072, s86\n" \
"ds_read_b64_tr_b16 a[126:127], v215 offset:27136\n" \
"v_mfma_f32_16x16x32_bf16 v[52:55], a[108:111], a[92:95], v[52:55]\n" \
"buffer_load_dwordx4 v218, s[36:39], s82 offen offset:2048 lds\n" \
"v_mfma_f32_16x16x32_bf16 v[48:51], a[96:99], a[88:91], 0\n" \
"v_add_u32 v214, s85, v212\n" \
"v_xor_b32 v215, 32, v214\n" \
"v_mfma_f32_16x16x32_bf16 v[48:51], a[100:103], a[92:95], v[48:51]\n" \
"s_waitcnt vmcnt(15) lgkmcnt(0)\n" \
"s_barrier\n" \
"v_mfma_f32_16x16x32_bf16 v[8:11], a[112:115], a[64:67], 0\n" \
"ds_read_b64 v[130:131], v223\n" \
"ds_read_b64_tr_b16 a[96:97], v214 offset:16384\n" \
"ds_read_b64_tr_b16 a[98:99], v214 offset:18432\n" \
"v_mfma_f32_16x16x32_bf16 v[8:11], a[116:119], a[68:71], v[8:11]\n" \
"ds_read_b64 v[72:73], v223 offset:8\n" \
"buffer_load_dwordx4 v219, s[36:39], s82 offen offset:3072 lds\n" \
"v_mfma_f32_16x16x32_bf16 v[12:15], a[120:123], a[64:67], 0\n" \
"s_xor_b32 s82, s48, s49\n" \
"s_cmp_lt_u32 s48, s50\n" \
"s_cselect_b32 s82, s82, s48\n" \
"ds_read_b64_tr_b16 a[100:101], v214 offset:24576\n" \
"ds_read_b64_tr_b16 a[102:103], v214 offset:26624\n" \
"v_mfma_f32_16x16x32_bf16 v[12:15], a[124:127], a[68:71], v[12:15]\n" \
"s_add_u32 s81, s82, s46\n" \
"ds_read_b128 a[64:67], v213 offset:0\n" \
"v_mfma_f32_16x16x32_bf16 v[28:31], a[120:123], a[72:75], 0\n" \
"s_add_u32 m0, s86, s79\n" \
"ds_read_b128 a[68:71], v213 offset:2048\n" \
"v_mfma_f32_16x16x32_bf16 v[28:31], a[124:127], a[76:79], v[28:31]\n" \
"buffer_load_dwordx4 v234, s[56:59], s81 offen offset:0 sc1 lds\n" \
"v_mfma_f32_16x16x32_bf16 v[24:27], a[112:115], a[72:75], 0\n" \
"ds_read_b64_tr_b16 a[104:105], v215 offset:16384\n" \
"ds_read_b64_tr_b16 a[106:107], v215 offset:18432\n" \
"v_mfma_f32_16x16x32_bf16 v[24:27], a[116:119], a[76:79], v[24:27]\n" \
"s_waitcnt lgkmcnt(4)\n" \
"s_lshl_b32 s82, s82, 5\n" \
"ds_read_b128 a[72:75], v213 offset:256\n" \
"v_mfma_f32_16x16x32_bf16 v[40:43], a[112:115], a[80:83], 0\n" \
"s_add_u32 s82, s82, s47\n" \
"ds_read_b128 a[76:79], v213 offset:2304\n" \
"v_mfma_f32_16x16x32_bf16 v[148:151], v[128:131], v[112:115], v[148:151]\n" \
"v_max_f32 v172, v172, v176\n" \
"v_max_f32 v173, v173, v177\n" \
"v_max_f32 v174, v174, v178\n" \
"v_max_f32 v175, v175, v179\n" \
"v_max_f32 v164, v164, v168\n" \
"v_max_f32 v165, v165, v169\n" \
"v_max_f32 v166, v166, v170\n" \
"v_max_f32 v167, v167, v171\n" \
"v_max_f32 v156, v156, v160\n" \
"v_max_f32 v157, v157, v161\n" \
"v_max_f32 v158, v158, v162\n" \
"v_max_f32 v159, v159, v163\n" \
"v_max_f32 v148, v148, v152\n" \
"v_max_f32 v149, v149, v153\n" \
"v_max_f32 v150, v150, v154\n" \
"v_max_f32 v151, v151, v155\n" \
"v_max3_f32 v183, v172, v173, v174\n" \
"v_max3_f32 v182, v164, v165, v166\n" \
"v_max3_f32 v181, v156, v157, v158\n" \
"v_mfma_f32_16x16x32_bf16 v[40:43], a[116:119], a[84:87], v[40:43]\n" \
"buffer_load_dwordx4 v234, s[56:59], s81 offen offset:1024 sc1 lds\n" \
"v_max3_f32 v180, v148, v149, v150\n" \
"v_mfma_f32_16x16x32_bf16 v[44:47], a[120:123], a[80:83], 0\n" \
"ds_read_b64_tr_b16 a[108:109], v215 offset:24576\n" \
"ds_read_b64_tr_b16 a[110:111], v215 offset:26624\n" \
"v_mfma_f32_16x16x32_bf16 v[44:47], a[124:127], a[84:87], v[44:47]\n" \
"buffer_load_dwordx4 v234, s[56:59], s81 offen offset:2048 sc1 lds\n" \
"v_max_f32 v183, v183, v175\n" \
"v_max_f32 v182, v182, v167\n" \
"v_max_f32 v181, v181, v159\n" \
"v_mfma_f32_16x16x32_bf16 v[60:63], a[120:123], a[88:91], 0\n" \
"ds_read_b128 a[80:83], v213 offset:4096\n" \
"s_waitcnt lgkmcnt(4)\n" \
"v_mfma_f32_16x16x32_bf16 v[60:63], a[124:127], a[92:95], v[60:63]\n" \
"ds_read_b128 a[84:87], v213 offset:6144\n" \
"v_max_f32 v180, v180, v151\n" \
"v_cvt_pk_bf16_f32 v76, v92, v93\n" \
"v_cvt_pk_bf16_f32 v77, v94, v95\n" \
"v_mfma_f32_16x16x32_bf16 v[56:59], a[112:115], a[88:91], 0\n" \
"buffer_load_dwordx4 v234, s[56:59], s81 offen offset:3072 sc1 lds\n" \
"v_mfma_f32_16x16x32_bf16 v[56:59], a[116:119], a[92:95], v[56:59]\n" \
"s_getpc_b64 s[44:45]\n" \
"Lop_%=:\n" \
"s_add_u32 s44, s44, Lor_%=-Lop_%=\n" \
"s_addc_u32 s45, s45, 0\n" \
"v_permlane32_swap_b32 v180, v181\n" \
"v_permlane32_swap_b32 v182, v183\n" \
"v_max_f32 v180, v180, v181\n" \
"v_mfma_f32_16x16x32_bf16 v[0:3], a[96:99], a[64:67], v[0:3]\n" \
"ds_read_b128 a[88:91], v213 offset:4352\n" \
"v_max_f32 v182, v182, v183\n" \
"v_mfma_f32_16x16x32_bf16 v[0:3], a[100:103], a[68:71], v[0:3]\n" \
"s_waitcnt lgkmcnt(2)\n" \
"ds_read_b128 a[92:95], v213 offset:6400\n" \
"v_cvt_pk_bf16_f32 v198, v96, v97\n" \
"v_cvt_pk_bf16_f32 v199, v98, v99\n" \
"v_permlane16_swap_b32 v180, v182\n" \
"v_mfma_f32_16x16x32_bf16 v[4:7], a[104:107], a[64:67], v[4:7]\n" \
"ds_read_b64_tr_b16 a[112:113], v214 offset:16896\n" \
"v_max_f32 v180, v180, v182\n" \
"v_mfma_f32_16x16x32_bf16 v[4:7], a[108:111], a[68:71], v[4:7]\n" \
"ds_read_b64_tr_b16 a[114:115], v214 offset:18944\n" \
"v_mul_f32 v180, v180, v144\n" \
"v_cvt_pk_bf16_f32 v78, v100, v101\n" \
"v_cvt_pk_bf16_f32 v79, v102, v103\n" \
"v_mov_b32 v182, v180\n" \
"v_mfma_f32_16x16x32_bf16 v[20:23], a[104:107], a[72:75], v[20:23]\n" \
"s_add_u32 m0, m0, 0x4000\n" \
"ds_read_b64_tr_b16 a[116:117], v214 offset:25088\n" \
"v_cvt_pk_bf16_f32 v200, v104, v105\n" \
"v_mfma_f32_16x16x32_bf16 v[20:23], a[108:111], a[76:79], v[20:23]\n" \
"buffer_load_dwordx4 v216, s[36:39], s82 offen offset:0 lds\n" \
"v_cvt_pk_bf16_f32 v201, v106, v107\n" \
"v_permlane16_swap_b32 v180, v182\n" \
"v_mfma_f32_16x16x32_bf16 v[16:19], a[96:99], a[72:75], v[16:19]\n" \
"s_sub_u32 s85, s85, 32768\n" \
"s_cselect_b32 s85, 131072, s85\n" \
"ds_read_b64_tr_b16 a[118:119], v214 offset:27136\n" \
"v_mfma_f32_16x16x32_bf16 v[16:19], a[100:103], a[76:79], v[16:19]\n" \
"s_waitcnt lgkmcnt(3)\n" \
"ds_read_b64_tr_b16 a[120:121], v215 offset:16896\n" \
"v_mov_b32 v181, v180\n" \
"v_mov_b32 v183, v182\n" \
"v_cvt_pk_bf16_f32 v80, v108, v109\n" \
"v_cvt_pk_bf16_f32 v81, v110, v111\n" \
"v_mfma_f32_16x16x32_bf16 v[32:35], a[96:99], a[80:83], v[32:35]\n" \
"s_add_u32 s48, s48, 0x1000\n" \
"ds_read_b64_tr_b16 a[122:123], v215 offset:18944\n" \
"v_permlane32_swap_b32 v180, v181\n" \
"v_mfma_f32_16x16x32_bf16 v[32:35], a[100:103], a[84:87], v[32:35]\n" \
"buffer_load_dwordx4 v217, s[36:39], s82 offen offset:1024 lds\n" \
"v_permlane32_swap_b32 v182, v183\n" \
"v_fma_f32 v172, v172, v144, -v183\n" \
"v_mfma_f32_16x16x32_bf16 v[36:39], a[104:107], a[80:83], v[36:39]\n" \
"s_sub_u32 s86, s86, 32768\n" \
"s_cselect_b32 s86, 131072, s86\n" \
"ds_read_b64_tr_b16 a[124:125], v215 offset:25088\n" \
"v_add_u32 v213, s85, v211\n" \
"v_fma_f32 v164, v164, v144, -v182\n" \
"v_mfma_f32_16x16x32_bf16 v[36:39], a[108:111], a[84:87], v[36:39]\n" \
"buffer_load_dwordx4 v218, s[36:39], s82 offen offset:2048 lds\n" \
"v_fma_f32 v156, v156, v144, -v181\n" \
"v_fma_f32 v148, v148, v144, -v180\n" \
"v_mfma_f32_16x16x32_bf16 v[52:55], a[104:107], a[88:91], v[52:55]\n" \
"ds_read_b64_tr_b16 a[126:127], v215 offset:27136\n" \
"v_fma_f32 v173, v173, v144, -v183\n" \
"v_mfma_f32_16x16x32_bf16 v[52:55], a[108:111], a[92:95], v[52:55]\n" \
"buffer_load_dwordx4 v219, s[36:39], s82 offen offset:3072 lds\n" \
"v_fma_f32 v165, v165, v144, -v182\n" \
"v_fma_f32 v157, v157, v144, -v181\n" \
"v_mfma_f32_16x16x32_bf16 v[48:51], a[96:99], a[88:91], v[48:51]\n" \
"s_xor_b32 s82, s48, s49\n" \
"s_cmp_lt_u32 s48, s50\n" \
"s_cselect_b32 s82, s82, s48\n" \
"v_add_u32 v214, s85, v212\n" \
"v_xor_b32 v215, 32, v214\n" \
"v_fma_f32 v149, v149, v144, -v180\n" \
"v_mfma_f32_16x16x32_bf16 v[48:51], a[100:103], a[92:95], v[48:51]\n" \
"s_add_u32 s81, s82, s46\n" \
"v_fma_f32 v174, v174, v144, -v183\n" \
"v_fma_f32 v166, v166, v144, -v182\n" \
"s_waitcnt vmcnt(16) lgkmcnt(0)\n" \
"s_barrier\n" \
"v_mfma_f32_16x16x32_bf16 v[8:11], a[112:115], a[64:67], v[8:11]\n" \
"s_add_u32 m0, s86, s79\n" \
"ds_read_b64_tr_b16 a[96:97], v214 offset:16384\n" \
"ds_read_b64_tr_b16 a[98:99], v214 offset:18432\n" \
"v_mfma_f32_16x16x32_bf16 v[8:11], a[116:119], a[68:71], v[8:11]\n" \
"buffer_load_dwordx4 v234, s[56:59], s81 offen offset:0 sc1 lds\n" \
"v_fma_f32 v158, v158, v144, -v181\n" \
"v_fma_f32 v150, v150, v144, -v180\n" \
"v_mfma_f32_16x16x32_bf16 v[12:15], a[120:123], a[64:67], v[12:15]\n" \
"ds_read_b64_tr_b16 a[100:101], v214 offset:24576\n" \
"ds_read_b64_tr_b16 a[102:103], v214 offset:26624\n" \
"v_mfma_f32_16x16x32_bf16 v[12:15], a[124:127], a[68:71], v[12:15]\n" \
"s_lshl_b32 s82, s82, 5\n" \
"ds_read_b128 a[64:67], v213 offset:0\n" \
"v_fma_f32 v175, v175, v144, -v183\n" \
"v_fma_f32 v167, v167, v144, -v182\n" \
"v_fma_f32 v159, v159, v144, -v181\n" \
"v_fma_f32 v151, v151, v144, -v180\n" \
"v_mfma_f32_16x16x32_bf16 v[28:31], a[120:123], a[72:75], v[28:31]\n" \
"s_add_u32 s82, s82, s47\n" \
"ds_read_b128 a[68:71], v213 offset:2048\n" \
"v_mfma_f32_16x16x32_bf16 v[28:31], a[124:127], a[76:79], v[28:31]\n" \
"buffer_load_dwordx4 v234, s[56:59], s81 offen offset:1024 sc1 lds\n" \
"v_exp_f32 v172, v172\n" \
"v_mfma_f32_16x16x32_bf16 v[24:27], a[112:115], a[72:75], v[24:27]\n" \
"ds_read_b64_tr_b16 a[104:105], v215 offset:16384\n" \
"ds_read_b64_tr_b16 a[106:107], v215 offset:18432\n" \
"v_mfma_f32_16x16x32_bf16 v[24:27], a[116:119], a[76:79], v[24:27]\n" \
"ds_read_b128 a[72:75], v213 offset:256\n" \
"v_exp_f32 v164, v164\n" \
"v_exp_f32 v156, v156\n" \
"v_mfma_f32_16x16x32_bf16 v[40:43], a[112:115], a[80:83], v[40:43]\n" \
"ds_read_b128 a[76:79], v213 offset:2304\n" \
"v_mfma_f32_16x16x32_bf16 v[40:43], a[116:119], a[84:87], v[40:43]\n" \
"buffer_load_dwordx4 v234, s[56:59], s81 offen offset:2048 sc1 lds\n" \
"v_exp_f32 v148, v148\n" \
"v_mfma_f32_16x16x32_bf16 v[44:47], a[120:123], a[80:83], v[44:47]\n" \
"ds_read_b64_tr_b16 a[108:109], v215 offset:24576\n" \
"ds_read_b64_tr_b16 a[110:111], v215 offset:26624\n" \
"v_mfma_f32_16x16x32_bf16 v[44:47], a[124:127], a[84:87], v[44:47]\n" \
"buffer_load_dwordx4 v234, s[56:59], s81 offen offset:3072 sc1 lds\n" \
"v_exp_f32 v173, v173\n" \
"v_mfma_f32_16x16x32_bf16 v[60:63], a[120:123], a[88:91], v[60:63]\n" \
"ds_read_b128 a[80:83], v213 offset:4096\n" \
"s_waitcnt lgkmcnt(4)\n" \
"v_mfma_f32_16x16x32_bf16 v[60:63], a[124:127], a[92:95], v[60:63]\n" \
"s_add_u32 m0, m0, 0x4000\n" \
"ds_read_b128 a[84:87], v213 offset:6144\n" \
"v_exp_f32 v165, v165\n" \
"v_exp_f32 v157, v157\n" \
"v_mfma_f32_16x16x32_bf16 v[56:59], a[112:115], a[88:91], v[56:59]\n" \
"buffer_load_dwordx4 v216, s[36:39], s82 offen offset:0 lds\n" \
"v_mfma_f32_16x16x32_bf16 v[56:59], a[116:119], a[92:95], v[56:59]\n" \
"v_exp_f32 v149, v149\n" \
"v_exp_f32 v174, v174\n" \
"v_mfma_f32_16x16x32_bf16 v[0:3], a[96:99], a[64:67], v[0:3]\n" \
"ds_read_b128 a[88:91], v213 offset:4352\n" \
"v_mfma_f32_16x16x32_bf16 v[0:3], a[100:103], a[68:71], v[0:3]\n" \
"s_waitcnt lgkmcnt(2)\n" \
"ds_read_b128 a[92:95], v213 offset:6400\n" \
"v_exp_f32 v166, v166\n" \
"v_exp_f32 v158, v158\n" \
"v_mfma_f32_16x16x32_bf16 v[4:7], a[104:107], a[64:67], v[4:7]\n" \
"ds_read_b64_tr_b16 a[112:113], v214 offset:16896\n" \
"v_mfma_f32_16x16x32_bf16 v[4:7], a[108:111], a[68:71], v[4:7]\n" \
"ds_read_b64_tr_b16 a[114:115], v214 offset:18944\n" \
"v_exp_f32 v150, v150\n" \
"v_exp_f32 v175, v175\n" \
"v_mfma_f32_16x16x32_bf16 v[20:23], a[104:107], a[72:75], v[20:23]\n" \
"s_add_u32 s48, s48, 0x1000\n" \
"ds_read_b64_tr_b16 a[116:117], v214 offset:25088\n" \
"v_mfma_f32_16x16x32_bf16 v[20:23], a[108:111], a[76:79], v[20:23]\n" \
"buffer_load_dwordx4 v217, s[36:39], s82 offen offset:1024 lds\n" \
"v_exp_f32 v167, v167\n" \
"v_mfma_f32_16x16x32_bf16 v[16:19], a[96:99], a[72:75], v[16:19]\n" \
"s_sub_u32 s85, s85, 32768\n" \
"s_cselect_b32 s85, 131072, s85\n" \
"ds_read_b64_tr_b16 a[118:119], v214 offset:27136\n" \
"v_mfma_f32_16x16x32_bf16 v[16:19], a[100:103], a[76:79], v[16:19]\n" \
"s_waitcnt lgkmcnt(3)\n" \
"ds_read_b64_tr_b16 a[120:121], v215 offset:16896\n" \
"v_exp_f32 v159, v159\n" \
"v_exp_f32 v151, v151\n" \
"v_mfma_f32_16x16x32_bf16 v[32:35], a[96:99], a[80:83], v[32:35]\n" \
"s_sub_u32 s86, s86, 32768\n" \
"s_cselect_b32 s86, 131072, s86\n" \
"ds_read_b64_tr_b16 a[122:123], v215 offset:18944\n" \
"v_mfma_f32_16x16x32_bf16 v[32:35], a[100:103], a[84:87], v[32:35]\n" \
"buffer_load_dwordx4 v218, s[36:39], s82 offen offset:2048 lds\n" \
"v_add_f32 v191, v172, v173\n" \
"v_add_f32 v190, v164, v165\n" \
"v_mfma_f32_16x16x32_bf16 v[36:39], a[104:107], a[80:83], v[36:39]\n" \
"ds_read_b64_tr_b16 a[124:125], v215 offset:25088\n" \
"v_add_u32 v213, s85, v211\n" \
"v_mfma_f32_16x16x32_bf16 v[36:39], a[108:111], a[84:87], v[36:39]\n" \
"buffer_load_dwordx4 v219, s[36:39], s82 offen offset:3072 lds\n" \
"v_add_f32 v189, v156, v157\n" \
"v_add_f32 v188, v148, v149\n" \
"v_add_f32 v191, v191, v174\n" \
"v_mfma_f32_16x16x32_bf16 v[52:55], a[104:107], a[88:91], v[52:55]\n" \
"s_xor_b32 s82, s48, s49\n" \
"s_cmp_lt_u32 s48, s50\n" \
"s_cselect_b32 s82, s82, s48\n" \
"s_add_u32 s81, s82, s46\n" \
"s_add_u32 m0, s86, s79\n" \
"ds_read_b64_tr_b16 a[126:127], v215 offset:27136\n" \
"v_add_f32 v190, v190, v166\n" \
"v_mfma_f32_16x16x32_bf16 v[52:55], a[108:111], a[92:95], v[52:55]\n" \
"buffer_load_dwordx4 v234, s[56:59], s81 offen offset:0 sc1 lds\n" \
"v_add_f32 v189, v189, v158\n" \
"v_mfma_f32_16x16x32_bf16 v[48:51], a[96:99], a[88:91], v[48:51]\n" \
"v_add_u32 v214, s85, v212\n" \
"v_xor_b32 v215, 32, v214\n" \
"v_mfma_f32_16x16x32_bf16 v[48:51], a[100:103], a[92:95], v[48:51]\n" \
"s_lshl_b32 s82, s82, 5\n" \
"v_add_f32 v188, v188, v150\n" \
"v_add_f32 v191, v191, v175\n" \
"v_add_f32 v190, v190, v167\n" \
"s_waitcnt vmcnt(17) lgkmcnt(0)\n" \
"s_barrier\n" \
"v_mfma_f32_16x16x32_bf16 v[8:11], a[112:115], a[64:67], v[8:11]\n" \
"s_add_u32 s82, s82, s47\n" \
"ds_read_b64_tr_b16 a[96:97], v214 offset:16384\n" \
"ds_read_b64_tr_b16 a[98:99], v214 offset:18432\n" \
"v_mfma_f32_16x16x32_bf16 v[8:11], a[116:119], a[68:71], v[8:11]\n" \
"buffer_load_dwordx4 v234, s[56:59], s81 offen offset:1024 sc1 lds\n" \
"v_add_f32 v189, v189, v159\n" \
"v_add_f32 v188, v188, v151\n" \
"v_mfma_f32_16x16x32_bf16 v[12:15], a[120:123], a[64:67], v[12:15]\n" \
"ds_read_b64_tr_b16 a[100:101], v214 offset:24576\n" \
"ds_read_b64_tr_b16 a[102:103], v214 offset:26624\n" \
"v_mfma_f32_16x16x32_bf16 v[12:15], a[124:127], a[68:71], v[12:15]\n" \
"ds_read_b128 a[64:67], v213 offset:0\n" \
"v_permlane32_swap_b32 v188, v189\n" \
"v_permlane32_swap_b32 v190, v191\n" \
"v_add_f32 v188, v188, v189\n" \
"v_add_f32 v190, v190, v191\n" \
"v_mfma_f32_16x16x32_bf16 v[28:31], a[120:123], a[72:75], v[28:31]\n" \
"ds_read_b128 a[68:71], v213 offset:2048\n" \
"v_mfma_f32_16x16x32_bf16 v[28:31], a[124:127], a[76:79], v[28:31]\n" \
"buffer_load_dwordx4 v234, s[56:59], s81 offen offset:2048 sc1 lds\n" \
"v_permlane16_swap_b32 v188, v190\n" \
"v_add_f32 v188, v188, v190\n" \
"v_mfma_f32_16x16x32_bf16 v[24:27], a[112:115], a[72:75], v[24:27]\n" \
"ds_read_b64_tr_b16 a[104:105], v215 offset:16384\n" \
"ds_read_b64_tr_b16 a[106:107], v215 offset:18432\n" \
"v_mfma_f32_16x16x32_bf16 v[24:27], a[116:119], a[76:79], v[24:27]\n" \
"ds_read_b128 a[72:75], v213 offset:256\n" \
"v_rcp_f32 v188, v188\n" \
"v_mfma_f32_16x16x32_bf16 v[40:43], a[112:115], a[80:83], v[40:43]\n" \
"ds_read_b128 a[76:79], v213 offset:2304\n" \
"v_mfma_f32_16x16x32_bf16 v[40:43], a[116:119], a[84:87], v[40:43]\n" \
"buffer_load_dwordx4 v234, s[56:59], s81 offen offset:3072 sc1 lds\n" \
"v_mov_b32 v190, v188\n" \
"v_mfma_f32_16x16x32_bf16 v[44:47], a[120:123], a[80:83], v[44:47]\n" \
"s_add_u32 m0, m0, 0x4000\n" \
"ds_read_b64_tr_b16 a[108:109], v215 offset:24576\n" \
"ds_read_b64_tr_b16 a[110:111], v215 offset:26624\n" \
"v_mfma_f32_16x16x32_bf16 v[44:47], a[124:127], a[84:87], v[44:47]\n" \
"buffer_load_dwordx4 v216, s[36:39], s82 offen offset:0 lds\n" \
"v_permlane16_swap_b32 v188, v190\n" \
"v_mov_b32 v189, v188\n" \
"v_mov_b32 v191, v190\n" \
"v_mfma_f32_16x16x32_bf16 v[60:63], a[120:123], a[88:91], v[60:63]\n" \
"ds_read_b128 a[80:83], v213 offset:4096\n" \
"s_waitcnt lgkmcnt(4)\n" \
"v_mfma_f32_16x16x32_bf16 v[60:63], a[124:127], a[92:95], v[60:63]\n" \
"s_add_u32 s48, s48, 0x1000\n" \
"ds_read_b128 a[84:87], v213 offset:6144\n" \
"v_permlane32_swap_b32 v188, v189\n" \
"v_permlane32_swap_b32 v190, v191\n" \
"v_mul_f32 v172, v172, v191\n" \
"v_mul_f32 v164, v164, v190\n" \
"v_mul_f32 v156, v156, v189\n" \
"v_mul_f32 v148, v148, v188\n" \
"v_mfma_f32_16x16x32_bf16 v[56:59], a[112:115], a[88:91], v[56:59]\n" \
"buffer_load_dwordx4 v217, s[36:39], s82 offen offset:1024 lds\n" \
"v_mfma_f32_16x16x32_bf16 v[56:59], a[116:119], a[92:95], v[56:59]\n" \
"v_mul_f32 v173, v173, v191\n" \
"v_mul_f32 v165, v165, v190\n" \
"v_mul_f32 v157, v157, v189\n" \
"v_mul_f32 v149, v149, v188\n" \
"v_mul_f32 v174, v174, v191\n" \
"v_mfma_f32_16x16x32_bf16 v[0:3], a[96:99], a[64:67], v[0:3]\n" \
"ds_read_b128 a[88:91], v213 offset:4352\n" \
"v_mul_f32 v166, v166, v190\n" \
"v_mfma_f32_16x16x32_bf16 v[0:3], a[100:103], a[68:71], v[0:3]\n" \
"s_waitcnt lgkmcnt(2)\n" \
"ds_read_b128 a[92:95], v213 offset:6400\n" \
"v_mul_f32 v158, v158, v189\n" \
"v_mul_f32 v150, v150, v188\n" \
"v_mul_f32 v175, v175, v191\n" \
"v_mfma_f32_16x16x32_bf16 v[4:7], a[104:107], a[64:67], v[4:7]\n" \
"ds_read_b64_tr_b16 a[112:113], v214 offset:16896\n" \
"v_mul_f32 v167, v167, v190\n" \
"v_mfma_f32_16x16x32_bf16 v[4:7], a[108:111], a[68:71], v[4:7]\n" \
"ds_read_b64_tr_b16 a[114:115], v214 offset:18944\n" \
"v_mul_f32 v159, v159, v189\n" \
"v_mul_f32 v151, v151, v188\n" \
"v_cvt_pk_bf16_f32 v178, v172, v173\n" \
"v_cvt_pk_bf16_f32 v179, v174, v175\n" \
"v_mfma_f32_16x16x32_bf16 v[20:23], a[104:107], a[72:75], v[20:23]\n" \
"s_sub_u32 s86, s86, 32768\n" \
"s_cselect_b32 s86, 131072, s86\n" \
"ds_read_b64_tr_b16 a[116:117], v214 offset:25088\n" \
"v_mfma_f32_16x16x32_bf16 v[20:23], a[108:111], a[76:79], v[20:23]\n" \
"buffer_load_dwordx4 v218, s[36:39], s82 offen offset:2048 lds\n" \
"v_cvt_pk_bf16_f32 v170, v164, v165\n" \
"v_cvt_pk_bf16_f32 v171, v166, v167\n" \
"v_mfma_f32_16x16x32_bf16 v[16:19], a[96:99], a[72:75], v[16:19]\n" \
"s_sub_u32 s85, s85, 32768\n" \
"s_cselect_b32 s85, 131072, s85\n" \
"ds_read_b64_tr_b16 a[118:119], v214 offset:27136\n" \
"v_mfma_f32_16x16x32_bf16 v[16:19], a[100:103], a[76:79], v[16:19]\n" \
"s_waitcnt lgkmcnt(3)\n" \
"ds_read_b64_tr_b16 a[120:121], v215 offset:16896\n" \
"v_cvt_pk_bf16_f32 v162, v156, v157\n" \
"v_cvt_pk_bf16_f32 v163, v158, v159\n" \
"v_cvt_pk_bf16_f32 v154, v148, v149\n" \
"v_cvt_pk_bf16_f32 v155, v150, v151\n" \
"v_mfma_f32_16x16x32_bf16 v[32:35], a[96:99], a[80:83], v[32:35]\n" \
"ds_read_b64_tr_b16 a[122:123], v215 offset:18944\n" \
"v_and_b32 v176, v178, v184\n" \
"v_mfma_f32_16x16x32_bf16 v[32:35], a[100:103], a[84:87], v[32:35]\n" \
"buffer_load_dwordx4 v219, s[36:39], s82 offen offset:3072 lds\n" \
"v_and_b32 v177, v179, v185\n" \
"v_xor_b32 v178, v178, v176\n" \
"v_mfma_f32_16x16x32_bf16 v[36:39], a[104:107], a[80:83], v[36:39]\n" \
"s_xor_b32 s82, s48, s49\n" \
"s_cmp_lt_u32 s48, s50\n" \
"s_cselect_b32 s82, s82, s48\n" \
"s_add_u32 s81, s82, s46\n" \
"s_add_u32 m0, s86, s79\n" \
"ds_read_b64_tr_b16 a[124:125], v215 offset:25088\n" \
"v_add_u32 v213, s85, v211\n" \
"v_xor_b32 v179, v179, v177\n" \
"v_mfma_f32_16x16x32_bf16 v[36:39], a[108:111], a[84:87], v[36:39]\n" \
"buffer_load_dwordx4 v234, s[56:59], s81 offen offset:0 sc1 lds\n" \
"v_and_b32 v168, v170, v184\n" \
"v_and_b32 v169, v171, v185\n" \
"v_mfma_f32_16x16x32_bf16 v[52:55], a[104:107], a[88:91], v[52:55]\n" \
"s_lshl_b32 s82, s82, 5\n" \
"s_add_u32 s82, s82, s47\n" \
"ds_read_b64_tr_b16 a[126:127], v215 offset:27136\n" \
"v_mfma_f32_16x16x32_bf16 v[52:55], a[108:111], a[92:95], v[52:55]\n" \
"buffer_load_dwordx4 v234, s[56:59], s81 offen offset:1024 sc1 lds\n" \
"v_xor_b32 v170, v170, v168\n" \
"v_xor_b32 v171, v171, v169\n" \
"v_mfma_f32_16x16x32_bf16 v[48:51], a[96:99], a[88:91], v[48:51]\n" \
"v_add_u32 v214, s85, v212\n" \
"v_xor_b32 v215, 32, v214\n" \
"v_mfma_f32_16x16x32_bf16 v[48:51], a[100:103], a[92:95], v[48:51]\n" \
"v_and_b32 v160, v162, v184\n" \
"v_and_b32 v161, v163, v185\n" \
"v_xor_b32 v162, v162, v160\n" \
"s_waitcnt vmcnt(18) lgkmcnt(0)\n" \
"s_barrier\n" \
"v_mfma_f32_16x16x32_bf16 v[8:11], a[112:115], a[64:67], v[8:11]\n" \
"ds_read_b64_tr_b16 a[96:97], v214 offset:16384\n" \
"ds_read_b64_tr_b16 a[98:99], v214 offset:18432\n" \
"v_mfma_f32_16x16x32_bf16 v[8:11], a[116:119], a[68:71], v[8:11]\n" \
"buffer_load_dwordx4 v234, s[56:59], s81 offen offset:2048 sc1 lds\n" \
"v_xor_b32 v163, v163, v161\n" \
"v_and_b32 v152, v154, v184\n" \
"v_mfma_f32_16x16x32_bf16 v[12:15], a[120:123], a[64:67], v[12:15]\n" \
"ds_read_b64_tr_b16 a[100:101], v214 offset:24576\n" \
"ds_read_b64_tr_b16 a[102:103], v214 offset:26624\n" \
"v_mfma_f32_16x16x32_bf16 v[12:15], a[124:127], a[68:71], v[12:15]\n" \
"ds_read_b128 a[64:67], v213 offset:0\n" \
"v_and_b32 v153, v155, v185\n" \
"v_xor_b32 v154, v154, v152\n" \
"v_xor_b32 v155, v155, v153\n" \
"v_mfma_f32_16x16x32_bf16 v[28:31], a[120:123], a[72:75], v[28:31]\n" \
"ds_read_b128 a[68:71], v213 offset:2048\n" \
"v_mfma_f32_16x16x32_bf16 v[136:139], v[198:201], v[176:179], 0\n" \
"v_mfma_f32_16x16x32_bf16 v[140:143], v[78:81], v[176:179], 0\n" \
"v_mfma_f32_16x16x32_bf16 v[128:131], v[196:199], v[168:171], 0\n" \
"v_mfma_f32_16x16x32_bf16 v[132:135], v[76:79], v[168:171], 0\n" \
"v_mfma_f32_16x16x32_bf16 v[120:123], v[194:197], v[160:163], 0\n" \
"v_mfma_f32_16x16x32_bf16 v[124:127], v[74:77], v[160:163], 0\n" \
"v_mfma_f32_16x16x32_bf16 v[112:115], v[192:195], v[152:155], 0\n" \
"v_mfma_f32_16x16x32_bf16 v[116:119], v[72:75], v[152:155], 0\n" \
"v_mfma_f32_16x16x32_bf16 v[28:31], a[124:127], a[76:79], v[28:31]\n" \
"buffer_load_dwordx4 v234, s[56:59], s81 offen offset:3072 sc1 lds\n" \
"v_mfma_f32_16x16x32_bf16 v[24:27], a[112:115], a[72:75], v[24:27]\n" \
"ds_read_b64_tr_b16 a[104:105], v215 offset:16384\n" \
"ds_read_b64_tr_b16 a[106:107], v215 offset:18432\n" \
"v_mfma_f32_16x16x32_bf16 v[24:27], a[116:119], a[76:79], v[24:27]\n" \
"ds_read_b128 a[72:75], v213 offset:256\n" \
"v_cvt_pk_bf16_f32 v136, v136, v137\n" \
"v_cvt_pk_bf16_f32 v137, v138, v139\n" \
"v_cvt_pk_bf16_f32 v138, v140, v141\n" \
"v_cvt_pk_bf16_f32 v139, v142, v143\n" \
"v_cndmask_b32 v140, v136, v138, s[76:77]\n" \
"v_mfma_f32_16x16x32_bf16 v[40:43], a[112:115], a[80:83], v[40:43]\n" \
"s_add_u32 m0, m0, 0x4000\n" \
"ds_read_b128 a[76:79], v213 offset:2304\n" \
"v_cndmask_b32 v141, v137, v139, s[76:77]\n" \
"v_mfma_f32_16x16x32_bf16 v[40:43], a[116:119], a[84:87], v[40:43]\n" \
"buffer_load_dwordx4 v216, s[36:39], s82 offen offset:0 lds\n" \
"v_cndmask_b32 v142, v138, v136, s[76:77]\n" \
"v_cndmask_b32 v143, v139, v137, s[76:77]\n" \
"v_mfma_f32_16x16x32_bf16 v[44:47], a[120:123], a[80:83], v[44:47]\n" \
"s_add_u32 s48, s48, 0x1000\n" \
"ds_read_b64_tr_b16 a[108:109], v215 offset:24576\n" \
"ds_read_b64_tr_b16 a[110:111], v215 offset:26624\n" \
"v_mfma_f32_16x16x32_bf16 v[44:47], a[124:127], a[84:87], v[44:47]\n" \
"buffer_load_dwordx4 v217, s[36:39], s82 offen offset:1024 lds\n" \
"v_cvt_pk_bf16_f32 v128, v128, v129\n" \
"v_cvt_pk_bf16_f32 v129, v130, v131\n" \
"v_cvt_pk_bf16_f32 v130, v132, v133\n" \
"v_mfma_f32_16x16x32_bf16 v[60:63], a[120:123], a[88:91], v[60:63]\n" \
"ds_read_b128 a[80:83], v213 offset:4096\n" \
"s_waitcnt lgkmcnt(4)\n" \
"v_mfma_f32_16x16x32_bf16 v[60:63], a[124:127], a[92:95], v[60:63]\n" \
"s_sub_u32 s86, s86, 32768\n" \
"s_cselect_b32 s86, 131072, s86\n" \
"ds_read_b128 a[84:87], v213 offset:6144\n" \
"v_cvt_pk_bf16_f32 v131, v134, v135\n" \
"v_cndmask_b32 v132, v128, v130, s[76:77]\n" \
"v_mfma_f32_16x16x32_bf16 v[56:59], a[112:115], a[88:91], v[56:59]\n" \
"buffer_load_dwordx4 v218, s[36:39], s82 offen offset:2048 lds\n" \
"v_mfma_f32_16x16x32_bf16 v[56:59], a[116:119], a[92:95], v[56:59]\n" \
"v_cndmask_b32 v133, v129, v131, s[76:77]\n" \
"v_cndmask_b32 v134, v130, v128, s[76:77]\n" \
"v_cndmask_b32 v135, v131, v129, s[76:77]\n" \
"v_cvt_pk_bf16_f32 v120, v120, v121\n" \
"v_cvt_pk_bf16_f32 v121, v122, v123\n" \
"v_mfma_f32_16x16x32_bf16 v[0:3], a[96:99], a[64:67], v[0:3]\n" \
"ds_read_b128 a[88:91], v213 offset:4352\n" \
"v_cvt_pk_bf16_f32 v122, v124, v125\n" \
"v_mfma_f32_16x16x32_bf16 v[0:3], a[100:103], a[68:71], v[0:3]\n" \
"s_waitcnt lgkmcnt(2)\n" \
"ds_read_b128 a[92:95], v213 offset:6400\n" \
"v_cvt_pk_bf16_f32 v123, v126, v127\n" \
"v_cndmask_b32 v124, v120, v122, s[76:77]\n" \
"v_cndmask_b32 v125, v121, v123, s[76:77]\n" \
"v_mfma_f32_16x16x32_bf16 v[4:7], a[104:107], a[64:67], v[4:7]\n" \
"ds_read_b64_tr_b16 a[112:113], v214 offset:16896\n" \
"v_cndmask_b32 v126, v122, v120, s[76:77]\n" \
"v_mfma_f32_16x16x32_bf16 v[4:7], a[108:111], a[68:71], v[4:7]\n" \
"Lpg10204_%=:\n" \
"s_cbranch_execz Lcb1_%=\n" \
"ds_read_b64_tr_b16 a[114:115], v214 offset:18944\n" \
"v_cndmask_b32 v127, v123, v121, s[76:77]\n" \
"v_cvt_pk_bf16_f32 v112, v112, v113\n" \
"v_cvt_pk_bf16_f32 v113, v114, v115\n" \
"v_cvt_pk_bf16_f32 v114, v116, v117\n" \
"v_mfma_f32_16x16x32_bf16 v[20:23], a[104:107], a[72:75], v[20:23]\n" \
"s_waitcnt lgkmcnt(4)\n" \
"ds_read_b64_tr_b16 a[116:117], v214 offset:25088\n" \
"v_cvt_pk_bf16_f32 v115, v118, v119\n" \
"v_mfma_f32_16x16x32_bf16 v[20:23], a[108:111], a[76:79], v[20:23]\n" \
"buffer_load_dwordx4 v219, s[36:39], s82 offen offset:3072 lds\n" \
"v_cndmask_b32 v116, v112, v114, s[76:77]\n" \
"v_cndmask_b32 v117, v113, v115, s[76:77]\n" \
"v_mfma_f32_16x16x32_bf16 v[16:19], a[96:99], a[72:75], v[16:19]\n" \
"s_sub_u32 s85, s85, 32768\n" \
"s_cselect_b32 s85, 131072, s85\n" \
"s_xor_b32 s82, s48, s49\n" \
"s_cmp_lt_u32 s48, s50\n" \
"s_cselect_b32 s82, s82, s48\n" \
"ds_read_b64_tr_b16 a[118:119], v214 offset:27136\n" \
"v_cndmask_b32 v118, v114, v112, s[76:77]\n" \
"v_mfma_f32_16x16x32_bf16 v[16:19], a[100:103], a[76:79], v[16:19]\n" \
"s_add_u32 s81, s82, s46\n" \
"ds_read_b64_tr_b16 a[120:121], v215 offset:16896\n" \
"v_cndmask_b32 v119, v115, v113, s[76:77]\n" \
"s_add_u32 s71, s93, 208896\n" \
"v_mfma_f32_16x16x32_bf16 v[32:35], a[96:99], a[80:83], v[32:35]\n" \
"s_add_u32 m0, s86, s79\n" \
"ds_read_b64_tr_b16 a[122:123], v215 offset:18944\n" \
"buffer_store_dwordx4 v[140:143], v236, s[56:59], s71 offen sc1\n" \
"s_add_u32 s71, s93, 139264\n" \
"v_mfma_f32_16x16x32_bf16 v[32:35], a[100:103], a[84:87], v[32:35]\n" \
"buffer_load_dwordx4 v234, s[56:59], s81 offen offset:0 sc1 lds\n" \
"v_mfma_f32_16x16x32_bf16 v[36:39], a[104:107], a[80:83], v[36:39]\n" \
"s_lshl_b32 s82, s82, 5\n" \
"s_add_u32 s82, s82, s47\n" \
"ds_read_b64_tr_b16 a[124:125], v215 offset:25088\n" \
"buffer_store_dwordx4 v[132:135], v236, s[56:59], s71 offen sc1\n" \
"s_add_u32 s71, s93, 69632\n" \
"v_add_u32_e64 v213, s85, v211\n" \
"v_mfma_f32_16x16x32_bf16 v[36:39], a[108:111], a[84:87], v[36:39]\n" \
"s_waitcnt lgkmcnt(3)\n" \
"buffer_load_dwordx4 v234, s[56:59], s81 offen offset:1024 sc1 lds\n" \
"v_mfma_f32_16x16x32_bf16 v[52:55], a[104:107], a[88:91], v[52:55]\n" \
"ds_read_b64_tr_b16 a[126:127], v215 offset:27136\n" \
"buffer_store_dwordx4 v[124:127], v236, s[56:59], s71 offen sc1\n" \
"s_add_u32 s71, s93, 0\n" \
"v_mfma_f32_16x16x32_bf16 v[52:55], a[108:111], a[92:95], v[52:55]\n" \
"buffer_load_dwordx4 v234, s[56:59], s81 offen offset:2048 sc1 lds\n" \
"v_mfma_f32_16x16x32_bf16 v[48:51], a[96:99], a[88:91], v[48:51]\n" \
"v_add_u32_e64 v214, s85, v212\n" \
"v_xor_b32_e64 v215, 32, v214\n" \
"v_mfma_f32_16x16x32_bf16 v[48:51], a[100:103], a[92:95], v[48:51]\n" \
"buffer_store_dwordx4 v[116:119], v236, s[56:59], s71 offen sc1\n" \
"s_waitcnt vmcnt(23) lgkmcnt(0)\n" \
"s_barrier\n" \
"v_mfma_f32_16x16x32_bf16 v[8:11], a[112:115], a[64:67], v[8:11]\n" \
"ds_read_b64_tr_b16 a[96:97], v214 offset:16384\n" \
"ds_read_b64_tr_b16 a[98:99], v214 offset:18432\n" \
"v_mfma_f32_16x16x32_bf16 v[8:11], a[116:119], a[68:71], v[8:11]\n" \
"buffer_load_dwordx4 v234, s[56:59], s81 offen offset:3072 sc1 lds\n" \
"v_mfma_f32_16x16x32_bf16 v[12:15], a[120:123], a[64:67], v[12:15]\n" \
"ds_read_b64_tr_b16 a[100:101], v214 offset:24576\n" \
"ds_read_b64_tr_b16 a[102:103], v214 offset:26624\n" \
"v_mfma_f32_16x16x32_bf16 v[12:15], a[124:127], a[68:71], v[12:15]\n" \
"ds_read_b128 a[64:67], v213 offset:0\n" \
"v_mfma_f32_16x16x32_bf16 v[28:31], a[120:123], a[72:75], v[28:31]\n" \
"s_add_u32 m0, m0, 0x4000\n" \
"ds_read_b128 a[68:71], v213 offset:2048\n" \
"v_mfma_f32_16x16x32_bf16 v[28:31], a[124:127], a[76:79], v[28:31]\n" \
"buffer_load_dwordx4 v216, s[36:39], s82 offen offset:0 lds\n" \
"v_mfma_f32_16x16x32_bf16 v[24:27], a[112:115], a[72:75], v[24:27]\n" \
"ds_read_b64_tr_b16 a[104:105], v215 offset:16384\n" \
"ds_read_b64_tr_b16 a[106:107], v215 offset:18432\n" \
"v_mfma_f32_16x16x32_bf16 v[24:27], a[116:119], a[76:79], v[24:27]\n" \
"ds_read_b128 a[72:75], v213 offset:256\n" \
"v_mfma_f32_16x16x32_bf16 v[40:43], a[112:115], a[80:83], v[40:43]\n" \
"s_add_u32 s48, s48, 0x1000\n" \
"ds_read_b128 a[76:79], v213 offset:2304\n" \
"v_mfma_f32_16x16x32_bf16 v[40:43], a[116:119], a[84:87], v[40:43]\n" \
"buffer_load_dwordx4 v217, s[36:39], s82 offen offset:1024 lds\n" \
"v_mfma_f32_16x16x32_bf16 v[44:47], a[120:123], a[80:83], v[44:47]\n" \
"s_sub_u32 s86, s86, 32768\n" \
"s_cselect_b32 s86, 131072, s86\n" \
"ds_read_b64_tr_b16 a[108:109], v215 offset:24576\n" \
"ds_read_b64_tr_b16 a[110:111], v215 offset:26624\n" \
"v_mfma_f32_16x16x32_bf16 v[44:47], a[124:127], a[84:87], v[44:47]\n" \
"buffer_load_dwordx4 v218, s[36:39], s82 offen offset:2048 lds\n" \
"v_mfma_f32_16x16x32_bf16 v[60:63], a[120:123], a[88:91], v[60:63]\n" \
"ds_read_b128 a[80:83], v213 offset:4096\n" \
"s_waitcnt lgkmcnt(4)\n" \
"v_mfma_f32_16x16x32_bf16 v[60:63], a[124:127], a[92:95], v[60:63]\n" \
"ds_read_b128 a[84:87], v213 offset:6144\n" \
"v_mfma_f32_16x16x32_bf16 v[56:59], a[112:115], a[88:91], v[56:59]\n" \
"buffer_load_dwordx4 v219, s[36:39], s82 offen offset:3072 lds\n" \
"v_mfma_f32_16x16x32_bf16 v[56:59], a[116:119], a[92:95], v[56:59]\n" \
"s_mov_b32 s72, 2\n" \
"s_nop 0\n" \
"Lo0_%=:\n" \
"v_mfma_f32_16x16x32_bf16 v[0:3], a[96:99], a[64:67], v[0:3]\n" \
"ds_read_b128 a[88:91], v213 offset:4352\n" \
"v_mfma_f32_16x16x32_bf16 v[0:3], a[100:103], a[68:71], v[0:3]\n" \
"s_waitcnt lgkmcnt(2)\n" \
"s_xor_b32 s82, s48, s49\n" \
"ds_read_b128 a[92:95], v213 offset:6400\n" \
"v_mfma_f32_16x16x32_bf16 v[4:7], a[104:107], a[64:67], v[4:7]\n" \
"s_cmp_lt_u32 s48, s50\n" \
"s_cselect_b32 s82, s82, s48\n" \
"ds_read_b64_tr_b16 a[112:113], v214 offset:16896\n" \
"v_mfma_f32_16x16x32_bf16 v[4:7], a[108:111], a[68:71], v[4:7]\n" \
"s_add_u32 s81, s82, s46\n" \
"ds_read_b64_tr_b16 a[114:115], v214 offset:18944\n" \
"v_mfma_f32_16x16x32_bf16 v[20:23], a[104:107], a[72:75], v[20:23]\n" \
"s_add_u32 m0, s86, s79\n" \
"ds_read_b64_tr_b16 a[116:117], v214 offset:25088\n" \
"v_mfma_f32_16x16x32_bf16 v[20:23], a[108:111], a[76:79], v[20:23]\n" \
"buffer_load_dwordx4 v234, s[56:59], s81 offen offset:0 sc1 lds\n" \
"v_mfma_f32_16x16x32_bf16 v[16:19], a[96:99], a[72:75], v[16:19]\n" \
"s_sub_u32 s85, s85, 32768\n" \
"s_cselect_b32 s85, 131072, s85\n" \
"ds_read_b64_tr_b16 a[118:119], v214 offset:27136\n" \
"v_mfma_f32_16x16x32_bf16 v[16:19], a[100:103], a[76:79], v[16:19]\n" \
"s_waitcnt lgkmcnt(3)\n" \
"s_lshl_b32 s82, s82, 5\n" \
"ds_read_b64_tr_b16 a[120:121], v215 offset:16896\n" \
"v_mfma_f32_16x16x32_bf16 v[32:35], a[96:99], a[80:83], v[32:35]\n" \
"s_add_u32 s82, s82, s47\n" \
"ds_read_b64_tr_b16 a[122:123], v215 offset:18944\n" \
"v_mfma_f32_16x16x32_bf16 v[32:35], a[100:103], a[84:87], v[32:35]\n" \
"buffer_load_dwordx4 v234, s[56:59], s81 offen offset:1024 sc1 lds\n" \
"v_mfma_f32_16x16x32_bf16 v[36:39], a[104:107], a[80:83], v[36:39]\n" \
"ds_read_b64_tr_b16 a[124:125], v215 offset:25088\n" \
"v_add_u32 v213, s85, v211\n" \
"v_mfma_f32_16x16x32_bf16 v[36:39], a[108:111], a[84:87], v[36:39]\n" \
"buffer_load_dwordx4 v234, s[56:59], s81 offen offset:2048 sc1 lds\n" \
"v_mfma_f32_16x16x32_bf16 v[52:55], a[104:107], a[88:91], v[52:55]\n" \
"ds_read_b64_tr_b16 a[126:127], v215 offset:27136\n" \
"v_mfma_f32_16x16x32_bf16 v[52:55], a[108:111], a[92:95], v[52:55]\n" \
"buffer_load_dwordx4 v234, s[56:59], s81 offen offset:3072 sc1 lds\n" \
"v_mfma_f32_16x16x32_bf16 v[48:51], a[96:99], a[88:91], v[48:51]\n" \
"v_add_u32 v214, s85, v212\n" \
"v_xor_b32 v215, 32, v214\n" \
"v_mfma_f32_16x16x32_bf16 v[48:51], a[100:103], a[92:95], v[48:51]\n" \
"s_waitcnt vmcnt(20) lgkmcnt(0)\n" \
"s_barrier\n" \
"v_mfma_f32_16x16x32_bf16 v[8:11], a[112:115], a[64:67], v[8:11]\n" \
"s_add_u32 m0, m0, 0x4000\n" \
"ds_read_b64_tr_b16 a[96:97], v214 offset:16384\n" \
"ds_read_b64_tr_b16 a[98:99], v214 offset:18432\n" \
"v_mfma_f32_16x16x32_bf16 v[8:11], a[116:119], a[68:71], v[8:11]\n" \
"buffer_load_dwordx4 v216, s[36:39], s82 offen offset:0 lds\n" \
"v_mfma_f32_16x16x32_bf16 v[12:15], a[120:123], a[64:67], v[12:15]\n" \
"ds_read_b64_tr_b16 a[100:101], v214 offset:24576\n" \
"ds_read_b64_tr_b16 a[102:103], v214 offset:26624\n" \
"v_mfma_f32_16x16x32_bf16 v[12:15], a[124:127], a[68:71], v[12:15]\n" \
"ds_read_b128 a[64:67], v213 offset:0\n" \
"v_mfma_f32_16x16x32_bf16 v[28:31], a[120:123], a[72:75], v[28:31]\n" \
"s_add_u32 s48, s48, 0x1000\n" \
"ds_read_b128 a[68:71], v213 offset:2048\n" \
"v_mfma_f32_16x16x32_bf16 v[28:31], a[124:127], a[76:79], v[28:31]\n" \
"buffer_load_dwordx4 v217, s[36:39], s82 offen offset:1024 lds\n" \
"v_mfma_f32_16x16x32_bf16 v[24:27], a[112:115], a[72:75], v[24:27]\n" \
"ds_read_b64_tr_b16 a[104:105], v215 offset:16384\n" \
"ds_read_b64_tr_b16 a[106:107], v215 offset:18432\n" \
"v_mfma_f32_16x16x32_bf16 v[24:27], a[116:119], a[76:79], v[24:27]\n" \
"ds_read_b128 a[72:75], v213 offset:256\n" \
"v_mfma_f32_16x16x32_bf16 v[40:43], a[112:115], a[80:83], v[40:43]\n" \
"s_sub_u32 s86, s86, 32768\n" \
"s_cselect_b32 s86, 131072, s86\n" \
"ds_read_b128 a[76:79], v213 offset:2304\n" \
"v_mfma_f32_16x16x32_bf16 v[40:43], a[116:119], a[84:87], v[40:43]\n" \
"buffer_load_dwordx4 v218, s[36:39], s82 offen offset:2048 lds\n" \
"v_mfma_f32_16x16x32_bf16 v[44:47], a[120:123], a[80:83], v[44:47]\n" \
"ds_read_b64_tr_b16 a[108:109], v215 offset:24576\n" \
"ds_read_b64_tr_b16 a[110:111], v215 offset:26624\n" \
"v_mfma_f32_16x16x32_bf16 v[44:47], a[124:127], a[84:87], v[44:47]\n" \
"buffer_load_dwordx4 v219, s[36:39], s82 offen offset:3072 lds\n" \
"v_mfma_f32_16x16x32_bf16 v[60:63], a[120:123], a[88:91], v[60:63]\n" \
"ds_read_b128 a[80:83], v213 offset:4096\n" \
"s_waitcnt lgkmcnt(4)\n" \
"v_mfma_f32_16x16x32_bf16 v[60:63], a[124:127], a[92:95], v[60:63]\n" \
"s_sub_u32 s72, s72, 1\n" \
"ds_read_b128 a[84:87], v213 offset:6144\n" \
"v_mfma_f32_16x16x32_bf16 v[56:59], a[112:115], a[88:91], v[56:59]\n" \
"v_mfma_f32_16x16x32_bf16 v[56:59], a[116:119], a[92:95], v[56:59]\n" \
"s_cbranch_scc0 Lo0_%=\n" \
"s_setpc_b64 s[44:45]\n" \
"Lor_%=:\n" \
"buffer_store_dword v230, v231, s[56:59], s12 offen sc1\n" \
"s_mov_b32 s72, s10\n" \
"s_call_b64 s[44:45], Lo0_%=\n" \
"buffer_load_dword v220, v221, s[56:59], s67 offen sc1\n" \
"s_mov_b32 s72, 1\n" \
"s_call_b64 s[44:45], Lo0_%=\n" \
"s_waitcnt vmcnt(16)\n" \
"v_cmp_eq_u32 vcc, s60, v220\n" \
"s_nop 1\n" \
"s_andn2_b32 s70, s74, vcc_lo\n" \
"s_cbranch_scc1 Lpl6_%=\n" \
"Lpr6_%=:\n" \
"s_cmp_eq_u32 s90, 0\n" \
"s_cselect_b32 s46, s46, s83\n" \
"s_cselect_b32 s47, s47, s84\n" \
"s_cselect_b32 s48, s48, 0\n" \
"s_cselect_b64 s[36:37], s[36:37], s[40:41]\n" \
"s_cselect_b32 s38, s38, s42\n" \
"s_mov_b32 s72, 3\n" \
"s_call_b64 s[44:45], Lo0_%=\n" \
"s_cmp_eq_u32 s90, 0\n" \
"s_cbranch_scc0 Lrole1_%=\n" \
"s_mov_b32 s72, s11\n" \
"s_call_b64 s[44:45], Lo0_%=\n" \
"buffer_load_dword v220, v241, s[56:59], s64 offen sc1\n" \
"s_mov_b32 s72, 1\n" \
"s_call_b64 s[44:45], Lo0_%=\n" \
"v_mfma_f32_16x16x32_bf16 v[0:3], a[96:99], a[64:67], v[0:3]\n" \
"s_waitcnt lgkmcnt(2)\n" \
"ds_read_b128 a[88:91], v213 offset:4352\n" \
"v_mov_b32 v74, 0x3f80\n" \
"v_mfma_f32_16x16x32_bf16 v[0:3], a[100:103], a[68:71], v[0:3]\n" \
"s_add_u32 s48, s86, 32768\n" \
"s_cmp_eq_u32 s48, 163840\n" \
"s_cselect_b32 s48, 0, s48\n" \
"ds_read_b128 a[92:95], v213 offset:6400\n" \
"v_mfma_f32_16x16x32_bf16 v[4:7], a[104:107], a[64:67], v[4:7]\n" \
"ds_read_b64_tr_b16 a[112:113], v214 offset:16896\n" \
"v_mov_b32 v64, 0\n" \
"v_mfma_f32_16x16x32_bf16 v[4:7], a[108:111], a[68:71], v[4:7]\n" \
"s_add_u32 s49, s48, 32768\n" \
"s_cmp_eq_u32 s49, 163840\n" \
"s_cselect_b32 s49, 0, s49\n" \
"ds_read_b64_tr_b16 a[114:115], v214 offset:18944\n" \
"v_mfma_f32_16x16x32_bf16 v[20:23], a[104:107], a[72:75], v[20:23]\n" \
"ds_read_b64_tr_b16 a[116:117], v214 offset:25088\n" \
"v_add_u32 v224, s49, v211\n" \
"v_mfma_f32_16x16x32_bf16 v[20:23], a[108:111], a[76:79], v[20:23]\n" \
"v_add_u32 v225, s49, v212\n" \
"v_xor_b32 v226, 32, v225\n" \
"v_mfma_f32_16x16x32_bf16 v[16:19], a[96:99], a[72:75], v[16:19]\n" \
"s_sub_u32 s85, s85, 32768\n" \
"s_cselect_b32 s85, 131072, s85\n" \
"ds_read_b64_tr_b16 a[118:119], v214 offset:27136\n" \
"v_mfma_f32_16x16x32_bf16 v[16:19], a[100:103], a[76:79], v[16:19]\n" \
"s_waitcnt lgkmcnt(3)\n" \
"ds_read_b64_tr_b16 a[120:121], v215 offset:16896\n" \
"v_add_u32 v227, s48, v211\n" \
"v_mfma_f32_16x16x32_bf16 v[32:35], a[96:99], a[80:83], v[32:35]\n" \
"ds_read_b64_tr_b16 a[122:123], v215 offset:18944\n" \
"v_add_u32 v228, s48, v212\n" \
"v_mfma_f32_16x16x32_bf16 v[32:35], a[100:103], a[84:87], v[32:35]\n" \
"v_xor_b32 v229, 32, v228\n" \
"v_mov_b32 v65, 0\n" \
"v_mfma_f32_16x16x32_bf16 v[36:39], a[104:107], a[80:83], v[36:39]\n" \
"ds_read_b64_tr_b16 a[124:125], v215 offset:25088\n" \
"v_add_u32 v213, s85, v211\n" \
"v_mfma_f32_16x16x32_bf16 v[36:39], a[108:111], a[84:87], v[36:39]\n" \
"v_mov_b32 v68, 0\n" \
"v_mov_b32 v69, 0\n" \
"v_mfma_f32_16x16x32_bf16 v[52:55], a[104:107], a[88:91], v[52:55]\n" \
"ds_read_b64_tr_b16 a[126:127], v215 offset:27136\n" \
"v_lshrrev_b32 v72, 8, v234\n" \
"v_mfma_f32_16x16x32_bf16 v[52:55], a[108:111], a[92:95], v[52:55]\n" \
"v_bfe_u32 v73, v234, 6, 2\n" \
"v_cmp_eq_u32 s[16:17], v72, v73\n" \
"v_mfma_f32_16x16x32_bf16 v[48:51], a[96:99], a[88:91], v[48:51]\n" \
"v_add_u32 v214, s85, v212\n" \
"v_xor_b32 v215, 32, v214\n" \
"v_mfma_f32_16x16x32_bf16 v[48:51], a[100:103], a[92:95], v[48:51]\n" \
"s_waitcnt vmcnt(8) lgkmcnt(0)\n" \
"s_barrier\n" \
"s_waitcnt vmcnt(16)\n" \
"v_cmp_eq_u32 vcc, s60, v220\n" \
"s_nop 1\n" \
"s_andn2_b32 s70, s69, vcc_lo\n" \
"s_cbranch_scc1 Lpl7_%=\n" \
"Lpr7_%=:\n" \
"v_mfma_f32_16x16x32_bf16 v[8:11], a[112:115], a[64:67], v[8:11]\n" \
"s_add_u32 s71, s87, 0\n" \
"ds_read_b128 a[0:3], v213 offset:0\n" \
"ds_read_b128 a[4:7], v213 offset:2048\n" \
"v_mfma_f32_16x16x32_bf16 v[8:11], a[116:119], a[68:71], v[8:11]\n" \
"buffer_load_dwordx4 v[176:179], v234, s[56:59], s71 offen offset:0 sc1\n" \
"v_mfma_f32_16x16x32_bf16 v[12:15], a[120:123], a[64:67], v[12:15]\n" \
"ds_read_b64_tr_b16 a[128:129], v214 offset:16384\n" \
"ds_read_b64_tr_b16 a[130:131], v214 offset:18432\n" \
"v_mfma_f32_16x16x32_bf16 v[12:15], a[124:127], a[68:71], v[12:15]\n" \
"buffer_load_dwordx4 v[180:183], v234, s[56:59], s71 offen offset:1024 sc1\n" \
"v_mfma_f32_16x16x32_bf16 v[28:31], a[120:123], a[72:75], v[28:31]\n" \
"ds_read_b64_tr_b16 a[132:133], v214 offset:24576\n" \
"ds_read_b64_tr_b16 a[134:135], v214 offset:26624\n" \
"v_mfma_f32_16x16x32_bf16 v[28:31], a[124:127], a[76:79], v[28:31]\n" \
"buffer_load_dwordx4 v[184:187], v234, s[56:59], s71 offen offset:2048 sc1\n" \
"v_mfma_f32_16x16x32_bf16 v[24:27], a[112:115], a[72:75], v[24:27]\n" \
"ds_read_b64_tr_b16 a[136:137], v215 offset:16384\n" \
"ds_read_b64_tr_b16 a[138:139], v215 offset:18432\n" \
"v_mfma_f32_16x16x32_bf16 v[24:27], a[116:119], a[76:79], v[24:27]\n" \
"buffer_load_dwordx4 v[188:191], v234, s[56:59], s71 offen offset:3072 sc1\n" \
"v_mfma_f32_16x16x32_bf16 v[40:43], a[112:115], a[80:83], v[40:43]\n" \
"ds_read_b64_tr_b16 a[140:141], v215 offset:24576\n" \
"ds_read_b64_tr_b16 a[142:143], v215 offset:26624\n" \
"v_mfma_f32_16x16x32_bf16 v[40:43], a[116:119], a[84:87], v[40:43]\n" \
"ds_read_b64_tr_b16 a[144:145], v214 offset:16896\n" \
"ds_read_b64_tr_b16 a[146:147], v214 offset:18944\n" \
"v_mfma_f32_16x16x32_bf16 v[44:47], a[120:123], a[80:83], v[44:47]\n" \
"ds_read_b64_tr_b16 a[148:149], v214 offset:25088\n" \
"ds_read_b64_tr_b16 a[150:151], v214 offset:27136\n" \
"v_mfma_f32_16x16x32_bf16 v[44:47], a[124:127], a[84:87], v[44:47]\n" \
"ds_read_b64_tr_b16 a[152:153], v215 offset:16896\n" \
"ds_read_b64_tr_b16 a[154:155], v215 offset:18944\n" \
"v_mfma_f32_16x16x32_bf16 v[60:63], a[120:123], a[88:91], v[60:63]\n" \
"ds_read_b64_tr_b16 a[156:157], v215 offset:25088\n" \
"ds_read_b64_tr_b16 a[158:159], v215 offset:27136\n" \
"v_mfma_f32_16x16x32_bf16 v[60:63], a[124:127], a[92:95], v[60:63]\n" \
"ds_read_b128 a[24:27], v213 offset:256\n" \
"ds_read_b128 a[28:31], v213 offset:2304\n" \
"v_mfma_f32_16x16x32_bf16 v[56:59], a[112:115], a[88:91], v[56:59]\n" \
"ds_read_b128 a[8:11], v224 offset:0\n" \
"ds_read_b128 a[12:15], v224 offset:2048\n" \
"v_mfma_f32_16x16x32_bf16 v[56:59], a[116:119], a[92:95], v[56:59]\n" \
"s_waitcnt lgkmcnt(8)\n" \
"ds_read_b64_tr_b16 a[160:161], v225 offset:16384\n" \
"ds_read_b64_tr_b16 a[162:163], v225 offset:18432\n" \
"v_mfma_f32_16x16x32_bf16 v[0:3], a[128:131], a[0:3], v[0:3]\n" \
"ds_read_b64_tr_b16 a[164:165], v225 offset:24576\n" \
"ds_read_b64_tr_b16 a[166:167], v225 offset:26624\n" \
"v_mfma_f32_16x16x32_bf16 v[0:3], a[132:135], a[4:7], v[0:3]\n" \
"ds_read_b64_tr_b16 a[168:169], v226 offset:16384\n" \
"ds_read_b64_tr_b16 a[170:171], v226 offset:18432\n" \
"v_mfma_f32_16x16x32_bf16 v[4:7], a[136:139], a[0:3], v[4:7]\n" \
"s_mov_b32 s70, 0x20000000\n" \
"ds_read_b64_tr_b16 a[172:173], v226 offset:24576\n" \
"ds_read_b64_tr_b16 a[174:175], v226 offset:26624\n" \
"v_mfma_f32_16x16x32_bf16 v[4:7], a[140:143], a[4:7], v[4:7]\n" \
"s_cmp_eq_u32 s64, 65616896\n" \
"s_cselect_b32 s70, 65593344, s70\n" \
"ds_read_b64_tr_b16 a[176:177], v225 offset:16896\n" \
"ds_read_b64_tr_b16 a[178:179], v225 offset:18944\n" \
"buffer_store_dword v230, v231, s[56:59], s70 offen sc1\n" \
"v_mfma_f32_16x16x32_bf16 v[8:11], a[144:147], a[0:3], v[8:11]\n" \
"s_waitcnt lgkmcnt(8)\n" \
"ds_read_b64_tr_b16 a[180:181], v225 offset:25088\n" \
"ds_read_b64_tr_b16 a[182:183], v225 offset:27136\n" \
"v_mfma_f32_16x16x32_bf16 v[8:11], a[148:151], a[4:7], v[8:11]\n" \
"s_sub_u32 s68, s86, 32768\n" \
"s_cselect_b32 s68, 131072, s68\n" \
"ds_read_b64_tr_b16 a[184:185], v226 offset:16896\n" \
"ds_read_b64_tr_b16 a[186:187], v226 offset:18944\n" \
"v_mfma_f32_16x16x32_bf16 v[12:15], a[152:155], a[0:3], v[12:15]\n" \
"ds_read_b64_tr_b16 a[188:189], v226 offset:25088\n" \
"ds_read_b64_tr_b16 a[190:191], v226 offset:27136\n" \
"v_mfma_f32_16x16x32_bf16 v[12:15], a[156:159], a[4:7], v[12:15]\n" \
"s_cmp_lt_u32 s61, 2\n" \
"s_cselect_b32 s68, s86, s68\n" \
"v_and_b32 v72, 16, v234\n" \
"v_lshlrev_b32 v72, v72, v74\n" \
"v_mfma_f32_16x16x32_bf16 v[28:31], a[152:155], a[24:27], v[28:31]\n" \
"s_and_b32 s50, s61, 1\n" \
"v_cndmask_b32 v72, 0, v72, s[16:17]\n" \
"v_bfe_i32 v73, v234, 5, 1\n" \
"v_mfma_f32_16x16x32_bf16 v[28:31], a[156:159], a[28:31], v[28:31]\n" \
"s_mul_i32 s50, s50, 9216\n" \
"v_and_b32 v67, v72, v73\n" \
"v_xor_b32 v66, v72, v67\n" \
"v_mfma_f32_16x16x32_bf16 v[24:27], a[144:147], a[24:27], v[24:27]\n" \
"s_add_u32 s68, s68, s50\n" \
"s_add_u32 s68, s68, %3\n" \
"v_lshrrev_b32 v132, 4, v234\n" \
"v_mfma_f32_16x16x32_bf16 v[24:27], a[148:151], a[28:31], v[24:27]\n" \
"v_and_b32 v133, 15, v132\n" \
"v_mul_u32_u24 v128, 144, v133\n" \
"v_lshrrev_b32 v133, 4, v132\n" \
"v_mfma_f32_16x16x32_bf16 v[20:23], a[136:139], a[24:27], v[20:23]\n" \
"s_waitcnt vmcnt(5)\n" \
"s_barrier\n" \
"v_mfma_f32_16x16x32_bf16 v[20:23], a[140:143], a[28:31], v[20:23]\n" \
"s_add_u32 s71, s87, 4096\n" \
"ds_read_b128 a[32:35], v224 offset:256\n" \
"ds_read_b128 a[36:39], v224 offset:2304\n" \
"v_mfma_f32_16x16x32_bf16 v[16:19], a[128:131], a[24:27], v[16:19]\n" \
"buffer_load_dwordx4 v[192:195], v234, s[56:59], s71 offen offset:0 sc1\n" \
"v_mfma_f32_16x16x32_bf16 v[16:19], a[132:135], a[28:31], v[16:19]\n" \
"s_waitcnt lgkmcnt(2)\n" \
"ds_read_b128 a[16:19], v227 offset:0\n" \
"ds_read_b128 a[20:23], v227 offset:2048\n" \
"v_mfma_f32_16x16x32_bf16 v[0:3], a[160:163], a[8:11], v[0:3]\n" \
"ds_read_b64_tr_b16 a[192:193], v228 offset:16384\n" \
"ds_read_b64_tr_b16 a[194:195], v228 offset:18432\n" \
"v_mfma_f32_16x16x32_bf16 v[0:3], a[164:167], a[12:15], v[0:3]\n" \
"buffer_load_dwordx4 v[196:199], v234, s[56:59], s71 offen offset:1024 sc1\n" \
"v_mfma_f32_16x16x32_bf16 v[4:7], a[168:171], a[8:11], v[4:7]\n" \
"ds_read_b64_tr_b16 a[196:197], v228 offset:24576\n" \
"ds_read_b64_tr_b16 a[198:199], v228 offset:26624\n" \
"v_mfma_f32_16x16x32_bf16 v[4:7], a[172:175], a[12:15], v[4:7]\n" \
"ds_read_b64_tr_b16 a[200:201], v229 offset:16384\n" \
"ds_read_b64_tr_b16 a[202:203], v229 offset:18432\n" \
"v_mfma_f32_16x16x32_bf16 v[8:11], a[176:179], a[8:11], v[8:11]\n" \
"buffer_load_dwordx4 v[200:203], v234, s[56:59], s71 offen offset:2048 sc1\n" \
"v_mfma_f32_16x16x32_bf16 v[8:11], a[180:183], a[12:15], v[8:11]\n" \
"ds_read_b64_tr_b16 a[204:205], v229 offset:24576\n" \
"ds_read_b64_tr_b16 a[206:207], v229 offset:26624\n" \
"v_lshl_add_u32 v128, v133, 3, v128\n" \
"v_mfma_f32_16x16x32_bf16 v[12:15], a[184:187], a[8:11], v[12:15]\n" \
"s_waitcnt lgkmcnt(6)\n" \
"ds_read_b64_tr_b16 a[208:209], v228 offset:16896\n" \
"ds_read_b64_tr_b16 a[210:211], v228 offset:18944\n" \
"v_mfma_f32_16x16x32_bf16 v[12:15], a[188:191], a[12:15], v[12:15]\n" \
"buffer_load_dwordx4 v[204:207], v234, s[56:59], s71 offen offset:3072 sc1\n" \
"v_mfma_f32_16x16x32_bf16 v[28:31], a[184:187], a[32:35], v[28:31]\n" \
"ds_read_b64_tr_b16 a[212:213], v228 offset:25088\n" \
"ds_read_b64_tr_b16 a[214:215], v228 offset:27136\n" \
"v_mfma_f32_16x16x32_bf16 v[28:31], a[188:191], a[36:39], v[28:31]\n" \
"ds_read_b64_tr_b16 a[216:217], v229 offset:16896\n" \
"ds_read_b64_tr_b16 a[218:219], v229 offset:18944\n" \
"v_mfma_f32_16x16x32_bf16 v[24:27], a[176:179], a[32:35], v[24:27]\n" \
"ds_read_b64_tr_b16 a[220:221], v229 offset:25088\n" \
"ds_read_b64_tr_b16 a[222:223], v229 offset:27136\n" \
"v_mfma_f32_16x16x32_bf16 v[24:27], a[180:183], a[36:39], v[24:27]\n" \
"ds_read_b128 a[40:43], v227 offset:256\n" \
"ds_read_b128 a[44:47], v227 offset:2304\n" \
"v_mfma_f32_16x16x32_bf16 v[20:23], a[168:171], a[32:35], v[20:23]\n" \
"ds_read_b128 a[248:251], v213 offset:4096\n" \
"ds_read_b128 a[252:255], v213 offset:6144\n" \
"v_mfma_f32_16x16x32_bf16 v[20:23], a[172:175], a[36:39], v[20:23]\n" \
"ds_read_b128 a[48:51], v224 offset:4096\n" \
"ds_read_b128 a[52:55], v224 offset:6144\n" \
"v_mfma_f32_16x16x32_bf16 v[16:19], a[160:163], a[32:35], v[16:19]\n" \
"ds_read_b128 a[56:59], v227 offset:4096\n" \
"ds_read_b128 a[60:63], v227 offset:6144\n" \
"v_mfma_f32_16x16x32_bf16 v[16:19], a[164:167], a[36:39], v[16:19]\n" \
"s_waitcnt lgkmcnt(8)\n" \
"ds_read_b128 a[224:227], v213 offset:4352\n" \
"ds_read_b128 a[228:231], v213 offset:6400\n" \
"v_add_u32 v128, s68, v128\n" \
"v_mfma_f32_16x16x32_bf16 v[0:3], a[192:195], a[16:19], v[0:3]\n" \
"s_waitcnt vmcnt(7)\n" \
"ds_read_b128 a[232:235], v224 offset:4352\n" \
"ds_read_b128 a[236:239], v224 offset:6400\n" \
"v_mfma_f32_16x16x32_bf16 v[0:3], a[196:199], a[20:23], v[0:3]\n" \
"ds_read_b128 a[240:243], v227 offset:4352\n" \
"ds_read_b128 a[244:247], v227 offset:6400\n" \
"v_mfma_f32_16x16x32_bf16 v[0:3], v[66:69], v[176:179], v[0:3]\n" \
"v_lshrrev_b32 v133, 3, v132\n" \
"v_and_b32 v134, 7, v132\n" \
"v_mfma_f32_16x16x32_bf16 v[4:7], a[200:203], a[16:19], v[4:7]\n" \
"v_mul_u32_u24 v129, 144, v133\n" \
"v_lshl_add_u32 v129, v134, 4, v129\n" \
"v_mfma_f32_16x16x32_bf16 v[4:7], a[204:207], a[20:23], v[4:7]\n" \
"v_add_u32 v129, s68, v129\n" \
"v_lshlrev_b32 v134, 4, v134\n" \
"v_mfma_f32_16x16x32_bf16 v[4:7], v[64:67], v[176:179], v[4:7]\n" \
"v_lshl_add_u32 v130, v133, 12, v134\n" \
"v_mfma_f32_16x16x32_bf16 v[8:11], a[208:211], a[16:19], v[8:11]\n" \
"v_mul_f32 v0, 4.0, v0\n" \
"v_mul_f32 v1, 4.0, v1\n" \
"v_mul_f32 v2, 4.0, v2\n" \
"v_mul_f32 v3, 4.0, v3\n" \
"v_cvt_pk_bf16_f32 v136, v0, v1\n" \
"v_cvt_pk_bf16_f32 v137, v2, v3\n" \
"v_mfma_f32_16x16x32_bf16 v[8:11], a[212:215], a[20:23], v[8:11]\n" \
"v_mfma_f32_16x16x32_bf16 v[8:11], v[66:69], v[180:183], v[8:11]\n" \
"ds_write_b64 v128, v[136:137] offset:0\n" \
"v_mfma_f32_16x16x32_bf16 v[12:15], a[216:219], a[16:19], v[12:15]\n" \
"v_mul_f32 v4, 4.0, v4\n" \
"v_mul_f32 v5, 4.0, v5\n" \
"v_mfma_f32_16x16x32_bf16 v[12:15], a[220:223], a[20:23], v[12:15]\n" \
"v_mul_f32 v6, 4.0, v6\n" \
"v_mul_f32 v7, 4.0, v7\n" \
"v_mfma_f32_16x16x32_bf16 v[12:15], v[64:67], v[180:183], v[12:15]\n" \
"s_waitcnt lgkmcnt(1)\n" \
"v_cvt_pk_bf16_f32 v138, v4, v5\n" \
"v_cvt_pk_bf16_f32 v139, v6, v7\n" \
"v_mfma_f32_16x16x32_bf16 v[16:19], a[192:195], a[40:43], v[16:19]\n" \
"s_waitcnt vmcnt(5)\n" \
"v_mfma_f32_16x16x32_bf16 v[16:19], a[196:199], a[44:47], v[16:19]\n" \
"v_mfma_f32_16x16x32_bf16 v[16:19], v[66:69], v[184:187], v[16:19]\n" \
"ds_write_b64 v128, v[138:139] offset:32\n" \
"v_mul_f32 v8, 4.0, v8\n" \
"v_mfma_f32_16x16x32_bf16 v[20:23], a[200:203], a[40:43], v[20:23]\n" \
"v_mul_f32 v9, 4.0, v9\n" \
"v_mul_f32 v10, 4.0, v10\n" \
"v_mfma_f32_16x16x32_bf16 v[20:23], a[204:207], a[44:47], v[20:23]\n" \
"v_mul_f32 v11, 4.0, v11\n" \
"v_cvt_pk_bf16_f32 v136, v8, v9\n" \
"v_mfma_f32_16x16x32_bf16 v[20:23], v[64:67], v[184:187], v[20:23]\n" \
"v_cvt_pk_bf16_f32 v137, v10, v11\n" \
"v_mfma_f32_16x16x32_bf16 v[24:27], a[208:211], a[40:43], v[24:27]\n" \
"v_mfma_f32_16x16x32_bf16 v[24:27], a[212:215], a[44:47], v[24:27]\n" \
"v_mfma_f32_16x16x32_bf16 v[24:27], v[66:69], v[188:191], v[24:27]\n" \
"ds_write_b64 v128, v[136:137] offset:64\n" \
"v_mul_f32 v12, 4.0, v12\n" \
"v_mfma_f32_16x16x32_bf16 v[28:31], a[216:219], a[40:43], v[28:31]\n" \
"v_mul_f32 v13, 4.0, v13\n" \
"v_mul_f32 v14, 4.0, v14\n" \
"v_mfma_f32_16x16x32_bf16 v[28:31], a[220:223], a[44:47], v[28:31]\n" \
"v_mul_f32 v15, 4.0, v15\n" \
"v_cvt_pk_bf16_f32 v138, v12, v13\n" \
"v_mfma_f32_16x16x32_bf16 v[28:31], v[64:67], v[188:191], v[28:31]\n" \
"v_cvt_pk_bf16_f32 v139, v14, v15\n" \
"v_mfma_f32_16x16x32_bf16 v[32:35], a[128:131], a[248:251], v[32:35]\n" \
"ds_write_b64 v128, v[138:139] offset:96\n" \
"v_mfma_f32_16x16x32_bf16 v[32:35], a[132:135], a[252:255], v[32:35]\n" \
"ds_read_b128 v[140:143], v129 offset:0\n" \
"ds_read_b128 v[144:147], v129 offset:1152\n" \
"v_mfma_f32_16x16x32_bf16 v[32:35], a[160:163], a[48:51], v[32:35]\n" \
"s_waitcnt vmcnt(2)\n" \
"v_mul_f32 v16, 4.0, v16\n" \
"v_mul_f32 v17, 4.0, v17\n" \
"v_mfma_f32_16x16x32_bf16 v[32:35], a[164:167], a[52:55], v[32:35]\n" \
"v_mul_f32 v18, 4.0, v18\n" \
"v_mul_f32 v19, 4.0, v19\n" \
"v_mfma_f32_16x16x32_bf16 v[32:35], v[66:69], v[192:195], v[32:35]\n" \
"s_add_u32 s70, s88, 0\n" \
"s_waitcnt lgkmcnt(1)\n" \
"v_cvt_pk_bf16_f32 v136, v16, v17\n" \
"v_mfma_f32_16x16x32_bf16 v[32:35], a[192:195], a[56:59], v[32:35]\n" \
"buffer_store_dwordx4 v[140:143], v130, s[52:55], s70 offen sc1\n" \
"v_cvt_pk_bf16_f32 v137, v18, v19\n" \
"v_mfma_f32_16x16x32_bf16 v[32:35], a[196:199], a[60:63], v[32:35]\n" \
"ds_write_b64 v128, v[136:137] offset:2304\n" \
"v_mfma_f32_16x16x32_bf16 v[36:39], a[204:207], a[60:63], v[36:39]\n" \
"s_add_u32 s70, s88, 32768\n" \
"s_waitcnt lgkmcnt(1)\n" \
"v_mul_f32 v20, 4.0, v20\n" \
"v_mfma_f32_16x16x32_bf16 v[36:39], a[200:203], a[56:59], v[36:39]\n" \
"buffer_store_dwordx4 v[144:147], v130, s[52:55], s70 offen sc1\n" \
"v_mfma_f32_16x16x32_bf16 v[36:39], a[172:175], a[52:55], v[36:39]\n" \
"v_mul_f32 v21, 4.0, v21\n" \
"v_mul_f32 v22, 4.0, v22\n" \
"v_mfma_f32_16x16x32_bf16 v[36:39], a[168:171], a[48:51], v[36:39]\n" \
"v_mul_f32 v23, 4.0, v23\n" \
"v_cvt_pk_bf16_f32 v138, v20, v21\n" \
"v_mfma_f32_16x16x32_bf16 v[36:39], v[64:67], v[192:195], v[36:39]\n" \
"v_cvt_pk_bf16_f32 v139, v22, v23\n" \
"v_mfma_f32_16x16x32_bf16 v[36:39], a[140:143], a[252:255], v[36:39]\n" \
"v_mfma_f32_16x16x32_bf16 v[36:39], a[136:139], a[248:251], v[36:39]\n" \
"ds_write_b64 v128, v[138:139] offset:2336\n" \
"v_mul_f32 v24, 4.0, v24\n" \
"v_mul_f32 v25, 4.0, v25\n" \
"v_mfma_f32_16x16x32_bf16 v[40:43], a[144:147], a[248:251], v[40:43]\n" \
"v_mul_f32 v26, 4.0, v26\n" \
"v_mul_f32 v27, 4.0, v27\n" \
"v_mfma_f32_16x16x32_bf16 v[40:43], a[148:151], a[252:255], v[40:43]\n" \
"v_cvt_pk_bf16_f32 v136, v24, v25\n" \
"v_cvt_pk_bf16_f32 v137, v26, v27\n" \
"v_mfma_f32_16x16x32_bf16 v[40:43], a[176:179], a[48:51], v[40:43]\n" \
"ds_write_b64 v128, v[136:137] offset:2368\n" \
"v_mfma_f32_16x16x32_bf16 v[40:43], a[180:183], a[52:55], v[40:43]\n" \
"v_mul_f32 v28, 4.0, v28\n" \
"v_mul_f32 v29, 4.0, v29\n" \
"v_mfma_f32_16x16x32_bf16 v[40:43], v[66:69], v[196:199], v[40:43]\n" \
"v_mul_f32 v30, 4.0, v30\n" \
"v_mul_f32 v31, 4.0, v31\n" \
"v_mfma_f32_16x16x32_bf16 v[40:43], a[208:211], a[56:59], v[40:43]\n" \
"v_cvt_pk_bf16_f32 v138, v28, v29\n" \
"v_cvt_pk_bf16_f32 v139, v30, v31\n" \
"v_mfma_f32_16x16x32_bf16 v[40:43], a[212:215], a[60:63], v[40:43]\n" \
"ds_write_b64 v128, v[138:139] offset:2400\n" \
"v_mfma_f32_16x16x32_bf16 v[44:47], a[220:223], a[60:63], v[44:47]\n" \
"ds_read_b128 v[148:151], v129 offset:2304\n" \
"ds_read_b128 v[152:155], v129 offset:3456\n" \
"v_mfma_f32_16x16x32_bf16 v[44:47], a[216:219], a[56:59], v[44:47]\n" \
"v_mul_f32 v32, 4.0, v32\n" \
"v_mul_f32 v33, 4.0, v33\n" \
"v_mfma_f32_16x16x32_bf16 v[44:47], a[188:191], a[52:55], v[44:47]\n" \
"v_mul_f32 v34, 4.0, v34\n" \
"v_mul_f32 v35, 4.0, v35\n" \
"v_mfma_f32_16x16x32_bf16 v[44:47], a[184:187], a[48:51], v[44:47]\n" \
"s_add_u32 s70, s88, 65536\n" \
"s_waitcnt lgkmcnt(1)\n" \
"v_cvt_pk_bf16_f32 v136, v32, v33\n" \
"v_mfma_f32_16x16x32_bf16 v[44:47], v[64:67], v[196:199], v[44:47]\n" \
"buffer_store_dwordx4 v[148:151], v130, s[52:55], s70 offen sc1\n" \
"v_cvt_pk_bf16_f32 v137, v34, v35\n" \
"v_mfma_f32_16x16x32_bf16 v[44:47], a[156:159], a[252:255], v[44:47]\n" \
"v_mfma_f32_16x16x32_bf16 v[44:47], a[152:155], a[248:251], v[44:47]\n" \
"s_add_u32 s70, s88, 98304\n" \
"s_waitcnt lgkmcnt(0)\n" \
"ds_write_b64 v128, v[136:137] offset:4608\n" \
"v_mul_f32 v36, 4.0, v36\n" \
"v_mfma_f32_16x16x32_bf16 v[48:51], a[128:131], a[224:227], v[48:51]\n" \
"buffer_store_dwordx4 v[152:155], v130, s[52:55], s70 offen sc1\n" \
"v_mul_f32 v37, 4.0, v37\n" \
"v_mfma_f32_16x16x32_bf16 v[48:51], a[132:135], a[228:231], v[48:51]\n" \
"v_mul_f32 v38, 4.0, v38\n" \
"v_mul_f32 v39, 4.0, v39\n" \
"v_mfma_f32_16x16x32_bf16 v[48:51], a[160:163], a[232:235], v[48:51]\n" \
"s_waitcnt vmcnt(4)\n" \
"v_cvt_pk_bf16_f32 v138, v36, v37\n" \
"v_cvt_pk_bf16_f32 v139, v38, v39\n" \
"v_mfma_f32_16x16x32_bf16 v[48:51], a[164:167], a[236:239], v[48:51]\n" \
"ds_write_b64 v128, v[138:139] offset:4640\n" \
"v_mul_f32 v40, 4.0, v40\n" \
"v_mfma_f32_16x16x32_bf16 v[48:51], v[66:69], v[200:203], v[48:51]\n" \
"v_mul_f32 v41, 4.0, v41\n" \
"v_mul_f32 v42, 4.0, v42\n" \
"Lpg10800_%=:\n" \
"s_cbranch_execz Lcb1_%=\n" \
"v_mfma_f32_16x16x32_bf16 v[48:51], a[192:195], a[240:243], v[48:51]\n" \
"v_mul_f32 v43, 4.0, v43\n" \
"v_cvt_pk_bf16_f32 v136, v40, v41\n" \
"v_mfma_f32_16x16x32_bf16 v[48:51], a[196:199], a[244:247], v[48:51]\n" \
"v_cvt_pk_bf16_f32 v137, v42, v43\n" \
"v_mfma_f32_16x16x32_bf16 v[52:55], a[204:207], a[244:247], v[52:55]\n" \
"ds_write_b64 v128, v[136:137] offset:4672\n" \
"v_mul_f32 v44, 4.0, v44\n" \
"v_mul_f32 v45, 4.0, v45\n" \
"v_mfma_f32_16x16x32_bf16 v[52:55], a[200:203], a[240:243], v[52:55]\n" \
"v_mul_f32 v46, 4.0, v46\n" \
"v_mul_f32 v47, 4.0, v47\n" \
"v_mfma_f32_16x16x32_bf16 v[52:55], a[172:175], a[236:239], v[52:55]\n" \
"v_cvt_pk_bf16_f32 v138, v44, v45\n" \
"v_cvt_pk_bf16_f32 v139, v46, v47\n" \
"v_mfma_f32_16x16x32_bf16 v[52:55], a[168:171], a[232:235], v[52:55]\n" \
"ds_write_b64 v128, v[138:139] offset:4704\n" \
"v_mfma_f32_16x16x32_bf16 v[52:55], v[64:67], v[200:203], v[52:55]\n" \
"ds_read_b128 v[156:159], v129 offset:4608\n" \
"ds_read_b128 v[160:163], v129 offset:5760\n" \
"v_mfma_f32_16x16x32_bf16 v[52:55], a[140:143], a[228:231], v[52:55]\n" \
"v_mul_f32 v48, 4.0, v48\n" \
"v_mul_f32 v49, 4.0, v49\n" \
"v_mfma_f32_16x16x32_bf16 v[52:55], a[136:139], a[224:227], v[52:55]\n" \
"v_mul_f32 v50, 4.0, v50\n" \
"v_mul_f32 v51, 4.0, v51\n" \
"v_mfma_f32_16x16x32_bf16 v[56:59], a[144:147], a[224:227], v[56:59]\n" \
"s_add_u32 s70, s88, 131072\n" \
"s_waitcnt lgkmcnt(1)\n" \
"v_cvt_pk_bf16_f32 v136, v48, v49\n" \
"v_mfma_f32_16x16x32_bf16 v[56:59], a[148:151], a[228:231], v[56:59]\n" \
"buffer_store_dwordx4 v[156:159], v130, s[52:55], s70 offen sc1\n" \
"v_cvt_pk_bf16_f32 v137, v50, v51\n" \
"v_mfma_f32_16x16x32_bf16 v[56:59], a[176:179], a[232:235], v[56:59]\n" \
"ds_write_b64 v128, v[136:137] offset:6912\n" \
"v_mfma_f32_16x16x32_bf16 v[56:59], a[180:183], a[236:239], v[56:59]\n" \
"s_add_u32 s70, s88, 163840\n" \
"s_waitcnt lgkmcnt(1)\n" \
"v_mul_f32 v52, 4.0, v52\n" \
"v_mfma_f32_16x16x32_bf16 v[56:59], v[66:69], v[204:207], v[56:59]\n" \
"buffer_store_dwordx4 v[160:163], v130, s[52:55], s70 offen sc1\n" \
"v_mfma_f32_16x16x32_bf16 v[56:59], a[208:211], a[240:243], v[56:59]\n" \
"v_mul_f32 v53, 4.0, v53\n" \
"v_mul_f32 v54, 4.0, v54\n" \
"v_mfma_f32_16x16x32_bf16 v[56:59], a[212:215], a[244:247], v[56:59]\n" \
"v_mul_f32 v55, 4.0, v55\n" \
"v_cvt_pk_bf16_f32 v138, v52, v53\n" \
"v_cvt_pk_bf16_f32 v139, v54, v55\n" \
"v_mfma_f32_16x16x32_bf16 v[60:63], a[220:223], a[244:247], v[60:63]\n" \
"ds_write_b64 v128, v[138:139] offset:6944\n" \
"v_mfma_f32_16x16x32_bf16 v[60:63], a[216:219], a[240:243], v[60:63]\n" \
"v_mfma_f32_16x16x32_bf16 v[60:63], a[188:191], a[236:239], v[60:63]\n" \
"v_mfma_f32_16x16x32_bf16 v[60:63], a[184:187], a[232:235], v[60:63]\n" \
"v_mul_f32 v56, 4.0, v56\n" \
"v_mul_f32 v57, 4.0, v57\n" \
"v_mfma_f32_16x16x32_bf16 v[60:63], v[64:67], v[204:207], v[60:63]\n" \
"v_mul_f32 v58, 4.0, v58\n" \
"v_mul_f32 v59, 4.0, v59\n" \
"v_mfma_f32_16x16x32_bf16 v[60:63], a[156:159], a[228:231], v[60:63]\n" \
"v_cvt_pk_bf16_f32 v136, v56, v57\n" \
"v_cvt_pk_bf16_f32 v137, v58, v59\n" \
"v_mfma_f32_16x16x32_bf16 v[60:63], a[152:155], a[224:227], v[60:63]\n" \
"ds_write_b64 v128, v[136:137] offset:6976\n" \
"s_nop 7\n" \
"v_mul_f32 v60, 4.0, v60\n" \
"v_mul_f32 v61, 4.0, v61\n" \
"v_mul_f32 v62, 4.0, v62\n" \
"v_mul_f32 v63, 4.0, v63\n" \
"v_cvt_pk_bf16_f32 v138, v60, v61\n" \
"v_cvt_pk_bf16_f32 v139, v62, v63\n" \
"ds_write_b64 v128, v[138:139] offset:7008\n" \
"ds_read_b128 v[164:167], v129 offset:6912\n" \
"ds_read_b128 v[168:171], v129 offset:8064\n" \
"s_add_u32 s70, s88, 196608\n" \
"s_waitcnt lgkmcnt(1)\n" \
"buffer_store_dwordx4 v[164:167], v130, s[52:55], s70 offen sc1\n" \
"s_add_u32 s70, s88, 229376\n" \
"s_waitcnt lgkmcnt(0)\n" \
"buffer_store_dwordx4 v[168:171], v130, s[52:55], s70 offen sc1\n" \
"s_endpgm\n" \
".p2align 12\n" \
"Lrole1_%=:\n" \
"v_mfma_f32_16x16x32_bf16 v[64:67], a[96:99], a[64:67], 0\n" \
"Lpg10884_%=:\n" \
"s_cbranch_execz Lcb1_%=\n" \
"ds_read_b128 a[88:91], v213 offset:4352\n" \
"v_mfma_f32_16x16x32_bf16 v[64:67], a[100:103], a[68:71], v[64:67]\n" \
"s_waitcnt lgkmcnt(2)\n" \
"s_xor_b32 s82, s48, s49\n" \
"ds_read_b128 a[92:95], v213 offset:6400\n" \
"v_mfma_f32_16x16x32_bf16 v[68:71], a[104:107], a[64:67], 0\n" \
"s_cmp_lt_u32 s48, s50\n" \
"s_cselect_b32 s82, s82, s48\n" \
"ds_read_b64_tr_b16 a[112:113], v214 offset:16896\n" \
"v_mfma_f32_16x16x32_bf16 v[68:71], a[108:111], a[68:71], v[68:71]\n" \
"s_add_u32 s81, s82, s46\n" \
"ds_read_b64_tr_b16 a[114:115], v214 offset:18944\n" \
"v_mfma_f32_16x16x32_bf16 v[84:87], a[104:107], a[72:75], 0\n" \
"s_waitcnt lgkmcnt(4)\n" \
"s_add_u32 m0, s86, s79\n" \
"ds_read_b64_tr_b16 a[116:117], v214 offset:25088\n" \
"v_mfma_f32_16x16x32_bf16 v[84:87], a[108:111], a[76:79], v[84:87]\n" \
"buffer_load_dwordx4 v234, s[56:59], s81 offen offset:0 sc1 lds\n" \
"v_mfma_f32_16x16x32_bf16 v[80:83], a[96:99], a[72:75], 0\n" \
"s_sub_u32 s85, s85, 32768\n" \
"s_cselect_b32 s85, 131072, s85\n" \
"ds_read_b64_tr_b16 a[118:119], v214 offset:27136\n" \
"v_mfma_f32_16x16x32_bf16 v[80:83], a[100:103], a[76:79], v[80:83]\n" \
"s_lshl_b32 s82, s82, 5\n" \
"ds_read_b64_tr_b16 a[120:121], v215 offset:16896\n" \
"v_cvt_pk_bf16_f32 v0, v0, v1\n" \
"v_mfma_f32_16x16x32_bf16 v[96:99], a[96:99], a[80:83], 0\n" \
"s_add_u32 s82, s82, s47\n" \
"ds_read_b64_tr_b16 a[122:123], v215 offset:18944\n" \
"v_cvt_pk_bf16_f32 v1, v2, v3\n" \
"v_mfma_f32_16x16x32_bf16 v[96:99], a[100:103], a[84:87], v[96:99]\n" \
"buffer_load_dwordx4 v234, s[56:59], s81 offen offset:1024 sc1 lds\n" \
"v_cvt_pk_bf16_f32 v2, v4, v5\n" \
"v_mfma_f32_16x16x32_bf16 v[100:103], a[104:107], a[80:83], 0\n" \
"s_waitcnt lgkmcnt(3)\n" \
"ds_read_b64_tr_b16 a[124:125], v215 offset:25088\n" \
"v_add_u32 v213, s85, v211\n" \
"v_mfma_f32_16x16x32_bf16 v[100:103], a[108:111], a[84:87], v[100:103]\n" \
"buffer_load_dwordx4 v234, s[56:59], s81 offen offset:2048 sc1 lds\n" \
"v_mfma_f32_16x16x32_bf16 v[116:119], a[104:107], a[88:91], 0\n" \
"ds_read_b64_tr_b16 a[126:127], v215 offset:27136\n" \
"v_cvt_pk_bf16_f32 v3, v6, v7\n" \
"v_mfma_f32_16x16x32_bf16 v[116:119], a[108:111], a[92:95], v[116:119]\n" \
"buffer_load_dwordx4 v234, s[56:59], s81 offen offset:3072 sc1 lds\n" \
"v_cvt_pk_bf16_f32 v4, v8, v9\n" \
"v_mfma_f32_16x16x32_bf16 v[112:115], a[96:99], a[88:91], 0\n" \
"v_add_u32 v214, s85, v212\n" \
"v_xor_b32 v215, 32, v214\n" \
"v_mfma_f32_16x16x32_bf16 v[112:115], a[100:103], a[92:95], v[112:115]\n" \
"v_cvt_pk_bf16_f32 v5, v10, v11\n" \
"s_waitcnt vmcnt(20) lgkmcnt(0)\n" \
"s_barrier\n" \
"v_mfma_f32_16x16x32_bf16 v[72:75], a[112:115], a[64:67], 0\n" \
"s_add_u32 m0, m0, 0x4000\n" \
"ds_read_b64_tr_b16 a[96:97], v214 offset:16384\n" \
"ds_read_b64_tr_b16 a[98:99], v214 offset:18432\n" \
"v_mfma_f32_16x16x32_bf16 v[72:75], a[116:119], a[68:71], v[72:75]\n" \
"buffer_load_dwordx4 v216, s[36:39], s82 offen offset:0 lds\n" \
"v_mfma_f32_16x16x32_bf16 v[76:79], a[120:123], a[64:67], 0\n" \
"ds_read_b64_tr_b16 a[100:101], v214 offset:24576\n" \
"ds_read_b64_tr_b16 a[102:103], v214 offset:26624\n" \
"v_mfma_f32_16x16x32_bf16 v[76:79], a[124:127], a[68:71], v[76:79]\n" \
"ds_read_b128 a[64:67], v213 offset:0\n" \
"v_cvt_pk_bf16_f32 v6, v12, v13\n" \
"v_cvt_pk_bf16_f32 v7, v14, v15\n" \
"v_mfma_f32_16x16x32_bf16 v[92:95], a[120:123], a[72:75], 0\n" \
"s_add_u32 s48, s48, 0x1000\n" \
"ds_read_b128 a[68:71], v213 offset:2048\n" \
"v_cvt_pk_bf16_f32 v8, v16, v17\n" \
"v_mfma_f32_16x16x32_bf16 v[92:95], a[124:127], a[76:79], v[92:95]\n" \
"buffer_load_dwordx4 v217, s[36:39], s82 offen offset:1024 lds\n" \
"v_mfma_f32_16x16x32_bf16 v[88:91], a[112:115], a[72:75], 0\n" \
"ds_read_b64_tr_b16 a[104:105], v215 offset:16384\n" \
"ds_read_b64_tr_b16 a[106:107], v215 offset:18432\n" \
"v_mfma_f32_16x16x32_bf16 v[88:91], a[116:119], a[76:79], v[88:91]\n" \
"ds_read_b128 a[72:75], v213 offset:256\n" \
"v_cvt_pk_bf16_f32 v9, v18, v19\n" \
"v_cvt_pk_bf16_f32 v10, v20, v21\n" \
"v_mfma_f32_16x16x32_bf16 v[104:107], a[112:115], a[80:83], 0\n" \
"s_sub_u32 s86, s86, 32768\n" \
"s_cselect_b32 s86, 131072, s86\n" \
"ds_read_b128 a[76:79], v213 offset:2304\n" \
"v_mfma_f32_16x16x32_bf16 v[104:107], a[116:119], a[84:87], v[104:107]\n" \
"buffer_load_dwordx4 v218, s[36:39], s82 offen offset:2048 lds\n" \
"v_cvt_pk_bf16_f32 v11, v22, v23\n" \
"v_mfma_f32_16x16x32_bf16 v[108:111], a[120:123], a[80:83], 0\n" \
"ds_read_b64_tr_b16 a[108:109], v215 offset:24576\n" \
"ds_read_b64_tr_b16 a[110:111], v215 offset:26624\n" \
"v_mfma_f32_16x16x32_bf16 v[108:111], a[124:127], a[84:87], v[108:111]\n" \
"buffer_load_dwordx4 v219, s[36:39], s82 offen offset:3072 lds\n" \
"v_mfma_f32_16x16x32_bf16 v[124:127], a[120:123], a[88:91], 0\n" \
"s_waitcnt lgkmcnt(3)\n" \
"ds_read_b128 a[80:83], v213 offset:4096\n" \
"v_mfma_f32_16x16x32_bf16 v[124:127], a[124:127], a[92:95], v[124:127]\n" \
"s_add_u32 s71, s87, 0\n" \
"ds_read_b128 a[84:87], v213 offset:6144\n" \
"v_cvt_pk_bf16_f32 v12, v24, v25\n" \
"v_mfma_f32_16x16x32_bf16 v[120:123], a[112:115], a[88:91], 0\n" \
"buffer_store_dwordx4 v[0:3], v234, s[56:59], s71 offen offset:0 sc1\n" \
"v_mfma_f32_16x16x32_bf16 v[120:123], a[116:119], a[92:95], v[120:123]\n" \
"buffer_store_dwordx4 v[4:7], v234, s[56:59], s71 offen offset:1024 sc1\n" \
"buffer_store_dwordx4 v[8:11], v234, s[56:59], s71 offen offset:2048 sc1\n" \
"v_mfma_f32_16x16x32_bf16 v[64:67], a[96:99], a[64:67], v[64:67]\n" \
"s_waitcnt lgkmcnt(2)\n" \
"ds_read_b128 a[88:91], v213 offset:4352\n" \
"v_cvt_pk_bf16_f32 v13, v26, v27\n" \
"v_mfma_f32_16x16x32_bf16 v[64:67], a[100:103], a[68:71], v[64:67]\n" \
"s_xor_b32 s82, s48, s49\n" \
"ds_read_b128 a[92:95], v213 offset:6400\n" \
"v_cvt_pk_bf16_f32 v14, v28, v29\n" \
"v_mfma_f32_16x16x32_bf16 v[68:71], a[104:107], a[64:67], v[68:71]\n" \
"s_cmp_lt_u32 s48, s50\n" \
"s_cselect_b32 s82, s82, s48\n" \
"ds_read_b64_tr_b16 a[112:113], v214 offset:16896\n" \
"v_mfma_f32_16x16x32_bf16 v[68:71], a[108:111], a[68:71], v[68:71]\n" \
"s_add_u32 s81, s82, s46\n" \
"ds_read_b64_tr_b16 a[114:115], v214 offset:18944\n" \
"v_cvt_pk_bf16_f32 v15, v30, v31\n" \
"v_mfma_f32_16x16x32_bf16 v[84:87], a[104:107], a[72:75], v[84:87]\n" \
"s_add_u32 m0, s86, s79\n" \
"ds_read_b64_tr_b16 a[116:117], v214 offset:25088\n" \
"v_cvt_pk_bf16_f32 v16, v32, v33\n" \
"v_mfma_f32_16x16x32_bf16 v[84:87], a[108:111], a[76:79], v[84:87]\n" \
"buffer_load_dwordx4 v234, s[56:59], s81 offen offset:0 sc1 lds\n" \
"v_mfma_f32_16x16x32_bf16 v[80:83], a[96:99], a[72:75], v[80:83]\n" \
"s_sub_u32 s85, s85, 32768\n" \
"s_cselect_b32 s85, 131072, s85\n" \
"ds_read_b64_tr_b16 a[118:119], v214 offset:27136\n" \
"v_mfma_f32_16x16x32_bf16 v[80:83], a[100:103], a[76:79], v[80:83]\n" \
"s_waitcnt lgkmcnt(3)\n" \
"s_lshl_b32 s82, s82, 5\n" \
"ds_read_b64_tr_b16 a[120:121], v215 offset:16896\n" \
"v_cvt_pk_bf16_f32 v17, v34, v35\n" \
"v_mfma_f32_16x16x32_bf16 v[96:99], a[96:99], a[80:83], v[96:99]\n" \
"s_add_u32 s82, s82, s47\n" \
"ds_read_b64_tr_b16 a[122:123], v215 offset:18944\n" \
"v_cvt_pk_bf16_f32 v18, v36, v37\n" \
"v_mfma_f32_16x16x32_bf16 v[96:99], a[100:103], a[84:87], v[96:99]\n" \
"buffer_load_dwordx4 v234, s[56:59], s81 offen offset:1024 sc1 lds\n" \
"v_mfma_f32_16x16x32_bf16 v[100:103], a[104:107], a[80:83], v[100:103]\n" \
"ds_read_b64_tr_b16 a[124:125], v215 offset:25088\n" \
"v_add_u32 v213, s85, v211\n" \
"v_mfma_f32_16x16x32_bf16 v[100:103], a[108:111], a[84:87], v[100:103]\n" \
"buffer_load_dwordx4 v234, s[56:59], s81 offen offset:2048 sc1 lds\n" \
"v_mfma_f32_16x16x32_bf16 v[116:119], a[104:107], a[88:91], v[116:119]\n" \
"ds_read_b64_tr_b16 a[126:127], v215 offset:27136\n" \
"v_cvt_pk_bf16_f32 v19, v38, v39\n" \
"v_mfma_f32_16x16x32_bf16 v[116:119], a[108:111], a[92:95], v[116:119]\n" \
"buffer_load_dwordx4 v234, s[56:59], s81 offen offset:3072 sc1 lds\n" \
"v_cvt_pk_bf16_f32 v20, v40, v41\n" \
"v_mfma_f32_16x16x32_bf16 v[112:115], a[96:99], a[88:91], v[112:115]\n" \
"v_add_u32 v214, s85, v212\n" \
"v_xor_b32 v215, 32, v214\n" \
"v_mfma_f32_16x16x32_bf16 v[112:115], a[100:103], a[92:95], v[112:115]\n" \
"s_waitcnt vmcnt(23) lgkmcnt(0)\n" \
"s_barrier\n" \
"v_mfma_f32_16x16x32_bf16 v[72:75], a[112:115], a[64:67], v[72:75]\n" \
"s_add_u32 m0, m0, 0x4000\n" \
"ds_read_b64_tr_b16 a[96:97], v214 offset:16384\n" \
"ds_read_b64_tr_b16 a[98:99], v214 offset:18432\n" \
"v_mfma_f32_16x16x32_bf16 v[72:75], a[116:119], a[68:71], v[72:75]\n" \
"buffer_load_dwordx4 v216, s[36:39], s82 offen offset:0 lds\n" \
"v_mfma_f32_16x16x32_bf16 v[76:79], a[120:123], a[64:67], v[76:79]\n" \
"ds_read_b64_tr_b16 a[100:101], v214 offset:24576\n" \
"ds_read_b64_tr_b16 a[102:103], v214 offset:26624\n" \
"v_mfma_f32_16x16x32_bf16 v[76:79], a[124:127], a[68:71], v[76:79]\n" \
"ds_read_b128 a[64:67], v213 offset:0\n" \
"v_cvt_pk_bf16_f32 v21, v42, v43\n" \
"v_mfma_f32_16x16x32_bf16 v[92:95], a[120:123], a[72:75], v[92:95]\n" \
"s_add_u32 s48, s48, 0x1000\n" \
"ds_read_b128 a[68:71], v213 offset:2048\n" \
"v_cvt_pk_bf16_f32 v22, v44, v45\n" \
"v_mfma_f32_16x16x32_bf16 v[92:95], a[124:127], a[76:79], v[92:95]\n" \
"buffer_load_dwordx4 v217, s[36:39], s82 offen offset:1024 lds\n" \
"v_mfma_f32_16x16x32_bf16 v[88:91], a[112:115], a[72:75], v[88:91]\n" \
"ds_read_b64_tr_b16 a[104:105], v215 offset:16384\n" \
"ds_read_b64_tr_b16 a[106:107], v215 offset:18432\n" \
"v_mfma_f32_16x16x32_bf16 v[88:91], a[116:119], a[76:79], v[88:91]\n" \
"ds_read_b128 a[72:75], v213 offset:256\n" \
"v_cvt_pk_bf16_f32 v23, v46, v47\n" \
"v_mfma_f32_16x16x32_bf16 v[104:107], a[112:115], a[80:83], v[104:107]\n" \
"s_sub_u32 s86, s86, 32768\n" \
"s_cselect_b32 s86, 131072, s86\n" \
"ds_read_b128 a[76:79], v213 offset:2304\n" \
"v_mfma_f32_16x16x32_bf16 v[104:107], a[116:119], a[84:87], v[104:107]\n" \
"buffer_load_dwordx4 v218, s[36:39], s82 offen offset:2048 lds\n" \
"v_mfma_f32_16x16x32_bf16 v[108:111], a[120:123], a[80:83], v[108:111]\n" \
"ds_read_b64_tr_b16 a[108:109], v215 offset:24576\n" \
"ds_read_b64_tr_b16 a[110:111], v215 offset:26624\n" \
"v_mfma_f32_16x16x32_bf16 v[108:111], a[124:127], a[84:87], v[108:111]\n" \
"buffer_load_dwordx4 v219, s[36:39], s82 offen offset:3072 lds\n" \
"v_mfma_f32_16x16x32_bf16 v[124:127], a[120:123], a[88:91], v[124:127]\n" \
"ds_read_b128 a[80:83], v213 offset:4096\n" \
"s_waitcnt lgkmcnt(4)\n" \
"v_mfma_f32_16x16x32_bf16 v[124:127], a[124:127], a[92:95], v[124:127]\n" \
"ds_read_b128 a[84:87], v213 offset:6144\n" \
"v_cvt_pk_bf16_f32 v24, v48, v49\n" \
"v_mfma_f32_16x16x32_bf16 v[120:123], a[112:115], a[88:91], v[120:123]\n" \
"buffer_store_dwordx4 v[12:15], v234, s[56:59], s71 offen offset:3072 sc1\n" \
"v_mfma_f32_16x16x32_bf16 v[120:123], a[116:119], a[92:95], v[120:123]\n" \
"s_add_u32 s71, s87, 4096\n" \
"buffer_store_dwordx4 v[16:19], v234, s[56:59], s71 offen offset:0 sc1\n" \
"buffer_store_dwordx4 v[20:23], v234, s[56:59], s71 offen offset:1024 sc1\n" \
"v_mfma_f32_16x16x32_bf16 v[64:67], a[96:99], a[64:67], v[64:67]\n" \
"s_waitcnt lgkmcnt(2)\n" \
"ds_read_b128 a[88:91], v213 offset:4352\n" \
"v_cvt_pk_bf16_f32 v25, v50, v51\n" \
"v_mfma_f32_16x16x32_bf16 v[64:67], a[100:103], a[68:71], v[64:67]\n" \
"s_xor_b32 s82, s48, s49\n" \
"ds_read_b128 a[92:95], v213 offset:6400\n" \
"v_cvt_pk_bf16_f32 v26, v52, v53\n" \
"v_mfma_f32_16x16x32_bf16 v[68:71], a[104:107], a[64:67], v[68:71]\n" \
"s_cmp_lt_u32 s48, s50\n" \
"s_cselect_b32 s82, s82, s48\n" \
"ds_read_b64_tr_b16 a[112:113], v214 offset:16896\n" \
"v_mfma_f32_16x16x32_bf16 v[68:71], a[108:111], a[68:71], v[68:71]\n" \
"s_add_u32 s81, s82, s46\n" \
"ds_read_b64_tr_b16 a[114:115], v214 offset:18944\n" \
"v_cvt_pk_bf16_f32 v27, v54, v55\n" \
"v_mfma_f32_16x16x32_bf16 v[84:87], a[104:107], a[72:75], v[84:87]\n" \
"s_add_u32 m0, s86, s79\n" \
"ds_read_b64_tr_b16 a[116:117], v214 offset:25088\n" \
"v_cvt_pk_bf16_f32 v28, v56, v57\n" \
"v_mfma_f32_16x16x32_bf16 v[84:87], a[108:111], a[76:79], v[84:87]\n" \
"buffer_load_dwordx4 v234, s[56:59], s81 offen offset:0 sc1 lds\n" \
"v_mfma_f32_16x16x32_bf16 v[80:83], a[96:99], a[72:75], v[80:83]\n" \
"s_sub_u32 s85, s85, 32768\n" \
"s_cselect_b32 s85, 131072, s85\n" \
"ds_read_b64_tr_b16 a[118:119], v214 offset:27136\n" \
"v_mfma_f32_16x16x32_bf16 v[80:83], a[100:103], a[76:79], v[80:83]\n" \
"s_waitcnt lgkmcnt(3)\n" \
"s_lshl_b32 s82, s82, 5\n" \
"ds_read_b64_tr_b16 a[120:121], v215 offset:16896\n" \
"v_cvt_pk_bf16_f32 v29, v58, v59\n" \
"v_mfma_f32_16x16x32_bf16 v[96:99], a[96:99], a[80:83], v[96:99]\n" \
"s_add_u32 s82, s82, s47\n" \
"ds_read_b64_tr_b16 a[122:123], v215 offset:18944\n" \
"v_cvt_pk_bf16_f32 v30, v60, v61\n" \
"v_mfma_f32_16x16x32_bf16 v[96:99], a[100:103], a[84:87], v[96:99]\n" \
"buffer_load_dwordx4 v234, s[56:59], s81 offen offset:1024 sc1 lds\n" \
"v_mfma_f32_16x16x32_bf16 v[100:103], a[104:107], a[80:83], v[100:103]\n" \
"ds_read_b64_tr_b16 a[124:125], v215 offset:25088\n" \
"v_add_u32 v213, s85, v211\n" \
"v_mfma_f32_16x16x32_bf16 v[100:103], a[108:111], a[84:87], v[100:103]\n" \
"buffer_load_dwordx4 v234, s[56:59], s81 offen offset:2048 sc1 lds\n" \
"v_mfma_f32_16x16x32_bf16 v[116:119], a[104:107], a[88:91], v[116:119]\n" \
"ds_read_b64_tr_b16 a[126:127], v215 offset:27136\n" \
"v_cvt_pk_bf16_f32 v31, v62, v63\n" \
"v_mfma_f32_16x16x32_bf16 v[116:119], a[108:111], a[92:95], v[116:119]\n" \
"buffer_load_dwordx4 v234, s[56:59], s81 offen offset:3072 sc1 lds\n" \
"v_mfma_f32_16x16x32_bf16 v[112:115], a[96:99], a[88:91], v[112:115]\n" \
"v_add_u32 v214, s85, v212\n" \
"v_xor_b32 v215, 32, v214\n" \
"v_mfma_f32_16x16x32_bf16 v[112:115], a[100:103], a[92:95], v[112:115]\n" \
"s_waitcnt vmcnt(26) lgkmcnt(0)\n" \
"s_barrier\n" \
"v_mfma_f32_16x16x32_bf16 v[72:75], a[112:115], a[64:67], v[72:75]\n" \
"s_add_u32 m0, m0, 0x4000\n" \
"ds_read_b64_tr_b16 a[96:97], v214 offset:16384\n" \
"ds_read_b64_tr_b16 a[98:99], v214 offset:18432\n" \
"v_mfma_f32_16x16x32_bf16 v[72:75], a[116:119], a[68:71], v[72:75]\n" \
"buffer_load_dwordx4 v216, s[36:39], s82 offen offset:0 lds\n" \
"v_mfma_f32_16x16x32_bf16 v[76:79], a[120:123], a[64:67], v[76:79]\n" \
"ds_read_b64_tr_b16 a[100:101], v214 offset:24576\n" \
"ds_read_b64_tr_b16 a[102:103], v214 offset:26624\n" \
"v_mfma_f32_16x16x32_bf16 v[76:79], a[124:127], a[68:71], v[76:79]\n" \
"ds_read_b128 a[64:67], v213 offset:0\n" \
"v_mfma_f32_16x16x32_bf16 v[92:95], a[120:123], a[72:75], v[92:95]\n" \
"s_add_u32 s48, s48, 0x1000\n" \
"ds_read_b128 a[68:71], v213 offset:2048\n" \
"v_mfma_f32_16x16x32_bf16 v[92:95], a[124:127], a[76:79], v[92:95]\n" \
"buffer_load_dwordx4 v217, s[36:39], s82 offen offset:1024 lds\n" \
"v_mfma_f32_16x16x32_bf16 v[88:91], a[112:115], a[72:75], v[88:91]\n" \
"ds_read_b64_tr_b16 a[104:105], v215 offset:16384\n" \
"ds_read_b64_tr_b16 a[106:107], v215 offset:18432\n" \
"v_mfma_f32_16x16x32_bf16 v[88:91], a[116:119], a[76:79], v[88:91]\n" \
"ds_read_b128 a[72:75], v213 offset:256\n" \
"v_mfma_f32_16x16x32_bf16 v[104:107], a[112:115], a[80:83], v[104:107]\n" \
"s_sub_u32 s86, s86, 32768\n" \
"s_cselect_b32 s86, 131072, s86\n" \
"ds_read_b128 a[76:79], v213 offset:2304\n" \
"v_mfma_f32_16x16x32_bf16 v[104:107], a[116:119], a[84:87], v[104:107]\n" \
"buffer_load_dwordx4 v218, s[36:39], s82 offen offset:2048 lds\n" \
"v_mfma_f32_16x16x32_bf16 v[108:111], a[120:123], a[80:83], v[108:111]\n" \
"ds_read_b64_tr_b16 a[108:109], v215 offset:24576\n" \
"ds_read_b64_tr_b16 a[110:111], v215 offset:26624\n" \
"v_mfma_f32_16x16x32_bf16 v[108:111], a[124:127], a[84:87], v[108:111]\n" \
"buffer_load_dwordx4 v219, s[36:39], s82 offen offset:3072 lds\n" \
"v_mfma_f32_16x16x32_bf16 v[124:127], a[120:123], a[88:91], v[124:127]\n" \
"ds_read_b128 a[80:83], v213 offset:4096\n" \
"s_waitcnt lgkmcnt(4)\n" \
"v_mfma_f32_16x16x32_bf16 v[124:127], a[124:127], a[92:95], v[124:127]\n" \
"ds_read_b128 a[84:87], v213 offset:6144\n" \
"v_mfma_f32_16x16x32_bf16 v[120:123], a[112:115], a[88:91], v[120:123]\n" \
"buffer_store_dwordx4 v[24:27], v234, s[56:59], s71 offen offset:2048 sc1\n" \
"v_mfma_f32_16x16x32_bf16 v[120:123], a[116:119], a[92:95], v[120:123]\n" \
"buffer_store_dwordx4 v[28:31], v234, s[56:59], s71 offen offset:3072 sc1\n" \
"v_mfma_f32_16x16x32_bf16 v[64:67], a[96:99], a[64:67], v[64:67]\n" \
"ds_read_b128 a[88:91], v213 offset:4352\n" \
"v_mfma_f32_16x16x32_bf16 v[64:67], a[100:103], a[68:71], v[64:67]\n" \
"s_waitcnt lgkmcnt(2)\n" \
"s_xor_b32 s82, s48, s49\n" \
"ds_read_b128 a[92:95], v213 offset:6400\n" \
"v_mfma_f32_16x16x32_bf16 v[68:71], a[104:107], a[64:67], v[68:71]\n" \
"s_cmp_lt_u32 s48, s50\n" \
"s_cselect_b32 s82, s82, s48\n" \
"ds_read_b64_tr_b16 a[112:113], v214 offset:16896\n" \
"v_mfma_f32_16x16x32_bf16 v[68:71], a[108:111], a[68:71], v[68:71]\n" \
"s_add_u32 s81, s82, s46\n" \
"ds_read_b64_tr_b16 a[114:115], v214 offset:18944\n" \
"v_mfma_f32_16x16x32_bf16 v[84:87], a[104:107], a[72:75], v[84:87]\n" \
"s_add_u32 m0, s86, s79\n" \
"ds_read_b64_tr_b16 a[116:117], v214 offset:25088\n" \
"v_mfma_f32_16x16x32_bf16 v[84:87], a[108:111], a[76:79], v[84:87]\n" \
"buffer_load_dwordx4 v234, s[56:59], s81 offen offset:0 sc1 lds\n" \
"v_mfma_f32_16x16x32_bf16 v[80:83], a[96:99], a[72:75], v[80:83]\n" \
"s_sub_u32 s85, s85, 32768\n" \
"s_cselect_b32 s85, 131072, s85\n" \
"ds_read_b64_tr_b16 a[118:119], v214 offset:27136\n" \
"v_mfma_f32_16x16x32_bf16 v[80:83], a[100:103], a[76:79], v[80:83]\n" \
"s_waitcnt lgkmcnt(3)\n" \
"s_lshl_b32 s82, s82, 5\n" \
"ds_read_b64_tr_b16 a[120:121], v215 offset:16896\n" \
"v_mfma_f32_16x16x32_bf16 v[96:99], a[96:99], a[80:83], v[96:99]\n" \
"s_add_u32 s82, s82, s47\n" \
"ds_read_b64_tr_b16 a[122:123], v215 offset:18944\n" \
"v_mfma_f32_16x16x32_bf16 v[96:99], a[100:103], a[84:87], v[96:99]\n" \
"buffer_load_dwordx4 v234, s[56:59], s81 offen offset:1024 sc1 lds\n" \
"v_mfma_f32_16x16x32_bf16 v[100:103], a[104:107], a[80:83], v[100:103]\n" \
"ds_read_b64_tr_b16 a[124:125], v215 offset:25088\n" \
"v_add_u32 v213, s85, v211\n" \
"v_mfma_f32_16x16x32_bf16 v[100:103], a[108:111], a[84:87], v[100:103]\n" \
"buffer_load_dwordx4 v234, s[56:59], s81 offen offset:2048 sc1 lds\n" \
"v_mfma_f32_16x16x32_bf16 v[116:119], a[104:107], a[88:91], v[116:119]\n" \
"ds_read_b64_tr_b16 a[126:127], v215 offset:27136\n" \
"v_mfma_f32_16x16x32_bf16 v[116:119], a[108:111], a[92:95], v[116:119]\n" \
"buffer_load_dwordx4 v234, s[56:59], s81 offen offset:3072 sc1 lds\n" \
"v_mfma_f32_16x16x32_bf16 v[112:115], a[96:99], a[88:91], v[112:115]\n" \
"v_add_u32 v214, s85, v212\n" \
"v_xor_b32 v215, 32, v214\n" \
"v_mfma_f32_16x16x32_bf16 v[112:115], a[100:103], a[92:95], v[112:115]\n" \
"s_waitcnt vmcnt(28) lgkmcnt(0)\n" \
"s_barrier\n" \
"v_mfma_f32_16x16x32_bf16 v[72:75], a[112:115], a[64:67], v[72:75]\n" \
"s_add_u32 m0, m0, 0x4000\n" \
"ds_read_b64_tr_b16 a[96:97], v214 offset:16384\n" \
"ds_read_b64_tr_b16 a[98:99], v214 offset:18432\n" \
"v_mfma_f32_16x16x32_bf16 v[72:75], a[116:119], a[68:71], v[72:75]\n" \
"buffer_load_dwordx4 v216, s[36:39], s82 offen offset:0 lds\n" \
"v_mfma_f32_16x16x32_bf16 v[76:79], a[120:123], a[64:67], v[76:79]\n" \
"ds_read_b64_tr_b16 a[100:101], v214 offset:24576\n" \
"ds_read_b64_tr_b16 a[102:103], v214 offset:26624\n" \
"v_mfma_f32_16x16x32_bf16 v[76:79], a[124:127], a[68:71], v[76:79]\n" \
"ds_read_b128 a[64:67], v213 offset:0\n" \
"v_mfma_f32_16x16x32_bf16 v[92:95], a[120:123], a[72:75], v[92:95]\n" \
"s_add_u32 s48, s48, 0x1000\n" \
"ds_read_b128 a[68:71], v213 offset:2048\n" \
"v_mfma_f32_16x16x32_bf16 v[92:95], a[124:127], a[76:79], v[92:95]\n" \
"buffer_load_dwordx4 v217, s[36:39], s82 offen offset:1024 lds\n" \
"v_mfma_f32_16x16x32_bf16 v[88:91], a[112:115], a[72:75], v[88:91]\n" \
"ds_read_b64_tr_b16 a[104:105], v215 offset:16384\n" \
"ds_read_b64_tr_b16 a[106:107], v215 offset:18432\n" \
"v_mfma_f32_16x16x32_bf16 v[88:91], a[116:119], a[76:79], v[88:91]\n" \
"ds_read_b128 a[72:75], v213 offset:256\n" \
"v_mfma_f32_16x16x32_bf16 v[104:107], a[112:115], a[80:83], v[104:107]\n" \
"s_sub_u32 s86, s86, 32768\n" \
"s_cselect_b32 s86, 131072, s86\n" \
"ds_read_b128 a[76:79], v213 offset:2304\n" \
"v_mfma_f32_16x16x32_bf16 v[104:107], a[116:119], a[84:87], v[104:107]\n" \
"buffer_load_dwordx4 v218, s[36:39], s82 offen offset:2048 lds\n" \
"v_mfma_f32_16x16x32_bf16 v[108:111], a[120:123], a[80:83], v[108:111]\n" \
"ds_read_b64_tr_b16 a[108:109], v215 offset:24576\n" \
"ds_read_b64_tr_b16 a[110:111], v215 offset:26624\n" \
"v_mfma_f32_16x16x32_bf16 v[108:111], a[124:127], a[84:87], v[108:111]\n" \
"buffer_load_dwordx4 v219, s[36:39], s82 offen offset:3072 lds\n" \
"v_mfma_f32_16x16x32_bf16 v[124:127], a[120:123], a[88:91], v[124:127]\n" \
"ds_read_b128 a[80:83], v213 offset:4096\n" \
"s_waitcnt lgkmcnt(4)\n" \
"v_mfma_f32_16x16x32_bf16 v[124:127], a[124:127], a[92:95], v[124:127]\n" \
"ds_read_b128 a[84:87], v213 offset:6144\n" \
"v_mfma_f32_16x16x32_bf16 v[120:123], a[112:115], a[88:91], v[120:123]\n" \
"v_mfma_f32_16x16x32_bf16 v[120:123], a[116:119], a[92:95], v[120:123]\n" \
"v_mfma_f32_16x16x32_bf16 v[64:67], a[96:99], a[64:67], v[64:67]\n" \
"ds_read_b128 a[88:91], v213 offset:4352\n" \
"v_mfma_f32_16x16x32_bf16 v[64:67], a[100:103], a[68:71], v[64:67]\n" \
"s_waitcnt lgkmcnt(2)\n" \
"s_xor_b32 s82, s48, s49\n" \
"ds_read_b128 a[92:95], v213 offset:6400\n" \
"v_mfma_f32_16x16x32_bf16 v[68:71], a[104:107], a[64:67], v[68:71]\n" \
"s_cmp_lt_u32 s48, s50\n" \
"s_cselect_b32 s82, s82, s48\n" \
"ds_read_b64_tr_b16 a[112:113], v214 offset:16896\n" \
"v_mfma_f32_16x16x32_bf16 v[68:71], a[108:111], a[68:71], v[68:71]\n" \
"s_add_u32 s81, s82, s46\n" \
"ds_read_b64_tr_b16 a[114:115], v214 offset:18944\n" \
"v_mfma_f32_16x16x32_bf16 v[84:87], a[104:107], a[72:75], v[84:87]\n" \
"s_add_u32 m0, s86, s79\n" \
"ds_read_b64_tr_b16 a[116:117], v214 offset:25088\n" \
"v_mfma_f32_16x16x32_bf16 v[84:87], a[108:111], a[76:79], v[84:87]\n" \
"buffer_load_dwordx4 v234, s[56:59], s81 offen offset:0 sc1 lds\n" \
"v_mfma_f32_16x16x32_bf16 v[80:83], a[96:99], a[72:75], v[80:83]\n" \
"s_sub_u32 s85, s85, 32768\n" \
"s_cselect_b32 s85, 131072, s85\n" \
"ds_read_b64_tr_b16 a[118:119], v214 offset:27136\n" \
"v_mfma_f32_16x16x32_bf16 v[80:83], a[100:103], a[76:79], v[80:83]\n" \
"s_waitcnt lgkmcnt(3)\n" \
"s_lshl_b32 s82, s82, 5\n" \
"ds_read_b64_tr_b16 a[120:121], v215 offset:16896\n" \
"v_mfma_f32_16x16x32_bf16 v[96:99], a[96:99], a[80:83], v[96:99]\n" \
"s_add_u32 s82, s82, s47\n" \
"ds_read_b64_tr_b16 a[122:123], v215 offset:18944\n" \
"v_mfma_f32_16x16x32_bf16 v[96:99], a[100:103], a[84:87], v[96:99]\n" \
"buffer_load_dwordx4 v234, s[56:59], s81 offen offset:1024 sc1 lds\n" \
"v_mfma_f32_16x16x32_bf16 v[100:103], a[104:107], a[80:83], v[100:103]\n" \
"ds_read_b64_tr_b16 a[124:125], v215 offset:25088\n" \
"v_add_u32 v213, s85, v211\n" \
"v_mfma_f32_16x16x32_bf16 v[100:103], a[108:111], a[84:87], v[100:103]\n" \
"buffer_load_dwordx4 v234, s[56:59], s81 offen offset:2048 sc1 lds\n" \
"v_mfma_f32_16x16x32_bf16 v[116:119], a[104:107], a[88:91], v[116:119]\n" \
"ds_read_b64_tr_b16 a[126:127], v215 offset:27136\n" \
"v_mfma_f32_16x16x32_bf16 v[116:119], a[108:111], a[92:95], v[116:119]\n" \
"buffer_load_dwordx4 v234, s[56:59], s81 offen offset:3072 sc1 lds\n" \
"v_mfma_f32_16x16x32_bf16 v[112:115], a[96:99], a[88:91], v[112:115]\n" \
"v_add_u32 v214, s85, v212\n" \
"v_xor_b32 v215, 32, v214\n" \
"v_mfma_f32_16x16x32_bf16 v[112:115], a[100:103], a[92:95], v[112:115]\n" \
"s_waitcnt vmcnt(25) lgkmcnt(0)\n" \
"s_barrier\n" \
"v_mfma_f32_16x16x32_bf16 v[72:75], a[112:115], a[64:67], v[72:75]\n" \
"s_add_u32 m0, m0, 0x4000\n" \
"ds_read_b64_tr_b16 a[96:97], v214 offset:16384\n" \
"ds_read_b64_tr_b16 a[98:99], v214 offset:18432\n" \
"v_mfma_f32_16x16x32_bf16 v[72:75], a[116:119], a[68:71], v[72:75]\n" \
"buffer_load_dwordx4 v216, s[36:39], s82 offen offset:0 lds\n" \
"v_mfma_f32_16x16x32_bf16 v[76:79], a[120:123], a[64:67], v[76:79]\n" \
"ds_read_b64_tr_b16 a[100:101], v214 offset:24576\n" \
"ds_read_b64_tr_b16 a[102:103], v214 offset:26624\n" \
"v_mfma_f32_16x16x32_bf16 v[76:79], a[124:127], a[68:71], v[76:79]\n" \
"ds_read_b128 a[64:67], v213 offset:0\n" \
"v_mfma_f32_16x16x32_bf16 v[92:95], a[120:123], a[72:75], v[92:95]\n" \
"s_add_u32 s48, s48, 0x1000\n" \
"ds_read_b128 a[68:71], v213 offset:2048\n" \
"v_mfma_f32_16x16x32_bf16 v[92:95], a[124:127], a[76:79], v[92:95]\n" \
"buffer_load_dwordx4 v217, s[36:39], s82 offen offset:1024 lds\n" \
"v_mfma_f32_16x16x32_bf16 v[88:91], a[112:115], a[72:75], v[88:91]\n" \
"ds_read_b64_tr_b16 a[104:105], v215 offset:16384\n" \
"ds_read_b64_tr_b16 a[106:107], v215 offset:18432\n" \
"v_mfma_f32_16x16x32_bf16 v[88:91], a[116:119], a[76:79], v[88:91]\n" \
"ds_read_b128 a[72:75], v213 offset:256\n" \
"v_mfma_f32_16x16x32_bf16 v[104:107], a[112:115], a[80:83], v[104:107]\n" \
"s_sub_u32 s86, s86, 32768\n" \
"s_cselect_b32 s86, 131072, s86\n" \
"ds_read_b128 a[76:79], v213 offset:2304\n" \
"v_mfma_f32_16x16x32_bf16 v[104:107], a[116:119], a[84:87], v[104:107]\n" \
"buffer_load_dwordx4 v218, s[36:39], s82 offen offset:2048 lds\n" \
"v_mfma_f32_16x16x32_bf16 v[108:111], a[120:123], a[80:83], v[108:111]\n" \
"ds_read_b64_tr_b16 a[108:109], v215 offset:24576\n" \
"ds_read_b64_tr_b16 a[110:111], v215 offset:26624\n" \
"v_mfma_f32_16x16x32_bf16 v[108:111], a[124:127], a[84:87], v[108:111]\n" \
"buffer_load_dwordx4 v219, s[36:39], s82 offen offset:3072 lds\n" \
"v_mfma_f32_16x16x32_bf16 v[124:127], a[120:123], a[88:91], v[124:127]\n" \
"ds_read_b128 a[80:83], v213 offset:4096\n" \
"s_waitcnt lgkmcnt(4)\n" \
"v_mfma_f32_16x16x32_bf16 v[124:127], a[124:127], a[92:95], v[124:127]\n" \
"ds_read_b128 a[84:87], v213 offset:6144\n" \
"v_mfma_f32_16x16x32_bf16 v[120:123], a[112:115], a[88:91], v[120:123]\n" \
"v_mfma_f32_16x16x32_bf16 v[120:123], a[116:119], a[92:95], v[120:123]\n" \
"v_mfma_f32_16x16x32_bf16 v[64:67], a[96:99], a[64:67], v[64:67]\n" \
"ds_read_b128 a[88:91], v213 offset:4352\n" \
"v_mfma_f32_16x16x32_bf16 v[64:67], a[100:103], a[68:71], v[64:67]\n" \
"s_waitcnt lgkmcnt(2)\n" \
"s_xor_b32 s82, s48, s49\n" \
"ds_read_b128 a[92:95], v213 offset:6400\n" \
"v_mfma_f32_16x16x32_bf16 v[68:71], a[104:107], a[64:67], v[68:71]\n" \
"s_cmp_lt_u32 s48, s50\n" \
"s_cselect_b32 s82, s82, s48\n" \
"ds_read_b64_tr_b16 a[112:113], v214 offset:16896\n" \
"v_mfma_f32_16x16x32_bf16 v[68:71], a[108:111], a[68:71], v[68:71]\n" \
"s_add_u32 s81, s82, s46\n" \
"ds_read_b64_tr_b16 a[114:115], v214 offset:18944\n" \
"v_mfma_f32_16x16x32_bf16 v[84:87], a[104:107], a[72:75], v[84:87]\n" \
"s_add_u32 m0, s86, s79\n" \
"ds_read_b64_tr_b16 a[116:117], v214 offset:25088\n" \
"v_mfma_f32_16x16x32_bf16 v[84:87], a[108:111], a[76:79], v[84:87]\n" \
"buffer_load_dwordx4 v234, s[56:59], s81 offen offset:0 sc1 lds\n" \
"v_mfma_f32_16x16x32_bf16 v[80:83], a[96:99], a[72:75], v[80:83]\n" \
"s_sub_u32 s85, s85, 32768\n" \
"s_cselect_b32 s85, 131072, s85\n" \
"ds_read_b64_tr_b16 a[118:119], v214 offset:27136\n" \
"v_mfma_f32_16x16x32_bf16 v[80:83], a[100:103], a[76:79], v[80:83]\n" \
"s_waitcnt lgkmcnt(3)\n" \
"s_lshl_b32 s82, s82, 5\n" \
"ds_read_b64_tr_b16 a[120:121], v215 offset:16896\n" \
"v_mfma_f32_16x16x32_bf16 v[96:99], a[96:99], a[80:83], v[96:99]\n" \
"s_add_u32 s82, s82, s47\n" \
"ds_read_b64_tr_b16 a[122:123], v215 offset:18944\n" \
"v_mfma_f32_16x16x32_bf16 v[96:99], a[100:103], a[84:87], v[96:99]\n" \
"buffer_load_dwordx4 v234, s[56:59], s81 offen offset:1024 sc1 lds\n" \
"v_mfma_f32_16x16x32_bf16 v[100:103], a[104:107], a[80:83], v[100:103]\n" \
"ds_read_b64_tr_b16 a[124:125], v215 offset:25088\n" \
"v_add_u32 v213, s85, v211\n" \
"v_mfma_f32_16x16x32_bf16 v[100:103], a[108:111], a[84:87], v[100:103]\n" \
"buffer_load_dwordx4 v234, s[56:59], s81 offen offset:2048 sc1 lds\n" \
"v_mfma_f32_16x16x32_bf16 v[116:119], a[104:107], a[88:91], v[116:119]\n" \
"ds_read_b64_tr_b16 a[126:127], v215 offset:27136\n" \
"v_mfma_f32_16x16x32_bf16 v[116:119], a[108:111], a[92:95], v[116:119]\n" \
"buffer_load_dwordx4 v234, s[56:59], s81 offen offset:3072 sc1 lds\n" \
"v_mfma_f32_16x16x32_bf16 v[112:115], a[96:99], a[88:91], v[112:115]\n" \
"v_add_u32 v214, s85, v212\n" \
"v_xor_b32 v215, 32, v214\n" \
"v_mfma_f32_16x16x32_bf16 v[112:115], a[100:103], a[92:95], v[112:115]\n" \
"s_waitcnt vmcnt(20) lgkmcnt(0)\n" \
"s_barrier\n" \
"buffer_store_dword v230, v231, s[56:59], s64 offen sc1\n" \
"v_mfma_f32_16x16x32_bf16 v[72:75], a[112:115], a[64:67], v[72:75]\n" \
"s_add_u32 m0, m0, 0x4000\n" \
"ds_read_b64_tr_b16 a[96:97], v214 offset:16384\n" \
"ds_read_b64_tr_b16 a[98:99], v214 offset:18432\n" \
"v_mfma_f32_16x16x32_bf16 v[72:75], a[116:119], a[68:71], v[72:75]\n" \
"buffer_load_dwordx4 v216, s[36:39], s82 offen offset:0 lds\n" \
"v_mfma_f32_16x16x32_bf16 v[76:79], a[120:123], a[64:67], v[76:79]\n" \
"ds_read_b64_tr_b16 a[100:101], v214 offset:24576\n" \
"ds_read_b64_tr_b16 a[102:103], v214 offset:26624\n" \
"v_mfma_f32_16x16x32_bf16 v[76:79], a[124:127], a[68:71], v[76:79]\n" \
"ds_read_b128 a[64:67], v213 offset:0\n" \
"v_mfma_f32_16x16x32_bf16 v[92:95], a[120:123], a[72:75], v[92:95]\n" \
"s_add_u32 s48, s48, 0x1000\n" \
"ds_read_b128 a[68:71], v213 offset:2048\n" \
"v_mfma_f32_16x16x32_bf16 v[92:95], a[124:127], a[76:79], v[92:95]\n" \
"buffer_load_dwordx4 v217, s[36:39], s82 offen offset:1024 lds\n" \
"v_mfma_f32_16x16x32_bf16 v[88:91], a[112:115], a[72:75], v[88:91]\n" \
"ds_read_b64_tr_b16 a[104:105], v215 offset:16384\n" \
"ds_read_b64_tr_b16 a[106:107], v215 offset:18432\n" \
"v_mfma_f32_16x16x32_bf16 v[88:91], a[116:119], a[76:79], v[88:91]\n" \
"ds_read_b128 a[72:75], v213 offset:256\n" \
"v_mfma_f32_16x16x32_bf16 v[104:107], a[112:115], a[80:83], v[104:107]\n" \
"s_sub_u32 s86, s86, 32768\n" \
"s_cselect_b32 s86, 131072, s86\n" \
"ds_read_b128 a[76:79], v213 offset:2304\n" \
"v_mfma_f32_16x16x32_bf16 v[104:107], a[116:119], a[84:87], v[104:107]\n" \
"buffer_load_dwordx4 v218, s[36:39], s82 offen offset:2048 lds\n" \
"v_mfma_f32_16x16x32_bf16 v[108:111], a[120:123], a[80:83], v[108:111]\n" \
"ds_read_b64_tr_b16 a[108:109], v215 offset:24576\n" \
"ds_read_b64_tr_b16 a[110:111], v215 offset:26624\n" \
"v_mfma_f32_16x16x32_bf16 v[108:111], a[124:127], a[84:87], v[108:111]\n" \
"buffer_load_dwordx4 v219, s[36:39], s82 offen offset:3072 lds\n" \
"v_mfma_f32_16x16x32_bf16 v[124:127], a[120:123], a[88:91], v[124:127]\n" \
"ds_read_b128 a[80:83], v213 offset:4096\n" \
"s_waitcnt lgkmcnt(4)\n" \
"v_mfma_f32_16x16x32_bf16 v[124:127], a[124:127], a[92:95], v[124:127]\n" \
"ds_read_b128 a[84:87], v213 offset:6144\n" \
"v_mfma_f32_16x16x32_bf16 v[120:123], a[112:115], a[88:91], v[120:123]\n" \
"v_mfma_f32_16x16x32_bf16 v[120:123], a[116:119], a[92:95], v[120:123]\n" \
"s_mov_b32 s72, 5\n" \
"s_nop 0\n" \
"s_nop 0\n" \
"Lo64_%=:\n" \
"v_mfma_f32_16x16x32_bf16 v[64:67], a[96:99], a[64:67], v[64:67]\n" \
"ds_read_b128 a[88:91], v213 offset:4352\n" \
"v_mfma_f32_16x16x32_bf16 v[64:67], a[100:103], a[68:71], v[64:67]\n" \
"Lpg11445_%=:\n" \
"s_cbranch_execz Lcb1_%=\n" \
"s_waitcnt lgkmcnt(2)\n" \
"s_xor_b32 s82, s48, s49\n" \
"ds_read_b128 a[92:95], v213 offset:6400\n" \
"v_mfma_f32_16x16x32_bf16 v[68:71], a[104:107], a[64:67], v[68:71]\n" \
"s_cmp_lt_u32 s48, s50\n" \
"s_cselect_b32 s82, s82, s48\n" \
"ds_read_b64_tr_b16 a[112:113], v214 offset:16896\n" \
"v_mfma_f32_16x16x32_bf16 v[68:71], a[108:111], a[68:71], v[68:71]\n" \
"s_add_u32 s81, s82, s46\n" \
"ds_read_b64_tr_b16 a[114:115], v214 offset:18944\n" \
"v_mfma_f32_16x16x32_bf16 v[84:87], a[104:107], a[72:75], v[84:87]\n" \
"s_add_u32 m0, s86, s79\n" \
"ds_read_b64_tr_b16 a[116:117], v214 offset:25088\n" \
"v_mfma_f32_16x16x32_bf16 v[84:87], a[108:111], a[76:79], v[84:87]\n" \
"buffer_load_dwordx4 v234, s[56:59], s81 offen offset:0 sc1 lds\n" \
"v_mfma_f32_16x16x32_bf16 v[80:83], a[96:99], a[72:75], v[80:83]\n" \
"s_sub_u32 s85, s85, 32768\n" \
"s_cselect_b32 s85, 131072, s85\n" \
"ds_read_b64_tr_b16 a[118:119], v214 offset:27136\n" \
"v_mfma_f32_16x16x32_bf16 v[80:83], a[100:103], a[76:79], v[80:83]\n" \
"s_waitcnt lgkmcnt(3)\n" \
"s_lshl_b32 s82, s82, 5\n" \
"ds_read_b64_tr_b16 a[120:121], v215 offset:16896\n" \
"v_mfma_f32_16x16x32_bf16 v[96:99], a[96:99], a[80:83], v[96:99]\n" \
"s_add_u32 s82, s82, s47\n" \
"ds_read_b64_tr_b16 a[122:123], v215 offset:18944\n" \
"v_mfma_f32_16x16x32_bf16 v[96:99], a[100:103], a[84:87], v[96:99]\n" \
"buffer_load_dwordx4 v234, s[56:59], s81 offen offset:1024 sc1 lds\n" \
"v_mfma_f32_16x16x32_bf16 v[100:103], a[104:107], a[80:83], v[100:103]\n" \
"ds_read_b64_tr_b16 a[124:125], v215 offset:25088\n" \
"v_add_u32 v213, s85, v211\n" \
"v_mfma_f32_16x16x32_bf16 v[100:103], a[108:111], a[84:87], v[100:103]\n" \
"buffer_load_dwordx4 v234, s[56:59], s81 offen offset:2048 sc1 lds\n" \
"v_mfma_f32_16x16x32_bf16 v[116:119], a[104:107], a[88:91], v[116:119]\n" \
"ds_read_b64_tr_b16 a[126:127], v215 offset:27136\n" \
"v_mfma_f32_16x16x32_bf16 v[116:119], a[108:111], a[92:95], v[116:119]\n" \
"buffer_load_dwordx4 v234, s[56:59], s81 offen offset:3072 sc1 lds\n" \
"v_mfma_f32_16x16x32_bf16 v[112:115], a[96:99], a[88:91], v[112:115]\n" \
"v_add_u32 v214, s85, v212\n" \
"v_xor_b32 v215, 32, v214\n" \
"v_mfma_f32_16x16x32_bf16 v[112:115], a[100:103], a[92:95], v[112:115]\n" \
"s_waitcnt vmcnt(20) lgkmcnt(0)\n" \
"s_barrier\n" \
"v_mfma_f32_16x16x32_bf16 v[72:75], a[112:115], a[64:67], v[72:75]\n" \
"s_add_u32 m0, m0, 0x4000\n" \
"ds_read_b64_tr_b16 a[96:97], v214 offset:16384\n" \
"ds_read_b64_tr_b16 a[98:99], v214 offset:18432\n" \
"v_mfma_f32_16x16x32_bf16 v[72:75], a[116:119], a[68:71], v[72:75]\n" \
"buffer_load_dwordx4 v216, s[36:39], s82 offen offset:0 lds\n" \
"v_mfma_f32_16x16x32_bf16 v[76:79], a[120:123], a[64:67], v[76:79]\n" \
"ds_read_b64_tr_b16 a[100:101], v214 offset:24576\n" \
"ds_read_b64_tr_b16 a[102:103], v214 offset:26624\n" \
"v_mfma_f32_16x16x32_bf16 v[76:79], a[124:127], a[68:71], v[76:79]\n" \
"ds_read_b128 a[64:67], v213 offset:0\n" \
"v_mfma_f32_16x16x32_bf16 v[92:95], a[120:123], a[72:75], v[92:95]\n" \
"s_add_u32 s48, s48, 0x1000\n" \
"ds_read_b128 a[68:71], v213 offset:2048\n" \
"v_mfma_f32_16x16x32_bf16 v[92:95], a[124:127], a[76:79], v[92:95]\n" \
"buffer_load_dwordx4 v217, s[36:39], s82 offen offset:1024 lds\n" \
"v_mfma_f32_16x16x32_bf16 v[88:91], a[112:115], a[72:75], v[88:91]\n" \
"ds_read_b64_tr_b16 a[104:105], v215 offset:16384\n" \
"ds_read_b64_tr_b16 a[106:107], v215 offset:18432\n" \
"v_mfma_f32_16x16x32_bf16 v[88:91], a[116:119], a[76:79], v[88:91]\n" \
"ds_read_b128 a[72:75], v213 offset:256\n" \
"v_mfma_f32_16x16x32_bf16 v[104:107], a[112:115], a[80:83], v[104:107]\n" \
"s_sub_u32 s86, s86, 32768\n" \
"s_cselect_b32 s86, 131072, s86\n" \
"ds_read_b128 a[76:79], v213 offset:2304\n" \
"v_mfma_f32_16x16x32_bf16 v[104:107], a[116:119], a[84:87], v[104:107]\n" \
"buffer_load_dwordx4 v218, s[36:39], s82 offen offset:2048 lds\n" \
"v_mfma_f32_16x16x32_bf16 v[108:111], a[120:123], a[80:83], v[108:111]\n" \
"ds_read_b64_tr_b16 a[108:109], v215 offset:24576\n" \
"ds_read_b64_tr_b16 a[110:111], v215 offset:26624\n" \
"v_mfma_f32_16x16x32_bf16 v[108:111], a[124:127], a[84:87], v[108:111]\n" \
"buffer_load_dwordx4 v219, s[36:39], s82 offen offset:3072 lds\n" \
"v_mfma_f32_16x16x32_bf16 v[124:127], a[120:123], a[88:91], v[124:127]\n" \
"ds_read_b128 a[80:83], v213 offset:4096\n" \
"s_waitcnt lgkmcnt(4)\n" \
"v_mfma_f32_16x16x32_bf16 v[124:127], a[124:127], a[92:95], v[124:127]\n" \
"s_sub_u32 s72, s72, 1\n" \
"ds_read_b128 a[84:87], v213 offset:6144\n" \
"v_mfma_f32_16x16x32_bf16 v[120:123], a[112:115], a[88:91], v[120:123]\n" \
"v_mfma_f32_16x16x32_bf16 v[120:123], a[116:119], a[92:95], v[120:123]\n" \
"s_cbranch_scc0 Lo64_%=\n" \
"v_mfma_f32_16x16x32_bf16 v[64:67], a[96:99], a[64:67], v[64:67]\n" \
"s_waitcnt lgkmcnt(2)\n" \
"ds_read_b128 a[88:91], v213 offset:4352\n" \
"v_mfma_f32_16x16x32_bf16 v[64:67], a[100:103], a[68:71], v[64:67]\n" \
"s_add_u32 s48, s86, 32768\n" \
"s_cmp_eq_u32 s48, 163840\n" \
"s_cselect_b32 s48, 0, s48\n" \
"ds_read_b128 a[92:95], v213 offset:6400\n" \
"v_mfma_f32_16x16x32_bf16 v[68:71], a[104:107], a[64:67], v[68:71]\n" \
"ds_read_b64_tr_b16 a[112:113], v214 offset:16896\n" \
"v_mfma_f32_16x16x32_bf16 v[68:71], a[108:111], a[68:71], v[68:71]\n" \
"s_add_u32 s49, s48, 32768\n" \
"s_cmp_eq_u32 s49, 163840\n" \
"s_cselect_b32 s49, 0, s49\n" \
"ds_read_b64_tr_b16 a[114:115], v214 offset:18944\n" \
"v_mfma_f32_16x16x32_bf16 v[84:87], a[104:107], a[72:75], v[84:87]\n" \
"ds_read_b64_tr_b16 a[116:117], v214 offset:25088\n" \
"v_add_u32 v224, s49, v211\n" \
"v_mfma_f32_16x16x32_bf16 v[84:87], a[108:111], a[76:79], v[84:87]\n" \
"v_add_u32 v225, s49, v212\n" \
"v_xor_b32 v226, 32, v225\n" \
"v_mfma_f32_16x16x32_bf16 v[80:83], a[96:99], a[72:75], v[80:83]\n" \
"s_sub_u32 s85, s85, 32768\n" \
"s_cselect_b32 s85, 131072, s85\n" \
"ds_read_b64_tr_b16 a[118:119], v214 offset:27136\n" \
"v_mfma_f32_16x16x32_bf16 v[80:83], a[100:103], a[76:79], v[80:83]\n" \
"s_waitcnt lgkmcnt(3)\n" \
"ds_read_b64_tr_b16 a[120:121], v215 offset:16896\n" \
"v_add_u32 v227, s48, v211\n" \
"v_mfma_f32_16x16x32_bf16 v[96:99], a[96:99], a[80:83], v[96:99]\n" \
"ds_read_b64_tr_b16 a[122:123], v215 offset:18944\n" \
"v_add_u32 v228, s48, v212\n" \
"v_mfma_f32_16x16x32_bf16 v[96:99], a[100:103], a[84:87], v[96:99]\n" \
"v_xor_b32 v229, 32, v228\n" \
"v_mfma_f32_16x16x32_bf16 v[100:103], a[104:107], a[80:83], v[100:103]\n" \
"ds_read_b64_tr_b16 a[124:125], v215 offset:25088\n" \
"v_add_u32 v213, s85, v211\n" \
"v_mfma_f32_16x16x32_bf16 v[100:103], a[108:111], a[84:87], v[100:103]\n" \
"v_mfma_f32_16x16x32_bf16 v[116:119], a[104:107], a[88:91], v[116:119]\n" \
"ds_read_b64_tr_b16 a[126:127], v215 offset:27136\n" \
"v_mfma_f32_16x16x32_bf16 v[116:119], a[108:111], a[92:95], v[116:119]\n" \
"v_mfma_f32_16x16x32_bf16 v[112:115], a[96:99], a[88:91], v[112:115]\n" \
"v_add_u32 v214, s85, v212\n" \
"v_xor_b32 v215, 32, v214\n" \
"v_mfma_f32_16x16x32_bf16 v[112:115], a[100:103], a[92:95], v[112:115]\n" \
"s_waitcnt vmcnt(8) lgkmcnt(0)\n" \
"s_barrier\n" \
"v_mfma_f32_16x16x32_bf16 v[72:75], a[112:115], a[64:67], v[72:75]\n" \
"ds_read_b128 a[0:3], v213 offset:0\n" \
"ds_read_b128 a[4:7], v213 offset:2048\n" \
"v_mfma_f32_16x16x32_bf16 v[72:75], a[116:119], a[68:71], v[72:75]\n" \
"s_sub_u32 s68, s86, 32768\n" \
"s_cselect_b32 s68, 131072, s68\n" \
"ds_read_b64_tr_b16 a[128:129], v214 offset:16384\n" \
"ds_read_b64_tr_b16 a[130:131], v214 offset:18432\n" \
"v_mfma_f32_16x16x32_bf16 v[76:79], a[120:123], a[64:67], v[76:79]\n" \
"ds_read_b64_tr_b16 a[132:133], v214 offset:24576\n" \
"ds_read_b64_tr_b16 a[134:135], v214 offset:26624\n" \
"v_mfma_f32_16x16x32_bf16 v[76:79], a[124:127], a[68:71], v[76:79]\n" \
"s_cmp_lt_u32 s61, 2\n" \
"s_cselect_b32 s68, s86, s68\n" \
"ds_read_b64_tr_b16 a[136:137], v215 offset:16384\n" \
"ds_read_b64_tr_b16 a[138:139], v215 offset:18432\n" \
"v_mfma_f32_16x16x32_bf16 v[92:95], a[120:123], a[72:75], v[92:95]\n" \
"s_and_b32 s50, s61, 1\n" \
"ds_read_b64_tr_b16 a[140:141], v215 offset:24576\n" \
"ds_read_b64_tr_b16 a[142:143], v215 offset:26624\n" \
"v_mfma_f32_16x16x32_bf16 v[92:95], a[124:127], a[76:79], v[92:95]\n" \
"s_mul_i32 s50, s50, 9216\n" \
"ds_read_b64_tr_b16 a[144:145], v214 offset:16896\n" \
"ds_read_b64_tr_b16 a[146:147], v214 offset:18944\n" \
"v_mfma_f32_16x16x32_bf16 v[88:91], a[112:115], a[72:75], v[88:91]\n" \
"s_add_u32 s68, s68, s50\n" \
"ds_read_b64_tr_b16 a[148:149], v214 offset:25088\n" \
"ds_read_b64_tr_b16 a[150:151], v214 offset:27136\n" \
"v_mfma_f32_16x16x32_bf16 v[88:91], a[116:119], a[76:79], v[88:91]\n" \
"s_add_u32 s68, s68, %3\n" \
"ds_read_b64_tr_b16 a[152:153], v215 offset:16896\n" \
"ds_read_b64_tr_b16 a[154:155], v215 offset:18944\n" \
"v_mfma_f32_16x16x32_bf16 v[104:107], a[112:115], a[80:83], v[104:107]\n" \
"ds_read_b64_tr_b16 a[156:157], v215 offset:25088\n" \
"ds_read_b64_tr_b16 a[158:159], v215 offset:27136\n" \
"v_mfma_f32_16x16x32_bf16 v[104:107], a[116:119], a[84:87], v[104:107]\n" \
"ds_read_b128 a[24:27], v213 offset:256\n" \
"ds_read_b128 a[28:31], v213 offset:2304\n" \
"v_mfma_f32_16x16x32_bf16 v[108:111], a[120:123], a[80:83], v[108:111]\n" \
"ds_read_b128 a[8:11], v224 offset:0\n" \
"ds_read_b128 a[12:15], v224 offset:2048\n" \
"v_mfma_f32_16x16x32_bf16 v[108:111], a[124:127], a[84:87], v[108:111]\n" \
"ds_read_b64_tr_b16 a[160:161], v225 offset:16384\n" \
"ds_read_b64_tr_b16 a[162:163], v225 offset:18432\n" \
"v_mfma_f32_16x16x32_bf16 v[124:127], a[120:123], a[88:91], v[124:127]\n" \
"ds_read_b64_tr_b16 a[164:165], v225 offset:24576\n" \
"ds_read_b64_tr_b16 a[166:167], v225 offset:26624\n" \
"v_mfma_f32_16x16x32_bf16 v[124:127], a[124:127], a[92:95], v[124:127]\n" \
"ds_read_b64_tr_b16 a[168:169], v226 offset:16384\n" \
"ds_read_b64_tr_b16 a[170:171], v226 offset:18432\n" \
"v_mfma_f32_16x16x32_bf16 v[120:123], a[112:115], a[88:91], v[120:123]\n" \
"ds_read_b64_tr_b16 a[172:173], v226 offset:24576\n" \
"ds_read_b64_tr_b16 a[174:175], v226 offset:26624\n" \
"v_mfma_f32_16x16x32_bf16 v[120:123], a[116:119], a[92:95], v[120:123]\n" \
"s_waitcnt lgkmcnt(8)\n" \
"ds_read_b64_tr_b16 a[176:177], v225 offset:16896\n" \
"ds_read_b64_tr_b16 a[178:179], v225 offset:18944\n" \
"v_mfma_f32_16x16x32_bf16 v[64:67], a[128:131], a[0:3], v[64:67]\n" \
"ds_read_b64_tr_b16 a[180:181], v225 offset:25088\n" \
"ds_read_b64_tr_b16 a[182:183], v225 offset:27136\n" \
"v_mfma_f32_16x16x32_bf16 v[64:67], a[132:135], a[4:7], v[64:67]\n" \
"ds_read_b64_tr_b16 a[184:185], v226 offset:16896\n" \
"ds_read_b64_tr_b16 a[186:187], v226 offset:18944\n" \
"v_mfma_f32_16x16x32_bf16 v[68:71], a[136:139], a[0:3], v[68:71]\n" \
"ds_read_b64_tr_b16 a[188:189], v226 offset:25088\n" \
"ds_read_b64_tr_b16 a[190:191], v226 offset:27136\n" \
"v_mfma_f32_16x16x32_bf16 v[68:71], a[140:143], a[4:7], v[68:71]\n" \
"v_lshrrev_b32 v132, 4, v234\n" \
"v_and_b32 v133, 15, v132\n" \
"v_mfma_f32_16x16x32_bf16 v[72:75], a[144:147], a[0:3], v[72:75]\n" \
"v_mul_u32_u24 v128, 144, v133\n" \
"v_lshrrev_b32 v133, 4, v132\n" \
"v_mfma_f32_16x16x32_bf16 v[72:75], a[148:151], a[4:7], v[72:75]\n" \
"v_lshl_add_u32 v128, v133, 3, v128\n" \
"v_add_u32 v128, s68, v128\n" \
"v_mfma_f32_16x16x32_bf16 v[76:79], a[152:155], a[0:3], v[76:79]\n" \
"v_lshrrev_b32 v133, 3, v132\n" \
"v_and_b32 v134, 7, v132\n" \
"v_mfma_f32_16x16x32_bf16 v[76:79], a[156:159], a[4:7], v[76:79]\n" \
"v_mul_u32_u24 v129, 144, v133\n" \
"v_lshl_add_u32 v129, v134, 4, v129\n" \
"v_mfma_f32_16x16x32_bf16 v[92:95], a[152:155], a[24:27], v[92:95]\n" \
"v_add_u32 v129, s68, v129\n" \
"v_lshlrev_b32 v134, 4, v134\n" \
"v_mfma_f32_16x16x32_bf16 v[92:95], a[156:159], a[28:31], v[92:95]\n" \
"v_lshl_add_u32 v130, v133, 13, v134\n" \
"v_mfma_f32_16x16x32_bf16 v[88:91], a[144:147], a[24:27], v[88:91]\n" \
"v_mfma_f32_16x16x32_bf16 v[88:91], a[148:151], a[28:31], v[88:91]\n" \
"v_mfma_f32_16x16x32_bf16 v[84:87], a[136:139], a[24:27], v[84:87]\n" \
"s_waitcnt vmcnt(0)\n" \
"s_barrier\n" \
"v_mfma_f32_16x16x32_bf16 v[84:87], a[140:143], a[28:31], v[84:87]\n" \
"ds_read_b128 a[32:35], v224 offset:256\n" \
"ds_read_b128 a[36:39], v224 offset:2304\n" \
"v_mfma_f32_16x16x32_bf16 v[80:83], a[128:131], a[24:27], v[80:83]\n" \
"ds_read_b128 a[16:19], v227 offset:0\n" \
"ds_read_b128 a[20:23], v227 offset:2048\n" \
"v_mfma_f32_16x16x32_bf16 v[80:83], a[132:135], a[28:31], v[80:83]\n" \
"s_waitcnt lgkmcnt(4)\n" \
"ds_read_b64_tr_b16 a[192:193], v228 offset:16384\n" \
"ds_read_b64_tr_b16 a[194:195], v228 offset:18432\n" \
"v_mfma_f32_16x16x32_bf16 v[64:67], a[160:163], a[8:11], v[64:67]\n" \
"ds_read_b64_tr_b16 a[196:197], v228 offset:24576\n" \
"ds_read_b64_tr_b16 a[198:199], v228 offset:26624\n" \
"v_mfma_f32_16x16x32_bf16 v[64:67], a[164:167], a[12:15], v[64:67]\n" \
"ds_read_b64_tr_b16 a[200:201], v229 offset:16384\n" \
"ds_read_b64_tr_b16 a[202:203], v229 offset:18432\n" \
"v_mfma_f32_16x16x32_bf16 v[68:71], a[168:171], a[8:11], v[68:71]\n" \
"ds_read_b64_tr_b16 a[204:205], v229 offset:24576\n" \
"ds_read_b64_tr_b16 a[206:207], v229 offset:26624\n" \
"v_mfma_f32_16x16x32_bf16 v[68:71], a[172:175], a[12:15], v[68:71]\n" \
"ds_read_b64_tr_b16 a[208:209], v228 offset:16896\n" \
"ds_read_b64_tr_b16 a[210:211], v228 offset:18944\n" \
"v_mfma_f32_16x16x32_bf16 v[72:75], a[176:179], a[8:11], v[72:75]\n" \
"ds_read_b64_tr_b16 a[212:213], v228 offset:25088\n" \
"ds_read_b64_tr_b16 a[214:215], v228 offset:27136\n" \
"v_mfma_f32_16x16x32_bf16 v[72:75], a[180:183], a[12:15], v[72:75]\n" \
"ds_read_b64_tr_b16 a[216:217], v229 offset:16896\n" \
"ds_read_b64_tr_b16 a[218:219], v229 offset:18944\n" \
"v_mfma_f32_16x16x32_bf16 v[76:79], a[184:187], a[8:11], v[76:79]\n" \
"ds_read_b64_tr_b16 a[220:221], v229 offset:25088\n" \
"ds_read_b64_tr_b16 a[222:223], v229 offset:27136\n" \
"v_mfma_f32_16x16x32_bf16 v[76:79], a[188:191], a[12:15], v[76:79]\n" \
"s_waitcnt lgkmcnt(8)\n" \
"ds_read_b128 a[40:43], v227 offset:256\n" \
"ds_read_b128 a[44:47], v227 offset:2304\n" \
"v_mfma_f32_16x16x32_bf16 v[92:95], a[184:187], a[32:35], v[92:95]\n" \
"ds_read_b128 a[248:251], v213 offset:4096\n" \
"ds_read_b128 a[252:255], v213 offset:6144\n" \
"v_mfma_f32_16x16x32_bf16 v[92:95], a[188:191], a[36:39], v[92:95]\n" \
"ds_read_b128 a[48:51], v224 offset:4096\n" \
"ds_read_b128 a[52:55], v224 offset:6144\n" \
"v_mfma_f32_16x16x32_bf16 v[88:91], a[176:179], a[32:35], v[88:91]\n" \
"ds_read_b128 a[56:59], v227 offset:4096\n" \
"ds_read_b128 a[60:63], v227 offset:6144\n" \
"v_mfma_f32_16x16x32_bf16 v[88:91], a[180:183], a[36:39], v[88:91]\n" \
"ds_read_b128 a[224:227], v213 offset:4352\n" \
"ds_read_b128 a[228:231], v213 offset:6400\n" \
"v_mfma_f32_16x16x32_bf16 v[84:87], a[168:171], a[32:35], v[84:87]\n" \
"ds_read_b128 a[232:235], v224 offset:4352\n" \
"ds_read_b128 a[236:239], v224 offset:6400\n" \
"v_mfma_f32_16x16x32_bf16 v[84:87], a[172:175], a[36:39], v[84:87]\n" \
"ds_read_b128 a[240:243], v227 offset:4352\n" \
"ds_read_b128 a[244:247], v227 offset:6400\n" \
"v_mfma_f32_16x16x32_bf16 v[80:83], a[160:163], a[32:35], v[80:83]\n" \
"v_mfma_f32_16x16x32_bf16 v[80:83], a[164:167], a[36:39], v[80:83]\n" \
"v_mfma_f32_16x16x32_bf16 v[64:67], a[192:195], a[16:19], v[64:67]\n" \
"v_mfma_f32_16x16x32_bf16 v[64:67], a[196:199], a[20:23], v[64:67]\n" \
"v_mfma_f32_16x16x32_bf16 v[68:71], a[200:203], a[16:19], v[68:71]\n" \
"v_mfma_f32_16x16x32_bf16 v[68:71], a[204:207], a[20:23], v[68:71]\n" \
"s_waitcnt lgkmcnt(0)\n" \
"v_mfma_f32_16x16x32_bf16 v[72:75], a[208:211], a[16:19], v[72:75]\n" \
"v_mfma_f32_16x16x32_bf16 v[72:75], a[212:215], a[20:23], v[72:75]\n" \
"v_cvt_pk_bf16_f32 v136, v64, v65\n" \
"v_cvt_pk_bf16_f32 v137, v66, v67\n" \
"v_mfma_f32_16x16x32_bf16 v[76:79], a[216:219], a[16:19], v[76:79]\n" \
"v_mfma_f32_16x16x32_bf16 v[76:79], a[220:223], a[20:23], v[76:79]\n" \
"ds_write_b64 v128, v[136:137] offset:0\n" \
"v_mfma_f32_16x16x32_bf16 v[80:83], a[192:195], a[40:43], v[80:83]\n" \
"v_cvt_pk_bf16_f32 v138, v68, v69\n" \
"v_cvt_pk_bf16_f32 v139, v70, v71\n" \
"v_mfma_f32_16x16x32_bf16 v[80:83], a[196:199], a[44:47], v[80:83]\n" \
"ds_write_b64 v128, v[138:139] offset:32\n" \
"v_mfma_f32_16x16x32_bf16 v[84:87], a[200:203], a[40:43], v[84:87]\n" \
"v_cvt_pk_bf16_f32 v136, v72, v73\n" \
"v_cvt_pk_bf16_f32 v137, v74, v75\n" \
"v_mfma_f32_16x16x32_bf16 v[84:87], a[204:207], a[44:47], v[84:87]\n" \
"ds_write_b64 v128, v[136:137] offset:64\n" \
"v_mfma_f32_16x16x32_bf16 v[88:91], a[208:211], a[40:43], v[88:91]\n" \
"v_cvt_pk_bf16_f32 v138, v76, v77\n" \
"v_cvt_pk_bf16_f32 v139, v78, v79\n" \
"v_mfma_f32_16x16x32_bf16 v[88:91], a[212:215], a[44:47], v[88:91]\n" \
"ds_write_b64 v128, v[138:139] offset:96\n" \
"v_mfma_f32_16x16x32_bf16 v[92:95], a[216:219], a[40:43], v[92:95]\n" \
"ds_read_b128 v[140:143], v129 offset:0\n" \
"ds_read_b128 v[144:147], v129 offset:1152\n" \
"v_mfma_f32_16x16x32_bf16 v[92:95], a[220:223], a[44:47], v[92:95]\n" \
"v_cvt_pk_bf16_f32 v136, v80, v81\n" \
"v_cvt_pk_bf16_f32 v137, v82, v83\n" \
"v_mfma_f32_16x16x32_bf16 v[96:99], a[128:131], a[248:251], v[96:99]\n" \
"ds_write_b64 v128, v[136:137] offset:2304\n" \
"v_mfma_f32_16x16x32_bf16 v[96:99], a[132:135], a[252:255], v[96:99]\n" \
"s_add_u32 s70, s88, 0\n" \
"s_waitcnt lgkmcnt(2)\n" \
"v_cvt_pk_bf16_f32 v138, v84, v85\n" \
"v_mfma_f32_16x16x32_bf16 v[96:99], a[160:163], a[48:51], v[96:99]\n" \
"buffer_store_dwordx4 v[140:143], v130, s[52:55], s70 offen sc1\n" \
"v_mfma_f32_16x16x32_bf16 v[96:99], a[164:167], a[52:55], v[96:99]\n" \
"v_cvt_pk_bf16_f32 v139, v86, v87\n" \
"v_mfma_f32_16x16x32_bf16 v[96:99], a[192:195], a[56:59], v[96:99]\n" \
"s_add_u32 s70, s88, 65536\n" \
"s_waitcnt lgkmcnt(1)\n" \
"v_mfma_f32_16x16x32_bf16 v[96:99], a[196:199], a[60:63], v[96:99]\n" \
"buffer_store_dwordx4 v[144:147], v130, s[52:55], s70 offen sc1\n" \
"ds_write_b64 v128, v[138:139] offset:2336\n" \
"v_mfma_f32_16x16x32_bf16 v[100:103], a[204:207], a[60:63], v[100:103]\n" \
"v_cvt_pk_bf16_f32 v136, v88, v89\n" \
"v_cvt_pk_bf16_f32 v137, v90, v91\n" \
"v_mfma_f32_16x16x32_bf16 v[100:103], a[200:203], a[56:59], v[100:103]\n" \
"ds_write_b64 v128, v[136:137] offset:2368\n" \
"v_mfma_f32_16x16x32_bf16 v[100:103], a[172:175], a[52:55], v[100:103]\n" \
"v_cvt_pk_bf16_f32 v138, v92, v93\n" \
"v_cvt_pk_bf16_f32 v139, v94, v95\n" \
"v_mfma_f32_16x16x32_bf16 v[100:103], a[168:171], a[48:51], v[100:103]\n" \
"v_mfma_f32_16x16x32_bf16 v[100:103], a[140:143], a[252:255], v[100:103]\n" \
"v_mfma_f32_16x16x32_bf16 v[100:103], a[136:139], a[248:251], v[100:103]\n" \
"ds_write_b64 v128, v[138:139] offset:2400\n" \
"v_mfma_f32_16x16x32_bf16 v[104:107], a[144:147], a[248:251], v[104:107]\n" \
"ds_read_b128 v[148:151], v129 offset:2304\n" \
"ds_read_b128 v[152:155], v129 offset:3456\n" \
"v_mfma_f32_16x16x32_bf16 v[104:107], a[148:151], a[252:255], v[104:107]\n" \
"v_cvt_pk_bf16_f32 v136, v96, v97\n" \
"v_cvt_pk_bf16_f32 v137, v98, v99\n" \
"v_mfma_f32_16x16x32_bf16 v[104:107], a[176:179], a[48:51], v[104:107]\n" \
"ds_write_b64 v128, v[136:137] offset:4608\n" \
"v_mfma_f32_16x16x32_bf16 v[104:107], a[180:183], a[52:55], v[104:107]\n" \
"v_cvt_pk_bf16_f32 v138, v100, v101\n" \
"v_cvt_pk_bf16_f32 v139, v102, v103\n" \
"v_mfma_f32_16x16x32_bf16 v[104:107], a[208:211], a[56:59], v[104:107]\n" \
"s_add_u32 s70, s88, 4096\n" \
"s_waitcnt lgkmcnt(2)\n" \
"v_mfma_f32_16x16x32_bf16 v[104:107], a[212:215], a[60:63], v[104:107]\n" \
"buffer_store_dwordx4 v[148:151], v130, s[52:55], s70 offen sc1\n" \
"ds_write_b64 v128, v[138:139] offset:4640\n" \
"v_mfma_f32_16x16x32_bf16 v[108:111], a[220:223], a[60:63], v[108:111]\n" \
"s_add_u32 s70, s88, 69632\n" \
"s_waitcnt lgkmcnt(2)\n" \
"v_mfma_f32_16x16x32_bf16 v[108:111], a[216:219], a[56:59], v[108:111]\n" \
"buffer_store_dwordx4 v[152:155], v130, s[52:55], s70 offen sc1\n" \
"v_mfma_f32_16x16x32_bf16 v[108:111], a[188:191], a[52:55], v[108:111]\n" \
"v_mfma_f32_16x16x32_bf16 v[108:111], a[184:187], a[48:51], v[108:111]\n" \
"v_cvt_pk_bf16_f32 v136, v104, v105\n" \
"v_cvt_pk_bf16_f32 v137, v106, v107\n" \
"v_mfma_f32_16x16x32_bf16 v[108:111], a[156:159], a[252:255], v[108:111]\n" \
"v_mfma_f32_16x16x32_bf16 v[108:111], a[152:155], a[248:251], v[108:111]\n" \
"ds_write_b64 v128, v[136:137] offset:4672\n" \
"v_mfma_f32_16x16x32_bf16 v[112:115], a[128:131], a[224:227], v[112:115]\n" \
"v_mfma_f32_16x16x32_bf16 v[112:115], a[132:135], a[228:231], v[112:115]\n" \
"v_mfma_f32_16x16x32_bf16 v[112:115], a[160:163], a[232:235], v[112:115]\n" \
"v_mfma_f32_16x16x32_bf16 v[112:115], a[164:167], a[236:239], v[112:115]\n" \
"v_cvt_pk_bf16_f32 v138, v108, v109\n" \
"v_cvt_pk_bf16_f32 v139, v110, v111\n" \
"v_mfma_f32_16x16x32_bf16 v[112:115], a[192:195], a[240:243], v[112:115]\n" \
"v_mfma_f32_16x16x32_bf16 v[112:115], a[196:199], a[244:247], v[112:115]\n" \
"ds_write_b64 v128, v[138:139] offset:4704\n" \
"v_mfma_f32_16x16x32_bf16 v[116:119], a[204:207], a[244:247], v[116:119]\n" \
"ds_read_b128 v[156:159], v129 offset:4608\n" \
"ds_read_b128 v[160:163], v129 offset:5760\n" \
"v_mfma_f32_16x16x32_bf16 v[116:119], a[200:203], a[240:243], v[116:119]\n" \
"v_mfma_f32_16x16x32_bf16 v[116:119], a[172:175], a[236:239], v[116:119]\n" \
"v_mfma_f32_16x16x32_bf16 v[116:119], a[168:171], a[232:235], v[116:119]\n" \
"v_cvt_pk_bf16_f32 v136, v112, v113\n" \
"v_cvt_pk_bf16_f32 v137, v114, v115\n" \
"v_mfma_f32_16x16x32_bf16 v[116:119], a[140:143], a[228:231], v[116:119]\n" \
"s_add_u32 s70, s88, 131072\n" \
"s_waitcnt lgkmcnt(1)\n" \
"v_mfma_f32_16x16x32_bf16 v[116:119], a[136:139], a[224:227], v[116:119]\n" \
"buffer_store_dwordx4 v[156:159], v130, s[52:55], s70 offen sc1\n" \
"ds_write_b64 v128, v[136:137] offset:6912\n" \
"v_mfma_f32_16x16x32_bf16 v[120:123], a[144:147], a[224:227], v[120:123]\n" \
"s_add_u32 s70, s88, 196608\n" \
"s_waitcnt lgkmcnt(1)\n" \
"v_mfma_f32_16x16x32_bf16 v[120:123], a[148:151], a[228:231], v[120:123]\n" \
"buffer_store_dwordx4 v[160:163], v130, s[52:55], s70 offen sc1\n" \
"v_mfma_f32_16x16x32_bf16 v[120:123], a[176:179], a[232:235], v[120:123]\n" \
"v_mfma_f32_16x16x32_bf16 v[120:123], a[180:183], a[236:239], v[120:123]\n" \
"v_cvt_pk_bf16_f32 v138, v116, v117\n" \
"v_cvt_pk_bf16_f32 v139, v118, v119\n" \
"v_mfma_f32_16x16x32_bf16 v[120:123], a[208:211], a[240:243], v[120:123]\n" \
"v_mfma_f32_16x16x32_bf16 v[120:123], a[212:215], a[244:247], v[120:123]\n" \
"ds_write_b64 v128, v[138:139] offset:6944\n" \
"v_mfma_f32_16x16x32_bf16 v[124:127], a[220:223], a[244:247], v[124:127]\n" \
"v_mfma_f32_16x16x32_bf16 v[124:127], a[216:219], a[240:243], v[124:127]\n" \
"v_mfma_f32_16x16x32_bf16 v[124:127], a[188:191], a[236:239], v[124:127]\n" \
"v_mfma_f32_16x16x32_bf16 v[124:127], a[184:187], a[232:235], v[124:127]\n" \
"v_cvt_pk_bf16_f32 v136, v120, v121\n" \
"v_cvt_pk_bf16_f32 v137, v122, v123\n" \
"v_mfma_f32_16x16x32_bf16 v[124:127], a[156:159], a[228:231], v[124:127]\n" \
"v_mfma_f32_16x16x32_bf16 v[124:127], a[152:155], a[224:227], v[124:127]\n" \
"ds_write_b64 v128, v[136:137] offset:6976\n" \
"s_nop 7\n" \
"v_cvt_pk_bf16_f32 v138, v124, v125\n" \
"v_cvt_pk_bf16_f32 v139, v126, v127\n" \
"ds_write_b64 v128, v[138:139] offset:7008\n" \
"ds_read_b128 v[164:167], v129 offset:6912\n" \
"ds_read_b128 v[168:171], v129 offset:8064\n" \
"s_add_u32 s70, s88, 135168\n" \
"s_waitcnt lgkmcnt(1)\n" \
"buffer_store_dwordx4 v[164:167], v130, s[52:55], s70 offen sc1\n" \
"s_add_u32 s70, s88, 200704\n" \
"s_waitcnt lgkmcnt(0)\n" \
"buffer_store_dwordx4 v[168:171], v130, s[52:55], s70 offen sc1\n" \
"s_endpgm\n" \
"Lhot_end_%=:\n" \
"Lpl1_%=:\n" \
"s_mov_b32 s72, 0\n" \
"Lpk1_%=:\n" \
"s_sleep 2\n" \
"buffer_load_dword v227, v242, s[56:59], s90 offen sc1\n" \
"s_waitcnt vmcnt(0)\n" \
"v_cmp_eq_u32 vcc, s60, v227\n" \
"s_nop 1\n" \
"s_andn2_b32 s70, 0xff, vcc_lo\n" \
"s_cbranch_scc0 Lpr1_%=\n" \
"s_add_u32 s72, s72, 1\n" \
"s_cmp_lt_u32 s72, 0x40000\n" \
"s_cbranch_scc1 Lpk1_%=\n" \
"s_branch Lpr1_%=\n" \
"Lpl2_%=:\n" \
"s_mov_b32 s72, 0\n" \
"Lpk2_%=:\n" \
"s_sleep 2\n" \
"buffer_load_dword v239, v242, s[56:59], s95 offen sc1\n" \
"s_waitcnt vmcnt(0)\n" \
"v_cmp_eq_u32 vcc, s60, v239\n" \
"s_nop 1\n" \
"s_andn2_b32 s70, s69, vcc_lo\n" \
"s_cbranch_scc0 Lpr2_%=\n" \
"s_add_u32 s72, s72, 1\n" \
"s_cmp_lt_u32 s72, 0x40000\n" \
"s_cbranch_scc1 Lpk2_%=\n" \
"s_branch Lpr2_%=\n" \
"Lpl3_%=:\n" \
"s_mov_b32 s72, 0\n" \
"Lpk3_%=:\n" \
"s_sleep 2\n" \
"buffer_load_dword v239, v242, s[56:59], s95 offen sc1\n" \
"s_waitcnt vmcnt(0)\n" \
"v_cmp_eq_u32 vcc, s60, v239\n" \
"s_nop 1\n" \
"s_andn2_b32 s70, s69, vcc_lo\n" \
"s_cbranch_scc0 Lpr3_%=\n" \
"s_add_u32 s72, s72, 1\n" \
"s_cmp_lt_u32 s72, 0x40000\n" \
"s_cbranch_scc1 Lpk3_%=\n" \
"s_branch Lpr3_%=\n" \
"Lexact_%=:\n" \
"v_mov_b32 v239, 0xff800000\n" \
"s_and_b32 s70, s61, 1\n" \
"s_lshl_b32 s70, s70, 4\n" \
"v_add_u32 v186, s70, v238\n" \
"v_accvgpr_read_b32 v160, a128\n" \
"v_accvgpr_read_b32 v161, a129\n" \
"v_accvgpr_read_b32 v162, a130\n" \
"v_accvgpr_read_b32 v163, a131\n" \
"v_accvgpr_read_b32 v164, a132\n" \
"v_accvgpr_read_b32 v165, a133\n" \
"v_accvgpr_read_b32 v166, a134\n" \
"v_accvgpr_read_b32 v167, a135\n" \
"v_accvgpr_read_b32 v168, a136\n" \
"v_accvgpr_read_b32 v169, a137\n" \
"v_accvgpr_read_b32 v170, a138\n" \
"v_accvgpr_read_b32 v171, a139\n" \
"v_accvgpr_read_b32 v172, a140\n" \
"v_accvgpr_read_b32 v173, a141\n" \
"v_accvgpr_read_b32 v174, a142\n" \
"v_accvgpr_read_b32 v175, a143\n" \
"s_add_u32 s83, s85, 0\n" \
"s_add_u32 s84, s87, 0\n" \
"s_call_b64 s[64:65], Lex1_%=\n" \
"v_accvgpr_read_b32 v160, a144\n" \
"v_accvgpr_read_b32 v161, a145\n" \
"v_accvgpr_read_b32 v162, a146\n" \
"v_accvgpr_read_b32 v163, a147\n" \
"v_accvgpr_read_b32 v164, a148\n" \
"v_accvgpr_read_b32 v165, a149\n" \
"v_accvgpr_read_b32 v166, a150\n" \
"v_accvgpr_read_b32 v167, a151\n" \
"v_accvgpr_read_b32 v168, a152\n" \
"v_accvgpr_read_b32 v169, a153\n" \
"v_accvgpr_read_b32 v170, a154\n" \
"v_accvgpr_read_b32 v171, a155\n" \
"v_accvgpr_read_b32 v172, a156\n" \
"v_accvgpr_read_b32 v173, a157\n" \
"v_accvgpr_read_b32 v174, a158\n" \
"v_accvgpr_read_b32 v175, a159\n" \
"s_add_u32 s83, s85, 2\n" \
"s_add_u32 s84, s87, 532480\n" \
"s_call_b64 s[64:65], Lex1_%=\n" \
"v_accvgpr_read_b32 v160, a160\n" \
"v_accvgpr_read_b32 v161, a161\n" \
"v_accvgpr_read_b32 v162, a162\n" \
"v_accvgpr_read_b32 v163, a163\n" \
"v_accvgpr_read_b32 v164, a164\n" \
"v_accvgpr_read_b32 v165, a165\n" \
"v_accvgpr_read_b32 v166, a166\n" \
"v_accvgpr_read_b32 v167, a167\n" \
"v_accvgpr_read_b32 v168, a168\n" \
"v_accvgpr_read_b32 v169, a169\n" \
"v_accvgpr_read_b32 v170, a170\n" \
"v_accvgpr_read_b32 v171, a171\n" \
"v_accvgpr_read_b32 v172, a172\n" \
"v_accvgpr_read_b32 v173, a173\n" \
"v_accvgpr_read_b32 v174, a174\n" \
"v_accvgpr_read_b32 v175, a175\n" \
"s_add_u32 s83, s86, 0\n" \
"s_add_u32 s84, s88, 0\n" \
"s_call_b64 s[64:65], Lex1_%=\n" \
"v_accvgpr_read_b32 v160, a176\n" \
"v_accvgpr_read_b32 v161, a177\n" \
"v_accvgpr_read_b32 v162, a178\n" \
"v_accvgpr_read_b32 v163, a179\n" \
"v_accvgpr_read_b32 v164, a180\n" \
"v_accvgpr_read_b32 v165, a181\n" \
"v_accvgpr_read_b32 v166, a182\n" \
"v_accvgpr_read_b32 v167, a183\n" \
"v_accvgpr_read_b32 v168, a184\n" \
"v_accvgpr_read_b32 v169, a185\n" \
"v_accvgpr_read_b32 v170, a186\n" \
"v_accvgpr_read_b32 v171, a187\n" \
"v_accvgpr_read_b32 v172, a188\n" \
"v_accvgpr_read_b32 v173, a189\n" \
"v_accvgpr_read_b32 v174, a190\n" \
"v_accvgpr_read_b32 v175, a191\n" \
"s_add_u32 s83, s86, 2\n" \
"s_add_u32 s84, s88, 532480\n" \
"s_call_b64 s[64:65], Lex1_%=\n" \
"s_waitcnt vmcnt(0)\n" \
"s_branch Lexr_%=\n" \
"Lex1_%=:\n" \
"v_mov_b32 v128, 0\n" \
"v_mov_b32 v129, 0\n" \
"v_mov_b32 v130, 0\n" \
"v_mov_b32 v131, 0\n" \
"v_mov_b32 v132, 0\n" \
"v_mov_b32 v133, 0\n" \
"v_mov_b32 v134, 0\n" \
"v_mov_b32 v135, 0\n" \
"v_mov_b32 v136, 0\n" \
"v_mov_b32 v137, 0\n" \
"v_mov_b32 v138, 0\n" \
"v_mov_b32 v139, 0\n" \
"v_mov_b32 v140, 0\n" \
"v_mov_b32 v141, 0\n" \
"v_mov_b32 v142, 0\n" \
"v_mov_b32 v143, 0\n" \
"v_mov_b32 v144, 0\n" \
"v_mov_b32 v145, 0\n" \
"v_mov_b32 v146, 0\n" \
"v_mov_b32 v147, 0\n" \
"v_mov_b32 v148, 0\n" \
"v_mov_b32 v149, 0\n" \
"v_mov_b32 v150, 0\n" \
"v_mov_b32 v151, 0\n" \
"v_mov_b32 v152, 0\n" \
"v_mov_b32 v153, 0\n" \
"v_mov_b32 v154, 0\n" \
"v_mov_b32 v155, 0\n" \
"v_mov_b32 v156, 0\n" \
"v_mov_b32 v157, 0\n" \
"v_mov_b32 v158, 0\n" \
"v_mov_b32 v159, 0\n" \
"v_mov_b32 v184, 0xf149f2ca\n" \
"v_mov_b32 v185, 0\n" \
"s_mov_b32 s81, 0\n" \
"s_mul_i32 s82, s62, 557824\n" \
"Lex2_%=:\n" \
"s_add_u32 s70, s82, 4096\n" \
"buffer_load_dwordx4 a[64:67], v236, s[56:59], s82 offen offset:0 sc1\n" \
"buffer_load_dwordx4 a[68:71], v236, s[56:59], s82 offen offset:2048 sc1\n" \
"buffer_load_dwordx4 a[72:75], v236, s[56:59], s70 offen offset:0 sc1\n" \
"buffer_load_dwordx4 a[76:79], v236, s[56:59], s70 offen offset:2048 sc1\n" \
"buffer_load_dwordx4 a[80:83], v236, s[56:59], s82 offen offset:256 sc1\n" \
"buffer_load_dwordx4 a[84:87], v236, s[56:59], s82 offen offset:2304 sc1\n" \
"buffer_load_dwordx4 a[88:91], v236, s[56:59], s70 offen offset:256 sc1\n" \
"buffer_load_dwordx4 a[92:95], v236, s[56:59], s70 offen offset:2304 sc1\n" \
"s_add_u32 s70, s82, 17850368\n" \
"s_add_u32 s71, s70, 4096\n" \
"buffer_load_dwordx4 a[96:99], v234, s[56:59], s70 offen offset:0 sc1\n" \
"buffer_load_dwordx4 a[100:103], v234, s[56:59], s70 offen offset:1024 sc1\n" \
"buffer_load_dwordx4 a[104:107], v234, s[56:59], s70 offen offset:2048 sc1\n" \
"buffer_load_dwordx4 a[108:111], v234, s[56:59], s70 offen offset:3072 sc1\n" \
"buffer_load_dwordx4 a[112:115], v234, s[56:59], s71 offen offset:0 sc1\n" \
"buffer_load_dwordx4 a[116:119], v234, s[56:59], s71 offen offset:1024 sc1\n" \
"buffer_load_dwordx4 a[120:123], v234, s[56:59], s71 offen offset:2048 sc1\n" \
"buffer_load_dwordx4 a[124:127], v234, s[56:59], s71 offen offset:3072 sc1\n" \
"s_waitcnt vmcnt(0)\n" \
"v_mfma_f32_16x16x32_bf16 v[176:179], a[64:67], v[160:163], 0\n" \
"v_mfma_f32_16x16x32_bf16 v[176:179], a[68:71], v[164:167], v[176:179]\n" \
"v_mfma_f32_16x16x32_bf16 v[176:179], a[72:75], v[168:171], v[176:179]\n" \
"v_mfma_f32_16x16x32_bf16 v[176:179], a[76:79], v[172:175], v[176:179]\n" \
"v_mfma_f32_16x16x32_bf16 v[180:183], a[80:83], v[160:163], 0\n" \
"v_mfma_f32_16x16x32_bf16 v[180:183], a[84:87], v[164:167], v[180:183]\n" \
"v_mfma_f32_16x16x32_bf16 v[180:183], a[88:91], v[168:171], v[180:183]\n" \
"v_mfma_f32_16x16x32_bf16 v[180:183], a[92:95], v[172:175], v[180:183]\n" \
"s_nop 7\n" \
"s_nop 1\n" \
"s_cmp_eq_u32 s81, s83\n" \
"s_cbranch_scc0 Lex3_%=\n" \
"v_cmp_gt_i32 vcc, 0, v186\n" \
"s_nop 1\n" \
"v_cndmask_b32 v176, v176, v239, vcc\n" \
"v_cmp_gt_i32 vcc, 1, v186\n" \
"s_nop 1\n" \
"v_cndmask_b32 v177, v177, v239, vcc\n" \
"v_cmp_gt_i32 vcc, 2, v186\n" \
"s_nop 1\n" \
"v_cndmask_b32 v178, v178, v239, vcc\n" \
"v_cmp_gt_i32 vcc, 3, v186\n" \
"s_nop 1\n" \
"v_cndmask_b32 v179, v179, v239, vcc\n" \
"v_cmp_gt_i32 vcc, 16, v186\n" \
"s_nop 1\n" \
"v_cndmask_b32 v180, v180, v239, vcc\n" \
"v_cmp_gt_i32 vcc, 17, v186\n" \
"s_nop 1\n" \
"v_cndmask_b32 v181, v181, v239, vcc\n" \
"v_cmp_gt_i32 vcc, 18, v186\n" \
"s_nop 1\n" \
"v_cndmask_b32 v182, v182, v239, vcc\n" \
"v_cmp_gt_i32 vcc, 19, v186\n" \
"s_nop 1\n" \
"v_cndmask_b32 v183, v183, v239, vcc\n" \
"Lex3_%=:\n" \
"v_max3_f32 v187, v176, v177, v178\n" \
"v_max3_f32 v187, v187, v179, v180\n" \
"v_max3_f32 v187, v187, v181, v182\n" \
"v_max3_f32 v187, v187, v183, v184\n" \
"v_mov_b32 v188, v187\n" \
"s_nop 1\n" \
"v_permlane16_swap_b32 v188, v187\n" \
"v_max_f32 v187, v187, v188\n" \
"v_mov_b32 v188, v187\n" \
"s_nop 1\n" \
"v_permlane32_swap_b32 v188, v187\n" \
"v_max_f32 v187, v187, v188\n" \
"v_sub_f32 v189, v184, v187\n" \
"v_exp_f32 v189, v189\n" \
"v_mov_b32 v184, v187\n" \
"v_sub_f32 v176, v176, v187\n" \
"v_sub_f32 v177, v177, v187\n" \
"v_sub_f32 v178, v178, v187\n" \
"v_sub_f32 v179, v179, v187\n" \
"v_sub_f32 v180, v180, v187\n" \
"v_sub_f32 v181, v181, v187\n" \
"v_sub_f32 v182, v182, v187\n" \
"v_sub_f32 v183, v183, v187\n" \
"v_exp_f32 v176, v176\n" \
"v_exp_f32 v177, v177\n" \
"v_exp_f32 v178, v178\n" \
"v_exp_f32 v179, v179\n" \
"v_exp_f32 v180, v180\n" \
"v_exp_f32 v181, v181\n" \
"v_exp_f32 v182, v182\n" \
"v_exp_f32 v183, v183\n" \
"v_mul_f32 v185, v185, v189\n" \
"v_add_f32 v185, v185, v176\n" \
"v_add_f32 v185, v185, v177\n" \
"v_add_f32 v185, v185, v178\n" \
"v_add_f32 v185, v185, v179\n" \
"v_add_f32 v185, v185, v180\n" \
"v_add_f32 v185, v185, v181\n" \
"v_add_f32 v185, v185, v182\n" \
"v_add_f32 v185, v185, v183\n" \
"v_cvt_pk_bf16_f32 v112, v176, v177\n" \
"v_cvt_pk_bf16_f32 v113, v178, v179\n" \
"v_cvt_pk_bf16_f32 v114, v180, v181\n" \
"v_cvt_pk_bf16_f32 v115, v182, v183\n" \
"v_mul_f32 v128, v128, v189\n" \
"v_mul_f32 v129, v129, v189\n" \
"v_mul_f32 v130, v130, v189\n" \
"v_mul_f32 v131, v131, v189\n" \
"v_mul_f32 v132, v132, v189\n" \
"v_mul_f32 v133, v133, v189\n" \
"v_mul_f32 v134, v134, v189\n" \
"v_mul_f32 v135, v135, v189\n" \
"v_mul_f32 v136, v136, v189\n" \
"v_mul_f32 v137, v137, v189\n" \
"v_mul_f32 v138, v138, v189\n" \
"v_mul_f32 v139, v139, v189\n" \
"v_mul_f32 v140, v140, v189\n" \
"v_mul_f32 v141, v141, v189\n" \
"v_mul_f32 v142, v142, v189\n" \
"v_mul_f32 v143, v143, v189\n" \
"v_mul_f32 v144, v144, v189\n" \
"v_mul_f32 v145, v145, v189\n" \
"v_mul_f32 v146, v146, v189\n" \
"v_mul_f32 v147, v147, v189\n" \
"v_mul_f32 v148, v148, v189\n" \
"v_mul_f32 v149, v149, v189\n" \
"v_mul_f32 v150, v150, v189\n" \
"v_mul_f32 v151, v151, v189\n" \
"v_mul_f32 v152, v152, v189\n" \
"v_mul_f32 v153, v153, v189\n" \
"v_mul_f32 v154, v154, v189\n" \
"v_mul_f32 v155, v155, v189\n" \
"v_mul_f32 v156, v156, v189\n" \
"v_mul_f32 v157, v157, v189\n" \
"v_mul_f32 v158, v158, v189\n" \
"v_mul_f32 v159, v159, v189\n" \
"s_nop 1\n" \
"v_mfma_f32_16x16x32_bf16 v[128:131], a[96:99], v[112:115], v[128:131]\n" \
"v_mfma_f32_16x16x32_bf16 v[132:135], a[100:103], v[112:115], v[132:135]\n" \
"v_mfma_f32_16x16x32_bf16 v[136:139], a[104:107], v[112:115], v[136:139]\n" \
"v_mfma_f32_16x16x32_bf16 v[140:143], a[108:111], v[112:115], v[140:143]\n" \
"v_mfma_f32_16x16x32_bf16 v[144:147], a[112:115], v[112:115], v[144:147]\n" \
"v_mfma_f32_16x16x32_bf16 v[148:151], a[116:119], v[112:115], v[148:151]\n" \
"v_mfma_f32_16x16x32_bf16 v[152:155], a[120:123], v[112:115], v[152:155]\n" \
"v_mfma_f32_16x16x32_bf16 v[156:159], a[124:127], v[112:115], v[156:159]\n" \
"s_nop 7\n" \
"s_nop 1\n" \
"s_and_b32 s70, s81, 1\n" \
"s_mul_i32 s70, s70, 1024\n" \
"s_add_u32 s82, s82, s70\n" \
"s_add_u32 s82, s82, 8192\n" \
"s_add_u32 s81, s81, 1\n" \
"s_cmp_le_u32 s81, s83\n" \
"s_cbranch_scc1 Lex2_%=\n" \
"v_mov_b32 v187, v185\n" \
"s_nop 1\n" \
"v_permlane16_swap_b32 v187, v185\n" \
"v_add_f32 v185, v185, v187\n" \
"v_mov_b32 v187, v185\n" \
"s_nop 1\n" \
"v_permlane32_swap_b32 v187, v185\n" \
"v_add_f32 v185, v185, v187\n" \
"v_rcp_f32 v185, v185\n" \
"s_nop 0\n" \
"v_mul_f32 v128, v128, v185\n" \
"v_mul_f32 v129, v129, v185\n" \
"v_mul_f32 v130, v130, v185\n" \
"v_mul_f32 v131, v131, v185\n" \
"v_mul_f32 v132, v132, v185\n" \
"v_mul_f32 v133, v133, v185\n" \
"v_mul_f32 v134, v134, v185\n" \
"v_mul_f32 v135, v135, v185\n" \
"v_mul_f32 v136, v136, v185\n" \
"v_mul_f32 v137, v137, v185\n" \
"v_mul_f32 v138, v138, v185\n" \
"v_mul_f32 v139, v139, v185\n" \
"v_mul_f32 v140, v140, v185\n" \
"v_mul_f32 v141, v141, v185\n" \
"v_mul_f32 v142, v142, v185\n" \
"v_mul_f32 v143, v143, v185\n" \
"v_mul_f32 v144, v144, v185\n" \
"v_mul_f32 v145, v145, v185\n" \
"v_mul_f32 v146, v146, v185\n" \
"v_mul_f32 v147, v147, v185\n" \
"v_mul_f32 v148, v148, v185\n" \
"v_mul_f32 v149, v149, v185\n" \
"v_mul_f32 v150, v150, v185\n" \
"v_mul_f32 v151, v151, v185\n" \
"v_mul_f32 v152, v152, v185\n" \
"v_mul_f32 v153, v153, v185\n" \
"v_mul_f32 v154, v154, v185\n" \
"v_mul_f32 v155, v155, v185\n" \
"v_mul_f32 v156, v156, v185\n" \
"v_mul_f32 v157, v157, v185\n" \
"v_mul_f32 v158, v158, v185\n" \
"v_mul_f32 v159, v159, v185\n" \
"s_add_u32 s70, s84, 4096\n" \
"v_cvt_pk_bf16_f32 v112, v128, v129\n" \
"v_cvt_pk_bf16_f32 v113, v130, v131\n" \
"v_cvt_pk_bf16_f32 v114, v132, v133\n" \
"v_cvt_pk_bf16_f32 v115, v134, v135\n" \
"buffer_store_dwordx4 v[112:115], v236, s[56:59], s84 offen offset:0 sc1\n" \
"v_cvt_pk_bf16_f32 v116, v136, v137\n" \
"v_cvt_pk_bf16_f32 v117, v138, v139\n" \
"v_cvt_pk_bf16_f32 v118, v140, v141\n" \
"v_cvt_pk_bf16_f32 v119, v142, v143\n" \
"buffer_store_dwordx4 v[116:119], v236, s[56:59], s84 offen offset:2048 sc1\n" \
"v_cvt_pk_bf16_f32 v120, v144, v145\n" \
"v_cvt_pk_bf16_f32 v121, v146, v147\n" \
"v_cvt_pk_bf16_f32 v122, v148, v149\n" \
"v_cvt_pk_bf16_f32 v123, v150, v151\n" \
"buffer_store_dwordx4 v[120:123], v236, s[56:59], s70 offen offset:0 sc1\n" \
"v_cvt_pk_bf16_f32 v124, v152, v153\n" \
"v_cvt_pk_bf16_f32 v125, v154, v155\n" \
"v_cvt_pk_bf16_f32 v126, v156, v157\n" \
"v_cvt_pk_bf16_f32 v127, v158, v159\n" \
"buffer_store_dwordx4 v[124:127], v236, s[56:59], s70 offen offset:2048 sc1\n" \
"s_waitcnt vmcnt(0)\n" \
"s_setpc_b64 s[64:65]\n" \
"Lpl4_%=:\n" \
"s_mov_b32 s72, 0\n" \
"Lpk4_%=:\n" \
"s_sleep 2\n" \
"buffer_load_dwordx2 v[226:227], v228, s[56:59], s13 offen sc1\n" \
"s_waitcnt vmcnt(0)\n" \
"v_cmp_eq_u32 vcc, s60, v226\n" \
"s_nop 1\n" \
"s_andn2_b32 s70, 0xff, vcc_lo\n" \
"s_cbranch_scc0 Lpr4_%=\n" \
"s_add_u32 s72, s72, 1\n" \
"s_cmp_lt_u32 s72, 0x40000\n" \
"s_cbranch_scc1 Lpk4_%=\n" \
"s_branch Lpr4_%=\n" \
"Lpl5_%=:\n" \
"s_mov_b32 s72, 0\n" \
"Lpk5_%=:\n" \
"s_sleep 2\n" \
"buffer_load_dword v220, v221, s[56:59], s66 offen sc1\n" \
"s_waitcnt vmcnt(0)\n" \
"v_cmp_eq_u32 vcc, s60, v220\n" \
"s_nop 1\n" \
"s_andn2_b32 s70, s65, vcc_lo\n" \
"s_cbranch_scc0 Lpr5_%=\n" \
"s_add_u32 s72, s72, 1\n" \
"s_cmp_lt_u32 s72, 0x40000\n" \
"s_cbranch_scc1 Lpk5_%=\n" \
"s_branch Lpr5_%=\n" \
"Lpl6_%=:\n" \
"s_mov_b32 s72, 0\n" \
"Lpk6_%=:\n" \
"s_sleep 2\n" \
"buffer_load_dword v220, v221, s[56:59], s67 offen sc1\n" \
"s_waitcnt vmcnt(0)\n" \
"v_cmp_eq_u32 vcc, s60, v220\n" \
"s_nop 1\n" \
"s_andn2_b32 s70, s74, vcc_lo\n" \
"s_cbranch_scc0 Lpr6_%=\n" \
"s_add_u32 s72, s72, 1\n" \
"s_cmp_lt_u32 s72, 0x40000\n" \
"s_cbranch_scc1 Lpk6_%=\n" \
"s_branch Lpr6_%=\n" \
"Lpl7_%=:\n" \
"s_mov_b32 s72, 0\n" \
"Lpk7_%=:\n" \
"s_sleep 2\n" \
"buffer_load_dword v220, v241, s[56:59], s64 offen sc1\n" \
"s_waitcnt vmcnt(0)\n" \
"v_cmp_eq_u32 vcc, s60, v220\n" \
"s_nop 1\n" \
"s_andn2_b32 s70, s69, vcc_lo\n" \
"s_cbranch_scc0 Lpr7_%=\n" \
"s_add_u32 s72, s72, 1\n" \
"s_cmp_lt_u32 s72, 0x40000\n" \
"s_cbranch_scc1 Lpk7_%=\n" \
"s_branch Lpr7_%=\n" \
"Lcode_end_%=:\n" \
".p2align 12\n" \
""
#define MK_ASM_CLOBBERS "v1", "v2", "v3", "v4", "v5", "v6", "v7", "v8", "v9", "v10", "v11", "v12", "v13", "v14", "v15", "v16", "v17", "v18", "v19", "v20", "v21", "v22", "v23", "v24", "v25", "v26", "v27", "v28", "v29", "v30", "v31", "v32", "v33", "v34", "v35", "v36", "v37", "v38", "v39", "v40", "v41", "v42", "v43", "v44", "v45", "v46", "v47", "v48", "v49", "v50", "v51", "v52", "v53", "v54", "v55", "v56", "v57", "v58", "v59", "v60", "v61", "v62", "v63", "v64", "v65", "v66", "v67", "v68", "v69", "v70", "v71", "v72", "v73", "v74", "v75", "v76", "v77", "v78", "v79", "v80", "v81", "v82", "v83", "v84", "v85", "v86", "v87", "v88", "v89", "v90", "v91", "v92", "v93", "v94", "v95", "v96", "v97", "v98", "v99", "v100", "v101", "v102", "v103", "v104", "v105", "v106", "v107", "v108", "v109", "v110", "v111", "v112", "v113", "v114", "v115", "v116", "v117", "v118", "v119", "v120", "v121", "v122", "v123", "v124", "v125", "v126", "v127", "v128", "v129", "v130", "v131", "v132", "v133", "v134", "v135", "v136", "v137", "v138", "v139", "v140", "v141", "v142", "v143", "v144", "v145", "v146", "v147", "v148", "v149", "v150", "v151", "v152", "v153", "v154", "v155", "v156", "v157", "v158", "v159", "v160", "v161", "v162", "v163", "v164", "v165", "v166", "v167", "v168", "v169", "v170", "v171", "v172", "v173", "v174", "v175", "v176", "v177", "v178", "v179", "v180", "v181", "v182", "v183", "v184", "v185", "v186", "v187", "v188", "v189", "v190", "v191", "v192", "v193", "v194", "v195", "v196", "v197", "v198", "v199", "v200", "v201", "v202", "v203", "v204", "v205", "v206", "v207", "v208", "v209", "v210", "v211", "v212", "v213", "v214", "v215", "v216", "v217", "v218", "v219", "v220", "v221", "v222", "v223", "v224", "v225", "v226", "v227", "v228", "v229", "v230", "v231", "v232", "v233", "v234", "v235", "v236", "v237", "v238", "v239", "v240", "v241", "v242", "v243", "v244", "v245", "v246", "v247", "v248", "v249", "v250", "v251", "v252", "v253", "v254", "v255", "a0", "a1", "a2", "a3", "a4", "a5", "a6", "a7", "a8", "a9", "a10", "a11", "a12", "a13", "a14", "a15", "a16", "a17", "a18", "a19", "a20", "a21", "a22", "a23", "a24", "a25", "a26", "a27", "a28", "a29", "a30", "a31", "a32", "a33", "a34", "a35", "a36", "a37", "a38", "a39", "a40", "a41", "a42", "a43", "a44", "a45", "a46", "a47", "a48", "a49", "a50", "a51", "a52", "a53", "a54", "a55", "a56", "a57", "a58", "a59", "a60", "a61", "a62", "a63", "a64", "a65", "a66", "a67", "a68", "a69", "a70", "a71", "a72", "a73", "a74", "a75", "a76", "a77", "a78", "a79", "a80", "a81", "a82", "a83", "a84", "a85", "a86", "a87", "a88", "a89", "a90", "a91", "a92", "a93", "a94", "a95", "a96", "a97", "a98", "a99", "a100", "a101", "a102", "a103", "a104", "a105", "a106", "a107", "a108", "a109", "a110", "a111", "a112", "a113", "a114", "a115", "a116", "a117", "a118", "a119", "a120", "a121", "a122", "a123", "a124", "a125", "a126", "a127", "a128", "a129", "a130", "a131", "a132", "a133", "a134", "a135", "a136", "a137", "a138", "a139", "a140", "a141", "a142", "a143", "a144", "a145", "a146", "a147", "a148", "a149", "a150", "a151", "a152", "a153", "a154", "a155", "a156", "a157", "a158", "a159", "a160", "a161", "a162", "a163", "a164", "a165", "a166", "a167", "a168", "a169", "a170", "a171", "a172", "a173", "a174", "a175", "a176", "a177", "a178", "a179", "a180", "a181", "a182", "a183", "a184", "a185", "a186", "a187", "a188", "a189", "a190", "a191", "a192", "a193", "a194", "a195", "a196", "a197", "a198", "a199", "a200", "a201", "a202", "a203", "a204", "a205", "a206", "a207", "a208", "a209", "a210", "a211", "a212", "a213", "a214", "a215", "a216", "a217", "a218", "a219", "a220", "a221", "a222", "a223", "a224", "a225", "a226", "a227", "a228", "a229", "a230", "a231", "a232", "a233", "a234", "a235", "a236", "a237", "a238", "a239", "a240", "a241", "a242", "a243", "a244", "a245", "a246", "a247", "a248", "a249", "a250", "a251", "a252", "a253", "a254", "a255", "s8", "s9", "s10", "s11", "s12", "s13", "s14", "s15", "s16", "s17", "s18", "s19", "s20", "s21", "s22", "s23", "s24", "s25", "s26", "s27", "s28", "s29", "s30", "s31", "s36", "s37", "s38", "s39", "s40", "s41", "s42", "s43", "s44", "s45", "s46", "s47", "s48", "s49", "s50", "s51", "s52", "s53", "s54", "s55", "s56", "s57", "s58", "s59", "s60", "s61", "s62", "s63", "s64", "s65", "s66", "s67", "s68", "s69", "s70", "s71", "s72", "s73", "s74", "s75", "s76", "s77", "s78", "s79", "s80", "s81", "s82", "s83", "s84", "s85", "s86", "s87", "s88", "s89", "s90", "s91", "s92", "s93", "s94", "s95", "vcc", "scc", "memory"
namespace mk {
constexpr int LDS_BYTES = 163840;
struct Args { const void* in[9]; void* out; void* ws; };
__global__ __launch_bounds__(256, 1) void mega_k(const Args a) {
    extern __shared__ __attribute__((aligned(16))) unsigned char shm[];
    const unsigned ldsb = (unsigned)(size_t)(LAS unsigned char*)shm;
    asm volatile(MK_ASM_BODY
                 :
                 : "s"(__builtin_amdgcn_kernarg_segment_ptr()), "s"(blockIdx.x), "v"(threadIdx.x), "s"(ldsb)
                 : MK_ASM_CLOBBERS);
}
}
extern "C" void kernel_launch(void* const* d_in, const int* in_sizes, int n_in, void* d_out, int out_size, void* d_ws, size_t ws_size, hipStream_t stream) {
    (void)hipFuncSetAttribute((const void*)mk::mega_k, hipFuncAttributeMaxDynamicSharedMemorySize, mk::LDS_BYTES);
    mk::Args a;
    for (int i = 0; i < 9; ++i) a.in[i] = d_in[i];
    a.out = d_out; a.ws = (char*)d_ws + ((ws_size - WS_END) & ~(size_t)0x1fffff);
    mk::mega_k<<<256, 256, mk::LDS_BYTES, stream>>>(a);
}
```

```cpp
#include <hip/hip_runtime.h>
#include <stdint.h>
#define LAS __attribute__((address_space(3)))
constexpr size_t LAY_TS = 17408, LAY_HS = 557824, LAY_CRS = 266240, LAY_ORS = 69632, WS_K = 0, WS_V = 17850368, WS_CTX = 35700736, WS_OB = 52740096, WS_FL = 65585152, WS_END = 65634304, LAY_VS = 2;
#define MK_ASM_BODY \
"s_load_dwordx16 s[8:23], %0, 0x0\n" \
"s_load_dwordx4 s[24:27], %0, 0x40\n" \
"s_load_dwordx2 s[28:29], %0, 0x50\n" \
"v_readfirstlane_b32 s61, %2\n" \
"s_lshr_b32 s61, s61, 6\n" \
"v_mbcnt_lo_u32_b32 v78, -1, 0\n" \
"v_mbcnt_hi_u32_b32 v78, -1, v78\n" \
"v_lshlrev_b32 v234, 4, v78\n" \
"s_getpc_b64 s[64:65]\n" \
"Lpc_%=:\n" \
"s_lshr_b32 s70, %1, 3\n" \
"s_lshl_b32 s70, s70, 2\n" \
"s_add_u32 s70, s70, s61\n" \
"s_and_b32 s71, s70, 31\n" \
"s_lshl_b32 s71, s71, 12\n" \
"v_lshl_add_u32 v237, v234, 2, s71\n" \
"s_mov_b32 s71, Lcode_end_%=-Lpc_%=-4\n" \
"v_min_u32 v237, s71, v237\n" \
"global_load_dword v238, v237, s[64:65]\n" \
"s_and_b32 s62, %1, 7\n" \
"s_lshl_b32 s62, s62, 2\n" \
"s_lshr_b32 s70, %1, 6\n" \
"s_add_u32 s62, s62, s70\n" \
"s_bfe_u32 s63, %1, 0x30003\n" \
"s_lshl_b32 s74, s61, 13\n" \
"s_add_u32 s74, s74, %3\n" \
"v_bfe_u32 v79, v78, 4, 1\n" \
"v_bfe_u32 v80, v78, 2, 2\n" \
"v_lshl_add_u32 v80, v79, 3, v80\n" \
"s_lshl_b32 s71, s61, 4\n" \
"v_add_u32 v80, s71, v80\n" \
"v_lshlrev_b32 v80, 13, v80\n" \
"v_lshlrev_b32 v79, 1, v79\n" \
"v_and_b32 v81, 3, v78\n" \
"v_xor_b32 v79, v79, v81\n" \
"v_lshl_add_u32 v80, v79, 4, v80\n" \
"v_lshrrev_b32 v90, 5, v78\n" \
"v_lshl_add_u32 v84, v90, 6, v80\n" \
"v_add_u32 v68, 4096, v84\n" \
"v_add_u32 v69, 3072, v84\n" \
"v_add_u32 v70, 2176, v84\n" \
"v_add_u32 v71, 1152, v84\n" \
"s_lshl_b32 s83, s62, 8\n" \
"s_add_u32 s84, s83, 32768\n" \
"v_lshrrev_b32 v85, 3, v78\n" \
"v_and_b32 v86, 7, v78\n" \
"v_xor_b32 v86, v86, v85\n" \
"v_lshlrev_b32 v86, 4, v86\n" \
"v_lshl_add_u32 v64, v85, 10, v86\n" \
"v_add_u32 v65, 7168, v64\n" \
"v_add_u32 v66, 14336, v64\n" \
"v_add_u32 v67, 21504, v64\n" \
"s_bitcmp1_b32 s61, 0\n" \
"s_cselect_b32 s70, 0, 15\n" \
"s_xor_b32 s81, s63, s70\n" \
"s_lshl_b32 s81, s81, 17\n" \
"s_lshr_b32 s70, s61, 1\n" \
"s_lshl_b32 s71, s70, 16\n" \
"s_add_u32 s81, s81, s71\n" \
"s_add_u32 s82, s81, 32768\n" \
"s_mov_b32 s42, 4198400\n" \
"s_mov_b32 s43, 0x20000\n" \
"s_mov_b32 s46, 4198400\n" \
"s_mov_b32 s47, 0x20000\n" \
"s_mov_b32 s38, 2097152\n" \
"s_mov_b32 s39, 0x20000\n" \
"s_waitcnt lgkmcnt(0)\n" \
"s_mov_b64 s[40:41], s[14:15]\n" \
"s_sub_u32 s40, s40, 4096\n" \
"s_subb_u32 s41, s41, 0\n" \
"s_and_b32 s41, s41, 0xffff\n" \
"s_mov_b64 s[44:45], s[16:17]\n" \
"s_sub_u32 s44, s44, 4096\n" \
"s_subb_u32 s45, s45, 0\n" \
"s_and_b32 s45, s45, 0xffff\n" \
"s_mov_b64 s[36:37], s[10:11]\n" \
"s_and_b32 s37, s37, 0xffff\n" \
"s_add_u32 m0, s74, 32768\n" \
"s_nop 0\n" \
"buffer_load_dwordx4 v68, s[40:43], s83 offen offset:0 lds\n" \
"buffer_load_dwordx4 v69, s[44:47], s83 offen offset:1024 lds\n" \
"buffer_load_dwordx4 v70, s[40:43], s83 offen offset:2048 lds\n" \
"buffer_load_dwordx4 v71, s[44:47], s83 offen offset:3072 lds\n" \
"s_xor_b32 s83, s83, 0x80000\n" \
"s_add_u32 m0, s74, 36864\n" \
"s_nop 0\n" \
"buffer_load_dwordx4 v68, s[40:43], s84 offen offset:0 lds\n" \
"buffer_load_dwordx4 v69, s[44:47], s84 offen offset:1024 lds\n" \
"buffer_load_dwordx4 v70, s[40:43], s84 offen offset:2048 lds\n" \
"buffer_load_dwordx4 v71, s[44:47], s84 offen offset:3072 lds\n" \
"s_xor_b32 s84, s84, 0x80000\n" \
"s_add_u32 m0, s74, 0\n" \
"s_nop 0\n" \
"buffer_load_dwordx4 v64, s[36:39], s81 offen offset:0 lds\n" \
"buffer_load_dwordx4 v65, s[36:39], s81 offen offset:1024 lds\n" \
"buffer_load_dwordx4 v66, s[36:39], s81 offen offset:2048 lds\n" \
"buffer_load_dwordx4 v67, s[36:39], s81 offen offset:3072 lds\n" \
"s_xor_b32 s81, s81, 0x80\n" \
"s_add_u32 m0, s74, 4096\n" \
"s_nop 0\n" \
"buffer_load_dwordx4 v64, s[36:39], s82 offen offset:0 lds\n" \
"buffer_load_dwordx4 v65, s[36:39], s82 offen offset:1024 lds\n" \
"buffer_load_dwordx4 v66, s[36:39], s82 offen offset:2048 lds\n" \
"buffer_load_dwordx4 v67, s[36:39], s82 offen offset:3072 lds\n" \
"s_xor_b32 s82, s82, 0x80\n" \
"v_lshrrev_b32 v87, 4, v78\n" \
"v_and_b32 v79, 15, v78\n" \
"v_and_b32 v80, 7, v78\n" \
"v_xor_b32 v80, v80, v87\n" \
"v_lshlrev_b32 v79, 7, v79\n" \
"s_lshl_b32 s70, s70, 14\n" \
"s_add_u32 s70, s70, %3\n" \
"v_lshl_add_u32 v88, v80, 4, v79\n" \
"v_add_u32 v72, s70, v88\n" \
"v_xor_b32 v73, 64, v72\n" \
"v_lshlrev_b32 v79, 3, v81\n" \
"v_bfe_u32 v80, v78, 2, 2\n" \
"v_lshl_add_u32 v79, v80, 6, v79\n" \
"v_bfe_u32 v80, v78, 4, 1\n" \
"v_lshl_add_u32 v79, v80, 8, v79\n" \
"v_lshl_add_u32 v89, v80, 5, v79\n" \
"s_and_b32 s70, s61, 1\n" \
"s_lshl_b32 s70, s70, 11\n" \
"s_add_u32 s70, s70, %3\n" \
"v_lshl_add_u32 v82, v90, 13, v89\n" \
"v_add_u32 v74, s70, v82\n" \
"v_xor_b32 v75, 32, v74\n" \
"s_mov_b64 s[56:57], s[28:29]\n" \
"s_and_b32 s57, s57, 0xffff\n" \
"s_mov_b32 s58, 65634304\n" \
"s_mov_b32 s59, 0x20000\n" \
"v_mov_b32 v241, 0\n" \
"v_lshl_add_u32 v110, v90, 9, v234\n" \
"s_lshl_b32 s66, %1, 2\n" \
"s_add_u32 s66, s66, s61\n" \
"s_lshl_b32 s66, s66, 10\n" \
"s_lshr_b32 s67, %1, 3\n" \
"s_lshl_b32 s67, s67, 2\n" \
"s_add_u32 s67, s67, s61\n" \
"s_lshl_b32 s67, s67, 13\n" \
"s_and_b32 s64, %1, 7\n" \
"s_lshl_b32 s64, s64, 10\n" \
"s_add_u32 s67, s67, s64\n" \
"s_mov_b64 exec, 0\n" \
"s_cmp_eq_u32 s61, 0\n" \
"s_cbranch_scc1 Lst0x0_%=\n" \
"s_cmp_eq_u32 s61, 1\n" \
"s_cbranch_scc1 Lst0x1_%=\n" \
"s_cmp_eq_u32 s61, 2\n" \
"s_cbranch_scc1 Lst0x2_%=\n" \
"s_branch Lst0x3_%=\n" \
"Lcb0_%=:\n" \
"s_mov_b64 exec, -1\n" \
"s_add_u32 m0, s74, 131072\n" \
"s_nop 0\n" \
"buffer_load_dwordx4 v68, s[40:43], s83 offen offset:0 lds\n" \
"buffer_load_dwordx4 v69, s[44:47], s83 offen offset:1024 lds\n" \
"buffer_load_dwordx4 v70, s[40:43], s83 offen offset:2048 lds\n" \
"buffer_load_dwordx4 v71, s[44:47], s83 offen offset:3072 lds\n" \
"s_xor_b32 s83, s83, 0x180000\n" \
"s_add_u32 m0, s74, 135168\n" \
"s_nop 0\n" \
"buffer_load_dwordx4 v68, s[40:43], s84 offen offset:0 lds\n" \
"buffer_load_dwordx4 v69, s[44:47], s84 offen offset:1024 lds\n" \
"buffer_load_dwordx4 v70, s[40:43], s84 offen offset:2048 lds\n" \
"buffer_load_dwordx4 v71, s[44:47], s84 offen offset:3072 lds\n" \
"s_xor_b32 s84, s84, 0x180000\n" \
"s_waitcnt vmcnt(8)\n" \
"s_barrier\n" \
"ds_read_b64_tr_b16 a[64:65], v74 offset:32768\n" \
"ds_read_b64_tr_b16 a[66:67], v74 offset:36864\n" \
"ds_read_b64_tr_b16 a[68:69], v74 offset:49152\n" \
"ds_read_b64_tr_b16 a[70:71], v74 offset:53248\n" \
"ds_read_b128 a[0:3], v72 offset:0\n" \
"ds_read_b128 a[4:7], v73 offset:0\n" \
"ds_read_b64_tr_b16 a[72:73], v75 offset:32768\n" \
"ds_read_b64_tr_b16 a[74:75], v75 offset:36864\n" \
"ds_read_b64_tr_b16 a[76:77], v75 offset:49152\n" \
"ds_read_b64_tr_b16 a[78:79], v75 offset:53248\n" \
"ds_read_b64_tr_b16 a[80:81], v74 offset:33280\n" \
"ds_read_b64_tr_b16 a[82:83], v74 offset:37376\n" \
"ds_read_b64_tr_b16 a[84:85], v74 offset:49664\n" \
"ds_read_b64_tr_b16 a[86:87], v74 offset:53760\n" \
"ds_read_b64_tr_b16 a[88:89], v75 offset:33280\n" \
"ds_read_b64_tr_b16 a[90:91], v75 offset:37376\n" \
"ds_read_b64_tr_b16 a[92:93], v75 offset:49664\n" \
"ds_read_b64_tr_b16 a[94:95], v75 offset:53760\n" \
"ds_read_b128 a[8:11], v72 offset:2048\n" \
"ds_read_b128 a[12:15], v73 offset:2048\n" \
"s_waitcnt lgkmcnt(14)\n" \
"v_mfma_f32_16x16x32_bf16 v[0:3], a[64:67], a[0:3], 0\n" \
"s_mov_b32 s93, 65593344\n" \
"ds_read_b128 a[16:19], v72 offset:4096\n" \
"v_mfma_f32_16x16x32_bf16 v[0:3], a[68:71], a[4:7], v[0:3]\n" \
"s_waitcnt lgkmcnt(11)\n" \
"buffer_load_dword v240, v241, s[56:59], s93 offen sc1\n" \
"v_mfma_f32_16x16x32_bf16 v[4:7], a[72:75], a[0:3], 0\n" \
"s_mov_b64 s[64:65], s[10:11]\n" \
"s_lshl1_add_u32 s68, s66, 0\n" \
"s_add_u32 s64, s64, s68\n" \
"s_addc_u32 s65, s65, 0\n" \
"ds_read_b128 a[20:23], v73 offset:4096\n" \
"v_mfma_f32_16x16x32_bf16 v[4:7], a[76:79], a[4:7], v[4:7]\n" \
"s_waitcnt lgkmcnt(8)\n" \
"global_load_dword v238, v110, s[64:65]\n" \
"v_mfma_f32_16x16x32_bf16 v[8:11], a[80:83], a[0:3], 0\n" \
"s_mov_b64 s[64:65], s[14:15]\n" \
"s_add_u32 s68, s67, 0x100000\n" \
"s_add_u32 s64, s64, s68\n" \
"s_addc_u32 s65, s65, 0\n" \
"ds_read_b128 a[24:27], v72 offset:6144\n" \
"v_mfma_f32_16x16x32_bf16 v[8:11], a[84:87], a[4:7], v[8:11]\n" \
"s_waitcnt lgkmcnt(2)\n" \
"global_load_dword v238, v234, s[64:65]\n" \
"v_mfma_f32_16x16x32_bf16 v[12:15], a[88:91], a[0:3], 0\n" \
"s_mov_b64 s[64:65], s[16:17]\n" \
"s_add_u32 s68, s67, 0x100000\n" \
"s_add_u32 s64, s64, s68\n" \
"s_addc_u32 s65, s65, 0\n" \
"ds_read_b128 a[28:31], v73 offset:6144\n" \
"v_mfma_f32_16x16x32_bf16 v[12:15], a[92:95], a[4:7], v[12:15]\n" \
"global_load_dword v238, v234, s[64:65]\n" \
"v_mfma_f32_16x16x32_bf16 v[28:31], a[88:91], a[8:11], 0\n" \
"s_add_u32 m0, s74, 65536\n" \
"ds_read_b128 a[32:35], v72 offset:8192\n" \
"v_mfma_f32_16x16x32_bf16 v[28:31], a[92:95], a[12:15], v[28:31]\n" \
"buffer_load_dwordx4 v64, s[36:39], s81 offen offset:0 lds\n" \
"v_mfma_f32_16x16x32_bf16 v[24:27], a[80:83], a[8:11], 0\n" \
"ds_read_b128 a[36:39], v73 offset:8192\n" \
"v_mfma_f32_16x16x32_bf16 v[24:27], a[84:87], a[12:15], v[24:27]\n" \
"v_mfma_f32_16x16x32_bf16 v[20:23], a[72:75], a[8:11], 0\n" \
"ds_read_b128 a[40:43], v72 offset:10240\n" \
"v_mfma_f32_16x16x32_bf16 v[20:23], a[76:79], a[12:15], v[20:23]\n" \
"buffer_load_dwordx4 v65, s[36:39], s81 offen offset:1024 lds\n" \
"v_mfma_f32_16x16x32_bf16 v[16:19], a[64:67], a[8:11], 0\n" \
"ds_read_b128 a[44:47], v73 offset:10240\n" \
"v_mfma_f32_16x16x32_bf16 v[16:19], a[68:71], a[12:15], v[16:19]\n" \
"s_waitcnt lgkmcnt(2)\n" \
"v_mfma_f32_16x16x32_bf16 v[32:35], a[64:67], a[16:19], 0\n" \
"ds_read_b128 a[48:51], v72 offset:12288\n" \
"v_mfma_f32_16x16x32_bf16 v[32:35], a[68:71], a[20:23], v[32:35]\n" \
"buffer_load_dwordx4 v66, s[36:39], s81 offen offset:2048 lds\n" \
"v_mfma_f32_16x16x32_bf16 v[36:39], a[72:75], a[16:19], 0\n" \
"ds_read_b128 a[52:55], v73 offset:12288\n" \
"v_mfma_f32_16x16x32_bf16 v[36:39], a[76:79], a[20:23], v[36:39]\n" \
"v_mfma_f32_16x16x32_bf16 v[40:43], a[80:83], a[16:19], 0\n" \
"ds_read_b128 a[56:59], v72 offset:14336\n" \
"v_mfma_f32_16x16x32_bf16 v[40:43], a[84:87], a[20:23], v[40:43]\n" \
"buffer_load_dwordx4 v67, s[36:39], s81 offen offset:3072 lds\n" \
"v_mfma_f32_16x16x32_bf16 v[44:47], a[88:91], a[16:19], 0\n" \
"ds_read_b128 a[60:63], v73 offset:14336\n" \
"v_mfma_f32_16x16x32_bf16 v[44:47], a[92:95], a[20:23], v[44:47]\n" \
"s_xor_b32 s81, s81, 0x180\n" \
"v_mfma_f32_16x16x32_bf16 v[60:63], a[88:91], a[24:27], 0\n" \
"s_add_u32 m0, s74, 69632\n" \
"ds_read_b64_tr_b16 a[96:97], v74 offset:33792\n" \
"v_mfma_f32_16x16x32_bf16 v[60:63], a[92:95], a[28:31], v[60:63]\n" \
"buffer_load_dwordx4 v64, s[36:39], s82 offen offset:0 lds\n" \
"v_mfma_f32_16x16x32_bf16 v[56:59], a[80:83], a[24:27], 0\n" \
"ds_read_b64_tr_b16 a[98:99], v74 offset:37888\n" \
"v_mfma_f32_16x16x32_bf16 v[56:59], a[84:87], a[28:31], v[56:59]\n" \
"v_mfma_f32_16x16x32_bf16 v[52:55], a[72:75], a[24:27], 0\n" \
"ds_read_b64_tr_b16 a[100:101], v74 offset:50176\n" \
"v_mfma_f32_16x16x32_bf16 v[52:55], a[76:79], a[28:31], v[52:55]\n" \
"buffer_load_dwordx4 v65, s[36:39], s82 offen offset:1024 lds\n" \
"v_mfma_f32_16x16x32_bf16 v[48:51], a[64:67], a[24:27], 0\n" \
"ds_read_b64_tr_b16 a[102:103], v74 offset:54272\n" \
"v_mfma_f32_16x16x32_bf16 v[48:51], a[68:71], a[28:31], v[48:51]\n" \
"v_mfma_f32_16x16x32_bf16 v[144:147], a[64:67], a[32:35], 0\n" \
"ds_read_b64_tr_b16 a[104:105], v75 offset:33792\n" \
"v_mfma_f32_16x16x32_bf16 v[144:147], a[68:71], a[36:39], v[144:147]\n" \
"buffer_load_dwordx4 v66, s[36:39], s82 offen offset:2048 lds\n" \
"v_mfma_f32_16x16x32_bf16 v[148:151], a[72:75], a[32:35], 0\n" \
"ds_read_b64_tr_b16 a[106:107], v75 offset:37888\n" \
"v_mfma_f32_16x16x32_bf16 v[148:151], a[76:79], a[36:39], v[148:151]\n" \
"v_mfma_f32_16x16x32_bf16 v[152:155], a[80:83], a[32:35], 0\n" \
"ds_read_b64_tr_b16 a[108:109], v75 offset:50176\n" \
"v_mfma_f32_16x16x32_bf16 v[152:155], a[84:87], a[36:39], v[152:155]\n" \
"buffer_load_dwordx4 v67, s[36:39], s82 offen offset:3072 lds\n" \
"v_mfma_f32_16x16x32_bf16 v[156:159], a[88:91], a[32:35], 0\n" \
"ds_read_b64_tr_b16 a[110:111], v75 offset:54272\n" \
"v_mfma_f32_16x16x32_bf16 v[156:159], a[92:95], a[36:39], v[156:159]\n" \
"s_waitcnt lgkmcnt(2)\n" \
"v_mfma_f32_16x16x32_bf16 v[188:191], a[88:91], a[40:43], 0\n" \
"s_xor_b32 s82, s82, 0x180\n" \
"ds_read_b64_tr_b16 a[112:113], v74 offset:34304\n" \
"v_add_u32 v96, 0x10000, v72\n" \
"v_mfma_f32_16x16x32_bf16 v[188:191], a[92:95], a[44:47], v[188:191]\n" \
"v_mfma_f32_16x16x32_bf16 v[184:187], a[80:83], a[40:43], 0\n" \
"ds_read_b64_tr_b16 a[114:115], v74 offset:38400\n" \
"v_add_u32 v97, 0x10000, v73\n" \
"v_mfma_f32_16x16x32_bf16 v[184:187], a[84:87], a[44:47], v[184:187]\n" \
"v_mfma_f32_16x16x32_bf16 v[180:183], a[72:75], a[40:43], 0\n" \
"ds_read_b64_tr_b16 a[116:117], v74 offset:50688\n" \
"v_add_u32 v98, 0x10000, v74\n" \
"v_mfma_f32_16x16x32_bf16 v[180:183], a[76:79], a[44:47], v[180:183]\n" \
"v_mfma_f32_16x16x32_bf16 v[176:179], a[64:67], a[40:43], 0\n" \
"ds_read_b64_tr_b16 a[118:119], v74 offset:54784\n" \
"v_add_u32 v99, 0x10000, v75\n" \
"v_mfma_f32_16x16x32_bf16 v[176:179], a[68:71], a[44:47], v[176:179]\n" \
"v_mfma_f32_16x16x32_bf16 v[192:195], a[64:67], a[48:51], 0\n" \
"ds_read_b64_tr_b16 a[120:121], v75 offset:34304\n" \
"v_add_u32 v100, 0x20000, v72\n" \
"v_mfma_f32_16x16x32_bf16 v[192:195], a[68:71], a[52:55], v[192:195]\n" \
"v_mfma_f32_16x16x32_bf16 v[196:199], a[72:75], a[48:51], 0\n" \
"ds_read_b64_tr_b16 a[122:123], v75 offset:38400\n" \
"v_add_u32 v101, 0x20000, v73\n" \
"v_mfma_f32_16x16x32_bf16 v[196:199], a[76:79], a[52:55], v[196:199]\n" \
"v_mfma_f32_16x16x32_bf16 v[200:203], a[80:83], a[48:51], 0\n" \
"s_add_u32 m0, s74, 98304\n" \
"ds_read_b64_tr_b16 a[124:125], v75 offset:50688\n" \
"v_add_u32 v102, 0x20000, v74\n" \
"v_mfma_f32_16x16x32_bf16 v[200:203], a[84:87], a[52:55], v[200:203]\n" \
"buffer_load_dwordx4 v68, s[40:43], s83 offen offset:0 lds\n" \
"v_mfma_f32_16x16x32_bf16 v[204:207], a[88:91], a[48:51], 0\n" \
"ds_read_b64_tr_b16 a[126:127], v75 offset:54784\n" \
"v_add_u32 v103, 0x20000, v75\n" \
"v_mfma_f32_16x16x32_bf16 v[204:207], a[92:95], a[52:55], v[204:207]\n" \
"v_mfma_f32_16x16x32_bf16 v[220:223], a[88:91], a[56:59], 0\n" \
"v_mfma_f32_16x16x32_bf16 v[220:223], a[92:95], a[60:63], v[220:223]\n" \
"buffer_load_dwordx4 v69, s[44:47], s83 offen offset:1024 lds\n" \
"v_mfma_f32_16x16x32_bf16 v[216:219], a[80:83], a[56:59], 0\n" \
"v_mfma_f32_16x16x32_bf16 v[216:219], a[84:87], a[60:63], v[216:219]\n" \
"v_mfma_f32_16x16x32_bf16 v[212:215], a[72:75], a[56:59], 0\n" \
"v_mfma_f32_16x16x32_bf16 v[212:215], a[76:79], a[60:63], v[212:215]\n" \
"buffer_load_dwordx4 v70, s[40:43], s83 offen offset:2048 lds\n" \
"v_mfma_f32_16x16x32_bf16 v[208:211], a[64:67], a[56:59], 0\n" \
"v_mfma_f32_16x16x32_bf16 v[208:211], a[68:71], a[60:63], v[208:211]\n" \
"s_waitcnt lgkmcnt(0)\n" \
"s_barrier\n" \
"v_mfma_f32_16x16x32_bf16 a[128:131], a[0:3], a[96:99], 0\n" \
"v_mfma_f32_16x16x32_bf16 a[128:131], a[4:7], a[100:103], a[128:131]\n" \
"buffer_load_dwordx4 v71, s[44:47], s83 offen offset:3072 lds\n" \
"v_mfma_f32_16x16x32_bf16 a[132:135], a[0:3], a[104:107], 0\n" \
"v_mfma_f32_16x16x32_bf16 a[132:135], a[4:7], a[108:111], a[132:135]\n" \
"s_xor_b32 s83, s83, 0x80000\n" \
"v_mfma_f32_16x16x32_bf16 a[136:139], a[0:3], a[112:115], 0\n" \
"s_add_u32 m0, s74, 102400\n" \
"v_mfma_f32_16x16x32_bf16 a[136:139], a[4:7], a[116:119], a[136:139]\n" \
"buffer_load_dwordx4 v68, s[40:43], s84 offen offset:0 lds\n" \
"v_mfma_f32_16x16x32_bf16 a[140:143], a[0:3], a[120:123], 0\n" \
"v_mfma_f32_16x16x32_bf16 a[140:143], a[4:7], a[124:127], a[140:143]\n" \
"v_mfma_f32_16x16x32_bf16 a[156:159], a[8:11], a[120:123], 0\n" \
"v_mfma_f32_16x16x32_bf16 a[156:159], a[12:15], a[124:127], a[156:159]\n" \
"buffer_load_dwordx4 v69, s[44:47], s84 offen offset:1024 lds\n" \
"v_mfma_f32_16x16x32_bf16 a[152:155], a[8:11], a[112:115], 0\n" \
"v_mfma_f32_16x16x32_bf16 a[152:155], a[12:15], a[116:119], a[152:155]\n" \
"v_mfma_f32_16x16x32_bf16 a[148:151], a[8:11], a[104:107], 0\n" \
"v_mfma_f32_16x16x32_bf16 a[148:151], a[12:15], a[108:111], a[148:151]\n" \
"buffer_load_dwordx4 v70, s[40:43], s84 offen offset:2048 lds\n" \
"v_mfma_f32_16x16x32_bf16 a[144:147], a[8:11], a[96:99], 0\n" \
"v_mfma_f32_16x16x32_bf16 a[144:147], a[12:15], a[100:103], a[144:147]\n" \
"v_mfma_f32_16x16x32_bf16 a[160:163], a[16:19], a[96:99], 0\n" \
"v_mfma_f32_16x16x32_bf16 a[160:163], a[20:23], a[100:103], a[160:163]\n" \
"buffer_load_dwordx4 v71, s[44:47], s84 offen offset:3072 lds\n" \
"v_mfma_f32_16x16x32_bf16 a[164:167], a[16:19], a[104:107], 0\n" \
"v_mfma_f32_16x16x32_bf16 a[164:167], a[20:23], a[108:111], a[164:167]\n" \
"v_mfma_f32_16x16x32_bf16 a[168:171], a[16:19], a[112:115], 0\n" \
"s_xor_b32 s84, s84, 0x80000\n" \
"s_add_u32 m0, s74, 32768\n" \
"v_mfma_f32_16x16x32_bf16 a[168:171], a[20:23], a[116:119], a[168:171]\n" \
"buffer_load_dwordx4 v64, s[36:39], s81 offen offset:0 lds\n" \
"v_mfma_f32_16x16x32_bf16 a[172:175], a[16:19], a[120:123], 0\n" \
"v_mfma_f32_16x16x32_bf16 a[172:175], a[20:23], a[124:127], a[172:175]\n" \
"v_mfma_f32_16x16x32_bf16 a[188:191], a[24:27], a[120:123], 0\n" \
"v_mfma_f32_16x16x32_bf16 a[188:191], a[28:31], a[124:127], a[188:191]\n" \
"buffer_load_dwordx4 v65, s[36:39], s81 offen offset:1024 lds\n" \
"v_mfma_f32_16x16x32_bf16 a[184:187], a[24:27], a[112:115], 0\n" \
"v_mfma_f32_16x16x32_bf16 a[184:187], a[28:31], a[116:119], a[184:187]\n" \
"v_mfma_f32_16x16x32_bf16 a[180:183], a[24:27], a[104:107], 0\n" \
"v_mfma_f32_16x16x32_bf16 a[180:183], a[28:31], a[108:111], a[180:183]\n" \
"buffer_load_dwordx4 v66, s[36:39], s81 offen offset:2048 lds\n" \
"v_mfma_f32_16x16x32_bf16 a[176:179], a[24:27], a[96:99], 0\n" \
"v_mfma_f32_16x16x32_bf16 a[176:179], a[28:31], a[100:103], a[176:179]\n" \
"v_mfma_f32_16x16x32_bf16 a[192:195], a[32:35], a[96:99], 0\n" \
"v_mfma_f32_16x16x32_bf16 a[192:195], a[36:39], a[100:103], a[192:195]\n" \
"buffer_load_dwordx4 v67, s[36:39], s81 offen offset:3072 lds\n" \
"v_mfma_f32_16x16x32_bf16 a[196:199], a[32:35], a[104:107], 0\n" \
"v_mfma_f32_16x16x32_bf16 a[196:199], a[36:39], a[108:111], a[196:199]\n" \
"s_xor_b32 s81, s81, 0x80\n" \
"v_mfma_f32_16x16x32_bf16 a[200:203], a[32:35], a[112:115], 0\n" \
"s_add_u32 m0, s74, 36864\n" \
"v_mfma_f32_16x16x32_bf16 a[200:203], a[36:39], a[116:119], a[200:203]\n" \
"buffer_load_dwordx4 v64, s[36:39], s82 offen offset:0 lds\n" \
"v_mfma_f32_16x16x32_bf16 a[204:207], a[32:35], a[120:123], 0\n" \
"v_mfma_f32_16x16x32_bf16 a[204:207], a[36:39], a[124:127], a[204:207]\n" \
"s_waitcnt vmcnt(13) lgkmcnt(0)\n" \
"s_barrier\n" \
"v_mfma_f32_16x16x32_bf16 a[220:223], a[40:43], a[120:123], 0\n" \
"ds_read_b64_tr_b16 a[64:65], v102 offset:0\n" \
"ds_read_b64_tr_b16 a[66:67], v102 offset:4096\n" \
"v_mfma_f32_16x16x32_bf16 a[220:223], a[44:47], a[124:127], a[220:223]\n" \
"buffer_load_dwordx4 v65, s[36:39], s82 offen offset:1024 lds\n" \
"v_mfma_f32_16x16x32_bf16 a[216:219], a[40:43], a[112:115], 0\n" \
"ds_read_b64_tr_b16 a[68:69], v102 offset:16384\n" \
"ds_read_b64_tr_b16 a[70:71], v102 offset:20480\n" \
"v_mfma_f32_16x16x32_bf16 a[216:219], a[44:47], a[116:119], a[216:219]\n" \
"ds_read_b128 a[0:3], v96 offset:0\n" \
"ds_read_b128 a[4:7], v97 offset:0\n" \
"v_mfma_f32_16x16x32_bf16 a[212:215], a[40:43], a[104:107], 0\n" \
"ds_read_b64_tr_b16 a[72:73], v103 offset:0\n" \
"ds_read_b64_tr_b16 a[74:75], v103 offset:4096\n" \
"v_mfma_f32_16x16x32_bf16 a[212:215], a[44:47], a[108:111], a[212:215]\n" \
"buffer_load_dwordx4 v66, s[36:39], s82 offen offset:2048 lds\n" \
"v_mfma_f32_16x16x32_bf16 a[208:211], a[40:43], a[96:99], 0\n" \
"ds_read_b64_tr_b16 a[76:77], v103 offset:16384\n" \
"ds_read_b64_tr_b16 a[78:79], v103 offset:20480\n" \
"v_mfma_f32_16x16x32_bf16 a[208:211], a[44:47], a[100:103], a[208:211]\n" \
"ds_read_b64_tr_b16 a[80:81], v102 offset:512\n" \
"ds_read_b64_tr_b16 a[82:83], v102 offset:4608\n" \
"v_mfma_f32_16x16x32_bf16 a[224:227], a[48:51], a[96:99], 0\n" \
"ds_read_b64_tr_b16 a[84:85], v102 offset:16896\n" \
"ds_read_b64_tr_b16 a[86:87], v102 offset:20992\n" \
"v_mfma_f32_16x16x32_bf16 a[224:227], a[52:55], a[100:103], a[224:227]\n" \
"buffer_load_dwordx4 v67, s[36:39], s82 offen offset:3072 lds\n" \
"v_mfma_f32_16x16x32_bf16 a[228:231], a[48:51], a[104:107], 0\n" \
"s_mov_b64 s[64:65], s[14:15]\n" \
"s_add_u32 s68, s67, 0x200000\n" \
"ds_read_b64_tr_b16 a[88:89], v103 offset:512\n" \
"ds_read_b64_tr_b16 a[90:91], v103 offset:4608\n" \
"v_mfma_f32_16x16x32_bf16 a[228:231], a[52:55], a[108:111], a[228:231]\n" \
"s_add_u32 s64, s64, s68\n" \
"s_addc_u32 s65, s65, 0\n" \
"ds_read_b64_tr_b16 a[92:93], v103 offset:16896\n" \
"ds_read_b64_tr_b16 a[94:95], v103 offset:20992\n" \
"v_mfma_f32_16x16x32_bf16 a[232:235], a[48:51], a[112:115], 0\n" \
"s_xor_b32 s82, s82, 0x80\n" \
"ds_read_b128 a[8:11], v96 offset:2048\n" \
"ds_read_b128 a[12:15], v97 offset:2048\n" \
"v_mfma_f32_16x16x32_bf16 a[232:235], a[52:55], a[116:119], a[232:235]\n" \
"global_load_dword v238, v234, s[64:65]\n" \
"v_mfma_f32_16x16x32_bf16 a[236:239], a[48:51], a[120:123], 0\n" \
"s_mov_b64 s[64:65], s[16:17]\n" \
"s_add_u32 s68, s67, 0x200000\n" \
"ds_read_b128 a[16:19], v96 offset:4096\n" \
"ds_read_b128 a[20:23], v97 offset:4096\n" \
"v_mfma_f32_16x16x32_bf16 a[236:239], a[52:55], a[124:127], a[236:239]\n" \
"s_add_u32 s64, s64, s68\n" \
"s_addc_u32 s65, s65, 0\n" \
"ds_read_b128 a[24:27], v96 offset:6144\n" \
"ds_read_b128 a[28:31], v97 offset:6144\n" \
"v_mfma_f32_16x16x32_bf16 a[252:255], a[56:59], a[120:123], 0\n" \
"ds_read_b128 a[32:35], v96 offset:8192\n" \
"ds_read_b128 a[36:39], v97 offset:8192\n" \
"v_mfma_f32_16x16x32_bf16 a[252:255], a[60:63], a[124:127], a[252:255]\n" \
"global_load_dword v238, v234, s[64:65]\n" \
"v_mfma_f32_16x16x32_bf16 a[248:251], a[56:59], a[112:115], 0\n" \
"ds_read_b128 a[40:43], v96 offset:10240\n" \
"ds_read_b128 a[44:47], v97 offset:10240\n" \
"v_mfma_f32_16x16x32_bf16 a[248:251], a[60:63], a[116:119], a[248:251]\n" \
"ds_read_b128 a[48:51], v96 offset:12288\n" \
"ds_read_b128 a[52:55], v97 offset:12288\n" \
"v_mfma_f32_16x16x32_bf16 a[244:247], a[56:59], a[104:107], 0\n" \
"v_mfma_f32_16x16x32_bf16 a[244:247], a[60:63], a[108:111], a[244:247]\n" \
"v_mfma_f32_16x16x32_bf16 a[240:243], a[56:59], a[96:99], 0\n" \
"v_mfma_f32_16x16x32_bf16 a[240:243], a[60:63], a[100:103], a[240:243]\n" \
"s_waitcnt lgkmcnt(2)\n" \
"v_mfma_f32_16x16x32_bf16 v[0:3], a[64:67], a[0:3], v[0:3]\n" \
"s_lshl_b32 s79, s61, 12\n" \
"ds_read_b128 a[56:59], v96 offset:14336\n" \
"v_mfma_f32_16x16x32_bf16 v[0:3], a[68:71], a[4:7], v[0:3]\n" \
"s_add_u32 s79, s79, %3\n" \
"v_add_u32 v228, 4096, v84\n" \
"v_add_u32 v229, 3200, v84\n" \
"v_mfma_f32_16x16x32_bf16 v[4:7], a[72:75], a[0:3], v[4:7]\n" \
"s_add_u32 m0, s74, 0\n" \
"ds_read_b128 a[60:63], v97 offset:14336\n" \
"v_mfma_f32_16x16x32_bf16 v[4:7], a[76:79], a[4:7], v[4:7]\n" \
"buffer_load_dwordx4 v68, s[40:43], s83 offen offset:0 lds\n" \
"v_mfma_f32_16x16x32_bf16 v[8:11], a[80:83], a[0:3], v[8:11]\n" \
"s_lshl_b32 s78, s62, 8\n" \
"ds_read_b64_tr_b16 a[96:97], v102 offset:1024\n" \
"v_mfma_f32_16x16x32_bf16 v[8:11], a[84:87], a[4:7], v[8:11]\n" \
"s_add_u32 s89, s78, 30720\n" \
"v_mfma_f32_16x16x32_bf16 v[12:15], a[88:91], a[0:3], v[12:15]\n" \
"s_mov_b64 s[48:49], s[8:9]\n" \
"ds_read_b64_tr_b16 a[98:99], v102 offset:5120\n" \
"v_mfma_f32_16x16x32_bf16 v[12:15], a[92:95], a[4:7], v[12:15]\n" \
"buffer_load_dwordx4 v69, s[44:47], s83 offen offset:1024 lds\n" \
"v_mfma_f32_16x16x32_bf16 v[28:31], a[88:91], a[8:11], v[28:31]\n" \
"s_and_b32 s49, s49, 0xffff\n" \
"ds_read_b64_tr_b16 a[100:101], v102 offset:17408\n" \
"v_mfma_f32_16x16x32_bf16 v[28:31], a[92:95], a[12:15], v[28:31]\n" \
"s_mov_b32 s50, 4194304\n" \
"v_mfma_f32_16x16x32_bf16 v[24:27], a[80:83], a[8:11], v[24:27]\n" \
"s_mov_b32 s51, 0x20000\n" \
"ds_read_b64_tr_b16 a[102:103], v102 offset:21504\n" \
"v_mfma_f32_16x16x32_bf16 v[24:27], a[84:87], a[12:15], v[24:27]\n" \
"buffer_load_dwordx4 v70, s[40:43], s83 offen offset:2048 lds\n" \
"v_mfma_f32_16x16x32_bf16 v[20:23], a[72:75], a[8:11], v[20:23]\n" \
"s_mov_b64 s[52:53], s[12:13]\n" \
"ds_read_b64_tr_b16 a[104:105], v103 offset:1024\n" \
"v_mfma_f32_16x16x32_bf16 v[20:23], a[76:79], a[12:15], v[20:23]\n" \
"s_sub_u32 s52, s52, 4096\n" \
"s_subb_u32 s53, s53, 0\n" \
"v_mfma_f32_16x16x32_bf16 v[16:19], a[64:67], a[8:11], v[16:19]\n" \
"s_and_b32 s53, s53, 0xffff\n" \
"ds_read_b64_tr_b16 a[106:107], v103 offset:5120\n" \
"v_mfma_f32_16x16x32_bf16 v[16:19], a[68:71], a[12:15], v[16:19]\n" \
"buffer_load_dwordx4 v71, s[44:47], s83 offen offset:3072 lds\n" \
"v_mfma_f32_16x16x32_bf16 v[32:35], a[64:67], a[16:19], v[32:35]\n" \
"s_mov_b32 s54, 8392704\n" \
"s_mov_b32 s55, 0x20000\n" \
"ds_read_b64_tr_b16 a[108:109], v103 offset:17408\n" \
"v_mfma_f32_16x16x32_bf16 v[32:35], a[68:71], a[20:23], v[32:35]\n" \
"s_xor_b32 s83, s83, 0x380000\n" \
"v_lshl_add_u32 v224, v85, 11, v86\n" \
"v_add_u32 v225, 15360, v224\n" \
"v_mfma_f32_16x16x32_bf16 v[36:39], a[72:75], a[16:19], v[36:39]\n" \
"s_add_u32 m0, s74, 4096\n" \
"ds_read_b64_tr_b16 a[110:111], v103 offset:21504\n" \
"v_add_u32 v226, 129024, v224\n" \
"v_mfma_f32_16x16x32_bf16 v[36:39], a[76:79], a[20:23], v[36:39]\n" \
"buffer_load_dwordx4 v68, s[40:43], s84 offen offset:0 lds\n" \
"v_mfma_f32_16x16x32_bf16 v[40:43], a[80:83], a[16:19], v[40:43]\n" \
"ds_read_b64_tr_b16 a[112:113], v102 offset:1536\n" \
"v_add_u32 v227, 144384, v224\n" \
"v_mfma_f32_16x16x32_bf16 v[40:43], a[84:87], a[20:23], v[40:43]\n" \
"s_lshl_b32 s70, s61, 15\n" \
"v_mfma_f32_16x16x32_bf16 v[44:47], a[88:91], a[16:19], v[44:47]\n" \
"s_lshl_b32 s75, s63, 18\n" \
"ds_read_b64_tr_b16 a[114:115], v102 offset:5632\n" \
"v_mfma_f32_16x16x32_bf16 v[44:47], a[92:95], a[20:23], v[44:47]\n" \
"buffer_load_dwordx4 v69, s[44:47], s84 offen offset:1024 lds\n" \
"v_mfma_f32_16x16x32_bf16 v[60:63], a[88:91], a[24:27], v[60:63]\n" \
"s_sub_u32 s77, 3932160, s75\n" \
"ds_read_b64_tr_b16 a[116:117], v102 offset:17920\n" \
"v_mfma_f32_16x16x32_bf16 v[60:63], a[92:95], a[28:31], v[60:63]\n" \
"s_lshr_b32 s71, %1, 6\n" \
"v_mfma_f32_16x16x32_bf16 v[56:59], a[80:83], a[24:27], v[56:59]\n" \
"s_xor_b32 s71, s71, s63\n" \
"ds_read_b64_tr_b16 a[118:119], v102 offset:22016\n" \
"v_mfma_f32_16x16x32_bf16 v[56:59], a[84:87], a[28:31], v[56:59]\n" \
"buffer_load_dwordx4 v70, s[40:43], s84 offen offset:2048 lds\n" \
"v_mfma_f32_16x16x32_bf16 v[52:55], a[72:75], a[24:27], v[52:55]\n" \
"s_and_b32 s71, s71, 0\n" \
"ds_read_b64_tr_b16 a[120:121], v103 offset:1536\n" \
"v_mfma_f32_16x16x32_bf16 v[52:55], a[76:79], a[28:31], v[52:55]\n" \
"s_lshl_b32 s71, s71, 7\n" \
"v_mfma_f32_16x16x32_bf16 v[48:51], a[64:67], a[24:27], v[48:51]\n" \
"s_add_u32 s70, s70, s71\n" \
"ds_read_b64_tr_b16 a[122:123], v103 offset:5632\n" \
"v_mfma_f32_16x16x32_bf16 v[48:51], a[68:71], a[28:31], v[48:51]\n" \
"buffer_load_dwordx4 v71, s[44:47], s84 offen offset:3072 lds\n" \
"v_mfma_f32_16x16x32_bf16 v[144:147], a[64:67], a[32:35], v[144:147]\n" \
"s_add_u32 s75, s75, s70\n" \
"ds_read_b64_tr_b16 a[124:125], v103 offset:17920\n" \
"v_mfma_f32_16x16x32_bf16 v[144:147], a[68:71], a[36:39], v[144:147]\n" \
"s_add_u32 s77, s77, s70\n" \
"v_mfma_f32_16x16x32_bf16 v[148:151], a[72:75], a[32:35], v[148:151]\n" \
"s_xor_b32 s84, s84, 0x380000\n" \
"ds_read_b64_tr_b16 a[126:127], v103 offset:22016\n" \
"v_mfma_f32_16x16x32_bf16 v[148:151], a[76:79], a[36:39], v[148:151]\n" \
"s_lshl_b32 s71, s71, 12\n" \
"v_mfma_f32_16x16x32_bf16 v[152:155], a[80:83], a[32:35], v[152:155]\n" \
"s_add_u32 s78, s78, s71\n" \
"v_mfma_f32_16x16x32_bf16 v[152:155], a[84:87], a[36:39], v[152:155]\n" \
"s_add_u32 s89, s89, s71\n" \
"v_lshlrev_b32 v242, 2, v78\n" \
"v_and_b32 v236, 0xf0, v234\n" \
"v_mfma_f32_16x16x32_bf16 v[156:159], a[88:91], a[32:35], v[156:159]\n" \
"v_lshl_add_u32 v236, v87, 9, v236\n" \
"v_mfma_f32_16x16x32_bf16 v[156:159], a[92:95], a[36:39], v[156:159]\n" \
"s_add_u32 s71, s74, 0x10000\n" \
"v_add_u32 v230, s71, v88\n" \
"v_xor_b32 v231, 64, v230\n" \
"v_mfma_f32_16x16x32_bf16 v[188:191], a[88:91], a[40:43], v[188:191]\n" \
"v_add_u32 v250, 0x10000, v230\n" \
"v_add_u32 v251, 0x10000, v231\n" \
"v_mfma_f32_16x16x32_bf16 v[188:191], a[92:95], a[44:47], v[188:191]\n" \
"v_lshl_add_u32 v82, v90, 12, v89\n" \
"v_add_u32 v232, %3, v82\n" \
"v_mfma_f32_16x16x32_bf16 v[184:187], a[80:83], a[40:43], v[184:187]\n" \
"v_xor_b32 v233, 32, v232\n" \
"v_mfma_f32_16x16x32_bf16 v[184:187], a[84:87], a[44:47], v[184:187]\n" \
"s_mul_i32 s87, s62, 557824\n" \
"v_mfma_f32_16x16x32_bf16 v[180:183], a[72:75], a[40:43], v[180:183]\n" \
"s_lshr_b32 s70, s61, 1\n" \
"v_mfma_f32_16x16x32_bf16 v[180:183], a[76:79], a[44:47], v[180:183]\n" \
"s_mul_i32 s70, s70, 17408\n" \
"v_mfma_f32_16x16x32_bf16 v[176:179], a[64:67], a[40:43], v[176:179]\n" \
"s_add_u32 s87, s87, s70\n" \
"v_mfma_f32_16x16x32_bf16 v[176:179], a[68:71], a[44:47], v[176:179]\n" \
"s_waitcnt lgkmcnt(0)\n" \
"s_and_b32 s70, s61, 1\n" \
"Lpg585_%=:\n" \
"Lst0x0_%=:\n" \
"s_cbranch_execz Lpg2763_%=\n" \
"v_mfma_f32_16x16x32_bf16 v[192:195], a[64:67], a[48:51], v[192:195]\n" \
"s_lshl_b32 s70, s70, 12\n" \
"v_mfma_f32_16x16x32_bf16 v[192:195], a[68:71], a[52:55], v[192:195]\n" \
"s_add_u32 s87, s87, s70\n" \
"v_mfma_f32_16x16x32_bf16 v[196:199], a[72:75], a[48:51], v[196:199]\n" \
"s_mul_i32 s70, s63, 34816\n" \
"v_mfma_f32_16x16x32_bf16 v[196:199], a[76:79], a[52:55], v[196:199]\n" \
"s_add_u32 s94, s87, 522240\n" \
"v_mfma_f32_16x16x32_bf16 v[200:203], a[80:83], a[48:51], v[200:203]\n" \
"s_sub_u32 s94, s94, s70\n" \
"v_mfma_f32_16x16x32_bf16 v[200:203], a[84:87], a[52:55], v[200:203]\n" \
"s_add_u32 s87, s87, s70\n" \
"v_mfma_f32_16x16x32_bf16 v[204:207], a[88:91], a[48:51], v[204:207]\n" \
"s_lshl_b32 s90, s62, 7\n" \
"v_mfma_f32_16x16x32_bf16 v[204:207], a[92:95], a[52:55], v[204:207]\n" \
"s_add_u32 s95, s90, 65597440\n" \
"v_mfma_f32_16x16x32_bf16 v[220:223], a[88:91], a[56:59], v[220:223]\n" \
"s_add_u32 s90, s90, 65585152\n" \
"v_mfma_f32_16x16x32_bf16 v[220:223], a[92:95], a[60:63], v[220:223]\n" \
"s_lshl_b32 s92, 0xff, s63\n" \
"v_mfma_f32_16x16x32_bf16 v[216:219], a[80:83], a[56:59], v[216:219]\n" \
"s_and_b32 s92, s92, 0xff\n" \
"v_mfma_f32_16x16x32_bf16 v[216:219], a[84:87], a[60:63], v[216:219]\n" \
"s_mov_b32 s76, 0x3e0293ee\n" \
"v_mfma_f32_16x16x32_bf16 v[212:215], a[72:75], a[56:59], v[212:215]\n" \
"s_mov_b32 s31, 0x3e800000\n" \
"v_mfma_f32_16x16x32_bf16 v[212:215], a[76:79], a[60:63], v[212:215]\n" \
"v_mfma_f32_16x16x32_bf16 v[208:211], a[64:67], a[56:59], v[208:211]\n" \
"v_mfma_f32_16x16x32_bf16 v[208:211], a[68:71], a[60:63], v[208:211]\n" \
"s_waitcnt vmcnt(10) lgkmcnt(0)\n" \
"s_barrier\n" \
"v_readfirstlane_b32 s60, v240\n" \
"s_add_u32 s60, s60, 0x9e3779b9\n" \
"v_mfma_f32_16x16x32_bf16 a[128:131], a[0:3], a[96:99], a[128:131]\n" \
"s_add_u32 m0, s74, 131072\n" \
"ds_read_b64_tr_b16 a[64:65], v98 offset:32768\n" \
"v_mfma_f32_16x16x32_bf16 a[128:131], a[4:7], a[100:103], a[128:131]\n" \
"buffer_load_dwordx4 v64, s[36:39], s81 offen offset:0 lds\n" \
"v_mfma_f32_16x16x32_bf16 a[132:135], a[0:3], a[104:107], a[132:135]\n" \
"ds_read_b64_tr_b16 a[66:67], v98 offset:36864\n" \
"v_mfma_f32_16x16x32_bf16 a[132:135], a[4:7], a[108:111], a[132:135]\n" \
"v_mfma_f32_16x16x32_bf16 a[136:139], a[0:3], a[112:115], a[136:139]\n" \
"ds_read_b64_tr_b16 a[68:69], v98 offset:49152\n" \
"v_mfma_f32_16x16x32_bf16 a[136:139], a[4:7], a[116:119], a[136:139]\n" \
"buffer_load_dwordx4 v65, s[36:39], s81 offen offset:1024 lds\n" \
"v_mfma_f32_16x16x32_bf16 a[140:143], a[0:3], a[120:123], a[140:143]\n" \
"ds_read_b64_tr_b16 a[70:71], v98 offset:53248\n" \
"v_mfma_f32_16x16x32_bf16 a[140:143], a[4:7], a[124:127], a[140:143]\n" \
"v_mfma_f32_16x16x32_bf16 a[156:159], a[8:11], a[120:123], a[156:159]\n" \
"ds_read_b128 a[0:3], v72 offset:32768\n" \
"v_mfma_f32_16x16x32_bf16 a[156:159], a[12:15], a[124:127], a[156:159]\n" \
"buffer_load_dwordx4 v66, s[36:39], s81 offen offset:2048 lds\n" \
"v_mfma_f32_16x16x32_bf16 a[152:155], a[8:11], a[112:115], a[152:155]\n" \
"ds_read_b64_tr_b16 a[72:73], v99 offset:32768\n" \
"v_mfma_f32_16x16x32_bf16 a[152:155], a[12:15], a[116:119], a[152:155]\n" \
"v_mfma_f32_16x16x32_bf16 a[148:151], a[8:11], a[104:107], a[148:151]\n" \
"ds_read_b128 a[4:7], v73 offset:32768\n" \
"v_mfma_f32_16x16x32_bf16 a[148:151], a[12:15], a[108:111], a[148:151]\n" \
"buffer_load_dwordx4 v67, s[36:39], s81 offen offset:3072 lds\n" \
"v_mfma_f32_16x16x32_bf16 a[144:147], a[8:11], a[96:99], a[144:147]\n" \
"ds_read_b64_tr_b16 a[74:75], v99 offset:36864\n" \
"v_mfma_f32_16x16x32_bf16 a[144:147], a[12:15], a[100:103], a[144:147]\n" \
"s_xor_b32 s81, s81, 0x380\n" \
"v_mfma_f32_16x16x32_bf16 a[160:163], a[16:19], a[96:99], a[160:163]\n" \
"s_add_u32 m0, s74, 135168\n" \
"ds_read_b128 a[8:11], v72 offset:34816\n" \
"v_mfma_f32_16x16x32_bf16 a[160:163], a[20:23], a[100:103], a[160:163]\n" \
"buffer_load_dwordx4 v64, s[36:39], s82 offen offset:0 lds\n" \
"v_mfma_f32_16x16x32_bf16 a[164:167], a[16:19], a[104:107], a[164:167]\n" \
"ds_read_b64_tr_b16 a[76:77], v99 offset:49152\n" \
"v_mfma_f32_16x16x32_bf16 a[164:167], a[20:23], a[108:111], a[164:167]\n" \
"v_mfma_f32_16x16x32_bf16 a[168:171], a[16:19], a[112:115], a[168:171]\n" \
"ds_read_b128 a[12:15], v73 offset:34816\n" \
"v_mfma_f32_16x16x32_bf16 a[168:171], a[20:23], a[116:119], a[168:171]\n" \
"buffer_load_dwordx4 v65, s[36:39], s82 offen offset:1024 lds\n" \
"v_mfma_f32_16x16x32_bf16 a[172:175], a[16:19], a[120:123], a[172:175]\n" \
"ds_read_b64_tr_b16 a[78:79], v99 offset:53248\n" \
"v_mfma_f32_16x16x32_bf16 a[172:175], a[20:23], a[124:127], a[172:175]\n" \
"v_mfma_f32_16x16x32_bf16 a[188:191], a[24:27], a[120:123], a[188:191]\n" \
"ds_read_b128 a[16:19], v72 offset:36864\n" \
"v_mfma_f32_16x16x32_bf16 a[188:191], a[28:31], a[124:127], a[188:191]\n" \
"buffer_load_dwordx4 v66, s[36:39], s82 offen offset:2048 lds\n" \
"v_mfma_f32_16x16x32_bf16 a[184:187], a[24:27], a[112:115], a[184:187]\n" \
"ds_read_b64_tr_b16 a[80:81], v98 offset:33280\n" \
"v_mfma_f32_16x16x32_bf16 a[184:187], a[28:31], a[116:119], a[184:187]\n" \
"v_mfma_f32_16x16x32_bf16 a[180:183], a[24:27], a[104:107], a[180:183]\n" \
"ds_read_b128 a[20:23], v73 offset:36864\n" \
"v_mfma_f32_16x16x32_bf16 a[180:183], a[28:31], a[108:111], a[180:183]\n" \
"buffer_load_dwordx4 v67, s[36:39], s82 offen offset:3072 lds\n" \
"v_mfma_f32_16x16x32_bf16 a[176:179], a[24:27], a[96:99], a[176:179]\n" \
"s_mov_b64 s[64:65], s[10:11]\n" \
"s_lshl1_add_u32 s68, s66, 512\n" \
"ds_read_b64_tr_b16 a[82:83], v98 offset:37376\n" \
"v_mfma_f32_16x16x32_bf16 a[176:179], a[28:31], a[100:103], a[176:179]\n" \
"s_add_u32 s64, s64, s68\n" \
"s_addc_u32 s65, s65, 0\n" \
"v_mfma_f32_16x16x32_bf16 a[192:195], a[32:35], a[96:99], a[192:195]\n" \
"s_xor_b32 s82, s82, 0x380\n" \
"ds_read_b128 a[24:27], v72 offset:38912\n" \
"v_mfma_f32_16x16x32_bf16 a[192:195], a[36:39], a[100:103], a[192:195]\n" \
"global_load_dword v238, v110, s[64:65]\n" \
"v_mfma_f32_16x16x32_bf16 a[196:199], a[32:35], a[104:107], a[196:199]\n" \
"s_mov_b64 s[64:65], s[14:15]\n" \
"s_add_u32 s68, s67, 0x300000\n" \
"ds_read_b64_tr_b16 a[84:85], v98 offset:49664\n" \
"v_mfma_f32_16x16x32_bf16 a[196:199], a[36:39], a[108:111], a[196:199]\n" \
"s_add_u32 s64, s64, s68\n" \
"s_addc_u32 s65, s65, 0\n" \
"v_mfma_f32_16x16x32_bf16 a[200:203], a[32:35], a[112:115], a[200:203]\n" \
"ds_read_b128 a[28:31], v73 offset:38912\n" \
"v_mfma_f32_16x16x32_bf16 a[200:203], a[36:39], a[116:119], a[200:203]\n" \
"global_load_dword v238, v234, s[64:65]\n" \
"v_mfma_f32_16x16x32_bf16 a[204:207], a[32:35], a[120:123], a[204:207]\n" \
"ds_read_b64_tr_b16 a[86:87], v98 offset:53760\n" \
"v_mfma_f32_16x16x32_bf16 a[204:207], a[36:39], a[124:127], a[204:207]\n" \
"v_mfma_f32_16x16x32_bf16 a[220:223], a[40:43], a[120:123], a[220:223]\n" \
"ds_read_b128 a[32:35], v72 offset:40960\n" \
"v_mfma_f32_16x16x32_bf16 a[220:223], a[44:47], a[124:127], a[220:223]\n" \
"v_mfma_f32_16x16x32_bf16 a[216:219], a[40:43], a[112:115], a[216:219]\n" \
"ds_read_b64_tr_b16 a[88:89], v99 offset:33280\n" \
"v_mfma_f32_16x16x32_bf16 a[216:219], a[44:47], a[116:119], a[216:219]\n" \
"v_mfma_f32_16x16x32_bf16 a[212:215], a[40:43], a[104:107], a[212:215]\n" \
"ds_read_b128 a[36:39], v73 offset:40960\n" \
"v_mfma_f32_16x16x32_bf16 a[212:215], a[44:47], a[108:111], a[212:215]\n" \
"v_mfma_f32_16x16x32_bf16 a[208:211], a[40:43], a[96:99], a[208:211]\n" \
"ds_read_b64_tr_b16 a[90:91], v99 offset:37376\n" \
"v_mfma_f32_16x16x32_bf16 a[208:211], a[44:47], a[100:103], a[208:211]\n" \
"v_mfma_f32_16x16x32_bf16 a[224:227], a[48:51], a[96:99], a[224:227]\n" \
"ds_read_b128 a[40:43], v72 offset:43008\n" \
"v_mfma_f32_16x16x32_bf16 a[224:227], a[52:55], a[100:103], a[224:227]\n" \
"v_mfma_f32_16x16x32_bf16 a[228:231], a[48:51], a[104:107], a[228:231]\n" \
"ds_read_b64_tr_b16 a[92:93], v99 offset:49664\n" \
"v_mfma_f32_16x16x32_bf16 a[228:231], a[52:55], a[108:111], a[228:231]\n" \
"v_mfma_f32_16x16x32_bf16 a[232:235], a[48:51], a[112:115], a[232:235]\n" \
"ds_read_b128 a[44:47], v73 offset:43008\n" \
"v_mfma_f32_16x16x32_bf16 a[232:235], a[52:55], a[116:119], a[232:235]\n" \
"v_mfma_f32_16x16x32_bf16 a[236:239], a[48:51], a[120:123], a[236:239]\n" \
"ds_read_b64_tr_b16 a[94:95], v99 offset:53760\n" \
"v_mfma_f32_16x16x32_bf16 a[236:239], a[52:55], a[124:127], a[236:239]\n" \
"v_mfma_f32_16x16x32_bf16 a[252:255], a[56:59], a[120:123], a[252:255]\n" \
"ds_read_b128 a[48:51], v72 offset:45056\n" \
"v_mfma_f32_16x16x32_bf16 a[252:255], a[60:63], a[124:127], a[252:255]\n" \
"v_mfma_f32_16x16x32_bf16 a[248:251], a[56:59], a[112:115], a[248:251]\n" \
"v_mfma_f32_16x16x32_bf16 a[248:251], a[60:63], a[116:119], a[248:251]\n" \
"v_mfma_f32_16x16x32_bf16 a[244:247], a[56:59], a[104:107], a[244:247]\n" \
"ds_read_b128 a[52:55], v73 offset:45056\n" \
"v_mfma_f32_16x16x32_bf16 a[244:247], a[60:63], a[108:111], a[244:247]\n" \
"v_mfma_f32_16x16x32_bf16 a[240:243], a[56:59], a[96:99], a[240:243]\n" \
"v_mfma_f32_16x16x32_bf16 a[240:243], a[60:63], a[100:103], a[240:243]\n" \
"s_waitcnt lgkmcnt(1)\n" \
"v_mfma_f32_16x16x32_bf16 v[0:3], a[64:67], a[0:3], v[0:3]\n" \
"ds_read_b128 a[56:59], v72 offset:47104\n" \
"v_lshrrev_b32 v104, 4, v234\n" \
"v_mfma_f32_16x16x32_bf16 v[0:3], a[68:71], a[4:7], v[0:3]\n" \
"v_bfe_u32 v105, v104, 4, 1\n" \
"v_bfe_u32 v106, v104, 2, 2\n" \
"v_mfma_f32_16x16x32_bf16 v[4:7], a[72:75], a[0:3], v[4:7]\n" \
"s_add_u32 m0, s74, 65536\n" \
"ds_read_b128 a[60:63], v73 offset:47104\n" \
"v_lshl_add_u32 v106, v105, 3, v106\n" \
"v_mfma_f32_16x16x32_bf16 v[4:7], a[76:79], a[4:7], v[4:7]\n" \
"buffer_load_dwordx4 v68, s[40:43], s83 offen offset:0 lds\n" \
"v_mfma_f32_16x16x32_bf16 v[8:11], a[80:83], a[0:3], v[8:11]\n" \
"s_lshl_b32 s71, s61, 4\n" \
"ds_read_b64_tr_b16 a[96:97], v98 offset:33792\n" \
"v_add_u32 v106, s71, v106\n" \
"v_mfma_f32_16x16x32_bf16 v[8:11], a[84:87], a[4:7], v[8:11]\n" \
"v_mul_u32_u24 v106, 6144, v106\n" \
"v_lshlrev_b32 v105, 1, v105\n" \
"v_mfma_f32_16x16x32_bf16 v[12:15], a[88:91], a[0:3], v[12:15]\n" \
"ds_read_b64_tr_b16 a[98:99], v98 offset:37888\n" \
"v_and_b32 v107, 3, v104\n" \
"v_mfma_f32_16x16x32_bf16 v[12:15], a[92:95], a[4:7], v[12:15]\n" \
"buffer_load_dwordx4 v69, s[44:47], s83 offen offset:1024 lds\n" \
"v_mfma_f32_16x16x32_bf16 v[28:31], a[88:91], a[8:11], v[28:31]\n" \
"ds_read_b64_tr_b16 a[100:101], v98 offset:50176\n" \
"v_xor_b32 v105, v105, v107\n" \
"v_mfma_f32_16x16x32_bf16 v[28:31], a[92:95], a[12:15], v[28:31]\n" \
"v_lshl_add_u32 v106, v105, 4, v106\n" \
"v_lshrrev_b32 v105, 5, v104\n" \
"v_mfma_f32_16x16x32_bf16 v[24:27], a[80:83], a[8:11], v[24:27]\n" \
"ds_read_b64_tr_b16 a[102:103], v98 offset:54272\n" \
"v_lshl_add_u32 v244, v105, 6, v106\n" \
"v_mfma_f32_16x16x32_bf16 v[24:27], a[84:87], a[12:15], v[24:27]\n" \
"buffer_load_dwordx4 v70, s[40:43], s83 offen offset:2048 lds\n" \
"v_mfma_f32_16x16x32_bf16 v[20:23], a[72:75], a[8:11], v[20:23]\n" \
"ds_read_b64_tr_b16 a[104:105], v99 offset:33792\n" \
"v_lshrrev_b32 v107, 3, v104\n" \
"v_mfma_f32_16x16x32_bf16 v[20:23], a[76:79], a[12:15], v[20:23]\n" \
"v_and_b32 v108, 7, v104\n" \
"v_xor_b32 v109, v108, v107\n" \
"v_mfma_f32_16x16x32_bf16 v[16:19], a[64:67], a[8:11], v[16:19]\n" \
"ds_read_b64_tr_b16 a[106:107], v99 offset:37888\n" \
"v_lshlrev_b32 v109, 4, v109\n" \
"v_mfma_f32_16x16x32_bf16 v[16:19], a[68:71], a[12:15], v[16:19]\n" \
"buffer_load_dwordx4 v71, s[44:47], s83 offen offset:3072 lds\n" \
"v_mfma_f32_16x16x32_bf16 v[32:35], a[64:67], a[16:19], v[32:35]\n" \
"ds_read_b64_tr_b16 a[108:109], v99 offset:50176\n" \
"v_lshl_add_u32 v245, v107, 12, v109\n" \
"v_mfma_f32_16x16x32_bf16 v[32:35], a[68:71], a[20:23], v[32:35]\n" \
"s_xor_b32 s83, s83, 0x80000\n" \
"v_mfma_f32_16x16x32_bf16 v[36:39], a[72:75], a[16:19], v[36:39]\n" \
"s_add_u32 m0, s74, 69632\n" \
"ds_read_b64_tr_b16 a[110:111], v99 offset:54272\n" \
"v_mfma_f32_16x16x32_bf16 v[36:39], a[76:79], a[20:23], v[36:39]\n" \
"buffer_load_dwordx4 v68, s[40:43], s84 offen offset:0 lds\n" \
"v_mfma_f32_16x16x32_bf16 v[40:43], a[80:83], a[16:19], v[40:43]\n" \
"s_lshr_b32 s71, s61, 1\n" \
"ds_read_b64_tr_b16 a[112:113], v98 offset:34304\n" \
"v_mfma_f32_16x16x32_bf16 v[40:43], a[84:87], a[20:23], v[40:43]\n" \
"s_lshl_b32 s70, s71, 11\n" \
"v_lshrrev_b32 v107, 4, v104\n" \
"v_xor_b32 v107, v107, v108\n" \
"v_mfma_f32_16x16x32_bf16 v[44:47], a[88:91], a[16:19], v[44:47]\n" \
"ds_read_b64_tr_b16 a[114:115], v98 offset:38400\n" \
"v_and_b32 v109, 15, v104\n" \
"v_mfma_f32_16x16x32_bf16 v[44:47], a[92:95], a[20:23], v[44:47]\n" \
"buffer_load_dwordx4 v69, s[44:47], s84 offen offset:1024 lds\n" \
"v_mfma_f32_16x16x32_bf16 v[60:63], a[88:91], a[24:27], v[60:63]\n" \
"ds_read_b64_tr_b16 a[116:117], v98 offset:50688\n" \
"v_lshlrev_b32 v106, 7, v109\n" \
"v_mfma_f32_16x16x32_bf16 v[60:63], a[92:95], a[28:31], v[60:63]\n" \
"v_lshl_add_u32 v106, v107, 4, v106\n" \
"v_mfma_f32_16x16x32_bf16 v[56:59], a[80:83], a[24:27], v[56:59]\n" \
"s_add_u32 s70, s70, %3\n" \
"ds_read_b64_tr_b16 a[118:119], v98 offset:54784\n" \
"v_add_u32 v247, s70, v106\n" \
"v_mfma_f32_16x16x32_bf16 v[56:59], a[84:87], a[28:31], v[56:59]\n" \
"buffer_load_dwordx4 v70, s[40:43], s84 offen offset:2048 lds\n" \
"v_mfma_f32_16x16x32_bf16 v[52:55], a[72:75], a[24:27], v[52:55]\n" \
"ds_read_b64_tr_b16 a[120:121], v99 offset:34304\n" \
"v_lshrrev_b32 v109, 3, v109\n" \
"v_mfma_f32_16x16x32_bf16 v[52:55], a[76:79], a[28:31], v[52:55]\n" \
"v_mul_u32_u24 v109, 7168, v109\n" \
"v_add_u32 v109, v109, v106\n" \
"v_mfma_f32_16x16x32_bf16 v[48:51], a[64:67], a[24:27], v[48:51]\n" \
"s_add_u32 s70, %3, 131072\n" \
"ds_read_b64_tr_b16 a[122:123], v99 offset:38400\n" \
"v_add_u32 v248, s70, v109\n" \
"v_mfma_f32_16x16x32_bf16 v[48:51], a[68:71], a[28:31], v[48:51]\n" \
"buffer_load_dwordx4 v71, s[44:47], s84 offen offset:3072 lds\n" \
"v_mfma_f32_16x16x32_bf16 v[144:147], a[64:67], a[32:35], v[144:147]\n" \
"ds_read_b64_tr_b16 a[124:125], v99 offset:50688\n" \
"v_and_b32 v106, 3, v104\n" \
"v_mfma_f32_16x16x32_bf16 v[144:147], a[68:71], a[36:39], v[144:147]\n" \
"v_lshlrev_b32 v106, 3, v106\n" \
"v_bfe_u32 v107, v104, 2, 2\n" \
"v_mfma_f32_16x16x32_bf16 v[148:151], a[72:75], a[32:35], v[148:151]\n" \
"s_xor_b32 s84, s84, 0x80000\n" \
"ds_read_b64_tr_b16 a[126:127], v99 offset:54784\n" \
"v_lshl_add_u32 v106, v107, 6, v106\n" \
"v_mfma_f32_16x16x32_bf16 v[148:151], a[76:79], a[36:39], v[148:151]\n" \
"v_bfe_u32 v107, v104, 4, 1\n" \
"v_mul_u32_u24 v107, 288, v107\n" \
"v_mfma_f32_16x16x32_bf16 v[152:155], a[80:83], a[32:35], v[152:155]\n" \
"v_mul_u32_u24 v108, 6144, v105\n" \
"v_add3_u32 v106, v106, v107, v108\n" \
"v_mfma_f32_16x16x32_bf16 v[152:155], a[84:87], a[36:39], v[152:155]\n" \
"s_and_b32 s70, s61, 1\n" \
"v_mfma_f32_16x16x32_bf16 v[156:159], a[88:91], a[32:35], v[156:159]\n" \
"s_lshl_b32 s70, s70, 9\n" \
"v_mfma_f32_16x16x32_bf16 v[156:159], a[92:95], a[36:39], v[156:159]\n" \
"s_add_u32 s70, s70, %3\n" \
"v_add_u32 v249, s70, v106\n" \
"v_mfma_f32_16x16x32_bf16 v[188:191], a[88:91], a[40:43], v[188:191]\n" \
"s_lshl_b32 s70, s71, 5\n" \
"v_xor_b32 v249, s70, v249\n" \
"v_mfma_f32_16x16x32_bf16 v[188:191], a[92:95], a[44:47], v[188:191]\n" \
"v_mfma_f32_16x16x32_bf16 v[184:187], a[80:83], a[40:43], v[184:187]\n" \
"v_mfma_f32_16x16x32_bf16 v[184:187], a[84:87], a[44:47], v[184:187]\n" \
"v_mfma_f32_16x16x32_bf16 v[180:183], a[72:75], a[40:43], v[180:183]\n" \
"v_mfma_f32_16x16x32_bf16 v[180:183], a[76:79], a[44:47], v[180:183]\n" \
"v_mfma_f32_16x16x32_bf16 v[176:179], a[64:67], a[40:43], v[176:179]\n" \
"v_mfma_f32_16x16x32_bf16 v[176:179], a[68:71], a[44:47], v[176:179]\n" \
"s_waitcnt lgkmcnt(0)\n" \
"v_mfma_f32_16x16x32_bf16 v[192:195], a[64:67], a[48:51], v[192:195]\n" \
"v_mfma_f32_16x16x32_bf16 v[192:195], a[68:71], a[52:55], v[192:195]\n" \
"v_mfma_f32_16x16x32_bf16 v[196:199], a[72:75], a[48:51], v[196:199]\n" \
"v_mfma_f32_16x16x32_bf16 v[196:199], a[76:79], a[52:55], v[196:199]\n" \
"v_mfma_f32_16x16x32_bf16 v[200:203], a[80:83], a[48:51], v[200:203]\n" \
"v_mfma_f32_16x16x32_bf16 v[200:203], a[84:87], a[52:55], v[200:203]\n" \
"v_mfma_f32_16x16x32_bf16 v[204:207], a[88:91], a[48:51], v[204:207]\n" \
"v_mfma_f32_16x16x32_bf16 v[204:207], a[92:95], a[52:55], v[204:207]\n" \
"v_mfma_f32_16x16x32_bf16 v[220:223], a[88:91], a[56:59], v[220:223]\n" \
"v_mfma_f32_16x16x32_bf16 v[220:223], a[92:95], a[60:63], v[220:223]\n" \
"v_mfma_f32_16x16x32_bf16 v[216:219], a[80:83], a[56:59], v[216:219]\n" \
"v_mfma_f32_16x16x32_bf16 v[216:219], a[84:87], a[60:63], v[216:219]\n" \
"v_mfma_f32_16x16x32_bf16 v[212:215], a[72:75], a[56:59], v[212:215]\n" \
"v_mfma_f32_16x16x32_bf16 v[212:215], a[76:79], a[60:63], v[212:215]\n" \
"v_mfma_f32_16x16x32_bf16 v[208:211], a[64:67], a[56:59], v[208:211]\n" \
"v_mfma_f32_16x16x32_bf16 v[208:211], a[68:71], a[60:63], v[208:211]\n" \
"s_waitcnt vmcnt(10) lgkmcnt(0)\n" \
"s_barrier\n" \
"v_mfma_f32_16x16x32_bf16 a[128:131], a[0:3], a[96:99], a[128:131]\n" \
"s_add_u32 m0, s74, 98304\n" \
"ds_read_b64_tr_b16 a[64:65], v74 offset:0\n" \
"v_mfma_f32_16x16x32_bf16 a[128:131], a[4:7], a[100:103], a[128:131]\n" \
"buffer_load_dwordx4 v64, s[36:39], s81 offen offset:0 lds\n" \
"v_mfma_f32_16x16x32_bf16 a[132:135], a[0:3], a[104:107], a[132:135]\n" \
"ds_read_b64_tr_b16 a[66:67], v74 offset:4096\n" \
"v_mfma_f32_16x16x32_bf16 a[132:135], a[4:7], a[108:111], a[132:135]\n" \
"v_mfma_f32_16x16x32_bf16 a[136:139], a[0:3], a[112:115], a[136:139]\n" \
"ds_read_b64_tr_b16 a[68:69], v74 offset:16384\n" \
"v_mfma_f32_16x16x32_bf16 a[136:139], a[4:7], a[116:119], a[136:139]\n" \
"buffer_load_dwordx4 v65, s[36:39], s81 offen offset:1024 lds\n" \
"v_mfma_f32_16x16x32_bf16 a[140:143], a[0:3], a[120:123], a[140:143]\n" \
"ds_read_b64_tr_b16 a[70:71], v74 offset:20480\n" \
"v_mfma_f32_16x16x32_bf16 a[140:143], a[4:7], a[124:127], a[140:143]\n" \
"v_mfma_f32_16x16x32_bf16 a[156:159], a[8:11], a[120:123], a[156:159]\n" \
"ds_read_b128 a[0:3], v100 offset:0\n" \
"v_mfma_f32_16x16x32_bf16 a[156:159], a[12:15], a[124:127], a[156:159]\n" \
"buffer_load_dwordx4 v66, s[36:39], s81 offen offset:2048 lds\n" \
"v_mfma_f32_16x16x32_bf16 a[152:155], a[8:11], a[112:115], a[152:155]\n" \
"ds_read_b64_tr_b16 a[72:73], v75 offset:0\n" \
"v_mfma_f32_16x16x32_bf16 a[152:155], a[12:15], a[116:119], a[152:155]\n" \
"v_mfma_f32_16x16x32_bf16 a[148:151], a[8:11], a[104:107], a[148:151]\n" \
"ds_read_b128 a[4:7], v101 offset:0\n" \
"v_mfma_f32_16x16x32_bf16 a[148:151], a[12:15], a[108:111], a[148:151]\n" \
"buffer_load_dwordx4 v67, s[36:39], s81 offen offset:3072 lds\n" \
"v_mfma_f32_16x16x32_bf16 a[144:147], a[8:11], a[96:99], a[144:147]\n" \
"ds_read_b64_tr_b16 a[74:75], v75 offset:4096\n" \
"v_mfma_f32_16x16x32_bf16 a[144:147], a[12:15], a[100:103], a[144:147]\n" \
"s_xor_b32 s81, s81, 0x80\n" \
"v_mfma_f32_16x16x32_bf16 a[160:163], a[16:19], a[96:99], a[160:163]\n" \
"s_add_u32 m0, s74, 102400\n" \
"ds_read_b128 a[8:11], v100 offset:2048\n" \
"v_mfma_f32_16x16x32_bf16 a[160:163], a[20:23], a[100:103], a[160:163]\n" \
"buffer_load_dwordx4 v64, s[36:39], s82 offen offset:0 lds\n" \
"v_mfma_f32_16x16x32_bf16 a[164:167], a[16:19], a[104:107], a[164:167]\n" \
"ds_read_b64_tr_b16 a[76:77], v75 offset:16384\n" \
"v_mfma_f32_16x16x32_bf16 a[164:167], a[20:23], a[108:111], a[164:167]\n" \
"v_mfma_f32_16x16x32_bf16 a[168:171], a[16:19], a[112:115], a[168:171]\n" \
"ds_read_b128 a[12:15], v101 offset:2048\n" \
"v_mfma_f32_16x16x32_bf16 a[168:171], a[20:23], a[116:119], a[168:171]\n" \
"buffer_load_dwordx4 v65, s[36:39], s82 offen offset:1024 lds\n" \
"v_mfma_f32_16x16x32_bf16 a[172:175], a[16:19], a[120:123], a[172:175]\n" \
"ds_read_b64_tr_b16 a[78:79], v75 offset:20480\n" \
"v_mfma_f32_16x16x32_bf16 a[172:175], a[20:23], a[124:127], a[172:175]\n" \
"v_mfma_f32_16x16x32_bf16 a[188:191], a[24:27], a[120:123], a[188:191]\n" \
"ds_read_b128 a[16:19], v100 offset:4096\n" \
"v_mfma_f32_16x16x32_bf16 a[188:191], a[28:31], a[124:127], a[188:191]\n" \
"buffer_load_dwordx4 v66, s[36:39], s82 offen offset:2048 lds\n" \
"v_mfma_f32_16x16x32_bf16 a[184:187], a[24:27], a[112:115], a[184:187]\n" \
"ds_read_b64_tr_b16 a[80:81], v74 offset:512\n" \
"v_mfma_f32_16x16x32_bf16 a[184:187], a[28:31], a[116:119], a[184:187]\n" \
"v_mfma_f32_16x16x32_bf16 a[180:183], a[24:27], a[104:107], a[180:183]\n" \
"ds_read_b128 a[20:23], v101 offset:4096\n" \
"v_mfma_f32_16x16x32_bf16 a[180:183], a[28:31], a[108:111], a[180:183]\n" \
"buffer_load_dwordx4 v67, s[36:39], s82 offen offset:3072 lds\n" \
"v_mfma_f32_16x16x32_bf16 a[176:179], a[24:27], a[96:99], a[176:179]\n" \
"s_mov_b64 s[64:65], s[16:17]\n" \
"s_add_u32 s68, s67, 0x300000\n" \
"ds_read_b64_tr_b16 a[82:83], v74 offset:4608\n" \
"v_mfma_f32_16x16x32_bf16 a[176:179], a[28:31], a[100:103], a[176:179]\n" \
"s_add_u32 s64, s64, s68\n" \
"s_addc_u32 s65, s65, 0\n" \
"v_mfma_f32_16x16x32_bf16 a[192:195], a[32:35], a[96:99], a[192:195]\n" \
"s_xor_b32 s82, s82, 0x80\n" \
"ds_read_b128 a[24:27], v100 offset:6144\n" \
"v_mfma_f32_16x16x32_bf16 a[192:195], a[36:39], a[100:103], a[192:195]\n" \
"global_load_dword v238, v234, s[64:65]\n" \
"v_mfma_f32_16x16x32_bf16 a[196:199], a[32:35], a[104:107], a[196:199]\n" \
"s_mov_b64 s[64:65], s[8:9]\n" \
"s_add_u32 s68, s66, 0x0\n" \
"ds_read_b64_tr_b16 a[84:85], v74 offset:16896\n" \
"v_mfma_f32_16x16x32_bf16 a[196:199], a[36:39], a[108:111], a[196:199]\n" \
"s_add_u32 s64, s64, s68\n" \
"s_addc_u32 s65, s65, 0\n" \
"v_mfma_f32_16x16x32_bf16 a[200:203], a[32:35], a[112:115], a[200:203]\n" \
"ds_read_b128 a[28:31], v101 offset:6144\n" \
"v_mfma_f32_16x16x32_bf16 a[200:203], a[36:39], a[116:119], a[200:203]\n" \
"global_load_dword v238, v234, s[64:65]\n" \
"v_mfma_f32_16x16x32_bf16 a[204:207], a[32:35], a[120:123], a[204:207]\n" \
"s_mov_b64 s[64:65], s[8:9]\n" \
"s_add_u32 s68, s66, 0x100000\n" \
"ds_read_b64_tr_b16 a[86:87], v74 offset:20992\n" \
"v_mfma_f32_16x16x32_bf16 a[204:207], a[36:39], a[124:127], a[204:207]\n" \
"s_add_u32 s64, s64, s68\n" \
"s_addc_u32 s65, s65, 0\n" \
"v_mfma_f32_16x16x32_bf16 a[220:223], a[40:43], a[120:123], a[220:223]\n" \
"ds_read_b128 a[32:35], v100 offset:8192\n" \
"v_mfma_f32_16x16x32_bf16 a[220:223], a[44:47], a[124:127], a[220:223]\n" \
"global_load_dword v238, v234, s[64:65]\n" \
"v_mfma_f32_16x16x32_bf16 a[216:219], a[40:43], a[112:115], a[216:219]\n" \
"ds_read_b64_tr_b16 a[88:89], v75 offset:512\n" \
"v_mfma_f32_16x16x32_bf16 a[216:219], a[44:47], a[116:119], a[216:219]\n" \
"v_mfma_f32_16x16x32_bf16 a[212:215], a[40:43], a[104:107], a[212:215]\n" \
"ds_read_b128 a[36:39], v101 offset:8192\n" \
"v_mfma_f32_16x16x32_bf16 a[212:215], a[44:47], a[108:111], a[212:215]\n" \
"v_mfma_f32_16x16x32_bf16 a[208:211], a[40:43], a[96:99], a[208:211]\n" \
"ds_read_b64_tr_b16 a[90:91], v75 offset:4608\n" \
"v_mfma_f32_16x16x32_bf16 a[208:211], a[44:47], a[100:103], a[208:211]\n" \
"v_mfma_f32_16x16x32_bf16 a[224:227], a[48:51], a[96:99], a[224:227]\n" \
"ds_read_b128 a[40:43], v100 offset:10240\n" \
"v_mfma_f32_16x16x32_bf16 a[224:227], a[52:55], a[100:103], a[224:227]\n" \
"v_mfma_f32_16x16x32_bf16 a[228:231], a[48:51], a[104:107], a[228:231]\n" \
"ds_read_b64_tr_b16 a[92:93], v75 offset:16896\n" \
"v_mfma_f32_16x16x32_bf16 a[228:231], a[52:55], a[108:111], a[228:231]\n" \
"v_mfma_f32_16x16x32_bf16 a[232:235], a[48:51], a[112:115], a[232:235]\n" \
"ds_read_b128 a[44:47], v101 offset:10240\n" \
"v_mfma_f32_16x16x32_bf16 a[232:235], a[52:55], a[116:119], a[232:235]\n" \
"v_mfma_f32_16x16x32_bf16 a[236:239], a[48:51], a[120:123], a[236:239]\n" \
"ds_read_b64_tr_b16 a[94:95], v75 offset:20992\n" \
"v_mfma_f32_16x16x32_bf16 a[236:239], a[52:55], a[124:127], a[236:239]\n" \
"v_mfma_f32_16x16x32_bf16 a[252:255], a[56:59], a[120:123], a[252:255]\n" \
"ds_read_b128 a[48:51], v100 offset:12288\n" \
"v_mfma_f32_16x16x32_bf16 a[252:255], a[60:63], a[124:127], a[252:255]\n" \
"v_mfma_f32_16x16x32_bf16 a[248:251], a[56:59], a[112:115], a[248:251]\n" \
"v_mfma_f32_16x16x32_bf16 a[248:251], a[60:63], a[116:119], a[248:251]\n" \
"v_mfma_f32_16x16x32_bf16 a[244:247], a[56:59], a[104:107], a[244:247]\n" \
"ds_read_b128 a[52:55], v101 offset:12288\n" \
"v_mfma_f32_16x16x32_bf16 a[244:247], a[60:63], a[108:111], a[244:247]\n" \
"v_mfma_f32_16x16x32_bf16 a[240:243], a[56:59], a[96:99], a[240:243]\n" \
"v_mfma_f32_16x16x32_bf16 a[240:243], a[60:63], a[100:103], a[240:243]\n" \
"s_waitcnt lgkmcnt(1)\n" \
"v_mfma_f32_16x16x32_bf16 v[0:3], a[64:67], a[0:3], v[0:3]\n" \
"ds_read_b128 a[56:59], v100 offset:14336\n" \
"v_mfma_f32_16x16x32_bf16 v[0:3], a[68:71], a[4:7], v[0:3]\n" \
"v_mfma_f32_16x16x32_bf16 v[4:7], a[72:75], a[0:3], v[4:7]\n" \
"s_add_u32 m0, s74, 32768\n" \
"ds_read_b128 a[60:63], v101 offset:14336\n" \
"v_mfma_f32_16x16x32_bf16 v[4:7], a[76:79], a[4:7], v[4:7]\n" \
"buffer_load_dwordx4 v68, s[40:43], s83 offen offset:0 lds\n" \
"v_mfma_f32_16x16x32_bf16 v[8:11], a[80:83], a[0:3], v[8:11]\n" \
"ds_read_b64_tr_b16 a[96:97], v74 offset:1024\n" \
"v_mfma_f32_16x16x32_bf16 v[8:11], a[84:87], a[4:7], v[8:11]\n" \
"v_mfma_f32_16x16x32_bf16 v[12:15], a[88:91], a[0:3], v[12:15]\n" \
"ds_read_b64_tr_b16 a[98:99], v74 offset:5120\n" \
"v_mfma_f32_16x16x32_bf16 v[12:15], a[92:95], a[4:7], v[12:15]\n" \
"buffer_load_dwordx4 v69, s[44:47], s83 offen offset:1024 lds\n" \
"v_mfma_f32_16x16x32_bf16 v[28:31], a[88:91], a[8:11], v[28:31]\n" \
"ds_read_b64_tr_b16 a[100:101], v74 offset:17408\n" \
"v_mfma_f32_16x16x32_bf16 v[28:31], a[92:95], a[12:15], v[28:31]\n" \
"v_mfma_f32_16x16x32_bf16 v[24:27], a[80:83], a[8:11], v[24:27]\n" \
"ds_read_b64_tr_b16 a[102:103], v74 offset:21504\n" \
"v_mfma_f32_16x16x32_bf16 v[24:27], a[84:87], a[12:15], v[24:27]\n" \
"buffer_load_dwordx4 v70, s[40:43], s83 offen offset:2048 lds\n" \
"v_mfma_f32_16x16x32_bf16 v[20:23], a[72:75], a[8:11], v[20:23]\n" \
"ds_read_b64_tr_b16 a[104:105], v75 offset:1024\n" \
"v_mfma_f32_16x16x32_bf16 v[20:23], a[76:79], a[12:15], v[20:23]\n" \
"v_mfma_f32_16x16x32_bf16 v[16:19], a[64:67], a[8:11], v[16:19]\n" \
"ds_read_b64_tr_b16 a[106:107], v75 offset:5120\n" \
"v_mfma_f32_16x16x32_bf16 v[16:19], a[68:71], a[12:15], v[16:19]\n" \
"buffer_load_dwordx4 v71, s[44:47], s83 offen offset:3072 lds\n" \
"v_mfma_f32_16x16x32_bf16 v[32:35], a[64:67], a[16:19], v[32:35]\n" \
"ds_read_b64_tr_b16 a[108:109], v75 offset:17408\n" \
"v_mfma_f32_16x16x32_bf16 v[32:35], a[68:71], a[20:23], v[32:35]\n" \
"s_xor_b32 s83, s83, 0x180000\n" \
"v_mfma_f32_16x16x32_bf16 v[36:39], a[72:75], a[16:19], v[36:39]\n" \
"s_add_u32 m0, s74, 36864\n" \
"ds_read_b64_tr_b16 a[110:111], v75 offset:21504\n" \
"v_mfma_f32_16x16x32_bf16 v[36:39], a[76:79], a[20:23], v[36:39]\n" \
"buffer_load_dwordx4 v68, s[40:43], s84 offen offset:0 lds\n" \
"v_mfma_f32_16x16x32_bf16 v[40:43], a[80:83], a[16:19], v[40:43]\n" \
"ds_read_b64_tr_b16 a[112:113], v74 offset:1536\n" \
"v_mfma_f32_16x16x32_bf16 v[40:43], a[84:87], a[20:23], v[40:43]\n" \
"v_mfma_f32_16x16x32_bf16 v[44:47], a[88:91], a[16:19], v[44:47]\n" \
"ds_read_b64_tr_b16 a[114:115], v74 offset:5632\n" \
"v_mfma_f32_16x16x32_bf16 v[44:47], a[92:95], a[20:23], v[44:47]\n" \
"buffer_load_dwordx4 v69, s[44:47], s84 offen offset:1024 lds\n" \
"v_mfma_f32_16x16x32_bf16 v[60:63], a[88:91], a[24:27], v[60:63]\n" \
"ds_read_b64_tr_b16 a[116:117], v74 offset:17920\n" \
"v_mfma_f32_16x16x32_bf16 v[60:63], a[92:95], a[28:31], v[60:63]\n" \
"v_mfma_f32_16x16x32_bf16 v[56:59], a[80:83], a[24:27], v[56:59]\n" \
"ds_read_b64_tr_b16 a[118:119], v74 offset:22016\n" \
"v_mfma_f32_16x16x32_bf16 v[56:59], a[84:87], a[28:31], v[56:59]\n" \
"buffer_load_dwordx4 v70, s[40:43], s84 offen offset:2048 lds\n" \
"v_mfma_f32_16x16x32_bf16 v[52:55], a[72:75], a[24:27], v[52:55]\n" \
"ds_read_b64_tr_b16 a[120:121], v75 offset:1536\n" \
"v_mfma_f32_16x16x32_bf16 v[52:55], a[76:79], a[28:31], v[52:55]\n" \
"v_mfma_f32_16x16x32_bf16 v[48:51], a[64:67], a[24:27], v[48:51]\n" \
"ds_read_b64_tr_b16 a[122:123], v75 offset:5632\n" \
"v_mfma_f32_16x16x32_bf16 v[48:51], a[68:71], a[28:31], v[48:51]\n" \
"buffer_load_dwordx4 v71, s[44:47], s84 offen offset:3072 lds\n" \
"v_mfma_f32_16x16x32_bf16 v[144:147], a[64:67], a[32:35], v[144:147]\n" \
"ds_read_b64_tr_b16 a[124:125], v75 offset:17920\n" \
"v_mfma_f32_16x16x32_bf16 v[144:147], a[68:71], a[36:39], v[144:147]\n" \
"v_mfma_f32_16x16x32_bf16 v[148:151], a[72:75], a[32:35], v[148:151]\n" \
"s_xor_b32 s84, s84, 0x180000\n" \
"ds_read_b64_tr_b16 a[126:127], v75 offset:22016\n" \
"v_mfma_f32_16x16x32_bf16 v[148:151], a[76:79], a[36:39], v[148:151]\n" \
"v_mfma_f32_16x16x32_bf16 v[152:155], a[80:83], a[32:35], v[152:155]\n" \
"v_mfma_f32_16x16x32_bf16 v[152:155], a[84:87], a[36:39], v[152:155]\n" \
"v_mfma_f32_16x16x32_bf16 v[156:159], a[88:91], a[32:35], v[156:159]\n" \
"v_mfma_f32_16x16x32_bf16 v[156:159], a[92:95], a[36:39], v[156:159]\n" \
"v_mfma_f32_16x16x32_bf16 v[188:191], a[88:91], a[40:43], v[188:191]\n" \
"v_mfma_f32_16x16x32_bf16 v[188:191], a[92:95], a[44:47], v[188:191]\n" \
"v_mfma_f32_16x16x32_bf16 v[184:187], a[80:83], a[40:43], v[184:187]\n" \
"v_mfma_f32_16x16x32_bf16 v[184:187], a[84:87], a[44:47], v[184:187]\n" \
"v_mfma_f32_16x16x32_bf16 v[180:183], a[72:75], a[40:43], v[180:183]\n" \
"v_mfma_f32_16x16x32_bf16 v[180:183], a[76:79], a[44:47], v[180:183]\n" \
"v_mfma_f32_16x16x32_bf16 v[176:179], a[64:67], a[40:43], v[176:179]\n" \
"v_mfma_f32_16x16x32_bf16 v[176:179], a[68:71], a[44:47], v[176:179]\n" \
"s_waitcnt lgkmcnt(0)\n" \
"v_mfma_f32_16x16x32_bf16 v[192:195], a[64:67], a[48:51], v[192:195]\n" \
"v_mfma_f32_16x16x32_bf16 v[192:195], a[68:71], a[52:55], v[192:195]\n" \
"v_mfma_f32_16x16x32_bf16 v[196:199], a[72:75], a[48:51], v[196:199]\n" \
"v_mfma_f32_16x16x32_bf16 v[196:199], a[76:79], a[52:55], v[196:199]\n" \
"v_mfma_f32_16x16x32_bf16 v[200:203], a[80:83], a[48:51], v[200:203]\n" \
"v_mfma_f32_16x16x32_bf16 v[200:203], a[84:87], a[52:55], v[200:203]\n" \
"v_mfma_f32_16x16x32_bf16 v[204:207], a[88:91], a[48:51], v[204:207]\n" \
"v_mfma_f32_16x16x32_bf16 v[204:207], a[92:95], a[52:55], v[204:207]\n" \
"v_mfma_f32_16x16x32_bf16 v[220:223], a[88:91], a[56:59], v[220:223]\n" \
"v_mfma_f32_16x16x32_bf16 v[220:223], a[92:95], a[60:63], v[220:223]\n" \
"v_mfma_f32_16x16x32_bf16 v[216:219], a[80:83], a[56:59], v[216:219]\n" \
"v_mfma_f32_16x16x32_bf16 v[216:219], a[84:87], a[60:63], v[216:219]\n" \
"v_mfma_f32_16x16x32_bf16 v[212:215], a[72:75], a[56:59], v[212:215]\n" \
"v_mfma_f32_16x16x32_bf16 v[212:215], a[76:79], a[60:63], v[212:215]\n" \
"v_mfma_f32_16x16x32_bf16 v[208:211], a[64:67], a[56:59], v[208:211]\n" \
"v_mfma_f32_16x16x32_bf16 v[208:211], a[68:71], a[60:63], v[208:211]\n" \
"s_waitcnt vmcnt(11) lgkmcnt(0)\n" \
"s_barrier\n" \
"v_mfma_f32_16x16x32_bf16 a[128:131], a[0:3], a[96:99], a[128:131]\n" \
"s_add_u32 m0, s74, 0\n" \
"ds_read_b64_tr_b16 a[64:65], v98 offset:0\n" \
"v_mfma_f32_16x16x32_bf16 a[128:131], a[4:7], a[100:103], a[128:131]\n" \
"buffer_load_dwordx4 v64, s[36:39], s81 offen offset:0 lds\n" \
"v_mfma_f32_16x16x32_bf16 a[132:135], a[0:3], a[104:107], a[132:135]\n" \
"ds_read_b64_tr_b16 a[66:67], v98 offset:4096\n" \
"v_mfma_f32_16x16x32_bf16 a[132:135], a[4:7], a[108:111], a[132:135]\n" \
"v_mfma_f32_16x16x32_bf16 a[136:139], a[0:3], a[112:115], a[136:139]\n" \
"ds_read_b64_tr_b16 a[68:69], v98 offset:16384\n" \
"v_mfma_f32_16x16x32_bf16 a[136:139], a[4:7], a[116:119], a[136:139]\n" \
"buffer_load_dwordx4 v65, s[36:39], s81 offen offset:1024 lds\n" \
"v_mfma_f32_16x16x32_bf16 a[140:143], a[0:3], a[120:123], a[140:143]\n" \
"ds_read_b64_tr_b16 a[70:71], v98 offset:20480\n" \
"v_mfma_f32_16x16x32_bf16 a[140:143], a[4:7], a[124:127], a[140:143]\n" \
"v_mfma_f32_16x16x32_bf16 a[156:159], a[8:11], a[120:123], a[156:159]\n" \
"ds_read_b128 a[0:3], v96 offset:32768\n" \
"v_mfma_f32_16x16x32_bf16 a[156:159], a[12:15], a[124:127], a[156:159]\n" \
"buffer_load_dwordx4 v66, s[36:39], s81 offen offset:2048 lds\n" \
"v_mfma_f32_16x16x32_bf16 a[152:155], a[8:11], a[112:115], a[152:155]\n" \
"ds_read_b64_tr_b16 a[72:73], v99 offset:0\n" \
"v_mfma_f32_16x16x32_bf16 a[152:155], a[12:15], a[116:119], a[152:155]\n" \
"v_mfma_f32_16x16x32_bf16 a[148:151], a[8:11], a[104:107], a[148:151]\n" \
"ds_read_b128 a[4:7], v97 offset:32768\n" \
"v_mfma_f32_16x16x32_bf16 a[148:151], a[12:15], a[108:111], a[148:151]\n" \
"buffer_load_dwordx4 v67, s[36:39], s81 offen offset:3072 lds\n" \
"v_mfma_f32_16x16x32_bf16 a[144:147], a[8:11], a[96:99], a[144:147]\n" \
"ds_read_b64_tr_b16 a[74:75], v99 offset:4096\n" \
"v_mfma_f32_16x16x32_bf16 a[144:147], a[12:15], a[100:103], a[144:147]\n" \
"s_xor_b32 s81, s81, 0x180\n" \
"v_mfma_f32_16x16x32_bf16 a[160:163], a[16:19], a[96:99], a[160:163]\n" \
"Lpg1129_%=:\n" \
"Lst0x1_%=:\n" \
"s_cbranch_execz Lpg3339_%=\n" \
"s_add_u32 m0, s74, 4096\n" \
"ds_read_b128 a[8:11], v96 offset:34816\n" \
"v_mfma_f32_16x16x32_bf16 a[160:163], a[20:23], a[100:103], a[160:163]\n" \
"buffer_load_dwordx4 v64, s[36:39], s82 offen offset:0 lds\n" \
"v_mfma_f32_16x16x32_bf16 a[164:167], a[16:19], a[104:107], a[164:167]\n" \
"ds_read_b64_tr_b16 a[76:77], v99 offset:16384\n" \
"v_mfma_f32_16x16x32_bf16 a[164:167], a[20:23], a[108:111], a[164:167]\n" \
"v_mfma_f32_16x16x32_bf16 a[168:171], a[16:19], a[112:115], a[168:171]\n" \
"ds_read_b128 a[12:15], v97 offset:34816\n" \
"v_mfma_f32_16x16x32_bf16 a[168:171], a[20:23], a[116:119], a[168:171]\n" \
"buffer_load_dwordx4 v65, s[36:39], s82 offen offset:1024 lds\n" \
"v_mfma_f32_16x16x32_bf16 a[172:175], a[16:19], a[120:123], a[172:175]\n" \
"ds_read_b64_tr_b16 a[78:79], v99 offset:20480\n" \
"v_mfma_f32_16x16x32_bf16 a[172:175], a[20:23], a[124:127], a[172:175]\n" \
"v_mfma_f32_16x16x32_bf16 a[188:191], a[24:27], a[120:123], a[188:191]\n" \
"ds_read_b128 a[16:19], v96 offset:36864\n" \
"v_mfma_f32_16x16x32_bf16 a[188:191], a[28:31], a[124:127], a[188:191]\n" \
"buffer_load_dwordx4 v66, s[36:39], s82 offen offset:2048 lds\n" \
"v_mfma_f32_16x16x32_bf16 a[184:187], a[24:27], a[112:115], a[184:187]\n" \
"ds_read_b64_tr_b16 a[80:81], v98 offset:512\n" \
"v_mfma_f32_16x16x32_bf16 a[184:187], a[28:31], a[116:119], a[184:187]\n" \
"v_mfma_f32_16x16x32_bf16 a[180:183], a[24:27], a[104:107], a[180:183]\n" \
"ds_read_b128 a[20:23], v97 offset:36864\n" \
"v_mfma_f32_16x16x32_bf16 a[180:183], a[28:31], a[108:111], a[180:183]\n" \
"buffer_load_dwordx4 v67, s[36:39], s82 offen offset:3072 lds\n" \
"v_mfma_f32_16x16x32_bf16 a[176:179], a[24:27], a[96:99], a[176:179]\n" \
"s_mov_b64 s[64:65], s[8:9]\n" \
"s_add_u32 s68, s66, 0x200000\n" \
"ds_read_b64_tr_b16 a[82:83], v98 offset:4608\n" \
"v_mfma_f32_16x16x32_bf16 a[176:179], a[28:31], a[100:103], a[176:179]\n" \
"s_add_u32 s64, s64, s68\n" \
"s_addc_u32 s65, s65, 0\n" \
"v_mfma_f32_16x16x32_bf16 a[192:195], a[32:35], a[96:99], a[192:195]\n" \
"s_xor_b32 s82, s82, 0x180\n" \
"ds_read_b128 a[24:27], v96 offset:38912\n" \
"v_mfma_f32_16x16x32_bf16 a[192:195], a[36:39], a[100:103], a[192:195]\n" \
"global_load_dword v238, v234, s[64:65]\n" \
"v_mfma_f32_16x16x32_bf16 a[196:199], a[32:35], a[104:107], a[196:199]\n" \
"s_mov_b64 s[64:65], s[8:9]\n" \
"s_add_u32 s68, s66, 0x300000\n" \
"ds_read_b64_tr_b16 a[84:85], v98 offset:16896\n" \
"v_mfma_f32_16x16x32_bf16 a[196:199], a[36:39], a[108:111], a[196:199]\n" \
"s_add_u32 s64, s64, s68\n" \
"s_addc_u32 s65, s65, 0\n" \
"v_mfma_f32_16x16x32_bf16 a[200:203], a[32:35], a[112:115], a[200:203]\n" \
"ds_read_b128 a[28:31], v97 offset:38912\n" \
"v_mfma_f32_16x16x32_bf16 a[200:203], a[36:39], a[116:119], a[200:203]\n" \
"global_load_dword v238, v234, s[64:65]\n" \
"v_mfma_f32_16x16x32_bf16 a[204:207], a[32:35], a[120:123], a[204:207]\n" \
"s_mov_b64 s[64:65], s[12:13]\n" \
"s_add_u32 s68, s67, 0x0\n" \
"ds_read_b64_tr_b16 a[86:87], v98 offset:20992\n" \
"v_mfma_f32_16x16x32_bf16 a[204:207], a[36:39], a[124:127], a[204:207]\n" \
"s_add_u32 s64, s64, s68\n" \
"s_addc_u32 s65, s65, 0\n" \
"v_mfma_f32_16x16x32_bf16 a[220:223], a[40:43], a[120:123], a[220:223]\n" \
"ds_read_b128 a[32:35], v96 offset:40960\n" \
"v_mfma_f32_16x16x32_bf16 a[220:223], a[44:47], a[124:127], a[220:223]\n" \
"global_load_dword v238, v234, s[64:65]\n" \
"v_mfma_f32_16x16x32_bf16 a[216:219], a[40:43], a[112:115], a[216:219]\n" \
"ds_read_b64_tr_b16 a[88:89], v99 offset:512\n" \
"v_mfma_f32_16x16x32_bf16 a[216:219], a[44:47], a[116:119], a[216:219]\n" \
"v_mfma_f32_16x16x32_bf16 a[212:215], a[40:43], a[104:107], a[212:215]\n" \
"ds_read_b128 a[36:39], v97 offset:40960\n" \
"v_mfma_f32_16x16x32_bf16 a[212:215], a[44:47], a[108:111], a[212:215]\n" \
"v_mfma_f32_16x16x32_bf16 a[208:211], a[40:43], a[96:99], a[208:211]\n" \
"ds_read_b64_tr_b16 a[90:91], v99 offset:4608\n" \
"v_mfma_f32_16x16x32_bf16 a[208:211], a[44:47], a[100:103], a[208:211]\n" \
"v_mfma_f32_16x16x32_bf16 a[224:227], a[48:51], a[96:99], a[224:227]\n" \
"ds_read_b128 a[40:43], v96 offset:43008\n" \
"v_mfma_f32_16x16x32_bf16 a[224:227], a[52:55], a[100:103], a[224:227]\n" \
"v_mfma_f32_16x16x32_bf16 a[228:231], a[48:51], a[104:107], a[228:231]\n" \
"ds_read_b64_tr_b16 a[92:93], v99 offset:16896\n" \
"v_mfma_f32_16x16x32_bf16 a[228:231], a[52:55], a[108:111], a[228:231]\n" \
"v_mfma_f32_16x16x32_bf16 a[232:235], a[48:51], a[112:115], a[232:235]\n" \
"ds_read_b128 a[44:47], v97 offset:43008\n" \
"v_mfma_f32_16x16x32_bf16 a[232:235], a[52:55], a[116:119], a[232:235]\n" \
"v_mfma_f32_16x16x32_bf16 a[236:239], a[48:51], a[120:123], a[236:239]\n" \
"ds_read_b64_tr_b16 a[94:95], v99 offset:20992\n" \
"v_mfma_f32_16x16x32_bf16 a[236:239], a[52:55], a[124:127], a[236:239]\n" \
"v_mfma_f32_16x16x32_bf16 a[252:255], a[56:59], a[120:123], a[252:255]\n" \
"ds_read_b128 a[48:51], v96 offset:45056\n" \
"v_mfma_f32_16x16x32_bf16 a[252:255], a[60:63], a[124:127], a[252:255]\n" \
"v_mfma_f32_16x16x32_bf16 a[248:251], a[56:59], a[112:115], a[248:251]\n" \
"v_mfma_f32_16x16x32_bf16 a[248:251], a[60:63], a[116:119], a[248:251]\n" \
"v_mfma_f32_16x16x32_bf16 a[244:247], a[56:59], a[104:107], a[244:247]\n" \
"ds_read_b128 a[52:55], v97 offset:45056\n" \
"v_mfma_f32_16x16x32_bf16 a[244:247], a[60:63], a[108:111], a[244:247]\n" \
"v_mfma_f32_16x16x32_bf16 a[240:243], a[56:59], a[96:99], a[240:243]\n" \
"v_mfma_f32_16x16x32_bf16 a[240:243], a[60:63], a[100:103], a[240:243]\n" \
"s_waitcnt lgkmcnt(1)\n" \
"v_mfma_f32_16x16x32_bf16 v[0:3], a[64:67], a[0:3], v[0:3]\n" \
"ds_read_b128 a[56:59], v96 offset:47104\n" \
"v_mfma_f32_16x16x32_bf16 v[0:3], a[68:71], a[4:7], v[0:3]\n" \
"v_mfma_f32_16x16x32_bf16 v[4:7], a[72:75], a[0:3], v[4:7]\n" \
"s_add_u32 m0, s74, 131072\n" \
"ds_read_b128 a[60:63], v97 offset:47104\n" \
"v_mfma_f32_16x16x32_bf16 v[4:7], a[76:79], a[4:7], v[4:7]\n" \
"buffer_load_dwordx4 v68, s[40:43], s83 offen offset:0 lds\n" \
"v_mfma_f32_16x16x32_bf16 v[8:11], a[80:83], a[0:3], v[8:11]\n" \
"ds_read_b64_tr_b16 a[96:97], v98 offset:1024\n" \
"v_mfma_f32_16x16x32_bf16 v[8:11], a[84:87], a[4:7], v[8:11]\n" \
"v_mfma_f32_16x16x32_bf16 v[12:15], a[88:91], a[0:3], v[12:15]\n" \
"ds_read_b64_tr_b16 a[98:99], v98 offset:5120\n" \
"v_mfma_f32_16x16x32_bf16 v[12:15], a[92:95], a[4:7], v[12:15]\n" \
"buffer_load_dwordx4 v69, s[44:47], s83 offen offset:1024 lds\n" \
"v_mfma_f32_16x16x32_bf16 v[28:31], a[88:91], a[8:11], v[28:31]\n" \
"ds_read_b64_tr_b16 a[100:101], v98 offset:17408\n" \
"v_mfma_f32_16x16x32_bf16 v[28:31], a[92:95], a[12:15], v[28:31]\n" \
"v_mfma_f32_16x16x32_bf16 v[24:27], a[80:83], a[8:11], v[24:27]\n" \
"ds_read_b64_tr_b16 a[102:103], v98 offset:21504\n" \
"v_mfma_f32_16x16x32_bf16 v[24:27], a[84:87], a[12:15], v[24:27]\n" \
"buffer_load_dwordx4 v70, s[40:43], s83 offen offset:2048 lds\n" \
"v_mfma_f32_16x16x32_bf16 v[20:23], a[72:75], a[8:11], v[20:23]\n" \
"ds_read_b64_tr_b16 a[104:105], v99 offset:1024\n" \
"v_mfma_f32_16x16x32_bf16 v[20:23], a[76:79], a[12:15], v[20:23]\n" \
"v_mfma_f32_16x16x32_bf16 v[16:19], a[64:67], a[8:11], v[16:19]\n" \
"ds_read_b64_tr_b16 a[106:107], v99 offset:5120\n" \
"v_mfma_f32_16x16x32_bf16 v[16:19], a[68:71], a[12:15], v[16:19]\n" \
"buffer_load_dwordx4 v71, s[44:47], s83 offen offset:3072 lds\n" \
"v_mfma_f32_16x16x32_bf16 v[32:35], a[64:67], a[16:19], v[32:35]\n" \
"ds_read_b64_tr_b16 a[108:109], v99 offset:17408\n" \
"v_mfma_f32_16x16x32_bf16 v[32:35], a[68:71], a[20:23], v[32:35]\n" \
"s_xor_b32 s83, s83, 0x80000\n" \
"v_mfma_f32_16x16x32_bf16 v[36:39], a[72:75], a[16:19], v[36:39]\n" \
"s_add_u32 m0, s74, 135168\n" \
"ds_read_b64_tr_b16 a[110:111], v99 offset:21504\n" \
"v_mfma_f32_16x16x32_bf16 v[36:39], a[76:79], a[20:23], v[36:39]\n" \
"buffer_load_dwordx4 v68, s[40:43], s84 offen offset:0 lds\n" \
"v_mfma_f32_16x16x32_bf16 v[40:43], a[80:83], a[16:19], v[40:43]\n" \
"ds_read_b64_tr_b16 a[112:113], v98 offset:1536\n" \
"v_mfma_f32_16x16x32_bf16 v[40:43], a[84:87], a[20:23], v[40:43]\n" \
"v_mfma_f32_16x16x32_bf16 v[44:47], a[88:91], a[16:19], v[44:47]\n" \
"ds_read_b64_tr_b16 a[114:115], v98 offset:5632\n" \
"v_mfma_f32_16x16x32_bf16 v[44:47], a[92:95], a[20:23], v[44:47]\n" \
"buffer_load_dwordx4 v69, s[44:47], s84 offen offset:1024 lds\n" \
"v_mfma_f32_16x16x32_bf16 v[60:63], a[88:91], a[24:27], v[60:63]\n" \
"ds_read_b64_tr_b16 a[116:117], v98 offset:17920\n" \
"v_mfma_f32_16x16x32_bf16 v[60:63], a[92:95], a[28:31], v[60:63]\n" \
"v_mfma_f32_16x16x32_bf16 v[56:59], a[80:83], a[24:27], v[56:59]\n" \
"ds_read_b64_tr_b16 a[118:119], v98 offset:22016\n" \
"v_mfma_f32_16x16x32_bf16 v[56:59], a[84:87], a[28:31], v[56:59]\n" \
"buffer_load_dwordx4 v70, s[40:43], s84 offen offset:2048 lds\n" \
"v_mfma_f32_16x16x32_bf16 v[52:55], a[72:75], a[24:27], v[52:55]\n" \
"ds_read_b64_tr_b16 a[120:121], v99 offset:1536\n" \
"v_mfma_f32_16x16x32_bf16 v[52:55], a[76:79], a[28:31], v[52:55]\n" \
"v_mfma_f32_16x16x32_bf16 v[48:51], a[64:67], a[24:27], v[48:51]\n" \
"ds_read_b64_tr_b16 a[122:123], v99 offset:5632\n" \
"v_mfma_f32_16x16x32_bf16 v[48:51], a[68:71], a[28:31], v[48:51]\n" \
"buffer_load_dwordx4 v71, s[44:47], s84 offen offset:3072 lds\n" \
"v_mfma_f32_16x16x32_bf16 v[144:147], a[64:67], a[32:35], v[144:147]\n" \
"ds_read_b64_tr_b16 a[124:125], v99 offset:17920\n" \
"v_mfma_f32_16x16x32_bf16 v[144:147], a[68:71], a[36:39], v[144:147]\n" \
"v_mfma_f32_16x16x32_bf16 v[148:151], a[72:75], a[32:35], v[148:151]\n" \
"s_xor_b32 s84, s84, 0x80000\n" \
"ds_read_b64_tr_b16 a[126:127], v99 offset:22016\n" \
"v_mfma_f32_16x16x32_bf16 v[148:151], a[76:79], a[36:39], v[148:151]\n" \
"v_mfma_f32_16x16x32_bf16 v[152:155], a[80:83], a[32:35], v[152:155]\n" \
"v_mfma_f32_16x16x32_bf16 v[152:155], a[84:87], a[36:39], v[152:155]\n" \
"v_mfma_f32_16x16x32_bf16 v[156:159], a[88:91], a[32:35], v[156:159]\n" \
"v_mfma_f32_16x16x32_bf16 v[156:159], a[92:95], a[36:39], v[156:159]\n" \
"v_mfma_f32_16x16x32_bf16 v[188:191], a[88:91], a[40:43], v[188:191]\n" \
"v_mfma_f32_16x16x32_bf16 v[188:191], a[92:95], a[44:47], v[188:191]\n" \
"v_mfma_f32_16x16x32_bf16 v[184:187], a[80:83], a[40:43], v[184:187]\n" \
"v_mfma_f32_16x16x32_bf16 v[184:187], a[84:87], a[44:47], v[184:187]\n" \
"v_mfma_f32_16x16x32_bf16 v[180:183], a[72:75], a[40:43], v[180:183]\n" \
"v_mfma_f32_16x16x32_bf16 v[180:183], a[76:79], a[44:47], v[180:183]\n" \
"v_mfma_f32_16x16x32_bf16 v[176:179], a[64:67], a[40:43], v[176:179]\n" \
"v_mfma_f32_16x16x32_bf16 v[176:179], a[68:71], a[44:47], v[176:179]\n" \
"s_waitcnt lgkmcnt(0)\n" \
"v_mfma_f32_16x16x32_bf16 v[192:195], a[64:67], a[48:51], v[192:195]\n" \
"v_mfma_f32_16x16x32_bf16 v[192:195], a[68:71], a[52:55], v[192:195]\n" \
"v_mfma_f32_16x16x32_bf16 v[196:199], a[72:75], a[48:51], v[196:199]\n" \
"v_mfma_f32_16x16x32_bf16 v[196:199], a[76:79], a[52:55], v[196:199]\n" \
"v_mfma_f32_16x16x32_bf16 v[200:203], a[80:83], a[48:51], v[200:203]\n" \
"v_mfma_f32_16x16x32_bf16 v[200:203], a[84:87], a[52:55], v[200:203]\n" \
"v_mfma_f32_16x16x32_bf16 v[204:207], a[88:91], a[48:51], v[204:207]\n" \
"v_mfma_f32_16x16x32_bf16 v[204:207], a[92:95], a[52:55], v[204:207]\n" \
"v_mfma_f32_16x16x32_bf16 v[220:223], a[88:91], a[56:59], v[220:223]\n" \
"v_mfma_f32_16x16x32_bf16 v[220:223], a[92:95], a[60:63], v[220:223]\n" \
"v_mfma_f32_16x16x32_bf16 v[216:219], a[80:83], a[56:59], v[216:219]\n" \
"v_mfma_f32_16x16x32_bf16 v[216:219], a[84:87], a[60:63], v[216:219]\n" \
"v_mfma_f32_16x16x32_bf16 v[212:215], a[72:75], a[56:59], v[212:215]\n" \
"v_mfma_f32_16x16x32_bf16 v[212:215], a[76:79], a[60:63], v[212:215]\n" \
"v_mfma_f32_16x16x32_bf16 v[208:211], a[64:67], a[56:59], v[208:211]\n" \
"v_mfma_f32_16x16x32_bf16 v[208:211], a[68:71], a[60:63], v[208:211]\n" \
"s_waitcnt vmcnt(11) lgkmcnt(0)\n" \
"s_barrier\n" \
"v_mfma_f32_16x16x32_bf16 a[128:131], a[0:3], a[96:99], a[128:131]\n" \
"s_add_u32 m0, s74, 65536\n" \
"ds_read_b64_tr_b16 a[64:65], v74 offset:32768\n" \
"v_mfma_f32_16x16x32_bf16 a[128:131], a[4:7], a[100:103], a[128:131]\n" \
"buffer_load_dwordx4 v64, s[36:39], s81 offen offset:0 lds\n" \
"v_mfma_f32_16x16x32_bf16 a[132:135], a[0:3], a[104:107], a[132:135]\n" \
"ds_read_b64_tr_b16 a[66:67], v74 offset:36864\n" \
"v_mfma_f32_16x16x32_bf16 a[132:135], a[4:7], a[108:111], a[132:135]\n" \
"v_mfma_f32_16x16x32_bf16 a[136:139], a[0:3], a[112:115], a[136:139]\n" \
"ds_read_b64_tr_b16 a[68:69], v74 offset:49152\n" \
"v_mfma_f32_16x16x32_bf16 a[136:139], a[4:7], a[116:119], a[136:139]\n" \
"buffer_load_dwordx4 v65, s[36:39], s81 offen offset:1024 lds\n" \
"v_mfma_f32_16x16x32_bf16 a[140:143], a[0:3], a[120:123], a[140:143]\n" \
"ds_read_b64_tr_b16 a[70:71], v74 offset:53248\n" \
"v_mfma_f32_16x16x32_bf16 a[140:143], a[4:7], a[124:127], a[140:143]\n" \
"v_mfma_f32_16x16x32_bf16 a[156:159], a[8:11], a[120:123], a[156:159]\n" \
"ds_read_b128 a[0:3], v72 offset:0\n" \
"v_mfma_f32_16x16x32_bf16 a[156:159], a[12:15], a[124:127], a[156:159]\n" \
"buffer_load_dwordx4 v66, s[36:39], s81 offen offset:2048 lds\n" \
"v_mfma_f32_16x16x32_bf16 a[152:155], a[8:11], a[112:115], a[152:155]\n" \
"ds_read_b64_tr_b16 a[72:73], v75 offset:32768\n" \
"v_mfma_f32_16x16x32_bf16 a[152:155], a[12:15], a[116:119], a[152:155]\n" \
"v_mfma_f32_16x16x32_bf16 a[148:151], a[8:11], a[104:107], a[148:151]\n" \
"ds_read_b128 a[4:7], v73 offset:0\n" \
"v_mfma_f32_16x16x32_bf16 a[148:151], a[12:15], a[108:111], a[148:151]\n" \
"buffer_load_dwordx4 v67, s[36:39], s81 offen offset:3072 lds\n" \
"v_mfma_f32_16x16x32_bf16 a[144:147], a[8:11], a[96:99], a[144:147]\n" \
"ds_read_b64_tr_b16 a[74:75], v75 offset:36864\n" \
"v_mfma_f32_16x16x32_bf16 a[144:147], a[12:15], a[100:103], a[144:147]\n" \
"s_xor_b32 s81, s81, 0x80\n" \
"v_mfma_f32_16x16x32_bf16 a[160:163], a[16:19], a[96:99], a[160:163]\n" \
"s_add_u32 m0, s74, 69632\n" \
"ds_read_b128 a[8:11], v72 offset:2048\n" \
"v_mfma_f32_16x16x32_bf16 a[160:163], a[20:23], a[100:103], a[160:163]\n" \
"buffer_load_dwordx4 v64, s[36:39], s82 offen offset:0 lds\n" \
"v_mfma_f32_16x16x32_bf16 a[164:167], a[16:19], a[104:107], a[164:167]\n" \
"ds_read_b64_tr_b16 a[76:77], v75 offset:49152\n" \
"v_mfma_f32_16x16x32_bf16 a[164:167], a[20:23], a[108:111], a[164:167]\n" \
"v_mfma_f32_16x16x32_bf16 a[168:171], a[16:19], a[112:115], a[168:171]\n" \
"ds_read_b128 a[12:15], v73 offset:2048\n" \
"v_mfma_f32_16x16x32_bf16 a[168:171], a[20:23], a[116:119], a[168:171]\n" \
"buffer_load_dwordx4 v65, s[36:39], s82 offen offset:1024 lds\n" \
"v_mfma_f32_16x16x32_bf16 a[172:175], a[16:19], a[120:123], a[172:175]\n" \
"ds_read_b64_tr_b16 a[78:79], v75 offset:53248\n" \
"v_mfma_f32_16x16x32_bf16 a[172:175], a[20:23], a[124:127], a[172:175]\n" \
"v_mfma_f32_16x16x32_bf16 a[188:191], a[24:27], a[120:123], a[188:191]\n" \
"ds_read_b128 a[16:19], v72 offset:4096\n" \
"v_mfma_f32_16x16x32_bf16 a[188:191], a[28:31], a[124:127], a[188:191]\n" \
"buffer_load_dwordx4 v66, s[36:39], s82 offen offset:2048 lds\n" \
"v_mfma_f32_16x16x32_bf16 a[184:187], a[24:27], a[112:115], a[184:187]\n" \
"ds_read_b64_tr_b16 a[80:81], v74 offset:33280\n" \
"v_mfma_f32_16x16x32_bf16 a[184:187], a[28:31], a[116:119], a[184:187]\n" \
"v_mfma_f32_16x16x32_bf16 a[180:183], a[24:27], a[104:107], a[180:183]\n" \
"ds_read_b128 a[20:23], v73 offset:4096\n" \
"v_mfma_f32_16x16x32_bf16 a[180:183], a[28:31], a[108:111], a[180:183]\n" \
"buffer_load_dwordx4 v67, s[36:39], s82 offen offset:3072 lds\n" \
"v_mfma_f32_16x16x32_bf16 a[176:179], a[24:27], a[96:99], a[176:179]\n" \
"s_mov_b64 s[64:65], s[12:13]\n" \
"s_add_u32 s68, s67, 0x100000\n" \
"ds_read_b64_tr_b16 a[82:83], v74 offset:37376\n" \
"v_mfma_f32_16x16x32_bf16 a[176:179], a[28:31], a[100:103], a[176:179]\n" \
"s_add_u32 s64, s64, s68\n" \
"s_addc_u32 s65, s65, 0\n" \
"v_mfma_f32_16x16x32_bf16 a[192:195], a[32:35], a[96:99], a[192:195]\n" \
"s_xor_b32 s82, s82, 0x80\n" \
"ds_read_b128 a[24:27], v72 offset:6144\n" \
"v_mfma_f32_16x16x32_bf16 a[192:195], a[36:39], a[100:103], a[192:195]\n" \
"global_load_dword v238, v234, s[64:65]\n" \
"v_mfma_f32_16x16x32_bf16 a[196:199], a[32:35], a[104:107], a[196:199]\n" \
"s_mov_b64 s[64:65], s[12:13]\n" \
"s_add_u32 s68, s67, 0x500000\n" \
"ds_read_b64_tr_b16 a[84:85], v74 offset:49664\n" \
"v_mfma_f32_16x16x32_bf16 a[196:199], a[36:39], a[108:111], a[196:199]\n" \
"s_add_u32 s64, s64, s68\n" \
"s_addc_u32 s65, s65, 0\n" \
"v_mfma_f32_16x16x32_bf16 a[200:203], a[32:35], a[112:115], a[200:203]\n" \
"ds_read_b128 a[28:31], v73 offset:6144\n" \
"v_mfma_f32_16x16x32_bf16 a[200:203], a[36:39], a[116:119], a[200:203]\n" \
"global_load_dword v238, v234, s[64:65]\n" \
"v_mfma_f32_16x16x32_bf16 a[204:207], a[32:35], a[120:123], a[204:207]\n" \
"s_mov_b64 s[64:65], s[12:13]\n" \
"s_add_u32 s68, s67, 0x200000\n" \
"ds_read_b64_tr_b16 a[86:87], v74 offset:53760\n" \
"v_mfma_f32_16x16x32_bf16 a[204:207], a[36:39], a[124:127], a[204:207]\n" \
"s_add_u32 s64, s64, s68\n" \
"s_addc_u32 s65, s65, 0\n" \
"v_mfma_f32_16x16x32_bf16 a[220:223], a[40:43], a[120:123], a[220:223]\n" \
"ds_read_b128 a[32:35], v72 offset:8192\n" \
"v_mfma_f32_16x16x32_bf16 a[220:223], a[44:47], a[124:127], a[220:223]\n" \
"global_load_dword v238, v234, s[64:65]\n" \
"v_mfma_f32_16x16x32_bf16 a[216:219], a[40:43], a[112:115], a[216:219]\n" \
"ds_read_b64_tr_b16 a[88:89], v75 offset:33280\n" \
"v_mfma_f32_16x16x32_bf16 a[216:219], a[44:47], a[116:119], a[216:219]\n" \
"v_mfma_f32_16x16x32_bf16 a[212:215], a[40:43], a[104:107], a[212:215]\n" \
"ds_read_b128 a[36:39], v73 offset:8192\n" \
"v_mfma_f32_16x16x32_bf16 a[212:215], a[44:47], a[108:111], a[212:215]\n" \
"v_mfma_f32_16x16x32_bf16 a[208:211], a[40:43], a[96:99], a[208:211]\n" \
"ds_read_b64_tr_b16 a[90:91], v75 offset:37376\n" \
"v_mfma_f32_16x16x32_bf16 a[208:211], a[44:47], a[100:103], a[208:211]\n" \
"v_mfma_f32_16x16x32_bf16 a[224:227], a[48:51], a[96:99], a[224:227]\n" \
"ds_read_b128 a[40:43], v72 offset:10240\n" \
"v_mfma_f32_16x16x32_bf16 a[224:227], a[52:55], a[100:103], a[224:227]\n" \
"v_mfma_f32_16x16x32_bf16 a[228:231], a[48:51], a[104:107], a[228:231]\n" \
"ds_read_b64_tr_b16 a[92:93], v75 offset:49664\n" \
"v_mfma_f32_16x16x32_bf16 a[228:231], a[52:55], a[108:111], a[228:231]\n" \
"v_mfma_f32_16x16x32_bf16 a[232:235], a[48:51], a[112:115], a[232:235]\n" \
"ds_read_b128 a[44:47], v73 offset:10240\n" \
"v_mfma_f32_16x16x32_bf16 a[232:235], a[52:55], a[116:119], a[232:235]\n" \
"v_mfma_f32_16x16x32_bf16 a[236:239], a[48:51], a[120:123], a[236:239]\n" \
"ds_read_b64_tr_b16 a[94:95], v75 offset:53760\n" \
"v_mfma_f32_16x16x32_bf16 a[236:239], a[52:55], a[124:127], a[236:239]\n" \
"v_mfma_f32_16x16x32_bf16 a[252:255], a[56:59], a[120:123], a[252:255]\n" \
"ds_read_b128 a[48:51], v72 offset:12288\n" \
"v_mfma_f32_16x16x32_bf16 a[252:255], a[60:63], a[124:127], a[252:255]\n" \
"v_mfma_f32_16x16x32_bf16 a[248:251], a[56:59], a[112:115], a[248:251]\n" \
"v_mfma_f32_16x16x32_bf16 a[248:251], a[60:63], a[116:119], a[248:251]\n" \
"v_mfma_f32_16x16x32_bf16 a[244:247], a[56:59], a[104:107], a[244:247]\n" \
"ds_read_b128 a[52:55], v73 offset:12288\n" \
"v_mfma_f32_16x16x32_bf16 a[244:247], a[60:63], a[108:111], a[244:247]\n" \
"v_mfma_f32_16x16x32_bf16 a[240:243], a[56:59], a[96:99], a[240:243]\n" \
"v_mfma_f32_16x16x32_bf16 a[240:243], a[60:63], a[100:103], a[240:243]\n" \
"s_waitcnt lgkmcnt(1)\n" \
"v_mfma_f32_16x16x32_bf16 v[0:3], a[64:67], a[0:3], v[0:3]\n" \
"ds_read_b128 a[56:59], v72 offset:14336\n" \
"v_mfma_f32_16x16x32_bf16 v[0:3], a[68:71], a[4:7], v[0:3]\n" \
"v_mfma_f32_16x16x32_bf16 v[4:7], a[72:75], a[0:3], v[4:7]\n" \
"s_add_u32 m0, s74, 98304\n" \
"ds_read_b128 a[60:63], v73 offset:14336\n" \
"v_mfma_f32_16x16x32_bf16 v[4:7], a[76:79], a[4:7], v[4:7]\n" \
"buffer_load_dwordx4 v68, s[40:43], s83 offen offset:0 lds\n" \
"v_mfma_f32_16x16x32_bf16 v[8:11], a[80:83], a[0:3], v[8:11]\n" \
"ds_read_b64_tr_b16 a[96:97], v74 offset:33792\n" \
"v_mfma_f32_16x16x32_bf16 v[8:11], a[84:87], a[4:7], v[8:11]\n" \
"v_mfma_f32_16x16x32_bf16 v[12:15], a[88:91], a[0:3], v[12:15]\n" \
"ds_read_b64_tr_b16 a[98:99], v74 offset:37888\n" \
"v_mfma_f32_16x16x32_bf16 v[12:15], a[92:95], a[4:7], v[12:15]\n" \
"buffer_load_dwordx4 v69, s[44:47], s83 offen offset:1024 lds\n" \
"v_mfma_f32_16x16x32_bf16 v[28:31], a[88:91], a[8:11], v[28:31]\n" \
"ds_read_b64_tr_b16 a[100:101], v74 offset:50176\n" \
"v_mfma_f32_16x16x32_bf16 v[28:31], a[92:95], a[12:15], v[28:31]\n" \
"v_mfma_f32_16x16x32_bf16 v[24:27], a[80:83], a[8:11], v[24:27]\n" \
"ds_read_b64_tr_b16 a[102:103], v74 offset:54272\n" \
"v_mfma_f32_16x16x32_bf16 v[24:27], a[84:87], a[12:15], v[24:27]\n" \
"buffer_load_dwordx4 v70, s[40:43], s83 offen offset:2048 lds\n" \
"v_mfma_f32_16x16x32_bf16 v[20:23], a[72:75], a[8:11], v[20:23]\n" \
"ds_read_b64_tr_b16 a[104:105], v75 offset:33792\n" \
"v_mfma_f32_16x16x32_bf16 v[20:23], a[76:79], a[12:15], v[20:23]\n" \
"v_mfma_f32_16x16x32_bf16 v[16:19], a[64:67], a[8:11], v[16:19]\n" \
"ds_read_b64_tr_b16 a[106:107], v75 offset:37888\n" \
"v_mfma_f32_16x16x32_bf16 v[16:19], a[68:71], a[12:15], v[16:19]\n" \
"buffer_load_dwordx4 v71, s[44:47], s83 offen offset:3072 lds\n" \
"v_mfma_f32_16x16x32_bf16 v[32:35], a[64:67], a[16:19], v[32:35]\n" \
"ds_read_b64_tr_b16 a[108:109], v75 offset:50176\n" \
"v_mfma_f32_16x16x32_bf16 v[32:35], a[68:71], a[20:23], v[32:35]\n" \
"v_mfma_f32_16x16x32_bf16 v[36:39], a[72:75], a[16:19], v[36:39]\n" \
"s_add_u32 m0, s74, 102400\n" \
"ds_read_b64_tr_b16 a[110:111], v75 offset:54272\n" \
"v_mfma_f32_16x16x32_bf16 v[36:39], a[76:79], a[20:23], v[36:39]\n" \
"buffer_load_dwordx4 v68, s[40:43], s84 offen offset:0 lds\n" \
"v_mfma_f32_16x16x32_bf16 v[40:43], a[80:83], a[16:19], v[40:43]\n" \
"ds_read_b64_tr_b16 a[112:113], v74 offset:34304\n" \
"v_mfma_f32_16x16x32_bf16 v[40:43], a[84:87], a[20:23], v[40:43]\n" \
"v_mfma_f32_16x16x32_bf16 v[44:47], a[88:91], a[16:19], v[44:47]\n" \
"ds_read_b64_tr_b16 a[114:115], v74 offset:38400\n" \
"v_mfma_f32_16x16x32_bf16 v[44:47], a[92:95], a[20:23], v[44:47]\n" \
"buffer_load_dwordx4 v69, s[44:47], s84 offen offset:1024 lds\n" \
"v_mfma_f32_16x16x32_bf16 v[60:63], a[88:91], a[24:27], v[60:63]\n" \
"ds_read_b64_tr_b16 a[116:117], v74 offset:50688\n" \
"v_mfma_f32_16x16x32_bf16 v[60:63], a[92:95], a[28:31], v[60:63]\n" \
"v_mfma_f32_16x16x32_bf16 v[56:59], a[80:83], a[24:27], v[56:59]\n" \
"ds_read_b64_tr_b16 a[118:119], v74 offset:54784\n" \
"v_mfma_f32_16x16x32_bf16 v[56:59], a[84:87], a[28:31], v[56:59]\n" \
"buffer_load_dwordx4 v70, s[40:43], s84 offen offset:2048 lds\n" \
"v_mfma_f32_16x16x32_bf16 v[52:55], a[72:75], a[24:27], v[52:55]\n" \
"ds_read_b64_tr_b16 a[120:121], v75 offset:34304\n" \
"v_mfma_f32_16x16x32_bf16 v[52:55], a[76:79], a[28:31], v[52:55]\n" \
"v_mfma_f32_16x16x32_bf16 v[48:51], a[64:67], a[24:27], v[48:51]\n" \
"ds_read_b64_tr_b16 a[122:123], v75 offset:38400\n" \
"v_mfma_f32_16x16x32_bf16 v[48:51], a[68:71], a[28:31], v[48:51]\n" \
"buffer_load_dwordx4 v71, s[44:47], s84 offen offset:3072 lds\n" \
"v_mfma_f32_16x16x32_bf16 v[144:147], a[64:67], a[32:35], v[144:147]\n" \
"ds_read_b64_tr_b16 a[124:125], v75 offset:50688\n" \
"v_mfma_f32_16x16x32_bf16 v[144:147], a[68:71], a[36:39], v[144:147]\n" \
"v_mfma_f32_16x16x32_bf16 v[148:151], a[72:75], a[32:35], v[148:151]\n" \
"ds_read_b64_tr_b16 a[126:127], v75 offset:54784\n" \
"v_mfma_f32_16x16x32_bf16 v[148:151], a[76:79], a[36:39], v[148:151]\n" \
"v_mfma_f32_16x16x32_bf16 v[152:155], a[80:83], a[32:35], v[152:155]\n" \
"v_mfma_f32_16x16x32_bf16 v[152:155], a[84:87], a[36:39], v[152:155]\n" \
"v_mfma_f32_16x16x32_bf16 v[156:159], a[88:91], a[32:35], v[156:159]\n" \
"v_mfma_f32_16x16x32_bf16 v[156:159], a[92:95], a[36:39], v[156:159]\n" \
"v_mfma_f32_16x16x32_bf16 v[188:191], a[88:91], a[40:43], v[188:191]\n" \
"v_mfma_f32_16x16x32_bf16 v[188:191], a[92:95], a[44:47], v[188:191]\n" \
"v_mfma_f32_16x16x32_bf16 v[184:187], a[80:83], a[40:43], v[184:187]\n" \
"v_mfma_f32_16x16x32_bf16 v[184:187], a[84:87], a[44:47], v[184:187]\n" \
"v_mfma_f32_16x16x32_bf16 v[180:183], a[72:75], a[40:43], v[180:183]\n" \
"v_mfma_f32_16x16x32_bf16 v[180:183], a[76:79], a[44:47], v[180:183]\n" \
"v_mfma_f32_16x16x32_bf16 v[176:179], a[64:67], a[40:43], v[176:179]\n" \
"v_mfma_f32_16x16x32_bf16 v[176:179], a[68:71], a[44:47], v[176:179]\n" \
"s_waitcnt lgkmcnt(0)\n" \
"v_mfma_f32_16x16x32_bf16 v[192:195], a[64:67], a[48:51], v[192:195]\n" \
"v_mfma_f32_16x16x32_bf16 v[192:195], a[68:71], a[52:55], v[192:195]\n" \
"v_mfma_f32_16x16x32_bf16 v[196:199], a[72:75], a[48:51], v[196:199]\n" \
"v_mfma_f32_16x16x32_bf16 v[196:199], a[76:79], a[52:55], v[196:199]\n" \
"v_mfma_f32_16x16x32_bf16 v[200:203], a[80:83], a[48:51], v[200:203]\n" \
"v_mfma_f32_16x16x32_bf16 v[200:203], a[84:87], a[52:55], v[200:203]\n" \
"v_mfma_f32_16x16x32_bf16 v[204:207], a[88:91], a[48:51], v[204:207]\n" \
"v_mfma_f32_16x16x32_bf16 v[204:207], a[92:95], a[52:55], v[204:207]\n" \
"v_mfma_f32_16x16x32_bf16 v[220:223], a[88:91], a[56:59], v[220:223]\n" \
"v_mfma_f32_16x16x32_bf16 v[220:223], a[92:95], a[60:63], v[220:223]\n" \
"v_mfma_f32_16x16x32_bf16 v[216:219], a[80:83], a[56:59], v[216:219]\n" \
"v_mfma_f32_16x16x32_bf16 v[216:219], a[84:87], a[60:63], v[216:219]\n" \
"v_mfma_f32_16x16x32_bf16 v[212:215], a[72:75], a[56:59], v[212:215]\n" \
"v_mfma_f32_16x16x32_bf16 v[212:215], a[76:79], a[60:63], v[212:215]\n" \
"v_mfma_f32_16x16x32_bf16 v[208:211], a[64:67], a[56:59], v[208:211]\n" \
"v_mfma_f32_16x16x32_bf16 v[208:211], a[68:71], a[60:63], v[208:211]\n" \
"s_waitcnt vmcnt(11) lgkmcnt(0)\n" \
"s_barrier\n" \
"v_mfma_f32_16x16x32_bf16 a[128:131], a[0:3], a[96:99], a[128:131]\n" \
"s_add_u32 m0, s74, 32768\n" \
"ds_read_b64_tr_b16 a[64:65], v102 offset:0\n" \
"v_mfma_f32_16x16x32_bf16 a[128:131], a[4:7], a[100:103], a[128:131]\n" \
"buffer_load_dwordx4 v64, s[36:39], s81 offen offset:0 lds\n" \
"v_mfma_f32_16x16x32_bf16 a[132:135], a[0:3], a[104:107], a[132:135]\n" \
"ds_read_b64_tr_b16 a[66:67], v102 offset:4096\n" \
"v_mfma_f32_16x16x32_bf16 a[132:135], a[4:7], a[108:111], a[132:135]\n" \
"v_mfma_f32_16x16x32_bf16 a[136:139], a[0:3], a[112:115], a[136:139]\n" \
"ds_read_b64_tr_b16 a[68:69], v102 offset:16384\n" \
"v_mfma_f32_16x16x32_bf16 a[136:139], a[4:7], a[116:119], a[136:139]\n" \
"buffer_load_dwordx4 v65, s[36:39], s81 offen offset:1024 lds\n" \
"v_mfma_f32_16x16x32_bf16 a[140:143], a[0:3], a[120:123], a[140:143]\n" \
"ds_read_b64_tr_b16 a[70:71], v102 offset:20480\n" \
"v_mfma_f32_16x16x32_bf16 a[140:143], a[4:7], a[124:127], a[140:143]\n" \
"v_mfma_f32_16x16x32_bf16 a[156:159], a[8:11], a[120:123], a[156:159]\n" \
"ds_read_b128 a[0:3], v96 offset:0\n" \
"v_mfma_f32_16x16x32_bf16 a[156:159], a[12:15], a[124:127], a[156:159]\n" \
"buffer_load_dwordx4 v66, s[36:39], s81 offen offset:2048 lds\n" \
"v_mfma_f32_16x16x32_bf16 a[152:155], a[8:11], a[112:115], a[152:155]\n" \
"ds_read_b64_tr_b16 a[72:73], v103 offset:0\n" \
"v_mfma_f32_16x16x32_bf16 a[152:155], a[12:15], a[116:119], a[152:155]\n" \
"v_mfma_f32_16x16x32_bf16 a[148:151], a[8:11], a[104:107], a[148:151]\n" \
"ds_read_b128 a[4:7], v97 offset:0\n" \
"v_mfma_f32_16x16x32_bf16 a[148:151], a[12:15], a[108:111], a[148:151]\n" \
"buffer_load_dwordx4 v67, s[36:39], s81 offen offset:3072 lds\n" \
"v_mfma_f32_16x16x32_bf16 a[144:147], a[8:11], a[96:99], a[144:147]\n" \
"ds_read_b64_tr_b16 a[74:75], v103 offset:4096\n" \
"v_mfma_f32_16x16x32_bf16 a[144:147], a[12:15], a[100:103], a[144:147]\n" \
"v_mfma_f32_16x16x32_bf16 a[160:163], a[16:19], a[96:99], a[160:163]\n" \
"s_add_u32 m0, s74, 36864\n" \
"ds_read_b128 a[8:11], v96 offset:2048\n" \
"v_mfma_f32_16x16x32_bf16 a[160:163], a[20:23], a[100:103], a[160:163]\n" \
"buffer_load_dwordx4 v64, s[36:39], s82 offen offset:0 lds\n" \
"v_mfma_f32_16x16x32_bf16 a[164:167], a[16:19], a[104:107], a[164:167]\n" \
"ds_read_b64_tr_b16 a[76:77], v103 offset:16384\n" \
"v_mfma_f32_16x16x32_bf16 a[164:167], a[20:23], a[108:111], a[164:167]\n" \
"v_mfma_f32_16x16x32_bf16 a[168:171], a[16:19], a[112:115], a[168:171]\n" \
"ds_read_b128 a[12:15], v97 offset:2048\n" \
"v_mfma_f32_16x16x32_bf16 a[168:171], a[20:23], a[116:119], a[168:171]\n" \
"buffer_load_dwordx4 v65, s[36:39], s82 offen offset:1024 lds\n" \
"v_mfma_f32_16x16x32_bf16 a[172:175], a[16:19], a[120:123], a[172:175]\n" \
"ds_read_b64_tr_b16 a[78:79], v103 offset:20480\n" \
"v_mfma_f32_16x16x32_bf16 a[172:175], a[20:23], a[124:127], a[172:175]\n" \
"v_mfma_f32_16x16x32_bf16 a[188:191], a[24:27], a[120:123], a[188:191]\n" \
"ds_read_b128 a[16:19], v96 offset:4096\n" \
"v_mfma_f32_16x16x32_bf16 a[188:191], a[28:31], a[124:127], a[188:191]\n" \
"buffer_load_dwordx4 v66, s[36:39], s82 offen offset:2048 lds\n" \
"v_mfma_f32_16x16x32_bf16 a[184:187], a[24:27], a[112:115], a[184:187]\n" \
"ds_read_b64_tr_b16 a[80:81], v102 offset:512\n" \
"v_mfma_f32_16x16x32_bf16 a[184:187], a[28:31], a[116:119], a[184:187]\n" \
"v_mfma_f32_16x16x32_bf16 a[180:183], a[24:27], a[104:107], a[180:183]\n" \
"ds_read_b128 a[20:23], v97 offset:4096\n" \
"v_mfma_f32_16x16x32_bf16 a[180:183], a[28:31], a[108:111], a[180:183]\n" \
"buffer_load_dwordx4 v67, s[36:39], s82 offen offset:3072 lds\n" \
"v_mfma_f32_16x16x32_bf16 a[176:179], a[24:27], a[96:99], a[176:179]\n" \
"s_mov_b64 s[64:65], s[12:13]\n" \
"s_add_u32 s68, s67, 0x300000\n" \
"ds_read_b64_tr_b16 a[82:83], v102 offset:4608\n" \
"v_mfma_f32_16x16x32_bf16 a[176:179], a[28:31], a[100:103], a[176:179]\n" \
"s_add_u32 s64, s64, s68\n" \
"s_addc_u32 s65, s65, 0\n" \
"v_mfma_f32_16x16x32_bf16 a[192:195], a[32:35], a[96:99], a[192:195]\n" \
"ds_read_b128 a[24:27], v96 offset:6144\n" \
"v_mfma_f32_16x16x32_bf16 a[192:195], a[36:39], a[100:103], a[192:195]\n" \
"global_load_dword v238, v234, s[64:65]\n" \
"v_mfma_f32_16x16x32_bf16 a[196:199], a[32:35], a[104:107], a[196:199]\n" \
"s_mov_b64 s[64:65], s[12:13]\n" \
"s_add_u32 s68, s67, 0x400000\n" \
"ds_read_b64_tr_b16 a[84:85], v102 offset:16896\n" \
"v_mfma_f32_16x16x32_bf16 a[196:199], a[36:39], a[108:111], a[196:199]\n" \
"s_add_u32 s64, s64, s68\n" \
"s_addc_u32 s65, s65, 0\n" \
"v_mfma_f32_16x16x32_bf16 a[200:203], a[32:35], a[112:115], a[200:203]\n" \
"ds_read_b128 a[28:31], v97 offset:6144\n" \
"v_mfma_f32_16x16x32_bf16 a[200:203], a[36:39], a[116:119], a[200:203]\n" \
"global_load_dword v238, v234, s[64:65]\n" \
"v_mfma_f32_16x16x32_bf16 a[204:207], a[32:35], a[120:123], a[204:207]\n" \
"s_mov_b64 s[64:65], s[12:13]\n" \
"s_add_u32 s68, s67, 0x600000\n" \
"ds_read_b64_tr_b16 a[86:87], v102 offset:20992\n" \
"v_mfma_f32_16x16x32_bf16 a[204:207], a[36:39], a[124:127], a[204:207]\n" \
"s_add_u32 s64, s64, s68\n" \
"s_addc_u32 s65, s65, 0\n" \
"v_mfma_f32_16x16x32_bf16 a[220:223], a[40:43], a[120:123], a[220:223]\n" \
"ds_read_b128 a[32:35], v96 offset:8192\n" \
"v_mfma_f32_16x16x32_bf16 a[220:223], a[44:47], a[124:127], a[220:223]\n" \
"global_load_dword v238, v234, s[64:65]\n" \
"v_mfma_f32_16x16x32_bf16 a[216:219], a[40:43], a[112:115], a[216:219]\n" \
"ds_read_b64_tr_b16 a[88:89], v103 offset:512\n" \
"v_mfma_f32_16x16x32_bf16 a[216:219], a[44:47], a[116:119], a[216:219]\n" \
"v_mfma_f32_16x16x32_bf16 a[212:215], a[40:43], a[104:107], a[212:215]\n" \
"ds_read_b128 a[36:39], v97 offset:8192\n" \
"v_mfma_f32_16x16x32_bf16 a[212:215], a[44:47], a[108:111], a[212:215]\n" \
"v_mfma_f32_16x16x32_bf16 a[208:211], a[40:43], a[96:99], a[208:211]\n" \
"ds_read_b64_tr_b16 a[90:91], v103 offset:4608\n" \
"v_mfma_f32_16x16x32_bf16 a[208:211], a[44:47], a[100:103], a[208:211]\n" \
"v_mfma_f32_16x16x32_bf16 a[224:227], a[48:51], a[96:99], a[224:227]\n" \
"ds_read_b128 a[40:43], v96 offset:10240\n" \
"v_mfma_f32_16x16x32_bf16 a[224:227], a[52:55], a[100:103], a[224:227]\n" \
"v_mfma_f32_16x16x32_bf16 a[228:231], a[48:51], a[104:107], a[228:231]\n" \
"ds_read_b64_tr_b16 a[92:93], v103 offset:16896\n" \
"v_mfma_f32_16x16x32_bf16 a[228:231], a[52:55], a[108:111], a[228:231]\n" \
"v_mfma_f32_16x16x32_bf16 a[232:235], a[48:51], a[112:115], a[232:235]\n" \
"ds_read_b128 a[44:47], v97 offset:10240\n" \
"v_mfma_f32_16x16x32_bf16 a[232:235], a[52:55], a[116:119], a[232:235]\n" \
"v_mfma_f32_16x16x32_bf16 a[236:239], a[48:51], a[120:123], a[236:239]\n" \
"ds_read_b64_tr_b16 a[94:95], v103 offset:20992\n" \
"v_mfma_f32_16x16x32_bf16 a[236:239], a[52:55], a[124:127], a[236:239]\n" \
"v_mfma_f32_16x16x32_bf16 a[252:255], a[56:59], a[120:123], a[252:255]\n" \
"ds_read_b128 a[48:51], v96 offset:12288\n" \
"v_mfma_f32_16x16x32_bf16 a[252:255], a[60:63], a[124:127], a[252:255]\n" \
"v_mfma_f32_16x16x32_bf16 a[248:251], a[56:59], a[112:115], a[248:251]\n" \
"v_mfma_f32_16x16x32_bf16 a[248:251], a[60:63], a[116:119], a[248:251]\n" \
"v_mfma_f32_16x16x32_bf16 a[244:247], a[56:59], a[104:107], a[244:247]\n" \
"ds_read_b128 a[52:55], v97 offset:12288\n" \
"v_mfma_f32_16x16x32_bf16 a[244:247], a[60:63], a[108:111], a[244:247]\n" \
"v_mfma_f32_16x16x32_bf16 a[240:243], a[56:59], a[96:99], a[240:243]\n" \
"v_mfma_f32_16x16x32_bf16 a[240:243], a[60:63], a[100:103], a[240:243]\n" \
"s_waitcnt lgkmcnt(1)\n" \
"v_mfma_f32_16x16x32_bf16 v[0:3], a[64:67], a[0:3], v[0:3]\n" \
"ds_read_b128 a[56:59], v96 offset:14336\n" \
"v_mfma_f32_16x16x32_bf16 v[0:3], a[68:71], a[4:7], v[0:3]\n" \
"v_mfma_f32_16x16x32_bf16 v[4:7], a[72:75], a[0:3], v[4:7]\n" \
"s_add_u32 m0, s79, 0\n" \
"ds_read_b128 a[60:63], v97 offset:14336\n" \
"Lpg1661_%=:\n" \
"Lst0x2_%=:\n" \
"s_cbranch_execz Lpg3894_%=\n" \
"v_mfma_f32_16x16x32_bf16 v[4:7], a[76:79], a[4:7], v[4:7]\n" \
"buffer_load_dwordx4 v228, s[52:55], s78 offen offset:0 lds\n" \
"v_mfma_f32_16x16x32_bf16 v[8:11], a[80:83], a[0:3], v[8:11]\n" \
"ds_read_b64_tr_b16 a[96:97], v102 offset:1024\n" \
"v_mfma_f32_16x16x32_bf16 v[8:11], a[84:87], a[4:7], v[8:11]\n" \
"v_mfma_f32_16x16x32_bf16 v[12:15], a[88:91], a[0:3], v[12:15]\n" \
"ds_read_b64_tr_b16 a[98:99], v102 offset:5120\n" \
"v_mfma_f32_16x16x32_bf16 v[12:15], a[92:95], a[4:7], v[12:15]\n" \
"buffer_load_dwordx4 v229, s[52:55], s78 offen offset:1024 lds\n" \
"v_mfma_f32_16x16x32_bf16 v[28:31], a[88:91], a[8:11], v[28:31]\n" \
"ds_read_b64_tr_b16 a[100:101], v102 offset:17408\n" \
"v_mfma_f32_16x16x32_bf16 v[28:31], a[92:95], a[12:15], v[28:31]\n" \
"v_mfma_f32_16x16x32_bf16 v[24:27], a[80:83], a[8:11], v[24:27]\n" \
"s_xor_b32 s78, s78, 0x180000\n" \
"ds_read_b64_tr_b16 a[102:103], v102 offset:21504\n" \
"v_mfma_f32_16x16x32_bf16 v[24:27], a[84:87], a[12:15], v[24:27]\n" \
"buffer_load_dwordx4 v228, s[52:55], s89 offen offset:2048 lds\n" \
"v_mfma_f32_16x16x32_bf16 v[20:23], a[72:75], a[8:11], v[20:23]\n" \
"ds_read_b64_tr_b16 a[104:105], v103 offset:1024\n" \
"v_mfma_f32_16x16x32_bf16 v[20:23], a[76:79], a[12:15], v[20:23]\n" \
"v_mfma_f32_16x16x32_bf16 v[16:19], a[64:67], a[8:11], v[16:19]\n" \
"ds_read_b64_tr_b16 a[106:107], v103 offset:5120\n" \
"v_mfma_f32_16x16x32_bf16 v[16:19], a[68:71], a[12:15], v[16:19]\n" \
"buffer_load_dwordx4 v229, s[52:55], s89 offen offset:3072 lds\n" \
"v_mfma_f32_16x16x32_bf16 v[32:35], a[64:67], a[16:19], v[32:35]\n" \
"ds_read_b64_tr_b16 a[108:109], v103 offset:17408\n" \
"v_mfma_f32_16x16x32_bf16 v[32:35], a[68:71], a[20:23], v[32:35]\n" \
"s_xor_b32 s89, s89, 0x180000\n" \
"v_mfma_f32_16x16x32_bf16 v[36:39], a[72:75], a[16:19], v[36:39]\n" \
"s_add_u32 m0, s79, 16384\n" \
"ds_read_b64_tr_b16 a[110:111], v103 offset:21504\n" \
"v_mfma_f32_16x16x32_bf16 v[36:39], a[76:79], a[20:23], v[36:39]\n" \
"buffer_load_dwordx4 v228, s[52:55], s78 offen offset:0 lds\n" \
"v_mfma_f32_16x16x32_bf16 v[40:43], a[80:83], a[16:19], v[40:43]\n" \
"ds_read_b64_tr_b16 a[112:113], v102 offset:1536\n" \
"v_mfma_f32_16x16x32_bf16 v[40:43], a[84:87], a[20:23], v[40:43]\n" \
"v_mfma_f32_16x16x32_bf16 v[44:47], a[88:91], a[16:19], v[44:47]\n" \
"ds_read_b64_tr_b16 a[114:115], v102 offset:5632\n" \
"v_mfma_f32_16x16x32_bf16 v[44:47], a[92:95], a[20:23], v[44:47]\n" \
"buffer_load_dwordx4 v229, s[52:55], s78 offen offset:1024 lds\n" \
"v_mfma_f32_16x16x32_bf16 v[60:63], a[88:91], a[24:27], v[60:63]\n" \
"ds_read_b64_tr_b16 a[116:117], v102 offset:17920\n" \
"v_mfma_f32_16x16x32_bf16 v[60:63], a[92:95], a[28:31], v[60:63]\n" \
"v_mfma_f32_16x16x32_bf16 v[56:59], a[80:83], a[24:27], v[56:59]\n" \
"s_xor_b32 s78, s78, 0x400000\n" \
"ds_read_b64_tr_b16 a[118:119], v102 offset:22016\n" \
"v_mfma_f32_16x16x32_bf16 v[56:59], a[84:87], a[28:31], v[56:59]\n" \
"buffer_load_dwordx4 v228, s[52:55], s89 offen offset:2048 lds\n" \
"v_mfma_f32_16x16x32_bf16 v[52:55], a[72:75], a[24:27], v[52:55]\n" \
"ds_read_b64_tr_b16 a[120:121], v103 offset:1536\n" \
"v_mfma_f32_16x16x32_bf16 v[52:55], a[76:79], a[28:31], v[52:55]\n" \
"v_mfma_f32_16x16x32_bf16 v[48:51], a[64:67], a[24:27], v[48:51]\n" \
"ds_read_b64_tr_b16 a[122:123], v103 offset:5632\n" \
"v_mfma_f32_16x16x32_bf16 v[48:51], a[68:71], a[28:31], v[48:51]\n" \
"buffer_load_dwordx4 v229, s[52:55], s89 offen offset:3072 lds\n" \
"v_mfma_f32_16x16x32_bf16 v[144:147], a[64:67], a[32:35], v[144:147]\n" \
"ds_read_b64_tr_b16 a[124:125], v103 offset:17920\n" \
"v_mfma_f32_16x16x32_bf16 v[144:147], a[68:71], a[36:39], v[144:147]\n" \
"v_mfma_f32_16x16x32_bf16 v[148:151], a[72:75], a[32:35], v[148:151]\n" \
"s_xor_b32 s89, s89, 0x400000\n" \
"ds_read_b64_tr_b16 a[126:127], v103 offset:22016\n" \
"v_mfma_f32_16x16x32_bf16 v[148:151], a[76:79], a[36:39], v[148:151]\n" \
"v_mfma_f32_16x16x32_bf16 v[152:155], a[80:83], a[32:35], v[152:155]\n" \
"v_mfma_f32_16x16x32_bf16 v[152:155], a[84:87], a[36:39], v[152:155]\n" \
"v_mfma_f32_16x16x32_bf16 v[156:159], a[88:91], a[32:35], v[156:159]\n" \
"v_mfma_f32_16x16x32_bf16 v[156:159], a[92:95], a[36:39], v[156:159]\n" \
"v_mfma_f32_16x16x32_bf16 v[188:191], a[88:91], a[40:43], v[188:191]\n" \
"v_mfma_f32_16x16x32_bf16 v[188:191], a[92:95], a[44:47], v[188:191]\n" \
"v_mfma_f32_16x16x32_bf16 v[184:187], a[80:83], a[40:43], v[184:187]\n" \
"v_mfma_f32_16x16x32_bf16 v[184:187], a[84:87], a[44:47], v[184:187]\n" \
"v_mfma_f32_16x16x32_bf16 v[180:183], a[72:75], a[40:43], v[180:183]\n" \
"v_mfma_f32_16x16x32_bf16 v[180:183], a[76:79], a[44:47], v[180:183]\n" \
"v_mfma_f32_16x16x32_bf16 v[176:179], a[64:67], a[40:43], v[176:179]\n" \
"v_mfma_f32_16x16x32_bf16 v[176:179], a[68:71], a[44:47], v[176:179]\n" \
"s_waitcnt lgkmcnt(0)\n" \
"v_mfma_f32_16x16x32_bf16 v[192:195], a[64:67], a[48:51], v[192:195]\n" \
"v_mfma_f32_16x16x32_bf16 v[192:195], a[68:71], a[52:55], v[192:195]\n" \
"v_mfma_f32_16x16x32_bf16 v[196:199], a[72:75], a[48:51], v[196:199]\n" \
"v_mfma_f32_16x16x32_bf16 v[196:199], a[76:79], a[52:55], v[196:199]\n" \
"v_mfma_f32_16x16x32_bf16 v[200:203], a[80:83], a[48:51], v[200:203]\n" \
"v_mfma_f32_16x16x32_bf16 v[200:203], a[84:87], a[52:55], v[200:203]\n" \
"v_mfma_f32_16x16x32_bf16 v[204:207], a[88:91], a[48:51], v[204:207]\n" \
"v_mfma_f32_16x16x32_bf16 v[204:207], a[92:95], a[52:55], v[204:207]\n" \
"v_mfma_f32_16x16x32_bf16 v[220:223], a[88:91], a[56:59], v[220:223]\n" \
"v_mfma_f32_16x16x32_bf16 v[220:223], a[92:95], a[60:63], v[220:223]\n" \
"v_mfma_f32_16x16x32_bf16 v[216:219], a[80:83], a[56:59], v[216:219]\n" \
"v_mfma_f32_16x16x32_bf16 v[216:219], a[84:87], a[60:63], v[216:219]\n" \
"v_mfma_f32_16x16x32_bf16 v[212:215], a[72:75], a[56:59], v[212:215]\n" \
"v_mfma_f32_16x16x32_bf16 v[212:215], a[76:79], a[60:63], v[212:215]\n" \
"v_mfma_f32_16x16x32_bf16 v[208:211], a[64:67], a[56:59], v[208:211]\n" \
"v_mfma_f32_16x16x32_bf16 v[208:211], a[68:71], a[60:63], v[208:211]\n" \
"s_waitcnt vmcnt(11) lgkmcnt(0)\n" \
"s_barrier\n" \
"v_mfma_f32_16x16x32_bf16 a[128:131], a[0:3], a[96:99], a[128:131]\n" \
"s_add_u32 m0, s74, 131072\n" \
"ds_read_b64_tr_b16 a[64:65], v98 offset:32768\n" \
"v_mfma_f32_16x16x32_bf16 a[128:131], a[4:7], a[100:103], a[128:131]\n" \
"buffer_load_dwordx4 v224, s[48:51], s75 offen offset:0 lds\n" \
"v_mfma_f32_16x16x32_bf16 a[132:135], a[0:3], a[104:107], a[132:135]\n" \
"ds_read_b64_tr_b16 a[66:67], v98 offset:36864\n" \
"v_mfma_f32_16x16x32_bf16 a[132:135], a[4:7], a[108:111], a[132:135]\n" \
"v_mfma_f32_16x16x32_bf16 a[136:139], a[0:3], a[112:115], a[136:139]\n" \
"ds_read_b64_tr_b16 a[68:69], v98 offset:49152\n" \
"v_mfma_f32_16x16x32_bf16 a[136:139], a[4:7], a[116:119], a[136:139]\n" \
"buffer_load_dwordx4 v225, s[48:51], s75 offen offset:1024 lds\n" \
"v_mfma_f32_16x16x32_bf16 a[140:143], a[0:3], a[120:123], a[140:143]\n" \
"ds_read_b64_tr_b16 a[70:71], v98 offset:53248\n" \
"v_mfma_f32_16x16x32_bf16 a[140:143], a[4:7], a[124:127], a[140:143]\n" \
"v_mfma_f32_16x16x32_bf16 a[156:159], a[8:11], a[120:123], a[156:159]\n" \
"ds_read_b128 a[0:3], v72 offset:32768\n" \
"v_mfma_f32_16x16x32_bf16 a[156:159], a[12:15], a[124:127], a[156:159]\n" \
"buffer_load_dwordx4 v226, s[48:51], s75 offen offset:2048 lds\n" \
"v_mfma_f32_16x16x32_bf16 a[152:155], a[8:11], a[112:115], a[152:155]\n" \
"ds_read_b64_tr_b16 a[72:73], v99 offset:32768\n" \
"v_mfma_f32_16x16x32_bf16 a[152:155], a[12:15], a[116:119], a[152:155]\n" \
"v_mfma_f32_16x16x32_bf16 a[148:151], a[8:11], a[104:107], a[148:151]\n" \
"ds_read_b128 a[4:7], v73 offset:32768\n" \
"v_mfma_f32_16x16x32_bf16 a[148:151], a[12:15], a[108:111], a[148:151]\n" \
"buffer_load_dwordx4 v227, s[48:51], s75 offen offset:3072 lds\n" \
"v_mfma_f32_16x16x32_bf16 a[144:147], a[8:11], a[96:99], a[144:147]\n" \
"ds_read_b64_tr_b16 a[74:75], v99 offset:36864\n" \
"v_mfma_f32_16x16x32_bf16 a[144:147], a[12:15], a[100:103], a[144:147]\n" \
"s_xor_b32 s75, s75, 0x180\n" \
"v_mfma_f32_16x16x32_bf16 a[160:163], a[16:19], a[96:99], a[160:163]\n" \
"s_add_u32 m0, s74, 135168\n" \
"ds_read_b128 a[8:11], v72 offset:34816\n" \
"v_mfma_f32_16x16x32_bf16 a[160:163], a[20:23], a[100:103], a[160:163]\n" \
"buffer_load_dwordx4 v224, s[48:51], s77 offen offset:0 lds\n" \
"v_mfma_f32_16x16x32_bf16 a[164:167], a[16:19], a[104:107], a[164:167]\n" \
"ds_read_b64_tr_b16 a[76:77], v99 offset:49152\n" \
"v_mfma_f32_16x16x32_bf16 a[164:167], a[20:23], a[108:111], a[164:167]\n" \
"v_mfma_f32_16x16x32_bf16 a[168:171], a[16:19], a[112:115], a[168:171]\n" \
"ds_read_b128 a[12:15], v73 offset:34816\n" \
"v_mfma_f32_16x16x32_bf16 a[168:171], a[20:23], a[116:119], a[168:171]\n" \
"buffer_load_dwordx4 v225, s[48:51], s77 offen offset:1024 lds\n" \
"v_mfma_f32_16x16x32_bf16 a[172:175], a[16:19], a[120:123], a[172:175]\n" \
"ds_read_b64_tr_b16 a[78:79], v99 offset:53248\n" \
"v_mfma_f32_16x16x32_bf16 a[172:175], a[20:23], a[124:127], a[172:175]\n" \
"v_mfma_f32_16x16x32_bf16 a[188:191], a[24:27], a[120:123], a[188:191]\n" \
"ds_read_b128 a[16:19], v72 offset:36864\n" \
"v_mfma_f32_16x16x32_bf16 a[188:191], a[28:31], a[124:127], a[188:191]\n" \
"buffer_load_dwordx4 v226, s[48:51], s77 offen offset:2048 lds\n" \
"v_mfma_f32_16x16x32_bf16 a[184:187], a[24:27], a[112:115], a[184:187]\n" \
"ds_read_b64_tr_b16 a[80:81], v98 offset:33280\n" \
"v_mfma_f32_16x16x32_bf16 a[184:187], a[28:31], a[116:119], a[184:187]\n" \
"v_mfma_f32_16x16x32_bf16 a[180:183], a[24:27], a[104:107], a[180:183]\n" \
"ds_read_b128 a[20:23], v73 offset:36864\n" \
"v_mfma_f32_16x16x32_bf16 a[180:183], a[28:31], a[108:111], a[180:183]\n" \
"buffer_load_dwordx4 v227, s[48:51], s77 offen offset:3072 lds\n" \
"v_mfma_f32_16x16x32_bf16 a[176:179], a[24:27], a[96:99], a[176:179]\n" \
"s_mov_b64 s[64:65], s[12:13]\n" \
"s_add_u32 s68, s67, 0x700000\n" \
"ds_read_b64_tr_b16 a[82:83], v98 offset:37376\n" \
"v_mfma_f32_16x16x32_bf16 a[176:179], a[28:31], a[100:103], a[176:179]\n" \
"s_add_u32 s64, s64, s68\n" \
"s_addc_u32 s65, s65, 0\n" \
"v_mfma_f32_16x16x32_bf16 a[192:195], a[32:35], a[96:99], a[192:195]\n" \
"s_xor_b32 s77, s77, 0x180\n" \
"ds_read_b128 a[24:27], v72 offset:38912\n" \
"v_mfma_f32_16x16x32_bf16 a[192:195], a[36:39], a[100:103], a[192:195]\n" \
"global_load_dword v238, v234, s[64:65]\n" \
"v_mfma_f32_16x16x32_bf16 a[196:199], a[32:35], a[104:107], a[196:199]\n" \
"ds_read_b64_tr_b16 a[84:85], v98 offset:49664\n" \
"v_mfma_f32_16x16x32_bf16 a[196:199], a[36:39], a[108:111], a[196:199]\n" \
"v_mfma_f32_16x16x32_bf16 a[200:203], a[32:35], a[112:115], a[200:203]\n" \
"s_lshr_b32 s68, s66, 2\n" \
"s_and_b32 s68, s68, 0xffff\n" \
"s_add_u32 s68, s68, 65585152\n" \
"ds_read_b128 a[28:31], v73 offset:38912\n" \
"v_mfma_f32_16x16x32_bf16 a[200:203], a[36:39], a[116:119], a[200:203]\n" \
"buffer_load_dword v238, v242, s[56:59], s68 offen sc1\n" \
"v_mfma_f32_16x16x32_bf16 a[204:207], a[32:35], a[120:123], a[204:207]\n" \
"ds_read_b64_tr_b16 a[86:87], v98 offset:53760\n" \
"v_mfma_f32_16x16x32_bf16 a[204:207], a[36:39], a[124:127], a[204:207]\n" \
"v_mfma_f32_16x16x32_bf16 a[220:223], a[40:43], a[120:123], a[220:223]\n" \
"ds_read_b128 a[32:35], v72 offset:40960\n" \
"v_mfma_f32_16x16x32_bf16 a[220:223], a[44:47], a[124:127], a[220:223]\n" \
"v_mfma_f32_16x16x32_bf16 a[216:219], a[40:43], a[112:115], a[216:219]\n" \
"ds_read_b64_tr_b16 a[88:89], v99 offset:33280\n" \
"v_mfma_f32_16x16x32_bf16 a[216:219], a[44:47], a[116:119], a[216:219]\n" \
"v_mfma_f32_16x16x32_bf16 a[212:215], a[40:43], a[104:107], a[212:215]\n" \
"ds_read_b128 a[36:39], v73 offset:40960\n" \
"v_mfma_f32_16x16x32_bf16 a[212:215], a[44:47], a[108:111], a[212:215]\n" \
"v_mfma_f32_16x16x32_bf16 a[208:211], a[40:43], a[96:99], a[208:211]\n" \
"ds_read_b64_tr_b16 a[90:91], v99 offset:37376\n" \
"v_mfma_f32_16x16x32_bf16 a[208:211], a[44:47], a[100:103], a[208:211]\n" \
"v_mfma_f32_16x16x32_bf16 a[224:227], a[48:51], a[96:99], a[224:227]\n" \
"ds_read_b128 a[40:43], v72 offset:43008\n" \
"v_mfma_f32_16x16x32_bf16 a[224:227], a[52:55], a[100:103], a[224:227]\n" \
"v_mfma_f32_16x16x32_bf16 a[228:231], a[48:51], a[104:107], a[228:231]\n" \
"ds_read_b64_tr_b16 a[92:93], v99 offset:49664\n" \
"v_mfma_f32_16x16x32_bf16 a[228:231], a[52:55], a[108:111], a[228:231]\n" \
"v_mfma_f32_16x16x32_bf16 a[232:235], a[48:51], a[112:115], a[232:235]\n" \
"ds_read_b128 a[44:47], v73 offset:43008\n" \
"v_mfma_f32_16x16x32_bf16 a[232:235], a[52:55], a[116:119], a[232:235]\n" \
"v_mfma_f32_16x16x32_bf16 a[236:239], a[48:51], a[120:123], a[236:239]\n" \
"ds_read_b64_tr_b16 a[94:95], v99 offset:53760\n" \
"v_mfma_f32_16x16x32_bf16 a[236:239], a[52:55], a[124:127], a[236:239]\n" \
"v_mfma_f32_16x16x32_bf16 a[252:255], a[56:59], a[120:123], a[252:255]\n" \
"ds_read_b128 a[48:51], v72 offset:45056\n" \
"v_mfma_f32_16x16x32_bf16 a[252:255], a[60:63], a[124:127], a[252:255]\n" \
"v_mfma_f32_16x16x32_bf16 a[248:251], a[56:59], a[112:115], a[248:251]\n" \
"v_mfma_f32_16x16x32_bf16 a[248:251], a[60:63], a[116:119], a[248:251]\n" \
"v_mfma_f32_16x16x32_bf16 a[244:247], a[56:59], a[104:107], a[244:247]\n" \
"ds_read_b128 a[52:55], v73 offset:45056\n" \
"v_mfma_f32_16x16x32_bf16 a[244:247], a[60:63], a[108:111], a[244:247]\n" \
"v_mfma_f32_16x16x32_bf16 a[240:243], a[56:59], a[96:99], a[240:243]\n" \
"v_mfma_f32_16x16x32_bf16 a[240:243], a[60:63], a[100:103], a[240:243]\n" \
"s_waitcnt lgkmcnt(1)\n" \
"v_mfma_f32_16x16x32_bf16 v[0:3], a[64:67], a[0:3], v[0:3]\n" \
"ds_read_b128 a[56:59], v72 offset:47104\n" \
"v_mfma_f32_16x16x32_bf16 v[0:3], a[68:71], a[4:7], v[0:3]\n" \
"v_mfma_f32_16x16x32_bf16 v[4:7], a[72:75], a[0:3], v[4:7]\n" \
"s_add_u32 m0, s74, 65536\n" \
"ds_read_b128 a[60:63], v73 offset:47104\n" \
"v_mfma_f32_16x16x32_bf16 v[4:7], a[76:79], a[4:7], v[4:7]\n" \
"buffer_load_dwordx4 v224, s[48:51], s75 offen offset:0 lds\n" \
"v_mfma_f32_16x16x32_bf16 v[8:11], a[80:83], a[0:3], v[8:11]\n" \
"ds_read_b64_tr_b16 a[96:97], v98 offset:33792\n" \
"v_mfma_f32_16x16x32_bf16 v[8:11], a[84:87], a[4:7], v[8:11]\n" \
"v_mfma_f32_16x16x32_bf16 v[12:15], a[88:91], a[0:3], v[12:15]\n" \
"ds_read_b64_tr_b16 a[98:99], v98 offset:37888\n" \
"v_mfma_f32_16x16x32_bf16 v[12:15], a[92:95], a[4:7], v[12:15]\n" \
"buffer_load_dwordx4 v225, s[48:51], s75 offen offset:1024 lds\n" \
"v_mfma_f32_16x16x32_bf16 v[28:31], a[88:91], a[8:11], v[28:31]\n" \
"ds_read_b64_tr_b16 a[100:101], v98 offset:50176\n" \
"v_mfma_f32_16x16x32_bf16 v[28:31], a[92:95], a[12:15], v[28:31]\n" \
"v_mfma_f32_16x16x32_bf16 v[24:27], a[80:83], a[8:11], v[24:27]\n" \
"ds_read_b64_tr_b16 a[102:103], v98 offset:54272\n" \
"v_mfma_f32_16x16x32_bf16 v[24:27], a[84:87], a[12:15], v[24:27]\n" \
"buffer_load_dwordx4 v226, s[48:51], s75 offen offset:2048 lds\n" \
"v_mfma_f32_16x16x32_bf16 v[20:23], a[72:75], a[8:11], v[20:23]\n" \
"ds_read_b64_tr_b16 a[104:105], v99 offset:33792\n" \
"v_mfma_f32_16x16x32_bf16 v[20:23], a[76:79], a[12:15], v[20:23]\n" \
"v_mfma_f32_16x16x32_bf16 v[16:19], a[64:67], a[8:11], v[16:19]\n" \
"ds_read_b64_tr_b16 a[106:107], v99 offset:37888\n" \
"v_mfma_f32_16x16x32_bf16 v[16:19], a[68:71], a[12:15], v[16:19]\n" \
"buffer_load_dwordx4 v227, s[48:51], s75 offen offset:3072 lds\n" \
"v_mfma_f32_16x16x32_bf16 v[32:35], a[64:67], a[16:19], v[32:35]\n" \
"ds_read_b64_tr_b16 a[108:109], v99 offset:50176\n" \
"v_mfma_f32_16x16x32_bf16 v[32:35], a[68:71], a[20:23], v[32:35]\n" \
"s_xor_b32 s75, s75, 0x400\n" \
"v_mfma_f32_16x16x32_bf16 v[36:39], a[72:75], a[16:19], v[36:39]\n" \
"s_add_u32 m0, s74, 69632\n" \
"ds_read_b64_tr_b16 a[110:111], v99 offset:54272\n" \
"v_mfma_f32_16x16x32_bf16 v[36:39], a[76:79], a[20:23], v[36:39]\n" \
"buffer_load_dwordx4 v224, s[48:51], s77 offen offset:0 lds\n" \
"v_mfma_f32_16x16x32_bf16 v[40:43], a[80:83], a[16:19], v[40:43]\n" \
"ds_read_b64_tr_b16 a[112:113], v98 offset:34304\n" \
"v_mfma_f32_16x16x32_bf16 v[40:43], a[84:87], a[20:23], v[40:43]\n" \
"v_mfma_f32_16x16x32_bf16 v[44:47], a[88:91], a[16:19], v[44:47]\n" \
"ds_read_b64_tr_b16 a[114:115], v98 offset:38400\n" \
"v_mfma_f32_16x16x32_bf16 v[44:47], a[92:95], a[20:23], v[44:47]\n" \
"buffer_load_dwordx4 v225, s[48:51], s77 offen offset:1024 lds\n" \
"v_mfma_f32_16x16x32_bf16 v[60:63], a[88:91], a[24:27], v[60:63]\n" \
"ds_read_b64_tr_b16 a[116:117], v98 offset:50688\n" \
"v_mfma_f32_16x16x32_bf16 v[60:63], a[92:95], a[28:31], v[60:63]\n" \
"v_mfma_f32_16x16x32_bf16 v[56:59], a[80:83], a[24:27], v[56:59]\n" \
"ds_read_b64_tr_b16 a[118:119], v98 offset:54784\n" \
"v_mfma_f32_16x16x32_bf16 v[56:59], a[84:87], a[28:31], v[56:59]\n" \
"buffer_load_dwordx4 v226, s[48:51], s77 offen offset:2048 lds\n" \
"v_mfma_f32_16x16x32_bf16 v[52:55], a[72:75], a[24:27], v[52:55]\n" \
"ds_read_b64_tr_b16 a[120:121], v99 offset:34304\n" \
"v_mfma_f32_16x16x32_bf16 v[52:55], a[76:79], a[28:31], v[52:55]\n" \
"v_mfma_f32_16x16x32_bf16 v[48:51], a[64:67], a[24:27], v[48:51]\n" \
"ds_read_b64_tr_b16 a[122:123], v99 offset:38400\n" \
"v_mfma_f32_16x16x32_bf16 v[48:51], a[68:71], a[28:31], v[48:51]\n" \
"buffer_load_dwordx4 v227, s[48:51], s77 offen offset:3072 lds\n" \
"v_mfma_f32_16x16x32_bf16 v[144:147], a[64:67], a[32:35], v[144:147]\n" \
"ds_read_b64_tr_b16 a[124:125], v99 offset:50688\n" \
"v_mfma_f32_16x16x32_bf16 v[144:147], a[68:71], a[36:39], v[144:147]\n" \
"v_mfma_f32_16x16x32_bf16 v[148:151], a[72:75], a[32:35], v[148:151]\n" \
"s_xor_b32 s77, s77, 0x400\n" \
"ds_read_b64_tr_b16 a[126:127], v99 offset:54784\n" \
"v_mfma_f32_16x16x32_bf16 v[148:151], a[76:79], a[36:39], v[148:151]\n" \
"v_mfma_f32_16x16x32_bf16 v[152:155], a[80:83], a[32:35], v[152:155]\n" \
"v_mfma_f32_16x16x32_bf16 v[152:155], a[84:87], a[36:39], v[152:155]\n" \
"v_mfma_f32_16x16x32_bf16 v[156:159], a[88:91], a[32:35], v[156:159]\n" \
"v_mfma_f32_16x16x32_bf16 v[156:159], a[92:95], a[36:39], v[156:159]\n" \
"v_mfma_f32_16x16x32_bf16 v[188:191], a[88:91], a[40:43], v[188:191]\n" \
"v_mfma_f32_16x16x32_bf16 v[188:191], a[92:95], a[44:47], v[188:191]\n" \
"v_mfma_f32_16x16x32_bf16 v[184:187], a[80:83], a[40:43], v[184:187]\n" \
"v_mfma_f32_16x16x32_bf16 v[184:187], a[84:87], a[44:47], v[184:187]\n" \
"v_mfma_f32_16x16x32_bf16 v[180:183], a[72:75], a[40:43], v[180:183]\n" \
"v_mfma_f32_16x16x32_bf16 v[180:183], a[76:79], a[44:47], v[180:183]\n" \
"v_mfma_f32_16x16x32_bf16 v[176:179], a[64:67], a[40:43], v[176:179]\n" \
"v_mfma_f32_16x16x32_bf16 v[176:179], a[68:71], a[44:47], v[176:179]\n" \
"s_waitcnt lgkmcnt(0)\n" \
"v_mfma_f32_16x16x32_bf16 v[192:195], a[64:67], a[48:51], v[192:195]\n" \
"v_mfma_f32_16x16x32_bf16 v[192:195], a[68:71], a[52:55], v[192:195]\n" \
"v_mfma_f32_16x16x32_bf16 v[196:199], a[72:75], a[48:51], v[196:199]\n" \
"v_mfma_f32_16x16x32_bf16 v[196:199], a[76:79], a[52:55], v[196:199]\n" \
"v_mfma_f32_16x16x32_bf16 v[200:203], a[80:83], a[48:51], v[200:203]\n" \
"v_mfma_f32_16x16x32_bf16 v[200:203], a[84:87], a[52:55], v[200:203]\n" \
"v_mfma_f32_16x16x32_bf16 v[204:207], a[88:91], a[48:51], v[204:207]\n" \
"v_mfma_f32_16x16x32_bf16 v[204:207], a[92:95], a[52:55], v[204:207]\n" \
"v_mfma_f32_16x16x32_bf16 v[220:223], a[88:91], a[56:59], v[220:223]\n" \
"v_mfma_f32_16x16x32_bf16 v[220:223], a[92:95], a[60:63], v[220:223]\n" \
"v_mfma_f32_16x16x32_bf16 v[216:219], a[80:83], a[56:59], v[216:219]\n" \
"v_mfma_f32_16x16x32_bf16 v[216:219], a[84:87], a[60:63], v[216:219]\n" \
"v_mfma_f32_16x16x32_bf16 v[212:215], a[72:75], a[56:59], v[212:215]\n" \
"v_mfma_f32_16x16x32_bf16 v[212:215], a[76:79], a[60:63], v[212:215]\n" \
"v_mfma_f32_16x16x32_bf16 v[208:211], a[64:67], a[56:59], v[208:211]\n" \
"v_mfma_f32_16x16x32_bf16 v[208:211], a[68:71], a[60:63], v[208:211]\n" \
"s_waitcnt vmcnt(10) lgkmcnt(0)\n" \
"s_barrier\n" \
"v_mfma_f32_16x16x32_bf16 a[128:131], a[0:3], a[96:99], a[128:131]\n" \
"s_add_u32 m0, s74, 98304\n" \
"v_mfma_f32_16x16x32_bf16 a[128:131], a[4:7], a[100:103], a[128:131]\n" \
"buffer_load_dwordx4 v224, s[48:51], s75 offen offset:0 lds\n" \
"v_mfma_f32_16x16x32_bf16 a[132:135], a[0:3], a[104:107], a[132:135]\n" \
"ds_read_b64_tr_b16 a[64:65], v232 offset:0\n" \
"ds_read_b64_tr_b16 a[66:67], v232 offset:2048\n" \
"v_mfma_f32_16x16x32_bf16 a[132:135], a[4:7], a[108:111], a[132:135]\n" \
"v_mfma_f32_16x16x32_bf16 a[136:139], a[0:3], a[112:115], a[136:139]\n" \
"v_mfma_f32_16x16x32_bf16 a[136:139], a[4:7], a[116:119], a[136:139]\n" \
"buffer_load_dwordx4 v225, s[48:51], s75 offen offset:1024 lds\n" \
"v_mfma_f32_16x16x32_bf16 a[140:143], a[0:3], a[120:123], a[140:143]\n" \
"ds_read_b64_tr_b16 a[68:69], v232 offset:8192\n" \
"ds_read_b64_tr_b16 a[70:71], v232 offset:10240\n" \
"v_mfma_f32_16x16x32_bf16 a[140:143], a[4:7], a[124:127], a[140:143]\n" \
"v_mfma_f32_16x16x32_bf16 a[156:159], a[8:11], a[120:123], a[156:159]\n" \
"v_mul_f32 v192, s76, v192\n" \
"v_mul_f32 v193, s76, v193\n" \
"v_mfma_f32_16x16x32_bf16 a[156:159], a[12:15], a[124:127], a[156:159]\n" \
"buffer_load_dwordx4 v226, s[48:51], s75 offen offset:2048 lds\n" \
"v_mfma_f32_16x16x32_bf16 a[152:155], a[8:11], a[112:115], a[152:155]\n" \
"ds_read_b64_tr_b16 a[72:73], v233 offset:0\n" \
"ds_read_b64_tr_b16 a[74:75], v233 offset:2048\n" \
"v_mfma_f32_16x16x32_bf16 a[152:155], a[12:15], a[116:119], a[152:155]\n" \
"v_mul_f32 v194, s76, v194\n" \
"v_mul_f32 v195, s76, v195\n" \
"v_mfma_f32_16x16x32_bf16 a[148:151], a[8:11], a[104:107], a[148:151]\n" \
"v_cvt_pk_bf16_f32 v192, v192, v193\n" \
"v_cvt_pk_bf16_f32 v193, v194, v195\n" \
"v_mfma_f32_16x16x32_bf16 a[148:151], a[12:15], a[108:111], a[148:151]\n" \
"buffer_load_dwordx4 v227, s[48:51], s75 offen offset:3072 lds\n" \
"v_mfma_f32_16x16x32_bf16 a[144:147], a[8:11], a[96:99], a[144:147]\n" \
"ds_read_b64_tr_b16 a[76:77], v233 offset:8192\n" \
"ds_read_b64_tr_b16 a[78:79], v233 offset:10240\n" \
"v_mfma_f32_16x16x32_bf16 a[144:147], a[12:15], a[100:103], a[144:147]\n" \
"s_xor_b32 s75, s75, 0x500\n" \
"v_mul_f32 v196, s76, v196\n" \
"v_mul_f32 v197, s76, v197\n" \
"v_mfma_f32_16x16x32_bf16 a[160:163], a[16:19], a[96:99], a[160:163]\n" \
"s_add_u32 m0, s74, 102400\n" \
"v_mul_f32 v198, s76, v198\n" \
"v_mul_f32 v199, s76, v199\n" \
"v_mfma_f32_16x16x32_bf16 a[160:163], a[20:23], a[100:103], a[160:163]\n" \
"buffer_load_dwordx4 v224, s[48:51], s77 offen offset:0 lds\n" \
"v_mfma_f32_16x16x32_bf16 a[164:167], a[16:19], a[104:107], a[164:167]\n" \
"ds_read_b64_tr_b16 a[80:81], v232 offset:512\n" \
"ds_read_b64_tr_b16 a[82:83], v232 offset:2560\n" \
"v_mfma_f32_16x16x32_bf16 a[164:167], a[20:23], a[108:111], a[164:167]\n" \
"v_cvt_pk_bf16_f32 v194, v196, v197\n" \
"v_cvt_pk_bf16_f32 v195, v198, v199\n" \
"v_mfma_f32_16x16x32_bf16 a[168:171], a[16:19], a[112:115], a[168:171]\n" \
"v_mul_f32 v208, s76, v208\n" \
"v_mul_f32 v209, s76, v209\n" \
"v_mfma_f32_16x16x32_bf16 a[168:171], a[20:23], a[116:119], a[168:171]\n" \
"buffer_load_dwordx4 v225, s[48:51], s77 offen offset:1024 lds\n" \
"v_mfma_f32_16x16x32_bf16 a[172:175], a[16:19], a[120:123], a[172:175]\n" \
"ds_read_b64_tr_b16 a[84:85], v232 offset:8704\n" \
"ds_read_b64_tr_b16 a[86:87], v232 offset:10752\n" \
"v_mfma_f32_16x16x32_bf16 a[172:175], a[20:23], a[124:127], a[172:175]\n" \
"v_mul_f32 v210, s76, v210\n" \
"v_mul_f32 v211, s76, v211\n" \
"v_mfma_f32_16x16x32_bf16 a[188:191], a[24:27], a[120:123], a[188:191]\n" \
"v_cvt_pk_bf16_f32 v208, v208, v209\n" \
"v_cvt_pk_bf16_f32 v209, v210, v211\n" \
"v_mfma_f32_16x16x32_bf16 a[188:191], a[28:31], a[124:127], a[188:191]\n" \
"buffer_load_dwordx4 v226, s[48:51], s77 offen offset:2048 lds\n" \
"v_mfma_f32_16x16x32_bf16 a[184:187], a[24:27], a[112:115], a[184:187]\n" \
"ds_read_b64_tr_b16 a[88:89], v233 offset:512\n" \
"ds_read_b64_tr_b16 a[90:91], v233 offset:2560\n" \
"v_mfma_f32_16x16x32_bf16 a[184:187], a[28:31], a[116:119], a[184:187]\n" \
"v_mul_f32 v212, s76, v212\n" \
"v_mul_f32 v213, s76, v213\n" \
"v_mfma_f32_16x16x32_bf16 a[180:183], a[24:27], a[104:107], a[180:183]\n" \
"v_mul_f32 v214, s76, v214\n" \
"v_mul_f32 v215, s76, v215\n" \
"v_mfma_f32_16x16x32_bf16 a[180:183], a[28:31], a[108:111], a[180:183]\n" \
"buffer_load_dwordx4 v227, s[48:51], s77 offen offset:3072 lds\n" \
"v_mfma_f32_16x16x32_bf16 a[176:179], a[24:27], a[96:99], a[176:179]\n" \
"ds_read_b64_tr_b16 a[92:93], v233 offset:8704\n" \
"ds_read_b64_tr_b16 a[94:95], v233 offset:10752\n" \
"v_mfma_f32_16x16x32_bf16 a[176:179], a[28:31], a[100:103], a[176:179]\n" \
"s_xor_b32 s77, s77, 0x500\n" \
"v_cvt_pk_bf16_f32 v210, v212, v213\n" \
"v_cvt_pk_bf16_f32 v211, v214, v215\n" \
"v_mfma_f32_16x16x32_bf16 a[192:195], a[32:35], a[96:99], a[192:195]\n" \
"s_add_u32 m0, s79, 32768\n" \
"v_mul_f32 v200, s76, v200\n" \
"v_mul_f32 v201, s76, v201\n" \
"v_mfma_f32_16x16x32_bf16 a[192:195], a[36:39], a[100:103], a[192:195]\n" \
"buffer_load_dwordx4 v228, s[52:55], s78 offen offset:0 lds\n" \
"v_mfma_f32_16x16x32_bf16 a[196:199], a[32:35], a[104:107], a[196:199]\n" \
"ds_read_b128 a[0:3], v250 offset:0\n" \
"ds_read_b128 a[4:7], v251 offset:0\n" \
"v_mfma_f32_16x16x32_bf16 a[196:199], a[36:39], a[108:111], a[196:199]\n" \
"v_mul_f32 v202, s76, v202\n" \
"v_mul_f32 v203, s76, v203\n" \
"v_mfma_f32_16x16x32_bf16 a[200:203], a[32:35], a[112:115], a[200:203]\n" \
"v_cvt_pk_bf16_f32 v200, v200, v201\n" \
"v_cvt_pk_bf16_f32 v201, v202, v203\n" \
"v_mfma_f32_16x16x32_bf16 a[200:203], a[36:39], a[116:119], a[200:203]\n" \
"buffer_load_dwordx4 v229, s[52:55], s78 offen offset:1024 lds\n" \
"v_mfma_f32_16x16x32_bf16 a[204:207], a[32:35], a[120:123], a[204:207]\n" \
"ds_read_b128 a[8:11], v250 offset:2048\n" \
"ds_read_b128 a[12:15], v251 offset:2048\n" \
"v_mfma_f32_16x16x32_bf16 a[204:207], a[36:39], a[124:127], a[204:207]\n" \
"v_mul_f32 v204, s76, v204\n" \
"v_mul_f32 v205, s76, v205\n" \
"v_mfma_f32_16x16x32_bf16 a[220:223], a[40:43], a[120:123], a[220:223]\n" \
"s_xor_b32 s78, s78, 0x500000\n" \
"v_mul_f32 v206, s76, v206\n" \
"v_mul_f32 v207, s76, v207\n" \
"v_mfma_f32_16x16x32_bf16 a[220:223], a[44:47], a[124:127], a[220:223]\n" \
"buffer_load_dwordx4 v228, s[52:55], s89 offen offset:2048 lds\n" \
"v_mfma_f32_16x16x32_bf16 a[216:219], a[40:43], a[112:115], a[216:219]\n" \
"ds_read_b128 a[16:19], v250 offset:4096\n" \
"ds_read_b128 a[20:23], v251 offset:4096\n" \
"v_mfma_f32_16x16x32_bf16 a[216:219], a[44:47], a[116:119], a[216:219]\n" \
"v_cvt_pk_bf16_f32 v202, v204, v205\n" \
"v_cvt_pk_bf16_f32 v203, v206, v207\n" \
"v_mfma_f32_16x16x32_bf16 a[212:215], a[40:43], a[104:107], a[212:215]\n" \
"v_mul_f32 v216, s76, v216\n" \
"v_mul_f32 v217, s76, v217\n" \
"v_mfma_f32_16x16x32_bf16 a[212:215], a[44:47], a[108:111], a[212:215]\n" \
"buffer_load_dwordx4 v229, s[52:55], s89 offen offset:3072 lds\n" \
"v_mfma_f32_16x16x32_bf16 a[208:211], a[40:43], a[96:99], a[208:211]\n" \
"v_mul_f32 v218, s76, v218\n" \
"v_mul_f32 v219, s76, v219\n" \
"v_mfma_f32_16x16x32_bf16 a[208:211], a[44:47], a[100:103], a[208:211]\n" \
"s_xor_b32 s89, s89, 0x500000\n" \
"v_cvt_pk_bf16_f32 v216, v216, v217\n" \
"v_cvt_pk_bf16_f32 v217, v218, v219\n" \
"v_mfma_f32_16x16x32_bf16 a[224:227], a[48:51], a[96:99], a[224:227]\n" \
"s_add_u32 m0, s79, 49152\n" \
"v_mul_f32 v220, s76, v220\n" \
"v_mul_f32 v221, s76, v221\n" \
"v_mfma_f32_16x16x32_bf16 a[224:227], a[52:55], a[100:103], a[224:227]\n" \
"buffer_load_dwordx4 v228, s[52:55], s78 offen offset:0 lds\n" \
"v_mfma_f32_16x16x32_bf16 a[228:231], a[48:51], a[104:107], a[228:231]\n" \
"v_mul_f32 v222, s76, v222\n" \
"v_mul_f32 v223, s76, v223\n" \
"v_mfma_f32_16x16x32_bf16 a[228:231], a[52:55], a[108:111], a[228:231]\n" \
"v_cvt_pk_bf16_f32 v218, v220, v221\n" \
"v_cvt_pk_bf16_f32 v219, v222, v223\n" \
"v_mfma_f32_16x16x32_bf16 a[232:235], a[48:51], a[112:115], a[232:235]\n" \
"v_mul_f32 v144, s76, v144\n" \
"v_mul_f32 v145, s76, v145\n" \
"v_mfma_f32_16x16x32_bf16 a[232:235], a[52:55], a[116:119], a[232:235]\n" \
"buffer_load_dwordx4 v229, s[52:55], s78 offen offset:1024 lds\n" \
"v_mfma_f32_16x16x32_bf16 a[236:239], a[48:51], a[120:123], a[236:239]\n" \
"v_mul_f32 v146, s76, v146\n" \
"v_mul_f32 v147, s76, v147\n" \
"v_mfma_f32_16x16x32_bf16 a[236:239], a[52:55], a[124:127], a[236:239]\n" \
"v_cvt_pk_bf16_f32 v196, v144, v145\n" \
"v_cvt_pk_bf16_f32 v197, v146, v147\n" \
"v_mfma_f32_16x16x32_bf16 a[252:255], a[56:59], a[120:123], a[252:255]\n" \
"s_xor_b32 s78, s78, 0x180000\n" \
"v_mul_f32 v148, s76, v148\n" \
"v_mul_f32 v149, s76, v149\n" \
"v_mfma_f32_16x16x32_bf16 a[252:255], a[60:63], a[124:127], a[252:255]\n" \
"buffer_load_dwordx4 v228, s[52:55], s89 offen offset:2048 lds\n" \
"v_mfma_f32_16x16x32_bf16 a[248:251], a[56:59], a[112:115], a[248:251]\n" \
"v_mul_f32 v150, s76, v150\n" \
"v_mul_f32 v151, s76, v151\n" \
"v_mfma_f32_16x16x32_bf16 a[248:251], a[60:63], a[116:119], a[248:251]\n" \
"v_cvt_pk_bf16_f32 v198, v148, v149\n" \
"v_cvt_pk_bf16_f32 v199, v150, v151\n" \
"v_mfma_f32_16x16x32_bf16 a[244:247], a[56:59], a[104:107], a[244:247]\n" \
"v_mul_f32 v176, s76, v176\n" \
"v_mul_f32 v177, s76, v177\n" \
"v_mfma_f32_16x16x32_bf16 a[244:247], a[60:63], a[108:111], a[244:247]\n" \
"buffer_load_dwordx4 v229, s[52:55], s89 offen offset:3072 lds\n" \
"v_mfma_f32_16x16x32_bf16 a[240:243], a[56:59], a[96:99], a[240:243]\n" \
"s_waitcnt lgkmcnt(0)\n" \
"v_mul_f32 v178, s76, v178\n" \
"v_mul_f32 v179, s76, v179\n" \
"v_mfma_f32_16x16x32_bf16 a[240:243], a[60:63], a[100:103], a[240:243]\n" \
"s_xor_b32 s89, s89, 0x180000\n" \
"v_cvt_pk_bf16_f32 v212, v176, v177\n" \
"v_cvt_pk_bf16_f32 v213, v178, v179\n" \
"v_mfma_f32_16x16x32_bf16 v[64:67], a[64:67], a[0:3], 0\n" \
"ds_read_b128 a[24:27], v250 offset:6144\n" \
"v_mul_f32 v180, s76, v180\n" \
"v_mfma_f32_16x16x32_bf16 v[64:67], a[68:71], a[4:7], v[64:67]\n" \
"s_add_u32 m0, s74, 131072\n" \
"ds_read_b128 a[28:31], v251 offset:6144\n" \
"v_mul_f32 v181, s76, v181\n" \
"v_mfma_f32_16x16x32_bf16 v[68:71], a[72:75], a[0:3], 0\n" \
"ds_read_b64_tr_b16 a[96:97], v232 offset:1024\n" \
"v_mul_f32 v182, s76, v182\n" \
"v_mfma_f32_16x16x32_bf16 v[68:71], a[76:79], a[4:7], v[68:71]\n" \
"buffer_load_dwordx4 v224, s[48:51], s75 offen offset:0 lds\n" \
"v_mfma_f32_16x16x32_bf16 v[72:75], a[80:83], a[0:3], 0\n" \
"ds_read_b64_tr_b16 a[98:99], v232 offset:3072\n" \
"v_mul_f32 v183, s76, v183\n" \
"v_mfma_f32_16x16x32_bf16 v[72:75], a[84:87], a[4:7], v[72:75]\n" \
"ds_read_b64_tr_b16 a[100:101], v232 offset:9216\n" \
"v_cvt_pk_bf16_f32 v214, v180, v181\n" \
"v_mfma_f32_16x16x32_bf16 v[76:79], a[88:91], a[0:3], 0\n" \
"ds_read_b64_tr_b16 a[102:103], v232 offset:11264\n" \
"v_cvt_pk_bf16_f32 v215, v182, v183\n" \
"v_mfma_f32_16x16x32_bf16 v[76:79], a[92:95], a[4:7], v[76:79]\n" \
"buffer_load_dwordx4 v225, s[48:51], s75 offen offset:1024 lds\n" \
"v_mfma_f32_16x16x32_bf16 v[108:111], a[88:91], a[8:11], 0\n" \
"ds_read_b64_tr_b16 a[104:105], v233 offset:1024\n" \
"v_mul_f32 v152, s76, v152\n" \
"v_mfma_f32_16x16x32_bf16 v[108:111], a[92:95], a[12:15], v[108:111]\n" \
"ds_read_b64_tr_b16 a[106:107], v233 offset:3072\n" \
"v_mul_f32 v153, s76, v153\n" \
"v_mfma_f32_16x16x32_bf16 v[104:107], a[80:83], a[8:11], 0\n" \
"ds_read_b64_tr_b16 a[108:109], v233 offset:9216\n" \
"v_mul_f32 v154, s76, v154\n" \
"v_mfma_f32_16x16x32_bf16 v[104:107], a[84:87], a[12:15], v[104:107]\n" \
"buffer_load_dwordx4 v226, s[48:51], s75 offen offset:2048 lds\n" \
"v_mfma_f32_16x16x32_bf16 v[100:103], a[72:75], a[8:11], 0\n" \
"ds_read_b64_tr_b16 a[110:111], v233 offset:11264\n" \
"v_mul_f32 v155, s76, v155\n" \
"v_mfma_f32_16x16x32_bf16 v[100:103], a[76:79], a[12:15], v[100:103]\n" \
"ds_read_b64_tr_b16 a[112:113], v232 offset:1536\n" \
"v_cvt_pk_bf16_f32 v204, v152, v153\n" \
"v_mfma_f32_16x16x32_bf16 v[96:99], a[64:67], a[8:11], 0\n" \
"ds_read_b64_tr_b16 a[114:115], v232 offset:3584\n" \
"v_cvt_pk_bf16_f32 v205, v154, v155\n" \
"v_mfma_f32_16x16x32_bf16 v[96:99], a[68:71], a[12:15], v[96:99]\n" \
"buffer_load_dwordx4 v227, s[48:51], s75 offen offset:3072 lds\n" \
"v_mfma_f32_16x16x32_bf16 v[128:131], a[64:67], a[16:19], 0\n" \
"s_xor_b32 s75, s75, 0x180\n" \
"ds_read_b64_tr_b16 a[116:117], v232 offset:9728\n" \
"v_mul_f32 v156, s76, v156\n" \
"v_mfma_f32_16x16x32_bf16 v[128:131], a[68:71], a[20:23], v[128:131]\n" \
"s_add_u32 m0, s74, 135168\n" \
"ds_read_b64_tr_b16 a[118:119], v232 offset:11776\n" \
"v_mul_f32 v157, s76, v157\n" \
"v_mfma_f32_16x16x32_bf16 v[132:135], a[72:75], a[16:19], 0\n" \
"ds_read_b64_tr_b16 a[120:121], v233 offset:1536\n" \
"v_mul_f32 v158, s76, v158\n" \
"v_mfma_f32_16x16x32_bf16 v[132:135], a[76:79], a[20:23], v[132:135]\n" \
"buffer_load_dwordx4 v224, s[48:51], s77 offen offset:0 lds\n" \
"v_mfma_f32_16x16x32_bf16 v[136:139], a[80:83], a[16:19], 0\n" \
"ds_read_b64_tr_b16 a[122:123], v233 offset:3584\n" \
"Lpg2207_%=:\n" \
"Lst0x3_%=:\n" \
"s_cbranch_execz Lpg4445_%=\n" \
"v_mul_f32 v159, s76, v159\n" \
"v_mfma_f32_16x16x32_bf16 v[136:139], a[84:87], a[20:23], v[136:139]\n" \
"ds_read_b64_tr_b16 a[124:125], v233 offset:9728\n" \
"v_cvt_pk_bf16_f32 v206, v156, v157\n" \
"v_mfma_f32_16x16x32_bf16 v[140:143], a[88:91], a[16:19], 0\n" \
"s_waitcnt lgkmcnt(3)\n" \
"ds_read_b64_tr_b16 a[126:127], v233 offset:11776\n" \
"v_cvt_pk_bf16_f32 v207, v158, v159\n" \
"v_mfma_f32_16x16x32_bf16 v[140:143], a[92:95], a[20:23], v[140:143]\n" \
"buffer_load_dwordx4 v225, s[48:51], s77 offen offset:1024 lds\n" \
"v_mfma_f32_16x16x32_bf16 v[172:175], a[88:91], a[24:27], 0\n" \
"s_add_u32 s93, s87, 8192\n" \
"v_mul_f32 v184, s76, v184\n" \
"v_mul_f32 v185, s76, v185\n" \
"v_mfma_f32_16x16x32_bf16 v[172:175], a[92:95], a[28:31], v[172:175]\n" \
"buffer_store_dwordx4 v[192:195], v236, s[56:59], s93 offen offset:0 sc1\n" \
"v_mfma_f32_16x16x32_bf16 v[168:171], a[80:83], a[24:27], 0\n" \
"v_mul_f32 v186, s76, v186\n" \
"v_mul_f32 v187, s76, v187\n" \
"v_mfma_f32_16x16x32_bf16 v[168:171], a[84:87], a[28:31], v[168:171]\n" \
"buffer_load_dwordx4 v226, s[48:51], s77 offen offset:2048 lds\n" \
"v_mfma_f32_16x16x32_bf16 v[164:167], a[72:75], a[24:27], 0\n" \
"v_cvt_pk_bf16_f32 v220, v184, v185\n" \
"v_cvt_pk_bf16_f32 v221, v186, v187\n" \
"v_mfma_f32_16x16x32_bf16 v[164:167], a[76:79], a[28:31], v[164:167]\n" \
"buffer_load_dwordx4 v227, s[48:51], s77 offen offset:3072 lds\n" \
"v_mfma_f32_16x16x32_bf16 v[160:163], a[64:67], a[24:27], 0\n" \
"v_mul_f32 v188, s76, v188\n" \
"v_mul_f32 v189, s76, v189\n" \
"v_mfma_f32_16x16x32_bf16 v[160:163], a[68:71], a[28:31], v[160:163]\n" \
"s_waitcnt vmcnt(43) lgkmcnt(0)\n" \
"s_barrier\n" \
"v_mfma_f32_16x16x32_bf16 v[80:83], a[96:99], a[0:3], 0\n" \
"s_xor_b32 s77, s77, 0x180\n" \
"ds_read_b64_tr_b16 a[64:65], v232 offset:16384\n" \
"v_mul_f32 v190, s76, v190\n" \
"v_mfma_f32_16x16x32_bf16 v[80:83], a[100:103], a[4:7], v[80:83]\n" \
"ds_read_b64_tr_b16 a[66:67], v232 offset:18432\n" \
"v_mul_f32 v191, s76, v191\n" \
"v_mfma_f32_16x16x32_bf16 v[84:87], a[104:107], a[0:3], 0\n" \
"s_add_u32 m0, s79, 0\n" \
"ds_read_b64_tr_b16 a[68:69], v232 offset:24576\n" \
"v_cvt_pk_bf16_f32 v222, v188, v189\n" \
"v_mfma_f32_16x16x32_bf16 v[84:87], a[108:111], a[4:7], v[84:87]\n" \
"buffer_load_dwordx4 v228, s[52:55], s78 offen offset:0 lds\n" \
"v_mfma_f32_16x16x32_bf16 v[88:91], a[112:115], a[0:3], 0\n" \
"ds_read_b64_tr_b16 a[70:71], v232 offset:26624\n" \
"v_cvt_pk_bf16_f32 v223, v190, v191\n" \
"v_mfma_f32_16x16x32_bf16 v[88:91], a[116:119], a[4:7], v[88:91]\n" \
"ds_read_b64_tr_b16 a[72:73], v233 offset:16384\n" \
"v_accvgpr_read_b32 v243, a224\n" \
"v_mfma_f32_16x16x32_bf16 v[92:95], a[120:123], a[0:3], 0\n" \
"ds_read_b64_tr_b16 a[74:75], v233 offset:18432\n" \
"v_accvgpr_read_b32 v252, a225\n" \
"v_mfma_f32_16x16x32_bf16 v[92:95], a[124:127], a[4:7], v[92:95]\n" \
"buffer_load_dwordx4 v229, s[52:55], s78 offen offset:1024 lds\n" \
"v_mfma_f32_16x16x32_bf16 v[124:127], a[120:123], a[8:11], 0\n" \
"s_waitcnt vmcnt(33)\n" \
"ds_read_b128 a[0:3], v230 offset:0\n" \
"v_accvgpr_read_b32 v253, a226\n" \
"v_mfma_f32_16x16x32_bf16 v[124:127], a[124:127], a[12:15], v[124:127]\n" \
"ds_read_b128 a[4:7], v231 offset:0\n" \
"v_accvgpr_read_b32 v254, a227\n" \
"v_mfma_f32_16x16x32_bf16 v[120:123], a[112:115], a[8:11], 0\n" \
"s_xor_b32 s78, s78, 0x300000\n" \
"ds_read_b64_tr_b16 a[76:77], v233 offset:24576\n" \
"v_mul_f32 v243, s31, v243\n" \
"v_mfma_f32_16x16x32_bf16 v[120:123], a[116:119], a[12:15], v[120:123]\n" \
"buffer_load_dwordx4 v228, s[52:55], s89 offen offset:2048 lds\n" \
"v_mfma_f32_16x16x32_bf16 v[116:119], a[104:107], a[8:11], 0\n" \
"ds_read_b64_tr_b16 a[78:79], v233 offset:26624\n" \
"v_mul_f32 v252, s31, v252\n" \
"v_mfma_f32_16x16x32_bf16 v[116:119], a[108:111], a[12:15], v[116:119]\n" \
"ds_read_b64_tr_b16 a[80:81], v232 offset:16896\n" \
"v_mul_f32 v253, s31, v253\n" \
"v_mfma_f32_16x16x32_bf16 v[112:115], a[96:99], a[8:11], 0\n" \
"ds_read_b64_tr_b16 a[82:83], v232 offset:18944\n" \
"v_mul_f32 v254, s31, v254\n" \
"v_mfma_f32_16x16x32_bf16 v[112:115], a[100:103], a[12:15], v[112:115]\n" \
"buffer_load_dwordx4 v229, s[52:55], s89 offen offset:3072 lds\n" \
"v_mfma_f32_16x16x32_bf16 v[144:147], a[96:99], a[16:19], 0\n" \
"s_waitcnt vmcnt(33)\n" \
"s_xor_b32 s89, s89, 0x300000\n" \
"ds_read_b128 a[8:11], v230 offset:2048\n" \
"v_mfma_f32_16x16x32_bf16 v[144:147], a[100:103], a[20:23], v[144:147]\n" \
"s_add_u32 m0, s74, 65536\n" \
"ds_read_b128 a[12:15], v231 offset:2048\n" \
"v_cvt_pk_bf16_f32 v243, v243, v252\n" \
"v_mfma_f32_16x16x32_bf16 v[148:151], a[104:107], a[16:19], 0\n" \
"s_waitcnt lgkmcnt(6)\n" \
"ds_read_b64_tr_b16 a[84:85], v232 offset:25088\n" \
"v_cvt_pk_bf16_f32 v253, v253, v254\n" \
"v_mfma_f32_16x16x32_bf16 v[148:151], a[108:111], a[20:23], v[148:151]\n" \
"buffer_load_dwordx4 v224, s[48:51], s75 offen offset:0 lds\n" \
"v_mfma_f32_16x16x32_bf16 v[152:155], a[112:115], a[16:19], 0\n" \
"ds_read_b64_tr_b16 a[86:87], v232 offset:27136\n" \
"v_accvgpr_write_b32 a224, v243\n" \
"v_mfma_f32_16x16x32_bf16 v[152:155], a[116:119], a[20:23], v[152:155]\n" \
"ds_read_b64_tr_b16 a[88:89], v233 offset:16896\n" \
"v_accvgpr_write_b32 a225, v253\n" \
"v_mfma_f32_16x16x32_bf16 v[156:159], a[120:123], a[16:19], 0\n" \
"ds_read_b64_tr_b16 a[90:91], v233 offset:18944\n" \
"v_accvgpr_read_b32 v243, a240\n" \
"v_mfma_f32_16x16x32_bf16 v[156:159], a[124:127], a[20:23], v[156:159]\n" \
"buffer_load_dwordx4 v225, s[48:51], s75 offen offset:1024 lds\n" \
"v_mfma_f32_16x16x32_bf16 v[188:191], a[120:123], a[24:27], 0\n" \
"s_waitcnt vmcnt(33)\n" \
"ds_read_b128 a[16:19], v230 offset:4096\n" \
"v_accvgpr_read_b32 v252, a241\n" \
"v_mfma_f32_16x16x32_bf16 v[188:191], a[124:127], a[28:31], v[188:191]\n" \
"ds_read_b128 a[20:23], v231 offset:4096\n" \
"v_accvgpr_read_b32 v253, a242\n" \
"v_mfma_f32_16x16x32_bf16 v[184:187], a[112:115], a[24:27], 0\n" \
"s_waitcnt lgkmcnt(6)\n" \
"ds_read_b64_tr_b16 a[92:93], v233 offset:25088\n" \
"v_accvgpr_read_b32 v254, a243\n" \
"v_mfma_f32_16x16x32_bf16 v[184:187], a[116:119], a[28:31], v[184:187]\n" \
"buffer_load_dwordx4 v226, s[48:51], s75 offen offset:2048 lds\n" \
"v_mfma_f32_16x16x32_bf16 v[180:183], a[104:107], a[24:27], 0\n" \
"ds_read_b64_tr_b16 a[94:95], v233 offset:27136\n" \
"v_mul_f32 v243, s31, v243\n" \
"v_mfma_f32_16x16x32_bf16 v[180:183], a[108:111], a[28:31], v[180:183]\n" \
"v_mul_f32 v252, s31, v252\n" \
"v_mul_f32 v253, s31, v253\n" \
"v_mfma_f32_16x16x32_bf16 v[176:179], a[96:99], a[24:27], 0\n" \
"v_mul_f32 v254, s31, v254\n" \
"v_cvt_pk_bf16_f32 v243, v243, v252\n" \
"v_mfma_f32_16x16x32_bf16 v[176:179], a[100:103], a[28:31], v[176:179]\n" \
"buffer_load_dwordx4 v227, s[48:51], s75 offen offset:3072 lds\n" \
"v_mfma_f32_16x16x32_bf16 v[64:67], a[64:67], a[0:3], v[64:67]\n" \
"s_waitcnt vmcnt(33)\n" \
"s_xor_b32 s75, s75, 0x300\n" \
"ds_read_b128 a[24:27], v230 offset:6144\n" \
"v_mfma_f32_16x16x32_bf16 v[64:67], a[68:71], a[4:7], v[64:67]\n" \
"s_add_u32 m0, s74, 69632\n" \
"ds_read_b128 a[28:31], v231 offset:6144\n" \
"v_cvt_pk_bf16_f32 v253, v253, v254\n" \
"v_mfma_f32_16x16x32_bf16 v[68:71], a[72:75], a[0:3], v[68:71]\n" \
"s_waitcnt lgkmcnt(4)\n" \
"ds_read_b64_tr_b16 a[96:97], v232 offset:17408\n" \
"v_accvgpr_write_b32 a226, v243\n" \
"v_mfma_f32_16x16x32_bf16 v[68:71], a[76:79], a[4:7], v[68:71]\n" \
"buffer_load_dwordx4 v224, s[48:51], s77 offen offset:0 lds\n" \
"v_mfma_f32_16x16x32_bf16 v[72:75], a[80:83], a[0:3], v[72:75]\n" \
"ds_read_b64_tr_b16 a[98:99], v232 offset:19456\n" \
"v_accvgpr_write_b32 a227, v253\n" \
"v_mfma_f32_16x16x32_bf16 v[72:75], a[84:87], a[4:7], v[72:75]\n" \
"s_waitcnt lgkmcnt(3)\n" \
"ds_read_b64_tr_b16 a[100:101], v232 offset:25600\n" \
"v_accvgpr_read_b32 v243, a228\n" \
"v_mfma_f32_16x16x32_bf16 v[76:79], a[88:91], a[0:3], v[76:79]\n" \
"ds_read_b64_tr_b16 a[102:103], v232 offset:27648\n" \
"v_accvgpr_read_b32 v252, a229\n" \
"v_mfma_f32_16x16x32_bf16 v[76:79], a[92:95], a[4:7], v[76:79]\n" \
"buffer_load_dwordx4 v225, s[48:51], s77 offen offset:1024 lds\n" \
"v_mfma_f32_16x16x32_bf16 v[108:111], a[88:91], a[8:11], v[108:111]\n" \
"ds_read_b64_tr_b16 a[104:105], v233 offset:17408\n" \
"v_accvgpr_read_b32 v253, a230\n" \
"v_mfma_f32_16x16x32_bf16 v[108:111], a[92:95], a[12:15], v[108:111]\n" \
"ds_read_b64_tr_b16 a[106:107], v233 offset:19456\n" \
"v_accvgpr_read_b32 v254, a231\n" \
"v_mfma_f32_16x16x32_bf16 v[104:107], a[80:83], a[8:11], v[104:107]\n" \
"s_waitcnt lgkmcnt(6)\n" \
"ds_read_b64_tr_b16 a[108:109], v233 offset:25600\n" \
"v_mul_f32 v243, s31, v243\n" \
"v_mfma_f32_16x16x32_bf16 v[104:107], a[84:87], a[12:15], v[104:107]\n" \
"buffer_load_dwordx4 v226, s[48:51], s77 offen offset:2048 lds\n" \
"v_mfma_f32_16x16x32_bf16 v[100:103], a[72:75], a[8:11], v[100:103]\n" \
"ds_read_b64_tr_b16 a[110:111], v233 offset:27648\n" \
"v_mul_f32 v252, s31, v252\n" \
"v_mfma_f32_16x16x32_bf16 v[100:103], a[76:79], a[12:15], v[100:103]\n" \
"ds_read_b64_tr_b16 a[112:113], v232 offset:17920\n" \
"v_mul_f32 v253, s31, v253\n" \
"v_mfma_f32_16x16x32_bf16 v[96:99], a[64:67], a[8:11], v[96:99]\n" \
"ds_read_b64_tr_b16 a[114:115], v232 offset:19968\n" \
"v_mul_f32 v254, s31, v254\n" \
"v_mfma_f32_16x16x32_bf16 v[96:99], a[68:71], a[12:15], v[96:99]\n" \
"buffer_load_dwordx4 v227, s[48:51], s77 offen offset:3072 lds\n" \
"v_mfma_f32_16x16x32_bf16 v[128:131], a[64:67], a[16:19], v[128:131]\n" \
"ds_read_b64_tr_b16 a[116:117], v232 offset:26112\n" \
"v_cvt_pk_bf16_f32 v243, v243, v252\n" \
"v_mfma_f32_16x16x32_bf16 v[128:131], a[68:71], a[20:23], v[128:131]\n" \
"ds_read_b64_tr_b16 a[118:119], v232 offset:28160\n" \
"v_cvt_pk_bf16_f32 v253, v253, v254\n" \
"v_mfma_f32_16x16x32_bf16 v[132:135], a[72:75], a[16:19], v[132:135]\n" \
"s_xor_b32 s77, s77, 0x300\n" \
"ds_read_b64_tr_b16 a[120:121], v233 offset:17920\n" \
"v_accvgpr_write_b32 a228, v243\n" \
"v_mfma_f32_16x16x32_bf16 v[132:135], a[76:79], a[20:23], v[132:135]\n" \
"v_accvgpr_write_b32 a229, v253\n" \
"v_accvgpr_read_b32 v243, a244\n" \
"v_mfma_f32_16x16x32_bf16 v[136:139], a[80:83], a[16:19], v[136:139]\n" \
"ds_read_b64_tr_b16 a[122:123], v233 offset:19968\n" \
"v_accvgpr_read_b32 v252, a245\n" \
"v_mfma_f32_16x16x32_bf16 v[136:139], a[84:87], a[20:23], v[136:139]\n" \
"ds_read_b64_tr_b16 a[124:125], v233 offset:26112\n" \
"v_accvgpr_read_b32 v253, a246\n" \
"v_mfma_f32_16x16x32_bf16 v[140:143], a[88:91], a[16:19], v[140:143]\n" \
"ds_read_b64_tr_b16 a[126:127], v233 offset:28160\n" \
"v_accvgpr_read_b32 v254, a247\n" \
"v_mfma_f32_16x16x32_bf16 v[140:143], a[92:95], a[20:23], v[140:143]\n" \
"buffer_store_dwordx4 v[208:211], v236, s[56:59], s93 offen offset:256 sc1\n" \
"v_mfma_f32_16x16x32_bf16 v[172:175], a[88:91], a[24:27], v[172:175]\n" \
"v_mul_f32 v243, s31, v243\n" \
"v_mul_f32 v252, s31, v252\n" \
"v_mfma_f32_16x16x32_bf16 v[172:175], a[92:95], a[28:31], v[172:175]\n" \
"v_mul_f32 v253, s31, v253\n" \
"v_mul_f32 v254, s31, v254\n" \
"v_mfma_f32_16x16x32_bf16 v[168:171], a[80:83], a[24:27], v[168:171]\n" \
"v_cvt_pk_bf16_f32 v243, v243, v252\n" \
"v_cvt_pk_bf16_f32 v253, v253, v254\n" \
"v_mfma_f32_16x16x32_bf16 v[168:171], a[84:87], a[28:31], v[168:171]\n" \
"v_accvgpr_write_b32 a230, v243\n" \
"v_accvgpr_write_b32 a231, v253\n" \
"v_mfma_f32_16x16x32_bf16 v[164:167], a[72:75], a[24:27], v[164:167]\n" \
"v_accvgpr_read_b32 v243, a232\n" \
"v_accvgpr_read_b32 v252, a233\n" \
"v_mfma_f32_16x16x32_bf16 v[164:167], a[76:79], a[28:31], v[164:167]\n" \
"v_accvgpr_read_b32 v253, a234\n" \
"v_accvgpr_read_b32 v254, a235\n" \
"v_mfma_f32_16x16x32_bf16 v[160:163], a[64:67], a[24:27], v[160:163]\n" \
"v_mul_f32 v243, s31, v243\n" \
"v_mul_f32 v252, s31, v252\n" \
"v_mfma_f32_16x16x32_bf16 v[160:163], a[68:71], a[28:31], v[160:163]\n" \
"s_waitcnt vmcnt(26) lgkmcnt(0)\n" \
"s_barrier\n" \
"v_mfma_f32_16x16x32_bf16 v[80:83], a[96:99], a[0:3], v[80:83]\n" \
"ds_read_b64_tr_b16 a[64:65], v232 offset:32768\n" \
"v_mul_f32 v253, s31, v253\n" \
"v_mfma_f32_16x16x32_bf16 v[80:83], a[100:103], a[4:7], v[80:83]\n" \
"ds_read_b64_tr_b16 a[66:67], v232 offset:34816\n" \
"v_mul_f32 v254, s31, v254\n" \
"v_mfma_f32_16x16x32_bf16 v[84:87], a[104:107], a[0:3], v[84:87]\n" \
"s_add_u32 m0, s79, 16384\n" \
"ds_read_b64_tr_b16 a[68:69], v232 offset:40960\n" \
"v_cvt_pk_bf16_f32 v243, v243, v252\n" \
"v_mfma_f32_16x16x32_bf16 v[84:87], a[108:111], a[4:7], v[84:87]\n" \
"buffer_load_dwordx4 v228, s[52:55], s78 offen offset:0 lds\n" \
"v_mfma_f32_16x16x32_bf16 v[88:91], a[112:115], a[0:3], v[88:91]\n" \
"ds_read_b64_tr_b16 a[70:71], v232 offset:43008\n" \
"v_cvt_pk_bf16_f32 v253, v253, v254\n" \
"v_mfma_f32_16x16x32_bf16 v[88:91], a[116:119], a[4:7], v[88:91]\n" \
"ds_read_b64_tr_b16 a[72:73], v233 offset:32768\n" \
"v_accvgpr_write_b32 a232, v243\n" \
"v_mfma_f32_16x16x32_bf16 v[92:95], a[120:123], a[0:3], v[92:95]\n" \
"ds_read_b64_tr_b16 a[74:75], v233 offset:34816\n" \
"v_accvgpr_write_b32 a233, v253\n" \
"v_mfma_f32_16x16x32_bf16 v[92:95], a[124:127], a[4:7], v[92:95]\n" \
"buffer_load_dwordx4 v229, s[52:55], s78 offen offset:1024 lds\n" \
"v_mfma_f32_16x16x32_bf16 v[124:127], a[120:123], a[8:11], v[124:127]\n" \
"ds_read_b128 a[0:3], v230 offset:32768\n" \
"v_accvgpr_read_b32 v243, a248\n" \
"v_mfma_f32_16x16x32_bf16 v[124:127], a[124:127], a[12:15], v[124:127]\n" \
"ds_read_b128 a[4:7], v231 offset:32768\n" \
"v_accvgpr_read_b32 v252, a249\n" \
"v_mfma_f32_16x16x32_bf16 v[120:123], a[112:115], a[8:11], v[120:123]\n" \
"s_xor_b32 s78, s78, 0x80000\n" \
"ds_read_b64_tr_b16 a[76:77], v233 offset:40960\n" \
"v_accvgpr_read_b32 v253, a250\n" \
"v_mfma_f32_16x16x32_bf16 v[120:123], a[116:119], a[12:15], v[120:123]\n" \
"buffer_load_dwordx4 v228, s[52:55], s89 offen offset:2048 lds\n" \
"v_mfma_f32_16x16x32_bf16 v[116:119], a[104:107], a[8:11], v[116:119]\n" \
"ds_read_b64_tr_b16 a[78:79], v233 offset:43008\n" \
"v_accvgpr_read_b32 v254, a251\n" \
"v_mfma_f32_16x16x32_bf16 v[116:119], a[108:111], a[12:15], v[116:119]\n" \
"ds_read_b64_tr_b16 a[80:81], v232 offset:33280\n" \
"v_mul_f32 v243, s31, v243\n" \
"v_mfma_f32_16x16x32_bf16 v[112:115], a[96:99], a[8:11], v[112:115]\n" \
"ds_read_b64_tr_b16 a[82:83], v232 offset:35328\n" \
"v_mul_f32 v252, s31, v252\n" \
"v_mfma_f32_16x16x32_bf16 v[112:115], a[100:103], a[12:15], v[112:115]\n" \
"buffer_load_dwordx4 v229, s[52:55], s89 offen offset:3072 lds\n" \
"v_mfma_f32_16x16x32_bf16 v[144:147], a[96:99], a[16:19], v[144:147]\n" \
"s_xor_b32 s89, s89, 0x80000\n" \
"ds_read_b128 a[8:11], v230 offset:34816\n" \
"v_mfma_f32_16x16x32_bf16 v[144:147], a[100:103], a[20:23], v[144:147]\n" \
"s_add_u32 m0, s74, 98304\n" \
"ds_read_b128 a[12:15], v231 offset:34816\n" \
"v_mul_f32 v253, s31, v253\n" \
"v_mfma_f32_16x16x32_bf16 v[148:151], a[104:107], a[16:19], v[148:151]\n" \
"s_waitcnt lgkmcnt(6)\n" \
"ds_read_b64_tr_b16 a[84:85], v232 offset:41472\n" \
"v_mul_f32 v254, s31, v254\n" \
"v_mfma_f32_16x16x32_bf16 v[148:151], a[108:111], a[20:23], v[148:151]\n" \
"buffer_load_dwordx4 v224, s[48:51], s75 offen offset:0 lds\n" \
"v_mfma_f32_16x16x32_bf16 v[152:155], a[112:115], a[16:19], v[152:155]\n" \
"ds_read_b64_tr_b16 a[86:87], v232 offset:43520\n" \
"v_cvt_pk_bf16_f32 v243, v243, v252\n" \
"v_mfma_f32_16x16x32_bf16 v[152:155], a[116:119], a[20:23], v[152:155]\n" \
"ds_read_b64_tr_b16 a[88:89], v233 offset:33280\n" \
"v_cvt_pk_bf16_f32 v253, v253, v254\n" \
"v_mfma_f32_16x16x32_bf16 v[156:159], a[120:123], a[16:19], v[156:159]\n" \
"ds_read_b64_tr_b16 a[90:91], v233 offset:35328\n" \
"v_accvgpr_write_b32 a234, v243\n" \
"v_mfma_f32_16x16x32_bf16 v[156:159], a[124:127], a[20:23], v[156:159]\n" \
"buffer_load_dwordx4 v225, s[48:51], s75 offen offset:1024 lds\n" \
"v_mfma_f32_16x16x32_bf16 v[188:191], a[120:123], a[24:27], v[188:191]\n" \
"ds_read_b128 a[16:19], v230 offset:36864\n" \
"v_accvgpr_write_b32 a235, v253\n" \
"v_mfma_f32_16x16x32_bf16 v[188:191], a[124:127], a[28:31], v[188:191]\n" \
"ds_read_b128 a[20:23], v231 offset:36864\n" \
"v_accvgpr_read_b32 v243, a236\n" \
"v_mfma_f32_16x16x32_bf16 v[184:187], a[112:115], a[24:27], v[184:187]\n" \
"s_waitcnt lgkmcnt(6)\n" \
"ds_read_b64_tr_b16 a[92:93], v233 offset:41472\n" \
"v_accvgpr_read_b32 v252, a237\n" \
"v_mfma_f32_16x16x32_bf16 v[184:187], a[116:119], a[28:31], v[184:187]\n" \
"buffer_load_dwordx4 v226, s[48:51], s75 offen offset:2048 lds\n" \
"v_mfma_f32_16x16x32_bf16 v[180:183], a[104:107], a[24:27], v[180:183]\n" \
"ds_read_b64_tr_b16 a[94:95], v233 offset:43520\n" \
"v_accvgpr_read_b32 v253, a238\n" \
"v_mfma_f32_16x16x32_bf16 v[180:183], a[108:111], a[28:31], v[180:183]\n" \
"v_accvgpr_read_b32 v254, a239\n" \
"v_mul_f32 v243, s31, v243\n" \
"v_mfma_f32_16x16x32_bf16 v[176:179], a[96:99], a[24:27], v[176:179]\n" \
"v_mul_f32 v252, s31, v252\n" \
"v_mul_f32 v253, s31, v253\n" \
"v_mfma_f32_16x16x32_bf16 v[176:179], a[100:103], a[28:31], v[176:179]\n" \
"buffer_load_dwordx4 v227, s[48:51], s75 offen offset:3072 lds\n" \
"v_mfma_f32_16x16x32_bf16 v[64:67], a[64:67], a[0:3], v[64:67]\n" \
"s_xor_b32 s75, s75, 0x80\n" \
"ds_read_b128 a[24:27], v230 offset:38912\n" \
"v_mfma_f32_16x16x32_bf16 v[64:67], a[68:71], a[4:7], v[64:67]\n" \
"s_add_u32 m0, s74, 102400\n" \
"ds_read_b128 a[28:31], v231 offset:38912\n" \
"v_mul_f32 v254, s31, v254\n" \
"v_mfma_f32_16x16x32_bf16 v[68:71], a[72:75], a[0:3], v[68:71]\n" \
"s_waitcnt lgkmcnt(4)\n" \
"ds_read_b64_tr_b16 a[96:97], v232 offset:33792\n" \
"v_cvt_pk_bf16_f32 v243, v243, v252\n" \
"v_mfma_f32_16x16x32_bf16 v[68:71], a[76:79], a[4:7], v[68:71]\n" \
"buffer_load_dwordx4 v224, s[48:51], s77 offen offset:0 lds\n" \
"v_mfma_f32_16x16x32_bf16 v[72:75], a[80:83], a[0:3], v[72:75]\n" \
"ds_read_b64_tr_b16 a[98:99], v232 offset:35840\n" \
"v_cvt_pk_bf16_f32 v253, v253, v254\n" \
"v_mfma_f32_16x16x32_bf16 v[72:75], a[84:87], a[4:7], v[72:75]\n" \
"s_waitcnt lgkmcnt(3)\n" \
"ds_read_b64_tr_b16 a[100:101], v232 offset:41984\n" \
"v_accvgpr_write_b32 a236, v243\n" \
"v_mfma_f32_16x16x32_bf16 v[76:79], a[88:91], a[0:3], v[76:79]\n" \
"ds_read_b64_tr_b16 a[102:103], v232 offset:44032\n" \
"v_accvgpr_write_b32 a237, v253\n" \
"v_mfma_f32_16x16x32_bf16 v[76:79], a[92:95], a[4:7], v[76:79]\n" \
"buffer_load_dwordx4 v225, s[48:51], s77 offen offset:1024 lds\n" \
"v_mfma_f32_16x16x32_bf16 v[108:111], a[88:91], a[8:11], v[108:111]\n" \
"ds_read_b64_tr_b16 a[104:105], v233 offset:33792\n" \
"v_accvgpr_read_b32 v243, a252\n" \
"v_mfma_f32_16x16x32_bf16 v[108:111], a[92:95], a[12:15], v[108:111]\n" \
"ds_read_b64_tr_b16 a[106:107], v233 offset:35840\n" \
"v_accvgpr_read_b32 v252, a253\n" \
"v_mfma_f32_16x16x32_bf16 v[104:107], a[80:83], a[8:11], v[104:107]\n" \
"s_waitcnt lgkmcnt(6)\n" \
"ds_read_b64_tr_b16 a[108:109], v233 offset:41984\n" \
"v_accvgpr_read_b32 v253, a254\n" \
"v_mfma_f32_16x16x32_bf16 v[104:107], a[84:87], a[12:15], v[104:107]\n" \
"buffer_load_dwordx4 v226, s[48:51], s77 offen offset:2048 lds\n" \
"v_mfma_f32_16x16x32_bf16 v[100:103], a[72:75], a[8:11], v[100:103]\n" \
"ds_read_b64_tr_b16 a[110:111], v233 offset:44032\n" \
"v_accvgpr_read_b32 v254, a255\n" \
"v_mfma_f32_16x16x32_bf16 v[100:103], a[76:79], a[12:15], v[100:103]\n" \
"ds_read_b64_tr_b16 a[112:113], v232 offset:34304\n" \
"v_mul_f32 v243, s31, v243\n" \
"v_mfma_f32_16x16x32_bf16 v[96:99], a[64:67], a[8:11], v[96:99]\n" \
"ds_read_b64_tr_b16 a[114:115], v232 offset:36352\n" \
"v_mul_f32 v252, s31, v252\n" \
"v_mfma_f32_16x16x32_bf16 v[96:99], a[68:71], a[12:15], v[96:99]\n" \
"buffer_load_dwordx4 v227, s[48:51], s77 offen offset:3072 lds\n" \
"v_mfma_f32_16x16x32_bf16 v[128:131], a[64:67], a[16:19], v[128:131]\n" \
"ds_read_b64_tr_b16 a[116:117], v232 offset:42496\n" \
"v_mul_f32 v253, s31, v253\n" \
"v_mfma_f32_16x16x32_bf16 v[128:131], a[68:71], a[20:23], v[128:131]\n" \
"ds_read_b64_tr_b16 a[118:119], v232 offset:44544\n" \
"v_mul_f32 v254, s31, v254\n" \
"v_mfma_f32_16x16x32_bf16 v[132:135], a[72:75], a[16:19], v[132:135]\n" \
"s_xor_b32 s77, s77, 0x80\n" \
"ds_read_b64_tr_b16 a[120:121], v233 offset:34304\n" \
"v_cvt_pk_bf16_f32 v243, v243, v252\n" \
"v_mfma_f32_16x16x32_bf16 v[132:135], a[76:79], a[20:23], v[132:135]\n" \
"v_cvt_pk_bf16_f32 v253, v253, v254\n" \
"v_accvgpr_write_b32 a238, v243\n" \
"v_mfma_f32_16x16x32_bf16 v[136:139], a[80:83], a[16:19], v[136:139]\n" \
"ds_read_b64_tr_b16 a[122:123], v233 offset:36352\n" \
"v_accvgpr_write_b32 a239, v253\n" \
"v_mfma_f32_16x16x32_bf16 v[136:139], a[84:87], a[20:23], v[136:139]\n" \
"ds_read_b64_tr_b16 a[124:125], v233 offset:42496\n" \
"v_accvgpr_read_b32 v243, a192\n" \
"v_mfma_f32_16x16x32_bf16 v[140:143], a[88:91], a[16:19], v[140:143]\n" \
"ds_read_b64_tr_b16 a[126:127], v233 offset:44544\n" \
"v_accvgpr_read_b32 v252, a193\n" \
"v_mfma_f32_16x16x32_bf16 v[140:143], a[92:95], a[20:23], v[140:143]\n" \
"buffer_store_dwordx4 v[200:203], v236, s[56:59], s93 offen offset:2048 sc1\n" \
"v_mfma_f32_16x16x32_bf16 v[172:175], a[88:91], a[24:27], v[172:175]\n" \
"v_accvgpr_read_b32 v253, a194\n" \
"v_accvgpr_read_b32 v254, a195\n" \
"v_mfma_f32_16x16x32_bf16 v[172:175], a[92:95], a[28:31], v[172:175]\n" \
"v_mul_f32 v243, s31, v243\n" \
"v_mul_f32 v252, s31, v252\n" \
"v_mfma_f32_16x16x32_bf16 v[168:171], a[80:83], a[24:27], v[168:171]\n" \
"v_mul_f32 v253, s31, v253\n" \
"v_mul_f32 v254, s31, v254\n" \
"v_mfma_f32_16x16x32_bf16 v[168:171], a[84:87], a[28:31], v[168:171]\n" \
"v_cvt_pk_bf16_f32 v243, v243, v252\n" \
"v_cvt_pk_bf16_f32 v253, v253, v254\n" \
"v_mfma_f32_16x16x32_bf16 v[164:167], a[72:75], a[24:27], v[164:167]\n" \
"v_accvgpr_write_b32 a192, v243\n" \
"v_accvgpr_write_b32 a193, v253\n" \
"v_mfma_f32_16x16x32_bf16 v[164:167], a[76:79], a[28:31], v[164:167]\n" \
"v_accvgpr_read_b32 v243, a208\n" \
"v_accvgpr_read_b32 v252, a209\n" \
"v_mfma_f32_16x16x32_bf16 v[160:163], a[64:67], a[24:27], v[160:163]\n" \
"v_accvgpr_read_b32 v253, a210\n" \
"v_accvgpr_read_b32 v254, a211\n" \
"v_mfma_f32_16x16x32_bf16 v[160:163], a[68:71], a[28:31], v[160:163]\n" \
"s_waitcnt vmcnt(35) lgkmcnt(0)\n" \
"s_barrier\n" \
"v_mfma_f32_16x16x32_bf16 v[80:83], a[96:99], a[0:3], v[80:83]\n" \
"ds_read_b64_tr_b16 a[64:65], v232 offset:49152\n" \
"v_mul_f32 v243, s31, v243\n" \
"v_mfma_f32_16x16x32_bf16 v[80:83], a[100:103], a[4:7], v[80:83]\n" \
"ds_read_b64_tr_b16 a[66:67], v232 offset:51200\n" \
"v_mul_f32 v252, s31, v252\n" \
"v_mfma_f32_16x16x32_bf16 v[84:87], a[104:107], a[0:3], v[84:87]\n" \
"s_add_u32 m0, s79, 32768\n" \
"ds_read_b64_tr_b16 a[68:69], v232 offset:57344\n" \
"v_mul_f32 v253, s31, v253\n" \
"v_mfma_f32_16x16x32_bf16 v[84:87], a[108:111], a[4:7], v[84:87]\n" \
"buffer_load_dwordx4 v228, s[52:55], s78 offen offset:0 lds\n" \
"v_mfma_f32_16x16x32_bf16 v[88:91], a[112:115], a[0:3], v[88:91]\n" \
"ds_read_b64_tr_b16 a[70:71], v232 offset:59392\n" \
"v_mul_f32 v254, s31, v254\n" \
"v_mfma_f32_16x16x32_bf16 v[88:91], a[116:119], a[4:7], v[88:91]\n" \
"ds_read_b64_tr_b16 a[72:73], v233 offset:49152\n" \
"v_cvt_pk_bf16_f32 v243, v243, v252\n" \
"v_mfma_f32_16x16x32_bf16 v[92:95], a[120:123], a[0:3], v[92:95]\n" \
"ds_read_b64_tr_b16 a[74:75], v233 offset:51200\n" \
"v_cvt_pk_bf16_f32 v253, v253, v254\n" \
"v_mfma_f32_16x16x32_bf16 v[92:95], a[124:127], a[4:7], v[92:95]\n" \
"buffer_load_dwordx4 v229, s[52:55], s78 offen offset:1024 lds\n" \
"v_mfma_f32_16x16x32_bf16 v[124:127], a[120:123], a[8:11], v[124:127]\n" \
"s_waitcnt vmcnt(35)\n" \
"ds_read_b128 a[0:3], v250 offset:0\n" \
"v_accvgpr_write_b32 a194, v243\n" \
"v_mfma_f32_16x16x32_bf16 v[124:127], a[124:127], a[12:15], v[124:127]\n" \
"ds_read_b128 a[4:7], v251 offset:0\n" \
"v_accvgpr_write_b32 a195, v253\n" \
"v_mfma_f32_16x16x32_bf16 v[120:123], a[112:115], a[8:11], v[120:123]\n" \
"s_xor_b32 s78, s78, 0x180000\n" \
"ds_read_b64_tr_b16 a[76:77], v233 offset:57344\n" \
"v_accvgpr_read_b32 v243, a196\n" \
"v_mfma_f32_16x16x32_bf16 v[120:123], a[116:119], a[12:15], v[120:123]\n" \
"buffer_load_dwordx4 v228, s[52:55], s89 offen offset:2048 lds\n" \
"v_mfma_f32_16x16x32_bf16 v[116:119], a[104:107], a[8:11], v[116:119]\n" \
"ds_read_b64_tr_b16 a[78:79], v233 offset:59392\n" \
"v_accvgpr_read_b32 v252, a197\n" \
"v_mfma_f32_16x16x32_bf16 v[116:119], a[108:111], a[12:15], v[116:119]\n" \
"ds_read_b64_tr_b16 a[80:81], v232 offset:49664\n" \
"v_accvgpr_read_b32 v253, a198\n" \
"v_mfma_f32_16x16x32_bf16 v[112:115], a[96:99], a[8:11], v[112:115]\n" \
"ds_read_b64_tr_b16 a[82:83], v232 offset:51712\n" \
"v_accvgpr_read_b32 v254, a199\n" \
"v_mfma_f32_16x16x32_bf16 v[112:115], a[100:103], a[12:15], v[112:115]\n" \
"buffer_load_dwordx4 v229, s[52:55], s89 offen offset:3072 lds\n" \
"v_mfma_f32_16x16x32_bf16 v[144:147], a[96:99], a[16:19], v[144:147]\n" \
"s_waitcnt vmcnt(35)\n" \
"s_xor_b32 s89, s89, 0x180000\n" \
"ds_read_b128 a[8:11], v250 offset:2048\n" \
"v_mfma_f32_16x16x32_bf16 v[144:147], a[100:103], a[20:23], v[144:147]\n" \
"s_add_u32 m0, s74, 131072\n" \
"ds_read_b128 a[12:15], v251 offset:2048\n" \
"v_mul_f32 v243, s31, v243\n" \
"v_mfma_f32_16x16x32_bf16 v[148:151], a[104:107], a[16:19], v[148:151]\n" \
"s_waitcnt lgkmcnt(6)\n" \
"ds_read_b64_tr_b16 a[84:85], v232 offset:57856\n" \
"v_mul_f32 v252, s31, v252\n" \
"v_mfma_f32_16x16x32_bf16 v[148:151], a[108:111], a[20:23], v[148:151]\n" \
"buffer_load_dwordx4 v224, s[48:51], s75 offen offset:0 lds\n" \
"v_mfma_f32_16x16x32_bf16 v[152:155], a[112:115], a[16:19], v[152:155]\n" \
"ds_read_b64_tr_b16 a[86:87], v232 offset:59904\n" \
"v_mul_f32 v253, s31, v253\n" \
"v_mfma_f32_16x16x32_bf16 v[152:155], a[116:119], a[20:23], v[152:155]\n" \
"ds_read_b64_tr_b16 a[88:89], v233 offset:49664\n" \
"v_mul_f32 v254, s31, v254\n" \
"v_mfma_f32_16x16x32_bf16 v[156:159], a[120:123], a[16:19], v[156:159]\n" \
"ds_read_b64_tr_b16 a[90:91], v233 offset:51712\n" \
"v_cvt_pk_bf16_f32 v243, v243, v252\n" \
"v_mfma_f32_16x16x32_bf16 v[156:159], a[124:127], a[20:23], v[156:159]\n" \
"buffer_load_dwordx4 v225, s[48:51], s75 offen offset:1024 lds\n" \
"v_mfma_f32_16x16x32_bf16 v[188:191], a[120:123], a[24:27], v[188:191]\n" \
"s_waitcnt vmcnt(35)\n" \
"ds_read_b128 a[16:19], v250 offset:4096\n" \
"v_cvt_pk_bf16_f32 v253, v253, v254\n" \
"v_mfma_f32_16x16x32_bf16 v[188:191], a[124:127], a[28:31], v[188:191]\n" \
"ds_read_b128 a[20:23], v251 offset:4096\n" \
"v_accvgpr_write_b32 a196, v243\n" \
"v_mfma_f32_16x16x32_bf16 v[184:187], a[112:115], a[24:27], v[184:187]\n" \
"s_waitcnt lgkmcnt(6)\n" \
"ds_read_b64_tr_b16 a[92:93], v233 offset:57856\n" \
"v_accvgpr_write_b32 a197, v253\n" \
"v_mfma_f32_16x16x32_bf16 v[184:187], a[116:119], a[28:31], v[184:187]\n" \
"buffer_load_dwordx4 v226, s[48:51], s75 offen offset:2048 lds\n" \
"v_mfma_f32_16x16x32_bf16 v[180:183], a[104:107], a[24:27], v[180:183]\n" \
"ds_read_b64_tr_b16 a[94:95], v233 offset:59904\n" \
"v_accvgpr_read_b32 v243, a212\n" \
"v_mfma_f32_16x16x32_bf16 v[180:183], a[108:111], a[28:31], v[180:183]\n" \
"v_accvgpr_read_b32 v252, a213\n" \
"v_accvgpr_read_b32 v253, a214\n" \
"v_mfma_f32_16x16x32_bf16 v[176:179], a[96:99], a[24:27], v[176:179]\n" \
"v_accvgpr_read_b32 v254, a215\n" \
"v_mul_f32 v243, s31, v243\n" \
"v_mfma_f32_16x16x32_bf16 v[176:179], a[100:103], a[28:31], v[176:179]\n" \
"buffer_load_dwordx4 v227, s[48:51], s75 offen offset:3072 lds\n" \
"v_mfma_f32_16x16x32_bf16 v[64:67], a[64:67], a[0:3], v[64:67]\n" \
"s_waitcnt vmcnt(34)\n" \
"s_xor_b32 s75, s75, 0x180\n" \
"ds_read_b128 a[24:27], v250 offset:6144\n" \
"v_mfma_f32_16x16x32_bf16 v[64:67], a[68:71], a[4:7], v[64:67]\n" \
"s_add_u32 m0, s74, 135168\n" \
"ds_read_b128 a[28:31], v251 offset:6144\n" \
"v_mul_f32 v252, s31, v252\n" \
"v_mfma_f32_16x16x32_bf16 v[68:71], a[72:75], a[0:3], v[68:71]\n" \
"s_waitcnt lgkmcnt(4)\n" \
"ds_read_b64_tr_b16 a[96:97], v232 offset:50176\n" \
"v_mul_f32 v253, s31, v253\n" \
"v_mfma_f32_16x16x32_bf16 v[68:71], a[76:79], a[4:7], v[68:71]\n" \
"buffer_load_dwordx4 v224, s[48:51], s77 offen offset:0 lds\n" \
"v_mfma_f32_16x16x32_bf16 v[72:75], a[80:83], a[0:3], v[72:75]\n" \
"ds_read_b64_tr_b16 a[98:99], v232 offset:52224\n" \
"v_mul_f32 v254, s31, v254\n" \
"v_mfma_f32_16x16x32_bf16 v[72:75], a[84:87], a[4:7], v[72:75]\n" \
"s_waitcnt lgkmcnt(3)\n" \
"ds_read_b64_tr_b16 a[100:101], v232 offset:58368\n" \
"v_cvt_pk_bf16_f32 v243, v243, v252\n" \
"v_mfma_f32_16x16x32_bf16 v[76:79], a[88:91], a[0:3], v[76:79]\n" \
"ds_read_b64_tr_b16 a[102:103], v232 offset:60416\n" \
"v_cvt_pk_bf16_f32 v253, v253, v254\n" \
"v_mfma_f32_16x16x32_bf16 v[76:79], a[92:95], a[4:7], v[76:79]\n" \
"buffer_load_dwordx4 v225, s[48:51], s77 offen offset:1024 lds\n" \
"v_mfma_f32_16x16x32_bf16 v[108:111], a[88:91], a[8:11], v[108:111]\n" \
"ds_read_b64_tr_b16 a[104:105], v233 offset:50176\n" \
"v_accvgpr_write_b32 a198, v243\n" \
"v_mfma_f32_16x16x32_bf16 v[108:111], a[92:95], a[12:15], v[108:111]\n" \
"ds_read_b64_tr_b16 a[106:107], v233 offset:52224\n" \
"v_accvgpr_write_b32 a199, v253\n" \
"v_mfma_f32_16x16x32_bf16 v[104:107], a[80:83], a[8:11], v[104:107]\n" \
"s_waitcnt lgkmcnt(6)\n" \
"ds_read_b64_tr_b16 a[108:109], v233 offset:58368\n" \
"v_accvgpr_read_b32 v243, a200\n" \
"v_mfma_f32_16x16x32_bf16 v[104:107], a[84:87], a[12:15], v[104:107]\n" \
"buffer_load_dwordx4 v226, s[48:51], s77 offen offset:2048 lds\n" \
"v_mfma_f32_16x16x32_bf16 v[100:103], a[72:75], a[8:11], v[100:103]\n" \
"ds_read_b64_tr_b16 a[110:111], v233 offset:60416\n" \
"v_accvgpr_read_b32 v252, a201\n" \
"v_mfma_f32_16x16x32_bf16 v[100:103], a[76:79], a[12:15], v[100:103]\n" \
"ds_read_b64_tr_b16 a[112:113], v232 offset:50688\n" \
"v_accvgpr_read_b32 v253, a202\n" \
"Lpg2763_%=:\n" \
"s_cbranch_execz Lpg5010_%=\n" \
"v_mfma_f32_16x16x32_bf16 v[96:99], a[64:67], a[8:11], v[96:99]\n" \
"ds_read_b64_tr_b16 a[114:115], v232 offset:52736\n" \
"v_accvgpr_read_b32 v254, a203\n" \
"v_mfma_f32_16x16x32_bf16 v[96:99], a[68:71], a[12:15], v[96:99]\n" \
"buffer_load_dwordx4 v227, s[48:51], s77 offen offset:3072 lds\n" \
"v_mfma_f32_16x16x32_bf16 v[128:131], a[64:67], a[16:19], v[128:131]\n" \
"ds_read_b64_tr_b16 a[116:117], v232 offset:58880\n" \
"v_mul_f32 v243, s31, v243\n" \
"v_mfma_f32_16x16x32_bf16 v[128:131], a[68:71], a[20:23], v[128:131]\n" \
"ds_read_b64_tr_b16 a[118:119], v232 offset:60928\n" \
"v_mul_f32 v252, s31, v252\n" \
"v_mfma_f32_16x16x32_bf16 v[132:135], a[72:75], a[16:19], v[132:135]\n" \
"s_xor_b32 s77, s77, 0x180\n" \
"ds_read_b64_tr_b16 a[120:121], v233 offset:50688\n" \
"v_mul_f32 v253, s31, v253\n" \
"v_mfma_f32_16x16x32_bf16 v[132:135], a[76:79], a[20:23], v[132:135]\n" \
"v_mul_f32 v254, s31, v254\n" \
"v_cvt_pk_bf16_f32 v243, v243, v252\n" \
"v_mfma_f32_16x16x32_bf16 v[136:139], a[80:83], a[16:19], v[136:139]\n" \
"ds_read_b64_tr_b16 a[122:123], v233 offset:52736\n" \
"v_cvt_pk_bf16_f32 v253, v253, v254\n" \
"v_mfma_f32_16x16x32_bf16 v[136:139], a[84:87], a[20:23], v[136:139]\n" \
"ds_read_b64_tr_b16 a[124:125], v233 offset:58880\n" \
"v_accvgpr_write_b32 a200, v243\n" \
"v_mfma_f32_16x16x32_bf16 v[140:143], a[88:91], a[16:19], v[140:143]\n" \
"ds_read_b64_tr_b16 a[126:127], v233 offset:60928\n" \
"v_accvgpr_write_b32 a201, v253\n" \
"v_mfma_f32_16x16x32_bf16 v[140:143], a[92:95], a[20:23], v[140:143]\n" \
"buffer_store_dwordx4 v[216:219], v236, s[56:59], s93 offen offset:2304 sc1\n" \
"v_mfma_f32_16x16x32_bf16 v[172:175], a[88:91], a[24:27], v[172:175]\n" \
"v_accvgpr_read_b32 v243, a216\n" \
"v_accvgpr_read_b32 v252, a217\n" \
"v_mfma_f32_16x16x32_bf16 v[172:175], a[92:95], a[28:31], v[172:175]\n" \
"v_accvgpr_read_b32 v253, a218\n" \
"v_accvgpr_read_b32 v254, a219\n" \
"v_mfma_f32_16x16x32_bf16 v[168:171], a[80:83], a[24:27], v[168:171]\n" \
"v_mul_f32 v243, s31, v243\n" \
"v_mul_f32 v252, s31, v252\n" \
"v_mfma_f32_16x16x32_bf16 v[168:171], a[84:87], a[28:31], v[168:171]\n" \
"v_mul_f32 v253, s31, v253\n" \
"v_mul_f32 v254, s31, v254\n" \
"v_mfma_f32_16x16x32_bf16 v[164:167], a[72:75], a[24:27], v[164:167]\n" \
"v_cvt_pk_bf16_f32 v243, v243, v252\n" \
"v_cvt_pk_bf16_f32 v253, v253, v254\n" \
"v_mfma_f32_16x16x32_bf16 v[164:167], a[76:79], a[28:31], v[164:167]\n" \
"v_accvgpr_write_b32 a202, v243\n" \
"v_accvgpr_write_b32 a203, v253\n" \
"v_mfma_f32_16x16x32_bf16 v[160:163], a[64:67], a[24:27], v[160:163]\n" \
"v_accvgpr_read_b32 v243, a204\n" \
"v_accvgpr_read_b32 v252, a205\n" \
"v_mfma_f32_16x16x32_bf16 v[160:163], a[68:71], a[28:31], v[160:163]\n" \
"s_waitcnt vmcnt(35) lgkmcnt(0)\n" \
"s_barrier\n" \
"v_mfma_f32_16x16x32_bf16 v[80:83], a[96:99], a[0:3], v[80:83]\n" \
"ds_read_b64_tr_b16 a[64:65], v232 offset:0\n" \
"v_accvgpr_read_b32 v253, a206\n" \
"v_mfma_f32_16x16x32_bf16 v[80:83], a[100:103], a[4:7], v[80:83]\n" \
"ds_read_b64_tr_b16 a[66:67], v232 offset:2048\n" \
"v_accvgpr_read_b32 v254, a207\n" \
"v_mfma_f32_16x16x32_bf16 v[84:87], a[104:107], a[0:3], v[84:87]\n" \
"s_add_u32 m0, s79, 49152\n" \
"ds_read_b64_tr_b16 a[68:69], v232 offset:8192\n" \
"v_mul_f32 v243, s31, v243\n" \
"v_mfma_f32_16x16x32_bf16 v[84:87], a[108:111], a[4:7], v[84:87]\n" \
"buffer_load_dwordx4 v228, s[52:55], s78 offen offset:0 lds\n" \
"v_mfma_f32_16x16x32_bf16 v[88:91], a[112:115], a[0:3], v[88:91]\n" \
"ds_read_b64_tr_b16 a[70:71], v232 offset:10240\n" \
"v_mul_f32 v252, s31, v252\n" \
"v_mfma_f32_16x16x32_bf16 v[88:91], a[116:119], a[4:7], v[88:91]\n" \
"ds_read_b64_tr_b16 a[72:73], v233 offset:0\n" \
"v_mul_f32 v253, s31, v253\n" \
"v_mfma_f32_16x16x32_bf16 v[92:95], a[120:123], a[0:3], v[92:95]\n" \
"ds_read_b64_tr_b16 a[74:75], v233 offset:2048\n" \
"v_mul_f32 v254, s31, v254\n" \
"v_mfma_f32_16x16x32_bf16 v[92:95], a[124:127], a[4:7], v[92:95]\n" \
"buffer_load_dwordx4 v229, s[52:55], s78 offen offset:1024 lds\n" \
"v_mfma_f32_16x16x32_bf16 v[124:127], a[120:123], a[8:11], v[124:127]\n" \
"s_waitcnt vmcnt(35)\n" \
"ds_read_b128 a[0:3], v230 offset:0\n" \
"v_cvt_pk_bf16_f32 v243, v243, v252\n" \
"v_mfma_f32_16x16x32_bf16 v[124:127], a[124:127], a[12:15], v[124:127]\n" \
"ds_read_b128 a[4:7], v231 offset:0\n" \
"v_cvt_pk_bf16_f32 v253, v253, v254\n" \
"v_mfma_f32_16x16x32_bf16 v[120:123], a[112:115], a[8:11], v[120:123]\n" \
"s_xor_b32 s78, s78, 0x80000\n" \
"ds_read_b64_tr_b16 a[76:77], v233 offset:8192\n" \
"v_accvgpr_write_b32 a204, v243\n" \
"v_mfma_f32_16x16x32_bf16 v[120:123], a[116:119], a[12:15], v[120:123]\n" \
"buffer_load_dwordx4 v228, s[52:55], s89 offen offset:2048 lds\n" \
"v_mfma_f32_16x16x32_bf16 v[116:119], a[104:107], a[8:11], v[116:119]\n" \
"ds_read_b64_tr_b16 a[78:79], v233 offset:10240\n" \
"v_accvgpr_write_b32 a205, v253\n" \
"v_mfma_f32_16x16x32_bf16 v[116:119], a[108:111], a[12:15], v[116:119]\n" \
"ds_read_b64_tr_b16 a[80:81], v232 offset:512\n" \
"v_accvgpr_read_b32 v243, a220\n" \
"v_mfma_f32_16x16x32_bf16 v[112:115], a[96:99], a[8:11], v[112:115]\n" \
"ds_read_b64_tr_b16 a[82:83], v232 offset:2560\n" \
"v_accvgpr_read_b32 v252, a221\n" \
"v_mfma_f32_16x16x32_bf16 v[112:115], a[100:103], a[12:15], v[112:115]\n" \
"buffer_load_dwordx4 v229, s[52:55], s89 offen offset:3072 lds\n" \
"v_mfma_f32_16x16x32_bf16 v[144:147], a[96:99], a[16:19], v[144:147]\n" \
"s_waitcnt vmcnt(35)\n" \
"s_xor_b32 s89, s89, 0x80000\n" \
"ds_read_b128 a[8:11], v230 offset:2048\n" \
"v_mfma_f32_16x16x32_bf16 v[144:147], a[100:103], a[20:23], v[144:147]\n" \
"s_add_u32 m0, s74, 65536\n" \
"ds_read_b128 a[12:15], v231 offset:2048\n" \
"v_accvgpr_read_b32 v253, a222\n" \
"v_mfma_f32_16x16x32_bf16 v[148:151], a[104:107], a[16:19], v[148:151]\n" \
"s_waitcnt lgkmcnt(6)\n" \
"ds_read_b64_tr_b16 a[84:85], v232 offset:8704\n" \
"v_accvgpr_read_b32 v254, a223\n" \
"v_mfma_f32_16x16x32_bf16 v[148:151], a[108:111], a[20:23], v[148:151]\n" \
"buffer_load_dwordx4 v224, s[48:51], s75 offen offset:0 lds\n" \
"v_mfma_f32_16x16x32_bf16 v[152:155], a[112:115], a[16:19], v[152:155]\n" \
"ds_read_b64_tr_b16 a[86:87], v232 offset:10752\n" \
"v_mul_f32 v243, s31, v243\n" \
"v_mfma_f32_16x16x32_bf16 v[152:155], a[116:119], a[20:23], v[152:155]\n" \
"ds_read_b64_tr_b16 a[88:89], v233 offset:512\n" \
"v_mul_f32 v252, s31, v252\n" \
"v_mfma_f32_16x16x32_bf16 v[156:159], a[120:123], a[16:19], v[156:159]\n" \
"ds_read_b64_tr_b16 a[90:91], v233 offset:2560\n" \
"v_mul_f32 v253, s31, v253\n" \
"v_mfma_f32_16x16x32_bf16 v[156:159], a[124:127], a[20:23], v[156:159]\n" \
"buffer_load_dwordx4 v225, s[48:51], s75 offen offset:1024 lds\n" \
"v_mfma_f32_16x16x32_bf16 v[188:191], a[120:123], a[24:27], v[188:191]\n" \
"s_waitcnt vmcnt(35)\n" \
"ds_read_b128 a[16:19], v230 offset:4096\n" \
"v_mul_f32 v254, s31, v254\n" \
"v_mfma_f32_16x16x32_bf16 v[188:191], a[124:127], a[28:31], v[188:191]\n" \
"ds_read_b128 a[20:23], v231 offset:4096\n" \
"v_cvt_pk_bf16_f32 v243, v243, v252\n" \
"v_mfma_f32_16x16x32_bf16 v[184:187], a[112:115], a[24:27], v[184:187]\n" \
"s_waitcnt lgkmcnt(6)\n" \
"ds_read_b64_tr_b16 a[92:93], v233 offset:8704\n" \
"v_cvt_pk_bf16_f32 v253, v253, v254\n" \
"v_mfma_f32_16x16x32_bf16 v[184:187], a[116:119], a[28:31], v[184:187]\n" \
"buffer_load_dwordx4 v226, s[48:51], s75 offen offset:2048 lds\n" \
"v_mfma_f32_16x16x32_bf16 v[180:183], a[104:107], a[24:27], v[180:183]\n" \
"ds_read_b64_tr_b16 a[94:95], v233 offset:10752\n" \
"v_accvgpr_write_b32 a206, v243\n" \
"v_mfma_f32_16x16x32_bf16 v[180:183], a[108:111], a[28:31], v[180:183]\n" \
"v_accvgpr_write_b32 a207, v253\n" \
"v_mul_f32 v32, s76, v32\n" \
"v_mfma_f32_16x16x32_bf16 v[176:179], a[96:99], a[24:27], v[176:179]\n" \
"v_mul_f32 v33, s76, v33\n" \
"v_mul_f32 v34, s76, v34\n" \
"v_mfma_f32_16x16x32_bf16 v[176:179], a[100:103], a[28:31], v[176:179]\n" \
"buffer_load_dwordx4 v227, s[48:51], s75 offen offset:3072 lds\n" \
"v_mfma_f32_16x16x32_bf16 v[64:67], a[64:67], a[0:3], v[64:67]\n" \
"s_waitcnt vmcnt(35)\n" \
"s_xor_b32 s75, s75, 0x80\n" \
"ds_read_b128 a[24:27], v230 offset:6144\n" \
"v_mfma_f32_16x16x32_bf16 v[64:67], a[68:71], a[4:7], v[64:67]\n" \
"s_add_u32 m0, s74, 69632\n" \
"ds_read_b128 a[28:31], v231 offset:6144\n" \
"v_mul_f32 v35, s76, v35\n" \
"v_mfma_f32_16x16x32_bf16 v[68:71], a[72:75], a[0:3], v[68:71]\n" \
"s_waitcnt lgkmcnt(4)\n" \
"ds_read_b64_tr_b16 a[96:97], v232 offset:1024\n" \
"v_cvt_pk_bf16_f32 v32, v32, v33\n" \
"v_mfma_f32_16x16x32_bf16 v[68:71], a[76:79], a[4:7], v[68:71]\n" \
"buffer_load_dwordx4 v224, s[48:51], s77 offen offset:0 lds\n" \
"v_mfma_f32_16x16x32_bf16 v[72:75], a[80:83], a[0:3], v[72:75]\n" \
"ds_read_b64_tr_b16 a[98:99], v232 offset:3072\n" \
"v_cvt_pk_bf16_f32 v33, v34, v35\n" \
"v_mfma_f32_16x16x32_bf16 v[72:75], a[84:87], a[4:7], v[72:75]\n" \
"s_waitcnt lgkmcnt(3)\n" \
"ds_read_b64_tr_b16 a[100:101], v232 offset:9216\n" \
"v_mul_f32 v36, s76, v36\n" \
"v_mfma_f32_16x16x32_bf16 v[76:79], a[88:91], a[0:3], v[76:79]\n" \
"ds_read_b64_tr_b16 a[102:103], v232 offset:11264\n" \
"v_mul_f32 v37, s76, v37\n" \
"v_mfma_f32_16x16x32_bf16 v[76:79], a[92:95], a[4:7], v[76:79]\n" \
"buffer_load_dwordx4 v225, s[48:51], s77 offen offset:1024 lds\n" \
"v_mfma_f32_16x16x32_bf16 v[108:111], a[88:91], a[8:11], v[108:111]\n" \
"ds_read_b64_tr_b16 a[104:105], v233 offset:1024\n" \
"v_mul_f32 v38, s76, v38\n" \
"v_mfma_f32_16x16x32_bf16 v[108:111], a[92:95], a[12:15], v[108:111]\n" \
"ds_read_b64_tr_b16 a[106:107], v233 offset:3072\n" \
"v_mul_f32 v39, s76, v39\n" \
"v_mfma_f32_16x16x32_bf16 v[104:107], a[80:83], a[8:11], v[104:107]\n" \
"s_waitcnt lgkmcnt(6)\n" \
"ds_read_b64_tr_b16 a[108:109], v233 offset:9216\n" \
"v_cvt_pk_bf16_f32 v34, v36, v37\n" \
"v_mfma_f32_16x16x32_bf16 v[104:107], a[84:87], a[12:15], v[104:107]\n" \
"buffer_load_dwordx4 v226, s[48:51], s77 offen offset:2048 lds\n" \
"v_mfma_f32_16x16x32_bf16 v[100:103], a[72:75], a[8:11], v[100:103]\n" \
"ds_read_b64_tr_b16 a[110:111], v233 offset:11264\n" \
"v_cvt_pk_bf16_f32 v35, v38, v39\n" \
"v_mfma_f32_16x16x32_bf16 v[100:103], a[76:79], a[12:15], v[100:103]\n" \
"ds_read_b64_tr_b16 a[112:113], v232 offset:1536\n" \
"v_mul_f32 v48, s76, v48\n" \
"v_mfma_f32_16x16x32_bf16 v[96:99], a[64:67], a[8:11], v[96:99]\n" \
"ds_read_b64_tr_b16 a[114:115], v232 offset:3584\n" \
"v_mul_f32 v49, s76, v49\n" \
"v_mfma_f32_16x16x32_bf16 v[96:99], a[68:71], a[12:15], v[96:99]\n" \
"buffer_load_dwordx4 v227, s[48:51], s77 offen offset:3072 lds\n" \
"v_mfma_f32_16x16x32_bf16 v[128:131], a[64:67], a[16:19], v[128:131]\n" \
"ds_read_b64_tr_b16 a[116:117], v232 offset:9728\n" \
"v_mul_f32 v50, s76, v50\n" \
"v_mfma_f32_16x16x32_bf16 v[128:131], a[68:71], a[20:23], v[128:131]\n" \
"ds_read_b64_tr_b16 a[118:119], v232 offset:11776\n" \
"v_mul_f32 v51, s76, v51\n" \
"v_mfma_f32_16x16x32_bf16 v[132:135], a[72:75], a[16:19], v[132:135]\n" \
"s_xor_b32 s77, s77, 0x80\n" \
"ds_read_b64_tr_b16 a[120:121], v233 offset:1536\n" \
"v_cvt_pk_bf16_f32 v48, v48, v49\n" \
"v_mfma_f32_16x16x32_bf16 v[132:135], a[76:79], a[20:23], v[132:135]\n" \
"v_cvt_pk_bf16_f32 v49, v50, v51\n" \
"v_mul_f32 v52, s76, v52\n" \
"v_mfma_f32_16x16x32_bf16 v[136:139], a[80:83], a[16:19], v[136:139]\n" \
"ds_read_b64_tr_b16 a[122:123], v233 offset:3584\n" \
"v_mul_f32 v53, s76, v53\n" \
"v_mfma_f32_16x16x32_bf16 v[136:139], a[84:87], a[20:23], v[136:139]\n" \
"ds_read_b64_tr_b16 a[124:125], v233 offset:9728\n" \
"v_mul_f32 v54, s76, v54\n" \
"v_mfma_f32_16x16x32_bf16 v[140:143], a[88:91], a[16:19], v[140:143]\n" \
"s_add_u32 s93, s87, 0\n" \
"ds_read_b64_tr_b16 a[126:127], v233 offset:11776\n" \
"v_mul_f32 v55, s76, v55\n" \
"v_mfma_f32_16x16x32_bf16 v[140:143], a[92:95], a[20:23], v[140:143]\n" \
"buffer_store_dwordx4 v[196:199], v236, s[56:59], s93 offen offset:0 sc1\n" \
"v_mfma_f32_16x16x32_bf16 v[172:175], a[88:91], a[24:27], v[172:175]\n" \
"v_cvt_pk_bf16_f32 v50, v52, v53\n" \
"v_cvt_pk_bf16_f32 v51, v54, v55\n" \
"v_mfma_f32_16x16x32_bf16 v[172:175], a[92:95], a[28:31], v[172:175]\n" \
"v_mul_f32 v40, s76, v40\n" \
"v_mul_f32 v41, s76, v41\n" \
"v_mfma_f32_16x16x32_bf16 v[168:171], a[80:83], a[24:27], v[168:171]\n" \
"v_mul_f32 v42, s76, v42\n" \
"v_mul_f32 v43, s76, v43\n" \
"v_mfma_f32_16x16x32_bf16 v[168:171], a[84:87], a[28:31], v[168:171]\n" \
"v_cvt_pk_bf16_f32 v40, v40, v41\n" \
"v_cvt_pk_bf16_f32 v41, v42, v43\n" \
"v_mfma_f32_16x16x32_bf16 v[164:167], a[72:75], a[24:27], v[164:167]\n" \
"v_mul_f32 v44, s76, v44\n" \
"v_mul_f32 v45, s76, v45\n" \
"v_mfma_f32_16x16x32_bf16 v[164:167], a[76:79], a[28:31], v[164:167]\n" \
"v_mul_f32 v46, s76, v46\n" \
"v_mul_f32 v47, s76, v47\n" \
"v_mfma_f32_16x16x32_bf16 v[160:163], a[64:67], a[24:27], v[160:163]\n" \
"v_cvt_pk_bf16_f32 v42, v44, v45\n" \
"v_cvt_pk_bf16_f32 v43, v46, v47\n" \
"v_mfma_f32_16x16x32_bf16 v[160:163], a[68:71], a[28:31], v[160:163]\n" \
"s_waitcnt vmcnt(35) lgkmcnt(0)\n" \
"s_barrier\n" \
"v_mfma_f32_16x16x32_bf16 v[80:83], a[96:99], a[0:3], v[80:83]\n" \
"ds_read_b64_tr_b16 a[64:65], v232 offset:16384\n" \
"v_mul_f32 v56, s76, v56\n" \
"v_mfma_f32_16x16x32_bf16 v[80:83], a[100:103], a[4:7], v[80:83]\n" \
"ds_read_b64_tr_b16 a[66:67], v232 offset:18432\n" \
"v_mul_f32 v57, s76, v57\n" \
"v_mfma_f32_16x16x32_bf16 v[84:87], a[104:107], a[0:3], v[84:87]\n" \
"s_add_u32 m0, s79, 0\n" \
"ds_read_b64_tr_b16 a[68:69], v232 offset:24576\n" \
"v_mul_f32 v58, s76, v58\n" \
"v_mfma_f32_16x16x32_bf16 v[84:87], a[108:111], a[4:7], v[84:87]\n" \
"buffer_load_dwordx4 v228, s[52:55], s78 offen offset:0 lds\n" \
"v_mfma_f32_16x16x32_bf16 v[88:91], a[112:115], a[0:3], v[88:91]\n" \
"ds_read_b64_tr_b16 a[70:71], v232 offset:26624\n" \
"v_mul_f32 v59, s76, v59\n" \
"v_mfma_f32_16x16x32_bf16 v[88:91], a[116:119], a[4:7], v[88:91]\n" \
"ds_read_b64_tr_b16 a[72:73], v233 offset:16384\n" \
"v_cvt_pk_bf16_f32 v56, v56, v57\n" \
"v_mfma_f32_16x16x32_bf16 v[92:95], a[120:123], a[0:3], v[92:95]\n" \
"ds_read_b64_tr_b16 a[74:75], v233 offset:18432\n" \
"v_cvt_pk_bf16_f32 v57, v58, v59\n" \
"v_mfma_f32_16x16x32_bf16 v[92:95], a[124:127], a[4:7], v[92:95]\n" \
"buffer_load_dwordx4 v229, s[52:55], s78 offen offset:1024 lds\n" \
"v_mfma_f32_16x16x32_bf16 v[124:127], a[120:123], a[8:11], v[124:127]\n" \
"s_waitcnt vmcnt(35)\n" \
"ds_read_b128 a[0:3], v230 offset:32768\n" \
"v_mul_f32 v60, s76, v60\n" \
"v_mfma_f32_16x16x32_bf16 v[124:127], a[124:127], a[12:15], v[124:127]\n" \
"ds_read_b128 a[4:7], v231 offset:32768\n" \
"v_mul_f32 v61, s76, v61\n" \
"v_mfma_f32_16x16x32_bf16 v[120:123], a[112:115], a[8:11], v[120:123]\n" \
"s_xor_b32 s78, s78, 0x780000\n" \
"ds_read_b64_tr_b16 a[76:77], v233 offset:24576\n" \
"v_mul_f32 v62, s76, v62\n" \
"v_mfma_f32_16x16x32_bf16 v[120:123], a[116:119], a[12:15], v[120:123]\n" \
"buffer_load_dwordx4 v228, s[52:55], s89 offen offset:2048 lds\n" \
"v_mfma_f32_16x16x32_bf16 v[116:119], a[104:107], a[8:11], v[116:119]\n" \
"ds_read_b64_tr_b16 a[78:79], v233 offset:26624\n" \
"v_mul_f32 v63, s76, v63\n" \
"v_mfma_f32_16x16x32_bf16 v[116:119], a[108:111], a[12:15], v[116:119]\n" \
"ds_read_b64_tr_b16 a[80:81], v232 offset:16896\n" \
"v_cvt_pk_bf16_f32 v58, v60, v61\n" \
"v_mfma_f32_16x16x32_bf16 v[112:115], a[96:99], a[8:11], v[112:115]\n" \
"ds_read_b64_tr_b16 a[82:83], v232 offset:18944\n" \
"v_cvt_pk_bf16_f32 v59, v62, v63\n" \
"v_mfma_f32_16x16x32_bf16 v[112:115], a[100:103], a[12:15], v[112:115]\n" \
"buffer_load_dwordx4 v229, s[52:55], s89 offen offset:3072 lds\n" \
"v_mfma_f32_16x16x32_bf16 v[144:147], a[96:99], a[16:19], v[144:147]\n" \
"s_waitcnt vmcnt(35)\n" \
"s_xor_b32 s89, s89, 0x780000\n" \
"ds_read_b128 a[8:11], v230 offset:34816\n" \
"v_mfma_f32_16x16x32_bf16 v[144:147], a[100:103], a[20:23], v[144:147]\n" \
"s_add_u32 m0, s74, 98304\n" \
"ds_read_b128 a[12:15], v231 offset:34816\n" \
"v_mul_f32 v0, s76, v0\n" \
"v_mfma_f32_16x16x32_bf16 v[148:151], a[104:107], a[16:19], v[148:151]\n" \
"s_waitcnt lgkmcnt(6)\n" \
"ds_read_b64_tr_b16 a[84:85], v232 offset:25088\n" \
"v_mul_f32 v1, s76, v1\n" \
"v_mfma_f32_16x16x32_bf16 v[148:151], a[108:111], a[20:23], v[148:151]\n" \
"buffer_load_dwordx4 v224, s[48:51], s75 offen offset:0 lds\n" \
"v_mfma_f32_16x16x32_bf16 v[152:155], a[112:115], a[16:19], v[152:155]\n" \
"ds_read_b64_tr_b16 a[86:87], v232 offset:27136\n" \
"v_mul_f32 v2, s76, v2\n" \
"v_mfma_f32_16x16x32_bf16 v[152:155], a[116:119], a[20:23], v[152:155]\n" \
"ds_read_b64_tr_b16 a[88:89], v233 offset:16896\n" \
"v_mul_f32 v3, s76, v3\n" \
"v_mfma_f32_16x16x32_bf16 v[156:159], a[120:123], a[16:19], v[156:159]\n" \
"ds_read_b64_tr_b16 a[90:91], v233 offset:18944\n" \
"v_cvt_pk_bf16_f32 v36, v0, v1\n" \
"v_mfma_f32_16x16x32_bf16 v[156:159], a[124:127], a[20:23], v[156:159]\n" \
"buffer_load_dwordx4 v225, s[48:51], s75 offen offset:1024 lds\n" \
"v_mfma_f32_16x16x32_bf16 v[188:191], a[120:123], a[24:27], v[188:191]\n" \
"s_waitcnt vmcnt(35)\n" \
"ds_read_b128 a[16:19], v230 offset:36864\n" \
"v_cvt_pk_bf16_f32 v37, v2, v3\n" \
"v_mfma_f32_16x16x32_bf16 v[188:191], a[124:127], a[28:31], v[188:191]\n" \
"ds_read_b128 a[20:23], v231 offset:36864\n" \
"v_mul_f32 v4, s76, v4\n" \
"v_mfma_f32_16x16x32_bf16 v[184:187], a[112:115], a[24:27], v[184:187]\n" \
"s_waitcnt lgkmcnt(6)\n" \
"ds_read_b64_tr_b16 a[92:93], v233 offset:25088\n" \
"v_mul_f32 v5, s76, v5\n" \
"v_mfma_f32_16x16x32_bf16 v[184:187], a[116:119], a[28:31], v[184:187]\n" \
"buffer_load_dwordx4 v226, s[48:51], s75 offen offset:2048 lds\n" \
"v_mfma_f32_16x16x32_bf16 v[180:183], a[104:107], a[24:27], v[180:183]\n" \
"ds_read_b64_tr_b16 a[94:95], v233 offset:27136\n" \
"v_mul_f32 v6, s76, v6\n" \
"v_mfma_f32_16x16x32_bf16 v[180:183], a[108:111], a[28:31], v[180:183]\n" \
"v_mul_f32 v7, s76, v7\n" \
"v_cvt_pk_bf16_f32 v38, v4, v5\n" \
"v_mfma_f32_16x16x32_bf16 v[176:179], a[96:99], a[24:27], v[176:179]\n" \
"v_cvt_pk_bf16_f32 v39, v6, v7\n" \
"v_mul_f32 v16, s76, v16\n" \
"v_mfma_f32_16x16x32_bf16 v[176:179], a[100:103], a[28:31], v[176:179]\n" \
"buffer_load_dwordx4 v227, s[48:51], s75 offen offset:3072 lds\n" \
"v_mfma_f32_16x16x32_bf16 v[64:67], a[64:67], a[0:3], v[64:67]\n" \
"s_waitcnt vmcnt(35)\n" \
"s_xor_b32 s75, s75, 0x780\n" \
"ds_read_b128 a[24:27], v230 offset:38912\n" \
"v_mfma_f32_16x16x32_bf16 v[64:67], a[68:71], a[4:7], v[64:67]\n" \
"s_add_u32 m0, s74, 102400\n" \
"ds_read_b128 a[28:31], v231 offset:38912\n" \
"v_mul_f32 v17, s76, v17\n" \
"v_mfma_f32_16x16x32_bf16 v[68:71], a[72:75], a[0:3], v[68:71]\n" \
"s_waitcnt lgkmcnt(4)\n" \
"ds_read_b64_tr_b16 a[96:97], v232 offset:17408\n" \
"v_mul_f32 v18, s76, v18\n" \
"v_mfma_f32_16x16x32_bf16 v[68:71], a[76:79], a[4:7], v[68:71]\n" \
"buffer_load_dwordx4 v224, s[48:51], s77 offen offset:0 lds\n" \
"v_mfma_f32_16x16x32_bf16 v[72:75], a[80:83], a[0:3], v[72:75]\n" \
"ds_read_b64_tr_b16 a[98:99], v232 offset:19456\n" \
"v_mul_f32 v19, s76, v19\n" \
"v_mfma_f32_16x16x32_bf16 v[72:75], a[84:87], a[4:7], v[72:75]\n" \
"s_waitcnt lgkmcnt(3)\n" \
"ds_read_b64_tr_b16 a[100:101], v232 offset:25600\n" \
"v_cvt_pk_bf16_f32 v16, v16, v17\n" \
"v_mfma_f32_16x16x32_bf16 v[76:79], a[88:91], a[0:3], v[76:79]\n" \
"ds_read_b64_tr_b16 a[102:103], v232 offset:27648\n" \
"v_cvt_pk_bf16_f32 v17, v18, v19\n" \
"v_mfma_f32_16x16x32_bf16 v[76:79], a[92:95], a[4:7], v[76:79]\n" \
"buffer_load_dwordx4 v225, s[48:51], s77 offen offset:1024 lds\n" \
"v_mfma_f32_16x16x32_bf16 v[108:111], a[88:91], a[8:11], v[108:111]\n" \
"ds_read_b64_tr_b16 a[104:105], v233 offset:17408\n" \
"v_mul_f32 v20, s76, v20\n" \
"v_mfma_f32_16x16x32_bf16 v[108:111], a[92:95], a[12:15], v[108:111]\n" \
"ds_read_b64_tr_b16 a[106:107], v233 offset:19456\n" \
"v_mul_f32 v21, s76, v21\n" \
"v_mfma_f32_16x16x32_bf16 v[104:107], a[80:83], a[8:11], v[104:107]\n" \
"s_waitcnt lgkmcnt(6)\n" \
"ds_read_b64_tr_b16 a[108:109], v233 offset:25600\n" \
"v_mul_f32 v22, s76, v22\n" \
"v_mfma_f32_16x16x32_bf16 v[104:107], a[84:87], a[12:15], v[104:107]\n" \
"buffer_load_dwordx4 v226, s[48:51], s77 offen offset:2048 lds\n" \
"v_mfma_f32_16x16x32_bf16 v[100:103], a[72:75], a[8:11], v[100:103]\n" \
"ds_read_b64_tr_b16 a[110:111], v233 offset:27648\n" \
"v_mul_f32 v23, s76, v23\n" \
"v_mfma_f32_16x16x32_bf16 v[100:103], a[76:79], a[12:15], v[100:103]\n" \
"ds_read_b64_tr_b16 a[112:113], v232 offset:17920\n" \
"v_cvt_pk_bf16_f32 v18, v20, v21\n" \
"v_mfma_f32_16x16x32_bf16 v[96:99], a[64:67], a[8:11], v[96:99]\n" \
"ds_read_b64_tr_b16 a[114:115], v232 offset:19968\n" \
"v_cvt_pk_bf16_f32 v19, v22, v23\n" \
"v_mfma_f32_16x16x32_bf16 v[96:99], a[68:71], a[12:15], v[96:99]\n" \
"buffer_load_dwordx4 v227, s[48:51], s77 offen offset:3072 lds\n" \
"v_mfma_f32_16x16x32_bf16 v[128:131], a[64:67], a[16:19], v[128:131]\n" \
"ds_read_b64_tr_b16 a[116:117], v232 offset:26112\n" \
"v_mul_f32 v8, s76, v8\n" \
"v_mfma_f32_16x16x32_bf16 v[128:131], a[68:71], a[20:23], v[128:131]\n" \
"ds_read_b64_tr_b16 a[118:119], v232 offset:28160\n" \
"v_mul_f32 v9, s76, v9\n" \
"v_mfma_f32_16x16x32_bf16 v[132:135], a[72:75], a[16:19], v[132:135]\n" \
"s_xor_b32 s77, s77, 0x780\n" \
"ds_read_b64_tr_b16 a[120:121], v233 offset:17920\n" \
"v_mul_f32 v10, s76, v10\n" \
"v_mfma_f32_16x16x32_bf16 v[132:135], a[76:79], a[20:23], v[132:135]\n" \
"v_mul_f32 v11, s76, v11\n" \
"v_cvt_pk_bf16_f32 v52, v8, v9\n" \
"v_mfma_f32_16x16x32_bf16 v[136:139], a[80:83], a[16:19], v[136:139]\n" \
"ds_read_b64_tr_b16 a[122:123], v233 offset:19968\n" \
"v_cvt_pk_bf16_f32 v53, v10, v11\n" \
"v_mfma_f32_16x16x32_bf16 v[136:139], a[84:87], a[20:23], v[136:139]\n" \
"ds_read_b64_tr_b16 a[124:125], v233 offset:26112\n" \
"v_mul_f32 v12, s76, v12\n" \
"v_mfma_f32_16x16x32_bf16 v[140:143], a[88:91], a[16:19], v[140:143]\n" \
"ds_read_b64_tr_b16 a[126:127], v233 offset:28160\n" \
"v_mul_f32 v13, s76, v13\n" \
"v_mfma_f32_16x16x32_bf16 v[140:143], a[92:95], a[20:23], v[140:143]\n" \
"buffer_store_dwordx4 v[212:215], v236, s[56:59], s93 offen offset:256 sc1\n" \
"v_mfma_f32_16x16x32_bf16 v[172:175], a[88:91], a[24:27], v[172:175]\n" \
"v_mul_f32 v14, s76, v14\n" \
"v_mul_f32 v15, s76, v15\n" \
"v_mfma_f32_16x16x32_bf16 v[172:175], a[92:95], a[28:31], v[172:175]\n" \
"v_cvt_pk_bf16_f32 v54, v12, v13\n" \
"v_cvt_pk_bf16_f32 v55, v14, v15\n" \
"v_mfma_f32_16x16x32_bf16 v[168:171], a[80:83], a[24:27], v[168:171]\n" \
"v_mul_f32 v24, s76, v24\n" \
"v_mul_f32 v25, s76, v25\n" \
"v_mfma_f32_16x16x32_bf16 v[168:171], a[84:87], a[28:31], v[168:171]\n" \
"v_mul_f32 v26, s76, v26\n" \
"v_mul_f32 v27, s76, v27\n" \
"v_mfma_f32_16x16x32_bf16 v[164:167], a[72:75], a[24:27], v[164:167]\n" \
"v_cvt_pk_bf16_f32 v24, v24, v25\n" \
"v_cvt_pk_bf16_f32 v25, v26, v27\n" \
"v_mfma_f32_16x16x32_bf16 v[164:167], a[76:79], a[28:31], v[164:167]\n" \
"v_mul_f32 v28, s76, v28\n" \
"v_mul_f32 v29, s76, v29\n" \
"v_mfma_f32_16x16x32_bf16 v[160:163], a[64:67], a[24:27], v[160:163]\n" \
"v_mul_f32 v30, s76, v30\n" \
"v_mul_f32 v31, s76, v31\n" \
"v_mfma_f32_16x16x32_bf16 v[160:163], a[68:71], a[28:31], v[160:163]\n" \
"s_waitcnt vmcnt(35) lgkmcnt(0)\n" \
"s_barrier\n" \
"v_mfma_f32_16x16x32_bf16 v[80:83], a[96:99], a[0:3], v[80:83]\n" \
"ds_read_b64_tr_b16 a[64:65], v232 offset:32768\n" \
"v_cvt_pk_bf16_f32 v26, v28, v29\n" \
"v_mfma_f32_16x16x32_bf16 v[80:83], a[100:103], a[4:7], v[80:83]\n" \
"ds_read_b64_tr_b16 a[66:67], v232 offset:34816\n" \
"v_cvt_pk_bf16_f32 v27, v30, v31\n" \
"v_mfma_f32_16x16x32_bf16 v[84:87], a[104:107], a[0:3], v[84:87]\n" \
"s_add_u32 m0, s79, 16384\n" \
"ds_read_b64_tr_b16 a[68:69], v232 offset:40960\n" \
"v_accvgpr_read_b32 v243, a160\n" \
"v_mfma_f32_16x16x32_bf16 v[84:87], a[108:111], a[4:7], v[84:87]\n" \
"buffer_load_dwordx4 v228, s[52:55], s78 offen offset:0 lds\n" \
"v_mfma_f32_16x16x32_bf16 v[88:91], a[112:115], a[0:3], v[88:91]\n" \
"ds_read_b64_tr_b16 a[70:71], v232 offset:43008\n" \
"v_accvgpr_read_b32 v252, a161\n" \
"v_mfma_f32_16x16x32_bf16 v[88:91], a[116:119], a[4:7], v[88:91]\n" \
"ds_read_b64_tr_b16 a[72:73], v233 offset:32768\n" \
"v_accvgpr_read_b32 v253, a162\n" \
"v_mfma_f32_16x16x32_bf16 v[92:95], a[120:123], a[0:3], v[92:95]\n" \
"ds_read_b64_tr_b16 a[74:75], v233 offset:34816\n" \
"v_accvgpr_read_b32 v254, a163\n" \
"v_mfma_f32_16x16x32_bf16 v[92:95], a[124:127], a[4:7], v[92:95]\n" \
"buffer_load_dwordx4 v229, s[52:55], s78 offen offset:1024 lds\n" \
"v_mfma_f32_16x16x32_bf16 v[124:127], a[120:123], a[8:11], v[124:127]\n" \
"s_waitcnt vmcnt(35)\n" \
"ds_read_b128 a[0:3], v250 offset:0\n" \
"v_mul_f32 v243, s31, v243\n" \
"v_mfma_f32_16x16x32_bf16 v[124:127], a[124:127], a[12:15], v[124:127]\n" \
"ds_read_b128 a[4:7], v251 offset:0\n" \
"v_mul_f32 v252, s31, v252\n" \
"v_mfma_f32_16x16x32_bf16 v[120:123], a[112:115], a[8:11], v[120:123]\n" \
"s_xor_b32 s78, s78, 0x80000\n" \
"ds_read_b64_tr_b16 a[76:77], v233 offset:40960\n" \
"v_mul_f32 v253, s31, v253\n" \
"v_mfma_f32_16x16x32_bf16 v[120:123], a[116:119], a[12:15], v[120:123]\n" \
"buffer_load_dwordx4 v228, s[52:55], s89 offen offset:2048 lds\n" \
"v_mfma_f32_16x16x32_bf16 v[116:119], a[104:107], a[8:11], v[116:119]\n" \
"ds_read_b64_tr_b16 a[78:79], v233 offset:43008\n" \
"v_mul_f32 v254, s31, v254\n" \
"v_mfma_f32_16x16x32_bf16 v[116:119], a[108:111], a[12:15], v[116:119]\n" \
"ds_read_b64_tr_b16 a[80:81], v232 offset:33280\n" \
"v_cvt_pk_bf16_f32 v44, v243, v252\n" \
"v_mfma_f32_16x16x32_bf16 v[112:115], a[96:99], a[8:11], v[112:115]\n" \
"ds_read_b64_tr_b16 a[82:83], v232 offset:35328\n" \
"v_cvt_pk_bf16_f32 v45, v253, v254\n" \
"v_mfma_f32_16x16x32_bf16 v[112:115], a[100:103], a[12:15], v[112:115]\n" \
"buffer_load_dwordx4 v229, s[52:55], s89 offen offset:3072 lds\n" \
"v_mfma_f32_16x16x32_bf16 v[144:147], a[96:99], a[16:19], v[144:147]\n" \
"s_waitcnt vmcnt(35)\n" \
"s_xor_b32 s89, s89, 0x80000\n" \
"ds_read_b128 a[8:11], v250 offset:2048\n" \
"v_mfma_f32_16x16x32_bf16 v[144:147], a[100:103], a[20:23], v[144:147]\n" \
"s_add_u32 m0, s74, 131072\n" \
"ds_read_b128 a[12:15], v251 offset:2048\n" \
"v_accvgpr_read_b32 v243, a176\n" \
"v_mfma_f32_16x16x32_bf16 v[148:151], a[104:107], a[16:19], v[148:151]\n" \
"s_waitcnt lgkmcnt(6)\n" \
"ds_read_b64_tr_b16 a[84:85], v232 offset:41472\n" \
"v_accvgpr_read_b32 v252, a177\n" \
"v_mfma_f32_16x16x32_bf16 v[148:151], a[108:111], a[20:23], v[148:151]\n" \
"buffer_load_dwordx4 v224, s[48:51], s75 offen offset:0 lds\n" \
"v_mfma_f32_16x16x32_bf16 v[152:155], a[112:115], a[16:19], v[152:155]\n" \
"ds_read_b64_tr_b16 a[86:87], v232 offset:43520\n" \
"v_accvgpr_read_b32 v253, a178\n" \
"v_mfma_f32_16x16x32_bf16 v[152:155], a[116:119], a[20:23], v[152:155]\n" \
"ds_read_b64_tr_b16 a[88:89], v233 offset:33280\n" \
"v_accvgpr_read_b32 v254, a179\n" \
"v_mfma_f32_16x16x32_bf16 v[156:159], a[120:123], a[16:19], v[156:159]\n" \
"ds_read_b64_tr_b16 a[90:91], v233 offset:35328\n" \
"v_mul_f32 v243, s31, v243\n" \
"v_mfma_f32_16x16x32_bf16 v[156:159], a[124:127], a[20:23], v[156:159]\n" \
"buffer_load_dwordx4 v225, s[48:51], s75 offen offset:1024 lds\n" \
"v_mfma_f32_16x16x32_bf16 v[188:191], a[120:123], a[24:27], v[188:191]\n" \
"s_waitcnt vmcnt(35)\n" \
"ds_read_b128 a[16:19], v250 offset:4096\n" \
"v_mul_f32 v252, s31, v252\n" \
"v_mfma_f32_16x16x32_bf16 v[188:191], a[124:127], a[28:31], v[188:191]\n" \
"ds_read_b128 a[20:23], v251 offset:4096\n" \
"v_mul_f32 v253, s31, v253\n" \
"v_mfma_f32_16x16x32_bf16 v[184:187], a[112:115], a[24:27], v[184:187]\n" \
"s_waitcnt lgkmcnt(6)\n" \
"ds_read_b64_tr_b16 a[92:93], v233 offset:41472\n" \
"v_mul_f32 v254, s31, v254\n" \
"v_mfma_f32_16x16x32_bf16 v[184:187], a[116:119], a[28:31], v[184:187]\n" \
"buffer_load_dwordx4 v226, s[48:51], s75 offen offset:2048 lds\n" \
"v_mfma_f32_16x16x32_bf16 v[180:183], a[104:107], a[24:27], v[180:183]\n" \
"ds_read_b64_tr_b16 a[94:95], v233 offset:43520\n" \
"v_cvt_pk_bf16_f32 v46, v243, v252\n" \
"v_mfma_f32_16x16x32_bf16 v[180:183], a[108:111], a[28:31], v[180:183]\n" \
"v_cvt_pk_bf16_f32 v47, v253, v254\n" \
"v_accvgpr_read_b32 v243, a164\n" \
"v_mfma_f32_16x16x32_bf16 v[176:179], a[96:99], a[24:27], v[176:179]\n" \
"v_accvgpr_read_b32 v252, a165\n" \
"v_accvgpr_read_b32 v253, a166\n" \
"v_mfma_f32_16x16x32_bf16 v[176:179], a[100:103], a[28:31], v[176:179]\n" \
"buffer_load_dwordx4 v227, s[48:51], s75 offen offset:3072 lds\n" \
"v_mfma_f32_16x16x32_bf16 v[64:67], a[64:67], a[0:3], v[64:67]\n" \
"s_waitcnt vmcnt(35)\n" \
"s_xor_b32 s75, s75, 0x80\n" \
"ds_read_b128 a[24:27], v250 offset:6144\n" \
"v_mfma_f32_16x16x32_bf16 v[64:67], a[68:71], a[4:7], v[64:67]\n" \
"s_add_u32 m0, s74, 135168\n" \
"ds_read_b128 a[28:31], v251 offset:6144\n" \
"v_accvgpr_read_b32 v254, a167\n" \
"v_mfma_f32_16x16x32_bf16 v[68:71], a[72:75], a[0:3], v[68:71]\n" \
"s_waitcnt lgkmcnt(4)\n" \
"ds_read_b64_tr_b16 a[96:97], v232 offset:33792\n" \
"v_mul_f32 v243, s31, v243\n" \
"v_mfma_f32_16x16x32_bf16 v[68:71], a[76:79], a[4:7], v[68:71]\n" \
"buffer_load_dwordx4 v224, s[48:51], s77 offen offset:0 lds\n" \
"v_mfma_f32_16x16x32_bf16 v[72:75], a[80:83], a[0:3], v[72:75]\n" \
"ds_read_b64_tr_b16 a[98:99], v232 offset:35840\n" \
"v_mul_f32 v252, s31, v252\n" \
"v_mfma_f32_16x16x32_bf16 v[72:75], a[84:87], a[4:7], v[72:75]\n" \
"s_waitcnt lgkmcnt(3)\n" \
"ds_read_b64_tr_b16 a[100:101], v232 offset:41984\n" \
"v_mul_f32 v253, s31, v253\n" \
"v_mfma_f32_16x16x32_bf16 v[76:79], a[88:91], a[0:3], v[76:79]\n" \
"ds_read_b64_tr_b16 a[102:103], v232 offset:44032\n" \
"v_mul_f32 v254, s31, v254\n" \
"v_mfma_f32_16x16x32_bf16 v[76:79], a[92:95], a[4:7], v[76:79]\n" \
"buffer_load_dwordx4 v225, s[48:51], s77 offen offset:1024 lds\n" \
"v_mfma_f32_16x16x32_bf16 v[108:111], a[88:91], a[8:11], v[108:111]\n" \
"ds_read_b64_tr_b16 a[104:105], v233 offset:33792\n" \
"v_cvt_pk_bf16_f32 v60, v243, v252\n" \
"v_mfma_f32_16x16x32_bf16 v[108:111], a[92:95], a[12:15], v[108:111]\n" \
"ds_read_b64_tr_b16 a[106:107], v233 offset:35840\n" \
"v_cvt_pk_bf16_f32 v61, v253, v254\n" \
"v_mfma_f32_16x16x32_bf16 v[104:107], a[80:83], a[8:11], v[104:107]\n" \
"s_waitcnt lgkmcnt(6)\n" \
"ds_read_b64_tr_b16 a[108:109], v233 offset:41984\n" \
"v_accvgpr_read_b32 v243, a180\n" \
"v_mfma_f32_16x16x32_bf16 v[104:107], a[84:87], a[12:15], v[104:107]\n" \
"buffer_load_dwordx4 v226, s[48:51], s77 offen offset:2048 lds\n" \
"v_mfma_f32_16x16x32_bf16 v[100:103], a[72:75], a[8:11], v[100:103]\n" \
"Lpg3339_%=:\n" \
"s_cbranch_execz Lpg5598_%=\n" \
"ds_read_b64_tr_b16 a[110:111], v233 offset:44032\n" \
"v_accvgpr_read_b32 v252, a181\n" \
"v_mfma_f32_16x16x32_bf16 v[100:103], a[76:79], a[12:15], v[100:103]\n" \
"ds_read_b64_tr_b16 a[112:113], v232 offset:34304\n" \
"v_accvgpr_read_b32 v253, a182\n" \
"v_mfma_f32_16x16x32_bf16 v[96:99], a[64:67], a[8:11], v[96:99]\n" \
"ds_read_b64_tr_b16 a[114:115], v232 offset:36352\n" \
"v_accvgpr_read_b32 v254, a183\n" \
"v_mfma_f32_16x16x32_bf16 v[96:99], a[68:71], a[12:15], v[96:99]\n" \
"buffer_load_dwordx4 v227, s[48:51], s77 offen offset:3072 lds\n" \
"v_mfma_f32_16x16x32_bf16 v[128:131], a[64:67], a[16:19], v[128:131]\n" \
"ds_read_b64_tr_b16 a[116:117], v232 offset:42496\n" \
"v_mul_f32 v243, s31, v243\n" \
"v_mfma_f32_16x16x32_bf16 v[128:131], a[68:71], a[20:23], v[128:131]\n" \
"ds_read_b64_tr_b16 a[118:119], v232 offset:44544\n" \
"v_mul_f32 v252, s31, v252\n" \
"v_mfma_f32_16x16x32_bf16 v[132:135], a[72:75], a[16:19], v[132:135]\n" \
"s_xor_b32 s77, s77, 0x80\n" \
"ds_read_b64_tr_b16 a[120:121], v233 offset:34304\n" \
"v_mul_f32 v253, s31, v253\n" \
"v_mfma_f32_16x16x32_bf16 v[132:135], a[76:79], a[20:23], v[132:135]\n" \
"v_mul_f32 v254, s31, v254\n" \
"v_cvt_pk_bf16_f32 v62, v243, v252\n" \
"v_mfma_f32_16x16x32_bf16 v[136:139], a[80:83], a[16:19], v[136:139]\n" \
"ds_read_b64_tr_b16 a[122:123], v233 offset:36352\n" \
"v_cvt_pk_bf16_f32 v63, v253, v254\n" \
"v_mfma_f32_16x16x32_bf16 v[136:139], a[84:87], a[20:23], v[136:139]\n" \
"ds_read_b64_tr_b16 a[124:125], v233 offset:42496\n" \
"v_accvgpr_read_b32 v243, a168\n" \
"v_mfma_f32_16x16x32_bf16 v[140:143], a[88:91], a[16:19], v[140:143]\n" \
"ds_read_b64_tr_b16 a[126:127], v233 offset:44544\n" \
"v_accvgpr_read_b32 v252, a169\n" \
"v_mfma_f32_16x16x32_bf16 v[140:143], a[92:95], a[20:23], v[140:143]\n" \
"buffer_store_dwordx4 v[204:207], v236, s[56:59], s93 offen offset:2048 sc1\n" \
"v_mfma_f32_16x16x32_bf16 v[172:175], a[88:91], a[24:27], v[172:175]\n" \
"v_accvgpr_read_b32 v253, a170\n" \
"v_accvgpr_read_b32 v254, a171\n" \
"v_mfma_f32_16x16x32_bf16 v[172:175], a[92:95], a[28:31], v[172:175]\n" \
"v_mul_f32 v243, s31, v243\n" \
"v_mul_f32 v252, s31, v252\n" \
"v_mfma_f32_16x16x32_bf16 v[168:171], a[80:83], a[24:27], v[168:171]\n" \
"v_mul_f32 v253, s31, v253\n" \
"v_mul_f32 v254, s31, v254\n" \
"v_mfma_f32_16x16x32_bf16 v[168:171], a[84:87], a[28:31], v[168:171]\n" \
"v_cvt_pk_bf16_f32 v20, v243, v252\n" \
"v_cvt_pk_bf16_f32 v21, v253, v254\n" \
"v_mfma_f32_16x16x32_bf16 v[164:167], a[72:75], a[24:27], v[164:167]\n" \
"v_accvgpr_read_b32 v243, a184\n" \
"v_accvgpr_read_b32 v252, a185\n" \
"v_mfma_f32_16x16x32_bf16 v[164:167], a[76:79], a[28:31], v[164:167]\n" \
"v_accvgpr_read_b32 v253, a186\n" \
"v_accvgpr_read_b32 v254, a187\n" \
"v_mfma_f32_16x16x32_bf16 v[160:163], a[64:67], a[24:27], v[160:163]\n" \
"v_mul_f32 v243, s31, v243\n" \
"v_mul_f32 v252, s31, v252\n" \
"v_mfma_f32_16x16x32_bf16 v[160:163], a[68:71], a[28:31], v[160:163]\n" \
"s_waitcnt vmcnt(35) lgkmcnt(0)\n" \
"s_barrier\n" \
"v_mfma_f32_16x16x32_bf16 v[80:83], a[96:99], a[0:3], v[80:83]\n" \
"ds_read_b64_tr_b16 a[64:65], v232 offset:49152\n" \
"v_mul_f32 v253, s31, v253\n" \
"v_mfma_f32_16x16x32_bf16 v[80:83], a[100:103], a[4:7], v[80:83]\n" \
"ds_read_b64_tr_b16 a[66:67], v232 offset:51200\n" \
"v_mul_f32 v254, s31, v254\n" \
"v_mfma_f32_16x16x32_bf16 v[84:87], a[104:107], a[0:3], v[84:87]\n" \
"s_add_u32 m0, s79, 32768\n" \
"ds_read_b64_tr_b16 a[68:69], v232 offset:57344\n" \
"v_cvt_pk_bf16_f32 v22, v243, v252\n" \
"v_mfma_f32_16x16x32_bf16 v[84:87], a[108:111], a[4:7], v[84:87]\n" \
"buffer_load_dwordx4 v228, s[52:55], s78 offen offset:0 lds\n" \
"v_mfma_f32_16x16x32_bf16 v[88:91], a[112:115], a[0:3], v[88:91]\n" \
"ds_read_b64_tr_b16 a[70:71], v232 offset:59392\n" \
"v_cvt_pk_bf16_f32 v23, v253, v254\n" \
"v_mfma_f32_16x16x32_bf16 v[88:91], a[116:119], a[4:7], v[88:91]\n" \
"ds_read_b64_tr_b16 a[72:73], v233 offset:49152\n" \
"v_accvgpr_read_b32 v243, a172\n" \
"v_mfma_f32_16x16x32_bf16 v[92:95], a[120:123], a[0:3], v[92:95]\n" \
"ds_read_b64_tr_b16 a[74:75], v233 offset:51200\n" \
"v_accvgpr_read_b32 v252, a173\n" \
"v_mfma_f32_16x16x32_bf16 v[92:95], a[124:127], a[4:7], v[92:95]\n" \
"buffer_load_dwordx4 v229, s[52:55], s78 offen offset:1024 lds\n" \
"v_mfma_f32_16x16x32_bf16 v[124:127], a[120:123], a[8:11], v[124:127]\n" \
"s_waitcnt vmcnt(35)\n" \
"ds_read_b128 a[0:3], v230 offset:0\n" \
"v_accvgpr_read_b32 v253, a174\n" \
"v_mfma_f32_16x16x32_bf16 v[124:127], a[124:127], a[12:15], v[124:127]\n" \
"ds_read_b128 a[4:7], v231 offset:0\n" \
"v_accvgpr_read_b32 v254, a175\n" \
"v_mfma_f32_16x16x32_bf16 v[120:123], a[112:115], a[8:11], v[120:123]\n" \
"s_xor_b32 s78, s78, 0x180000\n" \
"ds_read_b64_tr_b16 a[76:77], v233 offset:57344\n" \
"v_mul_f32 v243, s31, v243\n" \
"v_mfma_f32_16x16x32_bf16 v[120:123], a[116:119], a[12:15], v[120:123]\n" \
"buffer_load_dwordx4 v228, s[52:55], s89 offen offset:2048 lds\n" \
"v_mfma_f32_16x16x32_bf16 v[116:119], a[104:107], a[8:11], v[116:119]\n" \
"ds_read_b64_tr_b16 a[78:79], v233 offset:59392\n" \
"v_mul_f32 v252, s31, v252\n" \
"v_mfma_f32_16x16x32_bf16 v[116:119], a[108:111], a[12:15], v[116:119]\n" \
"ds_read_b64_tr_b16 a[80:81], v232 offset:49664\n" \
"v_mul_f32 v253, s31, v253\n" \
"v_mfma_f32_16x16x32_bf16 v[112:115], a[96:99], a[8:11], v[112:115]\n" \
"ds_read_b64_tr_b16 a[82:83], v232 offset:51712\n" \
"v_mul_f32 v254, s31, v254\n" \
"v_mfma_f32_16x16x32_bf16 v[112:115], a[100:103], a[12:15], v[112:115]\n" \
"buffer_load_dwordx4 v229, s[52:55], s89 offen offset:3072 lds\n" \
"v_mfma_f32_16x16x32_bf16 v[144:147], a[96:99], a[16:19], v[144:147]\n" \
"s_waitcnt vmcnt(35)\n" \
"s_xor_b32 s89, s89, 0x180000\n" \
"ds_read_b128 a[8:11], v230 offset:2048\n" \
"v_mfma_f32_16x16x32_bf16 v[144:147], a[100:103], a[20:23], v[144:147]\n" \
"s_add_u32 m0, s74, 65536\n" \
"ds_read_b128 a[12:15], v231 offset:2048\n" \
"v_cvt_pk_bf16_f32 v28, v243, v252\n" \
"v_mfma_f32_16x16x32_bf16 v[148:151], a[104:107], a[16:19], v[148:151]\n" \
"s_waitcnt lgkmcnt(6)\n" \
"ds_read_b64_tr_b16 a[84:85], v232 offset:57856\n" \
"v_cvt_pk_bf16_f32 v29, v253, v254\n" \
"v_mfma_f32_16x16x32_bf16 v[148:151], a[108:111], a[20:23], v[148:151]\n" \
"buffer_load_dwordx4 v224, s[48:51], s75 offen offset:0 lds\n" \
"v_mfma_f32_16x16x32_bf16 v[152:155], a[112:115], a[16:19], v[152:155]\n" \
"ds_read_b64_tr_b16 a[86:87], v232 offset:59904\n" \
"v_accvgpr_read_b32 v243, a188\n" \
"v_mfma_f32_16x16x32_bf16 v[152:155], a[116:119], a[20:23], v[152:155]\n" \
"ds_read_b64_tr_b16 a[88:89], v233 offset:49664\n" \
"v_accvgpr_read_b32 v252, a189\n" \
"v_mfma_f32_16x16x32_bf16 v[156:159], a[120:123], a[16:19], v[156:159]\n" \
"ds_read_b64_tr_b16 a[90:91], v233 offset:51712\n" \
"v_accvgpr_read_b32 v253, a190\n" \
"v_mfma_f32_16x16x32_bf16 v[156:159], a[124:127], a[20:23], v[156:159]\n" \
"buffer_load_dwordx4 v225, s[48:51], s75 offen offset:1024 lds\n" \
"v_mfma_f32_16x16x32_bf16 v[188:191], a[120:123], a[24:27], v[188:191]\n" \
"s_waitcnt vmcnt(35)\n" \
"ds_read_b128 a[16:19], v230 offset:4096\n" \
"v_accvgpr_read_b32 v254, a191\n" \
"v_mfma_f32_16x16x32_bf16 v[188:191], a[124:127], a[28:31], v[188:191]\n" \
"ds_read_b128 a[20:23], v231 offset:4096\n" \
"v_mul_f32 v243, s31, v243\n" \
"v_mfma_f32_16x16x32_bf16 v[184:187], a[112:115], a[24:27], v[184:187]\n" \
"s_waitcnt lgkmcnt(6)\n" \
"ds_read_b64_tr_b16 a[92:93], v233 offset:57856\n" \
"v_mul_f32 v252, s31, v252\n" \
"v_mfma_f32_16x16x32_bf16 v[184:187], a[116:119], a[28:31], v[184:187]\n" \
"buffer_load_dwordx4 v226, s[48:51], s75 offen offset:2048 lds\n" \
"v_mfma_f32_16x16x32_bf16 v[180:183], a[104:107], a[24:27], v[180:183]\n" \
"ds_read_b64_tr_b16 a[94:95], v233 offset:59904\n" \
"v_mul_f32 v253, s31, v253\n" \
"v_mfma_f32_16x16x32_bf16 v[180:183], a[108:111], a[28:31], v[180:183]\n" \
"v_mul_f32 v254, s31, v254\n" \
"v_cvt_pk_bf16_f32 v30, v243, v252\n" \
"v_mfma_f32_16x16x32_bf16 v[176:179], a[96:99], a[24:27], v[176:179]\n" \
"v_cvt_pk_bf16_f32 v31, v253, v254\n" \
"v_accvgpr_read_b32 v243, a128\n" \
"v_mfma_f32_16x16x32_bf16 v[176:179], a[100:103], a[28:31], v[176:179]\n" \
"buffer_load_dwordx4 v227, s[48:51], s75 offen offset:3072 lds\n" \
"v_mfma_f32_16x16x32_bf16 v[64:67], a[64:67], a[0:3], v[64:67]\n" \
"s_waitcnt vmcnt(35)\n" \
"s_xor_b32 s75, s75, 0x180\n" \
"ds_read_b128 a[24:27], v230 offset:6144\n" \
"v_mfma_f32_16x16x32_bf16 v[64:67], a[68:71], a[4:7], v[64:67]\n" \
"s_add_u32 m0, s74, 69632\n" \
"ds_read_b128 a[28:31], v231 offset:6144\n" \
"v_accvgpr_read_b32 v252, a129\n" \
"v_mfma_f32_16x16x32_bf16 v[68:71], a[72:75], a[0:3], v[68:71]\n" \
"s_waitcnt lgkmcnt(4)\n" \
"ds_read_b64_tr_b16 a[96:97], v232 offset:50176\n" \
"v_accvgpr_read_b32 v253, a130\n" \
"v_mfma_f32_16x16x32_bf16 v[68:71], a[76:79], a[4:7], v[68:71]\n" \
"buffer_load_dwordx4 v224, s[48:51], s77 offen offset:0 lds\n" \
"v_mfma_f32_16x16x32_bf16 v[72:75], a[80:83], a[0:3], v[72:75]\n" \
"ds_read_b64_tr_b16 a[98:99], v232 offset:52224\n" \
"v_accvgpr_read_b32 v254, a131\n" \
"v_mfma_f32_16x16x32_bf16 v[72:75], a[84:87], a[4:7], v[72:75]\n" \
"s_waitcnt lgkmcnt(3)\n" \
"ds_read_b64_tr_b16 a[100:101], v232 offset:58368\n" \
"v_mul_f32 v243, s31, v243\n" \
"v_mfma_f32_16x16x32_bf16 v[76:79], a[88:91], a[0:3], v[76:79]\n" \
"ds_read_b64_tr_b16 a[102:103], v232 offset:60416\n" \
"v_mul_f32 v252, s31, v252\n" \
"v_mfma_f32_16x16x32_bf16 v[76:79], a[92:95], a[4:7], v[76:79]\n" \
"buffer_load_dwordx4 v225, s[48:51], s77 offen offset:1024 lds\n" \
"v_mfma_f32_16x16x32_bf16 v[108:111], a[88:91], a[8:11], v[108:111]\n" \
"ds_read_b64_tr_b16 a[104:105], v233 offset:50176\n" \
"v_mul_f32 v253, s31, v253\n" \
"v_mfma_f32_16x16x32_bf16 v[108:111], a[92:95], a[12:15], v[108:111]\n" \
"ds_read_b64_tr_b16 a[106:107], v233 offset:52224\n" \
"v_mul_f32 v254, s31, v254\n" \
"v_mfma_f32_16x16x32_bf16 v[104:107], a[80:83], a[8:11], v[104:107]\n" \
"s_waitcnt lgkmcnt(6)\n" \
"ds_read_b64_tr_b16 a[108:109], v233 offset:58368\n" \
"v_cvt_pk_bf16_f32 v0, v243, v252\n" \
"v_mfma_f32_16x16x32_bf16 v[104:107], a[84:87], a[12:15], v[104:107]\n" \
"buffer_load_dwordx4 v226, s[48:51], s77 offen offset:2048 lds\n" \
"v_mfma_f32_16x16x32_bf16 v[100:103], a[72:75], a[8:11], v[100:103]\n" \
"ds_read_b64_tr_b16 a[110:111], v233 offset:60416\n" \
"v_cvt_pk_bf16_f32 v1, v253, v254\n" \
"v_mfma_f32_16x16x32_bf16 v[100:103], a[76:79], a[12:15], v[100:103]\n" \
"ds_read_b64_tr_b16 a[112:113], v232 offset:50688\n" \
"v_accvgpr_read_b32 v243, a144\n" \
"v_mfma_f32_16x16x32_bf16 v[96:99], a[64:67], a[8:11], v[96:99]\n" \
"ds_read_b64_tr_b16 a[114:115], v232 offset:52736\n" \
"v_accvgpr_read_b32 v252, a145\n" \
"v_mfma_f32_16x16x32_bf16 v[96:99], a[68:71], a[12:15], v[96:99]\n" \
"buffer_load_dwordx4 v227, s[48:51], s77 offen offset:3072 lds\n" \
"v_mfma_f32_16x16x32_bf16 v[128:131], a[64:67], a[16:19], v[128:131]\n" \
"ds_read_b64_tr_b16 a[116:117], v232 offset:58880\n" \
"v_accvgpr_read_b32 v253, a146\n" \
"v_mfma_f32_16x16x32_bf16 v[128:131], a[68:71], a[20:23], v[128:131]\n" \
"ds_read_b64_tr_b16 a[118:119], v232 offset:60928\n" \
"v_accvgpr_read_b32 v254, a147\n" \
"v_mfma_f32_16x16x32_bf16 v[132:135], a[72:75], a[16:19], v[132:135]\n" \
"s_xor_b32 s77, s77, 0x180\n" \
"ds_read_b64_tr_b16 a[120:121], v233 offset:50688\n" \
"v_mul_f32 v243, s31, v243\n" \
"v_mfma_f32_16x16x32_bf16 v[132:135], a[76:79], a[20:23], v[132:135]\n" \
"v_mul_f32 v252, s31, v252\n" \
"v_mul_f32 v253, s31, v253\n" \
"v_mfma_f32_16x16x32_bf16 v[136:139], a[80:83], a[16:19], v[136:139]\n" \
"ds_read_b64_tr_b16 a[122:123], v233 offset:52736\n" \
"v_mul_f32 v254, s31, v254\n" \
"v_mfma_f32_16x16x32_bf16 v[136:139], a[84:87], a[20:23], v[136:139]\n" \
"ds_read_b64_tr_b16 a[124:125], v233 offset:58880\n" \
"v_cvt_pk_bf16_f32 v2, v243, v252\n" \
"v_mfma_f32_16x16x32_bf16 v[140:143], a[88:91], a[16:19], v[140:143]\n" \
"ds_read_b64_tr_b16 a[126:127], v233 offset:60928\n" \
"v_cvt_pk_bf16_f32 v3, v253, v254\n" \
"v_mfma_f32_16x16x32_bf16 v[140:143], a[92:95], a[20:23], v[140:143]\n" \
"buffer_store_dwordx4 v[220:223], v236, s[56:59], s93 offen offset:2304 sc1\n" \
"v_mfma_f32_16x16x32_bf16 v[172:175], a[88:91], a[24:27], v[172:175]\n" \
"v_accvgpr_read_b32 v243, a132\n" \
"v_accvgpr_read_b32 v252, a133\n" \
"v_mfma_f32_16x16x32_bf16 v[172:175], a[92:95], a[28:31], v[172:175]\n" \
"v_accvgpr_read_b32 v253, a134\n" \
"v_accvgpr_read_b32 v254, a135\n" \
"v_mfma_f32_16x16x32_bf16 v[168:171], a[80:83], a[24:27], v[168:171]\n" \
"v_mul_f32 v243, s31, v243\n" \
"v_mul_f32 v252, s31, v252\n" \
"v_mfma_f32_16x16x32_bf16 v[168:171], a[84:87], a[28:31], v[168:171]\n" \
"v_mul_f32 v253, s31, v253\n" \
"v_mul_f32 v254, s31, v254\n" \
"v_mfma_f32_16x16x32_bf16 v[164:167], a[72:75], a[24:27], v[164:167]\n" \
"v_cvt_pk_bf16_f32 v4, v243, v252\n" \
"v_cvt_pk_bf16_f32 v5, v253, v254\n" \
"v_mfma_f32_16x16x32_bf16 v[164:167], a[76:79], a[28:31], v[164:167]\n" \
"v_accvgpr_read_b32 v243, a148\n" \
"v_accvgpr_read_b32 v252, a149\n" \
"v_mfma_f32_16x16x32_bf16 v[160:163], a[64:67], a[24:27], v[160:163]\n" \
"v_accvgpr_read_b32 v253, a150\n" \
"v_accvgpr_read_b32 v254, a151\n" \
"v_mfma_f32_16x16x32_bf16 v[160:163], a[68:71], a[28:31], v[160:163]\n" \
"s_waitcnt vmcnt(35) lgkmcnt(0)\n" \
"s_barrier\n" \
"v_mfma_f32_16x16x32_bf16 v[80:83], a[96:99], a[0:3], v[80:83]\n" \
"ds_read_b64_tr_b16 a[64:65], v232 offset:0\n" \
"v_mul_f32 v243, s31, v243\n" \
"v_mfma_f32_16x16x32_bf16 v[80:83], a[100:103], a[4:7], v[80:83]\n" \
"ds_read_b64_tr_b16 a[66:67], v232 offset:2048\n" \
"v_mul_f32 v252, s31, v252\n" \
"v_mfma_f32_16x16x32_bf16 v[84:87], a[104:107], a[0:3], v[84:87]\n" \
"s_add_u32 m0, s79, 49152\n" \
"ds_read_b64_tr_b16 a[68:69], v232 offset:8192\n" \
"v_mul_f32 v253, s31, v253\n" \
"v_mfma_f32_16x16x32_bf16 v[84:87], a[108:111], a[4:7], v[84:87]\n" \
"buffer_load_dwordx4 v228, s[52:55], s78 offen offset:0 lds\n" \
"v_mfma_f32_16x16x32_bf16 v[88:91], a[112:115], a[0:3], v[88:91]\n" \
"ds_read_b64_tr_b16 a[70:71], v232 offset:10240\n" \
"v_mul_f32 v254, s31, v254\n" \
"v_mfma_f32_16x16x32_bf16 v[88:91], a[116:119], a[4:7], v[88:91]\n" \
"ds_read_b64_tr_b16 a[72:73], v233 offset:0\n" \
"v_cvt_pk_bf16_f32 v6, v243, v252\n" \
"v_mfma_f32_16x16x32_bf16 v[92:95], a[120:123], a[0:3], v[92:95]\n" \
"ds_read_b64_tr_b16 a[74:75], v233 offset:2048\n" \
"v_cvt_pk_bf16_f32 v7, v253, v254\n" \
"v_mfma_f32_16x16x32_bf16 v[92:95], a[124:127], a[4:7], v[92:95]\n" \
"buffer_load_dwordx4 v229, s[52:55], s78 offen offset:1024 lds\n" \
"v_mfma_f32_16x16x32_bf16 v[124:127], a[120:123], a[8:11], v[124:127]\n" \
"s_waitcnt vmcnt(35)\n" \
"ds_read_b128 a[0:3], v230 offset:32768\n" \
"v_accvgpr_read_b32 v243, a136\n" \
"v_mfma_f32_16x16x32_bf16 v[124:127], a[124:127], a[12:15], v[124:127]\n" \
"ds_read_b128 a[4:7], v231 offset:32768\n" \
"v_accvgpr_read_b32 v252, a137\n" \
"v_mfma_f32_16x16x32_bf16 v[120:123], a[112:115], a[8:11], v[120:123]\n" \
"s_xor_b32 s78, s78, 0x300000\n" \
"ds_read_b64_tr_b16 a[76:77], v233 offset:8192\n" \
"v_accvgpr_read_b32 v253, a138\n" \
"v_mfma_f32_16x16x32_bf16 v[120:123], a[116:119], a[12:15], v[120:123]\n" \
"buffer_load_dwordx4 v228, s[52:55], s89 offen offset:2048 lds\n" \
"v_mfma_f32_16x16x32_bf16 v[116:119], a[104:107], a[8:11], v[116:119]\n" \
"ds_read_b64_tr_b16 a[78:79], v233 offset:10240\n" \
"v_accvgpr_read_b32 v254, a139\n" \
"v_mfma_f32_16x16x32_bf16 v[116:119], a[108:111], a[12:15], v[116:119]\n" \
"ds_read_b64_tr_b16 a[80:81], v232 offset:512\n" \
"v_mul_f32 v243, s31, v243\n" \
"v_mfma_f32_16x16x32_bf16 v[112:115], a[96:99], a[8:11], v[112:115]\n" \
"ds_read_b64_tr_b16 a[82:83], v232 offset:2560\n" \
"v_mul_f32 v252, s31, v252\n" \
"v_mfma_f32_16x16x32_bf16 v[112:115], a[100:103], a[12:15], v[112:115]\n" \
"buffer_load_dwordx4 v229, s[52:55], s89 offen offset:3072 lds\n" \
"v_mfma_f32_16x16x32_bf16 v[144:147], a[96:99], a[16:19], v[144:147]\n" \
"s_waitcnt vmcnt(35)\n" \
"s_xor_b32 s89, s89, 0x300000\n" \
"ds_read_b128 a[8:11], v230 offset:34816\n" \
"v_mfma_f32_16x16x32_bf16 v[144:147], a[100:103], a[20:23], v[144:147]\n" \
"s_add_u32 m0, s74, 98304\n" \
"ds_read_b128 a[12:15], v231 offset:34816\n" \
"v_mul_f32 v253, s31, v253\n" \
"v_mfma_f32_16x16x32_bf16 v[148:151], a[104:107], a[16:19], v[148:151]\n" \
"s_waitcnt lgkmcnt(6)\n" \
"ds_read_b64_tr_b16 a[84:85], v232 offset:8704\n" \
"v_mul_f32 v254, s31, v254\n" \
"v_mfma_f32_16x16x32_bf16 v[148:151], a[108:111], a[20:23], v[148:151]\n" \
"buffer_load_dwordx4 v224, s[48:51], s75 offen offset:0 lds\n" \
"v_mfma_f32_16x16x32_bf16 v[152:155], a[112:115], a[16:19], v[152:155]\n" \
"ds_read_b64_tr_b16 a[86:87], v232 offset:10752\n" \
"v_cvt_pk_bf16_f32 v8, v243, v252\n" \
"v_mfma_f32_16x16x32_bf16 v[152:155], a[116:119], a[20:23], v[152:155]\n" \
"ds_read_b64_tr_b16 a[88:89], v233 offset:512\n" \
"v_cvt_pk_bf16_f32 v9, v253, v254\n" \
"v_mfma_f32_16x16x32_bf16 v[156:159], a[120:123], a[16:19], v[156:159]\n" \
"ds_read_b64_tr_b16 a[90:91], v233 offset:2560\n" \
"v_accvgpr_read_b32 v243, a152\n" \
"v_mfma_f32_16x16x32_bf16 v[156:159], a[124:127], a[20:23], v[156:159]\n" \
"buffer_load_dwordx4 v225, s[48:51], s75 offen offset:1024 lds\n" \
"v_mfma_f32_16x16x32_bf16 v[188:191], a[120:123], a[24:27], v[188:191]\n" \
"s_waitcnt vmcnt(35)\n" \
"ds_read_b128 a[16:19], v230 offset:36864\n" \
"v_accvgpr_read_b32 v252, a153\n" \
"v_mfma_f32_16x16x32_bf16 v[188:191], a[124:127], a[28:31], v[188:191]\n" \
"ds_read_b128 a[20:23], v231 offset:36864\n" \
"v_accvgpr_read_b32 v253, a154\n" \
"v_mfma_f32_16x16x32_bf16 v[184:187], a[112:115], a[24:27], v[184:187]\n" \
"s_waitcnt lgkmcnt(6)\n" \
"ds_read_b64_tr_b16 a[92:93], v233 offset:8704\n" \
"v_accvgpr_read_b32 v254, a155\n" \
"v_mfma_f32_16x16x32_bf16 v[184:187], a[116:119], a[28:31], v[184:187]\n" \
"buffer_load_dwordx4 v226, s[48:51], s75 offen offset:2048 lds\n" \
"v_mfma_f32_16x16x32_bf16 v[180:183], a[104:107], a[24:27], v[180:183]\n" \
"ds_read_b64_tr_b16 a[94:95], v233 offset:10752\n" \
"v_mul_f32 v243, s31, v243\n" \
"v_mfma_f32_16x16x32_bf16 v[180:183], a[108:111], a[28:31], v[180:183]\n" \
"v_mul_f32 v252, s31, v252\n" \
"v_mul_f32 v253, s31, v253\n" \
"v_mfma_f32_16x16x32_bf16 v[176:179], a[96:99], a[24:27], v[176:179]\n" \
"v_mul_f32 v254, s31, v254\n" \
"v_cvt_pk_bf16_f32 v10, v243, v252\n" \
"v_mfma_f32_16x16x32_bf16 v[176:179], a[100:103], a[28:31], v[176:179]\n" \
"buffer_load_dwordx4 v227, s[48:51], s75 offen offset:3072 lds\n" \
"v_mfma_f32_16x16x32_bf16 v[64:67], a[64:67], a[0:3], v[64:67]\n" \
"s_waitcnt vmcnt(35)\n" \
"s_xor_b32 s75, s75, 0x300\n" \
"ds_read_b128 a[24:27], v230 offset:38912\n" \
"v_mfma_f32_16x16x32_bf16 v[64:67], a[68:71], a[4:7], v[64:67]\n" \
"s_add_u32 m0, s74, 102400\n" \
"ds_read_b128 a[28:31], v231 offset:38912\n" \
"v_cvt_pk_bf16_f32 v11, v253, v254\n" \
"v_mfma_f32_16x16x32_bf16 v[68:71], a[72:75], a[0:3], v[68:71]\n" \
"s_waitcnt lgkmcnt(4)\n" \
"ds_read_b64_tr_b16 a[96:97], v232 offset:1024\n" \
"v_accvgpr_read_b32 v243, a140\n" \
"v_mfma_f32_16x16x32_bf16 v[68:71], a[76:79], a[4:7], v[68:71]\n" \
"buffer_load_dwordx4 v224, s[48:51], s77 offen offset:0 lds\n" \
"v_mfma_f32_16x16x32_bf16 v[72:75], a[80:83], a[0:3], v[72:75]\n" \
"ds_read_b64_tr_b16 a[98:99], v232 offset:3072\n" \
"v_accvgpr_read_b32 v252, a141\n" \
"v_mfma_f32_16x16x32_bf16 v[72:75], a[84:87], a[4:7], v[72:75]\n" \
"s_waitcnt lgkmcnt(3)\n" \
"ds_read_b64_tr_b16 a[100:101], v232 offset:9216\n" \
"v_accvgpr_read_b32 v253, a142\n" \
"v_mfma_f32_16x16x32_bf16 v[76:79], a[88:91], a[0:3], v[76:79]\n" \
"ds_read_b64_tr_b16 a[102:103], v232 offset:11264\n" \
"v_accvgpr_read_b32 v254, a143\n" \
"v_mfma_f32_16x16x32_bf16 v[76:79], a[92:95], a[4:7], v[76:79]\n" \
"buffer_load_dwordx4 v225, s[48:51], s77 offen offset:1024 lds\n" \
"v_mfma_f32_16x16x32_bf16 v[108:111], a[88:91], a[8:11], v[108:111]\n" \
"ds_read_b64_tr_b16 a[104:105], v233 offset:1024\n" \
"v_mul_f32 v243, s31, v243\n" \
"v_mfma_f32_16x16x32_bf16 v[108:111], a[92:95], a[12:15], v[108:111]\n" \
"ds_read_b64_tr_b16 a[106:107], v233 offset:3072\n" \
"v_mul_f32 v252, s31, v252\n" \
"v_mfma_f32_16x16x32_bf16 v[104:107], a[80:83], a[8:11], v[104:107]\n" \
"s_waitcnt lgkmcnt(6)\n" \
"ds_read_b64_tr_b16 a[108:109], v233 offset:9216\n" \
"v_mul_f32 v253, s31, v253\n" \
"v_mfma_f32_16x16x32_bf16 v[104:107], a[84:87], a[12:15], v[104:107]\n" \
"buffer_load_dwordx4 v226, s[48:51], s77 offen offset:2048 lds\n" \
"v_mfma_f32_16x16x32_bf16 v[100:103], a[72:75], a[8:11], v[100:103]\n" \
"ds_read_b64_tr_b16 a[110:111], v233 offset:11264\n" \
"v_mul_f32 v254, s31, v254\n" \
"v_mfma_f32_16x16x32_bf16 v[100:103], a[76:79], a[12:15], v[100:103]\n" \
"ds_read_b64_tr_b16 a[112:113], v232 offset:1536\n" \
"v_cvt_pk_bf16_f32 v12, v243, v252\n" \
"v_mfma_f32_16x16x32_bf16 v[96:99], a[64:67], a[8:11], v[96:99]\n" \
"ds_read_b64_tr_b16 a[114:115], v232 offset:3584\n" \
"v_cvt_pk_bf16_f32 v13, v253, v254\n" \
"v_mfma_f32_16x16x32_bf16 v[96:99], a[68:71], a[12:15], v[96:99]\n" \
"buffer_load_dwordx4 v227, s[48:51], s77 offen offset:3072 lds\n" \
"v_mfma_f32_16x16x32_bf16 v[128:131], a[64:67], a[16:19], v[128:131]\n" \
"ds_read_b64_tr_b16 a[116:117], v232 offset:9728\n" \
"v_accvgpr_read_b32 v243, a156\n" \
"v_mfma_f32_16x16x32_bf16 v[128:131], a[68:71], a[20:23], v[128:131]\n" \
"ds_read_b64_tr_b16 a[118:119], v232 offset:11776\n" \
"v_accvgpr_read_b32 v252, a157\n" \
"v_mfma_f32_16x16x32_bf16 v[132:135], a[72:75], a[16:19], v[132:135]\n" \
"s_xor_b32 s77, s77, 0x300\n" \
"ds_read_b64_tr_b16 a[120:121], v233 offset:1536\n" \
"v_accvgpr_read_b32 v253, a158\n" \
"v_mfma_f32_16x16x32_bf16 v[132:135], a[76:79], a[20:23], v[132:135]\n" \
"v_accvgpr_read_b32 v254, a159\n" \
"v_mul_f32 v243, s31, v243\n" \
"v_mfma_f32_16x16x32_bf16 v[136:139], a[80:83], a[16:19], v[136:139]\n" \
"ds_read_b64_tr_b16 a[122:123], v233 offset:3584\n" \
"v_mul_f32 v252, s31, v252\n" \
"v_mfma_f32_16x16x32_bf16 v[136:139], a[84:87], a[20:23], v[136:139]\n" \
"ds_read_b64_tr_b16 a[124:125], v233 offset:9728\n" \
"v_mul_f32 v253, s31, v253\n" \
"v_mfma_f32_16x16x32_bf16 v[140:143], a[88:91], a[16:19], v[140:143]\n" \
"s_add_u32 s93, s87, 17858560\n" \
"ds_read_b64_tr_b16 a[126:127], v233 offset:11776\n" \
"v_mul_f32 v254, s31, v254\n" \
"v_mfma_f32_16x16x32_bf16 v[140:143], a[92:95], a[20:23], v[140:143]\n" \
"buffer_store_dwordx4 a[224:227], v234, s[56:59], s93 offen offset:0 sc1\n" \
"v_mfma_f32_16x16x32_bf16 v[172:175], a[88:91], a[24:27], v[172:175]\n" \
"v_cvt_pk_bf16_f32 v14, v243, v252\n" \
"v_cvt_pk_bf16_f32 v15, v253, v254\n" \
"v_mfma_f32_16x16x32_bf16 v[172:175], a[92:95], a[28:31], v[172:175]\n" \
"v_accvgpr_write_b32 a56, v244\n" \
"v_accvgpr_write_b32 a57, v245\n" \
"v_mfma_f32_16x16x32_bf16 v[168:171], a[80:83], a[24:27], v[168:171]\n" \
"v_accvgpr_write_b32 a59, v247\n" \
"v_accvgpr_write_b32 a60, v248\n" \
"v_mfma_f32_16x16x32_bf16 v[168:171], a[84:87], a[28:31], v[168:171]\n" \
"v_accvgpr_write_b32 a61, v249\n" \
"v_accvgpr_write_b32 a48, 0\n" \
"v_mfma_f32_16x16x32_bf16 v[164:167], a[72:75], a[24:27], v[164:167]\n" \
"v_accvgpr_write_b32 a49, 0\n" \
"v_accvgpr_write_b32 a50, 0\n" \
"v_mfma_f32_16x16x32_bf16 v[164:167], a[76:79], a[28:31], v[164:167]\n" \
"v_accvgpr_write_b32 a51, 0\n" \
"v_accvgpr_write_b32 a52, 0\n" \
"v_mfma_f32_16x16x32_bf16 v[160:163], a[64:67], a[24:27], v[160:163]\n" \
"v_accvgpr_write_b32 a53, 0\n" \
"v_mfma_f32_16x16x32_bf16 v[160:163], a[68:71], a[28:31], v[160:163]\n" \
"s_waitcnt vmcnt(35) lgkmcnt(0)\n" \
"s_barrier\n" \
"v_mfma_f32_16x16x32_bf16 v[80:83], a[96:99], a[0:3], v[80:83]\n" \
"ds_read_b64_tr_b16 a[64:65], v232 offset:16384\n" \
"v_mfma_f32_16x16x32_bf16 v[80:83], a[100:103], a[4:7], v[80:83]\n" \
"ds_read_b64_tr_b16 a[66:67], v232 offset:18432\n" \
"v_mfma_f32_16x16x32_bf16 v[84:87], a[104:107], a[0:3], v[84:87]\n" \
"s_add_u32 m0, s79, 0\n" \
"ds_read_b64_tr_b16 a[68:69], v232 offset:24576\n" \
"v_mfma_f32_16x16x32_bf16 v[84:87], a[108:111], a[4:7], v[84:87]\n" \
"buffer_load_dwordx4 v228, s[52:55], s78 offen offset:0 lds\n" \
"v_mfma_f32_16x16x32_bf16 v[88:91], a[112:115], a[0:3], v[88:91]\n" \
"ds_read_b64_tr_b16 a[70:71], v232 offset:26624\n" \
"v_mfma_f32_16x16x32_bf16 v[88:91], a[116:119], a[4:7], v[88:91]\n" \
"ds_read_b64_tr_b16 a[72:73], v233 offset:16384\n" \
"v_mfma_f32_16x16x32_bf16 v[92:95], a[120:123], a[0:3], v[92:95]\n" \
"ds_read_b64_tr_b16 a[74:75], v233 offset:18432\n" \
"v_mfma_f32_16x16x32_bf16 v[92:95], a[124:127], a[4:7], v[92:95]\n" \
"buffer_load_dwordx4 v229, s[52:55], s78 offen offset:1024 lds\n" \
"v_mfma_f32_16x16x32_bf16 v[124:127], a[120:123], a[8:11], v[124:127]\n" \
"s_waitcnt vmcnt(35)\n" \
"ds_read_b128 a[0:3], v250 offset:0\n" \
"v_mfma_f32_16x16x32_bf16 v[124:127], a[124:127], a[12:15], v[124:127]\n" \
"ds_read_b128 a[4:7], v251 offset:0\n" \
"v_mfma_f32_16x16x32_bf16 v[120:123], a[112:115], a[8:11], v[120:123]\n" \
"s_xor_b32 s78, s78, 0x80000\n" \
"ds_read_b64_tr_b16 a[76:77], v233 offset:24576\n" \
"v_mfma_f32_16x16x32_bf16 v[120:123], a[116:119], a[12:15], v[120:123]\n" \
"buffer_load_dwordx4 v228, s[52:55], s89 offen offset:2048 lds\n" \
"v_mfma_f32_16x16x32_bf16 v[116:119], a[104:107], a[8:11], v[116:119]\n" \
"ds_read_b64_tr_b16 a[78:79], v233 offset:26624\n" \
"v_mfma_f32_16x16x32_bf16 v[116:119], a[108:111], a[12:15], v[116:119]\n" \
"ds_read_b64_tr_b16 a[80:81], v232 offset:16896\n" \
"v_mfma_f32_16x16x32_bf16 v[112:115], a[96:99], a[8:11], v[112:115]\n" \
"ds_read_b64_tr_b16 a[82:83], v232 offset:18944\n" \
"v_mfma_f32_16x16x32_bf16 v[112:115], a[100:103], a[12:15], v[112:115]\n" \
"buffer_load_dwordx4 v229, s[52:55], s89 offen offset:3072 lds\n" \
"v_mfma_f32_16x16x32_bf16 v[144:147], a[96:99], a[16:19], v[144:147]\n" \
"s_waitcnt vmcnt(35)\n" \
"s_xor_b32 s89, s89, 0x80000\n" \
"ds_read_b128 a[8:11], v250 offset:2048\n" \
"v_mfma_f32_16x16x32_bf16 v[144:147], a[100:103], a[20:23], v[144:147]\n" \
"s_add_u32 m0, s74, 131072\n" \
"ds_read_b128 a[12:15], v251 offset:2048\n" \
"v_mfma_f32_16x16x32_bf16 v[148:151], a[104:107], a[16:19], v[148:151]\n" \
"s_waitcnt lgkmcnt(6)\n" \
"ds_read_b64_tr_b16 a[84:85], v232 offset:25088\n" \
"v_mfma_f32_16x16x32_bf16 v[148:151], a[108:111], a[20:23], v[148:151]\n" \
"buffer_load_dwordx4 v224, s[48:51], s75 offen offset:0 lds\n" \
"v_mfma_f32_16x16x32_bf16 v[152:155], a[112:115], a[16:19], v[152:155]\n" \
"ds_read_b64_tr_b16 a[86:87], v232 offset:27136\n" \
"v_mfma_f32_16x16x32_bf16 v[152:155], a[116:119], a[20:23], v[152:155]\n" \
"ds_read_b64_tr_b16 a[88:89], v233 offset:16896\n" \
"v_mfma_f32_16x16x32_bf16 v[156:159], a[120:123], a[16:19], v[156:159]\n" \
"ds_read_b64_tr_b16 a[90:91], v233 offset:18944\n" \
"v_mfma_f32_16x16x32_bf16 v[156:159], a[124:127], a[20:23], v[156:159]\n" \
"buffer_load_dwordx4 v225, s[48:51], s75 offen offset:1024 lds\n" \
"v_mfma_f32_16x16x32_bf16 v[188:191], a[120:123], a[24:27], v[188:191]\n" \
"s_waitcnt vmcnt(35)\n" \
"ds_read_b128 a[16:19], v250 offset:4096\n" \
"v_mfma_f32_16x16x32_bf16 v[188:191], a[124:127], a[28:31], v[188:191]\n" \
"ds_read_b128 a[20:23], v251 offset:4096\n" \
"v_mfma_f32_16x16x32_bf16 v[184:187], a[112:115], a[24:27], v[184:187]\n" \
"s_waitcnt lgkmcnt(6)\n" \
"ds_read_b64_tr_b16 a[92:93], v233 offset:25088\n" \
"v_mfma_f32_16x16x32_bf16 v[184:187], a[116:119], a[28:31], v[184:187]\n" \
"buffer_load_dwordx4 v226, s[48:51], s75 offen offset:2048 lds\n" \
"v_mfma_f32_16x16x32_bf16 v[180:183], a[104:107], a[24:27], v[180:183]\n" \
"ds_read_b64_tr_b16 a[94:95], v233 offset:27136\n" \
"v_mfma_f32_16x16x32_bf16 v[180:183], a[108:111], a[28:31], v[180:183]\n" \
"v_mfma_f32_16x16x32_bf16 v[176:179], a[96:99], a[24:27], v[176:179]\n" \
"v_mfma_f32_16x16x32_bf16 v[176:179], a[100:103], a[28:31], v[176:179]\n" \
"buffer_load_dwordx4 v227, s[48:51], s75 offen offset:3072 lds\n" \
"v_mfma_f32_16x16x32_bf16 v[64:67], a[64:67], a[0:3], v[64:67]\n" \
"s_waitcnt vmcnt(35)\n" \
"s_xor_b32 s75, s75, 0x80\n" \
"ds_read_b128 a[24:27], v250 offset:6144\n" \
"v_mfma_f32_16x16x32_bf16 v[64:67], a[68:71], a[4:7], v[64:67]\n" \
"s_add_u32 m0, s74, 135168\n" \
"ds_read_b128 a[28:31], v251 offset:6144\n" \
"v_mfma_f32_16x16x32_bf16 v[68:71], a[72:75], a[0:3], v[68:71]\n" \
"s_waitcnt lgkmcnt(2)\n" \
"ds_read_b64_tr_b16 a[96:97], v232 offset:17408\n" \
"v_mfma_f32_16x16x32_bf16 v[68:71], a[76:79], a[4:7], v[68:71]\n" \
"buffer_load_dwordx4 v224, s[48:51], s77 offen offset:0 lds\n" \
"v_mfma_f32_16x16x32_bf16 v[72:75], a[80:83], a[0:3], v[72:75]\n" \
"ds_read_b64_tr_b16 a[98:99], v232 offset:19456\n" \
"v_mfma_f32_16x16x32_bf16 v[72:75], a[84:87], a[4:7], v[72:75]\n" \
"ds_read_b64_tr_b16 a[100:101], v232 offset:25600\n" \
"v_mfma_f32_16x16x32_bf16 v[76:79], a[88:91], a[0:3], v[76:79]\n" \
"ds_read_b64_tr_b16 a[102:103], v232 offset:27648\n" \
"v_mfma_f32_16x16x32_bf16 v[76:79], a[92:95], a[4:7], v[76:79]\n" \
"buffer_load_dwordx4 v225, s[48:51], s77 offen offset:1024 lds\n" \
"v_mfma_f32_16x16x32_bf16 v[108:111], a[88:91], a[8:11], v[108:111]\n" \
"ds_read_b64_tr_b16 a[104:105], v233 offset:17408\n" \
"v_mfma_f32_16x16x32_bf16 v[108:111], a[92:95], a[12:15], v[108:111]\n" \
"ds_read_b64_tr_b16 a[106:107], v233 offset:19456\n" \
"v_mfma_f32_16x16x32_bf16 v[104:107], a[80:83], a[8:11], v[104:107]\n" \
"s_waitcnt lgkmcnt(6)\n" \
"ds_read_b64_tr_b16 a[108:109], v233 offset:25600\n" \
"v_mfma_f32_16x16x32_bf16 v[104:107], a[84:87], a[12:15], v[104:107]\n" \
"buffer_load_dwordx4 v226, s[48:51], s77 offen offset:2048 lds\n" \
"v_mfma_f32_16x16x32_bf16 v[100:103], a[72:75], a[8:11], v[100:103]\n" \
"ds_read_b64_tr_b16 a[110:111], v233 offset:27648\n" \
"v_mfma_f32_16x16x32_bf16 v[100:103], a[76:79], a[12:15], v[100:103]\n" \
"ds_read_b64_tr_b16 a[112:113], v232 offset:17920\n" \
"v_mfma_f32_16x16x32_bf16 v[96:99], a[64:67], a[8:11], v[96:99]\n" \
"ds_read_b64_tr_b16 a[114:115], v232 offset:19968\n" \
"v_mfma_f32_16x16x32_bf16 v[96:99], a[68:71], a[12:15], v[96:99]\n" \
"buffer_load_dwordx4 v227, s[48:51], s77 offen offset:3072 lds\n" \
"v_mfma_f32_16x16x32_bf16 v[128:131], a[64:67], a[16:19], v[128:131]\n" \
"ds_read_b64_tr_b16 a[116:117], v232 offset:26112\n" \
"Lpg3894_%=:\n" \
"s_cbranch_execz Lpg6050_%=\n" \
"v_mfma_f32_16x16x32_bf16 v[128:131], a[68:71], a[20:23], v[128:131]\n" \
"ds_read_b64_tr_b16 a[118:119], v232 offset:28160\n" \
"v_mfma_f32_16x16x32_bf16 v[132:135], a[72:75], a[16:19], v[132:135]\n" \
"s_xor_b32 s77, s77, 0x80\n" \
"ds_read_b64_tr_b16 a[120:121], v233 offset:17920\n" \
"v_mfma_f32_16x16x32_bf16 v[132:135], a[76:79], a[20:23], v[132:135]\n" \
"v_mfma_f32_16x16x32_bf16 v[136:139], a[80:83], a[16:19], v[136:139]\n" \
"ds_read_b64_tr_b16 a[122:123], v233 offset:19968\n" \
"v_mfma_f32_16x16x32_bf16 v[136:139], a[84:87], a[20:23], v[136:139]\n" \
"ds_read_b64_tr_b16 a[124:125], v233 offset:26112\n" \
"v_mfma_f32_16x16x32_bf16 v[140:143], a[88:91], a[16:19], v[140:143]\n" \
"ds_read_b64_tr_b16 a[126:127], v233 offset:28160\n" \
"v_mfma_f32_16x16x32_bf16 v[140:143], a[92:95], a[20:23], v[140:143]\n" \
"buffer_store_dwordx4 a[228:231], v234, s[56:59], s93 offen offset:1024 sc1\n" \
"v_mfma_f32_16x16x32_bf16 v[172:175], a[88:91], a[24:27], v[172:175]\n" \
"v_mfma_f32_16x16x32_bf16 v[172:175], a[92:95], a[28:31], v[172:175]\n" \
"v_mfma_f32_16x16x32_bf16 v[168:171], a[80:83], a[24:27], v[168:171]\n" \
"v_mfma_f32_16x16x32_bf16 v[168:171], a[84:87], a[28:31], v[168:171]\n" \
"v_mfma_f32_16x16x32_bf16 v[164:167], a[72:75], a[24:27], v[164:167]\n" \
"v_mfma_f32_16x16x32_bf16 v[164:167], a[76:79], a[28:31], v[164:167]\n" \
"v_mfma_f32_16x16x32_bf16 v[160:163], a[64:67], a[24:27], v[160:163]\n" \
"v_mfma_f32_16x16x32_bf16 v[160:163], a[68:71], a[28:31], v[160:163]\n" \
"s_waitcnt vmcnt(35) lgkmcnt(0)\n" \
"s_barrier\n" \
"v_mfma_f32_16x16x32_bf16 v[80:83], a[96:99], a[0:3], v[80:83]\n" \
"ds_read_b64_tr_b16 a[64:65], v232 offset:32768\n" \
"v_mfma_f32_16x16x32_bf16 v[80:83], a[100:103], a[4:7], v[80:83]\n" \
"ds_read_b64_tr_b16 a[66:67], v232 offset:34816\n" \
"v_mfma_f32_16x16x32_bf16 v[84:87], a[104:107], a[0:3], v[84:87]\n" \
"s_add_u32 m0, s79, 16384\n" \
"ds_read_b64_tr_b16 a[68:69], v232 offset:40960\n" \
"v_mfma_f32_16x16x32_bf16 v[84:87], a[108:111], a[4:7], v[84:87]\n" \
"buffer_load_dwordx4 v228, s[52:55], s78 offen offset:0 lds\n" \
"v_mfma_f32_16x16x32_bf16 v[88:91], a[112:115], a[0:3], v[88:91]\n" \
"ds_read_b64_tr_b16 a[70:71], v232 offset:43008\n" \
"v_mfma_f32_16x16x32_bf16 v[88:91], a[116:119], a[4:7], v[88:91]\n" \
"ds_read_b64_tr_b16 a[72:73], v233 offset:32768\n" \
"v_mfma_f32_16x16x32_bf16 v[92:95], a[120:123], a[0:3], v[92:95]\n" \
"ds_read_b64_tr_b16 a[74:75], v233 offset:34816\n" \
"v_mfma_f32_16x16x32_bf16 v[92:95], a[124:127], a[4:7], v[92:95]\n" \
"buffer_load_dwordx4 v229, s[52:55], s78 offen offset:1024 lds\n" \
"v_mfma_f32_16x16x32_bf16 v[124:127], a[120:123], a[8:11], v[124:127]\n" \
"s_waitcnt vmcnt(35)\n" \
"ds_read_b128 a[0:3], v230 offset:0\n" \
"v_mfma_f32_16x16x32_bf16 v[124:127], a[124:127], a[12:15], v[124:127]\n" \
"ds_read_b128 a[4:7], v231 offset:0\n" \
"v_mfma_f32_16x16x32_bf16 v[120:123], a[112:115], a[8:11], v[120:123]\n" \
"s_xor_b32 s78, s78, 0x180000\n" \
"ds_read_b64_tr_b16 a[76:77], v233 offset:40960\n" \
"v_mfma_f32_16x16x32_bf16 v[120:123], a[116:119], a[12:15], v[120:123]\n" \
"buffer_load_dwordx4 v228, s[52:55], s89 offen offset:2048 lds\n" \
"v_mfma_f32_16x16x32_bf16 v[116:119], a[104:107], a[8:11], v[116:119]\n" \
"ds_read_b64_tr_b16 a[78:79], v233 offset:43008\n" \
"v_mfma_f32_16x16x32_bf16 v[116:119], a[108:111], a[12:15], v[116:119]\n" \
"ds_read_b64_tr_b16 a[80:81], v232 offset:33280\n" \
"v_mfma_f32_16x16x32_bf16 v[112:115], a[96:99], a[8:11], v[112:115]\n" \
"ds_read_b64_tr_b16 a[82:83], v232 offset:35328\n" \
"v_mfma_f32_16x16x32_bf16 v[112:115], a[100:103], a[12:15], v[112:115]\n" \
"buffer_load_dwordx4 v229, s[52:55], s89 offen offset:3072 lds\n" \
"v_mfma_f32_16x16x32_bf16 v[144:147], a[96:99], a[16:19], v[144:147]\n" \
"s_waitcnt vmcnt(35)\n" \
"s_xor_b32 s89, s89, 0x180000\n" \
"ds_read_b128 a[8:11], v230 offset:2048\n" \
"v_mfma_f32_16x16x32_bf16 v[144:147], a[100:103], a[20:23], v[144:147]\n" \
"s_add_u32 m0, s74, 65536\n" \
"ds_read_b128 a[12:15], v231 offset:2048\n" \
"v_mfma_f32_16x16x32_bf16 v[148:151], a[104:107], a[16:19], v[148:151]\n" \
"s_waitcnt lgkmcnt(6)\n" \
"ds_read_b64_tr_b16 a[84:85], v232 offset:41472\n" \
"v_mfma_f32_16x16x32_bf16 v[148:151], a[108:111], a[20:23], v[148:151]\n" \
"buffer_load_dwordx4 v224, s[48:51], s75 offen offset:0 lds\n" \
"v_mfma_f32_16x16x32_bf16 v[152:155], a[112:115], a[16:19], v[152:155]\n" \
"ds_read_b64_tr_b16 a[86:87], v232 offset:43520\n" \
"v_mfma_f32_16x16x32_bf16 v[152:155], a[116:119], a[20:23], v[152:155]\n" \
"ds_read_b64_tr_b16 a[88:89], v233 offset:33280\n" \
"v_mfma_f32_16x16x32_bf16 v[156:159], a[120:123], a[16:19], v[156:159]\n" \
"ds_read_b64_tr_b16 a[90:91], v233 offset:35328\n" \
"v_mfma_f32_16x16x32_bf16 v[156:159], a[124:127], a[20:23], v[156:159]\n" \
"buffer_load_dwordx4 v225, s[48:51], s75 offen offset:1024 lds\n" \
"v_mfma_f32_16x16x32_bf16 v[188:191], a[120:123], a[24:27], v[188:191]\n" \
"s_waitcnt vmcnt(35)\n" \
"ds_read_b128 a[16:19], v230 offset:4096\n" \
"v_mfma_f32_16x16x32_bf16 v[188:191], a[124:127], a[28:31], v[188:191]\n" \
"ds_read_b128 a[20:23], v231 offset:4096\n" \
"v_mfma_f32_16x16x32_bf16 v[184:187], a[112:115], a[24:27], v[184:187]\n" \
"s_waitcnt lgkmcnt(6)\n" \
"ds_read_b64_tr_b16 a[92:93], v233 offset:41472\n" \
"v_mfma_f32_16x16x32_bf16 v[184:187], a[116:119], a[28:31], v[184:187]\n" \
"buffer_load_dwordx4 v226, s[48:51], s75 offen offset:2048 lds\n" \
"v_mfma_f32_16x16x32_bf16 v[180:183], a[104:107], a[24:27], v[180:183]\n" \
"ds_read_b64_tr_b16 a[94:95], v233 offset:43520\n" \
"v_mfma_f32_16x16x32_bf16 v[180:183], a[108:111], a[28:31], v[180:183]\n" \
"v_mfma_f32_16x16x32_bf16 v[176:179], a[96:99], a[24:27], v[176:179]\n" \
"v_mfma_f32_16x16x32_bf16 v[176:179], a[100:103], a[28:31], v[176:179]\n" \
"buffer_load_dwordx4 v227, s[48:51], s75 offen offset:3072 lds\n" \
"v_mfma_f32_16x16x32_bf16 v[64:67], a[64:67], a[0:3], v[64:67]\n" \
"s_waitcnt vmcnt(35)\n" \
"s_xor_b32 s75, s75, 0x180\n" \
"ds_read_b128 a[24:27], v230 offset:6144\n" \
"v_mfma_f32_16x16x32_bf16 v[64:67], a[68:71], a[4:7], v[64:67]\n" \
"s_add_u32 m0, s74, 69632\n" \
"ds_read_b128 a[28:31], v231 offset:6144\n" \
"v_mfma_f32_16x16x32_bf16 v[68:71], a[72:75], a[0:3], v[68:71]\n" \
"s_waitcnt lgkmcnt(2)\n" \
"ds_read_b64_tr_b16 a[96:97], v232 offset:33792\n" \
"v_mfma_f32_16x16x32_bf16 v[68:71], a[76:79], a[4:7], v[68:71]\n" \
"buffer_load_dwordx4 v224, s[48:51], s77 offen offset:0 lds\n" \
"v_mfma_f32_16x16x32_bf16 v[72:75], a[80:83], a[0:3], v[72:75]\n" \
"ds_read_b64_tr_b16 a[98:99], v232 offset:35840\n" \
"v_mfma_f32_16x16x32_bf16 v[72:75], a[84:87], a[4:7], v[72:75]\n" \
"ds_read_b64_tr_b16 a[100:101], v232 offset:41984\n" \
"v_mfma_f32_16x16x32_bf16 v[76:79], a[88:91], a[0:3], v[76:79]\n" \
"ds_read_b64_tr_b16 a[102:103], v232 offset:44032\n" \
"v_mfma_f32_16x16x32_bf16 v[76:79], a[92:95], a[4:7], v[76:79]\n" \
"buffer_load_dwordx4 v225, s[48:51], s77 offen offset:1024 lds\n" \
"v_mfma_f32_16x16x32_bf16 v[108:111], a[88:91], a[8:11], v[108:111]\n" \
"ds_read_b64_tr_b16 a[104:105], v233 offset:33792\n" \
"v_mfma_f32_16x16x32_bf16 v[108:111], a[92:95], a[12:15], v[108:111]\n" \
"ds_read_b64_tr_b16 a[106:107], v233 offset:35840\n" \
"v_mfma_f32_16x16x32_bf16 v[104:107], a[80:83], a[8:11], v[104:107]\n" \
"s_waitcnt lgkmcnt(6)\n" \
"ds_read_b64_tr_b16 a[108:109], v233 offset:41984\n" \
"v_mfma_f32_16x16x32_bf16 v[104:107], a[84:87], a[12:15], v[104:107]\n" \
"buffer_load_dwordx4 v226, s[48:51], s77 offen offset:2048 lds\n" \
"v_mfma_f32_16x16x32_bf16 v[100:103], a[72:75], a[8:11], v[100:103]\n" \
"ds_read_b64_tr_b16 a[110:111], v233 offset:44032\n" \
"v_mfma_f32_16x16x32_bf16 v[100:103], a[76:79], a[12:15], v[100:103]\n" \
"ds_read_b64_tr_b16 a[112:113], v232 offset:34304\n" \
"v_mfma_f32_16x16x32_bf16 v[96:99], a[64:67], a[8:11], v[96:99]\n" \
"ds_read_b64_tr_b16 a[114:115], v232 offset:36352\n" \
"v_mfma_f32_16x16x32_bf16 v[96:99], a[68:71], a[12:15], v[96:99]\n" \
"buffer_load_dwordx4 v227, s[48:51], s77 offen offset:3072 lds\n" \
"v_mfma_f32_16x16x32_bf16 v[128:131], a[64:67], a[16:19], v[128:131]\n" \
"ds_read_b64_tr_b16 a[116:117], v232 offset:42496\n" \
"v_mfma_f32_16x16x32_bf16 v[128:131], a[68:71], a[20:23], v[128:131]\n" \
"ds_read_b64_tr_b16 a[118:119], v232 offset:44544\n" \
"v_mfma_f32_16x16x32_bf16 v[132:135], a[72:75], a[16:19], v[132:135]\n" \
"s_xor_b32 s77, s77, 0x180\n" \
"ds_read_b64_tr_b16 a[120:121], v233 offset:34304\n" \
"v_mfma_f32_16x16x32_bf16 v[132:135], a[76:79], a[20:23], v[132:135]\n" \
"buffer_store_dwordx4 a[232:235], v234, s[56:59], s93 offen offset:2048 sc1\n" \
"v_mfma_f32_16x16x32_bf16 v[136:139], a[80:83], a[16:19], v[136:139]\n" \
"ds_read_b64_tr_b16 a[122:123], v233 offset:36352\n" \
"v_mfma_f32_16x16x32_bf16 v[136:139], a[84:87], a[20:23], v[136:139]\n" \
"ds_read_b64_tr_b16 a[124:125], v233 offset:42496\n" \
"v_mfma_f32_16x16x32_bf16 v[140:143], a[88:91], a[16:19], v[140:143]\n" \
"ds_read_b64_tr_b16 a[126:127], v233 offset:44544\n" \
"v_mfma_f32_16x16x32_bf16 v[140:143], a[92:95], a[20:23], v[140:143]\n" \
"v_mfma_f32_16x16x32_bf16 v[172:175], a[88:91], a[24:27], v[172:175]\n" \
"v_mfma_f32_16x16x32_bf16 v[172:175], a[92:95], a[28:31], v[172:175]\n" \
"v_mfma_f32_16x16x32_bf16 v[168:171], a[80:83], a[24:27], v[168:171]\n" \
"v_mfma_f32_16x16x32_bf16 v[168:171], a[84:87], a[28:31], v[168:171]\n" \
"buffer_store_dwordx4 a[236:239], v234, s[56:59], s93 offen offset:3072 sc1\n" \
"v_mfma_f32_16x16x32_bf16 v[164:167], a[72:75], a[24:27], v[164:167]\n" \
"v_mfma_f32_16x16x32_bf16 v[164:167], a[76:79], a[28:31], v[164:167]\n" \
"v_mfma_f32_16x16x32_bf16 v[160:163], a[64:67], a[24:27], v[160:163]\n" \
"v_mfma_f32_16x16x32_bf16 v[160:163], a[68:71], a[28:31], v[160:163]\n" \
"s_waitcnt vmcnt(36) lgkmcnt(0)\n" \
"s_barrier\n" \
"v_mfma_f32_16x16x32_bf16 v[80:83], a[96:99], a[0:3], v[80:83]\n" \
"ds_read_b64_tr_b16 a[64:65], v232 offset:49152\n" \
"v_mfma_f32_16x16x32_bf16 v[80:83], a[100:103], a[4:7], v[80:83]\n" \
"ds_read_b64_tr_b16 a[66:67], v232 offset:51200\n" \
"v_mfma_f32_16x16x32_bf16 v[84:87], a[104:107], a[0:3], v[84:87]\n" \
"s_add_u32 m0, s79, 32768\n" \
"ds_read_b64_tr_b16 a[68:69], v232 offset:57344\n" \
"v_mfma_f32_16x16x32_bf16 v[84:87], a[108:111], a[4:7], v[84:87]\n" \
"buffer_load_dwordx4 v228, s[52:55], s78 offen offset:0 lds\n" \
"v_mfma_f32_16x16x32_bf16 v[88:91], a[112:115], a[0:3], v[88:91]\n" \
"ds_read_b64_tr_b16 a[70:71], v232 offset:59392\n" \
"v_mfma_f32_16x16x32_bf16 v[88:91], a[116:119], a[4:7], v[88:91]\n" \
"ds_read_b64_tr_b16 a[72:73], v233 offset:49152\n" \
"v_mfma_f32_16x16x32_bf16 v[92:95], a[120:123], a[0:3], v[92:95]\n" \
"ds_read_b64_tr_b16 a[74:75], v233 offset:51200\n" \
"v_mfma_f32_16x16x32_bf16 v[92:95], a[124:127], a[4:7], v[92:95]\n" \
"buffer_load_dwordx4 v229, s[52:55], s78 offen offset:1024 lds\n" \
"v_mfma_f32_16x16x32_bf16 v[124:127], a[120:123], a[8:11], v[124:127]\n" \
"s_waitcnt vmcnt(36)\n" \
"ds_read_b128 a[0:3], v230 offset:32768\n" \
"v_mfma_f32_16x16x32_bf16 v[124:127], a[124:127], a[12:15], v[124:127]\n" \
"ds_read_b128 a[4:7], v231 offset:32768\n" \
"v_mfma_f32_16x16x32_bf16 v[120:123], a[112:115], a[8:11], v[120:123]\n" \
"s_xor_b32 s78, s78, 0x80000\n" \
"ds_read_b64_tr_b16 a[76:77], v233 offset:57344\n" \
"v_mfma_f32_16x16x32_bf16 v[120:123], a[116:119], a[12:15], v[120:123]\n" \
"buffer_load_dwordx4 v228, s[52:55], s89 offen offset:2048 lds\n" \
"v_mfma_f32_16x16x32_bf16 v[116:119], a[104:107], a[8:11], v[116:119]\n" \
"ds_read_b64_tr_b16 a[78:79], v233 offset:59392\n" \
"v_mfma_f32_16x16x32_bf16 v[116:119], a[108:111], a[12:15], v[116:119]\n" \
"ds_read_b64_tr_b16 a[80:81], v232 offset:49664\n" \
"v_mfma_f32_16x16x32_bf16 v[112:115], a[96:99], a[8:11], v[112:115]\n" \
"ds_read_b64_tr_b16 a[82:83], v232 offset:51712\n" \
"v_mfma_f32_16x16x32_bf16 v[112:115], a[100:103], a[12:15], v[112:115]\n" \
"buffer_load_dwordx4 v229, s[52:55], s89 offen offset:3072 lds\n" \
"v_mfma_f32_16x16x32_bf16 v[144:147], a[96:99], a[16:19], v[144:147]\n" \
"s_waitcnt vmcnt(36)\n" \
"s_xor_b32 s89, s89, 0x80000\n" \
"ds_read_b128 a[8:11], v230 offset:34816\n" \
"v_mfma_f32_16x16x32_bf16 v[144:147], a[100:103], a[20:23], v[144:147]\n" \
"s_add_u32 m0, s74, 98304\n" \
"ds_read_b128 a[12:15], v231 offset:34816\n" \
"v_mfma_f32_16x16x32_bf16 v[148:151], a[104:107], a[16:19], v[148:151]\n" \
"s_waitcnt lgkmcnt(6)\n" \
"ds_read_b64_tr_b16 a[84:85], v232 offset:57856\n" \
"v_mfma_f32_16x16x32_bf16 v[148:151], a[108:111], a[20:23], v[148:151]\n" \
"buffer_load_dwordx4 v224, s[48:51], s75 offen offset:0 lds\n" \
"v_mfma_f32_16x16x32_bf16 v[152:155], a[112:115], a[16:19], v[152:155]\n" \
"ds_read_b64_tr_b16 a[86:87], v232 offset:59904\n" \
"v_mfma_f32_16x16x32_bf16 v[152:155], a[116:119], a[20:23], v[152:155]\n" \
"ds_read_b64_tr_b16 a[88:89], v233 offset:49664\n" \
"v_mfma_f32_16x16x32_bf16 v[156:159], a[120:123], a[16:19], v[156:159]\n" \
"ds_read_b64_tr_b16 a[90:91], v233 offset:51712\n" \
"v_mfma_f32_16x16x32_bf16 v[156:159], a[124:127], a[20:23], v[156:159]\n" \
"buffer_load_dwordx4 v225, s[48:51], s75 offen offset:1024 lds\n" \
"v_mfma_f32_16x16x32_bf16 v[188:191], a[120:123], a[24:27], v[188:191]\n" \
"s_waitcnt vmcnt(36)\n" \
"ds_read_b128 a[16:19], v230 offset:36864\n" \
"v_mfma_f32_16x16x32_bf16 v[188:191], a[124:127], a[28:31], v[188:191]\n" \
"ds_read_b128 a[20:23], v231 offset:36864\n" \
"v_mfma_f32_16x16x32_bf16 v[184:187], a[112:115], a[24:27], v[184:187]\n" \
"s_waitcnt lgkmcnt(6)\n" \
"ds_read_b64_tr_b16 a[92:93], v233 offset:57856\n" \
"v_mfma_f32_16x16x32_bf16 v[184:187], a[116:119], a[28:31], v[184:187]\n" \
"buffer_load_dwordx4 v226, s[48:51], s75 offen offset:2048 lds\n" \
"v_mfma_f32_16x16x32_bf16 v[180:183], a[104:107], a[24:27], v[180:183]\n" \
"ds_read_b64_tr_b16 a[94:95], v233 offset:59904\n" \
"v_mfma_f32_16x16x32_bf16 v[180:183], a[108:111], a[28:31], v[180:183]\n" \
"v_mfma_f32_16x16x32_bf16 v[176:179], a[96:99], a[24:27], v[176:179]\n" \
"v_mfma_f32_16x16x32_bf16 v[176:179], a[100:103], a[28:31], v[176:179]\n" \
"buffer_load_dwordx4 v227, s[48:51], s75 offen offset:3072 lds\n" \
"v_mfma_f32_16x16x32_bf16 v[64:67], a[64:67], a[0:3], v[64:67]\n" \
"s_waitcnt vmcnt(36)\n" \
"s_xor_b32 s75, s75, 0x80\n" \
"ds_read_b128 a[24:27], v230 offset:38912\n" \
"v_mfma_f32_16x16x32_bf16 v[64:67], a[68:71], a[4:7], v[64:67]\n" \
"s_add_u32 m0, s74, 102400\n" \
"ds_read_b128 a[28:31], v231 offset:38912\n" \
"v_mfma_f32_16x16x32_bf16 v[68:71], a[72:75], a[0:3], v[68:71]\n" \
"s_waitcnt lgkmcnt(2)\n" \
"ds_read_b64_tr_b16 a[96:97], v232 offset:50176\n" \
"v_mfma_f32_16x16x32_bf16 v[68:71], a[76:79], a[4:7], v[68:71]\n" \
"buffer_load_dwordx4 v224, s[48:51], s77 offen offset:0 lds\n" \
"v_mfma_f32_16x16x32_bf16 v[72:75], a[80:83], a[0:3], v[72:75]\n" \
"ds_read_b64_tr_b16 a[98:99], v232 offset:52224\n" \
"v_mfma_f32_16x16x32_bf16 v[72:75], a[84:87], a[4:7], v[72:75]\n" \
"ds_read_b64_tr_b16 a[100:101], v232 offset:58368\n" \
"v_mfma_f32_16x16x32_bf16 v[76:79], a[88:91], a[0:3], v[76:79]\n" \
"ds_read_b64_tr_b16 a[102:103], v232 offset:60416\n" \
"v_mfma_f32_16x16x32_bf16 v[76:79], a[92:95], a[4:7], v[76:79]\n" \
"buffer_load_dwordx4 v225, s[48:51], s77 offen offset:1024 lds\n" \
"v_mfma_f32_16x16x32_bf16 v[108:111], a[88:91], a[8:11], v[108:111]\n" \
"ds_read_b64_tr_b16 a[104:105], v233 offset:50176\n" \
"v_mfma_f32_16x16x32_bf16 v[108:111], a[92:95], a[12:15], v[108:111]\n" \
"ds_read_b64_tr_b16 a[106:107], v233 offset:52224\n" \
"v_mfma_f32_16x16x32_bf16 v[104:107], a[80:83], a[8:11], v[104:107]\n" \
"s_waitcnt lgkmcnt(6)\n" \
"ds_read_b64_tr_b16 a[108:109], v233 offset:58368\n" \
"v_mfma_f32_16x16x32_bf16 v[104:107], a[84:87], a[12:15], v[104:107]\n" \
"buffer_load_dwordx4 v226, s[48:51], s77 offen offset:2048 lds\n" \
"v_mfma_f32_16x16x32_bf16 v[100:103], a[72:75], a[8:11], v[100:103]\n" \
"ds_read_b64_tr_b16 a[110:111], v233 offset:60416\n" \
"v_mfma_f32_16x16x32_bf16 v[100:103], a[76:79], a[12:15], v[100:103]\n" \
"ds_read_b64_tr_b16 a[112:113], v232 offset:50688\n" \
"v_mfma_f32_16x16x32_bf16 v[96:99], a[64:67], a[8:11], v[96:99]\n" \
"ds_read_b64_tr_b16 a[114:115], v232 offset:52736\n" \
"v_mfma_f32_16x16x32_bf16 v[96:99], a[68:71], a[12:15], v[96:99]\n" \
"buffer_load_dwordx4 v227, s[48:51], s77 offen offset:3072 lds\n" \
"v_mfma_f32_16x16x32_bf16 v[128:131], a[64:67], a[16:19], v[128:131]\n" \
"ds_read_b64_tr_b16 a[116:117], v232 offset:58880\n" \
"v_mfma_f32_16x16x32_bf16 v[128:131], a[68:71], a[20:23], v[128:131]\n" \
"ds_read_b64_tr_b16 a[118:119], v232 offset:60928\n" \
"v_mfma_f32_16x16x32_bf16 v[132:135], a[72:75], a[16:19], v[132:135]\n" \
"s_xor_b32 s77, s77, 0x80\n" \
"s_add_u32 s93, s87, 17850368\n" \
"ds_read_b64_tr_b16 a[120:121], v233 offset:50688\n" \
"v_mfma_f32_16x16x32_bf16 v[132:135], a[76:79], a[20:23], v[132:135]\n" \
"buffer_store_dwordx4 a[192:195], v234, s[56:59], s93 offen offset:0 sc1\n" \
"v_mfma_f32_16x16x32_bf16 v[136:139], a[80:83], a[16:19], v[136:139]\n" \
"ds_read_b64_tr_b16 a[122:123], v233 offset:52736\n" \
"v_mfma_f32_16x16x32_bf16 v[136:139], a[84:87], a[20:23], v[136:139]\n" \
"ds_read_b64_tr_b16 a[124:125], v233 offset:58880\n" \
"v_mfma_f32_16x16x32_bf16 v[140:143], a[88:91], a[16:19], v[140:143]\n" \
"ds_read_b64_tr_b16 a[126:127], v233 offset:60928\n" \
"v_mfma_f32_16x16x32_bf16 v[140:143], a[92:95], a[20:23], v[140:143]\n" \
"v_mfma_f32_16x16x32_bf16 v[172:175], a[88:91], a[24:27], v[172:175]\n" \
"v_mfma_f32_16x16x32_bf16 v[172:175], a[92:95], a[28:31], v[172:175]\n" \
"v_mfma_f32_16x16x32_bf16 v[168:171], a[80:83], a[24:27], v[168:171]\n" \
"v_mfma_f32_16x16x32_bf16 v[168:171], a[84:87], a[28:31], v[168:171]\n" \
"buffer_store_dwordx4 a[196:199], v234, s[56:59], s93 offen offset:1024 sc1\n" \
"v_mfma_f32_16x16x32_bf16 v[164:167], a[72:75], a[24:27], v[164:167]\n" \
"v_mfma_f32_16x16x32_bf16 v[164:167], a[76:79], a[28:31], v[164:167]\n" \
"v_mfma_f32_16x16x32_bf16 v[160:163], a[64:67], a[24:27], v[160:163]\n" \
"v_mfma_f32_16x16x32_bf16 v[160:163], a[68:71], a[28:31], v[160:163]\n" \
"s_waitcnt vmcnt(37) lgkmcnt(0)\n" \
"s_barrier\n" \
"v_mfma_f32_16x16x32_bf16 v[80:83], a[96:99], a[0:3], v[80:83]\n" \
"ds_read_b64_tr_b16 a[64:65], v232 offset:0\n" \
"v_mfma_f32_16x16x32_bf16 v[80:83], a[100:103], a[4:7], v[80:83]\n" \
"ds_read_b64_tr_b16 a[66:67], v232 offset:2048\n" \
"v_mfma_f32_16x16x32_bf16 v[84:87], a[104:107], a[0:3], v[84:87]\n" \
"s_add_u32 m0, s79, 49152\n" \
"ds_read_b64_tr_b16 a[68:69], v232 offset:8192\n" \
"v_mfma_f32_16x16x32_bf16 v[84:87], a[108:111], a[4:7], v[84:87]\n" \
"buffer_load_dwordx4 v228, s[52:55], s78 offen offset:0 lds\n" \
"v_mfma_f32_16x16x32_bf16 v[88:91], a[112:115], a[0:3], v[88:91]\n" \
"s_mul_i32 s73, s62, 557824\n" \
"s_add_u32 s85, s73, 17850368\n" \
"ds_read_b64_tr_b16 a[70:71], v232 offset:10240\n" \
"v_mfma_f32_16x16x32_bf16 v[88:91], a[116:119], a[4:7], v[88:91]\n" \
"s_mul_i32 s83, s63, 34816\n" \
"s_sub_u32 s86, 557056, s83\n" \
"ds_read_b64_tr_b16 a[72:73], v233 offset:0\n" \
"v_mfma_f32_16x16x32_bf16 v[92:95], a[120:123], a[0:3], v[92:95]\n" \
"ds_read_b64_tr_b16 a[74:75], v233 offset:2048\n" \
"v_add_u32 v237, %3, v236\n" \
"v_mfma_f32_16x16x32_bf16 v[92:95], a[124:127], a[4:7], v[92:95]\n" \
"buffer_load_dwordx4 v229, s[52:55], s78 offen offset:1024 lds\n" \
"v_mfma_f32_16x16x32_bf16 v[124:127], a[120:123], a[8:11], v[124:127]\n" \
"s_waitcnt vmcnt(37)\n" \
"ds_read_b128 a[0:3], v250 offset:0\n" \
"v_mfma_f32_16x16x32_bf16 v[124:127], a[124:127], a[12:15], v[124:127]\n" \
"s_add_u32 s36, s56, s73\n" \
"s_addc_u32 s37, s57, 0\n" \
"ds_read_b128 a[4:7], v251 offset:0\n" \
"v_mfma_f32_16x16x32_bf16 v[120:123], a[112:115], a[8:11], v[120:123]\n" \
"s_mov_b32 s38, s86\n" \
"s_mov_b32 s39, 0x20000\n" \
"ds_read_b64_tr_b16 a[76:77], v233 offset:8192\n" \
"v_mfma_f32_16x16x32_bf16 v[120:123], a[116:119], a[12:15], v[120:123]\n" \
"buffer_load_dwordx4 v228, s[52:55], s89 offen offset:2048 lds\n" \
"v_mfma_f32_16x16x32_bf16 v[116:119], a[104:107], a[8:11], v[116:119]\n" \
"ds_read_b64_tr_b16 a[78:79], v233 offset:10240\n" \
"v_add_u32 v235, %3, v234\n" \
"v_mfma_f32_16x16x32_bf16 v[116:119], a[108:111], a[12:15], v[116:119]\n" \
"s_add_u32 s40, s56, s85\n" \
"s_addc_u32 s41, s57, 0\n" \
"ds_read_b64_tr_b16 a[80:81], v232 offset:512\n" \
"v_mfma_f32_16x16x32_bf16 v[112:115], a[96:99], a[8:11], v[112:115]\n" \
"s_mov_b32 s42, s86\n" \
"s_mov_b32 s43, 0x20000\n" \
"ds_read_b64_tr_b16 a[82:83], v232 offset:2560\n" \
"v_mfma_f32_16x16x32_bf16 v[112:115], a[100:103], a[12:15], v[112:115]\n" \
"buffer_load_dwordx4 v229, s[52:55], s89 offen offset:3072 lds\n" \
"v_mfma_f32_16x16x32_bf16 v[144:147], a[96:99], a[16:19], v[144:147]\n" \
"s_waitcnt vmcnt(37)\n" \
"s_add_u32 s85, s74, 131072\n" \
"ds_read_b128 a[8:11], v250 offset:2048\n" \
"v_mfma_f32_16x16x32_bf16 v[144:147], a[100:103], a[20:23], v[144:147]\n" \
"s_add_u32 m0, s74, 131072\n" \
"ds_read_b128 a[12:15], v251 offset:2048\n" \
"v_add_u32 v239, s85, v234\n" \
"v_mfma_f32_16x16x32_bf16 v[148:151], a[104:107], a[16:19], v[148:151]\n" \
"s_waitcnt lgkmcnt(6)\n" \
"s_lshl_b32 s73, s61, 12\n" \
"ds_read_b64_tr_b16 a[84:85], v232 offset:8704\n" \
"v_mfma_f32_16x16x32_bf16 v[148:151], a[108:111], a[20:23], v[148:151]\n" \
"buffer_load_dwordx4 v224, s[48:51], s75 offen offset:0 lds\n" \
"v_mfma_f32_16x16x32_bf16 v[152:155], a[112:115], a[16:19], v[152:155]\n" \
"s_add_u32 s80, s79, 0x10000\n" \
"s_add_u32 s83, s83, s73\n" \
"ds_read_b64_tr_b16 a[86:87], v232 offset:10752\n" \
"v_mfma_f32_16x16x32_bf16 v[152:155], a[116:119], a[20:23], v[152:155]\n" \
"s_cmp_eq_u32 s63, 0\n" \
"s_cselect_b32 s85, 34816, 0\n" \
"ds_read_b64_tr_b16 a[88:89], v233 offset:512\n" \
"v_mfma_f32_16x16x32_bf16 v[156:159], a[120:123], a[16:19], v[156:159]\n" \
"s_add_u32 s73, s73, s85\n" \
"ds_read_b64_tr_b16 a[90:91], v233 offset:2560\n" \
"v_mfma_f32_16x16x32_bf16 v[156:159], a[124:127], a[20:23], v[156:159]\n" \
"buffer_load_dwordx4 v225, s[48:51], s75 offen offset:1024 lds\n" \
"v_mfma_f32_16x16x32_bf16 v[188:191], a[120:123], a[24:27], v[188:191]\n" \
"s_waitcnt vmcnt(37)\n" \
"ds_read_b128 a[16:19], v250 offset:4096\n" \
"v_mfma_f32_16x16x32_bf16 v[188:191], a[124:127], a[28:31], v[188:191]\n" \
"ds_read_b128 a[20:23], v251 offset:4096\n" \
"v_mfma_f32_16x16x32_bf16 v[184:187], a[112:115], a[24:27], v[184:187]\n" \
"s_waitcnt lgkmcnt(6)\n" \
"ds_read_b64_tr_b16 a[92:93], v233 offset:8704\n" \
"v_mfma_f32_16x16x32_bf16 v[184:187], a[116:119], a[28:31], v[184:187]\n" \
"buffer_load_dwordx4 v226, s[48:51], s75 offen offset:2048 lds\n" \
"v_mfma_f32_16x16x32_bf16 v[180:183], a[104:107], a[24:27], v[180:183]\n" \
"ds_read_b64_tr_b16 a[94:95], v233 offset:10752\n" \
"v_mfma_f32_16x16x32_bf16 v[180:183], a[108:111], a[28:31], v[180:183]\n" \
"v_mfma_f32_16x16x32_bf16 v[176:179], a[96:99], a[24:27], v[176:179]\n" \
"v_mfma_f32_16x16x32_bf16 v[176:179], a[100:103], a[28:31], v[176:179]\n" \
"buffer_load_dwordx4 v227, s[48:51], s75 offen offset:3072 lds\n" \
"v_mfma_f32_16x16x32_bf16 v[64:67], a[64:67], a[0:3], v[64:67]\n" \
"s_waitcnt vmcnt(37)\n" \
"ds_read_b128 a[24:27], v250 offset:6144\n" \
"v_mfma_f32_16x16x32_bf16 v[64:67], a[68:71], a[4:7], v[64:67]\n" \
"s_add_u32 m0, s74, 135168\n" \
"ds_read_b128 a[28:31], v251 offset:6144\n" \
"v_mfma_f32_16x16x32_bf16 v[68:71], a[72:75], a[0:3], v[68:71]\n" \
"s_waitcnt lgkmcnt(2)\n" \
"ds_read_b64_tr_b16 a[96:97], v232 offset:1024\n" \
"v_mfma_f32_16x16x32_bf16 v[68:71], a[76:79], a[4:7], v[68:71]\n" \
"buffer_load_dwordx4 v224, s[48:51], s77 offen offset:0 lds\n" \
"v_mfma_f32_16x16x32_bf16 v[72:75], a[80:83], a[0:3], v[72:75]\n" \
"ds_read_b64_tr_b16 a[98:99], v232 offset:3072\n" \
"v_mfma_f32_16x16x32_bf16 v[72:75], a[84:87], a[4:7], v[72:75]\n" \
"ds_read_b64_tr_b16 a[100:101], v232 offset:9216\n" \
"v_mfma_f32_16x16x32_bf16 v[76:79], a[88:91], a[0:3], v[76:79]\n" \
"ds_read_b64_tr_b16 a[102:103], v232 offset:11264\n" \
"v_mfma_f32_16x16x32_bf16 v[76:79], a[92:95], a[4:7], v[76:79]\n" \
"buffer_load_dwordx4 v225, s[48:51], s77 offen offset:1024 lds\n" \
"v_mfma_f32_16x16x32_bf16 v[108:111], a[88:91], a[8:11], v[108:111]\n" \
"ds_read_b64_tr_b16 a[104:105], v233 offset:1024\n" \
"v_mfma_f32_16x16x32_bf16 v[108:111], a[92:95], a[12:15], v[108:111]\n" \
"ds_read_b64_tr_b16 a[106:107], v233 offset:3072\n" \
"v_mfma_f32_16x16x32_bf16 v[104:107], a[80:83], a[8:11], v[104:107]\n" \
"s_waitcnt lgkmcnt(6)\n" \
"ds_read_b64_tr_b16 a[108:109], v233 offset:9216\n" \
"v_mfma_f32_16x16x32_bf16 v[104:107], a[84:87], a[12:15], v[104:107]\n" \
"buffer_load_dwordx4 v226, s[48:51], s77 offen offset:2048 lds\n" \
"v_mfma_f32_16x16x32_bf16 v[100:103], a[72:75], a[8:11], v[100:103]\n" \
"ds_read_b64_tr_b16 a[110:111], v233 offset:11264\n" \
"v_mfma_f32_16x16x32_bf16 v[100:103], a[76:79], a[12:15], v[100:103]\n" \
"ds_read_b64_tr_b16 a[112:113], v232 offset:1536\n" \
"v_mfma_f32_16x16x32_bf16 v[96:99], a[64:67], a[8:11], v[96:99]\n" \
"ds_read_b64_tr_b16 a[114:115], v232 offset:3584\n" \
"v_mfma_f32_16x16x32_bf16 v[96:99], a[68:71], a[12:15], v[96:99]\n" \
"buffer_load_dwordx4 v227, s[48:51], s77 offen offset:3072 lds\n" \
"v_mfma_f32_16x16x32_bf16 v[128:131], a[64:67], a[16:19], v[128:131]\n" \
"ds_read_b64_tr_b16 a[116:117], v232 offset:9728\n" \
"v_mfma_f32_16x16x32_bf16 v[128:131], a[68:71], a[20:23], v[128:131]\n" \
"ds_read_b64_tr_b16 a[118:119], v232 offset:11776\n" \
"v_mfma_f32_16x16x32_bf16 v[132:135], a[72:75], a[16:19], v[132:135]\n" \
"ds_read_b64_tr_b16 a[120:121], v233 offset:1536\n" \
"v_mfma_f32_16x16x32_bf16 v[132:135], a[76:79], a[20:23], v[132:135]\n" \
"buffer_store_dwordx4 a[200:203], v234, s[56:59], s93 offen offset:2048 sc1\n" \
"v_mfma_f32_16x16x32_bf16 v[136:139], a[80:83], a[16:19], v[136:139]\n" \
"ds_read_b64_tr_b16 a[122:123], v233 offset:3584\n" \
"v_mfma_f32_16x16x32_bf16 v[136:139], a[84:87], a[20:23], v[136:139]\n" \
"ds_read_b64_tr_b16 a[124:125], v233 offset:9728\n" \
"v_mfma_f32_16x16x32_bf16 v[140:143], a[88:91], a[16:19], v[140:143]\n" \
"ds_read_b64_tr_b16 a[126:127], v233 offset:11776\n" \
"v_mfma_f32_16x16x32_bf16 v[140:143], a[92:95], a[20:23], v[140:143]\n" \
"v_mfma_f32_16x16x32_bf16 v[172:175], a[88:91], a[24:27], v[172:175]\n" \
"v_mfma_f32_16x16x32_bf16 v[172:175], a[92:95], a[28:31], v[172:175]\n" \
"v_mfma_f32_16x16x32_bf16 v[168:171], a[80:83], a[24:27], v[168:171]\n" \
"v_mfma_f32_16x16x32_bf16 v[168:171], a[84:87], a[28:31], v[168:171]\n" \
"buffer_store_dwordx4 a[204:207], v234, s[56:59], s93 offen offset:3072 sc1\n" \
"v_mfma_f32_16x16x32_bf16 v[164:167], a[72:75], a[24:27], v[164:167]\n" \
"v_mfma_f32_16x16x32_bf16 v[164:167], a[76:79], a[28:31], v[164:167]\n" \
"v_mfma_f32_16x16x32_bf16 v[160:163], a[64:67], a[24:27], v[160:163]\n" \
"v_mfma_f32_16x16x32_bf16 v[160:163], a[68:71], a[28:31], v[160:163]\n" \
"s_waitcnt vmcnt(38) lgkmcnt(0)\n" \
"s_barrier\n" \
"v_mfma_f32_16x16x32_bf16 v[80:83], a[96:99], a[0:3], v[80:83]\n" \
"s_lshr_b32 s71, s61, 1\n" \
"s_lshl_b32 s71, s71, 14\n" \
"ds_read_b64_tr_b16 a[64:65], v232 offset:16384\n" \
"v_mfma_f32_16x16x32_bf16 v[80:83], a[100:103], a[4:7], v[80:83]\n" \
"s_xor_b32 s71, s71, 0x4000\n" \
"s_and_b32 s70, s61, 1\n" \
"ds_read_b64_tr_b16 a[66:67], v232 offset:18432\n" \
"v_mfma_f32_16x16x32_bf16 v[84:87], a[104:107], a[0:3], v[84:87]\n" \
"s_lshl_b32 s70, s70, 12\n" \
"s_add_u32 s71, s71, s70\n" \
"ds_read_b64_tr_b16 a[68:69], v232 offset:24576\n" \
"v_mfma_f32_16x16x32_bf16 v[84:87], a[108:111], a[4:7], v[84:87]\n" \
"v_add_u32 v224, s71, v237\n" \
"s_and_b32 s70, s63, 1\n" \
"v_mfma_f32_16x16x32_bf16 v[88:91], a[112:115], a[0:3], v[88:91]\n" \
"s_lshl_b32 s70, s70, 15\n" \
"s_xor_b32 s70, s70, 0x18000\n" \
"ds_read_b64_tr_b16 a[70:71], v232 offset:26624\n" \
"v_mfma_f32_16x16x32_bf16 v[88:91], a[116:119], a[4:7], v[88:91]\n" \
"s_add_u32 s71, s71, s70\n" \
"ds_read_b64_tr_b16 a[72:73], v233 offset:16384\n" \
"v_add_u32 v225, s71, v235\n" \
"v_mfma_f32_16x16x32_bf16 v[92:95], a[120:123], a[0:3], v[92:95]\n" \
"s_lshr_b32 s64, s22, 7\n" \
"s_sub_u32 s64, 3, s64\n" \
"ds_read_b64_tr_b16 a[74:75], v233 offset:18432\n" \
"v_mfma_f32_16x16x32_bf16 v[92:95], a[124:127], a[4:7], v[92:95]\n" \
"s_and_b32 s64, s64, 7\n" \
"s_and_b32 s65, %1, 1\n" \
"s_lshl_b32 s65, s65, 3\n" \
"v_mfma_f32_16x16x32_bf16 v[124:127], a[120:123], a[8:11], v[124:127]\n" \
"s_waitcnt vmcnt(36)\n" \
"s_add_u32 s64, s64, s65\n" \
"ds_read_b128 a[0:3], v230 offset:0\n" \
"v_mfma_f32_16x16x32_bf16 v[124:127], a[124:127], a[12:15], v[124:127]\n" \
"s_lshl_b32 s64, s64, 7\n" \
"s_lshr_b32 s65, %1, 3\n" \
"ds_read_b128 a[4:7], v231 offset:0\n" \
"v_mfma_f32_16x16x32_bf16 v[120:123], a[112:115], a[8:11], v[120:123]\n" \
"s_lshl_b32 s65, s65, 2\n" \
"s_add_u32 s65, s65, s61\n" \
"ds_read_b64_tr_b16 a[76:77], v233 offset:24576\n" \
"v_mfma_f32_16x16x32_bf16 v[120:123], a[116:119], a[12:15], v[120:123]\n" \
"s_mul_i32 s65, s65, 24576\n" \
"s_add_u32 s64, s64, s65\n" \
"v_bfe_u32 v254, v234, 7, 2\n" \
"v_mfma_f32_16x16x32_bf16 v[116:119], a[104:107], a[8:11], v[116:119]\n" \
"ds_read_b64_tr_b16 a[78:79], v233 offset:26624\n" \
"v_mul_u32_u24 v254, 3, v254\n" \
"v_mfma_f32_16x16x32_bf16 v[116:119], a[108:111], a[12:15], v[116:119]\n" \
"ds_read_b64_tr_b16 a[80:81], v232 offset:16896\n" \
"v_bfe_u32 v243, v234, 5, 2\n" \
"v_mfma_f32_16x16x32_bf16 v[112:115], a[96:99], a[8:11], v[112:115]\n" \
"s_add_u32 s93, s94, 8192\n" \
"ds_read_b64_tr_b16 a[82:83], v232 offset:18944\n" \
"v_min_u32 v243, 2, v243\n" \
"v_mfma_f32_16x16x32_bf16 v[112:115], a[100:103], a[12:15], v[112:115]\n" \
"buffer_store_dwordx4 v[32:35], v236, s[56:59], s93 offen offset:0 sc1\n" \
"v_mfma_f32_16x16x32_bf16 v[144:147], a[96:99], a[16:19], v[144:147]\n" \
"s_waitcnt vmcnt(35)\n" \
"ds_read_b128 a[8:11], v230 offset:2048\n" \
"v_add_u32 v254, v254, v243\n" \
"v_mfma_f32_16x16x32_bf16 v[144:147], a[100:103], a[20:23], v[144:147]\n" \
"ds_read_b128 a[12:15], v231 offset:2048\n" \
"v_and_b32 v243, 16, v234\n" \
"v_mfma_f32_16x16x32_bf16 v[148:151], a[104:107], a[16:19], v[148:151]\n" \
"ds_read_b64_tr_b16 a[84:85], v232 offset:25088\n" \
"v_lshl_add_u32 v243, v243, 2, s64\n" \
"v_mfma_f32_16x16x32_bf16 v[148:151], a[108:111], a[20:23], v[148:151]\n" \
"v_lshl_add_u32 v254, v254, 11, v243\n" \
"v_mfma_f32_16x16x32_bf16 v[152:155], a[112:115], a[16:19], v[152:155]\n" \
"ds_read_b64_tr_b16 a[86:87], v232 offset:27136\n" \
"v_mfma_f32_16x16x32_bf16 v[152:155], a[116:119], a[20:23], v[152:155]\n" \
"ds_read_b64_tr_b16 a[88:89], v233 offset:16896\n" \
"v_mfma_f32_16x16x32_bf16 v[156:159], a[120:123], a[16:19], v[156:159]\n" \
"ds_read_b64_tr_b16 a[90:91], v233 offset:18944\n" \
"v_mfma_f32_16x16x32_bf16 v[156:159], a[124:127], a[20:23], v[156:159]\n" \
"v_mfma_f32_16x16x32_bf16 v[188:191], a[120:123], a[24:27], v[188:191]\n" \
"s_waitcnt vmcnt(33)\n" \
"ds_read_b128 a[16:19], v230 offset:4096\n" \
"v_mfma_f32_16x16x32_bf16 v[188:191], a[124:127], a[28:31], v[188:191]\n" \
"ds_read_b128 a[20:23], v231 offset:4096\n" \
"v_mfma_f32_16x16x32_bf16 v[184:187], a[112:115], a[24:27], v[184:187]\n" \
"ds_read_b64_tr_b16 a[92:93], v233 offset:25088\n" \
"v_mfma_f32_16x16x32_bf16 v[184:187], a[116:119], a[28:31], v[184:187]\n" \
"v_mfma_f32_16x16x32_bf16 v[180:183], a[104:107], a[24:27], v[180:183]\n" \
"ds_read_b64_tr_b16 a[94:95], v233 offset:27136\n" \
"v_mfma_f32_16x16x32_bf16 v[180:183], a[108:111], a[28:31], v[180:183]\n" \
"v_mfma_f32_16x16x32_bf16 v[176:179], a[96:99], a[24:27], v[176:179]\n" \
"buffer_store_dwordx4 v[48:51], v236, s[56:59], s93 offen offset:256 sc1\n" \
"v_mfma_f32_16x16x32_bf16 v[176:179], a[100:103], a[28:31], v[176:179]\n" \
"s_waitcnt lgkmcnt(1)\n" \
"v_mfma_f32_16x16x32_bf16 v[64:67], a[64:67], a[0:3], v[64:67]\n" \
"s_waitcnt vmcnt(32)\n" \
"s_lshl_b32 s68, s63, 2\n" \
"ds_read_b128 a[24:27], v230 offset:6144\n" \
"v_mfma_f32_16x16x32_bf16 v[64:67], a[68:71], a[4:7], v[64:67]\n" \
"s_add_u32 s68, s68, s90\n" \
"ds_read_b128 a[28:31], v231 offset:6144\n" \
"v_mfma_f32_16x16x32_bf16 v[68:71], a[72:75], a[0:3], v[68:71]\n" \
"ds_read_b64_tr_b16 a[96:97], v232 offset:17408\n" \
"Lpg4445_%=:\n" \
"s_cbranch_execz Lpg6181_%=\n" \
"v_mfma_f32_16x16x32_bf16 v[68:71], a[76:79], a[4:7], v[68:71]\n" \
"v_mov_b32 v228, s60\n" \
"v_lshlrev_b32 v229, 28, s61\n" \
"v_mfma_f32_16x16x32_bf16 v[72:75], a[80:83], a[0:3], v[72:75]\n" \
"ds_read_b64_tr_b16 a[98:99], v232 offset:19456\n" \
"v_mfma_f32_16x16x32_bf16 v[72:75], a[84:87], a[4:7], v[72:75]\n" \
"s_waitcnt lgkmcnt(3)\n" \
"ds_read_b64_tr_b16 a[100:101], v232 offset:25600\n" \
"v_mfma_f32_16x16x32_bf16 v[76:79], a[88:91], a[0:3], v[76:79]\n" \
"ds_read_b64_tr_b16 a[102:103], v232 offset:27648\n" \
"v_mfma_f32_16x16x32_bf16 v[76:79], a[92:95], a[4:7], v[76:79]\n" \
"v_mfma_f32_16x16x32_bf16 v[108:111], a[88:91], a[8:11], v[108:111]\n" \
"ds_read_b64_tr_b16 a[104:105], v233 offset:17408\n" \
"v_mfma_f32_16x16x32_bf16 v[108:111], a[92:95], a[12:15], v[108:111]\n" \
"ds_read_b64_tr_b16 a[106:107], v233 offset:19456\n" \
"v_mfma_f32_16x16x32_bf16 v[104:107], a[80:83], a[8:11], v[104:107]\n" \
"ds_read_b64_tr_b16 a[108:109], v233 offset:25600\n" \
"v_mfma_f32_16x16x32_bf16 v[104:107], a[84:87], a[12:15], v[104:107]\n" \
"v_mfma_f32_16x16x32_bf16 v[100:103], a[72:75], a[8:11], v[100:103]\n" \
"ds_read_b64_tr_b16 a[110:111], v233 offset:27648\n" \
"v_mfma_f32_16x16x32_bf16 v[100:103], a[76:79], a[12:15], v[100:103]\n" \
"ds_read_b64_tr_b16 a[112:113], v232 offset:17920\n" \
"v_mfma_f32_16x16x32_bf16 v[96:99], a[64:67], a[8:11], v[96:99]\n" \
"ds_read_b64_tr_b16 a[114:115], v232 offset:19968\n" \
"v_mfma_f32_16x16x32_bf16 v[96:99], a[68:71], a[12:15], v[96:99]\n" \
"v_mfma_f32_16x16x32_bf16 v[128:131], a[64:67], a[16:19], v[128:131]\n" \
"ds_read_b64_tr_b16 a[116:117], v232 offset:26112\n" \
"v_mfma_f32_16x16x32_bf16 v[128:131], a[68:71], a[20:23], v[128:131]\n" \
"ds_read_b64_tr_b16 a[118:119], v232 offset:28160\n" \
"v_mfma_f32_16x16x32_bf16 v[132:135], a[72:75], a[16:19], v[132:135]\n" \
"ds_read_b64_tr_b16 a[120:121], v233 offset:17920\n" \
"v_mfma_f32_16x16x32_bf16 v[132:135], a[76:79], a[20:23], v[132:135]\n" \
"buffer_store_dwordx4 v[40:43], v236, s[56:59], s93 offen offset:2048 sc1\n" \
"v_mfma_f32_16x16x32_bf16 v[136:139], a[80:83], a[16:19], v[136:139]\n" \
"ds_read_b64_tr_b16 a[122:123], v233 offset:19968\n" \
"v_mfma_f32_16x16x32_bf16 v[136:139], a[84:87], a[20:23], v[136:139]\n" \
"ds_read_b64_tr_b16 a[124:125], v233 offset:26112\n" \
"v_mfma_f32_16x16x32_bf16 v[140:143], a[88:91], a[16:19], v[140:143]\n" \
"ds_read_b64_tr_b16 a[126:127], v233 offset:28160\n" \
"v_mfma_f32_16x16x32_bf16 v[140:143], a[92:95], a[20:23], v[140:143]\n" \
"s_waitcnt lgkmcnt(3)\n" \
"v_mfma_f32_16x16x32_bf16 v[172:175], a[88:91], a[24:27], v[172:175]\n" \
"v_mfma_f32_16x16x32_bf16 v[172:175], a[92:95], a[28:31], v[172:175]\n" \
"v_mfma_f32_16x16x32_bf16 v[168:171], a[80:83], a[24:27], v[168:171]\n" \
"v_mfma_f32_16x16x32_bf16 v[168:171], a[84:87], a[28:31], v[168:171]\n" \
"v_mfma_f32_16x16x32_bf16 v[164:167], a[72:75], a[24:27], v[164:167]\n" \
"v_mfma_f32_16x16x32_bf16 v[164:167], a[76:79], a[28:31], v[164:167]\n" \
"v_mfma_f32_16x16x32_bf16 v[160:163], a[64:67], a[24:27], v[160:163]\n" \
"v_mfma_f32_16x16x32_bf16 v[160:163], a[68:71], a[28:31], v[160:163]\n" \
"s_waitcnt vmcnt(3) lgkmcnt(0)\n" \
"s_barrier\n" \
"v_mfma_f32_16x16x32_bf16 v[80:83], a[96:99], a[0:3], v[80:83]\n" \
"ds_read_b64_tr_b16 a[64:65], v232 offset:32768\n" \
"v_mfma_f32_16x16x32_bf16 v[80:83], a[100:103], a[4:7], v[80:83]\n" \
"ds_read_b64_tr_b16 a[66:67], v232 offset:34816\n" \
"buffer_store_dword v228, v229, s[56:59], s68 offen sc1\n" \
"v_mfma_f32_16x16x32_bf16 v[84:87], a[104:107], a[0:3], v[84:87]\n" \
"ds_read_b64_tr_b16 a[68:69], v232 offset:40960\n" \
"v_lshrrev_b32 v238, 4, v234\n" \
"v_mfma_f32_16x16x32_bf16 v[84:87], a[108:111], a[4:7], v[84:87]\n" \
"buffer_store_dwordx4 v[56:59], v236, s[56:59], s93 offen offset:2304 sc1\n" \
"v_mfma_f32_16x16x32_bf16 v[88:91], a[112:115], a[0:3], v[88:91]\n" \
"ds_read_b64_tr_b16 a[70:71], v232 offset:43008\n" \
"v_lshrrev_b32 v240, 4, v238\n" \
"v_mfma_f32_16x16x32_bf16 v[88:91], a[116:119], a[4:7], v[88:91]\n" \
"ds_read_b64_tr_b16 a[72:73], v233 offset:32768\n" \
"v_lshlrev_b32 v240, 2, v240\n" \
"v_mfma_f32_16x16x32_bf16 v[92:95], a[120:123], a[0:3], v[92:95]\n" \
"ds_read_b64_tr_b16 a[74:75], v233 offset:34816\n" \
"v_and_b32 v238, 15, v238\n" \
"v_mfma_f32_16x16x32_bf16 v[92:95], a[124:127], a[4:7], v[92:95]\n" \
"ds_write_b128 v224, v[192:195] offset:8192\n" \
"v_mfma_f32_16x16x32_bf16 v[124:127], a[120:123], a[8:11], v[124:127]\n" \
"ds_read_b128 a[0:3], v230 offset:32768\n" \
"v_sub_u32 v238, v238, v240\n" \
"v_mfma_f32_16x16x32_bf16 v[124:127], a[124:127], a[12:15], v[124:127]\n" \
"ds_read_b128 a[4:7], v231 offset:32768\n" \
"v_subrev_u32 v248, 0, v238\n" \
"v_mfma_f32_16x16x32_bf16 v[120:123], a[112:115], a[8:11], v[120:123]\n" \
"ds_read_b64_tr_b16 a[76:77], v233 offset:40960\n" \
"v_subrev_u32 v249, 1, v238\n" \
"v_mfma_f32_16x16x32_bf16 v[120:123], a[116:119], a[12:15], v[120:123]\n" \
"ds_write_b128 v224, v[208:211] offset:8448\n" \
"v_mfma_f32_16x16x32_bf16 v[116:119], a[104:107], a[8:11], v[116:119]\n" \
"ds_read_b64_tr_b16 a[78:79], v233 offset:43008\n" \
"v_subrev_u32 v250, 2, v238\n" \
"v_mfma_f32_16x16x32_bf16 v[116:119], a[108:111], a[12:15], v[116:119]\n" \
"ds_read_b64_tr_b16 a[80:81], v232 offset:33280\n" \
"v_subrev_u32 v251, 3, v238\n" \
"v_mfma_f32_16x16x32_bf16 v[112:115], a[96:99], a[8:11], v[112:115]\n" \
"ds_read_b64_tr_b16 a[82:83], v232 offset:35328\n" \
"v_ashrrev_i32 v248, 31, v248\n" \
"v_mfma_f32_16x16x32_bf16 v[112:115], a[100:103], a[12:15], v[112:115]\n" \
"v_ashrrev_i32 v249, 31, v249\n" \
"v_ashrrev_i32 v250, 31, v250\n" \
"v_mfma_f32_16x16x32_bf16 v[144:147], a[96:99], a[16:19], v[144:147]\n" \
"ds_read_b128 a[8:11], v230 offset:34816\n" \
"v_ashrrev_i32 v251, 31, v251\n" \
"v_mfma_f32_16x16x32_bf16 v[144:147], a[100:103], a[20:23], v[144:147]\n" \
"ds_read_b128 a[12:15], v231 offset:34816\n" \
"v_lshlrev_b32 v248, 23, v248\n" \
"v_mfma_f32_16x16x32_bf16 v[148:151], a[104:107], a[16:19], v[148:151]\n" \
"ds_read_b64_tr_b16 a[84:85], v232 offset:41472\n" \
"v_lshlrev_b32 v249, 23, v249\n" \
"v_mfma_f32_16x16x32_bf16 v[148:151], a[108:111], a[20:23], v[148:151]\n" \
"ds_write_b128 v224, v[200:203] offset:10240\n" \
"v_mfma_f32_16x16x32_bf16 v[152:155], a[112:115], a[16:19], v[152:155]\n" \
"ds_read_b64_tr_b16 a[86:87], v232 offset:43520\n" \
"v_lshlrev_b32 v250, 23, v250\n" \
"v_mfma_f32_16x16x32_bf16 v[152:155], a[116:119], a[20:23], v[152:155]\n" \
"ds_read_b64_tr_b16 a[88:89], v233 offset:33280\n" \
"v_lshlrev_b32 v251, 23, v251\n" \
"v_mfma_f32_16x16x32_bf16 v[156:159], a[120:123], a[16:19], v[156:159]\n" \
"s_add_u32 s93, s94, 0\n" \
"ds_read_b64_tr_b16 a[90:91], v233 offset:35328\n" \
"v_mov_b32 v240, 0xff800000\n" \
"v_mfma_f32_16x16x32_bf16 v[156:159], a[124:127], a[20:23], v[156:159]\n" \
"buffer_store_dwordx4 v[36:39], v236, s[56:59], s93 offen offset:0 sc1\n" \
"v_mfma_f32_16x16x32_bf16 v[188:191], a[120:123], a[24:27], v[188:191]\n" \
"ds_read_b128 a[16:19], v230 offset:36864\n" \
"v_add_f32 v248, 0x42400000, v248\n" \
"v_mfma_f32_16x16x32_bf16 v[188:191], a[124:127], a[28:31], v[188:191]\n" \
"ds_read_b128 a[20:23], v231 offset:36864\n" \
"v_add_f32 v249, 0x42400000, v249\n" \
"v_mfma_f32_16x16x32_bf16 v[184:187], a[112:115], a[24:27], v[184:187]\n" \
"ds_read_b64_tr_b16 a[92:93], v233 offset:41472\n" \
"v_add_f32 v250, 0x42400000, v250\n" \
"v_mfma_f32_16x16x32_bf16 v[184:187], a[116:119], a[28:31], v[184:187]\n" \
"ds_write_b128 v224, v[216:219] offset:10496\n" \
"v_mfma_f32_16x16x32_bf16 v[180:183], a[104:107], a[24:27], v[180:183]\n" \
"ds_read_b64_tr_b16 a[94:95], v233 offset:43520\n" \
"v_add_f32 v251, 0x42400000, v251\n" \
"v_mfma_f32_16x16x32_bf16 v[180:183], a[108:111], a[28:31], v[180:183]\n" \
"s_mov_b32 s72, 0x42400000\n" \
"s_cmp_gt_u32 s61, 0\n" \
"s_cselect_b32 s70, s72, 0xff800000\n" \
"v_mfma_f32_16x16x32_bf16 v[176:179], a[96:99], a[24:27], v[176:179]\n" \
"ds_write_b128 v224, v[196:199] offset:0\n" \
"v_mfma_f32_16x16x32_bf16 v[176:179], a[100:103], a[28:31], v[176:179]\n" \
"s_waitcnt lgkmcnt(3)\n" \
"v_mov_b32 v252, s70\n" \
"v_mfma_f32_16x16x32_bf16 v[64:67], a[64:67], a[0:3], v[64:67]\n" \
"ds_read_b128 a[24:27], v230 offset:38912\n" \
"v_mfma_f32_16x16x32_bf16 v[64:67], a[68:71], a[4:7], v[64:67]\n" \
"s_cmp_gt_u32 s61, 1\n" \
"s_cselect_b32 s70, s72, 0xff800000\n" \
"ds_read_b128 a[28:31], v231 offset:38912\n" \
"v_mfma_f32_16x16x32_bf16 v[68:71], a[72:75], a[0:3], v[68:71]\n" \
"ds_read_b64_tr_b16 a[96:97], v232 offset:33792\n" \
"v_mov_b32 v253, s70\n" \
"v_mfma_f32_16x16x32_bf16 v[68:71], a[76:79], a[4:7], v[68:71]\n" \
"s_cmp_gt_u32 s61, 2\n" \
"s_cselect_b32 s70, s72, 0xff800000\n" \
"v_mov_b32 v226, s70\n" \
"v_mfma_f32_16x16x32_bf16 v[72:75], a[80:83], a[0:3], v[72:75]\n" \
"ds_read_b64_tr_b16 a[98:99], v232 offset:35840\n" \
"v_bfe_i32 v243, v234, 4, 1\n" \
"v_mfma_f32_16x16x32_bf16 v[72:75], a[84:87], a[4:7], v[72:75]\n" \
"s_waitcnt lgkmcnt(3)\n" \
"ds_read_b64_tr_b16 a[100:101], v232 offset:41984\n" \
"v_and_b32 v243, 0xaaaa, v243\n" \
"v_mfma_f32_16x16x32_bf16 v[76:79], a[88:91], a[0:3], v[76:79]\n" \
"ds_read_b64_tr_b16 a[102:103], v232 offset:44032\n" \
"v_or_b32 v243, 0x4444, v243\n" \
"v_mfma_f32_16x16x32_bf16 v[76:79], a[92:95], a[4:7], v[76:79]\n" \
"ds_write_b128 v224, v[212:215] offset:256\n" \
"v_mfma_f32_16x16x32_bf16 v[108:111], a[88:91], a[8:11], v[108:111]\n" \
"ds_read_b64_tr_b16 a[104:105], v233 offset:33792\n" \
"v_bfe_i32 v244, v234, 5, 1\n" \
"v_mfma_f32_16x16x32_bf16 v[108:111], a[92:95], a[12:15], v[108:111]\n" \
"ds_read_b64_tr_b16 a[106:107], v233 offset:35840\n" \
"v_not_b32 v244, v244\n" \
"v_mfma_f32_16x16x32_bf16 v[104:107], a[80:83], a[8:11], v[104:107]\n" \
"ds_read_b64_tr_b16 a[108:109], v233 offset:41984\n" \
"v_and_b32 v244, 0x3f803f80, v244\n" \
"v_mfma_f32_16x16x32_bf16 v[104:107], a[84:87], a[12:15], v[104:107]\n" \
"buffer_store_dwordx4 v[16:19], v236, s[56:59], s93 offen offset:256 sc1\n" \
"v_mfma_f32_16x16x32_bf16 v[100:103], a[72:75], a[8:11], v[100:103]\n" \
"ds_read_b64_tr_b16 a[110:111], v233 offset:44032\n" \
"v_mov_b32 v245, v244\n" \
"v_mfma_f32_16x16x32_bf16 v[100:103], a[76:79], a[12:15], v[100:103]\n" \
"ds_read_b64_tr_b16 a[112:113], v232 offset:34304\n" \
"v_mov_b32 v246, v244\n" \
"v_mfma_f32_16x16x32_bf16 v[96:99], a[64:67], a[8:11], v[96:99]\n" \
"ds_read_b64_tr_b16 a[114:115], v232 offset:36352\n" \
"v_mov_b32 v247, v244\n" \
"v_mfma_f32_16x16x32_bf16 v[96:99], a[68:71], a[12:15], v[96:99]\n" \
"ds_write_b128 v224, v[204:207] offset:2048\n" \
"v_mfma_f32_16x16x32_bf16 v[128:131], a[64:67], a[16:19], v[128:131]\n" \
"s_mov_b32 s76, s73\n" \
"s_and_b32 s77, s63, 1\n" \
"ds_read_b64_tr_b16 a[116:117], v232 offset:42496\n" \
"v_mfma_f32_16x16x32_bf16 v[128:131], a[68:71], a[20:23], v[128:131]\n" \
"s_lshl_b32 s77, s77, 15\n" \
"s_xor_b32 s89, s77, 0x8000\n" \
"ds_read_b64_tr_b16 a[118:119], v232 offset:44544\n" \
"v_mfma_f32_16x16x32_bf16 v[132:135], a[72:75], a[16:19], v[132:135]\n" \
"s_add_u32 s82, s89, 0x10000\n" \
"s_mov_b32 s74, 0\n" \
"ds_read_b64_tr_b16 a[120:121], v233 offset:34304\n" \
"v_mfma_f32_16x16x32_bf16 v[132:135], a[76:79], a[20:23], v[132:135]\n" \
"ds_write_b128 v224, v[220:223] offset:2304\n" \
"v_mfma_f32_16x16x32_bf16 v[136:139], a[80:83], a[16:19], v[136:139]\n" \
"s_waitcnt lgkmcnt(4)\n" \
"s_add_u32 s73, s73, 34816\n" \
"ds_read_b64_tr_b16 a[122:123], v233 offset:36352\n" \
"v_mfma_f32_16x16x32_bf16 v[136:139], a[84:87], a[20:23], v[136:139]\n" \
"s_cmp_eq_u32 s73, s83\n" \
"s_cselect_b32 s70, 34816, 0\n" \
"ds_read_b64_tr_b16 a[124:125], v233 offset:42496\n" \
"v_mfma_f32_16x16x32_bf16 v[140:143], a[88:91], a[16:19], v[140:143]\n" \
"s_add_u32 s73, s73, s70\n" \
"s_lshr_b32 s70, s61, 1\n" \
"ds_read_b64_tr_b16 a[126:127], v233 offset:44544\n" \
"v_mfma_f32_16x16x32_bf16 v[140:143], a[92:95], a[20:23], v[140:143]\n" \
"s_lshl_b32 s85, s63, 2\n" \
"s_sub_u32 s86, 60, s85\n" \
"s_add_u32 s85, s85, s70\n" \
"v_mfma_f32_16x16x32_bf16 v[172:175], a[88:91], a[24:27], v[172:175]\n" \
"s_add_u32 s86, s86, s70\n" \
"s_mul_i32 s87, s85, 266240\n" \
"v_add_u32 v230, 0x10000, v230\n" \
"v_mfma_f32_16x16x32_bf16 v[172:175], a[92:95], a[28:31], v[172:175]\n" \
"s_mul_i32 s88, s86, 266240\n" \
"s_and_b32 s75, s61, 1\n" \
"v_add_u32 v231, 0x10000, v231\n" \
"v_mfma_f32_16x16x32_bf16 v[168:171], a[80:83], a[24:27], v[168:171]\n" \
"s_lshl_b32 s70, s75, 8\n" \
"s_lshl_b32 s91, s62, 13\n" \
"s_add_u32 s91, s91, s70\n" \
"v_mfma_f32_16x16x32_bf16 v[168:171], a[84:87], a[28:31], v[168:171]\n" \
"buffer_store_dwordx4 v[52:55], v236, s[56:59], s93 offen offset:2048 sc1\n" \
"v_mfma_f32_16x16x32_bf16 v[164:167], a[72:75], a[24:27], v[164:167]\n" \
"s_add_u32 s91, s91, 35700736\n" \
"s_add_u32 s87, s87, s91\n" \
"s_add_u32 s88, s88, s91\n" \
"v_mfma_f32_16x16x32_bf16 v[164:167], a[76:79], a[28:31], v[164:167]\n" \
"s_lshl_b32 s75, s75, 14\n" \
"s_cmp_eq_u32 s63, 7\n" \
"s_cselect_b32 s75, 0, s75\n" \
"s_xor_b32 s78, s75, 0xc000\n" \
"s_mov_b32 s81, s78\n" \
"s_cmp_eq_u32 s63, 0\n" \
"s_cselect_b32 s91, s75, 0\n" \
"s_add_u32 s91, s91, 0x8000\n" \
"s_xor_b32 s75, s75, 0x8000\n" \
"s_sub_u32 s84, 7, s63\n" \
"v_mfma_f32_16x16x32_bf16 v[160:163], a[64:67], a[24:27], v[160:163]\n" \
"v_mfma_f32_16x16x32_bf16 v[160:163], a[68:71], a[28:31], v[160:163]\n" \
"s_waitcnt vmcnt(18) lgkmcnt(0)\n" \
"s_barrier\n" \
"v_mfma_f32_16x16x32_bf16 v[144:147], a[96:99], a[16:19], v[144:147]\n" \
"s_mov_b64 s[44:45], s[10:11]\n" \
"s_mov_b32 s46, 0\n" \
"ds_read_b64_tr_b16 a[64:65], v232 offset:49152\n" \
"v_mfma_f32_16x16x32_bf16 v[144:147], a[100:103], a[20:23], v[144:147]\n" \
"s_mov_b32 s47, 0\n" \
"s_cmp_eq_u32 s63, 7\n" \
"s_cselect_b64 s[44:45], s[18:19], s[44:45]\n" \
"s_cselect_b32 s46, 5, s46\n" \
"s_cmp_eq_u32 s63, 3\n" \
"s_cselect_b64 s[44:45], s[18:19], s[44:45]\n" \
"s_cselect_b32 s46, 3, s46\n" \
"ds_read_b64_tr_b16 a[66:67], v232 offset:51200\n" \
"v_mfma_f32_16x16x32_bf16 v[148:151], a[104:107], a[16:19], v[148:151]\n" \
"s_cmp_eq_u32 s63, 3\n" \
"s_cselect_b32 s47, 640, s47\n" \
"ds_read_b64_tr_b16 a[68:69], v232 offset:57344\n" \
"v_mfma_f32_16x16x32_bf16 v[148:151], a[108:111], a[20:23], v[148:151]\n" \
"ds_write_b128 v225, a[192:195] offset:0\n" \
"v_mfma_f32_16x16x32_bf16 v[152:155], a[112:115], a[16:19], v[152:155]\n" \
"ds_read_b64_tr_b16 a[70:71], v232 offset:59392\n" \
"v_mfma_f32_16x16x32_bf16 v[152:155], a[116:119], a[20:23], v[152:155]\n" \
"s_cmp_eq_u32 s63, 6\n" \
"s_cselect_b64 s[44:45], s[22:23], s[44:45]\n" \
"s_cselect_b32 s46, 6, s46\n" \
"ds_read_b64_tr_b16 a[72:73], v233 offset:49152\n" \
"v_mfma_f32_16x16x32_bf16 v[156:159], a[120:123], a[16:19], v[156:159]\n" \
"ds_read_b64_tr_b16 a[74:75], v233 offset:51200\n" \
"v_mfma_f32_16x16x32_bf16 v[156:159], a[124:127], a[20:23], v[156:159]\n" \
"s_cmp_eq_u32 s63, 4\n" \
"s_cselect_b64 s[44:45], s[20:21], s[44:45]\n" \
"s_cselect_b32 s46, 4, s46\n" \
"v_mfma_f32_16x16x32_bf16 v[188:191], a[120:123], a[24:27], v[188:191]\n" \
"ds_read_b128 a[16:19], v230 offset:0\n" \
"v_mfma_f32_16x16x32_bf16 v[188:191], a[124:127], a[28:31], v[188:191]\n" \
"s_cmp_eq_u32 s63, 2\n" \
"s_cselect_b64 s[44:45], s[24:25], s[44:45]\n" \
"s_cselect_b32 s46, 2, s46\n" \
"ds_read_b128 a[20:23], v231 offset:0\n" \
"v_mfma_f32_16x16x32_bf16 v[184:187], a[112:115], a[24:27], v[184:187]\n" \
"s_lshl_b32 s48, s62, 2\n" \
"s_add_u32 s48, s48, s61\n" \
"ds_read_b64_tr_b16 a[76:77], v233 offset:57344\n" \
"v_mfma_f32_16x16x32_bf16 v[184:187], a[116:119], a[28:31], v[184:187]\n" \
"ds_write_b128 v225, a[196:199] offset:1024\n" \
"v_mfma_f32_16x16x32_bf16 v[180:183], a[104:107], a[24:27], v[180:183]\n" \
"s_add_u32 s48, s48, s47\n" \
"s_lshl_b32 s48, s48, 13\n" \
"ds_read_b64_tr_b16 a[78:79], v233 offset:59392\n" \
"v_mfma_f32_16x16x32_bf16 v[180:183], a[108:111], a[28:31], v[180:183]\n" \
"s_add_u32 s44, s44, s48\n" \
"s_addc_u32 s45, s45, 0\n" \
"ds_read_b64_tr_b16 a[80:81], v232 offset:49664\n" \
"v_mfma_f32_16x16x32_bf16 v[176:179], a[96:99], a[24:27], v[176:179]\n" \
"s_cmp_eq_u32 s46, 0\n" \
"s_cselect_b32 s48, 28, 3\n" \
"ds_read_b64_tr_b16 a[82:83], v232 offset:51712\n" \
"v_mfma_f32_16x16x32_bf16 v[176:179], a[100:103], a[28:31], v[176:179]\n" \
"buffer_store_dwordx4 v[24:27], v236, s[56:59], s93 offen offset:2304 sc1\n" \
"v_mfma_f32_16x16x32_bf16 v[80:83], a[96:99], a[0:3], v[80:83]\n" \
"ds_read_b128 a[24:27], v230 offset:2048\n" \
"v_mfma_f32_16x16x32_bf16 v[80:83], a[100:103], a[4:7], v[80:83]\n" \
"ds_read_b128 a[28:31], v231 offset:2048\n" \
"v_mfma_f32_16x16x32_bf16 v[84:87], a[104:107], a[0:3], v[84:87]\n" \
"ds_read_b64_tr_b16 a[84:85], v232 offset:57856\n" \
"v_mfma_f32_16x16x32_bf16 v[84:87], a[108:111], a[4:7], v[84:87]\n" \
"ds_write_b128 v225, a[200:203] offset:2048\n" \
"v_mfma_f32_16x16x32_bf16 v[88:91], a[112:115], a[0:3], v[88:91]\n" \
"ds_read_b64_tr_b16 a[86:87], v232 offset:59904\n" \
"v_mfma_f32_16x16x32_bf16 v[88:91], a[116:119], a[4:7], v[88:91]\n" \
"ds_read_b64_tr_b16 a[88:89], v233 offset:49664\n" \
"v_mfma_f32_16x16x32_bf16 v[92:95], a[120:123], a[0:3], v[92:95]\n" \
"ds_read_b64_tr_b16 a[90:91], v233 offset:51712\n" \
"v_mfma_f32_16x16x32_bf16 v[92:95], a[124:127], a[4:7], v[92:95]\n" \
"ds_write_b128 v225, a[204:207] offset:3072\n" \
"v_mfma_f32_16x16x32_bf16 v[124:127], a[120:123], a[8:11], v[124:127]\n" \
"ds_read_b128 a[0:3], v230 offset:4096\n" \
"v_mfma_f32_16x16x32_bf16 v[124:127], a[124:127], a[12:15], v[124:127]\n" \
"ds_read_b128 a[4:7], v231 offset:4096\n" \
"v_mfma_f32_16x16x32_bf16 v[120:123], a[112:115], a[8:11], v[120:123]\n" \
"ds_read_b64_tr_b16 a[92:93], v233 offset:57856\n" \
"v_mfma_f32_16x16x32_bf16 v[120:123], a[116:119], a[12:15], v[120:123]\n" \
"v_mfma_f32_16x16x32_bf16 v[116:119], a[104:107], a[8:11], v[116:119]\n" \
"ds_read_b64_tr_b16 a[94:95], v233 offset:59904\n" \
"v_mfma_f32_16x16x32_bf16 v[116:119], a[108:111], a[12:15], v[116:119]\n" \
"v_mfma_f32_16x16x32_bf16 v[112:115], a[96:99], a[8:11], v[112:115]\n" \
"ds_write_b128 v225, a[224:227] offset:8192\n" \
"v_mfma_f32_16x16x32_bf16 v[112:115], a[100:103], a[12:15], v[112:115]\n" \
"s_waitcnt lgkmcnt(2)\n" \
"v_mfma_f32_16x16x32_bf16 v[64:67], a[64:67], a[16:19], v[64:67]\n" \
"ds_read_b128 a[8:11], v230 offset:6144\n" \
"ds_read_b128 a[12:15], v231 offset:6144\n" \
"v_mfma_f32_16x16x32_bf16 v[64:67], a[68:71], a[20:23], v[64:67]\n" \
"ds_read_b64_tr_b16 a[96:97], v232 offset:50176\n" \
"ds_read_b64_tr_b16 a[98:99], v232 offset:52224\n" \
"v_mfma_f32_16x16x32_bf16 v[68:71], a[72:75], a[16:19], v[68:71]\n" \
"s_waitcnt lgkmcnt(5)\n" \
"ds_read_b64_tr_b16 a[100:101], v232 offset:58368\n" \
"ds_read_b64_tr_b16 a[102:103], v232 offset:60416\n" \
"v_mfma_f32_16x16x32_bf16 v[68:71], a[76:79], a[20:23], v[68:71]\n" \
"s_getpc_b64 s[64:65]\n" \
"Lpd1_%=:\n" \
"s_lshl_b32 s66, s62, 2\n" \
"ds_read_b64_tr_b16 a[104:105], v233 offset:50176\n" \
"ds_read_b64_tr_b16 a[106:107], v233 offset:52224\n" \
"v_mfma_f32_16x16x32_bf16 v[72:75], a[80:83], a[16:19], v[72:75]\n" \
"s_add_u32 s66, s66, s61\n" \
"ds_read_b64_tr_b16 a[108:109], v233 offset:58368\n" \
"ds_read_b64_tr_b16 a[110:111], v233 offset:60416\n" \
"v_mfma_f32_16x16x32_bf16 v[72:75], a[84:87], a[20:23], v[72:75]\n" \
"s_and_b32 s66, s66, 15\n" \
"ds_read_b64_tr_b16 a[112:113], v232 offset:50688\n" \
"ds_read_b64_tr_b16 a[114:115], v232 offset:52736\n" \
"v_mfma_f32_16x16x32_bf16 v[76:79], a[88:91], a[16:19], v[76:79]\n" \
"s_lshl_b32 s66, s66, 12\n" \
"ds_read_b64_tr_b16 a[116:117], v232 offset:58880\n" \
"ds_read_b64_tr_b16 a[118:119], v232 offset:60928\n" \
"v_mfma_f32_16x16x32_bf16 v[76:79], a[92:95], a[20:23], v[76:79]\n" \
"s_add_u32 s64, s64, Lloop2_%=-Lpd1_%=\n" \
"s_addc_u32 s65, s65, 0\n" \
"s_mov_b32 s67, Lcode_end_%=-Lloop2_%=-4\n" \
"ds_read_b64_tr_b16 a[120:121], v233 offset:50688\n" \
"ds_read_b64_tr_b16 a[122:123], v233 offset:52736\n" \
"v_mfma_f32_16x16x32_bf16 v[96:99], a[64:67], a[24:27], v[96:99]\n" \
"s_add_u32 s93, s94, 17858560\n" \
"ds_read_b64_tr_b16 a[124:125], v233 offset:58880\n" \
"ds_read_b64_tr_b16 a[126:127], v233 offset:60928\n" \
"v_mfma_f32_16x16x32_bf16 v[96:99], a[68:71], a[28:31], v[96:99]\n" \
"buffer_store_dwordx4 v[44:47], v234, s[56:59], s93 offen offset:0 sc1\n" \
"v_mfma_f32_16x16x32_bf16 v[100:103], a[72:75], a[24:27], v[100:103]\n" \
"ds_write_b128 v225, a[228:231] offset:9216\n" \
"v_mfma_f32_16x16x32_bf16 v[100:103], a[76:79], a[28:31], v[100:103]\n" \
"v_lshl_add_u32 v230, v234, 2, s66\n" \
"v_min_u32 v230, s67, v230\n" \
"v_mfma_f32_16x16x32_bf16 v[104:107], a[80:83], a[24:27], v[104:107]\n" \
"buffer_load_dword v227, v242, s[56:59], s90 offen sc1\n" \
"v_mfma_f32_16x16x32_bf16 v[104:107], a[84:87], a[28:31], v[104:107]\n" \
"ds_write_b128 v225, a[232:235] offset:10240\n" \
"v_mfma_f32_16x16x32_bf16 v[108:111], a[88:91], a[24:27], v[108:111]\n" \
"global_load_dword v230, v230, s[64:65]\n" \
"v_mfma_f32_16x16x32_bf16 v[108:111], a[92:95], a[28:31], v[108:111]\n" \
"s_waitcnt lgkmcnt(1)\n" \
"v_cvt_pk_bf16_f32 v64, v64, v65\n" \
"v_cvt_pk_bf16_f32 v65, v66, v67\n" \
"v_mfma_f32_16x16x32_bf16 v[80:83], a[96:99], a[16:19], v[80:83]\n" \
"v_cvt_pk_bf16_f32 v66, v68, v69\n" \
"v_cvt_pk_bf16_f32 v67, v70, v71\n" \
"v_mfma_f32_16x16x32_bf16 v[80:83], a[100:103], a[20:23], v[80:83]\n" \
"v_accvgpr_write_b32 a128, v64\n" \
"v_accvgpr_write_b32 a129, v65\n" \
"v_accvgpr_write_b32 a130, v66\n" \
"v_mfma_f32_16x16x32_bf16 v[84:87], a[104:107], a[16:19], v[84:87]\n" \
"v_accvgpr_write_b32 a131, v67\n" \
"v_cvt_pk_bf16_f32 v72, v72, v73\n" \
"v_mfma_f32_16x16x32_bf16 v[84:87], a[108:111], a[20:23], v[84:87]\n" \
"buffer_store_dwordx4 v[60:63], v234, s[56:59], s93 offen offset:1024 sc1\n" \
"v_mfma_f32_16x16x32_bf16 v[88:91], a[112:115], a[16:19], v[88:91]\n" \
"ds_write_b128 v225, a[236:239] offset:11264\n" \
"v_mfma_f32_16x16x32_bf16 v[88:91], a[116:119], a[20:23], v[88:91]\n" \
"v_cvt_pk_bf16_f32 v73, v74, v75\n" \
"v_cvt_pk_bf16_f32 v74, v76, v77\n" \
"v_cvt_pk_bf16_f32 v75, v78, v79\n" \
"v_mfma_f32_16x16x32_bf16 v[92:95], a[120:123], a[16:19], v[92:95]\n" \
"v_accvgpr_write_b32 a132, v72\n" \
"v_accvgpr_write_b32 a133, v73\n" \
"v_mfma_f32_16x16x32_bf16 v[92:95], a[124:127], a[20:23], v[92:95]\n" \
"v_accvgpr_write_b32 a134, v74\n" \
"v_accvgpr_write_b32 a135, v75\n" \
"v_mfma_f32_16x16x32_bf16 v[112:115], a[96:99], a[24:27], v[112:115]\n" \
"v_cvt_pk_bf16_f32 v96, v96, v97\n" \
"v_cvt_pk_bf16_f32 v97, v98, v99\n" \
"v_mfma_f32_16x16x32_bf16 v[112:115], a[100:103], a[28:31], v[112:115]\n" \
"v_cvt_pk_bf16_f32 v98, v100, v101\n" \
"v_cvt_pk_bf16_f32 v99, v102, v103\n" \
"v_mfma_f32_16x16x32_bf16 v[116:119], a[104:107], a[24:27], v[116:119]\n" \
"v_cvt_pk_bf16_f32 v104, v104, v105\n" \
"v_cvt_pk_bf16_f32 v105, v106, v107\n" \
"v_mfma_f32_16x16x32_bf16 v[116:119], a[108:111], a[28:31], v[116:119]\n" \
"v_cvt_pk_bf16_f32 v106, v108, v109\n" \
"v_cvt_pk_bf16_f32 v107, v110, v111\n" \
"v_mfma_f32_16x16x32_bf16 v[120:123], a[112:115], a[24:27], v[120:123]\n" \
"v_mfma_f32_16x16x32_bf16 v[120:123], a[116:119], a[28:31], v[120:123]\n" \
"v_cvt_pk_bf16_f32 v80, v80, v81\n" \
"v_cvt_pk_bf16_f32 v81, v82, v83\n" \
"v_cvt_pk_bf16_f32 v82, v84, v85\n" \
"v_mfma_f32_16x16x32_bf16 v[124:127], a[120:123], a[24:27], v[124:127]\n" \
"v_cvt_pk_bf16_f32 v83, v86, v87\n" \
"v_accvgpr_write_b32 a136, v80\n" \
"v_mfma_f32_16x16x32_bf16 v[124:127], a[124:127], a[28:31], v[124:127]\n" \
"v_accvgpr_write_b32 a137, v81\n" \
"s_waitcnt lgkmcnt(0)\n" \
"s_barrier\n" \
"v_mfma_f32_16x16x32_bf16 v[128:131], a[64:67], a[0:3], v[128:131]\n" \
"v_accvgpr_write_b32 a138, v82\n" \
"v_accvgpr_write_b32 a139, v83\n" \
"v_mfma_f32_16x16x32_bf16 v[128:131], a[68:71], a[4:7], v[128:131]\n" \
"ds_read_b128 a[192:195], v237 offset:0\n" \
"v_cvt_pk_bf16_f32 v88, v88, v89\n" \
"v_mfma_f32_16x16x32_bf16 v[132:135], a[72:75], a[0:3], v[132:135]\n" \
"v_cvt_pk_bf16_f32 v89, v90, v91\n" \
"v_cvt_pk_bf16_f32 v90, v92, v93\n" \
"v_mfma_f32_16x16x32_bf16 v[132:135], a[76:79], a[4:7], v[132:135]\n" \
"ds_read_b128 a[196:199], v237 offset:2048\n" \
"v_cvt_pk_bf16_f32 v91, v94, v95\n" \
"v_mfma_f32_16x16x32_bf16 v[136:139], a[80:83], a[0:3], v[136:139]\n" \
"v_med3_f32 v208, v248, v252, s72\n" \
"v_med3_f32 v209, v249, v252, s72\n" \
"v_mfma_f32_16x16x32_bf16 v[136:139], a[84:87], a[4:7], v[136:139]\n" \
"ds_read_b128 a[200:203], v237 offset:4096\n" \
"v_med3_f32 v210, v250, v252, s72\n" \
"v_med3_f32 v211, v251, v252, s72\n" \
"v_mfma_f32_16x16x32_bf16 v[140:143], a[88:91], a[0:3], v[140:143]\n" \
"buffer_store_dwordx4 v[20:23], v234, s[56:59], s93 offen offset:2048 sc1\n" \
"v_mfma_f32_16x16x32_bf16 v[140:143], a[92:95], a[4:7], v[140:143]\n" \
"ds_read_b128 a[204:207], v237 offset:6144\n" \
"v_cvt_pk_bf16_f32 v112, v112, v113\n" \
"v_mfma_f32_16x16x32_bf16 v[144:147], a[96:99], a[0:3], v[144:147]\n" \
"v_cvt_pk_bf16_f32 v113, v114, v115\n" \
"v_cvt_pk_bf16_f32 v114, v116, v117\n" \
"v_mfma_f32_16x16x32_bf16 v[144:147], a[100:103], a[4:7], v[144:147]\n" \
"ds_read_b128 a[208:211], v237 offset:256\n" \
"v_cvt_pk_bf16_f32 v115, v118, v119\n" \
"v_cvt_pk_bf16_f32 v120, v120, v121\n" \
"v_mfma_f32_16x16x32_bf16 v[148:151], a[104:107], a[0:3], v[148:151]\n" \
"v_cvt_pk_bf16_f32 v121, v122, v123\n" \
"v_cvt_pk_bf16_f32 v122, v124, v125\n" \
"v_mfma_f32_16x16x32_bf16 v[148:151], a[108:111], a[4:7], v[148:151]\n" \
"ds_read_b128 a[212:215], v237 offset:2304\n" \
"v_cvt_pk_bf16_f32 v123, v126, v127\n" \
"v_mfma_f32_16x16x32_bf16 v[152:155], a[112:115], a[0:3], v[152:155]\n" \
"v_mfma_f32_16x16x32_bf16 v[152:155], a[116:119], a[4:7], v[152:155]\n" \
"ds_read_b128 a[216:219], v237 offset:4352\n" \
"v_cvt_pk_bf16_f32 v128, v128, v129\n" \
"v_mfma_f32_16x16x32_bf16 v[156:159], a[120:123], a[0:3], v[156:159]\n" \
"v_cvt_pk_bf16_f32 v129, v130, v131\n" \
"v_cvt_pk_bf16_f32 v130, v132, v133\n" \
"v_mfma_f32_16x16x32_bf16 v[156:159], a[124:127], a[4:7], v[156:159]\n" \
"ds_read_b128 a[220:223], v237 offset:6400\n" \
"v_cvt_pk_bf16_f32 v131, v134, v135\n" \
"v_cvt_pk_bf16_f32 v136, v136, v137\n" \
"v_mfma_f32_16x16x32_bf16 v[160:163], a[64:67], a[8:11], v[160:163]\n" \
"v_cvt_pk_bf16_f32 v137, v138, v139\n" \
"v_cvt_pk_bf16_f32 v138, v140, v141\n" \
"v_mfma_f32_16x16x32_bf16 v[160:163], a[68:71], a[12:15], v[160:163]\n" \
"ds_read_b128 a[224:227], v237 offset:8192\n" \
"v_cvt_pk_bf16_f32 v139, v142, v143\n" \
"v_mfma_f32_16x16x32_bf16 v[164:167], a[72:75], a[8:11], v[164:167]\n" \
"v_mfma_f32_16x16x32_bf16 v[164:167], a[76:79], a[12:15], v[164:167]\n" \
"ds_read_b128 a[228:231], v237 offset:10240\n" \
"v_mfma_f32_16x16x32_bf16 v[168:171], a[80:83], a[8:11], v[168:171]\n" \
"v_mfma_f32_16x16x32_bf16 v[168:171], a[84:87], a[12:15], v[168:171]\n" \
"ds_read_b128 a[232:235], v237 offset:12288\n" \
"v_cvt_pk_bf16_f32 v144, v144, v145\n" \
"v_cvt_pk_bf16_f32 v145, v146, v147\n" \
"v_mfma_f32_16x16x32_bf16 v[172:175], a[88:91], a[8:11], v[172:175]\n" \
"v_cvt_pk_bf16_f32 v146, v148, v149\n" \
"v_cvt_pk_bf16_f32 v147, v150, v151\n" \
"v_mfma_f32_16x16x32_bf16 v[172:175], a[92:95], a[12:15], v[172:175]\n" \
"ds_read_b128 a[236:239], v237 offset:14336\n" \
"v_cvt_pk_bf16_f32 v152, v152, v153\n" \
"v_mfma_f32_16x16x32_bf16 v[176:179], a[96:99], a[8:11], v[176:179]\n" \
"v_cvt_pk_bf16_f32 v153, v154, v155\n" \
"v_cvt_pk_bf16_f32 v154, v156, v157\n" \
"v_mfma_f32_16x16x32_bf16 v[176:179], a[100:103], a[12:15], v[176:179]\n" \
"ds_read_b128 a[240:243], v237 offset:8448\n" \
"v_cvt_pk_bf16_f32 v155, v158, v159\n" \
"v_mfma_f32_16x16x32_bf16 v[180:183], a[104:107], a[8:11], v[180:183]\n" \
"buffer_store_dwordx4 v[28:31], v234, s[56:59], s93 offen offset:3072 sc1\n" \
"v_mfma_f32_16x16x32_bf16 v[180:183], a[108:111], a[12:15], v[180:183]\n" \
"ds_read_b128 a[244:247], v237 offset:10496\n" \
"ds_write_b128 v239, v[128:131] offset:0\n" \
"v_mov_b32 v196, 0x42400000\n" \
"v_mfma_f32_16x16x32_bf16 v[184:187], a[112:115], a[8:11], v[184:187]\n" \
"v_mov_b32 v197, 0x42400000\n" \
"v_mov_b32 v198, 0x42400000\n" \
"v_mfma_f32_16x16x32_bf16 v[184:187], a[116:119], a[12:15], v[184:187]\n" \
"ds_read_b128 a[248:251], v237 offset:12544\n" \
"v_mov_b32 v199, 0x42400000\n" \
"v_cvt_pk_bf16_f32 v160, v160, v161\n" \
"v_mfma_f32_16x16x32_bf16 v[188:191], a[120:123], a[8:11], v[188:191]\n" \
"v_cvt_pk_bf16_f32 v161, v162, v163\n" \
"v_cvt_pk_bf16_f32 v162, v164, v165\n" \
"v_mfma_f32_16x16x32_bf16 v[188:191], a[124:127], a[12:15], v[188:191]\n" \
"s_waitcnt lgkmcnt(3)\n" \
"ds_read_b128 a[252:255], v237 offset:14592\n" \
"v_cvt_pk_bf16_f32 v163, v166, v167\n" \
"v_mfma_f32_16x16x32_bf16 v[16:19], a[192:195], v[96:99], v[208:211]\n" \
"v_cvt_pk_bf16_f32 v168, v168, v169\n" \
"v_cvt_pk_bf16_f32 v169, v170, v171\n" \
"v_mfma_f32_16x16x32_bf16 v[16:19], a[196:199], v[104:107], v[16:19]\n" \
"v_cvt_pk_bf16_f32 v170, v172, v173\n" \
"v_cvt_pk_bf16_f32 v171, v174, v175\n" \
"v_mfma_f32_16x16x32_bf16 v[16:19], a[200:203], v[112:115], v[16:19]\n" \
"v_cvt_pk_bf16_f32 v176, v176, v177\n" \
"v_cvt_pk_bf16_f32 v177, v178, v179\n" \
"v_mfma_f32_16x16x32_bf16 v[16:19], a[204:207], v[120:123], v[16:19]\n" \
"v_cvt_pk_bf16_f32 v178, v180, v181\n" \
"v_cvt_pk_bf16_f32 v179, v182, v183\n" \
"v_mfma_f32_16x16x32_bf16 v[32:35], a[204:207], v[152:155], v[196:199]\n" \
"v_cvt_pk_bf16_f32 v184, v184, v185\n" \
"v_cvt_pk_bf16_f32 v185, v186, v187\n" \
"v_mfma_f32_16x16x32_bf16 v[32:35], a[200:203], v[144:147], v[32:35]\n" \
"v_cvt_pk_bf16_f32 v186, v188, v189\n" \
"v_cvt_pk_bf16_f32 v187, v190, v191\n" \
"v_mfma_f32_16x16x32_bf16 v[32:35], a[196:199], v[136:139], v[32:35]\n" \
"s_add_u32 s93, s94, 17850368\n" \
"v_med3_f32 v212, v248, v253, v252\n" \
"v_med3_f32 v213, v249, v253, v252\n" \
"v_mfma_f32_16x16x32_bf16 v[32:35], a[192:195], v[128:131], v[32:35]\n" \
"buffer_store_dwordx4 v[0:3], v234, s[56:59], s93 offen offset:0 sc1\n" \
"v_mfma_f32_16x16x32_bf16 v[48:51], a[192:195], v[160:163], v[196:199]\n" \
"v_med3_f32 v214, v250, v253, v252\n" \
"v_med3_f32 v215, v251, v253, v252\n" \
"Lpg5010_%=:\n" \
"s_cbranch_execz Lpg6773_%=\n" \
"v_mfma_f32_16x16x32_bf16 v[48:51], a[196:199], v[168:171], v[48:51]\n" \
"ds_read_b128 a[192:195], v237 offset:16384\n" \
"v_med3_f32 v216, v248, v226, v253\n" \
"v_mfma_f32_16x16x32_bf16 v[48:51], a[200:203], v[176:179], v[48:51]\n" \
"ds_read_b128 a[196:199], v237 offset:18432\n" \
"v_med3_f32 v217, v249, v226, v253\n" \
"v_mfma_f32_16x16x32_bf16 v[48:51], a[204:207], v[184:187], v[48:51]\n" \
"ds_read_b128 a[200:203], v237 offset:20480\n" \
"ds_write_b128 v239, v[136:139] offset:1024\n" \
"v_mfma_f32_16x16x32_bf16 v[52:55], a[220:223], v[184:187], v[196:199]\n" \
"ds_read_b128 a[204:207], v237 offset:22528\n" \
"v_med3_f32 v218, v250, v226, v253\n" \
"v_mfma_f32_16x16x32_bf16 v[52:55], a[216:219], v[176:179], v[52:55]\n" \
"v_med3_f32 v219, v251, v226, v253\n" \
"v_med3_f32 v220, v248, v240, v226\n" \
"v_mfma_f32_16x16x32_bf16 v[52:55], a[212:215], v[168:171], v[52:55]\n" \
"ds_write_b128 v239, v[144:147] offset:2048\n" \
"v_mfma_f32_16x16x32_bf16 v[52:55], a[208:211], v[160:163], v[52:55]\n" \
"buffer_store_dwordx4 v[4:7], v234, s[56:59], s93 offen offset:1024 sc1\n" \
"v_mfma_f32_16x16x32_bf16 v[36:39], a[208:211], v[128:131], v[196:199]\n" \
"v_med3_f32 v221, v249, v240, v226\n" \
"v_med3_f32 v222, v250, v240, v226\n" \
"v_mfma_f32_16x16x32_bf16 v[36:39], a[212:215], v[136:139], v[36:39]\n" \
"v_med3_f32 v223, v251, v240, v226\n" \
"v_accvgpr_write_b32 a144, v96\n" \
"v_mfma_f32_16x16x32_bf16 v[36:39], a[216:219], v[144:147], v[36:39]\n" \
"v_accvgpr_write_b32 a145, v97\n" \
"v_accvgpr_write_b32 a146, v98\n" \
"v_mfma_f32_16x16x32_bf16 v[36:39], a[220:223], v[152:155], v[36:39]\n" \
"ds_write_b128 v239, v[152:155] offset:3072\n" \
"v_mfma_f32_16x16x32_bf16 v[20:23], a[220:223], v[120:123], v[212:215]\n" \
"v_accvgpr_write_b32 a147, v99\n" \
"v_accvgpr_write_b32 a148, v104\n" \
"v_mfma_f32_16x16x32_bf16 v[20:23], a[216:219], v[112:115], v[20:23]\n" \
"ds_read_b128 a[220:223], v237 offset:22784\n" \
"v_accvgpr_write_b32 a149, v105\n" \
"v_mfma_f32_16x16x32_bf16 v[20:23], a[212:215], v[104:107], v[20:23]\n" \
"ds_read_b128 a[216:219], v237 offset:20736\n" \
"v_accvgpr_write_b32 a150, v106\n" \
"v_mfma_f32_16x16x32_bf16 v[20:23], a[208:211], v[96:99], v[20:23]\n" \
"ds_read_b128 a[212:215], v237 offset:18688\n" \
"ds_write_b128 v239, v[160:163] offset:4096\n" \
"v_mfma_f32_16x16x32_bf16 v[24:27], a[224:227], v[96:99], v[216:219]\n" \
"ds_read_b128 a[208:211], v237 offset:16640\n" \
"v_accvgpr_write_b32 a151, v107\n" \
"v_mfma_f32_16x16x32_bf16 v[24:27], a[228:231], v[104:107], v[24:27]\n" \
"v_accvgpr_write_b32 a152, v112\n" \
"v_accvgpr_write_b32 a153, v113\n" \
"v_mfma_f32_16x16x32_bf16 v[24:27], a[232:235], v[112:115], v[24:27]\n" \
"ds_write_b128 v239, v[168:171] offset:5120\n" \
"v_mfma_f32_16x16x32_bf16 v[24:27], a[236:239], v[120:123], v[24:27]\n" \
"buffer_store_dwordx4 v[8:11], v234, s[56:59], s93 offen offset:2048 sc1\n" \
"v_mfma_f32_16x16x32_bf16 v[40:43], a[236:239], v[152:155], v[196:199]\n" \
"v_accvgpr_write_b32 a154, v114\n" \
"v_accvgpr_write_b32 a155, v115\n" \
"v_mfma_f32_16x16x32_bf16 v[40:43], a[232:235], v[144:147], v[40:43]\n" \
"v_accvgpr_write_b32 a156, v120\n" \
"v_accvgpr_write_b32 a157, v121\n" \
"v_mfma_f32_16x16x32_bf16 v[40:43], a[228:231], v[136:139], v[40:43]\n" \
"v_accvgpr_write_b32 a158, v122\n" \
"v_accvgpr_write_b32 a159, v123\n" \
"v_mfma_f32_16x16x32_bf16 v[40:43], a[224:227], v[128:131], v[40:43]\n" \
"ds_write_b128 v239, v[176:179] offset:6144\n" \
"v_mfma_f32_16x16x32_bf16 v[56:59], a[224:227], v[160:163], v[196:199]\n" \
"v_accvgpr_write_b32 a140, v88\n" \
"v_accvgpr_write_b32 a141, v89\n" \
"v_mfma_f32_16x16x32_bf16 v[56:59], a[228:231], v[168:171], v[56:59]\n" \
"ds_read_b128 a[224:227], v237 offset:24576\n" \
"v_accvgpr_write_b32 a142, v90\n" \
"v_mfma_f32_16x16x32_bf16 v[56:59], a[232:235], v[176:179], v[56:59]\n" \
"s_waitcnt lgkmcnt(2)\n" \
"ds_read_b128 a[228:231], v237 offset:26624\n" \
"v_accvgpr_write_b32 a143, v91\n" \
"v_mfma_f32_16x16x32_bf16 v[56:59], a[236:239], v[184:187], v[56:59]\n" \
"buffer_store_dwordx4 v[12:15], v234, s[56:59], s93 offen offset:3072 sc1\n" \
"ds_write_b128 v239, v[184:187] offset:7168\n" \
"v_mfma_f32_16x16x32_bf16 v[60:63], a[252:255], v[184:187], v[196:199]\n" \
"ds_read_b128 a[232:235], v237 offset:28672\n" \
"v_add_u32 v233, s82, v235\n" \
"v_mfma_f32_16x16x32_bf16 v[60:63], a[248:251], v[176:179], v[60:63]\n" \
"s_waitcnt vmcnt(8)\n" \
"ds_read_b128 a[236:239], v237 offset:30720\n" \
"v_cmp_eq_u32 vcc, s60, v227\n" \
"v_mfma_f32_16x16x32_bf16 v[60:63], a[244:247], v[168:171], v[60:63]\n" \
"v_exp_f32 v16, v16\n" \
"v_mfma_f32_16x16x32_bf16 v[60:63], a[240:243], v[160:163], v[60:63]\n" \
"s_andn2_b32 s70, 0xff, vcc_lo\n" \
"s_cbranch_scc1 Lpl1_%=\n" \
"Lpr1_%=:\n" \
"v_exp_f32 v17, v17\n" \
"v_exp_f32 v18, v18\n" \
"v_mfma_f32_16x16x32_bf16 v[44:47], a[240:243], v[128:131], v[196:199]\n" \
"v_exp_f32 v19, v19\n" \
"v_mfma_f32_16x16x32_bf16 v[44:47], a[244:247], v[136:139], v[44:47]\n" \
"s_add_u32 m0, s79, 32768\n" \
"v_exp_f32 v20, v20\n" \
"v_mfma_f32_16x16x32_bf16 v[44:47], a[248:251], v[144:147], v[44:47]\n" \
"s_add_u32 s71, s76, 0\n" \
"v_cvt_pk_bf16_f32 v16, v16, v17\n" \
"v_cvt_pk_bf16_f32 v17, v18, v19\n" \
"v_mfma_f32_16x16x32_bf16 v[44:47], a[252:255], v[152:155], v[44:47]\n" \
"buffer_load_dwordx4 v234, s[36:39], s71 offen offset:0 sc1 lds\n" \
"v_exp_f32 v21, v21\n" \
"v_exp_f32 v22, v22\n" \
"v_mfma_f32_16x16x32_bf16 v[28:31], a[252:255], v[120:123], v[220:223]\n" \
"v_exp_f32 v23, v23\n" \
"v_mfma_f32_16x16x32_bf16 v[28:31], a[248:251], v[112:115], v[28:31]\n" \
"ds_read_b128 a[252:255], v237 offset:30976\n" \
"v_cvt_pk_bf16_f32 v18, v20, v21\n" \
"v_mfma_f32_16x16x32_bf16 v[28:31], a[244:247], v[104:107], v[28:31]\n" \
"buffer_load_dwordx4 v234, s[36:39], s71 offen offset:1024 sc1 lds\n" \
"v_mfma_f32_16x16x32_bf16 v[28:31], a[240:243], v[96:99], v[28:31]\n" \
"ds_read_b128 a[244:247], v237 offset:26880\n" \
"ds_read_b128 a[248:251], v237 offset:28928\n" \
"v_exp_f32 v32, v32\n" \
"v_exp_f32 v33, v33\n" \
"v_mfma_f32_16x16x32_bf16 v[80:83], a[192:195], a[144:147], v[196:199]\n" \
"ds_read_b128 a[240:243], v237 offset:24832\n" \
"v_cvt_pk_bf16_f32 v19, v22, v23\n" \
"v_mfma_f32_16x16x32_bf16 v[80:83], a[196:199], a[148:151], v[80:83]\n" \
"buffer_load_dwordx4 v234, s[36:39], s71 offen offset:2048 sc1 lds\n" \
"v_mfma_f32_16x16x32_bf16 v[80:83], a[200:203], a[152:155], v[80:83]\n" \
"s_lshl_b32 s68, s63, 2\n" \
"s_add_u32 s68, s68, s95\n" \
"s_mov_b32 s69, 0\n" \
"v_mfma_f32_16x16x32_bf16 v[80:83], a[204:207], a[156:159], v[80:83]\n" \
"s_mov_b32 s89, s63\n" \
"ds_read_b128 a[44:47], v233 offset:15360\n" \
"v_mov_b32 v192, s60\n" \
"v_lshlrev_b32 v193, 28, s61\n" \
"v_exp_f32 v34, v34\n" \
"v_exp_f32 v35, v35\n" \
"v_mfma_f32_16x16x32_bf16 v[96:99], a[204:207], v[152:155], v[196:199]\n" \
"buffer_load_dwordx4 v234, s[36:39], s71 offen offset:3072 sc1 lds\n" \
"v_mfma_f32_16x16x32_bf16 v[96:99], a[200:203], v[144:147], v[96:99]\n" \
"s_add_u32 m0, s79, 49152\n" \
"s_mov_b32 s90, 0\n" \
"ds_read_b128 a[40:43], v233 offset:7168\n" \
"v_mfma_f32_16x16x32_bf16 v[96:99], a[196:199], v[136:139], v[96:99]\n" \
"s_add_u32 s71, s76, 17408\n" \
"v_exp_f32 v36, v36\n" \
"v_cvt_pk_bf16_f32 v32, v32, v33\n" \
"v_mfma_f32_16x16x32_bf16 v[96:99], a[192:195], v[128:131], v[96:99]\n" \
"buffer_load_dwordx4 v234, s[36:39], s71 offen offset:0 sc1 lds\n" \
"v_mfma_f32_16x16x32_bf16 v[112:115], a[192:195], v[160:163], v[196:199]\n" \
"ds_read_b128 a[36:39], v233 offset:14336\n" \
"v_cvt_pk_bf16_f32 v33, v34, v35\n" \
"v_mfma_f32_16x16x32_bf16 v[112:115], a[196:199], v[168:171], v[112:115]\n" \
"v_exp_f32 v37, v37\n" \
"v_mfma_f32_16x16x32_bf16 v[112:115], a[200:203], v[176:179], v[112:115]\n" \
"buffer_load_dwordx4 v234, s[36:39], s71 offen offset:1024 sc1 lds\n" \
"v_mfma_f32_16x16x32_bf16 v[112:115], a[204:207], v[184:187], v[112:115]\n" \
"ds_read_b128 a[32:35], v233 offset:6144\n" \
"v_exp_f32 v38, v38\n" \
"v_exp_f32 v39, v39\n" \
"v_mfma_f32_16x16x32_bf16 v[64:67], a[204:207], a[140:143], v[208:211]\n" \
"v_exp_f32 v48, v48\n" \
"v_mfma_f32_16x16x32_bf16 v[64:67], a[200:203], a[136:139], v[64:67]\n" \
"buffer_load_dwordx4 v234, s[36:39], s71 offen offset:2048 sc1 lds\n" \
"v_cvt_pk_bf16_f32 v34, v36, v37\n" \
"v_mfma_f32_16x16x32_bf16 v[64:67], a[196:199], a[132:135], v[64:67]\n" \
"ds_read_b128 a[204:207], v233 offset:9216\n" \
"v_cvt_pk_bf16_f32 v35, v38, v39\n" \
"v_mfma_f32_16x16x32_bf16 v[64:67], a[192:195], a[128:131], v[64:67]\n" \
"v_exp_f32 v49, v49\n" \
"v_exp_f32 v50, v50\n" \
"v_mfma_f32_16x16x32_bf16 v[68:71], a[208:211], a[128:131], v[212:215]\n" \
"buffer_load_dwordx4 v234, s[36:39], s71 offen offset:3072 sc1 lds\n" \
"v_mfma_f32_16x16x32_bf16 v[68:71], a[212:215], a[132:135], v[68:71]\n" \
"ds_read_b128 a[200:203], v233 offset:1024\n" \
"v_cvt_pk_bf16_f32 v48, v48, v49\n" \
"v_mfma_f32_16x16x32_bf16 v[68:71], a[216:219], a[136:139], v[68:71]\n" \
"s_add_u32 m0, s77, s80\n" \
"v_exp_f32 v51, v51\n" \
"v_mfma_f32_16x16x32_bf16 v[68:71], a[220:223], a[140:143], v[68:71]\n" \
"buffer_load_dwordx4 v234, s[40:43], s76 offen offset:0 sc1 lds\n" \
"v_exp_f32 v52, v52\n" \
"v_exp_f32 v53, v53\n" \
"v_mfma_f32_16x16x32_bf16 v[116:119], a[220:223], v[184:187], v[196:199]\n" \
"ds_read_b128 a[196:199], v233 offset:8192\n" \
"v_cvt_pk_bf16_f32 v49, v50, v51\n" \
"v_mfma_f32_16x16x32_bf16 v[116:119], a[216:219], v[176:179], v[116:119]\n" \
"ds_read_b128 a[160:163], v239 offset:0\n" \
"v_mfma_f32_16x16x32_bf16 v[116:119], a[212:215], v[168:171], v[116:119]\n" \
"s_add_u32 s71, s76, 17408\n" \
"ds_read_b128 a[192:195], v233 offset:0\n" \
"v_exp_f32 v54, v54\n" \
"v_mfma_f32_16x16x32_bf16 v[116:119], a[208:211], v[160:163], v[116:119]\n" \
"buffer_load_dwordx4 v234, s[40:43], s76 offen offset:1024 sc1 lds\n" \
"v_mfma_f32_16x16x32_bf16 v[100:103], a[208:211], v[128:131], v[196:199]\n" \
"ds_read_b128 a[164:167], v239 offset:1024\n" \
"v_cvt_pk_bf16_f32 v50, v52, v53\n" \
"v_mfma_f32_16x16x32_bf16 v[100:103], a[212:215], v[136:139], v[100:103]\n" \
"v_exp_f32 v55, v55\n" \
"v_mfma_f32_16x16x32_bf16 v[100:103], a[216:219], v[144:147], v[100:103]\n" \
"v_cvt_pk_bf16_f32 v51, v54, v55\n" \
"v_exp_f32 v24, v24\n" \
"v_mfma_f32_16x16x32_bf16 v[100:103], a[220:223], v[152:155], v[100:103]\n" \
"buffer_load_dwordx4 v234, s[40:43], s76 offen offset:2048 sc1 lds\n" \
"v_exp_f32 v25, v25\n" \
"v_mfma_f32_16x16x32_bf16 v[84:87], a[220:223], a[156:159], v[196:199]\n" \
"ds_read_b128 a[168:171], v239 offset:2048\n" \
"v_mfma_f32_16x16x32_bf16 v[84:87], a[216:219], a[152:155], v[84:87]\n" \
"ds_read_b128 a[220:223], v233 offset:11264\n" \
"v_mfma_f32_16x16x32_bf16 v[84:87], a[212:215], a[148:151], v[84:87]\n" \
"s_waitcnt lgkmcnt(2)\n" \
"v_exp_f32 v26, v26\n" \
"v_mfma_f32_16x16x32_bf16 v[84:87], a[208:211], a[144:147], v[84:87]\n" \
"buffer_load_dwordx4 v234, s[40:43], s76 offen offset:3072 sc1 lds\n" \
"v_exp_f32 v27, v27\n" \
"v_mfma_f32_16x16x32_bf16 v[88:91], a[224:227], a[144:147], v[196:199]\n" \
"ds_read_b128 a[216:219], v233 offset:3072\n" \
"v_cvt_pk_bf16_f32 v20, v24, v25\n" \
"v_mfma_f32_16x16x32_bf16 v[88:91], a[228:231], a[148:151], v[88:91]\n" \
"ds_read_b128 a[172:175], v239 offset:3072\n" \
"v_exp_f32 v28, v28\n" \
"v_mfma_f32_16x16x32_bf16 v[88:91], a[232:235], a[152:155], v[88:91]\n" \
"s_add_u32 m0, m0, 0x4000\n" \
"ds_read_b128 a[212:215], v233 offset:10240\n" \
"v_xor_b32 v248, 0x2000, v239\n" \
"v_mfma_f32_16x16x32_bf16 v[88:91], a[236:239], a[156:159], v[88:91]\n" \
"buffer_load_dwordx4 v234, s[40:43], s71 offen offset:0 sc1 lds\n" \
"v_exp_f32 v29, v29\n" \
"v_mfma_f32_16x16x32_bf16 v[104:107], a[236:239], v[152:155], v[196:199]\n" \
"ds_read_b128 a[208:211], v233 offset:2048\n" \
"v_lshlrev_b32 v252, s48, v234\n" \
"v_mfma_f32_16x16x32_bf16 v[104:107], a[232:235], v[144:147], v[104:107]\n" \
"ds_read_b128 a[176:179], v239 offset:4096\n" \
"v_mov_b32 v224, 0\n" \
"v_mfma_f32_16x16x32_bf16 v[104:107], a[228:231], v[136:139], v[104:107]\n" \
"v_mov_b32 v225, 0\n" \
"v_mov_b32 v228, 0\n" \
"v_mfma_f32_16x16x32_bf16 v[104:107], a[224:227], v[128:131], v[104:107]\n" \
"buffer_load_dwordx4 v234, s[40:43], s71 offen offset:1024 sc1 lds\n" \
"v_exp_f32 v30, v30\n" \
"v_cvt_pk_bf16_f32 v21, v26, v27\n" \
"v_mfma_f32_16x16x32_bf16 v[120:123], a[224:227], v[160:163], v[196:199]\n" \
"ds_read_b128 a[180:183], v239 offset:5120\n" \
"v_mov_b32 v229, 0\n" \
"v_mfma_f32_16x16x32_bf16 v[120:123], a[228:231], v[168:171], v[120:123]\n" \
"v_exp_f32 v31, v31\n" \
"v_mfma_f32_16x16x32_bf16 v[120:123], a[232:235], v[176:179], v[120:123]\n" \
"v_exp_f32 v40, v40\n" \
"v_cvt_pk_bf16_f32 v22, v28, v29\n" \
"v_mfma_f32_16x16x32_bf16 v[120:123], a[236:239], v[184:187], v[120:123]\n" \
"buffer_load_dwordx4 v234, s[40:43], s71 offen offset:2048 sc1 lds\n" \
"v_exp_f32 v41, v41\n" \
"v_mfma_f32_16x16x32_bf16 v[72:75], a[236:239], a[140:143], v[216:219]\n" \
"ds_read_b128 a[184:187], v239 offset:6144\n" \
"v_cvt_pk_bf16_f32 v23, v30, v31\n" \
"v_mfma_f32_16x16x32_bf16 v[72:75], a[232:235], a[136:139], v[72:75]\n" \
"ds_read_b128 a[236:239], v233 offset:13312\n" \
"v_mfma_f32_16x16x32_bf16 v[72:75], a[228:231], a[132:135], v[72:75]\n" \
"v_exp_f32 v42, v42\n" \
"v_mfma_f32_16x16x32_bf16 v[72:75], a[224:227], a[128:131], v[72:75]\n" \
"buffer_load_dwordx4 v234, s[40:43], s71 offen offset:3072 sc1 lds\n" \
"v_exp_f32 v43, v43\n" \
"v_exp_f32 v44, v44\n" \
"v_mfma_f32_16x16x32_bf16 v[76:79], a[240:243], a[128:131], v[220:223]\n" \
"ds_read_b128 a[232:235], v233 offset:5120\n" \
"v_cvt_pk_bf16_f32 v36, v40, v41\n" \
"v_mfma_f32_16x16x32_bf16 v[76:79], a[244:247], a[132:135], v[76:79]\n" \
"ds_read_b128 a[188:191], v239 offset:7168\n" \
"v_cvt_pk_bf16_f32 v37, v42, v43\n" \
"v_mfma_f32_16x16x32_bf16 v[76:79], a[248:251], a[136:139], v[76:79]\n" \
"ds_read_b128 a[228:231], v233 offset:12288\n" \
"v_mfma_f32_16x16x32_bf16 v[76:79], a[252:255], a[140:143], v[76:79]\n" \
"v_exp_f32 v45, v45\n" \
"v_exp_f32 v46, v46\n" \
"v_mfma_f32_16x16x32_bf16 v[124:127], a[252:255], v[184:187], v[196:199]\n" \
"ds_read_b128 a[224:227], v233 offset:4096\n" \
"v_cvt_pk_bf16_f32 v38, v44, v45\n" \
"v_mfma_f32_16x16x32_bf16 v[124:127], a[248:251], v[176:179], v[124:127]\n" \
"v_exp_f32 v47, v47\n" \
"v_mfma_f32_16x16x32_bf16 v[124:127], a[244:247], v[168:171], v[124:127]\n" \
"v_exp_f32 v56, v56\n" \
"v_exp_f32 v57, v57\n" \
"v_mfma_f32_16x16x32_bf16 v[124:127], a[240:243], v[160:163], v[124:127]\n" \
"v_exp_f32 v58, v58\n" \
"v_cvt_pk_bf16_f32 v39, v46, v47\n" \
"v_mfma_f32_16x16x32_bf16 v[108:111], a[240:243], v[128:131], v[196:199]\n" \
"v_exp_f32 v59, v59\n" \
"v_mfma_f32_16x16x32_bf16 v[108:111], a[244:247], v[136:139], v[108:111]\n" \
"v_exp_f32 v60, v60\n" \
"v_mfma_f32_16x16x32_bf16 v[108:111], a[248:251], v[144:147], v[108:111]\n" \
"v_cvt_pk_bf16_f32 v52, v56, v57\n" \
"v_cvt_pk_bf16_f32 v53, v58, v59\n" \
"v_mfma_f32_16x16x32_bf16 v[108:111], a[252:255], v[152:155], v[108:111]\n" \
"v_exp_f32 v61, v61\n" \
"v_exp_f32 v62, v62\n" \
"v_exp_f32 v63, v63\n" \
"v_mfma_f32_16x16x32_bf16 v[92:95], a[252:255], a[156:159], v[196:199]\n" \
"v_add_u32 v232, s91, v237\n" \
"v_cvt_pk_bf16_f32 v54, v60, v61\n" \
"v_mfma_f32_16x16x32_bf16 v[92:95], a[248:251], a[152:155], v[92:95]\n" \
"v_cvt_pk_bf16_f32 v55, v62, v63\n" \
"v_mfma_f32_16x16x32_bf16 v[92:95], a[244:247], a[148:151], v[92:95]\n" \
"s_waitcnt vmcnt(8) lgkmcnt(0)\n" \
"s_barrier\n" \
"v_mfma_f32_16x16x32_bf16 v[92:95], a[240:243], a[144:147], v[92:95]\n" \
"buffer_store_dword v192, v193, s[56:59], s68 offen sc1\n" \
"v_mfma_f32_16x16x32_bf16 v[160:163], a[192:195], v[16:19], 0\n" \
"s_add_u32 m0, s74, s79\n" \
"ds_read_b128 a[240:243], v232 offset:8448\n" \
"v_xor_b32 v233, 0x4000, v233\n" \
"v_mfma_f32_16x16x32_bf16 v[160:163], a[196:199], v[20:23], v[160:163]\n" \
"buffer_load_dwordx4 v234, s[36:39], s73 offen offset:0 sc1 lds\n" \
"v_exp_f32 v80, v80\n" \
"v_mfma_f32_16x16x32_bf16 a[64:67], a[192:195], v[32:35], 0\n" \
"ds_read_b128 a[244:247], v232 offset:10496\n" \
"v_mfma_f32_16x16x32_bf16 a[64:67], a[196:199], v[36:39], a[64:67]\n" \
"ds_read_b128 a[0:3], v248 offset:0\n" \
"v_exp_f32 v81, v81\n" \
"v_mfma_f32_16x16x32_bf16 a[96:99], a[192:195], v[48:51], 0\n" \
"v_exp_f32 v82, v82\n" \
"v_mfma_f32_16x16x32_bf16 a[96:99], a[196:199], v[52:55], a[96:99]\n" \
"s_add_u32 s71, s73, 17408\n" \
"ds_read_b128 a[192:195], v232 offset:0\n" \
"v_cvt_pk_bf16_f32 v80, v80, v81\n" \
"v_mfma_f32_16x16x32_bf16 a[100:103], a[200:203], v[48:51], 0\n" \
"buffer_load_dwordx4 v234, s[36:39], s73 offen offset:1024 sc1 lds\n" \
"v_mfma_f32_16x16x32_bf16 a[100:103], a[204:207], v[52:55], a[100:103]\n" \
"ds_read_b128 a[196:199], v232 offset:2048\n" \
"v_exp_f32 v83, v83\n" \
"v_mfma_f32_16x16x32_bf16 a[68:71], a[200:203], v[32:35], 0\n" \
"ds_read_b128 a[4:7], v248 offset:1024\n" \
"v_mfma_f32_16x16x32_bf16 a[68:71], a[204:207], v[36:39], a[68:71]\n" \
"v_exp_f32 v84, v84\n" \
"v_exp_f32 v85, v85\n" \
"v_mfma_f32_16x16x32_bf16 v[164:167], a[200:203], v[16:19], 0\n" \
"ds_read_b128 a[8:11], v248 offset:2048\n" \
"v_cvt_pk_bf16_f32 v81, v82, v83\n" \
"v_mfma_f32_16x16x32_bf16 v[164:167], a[204:207], v[20:23], v[164:167]\n" \
"buffer_load_dwordx4 v234, s[36:39], s73 offen offset:2048 sc1 lds\n" \
"v_mfma_f32_16x16x32_bf16 v[168:171], a[208:211], v[16:19], 0\n" \
"ds_read_b128 a[200:203], v232 offset:4096\n" \
"v_mfma_f32_16x16x32_bf16 v[168:171], a[212:215], v[20:23], v[168:171]\n" \
"ds_read_b128 a[204:207], v232 offset:6144\n" \
"v_exp_f32 v86, v86\n" \
"v_mfma_f32_16x16x32_bf16 a[72:75], a[208:211], v[32:35], 0\n" \
"ds_read_b128 a[12:15], v248 offset:3072\n" \
"v_cvt_pk_bf16_f32 v82, v84, v85\n" \
"v_mfma_f32_16x16x32_bf16 a[72:75], a[212:215], v[36:39], a[72:75]\n" \
"v_exp_f32 v87, v87\n" \
"v_exp_f32 v96, v96\n" \
"v_mfma_f32_16x16x32_bf16 a[104:107], a[208:211], v[48:51], 0\n" \
"buffer_load_dwordx4 v234, s[36:39], s73 offen offset:3072 sc1 lds\n" \
"v_mfma_f32_16x16x32_bf16 a[104:107], a[212:215], v[52:55], a[104:107]\n" \
"ds_read_b128 a[208:211], v232 offset:256\n" \
"v_cvt_pk_bf16_f32 v83, v86, v87\n" \
"v_smfmac_f32_16x16x64_bf16 a[52:55], v[244:247], v[16:23], v243\n" \
"ds_read_b128 a[212:215], v232 offset:2304\n" \
"v_exp_f32 v97, v97\n" \
"v_mfma_f32_16x16x32_bf16 a[108:111], a[216:219], v[48:51], 0\n" \
"ds_read_b128 a[16:19], v248 offset:4096\n" \
"v_mfma_f32_16x16x32_bf16 a[108:111], a[220:223], v[52:55], a[108:111]\n" \
"s_add_u32 m0, m0, 0x4000\n" \
"v_exp_f32 v98, v98\n" \
"v_mfma_f32_16x16x32_bf16 a[76:79], a[216:219], v[32:35], 0\n" \
"buffer_load_dwordx4 v234, s[36:39], s71 offen offset:0 sc1 lds\n" \
"v_mfma_f32_16x16x32_bf16 a[76:79], a[220:223], v[36:39], a[76:79]\n" \
"ds_read_b128 a[20:23], v248 offset:5120\n" \
"v_cvt_pk_bf16_f32 v96, v96, v97\n" \
"v_exp_f32 v99, v99\n" \
"v_mfma_f32_16x16x32_bf16 v[172:175], a[216:219], v[16:19], 0\n" \
"v_exp_f32 v100, v100\n" \
"v_mfma_f32_16x16x32_bf16 v[172:175], a[220:223], v[20:23], v[172:175]\n" \
"ds_read_b128 a[216:219], v232 offset:4352\n" \
"v_cvt_pk_bf16_f32 v97, v98, v99\n" \
"v_mfma_f32_32x32x16_bf16 v[128:143], a[48:51], a[48:51], 0\n" \
"ds_read_b128 a[220:223], v232 offset:6400\n" \
"v_exp_f32 v101, v101\n" \
"v_mfma_f32_16x16x32_bf16 v[176:179], a[224:227], v[16:19], 0\n" \
"buffer_load_dwordx4 v234, s[36:39], s71 offen offset:1024 sc1 lds\n" \
"v_mfma_f32_16x16x32_bf16 v[176:179], a[228:231], v[20:23], v[176:179]\n" \
"ds_read_b128 a[24:27], v248 offset:6144\n" \
"v_exp_f32 v102, v102\n" \
"v_mfma_f32_16x16x32_bf16 a[80:83], a[224:227], v[32:35], 0\n" \
"v_exp_f32 v103, v103\n" \
"v_mfma_f32_16x16x32_bf16 a[80:83], a[228:231], v[36:39], a[80:83]\n" \
"ds_read_b128 a[28:31], v248 offset:7168\n" \
"v_cvt_pk_bf16_f32 v98, v100, v101\n" \
"v_exp_f32 v112, v112\n" \
"v_mfma_f32_16x16x32_bf16 a[112:115], a[224:227], v[48:51], 0\n" \
"v_exp_f32 v113, v113\n" \
"v_mfma_f32_16x16x32_bf16 a[112:115], a[228:231], v[52:55], a[112:115]\n" \
"buffer_load_dwordx4 v234, s[36:39], s71 offen offset:2048 sc1 lds\n" \
"v_smfmac_f32_16x16x64_bf16 v[224:227], v[244:247], v[32:39], v243\n" \
"ds_read_b128 a[224:227], v232 offset:8192\n" \
"v_cvt_pk_bf16_f32 v99, v102, v103\n" \
"v_mfma_f32_16x16x32_bf16 a[116:119], a[232:235], v[48:51], 0\n" \
"ds_read_b128 a[228:231], v232 offset:10240\n" \
"v_mfma_f32_16x16x32_bf16 a[116:119], a[236:239], v[52:55], a[116:119]\n" \
"v_exp_f32 v114, v114\n" \
"v_exp_f32 v115, v115\n" \
"v_mfma_f32_16x16x32_bf16 a[84:87], a[232:235], v[32:35], 0\n" \
"v_exp_f32 v116, v116\n" \
"v_mfma_f32_16x16x32_bf16 a[84:87], a[236:239], v[36:39], a[84:87]\n" \
"buffer_load_dwordx4 v234, s[36:39], s71 offen offset:3072 sc1 lds\n" \
"v_cvt_pk_bf16_f32 v112, v112, v113\n" \
"v_mfma_f32_16x16x32_bf16 v[180:183], a[232:235], v[16:19], 0\n" \
"v_exp_f32 v117, v117\n" \
"v_mfma_f32_16x16x32_bf16 v[180:183], a[236:239], v[20:23], v[180:183]\n" \
"s_mov_b32 s76, s73\n" \
"s_add_u32 s73, s73, 34816\n" \
"ds_read_b128 a[232:235], v232 offset:12288\n" \
"v_mfma_f32_32x32x16_bf16 v[144:159], a[48:51], a[48:51], 0\n" \
"s_cmp_eq_u32 s73, s83\n" \
"s_cselect_b32 s70, 34816, 0\n" \
"ds_read_b128 a[236:239], v232 offset:14336\n" \
"v_exp_f32 v118, v118\n" \
"v_mfma_f32_16x16x32_bf16 v[184:187], a[32:35], v[16:19], 0\n" \
"s_add_u32 s73, s73, s70\n" \
"s_xor_b32 s74, s74, 0x8000\n" \
"ds_read_b128 a[248:251], v232 offset:12544\n" \
"v_mfma_f32_16x16x32_bf16 v[184:187], a[36:39], v[20:23], v[184:187]\n" \
"s_xor_b32 s77, s77, 0x8000\n" \
"ds_read_b128 a[252:255], v232 offset:14592\n" \
"v_cvt_pk_bf16_f32 v113, v114, v115\n" \
"v_mfma_f32_16x16x32_bf16 a[88:91], a[32:35], v[32:35], 0\n" \
"v_exp_f32 v119, v119\n" \
"v_mfma_f32_16x16x32_bf16 a[88:91], a[36:39], v[36:39], a[88:91]\n" \
"v_exp_f32 v64, v64\n" \
"v_cvt_pk_bf16_f32 v114, v116, v117\n" \
"v_mfma_f32_16x16x32_bf16 a[120:123], a[32:35], v[48:51], 0\n" \
"v_exp_f32 v65, v65\n" \
"v_mfma_f32_16x16x32_bf16 a[120:123], a[36:39], v[52:55], a[120:123]\n" \
"v_exp_f32 v66, v66\n" \
"v_cvt_pk_bf16_f32 v115, v118, v119\n" \
"v_smfmac_f32_16x16x64_bf16 v[228:231], v[244:247], v[48:55], v243\n" \
"v_cvt_pk_bf16_f32 v64, v64, v65\n" \
"v_mfma_f32_16x16x32_bf16 a[124:127], a[40:43], v[48:51], 0\n" \
"v_exp_f32 v67, v67\n" \
"v_mfma_f32_16x16x32_bf16 a[124:127], a[44:47], v[52:55], a[124:127]\n" \
"v_exp_f32 v68, v68\n" \
"v_exp_f32 v69, v69\n" \
"v_mfma_f32_16x16x32_bf16 a[92:95], a[40:43], v[32:35], 0\n" \
"v_exp_f32 v70, v70\n" \
"v_mfma_f32_16x16x32_bf16 a[92:95], a[44:47], v[36:39], a[92:95]\n" \
"v_cvt_pk_bf16_f32 v65, v66, v67\n" \
"v_cvt_pk_bf16_f32 v66, v68, v69\n" \
"v_mfma_f32_16x16x32_bf16 v[188:191], a[40:43], v[16:19], 0\n" \
"s_cmp_eq_u32 s63, 0\n" \
"v_exp_f32 v71, v71\n" \
"v_mfma_f32_16x16x32_bf16 v[188:191], a[44:47], v[20:23], v[188:191]\n" \
"s_waitcnt lgkmcnt(0)\n" \
"v_cvt_pk_bf16_f32 v67, v70, v71\n" \
"s_cbranch_scc1 Lloop2_%=\n" \
"Lloop_%=:\n" \
"v_mfma_f32_16x16x32_bf16 v[0:3], a[192:195], a[128:131], v[196:199]\n" \
"v_exp_f32 v72, v72\n" \
"v_mfma_f32_16x16x32_bf16 v[0:3], a[196:199], a[132:135], v[0:3]\n" \
"v_mfma_f32_16x16x32_bf16 v[0:3], a[200:203], a[136:139], v[0:3]\n" \
"v_exp_f32 v73, v73\n" \
"v_mfma_f32_16x16x32_bf16 v[0:3], a[204:207], a[140:143], v[0:3]\n" \
"ds_read_b128 a[32:35], v233 offset:6144\n" \
"v_cvt_pk_bf16_f32 v68, v72, v73\n" \
"v_mfma_f32_16x16x32_bf16 v[16:19], a[204:207], a[156:159], v[196:199]\n" \
"v_exp_f32 v74, v74\n" \
"v_mfma_f32_16x16x32_bf16 v[16:19], a[200:203], a[152:155], v[16:19]\n" \
"global_load_dword v253, v252, s[44:45]\n" \
"v_mfma_f32_16x16x32_bf16 v[16:19], a[196:199], a[148:151], v[16:19]\n" \
"s_sub_u32 s46, s46, 1\n" \
"v_exp_f32 v75, v75\n" \
"v_mfma_f32_16x16x32_bf16 v[16:19], a[192:195], a[144:147], v[16:19]\n" \
"ds_read_b128 a[36:39], v233 offset:14336\n" \
"v_cvt_pk_bf16_f32 v69, v74, v75\n" \
"v_mfma_f32_16x16x32_bf16 v[32:35], a[192:195], a[160:163], v[196:199]\n" \
"v_exp_f32 v76, v76\n" \
"v_mfma_f32_16x16x32_bf16 v[32:35], a[196:199], a[164:167], v[32:35]\n" \
"s_cmp_gt_i32 s46, 0\n" \
"s_cselect_b32 s47, 0x100000, 0\n" \
"v_mfma_f32_16x16x32_bf16 v[32:35], a[200:203], a[168:171], v[32:35]\n" \
"v_exp_f32 v77, v77\n" \
"v_mfma_f32_16x16x32_bf16 v[32:35], a[204:207], a[172:175], v[32:35]\n" \
"s_add_u32 s44, s44, s47\n" \
"s_addc_u32 s45, s45, 0\n" \
"ds_read_b128 a[40:43], v233 offset:7168\n" \
"v_cvt_pk_bf16_f32 v70, v76, v77\n" \
"v_mfma_f32_16x16x32_bf16 v[48:51], a[204:207], a[188:191], v[196:199]\n" \
"v_exp_f32 v78, v78\n" \
"v_mfma_f32_16x16x32_bf16 v[48:51], a[200:203], a[184:187], v[48:51]\n" \
"v_xor_b32 v232, 0x4000, v232\n" \
"v_mfma_f32_16x16x32_bf16 v[48:51], a[196:199], a[180:183], v[48:51]\n" \
"v_exp_f32 v79, v79\n" \
"v_mfma_f32_16x16x32_bf16 v[48:51], a[192:195], a[176:179], v[48:51]\n" \
"ds_read_b128 a[204:207], v233 offset:9216\n" \
"v_cvt_pk_bf16_f32 v71, v78, v79\n" \
"v_mfma_f32_16x16x32_bf16 v[52:55], a[208:211], a[176:179], v[196:199]\n" \
"v_exp_f32 v88, v88\n" \
"v_mfma_f32_16x16x32_bf16 v[52:55], a[212:215], a[180:183], v[52:55]\n" \
"v_mfma_f32_16x16x32_bf16 v[52:55], a[216:219], a[184:187], v[52:55]\n" \
"v_exp_f32 v89, v89\n" \
"v_mfma_f32_16x16x32_bf16 v[52:55], a[220:223], a[188:191], v[52:55]\n" \
"ds_read_b128 a[200:203], v233 offset:1024\n" \
"v_cvt_pk_bf16_f32 v84, v88, v89\n" \
"v_mfma_f32_16x16x32_bf16 v[36:39], a[220:223], a[172:175], v[196:199]\n" \
"v_exp_f32 v90, v90\n" \
"v_mfma_f32_16x16x32_bf16 v[36:39], a[216:219], a[168:171], v[36:39]\n" \
"v_mfma_f32_16x16x32_bf16 v[36:39], a[212:215], a[164:167], v[36:39]\n" \
"v_exp_f32 v91, v91\n" \
"v_mfma_f32_16x16x32_bf16 v[36:39], a[208:211], a[160:163], v[36:39]\n" \
"ds_read_b128 a[196:199], v233 offset:8192\n" \
"v_cvt_pk_bf16_f32 v85, v90, v91\n" \
"v_mfma_f32_16x16x32_bf16 v[20:23], a[208:211], a[144:147], v[196:199]\n" \
"v_exp_f32 v92, v92\n" \
"v_mfma_f32_16x16x32_bf16 v[20:23], a[212:215], a[148:151], v[20:23]\n" \
"v_mfma_f32_16x16x32_bf16 v[20:23], a[216:219], a[152:155], v[20:23]\n" \
"v_exp_f32 v93, v93\n" \
"v_mfma_f32_16x16x32_bf16 v[20:23], a[220:223], a[156:159], v[20:23]\n" \
"ds_read_b128 a[192:195], v233 offset:0\n" \
"v_cvt_pk_bf16_f32 v86, v92, v93\n" \
"v_mfma_f32_16x16x32_bf16 v[4:7], a[220:223], a[140:143], v[196:199]\n" \
"v_exp_f32 v94, v94\n" \
"v_mfma_f32_16x16x32_bf16 v[4:7], a[216:219], a[136:139], v[4:7]\n" \
"v_mfma_f32_16x16x32_bf16 v[4:7], a[212:215], a[132:135], v[4:7]\n" \
"s_waitcnt lgkmcnt(7)\n" \
"v_exp_f32 v95, v95\n" \
"v_mfma_f32_16x16x32_bf16 v[4:7], a[208:211], a[128:131], v[4:7]\n" \
"ds_read_b128 a[220:223], v233 offset:11264\n" \
"v_cvt_pk_bf16_f32 v87, v94, v95\n" \
"v_mfma_f32_16x16x32_bf16 v[8:11], a[224:227], a[128:131], v[196:199]\n" \
"v_exp_f32 v104, v104\n" \
"v_mfma_f32_16x16x32_bf16 v[8:11], a[228:231], a[132:135], v[8:11]\n" \
"v_mfma_f32_16x16x32_bf16 v[8:11], a[232:235], a[136:139], v[8:11]\n" \
"v_exp_f32 v105, v105\n" \
"v_mfma_f32_16x16x32_bf16 v[8:11], a[236:239], a[140:143], v[8:11]\n" \
"ds_read_b128 a[216:219], v233 offset:3072\n" \
"v_cvt_pk_bf16_f32 v100, v104, v105\n" \
"v_mfma_f32_16x16x32_bf16 v[24:27], a[236:239], a[156:159], v[196:199]\n" \
"v_exp_f32 v106, v106\n" \
"v_mfma_f32_16x16x32_bf16 v[24:27], a[232:235], a[152:155], v[24:27]\n" \
"v_mfma_f32_16x16x32_bf16 v[24:27], a[228:231], a[148:151], v[24:27]\n" \
"v_exp_f32 v107, v107\n" \
"v_mfma_f32_16x16x32_bf16 v[24:27], a[224:227], a[144:147], v[24:27]\n" \
"ds_read_b128 a[212:215], v233 offset:10240\n" \
"v_cvt_pk_bf16_f32 v101, v106, v107\n" \
"v_mfma_f32_16x16x32_bf16 v[40:43], a[224:227], a[160:163], v[196:199]\n" \
"v_exp_f32 v108, v108\n" \
"v_mfma_f32_16x16x32_bf16 v[40:43], a[228:231], a[164:167], v[40:43]\n" \
"v_mfma_f32_16x16x32_bf16 v[40:43], a[232:235], a[168:171], v[40:43]\n" \
"v_exp_f32 v109, v109\n" \
"v_mfma_f32_16x16x32_bf16 v[40:43], a[236:239], a[172:175], v[40:43]\n" \
"ds_read_b128 a[208:211], v233 offset:2048\n" \
"v_cvt_pk_bf16_f32 v102, v108, v109\n" \
"v_mfma_f32_16x16x32_bf16 v[56:59], a[236:239], a[188:191], v[196:199]\n" \
"v_exp_f32 v110, v110\n" \
"v_mfma_f32_16x16x32_bf16 v[56:59], a[232:235], a[184:187], v[56:59]\n" \
"v_mfma_f32_16x16x32_bf16 v[56:59], a[228:231], a[180:183], v[56:59]\n" \
"v_exp_f32 v111, v111\n" \
"v_mfma_f32_16x16x32_bf16 v[56:59], a[224:227], a[176:179], v[56:59]\n" \
"ds_read_b128 a[236:239], v233 offset:13312\n" \
"v_cvt_pk_bf16_f32 v103, v110, v111\n" \
"v_mfma_f32_16x16x32_bf16 v[60:63], a[240:243], a[176:179], v[196:199]\n" \
"v_exp_f32 v120, v120\n" \
"v_mfma_f32_16x16x32_bf16 v[60:63], a[244:247], a[180:183], v[60:63]\n" \
"v_mfma_f32_16x16x32_bf16 v[60:63], a[248:251], a[184:187], v[60:63]\n" \
"v_exp_f32 v121, v121\n" \
"v_mfma_f32_16x16x32_bf16 v[60:63], a[252:255], a[188:191], v[60:63]\n" \
"ds_read_b128 a[232:235], v233 offset:5120\n" \
"v_cvt_pk_bf16_f32 v116, v120, v121\n" \
"v_mfma_f32_16x16x32_bf16 v[44:47], a[252:255], a[172:175], v[196:199]\n" \
"v_exp_f32 v122, v122\n" \
"v_mfma_f32_16x16x32_bf16 v[44:47], a[248:251], a[168:171], v[44:47]\n" \
"v_mfma_f32_16x16x32_bf16 v[44:47], a[244:247], a[164:167], v[44:47]\n" \
"v_exp_f32 v123, v123\n" \
"v_mfma_f32_16x16x32_bf16 v[44:47], a[240:243], a[160:163], v[44:47]\n" \
"ds_read_b128 a[228:231], v233 offset:12288\n" \
"v_cvt_pk_bf16_f32 v117, v122, v123\n" \
"v_mfma_f32_16x16x32_bf16 v[28:31], a[240:243], a[144:147], v[196:199]\n" \
"v_exp_f32 v124, v124\n" \
"v_mfma_f32_16x16x32_bf16 v[28:31], a[244:247], a[148:151], v[28:31]\n" \
"v_mfma_f32_16x16x32_bf16 v[28:31], a[248:251], a[152:155], v[28:31]\n" \
"v_exp_f32 v125, v125\n" \
"v_mfma_f32_16x16x32_bf16 v[28:31], a[252:255], a[156:159], v[28:31]\n" \
"ds_read_b128 a[224:227], v233 offset:4096\n" \
"v_cvt_pk_bf16_f32 v118, v124, v125\n" \
"v_mfma_f32_16x16x32_bf16 v[12:15], a[252:255], a[140:143], v[196:199]\n" \
"v_exp_f32 v126, v126\n" \
"v_mfma_f32_16x16x32_bf16 v[12:15], a[248:251], a[136:139], v[12:15]\n" \
"v_mfma_f32_16x16x32_bf16 v[12:15], a[244:247], a[132:135], v[12:15]\n" \
"s_waitcnt lgkmcnt(2)\n" \
"v_exp_f32 v127, v127\n" \
"v_mfma_f32_16x16x32_bf16 v[12:15], a[240:243], a[128:131], v[12:15]\n" \
"ds_read_b128 a[44:47], v233 offset:15360\n" \
"v_cvt_pk_bf16_f32 v119, v126, v127\n" \
"v_mfma_f32_16x16x32_bf16 v[128:131], a[192:195], v[64:67], v[128:131]\n" \
"Lpg5598_%=:\n" \
"s_cbranch_execz Lpg6787_%=\n" \
"v_exp_f32 v0, v0\n" \
"v_mfma_f32_16x16x32_bf16 v[128:131], a[196:199], v[68:71], v[128:131]\n" \
"ds_read_b128 a[244:247], v232 offset:10496\n" \
"v_mfma_f32_16x16x32_bf16 v[160:163], a[192:195], v[80:83], v[160:163]\n" \
"v_exp_f32 v1, v1\n" \
"v_mfma_f32_16x16x32_bf16 v[160:163], a[196:199], v[84:87], v[160:163]\n" \
"ds_read_b128 a[240:243], v232 offset:8448\n" \
"v_cvt_pk_bf16_f32 v0, v0, v1\n" \
"v_mfma_f32_16x16x32_bf16 a[64:67], a[192:195], v[96:99], a[64:67]\n" \
"v_exp_f32 v2, v2\n" \
"v_mfma_f32_16x16x32_bf16 a[64:67], a[196:199], v[100:103], a[64:67]\n" \
"ds_read_b128 a[248:251], v232 offset:12544\n" \
"v_xor_b32 v233, 0xc000, v233\n" \
"v_mfma_f32_16x16x32_bf16 a[96:99], a[192:195], v[112:115], a[96:99]\n" \
"v_exp_f32 v3, v3\n" \
"v_mfma_f32_16x16x32_bf16 a[96:99], a[196:199], v[116:119], a[96:99]\n" \
"ds_read_b128 a[192:195], v232 offset:0\n" \
"v_cvt_pk_bf16_f32 v1, v2, v3\n" \
"v_mfma_f32_16x16x32_bf16 a[100:103], a[200:203], v[112:115], a[100:103]\n" \
"v_exp_f32 v4, v4\n" \
"v_mfma_f32_16x16x32_bf16 a[100:103], a[204:207], v[116:119], a[100:103]\n" \
"ds_read_b128 a[252:255], v232 offset:14592\n" \
"v_mfma_f32_16x16x32_bf16 a[68:71], a[200:203], v[96:99], a[68:71]\n" \
"v_exp_f32 v5, v5\n" \
"v_mfma_f32_16x16x32_bf16 a[68:71], a[204:207], v[100:103], a[68:71]\n" \
"ds_read_b128 a[196:199], v232 offset:2048\n" \
"v_cvt_pk_bf16_f32 v2, v4, v5\n" \
"v_smfmac_f32_16x16x64_bf16 a[48:51], v[244:247], v[64:71], v243\n" \
"v_mfma_f32_16x16x32_bf16 v[164:167], a[200:203], v[80:83], v[164:167]\n" \
"v_exp_f32 v6, v6\n" \
"v_mfma_f32_16x16x32_bf16 v[164:167], a[204:207], v[84:87], v[164:167]\n" \
"v_mfma_f32_16x16x32_bf16 v[132:135], a[200:203], v[64:67], v[132:135]\n" \
"v_exp_f32 v7, v7\n" \
"v_mfma_f32_16x16x32_bf16 v[132:135], a[204:207], v[68:71], v[132:135]\n" \
"ds_read_b128 a[200:203], v232 offset:4096\n" \
"v_cvt_pk_bf16_f32 v3, v6, v7\n" \
"v_mfma_f32_16x16x32_bf16 v[136:139], a[208:211], v[64:67], v[136:139]\n" \
"v_exp_f32 v16, v16\n" \
"v_mfma_f32_16x16x32_bf16 v[136:139], a[212:215], v[68:71], v[136:139]\n" \
"v_mfma_f32_16x16x32_bf16 v[168:171], a[208:211], v[80:83], v[168:171]\n" \
"v_exp_f32 v17, v17\n" \
"v_mfma_f32_16x16x32_bf16 v[168:171], a[212:215], v[84:87], v[168:171]\n" \
"ds_read_b128 a[204:207], v232 offset:6144\n" \
"v_cvt_pk_bf16_f32 v16, v16, v17\n" \
"v_mfma_f32_16x16x32_bf16 a[72:75], a[208:211], v[96:99], a[72:75]\n" \
"v_exp_f32 v18, v18\n" \
"v_mfma_f32_16x16x32_bf16 a[72:75], a[212:215], v[100:103], a[72:75]\n" \
"v_mfma_f32_16x16x32_bf16 a[104:107], a[208:211], v[112:115], a[104:107]\n" \
"v_exp_f32 v19, v19\n" \
"v_mfma_f32_16x16x32_bf16 a[104:107], a[212:215], v[116:119], a[104:107]\n" \
"ds_read_b128 a[208:211], v232 offset:256\n" \
"v_cvt_pk_bf16_f32 v17, v18, v19\n" \
"v_mfma_f32_16x16x32_bf16 a[108:111], a[216:219], v[112:115], a[108:111]\n" \
"v_exp_f32 v20, v20\n" \
"v_mfma_f32_16x16x32_bf16 a[108:111], a[220:223], v[116:119], a[108:111]\n" \
"v_mfma_f32_16x16x32_bf16 a[76:79], a[216:219], v[96:99], a[76:79]\n" \
"v_exp_f32 v21, v21\n" \
"v_mfma_f32_16x16x32_bf16 a[76:79], a[220:223], v[100:103], a[76:79]\n" \
"ds_read_b128 a[212:215], v232 offset:2304\n" \
"v_cvt_pk_bf16_f32 v18, v20, v21\n" \
"v_smfmac_f32_16x16x64_bf16 a[52:55], v[244:247], v[80:87], v243\n" \
"v_mfma_f32_16x16x32_bf16 v[172:175], a[216:219], v[80:83], v[172:175]\n" \
"v_exp_f32 v22, v22\n" \
"v_mfma_f32_16x16x32_bf16 v[172:175], a[220:223], v[84:87], v[172:175]\n" \
"v_mfma_f32_16x16x32_bf16 v[140:143], a[216:219], v[64:67], v[140:143]\n" \
"s_waitcnt lgkmcnt(10)\n" \
"v_exp_f32 v23, v23\n" \
"v_mfma_f32_16x16x32_bf16 v[140:143], a[220:223], v[68:71], v[140:143]\n" \
"ds_read_b128 a[216:219], v232 offset:4352\n" \
"v_cvt_pk_bf16_f32 v19, v22, v23\n" \
"v_mfma_f32_16x16x32_bf16 v[144:147], a[224:227], v[64:67], v[144:147]\n" \
"v_exp_f32 v32, v32\n" \
"v_mfma_f32_16x16x32_bf16 v[144:147], a[228:231], v[68:71], v[144:147]\n" \
"v_mfma_f32_16x16x32_bf16 v[176:179], a[224:227], v[80:83], v[176:179]\n" \
"v_exp_f32 v33, v33\n" \
"v_mfma_f32_16x16x32_bf16 v[176:179], a[228:231], v[84:87], v[176:179]\n" \
"ds_read_b128 a[220:223], v232 offset:6400\n" \
"v_cvt_pk_bf16_f32 v32, v32, v33\n" \
"v_mfma_f32_16x16x32_bf16 a[80:83], a[224:227], v[96:99], a[80:83]\n" \
"v_exp_f32 v34, v34\n" \
"v_mfma_f32_16x16x32_bf16 a[80:83], a[228:231], v[100:103], a[80:83]\n" \
"v_mfma_f32_16x16x32_bf16 a[112:115], a[224:227], v[112:115], a[112:115]\n" \
"v_exp_f32 v35, v35\n" \
"v_mfma_f32_16x16x32_bf16 a[112:115], a[228:231], v[116:119], a[112:115]\n" \
"ds_read_b128 a[224:227], v232 offset:8192\n" \
"v_cvt_pk_bf16_f32 v33, v34, v35\n" \
"v_mfma_f32_16x16x32_bf16 a[116:119], a[232:235], v[112:115], a[116:119]\n" \
"v_exp_f32 v36, v36\n" \
"v_mfma_f32_16x16x32_bf16 a[116:119], a[236:239], v[116:119], a[116:119]\n" \
"v_mfma_f32_16x16x32_bf16 a[84:87], a[232:235], v[96:99], a[84:87]\n" \
"v_exp_f32 v37, v37\n" \
"v_mfma_f32_16x16x32_bf16 a[84:87], a[236:239], v[100:103], a[84:87]\n" \
"ds_read_b128 a[228:231], v232 offset:10240\n" \
"v_cvt_pk_bf16_f32 v34, v36, v37\n" \
"v_smfmac_f32_16x16x64_bf16 v[224:227], v[244:247], v[96:103], v243\n" \
"v_mfma_f32_16x16x32_bf16 v[180:183], a[232:235], v[80:83], v[180:183]\n" \
"v_exp_f32 v38, v38\n" \
"v_mfma_f32_16x16x32_bf16 v[180:183], a[236:239], v[84:87], v[180:183]\n" \
"v_mfma_f32_16x16x32_bf16 v[148:151], a[232:235], v[64:67], v[148:151]\n" \
"v_exp_f32 v39, v39\n" \
"v_mfma_f32_16x16x32_bf16 v[148:151], a[236:239], v[68:71], v[148:151]\n" \
"ds_read_b128 a[232:235], v232 offset:12288\n" \
"v_cvt_pk_bf16_f32 v35, v38, v39\n" \
"v_mfma_f32_16x16x32_bf16 v[152:155], a[32:35], v[64:67], v[152:155]\n" \
"v_exp_f32 v48, v48\n" \
"v_mfma_f32_16x16x32_bf16 v[152:155], a[36:39], v[68:71], v[152:155]\n" \
"v_mfma_f32_16x16x32_bf16 v[184:187], a[32:35], v[80:83], v[184:187]\n" \
"v_exp_f32 v49, v49\n" \
"v_mfma_f32_16x16x32_bf16 v[184:187], a[36:39], v[84:87], v[184:187]\n" \
"ds_read_b128 a[236:239], v232 offset:14336\n" \
"v_cvt_pk_bf16_f32 v48, v48, v49\n" \
"v_mfma_f32_16x16x32_bf16 a[88:91], a[32:35], v[96:99], a[88:91]\n" \
"v_exp_f32 v50, v50\n" \
"v_mfma_f32_16x16x32_bf16 a[88:91], a[36:39], v[100:103], a[88:91]\n" \
"v_mfma_f32_16x16x32_bf16 a[120:123], a[32:35], v[112:115], a[120:123]\n" \
"v_exp_f32 v51, v51\n" \
"v_mfma_f32_16x16x32_bf16 a[120:123], a[36:39], v[116:119], a[120:123]\n" \
"v_cvt_pk_bf16_f32 v49, v50, v51\n" \
"v_mfma_f32_16x16x32_bf16 a[124:127], a[40:43], v[112:115], a[124:127]\n" \
"v_exp_f32 v52, v52\n" \
"v_mfma_f32_16x16x32_bf16 a[124:127], a[44:47], v[116:119], a[124:127]\n" \
"v_mfma_f32_16x16x32_bf16 a[92:95], a[40:43], v[96:99], a[92:95]\n" \
"v_exp_f32 v53, v53\n" \
"v_mfma_f32_16x16x32_bf16 a[92:95], a[44:47], v[100:103], a[92:95]\n" \
"v_cvt_pk_bf16_f32 v50, v52, v53\n" \
"v_smfmac_f32_16x16x64_bf16 v[228:231], v[244:247], v[112:119], v243\n" \
"v_mfma_f32_16x16x32_bf16 v[188:191], a[40:43], v[80:83], v[188:191]\n" \
"v_exp_f32 v54, v54\n" \
"v_mfma_f32_16x16x32_bf16 v[188:191], a[44:47], v[84:87], v[188:191]\n" \
"v_mfma_f32_16x16x32_bf16 v[156:159], a[40:43], v[64:67], v[156:159]\n" \
"s_waitcnt vmcnt(1) lgkmcnt(0)\n" \
"v_exp_f32 v55, v55\n" \
"v_mfma_f32_16x16x32_bf16 v[156:159], a[44:47], v[68:71], v[156:159]\n" \
"s_barrier\n" \
"v_cvt_pk_bf16_f32 v51, v54, v55\n" \
"v_mfma_f32_16x16x32_bf16 v[64:67], a[192:195], a[128:131], v[196:199]\n" \
"v_exp_f32 v8, v8\n" \
"v_mfma_f32_16x16x32_bf16 v[64:67], a[196:199], a[132:135], v[64:67]\n" \
"v_cmp_eq_u32 vcc, s60, v239\n" \
"v_mfma_f32_16x16x32_bf16 v[64:67], a[200:203], a[136:139], v[64:67]\n" \
"v_exp_f32 v9, v9\n" \
"v_mfma_f32_16x16x32_bf16 v[64:67], a[204:207], a[140:143], v[64:67]\n" \
"s_andn2_b32 s70, s69, vcc_lo\n" \
"s_cbranch_scc1 Lpl2_%=\n" \
"Lpr2_%=:\n" \
"ds_read_b128 a[32:35], v233 offset:6144\n" \
"v_cvt_pk_bf16_f32 v4, v8, v9\n" \
"v_mfma_f32_16x16x32_bf16 v[80:83], a[204:207], a[156:159], v[196:199]\n" \
"s_add_u32 m0, s77, s80\n" \
"v_exp_f32 v10, v10\n" \
"v_mfma_f32_16x16x32_bf16 v[80:83], a[200:203], a[152:155], v[80:83]\n" \
"buffer_load_dwordx4 v234, s[40:43], s76 offen offset:0 sc1 lds\n" \
"v_mfma_f32_16x16x32_bf16 v[80:83], a[196:199], a[148:151], v[80:83]\n" \
"s_sub_u32 s89, s89, 1\n" \
"v_exp_f32 v11, v11\n" \
"v_mfma_f32_16x16x32_bf16 v[80:83], a[192:195], a[144:147], v[80:83]\n" \
"s_cmp_eq_u32 s89, 0\n" \
"s_cselect_b32 s70, s78, 0xc000\n" \
"ds_read_b128 a[36:39], v233 offset:14336\n" \
"v_cvt_pk_bf16_f32 v5, v10, v11\n" \
"v_mfma_f32_16x16x32_bf16 v[96:99], a[192:195], a[160:163], v[196:199]\n" \
"s_add_u32 s71, s76, 17408\n" \
"v_exp_f32 v12, v12\n" \
"v_mfma_f32_16x16x32_bf16 v[96:99], a[196:199], a[164:167], v[96:99]\n" \
"buffer_load_dwordx4 v234, s[40:43], s76 offen offset:1024 sc1 lds\n" \
"v_mfma_f32_16x16x32_bf16 v[96:99], a[200:203], a[168:171], v[96:99]\n" \
"v_exp_f32 v13, v13\n" \
"v_mfma_f32_16x16x32_bf16 v[96:99], a[204:207], a[172:175], v[96:99]\n" \
"ds_read_b128 a[40:43], v233 offset:7168\n" \
"v_cvt_pk_bf16_f32 v6, v12, v13\n" \
"v_mfma_f32_16x16x32_bf16 v[112:115], a[204:207], a[188:191], v[196:199]\n" \
"v_exp_f32 v14, v14\n" \
"v_mfma_f32_16x16x32_bf16 v[112:115], a[200:203], a[184:187], v[112:115]\n" \
"v_xor_b32 v232, s70, v232\n" \
"v_mfma_f32_16x16x32_bf16 v[112:115], a[196:199], a[180:183], v[112:115]\n" \
"v_exp_f32 v15, v15\n" \
"v_mfma_f32_16x16x32_bf16 v[112:115], a[192:195], a[176:179], v[112:115]\n" \
"ds_read_b128 a[204:207], v233 offset:9216\n" \
"v_cvt_pk_bf16_f32 v7, v14, v15\n" \
"v_mfma_f32_16x16x32_bf16 v[116:119], a[208:211], a[176:179], v[196:199]\n" \
"v_exp_f32 v24, v24\n" \
"v_mfma_f32_16x16x32_bf16 v[116:119], a[212:215], a[180:183], v[116:119]\n" \
"buffer_load_dwordx4 v234, s[40:43], s76 offen offset:2048 sc1 lds\n" \
"v_mfma_f32_16x16x32_bf16 v[116:119], a[216:219], a[184:187], v[116:119]\n" \
"v_exp_f32 v25, v25\n" \
"v_mfma_f32_16x16x32_bf16 v[116:119], a[220:223], a[188:191], v[116:119]\n" \
"ds_read_b128 a[200:203], v233 offset:1024\n" \
"v_cvt_pk_bf16_f32 v20, v24, v25\n" \
"v_mfma_f32_16x16x32_bf16 v[100:103], a[220:223], a[172:175], v[196:199]\n" \
"v_exp_f32 v26, v26\n" \
"v_mfma_f32_16x16x32_bf16 v[100:103], a[216:219], a[168:171], v[100:103]\n" \
"buffer_load_dwordx4 v234, s[40:43], s76 offen offset:3072 sc1 lds\n" \
"v_mfma_f32_16x16x32_bf16 v[100:103], a[212:215], a[164:167], v[100:103]\n" \
"v_exp_f32 v27, v27\n" \
"v_mfma_f32_16x16x32_bf16 v[100:103], a[208:211], a[160:163], v[100:103]\n" \
"ds_read_b128 a[196:199], v233 offset:8192\n" \
"v_cvt_pk_bf16_f32 v21, v26, v27\n" \
"v_mfma_f32_16x16x32_bf16 v[84:87], a[208:211], a[144:147], v[196:199]\n" \
"v_exp_f32 v28, v28\n" \
"v_mfma_f32_16x16x32_bf16 v[84:87], a[212:215], a[148:151], v[84:87]\n" \
"v_mfma_f32_16x16x32_bf16 v[84:87], a[216:219], a[152:155], v[84:87]\n" \
"v_exp_f32 v29, v29\n" \
"v_mfma_f32_16x16x32_bf16 v[84:87], a[220:223], a[156:159], v[84:87]\n" \
"ds_read_b128 a[192:195], v233 offset:0\n" \
"v_cvt_pk_bf16_f32 v22, v28, v29\n" \
"v_mfma_f32_16x16x32_bf16 v[68:71], a[220:223], a[140:143], v[196:199]\n" \
"s_add_u32 m0, m0, 0x4000\n" \
"v_exp_f32 v30, v30\n" \
"v_mfma_f32_16x16x32_bf16 v[68:71], a[216:219], a[136:139], v[68:71]\n" \
"buffer_load_dwordx4 v234, s[40:43], s71 offen offset:0 sc1 lds\n" \
"v_mfma_f32_16x16x32_bf16 v[68:71], a[212:215], a[132:135], v[68:71]\n" \
"v_exp_f32 v31, v31\n" \
"v_mfma_f32_16x16x32_bf16 v[68:71], a[208:211], a[128:131], v[68:71]\n" \
"ds_read_b128 a[220:223], v233 offset:11264\n" \
"v_cvt_pk_bf16_f32 v23, v30, v31\n" \
"v_mfma_f32_16x16x32_bf16 v[72:75], a[224:227], a[128:131], v[196:199]\n" \
"v_exp_f32 v40, v40\n" \
"v_mfma_f32_16x16x32_bf16 v[72:75], a[228:231], a[132:135], v[72:75]\n" \
"buffer_load_dwordx4 v234, s[40:43], s71 offen offset:1024 sc1 lds\n" \
"v_mfma_f32_16x16x32_bf16 v[72:75], a[232:235], a[136:139], v[72:75]\n" \
"v_exp_f32 v41, v41\n" \
"v_mfma_f32_16x16x32_bf16 v[72:75], a[236:239], a[140:143], v[72:75]\n" \
"ds_read_b128 a[216:219], v233 offset:3072\n" \
"v_cvt_pk_bf16_f32 v36, v40, v41\n" \
"v_mfma_f32_16x16x32_bf16 v[88:91], a[236:239], a[156:159], v[196:199]\n" \
"v_exp_f32 v42, v42\n" \
"v_mfma_f32_16x16x32_bf16 v[88:91], a[232:235], a[152:155], v[88:91]\n" \
"v_mfma_f32_16x16x32_bf16 v[88:91], a[228:231], a[148:151], v[88:91]\n" \
"v_exp_f32 v43, v43\n" \
"v_mfma_f32_16x16x32_bf16 v[88:91], a[224:227], a[144:147], v[88:91]\n" \
"ds_read_b128 a[212:215], v233 offset:10240\n" \
"v_cvt_pk_bf16_f32 v37, v42, v43\n" \
"v_mfma_f32_16x16x32_bf16 v[104:107], a[224:227], a[160:163], v[196:199]\n" \
"v_exp_f32 v44, v44\n" \
"v_mfma_f32_16x16x32_bf16 v[104:107], a[228:231], a[164:167], v[104:107]\n" \
"buffer_load_dwordx4 v234, s[40:43], s71 offen offset:2048 sc1 lds\n" \
"v_mfma_f32_16x16x32_bf16 v[104:107], a[232:235], a[168:171], v[104:107]\n" \
"v_exp_f32 v45, v45\n" \
"v_mfma_f32_16x16x32_bf16 v[104:107], a[236:239], a[172:175], v[104:107]\n" \
"ds_read_b128 a[208:211], v233 offset:2048\n" \
"v_cvt_pk_bf16_f32 v38, v44, v45\n" \
"v_mfma_f32_16x16x32_bf16 v[120:123], a[236:239], a[188:191], v[196:199]\n" \
"v_exp_f32 v46, v46\n" \
"v_mfma_f32_16x16x32_bf16 v[120:123], a[232:235], a[184:187], v[120:123]\n" \
"buffer_load_dwordx4 v234, s[40:43], s71 offen offset:3072 sc1 lds\n" \
"v_mfma_f32_16x16x32_bf16 v[120:123], a[228:231], a[180:183], v[120:123]\n" \
"v_exp_f32 v47, v47\n" \
"v_mfma_f32_16x16x32_bf16 v[120:123], a[224:227], a[176:179], v[120:123]\n" \
"ds_read_b128 a[236:239], v233 offset:13312\n" \
"v_cvt_pk_bf16_f32 v39, v46, v47\n" \
"v_mfma_f32_16x16x32_bf16 v[124:127], a[240:243], a[176:179], v[196:199]\n" \
"v_exp_f32 v56, v56\n" \
"v_mfma_f32_16x16x32_bf16 v[124:127], a[244:247], a[180:183], v[124:127]\n" \
"v_mfma_f32_16x16x32_bf16 v[124:127], a[248:251], a[184:187], v[124:127]\n" \
"v_exp_f32 v57, v57\n" \
"v_mfma_f32_16x16x32_bf16 v[124:127], a[252:255], a[188:191], v[124:127]\n" \
"ds_read_b128 a[232:235], v233 offset:5120\n" \
"v_cvt_pk_bf16_f32 v52, v56, v57\n" \
"v_mfma_f32_16x16x32_bf16 v[108:111], a[252:255], a[172:175], v[196:199]\n" \
"v_exp_f32 v58, v58\n" \
"v_mfma_f32_16x16x32_bf16 v[108:111], a[248:251], a[168:171], v[108:111]\n" \
"v_mfma_f32_16x16x32_bf16 v[108:111], a[244:247], a[164:167], v[108:111]\n" \
"v_exp_f32 v59, v59\n" \
"v_mfma_f32_16x16x32_bf16 v[108:111], a[240:243], a[160:163], v[108:111]\n" \
"ds_read_b128 a[228:231], v233 offset:12288\n" \
"v_cvt_pk_bf16_f32 v53, v58, v59\n" \
"v_mfma_f32_16x16x32_bf16 v[92:95], a[240:243], a[144:147], v[196:199]\n" \
"v_exp_f32 v60, v60\n" \
"v_mfma_f32_16x16x32_bf16 v[92:95], a[244:247], a[148:151], v[92:95]\n" \
"v_mfma_f32_16x16x32_bf16 v[92:95], a[248:251], a[152:155], v[92:95]\n" \
"v_exp_f32 v61, v61\n" \
"v_mfma_f32_16x16x32_bf16 v[92:95], a[252:255], a[156:159], v[92:95]\n" \
"ds_read_b128 a[224:227], v233 offset:4096\n" \
"v_cvt_pk_bf16_f32 v54, v60, v61\n" \
"v_mfma_f32_16x16x32_bf16 v[76:79], a[252:255], a[140:143], v[196:199]\n" \
"v_exp_f32 v62, v62\n" \
"v_mfma_f32_16x16x32_bf16 v[76:79], a[248:251], a[136:139], v[76:79]\n" \
"v_mfma_f32_16x16x32_bf16 v[76:79], a[244:247], a[132:135], v[76:79]\n" \
"s_waitcnt lgkmcnt(2)\n" \
"v_exp_f32 v63, v63\n" \
"v_mfma_f32_16x16x32_bf16 v[76:79], a[240:243], a[128:131], v[76:79]\n" \
"ds_read_b128 a[44:47], v233 offset:15360\n" \
"v_cvt_pk_bf16_f32 v55, v62, v63\n" \
"v_mfma_f32_16x16x32_bf16 v[128:131], a[192:195], v[0:3], v[128:131]\n" \
"s_add_u32 m0, s74, s79\n" \
"v_exp_f32 v64, v64\n" \
"v_mfma_f32_16x16x32_bf16 v[128:131], a[196:199], v[4:7], v[128:131]\n" \
"buffer_load_dwordx4 v234, s[36:39], s73 offen offset:0 sc1 lds\n" \
"v_mfma_f32_16x16x32_bf16 v[160:163], a[192:195], v[16:19], v[160:163]\n" \
"v_exp_f32 v65, v65\n" \
"v_mfma_f32_16x16x32_bf16 v[160:163], a[196:199], v[20:23], v[160:163]\n" \
"ds_read_b128 a[240:243], v232 offset:8448\n" \
"v_cvt_pk_bf16_f32 v64, v64, v65\n" \
"v_mfma_f32_16x16x32_bf16 a[64:67], a[192:195], v[32:35], a[64:67]\n" \
"s_add_u32 s71, s73, 17408\n" \
"v_exp_f32 v66, v66\n" \
"v_mfma_f32_16x16x32_bf16 a[64:67], a[196:199], v[36:39], a[64:67]\n" \
"buffer_load_dwordx4 v234, s[36:39], s73 offen offset:1024 sc1 lds\n" \
"v_mfma_f32_16x16x32_bf16 a[96:99], a[192:195], v[48:51], a[96:99]\n" \
"v_exp_f32 v67, v67\n" \
"v_mfma_f32_16x16x32_bf16 a[96:99], a[196:199], v[52:55], a[96:99]\n" \
"ds_read_b128 a[192:195], v232 offset:0\n" \
"v_cvt_pk_bf16_f32 v65, v66, v67\n" \
"v_mfma_f32_16x16x32_bf16 a[100:103], a[200:203], v[48:51], a[100:103]\n" \
"v_exp_f32 v68, v68\n" \
"v_mfma_f32_16x16x32_bf16 a[100:103], a[204:207], v[52:55], a[100:103]\n" \
"v_xor_b32 v233, 0x4000, v233\n" \
"v_mfma_f32_16x16x32_bf16 a[68:71], a[200:203], v[32:35], a[68:71]\n" \
"v_exp_f32 v69, v69\n" \
"v_mfma_f32_16x16x32_bf16 a[68:71], a[204:207], v[36:39], a[68:71]\n" \
"ds_read_b128 a[196:199], v232 offset:2048\n" \
"v_cvt_pk_bf16_f32 v66, v68, v69\n" \
"v_smfmac_f32_16x16x64_bf16 a[48:51], v[244:247], v[0:7], v243\n" \
"buffer_load_dwordx4 v234, s[36:39], s73 offen offset:2048 sc1 lds\n" \
"v_mfma_f32_16x16x32_bf16 v[164:167], a[200:203], v[16:19], v[164:167]\n" \
"v_exp_f32 v70, v70\n" \
"v_mfma_f32_16x16x32_bf16 v[164:167], a[204:207], v[20:23], v[164:167]\n" \
"v_mfma_f32_16x16x32_bf16 v[132:135], a[200:203], v[0:3], v[132:135]\n" \
"v_exp_f32 v71, v71\n" \
"v_mfma_f32_16x16x32_bf16 v[132:135], a[204:207], v[4:7], v[132:135]\n" \
"ds_read_b128 a[200:203], v232 offset:4096\n" \
"v_cvt_pk_bf16_f32 v67, v70, v71\n" \
"v_mfma_f32_16x16x32_bf16 v[136:139], a[208:211], v[0:3], v[136:139]\n" \
"v_exp_f32 v80, v80\n" \
"v_mfma_f32_16x16x32_bf16 v[136:139], a[212:215], v[4:7], v[136:139]\n" \
"buffer_load_dwordx4 v234, s[36:39], s73 offen offset:3072 sc1 lds\n" \
"v_mfma_f32_16x16x32_bf16 v[168:171], a[208:211], v[16:19], v[168:171]\n" \
"v_exp_f32 v81, v81\n" \
"v_mfma_f32_16x16x32_bf16 v[168:171], a[212:215], v[20:23], v[168:171]\n" \
"ds_read_b128 a[204:207], v232 offset:6144\n" \
"v_cvt_pk_bf16_f32 v80, v80, v81\n" \
"v_mfma_f32_16x16x32_bf16 a[72:75], a[208:211], v[32:35], a[72:75]\n" \
"v_exp_f32 v82, v82\n" \
"v_mfma_f32_16x16x32_bf16 a[72:75], a[212:215], v[36:39], a[72:75]\n" \
"v_mfma_f32_16x16x32_bf16 a[104:107], a[208:211], v[48:51], a[104:107]\n" \
"v_exp_f32 v83, v83\n" \
"v_mfma_f32_16x16x32_bf16 a[104:107], a[212:215], v[52:55], a[104:107]\n" \
"ds_read_b128 a[208:211], v232 offset:256\n" \
"v_cvt_pk_bf16_f32 v81, v82, v83\n" \
"v_mfma_f32_16x16x32_bf16 a[108:111], a[216:219], v[48:51], a[108:111]\n" \
"s_add_u32 m0, m0, 0x4000\n" \
"v_exp_f32 v84, v84\n" \
"v_mfma_f32_16x16x32_bf16 a[108:111], a[220:223], v[52:55], a[108:111]\n" \
"buffer_load_dwordx4 v234, s[36:39], s71 offen offset:0 sc1 lds\n" \
"v_mfma_f32_16x16x32_bf16 a[76:79], a[216:219], v[32:35], a[76:79]\n" \
"v_exp_f32 v85, v85\n" \
"v_mfma_f32_16x16x32_bf16 a[76:79], a[220:223], v[36:39], a[76:79]\n" \
"ds_read_b128 a[212:215], v232 offset:2304\n" \
"v_cvt_pk_bf16_f32 v82, v84, v85\n" \
"v_smfmac_f32_16x16x64_bf16 a[52:55], v[244:247], v[16:23], v243\n" \
"v_mfma_f32_16x16x32_bf16 v[172:175], a[216:219], v[16:19], v[172:175]\n" \
"v_exp_f32 v86, v86\n" \
"v_mfma_f32_16x16x32_bf16 v[172:175], a[220:223], v[20:23], v[172:175]\n" \
"buffer_load_dwordx4 v234, s[36:39], s71 offen offset:1024 sc1 lds\n" \
"v_mfma_f32_16x16x32_bf16 v[140:143], a[216:219], v[0:3], v[140:143]\n" \
"s_waitcnt lgkmcnt(7)\n" \
"v_exp_f32 v87, v87\n" \
"v_mfma_f32_16x16x32_bf16 v[140:143], a[220:223], v[4:7], v[140:143]\n" \
"ds_read_b128 a[216:219], v232 offset:4352\n" \
"v_cvt_pk_bf16_f32 v83, v86, v87\n" \
"v_mfma_f32_16x16x32_bf16 v[144:147], a[224:227], v[0:3], v[144:147]\n" \
"v_exp_f32 v96, v96\n" \
"v_mfma_f32_16x16x32_bf16 v[144:147], a[228:231], v[4:7], v[144:147]\n" \
"buffer_load_dwordx4 v234, s[36:39], s71 offen offset:2048 sc1 lds\n" \
"v_mfma_f32_16x16x32_bf16 v[176:179], a[224:227], v[16:19], v[176:179]\n" \
"v_exp_f32 v97, v97\n" \
"v_mfma_f32_16x16x32_bf16 v[176:179], a[228:231], v[20:23], v[176:179]\n" \
"ds_read_b128 a[220:223], v232 offset:6400\n" \
"v_cvt_pk_bf16_f32 v96, v96, v97\n" \
"v_mfma_f32_16x16x32_bf16 a[80:83], a[224:227], v[32:35], a[80:83]\n" \
"v_exp_f32 v98, v98\n" \
"v_mfma_f32_16x16x32_bf16 a[80:83], a[228:231], v[36:39], a[80:83]\n" \
"v_mfma_f32_16x16x32_bf16 a[112:115], a[224:227], v[48:51], a[112:115]\n" \
"v_exp_f32 v99, v99\n" \
"v_mfma_f32_16x16x32_bf16 a[112:115], a[228:231], v[52:55], a[112:115]\n" \
"ds_read_b128 a[224:227], v232 offset:8192\n" \
"v_cvt_pk_bf16_f32 v97, v98, v99\n" \
"v_mfma_f32_16x16x32_bf16 a[116:119], a[232:235], v[48:51], a[116:119]\n" \
"v_exp_f32 v100, v100\n" \
"v_mfma_f32_16x16x32_bf16 a[116:119], a[236:239], v[52:55], a[116:119]\n" \
"buffer_load_dwordx4 v234, s[36:39], s71 offen offset:3072 sc1 lds\n" \
"v_mfma_f32_16x16x32_bf16 a[84:87], a[232:235], v[32:35], a[84:87]\n" \
"v_exp_f32 v101, v101\n" \
"v_mfma_f32_16x16x32_bf16 a[84:87], a[236:239], v[36:39], a[84:87]\n" \
"ds_read_b128 a[228:231], v232 offset:10240\n" \
"v_cvt_pk_bf16_f32 v98, v100, v101\n" \
"v_smfmac_f32_16x16x64_bf16 v[224:227], v[244:247], v[32:39], v243\n" \
"v_mfma_f32_16x16x32_bf16 v[180:183], a[232:235], v[16:19], v[180:183]\n" \
"v_exp_f32 v102, v102\n" \
"v_mfma_f32_16x16x32_bf16 v[180:183], a[236:239], v[20:23], v[180:183]\n" \
"buffer_load_dword v239, v242, s[56:59], s95 offen sc1\n" \
"v_mfma_f32_16x16x32_bf16 v[148:151], a[232:235], v[0:3], v[148:151]\n" \
"s_mov_b32 s69, s92\n" \
"v_exp_f32 v103, v103\n" \
"v_mfma_f32_16x16x32_bf16 v[148:151], a[236:239], v[4:7], v[148:151]\n" \
"s_mov_b32 s76, s73\n" \
"ds_read_b128 a[232:235], v232 offset:12288\n" \
"v_cvt_pk_bf16_f32 v99, v102, v103\n" \
"v_mfma_f32_16x16x32_bf16 v[152:155], a[32:35], v[0:3], v[152:155]\n" \
"s_add_u32 s73, s73, 34816\n" \
"v_exp_f32 v112, v112\n" \
"v_mfma_f32_16x16x32_bf16 v[152:155], a[36:39], v[4:7], v[152:155]\n" \
"s_cmp_eq_u32 s73, s83\n" \
"s_cselect_b32 s70, 34816, 0\n" \
"s_add_u32 s73, s73, s70\n" \
"v_mfma_f32_16x16x32_bf16 v[184:187], a[32:35], v[16:19], v[184:187]\n" \
"s_xor_b32 s74, s74, 0x8000\n" \
"v_exp_f32 v113, v113\n" \
"v_mfma_f32_16x16x32_bf16 v[184:187], a[36:39], v[20:23], v[184:187]\n" \
"s_xor_b32 s77, s77, 0x8000\n" \
"ds_read_b128 a[236:239], v232 offset:14336\n" \
"v_cvt_pk_bf16_f32 v112, v112, v113\n" \
"v_mfma_f32_16x16x32_bf16 a[88:91], a[32:35], v[32:35], a[88:91]\n" \
"v_exp_f32 v114, v114\n" \
"v_mfma_f32_16x16x32_bf16 a[88:91], a[36:39], v[36:39], a[88:91]\n" \
"v_mfma_f32_16x16x32_bf16 a[120:123], a[32:35], v[48:51], a[120:123]\n" \
"v_exp_f32 v115, v115\n" \
"v_mfma_f32_16x16x32_bf16 a[120:123], a[36:39], v[52:55], a[120:123]\n" \
"ds_read_b128 a[244:247], v232 offset:10496\n" \
"v_cvt_pk_bf16_f32 v113, v114, v115\n" \
"v_mfma_f32_16x16x32_bf16 a[124:127], a[40:43], v[48:51], a[124:127]\n" \
"v_exp_f32 v116, v116\n" \
"v_mfma_f32_16x16x32_bf16 a[124:127], a[44:47], v[52:55], a[124:127]\n" \
"v_mfma_f32_16x16x32_bf16 a[92:95], a[40:43], v[32:35], a[92:95]\n" \
"v_exp_f32 v117, v117\n" \
"v_mfma_f32_16x16x32_bf16 a[92:95], a[44:47], v[36:39], a[92:95]\n" \
"ds_read_b128 a[248:251], v232 offset:12544\n" \
"v_cvt_pk_bf16_f32 v114, v116, v117\n" \
"v_smfmac_f32_16x16x64_bf16 v[228:231], v[244:247], v[48:55], v243\n" \
"v_mfma_f32_16x16x32_bf16 v[188:191], a[40:43], v[16:19], v[188:191]\n" \
"v_exp_f32 v118, v118\n" \
"v_mfma_f32_16x16x32_bf16 v[188:191], a[44:47], v[20:23], v[188:191]\n" \
"s_cmp_lg_u32 s89, 0\n" \
"v_exp_f32 v119, v119\n" \
"v_mfma_f32_16x16x32_bf16 v[156:159], a[40:43], v[0:3], v[156:159]\n" \
"s_waitcnt lgkmcnt(2)\n" \
"ds_read_b128 a[252:255], v232 offset:14592\n" \
"v_cvt_pk_bf16_f32 v115, v118, v119\n" \
"v_mfma_f32_16x16x32_bf16 v[156:159], a[44:47], v[4:7], v[156:159]\n" \
"s_cbranch_scc1 Lloop_%=\n" \
"s_cmp_eq_u32 s63, 7\n" \
"s_nop 0\n" \
"s_cbranch_scc0 Lloop2_%=\n" \
"s_branch Lg_%=\n" \
"s_nop 0\n" \
"s_nop 0\n" \
"s_nop 0\n" \
"s_nop 0\n" \
"s_nop 0\n" \
"s_nop 0\n" \
"Lep_%=:\n" \
"Lpg6050_%=:\n" \
"s_cbranch_execz Lpg7367_%=\n" \
"s_nop 4\n" \
"v_accvgpr_read_b32 v92, a48\n" \
"v_accvgpr_read_b32 v93, a49\n" \
"v_accvgpr_read_b32 v94, a52\n" \
"v_accvgpr_read_b32 v95, a53\n" \
"v_accvgpr_write_b32 a48, 0\n" \
"v_accvgpr_write_b32 a49, 0\n" \
"v_accvgpr_write_b32 a52, 0\n" \
"v_accvgpr_write_b32 a53, 0\n" \
"v_add_f32 v86, v92, v93\n" \
"v_add_f32 v87, v94, v95\n" \
"v_rcp_f32 v84, v86\n" \
"v_rcp_f32 v85, v87\n" \
"v_add_u32 v240, 0x1000, v236\n" \
"v_add_f32 v80, v86, v87\n" \
"v_mul_f32 v80, 0x43800000, v80\n" \
"v_add_f32 v86, v84, v85\n" \
"v_fmac_f32 v80, 0x5f800000, v86\n" \
"v_mov_b32 v81, 0\n" \
"v_mov_b32 v82, 0\n" \
"v_mov_b32 v83, 0\n" \
"v_mov_b32 v88, 0\n" \
"v_mov_b32 v89, 0\n" \
"v_mov_b32 v90, 0\n" \
"v_mov_b32 v91, 0\n" \
"v_pk_mul_f32 v[128:129], v[128:129], v[84:85] op_sel:[0,0] op_sel_hi:[1,0]\n" \
"v_pk_mul_f32 v[130:131], v[130:131], v[84:85] op_sel:[0,0] op_sel_hi:[1,0]\n" \
"v_pk_mul_f32 v[132:133], v[132:133], v[84:85] op_sel:[0,0] op_sel_hi:[1,0]\n" \
"v_pk_mul_f32 v[134:135], v[134:135], v[84:85] op_sel:[0,0] op_sel_hi:[1,0]\n" \
"v_cvt_pk_bf16_f32 v64, v128, v129\n" \
"v_cvt_pk_bf16_f32 v65, v130, v131\n" \
"v_cvt_pk_bf16_f32 v66, v132, v133\n" \
"v_cvt_pk_bf16_f32 v67, v134, v135\n" \
"buffer_store_dwordx4 v[64:67], v236, s[56:59], s87 offen offset:0 sc1\n" \
"v_pk_mul_f32 v[136:137], v[136:137], v[84:85] op_sel:[0,0] op_sel_hi:[1,0]\n" \
"v_pk_mul_f32 v[138:139], v[138:139], v[84:85] op_sel:[0,0] op_sel_hi:[1,0]\n" \
"v_pk_mul_f32 v[140:141], v[140:141], v[84:85] op_sel:[0,0] op_sel_hi:[1,0]\n" \
"v_pk_mul_f32 v[142:143], v[142:143], v[84:85] op_sel:[0,0] op_sel_hi:[1,0]\n" \
"v_cvt_pk_bf16_f32 v68, v136, v137\n" \
"v_cvt_pk_bf16_f32 v69, v138, v139\n" \
"v_cvt_pk_bf16_f32 v70, v140, v141\n" \
"v_cvt_pk_bf16_f32 v71, v142, v143\n" \
"buffer_store_dwordx4 v[68:71], v236, s[56:59], s87 offen offset:2048 sc1\n" \
"v_pk_mul_f32 v[144:145], v[144:145], v[84:85] op_sel:[0,0] op_sel_hi:[1,0]\n" \
"v_pk_mul_f32 v[146:147], v[146:147], v[84:85] op_sel:[0,0] op_sel_hi:[1,0]\n" \
"v_pk_mul_f32 v[148:149], v[148:149], v[84:85] op_sel:[0,0] op_sel_hi:[1,0]\n" \
"v_pk_mul_f32 v[150:151], v[150:151], v[84:85] op_sel:[0,0] op_sel_hi:[1,0]\n" \
"v_cvt_pk_bf16_f32 v72, v144, v145\n" \
"v_cvt_pk_bf16_f32 v73, v146, v147\n" \
"v_cvt_pk_bf16_f32 v74, v148, v149\n" \
"v_cvt_pk_bf16_f32 v75, v150, v151\n" \
"buffer_store_dwordx4 v[72:75], v240, s[56:59], s87 offen offset:0 sc1\n" \
"v_pk_mul_f32 v[152:153], v[152:153], v[84:85] op_sel:[0,0] op_sel_hi:[1,0]\n" \
"v_pk_mul_f32 v[154:155], v[154:155], v[84:85] op_sel:[0,0] op_sel_hi:[1,0]\n" \
"v_pk_mul_f32 v[156:157], v[156:157], v[84:85] op_sel:[0,0] op_sel_hi:[1,0]\n" \
"v_pk_mul_f32 v[158:159], v[158:159], v[84:85] op_sel:[0,0] op_sel_hi:[1,0]\n" \
"v_cvt_pk_bf16_f32 v76, v152, v153\n" \
"v_cvt_pk_bf16_f32 v77, v154, v155\n" \
"v_cvt_pk_bf16_f32 v78, v156, v157\n" \
"v_cvt_pk_bf16_f32 v79, v158, v159\n" \
"buffer_store_dwordx4 v[76:79], v240, s[56:59], s87 offen offset:2048 sc1\n" \
"v_smfmac_f32_16x16x64_bf16 v[80:83], v[244:247], v[64:71], v243\n" \
"v_smfmac_f32_16x16x64_bf16 v[80:83], v[244:247], v[72:79], v243\n" \
"v_mfma_f32_32x32x16_bf16 v[128:143], v[88:91], v[88:91], 0\n" \
"v_mfma_f32_32x32x16_bf16 v[144:159], v[88:91], v[88:91], 0\n" \
"s_mov_b64 exec, 0\n" \
"s_cmp_eq_u32 s61, 0\n" \
"s_cbranch_scc1 Lst1x0_%=\n" \
"s_cmp_eq_u32 s61, 1\n" \
"s_cbranch_scc1 Lst1x1_%=\n" \
"s_cmp_eq_u32 s61, 2\n" \
"s_cbranch_scc1 Lst1x2_%=\n" \
"s_branch Lst1x3_%=\n" \
"Lcb1_%=:\n" \
"s_mov_b64 exec, -1\n" \
"s_add_u32 s72, s87, 532480\n" \
"v_pk_mul_f32 v[160:161], v[160:161], v[84:85] op_sel:[0,1] op_sel_hi:[1,1]\n" \
"v_pk_mul_f32 v[162:163], v[162:163], v[84:85] op_sel:[0,1] op_sel_hi:[1,1]\n" \
"v_pk_mul_f32 v[164:165], v[164:165], v[84:85] op_sel:[0,1] op_sel_hi:[1,1]\n" \
"v_pk_mul_f32 v[166:167], v[166:167], v[84:85] op_sel:[0,1] op_sel_hi:[1,1]\n" \
"v_cvt_pk_bf16_f32 v64, v160, v161\n" \
"v_cvt_pk_bf16_f32 v65, v162, v163\n" \
"v_cvt_pk_bf16_f32 v66, v164, v165\n" \
"v_cvt_pk_bf16_f32 v67, v166, v167\n" \
"buffer_store_dwordx4 v[64:67], v236, s[56:59], s72 offen offset:0 sc1\n" \
"v_pk_mul_f32 v[168:169], v[168:169], v[84:85] op_sel:[0,1] op_sel_hi:[1,1]\n" \
"v_pk_mul_f32 v[170:171], v[170:171], v[84:85] op_sel:[0,1] op_sel_hi:[1,1]\n" \
"v_pk_mul_f32 v[172:173], v[172:173], v[84:85] op_sel:[0,1] op_sel_hi:[1,1]\n" \
"v_pk_mul_f32 v[174:175], v[174:175], v[84:85] op_sel:[0,1] op_sel_hi:[1,1]\n" \
"v_cvt_pk_bf16_f32 v68, v168, v169\n" \
"v_cvt_pk_bf16_f32 v69, v170, v171\n" \
"v_cvt_pk_bf16_f32 v70, v172, v173\n" \
"v_cvt_pk_bf16_f32 v71, v174, v175\n" \
"buffer_store_dwordx4 v[68:71], v236, s[56:59], s72 offen offset:2048 sc1\n" \
"v_pk_mul_f32 v[176:177], v[176:177], v[84:85] op_sel:[0,1] op_sel_hi:[1,1]\n" \
"v_pk_mul_f32 v[178:179], v[178:179], v[84:85] op_sel:[0,1] op_sel_hi:[1,1]\n" \
"v_pk_mul_f32 v[180:181], v[180:181], v[84:85] op_sel:[0,1] op_sel_hi:[1,1]\n" \
"v_pk_mul_f32 v[182:183], v[182:183], v[84:85] op_sel:[0,1] op_sel_hi:[1,1]\n" \
"v_cvt_pk_bf16_f32 v72, v176, v177\n" \
"v_cvt_pk_bf16_f32 v73, v178, v179\n" \
"v_cvt_pk_bf16_f32 v74, v180, v181\n" \
"v_cvt_pk_bf16_f32 v75, v182, v183\n" \
"buffer_store_dwordx4 v[72:75], v240, s[56:59], s72 offen offset:0 sc1\n" \
"v_pk_mul_f32 v[184:185], v[184:185], v[84:85] op_sel:[0,1] op_sel_hi:[1,1]\n" \
"v_pk_mul_f32 v[186:187], v[186:187], v[84:85] op_sel:[0,1] op_sel_hi:[1,1]\n" \
"v_pk_mul_f32 v[188:189], v[188:189], v[84:85] op_sel:[0,1] op_sel_hi:[1,1]\n" \
"v_pk_mul_f32 v[190:191], v[190:191], v[84:85] op_sel:[0,1] op_sel_hi:[1,1]\n" \
"v_cvt_pk_bf16_f32 v76, v184, v185\n" \
"v_cvt_pk_bf16_f32 v77, v186, v187\n" \
"v_cvt_pk_bf16_f32 v78, v188, v189\n" \
"v_cvt_pk_bf16_f32 v79, v190, v191\n" \
"buffer_store_dwordx4 v[76:79], v240, s[56:59], s72 offen offset:2048 sc1\n" \
"v_smfmac_f32_16x16x64_bf16 v[80:83], v[244:247], v[64:71], v243\n" \
"v_smfmac_f32_16x16x64_bf16 v[80:83], v[244:247], v[72:79], v243\n" \
"v_mfma_f32_32x32x16_bf16 v[160:175], v[88:91], v[88:91], 0\n" \
"v_mfma_f32_32x32x16_bf16 v[176:191], v[88:91], v[88:91], 0\n" \
"s_nop 5\n" \
"v_add_f32 v80, v80, v81\n" \
"s_movk_i32 s64, 0x207\n" \
"v_cmp_class_f32 vcc, v80, s64\n" \
"s_nop 1\n" \
"s_cmp_lg_u64 vcc, 0\n" \
"s_addc_u32 s90, s90, 0\n" \
"s_waitcnt vmcnt(8)\n" \
"s_cmp_eq_u32 s63, 7\n" \
"s_cbranch_scc0 Lepr_%=\n" \
"s_branch Lepg_%=\n" \
".p2align 12\n" \
"Lloop2_%=:\n" \
"v_mfma_f32_16x16x32_bf16 v[0:3], a[192:195], a[0:3], v[196:199]\n" \
"Lpg6181_%=:\n" \
"s_cbranch_execz Lcb0_%=\n" \
"v_exp_f32 v72, v72\n" \
"v_mfma_f32_16x16x32_bf16 v[0:3], a[196:199], a[4:7], v[0:3]\n" \
"ds_read_b128 a[44:47], v233 offset:15360\n" \
"v_mfma_f32_16x16x32_bf16 v[0:3], a[200:203], a[8:11], v[0:3]\n" \
"v_exp_f32 v73, v73\n" \
"v_mfma_f32_16x16x32_bf16 v[0:3], a[204:207], a[12:15], v[0:3]\n" \
"ds_read_b128 a[32:35], v233 offset:6144\n" \
"v_cvt_pk_bf16_f32 v68, v72, v73\n" \
"v_mfma_f32_16x16x32_bf16 v[16:19], a[204:207], a[28:31], v[196:199]\n" \
"v_exp_f32 v74, v74\n" \
"v_mfma_f32_16x16x32_bf16 v[16:19], a[200:203], a[24:27], v[16:19]\n" \
"v_xor_b32 v232, 0x8000, v232\n" \
"v_mfma_f32_16x16x32_bf16 v[16:19], a[196:199], a[20:23], v[16:19]\n" \
"v_exp_f32 v75, v75\n" \
"v_mfma_f32_16x16x32_bf16 v[16:19], a[192:195], a[16:19], v[16:19]\n" \
"ds_read_b128 a[36:39], v233 offset:14336\n" \
"v_cvt_pk_bf16_f32 v69, v74, v75\n" \
"v_mfma_f32_16x16x32_bf16 v[32:35], a[192:195], a[160:163], v[196:199]\n" \
"v_exp_f32 v76, v76\n" \
"v_mfma_f32_16x16x32_bf16 v[32:35], a[196:199], a[164:167], v[32:35]\n" \
"v_mfma_f32_16x16x32_bf16 v[32:35], a[200:203], a[168:171], v[32:35]\n" \
"v_exp_f32 v77, v77\n" \
"v_mfma_f32_16x16x32_bf16 v[32:35], a[204:207], a[172:175], v[32:35]\n" \
"ds_read_b128 a[40:43], v233 offset:7168\n" \
"v_cvt_pk_bf16_f32 v70, v76, v77\n" \
"v_mfma_f32_16x16x32_bf16 v[48:51], a[204:207], a[188:191], v[196:199]\n" \
"v_exp_f32 v78, v78\n" \
"v_mfma_f32_16x16x32_bf16 v[48:51], a[200:203], a[184:187], v[48:51]\n" \
"v_mfma_f32_16x16x32_bf16 v[48:51], a[196:199], a[180:183], v[48:51]\n" \
"v_exp_f32 v79, v79\n" \
"v_mfma_f32_16x16x32_bf16 v[48:51], a[192:195], a[176:179], v[48:51]\n" \
"ds_read_b128 a[204:207], v233 offset:9216\n" \
"v_cvt_pk_bf16_f32 v71, v78, v79\n" \
"v_mfma_f32_16x16x32_bf16 v[52:55], a[208:211], a[176:179], v[196:199]\n" \
"v_exp_f32 v88, v88\n" \
"v_mfma_f32_16x16x32_bf16 v[52:55], a[212:215], a[180:183], v[52:55]\n" \
"v_mfma_f32_16x16x32_bf16 v[52:55], a[216:219], a[184:187], v[52:55]\n" \
"v_exp_f32 v89, v89\n" \
"v_mfma_f32_16x16x32_bf16 v[52:55], a[220:223], a[188:191], v[52:55]\n" \
"ds_read_b128 a[200:203], v233 offset:1024\n" \
"v_cvt_pk_bf16_f32 v84, v88, v89\n" \
"v_mfma_f32_16x16x32_bf16 v[36:39], a[220:223], a[172:175], v[196:199]\n" \
"v_exp_f32 v90, v90\n" \
"v_mfma_f32_16x16x32_bf16 v[36:39], a[216:219], a[168:171], v[36:39]\n" \
"v_mfma_f32_16x16x32_bf16 v[36:39], a[212:215], a[164:167], v[36:39]\n" \
"v_exp_f32 v91, v91\n" \
"v_mfma_f32_16x16x32_bf16 v[36:39], a[208:211], a[160:163], v[36:39]\n" \
"ds_read_b128 a[196:199], v233 offset:8192\n" \
"v_cvt_pk_bf16_f32 v85, v90, v91\n" \
"v_mfma_f32_16x16x32_bf16 v[20:23], a[208:211], a[16:19], v[196:199]\n" \
"v_exp_f32 v92, v92\n" \
"v_mfma_f32_16x16x32_bf16 v[20:23], a[212:215], a[20:23], v[20:23]\n" \
"v_mfma_f32_16x16x32_bf16 v[20:23], a[216:219], a[24:27], v[20:23]\n" \
"v_exp_f32 v93, v93\n" \
"v_mfma_f32_16x16x32_bf16 v[20:23], a[220:223], a[28:31], v[20:23]\n" \
"ds_read_b128 a[192:195], v233 offset:0\n" \
"v_cvt_pk_bf16_f32 v86, v92, v93\n" \
"v_mfma_f32_16x16x32_bf16 v[4:7], a[220:223], a[12:15], v[196:199]\n" \
"v_exp_f32 v94, v94\n" \
"v_mfma_f32_16x16x32_bf16 v[4:7], a[216:219], a[8:11], v[4:7]\n" \
"v_mfma_f32_16x16x32_bf16 v[4:7], a[212:215], a[4:7], v[4:7]\n" \
"s_waitcnt lgkmcnt(8)\n" \
"v_exp_f32 v95, v95\n" \
"v_mfma_f32_16x16x32_bf16 v[4:7], a[208:211], a[0:3], v[4:7]\n" \
"ds_read_b128 a[220:223], v233 offset:11264\n" \
"v_cvt_pk_bf16_f32 v87, v94, v95\n" \
"v_mfma_f32_16x16x32_bf16 v[8:11], a[224:227], a[0:3], v[196:199]\n" \
"v_exp_f32 v104, v104\n" \
"v_mfma_f32_16x16x32_bf16 v[8:11], a[228:231], a[4:7], v[8:11]\n" \
"v_mfma_f32_16x16x32_bf16 v[8:11], a[232:235], a[8:11], v[8:11]\n" \
"v_exp_f32 v105, v105\n" \
"v_mfma_f32_16x16x32_bf16 v[8:11], a[236:239], a[12:15], v[8:11]\n" \
"ds_read_b128 a[216:219], v233 offset:3072\n" \
"v_cvt_pk_bf16_f32 v100, v104, v105\n" \
"v_mfma_f32_16x16x32_bf16 v[24:27], a[236:239], a[28:31], v[196:199]\n" \
"v_exp_f32 v106, v106\n" \
"v_mfma_f32_16x16x32_bf16 v[24:27], a[232:235], a[24:27], v[24:27]\n" \
"v_mfma_f32_16x16x32_bf16 v[24:27], a[228:231], a[20:23], v[24:27]\n" \
"v_exp_f32 v107, v107\n" \
"v_mfma_f32_16x16x32_bf16 v[24:27], a[224:227], a[16:19], v[24:27]\n" \
"ds_read_b128 a[212:215], v233 offset:10240\n" \
"v_cvt_pk_bf16_f32 v101, v106, v107\n" \
"v_mfma_f32_16x16x32_bf16 v[40:43], a[224:227], a[160:163], v[196:199]\n" \
"v_exp_f32 v108, v108\n" \
"v_mfma_f32_16x16x32_bf16 v[40:43], a[228:231], a[164:167], v[40:43]\n" \
"v_mfma_f32_16x16x32_bf16 v[40:43], a[232:235], a[168:171], v[40:43]\n" \
"v_exp_f32 v109, v109\n" \
"v_mfma_f32_16x16x32_bf16 v[40:43], a[236:239], a[172:175], v[40:43]\n" \
"ds_read_b128 a[208:211], v233 offset:2048\n" \
"v_cvt_pk_bf16_f32 v102, v108, v109\n" \
"v_mfma_f32_16x16x32_bf16 v[56:59], a[236:239], a[188:191], v[196:199]\n" \
"v_exp_f32 v110, v110\n" \
"v_mfma_f32_16x16x32_bf16 v[56:59], a[232:235], a[184:187], v[56:59]\n" \
"v_mfma_f32_16x16x32_bf16 v[56:59], a[228:231], a[180:183], v[56:59]\n" \
"v_exp_f32 v111, v111\n" \
"v_mfma_f32_16x16x32_bf16 v[56:59], a[224:227], a[176:179], v[56:59]\n" \
"ds_read_b128 a[236:239], v233 offset:13312\n" \
"v_cvt_pk_bf16_f32 v103, v110, v111\n" \
"v_mfma_f32_16x16x32_bf16 v[60:63], a[240:243], a[176:179], v[196:199]\n" \
"v_exp_f32 v120, v120\n" \
"v_mfma_f32_16x16x32_bf16 v[60:63], a[244:247], a[180:183], v[60:63]\n" \
"ds_read_b128 a[232:235], v233 offset:5120\n" \
"v_mfma_f32_16x16x32_bf16 v[60:63], a[248:251], a[184:187], v[60:63]\n" \
"v_exp_f32 v121, v121\n" \
"v_mfma_f32_16x16x32_bf16 v[60:63], a[252:255], a[188:191], v[60:63]\n" \
"ds_read_b128 a[228:231], v233 offset:12288\n" \
"v_cvt_pk_bf16_f32 v116, v120, v121\n" \
"v_mfma_f32_16x16x32_bf16 v[44:47], a[252:255], a[172:175], v[196:199]\n" \
"v_exp_f32 v122, v122\n" \
"v_mfma_f32_16x16x32_bf16 v[44:47], a[248:251], a[168:171], v[44:47]\n" \
"ds_read_b128 a[224:227], v233 offset:4096\n" \
"v_mfma_f32_16x16x32_bf16 v[44:47], a[244:247], a[164:167], v[44:47]\n" \
"v_exp_f32 v123, v123\n" \
"v_mfma_f32_16x16x32_bf16 v[44:47], a[240:243], a[160:163], v[44:47]\n" \
"v_cvt_pk_bf16_f32 v117, v122, v123\n" \
"v_mfma_f32_16x16x32_bf16 v[28:31], a[240:243], a[16:19], v[196:199]\n" \
"v_exp_f32 v124, v124\n" \
"v_mfma_f32_16x16x32_bf16 v[28:31], a[244:247], a[20:23], v[28:31]\n" \
"v_mfma_f32_16x16x32_bf16 v[28:31], a[248:251], a[24:27], v[28:31]\n" \
"v_exp_f32 v125, v125\n" \
"v_mfma_f32_16x16x32_bf16 v[28:31], a[252:255], a[28:31], v[28:31]\n" \
"v_cvt_pk_bf16_f32 v118, v124, v125\n" \
"v_mfma_f32_16x16x32_bf16 v[12:15], a[252:255], a[12:15], v[196:199]\n" \
"v_exp_f32 v126, v126\n" \
"v_mfma_f32_16x16x32_bf16 v[12:15], a[248:251], a[8:11], v[12:15]\n" \
"v_mfma_f32_16x16x32_bf16 v[12:15], a[244:247], a[4:7], v[12:15]\n" \
"s_waitcnt vmcnt(0) lgkmcnt(0)\n" \
"v_exp_f32 v127, v127\n" \
"v_mfma_f32_16x16x32_bf16 v[12:15], a[240:243], a[0:3], v[12:15]\n" \
"s_barrier\n" \
"v_cvt_pk_bf16_f32 v119, v126, v127\n" \
"v_mfma_f32_16x16x32_bf16 v[128:131], a[192:195], v[64:67], v[128:131]\n" \
"v_exp_f32 v0, v0\n" \
"v_mfma_f32_16x16x32_bf16 v[128:131], a[196:199], v[68:71], v[128:131]\n" \
"v_cmp_eq_u32 vcc, s60, v239\n" \
"v_mfma_f32_16x16x32_bf16 v[160:163], a[192:195], v[80:83], v[160:163]\n" \
"v_exp_f32 v1, v1\n" \
"v_mfma_f32_16x16x32_bf16 v[160:163], a[196:199], v[84:87], v[160:163]\n" \
"s_andn2_b32 s70, s69, vcc_lo\n" \
"s_cbranch_scc1 Lpl3_%=\n" \
"Lpr3_%=:\n" \
"ds_read_b128 a[240:243], v232 offset:8448\n" \
"v_cvt_pk_bf16_f32 v0, v0, v1\n" \
"v_mfma_f32_16x16x32_bf16 a[64:67], a[192:195], v[96:99], a[64:67]\n" \
"s_add_u32 m0, s74, s79\n" \
"v_exp_f32 v2, v2\n" \
"v_mfma_f32_16x16x32_bf16 a[64:67], a[196:199], v[100:103], a[64:67]\n" \
"buffer_load_dwordx4 v234, s[36:39], s73 offen offset:0 sc1 lds\n" \
"v_mfma_f32_16x16x32_bf16 a[96:99], a[192:195], v[112:115], a[96:99]\n" \
"v_exp_f32 v3, v3\n" \
"v_mfma_f32_16x16x32_bf16 a[96:99], a[196:199], v[116:119], a[96:99]\n" \
"ds_read_b128 a[192:195], v232 offset:0\n" \
"v_cvt_pk_bf16_f32 v1, v2, v3\n" \
"v_mfma_f32_16x16x32_bf16 a[100:103], a[200:203], v[112:115], a[100:103]\n" \
"s_add_u32 s71, s73, 17408\n" \
"v_exp_f32 v4, v4\n" \
"v_mfma_f32_16x16x32_bf16 a[100:103], a[204:207], v[116:119], a[100:103]\n" \
"buffer_load_dwordx4 v234, s[36:39], s73 offen offset:1024 sc1 lds\n" \
"v_mfma_f32_16x16x32_bf16 a[68:71], a[200:203], v[96:99], a[68:71]\n" \
"v_exp_f32 v5, v5\n" \
"v_mfma_f32_16x16x32_bf16 a[68:71], a[204:207], v[100:103], a[68:71]\n" \
"ds_read_b128 a[196:199], v232 offset:2048\n" \
"v_cvt_pk_bf16_f32 v2, v4, v5\n" \
"v_smfmac_f32_16x16x64_bf16 a[48:51], v[244:247], v[64:71], v243\n" \
"buffer_load_dwordx4 v234, s[36:39], s73 offen offset:2048 sc1 lds\n" \
"v_mfma_f32_16x16x32_bf16 v[164:167], a[200:203], v[80:83], v[164:167]\n" \
"v_exp_f32 v6, v6\n" \
"v_mfma_f32_16x16x32_bf16 v[164:167], a[204:207], v[84:87], v[164:167]\n" \
"buffer_load_dwordx4 v234, s[36:39], s73 offen offset:3072 sc1 lds\n" \
"v_mfma_f32_16x16x32_bf16 v[132:135], a[200:203], v[64:67], v[132:135]\n" \
"v_exp_f32 v7, v7\n" \
"v_mfma_f32_16x16x32_bf16 v[132:135], a[204:207], v[68:71], v[132:135]\n" \
"ds_read_b128 a[200:203], v232 offset:4096\n" \
"v_cvt_pk_bf16_f32 v3, v6, v7\n" \
"v_mfma_f32_16x16x32_bf16 v[136:139], a[208:211], v[64:67], v[136:139]\n" \
"s_add_u32 m0, m0, 0x4000\n" \
"v_exp_f32 v16, v16\n" \
"v_mfma_f32_16x16x32_bf16 v[136:139], a[212:215], v[68:71], v[136:139]\n" \
"buffer_load_dwordx4 v234, s[36:39], s71 offen offset:0 sc1 lds\n" \
"v_mfma_f32_16x16x32_bf16 v[168:171], a[208:211], v[80:83], v[168:171]\n" \
"v_exp_f32 v17, v17\n" \
"v_mfma_f32_16x16x32_bf16 v[168:171], a[212:215], v[84:87], v[168:171]\n" \
"ds_read_b128 a[204:207], v232 offset:6144\n" \
"v_cvt_pk_bf16_f32 v16, v16, v17\n" \
"v_mfma_f32_16x16x32_bf16 a[72:75], a[208:211], v[96:99], a[72:75]\n" \
"v_exp_f32 v18, v18\n" \
"v_mfma_f32_16x16x32_bf16 a[72:75], a[212:215], v[100:103], a[72:75]\n" \
"buffer_load_dwordx4 v234, s[36:39], s71 offen offset:1024 sc1 lds\n" \
"v_mfma_f32_16x16x32_bf16 a[104:107], a[208:211], v[112:115], a[104:107]\n" \
"v_exp_f32 v19, v19\n" \
"v_mfma_f32_16x16x32_bf16 a[104:107], a[212:215], v[116:119], a[104:107]\n" \
"ds_read_b128 a[208:211], v232 offset:256\n" \
"v_cvt_pk_bf16_f32 v17, v18, v19\n" \
"v_mfma_f32_16x16x32_bf16 a[108:111], a[216:219], v[112:115], a[108:111]\n" \
"v_exp_f32 v20, v20\n" \
"v_mfma_f32_16x16x32_bf16 a[108:111], a[220:223], v[116:119], a[108:111]\n" \
"buffer_load_dwordx4 v234, s[36:39], s71 offen offset:2048 sc1 lds\n" \
"v_mfma_f32_16x16x32_bf16 a[76:79], a[216:219], v[96:99], a[76:79]\n" \
"v_exp_f32 v21, v21\n" \
"v_mfma_f32_16x16x32_bf16 a[76:79], a[220:223], v[100:103], a[76:79]\n" \
"ds_read_b128 a[212:215], v232 offset:2304\n" \
"v_cvt_pk_bf16_f32 v18, v20, v21\n" \
"v_smfmac_f32_16x16x64_bf16 a[52:55], v[244:247], v[80:87], v243\n" \
"buffer_load_dwordx4 v234, s[36:39], s71 offen offset:3072 sc1 lds\n" \
"v_mfma_f32_16x16x32_bf16 v[172:175], a[216:219], v[80:83], v[172:175]\n" \
"s_add_u32 m0, s77, s80\n" \
"v_exp_f32 v22, v22\n" \
"v_mfma_f32_16x16x32_bf16 v[172:175], a[220:223], v[84:87], v[172:175]\n" \
"buffer_load_dwordx4 v234, s[40:43], s76 offen offset:0 sc1 lds\n" \
"v_mfma_f32_16x16x32_bf16 v[140:143], a[216:219], v[64:67], v[140:143]\n" \
"v_exp_f32 v23, v23\n" \
"v_mfma_f32_16x16x32_bf16 v[140:143], a[220:223], v[68:71], v[140:143]\n" \
"ds_read_b128 a[216:219], v232 offset:4352\n" \
"v_cvt_pk_bf16_f32 v19, v22, v23\n" \
"v_mfma_f32_16x16x32_bf16 v[144:147], a[224:227], v[64:67], v[144:147]\n" \
"s_add_u32 s71, s76, 17408\n" \
"v_exp_f32 v32, v32\n" \
"v_mfma_f32_16x16x32_bf16 v[144:147], a[228:231], v[68:71], v[144:147]\n" \
"buffer_load_dwordx4 v234, s[40:43], s76 offen offset:1024 sc1 lds\n" \
"v_mfma_f32_16x16x32_bf16 v[176:179], a[224:227], v[80:83], v[176:179]\n" \
"v_exp_f32 v33, v33\n" \
"v_mfma_f32_16x16x32_bf16 v[176:179], a[228:231], v[84:87], v[176:179]\n" \
"ds_read_b128 a[220:223], v232 offset:6400\n" \
"v_cvt_pk_bf16_f32 v32, v32, v33\n" \
"v_mfma_f32_16x16x32_bf16 a[80:83], a[224:227], v[96:99], a[80:83]\n" \
"v_exp_f32 v34, v34\n" \
"v_mfma_f32_16x16x32_bf16 a[80:83], a[228:231], v[100:103], a[80:83]\n" \
"buffer_load_dwordx4 v234, s[40:43], s76 offen offset:2048 sc1 lds\n" \
"v_mfma_f32_16x16x32_bf16 a[112:115], a[224:227], v[112:115], a[112:115]\n" \
"v_exp_f32 v35, v35\n" \
"v_mfma_f32_16x16x32_bf16 a[112:115], a[228:231], v[116:119], a[112:115]\n" \
"ds_read_b128 a[224:227], v232 offset:8192\n" \
"v_cvt_pk_bf16_f32 v33, v34, v35\n" \
"v_mfma_f32_16x16x32_bf16 a[116:119], a[232:235], v[112:115], a[116:119]\n" \
"v_exp_f32 v36, v36\n" \
"v_mfma_f32_16x16x32_bf16 a[116:119], a[236:239], v[116:119], a[116:119]\n" \
"buffer_load_dwordx4 v234, s[40:43], s76 offen offset:3072 sc1 lds\n" \
"v_mfma_f32_16x16x32_bf16 a[84:87], a[232:235], v[96:99], a[84:87]\n" \
"v_exp_f32 v37, v37\n" \
"v_mfma_f32_16x16x32_bf16 a[84:87], a[236:239], v[100:103], a[84:87]\n" \
"s_add_u32 m0, m0, 0x4000\n" \
"ds_read_b128 a[228:231], v232 offset:10240\n" \
"v_cvt_pk_bf16_f32 v34, v36, v37\n" \
"v_smfmac_f32_16x16x64_bf16 v[224:227], v[244:247], v[96:103], v243\n" \
"buffer_load_dwordx4 v234, s[40:43], s71 offen offset:0 sc1 lds\n" \
"v_mfma_f32_16x16x32_bf16 v[180:183], a[232:235], v[80:83], v[180:183]\n" \
"v_exp_f32 v38, v38\n" \
"v_mfma_f32_16x16x32_bf16 v[180:183], a[236:239], v[84:87], v[180:183]\n" \
"buffer_load_dwordx4 v234, s[40:43], s71 offen offset:1024 sc1 lds\n" \
"v_mfma_f32_16x16x32_bf16 v[148:151], a[232:235], v[64:67], v[148:151]\n" \
"v_exp_f32 v39, v39\n" \
"v_mfma_f32_16x16x32_bf16 v[148:151], a[236:239], v[68:71], v[148:151]\n" \
"ds_read_b128 a[232:235], v232 offset:12288\n" \
"v_cvt_pk_bf16_f32 v35, v38, v39\n" \
"v_mfma_f32_16x16x32_bf16 v[152:155], a[32:35], v[64:67], v[152:155]\n" \
"v_exp_f32 v48, v48\n" \
"v_mfma_f32_16x16x32_bf16 v[152:155], a[36:39], v[68:71], v[152:155]\n" \
"buffer_load_dwordx4 v234, s[40:43], s71 offen offset:2048 sc1 lds\n" \
"v_mfma_f32_16x16x32_bf16 v[184:187], a[32:35], v[80:83], v[184:187]\n" \
"v_exp_f32 v49, v49\n" \
"v_mfma_f32_16x16x32_bf16 v[184:187], a[36:39], v[84:87], v[184:187]\n" \
"ds_read_b128 a[236:239], v232 offset:14336\n" \
"v_cvt_pk_bf16_f32 v48, v48, v49\n" \
"v_mfma_f32_16x16x32_bf16 a[88:91], a[32:35], v[96:99], a[88:91]\n" \
"v_exp_f32 v50, v50\n" \
"v_mfma_f32_16x16x32_bf16 a[88:91], a[36:39], v[100:103], a[88:91]\n" \
"buffer_load_dwordx4 v234, s[40:43], s71 offen offset:3072 sc1 lds\n" \
"v_mfma_f32_16x16x32_bf16 a[120:123], a[32:35], v[112:115], a[120:123]\n" \
"s_mov_b32 s76, s73\n" \
"v_exp_f32 v51, v51\n" \
"v_mfma_f32_16x16x32_bf16 a[120:123], a[36:39], v[116:119], a[120:123]\n" \
"s_add_u32 s73, s73, 34816\n" \
"ds_read_b128 a[244:247], v232 offset:10496\n" \
"v_cvt_pk_bf16_f32 v49, v50, v51\n" \
"v_mfma_f32_16x16x32_bf16 a[124:127], a[40:43], v[112:115], a[124:127]\n" \
"s_xor_b32 s74, s74, 0x8000\n" \
"v_exp_f32 v52, v52\n" \
"v_mfma_f32_16x16x32_bf16 a[124:127], a[44:47], v[116:119], a[124:127]\n" \
"s_xor_b32 s77, s77, 0x8000\n" \
"v_xor_b32 v233, s81, v233\n" \
"s_mov_b32 s81, 0x8000\n" \
"v_mfma_f32_16x16x32_bf16 a[92:95], a[40:43], v[96:99], a[92:95]\n" \
"v_exp_f32 v53, v53\n" \
"v_mfma_f32_16x16x32_bf16 a[92:95], a[44:47], v[100:103], a[92:95]\n" \
"ds_read_b128 a[248:251], v232 offset:12544\n" \
"v_cvt_pk_bf16_f32 v50, v52, v53\n" \
"v_smfmac_f32_16x16x64_bf16 v[228:231], v[244:247], v[112:119], v243\n" \
"v_mfma_f32_16x16x32_bf16 v[188:191], a[40:43], v[80:83], v[188:191]\n" \
"v_exp_f32 v54, v54\n" \
"v_mfma_f32_16x16x32_bf16 v[188:191], a[44:47], v[84:87], v[188:191]\n" \
"s_cmp_lg_u32 s82, 0\n" \
"v_exp_f32_e64 v55, v55\n" \
"v_mfma_f32_16x16x32_bf16 v[156:159], a[40:43], v[64:67], v[156:159]\n" \
"s_waitcnt lgkmcnt(2)\n" \
"ds_read_b128 a[252:255], v232 offset:14592\n" \
"v_cvt_pk_bf16_f32 v51, v54, v55\n" \
"v_mfma_f32_16x16x32_bf16 v[156:159], a[44:47], v[68:71], v[156:159]\n" \
"s_cbranch_scc1 Lep_%=\n" \
"Lepr_%=:\n" \
"v_mfma_f32_16x16x32_bf16 v[64:67], a[192:195], a[0:3], v[196:199]\n" \
"v_exp_f32 v8, v8\n" \
"v_mfma_f32_16x16x32_bf16 v[64:67], a[196:199], a[4:7], v[64:67]\n" \
"ds_read_b128 a[44:47], v233 offset:15360\n" \
"v_mfma_f32_16x16x32_bf16 v[64:67], a[200:203], a[8:11], v[64:67]\n" \
"v_exp_f32 v9, v9\n" \
"v_mfma_f32_16x16x32_bf16 v[64:67], a[204:207], a[12:15], v[64:67]\n" \
"ds_read_b128 a[32:35], v233 offset:6144\n" \
"v_cvt_pk_bf16_f32 v4, v8, v9\n" \
"v_mfma_f32_16x16x32_bf16 v[80:83], a[204:207], a[28:31], v[196:199]\n" \
"s_sub_u32 s84, s84, 1\n" \
"v_exp_f32 v10, v10\n" \
"v_mfma_f32_16x16x32_bf16 v[80:83], a[200:203], a[24:27], v[80:83]\n" \
"s_cmp_eq_u32 s84, 0\n" \
"s_cselect_b32 s70, s75, 0x8000\n" \
"v_xor_b32 v232, s70, v232\n" \
"v_mfma_f32_16x16x32_bf16 v[80:83], a[196:199], a[20:23], v[80:83]\n" \
"v_exp_f32 v11, v11\n" \
"v_mfma_f32_16x16x32_bf16 v[80:83], a[192:195], a[16:19], v[80:83]\n" \
"ds_read_b128 a[36:39], v233 offset:14336\n" \
"v_cvt_pk_bf16_f32 v5, v10, v11\n" \
"v_mfma_f32_16x16x32_bf16 v[96:99], a[192:195], a[160:163], v[196:199]\n" \
"v_exp_f32 v12, v12\n" \
"v_mfma_f32_16x16x32_bf16 v[96:99], a[196:199], a[164:167], v[96:99]\n" \
"v_mfma_f32_16x16x32_bf16 v[96:99], a[200:203], a[168:171], v[96:99]\n" \
"v_exp_f32 v13, v13\n" \
"v_mfma_f32_16x16x32_bf16 v[96:99], a[204:207], a[172:175], v[96:99]\n" \
"ds_read_b128 a[40:43], v233 offset:7168\n" \
"v_cvt_pk_bf16_f32 v6, v12, v13\n" \
"v_mfma_f32_16x16x32_bf16 v[112:115], a[204:207], a[188:191], v[196:199]\n" \
"v_exp_f32 v14, v14\n" \
"v_mfma_f32_16x16x32_bf16 v[112:115], a[200:203], a[184:187], v[112:115]\n" \
"v_mfma_f32_16x16x32_bf16 v[112:115], a[196:199], a[180:183], v[112:115]\n" \
"v_exp_f32 v15, v15\n" \
"v_mfma_f32_16x16x32_bf16 v[112:115], a[192:195], a[176:179], v[112:115]\n" \
"ds_read_b128 a[204:207], v233 offset:9216\n" \
"v_cvt_pk_bf16_f32 v7, v14, v15\n" \
"v_mfma_f32_16x16x32_bf16 v[116:119], a[208:211], a[176:179], v[196:199]\n" \
"v_exp_f32 v24, v24\n" \
"v_mfma_f32_16x16x32_bf16 v[116:119], a[212:215], a[180:183], v[116:119]\n" \
"v_mfma_f32_16x16x32_bf16 v[116:119], a[216:219], a[184:187], v[116:119]\n" \
"v_exp_f32 v25, v25\n" \
"v_mfma_f32_16x16x32_bf16 v[116:119], a[220:223], a[188:191], v[116:119]\n" \
"ds_read_b128 a[200:203], v233 offset:1024\n" \
"v_cvt_pk_bf16_f32 v20, v24, v25\n" \
"v_mfma_f32_16x16x32_bf16 v[100:103], a[220:223], a[172:175], v[196:199]\n" \
"v_exp_f32 v26, v26\n" \
"v_mfma_f32_16x16x32_bf16 v[100:103], a[216:219], a[168:171], v[100:103]\n" \
"v_mfma_f32_16x16x32_bf16 v[100:103], a[212:215], a[164:167], v[100:103]\n" \
"v_exp_f32 v27, v27\n" \
"v_mfma_f32_16x16x32_bf16 v[100:103], a[208:211], a[160:163], v[100:103]\n" \
"ds_read_b128 a[196:199], v233 offset:8192\n" \
"v_cvt_pk_bf16_f32 v21, v26, v27\n" \
"v_mfma_f32_16x16x32_bf16 v[84:87], a[208:211], a[16:19], v[196:199]\n" \
"v_exp_f32 v28, v28\n" \
"v_mfma_f32_16x16x32_bf16 v[84:87], a[212:215], a[20:23], v[84:87]\n" \
"v_mfma_f32_16x16x32_bf16 v[84:87], a[216:219], a[24:27], v[84:87]\n" \
"v_exp_f32 v29, v29\n" \
"v_mfma_f32_16x16x32_bf16 v[84:87], a[220:223], a[28:31], v[84:87]\n" \
"ds_read_b128 a[192:195], v233 offset:0\n" \
"v_cvt_pk_bf16_f32 v22, v28, v29\n" \
"v_mfma_f32_16x16x32_bf16 v[68:71], a[220:223], a[12:15], v[196:199]\n" \
"v_exp_f32 v30, v30\n" \
"v_mfma_f32_16x16x32_bf16 v[68:71], a[216:219], a[8:11], v[68:71]\n" \
"v_mfma_f32_16x16x32_bf16 v[68:71], a[212:215], a[4:7], v[68:71]\n" \
"s_waitcnt lgkmcnt(8)\n" \
"v_exp_f32 v31, v31\n" \
"v_mfma_f32_16x16x32_bf16 v[68:71], a[208:211], a[0:3], v[68:71]\n" \
"ds_read_b128 a[220:223], v233 offset:11264\n" \
"v_cvt_pk_bf16_f32 v23, v30, v31\n" \
"v_mfma_f32_16x16x32_bf16 v[72:75], a[224:227], a[0:3], v[196:199]\n" \
"v_exp_f32 v40, v40\n" \
"v_mfma_f32_16x16x32_bf16 v[72:75], a[228:231], a[4:7], v[72:75]\n" \
"v_mfma_f32_16x16x32_bf16 v[72:75], a[232:235], a[8:11], v[72:75]\n" \
"v_exp_f32 v41, v41\n" \
"v_mfma_f32_16x16x32_bf16 v[72:75], a[236:239], a[12:15], v[72:75]\n" \
"ds_read_b128 a[216:219], v233 offset:3072\n" \
"v_cvt_pk_bf16_f32 v36, v40, v41\n" \
"v_mfma_f32_16x16x32_bf16 v[88:91], a[236:239], a[28:31], v[196:199]\n" \
"v_exp_f32 v42, v42\n" \
"v_mfma_f32_16x16x32_bf16 v[88:91], a[232:235], a[24:27], v[88:91]\n" \
"v_mfma_f32_16x16x32_bf16 v[88:91], a[228:231], a[20:23], v[88:91]\n" \
"v_exp_f32 v43, v43\n" \
"v_mfma_f32_16x16x32_bf16 v[88:91], a[224:227], a[16:19], v[88:91]\n" \
"ds_read_b128 a[212:215], v233 offset:10240\n" \
"v_cvt_pk_bf16_f32 v37, v42, v43\n" \
"v_mfma_f32_16x16x32_bf16 v[104:107], a[224:227], a[160:163], v[196:199]\n" \
"v_exp_f32 v44, v44\n" \
"v_mfma_f32_16x16x32_bf16 v[104:107], a[228:231], a[164:167], v[104:107]\n" \
"v_mfma_f32_16x16x32_bf16 v[104:107], a[232:235], a[168:171], v[104:107]\n" \
"v_exp_f32 v45, v45\n" \
"v_mfma_f32_16x16x32_bf16 v[104:107], a[236:239], a[172:175], v[104:107]\n" \
"ds_read_b128 a[208:211], v233 offset:2048\n" \
"v_cvt_pk_bf16_f32 v38, v44, v45\n" \
"v_mfma_f32_16x16x32_bf16 v[120:123], a[236:239], a[188:191], v[196:199]\n" \
"v_exp_f32 v46, v46\n" \
"v_mfma_f32_16x16x32_bf16 v[120:123], a[232:235], a[184:187], v[120:123]\n" \
"v_mfma_f32_16x16x32_bf16 v[120:123], a[228:231], a[180:183], v[120:123]\n" \
"v_exp_f32 v47, v47\n" \
"v_mfma_f32_16x16x32_bf16 v[120:123], a[224:227], a[176:179], v[120:123]\n" \
"ds_read_b128 a[236:239], v233 offset:13312\n" \
"v_cvt_pk_bf16_f32 v39, v46, v47\n" \
"v_mfma_f32_16x16x32_bf16 v[124:127], a[240:243], a[176:179], v[196:199]\n" \
"v_exp_f32 v56, v56\n" \
"v_mfma_f32_16x16x32_bf16 v[124:127], a[244:247], a[180:183], v[124:127]\n" \
"ds_read_b128 a[232:235], v233 offset:5120\n" \
"v_mfma_f32_16x16x32_bf16 v[124:127], a[248:251], a[184:187], v[124:127]\n" \
"v_exp_f32 v57, v57\n" \
"v_mfma_f32_16x16x32_bf16 v[124:127], a[252:255], a[188:191], v[124:127]\n" \
"ds_read_b128 a[228:231], v233 offset:12288\n" \
"v_cvt_pk_bf16_f32 v52, v56, v57\n" \
"v_mfma_f32_16x16x32_bf16 v[108:111], a[252:255], a[172:175], v[196:199]\n" \
"v_exp_f32 v58, v58\n" \
"v_mfma_f32_16x16x32_bf16 v[108:111], a[248:251], a[168:171], v[108:111]\n" \
"ds_read_b128 a[224:227], v233 offset:4096\n" \
"v_mfma_f32_16x16x32_bf16 v[108:111], a[244:247], a[164:167], v[108:111]\n" \
"v_exp_f32 v59, v59\n" \
"v_mfma_f32_16x16x32_bf16 v[108:111], a[240:243], a[160:163], v[108:111]\n" \
"v_cvt_pk_bf16_f32 v53, v58, v59\n" \
"v_mfma_f32_16x16x32_bf16 v[92:95], a[240:243], a[16:19], v[196:199]\n" \
"v_exp_f32 v60, v60\n" \
"v_mfma_f32_16x16x32_bf16 v[92:95], a[244:247], a[20:23], v[92:95]\n" \
"v_mfma_f32_16x16x32_bf16 v[92:95], a[248:251], a[24:27], v[92:95]\n" \
"v_exp_f32 v61, v61\n" \
"v_mfma_f32_16x16x32_bf16 v[92:95], a[252:255], a[28:31], v[92:95]\n" \
"v_cvt_pk_bf16_f32 v54, v60, v61\n" \
"v_mfma_f32_16x16x32_bf16 v[76:79], a[252:255], a[12:15], v[196:199]\n" \
"v_exp_f32 v62, v62\n" \
"v_mfma_f32_16x16x32_bf16 v[76:79], a[248:251], a[8:11], v[76:79]\n" \
"s_cmp_lg_u32 s82, 0\n" \
"s_cbranch_scc1 Lnw_%=\n" \
"s_waitcnt vmcnt(0)\n" \
"Lnw_%=:\n" \
"v_mfma_f32_16x16x32_bf16 v[76:79], a[244:247], a[4:7], v[76:79]\n" \
"s_waitcnt lgkmcnt(0)\n" \
"v_exp_f32 v63, v63\n" \
"v_mfma_f32_16x16x32_bf16 v[76:79], a[240:243], a[0:3], v[76:79]\n" \
"s_barrier\n" \
"v_cvt_pk_bf16_f32 v55, v62, v63\n" \
"v_mfma_f32_16x16x32_bf16 v[128:131], a[192:195], v[0:3], v[128:131]\n" \
"s_add_u32 m0, s74, s79\n" \
"v_exp_f32 v64, v64\n" \
"v_mfma_f32_16x16x32_bf16 v[128:131], a[196:199], v[4:7], v[128:131]\n" \
"buffer_load_dwordx4 v234, s[36:39], s73 offen offset:0 sc1 lds\n" \
"v_mfma_f32_16x16x32_bf16 v[160:163], a[192:195], v[16:19], v[160:163]\n" \
"v_exp_f32 v65, v65\n" \
"v_mfma_f32_16x16x32_bf16 v[160:163], a[196:199], v[20:23], v[160:163]\n" \
"ds_read_b128 a[240:243], v232 offset:8448\n" \
"v_cvt_pk_bf16_f32 v64, v64, v65\n" \
"v_mfma_f32_16x16x32_bf16 a[64:67], a[192:195], v[32:35], a[64:67]\n" \
"s_add_u32 s71, s73, 17408\n" \
"v_exp_f32 v66, v66\n" \
"v_mfma_f32_16x16x32_bf16 a[64:67], a[196:199], v[36:39], a[64:67]\n" \
"buffer_load_dwordx4 v234, s[36:39], s73 offen offset:1024 sc1 lds\n" \
"v_mfma_f32_16x16x32_bf16 a[96:99], a[192:195], v[48:51], a[96:99]\n" \
"v_exp_f32 v67, v67\n" \
"v_mfma_f32_16x16x32_bf16 a[96:99], a[196:199], v[52:55], a[96:99]\n" \
"ds_read_b128 a[192:195], v232 offset:0\n" \
"v_cvt_pk_bf16_f32 v65, v66, v67\n" \
"v_mfma_f32_16x16x32_bf16 a[100:103], a[200:203], v[48:51], a[100:103]\n" \
"v_exp_f32 v68, v68\n" \
"v_mfma_f32_16x16x32_bf16 a[100:103], a[204:207], v[52:55], a[100:103]\n" \
"buffer_load_dwordx4 v234, s[36:39], s73 offen offset:2048 sc1 lds\n" \
"v_mfma_f32_16x16x32_bf16 a[68:71], a[200:203], v[32:35], a[68:71]\n" \
"v_exp_f32 v69, v69\n" \
"v_mfma_f32_16x16x32_bf16 a[68:71], a[204:207], v[36:39], a[68:71]\n" \
"ds_read_b128 a[196:199], v232 offset:2048\n" \
"v_cvt_pk_bf16_f32 v66, v68, v69\n" \
"v_smfmac_f32_16x16x64_bf16 a[48:51], v[244:247], v[0:7], v243\n" \
"buffer_load_dwordx4 v234, s[36:39], s73 offen offset:3072 sc1 lds\n" \
"v_mfma_f32_16x16x32_bf16 v[164:167], a[200:203], v[16:19], v[164:167]\n" \
"s_add_u32 m0, m0, 0x4000\n" \
"v_exp_f32 v70, v70\n" \
"v_mfma_f32_16x16x32_bf16 v[164:167], a[204:207], v[20:23], v[164:167]\n" \
"buffer_load_dwordx4 v234, s[36:39], s71 offen offset:0 sc1 lds\n" \
"v_mfma_f32_16x16x32_bf16 v[132:135], a[200:203], v[0:3], v[132:135]\n" \
"v_exp_f32 v71, v71\n" \
"v_mfma_f32_16x16x32_bf16 v[132:135], a[204:207], v[4:7], v[132:135]\n" \
"ds_read_b128 a[200:203], v232 offset:4096\n" \
"v_cvt_pk_bf16_f32 v67, v70, v71\n" \
"v_mfma_f32_16x16x32_bf16 v[136:139], a[208:211], v[0:3], v[136:139]\n" \
"v_exp_f32 v80, v80\n" \
"v_mfma_f32_16x16x32_bf16 v[136:139], a[212:215], v[4:7], v[136:139]\n" \
"buffer_load_dwordx4 v234, s[36:39], s71 offen offset:1024 sc1 lds\n" \
"v_mfma_f32_16x16x32_bf16 v[168:171], a[208:211], v[16:19], v[168:171]\n" \
"v_exp_f32 v81, v81\n" \
"v_mfma_f32_16x16x32_bf16 v[168:171], a[212:215], v[20:23], v[168:171]\n" \
"ds_read_b128 a[204:207], v232 offset:6144\n" \
"v_cvt_pk_bf16_f32 v80, v80, v81\n" \
"v_mfma_f32_16x16x32_bf16 a[72:75], a[208:211], v[32:35], a[72:75]\n" \
"v_exp_f32 v82, v82\n" \
"v_mfma_f32_16x16x32_bf16 a[72:75], a[212:215], v[36:39], a[72:75]\n" \
"buffer_load_dwordx4 v234, s[36:39], s71 offen offset:2048 sc1 lds\n" \
"v_mfma_f32_16x16x32_bf16 a[104:107], a[208:211], v[48:51], a[104:107]\n" \
"v_exp_f32 v83, v83\n" \
"v_mfma_f32_16x16x32_bf16 a[104:107], a[212:215], v[52:55], a[104:107]\n" \
"ds_read_b128 a[208:211], v232 offset:256\n" \
"v_cvt_pk_bf16_f32 v81, v82, v83\n" \
"v_mfma_f32_16x16x32_bf16 a[108:111], a[216:219], v[48:51], a[108:111]\n" \
"v_exp_f32 v84, v84\n" \
"v_mfma_f32_16x16x32_bf16 a[108:111], a[220:223], v[52:55], a[108:111]\n" \
"buffer_load_dwordx4 v234, s[36:39], s71 offen offset:3072 sc1 lds\n" \
"v_mfma_f32_16x16x32_bf16 a[76:79], a[216:219], v[32:35], a[76:79]\n" \
"v_exp_f32 v85, v85\n" \
"v_mfma_f32_16x16x32_bf16 a[76:79], a[220:223], v[36:39], a[76:79]\n" \
"s_add_u32 m0, s77, s80\n" \
"ds_read_b128 a[212:215], v232 offset:2304\n" \
"v_cvt_pk_bf16_f32 v82, v84, v85\n" \
"v_smfmac_f32_16x16x64_bf16 a[52:55], v[244:247], v[16:23], v243\n" \
"buffer_load_dwordx4 v234, s[40:43], s76 offen offset:0 sc1 lds\n" \
"v_mfma_f32_16x16x32_bf16 v[172:175], a[216:219], v[16:19], v[172:175]\n" \
"s_add_u32 s71, s76, 17408\n" \
"v_exp_f32 v86, v86\n" \
"v_mfma_f32_16x16x32_bf16 v[172:175], a[220:223], v[20:23], v[172:175]\n" \
"buffer_load_dwordx4 v234, s[40:43], s76 offen offset:1024 sc1 lds\n" \
"v_mfma_f32_16x16x32_bf16 v[140:143], a[216:219], v[0:3], v[140:143]\n" \
"v_exp_f32 v87, v87\n" \
"v_mfma_f32_16x16x32_bf16 v[140:143], a[220:223], v[4:7], v[140:143]\n" \
"ds_read_b128 a[216:219], v232 offset:4352\n" \
"v_cvt_pk_bf16_f32 v83, v86, v87\n" \
"v_mfma_f32_16x16x32_bf16 v[144:147], a[224:227], v[0:3], v[144:147]\n" \
"v_exp_f32 v96, v96\n" \
"v_mfma_f32_16x16x32_bf16 v[144:147], a[228:231], v[4:7], v[144:147]\n" \
"buffer_load_dwordx4 v234, s[40:43], s76 offen offset:2048 sc1 lds\n" \
"v_mfma_f32_16x16x32_bf16 v[176:179], a[224:227], v[16:19], v[176:179]\n" \
"v_exp_f32 v97, v97\n" \
"v_mfma_f32_16x16x32_bf16 v[176:179], a[228:231], v[20:23], v[176:179]\n" \
"ds_read_b128 a[220:223], v232 offset:6400\n" \
"v_cvt_pk_bf16_f32 v96, v96, v97\n" \
"v_mfma_f32_16x16x32_bf16 a[80:83], a[224:227], v[32:35], a[80:83]\n" \
"v_exp_f32 v98, v98\n" \
"v_mfma_f32_16x16x32_bf16 a[80:83], a[228:231], v[36:39], a[80:83]\n" \
"buffer_load_dwordx4 v234, s[40:43], s76 offen offset:3072 sc1 lds\n" \
"v_mfma_f32_16x16x32_bf16 a[112:115], a[224:227], v[48:51], a[112:115]\n" \
"v_exp_f32 v99, v99\n" \
"v_mfma_f32_16x16x32_bf16 a[112:115], a[228:231], v[52:55], a[112:115]\n" \
"ds_read_b128 a[224:227], v232 offset:8192\n" \
"v_cvt_pk_bf16_f32 v97, v98, v99\n" \
"v_mfma_f32_16x16x32_bf16 a[116:119], a[232:235], v[48:51], a[116:119]\n" \
"s_add_u32 m0, m0, 0x4000\n" \
"v_exp_f32 v100, v100\n" \
"v_mfma_f32_16x16x32_bf16 a[116:119], a[236:239], v[52:55], a[116:119]\n" \
"buffer_load_dwordx4 v234, s[40:43], s71 offen offset:0 sc1 lds\n" \
"v_mfma_f32_16x16x32_bf16 a[84:87], a[232:235], v[32:35], a[84:87]\n" \
"v_exp_f32 v101, v101\n" \
"v_mfma_f32_16x16x32_bf16 a[84:87], a[236:239], v[36:39], a[84:87]\n" \
"ds_read_b128 a[228:231], v232 offset:10240\n" \
"v_cvt_pk_bf16_f32 v98, v100, v101\n" \
"v_smfmac_f32_16x16x64_bf16 v[224:227], v[244:247], v[32:39], v243\n" \
"buffer_load_dwordx4 v234, s[40:43], s71 offen offset:1024 sc1 lds\n" \
"v_mfma_f32_16x16x32_bf16 v[180:183], a[232:235], v[16:19], v[180:183]\n" \
"v_exp_f32 v102, v102\n" \
"v_mfma_f32_16x16x32_bf16 v[180:183], a[236:239], v[20:23], v[180:183]\n" \
"buffer_load_dwordx4 v234, s[40:43], s71 offen offset:2048 sc1 lds\n" \
"v_mfma_f32_16x16x32_bf16 v[148:151], a[232:235], v[0:3], v[148:151]\n" \
"v_exp_f32 v103, v103\n" \
"v_mfma_f32_16x16x32_bf16 v[148:151], a[236:239], v[4:7], v[148:151]\n" \
"ds_read_b128 a[232:235], v232 offset:12288\n" \
"v_cvt_pk_bf16_f32 v99, v102, v103\n" \
"v_mfma_f32_16x16x32_bf16 v[152:155], a[32:35], v[0:3], v[152:155]\n" \
"v_exp_f32 v112, v112\n" \
"v_mfma_f32_16x16x32_bf16 v[152:155], a[36:39], v[4:7], v[152:155]\n" \
"buffer_load_dwordx4 v234, s[40:43], s71 offen offset:3072 sc1 lds\n" \
"v_mfma_f32_16x16x32_bf16 v[184:187], a[32:35], v[16:19], v[184:187]\n" \
"v_exp_f32 v113, v113\n" \
"v_mfma_f32_16x16x32_bf16 v[184:187], a[36:39], v[20:23], v[184:187]\n" \
"ds_read_b128 a[236:239], v232 offset:14336\n" \
"v_cvt_pk_bf16_f32 v112, v112, v113\n" \
"v_mfma_f32_16x16x32_bf16 a[88:91], a[32:35], v[32:35], a[88:91]\n" \
"v_exp_f32 v114, v114\n" \
"v_mfma_f32_16x16x32_bf16 a[88:91], a[36:39], v[36:39], a[88:91]\n" \
"buffer_load_dword v239, v242, s[56:59], s95 offen sc1\n" \
"v_mfma_f32_16x16x32_bf16 a[120:123], a[32:35], v[48:51], a[120:123]\n" \
"s_mov_b32 s69, s92\n" \
"v_exp_f32 v115, v115\n" \
"v_mfma_f32_16x16x32_bf16 a[120:123], a[36:39], v[52:55], a[120:123]\n" \
"s_mov_b32 s76, s73\n" \
"ds_read_b128 a[244:247], v232 offset:10496\n" \
"v_cvt_pk_bf16_f32 v113, v114, v115\n" \
"v_mfma_f32_16x16x32_bf16 a[124:127], a[40:43], v[48:51], a[124:127]\n" \
"s_add_u32 s73, s73, 34816\n" \
"v_exp_f32 v116, v116\n" \
"v_mfma_f32_16x16x32_bf16 a[124:127], a[44:47], v[52:55], a[124:127]\n" \
"s_xor_b32 s74, s74, 0x8000\n" \
"s_xor_b32 s77, s77, 0x8000\n" \
"v_xor_b32 v233, 0x8000, v233\n" \
"v_mfma_f32_16x16x32_bf16 a[92:95], a[40:43], v[32:35], a[92:95]\n" \
"s_mov_b32 s82, 0\n" \
"v_exp_f32 v117, v117\n" \
"v_mfma_f32_16x16x32_bf16 a[92:95], a[44:47], v[36:39], a[92:95]\n" \
"ds_read_b128 a[248:251], v232 offset:12544\n" \
"v_cvt_pk_bf16_f32 v114, v116, v117\n" \
"Lpg6773_%=:\n" \
"s_cbranch_execz Lcb0_%=\n" \
"v_smfmac_f32_16x16x64_bf16 v[228:231], v[244:247], v[48:55], v243\n" \
"v_mfma_f32_16x16x32_bf16 v[188:191], a[40:43], v[16:19], v[188:191]\n" \
"v_exp_f32_e64 v118, v118\n" \
"v_mfma_f32_16x16x32_bf16 v[188:191], a[44:47], v[20:23], v[188:191]\n" \
"s_cmp_lg_u32 s84, 0\n" \
"v_exp_f32_e64 v119, v119\n" \
"v_mfma_f32_16x16x32_bf16 v[156:159], a[40:43], v[0:3], v[156:159]\n" \
"s_waitcnt lgkmcnt(2)\n" \
"ds_read_b128 a[252:255], v232 offset:14592\n" \
"v_cvt_pk_bf16_f32 v115, v118, v119\n" \
"v_mfma_f32_16x16x32_bf16 v[156:159], a[44:47], v[4:7], v[156:159]\n" \
"s_cbranch_scc1 Lloop2_%=\n" \
"Lg_%=:\n" \
"Lpg6787_%=:\n" \
"s_cbranch_execz Lcb0_%=\n" \
"v_mfma_f32_16x16x32_bf16 v[32:35], a[192:195], a[160:163], v[208:211]\n" \
"s_mov_b64 s[44:45], s[20:21]\n" \
"s_memrealtime s[14:15]\n" \
"v_exp_f32 v72, v72\n" \
"v_mfma_f32_16x16x32_bf16 v[32:35], a[196:199], a[164:167], v[32:35]\n" \
"s_and_b32 s45, s45, 0xffff\n" \
"ds_read_b128 a[32:35], v233 offset:6144\n" \
"ds_read_b128 a[36:39], v233 offset:14336\n" \
"v_mfma_f32_16x16x32_bf16 v[32:35], a[200:203], a[168:171], v[32:35]\n" \
"s_mov_b32 s46, 4194304\n" \
"v_exp_f32 v73, v73\n" \
"v_mfma_f32_16x16x32_bf16 v[32:35], a[204:207], a[172:175], v[32:35]\n" \
"ds_read_b128 a[40:43], v233 offset:7168\n" \
"ds_read_b128 a[44:47], v233 offset:15360\n" \
"global_load_dword v253, v254, s[22:23]\n" \
"v_mfma_f32_16x16x32_bf16 v[48:51], a[204:207], a[188:191], v[196:199]\n" \
"s_mov_b32 s47, 0x20000\n" \
"v_exp_f32 v74, v74\n" \
"v_mfma_f32_16x16x32_bf16 v[48:51], a[200:203], a[184:187], v[48:51]\n" \
"s_mov_b64 s[48:49], s[22:23]\n" \
"v_exp_f32 v75, v75\n" \
"v_mfma_f32_16x16x32_bf16 v[48:51], a[196:199], a[180:183], v[48:51]\n" \
"s_and_b32 s49, s49, 0xffff\n" \
"v_exp_f32 v76, v76\n" \
"v_mfma_f32_16x16x32_bf16 v[48:51], a[192:195], a[176:179], v[48:51]\n" \
"s_mov_b32 s50, 6291456\n" \
"ds_read_b128 a[200:203], v233 offset:1024\n" \
"ds_read_b128 a[204:207], v233 offset:9216\n" \
"v_mfma_f32_16x16x32_bf16 v[52:55], a[208:211], a[176:179], v[196:199]\n" \
"s_mov_b32 s51, 0x20000\n" \
"v_cvt_pk_bf16_f32 v68, v72, v73\n" \
"v_cvt_pk_bf16_f32 v69, v74, v75\n" \
"v_mfma_f32_16x16x32_bf16 v[52:55], a[212:215], a[180:183], v[52:55]\n" \
"s_bfe_u32 s94, %1, 0x20001\n" \
"v_exp_f32 v77, v77\n" \
"v_mfma_f32_16x16x32_bf16 v[52:55], a[216:219], a[184:187], v[52:55]\n" \
"s_lshl_b32 s94, s94, 2\n" \
"v_exp_f32 v78, v78\n" \
"v_mfma_f32_16x16x32_bf16 v[52:55], a[220:223], a[188:191], v[52:55]\n" \
"s_lshr_b32 s70, %1, 6\n" \
"ds_read_b128 a[192:195], v233 offset:0\n" \
"ds_read_b128 a[196:199], v233 offset:8192\n" \
"v_mfma_f32_16x16x32_bf16 v[36:39], a[220:223], a[172:175], v[212:215]\n" \
"s_add_u32 s94, s94, s70\n" \
"v_exp_f32 v79, v79\n" \
"v_mfma_f32_16x16x32_bf16 v[36:39], a[216:219], a[168:171], v[36:39]\n" \
"s_lshl_b32 s73, s94, 18\n" \
"v_exp_f32 v88, v88\n" \
"v_mfma_f32_16x16x32_bf16 v[36:39], a[212:215], a[164:167], v[36:39]\n" \
"s_max_u32 s75, s73, 32768\n" \
"v_cvt_pk_bf16_f32 v70, v76, v77\n" \
"v_cvt_pk_bf16_f32 v71, v78, v79\n" \
"v_mfma_f32_16x16x32_bf16 v[36:39], a[208:211], a[160:163], v[36:39]\n" \
"s_sub_u32 s75, s75, 32768\n" \
"ds_read_b128 a[216:219], v233 offset:3072\n" \
"ds_read_b128 a[220:223], v233 offset:11264\n" \
"v_mfma_f32_16x16x32_bf16 v[40:43], a[224:227], a[160:163], v[216:219]\n" \
"s_and_b32 s70, s61, 1\n" \
"v_exp_f32 v89, v89\n" \
"v_mfma_f32_16x16x32_bf16 v[40:43], a[228:231], a[164:167], v[40:43]\n" \
"s_lshr_b32 s71, s61, 1\n" \
"v_exp_f32 v90, v90\n" \
"v_mfma_f32_16x16x32_bf16 v[40:43], a[232:235], a[168:171], v[40:43]\n" \
"s_lshl1_add_u32 s70, s70, s71\n" \
"v_exp_f32 v91, v91\n" \
"v_mfma_f32_16x16x32_bf16 v[40:43], a[236:239], a[172:175], v[40:43]\n" \
"s_lshl_b32 s70, s70, 10\n" \
"ds_read_b128 a[208:211], v233 offset:2048\n" \
"ds_read_b128 a[212:215], v233 offset:10240\n" \
"v_mfma_f32_16x16x32_bf16 v[56:59], a[236:239], a[188:191], v[196:199]\n" \
"s_add_u32 s75, s75, s70\n" \
"v_exp_f32 v92, v92\n" \
"v_mfma_f32_16x16x32_bf16 v[56:59], a[232:235], a[184:187], v[56:59]\n" \
"s_lshl_b32 s89, s61, 13\n" \
"v_cvt_pk_bf16_f32 v84, v88, v89\n" \
"v_cvt_pk_bf16_f32 v85, v90, v91\n" \
"v_mfma_f32_16x16x32_bf16 v[56:59], a[228:231], a[180:183], v[56:59]\n" \
"s_add_u32 s89, s89, %3\n" \
"v_exp_f32 v93, v93\n" \
"v_mfma_f32_16x16x32_bf16 v[56:59], a[224:227], a[176:179], v[56:59]\n" \
"s_lshr_b32 s71, s22, 7\n" \
"ds_read_b128 a[232:235], v233 offset:5120\n" \
"ds_read_b128 a[236:239], v233 offset:13312\n" \
"v_mfma_f32_16x16x32_bf16 v[60:63], a[240:243], a[176:179], v[196:199]\n" \
"s_waitcnt lgkmcnt(2)\n" \
"s_sub_u32 s71, s63, s71\n" \
"v_exp_f32 v94, v94\n" \
"v_mfma_f32_16x16x32_bf16 v[60:63], a[244:247], a[180:183], v[60:63]\n" \
"s_add_u32 s71, s71, 6\n" \
"v_exp_f32 v95, v95\n" \
"v_mfma_f32_16x16x32_bf16 v[60:63], a[248:251], a[184:187], v[60:63]\n" \
"s_and_b32 s71, s71, 7\n" \
"v_cvt_pk_bf16_f32 v86, v92, v93\n" \
"v_accvgpr_read_b32 v192, a57\n" \
"v_mfma_f32_16x16x32_bf16 v[60:63], a[252:255], a[188:191], v[60:63]\n" \
"s_and_b32 s70, %1, 1\n" \
"ds_read_b128 a[224:227], v233 offset:4096\n" \
"ds_read_b128 a[228:231], v233 offset:12288\n" \
"v_mfma_f32_16x16x32_bf16 v[44:47], a[252:255], a[172:175], v[220:223]\n" \
"s_lshl_b32 s70, s70, 3\n" \
"v_cvt_pk_bf16_f32 v87, v94, v95\n" \
"v_mfma_f32_16x16x32_bf16 v[44:47], a[248:251], a[168:171], v[44:47]\n" \
"s_add_u32 s70, s70, s71\n" \
"v_exp_f32 v104, v104\n" \
"v_mfma_f32_16x16x32_bf16 v[44:47], a[244:247], a[164:167], v[44:47]\n" \
"s_lshl_b32 s78, s70, 7\n" \
"v_accvgpr_read_b32 v197, a56\n" \
"v_add_u32 v198, 1024, v197\n" \
"v_mfma_f32_16x16x32_bf16 v[44:47], a[240:243], a[160:163], v[44:47]\n" \
"s_lshr_b32 s80, %1, 6\n" \
"v_add_u32 v199, 2048, v197\n" \
"v_add_u32 v193, 31744, v192\n" \
"v_mfma_f32_16x16x32_bf16 v[128:131], a[192:195], v[64:67], v[128:131]\n" \
"s_xor_b32 s80, s80, s63\n" \
"v_exp_f32 v105, v105\n" \
"v_mfma_f32_16x16x32_bf16 v[128:131], a[196:199], v[68:71], v[128:131]\n" \
"s_and_b32 s80, s80, 0\n" \
"ds_read_b128 a[0:3], v232 offset:16384\n" \
"ds_read_b128 a[4:7], v232 offset:18432\n" \
"v_mfma_f32_16x16x32_bf16 v[160:163], a[192:195], v[80:83], v[160:163]\n" \
"s_add_u32 m0, s89, 131072\n" \
"v_exp_f32 v106, v106\n" \
"v_mfma_f32_16x16x32_bf16 v[160:163], a[196:199], v[84:87], v[160:163]\n" \
"buffer_load_dwordx4 v192, s[44:47], s75 offen offset:0 lds\n" \
"v_exp_f32 v107, v107\n" \
"v_mfma_f32_16x16x32_bf16 v[164:167], a[200:203], v[80:83], v[164:167]\n" \
"s_lshl_b32 s93, s70, 12\n" \
"ds_read_b128 a[8:11], v232 offset:20480\n" \
"ds_read_b128 a[12:15], v232 offset:22528\n" \
"v_mfma_f32_16x16x32_bf16 v[164:167], a[204:207], v[84:87], v[164:167]\n" \
"s_add_u32 m0, s89, 131968\n" \
"v_exp_f32 v108, v108\n" \
"v_mfma_f32_16x16x32_bf16 v[132:135], a[200:203], v[64:67], v[132:135]\n" \
"buffer_load_dwordx4 v192, s[44:47], s75 offen offset:128 lds\n" \
"v_mfma_f32_16x16x32_bf16 v[132:135], a[204:207], v[68:71], v[132:135]\n" \
"s_mul_i32 s91, s61, 6144\n" \
"ds_read_b128 a[16:19], v232 offset:16640\n" \
"ds_read_b128 a[20:23], v232 offset:18688\n" \
"v_cvt_pk_bf16_f32 v100, v104, v105\n" \
"v_smfmac_f32_16x16x64_bf16 a[48:51], v[244:247], v[64:71], v243\n" \
"s_add_u32 m0, s89, 132864\n" \
"v_cvt_pk_bf16_f32 v101, v106, v107\n" \
"v_mfma_f32_16x16x32_bf16 v[136:139], a[208:211], v[64:67], v[136:139]\n" \
"buffer_load_dwordx4 v192, s[44:47], s75 offen offset:256 lds\n" \
"v_mfma_f32_16x16x32_bf16 v[136:139], a[212:215], v[68:71], v[136:139]\n" \
"s_add_u32 s91, s91, %3\n" \
"ds_read_b128 a[24:27], v232 offset:20736\n" \
"ds_read_b128 a[28:31], v232 offset:22784\n" \
"v_mfma_f32_16x16x32_bf16 v[168:171], a[208:211], v[80:83], v[168:171]\n" \
"s_add_u32 m0, s89, 133760\n" \
"v_exp_f32 v109, v109\n" \
"v_mfma_f32_16x16x32_bf16 v[168:171], a[212:215], v[84:87], v[168:171]\n" \
"buffer_load_dwordx4 v192, s[44:47], s75 offen offset:384 lds\n" \
"v_mfma_f32_16x16x32_bf16 v[172:175], a[216:219], v[80:83], v[172:175]\n" \
"s_lshl_b32 s71, s80, 7\n" \
"ds_read_b128 v[0:3], v232 offset:24576\n" \
"ds_read_b128 v[4:7], v232 offset:26624\n" \
"v_mfma_f32_16x16x32_bf16 v[172:175], a[220:223], v[84:87], v[172:175]\n" \
"s_add_u32 m0, s89, 134656\n" \
"v_exp_f32 v110, v110\n" \
"v_mfma_f32_16x16x32_bf16 v[140:143], a[216:219], v[64:67], v[140:143]\n" \
"buffer_load_dwordx4 v192, s[44:47], s75 offen offset:512 lds\n" \
"v_mfma_f32_16x16x32_bf16 v[140:143], a[220:223], v[68:71], v[140:143]\n" \
"s_lshl_b32 s70, s61, 16\n" \
"ds_read_b128 v[8:11], v232 offset:28672\n" \
"ds_read_b128 v[12:15], v232 offset:30720\n" \
"v_exp_f32 v111, v111\n" \
"v_smfmac_f32_16x16x64_bf16 a[52:55], v[244:247], v[80:87], v243\n" \
"s_waitcnt lgkmcnt(4)\n" \
"s_add_u32 m0, s89, 135552\n" \
"v_cvt_pk_bf16_f32 v102, v108, v109\n" \
"v_mfma_f32_16x16x32_bf16 v[144:147], a[224:227], v[64:67], v[144:147]\n" \
"buffer_load_dwordx4 v192, s[44:47], s75 offen offset:640 lds\n" \
"v_mfma_f32_16x16x32_bf16 v[144:147], a[228:231], v[68:71], v[144:147]\n" \
"s_add_u32 s73, s73, s70\n" \
"ds_read_b128 v[200:203], v232 offset:24832\n" \
"ds_read_b128 v[204:207], v232 offset:26880\n" \
"v_mfma_f32_16x16x32_bf16 v[176:179], a[224:227], v[80:83], v[176:179]\n" \
"s_add_u32 m0, s89, 136448\n" \
"v_exp_f32 v120, v120\n" \
"v_mfma_f32_16x16x32_bf16 v[176:179], a[228:231], v[84:87], v[176:179]\n" \
"buffer_load_dwordx4 v192, s[44:47], s75 offen offset:768 lds\n" \
"v_mfma_f32_16x16x32_bf16 v[180:183], a[232:235], v[80:83], v[180:183]\n" \
"s_add_u32 s73, s73, s71\n" \
"ds_read_b128 a[240:243], v232 offset:28928\n" \
"ds_read_b128 a[244:247], v232 offset:30976\n" \
"v_mfma_f32_16x16x32_bf16 v[180:183], a[236:239], v[84:87], v[180:183]\n" \
"s_add_u32 m0, s89, 137344\n" \
"v_exp_f32 v121, v121\n" \
"v_exp_f32 v122, v122\n" \
"v_mfma_f32_16x16x32_bf16 v[148:151], a[232:235], v[64:67], v[148:151]\n" \
"buffer_load_dwordx4 v192, s[44:47], s75 offen offset:896 lds\n" \
"v_mfma_f32_16x16x32_bf16 v[148:151], a[236:239], v[68:71], v[148:151]\n" \
"s_and_b32 s70, s61, 1\n" \
"v_cvt_pk_bf16_f32 v103, v110, v111\n" \
"v_cvt_pk_bf16_f32 v116, v120, v121\n" \
"v_mfma_f32_16x16x32_bf16 v[152:155], a[32:35], v[64:67], v[152:155]\n" \
"s_lshl_b32 s70, s70, 11\n" \
"v_add_u32 v233, 0x10000, v235\n" \
"s_lshl_b32 s72, s61, 13\n" \
"v_mfma_f32_16x16x32_bf16 v[152:155], a[36:39], v[68:71], v[152:155]\n" \
"s_add_u32 s93, s93, s70\n" \
"v_add_u32 v232, s72, v235\n" \
"v_exp_f32 v123, v123\n" \
"v_mfma_f32_16x16x32_bf16 v[184:187], a[32:35], v[80:83], v[184:187]\n" \
"s_add_u32 s93, s93, 52740096\n" \
"s_cmp_eq_u32 s94, 0\n" \
"s_cselect_b32 s92, -1, 0\n" \
"s_mul_i32 s70, s94, 278528\n" \
"s_add_u32 s93, s93, s70\n" \
"s_lshr_b32 s70, s61, 1\n" \
"s_lshl_b32 s70, s70, 8\n" \
"s_add_u32 s93, s93, s70\n" \
"v_mfma_f32_16x16x32_bf16 v[184:187], a[36:39], v[84:87], v[184:187]\n" \
"v_exp_f32 v124, v124\n" \
"v_mfma_f32_16x16x32_bf16 v[188:191], a[40:43], v[80:83], v[188:191]\n" \
"v_exp_f32 v125, v125\n" \
"v_mfma_f32_16x16x32_bf16 v[188:191], a[44:47], v[84:87], v[188:191]\n" \
"v_exp_f32 v126, v126\n" \
"v_cvt_pk_bf16_f32 v117, v122, v123\n" \
"v_mfma_f32_16x16x32_bf16 v[156:159], a[40:43], v[64:67], v[156:159]\n" \
"s_mul_i32 s76, s80, 393216\n" \
"v_exp_f32 v127, v127\n" \
"v_mfma_f32_16x16x32_bf16 v[156:159], a[44:47], v[68:71], v[156:159]\n" \
"s_add_u32 s76, s76, s78\n" \
"s_cmp_eq_u32 s63, 7\n" \
"s_waitcnt vmcnt(9) lgkmcnt(0)\n" \
"s_barrier\n" \
"s_cbranch_scc1 Lep_%=\n" \
"Lepg_%=:\n" \
"v_mfma_f32_16x16x32_bf16 v[16:19], a[0:3], a[176:179], v[208:211]\n" \
"s_add_u32 m0, s91, 106496\n" \
"ds_read_b128 v[64:67], v233 offset:16384\n" \
"ds_read_b128 v[68:71], v233 offset:24576\n" \
"v_mfma_f32_16x16x32_bf16 v[16:19], a[4:7], a[180:183], v[16:19]\n" \
"buffer_load_dwordx4 v197, s[48:51], s76 offen offset:0 lds\n" \
"v_mfma_f32_16x16x32_bf16 v[16:19], a[8:11], a[184:187], v[16:19]\n" \
"ds_read_b128 v[72:75], v233 offset:17408\n" \
"ds_read_b128 v[76:79], v233 offset:25600\n" \
"v_mfma_f32_16x16x32_bf16 v[16:19], a[12:15], a[188:191], v[16:19]\n" \
"ds_read_b128 v[80:83], v233 offset:18432\n" \
"ds_read_b128 v[84:87], v233 offset:26624\n" \
"v_cvt_pk_bf16_f32 v128, v128, v129\n" \
"v_cvt_pk_bf16_f32 v129, v130, v131\n" \
"v_cvt_pk_bf16_f32 v130, v132, v133\n" \
"v_cvt_pk_bf16_f32 v118, v124, v125\n" \
"v_mfma_f32_16x16x32_bf16 v[20:23], a[28:31], a[188:191], v[212:215]\n" \
"s_add_u32 s77, s76, 24576\n" \
"ds_read_b128 v[88:91], v233 offset:19456\n" \
"ds_read_b128 v[92:95], v233 offset:27648\n" \
"v_mfma_f32_16x16x32_bf16 v[20:23], a[24:27], a[184:187], v[20:23]\n" \
"buffer_load_dwordx4 v198, s[48:51], s76 offen offset:1024 lds\n" \
"v_cvt_pk_bf16_f32 v131, v134, v135\n" \
"v_cvt_pk_bf16_f32 v119, v126, v127\n" \
"v_exp_f32 v32, v32\n" \
"v_xor_b32 v248, 8192, v232\n" \
"v_mfma_f32_16x16x32_bf16 v[20:23], a[20:23], a[180:183], v[20:23]\n" \
"ds_write_b128 v232, v[128:131] offset:0\n" \
"v_exp_f32 v33, v33\n" \
"v_mfma_f32_16x16x32_bf16 v[20:23], a[16:19], a[176:179], v[20:23]\n" \
"ds_read_b128 a[248:251], v233 offset:7168\n" \
"ds_read_b128 a[252:255], v233 offset:15360\n" \
"v_exp_f32 v34, v34\n" \
"v_mfma_f32_16x16x32_bf16 v[24:27], v[0:3], a[176:179], v[216:219]\n" \
"s_xor_b32 s80, s80, 0x3\n" \
"v_cvt_pk_bf16_f32 v132, v136, v137\n" \
"v_cvt_pk_bf16_f32 v133, v138, v139\n" \
"v_mfma_f32_16x16x32_bf16 v[24:27], v[4:7], a[180:183], v[24:27]\n" \
"buffer_load_dwordx4 v199, s[48:51], s76 offen offset:2048 lds\n" \
"v_exp_f32 v35, v35\n" \
"v_lshrrev_b32 v249, 1, v234\n" \
"v_mfma_f32_16x16x32_bf16 v[24:27], v[8:11], a[184:187], v[24:27]\n" \
"s_lshl_b32 s72, s61, 9\n" \
"v_cvt_pk_bf16_f32 v134, v140, v141\n" \
"v_cvt_pk_bf16_f32 v135, v142, v143\n" \
"v_mfma_f32_16x16x32_bf16 v[24:27], v[12:15], a[188:191], v[24:27]\n" \
"s_add_u32 s72, s72, 57344\n" \
"s_add_u32 s72, s72, %3\n" \
"v_cvt_pk_bf16_f32 v136, v144, v145\n" \
"v_cvt_pk_bf16_f32 v137, v146, v147\n" \
"v_exp_f32 v36, v36\n" \
"v_cvt_pk_bf16_f32 v32, v32, v33\n" \
"v_mfma_f32_16x16x32_bf16 v[28:31], a[244:247], a[188:191], v[220:223]\n" \
"s_add_u32 m0, m0, 3072\n" \
"v_cvt_pk_bf16_f32 v138, v148, v149\n" \
"v_cvt_pk_bf16_f32 v139, v150, v151\n" \
"v_mfma_f32_16x16x32_bf16 v[28:31], a[240:243], a[184:187], v[28:31]\n" \
"buffer_load_dwordx4 v197, s[48:51], s77 offen offset:0 lds\n" \
"v_exp_f32 v37, v37\n" \
"v_mfma_f32_16x16x32_bf16 v[28:31], v[204:207], a[180:183], v[28:31]\n" \
"ds_write_b128 v232, v[132:135] offset:1024\n" \
"v_exp_f32 v38, v38\n" \
"v_cvt_pk_bf16_f32 v33, v34, v35\n" \
"v_add_u32 v249, s72, v249\n" \
"v_mfma_f32_16x16x32_bf16 v[28:31], v[200:203], a[176:179], v[28:31]\n" \
"v_cvt_pk_bf16_f32 v140, v152, v153\n" \
"v_cvt_pk_bf16_f32 v141, v154, v155\n" \
"v_exp_f32 v39, v39\n" \
"v_mfma_f32_16x16x32_bf16 a[64:67], a[192:195], v[96:99], a[64:67]\n" \
"v_cvt_pk_bf16_f32 v142, v156, v157\n" \
"v_cvt_pk_bf16_f32 v143, v158, v159\n" \
"v_mfma_f32_16x16x32_bf16 a[64:67], a[196:199], v[100:103], a[64:67]\n" \
"buffer_load_dwordx4 v198, s[48:51], s77 offen offset:1024 lds\n" \
"v_exp_f32 v48, v48\n" \
"v_cvt_pk_bf16_f32 v34, v36, v37\n" \
"v_mfma_f32_16x16x32_bf16 a[96:99], a[192:195], v[112:115], a[96:99]\n" \
"v_cvt_pk_bf16_f32 v144, v160, v161\n" \
"v_cvt_pk_bf16_f32 v145, v162, v163\n" \
"v_mfma_f32_16x16x32_bf16 a[96:99], a[196:199], v[116:119], a[96:99]\n" \
"ds_write_b128 v232, v[136:139] offset:2048\n" \
"v_exp_f32 v49, v49\n" \
"v_exp_f32 v50, v50\n" \
"v_cvt_pk_bf16_f32 v35, v38, v39\n" \
"v_accvgpr_read_b32 v250, a48\n" \
"v_mfma_f32_16x16x32_bf16 a[100:103], a[200:203], v[112:115], a[100:103]\n" \
"ds_read_b128 a[192:195], v233 offset:0\n" \
"ds_read_b128 a[196:199], v233 offset:8192\n" \
"v_mfma_f32_16x16x32_bf16 a[100:103], a[204:207], v[116:119], a[100:103]\n" \
"buffer_load_dwordx4 v199, s[48:51], s77 offen offset:2048 lds\n" \
"v_exp_f32 v51, v51\n" \
"v_exp_f32 v52, v52\n" \
"v_mfma_f32_16x16x32_bf16 a[68:71], a[200:203], v[96:99], a[68:71]\n" \
"v_cvt_pk_bf16_f32 v146, v164, v165\n" \
"v_cvt_pk_bf16_f32 v147, v166, v167\n" \
"v_mfma_f32_16x16x32_bf16 a[68:71], a[204:207], v[100:103], a[68:71]\n" \
"ds_read_b128 a[200:203], v233 offset:1024\n" \
"ds_read_b128 a[204:207], v233 offset:9216\n" \
"v_cvt_pk_bf16_f32 v48, v48, v49\n" \
"v_exp_f32 v53, v53\n" \
"v_smfmac_f32_16x16x64_bf16 v[224:227], v[244:247], v[96:103], v243\n" \
"s_add_u32 m0, s79, 40960\n" \
"v_cvt_pk_bf16_f32 v148, v168, v169\n" \
"v_exp_f32 v54, v54\n" \
"v_cvt_pk_bf16_f32 v49, v50, v51\n" \
"v_accvgpr_read_b32 v240, a49\n" \
"v_mfma_f32_16x16x32_bf16 a[72:75], a[208:211], v[96:99], a[72:75]\n" \
"buffer_load_dwordx4 v192, s[44:47], s73 offen offset:0 lds\n" \
"v_mfma_f32_16x16x32_bf16 a[72:75], a[212:215], v[100:103], a[72:75]\n" \
"ds_write_b128 v232, v[140:143] offset:3072\n" \
"v_exp_f32 v55, v55\n" \
"v_exp_f32 v40, v40\n" \
"v_mfma_f32_16x16x32_bf16 a[104:107], a[208:211], v[112:115], a[104:107]\n" \
"v_cvt_pk_bf16_f32 v149, v170, v171\n" \
"v_cvt_pk_bf16_f32 v150, v172, v173\n" \
"v_mfma_f32_16x16x32_bf16 a[104:107], a[212:215], v[116:119], a[104:107]\n" \
"ds_read_b128 a[208:211], v233 offset:2048\n" \
"ds_read_b128 a[212:215], v233 offset:10240\n" \
"v_cvt_pk_bf16_f32 v50, v52, v53\n" \
"v_exp_f32 v41, v41\n" \
"v_mfma_f32_16x16x32_bf16 a[108:111], a[216:219], v[112:115], a[108:111]\n" \
"buffer_load_dwordx4 v193, s[44:47], s73 offen offset:1024 lds\n" \
"v_mfma_f32_16x16x32_bf16 a[108:111], a[220:223], v[116:119], a[108:111]\n" \
"v_cvt_pk_bf16_f32 v151, v174, v175\n" \
"v_cvt_pk_bf16_f32 v152, v176, v177\n" \
"v_exp_f32 v42, v42\n" \
"v_cvt_pk_bf16_f32 v51, v54, v55\n" \
"v_exp_f32 v43, v43\n" \
"v_add_f32 v250, v250, v240\n" \
"v_mfma_f32_16x16x32_bf16 a[76:79], a[216:219], v[96:99], a[76:79]\n" \
"v_cvt_pk_bf16_f32 v153, v178, v179\n" \
"v_cvt_pk_bf16_f32 v154, v180, v181\n" \
"v_mfma_f32_16x16x32_bf16 a[76:79], a[220:223], v[100:103], a[76:79]\n" \
"ds_read_b128 a[216:219], v233 offset:3072\n" \
"ds_read_b128 a[220:223], v233 offset:11264\n" \
"v_exp_f32 v44, v44\n" \
"v_cvt_pk_bf16_f32 v36, v40, v41\n" \
"v_smfmac_f32_16x16x64_bf16 v[228:231], v[244:247], v[112:119], v243\n" \
"buffer_load_dwordx4 v192, s[44:47], s73 offen offset:2048 lds\n" \
"v_exp_f32 v45, v45\n" \
"v_mfma_f32_16x16x32_bf16 a[80:83], a[224:227], v[96:99], a[80:83]\n" \
"ds_write_b128 v232, v[144:147] offset:4096\n" \
"v_exp_f32 v46, v46\n" \
"v_cvt_pk_bf16_f32 v37, v42, v43\n" \
"v_accvgpr_read_b32 v251, a52\n" \
"v_mfma_f32_16x16x32_bf16 a[80:83], a[228:231], v[100:103], a[80:83]\n" \
"v_cvt_pk_bf16_f32 v155, v182, v183\n" \
"v_cvt_pk_bf16_f32 v156, v184, v185\n" \
"v_exp_f32 v47, v47\n" \
"v_mfma_f32_16x16x32_bf16 a[112:115], a[224:227], v[112:115], a[112:115]\n" \
"v_cvt_pk_bf16_f32 v157, v186, v187\n" \
"v_cvt_pk_bf16_f32 v158, v188, v189\n" \
"v_mfma_f32_16x16x32_bf16 a[112:115], a[228:231], v[116:119], a[112:115]\n" \
"buffer_load_dwordx4 v193, s[44:47], s73 offen offset:3072 lds\n" \
"v_exp_f32 v56, v56\n" \
"v_cvt_pk_bf16_f32 v38, v44, v45\n" \
"v_mfma_f32_16x16x32_bf16 a[116:119], a[232:235], v[112:115], a[116:119]\n" \
"ds_read_b128 a[224:227], v233 offset:4096\n" \
"ds_read_b128 a[228:231], v233 offset:12288\n" \
"v_mfma_f32_16x16x32_bf16 a[116:119], a[236:239], v[116:119], a[116:119]\n" \
"v_cvt_pk_bf16_f32 v159, v190, v191\n" \
"v_cvt_pk_bf16_f32 v39, v46, v47\n" \
"v_exp_f32 v57, v57\n" \
"v_exp_f32 v58, v58\n" \
"v_accvgpr_read_b32 v240, a53\n" \
"v_mfma_f32_16x16x32_bf16 a[84:87], a[232:235], v[96:99], a[84:87]\n" \
"s_xor_b32 s73, s73, 0x180\n" \
"v_exp_f32 v59, v59\n" \
"v_mfma_f32_16x16x32_bf16 a[84:87], a[236:239], v[100:103], a[84:87]\n" \
"ds_write_b128 v232, v[148:151] offset:5120\n" \
"v_exp_f32 v60, v60\n" \
"v_cvt_pk_bf16_f32 v52, v56, v57\n" \
"v_exp_f32 v61, v61\n" \
"v_mfma_f32_16x16x32_bf16 a[88:91], a[32:35], v[96:99], a[88:91]\n" \
"ds_read_b128 a[232:235], v233 offset:5120\n" \
"ds_read_b128 a[236:239], v233 offset:13312\n" \
"v_mfma_f32_16x16x32_bf16 a[88:91], a[36:39], v[100:103], a[88:91]\n" \
"ds_read_b128 a[240:243], v233 offset:6144\n" \
"ds_read_b128 a[244:247], v233 offset:14336\n" \
"v_exp_f32 v62, v62\n" \
"v_cvt_pk_bf16_f32 v53, v58, v59\n" \
"v_mfma_f32_16x16x32_bf16 a[120:123], a[32:35], v[112:115], a[120:123]\n" \
"ds_read_b128 v[0:3], v233 offset:20480\n" \
"ds_read_b128 v[4:7], v233 offset:28672\n" \
"v_mfma_f32_16x16x32_bf16 a[120:123], a[36:39], v[116:119], a[120:123]\n" \
"ds_read_b128 v[8:11], v233 offset:21504\n" \
"ds_read_b128 v[12:15], v233 offset:29696\n" \
"v_exp_f32 v63, v63\n" \
"v_cvt_pk_bf16_f32 v54, v60, v61\n" \
"v_add_f32 v251, v251, v240\n" \
"v_mfma_f32_16x16x32_bf16 a[124:127], a[40:43], v[112:115], a[124:127]\n" \
"v_cvt_pk_bf16_f32 v55, v62, v63\n" \
"v_mfma_f32_16x16x32_bf16 a[124:127], a[44:47], v[116:119], a[124:127]\n" \
"ds_write_b128 v232, v[152:155] offset:6144\n" \
"ds_write_b64 v249, v[250:251]\n" \
"v_mfma_f32_16x16x32_bf16 a[92:95], a[40:43], v[96:99], a[92:95]\n" \
"v_exp_f32 v16, v16\n" \
"v_mfma_f32_16x16x32_bf16 a[92:95], a[44:47], v[100:103], a[92:95]\n" \
"s_waitcnt lgkmcnt(4)\n" \
"ds_read_b128 v[96:99], v233 offset:22528\n" \
"ds_read_b128 v[100:103], v233 offset:30720\n" \
"v_exp_f32 v17, v17\n" \
"v_xor_b32 v249, 0x200, v249\n" \
"v_mfma_f32_16x16x32_bf16 a[64:67], a[192:195], v[32:35], a[64:67]\n" \
"ds_read_b128 v[104:107], v233 offset:23552\n" \
"ds_read_b128 v[108:111], v233 offset:31744\n" \
"v_mfma_f32_16x16x32_bf16 a[64:67], a[196:199], v[36:39], a[64:67]\n" \
"v_exp_f32 v18, v18\n" \
"v_exp_f32 v19, v19\n" \
"v_accvgpr_read_b32 v200, a59\n" \
"v_accvgpr_read_b32 v202, a60\n" \
"v_mfma_f32_16x16x32_bf16 a[96:99], a[192:195], v[48:51], a[96:99]\n" \
"v_exp_f32 v20, v20\n" \
"v_mfma_f32_16x16x32_bf16 a[96:99], a[196:199], v[52:55], a[96:99]\n" \
"v_cvt_pk_bf16_f32 v16, v16, v17\n" \
"v_cvt_pk_bf16_f32 v17, v18, v19\n" \
"v_accvgpr_read_b32 v204, a61\n" \
"v_xor_b32 v201, 64, v200\n" \
"v_mfma_f32_16x16x32_bf16 a[100:103], a[200:203], v[48:51], a[100:103]\n" \
"v_exp_f32 v21, v21\n" \
"v_mfma_f32_16x16x32_bf16 a[100:103], a[204:207], v[52:55], a[100:103]\n" \
"ds_write_b128 v232, v[156:159] offset:7168\n" \
"v_exp_f32 v22, v22\n" \
"v_xor_b32 v203, 64, v202\n" \
"v_xor_b32 v222, 32, v204\n" \
"v_mfma_f32_16x16x32_bf16 a[68:71], a[200:203], v[32:35], a[68:71]\n" \
"v_exp_f32 v23, v23\n" \
"v_mfma_f32_16x16x32_bf16 a[68:71], a[204:207], v[36:39], a[68:71]\n" \
"s_mul_i32 s76, s80, 393216\n" \
"s_add_u32 s76, s76, s78\n" \
"v_cvt_pk_bf16_f32 v18, v20, v21\n" \
"v_exp_f32 v24, v24\n" \
"v_add_u32 v209, 81920, v204\n" \
"v_add_u32 v210, 81920, v222\n" \
"v_smfmac_f32_16x16x64_bf16 v[224:227], v[244:247], v[32:39], v243\n" \
"s_waitcnt vmcnt(0) lgkmcnt(0)\n" \
"s_barrier\n" \
"v_mfma_f32_16x16x32_bf16 a[72:75], a[208:211], v[32:35], a[72:75]\n" \
"s_add_u32 m0, s91, 81920\n" \
"ds_read_b64_tr_b16 a[32:33], v209 offset:25600\n" \
"ds_read_b64_tr_b16 a[34:35], v209 offset:28672\n" \
"v_mfma_f32_16x16x32_bf16 a[72:75], a[212:215], v[36:39], a[72:75]\n" \
"buffer_load_dwordx4 v197, s[48:51], s76 offen offset:0 lds\n" \
"v_exp_f32 v25, v25\n" \
"v_mfma_f32_16x16x32_bf16 a[104:107], a[208:211], v[48:51], a[104:107]\n" \
"s_and_b32 s15, %1, 7\n" \
"ds_read_b64_tr_b16 a[36:37], v209 offset:37888\n" \
"ds_read_b64_tr_b16 a[38:39], v209 offset:40960\n" \
"v_mfma_f32_16x16x32_bf16 a[104:107], a[212:215], v[52:55], a[104:107]\n" \
"s_add_u32 s77, s76, 24576\n" \
"ds_read_b64_tr_b16 a[40:41], v209 offset:24576\n" \
"ds_read_b64_tr_b16 a[42:43], v209 offset:27648\n" \
"v_mfma_f32_16x16x32_bf16 a[108:111], a[216:219], v[48:51], a[108:111]\n" \
"buffer_load_dwordx4 v198, s[48:51], s76 offen offset:1024 lds\n" \
"v_mfma_f32_16x16x32_bf16 a[108:111], a[220:223], v[52:55], a[108:111]\n" \
"s_lshl3_add_u32 s14, s14, s15\n" \
"ds_read_b64_tr_b16 a[44:45], v209 offset:36864\n" \
"ds_read_b64_tr_b16 a[46:47], v209 offset:39936\n" \
"v_exp_f32 v26, v26\n" \
"v_cvt_pk_bf16_f32 v19, v22, v23\n" \
"v_mfma_f32_16x16x32_bf16 a[76:79], a[216:219], v[32:35], a[76:79]\n" \
"s_xor_b32 s80, s80, 0x8\n" \
"ds_read_b64_tr_b16 a[48:49], v210 offset:24576\n" \
"ds_read_b64_tr_b16 a[50:51], v210 offset:27648\n" \
"v_mfma_f32_16x16x32_bf16 a[76:79], a[220:223], v[36:39], a[76:79]\n" \
"buffer_load_dwordx4 v199, s[48:51], s76 offen offset:2048 lds\n" \
"v_smfmac_f32_16x16x64_bf16 v[228:231], v[244:247], v[48:55], v243\n" \
"ds_read_b64_tr_b16 a[52:53], v210 offset:36864\n" \
"ds_read_b64_tr_b16 a[54:55], v210 offset:39936\n" \
"v_exp_f32 v27, v27\n" \
"v_mfma_f32_16x16x32_bf16 a[80:83], a[224:227], v[32:35], a[80:83]\n" \
"s_add_u32 m0, m0, 3072\n" \
"ds_read_b128 a[0:3], v202 offset:0\n" \
"ds_read_b128 a[4:7], v203 offset:0\n" \
"v_mfma_f32_16x16x32_bf16 a[80:83], a[228:231], v[36:39], a[80:83]\n" \
"buffer_load_dwordx4 v197, s[48:51], s77 offen offset:0 lds\n" \
"v_mfma_f32_16x16x32_bf16 a[112:115], a[224:227], v[48:51], a[112:115]\n" \
"ds_read_b128 a[8:11], v200 offset:40960\n" \
"ds_read_b128 a[12:15], v201 offset:40960\n" \
"v_mfma_f32_16x16x32_bf16 a[112:115], a[228:231], v[52:55], a[112:115]\n" \
"ds_read_b128 a[16:19], v200 offset:45056\n" \
"ds_read_b128 a[20:23], v201 offset:45056\n" \
"v_exp_f32 v28, v28\n" \
"v_cvt_pk_bf16_f32 v20, v24, v25\n" \
"v_mfma_f32_16x16x32_bf16 a[116:119], a[232:235], v[48:51], a[116:119]\n" \
"buffer_load_dwordx4 v198, s[48:51], s77 offen offset:1024 lds\n" \
"v_mfma_f32_16x16x32_bf16 a[116:119], a[236:239], v[52:55], a[116:119]\n" \
"ds_read_b128 a[24:27], v200 offset:49152\n" \
"ds_read_b128 a[28:31], v201 offset:49152\n" \
"v_mfma_f32_16x16x32_bf16 a[84:87], a[232:235], v[32:35], a[84:87]\n" \
"ds_read_b64 v[250:251], v249\n" \
"ds_read_b128 v[128:131], v248 offset:0\n" \
"v_mfma_f32_16x16x32_bf16 a[84:87], a[236:239], v[36:39], a[84:87]\n" \
"buffer_load_dwordx4 v199, s[48:51], s77 offen offset:2048 lds\n" \
"v_exp_f32 v29, v29\n" \
"v_mfma_f32_16x16x32_bf16 a[88:91], a[240:243], v[32:35], a[88:91]\n" \
"ds_read_b128 v[132:135], v248 offset:1024\n" \
"ds_read_b128 v[136:139], v248 offset:2048\n" \
"v_mfma_f32_16x16x32_bf16 a[88:91], a[244:247], v[36:39], a[88:91]\n" \
"s_add_u32 m0, s79, 65536\n" \
"ds_read_b128 v[140:143], v248 offset:3072\n" \
"ds_read_b128 v[144:147], v248 offset:4096\n" \
"v_exp_f32 v30, v30\n" \
"v_mfma_f32_16x16x32_bf16 a[120:123], a[240:243], v[48:51], a[120:123]\n" \
"buffer_load_dwordx4 v192, s[44:47], s73 offen offset:0 lds\n" \
"v_mfma_f32_16x16x32_bf16 a[120:123], a[244:247], v[52:55], a[120:123]\n" \
"ds_read_b128 v[148:151], v248 offset:5120\n" \
"ds_read_b128 v[152:155], v248 offset:6144\n" \
"v_cvt_pk_bf16_f32 v21, v26, v27\n" \
"v_mfma_f32_16x16x32_bf16 a[124:127], a[248:251], v[48:51], a[124:127]\n" \
"ds_read_b128 v[156:159], v248 offset:7168\n" \
"v_cvt_pk_bf16_f32 v22, v28, v29\n" \
"v_mfma_f32_16x16x32_bf16 a[124:127], a[252:255], v[52:55], a[124:127]\n" \
"buffer_load_dwordx4 v193, s[44:47], s73 offen offset:1024 lds\n" \
"v_exp_f32 v31, v31\n" \
"v_mfma_f32_16x16x32_bf16 a[92:95], a[248:251], v[32:35], a[92:95]\n" \
"v_cvt_pk_bf16_f32 v23, v30, v31\n" \
"v_mov_b32 v218, s60\n" \
"v_mfma_f32_16x16x32_bf16 a[92:95], a[252:255], v[36:39], a[92:95]\n" \
"v_mov_b32 v219, s14\n" \
"v_lshlrev_b32 v220, 28, s61\n" \
"v_mfma_f32_16x16x32_bf16 a[96:99], v[64:67], v[16:19], a[96:99]\n" \
"buffer_load_dwordx4 v192, s[44:47], s73 offen offset:2048 lds\n" \
"v_mfma_f32_16x16x32_bf16 a[96:99], v[68:71], v[20:23], a[96:99]\n" \
"s_lshl3_add_u32 s15, s15, 65593856\n" \
"v_mov_b32 v170, 0x3f80\n" \
"v_mov_b32 v160, 0\n" \
"v_mfma_f32_16x16x32_bf16 a[100:103], v[72:75], v[16:19], a[100:103]\n" \
"s_lshr_b32 s14, %1, 3\n" \
"v_mov_b32 v161, 0\n" \
"v_mov_b32 v164, 0\n" \
"v_mfma_f32_16x16x32_bf16 a[100:103], v[76:79], v[20:23], a[100:103]\n" \
"buffer_load_dwordx4 v193, s[44:47], s73 offen offset:3072 lds\n" \
"v_smfmac_f32_16x16x64_bf16 v[228:231], v[244:247], v[16:23], v243\n" \
"s_cmp_eq_u32 s14, 3\n" \
"s_cselect_b32 s15, s15, 0x20000000\n" \
"v_mov_b32 v165, 0\n" \
"v_mfma_f32_16x16x32_bf16 a[104:107], v[80:83], v[16:19], a[104:107]\n" \
"buffer_store_dwordx2 v[218:219], v220, s[56:59], s15 offen sc1\n" \
"v_mfma_f32_16x16x32_bf16 a[104:107], v[84:87], v[20:23], a[104:107]\n" \
"s_xor_b32 s73, s73, 0x400\n" \
"v_lshrrev_b32 v168, 8, v234\n" \
"v_bfe_u32 v169, v234, 6, 2\n" \
"v_mfma_f32_16x16x32_bf16 a[108:111], v[88:91], v[16:19], a[108:111]\n" \
"v_cmp_eq_u32 s[16:17], v168, v169\n" \
"v_and_b32 v168, 16, v234\n" \
"v_mfma_f32_16x16x32_bf16 a[108:111], v[92:95], v[20:23], a[108:111]\n" \
"v_lshlrev_b32 v168, v168, v170\n" \
"v_cndmask_b32 v168, 0, v168, s[16:17]\n" \
"v_mfma_f32_16x16x32_bf16 a[112:115], v[0:3], v[16:19], a[112:115]\n" \
"Lpg7367_%=:\n" \
"s_cbranch_execz Lcb0_%=\n" \
"v_bfe_i32 v169, v234, 5, 1\n" \
"v_and_b32 v163, v168, v169\n" \
"v_mfma_f32_16x16x32_bf16 a[112:115], v[4:7], v[20:23], a[112:115]\n" \
"v_xor_b32 v162, v168, v163\n" \
"v_mfma_f32_16x16x32_bf16 a[116:119], v[8:11], v[16:19], a[116:119]\n" \
"v_mfma_f32_16x16x32_bf16 a[116:119], v[12:15], v[20:23], a[116:119]\n" \
"v_mfma_f32_16x16x32_bf16 a[120:123], v[96:99], v[16:19], a[120:123]\n" \
"v_mfma_f32_16x16x32_bf16 a[120:123], v[100:103], v[20:23], a[120:123]\n" \
"v_mfma_f32_16x16x32_bf16 a[124:127], v[104:107], v[16:19], a[124:127]\n" \
"v_mfma_f32_16x16x32_bf16 a[124:127], v[108:111], v[20:23], a[124:127]\n" \
"s_waitcnt lgkmcnt(0)\n" \
"s_barrier\n" \
"v_mfma_f32_16x16x32_bf16 v[32:35], a[32:35], a[0:3], 0\n" \
"s_add_u32 m0, s79, 0\n" \
"ds_read_b128 a[192:195], v200 offset:53248\n" \
"v_mfma_f32_16x16x32_bf16 v[32:35], a[36:39], a[4:7], v[32:35]\n" \
"buffer_load_dwordx4 v192, s[44:47], s73 offen offset:0 lds\n" \
"v_mfma_f32_16x16x32_bf16 v[40:43], a[32:35], a[8:11], 0\n" \
"ds_read_b128 a[196:199], v201 offset:53248\n" \
"v_add_u32 v205, 0x10000, v200\n" \
"v_mfma_f32_16x16x32_bf16 v[40:43], a[36:39], a[12:15], v[40:43]\n" \
"buffer_load_dwordx4 v193, s[44:47], s73 offen offset:1024 lds\n" \
"v_mfma_f32_16x16x32_bf16 a[64:67], v[162:165], v[128:131], a[64:67]\n" \
"v_mfma_f32_16x16x32_bf16 a[68:71], v[160:163], v[128:131], a[68:71]\n" \
"v_mfma_f32_16x16x32_bf16 a[72:75], v[162:165], v[132:135], a[72:75]\n" \
"v_mfma_f32_16x16x32_bf16 a[76:79], v[160:163], v[132:135], a[76:79]\n" \
"v_add_f32 v248, v224, v225\n" \
"v_add_f32 v249, v228, v229\n" \
"v_mfma_f32_16x16x32_bf16 v[0:3], a[40:43], a[8:11], 0\n" \
"ds_read_b64_tr_b16 a[200:201], v210 offset:25600\n" \
"ds_read_b64_tr_b16 a[202:203], v210 offset:28672\n" \
"v_mfma_f32_16x16x32_bf16 v[0:3], a[44:47], a[12:15], v[0:3]\n" \
"buffer_load_dwordx4 v192, s[44:47], s73 offen offset:2048 lds\n" \
"v_mfma_f32_16x16x32_bf16 v[4:7], a[48:51], a[8:11], 0\n" \
"ds_read_b64_tr_b16 a[204:205], v210 offset:37888\n" \
"ds_read_b64_tr_b16 a[206:207], v210 offset:40960\n" \
"v_mfma_f32_16x16x32_bf16 v[4:7], a[52:55], a[12:15], v[4:7]\n" \
"buffer_load_dwordx4 v193, s[44:47], s73 offen offset:3072 lds\n" \
"v_mfma_f32_16x16x32_bf16 v[12:15], a[48:51], a[16:19], 0\n" \
"s_xor_b32 s73, s73, 0x500\n" \
"s_mul_i32 s76, s80, 393216\n" \
"s_add_u32 s76, s76, s78\n" \
"s_add_u32 m0, s91, 16384\n" \
"ds_read_b64_tr_b16 a[208:209], v209 offset:26624\n" \
"ds_read_b64_tr_b16 a[210:211], v209 offset:29696\n" \
"v_add_f32 v248, v248, v250\n" \
"v_mfma_f32_16x16x32_bf16 v[12:15], a[52:55], a[20:23], v[12:15]\n" \
"buffer_load_dwordx4 v197, s[48:51], s76 offen offset:0 lds\n" \
"v_mfma_f32_16x16x32_bf16 v[8:11], a[40:43], a[16:19], 0\n" \
"s_add_u32 s77, s76, 24576\n" \
"ds_read_b64_tr_b16 a[212:213], v209 offset:38912\n" \
"ds_read_b64_tr_b16 a[214:215], v209 offset:41984\n" \
"v_mfma_f32_16x16x32_bf16 v[8:11], a[44:47], a[20:23], v[8:11]\n" \
"buffer_load_dwordx4 v198, s[48:51], s76 offen offset:1024 lds\n" \
"v_mfma_f32_16x16x32_bf16 v[48:51], a[32:35], a[16:19], 0\n" \
"s_xor_b32 s80, s80, 0xa\n" \
"ds_read_b64_tr_b16 a[216:217], v210 offset:26624\n" \
"v_add_u32 v206, 0x10000, v201\n" \
"v_mfma_f32_16x16x32_bf16 v[48:51], a[36:39], a[20:23], v[48:51]\n" \
"buffer_load_dwordx4 v199, s[48:51], s76 offen offset:2048 lds\n" \
"v_mfma_f32_16x16x32_bf16 v[56:59], a[32:35], a[24:27], 0\n" \
"s_add_u32 m0, m0, 3072\n" \
"ds_read_b64_tr_b16 a[218:219], v210 offset:29696\n" \
"v_add_u32 v207, 0x20000, v200\n" \
"v_mfma_f32_16x16x32_bf16 v[56:59], a[36:39], a[28:31], v[56:59]\n" \
"buffer_load_dwordx4 v197, s[48:51], s77 offen offset:0 lds\n" \
"v_add_f32 v249, v249, v251\n" \
"v_mfma_f32_16x16x32_bf16 v[16:19], a[40:43], a[24:27], 0\n" \
"ds_read_b64_tr_b16 a[220:221], v210 offset:38912\n" \
"v_add_u32 v208, 0x20000, v201\n" \
"v_mfma_f32_16x16x32_bf16 v[16:19], a[44:47], a[28:31], v[16:19]\n" \
"buffer_load_dwordx4 v198, s[48:51], s77 offen offset:1024 lds\n" \
"v_mfma_f32_16x16x32_bf16 v[20:23], a[48:51], a[24:27], 0\n" \
"s_waitcnt lgkmcnt(2)\n" \
"ds_read_b64_tr_b16 a[222:223], v210 offset:41984\n" \
"v_mfma_f32_16x16x32_bf16 v[20:23], a[52:55], a[28:31], v[20:23]\n" \
"buffer_load_dwordx4 v199, s[48:51], s77 offen offset:2048 lds\n" \
"v_mfma_f32_16x16x32_bf16 v[28:31], a[48:51], a[192:195], 0\n" \
"v_rcp_f32 v232, v248\n" \
"v_mfma_f32_16x16x32_bf16 v[28:31], a[52:55], a[196:199], v[28:31]\n" \
"v_rcp_f32 v233, v249\n" \
"v_add_u32 v240, 0x1000, v236\n" \
"v_mfma_f32_16x16x32_bf16 v[24:27], a[40:43], a[192:195], 0\n" \
"v_add_f32 v224, v248, v249\n" \
"v_mul_f32 v224, 0x43800000, v224\n" \
"v_mfma_f32_16x16x32_bf16 v[24:27], a[44:47], a[196:199], v[24:27]\n" \
"v_add_f32 v248, v232, v233\n" \
"v_fmac_f32 v224, 0x5f800000, v248\n" \
"v_mfma_f32_16x16x32_bf16 v[64:67], a[32:35], a[192:195], 0\n" \
"v_mov_b32 v225, 0\n" \
"v_mov_b32 v226, 0\n" \
"v_mfma_f32_16x16x32_bf16 v[64:67], a[36:39], a[196:199], v[64:67]\n" \
"s_waitcnt vmcnt(11) lgkmcnt(0)\n" \
"s_barrier\n" \
"v_mfma_f32_16x16x32_bf16 v[68:71], a[200:203], a[192:195], 0\n" \
"ds_read_b64_tr_b16 a[32:33], v209 offset:1024\n" \
"ds_read_b64_tr_b16 a[34:35], v209 offset:4096\n" \
"v_mfma_f32_16x16x32_bf16 v[68:71], a[204:207], a[196:199], v[68:71]\n" \
"ds_read_b64_tr_b16 a[36:37], v209 offset:13312\n" \
"ds_read_b64_tr_b16 a[38:39], v209 offset:16384\n" \
"v_mfma_f32_16x16x32_bf16 v[60:63], a[200:203], a[24:27], 0\n" \
"s_add_u32 m0, s79, 40960\n" \
"ds_read_b64_tr_b16 a[40:41], v209 offset:0\n" \
"v_mov_b32 v227, 0\n" \
"v_mfma_f32_16x16x32_bf16 v[60:63], a[204:207], a[28:31], v[60:63]\n" \
"buffer_load_dwordx4 v192, s[44:47], s73 offen offset:0 lds\n" \
"v_accvgpr_read_b32 v112, a64\n" \
"v_mfma_f32_16x16x32_bf16 v[52:55], a[200:203], a[16:19], 0\n" \
"ds_read_b64_tr_b16 a[42:43], v209 offset:3072\n" \
"v_accvgpr_read_b32 v113, a65\n" \
"v_mfma_f32_16x16x32_bf16 v[52:55], a[204:207], a[20:23], v[52:55]\n" \
"ds_read_b64_tr_b16 a[44:45], v209 offset:12288\n" \
"v_accvgpr_read_b32 v114, a66\n" \
"v_mfma_f32_16x16x32_bf16 v[44:47], a[200:203], a[8:11], 0\n" \
"ds_read_b64_tr_b16 a[46:47], v209 offset:15360\n" \
"v_accvgpr_read_b32 v115, a67\n" \
"v_mfma_f32_16x16x32_bf16 v[44:47], a[204:207], a[12:15], v[44:47]\n" \
"buffer_load_dwordx4 v193, s[44:47], s73 offen offset:1024 lds\n" \
"v_mfma_f32_16x16x32_bf16 v[72:75], a[0:3], a[208:211], 0\n" \
"ds_read_b64_tr_b16 a[48:49], v210 offset:0\n" \
"v_accvgpr_read_b32 v116, a68\n" \
"v_mfma_f32_16x16x32_bf16 v[72:75], a[4:7], a[212:215], v[72:75]\n" \
"ds_read_b128 a[0:3], v202 offset:3072\n" \
"v_accvgpr_read_b32 v117, a69\n" \
"v_accvgpr_read_b32 v118, a70\n" \
"v_mfma_f32_16x16x32_bf16 v[80:83], a[8:11], a[208:211], 0\n" \
"ds_read_b128 a[4:7], v203 offset:3072\n" \
"v_accvgpr_read_b32 v119, a71\n" \
"v_mfma_f32_16x16x32_bf16 v[80:83], a[12:15], a[212:215], v[80:83]\n" \
"buffer_load_dwordx4 v192, s[44:47], s73 offen offset:2048 lds\n" \
"v_mfma_f32_16x16x32_bf16 a[80:83], v[162:165], v[136:139], a[80:83]\n" \
"v_mul_f32 v112, v112, v232\n" \
"v_mul_f32 v113, v113, v232\n" \
"v_mfma_f32_16x16x32_bf16 v[84:87], a[8:11], a[216:219], 0\n" \
"ds_read_b64_tr_b16 a[50:51], v210 offset:3072\n" \
"v_mul_f32 v114, v114, v232\n" \
"v_mfma_f32_16x16x32_bf16 v[84:87], a[12:15], a[220:223], v[84:87]\n" \
"ds_read_b128 a[8:11], v205 offset:0\n" \
"v_mul_f32 v115, v115, v232\n" \
"v_mfma_f32_16x16x32_bf16 a[84:87], v[160:163], v[136:139], a[84:87]\n" \
"v_mul_f32 v116, v116, v232\n" \
"v_mul_f32 v117, v117, v232\n" \
"v_mfma_f32_16x16x32_bf16 v[92:95], a[16:19], a[216:219], 0\n" \
"ds_read_b128 a[12:15], v206 offset:0\n" \
"v_mul_f32 v118, v118, v232\n" \
"v_mfma_f32_16x16x32_bf16 v[92:95], a[20:23], a[220:223], v[92:95]\n" \
"buffer_load_dwordx4 v193, s[44:47], s73 offen offset:3072 lds\n" \
"v_mul_f32 v119, v119, v232\n" \
"v_mfma_f32_16x16x32_bf16 v[88:91], a[16:19], a[208:211], 0\n" \
"s_xor_b32 s73, s73, 0x180\n" \
"s_mul_i32 s76, s80, 393216\n" \
"ds_read_b64_tr_b16 a[52:53], v210 offset:12288\n" \
"v_mfma_f32_16x16x32_bf16 v[88:91], a[20:23], a[212:215], v[88:91]\n" \
"s_add_u32 s76, s76, s78\n" \
"ds_read_b128 a[16:19], v205 offset:4096\n" \
"v_cvt_pk_bf16_f32 v120, v112, v113\n" \
"v_mfma_f32_16x16x32_bf16 v[96:99], a[24:27], a[208:211], 0\n" \
"s_add_u32 m0, s91, 106496\n" \
"ds_read_b128 a[20:23], v206 offset:4096\n" \
"v_cvt_pk_bf16_f32 v121, v114, v115\n" \
"v_mfma_f32_16x16x32_bf16 v[96:99], a[28:31], a[212:215], v[96:99]\n" \
"buffer_load_dwordx4 v197, s[48:51], s76 offen offset:0 lds\n" \
"v_mfma_f32_16x16x32_bf16 v[100:103], a[24:27], a[216:219], 0\n" \
"ds_read_b64_tr_b16 a[54:55], v210 offset:15360\n" \
"v_cvt_pk_bf16_f32 v122, v116, v117\n" \
"v_mfma_f32_16x16x32_bf16 v[100:103], a[28:31], a[220:223], v[100:103]\n" \
"ds_read_b128 a[24:27], v205 offset:8192\n" \
"v_cvt_pk_bf16_f32 v123, v118, v119\n" \
"v_mfma_f32_16x16x32_bf16 v[108:111], a[192:195], a[216:219], 0\n" \
"s_add_u32 s77, s76, 24576\n" \
"ds_read_b128 a[28:31], v206 offset:8192\n" \
"v_mfma_f32_16x16x32_bf16 v[108:111], a[196:199], a[220:223], v[108:111]\n" \
"buffer_load_dwordx4 v198, s[48:51], s76 offen offset:1024 lds\n" \
"v_mfma_f32_16x16x32_bf16 v[104:107], a[192:195], a[208:211], 0\n" \
"s_waitcnt lgkmcnt(3)\n" \
"v_mfma_f32_16x16x32_bf16 v[104:107], a[196:199], a[212:215], v[104:107]\n" \
"buffer_store_dwordx4 v[120:123], v236, s[56:59], s88 offen offset:0 sc1\n" \
"v_mfma_f32_16x16x32_bf16 v[32:35], a[32:35], a[0:3], v[32:35]\n" \
"s_xor_b32 s80, s80, 0x3\n" \
"ds_read_b128 a[192:195], v205 offset:12288\n" \
"v_mfma_f32_16x16x32_bf16 v[32:35], a[36:39], a[4:7], v[32:35]\n" \
"buffer_load_dwordx4 v199, s[48:51], s76 offen offset:2048 lds\n" \
"v_mfma_f32_16x16x32_bf16 v[224:227], v[244:247], v[120:123], v[224:227]\n" \
"v_accvgpr_read_b32 v112, a72\n" \
"v_accvgpr_read_b32 v113, a73\n" \
"v_accvgpr_read_b32 v114, a74\n" \
"v_mfma_f32_16x16x32_bf16 v[40:43], a[32:35], a[8:11], v[40:43]\n" \
"ds_read_b128 a[196:199], v206 offset:12288\n" \
"v_accvgpr_read_b32 v115, a75\n" \
"v_mfma_f32_16x16x32_bf16 v[40:43], a[36:39], a[12:15], v[40:43]\n" \
"s_waitcnt lgkmcnt(2)\n" \
"ds_read_b64_tr_b16 a[200:201], v210 offset:1024\n" \
"v_accvgpr_read_b32 v116, a76\n" \
"v_accvgpr_read_b32 v117, a77\n" \
"v_mfma_f32_16x16x32_bf16 v[0:3], a[40:43], a[8:11], v[0:3]\n" \
"s_add_u32 m0, m0, 3072\n" \
"ds_read_b64_tr_b16 a[202:203], v210 offset:4096\n" \
"v_accvgpr_read_b32 v118, a78\n" \
"v_mfma_f32_16x16x32_bf16 v[0:3], a[44:47], a[12:15], v[0:3]\n" \
"buffer_load_dwordx4 v197, s[48:51], s77 offen offset:0 lds\n" \
"v_mfma_f32_16x16x32_bf16 v[4:7], a[48:51], a[8:11], v[4:7]\n" \
"ds_read_b64_tr_b16 a[204:205], v210 offset:13312\n" \
"v_accvgpr_read_b32 v119, a79\n" \
"v_mfma_f32_16x16x32_bf16 v[4:7], a[52:55], a[12:15], v[4:7]\n" \
"ds_read_b64_tr_b16 a[206:207], v210 offset:16384\n" \
"v_mfma_f32_16x16x32_bf16 a[88:91], v[162:165], v[140:143], a[88:91]\n" \
"v_mul_f32 v112, v112, v232\n" \
"v_mul_f32 v113, v113, v232\n" \
"v_mfma_f32_16x16x32_bf16 v[12:15], a[48:51], a[16:19], v[12:15]\n" \
"ds_read_b64_tr_b16 a[208:209], v209 offset:2048\n" \
"v_mul_f32 v114, v114, v232\n" \
"v_mfma_f32_16x16x32_bf16 v[12:15], a[52:55], a[20:23], v[12:15]\n" \
"buffer_load_dwordx4 v198, s[48:51], s77 offen offset:1024 lds\n" \
"v_mul_f32 v115, v115, v232\n" \
"v_mfma_f32_16x16x32_bf16 a[92:95], v[160:163], v[140:143], a[92:95]\n" \
"v_mul_f32 v116, v116, v232\n" \
"v_mul_f32 v117, v117, v232\n" \
"v_mfma_f32_16x16x32_bf16 v[8:11], a[40:43], a[16:19], v[8:11]\n" \
"ds_read_b64_tr_b16 a[210:211], v209 offset:5120\n" \
"v_mul_f32 v118, v118, v232\n" \
"v_mfma_f32_16x16x32_bf16 v[8:11], a[44:47], a[20:23], v[8:11]\n" \
"ds_read_b64_tr_b16 a[212:213], v209 offset:14336\n" \
"v_mul_f32 v119, v119, v232\n" \
"v_mfma_f32_16x16x32_bf16 v[48:51], a[32:35], a[16:19], v[48:51]\n" \
"ds_read_b64_tr_b16 a[214:215], v209 offset:17408\n" \
"v_cvt_pk_bf16_f32 v124, v112, v113\n" \
"v_mfma_f32_16x16x32_bf16 v[48:51], a[36:39], a[20:23], v[48:51]\n" \
"buffer_load_dwordx4 v199, s[48:51], s77 offen offset:2048 lds\n" \
"v_mfma_f32_16x16x32_bf16 v[56:59], a[32:35], a[24:27], v[56:59]\n" \
"ds_read_b64_tr_b16 a[216:217], v210 offset:2048\n" \
"v_cvt_pk_bf16_f32 v125, v114, v115\n" \
"v_mfma_f32_16x16x32_bf16 v[56:59], a[36:39], a[28:31], v[56:59]\n" \
"ds_read_b64_tr_b16 a[218:219], v210 offset:5120\n" \
"v_cvt_pk_bf16_f32 v126, v116, v117\n" \
"v_mfma_f32_16x16x32_bf16 v[16:19], a[40:43], a[24:27], v[16:19]\n" \
"ds_read_b64_tr_b16 a[220:221], v210 offset:14336\n" \
"v_cvt_pk_bf16_f32 v127, v118, v119\n" \
"v_mfma_f32_16x16x32_bf16 v[16:19], a[44:47], a[28:31], v[16:19]\n" \
"buffer_store_dwordx4 v[124:127], v236, s[56:59], s88 offen offset:2048 sc1\n" \
"v_mfma_f32_16x16x32_bf16 v[20:23], a[48:51], a[24:27], v[20:23]\n" \
"ds_read_b64_tr_b16 a[222:223], v210 offset:17408\n" \
"v_mfma_f32_16x16x32_bf16 v[20:23], a[52:55], a[28:31], v[20:23]\n" \
"s_waitcnt lgkmcnt(3)\n" \
"v_mfma_f32_16x16x32_bf16 v[224:227], v[244:247], v[124:127], v[224:227]\n" \
"v_accvgpr_read_b32 v112, a80\n" \
"v_accvgpr_read_b32 v113, a81\n" \
"v_accvgpr_read_b32 v114, a82\n" \
"v_mfma_f32_16x16x32_bf16 v[28:31], a[48:51], a[192:195], v[28:31]\n" \
"v_accvgpr_read_b32 v115, a83\n" \
"v_accvgpr_read_b32 v116, a84\n" \
"v_mfma_f32_16x16x32_bf16 v[28:31], a[52:55], a[196:199], v[28:31]\n" \
"v_accvgpr_read_b32 v117, a85\n" \
"v_accvgpr_read_b32 v118, a86\n" \
"v_mfma_f32_16x16x32_bf16 v[24:27], a[40:43], a[192:195], v[24:27]\n" \
"v_accvgpr_read_b32 v119, a87\n" \
"v_mfma_f32_16x16x32_bf16 v[24:27], a[44:47], a[196:199], v[24:27]\n" \
"v_mfma_f32_16x16x32_bf16 a[96:99], v[162:165], v[144:147], a[96:99]\n" \
"v_mul_f32 v112, v112, v232\n" \
"v_mul_f32 v113, v113, v232\n" \
"v_mul_f32 v114, v114, v232\n" \
"v_mfma_f32_16x16x32_bf16 v[64:67], a[32:35], a[192:195], v[64:67]\n" \
"v_mul_f32 v115, v115, v232\n" \
"v_mfma_f32_16x16x32_bf16 v[64:67], a[36:39], a[196:199], v[64:67]\n" \
"v_mfma_f32_16x16x32_bf16 a[100:103], v[160:163], v[144:147], a[100:103]\n" \
"s_waitcnt vmcnt(12) lgkmcnt(0)\n" \
"s_barrier\n" \
"v_mfma_f32_16x16x32_bf16 v[68:71], a[200:203], a[192:195], v[68:71]\n" \
"ds_read_b64_tr_b16 a[32:33], v204 offset:17408\n" \
"ds_read_b64_tr_b16 a[34:35], v204 offset:20480\n" \
"v_mfma_f32_16x16x32_bf16 v[68:71], a[204:207], a[196:199], v[68:71]\n" \
"ds_read_b64_tr_b16 a[36:37], v204 offset:29696\n" \
"ds_read_b64_tr_b16 a[38:39], v204 offset:32768\n" \
"v_mfma_f32_16x16x32_bf16 v[60:63], a[200:203], a[24:27], v[60:63]\n" \
"s_add_u32 m0, s79, 65536\n" \
"ds_read_b64_tr_b16 a[40:41], v204 offset:16384\n" \
"v_mul_f32 v116, v116, v232\n" \
"v_mfma_f32_16x16x32_bf16 v[60:63], a[204:207], a[28:31], v[60:63]\n" \
"buffer_load_dwordx4 v192, s[44:47], s73 offen offset:0 lds\n" \
"v_mul_f32 v117, v117, v232\n" \
"v_mfma_f32_16x16x32_bf16 v[52:55], a[200:203], a[16:19], v[52:55]\n" \
"ds_read_b64_tr_b16 a[42:43], v204 offset:19456\n" \
"v_mul_f32 v118, v118, v232\n" \
"v_mfma_f32_16x16x32_bf16 v[52:55], a[204:207], a[20:23], v[52:55]\n" \
"ds_read_b64_tr_b16 a[44:45], v204 offset:28672\n" \
"v_mul_f32 v119, v119, v232\n" \
"v_mfma_f32_16x16x32_bf16 v[44:47], a[200:203], a[8:11], v[44:47]\n" \
"ds_read_b64_tr_b16 a[46:47], v204 offset:31744\n" \
"v_cvt_pk_bf16_f32 v120, v112, v113\n" \
"v_mfma_f32_16x16x32_bf16 v[44:47], a[204:207], a[12:15], v[44:47]\n" \
"buffer_load_dwordx4 v193, s[44:47], s73 offen offset:1024 lds\n" \
"v_mfma_f32_16x16x32_bf16 v[72:75], a[0:3], a[208:211], v[72:75]\n" \
"ds_read_b64_tr_b16 a[48:49], v222 offset:16384\n" \
"v_cvt_pk_bf16_f32 v121, v114, v115\n" \
"v_mfma_f32_16x16x32_bf16 v[72:75], a[4:7], a[212:215], v[72:75]\n" \
"ds_read_b128 a[0:3], v202 offset:19456\n" \
"v_cvt_pk_bf16_f32 v122, v116, v117\n" \
"v_mfma_f32_16x16x32_bf16 v[80:83], a[8:11], a[208:211], v[80:83]\n" \
"ds_read_b128 a[4:7], v203 offset:19456\n" \
"v_cvt_pk_bf16_f32 v123, v118, v119\n" \
"v_mfma_f32_16x16x32_bf16 v[80:83], a[12:15], a[212:215], v[80:83]\n" \
"buffer_load_dwordx4 v192, s[44:47], s73 offen offset:2048 lds\n" \
"v_mfma_f32_16x16x32_bf16 v[84:87], a[8:11], a[216:219], v[84:87]\n" \
"ds_read_b64_tr_b16 a[50:51], v222 offset:19456\n" \
"v_mfma_f32_16x16x32_bf16 v[84:87], a[12:15], a[220:223], v[84:87]\n" \
"ds_read_b128 a[8:11], v200 offset:0\n" \
"buffer_store_dwordx4 v[120:123], v240, s[56:59], s88 offen offset:0 sc1\n" \
"v_mfma_f32_16x16x32_bf16 v[92:95], a[16:19], a[216:219], v[92:95]\n" \
"ds_read_b128 a[12:15], v201 offset:0\n" \
"v_mfma_f32_16x16x32_bf16 v[92:95], a[20:23], a[220:223], v[92:95]\n" \
"buffer_load_dwordx4 v193, s[44:47], s73 offen offset:3072 lds\n" \
"v_mfma_f32_16x16x32_bf16 v[224:227], v[244:247], v[120:123], v[224:227]\n" \
"v_accvgpr_read_b32 v112, a88\n" \
"v_accvgpr_read_b32 v113, a89\n" \
"v_accvgpr_read_b32 v114, a90\n" \
"v_mfma_f32_16x16x32_bf16 v[88:91], a[16:19], a[208:211], v[88:91]\n" \
"s_xor_b32 s73, s73, 0x300\n" \
"s_mul_i32 s76, s80, 393216\n" \
"ds_read_b64_tr_b16 a[52:53], v222 offset:28672\n" \
"v_mfma_f32_16x16x32_bf16 v[88:91], a[20:23], a[212:215], v[88:91]\n" \
"s_add_u32 s76, s76, s78\n" \
"ds_read_b128 a[16:19], v200 offset:4096\n" \
"v_accvgpr_read_b32 v115, a91\n" \
"v_accvgpr_read_b32 v116, a92\n" \
"v_mfma_f32_16x16x32_bf16 v[96:99], a[24:27], a[208:211], v[96:99]\n" \
"s_add_u32 m0, s91, 81920\n" \
"ds_read_b128 a[20:23], v201 offset:4096\n" \
"v_accvgpr_read_b32 v117, a93\n" \
"v_mfma_f32_16x16x32_bf16 v[96:99], a[28:31], a[212:215], v[96:99]\n" \
"buffer_load_dwordx4 v197, s[48:51], s76 offen offset:0 lds\n" \
"v_mfma_f32_16x16x32_bf16 v[100:103], a[24:27], a[216:219], v[100:103]\n" \
"ds_read_b64_tr_b16 a[54:55], v222 offset:31744\n" \
"v_accvgpr_read_b32 v118, a94\n" \
"v_mfma_f32_16x16x32_bf16 v[100:103], a[28:31], a[220:223], v[100:103]\n" \
"ds_read_b128 a[24:27], v200 offset:8192\n" \
"v_accvgpr_read_b32 v119, a95\n" \
"v_mfma_f32_16x16x32_bf16 a[104:107], v[162:165], v[148:151], a[104:107]\n" \
"v_mul_f32 v112, v112, v232\n" \
"v_mul_f32 v113, v113, v232\n" \
"v_mfma_f32_16x16x32_bf16 v[108:111], a[192:195], a[216:219], v[108:111]\n" \
"s_add_u32 s77, s76, 24576\n" \
"ds_read_b128 a[28:31], v201 offset:8192\n" \
"v_mul_f32 v114, v114, v232\n" \
"v_mfma_f32_16x16x32_bf16 v[108:111], a[196:199], a[220:223], v[108:111]\n" \
"buffer_load_dwordx4 v198, s[48:51], s76 offen offset:1024 lds\n" \
"v_mul_f32 v115, v115, v232\n" \
"v_mfma_f32_16x16x32_bf16 a[108:111], v[160:163], v[148:151], a[108:111]\n" \
"v_mul_f32 v116, v116, v232\n" \
"v_mul_f32 v117, v117, v232\n" \
"v_mfma_f32_16x16x32_bf16 v[104:107], a[192:195], a[208:211], v[104:107]\n" \
"v_mul_f32 v118, v118, v232\n" \
"v_mul_f32 v119, v119, v232\n" \
"v_mfma_f32_16x16x32_bf16 v[104:107], a[196:199], a[212:215], v[104:107]\n" \
"s_waitcnt lgkmcnt(2)\n" \
"v_cvt_pk_bf16_f32 v124, v112, v113\n" \
"v_cvt_pk_bf16_f32 v125, v114, v115\n" \
"v_mfma_f32_16x16x32_bf16 v[32:35], a[32:35], a[0:3], v[32:35]\n" \
"s_xor_b32 s80, s80, 0x6\n" \
"ds_read_b128 a[192:195], v200 offset:12288\n" \
"v_cvt_pk_bf16_f32 v126, v116, v117\n" \
"v_mfma_f32_16x16x32_bf16 v[32:35], a[36:39], a[4:7], v[32:35]\n" \
"buffer_load_dwordx4 v199, s[48:51], s76 offen offset:2048 lds\n" \
"v_mfma_f32_16x16x32_bf16 v[40:43], a[32:35], a[8:11], v[40:43]\n" \
"ds_read_b128 a[196:199], v201 offset:12288\n" \
"v_cvt_pk_bf16_f32 v127, v118, v119\n" \
"v_mfma_f32_16x16x32_bf16 v[40:43], a[36:39], a[12:15], v[40:43]\n" \
"ds_read_b64_tr_b16 a[200:201], v222 offset:17408\n" \
"buffer_store_dwordx4 v[124:127], v240, s[56:59], s88 offen offset:2048 sc1\n" \
"v_mfma_f32_16x16x32_bf16 v[0:3], a[40:43], a[8:11], v[0:3]\n" \
"s_add_u32 m0, m0, 3072\n" \
"ds_read_b64_tr_b16 a[202:203], v222 offset:20480\n" \
"v_mfma_f32_16x16x32_bf16 v[0:3], a[44:47], a[12:15], v[0:3]\n" \
"buffer_load_dwordx4 v197, s[48:51], s77 offen offset:0 lds\n" \
"v_mfma_f32_16x16x32_bf16 v[224:227], v[244:247], v[124:127], v[224:227]\n" \
"s_add_u32 s82, s88, 532480\n" \
"v_accvgpr_read_b32 v112, a96\n" \
"v_accvgpr_read_b32 v113, a97\n" \
"v_accvgpr_read_b32 v114, a98\n" \
"v_mfma_f32_16x16x32_bf16 v[4:7], a[48:51], a[8:11], v[4:7]\n" \
"ds_read_b64_tr_b16 a[204:205], v222 offset:29696\n" \
"v_accvgpr_read_b32 v115, a99\n" \
"v_mfma_f32_16x16x32_bf16 v[4:7], a[52:55], a[12:15], v[4:7]\n" \
"ds_read_b64_tr_b16 a[206:207], v222 offset:32768\n" \
"v_accvgpr_read_b32 v116, a100\n" \
"v_mfma_f32_16x16x32_bf16 v[12:15], a[48:51], a[16:19], v[12:15]\n" \
"ds_read_b64_tr_b16 a[208:209], v204 offset:18432\n" \
"v_accvgpr_read_b32 v117, a101\n" \
"v_mfma_f32_16x16x32_bf16 v[12:15], a[52:55], a[20:23], v[12:15]\n" \
"buffer_load_dwordx4 v198, s[48:51], s77 offen offset:1024 lds\n" \
"v_mfma_f32_16x16x32_bf16 v[8:11], a[40:43], a[16:19], v[8:11]\n" \
"ds_read_b64_tr_b16 a[210:211], v204 offset:21504\n" \
"v_accvgpr_read_b32 v118, a102\n" \
"v_mfma_f32_16x16x32_bf16 v[8:11], a[44:47], a[20:23], v[8:11]\n" \
"ds_read_b64_tr_b16 a[212:213], v204 offset:30720\n" \
"v_accvgpr_read_b32 v119, a103\n" \
"v_mfma_f32_16x16x32_bf16 a[112:115], v[162:165], v[152:155], a[112:115]\n" \
"v_mul_f32 v112, v112, v233\n" \
"v_mul_f32 v113, v113, v233\n" \
"v_mul_f32 v114, v114, v233\n" \
"v_mfma_f32_16x16x32_bf16 v[48:51], a[32:35], a[16:19], v[48:51]\n" \
"s_waitcnt lgkmcnt(3)\n" \
"ds_read_b64_tr_b16 a[214:215], v204 offset:33792\n" \
"v_mul_f32 v115, v115, v233\n" \
"v_mfma_f32_16x16x32_bf16 v[48:51], a[36:39], a[20:23], v[48:51]\n" \
"buffer_load_dwordx4 v199, s[48:51], s77 offen offset:2048 lds\n" \
"v_mfma_f32_16x16x32_bf16 a[116:119], v[160:163], v[152:155], a[116:119]\n" \
"v_mul_f32 v116, v116, v233\n" \
"v_mul_f32 v117, v117, v233\n" \
"v_mfma_f32_16x16x32_bf16 v[56:59], a[32:35], a[24:27], v[56:59]\n" \
"ds_read_b64_tr_b16 a[216:217], v222 offset:18432\n" \
"v_mul_f32 v118, v118, v233\n" \
"v_mfma_f32_16x16x32_bf16 v[56:59], a[36:39], a[28:31], v[56:59]\n" \
"ds_read_b64_tr_b16 a[218:219], v222 offset:21504\n" \
"v_mul_f32 v119, v119, v233\n" \
"v_mfma_f32_16x16x32_bf16 v[16:19], a[40:43], a[24:27], v[16:19]\n" \
"ds_read_b64_tr_b16 a[220:221], v222 offset:30720\n" \
"v_cvt_pk_bf16_f32 v120, v112, v113\n" \
"v_mfma_f32_16x16x32_bf16 v[16:19], a[44:47], a[28:31], v[16:19]\n" \
"v_cvt_pk_bf16_f32 v121, v114, v115\n" \
"v_cvt_pk_bf16_f32 v122, v116, v117\n" \
"v_cvt_pk_bf16_f32 v123, v118, v119\n" \
"v_mfma_f32_16x16x32_bf16 v[20:23], a[48:51], a[24:27], v[20:23]\n" \
"ds_read_b64_tr_b16 a[222:223], v222 offset:33792\n" \
"v_mfma_f32_16x16x32_bf16 v[20:23], a[52:55], a[28:31], v[20:23]\n" \
"buffer_store_dwordx4 v[120:123], v236, s[56:59], s82 offen offset:0 sc1\n" \
"v_mfma_f32_16x16x32_bf16 v[28:31], a[48:51], a[192:195], v[28:31]\n" \
"v_mfma_f32_16x16x32_bf16 v[28:31], a[52:55], a[196:199], v[28:31]\n" \
"v_mfma_f32_16x16x32_bf16 v[224:227], v[244:247], v[120:123], v[224:227]\n" \
"v_accvgpr_read_b32 v112, a104\n" \
"v_accvgpr_read_b32 v113, a105\n" \
"v_mfma_f32_16x16x32_bf16 v[24:27], a[40:43], a[192:195], v[24:27]\n" \
"v_accvgpr_read_b32 v114, a106\n" \
"v_accvgpr_read_b32 v115, a107\n" \
"v_mfma_f32_16x16x32_bf16 v[24:27], a[44:47], a[196:199], v[24:27]\n" \
"v_accvgpr_read_b32 v116, a108\n" \
"v_accvgpr_read_b32 v117, a109\n" \
"v_mfma_f32_16x16x32_bf16 v[64:67], a[32:35], a[192:195], v[64:67]\n" \
"v_accvgpr_read_b32 v118, a110\n" \
"v_accvgpr_read_b32 v119, a111\n" \
"v_mfma_f32_16x16x32_bf16 v[64:67], a[36:39], a[196:199], v[64:67]\n" \
"v_mfma_f32_16x16x32_bf16 a[120:123], v[162:165], v[156:159], a[120:123]\n" \
"v_mul_f32 v112, v112, v233\n" \
"s_waitcnt vmcnt(14) lgkmcnt(0)\n" \
"s_barrier\n" \
"v_mfma_f32_16x16x32_bf16 v[68:71], a[200:203], a[192:195], v[68:71]\n" \
"ds_read_b64_tr_b16 a[32:33], v209 offset:25600\n" \
"ds_read_b64_tr_b16 a[34:35], v209 offset:28672\n" \
"v_mfma_f32_16x16x32_bf16 v[68:71], a[204:207], a[196:199], v[68:71]\n" \
"ds_read_b64_tr_b16 a[36:37], v209 offset:37888\n" \
"ds_read_b64_tr_b16 a[38:39], v209 offset:40960\n" \
"v_mfma_f32_16x16x32_bf16 v[60:63], a[200:203], a[24:27], v[60:63]\n" \
"s_add_u32 m0, s79, 0\n" \
"ds_read_b64_tr_b16 a[40:41], v209 offset:24576\n" \
"v_mul_f32 v113, v113, v233\n" \
"v_mfma_f32_16x16x32_bf16 v[60:63], a[204:207], a[28:31], v[60:63]\n" \
"buffer_load_dwordx4 v192, s[44:47], s73 offen offset:0 lds\n" \
"v_mfma_f32_16x16x32_bf16 v[52:55], a[200:203], a[16:19], v[52:55]\n" \
"ds_read_b64_tr_b16 a[42:43], v209 offset:27648\n" \
"v_mul_f32 v114, v114, v233\n" \
"v_mfma_f32_16x16x32_bf16 v[52:55], a[204:207], a[20:23], v[52:55]\n" \
"ds_read_b64_tr_b16 a[44:45], v209 offset:36864\n" \
"v_mul_f32 v115, v115, v233\n" \
"v_mfma_f32_16x16x32_bf16 a[124:127], v[160:163], v[156:159], a[124:127]\n" \
"v_mul_f32 v116, v116, v233\n" \
"v_mul_f32 v117, v117, v233\n" \
"v_mul_f32 v118, v118, v233\n" \
"v_mfma_f32_16x16x32_bf16 v[44:47], a[200:203], a[8:11], v[44:47]\n" \
"ds_read_b64_tr_b16 a[46:47], v209 offset:39936\n" \
"v_mul_f32 v119, v119, v233\n" \
"v_mfma_f32_16x16x32_bf16 v[44:47], a[204:207], a[12:15], v[44:47]\n" \
"buffer_load_dwordx4 v193, s[44:47], s73 offen offset:1024 lds\n" \
"v_mfma_f32_16x16x32_bf16 v[72:75], a[0:3], a[208:211], v[72:75]\n" \
"ds_read_b64_tr_b16 a[48:49], v210 offset:24576\n" \
"v_cvt_pk_bf16_f32 v124, v112, v113\n" \
"v_mfma_f32_16x16x32_bf16 v[72:75], a[4:7], a[212:215], v[72:75]\n" \
"ds_read_b128 a[0:3], v202 offset:1024\n" \
"v_cvt_pk_bf16_f32 v125, v114, v115\n" \
"v_mfma_f32_16x16x32_bf16 v[80:83], a[8:11], a[208:211], v[80:83]\n" \
"ds_read_b128 a[4:7], v203 offset:1024\n" \
"v_cvt_pk_bf16_f32 v126, v116, v117\n" \
"v_mfma_f32_16x16x32_bf16 v[80:83], a[12:15], a[212:215], v[80:83]\n" \
"buffer_load_dwordx4 v192, s[44:47], s73 offen offset:2048 lds\n" \
"v_cvt_pk_bf16_f32 v127, v118, v119\n" \
"v_mfma_f32_16x16x32_bf16 v[84:87], a[8:11], a[216:219], v[84:87]\n" \
"ds_read_b64_tr_b16 a[50:51], v210 offset:27648\n" \
"v_mfma_f32_16x16x32_bf16 v[84:87], a[12:15], a[220:223], v[84:87]\n" \
"ds_read_b128 a[8:11], v200 offset:40960\n" \
"buffer_store_dwordx4 v[124:127], v236, s[56:59], s82 offen offset:2048 sc1\n" \
"v_mfma_f32_16x16x32_bf16 v[92:95], a[16:19], a[216:219], v[92:95]\n" \
"ds_read_b128 a[12:15], v201 offset:40960\n" \
"v_mfma_f32_16x16x32_bf16 v[92:95], a[20:23], a[220:223], v[92:95]\n" \
"buffer_load_dwordx4 v193, s[44:47], s73 offen offset:3072 lds\n" \
"v_mfma_f32_16x16x32_bf16 v[224:227], v[244:247], v[124:127], v[224:227]\n" \
"v_accvgpr_read_b32 v112, a112\n" \
"v_accvgpr_read_b32 v113, a113\n" \
"v_mfma_f32_16x16x32_bf16 v[88:91], a[16:19], a[208:211], v[88:91]\n" \
"s_xor_b32 s73, s73, 0x80\n" \
"s_mul_i32 s76, s80, 393216\n" \
"ds_read_b64_tr_b16 a[52:53], v210 offset:36864\n" \
"v_mfma_f32_16x16x32_bf16 v[88:91], a[20:23], a[212:215], v[88:91]\n" \
"s_add_u32 s76, s76, s78\n" \
"ds_read_b128 a[16:19], v200 offset:45056\n" \
"v_accvgpr_read_b32 v114, a114\n" \
"v_mfma_f32_16x16x32_bf16 v[96:99], a[24:27], a[208:211], v[96:99]\n" \
"s_add_u32 m0, s91, 16384\n" \
"ds_read_b128 a[20:23], v201 offset:45056\n" \
"v_accvgpr_read_b32 v115, a115\n" \
"v_mfma_f32_16x16x32_bf16 v[96:99], a[28:31], a[212:215], v[96:99]\n" \
"buffer_load_dwordx4 v197, s[48:51], s76 offen offset:0 lds\n" \
"v_accvgpr_read_b32 v116, a116\n" \
"v_mfma_f32_16x16x32_bf16 v[100:103], a[24:27], a[216:219], v[100:103]\n" \
"ds_read_b64_tr_b16 a[54:55], v210 offset:39936\n" \
"v_accvgpr_read_b32 v117, a117\n" \
"v_mfma_f32_16x16x32_bf16 v[100:103], a[28:31], a[220:223], v[100:103]\n" \
"ds_read_b128 a[24:27], v200 offset:49152\n" \
"v_accvgpr_read_b32 v118, a118\n" \
"v_mfma_f32_16x16x32_bf16 v[108:111], a[192:195], a[216:219], v[108:111]\n" \
"s_add_u32 s77, s76, 24576\n" \
"ds_read_b128 a[28:31], v201 offset:49152\n" \
"v_accvgpr_read_b32 v119, a119\n" \
"v_mfma_f32_16x16x32_bf16 v[108:111], a[196:199], a[220:223], v[108:111]\n" \
"buffer_load_dwordx4 v198, s[48:51], s76 offen offset:1024 lds\n" \
"v_mfma_f32_16x16x32_bf16 v[104:107], a[192:195], a[208:211], v[104:107]\n" \
"v_mul_f32 v112, v112, v233\n" \
"v_mul_f32 v113, v113, v233\n" \
"v_mfma_f32_16x16x32_bf16 v[104:107], a[196:199], a[212:215], v[104:107]\n" \
"s_waitcnt lgkmcnt(2)\n" \
"v_mul_f32 v114, v114, v233\n" \
"v_mul_f32 v115, v115, v233\n" \
"v_mul_f32 v116, v116, v233\n" \
"v_mfma_f32_16x16x32_bf16 v[32:35], a[32:35], a[0:3], v[32:35]\n" \
"s_xor_b32 s80, s80, 0x1\n" \
"ds_read_b128 a[192:195], v200 offset:53248\n" \
"v_mul_f32 v117, v117, v233\n" \
"v_mfma_f32_16x16x32_bf16 v[32:35], a[36:39], a[4:7], v[32:35]\n" \
"buffer_load_dwordx4 v199, s[48:51], s76 offen offset:2048 lds\n" \
"v_mfma_f32_16x16x32_bf16 v[40:43], a[32:35], a[8:11], v[40:43]\n" \
"ds_read_b128 a[196:199], v201 offset:53248\n" \
"v_mul_f32 v118, v118, v233\n" \
"v_mfma_f32_16x16x32_bf16 v[40:43], a[36:39], a[12:15], v[40:43]\n" \
"ds_read_b64_tr_b16 a[200:201], v210 offset:25600\n" \
"v_mul_f32 v119, v119, v233\n" \
"v_mfma_f32_16x16x32_bf16 v[0:3], a[40:43], a[8:11], v[0:3]\n" \
"s_add_u32 m0, m0, 3072\n" \
"ds_read_b64_tr_b16 a[202:203], v210 offset:28672\n" \
"v_cvt_pk_bf16_f32 v120, v112, v113\n" \
"v_mfma_f32_16x16x32_bf16 v[0:3], a[44:47], a[12:15], v[0:3]\n" \
"buffer_load_dwordx4 v197, s[48:51], s77 offen offset:0 lds\n" \
"v_cvt_pk_bf16_f32 v121, v114, v115\n" \
"v_mfma_f32_16x16x32_bf16 v[4:7], a[48:51], a[8:11], v[4:7]\n" \
"ds_read_b64_tr_b16 a[204:205], v210 offset:37888\n" \
"v_cvt_pk_bf16_f32 v122, v116, v117\n" \
"v_mfma_f32_16x16x32_bf16 v[4:7], a[52:55], a[12:15], v[4:7]\n" \
"ds_read_b64_tr_b16 a[206:207], v210 offset:40960\n" \
"v_cvt_pk_bf16_f32 v123, v118, v119\n" \
"v_mfma_f32_16x16x32_bf16 v[12:15], a[48:51], a[16:19], v[12:15]\n" \
"ds_read_b64_tr_b16 a[208:209], v209 offset:26624\n" \
"v_mfma_f32_16x16x32_bf16 v[12:15], a[52:55], a[20:23], v[12:15]\n" \
"buffer_load_dwordx4 v198, s[48:51], s77 offen offset:1024 lds\n" \
"v_mfma_f32_16x16x32_bf16 v[8:11], a[40:43], a[16:19], v[8:11]\n" \
"Lpg7927_%=:\n" \
"Lst1x0_%=:\n" \
"s_cbranch_execz Lpg10211_%=\n" \
"ds_read_b64_tr_b16 a[210:211], v209 offset:29696\n" \
"v_mfma_f32_16x16x32_bf16 v[8:11], a[44:47], a[20:23], v[8:11]\n" \
"ds_read_b64_tr_b16 a[212:213], v209 offset:38912\n" \
"buffer_store_dwordx4 v[120:123], v240, s[56:59], s82 offen offset:0 sc1\n" \
"v_mfma_f32_16x16x32_bf16 v[48:51], a[32:35], a[16:19], v[48:51]\n" \
"s_waitcnt lgkmcnt(3)\n" \
"ds_read_b64_tr_b16 a[214:215], v209 offset:41984\n" \
"v_mfma_f32_16x16x32_bf16 v[48:51], a[36:39], a[20:23], v[48:51]\n" \
"buffer_load_dwordx4 v199, s[48:51], s77 offen offset:2048 lds\n" \
"v_mfma_f32_16x16x32_bf16 v[224:227], v[244:247], v[120:123], v[224:227]\n" \
"v_accvgpr_read_b32 v112, a120\n" \
"v_accvgpr_read_b32 v113, a121\n" \
"v_accvgpr_read_b32 v114, a122\n" \
"v_mfma_f32_16x16x32_bf16 v[56:59], a[32:35], a[24:27], v[56:59]\n" \
"ds_read_b64_tr_b16 a[216:217], v210 offset:26624\n" \
"v_accvgpr_read_b32 v115, a123\n" \
"v_mfma_f32_16x16x32_bf16 v[56:59], a[36:39], a[28:31], v[56:59]\n" \
"ds_read_b64_tr_b16 a[218:219], v210 offset:29696\n" \
"v_accvgpr_read_b32 v116, a124\n" \
"v_mfma_f32_16x16x32_bf16 v[16:19], a[40:43], a[24:27], v[16:19]\n" \
"ds_read_b64_tr_b16 a[220:221], v210 offset:38912\n" \
"v_accvgpr_read_b32 v117, a125\n" \
"v_mfma_f32_16x16x32_bf16 v[16:19], a[44:47], a[28:31], v[16:19]\n" \
"v_accvgpr_read_b32 v118, a126\n" \
"v_accvgpr_read_b32 v119, a127\n" \
"v_mfma_f32_16x16x32_bf16 v[20:23], a[48:51], a[24:27], v[20:23]\n" \
"ds_read_b64_tr_b16 a[222:223], v210 offset:41984\n" \
"v_mul_f32 v112, v112, v233\n" \
"v_mfma_f32_16x16x32_bf16 v[20:23], a[52:55], a[28:31], v[20:23]\n" \
"v_mul_f32 v113, v113, v233\n" \
"v_mul_f32 v114, v114, v233\n" \
"v_mul_f32 v115, v115, v233\n" \
"v_mfma_f32_16x16x32_bf16 v[28:31], a[48:51], a[192:195], v[28:31]\n" \
"v_mul_f32 v116, v116, v233\n" \
"v_mul_f32 v117, v117, v233\n" \
"v_mfma_f32_16x16x32_bf16 v[28:31], a[52:55], a[196:199], v[28:31]\n" \
"v_mul_f32_e64 v118, v118, v233\n" \
"v_mul_f32_e64 v119, v119, v233\n" \
"v_mfma_f32_16x16x32_bf16 v[24:27], a[40:43], a[192:195], v[24:27]\n" \
"v_cvt_pk_bf16_f32 v124, v112, v113\n" \
"v_cvt_pk_bf16_f32 v125, v114, v115\n" \
"v_mfma_f32_16x16x32_bf16 v[24:27], a[44:47], a[196:199], v[24:27]\n" \
"v_cvt_pk_bf16_f32 v126, v116, v117\n" \
"v_cvt_pk_bf16_f32 v127, v118, v119\n" \
"v_mfma_f32_16x16x32_bf16 v[64:67], a[32:35], a[192:195], v[64:67]\n" \
"buffer_store_dwordx4 v[124:127], v240, s[56:59], s82 offen offset:2048 sc1\n" \
"v_mfma_f32_16x16x32_bf16 v[64:67], a[36:39], a[196:199], v[64:67]\n" \
"v_mfma_f32_16x16x32_bf16 v[224:227], v[244:247], v[124:127], v[224:227]\n" \
"s_waitcnt vmcnt(14) lgkmcnt(0)\n" \
"s_barrier\n" \
"v_mfma_f32_16x16x32_bf16 v[68:71], a[200:203], a[192:195], v[68:71]\n" \
"ds_read_b64_tr_b16 a[32:33], v209 offset:1024\n" \
"ds_read_b64_tr_b16 a[34:35], v209 offset:4096\n" \
"v_mfma_f32_16x16x32_bf16 v[68:71], a[204:207], a[196:199], v[68:71]\n" \
"ds_read_b64_tr_b16 a[36:37], v209 offset:13312\n" \
"ds_read_b64_tr_b16 a[38:39], v209 offset:16384\n" \
"v_mfma_f32_16x16x32_bf16 v[60:63], a[200:203], a[24:27], v[60:63]\n" \
"s_add_u32 m0, s79, 40960\n" \
"ds_read_b64_tr_b16 a[40:41], v209 offset:0\n" \
"v_mfma_f32_16x16x32_bf16 v[60:63], a[204:207], a[28:31], v[60:63]\n" \
"buffer_load_dwordx4 v192, s[44:47], s73 offen offset:0 lds\n" \
"v_mfma_f32_16x16x32_bf16 v[52:55], a[200:203], a[16:19], v[52:55]\n" \
"ds_read_b64_tr_b16 a[42:43], v209 offset:3072\n" \
"v_mfma_f32_16x16x32_bf16 v[52:55], a[204:207], a[20:23], v[52:55]\n" \
"ds_read_b64_tr_b16 a[44:45], v209 offset:12288\n" \
"v_mfma_f32_16x16x32_bf16 v[44:47], a[200:203], a[8:11], v[44:47]\n" \
"ds_read_b64_tr_b16 a[46:47], v209 offset:15360\n" \
"v_mfma_f32_16x16x32_bf16 v[44:47], a[204:207], a[12:15], v[44:47]\n" \
"buffer_load_dwordx4 v193, s[44:47], s73 offen offset:1024 lds\n" \
"v_mfma_f32_16x16x32_bf16 v[72:75], a[0:3], a[208:211], v[72:75]\n" \
"ds_read_b64_tr_b16 a[48:49], v210 offset:0\n" \
"v_mfma_f32_16x16x32_bf16 v[72:75], a[4:7], a[212:215], v[72:75]\n" \
"ds_read_b128 a[0:3], v202 offset:2048\n" \
"v_mfma_f32_16x16x32_bf16 v[80:83], a[8:11], a[208:211], v[80:83]\n" \
"ds_read_b128 a[4:7], v203 offset:2048\n" \
"v_mfma_f32_16x16x32_bf16 v[80:83], a[12:15], a[212:215], v[80:83]\n" \
"buffer_load_dwordx4 v192, s[44:47], s73 offen offset:2048 lds\n" \
"v_mfma_f32_16x16x32_bf16 v[84:87], a[8:11], a[216:219], v[84:87]\n" \
"ds_read_b64_tr_b16 a[50:51], v210 offset:3072\n" \
"v_mfma_f32_16x16x32_bf16 v[84:87], a[12:15], a[220:223], v[84:87]\n" \
"ds_read_b128 a[8:11], v205 offset:0\n" \
"v_mfma_f32_16x16x32_bf16 v[92:95], a[16:19], a[216:219], v[92:95]\n" \
"ds_read_b128 a[12:15], v206 offset:0\n" \
"v_mfma_f32_16x16x32_bf16 v[92:95], a[20:23], a[220:223], v[92:95]\n" \
"buffer_load_dwordx4 v193, s[44:47], s73 offen offset:3072 lds\n" \
"v_mfma_f32_16x16x32_bf16 v[88:91], a[16:19], a[208:211], v[88:91]\n" \
"s_xor_b32 s73, s73, 0x180\n" \
"s_mul_i32 s76, s80, 393216\n" \
"ds_read_b64_tr_b16 a[52:53], v210 offset:12288\n" \
"v_mfma_f32_16x16x32_bf16 v[88:91], a[20:23], a[212:215], v[88:91]\n" \
"s_add_u32 s76, s76, s78\n" \
"ds_read_b128 a[16:19], v205 offset:4096\n" \
"v_mfma_f32_16x16x32_bf16 v[96:99], a[24:27], a[208:211], v[96:99]\n" \
"s_add_u32 m0, s91, 106496\n" \
"ds_read_b128 a[20:23], v206 offset:4096\n" \
"v_mfma_f32_16x16x32_bf16 v[96:99], a[28:31], a[212:215], v[96:99]\n" \
"buffer_load_dwordx4 v197, s[48:51], s76 offen offset:0 lds\n" \
"v_mfma_f32_16x16x32_bf16 v[100:103], a[24:27], a[216:219], v[100:103]\n" \
"ds_read_b64_tr_b16 a[54:55], v210 offset:15360\n" \
"v_mfma_f32_16x16x32_bf16 v[100:103], a[28:31], a[220:223], v[100:103]\n" \
"ds_read_b128 a[24:27], v205 offset:8192\n" \
"v_mfma_f32_16x16x32_bf16 v[108:111], a[192:195], a[216:219], v[108:111]\n" \
"s_add_u32 s77, s76, 24576\n" \
"ds_read_b128 a[28:31], v206 offset:8192\n" \
"v_mfma_f32_16x16x32_bf16 v[108:111], a[196:199], a[220:223], v[108:111]\n" \
"buffer_load_dwordx4 v198, s[48:51], s76 offen offset:1024 lds\n" \
"v_mfma_f32_16x16x32_bf16 v[104:107], a[192:195], a[208:211], v[104:107]\n" \
"v_mfma_f32_16x16x32_bf16 v[104:107], a[196:199], a[212:215], v[104:107]\n" \
"s_waitcnt lgkmcnt(2)\n" \
"v_mfma_f32_16x16x32_bf16 v[32:35], a[32:35], a[0:3], v[32:35]\n" \
"s_xor_b32 s80, s80, 0x3\n" \
"ds_read_b128 a[192:195], v205 offset:12288\n" \
"v_mfma_f32_16x16x32_bf16 v[32:35], a[36:39], a[4:7], v[32:35]\n" \
"buffer_load_dwordx4 v199, s[48:51], s76 offen offset:2048 lds\n" \
"v_mfma_f32_16x16x32_bf16 v[40:43], a[32:35], a[8:11], v[40:43]\n" \
"ds_read_b128 a[196:199], v206 offset:12288\n" \
"v_mfma_f32_16x16x32_bf16 v[40:43], a[36:39], a[12:15], v[40:43]\n" \
"ds_read_b64_tr_b16 a[200:201], v210 offset:1024\n" \
"v_mfma_f32_16x16x32_bf16 v[0:3], a[40:43], a[8:11], v[0:3]\n" \
"s_add_u32 m0, m0, 3072\n" \
"ds_read_b64_tr_b16 a[202:203], v210 offset:4096\n" \
"v_mfma_f32_16x16x32_bf16 v[0:3], a[44:47], a[12:15], v[0:3]\n" \
"buffer_load_dwordx4 v197, s[48:51], s77 offen offset:0 lds\n" \
"v_mfma_f32_16x16x32_bf16 v[4:7], a[48:51], a[8:11], v[4:7]\n" \
"ds_read_b64_tr_b16 a[204:205], v210 offset:13312\n" \
"v_mfma_f32_16x16x32_bf16 v[4:7], a[52:55], a[12:15], v[4:7]\n" \
"ds_read_b64_tr_b16 a[206:207], v210 offset:16384\n" \
"v_mfma_f32_16x16x32_bf16 v[12:15], a[48:51], a[16:19], v[12:15]\n" \
"ds_read_b64_tr_b16 a[208:209], v209 offset:2048\n" \
"v_mfma_f32_16x16x32_bf16 v[12:15], a[52:55], a[20:23], v[12:15]\n" \
"buffer_load_dwordx4 v198, s[48:51], s77 offen offset:1024 lds\n" \
"v_mfma_f32_16x16x32_bf16 v[8:11], a[40:43], a[16:19], v[8:11]\n" \
"ds_read_b64_tr_b16 a[210:211], v209 offset:5120\n" \
"v_mfma_f32_16x16x32_bf16 v[8:11], a[44:47], a[20:23], v[8:11]\n" \
"ds_read_b64_tr_b16 a[212:213], v209 offset:14336\n" \
"v_mfma_f32_16x16x32_bf16 v[48:51], a[32:35], a[16:19], v[48:51]\n" \
"s_waitcnt lgkmcnt(3)\n" \
"ds_read_b64_tr_b16 a[214:215], v209 offset:17408\n" \
"v_mfma_f32_16x16x32_bf16 v[48:51], a[36:39], a[20:23], v[48:51]\n" \
"buffer_load_dwordx4 v199, s[48:51], s77 offen offset:2048 lds\n" \
"v_mfma_f32_16x16x32_bf16 v[56:59], a[32:35], a[24:27], v[56:59]\n" \
"ds_read_b64_tr_b16 a[216:217], v210 offset:2048\n" \
"v_mfma_f32_16x16x32_bf16 v[56:59], a[36:39], a[28:31], v[56:59]\n" \
"ds_read_b64_tr_b16 a[218:219], v210 offset:5120\n" \
"v_mfma_f32_16x16x32_bf16 v[16:19], a[40:43], a[24:27], v[16:19]\n" \
"ds_read_b64_tr_b16 a[220:221], v210 offset:14336\n" \
"v_mfma_f32_16x16x32_bf16 v[16:19], a[44:47], a[28:31], v[16:19]\n" \
"v_mfma_f32_16x16x32_bf16 v[20:23], a[48:51], a[24:27], v[20:23]\n" \
"ds_read_b64_tr_b16 a[222:223], v210 offset:17408\n" \
"v_mfma_f32_16x16x32_bf16 v[20:23], a[52:55], a[28:31], v[20:23]\n" \
"v_mfma_f32_16x16x32_bf16 v[28:31], a[48:51], a[192:195], v[28:31]\n" \
"v_mfma_f32_16x16x32_bf16 v[28:31], a[52:55], a[196:199], v[28:31]\n" \
"v_mfma_f32_16x16x32_bf16 v[24:27], a[40:43], a[192:195], v[24:27]\n" \
"v_mfma_f32_16x16x32_bf16 v[24:27], a[44:47], a[196:199], v[24:27]\n" \
"v_mfma_f32_16x16x32_bf16 v[64:67], a[32:35], a[192:195], v[64:67]\n" \
"v_mfma_f32_16x16x32_bf16 v[64:67], a[36:39], a[196:199], v[64:67]\n" \
"s_waitcnt vmcnt(11) lgkmcnt(0)\n" \
"s_barrier\n" \
"s_movk_i32 s64, 0x207\n" \
"v_cmp_class_f32 vcc, v224, s64\n" \
"s_nop 1\n" \
"s_cmp_lg_u64 vcc, 0\n" \
"s_addc_u32 s90, s90, 0\n" \
"s_cmp_lg_u32 s90, 0\n" \
"s_cbranch_scc1 Lexact_%=\n" \
"Lexr_%=:\n" \
"v_mfma_f32_16x16x32_bf16 v[68:71], a[200:203], a[192:195], v[68:71]\n" \
"s_lshl_b32 s15, s63, 7\n" \
"ds_read_b64_tr_b16 a[32:33], v204 offset:17408\n" \
"ds_read_b64_tr_b16 a[34:35], v204 offset:20480\n" \
"v_mfma_f32_16x16x32_bf16 v[68:71], a[204:207], a[196:199], v[68:71]\n" \
"s_lshl2_add_u32 s16, s62, 65589248\n" \
"ds_read_b64_tr_b16 a[36:37], v204 offset:29696\n" \
"ds_read_b64_tr_b16 a[38:39], v204 offset:32768\n" \
"v_mfma_f32_16x16x32_bf16 v[60:63], a[200:203], a[24:27], v[60:63]\n" \
"s_add_u32 m0, s79, 65536\n" \
"s_sub_u32 s17, s16, s15\n" \
"ds_read_b64_tr_b16 a[40:41], v204 offset:16384\n" \
"v_mfma_f32_16x16x32_bf16 v[60:63], a[204:207], a[28:31], v[60:63]\n" \
"buffer_load_dwordx4 v192, s[44:47], s73 offen offset:0 lds\n" \
"v_mfma_f32_16x16x32_bf16 v[52:55], a[200:203], a[16:19], v[52:55]\n" \
"s_add_u32 s15, s15, s16\n" \
"s_add_u32 s17, s17, 1920\n" \
"ds_read_b64_tr_b16 a[42:43], v204 offset:19456\n" \
"v_mfma_f32_16x16x32_bf16 v[52:55], a[204:207], a[20:23], v[52:55]\n" \
"ds_read_b64_tr_b16 a[44:45], v204 offset:28672\n" \
"v_mov_b32 v146, s60\n" \
"v_mfma_f32_16x16x32_bf16 v[44:47], a[200:203], a[8:11], v[44:47]\n" \
"ds_read_b64_tr_b16 a[46:47], v204 offset:31744\n" \
"v_lshlrev_b32 v147, 28, s61\n" \
"v_mfma_f32_16x16x32_bf16 v[44:47], a[204:207], a[12:15], v[44:47]\n" \
"buffer_load_dwordx4 v193, s[44:47], s73 offen offset:1024 lds\n" \
"v_mfma_f32_16x16x32_bf16 v[72:75], a[0:3], a[208:211], v[72:75]\n" \
"ds_read_b64_tr_b16 a[48:49], v222 offset:16384\n" \
"v_mfma_f32_16x16x32_bf16 v[72:75], a[4:7], a[212:215], v[72:75]\n" \
"ds_read_b128 a[0:3], v202 offset:4096\n" \
"v_mfma_f32_16x16x32_bf16 v[80:83], a[8:11], a[208:211], v[80:83]\n" \
"ds_read_b128 a[4:7], v203 offset:4096\n" \
"v_mfma_f32_16x16x32_bf16 v[80:83], a[12:15], a[212:215], v[80:83]\n" \
"buffer_load_dwordx4 v192, s[44:47], s73 offen offset:2048 lds\n" \
"v_mfma_f32_16x16x32_bf16 v[84:87], a[8:11], a[216:219], v[84:87]\n" \
"ds_read_b64_tr_b16 a[50:51], v222 offset:19456\n" \
"v_mfma_f32_16x16x32_bf16 v[84:87], a[12:15], a[220:223], v[84:87]\n" \
"ds_read_b128 a[8:11], v200 offset:0\n" \
"v_mfma_f32_16x16x32_bf16 v[92:95], a[16:19], a[216:219], v[92:95]\n" \
"ds_read_b128 a[12:15], v201 offset:0\n" \
"v_mfma_f32_16x16x32_bf16 v[92:95], a[20:23], a[220:223], v[92:95]\n" \
"buffer_load_dwordx4 v193, s[44:47], s73 offen offset:3072 lds\n" \
"v_mfma_f32_16x16x32_bf16 v[88:91], a[16:19], a[208:211], v[88:91]\n" \
"s_xor_b32 s73, s73, 0x80\n" \
"s_mul_i32 s76, s80, 393216\n" \
"ds_read_b64_tr_b16 a[52:53], v222 offset:28672\n" \
"v_mfma_f32_16x16x32_bf16 v[88:91], a[20:23], a[212:215], v[88:91]\n" \
"s_add_u32 s76, s76, s78\n" \
"ds_read_b128 a[16:19], v200 offset:4096\n" \
"v_mfma_f32_16x16x32_bf16 v[96:99], a[24:27], a[208:211], v[96:99]\n" \
"s_add_u32 m0, s91, 81920\n" \
"ds_read_b128 a[20:23], v201 offset:4096\n" \
"v_mfma_f32_16x16x32_bf16 v[96:99], a[28:31], a[212:215], v[96:99]\n" \
"buffer_load_dwordx4 v197, s[48:51], s76 offen offset:0 lds\n" \
"v_mfma_f32_16x16x32_bf16 v[100:103], a[24:27], a[216:219], v[100:103]\n" \
"ds_read_b64_tr_b16 a[54:55], v222 offset:31744\n" \
"v_mfma_f32_16x16x32_bf16 v[100:103], a[28:31], a[220:223], v[100:103]\n" \
"ds_read_b128 a[24:27], v200 offset:8192\n" \
"v_mfma_f32_16x16x32_bf16 v[108:111], a[192:195], a[216:219], v[108:111]\n" \
"s_add_u32 s77, s76, 24576\n" \
"ds_read_b128 a[28:31], v201 offset:8192\n" \
"v_mfma_f32_16x16x32_bf16 v[108:111], a[196:199], a[220:223], v[108:111]\n" \
"buffer_load_dwordx4 v198, s[48:51], s76 offen offset:1024 lds\n" \
"v_mfma_f32_16x16x32_bf16 v[104:107], a[192:195], a[208:211], v[104:107]\n" \
"v_mfma_f32_16x16x32_bf16 v[104:107], a[196:199], a[212:215], v[104:107]\n" \
"s_waitcnt lgkmcnt(2)\n" \
"v_mfma_f32_16x16x32_bf16 v[32:35], a[32:35], a[0:3], v[32:35]\n" \
"s_xor_b32 s80, s80, 0x1\n" \
"ds_read_b128 a[192:195], v200 offset:12288\n" \
"v_mfma_f32_16x16x32_bf16 v[32:35], a[36:39], a[4:7], v[32:35]\n" \
"buffer_load_dwordx4 v199, s[48:51], s76 offen offset:2048 lds\n" \
"v_mfma_f32_16x16x32_bf16 v[40:43], a[32:35], a[8:11], v[40:43]\n" \
"ds_read_b128 a[196:199], v201 offset:12288\n" \
"v_mfma_f32_16x16x32_bf16 v[40:43], a[36:39], a[12:15], v[40:43]\n" \
"ds_read_b64_tr_b16 a[200:201], v222 offset:17408\n" \
"v_mfma_f32_16x16x32_bf16 v[0:3], a[40:43], a[8:11], v[0:3]\n" \
"s_add_u32 m0, m0, 3072\n" \
"ds_read_b64_tr_b16 a[202:203], v222 offset:20480\n" \
"v_mfma_f32_16x16x32_bf16 v[0:3], a[44:47], a[12:15], v[0:3]\n" \
"buffer_load_dwordx4 v197, s[48:51], s77 offen offset:0 lds\n" \
"v_mfma_f32_16x16x32_bf16 v[4:7], a[48:51], a[8:11], v[4:7]\n" \
"ds_read_b64_tr_b16 a[204:205], v222 offset:29696\n" \
"v_mfma_f32_16x16x32_bf16 v[4:7], a[52:55], a[12:15], v[4:7]\n" \
"ds_read_b64_tr_b16 a[206:207], v222 offset:32768\n" \
"v_mfma_f32_16x16x32_bf16 v[12:15], a[48:51], a[16:19], v[12:15]\n" \
"ds_read_b64_tr_b16 a[208:209], v204 offset:18432\n" \
"v_mfma_f32_16x16x32_bf16 v[12:15], a[52:55], a[20:23], v[12:15]\n" \
"buffer_load_dwordx4 v198, s[48:51], s77 offen offset:1024 lds\n" \
"v_mfma_f32_16x16x32_bf16 v[8:11], a[40:43], a[16:19], v[8:11]\n" \
"ds_read_b64_tr_b16 a[210:211], v204 offset:21504\n" \
"v_mfma_f32_16x16x32_bf16 v[8:11], a[44:47], a[20:23], v[8:11]\n" \
"ds_read_b64_tr_b16 a[212:213], v204 offset:30720\n" \
"v_mfma_f32_16x16x32_bf16 v[48:51], a[32:35], a[16:19], v[48:51]\n" \
"s_waitcnt lgkmcnt(3)\n" \
"ds_read_b64_tr_b16 a[214:215], v204 offset:33792\n" \
"v_mfma_f32_16x16x32_bf16 v[48:51], a[36:39], a[20:23], v[48:51]\n" \
"buffer_load_dwordx4 v199, s[48:51], s77 offen offset:2048 lds\n" \
"v_mfma_f32_16x16x32_bf16 v[56:59], a[32:35], a[24:27], v[56:59]\n" \
"ds_read_b64_tr_b16 a[216:217], v222 offset:18432\n" \
"v_mfma_f32_16x16x32_bf16 v[56:59], a[36:39], a[28:31], v[56:59]\n" \
"ds_read_b64_tr_b16 a[218:219], v222 offset:21504\n" \
"v_mfma_f32_16x16x32_bf16 v[16:19], a[40:43], a[24:27], v[16:19]\n" \
"ds_read_b64_tr_b16 a[220:221], v222 offset:30720\n" \
"v_mfma_f32_16x16x32_bf16 v[16:19], a[44:47], a[28:31], v[16:19]\n" \
"v_mfma_f32_16x16x32_bf16 v[20:23], a[48:51], a[24:27], v[20:23]\n" \
"ds_read_b64_tr_b16 a[222:223], v222 offset:33792\n" \
"v_mfma_f32_16x16x32_bf16 v[20:23], a[52:55], a[28:31], v[20:23]\n" \
"v_mfma_f32_16x16x32_bf16 v[28:31], a[48:51], a[192:195], v[28:31]\n" \
"v_mfma_f32_16x16x32_bf16 v[28:31], a[52:55], a[196:199], v[28:31]\n" \
"v_mfma_f32_16x16x32_bf16 v[24:27], a[40:43], a[192:195], v[24:27]\n" \
"v_mfma_f32_16x16x32_bf16 v[24:27], a[44:47], a[196:199], v[24:27]\n" \
"v_mfma_f32_16x16x32_bf16 v[64:67], a[32:35], a[192:195], v[64:67]\n" \
"v_mfma_f32_16x16x32_bf16 v[64:67], a[36:39], a[196:199], v[64:67]\n" \
"s_waitcnt vmcnt(10) lgkmcnt(0)\n" \
"s_barrier\n" \
"v_mfma_f32_16x16x32_bf16 v[68:71], a[200:203], a[192:195], v[68:71]\n" \
"ds_read_b64_tr_b16 a[32:33], v209 offset:25600\n" \
"ds_read_b64_tr_b16 a[34:35], v209 offset:28672\n" \
"v_mfma_f32_16x16x32_bf16 v[68:71], a[204:207], a[196:199], v[68:71]\n" \
"ds_read_b64_tr_b16 a[36:37], v209 offset:37888\n" \
"ds_read_b64_tr_b16 a[38:39], v209 offset:40960\n" \
"v_mfma_f32_16x16x32_bf16 v[60:63], a[200:203], a[24:27], v[60:63]\n" \
"s_add_u32 m0, s79, 0\n" \
"ds_read_b64_tr_b16 a[40:41], v209 offset:24576\n" \
"buffer_store_dword v146, v147, s[56:59], s15 offen sc1\n" \
"v_mfma_f32_16x16x32_bf16 v[60:63], a[204:207], a[28:31], v[60:63]\n" \
"buffer_load_dwordx4 v192, s[44:47], s73 offen offset:0 lds\n" \
"v_mfma_f32_16x16x32_bf16 v[52:55], a[200:203], a[16:19], v[52:55]\n" \
"ds_read_b64_tr_b16 a[42:43], v209 offset:27648\n" \
"v_mfma_f32_16x16x32_bf16 v[52:55], a[204:207], a[20:23], v[52:55]\n" \
"ds_read_b64_tr_b16 a[44:45], v209 offset:36864\n" \
"buffer_store_dword v146, v147, s[56:59], s17 offen sc1\n" \
"v_mfma_f32_16x16x32_bf16 v[44:47], a[200:203], a[8:11], v[44:47]\n" \
"s_mov_b64 s[36:37], s[18:19]\n" \
"ds_read_b64_tr_b16 a[46:47], v209 offset:39936\n" \
"v_mfma_f32_16x16x32_bf16 v[44:47], a[204:207], a[12:15], v[44:47]\n" \
"buffer_load_dwordx4 v193, s[44:47], s73 offen offset:1024 lds\n" \
"v_mfma_f32_16x16x32_bf16 v[72:75], a[0:3], a[208:211], v[72:75]\n" \
"s_sub_u32 s36, s36, 4096\n" \
"s_subb_u32 s37, s37, 0\n" \
"ds_read_b64_tr_b16 a[48:49], v210 offset:24576\n" \
"v_mfma_f32_16x16x32_bf16 v[72:75], a[4:7], a[212:215], v[72:75]\n" \
"s_and_b32 s37, s37, 0xffff\n" \
"s_mov_b32 s38, 8392704\n" \
"ds_read_b128 a[0:3], v202 offset:5120\n" \
"v_mfma_f32_16x16x32_bf16 v[80:83], a[8:11], a[208:211], v[80:83]\n" \
"s_mov_b32 s39, 0x20000\n" \
"s_mov_b64 s[40:41], s[24:25]\n" \
"ds_read_b128 a[4:7], v203 offset:5120\n" \
"v_mfma_f32_16x16x32_bf16 v[80:83], a[12:15], a[212:215], v[80:83]\n" \
"buffer_load_dwordx4 v192, s[44:47], s73 offen offset:2048 lds\n" \
"v_mfma_f32_16x16x32_bf16 v[84:87], a[8:11], a[216:219], v[84:87]\n" \
"s_sub_u32 s40, s40, 4096\n" \
"s_subb_u32 s41, s41, 0\n" \
"ds_read_b64_tr_b16 a[50:51], v210 offset:27648\n" \
"v_mfma_f32_16x16x32_bf16 v[84:87], a[12:15], a[220:223], v[84:87]\n" \
"s_and_b32 s41, s41, 0xffff\n" \
"s_mov_b32 s42, 2101248\n" \
"ds_read_b128 a[8:11], v200 offset:40960\n" \
"v_mfma_f32_16x16x32_bf16 v[92:95], a[16:19], a[216:219], v[92:95]\n" \
"s_mov_b32 s43, 0x20000\n" \
"s_mov_b64 s[52:53], s[26:27]\n" \
"ds_read_b128 a[12:15], v201 offset:40960\n" \
"v_mfma_f32_16x16x32_bf16 v[92:95], a[20:23], a[220:223], v[92:95]\n" \
"buffer_load_dwordx4 v193, s[44:47], s73 offen offset:3072 lds\n" \
"v_mfma_f32_16x16x32_bf16 v[88:91], a[16:19], a[208:211], v[88:91]\n" \
"s_xor_b32 s73, s73, 0x780\n" \
"s_mul_i32 s76, s80, 393216\n" \
"ds_read_b64_tr_b16 a[52:53], v210 offset:36864\n" \
"v_mfma_f32_16x16x32_bf16 v[88:91], a[20:23], a[212:215], v[88:91]\n" \
"s_add_u32 s76, s76, s78\n" \
"s_and_b32 s53, s53, 0xffff\n" \
"ds_read_b128 a[16:19], v200 offset:45056\n" \
"v_mfma_f32_16x16x32_bf16 v[96:99], a[24:27], a[208:211], v[96:99]\n" \
"s_add_u32 m0, s91, 16384\n" \
"s_mov_b32 s54, 8388608\n" \
"ds_read_b128 a[20:23], v201 offset:45056\n" \
"v_mfma_f32_16x16x32_bf16 v[96:99], a[28:31], a[212:215], v[96:99]\n" \
"buffer_load_dwordx4 v197, s[48:51], s76 offen offset:0 lds\n" \
"v_mfma_f32_16x16x32_bf16 v[100:103], a[24:27], a[216:219], v[100:103]\n" \
"s_mov_b32 s55, 0x20000\n" \
"ds_read_b64_tr_b16 a[54:55], v210 offset:39936\n" \
"v_lshrrev_b32 v221, 2, v234\n" \
"v_mfma_f32_16x16x32_bf16 v[100:103], a[28:31], a[220:223], v[100:103]\n" \
"s_waitcnt lgkmcnt(3)\n" \
"ds_read_b128 a[24:27], v200 offset:49152\n" \
"v_lshrrev_b32 v244, 4, v234\n" \
"v_mfma_f32_16x16x32_bf16 v[108:111], a[192:195], a[216:219], v[108:111]\n" \
"s_add_u32 s77, s76, 24576\n" \
"s_lshl_b32 s12, s94, 7\n" \
"ds_read_b128 a[28:31], v201 offset:49152\n" \
"v_mfma_f32_16x16x32_bf16 v[108:111], a[196:199], a[220:223], v[108:111]\n" \
"buffer_load_dwordx4 v198, s[48:51], s76 offen offset:1024 lds\n" \
"v_mfma_f32_16x16x32_bf16 v[104:107], a[192:195], a[208:211], v[104:107]\n" \
"s_add_u32 s12, s12, 65591296\n" \
"s_and_b32 s70, %1, 1\n" \
"s_lshl3_add_u32 s70, s70, s63\n" \
"v_mfma_f32_16x16x32_bf16 v[104:107], a[196:199], a[212:215], v[104:107]\n" \
"s_lshl2_add_u32 s12, s70, s12\n" \
"s_lshr_b32 s68, s63, 1\n" \
"s_add_u32 s68, s68, 2\n" \
"v_mfma_f32_16x16x32_bf16 v[32:35], a[32:35], a[0:3], v[32:35]\n" \
"s_xor_b32 s80, s80, 0xf\n" \
"s_lshr_b32 s70, s18, 8\n" \
"ds_read_b128 a[192:195], v200 offset:53248\n" \
"v_mfma_f32_16x16x32_bf16 v[32:35], a[36:39], a[4:7], v[32:35]\n" \
"buffer_load_dwordx4 v199, s[48:51], s76 offen offset:2048 lds\n" \
"v_mfma_f32_16x16x32_bf16 v[40:43], a[32:35], a[8:11], v[40:43]\n" \
"s_sub_u32 s68, s68, s70\n" \
"s_and_b32 s68, s68, 3\n" \
"ds_read_b128 a[196:199], v201 offset:53248\n" \
"v_mfma_f32_16x16x32_bf16 v[40:43], a[36:39], a[12:15], v[40:43]\n" \
"s_and_b32 s70, s63, 1\n" \
"s_lshl_b32 s70, s70, 2\n" \
"ds_read_b64_tr_b16 a[200:201], v210 offset:25600\n" \
"v_mfma_f32_16x16x32_bf16 v[0:3], a[40:43], a[8:11], v[0:3]\n" \
"s_add_u32 m0, m0, 3072\n" \
"s_or_b32 s68, s68, s70\n" \
"ds_read_b64_tr_b16 a[202:203], v210 offset:28672\n" \
"v_mfma_f32_16x16x32_bf16 v[0:3], a[44:47], a[12:15], v[0:3]\n" \
"s_waitcnt lgkmcnt(3)\n" \
"buffer_load_dwordx4 v197, s[48:51], s77 offen offset:0 lds\n" \
"v_mfma_f32_16x16x32_bf16 v[4:7], a[48:51], a[8:11], v[4:7]\n" \
"s_lshr_b32 s84, s61, 1\n" \
"s_lshl_b32 s84, s84, 16\n" \
"ds_read_b64_tr_b16 a[204:205], v210 offset:37888\n" \
"v_mfma_f32_16x16x32_bf16 v[4:7], a[52:55], a[12:15], v[4:7]\n" \
"s_and_b32 s70, s61, 1\n" \
"s_lshl_b32 s70, s70, 14\n" \
"ds_read_b64_tr_b16 a[206:207], v210 offset:40960\n" \
"v_mfma_f32_16x16x32_bf16 v[12:15], a[48:51], a[16:19], v[12:15]\n" \
"s_add_u32 s84, s84, s70\n" \
"s_lshl_b32 s88, s68, 8\n" \
"ds_read_b64_tr_b16 a[208:209], v209 offset:26624\n" \
"v_mfma_f32_16x16x32_bf16 v[12:15], a[52:55], a[20:23], v[12:15]\n" \
"buffer_load_dwordx4 v198, s[48:51], s77 offen offset:1024 lds\n" \
"v_mfma_f32_16x16x32_bf16 v[8:11], a[40:43], a[16:19], v[8:11]\n" \
"s_add_u32 s84, s84, s88\n" \
"ds_read_b64_tr_b16 a[210:211], v209 offset:29696\n" \
"v_bfe_u32 v245, v244, 2, 3\n" \
"v_mfma_f32_16x16x32_bf16 v[8:11], a[44:47], a[20:23], v[8:11]\n" \
"ds_read_b64_tr_b16 a[212:213], v209 offset:38912\n" \
"v_lshlrev_b32 v245, 11, v245\n" \
"v_mfma_f32_16x16x32_bf16 v[48:51], a[32:35], a[16:19], v[48:51]\n" \
"ds_read_b64_tr_b16 a[214:215], v209 offset:41984\n" \
"v_lshrrev_b32 v246, 5, v244\n" \
"v_mfma_f32_16x16x32_bf16 v[48:51], a[36:39], a[20:23], v[48:51]\n" \
"buffer_load_dwordx4 v199, s[48:51], s77 offen offset:2048 lds\n" \
"v_mfma_f32_16x16x32_bf16 v[56:59], a[32:35], a[24:27], v[56:59]\n" \
"ds_read_b64_tr_b16 a[216:217], v210 offset:26624\n" \
"v_lshl_add_u32 v245, v246, 6, v245\n" \
"v_mfma_f32_16x16x32_bf16 v[56:59], a[36:39], a[28:31], v[56:59]\n" \
"ds_read_b64_tr_b16 a[218:219], v210 offset:29696\n" \
"v_and_b32 v246, 3, v244\n" \
"v_mfma_f32_16x16x32_bf16 v[16:19], a[40:43], a[24:27], v[16:19]\n" \
"ds_read_b64_tr_b16 a[220:221], v210 offset:38912\n" \
"v_add_u32 v254, 3145728, v254\n" \
"v_mfma_f32_16x16x32_bf16 v[16:19], a[44:47], a[28:31], v[16:19]\n" \
"global_load_dword v253, v254, s[22:23]\n" \
"v_mfma_f32_16x16x32_bf16 v[20:23], a[48:51], a[24:27], v[20:23]\n" \
"ds_read_b64_tr_b16 a[222:223], v210 offset:41984\n" \
"v_bfe_u32 v247, v244, 4, 1\n" \
"v_mfma_f32_16x16x32_bf16 v[20:23], a[52:55], a[28:31], v[20:23]\n" \
"s_waitcnt lgkmcnt(3)\n" \
"v_lshlrev_b32 v247, 1, v247\n" \
"v_xor_b32 v246, v246, v247\n" \
"v_mfma_f32_16x16x32_bf16 v[28:31], a[48:51], a[192:195], v[28:31]\n" \
"v_lshl_add_u32 v245, v246, 4, v245\n" \
"v_add_u32 v216, 4096, v245\n" \
"v_mfma_f32_16x16x32_bf16 v[28:31], a[52:55], a[196:199], v[28:31]\n" \
"v_add_u32 v217, 3200, v245\n" \
"v_add_u32 v218, 34816, v245\n" \
"v_mfma_f32_16x16x32_bf16 v[24:27], a[40:43], a[192:195], v[24:27]\n" \
"v_add_u32 v219, 33920, v245\n" \
"s_mov_b32 s86, 65536\n" \
"v_mfma_f32_16x16x32_bf16 v[24:27], a[44:47], a[196:199], v[24:27]\n" \
"s_lshr_b32 s70, s61, 1\n" \
"s_lshl_b32 s71, s70, 13\n" \
"s_add_u32 s71, s71, %3\n" \
"v_mfma_f32_16x16x32_bf16 v[64:67], a[32:35], a[192:195], v[64:67]\n" \
"v_add_u32 v211, s71, v236\n" \
"s_lshl_b32 s70, s70, 18\n" \
"v_mfma_f32_16x16x32_bf16 v[64:67], a[36:39], a[196:199], v[64:67]\n" \
"s_add_u32 s88, s88, s70\n" \
"s_waitcnt vmcnt(13) lgkmcnt(0)\n" \
"s_barrier\n" \
"v_mfma_f32_16x16x32_bf16 v[68:71], a[200:203], a[192:195], v[68:71]\n" \
"s_and_b32 s70, s61, 1\n" \
"ds_read_b64_tr_b16 a[32:33], v209 offset:1024\n" \
"ds_read_b64_tr_b16 a[34:35], v209 offset:4096\n" \
"v_mfma_f32_16x16x32_bf16 v[68:71], a[204:207], a[196:199], v[68:71]\n" \
"s_lshl_b32 s71, s70, 7\n" \
"ds_read_b64_tr_b16 a[36:37], v209 offset:13312\n" \
"ds_read_b64_tr_b16 a[38:39], v209 offset:16384\n" \
"v_mfma_f32_16x16x32_bf16 v[60:63], a[200:203], a[24:27], v[60:63]\n" \
"s_add_u32 m0, s79, 40960\n" \
"s_add_u32 s88, s88, s71\n" \
"ds_read_b64_tr_b16 a[40:41], v209 offset:0\n" \
"v_mfma_f32_16x16x32_bf16 v[60:63], a[204:207], a[28:31], v[60:63]\n" \
"buffer_load_dwordx4 v192, s[44:47], s73 offen offset:0 lds\n" \
"v_mfma_f32_16x16x32_bf16 v[52:55], a[200:203], a[16:19], v[52:55]\n" \
"ds_read_b64_tr_b16 a[42:43], v209 offset:3072\n" \
"v_and_b32 v245, 3, v244\n" \
"v_mfma_f32_16x16x32_bf16 v[52:55], a[204:207], a[20:23], v[52:55]\n" \
"ds_read_b64_tr_b16 a[44:45], v209 offset:12288\n" \
"v_lshlrev_b32 v245, 3, v245\n" \
"v_mfma_f32_16x16x32_bf16 v[44:47], a[200:203], a[8:11], v[44:47]\n" \
"ds_read_b64_tr_b16 a[46:47], v209 offset:15360\n" \
"v_bfe_u32 v246, v244, 2, 2\n" \
"v_mfma_f32_16x16x32_bf16 v[44:47], a[204:207], a[12:15], v[44:47]\n" \
"buffer_load_dwordx4 v193, s[44:47], s73 offen offset:1024 lds\n" \
"v_mfma_f32_16x16x32_bf16 v[72:75], a[0:3], a[208:211], v[72:75]\n" \
"ds_read_b64_tr_b16 a[48:49], v210 offset:0\n" \
"v_lshl_add_u32 v245, v246, 6, v245\n" \
"v_mfma_f32_16x16x32_bf16 v[72:75], a[4:7], a[212:215], v[72:75]\n" \
"ds_read_b128 a[0:3], v202 offset:6144\n" \
"v_bfe_u32 v246, v244, 4, 1\n" \
"v_mfma_f32_16x16x32_bf16 v[80:83], a[8:11], a[208:211], v[80:83]\n" \
"ds_read_b128 a[4:7], v203 offset:6144\n" \
"v_lshl_add_u32 v245, v246, 5, v245\n" \
"v_mfma_f32_16x16x32_bf16 v[80:83], a[12:15], a[212:215], v[80:83]\n" \
"buffer_load_dwordx4 v192, s[44:47], s73 offen offset:2048 lds\n" \
"v_mfma_f32_16x16x32_bf16 v[84:87], a[8:11], a[216:219], v[84:87]\n" \
"ds_read_b64_tr_b16 a[50:51], v210 offset:3072\n" \
"v_lshl_add_u32 v245, v246, 8, v245\n" \
"v_mfma_f32_16x16x32_bf16 v[84:87], a[12:15], a[220:223], v[84:87]\n" \
"ds_read_b128 a[8:11], v205 offset:0\n" \
"v_lshrrev_b32 v246, 5, v244\n" \
"v_mfma_f32_16x16x32_bf16 v[92:95], a[16:19], a[216:219], v[92:95]\n" \
"ds_read_b128 a[12:15], v206 offset:0\n" \
"v_lshl_add_u32 v245, v246, 12, v245\n" \
"v_mfma_f32_16x16x32_bf16 v[92:95], a[20:23], a[220:223], v[92:95]\n" \
"buffer_load_dwordx4 v193, s[44:47], s73 offen offset:3072 lds\n" \
"v_mfma_f32_16x16x32_bf16 v[88:91], a[16:19], a[208:211], v[88:91]\n" \
"s_xor_b32 s73, s73, 0x80\n" \
"s_mul_i32 s76, s80, 393216\n" \
"ds_read_b64_tr_b16 a[52:53], v210 offset:12288\n" \
"v_mfma_f32_16x16x32_bf16 v[88:91], a[20:23], a[212:215], v[88:91]\n" \
"s_add_u32 s76, s76, s78\n" \
"s_lshl_b32 s71, s70, 10\n" \
"ds_read_b128 a[16:19], v205 offset:4096\n" \
"v_mfma_f32_16x16x32_bf16 v[96:99], a[24:27], a[208:211], v[96:99]\n" \
"s_add_u32 m0, s91, 106496\n" \
"s_add_u32 s71, s71, %3\n" \
"ds_read_b128 a[20:23], v206 offset:4096\n" \
"v_mfma_f32_16x16x32_bf16 v[96:99], a[28:31], a[212:215], v[96:99]\n" \
"buffer_load_dwordx4 v197, s[48:51], s76 offen offset:0 lds\n" \
"v_mfma_f32_16x16x32_bf16 v[100:103], a[24:27], a[216:219], v[100:103]\n" \
"ds_read_b64_tr_b16 a[54:55], v210 offset:15360\n" \
"v_add_u32 v212, s71, v245\n" \
"v_mfma_f32_16x16x32_bf16 v[100:103], a[28:31], a[220:223], v[100:103]\n" \
"s_mov_b32 s69, 1\n" \
"s_mov_b32 s30, 0x0\n" \
"ds_read_b128 a[24:27], v205 offset:8192\n" \
"v_mfma_f32_16x16x32_bf16 v[108:111], a[192:195], a[216:219], v[108:111]\n" \
"s_add_u32 s77, s76, 24576\n" \
"s_mov_b32 s85, 98304\n" \
"ds_read_b128 a[28:31], v206 offset:8192\n" \
"v_mfma_f32_16x16x32_bf16 v[108:111], a[196:199], a[220:223], v[108:111]\n" \
"buffer_load_dwordx4 v198, s[48:51], s76 offen offset:1024 lds\n" \
"v_mfma_f32_16x16x32_bf16 v[104:107], a[192:195], a[208:211], v[104:107]\n" \
"s_sub_u32 s85, s85, 32768\n" \
"s_cselect_b32 s85, 131072, s85\n" \
"v_add_u32 v213, s85, v211\n" \
"v_mfma_f32_16x16x32_bf16 v[104:107], a[196:199], a[212:215], v[104:107]\n" \
"s_waitcnt lgkmcnt(2)\n" \
"v_add_u32 v214, s85, v212\n" \
"v_xor_b32 v215, 32, v214\n" \
"v_mfma_f32_16x16x32_bf16 v[32:35], a[32:35], a[0:3], v[32:35]\n" \
"s_xor_b32 s80, s80, 0x1\n" \
"s_mov_b32 s13, 65593856\n" \
"ds_read_b128 a[192:195], v205 offset:12288\n" \
"v_mfma_f32_16x16x32_bf16 v[32:35], a[36:39], a[4:7], v[32:35]\n" \
"buffer_load_dwordx4 v199, s[48:51], s76 offen offset:2048 lds\n" \
"v_mfma_f32_16x16x32_bf16 v[40:43], a[32:35], a[8:11], v[40:43]\n" \
"ds_read_b128 a[196:199], v206 offset:12288\n" \
"v_lshrrev_b32 v228, 1, v234\n" \
"v_mfma_f32_16x16x32_bf16 v[40:43], a[36:39], a[12:15], v[40:43]\n" \
"ds_read_b64_tr_b16 a[200:201], v210 offset:1024\n" \
"v_mov_b32 v230, s60\n" \
"v_mfma_f32_16x16x32_bf16 v[0:3], a[40:43], a[8:11], v[0:3]\n" \
"s_add_u32 m0, m0, 3072\n" \
"ds_read_b64_tr_b16 a[202:203], v210 offset:4096\n" \
"v_lshlrev_b32 v231, 28, s61\n" \
"v_mfma_f32_16x16x32_bf16 v[0:3], a[44:47], a[12:15], v[0:3]\n" \
"buffer_load_dwordx4 v197, s[48:51], s77 offen offset:0 lds\n" \
"v_mfma_f32_16x16x32_bf16 v[4:7], a[48:51], a[8:11], v[4:7]\n" \
"ds_read_b64_tr_b16 a[204:205], v210 offset:13312\n" \
"v_mfma_f32_16x16x32_bf16 v[4:7], a[52:55], a[12:15], v[4:7]\n" \
"ds_read_b64_tr_b16 a[206:207], v210 offset:16384\n" \
"v_mfma_f32_16x16x32_bf16 v[12:15], a[48:51], a[16:19], v[12:15]\n" \
"ds_read_b64_tr_b16 a[208:209], v209 offset:2048\n" \
"v_mfma_f32_16x16x32_bf16 v[12:15], a[52:55], a[20:23], v[12:15]\n" \
"buffer_load_dwordx4 v198, s[48:51], s77 offen offset:1024 lds\n" \
"Lpg8488_%=:\n" \
"Lst1x1_%=:\n" \
"s_cbranch_execz Lpg10807_%=\n" \
"v_mfma_f32_16x16x32_bf16 v[8:11], a[40:43], a[16:19], v[8:11]\n" \
"ds_read_b64_tr_b16 a[210:211], v209 offset:5120\n" \
"v_mfma_f32_16x16x32_bf16 v[8:11], a[44:47], a[20:23], v[8:11]\n" \
"ds_read_b64_tr_b16 a[212:213], v209 offset:14336\n" \
"v_mfma_f32_16x16x32_bf16 v[48:51], a[32:35], a[16:19], v[48:51]\n" \
"s_waitcnt lgkmcnt(3)\n" \
"ds_read_b64_tr_b16 a[214:215], v209 offset:17408\n" \
"v_mfma_f32_16x16x32_bf16 v[48:51], a[36:39], a[20:23], v[48:51]\n" \
"buffer_load_dwordx4 v199, s[48:51], s77 offen offset:2048 lds\n" \
"v_mfma_f32_16x16x32_bf16 v[56:59], a[32:35], a[24:27], v[56:59]\n" \
"ds_read_b64_tr_b16 a[216:217], v210 offset:2048\n" \
"v_mfma_f32_16x16x32_bf16 v[56:59], a[36:39], a[28:31], v[56:59]\n" \
"ds_read_b64_tr_b16 a[218:219], v210 offset:5120\n" \
"v_mfma_f32_16x16x32_bf16 v[16:19], a[40:43], a[24:27], v[16:19]\n" \
"ds_read_b64_tr_b16 a[220:221], v210 offset:14336\n" \
"v_mfma_f32_16x16x32_bf16 v[16:19], a[44:47], a[28:31], v[16:19]\n" \
"v_mfma_f32_16x16x32_bf16 v[20:23], a[48:51], a[24:27], v[20:23]\n" \
"ds_read_b64_tr_b16 a[222:223], v210 offset:17408\n" \
"v_mfma_f32_16x16x32_bf16 v[20:23], a[52:55], a[28:31], v[20:23]\n" \
"v_mfma_f32_16x16x32_bf16 v[28:31], a[48:51], a[192:195], v[28:31]\n" \
"v_mfma_f32_16x16x32_bf16 v[28:31], a[52:55], a[196:199], v[28:31]\n" \
"buffer_load_dwordx2 v[226:227], v228, s[56:59], s13 offen sc1\n" \
"v_mfma_f32_16x16x32_bf16 v[24:27], a[40:43], a[192:195], v[24:27]\n" \
"v_mfma_f32_16x16x32_bf16 v[24:27], a[44:47], a[196:199], v[24:27]\n" \
"v_mfma_f32_16x16x32_bf16 v[64:67], a[32:35], a[192:195], v[64:67]\n" \
"v_mfma_f32_16x16x32_bf16 v[64:67], a[36:39], a[196:199], v[64:67]\n" \
"s_waitcnt vmcnt(12) lgkmcnt(0)\n" \
"s_barrier\n" \
"v_mfma_f32_16x16x32_bf16 v[68:71], a[200:203], a[192:195], v[68:71]\n" \
"ds_read_b64_tr_b16 a[32:33], v204 offset:17408\n" \
"ds_read_b64_tr_b16 a[34:35], v204 offset:20480\n" \
"v_mfma_f32_16x16x32_bf16 v[68:71], a[204:207], a[196:199], v[68:71]\n" \
"ds_read_b64_tr_b16 a[36:37], v204 offset:29696\n" \
"ds_read_b64_tr_b16 a[38:39], v204 offset:32768\n" \
"v_mfma_f32_16x16x32_bf16 v[60:63], a[200:203], a[24:27], v[60:63]\n" \
"s_add_u32 m0, s79, 65536\n" \
"ds_read_b64_tr_b16 a[40:41], v204 offset:16384\n" \
"v_mfma_f32_16x16x32_bf16 v[60:63], a[204:207], a[28:31], v[60:63]\n" \
"buffer_load_dwordx4 v192, s[44:47], s73 offen offset:0 lds\n" \
"v_mfma_f32_16x16x32_bf16 v[52:55], a[200:203], a[16:19], v[52:55]\n" \
"ds_read_b64_tr_b16 a[42:43], v204 offset:19456\n" \
"v_mfma_f32_16x16x32_bf16 v[52:55], a[204:207], a[20:23], v[52:55]\n" \
"ds_read_b64_tr_b16 a[44:45], v204 offset:28672\n" \
"v_mfma_f32_16x16x32_bf16 v[44:47], a[200:203], a[8:11], v[44:47]\n" \
"ds_read_b64_tr_b16 a[46:47], v204 offset:31744\n" \
"v_mfma_f32_16x16x32_bf16 v[44:47], a[204:207], a[12:15], v[44:47]\n" \
"buffer_load_dwordx4 v193, s[44:47], s73 offen offset:1024 lds\n" \
"v_mfma_f32_16x16x32_bf16 v[72:75], a[0:3], a[208:211], v[72:75]\n" \
"ds_read_b64_tr_b16 a[48:49], v222 offset:16384\n" \
"v_mfma_f32_16x16x32_bf16 v[72:75], a[4:7], a[212:215], v[72:75]\n" \
"ds_read_b128 a[0:3], v202 offset:7168\n" \
"v_mfma_f32_16x16x32_bf16 v[80:83], a[8:11], a[208:211], v[80:83]\n" \
"ds_read_b128 a[4:7], v203 offset:7168\n" \
"v_mfma_f32_16x16x32_bf16 v[80:83], a[12:15], a[212:215], v[80:83]\n" \
"buffer_load_dwordx4 v192, s[44:47], s73 offen offset:2048 lds\n" \
"v_mfma_f32_16x16x32_bf16 v[84:87], a[8:11], a[216:219], v[84:87]\n" \
"ds_read_b64_tr_b16 a[50:51], v222 offset:19456\n" \
"v_mfma_f32_16x16x32_bf16 v[84:87], a[12:15], a[220:223], v[84:87]\n" \
"ds_read_b128 a[8:11], v200 offset:0\n" \
"v_mfma_f32_16x16x32_bf16 v[92:95], a[16:19], a[216:219], v[92:95]\n" \
"ds_read_b128 a[12:15], v201 offset:0\n" \
"v_mfma_f32_16x16x32_bf16 v[92:95], a[20:23], a[220:223], v[92:95]\n" \
"buffer_load_dwordx4 v193, s[44:47], s73 offen offset:3072 lds\n" \
"v_mfma_f32_16x16x32_bf16 v[88:91], a[16:19], a[208:211], v[88:91]\n" \
"s_xor_b32 s73, s73, 0x180\n" \
"s_mul_i32 s76, s80, 393216\n" \
"ds_read_b64_tr_b16 a[52:53], v222 offset:28672\n" \
"v_mfma_f32_16x16x32_bf16 v[88:91], a[20:23], a[212:215], v[88:91]\n" \
"s_add_u32 s76, s76, s78\n" \
"ds_read_b128 a[16:19], v200 offset:4096\n" \
"v_mfma_f32_16x16x32_bf16 v[96:99], a[24:27], a[208:211], v[96:99]\n" \
"s_add_u32 m0, s91, 81920\n" \
"ds_read_b128 a[20:23], v201 offset:4096\n" \
"v_mfma_f32_16x16x32_bf16 v[96:99], a[28:31], a[212:215], v[96:99]\n" \
"buffer_load_dwordx4 v197, s[48:51], s76 offen offset:0 lds\n" \
"v_mfma_f32_16x16x32_bf16 v[100:103], a[24:27], a[216:219], v[100:103]\n" \
"ds_read_b64_tr_b16 a[54:55], v222 offset:31744\n" \
"v_mfma_f32_16x16x32_bf16 v[100:103], a[28:31], a[220:223], v[100:103]\n" \
"ds_read_b128 a[24:27], v200 offset:8192\n" \
"v_mfma_f32_16x16x32_bf16 v[108:111], a[192:195], a[216:219], v[108:111]\n" \
"s_add_u32 s77, s76, 24576\n" \
"ds_read_b128 a[28:31], v201 offset:8192\n" \
"v_mfma_f32_16x16x32_bf16 v[108:111], a[196:199], a[220:223], v[108:111]\n" \
"buffer_load_dwordx4 v198, s[48:51], s76 offen offset:1024 lds\n" \
"v_mfma_f32_16x16x32_bf16 v[104:107], a[192:195], a[208:211], v[104:107]\n" \
"v_mfma_f32_16x16x32_bf16 v[104:107], a[196:199], a[212:215], v[104:107]\n" \
"s_waitcnt lgkmcnt(2)\n" \
"v_mfma_f32_16x16x32_bf16 v[32:35], a[32:35], a[0:3], v[32:35]\n" \
"s_xor_b32 s80, s80, 0x3\n" \
"ds_read_b128 a[192:195], v200 offset:12288\n" \
"v_mfma_f32_16x16x32_bf16 v[32:35], a[36:39], a[4:7], v[32:35]\n" \
"buffer_load_dwordx4 v199, s[48:51], s76 offen offset:2048 lds\n" \
"v_mfma_f32_16x16x32_bf16 v[40:43], a[32:35], a[8:11], v[40:43]\n" \
"ds_read_b128 a[196:199], v201 offset:12288\n" \
"v_mfma_f32_16x16x32_bf16 v[40:43], a[36:39], a[12:15], v[40:43]\n" \
"ds_read_b64_tr_b16 a[200:201], v222 offset:17408\n" \
"v_mfma_f32_16x16x32_bf16 v[0:3], a[40:43], a[8:11], v[0:3]\n" \
"s_add_u32 m0, m0, 3072\n" \
"ds_read_b64_tr_b16 a[202:203], v222 offset:20480\n" \
"v_mfma_f32_16x16x32_bf16 v[0:3], a[44:47], a[12:15], v[0:3]\n" \
"buffer_load_dwordx4 v197, s[48:51], s77 offen offset:0 lds\n" \
"v_mfma_f32_16x16x32_bf16 v[4:7], a[48:51], a[8:11], v[4:7]\n" \
"ds_read_b64_tr_b16 a[204:205], v222 offset:29696\n" \
"v_mfma_f32_16x16x32_bf16 v[4:7], a[52:55], a[12:15], v[4:7]\n" \
"ds_read_b64_tr_b16 a[206:207], v222 offset:32768\n" \
"v_mfma_f32_16x16x32_bf16 v[12:15], a[48:51], a[16:19], v[12:15]\n" \
"ds_read_b64_tr_b16 a[208:209], v204 offset:18432\n" \
"v_mfma_f32_16x16x32_bf16 v[12:15], a[52:55], a[20:23], v[12:15]\n" \
"buffer_load_dwordx4 v198, s[48:51], s77 offen offset:1024 lds\n" \
"v_mfma_f32_16x16x32_bf16 v[8:11], a[40:43], a[16:19], v[8:11]\n" \
"ds_read_b64_tr_b16 a[210:211], v204 offset:21504\n" \
"v_mfma_f32_16x16x32_bf16 v[8:11], a[44:47], a[20:23], v[8:11]\n" \
"ds_read_b64_tr_b16 a[212:213], v204 offset:30720\n" \
"v_mfma_f32_16x16x32_bf16 v[48:51], a[32:35], a[16:19], v[48:51]\n" \
"s_waitcnt lgkmcnt(3)\n" \
"ds_read_b64_tr_b16 a[214:215], v204 offset:33792\n" \
"v_mfma_f32_16x16x32_bf16 v[48:51], a[36:39], a[20:23], v[48:51]\n" \
"buffer_load_dwordx4 v199, s[48:51], s77 offen offset:2048 lds\n" \
"v_mfma_f32_16x16x32_bf16 v[56:59], a[32:35], a[24:27], v[56:59]\n" \
"ds_read_b64_tr_b16 a[216:217], v222 offset:18432\n" \
"v_mfma_f32_16x16x32_bf16 v[56:59], a[36:39], a[28:31], v[56:59]\n" \
"ds_read_b64_tr_b16 a[218:219], v222 offset:21504\n" \
"v_mfma_f32_16x16x32_bf16 v[16:19], a[40:43], a[24:27], v[16:19]\n" \
"ds_read_b64_tr_b16 a[220:221], v222 offset:30720\n" \
"v_mfma_f32_16x16x32_bf16 v[16:19], a[44:47], a[28:31], v[16:19]\n" \
"v_mfma_f32_16x16x32_bf16 v[20:23], a[48:51], a[24:27], v[20:23]\n" \
"ds_read_b64_tr_b16 a[222:223], v222 offset:33792\n" \
"v_mfma_f32_16x16x32_bf16 v[20:23], a[52:55], a[28:31], v[20:23]\n" \
"v_mfma_f32_16x16x32_bf16 v[28:31], a[48:51], a[192:195], v[28:31]\n" \
"v_mfma_f32_16x16x32_bf16 v[28:31], a[52:55], a[196:199], v[28:31]\n" \
"v_mfma_f32_16x16x32_bf16 v[24:27], a[40:43], a[192:195], v[24:27]\n" \
"v_mfma_f32_16x16x32_bf16 v[24:27], a[44:47], a[196:199], v[24:27]\n" \
"v_mfma_f32_16x16x32_bf16 v[64:67], a[32:35], a[192:195], v[64:67]\n" \
"v_mfma_f32_16x16x32_bf16 v[64:67], a[36:39], a[196:199], v[64:67]\n" \
"s_waitcnt vmcnt(11) lgkmcnt(0)\n" \
"s_barrier\n" \
"v_mfma_f32_16x16x32_bf16 v[68:71], a[200:203], a[192:195], v[68:71]\n" \
"ds_read_b64_tr_b16 a[32:33], v209 offset:25600\n" \
"ds_read_b64_tr_b16 a[34:35], v209 offset:28672\n" \
"v_mfma_f32_16x16x32_bf16 v[68:71], a[204:207], a[196:199], v[68:71]\n" \
"ds_read_b64_tr_b16 a[36:37], v209 offset:37888\n" \
"ds_read_b64_tr_b16 a[38:39], v209 offset:40960\n" \
"v_mfma_f32_16x16x32_bf16 v[60:63], a[200:203], a[24:27], v[60:63]\n" \
"s_add_u32 m0, s79, 0\n" \
"ds_read_b64_tr_b16 a[40:41], v209 offset:24576\n" \
"v_mfma_f32_16x16x32_bf16 v[60:63], a[204:207], a[28:31], v[60:63]\n" \
"buffer_load_dwordx4 v192, s[44:47], s73 offen offset:0 lds\n" \
"v_mfma_f32_16x16x32_bf16 v[52:55], a[200:203], a[16:19], v[52:55]\n" \
"ds_read_b64_tr_b16 a[42:43], v209 offset:27648\n" \
"v_mfma_f32_16x16x32_bf16 v[52:55], a[204:207], a[20:23], v[52:55]\n" \
"ds_read_b64_tr_b16 a[44:45], v209 offset:36864\n" \
"v_mfma_f32_16x16x32_bf16 v[44:47], a[200:203], a[8:11], v[44:47]\n" \
"ds_read_b64_tr_b16 a[46:47], v209 offset:39936\n" \
"v_mfma_f32_16x16x32_bf16 v[44:47], a[204:207], a[12:15], v[44:47]\n" \
"buffer_load_dwordx4 v193, s[44:47], s73 offen offset:1024 lds\n" \
"v_mfma_f32_16x16x32_bf16 v[72:75], a[0:3], a[208:211], v[72:75]\n" \
"ds_read_b64_tr_b16 a[48:49], v210 offset:24576\n" \
"v_mfma_f32_16x16x32_bf16 v[72:75], a[4:7], a[212:215], v[72:75]\n" \
"ds_read_b128 a[0:3], v202 offset:16384\n" \
"v_mfma_f32_16x16x32_bf16 v[80:83], a[8:11], a[208:211], v[80:83]\n" \
"ds_read_b128 a[4:7], v203 offset:16384\n" \
"v_mfma_f32_16x16x32_bf16 v[80:83], a[12:15], a[212:215], v[80:83]\n" \
"buffer_load_dwordx4 v192, s[44:47], s73 offen offset:2048 lds\n" \
"v_mfma_f32_16x16x32_bf16 v[84:87], a[8:11], a[216:219], v[84:87]\n" \
"ds_read_b64_tr_b16 a[50:51], v210 offset:27648\n" \
"v_mfma_f32_16x16x32_bf16 v[84:87], a[12:15], a[220:223], v[84:87]\n" \
"ds_read_b128 a[8:11], v200 offset:40960\n" \
"v_mfma_f32_16x16x32_bf16 v[92:95], a[16:19], a[216:219], v[92:95]\n" \
"ds_read_b128 a[12:15], v201 offset:40960\n" \
"v_mfma_f32_16x16x32_bf16 v[92:95], a[20:23], a[220:223], v[92:95]\n" \
"buffer_load_dwordx4 v193, s[44:47], s73 offen offset:3072 lds\n" \
"v_mfma_f32_16x16x32_bf16 v[88:91], a[16:19], a[208:211], v[88:91]\n" \
"s_xor_b32 s73, s73, 0x300\n" \
"s_mul_i32 s76, s80, 393216\n" \
"ds_read_b64_tr_b16 a[52:53], v210 offset:36864\n" \
"v_mfma_f32_16x16x32_bf16 v[88:91], a[20:23], a[212:215], v[88:91]\n" \
"s_add_u32 s76, s76, s78\n" \
"ds_read_b128 a[16:19], v200 offset:45056\n" \
"v_mfma_f32_16x16x32_bf16 v[96:99], a[24:27], a[208:211], v[96:99]\n" \
"s_add_u32 m0, s91, 16384\n" \
"ds_read_b128 a[20:23], v201 offset:45056\n" \
"v_mfma_f32_16x16x32_bf16 v[96:99], a[28:31], a[212:215], v[96:99]\n" \
"buffer_load_dwordx4 v197, s[48:51], s76 offen offset:0 lds\n" \
"v_mfma_f32_16x16x32_bf16 v[100:103], a[24:27], a[216:219], v[100:103]\n" \
"ds_read_b64_tr_b16 a[54:55], v210 offset:39936\n" \
"v_mfma_f32_16x16x32_bf16 v[100:103], a[28:31], a[220:223], v[100:103]\n" \
"ds_read_b128 a[24:27], v200 offset:49152\n" \
"v_mfma_f32_16x16x32_bf16 v[108:111], a[192:195], a[216:219], v[108:111]\n" \
"s_add_u32 s77, s76, 24576\n" \
"ds_read_b128 a[28:31], v201 offset:49152\n" \
"v_mfma_f32_16x16x32_bf16 v[108:111], a[196:199], a[220:223], v[108:111]\n" \
"buffer_load_dwordx4 v198, s[48:51], s76 offen offset:1024 lds\n" \
"v_mfma_f32_16x16x32_bf16 v[104:107], a[192:195], a[208:211], v[104:107]\n" \
"v_mfma_f32_16x16x32_bf16 v[104:107], a[196:199], a[212:215], v[104:107]\n" \
"s_waitcnt lgkmcnt(2)\n" \
"v_mfma_f32_16x16x32_bf16 v[32:35], a[32:35], a[0:3], v[32:35]\n" \
"s_xor_b32 s80, s80, 0x6\n" \
"ds_read_b128 a[192:195], v200 offset:53248\n" \
"v_mfma_f32_16x16x32_bf16 v[32:35], a[36:39], a[4:7], v[32:35]\n" \
"buffer_load_dwordx4 v199, s[48:51], s76 offen offset:2048 lds\n" \
"v_mfma_f32_16x16x32_bf16 v[40:43], a[32:35], a[8:11], v[40:43]\n" \
"ds_read_b128 a[196:199], v201 offset:53248\n" \
"v_mfma_f32_16x16x32_bf16 v[40:43], a[36:39], a[12:15], v[40:43]\n" \
"ds_read_b64_tr_b16 a[200:201], v210 offset:25600\n" \
"v_mfma_f32_16x16x32_bf16 v[0:3], a[40:43], a[8:11], v[0:3]\n" \
"s_add_u32 m0, m0, 3072\n" \
"ds_read_b64_tr_b16 a[202:203], v210 offset:28672\n" \
"v_mfma_f32_16x16x32_bf16 v[0:3], a[44:47], a[12:15], v[0:3]\n" \
"buffer_load_dwordx4 v197, s[48:51], s77 offen offset:0 lds\n" \
"v_mfma_f32_16x16x32_bf16 v[4:7], a[48:51], a[8:11], v[4:7]\n" \
"ds_read_b64_tr_b16 a[204:205], v210 offset:37888\n" \
"v_mfma_f32_16x16x32_bf16 v[4:7], a[52:55], a[12:15], v[4:7]\n" \
"ds_read_b64_tr_b16 a[206:207], v210 offset:40960\n" \
"v_mfma_f32_16x16x32_bf16 v[12:15], a[48:51], a[16:19], v[12:15]\n" \
"ds_read_b64_tr_b16 a[208:209], v209 offset:26624\n" \
"v_mfma_f32_16x16x32_bf16 v[12:15], a[52:55], a[20:23], v[12:15]\n" \
"buffer_load_dwordx4 v198, s[48:51], s77 offen offset:1024 lds\n" \
"v_mfma_f32_16x16x32_bf16 v[8:11], a[40:43], a[16:19], v[8:11]\n" \
"ds_read_b64_tr_b16 a[210:211], v209 offset:29696\n" \
"v_mfma_f32_16x16x32_bf16 v[8:11], a[44:47], a[20:23], v[8:11]\n" \
"ds_read_b64_tr_b16 a[212:213], v209 offset:38912\n" \
"v_mfma_f32_16x16x32_bf16 v[48:51], a[32:35], a[16:19], v[48:51]\n" \
"s_waitcnt lgkmcnt(3)\n" \
"ds_read_b64_tr_b16 a[214:215], v209 offset:41984\n" \
"v_mfma_f32_16x16x32_bf16 v[48:51], a[36:39], a[20:23], v[48:51]\n" \
"buffer_load_dwordx4 v199, s[48:51], s77 offen offset:2048 lds\n" \
"v_mfma_f32_16x16x32_bf16 v[56:59], a[32:35], a[24:27], v[56:59]\n" \
"ds_read_b64_tr_b16 a[216:217], v210 offset:26624\n" \
"v_mfma_f32_16x16x32_bf16 v[56:59], a[36:39], a[28:31], v[56:59]\n" \
"ds_read_b64_tr_b16 a[218:219], v210 offset:29696\n" \
"v_mfma_f32_16x16x32_bf16 v[16:19], a[40:43], a[24:27], v[16:19]\n" \
"ds_read_b64_tr_b16 a[220:221], v210 offset:38912\n" \
"v_mfma_f32_16x16x32_bf16 v[16:19], a[44:47], a[28:31], v[16:19]\n" \
"v_mfma_f32_16x16x32_bf16 v[20:23], a[48:51], a[24:27], v[20:23]\n" \
"ds_read_b64_tr_b16 a[222:223], v210 offset:41984\n" \
"v_mfma_f32_16x16x32_bf16 v[20:23], a[52:55], a[28:31], v[20:23]\n" \
"v_mfma_f32_16x16x32_bf16 v[28:31], a[48:51], a[192:195], v[28:31]\n" \
"v_mfma_f32_16x16x32_bf16 v[28:31], a[52:55], a[196:199], v[28:31]\n" \
"v_mfma_f32_16x16x32_bf16 v[24:27], a[40:43], a[192:195], v[24:27]\n" \
"v_mfma_f32_16x16x32_bf16 v[24:27], a[44:47], a[196:199], v[24:27]\n" \
"v_mfma_f32_16x16x32_bf16 v[64:67], a[32:35], a[192:195], v[64:67]\n" \
"v_mfma_f32_16x16x32_bf16 v[64:67], a[36:39], a[196:199], v[64:67]\n" \
"s_waitcnt vmcnt(10) lgkmcnt(0)\n" \
"s_barrier\n" \
"v_mfma_f32_16x16x32_bf16 v[68:71], a[200:203], a[192:195], v[68:71]\n" \
"ds_read_b64_tr_b16 a[32:33], v209 offset:1024\n" \
"ds_read_b64_tr_b16 a[34:35], v209 offset:4096\n" \
"v_mfma_f32_16x16x32_bf16 v[68:71], a[204:207], a[196:199], v[68:71]\n" \
"ds_read_b64_tr_b16 a[36:37], v209 offset:13312\n" \
"ds_read_b64_tr_b16 a[38:39], v209 offset:16384\n" \
"v_mfma_f32_16x16x32_bf16 v[60:63], a[200:203], a[24:27], v[60:63]\n" \
"s_add_u32 m0, s79, 40960\n" \
"ds_read_b64_tr_b16 a[40:41], v209 offset:0\n" \
"v_cmp_eq_u32 vcc, s60, v226\n" \
"v_mfma_f32_16x16x32_bf16 v[60:63], a[204:207], a[28:31], v[60:63]\n" \
"buffer_load_dwordx4 v192, s[44:47], s73 offen offset:0 lds\n" \
"v_mfma_f32_16x16x32_bf16 v[52:55], a[200:203], a[16:19], v[52:55]\n" \
"s_andn2_b32 s70, 0xff, vcc_lo\n" \
"s_nop 0\n" \
"s_cbranch_scc1 Lpl4_%=\n" \
"Lpr4_%=:\n" \
"ds_read_b64_tr_b16 a[42:43], v209 offset:3072\n" \
"v_mfma_f32_16x16x32_bf16 v[52:55], a[204:207], a[20:23], v[52:55]\n" \
"s_and_b32 s70, %1, 7\n" \
"ds_read_b64_tr_b16 a[44:45], v209 offset:12288\n" \
"v_readlane_b32 s70, v227, s70\n" \
"v_mfma_f32_16x16x32_bf16 v[44:47], a[200:203], a[8:11], v[44:47]\n" \
"ds_read_b64_tr_b16 a[46:47], v209 offset:15360\n" \
"v_mfma_f32_16x16x32_bf16 v[44:47], a[204:207], a[12:15], v[44:47]\n" \
"buffer_load_dwordx4 v193, s[44:47], s73 offen offset:1024 lds\n" \
"v_mfma_f32_16x16x32_bf16 v[72:75], a[0:3], a[208:211], v[72:75]\n" \
"ds_read_b64_tr_b16 a[48:49], v210 offset:0\n" \
"v_subrev_u32 v227, s70, v227\n" \
"v_mfma_f32_16x16x32_bf16 v[72:75], a[4:7], a[212:215], v[72:75]\n" \
"ds_read_b128 a[0:3], v202 offset:17408\n" \
"v_cmp_gt_i32 vcc, 0, v227\n" \
"v_mfma_f32_16x16x32_bf16 v[80:83], a[8:11], a[208:211], v[80:83]\n" \
"ds_read_b128 a[4:7], v203 offset:17408\n" \
"v_mfma_f32_16x16x32_bf16 v[80:83], a[12:15], a[212:215], v[80:83]\n" \
"buffer_load_dwordx4 v192, s[44:47], s73 offen offset:2048 lds\n" \
"v_mfma_f32_16x16x32_bf16 v[84:87], a[8:11], a[216:219], v[84:87]\n" \
"s_and_b32 s13, vcc_lo, 0xff\n" \
"s_bcnt1_i32_b32 s13, s13\n" \
"ds_read_b64_tr_b16 a[50:51], v210 offset:3072\n" \
"v_mfma_f32_16x16x32_bf16 v[84:87], a[12:15], a[220:223], v[84:87]\n" \
"s_lshr_b32 s90, s13, 2\n" \
"s_mul_i32 s70, s90, 7\n" \
"ds_read_b128 a[8:11], v205 offset:0\n" \
"v_mfma_f32_16x16x32_bf16 v[92:95], a[16:19], a[216:219], v[92:95]\n" \
"s_xor_b32 s13, s13, s70\n" \
"s_lshl_b32 s70, s13, 3\n" \
"ds_read_b128 a[12:15], v206 offset:0\n" \
"v_mfma_f32_16x16x32_bf16 v[92:95], a[20:23], a[220:223], v[92:95]\n" \
"buffer_load_dwordx4 v193, s[44:47], s73 offen offset:3072 lds\n" \
"v_mfma_f32_16x16x32_bf16 v[88:91], a[16:19], a[208:211], v[88:91]\n" \
"s_xor_b32 s73, s73, 0x80\n" \
"s_mul_i32 s76, s80, 393216\n" \
"ds_read_b64_tr_b16 a[52:53], v210 offset:12288\n" \
"v_mfma_f32_16x16x32_bf16 v[88:91], a[20:23], a[212:215], v[88:91]\n" \
"s_add_u32 s76, s76, s78\n" \
"s_lshr_b32 s9, 0x2a2b2d2f, s70\n" \
"ds_read_b128 a[16:19], v205 offset:4096\n" \
"v_mfma_f32_16x16x32_bf16 v[96:99], a[24:27], a[208:211], v[96:99]\n" \
"s_add_u32 m0, s91, 106496\n" \
"s_and_b32 s9, s9, 0xff\n" \
"ds_read_b128 a[20:23], v206 offset:4096\n" \
"v_mfma_f32_16x16x32_bf16 v[96:99], a[28:31], a[212:215], v[96:99]\n" \
"buffer_load_dwordx4 v197, s[48:51], s76 offen offset:0 lds\n" \
"v_mfma_f32_16x16x32_bf16 v[100:103], a[24:27], a[216:219], v[100:103]\n" \
"s_lshr_b32 s8, %1, 6\n" \
"s_lshl2_add_u32 s8, s13, s8\n" \
"ds_read_b64_tr_b16 a[54:55], v210 offset:15360\n" \
"v_mfma_f32_16x16x32_bf16 v[100:103], a[28:31], a[220:223], v[100:103]\n" \
"ds_read_b128 a[24:27], v205 offset:8192\n" \
"v_max_i32_dpp v226, v227, v227 quad_perm:[1,0,3,2] row_mask:0xf bank_mask:0xf\n" \
"v_mfma_f32_16x16x32_bf16 v[108:111], a[192:195], a[216:219], v[108:111]\n" \
"s_add_u32 s77, s76, 24576\n" \
"s_mov_b32 s14, 0\n" \
"ds_read_b128 a[28:31], v206 offset:8192\n" \
"v_mfma_f32_16x16x32_bf16 v[108:111], a[196:199], a[220:223], v[108:111]\n" \
"buffer_load_dwordx4 v198, s[48:51], s76 offen offset:1024 lds\n" \
"v_mfma_f32_16x16x32_bf16 v[104:107], a[192:195], a[208:211], v[104:107]\n" \
"v_readlane_b32 s15, v226, 2\n" \
"v_readlane_b32 s16, v226, 4\n" \
"v_mfma_f32_16x16x32_bf16 v[104:107], a[196:199], a[212:215], v[104:107]\n" \
"s_waitcnt lgkmcnt(2)\n" \
"v_readlane_b32 s17, v226, 6\n" \
"v_cmp_gt_i32 vcc, s15, v226\n" \
"v_mfma_f32_16x16x32_bf16 v[32:35], a[32:35], a[0:3], v[32:35]\n" \
"s_xor_b32 s80, s80, 0x1\n" \
"ds_read_b128 a[192:195], v205 offset:12288\n" \
"v_mfma_f32_16x16x32_bf16 v[32:35], a[36:39], a[4:7], v[32:35]\n" \
"buffer_load_dwordx4 v199, s[48:51], s76 offen offset:2048 lds\n" \
"v_mfma_f32_16x16x32_bf16 v[40:43], a[32:35], a[8:11], v[40:43]\n" \
"s_and_b32 s70, vcc_lo, 0x55\n" \
"s_bcnt1_i32_b32 s70, s70\n" \
"ds_read_b128 a[196:199], v206 offset:12288\n" \
"v_mfma_f32_16x16x32_bf16 v[40:43], a[36:39], a[12:15], v[40:43]\n" \
"s_cmp_eq_u32 s70, s13\n" \
"s_cselect_b32 s14, 4, s14\n" \
"ds_read_b64_tr_b16 a[200:201], v210 offset:1024\n" \
"v_mfma_f32_16x16x32_bf16 v[0:3], a[40:43], a[8:11], v[0:3]\n" \
"s_add_u32 m0, m0, 3072\n" \
"ds_read_b64_tr_b16 a[202:203], v210 offset:4096\n" \
"v_cmp_gt_i32 vcc, s16, v226\n" \
"v_mfma_f32_16x16x32_bf16 v[0:3], a[44:47], a[12:15], v[0:3]\n" \
"buffer_load_dwordx4 v197, s[48:51], s77 offen offset:0 lds\n" \
"v_mfma_f32_16x16x32_bf16 v[4:7], a[48:51], a[8:11], v[4:7]\n" \
"s_and_b32 s70, vcc_lo, 0x55\n" \
"s_bcnt1_i32_b32 s70, s70\n" \
"ds_read_b64_tr_b16 a[204:205], v210 offset:13312\n" \
"v_mfma_f32_16x16x32_bf16 v[4:7], a[52:55], a[12:15], v[4:7]\n" \
"s_cmp_eq_u32 s70, s13\n" \
"s_cselect_b32 s14, 8, s14\n" \
"ds_read_b64_tr_b16 a[206:207], v210 offset:16384\n" \
"v_mfma_f32_16x16x32_bf16 v[12:15], a[48:51], a[16:19], v[12:15]\n" \
"ds_read_b64_tr_b16 a[208:209], v209 offset:2048\n" \
"v_cmp_gt_i32 vcc, s17, v226\n" \
"v_mfma_f32_16x16x32_bf16 v[12:15], a[52:55], a[20:23], v[12:15]\n" \
"buffer_load_dwordx4 v198, s[48:51], s77 offen offset:1024 lds\n" \
"v_mfma_f32_16x16x32_bf16 v[8:11], a[40:43], a[16:19], v[8:11]\n" \
"s_and_b32 s70, vcc_lo, 0x55\n" \
"s_bcnt1_i32_b32 s70, s70\n" \
"ds_read_b64_tr_b16 a[210:211], v209 offset:5120\n" \
"v_mfma_f32_16x16x32_bf16 v[8:11], a[44:47], a[20:23], v[8:11]\n" \
"s_cmp_eq_u32 s70, s13\n" \
"s_cselect_b32 s14, 12, s14\n" \
"ds_read_b64_tr_b16 a[212:213], v209 offset:14336\n" \
"v_mfma_f32_16x16x32_bf16 v[48:51], a[32:35], a[16:19], v[48:51]\n" \
"s_lshr_b32 s70, %1, 6\n" \
"s_add_u32 s14, s14, s70\n" \
"ds_read_b64_tr_b16 a[214:215], v209 offset:17408\n" \
"v_mfma_f32_16x16x32_bf16 v[48:51], a[36:39], a[20:23], v[48:51]\n" \
"s_waitcnt lgkmcnt(3)\n" \
"buffer_load_dwordx4 v199, s[48:51], s77 offen offset:2048 lds\n" \
"v_mfma_f32_16x16x32_bf16 v[56:59], a[32:35], a[24:27], v[56:59]\n" \
"s_cmp_eq_u32 s90, 0\n" \
"s_cselect_b32 s14, s8, s14\n" \
"ds_read_b64_tr_b16 a[216:217], v210 offset:2048\n" \
"v_mfma_f32_16x16x32_bf16 v[56:59], a[36:39], a[28:31], v[56:59]\n" \
"s_sub_u32 s10, 49, s9\n" \
"s_lshl_b32 s11, s9, 1\n" \
"ds_read_b64_tr_b16 a[218:219], v210 offset:5120\n" \
"v_mfma_f32_16x16x32_bf16 v[16:19], a[40:43], a[24:27], v[16:19]\n" \
"s_sub_u32 s11, s11, 71\n" \
"s_lshl2_add_u32 s70, s8, s61\n" \
"ds_read_b64_tr_b16 a[220:221], v210 offset:14336\n" \
"v_mfma_f32_16x16x32_bf16 v[16:19], a[44:47], a[28:31], v[16:19]\n" \
"s_mul_i32 s81, s70, 266240\n" \
"s_mul_i32 s82, s90, s9\n" \
"s_lshl_b32 s71, s82, 12\n" \
"v_mfma_f32_16x16x32_bf16 v[20:23], a[48:51], a[24:27], v[20:23]\n" \
"s_add_u32 s81, s81, s71\n" \
"s_add_u32 s81, s81, 35700736\n" \
"ds_read_b64_tr_b16 a[222:223], v210 offset:17408\n" \
"v_mfma_f32_16x16x32_bf16 v[20:23], a[52:55], a[28:31], v[20:23]\n" \
"s_lshl2_add_u32 s70, s14, s61\n" \
"s_mul_i32 s83, s70, 69632\n" \
"s_add_u32 s83, s83, 52740096\n" \
"v_mfma_f32_16x16x32_bf16 v[28:31], a[48:51], a[192:195], v[28:31]\n" \
"s_lshl_b32 s82, s82, 17\n" \
"s_add_u32 s82, s82, s84\n" \
"s_lshl_b32 s70, s14, 19\n" \
"v_mfma_f32_16x16x32_bf16 v[28:31], a[52:55], a[196:199], v[28:31]\n" \
"s_add_u32 s88, s88, s70\n" \
"s_lshl_b32 s71, s90, 11\n" \
"s_add_u32 s88, s88, s71\n" \
"v_mfma_f32_16x16x32_bf16 v[24:27], a[40:43], a[192:195], v[24:27]\n" \
"s_lshl3_add_u32 s70, s8, s68\n" \
"s_lshl2_add_u32 s87, s70, s61\n" \
"s_lshl_b32 s87, s87, 14\n" \
"v_mfma_f32_16x16x32_bf16 v[24:27], a[44:47], a[196:199], v[24:27]\n" \
"s_add_u32 s87, s87, 57196544\n" \
"s_lshl_b32 s64, s70, 7\n" \
"s_add_u32 s64, s64, 65601536\n" \
"v_mfma_f32_16x16x32_bf16 v[64:67], a[32:35], a[192:195], v[64:67]\n" \
"s_lshl_b32 s66, s8, 7\n" \
"s_lshl_b32 s67, s14, 7\n" \
"s_add_u32 s67, s67, 65591296\n" \
"v_mfma_f32_16x16x32_bf16 v[64:67], a[36:39], a[196:199], v[64:67]\n" \
"s_add_u32 s66, s66, 65589248\n" \
"s_waitcnt vmcnt(10) lgkmcnt(0)\n" \
"s_barrier\n" \
"v_mfma_f32_16x16x32_bf16 v[68:71], a[200:203], a[192:195], v[68:71]\n" \
"s_add_u32 s70, s9, 1\n" \
"ds_read_b64_tr_b16 a[32:33], v204 offset:17408\n" \
"ds_read_b64_tr_b16 a[34:35], v204 offset:20480\n" \
"v_mfma_f32_16x16x32_bf16 v[68:71], a[204:207], a[196:199], v[68:71]\n" \
"s_sub_u32 s70, s70, s90\n" \
"ds_read_b64_tr_b16 a[36:37], v204 offset:29696\n" \
"ds_read_b64_tr_b16 a[38:39], v204 offset:32768\n" \
"v_mfma_f32_16x16x32_bf16 v[60:63], a[200:203], a[24:27], v[60:63]\n" \
"s_add_u32 m0, s79, 65536\n" \
"s_lshr_b32 s70, s70, 1\n" \
"ds_read_b64_tr_b16 a[40:41], v204 offset:16384\n" \
"v_mfma_f32_16x16x32_bf16 v[60:63], a[204:207], a[28:31], v[60:63]\n" \
"buffer_load_dwordx4 v192, s[44:47], s73 offen offset:0 lds\n" \
"v_mfma_f32_16x16x32_bf16 v[52:55], a[200:203], a[16:19], v[52:55]\n" \
"s_bfm_b32 s65, s70, 0\n" \
"s_sub_u32 s70, 0, s90\n" \
"ds_read_b64_tr_b16 a[42:43], v204 offset:19456\n" \
"v_mfma_f32_16x16x32_bf16 v[52:55], a[204:207], a[20:23], v[52:55]\n" \
"s_xor_b32 s65, s65, s70\n" \
"s_and_b32 s74, s70, 0xffff\n" \
"ds_read_b64_tr_b16 a[44:45], v204 offset:28672\n" \
"v_mfma_f32_16x16x32_bf16 v[44:47], a[200:203], a[8:11], v[44:47]\n" \
"s_xor_b32 s68, s68, s8\n" \
"s_and_b32 s68, s68, 3\n" \
"ds_read_b64_tr_b16 a[46:47], v204 offset:31744\n" \
"v_mfma_f32_16x16x32_bf16 v[44:47], a[204:207], a[12:15], v[44:47]\n" \
"buffer_load_dwordx4 v193, s[44:47], s73 offen offset:1024 lds\n" \
"v_mfma_f32_16x16x32_bf16 v[72:75], a[0:3], a[208:211], v[72:75]\n" \
"s_lshl_b32 s68, s68, 12\n" \
"ds_read_b64_tr_b16 a[48:49], v222 offset:16384\n" \
"v_mfma_f32_16x16x32_bf16 v[72:75], a[4:7], a[212:215], v[72:75]\n" \
"ds_read_b128 a[0:3], v202 offset:18432\n" \
"v_mfma_f32_16x16x32_bf16 v[80:83], a[8:11], a[208:211], v[80:83]\n" \
"ds_read_b128 a[4:7], v203 offset:18432\n" \
"v_mfma_f32_16x16x32_bf16 v[80:83], a[12:15], a[212:215], v[80:83]\n" \
"buffer_load_dwordx4 v192, s[44:47], s73 offen offset:2048 lds\n" \
"v_mfma_f32_16x16x32_bf16 v[84:87], a[8:11], a[216:219], v[84:87]\n" \
"ds_read_b64_tr_b16 a[50:51], v222 offset:19456\n" \
"v_mfma_f32_16x16x32_bf16 v[84:87], a[12:15], a[220:223], v[84:87]\n" \
"ds_read_b128 a[8:11], v200 offset:0\n" \
"v_mfma_f32_16x16x32_bf16 v[92:95], a[16:19], a[216:219], v[92:95]\n" \
"ds_read_b128 a[12:15], v201 offset:0\n" \
"v_mfma_f32_16x16x32_bf16 v[92:95], a[20:23], a[220:223], v[92:95]\n" \
"buffer_load_dwordx4 v193, s[44:47], s73 offen offset:3072 lds\n" \
"v_mfma_f32_16x16x32_bf16 v[88:91], a[16:19], a[208:211], v[88:91]\n" \
"s_xor_b32 s73, s73, 0x180\n" \
"s_mul_i32 s76, s80, 393216\n" \
"ds_read_b64_tr_b16 a[52:53], v222 offset:28672\n" \
"v_mfma_f32_16x16x32_bf16 v[88:91], a[20:23], a[212:215], v[88:91]\n" \
"s_add_u32 s76, s76, s78\n" \
"ds_read_b128 a[16:19], v200 offset:4096\n" \
"v_mfma_f32_16x16x32_bf16 v[96:99], a[24:27], a[208:211], v[96:99]\n" \
"s_add_u32 m0, s91, 81920\n" \
"ds_read_b128 a[20:23], v201 offset:4096\n" \
"v_mfma_f32_16x16x32_bf16 v[96:99], a[28:31], a[212:215], v[96:99]\n" \
"buffer_load_dwordx4 v197, s[48:51], s76 offen offset:0 lds\n" \
"v_mfma_f32_16x16x32_bf16 v[100:103], a[24:27], a[216:219], v[100:103]\n" \
"ds_read_b64_tr_b16 a[54:55], v222 offset:31744\n" \
"v_mfma_f32_16x16x32_bf16 v[100:103], a[28:31], a[220:223], v[100:103]\n" \
"ds_read_b128 a[24:27], v200 offset:8192\n" \
"v_mfma_f32_16x16x32_bf16 v[108:111], a[192:195], a[216:219], v[108:111]\n" \
"s_add_u32 s77, s76, 24576\n" \
"ds_read_b128 a[28:31], v201 offset:8192\n" \
"v_mfma_f32_16x16x32_bf16 v[108:111], a[196:199], a[220:223], v[108:111]\n" \
"buffer_load_dwordx4 v198, s[48:51], s76 offen offset:1024 lds\n" \
"v_mfma_f32_16x16x32_bf16 v[104:107], a[192:195], a[208:211], v[104:107]\n" \
"v_mfma_f32_16x16x32_bf16 v[104:107], a[196:199], a[212:215], v[104:107]\n" \
"s_waitcnt lgkmcnt(2)\n" \
"v_mfma_f32_16x16x32_bf16 v[32:35], a[32:35], a[0:3], v[32:35]\n" \
"s_xor_b32 s80, s80, 0x3\n" \
"ds_read_b128 a[192:195], v200 offset:12288\n" \
"v_mfma_f32_16x16x32_bf16 v[32:35], a[36:39], a[4:7], v[32:35]\n" \
"buffer_load_dwordx4 v199, s[48:51], s76 offen offset:2048 lds\n" \
"v_mfma_f32_16x16x32_bf16 v[40:43], a[32:35], a[8:11], v[40:43]\n" \
"ds_read_b128 a[196:199], v201 offset:12288\n" \
"v_mfma_f32_16x16x32_bf16 v[40:43], a[36:39], a[12:15], v[40:43]\n" \
"ds_read_b64_tr_b16 a[200:201], v222 offset:17408\n" \
"v_mfma_f32_16x16x32_bf16 v[0:3], a[40:43], a[8:11], v[0:3]\n" \
"s_add_u32 m0, m0, 3072\n" \
"ds_read_b64_tr_b16 a[202:203], v222 offset:20480\n" \
"v_mfma_f32_16x16x32_bf16 v[0:3], a[44:47], a[12:15], v[0:3]\n" \
"buffer_load_dwordx4 v197, s[48:51], s77 offen offset:0 lds\n" \
"v_mfma_f32_16x16x32_bf16 v[4:7], a[48:51], a[8:11], v[4:7]\n" \
"ds_read_b64_tr_b16 a[204:205], v222 offset:29696\n" \
"v_mfma_f32_16x16x32_bf16 v[4:7], a[52:55], a[12:15], v[4:7]\n" \
"ds_read_b64_tr_b16 a[206:207], v222 offset:32768\n" \
"v_mfma_f32_16x16x32_bf16 v[12:15], a[48:51], a[16:19], v[12:15]\n" \
"ds_read_b64_tr_b16 a[208:209], v204 offset:18432\n" \
"v_mfma_f32_16x16x32_bf16 v[12:15], a[52:55], a[20:23], v[12:15]\n" \
"buffer_load_dwordx4 v198, s[48:51], s77 offen offset:1024 lds\n" \
"v_mfma_f32_16x16x32_bf16 v[8:11], a[40:43], a[16:19], v[8:11]\n" \
"ds_read_b64_tr_b16 a[210:211], v204 offset:21504\n" \
"v_mfma_f32_16x16x32_bf16 v[8:11], a[44:47], a[20:23], v[8:11]\n" \
"ds_read_b64_tr_b16 a[212:213], v204 offset:30720\n" \
"v_mfma_f32_16x16x32_bf16 v[48:51], a[32:35], a[16:19], v[48:51]\n" \
"s_waitcnt lgkmcnt(3)\n" \
"ds_read_b64_tr_b16 a[214:215], v204 offset:33792\n" \
"v_mfma_f32_16x16x32_bf16 v[48:51], a[36:39], a[20:23], v[48:51]\n" \
"buffer_load_dwordx4 v199, s[48:51], s77 offen offset:2048 lds\n" \
"v_mfma_f32_16x16x32_bf16 v[56:59], a[32:35], a[24:27], v[56:59]\n" \
"ds_read_b64_tr_b16 a[216:217], v222 offset:18432\n" \
"v_mfma_f32_16x16x32_bf16 v[56:59], a[36:39], a[28:31], v[56:59]\n" \
"ds_read_b64_tr_b16 a[218:219], v222 offset:21504\n" \
"v_mfma_f32_16x16x32_bf16 v[16:19], a[40:43], a[24:27], v[16:19]\n" \
"ds_read_b64_tr_b16 a[220:221], v222 offset:30720\n" \
"v_mfma_f32_16x16x32_bf16 v[16:19], a[44:47], a[28:31], v[16:19]\n" \
"v_mfma_f32_16x16x32_bf16 v[20:23], a[48:51], a[24:27], v[20:23]\n" \
"ds_read_b64_tr_b16 a[222:223], v222 offset:33792\n" \
"v_mfma_f32_16x16x32_bf16 v[20:23], a[52:55], a[28:31], v[20:23]\n" \
"v_mfma_f32_16x16x32_bf16 v[28:31], a[48:51], a[192:195], v[28:31]\n" \
"v_mfma_f32_16x16x32_bf16 v[28:31], a[52:55], a[196:199], v[28:31]\n" \
"v_mfma_f32_16x16x32_bf16 v[24:27], a[40:43], a[192:195], v[24:27]\n" \
"v_mfma_f32_16x16x32_bf16 v[24:27], a[44:47], a[196:199], v[24:27]\n" \
"v_mfma_f32_16x16x32_bf16 v[64:67], a[32:35], a[192:195], v[64:67]\n" \
"v_mfma_f32_16x16x32_bf16 v[64:67], a[36:39], a[196:199], v[64:67]\n" \
"s_waitcnt vmcnt(10) lgkmcnt(0)\n" \
"s_barrier\n" \
"v_mfma_f32_16x16x32_bf16 v[68:71], a[200:203], a[192:195], v[68:71]\n" \
"ds_read_b64_tr_b16 a[32:33], v209 offset:25600\n" \
"ds_read_b64_tr_b16 a[34:35], v209 offset:28672\n" \
"v_mfma_f32_16x16x32_bf16 v[68:71], a[204:207], a[196:199], v[68:71]\n" \
"ds_read_b64_tr_b16 a[36:37], v209 offset:37888\n" \
"ds_read_b64_tr_b16 a[38:39], v209 offset:40960\n" \
"v_mfma_f32_16x16x32_bf16 v[60:63], a[200:203], a[24:27], v[60:63]\n" \
"s_add_u32 m0, s79, 0\n" \
"ds_read_b64_tr_b16 a[40:41], v209 offset:24576\n" \
"v_mfma_f32_16x16x32_bf16 v[60:63], a[204:207], a[28:31], v[60:63]\n" \
"buffer_load_dwordx4 v192, s[44:47], s73 offen offset:0 lds\n" \
"v_mfma_f32_16x16x32_bf16 v[52:55], a[200:203], a[16:19], v[52:55]\n" \
"ds_read_b64_tr_b16 a[42:43], v209 offset:27648\n" \
"v_mfma_f32_16x16x32_bf16 v[52:55], a[204:207], a[20:23], v[52:55]\n" \
"ds_read_b64_tr_b16 a[44:45], v209 offset:36864\n" \
"Lpg9045_%=:\n" \
"Lst1x2_%=:\n" \
"s_cbranch_execz Lpg10891_%=\n" \
"v_mfma_f32_16x16x32_bf16 v[44:47], a[200:203], a[8:11], v[44:47]\n" \
"ds_read_b64_tr_b16 a[46:47], v209 offset:39936\n" \
"v_mfma_f32_16x16x32_bf16 v[44:47], a[204:207], a[12:15], v[44:47]\n" \
"buffer_load_dwordx4 v193, s[44:47], s73 offen offset:1024 lds\n" \
"v_mfma_f32_16x16x32_bf16 v[72:75], a[0:3], a[208:211], v[72:75]\n" \
"ds_read_b64_tr_b16 a[48:49], v210 offset:24576\n" \
"v_mfma_f32_16x16x32_bf16 v[72:75], a[4:7], a[212:215], v[72:75]\n" \
"ds_read_b128 a[0:3], v202 offset:20480\n" \
"v_mfma_f32_16x16x32_bf16 v[80:83], a[8:11], a[208:211], v[80:83]\n" \
"ds_read_b128 a[4:7], v203 offset:20480\n" \
"v_mfma_f32_16x16x32_bf16 v[80:83], a[12:15], a[212:215], v[80:83]\n" \
"buffer_load_dwordx4 v192, s[44:47], s73 offen offset:2048 lds\n" \
"v_mfma_f32_16x16x32_bf16 v[84:87], a[8:11], a[216:219], v[84:87]\n" \
"ds_read_b64_tr_b16 a[50:51], v210 offset:27648\n" \
"v_mfma_f32_16x16x32_bf16 v[84:87], a[12:15], a[220:223], v[84:87]\n" \
"ds_read_b128 a[8:11], v200 offset:40960\n" \
"v_mfma_f32_16x16x32_bf16 v[92:95], a[16:19], a[216:219], v[92:95]\n" \
"ds_read_b128 a[12:15], v201 offset:40960\n" \
"v_mfma_f32_16x16x32_bf16 v[92:95], a[20:23], a[220:223], v[92:95]\n" \
"buffer_load_dwordx4 v193, s[44:47], s73 offen offset:3072 lds\n" \
"v_mfma_f32_16x16x32_bf16 v[88:91], a[16:19], a[208:211], v[88:91]\n" \
"s_xor_b32 s73, s73, 0x80\n" \
"s_mul_i32 s76, s80, 393216\n" \
"ds_read_b64_tr_b16 a[52:53], v210 offset:36864\n" \
"v_mfma_f32_16x16x32_bf16 v[88:91], a[20:23], a[212:215], v[88:91]\n" \
"s_add_u32 s76, s76, s78\n" \
"ds_read_b128 a[16:19], v200 offset:45056\n" \
"v_mfma_f32_16x16x32_bf16 v[96:99], a[24:27], a[208:211], v[96:99]\n" \
"s_add_u32 m0, s91, 16384\n" \
"ds_read_b128 a[20:23], v201 offset:45056\n" \
"v_mfma_f32_16x16x32_bf16 v[96:99], a[28:31], a[212:215], v[96:99]\n" \
"buffer_load_dwordx4 v197, s[48:51], s76 offen offset:0 lds\n" \
"v_mfma_f32_16x16x32_bf16 v[100:103], a[24:27], a[216:219], v[100:103]\n" \
"ds_read_b64_tr_b16 a[54:55], v210 offset:39936\n" \
"v_mfma_f32_16x16x32_bf16 v[100:103], a[28:31], a[220:223], v[100:103]\n" \
"ds_read_b128 a[24:27], v200 offset:49152\n" \
"v_mfma_f32_16x16x32_bf16 v[108:111], a[192:195], a[216:219], v[108:111]\n" \
"s_add_u32 s77, s76, 24576\n" \
"ds_read_b128 a[28:31], v201 offset:49152\n" \
"v_mfma_f32_16x16x32_bf16 v[108:111], a[196:199], a[220:223], v[108:111]\n" \
"buffer_load_dwordx4 v198, s[48:51], s76 offen offset:1024 lds\n" \
"v_mfma_f32_16x16x32_bf16 v[104:107], a[192:195], a[208:211], v[104:107]\n" \
"v_mfma_f32_16x16x32_bf16 v[104:107], a[196:199], a[212:215], v[104:107]\n" \
"s_waitcnt lgkmcnt(2)\n" \
"v_mfma_f32_16x16x32_bf16 v[32:35], a[32:35], a[0:3], v[32:35]\n" \
"s_xor_b32 s80, s80, 0x1\n" \
"ds_read_b128 a[192:195], v200 offset:53248\n" \
"v_mfma_f32_16x16x32_bf16 v[32:35], a[36:39], a[4:7], v[32:35]\n" \
"buffer_load_dwordx4 v199, s[48:51], s76 offen offset:2048 lds\n" \
"v_mfma_f32_16x16x32_bf16 v[40:43], a[32:35], a[8:11], v[40:43]\n" \
"ds_read_b128 a[196:199], v201 offset:53248\n" \
"v_mfma_f32_16x16x32_bf16 v[40:43], a[36:39], a[12:15], v[40:43]\n" \
"ds_read_b64_tr_b16 a[200:201], v210 offset:25600\n" \
"v_mfma_f32_16x16x32_bf16 v[0:3], a[40:43], a[8:11], v[0:3]\n" \
"s_add_u32 m0, m0, 3072\n" \
"ds_read_b64_tr_b16 a[202:203], v210 offset:28672\n" \
"v_mfma_f32_16x16x32_bf16 v[0:3], a[44:47], a[12:15], v[0:3]\n" \
"buffer_load_dwordx4 v197, s[48:51], s77 offen offset:0 lds\n" \
"v_mfma_f32_16x16x32_bf16 v[4:7], a[48:51], a[8:11], v[4:7]\n" \
"ds_read_b64_tr_b16 a[204:205], v210 offset:37888\n" \
"v_mfma_f32_16x16x32_bf16 v[4:7], a[52:55], a[12:15], v[4:7]\n" \
"ds_read_b64_tr_b16 a[206:207], v210 offset:40960\n" \
"v_mfma_f32_16x16x32_bf16 v[12:15], a[48:51], a[16:19], v[12:15]\n" \
"ds_read_b64_tr_b16 a[208:209], v209 offset:26624\n" \
"v_mfma_f32_16x16x32_bf16 v[12:15], a[52:55], a[20:23], v[12:15]\n" \
"buffer_load_dwordx4 v198, s[48:51], s77 offen offset:1024 lds\n" \
"v_mfma_f32_16x16x32_bf16 v[8:11], a[40:43], a[16:19], v[8:11]\n" \
"ds_read_b64_tr_b16 a[210:211], v209 offset:29696\n" \
"v_mfma_f32_16x16x32_bf16 v[8:11], a[44:47], a[20:23], v[8:11]\n" \
"ds_read_b64_tr_b16 a[212:213], v209 offset:38912\n" \
"v_mfma_f32_16x16x32_bf16 v[48:51], a[32:35], a[16:19], v[48:51]\n" \
"s_waitcnt lgkmcnt(3)\n" \
"ds_read_b64_tr_b16 a[214:215], v209 offset:41984\n" \
"v_mfma_f32_16x16x32_bf16 v[48:51], a[36:39], a[20:23], v[48:51]\n" \
"buffer_load_dwordx4 v199, s[48:51], s77 offen offset:2048 lds\n" \
"v_mfma_f32_16x16x32_bf16 v[56:59], a[32:35], a[24:27], v[56:59]\n" \
"ds_read_b64_tr_b16 a[216:217], v210 offset:26624\n" \
"v_mfma_f32_16x16x32_bf16 v[56:59], a[36:39], a[28:31], v[56:59]\n" \
"ds_read_b64_tr_b16 a[218:219], v210 offset:29696\n" \
"v_mfma_f32_16x16x32_bf16 v[16:19], a[40:43], a[24:27], v[16:19]\n" \
"ds_read_b64_tr_b16 a[220:221], v210 offset:38912\n" \
"v_mfma_f32_16x16x32_bf16 v[16:19], a[44:47], a[28:31], v[16:19]\n" \
"v_mfma_f32_16x16x32_bf16 v[20:23], a[48:51], a[24:27], v[20:23]\n" \
"ds_read_b64_tr_b16 a[222:223], v210 offset:41984\n" \
"v_mfma_f32_16x16x32_bf16 v[20:23], a[52:55], a[28:31], v[20:23]\n" \
"v_mfma_f32_16x16x32_bf16 v[28:31], a[48:51], a[192:195], v[28:31]\n" \
"v_mfma_f32_16x16x32_bf16 v[28:31], a[52:55], a[196:199], v[28:31]\n" \
"buffer_load_dword v220, v221, s[56:59], s66 offen sc1\n" \
"v_mfma_f32_16x16x32_bf16 v[24:27], a[40:43], a[192:195], v[24:27]\n" \
"v_mfma_f32_16x16x32_bf16 v[24:27], a[44:47], a[196:199], v[24:27]\n" \
"v_mfma_f32_16x16x32_bf16 v[64:67], a[32:35], a[192:195], v[64:67]\n" \
"v_mfma_f32_16x16x32_bf16 v[64:67], a[36:39], a[196:199], v[64:67]\n" \
"s_waitcnt vmcnt(11) lgkmcnt(0)\n" \
"s_barrier\n" \
"v_mfma_f32_16x16x32_bf16 v[68:71], a[200:203], a[192:195], v[68:71]\n" \
"ds_read_b64_tr_b16 a[32:33], v209 offset:1024\n" \
"ds_read_b64_tr_b16 a[34:35], v209 offset:4096\n" \
"v_mfma_f32_16x16x32_bf16 v[68:71], a[204:207], a[196:199], v[68:71]\n" \
"ds_read_b64_tr_b16 a[36:37], v209 offset:13312\n" \
"ds_read_b64_tr_b16 a[38:39], v209 offset:16384\n" \
"v_mfma_f32_16x16x32_bf16 v[60:63], a[200:203], a[24:27], v[60:63]\n" \
"s_add_u32 m0, s79, 131072\n" \
"ds_read_b64_tr_b16 a[40:41], v209 offset:0\n" \
"v_mfma_f32_16x16x32_bf16 v[60:63], a[204:207], a[28:31], v[60:63]\n" \
"buffer_load_dwordx4 v192, s[44:47], s73 offen offset:0 lds\n" \
"v_mfma_f32_16x16x32_bf16 v[52:55], a[200:203], a[16:19], v[52:55]\n" \
"ds_read_b64_tr_b16 a[42:43], v209 offset:3072\n" \
"v_mfma_f32_16x16x32_bf16 v[52:55], a[204:207], a[20:23], v[52:55]\n" \
"ds_read_b64_tr_b16 a[44:45], v209 offset:12288\n" \
"v_mfma_f32_16x16x32_bf16 v[44:47], a[200:203], a[8:11], v[44:47]\n" \
"ds_read_b64_tr_b16 a[46:47], v209 offset:15360\n" \
"v_mfma_f32_16x16x32_bf16 v[44:47], a[204:207], a[12:15], v[44:47]\n" \
"buffer_load_dwordx4 v193, s[44:47], s73 offen offset:1024 lds\n" \
"v_mfma_f32_16x16x32_bf16 v[72:75], a[0:3], a[208:211], v[72:75]\n" \
"ds_read_b64_tr_b16 a[48:49], v210 offset:0\n" \
"v_mfma_f32_16x16x32_bf16 v[72:75], a[4:7], a[212:215], v[72:75]\n" \
"ds_read_b128 a[0:3], v202 offset:21504\n" \
"v_mfma_f32_16x16x32_bf16 v[80:83], a[8:11], a[208:211], v[80:83]\n" \
"ds_read_b128 a[4:7], v203 offset:21504\n" \
"v_mfma_f32_16x16x32_bf16 v[80:83], a[12:15], a[212:215], v[80:83]\n" \
"buffer_load_dwordx4 v192, s[44:47], s73 offen offset:2048 lds\n" \
"v_mfma_f32_16x16x32_bf16 v[84:87], a[8:11], a[216:219], v[84:87]\n" \
"ds_read_b64_tr_b16 a[50:51], v210 offset:3072\n" \
"v_mfma_f32_16x16x32_bf16 v[84:87], a[12:15], a[220:223], v[84:87]\n" \
"ds_read_b128 a[8:11], v205 offset:0\n" \
"v_mfma_f32_16x16x32_bf16 v[92:95], a[16:19], a[216:219], v[92:95]\n" \
"ds_read_b128 a[12:15], v206 offset:0\n" \
"v_mfma_f32_16x16x32_bf16 v[92:95], a[20:23], a[220:223], v[92:95]\n" \
"buffer_load_dwordx4 v193, s[44:47], s73 offen offset:3072 lds\n" \
"v_mfma_f32_16x16x32_bf16 v[88:91], a[16:19], a[208:211], v[88:91]\n" \
"s_xor_b32 s73, s73, 0x80\n" \
"s_mul_i32 s76, s80, 393216\n" \
"ds_read_b64_tr_b16 a[52:53], v210 offset:12288\n" \
"v_mfma_f32_16x16x32_bf16 v[88:91], a[20:23], a[212:215], v[88:91]\n" \
"s_add_u32 s76, s76, s78\n" \
"ds_read_b128 a[16:19], v205 offset:4096\n" \
"v_mfma_f32_16x16x32_bf16 v[96:99], a[24:27], a[208:211], v[96:99]\n" \
"s_add_u32 m0, s91, 106496\n" \
"ds_read_b128 a[20:23], v206 offset:4096\n" \
"v_mfma_f32_16x16x32_bf16 v[96:99], a[28:31], a[212:215], v[96:99]\n" \
"buffer_load_dwordx4 v197, s[48:51], s76 offen offset:0 lds\n" \
"v_mfma_f32_16x16x32_bf16 v[100:103], a[24:27], a[216:219], v[100:103]\n" \
"ds_read_b64_tr_b16 a[54:55], v210 offset:15360\n" \
"v_mfma_f32_16x16x32_bf16 v[100:103], a[28:31], a[220:223], v[100:103]\n" \
"ds_read_b128 a[24:27], v205 offset:8192\n" \
"v_mfma_f32_16x16x32_bf16 v[108:111], a[192:195], a[216:219], v[108:111]\n" \
"s_add_u32 s77, s76, 24576\n" \
"ds_read_b128 a[28:31], v206 offset:8192\n" \
"v_mfma_f32_16x16x32_bf16 v[108:111], a[196:199], a[220:223], v[108:111]\n" \
"buffer_load_dwordx4 v198, s[48:51], s76 offen offset:1024 lds\n" \
"v_mfma_f32_16x16x32_bf16 v[104:107], a[192:195], a[208:211], v[104:107]\n" \
"v_mfma_f32_16x16x32_bf16 v[104:107], a[196:199], a[212:215], v[104:107]\n" \
"s_waitcnt lgkmcnt(2)\n" \
"v_mfma_f32_16x16x32_bf16 v[32:35], a[32:35], a[0:3], v[32:35]\n" \
"s_xor_b32 s80, s80, 0x1\n" \
"ds_read_b128 a[192:195], v205 offset:12288\n" \
"v_mfma_f32_16x16x32_bf16 v[32:35], a[36:39], a[4:7], v[32:35]\n" \
"buffer_load_dwordx4 v199, s[48:51], s76 offen offset:2048 lds\n" \
"v_mfma_f32_16x16x32_bf16 v[40:43], a[32:35], a[8:11], v[40:43]\n" \
"ds_read_b128 a[196:199], v206 offset:12288\n" \
"v_mfma_f32_16x16x32_bf16 v[40:43], a[36:39], a[12:15], v[40:43]\n" \
"ds_read_b64_tr_b16 a[200:201], v210 offset:1024\n" \
"v_mfma_f32_16x16x32_bf16 v[0:3], a[40:43], a[8:11], v[0:3]\n" \
"s_add_u32 m0, m0, 3072\n" \
"ds_read_b64_tr_b16 a[202:203], v210 offset:4096\n" \
"v_mfma_f32_16x16x32_bf16 v[0:3], a[44:47], a[12:15], v[0:3]\n" \
"buffer_load_dwordx4 v197, s[48:51], s77 offen offset:0 lds\n" \
"v_mfma_f32_16x16x32_bf16 v[4:7], a[48:51], a[8:11], v[4:7]\n" \
"ds_read_b64_tr_b16 a[204:205], v210 offset:13312\n" \
"v_mfma_f32_16x16x32_bf16 v[4:7], a[52:55], a[12:15], v[4:7]\n" \
"ds_read_b64_tr_b16 a[206:207], v210 offset:16384\n" \
"v_mfma_f32_16x16x32_bf16 v[12:15], a[48:51], a[16:19], v[12:15]\n" \
"ds_read_b64_tr_b16 a[208:209], v209 offset:2048\n" \
"v_mfma_f32_16x16x32_bf16 v[12:15], a[52:55], a[20:23], v[12:15]\n" \
"buffer_load_dwordx4 v198, s[48:51], s77 offen offset:1024 lds\n" \
"v_mfma_f32_16x16x32_bf16 v[8:11], a[40:43], a[16:19], v[8:11]\n" \
"ds_read_b64_tr_b16 a[210:211], v209 offset:5120\n" \
"v_mfma_f32_16x16x32_bf16 v[8:11], a[44:47], a[20:23], v[8:11]\n" \
"ds_read_b64_tr_b16 a[212:213], v209 offset:14336\n" \
"v_mfma_f32_16x16x32_bf16 v[48:51], a[32:35], a[16:19], v[48:51]\n" \
"s_waitcnt lgkmcnt(3)\n" \
"ds_read_b64_tr_b16 a[214:215], v209 offset:17408\n" \
"v_mfma_f32_16x16x32_bf16 v[48:51], a[36:39], a[20:23], v[48:51]\n" \
"buffer_load_dwordx4 v199, s[48:51], s77 offen offset:2048 lds\n" \
"v_mfma_f32_16x16x32_bf16 v[56:59], a[32:35], a[24:27], v[56:59]\n" \
"ds_read_b64_tr_b16 a[216:217], v210 offset:2048\n" \
"v_mfma_f32_16x16x32_bf16 v[56:59], a[36:39], a[28:31], v[56:59]\n" \
"ds_read_b64_tr_b16 a[218:219], v210 offset:5120\n" \
"v_mfma_f32_16x16x32_bf16 v[16:19], a[40:43], a[24:27], v[16:19]\n" \
"ds_read_b64_tr_b16 a[220:221], v210 offset:14336\n" \
"v_mfma_f32_16x16x32_bf16 v[16:19], a[44:47], a[28:31], v[16:19]\n" \
"v_mfma_f32_16x16x32_bf16 v[20:23], a[48:51], a[24:27], v[20:23]\n" \
"s_mov_b32 s46, s81\n" \
"s_mov_b32 s47, s82\n" \
"ds_read_b64_tr_b16 a[222:223], v210 offset:17408\n" \
"v_mfma_f32_16x16x32_bf16 v[20:23], a[52:55], a[28:31], v[20:23]\n" \
"s_mov_b32 s48, 0\n" \
"s_mov_b32 s49, s68\n" \
"s_sub_u32 s50, 64, s9\n" \
"v_mfma_f32_16x16x32_bf16 v[28:31], a[48:51], a[192:195], v[28:31]\n" \
"s_cmp_eq_u32 s90, 0\n" \
"s_cselect_b32 s50, s9, s50\n" \
"s_andn2_b32 s50, s50, 3\n" \
"v_mfma_f32_16x16x32_bf16 v[28:31], a[52:55], a[196:199], v[28:31]\n" \
"s_lshl_b32 s50, s50, 12\n" \
"s_waitcnt vmcnt(10)\n" \
"v_cmp_eq_u32 vcc, s60, v220\n" \
"v_mfma_f32_16x16x32_bf16 v[24:27], a[40:43], a[192:195], v[24:27]\n" \
"v_mfma_f32_16x16x32_bf16 v[24:27], a[44:47], a[196:199], v[24:27]\n" \
"s_andn2_b32 s70, s65, vcc_lo\n" \
"s_cbranch_scc1 Lpl5_%=\n" \
"Lpr5_%=:\n" \
"v_mfma_f32_16x16x32_bf16 v[64:67], a[32:35], a[192:195], v[64:67]\n" \
"v_mfma_f32_16x16x32_bf16 v[64:67], a[36:39], a[196:199], v[64:67]\n" \
"s_xor_b32 s82, s48, s49\n" \
"s_cmp_lt_u32 s48, s50\n" \
"s_cselect_b32 s82, s82, s48\n" \
"s_waitcnt vmcnt(11) lgkmcnt(0)\n" \
"s_barrier\n" \
"v_mfma_f32_16x16x32_bf16 v[68:71], a[200:203], a[192:195], v[68:71]\n" \
"ds_read_b64_tr_b16 a[32:33], v204 offset:17408\n" \
"ds_read_b64_tr_b16 a[34:35], v204 offset:20480\n" \
"v_mfma_f32_16x16x32_bf16 v[68:71], a[204:207], a[196:199], v[68:71]\n" \
"s_add_u32 s81, s82, s46\n" \
"s_lshr_b32 s78, s61, 1\n" \
"s_lshl_b32 s80, s78, 9\n" \
"s_add_u32 s80, s80, 511\n" \
"ds_read_b64_tr_b16 a[36:37], v204 offset:29696\n" \
"ds_read_b64_tr_b16 a[38:39], v204 offset:32768\n" \
"v_mfma_f32_16x16x32_bf16 v[60:63], a[200:203], a[24:27], v[60:63]\n" \
"s_add_u32 m0, s86, s79\n" \
"ds_read_b64_tr_b16 a[40:41], v204 offset:16384\n" \
"v_sub_u32 v145, s80, v234\n" \
"v_mfma_f32_16x16x32_bf16 v[60:63], a[204:207], a[28:31], v[60:63]\n" \
"buffer_load_dwordx4 v234, s[56:59], s81 offen offset:0 sc1 lds\n" \
"v_mfma_f32_16x16x32_bf16 v[52:55], a[200:203], a[16:19], v[52:55]\n" \
"s_lshl_b32 s80, s78, 3\n" \
"s_not_b32 s80, s80\n" \
"ds_read_b64_tr_b16 a[42:43], v204 offset:19456\n" \
"v_mfma_f32_16x16x32_bf16 v[52:55], a[204:207], a[20:23], v[52:55]\n" \
"s_lshl_b32 s82, s82, 5\n" \
"ds_read_b64_tr_b16 a[44:45], v204 offset:28672\n" \
"v_sub_u32 v146, s80, v238\n" \
"v_mfma_f32_16x16x32_bf16 v[44:47], a[200:203], a[8:11], v[44:47]\n" \
"s_add_u32 s82, s82, s47\n" \
"ds_read_b64_tr_b16 a[46:47], v204 offset:31744\n" \
"v_subrev_u32 v152, 0, v238\n" \
"v_mfma_f32_16x16x32_bf16 v[44:47], a[204:207], a[12:15], v[44:47]\n" \
"buffer_load_dwordx4 v234, s[56:59], s81 offen offset:1024 sc1 lds\n" \
"v_mfma_f32_16x16x32_bf16 v[72:75], a[0:3], a[208:211], v[72:75]\n" \
"ds_read_b64_tr_b16 a[48:49], v222 offset:16384\n" \
"v_sub_u32 v144, 7, v238\n" \
"v_mfma_f32_16x16x32_bf16 v[72:75], a[4:7], a[212:215], v[72:75]\n" \
"ds_read_b128 a[0:3], v202 offset:22528\n" \
"v_or_b32 v152, v152, v144\n" \
"v_mfma_f32_16x16x32_bf16 v[80:83], a[8:11], a[208:211], v[80:83]\n" \
"ds_read_b128 a[4:7], v203 offset:22528\n" \
"v_ashrrev_i32 v152, 31, v152\n" \
"v_mfma_f32_16x16x32_bf16 v[80:83], a[12:15], a[212:215], v[80:83]\n" \
"buffer_load_dwordx4 v234, s[56:59], s81 offen offset:2048 sc1 lds\n" \
"v_mfma_f32_16x16x32_bf16 v[84:87], a[8:11], a[216:219], v[84:87]\n" \
"ds_read_b64_tr_b16 a[50:51], v222 offset:19456\n" \
"v_and_b32 v152, 0xff800000, v152\n" \
"v_mfma_f32_16x16x32_bf16 v[84:87], a[12:15], a[220:223], v[84:87]\n" \
"ds_read_b128 a[8:11], v200 offset:0\n" \
"v_sub_u32 v156, -9, v238\n" \
"v_mfma_f32_16x16x32_bf16 v[92:95], a[16:19], a[216:219], v[92:95]\n" \
"ds_read_b128 a[12:15], v201 offset:0\n" \
"v_ashrrev_i32 v156, 31, v156\n" \
"v_mfma_f32_16x16x32_bf16 v[92:95], a[20:23], a[220:223], v[92:95]\n" \
"buffer_load_dwordx4 v234, s[56:59], s81 offen offset:3072 sc1 lds\n" \
"v_mfma_f32_16x16x32_bf16 v[88:91], a[16:19], a[208:211], v[88:91]\n" \
"ds_read_b64_tr_b16 a[52:53], v222 offset:28672\n" \
"v_and_b32 v156, 0xff800000, v156\n" \
"v_mfma_f32_16x16x32_bf16 v[88:91], a[20:23], a[212:215], v[88:91]\n" \
"ds_read_b128 a[16:19], v200 offset:4096\n" \
"v_add_u32 v148, 0, v146\n" \
"v_mfma_f32_16x16x32_bf16 v[96:99], a[24:27], a[208:211], v[96:99]\n" \
"s_add_u32 m0, m0, 0x4000\n" \
"ds_read_b128 a[20:23], v201 offset:4096\n" \
"v_or3_b32 v148, v148, v145, s92\n" \
"v_mfma_f32_16x16x32_bf16 v[96:99], a[28:31], a[212:215], v[96:99]\n" \
"buffer_load_dwordx4 v216, s[36:39], s82 offen offset:0 lds\n" \
"v_mfma_f32_16x16x32_bf16 v[100:103], a[24:27], a[216:219], v[100:103]\n" \
"ds_read_b64_tr_b16 a[54:55], v222 offset:31744\n" \
"v_ashrrev_i32 v148, 31, v148\n" \
"v_mfma_f32_16x16x32_bf16 v[100:103], a[28:31], a[220:223], v[100:103]\n" \
"ds_read_b128 a[24:27], v200 offset:8192\n" \
"v_and_b32 v148, 0xff800000, v148\n" \
"v_mfma_f32_16x16x32_bf16 v[108:111], a[192:195], a[216:219], v[108:111]\n" \
"s_add_u32 s48, s48, 0x1000\n" \
"ds_read_b128 a[28:31], v201 offset:8192\n" \
"v_subrev_u32 v153, 1, v238\n" \
"v_mfma_f32_16x16x32_bf16 v[108:111], a[196:199], a[220:223], v[108:111]\n" \
"buffer_load_dwordx4 v217, s[36:39], s82 offen offset:1024 lds\n" \
"v_mfma_f32_16x16x32_bf16 v[104:107], a[192:195], a[208:211], v[104:107]\n" \
"v_sub_u32 v144, 8, v238\n" \
"v_or_b32 v153, v153, v144\n" \
"v_mfma_f32_16x16x32_bf16 v[104:107], a[196:199], a[212:215], v[104:107]\n" \
"s_waitcnt lgkmcnt(2)\n" \
"v_ashrrev_i32 v153, 31, v153\n" \
"v_and_b32 v153, 0xff800000, v153\n" \
"v_mfma_f32_16x16x32_bf16 v[32:35], a[32:35], a[0:3], v[32:35]\n" \
"s_sub_u32 s86, s86, 32768\n" \
"s_cselect_b32 s86, 131072, s86\n" \
"ds_read_b128 a[192:195], v200 offset:12288\n" \
"v_mfma_f32_16x16x32_bf16 v[32:35], a[36:39], a[4:7], v[32:35]\n" \
"buffer_load_dwordx4 v218, s[36:39], s82 offen offset:2048 lds\n" \
"v_mfma_f32_16x16x32_bf16 v[40:43], a[32:35], a[8:11], v[40:43]\n" \
"ds_read_b128 a[196:199], v201 offset:12288\n" \
"v_sub_u32 v157, -8, v238\n" \
"v_mfma_f32_16x16x32_bf16 v[40:43], a[36:39], a[12:15], v[40:43]\n" \
"ds_read_b64_tr_b16 a[200:201], v222 offset:17408\n" \
"v_ashrrev_i32 v157, 31, v157\n" \
"v_mfma_f32_16x16x32_bf16 v[0:3], a[40:43], a[8:11], v[0:3]\n" \
"ds_read_b64_tr_b16 a[202:203], v222 offset:20480\n" \
"v_and_b32 v157, 0xff800000, v157\n" \
"v_mfma_f32_16x16x32_bf16 v[0:3], a[44:47], a[12:15], v[0:3]\n" \
"buffer_load_dwordx4 v219, s[36:39], s82 offen offset:3072 lds\n" \
"v_mfma_f32_16x16x32_bf16 v[4:7], a[48:51], a[8:11], v[4:7]\n" \
"ds_read_b64_tr_b16 a[204:205], v222 offset:29696\n" \
"v_add_u32 v149, 1, v146\n" \
"v_mfma_f32_16x16x32_bf16 v[4:7], a[52:55], a[12:15], v[4:7]\n" \
"ds_read_b64_tr_b16 a[206:207], v222 offset:32768\n" \
"v_or3_b32 v149, v149, v145, s92\n" \
"v_mfma_f32_16x16x32_bf16 v[12:15], a[48:51], a[16:19], v[12:15]\n" \
"ds_read_b64_tr_b16 a[208:209], v204 offset:18432\n" \
"v_ashrrev_i32 v149, 31, v149\n" \
"v_mfma_f32_16x16x32_bf16 v[12:15], a[52:55], a[20:23], v[12:15]\n" \
"v_and_b32 v149, 0xff800000, v149\n" \
"v_subrev_u32 v154, 2, v238\n" \
"v_mfma_f32_16x16x32_bf16 v[8:11], a[40:43], a[16:19], v[8:11]\n" \
"ds_read_b64_tr_b16 a[210:211], v204 offset:21504\n" \
"v_sub_u32 v144, 9, v238\n" \
"v_mfma_f32_16x16x32_bf16 v[8:11], a[44:47], a[20:23], v[8:11]\n" \
"ds_read_b64_tr_b16 a[212:213], v204 offset:30720\n" \
"v_or_b32 v154, v154, v144\n" \
"v_mfma_f32_16x16x32_bf16 v[48:51], a[32:35], a[16:19], v[48:51]\n" \
"ds_read_b64_tr_b16 a[214:215], v204 offset:33792\n" \
"v_ashrrev_i32 v154, 31, v154\n" \
"v_mfma_f32_16x16x32_bf16 v[48:51], a[36:39], a[20:23], v[48:51]\n" \
"s_waitcnt lgkmcnt(3)\n" \
"v_and_b32 v154, 0xff800000, v154\n" \
"v_sub_u32 v158, -7, v238\n" \
"v_mfma_f32_16x16x32_bf16 v[56:59], a[32:35], a[24:27], v[56:59]\n" \
"ds_read_b64_tr_b16 a[216:217], v222 offset:18432\n" \
"v_ashrrev_i32 v158, 31, v158\n" \
"v_mfma_f32_16x16x32_bf16 v[56:59], a[36:39], a[28:31], v[56:59]\n" \
"ds_read_b64_tr_b16 a[218:219], v222 offset:21504\n" \
"v_and_b32 v158, 0xff800000, v158\n" \
"v_mfma_f32_16x16x32_bf16 v[16:19], a[40:43], a[24:27], v[16:19]\n" \
"ds_read_b64_tr_b16 a[220:221], v222 offset:30720\n" \
"v_add_u32 v150, 2, v146\n" \
"v_mfma_f32_16x16x32_bf16 v[16:19], a[44:47], a[28:31], v[16:19]\n" \
"v_or3_b32 v150, v150, v145, s92\n" \
"v_ashrrev_i32 v150, 31, v150\n" \
"v_mfma_f32_16x16x32_bf16 v[20:23], a[48:51], a[24:27], v[20:23]\n" \
"ds_read_b64_tr_b16 a[222:223], v222 offset:33792\n" \
"v_and_b32 v150, 0xff800000, v150\n" \
"v_mfma_f32_16x16x32_bf16 v[20:23], a[52:55], a[28:31], v[20:23]\n" \
"v_subrev_u32 v155, 3, v238\n" \
"v_sub_u32 v144, 10, v238\n" \
"v_mfma_f32_16x16x32_bf16 v[28:31], a[48:51], a[192:195], v[28:31]\n" \
"v_or_b32 v155, v155, v144\n" \
"v_ashrrev_i32 v155, 31, v155\n" \
"v_mfma_f32_16x16x32_bf16 v[28:31], a[52:55], a[196:199], v[28:31]\n" \
"v_and_b32 v155, 0xff800000, v155\n" \
"v_sub_u32 v159, -6, v238\n" \
"v_mfma_f32_16x16x32_bf16 v[24:27], a[40:43], a[192:195], v[24:27]\n" \
"v_ashrrev_i32 v159, 31, v159\n" \
"v_and_b32 v159, 0xff800000, v159\n" \
"v_mfma_f32_16x16x32_bf16 v[24:27], a[44:47], a[196:199], v[24:27]\n" \
"v_add_u32 v151, 3, v146\n" \
"v_or3_b32 v151, v151, v145, s92\n" \
"v_mfma_f32_16x16x32_bf16 v[64:67], a[32:35], a[192:195], v[64:67]\n" \
"v_ashrrev_i32 v151, 31, v151\n" \
"v_and_b32 v151, 0xff800000, v151\n" \
"v_mfma_f32_16x16x32_bf16 v[64:67], a[36:39], a[196:199], v[64:67]\n" \
"s_xor_b32 s82, s48, s49\n" \
"s_cmp_lt_u32 s48, s50\n" \
"s_cselect_b32 s82, s82, s48\n" \
"s_waitcnt vmcnt(8) lgkmcnt(0)\n" \
"s_barrier\n" \
"v_mfma_f32_16x16x32_bf16 v[68:71], a[200:203], a[192:195], v[68:71]\n" \
"ds_read_b64_tr_b16 a[32:33], v209 offset:25600\n" \
"ds_read_b64_tr_b16 a[34:35], v209 offset:28672\n" \
"v_mfma_f32_16x16x32_bf16 v[68:71], a[204:207], a[196:199], v[68:71]\n" \
"s_add_u32 s81, s82, s46\n" \
"ds_read_b64_tr_b16 a[36:37], v209 offset:37888\n" \
"ds_read_b64_tr_b16 a[38:39], v209 offset:40960\n" \
"v_mfma_f32_16x16x32_bf16 v[60:63], a[200:203], a[24:27], v[60:63]\n" \
"s_add_u32 m0, s86, s79\n" \
"ds_read_b64_tr_b16 a[40:41], v209 offset:24576\n" \
"v_mov_b32 v186, 0xffff0000\n" \
"v_mfma_f32_16x16x32_bf16 v[60:63], a[204:207], a[28:31], v[60:63]\n" \
"buffer_load_dwordx4 v234, s[56:59], s81 offen offset:0 sc1 lds\n" \
"v_mfma_f32_16x16x32_bf16 v[52:55], a[200:203], a[16:19], v[52:55]\n" \
"ds_read_b64_tr_b16 a[42:43], v209 offset:27648\n" \
"v_subrev_u32 v144, 0, v238\n" \
"v_mfma_f32_16x16x32_bf16 v[52:55], a[204:207], a[20:23], v[52:55]\n" \
"s_lshl_b32 s82, s82, 5\n" \
"ds_read_b64_tr_b16 a[44:45], v209 offset:36864\n" \
"v_subrev_u32 v184, 1, v238\n" \
"v_mfma_f32_16x16x32_bf16 v[44:47], a[200:203], a[8:11], v[44:47]\n" \
"s_add_u32 s82, s82, s47\n" \
"ds_read_b64_tr_b16 a[46:47], v209 offset:39936\n" \
"v_lshrrev_b32 v144, 16, v144\n" \
"v_mfma_f32_16x16x32_bf16 v[44:47], a[204:207], a[12:15], v[44:47]\n" \
"buffer_load_dwordx4 v234, s[56:59], s81 offen offset:1024 sc1 lds\n" \
"v_mfma_f32_16x16x32_bf16 v[72:75], a[0:3], a[208:211], v[72:75]\n" \
"ds_read_b64_tr_b16 a[48:49], v210 offset:24576\n" \
"v_and_or_b32 v184, v184, v186, v144\n" \
"v_mfma_f32_16x16x32_bf16 v[72:75], a[4:7], a[212:215], v[72:75]\n" \
"ds_read_b128 a[0:3], v202 offset:23552\n" \
"v_subrev_u32 v144, 2, v238\n" \
"v_mfma_f32_16x16x32_bf16 v[80:83], a[8:11], a[208:211], v[80:83]\n" \
"ds_read_b128 a[4:7], v203 offset:23552\n" \
"v_subrev_u32 v185, 3, v238\n" \
"v_mfma_f32_16x16x32_bf16 v[80:83], a[12:15], a[212:215], v[80:83]\n" \
"buffer_load_dwordx4 v234, s[56:59], s81 offen offset:2048 sc1 lds\n" \
"v_mfma_f32_16x16x32_bf16 v[84:87], a[8:11], a[216:219], v[84:87]\n" \
"ds_read_b64_tr_b16 a[50:51], v210 offset:27648\n" \
"v_lshrrev_b32 v144, 16, v144\n" \
"v_mfma_f32_16x16x32_bf16 v[84:87], a[12:15], a[220:223], v[84:87]\n" \
"ds_read_b128 a[8:11], v207 offset:0\n" \
"v_and_or_b32 v185, v185, v186, v144\n" \
"v_mfma_f32_16x16x32_bf16 v[92:95], a[16:19], a[216:219], v[92:95]\n" \
"ds_read_b128 a[12:15], v208 offset:0\n" \
"v_mov_b32 v144, 0x3e8293ee\n" \
"v_mfma_f32_16x16x32_bf16 v[92:95], a[20:23], a[220:223], v[92:95]\n" \
"buffer_load_dwordx4 v234, s[56:59], s81 offen offset:3072 sc1 lds\n" \
"v_mfma_f32_16x16x32_bf16 v[88:91], a[16:19], a[208:211], v[88:91]\n" \
"s_add_u32 s80, s79, 117760\n" \
"ds_read_b64_tr_b16 a[52:53], v210 offset:36864\n" \
"v_add_u32 v223, s80, v234\n" \
"v_mfma_f32_16x16x32_bf16 v[88:91], a[20:23], a[212:215], v[88:91]\n" \
"s_cmp_lg_u32 s78, 0\n" \
"s_cselect_b64 s[76:77], -1, 0\n" \
"ds_read_b128 a[16:19], v207 offset:4096\n" \
"v_mfma_f32_16x16x32_bf16 v[96:99], a[24:27], a[208:211], v[96:99]\n" \
"s_add_u32 m0, m0, 0x4000\n" \
"ds_read_b128 a[20:23], v208 offset:4096\n" \
"v_mfma_f32_16x16x32_bf16 v[96:99], a[28:31], a[212:215], v[96:99]\n" \
"buffer_load_dwordx4 v216, s[36:39], s82 offen offset:0 lds\n" \
"v_mfma_f32_16x16x32_bf16 v[100:103], a[24:27], a[216:219], v[100:103]\n" \
"ds_read_b64_tr_b16 a[54:55], v210 offset:39936\n" \
"v_mfma_f32_16x16x32_bf16 v[100:103], a[28:31], a[220:223], v[100:103]\n" \
"ds_read_b128 a[24:27], v207 offset:8192\n" \
"v_mfma_f32_16x16x32_bf16 v[108:111], a[192:195], a[216:219], v[108:111]\n" \
"s_add_u32 s48, s48, 0x1000\n" \
"ds_read_b128 a[28:31], v208 offset:8192\n" \
"v_mfma_f32_16x16x32_bf16 v[108:111], a[196:199], a[220:223], v[108:111]\n" \
"buffer_load_dwordx4 v217, s[36:39], s82 offen offset:1024 lds\n" \
"v_mfma_f32_16x16x32_bf16 v[104:107], a[192:195], a[208:211], v[104:107]\n" \
"v_mfma_f32_16x16x32_bf16 v[104:107], a[196:199], a[212:215], v[104:107]\n" \
"s_waitcnt lgkmcnt(2)\n" \
"v_mfma_f32_16x16x32_bf16 v[32:35], a[32:35], a[0:3], v[32:35]\n" \
"s_sub_u32 s86, s86, 32768\n" \
"s_cselect_b32 s86, 131072, s86\n" \
"ds_read_b128 a[192:195], v207 offset:12288\n" \
"v_mfma_f32_16x16x32_bf16 v[32:35], a[36:39], a[4:7], v[32:35]\n" \
"buffer_load_dwordx4 v218, s[36:39], s82 offen offset:2048 lds\n" \
"v_mfma_f32_16x16x32_bf16 v[40:43], a[32:35], a[8:11], v[40:43]\n" \
"ds_read_b128 a[196:199], v208 offset:12288\n" \
"v_mfma_f32_16x16x32_bf16 v[40:43], a[36:39], a[12:15], v[40:43]\n" \
"ds_read_b64_tr_b16 a[200:201], v210 offset:25600\n" \
"v_mfma_f32_16x16x32_bf16 v[0:3], a[40:43], a[8:11], v[0:3]\n" \
"ds_read_b64_tr_b16 a[202:203], v210 offset:28672\n" \
"v_mfma_f32_16x16x32_bf16 v[0:3], a[44:47], a[12:15], v[0:3]\n" \
"buffer_load_dwordx4 v219, s[36:39], s82 offen offset:3072 lds\n" \
"v_mfma_f32_16x16x32_bf16 v[4:7], a[48:51], a[8:11], v[4:7]\n" \
"s_xor_b32 s82, s48, s49\n" \
"s_cmp_lt_u32 s48, s50\n" \
"s_cselect_b32 s82, s82, s48\n" \
"ds_read_b64_tr_b16 a[204:205], v210 offset:37888\n" \
"v_mfma_f32_16x16x32_bf16 v[4:7], a[52:55], a[12:15], v[4:7]\n" \
"s_add_u32 s81, s82, s46\n" \
"ds_read_b64_tr_b16 a[206:207], v210 offset:40960\n" \
"v_mfma_f32_16x16x32_bf16 v[12:15], a[48:51], a[16:19], v[12:15]\n" \
"s_add_u32 m0, s86, s79\n" \
"ds_read_b64_tr_b16 a[208:209], v209 offset:26624\n" \
"v_mfma_f32_16x16x32_bf16 v[12:15], a[52:55], a[20:23], v[12:15]\n" \
"buffer_load_dwordx4 v234, s[56:59], s81 offen offset:0 sc1 lds\n" \
"v_mfma_f32_16x16x32_bf16 v[8:11], a[40:43], a[16:19], v[8:11]\n" \
"ds_read_b64_tr_b16 a[210:211], v209 offset:29696\n" \
"v_mfma_f32_16x16x32_bf16 v[8:11], a[44:47], a[20:23], v[8:11]\n" \
"s_lshl_b32 s82, s82, 5\n" \
"ds_read_b64_tr_b16 a[212:213], v209 offset:38912\n" \
"v_mfma_f32_16x16x32_bf16 v[48:51], a[32:35], a[16:19], v[48:51]\n" \
"s_waitcnt lgkmcnt(3)\n" \
"s_add_u32 s82, s82, s47\n" \
"ds_read_b64_tr_b16 a[214:215], v209 offset:41984\n" \
"v_mfma_f32_16x16x32_bf16 v[48:51], a[36:39], a[20:23], v[48:51]\n" \
"buffer_load_dwordx4 v234, s[56:59], s81 offen offset:1024 sc1 lds\n" \
"v_mfma_f32_16x16x32_bf16 v[56:59], a[32:35], a[24:27], v[56:59]\n" \
"ds_read_b64_tr_b16 a[216:217], v210 offset:26624\n" \
"v_mfma_f32_16x16x32_bf16 v[56:59], a[36:39], a[28:31], v[56:59]\n" \
"ds_read_b64_tr_b16 a[218:219], v210 offset:29696\n" \
"v_mfma_f32_16x16x32_bf16 v[16:19], a[40:43], a[24:27], v[16:19]\n" \
"ds_read_b64_tr_b16 a[220:221], v210 offset:38912\n" \
"v_mfma_f32_16x16x32_bf16 v[16:19], a[44:47], a[28:31], v[16:19]\n" \
"buffer_load_dwordx4 v234, s[56:59], s81 offen offset:2048 sc1 lds\n" \
"v_mfma_f32_16x16x32_bf16 v[20:23], a[48:51], a[24:27], v[20:23]\n" \
"ds_read_b64_tr_b16 a[222:223], v210 offset:41984\n" \
"v_mfma_f32_16x16x32_bf16 v[20:23], a[52:55], a[28:31], v[20:23]\n" \
"v_mfma_f32_16x16x32_bf16 v[28:31], a[48:51], a[192:195], v[28:31]\n" \
"v_mfma_f32_16x16x32_bf16 v[28:31], a[52:55], a[196:199], v[28:31]\n" \
"buffer_load_dwordx4 v234, s[56:59], s81 offen offset:3072 sc1 lds\n" \
"v_mfma_f32_16x16x32_bf16 v[24:27], a[40:43], a[192:195], v[24:27]\n" \
"v_mfma_f32_16x16x32_bf16 v[24:27], a[44:47], a[196:199], v[24:27]\n" \
"v_mfma_f32_16x16x32_bf16 v[64:67], a[32:35], a[192:195], v[64:67]\n" \
"s_add_u32 m0, m0, 0x4000\n" \
"v_mfma_f32_16x16x32_bf16 v[64:67], a[36:39], a[196:199], v[64:67]\n" \
"buffer_load_dwordx4 v216, s[36:39], s82 offen offset:0 lds\n" \
"s_waitcnt vmcnt(13) lgkmcnt(0)\n" \
"s_barrier\n" \
"v_mfma_f32_16x16x32_bf16 v[44:47], a[200:203], a[8:11], v[44:47]\n" \
"ds_read_b64_tr_b16 a[96:97], v214 offset:16384\n" \
"v_mfma_f32_16x16x32_bf16 v[44:47], a[204:207], a[12:15], v[44:47]\n" \
"ds_read_b64_tr_b16 a[98:99], v214 offset:18432\n" \
"v_mfma_f32_16x16x32_bf16 v[52:55], a[200:203], a[16:19], v[52:55]\n" \
"s_add_u32 s48, s48, 0x1000\n" \
"ds_read_b64_tr_b16 a[100:101], v214 offset:24576\n" \
"v_mfma_f32_16x16x32_bf16 v[52:55], a[204:207], a[20:23], v[52:55]\n" \
"buffer_load_dwordx4 v217, s[36:39], s82 offen offset:1024 lds\n" \
"v_mfma_f32_16x16x32_bf16 v[60:63], a[200:203], a[24:27], v[60:63]\n" \
"ds_read_b64_tr_b16 a[102:103], v214 offset:26624\n" \
"v_cvt_pk_bf16_f32 v112, v0, v1\n" \
"v_mfma_f32_16x16x32_bf16 v[60:63], a[204:207], a[28:31], v[60:63]\n" \
"ds_read_b128 a[64:67], v213 offset:0\n" \
"v_cvt_pk_bf16_f32 v113, v2, v3\n" \
"v_mfma_f32_16x16x32_bf16 v[68:71], a[200:203], a[192:195], v[68:71]\n" \
"s_sub_u32 s86, s86, 32768\n" \
"s_cselect_b32 s86, 131072, s86\n" \
"ds_read_b128 a[68:71], v213 offset:2048\n" \
"v_mfma_f32_16x16x32_bf16 v[68:71], a[204:207], a[196:199], v[68:71]\n" \
"buffer_load_dwordx4 v218, s[36:39], s82 offen offset:2048 lds\n" \
"v_mfma_f32_16x16x32_bf16 v[72:75], a[0:3], a[208:211], v[72:75]\n" \
"ds_read_b64_tr_b16 a[104:105], v215 offset:16384\n" \
"v_cvt_pk_bf16_f32 v114, v4, v5\n" \
"v_mfma_f32_16x16x32_bf16 v[72:75], a[4:7], a[212:215], v[72:75]\n" \
"ds_read_b64_tr_b16 a[106:107], v215 offset:18432\n" \
"v_cvt_pk_bf16_f32 v115, v6, v7\n" \
"v_mfma_f32_16x16x32_bf16 v[80:83], a[8:11], a[208:211], v[80:83]\n" \
"ds_read_b64_tr_b16 a[108:109], v215 offset:24576\n" \
"v_cvt_pk_bf16_f32 v116, v8, v9\n" \
"v_mfma_f32_16x16x32_bf16 v[80:83], a[12:15], a[212:215], v[80:83]\n" \
"buffer_load_dwordx4 v219, s[36:39], s82 offen offset:3072 lds\n" \
"v_mfma_f32_16x16x32_bf16 v[84:87], a[8:11], a[216:219], v[84:87]\n" \
"s_xor_b32 s82, s48, s49\n" \
"s_cmp_lt_u32 s48, s50\n" \
"s_cselect_b32 s82, s82, s48\n" \
"ds_read_b64_tr_b16 a[110:111], v215 offset:26624\n" \
"v_mfma_f32_16x16x32_bf16 v[84:87], a[12:15], a[220:223], v[84:87]\n" \
"s_add_u32 s81, s82, s46\n" \
"ds_read_b128 a[72:75], v213 offset:256\n" \
"v_cvt_pk_bf16_f32 v117, v10, v11\n" \
"v_mfma_f32_16x16x32_bf16 v[92:95], a[16:19], a[216:219], v[92:95]\n" \
"s_add_u32 m0, s86, s79\n" \
"ds_read_b128 a[76:79], v213 offset:2304\n" \
"v_cvt_pk_bf16_f32 v118, v12, v13\n" \
"v_mfma_f32_16x16x32_bf16 v[92:95], a[20:23], a[220:223], v[92:95]\n" \
"buffer_load_dwordx4 v234, s[56:59], s81 offen offset:0 sc1 lds\n" \
"v_mfma_f32_16x16x32_bf16 v[88:91], a[16:19], a[208:211], v[88:91]\n" \
"ds_read_b128 a[80:83], v213 offset:4096\n" \
"Lpg9611_%=:\n" \
"Lst1x3_%=:\n" \
"s_cbranch_execz Lpg11452_%=\n" \
"v_cvt_pk_bf16_f32 v119, v14, v15\n" \
"v_mfma_f32_16x16x32_bf16 v[88:91], a[20:23], a[212:215], v[88:91]\n" \
"s_lshl_b32 s82, s82, 5\n" \
"ds_read_b128 a[84:87], v213 offset:6144\n" \
"v_cvt_pk_bf16_f32 v120, v16, v17\n" \
"v_mfma_f32_16x16x32_bf16 v[96:99], a[24:27], a[208:211], v[96:99]\n" \
"s_add_u32 s82, s82, s47\n" \
"v_cvt_pk_bf16_f32 v121, v18, v19\n" \
"v_cvt_pk_bf16_f32 v122, v20, v21\n" \
"v_mfma_f32_16x16x32_bf16 v[96:99], a[28:31], a[212:215], v[96:99]\n" \
"buffer_load_dwordx4 v234, s[56:59], s81 offen offset:1024 sc1 lds\n" \
"v_mfma_f32_16x16x32_bf16 v[100:103], a[24:27], a[216:219], v[100:103]\n" \
"v_cvt_pk_bf16_f32 v123, v22, v23\n" \
"v_cvt_pk_bf16_f32 v124, v24, v25\n" \
"v_mfma_f32_16x16x32_bf16 v[100:103], a[28:31], a[220:223], v[100:103]\n" \
"v_cvt_pk_bf16_f32 v125, v26, v27\n" \
"v_cvt_pk_bf16_f32 v126, v28, v29\n" \
"v_mfma_f32_16x16x32_bf16 v[108:111], a[192:195], a[216:219], v[108:111]\n" \
"v_cvt_pk_bf16_f32 v127, v30, v31\n" \
"v_cvt_pk_bf16_f32 v128, v32, v33\n" \
"v_mfma_f32_16x16x32_bf16 v[108:111], a[196:199], a[220:223], v[108:111]\n" \
"buffer_load_dwordx4 v234, s[56:59], s81 offen offset:2048 sc1 lds\n" \
"v_mfma_f32_16x16x32_bf16 v[104:107], a[192:195], a[208:211], v[104:107]\n" \
"v_cvt_pk_bf16_f32 v129, v34, v35\n" \
"v_xor_b32 v145, 0x2000, v223\n" \
"v_mfma_f32_16x16x32_bf16 v[104:107], a[196:199], a[212:215], v[104:107]\n" \
"s_waitcnt lgkmcnt(0)\n" \
"v_cvt_pk_bf16_f32 v192, v72, v73\n" \
"v_cvt_pk_bf16_f32 v193, v74, v75\n" \
"ds_write_b64 v145, v[128:129]\n" \
"s_nop 0\n" \
"ds_write_b64 v145, v[192:193] offset:8\n" \
"v_cvt_pk_bf16_f32 v132, v40, v41\n" \
"v_cvt_pk_bf16_f32 v133, v42, v43\n" \
"v_cvt_pk_bf16_f32 v134, v44, v45\n" \
"v_cvt_pk_bf16_f32 v135, v46, v47\n" \
"v_cvt_pk_bf16_f32 v136, v48, v49\n" \
"v_cvt_pk_bf16_f32 v137, v50, v51\n" \
"v_cvt_pk_bf16_f32 v138, v52, v53\n" \
"v_cvt_pk_bf16_f32 v139, v54, v55\n" \
"v_cvt_pk_bf16_f32 v140, v56, v57\n" \
"v_cvt_pk_bf16_f32 v141, v58, v59\n" \
"v_cvt_pk_bf16_f32 v142, v60, v61\n" \
"v_cvt_pk_bf16_f32 v143, v62, v63\n" \
"v_mfma_f32_16x16x32_bf16 v[0:3], a[96:99], a[64:67], 0\n" \
"ds_read_b128 a[88:91], v213 offset:4352\n" \
"v_mfma_f32_16x16x32_bf16 v[0:3], a[100:103], a[68:71], v[0:3]\n" \
"ds_read_b128 a[92:95], v213 offset:6400\n" \
"v_mfma_f32_16x16x32_bf16 v[4:7], a[104:107], a[64:67], 0\n" \
"ds_read_b64_tr_b16 a[112:113], v214 offset:16896\n" \
"v_mfma_f32_16x16x32_bf16 v[4:7], a[108:111], a[68:71], v[4:7]\n" \
"ds_read_b64_tr_b16 a[114:115], v214 offset:18944\n" \
"v_mfma_f32_16x16x32_bf16 v[20:23], a[104:107], a[72:75], 0\n" \
"ds_read_b64_tr_b16 a[116:117], v214 offset:25088\n" \
"v_cvt_pk_bf16_f32 v64, v64, v65\n" \
"v_mfma_f32_16x16x32_bf16 v[20:23], a[108:111], a[76:79], v[20:23]\n" \
"buffer_load_dwordx4 v234, s[56:59], s81 offen offset:3072 sc1 lds\n" \
"v_cvt_pk_bf16_f32 v65, v66, v67\n" \
"v_cvt_pk_bf16_f32 v66, v68, v69\n" \
"v_mfma_f32_16x16x32_bf16 v[16:19], a[96:99], a[72:75], 0\n" \
"s_sub_u32 s85, s85, 32768\n" \
"s_cselect_b32 s85, 131072, s85\n" \
"ds_read_b64_tr_b16 a[118:119], v214 offset:27136\n" \
"v_mfma_f32_16x16x32_bf16 v[16:19], a[100:103], a[76:79], v[16:19]\n" \
"s_waitcnt lgkmcnt(3)\n" \
"ds_read_b64_tr_b16 a[120:121], v215 offset:16896\n" \
"v_cvt_pk_bf16_f32 v67, v70, v71\n" \
"v_cvt_pk_bf16_f32 v194, v80, v81\n" \
"v_cvt_pk_bf16_f32 v195, v82, v83\n" \
"v_cvt_pk_bf16_f32 v74, v84, v85\n" \
"v_mfma_f32_16x16x32_bf16 v[32:35], a[96:99], a[80:83], 0\n" \
"s_add_u32 m0, m0, 0x4000\n" \
"ds_read_b64_tr_b16 a[122:123], v215 offset:18944\n" \
"v_cvt_pk_bf16_f32 v75, v86, v87\n" \
"v_mfma_f32_16x16x32_bf16 v[32:35], a[100:103], a[84:87], v[32:35]\n" \
"buffer_load_dwordx4 v216, s[36:39], s82 offen offset:0 lds\n" \
"v_cvt_pk_bf16_f32 v196, v88, v89\n" \
"v_cvt_pk_bf16_f32 v197, v90, v91\n" \
"v_mfma_f32_16x16x32_bf16 v[176:179], v[64:67], v[124:127], v[152:155]\n" \
"v_mfma_f32_16x16x32_bf16 v[172:175], v[140:143], v[124:127], v[156:159]\n" \
"v_mfma_f32_16x16x32_bf16 v[168:171], v[140:143], v[120:123], v[152:155]\n" \
"v_mfma_f32_16x16x32_bf16 v[164:167], v[136:139], v[120:123], v[156:159]\n" \
"v_mfma_f32_16x16x32_bf16 v[160:163], v[136:139], v[116:119], v[152:155]\n" \
"v_mfma_f32_16x16x32_bf16 v[156:159], v[132:135], v[116:119], v[156:159]\n" \
"v_mfma_f32_16x16x32_bf16 v[152:155], v[132:135], v[112:115], v[152:155]\n" \
"v_mfma_f32_16x16x32_bf16 v[36:39], a[104:107], a[80:83], 0\n" \
"s_add_u32 s48, s48, 0x1000\n" \
"ds_read_b64_tr_b16 a[124:125], v215 offset:25088\n" \
"v_add_u32 v213, s85, v211\n" \
"v_mfma_f32_16x16x32_bf16 v[36:39], a[108:111], a[84:87], v[36:39]\n" \
"buffer_load_dwordx4 v217, s[36:39], s82 offen offset:1024 lds\n" \
"v_mfma_f32_16x16x32_bf16 v[52:55], a[104:107], a[88:91], 0\n" \
"s_sub_u32 s86, s86, 32768\n" \
"s_cselect_b32 s86, 131072, s86\n" \
"ds_read_b64_tr_b16 a[126:127], v215 offset:27136\n" \
"v_mfma_f32_16x16x32_bf16 v[52:55], a[108:111], a[92:95], v[52:55]\n" \
"buffer_load_dwordx4 v218, s[36:39], s82 offen offset:2048 lds\n" \
"v_mfma_f32_16x16x32_bf16 v[48:51], a[96:99], a[88:91], 0\n" \
"v_add_u32 v214, s85, v212\n" \
"v_xor_b32 v215, 32, v214\n" \
"v_mfma_f32_16x16x32_bf16 v[48:51], a[100:103], a[92:95], v[48:51]\n" \
"s_waitcnt vmcnt(15) lgkmcnt(0)\n" \
"s_barrier\n" \
"v_mfma_f32_16x16x32_bf16 v[8:11], a[112:115], a[64:67], 0\n" \
"ds_read_b64 v[130:131], v223\n" \
"ds_read_b64_tr_b16 a[96:97], v214 offset:16384\n" \
"ds_read_b64_tr_b16 a[98:99], v214 offset:18432\n" \
"v_mfma_f32_16x16x32_bf16 v[8:11], a[116:119], a[68:71], v[8:11]\n" \
"ds_read_b64 v[72:73], v223 offset:8\n" \
"buffer_load_dwordx4 v219, s[36:39], s82 offen offset:3072 lds\n" \
"v_mfma_f32_16x16x32_bf16 v[12:15], a[120:123], a[64:67], 0\n" \
"s_xor_b32 s82, s48, s49\n" \
"s_cmp_lt_u32 s48, s50\n" \
"s_cselect_b32 s82, s82, s48\n" \
"ds_read_b64_tr_b16 a[100:101], v214 offset:24576\n" \
"ds_read_b64_tr_b16 a[102:103], v214 offset:26624\n" \
"v_mfma_f32_16x16x32_bf16 v[12:15], a[124:127], a[68:71], v[12:15]\n" \
"s_add_u32 s81, s82, s46\n" \
"ds_read_b128 a[64:67], v213 offset:0\n" \
"v_mfma_f32_16x16x32_bf16 v[28:31], a[120:123], a[72:75], 0\n" \
"s_add_u32 m0, s86, s79\n" \
"ds_read_b128 a[68:71], v213 offset:2048\n" \
"v_mfma_f32_16x16x32_bf16 v[28:31], a[124:127], a[76:79], v[28:31]\n" \
"buffer_load_dwordx4 v234, s[56:59], s81 offen offset:0 sc1 lds\n" \
"v_mfma_f32_16x16x32_bf16 v[24:27], a[112:115], a[72:75], 0\n" \
"ds_read_b64_tr_b16 a[104:105], v215 offset:16384\n" \
"ds_read_b64_tr_b16 a[106:107], v215 offset:18432\n" \
"v_mfma_f32_16x16x32_bf16 v[24:27], a[116:119], a[76:79], v[24:27]\n" \
"s_waitcnt lgkmcnt(4)\n" \
"s_lshl_b32 s82, s82, 5\n" \
"ds_read_b128 a[72:75], v213 offset:256\n" \
"v_mfma_f32_16x16x32_bf16 v[40:43], a[112:115], a[80:83], 0\n" \
"s_add_u32 s82, s82, s47\n" \
"ds_read_b128 a[76:79], v213 offset:2304\n" \
"v_mfma_f32_16x16x32_bf16 v[148:151], v[128:131], v[112:115], v[148:151]\n" \
"v_max_f32 v172, v172, v176\n" \
"v_max_f32 v173, v173, v177\n" \
"v_max_f32 v174, v174, v178\n" \
"v_max_f32 v175, v175, v179\n" \
"v_max_f32 v164, v164, v168\n" \
"v_max_f32 v165, v165, v169\n" \
"v_max_f32 v166, v166, v170\n" \
"v_max_f32 v167, v167, v171\n" \
"v_max_f32 v156, v156, v160\n" \
"v_max_f32 v157, v157, v161\n" \
"v_max_f32 v158, v158, v162\n" \
"v_max_f32 v159, v159, v163\n" \
"v_max_f32 v148, v148, v152\n" \
"v_max_f32 v149, v149, v153\n" \
"v_max_f32 v150, v150, v154\n" \
"v_max_f32 v151, v151, v155\n" \
"v_max3_f32 v183, v172, v173, v174\n" \
"v_max3_f32 v182, v164, v165, v166\n" \
"v_max3_f32 v181, v156, v157, v158\n" \
"v_mfma_f32_16x16x32_bf16 v[40:43], a[116:119], a[84:87], v[40:43]\n" \
"buffer_load_dwordx4 v234, s[56:59], s81 offen offset:1024 sc1 lds\n" \
"v_max3_f32 v180, v148, v149, v150\n" \
"v_mfma_f32_16x16x32_bf16 v[44:47], a[120:123], a[80:83], 0\n" \
"ds_read_b64_tr_b16 a[108:109], v215 offset:24576\n" \
"ds_read_b64_tr_b16 a[110:111], v215 offset:26624\n" \
"v_mfma_f32_16x16x32_bf16 v[44:47], a[124:127], a[84:87], v[44:47]\n" \
"buffer_load_dwordx4 v234, s[56:59], s81 offen offset:2048 sc1 lds\n" \
"v_max_f32 v183, v183, v175\n" \
"v_max_f32 v182, v182, v167\n" \
"v_max_f32 v181, v181, v159\n" \
"v_mfma_f32_16x16x32_bf16 v[60:63], a[120:123], a[88:91], 0\n" \
"ds_read_b128 a[80:83], v213 offset:4096\n" \
"s_waitcnt lgkmcnt(4)\n" \
"v_mfma_f32_16x16x32_bf16 v[60:63], a[124:127], a[92:95], v[60:63]\n" \
"ds_read_b128 a[84:87], v213 offset:6144\n" \
"v_max_f32 v180, v180, v151\n" \
"v_cvt_pk_bf16_f32 v76, v92, v93\n" \
"v_cvt_pk_bf16_f32 v77, v94, v95\n" \
"v_mfma_f32_16x16x32_bf16 v[56:59], a[112:115], a[88:91], 0\n" \
"buffer_load_dwordx4 v234, s[56:59], s81 offen offset:3072 sc1 lds\n" \
"v_mfma_f32_16x16x32_bf16 v[56:59], a[116:119], a[92:95], v[56:59]\n" \
"s_getpc_b64 s[44:45]\n" \
"Lop_%=:\n" \
"s_add_u32 s44, s44, Lor_%=-Lop_%=\n" \
"s_addc_u32 s45, s45, 0\n" \
"v_permlane32_swap_b32 v180, v181\n" \
"v_permlane32_swap_b32 v182, v183\n" \
"v_max_f32 v180, v180, v181\n" \
"v_mfma_f32_16x16x32_bf16 v[0:3], a[96:99], a[64:67], v[0:3]\n" \
"ds_read_b128 a[88:91], v213 offset:4352\n" \
"v_max_f32 v182, v182, v183\n" \
"v_mfma_f32_16x16x32_bf16 v[0:3], a[100:103], a[68:71], v[0:3]\n" \
"s_waitcnt lgkmcnt(2)\n" \
"ds_read_b128 a[92:95], v213 offset:6400\n" \
"v_cvt_pk_bf16_f32 v198, v96, v97\n" \
"v_cvt_pk_bf16_f32 v199, v98, v99\n" \
"v_permlane16_swap_b32 v180, v182\n" \
"v_mfma_f32_16x16x32_bf16 v[4:7], a[104:107], a[64:67], v[4:7]\n" \
"ds_read_b64_tr_b16 a[112:113], v214 offset:16896\n" \
"v_max_f32 v180, v180, v182\n" \
"v_mfma_f32_16x16x32_bf16 v[4:7], a[108:111], a[68:71], v[4:7]\n" \
"ds_read_b64_tr_b16 a[114:115], v214 offset:18944\n" \
"v_mul_f32 v180, v180, v144\n" \
"v_cvt_pk_bf16_f32 v78, v100, v101\n" \
"v_cvt_pk_bf16_f32 v79, v102, v103\n" \
"v_mov_b32 v182, v180\n" \
"v_mfma_f32_16x16x32_bf16 v[20:23], a[104:107], a[72:75], v[20:23]\n" \
"s_add_u32 m0, m0, 0x4000\n" \
"ds_read_b64_tr_b16 a[116:117], v214 offset:25088\n" \
"v_cvt_pk_bf16_f32 v200, v104, v105\n" \
"v_mfma_f32_16x16x32_bf16 v[20:23], a[108:111], a[76:79], v[20:23]\n" \
"buffer_load_dwordx4 v216, s[36:39], s82 offen offset:0 lds\n" \
"v_cvt_pk_bf16_f32 v201, v106, v107\n" \
"v_permlane16_swap_b32 v180, v182\n" \
"v_mfma_f32_16x16x32_bf16 v[16:19], a[96:99], a[72:75], v[16:19]\n" \
"s_sub_u32 s85, s85, 32768\n" \
"s_cselect_b32 s85, 131072, s85\n" \
"ds_read_b64_tr_b16 a[118:119], v214 offset:27136\n" \
"v_mfma_f32_16x16x32_bf16 v[16:19], a[100:103], a[76:79], v[16:19]\n" \
"s_waitcnt lgkmcnt(3)\n" \
"ds_read_b64_tr_b16 a[120:121], v215 offset:16896\n" \
"v_mov_b32 v181, v180\n" \
"v_mov_b32 v183, v182\n" \
"v_cvt_pk_bf16_f32 v80, v108, v109\n" \
"v_cvt_pk_bf16_f32 v81, v110, v111\n" \
"v_mfma_f32_16x16x32_bf16 v[32:35], a[96:99], a[80:83], v[32:35]\n" \
"s_add_u32 s48, s48, 0x1000\n" \
"ds_read_b64_tr_b16 a[122:123], v215 offset:18944\n" \
"v_permlane32_swap_b32 v180, v181\n" \
"v_mfma_f32_16x16x32_bf16 v[32:35], a[100:103], a[84:87], v[32:35]\n" \
"buffer_load_dwordx4 v217, s[36:39], s82 offen offset:1024 lds\n" \
"v_permlane32_swap_b32 v182, v183\n" \
"v_fma_f32 v172, v172, v144, -v183\n" \
"v_mfma_f32_16x16x32_bf16 v[36:39], a[104:107], a[80:83], v[36:39]\n" \
"s_sub_u32 s86, s86, 32768\n" \
"s_cselect_b32 s86, 131072, s86\n" \
"ds_read_b64_tr_b16 a[124:125], v215 offset:25088\n" \
"v_add_u32 v213, s85, v211\n" \
"v_fma_f32 v164, v164, v144, -v182\n" \
"v_mfma_f32_16x16x32_bf16 v[36:39], a[108:111], a[84:87], v[36:39]\n" \
"buffer_load_dwordx4 v218, s[36:39], s82 offen offset:2048 lds\n" \
"v_fma_f32 v156, v156, v144, -v181\n" \
"v_fma_f32 v148, v148, v144, -v180\n" \
"v_mfma_f32_16x16x32_bf16 v[52:55], a[104:107], a[88:91], v[52:55]\n" \
"ds_read_b64_tr_b16 a[126:127], v215 offset:27136\n" \
"v_fma_f32 v173, v173, v144, -v183\n" \
"v_mfma_f32_16x16x32_bf16 v[52:55], a[108:111], a[92:95], v[52:55]\n" \
"buffer_load_dwordx4 v219, s[36:39], s82 offen offset:3072 lds\n" \
"v_fma_f32 v165, v165, v144, -v182\n" \
"v_fma_f32 v157, v157, v144, -v181\n" \
"v_mfma_f32_16x16x32_bf16 v[48:51], a[96:99], a[88:91], v[48:51]\n" \
"s_xor_b32 s82, s48, s49\n" \
"s_cmp_lt_u32 s48, s50\n" \
"s_cselect_b32 s82, s82, s48\n" \
"v_add_u32 v214, s85, v212\n" \
"v_xor_b32 v215, 32, v214\n" \
"v_fma_f32 v149, v149, v144, -v180\n" \
"v_mfma_f32_16x16x32_bf16 v[48:51], a[100:103], a[92:95], v[48:51]\n" \
"s_add_u32 s81, s82, s46\n" \
"v_fma_f32 v174, v174, v144, -v183\n" \
"v_fma_f32 v166, v166, v144, -v182\n" \
"s_waitcnt vmcnt(16) lgkmcnt(0)\n" \
"s_barrier\n" \
"v_mfma_f32_16x16x32_bf16 v[8:11], a[112:115], a[64:67], v[8:11]\n" \
"s_add_u32 m0, s86, s79\n" \
"ds_read_b64_tr_b16 a[96:97], v214 offset:16384\n" \
"ds_read_b64_tr_b16 a[98:99], v214 offset:18432\n" \
"v_mfma_f32_16x16x32_bf16 v[8:11], a[116:119], a[68:71], v[8:11]\n" \
"buffer_load_dwordx4 v234, s[56:59], s81 offen offset:0 sc1 lds\n" \
"v_fma_f32 v158, v158, v144, -v181\n" \
"v_fma_f32 v150, v150, v144, -v180\n" \
"v_mfma_f32_16x16x32_bf16 v[12:15], a[120:123], a[64:67], v[12:15]\n" \
"ds_read_b64_tr_b16 a[100:101], v214 offset:24576\n" \
"ds_read_b64_tr_b16 a[102:103], v214 offset:26624\n" \
"v_mfma_f32_16x16x32_bf16 v[12:15], a[124:127], a[68:71], v[12:15]\n" \
"s_lshl_b32 s82, s82, 5\n" \
"ds_read_b128 a[64:67], v213 offset:0\n" \
"v_fma_f32 v175, v175, v144, -v183\n" \
"v_fma_f32 v167, v167, v144, -v182\n" \
"v_fma_f32 v159, v159, v144, -v181\n" \
"v_fma_f32 v151, v151, v144, -v180\n" \
"v_mfma_f32_16x16x32_bf16 v[28:31], a[120:123], a[72:75], v[28:31]\n" \
"s_add_u32 s82, s82, s47\n" \
"ds_read_b128 a[68:71], v213 offset:2048\n" \
"v_mfma_f32_16x16x32_bf16 v[28:31], a[124:127], a[76:79], v[28:31]\n" \
"buffer_load_dwordx4 v234, s[56:59], s81 offen offset:1024 sc1 lds\n" \
"v_exp_f32 v172, v172\n" \
"v_mfma_f32_16x16x32_bf16 v[24:27], a[112:115], a[72:75], v[24:27]\n" \
"ds_read_b64_tr_b16 a[104:105], v215 offset:16384\n" \
"ds_read_b64_tr_b16 a[106:107], v215 offset:18432\n" \
"v_mfma_f32_16x16x32_bf16 v[24:27], a[116:119], a[76:79], v[24:27]\n" \
"ds_read_b128 a[72:75], v213 offset:256\n" \
"v_exp_f32 v164, v164\n" \
"v_exp_f32 v156, v156\n" \
"v_mfma_f32_16x16x32_bf16 v[40:43], a[112:115], a[80:83], v[40:43]\n" \
"ds_read_b128 a[76:79], v213 offset:2304\n" \
"v_mfma_f32_16x16x32_bf16 v[40:43], a[116:119], a[84:87], v[40:43]\n" \
"buffer_load_dwordx4 v234, s[56:59], s81 offen offset:2048 sc1 lds\n" \
"v_exp_f32 v148, v148\n" \
"v_mfma_f32_16x16x32_bf16 v[44:47], a[120:123], a[80:83], v[44:47]\n" \
"ds_read_b64_tr_b16 a[108:109], v215 offset:24576\n" \
"ds_read_b64_tr_b16 a[110:111], v215 offset:26624\n" \
"v_mfma_f32_16x16x32_bf16 v[44:47], a[124:127], a[84:87], v[44:47]\n" \
"buffer_load_dwordx4 v234, s[56:59], s81 offen offset:3072 sc1 lds\n" \
"v_exp_f32 v173, v173\n" \
"v_mfma_f32_16x16x32_bf16 v[60:63], a[120:123], a[88:91], v[60:63]\n" \
"ds_read_b128 a[80:83], v213 offset:4096\n" \
"s_waitcnt lgkmcnt(4)\n" \
"v_mfma_f32_16x16x32_bf16 v[60:63], a[124:127], a[92:95], v[60:63]\n" \
"s_add_u32 m0, m0, 0x4000\n" \
"ds_read_b128 a[84:87], v213 offset:6144\n" \
"v_exp_f32 v165, v165\n" \
"v_exp_f32 v157, v157\n" \
"v_mfma_f32_16x16x32_bf16 v[56:59], a[112:115], a[88:91], v[56:59]\n" \
"buffer_load_dwordx4 v216, s[36:39], s82 offen offset:0 lds\n" \
"v_mfma_f32_16x16x32_bf16 v[56:59], a[116:119], a[92:95], v[56:59]\n" \
"v_exp_f32 v149, v149\n" \
"v_exp_f32 v174, v174\n" \
"v_mfma_f32_16x16x32_bf16 v[0:3], a[96:99], a[64:67], v[0:3]\n" \
"ds_read_b128 a[88:91], v213 offset:4352\n" \
"v_mfma_f32_16x16x32_bf16 v[0:3], a[100:103], a[68:71], v[0:3]\n" \
"s_waitcnt lgkmcnt(2)\n" \
"ds_read_b128 a[92:95], v213 offset:6400\n" \
"v_exp_f32 v166, v166\n" \
"v_exp_f32 v158, v158\n" \
"v_mfma_f32_16x16x32_bf16 v[4:7], a[104:107], a[64:67], v[4:7]\n" \
"ds_read_b64_tr_b16 a[112:113], v214 offset:16896\n" \
"v_mfma_f32_16x16x32_bf16 v[4:7], a[108:111], a[68:71], v[4:7]\n" \
"ds_read_b64_tr_b16 a[114:115], v214 offset:18944\n" \
"v_exp_f32 v150, v150\n" \
"v_exp_f32 v175, v175\n" \
"v_mfma_f32_16x16x32_bf16 v[20:23], a[104:107], a[72:75], v[20:23]\n" \
"s_add_u32 s48, s48, 0x1000\n" \
"ds_read_b64_tr_b16 a[116:117], v214 offset:25088\n" \
"v_mfma_f32_16x16x32_bf16 v[20:23], a[108:111], a[76:79], v[20:23]\n" \
"buffer_load_dwordx4 v217, s[36:39], s82 offen offset:1024 lds\n" \
"v_exp_f32 v167, v167\n" \
"v_mfma_f32_16x16x32_bf16 v[16:19], a[96:99], a[72:75], v[16:19]\n" \
"s_sub_u32 s85, s85, 32768\n" \
"s_cselect_b32 s85, 131072, s85\n" \
"ds_read_b64_tr_b16 a[118:119], v214 offset:27136\n" \
"v_mfma_f32_16x16x32_bf16 v[16:19], a[100:103], a[76:79], v[16:19]\n" \
"s_waitcnt lgkmcnt(3)\n" \
"ds_read_b64_tr_b16 a[120:121], v215 offset:16896\n" \
"v_exp_f32 v159, v159\n" \
"v_exp_f32 v151, v151\n" \
"v_mfma_f32_16x16x32_bf16 v[32:35], a[96:99], a[80:83], v[32:35]\n" \
"s_sub_u32 s86, s86, 32768\n" \
"s_cselect_b32 s86, 131072, s86\n" \
"ds_read_b64_tr_b16 a[122:123], v215 offset:18944\n" \
"v_mfma_f32_16x16x32_bf16 v[32:35], a[100:103], a[84:87], v[32:35]\n" \
"buffer_load_dwordx4 v218, s[36:39], s82 offen offset:2048 lds\n" \
"v_add_f32 v191, v172, v173\n" \
"v_add_f32 v190, v164, v165\n" \
"v_mfma_f32_16x16x32_bf16 v[36:39], a[104:107], a[80:83], v[36:39]\n" \
"ds_read_b64_tr_b16 a[124:125], v215 offset:25088\n" \
"v_add_u32 v213, s85, v211\n" \
"v_mfma_f32_16x16x32_bf16 v[36:39], a[108:111], a[84:87], v[36:39]\n" \
"buffer_load_dwordx4 v219, s[36:39], s82 offen offset:3072 lds\n" \
"v_add_f32 v189, v156, v157\n" \
"v_add_f32 v188, v148, v149\n" \
"v_add_f32 v191, v191, v174\n" \
"v_mfma_f32_16x16x32_bf16 v[52:55], a[104:107], a[88:91], v[52:55]\n" \
"s_xor_b32 s82, s48, s49\n" \
"s_cmp_lt_u32 s48, s50\n" \
"s_cselect_b32 s82, s82, s48\n" \
"s_add_u32 s81, s82, s46\n" \
"s_add_u32 m0, s86, s79\n" \
"ds_read_b64_tr_b16 a[126:127], v215 offset:27136\n" \
"v_add_f32 v190, v190, v166\n" \
"v_mfma_f32_16x16x32_bf16 v[52:55], a[108:111], a[92:95], v[52:55]\n" \
"buffer_load_dwordx4 v234, s[56:59], s81 offen offset:0 sc1 lds\n" \
"v_add_f32 v189, v189, v158\n" \
"v_mfma_f32_16x16x32_bf16 v[48:51], a[96:99], a[88:91], v[48:51]\n" \
"v_add_u32 v214, s85, v212\n" \
"v_xor_b32 v215, 32, v214\n" \
"v_mfma_f32_16x16x32_bf16 v[48:51], a[100:103], a[92:95], v[48:51]\n" \
"s_lshl_b32 s82, s82, 5\n" \
"v_add_f32 v188, v188, v150\n" \
"v_add_f32 v191, v191, v175\n" \
"v_add_f32 v190, v190, v167\n" \
"s_waitcnt vmcnt(17) lgkmcnt(0)\n" \
"s_barrier\n" \
"v_mfma_f32_16x16x32_bf16 v[8:11], a[112:115], a[64:67], v[8:11]\n" \
"s_add_u32 s82, s82, s47\n" \
"ds_read_b64_tr_b16 a[96:97], v214 offset:16384\n" \
"ds_read_b64_tr_b16 a[98:99], v214 offset:18432\n" \
"v_mfma_f32_16x16x32_bf16 v[8:11], a[116:119], a[68:71], v[8:11]\n" \
"buffer_load_dwordx4 v234, s[56:59], s81 offen offset:1024 sc1 lds\n" \
"v_add_f32 v189, v189, v159\n" \
"v_add_f32 v188, v188, v151\n" \
"v_mfma_f32_16x16x32_bf16 v[12:15], a[120:123], a[64:67], v[12:15]\n" \
"ds_read_b64_tr_b16 a[100:101], v214 offset:24576\n" \
"ds_read_b64_tr_b16 a[102:103], v214 offset:26624\n" \
"v_mfma_f32_16x16x32_bf16 v[12:15], a[124:127], a[68:71], v[12:15]\n" \
"ds_read_b128 a[64:67], v213 offset:0\n" \
"v_permlane32_swap_b32 v188, v189\n" \
"v_permlane32_swap_b32 v190, v191\n" \
"v_add_f32 v188, v188, v189\n" \
"v_add_f32 v190, v190, v191\n" \
"v_mfma_f32_16x16x32_bf16 v[28:31], a[120:123], a[72:75], v[28:31]\n" \
"ds_read_b128 a[68:71], v213 offset:2048\n" \
"v_mfma_f32_16x16x32_bf16 v[28:31], a[124:127], a[76:79], v[28:31]\n" \
"buffer_load_dwordx4 v234, s[56:59], s81 offen offset:2048 sc1 lds\n" \
"v_permlane16_swap_b32 v188, v190\n" \
"v_add_f32 v188, v188, v190\n" \
"v_mfma_f32_16x16x32_bf16 v[24:27], a[112:115], a[72:75], v[24:27]\n" \
"ds_read_b64_tr_b16 a[104:105], v215 offset:16384\n" \
"ds_read_b64_tr_b16 a[106:107], v215 offset:18432\n" \
"v_mfma_f32_16x16x32_bf16 v[24:27], a[116:119], a[76:79], v[24:27]\n" \
"ds_read_b128 a[72:75], v213 offset:256\n" \
"v_rcp_f32 v188, v188\n" \
"v_mfma_f32_16x16x32_bf16 v[40:43], a[112:115], a[80:83], v[40:43]\n" \
"ds_read_b128 a[76:79], v213 offset:2304\n" \
"v_mfma_f32_16x16x32_bf16 v[40:43], a[116:119], a[84:87], v[40:43]\n" \
"buffer_load_dwordx4 v234, s[56:59], s81 offen offset:3072 sc1 lds\n" \
"v_mov_b32 v190, v188\n" \
"v_mfma_f32_16x16x32_bf16 v[44:47], a[120:123], a[80:83], v[44:47]\n" \
"s_add_u32 m0, m0, 0x4000\n" \
"ds_read_b64_tr_b16 a[108:109], v215 offset:24576\n" \
"ds_read_b64_tr_b16 a[110:111], v215 offset:26624\n" \
"v_mfma_f32_16x16x32_bf16 v[44:47], a[124:127], a[84:87], v[44:47]\n" \
"buffer_load_dwordx4 v216, s[36:39], s82 offen offset:0 lds\n" \
"v_permlane16_swap_b32 v188, v190\n" \
"v_mov_b32 v189, v188\n" \
"v_mov_b32 v191, v190\n" \
"v_mfma_f32_16x16x32_bf16 v[60:63], a[120:123], a[88:91], v[60:63]\n" \
"ds_read_b128 a[80:83], v213 offset:4096\n" \
"s_waitcnt lgkmcnt(4)\n" \
"v_mfma_f32_16x16x32_bf16 v[60:63], a[124:127], a[92:95], v[60:63]\n" \
"s_add_u32 s48, s48, 0x1000\n" \
"ds_read_b128 a[84:87], v213 offset:6144\n" \
"v_permlane32_swap_b32 v188, v189\n" \
"v_permlane32_swap_b32 v190, v191\n" \
"v_mul_f32 v172, v172, v191\n" \
"v_mul_f32 v164, v164, v190\n" \
"v_mul_f32 v156, v156, v189\n" \
"v_mul_f32 v148, v148, v188\n" \
"v_mfma_f32_16x16x32_bf16 v[56:59], a[112:115], a[88:91], v[56:59]\n" \
"buffer_load_dwordx4 v217, s[36:39], s82 offen offset:1024 lds\n" \
"v_mfma_f32_16x16x32_bf16 v[56:59], a[116:119], a[92:95], v[56:59]\n" \
"v_mul_f32 v173, v173, v191\n" \
"v_mul_f32 v165, v165, v190\n" \
"v_mul_f32 v157, v157, v189\n" \
"v_mul_f32 v149, v149, v188\n" \
"v_mul_f32 v174, v174, v191\n" \
"v_mfma_f32_16x16x32_bf16 v[0:3], a[96:99], a[64:67], v[0:3]\n" \
"ds_read_b128 a[88:91], v213 offset:4352\n" \
"v_mul_f32 v166, v166, v190\n" \
"v_mfma_f32_16x16x32_bf16 v[0:3], a[100:103], a[68:71], v[0:3]\n" \
"s_waitcnt lgkmcnt(2)\n" \
"ds_read_b128 a[92:95], v213 offset:6400\n" \
"v_mul_f32 v158, v158, v189\n" \
"v_mul_f32 v150, v150, v188\n" \
"v_mul_f32 v175, v175, v191\n" \
"v_mfma_f32_16x16x32_bf16 v[4:7], a[104:107], a[64:67], v[4:7]\n" \
"ds_read_b64_tr_b16 a[112:113], v214 offset:16896\n" \
"v_mul_f32 v167, v167, v190\n" \
"v_mfma_f32_16x16x32_bf16 v[4:7], a[108:111], a[68:71], v[4:7]\n" \
"ds_read_b64_tr_b16 a[114:115], v214 offset:18944\n" \
"v_mul_f32 v159, v159, v189\n" \
"v_mul_f32 v151, v151, v188\n" \
"v_cvt_pk_bf16_f32 v178, v172, v173\n" \
"v_cvt_pk_bf16_f32 v179, v174, v175\n" \
"v_mfma_f32_16x16x32_bf16 v[20:23], a[104:107], a[72:75], v[20:23]\n" \
"s_sub_u32 s86, s86, 32768\n" \
"s_cselect_b32 s86, 131072, s86\n" \
"ds_read_b64_tr_b16 a[116:117], v214 offset:25088\n" \
"v_mfma_f32_16x16x32_bf16 v[20:23], a[108:111], a[76:79], v[20:23]\n" \
"buffer_load_dwordx4 v218, s[36:39], s82 offen offset:2048 lds\n" \
"v_cvt_pk_bf16_f32 v170, v164, v165\n" \
"v_cvt_pk_bf16_f32 v171, v166, v167\n" \
"v_mfma_f32_16x16x32_bf16 v[16:19], a[96:99], a[72:75], v[16:19]\n" \
"s_sub_u32 s85, s85, 32768\n" \
"s_cselect_b32 s85, 131072, s85\n" \
"ds_read_b64_tr_b16 a[118:119], v214 offset:27136\n" \
"v_mfma_f32_16x16x32_bf16 v[16:19], a[100:103], a[76:79], v[16:19]\n" \
"s_waitcnt lgkmcnt(3)\n" \
"ds_read_b64_tr_b16 a[120:121], v215 offset:16896\n" \
"v_cvt_pk_bf16_f32 v162, v156, v157\n" \
"v_cvt_pk_bf16_f32 v163, v158, v159\n" \
"v_cvt_pk_bf16_f32 v154, v148, v149\n" \
"v_cvt_pk_bf16_f32 v155, v150, v151\n" \
"v_mfma_f32_16x16x32_bf16 v[32:35], a[96:99], a[80:83], v[32:35]\n" \
"ds_read_b64_tr_b16 a[122:123], v215 offset:18944\n" \
"v_and_b32 v176, v178, v184\n" \
"v_mfma_f32_16x16x32_bf16 v[32:35], a[100:103], a[84:87], v[32:35]\n" \
"buffer_load_dwordx4 v219, s[36:39], s82 offen offset:3072 lds\n" \
"v_and_b32 v177, v179, v185\n" \
"v_xor_b32 v178, v178, v176\n" \
"v_mfma_f32_16x16x32_bf16 v[36:39], a[104:107], a[80:83], v[36:39]\n" \
"s_xor_b32 s82, s48, s49\n" \
"s_cmp_lt_u32 s48, s50\n" \
"s_cselect_b32 s82, s82, s48\n" \
"s_add_u32 s81, s82, s46\n" \
"s_add_u32 m0, s86, s79\n" \
"ds_read_b64_tr_b16 a[124:125], v215 offset:25088\n" \
"v_add_u32 v213, s85, v211\n" \
"v_xor_b32 v179, v179, v177\n" \
"v_mfma_f32_16x16x32_bf16 v[36:39], a[108:111], a[84:87], v[36:39]\n" \
"buffer_load_dwordx4 v234, s[56:59], s81 offen offset:0 sc1 lds\n" \
"v_and_b32 v168, v170, v184\n" \
"v_and_b32 v169, v171, v185\n" \
"v_mfma_f32_16x16x32_bf16 v[52:55], a[104:107], a[88:91], v[52:55]\n" \
"s_lshl_b32 s82, s82, 5\n" \
"s_add_u32 s82, s82, s47\n" \
"ds_read_b64_tr_b16 a[126:127], v215 offset:27136\n" \
"v_mfma_f32_16x16x32_bf16 v[52:55], a[108:111], a[92:95], v[52:55]\n" \
"buffer_load_dwordx4 v234, s[56:59], s81 offen offset:1024 sc1 lds\n" \
"v_xor_b32 v170, v170, v168\n" \
"v_xor_b32 v171, v171, v169\n" \
"v_mfma_f32_16x16x32_bf16 v[48:51], a[96:99], a[88:91], v[48:51]\n" \
"v_add_u32 v214, s85, v212\n" \
"v_xor_b32 v215, 32, v214\n" \
"v_mfma_f32_16x16x32_bf16 v[48:51], a[100:103], a[92:95], v[48:51]\n" \
"v_and_b32 v160, v162, v184\n" \
"v_and_b32 v161, v163, v185\n" \
"v_xor_b32 v162, v162, v160\n" \
"s_waitcnt vmcnt(18) lgkmcnt(0)\n" \
"s_barrier\n" \
"v_mfma_f32_16x16x32_bf16 v[8:11], a[112:115], a[64:67], v[8:11]\n" \
"ds_read_b64_tr_b16 a[96:97], v214 offset:16384\n" \
"ds_read_b64_tr_b16 a[98:99], v214 offset:18432\n" \
"v_mfma_f32_16x16x32_bf16 v[8:11], a[116:119], a[68:71], v[8:11]\n" \
"buffer_load_dwordx4 v234, s[56:59], s81 offen offset:2048 sc1 lds\n" \
"v_xor_b32 v163, v163, v161\n" \
"v_and_b32 v152, v154, v184\n" \
"v_mfma_f32_16x16x32_bf16 v[12:15], a[120:123], a[64:67], v[12:15]\n" \
"ds_read_b64_tr_b16 a[100:101], v214 offset:24576\n" \
"ds_read_b64_tr_b16 a[102:103], v214 offset:26624\n" \
"v_mfma_f32_16x16x32_bf16 v[12:15], a[124:127], a[68:71], v[12:15]\n" \
"ds_read_b128 a[64:67], v213 offset:0\n" \
"v_and_b32 v153, v155, v185\n" \
"v_xor_b32 v154, v154, v152\n" \
"v_xor_b32 v155, v155, v153\n" \
"v_mfma_f32_16x16x32_bf16 v[28:31], a[120:123], a[72:75], v[28:31]\n" \
"ds_read_b128 a[68:71], v213 offset:2048\n" \
"v_mfma_f32_16x16x32_bf16 v[136:139], v[198:201], v[176:179], 0\n" \
"v_mfma_f32_16x16x32_bf16 v[140:143], v[78:81], v[176:179], 0\n" \
"v_mfma_f32_16x16x32_bf16 v[128:131], v[196:199], v[168:171], 0\n" \
"v_mfma_f32_16x16x32_bf16 v[132:135], v[76:79], v[168:171], 0\n" \
"v_mfma_f32_16x16x32_bf16 v[120:123], v[194:197], v[160:163], 0\n" \
"v_mfma_f32_16x16x32_bf16 v[124:127], v[74:77], v[160:163], 0\n" \
"v_mfma_f32_16x16x32_bf16 v[112:115], v[192:195], v[152:155], 0\n" \
"v_mfma_f32_16x16x32_bf16 v[116:119], v[72:75], v[152:155], 0\n" \
"v_mfma_f32_16x16x32_bf16 v[28:31], a[124:127], a[76:79], v[28:31]\n" \
"buffer_load_dwordx4 v234, s[56:59], s81 offen offset:3072 sc1 lds\n" \
"v_mfma_f32_16x16x32_bf16 v[24:27], a[112:115], a[72:75], v[24:27]\n" \
"ds_read_b64_tr_b16 a[104:105], v215 offset:16384\n" \
"ds_read_b64_tr_b16 a[106:107], v215 offset:18432\n" \
"v_mfma_f32_16x16x32_bf16 v[24:27], a[116:119], a[76:79], v[24:27]\n" \
"ds_read_b128 a[72:75], v213 offset:256\n" \
"v_cvt_pk_bf16_f32 v136, v136, v137\n" \
"v_cvt_pk_bf16_f32 v137, v138, v139\n" \
"v_cvt_pk_bf16_f32 v138, v140, v141\n" \
"v_cvt_pk_bf16_f32 v139, v142, v143\n" \
"v_cndmask_b32 v140, v136, v138, s[76:77]\n" \
"v_mfma_f32_16x16x32_bf16 v[40:43], a[112:115], a[80:83], v[40:43]\n" \
"s_add_u32 m0, m0, 0x4000\n" \
"ds_read_b128 a[76:79], v213 offset:2304\n" \
"v_cndmask_b32 v141, v137, v139, s[76:77]\n" \
"v_mfma_f32_16x16x32_bf16 v[40:43], a[116:119], a[84:87], v[40:43]\n" \
"buffer_load_dwordx4 v216, s[36:39], s82 offen offset:0 lds\n" \
"v_cndmask_b32 v142, v138, v136, s[76:77]\n" \
"v_cndmask_b32 v143, v139, v137, s[76:77]\n" \
"v_mfma_f32_16x16x32_bf16 v[44:47], a[120:123], a[80:83], v[44:47]\n" \
"s_add_u32 s48, s48, 0x1000\n" \
"ds_read_b64_tr_b16 a[108:109], v215 offset:24576\n" \
"ds_read_b64_tr_b16 a[110:111], v215 offset:26624\n" \
"v_mfma_f32_16x16x32_bf16 v[44:47], a[124:127], a[84:87], v[44:47]\n" \
"buffer_load_dwordx4 v217, s[36:39], s82 offen offset:1024 lds\n" \
"v_cvt_pk_bf16_f32 v128, v128, v129\n" \
"v_cvt_pk_bf16_f32 v129, v130, v131\n" \
"v_cvt_pk_bf16_f32 v130, v132, v133\n" \
"v_mfma_f32_16x16x32_bf16 v[60:63], a[120:123], a[88:91], v[60:63]\n" \
"ds_read_b128 a[80:83], v213 offset:4096\n" \
"s_waitcnt lgkmcnt(4)\n" \
"v_mfma_f32_16x16x32_bf16 v[60:63], a[124:127], a[92:95], v[60:63]\n" \
"s_sub_u32 s86, s86, 32768\n" \
"s_cselect_b32 s86, 131072, s86\n" \
"ds_read_b128 a[84:87], v213 offset:6144\n" \
"v_cvt_pk_bf16_f32 v131, v134, v135\n" \
"v_cndmask_b32 v132, v128, v130, s[76:77]\n" \
"v_mfma_f32_16x16x32_bf16 v[56:59], a[112:115], a[88:91], v[56:59]\n" \
"buffer_load_dwordx4 v218, s[36:39], s82 offen offset:2048 lds\n" \
"v_mfma_f32_16x16x32_bf16 v[56:59], a[116:119], a[92:95], v[56:59]\n" \
"v_cndmask_b32 v133, v129, v131, s[76:77]\n" \
"v_cndmask_b32 v134, v130, v128, s[76:77]\n" \
"v_cndmask_b32 v135, v131, v129, s[76:77]\n" \
"v_cvt_pk_bf16_f32 v120, v120, v121\n" \
"v_cvt_pk_bf16_f32 v121, v122, v123\n" \
"v_mfma_f32_16x16x32_bf16 v[0:3], a[96:99], a[64:67], v[0:3]\n" \
"ds_read_b128 a[88:91], v213 offset:4352\n" \
"v_cvt_pk_bf16_f32 v122, v124, v125\n" \
"v_mfma_f32_16x16x32_bf16 v[0:3], a[100:103], a[68:71], v[0:3]\n" \
"s_waitcnt lgkmcnt(2)\n" \
"ds_read_b128 a[92:95], v213 offset:6400\n" \
"v_cvt_pk_bf16_f32 v123, v126, v127\n" \
"v_cndmask_b32 v124, v120, v122, s[76:77]\n" \
"v_cndmask_b32 v125, v121, v123, s[76:77]\n" \
"v_mfma_f32_16x16x32_bf16 v[4:7], a[104:107], a[64:67], v[4:7]\n" \
"ds_read_b64_tr_b16 a[112:113], v214 offset:16896\n" \
"v_cndmask_b32 v126, v122, v120, s[76:77]\n" \
"v_mfma_f32_16x16x32_bf16 v[4:7], a[108:111], a[68:71], v[4:7]\n" \
"Lpg10211_%=:\n" \
"s_cbranch_execz Lcb1_%=\n" \
"ds_read_b64_tr_b16 a[114:115], v214 offset:18944\n" \
"v_cndmask_b32 v127, v123, v121, s[76:77]\n" \
"v_cvt_pk_bf16_f32 v112, v112, v113\n" \
"v_cvt_pk_bf16_f32 v113, v114, v115\n" \
"v_cvt_pk_bf16_f32 v114, v116, v117\n" \
"v_mfma_f32_16x16x32_bf16 v[20:23], a[104:107], a[72:75], v[20:23]\n" \
"s_waitcnt lgkmcnt(4)\n" \
"ds_read_b64_tr_b16 a[116:117], v214 offset:25088\n" \
"v_cvt_pk_bf16_f32 v115, v118, v119\n" \
"v_mfma_f32_16x16x32_bf16 v[20:23], a[108:111], a[76:79], v[20:23]\n" \
"buffer_load_dwordx4 v219, s[36:39], s82 offen offset:3072 lds\n" \
"v_cndmask_b32 v116, v112, v114, s[76:77]\n" \
"v_cndmask_b32 v117, v113, v115, s[76:77]\n" \
"v_mfma_f32_16x16x32_bf16 v[16:19], a[96:99], a[72:75], v[16:19]\n" \
"s_sub_u32 s85, s85, 32768\n" \
"s_cselect_b32 s85, 131072, s85\n" \
"s_xor_b32 s82, s48, s49\n" \
"s_cmp_lt_u32 s48, s50\n" \
"s_cselect_b32 s82, s82, s48\n" \
"ds_read_b64_tr_b16 a[118:119], v214 offset:27136\n" \
"v_cndmask_b32 v118, v114, v112, s[76:77]\n" \
"v_mfma_f32_16x16x32_bf16 v[16:19], a[100:103], a[76:79], v[16:19]\n" \
"s_add_u32 s81, s82, s46\n" \
"ds_read_b64_tr_b16 a[120:121], v215 offset:16896\n" \
"v_cndmask_b32 v119, v115, v113, s[76:77]\n" \
"s_add_u32 s71, s93, 208896\n" \
"v_mfma_f32_16x16x32_bf16 v[32:35], a[96:99], a[80:83], v[32:35]\n" \
"s_add_u32 m0, s86, s79\n" \
"ds_read_b64_tr_b16 a[122:123], v215 offset:18944\n" \
"buffer_store_dwordx4 v[140:143], v236, s[56:59], s71 offen sc1\n" \
"s_add_u32 s71, s93, 139264\n" \
"v_mfma_f32_16x16x32_bf16 v[32:35], a[100:103], a[84:87], v[32:35]\n" \
"buffer_load_dwordx4 v234, s[56:59], s81 offen offset:0 sc1 lds\n" \
"v_mfma_f32_16x16x32_bf16 v[36:39], a[104:107], a[80:83], v[36:39]\n" \
"s_lshl_b32 s82, s82, 5\n" \
"s_add_u32 s82, s82, s47\n" \
"ds_read_b64_tr_b16 a[124:125], v215 offset:25088\n" \
"buffer_store_dwordx4 v[132:135], v236, s[56:59], s71 offen sc1\n" \
"s_add_u32 s71, s93, 69632\n" \
"v_add_u32_e64 v213, s85, v211\n" \
"v_mfma_f32_16x16x32_bf16 v[36:39], a[108:111], a[84:87], v[36:39]\n" \
"s_waitcnt lgkmcnt(3)\n" \
"buffer_load_dwordx4 v234, s[56:59], s81 offen offset:1024 sc1 lds\n" \
"v_mfma_f32_16x16x32_bf16 v[52:55], a[104:107], a[88:91], v[52:55]\n" \
"ds_read_b64_tr_b16 a[126:127], v215 offset:27136\n" \
"buffer_store_dwordx4 v[124:127], v236, s[56:59], s71 offen sc1\n" \
"s_add_u32 s71, s93, 0\n" \
"v_mfma_f32_16x16x32_bf16 v[52:55], a[108:111], a[92:95], v[52:55]\n" \
"buffer_load_dwordx4 v234, s[56:59], s81 offen offset:2048 sc1 lds\n" \
"v_mfma_f32_16x16x32_bf16 v[48:51], a[96:99], a[88:91], v[48:51]\n" \
"v_add_u32_e64 v214, s85, v212\n" \
"v_xor_b32_e64 v215, 32, v214\n" \
"v_mfma_f32_16x16x32_bf16 v[48:51], a[100:103], a[92:95], v[48:51]\n" \
"buffer_store_dwordx4 v[116:119], v236, s[56:59], s71 offen sc1\n" \
"s_waitcnt vmcnt(23) lgkmcnt(0)\n" \
"s_barrier\n" \
"v_mfma_f32_16x16x32_bf16 v[8:11], a[112:115], a[64:67], v[8:11]\n" \
"ds_read_b64_tr_b16 a[96:97], v214 offset:16384\n" \
"ds_read_b64_tr_b16 a[98:99], v214 offset:18432\n" \
"v_mfma_f32_16x16x32_bf16 v[8:11], a[116:119], a[68:71], v[8:11]\n" \
"buffer_load_dwordx4 v234, s[56:59], s81 offen offset:3072 sc1 lds\n" \
"v_mfma_f32_16x16x32_bf16 v[12:15], a[120:123], a[64:67], v[12:15]\n" \
"ds_read_b64_tr_b16 a[100:101], v214 offset:24576\n" \
"ds_read_b64_tr_b16 a[102:103], v214 offset:26624\n" \
"v_mfma_f32_16x16x32_bf16 v[12:15], a[124:127], a[68:71], v[12:15]\n" \
"ds_read_b128 a[64:67], v213 offset:0\n" \
"v_mfma_f32_16x16x32_bf16 v[28:31], a[120:123], a[72:75], v[28:31]\n" \
"s_add_u32 m0, m0, 0x4000\n" \
"ds_read_b128 a[68:71], v213 offset:2048\n" \
"v_mfma_f32_16x16x32_bf16 v[28:31], a[124:127], a[76:79], v[28:31]\n" \
"buffer_load_dwordx4 v216, s[36:39], s82 offen offset:0 lds\n" \
"v_mfma_f32_16x16x32_bf16 v[24:27], a[112:115], a[72:75], v[24:27]\n" \
"ds_read_b64_tr_b16 a[104:105], v215 offset:16384\n" \
"ds_read_b64_tr_b16 a[106:107], v215 offset:18432\n" \
"v_mfma_f32_16x16x32_bf16 v[24:27], a[116:119], a[76:79], v[24:27]\n" \
"ds_read_b128 a[72:75], v213 offset:256\n" \
"v_mfma_f32_16x16x32_bf16 v[40:43], a[112:115], a[80:83], v[40:43]\n" \
"s_add_u32 s48, s48, 0x1000\n" \
"ds_read_b128 a[76:79], v213 offset:2304\n" \
"v_mfma_f32_16x16x32_bf16 v[40:43], a[116:119], a[84:87], v[40:43]\n" \
"buffer_load_dwordx4 v217, s[36:39], s82 offen offset:1024 lds\n" \
"v_mfma_f32_16x16x32_bf16 v[44:47], a[120:123], a[80:83], v[44:47]\n" \
"s_sub_u32 s86, s86, 32768\n" \
"s_cselect_b32 s86, 131072, s86\n" \
"ds_read_b64_tr_b16 a[108:109], v215 offset:24576\n" \
"ds_read_b64_tr_b16 a[110:111], v215 offset:26624\n" \
"v_mfma_f32_16x16x32_bf16 v[44:47], a[124:127], a[84:87], v[44:47]\n" \
"buffer_load_dwordx4 v218, s[36:39], s82 offen offset:2048 lds\n" \
"v_mfma_f32_16x16x32_bf16 v[60:63], a[120:123], a[88:91], v[60:63]\n" \
"ds_read_b128 a[80:83], v213 offset:4096\n" \
"s_waitcnt lgkmcnt(4)\n" \
"v_mfma_f32_16x16x32_bf16 v[60:63], a[124:127], a[92:95], v[60:63]\n" \
"ds_read_b128 a[84:87], v213 offset:6144\n" \
"v_mfma_f32_16x16x32_bf16 v[56:59], a[112:115], a[88:91], v[56:59]\n" \
"buffer_load_dwordx4 v219, s[36:39], s82 offen offset:3072 lds\n" \
"v_mfma_f32_16x16x32_bf16 v[56:59], a[116:119], a[92:95], v[56:59]\n" \
"s_mov_b32 s72, 2\n" \
"s_nop 0\n" \
"Lo0_%=:\n" \
"v_mfma_f32_16x16x32_bf16 v[0:3], a[96:99], a[64:67], v[0:3]\n" \
"ds_read_b128 a[88:91], v213 offset:4352\n" \
"v_mfma_f32_16x16x32_bf16 v[0:3], a[100:103], a[68:71], v[0:3]\n" \
"s_waitcnt lgkmcnt(2)\n" \
"s_xor_b32 s82, s48, s49\n" \
"ds_read_b128 a[92:95], v213 offset:6400\n" \
"v_mfma_f32_16x16x32_bf16 v[4:7], a[104:107], a[64:67], v[4:7]\n" \
"s_cmp_lt_u32 s48, s50\n" \
"s_cselect_b32 s82, s82, s48\n" \
"ds_read_b64_tr_b16 a[112:113], v214 offset:16896\n" \
"v_mfma_f32_16x16x32_bf16 v[4:7], a[108:111], a[68:71], v[4:7]\n" \
"s_add_u32 s81, s82, s46\n" \
"ds_read_b64_tr_b16 a[114:115], v214 offset:18944\n" \
"v_mfma_f32_16x16x32_bf16 v[20:23], a[104:107], a[72:75], v[20:23]\n" \
"s_add_u32 m0, s86, s79\n" \
"ds_read_b64_tr_b16 a[116:117], v214 offset:25088\n" \
"v_mfma_f32_16x16x32_bf16 v[20:23], a[108:111], a[76:79], v[20:23]\n" \
"buffer_load_dwordx4 v234, s[56:59], s81 offen offset:0 sc1 lds\n" \
"v_mfma_f32_16x16x32_bf16 v[16:19], a[96:99], a[72:75], v[16:19]\n" \
"s_sub_u32 s85, s85, 32768\n" \
"s_cselect_b32 s85, 131072, s85\n" \
"ds_read_b64_tr_b16 a[118:119], v214 offset:27136\n" \
"v_mfma_f32_16x16x32_bf16 v[16:19], a[100:103], a[76:79], v[16:19]\n" \
"s_waitcnt lgkmcnt(3)\n" \
"s_lshl_b32 s82, s82, 5\n" \
"ds_read_b64_tr_b16 a[120:121], v215 offset:16896\n" \
"v_mfma_f32_16x16x32_bf16 v[32:35], a[96:99], a[80:83], v[32:35]\n" \
"s_add_u32 s82, s82, s47\n" \
"ds_read_b64_tr_b16 a[122:123], v215 offset:18944\n" \
"v_mfma_f32_16x16x32_bf16 v[32:35], a[100:103], a[84:87], v[32:35]\n" \
"buffer_load_dwordx4 v234, s[56:59], s81 offen offset:1024 sc1 lds\n" \
"v_mfma_f32_16x16x32_bf16 v[36:39], a[104:107], a[80:83], v[36:39]\n" \
"ds_read_b64_tr_b16 a[124:125], v215 offset:25088\n" \
"v_add_u32 v213, s85, v211\n" \
"v_mfma_f32_16x16x32_bf16 v[36:39], a[108:111], a[84:87], v[36:39]\n" \
"buffer_load_dwordx4 v234, s[56:59], s81 offen offset:2048 sc1 lds\n" \
"v_mfma_f32_16x16x32_bf16 v[52:55], a[104:107], a[88:91], v[52:55]\n" \
"ds_read_b64_tr_b16 a[126:127], v215 offset:27136\n" \
"v_mfma_f32_16x16x32_bf16 v[52:55], a[108:111], a[92:95], v[52:55]\n" \
"buffer_load_dwordx4 v234, s[56:59], s81 offen offset:3072 sc1 lds\n" \
"v_mfma_f32_16x16x32_bf16 v[48:51], a[96:99], a[88:91], v[48:51]\n" \
"v_add_u32 v214, s85, v212\n" \
"v_xor_b32 v215, 32, v214\n" \
"v_mfma_f32_16x16x32_bf16 v[48:51], a[100:103], a[92:95], v[48:51]\n" \
"s_waitcnt vmcnt(20) lgkmcnt(0)\n" \
"s_barrier\n" \
"v_mfma_f32_16x16x32_bf16 v[8:11], a[112:115], a[64:67], v[8:11]\n" \
"s_add_u32 m0, m0, 0x4000\n" \
"ds_read_b64_tr_b16 a[96:97], v214 offset:16384\n" \
"ds_read_b64_tr_b16 a[98:99], v214 offset:18432\n" \
"v_mfma_f32_16x16x32_bf16 v[8:11], a[116:119], a[68:71], v[8:11]\n" \
"buffer_load_dwordx4 v216, s[36:39], s82 offen offset:0 lds\n" \
"v_mfma_f32_16x16x32_bf16 v[12:15], a[120:123], a[64:67], v[12:15]\n" \
"ds_read_b64_tr_b16 a[100:101], v214 offset:24576\n" \
"ds_read_b64_tr_b16 a[102:103], v214 offset:26624\n" \
"v_mfma_f32_16x16x32_bf16 v[12:15], a[124:127], a[68:71], v[12:15]\n" \
"ds_read_b128 a[64:67], v213 offset:0\n" \
"v_mfma_f32_16x16x32_bf16 v[28:31], a[120:123], a[72:75], v[28:31]\n" \
"s_add_u32 s48, s48, 0x1000\n" \
"ds_read_b128 a[68:71], v213 offset:2048\n" \
"v_mfma_f32_16x16x32_bf16 v[28:31], a[124:127], a[76:79], v[28:31]\n" \
"buffer_load_dwordx4 v217, s[36:39], s82 offen offset:1024 lds\n" \
"v_mfma_f32_16x16x32_bf16 v[24:27], a[112:115], a[72:75], v[24:27]\n" \
"ds_read_b64_tr_b16 a[104:105], v215 offset:16384\n" \
"ds_read_b64_tr_b16 a[106:107], v215 offset:18432\n" \
"v_mfma_f32_16x16x32_bf16 v[24:27], a[116:119], a[76:79], v[24:27]\n" \
"ds_read_b128 a[72:75], v213 offset:256\n" \
"v_mfma_f32_16x16x32_bf16 v[40:43], a[112:115], a[80:83], v[40:43]\n" \
"s_sub_u32 s86, s86, 32768\n" \
"s_cselect_b32 s86, 131072, s86\n" \
"ds_read_b128 a[76:79], v213 offset:2304\n" \
"v_mfma_f32_16x16x32_bf16 v[40:43], a[116:119], a[84:87], v[40:43]\n" \
"buffer_load_dwordx4 v218, s[36:39], s82 offen offset:2048 lds\n" \
"v_mfma_f32_16x16x32_bf16 v[44:47], a[120:123], a[80:83], v[44:47]\n" \
"ds_read_b64_tr_b16 a[108:109], v215 offset:24576\n" \
"ds_read_b64_tr_b16 a[110:111], v215 offset:26624\n" \
"v_mfma_f32_16x16x32_bf16 v[44:47], a[124:127], a[84:87], v[44:47]\n" \
"buffer_load_dwordx4 v219, s[36:39], s82 offen offset:3072 lds\n" \
"v_mfma_f32_16x16x32_bf16 v[60:63], a[120:123], a[88:91], v[60:63]\n" \
"ds_read_b128 a[80:83], v213 offset:4096\n" \
"s_waitcnt lgkmcnt(4)\n" \
"v_mfma_f32_16x16x32_bf16 v[60:63], a[124:127], a[92:95], v[60:63]\n" \
"s_sub_u32 s72, s72, 1\n" \
"ds_read_b128 a[84:87], v213 offset:6144\n" \
"v_mfma_f32_16x16x32_bf16 v[56:59], a[112:115], a[88:91], v[56:59]\n" \
"v_mfma_f32_16x16x32_bf16 v[56:59], a[116:119], a[92:95], v[56:59]\n" \
"s_cbranch_scc0 Lo0_%=\n" \
"s_setpc_b64 s[44:45]\n" \
"Lor_%=:\n" \
"buffer_store_dword v230, v231, s[56:59], s12 offen sc1\n" \
"s_mov_b32 s72, s10\n" \
"s_call_b64 s[44:45], Lo0_%=\n" \
"buffer_load_dword v220, v221, s[56:59], s67 offen sc1\n" \
"s_mov_b32 s72, 1\n" \
"s_call_b64 s[44:45], Lo0_%=\n" \
"s_waitcnt vmcnt(16)\n" \
"v_cmp_eq_u32 vcc, s60, v220\n" \
"s_nop 1\n" \
"s_andn2_b32 s70, s74, vcc_lo\n" \
"s_cbranch_scc1 Lpl6_%=\n" \
"Lpr6_%=:\n" \
"s_cmp_eq_u32 s90, 0\n" \
"s_cselect_b32 s46, s46, s83\n" \
"s_cselect_b32 s47, s47, s84\n" \
"s_cselect_b32 s48, s48, 0\n" \
"s_cselect_b64 s[36:37], s[36:37], s[40:41]\n" \
"s_cselect_b32 s38, s38, s42\n" \
"s_mov_b32 s72, 3\n" \
"s_call_b64 s[44:45], Lo0_%=\n" \
"s_cmp_eq_u32 s90, 0\n" \
"s_cbranch_scc0 Lrole1_%=\n" \
"s_mov_b32 s72, s11\n" \
"s_call_b64 s[44:45], Lo0_%=\n" \
"buffer_load_dword v220, v241, s[56:59], s64 offen sc1\n" \
"s_mov_b32 s72, 1\n" \
"s_call_b64 s[44:45], Lo0_%=\n" \
"v_mfma_f32_16x16x32_bf16 v[0:3], a[96:99], a[64:67], v[0:3]\n" \
"s_waitcnt lgkmcnt(2)\n" \
"ds_read_b128 a[88:91], v213 offset:4352\n" \
"v_mov_b32 v74, 0x3f80\n" \
"v_mfma_f32_16x16x32_bf16 v[0:3], a[100:103], a[68:71], v[0:3]\n" \
"s_add_u32 s48, s86, 32768\n" \
"s_cmp_eq_u32 s48, 163840\n" \
"s_cselect_b32 s48, 0, s48\n" \
"ds_read_b128 a[92:95], v213 offset:6400\n" \
"v_mfma_f32_16x16x32_bf16 v[4:7], a[104:107], a[64:67], v[4:7]\n" \
"ds_read_b64_tr_b16 a[112:113], v214 offset:16896\n" \
"v_mov_b32 v64, 0\n" \
"v_mfma_f32_16x16x32_bf16 v[4:7], a[108:111], a[68:71], v[4:7]\n" \
"s_add_u32 s49, s48, 32768\n" \
"s_cmp_eq_u32 s49, 163840\n" \
"s_cselect_b32 s49, 0, s49\n" \
"ds_read_b64_tr_b16 a[114:115], v214 offset:18944\n" \
"v_mfma_f32_16x16x32_bf16 v[20:23], a[104:107], a[72:75], v[20:23]\n" \
"ds_read_b64_tr_b16 a[116:117], v214 offset:25088\n" \
"v_add_u32 v224, s49, v211\n" \
"v_mfma_f32_16x16x32_bf16 v[20:23], a[108:111], a[76:79], v[20:23]\n" \
"v_add_u32 v225, s49, v212\n" \
"v_xor_b32 v226, 32, v225\n" \
"v_mfma_f32_16x16x32_bf16 v[16:19], a[96:99], a[72:75], v[16:19]\n" \
"s_sub_u32 s85, s85, 32768\n" \
"s_cselect_b32 s85, 131072, s85\n" \
"ds_read_b64_tr_b16 a[118:119], v214 offset:27136\n" \
"v_mfma_f32_16x16x32_bf16 v[16:19], a[100:103], a[76:79], v[16:19]\n" \
"s_waitcnt lgkmcnt(3)\n" \
"ds_read_b64_tr_b16 a[120:121], v215 offset:16896\n" \
"v_add_u32 v227, s48, v211\n" \
"v_mfma_f32_16x16x32_bf16 v[32:35], a[96:99], a[80:83], v[32:35]\n" \
"ds_read_b64_tr_b16 a[122:123], v215 offset:18944\n" \
"v_add_u32 v228, s48, v212\n" \
"v_mfma_f32_16x16x32_bf16 v[32:35], a[100:103], a[84:87], v[32:35]\n" \
"v_xor_b32 v229, 32, v228\n" \
"v_mov_b32 v65, 0\n" \
"v_mfma_f32_16x16x32_bf16 v[36:39], a[104:107], a[80:83], v[36:39]\n" \
"ds_read_b64_tr_b16 a[124:125], v215 offset:25088\n" \
"v_add_u32 v213, s85, v211\n" \
"v_mfma_f32_16x16x32_bf16 v[36:39], a[108:111], a[84:87], v[36:39]\n" \
"v_mov_b32 v68, 0\n" \
"v_mov_b32 v69, 0\n" \
"v_mfma_f32_16x16x32_bf16 v[52:55], a[104:107], a[88:91], v[52:55]\n" \
"ds_read_b64_tr_b16 a[126:127], v215 offset:27136\n" \
"v_lshrrev_b32 v72, 8, v234\n" \
"v_mfma_f32_16x16x32_bf16 v[52:55], a[108:111], a[92:95], v[52:55]\n" \
"v_bfe_u32 v73, v234, 6, 2\n" \
"v_cmp_eq_u32 s[16:17], v72, v73\n" \
"v_mfma_f32_16x16x32_bf16 v[48:51], a[96:99], a[88:91], v[48:51]\n" \
"v_add_u32 v214, s85, v212\n" \
"v_xor_b32 v215, 32, v214\n" \
"v_mfma_f32_16x16x32_bf16 v[48:51], a[100:103], a[92:95], v[48:51]\n" \
"s_waitcnt vmcnt(8) lgkmcnt(0)\n" \
"s_barrier\n" \
"s_waitcnt vmcnt(16)\n" \
"v_cmp_eq_u32 vcc, s60, v220\n" \
"s_nop 1\n" \
"s_andn2_b32 s70, s69, vcc_lo\n" \
"s_cbranch_scc1 Lpl7_%=\n" \
"Lpr7_%=:\n" \
"v_mfma_f32_16x16x32_bf16 v[8:11], a[112:115], a[64:67], v[8:11]\n" \
"s_add_u32 s71, s87, 0\n" \
"ds_read_b128 a[0:3], v213 offset:0\n" \
"ds_read_b128 a[4:7], v213 offset:2048\n" \
"v_mfma_f32_16x16x32_bf16 v[8:11], a[116:119], a[68:71], v[8:11]\n" \
"buffer_load_dwordx4 v[176:179], v234, s[56:59], s71 offen offset:0 sc1\n" \
"v_mfma_f32_16x16x32_bf16 v[12:15], a[120:123], a[64:67], v[12:15]\n" \
"ds_read_b64_tr_b16 a[128:129], v214 offset:16384\n" \
"ds_read_b64_tr_b16 a[130:131], v214 offset:18432\n" \
"v_mfma_f32_16x16x32_bf16 v[12:15], a[124:127], a[68:71], v[12:15]\n" \
"buffer_load_dwordx4 v[180:183], v234, s[56:59], s71 offen offset:1024 sc1\n" \
"v_mfma_f32_16x16x32_bf16 v[28:31], a[120:123], a[72:75], v[28:31]\n" \
"ds_read_b64_tr_b16 a[132:133], v214 offset:24576\n" \
"ds_read_b64_tr_b16 a[134:135], v214 offset:26624\n" \
"v_mfma_f32_16x16x32_bf16 v[28:31], a[124:127], a[76:79], v[28:31]\n" \
"buffer_load_dwordx4 v[184:187], v234, s[56:59], s71 offen offset:2048 sc1\n" \
"v_mfma_f32_16x16x32_bf16 v[24:27], a[112:115], a[72:75], v[24:27]\n" \
"ds_read_b64_tr_b16 a[136:137], v215 offset:16384\n" \
"ds_read_b64_tr_b16 a[138:139], v215 offset:18432\n" \
"v_mfma_f32_16x16x32_bf16 v[24:27], a[116:119], a[76:79], v[24:27]\n" \
"buffer_load_dwordx4 v[188:191], v234, s[56:59], s71 offen offset:3072 sc1\n" \
"v_mfma_f32_16x16x32_bf16 v[40:43], a[112:115], a[80:83], v[40:43]\n" \
"ds_read_b64_tr_b16 a[140:141], v215 offset:24576\n" \
"ds_read_b64_tr_b16 a[142:143], v215 offset:26624\n" \
"v_mfma_f32_16x16x32_bf16 v[40:43], a[116:119], a[84:87], v[40:43]\n" \
"ds_read_b64_tr_b16 a[144:145], v214 offset:16896\n" \
"ds_read_b64_tr_b16 a[146:147], v214 offset:18944\n" \
"v_mfma_f32_16x16x32_bf16 v[44:47], a[120:123], a[80:83], v[44:47]\n" \
"ds_read_b64_tr_b16 a[148:149], v214 offset:25088\n" \
"ds_read_b64_tr_b16 a[150:151], v214 offset:27136\n" \
"v_mfma_f32_16x16x32_bf16 v[44:47], a[124:127], a[84:87], v[44:47]\n" \
"ds_read_b64_tr_b16 a[152:153], v215 offset:16896\n" \
"ds_read_b64_tr_b16 a[154:155], v215 offset:18944\n" \
"v_mfma_f32_16x16x32_bf16 v[60:63], a[120:123], a[88:91], v[60:63]\n" \
"ds_read_b64_tr_b16 a[156:157], v215 offset:25088\n" \
"ds_read_b64_tr_b16 a[158:159], v215 offset:27136\n" \
"v_mfma_f32_16x16x32_bf16 v[60:63], a[124:127], a[92:95], v[60:63]\n" \
"ds_read_b128 a[24:27], v213 offset:256\n" \
"ds_read_b128 a[28:31], v213 offset:2304\n" \
"v_mfma_f32_16x16x32_bf16 v[56:59], a[112:115], a[88:91], v[56:59]\n" \
"ds_read_b128 a[8:11], v224 offset:0\n" \
"ds_read_b128 a[12:15], v224 offset:2048\n" \
"v_mfma_f32_16x16x32_bf16 v[56:59], a[116:119], a[92:95], v[56:59]\n" \
"s_waitcnt lgkmcnt(8)\n" \
"ds_read_b64_tr_b16 a[160:161], v225 offset:16384\n" \
"ds_read_b64_tr_b16 a[162:163], v225 offset:18432\n" \
"v_mfma_f32_16x16x32_bf16 v[0:3], a[128:131], a[0:3], v[0:3]\n" \
"ds_read_b64_tr_b16 a[164:165], v225 offset:24576\n" \
"ds_read_b64_tr_b16 a[166:167], v225 offset:26624\n" \
"v_mfma_f32_16x16x32_bf16 v[0:3], a[132:135], a[4:7], v[0:3]\n" \
"ds_read_b64_tr_b16 a[168:169], v226 offset:16384\n" \
"ds_read_b64_tr_b16 a[170:171], v226 offset:18432\n" \
"v_mfma_f32_16x16x32_bf16 v[4:7], a[136:139], a[0:3], v[4:7]\n" \
"s_mov_b32 s70, 0x20000000\n" \
"ds_read_b64_tr_b16 a[172:173], v226 offset:24576\n" \
"ds_read_b64_tr_b16 a[174:175], v226 offset:26624\n" \
"v_mfma_f32_16x16x32_bf16 v[4:7], a[140:143], a[4:7], v[4:7]\n" \
"s_cmp_eq_u32 s64, 65616896\n" \
"s_cselect_b32 s70, 65593344, s70\n" \
"ds_read_b64_tr_b16 a[176:177], v225 offset:16896\n" \
"ds_read_b64_tr_b16 a[178:179], v225 offset:18944\n" \
"buffer_store_dword v230, v231, s[56:59], s70 offen sc1\n" \
"v_mfma_f32_16x16x32_bf16 v[8:11], a[144:147], a[0:3], v[8:11]\n" \
"s_waitcnt lgkmcnt(8)\n" \
"ds_read_b64_tr_b16 a[180:181], v225 offset:25088\n" \
"ds_read_b64_tr_b16 a[182:183], v225 offset:27136\n" \
"v_mfma_f32_16x16x32_bf16 v[8:11], a[148:151], a[4:7], v[8:11]\n" \
"s_sub_u32 s68, s86, 32768\n" \
"s_cselect_b32 s68, 131072, s68\n" \
"ds_read_b64_tr_b16 a[184:185], v226 offset:16896\n" \
"ds_read_b64_tr_b16 a[186:187], v226 offset:18944\n" \
"v_mfma_f32_16x16x32_bf16 v[12:15], a[152:155], a[0:3], v[12:15]\n" \
"ds_read_b64_tr_b16 a[188:189], v226 offset:25088\n" \
"ds_read_b64_tr_b16 a[190:191], v226 offset:27136\n" \
"v_mfma_f32_16x16x32_bf16 v[12:15], a[156:159], a[4:7], v[12:15]\n" \
"s_cmp_lt_u32 s61, 2\n" \
"s_cselect_b32 s68, s86, s68\n" \
"v_and_b32 v72, 16, v234\n" \
"v_lshlrev_b32 v72, v72, v74\n" \
"v_mfma_f32_16x16x32_bf16 v[28:31], a[152:155], a[24:27], v[28:31]\n" \
"s_and_b32 s50, s61, 1\n" \
"v_cndmask_b32 v72, 0, v72, s[16:17]\n" \
"v_bfe_i32 v73, v234, 5, 1\n" \
"v_mfma_f32_16x16x32_bf16 v[28:31], a[156:159], a[28:31], v[28:31]\n" \
"s_mul_i32 s50, s50, 9216\n" \
"v_and_b32 v67, v72, v73\n" \
"v_xor_b32 v66, v72, v67\n" \
"v_mfma_f32_16x16x32_bf16 v[24:27], a[144:147], a[24:27], v[24:27]\n" \
"s_add_u32 s68, s68, s50\n" \
"s_add_u32 s68, s68, %3\n" \
"v_lshrrev_b32 v132, 4, v234\n" \
"v_mfma_f32_16x16x32_bf16 v[24:27], a[148:151], a[28:31], v[24:27]\n" \
"v_and_b32 v133, 15, v132\n" \
"v_mul_u32_u24 v128, 144, v133\n" \
"v_lshrrev_b32 v133, 4, v132\n" \
"v_mfma_f32_16x16x32_bf16 v[20:23], a[136:139], a[24:27], v[20:23]\n" \
"s_waitcnt vmcnt(5)\n" \
"s_barrier\n" \
"v_mfma_f32_16x16x32_bf16 v[20:23], a[140:143], a[28:31], v[20:23]\n" \
"s_add_u32 s71, s87, 4096\n" \
"ds_read_b128 a[32:35], v224 offset:256\n" \
"ds_read_b128 a[36:39], v224 offset:2304\n" \
"v_mfma_f32_16x16x32_bf16 v[16:19], a[128:131], a[24:27], v[16:19]\n" \
"buffer_load_dwordx4 v[192:195], v234, s[56:59], s71 offen offset:0 sc1\n" \
"v_mfma_f32_16x16x32_bf16 v[16:19], a[132:135], a[28:31], v[16:19]\n" \
"s_waitcnt lgkmcnt(2)\n" \
"ds_read_b128 a[16:19], v227 offset:0\n" \
"ds_read_b128 a[20:23], v227 offset:2048\n" \
"v_mfma_f32_16x16x32_bf16 v[0:3], a[160:163], a[8:11], v[0:3]\n" \
"ds_read_b64_tr_b16 a[192:193], v228 offset:16384\n" \
"ds_read_b64_tr_b16 a[194:195], v228 offset:18432\n" \
"v_mfma_f32_16x16x32_bf16 v[0:3], a[164:167], a[12:15], v[0:3]\n" \
"buffer_load_dwordx4 v[196:199], v234, s[56:59], s71 offen offset:1024 sc1\n" \
"v_mfma_f32_16x16x32_bf16 v[4:7], a[168:171], a[8:11], v[4:7]\n" \
"ds_read_b64_tr_b16 a[196:197], v228 offset:24576\n" \
"ds_read_b64_tr_b16 a[198:199], v228 offset:26624\n" \
"v_mfma_f32_16x16x32_bf16 v[4:7], a[172:175], a[12:15], v[4:7]\n" \
"ds_read_b64_tr_b16 a[200:201], v229 offset:16384\n" \
"ds_read_b64_tr_b16 a[202:203], v229 offset:18432\n" \
"v_mfma_f32_16x16x32_bf16 v[8:11], a[176:179], a[8:11], v[8:11]\n" \
"buffer_load_dwordx4 v[200:203], v234, s[56:59], s71 offen offset:2048 sc1\n" \
"v_mfma_f32_16x16x32_bf16 v[8:11], a[180:183], a[12:15], v[8:11]\n" \
"ds_read_b64_tr_b16 a[204:205], v229 offset:24576\n" \
"ds_read_b64_tr_b16 a[206:207], v229 offset:26624\n" \
"v_lshl_add_u32 v128, v133, 3, v128\n" \
"v_mfma_f32_16x16x32_bf16 v[12:15], a[184:187], a[8:11], v[12:15]\n" \
"s_waitcnt lgkmcnt(6)\n" \
"ds_read_b64_tr_b16 a[208:209], v228 offset:16896\n" \
"ds_read_b64_tr_b16 a[210:211], v228 offset:18944\n" \
"v_mfma_f32_16x16x32_bf16 v[12:15], a[188:191], a[12:15], v[12:15]\n" \
"buffer_load_dwordx4 v[204:207], v234, s[56:59], s71 offen offset:3072 sc1\n" \
"v_mfma_f32_16x16x32_bf16 v[28:31], a[184:187], a[32:35], v[28:31]\n" \
"ds_read_b64_tr_b16 a[212:213], v228 offset:25088\n" \
"ds_read_b64_tr_b16 a[214:215], v228 offset:27136\n" \
"v_mfma_f32_16x16x32_bf16 v[28:31], a[188:191], a[36:39], v[28:31]\n" \
"ds_read_b64_tr_b16 a[216:217], v229 offset:16896\n" \
"ds_read_b64_tr_b16 a[218:219], v229 offset:18944\n" \
"v_mfma_f32_16x16x32_bf16 v[24:27], a[176:179], a[32:35], v[24:27]\n" \
"ds_read_b64_tr_b16 a[220:221], v229 offset:25088\n" \
"ds_read_b64_tr_b16 a[222:223], v229 offset:27136\n" \
"v_mfma_f32_16x16x32_bf16 v[24:27], a[180:183], a[36:39], v[24:27]\n" \
"ds_read_b128 a[40:43], v227 offset:256\n" \
"ds_read_b128 a[44:47], v227 offset:2304\n" \
"v_mfma_f32_16x16x32_bf16 v[20:23], a[168:171], a[32:35], v[20:23]\n" \
"ds_read_b128 a[248:251], v213 offset:4096\n" \
"ds_read_b128 a[252:255], v213 offset:6144\n" \
"v_mfma_f32_16x16x32_bf16 v[20:23], a[172:175], a[36:39], v[20:23]\n" \
"ds_read_b128 a[48:51], v224 offset:4096\n" \
"ds_read_b128 a[52:55], v224 offset:6144\n" \
"v_mfma_f32_16x16x32_bf16 v[16:19], a[160:163], a[32:35], v[16:19]\n" \
"ds_read_b128 a[56:59], v227 offset:4096\n" \
"ds_read_b128 a[60:63], v227 offset:6144\n" \
"v_mfma_f32_16x16x32_bf16 v[16:19], a[164:167], a[36:39], v[16:19]\n" \
"s_waitcnt lgkmcnt(8)\n" \
"ds_read_b128 a[224:227], v213 offset:4352\n" \
"ds_read_b128 a[228:231], v213 offset:6400\n" \
"v_add_u32 v128, s68, v128\n" \
"v_mfma_f32_16x16x32_bf16 v[0:3], a[192:195], a[16:19], v[0:3]\n" \
"s_waitcnt vmcnt(7)\n" \
"ds_read_b128 a[232:235], v224 offset:4352\n" \
"ds_read_b128 a[236:239], v224 offset:6400\n" \
"v_mfma_f32_16x16x32_bf16 v[0:3], a[196:199], a[20:23], v[0:3]\n" \
"ds_read_b128 a[240:243], v227 offset:4352\n" \
"ds_read_b128 a[244:247], v227 offset:6400\n" \
"v_mfma_f32_16x16x32_bf16 v[0:3], v[66:69], v[176:179], v[0:3]\n" \
"v_lshrrev_b32 v133, 3, v132\n" \
"v_and_b32 v134, 7, v132\n" \
"v_mfma_f32_16x16x32_bf16 v[4:7], a[200:203], a[16:19], v[4:7]\n" \
"v_mul_u32_u24 v129, 144, v133\n" \
"v_lshl_add_u32 v129, v134, 4, v129\n" \
"v_mfma_f32_16x16x32_bf16 v[4:7], a[204:207], a[20:23], v[4:7]\n" \
"v_add_u32 v129, s68, v129\n" \
"v_lshlrev_b32 v134, 4, v134\n" \
"v_mfma_f32_16x16x32_bf16 v[4:7], v[64:67], v[176:179], v[4:7]\n" \
"v_lshl_add_u32 v130, v133, 12, v134\n" \
"v_mfma_f32_16x16x32_bf16 v[8:11], a[208:211], a[16:19], v[8:11]\n" \
"v_mul_f32 v0, 4.0, v0\n" \
"v_mul_f32 v1, 4.0, v1\n" \
"v_mul_f32 v2, 4.0, v2\n" \
"v_mul_f32 v3, 4.0, v3\n" \
"v_cvt_pk_bf16_f32 v136, v0, v1\n" \
"v_cvt_pk_bf16_f32 v137, v2, v3\n" \
"v_mfma_f32_16x16x32_bf16 v[8:11], a[212:215], a[20:23], v[8:11]\n" \
"v_mfma_f32_16x16x32_bf16 v[8:11], v[66:69], v[180:183], v[8:11]\n" \
"ds_write_b64 v128, v[136:137] offset:0\n" \
"v_mfma_f32_16x16x32_bf16 v[12:15], a[216:219], a[16:19], v[12:15]\n" \
"v_mul_f32 v4, 4.0, v4\n" \
"v_mul_f32 v5, 4.0, v5\n" \
"v_mfma_f32_16x16x32_bf16 v[12:15], a[220:223], a[20:23], v[12:15]\n" \
"v_mul_f32 v6, 4.0, v6\n" \
"v_mul_f32 v7, 4.0, v7\n" \
"v_mfma_f32_16x16x32_bf16 v[12:15], v[64:67], v[180:183], v[12:15]\n" \
"s_waitcnt lgkmcnt(1)\n" \
"v_cvt_pk_bf16_f32 v138, v4, v5\n" \
"v_cvt_pk_bf16_f32 v139, v6, v7\n" \
"v_mfma_f32_16x16x32_bf16 v[16:19], a[192:195], a[40:43], v[16:19]\n" \
"s_waitcnt vmcnt(5)\n" \
"v_mfma_f32_16x16x32_bf16 v[16:19], a[196:199], a[44:47], v[16:19]\n" \
"v_mfma_f32_16x16x32_bf16 v[16:19], v[66:69], v[184:187], v[16:19]\n" \
"ds_write_b64 v128, v[138:139] offset:32\n" \
"v_mul_f32 v8, 4.0, v8\n" \
"v_mfma_f32_16x16x32_bf16 v[20:23], a[200:203], a[40:43], v[20:23]\n" \
"v_mul_f32 v9, 4.0, v9\n" \
"v_mul_f32 v10, 4.0, v10\n" \
"v_mfma_f32_16x16x32_bf16 v[20:23], a[204:207], a[44:47], v[20:23]\n" \
"v_mul_f32 v11, 4.0, v11\n" \
"v_cvt_pk_bf16_f32 v136, v8, v9\n" \
"v_mfma_f32_16x16x32_bf16 v[20:23], v[64:67], v[184:187], v[20:23]\n" \
"v_cvt_pk_bf16_f32 v137, v10, v11\n" \
"v_mfma_f32_16x16x32_bf16 v[24:27], a[208:211], a[40:43], v[24:27]\n" \
"v_mfma_f32_16x16x32_bf16 v[24:27], a[212:215], a[44:47], v[24:27]\n" \
"v_mfma_f32_16x16x32_bf16 v[24:27], v[66:69], v[188:191], v[24:27]\n" \
"ds_write_b64 v128, v[136:137] offset:64\n" \
"v_mul_f32 v12, 4.0, v12\n" \
"v_mfma_f32_16x16x32_bf16 v[28:31], a[216:219], a[40:43], v[28:31]\n" \
"v_mul_f32 v13, 4.0, v13\n" \
"v_mul_f32 v14, 4.0, v14\n" \
"v_mfma_f32_16x16x32_bf16 v[28:31], a[220:223], a[44:47], v[28:31]\n" \
"v_mul_f32 v15, 4.0, v15\n" \
"v_cvt_pk_bf16_f32 v138, v12, v13\n" \
"v_mfma_f32_16x16x32_bf16 v[28:31], v[64:67], v[188:191], v[28:31]\n" \
"v_cvt_pk_bf16_f32 v139, v14, v15\n" \
"v_mfma_f32_16x16x32_bf16 v[32:35], a[128:131], a[248:251], v[32:35]\n" \
"ds_write_b64 v128, v[138:139] offset:96\n" \
"v_mfma_f32_16x16x32_bf16 v[32:35], a[132:135], a[252:255], v[32:35]\n" \
"ds_read_b128 v[140:143], v129 offset:0\n" \
"ds_read_b128 v[144:147], v129 offset:1152\n" \
"v_mfma_f32_16x16x32_bf16 v[32:35], a[160:163], a[48:51], v[32:35]\n" \
"s_waitcnt vmcnt(2)\n" \
"v_mul_f32 v16, 4.0, v16\n" \
"v_mul_f32 v17, 4.0, v17\n" \
"v_mfma_f32_16x16x32_bf16 v[32:35], a[164:167], a[52:55], v[32:35]\n" \
"v_mul_f32 v18, 4.0, v18\n" \
"v_mul_f32 v19, 4.0, v19\n" \
"v_mfma_f32_16x16x32_bf16 v[32:35], v[66:69], v[192:195], v[32:35]\n" \
"s_add_u32 s70, s88, 0\n" \
"s_waitcnt lgkmcnt(1)\n" \
"v_cvt_pk_bf16_f32 v136, v16, v17\n" \
"v_mfma_f32_16x16x32_bf16 v[32:35], a[192:195], a[56:59], v[32:35]\n" \
"buffer_store_dwordx4 v[140:143], v130, s[52:55], s70 offen sc1\n" \
"v_cvt_pk_bf16_f32 v137, v18, v19\n" \
"v_mfma_f32_16x16x32_bf16 v[32:35], a[196:199], a[60:63], v[32:35]\n" \
"ds_write_b64 v128, v[136:137] offset:2304\n" \
"v_mfma_f32_16x16x32_bf16 v[36:39], a[204:207], a[60:63], v[36:39]\n" \
"s_add_u32 s70, s88, 32768\n" \
"s_waitcnt lgkmcnt(1)\n" \
"v_mul_f32 v20, 4.0, v20\n" \
"v_mfma_f32_16x16x32_bf16 v[36:39], a[200:203], a[56:59], v[36:39]\n" \
"buffer_store_dwordx4 v[144:147], v130, s[52:55], s70 offen sc1\n" \
"v_mfma_f32_16x16x32_bf16 v[36:39], a[172:175], a[52:55], v[36:39]\n" \
"v_mul_f32 v21, 4.0, v21\n" \
"v_mul_f32 v22, 4.0, v22\n" \
"v_mfma_f32_16x16x32_bf16 v[36:39], a[168:171], a[48:51], v[36:39]\n" \
"v_mul_f32 v23, 4.0, v23\n" \
"v_cvt_pk_bf16_f32 v138, v20, v21\n" \
"v_mfma_f32_16x16x32_bf16 v[36:39], v[64:67], v[192:195], v[36:39]\n" \
"v_cvt_pk_bf16_f32 v139, v22, v23\n" \
"v_mfma_f32_16x16x32_bf16 v[36:39], a[140:143], a[252:255], v[36:39]\n" \
"v_mfma_f32_16x16x32_bf16 v[36:39], a[136:139], a[248:251], v[36:39]\n" \
"ds_write_b64 v128, v[138:139] offset:2336\n" \
"v_mul_f32 v24, 4.0, v24\n" \
"v_mul_f32 v25, 4.0, v25\n" \
"v_mfma_f32_16x16x32_bf16 v[40:43], a[144:147], a[248:251], v[40:43]\n" \
"v_mul_f32 v26, 4.0, v26\n" \
"v_mul_f32 v27, 4.0, v27\n" \
"v_mfma_f32_16x16x32_bf16 v[40:43], a[148:151], a[252:255], v[40:43]\n" \
"v_cvt_pk_bf16_f32 v136, v24, v25\n" \
"v_cvt_pk_bf16_f32 v137, v26, v27\n" \
"v_mfma_f32_16x16x32_bf16 v[40:43], a[176:179], a[48:51], v[40:43]\n" \
"ds_write_b64 v128, v[136:137] offset:2368\n" \
"v_mfma_f32_16x16x32_bf16 v[40:43], a[180:183], a[52:55], v[40:43]\n" \
"v_mul_f32 v28, 4.0, v28\n" \
"v_mul_f32 v29, 4.0, v29\n" \
"v_mfma_f32_16x16x32_bf16 v[40:43], v[66:69], v[196:199], v[40:43]\n" \
"v_mul_f32 v30, 4.0, v30\n" \
"v_mul_f32 v31, 4.0, v31\n" \
"v_mfma_f32_16x16x32_bf16 v[40:43], a[208:211], a[56:59], v[40:43]\n" \
"v_cvt_pk_bf16_f32 v138, v28, v29\n" \
"v_cvt_pk_bf16_f32 v139, v30, v31\n" \
"v_mfma_f32_16x16x32_bf16 v[40:43], a[212:215], a[60:63], v[40:43]\n" \
"ds_write_b64 v128, v[138:139] offset:2400\n" \
"v_mfma_f32_16x16x32_bf16 v[44:47], a[220:223], a[60:63], v[44:47]\n" \
"ds_read_b128 v[148:151], v129 offset:2304\n" \
"ds_read_b128 v[152:155], v129 offset:3456\n" \
"v_mfma_f32_16x16x32_bf16 v[44:47], a[216:219], a[56:59], v[44:47]\n" \
"v_mul_f32 v32, 4.0, v32\n" \
"v_mul_f32 v33, 4.0, v33\n" \
"v_mfma_f32_16x16x32_bf16 v[44:47], a[188:191], a[52:55], v[44:47]\n" \
"v_mul_f32 v34, 4.0, v34\n" \
"v_mul_f32 v35, 4.0, v35\n" \
"v_mfma_f32_16x16x32_bf16 v[44:47], a[184:187], a[48:51], v[44:47]\n" \
"s_add_u32 s70, s88, 65536\n" \
"s_waitcnt lgkmcnt(1)\n" \
"v_cvt_pk_bf16_f32 v136, v32, v33\n" \
"v_mfma_f32_16x16x32_bf16 v[44:47], v[64:67], v[196:199], v[44:47]\n" \
"buffer_store_dwordx4 v[148:151], v130, s[52:55], s70 offen sc1\n" \
"v_cvt_pk_bf16_f32 v137, v34, v35\n" \
"v_mfma_f32_16x16x32_bf16 v[44:47], a[156:159], a[252:255], v[44:47]\n" \
"v_mfma_f32_16x16x32_bf16 v[44:47], a[152:155], a[248:251], v[44:47]\n" \
"s_add_u32 s70, s88, 98304\n" \
"s_waitcnt lgkmcnt(0)\n" \
"ds_write_b64 v128, v[136:137] offset:4608\n" \
"v_mul_f32 v36, 4.0, v36\n" \
"v_mfma_f32_16x16x32_bf16 v[48:51], a[128:131], a[224:227], v[48:51]\n" \
"buffer_store_dwordx4 v[152:155], v130, s[52:55], s70 offen sc1\n" \
"v_mul_f32 v37, 4.0, v37\n" \
"v_mfma_f32_16x16x32_bf16 v[48:51], a[132:135], a[228:231], v[48:51]\n" \
"v_mul_f32 v38, 4.0, v38\n" \
"v_mul_f32 v39, 4.0, v39\n" \
"v_mfma_f32_16x16x32_bf16 v[48:51], a[160:163], a[232:235], v[48:51]\n" \
"s_waitcnt vmcnt(4)\n" \
"v_cvt_pk_bf16_f32 v138, v36, v37\n" \
"v_cvt_pk_bf16_f32 v139, v38, v39\n" \
"v_mfma_f32_16x16x32_bf16 v[48:51], a[164:167], a[236:239], v[48:51]\n" \
"ds_write_b64 v128, v[138:139] offset:4640\n" \
"v_mul_f32 v40, 4.0, v40\n" \
"v_mfma_f32_16x16x32_bf16 v[48:51], v[66:69], v[200:203], v[48:51]\n" \
"v_mul_f32 v41, 4.0, v41\n" \
"v_mul_f32 v42, 4.0, v42\n" \
"Lpg10807_%=:\n" \
"s_cbranch_execz Lcb1_%=\n" \
"v_mfma_f32_16x16x32_bf16 v[48:51], a[192:195], a[240:243], v[48:51]\n" \
"v_mul_f32 v43, 4.0, v43\n" \
"v_cvt_pk_bf16_f32 v136, v40, v41\n" \
"v_mfma_f32_16x16x32_bf16 v[48:51], a[196:199], a[244:247], v[48:51]\n" \
"v_cvt_pk_bf16_f32 v137, v42, v43\n" \
"v_mfma_f32_16x16x32_bf16 v[52:55], a[204:207], a[244:247], v[52:55]\n" \
"ds_write_b64 v128, v[136:137] offset:4672\n" \
"v_mul_f32 v44, 4.0, v44\n" \
"v_mul_f32 v45, 4.0, v45\n" \
"v_mfma_f32_16x16x32_bf16 v[52:55], a[200:203], a[240:243], v[52:55]\n" \
"v_mul_f32 v46, 4.0, v46\n" \
"v_mul_f32 v47, 4.0, v47\n" \
"v_mfma_f32_16x16x32_bf16 v[52:55], a[172:175], a[236:239], v[52:55]\n" \
"v_cvt_pk_bf16_f32 v138, v44, v45\n" \
"v_cvt_pk_bf16_f32 v139, v46, v47\n" \
"v_mfma_f32_16x16x32_bf16 v[52:55], a[168:171], a[232:235], v[52:55]\n" \
"ds_write_b64 v128, v[138:139] offset:4704\n" \
"v_mfma_f32_16x16x32_bf16 v[52:55], v[64:67], v[200:203], v[52:55]\n" \
"ds_read_b128 v[156:159], v129 offset:4608\n" \
"ds_read_b128 v[160:163], v129 offset:5760\n" \
"v_mfma_f32_16x16x32_bf16 v[52:55], a[140:143], a[228:231], v[52:55]\n" \
"v_mul_f32 v48, 4.0, v48\n" \
"v_mul_f32 v49, 4.0, v49\n" \
"v_mfma_f32_16x16x32_bf16 v[52:55], a[136:139], a[224:227], v[52:55]\n" \
"v_mul_f32 v50, 4.0, v50\n" \
"v_mul_f32 v51, 4.0, v51\n" \
"v_mfma_f32_16x16x32_bf16 v[56:59], a[144:147], a[224:227], v[56:59]\n" \
"s_add_u32 s70, s88, 131072\n" \
"s_waitcnt lgkmcnt(1)\n" \
"v_cvt_pk_bf16_f32 v136, v48, v49\n" \
"v_mfma_f32_16x16x32_bf16 v[56:59], a[148:151], a[228:231], v[56:59]\n" \
"buffer_store_dwordx4 v[156:159], v130, s[52:55], s70 offen sc1\n" \
"v_cvt_pk_bf16_f32 v137, v50, v51\n" \
"v_mfma_f32_16x16x32_bf16 v[56:59], a[176:179], a[232:235], v[56:59]\n" \
"ds_write_b64 v128, v[136:137] offset:6912\n" \
"v_mfma_f32_16x16x32_bf16 v[56:59], a[180:183], a[236:239], v[56:59]\n" \
"s_add_u32 s70, s88, 163840\n" \
"s_waitcnt lgkmcnt(1)\n" \
"v_mul_f32 v52, 4.0, v52\n" \
"v_mfma_f32_16x16x32_bf16 v[56:59], v[66:69], v[204:207], v[56:59]\n" \
"buffer_store_dwordx4 v[160:163], v130, s[52:55], s70 offen sc1\n" \
"v_mfma_f32_16x16x32_bf16 v[56:59], a[208:211], a[240:243], v[56:59]\n" \
"v_mul_f32 v53, 4.0, v53\n" \
"v_mul_f32 v54, 4.0, v54\n" \
"v_mfma_f32_16x16x32_bf16 v[56:59], a[212:215], a[244:247], v[56:59]\n" \
"v_mul_f32 v55, 4.0, v55\n" \
"v_cvt_pk_bf16_f32 v138, v52, v53\n" \
"v_cvt_pk_bf16_f32 v139, v54, v55\n" \
"v_mfma_f32_16x16x32_bf16 v[60:63], a[220:223], a[244:247], v[60:63]\n" \
"ds_write_b64 v128, v[138:139] offset:6944\n" \
"v_mfma_f32_16x16x32_bf16 v[60:63], a[216:219], a[240:243], v[60:63]\n" \
"v_mfma_f32_16x16x32_bf16 v[60:63], a[188:191], a[236:239], v[60:63]\n" \
"v_mfma_f32_16x16x32_bf16 v[60:63], a[184:187], a[232:235], v[60:63]\n" \
"v_mul_f32 v56, 4.0, v56\n" \
"v_mul_f32 v57, 4.0, v57\n" \
"v_mfma_f32_16x16x32_bf16 v[60:63], v[64:67], v[204:207], v[60:63]\n" \
"v_mul_f32 v58, 4.0, v58\n" \
"v_mul_f32 v59, 4.0, v59\n" \
"v_mfma_f32_16x16x32_bf16 v[60:63], a[156:159], a[228:231], v[60:63]\n" \
"v_cvt_pk_bf16_f32 v136, v56, v57\n" \
"v_cvt_pk_bf16_f32 v137, v58, v59\n" \
"v_mfma_f32_16x16x32_bf16 v[60:63], a[152:155], a[224:227], v[60:63]\n" \
"ds_write_b64 v128, v[136:137] offset:6976\n" \
"s_nop 7\n" \
"v_mul_f32 v60, 4.0, v60\n" \
"v_mul_f32 v61, 4.0, v61\n" \
"v_mul_f32 v62, 4.0, v62\n" \
"v_mul_f32 v63, 4.0, v63\n" \
"v_cvt_pk_bf16_f32 v138, v60, v61\n" \
"v_cvt_pk_bf16_f32 v139, v62, v63\n" \
"ds_write_b64 v128, v[138:139] offset:7008\n" \
"ds_read_b128 v[164:167], v129 offset:6912\n" \
"ds_read_b128 v[168:171], v129 offset:8064\n" \
"s_add_u32 s70, s88, 196608\n" \
"s_waitcnt lgkmcnt(1)\n" \
"buffer_store_dwordx4 v[164:167], v130, s[52:55], s70 offen sc1\n" \
"s_add_u32 s70, s88, 229376\n" \
"s_waitcnt lgkmcnt(0)\n" \
"buffer_store_dwordx4 v[168:171], v130, s[52:55], s70 offen sc1\n" \
"s_endpgm\n" \
".p2align 12\n" \
"Lrole1_%=:\n" \
"v_mfma_f32_16x16x32_bf16 v[64:67], a[96:99], a[64:67], 0\n" \
"Lpg10891_%=:\n" \
"s_cbranch_execz Lcb1_%=\n" \
"ds_read_b128 a[88:91], v213 offset:4352\n" \
"v_mfma_f32_16x16x32_bf16 v[64:67], a[100:103], a[68:71], v[64:67]\n" \
"s_waitcnt lgkmcnt(2)\n" \
"s_xor_b32 s82, s48, s49\n" \
"ds_read_b128 a[92:95], v213 offset:6400\n" \
"v_mfma_f32_16x16x32_bf16 v[68:71], a[104:107], a[64:67], 0\n" \
"s_cmp_lt_u32 s48, s50\n" \
"s_cselect_b32 s82, s82, s48\n" \
"ds_read_b64_tr_b16 a[112:113], v214 offset:16896\n" \
"v_mfma_f32_16x16x32_bf16 v[68:71], a[108:111], a[68:71], v[68:71]\n" \
"s_add_u32 s81, s82, s46\n" \
"ds_read_b64_tr_b16 a[114:115], v214 offset:18944\n" \
"v_mfma_f32_16x16x32_bf16 v[84:87], a[104:107], a[72:75], 0\n" \
"s_waitcnt lgkmcnt(4)\n" \
"s_add_u32 m0, s86, s79\n" \
"ds_read_b64_tr_b16 a[116:117], v214 offset:25088\n" \
"v_mfma_f32_16x16x32_bf16 v[84:87], a[108:111], a[76:79], v[84:87]\n" \
"buffer_load_dwordx4 v234, s[56:59], s81 offen offset:0 sc1 lds\n" \
"v_mfma_f32_16x16x32_bf16 v[80:83], a[96:99], a[72:75], 0\n" \
"s_sub_u32 s85, s85, 32768\n" \
"s_cselect_b32 s85, 131072, s85\n" \
"ds_read_b64_tr_b16 a[118:119], v214 offset:27136\n" \
"v_mfma_f32_16x16x32_bf16 v[80:83], a[100:103], a[76:79], v[80:83]\n" \
"s_lshl_b32 s82, s82, 5\n" \
"ds_read_b64_tr_b16 a[120:121], v215 offset:16896\n" \
"v_cvt_pk_bf16_f32 v0, v0, v1\n" \
"v_mfma_f32_16x16x32_bf16 v[96:99], a[96:99], a[80:83], 0\n" \
"s_add_u32 s82, s82, s47\n" \
"ds_read_b64_tr_b16 a[122:123], v215 offset:18944\n" \
"v_cvt_pk_bf16_f32 v1, v2, v3\n" \
"v_mfma_f32_16x16x32_bf16 v[96:99], a[100:103], a[84:87], v[96:99]\n" \
"buffer_load_dwordx4 v234, s[56:59], s81 offen offset:1024 sc1 lds\n" \
"v_cvt_pk_bf16_f32 v2, v4, v5\n" \
"v_mfma_f32_16x16x32_bf16 v[100:103], a[104:107], a[80:83], 0\n" \
"s_waitcnt lgkmcnt(3)\n" \
"ds_read_b64_tr_b16 a[124:125], v215 offset:25088\n" \
"v_add_u32 v213, s85, v211\n" \
"v_mfma_f32_16x16x32_bf16 v[100:103], a[108:111], a[84:87], v[100:103]\n" \
"buffer_load_dwordx4 v234, s[56:59], s81 offen offset:2048 sc1 lds\n" \
"v_mfma_f32_16x16x32_bf16 v[116:119], a[104:107], a[88:91], 0\n" \
"ds_read_b64_tr_b16 a[126:127], v215 offset:27136\n" \
"v_cvt_pk_bf16_f32 v3, v6, v7\n" \
"v_mfma_f32_16x16x32_bf16 v[116:119], a[108:111], a[92:95], v[116:119]\n" \
"buffer_load_dwordx4 v234, s[56:59], s81 offen offset:3072 sc1 lds\n" \
"v_cvt_pk_bf16_f32 v4, v8, v9\n" \
"v_mfma_f32_16x16x32_bf16 v[112:115], a[96:99], a[88:91], 0\n" \
"v_add_u32 v214, s85, v212\n" \
"v_xor_b32 v215, 32, v214\n" \
"v_mfma_f32_16x16x32_bf16 v[112:115], a[100:103], a[92:95], v[112:115]\n" \
"v_cvt_pk_bf16_f32 v5, v10, v11\n" \
"s_waitcnt vmcnt(20) lgkmcnt(0)\n" \
"s_barrier\n" \
"v_mfma_f32_16x16x32_bf16 v[72:75], a[112:115], a[64:67], 0\n" \
"s_add_u32 m0, m0, 0x4000\n" \
"ds_read_b64_tr_b16 a[96:97], v214 offset:16384\n" \
"ds_read_b64_tr_b16 a[98:99], v214 offset:18432\n" \
"v_mfma_f32_16x16x32_bf16 v[72:75], a[116:119], a[68:71], v[72:75]\n" \
"buffer_load_dwordx4 v216, s[36:39], s82 offen offset:0 lds\n" \
"v_mfma_f32_16x16x32_bf16 v[76:79], a[120:123], a[64:67], 0\n" \
"ds_read_b64_tr_b16 a[100:101], v214 offset:24576\n" \
"ds_read_b64_tr_b16 a[102:103], v214 offset:26624\n" \
"v_mfma_f32_16x16x32_bf16 v[76:79], a[124:127], a[68:71], v[76:79]\n" \
"ds_read_b128 a[64:67], v213 offset:0\n" \
"v_cvt_pk_bf16_f32 v6, v12, v13\n" \
"v_cvt_pk_bf16_f32 v7, v14, v15\n" \
"v_mfma_f32_16x16x32_bf16 v[92:95], a[120:123], a[72:75], 0\n" \
"s_add_u32 s48, s48, 0x1000\n" \
"ds_read_b128 a[68:71], v213 offset:2048\n" \
"v_cvt_pk_bf16_f32 v8, v16, v17\n" \
"v_mfma_f32_16x16x32_bf16 v[92:95], a[124:127], a[76:79], v[92:95]\n" \
"buffer_load_dwordx4 v217, s[36:39], s82 offen offset:1024 lds\n" \
"v_mfma_f32_16x16x32_bf16 v[88:91], a[112:115], a[72:75], 0\n" \
"ds_read_b64_tr_b16 a[104:105], v215 offset:16384\n" \
"ds_read_b64_tr_b16 a[106:107], v215 offset:18432\n" \
"v_mfma_f32_16x16x32_bf16 v[88:91], a[116:119], a[76:79], v[88:91]\n" \
"ds_read_b128 a[72:75], v213 offset:256\n" \
"v_cvt_pk_bf16_f32 v9, v18, v19\n" \
"v_cvt_pk_bf16_f32 v10, v20, v21\n" \
"v_mfma_f32_16x16x32_bf16 v[104:107], a[112:115], a[80:83], 0\n" \
"s_sub_u32 s86, s86, 32768\n" \
"s_cselect_b32 s86, 131072, s86\n" \
"ds_read_b128 a[76:79], v213 offset:2304\n" \
"v_mfma_f32_16x16x32_bf16 v[104:107], a[116:119], a[84:87], v[104:107]\n" \
"buffer_load_dwordx4 v218, s[36:39], s82 offen offset:2048 lds\n" \
"v_cvt_pk_bf16_f32 v11, v22, v23\n" \
"v_mfma_f32_16x16x32_bf16 v[108:111], a[120:123], a[80:83], 0\n" \
"ds_read_b64_tr_b16 a[108:109], v215 offset:24576\n" \
"ds_read_b64_tr_b16 a[110:111], v215 offset:26624\n" \
"v_mfma_f32_16x16x32_bf16 v[108:111], a[124:127], a[84:87], v[108:111]\n" \
"buffer_load_dwordx4 v219, s[36:39], s82 offen offset:3072 lds\n" \
"v_mfma_f32_16x16x32_bf16 v[124:127], a[120:123], a[88:91], 0\n" \
"s_waitcnt lgkmcnt(3)\n" \
"ds_read_b128 a[80:83], v213 offset:4096\n" \
"v_mfma_f32_16x16x32_bf16 v[124:127], a[124:127], a[92:95], v[124:127]\n" \
"s_add_u32 s71, s87, 0\n" \
"ds_read_b128 a[84:87], v213 offset:6144\n" \
"v_cvt_pk_bf16_f32 v12, v24, v25\n" \
"v_mfma_f32_16x16x32_bf16 v[120:123], a[112:115], a[88:91], 0\n" \
"buffer_store_dwordx4 v[0:3], v234, s[56:59], s71 offen offset:0 sc1\n" \
"v_mfma_f32_16x16x32_bf16 v[120:123], a[116:119], a[92:95], v[120:123]\n" \
"buffer_store_dwordx4 v[4:7], v234, s[56:59], s71 offen offset:1024 sc1\n" \
"buffer_store_dwordx4 v[8:11], v234, s[56:59], s71 offen offset:2048 sc1\n" \
"v_mfma_f32_16x16x32_bf16 v[64:67], a[96:99], a[64:67], v[64:67]\n" \
"s_waitcnt lgkmcnt(2)\n" \
"ds_read_b128 a[88:91], v213 offset:4352\n" \
"v_cvt_pk_bf16_f32 v13, v26, v27\n" \
"v_mfma_f32_16x16x32_bf16 v[64:67], a[100:103], a[68:71], v[64:67]\n" \
"s_xor_b32 s82, s48, s49\n" \
"ds_read_b128 a[92:95], v213 offset:6400\n" \
"v_cvt_pk_bf16_f32 v14, v28, v29\n" \
"v_mfma_f32_16x16x32_bf16 v[68:71], a[104:107], a[64:67], v[68:71]\n" \
"s_cmp_lt_u32 s48, s50\n" \
"s_cselect_b32 s82, s82, s48\n" \
"ds_read_b64_tr_b16 a[112:113], v214 offset:16896\n" \
"v_mfma_f32_16x16x32_bf16 v[68:71], a[108:111], a[68:71], v[68:71]\n" \
"s_add_u32 s81, s82, s46\n" \
"ds_read_b64_tr_b16 a[114:115], v214 offset:18944\n" \
"v_cvt_pk_bf16_f32 v15, v30, v31\n" \
"v_mfma_f32_16x16x32_bf16 v[84:87], a[104:107], a[72:75], v[84:87]\n" \
"s_add_u32 m0, s86, s79\n" \
"ds_read_b64_tr_b16 a[116:117], v214 offset:25088\n" \
"v_cvt_pk_bf16_f32 v16, v32, v33\n" \
"v_mfma_f32_16x16x32_bf16 v[84:87], a[108:111], a[76:79], v[84:87]\n" \
"buffer_load_dwordx4 v234, s[56:59], s81 offen offset:0 sc1 lds\n" \
"v_mfma_f32_16x16x32_bf16 v[80:83], a[96:99], a[72:75], v[80:83]\n" \
"s_sub_u32 s85, s85, 32768\n" \
"s_cselect_b32 s85, 131072, s85\n" \
"ds_read_b64_tr_b16 a[118:119], v214 offset:27136\n" \
"v_mfma_f32_16x16x32_bf16 v[80:83], a[100:103], a[76:79], v[80:83]\n" \
"s_waitcnt lgkmcnt(3)\n" \
"s_lshl_b32 s82, s82, 5\n" \
"ds_read_b64_tr_b16 a[120:121], v215 offset:16896\n" \
"v_cvt_pk_bf16_f32 v17, v34, v35\n" \
"v_mfma_f32_16x16x32_bf16 v[96:99], a[96:99], a[80:83], v[96:99]\n" \
"s_add_u32 s82, s82, s47\n" \
"ds_read_b64_tr_b16 a[122:123], v215 offset:18944\n" \
"v_cvt_pk_bf16_f32 v18, v36, v37\n" \
"v_mfma_f32_16x16x32_bf16 v[96:99], a[100:103], a[84:87], v[96:99]\n" \
"buffer_load_dwordx4 v234, s[56:59], s81 offen offset:1024 sc1 lds\n" \
"v_mfma_f32_16x16x32_bf16 v[100:103], a[104:107], a[80:83], v[100:103]\n" \
"ds_read_b64_tr_b16 a[124:125], v215 offset:25088\n" \
"v_add_u32 v213, s85, v211\n" \
"v_mfma_f32_16x16x32_bf16 v[100:103], a[108:111], a[84:87], v[100:103]\n" \
"buffer_load_dwordx4 v234, s[56:59], s81 offen offset:2048 sc1 lds\n" \
"v_mfma_f32_16x16x32_bf16 v[116:119], a[104:107], a[88:91], v[116:119]\n" \
"ds_read_b64_tr_b16 a[126:127], v215 offset:27136\n" \
"v_cvt_pk_bf16_f32 v19, v38, v39\n" \
"v_mfma_f32_16x16x32_bf16 v[116:119], a[108:111], a[92:95], v[116:119]\n" \
"buffer_load_dwordx4 v234, s[56:59], s81 offen offset:3072 sc1 lds\n" \
"v_cvt_pk_bf16_f32 v20, v40, v41\n" \
"v_mfma_f32_16x16x32_bf16 v[112:115], a[96:99], a[88:91], v[112:115]\n" \
"v_add_u32 v214, s85, v212\n" \
"v_xor_b32 v215, 32, v214\n" \
"v_mfma_f32_16x16x32_bf16 v[112:115], a[100:103], a[92:95], v[112:115]\n" \
"s_waitcnt vmcnt(23) lgkmcnt(0)\n" \
"s_barrier\n" \
"v_mfma_f32_16x16x32_bf16 v[72:75], a[112:115], a[64:67], v[72:75]\n" \
"s_add_u32 m0, m0, 0x4000\n" \
"ds_read_b64_tr_b16 a[96:97], v214 offset:16384\n" \
"ds_read_b64_tr_b16 a[98:99], v214 offset:18432\n" \
"v_mfma_f32_16x16x32_bf16 v[72:75], a[116:119], a[68:71], v[72:75]\n" \
"buffer_load_dwordx4 v216, s[36:39], s82 offen offset:0 lds\n" \
"v_mfma_f32_16x16x32_bf16 v[76:79], a[120:123], a[64:67], v[76:79]\n" \
"ds_read_b64_tr_b16 a[100:101], v214 offset:24576\n" \
"ds_read_b64_tr_b16 a[102:103], v214 offset:26624\n" \
"v_mfma_f32_16x16x32_bf16 v[76:79], a[124:127], a[68:71], v[76:79]\n" \
"ds_read_b128 a[64:67], v213 offset:0\n" \
"v_cvt_pk_bf16_f32 v21, v42, v43\n" \
"v_mfma_f32_16x16x32_bf16 v[92:95], a[120:123], a[72:75], v[92:95]\n" \
"s_add_u32 s48, s48, 0x1000\n" \
"ds_read_b128 a[68:71], v213 offset:2048\n" \
"v_cvt_pk_bf16_f32 v22, v44, v45\n" \
"v_mfma_f32_16x16x32_bf16 v[92:95], a[124:127], a[76:79], v[92:95]\n" \
"buffer_load_dwordx4 v217, s[36:39], s82 offen offset:1024 lds\n" \
"v_mfma_f32_16x16x32_bf16 v[88:91], a[112:115], a[72:75], v[88:91]\n" \
"ds_read_b64_tr_b16 a[104:105], v215 offset:16384\n" \
"ds_read_b64_tr_b16 a[106:107], v215 offset:18432\n" \
"v_mfma_f32_16x16x32_bf16 v[88:91], a[116:119], a[76:79], v[88:91]\n" \
"ds_read_b128 a[72:75], v213 offset:256\n" \
"v_cvt_pk_bf16_f32 v23, v46, v47\n" \
"v_mfma_f32_16x16x32_bf16 v[104:107], a[112:115], a[80:83], v[104:107]\n" \
"s_sub_u32 s86, s86, 32768\n" \
"s_cselect_b32 s86, 131072, s86\n" \
"ds_read_b128 a[76:79], v213 offset:2304\n" \
"v_mfma_f32_16x16x32_bf16 v[104:107], a[116:119], a[84:87], v[104:107]\n" \
"buffer_load_dwordx4 v218, s[36:39], s82 offen offset:2048 lds\n" \
"v_mfma_f32_16x16x32_bf16 v[108:111], a[120:123], a[80:83], v[108:111]\n" \
"ds_read_b64_tr_b16 a[108:109], v215 offset:24576\n" \
"ds_read_b64_tr_b16 a[110:111], v215 offset:26624\n" \
"v_mfma_f32_16x16x32_bf16 v[108:111], a[124:127], a[84:87], v[108:111]\n" \
"buffer_load_dwordx4 v219, s[36:39], s82 offen offset:3072 lds\n" \
"v_mfma_f32_16x16x32_bf16 v[124:127], a[120:123], a[88:91], v[124:127]\n" \
"ds_read_b128 a[80:83], v213 offset:4096\n" \
"s_waitcnt lgkmcnt(4)\n" \
"v_mfma_f32_16x16x32_bf16 v[124:127], a[124:127], a[92:95], v[124:127]\n" \
"ds_read_b128 a[84:87], v213 offset:6144\n" \
"v_cvt_pk_bf16_f32 v24, v48, v49\n" \
"v_mfma_f32_16x16x32_bf16 v[120:123], a[112:115], a[88:91], v[120:123]\n" \
"buffer_store_dwordx4 v[12:15], v234, s[56:59], s71 offen offset:3072 sc1\n" \
"v_mfma_f32_16x16x32_bf16 v[120:123], a[116:119], a[92:95], v[120:123]\n" \
"s_add_u32 s71, s87, 4096\n" \
"buffer_store_dwordx4 v[16:19], v234, s[56:59], s71 offen offset:0 sc1\n" \
"buffer_store_dwordx4 v[20:23], v234, s[56:59], s71 offen offset:1024 sc1\n" \
"v_mfma_f32_16x16x32_bf16 v[64:67], a[96:99], a[64:67], v[64:67]\n" \
"s_waitcnt lgkmcnt(2)\n" \
"ds_read_b128 a[88:91], v213 offset:4352\n" \
"v_cvt_pk_bf16_f32 v25, v50, v51\n" \
"v_mfma_f32_16x16x32_bf16 v[64:67], a[100:103], a[68:71], v[64:67]\n" \
"s_xor_b32 s82, s48, s49\n" \
"ds_read_b128 a[92:95], v213 offset:6400\n" \
"v_cvt_pk_bf16_f32 v26, v52, v53\n" \
"v_mfma_f32_16x16x32_bf16 v[68:71], a[104:107], a[64:67], v[68:71]\n" \
"s_cmp_lt_u32 s48, s50\n" \
"s_cselect_b32 s82, s82, s48\n" \
"ds_read_b64_tr_b16 a[112:113], v214 offset:16896\n" \
"v_mfma_f32_16x16x32_bf16 v[68:71], a[108:111], a[68:71], v[68:71]\n" \
"s_add_u32 s81, s82, s46\n" \
"ds_read_b64_tr_b16 a[114:115], v214 offset:18944\n" \
"v_cvt_pk_bf16_f32 v27, v54, v55\n" \
"v_mfma_f32_16x16x32_bf16 v[84:87], a[104:107], a[72:75], v[84:87]\n" \
"s_add_u32 m0, s86, s79\n" \
"ds_read_b64_tr_b16 a[116:117], v214 offset:25088\n" \
"v_cvt_pk_bf16_f32 v28, v56, v57\n" \
"v_mfma_f32_16x16x32_bf16 v[84:87], a[108:111], a[76:79], v[84:87]\n" \
"buffer_load_dwordx4 v234, s[56:59], s81 offen offset:0 sc1 lds\n" \
"v_mfma_f32_16x16x32_bf16 v[80:83], a[96:99], a[72:75], v[80:83]\n" \
"s_sub_u32 s85, s85, 32768\n" \
"s_cselect_b32 s85, 131072, s85\n" \
"ds_read_b64_tr_b16 a[118:119], v214 offset:27136\n" \
"v_mfma_f32_16x16x32_bf16 v[80:83], a[100:103], a[76:79], v[80:83]\n" \
"s_waitcnt lgkmcnt(3)\n" \
"s_lshl_b32 s82, s82, 5\n" \
"ds_read_b64_tr_b16 a[120:121], v215 offset:16896\n" \
"v_cvt_pk_bf16_f32 v29, v58, v59\n" \
"v_mfma_f32_16x16x32_bf16 v[96:99], a[96:99], a[80:83], v[96:99]\n" \
"s_add_u32 s82, s82, s47\n" \
"ds_read_b64_tr_b16 a[122:123], v215 offset:18944\n" \
"v_cvt_pk_bf16_f32 v30, v60, v61\n" \
"v_mfma_f32_16x16x32_bf16 v[96:99], a[100:103], a[84:87], v[96:99]\n" \
"buffer_load_dwordx4 v234, s[56:59], s81 offen offset:1024 sc1 lds\n" \
"v_mfma_f32_16x16x32_bf16 v[100:103], a[104:107], a[80:83], v[100:103]\n" \
"ds_read_b64_tr_b16 a[124:125], v215 offset:25088\n" \
"v_add_u32 v213, s85, v211\n" \
"v_mfma_f32_16x16x32_bf16 v[100:103], a[108:111], a[84:87], v[100:103]\n" \
"buffer_load_dwordx4 v234, s[56:59], s81 offen offset:2048 sc1 lds\n" \
"v_mfma_f32_16x16x32_bf16 v[116:119], a[104:107], a[88:91], v[116:119]\n" \
"ds_read_b64_tr_b16 a[126:127], v215 offset:27136\n" \
"v_cvt_pk_bf16_f32 v31, v62, v63\n" \
"v_mfma_f32_16x16x32_bf16 v[116:119], a[108:111], a[92:95], v[116:119]\n" \
"buffer_load_dwordx4 v234, s[56:59], s81 offen offset:3072 sc1 lds\n" \
"v_mfma_f32_16x16x32_bf16 v[112:115], a[96:99], a[88:91], v[112:115]\n" \
"v_add_u32 v214, s85, v212\n" \
"v_xor_b32 v215, 32, v214\n" \
"v_mfma_f32_16x16x32_bf16 v[112:115], a[100:103], a[92:95], v[112:115]\n" \
"s_waitcnt vmcnt(26) lgkmcnt(0)\n" \
"s_barrier\n" \
"v_mfma_f32_16x16x32_bf16 v[72:75], a[112:115], a[64:67], v[72:75]\n" \
"s_add_u32 m0, m0, 0x4000\n" \
"ds_read_b64_tr_b16 a[96:97], v214 offset:16384\n" \
"ds_read_b64_tr_b16 a[98:99], v214 offset:18432\n" \
"v_mfma_f32_16x16x32_bf16 v[72:75], a[116:119], a[68:71], v[72:75]\n" \
"buffer_load_dwordx4 v216, s[36:39], s82 offen offset:0 lds\n" \
"v_mfma_f32_16x16x32_bf16 v[76:79], a[120:123], a[64:67], v[76:79]\n" \
"ds_read_b64_tr_b16 a[100:101], v214 offset:24576\n" \
"ds_read_b64_tr_b16 a[102:103], v214 offset:26624\n" \
"v_mfma_f32_16x16x32_bf16 v[76:79], a[124:127], a[68:71], v[76:79]\n" \
"ds_read_b128 a[64:67], v213 offset:0\n" \
"v_mfma_f32_16x16x32_bf16 v[92:95], a[120:123], a[72:75], v[92:95]\n" \
"s_add_u32 s48, s48, 0x1000\n" \
"ds_read_b128 a[68:71], v213 offset:2048\n" \
"v_mfma_f32_16x16x32_bf16 v[92:95], a[124:127], a[76:79], v[92:95]\n" \
"buffer_load_dwordx4 v217, s[36:39], s82 offen offset:1024 lds\n" \
"v_mfma_f32_16x16x32_bf16 v[88:91], a[112:115], a[72:75], v[88:91]\n" \
"ds_read_b64_tr_b16 a[104:105], v215 offset:16384\n" \
"ds_read_b64_tr_b16 a[106:107], v215 offset:18432\n" \
"v_mfma_f32_16x16x32_bf16 v[88:91], a[116:119], a[76:79], v[88:91]\n" \
"ds_read_b128 a[72:75], v213 offset:256\n" \
"v_mfma_f32_16x16x32_bf16 v[104:107], a[112:115], a[80:83], v[104:107]\n" \
"s_sub_u32 s86, s86, 32768\n" \
"s_cselect_b32 s86, 131072, s86\n" \
"ds_read_b128 a[76:79], v213 offset:2304\n" \
"v_mfma_f32_16x16x32_bf16 v[104:107], a[116:119], a[84:87], v[104:107]\n" \
"buffer_load_dwordx4 v218, s[36:39], s82 offen offset:2048 lds\n" \
"v_mfma_f32_16x16x32_bf16 v[108:111], a[120:123], a[80:83], v[108:111]\n" \
"ds_read_b64_tr_b16 a[108:109], v215 offset:24576\n" \
"ds_read_b64_tr_b16 a[110:111], v215 offset:26624\n" \
"v_mfma_f32_16x16x32_bf16 v[108:111], a[124:127], a[84:87], v[108:111]\n" \
"buffer_load_dwordx4 v219, s[36:39], s82 offen offset:3072 lds\n" \
"v_mfma_f32_16x16x32_bf16 v[124:127], a[120:123], a[88:91], v[124:127]\n" \
"ds_read_b128 a[80:83], v213 offset:4096\n" \
"s_waitcnt lgkmcnt(4)\n" \
"v_mfma_f32_16x16x32_bf16 v[124:127], a[124:127], a[92:95], v[124:127]\n" \
"ds_read_b128 a[84:87], v213 offset:6144\n" \
"v_mfma_f32_16x16x32_bf16 v[120:123], a[112:115], a[88:91], v[120:123]\n" \
"buffer_store_dwordx4 v[24:27], v234, s[56:59], s71 offen offset:2048 sc1\n" \
"v_mfma_f32_16x16x32_bf16 v[120:123], a[116:119], a[92:95], v[120:123]\n" \
"buffer_store_dwordx4 v[28:31], v234, s[56:59], s71 offen offset:3072 sc1\n" \
"v_mfma_f32_16x16x32_bf16 v[64:67], a[96:99], a[64:67], v[64:67]\n" \
"ds_read_b128 a[88:91], v213 offset:4352\n" \
"v_mfma_f32_16x16x32_bf16 v[64:67], a[100:103], a[68:71], v[64:67]\n" \
"s_waitcnt lgkmcnt(2)\n" \
"s_xor_b32 s82, s48, s49\n" \
"ds_read_b128 a[92:95], v213 offset:6400\n" \
"v_mfma_f32_16x16x32_bf16 v[68:71], a[104:107], a[64:67], v[68:71]\n" \
"s_cmp_lt_u32 s48, s50\n" \
"s_cselect_b32 s82, s82, s48\n" \
"ds_read_b64_tr_b16 a[112:113], v214 offset:16896\n" \
"v_mfma_f32_16x16x32_bf16 v[68:71], a[108:111], a[68:71], v[68:71]\n" \
"s_add_u32 s81, s82, s46\n" \
"ds_read_b64_tr_b16 a[114:115], v214 offset:18944\n" \
"v_mfma_f32_16x16x32_bf16 v[84:87], a[104:107], a[72:75], v[84:87]\n" \
"s_add_u32 m0, s86, s79\n" \
"ds_read_b64_tr_b16 a[116:117], v214 offset:25088\n" \
"v_mfma_f32_16x16x32_bf16 v[84:87], a[108:111], a[76:79], v[84:87]\n" \
"buffer_load_dwordx4 v234, s[56:59], s81 offen offset:0 sc1 lds\n" \
"v_mfma_f32_16x16x32_bf16 v[80:83], a[96:99], a[72:75], v[80:83]\n" \
"s_sub_u32 s85, s85, 32768\n" \
"s_cselect_b32 s85, 131072, s85\n" \
"ds_read_b64_tr_b16 a[118:119], v214 offset:27136\n" \
"v_mfma_f32_16x16x32_bf16 v[80:83], a[100:103], a[76:79], v[80:83]\n" \
"s_waitcnt lgkmcnt(3)\n" \
"s_lshl_b32 s82, s82, 5\n" \
"ds_read_b64_tr_b16 a[120:121], v215 offset:16896\n" \
"v_mfma_f32_16x16x32_bf16 v[96:99], a[96:99], a[80:83], v[96:99]\n" \
"s_add_u32 s82, s82, s47\n" \
"ds_read_b64_tr_b16 a[122:123], v215 offset:18944\n" \
"v_mfma_f32_16x16x32_bf16 v[96:99], a[100:103], a[84:87], v[96:99]\n" \
"buffer_load_dwordx4 v234, s[56:59], s81 offen offset:1024 sc1 lds\n" \
"v_mfma_f32_16x16x32_bf16 v[100:103], a[104:107], a[80:83], v[100:103]\n" \
"ds_read_b64_tr_b16 a[124:125], v215 offset:25088\n" \
"v_add_u32 v213, s85, v211\n" \
"v_mfma_f32_16x16x32_bf16 v[100:103], a[108:111], a[84:87], v[100:103]\n" \
"buffer_load_dwordx4 v234, s[56:59], s81 offen offset:2048 sc1 lds\n" \
"v_mfma_f32_16x16x32_bf16 v[116:119], a[104:107], a[88:91], v[116:119]\n" \
"ds_read_b64_tr_b16 a[126:127], v215 offset:27136\n" \
"v_mfma_f32_16x16x32_bf16 v[116:119], a[108:111], a[92:95], v[116:119]\n" \
"buffer_load_dwordx4 v234, s[56:59], s81 offen offset:3072 sc1 lds\n" \
"v_mfma_f32_16x16x32_bf16 v[112:115], a[96:99], a[88:91], v[112:115]\n" \
"v_add_u32 v214, s85, v212\n" \
"v_xor_b32 v215, 32, v214\n" \
"v_mfma_f32_16x16x32_bf16 v[112:115], a[100:103], a[92:95], v[112:115]\n" \
"s_waitcnt vmcnt(28) lgkmcnt(0)\n" \
"s_barrier\n" \
"v_mfma_f32_16x16x32_bf16 v[72:75], a[112:115], a[64:67], v[72:75]\n" \
"s_add_u32 m0, m0, 0x4000\n" \
"ds_read_b64_tr_b16 a[96:97], v214 offset:16384\n" \
"ds_read_b64_tr_b16 a[98:99], v214 offset:18432\n" \
"v_mfma_f32_16x16x32_bf16 v[72:75], a[116:119], a[68:71], v[72:75]\n" \
"buffer_load_dwordx4 v216, s[36:39], s82 offen offset:0 lds\n" \
"v_mfma_f32_16x16x32_bf16 v[76:79], a[120:123], a[64:67], v[76:79]\n" \
"ds_read_b64_tr_b16 a[100:101], v214 offset:24576\n" \
"ds_read_b64_tr_b16 a[102:103], v214 offset:26624\n" \
"v_mfma_f32_16x16x32_bf16 v[76:79], a[124:127], a[68:71], v[76:79]\n" \
"ds_read_b128 a[64:67], v213 offset:0\n" \
"v_mfma_f32_16x16x32_bf16 v[92:95], a[120:123], a[72:75], v[92:95]\n" \
"s_add_u32 s48, s48, 0x1000\n" \
"ds_read_b128 a[68:71], v213 offset:2048\n" \
"v_mfma_f32_16x16x32_bf16 v[92:95], a[124:127], a[76:79], v[92:95]\n" \
"buffer_load_dwordx4 v217, s[36:39], s82 offen offset:1024 lds\n" \
"v_mfma_f32_16x16x32_bf16 v[88:91], a[112:115], a[72:75], v[88:91]\n" \
"ds_read_b64_tr_b16 a[104:105], v215 offset:16384\n" \
"ds_read_b64_tr_b16 a[106:107], v215 offset:18432\n" \
"v_mfma_f32_16x16x32_bf16 v[88:91], a[116:119], a[76:79], v[88:91]\n" \
"ds_read_b128 a[72:75], v213 offset:256\n" \
"v_mfma_f32_16x16x32_bf16 v[104:107], a[112:115], a[80:83], v[104:107]\n" \
"s_sub_u32 s86, s86, 32768\n" \
"s_cselect_b32 s86, 131072, s86\n" \
"ds_read_b128 a[76:79], v213 offset:2304\n" \
"v_mfma_f32_16x16x32_bf16 v[104:107], a[116:119], a[84:87], v[104:107]\n" \
"buffer_load_dwordx4 v218, s[36:39], s82 offen offset:2048 lds\n" \
"v_mfma_f32_16x16x32_bf16 v[108:111], a[120:123], a[80:83], v[108:111]\n" \
"ds_read_b64_tr_b16 a[108:109], v215 offset:24576\n" \
"ds_read_b64_tr_b16 a[110:111], v215 offset:26624\n" \
"v_mfma_f32_16x16x32_bf16 v[108:111], a[124:127], a[84:87], v[108:111]\n" \
"buffer_load_dwordx4 v219, s[36:39], s82 offen offset:3072 lds\n" \
"v_mfma_f32_16x16x32_bf16 v[124:127], a[120:123], a[88:91], v[124:127]\n" \
"ds_read_b128 a[80:83], v213 offset:4096\n" \
"s_waitcnt lgkmcnt(4)\n" \
"v_mfma_f32_16x16x32_bf16 v[124:127], a[124:127], a[92:95], v[124:127]\n" \
"ds_read_b128 a[84:87], v213 offset:6144\n" \
"v_mfma_f32_16x16x32_bf16 v[120:123], a[112:115], a[88:91], v[120:123]\n" \
"v_mfma_f32_16x16x32_bf16 v[120:123], a[116:119], a[92:95], v[120:123]\n" \
"v_mfma_f32_16x16x32_bf16 v[64:67], a[96:99], a[64:67], v[64:67]\n" \
"ds_read_b128 a[88:91], v213 offset:4352\n" \
"v_mfma_f32_16x16x32_bf16 v[64:67], a[100:103], a[68:71], v[64:67]\n" \
"s_waitcnt lgkmcnt(2)\n" \
"s_xor_b32 s82, s48, s49\n" \
"ds_read_b128 a[92:95], v213 offset:6400\n" \
"v_mfma_f32_16x16x32_bf16 v[68:71], a[104:107], a[64:67], v[68:71]\n" \
"s_cmp_lt_u32 s48, s50\n" \
"s_cselect_b32 s82, s82, s48\n" \
"ds_read_b64_tr_b16 a[112:113], v214 offset:16896\n" \
"v_mfma_f32_16x16x32_bf16 v[68:71], a[108:111], a[68:71], v[68:71]\n" \
"s_add_u32 s81, s82, s46\n" \
"ds_read_b64_tr_b16 a[114:115], v214 offset:18944\n" \
"v_mfma_f32_16x16x32_bf16 v[84:87], a[104:107], a[72:75], v[84:87]\n" \
"s_add_u32 m0, s86, s79\n" \
"ds_read_b64_tr_b16 a[116:117], v214 offset:25088\n" \
"v_mfma_f32_16x16x32_bf16 v[84:87], a[108:111], a[76:79], v[84:87]\n" \
"buffer_load_dwordx4 v234, s[56:59], s81 offen offset:0 sc1 lds\n" \
"v_mfma_f32_16x16x32_bf16 v[80:83], a[96:99], a[72:75], v[80:83]\n" \
"s_sub_u32 s85, s85, 32768\n" \
"s_cselect_b32 s85, 131072, s85\n" \
"ds_read_b64_tr_b16 a[118:119], v214 offset:27136\n" \
"v_mfma_f32_16x16x32_bf16 v[80:83], a[100:103], a[76:79], v[80:83]\n" \
"s_waitcnt lgkmcnt(3)\n" \
"s_lshl_b32 s82, s82, 5\n" \
"ds_read_b64_tr_b16 a[120:121], v215 offset:16896\n" \
"v_mfma_f32_16x16x32_bf16 v[96:99], a[96:99], a[80:83], v[96:99]\n" \
"s_add_u32 s82, s82, s47\n" \
"ds_read_b64_tr_b16 a[122:123], v215 offset:18944\n" \
"v_mfma_f32_16x16x32_bf16 v[96:99], a[100:103], a[84:87], v[96:99]\n" \
"buffer_load_dwordx4 v234, s[56:59], s81 offen offset:1024 sc1 lds\n" \
"v_mfma_f32_16x16x32_bf16 v[100:103], a[104:107], a[80:83], v[100:103]\n" \
"ds_read_b64_tr_b16 a[124:125], v215 offset:25088\n" \
"v_add_u32 v213, s85, v211\n" \
"v_mfma_f32_16x16x32_bf16 v[100:103], a[108:111], a[84:87], v[100:103]\n" \
"buffer_load_dwordx4 v234, s[56:59], s81 offen offset:2048 sc1 lds\n" \
"v_mfma_f32_16x16x32_bf16 v[116:119], a[104:107], a[88:91], v[116:119]\n" \
"ds_read_b64_tr_b16 a[126:127], v215 offset:27136\n" \
"v_mfma_f32_16x16x32_bf16 v[116:119], a[108:111], a[92:95], v[116:119]\n" \
"buffer_load_dwordx4 v234, s[56:59], s81 offen offset:3072 sc1 lds\n" \
"v_mfma_f32_16x16x32_bf16 v[112:115], a[96:99], a[88:91], v[112:115]\n" \
"v_add_u32 v214, s85, v212\n" \
"v_xor_b32 v215, 32, v214\n" \
"v_mfma_f32_16x16x32_bf16 v[112:115], a[100:103], a[92:95], v[112:115]\n" \
"s_waitcnt vmcnt(25) lgkmcnt(0)\n" \
"s_barrier\n" \
"v_mfma_f32_16x16x32_bf16 v[72:75], a[112:115], a[64:67], v[72:75]\n" \
"s_add_u32 m0, m0, 0x4000\n" \
"ds_read_b64_tr_b16 a[96:97], v214 offset:16384\n" \
"ds_read_b64_tr_b16 a[98:99], v214 offset:18432\n" \
"v_mfma_f32_16x16x32_bf16 v[72:75], a[116:119], a[68:71], v[72:75]\n" \
"buffer_load_dwordx4 v216, s[36:39], s82 offen offset:0 lds\n" \
"v_mfma_f32_16x16x32_bf16 v[76:79], a[120:123], a[64:67], v[76:79]\n" \
"ds_read_b64_tr_b16 a[100:101], v214 offset:24576\n" \
"ds_read_b64_tr_b16 a[102:103], v214 offset:26624\n" \
"v_mfma_f32_16x16x32_bf16 v[76:79], a[124:127], a[68:71], v[76:79]\n" \
"ds_read_b128 a[64:67], v213 offset:0\n" \
"v_mfma_f32_16x16x32_bf16 v[92:95], a[120:123], a[72:75], v[92:95]\n" \
"s_add_u32 s48, s48, 0x1000\n" \
"ds_read_b128 a[68:71], v213 offset:2048\n" \
"v_mfma_f32_16x16x32_bf16 v[92:95], a[124:127], a[76:79], v[92:95]\n" \
"buffer_load_dwordx4 v217, s[36:39], s82 offen offset:1024 lds\n" \
"v_mfma_f32_16x16x32_bf16 v[88:91], a[112:115], a[72:75], v[88:91]\n" \
"ds_read_b64_tr_b16 a[104:105], v215 offset:16384\n" \
"ds_read_b64_tr_b16 a[106:107], v215 offset:18432\n" \
"v_mfma_f32_16x16x32_bf16 v[88:91], a[116:119], a[76:79], v[88:91]\n" \
"ds_read_b128 a[72:75], v213 offset:256\n" \
"v_mfma_f32_16x16x32_bf16 v[104:107], a[112:115], a[80:83], v[104:107]\n" \
"s_sub_u32 s86, s86, 32768\n" \
"s_cselect_b32 s86, 131072, s86\n" \
"ds_read_b128 a[76:79], v213 offset:2304\n" \
"v_mfma_f32_16x16x32_bf16 v[104:107], a[116:119], a[84:87], v[104:107]\n" \
"buffer_load_dwordx4 v218, s[36:39], s82 offen offset:2048 lds\n" \
"v_mfma_f32_16x16x32_bf16 v[108:111], a[120:123], a[80:83], v[108:111]\n" \
"ds_read_b64_tr_b16 a[108:109], v215 offset:24576\n" \
"ds_read_b64_tr_b16 a[110:111], v215 offset:26624\n" \
"v_mfma_f32_16x16x32_bf16 v[108:111], a[124:127], a[84:87], v[108:111]\n" \
"buffer_load_dwordx4 v219, s[36:39], s82 offen offset:3072 lds\n" \
"v_mfma_f32_16x16x32_bf16 v[124:127], a[120:123], a[88:91], v[124:127]\n" \
"ds_read_b128 a[80:83], v213 offset:4096\n" \
"s_waitcnt lgkmcnt(4)\n" \
"v_mfma_f32_16x16x32_bf16 v[124:127], a[124:127], a[92:95], v[124:127]\n" \
"ds_read_b128 a[84:87], v213 offset:6144\n" \
"v_mfma_f32_16x16x32_bf16 v[120:123], a[112:115], a[88:91], v[120:123]\n" \
"v_mfma_f32_16x16x32_bf16 v[120:123], a[116:119], a[92:95], v[120:123]\n" \
"v_mfma_f32_16x16x32_bf16 v[64:67], a[96:99], a[64:67], v[64:67]\n" \
"ds_read_b128 a[88:91], v213 offset:4352\n" \
"v_mfma_f32_16x16x32_bf16 v[64:67], a[100:103], a[68:71], v[64:67]\n" \
"s_waitcnt lgkmcnt(2)\n" \
"s_xor_b32 s82, s48, s49\n" \
"ds_read_b128 a[92:95], v213 offset:6400\n" \
"v_mfma_f32_16x16x32_bf16 v[68:71], a[104:107], a[64:67], v[68:71]\n" \
"s_cmp_lt_u32 s48, s50\n" \
"s_cselect_b32 s82, s82, s48\n" \
"ds_read_b64_tr_b16 a[112:113], v214 offset:16896\n" \
"v_mfma_f32_16x16x32_bf16 v[68:71], a[108:111], a[68:71], v[68:71]\n" \
"s_add_u32 s81, s82, s46\n" \
"ds_read_b64_tr_b16 a[114:115], v214 offset:18944\n" \
"v_mfma_f32_16x16x32_bf16 v[84:87], a[104:107], a[72:75], v[84:87]\n" \
"s_add_u32 m0, s86, s79\n" \
"ds_read_b64_tr_b16 a[116:117], v214 offset:25088\n" \
"v_mfma_f32_16x16x32_bf16 v[84:87], a[108:111], a[76:79], v[84:87]\n" \
"buffer_load_dwordx4 v234, s[56:59], s81 offen offset:0 sc1 lds\n" \
"v_mfma_f32_16x16x32_bf16 v[80:83], a[96:99], a[72:75], v[80:83]\n" \
"s_sub_u32 s85, s85, 32768\n" \
"s_cselect_b32 s85, 131072, s85\n" \
"ds_read_b64_tr_b16 a[118:119], v214 offset:27136\n" \
"v_mfma_f32_16x16x32_bf16 v[80:83], a[100:103], a[76:79], v[80:83]\n" \
"s_waitcnt lgkmcnt(3)\n" \
"s_lshl_b32 s82, s82, 5\n" \
"ds_read_b64_tr_b16 a[120:121], v215 offset:16896\n" \
"v_mfma_f32_16x16x32_bf16 v[96:99], a[96:99], a[80:83], v[96:99]\n" \
"s_add_u32 s82, s82, s47\n" \
"ds_read_b64_tr_b16 a[122:123], v215 offset:18944\n" \
"v_mfma_f32_16x16x32_bf16 v[96:99], a[100:103], a[84:87], v[96:99]\n" \
"buffer_load_dwordx4 v234, s[56:59], s81 offen offset:1024 sc1 lds\n" \
"v_mfma_f32_16x16x32_bf16 v[100:103], a[104:107], a[80:83], v[100:103]\n" \
"ds_read_b64_tr_b16 a[124:125], v215 offset:25088\n" \
"v_add_u32 v213, s85, v211\n" \
"v_mfma_f32_16x16x32_bf16 v[100:103], a[108:111], a[84:87], v[100:103]\n" \
"buffer_load_dwordx4 v234, s[56:59], s81 offen offset:2048 sc1 lds\n" \
"v_mfma_f32_16x16x32_bf16 v[116:119], a[104:107], a[88:91], v[116:119]\n" \
"ds_read_b64_tr_b16 a[126:127], v215 offset:27136\n" \
"v_mfma_f32_16x16x32_bf16 v[116:119], a[108:111], a[92:95], v[116:119]\n" \
"buffer_load_dwordx4 v234, s[56:59], s81 offen offset:3072 sc1 lds\n" \
"v_mfma_f32_16x16x32_bf16 v[112:115], a[96:99], a[88:91], v[112:115]\n" \
"v_add_u32 v214, s85, v212\n" \
"v_xor_b32 v215, 32, v214\n" \
"v_mfma_f32_16x16x32_bf16 v[112:115], a[100:103], a[92:95], v[112:115]\n" \
"s_waitcnt vmcnt(20) lgkmcnt(0)\n" \
"s_barrier\n" \
"buffer_store_dword v230, v231, s[56:59], s64 offen sc1\n" \
"v_mfma_f32_16x16x32_bf16 v[72:75], a[112:115], a[64:67], v[72:75]\n" \
"s_add_u32 m0, m0, 0x4000\n" \
"ds_read_b64_tr_b16 a[96:97], v214 offset:16384\n" \
"ds_read_b64_tr_b16 a[98:99], v214 offset:18432\n" \
"v_mfma_f32_16x16x32_bf16 v[72:75], a[116:119], a[68:71], v[72:75]\n" \
"buffer_load_dwordx4 v216, s[36:39], s82 offen offset:0 lds\n" \
"v_mfma_f32_16x16x32_bf16 v[76:79], a[120:123], a[64:67], v[76:79]\n" \
"ds_read_b64_tr_b16 a[100:101], v214 offset:24576\n" \
"ds_read_b64_tr_b16 a[102:103], v214 offset:26624\n" \
"v_mfma_f32_16x16x32_bf16 v[76:79], a[124:127], a[68:71], v[76:79]\n" \
"ds_read_b128 a[64:67], v213 offset:0\n" \
"v_mfma_f32_16x16x32_bf16 v[92:95], a[120:123], a[72:75], v[92:95]\n" \
"s_add_u32 s48, s48, 0x1000\n" \
"ds_read_b128 a[68:71], v213 offset:2048\n" \
"v_mfma_f32_16x16x32_bf16 v[92:95], a[124:127], a[76:79], v[92:95]\n" \
"buffer_load_dwordx4 v217, s[36:39], s82 offen offset:1024 lds\n" \
"v_mfma_f32_16x16x32_bf16 v[88:91], a[112:115], a[72:75], v[88:91]\n" \
"ds_read_b64_tr_b16 a[104:105], v215 offset:16384\n" \
"ds_read_b64_tr_b16 a[106:107], v215 offset:18432\n" \
"v_mfma_f32_16x16x32_bf16 v[88:91], a[116:119], a[76:79], v[88:91]\n" \
"ds_read_b128 a[72:75], v213 offset:256\n" \
"v_mfma_f32_16x16x32_bf16 v[104:107], a[112:115], a[80:83], v[104:107]\n" \
"s_sub_u32 s86, s86, 32768\n" \
"s_cselect_b32 s86, 131072, s86\n" \
"ds_read_b128 a[76:79], v213 offset:2304\n" \
"v_mfma_f32_16x16x32_bf16 v[104:107], a[116:119], a[84:87], v[104:107]\n" \
"buffer_load_dwordx4 v218, s[36:39], s82 offen offset:2048 lds\n" \
"v_mfma_f32_16x16x32_bf16 v[108:111], a[120:123], a[80:83], v[108:111]\n" \
"ds_read_b64_tr_b16 a[108:109], v215 offset:24576\n" \
"ds_read_b64_tr_b16 a[110:111], v215 offset:26624\n" \
"v_mfma_f32_16x16x32_bf16 v[108:111], a[124:127], a[84:87], v[108:111]\n" \
"buffer_load_dwordx4 v219, s[36:39], s82 offen offset:3072 lds\n" \
"v_mfma_f32_16x16x32_bf16 v[124:127], a[120:123], a[88:91], v[124:127]\n" \
"ds_read_b128 a[80:83], v213 offset:4096\n" \
"s_waitcnt lgkmcnt(4)\n" \
"v_mfma_f32_16x16x32_bf16 v[124:127], a[124:127], a[92:95], v[124:127]\n" \
"ds_read_b128 a[84:87], v213 offset:6144\n" \
"v_mfma_f32_16x16x32_bf16 v[120:123], a[112:115], a[88:91], v[120:123]\n" \
"v_mfma_f32_16x16x32_bf16 v[120:123], a[116:119], a[92:95], v[120:123]\n" \
"s_mov_b32 s72, 5\n" \
"s_nop 0\n" \
"s_nop 0\n" \
"Lo64_%=:\n" \
"v_mfma_f32_16x16x32_bf16 v[64:67], a[96:99], a[64:67], v[64:67]\n" \
"ds_read_b128 a[88:91], v213 offset:4352\n" \
"v_mfma_f32_16x16x32_bf16 v[64:67], a[100:103], a[68:71], v[64:67]\n" \
"Lpg11452_%=:\n" \
"s_cbranch_execz Lcb1_%=\n" \
"s_waitcnt lgkmcnt(2)\n" \
"s_xor_b32 s82, s48, s49\n" \
"ds_read_b128 a[92:95], v213 offset:6400\n" \
"v_mfma_f32_16x16x32_bf16 v[68:71], a[104:107], a[64:67], v[68:71]\n" \
"s_cmp_lt_u32 s48, s50\n" \
"s_cselect_b32 s82, s82, s48\n" \
"ds_read_b64_tr_b16 a[112:113], v214 offset:16896\n" \
"v_mfma_f32_16x16x32_bf16 v[68:71], a[108:111], a[68:71], v[68:71]\n" \
"s_add_u32 s81, s82, s46\n" \
"ds_read_b64_tr_b16 a[114:115], v214 offset:18944\n" \
"v_mfma_f32_16x16x32_bf16 v[84:87], a[104:107], a[72:75], v[84:87]\n" \
"s_add_u32 m0, s86, s79\n" \
"ds_read_b64_tr_b16 a[116:117], v214 offset:25088\n" \
"v_mfma_f32_16x16x32_bf16 v[84:87], a[108:111], a[76:79], v[84:87]\n" \
"buffer_load_dwordx4 v234, s[56:59], s81 offen offset:0 sc1 lds\n" \
"v_mfma_f32_16x16x32_bf16 v[80:83], a[96:99], a[72:75], v[80:83]\n" \
"s_sub_u32 s85, s85, 32768\n" \
"s_cselect_b32 s85, 131072, s85\n" \
"ds_read_b64_tr_b16 a[118:119], v214 offset:27136\n" \
"v_mfma_f32_16x16x32_bf16 v[80:83], a[100:103], a[76:79], v[80:83]\n" \
"s_waitcnt lgkmcnt(3)\n" \
"s_lshl_b32 s82, s82, 5\n" \
"ds_read_b64_tr_b16 a[120:121], v215 offset:16896\n" \
"v_mfma_f32_16x16x32_bf16 v[96:99], a[96:99], a[80:83], v[96:99]\n" \
"s_add_u32 s82, s82, s47\n" \
"ds_read_b64_tr_b16 a[122:123], v215 offset:18944\n" \
"v_mfma_f32_16x16x32_bf16 v[96:99], a[100:103], a[84:87], v[96:99]\n" \
"buffer_load_dwordx4 v234, s[56:59], s81 offen offset:1024 sc1 lds\n" \
"v_mfma_f32_16x16x32_bf16 v[100:103], a[104:107], a[80:83], v[100:103]\n" \
"ds_read_b64_tr_b16 a[124:125], v215 offset:25088\n" \
"v_add_u32 v213, s85, v211\n" \
"v_mfma_f32_16x16x32_bf16 v[100:103], a[108:111], a[84:87], v[100:103]\n" \
"buffer_load_dwordx4 v234, s[56:59], s81 offen offset:2048 sc1 lds\n" \
"v_mfma_f32_16x16x32_bf16 v[116:119], a[104:107], a[88:91], v[116:119]\n" \
"ds_read_b64_tr_b16 a[126:127], v215 offset:27136\n" \
"v_mfma_f32_16x16x32_bf16 v[116:119], a[108:111], a[92:95], v[116:119]\n" \
"buffer_load_dwordx4 v234, s[56:59], s81 offen offset:3072 sc1 lds\n" \
"v_mfma_f32_16x16x32_bf16 v[112:115], a[96:99], a[88:91], v[112:115]\n" \
"v_add_u32 v214, s85, v212\n" \
"v_xor_b32 v215, 32, v214\n" \
"v_mfma_f32_16x16x32_bf16 v[112:115], a[100:103], a[92:95], v[112:115]\n" \
"s_waitcnt vmcnt(20) lgkmcnt(0)\n" \
"s_barrier\n" \
"v_mfma_f32_16x16x32_bf16 v[72:75], a[112:115], a[64:67], v[72:75]\n" \
"s_add_u32 m0, m0, 0x4000\n" \
"ds_read_b64_tr_b16 a[96:97], v214 offset:16384\n" \
"ds_read_b64_tr_b16 a[98:99], v214 offset:18432\n" \
"v_mfma_f32_16x16x32_bf16 v[72:75], a[116:119], a[68:71], v[72:75]\n" \
"buffer_load_dwordx4 v216, s[36:39], s82 offen offset:0 lds\n" \
"v_mfma_f32_16x16x32_bf16 v[76:79], a[120:123], a[64:67], v[76:79]\n" \
"ds_read_b64_tr_b16 a[100:101], v214 offset:24576\n" \
"ds_read_b64_tr_b16 a[102:103], v214 offset:26624\n" \
"v_mfma_f32_16x16x32_bf16 v[76:79], a[124:127], a[68:71], v[76:79]\n" \
"ds_read_b128 a[64:67], v213 offset:0\n" \
"v_mfma_f32_16x16x32_bf16 v[92:95], a[120:123], a[72:75], v[92:95]\n" \
"s_add_u32 s48, s48, 0x1000\n" \
"ds_read_b128 a[68:71], v213 offset:2048\n" \
"v_mfma_f32_16x16x32_bf16 v[92:95], a[124:127], a[76:79], v[92:95]\n" \
"buffer_load_dwordx4 v217, s[36:39], s82 offen offset:1024 lds\n" \
"v_mfma_f32_16x16x32_bf16 v[88:91], a[112:115], a[72:75], v[88:91]\n" \
"ds_read_b64_tr_b16 a[104:105], v215 offset:16384\n" \
"ds_read_b64_tr_b16 a[106:107], v215 offset:18432\n" \
"v_mfma_f32_16x16x32_bf16 v[88:91], a[116:119], a[76:79], v[88:91]\n" \
"ds_read_b128 a[72:75], v213 offset:256\n" \
"v_mfma_f32_16x16x32_bf16 v[104:107], a[112:115], a[80:83], v[104:107]\n" \
"s_sub_u32 s86, s86, 32768\n" \
"s_cselect_b32 s86, 131072, s86\n" \
"ds_read_b128 a[76:79], v213 offset:2304\n" \
"v_mfma_f32_16x16x32_bf16 v[104:107], a[116:119], a[84:87], v[104:107]\n" \
"buffer_load_dwordx4 v218, s[36:39], s82 offen offset:2048 lds\n" \
"v_mfma_f32_16x16x32_bf16 v[108:111], a[120:123], a[80:83], v[108:111]\n" \
"ds_read_b64_tr_b16 a[108:109], v215 offset:24576\n" \
"ds_read_b64_tr_b16 a[110:111], v215 offset:26624\n" \
"v_mfma_f32_16x16x32_bf16 v[108:111], a[124:127], a[84:87], v[108:111]\n" \
"buffer_load_dwordx4 v219, s[36:39], s82 offen offset:3072 lds\n" \
"v_mfma_f32_16x16x32_bf16 v[124:127], a[120:123], a[88:91], v[124:127]\n" \
"ds_read_b128 a[80:83], v213 offset:4096\n" \
"s_waitcnt lgkmcnt(4)\n" \
"v_mfma_f32_16x16x32_bf16 v[124:127], a[124:127], a[92:95], v[124:127]\n" \
"s_sub_u32 s72, s72, 1\n" \
"ds_read_b128 a[84:87], v213 offset:6144\n" \
"v_mfma_f32_16x16x32_bf16 v[120:123], a[112:115], a[88:91], v[120:123]\n" \
"v_mfma_f32_16x16x32_bf16 v[120:123], a[116:119], a[92:95], v[120:123]\n" \
"s_cbranch_scc0 Lo64_%=\n" \
"v_mfma_f32_16x16x32_bf16 v[64:67], a[96:99], a[64:67], v[64:67]\n" \
"s_waitcnt lgkmcnt(2)\n" \
"ds_read_b128 a[88:91], v213 offset:4352\n" \
"v_mfma_f32_16x16x32_bf16 v[64:67], a[100:103], a[68:71], v[64:67]\n" \
"s_add_u32 s48, s86, 32768\n" \
"s_cmp_eq_u32 s48, 163840\n" \
"s_cselect_b32 s48, 0, s48\n" \
"ds_read_b128 a[92:95], v213 offset:6400\n" \
"v_mfma_f32_16x16x32_bf16 v[68:71], a[104:107], a[64:67], v[68:71]\n" \
"ds_read_b64_tr_b16 a[112:113], v214 offset:16896\n" \
"v_mfma_f32_16x16x32_bf16 v[68:71], a[108:111], a[68:71], v[68:71]\n" \
"s_add_u32 s49, s48, 32768\n" \
"s_cmp_eq_u32 s49, 163840\n" \
"s_cselect_b32 s49, 0, s49\n" \
"ds_read_b64_tr_b16 a[114:115], v214 offset:18944\n" \
"v_mfma_f32_16x16x32_bf16 v[84:87], a[104:107], a[72:75], v[84:87]\n" \
"ds_read_b64_tr_b16 a[116:117], v214 offset:25088\n" \
"v_add_u32 v224, s49, v211\n" \
"v_mfma_f32_16x16x32_bf16 v[84:87], a[108:111], a[76:79], v[84:87]\n" \
"v_add_u32 v225, s49, v212\n" \
"v_xor_b32 v226, 32, v225\n" \
"v_mfma_f32_16x16x32_bf16 v[80:83], a[96:99], a[72:75], v[80:83]\n" \
"s_sub_u32 s85, s85, 32768\n" \
"s_cselect_b32 s85, 131072, s85\n" \
"ds_read_b64_tr_b16 a[118:119], v214 offset:27136\n" \
"v_mfma_f32_16x16x32_bf16 v[80:83], a[100:103], a[76:79], v[80:83]\n" \
"s_waitcnt lgkmcnt(3)\n" \
"ds_read_b64_tr_b16 a[120:121], v215 offset:16896\n" \
"v_add_u32 v227, s48, v211\n" \
"v_mfma_f32_16x16x32_bf16 v[96:99], a[96:99], a[80:83], v[96:99]\n" \
"ds_read_b64_tr_b16 a[122:123], v215 offset:18944\n" \
"v_add_u32 v228, s48, v212\n" \
"v_mfma_f32_16x16x32_bf16 v[96:99], a[100:103], a[84:87], v[96:99]\n" \
"v_xor_b32 v229, 32, v228\n" \
"v_mfma_f32_16x16x32_bf16 v[100:103], a[104:107], a[80:83], v[100:103]\n" \
"ds_read_b64_tr_b16 a[124:125], v215 offset:25088\n" \
"v_add_u32 v213, s85, v211\n" \
"v_mfma_f32_16x16x32_bf16 v[100:103], a[108:111], a[84:87], v[100:103]\n" \
"v_mfma_f32_16x16x32_bf16 v[116:119], a[104:107], a[88:91], v[116:119]\n" \
"ds_read_b64_tr_b16 a[126:127], v215 offset:27136\n" \
"v_mfma_f32_16x16x32_bf16 v[116:119], a[108:111], a[92:95], v[116:119]\n" \
"v_mfma_f32_16x16x32_bf16 v[112:115], a[96:99], a[88:91], v[112:115]\n" \
"v_add_u32 v214, s85, v212\n" \
"v_xor_b32 v215, 32, v214\n" \
"v_mfma_f32_16x16x32_bf16 v[112:115], a[100:103], a[92:95], v[112:115]\n" \
"s_waitcnt vmcnt(8) lgkmcnt(0)\n" \
"s_barrier\n" \
"v_mfma_f32_16x16x32_bf16 v[72:75], a[112:115], a[64:67], v[72:75]\n" \
"ds_read_b128 a[0:3], v213 offset:0\n" \
"ds_read_b128 a[4:7], v213 offset:2048\n" \
"v_mfma_f32_16x16x32_bf16 v[72:75], a[116:119], a[68:71], v[72:75]\n" \
"s_sub_u32 s68, s86, 32768\n" \
"s_cselect_b32 s68, 131072, s68\n" \
"ds_read_b64_tr_b16 a[128:129], v214 offset:16384\n" \
"ds_read_b64_tr_b16 a[130:131], v214 offset:18432\n" \
"v_mfma_f32_16x16x32_bf16 v[76:79], a[120:123], a[64:67], v[76:79]\n" \
"ds_read_b64_tr_b16 a[132:133], v214 offset:24576\n" \
"ds_read_b64_tr_b16 a[134:135], v214 offset:26624\n" \
"v_mfma_f32_16x16x32_bf16 v[76:79], a[124:127], a[68:71], v[76:79]\n" \
"s_cmp_lt_u32 s61, 2\n" \
"s_cselect_b32 s68, s86, s68\n" \
"ds_read_b64_tr_b16 a[136:137], v215 offset:16384\n" \
"ds_read_b64_tr_b16 a[138:139], v215 offset:18432\n" \
"v_mfma_f32_16x16x32_bf16 v[92:95], a[120:123], a[72:75], v[92:95]\n" \
"s_and_b32 s50, s61, 1\n" \
"ds_read_b64_tr_b16 a[140:141], v215 offset:24576\n" \
"ds_read_b64_tr_b16 a[142:143], v215 offset:26624\n" \
"v_mfma_f32_16x16x32_bf16 v[92:95], a[124:127], a[76:79], v[92:95]\n" \
"s_mul_i32 s50, s50, 9216\n" \
"ds_read_b64_tr_b16 a[144:145], v214 offset:16896\n" \
"ds_read_b64_tr_b16 a[146:147], v214 offset:18944\n" \
"v_mfma_f32_16x16x32_bf16 v[88:91], a[112:115], a[72:75], v[88:91]\n" \
"s_add_u32 s68, s68, s50\n" \
"ds_read_b64_tr_b16 a[148:149], v214 offset:25088\n" \
"ds_read_b64_tr_b16 a[150:151], v214 offset:27136\n" \
"v_mfma_f32_16x16x32_bf16 v[88:91], a[116:119], a[76:79], v[88:91]\n" \
"s_add_u32 s68, s68, %3\n" \
"ds_read_b64_tr_b16 a[152:153], v215 offset:16896\n" \
"ds_read_b64_tr_b16 a[154:155], v215 offset:18944\n" \
"v_mfma_f32_16x16x32_bf16 v[104:107], a[112:115], a[80:83], v[104:107]\n" \
"ds_read_b64_tr_b16 a[156:157], v215 offset:25088\n" \
"ds_read_b64_tr_b16 a[158:159], v215 offset:27136\n" \
"v_mfma_f32_16x16x32_bf16 v[104:107], a[116:119], a[84:87], v[104:107]\n" \
"ds_read_b128 a[24:27], v213 offset:256\n" \
"ds_read_b128 a[28:31], v213 offset:2304\n" \
"v_mfma_f32_16x16x32_bf16 v[108:111], a[120:123], a[80:83], v[108:111]\n" \
"ds_read_b128 a[8:11], v224 offset:0\n" \
"ds_read_b128 a[12:15], v224 offset:2048\n" \
"v_mfma_f32_16x16x32_bf16 v[108:111], a[124:127], a[84:87], v[108:111]\n" \
"ds_read_b64_tr_b16 a[160:161], v225 offset:16384\n" \
"ds_read_b64_tr_b16 a[162:163], v225 offset:18432\n" \
"v_mfma_f32_16x16x32_bf16 v[124:127], a[120:123], a[88:91], v[124:127]\n" \
"ds_read_b64_tr_b16 a[164:165], v225 offset:24576\n" \
"ds_read_b64_tr_b16 a[166:167], v225 offset:26624\n" \
"v_mfma_f32_16x16x32_bf16 v[124:127], a[124:127], a[92:95], v[124:127]\n" \
"ds_read_b64_tr_b16 a[168:169], v226 offset:16384\n" \
"ds_read_b64_tr_b16 a[170:171], v226 offset:18432\n" \
"v_mfma_f32_16x16x32_bf16 v[120:123], a[112:115], a[88:91], v[120:123]\n" \
"ds_read_b64_tr_b16 a[172:173], v226 offset:24576\n" \
"ds_read_b64_tr_b16 a[174:175], v226 offset:26624\n" \
"v_mfma_f32_16x16x32_bf16 v[120:123], a[116:119], a[92:95], v[120:123]\n" \
"s_waitcnt lgkmcnt(8)\n" \
"ds_read_b64_tr_b16 a[176:177], v225 offset:16896\n" \
"ds_read_b64_tr_b16 a[178:179], v225 offset:18944\n" \
"v_mfma_f32_16x16x32_bf16 v[64:67], a[128:131], a[0:3], v[64:67]\n" \
"ds_read_b64_tr_b16 a[180:181], v225 offset:25088\n" \
"ds_read_b64_tr_b16 a[182:183], v225 offset:27136\n" \
"v_mfma_f32_16x16x32_bf16 v[64:67], a[132:135], a[4:7], v[64:67]\n" \
"ds_read_b64_tr_b16 a[184:185], v226 offset:16896\n" \
"ds_read_b64_tr_b16 a[186:187], v226 offset:18944\n" \
"v_mfma_f32_16x16x32_bf16 v[68:71], a[136:139], a[0:3], v[68:71]\n" \
"ds_read_b64_tr_b16 a[188:189], v226 offset:25088\n" \
"ds_read_b64_tr_b16 a[190:191], v226 offset:27136\n" \
"v_mfma_f32_16x16x32_bf16 v[68:71], a[140:143], a[4:7], v[68:71]\n" \
"v_lshrrev_b32 v132, 4, v234\n" \
"v_and_b32 v133, 15, v132\n" \
"v_mfma_f32_16x16x32_bf16 v[72:75], a[144:147], a[0:3], v[72:75]\n" \
"v_mul_u32_u24 v128, 144, v133\n" \
"v_lshrrev_b32 v133, 4, v132\n" \
"v_mfma_f32_16x16x32_bf16 v[72:75], a[148:151], a[4:7], v[72:75]\n" \
"v_lshl_add_u32 v128, v133, 3, v128\n" \
"v_add_u32 v128, s68, v128\n" \
"v_mfma_f32_16x16x32_bf16 v[76:79], a[152:155], a[0:3], v[76:79]\n" \
"v_lshrrev_b32 v133, 3, v132\n" \
"v_and_b32 v134, 7, v132\n" \
"v_mfma_f32_16x16x32_bf16 v[76:79], a[156:159], a[4:7], v[76:79]\n" \
"v_mul_u32_u24 v129, 144, v133\n" \
"v_lshl_add_u32 v129, v134, 4, v129\n" \
"v_mfma_f32_16x16x32_bf16 v[92:95], a[152:155], a[24:27], v[92:95]\n" \
"v_add_u32 v129, s68, v129\n" \
"v_lshlrev_b32 v134, 4, v134\n" \
"v_mfma_f32_16x16x32_bf16 v[92:95], a[156:159], a[28:31], v[92:95]\n" \
"v_lshl_add_u32 v130, v133, 13, v134\n" \
"v_mfma_f32_16x16x32_bf16 v[88:91], a[144:147], a[24:27], v[88:91]\n" \
"v_mfma_f32_16x16x32_bf16 v[88:91], a[148:151], a[28:31], v[88:91]\n" \
"v_mfma_f32_16x16x32_bf16 v[84:87], a[136:139], a[24:27], v[84:87]\n" \
"s_waitcnt vmcnt(0)\n" \
"s_barrier\n" \
"v_mfma_f32_16x16x32_bf16 v[84:87], a[140:143], a[28:31], v[84:87]\n" \
"ds_read_b128 a[32:35], v224 offset:256\n" \
"ds_read_b128 a[36:39], v224 offset:2304\n" \
"v_mfma_f32_16x16x32_bf16 v[80:83], a[128:131], a[24:27], v[80:83]\n" \
"ds_read_b128 a[16:19], v227 offset:0\n" \
"ds_read_b128 a[20:23], v227 offset:2048\n" \
"v_mfma_f32_16x16x32_bf16 v[80:83], a[132:135], a[28:31], v[80:83]\n" \
"s_waitcnt lgkmcnt(4)\n" \
"ds_read_b64_tr_b16 a[192:193], v228 offset:16384\n" \
"ds_read_b64_tr_b16 a[194:195], v228 offset:18432\n" \
"v_mfma_f32_16x16x32_bf16 v[64:67], a[160:163], a[8:11], v[64:67]\n" \
"ds_read_b64_tr_b16 a[196:197], v228 offset:24576\n" \
"ds_read_b64_tr_b16 a[198:199], v228 offset:26624\n" \
"v_mfma_f32_16x16x32_bf16 v[64:67], a[164:167], a[12:15], v[64:67]\n" \
"ds_read_b64_tr_b16 a[200:201], v229 offset:16384\n" \
"ds_read_b64_tr_b16 a[202:203], v229 offset:18432\n" \
"v_mfma_f32_16x16x32_bf16 v[68:71], a[168:171], a[8:11], v[68:71]\n" \
"ds_read_b64_tr_b16 a[204:205], v229 offset:24576\n" \
"ds_read_b64_tr_b16 a[206:207], v229 offset:26624\n" \
"v_mfma_f32_16x16x32_bf16 v[68:71], a[172:175], a[12:15], v[68:71]\n" \
"ds_read_b64_tr_b16 a[208:209], v228 offset:16896\n" \
"ds_read_b64_tr_b16 a[210:211], v228 offset:18944\n" \
"v_mfma_f32_16x16x32_bf16 v[72:75], a[176:179], a[8:11], v[72:75]\n" \
"ds_read_b64_tr_b16 a[212:213], v228 offset:25088\n" \
"ds_read_b64_tr_b16 a[214:215], v228 offset:27136\n" \
"v_mfma_f32_16x16x32_bf16 v[72:75], a[180:183], a[12:15], v[72:75]\n" \
"ds_read_b64_tr_b16 a[216:217], v229 offset:16896\n" \
"ds_read_b64_tr_b16 a[218:219], v229 offset:18944\n" \
"v_mfma_f32_16x16x32_bf16 v[76:79], a[184:187], a[8:11], v[76:79]\n" \
"ds_read_b64_tr_b16 a[220:221], v229 offset:25088\n" \
"ds_read_b64_tr_b16 a[222:223], v229 offset:27136\n" \
"v_mfma_f32_16x16x32_bf16 v[76:79], a[188:191], a[12:15], v[76:79]\n" \
"s_waitcnt lgkmcnt(8)\n" \
"ds_read_b128 a[40:43], v227 offset:256\n" \
"ds_read_b128 a[44:47], v227 offset:2304\n" \
"v_mfma_f32_16x16x32_bf16 v[92:95], a[184:187], a[32:35], v[92:95]\n" \
"ds_read_b128 a[248:251], v213 offset:4096\n" \
"ds_read_b128 a[252:255], v213 offset:6144\n" \
"v_mfma_f32_16x16x32_bf16 v[92:95], a[188:191], a[36:39], v[92:95]\n" \
"ds_read_b128 a[48:51], v224 offset:4096\n" \
"ds_read_b128 a[52:55], v224 offset:6144\n" \
"v_mfma_f32_16x16x32_bf16 v[88:91], a[176:179], a[32:35], v[88:91]\n" \
"ds_read_b128 a[56:59], v227 offset:4096\n" \
"ds_read_b128 a[60:63], v227 offset:6144\n" \
"v_mfma_f32_16x16x32_bf16 v[88:91], a[180:183], a[36:39], v[88:91]\n" \
"ds_read_b128 a[224:227], v213 offset:4352\n" \
"ds_read_b128 a[228:231], v213 offset:6400\n" \
"v_mfma_f32_16x16x32_bf16 v[84:87], a[168:171], a[32:35], v[84:87]\n" \
"ds_read_b128 a[232:235], v224 offset:4352\n" \
"ds_read_b128 a[236:239], v224 offset:6400\n" \
"v_mfma_f32_16x16x32_bf16 v[84:87], a[172:175], a[36:39], v[84:87]\n" \
"ds_read_b128 a[240:243], v227 offset:4352\n" \
"ds_read_b128 a[244:247], v227 offset:6400\n" \
"v_mfma_f32_16x16x32_bf16 v[80:83], a[160:163], a[32:35], v[80:83]\n" \
"v_mfma_f32_16x16x32_bf16 v[80:83], a[164:167], a[36:39], v[80:83]\n" \
"v_mfma_f32_16x16x32_bf16 v[64:67], a[192:195], a[16:19], v[64:67]\n" \
"v_mfma_f32_16x16x32_bf16 v[64:67], a[196:199], a[20:23], v[64:67]\n" \
"v_mfma_f32_16x16x32_bf16 v[68:71], a[200:203], a[16:19], v[68:71]\n" \
"v_mfma_f32_16x16x32_bf16 v[68:71], a[204:207], a[20:23], v[68:71]\n" \
"s_waitcnt lgkmcnt(0)\n" \
"v_mfma_f32_16x16x32_bf16 v[72:75], a[208:211], a[16:19], v[72:75]\n" \
"v_mfma_f32_16x16x32_bf16 v[72:75], a[212:215], a[20:23], v[72:75]\n" \
"v_cvt_pk_bf16_f32 v136, v64, v65\n" \
"v_cvt_pk_bf16_f32 v137, v66, v67\n" \
"v_mfma_f32_16x16x32_bf16 v[76:79], a[216:219], a[16:19], v[76:79]\n" \
"v_mfma_f32_16x16x32_bf16 v[76:79], a[220:223], a[20:23], v[76:79]\n" \
"ds_write_b64 v128, v[136:137] offset:0\n" \
"v_mfma_f32_16x16x32_bf16 v[80:83], a[192:195], a[40:43], v[80:83]\n" \
"v_cvt_pk_bf16_f32 v138, v68, v69\n" \
"v_cvt_pk_bf16_f32 v139, v70, v71\n" \
"v_mfma_f32_16x16x32_bf16 v[80:83], a[196:199], a[44:47], v[80:83]\n" \
"ds_write_b64 v128, v[138:139] offset:32\n" \
"v_mfma_f32_16x16x32_bf16 v[84:87], a[200:203], a[40:43], v[84:87]\n" \
"v_cvt_pk_bf16_f32 v136, v72, v73\n" \
"v_cvt_pk_bf16_f32 v137, v74, v75\n" \
"v_mfma_f32_16x16x32_bf16 v[84:87], a[204:207], a[44:47], v[84:87]\n" \
"ds_write_b64 v128, v[136:137] offset:64\n" \
"v_mfma_f32_16x16x32_bf16 v[88:91], a[208:211], a[40:43], v[88:91]\n" \
"v_cvt_pk_bf16_f32 v138, v76, v77\n" \
"v_cvt_pk_bf16_f32 v139, v78, v79\n" \
"v_mfma_f32_16x16x32_bf16 v[88:91], a[212:215], a[44:47], v[88:91]\n" \
"ds_write_b64 v128, v[138:139] offset:96\n" \
"v_mfma_f32_16x16x32_bf16 v[92:95], a[216:219], a[40:43], v[92:95]\n" \
"ds_read_b128 v[140:143], v129 offset:0\n" \
"ds_read_b128 v[144:147], v129 offset:1152\n" \
"v_mfma_f32_16x16x32_bf16 v[92:95], a[220:223], a[44:47], v[92:95]\n" \
"v_cvt_pk_bf16_f32 v136, v80, v81\n" \
"v_cvt_pk_bf16_f32 v137, v82, v83\n" \
"v_mfma_f32_16x16x32_bf16 v[96:99], a[128:131], a[248:251], v[96:99]\n" \
"ds_write_b64 v128, v[136:137] offset:2304\n" \
"v_mfma_f32_16x16x32_bf16 v[96:99], a[132:135], a[252:255], v[96:99]\n" \
"s_add_u32 s70, s88, 0\n" \
"s_waitcnt lgkmcnt(2)\n" \
"v_cvt_pk_bf16_f32 v138, v84, v85\n" \
"v_mfma_f32_16x16x32_bf16 v[96:99], a[160:163], a[48:51], v[96:99]\n" \
"buffer_store_dwordx4 v[140:143], v130, s[52:55], s70 offen sc1\n" \
"v_mfma_f32_16x16x32_bf16 v[96:99], a[164:167], a[52:55], v[96:99]\n" \
"v_cvt_pk_bf16_f32 v139, v86, v87\n" \
"v_mfma_f32_16x16x32_bf16 v[96:99], a[192:195], a[56:59], v[96:99]\n" \
"s_add_u32 s70, s88, 65536\n" \
"s_waitcnt lgkmcnt(1)\n" \
"v_mfma_f32_16x16x32_bf16 v[96:99], a[196:199], a[60:63], v[96:99]\n" \
"buffer_store_dwordx4 v[144:147], v130, s[52:55], s70 offen sc1\n" \
"ds_write_b64 v128, v[138:139] offset:2336\n" \
"v_mfma_f32_16x16x32_bf16 v[100:103], a[204:207], a[60:63], v[100:103]\n" \
"v_cvt_pk_bf16_f32 v136, v88, v89\n" \
"v_cvt_pk_bf16_f32 v137, v90, v91\n" \
"v_mfma_f32_16x16x32_bf16 v[100:103], a[200:203], a[56:59], v[100:103]\n" \
"ds_write_b64 v128, v[136:137] offset:2368\n" \
"v_mfma_f32_16x16x32_bf16 v[100:103], a[172:175], a[52:55], v[100:103]\n" \
"v_cvt_pk_bf16_f32 v138, v92, v93\n" \
"v_cvt_pk_bf16_f32 v139, v94, v95\n" \
"v_mfma_f32_16x16x32_bf16 v[100:103], a[168:171], a[48:51], v[100:103]\n" \
"v_mfma_f32_16x16x32_bf16 v[100:103], a[140:143], a[252:255], v[100:103]\n" \
"v_mfma_f32_16x16x32_bf16 v[100:103], a[136:139], a[248:251], v[100:103]\n" \
"ds_write_b64 v128, v[138:139] offset:2400\n" \
"v_mfma_f32_16x16x32_bf16 v[104:107], a[144:147], a[248:251], v[104:107]\n" \
"ds_read_b128 v[148:151], v129 offset:2304\n" \
"ds_read_b128 v[152:155], v129 offset:3456\n" \
"v_mfma_f32_16x16x32_bf16 v[104:107], a[148:151], a[252:255], v[104:107]\n" \
"v_cvt_pk_bf16_f32 v136, v96, v97\n" \
"v_cvt_pk_bf16_f32 v137, v98, v99\n" \
"v_mfma_f32_16x16x32_bf16 v[104:107], a[176:179], a[48:51], v[104:107]\n" \
"ds_write_b64 v128, v[136:137] offset:4608\n" \
"v_mfma_f32_16x16x32_bf16 v[104:107], a[180:183], a[52:55], v[104:107]\n" \
"v_cvt_pk_bf16_f32 v138, v100, v101\n" \
"v_cvt_pk_bf16_f32 v139, v102, v103\n" \
"v_mfma_f32_16x16x32_bf16 v[104:107], a[208:211], a[56:59], v[104:107]\n" \
"s_add_u32 s70, s88, 4096\n" \
"s_waitcnt lgkmcnt(2)\n" \
"v_mfma_f32_16x16x32_bf16 v[104:107], a[212:215], a[60:63], v[104:107]\n" \
"buffer_store_dwordx4 v[148:151], v130, s[52:55], s70 offen sc1\n" \
"ds_write_b64 v128, v[138:139] offset:4640\n" \
"v_mfma_f32_16x16x32_bf16 v[108:111], a[220:223], a[60:63], v[108:111]\n" \
"s_add_u32 s70, s88, 69632\n" \
"s_waitcnt lgkmcnt(2)\n" \
"v_mfma_f32_16x16x32_bf16 v[108:111], a[216:219], a[56:59], v[108:111]\n" \
"buffer_store_dwordx4 v[152:155], v130, s[52:55], s70 offen sc1\n" \
"v_mfma_f32_16x16x32_bf16 v[108:111], a[188:191], a[52:55], v[108:111]\n" \
"v_mfma_f32_16x16x32_bf16 v[108:111], a[184:187], a[48:51], v[108:111]\n" \
"v_cvt_pk_bf16_f32 v136, v104, v105\n" \
"v_cvt_pk_bf16_f32 v137, v106, v107\n" \
"v_mfma_f32_16x16x32_bf16 v[108:111], a[156:159], a[252:255], v[108:111]\n" \
"v_mfma_f32_16x16x32_bf16 v[108:111], a[152:155], a[248:251], v[108:111]\n" \
"ds_write_b64 v128, v[136:137] offset:4672\n" \
"v_mfma_f32_16x16x32_bf16 v[112:115], a[128:131], a[224:227], v[112:115]\n" \
"v_mfma_f32_16x16x32_bf16 v[112:115], a[132:135], a[228:231], v[112:115]\n" \
"v_mfma_f32_16x16x32_bf16 v[112:115], a[160:163], a[232:235], v[112:115]\n" \
"v_mfma_f32_16x16x32_bf16 v[112:115], a[164:167], a[236:239], v[112:115]\n" \
"v_cvt_pk_bf16_f32 v138, v108, v109\n" \
"v_cvt_pk_bf16_f32 v139, v110, v111\n" \
"v_mfma_f32_16x16x32_bf16 v[112:115], a[192:195], a[240:243], v[112:115]\n" \
"v_mfma_f32_16x16x32_bf16 v[112:115], a[196:199], a[244:247], v[112:115]\n" \
"ds_write_b64 v128, v[138:139] offset:4704\n" \
"v_mfma_f32_16x16x32_bf16 v[116:119], a[204:207], a[244:247], v[116:119]\n" \
"ds_read_b128 v[156:159], v129 offset:4608\n" \
"ds_read_b128 v[160:163], v129 offset:5760\n" \
"v_mfma_f32_16x16x32_bf16 v[116:119], a[200:203], a[240:243], v[116:119]\n" \
"v_mfma_f32_16x16x32_bf16 v[116:119], a[172:175], a[236:239], v[116:119]\n" \
"v_mfma_f32_16x16x32_bf16 v[116:119], a[168:171], a[232:235], v[116:119]\n" \
"v_cvt_pk_bf16_f32 v136, v112, v113\n" \
"v_cvt_pk_bf16_f32 v137, v114, v115\n" \
"v_mfma_f32_16x16x32_bf16 v[116:119], a[140:143], a[228:231], v[116:119]\n" \
"s_add_u32 s70, s88, 131072\n" \
"s_waitcnt lgkmcnt(1)\n" \
"v_mfma_f32_16x16x32_bf16 v[116:119], a[136:139], a[224:227], v[116:119]\n" \
"buffer_store_dwordx4 v[156:159], v130, s[52:55], s70 offen sc1\n" \
"ds_write_b64 v128, v[136:137] offset:6912\n" \
"v_mfma_f32_16x16x32_bf16 v[120:123], a[144:147], a[224:227], v[120:123]\n" \
"s_add_u32 s70, s88, 196608\n" \
"s_waitcnt lgkmcnt(1)\n" \
"v_mfma_f32_16x16x32_bf16 v[120:123], a[148:151], a[228:231], v[120:123]\n" \
"buffer_store_dwordx4 v[160:163], v130, s[52:55], s70 offen sc1\n" \
"v_mfma_f32_16x16x32_bf16 v[120:123], a[176:179], a[232:235], v[120:123]\n" \
"v_mfma_f32_16x16x32_bf16 v[120:123], a[180:183], a[236:239], v[120:123]\n" \
"v_cvt_pk_bf16_f32 v138, v116, v117\n" \
"v_cvt_pk_bf16_f32 v139, v118, v119\n" \
"v_mfma_f32_16x16x32_bf16 v[120:123], a[208:211], a[240:243], v[120:123]\n" \
"v_mfma_f32_16x16x32_bf16 v[120:123], a[212:215], a[244:247], v[120:123]\n" \
"ds_write_b64 v128, v[138:139] offset:6944\n" \
"v_mfma_f32_16x16x32_bf16 v[124:127], a[220:223], a[244:247], v[124:127]\n" \
"v_mfma_f32_16x16x32_bf16 v[124:127], a[216:219], a[240:243], v[124:127]\n" \
"v_mfma_f32_16x16x32_bf16 v[124:127], a[188:191], a[236:239], v[124:127]\n" \
"v_mfma_f32_16x16x32_bf16 v[124:127], a[184:187], a[232:235], v[124:127]\n" \
"v_cvt_pk_bf16_f32 v136, v120, v121\n" \
"v_cvt_pk_bf16_f32 v137, v122, v123\n" \
"v_mfma_f32_16x16x32_bf16 v[124:127], a[156:159], a[228:231], v[124:127]\n" \
"v_mfma_f32_16x16x32_bf16 v[124:127], a[152:155], a[224:227], v[124:127]\n" \
"ds_write_b64 v128, v[136:137] offset:6976\n" \
"s_nop 7\n" \
"v_cvt_pk_bf16_f32 v138, v124, v125\n" \
"v_cvt_pk_bf16_f32 v139, v126, v127\n" \
"ds_write_b64 v128, v[138:139] offset:7008\n" \
"ds_read_b128 v[164:167], v129 offset:6912\n" \
"ds_read_b128 v[168:171], v129 offset:8064\n" \
"s_add_u32 s70, s88, 135168\n" \
"s_waitcnt lgkmcnt(1)\n" \
"buffer_store_dwordx4 v[164:167], v130, s[52:55], s70 offen sc1\n" \
"s_add_u32 s70, s88, 200704\n" \
"s_waitcnt lgkmcnt(0)\n" \
"buffer_store_dwordx4 v[168:171], v130, s[52:55], s70 offen sc1\n" \
"s_endpgm\n" \
"Lhot_end_%=:\n" \
"Lpl1_%=:\n" \
"s_mov_b32 s72, 0\n" \
"Lpk1_%=:\n" \
"s_sleep 2\n" \
"buffer_load_dword v227, v242, s[56:59], s90 offen sc1\n" \
"s_waitcnt vmcnt(0)\n" \
"v_cmp_eq_u32 vcc, s60, v227\n" \
"s_nop 1\n" \
"s_andn2_b32 s70, 0xff, vcc_lo\n" \
"s_cbranch_scc0 Lpr1_%=\n" \
"s_add_u32 s72, s72, 1\n" \
"s_cmp_lt_u32 s72, 0x40000\n" \
"s_cbranch_scc1 Lpk1_%=\n" \
"s_branch Lpr1_%=\n" \
"Lpl2_%=:\n" \
"s_mov_b32 s72, 0\n" \
"Lpk2_%=:\n" \
"s_sleep 2\n" \
"buffer_load_dword v239, v242, s[56:59], s95 offen sc1\n" \
"s_waitcnt vmcnt(0)\n" \
"v_cmp_eq_u32 vcc, s60, v239\n" \
"s_nop 1\n" \
"s_andn2_b32 s70, s69, vcc_lo\n" \
"s_cbranch_scc0 Lpr2_%=\n" \
"s_add_u32 s72, s72, 1\n" \
"s_cmp_lt_u32 s72, 0x40000\n" \
"s_cbranch_scc1 Lpk2_%=\n" \
"s_branch Lpr2_%=\n" \
"Lpl3_%=:\n" \
"s_mov_b32 s72, 0\n" \
"Lpk3_%=:\n" \
"s_sleep 2\n" \
"buffer_load_dword v239, v242, s[56:59], s95 offen sc1\n" \
"s_waitcnt vmcnt(0)\n" \
"v_cmp_eq_u32 vcc, s60, v239\n" \
"s_nop 1\n" \
"s_andn2_b32 s70, s69, vcc_lo\n" \
"s_cbranch_scc0 Lpr3_%=\n" \
"s_add_u32 s72, s72, 1\n" \
"s_cmp_lt_u32 s72, 0x40000\n" \
"s_cbranch_scc1 Lpk3_%=\n" \
"s_branch Lpr3_%=\n" \
"Lexact_%=:\n" \
"v_mov_b32 v239, 0xff800000\n" \
"s_and_b32 s70, s61, 1\n" \
"s_lshl_b32 s70, s70, 4\n" \
"v_add_u32 v186, s70, v238\n" \
"v_accvgpr_read_b32 v160, a128\n" \
"v_accvgpr_read_b32 v161, a129\n" \
"v_accvgpr_read_b32 v162, a130\n" \
"v_accvgpr_read_b32 v163, a131\n" \
"v_accvgpr_read_b32 v164, a132\n" \
"v_accvgpr_read_b32 v165, a133\n" \
"v_accvgpr_read_b32 v166, a134\n" \
"v_accvgpr_read_b32 v167, a135\n" \
"v_accvgpr_read_b32 v168, a136\n" \
"v_accvgpr_read_b32 v169, a137\n" \
"v_accvgpr_read_b32 v170, a138\n" \
"v_accvgpr_read_b32 v171, a139\n" \
"v_accvgpr_read_b32 v172, a140\n" \
"v_accvgpr_read_b32 v173, a141\n" \
"v_accvgpr_read_b32 v174, a142\n" \
"v_accvgpr_read_b32 v175, a143\n" \
"s_add_u32 s83, s85, 0\n" \
"s_add_u32 s84, s87, 0\n" \
"s_call_b64 s[64:65], Lex1_%=\n" \
"v_accvgpr_read_b32 v160, a144\n" \
"v_accvgpr_read_b32 v161, a145\n" \
"v_accvgpr_read_b32 v162, a146\n" \
"v_accvgpr_read_b32 v163, a147\n" \
"v_accvgpr_read_b32 v164, a148\n" \
"v_accvgpr_read_b32 v165, a149\n" \
"v_accvgpr_read_b32 v166, a150\n" \
"v_accvgpr_read_b32 v167, a151\n" \
"v_accvgpr_read_b32 v168, a152\n" \
"v_accvgpr_read_b32 v169, a153\n" \
"v_accvgpr_read_b32 v170, a154\n" \
"v_accvgpr_read_b32 v171, a155\n" \
"v_accvgpr_read_b32 v172, a156\n" \
"v_accvgpr_read_b32 v173, a157\n" \
"v_accvgpr_read_b32 v174, a158\n" \
"v_accvgpr_read_b32 v175, a159\n" \
"s_add_u32 s83, s85, 2\n" \
"s_add_u32 s84, s87, 532480\n" \
"s_call_b64 s[64:65], Lex1_%=\n" \
"v_accvgpr_read_b32 v160, a160\n" \
"v_accvgpr_read_b32 v161, a161\n" \
"v_accvgpr_read_b32 v162, a162\n" \
"v_accvgpr_read_b32 v163, a163\n" \
"v_accvgpr_read_b32 v164, a164\n" \
"v_accvgpr_read_b32 v165, a165\n" \
"v_accvgpr_read_b32 v166, a166\n" \
"v_accvgpr_read_b32 v167, a167\n" \
"v_accvgpr_read_b32 v168, a168\n" \
"v_accvgpr_read_b32 v169, a169\n" \
"v_accvgpr_read_b32 v170, a170\n" \
"v_accvgpr_read_b32 v171, a171\n" \
"v_accvgpr_read_b32 v172, a172\n" \
"v_accvgpr_read_b32 v173, a173\n" \
"v_accvgpr_read_b32 v174, a174\n" \
"v_accvgpr_read_b32 v175, a175\n" \
"s_add_u32 s83, s86, 0\n" \
"s_add_u32 s84, s88, 0\n" \
"s_call_b64 s[64:65], Lex1_%=\n" \
"v_accvgpr_read_b32 v160, a176\n" \
"v_accvgpr_read_b32 v161, a177\n" \
"v_accvgpr_read_b32 v162, a178\n" \
"v_accvgpr_read_b32 v163, a179\n" \
"v_accvgpr_read_b32 v164, a180\n" \
"v_accvgpr_read_b32 v165, a181\n" \
"v_accvgpr_read_b32 v166, a182\n" \
"v_accvgpr_read_b32 v167, a183\n" \
"v_accvgpr_read_b32 v168, a184\n" \
"v_accvgpr_read_b32 v169, a185\n" \
"v_accvgpr_read_b32 v170, a186\n" \
"v_accvgpr_read_b32 v171, a187\n" \
"v_accvgpr_read_b32 v172, a188\n" \
"v_accvgpr_read_b32 v173, a189\n" \
"v_accvgpr_read_b32 v174, a190\n" \
"v_accvgpr_read_b32 v175, a191\n" \
"s_add_u32 s83, s86, 2\n" \
"s_add_u32 s84, s88, 532480\n" \
"s_call_b64 s[64:65], Lex1_%=\n" \
"s_waitcnt vmcnt(0)\n" \
"s_branch Lexr_%=\n" \
"Lex1_%=:\n" \
"v_mov_b32 v128, 0\n" \
"v_mov_b32 v129, 0\n" \
"v_mov_b32 v130, 0\n" \
"v_mov_b32 v131, 0\n" \
"v_mov_b32 v132, 0\n" \
"v_mov_b32 v133, 0\n" \
"v_mov_b32 v134, 0\n" \
"v_mov_b32 v135, 0\n" \
"v_mov_b32 v136, 0\n" \
"v_mov_b32 v137, 0\n" \
"v_mov_b32 v138, 0\n" \
"v_mov_b32 v139, 0\n" \
"v_mov_b32 v140, 0\n" \
"v_mov_b32 v141, 0\n" \
"v_mov_b32 v142, 0\n" \
"v_mov_b32 v143, 0\n" \
"v_mov_b32 v144, 0\n" \
"v_mov_b32 v145, 0\n" \
"v_mov_b32 v146, 0\n" \
"v_mov_b32 v147, 0\n" \
"v_mov_b32 v148, 0\n" \
"v_mov_b32 v149, 0\n" \
"v_mov_b32 v150, 0\n" \
"v_mov_b32 v151, 0\n" \
"v_mov_b32 v152, 0\n" \
"v_mov_b32 v153, 0\n" \
"v_mov_b32 v154, 0\n" \
"v_mov_b32 v155, 0\n" \
"v_mov_b32 v156, 0\n" \
"v_mov_b32 v157, 0\n" \
"v_mov_b32 v158, 0\n" \
"v_mov_b32 v159, 0\n" \
"v_mov_b32 v184, 0xf149f2ca\n" \
"v_mov_b32 v185, 0\n" \
"s_mov_b32 s81, 0\n" \
"s_mul_i32 s82, s62, 557824\n" \
"Lex2_%=:\n" \
"s_add_u32 s70, s82, 4096\n" \
"buffer_load_dwordx4 a[64:67], v236, s[56:59], s82 offen offset:0 sc1\n" \
"buffer_load_dwordx4 a[68:71], v236, s[56:59], s82 offen offset:2048 sc1\n" \
"buffer_load_dwordx4 a[72:75], v236, s[56:59], s70 offen offset:0 sc1\n" \
"buffer_load_dwordx4 a[76:79], v236, s[56:59], s70 offen offset:2048 sc1\n" \
"buffer_load_dwordx4 a[80:83], v236, s[56:59], s82 offen offset:256 sc1\n" \
"buffer_load_dwordx4 a[84:87], v236, s[56:59], s82 offen offset:2304 sc1\n" \
"buffer_load_dwordx4 a[88:91], v236, s[56:59], s70 offen offset:256 sc1\n" \
"buffer_load_dwordx4 a[92:95], v236, s[56:59], s70 offen offset:2304 sc1\n" \
"s_add_u32 s70, s82, 17850368\n" \
"s_add_u32 s71, s70, 4096\n" \
"buffer_load_dwordx4 a[96:99], v234, s[56:59], s70 offen offset:0 sc1\n" \
"buffer_load_dwordx4 a[100:103], v234, s[56:59], s70 offen offset:1024 sc1\n" \
"buffer_load_dwordx4 a[104:107], v234, s[56:59], s70 offen offset:2048 sc1\n" \
"buffer_load_dwordx4 a[108:111], v234, s[56:59], s70 offen offset:3072 sc1\n" \
"buffer_load_dwordx4 a[112:115], v234, s[56:59], s71 offen offset:0 sc1\n" \
"buffer_load_dwordx4 a[116:119], v234, s[56:59], s71 offen offset:1024 sc1\n" \
"buffer_load_dwordx4 a[120:123], v234, s[56:59], s71 offen offset:2048 sc1\n" \
"buffer_load_dwordx4 a[124:127], v234, s[56:59], s71 offen offset:3072 sc1\n" \
"s_waitcnt vmcnt(0)\n" \
"v_mfma_f32_16x16x32_bf16 v[176:179], a[64:67], v[160:163], 0\n" \
"v_mfma_f32_16x16x32_bf16 v[176:179], a[68:71], v[164:167], v[176:179]\n" \
"v_mfma_f32_16x16x32_bf16 v[176:179], a[72:75], v[168:171], v[176:179]\n" \
"v_mfma_f32_16x16x32_bf16 v[176:179], a[76:79], v[172:175], v[176:179]\n" \
"v_mfma_f32_16x16x32_bf16 v[180:183], a[80:83], v[160:163], 0\n" \
"v_mfma_f32_16x16x32_bf16 v[180:183], a[84:87], v[164:167], v[180:183]\n" \
"v_mfma_f32_16x16x32_bf16 v[180:183], a[88:91], v[168:171], v[180:183]\n" \
"v_mfma_f32_16x16x32_bf16 v[180:183], a[92:95], v[172:175], v[180:183]\n" \
"s_nop 7\n" \
"s_nop 1\n" \
"s_cmp_eq_u32 s81, s83\n" \
"s_cbranch_scc0 Lex3_%=\n" \
"v_cmp_gt_i32 vcc, 0, v186\n" \
"s_nop 1\n" \
"v_cndmask_b32 v176, v176, v239, vcc\n" \
"v_cmp_gt_i32 vcc, 1, v186\n" \
"s_nop 1\n" \
"v_cndmask_b32 v177, v177, v239, vcc\n" \
"v_cmp_gt_i32 vcc, 2, v186\n" \
"s_nop 1\n" \
"v_cndmask_b32 v178, v178, v239, vcc\n" \
"v_cmp_gt_i32 vcc, 3, v186\n" \
"s_nop 1\n" \
"v_cndmask_b32 v179, v179, v239, vcc\n" \
"v_cmp_gt_i32 vcc, 16, v186\n" \
"s_nop 1\n" \
"v_cndmask_b32 v180, v180, v239, vcc\n" \
"v_cmp_gt_i32 vcc, 17, v186\n" \
"s_nop 1\n" \
"v_cndmask_b32 v181, v181, v239, vcc\n" \
"v_cmp_gt_i32 vcc, 18, v186\n" \
"s_nop 1\n" \
"v_cndmask_b32 v182, v182, v239, vcc\n" \
"v_cmp_gt_i32 vcc, 19, v186\n" \
"s_nop 1\n" \
"v_cndmask_b32 v183, v183, v239, vcc\n" \
"Lex3_%=:\n" \
"v_max3_f32 v187, v176, v177, v178\n" \
"v_max3_f32 v187, v187, v179, v180\n" \
"v_max3_f32 v187, v187, v181, v182\n" \
"v_max3_f32 v187, v187, v183, v184\n" \
"v_mov_b32 v188, v187\n" \
"s_nop 1\n" \
"v_permlane16_swap_b32 v188, v187\n" \
"v_max_f32 v187, v187, v188\n" \
"v_mov_b32 v188, v187\n" \
"s_nop 1\n" \
"v_permlane32_swap_b32 v188, v187\n" \
"v_max_f32 v187, v187, v188\n" \
"v_sub_f32 v189, v184, v187\n" \
"v_exp_f32 v189, v189\n" \
"v_mov_b32 v184, v187\n" \
"v_sub_f32 v176, v176, v187\n" \
"v_sub_f32 v177, v177, v187\n" \
"v_sub_f32 v178, v178, v187\n" \
"v_sub_f32 v179, v179, v187\n" \
"v_sub_f32 v180, v180, v187\n" \
"v_sub_f32 v181, v181, v187\n" \
"v_sub_f32 v182, v182, v187\n" \
"v_sub_f32 v183, v183, v187\n" \
"v_exp_f32 v176, v176\n" \
"v_exp_f32 v177, v177\n" \
"v_exp_f32 v178, v178\n" \
"v_exp_f32 v179, v179\n" \
"v_exp_f32 v180, v180\n" \
"v_exp_f32 v181, v181\n" \
"v_exp_f32 v182, v182\n" \
"v_exp_f32 v183, v183\n" \
"v_mul_f32 v185, v185, v189\n" \
"v_add_f32 v185, v185, v176\n" \
"v_add_f32 v185, v185, v177\n" \
"v_add_f32 v185, v185, v178\n" \
"v_add_f32 v185, v185, v179\n" \
"v_add_f32 v185, v185, v180\n" \
"v_add_f32 v185, v185, v181\n" \
"v_add_f32 v185, v185, v182\n" \
"v_add_f32 v185, v185, v183\n" \
"v_cvt_pk_bf16_f32 v112, v176, v177\n" \
"v_cvt_pk_bf16_f32 v113, v178, v179\n" \
"v_cvt_pk_bf16_f32 v114, v180, v181\n" \
"v_cvt_pk_bf16_f32 v115, v182, v183\n" \
"v_mul_f32 v128, v128, v189\n" \
"v_mul_f32 v129, v129, v189\n" \
"v_mul_f32 v130, v130, v189\n" \
"v_mul_f32 v131, v131, v189\n" \
"v_mul_f32 v132, v132, v189\n" \
"v_mul_f32 v133, v133, v189\n" \
"v_mul_f32 v134, v134, v189\n" \
"v_mul_f32 v135, v135, v189\n" \
"v_mul_f32 v136, v136, v189\n" \
"v_mul_f32 v137, v137, v189\n" \
"v_mul_f32 v138, v138, v189\n" \
"v_mul_f32 v139, v139, v189\n" \
"v_mul_f32 v140, v140, v189\n" \
"v_mul_f32 v141, v141, v189\n" \
"v_mul_f32 v142, v142, v189\n" \
"v_mul_f32 v143, v143, v189\n" \
"v_mul_f32 v144, v144, v189\n" \
"v_mul_f32 v145, v145, v189\n" \
"v_mul_f32 v146, v146, v189\n" \
"v_mul_f32 v147, v147, v189\n" \
"v_mul_f32 v148, v148, v189\n" \
"v_mul_f32 v149, v149, v189\n" \
"v_mul_f32 v150, v150, v189\n" \
"v_mul_f32 v151, v151, v189\n" \
"v_mul_f32 v152, v152, v189\n" \
"v_mul_f32 v153, v153, v189\n" \
"v_mul_f32 v154, v154, v189\n" \
"v_mul_f32 v155, v155, v189\n" \
"v_mul_f32 v156, v156, v189\n" \
"v_mul_f32 v157, v157, v189\n" \
"v_mul_f32 v158, v158, v189\n" \
"v_mul_f32 v159, v159, v189\n" \
"s_nop 1\n" \
"v_mfma_f32_16x16x32_bf16 v[128:131], a[96:99], v[112:115], v[128:131]\n" \
"v_mfma_f32_16x16x32_bf16 v[132:135], a[100:103], v[112:115], v[132:135]\n" \
"v_mfma_f32_16x16x32_bf16 v[136:139], a[104:107], v[112:115], v[136:139]\n" \
"v_mfma_f32_16x16x32_bf16 v[140:143], a[108:111], v[112:115], v[140:143]\n" \
"v_mfma_f32_16x16x32_bf16 v[144:147], a[112:115], v[112:115], v[144:147]\n" \
"v_mfma_f32_16x16x32_bf16 v[148:151], a[116:119], v[112:115], v[148:151]\n" \
"v_mfma_f32_16x16x32_bf16 v[152:155], a[120:123], v[112:115], v[152:155]\n" \
"v_mfma_f32_16x16x32_bf16 v[156:159], a[124:127], v[112:115], v[156:159]\n" \
"s_nop 7\n" \
"s_nop 1\n" \
"s_and_b32 s70, s81, 1\n" \
"s_mul_i32 s70, s70, 1024\n" \
"s_add_u32 s82, s82, s70\n" \
"s_add_u32 s82, s82, 8192\n" \
"s_add_u32 s81, s81, 1\n" \
"s_cmp_le_u32 s81, s83\n" \
"s_cbranch_scc1 Lex2_%=\n" \
"v_mov_b32 v187, v185\n" \
"s_nop 1\n" \
"v_permlane16_swap_b32 v187, v185\n" \
"v_add_f32 v185, v185, v187\n" \
"v_mov_b32 v187, v185\n" \
"s_nop 1\n" \
"v_permlane32_swap_b32 v187, v185\n" \
"v_add_f32 v185, v185, v187\n" \
"v_rcp_f32 v185, v185\n" \
"s_nop 0\n" \
"v_mul_f32 v128, v128, v185\n" \
"v_mul_f32 v129, v129, v185\n" \
"v_mul_f32 v130, v130, v185\n" \
"v_mul_f32 v131, v131, v185\n" \
"v_mul_f32 v132, v132, v185\n" \
"v_mul_f32 v133, v133, v185\n" \
"v_mul_f32 v134, v134, v185\n" \
"v_mul_f32 v135, v135, v185\n" \
"v_mul_f32 v136, v136, v185\n" \
"v_mul_f32 v137, v137, v185\n" \
"v_mul_f32 v138, v138, v185\n" \
"v_mul_f32 v139, v139, v185\n" \
"v_mul_f32 v140, v140, v185\n" \
"v_mul_f32 v141, v141, v185\n" \
"v_mul_f32 v142, v142, v185\n" \
"v_mul_f32 v143, v143, v185\n" \
"v_mul_f32 v144, v144, v185\n" \
"v_mul_f32 v145, v145, v185\n" \
"v_mul_f32 v146, v146, v185\n" \
"v_mul_f32 v147, v147, v185\n" \
"v_mul_f32 v148, v148, v185\n" \
"v_mul_f32 v149, v149, v185\n" \
"v_mul_f32 v150, v150, v185\n" \
"v_mul_f32 v151, v151, v185\n" \
"v_mul_f32 v152, v152, v185\n" \
"v_mul_f32 v153, v153, v185\n" \
"v_mul_f32 v154, v154, v185\n" \
"v_mul_f32 v155, v155, v185\n" \
"v_mul_f32 v156, v156, v185\n" \
"v_mul_f32 v157, v157, v185\n" \
"v_mul_f32 v158, v158, v185\n" \
"v_mul_f32 v159, v159, v185\n" \
"s_add_u32 s70, s84, 4096\n" \
"v_cvt_pk_bf16_f32 v112, v128, v129\n" \
"v_cvt_pk_bf16_f32 v113, v130, v131\n" \
"v_cvt_pk_bf16_f32 v114, v132, v133\n" \
"v_cvt_pk_bf16_f32 v115, v134, v135\n" \
"buffer_store_dwordx4 v[112:115], v236, s[56:59], s84 offen offset:0 sc1\n" \
"v_cvt_pk_bf16_f32 v116, v136, v137\n" \
"v_cvt_pk_bf16_f32 v117, v138, v139\n" \
"v_cvt_pk_bf16_f32 v118, v140, v141\n" \
"v_cvt_pk_bf16_f32 v119, v142, v143\n" \
"buffer_store_dwordx4 v[116:119], v236, s[56:59], s84 offen offset:2048 sc1\n" \
"v_cvt_pk_bf16_f32 v120, v144, v145\n" \
"v_cvt_pk_bf16_f32 v121, v146, v147\n" \
"v_cvt_pk_bf16_f32 v122, v148, v149\n" \
"v_cvt_pk_bf16_f32 v123, v150, v151\n" \
"buffer_store_dwordx4 v[120:123], v236, s[56:59], s70 offen offset:0 sc1\n" \
"v_cvt_pk_bf16_f32 v124, v152, v153\n" \
"v_cvt_pk_bf16_f32 v125, v154, v155\n" \
"v_cvt_pk_bf16_f32 v126, v156, v157\n" \
"v_cvt_pk_bf16_f32 v127, v158, v159\n" \
"buffer_store_dwordx4 v[124:127], v236, s[56:59], s70 offen offset:2048 sc1\n" \
"s_waitcnt vmcnt(0)\n" \
"s_setpc_b64 s[64:65]\n" \
"Lpl4_%=:\n" \
"s_mov_b32 s72, 0\n" \
"Lpk4_%=:\n" \
"s_sleep 2\n" \
"buffer_load_dwordx2 v[226:227], v228, s[56:59], s13 offen sc1\n" \
"s_waitcnt vmcnt(0)\n" \
"v_cmp_eq_u32 vcc, s60, v226\n" \
"s_nop 1\n" \
"s_andn2_b32 s70, 0xff, vcc_lo\n" \
"s_cbranch_scc0 Lpr4_%=\n" \
"s_add_u32 s72, s72, 1\n" \
"s_cmp_lt_u32 s72, 0x40000\n" \
"s_cbranch_scc1 Lpk4_%=\n" \
"s_branch Lpr4_%=\n" \
"Lpl5_%=:\n" \
"s_mov_b32 s72, 0\n" \
"Lpk5_%=:\n" \
"s_sleep 2\n" \
"buffer_load_dword v220, v221, s[56:59], s66 offen sc1\n" \
"s_waitcnt vmcnt(0)\n" \
"v_cmp_eq_u32 vcc, s60, v220\n" \
"s_nop 1\n" \
"s_andn2_b32 s70, s65, vcc_lo\n" \
"s_cbranch_scc0 Lpr5_%=\n" \
"s_add_u32 s72, s72, 1\n" \
"s_cmp_lt_u32 s72, 0x40000\n" \
"s_cbranch_scc1 Lpk5_%=\n" \
"s_branch Lpr5_%=\n" \
"Lpl6_%=:\n" \
"s_mov_b32 s72, 0\n" \
"Lpk6_%=:\n" \
"s_sleep 2\n" \
"buffer_load_dword v220, v221, s[56:59], s67 offen sc1\n" \
"s_waitcnt vmcnt(0)\n" \
"v_cmp_eq_u32 vcc, s60, v220\n" \
"s_nop 1\n" \
"s_andn2_b32 s70, s74, vcc_lo\n" \
"s_cbranch_scc0 Lpr6_%=\n" \
"s_add_u32 s72, s72, 1\n" \
"s_cmp_lt_u32 s72, 0x40000\n" \
"s_cbranch_scc1 Lpk6_%=\n" \
"s_branch Lpr6_%=\n" \
"Lpl7_%=:\n" \
"s_mov_b32 s72, 0\n" \
"Lpk7_%=:\n" \
"s_sleep 2\n" \
"buffer_load_dword v220, v241, s[56:59], s64 offen sc1\n" \
"s_waitcnt vmcnt(0)\n" \
"v_cmp_eq_u32 vcc, s60, v220\n" \
"s_nop 1\n" \
"s_andn2_b32 s70, s69, vcc_lo\n" \
"s_cbranch_scc0 Lpr7_%=\n" \
"s_add_u32 s72, s72, 1\n" \
"s_cmp_lt_u32 s72, 0x40000\n" \
"s_cbranch_scc1 Lpk7_%=\n" \
"s_branch Lpr7_%=\n" \
"Lcode_end_%=:\n" \
".p2align 12\n" \
""
#define MK_ASM_CLOBBERS "v1", "v2", "v3", "v4", "v5", "v6", "v7", "v8", "v9", "v10", "v11", "v12", "v13", "v14", "v15", "v16", "v17", "v18", "v19", "v20", "v21", "v22", "v23", "v24", "v25", "v26", "v27", "v28", "v29", "v30", "v31", "v32", "v33", "v34", "v35", "v36", "v37", "v38", "v39", "v40", "v41", "v42", "v43", "v44", "v45", "v46", "v47", "v48", "v49", "v50", "v51", "v52", "v53", "v54", "v55", "v56", "v57", "v58", "v59", "v60", "v61", "v62", "v63", "v64", "v65", "v66", "v67", "v68", "v69", "v70", "v71", "v72", "v73", "v74", "v75", "v76", "v77", "v78", "v79", "v80", "v81", "v82", "v83", "v84", "v85", "v86", "v87", "v88", "v89", "v90", "v91", "v92", "v93", "v94", "v95", "v96", "v97", "v98", "v99", "v100", "v101", "v102", "v103", "v104", "v105", "v106", "v107", "v108", "v109", "v110", "v111", "v112", "v113", "v114", "v115", "v116", "v117", "v118", "v119", "v120", "v121", "v122", "v123", "v124", "v125", "v126", "v127", "v128", "v129", "v130", "v131", "v132", "v133", "v134", "v135", "v136", "v137", "v138", "v139", "v140", "v141", "v142", "v143", "v144", "v145", "v146", "v147", "v148", "v149", "v150", "v151", "v152", "v153", "v154", "v155", "v156", "v157", "v158", "v159", "v160", "v161", "v162", "v163", "v164", "v165", "v166", "v167", "v168", "v169", "v170", "v171", "v172", "v173", "v174", "v175", "v176", "v177", "v178", "v179", "v180", "v181", "v182", "v183", "v184", "v185", "v186", "v187", "v188", "v189", "v190", "v191", "v192", "v193", "v194", "v195", "v196", "v197", "v198", "v199", "v200", "v201", "v202", "v203", "v204", "v205", "v206", "v207", "v208", "v209", "v210", "v211", "v212", "v213", "v214", "v215", "v216", "v217", "v218", "v219", "v220", "v221", "v222", "v223", "v224", "v225", "v226", "v227", "v228", "v229", "v230", "v231", "v232", "v233", "v234", "v235", "v236", "v237", "v238", "v239", "v240", "v241", "v242", "v243", "v244", "v245", "v246", "v247", "v248", "v249", "v250", "v251", "v252", "v253", "v254", "v255", "a0", "a1", "a2", "a3", "a4", "a5", "a6", "a7", "a8", "a9", "a10", "a11", "a12", "a13", "a14", "a15", "a16", "a17", "a18", "a19", "a20", "a21", "a22", "a23", "a24", "a25", "a26", "a27", "a28", "a29", "a30", "a31", "a32", "a33", "a34", "a35", "a36", "a37", "a38", "a39", "a40", "a41", "a42", "a43", "a44", "a45", "a46", "a47", "a48", "a49", "a50", "a51", "a52", "a53", "a54", "a55", "a56", "a57", "a58", "a59", "a60", "a61", "a62", "a63", "a64", "a65", "a66", "a67", "a68", "a69", "a70", "a71", "a72", "a73", "a74", "a75", "a76", "a77", "a78", "a79", "a80", "a81", "a82", "a83", "a84", "a85", "a86", "a87", "a88", "a89", "a90", "a91", "a92", "a93", "a94", "a95", "a96", "a97", "a98", "a99", "a100", "a101", "a102", "a103", "a104", "a105", "a106", "a107", "a108", "a109", "a110", "a111", "a112", "a113", "a114", "a115", "a116", "a117", "a118", "a119", "a120", "a121", "a122", "a123", "a124", "a125", "a126", "a127", "a128", "a129", "a130", "a131", "a132", "a133", "a134", "a135", "a136", "a137", "a138", "a139", "a140", "a141", "a142", "a143", "a144", "a145", "a146", "a147", "a148", "a149", "a150", "a151", "a152", "a153", "a154", "a155", "a156", "a157", "a158", "a159", "a160", "a161", "a162", "a163", "a164", "a165", "a166", "a167", "a168", "a169", "a170", "a171", "a172", "a173", "a174", "a175", "a176", "a177", "a178", "a179", "a180", "a181", "a182", "a183", "a184", "a185", "a186", "a187", "a188", "a189", "a190", "a191", "a192", "a193", "a194", "a195", "a196", "a197", "a198", "a199", "a200", "a201", "a202", "a203", "a204", "a205", "a206", "a207", "a208", "a209", "a210", "a211", "a212", "a213", "a214", "a215", "a216", "a217", "a218", "a219", "a220", "a221", "a222", "a223", "a224", "a225", "a226", "a227", "a228", "a229", "a230", "a231", "a232", "a233", "a234", "a235", "a236", "a237", "a238", "a239", "a240", "a241", "a242", "a243", "a244", "a245", "a246", "a247", "a248", "a249", "a250", "a251", "a252", "a253", "a254", "a255", "s8", "s9", "s10", "s11", "s12", "s13", "s14", "s15", "s16", "s17", "s18", "s19", "s20", "s21", "s22", "s23", "s24", "s25", "s26", "s27", "s28", "s29", "s30", "s31", "s36", "s37", "s38", "s39", "s40", "s41", "s42", "s43", "s44", "s45", "s46", "s47", "s48", "s49", "s50", "s51", "s52", "s53", "s54", "s55", "s56", "s57", "s58", "s59", "s60", "s61", "s62", "s63", "s64", "s65", "s66", "s67", "s68", "s69", "s70", "s71", "s72", "s73", "s74", "s75", "s76", "s77", "s78", "s79", "s80", "s81", "s82", "s83", "s84", "s85", "s86", "s87", "s88", "s89", "s90", "s91", "s92", "s93", "s94", "s95", "vcc", "scc", "memory"
namespace mk {
constexpr int LDS_BYTES = 163840;
struct Args { const void* in[9]; void* out; void* ws; };
__global__ __launch_bounds__(256, 1) void mega_k(const Args a) {
    extern __shared__ __attribute__((aligned(16))) unsigned char shm[];
    const unsigned ldsb = (unsigned)(size_t)(LAS unsigned char*)shm;
    asm volatile(MK_ASM_BODY
                 :
                 : "s"(__builtin_amdgcn_kernarg_segment_ptr()), "s"(blockIdx.x), "v"(threadIdx.x), "s"(ldsb)
                 : MK_ASM_CLOBBERS);
}
}
extern "C" void kernel_launch(void* const* d_in, const int* in_sizes, int n_in, void* d_out, int out_size, void* d_ws, size_t ws_size, hipStream_t stream) {
    (void)hipFuncSetAttribute((const void*)mk::mega_k, hipFuncAttributeMaxDynamicSharedMemorySize, mk::LDS_BYTES);
    mk::Args a;
    for (int i = 0; i < 9; ++i) a.in[i] = d_in[i];
    a.out = d_out; a.ws = (char*)d_ws + ((ws_size - WS_END) & ~(size_t)0x1fffff);
    mk::mega_k<<<256, 256, mk::LDS_BYTES, stream>>>(a);
}
```

```cpp
#include <hip/hip_runtime.h>
#include <stdint.h>
#define LAS __attribute__((address_space(3)))
constexpr size_t LAY_TS = 17408, LAY_HS = 557824, LAY_CRS = 266240, LAY_ORS = 69632, WS_K = 0, WS_V = 17850368, WS_CTX = 35700736, WS_OB = 52740096, WS_FL = 65585152, WS_END = 65634304, LAY_VS = 2;
#define MK_ASM_BODY \
"s_load_dwordx16 s[8:23], %0, 0x0\n" \
"s_load_dwordx4 s[24:27], %0, 0x40\n" \
"s_load_dwordx2 s[28:29], %0, 0x50\n" \
"v_readfirstlane_b32 s61, %2\n" \
"s_lshr_b32 s61, s61, 6\n" \
"v_mbcnt_lo_u32_b32 v78, -1, 0\n" \
"v_mbcnt_hi_u32_b32 v78, -1, v78\n" \
"v_lshlrev_b32 v234, 4, v78\n" \
"s_getpc_b64 s[64:65]\n" \
"Lpc_%=:\n" \
"s_lshr_b32 s70, %1, 3\n" \
"s_lshl_b32 s70, s70, 2\n" \
"s_add_u32 s70, s70, s61\n" \
"s_and_b32 s71, s70, 127\n" \
"s_lshl_b32 s71, s71, 10\n" \
"v_lshl_add_u32 v237, v234, 0, s71\n" \
"s_mov_b32 s71, Lcode_end_%=-Lpc_%=-4\n" \
"v_min_u32 v237, s71, v237\n" \
"global_load_dword v238, v237, s[64:65]\n" \
"s_and_b32 s62, %1, 7\n" \
"s_lshl_b32 s62, s62, 2\n" \
"s_lshr_b32 s70, %1, 6\n" \
"s_add_u32 s62, s62, s70\n" \
"s_bfe_u32 s63, %1, 0x30003\n" \
"s_lshl_b32 s74, s61, 13\n" \
"s_add_u32 s74, s74, %3\n" \
"v_bfe_u32 v79, v78, 4, 1\n" \
"v_bfe_u32 v80, v78, 2, 2\n" \
"v_lshl_add_u32 v80, v79, 3, v80\n" \
"s_lshl_b32 s71, s61, 4\n" \
"v_add_u32 v80, s71, v80\n" \
"v_lshlrev_b32 v80, 13, v80\n" \
"v_lshlrev_b32 v79, 1, v79\n" \
"v_and_b32 v81, 3, v78\n" \
"v_xor_b32 v79, v79, v81\n" \
"v_lshl_add_u32 v80, v79, 4, v80\n" \
"v_lshrrev_b32 v90, 5, v78\n" \
"v_lshl_add_u32 v84, v90, 6, v80\n" \
"v_add_u32 v68, 4096, v84\n" \
"v_add_u32 v69, 3072, v84\n" \
"v_add_u32 v70, 2176, v84\n" \
"v_add_u32 v71, 1152, v84\n" \
"s_lshl_b32 s83, s62, 8\n" \
"s_add_u32 s84, s83, 32768\n" \
"v_lshrrev_b32 v85, 3, v78\n" \
"v_and_b32 v86, 7, v78\n" \
"v_xor_b32 v86, v86, v85\n" \
"v_lshlrev_b32 v86, 4, v86\n" \
"v_lshl_add_u32 v64, v85, 10, v86\n" \
"v_add_u32 v65, 7168, v64\n" \
"v_add_u32 v66, 14336, v64\n" \
"v_add_u32 v67, 21504, v64\n" \
"s_bitcmp1_b32 s61, 0\n" \
"s_cselect_b32 s70, 0, 15\n" \
"s_xor_b32 s81, s63, s70\n" \
"s_lshl_b32 s81, s81, 17\n" \
"s_lshr_b32 s70, s61, 1\n" \
"s_lshl_b32 s71, s70, 16\n" \
"s_add_u32 s81, s81, s71\n" \
"s_add_u32 s82, s81, 32768\n" \
"s_mov_b32 s42, 4198400\n" \
"s_mov_b32 s43, 0x20000\n" \
"s_mov_b32 s46, 4198400\n" \
"s_mov_b32 s47, 0x20000\n" \
"s_mov_b32 s38, 2097152\n" \
"s_mov_b32 s39, 0x20000\n" \
"s_lshr_b32 s69, s61, 1\n" \
"s_xor_b32 s69, s69, s63\n" \
"s_xor_b32 s69, s69, s62\n" \
"s_and_b32 s69, s69, 1\n" \
"s_cbranch_scc1 Lech_%=\n" \
"Lecr_%=:\n" \
"s_waitcnt lgkmcnt(0)\n" \
"s_mov_b64 s[40:41], s[14:15]\n" \
"s_sub_u32 s40, s40, 4096\n" \
"s_subb_u32 s41, s41, 0\n" \
"s_and_b32 s41, s41, 0xffff\n" \
"s_mov_b64 s[44:45], s[16:17]\n" \
"s_sub_u32 s44, s44, 4096\n" \
"s_subb_u32 s45, s45, 0\n" \
"s_and_b32 s45, s45, 0xffff\n" \
"s_mov_b64 s[36:37], s[10:11]\n" \
"s_and_b32 s37, s37, 0xffff\n" \
"s_add_u32 m0, s74, 32768\n" \
"s_nop 0\n" \
"buffer_load_dwordx4 v68, s[40:43], s83 offen offset:0 lds\n" \
"buffer_load_dwordx4 v70, s[40:43], s83 offen offset:2048 lds\n" \
"s_add_u32 m0, s74, 36864\n" \
"s_nop 0\n" \
"buffer_load_dwordx4 v68, s[40:43], s84 offen offset:0 lds\n" \
"buffer_load_dwordx4 v70, s[40:43], s84 offen offset:2048 lds\n" \
"s_add_u32 m0, s74, 0\n" \
"s_nop 0\n" \
"buffer_load_dwordx4 v64, s[36:39], s81 offen offset:0 lds\n" \
"buffer_load_dwordx4 v65, s[36:39], s81 offen offset:1024 lds\n" \
"buffer_load_dwordx4 v66, s[36:39], s81 offen offset:2048 lds\n" \
"buffer_load_dwordx4 v67, s[36:39], s81 offen offset:3072 lds\n" \
"s_xor_b32 s81, s81, 0x80\n" \
"s_add_u32 m0, s74, 4096\n" \
"s_nop 0\n" \
"buffer_load_dwordx4 v64, s[36:39], s82 offen offset:0 lds\n" \
"buffer_load_dwordx4 v65, s[36:39], s82 offen offset:1024 lds\n" \
"buffer_load_dwordx4 v66, s[36:39], s82 offen offset:2048 lds\n" \
"buffer_load_dwordx4 v67, s[36:39], s82 offen offset:3072 lds\n" \
"s_xor_b32 s82, s82, 0x80\n" \
"s_add_u32 m0, s74, 32768\n" \
"s_nop 0\n" \
"buffer_load_dwordx4 v69, s[44:47], s83 offen offset:1024 lds\n" \
"buffer_load_dwordx4 v71, s[44:47], s83 offen offset:3072 lds\n" \
"s_xor_b32 s83, s83, 0x80000\n" \
"s_add_u32 m0, s74, 36864\n" \
"s_nop 0\n" \
"buffer_load_dwordx4 v69, s[44:47], s84 offen offset:1024 lds\n" \
"buffer_load_dwordx4 v71, s[44:47], s84 offen offset:3072 lds\n" \
"s_xor_b32 s84, s84, 0x80000\n" \
"v_lshrrev_b32 v87, 4, v78\n" \
"v_and_b32 v79, 15, v78\n" \
"v_and_b32 v80, 7, v78\n" \
"v_xor_b32 v80, v80, v87\n" \
"v_lshlrev_b32 v79, 7, v79\n" \
"s_lshl_b32 s70, s70, 14\n" \
"s_add_u32 s70, s70, %3\n" \
"v_lshl_add_u32 v88, v80, 4, v79\n" \
"v_add_u32 v72, s70, v88\n" \
"v_xor_b32 v73, 64, v72\n" \
"v_lshlrev_b32 v79, 3, v81\n" \
"v_bfe_u32 v80, v78, 2, 2\n" \
"v_lshl_add_u32 v79, v80, 6, v79\n" \
"v_bfe_u32 v80, v78, 4, 1\n" \
"v_lshl_add_u32 v79, v80, 8, v79\n" \
"v_lshl_add_u32 v89, v80, 5, v79\n" \
"s_and_b32 s70, s61, 1\n" \
"s_lshl_b32 s70, s70, 11\n" \
"s_add_u32 s70, s70, %3\n" \
"v_lshl_add_u32 v82, v90, 13, v89\n" \
"v_add_u32 v74, s70, v82\n" \
"v_xor_b32 v75, 32, v74\n" \
"s_mov_b64 s[56:57], s[28:29]\n" \
"s_and_b32 s57, s57, 0xffff\n" \
"s_mov_b32 s58, 65634304\n" \
"s_mov_b32 s59, 0x20000\n" \
"v_mov_b32 v241, 0\n" \
"v_lshl_add_u32 v110, v90, 9, v234\n" \
"s_lshl_b32 s66, %1, 2\n" \
"s_add_u32 s66, s66, s61\n" \
"s_lshl_b32 s66, s66, 10\n" \
"s_lshr_b32 s67, %1, 3\n" \
"s_lshl_b32 s67, s67, 2\n" \
"s_add_u32 s67, s67, s61\n" \
"s_lshl_b32 s67, s67, 13\n" \
"s_and_b32 s64, %1, 7\n" \
"s_lshl_b32 s64, s64, 10\n" \
"s_add_u32 s67, s67, s64\n" \
"s_cmp_eq_u32 s69, 0\n" \
"s_cbranch_scc0 Lecs_%=\n" \
"Lech_%=:\n" \
"s_mov_b64 exec, 0\n" \
"s_cmp_eq_u32 s61, 0\n" \
"s_cbranch_scc1 Lst0x0_%=\n" \
"s_cmp_eq_u32 s61, 1\n" \
"s_cbranch_scc1 Lst0x1_%=\n" \
"s_cmp_eq_u32 s61, 2\n" \
"s_cbranch_scc1 Lst0x2_%=\n" \
"s_branch Lst0x3_%=\n" \
"Lcb0_%=:\n" \
"s_mov_b64 exec, -1\n" \
"s_lshl_b32 s69, s69, 1\n" \
"s_cbranch_scc1 Lecr_%=\n" \
"Lecs_%=:\n" \
"s_add_u32 m0, s74, 131072\n" \
"s_nop 0\n" \
"buffer_load_dwordx4 v68, s[40:43], s83 offen offset:0 lds\n" \
"buffer_load_dwordx4 v69, s[44:47], s83 offen offset:1024 lds\n" \
"buffer_load_dwordx4 v70, s[40:43], s83 offen offset:2048 lds\n" \
"buffer_load_dwordx4 v71, s[44:47], s83 offen offset:3072 lds\n" \
"s_xor_b32 s83, s83, 0x180000\n" \
"s_add_u32 m0, s74, 135168\n" \
"s_nop 0\n" \
"buffer_load_dwordx4 v68, s[40:43], s84 offen offset:0 lds\n" \
"buffer_load_dwordx4 v69, s[44:47], s84 offen offset:1024 lds\n" \
"buffer_load_dwordx4 v70, s[40:43], s84 offen offset:2048 lds\n" \
"buffer_load_dwordx4 v71, s[44:47], s84 offen offset:3072 lds\n" \
"s_xor_b32 s84, s84, 0x180000\n" \
"s_waitcnt vmcnt(12)\n" \
"s_barrier\n" \
"ds_read_b64_tr_b16 a[64:65], v74 offset:32768\n" \
"ds_read_b64_tr_b16 a[66:67], v74 offset:36864\n" \
"ds_read_b64_tr_b16 a[68:69], v74 offset:49152\n" \
"ds_read_b64_tr_b16 a[70:71], v74 offset:53248\n" \
"ds_read_b128 a[0:3], v72 offset:0\n" \
"ds_read_b128 a[4:7], v73 offset:0\n" \
"ds_read_b64_tr_b16 a[72:73], v75 offset:32768\n" \
"ds_read_b64_tr_b16 a[74:75], v75 offset:36864\n" \
"ds_read_b64_tr_b16 a[76:77], v75 offset:49152\n" \
"ds_read_b64_tr_b16 a[78:79], v75 offset:53248\n" \
"ds_read_b64_tr_b16 a[80:81], v74 offset:33280\n" \
"ds_read_b64_tr_b16 a[82:83], v74 offset:37376\n" \
"ds_read_b64_tr_b16 a[84:85], v74 offset:49664\n" \
"ds_read_b64_tr_b16 a[86:87], v74 offset:53760\n" \
"ds_read_b64_tr_b16 a[88:89], v75 offset:33280\n" \
"ds_read_b64_tr_b16 a[90:91], v75 offset:37376\n" \
"ds_read_b64_tr_b16 a[92:93], v75 offset:49664\n" \
"ds_read_b64_tr_b16 a[94:95], v75 offset:53760\n" \
"ds_read_b128 a[8:11], v72 offset:2048\n" \
"ds_read_b128 a[12:15], v73 offset:2048\n" \
"s_waitcnt lgkmcnt(14)\n" \
"v_mfma_f32_16x16x32_bf16 v[0:3], a[64:67], a[0:3], 0\n" \
"s_mov_b32 s93, 65593344\n" \
"ds_read_b128 a[16:19], v72 offset:4096\n" \
"v_mfma_f32_16x16x32_bf16 v[0:3], a[68:71], a[4:7], v[0:3]\n" \
"s_waitcnt lgkmcnt(11)\n" \
"buffer_load_dword v240, v241, s[56:59], s93 offen sc1\n" \
"v_mfma_f32_16x16x32_bf16 v[4:7], a[72:75], a[0:3], 0\n" \
"s_mov_b64 s[64:65], s[10:11]\n" \
"s_lshl1_add_u32 s68, s66, 0\n" \
"s_add_u32 s64, s64, s68\n" \
"s_addc_u32 s65, s65, 0\n" \
"ds_read_b128 a[20:23], v73 offset:4096\n" \
"v_mfma_f32_16x16x32_bf16 v[4:7], a[76:79], a[4:7], v[4:7]\n" \
"s_waitcnt lgkmcnt(8)\n" \
"global_load_dword v238, v110, s[64:65]\n" \
"v_mfma_f32_16x16x32_bf16 v[8:11], a[80:83], a[0:3], 0\n" \
"s_mov_b64 s[64:65], s[14:15]\n" \
"s_add_u32 s68, s67, 0x100000\n" \
"s_add_u32 s64, s64, s68\n" \
"s_addc_u32 s65, s65, 0\n" \
"ds_read_b128 a[24:27], v72 offset:6144\n" \
"v_mfma_f32_16x16x32_bf16 v[8:11], a[84:87], a[4:7], v[8:11]\n" \
"s_waitcnt lgkmcnt(2)\n" \
"global_load_dword v238, v234, s[64:65]\n" \
"v_mfma_f32_16x16x32_bf16 v[12:15], a[88:91], a[0:3], 0\n" \
"s_mov_b64 s[64:65], s[16:17]\n" \
"s_add_u32 s68, s67, 0x100000\n" \
"s_add_u32 s64, s64, s68\n" \
"s_addc_u32 s65, s65, 0\n" \
"ds_read_b128 a[28:31], v73 offset:6144\n" \
"v_mfma_f32_16x16x32_bf16 v[12:15], a[92:95], a[4:7], v[12:15]\n" \
"global_load_dword v238, v234, s[64:65]\n" \
"v_mfma_f32_16x16x32_bf16 v[28:31], a[88:91], a[8:11], 0\n" \
"s_add_u32 m0, s74, 65536\n" \
"ds_read_b128 a[32:35], v72 offset:8192\n" \
"v_mfma_f32_16x16x32_bf16 v[28:31], a[92:95], a[12:15], v[28:31]\n" \
"buffer_load_dwordx4 v64, s[36:39], s81 offen offset:0 lds\n" \
"v_mfma_f32_16x16x32_bf16 v[24:27], a[80:83], a[8:11], 0\n" \
"ds_read_b128 a[36:39], v73 offset:8192\n" \
"v_mfma_f32_16x16x32_bf16 v[24:27], a[84:87], a[12:15], v[24:27]\n" \
"v_mfma_f32_16x16x32_bf16 v[20:23], a[72:75], a[8:11], 0\n" \
"ds_read_b128 a[40:43], v72 offset:10240\n" \
"v_mfma_f32_16x16x32_bf16 v[20:23], a[76:79], a[12:15], v[20:23]\n" \
"buffer_load_dwordx4 v65, s[36:39], s81 offen offset:1024 lds\n" \
"v_mfma_f32_16x16x32_bf16 v[16:19], a[64:67], a[8:11], 0\n" \
"ds_read_b128 a[44:47], v73 offset:10240\n" \
"v_mfma_f32_16x16x32_bf16 v[16:19], a[68:71], a[12:15], v[16:19]\n" \
"s_waitcnt lgkmcnt(2)\n" \
"v_mfma_f32_16x16x32_bf16 v[32:35], a[64:67], a[16:19], 0\n" \
"ds_read_b128 a[48:51], v72 offset:12288\n" \
"v_mfma_f32_16x16x32_bf16 v[32:35], a[68:71], a[20:23], v[32:35]\n" \
"buffer_load_dwordx4 v66, s[36:39], s81 offen offset:2048 lds\n" \
"v_mfma_f32_16x16x32_bf16 v[36:39], a[72:75], a[16:19], 0\n" \
"ds_read_b128 a[52:55], v73 offset:12288\n" \
"v_mfma_f32_16x16x32_bf16 v[36:39], a[76:79], a[20:23], v[36:39]\n" \
"v_mfma_f32_16x16x32_bf16 v[40:43], a[80:83], a[16:19], 0\n" \
"ds_read_b128 a[56:59], v72 offset:14336\n" \
"v_mfma_f32_16x16x32_bf16 v[40:43], a[84:87], a[20:23], v[40:43]\n" \
"buffer_load_dwordx4 v67, s[36:39], s81 offen offset:3072 lds\n" \
"v_mfma_f32_16x16x32_bf16 v[44:47], a[88:91], a[16:19], 0\n" \
"ds_read_b128 a[60:63], v73 offset:14336\n" \
"v_mfma_f32_16x16x32_bf16 v[44:47], a[92:95], a[20:23], v[44:47]\n" \
"s_xor_b32 s81, s81, 0x180\n" \
"v_mfma_f32_16x16x32_bf16 v[60:63], a[88:91], a[24:27], 0\n" \
"s_add_u32 m0, s74, 69632\n" \
"v_mfma_f32_16x16x32_bf16 v[60:63], a[92:95], a[28:31], v[60:63]\n" \
"buffer_load_dwordx4 v64, s[36:39], s82 offen offset:0 lds\n" \
"v_mfma_f32_16x16x32_bf16 v[56:59], a[80:83], a[24:27], 0\n" \
"v_mfma_f32_16x16x32_bf16 v[56:59], a[84:87], a[28:31], v[56:59]\n" \
"v_mfma_f32_16x16x32_bf16 v[52:55], a[72:75], a[24:27], 0\n" \
"v_mfma_f32_16x16x32_bf16 v[52:55], a[76:79], a[28:31], v[52:55]\n" \
"buffer_load_dwordx4 v65, s[36:39], s82 offen offset:1024 lds\n" \
"v_mfma_f32_16x16x32_bf16 v[48:51], a[64:67], a[24:27], 0\n" \
"v_mfma_f32_16x16x32_bf16 v[48:51], a[68:71], a[28:31], v[48:51]\n" \
"s_waitcnt vmcnt(18) lgkmcnt(0)\n" \
"s_barrier\n" \
"v_mfma_f32_16x16x32_bf16 v[144:147], a[64:67], a[32:35], 0\n" \
"v_mfma_f32_16x16x32_bf16 v[144:147], a[68:71], a[36:39], v[144:147]\n" \
"ds_read_b64_tr_b16 a[96:97], v74 offset:33792\n" \
"ds_read_b64_tr_b16 a[98:99], v74 offset:37888\n" \
"buffer_load_dwordx4 v66, s[36:39], s82 offen offset:2048 lds\n" \
"v_mfma_f32_16x16x32_bf16 v[148:151], a[72:75], a[32:35], 0\n" \
"v_mfma_f32_16x16x32_bf16 v[148:151], a[76:79], a[36:39], v[148:151]\n" \
"ds_read_b64_tr_b16 a[100:101], v74 offset:50176\n" \
"ds_read_b64_tr_b16 a[102:103], v74 offset:54272\n" \
"v_mfma_f32_16x16x32_bf16 v[152:155], a[80:83], a[32:35], 0\n" \
"v_mfma_f32_16x16x32_bf16 v[152:155], a[84:87], a[36:39], v[152:155]\n" \
"ds_read_b64_tr_b16 a[104:105], v75 offset:33792\n" \
"ds_read_b64_tr_b16 a[106:107], v75 offset:37888\n" \
"buffer_load_dwordx4 v67, s[36:39], s82 offen offset:3072 lds\n" \
"v_mfma_f32_16x16x32_bf16 v[156:159], a[88:91], a[32:35], 0\n" \
"v_mfma_f32_16x16x32_bf16 v[156:159], a[92:95], a[36:39], v[156:159]\n" \
"ds_read_b64_tr_b16 a[108:109], v75 offset:50176\n" \
"ds_read_b64_tr_b16 a[110:111], v75 offset:54272\n" \
"v_mfma_f32_16x16x32_bf16 v[188:191], a[88:91], a[40:43], 0\n" \
"s_xor_b32 s82, s82, 0x180\n" \
"v_add_u32 v96, 0x10000, v72\n" \
"v_mfma_f32_16x16x32_bf16 v[188:191], a[92:95], a[44:47], v[188:191]\n" \
"ds_read_b64_tr_b16 a[112:113], v74 offset:34304\n" \
"ds_read_b64_tr_b16 a[114:115], v74 offset:38400\n" \
"v_mfma_f32_16x16x32_bf16 v[184:187], a[80:83], a[40:43], 0\n" \
"v_add_u32 v97, 0x10000, v73\n" \
"v_mfma_f32_16x16x32_bf16 v[184:187], a[84:87], a[44:47], v[184:187]\n" \
"ds_read_b64_tr_b16 a[116:117], v74 offset:50688\n" \
"ds_read_b64_tr_b16 a[118:119], v74 offset:54784\n" \
"v_mfma_f32_16x16x32_bf16 v[180:183], a[72:75], a[40:43], 0\n" \
"v_add_u32 v98, 0x10000, v74\n" \
"v_mfma_f32_16x16x32_bf16 v[180:183], a[76:79], a[44:47], v[180:183]\n" \
"ds_read_b64_tr_b16 a[120:121], v75 offset:34304\n" \
"ds_read_b64_tr_b16 a[122:123], v75 offset:38400\n" \
"v_mfma_f32_16x16x32_bf16 v[176:179], a[64:67], a[40:43], 0\n" \
"v_add_u32 v99, 0x10000, v75\n" \
"v_mfma_f32_16x16x32_bf16 v[176:179], a[68:71], a[44:47], v[176:179]\n" \
"ds_read_b64_tr_b16 a[124:125], v75 offset:50688\n" \
"ds_read_b64_tr_b16 a[126:127], v75 offset:54784\n" \
"v_mfma_f32_16x16x32_bf16 v[192:195], a[64:67], a[48:51], 0\n" \
"v_add_u32 v100, 0x20000, v72\n" \
"v_mfma_f32_16x16x32_bf16 v[192:195], a[68:71], a[52:55], v[192:195]\n" \
"v_mfma_f32_16x16x32_bf16 v[196:199], a[72:75], a[48:51], 0\n" \
"v_add_u32 v101, 0x20000, v73\n" \
"v_mfma_f32_16x16x32_bf16 v[196:199], a[76:79], a[52:55], v[196:199]\n" \
"v_mfma_f32_16x16x32_bf16 v[200:203], a[80:83], a[48:51], 0\n" \
"s_add_u32 m0, s74, 98304\n" \
"v_add_u32 v102, 0x20000, v74\n" \
"v_mfma_f32_16x16x32_bf16 v[200:203], a[84:87], a[52:55], v[200:203]\n" \
"buffer_load_dwordx4 v68, s[40:43], s83 offen offset:0 lds\n" \
"v_mfma_f32_16x16x32_bf16 v[204:207], a[88:91], a[48:51], 0\n" \
"v_add_u32 v103, 0x20000, v75\n" \
"v_mfma_f32_16x16x32_bf16 v[204:207], a[92:95], a[52:55], v[204:207]\n" \
"v_mfma_f32_16x16x32_bf16 v[220:223], a[88:91], a[56:59], 0\n" \
"v_mfma_f32_16x16x32_bf16 v[220:223], a[92:95], a[60:63], v[220:223]\n" \
"buffer_load_dwordx4 v69, s[44:47], s83 offen offset:1024 lds\n" \
"v_mfma_f32_16x16x32_bf16 v[216:219], a[80:83], a[56:59], 0\n" \
"v_mfma_f32_16x16x32_bf16 v[216:219], a[84:87], a[60:63], v[216:219]\n" \
"v_mfma_f32_16x16x32_bf16 v[212:215], a[72:75], a[56:59], 0\n" \
"v_mfma_f32_16x16x32_bf16 v[212:215], a[76:79], a[60:63], v[212:215]\n" \
"buffer_load_dwordx4 v70, s[40:43], s83 offen offset:2048 lds\n" \
"v_mfma_f32_16x16x32_bf16 v[208:211], a[64:67], a[56:59], 0\n" \
"v_mfma_f32_16x16x32_bf16 v[208:211], a[68:71], a[60:63], v[208:211]\n" \
"s_waitcnt lgkmcnt(0)\n" \
"s_barrier\n" \
"v_mfma_f32_16x16x32_bf16 a[128:131], a[0:3], a[96:99], 0\n" \
"v_mfma_f32_16x16x32_bf16 a[128:131], a[4:7], a[100:103], a[128:131]\n" \
"buffer_load_dwordx4 v71, s[44:47], s83 offen offset:3072 lds\n" \
"v_mfma_f32_16x16x32_bf16 a[132:135], a[0:3], a[104:107], 0\n" \
"v_mfma_f32_16x16x32_bf16 a[132:135], a[4:7], a[108:111], a[132:135]\n" \
"s_xor_b32 s83, s83, 0x80000\n" \
"v_mfma_f32_16x16x32_bf16 a[136:139], a[0:3], a[112:115], 0\n" \
"s_add_u32 m0, s74, 102400\n" \
"v_mfma_f32_16x16x32_bf16 a[136:139], a[4:7], a[116:119], a[136:139]\n" \
"buffer_load_dwordx4 v68, s[40:43], s84 offen offset:0 lds\n" \
"v_mfma_f32_16x16x32_bf16 a[140:143], a[0:3], a[120:123], 0\n" \
"v_mfma_f32_16x16x32_bf16 a[140:143], a[4:7], a[124:127], a[140:143]\n" \
"v_mfma_f32_16x16x32_bf16 a[156:159], a[8:11], a[120:123], 0\n" \
"v_mfma_f32_16x16x32_bf16 a[156:159], a[12:15], a[124:127], a[156:159]\n" \
"buffer_load_dwordx4 v69, s[44:47], s84 offen offset:1024 lds\n" \
"v_mfma_f32_16x16x32_bf16 a[152:155], a[8:11], a[112:115], 0\n" \
"v_mfma_f32_16x16x32_bf16 a[152:155], a[12:15], a[116:119], a[152:155]\n" \
"v_mfma_f32_16x16x32_bf16 a[148:151], a[8:11], a[104:107], 0\n" \
"v_mfma_f32_16x16x32_bf16 a[148:151], a[12:15], a[108:111], a[148:151]\n" \
"buffer_load_dwordx4 v70, s[40:43], s84 offen offset:2048 lds\n" \
"v_mfma_f32_16x16x32_bf16 a[144:147], a[8:11], a[96:99], 0\n" \
"v_mfma_f32_16x16x32_bf16 a[144:147], a[12:15], a[100:103], a[144:147]\n" \
"v_mfma_f32_16x16x32_bf16 a[160:163], a[16:19], a[96:99], 0\n" \
"v_mfma_f32_16x16x32_bf16 a[160:163], a[20:23], a[100:103], a[160:163]\n" \
"buffer_load_dwordx4 v71, s[44:47], s84 offen offset:3072 lds\n" \
"v_mfma_f32_16x16x32_bf16 a[164:167], a[16:19], a[104:107], 0\n" \
"v_mfma_f32_16x16x32_bf16 a[164:167], a[20:23], a[108:111], a[164:167]\n" \
"v_mfma_f32_16x16x32_bf16 a[168:171], a[16:19], a[112:115], 0\n" \
"s_xor_b32 s84, s84, 0x80000\n" \
"s_add_u32 m0, s74, 32768\n" \
"v_mfma_f32_16x16x32_bf16 a[168:171], a[20:23], a[116:119], a[168:171]\n" \
"buffer_load_dwordx4 v64, s[36:39], s81 offen offset:0 lds\n" \
"v_mfma_f32_16x16x32_bf16 a[172:175], a[16:19], a[120:123], 0\n" \
"v_mfma_f32_16x16x32_bf16 a[172:175], a[20:23], a[124:127], a[172:175]\n" \
"v_mfma_f32_16x16x32_bf16 a[188:191], a[24:27], a[120:123], 0\n" \
"v_mfma_f32_16x16x32_bf16 a[188:191], a[28:31], a[124:127], a[188:191]\n" \
"buffer_load_dwordx4 v65, s[36:39], s81 offen offset:1024 lds\n" \
"v_mfma_f32_16x16x32_bf16 a[184:187], a[24:27], a[112:115], 0\n" \
"v_mfma_f32_16x16x32_bf16 a[184:187], a[28:31], a[116:119], a[184:187]\n" \
"v_mfma_f32_16x16x32_bf16 a[180:183], a[24:27], a[104:107], 0\n" \
"v_mfma_f32_16x16x32_bf16 a[180:183], a[28:31], a[108:111], a[180:183]\n" \
"buffer_load_dwordx4 v66, s[36:39], s81 offen offset:2048 lds\n" \
"v_mfma_f32_16x16x32_bf16 a[176:179], a[24:27], a[96:99], 0\n" \
"v_mfma_f32_16x16x32_bf16 a[176:179], a[28:31], a[100:103], a[176:179]\n" \
"v_mfma_f32_16x16x32_bf16 a[192:195], a[32:35], a[96:99], 0\n" \
"v_mfma_f32_16x16x32_bf16 a[192:195], a[36:39], a[100:103], a[192:195]\n" \
"buffer_load_dwordx4 v67, s[36:39], s81 offen offset:3072 lds\n" \
"v_mfma_f32_16x16x32_bf16 a[196:199], a[32:35], a[104:107], 0\n" \
"v_mfma_f32_16x16x32_bf16 a[196:199], a[36:39], a[108:111], a[196:199]\n" \
"s_xor_b32 s81, s81, 0x80\n" \
"v_mfma_f32_16x16x32_bf16 a[200:203], a[32:35], a[112:115], 0\n" \
"s_add_u32 m0, s74, 36864\n" \
"v_mfma_f32_16x16x32_bf16 a[200:203], a[36:39], a[116:119], a[200:203]\n" \
"buffer_load_dwordx4 v64, s[36:39], s82 offen offset:0 lds\n" \
"v_mfma_f32_16x16x32_bf16 a[204:207], a[32:35], a[120:123], 0\n" \
"v_mfma_f32_16x16x32_bf16 a[204:207], a[36:39], a[124:127], a[204:207]\n" \
"s_waitcnt vmcnt(13) lgkmcnt(0)\n" \
"s_barrier\n" \
"v_mfma_f32_16x16x32_bf16 a[220:223], a[40:43], a[120:123], 0\n" \
"ds_read_b64_tr_b16 a[64:65], v102 offset:0\n" \
"ds_read_b64_tr_b16 a[66:67], v102 offset:4096\n" \
"v_mfma_f32_16x16x32_bf16 a[220:223], a[44:47], a[124:127], a[220:223]\n" \
"buffer_load_dwordx4 v65, s[36:39], s82 offen offset:1024 lds\n" \
"v_mfma_f32_16x16x32_bf16 a[216:219], a[40:43], a[112:115], 0\n" \
"ds_read_b64_tr_b16 a[68:69], v102 offset:16384\n" \
"ds_read_b64_tr_b16 a[70:71], v102 offset:20480\n" \
"v_mfma_f32_16x16x32_bf16 a[216:219], a[44:47], a[116:119], a[216:219]\n" \
"ds_read_b128 a[0:3], v96 offset:0\n" \
"ds_read_b128 a[4:7], v97 offset:0\n" \
"v_mfma_f32_16x16x32_bf16 a[212:215], a[40:43], a[104:107], 0\n" \
"ds_read_b64_tr_b16 a[72:73], v103 offset:0\n" \
"ds_read_b64_tr_b16 a[74:75], v103 offset:4096\n" \
"v_mfma_f32_16x16x32_bf16 a[212:215], a[44:47], a[108:111], a[212:215]\n" \
"buffer_load_dwordx4 v66, s[36:39], s82 offen offset:2048 lds\n" \
"v_mfma_f32_16x16x32_bf16 a[208:211], a[40:43], a[96:99], 0\n" \
"ds_read_b64_tr_b16 a[76:77], v103 offset:16384\n" \
"ds_read_b64_tr_b16 a[78:79], v103 offset:20480\n" \
"v_mfma_f32_16x16x32_bf16 a[208:211], a[44:47], a[100:103], a[208:211]\n" \
"ds_read_b64_tr_b16 a[80:81], v102 offset:512\n" \
"ds_read_b64_tr_b16 a[82:83], v102 offset:4608\n" \
"v_mfma_f32_16x16x32_bf16 a[224:227], a[48:51], a[96:99], 0\n" \
"ds_read_b64_tr_b16 a[84:85], v102 offset:16896\n" \
"ds_read_b64_tr_b16 a[86:87], v102 offset:20992\n" \
"v_mfma_f32_16x16x32_bf16 a[224:227], a[52:55], a[100:103], a[224:227]\n" \
"buffer_load_dwordx4 v67, s[36:39], s82 offen offset:3072 lds\n" \
"v_mfma_f32_16x16x32_bf16 a[228:231], a[48:51], a[104:107], 0\n" \
"s_mov_b64 s[64:65], s[14:15]\n" \
"s_add_u32 s68, s67, 0x200000\n" \
"ds_read_b64_tr_b16 a[88:89], v103 offset:512\n" \
"ds_read_b64_tr_b16 a[90:91], v103 offset:4608\n" \
"v_mfma_f32_16x16x32_bf16 a[228:231], a[52:55], a[108:111], a[228:231]\n" \
"s_add_u32 s64, s64, s68\n" \
"s_addc_u32 s65, s65, 0\n" \
"ds_read_b64_tr_b16 a[92:93], v103 offset:16896\n" \
"ds_read_b64_tr_b16 a[94:95], v103 offset:20992\n" \
"v_mfma_f32_16x16x32_bf16 a[232:235], a[48:51], a[112:115], 0\n" \
"s_xor_b32 s82, s82, 0x80\n" \
"ds_read_b128 a[8:11], v96 offset:2048\n" \
"ds_read_b128 a[12:15], v97 offset:2048\n" \
"v_mfma_f32_16x16x32_bf16 a[232:235], a[52:55], a[116:119], a[232:235]\n" \
"global_load_dword v238, v234, s[64:65]\n" \
"v_mfma_f32_16x16x32_bf16 a[236:239], a[48:51], a[120:123], 0\n" \
"s_mov_b64 s[64:65], s[16:17]\n" \
"s_add_u32 s68, s67, 0x200000\n" \
"ds_read_b128 a[16:19], v96 offset:4096\n" \
"ds_read_b128 a[20:23], v97 offset:4096\n" \
"v_mfma_f32_16x16x32_bf16 a[236:239], a[52:55], a[124:127], a[236:239]\n" \
"s_add_u32 s64, s64, s68\n" \
"s_addc_u32 s65, s65, 0\n" \
"ds_read_b128 a[24:27], v96 offset:6144\n" \
"ds_read_b128 a[28:31], v97 offset:6144\n" \
"v_mfma_f32_16x16x32_bf16 a[252:255], a[56:59], a[120:123], 0\n" \
"ds_read_b128 a[32:35], v96 offset:8192\n" \
"ds_read_b128 a[36:39], v97 offset:8192\n" \
"v_mfma_f32_16x16x32_bf16 a[252:255], a[60:63], a[124:127], a[252:255]\n" \
"global_load_dword v238, v234, s[64:65]\n" \
"v_mfma_f32_16x16x32_bf16 a[248:251], a[56:59], a[112:115], 0\n" \
"ds_read_b128 a[40:43], v96 offset:10240\n" \
"ds_read_b128 a[44:47], v97 offset:10240\n" \
"v_mfma_f32_16x16x32_bf16 a[248:251], a[60:63], a[116:119], a[248:251]\n" \
"ds_read_b128 a[48:51], v96 offset:12288\n" \
"ds_read_b128 a[52:55], v97 offset:12288\n" \
"v_mfma_f32_16x16x32_bf16 a[244:247], a[56:59], a[104:107], 0\n" \
"v_mfma_f32_16x16x32_bf16 a[244:247], a[60:63], a[108:111], a[244:247]\n" \
"v_mfma_f32_16x16x32_bf16 a[240:243], a[56:59], a[96:99], 0\n" \
"v_mfma_f32_16x16x32_bf16 a[240:243], a[60:63], a[100:103], a[240:243]\n" \
"s_waitcnt lgkmcnt(2)\n" \
"v_mfma_f32_16x16x32_bf16 v[0:3], a[64:67], a[0:3], v[0:3]\n" \
"s_lshl_b32 s79, s61, 12\n" \
"ds_read_b128 a[56:59], v96 offset:14336\n" \
"v_mfma_f32_16x16x32_bf16 v[0:3], a[68:71], a[4:7], v[0:3]\n" \
"s_add_u32 s79, s79, %3\n" \
"v_add_u32 v228, 4096, v84\n" \
"v_add_u32 v229, 3200, v84\n" \
"v_mfma_f32_16x16x32_bf16 v[4:7], a[72:75], a[0:3], v[4:7]\n" \
"s_add_u32 m0, s74, 0\n" \
"ds_read_b128 a[60:63], v97 offset:14336\n" \
"v_mfma_f32_16x16x32_bf16 v[4:7], a[76:79], a[4:7], v[4:7]\n" \
"buffer_load_dwordx4 v68, s[40:43], s83 offen offset:0 lds\n" \
"v_mfma_f32_16x16x32_bf16 v[8:11], a[80:83], a[0:3], v[8:11]\n" \
"s_lshl_b32 s78, s62, 8\n" \
"ds_read_b64_tr_b16 a[96:97], v102 offset:1024\n" \
"v_mfma_f32_16x16x32_bf16 v[8:11], a[84:87], a[4:7], v[8:11]\n" \
"s_add_u32 s89, s78, 30720\n" \
"v_mfma_f32_16x16x32_bf16 v[12:15], a[88:91], a[0:3], v[12:15]\n" \
"s_mov_b64 s[48:49], s[8:9]\n" \
"ds_read_b64_tr_b16 a[98:99], v102 offset:5120\n" \
"v_mfma_f32_16x16x32_bf16 v[12:15], a[92:95], a[4:7], v[12:15]\n" \
"buffer_load_dwordx4 v69, s[44:47], s83 offen offset:1024 lds\n" \
"v_mfma_f32_16x16x32_bf16 v[28:31], a[88:91], a[8:11], v[28:31]\n" \
"s_and_b32 s49, s49, 0xffff\n" \
"ds_read_b64_tr_b16 a[100:101], v102 offset:17408\n" \
"v_mfma_f32_16x16x32_bf16 v[28:31], a[92:95], a[12:15], v[28:31]\n" \
"s_mov_b32 s50, 4194304\n" \
"v_mfma_f32_16x16x32_bf16 v[24:27], a[80:83], a[8:11], v[24:27]\n" \
"s_mov_b32 s51, 0x20000\n" \
"ds_read_b64_tr_b16 a[102:103], v102 offset:21504\n" \
"v_mfma_f32_16x16x32_bf16 v[24:27], a[84:87], a[12:15], v[24:27]\n" \
"buffer_load_dwordx4 v70, s[40:43], s83 offen offset:2048 lds\n" \
"v_mfma_f32_16x16x32_bf16 v[20:23], a[72:75], a[8:11], v[20:23]\n" \
"s_mov_b64 s[52:53], s[12:13]\n" \
"ds_read_b64_tr_b16 a[104:105], v103 offset:1024\n" \
"v_mfma_f32_16x16x32_bf16 v[20:23], a[76:79], a[12:15], v[20:23]\n" \
"s_sub_u32 s52, s52, 4096\n" \
"s_subb_u32 s53, s53, 0\n" \
"v_mfma_f32_16x16x32_bf16 v[16:19], a[64:67], a[8:11], v[16:19]\n" \
"s_and_b32 s53, s53, 0xffff\n" \
"ds_read_b64_tr_b16 a[106:107], v103 offset:5120\n" \
"v_mfma_f32_16x16x32_bf16 v[16:19], a[68:71], a[12:15], v[16:19]\n" \
"buffer_load_dwordx4 v71, s[44:47], s83 offen offset:3072 lds\n" \
"v_mfma_f32_16x16x32_bf16 v[32:35], a[64:67], a[16:19], v[32:35]\n" \
"s_mov_b32 s54, 8392704\n" \
"s_mov_b32 s55, 0x20000\n" \
"ds_read_b64_tr_b16 a[108:109], v103 offset:17408\n" \
"v_mfma_f32_16x16x32_bf16 v[32:35], a[68:71], a[20:23], v[32:35]\n" \
"s_xor_b32 s83, s83, 0x380000\n" \
"v_lshl_add_u32 v224, v85, 11, v86\n" \
"v_add_u32 v225, 15360, v224\n" \
"v_mfma_f32_16x16x32_bf16 v[36:39], a[72:75], a[16:19], v[36:39]\n" \
"s_add_u32 m0, s74, 4096\n" \
"ds_read_b64_tr_b16 a[110:111], v103 offset:21504\n" \
"v_add_u32 v226, 129024, v224\n" \
"v_mfma_f32_16x16x32_bf16 v[36:39], a[76:79], a[20:23], v[36:39]\n" \
"buffer_load_dwordx4 v68, s[40:43], s84 offen offset:0 lds\n" \
"v_mfma_f32_16x16x32_bf16 v[40:43], a[80:83], a[16:19], v[40:43]\n" \
"ds_read_b64_tr_b16 a[112:113], v102 offset:1536\n" \
"v_add_u32 v227, 144384, v224\n" \
"v_mfma_f32_16x16x32_bf16 v[40:43], a[84:87], a[20:23], v[40:43]\n" \
"s_lshl_b32 s70, s61, 15\n" \
"v_mfma_f32_16x16x32_bf16 v[44:47], a[88:91], a[16:19], v[44:47]\n" \
"s_lshl_b32 s75, s63, 18\n" \
"ds_read_b64_tr_b16 a[114:115], v102 offset:5632\n" \
"v_mfma_f32_16x16x32_bf16 v[44:47], a[92:95], a[20:23], v[44:47]\n" \
"buffer_load_dwordx4 v69, s[44:47], s84 offen offset:1024 lds\n" \
"v_mfma_f32_16x16x32_bf16 v[60:63], a[88:91], a[24:27], v[60:63]\n" \
"s_sub_u32 s77, 3932160, s75\n" \
"ds_read_b64_tr_b16 a[116:117], v102 offset:17920\n" \
"v_mfma_f32_16x16x32_bf16 v[60:63], a[92:95], a[28:31], v[60:63]\n" \
"s_lshr_b32 s71, %1, 6\n" \
"v_mfma_f32_16x16x32_bf16 v[56:59], a[80:83], a[24:27], v[56:59]\n" \
"s_xor_b32 s71, s71, s63\n" \
"ds_read_b64_tr_b16 a[118:119], v102 offset:22016\n" \
"v_mfma_f32_16x16x32_bf16 v[56:59], a[84:87], a[28:31], v[56:59]\n" \
"buffer_load_dwordx4 v70, s[40:43], s84 offen offset:2048 lds\n" \
"v_mfma_f32_16x16x32_bf16 v[52:55], a[72:75], a[24:27], v[52:55]\n" \
"s_and_b32 s71, s71, 0\n" \
"ds_read_b64_tr_b16 a[120:121], v103 offset:1536\n" \
"v_mfma_f32_16x16x32_bf16 v[52:55], a[76:79], a[28:31], v[52:55]\n" \
"s_lshl_b32 s71, s71, 7\n" \
"v_mfma_f32_16x16x32_bf16 v[48:51], a[64:67], a[24:27], v[48:51]\n" \
"s_add_u32 s70, s70, s71\n" \
"ds_read_b64_tr_b16 a[122:123], v103 offset:5632\n" \
"v_mfma_f32_16x16x32_bf16 v[48:51], a[68:71], a[28:31], v[48:51]\n" \
"buffer_load_dwordx4 v71, s[44:47], s84 offen offset:3072 lds\n" \
"v_mfma_f32_16x16x32_bf16 v[144:147], a[64:67], a[32:35], v[144:147]\n" \
"s_add_u32 s75, s75, s70\n" \
"ds_read_b64_tr_b16 a[124:125], v103 offset:17920\n" \
"v_mfma_f32_16x16x32_bf16 v[144:147], a[68:71], a[36:39], v[144:147]\n" \
"s_add_u32 s77, s77, s70\n" \
"v_mfma_f32_16x16x32_bf16 v[148:151], a[72:75], a[32:35], v[148:151]\n" \
"s_xor_b32 s84, s84, 0x380000\n" \
"ds_read_b64_tr_b16 a[126:127], v103 offset:22016\n" \
"v_mfma_f32_16x16x32_bf16 v[148:151], a[76:79], a[36:39], v[148:151]\n" \
"s_lshl_b32 s71, s71, 12\n" \
"v_mfma_f32_16x16x32_bf16 v[152:155], a[80:83], a[32:35], v[152:155]\n" \
"s_add_u32 s78, s78, s71\n" \
"v_mfma_f32_16x16x32_bf16 v[152:155], a[84:87], a[36:39], v[152:155]\n" \
"s_add_u32 s89, s89, s71\n" \
"v_lshlrev_b32 v242, 2, v78\n" \
"v_and_b32 v236, 0xf0, v234\n" \
"v_mfma_f32_16x16x32_bf16 v[156:159], a[88:91], a[32:35], v[156:159]\n" \
"v_lshl_add_u32 v236, v87, 9, v236\n" \
"v_mfma_f32_16x16x32_bf16 v[156:159], a[92:95], a[36:39], v[156:159]\n" \
"s_add_u32 s71, s74, 0x10000\n" \
"v_add_u32 v230, s71, v88\n" \
"v_xor_b32 v231, 64, v230\n" \
"v_mfma_f32_16x16x32_bf16 v[188:191], a[88:91], a[40:43], v[188:191]\n" \
"v_add_u32 v250, 0x10000, v230\n" \
"v_add_u32 v251, 0x10000, v231\n" \
"v_mfma_f32_16x16x32_bf16 v[188:191], a[92:95], a[44:47], v[188:191]\n" \
"v_lshl_add_u32 v82, v90, 12, v89\n" \
"v_add_u32 v232, %3, v82\n" \
"v_mfma_f32_16x16x32_bf16 v[184:187], a[80:83], a[40:43], v[184:187]\n" \
"v_xor_b32 v233, 32, v232\n" \
"v_mfma_f32_16x16x32_bf16 v[184:187], a[84:87], a[44:47], v[184:187]\n" \
"Lpg592_%=:\n" \
"Lst0x0_%=:\n" \
"s_cbranch_execz Lpg2772_%=\n" \
"s_mul_i32 s87, s62, 557824\n" \
"v_mfma_f32_16x16x32_bf16 v[180:183], a[72:75], a[40:43], v[180:183]\n" \
"s_lshr_b32 s70, s61, 1\n" \
"v_mfma_f32_16x16x32_bf16 v[180:183], a[76:79], a[44:47], v[180:183]\n" \
"s_mul_i32 s70, s70, 17408\n" \
"v_mfma_f32_16x16x32_bf16 v[176:179], a[64:67], a[40:43], v[176:179]\n" \
"s_add_u32 s87, s87, s70\n" \
"v_mfma_f32_16x16x32_bf16 v[176:179], a[68:71], a[44:47], v[176:179]\n" \
"s_waitcnt lgkmcnt(0)\n" \
"s_and_b32 s70, s61, 1\n" \
"v_mfma_f32_16x16x32_bf16 v[192:195], a[64:67], a[48:51], v[192:195]\n" \
"s_lshl_b32 s70, s70, 12\n" \
"v_mfma_f32_16x16x32_bf16 v[192:195], a[68:71], a[52:55], v[192:195]\n" \
"s_add_u32 s87, s87, s70\n" \
"v_mfma_f32_16x16x32_bf16 v[196:199], a[72:75], a[48:51], v[196:199]\n" \
"s_mul_i32 s70, s63, 34816\n" \
"v_mfma_f32_16x16x32_bf16 v[196:199], a[76:79], a[52:55], v[196:199]\n" \
"s_add_u32 s94, s87, 522240\n" \
"v_mfma_f32_16x16x32_bf16 v[200:203], a[80:83], a[48:51], v[200:203]\n" \
"s_sub_u32 s94, s94, s70\n" \
"v_mfma_f32_16x16x32_bf16 v[200:203], a[84:87], a[52:55], v[200:203]\n" \
"s_add_u32 s87, s87, s70\n" \
"v_mfma_f32_16x16x32_bf16 v[204:207], a[88:91], a[48:51], v[204:207]\n" \
"s_lshl_b32 s90, s62, 7\n" \
"v_mfma_f32_16x16x32_bf16 v[204:207], a[92:95], a[52:55], v[204:207]\n" \
"s_add_u32 s95, s90, 65597440\n" \
"v_mfma_f32_16x16x32_bf16 v[220:223], a[88:91], a[56:59], v[220:223]\n" \
"s_add_u32 s90, s90, 65585152\n" \
"v_mfma_f32_16x16x32_bf16 v[220:223], a[92:95], a[60:63], v[220:223]\n" \
"s_lshl_b32 s92, 0xff, s63\n" \
"v_mfma_f32_16x16x32_bf16 v[216:219], a[80:83], a[56:59], v[216:219]\n" \
"s_and_b32 s92, s92, 0xff\n" \
"v_mfma_f32_16x16x32_bf16 v[216:219], a[84:87], a[60:63], v[216:219]\n" \
"s_mov_b32 s76, 0x3e0293ee\n" \
"v_mfma_f32_16x16x32_bf16 v[212:215], a[72:75], a[56:59], v[212:215]\n" \
"s_mov_b32 s31, 0x3e800000\n" \
"v_mfma_f32_16x16x32_bf16 v[212:215], a[76:79], a[60:63], v[212:215]\n" \
"v_mfma_f32_16x16x32_bf16 v[208:211], a[64:67], a[56:59], v[208:211]\n" \
"v_mfma_f32_16x16x32_bf16 v[208:211], a[68:71], a[60:63], v[208:211]\n" \
"s_waitcnt vmcnt(10) lgkmcnt(0)\n" \
"s_barrier\n" \
"v_readfirstlane_b32 s60, v240\n" \
"s_add_u32 s60, s60, 0x9e3779b9\n" \
"v_mfma_f32_16x16x32_bf16 a[128:131], a[0:3], a[96:99], a[128:131]\n" \
"s_add_u32 m0, s74, 131072\n" \
"ds_read_b64_tr_b16 a[64:65], v98 offset:32768\n" \
"v_mfma_f32_16x16x32_bf16 a[128:131], a[4:7], a[100:103], a[128:131]\n" \
"buffer_load_dwordx4 v64, s[36:39], s81 offen offset:0 lds\n" \
"v_mfma_f32_16x16x32_bf16 a[132:135], a[0:3], a[104:107], a[132:135]\n" \
"ds_read_b64_tr_b16 a[66:67], v98 offset:36864\n" \
"v_mfma_f32_16x16x32_bf16 a[132:135], a[4:7], a[108:111], a[132:135]\n" \
"v_mfma_f32_16x16x32_bf16 a[136:139], a[0:3], a[112:115], a[136:139]\n" \
"ds_read_b64_tr_b16 a[68:69], v98 offset:49152\n" \
"v_mfma_f32_16x16x32_bf16 a[136:139], a[4:7], a[116:119], a[136:139]\n" \
"buffer_load_dwordx4 v65, s[36:39], s81 offen offset:1024 lds\n" \
"v_mfma_f32_16x16x32_bf16 a[140:143], a[0:3], a[120:123], a[140:143]\n" \
"ds_read_b64_tr_b16 a[70:71], v98 offset:53248\n" \
"v_mfma_f32_16x16x32_bf16 a[140:143], a[4:7], a[124:127], a[140:143]\n" \
"v_mfma_f32_16x16x32_bf16 a[156:159], a[8:11], a[120:123], a[156:159]\n" \
"ds_read_b128 a[0:3], v72 offset:32768\n" \
"v_mfma_f32_16x16x32_bf16 a[156:159], a[12:15], a[124:127], a[156:159]\n" \
"buffer_load_dwordx4 v66, s[36:39], s81 offen offset:2048 lds\n" \
"v_mfma_f32_16x16x32_bf16 a[152:155], a[8:11], a[112:115], a[152:155]\n" \
"ds_read_b64_tr_b16 a[72:73], v99 offset:32768\n" \
"v_mfma_f32_16x16x32_bf16 a[152:155], a[12:15], a[116:119], a[152:155]\n" \
"v_mfma_f32_16x16x32_bf16 a[148:151], a[8:11], a[104:107], a[148:151]\n" \
"ds_read_b128 a[4:7], v73 offset:32768\n" \
"v_mfma_f32_16x16x32_bf16 a[148:151], a[12:15], a[108:111], a[148:151]\n" \
"buffer_load_dwordx4 v67, s[36:39], s81 offen offset:3072 lds\n" \
"v_mfma_f32_16x16x32_bf16 a[144:147], a[8:11], a[96:99], a[144:147]\n" \
"ds_read_b64_tr_b16 a[74:75], v99 offset:36864\n" \
"v_mfma_f32_16x16x32_bf16 a[144:147], a[12:15], a[100:103], a[144:147]\n" \
"s_xor_b32 s81, s81, 0x380\n" \
"v_mfma_f32_16x16x32_bf16 a[160:163], a[16:19], a[96:99], a[160:163]\n" \
"s_add_u32 m0, s74, 135168\n" \
"ds_read_b128 a[8:11], v72 offset:34816\n" \
"v_mfma_f32_16x16x32_bf16 a[160:163], a[20:23], a[100:103], a[160:163]\n" \
"buffer_load_dwordx4 v64, s[36:39], s82 offen offset:0 lds\n" \
"v_mfma_f32_16x16x32_bf16 a[164:167], a[16:19], a[104:107], a[164:167]\n" \
"ds_read_b64_tr_b16 a[76:77], v99 offset:49152\n" \
"v_mfma_f32_16x16x32_bf16 a[164:167], a[20:23], a[108:111], a[164:167]\n" \
"v_mfma_f32_16x16x32_bf16 a[168:171], a[16:19], a[112:115], a[168:171]\n" \
"ds_read_b128 a[12:15], v73 offset:34816\n" \
"v_mfma_f32_16x16x32_bf16 a[168:171], a[20:23], a[116:119], a[168:171]\n" \
"buffer_load_dwordx4 v65, s[36:39], s82 offen offset:1024 lds\n" \
"v_mfma_f32_16x16x32_bf16 a[172:175], a[16:19], a[120:123], a[172:175]\n" \
"ds_read_b64_tr_b16 a[78:79], v99 offset:53248\n" \
"v_mfma_f32_16x16x32_bf16 a[172:175], a[20:23], a[124:127], a[172:175]\n" \
"v_mfma_f32_16x16x32_bf16 a[188:191], a[24:27], a[120:123], a[188:191]\n" \
"ds_read_b128 a[16:19], v72 offset:36864\n" \
"v_mfma_f32_16x16x32_bf16 a[188:191], a[28:31], a[124:127], a[188:191]\n" \
"buffer_load_dwordx4 v66, s[36:39], s82 offen offset:2048 lds\n" \
"v_mfma_f32_16x16x32_bf16 a[184:187], a[24:27], a[112:115], a[184:187]\n" \
"ds_read_b64_tr_b16 a[80:81], v98 offset:33280\n" \
"v_mfma_f32_16x16x32_bf16 a[184:187], a[28:31], a[116:119], a[184:187]\n" \
"v_mfma_f32_16x16x32_bf16 a[180:183], a[24:27], a[104:107], a[180:183]\n" \
"ds_read_b128 a[20:23], v73 offset:36864\n" \
"v_mfma_f32_16x16x32_bf16 a[180:183], a[28:31], a[108:111], a[180:183]\n" \
"buffer_load_dwordx4 v67, s[36:39], s82 offen offset:3072 lds\n" \
"v_mfma_f32_16x16x32_bf16 a[176:179], a[24:27], a[96:99], a[176:179]\n" \
"s_mov_b64 s[64:65], s[10:11]\n" \
"s_lshl1_add_u32 s68, s66, 512\n" \
"ds_read_b64_tr_b16 a[82:83], v98 offset:37376\n" \
"v_mfma_f32_16x16x32_bf16 a[176:179], a[28:31], a[100:103], a[176:179]\n" \
"s_add_u32 s64, s64, s68\n" \
"s_addc_u32 s65, s65, 0\n" \
"v_mfma_f32_16x16x32_bf16 a[192:195], a[32:35], a[96:99], a[192:195]\n" \
"s_xor_b32 s82, s82, 0x380\n" \
"ds_read_b128 a[24:27], v72 offset:38912\n" \
"v_mfma_f32_16x16x32_bf16 a[192:195], a[36:39], a[100:103], a[192:195]\n" \
"global_load_dword v238, v110, s[64:65]\n" \
"v_mfma_f32_16x16x32_bf16 a[196:199], a[32:35], a[104:107], a[196:199]\n" \
"s_mov_b64 s[64:65], s[14:15]\n" \
"s_add_u32 s68, s67, 0x300000\n" \
"ds_read_b64_tr_b16 a[84:85], v98 offset:49664\n" \
"v_mfma_f32_16x16x32_bf16 a[196:199], a[36:39], a[108:111], a[196:199]\n" \
"s_add_u32 s64, s64, s68\n" \
"s_addc_u32 s65, s65, 0\n" \
"v_mfma_f32_16x16x32_bf16 a[200:203], a[32:35], a[112:115], a[200:203]\n" \
"ds_read_b128 a[28:31], v73 offset:38912\n" \
"v_mfma_f32_16x16x32_bf16 a[200:203], a[36:39], a[116:119], a[200:203]\n" \
"global_load_dword v238, v234, s[64:65]\n" \
"v_mfma_f32_16x16x32_bf16 a[204:207], a[32:35], a[120:123], a[204:207]\n" \
"ds_read_b64_tr_b16 a[86:87], v98 offset:53760\n" \
"v_mfma_f32_16x16x32_bf16 a[204:207], a[36:39], a[124:127], a[204:207]\n" \
"v_mfma_f32_16x16x32_bf16 a[220:223], a[40:43], a[120:123], a[220:223]\n" \
"ds_read_b128 a[32:35], v72 offset:40960\n" \
"v_mfma_f32_16x16x32_bf16 a[220:223], a[44:47], a[124:127], a[220:223]\n" \
"v_mfma_f32_16x16x32_bf16 a[216:219], a[40:43], a[112:115], a[216:219]\n" \
"ds_read_b64_tr_b16 a[88:89], v99 offset:33280\n" \
"v_mfma_f32_16x16x32_bf16 a[216:219], a[44:47], a[116:119], a[216:219]\n" \
"v_mfma_f32_16x16x32_bf16 a[212:215], a[40:43], a[104:107], a[212:215]\n" \
"ds_read_b128 a[36:39], v73 offset:40960\n" \
"v_mfma_f32_16x16x32_bf16 a[212:215], a[44:47], a[108:111], a[212:215]\n" \
"v_mfma_f32_16x16x32_bf16 a[208:211], a[40:43], a[96:99], a[208:211]\n" \
"ds_read_b64_tr_b16 a[90:91], v99 offset:37376\n" \
"v_mfma_f32_16x16x32_bf16 a[208:211], a[44:47], a[100:103], a[208:211]\n" \
"v_mfma_f32_16x16x32_bf16 a[224:227], a[48:51], a[96:99], a[224:227]\n" \
"ds_read_b128 a[40:43], v72 offset:43008\n" \
"v_mfma_f32_16x16x32_bf16 a[224:227], a[52:55], a[100:103], a[224:227]\n" \
"v_mfma_f32_16x16x32_bf16 a[228:231], a[48:51], a[104:107], a[228:231]\n" \
"ds_read_b64_tr_b16 a[92:93], v99 offset:49664\n" \
"v_mfma_f32_16x16x32_bf16 a[228:231], a[52:55], a[108:111], a[228:231]\n" \
"v_mfma_f32_16x16x32_bf16 a[232:235], a[48:51], a[112:115], a[232:235]\n" \
"ds_read_b128 a[44:47], v73 offset:43008\n" \
"v_mfma_f32_16x16x32_bf16 a[232:235], a[52:55], a[116:119], a[232:235]\n" \
"v_mfma_f32_16x16x32_bf16 a[236:239], a[48:51], a[120:123], a[236:239]\n" \
"ds_read_b64_tr_b16 a[94:95], v99 offset:53760\n" \
"v_mfma_f32_16x16x32_bf16 a[236:239], a[52:55], a[124:127], a[236:239]\n" \
"v_mfma_f32_16x16x32_bf16 a[252:255], a[56:59], a[120:123], a[252:255]\n" \
"ds_read_b128 a[48:51], v72 offset:45056\n" \
"v_mfma_f32_16x16x32_bf16 a[252:255], a[60:63], a[124:127], a[252:255]\n" \
"v_mfma_f32_16x16x32_bf16 a[248:251], a[56:59], a[112:115], a[248:251]\n" \
"v_mfma_f32_16x16x32_bf16 a[248:251], a[60:63], a[116:119], a[248:251]\n" \
"v_mfma_f32_16x16x32_bf16 a[244:247], a[56:59], a[104:107], a[244:247]\n" \
"ds_read_b128 a[52:55], v73 offset:45056\n" \
"v_mfma_f32_16x16x32_bf16 a[244:247], a[60:63], a[108:111], a[244:247]\n" \
"v_mfma_f32_16x16x32_bf16 a[240:243], a[56:59], a[96:99], a[240:243]\n" \
"v_mfma_f32_16x16x32_bf16 a[240:243], a[60:63], a[100:103], a[240:243]\n" \
"s_waitcnt lgkmcnt(1)\n" \
"v_mfma_f32_16x16x32_bf16 v[0:3], a[64:67], a[0:3], v[0:3]\n" \
"ds_read_b128 a[56:59], v72 offset:47104\n" \
"v_lshrrev_b32 v104, 4, v234\n" \
"v_mfma_f32_16x16x32_bf16 v[0:3], a[68:71], a[4:7], v[0:3]\n" \
"v_bfe_u32 v105, v104, 4, 1\n" \
"v_bfe_u32 v106, v104, 2, 2\n" \
"v_mfma_f32_16x16x32_bf16 v[4:7], a[72:75], a[0:3], v[4:7]\n" \
"s_add_u32 m0, s74, 65536\n" \
"ds_read_b128 a[60:63], v73 offset:47104\n" \
"v_lshl_add_u32 v106, v105, 3, v106\n" \
"v_mfma_f32_16x16x32_bf16 v[4:7], a[76:79], a[4:7], v[4:7]\n" \
"buffer_load_dwordx4 v68, s[40:43], s83 offen offset:0 lds\n" \
"v_mfma_f32_16x16x32_bf16 v[8:11], a[80:83], a[0:3], v[8:11]\n" \
"s_lshl_b32 s71, s61, 4\n" \
"ds_read_b64_tr_b16 a[96:97], v98 offset:33792\n" \
"v_add_u32 v106, s71, v106\n" \
"v_mfma_f32_16x16x32_bf16 v[8:11], a[84:87], a[4:7], v[8:11]\n" \
"v_mul_u32_u24 v106, 6144, v106\n" \
"v_lshlrev_b32 v105, 1, v105\n" \
"v_mfma_f32_16x16x32_bf16 v[12:15], a[88:91], a[0:3], v[12:15]\n" \
"ds_read_b64_tr_b16 a[98:99], v98 offset:37888\n" \
"v_and_b32 v107, 3, v104\n" \
"v_mfma_f32_16x16x32_bf16 v[12:15], a[92:95], a[4:7], v[12:15]\n" \
"buffer_load_dwordx4 v69, s[44:47], s83 offen offset:1024 lds\n" \
"v_mfma_f32_16x16x32_bf16 v[28:31], a[88:91], a[8:11], v[28:31]\n" \
"ds_read_b64_tr_b16 a[100:101], v98 offset:50176\n" \
"v_xor_b32 v105, v105, v107\n" \
"v_mfma_f32_16x16x32_bf16 v[28:31], a[92:95], a[12:15], v[28:31]\n" \
"v_lshl_add_u32 v106, v105, 4, v106\n" \
"v_lshrrev_b32 v105, 5, v104\n" \
"v_mfma_f32_16x16x32_bf16 v[24:27], a[80:83], a[8:11], v[24:27]\n" \
"ds_read_b64_tr_b16 a[102:103], v98 offset:54272\n" \
"v_lshl_add_u32 v244, v105, 6, v106\n" \
"v_mfma_f32_16x16x32_bf16 v[24:27], a[84:87], a[12:15], v[24:27]\n" \
"buffer_load_dwordx4 v70, s[40:43], s83 offen offset:2048 lds\n" \
"v_mfma_f32_16x16x32_bf16 v[20:23], a[72:75], a[8:11], v[20:23]\n" \
"ds_read_b64_tr_b16 a[104:105], v99 offset:33792\n" \
"v_lshrrev_b32 v107, 3, v104\n" \
"v_mfma_f32_16x16x32_bf16 v[20:23], a[76:79], a[12:15], v[20:23]\n" \
"v_and_b32 v108, 7, v104\n" \
"v_xor_b32 v109, v108, v107\n" \
"v_mfma_f32_16x16x32_bf16 v[16:19], a[64:67], a[8:11], v[16:19]\n" \
"ds_read_b64_tr_b16 a[106:107], v99 offset:37888\n" \
"v_lshlrev_b32 v109, 4, v109\n" \
"v_mfma_f32_16x16x32_bf16 v[16:19], a[68:71], a[12:15], v[16:19]\n" \
"buffer_load_dwordx4 v71, s[44:47], s83 offen offset:3072 lds\n" \
"v_mfma_f32_16x16x32_bf16 v[32:35], a[64:67], a[16:19], v[32:35]\n" \
"ds_read_b64_tr_b16 a[108:109], v99 offset:50176\n" \
"v_lshl_add_u32 v245, v107, 12, v109\n" \
"v_mfma_f32_16x16x32_bf16 v[32:35], a[68:71], a[20:23], v[32:35]\n" \
"s_xor_b32 s83, s83, 0x80000\n" \
"v_mfma_f32_16x16x32_bf16 v[36:39], a[72:75], a[16:19], v[36:39]\n" \
"s_add_u32 m0, s74, 69632\n" \
"ds_read_b64_tr_b16 a[110:111], v99 offset:54272\n" \
"v_mfma_f32_16x16x32_bf16 v[36:39], a[76:79], a[20:23], v[36:39]\n" \
"buffer_load_dwordx4 v68, s[40:43], s84 offen offset:0 lds\n" \
"v_mfma_f32_16x16x32_bf16 v[40:43], a[80:83], a[16:19], v[40:43]\n" \
"s_lshr_b32 s71, s61, 1\n" \
"ds_read_b64_tr_b16 a[112:113], v98 offset:34304\n" \
"v_mfma_f32_16x16x32_bf16 v[40:43], a[84:87], a[20:23], v[40:43]\n" \
"s_lshl_b32 s70, s71, 11\n" \
"v_lshrrev_b32 v107, 4, v104\n" \
"v_xor_b32 v107, v107, v108\n" \
"v_mfma_f32_16x16x32_bf16 v[44:47], a[88:91], a[16:19], v[44:47]\n" \
"ds_read_b64_tr_b16 a[114:115], v98 offset:38400\n" \
"v_and_b32 v109, 15, v104\n" \
"v_mfma_f32_16x16x32_bf16 v[44:47], a[92:95], a[20:23], v[44:47]\n" \
"buffer_load_dwordx4 v69, s[44:47], s84 offen offset:1024 lds\n" \
"v_mfma_f32_16x16x32_bf16 v[60:63], a[88:91], a[24:27], v[60:63]\n" \
"ds_read_b64_tr_b16 a[116:117], v98 offset:50688\n" \
"v_lshlrev_b32 v106, 7, v109\n" \
"v_mfma_f32_16x16x32_bf16 v[60:63], a[92:95], a[28:31], v[60:63]\n" \
"v_lshl_add_u32 v106, v107, 4, v106\n" \
"v_mfma_f32_16x16x32_bf16 v[56:59], a[80:83], a[24:27], v[56:59]\n" \
"s_add_u32 s70, s70, %3\n" \
"ds_read_b64_tr_b16 a[118:119], v98 offset:54784\n" \
"v_add_u32 v247, s70, v106\n" \
"v_mfma_f32_16x16x32_bf16 v[56:59], a[84:87], a[28:31], v[56:59]\n" \
"buffer_load_dwordx4 v70, s[40:43], s84 offen offset:2048 lds\n" \
"v_mfma_f32_16x16x32_bf16 v[52:55], a[72:75], a[24:27], v[52:55]\n" \
"ds_read_b64_tr_b16 a[120:121], v99 offset:34304\n" \
"v_lshrrev_b32 v109, 3, v109\n" \
"v_mfma_f32_16x16x32_bf16 v[52:55], a[76:79], a[28:31], v[52:55]\n" \
"v_mul_u32_u24 v109, 7168, v109\n" \
"v_add_u32 v109, v109, v106\n" \
"v_mfma_f32_16x16x32_bf16 v[48:51], a[64:67], a[24:27], v[48:51]\n" \
"s_add_u32 s70, %3, 131072\n" \
"ds_read_b64_tr_b16 a[122:123], v99 offset:38400\n" \
"v_add_u32 v248, s70, v109\n" \
"v_mfma_f32_16x16x32_bf16 v[48:51], a[68:71], a[28:31], v[48:51]\n" \
"buffer_load_dwordx4 v71, s[44:47], s84 offen offset:3072 lds\n" \
"v_mfma_f32_16x16x32_bf16 v[144:147], a[64:67], a[32:35], v[144:147]\n" \
"ds_read_b64_tr_b16 a[124:125], v99 offset:50688\n" \
"v_and_b32 v106, 3, v104\n" \
"v_mfma_f32_16x16x32_bf16 v[144:147], a[68:71], a[36:39], v[144:147]\n" \
"v_lshlrev_b32 v106, 3, v106\n" \
"v_bfe_u32 v107, v104, 2, 2\n" \
"v_mfma_f32_16x16x32_bf16 v[148:151], a[72:75], a[32:35], v[148:151]\n" \
"s_xor_b32 s84, s84, 0x80000\n" \
"ds_read_b64_tr_b16 a[126:127], v99 offset:54784\n" \
"v_lshl_add_u32 v106, v107, 6, v106\n" \
"v_mfma_f32_16x16x32_bf16 v[148:151], a[76:79], a[36:39], v[148:151]\n" \
"v_bfe_u32 v107, v104, 4, 1\n" \
"v_mul_u32_u24 v107, 288, v107\n" \
"v_mfma_f32_16x16x32_bf16 v[152:155], a[80:83], a[32:35], v[152:155]\n" \
"v_mul_u32_u24 v108, 6144, v105\n" \
"v_add3_u32 v106, v106, v107, v108\n" \
"v_mfma_f32_16x16x32_bf16 v[152:155], a[84:87], a[36:39], v[152:155]\n" \
"s_and_b32 s70, s61, 1\n" \
"v_mfma_f32_16x16x32_bf16 v[156:159], a[88:91], a[32:35], v[156:159]\n" \
"s_lshl_b32 s70, s70, 9\n" \
"v_mfma_f32_16x16x32_bf16 v[156:159], a[92:95], a[36:39], v[156:159]\n" \
"s_add_u32 s70, s70, %3\n" \
"v_add_u32 v249, s70, v106\n" \
"v_mfma_f32_16x16x32_bf16 v[188:191], a[88:91], a[40:43], v[188:191]\n" \
"s_lshl_b32 s70, s71, 5\n" \
"v_xor_b32 v249, s70, v249\n" \
"v_mfma_f32_16x16x32_bf16 v[188:191], a[92:95], a[44:47], v[188:191]\n" \
"v_mfma_f32_16x16x32_bf16 v[184:187], a[80:83], a[40:43], v[184:187]\n" \
"v_mfma_f32_16x16x32_bf16 v[184:187], a[84:87], a[44:47], v[184:187]\n" \
"v_mfma_f32_16x16x32_bf16 v[180:183], a[72:75], a[40:43], v[180:183]\n" \
"v_mfma_f32_16x16x32_bf16 v[180:183], a[76:79], a[44:47], v[180:183]\n" \
"v_mfma_f32_16x16x32_bf16 v[176:179], a[64:67], a[40:43], v[176:179]\n" \
"v_mfma_f32_16x16x32_bf16 v[176:179], a[68:71], a[44:47], v[176:179]\n" \
"s_waitcnt lgkmcnt(0)\n" \
"v_mfma_f32_16x16x32_bf16 v[192:195], a[64:67], a[48:51], v[192:195]\n" \
"v_mfma_f32_16x16x32_bf16 v[192:195], a[68:71], a[52:55], v[192:195]\n" \
"v_mfma_f32_16x16x32_bf16 v[196:199], a[72:75], a[48:51], v[196:199]\n" \
"v_mfma_f32_16x16x32_bf16 v[196:199], a[76:79], a[52:55], v[196:199]\n" \
"v_mfma_f32_16x16x32_bf16 v[200:203], a[80:83], a[48:51], v[200:203]\n" \
"v_mfma_f32_16x16x32_bf16 v[200:203], a[84:87], a[52:55], v[200:203]\n" \
"v_mfma_f32_16x16x32_bf16 v[204:207], a[88:91], a[48:51], v[204:207]\n" \
"v_mfma_f32_16x16x32_bf16 v[204:207], a[92:95], a[52:55], v[204:207]\n" \
"v_mfma_f32_16x16x32_bf16 v[220:223], a[88:91], a[56:59], v[220:223]\n" \
"v_mfma_f32_16x16x32_bf16 v[220:223], a[92:95], a[60:63], v[220:223]\n" \
"v_mfma_f32_16x16x32_bf16 v[216:219], a[80:83], a[56:59], v[216:219]\n" \
"v_mfma_f32_16x16x32_bf16 v[216:219], a[84:87], a[60:63], v[216:219]\n" \
"v_mfma_f32_16x16x32_bf16 v[212:215], a[72:75], a[56:59], v[212:215]\n" \
"v_mfma_f32_16x16x32_bf16 v[212:215], a[76:79], a[60:63], v[212:215]\n" \
"v_mfma_f32_16x16x32_bf16 v[208:211], a[64:67], a[56:59], v[208:211]\n" \
"v_mfma_f32_16x16x32_bf16 v[208:211], a[68:71], a[60:63], v[208:211]\n" \
"s_waitcnt vmcnt(10) lgkmcnt(0)\n" \
"s_barrier\n" \
"v_mfma_f32_16x16x32_bf16 a[128:131], a[0:3], a[96:99], a[128:131]\n" \
"s_add_u32 m0, s74, 98304\n" \
"ds_read_b64_tr_b16 a[64:65], v74 offset:0\n" \
"v_mfma_f32_16x16x32_bf16 a[128:131], a[4:7], a[100:103], a[128:131]\n" \
"buffer_load_dwordx4 v64, s[36:39], s81 offen offset:0 lds\n" \
"v_mfma_f32_16x16x32_bf16 a[132:135], a[0:3], a[104:107], a[132:135]\n" \
"ds_read_b64_tr_b16 a[66:67], v74 offset:4096\n" \
"v_mfma_f32_16x16x32_bf16 a[132:135], a[4:7], a[108:111], a[132:135]\n" \
"v_mfma_f32_16x16x32_bf16 a[136:139], a[0:3], a[112:115], a[136:139]\n" \
"ds_read_b64_tr_b16 a[68:69], v74 offset:16384\n" \
"v_mfma_f32_16x16x32_bf16 a[136:139], a[4:7], a[116:119], a[136:139]\n" \
"buffer_load_dwordx4 v65, s[36:39], s81 offen offset:1024 lds\n" \
"v_mfma_f32_16x16x32_bf16 a[140:143], a[0:3], a[120:123], a[140:143]\n" \
"ds_read_b64_tr_b16 a[70:71], v74 offset:20480\n" \
"v_mfma_f32_16x16x32_bf16 a[140:143], a[4:7], a[124:127], a[140:143]\n" \
"v_mfma_f32_16x16x32_bf16 a[156:159], a[8:11], a[120:123], a[156:159]\n" \
"ds_read_b128 a[0:3], v100 offset:0\n" \
"v_mfma_f32_16x16x32_bf16 a[156:159], a[12:15], a[124:127], a[156:159]\n" \
"buffer_load_dwordx4 v66, s[36:39], s81 offen offset:2048 lds\n" \
"v_mfma_f32_16x16x32_bf16 a[152:155], a[8:11], a[112:115], a[152:155]\n" \
"ds_read_b64_tr_b16 a[72:73], v75 offset:0\n" \
"v_mfma_f32_16x16x32_bf16 a[152:155], a[12:15], a[116:119], a[152:155]\n" \
"v_mfma_f32_16x16x32_bf16 a[148:151], a[8:11], a[104:107], a[148:151]\n" \
"ds_read_b128 a[4:7], v101 offset:0\n" \
"v_mfma_f32_16x16x32_bf16 a[148:151], a[12:15], a[108:111], a[148:151]\n" \
"buffer_load_dwordx4 v67, s[36:39], s81 offen offset:3072 lds\n" \
"v_mfma_f32_16x16x32_bf16 a[144:147], a[8:11], a[96:99], a[144:147]\n" \
"ds_read_b64_tr_b16 a[74:75], v75 offset:4096\n" \
"v_mfma_f32_16x16x32_bf16 a[144:147], a[12:15], a[100:103], a[144:147]\n" \
"s_xor_b32 s81, s81, 0x80\n" \
"v_mfma_f32_16x16x32_bf16 a[160:163], a[16:19], a[96:99], a[160:163]\n" \
"s_add_u32 m0, s74, 102400\n" \
"ds_read_b128 a[8:11], v100 offset:2048\n" \
"v_mfma_f32_16x16x32_bf16 a[160:163], a[20:23], a[100:103], a[160:163]\n" \
"buffer_load_dwordx4 v64, s[36:39], s82 offen offset:0 lds\n" \
"v_mfma_f32_16x16x32_bf16 a[164:167], a[16:19], a[104:107], a[164:167]\n" \
"ds_read_b64_tr_b16 a[76:77], v75 offset:16384\n" \
"v_mfma_f32_16x16x32_bf16 a[164:167], a[20:23], a[108:111], a[164:167]\n" \
"v_mfma_f32_16x16x32_bf16 a[168:171], a[16:19], a[112:115], a[168:171]\n" \
"ds_read_b128 a[12:15], v101 offset:2048\n" \
"v_mfma_f32_16x16x32_bf16 a[168:171], a[20:23], a[116:119], a[168:171]\n" \
"buffer_load_dwordx4 v65, s[36:39], s82 offen offset:1024 lds\n" \
"v_mfma_f32_16x16x32_bf16 a[172:175], a[16:19], a[120:123], a[172:175]\n" \
"ds_read_b64_tr_b16 a[78:79], v75 offset:20480\n" \
"v_mfma_f32_16x16x32_bf16 a[172:175], a[20:23], a[124:127], a[172:175]\n" \
"v_mfma_f32_16x16x32_bf16 a[188:191], a[24:27], a[120:123], a[188:191]\n" \
"ds_read_b128 a[16:19], v100 offset:4096\n" \
"v_mfma_f32_16x16x32_bf16 a[188:191], a[28:31], a[124:127], a[188:191]\n" \
"buffer_load_dwordx4 v66, s[36:39], s82 offen offset:2048 lds\n" \
"v_mfma_f32_16x16x32_bf16 a[184:187], a[24:27], a[112:115], a[184:187]\n" \
"ds_read_b64_tr_b16 a[80:81], v74 offset:512\n" \
"v_mfma_f32_16x16x32_bf16 a[184:187], a[28:31], a[116:119], a[184:187]\n" \
"v_mfma_f32_16x16x32_bf16 a[180:183], a[24:27], a[104:107], a[180:183]\n" \
"ds_read_b128 a[20:23], v101 offset:4096\n" \
"v_mfma_f32_16x16x32_bf16 a[180:183], a[28:31], a[108:111], a[180:183]\n" \
"buffer_load_dwordx4 v67, s[36:39], s82 offen offset:3072 lds\n" \
"v_mfma_f32_16x16x32_bf16 a[176:179], a[24:27], a[96:99], a[176:179]\n" \
"s_mov_b64 s[64:65], s[16:17]\n" \
"s_add_u32 s68, s67, 0x300000\n" \
"ds_read_b64_tr_b16 a[82:83], v74 offset:4608\n" \
"v_mfma_f32_16x16x32_bf16 a[176:179], a[28:31], a[100:103], a[176:179]\n" \
"s_add_u32 s64, s64, s68\n" \
"s_addc_u32 s65, s65, 0\n" \
"v_mfma_f32_16x16x32_bf16 a[192:195], a[32:35], a[96:99], a[192:195]\n" \
"s_xor_b32 s82, s82, 0x80\n" \
"ds_read_b128 a[24:27], v100 offset:6144\n" \
"v_mfma_f32_16x16x32_bf16 a[192:195], a[36:39], a[100:103], a[192:195]\n" \
"global_load_dword v238, v234, s[64:65]\n" \
"v_mfma_f32_16x16x32_bf16 a[196:199], a[32:35], a[104:107], a[196:199]\n" \
"s_mov_b64 s[64:65], s[8:9]\n" \
"s_add_u32 s68, s66, 0x0\n" \
"ds_read_b64_tr_b16 a[84:85], v74 offset:16896\n" \
"v_mfma_f32_16x16x32_bf16 a[196:199], a[36:39], a[108:111], a[196:199]\n" \
"s_add_u32 s64, s64, s68\n" \
"s_addc_u32 s65, s65, 0\n" \
"v_mfma_f32_16x16x32_bf16 a[200:203], a[32:35], a[112:115], a[200:203]\n" \
"ds_read_b128 a[28:31], v101 offset:6144\n" \
"v_mfma_f32_16x16x32_bf16 a[200:203], a[36:39], a[116:119], a[200:203]\n" \
"global_load_dword v238, v234, s[64:65]\n" \
"v_mfma_f32_16x16x32_bf16 a[204:207], a[32:35], a[120:123], a[204:207]\n" \
"s_mov_b64 s[64:65], s[8:9]\n" \
"s_add_u32 s68, s66, 0x100000\n" \
"ds_read_b64_tr_b16 a[86:87], v74 offset:20992\n" \
"v_mfma_f32_16x16x32_bf16 a[204:207], a[36:39], a[124:127], a[204:207]\n" \
"s_add_u32 s64, s64, s68\n" \
"s_addc_u32 s65, s65, 0\n" \
"v_mfma_f32_16x16x32_bf16 a[220:223], a[40:43], a[120:123], a[220:223]\n" \
"ds_read_b128 a[32:35], v100 offset:8192\n" \
"v_mfma_f32_16x16x32_bf16 a[220:223], a[44:47], a[124:127], a[220:223]\n" \
"global_load_dword v238, v234, s[64:65]\n" \
"v_mfma_f32_16x16x32_bf16 a[216:219], a[40:43], a[112:115], a[216:219]\n" \
"ds_read_b64_tr_b16 a[88:89], v75 offset:512\n" \
"v_mfma_f32_16x16x32_bf16 a[216:219], a[44:47], a[116:119], a[216:219]\n" \
"v_mfma_f32_16x16x32_bf16 a[212:215], a[40:43], a[104:107], a[212:215]\n" \
"ds_read_b128 a[36:39], v101 offset:8192\n" \
"v_mfma_f32_16x16x32_bf16 a[212:215], a[44:47], a[108:111], a[212:215]\n" \
"v_mfma_f32_16x16x32_bf16 a[208:211], a[40:43], a[96:99], a[208:211]\n" \
"ds_read_b64_tr_b16 a[90:91], v75 offset:4608\n" \
"v_mfma_f32_16x16x32_bf16 a[208:211], a[44:47], a[100:103], a[208:211]\n" \
"v_mfma_f32_16x16x32_bf16 a[224:227], a[48:51], a[96:99], a[224:227]\n" \
"ds_read_b128 a[40:43], v100 offset:10240\n" \
"v_mfma_f32_16x16x32_bf16 a[224:227], a[52:55], a[100:103], a[224:227]\n" \
"v_mfma_f32_16x16x32_bf16 a[228:231], a[48:51], a[104:107], a[228:231]\n" \
"ds_read_b64_tr_b16 a[92:93], v75 offset:16896\n" \
"v_mfma_f32_16x16x32_bf16 a[228:231], a[52:55], a[108:111], a[228:231]\n" \
"v_mfma_f32_16x16x32_bf16 a[232:235], a[48:51], a[112:115], a[232:235]\n" \
"ds_read_b128 a[44:47], v101 offset:10240\n" \
"v_mfma_f32_16x16x32_bf16 a[232:235], a[52:55], a[116:119], a[232:235]\n" \
"v_mfma_f32_16x16x32_bf16 a[236:239], a[48:51], a[120:123], a[236:239]\n" \
"ds_read_b64_tr_b16 a[94:95], v75 offset:20992\n" \
"v_mfma_f32_16x16x32_bf16 a[236:239], a[52:55], a[124:127], a[236:239]\n" \
"v_mfma_f32_16x16x32_bf16 a[252:255], a[56:59], a[120:123], a[252:255]\n" \
"ds_read_b128 a[48:51], v100 offset:12288\n" \
"v_mfma_f32_16x16x32_bf16 a[252:255], a[60:63], a[124:127], a[252:255]\n" \
"v_mfma_f32_16x16x32_bf16 a[248:251], a[56:59], a[112:115], a[248:251]\n" \
"v_mfma_f32_16x16x32_bf16 a[248:251], a[60:63], a[116:119], a[248:251]\n" \
"v_mfma_f32_16x16x32_bf16 a[244:247], a[56:59], a[104:107], a[244:247]\n" \
"ds_read_b128 a[52:55], v101 offset:12288\n" \
"v_mfma_f32_16x16x32_bf16 a[244:247], a[60:63], a[108:111], a[244:247]\n" \
"v_mfma_f32_16x16x32_bf16 a[240:243], a[56:59], a[96:99], a[240:243]\n" \
"v_mfma_f32_16x16x32_bf16 a[240:243], a[60:63], a[100:103], a[240:243]\n" \
"s_waitcnt lgkmcnt(1)\n" \
"v_mfma_f32_16x16x32_bf16 v[0:3], a[64:67], a[0:3], v[0:3]\n" \
"ds_read_b128 a[56:59], v100 offset:14336\n" \
"v_mfma_f32_16x16x32_bf16 v[0:3], a[68:71], a[4:7], v[0:3]\n" \
"v_mfma_f32_16x16x32_bf16 v[4:7], a[72:75], a[0:3], v[4:7]\n" \
"s_add_u32 m0, s74, 32768\n" \
"ds_read_b128 a[60:63], v101 offset:14336\n" \
"v_mfma_f32_16x16x32_bf16 v[4:7], a[76:79], a[4:7], v[4:7]\n" \
"buffer_load_dwordx4 v68, s[40:43], s83 offen offset:0 lds\n" \
"v_mfma_f32_16x16x32_bf16 v[8:11], a[80:83], a[0:3], v[8:11]\n" \
"ds_read_b64_tr_b16 a[96:97], v74 offset:1024\n" \
"v_mfma_f32_16x16x32_bf16 v[8:11], a[84:87], a[4:7], v[8:11]\n" \
"v_mfma_f32_16x16x32_bf16 v[12:15], a[88:91], a[0:3], v[12:15]\n" \
"ds_read_b64_tr_b16 a[98:99], v74 offset:5120\n" \
"v_mfma_f32_16x16x32_bf16 v[12:15], a[92:95], a[4:7], v[12:15]\n" \
"buffer_load_dwordx4 v69, s[44:47], s83 offen offset:1024 lds\n" \
"v_mfma_f32_16x16x32_bf16 v[28:31], a[88:91], a[8:11], v[28:31]\n" \
"ds_read_b64_tr_b16 a[100:101], v74 offset:17408\n" \
"v_mfma_f32_16x16x32_bf16 v[28:31], a[92:95], a[12:15], v[28:31]\n" \
"v_mfma_f32_16x16x32_bf16 v[24:27], a[80:83], a[8:11], v[24:27]\n" \
"ds_read_b64_tr_b16 a[102:103], v74 offset:21504\n" \
"v_mfma_f32_16x16x32_bf16 v[24:27], a[84:87], a[12:15], v[24:27]\n" \
"buffer_load_dwordx4 v70, s[40:43], s83 offen offset:2048 lds\n" \
"v_mfma_f32_16x16x32_bf16 v[20:23], a[72:75], a[8:11], v[20:23]\n" \
"ds_read_b64_tr_b16 a[104:105], v75 offset:1024\n" \
"v_mfma_f32_16x16x32_bf16 v[20:23], a[76:79], a[12:15], v[20:23]\n" \
"v_mfma_f32_16x16x32_bf16 v[16:19], a[64:67], a[8:11], v[16:19]\n" \
"ds_read_b64_tr_b16 a[106:107], v75 offset:5120\n" \
"v_mfma_f32_16x16x32_bf16 v[16:19], a[68:71], a[12:15], v[16:19]\n" \
"buffer_load_dwordx4 v71, s[44:47], s83 offen offset:3072 lds\n" \
"v_mfma_f32_16x16x32_bf16 v[32:35], a[64:67], a[16:19], v[32:35]\n" \
"ds_read_b64_tr_b16 a[108:109], v75 offset:17408\n" \
"v_mfma_f32_16x16x32_bf16 v[32:35], a[68:71], a[20:23], v[32:35]\n" \
"s_xor_b32 s83, s83, 0x180000\n" \
"v_mfma_f32_16x16x32_bf16 v[36:39], a[72:75], a[16:19], v[36:39]\n" \
"s_add_u32 m0, s74, 36864\n" \
"ds_read_b64_tr_b16 a[110:111], v75 offset:21504\n" \
"v_mfma_f32_16x16x32_bf16 v[36:39], a[76:79], a[20:23], v[36:39]\n" \
"buffer_load_dwordx4 v68, s[40:43], s84 offen offset:0 lds\n" \
"v_mfma_f32_16x16x32_bf16 v[40:43], a[80:83], a[16:19], v[40:43]\n" \
"ds_read_b64_tr_b16 a[112:113], v74 offset:1536\n" \
"v_mfma_f32_16x16x32_bf16 v[40:43], a[84:87], a[20:23], v[40:43]\n" \
"v_mfma_f32_16x16x32_bf16 v[44:47], a[88:91], a[16:19], v[44:47]\n" \
"ds_read_b64_tr_b16 a[114:115], v74 offset:5632\n" \
"v_mfma_f32_16x16x32_bf16 v[44:47], a[92:95], a[20:23], v[44:47]\n" \
"buffer_load_dwordx4 v69, s[44:47], s84 offen offset:1024 lds\n" \
"v_mfma_f32_16x16x32_bf16 v[60:63], a[88:91], a[24:27], v[60:63]\n" \
"ds_read_b64_tr_b16 a[116:117], v74 offset:17920\n" \
"v_mfma_f32_16x16x32_bf16 v[60:63], a[92:95], a[28:31], v[60:63]\n" \
"v_mfma_f32_16x16x32_bf16 v[56:59], a[80:83], a[24:27], v[56:59]\n" \
"ds_read_b64_tr_b16 a[118:119], v74 offset:22016\n" \
"v_mfma_f32_16x16x32_bf16 v[56:59], a[84:87], a[28:31], v[56:59]\n" \
"buffer_load_dwordx4 v70, s[40:43], s84 offen offset:2048 lds\n" \
"v_mfma_f32_16x16x32_bf16 v[52:55], a[72:75], a[24:27], v[52:55]\n" \
"ds_read_b64_tr_b16 a[120:121], v75 offset:1536\n" \
"v_mfma_f32_16x16x32_bf16 v[52:55], a[76:79], a[28:31], v[52:55]\n" \
"v_mfma_f32_16x16x32_bf16 v[48:51], a[64:67], a[24:27], v[48:51]\n" \
"ds_read_b64_tr_b16 a[122:123], v75 offset:5632\n" \
"v_mfma_f32_16x16x32_bf16 v[48:51], a[68:71], a[28:31], v[48:51]\n" \
"buffer_load_dwordx4 v71, s[44:47], s84 offen offset:3072 lds\n" \
"v_mfma_f32_16x16x32_bf16 v[144:147], a[64:67], a[32:35], v[144:147]\n" \
"ds_read_b64_tr_b16 a[124:125], v75 offset:17920\n" \
"v_mfma_f32_16x16x32_bf16 v[144:147], a[68:71], a[36:39], v[144:147]\n" \
"v_mfma_f32_16x16x32_bf16 v[148:151], a[72:75], a[32:35], v[148:151]\n" \
"s_xor_b32 s84, s84, 0x180000\n" \
"ds_read_b64_tr_b16 a[126:127], v75 offset:22016\n" \
"v_mfma_f32_16x16x32_bf16 v[148:151], a[76:79], a[36:39], v[148:151]\n" \
"v_mfma_f32_16x16x32_bf16 v[152:155], a[80:83], a[32:35], v[152:155]\n" \
"v_mfma_f32_16x16x32_bf16 v[152:155], a[84:87], a[36:39], v[152:155]\n" \
"v_mfma_f32_16x16x32_bf16 v[156:159], a[88:91], a[32:35], v[156:159]\n" \
"v_mfma_f32_16x16x32_bf16 v[156:159], a[92:95], a[36:39], v[156:159]\n" \
"v_mfma_f32_16x16x32_bf16 v[188:191], a[88:91], a[40:43], v[188:191]\n" \
"v_mfma_f32_16x16x32_bf16 v[188:191], a[92:95], a[44:47], v[188:191]\n" \
"v_mfma_f32_16x16x32_bf16 v[184:187], a[80:83], a[40:43], v[184:187]\n" \
"v_mfma_f32_16x16x32_bf16 v[184:187], a[84:87], a[44:47], v[184:187]\n" \
"v_mfma_f32_16x16x32_bf16 v[180:183], a[72:75], a[40:43], v[180:183]\n" \
"v_mfma_f32_16x16x32_bf16 v[180:183], a[76:79], a[44:47], v[180:183]\n" \
"v_mfma_f32_16x16x32_bf16 v[176:179], a[64:67], a[40:43], v[176:179]\n" \
"v_mfma_f32_16x16x32_bf16 v[176:179], a[68:71], a[44:47], v[176:179]\n" \
"s_waitcnt lgkmcnt(0)\n" \
"v_mfma_f32_16x16x32_bf16 v[192:195], a[64:67], a[48:51], v[192:195]\n" \
"v_mfma_f32_16x16x32_bf16 v[192:195], a[68:71], a[52:55], v[192:195]\n" \
"v_mfma_f32_16x16x32_bf16 v[196:199], a[72:75], a[48:51], v[196:199]\n" \
"v_mfma_f32_16x16x32_bf16 v[196:199], a[76:79], a[52:55], v[196:199]\n" \
"v_mfma_f32_16x16x32_bf16 v[200:203], a[80:83], a[48:51], v[200:203]\n" \
"v_mfma_f32_16x16x32_bf16 v[200:203], a[84:87], a[52:55], v[200:203]\n" \
"v_mfma_f32_16x16x32_bf16 v[204:207], a[88:91], a[48:51], v[204:207]\n" \
"v_mfma_f32_16x16x32_bf16 v[204:207], a[92:95], a[52:55], v[204:207]\n" \
"v_mfma_f32_16x16x32_bf16 v[220:223], a[88:91], a[56:59], v[220:223]\n" \
"v_mfma_f32_16x16x32_bf16 v[220:223], a[92:95], a[60:63], v[220:223]\n" \
"v_mfma_f32_16x16x32_bf16 v[216:219], a[80:83], a[56:59], v[216:219]\n" \
"v_mfma_f32_16x16x32_bf16 v[216:219], a[84:87], a[60:63], v[216:219]\n" \
"v_mfma_f32_16x16x32_bf16 v[212:215], a[72:75], a[56:59], v[212:215]\n" \
"v_mfma_f32_16x16x32_bf16 v[212:215], a[76:79], a[60:63], v[212:215]\n" \
"v_mfma_f32_16x16x32_bf16 v[208:211], a[64:67], a[56:59], v[208:211]\n" \
"v_mfma_f32_16x16x32_bf16 v[208:211], a[68:71], a[60:63], v[208:211]\n" \
"s_waitcnt vmcnt(11) lgkmcnt(0)\n" \
"s_barrier\n" \
"v_mfma_f32_16x16x32_bf16 a[128:131], a[0:3], a[96:99], a[128:131]\n" \
"s_add_u32 m0, s74, 0\n" \
"ds_read_b64_tr_b16 a[64:65], v98 offset:0\n" \
"v_mfma_f32_16x16x32_bf16 a[128:131], a[4:7], a[100:103], a[128:131]\n" \
"buffer_load_dwordx4 v64, s[36:39], s81 offen offset:0 lds\n" \
"v_mfma_f32_16x16x32_bf16 a[132:135], a[0:3], a[104:107], a[132:135]\n" \
"ds_read_b64_tr_b16 a[66:67], v98 offset:4096\n" \
"v_mfma_f32_16x16x32_bf16 a[132:135], a[4:7], a[108:111], a[132:135]\n" \
"v_mfma_f32_16x16x32_bf16 a[136:139], a[0:3], a[112:115], a[136:139]\n" \
"ds_read_b64_tr_b16 a[68:69], v98 offset:16384\n" \
"v_mfma_f32_16x16x32_bf16 a[136:139], a[4:7], a[116:119], a[136:139]\n" \
"buffer_load_dwordx4 v65, s[36:39], s81 offen offset:1024 lds\n" \
"v_mfma_f32_16x16x32_bf16 a[140:143], a[0:3], a[120:123], a[140:143]\n" \
"ds_read_b64_tr_b16 a[70:71], v98 offset:20480\n" \
"v_mfma_f32_16x16x32_bf16 a[140:143], a[4:7], a[124:127], a[140:143]\n" \
"v_mfma_f32_16x16x32_bf16 a[156:159], a[8:11], a[120:123], a[156:159]\n" \
"ds_read_b128 a[0:3], v96 offset:32768\n" \
"v_mfma_f32_16x16x32_bf16 a[156:159], a[12:15], a[124:127], a[156:159]\n" \
"buffer_load_dwordx4 v66, s[36:39], s81 offen offset:2048 lds\n" \
"v_mfma_f32_16x16x32_bf16 a[152:155], a[8:11], a[112:115], a[152:155]\n" \
"ds_read_b64_tr_b16 a[72:73], v99 offset:0\n" \
"v_mfma_f32_16x16x32_bf16 a[152:155], a[12:15], a[116:119], a[152:155]\n" \
"v_mfma_f32_16x16x32_bf16 a[148:151], a[8:11], a[104:107], a[148:151]\n" \
"Lpg1138_%=:\n" \
"Lst0x1_%=:\n" \
"s_cbranch_execz Lpg3347_%=\n" \
"ds_read_b128 a[4:7], v97 offset:32768\n" \
"v_mfma_f32_16x16x32_bf16 a[148:151], a[12:15], a[108:111], a[148:151]\n" \
"buffer_load_dwordx4 v67, s[36:39], s81 offen offset:3072 lds\n" \
"v_mfma_f32_16x16x32_bf16 a[144:147], a[8:11], a[96:99], a[144:147]\n" \
"ds_read_b64_tr_b16 a[74:75], v99 offset:4096\n" \
"v_mfma_f32_16x16x32_bf16 a[144:147], a[12:15], a[100:103], a[144:147]\n" \
"s_xor_b32 s81, s81, 0x180\n" \
"v_mfma_f32_16x16x32_bf16 a[160:163], a[16:19], a[96:99], a[160:163]\n" \
"s_add_u32 m0, s74, 4096\n" \
"ds_read_b128 a[8:11], v96 offset:34816\n" \
"v_mfma_f32_16x16x32_bf16 a[160:163], a[20:23], a[100:103], a[160:163]\n" \
"buffer_load_dwordx4 v64, s[36:39], s82 offen offset:0 lds\n" \
"v_mfma_f32_16x16x32_bf16 a[164:167], a[16:19], a[104:107], a[164:167]\n" \
"ds_read_b64_tr_b16 a[76:77], v99 offset:16384\n" \
"v_mfma_f32_16x16x32_bf16 a[164:167], a[20:23], a[108:111], a[164:167]\n" \
"v_mfma_f32_16x16x32_bf16 a[168:171], a[16:19], a[112:115], a[168:171]\n" \
"ds_read_b128 a[12:15], v97 offset:34816\n" \
"v_mfma_f32_16x16x32_bf16 a[168:171], a[20:23], a[116:119], a[168:171]\n" \
"buffer_load_dwordx4 v65, s[36:39], s82 offen offset:1024 lds\n" \
"v_mfma_f32_16x16x32_bf16 a[172:175], a[16:19], a[120:123], a[172:175]\n" \
"ds_read_b64_tr_b16 a[78:79], v99 offset:20480\n" \
"v_mfma_f32_16x16x32_bf16 a[172:175], a[20:23], a[124:127], a[172:175]\n" \
"v_mfma_f32_16x16x32_bf16 a[188:191], a[24:27], a[120:123], a[188:191]\n" \
"ds_read_b128 a[16:19], v96 offset:36864\n" \
"v_mfma_f32_16x16x32_bf16 a[188:191], a[28:31], a[124:127], a[188:191]\n" \
"buffer_load_dwordx4 v66, s[36:39], s82 offen offset:2048 lds\n" \
"v_mfma_f32_16x16x32_bf16 a[184:187], a[24:27], a[112:115], a[184:187]\n" \
"ds_read_b64_tr_b16 a[80:81], v98 offset:512\n" \
"v_mfma_f32_16x16x32_bf16 a[184:187], a[28:31], a[116:119], a[184:187]\n" \
"v_mfma_f32_16x16x32_bf16 a[180:183], a[24:27], a[104:107], a[180:183]\n" \
"ds_read_b128 a[20:23], v97 offset:36864\n" \
"v_mfma_f32_16x16x32_bf16 a[180:183], a[28:31], a[108:111], a[180:183]\n" \
"buffer_load_dwordx4 v67, s[36:39], s82 offen offset:3072 lds\n" \
"v_mfma_f32_16x16x32_bf16 a[176:179], a[24:27], a[96:99], a[176:179]\n" \
"s_mov_b64 s[64:65], s[8:9]\n" \
"s_add_u32 s68, s66, 0x200000\n" \
"ds_read_b64_tr_b16 a[82:83], v98 offset:4608\n" \
"v_mfma_f32_16x16x32_bf16 a[176:179], a[28:31], a[100:103], a[176:179]\n" \
"s_add_u32 s64, s64, s68\n" \
"s_addc_u32 s65, s65, 0\n" \
"v_mfma_f32_16x16x32_bf16 a[192:195], a[32:35], a[96:99], a[192:195]\n" \
"s_xor_b32 s82, s82, 0x180\n" \
"ds_read_b128 a[24:27], v96 offset:38912\n" \
"v_mfma_f32_16x16x32_bf16 a[192:195], a[36:39], a[100:103], a[192:195]\n" \
"global_load_dword v238, v234, s[64:65]\n" \
"v_mfma_f32_16x16x32_bf16 a[196:199], a[32:35], a[104:107], a[196:199]\n" \
"s_mov_b64 s[64:65], s[8:9]\n" \
"s_add_u32 s68, s66, 0x300000\n" \
"ds_read_b64_tr_b16 a[84:85], v98 offset:16896\n" \
"v_mfma_f32_16x16x32_bf16 a[196:199], a[36:39], a[108:111], a[196:199]\n" \
"s_add_u32 s64, s64, s68\n" \
"s_addc_u32 s65, s65, 0\n" \
"v_mfma_f32_16x16x32_bf16 a[200:203], a[32:35], a[112:115], a[200:203]\n" \
"ds_read_b128 a[28:31], v97 offset:38912\n" \
"v_mfma_f32_16x16x32_bf16 a[200:203], a[36:39], a[116:119], a[200:203]\n" \
"global_load_dword v238, v234, s[64:65]\n" \
"v_mfma_f32_16x16x32_bf16 a[204:207], a[32:35], a[120:123], a[204:207]\n" \
"s_mov_b64 s[64:65], s[12:13]\n" \
"s_add_u32 s68, s67, 0x0\n" \
"ds_read_b64_tr_b16 a[86:87], v98 offset:20992\n" \
"v_mfma_f32_16x16x32_bf16 a[204:207], a[36:39], a[124:127], a[204:207]\n" \
"s_add_u32 s64, s64, s68\n" \
"s_addc_u32 s65, s65, 0\n" \
"v_mfma_f32_16x16x32_bf16 a[220:223], a[40:43], a[120:123], a[220:223]\n" \
"ds_read_b128 a[32:35], v96 offset:40960\n" \
"v_mfma_f32_16x16x32_bf16 a[220:223], a[44:47], a[124:127], a[220:223]\n" \
"global_load_dword v238, v234, s[64:65]\n" \
"v_mfma_f32_16x16x32_bf16 a[216:219], a[40:43], a[112:115], a[216:219]\n" \
"ds_read_b64_tr_b16 a[88:89], v99 offset:512\n" \
"v_mfma_f32_16x16x32_bf16 a[216:219], a[44:47], a[116:119], a[216:219]\n" \
"v_mfma_f32_16x16x32_bf16 a[212:215], a[40:43], a[104:107], a[212:215]\n" \
"ds_read_b128 a[36:39], v97 offset:40960\n" \
"v_mfma_f32_16x16x32_bf16 a[212:215], a[44:47], a[108:111], a[212:215]\n" \
"v_mfma_f32_16x16x32_bf16 a[208:211], a[40:43], a[96:99], a[208:211]\n" \
"ds_read_b64_tr_b16 a[90:91], v99 offset:4608\n" \
"v_mfma_f32_16x16x32_bf16 a[208:211], a[44:47], a[100:103], a[208:211]\n" \
"v_mfma_f32_16x16x32_bf16 a[224:227], a[48:51], a[96:99], a[224:227]\n" \
"ds_read_b128 a[40:43], v96 offset:43008\n" \
"v_mfma_f32_16x16x32_bf16 a[224:227], a[52:55], a[100:103], a[224:227]\n" \
"v_mfma_f32_16x16x32_bf16 a[228:231], a[48:51], a[104:107], a[228:231]\n" \
"ds_read_b64_tr_b16 a[92:93], v99 offset:16896\n" \
"v_mfma_f32_16x16x32_bf16 a[228:231], a[52:55], a[108:111], a[228:231]\n" \
"v_mfma_f32_16x16x32_bf16 a[232:235], a[48:51], a[112:115], a[232:235]\n" \
"ds_read_b128 a[44:47], v97 offset:43008\n" \
"v_mfma_f32_16x16x32_bf16 a[232:235], a[52:55], a[116:119], a[232:235]\n" \
"v_mfma_f32_16x16x32_bf16 a[236:239], a[48:51], a[120:123], a[236:239]\n" \
"ds_read_b64_tr_b16 a[94:95], v99 offset:20992\n" \
"v_mfma_f32_16x16x32_bf16 a[236:239], a[52:55], a[124:127], a[236:239]\n" \
"v_mfma_f32_16x16x32_bf16 a[252:255], a[56:59], a[120:123], a[252:255]\n" \
"ds_read_b128 a[48:51], v96 offset:45056\n" \
"v_mfma_f32_16x16x32_bf16 a[252:255], a[60:63], a[124:127], a[252:255]\n" \
"v_mfma_f32_16x16x32_bf16 a[248:251], a[56:59], a[112:115], a[248:251]\n" \
"v_mfma_f32_16x16x32_bf16 a[248:251], a[60:63], a[116:119], a[248:251]\n" \
"v_mfma_f32_16x16x32_bf16 a[244:247], a[56:59], a[104:107], a[244:247]\n" \
"ds_read_b128 a[52:55], v97 offset:45056\n" \
"v_mfma_f32_16x16x32_bf16 a[244:247], a[60:63], a[108:111], a[244:247]\n" \
"v_mfma_f32_16x16x32_bf16 a[240:243], a[56:59], a[96:99], a[240:243]\n" \
"v_mfma_f32_16x16x32_bf16 a[240:243], a[60:63], a[100:103], a[240:243]\n" \
"s_waitcnt lgkmcnt(1)\n" \
"v_mfma_f32_16x16x32_bf16 v[0:3], a[64:67], a[0:3], v[0:3]\n" \
"ds_read_b128 a[56:59], v96 offset:47104\n" \
"v_mfma_f32_16x16x32_bf16 v[0:3], a[68:71], a[4:7], v[0:3]\n" \
"v_mfma_f32_16x16x32_bf16 v[4:7], a[72:75], a[0:3], v[4:7]\n" \
"s_add_u32 m0, s74, 131072\n" \
"ds_read_b128 a[60:63], v97 offset:47104\n" \
"v_mfma_f32_16x16x32_bf16 v[4:7], a[76:79], a[4:7], v[4:7]\n" \
"buffer_load_dwordx4 v68, s[40:43], s83 offen offset:0 lds\n" \
"v_mfma_f32_16x16x32_bf16 v[8:11], a[80:83], a[0:3], v[8:11]\n" \
"ds_read_b64_tr_b16 a[96:97], v98 offset:1024\n" \
"v_mfma_f32_16x16x32_bf16 v[8:11], a[84:87], a[4:7], v[8:11]\n" \
"v_mfma_f32_16x16x32_bf16 v[12:15], a[88:91], a[0:3], v[12:15]\n" \
"ds_read_b64_tr_b16 a[98:99], v98 offset:5120\n" \
"v_mfma_f32_16x16x32_bf16 v[12:15], a[92:95], a[4:7], v[12:15]\n" \
"buffer_load_dwordx4 v69, s[44:47], s83 offen offset:1024 lds\n" \
"v_mfma_f32_16x16x32_bf16 v[28:31], a[88:91], a[8:11], v[28:31]\n" \
"ds_read_b64_tr_b16 a[100:101], v98 offset:17408\n" \
"v_mfma_f32_16x16x32_bf16 v[28:31], a[92:95], a[12:15], v[28:31]\n" \
"v_mfma_f32_16x16x32_bf16 v[24:27], a[80:83], a[8:11], v[24:27]\n" \
"ds_read_b64_tr_b16 a[102:103], v98 offset:21504\n" \
"v_mfma_f32_16x16x32_bf16 v[24:27], a[84:87], a[12:15], v[24:27]\n" \
"buffer_load_dwordx4 v70, s[40:43], s83 offen offset:2048 lds\n" \
"v_mfma_f32_16x16x32_bf16 v[20:23], a[72:75], a[8:11], v[20:23]\n" \
"ds_read_b64_tr_b16 a[104:105], v99 offset:1024\n" \
"v_mfma_f32_16x16x32_bf16 v[20:23], a[76:79], a[12:15], v[20:23]\n" \
"v_mfma_f32_16x16x32_bf16 v[16:19], a[64:67], a[8:11], v[16:19]\n" \
"ds_read_b64_tr_b16 a[106:107], v99 offset:5120\n" \
"v_mfma_f32_16x16x32_bf16 v[16:19], a[68:71], a[12:15], v[16:19]\n" \
"buffer_load_dwordx4 v71, s[44:47], s83 offen offset:3072 lds\n" \
"v_mfma_f32_16x16x32_bf16 v[32:35], a[64:67], a[16:19], v[32:35]\n" \
"ds_read_b64_tr_b16 a[108:109], v99 offset:17408\n" \
"v_mfma_f32_16x16x32_bf16 v[32:35], a[68:71], a[20:23], v[32:35]\n" \
"s_xor_b32 s83, s83, 0x80000\n" \
"v_mfma_f32_16x16x32_bf16 v[36:39], a[72:75], a[16:19], v[36:39]\n" \
"s_add_u32 m0, s74, 135168\n" \
"ds_read_b64_tr_b16 a[110:111], v99 offset:21504\n" \
"v_mfma_f32_16x16x32_bf16 v[36:39], a[76:79], a[20:23], v[36:39]\n" \
"buffer_load_dwordx4 v68, s[40:43], s84 offen offset:0 lds\n" \
"v_mfma_f32_16x16x32_bf16 v[40:43], a[80:83], a[16:19], v[40:43]\n" \
"ds_read_b64_tr_b16 a[112:113], v98 offset:1536\n" \
"v_mfma_f32_16x16x32_bf16 v[40:43], a[84:87], a[20:23], v[40:43]\n" \
"v_mfma_f32_16x16x32_bf16 v[44:47], a[88:91], a[16:19], v[44:47]\n" \
"ds_read_b64_tr_b16 a[114:115], v98 offset:5632\n" \
"v_mfma_f32_16x16x32_bf16 v[44:47], a[92:95], a[20:23], v[44:47]\n" \
"buffer_load_dwordx4 v69, s[44:47], s84 offen offset:1024 lds\n" \
"v_mfma_f32_16x16x32_bf16 v[60:63], a[88:91], a[24:27], v[60:63]\n" \
"ds_read_b64_tr_b16 a[116:117], v98 offset:17920\n" \
"v_mfma_f32_16x16x32_bf16 v[60:63], a[92:95], a[28:31], v[60:63]\n" \
"v_mfma_f32_16x16x32_bf16 v[56:59], a[80:83], a[24:27], v[56:59]\n" \
"ds_read_b64_tr_b16 a[118:119], v98 offset:22016\n" \
"v_mfma_f32_16x16x32_bf16 v[56:59], a[84:87], a[28:31], v[56:59]\n" \
"buffer_load_dwordx4 v70, s[40:43], s84 offen offset:2048 lds\n" \
"v_mfma_f32_16x16x32_bf16 v[52:55], a[72:75], a[24:27], v[52:55]\n" \
"ds_read_b64_tr_b16 a[120:121], v99 offset:1536\n" \
"v_mfma_f32_16x16x32_bf16 v[52:55], a[76:79], a[28:31], v[52:55]\n" \
"v_mfma_f32_16x16x32_bf16 v[48:51], a[64:67], a[24:27], v[48:51]\n" \
"ds_read_b64_tr_b16 a[122:123], v99 offset:5632\n" \
"v_mfma_f32_16x16x32_bf16 v[48:51], a[68:71], a[28:31], v[48:51]\n" \
"buffer_load_dwordx4 v71, s[44:47], s84 offen offset:3072 lds\n" \
"v_mfma_f32_16x16x32_bf16 v[144:147], a[64:67], a[32:35], v[144:147]\n" \
"ds_read_b64_tr_b16 a[124:125], v99 offset:17920\n" \
"v_mfma_f32_16x16x32_bf16 v[144:147], a[68:71], a[36:39], v[144:147]\n" \
"v_mfma_f32_16x16x32_bf16 v[148:151], a[72:75], a[32:35], v[148:151]\n" \
"s_xor_b32 s84, s84, 0x80000\n" \
"ds_read_b64_tr_b16 a[126:127], v99 offset:22016\n" \
"v_mfma_f32_16x16x32_bf16 v[148:151], a[76:79], a[36:39], v[148:151]\n" \
"v_mfma_f32_16x16x32_bf16 v[152:155], a[80:83], a[32:35], v[152:155]\n" \
"v_mfma_f32_16x16x32_bf16 v[152:155], a[84:87], a[36:39], v[152:155]\n" \
"v_mfma_f32_16x16x32_bf16 v[156:159], a[88:91], a[32:35], v[156:159]\n" \
"v_mfma_f32_16x16x32_bf16 v[156:159], a[92:95], a[36:39], v[156:159]\n" \
"v_mfma_f32_16x16x32_bf16 v[188:191], a[88:91], a[40:43], v[188:191]\n" \
"v_mfma_f32_16x16x32_bf16 v[188:191], a[92:95], a[44:47], v[188:191]\n" \
"v_mfma_f32_16x16x32_bf16 v[184:187], a[80:83], a[40:43], v[184:187]\n" \
"v_mfma_f32_16x16x32_bf16 v[184:187], a[84:87], a[44:47], v[184:187]\n" \
"v_mfma_f32_16x16x32_bf16 v[180:183], a[72:75], a[40:43], v[180:183]\n" \
"v_mfma_f32_16x16x32_bf16 v[180:183], a[76:79], a[44:47], v[180:183]\n" \
"v_mfma_f32_16x16x32_bf16 v[176:179], a[64:67], a[40:43], v[176:179]\n" \
"v_mfma_f32_16x16x32_bf16 v[176:179], a[68:71], a[44:47], v[176:179]\n" \
"s_waitcnt lgkmcnt(0)\n" \
"v_mfma_f32_16x16x32_bf16 v[192:195], a[64:67], a[48:51], v[192:195]\n" \
"v_mfma_f32_16x16x32_bf16 v[192:195], a[68:71], a[52:55], v[192:195]\n" \
"v_mfma_f32_16x16x32_bf16 v[196:199], a[72:75], a[48:51], v[196:199]\n" \
"v_mfma_f32_16x16x32_bf16 v[196:199], a[76:79], a[52:55], v[196:199]\n" \
"v_mfma_f32_16x16x32_bf16 v[200:203], a[80:83], a[48:51], v[200:203]\n" \
"v_mfma_f32_16x16x32_bf16 v[200:203], a[84:87], a[52:55], v[200:203]\n" \
"v_mfma_f32_16x16x32_bf16 v[204:207], a[88:91], a[48:51], v[204:207]\n" \
"v_mfma_f32_16x16x32_bf16 v[204:207], a[92:95], a[52:55], v[204:207]\n" \
"v_mfma_f32_16x16x32_bf16 v[220:223], a[88:91], a[56:59], v[220:223]\n" \
"v_mfma_f32_16x16x32_bf16 v[220:223], a[92:95], a[60:63], v[220:223]\n" \
"v_mfma_f32_16x16x32_bf16 v[216:219], a[80:83], a[56:59], v[216:219]\n" \
"v_mfma_f32_16x16x32_bf16 v[216:219], a[84:87], a[60:63], v[216:219]\n" \
"v_mfma_f32_16x16x32_bf16 v[212:215], a[72:75], a[56:59], v[212:215]\n" \
"v_mfma_f32_16x16x32_bf16 v[212:215], a[76:79], a[60:63], v[212:215]\n" \
"v_mfma_f32_16x16x32_bf16 v[208:211], a[64:67], a[56:59], v[208:211]\n" \
"v_mfma_f32_16x16x32_bf16 v[208:211], a[68:71], a[60:63], v[208:211]\n" \
"s_waitcnt vmcnt(11) lgkmcnt(0)\n" \
"s_barrier\n" \
"v_mfma_f32_16x16x32_bf16 a[128:131], a[0:3], a[96:99], a[128:131]\n" \
"s_add_u32 m0, s74, 65536\n" \
"ds_read_b64_tr_b16 a[64:65], v74 offset:32768\n" \
"v_mfma_f32_16x16x32_bf16 a[128:131], a[4:7], a[100:103], a[128:131]\n" \
"buffer_load_dwordx4 v64, s[36:39], s81 offen offset:0 lds\n" \
"v_mfma_f32_16x16x32_bf16 a[132:135], a[0:3], a[104:107], a[132:135]\n" \
"ds_read_b64_tr_b16 a[66:67], v74 offset:36864\n" \
"v_mfma_f32_16x16x32_bf16 a[132:135], a[4:7], a[108:111], a[132:135]\n" \
"v_mfma_f32_16x16x32_bf16 a[136:139], a[0:3], a[112:115], a[136:139]\n" \
"ds_read_b64_tr_b16 a[68:69], v74 offset:49152\n" \
"v_mfma_f32_16x16x32_bf16 a[136:139], a[4:7], a[116:119], a[136:139]\n" \
"buffer_load_dwordx4 v65, s[36:39], s81 offen offset:1024 lds\n" \
"v_mfma_f32_16x16x32_bf16 a[140:143], a[0:3], a[120:123], a[140:143]\n" \
"ds_read_b64_tr_b16 a[70:71], v74 offset:53248\n" \
"v_mfma_f32_16x16x32_bf16 a[140:143], a[4:7], a[124:127], a[140:143]\n" \
"v_mfma_f32_16x16x32_bf16 a[156:159], a[8:11], a[120:123], a[156:159]\n" \
"ds_read_b128 a[0:3], v72 offset:0\n" \
"v_mfma_f32_16x16x32_bf16 a[156:159], a[12:15], a[124:127], a[156:159]\n" \
"buffer_load_dwordx4 v66, s[36:39], s81 offen offset:2048 lds\n" \
"v_mfma_f32_16x16x32_bf16 a[152:155], a[8:11], a[112:115], a[152:155]\n" \
"ds_read_b64_tr_b16 a[72:73], v75 offset:32768\n" \
"v_mfma_f32_16x16x32_bf16 a[152:155], a[12:15], a[116:119], a[152:155]\n" \
"v_mfma_f32_16x16x32_bf16 a[148:151], a[8:11], a[104:107], a[148:151]\n" \
"ds_read_b128 a[4:7], v73 offset:0\n" \
"v_mfma_f32_16x16x32_bf16 a[148:151], a[12:15], a[108:111], a[148:151]\n" \
"buffer_load_dwordx4 v67, s[36:39], s81 offen offset:3072 lds\n" \
"v_mfma_f32_16x16x32_bf16 a[144:147], a[8:11], a[96:99], a[144:147]\n" \
"ds_read_b64_tr_b16 a[74:75], v75 offset:36864\n" \
"v_mfma_f32_16x16x32_bf16 a[144:147], a[12:15], a[100:103], a[144:147]\n" \
"s_xor_b32 s81, s81, 0x80\n" \
"v_mfma_f32_16x16x32_bf16 a[160:163], a[16:19], a[96:99], a[160:163]\n" \
"s_add_u32 m0, s74, 69632\n" \
"ds_read_b128 a[8:11], v72 offset:2048\n" \
"v_mfma_f32_16x16x32_bf16 a[160:163], a[20:23], a[100:103], a[160:163]\n" \
"buffer_load_dwordx4 v64, s[36:39], s82 offen offset:0 lds\n" \
"v_mfma_f32_16x16x32_bf16 a[164:167], a[16:19], a[104:107], a[164:167]\n" \
"ds_read_b64_tr_b16 a[76:77], v75 offset:49152\n" \
"v_mfma_f32_16x16x32_bf16 a[164:167], a[20:23], a[108:111], a[164:167]\n" \
"v_mfma_f32_16x16x32_bf16 a[168:171], a[16:19], a[112:115], a[168:171]\n" \
"ds_read_b128 a[12:15], v73 offset:2048\n" \
"v_mfma_f32_16x16x32_bf16 a[168:171], a[20:23], a[116:119], a[168:171]\n" \
"buffer_load_dwordx4 v65, s[36:39], s82 offen offset:1024 lds\n" \
"v_mfma_f32_16x16x32_bf16 a[172:175], a[16:19], a[120:123], a[172:175]\n" \
"ds_read_b64_tr_b16 a[78:79], v75 offset:53248\n" \
"v_mfma_f32_16x16x32_bf16 a[172:175], a[20:23], a[124:127], a[172:175]\n" \
"v_mfma_f32_16x16x32_bf16 a[188:191], a[24:27], a[120:123], a[188:191]\n" \
"ds_read_b128 a[16:19], v72 offset:4096\n" \
"v_mfma_f32_16x16x32_bf16 a[188:191], a[28:31], a[124:127], a[188:191]\n" \
"buffer_load_dwordx4 v66, s[36:39], s82 offen offset:2048 lds\n" \
"v_mfma_f32_16x16x32_bf16 a[184:187], a[24:27], a[112:115], a[184:187]\n" \
"ds_read_b64_tr_b16 a[80:81], v74 offset:33280\n" \
"v_mfma_f32_16x16x32_bf16 a[184:187], a[28:31], a[116:119], a[184:187]\n" \
"v_mfma_f32_16x16x32_bf16 a[180:183], a[24:27], a[104:107], a[180:183]\n" \
"ds_read_b128 a[20:23], v73 offset:4096\n" \
"v_mfma_f32_16x16x32_bf16 a[180:183], a[28:31], a[108:111], a[180:183]\n" \
"buffer_load_dwordx4 v67, s[36:39], s82 offen offset:3072 lds\n" \
"v_mfma_f32_16x16x32_bf16 a[176:179], a[24:27], a[96:99], a[176:179]\n" \
"s_mov_b64 s[64:65], s[12:13]\n" \
"s_add_u32 s68, s67, 0x100000\n" \
"ds_read_b64_tr_b16 a[82:83], v74 offset:37376\n" \
"v_mfma_f32_16x16x32_bf16 a[176:179], a[28:31], a[100:103], a[176:179]\n" \
"s_add_u32 s64, s64, s68\n" \
"s_addc_u32 s65, s65, 0\n" \
"v_mfma_f32_16x16x32_bf16 a[192:195], a[32:35], a[96:99], a[192:195]\n" \
"s_xor_b32 s82, s82, 0x80\n" \
"ds_read_b128 a[24:27], v72 offset:6144\n" \
"v_mfma_f32_16x16x32_bf16 a[192:195], a[36:39], a[100:103], a[192:195]\n" \
"global_load_dword v238, v234, s[64:65]\n" \
"v_mfma_f32_16x16x32_bf16 a[196:199], a[32:35], a[104:107], a[196:199]\n" \
"s_mov_b64 s[64:65], s[12:13]\n" \
"s_add_u32 s68, s67, 0x500000\n" \
"ds_read_b64_tr_b16 a[84:85], v74 offset:49664\n" \
"v_mfma_f32_16x16x32_bf16 a[196:199], a[36:39], a[108:111], a[196:199]\n" \
"s_add_u32 s64, s64, s68\n" \
"s_addc_u32 s65, s65, 0\n" \
"v_mfma_f32_16x16x32_bf16 a[200:203], a[32:35], a[112:115], a[200:203]\n" \
"ds_read_b128 a[28:31], v73 offset:6144\n" \
"v_mfma_f32_16x16x32_bf16 a[200:203], a[36:39], a[116:119], a[200:203]\n" \
"global_load_dword v238, v234, s[64:65]\n" \
"v_mfma_f32_16x16x32_bf16 a[204:207], a[32:35], a[120:123], a[204:207]\n" \
"s_mov_b64 s[64:65], s[12:13]\n" \
"s_add_u32 s68, s67, 0x200000\n" \
"ds_read_b64_tr_b16 a[86:87], v74 offset:53760\n" \
"v_mfma_f32_16x16x32_bf16 a[204:207], a[36:39], a[124:127], a[204:207]\n" \
"s_add_u32 s64, s64, s68\n" \
"s_addc_u32 s65, s65, 0\n" \
"v_mfma_f32_16x16x32_bf16 a[220:223], a[40:43], a[120:123], a[220:223]\n" \
"ds_read_b128 a[32:35], v72 offset:8192\n" \
"v_mfma_f32_16x16x32_bf16 a[220:223], a[44:47], a[124:127], a[220:223]\n" \
"global_load_dword v238, v234, s[64:65]\n" \
"v_mfma_f32_16x16x32_bf16 a[216:219], a[40:43], a[112:115], a[216:219]\n" \
"ds_read_b64_tr_b16 a[88:89], v75 offset:33280\n" \
"v_mfma_f32_16x16x32_bf16 a[216:219], a[44:47], a[116:119], a[216:219]\n" \
"v_mfma_f32_16x16x32_bf16 a[212:215], a[40:43], a[104:107], a[212:215]\n" \
"ds_read_b128 a[36:39], v73 offset:8192\n" \
"v_mfma_f32_16x16x32_bf16 a[212:215], a[44:47], a[108:111], a[212:215]\n" \
"v_mfma_f32_16x16x32_bf16 a[208:211], a[40:43], a[96:99], a[208:211]\n" \
"ds_read_b64_tr_b16 a[90:91], v75 offset:37376\n" \
"v_mfma_f32_16x16x32_bf16 a[208:211], a[44:47], a[100:103], a[208:211]\n" \
"v_mfma_f32_16x16x32_bf16 a[224:227], a[48:51], a[96:99], a[224:227]\n" \
"ds_read_b128 a[40:43], v72 offset:10240\n" \
"v_mfma_f32_16x16x32_bf16 a[224:227], a[52:55], a[100:103], a[224:227]\n" \
"v_mfma_f32_16x16x32_bf16 a[228:231], a[48:51], a[104:107], a[228:231]\n" \
"ds_read_b64_tr_b16 a[92:93], v75 offset:49664\n" \
"v_mfma_f32_16x16x32_bf16 a[228:231], a[52:55], a[108:111], a[228:231]\n" \
"v_mfma_f32_16x16x32_bf16 a[232:235], a[48:51], a[112:115], a[232:235]\n" \
"ds_read_b128 a[44:47], v73 offset:10240\n" \
"v_mfma_f32_16x16x32_bf16 a[232:235], a[52:55], a[116:119], a[232:235]\n" \
"v_mfma_f32_16x16x32_bf16 a[236:239], a[48:51], a[120:123], a[236:239]\n" \
"ds_read_b64_tr_b16 a[94:95], v75 offset:53760\n" \
"v_mfma_f32_16x16x32_bf16 a[236:239], a[52:55], a[124:127], a[236:239]\n" \
"v_mfma_f32_16x16x32_bf16 a[252:255], a[56:59], a[120:123], a[252:255]\n" \
"ds_read_b128 a[48:51], v72 offset:12288\n" \
"v_mfma_f32_16x16x32_bf16 a[252:255], a[60:63], a[124:127], a[252:255]\n" \
"v_mfma_f32_16x16x32_bf16 a[248:251], a[56:59], a[112:115], a[248:251]\n" \
"v_mfma_f32_16x16x32_bf16 a[248:251], a[60:63], a[116:119], a[248:251]\n" \
"v_mfma_f32_16x16x32_bf16 a[244:247], a[56:59], a[104:107], a[244:247]\n" \
"ds_read_b128 a[52:55], v73 offset:12288\n" \
"v_mfma_f32_16x16x32_bf16 a[244:247], a[60:63], a[108:111], a[244:247]\n" \
"v_mfma_f32_16x16x32_bf16 a[240:243], a[56:59], a[96:99], a[240:243]\n" \
"v_mfma_f32_16x16x32_bf16 a[240:243], a[60:63], a[100:103], a[240:243]\n" \
"s_waitcnt lgkmcnt(1)\n" \
"v_mfma_f32_16x16x32_bf16 v[0:3], a[64:67], a[0:3], v[0:3]\n" \
"ds_read_b128 a[56:59], v72 offset:14336\n" \
"v_mfma_f32_16x16x32_bf16 v[0:3], a[68:71], a[4:7], v[0:3]\n" \
"v_mfma_f32_16x16x32_bf16 v[4:7], a[72:75], a[0:3], v[4:7]\n" \
"s_add_u32 m0, s74, 98304\n" \
"ds_read_b128 a[60:63], v73 offset:14336\n" \
"v_mfma_f32_16x16x32_bf16 v[4:7], a[76:79], a[4:7], v[4:7]\n" \
"buffer_load_dwordx4 v68, s[40:43], s83 offen offset:0 lds\n" \
"v_mfma_f32_16x16x32_bf16 v[8:11], a[80:83], a[0:3], v[8:11]\n" \
"ds_read_b64_tr_b16 a[96:97], v74 offset:33792\n" \
"v_mfma_f32_16x16x32_bf16 v[8:11], a[84:87], a[4:7], v[8:11]\n" \
"v_mfma_f32_16x16x32_bf16 v[12:15], a[88:91], a[0:3], v[12:15]\n" \
"ds_read_b64_tr_b16 a[98:99], v74 offset:37888\n" \
"v_mfma_f32_16x16x32_bf16 v[12:15], a[92:95], a[4:7], v[12:15]\n" \
"buffer_load_dwordx4 v69, s[44:47], s83 offen offset:1024 lds\n" \
"v_mfma_f32_16x16x32_bf16 v[28:31], a[88:91], a[8:11], v[28:31]\n" \
"ds_read_b64_tr_b16 a[100:101], v74 offset:50176\n" \
"v_mfma_f32_16x16x32_bf16 v[28:31], a[92:95], a[12:15], v[28:31]\n" \
"v_mfma_f32_16x16x32_bf16 v[24:27], a[80:83], a[8:11], v[24:27]\n" \
"ds_read_b64_tr_b16 a[102:103], v74 offset:54272\n" \
"v_mfma_f32_16x16x32_bf16 v[24:27], a[84:87], a[12:15], v[24:27]\n" \
"buffer_load_dwordx4 v70, s[40:43], s83 offen offset:2048 lds\n" \
"v_mfma_f32_16x16x32_bf16 v[20:23], a[72:75], a[8:11], v[20:23]\n" \
"ds_read_b64_tr_b16 a[104:105], v75 offset:33792\n" \
"v_mfma_f32_16x16x32_bf16 v[20:23], a[76:79], a[12:15], v[20:23]\n" \
"v_mfma_f32_16x16x32_bf16 v[16:19], a[64:67], a[8:11], v[16:19]\n" \
"ds_read_b64_tr_b16 a[106:107], v75 offset:37888\n" \
"v_mfma_f32_16x16x32_bf16 v[16:19], a[68:71], a[12:15], v[16:19]\n" \
"buffer_load_dwordx4 v71, s[44:47], s83 offen offset:3072 lds\n" \
"v_mfma_f32_16x16x32_bf16 v[32:35], a[64:67], a[16:19], v[32:35]\n" \
"ds_read_b64_tr_b16 a[108:109], v75 offset:50176\n" \
"v_mfma_f32_16x16x32_bf16 v[32:35], a[68:71], a[20:23], v[32:35]\n" \
"v_mfma_f32_16x16x32_bf16 v[36:39], a[72:75], a[16:19], v[36:39]\n" \
"s_add_u32 m0, s74, 102400\n" \
"ds_read_b64_tr_b16 a[110:111], v75 offset:54272\n" \
"v_mfma_f32_16x16x32_bf16 v[36:39], a[76:79], a[20:23], v[36:39]\n" \
"buffer_load_dwordx4 v68, s[40:43], s84 offen offset:0 lds\n" \
"v_mfma_f32_16x16x32_bf16 v[40:43], a[80:83], a[16:19], v[40:43]\n" \
"ds_read_b64_tr_b16 a[112:113], v74 offset:34304\n" \
"v_mfma_f32_16x16x32_bf16 v[40:43], a[84:87], a[20:23], v[40:43]\n" \
"v_mfma_f32_16x16x32_bf16 v[44:47], a[88:91], a[16:19], v[44:47]\n" \
"ds_read_b64_tr_b16 a[114:115], v74 offset:38400\n" \
"v_mfma_f32_16x16x32_bf16 v[44:47], a[92:95], a[20:23], v[44:47]\n" \
"buffer_load_dwordx4 v69, s[44:47], s84 offen offset:1024 lds\n" \
"v_mfma_f32_16x16x32_bf16 v[60:63], a[88:91], a[24:27], v[60:63]\n" \
"ds_read_b64_tr_b16 a[116:117], v74 offset:50688\n" \
"v_mfma_f32_16x16x32_bf16 v[60:63], a[92:95], a[28:31], v[60:63]\n" \
"v_mfma_f32_16x16x32_bf16 v[56:59], a[80:83], a[24:27], v[56:59]\n" \
"ds_read_b64_tr_b16 a[118:119], v74 offset:54784\n" \
"v_mfma_f32_16x16x32_bf16 v[56:59], a[84:87], a[28:31], v[56:59]\n" \
"buffer_load_dwordx4 v70, s[40:43], s84 offen offset:2048 lds\n" \
"v_mfma_f32_16x16x32_bf16 v[52:55], a[72:75], a[24:27], v[52:55]\n" \
"ds_read_b64_tr_b16 a[120:121], v75 offset:34304\n" \
"v_mfma_f32_16x16x32_bf16 v[52:55], a[76:79], a[28:31], v[52:55]\n" \
"v_mfma_f32_16x16x32_bf16 v[48:51], a[64:67], a[24:27], v[48:51]\n" \
"ds_read_b64_tr_b16 a[122:123], v75 offset:38400\n" \
"v_mfma_f32_16x16x32_bf16 v[48:51], a[68:71], a[28:31], v[48:51]\n" \
"buffer_load_dwordx4 v71, s[44:47], s84 offen offset:3072 lds\n" \
"v_mfma_f32_16x16x32_bf16 v[144:147], a[64:67], a[32:35], v[144:147]\n" \
"ds_read_b64_tr_b16 a[124:125], v75 offset:50688\n" \
"v_mfma_f32_16x16x32_bf16 v[144:147], a[68:71], a[36:39], v[144:147]\n" \
"v_mfma_f32_16x16x32_bf16 v[148:151], a[72:75], a[32:35], v[148:151]\n" \
"ds_read_b64_tr_b16 a[126:127], v75 offset:54784\n" \
"v_mfma_f32_16x16x32_bf16 v[148:151], a[76:79], a[36:39], v[148:151]\n" \
"v_mfma_f32_16x16x32_bf16 v[152:155], a[80:83], a[32:35], v[152:155]\n" \
"v_mfma_f32_16x16x32_bf16 v[152:155], a[84:87], a[36:39], v[152:155]\n" \
"v_mfma_f32_16x16x32_bf16 v[156:159], a[88:91], a[32:35], v[156:159]\n" \
"v_mfma_f32_16x16x32_bf16 v[156:159], a[92:95], a[36:39], v[156:159]\n" \
"v_mfma_f32_16x16x32_bf16 v[188:191], a[88:91], a[40:43], v[188:191]\n" \
"v_mfma_f32_16x16x32_bf16 v[188:191], a[92:95], a[44:47], v[188:191]\n" \
"v_mfma_f32_16x16x32_bf16 v[184:187], a[80:83], a[40:43], v[184:187]\n" \
"v_mfma_f32_16x16x32_bf16 v[184:187], a[84:87], a[44:47], v[184:187]\n" \
"v_mfma_f32_16x16x32_bf16 v[180:183], a[72:75], a[40:43], v[180:183]\n" \
"v_mfma_f32_16x16x32_bf16 v[180:183], a[76:79], a[44:47], v[180:183]\n" \
"v_mfma_f32_16x16x32_bf16 v[176:179], a[64:67], a[40:43], v[176:179]\n" \
"v_mfma_f32_16x16x32_bf16 v[176:179], a[68:71], a[44:47], v[176:179]\n" \
"s_waitcnt lgkmcnt(0)\n" \
"v_mfma_f32_16x16x32_bf16 v[192:195], a[64:67], a[48:51], v[192:195]\n" \
"v_mfma_f32_16x16x32_bf16 v[192:195], a[68:71], a[52:55], v[192:195]\n" \
"v_mfma_f32_16x16x32_bf16 v[196:199], a[72:75], a[48:51], v[196:199]\n" \
"v_mfma_f32_16x16x32_bf16 v[196:199], a[76:79], a[52:55], v[196:199]\n" \
"v_mfma_f32_16x16x32_bf16 v[200:203], a[80:83], a[48:51], v[200:203]\n" \
"v_mfma_f32_16x16x32_bf16 v[200:203], a[84:87], a[52:55], v[200:203]\n" \
"v_mfma_f32_16x16x32_bf16 v[204:207], a[88:91], a[48:51], v[204:207]\n" \
"v_mfma_f32_16x16x32_bf16 v[204:207], a[92:95], a[52:55], v[204:207]\n" \
"v_mfma_f32_16x16x32_bf16 v[220:223], a[88:91], a[56:59], v[220:223]\n" \
"v_mfma_f32_16x16x32_bf16 v[220:223], a[92:95], a[60:63], v[220:223]\n" \
"v_mfma_f32_16x16x32_bf16 v[216:219], a[80:83], a[56:59], v[216:219]\n" \
"v_mfma_f32_16x16x32_bf16 v[216:219], a[84:87], a[60:63], v[216:219]\n" \
"v_mfma_f32_16x16x32_bf16 v[212:215], a[72:75], a[56:59], v[212:215]\n" \
"v_mfma_f32_16x16x32_bf16 v[212:215], a[76:79], a[60:63], v[212:215]\n" \
"v_mfma_f32_16x16x32_bf16 v[208:211], a[64:67], a[56:59], v[208:211]\n" \
"v_mfma_f32_16x16x32_bf16 v[208:211], a[68:71], a[60:63], v[208:211]\n" \
"s_waitcnt vmcnt(11) lgkmcnt(0)\n" \
"s_barrier\n" \
"v_mfma_f32_16x16x32_bf16 a[128:131], a[0:3], a[96:99], a[128:131]\n" \
"s_add_u32 m0, s74, 32768\n" \
"ds_read_b64_tr_b16 a[64:65], v102 offset:0\n" \
"v_mfma_f32_16x16x32_bf16 a[128:131], a[4:7], a[100:103], a[128:131]\n" \
"buffer_load_dwordx4 v64, s[36:39], s81 offen offset:0 lds\n" \
"v_mfma_f32_16x16x32_bf16 a[132:135], a[0:3], a[104:107], a[132:135]\n" \
"ds_read_b64_tr_b16 a[66:67], v102 offset:4096\n" \
"v_mfma_f32_16x16x32_bf16 a[132:135], a[4:7], a[108:111], a[132:135]\n" \
"v_mfma_f32_16x16x32_bf16 a[136:139], a[0:3], a[112:115], a[136:139]\n" \
"ds_read_b64_tr_b16 a[68:69], v102 offset:16384\n" \
"v_mfma_f32_16x16x32_bf16 a[136:139], a[4:7], a[116:119], a[136:139]\n" \
"buffer_load_dwordx4 v65, s[36:39], s81 offen offset:1024 lds\n" \
"v_mfma_f32_16x16x32_bf16 a[140:143], a[0:3], a[120:123], a[140:143]\n" \
"ds_read_b64_tr_b16 a[70:71], v102 offset:20480\n" \
"v_mfma_f32_16x16x32_bf16 a[140:143], a[4:7], a[124:127], a[140:143]\n" \
"v_mfma_f32_16x16x32_bf16 a[156:159], a[8:11], a[120:123], a[156:159]\n" \
"ds_read_b128 a[0:3], v96 offset:0\n" \
"v_mfma_f32_16x16x32_bf16 a[156:159], a[12:15], a[124:127], a[156:159]\n" \
"buffer_load_dwordx4 v66, s[36:39], s81 offen offset:2048 lds\n" \
"v_mfma_f32_16x16x32_bf16 a[152:155], a[8:11], a[112:115], a[152:155]\n" \
"ds_read_b64_tr_b16 a[72:73], v103 offset:0\n" \
"v_mfma_f32_16x16x32_bf16 a[152:155], a[12:15], a[116:119], a[152:155]\n" \
"v_mfma_f32_16x16x32_bf16 a[148:151], a[8:11], a[104:107], a[148:151]\n" \
"ds_read_b128 a[4:7], v97 offset:0\n" \
"v_mfma_f32_16x16x32_bf16 a[148:151], a[12:15], a[108:111], a[148:151]\n" \
"buffer_load_dwordx4 v67, s[36:39], s81 offen offset:3072 lds\n" \
"v_mfma_f32_16x16x32_bf16 a[144:147], a[8:11], a[96:99], a[144:147]\n" \
"ds_read_b64_tr_b16 a[74:75], v103 offset:4096\n" \
"v_mfma_f32_16x16x32_bf16 a[144:147], a[12:15], a[100:103], a[144:147]\n" \
"v_mfma_f32_16x16x32_bf16 a[160:163], a[16:19], a[96:99], a[160:163]\n" \
"s_add_u32 m0, s74, 36864\n" \
"ds_read_b128 a[8:11], v96 offset:2048\n" \
"v_mfma_f32_16x16x32_bf16 a[160:163], a[20:23], a[100:103], a[160:163]\n" \
"buffer_load_dwordx4 v64, s[36:39], s82 offen offset:0 lds\n" \
"v_mfma_f32_16x16x32_bf16 a[164:167], a[16:19], a[104:107], a[164:167]\n" \
"ds_read_b64_tr_b16 a[76:77], v103 offset:16384\n" \
"v_mfma_f32_16x16x32_bf16 a[164:167], a[20:23], a[108:111], a[164:167]\n" \
"v_mfma_f32_16x16x32_bf16 a[168:171], a[16:19], a[112:115], a[168:171]\n" \
"ds_read_b128 a[12:15], v97 offset:2048\n" \
"v_mfma_f32_16x16x32_bf16 a[168:171], a[20:23], a[116:119], a[168:171]\n" \
"buffer_load_dwordx4 v65, s[36:39], s82 offen offset:1024 lds\n" \
"v_mfma_f32_16x16x32_bf16 a[172:175], a[16:19], a[120:123], a[172:175]\n" \
"ds_read_b64_tr_b16 a[78:79], v103 offset:20480\n" \
"v_mfma_f32_16x16x32_bf16 a[172:175], a[20:23], a[124:127], a[172:175]\n" \
"v_mfma_f32_16x16x32_bf16 a[188:191], a[24:27], a[120:123], a[188:191]\n" \
"ds_read_b128 a[16:19], v96 offset:4096\n" \
"v_mfma_f32_16x16x32_bf16 a[188:191], a[28:31], a[124:127], a[188:191]\n" \
"buffer_load_dwordx4 v66, s[36:39], s82 offen offset:2048 lds\n" \
"v_mfma_f32_16x16x32_bf16 a[184:187], a[24:27], a[112:115], a[184:187]\n" \
"ds_read_b64_tr_b16 a[80:81], v102 offset:512\n" \
"v_mfma_f32_16x16x32_bf16 a[184:187], a[28:31], a[116:119], a[184:187]\n" \
"v_mfma_f32_16x16x32_bf16 a[180:183], a[24:27], a[104:107], a[180:183]\n" \
"ds_read_b128 a[20:23], v97 offset:4096\n" \
"v_mfma_f32_16x16x32_bf16 a[180:183], a[28:31], a[108:111], a[180:183]\n" \
"buffer_load_dwordx4 v67, s[36:39], s82 offen offset:3072 lds\n" \
"v_mfma_f32_16x16x32_bf16 a[176:179], a[24:27], a[96:99], a[176:179]\n" \
"s_mov_b64 s[64:65], s[12:13]\n" \
"s_add_u32 s68, s67, 0x300000\n" \
"ds_read_b64_tr_b16 a[82:83], v102 offset:4608\n" \
"v_mfma_f32_16x16x32_bf16 a[176:179], a[28:31], a[100:103], a[176:179]\n" \
"s_add_u32 s64, s64, s68\n" \
"s_addc_u32 s65, s65, 0\n" \
"v_mfma_f32_16x16x32_bf16 a[192:195], a[32:35], a[96:99], a[192:195]\n" \
"ds_read_b128 a[24:27], v96 offset:6144\n" \
"v_mfma_f32_16x16x32_bf16 a[192:195], a[36:39], a[100:103], a[192:195]\n" \
"global_load_dword v238, v234, s[64:65]\n" \
"v_mfma_f32_16x16x32_bf16 a[196:199], a[32:35], a[104:107], a[196:199]\n" \
"s_mov_b64 s[64:65], s[12:13]\n" \
"s_add_u32 s68, s67, 0x400000\n" \
"ds_read_b64_tr_b16 a[84:85], v102 offset:16896\n" \
"v_mfma_f32_16x16x32_bf16 a[196:199], a[36:39], a[108:111], a[196:199]\n" \
"s_add_u32 s64, s64, s68\n" \
"s_addc_u32 s65, s65, 0\n" \
"v_mfma_f32_16x16x32_bf16 a[200:203], a[32:35], a[112:115], a[200:203]\n" \
"ds_read_b128 a[28:31], v97 offset:6144\n" \
"v_mfma_f32_16x16x32_bf16 a[200:203], a[36:39], a[116:119], a[200:203]\n" \
"global_load_dword v238, v234, s[64:65]\n" \
"v_mfma_f32_16x16x32_bf16 a[204:207], a[32:35], a[120:123], a[204:207]\n" \
"s_mov_b64 s[64:65], s[12:13]\n" \
"s_add_u32 s68, s67, 0x600000\n" \
"ds_read_b64_tr_b16 a[86:87], v102 offset:20992\n" \
"v_mfma_f32_16x16x32_bf16 a[204:207], a[36:39], a[124:127], a[204:207]\n" \
"s_add_u32 s64, s64, s68\n" \
"s_addc_u32 s65, s65, 0\n" \
"v_mfma_f32_16x16x32_bf16 a[220:223], a[40:43], a[120:123], a[220:223]\n" \
"ds_read_b128 a[32:35], v96 offset:8192\n" \
"v_mfma_f32_16x16x32_bf16 a[220:223], a[44:47], a[124:127], a[220:223]\n" \
"global_load_dword v238, v234, s[64:65]\n" \
"v_mfma_f32_16x16x32_bf16 a[216:219], a[40:43], a[112:115], a[216:219]\n" \
"ds_read_b64_tr_b16 a[88:89], v103 offset:512\n" \
"v_mfma_f32_16x16x32_bf16 a[216:219], a[44:47], a[116:119], a[216:219]\n" \
"v_mfma_f32_16x16x32_bf16 a[212:215], a[40:43], a[104:107], a[212:215]\n" \
"ds_read_b128 a[36:39], v97 offset:8192\n" \
"v_mfma_f32_16x16x32_bf16 a[212:215], a[44:47], a[108:111], a[212:215]\n" \
"v_mfma_f32_16x16x32_bf16 a[208:211], a[40:43], a[96:99], a[208:211]\n" \
"ds_read_b64_tr_b16 a[90:91], v103 offset:4608\n" \
"v_mfma_f32_16x16x32_bf16 a[208:211], a[44:47], a[100:103], a[208:211]\n" \
"v_mfma_f32_16x16x32_bf16 a[224:227], a[48:51], a[96:99], a[224:227]\n" \
"ds_read_b128 a[40:43], v96 offset:10240\n" \
"v_mfma_f32_16x16x32_bf16 a[224:227], a[52:55], a[100:103], a[224:227]\n" \
"v_mfma_f32_16x16x32_bf16 a[228:231], a[48:51], a[104:107], a[228:231]\n" \
"ds_read_b64_tr_b16 a[92:93], v103 offset:16896\n" \
"v_mfma_f32_16x16x32_bf16 a[228:231], a[52:55], a[108:111], a[228:231]\n" \
"v_mfma_f32_16x16x32_bf16 a[232:235], a[48:51], a[112:115], a[232:235]\n" \
"ds_read_b128 a[44:47], v97 offset:10240\n" \
"v_mfma_f32_16x16x32_bf16 a[232:235], a[52:55], a[116:119], a[232:235]\n" \
"v_mfma_f32_16x16x32_bf16 a[236:239], a[48:51], a[120:123], a[236:239]\n" \
"ds_read_b64_tr_b16 a[94:95], v103 offset:20992\n" \
"v_mfma_f32_16x16x32_bf16 a[236:239], a[52:55], a[124:127], a[236:239]\n" \
"v_mfma_f32_16x16x32_bf16 a[252:255], a[56:59], a[120:123], a[252:255]\n" \
"ds_read_b128 a[48:51], v96 offset:12288\n" \
"v_mfma_f32_16x16x32_bf16 a[252:255], a[60:63], a[124:127], a[252:255]\n" \
"v_mfma_f32_16x16x32_bf16 a[248:251], a[56:59], a[112:115], a[248:251]\n" \
"v_mfma_f32_16x16x32_bf16 a[248:251], a[60:63], a[116:119], a[248:251]\n" \
"v_mfma_f32_16x16x32_bf16 a[244:247], a[56:59], a[104:107], a[244:247]\n" \
"ds_read_b128 a[52:55], v97 offset:12288\n" \
"Lpg1668_%=:\n" \
"Lst0x2_%=:\n" \
"s_cbranch_execz Lpg3903_%=\n" \
"v_mfma_f32_16x16x32_bf16 a[244:247], a[60:63], a[108:111], a[244:247]\n" \
"v_mfma_f32_16x16x32_bf16 a[240:243], a[56:59], a[96:99], a[240:243]\n" \
"v_mfma_f32_16x16x32_bf16 a[240:243], a[60:63], a[100:103], a[240:243]\n" \
"s_waitcnt lgkmcnt(1)\n" \
"v_mfma_f32_16x16x32_bf16 v[0:3], a[64:67], a[0:3], v[0:3]\n" \
"ds_read_b128 a[56:59], v96 offset:14336\n" \
"v_mfma_f32_16x16x32_bf16 v[0:3], a[68:71], a[4:7], v[0:3]\n" \
"v_mfma_f32_16x16x32_bf16 v[4:7], a[72:75], a[0:3], v[4:7]\n" \
"s_add_u32 m0, s79, 0\n" \
"ds_read_b128 a[60:63], v97 offset:14336\n" \
"v_mfma_f32_16x16x32_bf16 v[4:7], a[76:79], a[4:7], v[4:7]\n" \
"buffer_load_dwordx4 v228, s[52:55], s78 offen offset:0 lds\n" \
"v_mfma_f32_16x16x32_bf16 v[8:11], a[80:83], a[0:3], v[8:11]\n" \
"ds_read_b64_tr_b16 a[96:97], v102 offset:1024\n" \
"v_mfma_f32_16x16x32_bf16 v[8:11], a[84:87], a[4:7], v[8:11]\n" \
"v_mfma_f32_16x16x32_bf16 v[12:15], a[88:91], a[0:3], v[12:15]\n" \
"ds_read_b64_tr_b16 a[98:99], v102 offset:5120\n" \
"v_mfma_f32_16x16x32_bf16 v[12:15], a[92:95], a[4:7], v[12:15]\n" \
"buffer_load_dwordx4 v229, s[52:55], s78 offen offset:1024 lds\n" \
"v_mfma_f32_16x16x32_bf16 v[28:31], a[88:91], a[8:11], v[28:31]\n" \
"ds_read_b64_tr_b16 a[100:101], v102 offset:17408\n" \
"v_mfma_f32_16x16x32_bf16 v[28:31], a[92:95], a[12:15], v[28:31]\n" \
"v_mfma_f32_16x16x32_bf16 v[24:27], a[80:83], a[8:11], v[24:27]\n" \
"s_xor_b32 s78, s78, 0x180000\n" \
"ds_read_b64_tr_b16 a[102:103], v102 offset:21504\n" \
"v_mfma_f32_16x16x32_bf16 v[24:27], a[84:87], a[12:15], v[24:27]\n" \
"buffer_load_dwordx4 v228, s[52:55], s89 offen offset:2048 lds\n" \
"v_mfma_f32_16x16x32_bf16 v[20:23], a[72:75], a[8:11], v[20:23]\n" \
"ds_read_b64_tr_b16 a[104:105], v103 offset:1024\n" \
"v_mfma_f32_16x16x32_bf16 v[20:23], a[76:79], a[12:15], v[20:23]\n" \
"v_mfma_f32_16x16x32_bf16 v[16:19], a[64:67], a[8:11], v[16:19]\n" \
"ds_read_b64_tr_b16 a[106:107], v103 offset:5120\n" \
"v_mfma_f32_16x16x32_bf16 v[16:19], a[68:71], a[12:15], v[16:19]\n" \
"buffer_load_dwordx4 v229, s[52:55], s89 offen offset:3072 lds\n" \
"v_mfma_f32_16x16x32_bf16 v[32:35], a[64:67], a[16:19], v[32:35]\n" \
"ds_read_b64_tr_b16 a[108:109], v103 offset:17408\n" \
"v_mfma_f32_16x16x32_bf16 v[32:35], a[68:71], a[20:23], v[32:35]\n" \
"s_xor_b32 s89, s89, 0x180000\n" \
"v_mfma_f32_16x16x32_bf16 v[36:39], a[72:75], a[16:19], v[36:39]\n" \
"s_add_u32 m0, s79, 16384\n" \
"ds_read_b64_tr_b16 a[110:111], v103 offset:21504\n" \
"v_mfma_f32_16x16x32_bf16 v[36:39], a[76:79], a[20:23], v[36:39]\n" \
"buffer_load_dwordx4 v228, s[52:55], s78 offen offset:0 lds\n" \
"v_mfma_f32_16x16x32_bf16 v[40:43], a[80:83], a[16:19], v[40:43]\n" \
"ds_read_b64_tr_b16 a[112:113], v102 offset:1536\n" \
"v_mfma_f32_16x16x32_bf16 v[40:43], a[84:87], a[20:23], v[40:43]\n" \
"v_mfma_f32_16x16x32_bf16 v[44:47], a[88:91], a[16:19], v[44:47]\n" \
"ds_read_b64_tr_b16 a[114:115], v102 offset:5632\n" \
"v_mfma_f32_16x16x32_bf16 v[44:47], a[92:95], a[20:23], v[44:47]\n" \
"buffer_load_dwordx4 v229, s[52:55], s78 offen offset:1024 lds\n" \
"v_mfma_f32_16x16x32_bf16 v[60:63], a[88:91], a[24:27], v[60:63]\n" \
"ds_read_b64_tr_b16 a[116:117], v102 offset:17920\n" \
"v_mfma_f32_16x16x32_bf16 v[60:63], a[92:95], a[28:31], v[60:63]\n" \
"v_mfma_f32_16x16x32_bf16 v[56:59], a[80:83], a[24:27], v[56:59]\n" \
"s_xor_b32 s78, s78, 0x400000\n" \
"ds_read_b64_tr_b16 a[118:119], v102 offset:22016\n" \
"v_mfma_f32_16x16x32_bf16 v[56:59], a[84:87], a[28:31], v[56:59]\n" \
"buffer_load_dwordx4 v228, s[52:55], s89 offen offset:2048 lds\n" \
"v_mfma_f32_16x16x32_bf16 v[52:55], a[72:75], a[24:27], v[52:55]\n" \
"ds_read_b64_tr_b16 a[120:121], v103 offset:1536\n" \
"v_mfma_f32_16x16x32_bf16 v[52:55], a[76:79], a[28:31], v[52:55]\n" \
"v_mfma_f32_16x16x32_bf16 v[48:51], a[64:67], a[24:27], v[48:51]\n" \
"ds_read_b64_tr_b16 a[122:123], v103 offset:5632\n" \
"v_mfma_f32_16x16x32_bf16 v[48:51], a[68:71], a[28:31], v[48:51]\n" \
"buffer_load_dwordx4 v229, s[52:55], s89 offen offset:3072 lds\n" \
"v_mfma_f32_16x16x32_bf16 v[144:147], a[64:67], a[32:35], v[144:147]\n" \
"ds_read_b64_tr_b16 a[124:125], v103 offset:17920\n" \
"v_mfma_f32_16x16x32_bf16 v[144:147], a[68:71], a[36:39], v[144:147]\n" \
"v_mfma_f32_16x16x32_bf16 v[148:151], a[72:75], a[32:35], v[148:151]\n" \
"s_xor_b32 s89, s89, 0x400000\n" \
"ds_read_b64_tr_b16 a[126:127], v103 offset:22016\n" \
"v_mfma_f32_16x16x32_bf16 v[148:151], a[76:79], a[36:39], v[148:151]\n" \
"v_mfma_f32_16x16x32_bf16 v[152:155], a[80:83], a[32:35], v[152:155]\n" \
"v_mfma_f32_16x16x32_bf16 v[152:155], a[84:87], a[36:39], v[152:155]\n" \
"v_mfma_f32_16x16x32_bf16 v[156:159], a[88:91], a[32:35], v[156:159]\n" \
"v_mfma_f32_16x16x32_bf16 v[156:159], a[92:95], a[36:39], v[156:159]\n" \
"v_mfma_f32_16x16x32_bf16 v[188:191], a[88:91], a[40:43], v[188:191]\n" \
"v_mfma_f32_16x16x32_bf16 v[188:191], a[92:95], a[44:47], v[188:191]\n" \
"v_mfma_f32_16x16x32_bf16 v[184:187], a[80:83], a[40:43], v[184:187]\n" \
"v_mfma_f32_16x16x32_bf16 v[184:187], a[84:87], a[44:47], v[184:187]\n" \
"v_mfma_f32_16x16x32_bf16 v[180:183], a[72:75], a[40:43], v[180:183]\n" \
"v_mfma_f32_16x16x32_bf16 v[180:183], a[76:79], a[44:47], v[180:183]\n" \
"v_mfma_f32_16x16x32_bf16 v[176:179], a[64:67], a[40:43], v[176:179]\n" \
"v_mfma_f32_16x16x32_bf16 v[176:179], a[68:71], a[44:47], v[176:179]\n" \
"s_waitcnt lgkmcnt(0)\n" \
"v_mfma_f32_16x16x32_bf16 v[192:195], a[64:67], a[48:51], v[192:195]\n" \
"v_mfma_f32_16x16x32_bf16 v[192:195], a[68:71], a[52:55], v[192:195]\n" \
"v_mfma_f32_16x16x32_bf16 v[196:199], a[72:75], a[48:51], v[196:199]\n" \
"v_mfma_f32_16x16x32_bf16 v[196:199], a[76:79], a[52:55], v[196:199]\n" \
"v_mfma_f32_16x16x32_bf16 v[200:203], a[80:83], a[48:51], v[200:203]\n" \
"v_mfma_f32_16x16x32_bf16 v[200:203], a[84:87], a[52:55], v[200:203]\n" \
"v_mfma_f32_16x16x32_bf16 v[204:207], a[88:91], a[48:51], v[204:207]\n" \
"v_mfma_f32_16x16x32_bf16 v[204:207], a[92:95], a[52:55], v[204:207]\n" \
"v_mfma_f32_16x16x32_bf16 v[220:223], a[88:91], a[56:59], v[220:223]\n" \
"v_mfma_f32_16x16x32_bf16 v[220:223], a[92:95], a[60:63], v[220:223]\n" \
"v_mfma_f32_16x16x32_bf16 v[216:219], a[80:83], a[56:59], v[216:219]\n" \
"v_mfma_f32_16x16x32_bf16 v[216:219], a[84:87], a[60:63], v[216:219]\n" \
"v_mfma_f32_16x16x32_bf16 v[212:215], a[72:75], a[56:59], v[212:215]\n" \
"v_mfma_f32_16x16x32_bf16 v[212:215], a[76:79], a[60:63], v[212:215]\n" \
"v_mfma_f32_16x16x32_bf16 v[208:211], a[64:67], a[56:59], v[208:211]\n" \
"v_mfma_f32_16x16x32_bf16 v[208:211], a[68:71], a[60:63], v[208:211]\n" \
"s_waitcnt vmcnt(11) lgkmcnt(0)\n" \
"s_barrier\n" \
"v_mfma_f32_16x16x32_bf16 a[128:131], a[0:3], a[96:99], a[128:131]\n" \
"s_add_u32 m0, s74, 131072\n" \
"ds_read_b64_tr_b16 a[64:65], v98 offset:32768\n" \
"v_mfma_f32_16x16x32_bf16 a[128:131], a[4:7], a[100:103], a[128:131]\n" \
"buffer_load_dwordx4 v224, s[48:51], s75 offen offset:0 lds\n" \
"v_mfma_f32_16x16x32_bf16 a[132:135], a[0:3], a[104:107], a[132:135]\n" \
"ds_read_b64_tr_b16 a[66:67], v98 offset:36864\n" \
"v_mfma_f32_16x16x32_bf16 a[132:135], a[4:7], a[108:111], a[132:135]\n" \
"v_mfma_f32_16x16x32_bf16 a[136:139], a[0:3], a[112:115], a[136:139]\n" \
"ds_read_b64_tr_b16 a[68:69], v98 offset:49152\n" \
"v_mfma_f32_16x16x32_bf16 a[136:139], a[4:7], a[116:119], a[136:139]\n" \
"buffer_load_dwordx4 v225, s[48:51], s75 offen offset:1024 lds\n" \
"v_mfma_f32_16x16x32_bf16 a[140:143], a[0:3], a[120:123], a[140:143]\n" \
"ds_read_b64_tr_b16 a[70:71], v98 offset:53248\n" \
"v_mfma_f32_16x16x32_bf16 a[140:143], a[4:7], a[124:127], a[140:143]\n" \
"v_mfma_f32_16x16x32_bf16 a[156:159], a[8:11], a[120:123], a[156:159]\n" \
"ds_read_b128 a[0:3], v72 offset:32768\n" \
"v_mfma_f32_16x16x32_bf16 a[156:159], a[12:15], a[124:127], a[156:159]\n" \
"buffer_load_dwordx4 v226, s[48:51], s75 offen offset:2048 lds\n" \
"v_mfma_f32_16x16x32_bf16 a[152:155], a[8:11], a[112:115], a[152:155]\n" \
"ds_read_b64_tr_b16 a[72:73], v99 offset:32768\n" \
"v_mfma_f32_16x16x32_bf16 a[152:155], a[12:15], a[116:119], a[152:155]\n" \
"v_mfma_f32_16x16x32_bf16 a[148:151], a[8:11], a[104:107], a[148:151]\n" \
"ds_read_b128 a[4:7], v73 offset:32768\n" \
"v_mfma_f32_16x16x32_bf16 a[148:151], a[12:15], a[108:111], a[148:151]\n" \
"buffer_load_dwordx4 v227, s[48:51], s75 offen offset:3072 lds\n" \
"v_mfma_f32_16x16x32_bf16 a[144:147], a[8:11], a[96:99], a[144:147]\n" \
"ds_read_b64_tr_b16 a[74:75], v99 offset:36864\n" \
"v_mfma_f32_16x16x32_bf16 a[144:147], a[12:15], a[100:103], a[144:147]\n" \
"s_xor_b32 s75, s75, 0x180\n" \
"v_mfma_f32_16x16x32_bf16 a[160:163], a[16:19], a[96:99], a[160:163]\n" \
"s_add_u32 m0, s74, 135168\n" \
"ds_read_b128 a[8:11], v72 offset:34816\n" \
"v_mfma_f32_16x16x32_bf16 a[160:163], a[20:23], a[100:103], a[160:163]\n" \
"buffer_load_dwordx4 v224, s[48:51], s77 offen offset:0 lds\n" \
"v_mfma_f32_16x16x32_bf16 a[164:167], a[16:19], a[104:107], a[164:167]\n" \
"ds_read_b64_tr_b16 a[76:77], v99 offset:49152\n" \
"v_mfma_f32_16x16x32_bf16 a[164:167], a[20:23], a[108:111], a[164:167]\n" \
"v_mfma_f32_16x16x32_bf16 a[168:171], a[16:19], a[112:115], a[168:171]\n" \
"ds_read_b128 a[12:15], v73 offset:34816\n" \
"v_mfma_f32_16x16x32_bf16 a[168:171], a[20:23], a[116:119], a[168:171]\n" \
"buffer_load_dwordx4 v225, s[48:51], s77 offen offset:1024 lds\n" \
"v_mfma_f32_16x16x32_bf16 a[172:175], a[16:19], a[120:123], a[172:175]\n" \
"ds_read_b64_tr_b16 a[78:79], v99 offset:53248\n" \
"v_mfma_f32_16x16x32_bf16 a[172:175], a[20:23], a[124:127], a[172:175]\n" \
"v_mfma_f32_16x16x32_bf16 a[188:191], a[24:27], a[120:123], a[188:191]\n" \
"ds_read_b128 a[16:19], v72 offset:36864\n" \
"v_mfma_f32_16x16x32_bf16 a[188:191], a[28:31], a[124:127], a[188:191]\n" \
"buffer_load_dwordx4 v226, s[48:51], s77 offen offset:2048 lds\n" \
"v_mfma_f32_16x16x32_bf16 a[184:187], a[24:27], a[112:115], a[184:187]\n" \
"ds_read_b64_tr_b16 a[80:81], v98 offset:33280\n" \
"v_mfma_f32_16x16x32_bf16 a[184:187], a[28:31], a[116:119], a[184:187]\n" \
"v_mfma_f32_16x16x32_bf16 a[180:183], a[24:27], a[104:107], a[180:183]\n" \
"ds_read_b128 a[20:23], v73 offset:36864\n" \
"v_mfma_f32_16x16x32_bf16 a[180:183], a[28:31], a[108:111], a[180:183]\n" \
"buffer_load_dwordx4 v227, s[48:51], s77 offen offset:3072 lds\n" \
"v_mfma_f32_16x16x32_bf16 a[176:179], a[24:27], a[96:99], a[176:179]\n" \
"s_mov_b64 s[64:65], s[12:13]\n" \
"s_add_u32 s68, s67, 0x700000\n" \
"ds_read_b64_tr_b16 a[82:83], v98 offset:37376\n" \
"v_mfma_f32_16x16x32_bf16 a[176:179], a[28:31], a[100:103], a[176:179]\n" \
"s_add_u32 s64, s64, s68\n" \
"s_addc_u32 s65, s65, 0\n" \
"v_mfma_f32_16x16x32_bf16 a[192:195], a[32:35], a[96:99], a[192:195]\n" \
"s_xor_b32 s77, s77, 0x180\n" \
"ds_read_b128 a[24:27], v72 offset:38912\n" \
"v_mfma_f32_16x16x32_bf16 a[192:195], a[36:39], a[100:103], a[192:195]\n" \
"global_load_dword v238, v234, s[64:65]\n" \
"v_mfma_f32_16x16x32_bf16 a[196:199], a[32:35], a[104:107], a[196:199]\n" \
"ds_read_b64_tr_b16 a[84:85], v98 offset:49664\n" \
"v_mfma_f32_16x16x32_bf16 a[196:199], a[36:39], a[108:111], a[196:199]\n" \
"v_mfma_f32_16x16x32_bf16 a[200:203], a[32:35], a[112:115], a[200:203]\n" \
"s_lshr_b32 s68, s66, 2\n" \
"s_and_b32 s68, s68, 0xffff\n" \
"s_add_u32 s68, s68, 65585152\n" \
"ds_read_b128 a[28:31], v73 offset:38912\n" \
"v_mfma_f32_16x16x32_bf16 a[200:203], a[36:39], a[116:119], a[200:203]\n" \
"buffer_load_dword v238, v242, s[56:59], s68 offen sc1\n" \
"v_mfma_f32_16x16x32_bf16 a[204:207], a[32:35], a[120:123], a[204:207]\n" \
"ds_read_b64_tr_b16 a[86:87], v98 offset:53760\n" \
"v_mfma_f32_16x16x32_bf16 a[204:207], a[36:39], a[124:127], a[204:207]\n" \
"v_mfma_f32_16x16x32_bf16 a[220:223], a[40:43], a[120:123], a[220:223]\n" \
"ds_read_b128 a[32:35], v72 offset:40960\n" \
"v_mfma_f32_16x16x32_bf16 a[220:223], a[44:47], a[124:127], a[220:223]\n" \
"v_mfma_f32_16x16x32_bf16 a[216:219], a[40:43], a[112:115], a[216:219]\n" \
"ds_read_b64_tr_b16 a[88:89], v99 offset:33280\n" \
"v_mfma_f32_16x16x32_bf16 a[216:219], a[44:47], a[116:119], a[216:219]\n" \
"v_mfma_f32_16x16x32_bf16 a[212:215], a[40:43], a[104:107], a[212:215]\n" \
"ds_read_b128 a[36:39], v73 offset:40960\n" \
"v_mfma_f32_16x16x32_bf16 a[212:215], a[44:47], a[108:111], a[212:215]\n" \
"v_mfma_f32_16x16x32_bf16 a[208:211], a[40:43], a[96:99], a[208:211]\n" \
"ds_read_b64_tr_b16 a[90:91], v99 offset:37376\n" \
"v_mfma_f32_16x16x32_bf16 a[208:211], a[44:47], a[100:103], a[208:211]\n" \
"v_mfma_f32_16x16x32_bf16 a[224:227], a[48:51], a[96:99], a[224:227]\n" \
"ds_read_b128 a[40:43], v72 offset:43008\n" \
"v_mfma_f32_16x16x32_bf16 a[224:227], a[52:55], a[100:103], a[224:227]\n" \
"v_mfma_f32_16x16x32_bf16 a[228:231], a[48:51], a[104:107], a[228:231]\n" \
"ds_read_b64_tr_b16 a[92:93], v99 offset:49664\n" \
"v_mfma_f32_16x16x32_bf16 a[228:231], a[52:55], a[108:111], a[228:231]\n" \
"v_mfma_f32_16x16x32_bf16 a[232:235], a[48:51], a[112:115], a[232:235]\n" \
"ds_read_b128 a[44:47], v73 offset:43008\n" \
"v_mfma_f32_16x16x32_bf16 a[232:235], a[52:55], a[116:119], a[232:235]\n" \
"v_mfma_f32_16x16x32_bf16 a[236:239], a[48:51], a[120:123], a[236:239]\n" \
"ds_read_b64_tr_b16 a[94:95], v99 offset:53760\n" \
"v_mfma_f32_16x16x32_bf16 a[236:239], a[52:55], a[124:127], a[236:239]\n" \
"v_mfma_f32_16x16x32_bf16 a[252:255], a[56:59], a[120:123], a[252:255]\n" \
"ds_read_b128 a[48:51], v72 offset:45056\n" \
"v_mfma_f32_16x16x32_bf16 a[252:255], a[60:63], a[124:127], a[252:255]\n" \
"v_mfma_f32_16x16x32_bf16 a[248:251], a[56:59], a[112:115], a[248:251]\n" \
"v_mfma_f32_16x16x32_bf16 a[248:251], a[60:63], a[116:119], a[248:251]\n" \
"v_mfma_f32_16x16x32_bf16 a[244:247], a[56:59], a[104:107], a[244:247]\n" \
"ds_read_b128 a[52:55], v73 offset:45056\n" \
"v_mfma_f32_16x16x32_bf16 a[244:247], a[60:63], a[108:111], a[244:247]\n" \
"v_mfma_f32_16x16x32_bf16 a[240:243], a[56:59], a[96:99], a[240:243]\n" \
"v_mfma_f32_16x16x32_bf16 a[240:243], a[60:63], a[100:103], a[240:243]\n" \
"s_waitcnt lgkmcnt(1)\n" \
"v_mfma_f32_16x16x32_bf16 v[0:3], a[64:67], a[0:3], v[0:3]\n" \
"ds_read_b128 a[56:59], v72 offset:47104\n" \
"v_mfma_f32_16x16x32_bf16 v[0:3], a[68:71], a[4:7], v[0:3]\n" \
"v_mfma_f32_16x16x32_bf16 v[4:7], a[72:75], a[0:3], v[4:7]\n" \
"s_add_u32 m0, s74, 65536\n" \
"ds_read_b128 a[60:63], v73 offset:47104\n" \
"v_mfma_f32_16x16x32_bf16 v[4:7], a[76:79], a[4:7], v[4:7]\n" \
"buffer_load_dwordx4 v224, s[48:51], s75 offen offset:0 lds\n" \
"v_mfma_f32_16x16x32_bf16 v[8:11], a[80:83], a[0:3], v[8:11]\n" \
"ds_read_b64_tr_b16 a[96:97], v98 offset:33792\n" \
"v_mfma_f32_16x16x32_bf16 v[8:11], a[84:87], a[4:7], v[8:11]\n" \
"v_mfma_f32_16x16x32_bf16 v[12:15], a[88:91], a[0:3], v[12:15]\n" \
"ds_read_b64_tr_b16 a[98:99], v98 offset:37888\n" \
"v_mfma_f32_16x16x32_bf16 v[12:15], a[92:95], a[4:7], v[12:15]\n" \
"buffer_load_dwordx4 v225, s[48:51], s75 offen offset:1024 lds\n" \
"v_mfma_f32_16x16x32_bf16 v[28:31], a[88:91], a[8:11], v[28:31]\n" \
"ds_read_b64_tr_b16 a[100:101], v98 offset:50176\n" \
"v_mfma_f32_16x16x32_bf16 v[28:31], a[92:95], a[12:15], v[28:31]\n" \
"v_mfma_f32_16x16x32_bf16 v[24:27], a[80:83], a[8:11], v[24:27]\n" \
"ds_read_b64_tr_b16 a[102:103], v98 offset:54272\n" \
"v_mfma_f32_16x16x32_bf16 v[24:27], a[84:87], a[12:15], v[24:27]\n" \
"buffer_load_dwordx4 v226, s[48:51], s75 offen offset:2048 lds\n" \
"v_mfma_f32_16x16x32_bf16 v[20:23], a[72:75], a[8:11], v[20:23]\n" \
"ds_read_b64_tr_b16 a[104:105], v99 offset:33792\n" \
"v_mfma_f32_16x16x32_bf16 v[20:23], a[76:79], a[12:15], v[20:23]\n" \
"v_mfma_f32_16x16x32_bf16 v[16:19], a[64:67], a[8:11], v[16:19]\n" \
"ds_read_b64_tr_b16 a[106:107], v99 offset:37888\n" \
"v_mfma_f32_16x16x32_bf16 v[16:19], a[68:71], a[12:15], v[16:19]\n" \
"buffer_load_dwordx4 v227, s[48:51], s75 offen offset:3072 lds\n" \
"v_mfma_f32_16x16x32_bf16 v[32:35], a[64:67], a[16:19], v[32:35]\n" \
"ds_read_b64_tr_b16 a[108:109], v99 offset:50176\n" \
"v_mfma_f32_16x16x32_bf16 v[32:35], a[68:71], a[20:23], v[32:35]\n" \
"s_xor_b32 s75, s75, 0x400\n" \
"v_mfma_f32_16x16x32_bf16 v[36:39], a[72:75], a[16:19], v[36:39]\n" \
"s_add_u32 m0, s74, 69632\n" \
"ds_read_b64_tr_b16 a[110:111], v99 offset:54272\n" \
"v_mfma_f32_16x16x32_bf16 v[36:39], a[76:79], a[20:23], v[36:39]\n" \
"buffer_load_dwordx4 v224, s[48:51], s77 offen offset:0 lds\n" \
"v_mfma_f32_16x16x32_bf16 v[40:43], a[80:83], a[16:19], v[40:43]\n" \
"ds_read_b64_tr_b16 a[112:113], v98 offset:34304\n" \
"v_mfma_f32_16x16x32_bf16 v[40:43], a[84:87], a[20:23], v[40:43]\n" \
"v_mfma_f32_16x16x32_bf16 v[44:47], a[88:91], a[16:19], v[44:47]\n" \
"ds_read_b64_tr_b16 a[114:115], v98 offset:38400\n" \
"v_mfma_f32_16x16x32_bf16 v[44:47], a[92:95], a[20:23], v[44:47]\n" \
"buffer_load_dwordx4 v225, s[48:51], s77 offen offset:1024 lds\n" \
"v_mfma_f32_16x16x32_bf16 v[60:63], a[88:91], a[24:27], v[60:63]\n" \
"ds_read_b64_tr_b16 a[116:117], v98 offset:50688\n" \
"v_mfma_f32_16x16x32_bf16 v[60:63], a[92:95], a[28:31], v[60:63]\n" \
"v_mfma_f32_16x16x32_bf16 v[56:59], a[80:83], a[24:27], v[56:59]\n" \
"ds_read_b64_tr_b16 a[118:119], v98 offset:54784\n" \
"v_mfma_f32_16x16x32_bf16 v[56:59], a[84:87], a[28:31], v[56:59]\n" \
"buffer_load_dwordx4 v226, s[48:51], s77 offen offset:2048 lds\n" \
"v_mfma_f32_16x16x32_bf16 v[52:55], a[72:75], a[24:27], v[52:55]\n" \
"ds_read_b64_tr_b16 a[120:121], v99 offset:34304\n" \
"v_mfma_f32_16x16x32_bf16 v[52:55], a[76:79], a[28:31], v[52:55]\n" \
"v_mfma_f32_16x16x32_bf16 v[48:51], a[64:67], a[24:27], v[48:51]\n" \
"ds_read_b64_tr_b16 a[122:123], v99 offset:38400\n" \
"v_mfma_f32_16x16x32_bf16 v[48:51], a[68:71], a[28:31], v[48:51]\n" \
"buffer_load_dwordx4 v227, s[48:51], s77 offen offset:3072 lds\n" \
"v_mfma_f32_16x16x32_bf16 v[144:147], a[64:67], a[32:35], v[144:147]\n" \
"ds_read_b64_tr_b16 a[124:125], v99 offset:50688\n" \
"v_mfma_f32_16x16x32_bf16 v[144:147], a[68:71], a[36:39], v[144:147]\n" \
"v_mfma_f32_16x16x32_bf16 v[148:151], a[72:75], a[32:35], v[148:151]\n" \
"s_xor_b32 s77, s77, 0x400\n" \
"ds_read_b64_tr_b16 a[126:127], v99 offset:54784\n" \
"v_mfma_f32_16x16x32_bf16 v[148:151], a[76:79], a[36:39], v[148:151]\n" \
"v_mfma_f32_16x16x32_bf16 v[152:155], a[80:83], a[32:35], v[152:155]\n" \
"v_mfma_f32_16x16x32_bf16 v[152:155], a[84:87], a[36:39], v[152:155]\n" \
"v_mfma_f32_16x16x32_bf16 v[156:159], a[88:91], a[32:35], v[156:159]\n" \
"v_mfma_f32_16x16x32_bf16 v[156:159], a[92:95], a[36:39], v[156:159]\n" \
"v_mfma_f32_16x16x32_bf16 v[188:191], a[88:91], a[40:43], v[188:191]\n" \
"v_mfma_f32_16x16x32_bf16 v[188:191], a[92:95], a[44:47], v[188:191]\n" \
"v_mfma_f32_16x16x32_bf16 v[184:187], a[80:83], a[40:43], v[184:187]\n" \
"v_mfma_f32_16x16x32_bf16 v[184:187], a[84:87], a[44:47], v[184:187]\n" \
"v_mfma_f32_16x16x32_bf16 v[180:183], a[72:75], a[40:43], v[180:183]\n" \
"v_mfma_f32_16x16x32_bf16 v[180:183], a[76:79], a[44:47], v[180:183]\n" \
"v_mfma_f32_16x16x32_bf16 v[176:179], a[64:67], a[40:43], v[176:179]\n" \
"v_mfma_f32_16x16x32_bf16 v[176:179], a[68:71], a[44:47], v[176:179]\n" \
"s_waitcnt lgkmcnt(0)\n" \
"v_mfma_f32_16x16x32_bf16 v[192:195], a[64:67], a[48:51], v[192:195]\n" \
"v_mfma_f32_16x16x32_bf16 v[192:195], a[68:71], a[52:55], v[192:195]\n" \
"v_mfma_f32_16x16x32_bf16 v[196:199], a[72:75], a[48:51], v[196:199]\n" \
"v_mfma_f32_16x16x32_bf16 v[196:199], a[76:79], a[52:55], v[196:199]\n" \
"v_mfma_f32_16x16x32_bf16 v[200:203], a[80:83], a[48:51], v[200:203]\n" \
"v_mfma_f32_16x16x32_bf16 v[200:203], a[84:87], a[52:55], v[200:203]\n" \
"v_mfma_f32_16x16x32_bf16 v[204:207], a[88:91], a[48:51], v[204:207]\n" \
"v_mfma_f32_16x16x32_bf16 v[204:207], a[92:95], a[52:55], v[204:207]\n" \
"v_mfma_f32_16x16x32_bf16 v[220:223], a[88:91], a[56:59], v[220:223]\n" \
"v_mfma_f32_16x16x32_bf16 v[220:223], a[92:95], a[60:63], v[220:223]\n" \
"v_mfma_f32_16x16x32_bf16 v[216:219], a[80:83], a[56:59], v[216:219]\n" \
"v_mfma_f32_16x16x32_bf16 v[216:219], a[84:87], a[60:63], v[216:219]\n" \
"v_mfma_f32_16x16x32_bf16 v[212:215], a[72:75], a[56:59], v[212:215]\n" \
"v_mfma_f32_16x16x32_bf16 v[212:215], a[76:79], a[60:63], v[212:215]\n" \
"v_mfma_f32_16x16x32_bf16 v[208:211], a[64:67], a[56:59], v[208:211]\n" \
"v_mfma_f32_16x16x32_bf16 v[208:211], a[68:71], a[60:63], v[208:211]\n" \
"s_waitcnt vmcnt(10) lgkmcnt(0)\n" \
"s_barrier\n" \
"v_mfma_f32_16x16x32_bf16 a[128:131], a[0:3], a[96:99], a[128:131]\n" \
"s_add_u32 m0, s74, 98304\n" \
"v_mfma_f32_16x16x32_bf16 a[128:131], a[4:7], a[100:103], a[128:131]\n" \
"buffer_load_dwordx4 v224, s[48:51], s75 offen offset:0 lds\n" \
"v_mfma_f32_16x16x32_bf16 a[132:135], a[0:3], a[104:107], a[132:135]\n" \
"ds_read_b64_tr_b16 a[64:65], v232 offset:0\n" \
"ds_read_b64_tr_b16 a[66:67], v232 offset:2048\n" \
"v_mfma_f32_16x16x32_bf16 a[132:135], a[4:7], a[108:111], a[132:135]\n" \
"v_mfma_f32_16x16x32_bf16 a[136:139], a[0:3], a[112:115], a[136:139]\n" \
"v_mfma_f32_16x16x32_bf16 a[136:139], a[4:7], a[116:119], a[136:139]\n" \
"buffer_load_dwordx4 v225, s[48:51], s75 offen offset:1024 lds\n" \
"v_mfma_f32_16x16x32_bf16 a[140:143], a[0:3], a[120:123], a[140:143]\n" \
"ds_read_b64_tr_b16 a[68:69], v232 offset:8192\n" \
"ds_read_b64_tr_b16 a[70:71], v232 offset:10240\n" \
"v_mfma_f32_16x16x32_bf16 a[140:143], a[4:7], a[124:127], a[140:143]\n" \
"v_mfma_f32_16x16x32_bf16 a[156:159], a[8:11], a[120:123], a[156:159]\n" \
"v_mul_f32 v192, s76, v192\n" \
"v_mul_f32 v193, s76, v193\n" \
"v_mfma_f32_16x16x32_bf16 a[156:159], a[12:15], a[124:127], a[156:159]\n" \
"buffer_load_dwordx4 v226, s[48:51], s75 offen offset:2048 lds\n" \
"v_mfma_f32_16x16x32_bf16 a[152:155], a[8:11], a[112:115], a[152:155]\n" \
"ds_read_b64_tr_b16 a[72:73], v233 offset:0\n" \
"ds_read_b64_tr_b16 a[74:75], v233 offset:2048\n" \
"v_mfma_f32_16x16x32_bf16 a[152:155], a[12:15], a[116:119], a[152:155]\n" \
"v_mul_f32 v194, s76, v194\n" \
"v_mul_f32 v195, s76, v195\n" \
"v_mfma_f32_16x16x32_bf16 a[148:151], a[8:11], a[104:107], a[148:151]\n" \
"v_cvt_pk_bf16_f32 v192, v192, v193\n" \
"v_cvt_pk_bf16_f32 v193, v194, v195\n" \
"v_mfma_f32_16x16x32_bf16 a[148:151], a[12:15], a[108:111], a[148:151]\n" \
"buffer_load_dwordx4 v227, s[48:51], s75 offen offset:3072 lds\n" \
"v_mfma_f32_16x16x32_bf16 a[144:147], a[8:11], a[96:99], a[144:147]\n" \
"ds_read_b64_tr_b16 a[76:77], v233 offset:8192\n" \
"ds_read_b64_tr_b16 a[78:79], v233 offset:10240\n" \
"v_mfma_f32_16x16x32_bf16 a[144:147], a[12:15], a[100:103], a[144:147]\n" \
"s_xor_b32 s75, s75, 0x500\n" \
"v_mul_f32 v196, s76, v196\n" \
"v_mul_f32 v197, s76, v197\n" \
"v_mfma_f32_16x16x32_bf16 a[160:163], a[16:19], a[96:99], a[160:163]\n" \
"s_add_u32 m0, s74, 102400\n" \
"v_mul_f32 v198, s76, v198\n" \
"v_mul_f32 v199, s76, v199\n" \
"v_mfma_f32_16x16x32_bf16 a[160:163], a[20:23], a[100:103], a[160:163]\n" \
"buffer_load_dwordx4 v224, s[48:51], s77 offen offset:0 lds\n" \
"v_mfma_f32_16x16x32_bf16 a[164:167], a[16:19], a[104:107], a[164:167]\n" \
"ds_read_b64_tr_b16 a[80:81], v232 offset:512\n" \
"ds_read_b64_tr_b16 a[82:83], v232 offset:2560\n" \
"v_mfma_f32_16x16x32_bf16 a[164:167], a[20:23], a[108:111], a[164:167]\n" \
"v_cvt_pk_bf16_f32 v194, v196, v197\n" \
"v_cvt_pk_bf16_f32 v195, v198, v199\n" \
"v_mfma_f32_16x16x32_bf16 a[168:171], a[16:19], a[112:115], a[168:171]\n" \
"v_mul_f32 v208, s76, v208\n" \
"v_mul_f32 v209, s76, v209\n" \
"v_mfma_f32_16x16x32_bf16 a[168:171], a[20:23], a[116:119], a[168:171]\n" \
"buffer_load_dwordx4 v225, s[48:51], s77 offen offset:1024 lds\n" \
"v_mfma_f32_16x16x32_bf16 a[172:175], a[16:19], a[120:123], a[172:175]\n" \
"ds_read_b64_tr_b16 a[84:85], v232 offset:8704\n" \
"ds_read_b64_tr_b16 a[86:87], v232 offset:10752\n" \
"v_mfma_f32_16x16x32_bf16 a[172:175], a[20:23], a[124:127], a[172:175]\n" \
"v_mul_f32 v210, s76, v210\n" \
"v_mul_f32 v211, s76, v211\n" \
"v_mfma_f32_16x16x32_bf16 a[188:191], a[24:27], a[120:123], a[188:191]\n" \
"v_cvt_pk_bf16_f32 v208, v208, v209\n" \
"v_cvt_pk_bf16_f32 v209, v210, v211\n" \
"v_mfma_f32_16x16x32_bf16 a[188:191], a[28:31], a[124:127], a[188:191]\n" \
"buffer_load_dwordx4 v226, s[48:51], s77 offen offset:2048 lds\n" \
"v_mfma_f32_16x16x32_bf16 a[184:187], a[24:27], a[112:115], a[184:187]\n" \
"ds_read_b64_tr_b16 a[88:89], v233 offset:512\n" \
"ds_read_b64_tr_b16 a[90:91], v233 offset:2560\n" \
"v_mfma_f32_16x16x32_bf16 a[184:187], a[28:31], a[116:119], a[184:187]\n" \
"v_mul_f32 v212, s76, v212\n" \
"v_mul_f32 v213, s76, v213\n" \
"v_mfma_f32_16x16x32_bf16 a[180:183], a[24:27], a[104:107], a[180:183]\n" \
"v_mul_f32 v214, s76, v214\n" \
"v_mul_f32 v215, s76, v215\n" \
"v_mfma_f32_16x16x32_bf16 a[180:183], a[28:31], a[108:111], a[180:183]\n" \
"buffer_load_dwordx4 v227, s[48:51], s77 offen offset:3072 lds\n" \
"v_mfma_f32_16x16x32_bf16 a[176:179], a[24:27], a[96:99], a[176:179]\n" \
"ds_read_b64_tr_b16 a[92:93], v233 offset:8704\n" \
"ds_read_b64_tr_b16 a[94:95], v233 offset:10752\n" \
"v_mfma_f32_16x16x32_bf16 a[176:179], a[28:31], a[100:103], a[176:179]\n" \
"s_xor_b32 s77, s77, 0x500\n" \
"v_cvt_pk_bf16_f32 v210, v212, v213\n" \
"v_cvt_pk_bf16_f32 v211, v214, v215\n" \
"v_mfma_f32_16x16x32_bf16 a[192:195], a[32:35], a[96:99], a[192:195]\n" \
"s_add_u32 m0, s79, 32768\n" \
"v_mul_f32 v200, s76, v200\n" \
"v_mul_f32 v201, s76, v201\n" \
"v_mfma_f32_16x16x32_bf16 a[192:195], a[36:39], a[100:103], a[192:195]\n" \
"buffer_load_dwordx4 v228, s[52:55], s78 offen offset:0 lds\n" \
"v_mfma_f32_16x16x32_bf16 a[196:199], a[32:35], a[104:107], a[196:199]\n" \
"ds_read_b128 a[0:3], v250 offset:0\n" \
"ds_read_b128 a[4:7], v251 offset:0\n" \
"v_mfma_f32_16x16x32_bf16 a[196:199], a[36:39], a[108:111], a[196:199]\n" \
"v_mul_f32 v202, s76, v202\n" \
"v_mul_f32 v203, s76, v203\n" \
"v_mfma_f32_16x16x32_bf16 a[200:203], a[32:35], a[112:115], a[200:203]\n" \
"v_cvt_pk_bf16_f32 v200, v200, v201\n" \
"v_cvt_pk_bf16_f32 v201, v202, v203\n" \
"v_mfma_f32_16x16x32_bf16 a[200:203], a[36:39], a[116:119], a[200:203]\n" \
"buffer_load_dwordx4 v229, s[52:55], s78 offen offset:1024 lds\n" \
"v_mfma_f32_16x16x32_bf16 a[204:207], a[32:35], a[120:123], a[204:207]\n" \
"ds_read_b128 a[8:11], v250 offset:2048\n" \
"ds_read_b128 a[12:15], v251 offset:2048\n" \
"v_mfma_f32_16x16x32_bf16 a[204:207], a[36:39], a[124:127], a[204:207]\n" \
"v_mul_f32 v204, s76, v204\n" \
"v_mul_f32 v205, s76, v205\n" \
"v_mfma_f32_16x16x32_bf16 a[220:223], a[40:43], a[120:123], a[220:223]\n" \
"s_xor_b32 s78, s78, 0x500000\n" \
"v_mul_f32 v206, s76, v206\n" \
"v_mul_f32 v207, s76, v207\n" \
"v_mfma_f32_16x16x32_bf16 a[220:223], a[44:47], a[124:127], a[220:223]\n" \
"buffer_load_dwordx4 v228, s[52:55], s89 offen offset:2048 lds\n" \
"v_mfma_f32_16x16x32_bf16 a[216:219], a[40:43], a[112:115], a[216:219]\n" \
"ds_read_b128 a[16:19], v250 offset:4096\n" \
"ds_read_b128 a[20:23], v251 offset:4096\n" \
"v_mfma_f32_16x16x32_bf16 a[216:219], a[44:47], a[116:119], a[216:219]\n" \
"v_cvt_pk_bf16_f32 v202, v204, v205\n" \
"v_cvt_pk_bf16_f32 v203, v206, v207\n" \
"v_mfma_f32_16x16x32_bf16 a[212:215], a[40:43], a[104:107], a[212:215]\n" \
"v_mul_f32 v216, s76, v216\n" \
"v_mul_f32 v217, s76, v217\n" \
"v_mfma_f32_16x16x32_bf16 a[212:215], a[44:47], a[108:111], a[212:215]\n" \
"buffer_load_dwordx4 v229, s[52:55], s89 offen offset:3072 lds\n" \
"v_mfma_f32_16x16x32_bf16 a[208:211], a[40:43], a[96:99], a[208:211]\n" \
"v_mul_f32 v218, s76, v218\n" \
"v_mul_f32 v219, s76, v219\n" \
"v_mfma_f32_16x16x32_bf16 a[208:211], a[44:47], a[100:103], a[208:211]\n" \
"s_xor_b32 s89, s89, 0x500000\n" \
"v_cvt_pk_bf16_f32 v216, v216, v217\n" \
"v_cvt_pk_bf16_f32 v217, v218, v219\n" \
"v_mfma_f32_16x16x32_bf16 a[224:227], a[48:51], a[96:99], a[224:227]\n" \
"s_add_u32 m0, s79, 49152\n" \
"v_mul_f32 v220, s76, v220\n" \
"v_mul_f32 v221, s76, v221\n" \
"v_mfma_f32_16x16x32_bf16 a[224:227], a[52:55], a[100:103], a[224:227]\n" \
"buffer_load_dwordx4 v228, s[52:55], s78 offen offset:0 lds\n" \
"v_mfma_f32_16x16x32_bf16 a[228:231], a[48:51], a[104:107], a[228:231]\n" \
"v_mul_f32 v222, s76, v222\n" \
"v_mul_f32 v223, s76, v223\n" \
"v_mfma_f32_16x16x32_bf16 a[228:231], a[52:55], a[108:111], a[228:231]\n" \
"v_cvt_pk_bf16_f32 v218, v220, v221\n" \
"v_cvt_pk_bf16_f32 v219, v222, v223\n" \
"v_mfma_f32_16x16x32_bf16 a[232:235], a[48:51], a[112:115], a[232:235]\n" \
"v_mul_f32 v144, s76, v144\n" \
"v_mul_f32 v145, s76, v145\n" \
"v_mfma_f32_16x16x32_bf16 a[232:235], a[52:55], a[116:119], a[232:235]\n" \
"buffer_load_dwordx4 v229, s[52:55], s78 offen offset:1024 lds\n" \
"v_mfma_f32_16x16x32_bf16 a[236:239], a[48:51], a[120:123], a[236:239]\n" \
"v_mul_f32 v146, s76, v146\n" \
"v_mul_f32 v147, s76, v147\n" \
"v_mfma_f32_16x16x32_bf16 a[236:239], a[52:55], a[124:127], a[236:239]\n" \
"v_cvt_pk_bf16_f32 v196, v144, v145\n" \
"v_cvt_pk_bf16_f32 v197, v146, v147\n" \
"v_mfma_f32_16x16x32_bf16 a[252:255], a[56:59], a[120:123], a[252:255]\n" \
"s_xor_b32 s78, s78, 0x180000\n" \
"v_mul_f32 v148, s76, v148\n" \
"v_mul_f32 v149, s76, v149\n" \
"v_mfma_f32_16x16x32_bf16 a[252:255], a[60:63], a[124:127], a[252:255]\n" \
"buffer_load_dwordx4 v228, s[52:55], s89 offen offset:2048 lds\n" \
"v_mfma_f32_16x16x32_bf16 a[248:251], a[56:59], a[112:115], a[248:251]\n" \
"v_mul_f32 v150, s76, v150\n" \
"v_mul_f32 v151, s76, v151\n" \
"v_mfma_f32_16x16x32_bf16 a[248:251], a[60:63], a[116:119], a[248:251]\n" \
"v_cvt_pk_bf16_f32 v198, v148, v149\n" \
"v_cvt_pk_bf16_f32 v199, v150, v151\n" \
"v_mfma_f32_16x16x32_bf16 a[244:247], a[56:59], a[104:107], a[244:247]\n" \
"v_mul_f32 v176, s76, v176\n" \
"v_mul_f32 v177, s76, v177\n" \
"v_mfma_f32_16x16x32_bf16 a[244:247], a[60:63], a[108:111], a[244:247]\n" \
"buffer_load_dwordx4 v229, s[52:55], s89 offen offset:3072 lds\n" \
"v_mfma_f32_16x16x32_bf16 a[240:243], a[56:59], a[96:99], a[240:243]\n" \
"s_waitcnt lgkmcnt(0)\n" \
"v_mul_f32 v178, s76, v178\n" \
"v_mul_f32 v179, s76, v179\n" \
"v_mfma_f32_16x16x32_bf16 a[240:243], a[60:63], a[100:103], a[240:243]\n" \
"s_xor_b32 s89, s89, 0x180000\n" \
"v_cvt_pk_bf16_f32 v212, v176, v177\n" \
"v_cvt_pk_bf16_f32 v213, v178, v179\n" \
"v_mfma_f32_16x16x32_bf16 v[64:67], a[64:67], a[0:3], 0\n" \
"ds_read_b128 a[24:27], v250 offset:6144\n" \
"v_mul_f32 v180, s76, v180\n" \
"v_mfma_f32_16x16x32_bf16 v[64:67], a[68:71], a[4:7], v[64:67]\n" \
"s_add_u32 m0, s74, 131072\n" \
"ds_read_b128 a[28:31], v251 offset:6144\n" \
"v_mul_f32 v181, s76, v181\n" \
"v_mfma_f32_16x16x32_bf16 v[68:71], a[72:75], a[0:3], 0\n" \
"ds_read_b64_tr_b16 a[96:97], v232 offset:1024\n" \
"v_mul_f32 v182, s76, v182\n" \
"v_mfma_f32_16x16x32_bf16 v[68:71], a[76:79], a[4:7], v[68:71]\n" \
"buffer_load_dwordx4 v224, s[48:51], s75 offen offset:0 lds\n" \
"v_mfma_f32_16x16x32_bf16 v[72:75], a[80:83], a[0:3], 0\n" \
"ds_read_b64_tr_b16 a[98:99], v232 offset:3072\n" \
"v_mul_f32 v183, s76, v183\n" \
"v_mfma_f32_16x16x32_bf16 v[72:75], a[84:87], a[4:7], v[72:75]\n" \
"ds_read_b64_tr_b16 a[100:101], v232 offset:9216\n" \
"v_cvt_pk_bf16_f32 v214, v180, v181\n" \
"v_mfma_f32_16x16x32_bf16 v[76:79], a[88:91], a[0:3], 0\n" \
"ds_read_b64_tr_b16 a[102:103], v232 offset:11264\n" \
"v_cvt_pk_bf16_f32 v215, v182, v183\n" \
"v_mfma_f32_16x16x32_bf16 v[76:79], a[92:95], a[4:7], v[76:79]\n" \
"buffer_load_dwordx4 v225, s[48:51], s75 offen offset:1024 lds\n" \
"v_mfma_f32_16x16x32_bf16 v[108:111], a[88:91], a[8:11], 0\n" \
"ds_read_b64_tr_b16 a[104:105], v233 offset:1024\n" \
"v_mul_f32 v152, s76, v152\n" \
"v_mfma_f32_16x16x32_bf16 v[108:111], a[92:95], a[12:15], v[108:111]\n" \
"ds_read_b64_tr_b16 a[106:107], v233 offset:3072\n" \
"v_mul_f32 v153, s76, v153\n" \
"v_mfma_f32_16x16x32_bf16 v[104:107], a[80:83], a[8:11], 0\n" \
"ds_read_b64_tr_b16 a[108:109], v233 offset:9216\n" \
"v_mul_f32 v154, s76, v154\n" \
"v_mfma_f32_16x16x32_bf16 v[104:107], a[84:87], a[12:15], v[104:107]\n" \
"buffer_load_dwordx4 v226, s[48:51], s75 offen offset:2048 lds\n" \
"v_mfma_f32_16x16x32_bf16 v[100:103], a[72:75], a[8:11], 0\n" \
"ds_read_b64_tr_b16 a[110:111], v233 offset:11264\n" \
"v_mul_f32 v155, s76, v155\n" \
"v_mfma_f32_16x16x32_bf16 v[100:103], a[76:79], a[12:15], v[100:103]\n" \
"ds_read_b64_tr_b16 a[112:113], v232 offset:1536\n" \
"v_cvt_pk_bf16_f32 v204, v152, v153\n" \
"v_mfma_f32_16x16x32_bf16 v[96:99], a[64:67], a[8:11], 0\n" \
"ds_read_b64_tr_b16 a[114:115], v232 offset:3584\n" \
"v_cvt_pk_bf16_f32 v205, v154, v155\n" \
"v_mfma_f32_16x16x32_bf16 v[96:99], a[68:71], a[12:15], v[96:99]\n" \
"buffer_load_dwordx4 v227, s[48:51], s75 offen offset:3072 lds\n" \
"v_mfma_f32_16x16x32_bf16 v[128:131], a[64:67], a[16:19], 0\n" \
"s_xor_b32 s75, s75, 0x180\n" \
"ds_read_b64_tr_b16 a[116:117], v232 offset:9728\n" \
"v_mul_f32 v156, s76, v156\n" \
"v_mfma_f32_16x16x32_bf16 v[128:131], a[68:71], a[20:23], v[128:131]\n" \
"Lpg2214_%=:\n" \
"Lst0x3_%=:\n" \
"s_cbranch_execz Lpg4452_%=\n" \
"s_add_u32 m0, s74, 135168\n" \
"ds_read_b64_tr_b16 a[118:119], v232 offset:11776\n" \
"v_mul_f32 v157, s76, v157\n" \
"v_mfma_f32_16x16x32_bf16 v[132:135], a[72:75], a[16:19], 0\n" \
"ds_read_b64_tr_b16 a[120:121], v233 offset:1536\n" \
"v_mul_f32 v158, s76, v158\n" \
"v_mfma_f32_16x16x32_bf16 v[132:135], a[76:79], a[20:23], v[132:135]\n" \
"buffer_load_dwordx4 v224, s[48:51], s77 offen offset:0 lds\n" \
"v_mfma_f32_16x16x32_bf16 v[136:139], a[80:83], a[16:19], 0\n" \
"ds_read_b64_tr_b16 a[122:123], v233 offset:3584\n" \
"v_mul_f32 v159, s76, v159\n" \
"v_mfma_f32_16x16x32_bf16 v[136:139], a[84:87], a[20:23], v[136:139]\n" \
"ds_read_b64_tr_b16 a[124:125], v233 offset:9728\n" \
"v_cvt_pk_bf16_f32 v206, v156, v157\n" \
"v_mfma_f32_16x16x32_bf16 v[140:143], a[88:91], a[16:19], 0\n" \
"s_waitcnt lgkmcnt(3)\n" \
"ds_read_b64_tr_b16 a[126:127], v233 offset:11776\n" \
"v_cvt_pk_bf16_f32 v207, v158, v159\n" \
"v_mfma_f32_16x16x32_bf16 v[140:143], a[92:95], a[20:23], v[140:143]\n" \
"buffer_load_dwordx4 v225, s[48:51], s77 offen offset:1024 lds\n" \
"v_mfma_f32_16x16x32_bf16 v[172:175], a[88:91], a[24:27], 0\n" \
"s_add_u32 s93, s87, 8192\n" \
"v_mul_f32 v184, s76, v184\n" \
"v_mul_f32 v185, s76, v185\n" \
"v_mfma_f32_16x16x32_bf16 v[172:175], a[92:95], a[28:31], v[172:175]\n" \
"buffer_store_dwordx4 v[192:195], v236, s[56:59], s93 offen offset:0 sc1\n" \
"v_mfma_f32_16x16x32_bf16 v[168:171], a[80:83], a[24:27], 0\n" \
"v_mul_f32 v186, s76, v186\n" \
"v_mul_f32 v187, s76, v187\n" \
"v_mfma_f32_16x16x32_bf16 v[168:171], a[84:87], a[28:31], v[168:171]\n" \
"buffer_load_dwordx4 v226, s[48:51], s77 offen offset:2048 lds\n" \
"v_mfma_f32_16x16x32_bf16 v[164:167], a[72:75], a[24:27], 0\n" \
"v_cvt_pk_bf16_f32 v220, v184, v185\n" \
"v_cvt_pk_bf16_f32 v221, v186, v187\n" \
"v_mfma_f32_16x16x32_bf16 v[164:167], a[76:79], a[28:31], v[164:167]\n" \
"buffer_load_dwordx4 v227, s[48:51], s77 offen offset:3072 lds\n" \
"v_mfma_f32_16x16x32_bf16 v[160:163], a[64:67], a[24:27], 0\n" \
"v_mul_f32 v188, s76, v188\n" \
"v_mul_f32 v189, s76, v189\n" \
"v_mfma_f32_16x16x32_bf16 v[160:163], a[68:71], a[28:31], v[160:163]\n" \
"s_waitcnt vmcnt(43) lgkmcnt(0)\n" \
"s_barrier\n" \
"v_mfma_f32_16x16x32_bf16 v[80:83], a[96:99], a[0:3], 0\n" \
"s_xor_b32 s77, s77, 0x180\n" \
"ds_read_b64_tr_b16 a[64:65], v232 offset:16384\n" \
"v_mul_f32 v190, s76, v190\n" \
"v_mfma_f32_16x16x32_bf16 v[80:83], a[100:103], a[4:7], v[80:83]\n" \
"ds_read_b64_tr_b16 a[66:67], v232 offset:18432\n" \
"v_mul_f32 v191, s76, v191\n" \
"v_mfma_f32_16x16x32_bf16 v[84:87], a[104:107], a[0:3], 0\n" \
"s_add_u32 m0, s79, 0\n" \
"ds_read_b64_tr_b16 a[68:69], v232 offset:24576\n" \
"v_cvt_pk_bf16_f32 v222, v188, v189\n" \
"v_mfma_f32_16x16x32_bf16 v[84:87], a[108:111], a[4:7], v[84:87]\n" \
"buffer_load_dwordx4 v228, s[52:55], s78 offen offset:0 lds\n" \
"v_mfma_f32_16x16x32_bf16 v[88:91], a[112:115], a[0:3], 0\n" \
"ds_read_b64_tr_b16 a[70:71], v232 offset:26624\n" \
"v_cvt_pk_bf16_f32 v223, v190, v191\n" \
"v_mfma_f32_16x16x32_bf16 v[88:91], a[116:119], a[4:7], v[88:91]\n" \
"ds_read_b64_tr_b16 a[72:73], v233 offset:16384\n" \
"v_accvgpr_read_b32 v243, a224\n" \
"v_mfma_f32_16x16x32_bf16 v[92:95], a[120:123], a[0:3], 0\n" \
"ds_read_b64_tr_b16 a[74:75], v233 offset:18432\n" \
"v_accvgpr_read_b32 v252, a225\n" \
"v_mfma_f32_16x16x32_bf16 v[92:95], a[124:127], a[4:7], v[92:95]\n" \
"buffer_load_dwordx4 v229, s[52:55], s78 offen offset:1024 lds\n" \
"v_mfma_f32_16x16x32_bf16 v[124:127], a[120:123], a[8:11], 0\n" \
"s_waitcnt vmcnt(33)\n" \
"ds_read_b128 a[0:3], v230 offset:0\n" \
"v_accvgpr_read_b32 v253, a226\n" \
"v_mfma_f32_16x16x32_bf16 v[124:127], a[124:127], a[12:15], v[124:127]\n" \
"ds_read_b128 a[4:7], v231 offset:0\n" \
"v_accvgpr_read_b32 v254, a227\n" \
"v_mfma_f32_16x16x32_bf16 v[120:123], a[112:115], a[8:11], 0\n" \
"s_xor_b32 s78, s78, 0x300000\n" \
"ds_read_b64_tr_b16 a[76:77], v233 offset:24576\n" \
"v_mul_f32 v243, s31, v243\n" \
"v_mfma_f32_16x16x32_bf16 v[120:123], a[116:119], a[12:15], v[120:123]\n" \
"buffer_load_dwordx4 v228, s[52:55], s89 offen offset:2048 lds\n" \
"v_mfma_f32_16x16x32_bf16 v[116:119], a[104:107], a[8:11], 0\n" \
"ds_read_b64_tr_b16 a[78:79], v233 offset:26624\n" \
"v_mul_f32 v252, s31, v252\n" \
"v_mfma_f32_16x16x32_bf16 v[116:119], a[108:111], a[12:15], v[116:119]\n" \
"ds_read_b64_tr_b16 a[80:81], v232 offset:16896\n" \
"v_mul_f32 v253, s31, v253\n" \
"v_mfma_f32_16x16x32_bf16 v[112:115], a[96:99], a[8:11], 0\n" \
"ds_read_b64_tr_b16 a[82:83], v232 offset:18944\n" \
"v_mul_f32 v254, s31, v254\n" \
"v_mfma_f32_16x16x32_bf16 v[112:115], a[100:103], a[12:15], v[112:115]\n" \
"buffer_load_dwordx4 v229, s[52:55], s89 offen offset:3072 lds\n" \
"v_mfma_f32_16x16x32_bf16 v[144:147], a[96:99], a[16:19], 0\n" \
"s_waitcnt vmcnt(33)\n" \
"s_xor_b32 s89, s89, 0x300000\n" \
"ds_read_b128 a[8:11], v230 offset:2048\n" \
"v_mfma_f32_16x16x32_bf16 v[144:147], a[100:103], a[20:23], v[144:147]\n" \
"s_add_u32 m0, s74, 65536\n" \
"ds_read_b128 a[12:15], v231 offset:2048\n" \
"v_cvt_pk_bf16_f32 v243, v243, v252\n" \
"v_mfma_f32_16x16x32_bf16 v[148:151], a[104:107], a[16:19], 0\n" \
"s_waitcnt lgkmcnt(6)\n" \
"ds_read_b64_tr_b16 a[84:85], v232 offset:25088\n" \
"v_cvt_pk_bf16_f32 v253, v253, v254\n" \
"v_mfma_f32_16x16x32_bf16 v[148:151], a[108:111], a[20:23], v[148:151]\n" \
"buffer_load_dwordx4 v224, s[48:51], s75 offen offset:0 lds\n" \
"v_mfma_f32_16x16x32_bf16 v[152:155], a[112:115], a[16:19], 0\n" \
"ds_read_b64_tr_b16 a[86:87], v232 offset:27136\n" \
"v_accvgpr_write_b32 a224, v243\n" \
"v_mfma_f32_16x16x32_bf16 v[152:155], a[116:119], a[20:23], v[152:155]\n" \
"ds_read_b64_tr_b16 a[88:89], v233 offset:16896\n" \
"v_accvgpr_write_b32 a225, v253\n" \
"v_mfma_f32_16x16x32_bf16 v[156:159], a[120:123], a[16:19], 0\n" \
"ds_read_b64_tr_b16 a[90:91], v233 offset:18944\n" \
"v_accvgpr_read_b32 v243, a240\n" \
"v_mfma_f32_16x16x32_bf16 v[156:159], a[124:127], a[20:23], v[156:159]\n" \
"buffer_load_dwordx4 v225, s[48:51], s75 offen offset:1024 lds\n" \
"v_mfma_f32_16x16x32_bf16 v[188:191], a[120:123], a[24:27], 0\n" \
"s_waitcnt vmcnt(33)\n" \
"ds_read_b128 a[16:19], v230 offset:4096\n" \
"v_accvgpr_read_b32 v252, a241\n" \
"v_mfma_f32_16x16x32_bf16 v[188:191], a[124:127], a[28:31], v[188:191]\n" \
"ds_read_b128 a[20:23], v231 offset:4096\n" \
"v_accvgpr_read_b32 v253, a242\n" \
"v_mfma_f32_16x16x32_bf16 v[184:187], a[112:115], a[24:27], 0\n" \
"s_waitcnt lgkmcnt(6)\n" \
"ds_read_b64_tr_b16 a[92:93], v233 offset:25088\n" \
"v_accvgpr_read_b32 v254, a243\n" \
"v_mfma_f32_16x16x32_bf16 v[184:187], a[116:119], a[28:31], v[184:187]\n" \
"buffer_load_dwordx4 v226, s[48:51], s75 offen offset:2048 lds\n" \
"v_mfma_f32_16x16x32_bf16 v[180:183], a[104:107], a[24:27], 0\n" \
"ds_read_b64_tr_b16 a[94:95], v233 offset:27136\n" \
"v_mul_f32 v243, s31, v243\n" \
"v_mfma_f32_16x16x32_bf16 v[180:183], a[108:111], a[28:31], v[180:183]\n" \
"v_mul_f32 v252, s31, v252\n" \
"v_mul_f32 v253, s31, v253\n" \
"v_mfma_f32_16x16x32_bf16 v[176:179], a[96:99], a[24:27], 0\n" \
"v_mul_f32 v254, s31, v254\n" \
"v_cvt_pk_bf16_f32 v243, v243, v252\n" \
"v_mfma_f32_16x16x32_bf16 v[176:179], a[100:103], a[28:31], v[176:179]\n" \
"buffer_load_dwordx4 v227, s[48:51], s75 offen offset:3072 lds\n" \
"v_mfma_f32_16x16x32_bf16 v[64:67], a[64:67], a[0:3], v[64:67]\n" \
"s_waitcnt vmcnt(33)\n" \
"s_xor_b32 s75, s75, 0x300\n" \
"ds_read_b128 a[24:27], v230 offset:6144\n" \
"v_mfma_f32_16x16x32_bf16 v[64:67], a[68:71], a[4:7], v[64:67]\n" \
"s_add_u32 m0, s74, 69632\n" \
"ds_read_b128 a[28:31], v231 offset:6144\n" \
"v_cvt_pk_bf16_f32 v253, v253, v254\n" \
"v_mfma_f32_16x16x32_bf16 v[68:71], a[72:75], a[0:3], v[68:71]\n" \
"s_waitcnt lgkmcnt(4)\n" \
"ds_read_b64_tr_b16 a[96:97], v232 offset:17408\n" \
"v_accvgpr_write_b32 a226, v243\n" \
"v_mfma_f32_16x16x32_bf16 v[68:71], a[76:79], a[4:7], v[68:71]\n" \
"buffer_load_dwordx4 v224, s[48:51], s77 offen offset:0 lds\n" \
"v_mfma_f32_16x16x32_bf16 v[72:75], a[80:83], a[0:3], v[72:75]\n" \
"ds_read_b64_tr_b16 a[98:99], v232 offset:19456\n" \
"v_accvgpr_write_b32 a227, v253\n" \
"v_mfma_f32_16x16x32_bf16 v[72:75], a[84:87], a[4:7], v[72:75]\n" \
"s_waitcnt lgkmcnt(3)\n" \
"ds_read_b64_tr_b16 a[100:101], v232 offset:25600\n" \
"v_accvgpr_read_b32 v243, a228\n" \
"v_mfma_f32_16x16x32_bf16 v[76:79], a[88:91], a[0:3], v[76:79]\n" \
"ds_read_b64_tr_b16 a[102:103], v232 offset:27648\n" \
"v_accvgpr_read_b32 v252, a229\n" \
"v_mfma_f32_16x16x32_bf16 v[76:79], a[92:95], a[4:7], v[76:79]\n" \
"buffer_load_dwordx4 v225, s[48:51], s77 offen offset:1024 lds\n" \
"v_mfma_f32_16x16x32_bf16 v[108:111], a[88:91], a[8:11], v[108:111]\n" \
"ds_read_b64_tr_b16 a[104:105], v233 offset:17408\n" \
"v_accvgpr_read_b32 v253, a230\n" \
"v_mfma_f32_16x16x32_bf16 v[108:111], a[92:95], a[12:15], v[108:111]\n" \
"ds_read_b64_tr_b16 a[106:107], v233 offset:19456\n" \
"v_accvgpr_read_b32 v254, a231\n" \
"v_mfma_f32_16x16x32_bf16 v[104:107], a[80:83], a[8:11], v[104:107]\n" \
"s_waitcnt lgkmcnt(6)\n" \
"ds_read_b64_tr_b16 a[108:109], v233 offset:25600\n" \
"v_mul_f32 v243, s31, v243\n" \
"v_mfma_f32_16x16x32_bf16 v[104:107], a[84:87], a[12:15], v[104:107]\n" \
"buffer_load_dwordx4 v226, s[48:51], s77 offen offset:2048 lds\n" \
"v_mfma_f32_16x16x32_bf16 v[100:103], a[72:75], a[8:11], v[100:103]\n" \
"ds_read_b64_tr_b16 a[110:111], v233 offset:27648\n" \
"v_mul_f32 v252, s31, v252\n" \
"v_mfma_f32_16x16x32_bf16 v[100:103], a[76:79], a[12:15], v[100:103]\n" \
"ds_read_b64_tr_b16 a[112:113], v232 offset:17920\n" \
"v_mul_f32 v253, s31, v253\n" \
"v_mfma_f32_16x16x32_bf16 v[96:99], a[64:67], a[8:11], v[96:99]\n" \
"ds_read_b64_tr_b16 a[114:115], v232 offset:19968\n" \
"v_mul_f32 v254, s31, v254\n" \
"v_mfma_f32_16x16x32_bf16 v[96:99], a[68:71], a[12:15], v[96:99]\n" \
"buffer_load_dwordx4 v227, s[48:51], s77 offen offset:3072 lds\n" \
"v_mfma_f32_16x16x32_bf16 v[128:131], a[64:67], a[16:19], v[128:131]\n" \
"ds_read_b64_tr_b16 a[116:117], v232 offset:26112\n" \
"v_cvt_pk_bf16_f32 v243, v243, v252\n" \
"v_mfma_f32_16x16x32_bf16 v[128:131], a[68:71], a[20:23], v[128:131]\n" \
"ds_read_b64_tr_b16 a[118:119], v232 offset:28160\n" \
"v_cvt_pk_bf16_f32 v253, v253, v254\n" \
"v_mfma_f32_16x16x32_bf16 v[132:135], a[72:75], a[16:19], v[132:135]\n" \
"s_xor_b32 s77, s77, 0x300\n" \
"ds_read_b64_tr_b16 a[120:121], v233 offset:17920\n" \
"v_accvgpr_write_b32 a228, v243\n" \
"v_mfma_f32_16x16x32_bf16 v[132:135], a[76:79], a[20:23], v[132:135]\n" \
"v_accvgpr_write_b32 a229, v253\n" \
"v_accvgpr_read_b32 v243, a244\n" \
"v_mfma_f32_16x16x32_bf16 v[136:139], a[80:83], a[16:19], v[136:139]\n" \
"ds_read_b64_tr_b16 a[122:123], v233 offset:19968\n" \
"v_accvgpr_read_b32 v252, a245\n" \
"v_mfma_f32_16x16x32_bf16 v[136:139], a[84:87], a[20:23], v[136:139]\n" \
"ds_read_b64_tr_b16 a[124:125], v233 offset:26112\n" \
"v_accvgpr_read_b32 v253, a246\n" \
"v_mfma_f32_16x16x32_bf16 v[140:143], a[88:91], a[16:19], v[140:143]\n" \
"ds_read_b64_tr_b16 a[126:127], v233 offset:28160\n" \
"v_accvgpr_read_b32 v254, a247\n" \
"v_mfma_f32_16x16x32_bf16 v[140:143], a[92:95], a[20:23], v[140:143]\n" \
"buffer_store_dwordx4 v[208:211], v236, s[56:59], s93 offen offset:256 sc1\n" \
"v_mfma_f32_16x16x32_bf16 v[172:175], a[88:91], a[24:27], v[172:175]\n" \
"v_mul_f32 v243, s31, v243\n" \
"v_mul_f32 v252, s31, v252\n" \
"v_mfma_f32_16x16x32_bf16 v[172:175], a[92:95], a[28:31], v[172:175]\n" \
"v_mul_f32 v253, s31, v253\n" \
"v_mul_f32 v254, s31, v254\n" \
"v_mfma_f32_16x16x32_bf16 v[168:171], a[80:83], a[24:27], v[168:171]\n" \
"v_cvt_pk_bf16_f32 v243, v243, v252\n" \
"v_cvt_pk_bf16_f32 v253, v253, v254\n" \
"v_mfma_f32_16x16x32_bf16 v[168:171], a[84:87], a[28:31], v[168:171]\n" \
"v_accvgpr_write_b32 a230, v243\n" \
"v_accvgpr_write_b32 a231, v253\n" \
"v_mfma_f32_16x16x32_bf16 v[164:167], a[72:75], a[24:27], v[164:167]\n" \
"v_accvgpr_read_b32 v243, a232\n" \
"v_accvgpr_read_b32 v252, a233\n" \
"v_mfma_f32_16x16x32_bf16 v[164:167], a[76:79], a[28:31], v[164:167]\n" \
"v_accvgpr_read_b32 v253, a234\n" \
"v_accvgpr_read_b32 v254, a235\n" \
"v_mfma_f32_16x16x32_bf16 v[160:163], a[64:67], a[24:27], v[160:163]\n" \
"v_mul_f32 v243, s31, v243\n" \
"v_mul_f32 v252, s31, v252\n" \
"v_mfma_f32_16x16x32_bf16 v[160:163], a[68:71], a[28:31], v[160:163]\n" \
"s_waitcnt vmcnt(26) lgkmcnt(0)\n" \
"s_barrier\n" \
"v_mfma_f32_16x16x32_bf16 v[80:83], a[96:99], a[0:3], v[80:83]\n" \
"ds_read_b64_tr_b16 a[64:65], v232 offset:32768\n" \
"v_mul_f32 v253, s31, v253\n" \
"v_mfma_f32_16x16x32_bf16 v[80:83], a[100:103], a[4:7], v[80:83]\n" \
"ds_read_b64_tr_b16 a[66:67], v232 offset:34816\n" \
"v_mul_f32 v254, s31, v254\n" \
"v_mfma_f32_16x16x32_bf16 v[84:87], a[104:107], a[0:3], v[84:87]\n" \
"s_add_u32 m0, s79, 16384\n" \
"ds_read_b64_tr_b16 a[68:69], v232 offset:40960\n" \
"v_cvt_pk_bf16_f32 v243, v243, v252\n" \
"v_mfma_f32_16x16x32_bf16 v[84:87], a[108:111], a[4:7], v[84:87]\n" \
"buffer_load_dwordx4 v228, s[52:55], s78 offen offset:0 lds\n" \
"v_mfma_f32_16x16x32_bf16 v[88:91], a[112:115], a[0:3], v[88:91]\n" \
"ds_read_b64_tr_b16 a[70:71], v232 offset:43008\n" \
"v_cvt_pk_bf16_f32 v253, v253, v254\n" \
"v_mfma_f32_16x16x32_bf16 v[88:91], a[116:119], a[4:7], v[88:91]\n" \
"ds_read_b64_tr_b16 a[72:73], v233 offset:32768\n" \
"v_accvgpr_write_b32 a232, v243\n" \
"v_mfma_f32_16x16x32_bf16 v[92:95], a[120:123], a[0:3], v[92:95]\n" \
"ds_read_b64_tr_b16 a[74:75], v233 offset:34816\n" \
"v_accvgpr_write_b32 a233, v253\n" \
"v_mfma_f32_16x16x32_bf16 v[92:95], a[124:127], a[4:7], v[92:95]\n" \
"buffer_load_dwordx4 v229, s[52:55], s78 offen offset:1024 lds\n" \
"v_mfma_f32_16x16x32_bf16 v[124:127], a[120:123], a[8:11], v[124:127]\n" \
"ds_read_b128 a[0:3], v230 offset:32768\n" \
"v_accvgpr_read_b32 v243, a248\n" \
"v_mfma_f32_16x16x32_bf16 v[124:127], a[124:127], a[12:15], v[124:127]\n" \
"ds_read_b128 a[4:7], v231 offset:32768\n" \
"v_accvgpr_read_b32 v252, a249\n" \
"v_mfma_f32_16x16x32_bf16 v[120:123], a[112:115], a[8:11], v[120:123]\n" \
"s_xor_b32 s78, s78, 0x80000\n" \
"ds_read_b64_tr_b16 a[76:77], v233 offset:40960\n" \
"v_accvgpr_read_b32 v253, a250\n" \
"v_mfma_f32_16x16x32_bf16 v[120:123], a[116:119], a[12:15], v[120:123]\n" \
"buffer_load_dwordx4 v228, s[52:55], s89 offen offset:2048 lds\n" \
"v_mfma_f32_16x16x32_bf16 v[116:119], a[104:107], a[8:11], v[116:119]\n" \
"ds_read_b64_tr_b16 a[78:79], v233 offset:43008\n" \
"v_accvgpr_read_b32 v254, a251\n" \
"v_mfma_f32_16x16x32_bf16 v[116:119], a[108:111], a[12:15], v[116:119]\n" \
"ds_read_b64_tr_b16 a[80:81], v232 offset:33280\n" \
"v_mul_f32 v243, s31, v243\n" \
"v_mfma_f32_16x16x32_bf16 v[112:115], a[96:99], a[8:11], v[112:115]\n" \
"ds_read_b64_tr_b16 a[82:83], v232 offset:35328\n" \
"v_mul_f32 v252, s31, v252\n" \
"v_mfma_f32_16x16x32_bf16 v[112:115], a[100:103], a[12:15], v[112:115]\n" \
"buffer_load_dwordx4 v229, s[52:55], s89 offen offset:3072 lds\n" \
"v_mfma_f32_16x16x32_bf16 v[144:147], a[96:99], a[16:19], v[144:147]\n" \
"s_xor_b32 s89, s89, 0x80000\n" \
"ds_read_b128 a[8:11], v230 offset:34816\n" \
"v_mfma_f32_16x16x32_bf16 v[144:147], a[100:103], a[20:23], v[144:147]\n" \
"s_add_u32 m0, s74, 98304\n" \
"ds_read_b128 a[12:15], v231 offset:34816\n" \
"v_mul_f32 v253, s31, v253\n" \
"v_mfma_f32_16x16x32_bf16 v[148:151], a[104:107], a[16:19], v[148:151]\n" \
"s_waitcnt lgkmcnt(6)\n" \
"ds_read_b64_tr_b16 a[84:85], v232 offset:41472\n" \
"v_mul_f32 v254, s31, v254\n" \
"v_mfma_f32_16x16x32_bf16 v[148:151], a[108:111], a[20:23], v[148:151]\n" \
"buffer_load_dwordx4 v224, s[48:51], s75 offen offset:0 lds\n" \
"v_mfma_f32_16x16x32_bf16 v[152:155], a[112:115], a[16:19], v[152:155]\n" \
"ds_read_b64_tr_b16 a[86:87], v232 offset:43520\n" \
"v_cvt_pk_bf16_f32 v243, v243, v252\n" \
"v_mfma_f32_16x16x32_bf16 v[152:155], a[116:119], a[20:23], v[152:155]\n" \
"ds_read_b64_tr_b16 a[88:89], v233 offset:33280\n" \
"v_cvt_pk_bf16_f32 v253, v253, v254\n" \
"v_mfma_f32_16x16x32_bf16 v[156:159], a[120:123], a[16:19], v[156:159]\n" \
"ds_read_b64_tr_b16 a[90:91], v233 offset:35328\n" \
"v_accvgpr_write_b32 a234, v243\n" \
"v_mfma_f32_16x16x32_bf16 v[156:159], a[124:127], a[20:23], v[156:159]\n" \
"buffer_load_dwordx4 v225, s[48:51], s75 offen offset:1024 lds\n" \
"v_mfma_f32_16x16x32_bf16 v[188:191], a[120:123], a[24:27], v[188:191]\n" \
"ds_read_b128 a[16:19], v230 offset:36864\n" \
"v_accvgpr_write_b32 a235, v253\n" \
"v_mfma_f32_16x16x32_bf16 v[188:191], a[124:127], a[28:31], v[188:191]\n" \
"ds_read_b128 a[20:23], v231 offset:36864\n" \
"v_accvgpr_read_b32 v243, a236\n" \
"v_mfma_f32_16x16x32_bf16 v[184:187], a[112:115], a[24:27], v[184:187]\n" \
"s_waitcnt lgkmcnt(6)\n" \
"ds_read_b64_tr_b16 a[92:93], v233 offset:41472\n" \
"v_accvgpr_read_b32 v252, a237\n" \
"v_mfma_f32_16x16x32_bf16 v[184:187], a[116:119], a[28:31], v[184:187]\n" \
"buffer_load_dwordx4 v226, s[48:51], s75 offen offset:2048 lds\n" \
"v_mfma_f32_16x16x32_bf16 v[180:183], a[104:107], a[24:27], v[180:183]\n" \
"ds_read_b64_tr_b16 a[94:95], v233 offset:43520\n" \
"v_accvgpr_read_b32 v253, a238\n" \
"v_mfma_f32_16x16x32_bf16 v[180:183], a[108:111], a[28:31], v[180:183]\n" \
"v_accvgpr_read_b32 v254, a239\n" \
"v_mul_f32 v243, s31, v243\n" \
"v_mfma_f32_16x16x32_bf16 v[176:179], a[96:99], a[24:27], v[176:179]\n" \
"v_mul_f32 v252, s31, v252\n" \
"v_mul_f32 v253, s31, v253\n" \
"v_mfma_f32_16x16x32_bf16 v[176:179], a[100:103], a[28:31], v[176:179]\n" \
"buffer_load_dwordx4 v227, s[48:51], s75 offen offset:3072 lds\n" \
"v_mfma_f32_16x16x32_bf16 v[64:67], a[64:67], a[0:3], v[64:67]\n" \
"s_xor_b32 s75, s75, 0x80\n" \
"ds_read_b128 a[24:27], v230 offset:38912\n" \
"v_mfma_f32_16x16x32_bf16 v[64:67], a[68:71], a[4:7], v[64:67]\n" \
"s_add_u32 m0, s74, 102400\n" \
"ds_read_b128 a[28:31], v231 offset:38912\n" \
"v_mul_f32 v254, s31, v254\n" \
"v_mfma_f32_16x16x32_bf16 v[68:71], a[72:75], a[0:3], v[68:71]\n" \
"s_waitcnt lgkmcnt(4)\n" \
"ds_read_b64_tr_b16 a[96:97], v232 offset:33792\n" \
"v_cvt_pk_bf16_f32 v243, v243, v252\n" \
"v_mfma_f32_16x16x32_bf16 v[68:71], a[76:79], a[4:7], v[68:71]\n" \
"buffer_load_dwordx4 v224, s[48:51], s77 offen offset:0 lds\n" \
"v_mfma_f32_16x16x32_bf16 v[72:75], a[80:83], a[0:3], v[72:75]\n" \
"ds_read_b64_tr_b16 a[98:99], v232 offset:35840\n" \
"v_cvt_pk_bf16_f32 v253, v253, v254\n" \
"v_mfma_f32_16x16x32_bf16 v[72:75], a[84:87], a[4:7], v[72:75]\n" \
"s_waitcnt lgkmcnt(3)\n" \
"ds_read_b64_tr_b16 a[100:101], v232 offset:41984\n" \
"v_accvgpr_write_b32 a236, v243\n" \
"v_mfma_f32_16x16x32_bf16 v[76:79], a[88:91], a[0:3], v[76:79]\n" \
"ds_read_b64_tr_b16 a[102:103], v232 offset:44032\n" \
"v_accvgpr_write_b32 a237, v253\n" \
"v_mfma_f32_16x16x32_bf16 v[76:79], a[92:95], a[4:7], v[76:79]\n" \
"buffer_load_dwordx4 v225, s[48:51], s77 offen offset:1024 lds\n" \
"v_mfma_f32_16x16x32_bf16 v[108:111], a[88:91], a[8:11], v[108:111]\n" \
"ds_read_b64_tr_b16 a[104:105], v233 offset:33792\n" \
"v_accvgpr_read_b32 v243, a252\n" \
"v_mfma_f32_16x16x32_bf16 v[108:111], a[92:95], a[12:15], v[108:111]\n" \
"ds_read_b64_tr_b16 a[106:107], v233 offset:35840\n" \
"v_accvgpr_read_b32 v252, a253\n" \
"v_mfma_f32_16x16x32_bf16 v[104:107], a[80:83], a[8:11], v[104:107]\n" \
"s_waitcnt lgkmcnt(6)\n" \
"ds_read_b64_tr_b16 a[108:109], v233 offset:41984\n" \
"v_accvgpr_read_b32 v253, a254\n" \
"v_mfma_f32_16x16x32_bf16 v[104:107], a[84:87], a[12:15], v[104:107]\n" \
"buffer_load_dwordx4 v226, s[48:51], s77 offen offset:2048 lds\n" \
"v_mfma_f32_16x16x32_bf16 v[100:103], a[72:75], a[8:11], v[100:103]\n" \
"ds_read_b64_tr_b16 a[110:111], v233 offset:44032\n" \
"v_accvgpr_read_b32 v254, a255\n" \
"v_mfma_f32_16x16x32_bf16 v[100:103], a[76:79], a[12:15], v[100:103]\n" \
"ds_read_b64_tr_b16 a[112:113], v232 offset:34304\n" \
"v_mul_f32 v243, s31, v243\n" \
"v_mfma_f32_16x16x32_bf16 v[96:99], a[64:67], a[8:11], v[96:99]\n" \
"ds_read_b64_tr_b16 a[114:115], v232 offset:36352\n" \
"v_mul_f32 v252, s31, v252\n" \
"v_mfma_f32_16x16x32_bf16 v[96:99], a[68:71], a[12:15], v[96:99]\n" \
"buffer_load_dwordx4 v227, s[48:51], s77 offen offset:3072 lds\n" \
"v_mfma_f32_16x16x32_bf16 v[128:131], a[64:67], a[16:19], v[128:131]\n" \
"ds_read_b64_tr_b16 a[116:117], v232 offset:42496\n" \
"v_mul_f32 v253, s31, v253\n" \
"v_mfma_f32_16x16x32_bf16 v[128:131], a[68:71], a[20:23], v[128:131]\n" \
"ds_read_b64_tr_b16 a[118:119], v232 offset:44544\n" \
"v_mul_f32 v254, s31, v254\n" \
"v_mfma_f32_16x16x32_bf16 v[132:135], a[72:75], a[16:19], v[132:135]\n" \
"s_xor_b32 s77, s77, 0x80\n" \
"ds_read_b64_tr_b16 a[120:121], v233 offset:34304\n" \
"v_cvt_pk_bf16_f32 v243, v243, v252\n" \
"v_mfma_f32_16x16x32_bf16 v[132:135], a[76:79], a[20:23], v[132:135]\n" \
"v_cvt_pk_bf16_f32 v253, v253, v254\n" \
"v_accvgpr_write_b32 a238, v243\n" \
"v_mfma_f32_16x16x32_bf16 v[136:139], a[80:83], a[16:19], v[136:139]\n" \
"ds_read_b64_tr_b16 a[122:123], v233 offset:36352\n" \
"v_accvgpr_write_b32 a239, v253\n" \
"v_mfma_f32_16x16x32_bf16 v[136:139], a[84:87], a[20:23], v[136:139]\n" \
"ds_read_b64_tr_b16 a[124:125], v233 offset:42496\n" \
"v_accvgpr_read_b32 v243, a192\n" \
"v_mfma_f32_16x16x32_bf16 v[140:143], a[88:91], a[16:19], v[140:143]\n" \
"ds_read_b64_tr_b16 a[126:127], v233 offset:44544\n" \
"v_accvgpr_read_b32 v252, a193\n" \
"v_mfma_f32_16x16x32_bf16 v[140:143], a[92:95], a[20:23], v[140:143]\n" \
"buffer_store_dwordx4 v[200:203], v236, s[56:59], s93 offen offset:2048 sc1\n" \
"v_mfma_f32_16x16x32_bf16 v[172:175], a[88:91], a[24:27], v[172:175]\n" \
"v_accvgpr_read_b32 v253, a194\n" \
"v_accvgpr_read_b32 v254, a195\n" \
"v_mfma_f32_16x16x32_bf16 v[172:175], a[92:95], a[28:31], v[172:175]\n" \
"v_mul_f32 v243, s31, v243\n" \
"v_mul_f32 v252, s31, v252\n" \
"v_mfma_f32_16x16x32_bf16 v[168:171], a[80:83], a[24:27], v[168:171]\n" \
"v_mul_f32 v253, s31, v253\n" \
"v_mul_f32 v254, s31, v254\n" \
"v_mfma_f32_16x16x32_bf16 v[168:171], a[84:87], a[28:31], v[168:171]\n" \
"v_cvt_pk_bf16_f32 v243, v243, v252\n" \
"v_cvt_pk_bf16_f32 v253, v253, v254\n" \
"v_mfma_f32_16x16x32_bf16 v[164:167], a[72:75], a[24:27], v[164:167]\n" \
"v_accvgpr_write_b32 a192, v243\n" \
"v_accvgpr_write_b32 a193, v253\n" \
"v_mfma_f32_16x16x32_bf16 v[164:167], a[76:79], a[28:31], v[164:167]\n" \
"v_accvgpr_read_b32 v243, a208\n" \
"v_accvgpr_read_b32 v252, a209\n" \
"v_mfma_f32_16x16x32_bf16 v[160:163], a[64:67], a[24:27], v[160:163]\n" \
"v_accvgpr_read_b32 v253, a210\n" \
"v_accvgpr_read_b32 v254, a211\n" \
"v_mfma_f32_16x16x32_bf16 v[160:163], a[68:71], a[28:31], v[160:163]\n" \
"s_waitcnt vmcnt(35) lgkmcnt(0)\n" \
"s_barrier\n" \
"v_mfma_f32_16x16x32_bf16 v[80:83], a[96:99], a[0:3], v[80:83]\n" \
"ds_read_b64_tr_b16 a[64:65], v232 offset:49152\n" \
"v_mul_f32 v243, s31, v243\n" \
"v_mfma_f32_16x16x32_bf16 v[80:83], a[100:103], a[4:7], v[80:83]\n" \
"ds_read_b64_tr_b16 a[66:67], v232 offset:51200\n" \
"v_mul_f32 v252, s31, v252\n" \
"v_mfma_f32_16x16x32_bf16 v[84:87], a[104:107], a[0:3], v[84:87]\n" \
"s_add_u32 m0, s79, 32768\n" \
"ds_read_b64_tr_b16 a[68:69], v232 offset:57344\n" \
"v_mul_f32 v253, s31, v253\n" \
"v_mfma_f32_16x16x32_bf16 v[84:87], a[108:111], a[4:7], v[84:87]\n" \
"buffer_load_dwordx4 v228, s[52:55], s78 offen offset:0 lds\n" \
"v_mfma_f32_16x16x32_bf16 v[88:91], a[112:115], a[0:3], v[88:91]\n" \
"ds_read_b64_tr_b16 a[70:71], v232 offset:59392\n" \
"v_mul_f32 v254, s31, v254\n" \
"v_mfma_f32_16x16x32_bf16 v[88:91], a[116:119], a[4:7], v[88:91]\n" \
"ds_read_b64_tr_b16 a[72:73], v233 offset:49152\n" \
"v_cvt_pk_bf16_f32 v243, v243, v252\n" \
"v_mfma_f32_16x16x32_bf16 v[92:95], a[120:123], a[0:3], v[92:95]\n" \
"ds_read_b64_tr_b16 a[74:75], v233 offset:51200\n" \
"v_cvt_pk_bf16_f32 v253, v253, v254\n" \
"v_mfma_f32_16x16x32_bf16 v[92:95], a[124:127], a[4:7], v[92:95]\n" \
"buffer_load_dwordx4 v229, s[52:55], s78 offen offset:1024 lds\n" \
"v_mfma_f32_16x16x32_bf16 v[124:127], a[120:123], a[8:11], v[124:127]\n" \
"s_waitcnt vmcnt(35)\n" \
"ds_read_b128 a[0:3], v250 offset:0\n" \
"v_accvgpr_write_b32 a194, v243\n" \
"v_mfma_f32_16x16x32_bf16 v[124:127], a[124:127], a[12:15], v[124:127]\n" \
"ds_read_b128 a[4:7], v251 offset:0\n" \
"v_accvgpr_write_b32 a195, v253\n" \
"v_mfma_f32_16x16x32_bf16 v[120:123], a[112:115], a[8:11], v[120:123]\n" \
"s_xor_b32 s78, s78, 0x180000\n" \
"ds_read_b64_tr_b16 a[76:77], v233 offset:57344\n" \
"v_accvgpr_read_b32 v243, a196\n" \
"v_mfma_f32_16x16x32_bf16 v[120:123], a[116:119], a[12:15], v[120:123]\n" \
"buffer_load_dwordx4 v228, s[52:55], s89 offen offset:2048 lds\n" \
"v_mfma_f32_16x16x32_bf16 v[116:119], a[104:107], a[8:11], v[116:119]\n" \
"ds_read_b64_tr_b16 a[78:79], v233 offset:59392\n" \
"v_accvgpr_read_b32 v252, a197\n" \
"v_mfma_f32_16x16x32_bf16 v[116:119], a[108:111], a[12:15], v[116:119]\n" \
"ds_read_b64_tr_b16 a[80:81], v232 offset:49664\n" \
"v_accvgpr_read_b32 v253, a198\n" \
"v_mfma_f32_16x16x32_bf16 v[112:115], a[96:99], a[8:11], v[112:115]\n" \
"ds_read_b64_tr_b16 a[82:83], v232 offset:51712\n" \
"v_accvgpr_read_b32 v254, a199\n" \
"v_mfma_f32_16x16x32_bf16 v[112:115], a[100:103], a[12:15], v[112:115]\n" \
"buffer_load_dwordx4 v229, s[52:55], s89 offen offset:3072 lds\n" \
"v_mfma_f32_16x16x32_bf16 v[144:147], a[96:99], a[16:19], v[144:147]\n" \
"s_waitcnt vmcnt(35)\n" \
"s_xor_b32 s89, s89, 0x180000\n" \
"ds_read_b128 a[8:11], v250 offset:2048\n" \
"v_mfma_f32_16x16x32_bf16 v[144:147], a[100:103], a[20:23], v[144:147]\n" \
"s_add_u32 m0, s74, 131072\n" \
"ds_read_b128 a[12:15], v251 offset:2048\n" \
"v_mul_f32 v243, s31, v243\n" \
"v_mfma_f32_16x16x32_bf16 v[148:151], a[104:107], a[16:19], v[148:151]\n" \
"s_waitcnt lgkmcnt(6)\n" \
"ds_read_b64_tr_b16 a[84:85], v232 offset:57856\n" \
"v_mul_f32 v252, s31, v252\n" \
"v_mfma_f32_16x16x32_bf16 v[148:151], a[108:111], a[20:23], v[148:151]\n" \
"buffer_load_dwordx4 v224, s[48:51], s75 offen offset:0 lds\n" \
"v_mfma_f32_16x16x32_bf16 v[152:155], a[112:115], a[16:19], v[152:155]\n" \
"ds_read_b64_tr_b16 a[86:87], v232 offset:59904\n" \
"v_mul_f32 v253, s31, v253\n" \
"v_mfma_f32_16x16x32_bf16 v[152:155], a[116:119], a[20:23], v[152:155]\n" \
"ds_read_b64_tr_b16 a[88:89], v233 offset:49664\n" \
"v_mul_f32 v254, s31, v254\n" \
"v_mfma_f32_16x16x32_bf16 v[156:159], a[120:123], a[16:19], v[156:159]\n" \
"ds_read_b64_tr_b16 a[90:91], v233 offset:51712\n" \
"v_cvt_pk_bf16_f32 v243, v243, v252\n" \
"v_mfma_f32_16x16x32_bf16 v[156:159], a[124:127], a[20:23], v[156:159]\n" \
"buffer_load_dwordx4 v225, s[48:51], s75 offen offset:1024 lds\n" \
"v_mfma_f32_16x16x32_bf16 v[188:191], a[120:123], a[24:27], v[188:191]\n" \
"s_waitcnt vmcnt(35)\n" \
"ds_read_b128 a[16:19], v250 offset:4096\n" \
"v_cvt_pk_bf16_f32 v253, v253, v254\n" \
"v_mfma_f32_16x16x32_bf16 v[188:191], a[124:127], a[28:31], v[188:191]\n" \
"ds_read_b128 a[20:23], v251 offset:4096\n" \
"v_accvgpr_write_b32 a196, v243\n" \
"v_mfma_f32_16x16x32_bf16 v[184:187], a[112:115], a[24:27], v[184:187]\n" \
"s_waitcnt lgkmcnt(6)\n" \
"ds_read_b64_tr_b16 a[92:93], v233 offset:57856\n" \
"v_accvgpr_write_b32 a197, v253\n" \
"v_mfma_f32_16x16x32_bf16 v[184:187], a[116:119], a[28:31], v[184:187]\n" \
"buffer_load_dwordx4 v226, s[48:51], s75 offen offset:2048 lds\n" \
"v_mfma_f32_16x16x32_bf16 v[180:183], a[104:107], a[24:27], v[180:183]\n" \
"ds_read_b64_tr_b16 a[94:95], v233 offset:59904\n" \
"v_accvgpr_read_b32 v243, a212\n" \
"v_mfma_f32_16x16x32_bf16 v[180:183], a[108:111], a[28:31], v[180:183]\n" \
"v_accvgpr_read_b32 v252, a213\n" \
"v_accvgpr_read_b32 v253, a214\n" \
"v_mfma_f32_16x16x32_bf16 v[176:179], a[96:99], a[24:27], v[176:179]\n" \
"v_accvgpr_read_b32 v254, a215\n" \
"v_mul_f32 v243, s31, v243\n" \
"v_mfma_f32_16x16x32_bf16 v[176:179], a[100:103], a[28:31], v[176:179]\n" \
"buffer_load_dwordx4 v227, s[48:51], s75 offen offset:3072 lds\n" \
"v_mfma_f32_16x16x32_bf16 v[64:67], a[64:67], a[0:3], v[64:67]\n" \
"s_waitcnt vmcnt(34)\n" \
"s_xor_b32 s75, s75, 0x180\n" \
"ds_read_b128 a[24:27], v250 offset:6144\n" \
"v_mfma_f32_16x16x32_bf16 v[64:67], a[68:71], a[4:7], v[64:67]\n" \
"s_add_u32 m0, s74, 135168\n" \
"ds_read_b128 a[28:31], v251 offset:6144\n" \
"v_mul_f32 v252, s31, v252\n" \
"v_mfma_f32_16x16x32_bf16 v[68:71], a[72:75], a[0:3], v[68:71]\n" \
"s_waitcnt lgkmcnt(4)\n" \
"ds_read_b64_tr_b16 a[96:97], v232 offset:50176\n" \
"v_mul_f32 v253, s31, v253\n" \
"v_mfma_f32_16x16x32_bf16 v[68:71], a[76:79], a[4:7], v[68:71]\n" \
"buffer_load_dwordx4 v224, s[48:51], s77 offen offset:0 lds\n" \
"v_mfma_f32_16x16x32_bf16 v[72:75], a[80:83], a[0:3], v[72:75]\n" \
"ds_read_b64_tr_b16 a[98:99], v232 offset:52224\n" \
"v_mul_f32 v254, s31, v254\n" \
"v_mfma_f32_16x16x32_bf16 v[72:75], a[84:87], a[4:7], v[72:75]\n" \
"s_waitcnt lgkmcnt(3)\n" \
"ds_read_b64_tr_b16 a[100:101], v232 offset:58368\n" \
"v_cvt_pk_bf16_f32 v243, v243, v252\n" \
"v_mfma_f32_16x16x32_bf16 v[76:79], a[88:91], a[0:3], v[76:79]\n" \
"ds_read_b64_tr_b16 a[102:103], v232 offset:60416\n" \
"v_cvt_pk_bf16_f32 v253, v253, v254\n" \
"v_mfma_f32_16x16x32_bf16 v[76:79], a[92:95], a[4:7], v[76:79]\n" \
"buffer_load_dwordx4 v225, s[48:51], s77 offen offset:1024 lds\n" \
"v_mfma_f32_16x16x32_bf16 v[108:111], a[88:91], a[8:11], v[108:111]\n" \
"ds_read_b64_tr_b16 a[104:105], v233 offset:50176\n" \
"v_accvgpr_write_b32 a198, v243\n" \
"v_mfma_f32_16x16x32_bf16 v[108:111], a[92:95], a[12:15], v[108:111]\n" \
"ds_read_b64_tr_b16 a[106:107], v233 offset:52224\n" \
"v_accvgpr_write_b32 a199, v253\n" \
"v_mfma_f32_16x16x32_bf16 v[104:107], a[80:83], a[8:11], v[104:107]\n" \
"s_waitcnt lgkmcnt(6)\n" \
"ds_read_b64_tr_b16 a[108:109], v233 offset:58368\n" \
"v_accvgpr_read_b32 v243, a200\n" \
"Lpg2772_%=:\n" \
"s_cbranch_execz Lpg5018_%=\n" \
"v_mfma_f32_16x16x32_bf16 v[104:107], a[84:87], a[12:15], v[104:107]\n" \
"buffer_load_dwordx4 v226, s[48:51], s77 offen offset:2048 lds\n" \
"v_mfma_f32_16x16x32_bf16 v[100:103], a[72:75], a[8:11], v[100:103]\n" \
"ds_read_b64_tr_b16 a[110:111], v233 offset:60416\n" \
"v_accvgpr_read_b32 v252, a201\n" \
"v_mfma_f32_16x16x32_bf16 v[100:103], a[76:79], a[12:15], v[100:103]\n" \
"ds_read_b64_tr_b16 a[112:113], v232 offset:50688\n" \
"v_accvgpr_read_b32 v253, a202\n" \
"v_mfma_f32_16x16x32_bf16 v[96:99], a[64:67], a[8:11], v[96:99]\n" \
"ds_read_b64_tr_b16 a[114:115], v232 offset:52736\n" \
"v_accvgpr_read_b32 v254, a203\n" \
"v_mfma_f32_16x16x32_bf16 v[96:99], a[68:71], a[12:15], v[96:99]\n" \
"buffer_load_dwordx4 v227, s[48:51], s77 offen offset:3072 lds\n" \
"v_mfma_f32_16x16x32_bf16 v[128:131], a[64:67], a[16:19], v[128:131]\n" \
"ds_read_b64_tr_b16 a[116:117], v232 offset:58880\n" \
"v_mul_f32 v243, s31, v243\n" \
"v_mfma_f32_16x16x32_bf16 v[128:131], a[68:71], a[20:23], v[128:131]\n" \
"ds_read_b64_tr_b16 a[118:119], v232 offset:60928\n" \
"v_mul_f32 v252, s31, v252\n" \
"v_mfma_f32_16x16x32_bf16 v[132:135], a[72:75], a[16:19], v[132:135]\n" \
"s_xor_b32 s77, s77, 0x180\n" \
"ds_read_b64_tr_b16 a[120:121], v233 offset:50688\n" \
"v_mul_f32 v253, s31, v253\n" \
"v_mfma_f32_16x16x32_bf16 v[132:135], a[76:79], a[20:23], v[132:135]\n" \
"v_mul_f32 v254, s31, v254\n" \
"v_cvt_pk_bf16_f32 v243, v243, v252\n" \
"v_mfma_f32_16x16x32_bf16 v[136:139], a[80:83], a[16:19], v[136:139]\n" \
"ds_read_b64_tr_b16 a[122:123], v233 offset:52736\n" \
"v_cvt_pk_bf16_f32 v253, v253, v254\n" \
"v_mfma_f32_16x16x32_bf16 v[136:139], a[84:87], a[20:23], v[136:139]\n" \
"ds_read_b64_tr_b16 a[124:125], v233 offset:58880\n" \
"v_accvgpr_write_b32 a200, v243\n" \
"v_mfma_f32_16x16x32_bf16 v[140:143], a[88:91], a[16:19], v[140:143]\n" \
"ds_read_b64_tr_b16 a[126:127], v233 offset:60928\n" \
"v_accvgpr_write_b32 a201, v253\n" \
"v_mfma_f32_16x16x32_bf16 v[140:143], a[92:95], a[20:23], v[140:143]\n" \
"buffer_store_dwordx4 v[216:219], v236, s[56:59], s93 offen offset:2304 sc1\n" \
"v_mfma_f32_16x16x32_bf16 v[172:175], a[88:91], a[24:27], v[172:175]\n" \
"v_accvgpr_read_b32 v243, a216\n" \
"v_accvgpr_read_b32 v252, a217\n" \
"v_mfma_f32_16x16x32_bf16 v[172:175], a[92:95], a[28:31], v[172:175]\n" \
"v_accvgpr_read_b32 v253, a218\n" \
"v_accvgpr_read_b32 v254, a219\n" \
"v_mfma_f32_16x16x32_bf16 v[168:171], a[80:83], a[24:27], v[168:171]\n" \
"v_mul_f32 v243, s31, v243\n" \
"v_mul_f32 v252, s31, v252\n" \
"v_mfma_f32_16x16x32_bf16 v[168:171], a[84:87], a[28:31], v[168:171]\n" \
"v_mul_f32 v253, s31, v253\n" \
"v_mul_f32 v254, s31, v254\n" \
"v_mfma_f32_16x16x32_bf16 v[164:167], a[72:75], a[24:27], v[164:167]\n" \
"v_cvt_pk_bf16_f32 v243, v243, v252\n" \
"v_cvt_pk_bf16_f32 v253, v253, v254\n" \
"v_mfma_f32_16x16x32_bf16 v[164:167], a[76:79], a[28:31], v[164:167]\n" \
"v_accvgpr_write_b32 a202, v243\n" \
"v_accvgpr_write_b32 a203, v253\n" \
"v_mfma_f32_16x16x32_bf16 v[160:163], a[64:67], a[24:27], v[160:163]\n" \
"v_accvgpr_read_b32 v243, a204\n" \
"v_accvgpr_read_b32 v252, a205\n" \
"v_mfma_f32_16x16x32_bf16 v[160:163], a[68:71], a[28:31], v[160:163]\n" \
"s_waitcnt vmcnt(35) lgkmcnt(0)\n" \
"s_barrier\n" \
"v_mfma_f32_16x16x32_bf16 v[80:83], a[96:99], a[0:3], v[80:83]\n" \
"ds_read_b64_tr_b16 a[64:65], v232 offset:0\n" \
"v_accvgpr_read_b32 v253, a206\n" \
"v_mfma_f32_16x16x32_bf16 v[80:83], a[100:103], a[4:7], v[80:83]\n" \
"ds_read_b64_tr_b16 a[66:67], v232 offset:2048\n" \
"v_accvgpr_read_b32 v254, a207\n" \
"v_mfma_f32_16x16x32_bf16 v[84:87], a[104:107], a[0:3], v[84:87]\n" \
"s_add_u32 m0, s79, 49152\n" \
"ds_read_b64_tr_b16 a[68:69], v232 offset:8192\n" \
"v_mul_f32 v243, s31, v243\n" \
"v_mfma_f32_16x16x32_bf16 v[84:87], a[108:111], a[4:7], v[84:87]\n" \
"buffer_load_dwordx4 v228, s[52:55], s78 offen offset:0 lds\n" \
"v_mfma_f32_16x16x32_bf16 v[88:91], a[112:115], a[0:3], v[88:91]\n" \
"ds_read_b64_tr_b16 a[70:71], v232 offset:10240\n" \
"v_mul_f32 v252, s31, v252\n" \
"v_mfma_f32_16x16x32_bf16 v[88:91], a[116:119], a[4:7], v[88:91]\n" \
"ds_read_b64_tr_b16 a[72:73], v233 offset:0\n" \
"v_mul_f32 v253, s31, v253\n" \
"v_mfma_f32_16x16x32_bf16 v[92:95], a[120:123], a[0:3], v[92:95]\n" \
"ds_read_b64_tr_b16 a[74:75], v233 offset:2048\n" \
"v_mul_f32 v254, s31, v254\n" \
"v_mfma_f32_16x16x32_bf16 v[92:95], a[124:127], a[4:7], v[92:95]\n" \
"buffer_load_dwordx4 v229, s[52:55], s78 offen offset:1024 lds\n" \
"v_mfma_f32_16x16x32_bf16 v[124:127], a[120:123], a[8:11], v[124:127]\n" \
"s_waitcnt vmcnt(35)\n" \
"ds_read_b128 a[0:3], v230 offset:0\n" \
"v_cvt_pk_bf16_f32 v243, v243, v252\n" \
"v_mfma_f32_16x16x32_bf16 v[124:127], a[124:127], a[12:15], v[124:127]\n" \
"ds_read_b128 a[4:7], v231 offset:0\n" \
"v_cvt_pk_bf16_f32 v253, v253, v254\n" \
"v_mfma_f32_16x16x32_bf16 v[120:123], a[112:115], a[8:11], v[120:123]\n" \
"s_xor_b32 s78, s78, 0x80000\n" \
"ds_read_b64_tr_b16 a[76:77], v233 offset:8192\n" \
"v_accvgpr_write_b32 a204, v243\n" \
"v_mfma_f32_16x16x32_bf16 v[120:123], a[116:119], a[12:15], v[120:123]\n" \
"buffer_load_dwordx4 v228, s[52:55], s89 offen offset:2048 lds\n" \
"v_mfma_f32_16x16x32_bf16 v[116:119], a[104:107], a[8:11], v[116:119]\n" \
"ds_read_b64_tr_b16 a[78:79], v233 offset:10240\n" \
"v_accvgpr_write_b32 a205, v253\n" \
"v_mfma_f32_16x16x32_bf16 v[116:119], a[108:111], a[12:15], v[116:119]\n" \
"ds_read_b64_tr_b16 a[80:81], v232 offset:512\n" \
"v_accvgpr_read_b32 v243, a220\n" \
"v_mfma_f32_16x16x32_bf16 v[112:115], a[96:99], a[8:11], v[112:115]\n" \
"ds_read_b64_tr_b16 a[82:83], v232 offset:2560\n" \
"v_accvgpr_read_b32 v252, a221\n" \
"v_mfma_f32_16x16x32_bf16 v[112:115], a[100:103], a[12:15], v[112:115]\n" \
"buffer_load_dwordx4 v229, s[52:55], s89 offen offset:3072 lds\n" \
"v_mfma_f32_16x16x32_bf16 v[144:147], a[96:99], a[16:19], v[144:147]\n" \
"s_waitcnt vmcnt(35)\n" \
"s_xor_b32 s89, s89, 0x80000\n" \
"ds_read_b128 a[8:11], v230 offset:2048\n" \
"v_mfma_f32_16x16x32_bf16 v[144:147], a[100:103], a[20:23], v[144:147]\n" \
"s_add_u32 m0, s74, 65536\n" \
"ds_read_b128 a[12:15], v231 offset:2048\n" \
"v_accvgpr_read_b32 v253, a222\n" \
"v_mfma_f32_16x16x32_bf16 v[148:151], a[104:107], a[16:19], v[148:151]\n" \
"s_waitcnt lgkmcnt(6)\n" \
"ds_read_b64_tr_b16 a[84:85], v232 offset:8704\n" \
"v_accvgpr_read_b32 v254, a223\n" \
"v_mfma_f32_16x16x32_bf16 v[148:151], a[108:111], a[20:23], v[148:151]\n" \
"buffer_load_dwordx4 v224, s[48:51], s75 offen offset:0 lds\n" \
"v_mfma_f32_16x16x32_bf16 v[152:155], a[112:115], a[16:19], v[152:155]\n" \
"ds_read_b64_tr_b16 a[86:87], v232 offset:10752\n" \
"v_mul_f32 v243, s31, v243\n" \
"v_mfma_f32_16x16x32_bf16 v[152:155], a[116:119], a[20:23], v[152:155]\n" \
"ds_read_b64_tr_b16 a[88:89], v233 offset:512\n" \
"v_mul_f32 v252, s31, v252\n" \
"v_mfma_f32_16x16x32_bf16 v[156:159], a[120:123], a[16:19], v[156:159]\n" \
"ds_read_b64_tr_b16 a[90:91], v233 offset:2560\n" \
"v_mul_f32 v253, s31, v253\n" \
"v_mfma_f32_16x16x32_bf16 v[156:159], a[124:127], a[20:23], v[156:159]\n" \
"buffer_load_dwordx4 v225, s[48:51], s75 offen offset:1024 lds\n" \
"v_mfma_f32_16x16x32_bf16 v[188:191], a[120:123], a[24:27], v[188:191]\n" \
"s_waitcnt vmcnt(35)\n" \
"ds_read_b128 a[16:19], v230 offset:4096\n" \
"v_mul_f32 v254, s31, v254\n" \
"v_mfma_f32_16x16x32_bf16 v[188:191], a[124:127], a[28:31], v[188:191]\n" \
"ds_read_b128 a[20:23], v231 offset:4096\n" \
"v_cvt_pk_bf16_f32 v243, v243, v252\n" \
"v_mfma_f32_16x16x32_bf16 v[184:187], a[112:115], a[24:27], v[184:187]\n" \
"s_waitcnt lgkmcnt(6)\n" \
"ds_read_b64_tr_b16 a[92:93], v233 offset:8704\n" \
"v_cvt_pk_bf16_f32 v253, v253, v254\n" \
"v_mfma_f32_16x16x32_bf16 v[184:187], a[116:119], a[28:31], v[184:187]\n" \
"buffer_load_dwordx4 v226, s[48:51], s75 offen offset:2048 lds\n" \
"v_mfma_f32_16x16x32_bf16 v[180:183], a[104:107], a[24:27], v[180:183]\n" \
"ds_read_b64_tr_b16 a[94:95], v233 offset:10752\n" \
"v_accvgpr_write_b32 a206, v243\n" \
"v_mfma_f32_16x16x32_bf16 v[180:183], a[108:111], a[28:31], v[180:183]\n" \
"v_accvgpr_write_b32 a207, v253\n" \
"v_mul_f32 v32, s76, v32\n" \
"v_mfma_f32_16x16x32_bf16 v[176:179], a[96:99], a[24:27], v[176:179]\n" \
"v_mul_f32 v33, s76, v33\n" \
"v_mul_f32 v34, s76, v34\n" \
"v_mfma_f32_16x16x32_bf16 v[176:179], a[100:103], a[28:31], v[176:179]\n" \
"buffer_load_dwordx4 v227, s[48:51], s75 offen offset:3072 lds\n" \
"v_mfma_f32_16x16x32_bf16 v[64:67], a[64:67], a[0:3], v[64:67]\n" \
"s_waitcnt vmcnt(35)\n" \
"s_xor_b32 s75, s75, 0x80\n" \
"ds_read_b128 a[24:27], v230 offset:6144\n" \
"v_mfma_f32_16x16x32_bf16 v[64:67], a[68:71], a[4:7], v[64:67]\n" \
"s_add_u32 m0, s74, 69632\n" \
"ds_read_b128 a[28:31], v231 offset:6144\n" \
"v_mul_f32 v35, s76, v35\n" \
"v_mfma_f32_16x16x32_bf16 v[68:71], a[72:75], a[0:3], v[68:71]\n" \
"s_waitcnt lgkmcnt(4)\n" \
"ds_read_b64_tr_b16 a[96:97], v232 offset:1024\n" \
"v_cvt_pk_bf16_f32 v32, v32, v33\n" \
"v_mfma_f32_16x16x32_bf16 v[68:71], a[76:79], a[4:7], v[68:71]\n" \
"buffer_load_dwordx4 v224, s[48:51], s77 offen offset:0 lds\n" \
"v_mfma_f32_16x16x32_bf16 v[72:75], a[80:83], a[0:3], v[72:75]\n" \
"ds_read_b64_tr_b16 a[98:99], v232 offset:3072\n" \
"v_cvt_pk_bf16_f32 v33, v34, v35\n" \
"v_mfma_f32_16x16x32_bf16 v[72:75], a[84:87], a[4:7], v[72:75]\n" \
"s_waitcnt lgkmcnt(3)\n" \
"ds_read_b64_tr_b16 a[100:101], v232 offset:9216\n" \
"v_mul_f32 v36, s76, v36\n" \
"v_mfma_f32_16x16x32_bf16 v[76:79], a[88:91], a[0:3], v[76:79]\n" \
"ds_read_b64_tr_b16 a[102:103], v232 offset:11264\n" \
"v_mul_f32 v37, s76, v37\n" \
"v_mfma_f32_16x16x32_bf16 v[76:79], a[92:95], a[4:7], v[76:79]\n" \
"buffer_load_dwordx4 v225, s[48:51], s77 offen offset:1024 lds\n" \
"v_mfma_f32_16x16x32_bf16 v[108:111], a[88:91], a[8:11], v[108:111]\n" \
"ds_read_b64_tr_b16 a[104:105], v233 offset:1024\n" \
"v_mul_f32 v38, s76, v38\n" \
"v_mfma_f32_16x16x32_bf16 v[108:111], a[92:95], a[12:15], v[108:111]\n" \
"ds_read_b64_tr_b16 a[106:107], v233 offset:3072\n" \
"v_mul_f32 v39, s76, v39\n" \
"v_mfma_f32_16x16x32_bf16 v[104:107], a[80:83], a[8:11], v[104:107]\n" \
"s_waitcnt lgkmcnt(6)\n" \
"ds_read_b64_tr_b16 a[108:109], v233 offset:9216\n" \
"v_cvt_pk_bf16_f32 v34, v36, v37\n" \
"v_mfma_f32_16x16x32_bf16 v[104:107], a[84:87], a[12:15], v[104:107]\n" \
"buffer_load_dwordx4 v226, s[48:51], s77 offen offset:2048 lds\n" \
"v_mfma_f32_16x16x32_bf16 v[100:103], a[72:75], a[8:11], v[100:103]\n" \
"ds_read_b64_tr_b16 a[110:111], v233 offset:11264\n" \
"v_cvt_pk_bf16_f32 v35, v38, v39\n" \
"v_mfma_f32_16x16x32_bf16 v[100:103], a[76:79], a[12:15], v[100:103]\n" \
"ds_read_b64_tr_b16 a[112:113], v232 offset:1536\n" \
"v_mul_f32 v48, s76, v48\n" \
"v_mfma_f32_16x16x32_bf16 v[96:99], a[64:67], a[8:11], v[96:99]\n" \
"ds_read_b64_tr_b16 a[114:115], v232 offset:3584\n" \
"v_mul_f32 v49, s76, v49\n" \
"v_mfma_f32_16x16x32_bf16 v[96:99], a[68:71], a[12:15], v[96:99]\n" \
"buffer_load_dwordx4 v227, s[48:51], s77 offen offset:3072 lds\n" \
"v_mfma_f32_16x16x32_bf16 v[128:131], a[64:67], a[16:19], v[128:131]\n" \
"ds_read_b64_tr_b16 a[116:117], v232 offset:9728\n" \
"v_mul_f32 v50, s76, v50\n" \
"v_mfma_f32_16x16x32_bf16 v[128:131], a[68:71], a[20:23], v[128:131]\n" \
"ds_read_b64_tr_b16 a[118:119], v232 offset:11776\n" \
"v_mul_f32 v51, s76, v51\n" \
"v_mfma_f32_16x16x32_bf16 v[132:135], a[72:75], a[16:19], v[132:135]\n" \
"s_xor_b32 s77, s77, 0x80\n" \
"ds_read_b64_tr_b16 a[120:121], v233 offset:1536\n" \
"v_cvt_pk_bf16_f32 v48, v48, v49\n" \
"v_mfma_f32_16x16x32_bf16 v[132:135], a[76:79], a[20:23], v[132:135]\n" \
"v_cvt_pk_bf16_f32 v49, v50, v51\n" \
"v_mul_f32 v52, s76, v52\n" \
"v_mfma_f32_16x16x32_bf16 v[136:139], a[80:83], a[16:19], v[136:139]\n" \
"ds_read_b64_tr_b16 a[122:123], v233 offset:3584\n" \
"v_mul_f32 v53, s76, v53\n" \
"v_mfma_f32_16x16x32_bf16 v[136:139], a[84:87], a[20:23], v[136:139]\n" \
"ds_read_b64_tr_b16 a[124:125], v233 offset:9728\n" \
"v_mul_f32 v54, s76, v54\n" \
"v_mfma_f32_16x16x32_bf16 v[140:143], a[88:91], a[16:19], v[140:143]\n" \
"s_add_u32 s93, s87, 0\n" \
"ds_read_b64_tr_b16 a[126:127], v233 offset:11776\n" \
"v_mul_f32 v55, s76, v55\n" \
"v_mfma_f32_16x16x32_bf16 v[140:143], a[92:95], a[20:23], v[140:143]\n" \
"buffer_store_dwordx4 v[196:199], v236, s[56:59], s93 offen offset:0 sc1\n" \
"v_mfma_f32_16x16x32_bf16 v[172:175], a[88:91], a[24:27], v[172:175]\n" \
"v_cvt_pk_bf16_f32 v50, v52, v53\n" \
"v_cvt_pk_bf16_f32 v51, v54, v55\n" \
"v_mfma_f32_16x16x32_bf16 v[172:175], a[92:95], a[28:31], v[172:175]\n" \
"v_mul_f32 v40, s76, v40\n" \
"v_mul_f32 v41, s76, v41\n" \
"v_mfma_f32_16x16x32_bf16 v[168:171], a[80:83], a[24:27], v[168:171]\n" \
"v_mul_f32 v42, s76, v42\n" \
"v_mul_f32 v43, s76, v43\n" \
"v_mfma_f32_16x16x32_bf16 v[168:171], a[84:87], a[28:31], v[168:171]\n" \
"v_cvt_pk_bf16_f32 v40, v40, v41\n" \
"v_cvt_pk_bf16_f32 v41, v42, v43\n" \
"v_mfma_f32_16x16x32_bf16 v[164:167], a[72:75], a[24:27], v[164:167]\n" \
"v_mul_f32 v44, s76, v44\n" \
"v_mul_f32 v45, s76, v45\n" \
"v_mfma_f32_16x16x32_bf16 v[164:167], a[76:79], a[28:31], v[164:167]\n" \
"v_mul_f32 v46, s76, v46\n" \
"v_mul_f32 v47, s76, v47\n" \
"v_mfma_f32_16x16x32_bf16 v[160:163], a[64:67], a[24:27], v[160:163]\n" \
"v_cvt_pk_bf16_f32 v42, v44, v45\n" \
"v_cvt_pk_bf16_f32 v43, v46, v47\n" \
"v_mfma_f32_16x16x32_bf16 v[160:163], a[68:71], a[28:31], v[160:163]\n" \
"s_waitcnt vmcnt(35) lgkmcnt(0)\n" \
"s_barrier\n" \
"v_mfma_f32_16x16x32_bf16 v[80:83], a[96:99], a[0:3], v[80:83]\n" \
"ds_read_b64_tr_b16 a[64:65], v232 offset:16384\n" \
"v_mul_f32 v56, s76, v56\n" \
"v_mfma_f32_16x16x32_bf16 v[80:83], a[100:103], a[4:7], v[80:83]\n" \
"ds_read_b64_tr_b16 a[66:67], v232 offset:18432\n" \
"v_mul_f32 v57, s76, v57\n" \
"v_mfma_f32_16x16x32_bf16 v[84:87], a[104:107], a[0:3], v[84:87]\n" \
"s_add_u32 m0, s79, 0\n" \
"ds_read_b64_tr_b16 a[68:69], v232 offset:24576\n" \
"v_mul_f32 v58, s76, v58\n" \
"v_mfma_f32_16x16x32_bf16 v[84:87], a[108:111], a[4:7], v[84:87]\n" \
"buffer_load_dwordx4 v228, s[52:55], s78 offen offset:0 lds\n" \
"v_mfma_f32_16x16x32_bf16 v[88:91], a[112:115], a[0:3], v[88:91]\n" \
"ds_read_b64_tr_b16 a[70:71], v232 offset:26624\n" \
"v_mul_f32 v59, s76, v59\n" \
"v_mfma_f32_16x16x32_bf16 v[88:91], a[116:119], a[4:7], v[88:91]\n" \
"ds_read_b64_tr_b16 a[72:73], v233 offset:16384\n" \
"v_cvt_pk_bf16_f32 v56, v56, v57\n" \
"v_mfma_f32_16x16x32_bf16 v[92:95], a[120:123], a[0:3], v[92:95]\n" \
"ds_read_b64_tr_b16 a[74:75], v233 offset:18432\n" \
"v_cvt_pk_bf16_f32 v57, v58, v59\n" \
"v_mfma_f32_16x16x32_bf16 v[92:95], a[124:127], a[4:7], v[92:95]\n" \
"buffer_load_dwordx4 v229, s[52:55], s78 offen offset:1024 lds\n" \
"v_mfma_f32_16x16x32_bf16 v[124:127], a[120:123], a[8:11], v[124:127]\n" \
"s_waitcnt vmcnt(35)\n" \
"ds_read_b128 a[0:3], v230 offset:32768\n" \
"v_mul_f32 v60, s76, v60\n" \
"v_mfma_f32_16x16x32_bf16 v[124:127], a[124:127], a[12:15], v[124:127]\n" \
"ds_read_b128 a[4:7], v231 offset:32768\n" \
"v_mul_f32 v61, s76, v61\n" \
"v_mfma_f32_16x16x32_bf16 v[120:123], a[112:115], a[8:11], v[120:123]\n" \
"s_xor_b32 s78, s78, 0x780000\n" \
"ds_read_b64_tr_b16 a[76:77], v233 offset:24576\n" \
"v_mul_f32 v62, s76, v62\n" \
"v_mfma_f32_16x16x32_bf16 v[120:123], a[116:119], a[12:15], v[120:123]\n" \
"buffer_load_dwordx4 v228, s[52:55], s89 offen offset:2048 lds\n" \
"v_mfma_f32_16x16x32_bf16 v[116:119], a[104:107], a[8:11], v[116:119]\n" \
"ds_read_b64_tr_b16 a[78:79], v233 offset:26624\n" \
"v_mul_f32 v63, s76, v63\n" \
"v_mfma_f32_16x16x32_bf16 v[116:119], a[108:111], a[12:15], v[116:119]\n" \
"ds_read_b64_tr_b16 a[80:81], v232 offset:16896\n" \
"v_cvt_pk_bf16_f32 v58, v60, v61\n" \
"v_mfma_f32_16x16x32_bf16 v[112:115], a[96:99], a[8:11], v[112:115]\n" \
"ds_read_b64_tr_b16 a[82:83], v232 offset:18944\n" \
"v_cvt_pk_bf16_f32 v59, v62, v63\n" \
"v_mfma_f32_16x16x32_bf16 v[112:115], a[100:103], a[12:15], v[112:115]\n" \
"buffer_load_dwordx4 v229, s[52:55], s89 offen offset:3072 lds\n" \
"v_mfma_f32_16x16x32_bf16 v[144:147], a[96:99], a[16:19], v[144:147]\n" \
"s_waitcnt vmcnt(35)\n" \
"s_xor_b32 s89, s89, 0x780000\n" \
"ds_read_b128 a[8:11], v230 offset:34816\n" \
"v_mfma_f32_16x16x32_bf16 v[144:147], a[100:103], a[20:23], v[144:147]\n" \
"s_add_u32 m0, s74, 98304\n" \
"ds_read_b128 a[12:15], v231 offset:34816\n" \
"v_mul_f32 v0, s76, v0\n" \
"v_mfma_f32_16x16x32_bf16 v[148:151], a[104:107], a[16:19], v[148:151]\n" \
"s_waitcnt lgkmcnt(6)\n" \
"ds_read_b64_tr_b16 a[84:85], v232 offset:25088\n" \
"v_mul_f32 v1, s76, v1\n" \
"v_mfma_f32_16x16x32_bf16 v[148:151], a[108:111], a[20:23], v[148:151]\n" \
"buffer_load_dwordx4 v224, s[48:51], s75 offen offset:0 lds\n" \
"v_mfma_f32_16x16x32_bf16 v[152:155], a[112:115], a[16:19], v[152:155]\n" \
"ds_read_b64_tr_b16 a[86:87], v232 offset:27136\n" \
"v_mul_f32 v2, s76, v2\n" \
"v_mfma_f32_16x16x32_bf16 v[152:155], a[116:119], a[20:23], v[152:155]\n" \
"ds_read_b64_tr_b16 a[88:89], v233 offset:16896\n" \
"v_mul_f32 v3, s76, v3\n" \
"v_mfma_f32_16x16x32_bf16 v[156:159], a[120:123], a[16:19], v[156:159]\n" \
"ds_read_b64_tr_b16 a[90:91], v233 offset:18944\n" \
"v_cvt_pk_bf16_f32 v36, v0, v1\n" \
"v_mfma_f32_16x16x32_bf16 v[156:159], a[124:127], a[20:23], v[156:159]\n" \
"buffer_load_dwordx4 v225, s[48:51], s75 offen offset:1024 lds\n" \
"v_mfma_f32_16x16x32_bf16 v[188:191], a[120:123], a[24:27], v[188:191]\n" \
"s_waitcnt vmcnt(35)\n" \
"ds_read_b128 a[16:19], v230 offset:36864\n" \
"v_cvt_pk_bf16_f32 v37, v2, v3\n" \
"v_mfma_f32_16x16x32_bf16 v[188:191], a[124:127], a[28:31], v[188:191]\n" \
"ds_read_b128 a[20:23], v231 offset:36864\n" \
"v_mul_f32 v4, s76, v4\n" \
"v_mfma_f32_16x16x32_bf16 v[184:187], a[112:115], a[24:27], v[184:187]\n" \
"s_waitcnt lgkmcnt(6)\n" \
"ds_read_b64_tr_b16 a[92:93], v233 offset:25088\n" \
"v_mul_f32 v5, s76, v5\n" \
"v_mfma_f32_16x16x32_bf16 v[184:187], a[116:119], a[28:31], v[184:187]\n" \
"buffer_load_dwordx4 v226, s[48:51], s75 offen offset:2048 lds\n" \
"v_mfma_f32_16x16x32_bf16 v[180:183], a[104:107], a[24:27], v[180:183]\n" \
"ds_read_b64_tr_b16 a[94:95], v233 offset:27136\n" \
"v_mul_f32 v6, s76, v6\n" \
"v_mfma_f32_16x16x32_bf16 v[180:183], a[108:111], a[28:31], v[180:183]\n" \
"v_mul_f32 v7, s76, v7\n" \
"v_cvt_pk_bf16_f32 v38, v4, v5\n" \
"v_mfma_f32_16x16x32_bf16 v[176:179], a[96:99], a[24:27], v[176:179]\n" \
"v_cvt_pk_bf16_f32 v39, v6, v7\n" \
"v_mul_f32 v16, s76, v16\n" \
"v_mfma_f32_16x16x32_bf16 v[176:179], a[100:103], a[28:31], v[176:179]\n" \
"buffer_load_dwordx4 v227, s[48:51], s75 offen offset:3072 lds\n" \
"v_mfma_f32_16x16x32_bf16 v[64:67], a[64:67], a[0:3], v[64:67]\n" \
"s_waitcnt vmcnt(35)\n" \
"s_xor_b32 s75, s75, 0x780\n" \
"ds_read_b128 a[24:27], v230 offset:38912\n" \
"v_mfma_f32_16x16x32_bf16 v[64:67], a[68:71], a[4:7], v[64:67]\n" \
"s_add_u32 m0, s74, 102400\n" \
"ds_read_b128 a[28:31], v231 offset:38912\n" \
"v_mul_f32 v17, s76, v17\n" \
"v_mfma_f32_16x16x32_bf16 v[68:71], a[72:75], a[0:3], v[68:71]\n" \
"s_waitcnt lgkmcnt(4)\n" \
"ds_read_b64_tr_b16 a[96:97], v232 offset:17408\n" \
"v_mul_f32 v18, s76, v18\n" \
"v_mfma_f32_16x16x32_bf16 v[68:71], a[76:79], a[4:7], v[68:71]\n" \
"buffer_load_dwordx4 v224, s[48:51], s77 offen offset:0 lds\n" \
"v_mfma_f32_16x16x32_bf16 v[72:75], a[80:83], a[0:3], v[72:75]\n" \
"ds_read_b64_tr_b16 a[98:99], v232 offset:19456\n" \
"v_mul_f32 v19, s76, v19\n" \
"v_mfma_f32_16x16x32_bf16 v[72:75], a[84:87], a[4:7], v[72:75]\n" \
"s_waitcnt lgkmcnt(3)\n" \
"ds_read_b64_tr_b16 a[100:101], v232 offset:25600\n" \
"v_cvt_pk_bf16_f32 v16, v16, v17\n" \
"v_mfma_f32_16x16x32_bf16 v[76:79], a[88:91], a[0:3], v[76:79]\n" \
"ds_read_b64_tr_b16 a[102:103], v232 offset:27648\n" \
"v_cvt_pk_bf16_f32 v17, v18, v19\n" \
"v_mfma_f32_16x16x32_bf16 v[76:79], a[92:95], a[4:7], v[76:79]\n" \
"buffer_load_dwordx4 v225, s[48:51], s77 offen offset:1024 lds\n" \
"v_mfma_f32_16x16x32_bf16 v[108:111], a[88:91], a[8:11], v[108:111]\n" \
"ds_read_b64_tr_b16 a[104:105], v233 offset:17408\n" \
"v_mul_f32 v20, s76, v20\n" \
"v_mfma_f32_16x16x32_bf16 v[108:111], a[92:95], a[12:15], v[108:111]\n" \
"ds_read_b64_tr_b16 a[106:107], v233 offset:19456\n" \
"v_mul_f32 v21, s76, v21\n" \
"v_mfma_f32_16x16x32_bf16 v[104:107], a[80:83], a[8:11], v[104:107]\n" \
"s_waitcnt lgkmcnt(6)\n" \
"ds_read_b64_tr_b16 a[108:109], v233 offset:25600\n" \
"v_mul_f32 v22, s76, v22\n" \
"v_mfma_f32_16x16x32_bf16 v[104:107], a[84:87], a[12:15], v[104:107]\n" \
"buffer_load_dwordx4 v226, s[48:51], s77 offen offset:2048 lds\n" \
"v_mfma_f32_16x16x32_bf16 v[100:103], a[72:75], a[8:11], v[100:103]\n" \
"ds_read_b64_tr_b16 a[110:111], v233 offset:27648\n" \
"v_mul_f32 v23, s76, v23\n" \
"v_mfma_f32_16x16x32_bf16 v[100:103], a[76:79], a[12:15], v[100:103]\n" \
"ds_read_b64_tr_b16 a[112:113], v232 offset:17920\n" \
"v_cvt_pk_bf16_f32 v18, v20, v21\n" \
"v_mfma_f32_16x16x32_bf16 v[96:99], a[64:67], a[8:11], v[96:99]\n" \
"ds_read_b64_tr_b16 a[114:115], v232 offset:19968\n" \
"v_cvt_pk_bf16_f32 v19, v22, v23\n" \
"v_mfma_f32_16x16x32_bf16 v[96:99], a[68:71], a[12:15], v[96:99]\n" \
"buffer_load_dwordx4 v227, s[48:51], s77 offen offset:3072 lds\n" \
"v_mfma_f32_16x16x32_bf16 v[128:131], a[64:67], a[16:19], v[128:131]\n" \
"ds_read_b64_tr_b16 a[116:117], v232 offset:26112\n" \
"v_mul_f32 v8, s76, v8\n" \
"v_mfma_f32_16x16x32_bf16 v[128:131], a[68:71], a[20:23], v[128:131]\n" \
"ds_read_b64_tr_b16 a[118:119], v232 offset:28160\n" \
"v_mul_f32 v9, s76, v9\n" \
"v_mfma_f32_16x16x32_bf16 v[132:135], a[72:75], a[16:19], v[132:135]\n" \
"s_xor_b32 s77, s77, 0x780\n" \
"ds_read_b64_tr_b16 a[120:121], v233 offset:17920\n" \
"v_mul_f32 v10, s76, v10\n" \
"v_mfma_f32_16x16x32_bf16 v[132:135], a[76:79], a[20:23], v[132:135]\n" \
"v_mul_f32 v11, s76, v11\n" \
"v_cvt_pk_bf16_f32 v52, v8, v9\n" \
"v_mfma_f32_16x16x32_bf16 v[136:139], a[80:83], a[16:19], v[136:139]\n" \
"ds_read_b64_tr_b16 a[122:123], v233 offset:19968\n" \
"v_cvt_pk_bf16_f32 v53, v10, v11\n" \
"v_mfma_f32_16x16x32_bf16 v[136:139], a[84:87], a[20:23], v[136:139]\n" \
"ds_read_b64_tr_b16 a[124:125], v233 offset:26112\n" \
"v_mul_f32 v12, s76, v12\n" \
"v_mfma_f32_16x16x32_bf16 v[140:143], a[88:91], a[16:19], v[140:143]\n" \
"ds_read_b64_tr_b16 a[126:127], v233 offset:28160\n" \
"v_mul_f32 v13, s76, v13\n" \
"v_mfma_f32_16x16x32_bf16 v[140:143], a[92:95], a[20:23], v[140:143]\n" \
"buffer_store_dwordx4 v[212:215], v236, s[56:59], s93 offen offset:256 sc1\n" \
"v_mfma_f32_16x16x32_bf16 v[172:175], a[88:91], a[24:27], v[172:175]\n" \
"v_mul_f32 v14, s76, v14\n" \
"v_mul_f32 v15, s76, v15\n" \
"v_mfma_f32_16x16x32_bf16 v[172:175], a[92:95], a[28:31], v[172:175]\n" \
"v_cvt_pk_bf16_f32 v54, v12, v13\n" \
"v_cvt_pk_bf16_f32 v55, v14, v15\n" \
"v_mfma_f32_16x16x32_bf16 v[168:171], a[80:83], a[24:27], v[168:171]\n" \
"v_mul_f32 v24, s76, v24\n" \
"v_mul_f32 v25, s76, v25\n" \
"v_mfma_f32_16x16x32_bf16 v[168:171], a[84:87], a[28:31], v[168:171]\n" \
"v_mul_f32 v26, s76, v26\n" \
"v_mul_f32 v27, s76, v27\n" \
"v_mfma_f32_16x16x32_bf16 v[164:167], a[72:75], a[24:27], v[164:167]\n" \
"v_cvt_pk_bf16_f32 v24, v24, v25\n" \
"v_cvt_pk_bf16_f32 v25, v26, v27\n" \
"v_mfma_f32_16x16x32_bf16 v[164:167], a[76:79], a[28:31], v[164:167]\n" \
"v_mul_f32 v28, s76, v28\n" \
"v_mul_f32 v29, s76, v29\n" \
"v_mfma_f32_16x16x32_bf16 v[160:163], a[64:67], a[24:27], v[160:163]\n" \
"v_mul_f32 v30, s76, v30\n" \
"v_mul_f32 v31, s76, v31\n" \
"v_mfma_f32_16x16x32_bf16 v[160:163], a[68:71], a[28:31], v[160:163]\n" \
"s_waitcnt vmcnt(35) lgkmcnt(0)\n" \
"s_barrier\n" \
"v_mfma_f32_16x16x32_bf16 v[80:83], a[96:99], a[0:3], v[80:83]\n" \
"ds_read_b64_tr_b16 a[64:65], v232 offset:32768\n" \
"v_cvt_pk_bf16_f32 v26, v28, v29\n" \
"v_mfma_f32_16x16x32_bf16 v[80:83], a[100:103], a[4:7], v[80:83]\n" \
"ds_read_b64_tr_b16 a[66:67], v232 offset:34816\n" \
"v_cvt_pk_bf16_f32 v27, v30, v31\n" \
"v_mfma_f32_16x16x32_bf16 v[84:87], a[104:107], a[0:3], v[84:87]\n" \
"s_add_u32 m0, s79, 16384\n" \
"ds_read_b64_tr_b16 a[68:69], v232 offset:40960\n" \
"v_accvgpr_read_b32 v243, a160\n" \
"v_mfma_f32_16x16x32_bf16 v[84:87], a[108:111], a[4:7], v[84:87]\n" \
"buffer_load_dwordx4 v228, s[52:55], s78 offen offset:0 lds\n" \
"v_mfma_f32_16x16x32_bf16 v[88:91], a[112:115], a[0:3], v[88:91]\n" \
"ds_read_b64_tr_b16 a[70:71], v232 offset:43008\n" \
"v_accvgpr_read_b32 v252, a161\n" \
"v_mfma_f32_16x16x32_bf16 v[88:91], a[116:119], a[4:7], v[88:91]\n" \
"ds_read_b64_tr_b16 a[72:73], v233 offset:32768\n" \
"v_accvgpr_read_b32 v253, a162\n" \
"v_mfma_f32_16x16x32_bf16 v[92:95], a[120:123], a[0:3], v[92:95]\n" \
"ds_read_b64_tr_b16 a[74:75], v233 offset:34816\n" \
"v_accvgpr_read_b32 v254, a163\n" \
"v_mfma_f32_16x16x32_bf16 v[92:95], a[124:127], a[4:7], v[92:95]\n" \
"buffer_load_dwordx4 v229, s[52:55], s78 offen offset:1024 lds\n" \
"v_mfma_f32_16x16x32_bf16 v[124:127], a[120:123], a[8:11], v[124:127]\n" \
"s_waitcnt vmcnt(35)\n" \
"ds_read_b128 a[0:3], v250 offset:0\n" \
"v_mul_f32 v243, s31, v243\n" \
"v_mfma_f32_16x16x32_bf16 v[124:127], a[124:127], a[12:15], v[124:127]\n" \
"ds_read_b128 a[4:7], v251 offset:0\n" \
"v_mul_f32 v252, s31, v252\n" \
"v_mfma_f32_16x16x32_bf16 v[120:123], a[112:115], a[8:11], v[120:123]\n" \
"s_xor_b32 s78, s78, 0x80000\n" \
"ds_read_b64_tr_b16 a[76:77], v233 offset:40960\n" \
"v_mul_f32 v253, s31, v253\n" \
"v_mfma_f32_16x16x32_bf16 v[120:123], a[116:119], a[12:15], v[120:123]\n" \
"buffer_load_dwordx4 v228, s[52:55], s89 offen offset:2048 lds\n" \
"v_mfma_f32_16x16x32_bf16 v[116:119], a[104:107], a[8:11], v[116:119]\n" \
"ds_read_b64_tr_b16 a[78:79], v233 offset:43008\n" \
"v_mul_f32 v254, s31, v254\n" \
"v_mfma_f32_16x16x32_bf16 v[116:119], a[108:111], a[12:15], v[116:119]\n" \
"ds_read_b64_tr_b16 a[80:81], v232 offset:33280\n" \
"v_cvt_pk_bf16_f32 v44, v243, v252\n" \
"v_mfma_f32_16x16x32_bf16 v[112:115], a[96:99], a[8:11], v[112:115]\n" \
"ds_read_b64_tr_b16 a[82:83], v232 offset:35328\n" \
"v_cvt_pk_bf16_f32 v45, v253, v254\n" \
"v_mfma_f32_16x16x32_bf16 v[112:115], a[100:103], a[12:15], v[112:115]\n" \
"buffer_load_dwordx4 v229, s[52:55], s89 offen offset:3072 lds\n" \
"v_mfma_f32_16x16x32_bf16 v[144:147], a[96:99], a[16:19], v[144:147]\n" \
"s_waitcnt vmcnt(35)\n" \
"s_xor_b32 s89, s89, 0x80000\n" \
"ds_read_b128 a[8:11], v250 offset:2048\n" \
"v_mfma_f32_16x16x32_bf16 v[144:147], a[100:103], a[20:23], v[144:147]\n" \
"s_add_u32 m0, s74, 131072\n" \
"ds_read_b128 a[12:15], v251 offset:2048\n" \
"v_accvgpr_read_b32 v243, a176\n" \
"v_mfma_f32_16x16x32_bf16 v[148:151], a[104:107], a[16:19], v[148:151]\n" \
"s_waitcnt lgkmcnt(6)\n" \
"ds_read_b64_tr_b16 a[84:85], v232 offset:41472\n" \
"v_accvgpr_read_b32 v252, a177\n" \
"v_mfma_f32_16x16x32_bf16 v[148:151], a[108:111], a[20:23], v[148:151]\n" \
"buffer_load_dwordx4 v224, s[48:51], s75 offen offset:0 lds\n" \
"v_mfma_f32_16x16x32_bf16 v[152:155], a[112:115], a[16:19], v[152:155]\n" \
"ds_read_b64_tr_b16 a[86:87], v232 offset:43520\n" \
"v_accvgpr_read_b32 v253, a178\n" \
"v_mfma_f32_16x16x32_bf16 v[152:155], a[116:119], a[20:23], v[152:155]\n" \
"ds_read_b64_tr_b16 a[88:89], v233 offset:33280\n" \
"v_accvgpr_read_b32 v254, a179\n" \
"v_mfma_f32_16x16x32_bf16 v[156:159], a[120:123], a[16:19], v[156:159]\n" \
"ds_read_b64_tr_b16 a[90:91], v233 offset:35328\n" \
"v_mul_f32 v243, s31, v243\n" \
"v_mfma_f32_16x16x32_bf16 v[156:159], a[124:127], a[20:23], v[156:159]\n" \
"buffer_load_dwordx4 v225, s[48:51], s75 offen offset:1024 lds\n" \
"v_mfma_f32_16x16x32_bf16 v[188:191], a[120:123], a[24:27], v[188:191]\n" \
"s_waitcnt vmcnt(35)\n" \
"ds_read_b128 a[16:19], v250 offset:4096\n" \
"v_mul_f32 v252, s31, v252\n" \
"v_mfma_f32_16x16x32_bf16 v[188:191], a[124:127], a[28:31], v[188:191]\n" \
"ds_read_b128 a[20:23], v251 offset:4096\n" \
"v_mul_f32 v253, s31, v253\n" \
"v_mfma_f32_16x16x32_bf16 v[184:187], a[112:115], a[24:27], v[184:187]\n" \
"s_waitcnt lgkmcnt(6)\n" \
"ds_read_b64_tr_b16 a[92:93], v233 offset:41472\n" \
"v_mul_f32 v254, s31, v254\n" \
"v_mfma_f32_16x16x32_bf16 v[184:187], a[116:119], a[28:31], v[184:187]\n" \
"buffer_load_dwordx4 v226, s[48:51], s75 offen offset:2048 lds\n" \
"v_mfma_f32_16x16x32_bf16 v[180:183], a[104:107], a[24:27], v[180:183]\n" \
"ds_read_b64_tr_b16 a[94:95], v233 offset:43520\n" \
"v_cvt_pk_bf16_f32 v46, v243, v252\n" \
"v_mfma_f32_16x16x32_bf16 v[180:183], a[108:111], a[28:31], v[180:183]\n" \
"v_cvt_pk_bf16_f32 v47, v253, v254\n" \
"v_accvgpr_read_b32 v243, a164\n" \
"v_mfma_f32_16x16x32_bf16 v[176:179], a[96:99], a[24:27], v[176:179]\n" \
"v_accvgpr_read_b32 v252, a165\n" \
"v_accvgpr_read_b32 v253, a166\n" \
"v_mfma_f32_16x16x32_bf16 v[176:179], a[100:103], a[28:31], v[176:179]\n" \
"buffer_load_dwordx4 v227, s[48:51], s75 offen offset:3072 lds\n" \
"v_mfma_f32_16x16x32_bf16 v[64:67], a[64:67], a[0:3], v[64:67]\n" \
"s_waitcnt vmcnt(35)\n" \
"s_xor_b32 s75, s75, 0x80\n" \
"ds_read_b128 a[24:27], v250 offset:6144\n" \
"v_mfma_f32_16x16x32_bf16 v[64:67], a[68:71], a[4:7], v[64:67]\n" \
"s_add_u32 m0, s74, 135168\n" \
"ds_read_b128 a[28:31], v251 offset:6144\n" \
"v_accvgpr_read_b32 v254, a167\n" \
"v_mfma_f32_16x16x32_bf16 v[68:71], a[72:75], a[0:3], v[68:71]\n" \
"s_waitcnt lgkmcnt(4)\n" \
"ds_read_b64_tr_b16 a[96:97], v232 offset:33792\n" \
"v_mul_f32 v243, s31, v243\n" \
"v_mfma_f32_16x16x32_bf16 v[68:71], a[76:79], a[4:7], v[68:71]\n" \
"buffer_load_dwordx4 v224, s[48:51], s77 offen offset:0 lds\n" \
"v_mfma_f32_16x16x32_bf16 v[72:75], a[80:83], a[0:3], v[72:75]\n" \
"ds_read_b64_tr_b16 a[98:99], v232 offset:35840\n" \
"v_mul_f32 v252, s31, v252\n" \
"v_mfma_f32_16x16x32_bf16 v[72:75], a[84:87], a[4:7], v[72:75]\n" \
"s_waitcnt lgkmcnt(3)\n" \
"ds_read_b64_tr_b16 a[100:101], v232 offset:41984\n" \
"v_mul_f32 v253, s31, v253\n" \
"v_mfma_f32_16x16x32_bf16 v[76:79], a[88:91], a[0:3], v[76:79]\n" \
"ds_read_b64_tr_b16 a[102:103], v232 offset:44032\n" \
"v_mul_f32 v254, s31, v254\n" \
"v_mfma_f32_16x16x32_bf16 v[76:79], a[92:95], a[4:7], v[76:79]\n" \
"buffer_load_dwordx4 v225, s[48:51], s77 offen offset:1024 lds\n" \
"v_mfma_f32_16x16x32_bf16 v[108:111], a[88:91], a[8:11], v[108:111]\n" \
"ds_read_b64_tr_b16 a[104:105], v233 offset:33792\n" \
"v_cvt_pk_bf16_f32 v60, v243, v252\n" \
"v_mfma_f32_16x16x32_bf16 v[108:111], a[92:95], a[12:15], v[108:111]\n" \
"Lpg3347_%=:\n" \
"s_cbranch_execz Lpg5603_%=\n" \
"ds_read_b64_tr_b16 a[106:107], v233 offset:35840\n" \
"v_cvt_pk_bf16_f32 v61, v253, v254\n" \
"v_mfma_f32_16x16x32_bf16 v[104:107], a[80:83], a[8:11], v[104:107]\n" \
"s_waitcnt lgkmcnt(6)\n" \
"ds_read_b64_tr_b16 a[108:109], v233 offset:41984\n" \
"v_accvgpr_read_b32 v243, a180\n" \
"v_mfma_f32_16x16x32_bf16 v[104:107], a[84:87], a[12:15], v[104:107]\n" \
"buffer_load_dwordx4 v226, s[48:51], s77 offen offset:2048 lds\n" \
"v_mfma_f32_16x16x32_bf16 v[100:103], a[72:75], a[8:11], v[100:103]\n" \
"ds_read_b64_tr_b16 a[110:111], v233 offset:44032\n" \
"v_accvgpr_read_b32 v252, a181\n" \
"v_mfma_f32_16x16x32_bf16 v[100:103], a[76:79], a[12:15], v[100:103]\n" \
"ds_read_b64_tr_b16 a[112:113], v232 offset:34304\n" \
"v_accvgpr_read_b32 v253, a182\n" \
"v_mfma_f32_16x16x32_bf16 v[96:99], a[64:67], a[8:11], v[96:99]\n" \
"ds_read_b64_tr_b16 a[114:115], v232 offset:36352\n" \
"v_accvgpr_read_b32 v254, a183\n" \
"v_mfma_f32_16x16x32_bf16 v[96:99], a[68:71], a[12:15], v[96:99]\n" \
"buffer_load_dwordx4 v227, s[48:51], s77 offen offset:3072 lds\n" \
"v_mfma_f32_16x16x32_bf16 v[128:131], a[64:67], a[16:19], v[128:131]\n" \
"ds_read_b64_tr_b16 a[116:117], v232 offset:42496\n" \
"v_mul_f32 v243, s31, v243\n" \
"v_mfma_f32_16x16x32_bf16 v[128:131], a[68:71], a[20:23], v[128:131]\n" \
"ds_read_b64_tr_b16 a[118:119], v232 offset:44544\n" \
"v_mul_f32 v252, s31, v252\n" \
"v_mfma_f32_16x16x32_bf16 v[132:135], a[72:75], a[16:19], v[132:135]\n" \
"s_xor_b32 s77, s77, 0x80\n" \
"ds_read_b64_tr_b16 a[120:121], v233 offset:34304\n" \
"v_mul_f32 v253, s31, v253\n" \
"v_mfma_f32_16x16x32_bf16 v[132:135], a[76:79], a[20:23], v[132:135]\n" \
"v_mul_f32 v254, s31, v254\n" \
"v_cvt_pk_bf16_f32 v62, v243, v252\n" \
"v_mfma_f32_16x16x32_bf16 v[136:139], a[80:83], a[16:19], v[136:139]\n" \
"ds_read_b64_tr_b16 a[122:123], v233 offset:36352\n" \
"v_cvt_pk_bf16_f32 v63, v253, v254\n" \
"v_mfma_f32_16x16x32_bf16 v[136:139], a[84:87], a[20:23], v[136:139]\n" \
"ds_read_b64_tr_b16 a[124:125], v233 offset:42496\n" \
"v_accvgpr_read_b32 v243, a168\n" \
"v_mfma_f32_16x16x32_bf16 v[140:143], a[88:91], a[16:19], v[140:143]\n" \
"ds_read_b64_tr_b16 a[126:127], v233 offset:44544\n" \
"v_accvgpr_read_b32 v252, a169\n" \
"v_mfma_f32_16x16x32_bf16 v[140:143], a[92:95], a[20:23], v[140:143]\n" \
"buffer_store_dwordx4 v[204:207], v236, s[56:59], s93 offen offset:2048 sc1\n" \
"v_mfma_f32_16x16x32_bf16 v[172:175], a[88:91], a[24:27], v[172:175]\n" \
"v_accvgpr_read_b32 v253, a170\n" \
"v_accvgpr_read_b32 v254, a171\n" \
"v_mfma_f32_16x16x32_bf16 v[172:175], a[92:95], a[28:31], v[172:175]\n" \
"v_mul_f32 v243, s31, v243\n" \
"v_mul_f32 v252, s31, v252\n" \
"v_mfma_f32_16x16x32_bf16 v[168:171], a[80:83], a[24:27], v[168:171]\n" \
"v_mul_f32 v253, s31, v253\n" \
"v_mul_f32 v254, s31, v254\n" \
"v_mfma_f32_16x16x32_bf16 v[168:171], a[84:87], a[28:31], v[168:171]\n" \
"v_cvt_pk_bf16_f32 v20, v243, v252\n" \
"v_cvt_pk_bf16_f32 v21, v253, v254\n" \
"v_mfma_f32_16x16x32_bf16 v[164:167], a[72:75], a[24:27], v[164:167]\n" \
"v_accvgpr_read_b32 v243, a184\n" \
"v_accvgpr_read_b32 v252, a185\n" \
"v_mfma_f32_16x16x32_bf16 v[164:167], a[76:79], a[28:31], v[164:167]\n" \
"v_accvgpr_read_b32 v253, a186\n" \
"v_accvgpr_read_b32 v254, a187\n" \
"v_mfma_f32_16x16x32_bf16 v[160:163], a[64:67], a[24:27], v[160:163]\n" \
"v_mul_f32 v243, s31, v243\n" \
"v_mul_f32 v252, s31, v252\n" \
"v_mfma_f32_16x16x32_bf16 v[160:163], a[68:71], a[28:31], v[160:163]\n" \
"s_waitcnt vmcnt(35) lgkmcnt(0)\n" \
"s_barrier\n" \
"v_mfma_f32_16x16x32_bf16 v[80:83], a[96:99], a[0:3], v[80:83]\n" \
"ds_read_b64_tr_b16 a[64:65], v232 offset:49152\n" \
"v_mul_f32 v253, s31, v253\n" \
"v_mfma_f32_16x16x32_bf16 v[80:83], a[100:103], a[4:7], v[80:83]\n" \
"ds_read_b64_tr_b16 a[66:67], v232 offset:51200\n" \
"v_mul_f32 v254, s31, v254\n" \
"v_mfma_f32_16x16x32_bf16 v[84:87], a[104:107], a[0:3], v[84:87]\n" \
"s_add_u32 m0, s79, 32768\n" \
"ds_read_b64_tr_b16 a[68:69], v232 offset:57344\n" \
"v_cvt_pk_bf16_f32 v22, v243, v252\n" \
"v_mfma_f32_16x16x32_bf16 v[84:87], a[108:111], a[4:7], v[84:87]\n" \
"buffer_load_dwordx4 v228, s[52:55], s78 offen offset:0 lds\n" \
"v_mfma_f32_16x16x32_bf16 v[88:91], a[112:115], a[0:3], v[88:91]\n" \
"ds_read_b64_tr_b16 a[70:71], v232 offset:59392\n" \
"v_cvt_pk_bf16_f32 v23, v253, v254\n" \
"v_mfma_f32_16x16x32_bf16 v[88:91], a[116:119], a[4:7], v[88:91]\n" \
"ds_read_b64_tr_b16 a[72:73], v233 offset:49152\n" \
"v_accvgpr_read_b32 v243, a172\n" \
"v_mfma_f32_16x16x32_bf16 v[92:95], a[120:123], a[0:3], v[92:95]\n" \
"ds_read_b64_tr_b16 a[74:75], v233 offset:51200\n" \
"v_accvgpr_read_b32 v252, a173\n" \
"v_mfma_f32_16x16x32_bf16 v[92:95], a[124:127], a[4:7], v[92:95]\n" \
"buffer_load_dwordx4 v229, s[52:55], s78 offen offset:1024 lds\n" \
"v_mfma_f32_16x16x32_bf16 v[124:127], a[120:123], a[8:11], v[124:127]\n" \
"s_waitcnt vmcnt(35)\n" \
"ds_read_b128 a[0:3], v230 offset:0\n" \
"v_accvgpr_read_b32 v253, a174\n" \
"v_mfma_f32_16x16x32_bf16 v[124:127], a[124:127], a[12:15], v[124:127]\n" \
"ds_read_b128 a[4:7], v231 offset:0\n" \
"v_accvgpr_read_b32 v254, a175\n" \
"v_mfma_f32_16x16x32_bf16 v[120:123], a[112:115], a[8:11], v[120:123]\n" \
"s_xor_b32 s78, s78, 0x180000\n" \
"ds_read_b64_tr_b16 a[76:77], v233 offset:57344\n" \
"v_mul_f32 v243, s31, v243\n" \
"v_mfma_f32_16x16x32_bf16 v[120:123], a[116:119], a[12:15], v[120:123]\n" \
"buffer_load_dwordx4 v228, s[52:55], s89 offen offset:2048 lds\n" \
"v_mfma_f32_16x16x32_bf16 v[116:119], a[104:107], a[8:11], v[116:119]\n" \
"ds_read_b64_tr_b16 a[78:79], v233 offset:59392\n" \
"v_mul_f32 v252, s31, v252\n" \
"v_mfma_f32_16x16x32_bf16 v[116:119], a[108:111], a[12:15], v[116:119]\n" \
"ds_read_b64_tr_b16 a[80:81], v232 offset:49664\n" \
"v_mul_f32 v253, s31, v253\n" \
"v_mfma_f32_16x16x32_bf16 v[112:115], a[96:99], a[8:11], v[112:115]\n" \
"ds_read_b64_tr_b16 a[82:83], v232 offset:51712\n" \
"v_mul_f32 v254, s31, v254\n" \
"v_mfma_f32_16x16x32_bf16 v[112:115], a[100:103], a[12:15], v[112:115]\n" \
"buffer_load_dwordx4 v229, s[52:55], s89 offen offset:3072 lds\n" \
"v_mfma_f32_16x16x32_bf16 v[144:147], a[96:99], a[16:19], v[144:147]\n" \
"s_waitcnt vmcnt(35)\n" \
"s_xor_b32 s89, s89, 0x180000\n" \
"ds_read_b128 a[8:11], v230 offset:2048\n" \
"v_mfma_f32_16x16x32_bf16 v[144:147], a[100:103], a[20:23], v[144:147]\n" \
"s_add_u32 m0, s74, 65536\n" \
"ds_read_b128 a[12:15], v231 offset:2048\n" \
"v_cvt_pk_bf16_f32 v28, v243, v252\n" \
"v_mfma_f32_16x16x32_bf16 v[148:151], a[104:107], a[16:19], v[148:151]\n" \
"s_waitcnt lgkmcnt(6)\n" \
"ds_read_b64_tr_b16 a[84:85], v232 offset:57856\n" \
"v_cvt_pk_bf16_f32 v29, v253, v254\n" \
"v_mfma_f32_16x16x32_bf16 v[148:151], a[108:111], a[20:23], v[148:151]\n" \
"buffer_load_dwordx4 v224, s[48:51], s75 offen offset:0 lds\n" \
"v_mfma_f32_16x16x32_bf16 v[152:155], a[112:115], a[16:19], v[152:155]\n" \
"ds_read_b64_tr_b16 a[86:87], v232 offset:59904\n" \
"v_accvgpr_read_b32 v243, a188\n" \
"v_mfma_f32_16x16x32_bf16 v[152:155], a[116:119], a[20:23], v[152:155]\n" \
"ds_read_b64_tr_b16 a[88:89], v233 offset:49664\n" \
"v_accvgpr_read_b32 v252, a189\n" \
"v_mfma_f32_16x16x32_bf16 v[156:159], a[120:123], a[16:19], v[156:159]\n" \
"ds_read_b64_tr_b16 a[90:91], v233 offset:51712\n" \
"v_accvgpr_read_b32 v253, a190\n" \
"v_mfma_f32_16x16x32_bf16 v[156:159], a[124:127], a[20:23], v[156:159]\n" \
"buffer_load_dwordx4 v225, s[48:51], s75 offen offset:1024 lds\n" \
"v_mfma_f32_16x16x32_bf16 v[188:191], a[120:123], a[24:27], v[188:191]\n" \
"s_waitcnt vmcnt(35)\n" \
"ds_read_b128 a[16:19], v230 offset:4096\n" \
"v_accvgpr_read_b32 v254, a191\n" \
"v_mfma_f32_16x16x32_bf16 v[188:191], a[124:127], a[28:31], v[188:191]\n" \
"ds_read_b128 a[20:23], v231 offset:4096\n" \
"v_mul_f32 v243, s31, v243\n" \
"v_mfma_f32_16x16x32_bf16 v[184:187], a[112:115], a[24:27], v[184:187]\n" \
"s_waitcnt lgkmcnt(6)\n" \
"ds_read_b64_tr_b16 a[92:93], v233 offset:57856\n" \
"v_mul_f32 v252, s31, v252\n" \
"v_mfma_f32_16x16x32_bf16 v[184:187], a[116:119], a[28:31], v[184:187]\n" \
"buffer_load_dwordx4 v226, s[48:51], s75 offen offset:2048 lds\n" \
"v_mfma_f32_16x16x32_bf16 v[180:183], a[104:107], a[24:27], v[180:183]\n" \
"ds_read_b64_tr_b16 a[94:95], v233 offset:59904\n" \
"v_mul_f32 v253, s31, v253\n" \
"v_mfma_f32_16x16x32_bf16 v[180:183], a[108:111], a[28:31], v[180:183]\n" \
"v_mul_f32 v254, s31, v254\n" \
"v_cvt_pk_bf16_f32 v30, v243, v252\n" \
"v_mfma_f32_16x16x32_bf16 v[176:179], a[96:99], a[24:27], v[176:179]\n" \
"v_cvt_pk_bf16_f32 v31, v253, v254\n" \
"v_accvgpr_read_b32 v243, a128\n" \
"v_mfma_f32_16x16x32_bf16 v[176:179], a[100:103], a[28:31], v[176:179]\n" \
"buffer_load_dwordx4 v227, s[48:51], s75 offen offset:3072 lds\n" \
"v_mfma_f32_16x16x32_bf16 v[64:67], a[64:67], a[0:3], v[64:67]\n" \
"s_waitcnt vmcnt(35)\n" \
"s_xor_b32 s75, s75, 0x180\n" \
"ds_read_b128 a[24:27], v230 offset:6144\n" \
"v_mfma_f32_16x16x32_bf16 v[64:67], a[68:71], a[4:7], v[64:67]\n" \
"s_add_u32 m0, s74, 69632\n" \
"ds_read_b128 a[28:31], v231 offset:6144\n" \
"v_accvgpr_read_b32 v252, a129\n" \
"v_mfma_f32_16x16x32_bf16 v[68:71], a[72:75], a[0:3], v[68:71]\n" \
"s_waitcnt lgkmcnt(4)\n" \
"ds_read_b64_tr_b16 a[96:97], v232 offset:50176\n" \
"v_accvgpr_read_b32 v253, a130\n" \
"v_mfma_f32_16x16x32_bf16 v[68:71], a[76:79], a[4:7], v[68:71]\n" \
"buffer_load_dwordx4 v224, s[48:51], s77 offen offset:0 lds\n" \
"v_mfma_f32_16x16x32_bf16 v[72:75], a[80:83], a[0:3], v[72:75]\n" \
"ds_read_b64_tr_b16 a[98:99], v232 offset:52224\n" \
"v_accvgpr_read_b32 v254, a131\n" \
"v_mfma_f32_16x16x32_bf16 v[72:75], a[84:87], a[4:7], v[72:75]\n" \
"s_waitcnt lgkmcnt(3)\n" \
"ds_read_b64_tr_b16 a[100:101], v232 offset:58368\n" \
"v_mul_f32 v243, s31, v243\n" \
"v_mfma_f32_16x16x32_bf16 v[76:79], a[88:91], a[0:3], v[76:79]\n" \
"ds_read_b64_tr_b16 a[102:103], v232 offset:60416\n" \
"v_mul_f32 v252, s31, v252\n" \
"v_mfma_f32_16x16x32_bf16 v[76:79], a[92:95], a[4:7], v[76:79]\n" \
"buffer_load_dwordx4 v225, s[48:51], s77 offen offset:1024 lds\n" \
"v_mfma_f32_16x16x32_bf16 v[108:111], a[88:91], a[8:11], v[108:111]\n" \
"ds_read_b64_tr_b16 a[104:105], v233 offset:50176\n" \
"v_mul_f32 v253, s31, v253\n" \
"v_mfma_f32_16x16x32_bf16 v[108:111], a[92:95], a[12:15], v[108:111]\n" \
"ds_read_b64_tr_b16 a[106:107], v233 offset:52224\n" \
"v_mul_f32 v254, s31, v254\n" \
"v_mfma_f32_16x16x32_bf16 v[104:107], a[80:83], a[8:11], v[104:107]\n" \
"s_waitcnt lgkmcnt(6)\n" \
"ds_read_b64_tr_b16 a[108:109], v233 offset:58368\n" \
"v_cvt_pk_bf16_f32 v0, v243, v252\n" \
"v_mfma_f32_16x16x32_bf16 v[104:107], a[84:87], a[12:15], v[104:107]\n" \
"buffer_load_dwordx4 v226, s[48:51], s77 offen offset:2048 lds\n" \
"v_mfma_f32_16x16x32_bf16 v[100:103], a[72:75], a[8:11], v[100:103]\n" \
"ds_read_b64_tr_b16 a[110:111], v233 offset:60416\n" \
"v_cvt_pk_bf16_f32 v1, v253, v254\n" \
"v_mfma_f32_16x16x32_bf16 v[100:103], a[76:79], a[12:15], v[100:103]\n" \
"ds_read_b64_tr_b16 a[112:113], v232 offset:50688\n" \
"v_accvgpr_read_b32 v243, a144\n" \
"v_mfma_f32_16x16x32_bf16 v[96:99], a[64:67], a[8:11], v[96:99]\n" \
"ds_read_b64_tr_b16 a[114:115], v232 offset:52736\n" \
"v_accvgpr_read_b32 v252, a145\n" \
"v_mfma_f32_16x16x32_bf16 v[96:99], a[68:71], a[12:15], v[96:99]\n" \
"buffer_load_dwordx4 v227, s[48:51], s77 offen offset:3072 lds\n" \
"v_mfma_f32_16x16x32_bf16 v[128:131], a[64:67], a[16:19], v[128:131]\n" \
"ds_read_b64_tr_b16 a[116:117], v232 offset:58880\n" \
"v_accvgpr_read_b32 v253, a146\n" \
"v_mfma_f32_16x16x32_bf16 v[128:131], a[68:71], a[20:23], v[128:131]\n" \
"ds_read_b64_tr_b16 a[118:119], v232 offset:60928\n" \
"v_accvgpr_read_b32 v254, a147\n" \
"v_mfma_f32_16x16x32_bf16 v[132:135], a[72:75], a[16:19], v[132:135]\n" \
"s_xor_b32 s77, s77, 0x180\n" \
"ds_read_b64_tr_b16 a[120:121], v233 offset:50688\n" \
"v_mul_f32 v243, s31, v243\n" \
"v_mfma_f32_16x16x32_bf16 v[132:135], a[76:79], a[20:23], v[132:135]\n" \
"v_mul_f32 v252, s31, v252\n" \
"v_mul_f32 v253, s31, v253\n" \
"v_mfma_f32_16x16x32_bf16 v[136:139], a[80:83], a[16:19], v[136:139]\n" \
"ds_read_b64_tr_b16 a[122:123], v233 offset:52736\n" \
"v_mul_f32 v254, s31, v254\n" \
"v_mfma_f32_16x16x32_bf16 v[136:139], a[84:87], a[20:23], v[136:139]\n" \
"ds_read_b64_tr_b16 a[124:125], v233 offset:58880\n" \
"v_cvt_pk_bf16_f32 v2, v243, v252\n" \
"v_mfma_f32_16x16x32_bf16 v[140:143], a[88:91], a[16:19], v[140:143]\n" \
"ds_read_b64_tr_b16 a[126:127], v233 offset:60928\n" \
"v_cvt_pk_bf16_f32 v3, v253, v254\n" \
"v_mfma_f32_16x16x32_bf16 v[140:143], a[92:95], a[20:23], v[140:143]\n" \
"buffer_store_dwordx4 v[220:223], v236, s[56:59], s93 offen offset:2304 sc1\n" \
"v_mfma_f32_16x16x32_bf16 v[172:175], a[88:91], a[24:27], v[172:175]\n" \
"v_accvgpr_read_b32 v243, a132\n" \
"v_accvgpr_read_b32 v252, a133\n" \
"v_mfma_f32_16x16x32_bf16 v[172:175], a[92:95], a[28:31], v[172:175]\n" \
"v_accvgpr_read_b32 v253, a134\n" \
"v_accvgpr_read_b32 v254, a135\n" \
"v_mfma_f32_16x16x32_bf16 v[168:171], a[80:83], a[24:27], v[168:171]\n" \
"v_mul_f32 v243, s31, v243\n" \
"v_mul_f32 v252, s31, v252\n" \
"v_mfma_f32_16x16x32_bf16 v[168:171], a[84:87], a[28:31], v[168:171]\n" \
"v_mul_f32 v253, s31, v253\n" \
"v_mul_f32 v254, s31, v254\n" \
"v_mfma_f32_16x16x32_bf16 v[164:167], a[72:75], a[24:27], v[164:167]\n" \
"v_cvt_pk_bf16_f32 v4, v243, v252\n" \
"v_cvt_pk_bf16_f32 v5, v253, v254\n" \
"v_mfma_f32_16x16x32_bf16 v[164:167], a[76:79], a[28:31], v[164:167]\n" \
"v_accvgpr_read_b32 v243, a148\n" \
"v_accvgpr_read_b32 v252, a149\n" \
"v_mfma_f32_16x16x32_bf16 v[160:163], a[64:67], a[24:27], v[160:163]\n" \
"v_accvgpr_read_b32 v253, a150\n" \
"v_accvgpr_read_b32 v254, a151\n" \
"v_mfma_f32_16x16x32_bf16 v[160:163], a[68:71], a[28:31], v[160:163]\n" \
"s_waitcnt vmcnt(35) lgkmcnt(0)\n" \
"s_barrier\n" \
"v_mfma_f32_16x16x32_bf16 v[80:83], a[96:99], a[0:3], v[80:83]\n" \
"ds_read_b64_tr_b16 a[64:65], v232 offset:0\n" \
"v_mul_f32 v243, s31, v243\n" \
"v_mfma_f32_16x16x32_bf16 v[80:83], a[100:103], a[4:7], v[80:83]\n" \
"ds_read_b64_tr_b16 a[66:67], v232 offset:2048\n" \
"v_mul_f32 v252, s31, v252\n" \
"v_mfma_f32_16x16x32_bf16 v[84:87], a[104:107], a[0:3], v[84:87]\n" \
"s_add_u32 m0, s79, 49152\n" \
"ds_read_b64_tr_b16 a[68:69], v232 offset:8192\n" \
"v_mul_f32 v253, s31, v253\n" \
"v_mfma_f32_16x16x32_bf16 v[84:87], a[108:111], a[4:7], v[84:87]\n" \
"buffer_load_dwordx4 v228, s[52:55], s78 offen offset:0 lds\n" \
"v_mfma_f32_16x16x32_bf16 v[88:91], a[112:115], a[0:3], v[88:91]\n" \
"ds_read_b64_tr_b16 a[70:71], v232 offset:10240\n" \
"v_mul_f32 v254, s31, v254\n" \
"v_mfma_f32_16x16x32_bf16 v[88:91], a[116:119], a[4:7], v[88:91]\n" \
"ds_read_b64_tr_b16 a[72:73], v233 offset:0\n" \
"v_cvt_pk_bf16_f32 v6, v243, v252\n" \
"v_mfma_f32_16x16x32_bf16 v[92:95], a[120:123], a[0:3], v[92:95]\n" \
"ds_read_b64_tr_b16 a[74:75], v233 offset:2048\n" \
"v_cvt_pk_bf16_f32 v7, v253, v254\n" \
"v_mfma_f32_16x16x32_bf16 v[92:95], a[124:127], a[4:7], v[92:95]\n" \
"buffer_load_dwordx4 v229, s[52:55], s78 offen offset:1024 lds\n" \
"v_mfma_f32_16x16x32_bf16 v[124:127], a[120:123], a[8:11], v[124:127]\n" \
"s_waitcnt vmcnt(35)\n" \
"ds_read_b128 a[0:3], v230 offset:32768\n" \
"v_accvgpr_read_b32 v243, a136\n" \
"v_mfma_f32_16x16x32_bf16 v[124:127], a[124:127], a[12:15], v[124:127]\n" \
"ds_read_b128 a[4:7], v231 offset:32768\n" \
"v_accvgpr_read_b32 v252, a137\n" \
"v_mfma_f32_16x16x32_bf16 v[120:123], a[112:115], a[8:11], v[120:123]\n" \
"s_xor_b32 s78, s78, 0x300000\n" \
"ds_read_b64_tr_b16 a[76:77], v233 offset:8192\n" \
"v_accvgpr_read_b32 v253, a138\n" \
"v_mfma_f32_16x16x32_bf16 v[120:123], a[116:119], a[12:15], v[120:123]\n" \
"buffer_load_dwordx4 v228, s[52:55], s89 offen offset:2048 lds\n" \
"v_mfma_f32_16x16x32_bf16 v[116:119], a[104:107], a[8:11], v[116:119]\n" \
"ds_read_b64_tr_b16 a[78:79], v233 offset:10240\n" \
"v_accvgpr_read_b32 v254, a139\n" \
"v_mfma_f32_16x16x32_bf16 v[116:119], a[108:111], a[12:15], v[116:119]\n" \
"ds_read_b64_tr_b16 a[80:81], v232 offset:512\n" \
"v_mul_f32 v243, s31, v243\n" \
"v_mfma_f32_16x16x32_bf16 v[112:115], a[96:99], a[8:11], v[112:115]\n" \
"ds_read_b64_tr_b16 a[82:83], v232 offset:2560\n" \
"v_mul_f32 v252, s31, v252\n" \
"v_mfma_f32_16x16x32_bf16 v[112:115], a[100:103], a[12:15], v[112:115]\n" \
"buffer_load_dwordx4 v229, s[52:55], s89 offen offset:3072 lds\n" \
"v_mfma_f32_16x16x32_bf16 v[144:147], a[96:99], a[16:19], v[144:147]\n" \
"s_waitcnt vmcnt(35)\n" \
"s_xor_b32 s89, s89, 0x300000\n" \
"ds_read_b128 a[8:11], v230 offset:34816\n" \
"v_mfma_f32_16x16x32_bf16 v[144:147], a[100:103], a[20:23], v[144:147]\n" \
"s_add_u32 m0, s74, 98304\n" \
"ds_read_b128 a[12:15], v231 offset:34816\n" \
"v_mul_f32 v253, s31, v253\n" \
"v_mfma_f32_16x16x32_bf16 v[148:151], a[104:107], a[16:19], v[148:151]\n" \
"s_waitcnt lgkmcnt(6)\n" \
"ds_read_b64_tr_b16 a[84:85], v232 offset:8704\n" \
"v_mul_f32 v254, s31, v254\n" \
"v_mfma_f32_16x16x32_bf16 v[148:151], a[108:111], a[20:23], v[148:151]\n" \
"buffer_load_dwordx4 v224, s[48:51], s75 offen offset:0 lds\n" \
"v_mfma_f32_16x16x32_bf16 v[152:155], a[112:115], a[16:19], v[152:155]\n" \
"ds_read_b64_tr_b16 a[86:87], v232 offset:10752\n" \
"v_cvt_pk_bf16_f32 v8, v243, v252\n" \
"v_mfma_f32_16x16x32_bf16 v[152:155], a[116:119], a[20:23], v[152:155]\n" \
"ds_read_b64_tr_b16 a[88:89], v233 offset:512\n" \
"v_cvt_pk_bf16_f32 v9, v253, v254\n" \
"v_mfma_f32_16x16x32_bf16 v[156:159], a[120:123], a[16:19], v[156:159]\n" \
"ds_read_b64_tr_b16 a[90:91], v233 offset:2560\n" \
"v_accvgpr_read_b32 v243, a152\n" \
"v_mfma_f32_16x16x32_bf16 v[156:159], a[124:127], a[20:23], v[156:159]\n" \
"buffer_load_dwordx4 v225, s[48:51], s75 offen offset:1024 lds\n" \
"v_mfma_f32_16x16x32_bf16 v[188:191], a[120:123], a[24:27], v[188:191]\n" \
"s_waitcnt vmcnt(35)\n" \
"ds_read_b128 a[16:19], v230 offset:36864\n" \
"v_accvgpr_read_b32 v252, a153\n" \
"v_mfma_f32_16x16x32_bf16 v[188:191], a[124:127], a[28:31], v[188:191]\n" \
"ds_read_b128 a[20:23], v231 offset:36864\n" \
"v_accvgpr_read_b32 v253, a154\n" \
"v_mfma_f32_16x16x32_bf16 v[184:187], a[112:115], a[24:27], v[184:187]\n" \
"s_waitcnt lgkmcnt(6)\n" \
"ds_read_b64_tr_b16 a[92:93], v233 offset:8704\n" \
"v_accvgpr_read_b32 v254, a155\n" \
"v_mfma_f32_16x16x32_bf16 v[184:187], a[116:119], a[28:31], v[184:187]\n" \
"buffer_load_dwordx4 v226, s[48:51], s75 offen offset:2048 lds\n" \
"v_mfma_f32_16x16x32_bf16 v[180:183], a[104:107], a[24:27], v[180:183]\n" \
"ds_read_b64_tr_b16 a[94:95], v233 offset:10752\n" \
"v_mul_f32 v243, s31, v243\n" \
"v_mfma_f32_16x16x32_bf16 v[180:183], a[108:111], a[28:31], v[180:183]\n" \
"v_mul_f32 v252, s31, v252\n" \
"v_mul_f32 v253, s31, v253\n" \
"v_mfma_f32_16x16x32_bf16 v[176:179], a[96:99], a[24:27], v[176:179]\n" \
"v_mul_f32 v254, s31, v254\n" \
"v_cvt_pk_bf16_f32 v10, v243, v252\n" \
"v_mfma_f32_16x16x32_bf16 v[176:179], a[100:103], a[28:31], v[176:179]\n" \
"buffer_load_dwordx4 v227, s[48:51], s75 offen offset:3072 lds\n" \
"v_mfma_f32_16x16x32_bf16 v[64:67], a[64:67], a[0:3], v[64:67]\n" \
"s_waitcnt vmcnt(35)\n" \
"s_xor_b32 s75, s75, 0x300\n" \
"ds_read_b128 a[24:27], v230 offset:38912\n" \
"v_mfma_f32_16x16x32_bf16 v[64:67], a[68:71], a[4:7], v[64:67]\n" \
"s_add_u32 m0, s74, 102400\n" \
"ds_read_b128 a[28:31], v231 offset:38912\n" \
"v_cvt_pk_bf16_f32 v11, v253, v254\n" \
"v_mfma_f32_16x16x32_bf16 v[68:71], a[72:75], a[0:3], v[68:71]\n" \
"s_waitcnt lgkmcnt(4)\n" \
"ds_read_b64_tr_b16 a[96:97], v232 offset:1024\n" \
"v_accvgpr_read_b32 v243, a140\n" \
"v_mfma_f32_16x16x32_bf16 v[68:71], a[76:79], a[4:7], v[68:71]\n" \
"buffer_load_dwordx4 v224, s[48:51], s77 offen offset:0 lds\n" \
"v_mfma_f32_16x16x32_bf16 v[72:75], a[80:83], a[0:3], v[72:75]\n" \
"ds_read_b64_tr_b16 a[98:99], v232 offset:3072\n" \
"v_accvgpr_read_b32 v252, a141\n" \
"v_mfma_f32_16x16x32_bf16 v[72:75], a[84:87], a[4:7], v[72:75]\n" \
"s_waitcnt lgkmcnt(3)\n" \
"ds_read_b64_tr_b16 a[100:101], v232 offset:9216\n" \
"v_accvgpr_read_b32 v253, a142\n" \
"v_mfma_f32_16x16x32_bf16 v[76:79], a[88:91], a[0:3], v[76:79]\n" \
"ds_read_b64_tr_b16 a[102:103], v232 offset:11264\n" \
"v_accvgpr_read_b32 v254, a143\n" \
"v_mfma_f32_16x16x32_bf16 v[76:79], a[92:95], a[4:7], v[76:79]\n" \
"buffer_load_dwordx4 v225, s[48:51], s77 offen offset:1024 lds\n" \
"v_mfma_f32_16x16x32_bf16 v[108:111], a[88:91], a[8:11], v[108:111]\n" \
"ds_read_b64_tr_b16 a[104:105], v233 offset:1024\n" \
"v_mul_f32 v243, s31, v243\n" \
"v_mfma_f32_16x16x32_bf16 v[108:111], a[92:95], a[12:15], v[108:111]\n" \
"ds_read_b64_tr_b16 a[106:107], v233 offset:3072\n" \
"v_mul_f32 v252, s31, v252\n" \
"v_mfma_f32_16x16x32_bf16 v[104:107], a[80:83], a[8:11], v[104:107]\n" \
"s_waitcnt lgkmcnt(6)\n" \
"ds_read_b64_tr_b16 a[108:109], v233 offset:9216\n" \
"v_mul_f32 v253, s31, v253\n" \
"v_mfma_f32_16x16x32_bf16 v[104:107], a[84:87], a[12:15], v[104:107]\n" \
"buffer_load_dwordx4 v226, s[48:51], s77 offen offset:2048 lds\n" \
"v_mfma_f32_16x16x32_bf16 v[100:103], a[72:75], a[8:11], v[100:103]\n" \
"ds_read_b64_tr_b16 a[110:111], v233 offset:11264\n" \
"v_mul_f32 v254, s31, v254\n" \
"v_mfma_f32_16x16x32_bf16 v[100:103], a[76:79], a[12:15], v[100:103]\n" \
"ds_read_b64_tr_b16 a[112:113], v232 offset:1536\n" \
"v_cvt_pk_bf16_f32 v12, v243, v252\n" \
"v_mfma_f32_16x16x32_bf16 v[96:99], a[64:67], a[8:11], v[96:99]\n" \
"ds_read_b64_tr_b16 a[114:115], v232 offset:3584\n" \
"v_cvt_pk_bf16_f32 v13, v253, v254\n" \
"v_mfma_f32_16x16x32_bf16 v[96:99], a[68:71], a[12:15], v[96:99]\n" \
"buffer_load_dwordx4 v227, s[48:51], s77 offen offset:3072 lds\n" \
"v_mfma_f32_16x16x32_bf16 v[128:131], a[64:67], a[16:19], v[128:131]\n" \
"ds_read_b64_tr_b16 a[116:117], v232 offset:9728\n" \
"v_accvgpr_read_b32 v243, a156\n" \
"v_mfma_f32_16x16x32_bf16 v[128:131], a[68:71], a[20:23], v[128:131]\n" \
"ds_read_b64_tr_b16 a[118:119], v232 offset:11776\n" \
"v_accvgpr_read_b32 v252, a157\n" \
"v_mfma_f32_16x16x32_bf16 v[132:135], a[72:75], a[16:19], v[132:135]\n" \
"s_xor_b32 s77, s77, 0x300\n" \
"ds_read_b64_tr_b16 a[120:121], v233 offset:1536\n" \
"v_accvgpr_read_b32 v253, a158\n" \
"v_mfma_f32_16x16x32_bf16 v[132:135], a[76:79], a[20:23], v[132:135]\n" \
"v_accvgpr_read_b32 v254, a159\n" \
"v_mul_f32 v243, s31, v243\n" \
"v_mfma_f32_16x16x32_bf16 v[136:139], a[80:83], a[16:19], v[136:139]\n" \
"ds_read_b64_tr_b16 a[122:123], v233 offset:3584\n" \
"v_mul_f32 v252, s31, v252\n" \
"v_mfma_f32_16x16x32_bf16 v[136:139], a[84:87], a[20:23], v[136:139]\n" \
"ds_read_b64_tr_b16 a[124:125], v233 offset:9728\n" \
"v_mul_f32 v253, s31, v253\n" \
"v_mfma_f32_16x16x32_bf16 v[140:143], a[88:91], a[16:19], v[140:143]\n" \
"s_add_u32 s93, s87, 17858560\n" \
"ds_read_b64_tr_b16 a[126:127], v233 offset:11776\n" \
"v_mul_f32 v254, s31, v254\n" \
"v_mfma_f32_16x16x32_bf16 v[140:143], a[92:95], a[20:23], v[140:143]\n" \
"buffer_store_dwordx4 a[224:227], v234, s[56:59], s93 offen offset:0 sc1\n" \
"v_mfma_f32_16x16x32_bf16 v[172:175], a[88:91], a[24:27], v[172:175]\n" \
"v_cvt_pk_bf16_f32 v14, v243, v252\n" \
"v_cvt_pk_bf16_f32 v15, v253, v254\n" \
"v_mfma_f32_16x16x32_bf16 v[172:175], a[92:95], a[28:31], v[172:175]\n" \
"v_accvgpr_write_b32 a56, v244\n" \
"v_accvgpr_write_b32 a57, v245\n" \
"v_mfma_f32_16x16x32_bf16 v[168:171], a[80:83], a[24:27], v[168:171]\n" \
"v_accvgpr_write_b32 a59, v247\n" \
"v_accvgpr_write_b32 a60, v248\n" \
"v_mfma_f32_16x16x32_bf16 v[168:171], a[84:87], a[28:31], v[168:171]\n" \
"v_accvgpr_write_b32 a61, v249\n" \
"v_accvgpr_write_b32 a48, 0\n" \
"v_mfma_f32_16x16x32_bf16 v[164:167], a[72:75], a[24:27], v[164:167]\n" \
"v_accvgpr_write_b32 a49, 0\n" \
"v_accvgpr_write_b32 a50, 0\n" \
"v_mfma_f32_16x16x32_bf16 v[164:167], a[76:79], a[28:31], v[164:167]\n" \
"v_accvgpr_write_b32 a51, 0\n" \
"v_accvgpr_write_b32 a52, 0\n" \
"v_mfma_f32_16x16x32_bf16 v[160:163], a[64:67], a[24:27], v[160:163]\n" \
"v_accvgpr_write_b32 a53, 0\n" \
"v_mfma_f32_16x16x32_bf16 v[160:163], a[68:71], a[28:31], v[160:163]\n" \
"s_waitcnt vmcnt(35) lgkmcnt(0)\n" \
"s_barrier\n" \
"v_mfma_f32_16x16x32_bf16 v[80:83], a[96:99], a[0:3], v[80:83]\n" \
"ds_read_b64_tr_b16 a[64:65], v232 offset:16384\n" \
"v_mfma_f32_16x16x32_bf16 v[80:83], a[100:103], a[4:7], v[80:83]\n" \
"ds_read_b64_tr_b16 a[66:67], v232 offset:18432\n" \
"v_mfma_f32_16x16x32_bf16 v[84:87], a[104:107], a[0:3], v[84:87]\n" \
"s_add_u32 m0, s79, 0\n" \
"ds_read_b64_tr_b16 a[68:69], v232 offset:24576\n" \
"v_mfma_f32_16x16x32_bf16 v[84:87], a[108:111], a[4:7], v[84:87]\n" \
"buffer_load_dwordx4 v228, s[52:55], s78 offen offset:0 lds\n" \
"v_mfma_f32_16x16x32_bf16 v[88:91], a[112:115], a[0:3], v[88:91]\n" \
"ds_read_b64_tr_b16 a[70:71], v232 offset:26624\n" \
"v_mfma_f32_16x16x32_bf16 v[88:91], a[116:119], a[4:7], v[88:91]\n" \
"ds_read_b64_tr_b16 a[72:73], v233 offset:16384\n" \
"v_mfma_f32_16x16x32_bf16 v[92:95], a[120:123], a[0:3], v[92:95]\n" \
"ds_read_b64_tr_b16 a[74:75], v233 offset:18432\n" \
"v_mfma_f32_16x16x32_bf16 v[92:95], a[124:127], a[4:7], v[92:95]\n" \
"buffer_load_dwordx4 v229, s[52:55], s78 offen offset:1024 lds\n" \
"v_mfma_f32_16x16x32_bf16 v[124:127], a[120:123], a[8:11], v[124:127]\n" \
"s_waitcnt vmcnt(35)\n" \
"ds_read_b128 a[0:3], v250 offset:0\n" \
"v_mfma_f32_16x16x32_bf16 v[124:127], a[124:127], a[12:15], v[124:127]\n" \
"ds_read_b128 a[4:7], v251 offset:0\n" \
"v_mfma_f32_16x16x32_bf16 v[120:123], a[112:115], a[8:11], v[120:123]\n" \
"s_xor_b32 s78, s78, 0x80000\n" \
"ds_read_b64_tr_b16 a[76:77], v233 offset:24576\n" \
"v_mfma_f32_16x16x32_bf16 v[120:123], a[116:119], a[12:15], v[120:123]\n" \
"buffer_load_dwordx4 v228, s[52:55], s89 offen offset:2048 lds\n" \
"v_mfma_f32_16x16x32_bf16 v[116:119], a[104:107], a[8:11], v[116:119]\n" \
"ds_read_b64_tr_b16 a[78:79], v233 offset:26624\n" \
"v_mfma_f32_16x16x32_bf16 v[116:119], a[108:111], a[12:15], v[116:119]\n" \
"ds_read_b64_tr_b16 a[80:81], v232 offset:16896\n" \
"v_mfma_f32_16x16x32_bf16 v[112:115], a[96:99], a[8:11], v[112:115]\n" \
"ds_read_b64_tr_b16 a[82:83], v232 offset:18944\n" \
"v_mfma_f32_16x16x32_bf16 v[112:115], a[100:103], a[12:15], v[112:115]\n" \
"buffer_load_dwordx4 v229, s[52:55], s89 offen offset:3072 lds\n" \
"v_mfma_f32_16x16x32_bf16 v[144:147], a[96:99], a[16:19], v[144:147]\n" \
"s_waitcnt vmcnt(35)\n" \
"s_xor_b32 s89, s89, 0x80000\n" \
"ds_read_b128 a[8:11], v250 offset:2048\n" \
"v_mfma_f32_16x16x32_bf16 v[144:147], a[100:103], a[20:23], v[144:147]\n" \
"s_add_u32 m0, s74, 131072\n" \
"ds_read_b128 a[12:15], v251 offset:2048\n" \
"v_mfma_f32_16x16x32_bf16 v[148:151], a[104:107], a[16:19], v[148:151]\n" \
"s_waitcnt lgkmcnt(6)\n" \
"ds_read_b64_tr_b16 a[84:85], v232 offset:25088\n" \
"v_mfma_f32_16x16x32_bf16 v[148:151], a[108:111], a[20:23], v[148:151]\n" \
"buffer_load_dwordx4 v224, s[48:51], s75 offen offset:0 lds\n" \
"v_mfma_f32_16x16x32_bf16 v[152:155], a[112:115], a[16:19], v[152:155]\n" \
"ds_read_b64_tr_b16 a[86:87], v232 offset:27136\n" \
"v_mfma_f32_16x16x32_bf16 v[152:155], a[116:119], a[20:23], v[152:155]\n" \
"ds_read_b64_tr_b16 a[88:89], v233 offset:16896\n" \
"v_mfma_f32_16x16x32_bf16 v[156:159], a[120:123], a[16:19], v[156:159]\n" \
"ds_read_b64_tr_b16 a[90:91], v233 offset:18944\n" \
"v_mfma_f32_16x16x32_bf16 v[156:159], a[124:127], a[20:23], v[156:159]\n" \
"buffer_load_dwordx4 v225, s[48:51], s75 offen offset:1024 lds\n" \
"v_mfma_f32_16x16x32_bf16 v[188:191], a[120:123], a[24:27], v[188:191]\n" \
"s_waitcnt vmcnt(35)\n" \
"ds_read_b128 a[16:19], v250 offset:4096\n" \
"v_mfma_f32_16x16x32_bf16 v[188:191], a[124:127], a[28:31], v[188:191]\n" \
"ds_read_b128 a[20:23], v251 offset:4096\n" \
"v_mfma_f32_16x16x32_bf16 v[184:187], a[112:115], a[24:27], v[184:187]\n" \
"s_waitcnt lgkmcnt(6)\n" \
"ds_read_b64_tr_b16 a[92:93], v233 offset:25088\n" \
"v_mfma_f32_16x16x32_bf16 v[184:187], a[116:119], a[28:31], v[184:187]\n" \
"buffer_load_dwordx4 v226, s[48:51], s75 offen offset:2048 lds\n" \
"v_mfma_f32_16x16x32_bf16 v[180:183], a[104:107], a[24:27], v[180:183]\n" \
"ds_read_b64_tr_b16 a[94:95], v233 offset:27136\n" \
"v_mfma_f32_16x16x32_bf16 v[180:183], a[108:111], a[28:31], v[180:183]\n" \
"v_mfma_f32_16x16x32_bf16 v[176:179], a[96:99], a[24:27], v[176:179]\n" \
"v_mfma_f32_16x16x32_bf16 v[176:179], a[100:103], a[28:31], v[176:179]\n" \
"buffer_load_dwordx4 v227, s[48:51], s75 offen offset:3072 lds\n" \
"v_mfma_f32_16x16x32_bf16 v[64:67], a[64:67], a[0:3], v[64:67]\n" \
"s_waitcnt vmcnt(35)\n" \
"s_xor_b32 s75, s75, 0x80\n" \
"ds_read_b128 a[24:27], v250 offset:6144\n" \
"v_mfma_f32_16x16x32_bf16 v[64:67], a[68:71], a[4:7], v[64:67]\n" \
"s_add_u32 m0, s74, 135168\n" \
"ds_read_b128 a[28:31], v251 offset:6144\n" \
"v_mfma_f32_16x16x32_bf16 v[68:71], a[72:75], a[0:3], v[68:71]\n" \
"s_waitcnt lgkmcnt(2)\n" \
"ds_read_b64_tr_b16 a[96:97], v232 offset:17408\n" \
"v_mfma_f32_16x16x32_bf16 v[68:71], a[76:79], a[4:7], v[68:71]\n" \
"buffer_load_dwordx4 v224, s[48:51], s77 offen offset:0 lds\n" \
"v_mfma_f32_16x16x32_bf16 v[72:75], a[80:83], a[0:3], v[72:75]\n" \
"ds_read_b64_tr_b16 a[98:99], v232 offset:19456\n" \
"v_mfma_f32_16x16x32_bf16 v[72:75], a[84:87], a[4:7], v[72:75]\n" \
"ds_read_b64_tr_b16 a[100:101], v232 offset:25600\n" \
"v_mfma_f32_16x16x32_bf16 v[76:79], a[88:91], a[0:3], v[76:79]\n" \
"ds_read_b64_tr_b16 a[102:103], v232 offset:27648\n" \
"v_mfma_f32_16x16x32_bf16 v[76:79], a[92:95], a[4:7], v[76:79]\n" \
"buffer_load_dwordx4 v225, s[48:51], s77 offen offset:1024 lds\n" \
"v_mfma_f32_16x16x32_bf16 v[108:111], a[88:91], a[8:11], v[108:111]\n" \
"ds_read_b64_tr_b16 a[104:105], v233 offset:17408\n" \
"v_mfma_f32_16x16x32_bf16 v[108:111], a[92:95], a[12:15], v[108:111]\n" \
"ds_read_b64_tr_b16 a[106:107], v233 offset:19456\n" \
"v_mfma_f32_16x16x32_bf16 v[104:107], a[80:83], a[8:11], v[104:107]\n" \
"s_waitcnt lgkmcnt(6)\n" \
"ds_read_b64_tr_b16 a[108:109], v233 offset:25600\n" \
"v_mfma_f32_16x16x32_bf16 v[104:107], a[84:87], a[12:15], v[104:107]\n" \
"buffer_load_dwordx4 v226, s[48:51], s77 offen offset:2048 lds\n" \
"v_mfma_f32_16x16x32_bf16 v[100:103], a[72:75], a[8:11], v[100:103]\n" \
"ds_read_b64_tr_b16 a[110:111], v233 offset:27648\n" \
"Lpg3903_%=:\n" \
"s_cbranch_execz Lpg6062_%=\n" \
"v_mfma_f32_16x16x32_bf16 v[100:103], a[76:79], a[12:15], v[100:103]\n" \
"ds_read_b64_tr_b16 a[112:113], v232 offset:17920\n" \
"v_mfma_f32_16x16x32_bf16 v[96:99], a[64:67], a[8:11], v[96:99]\n" \
"ds_read_b64_tr_b16 a[114:115], v232 offset:19968\n" \
"v_mfma_f32_16x16x32_bf16 v[96:99], a[68:71], a[12:15], v[96:99]\n" \
"buffer_load_dwordx4 v227, s[48:51], s77 offen offset:3072 lds\n" \
"v_mfma_f32_16x16x32_bf16 v[128:131], a[64:67], a[16:19], v[128:131]\n" \
"ds_read_b64_tr_b16 a[116:117], v232 offset:26112\n" \
"v_mfma_f32_16x16x32_bf16 v[128:131], a[68:71], a[20:23], v[128:131]\n" \
"ds_read_b64_tr_b16 a[118:119], v232 offset:28160\n" \
"v_mfma_f32_16x16x32_bf16 v[132:135], a[72:75], a[16:19], v[132:135]\n" \
"s_xor_b32 s77, s77, 0x80\n" \
"ds_read_b64_tr_b16 a[120:121], v233 offset:17920\n" \
"v_mfma_f32_16x16x32_bf16 v[132:135], a[76:79], a[20:23], v[132:135]\n" \
"v_mfma_f32_16x16x32_bf16 v[136:139], a[80:83], a[16:19], v[136:139]\n" \
"ds_read_b64_tr_b16 a[122:123], v233 offset:19968\n" \
"v_mfma_f32_16x16x32_bf16 v[136:139], a[84:87], a[20:23], v[136:139]\n" \
"ds_read_b64_tr_b16 a[124:125], v233 offset:26112\n" \
"v_mfma_f32_16x16x32_bf16 v[140:143], a[88:91], a[16:19], v[140:143]\n" \
"ds_read_b64_tr_b16 a[126:127], v233 offset:28160\n" \
"v_mfma_f32_16x16x32_bf16 v[140:143], a[92:95], a[20:23], v[140:143]\n" \
"buffer_store_dwordx4 a[228:231], v234, s[56:59], s93 offen offset:1024 sc1\n" \
"v_mfma_f32_16x16x32_bf16 v[172:175], a[88:91], a[24:27], v[172:175]\n" \
"v_mfma_f32_16x16x32_bf16 v[172:175], a[92:95], a[28:31], v[172:175]\n" \
"v_mfma_f32_16x16x32_bf16 v[168:171], a[80:83], a[24:27], v[168:171]\n" \
"v_mfma_f32_16x16x32_bf16 v[168:171], a[84:87], a[28:31], v[168:171]\n" \
"v_mfma_f32_16x16x32_bf16 v[164:167], a[72:75], a[24:27], v[164:167]\n" \
"v_mfma_f32_16x16x32_bf16 v[164:167], a[76:79], a[28:31], v[164:167]\n" \
"v_mfma_f32_16x16x32_bf16 v[160:163], a[64:67], a[24:27], v[160:163]\n" \
"v_mfma_f32_16x16x32_bf16 v[160:163], a[68:71], a[28:31], v[160:163]\n" \
"s_waitcnt vmcnt(35) lgkmcnt(0)\n" \
"s_barrier\n" \
"v_mfma_f32_16x16x32_bf16 v[80:83], a[96:99], a[0:3], v[80:83]\n" \
"ds_read_b64_tr_b16 a[64:65], v232 offset:32768\n" \
"v_mfma_f32_16x16x32_bf16 v[80:83], a[100:103], a[4:7], v[80:83]\n" \
"ds_read_b64_tr_b16 a[66:67], v232 offset:34816\n" \
"v_mfma_f32_16x16x32_bf16 v[84:87], a[104:107], a[0:3], v[84:87]\n" \
"s_add_u32 m0, s79, 16384\n" \
"ds_read_b64_tr_b16 a[68:69], v232 offset:40960\n" \
"v_mfma_f32_16x16x32_bf16 v[84:87], a[108:111], a[4:7], v[84:87]\n" \
"buffer_load_dwordx4 v228, s[52:55], s78 offen offset:0 lds\n" \
"v_mfma_f32_16x16x32_bf16 v[88:91], a[112:115], a[0:3], v[88:91]\n" \
"ds_read_b64_tr_b16 a[70:71], v232 offset:43008\n" \
"v_mfma_f32_16x16x32_bf16 v[88:91], a[116:119], a[4:7], v[88:91]\n" \
"ds_read_b64_tr_b16 a[72:73], v233 offset:32768\n" \
"v_mfma_f32_16x16x32_bf16 v[92:95], a[120:123], a[0:3], v[92:95]\n" \
"ds_read_b64_tr_b16 a[74:75], v233 offset:34816\n" \
"v_mfma_f32_16x16x32_bf16 v[92:95], a[124:127], a[4:7], v[92:95]\n" \
"buffer_load_dwordx4 v229, s[52:55], s78 offen offset:1024 lds\n" \
"v_mfma_f32_16x16x32_bf16 v[124:127], a[120:123], a[8:11], v[124:127]\n" \
"s_waitcnt vmcnt(35)\n" \
"ds_read_b128 a[0:3], v230 offset:0\n" \
"v_mfma_f32_16x16x32_bf16 v[124:127], a[124:127], a[12:15], v[124:127]\n" \
"ds_read_b128 a[4:7], v231 offset:0\n" \
"v_mfma_f32_16x16x32_bf16 v[120:123], a[112:115], a[8:11], v[120:123]\n" \
"s_xor_b32 s78, s78, 0x180000\n" \
"ds_read_b64_tr_b16 a[76:77], v233 offset:40960\n" \
"v_mfma_f32_16x16x32_bf16 v[120:123], a[116:119], a[12:15], v[120:123]\n" \
"buffer_load_dwordx4 v228, s[52:55], s89 offen offset:2048 lds\n" \
"v_mfma_f32_16x16x32_bf16 v[116:119], a[104:107], a[8:11], v[116:119]\n" \
"ds_read_b64_tr_b16 a[78:79], v233 offset:43008\n" \
"v_mfma_f32_16x16x32_bf16 v[116:119], a[108:111], a[12:15], v[116:119]\n" \
"ds_read_b64_tr_b16 a[80:81], v232 offset:33280\n" \
"v_mfma_f32_16x16x32_bf16 v[112:115], a[96:99], a[8:11], v[112:115]\n" \
"ds_read_b64_tr_b16 a[82:83], v232 offset:35328\n" \
"v_mfma_f32_16x16x32_bf16 v[112:115], a[100:103], a[12:15], v[112:115]\n" \
"buffer_load_dwordx4 v229, s[52:55], s89 offen offset:3072 lds\n" \
"v_mfma_f32_16x16x32_bf16 v[144:147], a[96:99], a[16:19], v[144:147]\n" \
"s_waitcnt vmcnt(35)\n" \
"s_xor_b32 s89, s89, 0x180000\n" \
"ds_read_b128 a[8:11], v230 offset:2048\n" \
"v_mfma_f32_16x16x32_bf16 v[144:147], a[100:103], a[20:23], v[144:147]\n" \
"s_add_u32 m0, s74, 65536\n" \
"ds_read_b128 a[12:15], v231 offset:2048\n" \
"v_mfma_f32_16x16x32_bf16 v[148:151], a[104:107], a[16:19], v[148:151]\n" \
"s_waitcnt lgkmcnt(6)\n" \
"ds_read_b64_tr_b16 a[84:85], v232 offset:41472\n" \
"v_mfma_f32_16x16x32_bf16 v[148:151], a[108:111], a[20:23], v[148:151]\n" \
"buffer_load_dwordx4 v224, s[48:51], s75 offen offset:0 lds\n" \
"v_mfma_f32_16x16x32_bf16 v[152:155], a[112:115], a[16:19], v[152:155]\n" \
"ds_read_b64_tr_b16 a[86:87], v232 offset:43520\n" \
"v_mfma_f32_16x16x32_bf16 v[152:155], a[116:119], a[20:23], v[152:155]\n" \
"ds_read_b64_tr_b16 a[88:89], v233 offset:33280\n" \
"v_mfma_f32_16x16x32_bf16 v[156:159], a[120:123], a[16:19], v[156:159]\n" \
"ds_read_b64_tr_b16 a[90:91], v233 offset:35328\n" \
"v_mfma_f32_16x16x32_bf16 v[156:159], a[124:127], a[20:23], v[156:159]\n" \
"buffer_load_dwordx4 v225, s[48:51], s75 offen offset:1024 lds\n" \
"v_mfma_f32_16x16x32_bf16 v[188:191], a[120:123], a[24:27], v[188:191]\n" \
"s_waitcnt vmcnt(35)\n" \
"ds_read_b128 a[16:19], v230 offset:4096\n" \
"v_mfma_f32_16x16x32_bf16 v[188:191], a[124:127], a[28:31], v[188:191]\n" \
"ds_read_b128 a[20:23], v231 offset:4096\n" \
"v_mfma_f32_16x16x32_bf16 v[184:187], a[112:115], a[24:27], v[184:187]\n" \
"s_waitcnt lgkmcnt(6)\n" \
"ds_read_b64_tr_b16 a[92:93], v233 offset:41472\n" \
"v_mfma_f32_16x16x32_bf16 v[184:187], a[116:119], a[28:31], v[184:187]\n" \
"buffer_load_dwordx4 v226, s[48:51], s75 offen offset:2048 lds\n" \
"v_mfma_f32_16x16x32_bf16 v[180:183], a[104:107], a[24:27], v[180:183]\n" \
"ds_read_b64_tr_b16 a[94:95], v233 offset:43520\n" \
"v_mfma_f32_16x16x32_bf16 v[180:183], a[108:111], a[28:31], v[180:183]\n" \
"v_mfma_f32_16x16x32_bf16 v[176:179], a[96:99], a[24:27], v[176:179]\n" \
"v_mfma_f32_16x16x32_bf16 v[176:179], a[100:103], a[28:31], v[176:179]\n" \
"buffer_load_dwordx4 v227, s[48:51], s75 offen offset:3072 lds\n" \
"v_mfma_f32_16x16x32_bf16 v[64:67], a[64:67], a[0:3], v[64:67]\n" \
"s_waitcnt vmcnt(35)\n" \
"s_xor_b32 s75, s75, 0x180\n" \
"ds_read_b128 a[24:27], v230 offset:6144\n" \
"v_mfma_f32_16x16x32_bf16 v[64:67], a[68:71], a[4:7], v[64:67]\n" \
"s_add_u32 m0, s74, 69632\n" \
"ds_read_b128 a[28:31], v231 offset:6144\n" \
"v_mfma_f32_16x16x32_bf16 v[68:71], a[72:75], a[0:3], v[68:71]\n" \
"s_waitcnt lgkmcnt(2)\n" \
"ds_read_b64_tr_b16 a[96:97], v232 offset:33792\n" \
"v_mfma_f32_16x16x32_bf16 v[68:71], a[76:79], a[4:7], v[68:71]\n" \
"buffer_load_dwordx4 v224, s[48:51], s77 offen offset:0 lds\n" \
"v_mfma_f32_16x16x32_bf16 v[72:75], a[80:83], a[0:3], v[72:75]\n" \
"ds_read_b64_tr_b16 a[98:99], v232 offset:35840\n" \
"v_mfma_f32_16x16x32_bf16 v[72:75], a[84:87], a[4:7], v[72:75]\n" \
"ds_read_b64_tr_b16 a[100:101], v232 offset:41984\n" \
"v_mfma_f32_16x16x32_bf16 v[76:79], a[88:91], a[0:3], v[76:79]\n" \
"ds_read_b64_tr_b16 a[102:103], v232 offset:44032\n" \
"v_mfma_f32_16x16x32_bf16 v[76:79], a[92:95], a[4:7], v[76:79]\n" \
"buffer_load_dwordx4 v225, s[48:51], s77 offen offset:1024 lds\n" \
"v_mfma_f32_16x16x32_bf16 v[108:111], a[88:91], a[8:11], v[108:111]\n" \
"ds_read_b64_tr_b16 a[104:105], v233 offset:33792\n" \
"v_mfma_f32_16x16x32_bf16 v[108:111], a[92:95], a[12:15], v[108:111]\n" \
"ds_read_b64_tr_b16 a[106:107], v233 offset:35840\n" \
"v_mfma_f32_16x16x32_bf16 v[104:107], a[80:83], a[8:11], v[104:107]\n" \
"s_waitcnt lgkmcnt(6)\n" \
"ds_read_b64_tr_b16 a[108:109], v233 offset:41984\n" \
"v_mfma_f32_16x16x32_bf16 v[104:107], a[84:87], a[12:15], v[104:107]\n" \
"buffer_load_dwordx4 v226, s[48:51], s77 offen offset:2048 lds\n" \
"v_mfma_f32_16x16x32_bf16 v[100:103], a[72:75], a[8:11], v[100:103]\n" \
"ds_read_b64_tr_b16 a[110:111], v233 offset:44032\n" \
"v_mfma_f32_16x16x32_bf16 v[100:103], a[76:79], a[12:15], v[100:103]\n" \
"ds_read_b64_tr_b16 a[112:113], v232 offset:34304\n" \
"v_mfma_f32_16x16x32_bf16 v[96:99], a[64:67], a[8:11], v[96:99]\n" \
"ds_read_b64_tr_b16 a[114:115], v232 offset:36352\n" \
"v_mfma_f32_16x16x32_bf16 v[96:99], a[68:71], a[12:15], v[96:99]\n" \
"buffer_load_dwordx4 v227, s[48:51], s77 offen offset:3072 lds\n" \
"v_mfma_f32_16x16x32_bf16 v[128:131], a[64:67], a[16:19], v[128:131]\n" \
"ds_read_b64_tr_b16 a[116:117], v232 offset:42496\n" \
"v_mfma_f32_16x16x32_bf16 v[128:131], a[68:71], a[20:23], v[128:131]\n" \
"ds_read_b64_tr_b16 a[118:119], v232 offset:44544\n" \
"v_mfma_f32_16x16x32_bf16 v[132:135], a[72:75], a[16:19], v[132:135]\n" \
"s_xor_b32 s77, s77, 0x180\n" \
"ds_read_b64_tr_b16 a[120:121], v233 offset:34304\n" \
"v_mfma_f32_16x16x32_bf16 v[132:135], a[76:79], a[20:23], v[132:135]\n" \
"buffer_store_dwordx4 a[232:235], v234, s[56:59], s93 offen offset:2048 sc1\n" \
"v_mfma_f32_16x16x32_bf16 v[136:139], a[80:83], a[16:19], v[136:139]\n" \
"ds_read_b64_tr_b16 a[122:123], v233 offset:36352\n" \
"v_mfma_f32_16x16x32_bf16 v[136:139], a[84:87], a[20:23], v[136:139]\n" \
"ds_read_b64_tr_b16 a[124:125], v233 offset:42496\n" \
"v_mfma_f32_16x16x32_bf16 v[140:143], a[88:91], a[16:19], v[140:143]\n" \
"ds_read_b64_tr_b16 a[126:127], v233 offset:44544\n" \
"v_mfma_f32_16x16x32_bf16 v[140:143], a[92:95], a[20:23], v[140:143]\n" \
"v_mfma_f32_16x16x32_bf16 v[172:175], a[88:91], a[24:27], v[172:175]\n" \
"v_mfma_f32_16x16x32_bf16 v[172:175], a[92:95], a[28:31], v[172:175]\n" \
"v_mfma_f32_16x16x32_bf16 v[168:171], a[80:83], a[24:27], v[168:171]\n" \
"v_mfma_f32_16x16x32_bf16 v[168:171], a[84:87], a[28:31], v[168:171]\n" \
"buffer_store_dwordx4 a[236:239], v234, s[56:59], s93 offen offset:3072 sc1\n" \
"v_mfma_f32_16x16x32_bf16 v[164:167], a[72:75], a[24:27], v[164:167]\n" \
"v_mfma_f32_16x16x32_bf16 v[164:167], a[76:79], a[28:31], v[164:167]\n" \
"v_mfma_f32_16x16x32_bf16 v[160:163], a[64:67], a[24:27], v[160:163]\n" \
"v_mfma_f32_16x16x32_bf16 v[160:163], a[68:71], a[28:31], v[160:163]\n" \
"s_waitcnt vmcnt(36) lgkmcnt(0)\n" \
"s_barrier\n" \
"v_mfma_f32_16x16x32_bf16 v[80:83], a[96:99], a[0:3], v[80:83]\n" \
"ds_read_b64_tr_b16 a[64:65], v232 offset:49152\n" \
"v_mfma_f32_16x16x32_bf16 v[80:83], a[100:103], a[4:7], v[80:83]\n" \
"ds_read_b64_tr_b16 a[66:67], v232 offset:51200\n" \
"v_mfma_f32_16x16x32_bf16 v[84:87], a[104:107], a[0:3], v[84:87]\n" \
"s_add_u32 m0, s79, 32768\n" \
"ds_read_b64_tr_b16 a[68:69], v232 offset:57344\n" \
"v_mfma_f32_16x16x32_bf16 v[84:87], a[108:111], a[4:7], v[84:87]\n" \
"buffer_load_dwordx4 v228, s[52:55], s78 offen offset:0 lds\n" \
"v_mfma_f32_16x16x32_bf16 v[88:91], a[112:115], a[0:3], v[88:91]\n" \
"ds_read_b64_tr_b16 a[70:71], v232 offset:59392\n" \
"v_mfma_f32_16x16x32_bf16 v[88:91], a[116:119], a[4:7], v[88:91]\n" \
"ds_read_b64_tr_b16 a[72:73], v233 offset:49152\n" \
"v_mfma_f32_16x16x32_bf16 v[92:95], a[120:123], a[0:3], v[92:95]\n" \
"ds_read_b64_tr_b16 a[74:75], v233 offset:51200\n" \
"v_mfma_f32_16x16x32_bf16 v[92:95], a[124:127], a[4:7], v[92:95]\n" \
"buffer_load_dwordx4 v229, s[52:55], s78 offen offset:1024 lds\n" \
"v_mfma_f32_16x16x32_bf16 v[124:127], a[120:123], a[8:11], v[124:127]\n" \
"s_waitcnt vmcnt(36)\n" \
"ds_read_b128 a[0:3], v230 offset:32768\n" \
"v_mfma_f32_16x16x32_bf16 v[124:127], a[124:127], a[12:15], v[124:127]\n" \
"ds_read_b128 a[4:7], v231 offset:32768\n" \
"v_mfma_f32_16x16x32_bf16 v[120:123], a[112:115], a[8:11], v[120:123]\n" \
"s_xor_b32 s78, s78, 0x80000\n" \
"ds_read_b64_tr_b16 a[76:77], v233 offset:57344\n" \
"v_mfma_f32_16x16x32_bf16 v[120:123], a[116:119], a[12:15], v[120:123]\n" \
"buffer_load_dwordx4 v228, s[52:55], s89 offen offset:2048 lds\n" \
"v_mfma_f32_16x16x32_bf16 v[116:119], a[104:107], a[8:11], v[116:119]\n" \
"ds_read_b64_tr_b16 a[78:79], v233 offset:59392\n" \
"v_mfma_f32_16x16x32_bf16 v[116:119], a[108:111], a[12:15], v[116:119]\n" \
"ds_read_b64_tr_b16 a[80:81], v232 offset:49664\n" \
"v_mfma_f32_16x16x32_bf16 v[112:115], a[96:99], a[8:11], v[112:115]\n" \
"ds_read_b64_tr_b16 a[82:83], v232 offset:51712\n" \
"v_mfma_f32_16x16x32_bf16 v[112:115], a[100:103], a[12:15], v[112:115]\n" \
"buffer_load_dwordx4 v229, s[52:55], s89 offen offset:3072 lds\n" \
"v_mfma_f32_16x16x32_bf16 v[144:147], a[96:99], a[16:19], v[144:147]\n" \
"s_waitcnt vmcnt(36)\n" \
"s_xor_b32 s89, s89, 0x80000\n" \
"ds_read_b128 a[8:11], v230 offset:34816\n" \
"v_mfma_f32_16x16x32_bf16 v[144:147], a[100:103], a[20:23], v[144:147]\n" \
"s_add_u32 m0, s74, 98304\n" \
"ds_read_b128 a[12:15], v231 offset:34816\n" \
"v_mfma_f32_16x16x32_bf16 v[148:151], a[104:107], a[16:19], v[148:151]\n" \
"s_waitcnt lgkmcnt(6)\n" \
"ds_read_b64_tr_b16 a[84:85], v232 offset:57856\n" \
"v_mfma_f32_16x16x32_bf16 v[148:151], a[108:111], a[20:23], v[148:151]\n" \
"buffer_load_dwordx4 v224, s[48:51], s75 offen offset:0 lds\n" \
"v_mfma_f32_16x16x32_bf16 v[152:155], a[112:115], a[16:19], v[152:155]\n" \
"ds_read_b64_tr_b16 a[86:87], v232 offset:59904\n" \
"v_mfma_f32_16x16x32_bf16 v[152:155], a[116:119], a[20:23], v[152:155]\n" \
"ds_read_b64_tr_b16 a[88:89], v233 offset:49664\n" \
"v_mfma_f32_16x16x32_bf16 v[156:159], a[120:123], a[16:19], v[156:159]\n" \
"ds_read_b64_tr_b16 a[90:91], v233 offset:51712\n" \
"v_mfma_f32_16x16x32_bf16 v[156:159], a[124:127], a[20:23], v[156:159]\n" \
"buffer_load_dwordx4 v225, s[48:51], s75 offen offset:1024 lds\n" \
"v_mfma_f32_16x16x32_bf16 v[188:191], a[120:123], a[24:27], v[188:191]\n" \
"s_waitcnt vmcnt(36)\n" \
"ds_read_b128 a[16:19], v230 offset:36864\n" \
"v_mfma_f32_16x16x32_bf16 v[188:191], a[124:127], a[28:31], v[188:191]\n" \
"ds_read_b128 a[20:23], v231 offset:36864\n" \
"v_mfma_f32_16x16x32_bf16 v[184:187], a[112:115], a[24:27], v[184:187]\n" \
"s_waitcnt lgkmcnt(6)\n" \
"ds_read_b64_tr_b16 a[92:93], v233 offset:57856\n" \
"v_mfma_f32_16x16x32_bf16 v[184:187], a[116:119], a[28:31], v[184:187]\n" \
"buffer_load_dwordx4 v226, s[48:51], s75 offen offset:2048 lds\n" \
"v_mfma_f32_16x16x32_bf16 v[180:183], a[104:107], a[24:27], v[180:183]\n" \
"ds_read_b64_tr_b16 a[94:95], v233 offset:59904\n" \
"v_mfma_f32_16x16x32_bf16 v[180:183], a[108:111], a[28:31], v[180:183]\n" \
"v_mfma_f32_16x16x32_bf16 v[176:179], a[96:99], a[24:27], v[176:179]\n" \
"v_mfma_f32_16x16x32_bf16 v[176:179], a[100:103], a[28:31], v[176:179]\n" \
"buffer_load_dwordx4 v227, s[48:51], s75 offen offset:3072 lds\n" \
"v_mfma_f32_16x16x32_bf16 v[64:67], a[64:67], a[0:3], v[64:67]\n" \
"s_waitcnt vmcnt(36)\n" \
"s_xor_b32 s75, s75, 0x80\n" \
"ds_read_b128 a[24:27], v230 offset:38912\n" \
"v_mfma_f32_16x16x32_bf16 v[64:67], a[68:71], a[4:7], v[64:67]\n" \
"s_add_u32 m0, s74, 102400\n" \
"ds_read_b128 a[28:31], v231 offset:38912\n" \
"v_mfma_f32_16x16x32_bf16 v[68:71], a[72:75], a[0:3], v[68:71]\n" \
"s_waitcnt lgkmcnt(2)\n" \
"ds_read_b64_tr_b16 a[96:97], v232 offset:50176\n" \
"v_mfma_f32_16x16x32_bf16 v[68:71], a[76:79], a[4:7], v[68:71]\n" \
"buffer_load_dwordx4 v224, s[48:51], s77 offen offset:0 lds\n" \
"v_mfma_f32_16x16x32_bf16 v[72:75], a[80:83], a[0:3], v[72:75]\n" \
"ds_read_b64_tr_b16 a[98:99], v232 offset:52224\n" \
"v_mfma_f32_16x16x32_bf16 v[72:75], a[84:87], a[4:7], v[72:75]\n" \
"ds_read_b64_tr_b16 a[100:101], v232 offset:58368\n" \
"v_mfma_f32_16x16x32_bf16 v[76:79], a[88:91], a[0:3], v[76:79]\n" \
"ds_read_b64_tr_b16 a[102:103], v232 offset:60416\n" \
"v_mfma_f32_16x16x32_bf16 v[76:79], a[92:95], a[4:7], v[76:79]\n" \
"buffer_load_dwordx4 v225, s[48:51], s77 offen offset:1024 lds\n" \
"v_mfma_f32_16x16x32_bf16 v[108:111], a[88:91], a[8:11], v[108:111]\n" \
"ds_read_b64_tr_b16 a[104:105], v233 offset:50176\n" \
"v_mfma_f32_16x16x32_bf16 v[108:111], a[92:95], a[12:15], v[108:111]\n" \
"ds_read_b64_tr_b16 a[106:107], v233 offset:52224\n" \
"v_mfma_f32_16x16x32_bf16 v[104:107], a[80:83], a[8:11], v[104:107]\n" \
"s_waitcnt lgkmcnt(6)\n" \
"ds_read_b64_tr_b16 a[108:109], v233 offset:58368\n" \
"v_mfma_f32_16x16x32_bf16 v[104:107], a[84:87], a[12:15], v[104:107]\n" \
"buffer_load_dwordx4 v226, s[48:51], s77 offen offset:2048 lds\n" \
"v_mfma_f32_16x16x32_bf16 v[100:103], a[72:75], a[8:11], v[100:103]\n" \
"ds_read_b64_tr_b16 a[110:111], v233 offset:60416\n" \
"v_mfma_f32_16x16x32_bf16 v[100:103], a[76:79], a[12:15], v[100:103]\n" \
"ds_read_b64_tr_b16 a[112:113], v232 offset:50688\n" \
"v_mfma_f32_16x16x32_bf16 v[96:99], a[64:67], a[8:11], v[96:99]\n" \
"ds_read_b64_tr_b16 a[114:115], v232 offset:52736\n" \
"v_mfma_f32_16x16x32_bf16 v[96:99], a[68:71], a[12:15], v[96:99]\n" \
"buffer_load_dwordx4 v227, s[48:51], s77 offen offset:3072 lds\n" \
"v_mfma_f32_16x16x32_bf16 v[128:131], a[64:67], a[16:19], v[128:131]\n" \
"ds_read_b64_tr_b16 a[116:117], v232 offset:58880\n" \
"v_mfma_f32_16x16x32_bf16 v[128:131], a[68:71], a[20:23], v[128:131]\n" \
"ds_read_b64_tr_b16 a[118:119], v232 offset:60928\n" \
"v_mfma_f32_16x16x32_bf16 v[132:135], a[72:75], a[16:19], v[132:135]\n" \
"s_xor_b32 s77, s77, 0x80\n" \
"s_add_u32 s93, s87, 17850368\n" \
"ds_read_b64_tr_b16 a[120:121], v233 offset:50688\n" \
"v_mfma_f32_16x16x32_bf16 v[132:135], a[76:79], a[20:23], v[132:135]\n" \
"buffer_store_dwordx4 a[192:195], v234, s[56:59], s93 offen offset:0 sc1\n" \
"v_mfma_f32_16x16x32_bf16 v[136:139], a[80:83], a[16:19], v[136:139]\n" \
"ds_read_b64_tr_b16 a[122:123], v233 offset:52736\n" \
"v_mfma_f32_16x16x32_bf16 v[136:139], a[84:87], a[20:23], v[136:139]\n" \
"ds_read_b64_tr_b16 a[124:125], v233 offset:58880\n" \
"v_mfma_f32_16x16x32_bf16 v[140:143], a[88:91], a[16:19], v[140:143]\n" \
"ds_read_b64_tr_b16 a[126:127], v233 offset:60928\n" \
"v_mfma_f32_16x16x32_bf16 v[140:143], a[92:95], a[20:23], v[140:143]\n" \
"v_mfma_f32_16x16x32_bf16 v[172:175], a[88:91], a[24:27], v[172:175]\n" \
"v_mfma_f32_16x16x32_bf16 v[172:175], a[92:95], a[28:31], v[172:175]\n" \
"v_mfma_f32_16x16x32_bf16 v[168:171], a[80:83], a[24:27], v[168:171]\n" \
"v_mfma_f32_16x16x32_bf16 v[168:171], a[84:87], a[28:31], v[168:171]\n" \
"buffer_store_dwordx4 a[196:199], v234, s[56:59], s93 offen offset:1024 sc1\n" \
"v_mfma_f32_16x16x32_bf16 v[164:167], a[72:75], a[24:27], v[164:167]\n" \
"v_mfma_f32_16x16x32_bf16 v[164:167], a[76:79], a[28:31], v[164:167]\n" \
"v_mfma_f32_16x16x32_bf16 v[160:163], a[64:67], a[24:27], v[160:163]\n" \
"v_mfma_f32_16x16x32_bf16 v[160:163], a[68:71], a[28:31], v[160:163]\n" \
"s_waitcnt vmcnt(37) lgkmcnt(0)\n" \
"s_barrier\n" \
"v_mfma_f32_16x16x32_bf16 v[80:83], a[96:99], a[0:3], v[80:83]\n" \
"ds_read_b64_tr_b16 a[64:65], v232 offset:0\n" \
"v_mfma_f32_16x16x32_bf16 v[80:83], a[100:103], a[4:7], v[80:83]\n" \
"ds_read_b64_tr_b16 a[66:67], v232 offset:2048\n" \
"v_mfma_f32_16x16x32_bf16 v[84:87], a[104:107], a[0:3], v[84:87]\n" \
"s_add_u32 m0, s79, 49152\n" \
"ds_read_b64_tr_b16 a[68:69], v232 offset:8192\n" \
"v_mfma_f32_16x16x32_bf16 v[84:87], a[108:111], a[4:7], v[84:87]\n" \
"buffer_load_dwordx4 v228, s[52:55], s78 offen offset:0 lds\n" \
"v_mfma_f32_16x16x32_bf16 v[88:91], a[112:115], a[0:3], v[88:91]\n" \
"s_mul_i32 s73, s62, 557824\n" \
"s_add_u32 s85, s73, 17850368\n" \
"ds_read_b64_tr_b16 a[70:71], v232 offset:10240\n" \
"v_mfma_f32_16x16x32_bf16 v[88:91], a[116:119], a[4:7], v[88:91]\n" \
"s_mul_i32 s83, s63, 34816\n" \
"s_sub_u32 s86, 557056, s83\n" \
"ds_read_b64_tr_b16 a[72:73], v233 offset:0\n" \
"v_mfma_f32_16x16x32_bf16 v[92:95], a[120:123], a[0:3], v[92:95]\n" \
"ds_read_b64_tr_b16 a[74:75], v233 offset:2048\n" \
"v_add_u32 v237, %3, v236\n" \
"v_mfma_f32_16x16x32_bf16 v[92:95], a[124:127], a[4:7], v[92:95]\n" \
"buffer_load_dwordx4 v229, s[52:55], s78 offen offset:1024 lds\n" \
"v_mfma_f32_16x16x32_bf16 v[124:127], a[120:123], a[8:11], v[124:127]\n" \
"s_waitcnt vmcnt(37)\n" \
"ds_read_b128 a[0:3], v250 offset:0\n" \
"v_mfma_f32_16x16x32_bf16 v[124:127], a[124:127], a[12:15], v[124:127]\n" \
"s_add_u32 s36, s56, s73\n" \
"s_addc_u32 s37, s57, 0\n" \
"ds_read_b128 a[4:7], v251 offset:0\n" \
"v_mfma_f32_16x16x32_bf16 v[120:123], a[112:115], a[8:11], v[120:123]\n" \
"s_mov_b32 s38, s86\n" \
"s_mov_b32 s39, 0x20000\n" \
"ds_read_b64_tr_b16 a[76:77], v233 offset:8192\n" \
"v_mfma_f32_16x16x32_bf16 v[120:123], a[116:119], a[12:15], v[120:123]\n" \
"buffer_load_dwordx4 v228, s[52:55], s89 offen offset:2048 lds\n" \
"v_mfma_f32_16x16x32_bf16 v[116:119], a[104:107], a[8:11], v[116:119]\n" \
"ds_read_b64_tr_b16 a[78:79], v233 offset:10240\n" \
"v_add_u32 v235, %3, v234\n" \
"v_mfma_f32_16x16x32_bf16 v[116:119], a[108:111], a[12:15], v[116:119]\n" \
"s_add_u32 s40, s56, s85\n" \
"s_addc_u32 s41, s57, 0\n" \
"ds_read_b64_tr_b16 a[80:81], v232 offset:512\n" \
"v_mfma_f32_16x16x32_bf16 v[112:115], a[96:99], a[8:11], v[112:115]\n" \
"s_mov_b32 s42, s86\n" \
"s_mov_b32 s43, 0x20000\n" \
"ds_read_b64_tr_b16 a[82:83], v232 offset:2560\n" \
"v_mfma_f32_16x16x32_bf16 v[112:115], a[100:103], a[12:15], v[112:115]\n" \
"buffer_load_dwordx4 v229, s[52:55], s89 offen offset:3072 lds\n" \
"v_mfma_f32_16x16x32_bf16 v[144:147], a[96:99], a[16:19], v[144:147]\n" \
"s_waitcnt vmcnt(37)\n" \
"s_add_u32 s85, s74, 131072\n" \
"ds_read_b128 a[8:11], v250 offset:2048\n" \
"v_mfma_f32_16x16x32_bf16 v[144:147], a[100:103], a[20:23], v[144:147]\n" \
"s_add_u32 m0, s74, 131072\n" \
"ds_read_b128 a[12:15], v251 offset:2048\n" \
"v_add_u32 v239, s85, v234\n" \
"v_mfma_f32_16x16x32_bf16 v[148:151], a[104:107], a[16:19], v[148:151]\n" \
"s_waitcnt lgkmcnt(6)\n" \
"s_lshl_b32 s73, s61, 12\n" \
"ds_read_b64_tr_b16 a[84:85], v232 offset:8704\n" \
"v_mfma_f32_16x16x32_bf16 v[148:151], a[108:111], a[20:23], v[148:151]\n" \
"buffer_load_dwordx4 v224, s[48:51], s75 offen offset:0 lds\n" \
"v_mfma_f32_16x16x32_bf16 v[152:155], a[112:115], a[16:19], v[152:155]\n" \
"s_add_u32 s80, s79, 0x10000\n" \
"s_add_u32 s83, s83, s73\n" \
"ds_read_b64_tr_b16 a[86:87], v232 offset:10752\n" \
"v_mfma_f32_16x16x32_bf16 v[152:155], a[116:119], a[20:23], v[152:155]\n" \
"s_cmp_eq_u32 s63, 0\n" \
"s_cselect_b32 s85, 34816, 0\n" \
"ds_read_b64_tr_b16 a[88:89], v233 offset:512\n" \
"v_mfma_f32_16x16x32_bf16 v[156:159], a[120:123], a[16:19], v[156:159]\n" \
"s_add_u32 s73, s73, s85\n" \
"ds_read_b64_tr_b16 a[90:91], v233 offset:2560\n" \
"v_mfma_f32_16x16x32_bf16 v[156:159], a[124:127], a[20:23], v[156:159]\n" \
"buffer_load_dwordx4 v225, s[48:51], s75 offen offset:1024 lds\n" \
"v_mfma_f32_16x16x32_bf16 v[188:191], a[120:123], a[24:27], v[188:191]\n" \
"s_waitcnt vmcnt(37)\n" \
"ds_read_b128 a[16:19], v250 offset:4096\n" \
"v_mfma_f32_16x16x32_bf16 v[188:191], a[124:127], a[28:31], v[188:191]\n" \
"ds_read_b128 a[20:23], v251 offset:4096\n" \
"v_mfma_f32_16x16x32_bf16 v[184:187], a[112:115], a[24:27], v[184:187]\n" \
"s_waitcnt lgkmcnt(6)\n" \
"ds_read_b64_tr_b16 a[92:93], v233 offset:8704\n" \
"v_mfma_f32_16x16x32_bf16 v[184:187], a[116:119], a[28:31], v[184:187]\n" \
"buffer_load_dwordx4 v226, s[48:51], s75 offen offset:2048 lds\n" \
"v_mfma_f32_16x16x32_bf16 v[180:183], a[104:107], a[24:27], v[180:183]\n" \
"ds_read_b64_tr_b16 a[94:95], v233 offset:10752\n" \
"v_mfma_f32_16x16x32_bf16 v[180:183], a[108:111], a[28:31], v[180:183]\n" \
"v_mfma_f32_16x16x32_bf16 v[176:179], a[96:99], a[24:27], v[176:179]\n" \
"v_mfma_f32_16x16x32_bf16 v[176:179], a[100:103], a[28:31], v[176:179]\n" \
"buffer_load_dwordx4 v227, s[48:51], s75 offen offset:3072 lds\n" \
"v_mfma_f32_16x16x32_bf16 v[64:67], a[64:67], a[0:3], v[64:67]\n" \
"s_waitcnt vmcnt(37)\n" \
"ds_read_b128 a[24:27], v250 offset:6144\n" \
"v_mfma_f32_16x16x32_bf16 v[64:67], a[68:71], a[4:7], v[64:67]\n" \
"s_add_u32 m0, s74, 135168\n" \
"ds_read_b128 a[28:31], v251 offset:6144\n" \
"v_mfma_f32_16x16x32_bf16 v[68:71], a[72:75], a[0:3], v[68:71]\n" \
"s_waitcnt lgkmcnt(2)\n" \
"ds_read_b64_tr_b16 a[96:97], v232 offset:1024\n" \
"v_mfma_f32_16x16x32_bf16 v[68:71], a[76:79], a[4:7], v[68:71]\n" \
"buffer_load_dwordx4 v224, s[48:51], s77 offen offset:0 lds\n" \
"v_mfma_f32_16x16x32_bf16 v[72:75], a[80:83], a[0:3], v[72:75]\n" \
"ds_read_b64_tr_b16 a[98:99], v232 offset:3072\n" \
"v_mfma_f32_16x16x32_bf16 v[72:75], a[84:87], a[4:7], v[72:75]\n" \
"ds_read_b64_tr_b16 a[100:101], v232 offset:9216\n" \
"v_mfma_f32_16x16x32_bf16 v[76:79], a[88:91], a[0:3], v[76:79]\n" \
"ds_read_b64_tr_b16 a[102:103], v232 offset:11264\n" \
"v_mfma_f32_16x16x32_bf16 v[76:79], a[92:95], a[4:7], v[76:79]\n" \
"buffer_load_dwordx4 v225, s[48:51], s77 offen offset:1024 lds\n" \
"v_mfma_f32_16x16x32_bf16 v[108:111], a[88:91], a[8:11], v[108:111]\n" \
"ds_read_b64_tr_b16 a[104:105], v233 offset:1024\n" \
"v_mfma_f32_16x16x32_bf16 v[108:111], a[92:95], a[12:15], v[108:111]\n" \
"ds_read_b64_tr_b16 a[106:107], v233 offset:3072\n" \
"v_mfma_f32_16x16x32_bf16 v[104:107], a[80:83], a[8:11], v[104:107]\n" \
"s_waitcnt lgkmcnt(6)\n" \
"ds_read_b64_tr_b16 a[108:109], v233 offset:9216\n" \
"v_mfma_f32_16x16x32_bf16 v[104:107], a[84:87], a[12:15], v[104:107]\n" \
"buffer_load_dwordx4 v226, s[48:51], s77 offen offset:2048 lds\n" \
"v_mfma_f32_16x16x32_bf16 v[100:103], a[72:75], a[8:11], v[100:103]\n" \
"ds_read_b64_tr_b16 a[110:111], v233 offset:11264\n" \
"v_mfma_f32_16x16x32_bf16 v[100:103], a[76:79], a[12:15], v[100:103]\n" \
"ds_read_b64_tr_b16 a[112:113], v232 offset:1536\n" \
"v_mfma_f32_16x16x32_bf16 v[96:99], a[64:67], a[8:11], v[96:99]\n" \
"ds_read_b64_tr_b16 a[114:115], v232 offset:3584\n" \
"v_mfma_f32_16x16x32_bf16 v[96:99], a[68:71], a[12:15], v[96:99]\n" \
"buffer_load_dwordx4 v227, s[48:51], s77 offen offset:3072 lds\n" \
"v_mfma_f32_16x16x32_bf16 v[128:131], a[64:67], a[16:19], v[128:131]\n" \
"ds_read_b64_tr_b16 a[116:117], v232 offset:9728\n" \
"v_mfma_f32_16x16x32_bf16 v[128:131], a[68:71], a[20:23], v[128:131]\n" \
"ds_read_b64_tr_b16 a[118:119], v232 offset:11776\n" \
"v_mfma_f32_16x16x32_bf16 v[132:135], a[72:75], a[16:19], v[132:135]\n" \
"ds_read_b64_tr_b16 a[120:121], v233 offset:1536\n" \
"v_mfma_f32_16x16x32_bf16 v[132:135], a[76:79], a[20:23], v[132:135]\n" \
"buffer_store_dwordx4 a[200:203], v234, s[56:59], s93 offen offset:2048 sc1\n" \
"v_mfma_f32_16x16x32_bf16 v[136:139], a[80:83], a[16:19], v[136:139]\n" \
"ds_read_b64_tr_b16 a[122:123], v233 offset:3584\n" \
"v_mfma_f32_16x16x32_bf16 v[136:139], a[84:87], a[20:23], v[136:139]\n" \
"ds_read_b64_tr_b16 a[124:125], v233 offset:9728\n" \
"v_mfma_f32_16x16x32_bf16 v[140:143], a[88:91], a[16:19], v[140:143]\n" \
"ds_read_b64_tr_b16 a[126:127], v233 offset:11776\n" \
"v_mfma_f32_16x16x32_bf16 v[140:143], a[92:95], a[20:23], v[140:143]\n" \
"v_mfma_f32_16x16x32_bf16 v[172:175], a[88:91], a[24:27], v[172:175]\n" \
"v_mfma_f32_16x16x32_bf16 v[172:175], a[92:95], a[28:31], v[172:175]\n" \
"v_mfma_f32_16x16x32_bf16 v[168:171], a[80:83], a[24:27], v[168:171]\n" \
"v_mfma_f32_16x16x32_bf16 v[168:171], a[84:87], a[28:31], v[168:171]\n" \
"buffer_store_dwordx4 a[204:207], v234, s[56:59], s93 offen offset:3072 sc1\n" \
"v_mfma_f32_16x16x32_bf16 v[164:167], a[72:75], a[24:27], v[164:167]\n" \
"v_mfma_f32_16x16x32_bf16 v[164:167], a[76:79], a[28:31], v[164:167]\n" \
"v_mfma_f32_16x16x32_bf16 v[160:163], a[64:67], a[24:27], v[160:163]\n" \
"v_mfma_f32_16x16x32_bf16 v[160:163], a[68:71], a[28:31], v[160:163]\n" \
"s_waitcnt vmcnt(38) lgkmcnt(0)\n" \
"s_barrier\n" \
"v_mfma_f32_16x16x32_bf16 v[80:83], a[96:99], a[0:3], v[80:83]\n" \
"s_lshr_b32 s71, s61, 1\n" \
"s_lshl_b32 s71, s71, 14\n" \
"ds_read_b64_tr_b16 a[64:65], v232 offset:16384\n" \
"v_mfma_f32_16x16x32_bf16 v[80:83], a[100:103], a[4:7], v[80:83]\n" \
"s_xor_b32 s71, s71, 0x4000\n" \
"s_and_b32 s70, s61, 1\n" \
"ds_read_b64_tr_b16 a[66:67], v232 offset:18432\n" \
"v_mfma_f32_16x16x32_bf16 v[84:87], a[104:107], a[0:3], v[84:87]\n" \
"s_lshl_b32 s70, s70, 12\n" \
"s_add_u32 s71, s71, s70\n" \
"ds_read_b64_tr_b16 a[68:69], v232 offset:24576\n" \
"v_mfma_f32_16x16x32_bf16 v[84:87], a[108:111], a[4:7], v[84:87]\n" \
"v_add_u32 v224, s71, v237\n" \
"s_and_b32 s70, s63, 1\n" \
"v_mfma_f32_16x16x32_bf16 v[88:91], a[112:115], a[0:3], v[88:91]\n" \
"s_lshl_b32 s70, s70, 15\n" \
"s_xor_b32 s70, s70, 0x18000\n" \
"ds_read_b64_tr_b16 a[70:71], v232 offset:26624\n" \
"v_mfma_f32_16x16x32_bf16 v[88:91], a[116:119], a[4:7], v[88:91]\n" \
"s_add_u32 s71, s71, s70\n" \
"ds_read_b64_tr_b16 a[72:73], v233 offset:16384\n" \
"v_add_u32 v225, s71, v235\n" \
"v_mfma_f32_16x16x32_bf16 v[92:95], a[120:123], a[0:3], v[92:95]\n" \
"s_lshr_b32 s64, s22, 7\n" \
"s_sub_u32 s64, 3, s64\n" \
"ds_read_b64_tr_b16 a[74:75], v233 offset:18432\n" \
"v_mfma_f32_16x16x32_bf16 v[92:95], a[124:127], a[4:7], v[92:95]\n" \
"s_and_b32 s64, s64, 7\n" \
"s_and_b32 s65, %1, 1\n" \
"s_lshl_b32 s65, s65, 3\n" \
"v_mfma_f32_16x16x32_bf16 v[124:127], a[120:123], a[8:11], v[124:127]\n" \
"s_waitcnt vmcnt(36)\n" \
"s_add_u32 s64, s64, s65\n" \
"ds_read_b128 a[0:3], v230 offset:0\n" \
"v_mfma_f32_16x16x32_bf16 v[124:127], a[124:127], a[12:15], v[124:127]\n" \
"s_lshl_b32 s64, s64, 7\n" \
"s_lshr_b32 s65, %1, 3\n" \
"ds_read_b128 a[4:7], v231 offset:0\n" \
"v_mfma_f32_16x16x32_bf16 v[120:123], a[112:115], a[8:11], v[120:123]\n" \
"s_lshl_b32 s65, s65, 2\n" \
"s_add_u32 s65, s65, s61\n" \
"ds_read_b64_tr_b16 a[76:77], v233 offset:24576\n" \
"v_mfma_f32_16x16x32_bf16 v[120:123], a[116:119], a[12:15], v[120:123]\n" \
"s_mul_i32 s65, s65, 24576\n" \
"s_add_u32 s64, s64, s65\n" \
"v_bfe_u32 v254, v234, 7, 2\n" \
"v_mfma_f32_16x16x32_bf16 v[116:119], a[104:107], a[8:11], v[116:119]\n" \
"ds_read_b64_tr_b16 a[78:79], v233 offset:26624\n" \
"v_mul_u32_u24 v254, 3, v254\n" \
"v_mfma_f32_16x16x32_bf16 v[116:119], a[108:111], a[12:15], v[116:119]\n" \
"ds_read_b64_tr_b16 a[80:81], v232 offset:16896\n" \
"v_bfe_u32 v243, v234, 5, 2\n" \
"v_mfma_f32_16x16x32_bf16 v[112:115], a[96:99], a[8:11], v[112:115]\n" \
"s_add_u32 s93, s94, 8192\n" \
"ds_read_b64_tr_b16 a[82:83], v232 offset:18944\n" \
"v_min_u32 v243, 2, v243\n" \
"v_mfma_f32_16x16x32_bf16 v[112:115], a[100:103], a[12:15], v[112:115]\n" \
"buffer_store_dwordx4 v[32:35], v236, s[56:59], s93 offen offset:0 sc1\n" \
"v_mfma_f32_16x16x32_bf16 v[144:147], a[96:99], a[16:19], v[144:147]\n" \
"s_waitcnt vmcnt(35)\n" \
"ds_read_b128 a[8:11], v230 offset:2048\n" \
"v_add_u32 v254, v254, v243\n" \
"v_mfma_f32_16x16x32_bf16 v[144:147], a[100:103], a[20:23], v[144:147]\n" \
"ds_read_b128 a[12:15], v231 offset:2048\n" \
"v_and_b32 v243, 16, v234\n" \
"v_mfma_f32_16x16x32_bf16 v[148:151], a[104:107], a[16:19], v[148:151]\n" \
"ds_read_b64_tr_b16 a[84:85], v232 offset:25088\n" \
"v_lshl_add_u32 v243, v243, 2, s64\n" \
"v_mfma_f32_16x16x32_bf16 v[148:151], a[108:111], a[20:23], v[148:151]\n" \
"v_lshl_add_u32 v254, v254, 11, v243\n" \
"v_mfma_f32_16x16x32_bf16 v[152:155], a[112:115], a[16:19], v[152:155]\n" \
"ds_read_b64_tr_b16 a[86:87], v232 offset:27136\n" \
"v_mfma_f32_16x16x32_bf16 v[152:155], a[116:119], a[20:23], v[152:155]\n" \
"ds_read_b64_tr_b16 a[88:89], v233 offset:16896\n" \
"v_mfma_f32_16x16x32_bf16 v[156:159], a[120:123], a[16:19], v[156:159]\n" \
"ds_read_b64_tr_b16 a[90:91], v233 offset:18944\n" \
"v_mfma_f32_16x16x32_bf16 v[156:159], a[124:127], a[20:23], v[156:159]\n" \
"v_mfma_f32_16x16x32_bf16 v[188:191], a[120:123], a[24:27], v[188:191]\n" \
"s_waitcnt vmcnt(33)\n" \
"ds_read_b128 a[16:19], v230 offset:4096\n" \
"v_mfma_f32_16x16x32_bf16 v[188:191], a[124:127], a[28:31], v[188:191]\n" \
"ds_read_b128 a[20:23], v231 offset:4096\n" \
"v_mfma_f32_16x16x32_bf16 v[184:187], a[112:115], a[24:27], v[184:187]\n" \
"ds_read_b64_tr_b16 a[92:93], v233 offset:25088\n" \
"v_mfma_f32_16x16x32_bf16 v[184:187], a[116:119], a[28:31], v[184:187]\n" \
"v_mfma_f32_16x16x32_bf16 v[180:183], a[104:107], a[24:27], v[180:183]\n" \
"ds_read_b64_tr_b16 a[94:95], v233 offset:27136\n" \
"v_mfma_f32_16x16x32_bf16 v[180:183], a[108:111], a[28:31], v[180:183]\n" \
"v_mfma_f32_16x16x32_bf16 v[176:179], a[96:99], a[24:27], v[176:179]\n" \
"buffer_store_dwordx4 v[48:51], v236, s[56:59], s93 offen offset:256 sc1\n" \
"v_mfma_f32_16x16x32_bf16 v[176:179], a[100:103], a[28:31], v[176:179]\n" \
"Lpg4452_%=:\n" \
"s_cbranch_execz Lpg6193_%=\n" \
"s_waitcnt lgkmcnt(1)\n" \
"v_mfma_f32_16x16x32_bf16 v[64:67], a[64:67], a[0:3], v[64:67]\n" \
"s_waitcnt vmcnt(32)\n" \
"s_lshl_b32 s68, s63, 2\n" \
"ds_read_b128 a[24:27], v230 offset:6144\n" \
"v_mfma_f32_16x16x32_bf16 v[64:67], a[68:71], a[4:7], v[64:67]\n" \
"s_add_u32 s68, s68, s90\n" \
"ds_read_b128 a[28:31], v231 offset:6144\n" \
"v_mfma_f32_16x16x32_bf16 v[68:71], a[72:75], a[0:3], v[68:71]\n" \
"ds_read_b64_tr_b16 a[96:97], v232 offset:17408\n" \
"v_mfma_f32_16x16x32_bf16 v[68:71], a[76:79], a[4:7], v[68:71]\n" \
"v_mov_b32 v228, s60\n" \
"v_lshlrev_b32 v229, 28, s61\n" \
"v_mfma_f32_16x16x32_bf16 v[72:75], a[80:83], a[0:3], v[72:75]\n" \
"ds_read_b64_tr_b16 a[98:99], v232 offset:19456\n" \
"v_mfma_f32_16x16x32_bf16 v[72:75], a[84:87], a[4:7], v[72:75]\n" \
"s_waitcnt lgkmcnt(3)\n" \
"ds_read_b64_tr_b16 a[100:101], v232 offset:25600\n" \
"v_mfma_f32_16x16x32_bf16 v[76:79], a[88:91], a[0:3], v[76:79]\n" \
"ds_read_b64_tr_b16 a[102:103], v232 offset:27648\n" \
"v_mfma_f32_16x16x32_bf16 v[76:79], a[92:95], a[4:7], v[76:79]\n" \
"v_mfma_f32_16x16x32_bf16 v[108:111], a[88:91], a[8:11], v[108:111]\n" \
"ds_read_b64_tr_b16 a[104:105], v233 offset:17408\n" \
"v_mfma_f32_16x16x32_bf16 v[108:111], a[92:95], a[12:15], v[108:111]\n" \
"ds_read_b64_tr_b16 a[106:107], v233 offset:19456\n" \
"v_mfma_f32_16x16x32_bf16 v[104:107], a[80:83], a[8:11], v[104:107]\n" \
"ds_read_b64_tr_b16 a[108:109], v233 offset:25600\n" \
"v_mfma_f32_16x16x32_bf16 v[104:107], a[84:87], a[12:15], v[104:107]\n" \
"v_mfma_f32_16x16x32_bf16 v[100:103], a[72:75], a[8:11], v[100:103]\n" \
"ds_read_b64_tr_b16 a[110:111], v233 offset:27648\n" \
"v_mfma_f32_16x16x32_bf16 v[100:103], a[76:79], a[12:15], v[100:103]\n" \
"ds_read_b64_tr_b16 a[112:113], v232 offset:17920\n" \
"v_mfma_f32_16x16x32_bf16 v[96:99], a[64:67], a[8:11], v[96:99]\n" \
"ds_read_b64_tr_b16 a[114:115], v232 offset:19968\n" \
"v_mfma_f32_16x16x32_bf16 v[96:99], a[68:71], a[12:15], v[96:99]\n" \
"v_mfma_f32_16x16x32_bf16 v[128:131], a[64:67], a[16:19], v[128:131]\n" \
"ds_read_b64_tr_b16 a[116:117], v232 offset:26112\n" \
"v_mfma_f32_16x16x32_bf16 v[128:131], a[68:71], a[20:23], v[128:131]\n" \
"ds_read_b64_tr_b16 a[118:119], v232 offset:28160\n" \
"v_mfma_f32_16x16x32_bf16 v[132:135], a[72:75], a[16:19], v[132:135]\n" \
"ds_read_b64_tr_b16 a[120:121], v233 offset:17920\n" \
"v_mfma_f32_16x16x32_bf16 v[132:135], a[76:79], a[20:23], v[132:135]\n" \
"buffer_store_dwordx4 v[40:43], v236, s[56:59], s93 offen offset:2048 sc1\n" \
"v_mfma_f32_16x16x32_bf16 v[136:139], a[80:83], a[16:19], v[136:139]\n" \
"ds_read_b64_tr_b16 a[122:123], v233 offset:19968\n" \
"v_mfma_f32_16x16x32_bf16 v[136:139], a[84:87], a[20:23], v[136:139]\n" \
"ds_read_b64_tr_b16 a[124:125], v233 offset:26112\n" \
"v_mfma_f32_16x16x32_bf16 v[140:143], a[88:91], a[16:19], v[140:143]\n" \
"ds_read_b64_tr_b16 a[126:127], v233 offset:28160\n" \
"v_mfma_f32_16x16x32_bf16 v[140:143], a[92:95], a[20:23], v[140:143]\n" \
"s_waitcnt lgkmcnt(3)\n" \
"v_mfma_f32_16x16x32_bf16 v[172:175], a[88:91], a[24:27], v[172:175]\n" \
"v_mfma_f32_16x16x32_bf16 v[172:175], a[92:95], a[28:31], v[172:175]\n" \
"v_mfma_f32_16x16x32_bf16 v[168:171], a[80:83], a[24:27], v[168:171]\n" \
"v_mfma_f32_16x16x32_bf16 v[168:171], a[84:87], a[28:31], v[168:171]\n" \
"v_mfma_f32_16x16x32_bf16 v[164:167], a[72:75], a[24:27], v[164:167]\n" \
"v_mfma_f32_16x16x32_bf16 v[164:167], a[76:79], a[28:31], v[164:167]\n" \
"v_mfma_f32_16x16x32_bf16 v[160:163], a[64:67], a[24:27], v[160:163]\n" \
"v_mfma_f32_16x16x32_bf16 v[160:163], a[68:71], a[28:31], v[160:163]\n" \
"s_waitcnt vmcnt(3) lgkmcnt(0)\n" \
"s_barrier\n" \
"v_mfma_f32_16x16x32_bf16 v[80:83], a[96:99], a[0:3], v[80:83]\n" \
"ds_read_b64_tr_b16 a[64:65], v232 offset:32768\n" \
"v_mfma_f32_16x16x32_bf16 v[80:83], a[100:103], a[4:7], v[80:83]\n" \
"ds_read_b64_tr_b16 a[66:67], v232 offset:34816\n" \
"buffer_store_dword v228, v229, s[56:59], s68 offen sc1\n" \
"v_mfma_f32_16x16x32_bf16 v[84:87], a[104:107], a[0:3], v[84:87]\n" \
"ds_read_b64_tr_b16 a[68:69], v232 offset:40960\n" \
"v_lshrrev_b32 v238, 4, v234\n" \
"v_mfma_f32_16x16x32_bf16 v[84:87], a[108:111], a[4:7], v[84:87]\n" \
"buffer_store_dwordx4 v[56:59], v236, s[56:59], s93 offen offset:2304 sc1\n" \
"v_mfma_f32_16x16x32_bf16 v[88:91], a[112:115], a[0:3], v[88:91]\n" \
"ds_read_b64_tr_b16 a[70:71], v232 offset:43008\n" \
"v_lshrrev_b32 v240, 4, v238\n" \
"v_mfma_f32_16x16x32_bf16 v[88:91], a[116:119], a[4:7], v[88:91]\n" \
"ds_read_b64_tr_b16 a[72:73], v233 offset:32768\n" \
"v_lshlrev_b32 v240, 2, v240\n" \
"v_mfma_f32_16x16x32_bf16 v[92:95], a[120:123], a[0:3], v[92:95]\n" \
"ds_read_b64_tr_b16 a[74:75], v233 offset:34816\n" \
"v_and_b32 v238, 15, v238\n" \
"v_mfma_f32_16x16x32_bf16 v[92:95], a[124:127], a[4:7], v[92:95]\n" \
"ds_write_b128 v224, v[192:195] offset:8192\n" \
"v_mfma_f32_16x16x32_bf16 v[124:127], a[120:123], a[8:11], v[124:127]\n" \
"ds_read_b128 a[0:3], v230 offset:32768\n" \
"v_sub_u32 v238, v238, v240\n" \
"v_mfma_f32_16x16x32_bf16 v[124:127], a[124:127], a[12:15], v[124:127]\n" \
"ds_read_b128 a[4:7], v231 offset:32768\n" \
"v_subrev_u32 v248, 0, v238\n" \
"v_mfma_f32_16x16x32_bf16 v[120:123], a[112:115], a[8:11], v[120:123]\n" \
"ds_read_b64_tr_b16 a[76:77], v233 offset:40960\n" \
"v_subrev_u32 v249, 1, v238\n" \
"v_mfma_f32_16x16x32_bf16 v[120:123], a[116:119], a[12:15], v[120:123]\n" \
"ds_write_b128 v224, v[208:211] offset:8448\n" \
"v_mfma_f32_16x16x32_bf16 v[116:119], a[104:107], a[8:11], v[116:119]\n" \
"ds_read_b64_tr_b16 a[78:79], v233 offset:43008\n" \
"v_subrev_u32 v250, 2, v238\n" \
"v_mfma_f32_16x16x32_bf16 v[116:119], a[108:111], a[12:15], v[116:119]\n" \
"ds_read_b64_tr_b16 a[80:81], v232 offset:33280\n" \
"v_subrev_u32 v251, 3, v238\n" \
"v_mfma_f32_16x16x32_bf16 v[112:115], a[96:99], a[8:11], v[112:115]\n" \
"ds_read_b64_tr_b16 a[82:83], v232 offset:35328\n" \
"v_ashrrev_i32 v248, 31, v248\n" \
"v_mfma_f32_16x16x32_bf16 v[112:115], a[100:103], a[12:15], v[112:115]\n" \
"v_ashrrev_i32 v249, 31, v249\n" \
"v_ashrrev_i32 v250, 31, v250\n" \
"v_mfma_f32_16x16x32_bf16 v[144:147], a[96:99], a[16:19], v[144:147]\n" \
"ds_read_b128 a[8:11], v230 offset:34816\n" \
"v_ashrrev_i32 v251, 31, v251\n" \
"v_mfma_f32_16x16x32_bf16 v[144:147], a[100:103], a[20:23], v[144:147]\n" \
"ds_read_b128 a[12:15], v231 offset:34816\n" \
"v_lshlrev_b32 v248, 23, v248\n" \
"v_mfma_f32_16x16x32_bf16 v[148:151], a[104:107], a[16:19], v[148:151]\n" \
"ds_read_b64_tr_b16 a[84:85], v232 offset:41472\n" \
"v_lshlrev_b32 v249, 23, v249\n" \
"v_mfma_f32_16x16x32_bf16 v[148:151], a[108:111], a[20:23], v[148:151]\n" \
"ds_write_b128 v224, v[200:203] offset:10240\n" \
"v_mfma_f32_16x16x32_bf16 v[152:155], a[112:115], a[16:19], v[152:155]\n" \
"ds_read_b64_tr_b16 a[86:87], v232 offset:43520\n" \
"v_lshlrev_b32 v250, 23, v250\n" \
"v_mfma_f32_16x16x32_bf16 v[152:155], a[116:119], a[20:23], v[152:155]\n" \
"ds_read_b64_tr_b16 a[88:89], v233 offset:33280\n" \
"v_lshlrev_b32 v251, 23, v251\n" \
"v_mfma_f32_16x16x32_bf16 v[156:159], a[120:123], a[16:19], v[156:159]\n" \
"s_add_u32 s93, s94, 0\n" \
"ds_read_b64_tr_b16 a[90:91], v233 offset:35328\n" \
"v_mov_b32 v240, 0xff800000\n" \
"v_mfma_f32_16x16x32_bf16 v[156:159], a[124:127], a[20:23], v[156:159]\n" \
"buffer_store_dwordx4 v[36:39], v236, s[56:59], s93 offen offset:0 sc1\n" \
"v_mfma_f32_16x16x32_bf16 v[188:191], a[120:123], a[24:27], v[188:191]\n" \
"ds_read_b128 a[16:19], v230 offset:36864\n" \
"v_add_f32 v248, 0x42400000, v248\n" \
"v_mfma_f32_16x16x32_bf16 v[188:191], a[124:127], a[28:31], v[188:191]\n" \
"ds_read_b128 a[20:23], v231 offset:36864\n" \
"v_add_f32 v249, 0x42400000, v249\n" \
"v_mfma_f32_16x16x32_bf16 v[184:187], a[112:115], a[24:27], v[184:187]\n" \
"ds_read_b64_tr_b16 a[92:93], v233 offset:41472\n" \
"v_add_f32 v250, 0x42400000, v250\n" \
"v_mfma_f32_16x16x32_bf16 v[184:187], a[116:119], a[28:31], v[184:187]\n" \
"ds_write_b128 v224, v[216:219] offset:10496\n" \
"v_mfma_f32_16x16x32_bf16 v[180:183], a[104:107], a[24:27], v[180:183]\n" \
"ds_read_b64_tr_b16 a[94:95], v233 offset:43520\n" \
"v_add_f32 v251, 0x42400000, v251\n" \
"v_mfma_f32_16x16x32_bf16 v[180:183], a[108:111], a[28:31], v[180:183]\n" \
"s_mov_b32 s72, 0x42400000\n" \
"s_cmp_gt_u32 s61, 0\n" \
"s_cselect_b32 s70, s72, 0xff800000\n" \
"v_mfma_f32_16x16x32_bf16 v[176:179], a[96:99], a[24:27], v[176:179]\n" \
"ds_write_b128 v224, v[196:199] offset:0\n" \
"v_mfma_f32_16x16x32_bf16 v[176:179], a[100:103], a[28:31], v[176:179]\n" \
"s_waitcnt lgkmcnt(3)\n" \
"v_mov_b32 v252, s70\n" \
"v_mfma_f32_16x16x32_bf16 v[64:67], a[64:67], a[0:3], v[64:67]\n" \
"ds_read_b128 a[24:27], v230 offset:38912\n" \
"v_mfma_f32_16x16x32_bf16 v[64:67], a[68:71], a[4:7], v[64:67]\n" \
"s_cmp_gt_u32 s61, 1\n" \
"s_cselect_b32 s70, s72, 0xff800000\n" \
"ds_read_b128 a[28:31], v231 offset:38912\n" \
"v_mfma_f32_16x16x32_bf16 v[68:71], a[72:75], a[0:3], v[68:71]\n" \
"ds_read_b64_tr_b16 a[96:97], v232 offset:33792\n" \
"v_mov_b32 v253, s70\n" \
"v_mfma_f32_16x16x32_bf16 v[68:71], a[76:79], a[4:7], v[68:71]\n" \
"s_cmp_gt_u32 s61, 2\n" \
"s_cselect_b32 s70, s72, 0xff800000\n" \
"v_mov_b32 v226, s70\n" \
"v_mfma_f32_16x16x32_bf16 v[72:75], a[80:83], a[0:3], v[72:75]\n" \
"ds_read_b64_tr_b16 a[98:99], v232 offset:35840\n" \
"v_bfe_i32 v243, v234, 4, 1\n" \
"v_mfma_f32_16x16x32_bf16 v[72:75], a[84:87], a[4:7], v[72:75]\n" \
"s_waitcnt lgkmcnt(3)\n" \
"ds_read_b64_tr_b16 a[100:101], v232 offset:41984\n" \
"v_and_b32 v243, 0xaaaa, v243\n" \
"v_mfma_f32_16x16x32_bf16 v[76:79], a[88:91], a[0:3], v[76:79]\n" \
"ds_read_b64_tr_b16 a[102:103], v232 offset:44032\n" \
"v_or_b32 v243, 0x4444, v243\n" \
"v_mfma_f32_16x16x32_bf16 v[76:79], a[92:95], a[4:7], v[76:79]\n" \
"ds_write_b128 v224, v[212:215] offset:256\n" \
"v_mfma_f32_16x16x32_bf16 v[108:111], a[88:91], a[8:11], v[108:111]\n" \
"ds_read_b64_tr_b16 a[104:105], v233 offset:33792\n" \
"v_bfe_i32 v244, v234, 5, 1\n" \
"v_mfma_f32_16x16x32_bf16 v[108:111], a[92:95], a[12:15], v[108:111]\n" \
"ds_read_b64_tr_b16 a[106:107], v233 offset:35840\n" \
"v_not_b32 v244, v244\n" \
"v_mfma_f32_16x16x32_bf16 v[104:107], a[80:83], a[8:11], v[104:107]\n" \
"ds_read_b64_tr_b16 a[108:109], v233 offset:41984\n" \
"v_and_b32 v244, 0x3f803f80, v244\n" \
"v_mfma_f32_16x16x32_bf16 v[104:107], a[84:87], a[12:15], v[104:107]\n" \
"buffer_store_dwordx4 v[16:19], v236, s[56:59], s93 offen offset:256 sc1\n" \
"v_mfma_f32_16x16x32_bf16 v[100:103], a[72:75], a[8:11], v[100:103]\n" \
"ds_read_b64_tr_b16 a[110:111], v233 offset:44032\n" \
"v_mov_b32 v245, v244\n" \
"v_mfma_f32_16x16x32_bf16 v[100:103], a[76:79], a[12:15], v[100:103]\n" \
"ds_read_b64_tr_b16 a[112:113], v232 offset:34304\n" \
"v_mov_b32 v246, v244\n" \
"v_mfma_f32_16x16x32_bf16 v[96:99], a[64:67], a[8:11], v[96:99]\n" \
"ds_read_b64_tr_b16 a[114:115], v232 offset:36352\n" \
"v_mov_b32 v247, v244\n" \
"v_mfma_f32_16x16x32_bf16 v[96:99], a[68:71], a[12:15], v[96:99]\n" \
"ds_write_b128 v224, v[204:207] offset:2048\n" \
"v_mfma_f32_16x16x32_bf16 v[128:131], a[64:67], a[16:19], v[128:131]\n" \
"s_mov_b32 s76, s73\n" \
"s_and_b32 s77, s63, 1\n" \
"ds_read_b64_tr_b16 a[116:117], v232 offset:42496\n" \
"v_mfma_f32_16x16x32_bf16 v[128:131], a[68:71], a[20:23], v[128:131]\n" \
"s_lshl_b32 s77, s77, 15\n" \
"s_xor_b32 s89, s77, 0x8000\n" \
"ds_read_b64_tr_b16 a[118:119], v232 offset:44544\n" \
"v_mfma_f32_16x16x32_bf16 v[132:135], a[72:75], a[16:19], v[132:135]\n" \
"s_add_u32 s82, s89, 0x10000\n" \
"s_mov_b32 s74, 0\n" \
"ds_read_b64_tr_b16 a[120:121], v233 offset:34304\n" \
"v_mfma_f32_16x16x32_bf16 v[132:135], a[76:79], a[20:23], v[132:135]\n" \
"ds_write_b128 v224, v[220:223] offset:2304\n" \
"v_mfma_f32_16x16x32_bf16 v[136:139], a[80:83], a[16:19], v[136:139]\n" \
"s_waitcnt lgkmcnt(4)\n" \
"s_add_u32 s73, s73, 34816\n" \
"ds_read_b64_tr_b16 a[122:123], v233 offset:36352\n" \
"v_mfma_f32_16x16x32_bf16 v[136:139], a[84:87], a[20:23], v[136:139]\n" \
"s_cmp_eq_u32 s73, s83\n" \
"s_cselect_b32 s70, 34816, 0\n" \
"ds_read_b64_tr_b16 a[124:125], v233 offset:42496\n" \
"v_mfma_f32_16x16x32_bf16 v[140:143], a[88:91], a[16:19], v[140:143]\n" \
"s_add_u32 s73, s73, s70\n" \
"s_lshr_b32 s70, s61, 1\n" \
"ds_read_b64_tr_b16 a[126:127], v233 offset:44544\n" \
"v_mfma_f32_16x16x32_bf16 v[140:143], a[92:95], a[20:23], v[140:143]\n" \
"s_lshl_b32 s85, s63, 2\n" \
"s_sub_u32 s86, 60, s85\n" \
"s_add_u32 s85, s85, s70\n" \
"v_mfma_f32_16x16x32_bf16 v[172:175], a[88:91], a[24:27], v[172:175]\n" \
"s_add_u32 s86, s86, s70\n" \
"s_mul_i32 s87, s85, 266240\n" \
"v_add_u32 v230, 0x10000, v230\n" \
"v_mfma_f32_16x16x32_bf16 v[172:175], a[92:95], a[28:31], v[172:175]\n" \
"s_mul_i32 s88, s86, 266240\n" \
"s_and_b32 s75, s61, 1\n" \
"v_add_u32 v231, 0x10000, v231\n" \
"v_mfma_f32_16x16x32_bf16 v[168:171], a[80:83], a[24:27], v[168:171]\n" \
"s_lshl_b32 s70, s75, 8\n" \
"s_lshl_b32 s91, s62, 13\n" \
"s_add_u32 s91, s91, s70\n" \
"v_mfma_f32_16x16x32_bf16 v[168:171], a[84:87], a[28:31], v[168:171]\n" \
"buffer_store_dwordx4 v[52:55], v236, s[56:59], s93 offen offset:2048 sc1\n" \
"v_mfma_f32_16x16x32_bf16 v[164:167], a[72:75], a[24:27], v[164:167]\n" \
"s_add_u32 s91, s91, 35700736\n" \
"s_add_u32 s87, s87, s91\n" \
"s_add_u32 s88, s88, s91\n" \
"v_mfma_f32_16x16x32_bf16 v[164:167], a[76:79], a[28:31], v[164:167]\n" \
"s_lshl_b32 s75, s75, 14\n" \
"s_cmp_eq_u32 s63, 7\n" \
"s_cselect_b32 s75, 0, s75\n" \
"s_xor_b32 s78, s75, 0xc000\n" \
"s_mov_b32 s81, s78\n" \
"s_cmp_eq_u32 s63, 0\n" \
"s_cselect_b32 s91, s75, 0\n" \
"s_add_u32 s91, s91, 0x8000\n" \
"s_xor_b32 s75, s75, 0x8000\n" \
"s_sub_u32 s84, 7, s63\n" \
"v_mfma_f32_16x16x32_bf16 v[160:163], a[64:67], a[24:27], v[160:163]\n" \
"v_mfma_f32_16x16x32_bf16 v[160:163], a[68:71], a[28:31], v[160:163]\n" \
"s_waitcnt vmcnt(18) lgkmcnt(0)\n" \
"s_barrier\n" \
"v_mfma_f32_16x16x32_bf16 v[144:147], a[96:99], a[16:19], v[144:147]\n" \
"s_mov_b64 s[44:45], s[10:11]\n" \
"s_mov_b32 s46, 0\n" \
"ds_read_b64_tr_b16 a[64:65], v232 offset:49152\n" \
"v_mfma_f32_16x16x32_bf16 v[144:147], a[100:103], a[20:23], v[144:147]\n" \
"s_mov_b32 s47, 0\n" \
"s_cmp_eq_u32 s63, 7\n" \
"s_cselect_b64 s[44:45], s[18:19], s[44:45]\n" \
"s_cselect_b32 s46, 5, s46\n" \
"s_cmp_eq_u32 s63, 3\n" \
"s_cselect_b64 s[44:45], s[18:19], s[44:45]\n" \
"s_cselect_b32 s46, 3, s46\n" \
"ds_read_b64_tr_b16 a[66:67], v232 offset:51200\n" \
"v_mfma_f32_16x16x32_bf16 v[148:151], a[104:107], a[16:19], v[148:151]\n" \
"s_cmp_eq_u32 s63, 3\n" \
"s_cselect_b32 s47, 640, s47\n" \
"ds_read_b64_tr_b16 a[68:69], v232 offset:57344\n" \
"v_mfma_f32_16x16x32_bf16 v[148:151], a[108:111], a[20:23], v[148:151]\n" \
"ds_write_b128 v225, a[192:195] offset:0\n" \
"v_mfma_f32_16x16x32_bf16 v[152:155], a[112:115], a[16:19], v[152:155]\n" \
"ds_read_b64_tr_b16 a[70:71], v232 offset:59392\n" \
"v_mfma_f32_16x16x32_bf16 v[152:155], a[116:119], a[20:23], v[152:155]\n" \
"s_cmp_eq_u32 s63, 6\n" \
"s_cselect_b64 s[44:45], s[22:23], s[44:45]\n" \
"s_cselect_b32 s46, 6, s46\n" \
"ds_read_b64_tr_b16 a[72:73], v233 offset:49152\n" \
"v_mfma_f32_16x16x32_bf16 v[156:159], a[120:123], a[16:19], v[156:159]\n" \
"ds_read_b64_tr_b16 a[74:75], v233 offset:51200\n" \
"v_mfma_f32_16x16x32_bf16 v[156:159], a[124:127], a[20:23], v[156:159]\n" \
"s_cmp_eq_u32 s63, 4\n" \
"s_cselect_b64 s[44:45], s[20:21], s[44:45]\n" \
"s_cselect_b32 s46, 4, s46\n" \
"v_mfma_f32_16x16x32_bf16 v[188:191], a[120:123], a[24:27], v[188:191]\n" \
"ds_read_b128 a[16:19], v230 offset:0\n" \
"v_mfma_f32_16x16x32_bf16 v[188:191], a[124:127], a[28:31], v[188:191]\n" \
"s_cmp_eq_u32 s63, 2\n" \
"s_cselect_b64 s[44:45], s[24:25], s[44:45]\n" \
"s_cselect_b32 s46, 2, s46\n" \
"ds_read_b128 a[20:23], v231 offset:0\n" \
"v_mfma_f32_16x16x32_bf16 v[184:187], a[112:115], a[24:27], v[184:187]\n" \
"s_lshl_b32 s48, s62, 2\n" \
"s_add_u32 s48, s48, s61\n" \
"ds_read_b64_tr_b16 a[76:77], v233 offset:57344\n" \
"v_mfma_f32_16x16x32_bf16 v[184:187], a[116:119], a[28:31], v[184:187]\n" \
"ds_write_b128 v225, a[196:199] offset:1024\n" \
"v_mfma_f32_16x16x32_bf16 v[180:183], a[104:107], a[24:27], v[180:183]\n" \
"s_add_u32 s48, s48, s47\n" \
"s_lshl_b32 s48, s48, 13\n" \
"ds_read_b64_tr_b16 a[78:79], v233 offset:59392\n" \
"v_mfma_f32_16x16x32_bf16 v[180:183], a[108:111], a[28:31], v[180:183]\n" \
"s_add_u32 s44, s44, s48\n" \
"s_addc_u32 s45, s45, 0\n" \
"ds_read_b64_tr_b16 a[80:81], v232 offset:49664\n" \
"v_mfma_f32_16x16x32_bf16 v[176:179], a[96:99], a[24:27], v[176:179]\n" \
"s_cmp_eq_u32 s46, 0\n" \
"s_cselect_b32 s48, 28, 3\n" \
"ds_read_b64_tr_b16 a[82:83], v232 offset:51712\n" \
"v_mfma_f32_16x16x32_bf16 v[176:179], a[100:103], a[28:31], v[176:179]\n" \
"buffer_store_dwordx4 v[24:27], v236, s[56:59], s93 offen offset:2304 sc1\n" \
"v_mfma_f32_16x16x32_bf16 v[80:83], a[96:99], a[0:3], v[80:83]\n" \
"ds_read_b128 a[24:27], v230 offset:2048\n" \
"v_mfma_f32_16x16x32_bf16 v[80:83], a[100:103], a[4:7], v[80:83]\n" \
"ds_read_b128 a[28:31], v231 offset:2048\n" \
"v_mfma_f32_16x16x32_bf16 v[84:87], a[104:107], a[0:3], v[84:87]\n" \
"ds_read_b64_tr_b16 a[84:85], v232 offset:57856\n" \
"v_mfma_f32_16x16x32_bf16 v[84:87], a[108:111], a[4:7], v[84:87]\n" \
"ds_write_b128 v225, a[200:203] offset:2048\n" \
"v_mfma_f32_16x16x32_bf16 v[88:91], a[112:115], a[0:3], v[88:91]\n" \
"ds_read_b64_tr_b16 a[86:87], v232 offset:59904\n" \
"v_mfma_f32_16x16x32_bf16 v[88:91], a[116:119], a[4:7], v[88:91]\n" \
"ds_read_b64_tr_b16 a[88:89], v233 offset:49664\n" \
"v_mfma_f32_16x16x32_bf16 v[92:95], a[120:123], a[0:3], v[92:95]\n" \
"ds_read_b64_tr_b16 a[90:91], v233 offset:51712\n" \
"v_mfma_f32_16x16x32_bf16 v[92:95], a[124:127], a[4:7], v[92:95]\n" \
"ds_write_b128 v225, a[204:207] offset:3072\n" \
"v_mfma_f32_16x16x32_bf16 v[124:127], a[120:123], a[8:11], v[124:127]\n" \
"ds_read_b128 a[0:3], v230 offset:4096\n" \
"v_mfma_f32_16x16x32_bf16 v[124:127], a[124:127], a[12:15], v[124:127]\n" \
"ds_read_b128 a[4:7], v231 offset:4096\n" \
"v_mfma_f32_16x16x32_bf16 v[120:123], a[112:115], a[8:11], v[120:123]\n" \
"ds_read_b64_tr_b16 a[92:93], v233 offset:57856\n" \
"v_mfma_f32_16x16x32_bf16 v[120:123], a[116:119], a[12:15], v[120:123]\n" \
"v_mfma_f32_16x16x32_bf16 v[116:119], a[104:107], a[8:11], v[116:119]\n" \
"ds_read_b64_tr_b16 a[94:95], v233 offset:59904\n" \
"v_mfma_f32_16x16x32_bf16 v[116:119], a[108:111], a[12:15], v[116:119]\n" \
"v_mfma_f32_16x16x32_bf16 v[112:115], a[96:99], a[8:11], v[112:115]\n" \
"ds_write_b128 v225, a[224:227] offset:8192\n" \
"v_mfma_f32_16x16x32_bf16 v[112:115], a[100:103], a[12:15], v[112:115]\n" \
"s_waitcnt lgkmcnt(2)\n" \
"v_mfma_f32_16x16x32_bf16 v[64:67], a[64:67], a[16:19], v[64:67]\n" \
"ds_read_b128 a[8:11], v230 offset:6144\n" \
"ds_read_b128 a[12:15], v231 offset:6144\n" \
"v_mfma_f32_16x16x32_bf16 v[64:67], a[68:71], a[20:23], v[64:67]\n" \
"ds_read_b64_tr_b16 a[96:97], v232 offset:50176\n" \
"ds_read_b64_tr_b16 a[98:99], v232 offset:52224\n" \
"v_mfma_f32_16x16x32_bf16 v[68:71], a[72:75], a[16:19], v[68:71]\n" \
"s_waitcnt lgkmcnt(5)\n" \
"ds_read_b64_tr_b16 a[100:101], v232 offset:58368\n" \
"ds_read_b64_tr_b16 a[102:103], v232 offset:60416\n" \
"v_mfma_f32_16x16x32_bf16 v[68:71], a[76:79], a[20:23], v[68:71]\n" \
"s_getpc_b64 s[64:65]\n" \
"Lpd1_%=:\n" \
"s_lshl_b32 s66, s62, 2\n" \
"ds_read_b64_tr_b16 a[104:105], v233 offset:50176\n" \
"ds_read_b64_tr_b16 a[106:107], v233 offset:52224\n" \
"v_mfma_f32_16x16x32_bf16 v[72:75], a[80:83], a[16:19], v[72:75]\n" \
"s_add_u32 s66, s66, s61\n" \
"ds_read_b64_tr_b16 a[108:109], v233 offset:58368\n" \
"ds_read_b64_tr_b16 a[110:111], v233 offset:60416\n" \
"v_mfma_f32_16x16x32_bf16 v[72:75], a[84:87], a[20:23], v[72:75]\n" \
"s_and_b32 s66, s66, 15\n" \
"ds_read_b64_tr_b16 a[112:113], v232 offset:50688\n" \
"ds_read_b64_tr_b16 a[114:115], v232 offset:52736\n" \
"v_mfma_f32_16x16x32_bf16 v[76:79], a[88:91], a[16:19], v[76:79]\n" \
"s_lshl_b32 s66, s66, 12\n" \
"ds_read_b64_tr_b16 a[116:117], v232 offset:58880\n" \
"ds_read_b64_tr_b16 a[118:119], v232 offset:60928\n" \
"v_mfma_f32_16x16x32_bf16 v[76:79], a[92:95], a[20:23], v[76:79]\n" \
"s_add_u32 s64, s64, Lloop2_%=-Lpd1_%=\n" \
"s_addc_u32 s65, s65, 0\n" \
"s_mov_b32 s67, Lcode_end_%=-Lloop2_%=-4\n" \
"ds_read_b64_tr_b16 a[120:121], v233 offset:50688\n" \
"ds_read_b64_tr_b16 a[122:123], v233 offset:52736\n" \
"v_mfma_f32_16x16x32_bf16 v[96:99], a[64:67], a[24:27], v[96:99]\n" \
"s_add_u32 s93, s94, 17858560\n" \
"ds_read_b64_tr_b16 a[124:125], v233 offset:58880\n" \
"ds_read_b64_tr_b16 a[126:127], v233 offset:60928\n" \
"v_mfma_f32_16x16x32_bf16 v[96:99], a[68:71], a[28:31], v[96:99]\n" \
"buffer_store_dwordx4 v[44:47], v234, s[56:59], s93 offen offset:0 sc1\n" \
"v_mfma_f32_16x16x32_bf16 v[100:103], a[72:75], a[24:27], v[100:103]\n" \
"ds_write_b128 v225, a[228:231] offset:9216\n" \
"v_mfma_f32_16x16x32_bf16 v[100:103], a[76:79], a[28:31], v[100:103]\n" \
"v_lshl_add_u32 v230, v234, 2, s66\n" \
"v_min_u32 v230, s67, v230\n" \
"v_mfma_f32_16x16x32_bf16 v[104:107], a[80:83], a[24:27], v[104:107]\n" \
"buffer_load_dword v227, v242, s[56:59], s90 offen sc1\n" \
"v_mfma_f32_16x16x32_bf16 v[104:107], a[84:87], a[28:31], v[104:107]\n" \
"ds_write_b128 v225, a[232:235] offset:10240\n" \
"v_mfma_f32_16x16x32_bf16 v[108:111], a[88:91], a[24:27], v[108:111]\n" \
"global_load_dword v230, v230, s[64:65]\n" \
"v_mfma_f32_16x16x32_bf16 v[108:111], a[92:95], a[28:31], v[108:111]\n" \
"s_waitcnt lgkmcnt(1)\n" \
"v_cvt_pk_bf16_f32 v64, v64, v65\n" \
"v_cvt_pk_bf16_f32 v65, v66, v67\n" \
"v_mfma_f32_16x16x32_bf16 v[80:83], a[96:99], a[16:19], v[80:83]\n" \
"v_cvt_pk_bf16_f32 v66, v68, v69\n" \
"v_cvt_pk_bf16_f32 v67, v70, v71\n" \
"v_mfma_f32_16x16x32_bf16 v[80:83], a[100:103], a[20:23], v[80:83]\n" \
"v_accvgpr_write_b32 a128, v64\n" \
"v_accvgpr_write_b32 a129, v65\n" \
"v_accvgpr_write_b32 a130, v66\n" \
"v_mfma_f32_16x16x32_bf16 v[84:87], a[104:107], a[16:19], v[84:87]\n" \
"v_accvgpr_write_b32 a131, v67\n" \
"v_cvt_pk_bf16_f32 v72, v72, v73\n" \
"v_mfma_f32_16x16x32_bf16 v[84:87], a[108:111], a[20:23], v[84:87]\n" \
"buffer_store_dwordx4 v[60:63], v234, s[56:59], s93 offen offset:1024 sc1\n" \
"v_mfma_f32_16x16x32_bf16 v[88:91], a[112:115], a[16:19], v[88:91]\n" \
"ds_write_b128 v225, a[236:239] offset:11264\n" \
"v_mfma_f32_16x16x32_bf16 v[88:91], a[116:119], a[20:23], v[88:91]\n" \
"v_cvt_pk_bf16_f32 v73, v74, v75\n" \
"v_cvt_pk_bf16_f32 v74, v76, v77\n" \
"v_cvt_pk_bf16_f32 v75, v78, v79\n" \
"v_mfma_f32_16x16x32_bf16 v[92:95], a[120:123], a[16:19], v[92:95]\n" \
"v_accvgpr_write_b32 a132, v72\n" \
"v_accvgpr_write_b32 a133, v73\n" \
"v_mfma_f32_16x16x32_bf16 v[92:95], a[124:127], a[20:23], v[92:95]\n" \
"v_accvgpr_write_b32 a134, v74\n" \
"v_accvgpr_write_b32 a135, v75\n" \
"v_mfma_f32_16x16x32_bf16 v[112:115], a[96:99], a[24:27], v[112:115]\n" \
"v_cvt_pk_bf16_f32 v96, v96, v97\n" \
"v_cvt_pk_bf16_f32 v97, v98, v99\n" \
"v_mfma_f32_16x16x32_bf16 v[112:115], a[100:103], a[28:31], v[112:115]\n" \
"v_cvt_pk_bf16_f32 v98, v100, v101\n" \
"v_cvt_pk_bf16_f32 v99, v102, v103\n" \
"v_mfma_f32_16x16x32_bf16 v[116:119], a[104:107], a[24:27], v[116:119]\n" \
"v_cvt_pk_bf16_f32 v104, v104, v105\n" \
"v_cvt_pk_bf16_f32 v105, v106, v107\n" \
"v_mfma_f32_16x16x32_bf16 v[116:119], a[108:111], a[28:31], v[116:119]\n" \
"v_cvt_pk_bf16_f32 v106, v108, v109\n" \
"v_cvt_pk_bf16_f32 v107, v110, v111\n" \
"v_mfma_f32_16x16x32_bf16 v[120:123], a[112:115], a[24:27], v[120:123]\n" \
"v_mfma_f32_16x16x32_bf16 v[120:123], a[116:119], a[28:31], v[120:123]\n" \
"v_cvt_pk_bf16_f32 v80, v80, v81\n" \
"v_cvt_pk_bf16_f32 v81, v82, v83\n" \
"v_cvt_pk_bf16_f32 v82, v84, v85\n" \
"v_mfma_f32_16x16x32_bf16 v[124:127], a[120:123], a[24:27], v[124:127]\n" \
"v_cvt_pk_bf16_f32 v83, v86, v87\n" \
"v_accvgpr_write_b32 a136, v80\n" \
"v_mfma_f32_16x16x32_bf16 v[124:127], a[124:127], a[28:31], v[124:127]\n" \
"v_accvgpr_write_b32 a137, v81\n" \
"s_waitcnt lgkmcnt(0)\n" \
"s_barrier\n" \
"v_mfma_f32_16x16x32_bf16 v[128:131], a[64:67], a[0:3], v[128:131]\n" \
"v_accvgpr_write_b32 a138, v82\n" \
"v_accvgpr_write_b32 a139, v83\n" \
"v_mfma_f32_16x16x32_bf16 v[128:131], a[68:71], a[4:7], v[128:131]\n" \
"ds_read_b128 a[192:195], v237 offset:0\n" \
"v_cvt_pk_bf16_f32 v88, v88, v89\n" \
"v_mfma_f32_16x16x32_bf16 v[132:135], a[72:75], a[0:3], v[132:135]\n" \
"v_cvt_pk_bf16_f32 v89, v90, v91\n" \
"v_cvt_pk_bf16_f32 v90, v92, v93\n" \
"v_mfma_f32_16x16x32_bf16 v[132:135], a[76:79], a[4:7], v[132:135]\n" \
"ds_read_b128 a[196:199], v237 offset:2048\n" \
"v_cvt_pk_bf16_f32 v91, v94, v95\n" \
"v_mfma_f32_16x16x32_bf16 v[136:139], a[80:83], a[0:3], v[136:139]\n" \
"v_med3_f32 v208, v248, v252, s72\n" \
"v_med3_f32 v209, v249, v252, s72\n" \
"v_mfma_f32_16x16x32_bf16 v[136:139], a[84:87], a[4:7], v[136:139]\n" \
"ds_read_b128 a[200:203], v237 offset:4096\n" \
"v_med3_f32 v210, v250, v252, s72\n" \
"v_med3_f32 v211, v251, v252, s72\n" \
"v_mfma_f32_16x16x32_bf16 v[140:143], a[88:91], a[0:3], v[140:143]\n" \
"buffer_store_dwordx4 v[20:23], v234, s[56:59], s93 offen offset:2048 sc1\n" \
"v_mfma_f32_16x16x32_bf16 v[140:143], a[92:95], a[4:7], v[140:143]\n" \
"ds_read_b128 a[204:207], v237 offset:6144\n" \
"v_cvt_pk_bf16_f32 v112, v112, v113\n" \
"v_mfma_f32_16x16x32_bf16 v[144:147], a[96:99], a[0:3], v[144:147]\n" \
"v_cvt_pk_bf16_f32 v113, v114, v115\n" \
"v_cvt_pk_bf16_f32 v114, v116, v117\n" \
"v_mfma_f32_16x16x32_bf16 v[144:147], a[100:103], a[4:7], v[144:147]\n" \
"ds_read_b128 a[208:211], v237 offset:256\n" \
"v_cvt_pk_bf16_f32 v115, v118, v119\n" \
"v_cvt_pk_bf16_f32 v120, v120, v121\n" \
"v_mfma_f32_16x16x32_bf16 v[148:151], a[104:107], a[0:3], v[148:151]\n" \
"v_cvt_pk_bf16_f32 v121, v122, v123\n" \
"v_cvt_pk_bf16_f32 v122, v124, v125\n" \
"v_mfma_f32_16x16x32_bf16 v[148:151], a[108:111], a[4:7], v[148:151]\n" \
"ds_read_b128 a[212:215], v237 offset:2304\n" \
"v_cvt_pk_bf16_f32 v123, v126, v127\n" \
"v_mfma_f32_16x16x32_bf16 v[152:155], a[112:115], a[0:3], v[152:155]\n" \
"v_mfma_f32_16x16x32_bf16 v[152:155], a[116:119], a[4:7], v[152:155]\n" \
"ds_read_b128 a[216:219], v237 offset:4352\n" \
"v_cvt_pk_bf16_f32 v128, v128, v129\n" \
"v_mfma_f32_16x16x32_bf16 v[156:159], a[120:123], a[0:3], v[156:159]\n" \
"v_cvt_pk_bf16_f32 v129, v130, v131\n" \
"v_cvt_pk_bf16_f32 v130, v132, v133\n" \
"v_mfma_f32_16x16x32_bf16 v[156:159], a[124:127], a[4:7], v[156:159]\n" \
"ds_read_b128 a[220:223], v237 offset:6400\n" \
"v_cvt_pk_bf16_f32 v131, v134, v135\n" \
"v_cvt_pk_bf16_f32 v136, v136, v137\n" \
"v_mfma_f32_16x16x32_bf16 v[160:163], a[64:67], a[8:11], v[160:163]\n" \
"v_cvt_pk_bf16_f32 v137, v138, v139\n" \
"v_cvt_pk_bf16_f32 v138, v140, v141\n" \
"v_mfma_f32_16x16x32_bf16 v[160:163], a[68:71], a[12:15], v[160:163]\n" \
"ds_read_b128 a[224:227], v237 offset:8192\n" \
"v_cvt_pk_bf16_f32 v139, v142, v143\n" \
"v_mfma_f32_16x16x32_bf16 v[164:167], a[72:75], a[8:11], v[164:167]\n" \
"v_mfma_f32_16x16x32_bf16 v[164:167], a[76:79], a[12:15], v[164:167]\n" \
"ds_read_b128 a[228:231], v237 offset:10240\n" \
"v_mfma_f32_16x16x32_bf16 v[168:171], a[80:83], a[8:11], v[168:171]\n" \
"v_mfma_f32_16x16x32_bf16 v[168:171], a[84:87], a[12:15], v[168:171]\n" \
"ds_read_b128 a[232:235], v237 offset:12288\n" \
"v_cvt_pk_bf16_f32 v144, v144, v145\n" \
"v_cvt_pk_bf16_f32 v145, v146, v147\n" \
"v_mfma_f32_16x16x32_bf16 v[172:175], a[88:91], a[8:11], v[172:175]\n" \
"v_cvt_pk_bf16_f32 v146, v148, v149\n" \
"v_cvt_pk_bf16_f32 v147, v150, v151\n" \
"v_mfma_f32_16x16x32_bf16 v[172:175], a[92:95], a[12:15], v[172:175]\n" \
"ds_read_b128 a[236:239], v237 offset:14336\n" \
"v_cvt_pk_bf16_f32 v152, v152, v153\n" \
"v_mfma_f32_16x16x32_bf16 v[176:179], a[96:99], a[8:11], v[176:179]\n" \
"v_cvt_pk_bf16_f32 v153, v154, v155\n" \
"v_cvt_pk_bf16_f32 v154, v156, v157\n" \
"v_mfma_f32_16x16x32_bf16 v[176:179], a[100:103], a[12:15], v[176:179]\n" \
"ds_read_b128 a[240:243], v237 offset:8448\n" \
"v_cvt_pk_bf16_f32 v155, v158, v159\n" \
"v_mfma_f32_16x16x32_bf16 v[180:183], a[104:107], a[8:11], v[180:183]\n" \
"buffer_store_dwordx4 v[28:31], v234, s[56:59], s93 offen offset:3072 sc1\n" \
"v_mfma_f32_16x16x32_bf16 v[180:183], a[108:111], a[12:15], v[180:183]\n" \
"ds_read_b128 a[244:247], v237 offset:10496\n" \
"ds_write_b128 v239, v[128:131] offset:0\n" \
"v_mov_b32 v196, 0x42400000\n" \
"v_mfma_f32_16x16x32_bf16 v[184:187], a[112:115], a[8:11], v[184:187]\n" \
"v_mov_b32 v197, 0x42400000\n" \
"v_mov_b32 v198, 0x42400000\n" \
"v_mfma_f32_16x16x32_bf16 v[184:187], a[116:119], a[12:15], v[184:187]\n" \
"ds_read_b128 a[248:251], v237 offset:12544\n" \
"v_mov_b32 v199, 0x42400000\n" \
"v_cvt_pk_bf16_f32 v160, v160, v161\n" \
"v_mfma_f32_16x16x32_bf16 v[188:191], a[120:123], a[8:11], v[188:191]\n" \
"v_cvt_pk_bf16_f32 v161, v162, v163\n" \
"v_cvt_pk_bf16_f32 v162, v164, v165\n" \
"v_mfma_f32_16x16x32_bf16 v[188:191], a[124:127], a[12:15], v[188:191]\n" \
"s_waitcnt lgkmcnt(3)\n" \
"ds_read_b128 a[252:255], v237 offset:14592\n" \
"v_cvt_pk_bf16_f32 v163, v166, v167\n" \
"v_mfma_f32_16x16x32_bf16 v[16:19], a[192:195], v[96:99], v[208:211]\n" \
"v_cvt_pk_bf16_f32 v168, v168, v169\n" \
"v_cvt_pk_bf16_f32 v169, v170, v171\n" \
"v_mfma_f32_16x16x32_bf16 v[16:19], a[196:199], v[104:107], v[16:19]\n" \
"v_cvt_pk_bf16_f32 v170, v172, v173\n" \
"v_cvt_pk_bf16_f32 v171, v174, v175\n" \
"v_mfma_f32_16x16x32_bf16 v[16:19], a[200:203], v[112:115], v[16:19]\n" \
"v_cvt_pk_bf16_f32 v176, v176, v177\n" \
"v_cvt_pk_bf16_f32 v177, v178, v179\n" \
"v_mfma_f32_16x16x32_bf16 v[16:19], a[204:207], v[120:123], v[16:19]\n" \
"v_cvt_pk_bf16_f32 v178, v180, v181\n" \
"v_cvt_pk_bf16_f32 v179, v182, v183\n" \
"v_mfma_f32_16x16x32_bf16 v[32:35], a[204:207], v[152:155], v[196:199]\n" \
"v_cvt_pk_bf16_f32 v184, v184, v185\n" \
"v_cvt_pk_bf16_f32 v185, v186, v187\n" \
"v_mfma_f32_16x16x32_bf16 v[32:35], a[200:203], v[144:147], v[32:35]\n" \
"v_cvt_pk_bf16_f32 v186, v188, v189\n" \
"v_cvt_pk_bf16_f32 v187, v190, v191\n" \
"Lpg5018_%=:\n" \
"s_cbranch_execz Lpg6785_%=\n" \
"v_mfma_f32_16x16x32_bf16 v[32:35], a[196:199], v[136:139], v[32:35]\n" \
"s_add_u32 s93, s94, 17850368\n" \
"v_med3_f32 v212, v248, v253, v252\n" \
"v_med3_f32 v213, v249, v253, v252\n" \
"v_mfma_f32_16x16x32_bf16 v[32:35], a[192:195], v[128:131], v[32:35]\n" \
"buffer_store_dwordx4 v[0:3], v234, s[56:59], s93 offen offset:0 sc1\n" \
"v_mfma_f32_16x16x32_bf16 v[48:51], a[192:195], v[160:163], v[196:199]\n" \
"v_med3_f32 v214, v250, v253, v252\n" \
"v_med3_f32 v215, v251, v253, v252\n" \
"v_mfma_f32_16x16x32_bf16 v[48:51], a[196:199], v[168:171], v[48:51]\n" \
"ds_read_b128 a[192:195], v237 offset:16384\n" \
"v_med3_f32 v216, v248, v226, v253\n" \
"v_mfma_f32_16x16x32_bf16 v[48:51], a[200:203], v[176:179], v[48:51]\n" \
"ds_read_b128 a[196:199], v237 offset:18432\n" \
"v_med3_f32 v217, v249, v226, v253\n" \
"v_mfma_f32_16x16x32_bf16 v[48:51], a[204:207], v[184:187], v[48:51]\n" \
"ds_read_b128 a[200:203], v237 offset:20480\n" \
"ds_write_b128 v239, v[136:139] offset:1024\n" \
"v_mfma_f32_16x16x32_bf16 v[52:55], a[220:223], v[184:187], v[196:199]\n" \
"ds_read_b128 a[204:207], v237 offset:22528\n" \
"v_med3_f32 v218, v250, v226, v253\n" \
"v_mfma_f32_16x16x32_bf16 v[52:55], a[216:219], v[176:179], v[52:55]\n" \
"v_med3_f32 v219, v251, v226, v253\n" \
"v_med3_f32 v220, v248, v240, v226\n" \
"v_mfma_f32_16x16x32_bf16 v[52:55], a[212:215], v[168:171], v[52:55]\n" \
"ds_write_b128 v239, v[144:147] offset:2048\n" \
"v_mfma_f32_16x16x32_bf16 v[52:55], a[208:211], v[160:163], v[52:55]\n" \
"buffer_store_dwordx4 v[4:7], v234, s[56:59], s93 offen offset:1024 sc1\n" \
"v_mfma_f32_16x16x32_bf16 v[36:39], a[208:211], v[128:131], v[196:199]\n" \
"v_med3_f32 v221, v249, v240, v226\n" \
"v_med3_f32 v222, v250, v240, v226\n" \
"v_mfma_f32_16x16x32_bf16 v[36:39], a[212:215], v[136:139], v[36:39]\n" \
"v_med3_f32 v223, v251, v240, v226\n" \
"v_accvgpr_write_b32 a144, v96\n" \
"v_mfma_f32_16x16x32_bf16 v[36:39], a[216:219], v[144:147], v[36:39]\n" \
"v_accvgpr_write_b32 a145, v97\n" \
"v_accvgpr_write_b32 a146, v98\n" \
"v_mfma_f32_16x16x32_bf16 v[36:39], a[220:223], v[152:155], v[36:39]\n" \
"ds_write_b128 v239, v[152:155] offset:3072\n" \
"v_mfma_f32_16x16x32_bf16 v[20:23], a[220:223], v[120:123], v[212:215]\n" \
"v_accvgpr_write_b32 a147, v99\n" \
"v_accvgpr_write_b32 a148, v104\n" \
"v_mfma_f32_16x16x32_bf16 v[20:23], a[216:219], v[112:115], v[20:23]\n" \
"ds_read_b128 a[220:223], v237 offset:22784\n" \
"v_accvgpr_write_b32 a149, v105\n" \
"v_mfma_f32_16x16x32_bf16 v[20:23], a[212:215], v[104:107], v[20:23]\n" \
"ds_read_b128 a[216:219], v237 offset:20736\n" \
"v_accvgpr_write_b32 a150, v106\n" \
"v_mfma_f32_16x16x32_bf16 v[20:23], a[208:211], v[96:99], v[20:23]\n" \
"ds_read_b128 a[212:215], v237 offset:18688\n" \
"ds_write_b128 v239, v[160:163] offset:4096\n" \
"v_mfma_f32_16x16x32_bf16 v[24:27], a[224:227], v[96:99], v[216:219]\n" \
"ds_read_b128 a[208:211], v237 offset:16640\n" \
"v_accvgpr_write_b32 a151, v107\n" \
"v_mfma_f32_16x16x32_bf16 v[24:27], a[228:231], v[104:107], v[24:27]\n" \
"v_accvgpr_write_b32 a152, v112\n" \
"v_accvgpr_write_b32 a153, v113\n" \
"v_mfma_f32_16x16x32_bf16 v[24:27], a[232:235], v[112:115], v[24:27]\n" \
"ds_write_b128 v239, v[168:171] offset:5120\n" \
"v_mfma_f32_16x16x32_bf16 v[24:27], a[236:239], v[120:123], v[24:27]\n" \
"buffer_store_dwordx4 v[8:11], v234, s[56:59], s93 offen offset:2048 sc1\n" \
"v_mfma_f32_16x16x32_bf16 v[40:43], a[236:239], v[152:155], v[196:199]\n" \
"v_accvgpr_write_b32 a154, v114\n" \
"v_accvgpr_write_b32 a155, v115\n" \
"v_mfma_f32_16x16x32_bf16 v[40:43], a[232:235], v[144:147], v[40:43]\n" \
"v_accvgpr_write_b32 a156, v120\n" \
"v_accvgpr_write_b32 a157, v121\n" \
"v_mfma_f32_16x16x32_bf16 v[40:43], a[228:231], v[136:139], v[40:43]\n" \
"v_accvgpr_write_b32 a158, v122\n" \
"v_accvgpr_write_b32 a159, v123\n" \
"v_mfma_f32_16x16x32_bf16 v[40:43], a[224:227], v[128:131], v[40:43]\n" \
"ds_write_b128 v239, v[176:179] offset:6144\n" \
"v_mfma_f32_16x16x32_bf16 v[56:59], a[224:227], v[160:163], v[196:199]\n" \
"v_accvgpr_write_b32 a140, v88\n" \
"v_accvgpr_write_b32 a141, v89\n" \
"v_mfma_f32_16x16x32_bf16 v[56:59], a[228:231], v[168:171], v[56:59]\n" \
"ds_read_b128 a[224:227], v237 offset:24576\n" \
"v_accvgpr_write_b32 a142, v90\n" \
"v_mfma_f32_16x16x32_bf16 v[56:59], a[232:235], v[176:179], v[56:59]\n" \
"s_waitcnt lgkmcnt(2)\n" \
"ds_read_b128 a[228:231], v237 offset:26624\n" \
"v_accvgpr_write_b32 a143, v91\n" \
"v_mfma_f32_16x16x32_bf16 v[56:59], a[236:239], v[184:187], v[56:59]\n" \
"buffer_store_dwordx4 v[12:15], v234, s[56:59], s93 offen offset:3072 sc1\n" \
"ds_write_b128 v239, v[184:187] offset:7168\n" \
"v_mfma_f32_16x16x32_bf16 v[60:63], a[252:255], v[184:187], v[196:199]\n" \
"ds_read_b128 a[232:235], v237 offset:28672\n" \
"v_add_u32_e64 v233, s82, v235\n" \
"v_mfma_f32_16x16x32_bf16 v[60:63], a[248:251], v[176:179], v[60:63]\n" \
"s_waitcnt vmcnt(8)\n" \
"ds_read_b128 a[236:239], v237 offset:30720\n" \
"v_cmp_eq_u32 vcc, s60, v227\n" \
"v_mfma_f32_16x16x32_bf16 v[60:63], a[244:247], v[168:171], v[60:63]\n" \
"v_exp_f32_e64 v16, v16\n" \
"v_mfma_f32_16x16x32_bf16 v[60:63], a[240:243], v[160:163], v[60:63]\n" \
"s_andn2_b32 s70, 0xff, vcc_lo\n" \
"s_cbranch_scc1 Lpl1_%=\n" \
"Lpr1_%=:\n" \
"v_exp_f32 v17, v17\n" \
"v_exp_f32 v18, v18\n" \
"v_mfma_f32_16x16x32_bf16 v[44:47], a[240:243], v[128:131], v[196:199]\n" \
"v_exp_f32 v19, v19\n" \
"v_mfma_f32_16x16x32_bf16 v[44:47], a[244:247], v[136:139], v[44:47]\n" \
"s_add_u32 m0, s79, 32768\n" \
"v_exp_f32 v20, v20\n" \
"v_mfma_f32_16x16x32_bf16 v[44:47], a[248:251], v[144:147], v[44:47]\n" \
"s_add_u32 s71, s76, 0\n" \
"v_cvt_pk_bf16_f32 v16, v16, v17\n" \
"v_cvt_pk_bf16_f32 v17, v18, v19\n" \
"v_mfma_f32_16x16x32_bf16 v[44:47], a[252:255], v[152:155], v[44:47]\n" \
"buffer_load_dwordx4 v234, s[36:39], s71 offen offset:0 sc1 lds\n" \
"v_exp_f32 v21, v21\n" \
"v_exp_f32 v22, v22\n" \
"v_mfma_f32_16x16x32_bf16 v[28:31], a[252:255], v[120:123], v[220:223]\n" \
"v_exp_f32 v23, v23\n" \
"v_mfma_f32_16x16x32_bf16 v[28:31], a[248:251], v[112:115], v[28:31]\n" \
"ds_read_b128 a[252:255], v237 offset:30976\n" \
"v_cvt_pk_bf16_f32 v18, v20, v21\n" \
"v_mfma_f32_16x16x32_bf16 v[28:31], a[244:247], v[104:107], v[28:31]\n" \
"buffer_load_dwordx4 v234, s[36:39], s71 offen offset:1024 sc1 lds\n" \
"v_mfma_f32_16x16x32_bf16 v[28:31], a[240:243], v[96:99], v[28:31]\n" \
"ds_read_b128 a[244:247], v237 offset:26880\n" \
"ds_read_b128 a[248:251], v237 offset:28928\n" \
"v_exp_f32 v32, v32\n" \
"v_exp_f32 v33, v33\n" \
"v_mfma_f32_16x16x32_bf16 v[80:83], a[192:195], a[144:147], v[196:199]\n" \
"ds_read_b128 a[240:243], v237 offset:24832\n" \
"v_cvt_pk_bf16_f32 v19, v22, v23\n" \
"v_mfma_f32_16x16x32_bf16 v[80:83], a[196:199], a[148:151], v[80:83]\n" \
"buffer_load_dwordx4 v234, s[36:39], s71 offen offset:2048 sc1 lds\n" \
"v_mfma_f32_16x16x32_bf16 v[80:83], a[200:203], a[152:155], v[80:83]\n" \
"s_lshl_b32 s68, s63, 2\n" \
"s_add_u32 s68, s68, s95\n" \
"s_mov_b32 s69, 0\n" \
"v_mfma_f32_16x16x32_bf16 v[80:83], a[204:207], a[156:159], v[80:83]\n" \
"s_mov_b32 s89, s63\n" \
"ds_read_b128 a[44:47], v233 offset:15360\n" \
"v_mov_b32 v192, s60\n" \
"v_lshlrev_b32 v193, 28, s61\n" \
"v_exp_f32 v34, v34\n" \
"v_exp_f32 v35, v35\n" \
"v_mfma_f32_16x16x32_bf16 v[96:99], a[204:207], v[152:155], v[196:199]\n" \
"buffer_load_dwordx4 v234, s[36:39], s71 offen offset:3072 sc1 lds\n" \
"v_mfma_f32_16x16x32_bf16 v[96:99], a[200:203], v[144:147], v[96:99]\n" \
"s_add_u32 m0, s79, 49152\n" \
"s_mov_b32 s90, 0\n" \
"ds_read_b128 a[40:43], v233 offset:7168\n" \
"v_mfma_f32_16x16x32_bf16 v[96:99], a[196:199], v[136:139], v[96:99]\n" \
"s_add_u32 s71, s76, 17408\n" \
"v_exp_f32 v36, v36\n" \
"v_cvt_pk_bf16_f32 v32, v32, v33\n" \
"v_mfma_f32_16x16x32_bf16 v[96:99], a[192:195], v[128:131], v[96:99]\n" \
"buffer_load_dwordx4 v234, s[36:39], s71 offen offset:0 sc1 lds\n" \
"v_mfma_f32_16x16x32_bf16 v[112:115], a[192:195], v[160:163], v[196:199]\n" \
"ds_read_b128 a[36:39], v233 offset:14336\n" \
"v_cvt_pk_bf16_f32 v33, v34, v35\n" \
"v_mfma_f32_16x16x32_bf16 v[112:115], a[196:199], v[168:171], v[112:115]\n" \
"v_exp_f32 v37, v37\n" \
"v_mfma_f32_16x16x32_bf16 v[112:115], a[200:203], v[176:179], v[112:115]\n" \
"buffer_load_dwordx4 v234, s[36:39], s71 offen offset:1024 sc1 lds\n" \
"v_mfma_f32_16x16x32_bf16 v[112:115], a[204:207], v[184:187], v[112:115]\n" \
"ds_read_b128 a[32:35], v233 offset:6144\n" \
"v_exp_f32 v38, v38\n" \
"v_exp_f32 v39, v39\n" \
"v_mfma_f32_16x16x32_bf16 v[64:67], a[204:207], a[140:143], v[208:211]\n" \
"v_exp_f32 v48, v48\n" \
"v_mfma_f32_16x16x32_bf16 v[64:67], a[200:203], a[136:139], v[64:67]\n" \
"buffer_load_dwordx4 v234, s[36:39], s71 offen offset:2048 sc1 lds\n" \
"v_cvt_pk_bf16_f32 v34, v36, v37\n" \
"v_mfma_f32_16x16x32_bf16 v[64:67], a[196:199], a[132:135], v[64:67]\n" \
"ds_read_b128 a[204:207], v233 offset:9216\n" \
"v_cvt_pk_bf16_f32 v35, v38, v39\n" \
"v_mfma_f32_16x16x32_bf16 v[64:67], a[192:195], a[128:131], v[64:67]\n" \
"v_exp_f32 v49, v49\n" \
"v_exp_f32 v50, v50\n" \
"v_mfma_f32_16x16x32_bf16 v[68:71], a[208:211], a[128:131], v[212:215]\n" \
"buffer_load_dwordx4 v234, s[36:39], s71 offen offset:3072 sc1 lds\n" \
"v_mfma_f32_16x16x32_bf16 v[68:71], a[212:215], a[132:135], v[68:71]\n" \
"ds_read_b128 a[200:203], v233 offset:1024\n" \
"v_cvt_pk_bf16_f32 v48, v48, v49\n" \
"v_mfma_f32_16x16x32_bf16 v[68:71], a[216:219], a[136:139], v[68:71]\n" \
"s_add_u32 m0, s77, s80\n" \
"v_exp_f32 v51, v51\n" \
"v_mfma_f32_16x16x32_bf16 v[68:71], a[220:223], a[140:143], v[68:71]\n" \
"buffer_load_dwordx4 v234, s[40:43], s76 offen offset:0 sc1 lds\n" \
"v_exp_f32 v52, v52\n" \
"v_exp_f32 v53, v53\n" \
"v_mfma_f32_16x16x32_bf16 v[116:119], a[220:223], v[184:187], v[196:199]\n" \
"ds_read_b128 a[196:199], v233 offset:8192\n" \
"v_cvt_pk_bf16_f32 v49, v50, v51\n" \
"v_mfma_f32_16x16x32_bf16 v[116:119], a[216:219], v[176:179], v[116:119]\n" \
"ds_read_b128 a[160:163], v239 offset:0\n" \
"v_mfma_f32_16x16x32_bf16 v[116:119], a[212:215], v[168:171], v[116:119]\n" \
"s_add_u32 s71, s76, 17408\n" \
"ds_read_b128 a[192:195], v233 offset:0\n" \
"v_exp_f32 v54, v54\n" \
"v_mfma_f32_16x16x32_bf16 v[116:119], a[208:211], v[160:163], v[116:119]\n" \
"buffer_load_dwordx4 v234, s[40:43], s76 offen offset:1024 sc1 lds\n" \
"v_mfma_f32_16x16x32_bf16 v[100:103], a[208:211], v[128:131], v[196:199]\n" \
"ds_read_b128 a[164:167], v239 offset:1024\n" \
"v_cvt_pk_bf16_f32 v50, v52, v53\n" \
"v_mfma_f32_16x16x32_bf16 v[100:103], a[212:215], v[136:139], v[100:103]\n" \
"v_exp_f32 v55, v55\n" \
"v_mfma_f32_16x16x32_bf16 v[100:103], a[216:219], v[144:147], v[100:103]\n" \
"v_cvt_pk_bf16_f32 v51, v54, v55\n" \
"v_exp_f32 v24, v24\n" \
"v_mfma_f32_16x16x32_bf16 v[100:103], a[220:223], v[152:155], v[100:103]\n" \
"buffer_load_dwordx4 v234, s[40:43], s76 offen offset:2048 sc1 lds\n" \
"v_exp_f32 v25, v25\n" \
"v_mfma_f32_16x16x32_bf16 v[84:87], a[220:223], a[156:159], v[196:199]\n" \
"ds_read_b128 a[168:171], v239 offset:2048\n" \
"v_mfma_f32_16x16x32_bf16 v[84:87], a[216:219], a[152:155], v[84:87]\n" \
"ds_read_b128 a[220:223], v233 offset:11264\n" \
"v_mfma_f32_16x16x32_bf16 v[84:87], a[212:215], a[148:151], v[84:87]\n" \
"s_waitcnt lgkmcnt(2)\n" \
"v_exp_f32 v26, v26\n" \
"v_mfma_f32_16x16x32_bf16 v[84:87], a[208:211], a[144:147], v[84:87]\n" \
"buffer_load_dwordx4 v234, s[40:43], s76 offen offset:3072 sc1 lds\n" \
"v_exp_f32 v27, v27\n" \
"v_mfma_f32_16x16x32_bf16 v[88:91], a[224:227], a[144:147], v[196:199]\n" \
"ds_read_b128 a[216:219], v233 offset:3072\n" \
"v_cvt_pk_bf16_f32 v20, v24, v25\n" \
"v_mfma_f32_16x16x32_bf16 v[88:91], a[228:231], a[148:151], v[88:91]\n" \
"ds_read_b128 a[172:175], v239 offset:3072\n" \
"v_exp_f32 v28, v28\n" \
"v_mfma_f32_16x16x32_bf16 v[88:91], a[232:235], a[152:155], v[88:91]\n" \
"s_add_u32 m0, m0, 0x4000\n" \
"ds_read_b128 a[212:215], v233 offset:10240\n" \
"v_xor_b32 v248, 0x2000, v239\n" \
"v_mfma_f32_16x16x32_bf16 v[88:91], a[236:239], a[156:159], v[88:91]\n" \
"buffer_load_dwordx4 v234, s[40:43], s71 offen offset:0 sc1 lds\n" \
"v_exp_f32 v29, v29\n" \
"v_mfma_f32_16x16x32_bf16 v[104:107], a[236:239], v[152:155], v[196:199]\n" \
"ds_read_b128 a[208:211], v233 offset:2048\n" \
"v_lshlrev_b32 v252, s48, v234\n" \
"v_mfma_f32_16x16x32_bf16 v[104:107], a[232:235], v[144:147], v[104:107]\n" \
"ds_read_b128 a[176:179], v239 offset:4096\n" \
"v_mov_b32 v224, 0\n" \
"v_mfma_f32_16x16x32_bf16 v[104:107], a[228:231], v[136:139], v[104:107]\n" \
"v_mov_b32 v225, 0\n" \
"v_mov_b32 v228, 0\n" \
"v_mfma_f32_16x16x32_bf16 v[104:107], a[224:227], v[128:131], v[104:107]\n" \
"buffer_load_dwordx4 v234, s[40:43], s71 offen offset:1024 sc1 lds\n" \
"v_exp_f32 v30, v30\n" \
"v_cvt_pk_bf16_f32 v21, v26, v27\n" \
"v_mfma_f32_16x16x32_bf16 v[120:123], a[224:227], v[160:163], v[196:199]\n" \
"ds_read_b128 a[180:183], v239 offset:5120\n" \
"v_mov_b32 v229, 0\n" \
"v_mfma_f32_16x16x32_bf16 v[120:123], a[228:231], v[168:171], v[120:123]\n" \
"v_exp_f32 v31, v31\n" \
"v_mfma_f32_16x16x32_bf16 v[120:123], a[232:235], v[176:179], v[120:123]\n" \
"v_exp_f32 v40, v40\n" \
"v_cvt_pk_bf16_f32 v22, v28, v29\n" \
"v_mfma_f32_16x16x32_bf16 v[120:123], a[236:239], v[184:187], v[120:123]\n" \
"buffer_load_dwordx4 v234, s[40:43], s71 offen offset:2048 sc1 lds\n" \
"v_exp_f32 v41, v41\n" \
"v_mfma_f32_16x16x32_bf16 v[72:75], a[236:239], a[140:143], v[216:219]\n" \
"ds_read_b128 a[184:187], v239 offset:6144\n" \
"v_cvt_pk_bf16_f32 v23, v30, v31\n" \
"v_mfma_f32_16x16x32_bf16 v[72:75], a[232:235], a[136:139], v[72:75]\n" \
"ds_read_b128 a[236:239], v233 offset:13312\n" \
"v_mfma_f32_16x16x32_bf16 v[72:75], a[228:231], a[132:135], v[72:75]\n" \
"v_exp_f32 v42, v42\n" \
"v_mfma_f32_16x16x32_bf16 v[72:75], a[224:227], a[128:131], v[72:75]\n" \
"buffer_load_dwordx4 v234, s[40:43], s71 offen offset:3072 sc1 lds\n" \
"v_exp_f32 v43, v43\n" \
"v_exp_f32 v44, v44\n" \
"v_mfma_f32_16x16x32_bf16 v[76:79], a[240:243], a[128:131], v[220:223]\n" \
"ds_read_b128 a[232:235], v233 offset:5120\n" \
"v_cvt_pk_bf16_f32 v36, v40, v41\n" \
"v_mfma_f32_16x16x32_bf16 v[76:79], a[244:247], a[132:135], v[76:79]\n" \
"ds_read_b128 a[188:191], v239 offset:7168\n" \
"v_cvt_pk_bf16_f32 v37, v42, v43\n" \
"v_mfma_f32_16x16x32_bf16 v[76:79], a[248:251], a[136:139], v[76:79]\n" \
"ds_read_b128 a[228:231], v233 offset:12288\n" \
"v_mfma_f32_16x16x32_bf16 v[76:79], a[252:255], a[140:143], v[76:79]\n" \
"v_exp_f32 v45, v45\n" \
"v_exp_f32 v46, v46\n" \
"v_mfma_f32_16x16x32_bf16 v[124:127], a[252:255], v[184:187], v[196:199]\n" \
"ds_read_b128 a[224:227], v233 offset:4096\n" \
"v_cvt_pk_bf16_f32 v38, v44, v45\n" \
"v_mfma_f32_16x16x32_bf16 v[124:127], a[248:251], v[176:179], v[124:127]\n" \
"v_exp_f32 v47, v47\n" \
"v_mfma_f32_16x16x32_bf16 v[124:127], a[244:247], v[168:171], v[124:127]\n" \
"v_exp_f32 v56, v56\n" \
"v_exp_f32 v57, v57\n" \
"v_mfma_f32_16x16x32_bf16 v[124:127], a[240:243], v[160:163], v[124:127]\n" \
"v_exp_f32 v58, v58\n" \
"v_cvt_pk_bf16_f32 v39, v46, v47\n" \
"v_mfma_f32_16x16x32_bf16 v[108:111], a[240:243], v[128:131], v[196:199]\n" \
"v_exp_f32 v59, v59\n" \
"v_mfma_f32_16x16x32_bf16 v[108:111], a[244:247], v[136:139], v[108:111]\n" \
"v_exp_f32 v60, v60\n" \
"v_mfma_f32_16x16x32_bf16 v[108:111], a[248:251], v[144:147], v[108:111]\n" \
"v_cvt_pk_bf16_f32 v52, v56, v57\n" \
"v_cvt_pk_bf16_f32 v53, v58, v59\n" \
"v_mfma_f32_16x16x32_bf16 v[108:111], a[252:255], v[152:155], v[108:111]\n" \
"v_exp_f32 v61, v61\n" \
"v_exp_f32 v62, v62\n" \
"v_exp_f32 v63, v63\n" \
"v_mfma_f32_16x16x32_bf16 v[92:95], a[252:255], a[156:159], v[196:199]\n" \
"v_add_u32 v232, s91, v237\n" \
"v_cvt_pk_bf16_f32 v54, v60, v61\n" \
"v_mfma_f32_16x16x32_bf16 v[92:95], a[248:251], a[152:155], v[92:95]\n" \
"v_cvt_pk_bf16_f32 v55, v62, v63\n" \
"v_mfma_f32_16x16x32_bf16 v[92:95], a[244:247], a[148:151], v[92:95]\n" \
"s_waitcnt vmcnt(8) lgkmcnt(0)\n" \
"s_barrier\n" \
"v_mfma_f32_16x16x32_bf16 v[92:95], a[240:243], a[144:147], v[92:95]\n" \
"buffer_store_dword v192, v193, s[56:59], s68 offen sc1\n" \
"v_mfma_f32_16x16x32_bf16 v[160:163], a[192:195], v[16:19], 0\n" \
"s_add_u32 m0, s74, s79\n" \
"ds_read_b128 a[240:243], v232 offset:8448\n" \
"v_xor_b32 v233, 0x4000, v233\n" \
"v_mfma_f32_16x16x32_bf16 v[160:163], a[196:199], v[20:23], v[160:163]\n" \
"buffer_load_dwordx4 v234, s[36:39], s73 offen offset:0 sc1 lds\n" \
"v_exp_f32 v80, v80\n" \
"v_mfma_f32_16x16x32_bf16 a[64:67], a[192:195], v[32:35], 0\n" \
"ds_read_b128 a[244:247], v232 offset:10496\n" \
"v_mfma_f32_16x16x32_bf16 a[64:67], a[196:199], v[36:39], a[64:67]\n" \
"ds_read_b128 a[0:3], v248 offset:0\n" \
"v_exp_f32 v81, v81\n" \
"v_mfma_f32_16x16x32_bf16 a[96:99], a[192:195], v[48:51], 0\n" \
"v_exp_f32 v82, v82\n" \
"v_mfma_f32_16x16x32_bf16 a[96:99], a[196:199], v[52:55], a[96:99]\n" \
"s_add_u32 s71, s73, 17408\n" \
"ds_read_b128 a[192:195], v232 offset:0\n" \
"v_cvt_pk_bf16_f32 v80, v80, v81\n" \
"v_mfma_f32_16x16x32_bf16 a[100:103], a[200:203], v[48:51], 0\n" \
"buffer_load_dwordx4 v234, s[36:39], s73 offen offset:1024 sc1 lds\n" \
"v_mfma_f32_16x16x32_bf16 a[100:103], a[204:207], v[52:55], a[100:103]\n" \
"ds_read_b128 a[196:199], v232 offset:2048\n" \
"v_exp_f32 v83, v83\n" \
"v_mfma_f32_16x16x32_bf16 a[68:71], a[200:203], v[32:35], 0\n" \
"ds_read_b128 a[4:7], v248 offset:1024\n" \
"v_mfma_f32_16x16x32_bf16 a[68:71], a[204:207], v[36:39], a[68:71]\n" \
"v_exp_f32 v84, v84\n" \
"v_exp_f32 v85, v85\n" \
"v_mfma_f32_16x16x32_bf16 v[164:167], a[200:203], v[16:19], 0\n" \
"ds_read_b128 a[8:11], v248 offset:2048\n" \
"v_cvt_pk_bf16_f32 v81, v82, v83\n" \
"v_mfma_f32_16x16x32_bf16 v[164:167], a[204:207], v[20:23], v[164:167]\n" \
"buffer_load_dwordx4 v234, s[36:39], s73 offen offset:2048 sc1 lds\n" \
"v_mfma_f32_16x16x32_bf16 v[168:171], a[208:211], v[16:19], 0\n" \
"ds_read_b128 a[200:203], v232 offset:4096\n" \
"v_mfma_f32_16x16x32_bf16 v[168:171], a[212:215], v[20:23], v[168:171]\n" \
"ds_read_b128 a[204:207], v232 offset:6144\n" \
"v_exp_f32 v86, v86\n" \
"v_mfma_f32_16x16x32_bf16 a[72:75], a[208:211], v[32:35], 0\n" \
"ds_read_b128 a[12:15], v248 offset:3072\n" \
"v_cvt_pk_bf16_f32 v82, v84, v85\n" \
"v_mfma_f32_16x16x32_bf16 a[72:75], a[212:215], v[36:39], a[72:75]\n" \
"v_exp_f32 v87, v87\n" \
"v_exp_f32 v96, v96\n" \
"v_mfma_f32_16x16x32_bf16 a[104:107], a[208:211], v[48:51], 0\n" \
"buffer_load_dwordx4 v234, s[36:39], s73 offen offset:3072 sc1 lds\n" \
"v_mfma_f32_16x16x32_bf16 a[104:107], a[212:215], v[52:55], a[104:107]\n" \
"ds_read_b128 a[208:211], v232 offset:256\n" \
"v_cvt_pk_bf16_f32 v83, v86, v87\n" \
"v_smfmac_f32_16x16x64_bf16 a[52:55], v[244:247], v[16:23], v243\n" \
"ds_read_b128 a[212:215], v232 offset:2304\n" \
"v_exp_f32 v97, v97\n" \
"v_mfma_f32_16x16x32_bf16 a[108:111], a[216:219], v[48:51], 0\n" \
"ds_read_b128 a[16:19], v248 offset:4096\n" \
"v_mfma_f32_16x16x32_bf16 a[108:111], a[220:223], v[52:55], a[108:111]\n" \
"s_add_u32 m0, m0, 0x4000\n" \
"v_exp_f32 v98, v98\n" \
"v_mfma_f32_16x16x32_bf16 a[76:79], a[216:219], v[32:35], 0\n" \
"buffer_load_dwordx4 v234, s[36:39], s71 offen offset:0 sc1 lds\n" \
"v_mfma_f32_16x16x32_bf16 a[76:79], a[220:223], v[36:39], a[76:79]\n" \
"ds_read_b128 a[20:23], v248 offset:5120\n" \
"v_cvt_pk_bf16_f32 v96, v96, v97\n" \
"v_exp_f32 v99, v99\n" \
"v_mfma_f32_16x16x32_bf16 v[172:175], a[216:219], v[16:19], 0\n" \
"v_exp_f32 v100, v100\n" \
"v_mfma_f32_16x16x32_bf16 v[172:175], a[220:223], v[20:23], v[172:175]\n" \
"ds_read_b128 a[216:219], v232 offset:4352\n" \
"v_cvt_pk_bf16_f32 v97, v98, v99\n" \
"v_mfma_f32_32x32x16_bf16 v[128:143], a[48:51], a[48:51], 0\n" \
"ds_read_b128 a[220:223], v232 offset:6400\n" \
"v_exp_f32 v101, v101\n" \
"v_mfma_f32_16x16x32_bf16 v[176:179], a[224:227], v[16:19], 0\n" \
"buffer_load_dwordx4 v234, s[36:39], s71 offen offset:1024 sc1 lds\n" \
"v_mfma_f32_16x16x32_bf16 v[176:179], a[228:231], v[20:23], v[176:179]\n" \
"ds_read_b128 a[24:27], v248 offset:6144\n" \
"v_exp_f32 v102, v102\n" \
"v_mfma_f32_16x16x32_bf16 a[80:83], a[224:227], v[32:35], 0\n" \
"v_exp_f32 v103, v103\n" \
"v_mfma_f32_16x16x32_bf16 a[80:83], a[228:231], v[36:39], a[80:83]\n" \
"ds_read_b128 a[28:31], v248 offset:7168\n" \
"v_cvt_pk_bf16_f32 v98, v100, v101\n" \
"v_exp_f32 v112, v112\n" \
"v_mfma_f32_16x16x32_bf16 a[112:115], a[224:227], v[48:51], 0\n" \
"v_exp_f32 v113, v113\n" \
"v_mfma_f32_16x16x32_bf16 a[112:115], a[228:231], v[52:55], a[112:115]\n" \
"buffer_load_dwordx4 v234, s[36:39], s71 offen offset:2048 sc1 lds\n" \
"v_smfmac_f32_16x16x64_bf16 v[224:227], v[244:247], v[32:39], v243\n" \
"ds_read_b128 a[224:227], v232 offset:8192\n" \
"v_cvt_pk_bf16_f32 v99, v102, v103\n" \
"v_mfma_f32_16x16x32_bf16 a[116:119], a[232:235], v[48:51], 0\n" \
"ds_read_b128 a[228:231], v232 offset:10240\n" \
"v_mfma_f32_16x16x32_bf16 a[116:119], a[236:239], v[52:55], a[116:119]\n" \
"v_exp_f32 v114, v114\n" \
"v_exp_f32 v115, v115\n" \
"v_mfma_f32_16x16x32_bf16 a[84:87], a[232:235], v[32:35], 0\n" \
"v_exp_f32 v116, v116\n" \
"v_mfma_f32_16x16x32_bf16 a[84:87], a[236:239], v[36:39], a[84:87]\n" \
"buffer_load_dwordx4 v234, s[36:39], s71 offen offset:3072 sc1 lds\n" \
"v_cvt_pk_bf16_f32 v112, v112, v113\n" \
"v_mfma_f32_16x16x32_bf16 v[180:183], a[232:235], v[16:19], 0\n" \
"v_exp_f32 v117, v117\n" \
"v_mfma_f32_16x16x32_bf16 v[180:183], a[236:239], v[20:23], v[180:183]\n" \
"s_mov_b32 s76, s73\n" \
"s_add_u32 s73, s73, 34816\n" \
"ds_read_b128 a[232:235], v232 offset:12288\n" \
"v_mfma_f32_32x32x16_bf16 v[144:159], a[48:51], a[48:51], 0\n" \
"s_cmp_eq_u32 s73, s83\n" \
"s_cselect_b32 s70, 34816, 0\n" \
"ds_read_b128 a[236:239], v232 offset:14336\n" \
"v_exp_f32 v118, v118\n" \
"v_mfma_f32_16x16x32_bf16 v[184:187], a[32:35], v[16:19], 0\n" \
"s_add_u32 s73, s73, s70\n" \
"s_xor_b32 s74, s74, 0x8000\n" \
"ds_read_b128 a[248:251], v232 offset:12544\n" \
"v_mfma_f32_16x16x32_bf16 v[184:187], a[36:39], v[20:23], v[184:187]\n" \
"s_xor_b32 s77, s77, 0x8000\n" \
"ds_read_b128 a[252:255], v232 offset:14592\n" \
"v_cvt_pk_bf16_f32 v113, v114, v115\n" \
"v_mfma_f32_16x16x32_bf16 a[88:91], a[32:35], v[32:35], 0\n" \
"v_exp_f32 v119, v119\n" \
"v_mfma_f32_16x16x32_bf16 a[88:91], a[36:39], v[36:39], a[88:91]\n" \
"v_exp_f32 v64, v64\n" \
"v_cvt_pk_bf16_f32 v114, v116, v117\n" \
"v_mfma_f32_16x16x32_bf16 a[120:123], a[32:35], v[48:51], 0\n" \
"v_exp_f32 v65, v65\n" \
"v_mfma_f32_16x16x32_bf16 a[120:123], a[36:39], v[52:55], a[120:123]\n" \
"v_exp_f32 v66, v66\n" \
"v_cvt_pk_bf16_f32 v115, v118, v119\n" \
"v_smfmac_f32_16x16x64_bf16 v[228:231], v[244:247], v[48:55], v243\n" \
"v_cvt_pk_bf16_f32 v64, v64, v65\n" \
"v_mfma_f32_16x16x32_bf16 a[124:127], a[40:43], v[48:51], 0\n" \
"v_exp_f32 v67, v67\n" \
"v_mfma_f32_16x16x32_bf16 a[124:127], a[44:47], v[52:55], a[124:127]\n" \
"v_exp_f32 v68, v68\n" \
"v_exp_f32 v69, v69\n" \
"v_mfma_f32_16x16x32_bf16 a[92:95], a[40:43], v[32:35], 0\n" \
"v_exp_f32 v70, v70\n" \
"v_mfma_f32_16x16x32_bf16 a[92:95], a[44:47], v[36:39], a[92:95]\n" \
"v_cvt_pk_bf16_f32 v65, v66, v67\n" \
"v_cvt_pk_bf16_f32 v66, v68, v69\n" \
"v_mfma_f32_16x16x32_bf16 v[188:191], a[40:43], v[16:19], 0\n" \
"s_cmp_eq_u32 s63, 0\n" \
"v_exp_f32_e64 v71, v71\n" \
"v_mfma_f32_16x16x32_bf16 v[188:191], a[44:47], v[20:23], v[188:191]\n" \
"s_waitcnt lgkmcnt(0)\n" \
"v_cvt_pk_bf16_f32 v67, v70, v71\n" \
"s_cbranch_scc1 Lloop2_%=\n" \
"Lloop_%=:\n" \
"v_mfma_f32_16x16x32_bf16 v[0:3], a[192:195], a[128:131], v[196:199]\n" \
"v_exp_f32 v72, v72\n" \
"v_mfma_f32_16x16x32_bf16 v[0:3], a[196:199], a[132:135], v[0:3]\n" \
"v_mfma_f32_16x16x32_bf16 v[0:3], a[200:203], a[136:139], v[0:3]\n" \
"v_exp_f32 v73, v73\n" \
"v_mfma_f32_16x16x32_bf16 v[0:3], a[204:207], a[140:143], v[0:3]\n" \
"ds_read_b128 a[32:35], v233 offset:6144\n" \
"v_cvt_pk_bf16_f32 v68, v72, v73\n" \
"v_mfma_f32_16x16x32_bf16 v[16:19], a[204:207], a[156:159], v[196:199]\n" \
"v_exp_f32 v74, v74\n" \
"v_mfma_f32_16x16x32_bf16 v[16:19], a[200:203], a[152:155], v[16:19]\n" \
"global_load_dword v253, v252, s[44:45]\n" \
"v_mfma_f32_16x16x32_bf16 v[16:19], a[196:199], a[148:151], v[16:19]\n" \
"s_sub_u32 s46, s46, 1\n" \
"v_exp_f32 v75, v75\n" \
"v_mfma_f32_16x16x32_bf16 v[16:19], a[192:195], a[144:147], v[16:19]\n" \
"ds_read_b128 a[36:39], v233 offset:14336\n" \
"v_cvt_pk_bf16_f32 v69, v74, v75\n" \
"v_mfma_f32_16x16x32_bf16 v[32:35], a[192:195], a[160:163], v[196:199]\n" \
"v_exp_f32 v76, v76\n" \
"v_mfma_f32_16x16x32_bf16 v[32:35], a[196:199], a[164:167], v[32:35]\n" \
"s_cmp_gt_i32 s46, 0\n" \
"s_cselect_b32 s47, 0x100000, 0\n" \
"v_mfma_f32_16x16x32_bf16 v[32:35], a[200:203], a[168:171], v[32:35]\n" \
"v_exp_f32 v77, v77\n" \
"v_mfma_f32_16x16x32_bf16 v[32:35], a[204:207], a[172:175], v[32:35]\n" \
"s_add_u32 s44, s44, s47\n" \
"s_addc_u32 s45, s45, 0\n" \
"ds_read_b128 a[40:43], v233 offset:7168\n" \
"v_cvt_pk_bf16_f32 v70, v76, v77\n" \
"v_mfma_f32_16x16x32_bf16 v[48:51], a[204:207], a[188:191], v[196:199]\n" \
"v_exp_f32 v78, v78\n" \
"v_mfma_f32_16x16x32_bf16 v[48:51], a[200:203], a[184:187], v[48:51]\n" \
"v_xor_b32 v232, 0x4000, v232\n" \
"v_mfma_f32_16x16x32_bf16 v[48:51], a[196:199], a[180:183], v[48:51]\n" \
"v_exp_f32 v79, v79\n" \
"v_mfma_f32_16x16x32_bf16 v[48:51], a[192:195], a[176:179], v[48:51]\n" \
"ds_read_b128 a[204:207], v233 offset:9216\n" \
"v_cvt_pk_bf16_f32 v71, v78, v79\n" \
"v_mfma_f32_16x16x32_bf16 v[52:55], a[208:211], a[176:179], v[196:199]\n" \
"v_exp_f32 v88, v88\n" \
"v_mfma_f32_16x16x32_bf16 v[52:55], a[212:215], a[180:183], v[52:55]\n" \
"v_mfma_f32_16x16x32_bf16 v[52:55], a[216:219], a[184:187], v[52:55]\n" \
"v_exp_f32 v89, v89\n" \
"v_mfma_f32_16x16x32_bf16 v[52:55], a[220:223], a[188:191], v[52:55]\n" \
"ds_read_b128 a[200:203], v233 offset:1024\n" \
"v_cvt_pk_bf16_f32 v84, v88, v89\n" \
"v_mfma_f32_16x16x32_bf16 v[36:39], a[220:223], a[172:175], v[196:199]\n" \
"v_exp_f32 v90, v90\n" \
"v_mfma_f32_16x16x32_bf16 v[36:39], a[216:219], a[168:171], v[36:39]\n" \
"v_mfma_f32_16x16x32_bf16 v[36:39], a[212:215], a[164:167], v[36:39]\n" \
"v_exp_f32 v91, v91\n" \
"v_mfma_f32_16x16x32_bf16 v[36:39], a[208:211], a[160:163], v[36:39]\n" \
"ds_read_b128 a[196:199], v233 offset:8192\n" \
"v_cvt_pk_bf16_f32 v85, v90, v91\n" \
"v_mfma_f32_16x16x32_bf16 v[20:23], a[208:211], a[144:147], v[196:199]\n" \
"v_exp_f32 v92, v92\n" \
"v_mfma_f32_16x16x32_bf16 v[20:23], a[212:215], a[148:151], v[20:23]\n" \
"v_mfma_f32_16x16x32_bf16 v[20:23], a[216:219], a[152:155], v[20:23]\n" \
"v_exp_f32 v93, v93\n" \
"v_mfma_f32_16x16x32_bf16 v[20:23], a[220:223], a[156:159], v[20:23]\n" \
"ds_read_b128 a[192:195], v233 offset:0\n" \
"v_cvt_pk_bf16_f32 v86, v92, v93\n" \
"v_mfma_f32_16x16x32_bf16 v[4:7], a[220:223], a[140:143], v[196:199]\n" \
"v_exp_f32 v94, v94\n" \
"v_mfma_f32_16x16x32_bf16 v[4:7], a[216:219], a[136:139], v[4:7]\n" \
"v_mfma_f32_16x16x32_bf16 v[4:7], a[212:215], a[132:135], v[4:7]\n" \
"s_waitcnt lgkmcnt(7)\n" \
"v_exp_f32 v95, v95\n" \
"v_mfma_f32_16x16x32_bf16 v[4:7], a[208:211], a[128:131], v[4:7]\n" \
"ds_read_b128 a[220:223], v233 offset:11264\n" \
"v_cvt_pk_bf16_f32 v87, v94, v95\n" \
"v_mfma_f32_16x16x32_bf16 v[8:11], a[224:227], a[128:131], v[196:199]\n" \
"v_exp_f32 v104, v104\n" \
"v_mfma_f32_16x16x32_bf16 v[8:11], a[228:231], a[132:135], v[8:11]\n" \
"v_mfma_f32_16x16x32_bf16 v[8:11], a[232:235], a[136:139], v[8:11]\n" \
"v_exp_f32 v105, v105\n" \
"v_mfma_f32_16x16x32_bf16 v[8:11], a[236:239], a[140:143], v[8:11]\n" \
"ds_read_b128 a[216:219], v233 offset:3072\n" \
"v_cvt_pk_bf16_f32 v100, v104, v105\n" \
"v_mfma_f32_16x16x32_bf16 v[24:27], a[236:239], a[156:159], v[196:199]\n" \
"v_exp_f32 v106, v106\n" \
"v_mfma_f32_16x16x32_bf16 v[24:27], a[232:235], a[152:155], v[24:27]\n" \
"v_mfma_f32_16x16x32_bf16 v[24:27], a[228:231], a[148:151], v[24:27]\n" \
"v_exp_f32 v107, v107\n" \
"v_mfma_f32_16x16x32_bf16 v[24:27], a[224:227], a[144:147], v[24:27]\n" \
"ds_read_b128 a[212:215], v233 offset:10240\n" \
"v_cvt_pk_bf16_f32 v101, v106, v107\n" \
"v_mfma_f32_16x16x32_bf16 v[40:43], a[224:227], a[160:163], v[196:199]\n" \
"v_exp_f32 v108, v108\n" \
"v_mfma_f32_16x16x32_bf16 v[40:43], a[228:231], a[164:167], v[40:43]\n" \
"v_mfma_f32_16x16x32_bf16 v[40:43], a[232:235], a[168:171], v[40:43]\n" \
"v_exp_f32 v109, v109\n" \
"v_mfma_f32_16x16x32_bf16 v[40:43], a[236:239], a[172:175], v[40:43]\n" \
"ds_read_b128 a[208:211], v233 offset:2048\n" \
"v_cvt_pk_bf16_f32 v102, v108, v109\n" \
"v_mfma_f32_16x16x32_bf16 v[56:59], a[236:239], a[188:191], v[196:199]\n" \
"v_exp_f32 v110, v110\n" \
"v_mfma_f32_16x16x32_bf16 v[56:59], a[232:235], a[184:187], v[56:59]\n" \
"v_mfma_f32_16x16x32_bf16 v[56:59], a[228:231], a[180:183], v[56:59]\n" \
"v_exp_f32 v111, v111\n" \
"v_mfma_f32_16x16x32_bf16 v[56:59], a[224:227], a[176:179], v[56:59]\n" \
"ds_read_b128 a[236:239], v233 offset:13312\n" \
"v_cvt_pk_bf16_f32 v103, v110, v111\n" \
"v_mfma_f32_16x16x32_bf16 v[60:63], a[240:243], a[176:179], v[196:199]\n" \
"v_exp_f32 v120, v120\n" \
"v_mfma_f32_16x16x32_bf16 v[60:63], a[244:247], a[180:183], v[60:63]\n" \
"v_mfma_f32_16x16x32_bf16 v[60:63], a[248:251], a[184:187], v[60:63]\n" \
"v_exp_f32 v121, v121\n" \
"v_mfma_f32_16x16x32_bf16 v[60:63], a[252:255], a[188:191], v[60:63]\n" \
"ds_read_b128 a[232:235], v233 offset:5120\n" \
"v_cvt_pk_bf16_f32 v116, v120, v121\n" \
"v_mfma_f32_16x16x32_bf16 v[44:47], a[252:255], a[172:175], v[196:199]\n" \
"v_exp_f32 v122, v122\n" \
"v_mfma_f32_16x16x32_bf16 v[44:47], a[248:251], a[168:171], v[44:47]\n" \
"v_mfma_f32_16x16x32_bf16 v[44:47], a[244:247], a[164:167], v[44:47]\n" \
"v_exp_f32 v123, v123\n" \
"v_mfma_f32_16x16x32_bf16 v[44:47], a[240:243], a[160:163], v[44:47]\n" \
"ds_read_b128 a[228:231], v233 offset:12288\n" \
"v_cvt_pk_bf16_f32 v117, v122, v123\n" \
"v_mfma_f32_16x16x32_bf16 v[28:31], a[240:243], a[144:147], v[196:199]\n" \
"v_exp_f32 v124, v124\n" \
"v_mfma_f32_16x16x32_bf16 v[28:31], a[244:247], a[148:151], v[28:31]\n" \
"v_mfma_f32_16x16x32_bf16 v[28:31], a[248:251], a[152:155], v[28:31]\n" \
"v_exp_f32 v125, v125\n" \
"v_mfma_f32_16x16x32_bf16 v[28:31], a[252:255], a[156:159], v[28:31]\n" \
"Lpg5603_%=:\n" \
"s_cbranch_execz Lpg6799_%=\n" \
"ds_read_b128 a[224:227], v233 offset:4096\n" \
"v_cvt_pk_bf16_f32 v118, v124, v125\n" \
"v_mfma_f32_16x16x32_bf16 v[12:15], a[252:255], a[140:143], v[196:199]\n" \
"v_exp_f32 v126, v126\n" \
"v_mfma_f32_16x16x32_bf16 v[12:15], a[248:251], a[136:139], v[12:15]\n" \
"v_mfma_f32_16x16x32_bf16 v[12:15], a[244:247], a[132:135], v[12:15]\n" \
"s_waitcnt lgkmcnt(2)\n" \
"v_exp_f32 v127, v127\n" \
"v_mfma_f32_16x16x32_bf16 v[12:15], a[240:243], a[128:131], v[12:15]\n" \
"ds_read_b128 a[44:47], v233 offset:15360\n" \
"v_cvt_pk_bf16_f32 v119, v126, v127\n" \
"v_mfma_f32_16x16x32_bf16 v[128:131], a[192:195], v[64:67], v[128:131]\n" \
"v_exp_f32 v0, v0\n" \
"v_mfma_f32_16x16x32_bf16 v[128:131], a[196:199], v[68:71], v[128:131]\n" \
"ds_read_b128 a[244:247], v232 offset:10496\n" \
"v_mfma_f32_16x16x32_bf16 v[160:163], a[192:195], v[80:83], v[160:163]\n" \
"v_exp_f32 v1, v1\n" \
"v_mfma_f32_16x16x32_bf16 v[160:163], a[196:199], v[84:87], v[160:163]\n" \
"ds_read_b128 a[240:243], v232 offset:8448\n" \
"v_cvt_pk_bf16_f32 v0, v0, v1\n" \
"v_mfma_f32_16x16x32_bf16 a[64:67], a[192:195], v[96:99], a[64:67]\n" \
"v_exp_f32 v2, v2\n" \
"v_mfma_f32_16x16x32_bf16 a[64:67], a[196:199], v[100:103], a[64:67]\n" \
"ds_read_b128 a[248:251], v232 offset:12544\n" \
"v_xor_b32 v233, 0xc000, v233\n" \
"v_mfma_f32_16x16x32_bf16 a[96:99], a[192:195], v[112:115], a[96:99]\n" \
"v_exp_f32 v3, v3\n" \
"v_mfma_f32_16x16x32_bf16 a[96:99], a[196:199], v[116:119], a[96:99]\n" \
"ds_read_b128 a[192:195], v232 offset:0\n" \
"v_cvt_pk_bf16_f32 v1, v2, v3\n" \
"v_mfma_f32_16x16x32_bf16 a[100:103], a[200:203], v[112:115], a[100:103]\n" \
"v_exp_f32 v4, v4\n" \
"v_mfma_f32_16x16x32_bf16 a[100:103], a[204:207], v[116:119], a[100:103]\n" \
"ds_read_b128 a[252:255], v232 offset:14592\n" \
"v_mfma_f32_16x16x32_bf16 a[68:71], a[200:203], v[96:99], a[68:71]\n" \
"v_exp_f32 v5, v5\n" \
"v_mfma_f32_16x16x32_bf16 a[68:71], a[204:207], v[100:103], a[68:71]\n" \
"ds_read_b128 a[196:199], v232 offset:2048\n" \
"v_cvt_pk_bf16_f32 v2, v4, v5\n" \
"v_smfmac_f32_16x16x64_bf16 a[48:51], v[244:247], v[64:71], v243\n" \
"v_mfma_f32_16x16x32_bf16 v[164:167], a[200:203], v[80:83], v[164:167]\n" \
"v_exp_f32 v6, v6\n" \
"v_mfma_f32_16x16x32_bf16 v[164:167], a[204:207], v[84:87], v[164:167]\n" \
"v_mfma_f32_16x16x32_bf16 v[132:135], a[200:203], v[64:67], v[132:135]\n" \
"v_exp_f32 v7, v7\n" \
"v_mfma_f32_16x16x32_bf16 v[132:135], a[204:207], v[68:71], v[132:135]\n" \
"ds_read_b128 a[200:203], v232 offset:4096\n" \
"v_cvt_pk_bf16_f32 v3, v6, v7\n" \
"v_mfma_f32_16x16x32_bf16 v[136:139], a[208:211], v[64:67], v[136:139]\n" \
"v_exp_f32 v16, v16\n" \
"v_mfma_f32_16x16x32_bf16 v[136:139], a[212:215], v[68:71], v[136:139]\n" \
"v_mfma_f32_16x16x32_bf16 v[168:171], a[208:211], v[80:83], v[168:171]\n" \
"v_exp_f32 v17, v17\n" \
"v_mfma_f32_16x16x32_bf16 v[168:171], a[212:215], v[84:87], v[168:171]\n" \
"ds_read_b128 a[204:207], v232 offset:6144\n" \
"v_cvt_pk_bf16_f32 v16, v16, v17\n" \
"v_mfma_f32_16x16x32_bf16 a[72:75], a[208:211], v[96:99], a[72:75]\n" \
"v_exp_f32 v18, v18\n" \
"v_mfma_f32_16x16x32_bf16 a[72:75], a[212:215], v[100:103], a[72:75]\n" \
"v_mfma_f32_16x16x32_bf16 a[104:107], a[208:211], v[112:115], a[104:107]\n" \
"v_exp_f32 v19, v19\n" \
"v_mfma_f32_16x16x32_bf16 a[104:107], a[212:215], v[116:119], a[104:107]\n" \
"ds_read_b128 a[208:211], v232 offset:256\n" \
"v_cvt_pk_bf16_f32 v17, v18, v19\n" \
"v_mfma_f32_16x16x32_bf16 a[108:111], a[216:219], v[112:115], a[108:111]\n" \
"v_exp_f32 v20, v20\n" \
"v_mfma_f32_16x16x32_bf16 a[108:111], a[220:223], v[116:119], a[108:111]\n" \
"v_mfma_f32_16x16x32_bf16 a[76:79], a[216:219], v[96:99], a[76:79]\n" \
"v_exp_f32 v21, v21\n" \
"v_mfma_f32_16x16x32_bf16 a[76:79], a[220:223], v[100:103], a[76:79]\n" \
"ds_read_b128 a[212:215], v232 offset:2304\n" \
"v_cvt_pk_bf16_f32 v18, v20, v21\n" \
"v_smfmac_f32_16x16x64_bf16 a[52:55], v[244:247], v[80:87], v243\n" \
"v_mfma_f32_16x16x32_bf16 v[172:175], a[216:219], v[80:83], v[172:175]\n" \
"v_exp_f32 v22, v22\n" \
"v_mfma_f32_16x16x32_bf16 v[172:175], a[220:223], v[84:87], v[172:175]\n" \
"v_mfma_f32_16x16x32_bf16 v[140:143], a[216:219], v[64:67], v[140:143]\n" \
"s_waitcnt lgkmcnt(10)\n" \
"v_exp_f32 v23, v23\n" \
"v_mfma_f32_16x16x32_bf16 v[140:143], a[220:223], v[68:71], v[140:143]\n" \
"ds_read_b128 a[216:219], v232 offset:4352\n" \
"v_cvt_pk_bf16_f32 v19, v22, v23\n" \
"v_mfma_f32_16x16x32_bf16 v[144:147], a[224:227], v[64:67], v[144:147]\n" \
"v_exp_f32 v32, v32\n" \
"v_mfma_f32_16x16x32_bf16 v[144:147], a[228:231], v[68:71], v[144:147]\n" \
"v_mfma_f32_16x16x32_bf16 v[176:179], a[224:227], v[80:83], v[176:179]\n" \
"v_exp_f32 v33, v33\n" \
"v_mfma_f32_16x16x32_bf16 v[176:179], a[228:231], v[84:87], v[176:179]\n" \
"ds_read_b128 a[220:223], v232 offset:6400\n" \
"v_cvt_pk_bf16_f32 v32, v32, v33\n" \
"v_mfma_f32_16x16x32_bf16 a[80:83], a[224:227], v[96:99], a[80:83]\n" \
"v_exp_f32 v34, v34\n" \
"v_mfma_f32_16x16x32_bf16 a[80:83], a[228:231], v[100:103], a[80:83]\n" \
"v_mfma_f32_16x16x32_bf16 a[112:115], a[224:227], v[112:115], a[112:115]\n" \
"v_exp_f32 v35, v35\n" \
"v_mfma_f32_16x16x32_bf16 a[112:115], a[228:231], v[116:119], a[112:115]\n" \
"ds_read_b128 a[224:227], v232 offset:8192\n" \
"v_cvt_pk_bf16_f32 v33, v34, v35\n" \
"v_mfma_f32_16x16x32_bf16 a[116:119], a[232:235], v[112:115], a[116:119]\n" \
"v_exp_f32 v36, v36\n" \
"v_mfma_f32_16x16x32_bf16 a[116:119], a[236:239], v[116:119], a[116:119]\n" \
"v_mfma_f32_16x16x32_bf16 a[84:87], a[232:235], v[96:99], a[84:87]\n" \
"v_exp_f32 v37, v37\n" \
"v_mfma_f32_16x16x32_bf16 a[84:87], a[236:239], v[100:103], a[84:87]\n" \
"ds_read_b128 a[228:231], v232 offset:10240\n" \
"v_cvt_pk_bf16_f32 v34, v36, v37\n" \
"v_smfmac_f32_16x16x64_bf16 v[224:227], v[244:247], v[96:103], v243\n" \
"v_mfma_f32_16x16x32_bf16 v[180:183], a[232:235], v[80:83], v[180:183]\n" \
"v_exp_f32 v38, v38\n" \
"v_mfma_f32_16x16x32_bf16 v[180:183], a[236:239], v[84:87], v[180:183]\n" \
"v_mfma_f32_16x16x32_bf16 v[148:151], a[232:235], v[64:67], v[148:151]\n" \
"v_exp_f32 v39, v39\n" \
"v_mfma_f32_16x16x32_bf16 v[148:151], a[236:239], v[68:71], v[148:151]\n" \
"ds_read_b128 a[232:235], v232 offset:12288\n" \
"v_cvt_pk_bf16_f32 v35, v38, v39\n" \
"v_mfma_f32_16x16x32_bf16 v[152:155], a[32:35], v[64:67], v[152:155]\n" \
"v_exp_f32 v48, v48\n" \
"v_mfma_f32_16x16x32_bf16 v[152:155], a[36:39], v[68:71], v[152:155]\n" \
"v_mfma_f32_16x16x32_bf16 v[184:187], a[32:35], v[80:83], v[184:187]\n" \
"v_exp_f32 v49, v49\n" \
"v_mfma_f32_16x16x32_bf16 v[184:187], a[36:39], v[84:87], v[184:187]\n" \
"ds_read_b128 a[236:239], v232 offset:14336\n" \
"v_cvt_pk_bf16_f32 v48, v48, v49\n" \
"v_mfma_f32_16x16x32_bf16 a[88:91], a[32:35], v[96:99], a[88:91]\n" \
"v_exp_f32 v50, v50\n" \
"v_mfma_f32_16x16x32_bf16 a[88:91], a[36:39], v[100:103], a[88:91]\n" \
"v_mfma_f32_16x16x32_bf16 a[120:123], a[32:35], v[112:115], a[120:123]\n" \
"v_exp_f32 v51, v51\n" \
"v_mfma_f32_16x16x32_bf16 a[120:123], a[36:39], v[116:119], a[120:123]\n" \
"v_cvt_pk_bf16_f32 v49, v50, v51\n" \
"v_mfma_f32_16x16x32_bf16 a[124:127], a[40:43], v[112:115], a[124:127]\n" \
"v_exp_f32 v52, v52\n" \
"v_mfma_f32_16x16x32_bf16 a[124:127], a[44:47], v[116:119], a[124:127]\n" \
"v_mfma_f32_16x16x32_bf16 a[92:95], a[40:43], v[96:99], a[92:95]\n" \
"v_exp_f32 v53, v53\n" \
"v_mfma_f32_16x16x32_bf16 a[92:95], a[44:47], v[100:103], a[92:95]\n" \
"v_cvt_pk_bf16_f32 v50, v52, v53\n" \
"v_smfmac_f32_16x16x64_bf16 v[228:231], v[244:247], v[112:119], v243\n" \
"v_mfma_f32_16x16x32_bf16 v[188:191], a[40:43], v[80:83], v[188:191]\n" \
"v_exp_f32 v54, v54\n" \
"v_mfma_f32_16x16x32_bf16 v[188:191], a[44:47], v[84:87], v[188:191]\n" \
"v_mfma_f32_16x16x32_bf16 v[156:159], a[40:43], v[64:67], v[156:159]\n" \
"s_waitcnt vmcnt(1) lgkmcnt(0)\n" \
"v_exp_f32 v55, v55\n" \
"v_mfma_f32_16x16x32_bf16 v[156:159], a[44:47], v[68:71], v[156:159]\n" \
"s_barrier\n" \
"v_cvt_pk_bf16_f32 v51, v54, v55\n" \
"v_mfma_f32_16x16x32_bf16 v[64:67], a[192:195], a[128:131], v[196:199]\n" \
"v_exp_f32 v8, v8\n" \
"v_mfma_f32_16x16x32_bf16 v[64:67], a[196:199], a[132:135], v[64:67]\n" \
"v_cmp_eq_u32 vcc, s60, v239\n" \
"v_mfma_f32_16x16x32_bf16 v[64:67], a[200:203], a[136:139], v[64:67]\n" \
"v_exp_f32_e64 v9, v9\n" \
"v_mfma_f32_16x16x32_bf16 v[64:67], a[204:207], a[140:143], v[64:67]\n" \
"s_andn2_b32 s70, s69, vcc_lo\n" \
"s_cbranch_scc1 Lpl2_%=\n" \
"Lpr2_%=:\n" \
"ds_read_b128 a[32:35], v233 offset:6144\n" \
"v_cvt_pk_bf16_f32 v4, v8, v9\n" \
"v_mfma_f32_16x16x32_bf16 v[80:83], a[204:207], a[156:159], v[196:199]\n" \
"s_add_u32 m0, s77, s80\n" \
"v_exp_f32 v10, v10\n" \
"v_mfma_f32_16x16x32_bf16 v[80:83], a[200:203], a[152:155], v[80:83]\n" \
"buffer_load_dwordx4 v234, s[40:43], s76 offen offset:0 sc1 lds\n" \
"v_mfma_f32_16x16x32_bf16 v[80:83], a[196:199], a[148:151], v[80:83]\n" \
"s_sub_u32 s89, s89, 1\n" \
"v_exp_f32 v11, v11\n" \
"v_mfma_f32_16x16x32_bf16 v[80:83], a[192:195], a[144:147], v[80:83]\n" \
"s_cmp_eq_u32 s89, 0\n" \
"s_cselect_b32 s70, s78, 0xc000\n" \
"ds_read_b128 a[36:39], v233 offset:14336\n" \
"v_cvt_pk_bf16_f32 v5, v10, v11\n" \
"v_mfma_f32_16x16x32_bf16 v[96:99], a[192:195], a[160:163], v[196:199]\n" \
"s_add_u32 s71, s76, 17408\n" \
"v_exp_f32 v12, v12\n" \
"v_mfma_f32_16x16x32_bf16 v[96:99], a[196:199], a[164:167], v[96:99]\n" \
"buffer_load_dwordx4 v234, s[40:43], s76 offen offset:1024 sc1 lds\n" \
"v_mfma_f32_16x16x32_bf16 v[96:99], a[200:203], a[168:171], v[96:99]\n" \
"v_exp_f32 v13, v13\n" \
"v_mfma_f32_16x16x32_bf16 v[96:99], a[204:207], a[172:175], v[96:99]\n" \
"ds_read_b128 a[40:43], v233 offset:7168\n" \
"v_cvt_pk_bf16_f32 v6, v12, v13\n" \
"v_mfma_f32_16x16x32_bf16 v[112:115], a[204:207], a[188:191], v[196:199]\n" \
"v_exp_f32 v14, v14\n" \
"v_mfma_f32_16x16x32_bf16 v[112:115], a[200:203], a[184:187], v[112:115]\n" \
"v_xor_b32 v232, s70, v232\n" \
"v_mfma_f32_16x16x32_bf16 v[112:115], a[196:199], a[180:183], v[112:115]\n" \
"v_exp_f32 v15, v15\n" \
"v_mfma_f32_16x16x32_bf16 v[112:115], a[192:195], a[176:179], v[112:115]\n" \
"ds_read_b128 a[204:207], v233 offset:9216\n" \
"v_cvt_pk_bf16_f32 v7, v14, v15\n" \
"v_mfma_f32_16x16x32_bf16 v[116:119], a[208:211], a[176:179], v[196:199]\n" \
"v_exp_f32 v24, v24\n" \
"v_mfma_f32_16x16x32_bf16 v[116:119], a[212:215], a[180:183], v[116:119]\n" \
"buffer_load_dwordx4 v234, s[40:43], s76 offen offset:2048 sc1 lds\n" \
"v_mfma_f32_16x16x32_bf16 v[116:119], a[216:219], a[184:187], v[116:119]\n" \
"v_exp_f32 v25, v25\n" \
"v_mfma_f32_16x16x32_bf16 v[116:119], a[220:223], a[188:191], v[116:119]\n" \
"ds_read_b128 a[200:203], v233 offset:1024\n" \
"v_cvt_pk_bf16_f32 v20, v24, v25\n" \
"v_mfma_f32_16x16x32_bf16 v[100:103], a[220:223], a[172:175], v[196:199]\n" \
"v_exp_f32 v26, v26\n" \
"v_mfma_f32_16x16x32_bf16 v[100:103], a[216:219], a[168:171], v[100:103]\n" \
"buffer_load_dwordx4 v234, s[40:43], s76 offen offset:3072 sc1 lds\n" \
"v_mfma_f32_16x16x32_bf16 v[100:103], a[212:215], a[164:167], v[100:103]\n" \
"v_exp_f32 v27, v27\n" \
"v_mfma_f32_16x16x32_bf16 v[100:103], a[208:211], a[160:163], v[100:103]\n" \
"ds_read_b128 a[196:199], v233 offset:8192\n" \
"v_cvt_pk_bf16_f32 v21, v26, v27\n" \
"v_mfma_f32_16x16x32_bf16 v[84:87], a[208:211], a[144:147], v[196:199]\n" \
"v_exp_f32 v28, v28\n" \
"v_mfma_f32_16x16x32_bf16 v[84:87], a[212:215], a[148:151], v[84:87]\n" \
"v_mfma_f32_16x16x32_bf16 v[84:87], a[216:219], a[152:155], v[84:87]\n" \
"v_exp_f32 v29, v29\n" \
"v_mfma_f32_16x16x32_bf16 v[84:87], a[220:223], a[156:159], v[84:87]\n" \
"ds_read_b128 a[192:195], v233 offset:0\n" \
"v_cvt_pk_bf16_f32 v22, v28, v29\n" \
"v_mfma_f32_16x16x32_bf16 v[68:71], a[220:223], a[140:143], v[196:199]\n" \
"s_add_u32 m0, m0, 0x4000\n" \
"v_exp_f32 v30, v30\n" \
"v_mfma_f32_16x16x32_bf16 v[68:71], a[216:219], a[136:139], v[68:71]\n" \
"buffer_load_dwordx4 v234, s[40:43], s71 offen offset:0 sc1 lds\n" \
"v_mfma_f32_16x16x32_bf16 v[68:71], a[212:215], a[132:135], v[68:71]\n" \
"v_exp_f32 v31, v31\n" \
"v_mfma_f32_16x16x32_bf16 v[68:71], a[208:211], a[128:131], v[68:71]\n" \
"ds_read_b128 a[220:223], v233 offset:11264\n" \
"v_cvt_pk_bf16_f32 v23, v30, v31\n" \
"v_mfma_f32_16x16x32_bf16 v[72:75], a[224:227], a[128:131], v[196:199]\n" \
"v_exp_f32 v40, v40\n" \
"v_mfma_f32_16x16x32_bf16 v[72:75], a[228:231], a[132:135], v[72:75]\n" \
"buffer_load_dwordx4 v234, s[40:43], s71 offen offset:1024 sc1 lds\n" \
"v_mfma_f32_16x16x32_bf16 v[72:75], a[232:235], a[136:139], v[72:75]\n" \
"v_exp_f32 v41, v41\n" \
"v_mfma_f32_16x16x32_bf16 v[72:75], a[236:239], a[140:143], v[72:75]\n" \
"ds_read_b128 a[216:219], v233 offset:3072\n" \
"v_cvt_pk_bf16_f32 v36, v40, v41\n" \
"v_mfma_f32_16x16x32_bf16 v[88:91], a[236:239], a[156:159], v[196:199]\n" \
"v_exp_f32 v42, v42\n" \
"v_mfma_f32_16x16x32_bf16 v[88:91], a[232:235], a[152:155], v[88:91]\n" \
"v_mfma_f32_16x16x32_bf16 v[88:91], a[228:231], a[148:151], v[88:91]\n" \
"v_exp_f32 v43, v43\n" \
"v_mfma_f32_16x16x32_bf16 v[88:91], a[224:227], a[144:147], v[88:91]\n" \
"ds_read_b128 a[212:215], v233 offset:10240\n" \
"v_cvt_pk_bf16_f32 v37, v42, v43\n" \
"v_mfma_f32_16x16x32_bf16 v[104:107], a[224:227], a[160:163], v[196:199]\n" \
"v_exp_f32 v44, v44\n" \
"v_mfma_f32_16x16x32_bf16 v[104:107], a[228:231], a[164:167], v[104:107]\n" \
"buffer_load_dwordx4 v234, s[40:43], s71 offen offset:2048 sc1 lds\n" \
"v_mfma_f32_16x16x32_bf16 v[104:107], a[232:235], a[168:171], v[104:107]\n" \
"v_exp_f32 v45, v45\n" \
"v_mfma_f32_16x16x32_bf16 v[104:107], a[236:239], a[172:175], v[104:107]\n" \
"ds_read_b128 a[208:211], v233 offset:2048\n" \
"v_cvt_pk_bf16_f32 v38, v44, v45\n" \
"v_mfma_f32_16x16x32_bf16 v[120:123], a[236:239], a[188:191], v[196:199]\n" \
"v_exp_f32 v46, v46\n" \
"v_mfma_f32_16x16x32_bf16 v[120:123], a[232:235], a[184:187], v[120:123]\n" \
"buffer_load_dwordx4 v234, s[40:43], s71 offen offset:3072 sc1 lds\n" \
"v_mfma_f32_16x16x32_bf16 v[120:123], a[228:231], a[180:183], v[120:123]\n" \
"v_exp_f32 v47, v47\n" \
"v_mfma_f32_16x16x32_bf16 v[120:123], a[224:227], a[176:179], v[120:123]\n" \
"ds_read_b128 a[236:239], v233 offset:13312\n" \
"v_cvt_pk_bf16_f32 v39, v46, v47\n" \
"v_mfma_f32_16x16x32_bf16 v[124:127], a[240:243], a[176:179], v[196:199]\n" \
"v_exp_f32 v56, v56\n" \
"v_mfma_f32_16x16x32_bf16 v[124:127], a[244:247], a[180:183], v[124:127]\n" \
"v_mfma_f32_16x16x32_bf16 v[124:127], a[248:251], a[184:187], v[124:127]\n" \
"v_exp_f32 v57, v57\n" \
"v_mfma_f32_16x16x32_bf16 v[124:127], a[252:255], a[188:191], v[124:127]\n" \
"ds_read_b128 a[232:235], v233 offset:5120\n" \
"v_cvt_pk_bf16_f32 v52, v56, v57\n" \
"v_mfma_f32_16x16x32_bf16 v[108:111], a[252:255], a[172:175], v[196:199]\n" \
"v_exp_f32 v58, v58\n" \
"v_mfma_f32_16x16x32_bf16 v[108:111], a[248:251], a[168:171], v[108:111]\n" \
"v_mfma_f32_16x16x32_bf16 v[108:111], a[244:247], a[164:167], v[108:111]\n" \
"v_exp_f32 v59, v59\n" \
"v_mfma_f32_16x16x32_bf16 v[108:111], a[240:243], a[160:163], v[108:111]\n" \
"ds_read_b128 a[228:231], v233 offset:12288\n" \
"v_cvt_pk_bf16_f32 v53, v58, v59\n" \
"v_mfma_f32_16x16x32_bf16 v[92:95], a[240:243], a[144:147], v[196:199]\n" \
"v_exp_f32 v60, v60\n" \
"v_mfma_f32_16x16x32_bf16 v[92:95], a[244:247], a[148:151], v[92:95]\n" \
"v_mfma_f32_16x16x32_bf16 v[92:95], a[248:251], a[152:155], v[92:95]\n" \
"v_exp_f32 v61, v61\n" \
"v_mfma_f32_16x16x32_bf16 v[92:95], a[252:255], a[156:159], v[92:95]\n" \
"ds_read_b128 a[224:227], v233 offset:4096\n" \
"v_cvt_pk_bf16_f32 v54, v60, v61\n" \
"v_mfma_f32_16x16x32_bf16 v[76:79], a[252:255], a[140:143], v[196:199]\n" \
"v_exp_f32 v62, v62\n" \
"v_mfma_f32_16x16x32_bf16 v[76:79], a[248:251], a[136:139], v[76:79]\n" \
"v_mfma_f32_16x16x32_bf16 v[76:79], a[244:247], a[132:135], v[76:79]\n" \
"s_waitcnt lgkmcnt(2)\n" \
"v_exp_f32 v63, v63\n" \
"v_mfma_f32_16x16x32_bf16 v[76:79], a[240:243], a[128:131], v[76:79]\n" \
"ds_read_b128 a[44:47], v233 offset:15360\n" \
"v_cvt_pk_bf16_f32 v55, v62, v63\n" \
"v_mfma_f32_16x16x32_bf16 v[128:131], a[192:195], v[0:3], v[128:131]\n" \
"s_add_u32 m0, s74, s79\n" \
"v_exp_f32 v64, v64\n" \
"v_mfma_f32_16x16x32_bf16 v[128:131], a[196:199], v[4:7], v[128:131]\n" \
"buffer_load_dwordx4 v234, s[36:39], s73 offen offset:0 sc1 lds\n" \
"v_mfma_f32_16x16x32_bf16 v[160:163], a[192:195], v[16:19], v[160:163]\n" \
"v_exp_f32 v65, v65\n" \
"v_mfma_f32_16x16x32_bf16 v[160:163], a[196:199], v[20:23], v[160:163]\n" \
"ds_read_b128 a[240:243], v232 offset:8448\n" \
"v_cvt_pk_bf16_f32 v64, v64, v65\n" \
"v_mfma_f32_16x16x32_bf16 a[64:67], a[192:195], v[32:35], a[64:67]\n" \
"s_add_u32 s71, s73, 17408\n" \
"v_exp_f32 v66, v66\n" \
"v_mfma_f32_16x16x32_bf16 a[64:67], a[196:199], v[36:39], a[64:67]\n" \
"buffer_load_dwordx4 v234, s[36:39], s73 offen offset:1024 sc1 lds\n" \
"v_mfma_f32_16x16x32_bf16 a[96:99], a[192:195], v[48:51], a[96:99]\n" \
"v_exp_f32 v67, v67\n" \
"v_mfma_f32_16x16x32_bf16 a[96:99], a[196:199], v[52:55], a[96:99]\n" \
"ds_read_b128 a[192:195], v232 offset:0\n" \
"v_cvt_pk_bf16_f32 v65, v66, v67\n" \
"v_mfma_f32_16x16x32_bf16 a[100:103], a[200:203], v[48:51], a[100:103]\n" \
"v_exp_f32 v68, v68\n" \
"v_mfma_f32_16x16x32_bf16 a[100:103], a[204:207], v[52:55], a[100:103]\n" \
"v_xor_b32 v233, 0x4000, v233\n" \
"v_mfma_f32_16x16x32_bf16 a[68:71], a[200:203], v[32:35], a[68:71]\n" \
"v_exp_f32 v69, v69\n" \
"v_mfma_f32_16x16x32_bf16 a[68:71], a[204:207], v[36:39], a[68:71]\n" \
"ds_read_b128 a[196:199], v232 offset:2048\n" \
"v_cvt_pk_bf16_f32 v66, v68, v69\n" \
"v_smfmac_f32_16x16x64_bf16 a[48:51], v[244:247], v[0:7], v243\n" \
"buffer_load_dwordx4 v234, s[36:39], s73 offen offset:2048 sc1 lds\n" \
"v_mfma_f32_16x16x32_bf16 v[164:167], a[200:203], v[16:19], v[164:167]\n" \
"v_exp_f32 v70, v70\n" \
"v_mfma_f32_16x16x32_bf16 v[164:167], a[204:207], v[20:23], v[164:167]\n" \
"v_mfma_f32_16x16x32_bf16 v[132:135], a[200:203], v[0:3], v[132:135]\n" \
"v_exp_f32 v71, v71\n" \
"v_mfma_f32_16x16x32_bf16 v[132:135], a[204:207], v[4:7], v[132:135]\n" \
"ds_read_b128 a[200:203], v232 offset:4096\n" \
"v_cvt_pk_bf16_f32 v67, v70, v71\n" \
"v_mfma_f32_16x16x32_bf16 v[136:139], a[208:211], v[0:3], v[136:139]\n" \
"v_exp_f32 v80, v80\n" \
"v_mfma_f32_16x16x32_bf16 v[136:139], a[212:215], v[4:7], v[136:139]\n" \
"buffer_load_dwordx4 v234, s[36:39], s73 offen offset:3072 sc1 lds\n" \
"v_mfma_f32_16x16x32_bf16 v[168:171], a[208:211], v[16:19], v[168:171]\n" \
"v_exp_f32 v81, v81\n" \
"v_mfma_f32_16x16x32_bf16 v[168:171], a[212:215], v[20:23], v[168:171]\n" \
"ds_read_b128 a[204:207], v232 offset:6144\n" \
"v_cvt_pk_bf16_f32 v80, v80, v81\n" \
"v_mfma_f32_16x16x32_bf16 a[72:75], a[208:211], v[32:35], a[72:75]\n" \
"v_exp_f32 v82, v82\n" \
"v_mfma_f32_16x16x32_bf16 a[72:75], a[212:215], v[36:39], a[72:75]\n" \
"v_mfma_f32_16x16x32_bf16 a[104:107], a[208:211], v[48:51], a[104:107]\n" \
"v_exp_f32 v83, v83\n" \
"v_mfma_f32_16x16x32_bf16 a[104:107], a[212:215], v[52:55], a[104:107]\n" \
"ds_read_b128 a[208:211], v232 offset:256\n" \
"v_cvt_pk_bf16_f32 v81, v82, v83\n" \
"v_mfma_f32_16x16x32_bf16 a[108:111], a[216:219], v[48:51], a[108:111]\n" \
"s_add_u32 m0, m0, 0x4000\n" \
"v_exp_f32 v84, v84\n" \
"v_mfma_f32_16x16x32_bf16 a[108:111], a[220:223], v[52:55], a[108:111]\n" \
"buffer_load_dwordx4 v234, s[36:39], s71 offen offset:0 sc1 lds\n" \
"v_mfma_f32_16x16x32_bf16 a[76:79], a[216:219], v[32:35], a[76:79]\n" \
"v_exp_f32 v85, v85\n" \
"v_mfma_f32_16x16x32_bf16 a[76:79], a[220:223], v[36:39], a[76:79]\n" \
"ds_read_b128 a[212:215], v232 offset:2304\n" \
"v_cvt_pk_bf16_f32 v82, v84, v85\n" \
"v_smfmac_f32_16x16x64_bf16 a[52:55], v[244:247], v[16:23], v243\n" \
"v_mfma_f32_16x16x32_bf16 v[172:175], a[216:219], v[16:19], v[172:175]\n" \
"v_exp_f32 v86, v86\n" \
"v_mfma_f32_16x16x32_bf16 v[172:175], a[220:223], v[20:23], v[172:175]\n" \
"buffer_load_dwordx4 v234, s[36:39], s71 offen offset:1024 sc1 lds\n" \
"v_mfma_f32_16x16x32_bf16 v[140:143], a[216:219], v[0:3], v[140:143]\n" \
"s_waitcnt lgkmcnt(7)\n" \
"v_exp_f32 v87, v87\n" \
"v_mfma_f32_16x16x32_bf16 v[140:143], a[220:223], v[4:7], v[140:143]\n" \
"ds_read_b128 a[216:219], v232 offset:4352\n" \
"v_cvt_pk_bf16_f32 v83, v86, v87\n" \
"v_mfma_f32_16x16x32_bf16 v[144:147], a[224:227], v[0:3], v[144:147]\n" \
"v_exp_f32 v96, v96\n" \
"v_mfma_f32_16x16x32_bf16 v[144:147], a[228:231], v[4:7], v[144:147]\n" \
"buffer_load_dwordx4 v234, s[36:39], s71 offen offset:2048 sc1 lds\n" \
"v_mfma_f32_16x16x32_bf16 v[176:179], a[224:227], v[16:19], v[176:179]\n" \
"v_exp_f32 v97, v97\n" \
"v_mfma_f32_16x16x32_bf16 v[176:179], a[228:231], v[20:23], v[176:179]\n" \
"ds_read_b128 a[220:223], v232 offset:6400\n" \
"v_cvt_pk_bf16_f32 v96, v96, v97\n" \
"v_mfma_f32_16x16x32_bf16 a[80:83], a[224:227], v[32:35], a[80:83]\n" \
"v_exp_f32 v98, v98\n" \
"v_mfma_f32_16x16x32_bf16 a[80:83], a[228:231], v[36:39], a[80:83]\n" \
"v_mfma_f32_16x16x32_bf16 a[112:115], a[224:227], v[48:51], a[112:115]\n" \
"v_exp_f32 v99, v99\n" \
"v_mfma_f32_16x16x32_bf16 a[112:115], a[228:231], v[52:55], a[112:115]\n" \
"ds_read_b128 a[224:227], v232 offset:8192\n" \
"v_cvt_pk_bf16_f32 v97, v98, v99\n" \
"v_mfma_f32_16x16x32_bf16 a[116:119], a[232:235], v[48:51], a[116:119]\n" \
"v_exp_f32 v100, v100\n" \
"v_mfma_f32_16x16x32_bf16 a[116:119], a[236:239], v[52:55], a[116:119]\n" \
"buffer_load_dwordx4 v234, s[36:39], s71 offen offset:3072 sc1 lds\n" \
"v_mfma_f32_16x16x32_bf16 a[84:87], a[232:235], v[32:35], a[84:87]\n" \
"v_exp_f32 v101, v101\n" \
"v_mfma_f32_16x16x32_bf16 a[84:87], a[236:239], v[36:39], a[84:87]\n" \
"ds_read_b128 a[228:231], v232 offset:10240\n" \
"v_cvt_pk_bf16_f32 v98, v100, v101\n" \
"v_smfmac_f32_16x16x64_bf16 v[224:227], v[244:247], v[32:39], v243\n" \
"v_mfma_f32_16x16x32_bf16 v[180:183], a[232:235], v[16:19], v[180:183]\n" \
"v_exp_f32 v102, v102\n" \
"v_mfma_f32_16x16x32_bf16 v[180:183], a[236:239], v[20:23], v[180:183]\n" \
"buffer_load_dword v239, v242, s[56:59], s95 offen sc1\n" \
"v_mfma_f32_16x16x32_bf16 v[148:151], a[232:235], v[0:3], v[148:151]\n" \
"s_mov_b32 s69, s92\n" \
"v_exp_f32 v103, v103\n" \
"v_mfma_f32_16x16x32_bf16 v[148:151], a[236:239], v[4:7], v[148:151]\n" \
"s_mov_b32 s76, s73\n" \
"ds_read_b128 a[232:235], v232 offset:12288\n" \
"v_cvt_pk_bf16_f32 v99, v102, v103\n" \
"v_mfma_f32_16x16x32_bf16 v[152:155], a[32:35], v[0:3], v[152:155]\n" \
"s_add_u32 s73, s73, 34816\n" \
"v_exp_f32 v112, v112\n" \
"v_mfma_f32_16x16x32_bf16 v[152:155], a[36:39], v[4:7], v[152:155]\n" \
"s_cmp_eq_u32 s73, s83\n" \
"s_cselect_b32 s70, 34816, 0\n" \
"s_add_u32 s73, s73, s70\n" \
"v_mfma_f32_16x16x32_bf16 v[184:187], a[32:35], v[16:19], v[184:187]\n" \
"s_xor_b32 s74, s74, 0x8000\n" \
"v_exp_f32 v113, v113\n" \
"v_mfma_f32_16x16x32_bf16 v[184:187], a[36:39], v[20:23], v[184:187]\n" \
"s_xor_b32 s77, s77, 0x8000\n" \
"ds_read_b128 a[236:239], v232 offset:14336\n" \
"v_cvt_pk_bf16_f32 v112, v112, v113\n" \
"v_mfma_f32_16x16x32_bf16 a[88:91], a[32:35], v[32:35], a[88:91]\n" \
"v_exp_f32 v114, v114\n" \
"v_mfma_f32_16x16x32_bf16 a[88:91], a[36:39], v[36:39], a[88:91]\n" \
"v_mfma_f32_16x16x32_bf16 a[120:123], a[32:35], v[48:51], a[120:123]\n" \
"v_exp_f32 v115, v115\n" \
"v_mfma_f32_16x16x32_bf16 a[120:123], a[36:39], v[52:55], a[120:123]\n" \
"ds_read_b128 a[244:247], v232 offset:10496\n" \
"v_cvt_pk_bf16_f32 v113, v114, v115\n" \
"v_mfma_f32_16x16x32_bf16 a[124:127], a[40:43], v[48:51], a[124:127]\n" \
"v_exp_f32 v116, v116\n" \
"v_mfma_f32_16x16x32_bf16 a[124:127], a[44:47], v[52:55], a[124:127]\n" \
"v_mfma_f32_16x16x32_bf16 a[92:95], a[40:43], v[32:35], a[92:95]\n" \
"v_exp_f32 v117, v117\n" \
"v_mfma_f32_16x16x32_bf16 a[92:95], a[44:47], v[36:39], a[92:95]\n" \
"ds_read_b128 a[248:251], v232 offset:12544\n" \
"v_cvt_pk_bf16_f32 v114, v116, v117\n" \
"v_smfmac_f32_16x16x64_bf16 v[228:231], v[244:247], v[48:55], v243\n" \
"v_mfma_f32_16x16x32_bf16 v[188:191], a[40:43], v[16:19], v[188:191]\n" \
"v_exp_f32 v118, v118\n" \
"v_mfma_f32_16x16x32_bf16 v[188:191], a[44:47], v[20:23], v[188:191]\n" \
"s_cmp_lg_u32 s89, 0\n" \
"v_exp_f32 v119, v119\n" \
"v_mfma_f32_16x16x32_bf16 v[156:159], a[40:43], v[0:3], v[156:159]\n" \
"s_waitcnt lgkmcnt(2)\n" \
"ds_read_b128 a[252:255], v232 offset:14592\n" \
"v_cvt_pk_bf16_f32 v115, v118, v119\n" \
"v_mfma_f32_16x16x32_bf16 v[156:159], a[44:47], v[4:7], v[156:159]\n" \
"s_cbranch_scc1 Lloop_%=\n" \
"s_cmp_eq_u32 s63, 7\n" \
"s_cbranch_scc0 Lloop2_%=\n" \
"s_branch Lg_%=\n" \
"s_nop 0\n" \
"s_nop 0\n" \
"Lep_%=:\n" \
"Lpg6062_%=:\n" \
"s_cbranch_execz Lpg7379_%=\n" \
"s_nop 4\n" \
"v_accvgpr_read_b32 v92, a48\n" \
"v_accvgpr_read_b32 v93, a49\n" \
"v_accvgpr_read_b32 v94, a52\n" \
"v_accvgpr_read_b32 v95, a53\n" \
"v_accvgpr_write_b32 a48, 0\n" \
"v_accvgpr_write_b32 a49, 0\n" \
"v_accvgpr_write_b32 a52, 0\n" \
"v_accvgpr_write_b32 a53, 0\n" \
"v_add_f32 v86, v92, v93\n" \
"v_add_f32 v87, v94, v95\n" \
"v_rcp_f32 v84, v86\n" \
"v_rcp_f32 v85, v87\n" \
"v_add_u32 v240, 0x1000, v236\n" \
"v_add_f32 v80, v86, v87\n" \
"v_mul_f32 v80, 0x43800000, v80\n" \
"v_add_f32 v86, v84, v85\n" \
"v_fmac_f32 v80, 0x5f800000, v86\n" \
"v_mov_b32 v81, 0\n" \
"v_mov_b32 v82, 0\n" \
"v_mov_b32 v83, 0\n" \
"v_mov_b32 v88, 0\n" \
"v_mov_b32 v89, 0\n" \
"v_mov_b32 v90, 0\n" \
"v_mov_b32 v91, 0\n" \
"v_pk_mul_f32 v[128:129], v[128:129], v[84:85] op_sel:[0,0] op_sel_hi:[1,0]\n" \
"v_pk_mul_f32 v[130:131], v[130:131], v[84:85] op_sel:[0,0] op_sel_hi:[1,0]\n" \
"v_pk_mul_f32 v[132:133], v[132:133], v[84:85] op_sel:[0,0] op_sel_hi:[1,0]\n" \
"v_pk_mul_f32 v[134:135], v[134:135], v[84:85] op_sel:[0,0] op_sel_hi:[1,0]\n" \
"v_cvt_pk_bf16_f32 v64, v128, v129\n" \
"v_cvt_pk_bf16_f32 v65, v130, v131\n" \
"v_cvt_pk_bf16_f32 v66, v132, v133\n" \
"v_cvt_pk_bf16_f32 v67, v134, v135\n" \
"buffer_store_dwordx4 v[64:67], v236, s[56:59], s87 offen offset:0 sc1\n" \
"v_pk_mul_f32 v[136:137], v[136:137], v[84:85] op_sel:[0,0] op_sel_hi:[1,0]\n" \
"v_pk_mul_f32 v[138:139], v[138:139], v[84:85] op_sel:[0,0] op_sel_hi:[1,0]\n" \
"v_pk_mul_f32 v[140:141], v[140:141], v[84:85] op_sel:[0,0] op_sel_hi:[1,0]\n" \
"v_pk_mul_f32 v[142:143], v[142:143], v[84:85] op_sel:[0,0] op_sel_hi:[1,0]\n" \
"v_cvt_pk_bf16_f32 v68, v136, v137\n" \
"v_cvt_pk_bf16_f32 v69, v138, v139\n" \
"v_cvt_pk_bf16_f32 v70, v140, v141\n" \
"v_cvt_pk_bf16_f32 v71, v142, v143\n" \
"buffer_store_dwordx4 v[68:71], v236, s[56:59], s87 offen offset:2048 sc1\n" \
"v_pk_mul_f32 v[144:145], v[144:145], v[84:85] op_sel:[0,0] op_sel_hi:[1,0]\n" \
"v_pk_mul_f32 v[146:147], v[146:147], v[84:85] op_sel:[0,0] op_sel_hi:[1,0]\n" \
"v_pk_mul_f32 v[148:149], v[148:149], v[84:85] op_sel:[0,0] op_sel_hi:[1,0]\n" \
"v_pk_mul_f32 v[150:151], v[150:151], v[84:85] op_sel:[0,0] op_sel_hi:[1,0]\n" \
"v_cvt_pk_bf16_f32 v72, v144, v145\n" \
"v_cvt_pk_bf16_f32 v73, v146, v147\n" \
"v_cvt_pk_bf16_f32 v74, v148, v149\n" \
"v_cvt_pk_bf16_f32 v75, v150, v151\n" \
"buffer_store_dwordx4 v[72:75], v240, s[56:59], s87 offen offset:0 sc1\n" \
"v_pk_mul_f32 v[152:153], v[152:153], v[84:85] op_sel:[0,0] op_sel_hi:[1,0]\n" \
"v_pk_mul_f32 v[154:155], v[154:155], v[84:85] op_sel:[0,0] op_sel_hi:[1,0]\n" \
"v_pk_mul_f32 v[156:157], v[156:157], v[84:85] op_sel:[0,0] op_sel_hi:[1,0]\n" \
"v_pk_mul_f32 v[158:159], v[158:159], v[84:85] op_sel:[0,0] op_sel_hi:[1,0]\n" \
"v_cvt_pk_bf16_f32 v76, v152, v153\n" \
"v_cvt_pk_bf16_f32 v77, v154, v155\n" \
"v_cvt_pk_bf16_f32 v78, v156, v157\n" \
"v_cvt_pk_bf16_f32 v79, v158, v159\n" \
"buffer_store_dwordx4 v[76:79], v240, s[56:59], s87 offen offset:2048 sc1\n" \
"v_smfmac_f32_16x16x64_bf16 v[80:83], v[244:247], v[64:71], v243\n" \
"v_smfmac_f32_16x16x64_bf16 v[80:83], v[244:247], v[72:79], v243\n" \
"v_mfma_f32_32x32x16_bf16 v[128:143], v[88:91], v[88:91], 0\n" \
"v_mfma_f32_32x32x16_bf16 v[144:159], v[88:91], v[88:91], 0\n" \
"s_mov_b64 exec, 0\n" \
"s_cmp_eq_u32 s61, 0\n" \
"s_cbranch_scc1 Lst1x0_%=\n" \
"s_cmp_eq_u32 s61, 1\n" \
"s_cbranch_scc1 Lst1x1_%=\n" \
"s_cmp_eq_u32 s61, 2\n" \
"s_cbranch_scc1 Lst1x2_%=\n" \
"s_branch Lst1x3_%=\n" \
"Lcb1_%=:\n" \
"s_mov_b64 exec, -1\n" \
"s_add_u32 s72, s87, 532480\n" \
"v_pk_mul_f32 v[160:161], v[160:161], v[84:85] op_sel:[0,1] op_sel_hi:[1,1]\n" \
"v_pk_mul_f32 v[162:163], v[162:163], v[84:85] op_sel:[0,1] op_sel_hi:[1,1]\n" \
"v_pk_mul_f32 v[164:165], v[164:165], v[84:85] op_sel:[0,1] op_sel_hi:[1,1]\n" \
"v_pk_mul_f32 v[166:167], v[166:167], v[84:85] op_sel:[0,1] op_sel_hi:[1,1]\n" \
"v_cvt_pk_bf16_f32 v64, v160, v161\n" \
"v_cvt_pk_bf16_f32 v65, v162, v163\n" \
"v_cvt_pk_bf16_f32 v66, v164, v165\n" \
"v_cvt_pk_bf16_f32 v67, v166, v167\n" \
"buffer_store_dwordx4 v[64:67], v236, s[56:59], s72 offen offset:0 sc1\n" \
"v_pk_mul_f32 v[168:169], v[168:169], v[84:85] op_sel:[0,1] op_sel_hi:[1,1]\n" \
"v_pk_mul_f32 v[170:171], v[170:171], v[84:85] op_sel:[0,1] op_sel_hi:[1,1]\n" \
"v_pk_mul_f32 v[172:173], v[172:173], v[84:85] op_sel:[0,1] op_sel_hi:[1,1]\n" \
"v_pk_mul_f32 v[174:175], v[174:175], v[84:85] op_sel:[0,1] op_sel_hi:[1,1]\n" \
"v_cvt_pk_bf16_f32 v68, v168, v169\n" \
"v_cvt_pk_bf16_f32 v69, v170, v171\n" \
"v_cvt_pk_bf16_f32 v70, v172, v173\n" \
"v_cvt_pk_bf16_f32 v71, v174, v175\n" \
"buffer_store_dwordx4 v[68:71], v236, s[56:59], s72 offen offset:2048 sc1\n" \
"v_pk_mul_f32 v[176:177], v[176:177], v[84:85] op_sel:[0,1] op_sel_hi:[1,1]\n" \
"v_pk_mul_f32 v[178:179], v[178:179], v[84:85] op_sel:[0,1] op_sel_hi:[1,1]\n" \
"v_pk_mul_f32 v[180:181], v[180:181], v[84:85] op_sel:[0,1] op_sel_hi:[1,1]\n" \
"v_pk_mul_f32 v[182:183], v[182:183], v[84:85] op_sel:[0,1] op_sel_hi:[1,1]\n" \
"v_cvt_pk_bf16_f32 v72, v176, v177\n" \
"v_cvt_pk_bf16_f32 v73, v178, v179\n" \
"v_cvt_pk_bf16_f32 v74, v180, v181\n" \
"v_cvt_pk_bf16_f32 v75, v182, v183\n" \
"buffer_store_dwordx4 v[72:75], v240, s[56:59], s72 offen offset:0 sc1\n" \
"v_pk_mul_f32 v[184:185], v[184:185], v[84:85] op_sel:[0,1] op_sel_hi:[1,1]\n" \
"v_pk_mul_f32 v[186:187], v[186:187], v[84:85] op_sel:[0,1] op_sel_hi:[1,1]\n" \
"v_pk_mul_f32 v[188:189], v[188:189], v[84:85] op_sel:[0,1] op_sel_hi:[1,1]\n" \
"v_pk_mul_f32 v[190:191], v[190:191], v[84:85] op_sel:[0,1] op_sel_hi:[1,1]\n" \
"v_cvt_pk_bf16_f32 v76, v184, v185\n" \
"v_cvt_pk_bf16_f32 v77, v186, v187\n" \
"v_cvt_pk_bf16_f32 v78, v188, v189\n" \
"v_cvt_pk_bf16_f32 v79, v190, v191\n" \
"buffer_store_dwordx4 v[76:79], v240, s[56:59], s72 offen offset:2048 sc1\n" \
"v_smfmac_f32_16x16x64_bf16 v[80:83], v[244:247], v[64:71], v243\n" \
"v_smfmac_f32_16x16x64_bf16 v[80:83], v[244:247], v[72:79], v243\n" \
"v_mfma_f32_32x32x16_bf16 v[160:175], v[88:91], v[88:91], 0\n" \
"v_mfma_f32_32x32x16_bf16 v[176:191], v[88:91], v[88:91], 0\n" \
"s_nop 5\n" \
"v_add_f32 v80, v80, v81\n" \
"s_movk_i32 s64, 0x207\n" \
"v_cmp_class_f32 vcc, v80, s64\n" \
"s_nop 1\n" \
"s_cmp_lg_u64 vcc, 0\n" \
"s_addc_u32 s90, s90, 0\n" \
"s_waitcnt vmcnt(8)\n" \
"s_cmp_eq_u32 s63, 7\n" \
"s_cbranch_scc0 Lepr_%=\n" \
"s_branch Lepg_%=\n" \
".p2align 12\n" \
"Lloop2_%=:\n" \
"v_mfma_f32_16x16x32_bf16 v[0:3], a[192:195], a[0:3], v[196:199]\n" \
"Lpg6193_%=:\n" \
"s_cbranch_execz Lcb0_%=\n" \
"v_exp_f32 v72, v72\n" \
"v_mfma_f32_16x16x32_bf16 v[0:3], a[196:199], a[4:7], v[0:3]\n" \
"ds_read_b128 a[44:47], v233 offset:15360\n" \
"v_mfma_f32_16x16x32_bf16 v[0:3], a[200:203], a[8:11], v[0:3]\n" \
"v_exp_f32 v73, v73\n" \
"v_mfma_f32_16x16x32_bf16 v[0:3], a[204:207], a[12:15], v[0:3]\n" \
"ds_read_b128 a[32:35], v233 offset:6144\n" \
"v_cvt_pk_bf16_f32 v68, v72, v73\n" \
"v_mfma_f32_16x16x32_bf16 v[16:19], a[204:207], a[28:31], v[196:199]\n" \
"v_exp_f32 v74, v74\n" \
"v_mfma_f32_16x16x32_bf16 v[16:19], a[200:203], a[24:27], v[16:19]\n" \
"v_xor_b32 v232, 0x8000, v232\n" \
"v_mfma_f32_16x16x32_bf16 v[16:19], a[196:199], a[20:23], v[16:19]\n" \
"v_exp_f32 v75, v75\n" \
"v_mfma_f32_16x16x32_bf16 v[16:19], a[192:195], a[16:19], v[16:19]\n" \
"ds_read_b128 a[36:39], v233 offset:14336\n" \
"v_cvt_pk_bf16_f32 v69, v74, v75\n" \
"v_mfma_f32_16x16x32_bf16 v[32:35], a[192:195], a[160:163], v[196:199]\n" \
"v_exp_f32 v76, v76\n" \
"v_mfma_f32_16x16x32_bf16 v[32:35], a[196:199], a[164:167], v[32:35]\n" \
"v_mfma_f32_16x16x32_bf16 v[32:35], a[200:203], a[168:171], v[32:35]\n" \
"v_exp_f32 v77, v77\n" \
"v_mfma_f32_16x16x32_bf16 v[32:35], a[204:207], a[172:175], v[32:35]\n" \
"ds_read_b128 a[40:43], v233 offset:7168\n" \
"v_cvt_pk_bf16_f32 v70, v76, v77\n" \
"v_mfma_f32_16x16x32_bf16 v[48:51], a[204:207], a[188:191], v[196:199]\n" \
"v_exp_f32 v78, v78\n" \
"v_mfma_f32_16x16x32_bf16 v[48:51], a[200:203], a[184:187], v[48:51]\n" \
"v_mfma_f32_16x16x32_bf16 v[48:51], a[196:199], a[180:183], v[48:51]\n" \
"v_exp_f32 v79, v79\n" \
"v_mfma_f32_16x16x32_bf16 v[48:51], a[192:195], a[176:179], v[48:51]\n" \
"ds_read_b128 a[204:207], v233 offset:9216\n" \
"v_cvt_pk_bf16_f32 v71, v78, v79\n" \
"v_mfma_f32_16x16x32_bf16 v[52:55], a[208:211], a[176:179], v[196:199]\n" \
"v_exp_f32 v88, v88\n" \
"v_mfma_f32_16x16x32_bf16 v[52:55], a[212:215], a[180:183], v[52:55]\n" \
"v_mfma_f32_16x16x32_bf16 v[52:55], a[216:219], a[184:187], v[52:55]\n" \
"v_exp_f32 v89, v89\n" \
"v_mfma_f32_16x16x32_bf16 v[52:55], a[220:223], a[188:191], v[52:55]\n" \
"ds_read_b128 a[200:203], v233 offset:1024\n" \
"v_cvt_pk_bf16_f32 v84, v88, v89\n" \
"v_mfma_f32_16x16x32_bf16 v[36:39], a[220:223], a[172:175], v[196:199]\n" \
"v_exp_f32 v90, v90\n" \
"v_mfma_f32_16x16x32_bf16 v[36:39], a[216:219], a[168:171], v[36:39]\n" \
"v_mfma_f32_16x16x32_bf16 v[36:39], a[212:215], a[164:167], v[36:39]\n" \
"v_exp_f32 v91, v91\n" \
"v_mfma_f32_16x16x32_bf16 v[36:39], a[208:211], a[160:163], v[36:39]\n" \
"ds_read_b128 a[196:199], v233 offset:8192\n" \
"v_cvt_pk_bf16_f32 v85, v90, v91\n" \
"v_mfma_f32_16x16x32_bf16 v[20:23], a[208:211], a[16:19], v[196:199]\n" \
"v_exp_f32 v92, v92\n" \
"v_mfma_f32_16x16x32_bf16 v[20:23], a[212:215], a[20:23], v[20:23]\n" \
"v_mfma_f32_16x16x32_bf16 v[20:23], a[216:219], a[24:27], v[20:23]\n" \
"v_exp_f32 v93, v93\n" \
"v_mfma_f32_16x16x32_bf16 v[20:23], a[220:223], a[28:31], v[20:23]\n" \
"ds_read_b128 a[192:195], v233 offset:0\n" \
"v_cvt_pk_bf16_f32 v86, v92, v93\n" \
"v_mfma_f32_16x16x32_bf16 v[4:7], a[220:223], a[12:15], v[196:199]\n" \
"v_exp_f32 v94, v94\n" \
"v_mfma_f32_16x16x32_bf16 v[4:7], a[216:219], a[8:11], v[4:7]\n" \
"v_mfma_f32_16x16x32_bf16 v[4:7], a[212:215], a[4:7], v[4:7]\n" \
"s_waitcnt lgkmcnt(8)\n" \
"v_exp_f32 v95, v95\n" \
"v_mfma_f32_16x16x32_bf16 v[4:7], a[208:211], a[0:3], v[4:7]\n" \
"ds_read_b128 a[220:223], v233 offset:11264\n" \
"v_cvt_pk_bf16_f32 v87, v94, v95\n" \
"v_mfma_f32_16x16x32_bf16 v[8:11], a[224:227], a[0:3], v[196:199]\n" \
"v_exp_f32 v104, v104\n" \
"v_mfma_f32_16x16x32_bf16 v[8:11], a[228:231], a[4:7], v[8:11]\n" \
"v_mfma_f32_16x16x32_bf16 v[8:11], a[232:235], a[8:11], v[8:11]\n" \
"v_exp_f32 v105, v105\n" \
"v_mfma_f32_16x16x32_bf16 v[8:11], a[236:239], a[12:15], v[8:11]\n" \
"ds_read_b128 a[216:219], v233 offset:3072\n" \
"v_cvt_pk_bf16_f32 v100, v104, v105\n" \
"v_mfma_f32_16x16x32_bf16 v[24:27], a[236:239], a[28:31], v[196:199]\n" \
"v_exp_f32 v106, v106\n" \
"v_mfma_f32_16x16x32_bf16 v[24:27], a[232:235], a[24:27], v[24:27]\n" \
"v_mfma_f32_16x16x32_bf16 v[24:27], a[228:231], a[20:23], v[24:27]\n" \
"v_exp_f32 v107, v107\n" \
"v_mfma_f32_16x16x32_bf16 v[24:27], a[224:227], a[16:19], v[24:27]\n" \
"ds_read_b128 a[212:215], v233 offset:10240\n" \
"v_cvt_pk_bf16_f32 v101, v106, v107\n" \
"v_mfma_f32_16x16x32_bf16 v[40:43], a[224:227], a[160:163], v[196:199]\n" \
"v_exp_f32 v108, v108\n" \
"v_mfma_f32_16x16x32_bf16 v[40:43], a[228:231], a[164:167], v[40:43]\n" \
"v_mfma_f32_16x16x32_bf16 v[40:43], a[232:235], a[168:171], v[40:43]\n" \
"v_exp_f32 v109, v109\n" \
"v_mfma_f32_16x16x32_bf16 v[40:43], a[236:239], a[172:175], v[40:43]\n" \
"ds_read_b128 a[208:211], v233 offset:2048\n" \
"v_cvt_pk_bf16_f32 v102, v108, v109\n" \
"v_mfma_f32_16x16x32_bf16 v[56:59], a[236:239], a[188:191], v[196:199]\n" \
"v_exp_f32 v110, v110\n" \
"v_mfma_f32_16x16x32_bf16 v[56:59], a[232:235], a[184:187], v[56:59]\n" \
"v_mfma_f32_16x16x32_bf16 v[56:59], a[228:231], a[180:183], v[56:59]\n" \
"v_exp_f32 v111, v111\n" \
"v_mfma_f32_16x16x32_bf16 v[56:59], a[224:227], a[176:179], v[56:59]\n" \
"ds_read_b128 a[236:239], v233 offset:13312\n" \
"v_cvt_pk_bf16_f32 v103, v110, v111\n" \
"v_mfma_f32_16x16x32_bf16 v[60:63], a[240:243], a[176:179], v[196:199]\n" \
"v_exp_f32 v120, v120\n" \
"v_mfma_f32_16x16x32_bf16 v[60:63], a[244:247], a[180:183], v[60:63]\n" \
"ds_read_b128 a[232:235], v233 offset:5120\n" \
"v_mfma_f32_16x16x32_bf16 v[60:63], a[248:251], a[184:187], v[60:63]\n" \
"v_exp_f32 v121, v121\n" \
"v_mfma_f32_16x16x32_bf16 v[60:63], a[252:255], a[188:191], v[60:63]\n" \
"ds_read_b128 a[228:231], v233 offset:12288\n" \
"v_cvt_pk_bf16_f32 v116, v120, v121\n" \
"v_mfma_f32_16x16x32_bf16 v[44:47], a[252:255], a[172:175], v[196:199]\n" \
"v_exp_f32 v122, v122\n" \
"v_mfma_f32_16x16x32_bf16 v[44:47], a[248:251], a[168:171], v[44:47]\n" \
"ds_read_b128 a[224:227], v233 offset:4096\n" \
"v_mfma_f32_16x16x32_bf16 v[44:47], a[244:247], a[164:167], v[44:47]\n" \
"v_exp_f32 v123, v123\n" \
"v_mfma_f32_16x16x32_bf16 v[44:47], a[240:243], a[160:163], v[44:47]\n" \
"v_cvt_pk_bf16_f32 v117, v122, v123\n" \
"v_mfma_f32_16x16x32_bf16 v[28:31], a[240:243], a[16:19], v[196:199]\n" \
"v_exp_f32 v124, v124\n" \
"v_mfma_f32_16x16x32_bf16 v[28:31], a[244:247], a[20:23], v[28:31]\n" \
"v_mfma_f32_16x16x32_bf16 v[28:31], a[248:251], a[24:27], v[28:31]\n" \
"v_exp_f32 v125, v125\n" \
"v_mfma_f32_16x16x32_bf16 v[28:31], a[252:255], a[28:31], v[28:31]\n" \
"v_cvt_pk_bf16_f32 v118, v124, v125\n" \
"v_mfma_f32_16x16x32_bf16 v[12:15], a[252:255], a[12:15], v[196:199]\n" \
"v_exp_f32 v126, v126\n" \
"v_mfma_f32_16x16x32_bf16 v[12:15], a[248:251], a[8:11], v[12:15]\n" \
"v_mfma_f32_16x16x32_bf16 v[12:15], a[244:247], a[4:7], v[12:15]\n" \
"s_waitcnt vmcnt(0) lgkmcnt(0)\n" \
"v_exp_f32 v127, v127\n" \
"v_mfma_f32_16x16x32_bf16 v[12:15], a[240:243], a[0:3], v[12:15]\n" \
"s_barrier\n" \
"v_cvt_pk_bf16_f32 v119, v126, v127\n" \
"v_mfma_f32_16x16x32_bf16 v[128:131], a[192:195], v[64:67], v[128:131]\n" \
"v_exp_f32 v0, v0\n" \
"v_mfma_f32_16x16x32_bf16 v[128:131], a[196:199], v[68:71], v[128:131]\n" \
"v_cmp_eq_u32 vcc, s60, v239\n" \
"v_mfma_f32_16x16x32_bf16 v[160:163], a[192:195], v[80:83], v[160:163]\n" \
"v_exp_f32 v1, v1\n" \
"v_mfma_f32_16x16x32_bf16 v[160:163], a[196:199], v[84:87], v[160:163]\n" \
"s_andn2_b32 s70, s69, vcc_lo\n" \
"s_cbranch_scc1 Lpl3_%=\n" \
"Lpr3_%=:\n" \
"ds_read_b128 a[240:243], v232 offset:8448\n" \
"v_cvt_pk_bf16_f32 v0, v0, v1\n" \
"v_mfma_f32_16x16x32_bf16 a[64:67], a[192:195], v[96:99], a[64:67]\n" \
"s_add_u32 m0, s74, s79\n" \
"v_exp_f32 v2, v2\n" \
"v_mfma_f32_16x16x32_bf16 a[64:67], a[196:199], v[100:103], a[64:67]\n" \
"buffer_load_dwordx4 v234, s[36:39], s73 offen offset:0 sc1 lds\n" \
"v_mfma_f32_16x16x32_bf16 a[96:99], a[192:195], v[112:115], a[96:99]\n" \
"v_exp_f32 v3, v3\n" \
"v_mfma_f32_16x16x32_bf16 a[96:99], a[196:199], v[116:119], a[96:99]\n" \
"ds_read_b128 a[192:195], v232 offset:0\n" \
"v_cvt_pk_bf16_f32 v1, v2, v3\n" \
"v_mfma_f32_16x16x32_bf16 a[100:103], a[200:203], v[112:115], a[100:103]\n" \
"s_add_u32 s71, s73, 17408\n" \
"v_exp_f32 v4, v4\n" \
"v_mfma_f32_16x16x32_bf16 a[100:103], a[204:207], v[116:119], a[100:103]\n" \
"buffer_load_dwordx4 v234, s[36:39], s73 offen offset:1024 sc1 lds\n" \
"v_mfma_f32_16x16x32_bf16 a[68:71], a[200:203], v[96:99], a[68:71]\n" \
"v_exp_f32 v5, v5\n" \
"v_mfma_f32_16x16x32_bf16 a[68:71], a[204:207], v[100:103], a[68:71]\n" \
"ds_read_b128 a[196:199], v232 offset:2048\n" \
"v_cvt_pk_bf16_f32 v2, v4, v5\n" \
"v_smfmac_f32_16x16x64_bf16 a[48:51], v[244:247], v[64:71], v243\n" \
"buffer_load_dwordx4 v234, s[36:39], s73 offen offset:2048 sc1 lds\n" \
"v_mfma_f32_16x16x32_bf16 v[164:167], a[200:203], v[80:83], v[164:167]\n" \
"v_exp_f32 v6, v6\n" \
"v_mfma_f32_16x16x32_bf16 v[164:167], a[204:207], v[84:87], v[164:167]\n" \
"buffer_load_dwordx4 v234, s[36:39], s73 offen offset:3072 sc1 lds\n" \
"v_mfma_f32_16x16x32_bf16 v[132:135], a[200:203], v[64:67], v[132:135]\n" \
"v_exp_f32 v7, v7\n" \
"v_mfma_f32_16x16x32_bf16 v[132:135], a[204:207], v[68:71], v[132:135]\n" \
"ds_read_b128 a[200:203], v232 offset:4096\n" \
"v_cvt_pk_bf16_f32 v3, v6, v7\n" \
"v_mfma_f32_16x16x32_bf16 v[136:139], a[208:211], v[64:67], v[136:139]\n" \
"s_add_u32 m0, m0, 0x4000\n" \
"v_exp_f32 v16, v16\n" \
"v_mfma_f32_16x16x32_bf16 v[136:139], a[212:215], v[68:71], v[136:139]\n" \
"buffer_load_dwordx4 v234, s[36:39], s71 offen offset:0 sc1 lds\n" \
"v_mfma_f32_16x16x32_bf16 v[168:171], a[208:211], v[80:83], v[168:171]\n" \
"v_exp_f32 v17, v17\n" \
"v_mfma_f32_16x16x32_bf16 v[168:171], a[212:215], v[84:87], v[168:171]\n" \
"ds_read_b128 a[204:207], v232 offset:6144\n" \
"v_cvt_pk_bf16_f32 v16, v16, v17\n" \
"v_mfma_f32_16x16x32_bf16 a[72:75], a[208:211], v[96:99], a[72:75]\n" \
"v_exp_f32 v18, v18\n" \
"v_mfma_f32_16x16x32_bf16 a[72:75], a[212:215], v[100:103], a[72:75]\n" \
"buffer_load_dwordx4 v234, s[36:39], s71 offen offset:1024 sc1 lds\n" \
"v_mfma_f32_16x16x32_bf16 a[104:107], a[208:211], v[112:115], a[104:107]\n" \
"v_exp_f32 v19, v19\n" \
"v_mfma_f32_16x16x32_bf16 a[104:107], a[212:215], v[116:119], a[104:107]\n" \
"ds_read_b128 a[208:211], v232 offset:256\n" \
"v_cvt_pk_bf16_f32 v17, v18, v19\n" \
"v_mfma_f32_16x16x32_bf16 a[108:111], a[216:219], v[112:115], a[108:111]\n" \
"v_exp_f32 v20, v20\n" \
"v_mfma_f32_16x16x32_bf16 a[108:111], a[220:223], v[116:119], a[108:111]\n" \
"buffer_load_dwordx4 v234, s[36:39], s71 offen offset:2048 sc1 lds\n" \
"v_mfma_f32_16x16x32_bf16 a[76:79], a[216:219], v[96:99], a[76:79]\n" \
"v_exp_f32 v21, v21\n" \
"v_mfma_f32_16x16x32_bf16 a[76:79], a[220:223], v[100:103], a[76:79]\n" \
"ds_read_b128 a[212:215], v232 offset:2304\n" \
"v_cvt_pk_bf16_f32 v18, v20, v21\n" \
"v_smfmac_f32_16x16x64_bf16 a[52:55], v[244:247], v[80:87], v243\n" \
"buffer_load_dwordx4 v234, s[36:39], s71 offen offset:3072 sc1 lds\n" \
"v_mfma_f32_16x16x32_bf16 v[172:175], a[216:219], v[80:83], v[172:175]\n" \
"s_add_u32 m0, s77, s80\n" \
"v_exp_f32 v22, v22\n" \
"v_mfma_f32_16x16x32_bf16 v[172:175], a[220:223], v[84:87], v[172:175]\n" \
"buffer_load_dwordx4 v234, s[40:43], s76 offen offset:0 sc1 lds\n" \
"v_mfma_f32_16x16x32_bf16 v[140:143], a[216:219], v[64:67], v[140:143]\n" \
"v_exp_f32 v23, v23\n" \
"v_mfma_f32_16x16x32_bf16 v[140:143], a[220:223], v[68:71], v[140:143]\n" \
"ds_read_b128 a[216:219], v232 offset:4352\n" \
"v_cvt_pk_bf16_f32 v19, v22, v23\n" \
"v_mfma_f32_16x16x32_bf16 v[144:147], a[224:227], v[64:67], v[144:147]\n" \
"s_add_u32 s71, s76, 17408\n" \
"v_exp_f32 v32, v32\n" \
"v_mfma_f32_16x16x32_bf16 v[144:147], a[228:231], v[68:71], v[144:147]\n" \
"buffer_load_dwordx4 v234, s[40:43], s76 offen offset:1024 sc1 lds\n" \
"v_mfma_f32_16x16x32_bf16 v[176:179], a[224:227], v[80:83], v[176:179]\n" \
"v_exp_f32 v33, v33\n" \
"v_mfma_f32_16x16x32_bf16 v[176:179], a[228:231], v[84:87], v[176:179]\n" \
"ds_read_b128 a[220:223], v232 offset:6400\n" \
"v_cvt_pk_bf16_f32 v32, v32, v33\n" \
"v_mfma_f32_16x16x32_bf16 a[80:83], a[224:227], v[96:99], a[80:83]\n" \
"v_exp_f32 v34, v34\n" \
"v_mfma_f32_16x16x32_bf16 a[80:83], a[228:231], v[100:103], a[80:83]\n" \
"buffer_load_dwordx4 v234, s[40:43], s76 offen offset:2048 sc1 lds\n" \
"v_mfma_f32_16x16x32_bf16 a[112:115], a[224:227], v[112:115], a[112:115]\n" \
"v_exp_f32 v35, v35\n" \
"v_mfma_f32_16x16x32_bf16 a[112:115], a[228:231], v[116:119], a[112:115]\n" \
"ds_read_b128 a[224:227], v232 offset:8192\n" \
"v_cvt_pk_bf16_f32 v33, v34, v35\n" \
"v_mfma_f32_16x16x32_bf16 a[116:119], a[232:235], v[112:115], a[116:119]\n" \
"v_exp_f32 v36, v36\n" \
"v_mfma_f32_16x16x32_bf16 a[116:119], a[236:239], v[116:119], a[116:119]\n" \
"buffer_load_dwordx4 v234, s[40:43], s76 offen offset:3072 sc1 lds\n" \
"v_mfma_f32_16x16x32_bf16 a[84:87], a[232:235], v[96:99], a[84:87]\n" \
"v_exp_f32 v37, v37\n" \
"v_mfma_f32_16x16x32_bf16 a[84:87], a[236:239], v[100:103], a[84:87]\n" \
"s_add_u32 m0, m0, 0x4000\n" \
"ds_read_b128 a[228:231], v232 offset:10240\n" \
"v_cvt_pk_bf16_f32 v34, v36, v37\n" \
"v_smfmac_f32_16x16x64_bf16 v[224:227], v[244:247], v[96:103], v243\n" \
"buffer_load_dwordx4 v234, s[40:43], s71 offen offset:0 sc1 lds\n" \
"v_mfma_f32_16x16x32_bf16 v[180:183], a[232:235], v[80:83], v[180:183]\n" \
"v_exp_f32 v38, v38\n" \
"v_mfma_f32_16x16x32_bf16 v[180:183], a[236:239], v[84:87], v[180:183]\n" \
"buffer_load_dwordx4 v234, s[40:43], s71 offen offset:1024 sc1 lds\n" \
"v_mfma_f32_16x16x32_bf16 v[148:151], a[232:235], v[64:67], v[148:151]\n" \
"v_exp_f32 v39, v39\n" \
"v_mfma_f32_16x16x32_bf16 v[148:151], a[236:239], v[68:71], v[148:151]\n" \
"ds_read_b128 a[232:235], v232 offset:12288\n" \
"v_cvt_pk_bf16_f32 v35, v38, v39\n" \
"v_mfma_f32_16x16x32_bf16 v[152:155], a[32:35], v[64:67], v[152:155]\n" \
"v_exp_f32 v48, v48\n" \
"v_mfma_f32_16x16x32_bf16 v[152:155], a[36:39], v[68:71], v[152:155]\n" \
"buffer_load_dwordx4 v234, s[40:43], s71 offen offset:2048 sc1 lds\n" \
"v_mfma_f32_16x16x32_bf16 v[184:187], a[32:35], v[80:83], v[184:187]\n" \
"v_exp_f32 v49, v49\n" \
"v_mfma_f32_16x16x32_bf16 v[184:187], a[36:39], v[84:87], v[184:187]\n" \
"ds_read_b128 a[236:239], v232 offset:14336\n" \
"v_cvt_pk_bf16_f32 v48, v48, v49\n" \
"v_mfma_f32_16x16x32_bf16 a[88:91], a[32:35], v[96:99], a[88:91]\n" \
"v_exp_f32 v50, v50\n" \
"v_mfma_f32_16x16x32_bf16 a[88:91], a[36:39], v[100:103], a[88:91]\n" \
"buffer_load_dwordx4 v234, s[40:43], s71 offen offset:3072 sc1 lds\n" \
"v_mfma_f32_16x16x32_bf16 a[120:123], a[32:35], v[112:115], a[120:123]\n" \
"s_mov_b32 s76, s73\n" \
"v_exp_f32 v51, v51\n" \
"v_mfma_f32_16x16x32_bf16 a[120:123], a[36:39], v[116:119], a[120:123]\n" \
"s_add_u32 s73, s73, 34816\n" \
"ds_read_b128 a[244:247], v232 offset:10496\n" \
"v_cvt_pk_bf16_f32 v49, v50, v51\n" \
"v_mfma_f32_16x16x32_bf16 a[124:127], a[40:43], v[112:115], a[124:127]\n" \
"s_xor_b32 s74, s74, 0x8000\n" \
"v_exp_f32 v52, v52\n" \
"v_mfma_f32_16x16x32_bf16 a[124:127], a[44:47], v[116:119], a[124:127]\n" \
"s_xor_b32 s77, s77, 0x8000\n" \
"v_xor_b32 v233, s81, v233\n" \
"s_mov_b32 s81, 0x8000\n" \
"v_mfma_f32_16x16x32_bf16 a[92:95], a[40:43], v[96:99], a[92:95]\n" \
"v_exp_f32 v53, v53\n" \
"v_mfma_f32_16x16x32_bf16 a[92:95], a[44:47], v[100:103], a[92:95]\n" \
"ds_read_b128 a[248:251], v232 offset:12544\n" \
"v_cvt_pk_bf16_f32 v50, v52, v53\n" \
"v_smfmac_f32_16x16x64_bf16 v[228:231], v[244:247], v[112:119], v243\n" \
"v_mfma_f32_16x16x32_bf16 v[188:191], a[40:43], v[80:83], v[188:191]\n" \
"v_exp_f32 v54, v54\n" \
"v_mfma_f32_16x16x32_bf16 v[188:191], a[44:47], v[84:87], v[188:191]\n" \
"s_cmp_lg_u32 s82, 0\n" \
"v_exp_f32_e64 v55, v55\n" \
"v_mfma_f32_16x16x32_bf16 v[156:159], a[40:43], v[64:67], v[156:159]\n" \
"s_waitcnt lgkmcnt(2)\n" \
"ds_read_b128 a[252:255], v232 offset:14592\n" \
"v_cvt_pk_bf16_f32 v51, v54, v55\n" \
"v_mfma_f32_16x16x32_bf16 v[156:159], a[44:47], v[68:71], v[156:159]\n" \
"s_cbranch_scc1 Lep_%=\n" \
"Lepr_%=:\n" \
"v_mfma_f32_16x16x32_bf16 v[64:67], a[192:195], a[0:3], v[196:199]\n" \
"v_exp_f32 v8, v8\n" \
"v_mfma_f32_16x16x32_bf16 v[64:67], a[196:199], a[4:7], v[64:67]\n" \
"ds_read_b128 a[44:47], v233 offset:15360\n" \
"v_mfma_f32_16x16x32_bf16 v[64:67], a[200:203], a[8:11], v[64:67]\n" \
"v_exp_f32 v9, v9\n" \
"v_mfma_f32_16x16x32_bf16 v[64:67], a[204:207], a[12:15], v[64:67]\n" \
"ds_read_b128 a[32:35], v233 offset:6144\n" \
"v_cvt_pk_bf16_f32 v4, v8, v9\n" \
"v_mfma_f32_16x16x32_bf16 v[80:83], a[204:207], a[28:31], v[196:199]\n" \
"s_sub_u32 s84, s84, 1\n" \
"v_exp_f32 v10, v10\n" \
"v_mfma_f32_16x16x32_bf16 v[80:83], a[200:203], a[24:27], v[80:83]\n" \
"s_cmp_eq_u32 s84, 0\n" \
"s_cselect_b32 s70, s75, 0x8000\n" \
"v_xor_b32 v232, s70, v232\n" \
"v_mfma_f32_16x16x32_bf16 v[80:83], a[196:199], a[20:23], v[80:83]\n" \
"v_exp_f32 v11, v11\n" \
"v_mfma_f32_16x16x32_bf16 v[80:83], a[192:195], a[16:19], v[80:83]\n" \
"ds_read_b128 a[36:39], v233 offset:14336\n" \
"v_cvt_pk_bf16_f32 v5, v10, v11\n" \
"v_mfma_f32_16x16x32_bf16 v[96:99], a[192:195], a[160:163], v[196:199]\n" \
"v_exp_f32 v12, v12\n" \
"v_mfma_f32_16x16x32_bf16 v[96:99], a[196:199], a[164:167], v[96:99]\n" \
"v_mfma_f32_16x16x32_bf16 v[96:99], a[200:203], a[168:171], v[96:99]\n" \
"v_exp_f32 v13, v13\n" \
"v_mfma_f32_16x16x32_bf16 v[96:99], a[204:207], a[172:175], v[96:99]\n" \
"ds_read_b128 a[40:43], v233 offset:7168\n" \
"v_cvt_pk_bf16_f32 v6, v12, v13\n" \
"v_mfma_f32_16x16x32_bf16 v[112:115], a[204:207], a[188:191], v[196:199]\n" \
"v_exp_f32 v14, v14\n" \
"v_mfma_f32_16x16x32_bf16 v[112:115], a[200:203], a[184:187], v[112:115]\n" \
"v_mfma_f32_16x16x32_bf16 v[112:115], a[196:199], a[180:183], v[112:115]\n" \
"v_exp_f32 v15, v15\n" \
"v_mfma_f32_16x16x32_bf16 v[112:115], a[192:195], a[176:179], v[112:115]\n" \
"ds_read_b128 a[204:207], v233 offset:9216\n" \
"v_cvt_pk_bf16_f32 v7, v14, v15\n" \
"v_mfma_f32_16x16x32_bf16 v[116:119], a[208:211], a[176:179], v[196:199]\n" \
"v_exp_f32 v24, v24\n" \
"v_mfma_f32_16x16x32_bf16 v[116:119], a[212:215], a[180:183], v[116:119]\n" \
"v_mfma_f32_16x16x32_bf16 v[116:119], a[216:219], a[184:187], v[116:119]\n" \
"v_exp_f32 v25, v25\n" \
"v_mfma_f32_16x16x32_bf16 v[116:119], a[220:223], a[188:191], v[116:119]\n" \
"ds_read_b128 a[200:203], v233 offset:1024\n" \
"v_cvt_pk_bf16_f32 v20, v24, v25\n" \
"v_mfma_f32_16x16x32_bf16 v[100:103], a[220:223], a[172:175], v[196:199]\n" \
"v_exp_f32 v26, v26\n" \
"v_mfma_f32_16x16x32_bf16 v[100:103], a[216:219], a[168:171], v[100:103]\n" \
"v_mfma_f32_16x16x32_bf16 v[100:103], a[212:215], a[164:167], v[100:103]\n" \
"v_exp_f32 v27, v27\n" \
"v_mfma_f32_16x16x32_bf16 v[100:103], a[208:211], a[160:163], v[100:103]\n" \
"ds_read_b128 a[196:199], v233 offset:8192\n" \
"v_cvt_pk_bf16_f32 v21, v26, v27\n" \
"v_mfma_f32_16x16x32_bf16 v[84:87], a[208:211], a[16:19], v[196:199]\n" \
"v_exp_f32 v28, v28\n" \
"v_mfma_f32_16x16x32_bf16 v[84:87], a[212:215], a[20:23], v[84:87]\n" \
"v_mfma_f32_16x16x32_bf16 v[84:87], a[216:219], a[24:27], v[84:87]\n" \
"v_exp_f32 v29, v29\n" \
"v_mfma_f32_16x16x32_bf16 v[84:87], a[220:223], a[28:31], v[84:87]\n" \
"ds_read_b128 a[192:195], v233 offset:0\n" \
"v_cvt_pk_bf16_f32 v22, v28, v29\n" \
"v_mfma_f32_16x16x32_bf16 v[68:71], a[220:223], a[12:15], v[196:199]\n" \
"v_exp_f32 v30, v30\n" \
"v_mfma_f32_16x16x32_bf16 v[68:71], a[216:219], a[8:11], v[68:71]\n" \
"v_mfma_f32_16x16x32_bf16 v[68:71], a[212:215], a[4:7], v[68:71]\n" \
"s_waitcnt lgkmcnt(8)\n" \
"v_exp_f32 v31, v31\n" \
"v_mfma_f32_16x16x32_bf16 v[68:71], a[208:211], a[0:3], v[68:71]\n" \
"ds_read_b128 a[220:223], v233 offset:11264\n" \
"v_cvt_pk_bf16_f32 v23, v30, v31\n" \
"v_mfma_f32_16x16x32_bf16 v[72:75], a[224:227], a[0:3], v[196:199]\n" \
"v_exp_f32 v40, v40\n" \
"v_mfma_f32_16x16x32_bf16 v[72:75], a[228:231], a[4:7], v[72:75]\n" \
"v_mfma_f32_16x16x32_bf16 v[72:75], a[232:235], a[8:11], v[72:75]\n" \
"v_exp_f32 v41, v41\n" \
"v_mfma_f32_16x16x32_bf16 v[72:75], a[236:239], a[12:15], v[72:75]\n" \
"ds_read_b128 a[216:219], v233 offset:3072\n" \
"v_cvt_pk_bf16_f32 v36, v40, v41\n" \
"v_mfma_f32_16x16x32_bf16 v[88:91], a[236:239], a[28:31], v[196:199]\n" \
"v_exp_f32 v42, v42\n" \
"v_mfma_f32_16x16x32_bf16 v[88:91], a[232:235], a[24:27], v[88:91]\n" \
"v_mfma_f32_16x16x32_bf16 v[88:91], a[228:231], a[20:23], v[88:91]\n" \
"v_exp_f32 v43, v43\n" \
"v_mfma_f32_16x16x32_bf16 v[88:91], a[224:227], a[16:19], v[88:91]\n" \
"ds_read_b128 a[212:215], v233 offset:10240\n" \
"v_cvt_pk_bf16_f32 v37, v42, v43\n" \
"v_mfma_f32_16x16x32_bf16 v[104:107], a[224:227], a[160:163], v[196:199]\n" \
"v_exp_f32 v44, v44\n" \
"v_mfma_f32_16x16x32_bf16 v[104:107], a[228:231], a[164:167], v[104:107]\n" \
"v_mfma_f32_16x16x32_bf16 v[104:107], a[232:235], a[168:171], v[104:107]\n" \
"v_exp_f32 v45, v45\n" \
"v_mfma_f32_16x16x32_bf16 v[104:107], a[236:239], a[172:175], v[104:107]\n" \
"ds_read_b128 a[208:211], v233 offset:2048\n" \
"v_cvt_pk_bf16_f32 v38, v44, v45\n" \
"v_mfma_f32_16x16x32_bf16 v[120:123], a[236:239], a[188:191], v[196:199]\n" \
"v_exp_f32 v46, v46\n" \
"v_mfma_f32_16x16x32_bf16 v[120:123], a[232:235], a[184:187], v[120:123]\n" \
"v_mfma_f32_16x16x32_bf16 v[120:123], a[228:231], a[180:183], v[120:123]\n" \
"v_exp_f32 v47, v47\n" \
"v_mfma_f32_16x16x32_bf16 v[120:123], a[224:227], a[176:179], v[120:123]\n" \
"ds_read_b128 a[236:239], v233 offset:13312\n" \
"v_cvt_pk_bf16_f32 v39, v46, v47\n" \
"v_mfma_f32_16x16x32_bf16 v[124:127], a[240:243], a[176:179], v[196:199]\n" \
"v_exp_f32 v56, v56\n" \
"v_mfma_f32_16x16x32_bf16 v[124:127], a[244:247], a[180:183], v[124:127]\n" \
"ds_read_b128 a[232:235], v233 offset:5120\n" \
"v_mfma_f32_16x16x32_bf16 v[124:127], a[248:251], a[184:187], v[124:127]\n" \
"v_exp_f32 v57, v57\n" \
"v_mfma_f32_16x16x32_bf16 v[124:127], a[252:255], a[188:191], v[124:127]\n" \
"ds_read_b128 a[228:231], v233 offset:12288\n" \
"v_cvt_pk_bf16_f32 v52, v56, v57\n" \
"v_mfma_f32_16x16x32_bf16 v[108:111], a[252:255], a[172:175], v[196:199]\n" \
"v_exp_f32 v58, v58\n" \
"v_mfma_f32_16x16x32_bf16 v[108:111], a[248:251], a[168:171], v[108:111]\n" \
"ds_read_b128 a[224:227], v233 offset:4096\n" \
"v_mfma_f32_16x16x32_bf16 v[108:111], a[244:247], a[164:167], v[108:111]\n" \
"v_exp_f32 v59, v59\n" \
"v_mfma_f32_16x16x32_bf16 v[108:111], a[240:243], a[160:163], v[108:111]\n" \
"v_cvt_pk_bf16_f32 v53, v58, v59\n" \
"v_mfma_f32_16x16x32_bf16 v[92:95], a[240:243], a[16:19], v[196:199]\n" \
"v_exp_f32 v60, v60\n" \
"v_mfma_f32_16x16x32_bf16 v[92:95], a[244:247], a[20:23], v[92:95]\n" \
"v_mfma_f32_16x16x32_bf16 v[92:95], a[248:251], a[24:27], v[92:95]\n" \
"v_exp_f32 v61, v61\n" \
"v_mfma_f32_16x16x32_bf16 v[92:95], a[252:255], a[28:31], v[92:95]\n" \
"v_cvt_pk_bf16_f32 v54, v60, v61\n" \
"v_mfma_f32_16x16x32_bf16 v[76:79], a[252:255], a[12:15], v[196:199]\n" \
"v_exp_f32 v62, v62\n" \
"v_mfma_f32_16x16x32_bf16 v[76:79], a[248:251], a[8:11], v[76:79]\n" \
"s_cmp_lg_u32 s82, 0\n" \
"s_cbranch_scc1 Lnw_%=\n" \
"s_waitcnt vmcnt(0)\n" \
"Lnw_%=:\n" \
"v_mfma_f32_16x16x32_bf16 v[76:79], a[244:247], a[4:7], v[76:79]\n" \
"s_waitcnt lgkmcnt(0)\n" \
"v_exp_f32 v63, v63\n" \
"v_mfma_f32_16x16x32_bf16 v[76:79], a[240:243], a[0:3], v[76:79]\n" \
"s_barrier\n" \
"v_cvt_pk_bf16_f32 v55, v62, v63\n" \
"v_mfma_f32_16x16x32_bf16 v[128:131], a[192:195], v[0:3], v[128:131]\n" \
"s_add_u32 m0, s74, s79\n" \
"v_exp_f32 v64, v64\n" \
"v_mfma_f32_16x16x32_bf16 v[128:131], a[196:199], v[4:7], v[128:131]\n" \
"buffer_load_dwordx4 v234, s[36:39], s73 offen offset:0 sc1 lds\n" \
"v_mfma_f32_16x16x32_bf16 v[160:163], a[192:195], v[16:19], v[160:163]\n" \
"v_exp_f32 v65, v65\n" \
"v_mfma_f32_16x16x32_bf16 v[160:163], a[196:199], v[20:23], v[160:163]\n" \
"ds_read_b128 a[240:243], v232 offset:8448\n" \
"v_cvt_pk_bf16_f32 v64, v64, v65\n" \
"v_mfma_f32_16x16x32_bf16 a[64:67], a[192:195], v[32:35], a[64:67]\n" \
"s_add_u32 s71, s73, 17408\n" \
"v_exp_f32 v66, v66\n" \
"v_mfma_f32_16x16x32_bf16 a[64:67], a[196:199], v[36:39], a[64:67]\n" \
"buffer_load_dwordx4 v234, s[36:39], s73 offen offset:1024 sc1 lds\n" \
"v_mfma_f32_16x16x32_bf16 a[96:99], a[192:195], v[48:51], a[96:99]\n" \
"v_exp_f32 v67, v67\n" \
"v_mfma_f32_16x16x32_bf16 a[96:99], a[196:199], v[52:55], a[96:99]\n" \
"ds_read_b128 a[192:195], v232 offset:0\n" \
"v_cvt_pk_bf16_f32 v65, v66, v67\n" \
"v_mfma_f32_16x16x32_bf16 a[100:103], a[200:203], v[48:51], a[100:103]\n" \
"v_exp_f32 v68, v68\n" \
"v_mfma_f32_16x16x32_bf16 a[100:103], a[204:207], v[52:55], a[100:103]\n" \
"buffer_load_dwordx4 v234, s[36:39], s73 offen offset:2048 sc1 lds\n" \
"v_mfma_f32_16x16x32_bf16 a[68:71], a[200:203], v[32:35], a[68:71]\n" \
"v_exp_f32 v69, v69\n" \
"v_mfma_f32_16x16x32_bf16 a[68:71], a[204:207], v[36:39], a[68:71]\n" \
"ds_read_b128 a[196:199], v232 offset:2048\n" \
"v_cvt_pk_bf16_f32 v66, v68, v69\n" \
"v_smfmac_f32_16x16x64_bf16 a[48:51], v[244:247], v[0:7], v243\n" \
"buffer_load_dwordx4 v234, s[36:39], s73 offen offset:3072 sc1 lds\n" \
"v_mfma_f32_16x16x32_bf16 v[164:167], a[200:203], v[16:19], v[164:167]\n" \
"s_add_u32 m0, m0, 0x4000\n" \
"v_exp_f32 v70, v70\n" \
"v_mfma_f32_16x16x32_bf16 v[164:167], a[204:207], v[20:23], v[164:167]\n" \
"buffer_load_dwordx4 v234, s[36:39], s71 offen offset:0 sc1 lds\n" \
"v_mfma_f32_16x16x32_bf16 v[132:135], a[200:203], v[0:3], v[132:135]\n" \
"v_exp_f32 v71, v71\n" \
"v_mfma_f32_16x16x32_bf16 v[132:135], a[204:207], v[4:7], v[132:135]\n" \
"ds_read_b128 a[200:203], v232 offset:4096\n" \
"v_cvt_pk_bf16_f32 v67, v70, v71\n" \
"v_mfma_f32_16x16x32_bf16 v[136:139], a[208:211], v[0:3], v[136:139]\n" \
"v_exp_f32 v80, v80\n" \
"v_mfma_f32_16x16x32_bf16 v[136:139], a[212:215], v[4:7], v[136:139]\n" \
"buffer_load_dwordx4 v234, s[36:39], s71 offen offset:1024 sc1 lds\n" \
"v_mfma_f32_16x16x32_bf16 v[168:171], a[208:211], v[16:19], v[168:171]\n" \
"v_exp_f32 v81, v81\n" \
"v_mfma_f32_16x16x32_bf16 v[168:171], a[212:215], v[20:23], v[168:171]\n" \
"ds_read_b128 a[204:207], v232 offset:6144\n" \
"v_cvt_pk_bf16_f32 v80, v80, v81\n" \
"v_mfma_f32_16x16x32_bf16 a[72:75], a[208:211], v[32:35], a[72:75]\n" \
"v_exp_f32 v82, v82\n" \
"v_mfma_f32_16x16x32_bf16 a[72:75], a[212:215], v[36:39], a[72:75]\n" \
"buffer_load_dwordx4 v234, s[36:39], s71 offen offset:2048 sc1 lds\n" \
"v_mfma_f32_16x16x32_bf16 a[104:107], a[208:211], v[48:51], a[104:107]\n" \
"v_exp_f32 v83, v83\n" \
"v_mfma_f32_16x16x32_bf16 a[104:107], a[212:215], v[52:55], a[104:107]\n" \
"ds_read_b128 a[208:211], v232 offset:256\n" \
"v_cvt_pk_bf16_f32 v81, v82, v83\n" \
"v_mfma_f32_16x16x32_bf16 a[108:111], a[216:219], v[48:51], a[108:111]\n" \
"v_exp_f32 v84, v84\n" \
"v_mfma_f32_16x16x32_bf16 a[108:111], a[220:223], v[52:55], a[108:111]\n" \
"buffer_load_dwordx4 v234, s[36:39], s71 offen offset:3072 sc1 lds\n" \
"v_mfma_f32_16x16x32_bf16 a[76:79], a[216:219], v[32:35], a[76:79]\n" \
"v_exp_f32 v85, v85\n" \
"v_mfma_f32_16x16x32_bf16 a[76:79], a[220:223], v[36:39], a[76:79]\n" \
"s_add_u32 m0, s77, s80\n" \
"ds_read_b128 a[212:215], v232 offset:2304\n" \
"v_cvt_pk_bf16_f32 v82, v84, v85\n" \
"v_smfmac_f32_16x16x64_bf16 a[52:55], v[244:247], v[16:23], v243\n" \
"buffer_load_dwordx4 v234, s[40:43], s76 offen offset:0 sc1 lds\n" \
"v_mfma_f32_16x16x32_bf16 v[172:175], a[216:219], v[16:19], v[172:175]\n" \
"s_add_u32 s71, s76, 17408\n" \
"v_exp_f32 v86, v86\n" \
"v_mfma_f32_16x16x32_bf16 v[172:175], a[220:223], v[20:23], v[172:175]\n" \
"buffer_load_dwordx4 v234, s[40:43], s76 offen offset:1024 sc1 lds\n" \
"v_mfma_f32_16x16x32_bf16 v[140:143], a[216:219], v[0:3], v[140:143]\n" \
"v_exp_f32 v87, v87\n" \
"v_mfma_f32_16x16x32_bf16 v[140:143], a[220:223], v[4:7], v[140:143]\n" \
"ds_read_b128 a[216:219], v232 offset:4352\n" \
"v_cvt_pk_bf16_f32 v83, v86, v87\n" \
"v_mfma_f32_16x16x32_bf16 v[144:147], a[224:227], v[0:3], v[144:147]\n" \
"v_exp_f32 v96, v96\n" \
"v_mfma_f32_16x16x32_bf16 v[144:147], a[228:231], v[4:7], v[144:147]\n" \
"buffer_load_dwordx4 v234, s[40:43], s76 offen offset:2048 sc1 lds\n" \
"v_mfma_f32_16x16x32_bf16 v[176:179], a[224:227], v[16:19], v[176:179]\n" \
"v_exp_f32 v97, v97\n" \
"v_mfma_f32_16x16x32_bf16 v[176:179], a[228:231], v[20:23], v[176:179]\n" \
"ds_read_b128 a[220:223], v232 offset:6400\n" \
"v_cvt_pk_bf16_f32 v96, v96, v97\n" \
"v_mfma_f32_16x16x32_bf16 a[80:83], a[224:227], v[32:35], a[80:83]\n" \
"v_exp_f32 v98, v98\n" \
"v_mfma_f32_16x16x32_bf16 a[80:83], a[228:231], v[36:39], a[80:83]\n" \
"buffer_load_dwordx4 v234, s[40:43], s76 offen offset:3072 sc1 lds\n" \
"v_mfma_f32_16x16x32_bf16 a[112:115], a[224:227], v[48:51], a[112:115]\n" \
"v_exp_f32 v99, v99\n" \
"v_mfma_f32_16x16x32_bf16 a[112:115], a[228:231], v[52:55], a[112:115]\n" \
"ds_read_b128 a[224:227], v232 offset:8192\n" \
"v_cvt_pk_bf16_f32 v97, v98, v99\n" \
"v_mfma_f32_16x16x32_bf16 a[116:119], a[232:235], v[48:51], a[116:119]\n" \
"s_add_u32 m0, m0, 0x4000\n" \
"v_exp_f32 v100, v100\n" \
"v_mfma_f32_16x16x32_bf16 a[116:119], a[236:239], v[52:55], a[116:119]\n" \
"buffer_load_dwordx4 v234, s[40:43], s71 offen offset:0 sc1 lds\n" \
"v_mfma_f32_16x16x32_bf16 a[84:87], a[232:235], v[32:35], a[84:87]\n" \
"v_exp_f32 v101, v101\n" \
"v_mfma_f32_16x16x32_bf16 a[84:87], a[236:239], v[36:39], a[84:87]\n" \
"ds_read_b128 a[228:231], v232 offset:10240\n" \
"v_cvt_pk_bf16_f32 v98, v100, v101\n" \
"v_smfmac_f32_16x16x64_bf16 v[224:227], v[244:247], v[32:39], v243\n" \
"buffer_load_dwordx4 v234, s[40:43], s71 offen offset:1024 sc1 lds\n" \
"v_mfma_f32_16x16x32_bf16 v[180:183], a[232:235], v[16:19], v[180:183]\n" \
"v_exp_f32 v102, v102\n" \
"v_mfma_f32_16x16x32_bf16 v[180:183], a[236:239], v[20:23], v[180:183]\n" \
"buffer_load_dwordx4 v234, s[40:43], s71 offen offset:2048 sc1 lds\n" \
"v_mfma_f32_16x16x32_bf16 v[148:151], a[232:235], v[0:3], v[148:151]\n" \
"v_exp_f32 v103, v103\n" \
"v_mfma_f32_16x16x32_bf16 v[148:151], a[236:239], v[4:7], v[148:151]\n" \
"ds_read_b128 a[232:235], v232 offset:12288\n" \
"v_cvt_pk_bf16_f32 v99, v102, v103\n" \
"v_mfma_f32_16x16x32_bf16 v[152:155], a[32:35], v[0:3], v[152:155]\n" \
"v_exp_f32 v112, v112\n" \
"v_mfma_f32_16x16x32_bf16 v[152:155], a[36:39], v[4:7], v[152:155]\n" \
"buffer_load_dwordx4 v234, s[40:43], s71 offen offset:3072 sc1 lds\n" \
"v_mfma_f32_16x16x32_bf16 v[184:187], a[32:35], v[16:19], v[184:187]\n" \
"v_exp_f32 v113, v113\n" \
"v_mfma_f32_16x16x32_bf16 v[184:187], a[36:39], v[20:23], v[184:187]\n" \
"ds_read_b128 a[236:239], v232 offset:14336\n" \
"v_cvt_pk_bf16_f32 v112, v112, v113\n" \
"v_mfma_f32_16x16x32_bf16 a[88:91], a[32:35], v[32:35], a[88:91]\n" \
"v_exp_f32 v114, v114\n" \
"v_mfma_f32_16x16x32_bf16 a[88:91], a[36:39], v[36:39], a[88:91]\n" \
"buffer_load_dword v239, v242, s[56:59], s95 offen sc1\n" \
"v_mfma_f32_16x16x32_bf16 a[120:123], a[32:35], v[48:51], a[120:123]\n" \
"s_mov_b32 s69, s92\n" \
"v_exp_f32 v115, v115\n" \
"v_mfma_f32_16x16x32_bf16 a[120:123], a[36:39], v[52:55], a[120:123]\n" \
"s_mov_b32 s76, s73\n" \
"ds_read_b128 a[244:247], v232 offset:10496\n" \
"v_cvt_pk_bf16_f32 v113, v114, v115\n" \
"v_mfma_f32_16x16x32_bf16 a[124:127], a[40:43], v[48:51], a[124:127]\n" \
"s_add_u32 s73, s73, 34816\n" \
"v_exp_f32 v116, v116\n" \
"v_mfma_f32_16x16x32_bf16 a[124:127], a[44:47], v[52:55], a[124:127]\n" \
"s_xor_b32 s74, s74, 0x8000\n" \
"s_xor_b32 s77, s77, 0x8000\n" \
"v_xor_b32 v233, 0x8000, v233\n" \
"v_mfma_f32_16x16x32_bf16 a[92:95], a[40:43], v[32:35], a[92:95]\n" \
"s_mov_b32 s82, 0\n" \
"v_exp_f32 v117, v117\n" \
"v_mfma_f32_16x16x32_bf16 a[92:95], a[44:47], v[36:39], a[92:95]\n" \
"ds_read_b128 a[248:251], v232 offset:12544\n" \
"v_cvt_pk_bf16_f32 v114, v116, v117\n" \
"Lpg6785_%=:\n" \
"s_cbranch_execz Lcb0_%=\n" \
"v_smfmac_f32_16x16x64_bf16 v[228:231], v[244:247], v[48:55], v243\n" \
"v_mfma_f32_16x16x32_bf16 v[188:191], a[40:43], v[16:19], v[188:191]\n" \
"v_exp_f32_e64 v118, v118\n" \
"v_mfma_f32_16x16x32_bf16 v[188:191], a[44:47], v[20:23], v[188:191]\n" \
"s_cmp_lg_u32 s84, 0\n" \
"v_exp_f32_e64 v119, v119\n" \
"v_mfma_f32_16x16x32_bf16 v[156:159], a[40:43], v[0:3], v[156:159]\n" \
"s_waitcnt lgkmcnt(2)\n" \
"ds_read_b128 a[252:255], v232 offset:14592\n" \
"v_cvt_pk_bf16_f32 v115, v118, v119\n" \
"v_mfma_f32_16x16x32_bf16 v[156:159], a[44:47], v[4:7], v[156:159]\n" \
"s_cbranch_scc1 Lloop2_%=\n" \
"Lg_%=:\n" \
"Lpg6799_%=:\n" \
"s_cbranch_execz Lcb0_%=\n" \
"v_mfma_f32_16x16x32_bf16 v[32:35], a[192:195], a[160:163], v[208:211]\n" \
"s_mov_b64 s[44:45], s[20:21]\n" \
"s_memrealtime s[14:15]\n" \
"v_exp_f32 v72, v72\n" \
"v_mfma_f32_16x16x32_bf16 v[32:35], a[196:199], a[164:167], v[32:35]\n" \
"s_and_b32 s45, s45, 0xffff\n" \
"ds_read_b128 a[32:35], v233 offset:6144\n" \
"ds_read_b128 a[36:39], v233 offset:14336\n" \
"v_mfma_f32_16x16x32_bf16 v[32:35], a[200:203], a[168:171], v[32:35]\n" \
"s_mov_b32 s46, 4194304\n" \
"v_exp_f32 v73, v73\n" \
"v_mfma_f32_16x16x32_bf16 v[32:35], a[204:207], a[172:175], v[32:35]\n" \
"ds_read_b128 a[40:43], v233 offset:7168\n" \
"ds_read_b128 a[44:47], v233 offset:15360\n" \
"global_load_dword v253, v254, s[22:23]\n" \
"v_mfma_f32_16x16x32_bf16 v[48:51], a[204:207], a[188:191], v[196:199]\n" \
"s_mov_b32 s47, 0x20000\n" \
"v_exp_f32 v74, v74\n" \
"v_mfma_f32_16x16x32_bf16 v[48:51], a[200:203], a[184:187], v[48:51]\n" \
"s_mov_b64 s[48:49], s[22:23]\n" \
"v_exp_f32 v75, v75\n" \
"v_mfma_f32_16x16x32_bf16 v[48:51], a[196:199], a[180:183], v[48:51]\n" \
"s_and_b32 s49, s49, 0xffff\n" \
"v_exp_f32 v76, v76\n" \
"v_mfma_f32_16x16x32_bf16 v[48:51], a[192:195], a[176:179], v[48:51]\n" \
"s_mov_b32 s50, 6291456\n" \
"ds_read_b128 a[200:203], v233 offset:1024\n" \
"ds_read_b128 a[204:207], v233 offset:9216\n" \
"v_mfma_f32_16x16x32_bf16 v[52:55], a[208:211], a[176:179], v[196:199]\n" \
"s_mov_b32 s51, 0x20000\n" \
"v_cvt_pk_bf16_f32 v68, v72, v73\n" \
"v_cvt_pk_bf16_f32 v69, v74, v75\n" \
"v_mfma_f32_16x16x32_bf16 v[52:55], a[212:215], a[180:183], v[52:55]\n" \
"s_bfe_u32 s94, %1, 0x20001\n" \
"v_exp_f32 v77, v77\n" \
"v_mfma_f32_16x16x32_bf16 v[52:55], a[216:219], a[184:187], v[52:55]\n" \
"s_lshl_b32 s94, s94, 2\n" \
"v_exp_f32 v78, v78\n" \
"v_mfma_f32_16x16x32_bf16 v[52:55], a[220:223], a[188:191], v[52:55]\n" \
"s_lshr_b32 s70, %1, 6\n" \
"ds_read_b128 a[192:195], v233 offset:0\n" \
"ds_read_b128 a[196:199], v233 offset:8192\n" \
"v_mfma_f32_16x16x32_bf16 v[36:39], a[220:223], a[172:175], v[212:215]\n" \
"s_add_u32 s94, s94, s70\n" \
"v_exp_f32 v79, v79\n" \
"v_mfma_f32_16x16x32_bf16 v[36:39], a[216:219], a[168:171], v[36:39]\n" \
"s_lshl_b32 s73, s94, 18\n" \
"v_exp_f32 v88, v88\n" \
"v_mfma_f32_16x16x32_bf16 v[36:39], a[212:215], a[164:167], v[36:39]\n" \
"s_max_u32 s75, s73, 32768\n" \
"v_cvt_pk_bf16_f32 v70, v76, v77\n" \
"v_cvt_pk_bf16_f32 v71, v78, v79\n" \
"v_mfma_f32_16x16x32_bf16 v[36:39], a[208:211], a[160:163], v[36:39]\n" \
"s_sub_u32 s75, s75, 32768\n" \
"ds_read_b128 a[216:219], v233 offset:3072\n" \
"ds_read_b128 a[220:223], v233 offset:11264\n" \
"v_mfma_f32_16x16x32_bf16 v[40:43], a[224:227], a[160:163], v[216:219]\n" \
"s_and_b32 s70, s61, 1\n" \
"v_exp_f32 v89, v89\n" \
"v_mfma_f32_16x16x32_bf16 v[40:43], a[228:231], a[164:167], v[40:43]\n" \
"s_lshr_b32 s71, s61, 1\n" \
"v_exp_f32 v90, v90\n" \
"v_mfma_f32_16x16x32_bf16 v[40:43], a[232:235], a[168:171], v[40:43]\n" \
"s_lshl1_add_u32 s70, s70, s71\n" \
"v_exp_f32 v91, v91\n" \
"v_mfma_f32_16x16x32_bf16 v[40:43], a[236:239], a[172:175], v[40:43]\n" \
"s_lshl_b32 s70, s70, 10\n" \
"ds_read_b128 a[208:211], v233 offset:2048\n" \
"ds_read_b128 a[212:215], v233 offset:10240\n" \
"v_mfma_f32_16x16x32_bf16 v[56:59], a[236:239], a[188:191], v[196:199]\n" \
"s_add_u32 s75, s75, s70\n" \
"v_exp_f32 v92, v92\n" \
"v_mfma_f32_16x16x32_bf16 v[56:59], a[232:235], a[184:187], v[56:59]\n" \
"s_lshl_b32 s89, s61, 13\n" \
"v_cvt_pk_bf16_f32 v84, v88, v89\n" \
"v_cvt_pk_bf16_f32 v85, v90, v91\n" \
"v_mfma_f32_16x16x32_bf16 v[56:59], a[228:231], a[180:183], v[56:59]\n" \
"s_add_u32 s89, s89, %3\n" \
"v_exp_f32 v93, v93\n" \
"v_mfma_f32_16x16x32_bf16 v[56:59], a[224:227], a[176:179], v[56:59]\n" \
"s_lshr_b32 s71, s22, 7\n" \
"ds_read_b128 a[232:235], v233 offset:5120\n" \
"ds_read_b128 a[236:239], v233 offset:13312\n" \
"v_mfma_f32_16x16x32_bf16 v[60:63], a[240:243], a[176:179], v[196:199]\n" \
"s_waitcnt lgkmcnt(2)\n" \
"s_sub_u32 s71, s63, s71\n" \
"v_exp_f32 v94, v94\n" \
"v_mfma_f32_16x16x32_bf16 v[60:63], a[244:247], a[180:183], v[60:63]\n" \
"s_add_u32 s71, s71, 6\n" \
"v_exp_f32 v95, v95\n" \
"v_mfma_f32_16x16x32_bf16 v[60:63], a[248:251], a[184:187], v[60:63]\n" \
"s_and_b32 s71, s71, 7\n" \
"v_cvt_pk_bf16_f32 v86, v92, v93\n" \
"v_accvgpr_read_b32 v192, a57\n" \
"v_mfma_f32_16x16x32_bf16 v[60:63], a[252:255], a[188:191], v[60:63]\n" \
"s_and_b32 s70, %1, 1\n" \
"ds_read_b128 a[224:227], v233 offset:4096\n" \
"ds_read_b128 a[228:231], v233 offset:12288\n" \
"v_mfma_f32_16x16x32_bf16 v[44:47], a[252:255], a[172:175], v[220:223]\n" \
"s_lshl_b32 s70, s70, 3\n" \
"v_cvt_pk_bf16_f32 v87, v94, v95\n" \
"v_mfma_f32_16x16x32_bf16 v[44:47], a[248:251], a[168:171], v[44:47]\n" \
"s_add_u32 s70, s70, s71\n" \
"v_exp_f32 v104, v104\n" \
"v_mfma_f32_16x16x32_bf16 v[44:47], a[244:247], a[164:167], v[44:47]\n" \
"s_lshl_b32 s78, s70, 7\n" \
"v_accvgpr_read_b32 v197, a56\n" \
"v_add_u32 v198, 1024, v197\n" \
"v_mfma_f32_16x16x32_bf16 v[44:47], a[240:243], a[160:163], v[44:47]\n" \
"s_lshr_b32 s80, %1, 6\n" \
"v_add_u32 v199, 2048, v197\n" \
"v_add_u32 v193, 31744, v192\n" \
"v_mfma_f32_16x16x32_bf16 v[128:131], a[192:195], v[64:67], v[128:131]\n" \
"s_xor_b32 s80, s80, s63\n" \
"v_exp_f32 v105, v105\n" \
"v_mfma_f32_16x16x32_bf16 v[128:131], a[196:199], v[68:71], v[128:131]\n" \
"s_and_b32 s80, s80, 0\n" \
"ds_read_b128 a[0:3], v232 offset:16384\n" \
"ds_read_b128 a[4:7], v232 offset:18432\n" \
"v_mfma_f32_16x16x32_bf16 v[160:163], a[192:195], v[80:83], v[160:163]\n" \
"s_add_u32 m0, s89, 131072\n" \
"v_exp_f32 v106, v106\n" \
"v_mfma_f32_16x16x32_bf16 v[160:163], a[196:199], v[84:87], v[160:163]\n" \
"buffer_load_dwordx4 v192, s[44:47], s75 offen offset:0 lds\n" \
"v_exp_f32 v107, v107\n" \
"v_mfma_f32_16x16x32_bf16 v[164:167], a[200:203], v[80:83], v[164:167]\n" \
"s_lshl_b32 s93, s70, 12\n" \
"ds_read_b128 a[8:11], v232 offset:20480\n" \
"ds_read_b128 a[12:15], v232 offset:22528\n" \
"v_mfma_f32_16x16x32_bf16 v[164:167], a[204:207], v[84:87], v[164:167]\n" \
"s_add_u32 m0, s89, 131968\n" \
"v_exp_f32 v108, v108\n" \
"v_mfma_f32_16x16x32_bf16 v[132:135], a[200:203], v[64:67], v[132:135]\n" \
"buffer_load_dwordx4 v192, s[44:47], s75 offen offset:128 lds\n" \
"v_mfma_f32_16x16x32_bf16 v[132:135], a[204:207], v[68:71], v[132:135]\n" \
"s_mul_i32 s91, s61, 6144\n" \
"ds_read_b128 a[16:19], v232 offset:16640\n" \
"ds_read_b128 a[20:23], v232 offset:18688\n" \
"v_cvt_pk_bf16_f32 v100, v104, v105\n" \
"v_smfmac_f32_16x16x64_bf16 a[48:51], v[244:247], v[64:71], v243\n" \
"s_add_u32 m0, s89, 132864\n" \
"v_cvt_pk_bf16_f32 v101, v106, v107\n" \
"v_mfma_f32_16x16x32_bf16 v[136:139], a[208:211], v[64:67], v[136:139]\n" \
"buffer_load_dwordx4 v192, s[44:47], s75 offen offset:256 lds\n" \
"v_mfma_f32_16x16x32_bf16 v[136:139], a[212:215], v[68:71], v[136:139]\n" \
"s_add_u32 s91, s91, %3\n" \
"ds_read_b128 a[24:27], v232 offset:20736\n" \
"ds_read_b128 a[28:31], v232 offset:22784\n" \
"v_mfma_f32_16x16x32_bf16 v[168:171], a[208:211], v[80:83], v[168:171]\n" \
"s_add_u32 m0, s89, 133760\n" \
"v_exp_f32 v109, v109\n" \
"v_mfma_f32_16x16x32_bf16 v[168:171], a[212:215], v[84:87], v[168:171]\n" \
"buffer_load_dwordx4 v192, s[44:47], s75 offen offset:384 lds\n" \
"v_mfma_f32_16x16x32_bf16 v[172:175], a[216:219], v[80:83], v[172:175]\n" \
"s_lshl_b32 s71, s80, 7\n" \
"ds_read_b128 v[0:3], v232 offset:24576\n" \
"ds_read_b128 v[4:7], v232 offset:26624\n" \
"v_mfma_f32_16x16x32_bf16 v[172:175], a[220:223], v[84:87], v[172:175]\n" \
"s_add_u32 m0, s89, 134656\n" \
"v_exp_f32 v110, v110\n" \
"v_mfma_f32_16x16x32_bf16 v[140:143], a[216:219], v[64:67], v[140:143]\n" \
"buffer_load_dwordx4 v192, s[44:47], s75 offen offset:512 lds\n" \
"v_mfma_f32_16x16x32_bf16 v[140:143], a[220:223], v[68:71], v[140:143]\n" \
"s_lshl_b32 s70, s61, 16\n" \
"ds_read_b128 v[8:11], v232 offset:28672\n" \
"ds_read_b128 v[12:15], v232 offset:30720\n" \
"v_exp_f32 v111, v111\n" \
"v_smfmac_f32_16x16x64_bf16 a[52:55], v[244:247], v[80:87], v243\n" \
"s_waitcnt lgkmcnt(4)\n" \
"s_add_u32 m0, s89, 135552\n" \
"v_cvt_pk_bf16_f32 v102, v108, v109\n" \
"v_mfma_f32_16x16x32_bf16 v[144:147], a[224:227], v[64:67], v[144:147]\n" \
"buffer_load_dwordx4 v192, s[44:47], s75 offen offset:640 lds\n" \
"v_mfma_f32_16x16x32_bf16 v[144:147], a[228:231], v[68:71], v[144:147]\n" \
"s_add_u32 s73, s73, s70\n" \
"ds_read_b128 v[200:203], v232 offset:24832\n" \
"ds_read_b128 v[204:207], v232 offset:26880\n" \
"v_mfma_f32_16x16x32_bf16 v[176:179], a[224:227], v[80:83], v[176:179]\n" \
"s_add_u32 m0, s89, 136448\n" \
"v_exp_f32 v120, v120\n" \
"v_mfma_f32_16x16x32_bf16 v[176:179], a[228:231], v[84:87], v[176:179]\n" \
"buffer_load_dwordx4 v192, s[44:47], s75 offen offset:768 lds\n" \
"v_mfma_f32_16x16x32_bf16 v[180:183], a[232:235], v[80:83], v[180:183]\n" \
"s_add_u32 s73, s73, s71\n" \
"ds_read_b128 a[240:243], v232 offset:28928\n" \
"ds_read_b128 a[244:247], v232 offset:30976\n" \
"v_mfma_f32_16x16x32_bf16 v[180:183], a[236:239], v[84:87], v[180:183]\n" \
"s_add_u32 m0, s89, 137344\n" \
"v_exp_f32 v121, v121\n" \
"v_exp_f32 v122, v122\n" \
"v_mfma_f32_16x16x32_bf16 v[148:151], a[232:235], v[64:67], v[148:151]\n" \
"buffer_load_dwordx4 v192, s[44:47], s75 offen offset:896 lds\n" \
"v_mfma_f32_16x16x32_bf16 v[148:151], a[236:239], v[68:71], v[148:151]\n" \
"s_and_b32 s70, s61, 1\n" \
"v_cvt_pk_bf16_f32 v103, v110, v111\n" \
"v_cvt_pk_bf16_f32 v116, v120, v121\n" \
"v_mfma_f32_16x16x32_bf16 v[152:155], a[32:35], v[64:67], v[152:155]\n" \
"s_lshl_b32 s70, s70, 11\n" \
"v_add_u32 v233, 0x10000, v235\n" \
"s_lshl_b32 s72, s61, 13\n" \
"v_mfma_f32_16x16x32_bf16 v[152:155], a[36:39], v[68:71], v[152:155]\n" \
"s_add_u32 s93, s93, s70\n" \
"v_add_u32 v232, s72, v235\n" \
"v_exp_f32 v123, v123\n" \
"v_mfma_f32_16x16x32_bf16 v[184:187], a[32:35], v[80:83], v[184:187]\n" \
"s_add_u32 s93, s93, 52740096\n" \
"s_cmp_eq_u32 s94, 0\n" \
"s_cselect_b32 s92, -1, 0\n" \
"s_mul_i32 s70, s94, 278528\n" \
"s_add_u32 s93, s93, s70\n" \
"s_lshr_b32 s70, s61, 1\n" \
"s_lshl_b32 s70, s70, 8\n" \
"s_add_u32 s93, s93, s70\n" \
"v_mfma_f32_16x16x32_bf16 v[184:187], a[36:39], v[84:87], v[184:187]\n" \
"v_exp_f32 v124, v124\n" \
"v_mfma_f32_16x16x32_bf16 v[188:191], a[40:43], v[80:83], v[188:191]\n" \
"v_exp_f32 v125, v125\n" \
"v_mfma_f32_16x16x32_bf16 v[188:191], a[44:47], v[84:87], v[188:191]\n" \
"v_exp_f32 v126, v126\n" \
"v_cvt_pk_bf16_f32 v117, v122, v123\n" \
"v_mfma_f32_16x16x32_bf16 v[156:159], a[40:43], v[64:67], v[156:159]\n" \
"s_mul_i32 s76, s80, 393216\n" \
"v_exp_f32 v127, v127\n" \
"v_mfma_f32_16x16x32_bf16 v[156:159], a[44:47], v[68:71], v[156:159]\n" \
"s_add_u32 s76, s76, s78\n" \
"s_cmp_eq_u32 s63, 7\n" \
"s_waitcnt vmcnt(9) lgkmcnt(0)\n" \
"s_barrier\n" \
"s_cbranch_scc1 Lep_%=\n" \
"Lepg_%=:\n" \
"v_mfma_f32_16x16x32_bf16 v[16:19], a[0:3], a[176:179], v[208:211]\n" \
"s_add_u32 m0, s91, 106496\n" \
"ds_read_b128 v[64:67], v233 offset:16384\n" \
"ds_read_b128 v[68:71], v233 offset:24576\n" \
"v_mfma_f32_16x16x32_bf16 v[16:19], a[4:7], a[180:183], v[16:19]\n" \
"buffer_load_dwordx4 v197, s[48:51], s76 offen offset:0 lds\n" \
"v_mfma_f32_16x16x32_bf16 v[16:19], a[8:11], a[184:187], v[16:19]\n" \
"ds_read_b128 v[72:75], v233 offset:17408\n" \
"ds_read_b128 v[76:79], v233 offset:25600\n" \
"v_mfma_f32_16x16x32_bf16 v[16:19], a[12:15], a[188:191], v[16:19]\n" \
"ds_read_b128 v[80:83], v233 offset:18432\n" \
"ds_read_b128 v[84:87], v233 offset:26624\n" \
"v_cvt_pk_bf16_f32 v128, v128, v129\n" \
"v_cvt_pk_bf16_f32 v129, v130, v131\n" \
"v_cvt_pk_bf16_f32 v130, v132, v133\n" \
"v_cvt_pk_bf16_f32 v118, v124, v125\n" \
"v_mfma_f32_16x16x32_bf16 v[20:23], a[28:31], a[188:191], v[212:215]\n" \
"s_add_u32 s77, s76, 24576\n" \
"ds_read_b128 v[88:91], v233 offset:19456\n" \
"ds_read_b128 v[92:95], v233 offset:27648\n" \
"v_mfma_f32_16x16x32_bf16 v[20:23], a[24:27], a[184:187], v[20:23]\n" \
"buffer_load_dwordx4 v198, s[48:51], s76 offen offset:1024 lds\n" \
"v_cvt_pk_bf16_f32 v131, v134, v135\n" \
"v_cvt_pk_bf16_f32 v119, v126, v127\n" \
"v_exp_f32 v32, v32\n" \
"v_xor_b32 v248, 8192, v232\n" \
"v_mfma_f32_16x16x32_bf16 v[20:23], a[20:23], a[180:183], v[20:23]\n" \
"ds_write_b128 v232, v[128:131] offset:0\n" \
"v_exp_f32 v33, v33\n" \
"v_mfma_f32_16x16x32_bf16 v[20:23], a[16:19], a[176:179], v[20:23]\n" \
"ds_read_b128 a[248:251], v233 offset:7168\n" \
"ds_read_b128 a[252:255], v233 offset:15360\n" \
"v_exp_f32 v34, v34\n" \
"v_mfma_f32_16x16x32_bf16 v[24:27], v[0:3], a[176:179], v[216:219]\n" \
"s_xor_b32 s80, s80, 0x3\n" \
"v_cvt_pk_bf16_f32 v132, v136, v137\n" \
"v_cvt_pk_bf16_f32 v133, v138, v139\n" \
"v_mfma_f32_16x16x32_bf16 v[24:27], v[4:7], a[180:183], v[24:27]\n" \
"buffer_load_dwordx4 v199, s[48:51], s76 offen offset:2048 lds\n" \
"v_exp_f32 v35, v35\n" \
"v_lshrrev_b32 v249, 1, v234\n" \
"v_mfma_f32_16x16x32_bf16 v[24:27], v[8:11], a[184:187], v[24:27]\n" \
"s_lshl_b32 s72, s61, 9\n" \
"v_cvt_pk_bf16_f32 v134, v140, v141\n" \
"v_cvt_pk_bf16_f32 v135, v142, v143\n" \
"v_mfma_f32_16x16x32_bf16 v[24:27], v[12:15], a[188:191], v[24:27]\n" \
"s_add_u32 s72, s72, 57344\n" \
"s_add_u32 s72, s72, %3\n" \
"v_cvt_pk_bf16_f32 v136, v144, v145\n" \
"v_cvt_pk_bf16_f32 v137, v146, v147\n" \
"v_exp_f32 v36, v36\n" \
"v_cvt_pk_bf16_f32 v32, v32, v33\n" \
"v_mfma_f32_16x16x32_bf16 v[28:31], a[244:247], a[188:191], v[220:223]\n" \
"s_add_u32 m0, m0, 3072\n" \
"v_cvt_pk_bf16_f32 v138, v148, v149\n" \
"v_cvt_pk_bf16_f32 v139, v150, v151\n" \
"v_mfma_f32_16x16x32_bf16 v[28:31], a[240:243], a[184:187], v[28:31]\n" \
"buffer_load_dwordx4 v197, s[48:51], s77 offen offset:0 lds\n" \
"v_exp_f32 v37, v37\n" \
"v_mfma_f32_16x16x32_bf16 v[28:31], v[204:207], a[180:183], v[28:31]\n" \
"ds_write_b128 v232, v[132:135] offset:1024\n" \
"v_exp_f32 v38, v38\n" \
"v_cvt_pk_bf16_f32 v33, v34, v35\n" \
"v_add_u32 v249, s72, v249\n" \
"v_mfma_f32_16x16x32_bf16 v[28:31], v[200:203], a[176:179], v[28:31]\n" \
"v_cvt_pk_bf16_f32 v140, v152, v153\n" \
"v_cvt_pk_bf16_f32 v141, v154, v155\n" \
"v_exp_f32 v39, v39\n" \
"v_mfma_f32_16x16x32_bf16 a[64:67], a[192:195], v[96:99], a[64:67]\n" \
"v_cvt_pk_bf16_f32 v142, v156, v157\n" \
"v_cvt_pk_bf16_f32 v143, v158, v159\n" \
"v_mfma_f32_16x16x32_bf16 a[64:67], a[196:199], v[100:103], a[64:67]\n" \
"buffer_load_dwordx4 v198, s[48:51], s77 offen offset:1024 lds\n" \
"v_exp_f32 v48, v48\n" \
"v_cvt_pk_bf16_f32 v34, v36, v37\n" \
"v_mfma_f32_16x16x32_bf16 a[96:99], a[192:195], v[112:115], a[96:99]\n" \
"v_cvt_pk_bf16_f32 v144, v160, v161\n" \
"v_cvt_pk_bf16_f32 v145, v162, v163\n" \
"v_mfma_f32_16x16x32_bf16 a[96:99], a[196:199], v[116:119], a[96:99]\n" \
"ds_write_b128 v232, v[136:139] offset:2048\n" \
"v_exp_f32 v49, v49\n" \
"v_exp_f32 v50, v50\n" \
"v_cvt_pk_bf16_f32 v35, v38, v39\n" \
"v_accvgpr_read_b32 v250, a48\n" \
"v_mfma_f32_16x16x32_bf16 a[100:103], a[200:203], v[112:115], a[100:103]\n" \
"ds_read_b128 a[192:195], v233 offset:0\n" \
"ds_read_b128 a[196:199], v233 offset:8192\n" \
"v_mfma_f32_16x16x32_bf16 a[100:103], a[204:207], v[116:119], a[100:103]\n" \
"buffer_load_dwordx4 v199, s[48:51], s77 offen offset:2048 lds\n" \
"v_exp_f32 v51, v51\n" \
"v_exp_f32 v52, v52\n" \
"v_mfma_f32_16x16x32_bf16 a[68:71], a[200:203], v[96:99], a[68:71]\n" \
"v_cvt_pk_bf16_f32 v146, v164, v165\n" \
"v_cvt_pk_bf16_f32 v147, v166, v167\n" \
"v_mfma_f32_16x16x32_bf16 a[68:71], a[204:207], v[100:103], a[68:71]\n" \
"ds_read_b128 a[200:203], v233 offset:1024\n" \
"ds_read_b128 a[204:207], v233 offset:9216\n" \
"v_cvt_pk_bf16_f32 v48, v48, v49\n" \
"v_exp_f32 v53, v53\n" \
"v_smfmac_f32_16x16x64_bf16 v[224:227], v[244:247], v[96:103], v243\n" \
"s_add_u32 m0, s79, 40960\n" \
"v_cvt_pk_bf16_f32 v148, v168, v169\n" \
"v_exp_f32 v54, v54\n" \
"v_cvt_pk_bf16_f32 v49, v50, v51\n" \
"v_accvgpr_read_b32 v240, a49\n" \
"v_mfma_f32_16x16x32_bf16 a[72:75], a[208:211], v[96:99], a[72:75]\n" \
"buffer_load_dwordx4 v192, s[44:47], s73 offen offset:0 lds\n" \
"v_mfma_f32_16x16x32_bf16 a[72:75], a[212:215], v[100:103], a[72:75]\n" \
"ds_write_b128 v232, v[140:143] offset:3072\n" \
"v_exp_f32 v55, v55\n" \
"v_exp_f32 v40, v40\n" \
"v_mfma_f32_16x16x32_bf16 a[104:107], a[208:211], v[112:115], a[104:107]\n" \
"v_cvt_pk_bf16_f32 v149, v170, v171\n" \
"v_cvt_pk_bf16_f32 v150, v172, v173\n" \
"v_mfma_f32_16x16x32_bf16 a[104:107], a[212:215], v[116:119], a[104:107]\n" \
"ds_read_b128 a[208:211], v233 offset:2048\n" \
"ds_read_b128 a[212:215], v233 offset:10240\n" \
"v_cvt_pk_bf16_f32 v50, v52, v53\n" \
"v_exp_f32 v41, v41\n" \
"v_mfma_f32_16x16x32_bf16 a[108:111], a[216:219], v[112:115], a[108:111]\n" \
"buffer_load_dwordx4 v193, s[44:47], s73 offen offset:1024 lds\n" \
"v_mfma_f32_16x16x32_bf16 a[108:111], a[220:223], v[116:119], a[108:111]\n" \
"v_cvt_pk_bf16_f32 v151, v174, v175\n" \
"v_cvt_pk_bf16_f32 v152, v176, v177\n" \
"v_exp_f32 v42, v42\n" \
"v_cvt_pk_bf16_f32 v51, v54, v55\n" \
"v_exp_f32 v43, v43\n" \
"v_add_f32 v250, v250, v240\n" \
"v_mfma_f32_16x16x32_bf16 a[76:79], a[216:219], v[96:99], a[76:79]\n" \
"v_cvt_pk_bf16_f32 v153, v178, v179\n" \
"v_cvt_pk_bf16_f32 v154, v180, v181\n" \
"v_mfma_f32_16x16x32_bf16 a[76:79], a[220:223], v[100:103], a[76:79]\n" \
"ds_read_b128 a[216:219], v233 offset:3072\n" \
"ds_read_b128 a[220:223], v233 offset:11264\n" \
"v_exp_f32 v44, v44\n" \
"v_cvt_pk_bf16_f32 v36, v40, v41\n" \
"v_smfmac_f32_16x16x64_bf16 v[228:231], v[244:247], v[112:119], v243\n" \
"buffer_load_dwordx4 v192, s[44:47], s73 offen offset:2048 lds\n" \
"v_exp_f32 v45, v45\n" \
"v_mfma_f32_16x16x32_bf16 a[80:83], a[224:227], v[96:99], a[80:83]\n" \
"ds_write_b128 v232, v[144:147] offset:4096\n" \
"v_exp_f32 v46, v46\n" \
"v_cvt_pk_bf16_f32 v37, v42, v43\n" \
"v_accvgpr_read_b32 v251, a52\n" \
"v_mfma_f32_16x16x32_bf16 a[80:83], a[228:231], v[100:103], a[80:83]\n" \
"v_cvt_pk_bf16_f32 v155, v182, v183\n" \
"v_cvt_pk_bf16_f32 v156, v184, v185\n" \
"v_exp_f32 v47, v47\n" \
"v_mfma_f32_16x16x32_bf16 a[112:115], a[224:227], v[112:115], a[112:115]\n" \
"v_cvt_pk_bf16_f32 v157, v186, v187\n" \
"v_cvt_pk_bf16_f32 v158, v188, v189\n" \
"v_mfma_f32_16x16x32_bf16 a[112:115], a[228:231], v[116:119], a[112:115]\n" \
"buffer_load_dwordx4 v193, s[44:47], s73 offen offset:3072 lds\n" \
"v_exp_f32 v56, v56\n" \
"v_cvt_pk_bf16_f32 v38, v44, v45\n" \
"v_mfma_f32_16x16x32_bf16 a[116:119], a[232:235], v[112:115], a[116:119]\n" \
"ds_read_b128 a[224:227], v233 offset:4096\n" \
"ds_read_b128 a[228:231], v233 offset:12288\n" \
"v_mfma_f32_16x16x32_bf16 a[116:119], a[236:239], v[116:119], a[116:119]\n" \
"v_cvt_pk_bf16_f32 v159, v190, v191\n" \
"v_cvt_pk_bf16_f32 v39, v46, v47\n" \
"v_exp_f32 v57, v57\n" \
"v_exp_f32 v58, v58\n" \
"v_accvgpr_read_b32 v240, a53\n" \
"v_mfma_f32_16x16x32_bf16 a[84:87], a[232:235], v[96:99], a[84:87]\n" \
"s_xor_b32 s73, s73, 0x180\n" \
"v_exp_f32 v59, v59\n" \
"v_mfma_f32_16x16x32_bf16 a[84:87], a[236:239], v[100:103], a[84:87]\n" \
"ds_write_b128 v232, v[148:151] offset:5120\n" \
"v_exp_f32 v60, v60\n" \
"v_cvt_pk_bf16_f32 v52, v56, v57\n" \
"v_exp_f32 v61, v61\n" \
"v_mfma_f32_16x16x32_bf16 a[88:91], a[32:35], v[96:99], a[88:91]\n" \
"ds_read_b128 a[232:235], v233 offset:5120\n" \
"ds_read_b128 a[236:239], v233 offset:13312\n" \
"v_mfma_f32_16x16x32_bf16 a[88:91], a[36:39], v[100:103], a[88:91]\n" \
"ds_read_b128 a[240:243], v233 offset:6144\n" \
"ds_read_b128 a[244:247], v233 offset:14336\n" \
"v_exp_f32 v62, v62\n" \
"v_cvt_pk_bf16_f32 v53, v58, v59\n" \
"v_mfma_f32_16x16x32_bf16 a[120:123], a[32:35], v[112:115], a[120:123]\n" \
"ds_read_b128 v[0:3], v233 offset:20480\n" \
"ds_read_b128 v[4:7], v233 offset:28672\n" \
"v_mfma_f32_16x16x32_bf16 a[120:123], a[36:39], v[116:119], a[120:123]\n" \
"ds_read_b128 v[8:11], v233 offset:21504\n" \
"ds_read_b128 v[12:15], v233 offset:29696\n" \
"v_exp_f32 v63, v63\n" \
"v_cvt_pk_bf16_f32 v54, v60, v61\n" \
"v_add_f32 v251, v251, v240\n" \
"v_mfma_f32_16x16x32_bf16 a[124:127], a[40:43], v[112:115], a[124:127]\n" \
"v_cvt_pk_bf16_f32 v55, v62, v63\n" \
"v_mfma_f32_16x16x32_bf16 a[124:127], a[44:47], v[116:119], a[124:127]\n" \
"ds_write_b128 v232, v[152:155] offset:6144\n" \
"ds_write_b64 v249, v[250:251]\n" \
"v_mfma_f32_16x16x32_bf16 a[92:95], a[40:43], v[96:99], a[92:95]\n" \
"v_exp_f32 v16, v16\n" \
"v_mfma_f32_16x16x32_bf16 a[92:95], a[44:47], v[100:103], a[92:95]\n" \
"s_waitcnt lgkmcnt(4)\n" \
"ds_read_b128 v[96:99], v233 offset:22528\n" \
"ds_read_b128 v[100:103], v233 offset:30720\n" \
"v_exp_f32 v17, v17\n" \
"v_xor_b32 v249, 0x200, v249\n" \
"v_mfma_f32_16x16x32_bf16 a[64:67], a[192:195], v[32:35], a[64:67]\n" \
"ds_read_b128 v[104:107], v233 offset:23552\n" \
"ds_read_b128 v[108:111], v233 offset:31744\n" \
"v_mfma_f32_16x16x32_bf16 a[64:67], a[196:199], v[36:39], a[64:67]\n" \
"v_exp_f32 v18, v18\n" \
"v_exp_f32 v19, v19\n" \
"v_accvgpr_read_b32 v200, a59\n" \
"v_accvgpr_read_b32 v202, a60\n" \
"v_mfma_f32_16x16x32_bf16 a[96:99], a[192:195], v[48:51], a[96:99]\n" \
"v_exp_f32 v20, v20\n" \
"v_mfma_f32_16x16x32_bf16 a[96:99], a[196:199], v[52:55], a[96:99]\n" \
"v_cvt_pk_bf16_f32 v16, v16, v17\n" \
"v_cvt_pk_bf16_f32 v17, v18, v19\n" \
"v_accvgpr_read_b32 v204, a61\n" \
"v_xor_b32 v201, 64, v200\n" \
"v_mfma_f32_16x16x32_bf16 a[100:103], a[200:203], v[48:51], a[100:103]\n" \
"v_exp_f32 v21, v21\n" \
"v_mfma_f32_16x16x32_bf16 a[100:103], a[204:207], v[52:55], a[100:103]\n" \
"ds_write_b128 v232, v[156:159] offset:7168\n" \
"v_exp_f32 v22, v22\n" \
"v_xor_b32 v203, 64, v202\n" \
"v_xor_b32 v222, 32, v204\n" \
"v_mfma_f32_16x16x32_bf16 a[68:71], a[200:203], v[32:35], a[68:71]\n" \
"v_exp_f32 v23, v23\n" \
"v_mfma_f32_16x16x32_bf16 a[68:71], a[204:207], v[36:39], a[68:71]\n" \
"s_mul_i32 s76, s80, 393216\n" \
"s_add_u32 s76, s76, s78\n" \
"v_cvt_pk_bf16_f32 v18, v20, v21\n" \
"v_exp_f32 v24, v24\n" \
"v_add_u32 v209, 81920, v204\n" \
"v_add_u32 v210, 81920, v222\n" \
"v_smfmac_f32_16x16x64_bf16 v[224:227], v[244:247], v[32:39], v243\n" \
"s_waitcnt vmcnt(0) lgkmcnt(0)\n" \
"s_barrier\n" \
"v_mfma_f32_16x16x32_bf16 a[72:75], a[208:211], v[32:35], a[72:75]\n" \
"s_add_u32 m0, s91, 81920\n" \
"ds_read_b64_tr_b16 a[32:33], v209 offset:25600\n" \
"ds_read_b64_tr_b16 a[34:35], v209 offset:28672\n" \
"v_mfma_f32_16x16x32_bf16 a[72:75], a[212:215], v[36:39], a[72:75]\n" \
"buffer_load_dwordx4 v197, s[48:51], s76 offen offset:0 lds\n" \
"v_exp_f32 v25, v25\n" \
"v_mfma_f32_16x16x32_bf16 a[104:107], a[208:211], v[48:51], a[104:107]\n" \
"s_and_b32 s15, %1, 7\n" \
"ds_read_b64_tr_b16 a[36:37], v209 offset:37888\n" \
"ds_read_b64_tr_b16 a[38:39], v209 offset:40960\n" \
"v_mfma_f32_16x16x32_bf16 a[104:107], a[212:215], v[52:55], a[104:107]\n" \
"s_add_u32 s77, s76, 24576\n" \
"ds_read_b64_tr_b16 a[40:41], v209 offset:24576\n" \
"ds_read_b64_tr_b16 a[42:43], v209 offset:27648\n" \
"v_mfma_f32_16x16x32_bf16 a[108:111], a[216:219], v[48:51], a[108:111]\n" \
"buffer_load_dwordx4 v198, s[48:51], s76 offen offset:1024 lds\n" \
"v_mfma_f32_16x16x32_bf16 a[108:111], a[220:223], v[52:55], a[108:111]\n" \
"s_lshl3_add_u32 s14, s14, s15\n" \
"ds_read_b64_tr_b16 a[44:45], v209 offset:36864\n" \
"ds_read_b64_tr_b16 a[46:47], v209 offset:39936\n" \
"v_exp_f32 v26, v26\n" \
"v_cvt_pk_bf16_f32 v19, v22, v23\n" \
"v_mfma_f32_16x16x32_bf16 a[76:79], a[216:219], v[32:35], a[76:79]\n" \
"s_xor_b32 s80, s80, 0x8\n" \
"ds_read_b64_tr_b16 a[48:49], v210 offset:24576\n" \
"ds_read_b64_tr_b16 a[50:51], v210 offset:27648\n" \
"v_mfma_f32_16x16x32_bf16 a[76:79], a[220:223], v[36:39], a[76:79]\n" \
"buffer_load_dwordx4 v199, s[48:51], s76 offen offset:2048 lds\n" \
"v_smfmac_f32_16x16x64_bf16 v[228:231], v[244:247], v[48:55], v243\n" \
"ds_read_b64_tr_b16 a[52:53], v210 offset:36864\n" \
"ds_read_b64_tr_b16 a[54:55], v210 offset:39936\n" \
"v_exp_f32 v27, v27\n" \
"v_mfma_f32_16x16x32_bf16 a[80:83], a[224:227], v[32:35], a[80:83]\n" \
"s_add_u32 m0, m0, 3072\n" \
"ds_read_b128 a[0:3], v202 offset:0\n" \
"ds_read_b128 a[4:7], v203 offset:0\n" \
"v_mfma_f32_16x16x32_bf16 a[80:83], a[228:231], v[36:39], a[80:83]\n" \
"buffer_load_dwordx4 v197, s[48:51], s77 offen offset:0 lds\n" \
"v_mfma_f32_16x16x32_bf16 a[112:115], a[224:227], v[48:51], a[112:115]\n" \
"ds_read_b128 a[8:11], v200 offset:40960\n" \
"ds_read_b128 a[12:15], v201 offset:40960\n" \
"v_mfma_f32_16x16x32_bf16 a[112:115], a[228:231], v[52:55], a[112:115]\n" \
"ds_read_b128 a[16:19], v200 offset:45056\n" \
"ds_read_b128 a[20:23], v201 offset:45056\n" \
"v_exp_f32 v28, v28\n" \
"v_cvt_pk_bf16_f32 v20, v24, v25\n" \
"v_mfma_f32_16x16x32_bf16 a[116:119], a[232:235], v[48:51], a[116:119]\n" \
"buffer_load_dwordx4 v198, s[48:51], s77 offen offset:1024 lds\n" \
"v_mfma_f32_16x16x32_bf16 a[116:119], a[236:239], v[52:55], a[116:119]\n" \
"ds_read_b128 a[24:27], v200 offset:49152\n" \
"ds_read_b128 a[28:31], v201 offset:49152\n" \
"v_mfma_f32_16x16x32_bf16 a[84:87], a[232:235], v[32:35], a[84:87]\n" \
"ds_read_b64 v[250:251], v249\n" \
"ds_read_b128 v[128:131], v248 offset:0\n" \
"v_mfma_f32_16x16x32_bf16 a[84:87], a[236:239], v[36:39], a[84:87]\n" \
"buffer_load_dwordx4 v199, s[48:51], s77 offen offset:2048 lds\n" \
"v_exp_f32 v29, v29\n" \
"v_mfma_f32_16x16x32_bf16 a[88:91], a[240:243], v[32:35], a[88:91]\n" \
"ds_read_b128 v[132:135], v248 offset:1024\n" \
"ds_read_b128 v[136:139], v248 offset:2048\n" \
"v_mfma_f32_16x16x32_bf16 a[88:91], a[244:247], v[36:39], a[88:91]\n" \
"s_add_u32 m0, s79, 65536\n" \
"ds_read_b128 v[140:143], v248 offset:3072\n" \
"ds_read_b128 v[144:147], v248 offset:4096\n" \
"v_exp_f32 v30, v30\n" \
"v_mfma_f32_16x16x32_bf16 a[120:123], a[240:243], v[48:51], a[120:123]\n" \
"buffer_load_dwordx4 v192, s[44:47], s73 offen offset:0 lds\n" \
"v_mfma_f32_16x16x32_bf16 a[120:123], a[244:247], v[52:55], a[120:123]\n" \
"ds_read_b128 v[148:151], v248 offset:5120\n" \
"ds_read_b128 v[152:155], v248 offset:6144\n" \
"v_cvt_pk_bf16_f32 v21, v26, v27\n" \
"v_mfma_f32_16x16x32_bf16 a[124:127], a[248:251], v[48:51], a[124:127]\n" \
"ds_read_b128 v[156:159], v248 offset:7168\n" \
"v_cvt_pk_bf16_f32 v22, v28, v29\n" \
"v_mfma_f32_16x16x32_bf16 a[124:127], a[252:255], v[52:55], a[124:127]\n" \
"buffer_load_dwordx4 v193, s[44:47], s73 offen offset:1024 lds\n" \
"v_exp_f32 v31, v31\n" \
"v_mfma_f32_16x16x32_bf16 a[92:95], a[248:251], v[32:35], a[92:95]\n" \
"v_cvt_pk_bf16_f32 v23, v30, v31\n" \
"v_mov_b32 v218, s60\n" \
"v_mfma_f32_16x16x32_bf16 a[92:95], a[252:255], v[36:39], a[92:95]\n" \
"v_mov_b32 v219, s14\n" \
"v_lshlrev_b32 v220, 28, s61\n" \
"v_mfma_f32_16x16x32_bf16 a[96:99], v[64:67], v[16:19], a[96:99]\n" \
"buffer_load_dwordx4 v192, s[44:47], s73 offen offset:2048 lds\n" \
"v_mfma_f32_16x16x32_bf16 a[96:99], v[68:71], v[20:23], a[96:99]\n" \
"s_lshl3_add_u32 s15, s15, 65593856\n" \
"v_mov_b32 v170, 0x3f80\n" \
"v_mov_b32 v160, 0\n" \
"v_mfma_f32_16x16x32_bf16 a[100:103], v[72:75], v[16:19], a[100:103]\n" \
"s_lshr_b32 s14, %1, 3\n" \
"v_mov_b32 v161, 0\n" \
"v_mov_b32 v164, 0\n" \
"v_mfma_f32_16x16x32_bf16 a[100:103], v[76:79], v[20:23], a[100:103]\n" \
"buffer_load_dwordx4 v193, s[44:47], s73 offen offset:3072 lds\n" \
"v_smfmac_f32_16x16x64_bf16 v[228:231], v[244:247], v[16:23], v243\n" \
"s_cmp_eq_u32 s14, 3\n" \
"s_cselect_b32 s15, s15, 0x20000000\n" \
"v_mov_b32 v165, 0\n" \
"v_mfma_f32_16x16x32_bf16 a[104:107], v[80:83], v[16:19], a[104:107]\n" \
"buffer_store_dwordx2 v[218:219], v220, s[56:59], s15 offen sc1\n" \
"v_mfma_f32_16x16x32_bf16 a[104:107], v[84:87], v[20:23], a[104:107]\n" \
"s_xor_b32 s73, s73, 0x400\n" \
"v_lshrrev_b32 v168, 8, v234\n" \
"v_bfe_u32 v169, v234, 6, 2\n" \
"v_mfma_f32_16x16x32_bf16 a[108:111], v[88:91], v[16:19], a[108:111]\n" \
"v_cmp_eq_u32 s[16:17], v168, v169\n" \
"v_and_b32 v168, 16, v234\n" \
"v_mfma_f32_16x16x32_bf16 a[108:111], v[92:95], v[20:23], a[108:111]\n" \
"v_lshlrev_b32 v168, v168, v170\n" \
"v_cndmask_b32 v168, 0, v168, s[16:17]\n" \
"v_mfma_f32_16x16x32_bf16 a[112:115], v[0:3], v[16:19], a[112:115]\n" \
"Lpg7379_%=:\n" \
"s_cbranch_execz Lcb0_%=\n" \
"v_bfe_i32 v169, v234, 5, 1\n" \
"v_and_b32 v163, v168, v169\n" \
"v_mfma_f32_16x16x32_bf16 a[112:115], v[4:7], v[20:23], a[112:115]\n" \
"v_xor_b32 v162, v168, v163\n" \
"v_mfma_f32_16x16x32_bf16 a[116:119], v[8:11], v[16:19], a[116:119]\n" \
"v_mfma_f32_16x16x32_bf16 a[116:119], v[12:15], v[20:23], a[116:119]\n" \
"v_mfma_f32_16x16x32_bf16 a[120:123], v[96:99], v[16:19], a[120:123]\n" \
"v_mfma_f32_16x16x32_bf16 a[120:123], v[100:103], v[20:23], a[120:123]\n" \
"v_mfma_f32_16x16x32_bf16 a[124:127], v[104:107], v[16:19], a[124:127]\n" \
"v_mfma_f32_16x16x32_bf16 a[124:127], v[108:111], v[20:23], a[124:127]\n" \
"s_waitcnt lgkmcnt(0)\n" \
"s_barrier\n" \
"v_mfma_f32_16x16x32_bf16 v[32:35], a[32:35], a[0:3], 0\n" \
"s_add_u32 m0, s79, 0\n" \
"ds_read_b128 a[192:195], v200 offset:53248\n" \
"v_mfma_f32_16x16x32_bf16 v[32:35], a[36:39], a[4:7], v[32:35]\n" \
"buffer_load_dwordx4 v192, s[44:47], s73 offen offset:0 lds\n" \
"v_mfma_f32_16x16x32_bf16 v[40:43], a[32:35], a[8:11], 0\n" \
"ds_read_b128 a[196:199], v201 offset:53248\n" \
"v_add_u32 v205, 0x10000, v200\n" \
"v_mfma_f32_16x16x32_bf16 v[40:43], a[36:39], a[12:15], v[40:43]\n" \
"buffer_load_dwordx4 v193, s[44:47], s73 offen offset:1024 lds\n" \
"v_mfma_f32_16x16x32_bf16 a[64:67], v[162:165], v[128:131], a[64:67]\n" \
"v_mfma_f32_16x16x32_bf16 a[68:71], v[160:163], v[128:131], a[68:71]\n" \
"v_mfma_f32_16x16x32_bf16 a[72:75], v[162:165], v[132:135], a[72:75]\n" \
"v_mfma_f32_16x16x32_bf16 a[76:79], v[160:163], v[132:135], a[76:79]\n" \
"v_add_f32 v248, v224, v225\n" \
"v_add_f32 v249, v228, v229\n" \
"v_mfma_f32_16x16x32_bf16 v[0:3], a[40:43], a[8:11], 0\n" \
"ds_read_b64_tr_b16 a[200:201], v210 offset:25600\n" \
"ds_read_b64_tr_b16 a[202:203], v210 offset:28672\n" \
"v_mfma_f32_16x16x32_bf16 v[0:3], a[44:47], a[12:15], v[0:3]\n" \
"buffer_load_dwordx4 v192, s[44:47], s73 offen offset:2048 lds\n" \
"v_mfma_f32_16x16x32_bf16 v[4:7], a[48:51], a[8:11], 0\n" \
"ds_read_b64_tr_b16 a[204:205], v210 offset:37888\n" \
"ds_read_b64_tr_b16 a[206:207], v210 offset:40960\n" \
"v_mfma_f32_16x16x32_bf16 v[4:7], a[52:55], a[12:15], v[4:7]\n" \
"buffer_load_dwordx4 v193, s[44:47], s73 offen offset:3072 lds\n" \
"v_mfma_f32_16x16x32_bf16 v[12:15], a[48:51], a[16:19], 0\n" \
"s_xor_b32 s73, s73, 0x500\n" \
"s_mul_i32 s76, s80, 393216\n" \
"s_add_u32 s76, s76, s78\n" \
"s_add_u32 m0, s91, 16384\n" \
"ds_read_b64_tr_b16 a[208:209], v209 offset:26624\n" \
"ds_read_b64_tr_b16 a[210:211], v209 offset:29696\n" \
"v_add_f32 v248, v248, v250\n" \
"v_mfma_f32_16x16x32_bf16 v[12:15], a[52:55], a[20:23], v[12:15]\n" \
"buffer_load_dwordx4 v197, s[48:51], s76 offen offset:0 lds\n" \
"v_mfma_f32_16x16x32_bf16 v[8:11], a[40:43], a[16:19], 0\n" \
"s_add_u32 s77, s76, 24576\n" \
"ds_read_b64_tr_b16 a[212:213], v209 offset:38912\n" \
"ds_read_b64_tr_b16 a[214:215], v209 offset:41984\n" \
"v_mfma_f32_16x16x32_bf16 v[8:11], a[44:47], a[20:23], v[8:11]\n" \
"buffer_load_dwordx4 v198, s[48:51], s76 offen offset:1024 lds\n" \
"v_mfma_f32_16x16x32_bf16 v[48:51], a[32:35], a[16:19], 0\n" \
"s_xor_b32 s80, s80, 0xa\n" \
"ds_read_b64_tr_b16 a[216:217], v210 offset:26624\n" \
"v_add_u32 v206, 0x10000, v201\n" \
"v_mfma_f32_16x16x32_bf16 v[48:51], a[36:39], a[20:23], v[48:51]\n" \
"buffer_load_dwordx4 v199, s[48:51], s76 offen offset:2048 lds\n" \
"v_mfma_f32_16x16x32_bf16 v[56:59], a[32:35], a[24:27], 0\n" \
"s_add_u32 m0, m0, 3072\n" \
"ds_read_b64_tr_b16 a[218:219], v210 offset:29696\n" \
"v_add_u32 v207, 0x20000, v200\n" \
"v_mfma_f32_16x16x32_bf16 v[56:59], a[36:39], a[28:31], v[56:59]\n" \
"buffer_load_dwordx4 v197, s[48:51], s77 offen offset:0 lds\n" \
"v_add_f32 v249, v249, v251\n" \
"v_mfma_f32_16x16x32_bf16 v[16:19], a[40:43], a[24:27], 0\n" \
"ds_read_b64_tr_b16 a[220:221], v210 offset:38912\n" \
"v_add_u32 v208, 0x20000, v201\n" \
"v_mfma_f32_16x16x32_bf16 v[16:19], a[44:47], a[28:31], v[16:19]\n" \
"buffer_load_dwordx4 v198, s[48:51], s77 offen offset:1024 lds\n" \
"v_mfma_f32_16x16x32_bf16 v[20:23], a[48:51], a[24:27], 0\n" \
"s_waitcnt lgkmcnt(2)\n" \
"ds_read_b64_tr_b16 a[222:223], v210 offset:41984\n" \
"v_mfma_f32_16x16x32_bf16 v[20:23], a[52:55], a[28:31], v[20:23]\n" \
"buffer_load_dwordx4 v199, s[48:51], s77 offen offset:2048 lds\n" \
"v_mfma_f32_16x16x32_bf16 v[28:31], a[48:51], a[192:195], 0\n" \
"v_rcp_f32 v232, v248\n" \
"v_mfma_f32_16x16x32_bf16 v[28:31], a[52:55], a[196:199], v[28:31]\n" \
"v_rcp_f32 v233, v249\n" \
"v_add_u32 v240, 0x1000, v236\n" \
"v_mfma_f32_16x16x32_bf16 v[24:27], a[40:43], a[192:195], 0\n" \
"v_add_f32 v224, v248, v249\n" \
"v_mul_f32 v224, 0x43800000, v224\n" \
"v_mfma_f32_16x16x32_bf16 v[24:27], a[44:47], a[196:199], v[24:27]\n" \
"v_add_f32 v248, v232, v233\n" \
"v_fmac_f32 v224, 0x5f800000, v248\n" \
"v_mfma_f32_16x16x32_bf16 v[64:67], a[32:35], a[192:195], 0\n" \
"v_mov_b32 v225, 0\n" \
"v_mov_b32 v226, 0\n" \
"v_mfma_f32_16x16x32_bf16 v[64:67], a[36:39], a[196:199], v[64:67]\n" \
"s_waitcnt vmcnt(11) lgkmcnt(0)\n" \
"s_barrier\n" \
"v_mfma_f32_16x16x32_bf16 v[68:71], a[200:203], a[192:195], 0\n" \
"ds_read_b64_tr_b16 a[32:33], v209 offset:1024\n" \
"ds_read_b64_tr_b16 a[34:35], v209 offset:4096\n" \
"v_mfma_f32_16x16x32_bf16 v[68:71], a[204:207], a[196:199], v[68:71]\n" \
"ds_read_b64_tr_b16 a[36:37], v209 offset:13312\n" \
"ds_read_b64_tr_b16 a[38:39], v209 offset:16384\n" \
"v_mfma_f32_16x16x32_bf16 v[60:63], a[200:203], a[24:27], 0\n" \
"s_add_u32 m0, s79, 40960\n" \
"ds_read_b64_tr_b16 a[40:41], v209 offset:0\n" \
"v_mov_b32 v227, 0\n" \
"v_mfma_f32_16x16x32_bf16 v[60:63], a[204:207], a[28:31], v[60:63]\n" \
"buffer_load_dwordx4 v192, s[44:47], s73 offen offset:0 lds\n" \
"v_accvgpr_read_b32 v112, a64\n" \
"v_mfma_f32_16x16x32_bf16 v[52:55], a[200:203], a[16:19], 0\n" \
"ds_read_b64_tr_b16 a[42:43], v209 offset:3072\n" \
"v_accvgpr_read_b32 v113, a65\n" \
"v_mfma_f32_16x16x32_bf16 v[52:55], a[204:207], a[20:23], v[52:55]\n" \
"ds_read_b64_tr_b16 a[44:45], v209 offset:12288\n" \
"v_accvgpr_read_b32 v114, a66\n" \
"v_mfma_f32_16x16x32_bf16 v[44:47], a[200:203], a[8:11], 0\n" \
"ds_read_b64_tr_b16 a[46:47], v209 offset:15360\n" \
"v_accvgpr_read_b32 v115, a67\n" \
"v_mfma_f32_16x16x32_bf16 v[44:47], a[204:207], a[12:15], v[44:47]\n" \
"buffer_load_dwordx4 v193, s[44:47], s73 offen offset:1024 lds\n" \
"v_mfma_f32_16x16x32_bf16 v[72:75], a[0:3], a[208:211], 0\n" \
"ds_read_b64_tr_b16 a[48:49], v210 offset:0\n" \
"v_accvgpr_read_b32 v116, a68\n" \
"v_mfma_f32_16x16x32_bf16 v[72:75], a[4:7], a[212:215], v[72:75]\n" \
"ds_read_b128 a[0:3], v202 offset:3072\n" \
"v_accvgpr_read_b32 v117, a69\n" \
"v_accvgpr_read_b32 v118, a70\n" \
"v_mfma_f32_16x16x32_bf16 v[80:83], a[8:11], a[208:211], 0\n" \
"ds_read_b128 a[4:7], v203 offset:3072\n" \
"v_accvgpr_read_b32 v119, a71\n" \
"v_mfma_f32_16x16x32_bf16 v[80:83], a[12:15], a[212:215], v[80:83]\n" \
"buffer_load_dwordx4 v192, s[44:47], s73 offen offset:2048 lds\n" \
"v_mfma_f32_16x16x32_bf16 a[80:83], v[162:165], v[136:139], a[80:83]\n" \
"v_mul_f32 v112, v112, v232\n" \
"v_mul_f32 v113, v113, v232\n" \
"v_mfma_f32_16x16x32_bf16 v[84:87], a[8:11], a[216:219], 0\n" \
"ds_read_b64_tr_b16 a[50:51], v210 offset:3072\n" \
"v_mul_f32 v114, v114, v232\n" \
"v_mfma_f32_16x16x32_bf16 v[84:87], a[12:15], a[220:223], v[84:87]\n" \
"ds_read_b128 a[8:11], v205 offset:0\n" \
"v_mul_f32 v115, v115, v232\n" \
"v_mfma_f32_16x16x32_bf16 a[84:87], v[160:163], v[136:139], a[84:87]\n" \
"v_mul_f32 v116, v116, v232\n" \
"v_mul_f32 v117, v117, v232\n" \
"v_mfma_f32_16x16x32_bf16 v[92:95], a[16:19], a[216:219], 0\n" \
"ds_read_b128 a[12:15], v206 offset:0\n" \
"v_mul_f32 v118, v118, v232\n" \
"v_mfma_f32_16x16x32_bf16 v[92:95], a[20:23], a[220:223], v[92:95]\n" \
"buffer_load_dwordx4 v193, s[44:47], s73 offen offset:3072 lds\n" \
"v_mul_f32 v119, v119, v232\n" \
"v_mfma_f32_16x16x32_bf16 v[88:91], a[16:19], a[208:211], 0\n" \
"s_xor_b32 s73, s73, 0x180\n" \
"s_mul_i32 s76, s80, 393216\n" \
"ds_read_b64_tr_b16 a[52:53], v210 offset:12288\n" \
"v_mfma_f32_16x16x32_bf16 v[88:91], a[20:23], a[212:215], v[88:91]\n" \
"s_add_u32 s76, s76, s78\n" \
"ds_read_b128 a[16:19], v205 offset:4096\n" \
"v_cvt_pk_bf16_f32 v120, v112, v113\n" \
"v_mfma_f32_16x16x32_bf16 v[96:99], a[24:27], a[208:211], 0\n" \
"s_add_u32 m0, s91, 106496\n" \
"ds_read_b128 a[20:23], v206 offset:4096\n" \
"v_cvt_pk_bf16_f32 v121, v114, v115\n" \
"v_mfma_f32_16x16x32_bf16 v[96:99], a[28:31], a[212:215], v[96:99]\n" \
"buffer_load_dwordx4 v197, s[48:51], s76 offen offset:0 lds\n" \
"v_mfma_f32_16x16x32_bf16 v[100:103], a[24:27], a[216:219], 0\n" \
"ds_read_b64_tr_b16 a[54:55], v210 offset:15360\n" \
"v_cvt_pk_bf16_f32 v122, v116, v117\n" \
"v_mfma_f32_16x16x32_bf16 v[100:103], a[28:31], a[220:223], v[100:103]\n" \
"ds_read_b128 a[24:27], v205 offset:8192\n" \
"v_cvt_pk_bf16_f32 v123, v118, v119\n" \
"v_mfma_f32_16x16x32_bf16 v[108:111], a[192:195], a[216:219], 0\n" \
"s_add_u32 s77, s76, 24576\n" \
"ds_read_b128 a[28:31], v206 offset:8192\n" \
"v_mfma_f32_16x16x32_bf16 v[108:111], a[196:199], a[220:223], v[108:111]\n" \
"buffer_load_dwordx4 v198, s[48:51], s76 offen offset:1024 lds\n" \
"v_mfma_f32_16x16x32_bf16 v[104:107], a[192:195], a[208:211], 0\n" \
"s_waitcnt lgkmcnt(3)\n" \
"v_mfma_f32_16x16x32_bf16 v[104:107], a[196:199], a[212:215], v[104:107]\n" \
"buffer_store_dwordx4 v[120:123], v236, s[56:59], s88 offen offset:0 sc1\n" \
"v_mfma_f32_16x16x32_bf16 v[32:35], a[32:35], a[0:3], v[32:35]\n" \
"s_xor_b32 s80, s80, 0x3\n" \
"ds_read_b128 a[192:195], v205 offset:12288\n" \
"v_mfma_f32_16x16x32_bf16 v[32:35], a[36:39], a[4:7], v[32:35]\n" \
"buffer_load_dwordx4 v199, s[48:51], s76 offen offset:2048 lds\n" \
"v_mfma_f32_16x16x32_bf16 v[224:227], v[244:247], v[120:123], v[224:227]\n" \
"v_accvgpr_read_b32 v112, a72\n" \
"v_accvgpr_read_b32 v113, a73\n" \
"v_accvgpr_read_b32 v114, a74\n" \
"v_mfma_f32_16x16x32_bf16 v[40:43], a[32:35], a[8:11], v[40:43]\n" \
"ds_read_b128 a[196:199], v206 offset:12288\n" \
"v_accvgpr_read_b32 v115, a75\n" \
"v_mfma_f32_16x16x32_bf16 v[40:43], a[36:39], a[12:15], v[40:43]\n" \
"s_waitcnt lgkmcnt(2)\n" \
"ds_read_b64_tr_b16 a[200:201], v210 offset:1024\n" \
"v_accvgpr_read_b32 v116, a76\n" \
"v_accvgpr_read_b32 v117, a77\n" \
"v_mfma_f32_16x16x32_bf16 v[0:3], a[40:43], a[8:11], v[0:3]\n" \
"s_add_u32 m0, m0, 3072\n" \
"ds_read_b64_tr_b16 a[202:203], v210 offset:4096\n" \
"v_accvgpr_read_b32 v118, a78\n" \
"v_mfma_f32_16x16x32_bf16 v[0:3], a[44:47], a[12:15], v[0:3]\n" \
"buffer_load_dwordx4 v197, s[48:51], s77 offen offset:0 lds\n" \
"v_mfma_f32_16x16x32_bf16 v[4:7], a[48:51], a[8:11], v[4:7]\n" \
"ds_read_b64_tr_b16 a[204:205], v210 offset:13312\n" \
"v_accvgpr_read_b32 v119, a79\n" \
"v_mfma_f32_16x16x32_bf16 v[4:7], a[52:55], a[12:15], v[4:7]\n" \
"ds_read_b64_tr_b16 a[206:207], v210 offset:16384\n" \
"v_mfma_f32_16x16x32_bf16 a[88:91], v[162:165], v[140:143], a[88:91]\n" \
"v_mul_f32 v112, v112, v232\n" \
"v_mul_f32 v113, v113, v232\n" \
"v_mfma_f32_16x16x32_bf16 v[12:15], a[48:51], a[16:19], v[12:15]\n" \
"ds_read_b64_tr_b16 a[208:209], v209 offset:2048\n" \
"v_mul_f32 v114, v114, v232\n" \
"v_mfma_f32_16x16x32_bf16 v[12:15], a[52:55], a[20:23], v[12:15]\n" \
"buffer_load_dwordx4 v198, s[48:51], s77 offen offset:1024 lds\n" \
"v_mul_f32 v115, v115, v232\n" \
"v_mfma_f32_16x16x32_bf16 a[92:95], v[160:163], v[140:143], a[92:95]\n" \
"v_mul_f32 v116, v116, v232\n" \
"v_mul_f32 v117, v117, v232\n" \
"v_mfma_f32_16x16x32_bf16 v[8:11], a[40:43], a[16:19], v[8:11]\n" \
"ds_read_b64_tr_b16 a[210:211], v209 offset:5120\n" \
"v_mul_f32 v118, v118, v232\n" \
"v_mfma_f32_16x16x32_bf16 v[8:11], a[44:47], a[20:23], v[8:11]\n" \
"ds_read_b64_tr_b16 a[212:213], v209 offset:14336\n" \
"v_mul_f32 v119, v119, v232\n" \
"v_mfma_f32_16x16x32_bf16 v[48:51], a[32:35], a[16:19], v[48:51]\n" \
"ds_read_b64_tr_b16 a[214:215], v209 offset:17408\n" \
"v_cvt_pk_bf16_f32 v124, v112, v113\n" \
"v_mfma_f32_16x16x32_bf16 v[48:51], a[36:39], a[20:23], v[48:51]\n" \
"buffer_load_dwordx4 v199, s[48:51], s77 offen offset:2048 lds\n" \
"v_mfma_f32_16x16x32_bf16 v[56:59], a[32:35], a[24:27], v[56:59]\n" \
"ds_read_b64_tr_b16 a[216:217], v210 offset:2048\n" \
"v_cvt_pk_bf16_f32 v125, v114, v115\n" \
"v_mfma_f32_16x16x32_bf16 v[56:59], a[36:39], a[28:31], v[56:59]\n" \
"ds_read_b64_tr_b16 a[218:219], v210 offset:5120\n" \
"v_cvt_pk_bf16_f32 v126, v116, v117\n" \
"v_mfma_f32_16x16x32_bf16 v[16:19], a[40:43], a[24:27], v[16:19]\n" \
"ds_read_b64_tr_b16 a[220:221], v210 offset:14336\n" \
"v_cvt_pk_bf16_f32 v127, v118, v119\n" \
"v_mfma_f32_16x16x32_bf16 v[16:19], a[44:47], a[28:31], v[16:19]\n" \
"buffer_store_dwordx4 v[124:127], v236, s[56:59], s88 offen offset:2048 sc1\n" \
"v_mfma_f32_16x16x32_bf16 v[20:23], a[48:51], a[24:27], v[20:23]\n" \
"ds_read_b64_tr_b16 a[222:223], v210 offset:17408\n" \
"v_mfma_f32_16x16x32_bf16 v[20:23], a[52:55], a[28:31], v[20:23]\n" \
"s_waitcnt lgkmcnt(3)\n" \
"v_mfma_f32_16x16x32_bf16 v[224:227], v[244:247], v[124:127], v[224:227]\n" \
"v_accvgpr_read_b32 v112, a80\n" \
"v_accvgpr_read_b32 v113, a81\n" \
"v_accvgpr_read_b32 v114, a82\n" \
"v_mfma_f32_16x16x32_bf16 v[28:31], a[48:51], a[192:195], v[28:31]\n" \
"v_accvgpr_read_b32 v115, a83\n" \
"v_accvgpr_read_b32 v116, a84\n" \
"v_mfma_f32_16x16x32_bf16 v[28:31], a[52:55], a[196:199], v[28:31]\n" \
"v_accvgpr_read_b32 v117, a85\n" \
"v_accvgpr_read_b32 v118, a86\n" \
"v_mfma_f32_16x16x32_bf16 v[24:27], a[40:43], a[192:195], v[24:27]\n" \
"v_accvgpr_read_b32 v119, a87\n" \
"v_mfma_f32_16x16x32_bf16 v[24:27], a[44:47], a[196:199], v[24:27]\n" \
"v_mfma_f32_16x16x32_bf16 a[96:99], v[162:165], v[144:147], a[96:99]\n" \
"v_mul_f32 v112, v112, v232\n" \
"v_mul_f32 v113, v113, v232\n" \
"v_mul_f32 v114, v114, v232\n" \
"v_mfma_f32_16x16x32_bf16 v[64:67], a[32:35], a[192:195], v[64:67]\n" \
"v_mul_f32 v115, v115, v232\n" \
"v_mfma_f32_16x16x32_bf16 v[64:67], a[36:39], a[196:199], v[64:67]\n" \
"v_mfma_f32_16x16x32_bf16 a[100:103], v[160:163], v[144:147], a[100:103]\n" \
"s_waitcnt vmcnt(12) lgkmcnt(0)\n" \
"s_barrier\n" \
"v_mfma_f32_16x16x32_bf16 v[68:71], a[200:203], a[192:195], v[68:71]\n" \
"ds_read_b64_tr_b16 a[32:33], v204 offset:17408\n" \
"ds_read_b64_tr_b16 a[34:35], v204 offset:20480\n" \
"v_mfma_f32_16x16x32_bf16 v[68:71], a[204:207], a[196:199], v[68:71]\n" \
"ds_read_b64_tr_b16 a[36:37], v204 offset:29696\n" \
"ds_read_b64_tr_b16 a[38:39], v204 offset:32768\n" \
"v_mfma_f32_16x16x32_bf16 v[60:63], a[200:203], a[24:27], v[60:63]\n" \
"s_add_u32 m0, s79, 65536\n" \
"ds_read_b64_tr_b16 a[40:41], v204 offset:16384\n" \
"v_mul_f32 v116, v116, v232\n" \
"v_mfma_f32_16x16x32_bf16 v[60:63], a[204:207], a[28:31], v[60:63]\n" \
"buffer_load_dwordx4 v192, s[44:47], s73 offen offset:0 lds\n" \
"v_mul_f32 v117, v117, v232\n" \
"v_mfma_f32_16x16x32_bf16 v[52:55], a[200:203], a[16:19], v[52:55]\n" \
"ds_read_b64_tr_b16 a[42:43], v204 offset:19456\n" \
"v_mul_f32 v118, v118, v232\n" \
"v_mfma_f32_16x16x32_bf16 v[52:55], a[204:207], a[20:23], v[52:55]\n" \
"ds_read_b64_tr_b16 a[44:45], v204 offset:28672\n" \
"v_mul_f32 v119, v119, v232\n" \
"v_mfma_f32_16x16x32_bf16 v[44:47], a[200:203], a[8:11], v[44:47]\n" \
"ds_read_b64_tr_b16 a[46:47], v204 offset:31744\n" \
"v_cvt_pk_bf16_f32 v120, v112, v113\n" \
"v_mfma_f32_16x16x32_bf16 v[44:47], a[204:207], a[12:15], v[44:47]\n" \
"buffer_load_dwordx4 v193, s[44:47], s73 offen offset:1024 lds\n" \
"v_mfma_f32_16x16x32_bf16 v[72:75], a[0:3], a[208:211], v[72:75]\n" \
"ds_read_b64_tr_b16 a[48:49], v222 offset:16384\n" \
"v_cvt_pk_bf16_f32 v121, v114, v115\n" \
"v_mfma_f32_16x16x32_bf16 v[72:75], a[4:7], a[212:215], v[72:75]\n" \
"ds_read_b128 a[0:3], v202 offset:19456\n" \
"v_cvt_pk_bf16_f32 v122, v116, v117\n" \
"v_mfma_f32_16x16x32_bf16 v[80:83], a[8:11], a[208:211], v[80:83]\n" \
"ds_read_b128 a[4:7], v203 offset:19456\n" \
"v_cvt_pk_bf16_f32 v123, v118, v119\n" \
"v_mfma_f32_16x16x32_bf16 v[80:83], a[12:15], a[212:215], v[80:83]\n" \
"buffer_load_dwordx4 v192, s[44:47], s73 offen offset:2048 lds\n" \
"v_mfma_f32_16x16x32_bf16 v[84:87], a[8:11], a[216:219], v[84:87]\n" \
"ds_read_b64_tr_b16 a[50:51], v222 offset:19456\n" \
"v_mfma_f32_16x16x32_bf16 v[84:87], a[12:15], a[220:223], v[84:87]\n" \
"ds_read_b128 a[8:11], v200 offset:0\n" \
"buffer_store_dwordx4 v[120:123], v240, s[56:59], s88 offen offset:0 sc1\n" \
"v_mfma_f32_16x16x32_bf16 v[92:95], a[16:19], a[216:219], v[92:95]\n" \
"ds_read_b128 a[12:15], v201 offset:0\n" \
"v_mfma_f32_16x16x32_bf16 v[92:95], a[20:23], a[220:223], v[92:95]\n" \
"buffer_load_dwordx4 v193, s[44:47], s73 offen offset:3072 lds\n" \
"v_mfma_f32_16x16x32_bf16 v[224:227], v[244:247], v[120:123], v[224:227]\n" \
"v_accvgpr_read_b32 v112, a88\n" \
"v_accvgpr_read_b32 v113, a89\n" \
"v_accvgpr_read_b32 v114, a90\n" \
"v_mfma_f32_16x16x32_bf16 v[88:91], a[16:19], a[208:211], v[88:91]\n" \
"s_xor_b32 s73, s73, 0x300\n" \
"s_mul_i32 s76, s80, 393216\n" \
"ds_read_b64_tr_b16 a[52:53], v222 offset:28672\n" \
"v_mfma_f32_16x16x32_bf16 v[88:91], a[20:23], a[212:215], v[88:91]\n" \
"s_add_u32 s76, s76, s78\n" \
"ds_read_b128 a[16:19], v200 offset:4096\n" \
"v_accvgpr_read_b32 v115, a91\n" \
"v_accvgpr_read_b32 v116, a92\n" \
"v_mfma_f32_16x16x32_bf16 v[96:99], a[24:27], a[208:211], v[96:99]\n" \
"s_add_u32 m0, s91, 81920\n" \
"ds_read_b128 a[20:23], v201 offset:4096\n" \
"v_accvgpr_read_b32 v117, a93\n" \
"v_mfma_f32_16x16x32_bf16 v[96:99], a[28:31], a[212:215], v[96:99]\n" \
"buffer_load_dwordx4 v197, s[48:51], s76 offen offset:0 lds\n" \
"v_mfma_f32_16x16x32_bf16 v[100:103], a[24:27], a[216:219], v[100:103]\n" \
"ds_read_b64_tr_b16 a[54:55], v222 offset:31744\n" \
"v_accvgpr_read_b32 v118, a94\n" \
"v_mfma_f32_16x16x32_bf16 v[100:103], a[28:31], a[220:223], v[100:103]\n" \
"ds_read_b128 a[24:27], v200 offset:8192\n" \
"v_accvgpr_read_b32 v119, a95\n" \
"v_mfma_f32_16x16x32_bf16 a[104:107], v[162:165], v[148:151], a[104:107]\n" \
"v_mul_f32 v112, v112, v232\n" \
"v_mul_f32 v113, v113, v232\n" \
"v_mfma_f32_16x16x32_bf16 v[108:111], a[192:195], a[216:219], v[108:111]\n" \
"s_add_u32 s77, s76, 24576\n" \
"ds_read_b128 a[28:31], v201 offset:8192\n" \
"v_mul_f32 v114, v114, v232\n" \
"v_mfma_f32_16x16x32_bf16 v[108:111], a[196:199], a[220:223], v[108:111]\n" \
"buffer_load_dwordx4 v198, s[48:51], s76 offen offset:1024 lds\n" \
"v_mul_f32 v115, v115, v232\n" \
"v_mfma_f32_16x16x32_bf16 a[108:111], v[160:163], v[148:151], a[108:111]\n" \
"v_mul_f32 v116, v116, v232\n" \
"v_mul_f32 v117, v117, v232\n" \
"v_mfma_f32_16x16x32_bf16 v[104:107], a[192:195], a[208:211], v[104:107]\n" \
"v_mul_f32 v118, v118, v232\n" \
"v_mul_f32 v119, v119, v232\n" \
"v_mfma_f32_16x16x32_bf16 v[104:107], a[196:199], a[212:215], v[104:107]\n" \
"s_waitcnt lgkmcnt(2)\n" \
"v_cvt_pk_bf16_f32 v124, v112, v113\n" \
"v_cvt_pk_bf16_f32 v125, v114, v115\n" \
"v_mfma_f32_16x16x32_bf16 v[32:35], a[32:35], a[0:3], v[32:35]\n" \
"s_xor_b32 s80, s80, 0x6\n" \
"ds_read_b128 a[192:195], v200 offset:12288\n" \
"v_cvt_pk_bf16_f32 v126, v116, v117\n" \
"v_mfma_f32_16x16x32_bf16 v[32:35], a[36:39], a[4:7], v[32:35]\n" \
"buffer_load_dwordx4 v199, s[48:51], s76 offen offset:2048 lds\n" \
"v_mfma_f32_16x16x32_bf16 v[40:43], a[32:35], a[8:11], v[40:43]\n" \
"ds_read_b128 a[196:199], v201 offset:12288\n" \
"v_cvt_pk_bf16_f32 v127, v118, v119\n" \
"v_mfma_f32_16x16x32_bf16 v[40:43], a[36:39], a[12:15], v[40:43]\n" \
"ds_read_b64_tr_b16 a[200:201], v222 offset:17408\n" \
"buffer_store_dwordx4 v[124:127], v240, s[56:59], s88 offen offset:2048 sc1\n" \
"v_mfma_f32_16x16x32_bf16 v[0:3], a[40:43], a[8:11], v[0:3]\n" \
"s_add_u32 m0, m0, 3072\n" \
"ds_read_b64_tr_b16 a[202:203], v222 offset:20480\n" \
"v_mfma_f32_16x16x32_bf16 v[0:3], a[44:47], a[12:15], v[0:3]\n" \
"buffer_load_dwordx4 v197, s[48:51], s77 offen offset:0 lds\n" \
"v_mfma_f32_16x16x32_bf16 v[224:227], v[244:247], v[124:127], v[224:227]\n" \
"s_add_u32 s82, s88, 532480\n" \
"v_accvgpr_read_b32 v112, a96\n" \
"v_accvgpr_read_b32 v113, a97\n" \
"v_accvgpr_read_b32 v114, a98\n" \
"v_mfma_f32_16x16x32_bf16 v[4:7], a[48:51], a[8:11], v[4:7]\n" \
"ds_read_b64_tr_b16 a[204:205], v222 offset:29696\n" \
"v_accvgpr_read_b32 v115, a99\n" \
"v_mfma_f32_16x16x32_bf16 v[4:7], a[52:55], a[12:15], v[4:7]\n" \
"ds_read_b64_tr_b16 a[206:207], v222 offset:32768\n" \
"v_accvgpr_read_b32 v116, a100\n" \
"v_mfma_f32_16x16x32_bf16 v[12:15], a[48:51], a[16:19], v[12:15]\n" \
"ds_read_b64_tr_b16 a[208:209], v204 offset:18432\n" \
"v_accvgpr_read_b32 v117, a101\n" \
"v_mfma_f32_16x16x32_bf16 v[12:15], a[52:55], a[20:23], v[12:15]\n" \
"buffer_load_dwordx4 v198, s[48:51], s77 offen offset:1024 lds\n" \
"v_mfma_f32_16x16x32_bf16 v[8:11], a[40:43], a[16:19], v[8:11]\n" \
"ds_read_b64_tr_b16 a[210:211], v204 offset:21504\n" \
"v_accvgpr_read_b32 v118, a102\n" \
"v_mfma_f32_16x16x32_bf16 v[8:11], a[44:47], a[20:23], v[8:11]\n" \
"ds_read_b64_tr_b16 a[212:213], v204 offset:30720\n" \
"v_accvgpr_read_b32 v119, a103\n" \
"v_mfma_f32_16x16x32_bf16 a[112:115], v[162:165], v[152:155], a[112:115]\n" \
"v_mul_f32 v112, v112, v233\n" \
"v_mul_f32 v113, v113, v233\n" \
"v_mul_f32 v114, v114, v233\n" \
"v_mfma_f32_16x16x32_bf16 v[48:51], a[32:35], a[16:19], v[48:51]\n" \
"s_waitcnt lgkmcnt(3)\n" \
"ds_read_b64_tr_b16 a[214:215], v204 offset:33792\n" \
"v_mul_f32 v115, v115, v233\n" \
"v_mfma_f32_16x16x32_bf16 v[48:51], a[36:39], a[20:23], v[48:51]\n" \
"buffer_load_dwordx4 v199, s[48:51], s77 offen offset:2048 lds\n" \
"v_mfma_f32_16x16x32_bf16 a[116:119], v[160:163], v[152:155], a[116:119]\n" \
"v_mul_f32 v116, v116, v233\n" \
"v_mul_f32 v117, v117, v233\n" \
"v_mfma_f32_16x16x32_bf16 v[56:59], a[32:35], a[24:27], v[56:59]\n" \
"ds_read_b64_tr_b16 a[216:217], v222 offset:18432\n" \
"v_mul_f32 v118, v118, v233\n" \
"v_mfma_f32_16x16x32_bf16 v[56:59], a[36:39], a[28:31], v[56:59]\n" \
"ds_read_b64_tr_b16 a[218:219], v222 offset:21504\n" \
"v_mul_f32 v119, v119, v233\n" \
"v_mfma_f32_16x16x32_bf16 v[16:19], a[40:43], a[24:27], v[16:19]\n" \
"ds_read_b64_tr_b16 a[220:221], v222 offset:30720\n" \
"v_cvt_pk_bf16_f32 v120, v112, v113\n" \
"v_mfma_f32_16x16x32_bf16 v[16:19], a[44:47], a[28:31], v[16:19]\n" \
"v_cvt_pk_bf16_f32 v121, v114, v115\n" \
"v_cvt_pk_bf16_f32 v122, v116, v117\n" \
"v_cvt_pk_bf16_f32 v123, v118, v119\n" \
"v_mfma_f32_16x16x32_bf16 v[20:23], a[48:51], a[24:27], v[20:23]\n" \
"ds_read_b64_tr_b16 a[222:223], v222 offset:33792\n" \
"v_mfma_f32_16x16x32_bf16 v[20:23], a[52:55], a[28:31], v[20:23]\n" \
"buffer_store_dwordx4 v[120:123], v236, s[56:59], s82 offen offset:0 sc1\n" \
"v_mfma_f32_16x16x32_bf16 v[28:31], a[48:51], a[192:195], v[28:31]\n" \
"v_mfma_f32_16x16x32_bf16 v[28:31], a[52:55], a[196:199], v[28:31]\n" \
"v_mfma_f32_16x16x32_bf16 v[224:227], v[244:247], v[120:123], v[224:227]\n" \
"v_accvgpr_read_b32 v112, a104\n" \
"v_accvgpr_read_b32 v113, a105\n" \
"v_mfma_f32_16x16x32_bf16 v[24:27], a[40:43], a[192:195], v[24:27]\n" \
"v_accvgpr_read_b32 v114, a106\n" \
"v_accvgpr_read_b32 v115, a107\n" \
"v_mfma_f32_16x16x32_bf16 v[24:27], a[44:47], a[196:199], v[24:27]\n" \
"v_accvgpr_read_b32 v116, a108\n" \
"v_accvgpr_read_b32 v117, a109\n" \
"v_mfma_f32_16x16x32_bf16 v[64:67], a[32:35], a[192:195], v[64:67]\n" \
"v_accvgpr_read_b32 v118, a110\n" \
"v_accvgpr_read_b32 v119, a111\n" \
"v_mfma_f32_16x16x32_bf16 v[64:67], a[36:39], a[196:199], v[64:67]\n" \
"v_mfma_f32_16x16x32_bf16 a[120:123], v[162:165], v[156:159], a[120:123]\n" \
"v_mul_f32 v112, v112, v233\n" \
"s_waitcnt vmcnt(14) lgkmcnt(0)\n" \
"s_barrier\n" \
"v_mfma_f32_16x16x32_bf16 v[68:71], a[200:203], a[192:195], v[68:71]\n" \
"ds_read_b64_tr_b16 a[32:33], v209 offset:25600\n" \
"ds_read_b64_tr_b16 a[34:35], v209 offset:28672\n" \
"v_mfma_f32_16x16x32_bf16 v[68:71], a[204:207], a[196:199], v[68:71]\n" \
"ds_read_b64_tr_b16 a[36:37], v209 offset:37888\n" \
"ds_read_b64_tr_b16 a[38:39], v209 offset:40960\n" \
"v_mfma_f32_16x16x32_bf16 v[60:63], a[200:203], a[24:27], v[60:63]\n" \
"s_add_u32 m0, s79, 0\n" \
"ds_read_b64_tr_b16 a[40:41], v209 offset:24576\n" \
"v_mul_f32 v113, v113, v233\n" \
"v_mfma_f32_16x16x32_bf16 v[60:63], a[204:207], a[28:31], v[60:63]\n" \
"buffer_load_dwordx4 v192, s[44:47], s73 offen offset:0 lds\n" \
"v_mfma_f32_16x16x32_bf16 v[52:55], a[200:203], a[16:19], v[52:55]\n" \
"ds_read_b64_tr_b16 a[42:43], v209 offset:27648\n" \
"v_mul_f32 v114, v114, v233\n" \
"v_mfma_f32_16x16x32_bf16 v[52:55], a[204:207], a[20:23], v[52:55]\n" \
"ds_read_b64_tr_b16 a[44:45], v209 offset:36864\n" \
"v_mul_f32 v115, v115, v233\n" \
"v_mfma_f32_16x16x32_bf16 a[124:127], v[160:163], v[156:159], a[124:127]\n" \
"v_mul_f32 v116, v116, v233\n" \
"v_mul_f32 v117, v117, v233\n" \
"v_mul_f32 v118, v118, v233\n" \
"v_mfma_f32_16x16x32_bf16 v[44:47], a[200:203], a[8:11], v[44:47]\n" \
"ds_read_b64_tr_b16 a[46:47], v209 offset:39936\n" \
"v_mul_f32 v119, v119, v233\n" \
"v_mfma_f32_16x16x32_bf16 v[44:47], a[204:207], a[12:15], v[44:47]\n" \
"buffer_load_dwordx4 v193, s[44:47], s73 offen offset:1024 lds\n" \
"v_mfma_f32_16x16x32_bf16 v[72:75], a[0:3], a[208:211], v[72:75]\n" \
"ds_read_b64_tr_b16 a[48:49], v210 offset:24576\n" \
"v_cvt_pk_bf16_f32 v124, v112, v113\n" \
"v_mfma_f32_16x16x32_bf16 v[72:75], a[4:7], a[212:215], v[72:75]\n" \
"ds_read_b128 a[0:3], v202 offset:1024\n" \
"v_cvt_pk_bf16_f32 v125, v114, v115\n" \
"v_mfma_f32_16x16x32_bf16 v[80:83], a[8:11], a[208:211], v[80:83]\n" \
"ds_read_b128 a[4:7], v203 offset:1024\n" \
"v_cvt_pk_bf16_f32 v126, v116, v117\n" \
"v_mfma_f32_16x16x32_bf16 v[80:83], a[12:15], a[212:215], v[80:83]\n" \
"buffer_load_dwordx4 v192, s[44:47], s73 offen offset:2048 lds\n" \
"v_cvt_pk_bf16_f32 v127, v118, v119\n" \
"v_mfma_f32_16x16x32_bf16 v[84:87], a[8:11], a[216:219], v[84:87]\n" \
"ds_read_b64_tr_b16 a[50:51], v210 offset:27648\n" \
"v_mfma_f32_16x16x32_bf16 v[84:87], a[12:15], a[220:223], v[84:87]\n" \
"ds_read_b128 a[8:11], v200 offset:40960\n" \
"buffer_store_dwordx4 v[124:127], v236, s[56:59], s82 offen offset:2048 sc1\n" \
"v_mfma_f32_16x16x32_bf16 v[92:95], a[16:19], a[216:219], v[92:95]\n" \
"ds_read_b128 a[12:15], v201 offset:40960\n" \
"v_mfma_f32_16x16x32_bf16 v[92:95], a[20:23], a[220:223], v[92:95]\n" \
"buffer_load_dwordx4 v193, s[44:47], s73 offen offset:3072 lds\n" \
"v_mfma_f32_16x16x32_bf16 v[224:227], v[244:247], v[124:127], v[224:227]\n" \
"v_accvgpr_read_b32 v112, a112\n" \
"v_accvgpr_read_b32 v113, a113\n" \
"v_mfma_f32_16x16x32_bf16 v[88:91], a[16:19], a[208:211], v[88:91]\n" \
"s_xor_b32 s73, s73, 0x80\n" \
"s_mul_i32 s76, s80, 393216\n" \
"ds_read_b64_tr_b16 a[52:53], v210 offset:36864\n" \
"v_mfma_f32_16x16x32_bf16 v[88:91], a[20:23], a[212:215], v[88:91]\n" \
"s_add_u32 s76, s76, s78\n" \
"ds_read_b128 a[16:19], v200 offset:45056\n" \
"v_accvgpr_read_b32 v114, a114\n" \
"v_mfma_f32_16x16x32_bf16 v[96:99], a[24:27], a[208:211], v[96:99]\n" \
"s_add_u32 m0, s91, 16384\n" \
"ds_read_b128 a[20:23], v201 offset:45056\n" \
"v_accvgpr_read_b32 v115, a115\n" \
"v_mfma_f32_16x16x32_bf16 v[96:99], a[28:31], a[212:215], v[96:99]\n" \
"buffer_load_dwordx4 v197, s[48:51], s76 offen offset:0 lds\n" \
"v_accvgpr_read_b32 v116, a116\n" \
"v_mfma_f32_16x16x32_bf16 v[100:103], a[24:27], a[216:219], v[100:103]\n" \
"ds_read_b64_tr_b16 a[54:55], v210 offset:39936\n" \
"v_accvgpr_read_b32 v117, a117\n" \
"v_mfma_f32_16x16x32_bf16 v[100:103], a[28:31], a[220:223], v[100:103]\n" \
"ds_read_b128 a[24:27], v200 offset:49152\n" \
"v_accvgpr_read_b32 v118, a118\n" \
"v_mfma_f32_16x16x32_bf16 v[108:111], a[192:195], a[216:219], v[108:111]\n" \
"s_add_u32 s77, s76, 24576\n" \
"ds_read_b128 a[28:31], v201 offset:49152\n" \
"v_accvgpr_read_b32 v119, a119\n" \
"v_mfma_f32_16x16x32_bf16 v[108:111], a[196:199], a[220:223], v[108:111]\n" \
"buffer_load_dwordx4 v198, s[48:51], s76 offen offset:1024 lds\n" \
"v_mfma_f32_16x16x32_bf16 v[104:107], a[192:195], a[208:211], v[104:107]\n" \
"v_mul_f32 v112, v112, v233\n" \
"v_mul_f32 v113, v113, v233\n" \
"v_mfma_f32_16x16x32_bf16 v[104:107], a[196:199], a[212:215], v[104:107]\n" \
"s_waitcnt lgkmcnt(2)\n" \
"v_mul_f32 v114, v114, v233\n" \
"v_mul_f32 v115, v115, v233\n" \
"v_mul_f32 v116, v116, v233\n" \
"v_mfma_f32_16x16x32_bf16 v[32:35], a[32:35], a[0:3], v[32:35]\n" \
"s_xor_b32 s80, s80, 0x1\n" \
"ds_read_b128 a[192:195], v200 offset:53248\n" \
"v_mul_f32 v117, v117, v233\n" \
"v_mfma_f32_16x16x32_bf16 v[32:35], a[36:39], a[4:7], v[32:35]\n" \
"buffer_load_dwordx4 v199, s[48:51], s76 offen offset:2048 lds\n" \
"v_mfma_f32_16x16x32_bf16 v[40:43], a[32:35], a[8:11], v[40:43]\n" \
"ds_read_b128 a[196:199], v201 offset:53248\n" \
"v_mul_f32 v118, v118, v233\n" \
"v_mfma_f32_16x16x32_bf16 v[40:43], a[36:39], a[12:15], v[40:43]\n" \
"ds_read_b64_tr_b16 a[200:201], v210 offset:25600\n" \
"v_mul_f32 v119, v119, v233\n" \
"v_mfma_f32_16x16x32_bf16 v[0:3], a[40:43], a[8:11], v[0:3]\n" \
"s_add_u32 m0, m0, 3072\n" \
"ds_read_b64_tr_b16 a[202:203], v210 offset:28672\n" \
"v_cvt_pk_bf16_f32 v120, v112, v113\n" \
"v_mfma_f32_16x16x32_bf16 v[0:3], a[44:47], a[12:15], v[0:3]\n" \
"buffer_load_dwordx4 v197, s[48:51], s77 offen offset:0 lds\n" \
"v_cvt_pk_bf16_f32 v121, v114, v115\n" \
"v_mfma_f32_16x16x32_bf16 v[4:7], a[48:51], a[8:11], v[4:7]\n" \
"ds_read_b64_tr_b16 a[204:205], v210 offset:37888\n" \
"v_cvt_pk_bf16_f32 v122, v116, v117\n" \
"v_mfma_f32_16x16x32_bf16 v[4:7], a[52:55], a[12:15], v[4:7]\n" \
"ds_read_b64_tr_b16 a[206:207], v210 offset:40960\n" \
"v_cvt_pk_bf16_f32 v123, v118, v119\n" \
"v_mfma_f32_16x16x32_bf16 v[12:15], a[48:51], a[16:19], v[12:15]\n" \
"ds_read_b64_tr_b16 a[208:209], v209 offset:26624\n" \
"v_mfma_f32_16x16x32_bf16 v[12:15], a[52:55], a[20:23], v[12:15]\n" \
"buffer_load_dwordx4 v198, s[48:51], s77 offen offset:1024 lds\n" \
"v_mfma_f32_16x16x32_bf16 v[8:11], a[40:43], a[16:19], v[8:11]\n" \
"Lpg7939_%=:\n" \
"Lst1x0_%=:\n" \
"s_cbranch_execz Lpg10223_%=\n" \
"ds_read_b64_tr_b16 a[210:211], v209 offset:29696\n" \
"v_mfma_f32_16x16x32_bf16 v[8:11], a[44:47], a[20:23], v[8:11]\n" \
"ds_read_b64_tr_b16 a[212:213], v209 offset:38912\n" \
"buffer_store_dwordx4 v[120:123], v240, s[56:59], s82 offen offset:0 sc1\n" \
"v_mfma_f32_16x16x32_bf16 v[48:51], a[32:35], a[16:19], v[48:51]\n" \
"s_waitcnt lgkmcnt(3)\n" \
"ds_read_b64_tr_b16 a[214:215], v209 offset:41984\n" \
"v_mfma_f32_16x16x32_bf16 v[48:51], a[36:39], a[20:23], v[48:51]\n" \
"buffer_load_dwordx4 v199, s[48:51], s77 offen offset:2048 lds\n" \
"v_mfma_f32_16x16x32_bf16 v[224:227], v[244:247], v[120:123], v[224:227]\n" \
"v_accvgpr_read_b32 v112, a120\n" \
"v_accvgpr_read_b32 v113, a121\n" \
"v_accvgpr_read_b32 v114, a122\n" \
"v_mfma_f32_16x16x32_bf16 v[56:59], a[32:35], a[24:27], v[56:59]\n" \
"ds_read_b64_tr_b16 a[216:217], v210 offset:26624\n" \
"v_accvgpr_read_b32 v115, a123\n" \
"v_mfma_f32_16x16x32_bf16 v[56:59], a[36:39], a[28:31], v[56:59]\n" \
"ds_read_b64_tr_b16 a[218:219], v210 offset:29696\n" \
"v_accvgpr_read_b32 v116, a124\n" \
"v_mfma_f32_16x16x32_bf16 v[16:19], a[40:43], a[24:27], v[16:19]\n" \
"ds_read_b64_tr_b16 a[220:221], v210 offset:38912\n" \
"v_accvgpr_read_b32 v117, a125\n" \
"v_mfma_f32_16x16x32_bf16 v[16:19], a[44:47], a[28:31], v[16:19]\n" \
"v_accvgpr_read_b32 v118, a126\n" \
"v_accvgpr_read_b32 v119, a127\n" \
"v_mfma_f32_16x16x32_bf16 v[20:23], a[48:51], a[24:27], v[20:23]\n" \
"ds_read_b64_tr_b16 a[222:223], v210 offset:41984\n" \
"v_mul_f32 v112, v112, v233\n" \
"v_mfma_f32_16x16x32_bf16 v[20:23], a[52:55], a[28:31], v[20:23]\n" \
"v_mul_f32 v113, v113, v233\n" \
"v_mul_f32 v114, v114, v233\n" \
"v_mul_f32 v115, v115, v233\n" \
"v_mfma_f32_16x16x32_bf16 v[28:31], a[48:51], a[192:195], v[28:31]\n" \
"v_mul_f32 v116, v116, v233\n" \
"v_mul_f32 v117, v117, v233\n" \
"v_mfma_f32_16x16x32_bf16 v[28:31], a[52:55], a[196:199], v[28:31]\n" \
"v_mul_f32_e64 v118, v118, v233\n" \
"v_mul_f32_e64 v119, v119, v233\n" \
"v_mfma_f32_16x16x32_bf16 v[24:27], a[40:43], a[192:195], v[24:27]\n" \
"v_cvt_pk_bf16_f32 v124, v112, v113\n" \
"v_cvt_pk_bf16_f32 v125, v114, v115\n" \
"v_mfma_f32_16x16x32_bf16 v[24:27], a[44:47], a[196:199], v[24:27]\n" \
"v_cvt_pk_bf16_f32 v126, v116, v117\n" \
"v_cvt_pk_bf16_f32 v127, v118, v119\n" \
"v_mfma_f32_16x16x32_bf16 v[64:67], a[32:35], a[192:195], v[64:67]\n" \
"buffer_store_dwordx4 v[124:127], v240, s[56:59], s82 offen offset:2048 sc1\n" \
"v_mfma_f32_16x16x32_bf16 v[64:67], a[36:39], a[196:199], v[64:67]\n" \
"v_mfma_f32_16x16x32_bf16 v[224:227], v[244:247], v[124:127], v[224:227]\n" \
"s_waitcnt vmcnt(14) lgkmcnt(0)\n" \
"s_barrier\n" \
"v_mfma_f32_16x16x32_bf16 v[68:71], a[200:203], a[192:195], v[68:71]\n" \
"ds_read_b64_tr_b16 a[32:33], v209 offset:1024\n" \
"ds_read_b64_tr_b16 a[34:35], v209 offset:4096\n" \
"v_mfma_f32_16x16x32_bf16 v[68:71], a[204:207], a[196:199], v[68:71]\n" \
"ds_read_b64_tr_b16 a[36:37], v209 offset:13312\n" \
"ds_read_b64_tr_b16 a[38:39], v209 offset:16384\n" \
"v_mfma_f32_16x16x32_bf16 v[60:63], a[200:203], a[24:27], v[60:63]\n" \
"s_add_u32 m0, s79, 40960\n" \
"ds_read_b64_tr_b16 a[40:41], v209 offset:0\n" \
"v_mfma_f32_16x16x32_bf16 v[60:63], a[204:207], a[28:31], v[60:63]\n" \
"buffer_load_dwordx4 v192, s[44:47], s73 offen offset:0 lds\n" \
"v_mfma_f32_16x16x32_bf16 v[52:55], a[200:203], a[16:19], v[52:55]\n" \
"ds_read_b64_tr_b16 a[42:43], v209 offset:3072\n" \
"v_mfma_f32_16x16x32_bf16 v[52:55], a[204:207], a[20:23], v[52:55]\n" \
"ds_read_b64_tr_b16 a[44:45], v209 offset:12288\n" \
"v_mfma_f32_16x16x32_bf16 v[44:47], a[200:203], a[8:11], v[44:47]\n" \
"ds_read_b64_tr_b16 a[46:47], v209 offset:15360\n" \
"v_mfma_f32_16x16x32_bf16 v[44:47], a[204:207], a[12:15], v[44:47]\n" \
"buffer_load_dwordx4 v193, s[44:47], s73 offen offset:1024 lds\n" \
"v_mfma_f32_16x16x32_bf16 v[72:75], a[0:3], a[208:211], v[72:75]\n" \
"ds_read_b64_tr_b16 a[48:49], v210 offset:0\n" \
"v_mfma_f32_16x16x32_bf16 v[72:75], a[4:7], a[212:215], v[72:75]\n" \
"ds_read_b128 a[0:3], v202 offset:2048\n" \
"v_mfma_f32_16x16x32_bf16 v[80:83], a[8:11], a[208:211], v[80:83]\n" \
"ds_read_b128 a[4:7], v203 offset:2048\n" \
"v_mfma_f32_16x16x32_bf16 v[80:83], a[12:15], a[212:215], v[80:83]\n" \
"buffer_load_dwordx4 v192, s[44:47], s73 offen offset:2048 lds\n" \
"v_mfma_f32_16x16x32_bf16 v[84:87], a[8:11], a[216:219], v[84:87]\n" \
"ds_read_b64_tr_b16 a[50:51], v210 offset:3072\n" \
"v_mfma_f32_16x16x32_bf16 v[84:87], a[12:15], a[220:223], v[84:87]\n" \
"ds_read_b128 a[8:11], v205 offset:0\n" \
"v_mfma_f32_16x16x32_bf16 v[92:95], a[16:19], a[216:219], v[92:95]\n" \
"ds_read_b128 a[12:15], v206 offset:0\n" \
"v_mfma_f32_16x16x32_bf16 v[92:95], a[20:23], a[220:223], v[92:95]\n" \
"buffer_load_dwordx4 v193, s[44:47], s73 offen offset:3072 lds\n" \
"v_mfma_f32_16x16x32_bf16 v[88:91], a[16:19], a[208:211], v[88:91]\n" \
"s_xor_b32 s73, s73, 0x180\n" \
"s_mul_i32 s76, s80, 393216\n" \
"ds_read_b64_tr_b16 a[52:53], v210 offset:12288\n" \
"v_mfma_f32_16x16x32_bf16 v[88:91], a[20:23], a[212:215], v[88:91]\n" \
"s_add_u32 s76, s76, s78\n" \
"ds_read_b128 a[16:19], v205 offset:4096\n" \
"v_mfma_f32_16x16x32_bf16 v[96:99], a[24:27], a[208:211], v[96:99]\n" \
"s_add_u32 m0, s91, 106496\n" \
"ds_read_b128 a[20:23], v206 offset:4096\n" \
"v_mfma_f32_16x16x32_bf16 v[96:99], a[28:31], a[212:215], v[96:99]\n" \
"buffer_load_dwordx4 v197, s[48:51], s76 offen offset:0 lds\n" \
"v_mfma_f32_16x16x32_bf16 v[100:103], a[24:27], a[216:219], v[100:103]\n" \
"ds_read_b64_tr_b16 a[54:55], v210 offset:15360\n" \
"v_mfma_f32_16x16x32_bf16 v[100:103], a[28:31], a[220:223], v[100:103]\n" \
"ds_read_b128 a[24:27], v205 offset:8192\n" \
"v_mfma_f32_16x16x32_bf16 v[108:111], a[192:195], a[216:219], v[108:111]\n" \
"s_add_u32 s77, s76, 24576\n" \
"ds_read_b128 a[28:31], v206 offset:8192\n" \
"v_mfma_f32_16x16x32_bf16 v[108:111], a[196:199], a[220:223], v[108:111]\n" \
"buffer_load_dwordx4 v198, s[48:51], s76 offen offset:1024 lds\n" \
"v_mfma_f32_16x16x32_bf16 v[104:107], a[192:195], a[208:211], v[104:107]\n" \
"v_mfma_f32_16x16x32_bf16 v[104:107], a[196:199], a[212:215], v[104:107]\n" \
"s_waitcnt lgkmcnt(2)\n" \
"v_mfma_f32_16x16x32_bf16 v[32:35], a[32:35], a[0:3], v[32:35]\n" \
"s_xor_b32 s80, s80, 0x3\n" \
"ds_read_b128 a[192:195], v205 offset:12288\n" \
"v_mfma_f32_16x16x32_bf16 v[32:35], a[36:39], a[4:7], v[32:35]\n" \
"buffer_load_dwordx4 v199, s[48:51], s76 offen offset:2048 lds\n" \
"v_mfma_f32_16x16x32_bf16 v[40:43], a[32:35], a[8:11], v[40:43]\n" \
"ds_read_b128 a[196:199], v206 offset:12288\n" \
"v_mfma_f32_16x16x32_bf16 v[40:43], a[36:39], a[12:15], v[40:43]\n" \
"ds_read_b64_tr_b16 a[200:201], v210 offset:1024\n" \
"v_mfma_f32_16x16x32_bf16 v[0:3], a[40:43], a[8:11], v[0:3]\n" \
"s_add_u32 m0, m0, 3072\n" \
"ds_read_b64_tr_b16 a[202:203], v210 offset:4096\n" \
"v_mfma_f32_16x16x32_bf16 v[0:3], a[44:47], a[12:15], v[0:3]\n" \
"buffer_load_dwordx4 v197, s[48:51], s77 offen offset:0 lds\n" \
"v_mfma_f32_16x16x32_bf16 v[4:7], a[48:51], a[8:11], v[4:7]\n" \
"ds_read_b64_tr_b16 a[204:205], v210 offset:13312\n" \
"v_mfma_f32_16x16x32_bf16 v[4:7], a[52:55], a[12:15], v[4:7]\n" \
"ds_read_b64_tr_b16 a[206:207], v210 offset:16384\n" \
"v_mfma_f32_16x16x32_bf16 v[12:15], a[48:51], a[16:19], v[12:15]\n" \
"ds_read_b64_tr_b16 a[208:209], v209 offset:2048\n" \
"v_mfma_f32_16x16x32_bf16 v[12:15], a[52:55], a[20:23], v[12:15]\n" \
"buffer_load_dwordx4 v198, s[48:51], s77 offen offset:1024 lds\n" \
"v_mfma_f32_16x16x32_bf16 v[8:11], a[40:43], a[16:19], v[8:11]\n" \
"ds_read_b64_tr_b16 a[210:211], v209 offset:5120\n" \
"v_mfma_f32_16x16x32_bf16 v[8:11], a[44:47], a[20:23], v[8:11]\n" \
"ds_read_b64_tr_b16 a[212:213], v209 offset:14336\n" \
"v_mfma_f32_16x16x32_bf16 v[48:51], a[32:35], a[16:19], v[48:51]\n" \
"s_waitcnt lgkmcnt(3)\n" \
"ds_read_b64_tr_b16 a[214:215], v209 offset:17408\n" \
"v_mfma_f32_16x16x32_bf16 v[48:51], a[36:39], a[20:23], v[48:51]\n" \
"buffer_load_dwordx4 v199, s[48:51], s77 offen offset:2048 lds\n" \
"v_mfma_f32_16x16x32_bf16 v[56:59], a[32:35], a[24:27], v[56:59]\n" \
"ds_read_b64_tr_b16 a[216:217], v210 offset:2048\n" \
"v_mfma_f32_16x16x32_bf16 v[56:59], a[36:39], a[28:31], v[56:59]\n" \
"ds_read_b64_tr_b16 a[218:219], v210 offset:5120\n" \
"v_mfma_f32_16x16x32_bf16 v[16:19], a[40:43], a[24:27], v[16:19]\n" \
"ds_read_b64_tr_b16 a[220:221], v210 offset:14336\n" \
"v_mfma_f32_16x16x32_bf16 v[16:19], a[44:47], a[28:31], v[16:19]\n" \
"v_mfma_f32_16x16x32_bf16 v[20:23], a[48:51], a[24:27], v[20:23]\n" \
"ds_read_b64_tr_b16 a[222:223], v210 offset:17408\n" \
"v_mfma_f32_16x16x32_bf16 v[20:23], a[52:55], a[28:31], v[20:23]\n" \
"v_mfma_f32_16x16x32_bf16 v[28:31], a[48:51], a[192:195], v[28:31]\n" \
"v_mfma_f32_16x16x32_bf16 v[28:31], a[52:55], a[196:199], v[28:31]\n" \
"v_mfma_f32_16x16x32_bf16 v[24:27], a[40:43], a[192:195], v[24:27]\n" \
"v_mfma_f32_16x16x32_bf16 v[24:27], a[44:47], a[196:199], v[24:27]\n" \
"v_mfma_f32_16x16x32_bf16 v[64:67], a[32:35], a[192:195], v[64:67]\n" \
"v_mfma_f32_16x16x32_bf16 v[64:67], a[36:39], a[196:199], v[64:67]\n" \
"s_waitcnt vmcnt(11) lgkmcnt(0)\n" \
"s_barrier\n" \
"s_movk_i32 s64, 0x207\n" \
"v_cmp_class_f32 vcc, v224, s64\n" \
"s_nop 1\n" \
"s_cmp_lg_u64 vcc, 0\n" \
"s_addc_u32 s90, s90, 0\n" \
"s_cmp_lg_u32 s90, 0\n" \
"s_cbranch_scc1 Lexact_%=\n" \
"Lexr_%=:\n" \
"v_mfma_f32_16x16x32_bf16 v[68:71], a[200:203], a[192:195], v[68:71]\n" \
"s_lshl_b32 s15, s63, 7\n" \
"ds_read_b64_tr_b16 a[32:33], v204 offset:17408\n" \
"ds_read_b64_tr_b16 a[34:35], v204 offset:20480\n" \
"v_mfma_f32_16x16x32_bf16 v[68:71], a[204:207], a[196:199], v[68:71]\n" \
"s_lshl2_add_u32 s16, s62, 65589248\n" \
"ds_read_b64_tr_b16 a[36:37], v204 offset:29696\n" \
"ds_read_b64_tr_b16 a[38:39], v204 offset:32768\n" \
"v_mfma_f32_16x16x32_bf16 v[60:63], a[200:203], a[24:27], v[60:63]\n" \
"s_add_u32 m0, s79, 65536\n" \
"s_sub_u32 s17, s16, s15\n" \
"ds_read_b64_tr_b16 a[40:41], v204 offset:16384\n" \
"v_mfma_f32_16x16x32_bf16 v[60:63], a[204:207], a[28:31], v[60:63]\n" \
"buffer_load_dwordx4 v192, s[44:47], s73 offen offset:0 lds\n" \
"v_mfma_f32_16x16x32_bf16 v[52:55], a[200:203], a[16:19], v[52:55]\n" \
"s_add_u32 s15, s15, s16\n" \
"s_add_u32 s17, s17, 1920\n" \
"ds_read_b64_tr_b16 a[42:43], v204 offset:19456\n" \
"v_mfma_f32_16x16x32_bf16 v[52:55], a[204:207], a[20:23], v[52:55]\n" \
"ds_read_b64_tr_b16 a[44:45], v204 offset:28672\n" \
"v_mov_b32 v146, s60\n" \
"v_mfma_f32_16x16x32_bf16 v[44:47], a[200:203], a[8:11], v[44:47]\n" \
"ds_read_b64_tr_b16 a[46:47], v204 offset:31744\n" \
"v_lshlrev_b32 v147, 28, s61\n" \
"v_mfma_f32_16x16x32_bf16 v[44:47], a[204:207], a[12:15], v[44:47]\n" \
"buffer_load_dwordx4 v193, s[44:47], s73 offen offset:1024 lds\n" \
"v_mfma_f32_16x16x32_bf16 v[72:75], a[0:3], a[208:211], v[72:75]\n" \
"ds_read_b64_tr_b16 a[48:49], v222 offset:16384\n" \
"v_mfma_f32_16x16x32_bf16 v[72:75], a[4:7], a[212:215], v[72:75]\n" \
"ds_read_b128 a[0:3], v202 offset:4096\n" \
"v_mfma_f32_16x16x32_bf16 v[80:83], a[8:11], a[208:211], v[80:83]\n" \
"ds_read_b128 a[4:7], v203 offset:4096\n" \
"v_mfma_f32_16x16x32_bf16 v[80:83], a[12:15], a[212:215], v[80:83]\n" \
"buffer_load_dwordx4 v192, s[44:47], s73 offen offset:2048 lds\n" \
"v_mfma_f32_16x16x32_bf16 v[84:87], a[8:11], a[216:219], v[84:87]\n" \
"ds_read_b64_tr_b16 a[50:51], v222 offset:19456\n" \
"v_mfma_f32_16x16x32_bf16 v[84:87], a[12:15], a[220:223], v[84:87]\n" \
"ds_read_b128 a[8:11], v200 offset:0\n" \
"v_mfma_f32_16x16x32_bf16 v[92:95], a[16:19], a[216:219], v[92:95]\n" \
"ds_read_b128 a[12:15], v201 offset:0\n" \
"v_mfma_f32_16x16x32_bf16 v[92:95], a[20:23], a[220:223], v[92:95]\n" \
"buffer_load_dwordx4 v193, s[44:47], s73 offen offset:3072 lds\n" \
"v_mfma_f32_16x16x32_bf16 v[88:91], a[16:19], a[208:211], v[88:91]\n" \
"s_xor_b32 s73, s73, 0x80\n" \
"s_mul_i32 s76, s80, 393216\n" \
"ds_read_b64_tr_b16 a[52:53], v222 offset:28672\n" \
"v_mfma_f32_16x16x32_bf16 v[88:91], a[20:23], a[212:215], v[88:91]\n" \
"s_add_u32 s76, s76, s78\n" \
"ds_read_b128 a[16:19], v200 offset:4096\n" \
"v_mfma_f32_16x16x32_bf16 v[96:99], a[24:27], a[208:211], v[96:99]\n" \
"s_add_u32 m0, s91, 81920\n" \
"ds_read_b128 a[20:23], v201 offset:4096\n" \
"v_mfma_f32_16x16x32_bf16 v[96:99], a[28:31], a[212:215], v[96:99]\n" \
"buffer_load_dwordx4 v197, s[48:51], s76 offen offset:0 lds\n" \
"v_mfma_f32_16x16x32_bf16 v[100:103], a[24:27], a[216:219], v[100:103]\n" \
"ds_read_b64_tr_b16 a[54:55], v222 offset:31744\n" \
"v_mfma_f32_16x16x32_bf16 v[100:103], a[28:31], a[220:223], v[100:103]\n" \
"ds_read_b128 a[24:27], v200 offset:8192\n" \
"v_mfma_f32_16x16x32_bf16 v[108:111], a[192:195], a[216:219], v[108:111]\n" \
"s_add_u32 s77, s76, 24576\n" \
"ds_read_b128 a[28:31], v201 offset:8192\n" \
"v_mfma_f32_16x16x32_bf16 v[108:111], a[196:199], a[220:223], v[108:111]\n" \
"buffer_load_dwordx4 v198, s[48:51], s76 offen offset:1024 lds\n" \
"v_mfma_f32_16x16x32_bf16 v[104:107], a[192:195], a[208:211], v[104:107]\n" \
"v_mfma_f32_16x16x32_bf16 v[104:107], a[196:199], a[212:215], v[104:107]\n" \
"s_waitcnt lgkmcnt(2)\n" \
"v_mfma_f32_16x16x32_bf16 v[32:35], a[32:35], a[0:3], v[32:35]\n" \
"s_xor_b32 s80, s80, 0x1\n" \
"ds_read_b128 a[192:195], v200 offset:12288\n" \
"v_mfma_f32_16x16x32_bf16 v[32:35], a[36:39], a[4:7], v[32:35]\n" \
"buffer_load_dwordx4 v199, s[48:51], s76 offen offset:2048 lds\n" \
"v_mfma_f32_16x16x32_bf16 v[40:43], a[32:35], a[8:11], v[40:43]\n" \
"ds_read_b128 a[196:199], v201 offset:12288\n" \
"v_mfma_f32_16x16x32_bf16 v[40:43], a[36:39], a[12:15], v[40:43]\n" \
"ds_read_b64_tr_b16 a[200:201], v222 offset:17408\n" \
"v_mfma_f32_16x16x32_bf16 v[0:3], a[40:43], a[8:11], v[0:3]\n" \
"s_add_u32 m0, m0, 3072\n" \
"ds_read_b64_tr_b16 a[202:203], v222 offset:20480\n" \
"v_mfma_f32_16x16x32_bf16 v[0:3], a[44:47], a[12:15], v[0:3]\n" \
"buffer_load_dwordx4 v197, s[48:51], s77 offen offset:0 lds\n" \
"v_mfma_f32_16x16x32_bf16 v[4:7], a[48:51], a[8:11], v[4:7]\n" \
"ds_read_b64_tr_b16 a[204:205], v222 offset:29696\n" \
"v_mfma_f32_16x16x32_bf16 v[4:7], a[52:55], a[12:15], v[4:7]\n" \
"ds_read_b64_tr_b16 a[206:207], v222 offset:32768\n" \
"v_mfma_f32_16x16x32_bf16 v[12:15], a[48:51], a[16:19], v[12:15]\n" \
"ds_read_b64_tr_b16 a[208:209], v204 offset:18432\n" \
"v_mfma_f32_16x16x32_bf16 v[12:15], a[52:55], a[20:23], v[12:15]\n" \
"buffer_load_dwordx4 v198, s[48:51], s77 offen offset:1024 lds\n" \
"v_mfma_f32_16x16x32_bf16 v[8:11], a[40:43], a[16:19], v[8:11]\n" \
"ds_read_b64_tr_b16 a[210:211], v204 offset:21504\n" \
"v_mfma_f32_16x16x32_bf16 v[8:11], a[44:47], a[20:23], v[8:11]\n" \
"ds_read_b64_tr_b16 a[212:213], v204 offset:30720\n" \
"v_mfma_f32_16x16x32_bf16 v[48:51], a[32:35], a[16:19], v[48:51]\n" \
"s_waitcnt lgkmcnt(3)\n" \
"ds_read_b64_tr_b16 a[214:215], v204 offset:33792\n" \
"v_mfma_f32_16x16x32_bf16 v[48:51], a[36:39], a[20:23], v[48:51]\n" \
"buffer_load_dwordx4 v199, s[48:51], s77 offen offset:2048 lds\n" \
"v_mfma_f32_16x16x32_bf16 v[56:59], a[32:35], a[24:27], v[56:59]\n" \
"ds_read_b64_tr_b16 a[216:217], v222 offset:18432\n" \
"v_mfma_f32_16x16x32_bf16 v[56:59], a[36:39], a[28:31], v[56:59]\n" \
"ds_read_b64_tr_b16 a[218:219], v222 offset:21504\n" \
"v_mfma_f32_16x16x32_bf16 v[16:19], a[40:43], a[24:27], v[16:19]\n" \
"ds_read_b64_tr_b16 a[220:221], v222 offset:30720\n" \
"v_mfma_f32_16x16x32_bf16 v[16:19], a[44:47], a[28:31], v[16:19]\n" \
"v_mfma_f32_16x16x32_bf16 v[20:23], a[48:51], a[24:27], v[20:23]\n" \
"ds_read_b64_tr_b16 a[222:223], v222 offset:33792\n" \
"v_mfma_f32_16x16x32_bf16 v[20:23], a[52:55], a[28:31], v[20:23]\n" \
"v_mfma_f32_16x16x32_bf16 v[28:31], a[48:51], a[192:195], v[28:31]\n" \
"v_mfma_f32_16x16x32_bf16 v[28:31], a[52:55], a[196:199], v[28:31]\n" \
"v_mfma_f32_16x16x32_bf16 v[24:27], a[40:43], a[192:195], v[24:27]\n" \
"v_mfma_f32_16x16x32_bf16 v[24:27], a[44:47], a[196:199], v[24:27]\n" \
"v_mfma_f32_16x16x32_bf16 v[64:67], a[32:35], a[192:195], v[64:67]\n" \
"v_mfma_f32_16x16x32_bf16 v[64:67], a[36:39], a[196:199], v[64:67]\n" \
"s_waitcnt vmcnt(10) lgkmcnt(0)\n" \
"s_barrier\n" \
"v_mfma_f32_16x16x32_bf16 v[68:71], a[200:203], a[192:195], v[68:71]\n" \
"ds_read_b64_tr_b16 a[32:33], v209 offset:25600\n" \
"ds_read_b64_tr_b16 a[34:35], v209 offset:28672\n" \
"v_mfma_f32_16x16x32_bf16 v[68:71], a[204:207], a[196:199], v[68:71]\n" \
"ds_read_b64_tr_b16 a[36:37], v209 offset:37888\n" \
"ds_read_b64_tr_b16 a[38:39], v209 offset:40960\n" \
"v_mfma_f32_16x16x32_bf16 v[60:63], a[200:203], a[24:27], v[60:63]\n" \
"s_add_u32 m0, s79, 0\n" \
"ds_read_b64_tr_b16 a[40:41], v209 offset:24576\n" \
"buffer_store_dword v146, v147, s[56:59], s15 offen sc1\n" \
"v_mfma_f32_16x16x32_bf16 v[60:63], a[204:207], a[28:31], v[60:63]\n" \
"buffer_load_dwordx4 v192, s[44:47], s73 offen offset:0 lds\n" \
"v_mfma_f32_16x16x32_bf16 v[52:55], a[200:203], a[16:19], v[52:55]\n" \
"ds_read_b64_tr_b16 a[42:43], v209 offset:27648\n" \
"v_mfma_f32_16x16x32_bf16 v[52:55], a[204:207], a[20:23], v[52:55]\n" \
"ds_read_b64_tr_b16 a[44:45], v209 offset:36864\n" \
"buffer_store_dword v146, v147, s[56:59], s17 offen sc1\n" \
"v_mfma_f32_16x16x32_bf16 v[44:47], a[200:203], a[8:11], v[44:47]\n" \
"s_mov_b64 s[36:37], s[18:19]\n" \
"ds_read_b64_tr_b16 a[46:47], v209 offset:39936\n" \
"v_mfma_f32_16x16x32_bf16 v[44:47], a[204:207], a[12:15], v[44:47]\n" \
"buffer_load_dwordx4 v193, s[44:47], s73 offen offset:1024 lds\n" \
"v_mfma_f32_16x16x32_bf16 v[72:75], a[0:3], a[208:211], v[72:75]\n" \
"s_sub_u32 s36, s36, 4096\n" \
"s_subb_u32 s37, s37, 0\n" \
"ds_read_b64_tr_b16 a[48:49], v210 offset:24576\n" \
"v_mfma_f32_16x16x32_bf16 v[72:75], a[4:7], a[212:215], v[72:75]\n" \
"s_and_b32 s37, s37, 0xffff\n" \
"s_mov_b32 s38, 8392704\n" \
"ds_read_b128 a[0:3], v202 offset:5120\n" \
"v_mfma_f32_16x16x32_bf16 v[80:83], a[8:11], a[208:211], v[80:83]\n" \
"s_mov_b32 s39, 0x20000\n" \
"s_mov_b64 s[40:41], s[24:25]\n" \
"ds_read_b128 a[4:7], v203 offset:5120\n" \
"v_mfma_f32_16x16x32_bf16 v[80:83], a[12:15], a[212:215], v[80:83]\n" \
"buffer_load_dwordx4 v192, s[44:47], s73 offen offset:2048 lds\n" \
"v_mfma_f32_16x16x32_bf16 v[84:87], a[8:11], a[216:219], v[84:87]\n" \
"s_sub_u32 s40, s40, 4096\n" \
"s_subb_u32 s41, s41, 0\n" \
"ds_read_b64_tr_b16 a[50:51], v210 offset:27648\n" \
"v_mfma_f32_16x16x32_bf16 v[84:87], a[12:15], a[220:223], v[84:87]\n" \
"s_and_b32 s41, s41, 0xffff\n" \
"s_mov_b32 s42, 2101248\n" \
"ds_read_b128 a[8:11], v200 offset:40960\n" \
"v_mfma_f32_16x16x32_bf16 v[92:95], a[16:19], a[216:219], v[92:95]\n" \
"s_mov_b32 s43, 0x20000\n" \
"s_mov_b64 s[52:53], s[26:27]\n" \
"ds_read_b128 a[12:15], v201 offset:40960\n" \
"v_mfma_f32_16x16x32_bf16 v[92:95], a[20:23], a[220:223], v[92:95]\n" \
"buffer_load_dwordx4 v193, s[44:47], s73 offen offset:3072 lds\n" \
"v_mfma_f32_16x16x32_bf16 v[88:91], a[16:19], a[208:211], v[88:91]\n" \
"s_xor_b32 s73, s73, 0x780\n" \
"s_mul_i32 s76, s80, 393216\n" \
"ds_read_b64_tr_b16 a[52:53], v210 offset:36864\n" \
"v_mfma_f32_16x16x32_bf16 v[88:91], a[20:23], a[212:215], v[88:91]\n" \
"s_add_u32 s76, s76, s78\n" \
"s_and_b32 s53, s53, 0xffff\n" \
"ds_read_b128 a[16:19], v200 offset:45056\n" \
"v_mfma_f32_16x16x32_bf16 v[96:99], a[24:27], a[208:211], v[96:99]\n" \
"s_add_u32 m0, s91, 16384\n" \
"s_mov_b32 s54, 8388608\n" \
"ds_read_b128 a[20:23], v201 offset:45056\n" \
"v_mfma_f32_16x16x32_bf16 v[96:99], a[28:31], a[212:215], v[96:99]\n" \
"buffer_load_dwordx4 v197, s[48:51], s76 offen offset:0 lds\n" \
"v_mfma_f32_16x16x32_bf16 v[100:103], a[24:27], a[216:219], v[100:103]\n" \
"s_mov_b32 s55, 0x20000\n" \
"ds_read_b64_tr_b16 a[54:55], v210 offset:39936\n" \
"v_lshrrev_b32 v221, 2, v234\n" \
"v_mfma_f32_16x16x32_bf16 v[100:103], a[28:31], a[220:223], v[100:103]\n" \
"s_waitcnt lgkmcnt(3)\n" \
"ds_read_b128 a[24:27], v200 offset:49152\n" \
"v_lshrrev_b32 v244, 4, v234\n" \
"v_mfma_f32_16x16x32_bf16 v[108:111], a[192:195], a[216:219], v[108:111]\n" \
"s_add_u32 s77, s76, 24576\n" \
"s_lshl_b32 s12, s94, 7\n" \
"ds_read_b128 a[28:31], v201 offset:49152\n" \
"v_mfma_f32_16x16x32_bf16 v[108:111], a[196:199], a[220:223], v[108:111]\n" \
"buffer_load_dwordx4 v198, s[48:51], s76 offen offset:1024 lds\n" \
"v_mfma_f32_16x16x32_bf16 v[104:107], a[192:195], a[208:211], v[104:107]\n" \
"s_add_u32 s12, s12, 65591296\n" \
"s_and_b32 s70, %1, 1\n" \
"s_lshl3_add_u32 s70, s70, s63\n" \
"v_mfma_f32_16x16x32_bf16 v[104:107], a[196:199], a[212:215], v[104:107]\n" \
"s_lshl2_add_u32 s12, s70, s12\n" \
"s_lshr_b32 s68, s63, 1\n" \
"s_add_u32 s68, s68, 2\n" \
"v_mfma_f32_16x16x32_bf16 v[32:35], a[32:35], a[0:3], v[32:35]\n" \
"s_xor_b32 s80, s80, 0xf\n" \
"s_lshr_b32 s70, s18, 8\n" \
"ds_read_b128 a[192:195], v200 offset:53248\n" \
"v_mfma_f32_16x16x32_bf16 v[32:35], a[36:39], a[4:7], v[32:35]\n" \
"buffer_load_dwordx4 v199, s[48:51], s76 offen offset:2048 lds\n" \
"v_mfma_f32_16x16x32_bf16 v[40:43], a[32:35], a[8:11], v[40:43]\n" \
"s_sub_u32 s68, s68, s70\n" \
"s_and_b32 s68, s68, 3\n" \
"ds_read_b128 a[196:199], v201 offset:53248\n" \
"v_mfma_f32_16x16x32_bf16 v[40:43], a[36:39], a[12:15], v[40:43]\n" \
"s_and_b32 s70, s63, 1\n" \
"s_lshl_b32 s70, s70, 2\n" \
"ds_read_b64_tr_b16 a[200:201], v210 offset:25600\n" \
"v_mfma_f32_16x16x32_bf16 v[0:3], a[40:43], a[8:11], v[0:3]\n" \
"s_add_u32 m0, m0, 3072\n" \
"s_or_b32 s68, s68, s70\n" \
"ds_read_b64_tr_b16 a[202:203], v210 offset:28672\n" \
"v_mfma_f32_16x16x32_bf16 v[0:3], a[44:47], a[12:15], v[0:3]\n" \
"s_waitcnt lgkmcnt(3)\n" \
"buffer_load_dwordx4 v197, s[48:51], s77 offen offset:0 lds\n" \
"v_mfma_f32_16x16x32_bf16 v[4:7], a[48:51], a[8:11], v[4:7]\n" \
"s_lshr_b32 s84, s61, 1\n" \
"s_lshl_b32 s84, s84, 16\n" \
"ds_read_b64_tr_b16 a[204:205], v210 offset:37888\n" \
"v_mfma_f32_16x16x32_bf16 v[4:7], a[52:55], a[12:15], v[4:7]\n" \
"s_and_b32 s70, s61, 1\n" \
"s_lshl_b32 s70, s70, 14\n" \
"ds_read_b64_tr_b16 a[206:207], v210 offset:40960\n" \
"v_mfma_f32_16x16x32_bf16 v[12:15], a[48:51], a[16:19], v[12:15]\n" \
"s_add_u32 s84, s84, s70\n" \
"s_lshl_b32 s88, s68, 8\n" \
"ds_read_b64_tr_b16 a[208:209], v209 offset:26624\n" \
"v_mfma_f32_16x16x32_bf16 v[12:15], a[52:55], a[20:23], v[12:15]\n" \
"buffer_load_dwordx4 v198, s[48:51], s77 offen offset:1024 lds\n" \
"v_mfma_f32_16x16x32_bf16 v[8:11], a[40:43], a[16:19], v[8:11]\n" \
"s_add_u32 s84, s84, s88\n" \
"ds_read_b64_tr_b16 a[210:211], v209 offset:29696\n" \
"v_bfe_u32 v245, v244, 2, 3\n" \
"v_mfma_f32_16x16x32_bf16 v[8:11], a[44:47], a[20:23], v[8:11]\n" \
"ds_read_b64_tr_b16 a[212:213], v209 offset:38912\n" \
"v_lshlrev_b32 v245, 11, v245\n" \
"v_mfma_f32_16x16x32_bf16 v[48:51], a[32:35], a[16:19], v[48:51]\n" \
"ds_read_b64_tr_b16 a[214:215], v209 offset:41984\n" \
"v_lshrrev_b32 v246, 5, v244\n" \
"v_mfma_f32_16x16x32_bf16 v[48:51], a[36:39], a[20:23], v[48:51]\n" \
"buffer_load_dwordx4 v199, s[48:51], s77 offen offset:2048 lds\n" \
"v_mfma_f32_16x16x32_bf16 v[56:59], a[32:35], a[24:27], v[56:59]\n" \
"ds_read_b64_tr_b16 a[216:217], v210 offset:26624\n" \
"v_lshl_add_u32 v245, v246, 6, v245\n" \
"v_mfma_f32_16x16x32_bf16 v[56:59], a[36:39], a[28:31], v[56:59]\n" \
"ds_read_b64_tr_b16 a[218:219], v210 offset:29696\n" \
"v_and_b32 v246, 3, v244\n" \
"v_mfma_f32_16x16x32_bf16 v[16:19], a[40:43], a[24:27], v[16:19]\n" \
"ds_read_b64_tr_b16 a[220:221], v210 offset:38912\n" \
"v_add_u32 v254, 3145728, v254\n" \
"v_mfma_f32_16x16x32_bf16 v[16:19], a[44:47], a[28:31], v[16:19]\n" \
"global_load_dword v253, v254, s[22:23]\n" \
"v_mfma_f32_16x16x32_bf16 v[20:23], a[48:51], a[24:27], v[20:23]\n" \
"ds_read_b64_tr_b16 a[222:223], v210 offset:41984\n" \
"v_bfe_u32 v247, v244, 4, 1\n" \
"v_mfma_f32_16x16x32_bf16 v[20:23], a[52:55], a[28:31], v[20:23]\n" \
"s_waitcnt lgkmcnt(3)\n" \
"v_lshlrev_b32 v247, 1, v247\n" \
"v_xor_b32 v246, v246, v247\n" \
"v_mfma_f32_16x16x32_bf16 v[28:31], a[48:51], a[192:195], v[28:31]\n" \
"v_lshl_add_u32 v245, v246, 4, v245\n" \
"v_add_u32 v216, 4096, v245\n" \
"v_mfma_f32_16x16x32_bf16 v[28:31], a[52:55], a[196:199], v[28:31]\n" \
"v_add_u32 v217, 3200, v245\n" \
"v_add_u32 v218, 34816, v245\n" \
"v_mfma_f32_16x16x32_bf16 v[24:27], a[40:43], a[192:195], v[24:27]\n" \
"v_add_u32 v219, 33920, v245\n" \
"s_mov_b32 s86, 65536\n" \
"v_mfma_f32_16x16x32_bf16 v[24:27], a[44:47], a[196:199], v[24:27]\n" \
"s_lshr_b32 s70, s61, 1\n" \
"s_lshl_b32 s71, s70, 13\n" \
"s_add_u32 s71, s71, %3\n" \
"v_mfma_f32_16x16x32_bf16 v[64:67], a[32:35], a[192:195], v[64:67]\n" \
"v_add_u32 v211, s71, v236\n" \
"s_lshl_b32 s70, s70, 18\n" \
"v_mfma_f32_16x16x32_bf16 v[64:67], a[36:39], a[196:199], v[64:67]\n" \
"s_add_u32 s88, s88, s70\n" \
"s_waitcnt vmcnt(13) lgkmcnt(0)\n" \
"s_barrier\n" \
"v_mfma_f32_16x16x32_bf16 v[68:71], a[200:203], a[192:195], v[68:71]\n" \
"s_and_b32 s70, s61, 1\n" \
"ds_read_b64_tr_b16 a[32:33], v209 offset:1024\n" \
"ds_read_b64_tr_b16 a[34:35], v209 offset:4096\n" \
"v_mfma_f32_16x16x32_bf16 v[68:71], a[204:207], a[196:199], v[68:71]\n" \
"s_lshl_b32 s71, s70, 7\n" \
"ds_read_b64_tr_b16 a[36:37], v209 offset:13312\n" \
"ds_read_b64_tr_b16 a[38:39], v209 offset:16384\n" \
"v_mfma_f32_16x16x32_bf16 v[60:63], a[200:203], a[24:27], v[60:63]\n" \
"s_add_u32 m0, s79, 40960\n" \
"s_add_u32 s88, s88, s71\n" \
"ds_read_b64_tr_b16 a[40:41], v209 offset:0\n" \
"v_mfma_f32_16x16x32_bf16 v[60:63], a[204:207], a[28:31], v[60:63]\n" \
"buffer_load_dwordx4 v192, s[44:47], s73 offen offset:0 lds\n" \
"v_mfma_f32_16x16x32_bf16 v[52:55], a[200:203], a[16:19], v[52:55]\n" \
"ds_read_b64_tr_b16 a[42:43], v209 offset:3072\n" \
"v_and_b32 v245, 3, v244\n" \
"v_mfma_f32_16x16x32_bf16 v[52:55], a[204:207], a[20:23], v[52:55]\n" \
"ds_read_b64_tr_b16 a[44:45], v209 offset:12288\n" \
"v_lshlrev_b32 v245, 3, v245\n" \
"v_mfma_f32_16x16x32_bf16 v[44:47], a[200:203], a[8:11], v[44:47]\n" \
"ds_read_b64_tr_b16 a[46:47], v209 offset:15360\n" \
"v_bfe_u32 v246, v244, 2, 2\n" \
"v_mfma_f32_16x16x32_bf16 v[44:47], a[204:207], a[12:15], v[44:47]\n" \
"buffer_load_dwordx4 v193, s[44:47], s73 offen offset:1024 lds\n" \
"v_mfma_f32_16x16x32_bf16 v[72:75], a[0:3], a[208:211], v[72:75]\n" \
"ds_read_b64_tr_b16 a[48:49], v210 offset:0\n" \
"v_lshl_add_u32 v245, v246, 6, v245\n" \
"v_mfma_f32_16x16x32_bf16 v[72:75], a[4:7], a[212:215], v[72:75]\n" \
"ds_read_b128 a[0:3], v202 offset:6144\n" \
"v_bfe_u32 v246, v244, 4, 1\n" \
"v_mfma_f32_16x16x32_bf16 v[80:83], a[8:11], a[208:211], v[80:83]\n" \
"ds_read_b128 a[4:7], v203 offset:6144\n" \
"v_lshl_add_u32 v245, v246, 5, v245\n" \
"v_mfma_f32_16x16x32_bf16 v[80:83], a[12:15], a[212:215], v[80:83]\n" \
"buffer_load_dwordx4 v192, s[44:47], s73 offen offset:2048 lds\n" \
"v_mfma_f32_16x16x32_bf16 v[84:87], a[8:11], a[216:219], v[84:87]\n" \
"ds_read_b64_tr_b16 a[50:51], v210 offset:3072\n" \
"v_lshl_add_u32 v245, v246, 8, v245\n" \
"v_mfma_f32_16x16x32_bf16 v[84:87], a[12:15], a[220:223], v[84:87]\n" \
"ds_read_b128 a[8:11], v205 offset:0\n" \
"v_lshrrev_b32 v246, 5, v244\n" \
"v_mfma_f32_16x16x32_bf16 v[92:95], a[16:19], a[216:219], v[92:95]\n" \
"ds_read_b128 a[12:15], v206 offset:0\n" \
"v_lshl_add_u32 v245, v246, 12, v245\n" \
"v_mfma_f32_16x16x32_bf16 v[92:95], a[20:23], a[220:223], v[92:95]\n" \
"buffer_load_dwordx4 v193, s[44:47], s73 offen offset:3072 lds\n" \
"v_mfma_f32_16x16x32_bf16 v[88:91], a[16:19], a[208:211], v[88:91]\n" \
"s_xor_b32 s73, s73, 0x80\n" \
"s_mul_i32 s76, s80, 393216\n" \
"ds_read_b64_tr_b16 a[52:53], v210 offset:12288\n" \
"v_mfma_f32_16x16x32_bf16 v[88:91], a[20:23], a[212:215], v[88:91]\n" \
"s_add_u32 s76, s76, s78\n" \
"s_lshl_b32 s71, s70, 10\n" \
"ds_read_b128 a[16:19], v205 offset:4096\n" \
"v_mfma_f32_16x16x32_bf16 v[96:99], a[24:27], a[208:211], v[96:99]\n" \
"s_add_u32 m0, s91, 106496\n" \
"s_add_u32 s71, s71, %3\n" \
"ds_read_b128 a[20:23], v206 offset:4096\n" \
"v_mfma_f32_16x16x32_bf16 v[96:99], a[28:31], a[212:215], v[96:99]\n" \
"buffer_load_dwordx4 v197, s[48:51], s76 offen offset:0 lds\n" \
"v_mfma_f32_16x16x32_bf16 v[100:103], a[24:27], a[216:219], v[100:103]\n" \
"ds_read_b64_tr_b16 a[54:55], v210 offset:15360\n" \
"v_add_u32 v212, s71, v245\n" \
"v_mfma_f32_16x16x32_bf16 v[100:103], a[28:31], a[220:223], v[100:103]\n" \
"s_mov_b32 s69, 1\n" \
"s_mov_b32 s30, 0x0\n" \
"ds_read_b128 a[24:27], v205 offset:8192\n" \
"v_mfma_f32_16x16x32_bf16 v[108:111], a[192:195], a[216:219], v[108:111]\n" \
"s_add_u32 s77, s76, 24576\n" \
"s_mov_b32 s85, 98304\n" \
"ds_read_b128 a[28:31], v206 offset:8192\n" \
"v_mfma_f32_16x16x32_bf16 v[108:111], a[196:199], a[220:223], v[108:111]\n" \
"buffer_load_dwordx4 v198, s[48:51], s76 offen offset:1024 lds\n" \
"v_mfma_f32_16x16x32_bf16 v[104:107], a[192:195], a[208:211], v[104:107]\n" \
"s_sub_u32 s85, s85, 32768\n" \
"s_cselect_b32 s85, 131072, s85\n" \
"v_add_u32 v213, s85, v211\n" \
"v_mfma_f32_16x16x32_bf16 v[104:107], a[196:199], a[212:215], v[104:107]\n" \
"s_waitcnt lgkmcnt(2)\n" \
"v_add_u32 v214, s85, v212\n" \
"v_xor_b32 v215, 32, v214\n" \
"v_mfma_f32_16x16x32_bf16 v[32:35], a[32:35], a[0:3], v[32:35]\n" \
"s_xor_b32 s80, s80, 0x1\n" \
"s_mov_b32 s13, 65593856\n" \
"ds_read_b128 a[192:195], v205 offset:12288\n" \
"v_mfma_f32_16x16x32_bf16 v[32:35], a[36:39], a[4:7], v[32:35]\n" \
"buffer_load_dwordx4 v199, s[48:51], s76 offen offset:2048 lds\n" \
"v_mfma_f32_16x16x32_bf16 v[40:43], a[32:35], a[8:11], v[40:43]\n" \
"ds_read_b128 a[196:199], v206 offset:12288\n" \
"v_lshrrev_b32 v228, 1, v234\n" \
"v_mfma_f32_16x16x32_bf16 v[40:43], a[36:39], a[12:15], v[40:43]\n" \
"ds_read_b64_tr_b16 a[200:201], v210 offset:1024\n" \
"v_mov_b32 v230, s60\n" \
"v_mfma_f32_16x16x32_bf16 v[0:3], a[40:43], a[8:11], v[0:3]\n" \
"s_add_u32 m0, m0, 3072\n" \
"ds_read_b64_tr_b16 a[202:203], v210 offset:4096\n" \
"v_lshlrev_b32 v231, 28, s61\n" \
"v_mfma_f32_16x16x32_bf16 v[0:3], a[44:47], a[12:15], v[0:3]\n" \
"buffer_load_dwordx4 v197, s[48:51], s77 offen offset:0 lds\n" \
"v_mfma_f32_16x16x32_bf16 v[4:7], a[48:51], a[8:11], v[4:7]\n" \
"ds_read_b64_tr_b16 a[204:205], v210 offset:13312\n" \
"v_mfma_f32_16x16x32_bf16 v[4:7], a[52:55], a[12:15], v[4:7]\n" \
"ds_read_b64_tr_b16 a[206:207], v210 offset:16384\n" \
"v_mfma_f32_16x16x32_bf16 v[12:15], a[48:51], a[16:19], v[12:15]\n" \
"ds_read_b64_tr_b16 a[208:209], v209 offset:2048\n" \
"v_mfma_f32_16x16x32_bf16 v[12:15], a[52:55], a[20:23], v[12:15]\n" \
"buffer_load_dwordx4 v198, s[48:51], s77 offen offset:1024 lds\n" \
"Lpg8500_%=:\n" \
"Lst1x1_%=:\n" \
"s_cbranch_execz Lpg10819_%=\n" \
"v_mfma_f32_16x16x32_bf16 v[8:11], a[40:43], a[16:19], v[8:11]\n" \
"ds_read_b64_tr_b16 a[210:211], v209 offset:5120\n" \
"v_mfma_f32_16x16x32_bf16 v[8:11], a[44:47], a[20:23], v[8:11]\n" \
"ds_read_b64_tr_b16 a[212:213], v209 offset:14336\n" \
"v_mfma_f32_16x16x32_bf16 v[48:51], a[32:35], a[16:19], v[48:51]\n" \
"s_waitcnt lgkmcnt(3)\n" \
"ds_read_b64_tr_b16 a[214:215], v209 offset:17408\n" \
"v_mfma_f32_16x16x32_bf16 v[48:51], a[36:39], a[20:23], v[48:51]\n" \
"buffer_load_dwordx4 v199, s[48:51], s77 offen offset:2048 lds\n" \
"v_mfma_f32_16x16x32_bf16 v[56:59], a[32:35], a[24:27], v[56:59]\n" \
"ds_read_b64_tr_b16 a[216:217], v210 offset:2048\n" \
"v_mfma_f32_16x16x32_bf16 v[56:59], a[36:39], a[28:31], v[56:59]\n" \
"ds_read_b64_tr_b16 a[218:219], v210 offset:5120\n" \
"v_mfma_f32_16x16x32_bf16 v[16:19], a[40:43], a[24:27], v[16:19]\n" \
"ds_read_b64_tr_b16 a[220:221], v210 offset:14336\n" \
"v_mfma_f32_16x16x32_bf16 v[16:19], a[44:47], a[28:31], v[16:19]\n" \
"v_mfma_f32_16x16x32_bf16 v[20:23], a[48:51], a[24:27], v[20:23]\n" \
"ds_read_b64_tr_b16 a[222:223], v210 offset:17408\n" \
"v_mfma_f32_16x16x32_bf16 v[20:23], a[52:55], a[28:31], v[20:23]\n" \
"v_mfma_f32_16x16x32_bf16 v[28:31], a[48:51], a[192:195], v[28:31]\n" \
"v_mfma_f32_16x16x32_bf16 v[28:31], a[52:55], a[196:199], v[28:31]\n" \
"buffer_load_dwordx2 v[226:227], v228, s[56:59], s13 offen sc1\n" \
"v_mfma_f32_16x16x32_bf16 v[24:27], a[40:43], a[192:195], v[24:27]\n" \
"v_mfma_f32_16x16x32_bf16 v[24:27], a[44:47], a[196:199], v[24:27]\n" \
"v_mfma_f32_16x16x32_bf16 v[64:67], a[32:35], a[192:195], v[64:67]\n" \
"v_mfma_f32_16x16x32_bf16 v[64:67], a[36:39], a[196:199], v[64:67]\n" \
"s_waitcnt vmcnt(12) lgkmcnt(0)\n" \
"s_barrier\n" \
"v_mfma_f32_16x16x32_bf16 v[68:71], a[200:203], a[192:195], v[68:71]\n" \
"ds_read_b64_tr_b16 a[32:33], v204 offset:17408\n" \
"ds_read_b64_tr_b16 a[34:35], v204 offset:20480\n" \
"v_mfma_f32_16x16x32_bf16 v[68:71], a[204:207], a[196:199], v[68:71]\n" \
"ds_read_b64_tr_b16 a[36:37], v204 offset:29696\n" \
"ds_read_b64_tr_b16 a[38:39], v204 offset:32768\n" \
"v_mfma_f32_16x16x32_bf16 v[60:63], a[200:203], a[24:27], v[60:63]\n" \
"s_add_u32 m0, s79, 65536\n" \
"ds_read_b64_tr_b16 a[40:41], v204 offset:16384\n" \
"v_mfma_f32_16x16x32_bf16 v[60:63], a[204:207], a[28:31], v[60:63]\n" \
"buffer_load_dwordx4 v192, s[44:47], s73 offen offset:0 lds\n" \
"v_mfma_f32_16x16x32_bf16 v[52:55], a[200:203], a[16:19], v[52:55]\n" \
"ds_read_b64_tr_b16 a[42:43], v204 offset:19456\n" \
"v_mfma_f32_16x16x32_bf16 v[52:55], a[204:207], a[20:23], v[52:55]\n" \
"ds_read_b64_tr_b16 a[44:45], v204 offset:28672\n" \
"v_mfma_f32_16x16x32_bf16 v[44:47], a[200:203], a[8:11], v[44:47]\n" \
"ds_read_b64_tr_b16 a[46:47], v204 offset:31744\n" \
"v_mfma_f32_16x16x32_bf16 v[44:47], a[204:207], a[12:15], v[44:47]\n" \
"buffer_load_dwordx4 v193, s[44:47], s73 offen offset:1024 lds\n" \
"v_mfma_f32_16x16x32_bf16 v[72:75], a[0:3], a[208:211], v[72:75]\n" \
"ds_read_b64_tr_b16 a[48:49], v222 offset:16384\n" \
"v_mfma_f32_16x16x32_bf16 v[72:75], a[4:7], a[212:215], v[72:75]\n" \
"ds_read_b128 a[0:3], v202 offset:7168\n" \
"v_mfma_f32_16x16x32_bf16 v[80:83], a[8:11], a[208:211], v[80:83]\n" \
"ds_read_b128 a[4:7], v203 offset:7168\n" \
"v_mfma_f32_16x16x32_bf16 v[80:83], a[12:15], a[212:215], v[80:83]\n" \
"buffer_load_dwordx4 v192, s[44:47], s73 offen offset:2048 lds\n" \
"v_mfma_f32_16x16x32_bf16 v[84:87], a[8:11], a[216:219], v[84:87]\n" \
"ds_read_b64_tr_b16 a[50:51], v222 offset:19456\n" \
"v_mfma_f32_16x16x32_bf16 v[84:87], a[12:15], a[220:223], v[84:87]\n" \
"ds_read_b128 a[8:11], v200 offset:0\n" \
"v_mfma_f32_16x16x32_bf16 v[92:95], a[16:19], a[216:219], v[92:95]\n" \
"ds_read_b128 a[12:15], v201 offset:0\n" \
"v_mfma_f32_16x16x32_bf16 v[92:95], a[20:23], a[220:223], v[92:95]\n" \
"buffer_load_dwordx4 v193, s[44:47], s73 offen offset:3072 lds\n" \
"v_mfma_f32_16x16x32_bf16 v[88:91], a[16:19], a[208:211], v[88:91]\n" \
"s_xor_b32 s73, s73, 0x180\n" \
"s_mul_i32 s76, s80, 393216\n" \
"ds_read_b64_tr_b16 a[52:53], v222 offset:28672\n" \
"v_mfma_f32_16x16x32_bf16 v[88:91], a[20:23], a[212:215], v[88:91]\n" \
"s_add_u32 s76, s76, s78\n" \
"ds_read_b128 a[16:19], v200 offset:4096\n" \
"v_mfma_f32_16x16x32_bf16 v[96:99], a[24:27], a[208:211], v[96:99]\n" \
"s_add_u32 m0, s91, 81920\n" \
"ds_read_b128 a[20:23], v201 offset:4096\n" \
"v_mfma_f32_16x16x32_bf16 v[96:99], a[28:31], a[212:215], v[96:99]\n" \
"buffer_load_dwordx4 v197, s[48:51], s76 offen offset:0 lds\n" \
"v_mfma_f32_16x16x32_bf16 v[100:103], a[24:27], a[216:219], v[100:103]\n" \
"ds_read_b64_tr_b16 a[54:55], v222 offset:31744\n" \
"v_mfma_f32_16x16x32_bf16 v[100:103], a[28:31], a[220:223], v[100:103]\n" \
"ds_read_b128 a[24:27], v200 offset:8192\n" \
"v_mfma_f32_16x16x32_bf16 v[108:111], a[192:195], a[216:219], v[108:111]\n" \
"s_add_u32 s77, s76, 24576\n" \
"ds_read_b128 a[28:31], v201 offset:8192\n" \
"v_mfma_f32_16x16x32_bf16 v[108:111], a[196:199], a[220:223], v[108:111]\n" \
"buffer_load_dwordx4 v198, s[48:51], s76 offen offset:1024 lds\n" \
"v_mfma_f32_16x16x32_bf16 v[104:107], a[192:195], a[208:211], v[104:107]\n" \
"v_mfma_f32_16x16x32_bf16 v[104:107], a[196:199], a[212:215], v[104:107]\n" \
"s_waitcnt lgkmcnt(2)\n" \
"v_mfma_f32_16x16x32_bf16 v[32:35], a[32:35], a[0:3], v[32:35]\n" \
"s_xor_b32 s80, s80, 0x3\n" \
"ds_read_b128 a[192:195], v200 offset:12288\n" \
"v_mfma_f32_16x16x32_bf16 v[32:35], a[36:39], a[4:7], v[32:35]\n" \
"buffer_load_dwordx4 v199, s[48:51], s76 offen offset:2048 lds\n" \
"v_mfma_f32_16x16x32_bf16 v[40:43], a[32:35], a[8:11], v[40:43]\n" \
"ds_read_b128 a[196:199], v201 offset:12288\n" \
"v_mfma_f32_16x16x32_bf16 v[40:43], a[36:39], a[12:15], v[40:43]\n" \
"ds_read_b64_tr_b16 a[200:201], v222 offset:17408\n" \
"v_mfma_f32_16x16x32_bf16 v[0:3], a[40:43], a[8:11], v[0:3]\n" \
"s_add_u32 m0, m0, 3072\n" \
"ds_read_b64_tr_b16 a[202:203], v222 offset:20480\n" \
"v_mfma_f32_16x16x32_bf16 v[0:3], a[44:47], a[12:15], v[0:3]\n" \
"buffer_load_dwordx4 v197, s[48:51], s77 offen offset:0 lds\n" \
"v_mfma_f32_16x16x32_bf16 v[4:7], a[48:51], a[8:11], v[4:7]\n" \
"ds_read_b64_tr_b16 a[204:205], v222 offset:29696\n" \
"v_mfma_f32_16x16x32_bf16 v[4:7], a[52:55], a[12:15], v[4:7]\n" \
"ds_read_b64_tr_b16 a[206:207], v222 offset:32768\n" \
"v_mfma_f32_16x16x32_bf16 v[12:15], a[48:51], a[16:19], v[12:15]\n" \
"ds_read_b64_tr_b16 a[208:209], v204 offset:18432\n" \
"v_mfma_f32_16x16x32_bf16 v[12:15], a[52:55], a[20:23], v[12:15]\n" \
"buffer_load_dwordx4 v198, s[48:51], s77 offen offset:1024 lds\n" \
"v_mfma_f32_16x16x32_bf16 v[8:11], a[40:43], a[16:19], v[8:11]\n" \
"ds_read_b64_tr_b16 a[210:211], v204 offset:21504\n" \
"v_mfma_f32_16x16x32_bf16 v[8:11], a[44:47], a[20:23], v[8:11]\n" \
"ds_read_b64_tr_b16 a[212:213], v204 offset:30720\n" \
"v_mfma_f32_16x16x32_bf16 v[48:51], a[32:35], a[16:19], v[48:51]\n" \
"s_waitcnt lgkmcnt(3)\n" \
"ds_read_b64_tr_b16 a[214:215], v204 offset:33792\n" \
"v_mfma_f32_16x16x32_bf16 v[48:51], a[36:39], a[20:23], v[48:51]\n" \
"buffer_load_dwordx4 v199, s[48:51], s77 offen offset:2048 lds\n" \
"v_mfma_f32_16x16x32_bf16 v[56:59], a[32:35], a[24:27], v[56:59]\n" \
"ds_read_b64_tr_b16 a[216:217], v222 offset:18432\n" \
"v_mfma_f32_16x16x32_bf16 v[56:59], a[36:39], a[28:31], v[56:59]\n" \
"ds_read_b64_tr_b16 a[218:219], v222 offset:21504\n" \
"v_mfma_f32_16x16x32_bf16 v[16:19], a[40:43], a[24:27], v[16:19]\n" \
"ds_read_b64_tr_b16 a[220:221], v222 offset:30720\n" \
"v_mfma_f32_16x16x32_bf16 v[16:19], a[44:47], a[28:31], v[16:19]\n" \
"v_mfma_f32_16x16x32_bf16 v[20:23], a[48:51], a[24:27], v[20:23]\n" \
"ds_read_b64_tr_b16 a[222:223], v222 offset:33792\n" \
"v_mfma_f32_16x16x32_bf16 v[20:23], a[52:55], a[28:31], v[20:23]\n" \
"v_mfma_f32_16x16x32_bf16 v[28:31], a[48:51], a[192:195], v[28:31]\n" \
"v_mfma_f32_16x16x32_bf16 v[28:31], a[52:55], a[196:199], v[28:31]\n" \
"v_mfma_f32_16x16x32_bf16 v[24:27], a[40:43], a[192:195], v[24:27]\n" \
"v_mfma_f32_16x16x32_bf16 v[24:27], a[44:47], a[196:199], v[24:27]\n" \
"v_mfma_f32_16x16x32_bf16 v[64:67], a[32:35], a[192:195], v[64:67]\n" \
"v_mfma_f32_16x16x32_bf16 v[64:67], a[36:39], a[196:199], v[64:67]\n" \
"s_waitcnt vmcnt(11) lgkmcnt(0)\n" \
"s_barrier\n" \
"v_mfma_f32_16x16x32_bf16 v[68:71], a[200:203], a[192:195], v[68:71]\n" \
"ds_read_b64_tr_b16 a[32:33], v209 offset:25600\n" \
"ds_read_b64_tr_b16 a[34:35], v209 offset:28672\n" \
"v_mfma_f32_16x16x32_bf16 v[68:71], a[204:207], a[196:199], v[68:71]\n" \
"ds_read_b64_tr_b16 a[36:37], v209 offset:37888\n" \
"ds_read_b64_tr_b16 a[38:39], v209 offset:40960\n" \
"v_mfma_f32_16x16x32_bf16 v[60:63], a[200:203], a[24:27], v[60:63]\n" \
"s_add_u32 m0, s79, 0\n" \
"ds_read_b64_tr_b16 a[40:41], v209 offset:24576\n" \
"v_mfma_f32_16x16x32_bf16 v[60:63], a[204:207], a[28:31], v[60:63]\n" \
"buffer_load_dwordx4 v192, s[44:47], s73 offen offset:0 lds\n" \
"v_mfma_f32_16x16x32_bf16 v[52:55], a[200:203], a[16:19], v[52:55]\n" \
"ds_read_b64_tr_b16 a[42:43], v209 offset:27648\n" \
"v_mfma_f32_16x16x32_bf16 v[52:55], a[204:207], a[20:23], v[52:55]\n" \
"ds_read_b64_tr_b16 a[44:45], v209 offset:36864\n" \
"v_mfma_f32_16x16x32_bf16 v[44:47], a[200:203], a[8:11], v[44:47]\n" \
"ds_read_b64_tr_b16 a[46:47], v209 offset:39936\n" \
"v_mfma_f32_16x16x32_bf16 v[44:47], a[204:207], a[12:15], v[44:47]\n" \
"buffer_load_dwordx4 v193, s[44:47], s73 offen offset:1024 lds\n" \
"v_mfma_f32_16x16x32_bf16 v[72:75], a[0:3], a[208:211], v[72:75]\n" \
"ds_read_b64_tr_b16 a[48:49], v210 offset:24576\n" \
"v_mfma_f32_16x16x32_bf16 v[72:75], a[4:7], a[212:215], v[72:75]\n" \
"ds_read_b128 a[0:3], v202 offset:16384\n" \
"v_mfma_f32_16x16x32_bf16 v[80:83], a[8:11], a[208:211], v[80:83]\n" \
"ds_read_b128 a[4:7], v203 offset:16384\n" \
"v_mfma_f32_16x16x32_bf16 v[80:83], a[12:15], a[212:215], v[80:83]\n" \
"buffer_load_dwordx4 v192, s[44:47], s73 offen offset:2048 lds\n" \
"v_mfma_f32_16x16x32_bf16 v[84:87], a[8:11], a[216:219], v[84:87]\n" \
"ds_read_b64_tr_b16 a[50:51], v210 offset:27648\n" \
"v_mfma_f32_16x16x32_bf16 v[84:87], a[12:15], a[220:223], v[84:87]\n" \
"ds_read_b128 a[8:11], v200 offset:40960\n" \
"v_mfma_f32_16x16x32_bf16 v[92:95], a[16:19], a[216:219], v[92:95]\n" \
"ds_read_b128 a[12:15], v201 offset:40960\n" \
"v_mfma_f32_16x16x32_bf16 v[92:95], a[20:23], a[220:223], v[92:95]\n" \
"buffer_load_dwordx4 v193, s[44:47], s73 offen offset:3072 lds\n" \
"v_mfma_f32_16x16x32_bf16 v[88:91], a[16:19], a[208:211], v[88:91]\n" \
"s_xor_b32 s73, s73, 0x300\n" \
"s_mul_i32 s76, s80, 393216\n" \
"ds_read_b64_tr_b16 a[52:53], v210 offset:36864\n" \
"v_mfma_f32_16x16x32_bf16 v[88:91], a[20:23], a[212:215], v[88:91]\n" \
"s_add_u32 s76, s76, s78\n" \
"ds_read_b128 a[16:19], v200 offset:45056\n" \
"v_mfma_f32_16x16x32_bf16 v[96:99], a[24:27], a[208:211], v[96:99]\n" \
"s_add_u32 m0, s91, 16384\n" \
"ds_read_b128 a[20:23], v201 offset:45056\n" \
"v_mfma_f32_16x16x32_bf16 v[96:99], a[28:31], a[212:215], v[96:99]\n" \
"buffer_load_dwordx4 v197, s[48:51], s76 offen offset:0 lds\n" \
"v_mfma_f32_16x16x32_bf16 v[100:103], a[24:27], a[216:219], v[100:103]\n" \
"ds_read_b64_tr_b16 a[54:55], v210 offset:39936\n" \
"v_mfma_f32_16x16x32_bf16 v[100:103], a[28:31], a[220:223], v[100:103]\n" \
"ds_read_b128 a[24:27], v200 offset:49152\n" \
"v_mfma_f32_16x16x32_bf16 v[108:111], a[192:195], a[216:219], v[108:111]\n" \
"s_add_u32 s77, s76, 24576\n" \
"ds_read_b128 a[28:31], v201 offset:49152\n" \
"v_mfma_f32_16x16x32_bf16 v[108:111], a[196:199], a[220:223], v[108:111]\n" \
"buffer_load_dwordx4 v198, s[48:51], s76 offen offset:1024 lds\n" \
"v_mfma_f32_16x16x32_bf16 v[104:107], a[192:195], a[208:211], v[104:107]\n" \
"v_mfma_f32_16x16x32_bf16 v[104:107], a[196:199], a[212:215], v[104:107]\n" \
"s_waitcnt lgkmcnt(2)\n" \
"v_mfma_f32_16x16x32_bf16 v[32:35], a[32:35], a[0:3], v[32:35]\n" \
"s_xor_b32 s80, s80, 0x6\n" \
"ds_read_b128 a[192:195], v200 offset:53248\n" \
"v_mfma_f32_16x16x32_bf16 v[32:35], a[36:39], a[4:7], v[32:35]\n" \
"buffer_load_dwordx4 v199, s[48:51], s76 offen offset:2048 lds\n" \
"v_mfma_f32_16x16x32_bf16 v[40:43], a[32:35], a[8:11], v[40:43]\n" \
"ds_read_b128 a[196:199], v201 offset:53248\n" \
"v_mfma_f32_16x16x32_bf16 v[40:43], a[36:39], a[12:15], v[40:43]\n" \
"ds_read_b64_tr_b16 a[200:201], v210 offset:25600\n" \
"v_mfma_f32_16x16x32_bf16 v[0:3], a[40:43], a[8:11], v[0:3]\n" \
"s_add_u32 m0, m0, 3072\n" \
"ds_read_b64_tr_b16 a[202:203], v210 offset:28672\n" \
"v_mfma_f32_16x16x32_bf16 v[0:3], a[44:47], a[12:15], v[0:3]\n" \
"buffer_load_dwordx4 v197, s[48:51], s77 offen offset:0 lds\n" \
"v_mfma_f32_16x16x32_bf16 v[4:7], a[48:51], a[8:11], v[4:7]\n" \
"ds_read_b64_tr_b16 a[204:205], v210 offset:37888\n" \
"v_mfma_f32_16x16x32_bf16 v[4:7], a[52:55], a[12:15], v[4:7]\n" \
"ds_read_b64_tr_b16 a[206:207], v210 offset:40960\n" \
"v_mfma_f32_16x16x32_bf16 v[12:15], a[48:51], a[16:19], v[12:15]\n" \
"ds_read_b64_tr_b16 a[208:209], v209 offset:26624\n" \
"v_mfma_f32_16x16x32_bf16 v[12:15], a[52:55], a[20:23], v[12:15]\n" \
"buffer_load_dwordx4 v198, s[48:51], s77 offen offset:1024 lds\n" \
"v_mfma_f32_16x16x32_bf16 v[8:11], a[40:43], a[16:19], v[8:11]\n" \
"ds_read_b64_tr_b16 a[210:211], v209 offset:29696\n" \
"v_mfma_f32_16x16x32_bf16 v[8:11], a[44:47], a[20:23], v[8:11]\n" \
"ds_read_b64_tr_b16 a[212:213], v209 offset:38912\n" \
"v_mfma_f32_16x16x32_bf16 v[48:51], a[32:35], a[16:19], v[48:51]\n" \
"s_waitcnt lgkmcnt(3)\n" \
"ds_read_b64_tr_b16 a[214:215], v209 offset:41984\n" \
"v_mfma_f32_16x16x32_bf16 v[48:51], a[36:39], a[20:23], v[48:51]\n" \
"buffer_load_dwordx4 v199, s[48:51], s77 offen offset:2048 lds\n" \
"v_mfma_f32_16x16x32_bf16 v[56:59], a[32:35], a[24:27], v[56:59]\n" \
"ds_read_b64_tr_b16 a[216:217], v210 offset:26624\n" \
"v_mfma_f32_16x16x32_bf16 v[56:59], a[36:39], a[28:31], v[56:59]\n" \
"ds_read_b64_tr_b16 a[218:219], v210 offset:29696\n" \
"v_mfma_f32_16x16x32_bf16 v[16:19], a[40:43], a[24:27], v[16:19]\n" \
"ds_read_b64_tr_b16 a[220:221], v210 offset:38912\n" \
"v_mfma_f32_16x16x32_bf16 v[16:19], a[44:47], a[28:31], v[16:19]\n" \
"v_mfma_f32_16x16x32_bf16 v[20:23], a[48:51], a[24:27], v[20:23]\n" \
"ds_read_b64_tr_b16 a[222:223], v210 offset:41984\n" \
"v_mfma_f32_16x16x32_bf16 v[20:23], a[52:55], a[28:31], v[20:23]\n" \
"v_mfma_f32_16x16x32_bf16 v[28:31], a[48:51], a[192:195], v[28:31]\n" \
"v_mfma_f32_16x16x32_bf16 v[28:31], a[52:55], a[196:199], v[28:31]\n" \
"v_mfma_f32_16x16x32_bf16 v[24:27], a[40:43], a[192:195], v[24:27]\n" \
"v_mfma_f32_16x16x32_bf16 v[24:27], a[44:47], a[196:199], v[24:27]\n" \
"v_mfma_f32_16x16x32_bf16 v[64:67], a[32:35], a[192:195], v[64:67]\n" \
"v_mfma_f32_16x16x32_bf16 v[64:67], a[36:39], a[196:199], v[64:67]\n" \
"s_waitcnt vmcnt(10) lgkmcnt(0)\n" \
"s_barrier\n" \
"v_mfma_f32_16x16x32_bf16 v[68:71], a[200:203], a[192:195], v[68:71]\n" \
"ds_read_b64_tr_b16 a[32:33], v209 offset:1024\n" \
"ds_read_b64_tr_b16 a[34:35], v209 offset:4096\n" \
"v_mfma_f32_16x16x32_bf16 v[68:71], a[204:207], a[196:199], v[68:71]\n" \
"ds_read_b64_tr_b16 a[36:37], v209 offset:13312\n" \
"ds_read_b64_tr_b16 a[38:39], v209 offset:16384\n" \
"v_mfma_f32_16x16x32_bf16 v[60:63], a[200:203], a[24:27], v[60:63]\n" \
"s_add_u32 m0, s79, 40960\n" \
"ds_read_b64_tr_b16 a[40:41], v209 offset:0\n" \
"v_cmp_eq_u32 vcc, s60, v226\n" \
"v_mfma_f32_16x16x32_bf16 v[60:63], a[204:207], a[28:31], v[60:63]\n" \
"buffer_load_dwordx4 v192, s[44:47], s73 offen offset:0 lds\n" \
"v_mfma_f32_16x16x32_bf16 v[52:55], a[200:203], a[16:19], v[52:55]\n" \
"s_andn2_b32 s70, 0xff, vcc_lo\n" \
"s_nop 0\n" \
"s_cbranch_scc1 Lpl4_%=\n" \
"Lpr4_%=:\n" \
"ds_read_b64_tr_b16 a[42:43], v209 offset:3072\n" \
"v_mfma_f32_16x16x32_bf16 v[52:55], a[204:207], a[20:23], v[52:55]\n" \
"s_and_b32 s70, %1, 7\n" \
"ds_read_b64_tr_b16 a[44:45], v209 offset:12288\n" \
"v_readlane_b32 s70, v227, s70\n" \
"v_mfma_f32_16x16x32_bf16 v[44:47], a[200:203], a[8:11], v[44:47]\n" \
"ds_read_b64_tr_b16 a[46:47], v209 offset:15360\n" \
"v_mfma_f32_16x16x32_bf16 v[44:47], a[204:207], a[12:15], v[44:47]\n" \
"buffer_load_dwordx4 v193, s[44:47], s73 offen offset:1024 lds\n" \
"v_mfma_f32_16x16x32_bf16 v[72:75], a[0:3], a[208:211], v[72:75]\n" \
"ds_read_b64_tr_b16 a[48:49], v210 offset:0\n" \
"v_subrev_u32 v227, s70, v227\n" \
"v_mfma_f32_16x16x32_bf16 v[72:75], a[4:7], a[212:215], v[72:75]\n" \
"ds_read_b128 a[0:3], v202 offset:17408\n" \
"v_cmp_gt_i32 vcc, 0, v227\n" \
"v_mfma_f32_16x16x32_bf16 v[80:83], a[8:11], a[208:211], v[80:83]\n" \
"ds_read_b128 a[4:7], v203 offset:17408\n" \
"v_mfma_f32_16x16x32_bf16 v[80:83], a[12:15], a[212:215], v[80:83]\n" \
"buffer_load_dwordx4 v192, s[44:47], s73 offen offset:2048 lds\n" \
"v_mfma_f32_16x16x32_bf16 v[84:87], a[8:11], a[216:219], v[84:87]\n" \
"s_and_b32 s13, vcc_lo, 0xff\n" \
"s_bcnt1_i32_b32 s13, s13\n" \
"ds_read_b64_tr_b16 a[50:51], v210 offset:3072\n" \
"v_mfma_f32_16x16x32_bf16 v[84:87], a[12:15], a[220:223], v[84:87]\n" \
"s_lshr_b32 s90, s13, 2\n" \
"s_mul_i32 s70, s90, 7\n" \
"ds_read_b128 a[8:11], v205 offset:0\n" \
"v_mfma_f32_16x16x32_bf16 v[92:95], a[16:19], a[216:219], v[92:95]\n" \
"s_xor_b32 s13, s13, s70\n" \
"s_lshl_b32 s70, s13, 3\n" \
"ds_read_b128 a[12:15], v206 offset:0\n" \
"v_mfma_f32_16x16x32_bf16 v[92:95], a[20:23], a[220:223], v[92:95]\n" \
"buffer_load_dwordx4 v193, s[44:47], s73 offen offset:3072 lds\n" \
"v_mfma_f32_16x16x32_bf16 v[88:91], a[16:19], a[208:211], v[88:91]\n" \
"s_xor_b32 s73, s73, 0x80\n" \
"s_mul_i32 s76, s80, 393216\n" \
"ds_read_b64_tr_b16 a[52:53], v210 offset:12288\n" \
"v_mfma_f32_16x16x32_bf16 v[88:91], a[20:23], a[212:215], v[88:91]\n" \
"s_add_u32 s76, s76, s78\n" \
"s_lshr_b32 s9, 0x2a2b2d2f, s70\n" \
"ds_read_b128 a[16:19], v205 offset:4096\n" \
"v_mfma_f32_16x16x32_bf16 v[96:99], a[24:27], a[208:211], v[96:99]\n" \
"s_add_u32 m0, s91, 106496\n" \
"s_and_b32 s9, s9, 0xff\n" \
"ds_read_b128 a[20:23], v206 offset:4096\n" \
"v_mfma_f32_16x16x32_bf16 v[96:99], a[28:31], a[212:215], v[96:99]\n" \
"buffer_load_dwordx4 v197, s[48:51], s76 offen offset:0 lds\n" \
"v_mfma_f32_16x16x32_bf16 v[100:103], a[24:27], a[216:219], v[100:103]\n" \
"s_lshr_b32 s8, %1, 6\n" \
"s_lshl2_add_u32 s8, s13, s8\n" \
"ds_read_b64_tr_b16 a[54:55], v210 offset:15360\n" \
"v_mfma_f32_16x16x32_bf16 v[100:103], a[28:31], a[220:223], v[100:103]\n" \
"ds_read_b128 a[24:27], v205 offset:8192\n" \
"v_max_i32_dpp v226, v227, v227 quad_perm:[1,0,3,2] row_mask:0xf bank_mask:0xf\n" \
"v_mfma_f32_16x16x32_bf16 v[108:111], a[192:195], a[216:219], v[108:111]\n" \
"s_add_u32 s77, s76, 24576\n" \
"s_mov_b32 s14, 0\n" \
"ds_read_b128 a[28:31], v206 offset:8192\n" \
"v_mfma_f32_16x16x32_bf16 v[108:111], a[196:199], a[220:223], v[108:111]\n" \
"buffer_load_dwordx4 v198, s[48:51], s76 offen offset:1024 lds\n" \
"v_mfma_f32_16x16x32_bf16 v[104:107], a[192:195], a[208:211], v[104:107]\n" \
"v_readlane_b32 s15, v226, 2\n" \
"v_readlane_b32 s16, v226, 4\n" \
"v_mfma_f32_16x16x32_bf16 v[104:107], a[196:199], a[212:215], v[104:107]\n" \
"s_waitcnt lgkmcnt(2)\n" \
"v_readlane_b32 s17, v226, 6\n" \
"v_cmp_gt_i32 vcc, s15, v226\n" \
"v_mfma_f32_16x16x32_bf16 v[32:35], a[32:35], a[0:3], v[32:35]\n" \
"s_xor_b32 s80, s80, 0x1\n" \
"ds_read_b128 a[192:195], v205 offset:12288\n" \
"v_mfma_f32_16x16x32_bf16 v[32:35], a[36:39], a[4:7], v[32:35]\n" \
"buffer_load_dwordx4 v199, s[48:51], s76 offen offset:2048 lds\n" \
"v_mfma_f32_16x16x32_bf16 v[40:43], a[32:35], a[8:11], v[40:43]\n" \
"s_and_b32 s70, vcc_lo, 0x55\n" \
"s_bcnt1_i32_b32 s70, s70\n" \
"ds_read_b128 a[196:199], v206 offset:12288\n" \
"v_mfma_f32_16x16x32_bf16 v[40:43], a[36:39], a[12:15], v[40:43]\n" \
"s_cmp_eq_u32 s70, s13\n" \
"s_cselect_b32 s14, 4, s14\n" \
"ds_read_b64_tr_b16 a[200:201], v210 offset:1024\n" \
"v_mfma_f32_16x16x32_bf16 v[0:3], a[40:43], a[8:11], v[0:3]\n" \
"s_add_u32 m0, m0, 3072\n" \
"ds_read_b64_tr_b16 a[202:203], v210 offset:4096\n" \
"v_cmp_gt_i32 vcc, s16, v226\n" \
"v_mfma_f32_16x16x32_bf16 v[0:3], a[44:47], a[12:15], v[0:3]\n" \
"buffer_load_dwordx4 v197, s[48:51], s77 offen offset:0 lds\n" \
"v_mfma_f32_16x16x32_bf16 v[4:7], a[48:51], a[8:11], v[4:7]\n" \
"s_and_b32 s70, vcc_lo, 0x55\n" \
"s_bcnt1_i32_b32 s70, s70\n" \
"ds_read_b64_tr_b16 a[204:205], v210 offset:13312\n" \
"v_mfma_f32_16x16x32_bf16 v[4:7], a[52:55], a[12:15], v[4:7]\n" \
"s_cmp_eq_u32 s70, s13\n" \
"s_cselect_b32 s14, 8, s14\n" \
"ds_read_b64_tr_b16 a[206:207], v210 offset:16384\n" \
"v_mfma_f32_16x16x32_bf16 v[12:15], a[48:51], a[16:19], v[12:15]\n" \
"ds_read_b64_tr_b16 a[208:209], v209 offset:2048\n" \
"v_cmp_gt_i32 vcc, s17, v226\n" \
"v_mfma_f32_16x16x32_bf16 v[12:15], a[52:55], a[20:23], v[12:15]\n" \
"buffer_load_dwordx4 v198, s[48:51], s77 offen offset:1024 lds\n" \
"v_mfma_f32_16x16x32_bf16 v[8:11], a[40:43], a[16:19], v[8:11]\n" \
"s_and_b32 s70, vcc_lo, 0x55\n" \
"s_bcnt1_i32_b32 s70, s70\n" \
"ds_read_b64_tr_b16 a[210:211], v209 offset:5120\n" \
"v_mfma_f32_16x16x32_bf16 v[8:11], a[44:47], a[20:23], v[8:11]\n" \
"s_cmp_eq_u32 s70, s13\n" \
"s_cselect_b32 s14, 12, s14\n" \
"ds_read_b64_tr_b16 a[212:213], v209 offset:14336\n" \
"v_mfma_f32_16x16x32_bf16 v[48:51], a[32:35], a[16:19], v[48:51]\n" \
"s_lshr_b32 s70, %1, 6\n" \
"s_add_u32 s14, s14, s70\n" \
"ds_read_b64_tr_b16 a[214:215], v209 offset:17408\n" \
"v_mfma_f32_16x16x32_bf16 v[48:51], a[36:39], a[20:23], v[48:51]\n" \
"s_waitcnt lgkmcnt(3)\n" \
"buffer_load_dwordx4 v199, s[48:51], s77 offen offset:2048 lds\n" \
"v_mfma_f32_16x16x32_bf16 v[56:59], a[32:35], a[24:27], v[56:59]\n" \
"s_cmp_eq_u32 s90, 0\n" \
"s_cselect_b32 s14, s8, s14\n" \
"ds_read_b64_tr_b16 a[216:217], v210 offset:2048\n" \
"v_mfma_f32_16x16x32_bf16 v[56:59], a[36:39], a[28:31], v[56:59]\n" \
"s_sub_u32 s10, 49, s9\n" \
"s_lshl_b32 s11, s9, 1\n" \
"ds_read_b64_tr_b16 a[218:219], v210 offset:5120\n" \
"v_mfma_f32_16x16x32_bf16 v[16:19], a[40:43], a[24:27], v[16:19]\n" \
"s_sub_u32 s11, s11, 71\n" \
"s_lshl2_add_u32 s70, s8, s61\n" \
"ds_read_b64_tr_b16 a[220:221], v210 offset:14336\n" \
"v_mfma_f32_16x16x32_bf16 v[16:19], a[44:47], a[28:31], v[16:19]\n" \
"s_mul_i32 s81, s70, 266240\n" \
"s_mul_i32 s82, s90, s9\n" \
"s_lshl_b32 s71, s82, 12\n" \
"v_mfma_f32_16x16x32_bf16 v[20:23], a[48:51], a[24:27], v[20:23]\n" \
"s_add_u32 s81, s81, s71\n" \
"s_add_u32 s81, s81, 35700736\n" \
"ds_read_b64_tr_b16 a[222:223], v210 offset:17408\n" \
"v_mfma_f32_16x16x32_bf16 v[20:23], a[52:55], a[28:31], v[20:23]\n" \
"s_lshl2_add_u32 s70, s14, s61\n" \
"s_mul_i32 s83, s70, 69632\n" \
"s_add_u32 s83, s83, 52740096\n" \
"v_mfma_f32_16x16x32_bf16 v[28:31], a[48:51], a[192:195], v[28:31]\n" \
"s_lshl_b32 s82, s82, 17\n" \
"s_add_u32 s82, s82, s84\n" \
"s_lshl_b32 s70, s14, 19\n" \
"v_mfma_f32_16x16x32_bf16 v[28:31], a[52:55], a[196:199], v[28:31]\n" \
"s_add_u32 s88, s88, s70\n" \
"s_lshl_b32 s71, s90, 11\n" \
"s_add_u32 s88, s88, s71\n" \
"v_mfma_f32_16x16x32_bf16 v[24:27], a[40:43], a[192:195], v[24:27]\n" \
"s_lshl3_add_u32 s70, s8, s68\n" \
"s_lshl2_add_u32 s87, s70, s61\n" \
"s_lshl_b32 s87, s87, 14\n" \
"v_mfma_f32_16x16x32_bf16 v[24:27], a[44:47], a[196:199], v[24:27]\n" \
"s_add_u32 s87, s87, 57196544\n" \
"s_lshl_b32 s64, s70, 7\n" \
"s_add_u32 s64, s64, 65601536\n" \
"v_mfma_f32_16x16x32_bf16 v[64:67], a[32:35], a[192:195], v[64:67]\n" \
"s_lshl_b32 s66, s8, 7\n" \
"s_lshl_b32 s67, s14, 7\n" \
"s_add_u32 s67, s67, 65591296\n" \
"v_mfma_f32_16x16x32_bf16 v[64:67], a[36:39], a[196:199], v[64:67]\n" \
"s_add_u32 s66, s66, 65589248\n" \
"s_waitcnt vmcnt(10) lgkmcnt(0)\n" \
"s_barrier\n" \
"v_mfma_f32_16x16x32_bf16 v[68:71], a[200:203], a[192:195], v[68:71]\n" \
"s_add_u32 s70, s9, 1\n" \
"ds_read_b64_tr_b16 a[32:33], v204 offset:17408\n" \
"ds_read_b64_tr_b16 a[34:35], v204 offset:20480\n" \
"v_mfma_f32_16x16x32_bf16 v[68:71], a[204:207], a[196:199], v[68:71]\n" \
"s_sub_u32 s70, s70, s90\n" \
"ds_read_b64_tr_b16 a[36:37], v204 offset:29696\n" \
"ds_read_b64_tr_b16 a[38:39], v204 offset:32768\n" \
"v_mfma_f32_16x16x32_bf16 v[60:63], a[200:203], a[24:27], v[60:63]\n" \
"s_add_u32 m0, s79, 65536\n" \
"s_lshr_b32 s70, s70, 1\n" \
"ds_read_b64_tr_b16 a[40:41], v204 offset:16384\n" \
"v_mfma_f32_16x16x32_bf16 v[60:63], a[204:207], a[28:31], v[60:63]\n" \
"buffer_load_dwordx4 v192, s[44:47], s73 offen offset:0 lds\n" \
"v_mfma_f32_16x16x32_bf16 v[52:55], a[200:203], a[16:19], v[52:55]\n" \
"s_bfm_b32 s65, s70, 0\n" \
"s_sub_u32 s70, 0, s90\n" \
"ds_read_b64_tr_b16 a[42:43], v204 offset:19456\n" \
"v_mfma_f32_16x16x32_bf16 v[52:55], a[204:207], a[20:23], v[52:55]\n" \
"s_xor_b32 s65, s65, s70\n" \
"s_and_b32 s74, s70, 0xffff\n" \
"ds_read_b64_tr_b16 a[44:45], v204 offset:28672\n" \
"v_mfma_f32_16x16x32_bf16 v[44:47], a[200:203], a[8:11], v[44:47]\n" \
"s_xor_b32 s68, s68, s8\n" \
"s_and_b32 s68, s68, 3\n" \
"ds_read_b64_tr_b16 a[46:47], v204 offset:31744\n" \
"v_mfma_f32_16x16x32_bf16 v[44:47], a[204:207], a[12:15], v[44:47]\n" \
"buffer_load_dwordx4 v193, s[44:47], s73 offen offset:1024 lds\n" \
"v_mfma_f32_16x16x32_bf16 v[72:75], a[0:3], a[208:211], v[72:75]\n" \
"s_lshl_b32 s68, s68, 12\n" \
"ds_read_b64_tr_b16 a[48:49], v222 offset:16384\n" \
"v_mfma_f32_16x16x32_bf16 v[72:75], a[4:7], a[212:215], v[72:75]\n" \
"ds_read_b128 a[0:3], v202 offset:18432\n" \
"v_mfma_f32_16x16x32_bf16 v[80:83], a[8:11], a[208:211], v[80:83]\n" \
"ds_read_b128 a[4:7], v203 offset:18432\n" \
"v_mfma_f32_16x16x32_bf16 v[80:83], a[12:15], a[212:215], v[80:83]\n" \
"buffer_load_dwordx4 v192, s[44:47], s73 offen offset:2048 lds\n" \
"v_mfma_f32_16x16x32_bf16 v[84:87], a[8:11], a[216:219], v[84:87]\n" \
"ds_read_b64_tr_b16 a[50:51], v222 offset:19456\n" \
"v_mfma_f32_16x16x32_bf16 v[84:87], a[12:15], a[220:223], v[84:87]\n" \
"ds_read_b128 a[8:11], v200 offset:0\n" \
"v_mfma_f32_16x16x32_bf16 v[92:95], a[16:19], a[216:219], v[92:95]\n" \
"ds_read_b128 a[12:15], v201 offset:0\n" \
"v_mfma_f32_16x16x32_bf16 v[92:95], a[20:23], a[220:223], v[92:95]\n" \
"buffer_load_dwordx4 v193, s[44:47], s73 offen offset:3072 lds\n" \
"v_mfma_f32_16x16x32_bf16 v[88:91], a[16:19], a[208:211], v[88:91]\n" \
"s_xor_b32 s73, s73, 0x180\n" \
"s_mul_i32 s76, s80, 393216\n" \
"ds_read_b64_tr_b16 a[52:53], v222 offset:28672\n" \
"v_mfma_f32_16x16x32_bf16 v[88:91], a[20:23], a[212:215], v[88:91]\n" \
"s_add_u32 s76, s76, s78\n" \
"ds_read_b128 a[16:19], v200 offset:4096\n" \
"v_mfma_f32_16x16x32_bf16 v[96:99], a[24:27], a[208:211], v[96:99]\n" \
"s_add_u32 m0, s91, 81920\n" \
"ds_read_b128 a[20:23], v201 offset:4096\n" \
"v_mfma_f32_16x16x32_bf16 v[96:99], a[28:31], a[212:215], v[96:99]\n" \
"buffer_load_dwordx4 v197, s[48:51], s76 offen offset:0 lds\n" \
"v_mfma_f32_16x16x32_bf16 v[100:103], a[24:27], a[216:219], v[100:103]\n" \
"ds_read_b64_tr_b16 a[54:55], v222 offset:31744\n" \
"v_mfma_f32_16x16x32_bf16 v[100:103], a[28:31], a[220:223], v[100:103]\n" \
"ds_read_b128 a[24:27], v200 offset:8192\n" \
"v_mfma_f32_16x16x32_bf16 v[108:111], a[192:195], a[216:219], v[108:111]\n" \
"s_add_u32 s77, s76, 24576\n" \
"ds_read_b128 a[28:31], v201 offset:8192\n" \
"v_mfma_f32_16x16x32_bf16 v[108:111], a[196:199], a[220:223], v[108:111]\n" \
"buffer_load_dwordx4 v198, s[48:51], s76 offen offset:1024 lds\n" \
"v_mfma_f32_16x16x32_bf16 v[104:107], a[192:195], a[208:211], v[104:107]\n" \
"v_mfma_f32_16x16x32_bf16 v[104:107], a[196:199], a[212:215], v[104:107]\n" \
"s_waitcnt lgkmcnt(2)\n" \
"v_mfma_f32_16x16x32_bf16 v[32:35], a[32:35], a[0:3], v[32:35]\n" \
"s_xor_b32 s80, s80, 0x3\n" \
"ds_read_b128 a[192:195], v200 offset:12288\n" \
"v_mfma_f32_16x16x32_bf16 v[32:35], a[36:39], a[4:7], v[32:35]\n" \
"buffer_load_dwordx4 v199, s[48:51], s76 offen offset:2048 lds\n" \
"v_mfma_f32_16x16x32_bf16 v[40:43], a[32:35], a[8:11], v[40:43]\n" \
"ds_read_b128 a[196:199], v201 offset:12288\n" \
"v_mfma_f32_16x16x32_bf16 v[40:43], a[36:39], a[12:15], v[40:43]\n" \
"ds_read_b64_tr_b16 a[200:201], v222 offset:17408\n" \
"v_mfma_f32_16x16x32_bf16 v[0:3], a[40:43], a[8:11], v[0:3]\n" \
"s_add_u32 m0, m0, 3072\n" \
"ds_read_b64_tr_b16 a[202:203], v222 offset:20480\n" \
"v_mfma_f32_16x16x32_bf16 v[0:3], a[44:47], a[12:15], v[0:3]\n" \
"buffer_load_dwordx4 v197, s[48:51], s77 offen offset:0 lds\n" \
"v_mfma_f32_16x16x32_bf16 v[4:7], a[48:51], a[8:11], v[4:7]\n" \
"ds_read_b64_tr_b16 a[204:205], v222 offset:29696\n" \
"v_mfma_f32_16x16x32_bf16 v[4:7], a[52:55], a[12:15], v[4:7]\n" \
"ds_read_b64_tr_b16 a[206:207], v222 offset:32768\n" \
"v_mfma_f32_16x16x32_bf16 v[12:15], a[48:51], a[16:19], v[12:15]\n" \
"ds_read_b64_tr_b16 a[208:209], v204 offset:18432\n" \
"v_mfma_f32_16x16x32_bf16 v[12:15], a[52:55], a[20:23], v[12:15]\n" \
"buffer_load_dwordx4 v198, s[48:51], s77 offen offset:1024 lds\n" \
"v_mfma_f32_16x16x32_bf16 v[8:11], a[40:43], a[16:19], v[8:11]\n" \
"ds_read_b64_tr_b16 a[210:211], v204 offset:21504\n" \
"v_mfma_f32_16x16x32_bf16 v[8:11], a[44:47], a[20:23], v[8:11]\n" \
"ds_read_b64_tr_b16 a[212:213], v204 offset:30720\n" \
"v_mfma_f32_16x16x32_bf16 v[48:51], a[32:35], a[16:19], v[48:51]\n" \
"s_waitcnt lgkmcnt(3)\n" \
"ds_read_b64_tr_b16 a[214:215], v204 offset:33792\n" \
"v_mfma_f32_16x16x32_bf16 v[48:51], a[36:39], a[20:23], v[48:51]\n" \
"buffer_load_dwordx4 v199, s[48:51], s77 offen offset:2048 lds\n" \
"v_mfma_f32_16x16x32_bf16 v[56:59], a[32:35], a[24:27], v[56:59]\n" \
"ds_read_b64_tr_b16 a[216:217], v222 offset:18432\n" \
"v_mfma_f32_16x16x32_bf16 v[56:59], a[36:39], a[28:31], v[56:59]\n" \
"ds_read_b64_tr_b16 a[218:219], v222 offset:21504\n" \
"v_mfma_f32_16x16x32_bf16 v[16:19], a[40:43], a[24:27], v[16:19]\n" \
"ds_read_b64_tr_b16 a[220:221], v222 offset:30720\n" \
"v_mfma_f32_16x16x32_bf16 v[16:19], a[44:47], a[28:31], v[16:19]\n" \
"v_mfma_f32_16x16x32_bf16 v[20:23], a[48:51], a[24:27], v[20:23]\n" \
"ds_read_b64_tr_b16 a[222:223], v222 offset:33792\n" \
"v_mfma_f32_16x16x32_bf16 v[20:23], a[52:55], a[28:31], v[20:23]\n" \
"v_mfma_f32_16x16x32_bf16 v[28:31], a[48:51], a[192:195], v[28:31]\n" \
"v_mfma_f32_16x16x32_bf16 v[28:31], a[52:55], a[196:199], v[28:31]\n" \
"v_mfma_f32_16x16x32_bf16 v[24:27], a[40:43], a[192:195], v[24:27]\n" \
"v_mfma_f32_16x16x32_bf16 v[24:27], a[44:47], a[196:199], v[24:27]\n" \
"v_mfma_f32_16x16x32_bf16 v[64:67], a[32:35], a[192:195], v[64:67]\n" \
"v_mfma_f32_16x16x32_bf16 v[64:67], a[36:39], a[196:199], v[64:67]\n" \
"s_waitcnt vmcnt(10) lgkmcnt(0)\n" \
"s_barrier\n" \
"v_mfma_f32_16x16x32_bf16 v[68:71], a[200:203], a[192:195], v[68:71]\n" \
"ds_read_b64_tr_b16 a[32:33], v209 offset:25600\n" \
"ds_read_b64_tr_b16 a[34:35], v209 offset:28672\n" \
"v_mfma_f32_16x16x32_bf16 v[68:71], a[204:207], a[196:199], v[68:71]\n" \
"ds_read_b64_tr_b16 a[36:37], v209 offset:37888\n" \
"ds_read_b64_tr_b16 a[38:39], v209 offset:40960\n" \
"v_mfma_f32_16x16x32_bf16 v[60:63], a[200:203], a[24:27], v[60:63]\n" \
"s_add_u32 m0, s79, 0\n" \
"ds_read_b64_tr_b16 a[40:41], v209 offset:24576\n" \
"v_mfma_f32_16x16x32_bf16 v[60:63], a[204:207], a[28:31], v[60:63]\n" \
"buffer_load_dwordx4 v192, s[44:47], s73 offen offset:0 lds\n" \
"v_mfma_f32_16x16x32_bf16 v[52:55], a[200:203], a[16:19], v[52:55]\n" \
"ds_read_b64_tr_b16 a[42:43], v209 offset:27648\n" \
"v_mfma_f32_16x16x32_bf16 v[52:55], a[204:207], a[20:23], v[52:55]\n" \
"ds_read_b64_tr_b16 a[44:45], v209 offset:36864\n" \
"Lpg9057_%=:\n" \
"Lst1x2_%=:\n" \
"s_cbranch_execz Lpg10903_%=\n" \
"v_mfma_f32_16x16x32_bf16 v[44:47], a[200:203], a[8:11], v[44:47]\n" \
"ds_read_b64_tr_b16 a[46:47], v209 offset:39936\n" \
"v_mfma_f32_16x16x32_bf16 v[44:47], a[204:207], a[12:15], v[44:47]\n" \
"buffer_load_dwordx4 v193, s[44:47], s73 offen offset:1024 lds\n" \
"v_mfma_f32_16x16x32_bf16 v[72:75], a[0:3], a[208:211], v[72:75]\n" \
"ds_read_b64_tr_b16 a[48:49], v210 offset:24576\n" \
"v_mfma_f32_16x16x32_bf16 v[72:75], a[4:7], a[212:215], v[72:75]\n" \
"ds_read_b128 a[0:3], v202 offset:20480\n" \
"v_mfma_f32_16x16x32_bf16 v[80:83], a[8:11], a[208:211], v[80:83]\n" \
"ds_read_b128 a[4:7], v203 offset:20480\n" \
"v_mfma_f32_16x16x32_bf16 v[80:83], a[12:15], a[212:215], v[80:83]\n" \
"buffer_load_dwordx4 v192, s[44:47], s73 offen offset:2048 lds\n" \
"v_mfma_f32_16x16x32_bf16 v[84:87], a[8:11], a[216:219], v[84:87]\n" \
"ds_read_b64_tr_b16 a[50:51], v210 offset:27648\n" \
"v_mfma_f32_16x16x32_bf16 v[84:87], a[12:15], a[220:223], v[84:87]\n" \
"ds_read_b128 a[8:11], v200 offset:40960\n" \
"v_mfma_f32_16x16x32_bf16 v[92:95], a[16:19], a[216:219], v[92:95]\n" \
"ds_read_b128 a[12:15], v201 offset:40960\n" \
"v_mfma_f32_16x16x32_bf16 v[92:95], a[20:23], a[220:223], v[92:95]\n" \
"buffer_load_dwordx4 v193, s[44:47], s73 offen offset:3072 lds\n" \
"v_mfma_f32_16x16x32_bf16 v[88:91], a[16:19], a[208:211], v[88:91]\n" \
"s_xor_b32 s73, s73, 0x80\n" \
"s_mul_i32 s76, s80, 393216\n" \
"ds_read_b64_tr_b16 a[52:53], v210 offset:36864\n" \
"v_mfma_f32_16x16x32_bf16 v[88:91], a[20:23], a[212:215], v[88:91]\n" \
"s_add_u32 s76, s76, s78\n" \
"ds_read_b128 a[16:19], v200 offset:45056\n" \
"v_mfma_f32_16x16x32_bf16 v[96:99], a[24:27], a[208:211], v[96:99]\n" \
"s_add_u32 m0, s91, 16384\n" \
"ds_read_b128 a[20:23], v201 offset:45056\n" \
"v_mfma_f32_16x16x32_bf16 v[96:99], a[28:31], a[212:215], v[96:99]\n" \
"buffer_load_dwordx4 v197, s[48:51], s76 offen offset:0 lds\n" \
"v_mfma_f32_16x16x32_bf16 v[100:103], a[24:27], a[216:219], v[100:103]\n" \
"ds_read_b64_tr_b16 a[54:55], v210 offset:39936\n" \
"v_mfma_f32_16x16x32_bf16 v[100:103], a[28:31], a[220:223], v[100:103]\n" \
"ds_read_b128 a[24:27], v200 offset:49152\n" \
"v_mfma_f32_16x16x32_bf16 v[108:111], a[192:195], a[216:219], v[108:111]\n" \
"s_add_u32 s77, s76, 24576\n" \
"ds_read_b128 a[28:31], v201 offset:49152\n" \
"v_mfma_f32_16x16x32_bf16 v[108:111], a[196:199], a[220:223], v[108:111]\n" \
"buffer_load_dwordx4 v198, s[48:51], s76 offen offset:1024 lds\n" \
"v_mfma_f32_16x16x32_bf16 v[104:107], a[192:195], a[208:211], v[104:107]\n" \
"v_mfma_f32_16x16x32_bf16 v[104:107], a[196:199], a[212:215], v[104:107]\n" \
"s_waitcnt lgkmcnt(2)\n" \
"v_mfma_f32_16x16x32_bf16 v[32:35], a[32:35], a[0:3], v[32:35]\n" \
"s_xor_b32 s80, s80, 0x1\n" \
"ds_read_b128 a[192:195], v200 offset:53248\n" \
"v_mfma_f32_16x16x32_bf16 v[32:35], a[36:39], a[4:7], v[32:35]\n" \
"buffer_load_dwordx4 v199, s[48:51], s76 offen offset:2048 lds\n" \
"v_mfma_f32_16x16x32_bf16 v[40:43], a[32:35], a[8:11], v[40:43]\n" \
"ds_read_b128 a[196:199], v201 offset:53248\n" \
"v_mfma_f32_16x16x32_bf16 v[40:43], a[36:39], a[12:15], v[40:43]\n" \
"ds_read_b64_tr_b16 a[200:201], v210 offset:25600\n" \
"v_mfma_f32_16x16x32_bf16 v[0:3], a[40:43], a[8:11], v[0:3]\n" \
"s_add_u32 m0, m0, 3072\n" \
"ds_read_b64_tr_b16 a[202:203], v210 offset:28672\n" \
"v_mfma_f32_16x16x32_bf16 v[0:3], a[44:47], a[12:15], v[0:3]\n" \
"buffer_load_dwordx4 v197, s[48:51], s77 offen offset:0 lds\n" \
"v_mfma_f32_16x16x32_bf16 v[4:7], a[48:51], a[8:11], v[4:7]\n" \
"ds_read_b64_tr_b16 a[204:205], v210 offset:37888\n" \
"v_mfma_f32_16x16x32_bf16 v[4:7], a[52:55], a[12:15], v[4:7]\n" \
"ds_read_b64_tr_b16 a[206:207], v210 offset:40960\n" \
"v_mfma_f32_16x16x32_bf16 v[12:15], a[48:51], a[16:19], v[12:15]\n" \
"ds_read_b64_tr_b16 a[208:209], v209 offset:26624\n" \
"v_mfma_f32_16x16x32_bf16 v[12:15], a[52:55], a[20:23], v[12:15]\n" \
"buffer_load_dwordx4 v198, s[48:51], s77 offen offset:1024 lds\n" \
"v_mfma_f32_16x16x32_bf16 v[8:11], a[40:43], a[16:19], v[8:11]\n" \
"ds_read_b64_tr_b16 a[210:211], v209 offset:29696\n" \
"v_mfma_f32_16x16x32_bf16 v[8:11], a[44:47], a[20:23], v[8:11]\n" \
"ds_read_b64_tr_b16 a[212:213], v209 offset:38912\n" \
"v_mfma_f32_16x16x32_bf16 v[48:51], a[32:35], a[16:19], v[48:51]\n" \
"s_waitcnt lgkmcnt(3)\n" \
"ds_read_b64_tr_b16 a[214:215], v209 offset:41984\n" \
"v_mfma_f32_16x16x32_bf16 v[48:51], a[36:39], a[20:23], v[48:51]\n" \
"buffer_load_dwordx4 v199, s[48:51], s77 offen offset:2048 lds\n" \
"v_mfma_f32_16x16x32_bf16 v[56:59], a[32:35], a[24:27], v[56:59]\n" \
"ds_read_b64_tr_b16 a[216:217], v210 offset:26624\n" \
"v_mfma_f32_16x16x32_bf16 v[56:59], a[36:39], a[28:31], v[56:59]\n" \
"ds_read_b64_tr_b16 a[218:219], v210 offset:29696\n" \
"v_mfma_f32_16x16x32_bf16 v[16:19], a[40:43], a[24:27], v[16:19]\n" \
"ds_read_b64_tr_b16 a[220:221], v210 offset:38912\n" \
"v_mfma_f32_16x16x32_bf16 v[16:19], a[44:47], a[28:31], v[16:19]\n" \
"v_mfma_f32_16x16x32_bf16 v[20:23], a[48:51], a[24:27], v[20:23]\n" \
"ds_read_b64_tr_b16 a[222:223], v210 offset:41984\n" \
"v_mfma_f32_16x16x32_bf16 v[20:23], a[52:55], a[28:31], v[20:23]\n" \
"v_mfma_f32_16x16x32_bf16 v[28:31], a[48:51], a[192:195], v[28:31]\n" \
"v_mfma_f32_16x16x32_bf16 v[28:31], a[52:55], a[196:199], v[28:31]\n" \
"buffer_load_dword v220, v221, s[56:59], s66 offen sc1\n" \
"v_mfma_f32_16x16x32_bf16 v[24:27], a[40:43], a[192:195], v[24:27]\n" \
"v_mfma_f32_16x16x32_bf16 v[24:27], a[44:47], a[196:199], v[24:27]\n" \
"v_mfma_f32_16x16x32_bf16 v[64:67], a[32:35], a[192:195], v[64:67]\n" \
"v_mfma_f32_16x16x32_bf16 v[64:67], a[36:39], a[196:199], v[64:67]\n" \
"s_waitcnt vmcnt(11) lgkmcnt(0)\n" \
"s_barrier\n" \
"v_mfma_f32_16x16x32_bf16 v[68:71], a[200:203], a[192:195], v[68:71]\n" \
"ds_read_b64_tr_b16 a[32:33], v209 offset:1024\n" \
"ds_read_b64_tr_b16 a[34:35], v209 offset:4096\n" \
"v_mfma_f32_16x16x32_bf16 v[68:71], a[204:207], a[196:199], v[68:71]\n" \
"ds_read_b64_tr_b16 a[36:37], v209 offset:13312\n" \
"ds_read_b64_tr_b16 a[38:39], v209 offset:16384\n" \
"v_mfma_f32_16x16x32_bf16 v[60:63], a[200:203], a[24:27], v[60:63]\n" \
"s_add_u32 m0, s79, 131072\n" \
"ds_read_b64_tr_b16 a[40:41], v209 offset:0\n" \
"v_mfma_f32_16x16x32_bf16 v[60:63], a[204:207], a[28:31], v[60:63]\n" \
"buffer_load_dwordx4 v192, s[44:47], s73 offen offset:0 lds\n" \
"v_mfma_f32_16x16x32_bf16 v[52:55], a[200:203], a[16:19], v[52:55]\n" \
"ds_read_b64_tr_b16 a[42:43], v209 offset:3072\n" \
"v_mfma_f32_16x16x32_bf16 v[52:55], a[204:207], a[20:23], v[52:55]\n" \
"ds_read_b64_tr_b16 a[44:45], v209 offset:12288\n" \
"v_mfma_f32_16x16x32_bf16 v[44:47], a[200:203], a[8:11], v[44:47]\n" \
"ds_read_b64_tr_b16 a[46:47], v209 offset:15360\n" \
"v_mfma_f32_16x16x32_bf16 v[44:47], a[204:207], a[12:15], v[44:47]\n" \
"buffer_load_dwordx4 v193, s[44:47], s73 offen offset:1024 lds\n" \
"v_mfma_f32_16x16x32_bf16 v[72:75], a[0:3], a[208:211], v[72:75]\n" \
"ds_read_b64_tr_b16 a[48:49], v210 offset:0\n" \
"v_mfma_f32_16x16x32_bf16 v[72:75], a[4:7], a[212:215], v[72:75]\n" \
"ds_read_b128 a[0:3], v202 offset:21504\n" \
"v_mfma_f32_16x16x32_bf16 v[80:83], a[8:11], a[208:211], v[80:83]\n" \
"ds_read_b128 a[4:7], v203 offset:21504\n" \
"v_mfma_f32_16x16x32_bf16 v[80:83], a[12:15], a[212:215], v[80:83]\n" \
"buffer_load_dwordx4 v192, s[44:47], s73 offen offset:2048 lds\n" \
"v_mfma_f32_16x16x32_bf16 v[84:87], a[8:11], a[216:219], v[84:87]\n" \
"ds_read_b64_tr_b16 a[50:51], v210 offset:3072\n" \
"v_mfma_f32_16x16x32_bf16 v[84:87], a[12:15], a[220:223], v[84:87]\n" \
"ds_read_b128 a[8:11], v205 offset:0\n" \
"v_mfma_f32_16x16x32_bf16 v[92:95], a[16:19], a[216:219], v[92:95]\n" \
"ds_read_b128 a[12:15], v206 offset:0\n" \
"v_mfma_f32_16x16x32_bf16 v[92:95], a[20:23], a[220:223], v[92:95]\n" \
"buffer_load_dwordx4 v193, s[44:47], s73 offen offset:3072 lds\n" \
"v_mfma_f32_16x16x32_bf16 v[88:91], a[16:19], a[208:211], v[88:91]\n" \
"s_xor_b32 s73, s73, 0x80\n" \
"s_mul_i32 s76, s80, 393216\n" \
"ds_read_b64_tr_b16 a[52:53], v210 offset:12288\n" \
"v_mfma_f32_16x16x32_bf16 v[88:91], a[20:23], a[212:215], v[88:91]\n" \
"s_add_u32 s76, s76, s78\n" \
"ds_read_b128 a[16:19], v205 offset:4096\n" \
"v_mfma_f32_16x16x32_bf16 v[96:99], a[24:27], a[208:211], v[96:99]\n" \
"s_add_u32 m0, s91, 106496\n" \
"ds_read_b128 a[20:23], v206 offset:4096\n" \
"v_mfma_f32_16x16x32_bf16 v[96:99], a[28:31], a[212:215], v[96:99]\n" \
"buffer_load_dwordx4 v197, s[48:51], s76 offen offset:0 lds\n" \
"v_mfma_f32_16x16x32_bf16 v[100:103], a[24:27], a[216:219], v[100:103]\n" \
"ds_read_b64_tr_b16 a[54:55], v210 offset:15360\n" \
"v_mfma_f32_16x16x32_bf16 v[100:103], a[28:31], a[220:223], v[100:103]\n" \
"ds_read_b128 a[24:27], v205 offset:8192\n" \
"v_mfma_f32_16x16x32_bf16 v[108:111], a[192:195], a[216:219], v[108:111]\n" \
"s_add_u32 s77, s76, 24576\n" \
"ds_read_b128 a[28:31], v206 offset:8192\n" \
"v_mfma_f32_16x16x32_bf16 v[108:111], a[196:199], a[220:223], v[108:111]\n" \
"buffer_load_dwordx4 v198, s[48:51], s76 offen offset:1024 lds\n" \
"v_mfma_f32_16x16x32_bf16 v[104:107], a[192:195], a[208:211], v[104:107]\n" \
"v_mfma_f32_16x16x32_bf16 v[104:107], a[196:199], a[212:215], v[104:107]\n" \
"s_waitcnt lgkmcnt(2)\n" \
"v_mfma_f32_16x16x32_bf16 v[32:35], a[32:35], a[0:3], v[32:35]\n" \
"s_xor_b32 s80, s80, 0x1\n" \
"ds_read_b128 a[192:195], v205 offset:12288\n" \
"v_mfma_f32_16x16x32_bf16 v[32:35], a[36:39], a[4:7], v[32:35]\n" \
"buffer_load_dwordx4 v199, s[48:51], s76 offen offset:2048 lds\n" \
"v_mfma_f32_16x16x32_bf16 v[40:43], a[32:35], a[8:11], v[40:43]\n" \
"ds_read_b128 a[196:199], v206 offset:12288\n" \
"v_mfma_f32_16x16x32_bf16 v[40:43], a[36:39], a[12:15], v[40:43]\n" \
"ds_read_b64_tr_b16 a[200:201], v210 offset:1024\n" \
"v_mfma_f32_16x16x32_bf16 v[0:3], a[40:43], a[8:11], v[0:3]\n" \
"s_add_u32 m0, m0, 3072\n" \
"ds_read_b64_tr_b16 a[202:203], v210 offset:4096\n" \
"v_mfma_f32_16x16x32_bf16 v[0:3], a[44:47], a[12:15], v[0:3]\n" \
"buffer_load_dwordx4 v197, s[48:51], s77 offen offset:0 lds\n" \
"v_mfma_f32_16x16x32_bf16 v[4:7], a[48:51], a[8:11], v[4:7]\n" \
"ds_read_b64_tr_b16 a[204:205], v210 offset:13312\n" \
"v_mfma_f32_16x16x32_bf16 v[4:7], a[52:55], a[12:15], v[4:7]\n" \
"ds_read_b64_tr_b16 a[206:207], v210 offset:16384\n" \
"v_mfma_f32_16x16x32_bf16 v[12:15], a[48:51], a[16:19], v[12:15]\n" \
"ds_read_b64_tr_b16 a[208:209], v209 offset:2048\n" \
"v_mfma_f32_16x16x32_bf16 v[12:15], a[52:55], a[20:23], v[12:15]\n" \
"buffer_load_dwordx4 v198, s[48:51], s77 offen offset:1024 lds\n" \
"v_mfma_f32_16x16x32_bf16 v[8:11], a[40:43], a[16:19], v[8:11]\n" \
"ds_read_b64_tr_b16 a[210:211], v209 offset:5120\n" \
"v_mfma_f32_16x16x32_bf16 v[8:11], a[44:47], a[20:23], v[8:11]\n" \
"ds_read_b64_tr_b16 a[212:213], v209 offset:14336\n" \
"v_mfma_f32_16x16x32_bf16 v[48:51], a[32:35], a[16:19], v[48:51]\n" \
"s_waitcnt lgkmcnt(3)\n" \
"ds_read_b64_tr_b16 a[214:215], v209 offset:17408\n" \
"v_mfma_f32_16x16x32_bf16 v[48:51], a[36:39], a[20:23], v[48:51]\n" \
"buffer_load_dwordx4 v199, s[48:51], s77 offen offset:2048 lds\n" \
"v_mfma_f32_16x16x32_bf16 v[56:59], a[32:35], a[24:27], v[56:59]\n" \
"ds_read_b64_tr_b16 a[216:217], v210 offset:2048\n" \
"v_mfma_f32_16x16x32_bf16 v[56:59], a[36:39], a[28:31], v[56:59]\n" \
"ds_read_b64_tr_b16 a[218:219], v210 offset:5120\n" \
"v_mfma_f32_16x16x32_bf16 v[16:19], a[40:43], a[24:27], v[16:19]\n" \
"ds_read_b64_tr_b16 a[220:221], v210 offset:14336\n" \
"v_mfma_f32_16x16x32_bf16 v[16:19], a[44:47], a[28:31], v[16:19]\n" \
"v_mfma_f32_16x16x32_bf16 v[20:23], a[48:51], a[24:27], v[20:23]\n" \
"s_mov_b32 s46, s81\n" \
"s_mov_b32 s47, s82\n" \
"ds_read_b64_tr_b16 a[222:223], v210 offset:17408\n" \
"v_mfma_f32_16x16x32_bf16 v[20:23], a[52:55], a[28:31], v[20:23]\n" \
"s_mov_b32 s48, 0\n" \
"s_mov_b32 s49, s68\n" \
"s_sub_u32 s50, 64, s9\n" \
"v_mfma_f32_16x16x32_bf16 v[28:31], a[48:51], a[192:195], v[28:31]\n" \
"s_cmp_eq_u32 s90, 0\n" \
"s_cselect_b32 s50, s9, s50\n" \
"s_andn2_b32 s50, s50, 3\n" \
"v_mfma_f32_16x16x32_bf16 v[28:31], a[52:55], a[196:199], v[28:31]\n" \
"s_lshl_b32 s50, s50, 12\n" \
"s_waitcnt vmcnt(10)\n" \
"v_cmp_eq_u32 vcc, s60, v220\n" \
"v_mfma_f32_16x16x32_bf16 v[24:27], a[40:43], a[192:195], v[24:27]\n" \
"v_mfma_f32_16x16x32_bf16 v[24:27], a[44:47], a[196:199], v[24:27]\n" \
"s_andn2_b32 s70, s65, vcc_lo\n" \
"s_cbranch_scc1 Lpl5_%=\n" \
"Lpr5_%=:\n" \
"v_mfma_f32_16x16x32_bf16 v[64:67], a[32:35], a[192:195], v[64:67]\n" \
"v_mfma_f32_16x16x32_bf16 v[64:67], a[36:39], a[196:199], v[64:67]\n" \
"s_xor_b32 s82, s48, s49\n" \
"s_cmp_lt_u32 s48, s50\n" \
"s_cselect_b32 s82, s82, s48\n" \
"s_waitcnt vmcnt(11) lgkmcnt(0)\n" \
"s_barrier\n" \
"v_mfma_f32_16x16x32_bf16 v[68:71], a[200:203], a[192:195], v[68:71]\n" \
"ds_read_b64_tr_b16 a[32:33], v204 offset:17408\n" \
"ds_read_b64_tr_b16 a[34:35], v204 offset:20480\n" \
"v_mfma_f32_16x16x32_bf16 v[68:71], a[204:207], a[196:199], v[68:71]\n" \
"s_add_u32 s81, s82, s46\n" \
"s_lshr_b32 s78, s61, 1\n" \
"s_lshl_b32 s80, s78, 9\n" \
"s_add_u32 s80, s80, 511\n" \
"ds_read_b64_tr_b16 a[36:37], v204 offset:29696\n" \
"ds_read_b64_tr_b16 a[38:39], v204 offset:32768\n" \
"v_mfma_f32_16x16x32_bf16 v[60:63], a[200:203], a[24:27], v[60:63]\n" \
"s_add_u32 m0, s86, s79\n" \
"ds_read_b64_tr_b16 a[40:41], v204 offset:16384\n" \
"v_sub_u32 v145, s80, v234\n" \
"v_mfma_f32_16x16x32_bf16 v[60:63], a[204:207], a[28:31], v[60:63]\n" \
"buffer_load_dwordx4 v234, s[56:59], s81 offen offset:0 sc1 lds\n" \
"v_mfma_f32_16x16x32_bf16 v[52:55], a[200:203], a[16:19], v[52:55]\n" \
"s_lshl_b32 s80, s78, 3\n" \
"s_not_b32 s80, s80\n" \
"ds_read_b64_tr_b16 a[42:43], v204 offset:19456\n" \
"v_mfma_f32_16x16x32_bf16 v[52:55], a[204:207], a[20:23], v[52:55]\n" \
"s_lshl_b32 s82, s82, 5\n" \
"ds_read_b64_tr_b16 a[44:45], v204 offset:28672\n" \
"v_sub_u32 v146, s80, v238\n" \
"v_mfma_f32_16x16x32_bf16 v[44:47], a[200:203], a[8:11], v[44:47]\n" \
"s_add_u32 s82, s82, s47\n" \
"ds_read_b64_tr_b16 a[46:47], v204 offset:31744\n" \
"v_subrev_u32 v152, 0, v238\n" \
"v_mfma_f32_16x16x32_bf16 v[44:47], a[204:207], a[12:15], v[44:47]\n" \
"buffer_load_dwordx4 v234, s[56:59], s81 offen offset:1024 sc1 lds\n" \
"v_mfma_f32_16x16x32_bf16 v[72:75], a[0:3], a[208:211], v[72:75]\n" \
"ds_read_b64_tr_b16 a[48:49], v222 offset:16384\n" \
"v_sub_u32 v144, 7, v238\n" \
"v_mfma_f32_16x16x32_bf16 v[72:75], a[4:7], a[212:215], v[72:75]\n" \
"ds_read_b128 a[0:3], v202 offset:22528\n" \
"v_or_b32 v152, v152, v144\n" \
"v_mfma_f32_16x16x32_bf16 v[80:83], a[8:11], a[208:211], v[80:83]\n" \
"ds_read_b128 a[4:7], v203 offset:22528\n" \
"v_ashrrev_i32 v152, 31, v152\n" \
"v_mfma_f32_16x16x32_bf16 v[80:83], a[12:15], a[212:215], v[80:83]\n" \
"buffer_load_dwordx4 v234, s[56:59], s81 offen offset:2048 sc1 lds\n" \
"v_mfma_f32_16x16x32_bf16 v[84:87], a[8:11], a[216:219], v[84:87]\n" \
"ds_read_b64_tr_b16 a[50:51], v222 offset:19456\n" \
"v_and_b32 v152, 0xff800000, v152\n" \
"v_mfma_f32_16x16x32_bf16 v[84:87], a[12:15], a[220:223], v[84:87]\n" \
"ds_read_b128 a[8:11], v200 offset:0\n" \
"v_sub_u32 v156, -9, v238\n" \
"v_mfma_f32_16x16x32_bf16 v[92:95], a[16:19], a[216:219], v[92:95]\n" \
"ds_read_b128 a[12:15], v201 offset:0\n" \
"v_ashrrev_i32 v156, 31, v156\n" \
"v_mfma_f32_16x16x32_bf16 v[92:95], a[20:23], a[220:223], v[92:95]\n" \
"buffer_load_dwordx4 v234, s[56:59], s81 offen offset:3072 sc1 lds\n" \
"v_mfma_f32_16x16x32_bf16 v[88:91], a[16:19], a[208:211], v[88:91]\n" \
"ds_read_b64_tr_b16 a[52:53], v222 offset:28672\n" \
"v_and_b32 v156, 0xff800000, v156\n" \
"v_mfma_f32_16x16x32_bf16 v[88:91], a[20:23], a[212:215], v[88:91]\n" \
"ds_read_b128 a[16:19], v200 offset:4096\n" \
"v_add_u32 v148, 0, v146\n" \
"v_mfma_f32_16x16x32_bf16 v[96:99], a[24:27], a[208:211], v[96:99]\n" \
"s_add_u32 m0, m0, 0x4000\n" \
"ds_read_b128 a[20:23], v201 offset:4096\n" \
"v_or3_b32 v148, v148, v145, s92\n" \
"v_mfma_f32_16x16x32_bf16 v[96:99], a[28:31], a[212:215], v[96:99]\n" \
"buffer_load_dwordx4 v216, s[36:39], s82 offen offset:0 lds\n" \
"v_mfma_f32_16x16x32_bf16 v[100:103], a[24:27], a[216:219], v[100:103]\n" \
"ds_read_b64_tr_b16 a[54:55], v222 offset:31744\n" \
"v_ashrrev_i32 v148, 31, v148\n" \
"v_mfma_f32_16x16x32_bf16 v[100:103], a[28:31], a[220:223], v[100:103]\n" \
"ds_read_b128 a[24:27], v200 offset:8192\n" \
"v_and_b32 v148, 0xff800000, v148\n" \
"v_mfma_f32_16x16x32_bf16 v[108:111], a[192:195], a[216:219], v[108:111]\n" \
"s_add_u32 s48, s48, 0x1000\n" \
"ds_read_b128 a[28:31], v201 offset:8192\n" \
"v_subrev_u32 v153, 1, v238\n" \
"v_mfma_f32_16x16x32_bf16 v[108:111], a[196:199], a[220:223], v[108:111]\n" \
"buffer_load_dwordx4 v217, s[36:39], s82 offen offset:1024 lds\n" \
"v_mfma_f32_16x16x32_bf16 v[104:107], a[192:195], a[208:211], v[104:107]\n" \
"v_sub_u32 v144, 8, v238\n" \
"v_or_b32 v153, v153, v144\n" \
"v_mfma_f32_16x16x32_bf16 v[104:107], a[196:199], a[212:215], v[104:107]\n" \
"s_waitcnt lgkmcnt(2)\n" \
"v_ashrrev_i32 v153, 31, v153\n" \
"v_and_b32 v153, 0xff800000, v153\n" \
"v_mfma_f32_16x16x32_bf16 v[32:35], a[32:35], a[0:3], v[32:35]\n" \
"s_sub_u32 s86, s86, 32768\n" \
"s_cselect_b32 s86, 131072, s86\n" \
"ds_read_b128 a[192:195], v200 offset:12288\n" \
"v_mfma_f32_16x16x32_bf16 v[32:35], a[36:39], a[4:7], v[32:35]\n" \
"buffer_load_dwordx4 v218, s[36:39], s82 offen offset:2048 lds\n" \
"v_mfma_f32_16x16x32_bf16 v[40:43], a[32:35], a[8:11], v[40:43]\n" \
"ds_read_b128 a[196:199], v201 offset:12288\n" \
"v_sub_u32 v157, -8, v238\n" \
"v_mfma_f32_16x16x32_bf16 v[40:43], a[36:39], a[12:15], v[40:43]\n" \
"ds_read_b64_tr_b16 a[200:201], v222 offset:17408\n" \
"v_ashrrev_i32 v157, 31, v157\n" \
"v_mfma_f32_16x16x32_bf16 v[0:3], a[40:43], a[8:11], v[0:3]\n" \
"ds_read_b64_tr_b16 a[202:203], v222 offset:20480\n" \
"v_and_b32 v157, 0xff800000, v157\n" \
"v_mfma_f32_16x16x32_bf16 v[0:3], a[44:47], a[12:15], v[0:3]\n" \
"buffer_load_dwordx4 v219, s[36:39], s82 offen offset:3072 lds\n" \
"v_mfma_f32_16x16x32_bf16 v[4:7], a[48:51], a[8:11], v[4:7]\n" \
"ds_read_b64_tr_b16 a[204:205], v222 offset:29696\n" \
"v_add_u32 v149, 1, v146\n" \
"v_mfma_f32_16x16x32_bf16 v[4:7], a[52:55], a[12:15], v[4:7]\n" \
"ds_read_b64_tr_b16 a[206:207], v222 offset:32768\n" \
"v_or3_b32 v149, v149, v145, s92\n" \
"v_mfma_f32_16x16x32_bf16 v[12:15], a[48:51], a[16:19], v[12:15]\n" \
"ds_read_b64_tr_b16 a[208:209], v204 offset:18432\n" \
"v_ashrrev_i32 v149, 31, v149\n" \
"v_mfma_f32_16x16x32_bf16 v[12:15], a[52:55], a[20:23], v[12:15]\n" \
"v_and_b32 v149, 0xff800000, v149\n" \
"v_subrev_u32 v154, 2, v238\n" \
"v_mfma_f32_16x16x32_bf16 v[8:11], a[40:43], a[16:19], v[8:11]\n" \
"ds_read_b64_tr_b16 a[210:211], v204 offset:21504\n" \
"v_sub_u32 v144, 9, v238\n" \
"v_mfma_f32_16x16x32_bf16 v[8:11], a[44:47], a[20:23], v[8:11]\n" \
"ds_read_b64_tr_b16 a[212:213], v204 offset:30720\n" \
"v_or_b32 v154, v154, v144\n" \
"v_mfma_f32_16x16x32_bf16 v[48:51], a[32:35], a[16:19], v[48:51]\n" \
"ds_read_b64_tr_b16 a[214:215], v204 offset:33792\n" \
"v_ashrrev_i32 v154, 31, v154\n" \
"v_mfma_f32_16x16x32_bf16 v[48:51], a[36:39], a[20:23], v[48:51]\n" \
"s_waitcnt lgkmcnt(3)\n" \
"v_and_b32 v154, 0xff800000, v154\n" \
"v_sub_u32 v158, -7, v238\n" \
"v_mfma_f32_16x16x32_bf16 v[56:59], a[32:35], a[24:27], v[56:59]\n" \
"ds_read_b64_tr_b16 a[216:217], v222 offset:18432\n" \
"v_ashrrev_i32 v158, 31, v158\n" \
"v_mfma_f32_16x16x32_bf16 v[56:59], a[36:39], a[28:31], v[56:59]\n" \
"ds_read_b64_tr_b16 a[218:219], v222 offset:21504\n" \
"v_and_b32 v158, 0xff800000, v158\n" \
"v_mfma_f32_16x16x32_bf16 v[16:19], a[40:43], a[24:27], v[16:19]\n" \
"ds_read_b64_tr_b16 a[220:221], v222 offset:30720\n" \
"v_add_u32 v150, 2, v146\n" \
"v_mfma_f32_16x16x32_bf16 v[16:19], a[44:47], a[28:31], v[16:19]\n" \
"v_or3_b32 v150, v150, v145, s92\n" \
"v_ashrrev_i32 v150, 31, v150\n" \
"v_mfma_f32_16x16x32_bf16 v[20:23], a[48:51], a[24:27], v[20:23]\n" \
"ds_read_b64_tr_b16 a[222:223], v222 offset:33792\n" \
"v_and_b32 v150, 0xff800000, v150\n" \
"v_mfma_f32_16x16x32_bf16 v[20:23], a[52:55], a[28:31], v[20:23]\n" \
"v_subrev_u32 v155, 3, v238\n" \
"v_sub_u32 v144, 10, v238\n" \
"v_mfma_f32_16x16x32_bf16 v[28:31], a[48:51], a[192:195], v[28:31]\n" \
"v_or_b32 v155, v155, v144\n" \
"v_ashrrev_i32 v155, 31, v155\n" \
"v_mfma_f32_16x16x32_bf16 v[28:31], a[52:55], a[196:199], v[28:31]\n" \
"v_and_b32 v155, 0xff800000, v155\n" \
"v_sub_u32 v159, -6, v238\n" \
"v_mfma_f32_16x16x32_bf16 v[24:27], a[40:43], a[192:195], v[24:27]\n" \
"v_ashrrev_i32 v159, 31, v159\n" \
"v_and_b32 v159, 0xff800000, v159\n" \
"v_mfma_f32_16x16x32_bf16 v[24:27], a[44:47], a[196:199], v[24:27]\n" \
"v_add_u32 v151, 3, v146\n" \
"v_or3_b32 v151, v151, v145, s92\n" \
"v_mfma_f32_16x16x32_bf16 v[64:67], a[32:35], a[192:195], v[64:67]\n" \
"v_ashrrev_i32 v151, 31, v151\n" \
"v_and_b32 v151, 0xff800000, v151\n" \
"v_mfma_f32_16x16x32_bf16 v[64:67], a[36:39], a[196:199], v[64:67]\n" \
"s_xor_b32 s82, s48, s49\n" \
"s_cmp_lt_u32 s48, s50\n" \
"s_cselect_b32 s82, s82, s48\n" \
"s_waitcnt vmcnt(8) lgkmcnt(0)\n" \
"s_barrier\n" \
"v_mfma_f32_16x16x32_bf16 v[68:71], a[200:203], a[192:195], v[68:71]\n" \
"ds_read_b64_tr_b16 a[32:33], v209 offset:25600\n" \
"ds_read_b64_tr_b16 a[34:35], v209 offset:28672\n" \
"v_mfma_f32_16x16x32_bf16 v[68:71], a[204:207], a[196:199], v[68:71]\n" \
"s_add_u32 s81, s82, s46\n" \
"ds_read_b64_tr_b16 a[36:37], v209 offset:37888\n" \
"ds_read_b64_tr_b16 a[38:39], v209 offset:40960\n" \
"v_mfma_f32_16x16x32_bf16 v[60:63], a[200:203], a[24:27], v[60:63]\n" \
"s_add_u32 m0, s86, s79\n" \
"ds_read_b64_tr_b16 a[40:41], v209 offset:24576\n" \
"v_mov_b32 v186, 0xffff0000\n" \
"v_mfma_f32_16x16x32_bf16 v[60:63], a[204:207], a[28:31], v[60:63]\n" \
"buffer_load_dwordx4 v234, s[56:59], s81 offen offset:0 sc1 lds\n" \
"v_mfma_f32_16x16x32_bf16 v[52:55], a[200:203], a[16:19], v[52:55]\n" \
"ds_read_b64_tr_b16 a[42:43], v209 offset:27648\n" \
"v_subrev_u32 v144, 0, v238\n" \
"v_mfma_f32_16x16x32_bf16 v[52:55], a[204:207], a[20:23], v[52:55]\n" \
"s_lshl_b32 s82, s82, 5\n" \
"ds_read_b64_tr_b16 a[44:45], v209 offset:36864\n" \
"v_subrev_u32 v184, 1, v238\n" \
"v_mfma_f32_16x16x32_bf16 v[44:47], a[200:203], a[8:11], v[44:47]\n" \
"s_add_u32 s82, s82, s47\n" \
"ds_read_b64_tr_b16 a[46:47], v209 offset:39936\n" \
"v_lshrrev_b32 v144, 16, v144\n" \
"v_mfma_f32_16x16x32_bf16 v[44:47], a[204:207], a[12:15], v[44:47]\n" \
"buffer_load_dwordx4 v234, s[56:59], s81 offen offset:1024 sc1 lds\n" \
"v_mfma_f32_16x16x32_bf16 v[72:75], a[0:3], a[208:211], v[72:75]\n" \
"ds_read_b64_tr_b16 a[48:49], v210 offset:24576\n" \
"v_and_or_b32 v184, v184, v186, v144\n" \
"v_mfma_f32_16x16x32_bf16 v[72:75], a[4:7], a[212:215], v[72:75]\n" \
"ds_read_b128 a[0:3], v202 offset:23552\n" \
"v_subrev_u32 v144, 2, v238\n" \
"v_mfma_f32_16x16x32_bf16 v[80:83], a[8:11], a[208:211], v[80:83]\n" \
"ds_read_b128 a[4:7], v203 offset:23552\n" \
"v_subrev_u32 v185, 3, v238\n" \
"v_mfma_f32_16x16x32_bf16 v[80:83], a[12:15], a[212:215], v[80:83]\n" \
"buffer_load_dwordx4 v234, s[56:59], s81 offen offset:2048 sc1 lds\n" \
"v_mfma_f32_16x16x32_bf16 v[84:87], a[8:11], a[216:219], v[84:87]\n" \
"ds_read_b64_tr_b16 a[50:51], v210 offset:27648\n" \
"v_lshrrev_b32 v144, 16, v144\n" \
"v_mfma_f32_16x16x32_bf16 v[84:87], a[12:15], a[220:223], v[84:87]\n" \
"ds_read_b128 a[8:11], v207 offset:0\n" \
"v_and_or_b32 v185, v185, v186, v144\n" \
"v_mfma_f32_16x16x32_bf16 v[92:95], a[16:19], a[216:219], v[92:95]\n" \
"ds_read_b128 a[12:15], v208 offset:0\n" \
"v_mov_b32 v144, 0x3e8293ee\n" \
"v_mfma_f32_16x16x32_bf16 v[92:95], a[20:23], a[220:223], v[92:95]\n" \
"buffer_load_dwordx4 v234, s[56:59], s81 offen offset:3072 sc1 lds\n" \
"v_mfma_f32_16x16x32_bf16 v[88:91], a[16:19], a[208:211], v[88:91]\n" \
"s_add_u32 s80, s79, 117760\n" \
"ds_read_b64_tr_b16 a[52:53], v210 offset:36864\n" \
"v_add_u32 v223, s80, v234\n" \
"v_mfma_f32_16x16x32_bf16 v[88:91], a[20:23], a[212:215], v[88:91]\n" \
"s_cmp_lg_u32 s78, 0\n" \
"s_cselect_b64 s[76:77], -1, 0\n" \
"ds_read_b128 a[16:19], v207 offset:4096\n" \
"v_mfma_f32_16x16x32_bf16 v[96:99], a[24:27], a[208:211], v[96:99]\n" \
"s_add_u32 m0, m0, 0x4000\n" \
"ds_read_b128 a[20:23], v208 offset:4096\n" \
"v_mfma_f32_16x16x32_bf16 v[96:99], a[28:31], a[212:215], v[96:99]\n" \
"buffer_load_dwordx4 v216, s[36:39], s82 offen offset:0 lds\n" \
"v_mfma_f32_16x16x32_bf16 v[100:103], a[24:27], a[216:219], v[100:103]\n" \
"ds_read_b64_tr_b16 a[54:55], v210 offset:39936\n" \
"v_mfma_f32_16x16x32_bf16 v[100:103], a[28:31], a[220:223], v[100:103]\n" \
"ds_read_b128 a[24:27], v207 offset:8192\n" \
"v_mfma_f32_16x16x32_bf16 v[108:111], a[192:195], a[216:219], v[108:111]\n" \
"s_add_u32 s48, s48, 0x1000\n" \
"ds_read_b128 a[28:31], v208 offset:8192\n" \
"v_mfma_f32_16x16x32_bf16 v[108:111], a[196:199], a[220:223], v[108:111]\n" \
"buffer_load_dwordx4 v217, s[36:39], s82 offen offset:1024 lds\n" \
"v_mfma_f32_16x16x32_bf16 v[104:107], a[192:195], a[208:211], v[104:107]\n" \
"v_mfma_f32_16x16x32_bf16 v[104:107], a[196:199], a[212:215], v[104:107]\n" \
"s_waitcnt lgkmcnt(2)\n" \
"v_mfma_f32_16x16x32_bf16 v[32:35], a[32:35], a[0:3], v[32:35]\n" \
"s_sub_u32 s86, s86, 32768\n" \
"s_cselect_b32 s86, 131072, s86\n" \
"ds_read_b128 a[192:195], v207 offset:12288\n" \
"v_mfma_f32_16x16x32_bf16 v[32:35], a[36:39], a[4:7], v[32:35]\n" \
"buffer_load_dwordx4 v218, s[36:39], s82 offen offset:2048 lds\n" \
"v_mfma_f32_16x16x32_bf16 v[40:43], a[32:35], a[8:11], v[40:43]\n" \
"ds_read_b128 a[196:199], v208 offset:12288\n" \
"v_mfma_f32_16x16x32_bf16 v[40:43], a[36:39], a[12:15], v[40:43]\n" \
"ds_read_b64_tr_b16 a[200:201], v210 offset:25600\n" \
"v_mfma_f32_16x16x32_bf16 v[0:3], a[40:43], a[8:11], v[0:3]\n" \
"ds_read_b64_tr_b16 a[202:203], v210 offset:28672\n" \
"v_mfma_f32_16x16x32_bf16 v[0:3], a[44:47], a[12:15], v[0:3]\n" \
"buffer_load_dwordx4 v219, s[36:39], s82 offen offset:3072 lds\n" \
"v_mfma_f32_16x16x32_bf16 v[4:7], a[48:51], a[8:11], v[4:7]\n" \
"s_xor_b32 s82, s48, s49\n" \
"s_cmp_lt_u32 s48, s50\n" \
"s_cselect_b32 s82, s82, s48\n" \
"ds_read_b64_tr_b16 a[204:205], v210 offset:37888\n" \
"v_mfma_f32_16x16x32_bf16 v[4:7], a[52:55], a[12:15], v[4:7]\n" \
"s_add_u32 s81, s82, s46\n" \
"ds_read_b64_tr_b16 a[206:207], v210 offset:40960\n" \
"v_mfma_f32_16x16x32_bf16 v[12:15], a[48:51], a[16:19], v[12:15]\n" \
"s_add_u32 m0, s86, s79\n" \
"ds_read_b64_tr_b16 a[208:209], v209 offset:26624\n" \
"v_mfma_f32_16x16x32_bf16 v[12:15], a[52:55], a[20:23], v[12:15]\n" \
"buffer_load_dwordx4 v234, s[56:59], s81 offen offset:0 sc1 lds\n" \
"v_mfma_f32_16x16x32_bf16 v[8:11], a[40:43], a[16:19], v[8:11]\n" \
"ds_read_b64_tr_b16 a[210:211], v209 offset:29696\n" \
"v_mfma_f32_16x16x32_bf16 v[8:11], a[44:47], a[20:23], v[8:11]\n" \
"s_lshl_b32 s82, s82, 5\n" \
"ds_read_b64_tr_b16 a[212:213], v209 offset:38912\n" \
"v_mfma_f32_16x16x32_bf16 v[48:51], a[32:35], a[16:19], v[48:51]\n" \
"s_waitcnt lgkmcnt(3)\n" \
"s_add_u32 s82, s82, s47\n" \
"ds_read_b64_tr_b16 a[214:215], v209 offset:41984\n" \
"v_mfma_f32_16x16x32_bf16 v[48:51], a[36:39], a[20:23], v[48:51]\n" \
"buffer_load_dwordx4 v234, s[56:59], s81 offen offset:1024 sc1 lds\n" \
"v_mfma_f32_16x16x32_bf16 v[56:59], a[32:35], a[24:27], v[56:59]\n" \
"ds_read_b64_tr_b16 a[216:217], v210 offset:26624\n" \
"v_mfma_f32_16x16x32_bf16 v[56:59], a[36:39], a[28:31], v[56:59]\n" \
"ds_read_b64_tr_b16 a[218:219], v210 offset:29696\n" \
"v_mfma_f32_16x16x32_bf16 v[16:19], a[40:43], a[24:27], v[16:19]\n" \
"ds_read_b64_tr_b16 a[220:221], v210 offset:38912\n" \
"v_mfma_f32_16x16x32_bf16 v[16:19], a[44:47], a[28:31], v[16:19]\n" \
"buffer_load_dwordx4 v234, s[56:59], s81 offen offset:2048 sc1 lds\n" \
"v_mfma_f32_16x16x32_bf16 v[20:23], a[48:51], a[24:27], v[20:23]\n" \
"ds_read_b64_tr_b16 a[222:223], v210 offset:41984\n" \
"v_mfma_f32_16x16x32_bf16 v[20:23], a[52:55], a[28:31], v[20:23]\n" \
"v_mfma_f32_16x16x32_bf16 v[28:31], a[48:51], a[192:195], v[28:31]\n" \
"v_mfma_f32_16x16x32_bf16 v[28:31], a[52:55], a[196:199], v[28:31]\n" \
"buffer_load_dwordx4 v234, s[56:59], s81 offen offset:3072 sc1 lds\n" \
"v_mfma_f32_16x16x32_bf16 v[24:27], a[40:43], a[192:195], v[24:27]\n" \
"v_mfma_f32_16x16x32_bf16 v[24:27], a[44:47], a[196:199], v[24:27]\n" \
"v_mfma_f32_16x16x32_bf16 v[64:67], a[32:35], a[192:195], v[64:67]\n" \
"s_add_u32 m0, m0, 0x4000\n" \
"v_mfma_f32_16x16x32_bf16 v[64:67], a[36:39], a[196:199], v[64:67]\n" \
"buffer_load_dwordx4 v216, s[36:39], s82 offen offset:0 lds\n" \
"s_waitcnt vmcnt(13) lgkmcnt(0)\n" \
"s_barrier\n" \
"v_mfma_f32_16x16x32_bf16 v[44:47], a[200:203], a[8:11], v[44:47]\n" \
"ds_read_b64_tr_b16 a[96:97], v214 offset:16384\n" \
"v_mfma_f32_16x16x32_bf16 v[44:47], a[204:207], a[12:15], v[44:47]\n" \
"ds_read_b64_tr_b16 a[98:99], v214 offset:18432\n" \
"v_mfma_f32_16x16x32_bf16 v[52:55], a[200:203], a[16:19], v[52:55]\n" \
"s_add_u32 s48, s48, 0x1000\n" \
"ds_read_b64_tr_b16 a[100:101], v214 offset:24576\n" \
"v_mfma_f32_16x16x32_bf16 v[52:55], a[204:207], a[20:23], v[52:55]\n" \
"buffer_load_dwordx4 v217, s[36:39], s82 offen offset:1024 lds\n" \
"v_mfma_f32_16x16x32_bf16 v[60:63], a[200:203], a[24:27], v[60:63]\n" \
"ds_read_b64_tr_b16 a[102:103], v214 offset:26624\n" \
"v_cvt_pk_bf16_f32 v112, v0, v1\n" \
"v_mfma_f32_16x16x32_bf16 v[60:63], a[204:207], a[28:31], v[60:63]\n" \
"ds_read_b128 a[64:67], v213 offset:0\n" \
"v_cvt_pk_bf16_f32 v113, v2, v3\n" \
"v_mfma_f32_16x16x32_bf16 v[68:71], a[200:203], a[192:195], v[68:71]\n" \
"s_sub_u32 s86, s86, 32768\n" \
"s_cselect_b32 s86, 131072, s86\n" \
"ds_read_b128 a[68:71], v213 offset:2048\n" \
"v_mfma_f32_16x16x32_bf16 v[68:71], a[204:207], a[196:199], v[68:71]\n" \
"buffer_load_dwordx4 v218, s[36:39], s82 offen offset:2048 lds\n" \
"v_mfma_f32_16x16x32_bf16 v[72:75], a[0:3], a[208:211], v[72:75]\n" \
"ds_read_b64_tr_b16 a[104:105], v215 offset:16384\n" \
"v_cvt_pk_bf16_f32 v114, v4, v5\n" \
"v_mfma_f32_16x16x32_bf16 v[72:75], a[4:7], a[212:215], v[72:75]\n" \
"ds_read_b64_tr_b16 a[106:107], v215 offset:18432\n" \
"v_cvt_pk_bf16_f32 v115, v6, v7\n" \
"v_mfma_f32_16x16x32_bf16 v[80:83], a[8:11], a[208:211], v[80:83]\n" \
"ds_read_b64_tr_b16 a[108:109], v215 offset:24576\n" \
"v_cvt_pk_bf16_f32 v116, v8, v9\n" \
"v_mfma_f32_16x16x32_bf16 v[80:83], a[12:15], a[212:215], v[80:83]\n" \
"buffer_load_dwordx4 v219, s[36:39], s82 offen offset:3072 lds\n" \
"v_mfma_f32_16x16x32_bf16 v[84:87], a[8:11], a[216:219], v[84:87]\n" \
"s_xor_b32 s82, s48, s49\n" \
"s_cmp_lt_u32 s48, s50\n" \
"s_cselect_b32 s82, s82, s48\n" \
"ds_read_b64_tr_b16 a[110:111], v215 offset:26624\n" \
"v_mfma_f32_16x16x32_bf16 v[84:87], a[12:15], a[220:223], v[84:87]\n" \
"s_add_u32 s81, s82, s46\n" \
"ds_read_b128 a[72:75], v213 offset:256\n" \
"v_cvt_pk_bf16_f32 v117, v10, v11\n" \
"v_mfma_f32_16x16x32_bf16 v[92:95], a[16:19], a[216:219], v[92:95]\n" \
"s_add_u32 m0, s86, s79\n" \
"ds_read_b128 a[76:79], v213 offset:2304\n" \
"v_cvt_pk_bf16_f32 v118, v12, v13\n" \
"v_mfma_f32_16x16x32_bf16 v[92:95], a[20:23], a[220:223], v[92:95]\n" \
"buffer_load_dwordx4 v234, s[56:59], s81 offen offset:0 sc1 lds\n" \
"v_mfma_f32_16x16x32_bf16 v[88:91], a[16:19], a[208:211], v[88:91]\n" \
"ds_read_b128 a[80:83], v213 offset:4096\n" \
"Lpg9623_%=:\n" \
"Lst1x3_%=:\n" \
"s_cbranch_execz Lpg11464_%=\n" \
"v_cvt_pk_bf16_f32 v119, v14, v15\n" \
"v_mfma_f32_16x16x32_bf16 v[88:91], a[20:23], a[212:215], v[88:91]\n" \
"s_lshl_b32 s82, s82, 5\n" \
"ds_read_b128 a[84:87], v213 offset:6144\n" \
"v_cvt_pk_bf16_f32 v120, v16, v17\n" \
"v_mfma_f32_16x16x32_bf16 v[96:99], a[24:27], a[208:211], v[96:99]\n" \
"s_add_u32 s82, s82, s47\n" \
"v_cvt_pk_bf16_f32 v121, v18, v19\n" \
"v_cvt_pk_bf16_f32 v122, v20, v21\n" \
"v_mfma_f32_16x16x32_bf16 v[96:99], a[28:31], a[212:215], v[96:99]\n" \
"buffer_load_dwordx4 v234, s[56:59], s81 offen offset:1024 sc1 lds\n" \
"v_mfma_f32_16x16x32_bf16 v[100:103], a[24:27], a[216:219], v[100:103]\n" \
"v_cvt_pk_bf16_f32 v123, v22, v23\n" \
"v_cvt_pk_bf16_f32 v124, v24, v25\n" \
"v_mfma_f32_16x16x32_bf16 v[100:103], a[28:31], a[220:223], v[100:103]\n" \
"v_cvt_pk_bf16_f32 v125, v26, v27\n" \
"v_cvt_pk_bf16_f32 v126, v28, v29\n" \
"v_mfma_f32_16x16x32_bf16 v[108:111], a[192:195], a[216:219], v[108:111]\n" \
"v_cvt_pk_bf16_f32 v127, v30, v31\n" \
"v_cvt_pk_bf16_f32 v128, v32, v33\n" \
"v_mfma_f32_16x16x32_bf16 v[108:111], a[196:199], a[220:223], v[108:111]\n" \
"buffer_load_dwordx4 v234, s[56:59], s81 offen offset:2048 sc1 lds\n" \
"v_mfma_f32_16x16x32_bf16 v[104:107], a[192:195], a[208:211], v[104:107]\n" \
"v_cvt_pk_bf16_f32 v129, v34, v35\n" \
"v_xor_b32 v145, 0x2000, v223\n" \
"v_mfma_f32_16x16x32_bf16 v[104:107], a[196:199], a[212:215], v[104:107]\n" \
"s_waitcnt lgkmcnt(0)\n" \
"v_cvt_pk_bf16_f32 v192, v72, v73\n" \
"v_cvt_pk_bf16_f32 v193, v74, v75\n" \
"ds_write_b64 v145, v[128:129]\n" \
"s_nop 0\n" \
"ds_write_b64 v145, v[192:193] offset:8\n" \
"v_cvt_pk_bf16_f32 v132, v40, v41\n" \
"v_cvt_pk_bf16_f32 v133, v42, v43\n" \
"v_cvt_pk_bf16_f32 v134, v44, v45\n" \
"v_cvt_pk_bf16_f32 v135, v46, v47\n" \
"v_cvt_pk_bf16_f32 v136, v48, v49\n" \
"v_cvt_pk_bf16_f32 v137, v50, v51\n" \
"v_cvt_pk_bf16_f32 v138, v52, v53\n" \
"v_cvt_pk_bf16_f32 v139, v54, v55\n" \
"v_cvt_pk_bf16_f32 v140, v56, v57\n" \
"v_cvt_pk_bf16_f32 v141, v58, v59\n" \
"v_cvt_pk_bf16_f32 v142, v60, v61\n" \
"v_cvt_pk_bf16_f32 v143, v62, v63\n" \
"v_mfma_f32_16x16x32_bf16 v[0:3], a[96:99], a[64:67], 0\n" \
"ds_read_b128 a[88:91], v213 offset:4352\n" \
"v_mfma_f32_16x16x32_bf16 v[0:3], a[100:103], a[68:71], v[0:3]\n" \
"ds_read_b128 a[92:95], v213 offset:6400\n" \
"v_mfma_f32_16x16x32_bf16 v[4:7], a[104:107], a[64:67], 0\n" \
"ds_read_b64_tr_b16 a[112:113], v214 offset:16896\n" \
"v_mfma_f32_16x16x32_bf16 v[4:7], a[108:111], a[68:71], v[4:7]\n" \
"ds_read_b64_tr_b16 a[114:115], v214 offset:18944\n" \
"v_mfma_f32_16x16x32_bf16 v[20:23], a[104:107], a[72:75], 0\n" \
"ds_read_b64_tr_b16 a[116:117], v214 offset:25088\n" \
"v_cvt_pk_bf16_f32 v64, v64, v65\n" \
"v_mfma_f32_16x16x32_bf16 v[20:23], a[108:111], a[76:79], v[20:23]\n" \
"buffer_load_dwordx4 v234, s[56:59], s81 offen offset:3072 sc1 lds\n" \
"v_cvt_pk_bf16_f32 v65, v66, v67\n" \
"v_cvt_pk_bf16_f32 v66, v68, v69\n" \
"v_mfma_f32_16x16x32_bf16 v[16:19], a[96:99], a[72:75], 0\n" \
"s_sub_u32 s85, s85, 32768\n" \
"s_cselect_b32 s85, 131072, s85\n" \
"ds_read_b64_tr_b16 a[118:119], v214 offset:27136\n" \
"v_mfma_f32_16x16x32_bf16 v[16:19], a[100:103], a[76:79], v[16:19]\n" \
"s_waitcnt lgkmcnt(3)\n" \
"ds_read_b64_tr_b16 a[120:121], v215 offset:16896\n" \
"v_cvt_pk_bf16_f32 v67, v70, v71\n" \
"v_cvt_pk_bf16_f32 v194, v80, v81\n" \
"v_cvt_pk_bf16_f32 v195, v82, v83\n" \
"v_cvt_pk_bf16_f32 v74, v84, v85\n" \
"v_mfma_f32_16x16x32_bf16 v[32:35], a[96:99], a[80:83], 0\n" \
"s_add_u32 m0, m0, 0x4000\n" \
"ds_read_b64_tr_b16 a[122:123], v215 offset:18944\n" \
"v_cvt_pk_bf16_f32 v75, v86, v87\n" \
"v_mfma_f32_16x16x32_bf16 v[32:35], a[100:103], a[84:87], v[32:35]\n" \
"buffer_load_dwordx4 v216, s[36:39], s82 offen offset:0 lds\n" \
"v_cvt_pk_bf16_f32 v196, v88, v89\n" \
"v_cvt_pk_bf16_f32 v197, v90, v91\n" \
"v_mfma_f32_16x16x32_bf16 v[176:179], v[64:67], v[124:127], v[152:155]\n" \
"v_mfma_f32_16x16x32_bf16 v[172:175], v[140:143], v[124:127], v[156:159]\n" \
"v_mfma_f32_16x16x32_bf16 v[168:171], v[140:143], v[120:123], v[152:155]\n" \
"v_mfma_f32_16x16x32_bf16 v[164:167], v[136:139], v[120:123], v[156:159]\n" \
"v_mfma_f32_16x16x32_bf16 v[160:163], v[136:139], v[116:119], v[152:155]\n" \
"v_mfma_f32_16x16x32_bf16 v[156:159], v[132:135], v[116:119], v[156:159]\n" \
"v_mfma_f32_16x16x32_bf16 v[152:155], v[132:135], v[112:115], v[152:155]\n" \
"v_mfma_f32_16x16x32_bf16 v[36:39], a[104:107], a[80:83], 0\n" \
"s_add_u32 s48, s48, 0x1000\n" \
"ds_read_b64_tr_b16 a[124:125], v215 offset:25088\n" \
"v_add_u32 v213, s85, v211\n" \
"v_mfma_f32_16x16x32_bf16 v[36:39], a[108:111], a[84:87], v[36:39]\n" \
"buffer_load_dwordx4 v217, s[36:39], s82 offen offset:1024 lds\n" \
"v_mfma_f32_16x16x32_bf16 v[52:55], a[104:107], a[88:91], 0\n" \
"s_sub_u32 s86, s86, 32768\n" \
"s_cselect_b32 s86, 131072, s86\n" \
"ds_read_b64_tr_b16 a[126:127], v215 offset:27136\n" \
"v_mfma_f32_16x16x32_bf16 v[52:55], a[108:111], a[92:95], v[52:55]\n" \
"buffer_load_dwordx4 v218, s[36:39], s82 offen offset:2048 lds\n" \
"v_mfma_f32_16x16x32_bf16 v[48:51], a[96:99], a[88:91], 0\n" \
"v_add_u32 v214, s85, v212\n" \
"v_xor_b32 v215, 32, v214\n" \
"v_mfma_f32_16x16x32_bf16 v[48:51], a[100:103], a[92:95], v[48:51]\n" \
"s_waitcnt vmcnt(15) lgkmcnt(0)\n" \
"s_barrier\n" \
"v_mfma_f32_16x16x32_bf16 v[8:11], a[112:115], a[64:67], 0\n" \
"ds_read_b64 v[130:131], v223\n" \
"ds_read_b64_tr_b16 a[96:97], v214 offset:16384\n" \
"ds_read_b64_tr_b16 a[98:99], v214 offset:18432\n" \
"v_mfma_f32_16x16x32_bf16 v[8:11], a[116:119], a[68:71], v[8:11]\n" \
"ds_read_b64 v[72:73], v223 offset:8\n" \
"buffer_load_dwordx4 v219, s[36:39], s82 offen offset:3072 lds\n" \
"v_mfma_f32_16x16x32_bf16 v[12:15], a[120:123], a[64:67], 0\n" \
"s_xor_b32 s82, s48, s49\n" \
"s_cmp_lt_u32 s48, s50\n" \
"s_cselect_b32 s82, s82, s48\n" \
"ds_read_b64_tr_b16 a[100:101], v214 offset:24576\n" \
"ds_read_b64_tr_b16 a[102:103], v214 offset:26624\n" \
"v_mfma_f32_16x16x32_bf16 v[12:15], a[124:127], a[68:71], v[12:15]\n" \
"s_add_u32 s81, s82, s46\n" \
"ds_read_b128 a[64:67], v213 offset:0\n" \
"v_mfma_f32_16x16x32_bf16 v[28:31], a[120:123], a[72:75], 0\n" \
"s_add_u32 m0, s86, s79\n" \
"ds_read_b128 a[68:71], v213 offset:2048\n" \
"v_mfma_f32_16x16x32_bf16 v[28:31], a[124:127], a[76:79], v[28:31]\n" \
"buffer_load_dwordx4 v234, s[56:59], s81 offen offset:0 sc1 lds\n" \
"v_mfma_f32_16x16x32_bf16 v[24:27], a[112:115], a[72:75], 0\n" \
"ds_read_b64_tr_b16 a[104:105], v215 offset:16384\n" \
"ds_read_b64_tr_b16 a[106:107], v215 offset:18432\n" \
"v_mfma_f32_16x16x32_bf16 v[24:27], a[116:119], a[76:79], v[24:27]\n" \
"s_waitcnt lgkmcnt(4)\n" \
"s_lshl_b32 s82, s82, 5\n" \
"ds_read_b128 a[72:75], v213 offset:256\n" \
"v_mfma_f32_16x16x32_bf16 v[40:43], a[112:115], a[80:83], 0\n" \
"s_add_u32 s82, s82, s47\n" \
"ds_read_b128 a[76:79], v213 offset:2304\n" \
"v_mfma_f32_16x16x32_bf16 v[148:151], v[128:131], v[112:115], v[148:151]\n" \
"v_max_f32 v172, v172, v176\n" \
"v_max_f32 v173, v173, v177\n" \
"v_max_f32 v174, v174, v178\n" \
"v_max_f32 v175, v175, v179\n" \
"v_max_f32 v164, v164, v168\n" \
"v_max_f32 v165, v165, v169\n" \
"v_max_f32 v166, v166, v170\n" \
"v_max_f32 v167, v167, v171\n" \
"v_max_f32 v156, v156, v160\n" \
"v_max_f32 v157, v157, v161\n" \
"v_max_f32 v158, v158, v162\n" \
"v_max_f32 v159, v159, v163\n" \
"v_max_f32 v148, v148, v152\n" \
"v_max_f32 v149, v149, v153\n" \
"v_max_f32 v150, v150, v154\n" \
"v_max_f32 v151, v151, v155\n" \
"v_max3_f32 v183, v172, v173, v174\n" \
"v_max3_f32 v182, v164, v165, v166\n" \
"v_max3_f32 v181, v156, v157, v158\n" \
"v_mfma_f32_16x16x32_bf16 v[40:43], a[116:119], a[84:87], v[40:43]\n" \
"buffer_load_dwordx4 v234, s[56:59], s81 offen offset:1024 sc1 lds\n" \
"v_max3_f32 v180, v148, v149, v150\n" \
"v_mfma_f32_16x16x32_bf16 v[44:47], a[120:123], a[80:83], 0\n" \
"ds_read_b64_tr_b16 a[108:109], v215 offset:24576\n" \
"ds_read_b64_tr_b16 a[110:111], v215 offset:26624\n" \
"v_mfma_f32_16x16x32_bf16 v[44:47], a[124:127], a[84:87], v[44:47]\n" \
"buffer_load_dwordx4 v234, s[56:59], s81 offen offset:2048 sc1 lds\n" \
"v_max_f32 v183, v183, v175\n" \
"v_max_f32 v182, v182, v167\n" \
"v_max_f32 v181, v181, v159\n" \
"v_mfma_f32_16x16x32_bf16 v[60:63], a[120:123], a[88:91], 0\n" \
"ds_read_b128 a[80:83], v213 offset:4096\n" \
"s_waitcnt lgkmcnt(4)\n" \
"v_mfma_f32_16x16x32_bf16 v[60:63], a[124:127], a[92:95], v[60:63]\n" \
"ds_read_b128 a[84:87], v213 offset:6144\n" \
"v_max_f32 v180, v180, v151\n" \
"v_cvt_pk_bf16_f32 v76, v92, v93\n" \
"v_cvt_pk_bf16_f32 v77, v94, v95\n" \
"v_mfma_f32_16x16x32_bf16 v[56:59], a[112:115], a[88:91], 0\n" \
"buffer_load_dwordx4 v234, s[56:59], s81 offen offset:3072 sc1 lds\n" \
"v_mfma_f32_16x16x32_bf16 v[56:59], a[116:119], a[92:95], v[56:59]\n" \
"s_getpc_b64 s[44:45]\n" \
"Lop_%=:\n" \
"s_add_u32 s44, s44, Lor_%=-Lop_%=\n" \
"s_addc_u32 s45, s45, 0\n" \
"v_permlane32_swap_b32 v180, v181\n" \
"v_permlane32_swap_b32 v182, v183\n" \
"v_max_f32 v180, v180, v181\n" \
"v_mfma_f32_16x16x32_bf16 v[0:3], a[96:99], a[64:67], v[0:3]\n" \
"ds_read_b128 a[88:91], v213 offset:4352\n" \
"v_max_f32 v182, v182, v183\n" \
"v_mfma_f32_16x16x32_bf16 v[0:3], a[100:103], a[68:71], v[0:3]\n" \
"s_waitcnt lgkmcnt(2)\n" \
"ds_read_b128 a[92:95], v213 offset:6400\n" \
"v_cvt_pk_bf16_f32 v198, v96, v97\n" \
"v_cvt_pk_bf16_f32 v199, v98, v99\n" \
"v_permlane16_swap_b32 v180, v182\n" \
"v_mfma_f32_16x16x32_bf16 v[4:7], a[104:107], a[64:67], v[4:7]\n" \
"ds_read_b64_tr_b16 a[112:113], v214 offset:16896\n" \
"v_max_f32 v180, v180, v182\n" \
"v_mfma_f32_16x16x32_bf16 v[4:7], a[108:111], a[68:71], v[4:7]\n" \
"ds_read_b64_tr_b16 a[114:115], v214 offset:18944\n" \
"v_mul_f32 v180, v180, v144\n" \
"v_cvt_pk_bf16_f32 v78, v100, v101\n" \
"v_cvt_pk_bf16_f32 v79, v102, v103\n" \
"v_mov_b32 v182, v180\n" \
"v_mfma_f32_16x16x32_bf16 v[20:23], a[104:107], a[72:75], v[20:23]\n" \
"s_add_u32 m0, m0, 0x4000\n" \
"ds_read_b64_tr_b16 a[116:117], v214 offset:25088\n" \
"v_cvt_pk_bf16_f32 v200, v104, v105\n" \
"v_mfma_f32_16x16x32_bf16 v[20:23], a[108:111], a[76:79], v[20:23]\n" \
"buffer_load_dwordx4 v216, s[36:39], s82 offen offset:0 lds\n" \
"v_cvt_pk_bf16_f32 v201, v106, v107\n" \
"v_permlane16_swap_b32 v180, v182\n" \
"v_mfma_f32_16x16x32_bf16 v[16:19], a[96:99], a[72:75], v[16:19]\n" \
"s_sub_u32 s85, s85, 32768\n" \
"s_cselect_b32 s85, 131072, s85\n" \
"ds_read_b64_tr_b16 a[118:119], v214 offset:27136\n" \
"v_mfma_f32_16x16x32_bf16 v[16:19], a[100:103], a[76:79], v[16:19]\n" \
"s_waitcnt lgkmcnt(3)\n" \
"ds_read_b64_tr_b16 a[120:121], v215 offset:16896\n" \
"v_mov_b32 v181, v180\n" \
"v_mov_b32 v183, v182\n" \
"v_cvt_pk_bf16_f32 v80, v108, v109\n" \
"v_cvt_pk_bf16_f32 v81, v110, v111\n" \
"v_mfma_f32_16x16x32_bf16 v[32:35], a[96:99], a[80:83], v[32:35]\n" \
"s_add_u32 s48, s48, 0x1000\n" \
"ds_read_b64_tr_b16 a[122:123], v215 offset:18944\n" \
"v_permlane32_swap_b32 v180, v181\n" \
"v_mfma_f32_16x16x32_bf16 v[32:35], a[100:103], a[84:87], v[32:35]\n" \
"buffer_load_dwordx4 v217, s[36:39], s82 offen offset:1024 lds\n" \
"v_permlane32_swap_b32 v182, v183\n" \
"v_fma_f32 v172, v172, v144, -v183\n" \
"v_mfma_f32_16x16x32_bf16 v[36:39], a[104:107], a[80:83], v[36:39]\n" \
"s_sub_u32 s86, s86, 32768\n" \
"s_cselect_b32 s86, 131072, s86\n" \
"ds_read_b64_tr_b16 a[124:125], v215 offset:25088\n" \
"v_add_u32 v213, s85, v211\n" \
"v_fma_f32 v164, v164, v144, -v182\n" \
"v_mfma_f32_16x16x32_bf16 v[36:39], a[108:111], a[84:87], v[36:39]\n" \
"buffer_load_dwordx4 v218, s[36:39], s82 offen offset:2048 lds\n" \
"v_fma_f32 v156, v156, v144, -v181\n" \
"v_fma_f32 v148, v148, v144, -v180\n" \
"v_mfma_f32_16x16x32_bf16 v[52:55], a[104:107], a[88:91], v[52:55]\n" \
"ds_read_b64_tr_b16 a[126:127], v215 offset:27136\n" \
"v_fma_f32 v173, v173, v144, -v183\n" \
"v_mfma_f32_16x16x32_bf16 v[52:55], a[108:111], a[92:95], v[52:55]\n" \
"buffer_load_dwordx4 v219, s[36:39], s82 offen offset:3072 lds\n" \
"v_fma_f32 v165, v165, v144, -v182\n" \
"v_fma_f32 v157, v157, v144, -v181\n" \
"v_mfma_f32_16x16x32_bf16 v[48:51], a[96:99], a[88:91], v[48:51]\n" \
"s_xor_b32 s82, s48, s49\n" \
"s_cmp_lt_u32 s48, s50\n" \
"s_cselect_b32 s82, s82, s48\n" \
"v_add_u32 v214, s85, v212\n" \
"v_xor_b32 v215, 32, v214\n" \
"v_fma_f32 v149, v149, v144, -v180\n" \
"v_mfma_f32_16x16x32_bf16 v[48:51], a[100:103], a[92:95], v[48:51]\n" \
"s_add_u32 s81, s82, s46\n" \
"v_fma_f32 v174, v174, v144, -v183\n" \
"v_fma_f32 v166, v166, v144, -v182\n" \
"s_waitcnt vmcnt(16) lgkmcnt(0)\n" \
"s_barrier\n" \
"v_mfma_f32_16x16x32_bf16 v[8:11], a[112:115], a[64:67], v[8:11]\n" \
"s_add_u32 m0, s86, s79\n" \
"ds_read_b64_tr_b16 a[96:97], v214 offset:16384\n" \
"ds_read_b64_tr_b16 a[98:99], v214 offset:18432\n" \
"v_mfma_f32_16x16x32_bf16 v[8:11], a[116:119], a[68:71], v[8:11]\n" \
"buffer_load_dwordx4 v234, s[56:59], s81 offen offset:0 sc1 lds\n" \
"v_fma_f32 v158, v158, v144, -v181\n" \
"v_fma_f32 v150, v150, v144, -v180\n" \
"v_mfma_f32_16x16x32_bf16 v[12:15], a[120:123], a[64:67], v[12:15]\n" \
"ds_read_b64_tr_b16 a[100:101], v214 offset:24576\n" \
"ds_read_b64_tr_b16 a[102:103], v214 offset:26624\n" \
"v_mfma_f32_16x16x32_bf16 v[12:15], a[124:127], a[68:71], v[12:15]\n" \
"s_lshl_b32 s82, s82, 5\n" \
"ds_read_b128 a[64:67], v213 offset:0\n" \
"v_fma_f32 v175, v175, v144, -v183\n" \
"v_fma_f32 v167, v167, v144, -v182\n" \
"v_fma_f32 v159, v159, v144, -v181\n" \
"v_fma_f32 v151, v151, v144, -v180\n" \
"v_mfma_f32_16x16x32_bf16 v[28:31], a[120:123], a[72:75], v[28:31]\n" \
"s_add_u32 s82, s82, s47\n" \
"ds_read_b128 a[68:71], v213 offset:2048\n" \
"v_mfma_f32_16x16x32_bf16 v[28:31], a[124:127], a[76:79], v[28:31]\n" \
"buffer_load_dwordx4 v234, s[56:59], s81 offen offset:1024 sc1 lds\n" \
"v_exp_f32 v172, v172\n" \
"v_mfma_f32_16x16x32_bf16 v[24:27], a[112:115], a[72:75], v[24:27]\n" \
"ds_read_b64_tr_b16 a[104:105], v215 offset:16384\n" \
"ds_read_b64_tr_b16 a[106:107], v215 offset:18432\n" \
"v_mfma_f32_16x16x32_bf16 v[24:27], a[116:119], a[76:79], v[24:27]\n" \
"ds_read_b128 a[72:75], v213 offset:256\n" \
"v_exp_f32 v164, v164\n" \
"v_exp_f32 v156, v156\n" \
"v_mfma_f32_16x16x32_bf16 v[40:43], a[112:115], a[80:83], v[40:43]\n" \
"ds_read_b128 a[76:79], v213 offset:2304\n" \
"v_mfma_f32_16x16x32_bf16 v[40:43], a[116:119], a[84:87], v[40:43]\n" \
"buffer_load_dwordx4 v234, s[56:59], s81 offen offset:2048 sc1 lds\n" \
"v_exp_f32 v148, v148\n" \
"v_mfma_f32_16x16x32_bf16 v[44:47], a[120:123], a[80:83], v[44:47]\n" \
"ds_read_b64_tr_b16 a[108:109], v215 offset:24576\n" \
"ds_read_b64_tr_b16 a[110:111], v215 offset:26624\n" \
"v_mfma_f32_16x16x32_bf16 v[44:47], a[124:127], a[84:87], v[44:47]\n" \
"buffer_load_dwordx4 v234, s[56:59], s81 offen offset:3072 sc1 lds\n" \
"v_exp_f32 v173, v173\n" \
"v_mfma_f32_16x16x32_bf16 v[60:63], a[120:123], a[88:91], v[60:63]\n" \
"ds_read_b128 a[80:83], v213 offset:4096\n" \
"s_waitcnt lgkmcnt(4)\n" \
"v_mfma_f32_16x16x32_bf16 v[60:63], a[124:127], a[92:95], v[60:63]\n" \
"s_add_u32 m0, m0, 0x4000\n" \
"ds_read_b128 a[84:87], v213 offset:6144\n" \
"v_exp_f32 v165, v165\n" \
"v_exp_f32 v157, v157\n" \
"v_mfma_f32_16x16x32_bf16 v[56:59], a[112:115], a[88:91], v[56:59]\n" \
"buffer_load_dwordx4 v216, s[36:39], s82 offen offset:0 lds\n" \
"v_mfma_f32_16x16x32_bf16 v[56:59], a[116:119], a[92:95], v[56:59]\n" \
"v_exp_f32 v149, v149\n" \
"v_exp_f32 v174, v174\n" \
"v_mfma_f32_16x16x32_bf16 v[0:3], a[96:99], a[64:67], v[0:3]\n" \
"ds_read_b128 a[88:91], v213 offset:4352\n" \
"v_mfma_f32_16x16x32_bf16 v[0:3], a[100:103], a[68:71], v[0:3]\n" \
"s_waitcnt lgkmcnt(2)\n" \
"ds_read_b128 a[92:95], v213 offset:6400\n" \
"v_exp_f32 v166, v166\n" \
"v_exp_f32 v158, v158\n" \
"v_mfma_f32_16x16x32_bf16 v[4:7], a[104:107], a[64:67], v[4:7]\n" \
"ds_read_b64_tr_b16 a[112:113], v214 offset:16896\n" \
"v_mfma_f32_16x16x32_bf16 v[4:7], a[108:111], a[68:71], v[4:7]\n" \
"ds_read_b64_tr_b16 a[114:115], v214 offset:18944\n" \
"v_exp_f32 v150, v150\n" \
"v_exp_f32 v175, v175\n" \
"v_mfma_f32_16x16x32_bf16 v[20:23], a[104:107], a[72:75], v[20:23]\n" \
"s_add_u32 s48, s48, 0x1000\n" \
"ds_read_b64_tr_b16 a[116:117], v214 offset:25088\n" \
"v_mfma_f32_16x16x32_bf16 v[20:23], a[108:111], a[76:79], v[20:23]\n" \
"buffer_load_dwordx4 v217, s[36:39], s82 offen offset:1024 lds\n" \
"v_exp_f32 v167, v167\n" \
"v_mfma_f32_16x16x32_bf16 v[16:19], a[96:99], a[72:75], v[16:19]\n" \
"s_sub_u32 s85, s85, 32768\n" \
"s_cselect_b32 s85, 131072, s85\n" \
"ds_read_b64_tr_b16 a[118:119], v214 offset:27136\n" \
"v_mfma_f32_16x16x32_bf16 v[16:19], a[100:103], a[76:79], v[16:19]\n" \
"s_waitcnt lgkmcnt(3)\n" \
"ds_read_b64_tr_b16 a[120:121], v215 offset:16896\n" \
"v_exp_f32 v159, v159\n" \
"v_exp_f32 v151, v151\n" \
"v_mfma_f32_16x16x32_bf16 v[32:35], a[96:99], a[80:83], v[32:35]\n" \
"s_sub_u32 s86, s86, 32768\n" \
"s_cselect_b32 s86, 131072, s86\n" \
"ds_read_b64_tr_b16 a[122:123], v215 offset:18944\n" \
"v_mfma_f32_16x16x32_bf16 v[32:35], a[100:103], a[84:87], v[32:35]\n" \
"buffer_load_dwordx4 v218, s[36:39], s82 offen offset:2048 lds\n" \
"v_add_f32 v191, v172, v173\n" \
"v_add_f32 v190, v164, v165\n" \
"v_mfma_f32_16x16x32_bf16 v[36:39], a[104:107], a[80:83], v[36:39]\n" \
"ds_read_b64_tr_b16 a[124:125], v215 offset:25088\n" \
"v_add_u32 v213, s85, v211\n" \
"v_mfma_f32_16x16x32_bf16 v[36:39], a[108:111], a[84:87], v[36:39]\n" \
"buffer_load_dwordx4 v219, s[36:39], s82 offen offset:3072 lds\n" \
"v_add_f32 v189, v156, v157\n" \
"v_add_f32 v188, v148, v149\n" \
"v_add_f32 v191, v191, v174\n" \
"v_mfma_f32_16x16x32_bf16 v[52:55], a[104:107], a[88:91], v[52:55]\n" \
"s_xor_b32 s82, s48, s49\n" \
"s_cmp_lt_u32 s48, s50\n" \
"s_cselect_b32 s82, s82, s48\n" \
"s_add_u32 s81, s82, s46\n" \
"s_add_u32 m0, s86, s79\n" \
"ds_read_b64_tr_b16 a[126:127], v215 offset:27136\n" \
"v_add_f32 v190, v190, v166\n" \
"v_mfma_f32_16x16x32_bf16 v[52:55], a[108:111], a[92:95], v[52:55]\n" \
"buffer_load_dwordx4 v234, s[56:59], s81 offen offset:0 sc1 lds\n" \
"v_add_f32 v189, v189, v158\n" \
"v_mfma_f32_16x16x32_bf16 v[48:51], a[96:99], a[88:91], v[48:51]\n" \
"v_add_u32 v214, s85, v212\n" \
"v_xor_b32 v215, 32, v214\n" \
"v_mfma_f32_16x16x32_bf16 v[48:51], a[100:103], a[92:95], v[48:51]\n" \
"s_lshl_b32 s82, s82, 5\n" \
"v_add_f32 v188, v188, v150\n" \
"v_add_f32 v191, v191, v175\n" \
"v_add_f32 v190, v190, v167\n" \
"s_waitcnt vmcnt(17) lgkmcnt(0)\n" \
"s_barrier\n" \
"v_mfma_f32_16x16x32_bf16 v[8:11], a[112:115], a[64:67], v[8:11]\n" \
"s_add_u32 s82, s82, s47\n" \
"ds_read_b64_tr_b16 a[96:97], v214 offset:16384\n" \
"ds_read_b64_tr_b16 a[98:99], v214 offset:18432\n" \
"v_mfma_f32_16x16x32_bf16 v[8:11], a[116:119], a[68:71], v[8:11]\n" \
"buffer_load_dwordx4 v234, s[56:59], s81 offen offset:1024 sc1 lds\n" \
"v_add_f32 v189, v189, v159\n" \
"v_add_f32 v188, v188, v151\n" \
"v_mfma_f32_16x16x32_bf16 v[12:15], a[120:123], a[64:67], v[12:15]\n" \
"ds_read_b64_tr_b16 a[100:101], v214 offset:24576\n" \
"ds_read_b64_tr_b16 a[102:103], v214 offset:26624\n" \
"v_mfma_f32_16x16x32_bf16 v[12:15], a[124:127], a[68:71], v[12:15]\n" \
"ds_read_b128 a[64:67], v213 offset:0\n" \
"v_permlane32_swap_b32 v188, v189\n" \
"v_permlane32_swap_b32 v190, v191\n" \
"v_add_f32 v188, v188, v189\n" \
"v_add_f32 v190, v190, v191\n" \
"v_mfma_f32_16x16x32_bf16 v[28:31], a[120:123], a[72:75], v[28:31]\n" \
"ds_read_b128 a[68:71], v213 offset:2048\n" \
"v_mfma_f32_16x16x32_bf16 v[28:31], a[124:127], a[76:79], v[28:31]\n" \
"buffer_load_dwordx4 v234, s[56:59], s81 offen offset:2048 sc1 lds\n" \
"v_permlane16_swap_b32 v188, v190\n" \
"v_add_f32 v188, v188, v190\n" \
"v_mfma_f32_16x16x32_bf16 v[24:27], a[112:115], a[72:75], v[24:27]\n" \
"ds_read_b64_tr_b16 a[104:105], v215 offset:16384\n" \
"ds_read_b64_tr_b16 a[106:107], v215 offset:18432\n" \
"v_mfma_f32_16x16x32_bf16 v[24:27], a[116:119], a[76:79], v[24:27]\n" \
"ds_read_b128 a[72:75], v213 offset:256\n" \
"v_rcp_f32 v188, v188\n" \
"v_mfma_f32_16x16x32_bf16 v[40:43], a[112:115], a[80:83], v[40:43]\n" \
"ds_read_b128 a[76:79], v213 offset:2304\n" \
"v_mfma_f32_16x16x32_bf16 v[40:43], a[116:119], a[84:87], v[40:43]\n" \
"buffer_load_dwordx4 v234, s[56:59], s81 offen offset:3072 sc1 lds\n" \
"v_mov_b32 v190, v188\n" \
"v_mfma_f32_16x16x32_bf16 v[44:47], a[120:123], a[80:83], v[44:47]\n" \
"s_add_u32 m0, m0, 0x4000\n" \
"ds_read_b64_tr_b16 a[108:109], v215 offset:24576\n" \
"ds_read_b64_tr_b16 a[110:111], v215 offset:26624\n" \
"v_mfma_f32_16x16x32_bf16 v[44:47], a[124:127], a[84:87], v[44:47]\n" \
"buffer_load_dwordx4 v216, s[36:39], s82 offen offset:0 lds\n" \
"v_permlane16_swap_b32 v188, v190\n" \
"v_mov_b32 v189, v188\n" \
"v_mov_b32 v191, v190\n" \
"v_mfma_f32_16x16x32_bf16 v[60:63], a[120:123], a[88:91], v[60:63]\n" \
"ds_read_b128 a[80:83], v213 offset:4096\n" \
"s_waitcnt lgkmcnt(4)\n" \
"v_mfma_f32_16x16x32_bf16 v[60:63], a[124:127], a[92:95], v[60:63]\n" \
"s_add_u32 s48, s48, 0x1000\n" \
"ds_read_b128 a[84:87], v213 offset:6144\n" \
"v_permlane32_swap_b32 v188, v189\n" \
"v_permlane32_swap_b32 v190, v191\n" \
"v_mul_f32 v172, v172, v191\n" \
"v_mul_f32 v164, v164, v190\n" \
"v_mul_f32 v156, v156, v189\n" \
"v_mul_f32 v148, v148, v188\n" \
"v_mfma_f32_16x16x32_bf16 v[56:59], a[112:115], a[88:91], v[56:59]\n" \
"buffer_load_dwordx4 v217, s[36:39], s82 offen offset:1024 lds\n" \
"v_mfma_f32_16x16x32_bf16 v[56:59], a[116:119], a[92:95], v[56:59]\n" \
"v_mul_f32 v173, v173, v191\n" \
"v_mul_f32 v165, v165, v190\n" \
"v_mul_f32 v157, v157, v189\n" \
"v_mul_f32 v149, v149, v188\n" \
"v_mul_f32 v174, v174, v191\n" \
"v_mfma_f32_16x16x32_bf16 v[0:3], a[96:99], a[64:67], v[0:3]\n" \
"ds_read_b128 a[88:91], v213 offset:4352\n" \
"v_mul_f32 v166, v166, v190\n" \
"v_mfma_f32_16x16x32_bf16 v[0:3], a[100:103], a[68:71], v[0:3]\n" \
"s_waitcnt lgkmcnt(2)\n" \
"ds_read_b128 a[92:95], v213 offset:6400\n" \
"v_mul_f32 v158, v158, v189\n" \
"v_mul_f32 v150, v150, v188\n" \
"v_mul_f32 v175, v175, v191\n" \
"v_mfma_f32_16x16x32_bf16 v[4:7], a[104:107], a[64:67], v[4:7]\n" \
"ds_read_b64_tr_b16 a[112:113], v214 offset:16896\n" \
"v_mul_f32 v167, v167, v190\n" \
"v_mfma_f32_16x16x32_bf16 v[4:7], a[108:111], a[68:71], v[4:7]\n" \
"ds_read_b64_tr_b16 a[114:115], v214 offset:18944\n" \
"v_mul_f32 v159, v159, v189\n" \
"v_mul_f32 v151, v151, v188\n" \
"v_cvt_pk_bf16_f32 v178, v172, v173\n" \
"v_cvt_pk_bf16_f32 v179, v174, v175\n" \
"v_mfma_f32_16x16x32_bf16 v[20:23], a[104:107], a[72:75], v[20:23]\n" \
"s_sub_u32 s86, s86, 32768\n" \
"s_cselect_b32 s86, 131072, s86\n" \
"ds_read_b64_tr_b16 a[116:117], v214 offset:25088\n" \
"v_mfma_f32_16x16x32_bf16 v[20:23], a[108:111], a[76:79], v[20:23]\n" \
"buffer_load_dwordx4 v218, s[36:39], s82 offen offset:2048 lds\n" \
"v_cvt_pk_bf16_f32 v170, v164, v165\n" \
"v_cvt_pk_bf16_f32 v171, v166, v167\n" \
"v_mfma_f32_16x16x32_bf16 v[16:19], a[96:99], a[72:75], v[16:19]\n" \
"s_sub_u32 s85, s85, 32768\n" \
"s_cselect_b32 s85, 131072, s85\n" \
"ds_read_b64_tr_b16 a[118:119], v214 offset:27136\n" \
"v_mfma_f32_16x16x32_bf16 v[16:19], a[100:103], a[76:79], v[16:19]\n" \
"s_waitcnt lgkmcnt(3)\n" \
"ds_read_b64_tr_b16 a[120:121], v215 offset:16896\n" \
"v_cvt_pk_bf16_f32 v162, v156, v157\n" \
"v_cvt_pk_bf16_f32 v163, v158, v159\n" \
"v_cvt_pk_bf16_f32 v154, v148, v149\n" \
"v_cvt_pk_bf16_f32 v155, v150, v151\n" \
"v_mfma_f32_16x16x32_bf16 v[32:35], a[96:99], a[80:83], v[32:35]\n" \
"ds_read_b64_tr_b16 a[122:123], v215 offset:18944\n" \
"v_and_b32 v176, v178, v184\n" \
"v_mfma_f32_16x16x32_bf16 v[32:35], a[100:103], a[84:87], v[32:35]\n" \
"buffer_load_dwordx4 v219, s[36:39], s82 offen offset:3072 lds\n" \
"v_and_b32 v177, v179, v185\n" \
"v_xor_b32 v178, v178, v176\n" \
"v_mfma_f32_16x16x32_bf16 v[36:39], a[104:107], a[80:83], v[36:39]\n" \
"s_xor_b32 s82, s48, s49\n" \
"s_cmp_lt_u32 s48, s50\n" \
"s_cselect_b32 s82, s82, s48\n" \
"s_add_u32 s81, s82, s46\n" \
"s_add_u32 m0, s86, s79\n" \
"ds_read_b64_tr_b16 a[124:125], v215 offset:25088\n" \
"v_add_u32 v213, s85, v211\n" \
"v_xor_b32 v179, v179, v177\n" \
"v_mfma_f32_16x16x32_bf16 v[36:39], a[108:111], a[84:87], v[36:39]\n" \
"buffer_load_dwordx4 v234, s[56:59], s81 offen offset:0 sc1 lds\n" \
"v_and_b32 v168, v170, v184\n" \
"v_and_b32 v169, v171, v185\n" \
"v_mfma_f32_16x16x32_bf16 v[52:55], a[104:107], a[88:91], v[52:55]\n" \
"s_lshl_b32 s82, s82, 5\n" \
"s_add_u32 s82, s82, s47\n" \
"ds_read_b64_tr_b16 a[126:127], v215 offset:27136\n" \
"v_mfma_f32_16x16x32_bf16 v[52:55], a[108:111], a[92:95], v[52:55]\n" \
"buffer_load_dwordx4 v234, s[56:59], s81 offen offset:1024 sc1 lds\n" \
"v_xor_b32 v170, v170, v168\n" \
"v_xor_b32 v171, v171, v169\n" \
"v_mfma_f32_16x16x32_bf16 v[48:51], a[96:99], a[88:91], v[48:51]\n" \
"v_add_u32 v214, s85, v212\n" \
"v_xor_b32 v215, 32, v214\n" \
"v_mfma_f32_16x16x32_bf16 v[48:51], a[100:103], a[92:95], v[48:51]\n" \
"v_and_b32 v160, v162, v184\n" \
"v_and_b32 v161, v163, v185\n" \
"v_xor_b32 v162, v162, v160\n" \
"s_waitcnt vmcnt(18) lgkmcnt(0)\n" \
"s_barrier\n" \
"v_mfma_f32_16x16x32_bf16 v[8:11], a[112:115], a[64:67], v[8:11]\n" \
"ds_read_b64_tr_b16 a[96:97], v214 offset:16384\n" \
"ds_read_b64_tr_b16 a[98:99], v214 offset:18432\n" \
"v_mfma_f32_16x16x32_bf16 v[8:11], a[116:119], a[68:71], v[8:11]\n" \
"buffer_load_dwordx4 v234, s[56:59], s81 offen offset:2048 sc1 lds\n" \
"v_xor_b32 v163, v163, v161\n" \
"v_and_b32 v152, v154, v184\n" \
"v_mfma_f32_16x16x32_bf16 v[12:15], a[120:123], a[64:67], v[12:15]\n" \
"ds_read_b64_tr_b16 a[100:101], v214 offset:24576\n" \
"ds_read_b64_tr_b16 a[102:103], v214 offset:26624\n" \
"v_mfma_f32_16x16x32_bf16 v[12:15], a[124:127], a[68:71], v[12:15]\n" \
"ds_read_b128 a[64:67], v213 offset:0\n" \
"v_and_b32 v153, v155, v185\n" \
"v_xor_b32 v154, v154, v152\n" \
"v_xor_b32 v155, v155, v153\n" \
"v_mfma_f32_16x16x32_bf16 v[28:31], a[120:123], a[72:75], v[28:31]\n" \
"ds_read_b128 a[68:71], v213 offset:2048\n" \
"v_mfma_f32_16x16x32_bf16 v[136:139], v[198:201], v[176:179], 0\n" \
"v_mfma_f32_16x16x32_bf16 v[140:143], v[78:81], v[176:179], 0\n" \
"v_mfma_f32_16x16x32_bf16 v[128:131], v[196:199], v[168:171], 0\n" \
"v_mfma_f32_16x16x32_bf16 v[132:135], v[76:79], v[168:171], 0\n" \
"v_mfma_f32_16x16x32_bf16 v[120:123], v[194:197], v[160:163], 0\n" \
"v_mfma_f32_16x16x32_bf16 v[124:127], v[74:77], v[160:163], 0\n" \
"v_mfma_f32_16x16x32_bf16 v[112:115], v[192:195], v[152:155], 0\n" \
"v_mfma_f32_16x16x32_bf16 v[116:119], v[72:75], v[152:155], 0\n" \
"v_mfma_f32_16x16x32_bf16 v[28:31], a[124:127], a[76:79], v[28:31]\n" \
"buffer_load_dwordx4 v234, s[56:59], s81 offen offset:3072 sc1 lds\n" \
"v_mfma_f32_16x16x32_bf16 v[24:27], a[112:115], a[72:75], v[24:27]\n" \
"ds_read_b64_tr_b16 a[104:105], v215 offset:16384\n" \
"ds_read_b64_tr_b16 a[106:107], v215 offset:18432\n" \
"v_mfma_f32_16x16x32_bf16 v[24:27], a[116:119], a[76:79], v[24:27]\n" \
"ds_read_b128 a[72:75], v213 offset:256\n" \
"v_cvt_pk_bf16_f32 v136, v136, v137\n" \
"v_cvt_pk_bf16_f32 v137, v138, v139\n" \
"v_cvt_pk_bf16_f32 v138, v140, v141\n" \
"v_cvt_pk_bf16_f32 v139, v142, v143\n" \
"v_cndmask_b32 v140, v136, v138, s[76:77]\n" \
"v_mfma_f32_16x16x32_bf16 v[40:43], a[112:115], a[80:83], v[40:43]\n" \
"s_add_u32 m0, m0, 0x4000\n" \
"ds_read_b128 a[76:79], v213 offset:2304\n" \
"v_cndmask_b32 v141, v137, v139, s[76:77]\n" \
"v_mfma_f32_16x16x32_bf16 v[40:43], a[116:119], a[84:87], v[40:43]\n" \
"buffer_load_dwordx4 v216, s[36:39], s82 offen offset:0 lds\n" \
"v_cndmask_b32 v142, v138, v136, s[76:77]\n" \
"v_cndmask_b32 v143, v139, v137, s[76:77]\n" \
"v_mfma_f32_16x16x32_bf16 v[44:47], a[120:123], a[80:83], v[44:47]\n" \
"s_add_u32 s48, s48, 0x1000\n" \
"ds_read_b64_tr_b16 a[108:109], v215 offset:24576\n" \
"ds_read_b64_tr_b16 a[110:111], v215 offset:26624\n" \
"v_mfma_f32_16x16x32_bf16 v[44:47], a[124:127], a[84:87], v[44:47]\n" \
"buffer_load_dwordx4 v217, s[36:39], s82 offen offset:1024 lds\n" \
"v_cvt_pk_bf16_f32 v128, v128, v129\n" \
"v_cvt_pk_bf16_f32 v129, v130, v131\n" \
"v_cvt_pk_bf16_f32 v130, v132, v133\n" \
"v_mfma_f32_16x16x32_bf16 v[60:63], a[120:123], a[88:91], v[60:63]\n" \
"ds_read_b128 a[80:83], v213 offset:4096\n" \
"s_waitcnt lgkmcnt(4)\n" \
"v_mfma_f32_16x16x32_bf16 v[60:63], a[124:127], a[92:95], v[60:63]\n" \
"s_sub_u32 s86, s86, 32768\n" \
"s_cselect_b32 s86, 131072, s86\n" \
"ds_read_b128 a[84:87], v213 offset:6144\n" \
"v_cvt_pk_bf16_f32 v131, v134, v135\n" \
"v_cndmask_b32 v132, v128, v130, s[76:77]\n" \
"v_mfma_f32_16x16x32_bf16 v[56:59], a[112:115], a[88:91], v[56:59]\n" \
"buffer_load_dwordx4 v218, s[36:39], s82 offen offset:2048 lds\n" \
"v_mfma_f32_16x16x32_bf16 v[56:59], a[116:119], a[92:95], v[56:59]\n" \
"v_cndmask_b32 v133, v129, v131, s[76:77]\n" \
"v_cndmask_b32 v134, v130, v128, s[76:77]\n" \
"v_cndmask_b32 v135, v131, v129, s[76:77]\n" \
"v_cvt_pk_bf16_f32 v120, v120, v121\n" \
"v_cvt_pk_bf16_f32 v121, v122, v123\n" \
"v_mfma_f32_16x16x32_bf16 v[0:3], a[96:99], a[64:67], v[0:3]\n" \
"ds_read_b128 a[88:91], v213 offset:4352\n" \
"v_cvt_pk_bf16_f32 v122, v124, v125\n" \
"v_mfma_f32_16x16x32_bf16 v[0:3], a[100:103], a[68:71], v[0:3]\n" \
"s_waitcnt lgkmcnt(2)\n" \
"ds_read_b128 a[92:95], v213 offset:6400\n" \
"v_cvt_pk_bf16_f32 v123, v126, v127\n" \
"v_cndmask_b32 v124, v120, v122, s[76:77]\n" \
"v_cndmask_b32 v125, v121, v123, s[76:77]\n" \
"v_mfma_f32_16x16x32_bf16 v[4:7], a[104:107], a[64:67], v[4:7]\n" \
"ds_read_b64_tr_b16 a[112:113], v214 offset:16896\n" \
"v_cndmask_b32 v126, v122, v120, s[76:77]\n" \
"v_mfma_f32_16x16x32_bf16 v[4:7], a[108:111], a[68:71], v[4:7]\n" \
"Lpg10223_%=:\n" \
"s_cbranch_execz Lcb1_%=\n" \
"ds_read_b64_tr_b16 a[114:115], v214 offset:18944\n" \
"v_cndmask_b32 v127, v123, v121, s[76:77]\n" \
"v_cvt_pk_bf16_f32 v112, v112, v113\n" \
"v_cvt_pk_bf16_f32 v113, v114, v115\n" \
"v_cvt_pk_bf16_f32 v114, v116, v117\n" \
"v_mfma_f32_16x16x32_bf16 v[20:23], a[104:107], a[72:75], v[20:23]\n" \
"s_waitcnt lgkmcnt(4)\n" \
"ds_read_b64_tr_b16 a[116:117], v214 offset:25088\n" \
"v_cvt_pk_bf16_f32 v115, v118, v119\n" \
"v_mfma_f32_16x16x32_bf16 v[20:23], a[108:111], a[76:79], v[20:23]\n" \
"buffer_load_dwordx4 v219, s[36:39], s82 offen offset:3072 lds\n" \
"v_cndmask_b32 v116, v112, v114, s[76:77]\n" \
"v_cndmask_b32 v117, v113, v115, s[76:77]\n" \
"v_mfma_f32_16x16x32_bf16 v[16:19], a[96:99], a[72:75], v[16:19]\n" \
"s_sub_u32 s85, s85, 32768\n" \
"s_cselect_b32 s85, 131072, s85\n" \
"s_xor_b32 s82, s48, s49\n" \
"s_cmp_lt_u32 s48, s50\n" \
"s_cselect_b32 s82, s82, s48\n" \
"ds_read_b64_tr_b16 a[118:119], v214 offset:27136\n" \
"v_cndmask_b32 v118, v114, v112, s[76:77]\n" \
"v_mfma_f32_16x16x32_bf16 v[16:19], a[100:103], a[76:79], v[16:19]\n" \
"s_add_u32 s81, s82, s46\n" \
"ds_read_b64_tr_b16 a[120:121], v215 offset:16896\n" \
"v_cndmask_b32 v119, v115, v113, s[76:77]\n" \
"s_add_u32 s71, s93, 208896\n" \
"v_mfma_f32_16x16x32_bf16 v[32:35], a[96:99], a[80:83], v[32:35]\n" \
"s_add_u32 m0, s86, s79\n" \
"ds_read_b64_tr_b16 a[122:123], v215 offset:18944\n" \
"buffer_store_dwordx4 v[140:143], v236, s[56:59], s71 offen sc1\n" \
"s_add_u32 s71, s93, 139264\n" \
"v_mfma_f32_16x16x32_bf16 v[32:35], a[100:103], a[84:87], v[32:35]\n" \
"buffer_load_dwordx4 v234, s[56:59], s81 offen offset:0 sc1 lds\n" \
"v_mfma_f32_16x16x32_bf16 v[36:39], a[104:107], a[80:83], v[36:39]\n" \
"s_lshl_b32 s82, s82, 5\n" \
"s_add_u32 s82, s82, s47\n" \
"ds_read_b64_tr_b16 a[124:125], v215 offset:25088\n" \
"buffer_store_dwordx4 v[132:135], v236, s[56:59], s71 offen sc1\n" \
"s_add_u32 s71, s93, 69632\n" \
"v_add_u32_e64 v213, s85, v211\n" \
"v_mfma_f32_16x16x32_bf16 v[36:39], a[108:111], a[84:87], v[36:39]\n" \
"s_waitcnt lgkmcnt(3)\n" \
"buffer_load_dwordx4 v234, s[56:59], s81 offen offset:1024 sc1 lds\n" \
"v_mfma_f32_16x16x32_bf16 v[52:55], a[104:107], a[88:91], v[52:55]\n" \
"ds_read_b64_tr_b16 a[126:127], v215 offset:27136\n" \
"buffer_store_dwordx4 v[124:127], v236, s[56:59], s71 offen sc1\n" \
"s_add_u32 s71, s93, 0\n" \
"v_mfma_f32_16x16x32_bf16 v[52:55], a[108:111], a[92:95], v[52:55]\n" \
"buffer_load_dwordx4 v234, s[56:59], s81 offen offset:2048 sc1 lds\n" \
"v_mfma_f32_16x16x32_bf16 v[48:51], a[96:99], a[88:91], v[48:51]\n" \
"v_add_u32_e64 v214, s85, v212\n" \
"v_xor_b32_e64 v215, 32, v214\n" \
"v_mfma_f32_16x16x32_bf16 v[48:51], a[100:103], a[92:95], v[48:51]\n" \
"buffer_store_dwordx4 v[116:119], v236, s[56:59], s71 offen sc1\n" \
"s_waitcnt vmcnt(23) lgkmcnt(0)\n" \
"s_barrier\n" \
"v_mfma_f32_16x16x32_bf16 v[8:11], a[112:115], a[64:67], v[8:11]\n" \
"ds_read_b64_tr_b16 a[96:97], v214 offset:16384\n" \
"ds_read_b64_tr_b16 a[98:99], v214 offset:18432\n" \
"v_mfma_f32_16x16x32_bf16 v[8:11], a[116:119], a[68:71], v[8:11]\n" \
"buffer_load_dwordx4 v234, s[56:59], s81 offen offset:3072 sc1 lds\n" \
"v_mfma_f32_16x16x32_bf16 v[12:15], a[120:123], a[64:67], v[12:15]\n" \
"ds_read_b64_tr_b16 a[100:101], v214 offset:24576\n" \
"ds_read_b64_tr_b16 a[102:103], v214 offset:26624\n" \
"v_mfma_f32_16x16x32_bf16 v[12:15], a[124:127], a[68:71], v[12:15]\n" \
"ds_read_b128 a[64:67], v213 offset:0\n" \
"v_mfma_f32_16x16x32_bf16 v[28:31], a[120:123], a[72:75], v[28:31]\n" \
"s_add_u32 m0, m0, 0x4000\n" \
"ds_read_b128 a[68:71], v213 offset:2048\n" \
"v_mfma_f32_16x16x32_bf16 v[28:31], a[124:127], a[76:79], v[28:31]\n" \
"buffer_load_dwordx4 v216, s[36:39], s82 offen offset:0 lds\n" \
"v_mfma_f32_16x16x32_bf16 v[24:27], a[112:115], a[72:75], v[24:27]\n" \
"ds_read_b64_tr_b16 a[104:105], v215 offset:16384\n" \
"ds_read_b64_tr_b16 a[106:107], v215 offset:18432\n" \
"v_mfma_f32_16x16x32_bf16 v[24:27], a[116:119], a[76:79], v[24:27]\n" \
"ds_read_b128 a[72:75], v213 offset:256\n" \
"v_mfma_f32_16x16x32_bf16 v[40:43], a[112:115], a[80:83], v[40:43]\n" \
"s_add_u32 s48, s48, 0x1000\n" \
"ds_read_b128 a[76:79], v213 offset:2304\n" \
"v_mfma_f32_16x16x32_bf16 v[40:43], a[116:119], a[84:87], v[40:43]\n" \
"buffer_load_dwordx4 v217, s[36:39], s82 offen offset:1024 lds\n" \
"v_mfma_f32_16x16x32_bf16 v[44:47], a[120:123], a[80:83], v[44:47]\n" \
"s_sub_u32 s86, s86, 32768\n" \
"s_cselect_b32 s86, 131072, s86\n" \
"ds_read_b64_tr_b16 a[108:109], v215 offset:24576\n" \
"ds_read_b64_tr_b16 a[110:111], v215 offset:26624\n" \
"v_mfma_f32_16x16x32_bf16 v[44:47], a[124:127], a[84:87], v[44:47]\n" \
"buffer_load_dwordx4 v218, s[36:39], s82 offen offset:2048 lds\n" \
"v_mfma_f32_16x16x32_bf16 v[60:63], a[120:123], a[88:91], v[60:63]\n" \
"ds_read_b128 a[80:83], v213 offset:4096\n" \
"s_waitcnt lgkmcnt(4)\n" \
"v_mfma_f32_16x16x32_bf16 v[60:63], a[124:127], a[92:95], v[60:63]\n" \
"ds_read_b128 a[84:87], v213 offset:6144\n" \
"v_mfma_f32_16x16x32_bf16 v[56:59], a[112:115], a[88:91], v[56:59]\n" \
"buffer_load_dwordx4 v219, s[36:39], s82 offen offset:3072 lds\n" \
"v_mfma_f32_16x16x32_bf16 v[56:59], a[116:119], a[92:95], v[56:59]\n" \
"s_mov_b32 s72, 2\n" \
"s_nop 0\n" \
"Lo0_%=:\n" \
"v_mfma_f32_16x16x32_bf16 v[0:3], a[96:99], a[64:67], v[0:3]\n" \
"ds_read_b128 a[88:91], v213 offset:4352\n" \
"v_mfma_f32_16x16x32_bf16 v[0:3], a[100:103], a[68:71], v[0:3]\n" \
"s_waitcnt lgkmcnt(2)\n" \
"s_xor_b32 s82, s48, s49\n" \
"ds_read_b128 a[92:95], v213 offset:6400\n" \
"v_mfma_f32_16x16x32_bf16 v[4:7], a[104:107], a[64:67], v[4:7]\n" \
"s_cmp_lt_u32 s48, s50\n" \
"s_cselect_b32 s82, s82, s48\n" \
"ds_read_b64_tr_b16 a[112:113], v214 offset:16896\n" \
"v_mfma_f32_16x16x32_bf16 v[4:7], a[108:111], a[68:71], v[4:7]\n" \
"s_add_u32 s81, s82, s46\n" \
"ds_read_b64_tr_b16 a[114:115], v214 offset:18944\n" \
"v_mfma_f32_16x16x32_bf16 v[20:23], a[104:107], a[72:75], v[20:23]\n" \
"s_add_u32 m0, s86, s79\n" \
"ds_read_b64_tr_b16 a[116:117], v214 offset:25088\n" \
"v_mfma_f32_16x16x32_bf16 v[20:23], a[108:111], a[76:79], v[20:23]\n" \
"buffer_load_dwordx4 v234, s[56:59], s81 offen offset:0 sc1 lds\n" \
"v_mfma_f32_16x16x32_bf16 v[16:19], a[96:99], a[72:75], v[16:19]\n" \
"s_sub_u32 s85, s85, 32768\n" \
"s_cselect_b32 s85, 131072, s85\n" \
"ds_read_b64_tr_b16 a[118:119], v214 offset:27136\n" \
"v_mfma_f32_16x16x32_bf16 v[16:19], a[100:103], a[76:79], v[16:19]\n" \
"s_waitcnt lgkmcnt(3)\n" \
"s_lshl_b32 s82, s82, 5\n" \
"ds_read_b64_tr_b16 a[120:121], v215 offset:16896\n" \
"v_mfma_f32_16x16x32_bf16 v[32:35], a[96:99], a[80:83], v[32:35]\n" \
"s_add_u32 s82, s82, s47\n" \
"ds_read_b64_tr_b16 a[122:123], v215 offset:18944\n" \
"v_mfma_f32_16x16x32_bf16 v[32:35], a[100:103], a[84:87], v[32:35]\n" \
"buffer_load_dwordx4 v234, s[56:59], s81 offen offset:1024 sc1 lds\n" \
"v_mfma_f32_16x16x32_bf16 v[36:39], a[104:107], a[80:83], v[36:39]\n" \
"ds_read_b64_tr_b16 a[124:125], v215 offset:25088\n" \
"v_add_u32 v213, s85, v211\n" \
"v_mfma_f32_16x16x32_bf16 v[36:39], a[108:111], a[84:87], v[36:39]\n" \
"buffer_load_dwordx4 v234, s[56:59], s81 offen offset:2048 sc1 lds\n" \
"v_mfma_f32_16x16x32_bf16 v[52:55], a[104:107], a[88:91], v[52:55]\n" \
"ds_read_b64_tr_b16 a[126:127], v215 offset:27136\n" \
"v_mfma_f32_16x16x32_bf16 v[52:55], a[108:111], a[92:95], v[52:55]\n" \
"buffer_load_dwordx4 v234, s[56:59], s81 offen offset:3072 sc1 lds\n" \
"v_mfma_f32_16x16x32_bf16 v[48:51], a[96:99], a[88:91], v[48:51]\n" \
"v_add_u32 v214, s85, v212\n" \
"v_xor_b32 v215, 32, v214\n" \
"v_mfma_f32_16x16x32_bf16 v[48:51], a[100:103], a[92:95], v[48:51]\n" \
"s_waitcnt vmcnt(20) lgkmcnt(0)\n" \
"s_barrier\n" \
"v_mfma_f32_16x16x32_bf16 v[8:11], a[112:115], a[64:67], v[8:11]\n" \
"s_add_u32 m0, m0, 0x4000\n" \
"ds_read_b64_tr_b16 a[96:97], v214 offset:16384\n" \
"ds_read_b64_tr_b16 a[98:99], v214 offset:18432\n" \
"v_mfma_f32_16x16x32_bf16 v[8:11], a[116:119], a[68:71], v[8:11]\n" \
"buffer_load_dwordx4 v216, s[36:39], s82 offen offset:0 lds\n" \
"v_mfma_f32_16x16x32_bf16 v[12:15], a[120:123], a[64:67], v[12:15]\n" \
"ds_read_b64_tr_b16 a[100:101], v214 offset:24576\n" \
"ds_read_b64_tr_b16 a[102:103], v214 offset:26624\n" \
"v_mfma_f32_16x16x32_bf16 v[12:15], a[124:127], a[68:71], v[12:15]\n" \
"ds_read_b128 a[64:67], v213 offset:0\n" \
"v_mfma_f32_16x16x32_bf16 v[28:31], a[120:123], a[72:75], v[28:31]\n" \
"s_add_u32 s48, s48, 0x1000\n" \
"ds_read_b128 a[68:71], v213 offset:2048\n" \
"v_mfma_f32_16x16x32_bf16 v[28:31], a[124:127], a[76:79], v[28:31]\n" \
"buffer_load_dwordx4 v217, s[36:39], s82 offen offset:1024 lds\n" \
"v_mfma_f32_16x16x32_bf16 v[24:27], a[112:115], a[72:75], v[24:27]\n" \
"ds_read_b64_tr_b16 a[104:105], v215 offset:16384\n" \
"ds_read_b64_tr_b16 a[106:107], v215 offset:18432\n" \
"v_mfma_f32_16x16x32_bf16 v[24:27], a[116:119], a[76:79], v[24:27]\n" \
"ds_read_b128 a[72:75], v213 offset:256\n" \
"v_mfma_f32_16x16x32_bf16 v[40:43], a[112:115], a[80:83], v[40:43]\n" \
"s_sub_u32 s86, s86, 32768\n" \
"s_cselect_b32 s86, 131072, s86\n" \
"ds_read_b128 a[76:79], v213 offset:2304\n" \
"v_mfma_f32_16x16x32_bf16 v[40:43], a[116:119], a[84:87], v[40:43]\n" \
"buffer_load_dwordx4 v218, s[36:39], s82 offen offset:2048 lds\n" \
"v_mfma_f32_16x16x32_bf16 v[44:47], a[120:123], a[80:83], v[44:47]\n" \
"ds_read_b64_tr_b16 a[108:109], v215 offset:24576\n" \
"ds_read_b64_tr_b16 a[110:111], v215 offset:26624\n" \
"v_mfma_f32_16x16x32_bf16 v[44:47], a[124:127], a[84:87], v[44:47]\n" \
"buffer_load_dwordx4 v219, s[36:39], s82 offen offset:3072 lds\n" \
"v_mfma_f32_16x16x32_bf16 v[60:63], a[120:123], a[88:91], v[60:63]\n" \
"ds_read_b128 a[80:83], v213 offset:4096\n" \
"s_waitcnt lgkmcnt(4)\n" \
"v_mfma_f32_16x16x32_bf16 v[60:63], a[124:127], a[92:95], v[60:63]\n" \
"s_sub_u32 s72, s72, 1\n" \
"ds_read_b128 a[84:87], v213 offset:6144\n" \
"v_mfma_f32_16x16x32_bf16 v[56:59], a[112:115], a[88:91], v[56:59]\n" \
"v_mfma_f32_16x16x32_bf16 v[56:59], a[116:119], a[92:95], v[56:59]\n" \
"s_cbranch_scc0 Lo0_%=\n" \
"s_setpc_b64 s[44:45]\n" \
"Lor_%=:\n" \
"buffer_store_dword v230, v231, s[56:59], s12 offen sc1\n" \
"s_mov_b32 s72, s10\n" \
"s_call_b64 s[44:45], Lo0_%=\n" \
"buffer_load_dword v220, v221, s[56:59], s67 offen sc1\n" \
"s_mov_b32 s72, 1\n" \
"s_call_b64 s[44:45], Lo0_%=\n" \
"s_waitcnt vmcnt(16)\n" \
"v_cmp_eq_u32 vcc, s60, v220\n" \
"s_nop 1\n" \
"s_andn2_b32 s70, s74, vcc_lo\n" \
"s_cbranch_scc1 Lpl6_%=\n" \
"Lpr6_%=:\n" \
"s_cmp_eq_u32 s90, 0\n" \
"s_cselect_b32 s46, s46, s83\n" \
"s_cselect_b32 s47, s47, s84\n" \
"s_cselect_b32 s48, s48, 0\n" \
"s_cselect_b64 s[36:37], s[36:37], s[40:41]\n" \
"s_cselect_b32 s38, s38, s42\n" \
"s_mov_b32 s72, 3\n" \
"s_call_b64 s[44:45], Lo0_%=\n" \
"s_cmp_eq_u32 s90, 0\n" \
"s_cbranch_scc0 Lrole1_%=\n" \
"s_mov_b32 s72, s11\n" \
"s_call_b64 s[44:45], Lo0_%=\n" \
"buffer_load_dword v220, v241, s[56:59], s64 offen sc1\n" \
"s_mov_b32 s72, 1\n" \
"s_call_b64 s[44:45], Lo0_%=\n" \
"v_mfma_f32_16x16x32_bf16 v[0:3], a[96:99], a[64:67], v[0:3]\n" \
"s_waitcnt lgkmcnt(2)\n" \
"ds_read_b128 a[88:91], v213 offset:4352\n" \
"v_mov_b32 v74, 0x3f80\n" \
"v_mfma_f32_16x16x32_bf16 v[0:3], a[100:103], a[68:71], v[0:3]\n" \
"s_add_u32 s48, s86, 32768\n" \
"s_cmp_eq_u32 s48, 163840\n" \
"s_cselect_b32 s48, 0, s48\n" \
"ds_read_b128 a[92:95], v213 offset:6400\n" \
"v_mfma_f32_16x16x32_bf16 v[4:7], a[104:107], a[64:67], v[4:7]\n" \
"ds_read_b64_tr_b16 a[112:113], v214 offset:16896\n" \
"v_mov_b32 v64, 0\n" \
"v_mfma_f32_16x16x32_bf16 v[4:7], a[108:111], a[68:71], v[4:7]\n" \
"s_add_u32 s49, s48, 32768\n" \
"s_cmp_eq_u32 s49, 163840\n" \
"s_cselect_b32 s49, 0, s49\n" \
"ds_read_b64_tr_b16 a[114:115], v214 offset:18944\n" \
"v_mfma_f32_16x16x32_bf16 v[20:23], a[104:107], a[72:75], v[20:23]\n" \
"ds_read_b64_tr_b16 a[116:117], v214 offset:25088\n" \
"v_add_u32 v224, s49, v211\n" \
"v_mfma_f32_16x16x32_bf16 v[20:23], a[108:111], a[76:79], v[20:23]\n" \
"v_add_u32 v225, s49, v212\n" \
"v_xor_b32 v226, 32, v225\n" \
"v_mfma_f32_16x16x32_bf16 v[16:19], a[96:99], a[72:75], v[16:19]\n" \
"s_sub_u32 s85, s85, 32768\n" \
"s_cselect_b32 s85, 131072, s85\n" \
"ds_read_b64_tr_b16 a[118:119], v214 offset:27136\n" \
"v_mfma_f32_16x16x32_bf16 v[16:19], a[100:103], a[76:79], v[16:19]\n" \
"s_waitcnt lgkmcnt(3)\n" \
"ds_read_b64_tr_b16 a[120:121], v215 offset:16896\n" \
"v_add_u32 v227, s48, v211\n" \
"v_mfma_f32_16x16x32_bf16 v[32:35], a[96:99], a[80:83], v[32:35]\n" \
"ds_read_b64_tr_b16 a[122:123], v215 offset:18944\n" \
"v_add_u32 v228, s48, v212\n" \
"v_mfma_f32_16x16x32_bf16 v[32:35], a[100:103], a[84:87], v[32:35]\n" \
"v_xor_b32 v229, 32, v228\n" \
"v_mov_b32 v65, 0\n" \
"v_mfma_f32_16x16x32_bf16 v[36:39], a[104:107], a[80:83], v[36:39]\n" \
"ds_read_b64_tr_b16 a[124:125], v215 offset:25088\n" \
"v_add_u32 v213, s85, v211\n" \
"v_mfma_f32_16x16x32_bf16 v[36:39], a[108:111], a[84:87], v[36:39]\n" \
"v_mov_b32 v68, 0\n" \
"v_mov_b32 v69, 0\n" \
"v_mfma_f32_16x16x32_bf16 v[52:55], a[104:107], a[88:91], v[52:55]\n" \
"ds_read_b64_tr_b16 a[126:127], v215 offset:27136\n" \
"v_lshrrev_b32 v72, 8, v234\n" \
"v_mfma_f32_16x16x32_bf16 v[52:55], a[108:111], a[92:95], v[52:55]\n" \
"v_bfe_u32 v73, v234, 6, 2\n" \
"v_cmp_eq_u32 s[16:17], v72, v73\n" \
"v_mfma_f32_16x16x32_bf16 v[48:51], a[96:99], a[88:91], v[48:51]\n" \
"v_add_u32 v214, s85, v212\n" \
"v_xor_b32 v215, 32, v214\n" \
"v_mfma_f32_16x16x32_bf16 v[48:51], a[100:103], a[92:95], v[48:51]\n" \
"s_waitcnt vmcnt(8) lgkmcnt(0)\n" \
"s_barrier\n" \
"s_waitcnt vmcnt(16)\n" \
"v_cmp_eq_u32 vcc, s60, v220\n" \
"s_nop 1\n" \
"s_andn2_b32 s70, s69, vcc_lo\n" \
"s_cbranch_scc1 Lpl7_%=\n" \
"Lpr7_%=:\n" \
"v_mfma_f32_16x16x32_bf16 v[8:11], a[112:115], a[64:67], v[8:11]\n" \
"s_add_u32 s71, s87, 0\n" \
"ds_read_b128 a[0:3], v213 offset:0\n" \
"ds_read_b128 a[4:7], v213 offset:2048\n" \
"v_mfma_f32_16x16x32_bf16 v[8:11], a[116:119], a[68:71], v[8:11]\n" \
"buffer_load_dwordx4 v[176:179], v234, s[56:59], s71 offen offset:0 sc1\n" \
"v_mfma_f32_16x16x32_bf16 v[12:15], a[120:123], a[64:67], v[12:15]\n" \
"ds_read_b64_tr_b16 a[128:129], v214 offset:16384\n" \
"ds_read_b64_tr_b16 a[130:131], v214 offset:18432\n" \
"v_mfma_f32_16x16x32_bf16 v[12:15], a[124:127], a[68:71], v[12:15]\n" \
"buffer_load_dwordx4 v[180:183], v234, s[56:59], s71 offen offset:1024 sc1\n" \
"v_mfma_f32_16x16x32_bf16 v[28:31], a[120:123], a[72:75], v[28:31]\n" \
"ds_read_b64_tr_b16 a[132:133], v214 offset:24576\n" \
"ds_read_b64_tr_b16 a[134:135], v214 offset:26624\n" \
"v_mfma_f32_16x16x32_bf16 v[28:31], a[124:127], a[76:79], v[28:31]\n" \
"buffer_load_dwordx4 v[184:187], v234, s[56:59], s71 offen offset:2048 sc1\n" \
"v_mfma_f32_16x16x32_bf16 v[24:27], a[112:115], a[72:75], v[24:27]\n" \
"ds_read_b64_tr_b16 a[136:137], v215 offset:16384\n" \
"ds_read_b64_tr_b16 a[138:139], v215 offset:18432\n" \
"v_mfma_f32_16x16x32_bf16 v[24:27], a[116:119], a[76:79], v[24:27]\n" \
"buffer_load_dwordx4 v[188:191], v234, s[56:59], s71 offen offset:3072 sc1\n" \
"v_mfma_f32_16x16x32_bf16 v[40:43], a[112:115], a[80:83], v[40:43]\n" \
"ds_read_b64_tr_b16 a[140:141], v215 offset:24576\n" \
"ds_read_b64_tr_b16 a[142:143], v215 offset:26624\n" \
"v_mfma_f32_16x16x32_bf16 v[40:43], a[116:119], a[84:87], v[40:43]\n" \
"ds_read_b64_tr_b16 a[144:145], v214 offset:16896\n" \
"ds_read_b64_tr_b16 a[146:147], v214 offset:18944\n" \
"v_mfma_f32_16x16x32_bf16 v[44:47], a[120:123], a[80:83], v[44:47]\n" \
"ds_read_b64_tr_b16 a[148:149], v214 offset:25088\n" \
"ds_read_b64_tr_b16 a[150:151], v214 offset:27136\n" \
"v_mfma_f32_16x16x32_bf16 v[44:47], a[124:127], a[84:87], v[44:47]\n" \
"ds_read_b64_tr_b16 a[152:153], v215 offset:16896\n" \
"ds_read_b64_tr_b16 a[154:155], v215 offset:18944\n" \
"v_mfma_f32_16x16x32_bf16 v[60:63], a[120:123], a[88:91], v[60:63]\n" \
"ds_read_b64_tr_b16 a[156:157], v215 offset:25088\n" \
"ds_read_b64_tr_b16 a[158:159], v215 offset:27136\n" \
"v_mfma_f32_16x16x32_bf16 v[60:63], a[124:127], a[92:95], v[60:63]\n" \
"ds_read_b128 a[24:27], v213 offset:256\n" \
"ds_read_b128 a[28:31], v213 offset:2304\n" \
"v_mfma_f32_16x16x32_bf16 v[56:59], a[112:115], a[88:91], v[56:59]\n" \
"ds_read_b128 a[8:11], v224 offset:0\n" \
"ds_read_b128 a[12:15], v224 offset:2048\n" \
"v_mfma_f32_16x16x32_bf16 v[56:59], a[116:119], a[92:95], v[56:59]\n" \
"s_waitcnt lgkmcnt(8)\n" \
"ds_read_b64_tr_b16 a[160:161], v225 offset:16384\n" \
"ds_read_b64_tr_b16 a[162:163], v225 offset:18432\n" \
"v_mfma_f32_16x16x32_bf16 v[0:3], a[128:131], a[0:3], v[0:3]\n" \
"ds_read_b64_tr_b16 a[164:165], v225 offset:24576\n" \
"ds_read_b64_tr_b16 a[166:167], v225 offset:26624\n" \
"v_mfma_f32_16x16x32_bf16 v[0:3], a[132:135], a[4:7], v[0:3]\n" \
"ds_read_b64_tr_b16 a[168:169], v226 offset:16384\n" \
"ds_read_b64_tr_b16 a[170:171], v226 offset:18432\n" \
"v_mfma_f32_16x16x32_bf16 v[4:7], a[136:139], a[0:3], v[4:7]\n" \
"s_mov_b32 s70, 0x20000000\n" \
"ds_read_b64_tr_b16 a[172:173], v226 offset:24576\n" \
"ds_read_b64_tr_b16 a[174:175], v226 offset:26624\n" \
"v_mfma_f32_16x16x32_bf16 v[4:7], a[140:143], a[4:7], v[4:7]\n" \
"s_cmp_eq_u32 s64, 65616896\n" \
"s_cselect_b32 s70, 65593344, s70\n" \
"ds_read_b64_tr_b16 a[176:177], v225 offset:16896\n" \
"ds_read_b64_tr_b16 a[178:179], v225 offset:18944\n" \
"buffer_store_dword v230, v231, s[56:59], s70 offen sc1\n" \
"v_mfma_f32_16x16x32_bf16 v[8:11], a[144:147], a[0:3], v[8:11]\n" \
"s_waitcnt lgkmcnt(8)\n" \
"ds_read_b64_tr_b16 a[180:181], v225 offset:25088\n" \
"ds_read_b64_tr_b16 a[182:183], v225 offset:27136\n" \
"v_mfma_f32_16x16x32_bf16 v[8:11], a[148:151], a[4:7], v[8:11]\n" \
"s_sub_u32 s68, s86, 32768\n" \
"s_cselect_b32 s68, 131072, s68\n" \
"ds_read_b64_tr_b16 a[184:185], v226 offset:16896\n" \
"ds_read_b64_tr_b16 a[186:187], v226 offset:18944\n" \
"v_mfma_f32_16x16x32_bf16 v[12:15], a[152:155], a[0:3], v[12:15]\n" \
"ds_read_b64_tr_b16 a[188:189], v226 offset:25088\n" \
"ds_read_b64_tr_b16 a[190:191], v226 offset:27136\n" \
"v_mfma_f32_16x16x32_bf16 v[12:15], a[156:159], a[4:7], v[12:15]\n" \
"s_cmp_lt_u32 s61, 2\n" \
"s_cselect_b32 s68, s86, s68\n" \
"v_and_b32 v72, 16, v234\n" \
"v_lshlrev_b32 v72, v72, v74\n" \
"v_mfma_f32_16x16x32_bf16 v[28:31], a[152:155], a[24:27], v[28:31]\n" \
"s_and_b32 s50, s61, 1\n" \
"v_cndmask_b32 v72, 0, v72, s[16:17]\n" \
"v_bfe_i32 v73, v234, 5, 1\n" \
"v_mfma_f32_16x16x32_bf16 v[28:31], a[156:159], a[28:31], v[28:31]\n" \
"s_mul_i32 s50, s50, 9216\n" \
"v_and_b32 v67, v72, v73\n" \
"v_xor_b32 v66, v72, v67\n" \
"v_mfma_f32_16x16x32_bf16 v[24:27], a[144:147], a[24:27], v[24:27]\n" \
"s_add_u32 s68, s68, s50\n" \
"s_add_u32 s68, s68, %3\n" \
"v_lshrrev_b32 v132, 4, v234\n" \
"v_mfma_f32_16x16x32_bf16 v[24:27], a[148:151], a[28:31], v[24:27]\n" \
"v_and_b32 v133, 15, v132\n" \
"v_mul_u32_u24 v128, 144, v133\n" \
"v_lshrrev_b32 v133, 4, v132\n" \
"v_mfma_f32_16x16x32_bf16 v[20:23], a[136:139], a[24:27], v[20:23]\n" \
"s_waitcnt vmcnt(5)\n" \
"s_barrier\n" \
"v_mfma_f32_16x16x32_bf16 v[20:23], a[140:143], a[28:31], v[20:23]\n" \
"s_add_u32 s71, s87, 4096\n" \
"ds_read_b128 a[32:35], v224 offset:256\n" \
"ds_read_b128 a[36:39], v224 offset:2304\n" \
"v_mfma_f32_16x16x32_bf16 v[16:19], a[128:131], a[24:27], v[16:19]\n" \
"buffer_load_dwordx4 v[192:195], v234, s[56:59], s71 offen offset:0 sc1\n" \
"v_mfma_f32_16x16x32_bf16 v[16:19], a[132:135], a[28:31], v[16:19]\n" \
"s_waitcnt lgkmcnt(2)\n" \
"ds_read_b128 a[16:19], v227 offset:0\n" \
"ds_read_b128 a[20:23], v227 offset:2048\n" \
"v_mfma_f32_16x16x32_bf16 v[0:3], a[160:163], a[8:11], v[0:3]\n" \
"ds_read_b64_tr_b16 a[192:193], v228 offset:16384\n" \
"ds_read_b64_tr_b16 a[194:195], v228 offset:18432\n" \
"v_mfma_f32_16x16x32_bf16 v[0:3], a[164:167], a[12:15], v[0:3]\n" \
"buffer_load_dwordx4 v[196:199], v234, s[56:59], s71 offen offset:1024 sc1\n" \
"v_mfma_f32_16x16x32_bf16 v[4:7], a[168:171], a[8:11], v[4:7]\n" \
"ds_read_b64_tr_b16 a[196:197], v228 offset:24576\n" \
"ds_read_b64_tr_b16 a[198:199], v228 offset:26624\n" \
"v_mfma_f32_16x16x32_bf16 v[4:7], a[172:175], a[12:15], v[4:7]\n" \
"ds_read_b64_tr_b16 a[200:201], v229 offset:16384\n" \
"ds_read_b64_tr_b16 a[202:203], v229 offset:18432\n" \
"v_mfma_f32_16x16x32_bf16 v[8:11], a[176:179], a[8:11], v[8:11]\n" \
"buffer_load_dwordx4 v[200:203], v234, s[56:59], s71 offen offset:2048 sc1\n" \
"v_mfma_f32_16x16x32_bf16 v[8:11], a[180:183], a[12:15], v[8:11]\n" \
"ds_read_b64_tr_b16 a[204:205], v229 offset:24576\n" \
"ds_read_b64_tr_b16 a[206:207], v229 offset:26624\n" \
"v_lshl_add_u32 v128, v133, 3, v128\n" \
"v_mfma_f32_16x16x32_bf16 v[12:15], a[184:187], a[8:11], v[12:15]\n" \
"s_waitcnt lgkmcnt(6)\n" \
"ds_read_b64_tr_b16 a[208:209], v228 offset:16896\n" \
"ds_read_b64_tr_b16 a[210:211], v228 offset:18944\n" \
"v_mfma_f32_16x16x32_bf16 v[12:15], a[188:191], a[12:15], v[12:15]\n" \
"buffer_load_dwordx4 v[204:207], v234, s[56:59], s71 offen offset:3072 sc1\n" \
"v_mfma_f32_16x16x32_bf16 v[28:31], a[184:187], a[32:35], v[28:31]\n" \
"ds_read_b64_tr_b16 a[212:213], v228 offset:25088\n" \
"ds_read_b64_tr_b16 a[214:215], v228 offset:27136\n" \
"v_mfma_f32_16x16x32_bf16 v[28:31], a[188:191], a[36:39], v[28:31]\n" \
"ds_read_b64_tr_b16 a[216:217], v229 offset:16896\n" \
"ds_read_b64_tr_b16 a[218:219], v229 offset:18944\n" \
"v_mfma_f32_16x16x32_bf16 v[24:27], a[176:179], a[32:35], v[24:27]\n" \
"ds_read_b64_tr_b16 a[220:221], v229 offset:25088\n" \
"ds_read_b64_tr_b16 a[222:223], v229 offset:27136\n" \
"v_mfma_f32_16x16x32_bf16 v[24:27], a[180:183], a[36:39], v[24:27]\n" \
"ds_read_b128 a[40:43], v227 offset:256\n" \
"ds_read_b128 a[44:47], v227 offset:2304\n" \
"v_mfma_f32_16x16x32_bf16 v[20:23], a[168:171], a[32:35], v[20:23]\n" \
"ds_read_b128 a[248:251], v213 offset:4096\n" \
"ds_read_b128 a[252:255], v213 offset:6144\n" \
"v_mfma_f32_16x16x32_bf16 v[20:23], a[172:175], a[36:39], v[20:23]\n" \
"ds_read_b128 a[48:51], v224 offset:4096\n" \
"ds_read_b128 a[52:55], v224 offset:6144\n" \
"v_mfma_f32_16x16x32_bf16 v[16:19], a[160:163], a[32:35], v[16:19]\n" \
"ds_read_b128 a[56:59], v227 offset:4096\n" \
"ds_read_b128 a[60:63], v227 offset:6144\n" \
"v_mfma_f32_16x16x32_bf16 v[16:19], a[164:167], a[36:39], v[16:19]\n" \
"s_waitcnt lgkmcnt(8)\n" \
"ds_read_b128 a[224:227], v213 offset:4352\n" \
"ds_read_b128 a[228:231], v213 offset:6400\n" \
"v_add_u32 v128, s68, v128\n" \
"v_mfma_f32_16x16x32_bf16 v[0:3], a[192:195], a[16:19], v[0:3]\n" \
"s_waitcnt vmcnt(7)\n" \
"ds_read_b128 a[232:235], v224 offset:4352\n" \
"ds_read_b128 a[236:239], v224 offset:6400\n" \
"v_mfma_f32_16x16x32_bf16 v[0:3], a[196:199], a[20:23], v[0:3]\n" \
"ds_read_b128 a[240:243], v227 offset:4352\n" \
"ds_read_b128 a[244:247], v227 offset:6400\n" \
"v_mfma_f32_16x16x32_bf16 v[0:3], v[66:69], v[176:179], v[0:3]\n" \
"v_lshrrev_b32 v133, 3, v132\n" \
"v_and_b32 v134, 7, v132\n" \
"v_mfma_f32_16x16x32_bf16 v[4:7], a[200:203], a[16:19], v[4:7]\n" \
"v_mul_u32_u24 v129, 144, v133\n" \
"v_lshl_add_u32 v129, v134, 4, v129\n" \
"v_mfma_f32_16x16x32_bf16 v[4:7], a[204:207], a[20:23], v[4:7]\n" \
"v_add_u32 v129, s68, v129\n" \
"v_lshlrev_b32 v134, 4, v134\n" \
"v_mfma_f32_16x16x32_bf16 v[4:7], v[64:67], v[176:179], v[4:7]\n" \
"v_lshl_add_u32 v130, v133, 12, v134\n" \
"v_mfma_f32_16x16x32_bf16 v[8:11], a[208:211], a[16:19], v[8:11]\n" \
"v_mul_f32 v0, 4.0, v0\n" \
"v_mul_f32 v1, 4.0, v1\n" \
"v_mul_f32 v2, 4.0, v2\n" \
"v_mul_f32 v3, 4.0, v3\n" \
"v_cvt_pk_bf16_f32 v136, v0, v1\n" \
"v_cvt_pk_bf16_f32 v137, v2, v3\n" \
"v_mfma_f32_16x16x32_bf16 v[8:11], a[212:215], a[20:23], v[8:11]\n" \
"v_mfma_f32_16x16x32_bf16 v[8:11], v[66:69], v[180:183], v[8:11]\n" \
"ds_write_b64 v128, v[136:137] offset:0\n" \
"v_mfma_f32_16x16x32_bf16 v[12:15], a[216:219], a[16:19], v[12:15]\n" \
"v_mul_f32 v4, 4.0, v4\n" \
"v_mul_f32 v5, 4.0, v5\n" \
"v_mfma_f32_16x16x32_bf16 v[12:15], a[220:223], a[20:23], v[12:15]\n" \
"v_mul_f32 v6, 4.0, v6\n" \
"v_mul_f32 v7, 4.0, v7\n" \
"v_mfma_f32_16x16x32_bf16 v[12:15], v[64:67], v[180:183], v[12:15]\n" \
"s_waitcnt lgkmcnt(1)\n" \
"v_cvt_pk_bf16_f32 v138, v4, v5\n" \
"v_cvt_pk_bf16_f32 v139, v6, v7\n" \
"v_mfma_f32_16x16x32_bf16 v[16:19], a[192:195], a[40:43], v[16:19]\n" \
"s_waitcnt vmcnt(5)\n" \
"v_mfma_f32_16x16x32_bf16 v[16:19], a[196:199], a[44:47], v[16:19]\n" \
"v_mfma_f32_16x16x32_bf16 v[16:19], v[66:69], v[184:187], v[16:19]\n" \
"ds_write_b64 v128, v[138:139] offset:32\n" \
"v_mul_f32 v8, 4.0, v8\n" \
"v_mfma_f32_16x16x32_bf16 v[20:23], a[200:203], a[40:43], v[20:23]\n" \
"v_mul_f32 v9, 4.0, v9\n" \
"v_mul_f32 v10, 4.0, v10\n" \
"v_mfma_f32_16x16x32_bf16 v[20:23], a[204:207], a[44:47], v[20:23]\n" \
"v_mul_f32 v11, 4.0, v11\n" \
"v_cvt_pk_bf16_f32 v136, v8, v9\n" \
"v_mfma_f32_16x16x32_bf16 v[20:23], v[64:67], v[184:187], v[20:23]\n" \
"v_cvt_pk_bf16_f32 v137, v10, v11\n" \
"v_mfma_f32_16x16x32_bf16 v[24:27], a[208:211], a[40:43], v[24:27]\n" \
"v_mfma_f32_16x16x32_bf16 v[24:27], a[212:215], a[44:47], v[24:27]\n" \
"v_mfma_f32_16x16x32_bf16 v[24:27], v[66:69], v[188:191], v[24:27]\n" \
"ds_write_b64 v128, v[136:137] offset:64\n" \
"v_mul_f32 v12, 4.0, v12\n" \
"v_mfma_f32_16x16x32_bf16 v[28:31], a[216:219], a[40:43], v[28:31]\n" \
"v_mul_f32 v13, 4.0, v13\n" \
"v_mul_f32 v14, 4.0, v14\n" \
"v_mfma_f32_16x16x32_bf16 v[28:31], a[220:223], a[44:47], v[28:31]\n" \
"v_mul_f32 v15, 4.0, v15\n" \
"v_cvt_pk_bf16_f32 v138, v12, v13\n" \
"v_mfma_f32_16x16x32_bf16 v[28:31], v[64:67], v[188:191], v[28:31]\n" \
"v_cvt_pk_bf16_f32 v139, v14, v15\n" \
"v_mfma_f32_16x16x32_bf16 v[32:35], a[128:131], a[248:251], v[32:35]\n" \
"ds_write_b64 v128, v[138:139] offset:96\n" \
"v_mfma_f32_16x16x32_bf16 v[32:35], a[132:135], a[252:255], v[32:35]\n" \
"ds_read_b128 v[140:143], v129 offset:0\n" \
"ds_read_b128 v[144:147], v129 offset:1152\n" \
"v_mfma_f32_16x16x32_bf16 v[32:35], a[160:163], a[48:51], v[32:35]\n" \
"s_waitcnt vmcnt(2)\n" \
"v_mul_f32 v16, 4.0, v16\n" \
"v_mul_f32 v17, 4.0, v17\n" \
"v_mfma_f32_16x16x32_bf16 v[32:35], a[164:167], a[52:55], v[32:35]\n" \
"v_mul_f32 v18, 4.0, v18\n" \
"v_mul_f32 v19, 4.0, v19\n" \
"v_mfma_f32_16x16x32_bf16 v[32:35], v[66:69], v[192:195], v[32:35]\n" \
"s_add_u32 s70, s88, 0\n" \
"s_waitcnt lgkmcnt(1)\n" \
"v_cvt_pk_bf16_f32 v136, v16, v17\n" \
"v_mfma_f32_16x16x32_bf16 v[32:35], a[192:195], a[56:59], v[32:35]\n" \
"buffer_store_dwordx4 v[140:143], v130, s[52:55], s70 offen sc1\n" \
"v_cvt_pk_bf16_f32 v137, v18, v19\n" \
"v_mfma_f32_16x16x32_bf16 v[32:35], a[196:199], a[60:63], v[32:35]\n" \
"ds_write_b64 v128, v[136:137] offset:2304\n" \
"v_mfma_f32_16x16x32_bf16 v[36:39], a[204:207], a[60:63], v[36:39]\n" \
"s_add_u32 s70, s88, 32768\n" \
"s_waitcnt lgkmcnt(1)\n" \
"v_mul_f32 v20, 4.0, v20\n" \
"v_mfma_f32_16x16x32_bf16 v[36:39], a[200:203], a[56:59], v[36:39]\n" \
"buffer_store_dwordx4 v[144:147], v130, s[52:55], s70 offen sc1\n" \
"v_mfma_f32_16x16x32_bf16 v[36:39], a[172:175], a[52:55], v[36:39]\n" \
"v_mul_f32 v21, 4.0, v21\n" \
"v_mul_f32 v22, 4.0, v22\n" \
"v_mfma_f32_16x16x32_bf16 v[36:39], a[168:171], a[48:51], v[36:39]\n" \
"v_mul_f32 v23, 4.0, v23\n" \
"v_cvt_pk_bf16_f32 v138, v20, v21\n" \
"v_mfma_f32_16x16x32_bf16 v[36:39], v[64:67], v[192:195], v[36:39]\n" \
"v_cvt_pk_bf16_f32 v139, v22, v23\n" \
"v_mfma_f32_16x16x32_bf16 v[36:39], a[140:143], a[252:255], v[36:39]\n" \
"v_mfma_f32_16x16x32_bf16 v[36:39], a[136:139], a[248:251], v[36:39]\n" \
"ds_write_b64 v128, v[138:139] offset:2336\n" \
"v_mul_f32 v24, 4.0, v24\n" \
"v_mul_f32 v25, 4.0, v25\n" \
"v_mfma_f32_16x16x32_bf16 v[40:43], a[144:147], a[248:251], v[40:43]\n" \
"v_mul_f32 v26, 4.0, v26\n" \
"v_mul_f32 v27, 4.0, v27\n" \
"v_mfma_f32_16x16x32_bf16 v[40:43], a[148:151], a[252:255], v[40:43]\n" \
"v_cvt_pk_bf16_f32 v136, v24, v25\n" \
"v_cvt_pk_bf16_f32 v137, v26, v27\n" \
"v_mfma_f32_16x16x32_bf16 v[40:43], a[176:179], a[48:51], v[40:43]\n" \
"ds_write_b64 v128, v[136:137] offset:2368\n" \
"v_mfma_f32_16x16x32_bf16 v[40:43], a[180:183], a[52:55], v[40:43]\n" \
"v_mul_f32 v28, 4.0, v28\n" \
"v_mul_f32 v29, 4.0, v29\n" \
"v_mfma_f32_16x16x32_bf16 v[40:43], v[66:69], v[196:199], v[40:43]\n" \
"v_mul_f32 v30, 4.0, v30\n" \
"v_mul_f32 v31, 4.0, v31\n" \
"v_mfma_f32_16x16x32_bf16 v[40:43], a[208:211], a[56:59], v[40:43]\n" \
"v_cvt_pk_bf16_f32 v138, v28, v29\n" \
"v_cvt_pk_bf16_f32 v139, v30, v31\n" \
"v_mfma_f32_16x16x32_bf16 v[40:43], a[212:215], a[60:63], v[40:43]\n" \
"ds_write_b64 v128, v[138:139] offset:2400\n" \
"v_mfma_f32_16x16x32_bf16 v[44:47], a[220:223], a[60:63], v[44:47]\n" \
"ds_read_b128 v[148:151], v129 offset:2304\n" \
"ds_read_b128 v[152:155], v129 offset:3456\n" \
"v_mfma_f32_16x16x32_bf16 v[44:47], a[216:219], a[56:59], v[44:47]\n" \
"v_mul_f32 v32, 4.0, v32\n" \
"v_mul_f32 v33, 4.0, v33\n" \
"v_mfma_f32_16x16x32_bf16 v[44:47], a[188:191], a[52:55], v[44:47]\n" \
"v_mul_f32 v34, 4.0, v34\n" \
"v_mul_f32 v35, 4.0, v35\n" \
"v_mfma_f32_16x16x32_bf16 v[44:47], a[184:187], a[48:51], v[44:47]\n" \
"s_add_u32 s70, s88, 65536\n" \
"s_waitcnt lgkmcnt(1)\n" \
"v_cvt_pk_bf16_f32 v136, v32, v33\n" \
"v_mfma_f32_16x16x32_bf16 v[44:47], v[64:67], v[196:199], v[44:47]\n" \
"buffer_store_dwordx4 v[148:151], v130, s[52:55], s70 offen sc1\n" \
"v_cvt_pk_bf16_f32 v137, v34, v35\n" \
"v_mfma_f32_16x16x32_bf16 v[44:47], a[156:159], a[252:255], v[44:47]\n" \
"v_mfma_f32_16x16x32_bf16 v[44:47], a[152:155], a[248:251], v[44:47]\n" \
"s_add_u32 s70, s88, 98304\n" \
"s_waitcnt lgkmcnt(0)\n" \
"ds_write_b64 v128, v[136:137] offset:4608\n" \
"v_mul_f32 v36, 4.0, v36\n" \
"v_mfma_f32_16x16x32_bf16 v[48:51], a[128:131], a[224:227], v[48:51]\n" \
"buffer_store_dwordx4 v[152:155], v130, s[52:55], s70 offen sc1\n" \
"v_mul_f32 v37, 4.0, v37\n" \
"v_mfma_f32_16x16x32_bf16 v[48:51], a[132:135], a[228:231], v[48:51]\n" \
"v_mul_f32 v38, 4.0, v38\n" \
"v_mul_f32 v39, 4.0, v39\n" \
"v_mfma_f32_16x16x32_bf16 v[48:51], a[160:163], a[232:235], v[48:51]\n" \
"s_waitcnt vmcnt(4)\n" \
"v_cvt_pk_bf16_f32 v138, v36, v37\n" \
"v_cvt_pk_bf16_f32 v139, v38, v39\n" \
"v_mfma_f32_16x16x32_bf16 v[48:51], a[164:167], a[236:239], v[48:51]\n" \
"ds_write_b64 v128, v[138:139] offset:4640\n" \
"v_mul_f32 v40, 4.0, v40\n" \
"v_mfma_f32_16x16x32_bf16 v[48:51], v[66:69], v[200:203], v[48:51]\n" \
"v_mul_f32 v41, 4.0, v41\n" \
"v_mul_f32 v42, 4.0, v42\n" \
"Lpg10819_%=:\n" \
"s_cbranch_execz Lcb1_%=\n" \
"v_mfma_f32_16x16x32_bf16 v[48:51], a[192:195], a[240:243], v[48:51]\n" \
"v_mul_f32 v43, 4.0, v43\n" \
"v_cvt_pk_bf16_f32 v136, v40, v41\n" \
"v_mfma_f32_16x16x32_bf16 v[48:51], a[196:199], a[244:247], v[48:51]\n" \
"v_cvt_pk_bf16_f32 v137, v42, v43\n" \
"v_mfma_f32_16x16x32_bf16 v[52:55], a[204:207], a[244:247], v[52:55]\n" \
"ds_write_b64 v128, v[136:137] offset:4672\n" \
"v_mul_f32 v44, 4.0, v44\n" \
"v_mul_f32 v45, 4.0, v45\n" \
"v_mfma_f32_16x16x32_bf16 v[52:55], a[200:203], a[240:243], v[52:55]\n" \
"v_mul_f32 v46, 4.0, v46\n" \
"v_mul_f32 v47, 4.0, v47\n" \
"v_mfma_f32_16x16x32_bf16 v[52:55], a[172:175], a[236:239], v[52:55]\n" \
"v_cvt_pk_bf16_f32 v138, v44, v45\n" \
"v_cvt_pk_bf16_f32 v139, v46, v47\n" \
"v_mfma_f32_16x16x32_bf16 v[52:55], a[168:171], a[232:235], v[52:55]\n" \
"ds_write_b64 v128, v[138:139] offset:4704\n" \
"v_mfma_f32_16x16x32_bf16 v[52:55], v[64:67], v[200:203], v[52:55]\n" \
"ds_read_b128 v[156:159], v129 offset:4608\n" \
"ds_read_b128 v[160:163], v129 offset:5760\n" \
"v_mfma_f32_16x16x32_bf16 v[52:55], a[140:143], a[228:231], v[52:55]\n" \
"v_mul_f32 v48, 4.0, v48\n" \
"v_mul_f32 v49, 4.0, v49\n" \
"v_mfma_f32_16x16x32_bf16 v[52:55], a[136:139], a[224:227], v[52:55]\n" \
"v_mul_f32 v50, 4.0, v50\n" \
"v_mul_f32 v51, 4.0, v51\n" \
"v_mfma_f32_16x16x32_bf16 v[56:59], a[144:147], a[224:227], v[56:59]\n" \
"s_add_u32 s70, s88, 131072\n" \
"s_waitcnt lgkmcnt(1)\n" \
"v_cvt_pk_bf16_f32 v136, v48, v49\n" \
"v_mfma_f32_16x16x32_bf16 v[56:59], a[148:151], a[228:231], v[56:59]\n" \
"buffer_store_dwordx4 v[156:159], v130, s[52:55], s70 offen sc1\n" \
"v_cvt_pk_bf16_f32 v137, v50, v51\n" \
"v_mfma_f32_16x16x32_bf16 v[56:59], a[176:179], a[232:235], v[56:59]\n" \
"ds_write_b64 v128, v[136:137] offset:6912\n" \
"v_mfma_f32_16x16x32_bf16 v[56:59], a[180:183], a[236:239], v[56:59]\n" \
"s_add_u32 s70, s88, 163840\n" \
"s_waitcnt lgkmcnt(1)\n" \
"v_mul_f32 v52, 4.0, v52\n" \
"v_mfma_f32_16x16x32_bf16 v[56:59], v[66:69], v[204:207], v[56:59]\n" \
"buffer_store_dwordx4 v[160:163], v130, s[52:55], s70 offen sc1\n" \
"v_mfma_f32_16x16x32_bf16 v[56:59], a[208:211], a[240:243], v[56:59]\n" \
"v_mul_f32 v53, 4.0, v53\n" \
"v_mul_f32 v54, 4.0, v54\n" \
"v_mfma_f32_16x16x32_bf16 v[56:59], a[212:215], a[244:247], v[56:59]\n" \
"v_mul_f32 v55, 4.0, v55\n" \
"v_cvt_pk_bf16_f32 v138, v52, v53\n" \
"v_cvt_pk_bf16_f32 v139, v54, v55\n" \
"v_mfma_f32_16x16x32_bf16 v[60:63], a[220:223], a[244:247], v[60:63]\n" \
"ds_write_b64 v128, v[138:139] offset:6944\n" \
"v_mfma_f32_16x16x32_bf16 v[60:63], a[216:219], a[240:243], v[60:63]\n" \
"v_mfma_f32_16x16x32_bf16 v[60:63], a[188:191], a[236:239], v[60:63]\n" \
"v_mfma_f32_16x16x32_bf16 v[60:63], a[184:187], a[232:235], v[60:63]\n" \
"v_mul_f32 v56, 4.0, v56\n" \
"v_mul_f32 v57, 4.0, v57\n" \
"v_mfma_f32_16x16x32_bf16 v[60:63], v[64:67], v[204:207], v[60:63]\n" \
"v_mul_f32 v58, 4.0, v58\n" \
"v_mul_f32 v59, 4.0, v59\n" \
"v_mfma_f32_16x16x32_bf16 v[60:63], a[156:159], a[228:231], v[60:63]\n" \
"v_cvt_pk_bf16_f32 v136, v56, v57\n" \
"v_cvt_pk_bf16_f32 v137, v58, v59\n" \
"v_mfma_f32_16x16x32_bf16 v[60:63], a[152:155], a[224:227], v[60:63]\n" \
"ds_write_b64 v128, v[136:137] offset:6976\n" \
"s_nop 7\n" \
"v_mul_f32 v60, 4.0, v60\n" \
"v_mul_f32 v61, 4.0, v61\n" \
"v_mul_f32 v62, 4.0, v62\n" \
"v_mul_f32 v63, 4.0, v63\n" \
"v_cvt_pk_bf16_f32 v138, v60, v61\n" \
"v_cvt_pk_bf16_f32 v139, v62, v63\n" \
"ds_write_b64 v128, v[138:139] offset:7008\n" \
"ds_read_b128 v[164:167], v129 offset:6912\n" \
"ds_read_b128 v[168:171], v129 offset:8064\n" \
"s_add_u32 s70, s88, 196608\n" \
"s_waitcnt lgkmcnt(1)\n" \
"buffer_store_dwordx4 v[164:167], v130, s[52:55], s70 offen sc1\n" \
"s_add_u32 s70, s88, 229376\n" \
"s_waitcnt lgkmcnt(0)\n" \
"buffer_store_dwordx4 v[168:171], v130, s[52:55], s70 offen sc1\n" \
"s_endpgm\n" \
".p2align 12\n" \
"Lrole1_%=:\n" \
"v_mfma_f32_16x16x32_bf16 v[64:67], a[96:99], a[64:67], 0\n" \
"Lpg10903_%=:\n" \
"s_cbranch_execz Lcb1_%=\n" \
"ds_read_b128 a[88:91], v213 offset:4352\n" \
"v_mfma_f32_16x16x32_bf16 v[64:67], a[100:103], a[68:71], v[64:67]\n" \
"s_waitcnt lgkmcnt(2)\n" \
"s_xor_b32 s82, s48, s49\n" \
"ds_read_b128 a[92:95], v213 offset:6400\n" \
"v_mfma_f32_16x16x32_bf16 v[68:71], a[104:107], a[64:67], 0\n" \
"s_cmp_lt_u32 s48, s50\n" \
"s_cselect_b32 s82, s82, s48\n" \
"ds_read_b64_tr_b16 a[112:113], v214 offset:16896\n" \
"v_mfma_f32_16x16x32_bf16 v[68:71], a[108:111], a[68:71], v[68:71]\n" \
"s_add_u32 s81, s82, s46\n" \
"ds_read_b64_tr_b16 a[114:115], v214 offset:18944\n" \
"v_mfma_f32_16x16x32_bf16 v[84:87], a[104:107], a[72:75], 0\n" \
"s_waitcnt lgkmcnt(4)\n" \
"s_add_u32 m0, s86, s79\n" \
"ds_read_b64_tr_b16 a[116:117], v214 offset:25088\n" \
"v_mfma_f32_16x16x32_bf16 v[84:87], a[108:111], a[76:79], v[84:87]\n" \
"buffer_load_dwordx4 v234, s[56:59], s81 offen offset:0 sc1 lds\n" \
"v_mfma_f32_16x16x32_bf16 v[80:83], a[96:99], a[72:75], 0\n" \
"s_sub_u32 s85, s85, 32768\n" \
"s_cselect_b32 s85, 131072, s85\n" \
"ds_read_b64_tr_b16 a[118:119], v214 offset:27136\n" \
"v_mfma_f32_16x16x32_bf16 v[80:83], a[100:103], a[76:79], v[80:83]\n" \
"s_lshl_b32 s82, s82, 5\n" \
"ds_read_b64_tr_b16 a[120:121], v215 offset:16896\n" \
"v_cvt_pk_bf16_f32 v0, v0, v1\n" \
"v_mfma_f32_16x16x32_bf16 v[96:99], a[96:99], a[80:83], 0\n" \
"s_add_u32 s82, s82, s47\n" \
"ds_read_b64_tr_b16 a[122:123], v215 offset:18944\n" \
"v_cvt_pk_bf16_f32 v1, v2, v3\n" \
"v_mfma_f32_16x16x32_bf16 v[96:99], a[100:103], a[84:87], v[96:99]\n" \
"buffer_load_dwordx4 v234, s[56:59], s81 offen offset:1024 sc1 lds\n" \
"v_cvt_pk_bf16_f32 v2, v4, v5\n" \
"v_mfma_f32_16x16x32_bf16 v[100:103], a[104:107], a[80:83], 0\n" \
"s_waitcnt lgkmcnt(3)\n" \
"ds_read_b64_tr_b16 a[124:125], v215 offset:25088\n" \
"v_add_u32 v213, s85, v211\n" \
"v_mfma_f32_16x16x32_bf16 v[100:103], a[108:111], a[84:87], v[100:103]\n" \
"buffer_load_dwordx4 v234, s[56:59], s81 offen offset:2048 sc1 lds\n" \
"v_mfma_f32_16x16x32_bf16 v[116:119], a[104:107], a[88:91], 0\n" \
"ds_read_b64_tr_b16 a[126:127], v215 offset:27136\n" \
"v_cvt_pk_bf16_f32 v3, v6, v7\n" \
"v_mfma_f32_16x16x32_bf16 v[116:119], a[108:111], a[92:95], v[116:119]\n" \
"buffer_load_dwordx4 v234, s[56:59], s81 offen offset:3072 sc1 lds\n" \
"v_cvt_pk_bf16_f32 v4, v8, v9\n" \
"v_mfma_f32_16x16x32_bf16 v[112:115], a[96:99], a[88:91], 0\n" \
"v_add_u32 v214, s85, v212\n" \
"v_xor_b32 v215, 32, v214\n" \
"v_mfma_f32_16x16x32_bf16 v[112:115], a[100:103], a[92:95], v[112:115]\n" \
"v_cvt_pk_bf16_f32 v5, v10, v11\n" \
"s_waitcnt vmcnt(20) lgkmcnt(0)\n" \
"s_barrier\n" \
"v_mfma_f32_16x16x32_bf16 v[72:75], a[112:115], a[64:67], 0\n" \
"s_add_u32 m0, m0, 0x4000\n" \
"ds_read_b64_tr_b16 a[96:97], v214 offset:16384\n" \
"ds_read_b64_tr_b16 a[98:99], v214 offset:18432\n" \
"v_mfma_f32_16x16x32_bf16 v[72:75], a[116:119], a[68:71], v[72:75]\n" \
"buffer_load_dwordx4 v216, s[36:39], s82 offen offset:0 lds\n" \
"v_mfma_f32_16x16x32_bf16 v[76:79], a[120:123], a[64:67], 0\n" \
"ds_read_b64_tr_b16 a[100:101], v214 offset:24576\n" \
"ds_read_b64_tr_b16 a[102:103], v214 offset:26624\n" \
"v_mfma_f32_16x16x32_bf16 v[76:79], a[124:127], a[68:71], v[76:79]\n" \
"ds_read_b128 a[64:67], v213 offset:0\n" \
"v_cvt_pk_bf16_f32 v6, v12, v13\n" \
"v_cvt_pk_bf16_f32 v7, v14, v15\n" \
"v_mfma_f32_16x16x32_bf16 v[92:95], a[120:123], a[72:75], 0\n" \
"s_add_u32 s48, s48, 0x1000\n" \
"ds_read_b128 a[68:71], v213 offset:2048\n" \
"v_cvt_pk_bf16_f32 v8, v16, v17\n" \
"v_mfma_f32_16x16x32_bf16 v[92:95], a[124:127], a[76:79], v[92:95]\n" \
"buffer_load_dwordx4 v217, s[36:39], s82 offen offset:1024 lds\n" \
"v_mfma_f32_16x16x32_bf16 v[88:91], a[112:115], a[72:75], 0\n" \
"ds_read_b64_tr_b16 a[104:105], v215 offset:16384\n" \
"ds_read_b64_tr_b16 a[106:107], v215 offset:18432\n" \
"v_mfma_f32_16x16x32_bf16 v[88:91], a[116:119], a[76:79], v[88:91]\n" \
"ds_read_b128 a[72:75], v213 offset:256\n" \
"v_cvt_pk_bf16_f32 v9, v18, v19\n" \
"v_cvt_pk_bf16_f32 v10, v20, v21\n" \
"v_mfma_f32_16x16x32_bf16 v[104:107], a[112:115], a[80:83], 0\n" \
"s_sub_u32 s86, s86, 32768\n" \
"s_cselect_b32 s86, 131072, s86\n" \
"ds_read_b128 a[76:79], v213 offset:2304\n" \
"v_mfma_f32_16x16x32_bf16 v[104:107], a[116:119], a[84:87], v[104:107]\n" \
"buffer_load_dwordx4 v218, s[36:39], s82 offen offset:2048 lds\n" \
"v_cvt_pk_bf16_f32 v11, v22, v23\n" \
"v_mfma_f32_16x16x32_bf16 v[108:111], a[120:123], a[80:83], 0\n" \
"ds_read_b64_tr_b16 a[108:109], v215 offset:24576\n" \
"ds_read_b64_tr_b16 a[110:111], v215 offset:26624\n" \
"v_mfma_f32_16x16x32_bf16 v[108:111], a[124:127], a[84:87], v[108:111]\n" \
"buffer_load_dwordx4 v219, s[36:39], s82 offen offset:3072 lds\n" \
"v_mfma_f32_16x16x32_bf16 v[124:127], a[120:123], a[88:91], 0\n" \
"s_waitcnt lgkmcnt(3)\n" \
"ds_read_b128 a[80:83], v213 offset:4096\n" \
"v_mfma_f32_16x16x32_bf16 v[124:127], a[124:127], a[92:95], v[124:127]\n" \
"s_add_u32 s71, s87, 0\n" \
"ds_read_b128 a[84:87], v213 offset:6144\n" \
"v_cvt_pk_bf16_f32 v12, v24, v25\n" \
"v_mfma_f32_16x16x32_bf16 v[120:123], a[112:115], a[88:91], 0\n" \
"buffer_store_dwordx4 v[0:3], v234, s[56:59], s71 offen offset:0 sc1\n" \
"v_mfma_f32_16x16x32_bf16 v[120:123], a[116:119], a[92:95], v[120:123]\n" \
"buffer_store_dwordx4 v[4:7], v234, s[56:59], s71 offen offset:1024 sc1\n" \
"buffer_store_dwordx4 v[8:11], v234, s[56:59], s71 offen offset:2048 sc1\n" \
"v_mfma_f32_16x16x32_bf16 v[64:67], a[96:99], a[64:67], v[64:67]\n" \
"s_waitcnt lgkmcnt(2)\n" \
"ds_read_b128 a[88:91], v213 offset:4352\n" \
"v_cvt_pk_bf16_f32 v13, v26, v27\n" \
"v_mfma_f32_16x16x32_bf16 v[64:67], a[100:103], a[68:71], v[64:67]\n" \
"s_xor_b32 s82, s48, s49\n" \
"ds_read_b128 a[92:95], v213 offset:6400\n" \
"v_cvt_pk_bf16_f32 v14, v28, v29\n" \
"v_mfma_f32_16x16x32_bf16 v[68:71], a[104:107], a[64:67], v[68:71]\n" \
"s_cmp_lt_u32 s48, s50\n" \
"s_cselect_b32 s82, s82, s48\n" \
"ds_read_b64_tr_b16 a[112:113], v214 offset:16896\n" \
"v_mfma_f32_16x16x32_bf16 v[68:71], a[108:111], a[68:71], v[68:71]\n" \
"s_add_u32 s81, s82, s46\n" \
"ds_read_b64_tr_b16 a[114:115], v214 offset:18944\n" \
"v_cvt_pk_bf16_f32 v15, v30, v31\n" \
"v_mfma_f32_16x16x32_bf16 v[84:87], a[104:107], a[72:75], v[84:87]\n" \
"s_add_u32 m0, s86, s79\n" \
"ds_read_b64_tr_b16 a[116:117], v214 offset:25088\n" \
"v_cvt_pk_bf16_f32 v16, v32, v33\n" \
"v_mfma_f32_16x16x32_bf16 v[84:87], a[108:111], a[76:79], v[84:87]\n" \
"buffer_load_dwordx4 v234, s[56:59], s81 offen offset:0 sc1 lds\n" \
"v_mfma_f32_16x16x32_bf16 v[80:83], a[96:99], a[72:75], v[80:83]\n" \
"s_sub_u32 s85, s85, 32768\n" \
"s_cselect_b32 s85, 131072, s85\n" \
"ds_read_b64_tr_b16 a[118:119], v214 offset:27136\n" \
"v_mfma_f32_16x16x32_bf16 v[80:83], a[100:103], a[76:79], v[80:83]\n" \
"s_waitcnt lgkmcnt(3)\n" \
"s_lshl_b32 s82, s82, 5\n" \
"ds_read_b64_tr_b16 a[120:121], v215 offset:16896\n" \
"v_cvt_pk_bf16_f32 v17, v34, v35\n" \
"v_mfma_f32_16x16x32_bf16 v[96:99], a[96:99], a[80:83], v[96:99]\n" \
"s_add_u32 s82, s82, s47\n" \
"ds_read_b64_tr_b16 a[122:123], v215 offset:18944\n" \
"v_cvt_pk_bf16_f32 v18, v36, v37\n" \
"v_mfma_f32_16x16x32_bf16 v[96:99], a[100:103], a[84:87], v[96:99]\n" \
"buffer_load_dwordx4 v234, s[56:59], s81 offen offset:1024 sc1 lds\n" \
"v_mfma_f32_16x16x32_bf16 v[100:103], a[104:107], a[80:83], v[100:103]\n" \
"ds_read_b64_tr_b16 a[124:125], v215 offset:25088\n" \
"v_add_u32 v213, s85, v211\n" \
"v_mfma_f32_16x16x32_bf16 v[100:103], a[108:111], a[84:87], v[100:103]\n" \
"buffer_load_dwordx4 v234, s[56:59], s81 offen offset:2048 sc1 lds\n" \
"v_mfma_f32_16x16x32_bf16 v[116:119], a[104:107], a[88:91], v[116:119]\n" \
"ds_read_b64_tr_b16 a[126:127], v215 offset:27136\n" \
"v_cvt_pk_bf16_f32 v19, v38, v39\n" \
"v_mfma_f32_16x16x32_bf16 v[116:119], a[108:111], a[92:95], v[116:119]\n" \
"buffer_load_dwordx4 v234, s[56:59], s81 offen offset:3072 sc1 lds\n" \
"v_cvt_pk_bf16_f32 v20, v40, v41\n" \
"v_mfma_f32_16x16x32_bf16 v[112:115], a[96:99], a[88:91], v[112:115]\n" \
"v_add_u32 v214, s85, v212\n" \
"v_xor_b32 v215, 32, v214\n" \
"v_mfma_f32_16x16x32_bf16 v[112:115], a[100:103], a[92:95], v[112:115]\n" \
"s_waitcnt vmcnt(23) lgkmcnt(0)\n" \
"s_barrier\n" \
"v_mfma_f32_16x16x32_bf16 v[72:75], a[112:115], a[64:67], v[72:75]\n" \
"s_add_u32 m0, m0, 0x4000\n" \
"ds_read_b64_tr_b16 a[96:97], v214 offset:16384\n" \
"ds_read_b64_tr_b16 a[98:99], v214 offset:18432\n" \
"v_mfma_f32_16x16x32_bf16 v[72:75], a[116:119], a[68:71], v[72:75]\n" \
"buffer_load_dwordx4 v216, s[36:39], s82 offen offset:0 lds\n" \
"v_mfma_f32_16x16x32_bf16 v[76:79], a[120:123], a[64:67], v[76:79]\n" \
"ds_read_b64_tr_b16 a[100:101], v214 offset:24576\n" \
"ds_read_b64_tr_b16 a[102:103], v214 offset:26624\n" \
"v_mfma_f32_16x16x32_bf16 v[76:79], a[124:127], a[68:71], v[76:79]\n" \
"ds_read_b128 a[64:67], v213 offset:0\n" \
"v_cvt_pk_bf16_f32 v21, v42, v43\n" \
"v_mfma_f32_16x16x32_bf16 v[92:95], a[120:123], a[72:75], v[92:95]\n" \
"s_add_u32 s48, s48, 0x1000\n" \
"ds_read_b128 a[68:71], v213 offset:2048\n" \
"v_cvt_pk_bf16_f32 v22, v44, v45\n" \
"v_mfma_f32_16x16x32_bf16 v[92:95], a[124:127], a[76:79], v[92:95]\n" \
"buffer_load_dwordx4 v217, s[36:39], s82 offen offset:1024 lds\n" \
"v_mfma_f32_16x16x32_bf16 v[88:91], a[112:115], a[72:75], v[88:91]\n" \
"ds_read_b64_tr_b16 a[104:105], v215 offset:16384\n" \
"ds_read_b64_tr_b16 a[106:107], v215 offset:18432\n" \
"v_mfma_f32_16x16x32_bf16 v[88:91], a[116:119], a[76:79], v[88:91]\n" \
"ds_read_b128 a[72:75], v213 offset:256\n" \
"v_cvt_pk_bf16_f32 v23, v46, v47\n" \
"v_mfma_f32_16x16x32_bf16 v[104:107], a[112:115], a[80:83], v[104:107]\n" \
"s_sub_u32 s86, s86, 32768\n" \
"s_cselect_b32 s86, 131072, s86\n" \
"ds_read_b128 a[76:79], v213 offset:2304\n" \
"v_mfma_f32_16x16x32_bf16 v[104:107], a[116:119], a[84:87], v[104:107]\n" \
"buffer_load_dwordx4 v218, s[36:39], s82 offen offset:2048 lds\n" \
"v_mfma_f32_16x16x32_bf16 v[108:111], a[120:123], a[80:83], v[108:111]\n" \
"ds_read_b64_tr_b16 a[108:109], v215 offset:24576\n" \
"ds_read_b64_tr_b16 a[110:111], v215 offset:26624\n" \
"v_mfma_f32_16x16x32_bf16 v[108:111], a[124:127], a[84:87], v[108:111]\n" \
"buffer_load_dwordx4 v219, s[36:39], s82 offen offset:3072 lds\n" \
"v_mfma_f32_16x16x32_bf16 v[124:127], a[120:123], a[88:91], v[124:127]\n" \
"ds_read_b128 a[80:83], v213 offset:4096\n" \
"s_waitcnt lgkmcnt(4)\n" \
"v_mfma_f32_16x16x32_bf16 v[124:127], a[124:127], a[92:95], v[124:127]\n" \
"ds_read_b128 a[84:87], v213 offset:6144\n" \
"v_cvt_pk_bf16_f32 v24, v48, v49\n" \
"v_mfma_f32_16x16x32_bf16 v[120:123], a[112:115], a[88:91], v[120:123]\n" \
"buffer_store_dwordx4 v[12:15], v234, s[56:59], s71 offen offset:3072 sc1\n" \
"v_mfma_f32_16x16x32_bf16 v[120:123], a[116:119], a[92:95], v[120:123]\n" \
"s_add_u32 s71, s87, 4096\n" \
"buffer_store_dwordx4 v[16:19], v234, s[56:59], s71 offen offset:0 sc1\n" \
"buffer_store_dwordx4 v[20:23], v234, s[56:59], s71 offen offset:1024 sc1\n" \
"v_mfma_f32_16x16x32_bf16 v[64:67], a[96:99], a[64:67], v[64:67]\n" \
"s_waitcnt lgkmcnt(2)\n" \
"ds_read_b128 a[88:91], v213 offset:4352\n" \
"v_cvt_pk_bf16_f32 v25, v50, v51\n" \
"v_mfma_f32_16x16x32_bf16 v[64:67], a[100:103], a[68:71], v[64:67]\n" \
"s_xor_b32 s82, s48, s49\n" \
"ds_read_b128 a[92:95], v213 offset:6400\n" \
"v_cvt_pk_bf16_f32 v26, v52, v53\n" \
"v_mfma_f32_16x16x32_bf16 v[68:71], a[104:107], a[64:67], v[68:71]\n" \
"s_cmp_lt_u32 s48, s50\n" \
"s_cselect_b32 s82, s82, s48\n" \
"ds_read_b64_tr_b16 a[112:113], v214 offset:16896\n" \
"v_mfma_f32_16x16x32_bf16 v[68:71], a[108:111], a[68:71], v[68:71]\n" \
"s_add_u32 s81, s82, s46\n" \
"ds_read_b64_tr_b16 a[114:115], v214 offset:18944\n" \
"v_cvt_pk_bf16_f32 v27, v54, v55\n" \
"v_mfma_f32_16x16x32_bf16 v[84:87], a[104:107], a[72:75], v[84:87]\n" \
"s_add_u32 m0, s86, s79\n" \
"ds_read_b64_tr_b16 a[116:117], v214 offset:25088\n" \
"v_cvt_pk_bf16_f32 v28, v56, v57\n" \
"v_mfma_f32_16x16x32_bf16 v[84:87], a[108:111], a[76:79], v[84:87]\n" \
"buffer_load_dwordx4 v234, s[56:59], s81 offen offset:0 sc1 lds\n" \
"v_mfma_f32_16x16x32_bf16 v[80:83], a[96:99], a[72:75], v[80:83]\n" \
"s_sub_u32 s85, s85, 32768\n" \
"s_cselect_b32 s85, 131072, s85\n" \
"ds_read_b64_tr_b16 a[118:119], v214 offset:27136\n" \
"v_mfma_f32_16x16x32_bf16 v[80:83], a[100:103], a[76:79], v[80:83]\n" \
"s_waitcnt lgkmcnt(3)\n" \
"s_lshl_b32 s82, s82, 5\n" \
"ds_read_b64_tr_b16 a[120:121], v215 offset:16896\n" \
"v_cvt_pk_bf16_f32 v29, v58, v59\n" \
"v_mfma_f32_16x16x32_bf16 v[96:99], a[96:99], a[80:83], v[96:99]\n" \
"s_add_u32 s82, s82, s47\n" \
"ds_read_b64_tr_b16 a[122:123], v215 offset:18944\n" \
"v_cvt_pk_bf16_f32 v30, v60, v61\n" \
"v_mfma_f32_16x16x32_bf16 v[96:99], a[100:103], a[84:87], v[96:99]\n" \
"buffer_load_dwordx4 v234, s[56:59], s81 offen offset:1024 sc1 lds\n" \
"v_mfma_f32_16x16x32_bf16 v[100:103], a[104:107], a[80:83], v[100:103]\n" \
"ds_read_b64_tr_b16 a[124:125], v215 offset:25088\n" \
"v_add_u32 v213, s85, v211\n" \
"v_mfma_f32_16x16x32_bf16 v[100:103], a[108:111], a[84:87], v[100:103]\n" \
"buffer_load_dwordx4 v234, s[56:59], s81 offen offset:2048 sc1 lds\n" \
"v_mfma_f32_16x16x32_bf16 v[116:119], a[104:107], a[88:91], v[116:119]\n" \
"ds_read_b64_tr_b16 a[126:127], v215 offset:27136\n" \
"v_cvt_pk_bf16_f32 v31, v62, v63\n" \
"v_mfma_f32_16x16x32_bf16 v[116:119], a[108:111], a[92:95], v[116:119]\n" \
"buffer_load_dwordx4 v234, s[56:59], s81 offen offset:3072 sc1 lds\n" \
"v_mfma_f32_16x16x32_bf16 v[112:115], a[96:99], a[88:91], v[112:115]\n" \
"v_add_u32 v214, s85, v212\n" \
"v_xor_b32 v215, 32, v214\n" \
"v_mfma_f32_16x16x32_bf16 v[112:115], a[100:103], a[92:95], v[112:115]\n" \
"s_waitcnt vmcnt(26) lgkmcnt(0)\n" \
"s_barrier\n" \
"v_mfma_f32_16x16x32_bf16 v[72:75], a[112:115], a[64:67], v[72:75]\n" \
"s_add_u32 m0, m0, 0x4000\n" \
"ds_read_b64_tr_b16 a[96:97], v214 offset:16384\n" \
"ds_read_b64_tr_b16 a[98:99], v214 offset:18432\n" \
"v_mfma_f32_16x16x32_bf16 v[72:75], a[116:119], a[68:71], v[72:75]\n" \
"buffer_load_dwordx4 v216, s[36:39], s82 offen offset:0 lds\n" \
"v_mfma_f32_16x16x32_bf16 v[76:79], a[120:123], a[64:67], v[76:79]\n" \
"ds_read_b64_tr_b16 a[100:101], v214 offset:24576\n" \
"ds_read_b64_tr_b16 a[102:103], v214 offset:26624\n" \
"v_mfma_f32_16x16x32_bf16 v[76:79], a[124:127], a[68:71], v[76:79]\n" \
"ds_read_b128 a[64:67], v213 offset:0\n" \
"v_mfma_f32_16x16x32_bf16 v[92:95], a[120:123], a[72:75], v[92:95]\n" \
"s_add_u32 s48, s48, 0x1000\n" \
"ds_read_b128 a[68:71], v213 offset:2048\n" \
"v_mfma_f32_16x16x32_bf16 v[92:95], a[124:127], a[76:79], v[92:95]\n" \
"buffer_load_dwordx4 v217, s[36:39], s82 offen offset:1024 lds\n" \
"v_mfma_f32_16x16x32_bf16 v[88:91], a[112:115], a[72:75], v[88:91]\n" \
"ds_read_b64_tr_b16 a[104:105], v215 offset:16384\n" \
"ds_read_b64_tr_b16 a[106:107], v215 offset:18432\n" \
"v_mfma_f32_16x16x32_bf16 v[88:91], a[116:119], a[76:79], v[88:91]\n" \
"ds_read_b128 a[72:75], v213 offset:256\n" \
"v_mfma_f32_16x16x32_bf16 v[104:107], a[112:115], a[80:83], v[104:107]\n" \
"s_sub_u32 s86, s86, 32768\n" \
"s_cselect_b32 s86, 131072, s86\n" \
"ds_read_b128 a[76:79], v213 offset:2304\n" \
"v_mfma_f32_16x16x32_bf16 v[104:107], a[116:119], a[84:87], v[104:107]\n" \
"buffer_load_dwordx4 v218, s[36:39], s82 offen offset:2048 lds\n" \
"v_mfma_f32_16x16x32_bf16 v[108:111], a[120:123], a[80:83], v[108:111]\n" \
"ds_read_b64_tr_b16 a[108:109], v215 offset:24576\n" \
"ds_read_b64_tr_b16 a[110:111], v215 offset:26624\n" \
"v_mfma_f32_16x16x32_bf16 v[108:111], a[124:127], a[84:87], v[108:111]\n" \
"buffer_load_dwordx4 v219, s[36:39], s82 offen offset:3072 lds\n" \
"v_mfma_f32_16x16x32_bf16 v[124:127], a[120:123], a[88:91], v[124:127]\n" \
"ds_read_b128 a[80:83], v213 offset:4096\n" \
"s_waitcnt lgkmcnt(4)\n" \
"v_mfma_f32_16x16x32_bf16 v[124:127], a[124:127], a[92:95], v[124:127]\n" \
"ds_read_b128 a[84:87], v213 offset:6144\n" \
"v_mfma_f32_16x16x32_bf16 v[120:123], a[112:115], a[88:91], v[120:123]\n" \
"buffer_store_dwordx4 v[24:27], v234, s[56:59], s71 offen offset:2048 sc1\n" \
"v_mfma_f32_16x16x32_bf16 v[120:123], a[116:119], a[92:95], v[120:123]\n" \
"buffer_store_dwordx4 v[28:31], v234, s[56:59], s71 offen offset:3072 sc1\n" \
"v_mfma_f32_16x16x32_bf16 v[64:67], a[96:99], a[64:67], v[64:67]\n" \
"ds_read_b128 a[88:91], v213 offset:4352\n" \
"v_mfma_f32_16x16x32_bf16 v[64:67], a[100:103], a[68:71], v[64:67]\n" \
"s_waitcnt lgkmcnt(2)\n" \
"s_xor_b32 s82, s48, s49\n" \
"ds_read_b128 a[92:95], v213 offset:6400\n" \
"v_mfma_f32_16x16x32_bf16 v[68:71], a[104:107], a[64:67], v[68:71]\n" \
"s_cmp_lt_u32 s48, s50\n" \
"s_cselect_b32 s82, s82, s48\n" \
"ds_read_b64_tr_b16 a[112:113], v214 offset:16896\n" \
"v_mfma_f32_16x16x32_bf16 v[68:71], a[108:111], a[68:71], v[68:71]\n" \
"s_add_u32 s81, s82, s46\n" \
"ds_read_b64_tr_b16 a[114:115], v214 offset:18944\n" \
"v_mfma_f32_16x16x32_bf16 v[84:87], a[104:107], a[72:75], v[84:87]\n" \
"s_add_u32 m0, s86, s79\n" \
"ds_read_b64_tr_b16 a[116:117], v214 offset:25088\n" \
"v_mfma_f32_16x16x32_bf16 v[84:87], a[108:111], a[76:79], v[84:87]\n" \
"buffer_load_dwordx4 v234, s[56:59], s81 offen offset:0 sc1 lds\n" \
"v_mfma_f32_16x16x32_bf16 v[80:83], a[96:99], a[72:75], v[80:83]\n" \
"s_sub_u32 s85, s85, 32768\n" \
"s_cselect_b32 s85, 131072, s85\n" \
"ds_read_b64_tr_b16 a[118:119], v214 offset:27136\n" \
"v_mfma_f32_16x16x32_bf16 v[80:83], a[100:103], a[76:79], v[80:83]\n" \
"s_waitcnt lgkmcnt(3)\n" \
"s_lshl_b32 s82, s82, 5\n" \
"ds_read_b64_tr_b16 a[120:121], v215 offset:16896\n" \
"v_mfma_f32_16x16x32_bf16 v[96:99], a[96:99], a[80:83], v[96:99]\n" \
"s_add_u32 s82, s82, s47\n" \
"ds_read_b64_tr_b16 a[122:123], v215 offset:18944\n" \
"v_mfma_f32_16x16x32_bf16 v[96:99], a[100:103], a[84:87], v[96:99]\n" \
"buffer_load_dwordx4 v234, s[56:59], s81 offen offset:1024 sc1 lds\n" \
"v_mfma_f32_16x16x32_bf16 v[100:103], a[104:107], a[80:83], v[100:103]\n" \
"ds_read_b64_tr_b16 a[124:125], v215 offset:25088\n" \
"v_add_u32 v213, s85, v211\n" \
"v_mfma_f32_16x16x32_bf16 v[100:103], a[108:111], a[84:87], v[100:103]\n" \
"buffer_load_dwordx4 v234, s[56:59], s81 offen offset:2048 sc1 lds\n" \
"v_mfma_f32_16x16x32_bf16 v[116:119], a[104:107], a[88:91], v[116:119]\n" \
"ds_read_b64_tr_b16 a[126:127], v215 offset:27136\n" \
"v_mfma_f32_16x16x32_bf16 v[116:119], a[108:111], a[92:95], v[116:119]\n" \
"buffer_load_dwordx4 v234, s[56:59], s81 offen offset:3072 sc1 lds\n" \
"v_mfma_f32_16x16x32_bf16 v[112:115], a[96:99], a[88:91], v[112:115]\n" \
"v_add_u32 v214, s85, v212\n" \
"v_xor_b32 v215, 32, v214\n" \
"v_mfma_f32_16x16x32_bf16 v[112:115], a[100:103], a[92:95], v[112:115]\n" \
"s_waitcnt vmcnt(28) lgkmcnt(0)\n" \
"s_barrier\n" \
"v_mfma_f32_16x16x32_bf16 v[72:75], a[112:115], a[64:67], v[72:75]\n" \
"s_add_u32 m0, m0, 0x4000\n" \
"ds_read_b64_tr_b16 a[96:97], v214 offset:16384\n" \
"ds_read_b64_tr_b16 a[98:99], v214 offset:18432\n" \
"v_mfma_f32_16x16x32_bf16 v[72:75], a[116:119], a[68:71], v[72:75]\n" \
"buffer_load_dwordx4 v216, s[36:39], s82 offen offset:0 lds\n" \
"v_mfma_f32_16x16x32_bf16 v[76:79], a[120:123], a[64:67], v[76:79]\n" \
"ds_read_b64_tr_b16 a[100:101], v214 offset:24576\n" \
"ds_read_b64_tr_b16 a[102:103], v214 offset:26624\n" \
"v_mfma_f32_16x16x32_bf16 v[76:79], a[124:127], a[68:71], v[76:79]\n" \
"ds_read_b128 a[64:67], v213 offset:0\n" \
"v_mfma_f32_16x16x32_bf16 v[92:95], a[120:123], a[72:75], v[92:95]\n" \
"s_add_u32 s48, s48, 0x1000\n" \
"ds_read_b128 a[68:71], v213 offset:2048\n" \
"v_mfma_f32_16x16x32_bf16 v[92:95], a[124:127], a[76:79], v[92:95]\n" \
"buffer_load_dwordx4 v217, s[36:39], s82 offen offset:1024 lds\n" \
"v_mfma_f32_16x16x32_bf16 v[88:91], a[112:115], a[72:75], v[88:91]\n" \
"ds_read_b64_tr_b16 a[104:105], v215 offset:16384\n" \
"ds_read_b64_tr_b16 a[106:107], v215 offset:18432\n" \
"v_mfma_f32_16x16x32_bf16 v[88:91], a[116:119], a[76:79], v[88:91]\n" \
"ds_read_b128 a[72:75], v213 offset:256\n" \
"v_mfma_f32_16x16x32_bf16 v[104:107], a[112:115], a[80:83], v[104:107]\n" \
"s_sub_u32 s86, s86, 32768\n" \
"s_cselect_b32 s86, 131072, s86\n" \
"ds_read_b128 a[76:79], v213 offset:2304\n" \
"v_mfma_f32_16x16x32_bf16 v[104:107], a[116:119], a[84:87], v[104:107]\n" \
"buffer_load_dwordx4 v218, s[36:39], s82 offen offset:2048 lds\n" \
"v_mfma_f32_16x16x32_bf16 v[108:111], a[120:123], a[80:83], v[108:111]\n" \
"ds_read_b64_tr_b16 a[108:109], v215 offset:24576\n" \
"ds_read_b64_tr_b16 a[110:111], v215 offset:26624\n" \
"v_mfma_f32_16x16x32_bf16 v[108:111], a[124:127], a[84:87], v[108:111]\n" \
"buffer_load_dwordx4 v219, s[36:39], s82 offen offset:3072 lds\n" \
"v_mfma_f32_16x16x32_bf16 v[124:127], a[120:123], a[88:91], v[124:127]\n" \
"ds_read_b128 a[80:83], v213 offset:4096\n" \
"s_waitcnt lgkmcnt(4)\n" \
"v_mfma_f32_16x16x32_bf16 v[124:127], a[124:127], a[92:95], v[124:127]\n" \
"ds_read_b128 a[84:87], v213 offset:6144\n" \
"v_mfma_f32_16x16x32_bf16 v[120:123], a[112:115], a[88:91], v[120:123]\n" \
"v_mfma_f32_16x16x32_bf16 v[120:123], a[116:119], a[92:95], v[120:123]\n" \
"v_mfma_f32_16x16x32_bf16 v[64:67], a[96:99], a[64:67], v[64:67]\n" \
"ds_read_b128 a[88:91], v213 offset:4352\n" \
"v_mfma_f32_16x16x32_bf16 v[64:67], a[100:103], a[68:71], v[64:67]\n" \
"s_waitcnt lgkmcnt(2)\n" \
"s_xor_b32 s82, s48, s49\n" \
"ds_read_b128 a[92:95], v213 offset:6400\n" \
"v_mfma_f32_16x16x32_bf16 v[68:71], a[104:107], a[64:67], v[68:71]\n" \
"s_cmp_lt_u32 s48, s50\n" \
"s_cselect_b32 s82, s82, s48\n" \
"ds_read_b64_tr_b16 a[112:113], v214 offset:16896\n" \
"v_mfma_f32_16x16x32_bf16 v[68:71], a[108:111], a[68:71], v[68:71]\n" \
"s_add_u32 s81, s82, s46\n" \
"ds_read_b64_tr_b16 a[114:115], v214 offset:18944\n" \
"v_mfma_f32_16x16x32_bf16 v[84:87], a[104:107], a[72:75], v[84:87]\n" \
"s_add_u32 m0, s86, s79\n" \
"ds_read_b64_tr_b16 a[116:117], v214 offset:25088\n" \
"v_mfma_f32_16x16x32_bf16 v[84:87], a[108:111], a[76:79], v[84:87]\n" \
"buffer_load_dwordx4 v234, s[56:59], s81 offen offset:0 sc1 lds\n" \
"v_mfma_f32_16x16x32_bf16 v[80:83], a[96:99], a[72:75], v[80:83]\n" \
"s_sub_u32 s85, s85, 32768\n" \
"s_cselect_b32 s85, 131072, s85\n" \
"ds_read_b64_tr_b16 a[118:119], v214 offset:27136\n" \
"v_mfma_f32_16x16x32_bf16 v[80:83], a[100:103], a[76:79], v[80:83]\n" \
"s_waitcnt lgkmcnt(3)\n" \
"s_lshl_b32 s82, s82, 5\n" \
"ds_read_b64_tr_b16 a[120:121], v215 offset:16896\n" \
"v_mfma_f32_16x16x32_bf16 v[96:99], a[96:99], a[80:83], v[96:99]\n" \
"s_add_u32 s82, s82, s47\n" \
"ds_read_b64_tr_b16 a[122:123], v215 offset:18944\n" \
"v_mfma_f32_16x16x32_bf16 v[96:99], a[100:103], a[84:87], v[96:99]\n" \
"buffer_load_dwordx4 v234, s[56:59], s81 offen offset:1024 sc1 lds\n" \
"v_mfma_f32_16x16x32_bf16 v[100:103], a[104:107], a[80:83], v[100:103]\n" \
"ds_read_b64_tr_b16 a[124:125], v215 offset:25088\n" \
"v_add_u32 v213, s85, v211\n" \
"v_mfma_f32_16x16x32_bf16 v[100:103], a[108:111], a[84:87], v[100:103]\n" \
"buffer_load_dwordx4 v234, s[56:59], s81 offen offset:2048 sc1 lds\n" \
"v_mfma_f32_16x16x32_bf16 v[116:119], a[104:107], a[88:91], v[116:119]\n" \
"ds_read_b64_tr_b16 a[126:127], v215 offset:27136\n" \
"v_mfma_f32_16x16x32_bf16 v[116:119], a[108:111], a[92:95], v[116:119]\n" \
"buffer_load_dwordx4 v234, s[56:59], s81 offen offset:3072 sc1 lds\n" \
"v_mfma_f32_16x16x32_bf16 v[112:115], a[96:99], a[88:91], v[112:115]\n" \
"v_add_u32 v214, s85, v212\n" \
"v_xor_b32 v215, 32, v214\n" \
"v_mfma_f32_16x16x32_bf16 v[112:115], a[100:103], a[92:95], v[112:115]\n" \
"s_waitcnt vmcnt(25) lgkmcnt(0)\n" \
"s_barrier\n" \
"v_mfma_f32_16x16x32_bf16 v[72:75], a[112:115], a[64:67], v[72:75]\n" \
"s_add_u32 m0, m0, 0x4000\n" \
"ds_read_b64_tr_b16 a[96:97], v214 offset:16384\n" \
"ds_read_b64_tr_b16 a[98:99], v214 offset:18432\n" \
"v_mfma_f32_16x16x32_bf16 v[72:75], a[116:119], a[68:71], v[72:75]\n" \
"buffer_load_dwordx4 v216, s[36:39], s82 offen offset:0 lds\n" \
"v_mfma_f32_16x16x32_bf16 v[76:79], a[120:123], a[64:67], v[76:79]\n" \
"ds_read_b64_tr_b16 a[100:101], v214 offset:24576\n" \
"ds_read_b64_tr_b16 a[102:103], v214 offset:26624\n" \
"v_mfma_f32_16x16x32_bf16 v[76:79], a[124:127], a[68:71], v[76:79]\n" \
"ds_read_b128 a[64:67], v213 offset:0\n" \
"v_mfma_f32_16x16x32_bf16 v[92:95], a[120:123], a[72:75], v[92:95]\n" \
"s_add_u32 s48, s48, 0x1000\n" \
"ds_read_b128 a[68:71], v213 offset:2048\n" \
"v_mfma_f32_16x16x32_bf16 v[92:95], a[124:127], a[76:79], v[92:95]\n" \
"buffer_load_dwordx4 v217, s[36:39], s82 offen offset:1024 lds\n" \
"v_mfma_f32_16x16x32_bf16 v[88:91], a[112:115], a[72:75], v[88:91]\n" \
"ds_read_b64_tr_b16 a[104:105], v215 offset:16384\n" \
"ds_read_b64_tr_b16 a[106:107], v215 offset:18432\n" \
"v_mfma_f32_16x16x32_bf16 v[88:91], a[116:119], a[76:79], v[88:91]\n" \
"ds_read_b128 a[72:75], v213 offset:256\n" \
"v_mfma_f32_16x16x32_bf16 v[104:107], a[112:115], a[80:83], v[104:107]\n" \
"s_sub_u32 s86, s86, 32768\n" \
"s_cselect_b32 s86, 131072, s86\n" \
"ds_read_b128 a[76:79], v213 offset:2304\n" \
"v_mfma_f32_16x16x32_bf16 v[104:107], a[116:119], a[84:87], v[104:107]\n" \
"buffer_load_dwordx4 v218, s[36:39], s82 offen offset:2048 lds\n" \
"v_mfma_f32_16x16x32_bf16 v[108:111], a[120:123], a[80:83], v[108:111]\n" \
"ds_read_b64_tr_b16 a[108:109], v215 offset:24576\n" \
"ds_read_b64_tr_b16 a[110:111], v215 offset:26624\n" \
"v_mfma_f32_16x16x32_bf16 v[108:111], a[124:127], a[84:87], v[108:111]\n" \
"buffer_load_dwordx4 v219, s[36:39], s82 offen offset:3072 lds\n" \
"v_mfma_f32_16x16x32_bf16 v[124:127], a[120:123], a[88:91], v[124:127]\n" \
"ds_read_b128 a[80:83], v213 offset:4096\n" \
"s_waitcnt lgkmcnt(4)\n" \
"v_mfma_f32_16x16x32_bf16 v[124:127], a[124:127], a[92:95], v[124:127]\n" \
"ds_read_b128 a[84:87], v213 offset:6144\n" \
"v_mfma_f32_16x16x32_bf16 v[120:123], a[112:115], a[88:91], v[120:123]\n" \
"v_mfma_f32_16x16x32_bf16 v[120:123], a[116:119], a[92:95], v[120:123]\n" \
"v_mfma_f32_16x16x32_bf16 v[64:67], a[96:99], a[64:67], v[64:67]\n" \
"ds_read_b128 a[88:91], v213 offset:4352\n" \
"v_mfma_f32_16x16x32_bf16 v[64:67], a[100:103], a[68:71], v[64:67]\n" \
"s_waitcnt lgkmcnt(2)\n" \
"s_xor_b32 s82, s48, s49\n" \
"ds_read_b128 a[92:95], v213 offset:6400\n" \
"v_mfma_f32_16x16x32_bf16 v[68:71], a[104:107], a[64:67], v[68:71]\n" \
"s_cmp_lt_u32 s48, s50\n" \
"s_cselect_b32 s82, s82, s48\n" \
"ds_read_b64_tr_b16 a[112:113], v214 offset:16896\n" \
"v_mfma_f32_16x16x32_bf16 v[68:71], a[108:111], a[68:71], v[68:71]\n" \
"s_add_u32 s81, s82, s46\n" \
"ds_read_b64_tr_b16 a[114:115], v214 offset:18944\n" \
"v_mfma_f32_16x16x32_bf16 v[84:87], a[104:107], a[72:75], v[84:87]\n" \
"s_add_u32 m0, s86, s79\n" \
"ds_read_b64_tr_b16 a[116:117], v214 offset:25088\n" \
"v_mfma_f32_16x16x32_bf16 v[84:87], a[108:111], a[76:79], v[84:87]\n" \
"buffer_load_dwordx4 v234, s[56:59], s81 offen offset:0 sc1 lds\n" \
"v_mfma_f32_16x16x32_bf16 v[80:83], a[96:99], a[72:75], v[80:83]\n" \
"s_sub_u32 s85, s85, 32768\n" \
"s_cselect_b32 s85, 131072, s85\n" \
"ds_read_b64_tr_b16 a[118:119], v214 offset:27136\n" \
"v_mfma_f32_16x16x32_bf16 v[80:83], a[100:103], a[76:79], v[80:83]\n" \
"s_waitcnt lgkmcnt(3)\n" \
"s_lshl_b32 s82, s82, 5\n" \
"ds_read_b64_tr_b16 a[120:121], v215 offset:16896\n" \
"v_mfma_f32_16x16x32_bf16 v[96:99], a[96:99], a[80:83], v[96:99]\n" \
"s_add_u32 s82, s82, s47\n" \
"ds_read_b64_tr_b16 a[122:123], v215 offset:18944\n" \
"v_mfma_f32_16x16x32_bf16 v[96:99], a[100:103], a[84:87], v[96:99]\n" \
"buffer_load_dwordx4 v234, s[56:59], s81 offen offset:1024 sc1 lds\n" \
"v_mfma_f32_16x16x32_bf16 v[100:103], a[104:107], a[80:83], v[100:103]\n" \
"ds_read_b64_tr_b16 a[124:125], v215 offset:25088\n" \
"v_add_u32 v213, s85, v211\n" \
"v_mfma_f32_16x16x32_bf16 v[100:103], a[108:111], a[84:87], v[100:103]\n" \
"buffer_load_dwordx4 v234, s[56:59], s81 offen offset:2048 sc1 lds\n" \
"v_mfma_f32_16x16x32_bf16 v[116:119], a[104:107], a[88:91], v[116:119]\n" \
"ds_read_b64_tr_b16 a[126:127], v215 offset:27136\n" \
"v_mfma_f32_16x16x32_bf16 v[116:119], a[108:111], a[92:95], v[116:119]\n" \
"buffer_load_dwordx4 v234, s[56:59], s81 offen offset:3072 sc1 lds\n" \
"v_mfma_f32_16x16x32_bf16 v[112:115], a[96:99], a[88:91], v[112:115]\n" \
"v_add_u32 v214, s85, v212\n" \
"v_xor_b32 v215, 32, v214\n" \
"v_mfma_f32_16x16x32_bf16 v[112:115], a[100:103], a[92:95], v[112:115]\n" \
"s_waitcnt vmcnt(20) lgkmcnt(0)\n" \
"s_barrier\n" \
"buffer_store_dword v230, v231, s[56:59], s64 offen sc1\n" \
"v_mfma_f32_16x16x32_bf16 v[72:75], a[112:115], a[64:67], v[72:75]\n" \
"s_add_u32 m0, m0, 0x4000\n" \
"ds_read_b64_tr_b16 a[96:97], v214 offset:16384\n" \
"ds_read_b64_tr_b16 a[98:99], v214 offset:18432\n" \
"v_mfma_f32_16x16x32_bf16 v[72:75], a[116:119], a[68:71], v[72:75]\n" \
"buffer_load_dwordx4 v216, s[36:39], s82 offen offset:0 lds\n" \
"v_mfma_f32_16x16x32_bf16 v[76:79], a[120:123], a[64:67], v[76:79]\n" \
"ds_read_b64_tr_b16 a[100:101], v214 offset:24576\n" \
"ds_read_b64_tr_b16 a[102:103], v214 offset:26624\n" \
"v_mfma_f32_16x16x32_bf16 v[76:79], a[124:127], a[68:71], v[76:79]\n" \
"ds_read_b128 a[64:67], v213 offset:0\n" \
"v_mfma_f32_16x16x32_bf16 v[92:95], a[120:123], a[72:75], v[92:95]\n" \
"s_add_u32 s48, s48, 0x1000\n" \
"ds_read_b128 a[68:71], v213 offset:2048\n" \
"v_mfma_f32_16x16x32_bf16 v[92:95], a[124:127], a[76:79], v[92:95]\n" \
"buffer_load_dwordx4 v217, s[36:39], s82 offen offset:1024 lds\n" \
"v_mfma_f32_16x16x32_bf16 v[88:91], a[112:115], a[72:75], v[88:91]\n" \
"ds_read_b64_tr_b16 a[104:105], v215 offset:16384\n" \
"ds_read_b64_tr_b16 a[106:107], v215 offset:18432\n" \
"v_mfma_f32_16x16x32_bf16 v[88:91], a[116:119], a[76:79], v[88:91]\n" \
"ds_read_b128 a[72:75], v213 offset:256\n" \
"v_mfma_f32_16x16x32_bf16 v[104:107], a[112:115], a[80:83], v[104:107]\n" \
"s_sub_u32 s86, s86, 32768\n" \
"s_cselect_b32 s86, 131072, s86\n" \
"ds_read_b128 a[76:79], v213 offset:2304\n" \
"v_mfma_f32_16x16x32_bf16 v[104:107], a[116:119], a[84:87], v[104:107]\n" \
"buffer_load_dwordx4 v218, s[36:39], s82 offen offset:2048 lds\n" \
"v_mfma_f32_16x16x32_bf16 v[108:111], a[120:123], a[80:83], v[108:111]\n" \
"ds_read_b64_tr_b16 a[108:109], v215 offset:24576\n" \
"ds_read_b64_tr_b16 a[110:111], v215 offset:26624\n" \
"v_mfma_f32_16x16x32_bf16 v[108:111], a[124:127], a[84:87], v[108:111]\n" \
"buffer_load_dwordx4 v219, s[36:39], s82 offen offset:3072 lds\n" \
"v_mfma_f32_16x16x32_bf16 v[124:127], a[120:123], a[88:91], v[124:127]\n" \
"ds_read_b128 a[80:83], v213 offset:4096\n" \
"s_waitcnt lgkmcnt(4)\n" \
"v_mfma_f32_16x16x32_bf16 v[124:127], a[124:127], a[92:95], v[124:127]\n" \
"ds_read_b128 a[84:87], v213 offset:6144\n" \
"v_mfma_f32_16x16x32_bf16 v[120:123], a[112:115], a[88:91], v[120:123]\n" \
"v_mfma_f32_16x16x32_bf16 v[120:123], a[116:119], a[92:95], v[120:123]\n" \
"s_mov_b32 s72, 5\n" \
"s_nop 0\n" \
"s_nop 0\n" \
"Lo64_%=:\n" \
"v_mfma_f32_16x16x32_bf16 v[64:67], a[96:99], a[64:67], v[64:67]\n" \
"ds_read_b128 a[88:91], v213 offset:4352\n" \
"v_mfma_f32_16x16x32_bf16 v[64:67], a[100:103], a[68:71], v[64:67]\n" \
"Lpg11464_%=:\n" \
"s_cbranch_execz Lcb1_%=\n" \
"s_waitcnt lgkmcnt(2)\n" \
"s_xor_b32 s82, s48, s49\n" \
"ds_read_b128 a[92:95], v213 offset:6400\n" \
"v_mfma_f32_16x16x32_bf16 v[68:71], a[104:107], a[64:67], v[68:71]\n" \
"s_cmp_lt_u32 s48, s50\n" \
"s_cselect_b32 s82, s82, s48\n" \
"ds_read_b64_tr_b16 a[112:113], v214 offset:16896\n" \
"v_mfma_f32_16x16x32_bf16 v[68:71], a[108:111], a[68:71], v[68:71]\n" \
"s_add_u32 s81, s82, s46\n" \
"ds_read_b64_tr_b16 a[114:115], v214 offset:18944\n" \
"v_mfma_f32_16x16x32_bf16 v[84:87], a[104:107], a[72:75], v[84:87]\n" \
"s_add_u32 m0, s86, s79\n" \
"ds_read_b64_tr_b16 a[116:117], v214 offset:25088\n" \
"v_mfma_f32_16x16x32_bf16 v[84:87], a[108:111], a[76:79], v[84:87]\n" \
"buffer_load_dwordx4 v234, s[56:59], s81 offen offset:0 sc1 lds\n" \
"v_mfma_f32_16x16x32_bf16 v[80:83], a[96:99], a[72:75], v[80:83]\n" \
"s_sub_u32 s85, s85, 32768\n" \
"s_cselect_b32 s85, 131072, s85\n" \
"ds_read_b64_tr_b16 a[118:119], v214 offset:27136\n" \
"v_mfma_f32_16x16x32_bf16 v[80:83], a[100:103], a[76:79], v[80:83]\n" \
"s_waitcnt lgkmcnt(3)\n" \
"s_lshl_b32 s82, s82, 5\n" \
"ds_read_b64_tr_b16 a[120:121], v215 offset:16896\n" \
"v_mfma_f32_16x16x32_bf16 v[96:99], a[96:99], a[80:83], v[96:99]\n" \
"s_add_u32 s82, s82, s47\n" \
"ds_read_b64_tr_b16 a[122:123], v215 offset:18944\n" \
"v_mfma_f32_16x16x32_bf16 v[96:99], a[100:103], a[84:87], v[96:99]\n" \
"buffer_load_dwordx4 v234, s[56:59], s81 offen offset:1024 sc1 lds\n" \
"v_mfma_f32_16x16x32_bf16 v[100:103], a[104:107], a[80:83], v[100:103]\n" \
"ds_read_b64_tr_b16 a[124:125], v215 offset:25088\n" \
"v_add_u32 v213, s85, v211\n" \
"v_mfma_f32_16x16x32_bf16 v[100:103], a[108:111], a[84:87], v[100:103]\n" \
"buffer_load_dwordx4 v234, s[56:59], s81 offen offset:2048 sc1 lds\n" \
"v_mfma_f32_16x16x32_bf16 v[116:119], a[104:107], a[88:91], v[116:119]\n" \
"ds_read_b64_tr_b16 a[126:127], v215 offset:27136\n" \
"v_mfma_f32_16x16x32_bf16 v[116:119], a[108:111], a[92:95], v[116:119]\n" \
"buffer_load_dwordx4 v234, s[56:59], s81 offen offset:3072 sc1 lds\n" \
"v_mfma_f32_16x16x32_bf16 v[112:115], a[96:99], a[88:91], v[112:115]\n" \
"v_add_u32 v214, s85, v212\n" \
"v_xor_b32 v215, 32, v214\n" \
"v_mfma_f32_16x16x32_bf16 v[112:115], a[100:103], a[92:95], v[112:115]\n" \
"s_waitcnt vmcnt(20) lgkmcnt(0)\n" \
"s_barrier\n" \
"v_mfma_f32_16x16x32_bf16 v[72:75], a[112:115], a[64:67], v[72:75]\n" \
"s_add_u32 m0, m0, 0x4000\n" \
"ds_read_b64_tr_b16 a[96:97], v214 offset:16384\n" \
"ds_read_b64_tr_b16 a[98:99], v214 offset:18432\n" \
"v_mfma_f32_16x16x32_bf16 v[72:75], a[116:119], a[68:71], v[72:75]\n" \
"buffer_load_dwordx4 v216, s[36:39], s82 offen offset:0 lds\n" \
"v_mfma_f32_16x16x32_bf16 v[76:79], a[120:123], a[64:67], v[76:79]\n" \
"ds_read_b64_tr_b16 a[100:101], v214 offset:24576\n" \
"ds_read_b64_tr_b16 a[102:103], v214 offset:26624\n" \
"v_mfma_f32_16x16x32_bf16 v[76:79], a[124:127], a[68:71], v[76:79]\n" \
"ds_read_b128 a[64:67], v213 offset:0\n" \
"v_mfma_f32_16x16x32_bf16 v[92:95], a[120:123], a[72:75], v[92:95]\n" \
"s_add_u32 s48, s48, 0x1000\n" \
"ds_read_b128 a[68:71], v213 offset:2048\n" \
"v_mfma_f32_16x16x32_bf16 v[92:95], a[124:127], a[76:79], v[92:95]\n" \
"buffer_load_dwordx4 v217, s[36:39], s82 offen offset:1024 lds\n" \
"v_mfma_f32_16x16x32_bf16 v[88:91], a[112:115], a[72:75], v[88:91]\n" \
"ds_read_b64_tr_b16 a[104:105], v215 offset:16384\n" \
"ds_read_b64_tr_b16 a[106:107], v215 offset:18432\n" \
"v_mfma_f32_16x16x32_bf16 v[88:91], a[116:119], a[76:79], v[88:91]\n" \
"ds_read_b128 a[72:75], v213 offset:256\n" \
"v_mfma_f32_16x16x32_bf16 v[104:107], a[112:115], a[80:83], v[104:107]\n" \
"s_sub_u32 s86, s86, 32768\n" \
"s_cselect_b32 s86, 131072, s86\n" \
"ds_read_b128 a[76:79], v213 offset:2304\n" \
"v_mfma_f32_16x16x32_bf16 v[104:107], a[116:119], a[84:87], v[104:107]\n" \
"buffer_load_dwordx4 v218, s[36:39], s82 offen offset:2048 lds\n" \
"v_mfma_f32_16x16x32_bf16 v[108:111], a[120:123], a[80:83], v[108:111]\n" \
"ds_read_b64_tr_b16 a[108:109], v215 offset:24576\n" \
"ds_read_b64_tr_b16 a[110:111], v215 offset:26624\n" \
"v_mfma_f32_16x16x32_bf16 v[108:111], a[124:127], a[84:87], v[108:111]\n" \
"buffer_load_dwordx4 v219, s[36:39], s82 offen offset:3072 lds\n" \
"v_mfma_f32_16x16x32_bf16 v[124:127], a[120:123], a[88:91], v[124:127]\n" \
"ds_read_b128 a[80:83], v213 offset:4096\n" \
"s_waitcnt lgkmcnt(4)\n" \
"v_mfma_f32_16x16x32_bf16 v[124:127], a[124:127], a[92:95], v[124:127]\n" \
"s_sub_u32 s72, s72, 1\n" \
"ds_read_b128 a[84:87], v213 offset:6144\n" \
"v_mfma_f32_16x16x32_bf16 v[120:123], a[112:115], a[88:91], v[120:123]\n" \
"v_mfma_f32_16x16x32_bf16 v[120:123], a[116:119], a[92:95], v[120:123]\n" \
"s_cbranch_scc0 Lo64_%=\n" \
"v_mfma_f32_16x16x32_bf16 v[64:67], a[96:99], a[64:67], v[64:67]\n" \
"s_waitcnt lgkmcnt(2)\n" \
"ds_read_b128 a[88:91], v213 offset:4352\n" \
"v_mfma_f32_16x16x32_bf16 v[64:67], a[100:103], a[68:71], v[64:67]\n" \
"s_add_u32 s48, s86, 32768\n" \
"s_cmp_eq_u32 s48, 163840\n" \
"s_cselect_b32 s48, 0, s48\n" \
"ds_read_b128 a[92:95], v213 offset:6400\n" \
"v_mfma_f32_16x16x32_bf16 v[68:71], a[104:107], a[64:67], v[68:71]\n" \
"ds_read_b64_tr_b16 a[112:113], v214 offset:16896\n" \
"v_mfma_f32_16x16x32_bf16 v[68:71], a[108:111], a[68:71], v[68:71]\n" \
"s_add_u32 s49, s48, 32768\n" \
"s_cmp_eq_u32 s49, 163840\n" \
"s_cselect_b32 s49, 0, s49\n" \
"ds_read_b64_tr_b16 a[114:115], v214 offset:18944\n" \
"v_mfma_f32_16x16x32_bf16 v[84:87], a[104:107], a[72:75], v[84:87]\n" \
"ds_read_b64_tr_b16 a[116:117], v214 offset:25088\n" \
"v_add_u32 v224, s49, v211\n" \
"v_mfma_f32_16x16x32_bf16 v[84:87], a[108:111], a[76:79], v[84:87]\n" \
"v_add_u32 v225, s49, v212\n" \
"v_xor_b32 v226, 32, v225\n" \
"v_mfma_f32_16x16x32_bf16 v[80:83], a[96:99], a[72:75], v[80:83]\n" \
"s_sub_u32 s85, s85, 32768\n" \
"s_cselect_b32 s85, 131072, s85\n" \
"ds_read_b64_tr_b16 a[118:119], v214 offset:27136\n" \
"v_mfma_f32_16x16x32_bf16 v[80:83], a[100:103], a[76:79], v[80:83]\n" \
"s_waitcnt lgkmcnt(3)\n" \
"ds_read_b64_tr_b16 a[120:121], v215 offset:16896\n" \
"v_add_u32 v227, s48, v211\n" \
"v_mfma_f32_16x16x32_bf16 v[96:99], a[96:99], a[80:83], v[96:99]\n" \
"ds_read_b64_tr_b16 a[122:123], v215 offset:18944\n" \
"v_add_u32 v228, s48, v212\n" \
"v_mfma_f32_16x16x32_bf16 v[96:99], a[100:103], a[84:87], v[96:99]\n" \
"v_xor_b32 v229, 32, v228\n" \
"v_mfma_f32_16x16x32_bf16 v[100:103], a[104:107], a[80:83], v[100:103]\n" \
"ds_read_b64_tr_b16 a[124:125], v215 offset:25088\n" \
"v_add_u32 v213, s85, v211\n" \
"v_mfma_f32_16x16x32_bf16 v[100:103], a[108:111], a[84:87], v[100:103]\n" \
"v_mfma_f32_16x16x32_bf16 v[116:119], a[104:107], a[88:91], v[116:119]\n" \
"ds_read_b64_tr_b16 a[126:127], v215 offset:27136\n" \
"v_mfma_f32_16x16x32_bf16 v[116:119], a[108:111], a[92:95], v[116:119]\n" \
"v_mfma_f32_16x16x32_bf16 v[112:115], a[96:99], a[88:91], v[112:115]\n" \
"v_add_u32 v214, s85, v212\n" \
"v_xor_b32 v215, 32, v214\n" \
"v_mfma_f32_16x16x32_bf16 v[112:115], a[100:103], a[92:95], v[112:115]\n" \
"s_waitcnt vmcnt(8) lgkmcnt(0)\n" \
"s_barrier\n" \
"v_mfma_f32_16x16x32_bf16 v[72:75], a[112:115], a[64:67], v[72:75]\n" \
"ds_read_b128 a[0:3], v213 offset:0\n" \
"ds_read_b128 a[4:7], v213 offset:2048\n" \
"v_mfma_f32_16x16x32_bf16 v[72:75], a[116:119], a[68:71], v[72:75]\n" \
"s_sub_u32 s68, s86, 32768\n" \
"s_cselect_b32 s68, 131072, s68\n" \
"ds_read_b64_tr_b16 a[128:129], v214 offset:16384\n" \
"ds_read_b64_tr_b16 a[130:131], v214 offset:18432\n" \
"v_mfma_f32_16x16x32_bf16 v[76:79], a[120:123], a[64:67], v[76:79]\n" \
"ds_read_b64_tr_b16 a[132:133], v214 offset:24576\n" \
"ds_read_b64_tr_b16 a[134:135], v214 offset:26624\n" \
"v_mfma_f32_16x16x32_bf16 v[76:79], a[124:127], a[68:71], v[76:79]\n" \
"s_cmp_lt_u32 s61, 2\n" \
"s_cselect_b32 s68, s86, s68\n" \
"ds_read_b64_tr_b16 a[136:137], v215 offset:16384\n" \
"ds_read_b64_tr_b16 a[138:139], v215 offset:18432\n" \
"v_mfma_f32_16x16x32_bf16 v[92:95], a[120:123], a[72:75], v[92:95]\n" \
"s_and_b32 s50, s61, 1\n" \
"ds_read_b64_tr_b16 a[140:141], v215 offset:24576\n" \
"ds_read_b64_tr_b16 a[142:143], v215 offset:26624\n" \
"v_mfma_f32_16x16x32_bf16 v[92:95], a[124:127], a[76:79], v[92:95]\n" \
"s_mul_i32 s50, s50, 9216\n" \
"ds_read_b64_tr_b16 a[144:145], v214 offset:16896\n" \
"ds_read_b64_tr_b16 a[146:147], v214 offset:18944\n" \
"v_mfma_f32_16x16x32_bf16 v[88:91], a[112:115], a[72:75], v[88:91]\n" \
"s_add_u32 s68, s68, s50\n" \
"ds_read_b64_tr_b16 a[148:149], v214 offset:25088\n" \
"ds_read_b64_tr_b16 a[150:151], v214 offset:27136\n" \
"v_mfma_f32_16x16x32_bf16 v[88:91], a[116:119], a[76:79], v[88:91]\n" \
"s_add_u32 s68, s68, %3\n" \
"ds_read_b64_tr_b16 a[152:153], v215 offset:16896\n" \
"ds_read_b64_tr_b16 a[154:155], v215 offset:18944\n" \
"v_mfma_f32_16x16x32_bf16 v[104:107], a[112:115], a[80:83], v[104:107]\n" \
"ds_read_b64_tr_b16 a[156:157], v215 offset:25088\n" \
"ds_read_b64_tr_b16 a[158:159], v215 offset:27136\n" \
"v_mfma_f32_16x16x32_bf16 v[104:107], a[116:119], a[84:87], v[104:107]\n" \
"ds_read_b128 a[24:27], v213 offset:256\n" \
"ds_read_b128 a[28:31], v213 offset:2304\n" \
"v_mfma_f32_16x16x32_bf16 v[108:111], a[120:123], a[80:83], v[108:111]\n" \
"ds_read_b128 a[8:11], v224 offset:0\n" \
"ds_read_b128 a[12:15], v224 offset:2048\n" \
"v_mfma_f32_16x16x32_bf16 v[108:111], a[124:127], a[84:87], v[108:111]\n" \
"ds_read_b64_tr_b16 a[160:161], v225 offset:16384\n" \
"ds_read_b64_tr_b16 a[162:163], v225 offset:18432\n" \
"v_mfma_f32_16x16x32_bf16 v[124:127], a[120:123], a[88:91], v[124:127]\n" \
"ds_read_b64_tr_b16 a[164:165], v225 offset:24576\n" \
"ds_read_b64_tr_b16 a[166:167], v225 offset:26624\n" \
"v_mfma_f32_16x16x32_bf16 v[124:127], a[124:127], a[92:95], v[124:127]\n" \
"ds_read_b64_tr_b16 a[168:169], v226 offset:16384\n" \
"ds_read_b64_tr_b16 a[170:171], v226 offset:18432\n" \
"v_mfma_f32_16x16x32_bf16 v[120:123], a[112:115], a[88:91], v[120:123]\n" \
"ds_read_b64_tr_b16 a[172:173], v226 offset:24576\n" \
"ds_read_b64_tr_b16 a[174:175], v226 offset:26624\n" \
"v_mfma_f32_16x16x32_bf16 v[120:123], a[116:119], a[92:95], v[120:123]\n" \
"s_waitcnt lgkmcnt(8)\n" \
"ds_read_b64_tr_b16 a[176:177], v225 offset:16896\n" \
"ds_read_b64_tr_b16 a[178:179], v225 offset:18944\n" \
"v_mfma_f32_16x16x32_bf16 v[64:67], a[128:131], a[0:3], v[64:67]\n" \
"ds_read_b64_tr_b16 a[180:181], v225 offset:25088\n" \
"ds_read_b64_tr_b16 a[182:183], v225 offset:27136\n" \
"v_mfma_f32_16x16x32_bf16 v[64:67], a[132:135], a[4:7], v[64:67]\n" \
"ds_read_b64_tr_b16 a[184:185], v226 offset:16896\n" \
"ds_read_b64_tr_b16 a[186:187], v226 offset:18944\n" \
"v_mfma_f32_16x16x32_bf16 v[68:71], a[136:139], a[0:3], v[68:71]\n" \
"ds_read_b64_tr_b16 a[188:189], v226 offset:25088\n" \
"ds_read_b64_tr_b16 a[190:191], v226 offset:27136\n" \
"v_mfma_f32_16x16x32_bf16 v[68:71], a[140:143], a[4:7], v[68:71]\n" \
"v_lshrrev_b32 v132, 4, v234\n" \
"v_and_b32 v133, 15, v132\n" \
"v_mfma_f32_16x16x32_bf16 v[72:75], a[144:147], a[0:3], v[72:75]\n" \
"v_mul_u32_u24 v128, 144, v133\n" \
"v_lshrrev_b32 v133, 4, v132\n" \
"v_mfma_f32_16x16x32_bf16 v[72:75], a[148:151], a[4:7], v[72:75]\n" \
"v_lshl_add_u32 v128, v133, 3, v128\n" \
"v_add_u32 v128, s68, v128\n" \
"v_mfma_f32_16x16x32_bf16 v[76:79], a[152:155], a[0:3], v[76:79]\n" \
"v_lshrrev_b32 v133, 3, v132\n" \
"v_and_b32 v134, 7, v132\n" \
"v_mfma_f32_16x16x32_bf16 v[76:79], a[156:159], a[4:7], v[76:79]\n" \
"v_mul_u32_u24 v129, 144, v133\n" \
"v_lshl_add_u32 v129, v134, 4, v129\n" \
"v_mfma_f32_16x16x32_bf16 v[92:95], a[152:155], a[24:27], v[92:95]\n" \
"v_add_u32 v129, s68, v129\n" \
"v_lshlrev_b32 v134, 4, v134\n" \
"v_mfma_f32_16x16x32_bf16 v[92:95], a[156:159], a[28:31], v[92:95]\n" \
"v_lshl_add_u32 v130, v133, 13, v134\n" \
"v_mfma_f32_16x16x32_bf16 v[88:91], a[144:147], a[24:27], v[88:91]\n" \
"v_mfma_f32_16x16x32_bf16 v[88:91], a[148:151], a[28:31], v[88:91]\n" \
"v_mfma_f32_16x16x32_bf16 v[84:87], a[136:139], a[24:27], v[84:87]\n" \
"s_waitcnt vmcnt(0)\n" \
"s_barrier\n" \
"v_mfma_f32_16x16x32_bf16 v[84:87], a[140:143], a[28:31], v[84:87]\n" \
"ds_read_b128 a[32:35], v224 offset:256\n" \
"ds_read_b128 a[36:39], v224 offset:2304\n" \
"v_mfma_f32_16x16x32_bf16 v[80:83], a[128:131], a[24:27], v[80:83]\n" \
"ds_read_b128 a[16:19], v227 offset:0\n" \
"ds_read_b128 a[20:23], v227 offset:2048\n" \
"v_mfma_f32_16x16x32_bf16 v[80:83], a[132:135], a[28:31], v[80:83]\n" \
"s_waitcnt lgkmcnt(4)\n" \
"ds_read_b64_tr_b16 a[192:193], v228 offset:16384\n" \
"ds_read_b64_tr_b16 a[194:195], v228 offset:18432\n" \
"v_mfma_f32_16x16x32_bf16 v[64:67], a[160:163], a[8:11], v[64:67]\n" \
"ds_read_b64_tr_b16 a[196:197], v228 offset:24576\n" \
"ds_read_b64_tr_b16 a[198:199], v228 offset:26624\n" \
"v_mfma_f32_16x16x32_bf16 v[64:67], a[164:167], a[12:15], v[64:67]\n" \
"ds_read_b64_tr_b16 a[200:201], v229 offset:16384\n" \
"ds_read_b64_tr_b16 a[202:203], v229 offset:18432\n" \
"v_mfma_f32_16x16x32_bf16 v[68:71], a[168:171], a[8:11], v[68:71]\n" \
"ds_read_b64_tr_b16 a[204:205], v229 offset:24576\n" \
"ds_read_b64_tr_b16 a[206:207], v229 offset:26624\n" \
"v_mfma_f32_16x16x32_bf16 v[68:71], a[172:175], a[12:15], v[68:71]\n" \
"ds_read_b64_tr_b16 a[208:209], v228 offset:16896\n" \
"ds_read_b64_tr_b16 a[210:211], v228 offset:18944\n" \
"v_mfma_f32_16x16x32_bf16 v[72:75], a[176:179], a[8:11], v[72:75]\n" \
"ds_read_b64_tr_b16 a[212:213], v228 offset:25088\n" \
"ds_read_b64_tr_b16 a[214:215], v228 offset:27136\n" \
"v_mfma_f32_16x16x32_bf16 v[72:75], a[180:183], a[12:15], v[72:75]\n" \
"ds_read_b64_tr_b16 a[216:217], v229 offset:16896\n" \
"ds_read_b64_tr_b16 a[218:219], v229 offset:18944\n" \
"v_mfma_f32_16x16x32_bf16 v[76:79], a[184:187], a[8:11], v[76:79]\n" \
"ds_read_b64_tr_b16 a[220:221], v229 offset:25088\n" \
"ds_read_b64_tr_b16 a[222:223], v229 offset:27136\n" \
"v_mfma_f32_16x16x32_bf16 v[76:79], a[188:191], a[12:15], v[76:79]\n" \
"s_waitcnt lgkmcnt(8)\n" \
"ds_read_b128 a[40:43], v227 offset:256\n" \
"ds_read_b128 a[44:47], v227 offset:2304\n" \
"v_mfma_f32_16x16x32_bf16 v[92:95], a[184:187], a[32:35], v[92:95]\n" \
"ds_read_b128 a[248:251], v213 offset:4096\n" \
"ds_read_b128 a[252:255], v213 offset:6144\n" \
"v_mfma_f32_16x16x32_bf16 v[92:95], a[188:191], a[36:39], v[92:95]\n" \
"ds_read_b128 a[48:51], v224 offset:4096\n" \
"ds_read_b128 a[52:55], v224 offset:6144\n" \
"v_mfma_f32_16x16x32_bf16 v[88:91], a[176:179], a[32:35], v[88:91]\n" \
"ds_read_b128 a[56:59], v227 offset:4096\n" \
"ds_read_b128 a[60:63], v227 offset:6144\n" \
"v_mfma_f32_16x16x32_bf16 v[88:91], a[180:183], a[36:39], v[88:91]\n" \
"ds_read_b128 a[224:227], v213 offset:4352\n" \
"ds_read_b128 a[228:231], v213 offset:6400\n" \
"v_mfma_f32_16x16x32_bf16 v[84:87], a[168:171], a[32:35], v[84:87]\n" \
"ds_read_b128 a[232:235], v224 offset:4352\n" \
"ds_read_b128 a[236:239], v224 offset:6400\n" \
"v_mfma_f32_16x16x32_bf16 v[84:87], a[172:175], a[36:39], v[84:87]\n" \
"ds_read_b128 a[240:243], v227 offset:4352\n" \
"ds_read_b128 a[244:247], v227 offset:6400\n" \
"v_mfma_f32_16x16x32_bf16 v[80:83], a[160:163], a[32:35], v[80:83]\n" \
"v_mfma_f32_16x16x32_bf16 v[80:83], a[164:167], a[36:39], v[80:83]\n" \
"v_mfma_f32_16x16x32_bf16 v[64:67], a[192:195], a[16:19], v[64:67]\n" \
"v_mfma_f32_16x16x32_bf16 v[64:67], a[196:199], a[20:23], v[64:67]\n" \
"v_mfma_f32_16x16x32_bf16 v[68:71], a[200:203], a[16:19], v[68:71]\n" \
"v_mfma_f32_16x16x32_bf16 v[68:71], a[204:207], a[20:23], v[68:71]\n" \
"s_waitcnt lgkmcnt(0)\n" \
"v_mfma_f32_16x16x32_bf16 v[72:75], a[208:211], a[16:19], v[72:75]\n" \
"v_mfma_f32_16x16x32_bf16 v[72:75], a[212:215], a[20:23], v[72:75]\n" \
"v_cvt_pk_bf16_f32 v136, v64, v65\n" \
"v_cvt_pk_bf16_f32 v137, v66, v67\n" \
"v_mfma_f32_16x16x32_bf16 v[76:79], a[216:219], a[16:19], v[76:79]\n" \
"v_mfma_f32_16x16x32_bf16 v[76:79], a[220:223], a[20:23], v[76:79]\n" \
"ds_write_b64 v128, v[136:137] offset:0\n" \
"v_mfma_f32_16x16x32_bf16 v[80:83], a[192:195], a[40:43], v[80:83]\n" \
"v_cvt_pk_bf16_f32 v138, v68, v69\n" \
"v_cvt_pk_bf16_f32 v139, v70, v71\n" \
"v_mfma_f32_16x16x32_bf16 v[80:83], a[196:199], a[44:47], v[80:83]\n" \
"ds_write_b64 v128, v[138:139] offset:32\n" \
"v_mfma_f32_16x16x32_bf16 v[84:87], a[200:203], a[40:43], v[84:87]\n" \
"v_cvt_pk_bf16_f32 v136, v72, v73\n" \
"v_cvt_pk_bf16_f32 v137, v74, v75\n" \
"v_mfma_f32_16x16x32_bf16 v[84:87], a[204:207], a[44:47], v[84:87]\n" \
"ds_write_b64 v128, v[136:137] offset:64\n" \
"v_mfma_f32_16x16x32_bf16 v[88:91], a[208:211], a[40:43], v[88:91]\n" \
"v_cvt_pk_bf16_f32 v138, v76, v77\n" \
"v_cvt_pk_bf16_f32 v139, v78, v79\n" \
"v_mfma_f32_16x16x32_bf16 v[88:91], a[212:215], a[44:47], v[88:91]\n" \
"ds_write_b64 v128, v[138:139] offset:96\n" \
"v_mfma_f32_16x16x32_bf16 v[92:95], a[216:219], a[40:43], v[92:95]\n" \
"ds_read_b128 v[140:143], v129 offset:0\n" \
"ds_read_b128 v[144:147], v129 offset:1152\n" \
"v_mfma_f32_16x16x32_bf16 v[92:95], a[220:223], a[44:47], v[92:95]\n" \
"v_cvt_pk_bf16_f32 v136, v80, v81\n" \
"v_cvt_pk_bf16_f32 v137, v82, v83\n" \
"v_mfma_f32_16x16x32_bf16 v[96:99], a[128:131], a[248:251], v[96:99]\n" \
"ds_write_b64 v128, v[136:137] offset:2304\n" \
"v_mfma_f32_16x16x32_bf16 v[96:99], a[132:135], a[252:255], v[96:99]\n" \
"s_add_u32 s70, s88, 0\n" \
"s_waitcnt lgkmcnt(2)\n" \
"v_cvt_pk_bf16_f32 v138, v84, v85\n" \
"v_mfma_f32_16x16x32_bf16 v[96:99], a[160:163], a[48:51], v[96:99]\n" \
"buffer_store_dwordx4 v[140:143], v130, s[52:55], s70 offen sc1\n" \
"v_mfma_f32_16x16x32_bf16 v[96:99], a[164:167], a[52:55], v[96:99]\n" \
"v_cvt_pk_bf16_f32 v139, v86, v87\n" \
"v_mfma_f32_16x16x32_bf16 v[96:99], a[192:195], a[56:59], v[96:99]\n" \
"s_add_u32 s70, s88, 65536\n" \
"s_waitcnt lgkmcnt(1)\n" \
"v_mfma_f32_16x16x32_bf16 v[96:99], a[196:199], a[60:63], v[96:99]\n" \
"buffer_store_dwordx4 v[144:147], v130, s[52:55], s70 offen sc1\n" \
"ds_write_b64 v128, v[138:139] offset:2336\n" \
"v_mfma_f32_16x16x32_bf16 v[100:103], a[204:207], a[60:63], v[100:103]\n" \
"v_cvt_pk_bf16_f32 v136, v88, v89\n" \
"v_cvt_pk_bf16_f32 v137, v90, v91\n" \
"v_mfma_f32_16x16x32_bf16 v[100:103], a[200:203], a[56:59], v[100:103]\n" \
"ds_write_b64 v128, v[136:137] offset:2368\n" \
"v_mfma_f32_16x16x32_bf16 v[100:103], a[172:175], a[52:55], v[100:103]\n" \
"v_cvt_pk_bf16_f32 v138, v92, v93\n" \
"v_cvt_pk_bf16_f32 v139, v94, v95\n" \
"v_mfma_f32_16x16x32_bf16 v[100:103], a[168:171], a[48:51], v[100:103]\n" \
"v_mfma_f32_16x16x32_bf16 v[100:103], a[140:143], a[252:255], v[100:103]\n" \
"v_mfma_f32_16x16x32_bf16 v[100:103], a[136:139], a[248:251], v[100:103]\n" \
"ds_write_b64 v128, v[138:139] offset:2400\n" \
"v_mfma_f32_16x16x32_bf16 v[104:107], a[144:147], a[248:251], v[104:107]\n" \
"ds_read_b128 v[148:151], v129 offset:2304\n" \
"ds_read_b128 v[152:155], v129 offset:3456\n" \
"v_mfma_f32_16x16x32_bf16 v[104:107], a[148:151], a[252:255], v[104:107]\n" \
"v_cvt_pk_bf16_f32 v136, v96, v97\n" \
"v_cvt_pk_bf16_f32 v137, v98, v99\n" \
"v_mfma_f32_16x16x32_bf16 v[104:107], a[176:179], a[48:51], v[104:107]\n" \
"ds_write_b64 v128, v[136:137] offset:4608\n" \
"v_mfma_f32_16x16x32_bf16 v[104:107], a[180:183], a[52:55], v[104:107]\n" \
"v_cvt_pk_bf16_f32 v138, v100, v101\n" \
"v_cvt_pk_bf16_f32 v139, v102, v103\n" \
"v_mfma_f32_16x16x32_bf16 v[104:107], a[208:211], a[56:59], v[104:107]\n" \
"s_add_u32 s70, s88, 4096\n" \
"s_waitcnt lgkmcnt(2)\n" \
"v_mfma_f32_16x16x32_bf16 v[104:107], a[212:215], a[60:63], v[104:107]\n" \
"buffer_store_dwordx4 v[148:151], v130, s[52:55], s70 offen sc1\n" \
"ds_write_b64 v128, v[138:139] offset:4640\n" \
"v_mfma_f32_16x16x32_bf16 v[108:111], a[220:223], a[60:63], v[108:111]\n" \
"s_add_u32 s70, s88, 69632\n" \
"s_waitcnt lgkmcnt(2)\n" \
"v_mfma_f32_16x16x32_bf16 v[108:111], a[216:219], a[56:59], v[108:111]\n" \
"buffer_store_dwordx4 v[152:155], v130, s[52:55], s70 offen sc1\n" \
"v_mfma_f32_16x16x32_bf16 v[108:111], a[188:191], a[52:55], v[108:111]\n" \
"v_mfma_f32_16x16x32_bf16 v[108:111], a[184:187], a[48:51], v[108:111]\n" \
"v_cvt_pk_bf16_f32 v136, v104, v105\n" \
"v_cvt_pk_bf16_f32 v137, v106, v107\n" \
"v_mfma_f32_16x16x32_bf16 v[108:111], a[156:159], a[252:255], v[108:111]\n" \
"v_mfma_f32_16x16x32_bf16 v[108:111], a[152:155], a[248:251], v[108:111]\n" \
"ds_write_b64 v128, v[136:137] offset:4672\n" \
"v_mfma_f32_16x16x32_bf16 v[112:115], a[128:131], a[224:227], v[112:115]\n" \
"v_mfma_f32_16x16x32_bf16 v[112:115], a[132:135], a[228:231], v[112:115]\n" \
"v_mfma_f32_16x16x32_bf16 v[112:115], a[160:163], a[232:235], v[112:115]\n" \
"v_mfma_f32_16x16x32_bf16 v[112:115], a[164:167], a[236:239], v[112:115]\n" \
"v_cvt_pk_bf16_f32 v138, v108, v109\n" \
"v_cvt_pk_bf16_f32 v139, v110, v111\n" \
"v_mfma_f32_16x16x32_bf16 v[112:115], a[192:195], a[240:243], v[112:115]\n" \
"v_mfma_f32_16x16x32_bf16 v[112:115], a[196:199], a[244:247], v[112:115]\n" \
"ds_write_b64 v128, v[138:139] offset:4704\n" \
"v_mfma_f32_16x16x32_bf16 v[116:119], a[204:207], a[244:247], v[116:119]\n" \
"ds_read_b128 v[156:159], v129 offset:4608\n" \
"ds_read_b128 v[160:163], v129 offset:5760\n" \
"v_mfma_f32_16x16x32_bf16 v[116:119], a[200:203], a[240:243], v[116:119]\n" \
"v_mfma_f32_16x16x32_bf16 v[116:119], a[172:175], a[236:239], v[116:119]\n" \
"v_mfma_f32_16x16x32_bf16 v[116:119], a[168:171], a[232:235], v[116:119]\n" \
"v_cvt_pk_bf16_f32 v136, v112, v113\n" \
"v_cvt_pk_bf16_f32 v137, v114, v115\n" \
"v_mfma_f32_16x16x32_bf16 v[116:119], a[140:143], a[228:231], v[116:119]\n" \
"s_add_u32 s70, s88, 131072\n" \
"s_waitcnt lgkmcnt(1)\n" \
"v_mfma_f32_16x16x32_bf16 v[116:119], a[136:139], a[224:227], v[116:119]\n" \
"buffer_store_dwordx4 v[156:159], v130, s[52:55], s70 offen sc1\n" \
"ds_write_b64 v128, v[136:137] offset:6912\n" \
"v_mfma_f32_16x16x32_bf16 v[120:123], a[144:147], a[224:227], v[120:123]\n" \
"s_add_u32 s70, s88, 196608\n" \
"s_waitcnt lgkmcnt(1)\n" \
"v_mfma_f32_16x16x32_bf16 v[120:123], a[148:151], a[228:231], v[120:123]\n" \
"buffer_store_dwordx4 v[160:163], v130, s[52:55], s70 offen sc1\n" \
"v_mfma_f32_16x16x32_bf16 v[120:123], a[176:179], a[232:235], v[120:123]\n" \
"v_mfma_f32_16x16x32_bf16 v[120:123], a[180:183], a[236:239], v[120:123]\n" \
"v_cvt_pk_bf16_f32 v138, v116, v117\n" \
"v_cvt_pk_bf16_f32 v139, v118, v119\n" \
"v_mfma_f32_16x16x32_bf16 v[120:123], a[208:211], a[240:243], v[120:123]\n" \
"v_mfma_f32_16x16x32_bf16 v[120:123], a[212:215], a[244:247], v[120:123]\n" \
"ds_write_b64 v128, v[138:139] offset:6944\n" \
"v_mfma_f32_16x16x32_bf16 v[124:127], a[220:223], a[244:247], v[124:127]\n" \
"v_mfma_f32_16x16x32_bf16 v[124:127], a[216:219], a[240:243], v[124:127]\n" \
"v_mfma_f32_16x16x32_bf16 v[124:127], a[188:191], a[236:239], v[124:127]\n" \
"v_mfma_f32_16x16x32_bf16 v[124:127], a[184:187], a[232:235], v[124:127]\n" \
"v_cvt_pk_bf16_f32 v136, v120, v121\n" \
"v_cvt_pk_bf16_f32 v137, v122, v123\n" \
"v_mfma_f32_16x16x32_bf16 v[124:127], a[156:159], a[228:231], v[124:127]\n" \
"v_mfma_f32_16x16x32_bf16 v[124:127], a[152:155], a[224:227], v[124:127]\n" \
"ds_write_b64 v128, v[136:137] offset:6976\n" \
"s_nop 7\n" \
"v_cvt_pk_bf16_f32 v138, v124, v125\n" \
"v_cvt_pk_bf16_f32 v139, v126, v127\n" \
"ds_write_b64 v128, v[138:139] offset:7008\n" \
"ds_read_b128 v[164:167], v129 offset:6912\n" \
"ds_read_b128 v[168:171], v129 offset:8064\n" \
"s_add_u32 s70, s88, 135168\n" \
"s_waitcnt lgkmcnt(1)\n" \
"buffer_store_dwordx4 v[164:167], v130, s[52:55], s70 offen sc1\n" \
"s_add_u32 s70, s88, 200704\n" \
"s_waitcnt lgkmcnt(0)\n" \
"buffer_store_dwordx4 v[168:171], v130, s[52:55], s70 offen sc1\n" \
"s_endpgm\n" \
"Lhot_end_%=:\n" \
"Lpl1_%=:\n" \
"s_mov_b32 s72, 0\n" \
"Lpk1_%=:\n" \
"s_sleep 2\n" \
"buffer_load_dword v227, v242, s[56:59], s90 offen sc1\n" \
"s_waitcnt vmcnt(0)\n" \
"v_cmp_eq_u32 vcc, s60, v227\n" \
"s_nop 1\n" \
"s_andn2_b32 s70, 0xff, vcc_lo\n" \
"s_cbranch_scc0 Lpr1_%=\n" \
"s_add_u32 s72, s72, 1\n" \
"s_cmp_lt_u32 s72, 0x40000\n" \
"s_cbranch_scc1 Lpk1_%=\n" \
"s_branch Lpr1_%=\n" \
"Lpl2_%=:\n" \
"s_mov_b32 s72, 0\n" \
"Lpk2_%=:\n" \
"s_sleep 2\n" \
"buffer_load_dword v239, v242, s[56:59], s95 offen sc1\n" \
"s_waitcnt vmcnt(0)\n" \
"v_cmp_eq_u32 vcc, s60, v239\n" \
"s_nop 1\n" \
"s_andn2_b32 s70, s69, vcc_lo\n" \
"s_cbranch_scc0 Lpr2_%=\n" \
"s_add_u32 s72, s72, 1\n" \
"s_cmp_lt_u32 s72, 0x40000\n" \
"s_cbranch_scc1 Lpk2_%=\n" \
"s_branch Lpr2_%=\n" \
"Lpl3_%=:\n" \
"s_mov_b32 s72, 0\n" \
"Lpk3_%=:\n" \
"s_sleep 2\n" \
"buffer_load_dword v239, v242, s[56:59], s95 offen sc1\n" \
"s_waitcnt vmcnt(0)\n" \
"v_cmp_eq_u32 vcc, s60, v239\n" \
"s_nop 1\n" \
"s_andn2_b32 s70, s69, vcc_lo\n" \
"s_cbranch_scc0 Lpr3_%=\n" \
"s_add_u32 s72, s72, 1\n" \
"s_cmp_lt_u32 s72, 0x40000\n" \
"s_cbranch_scc1 Lpk3_%=\n" \
"s_branch Lpr3_%=\n" \
"Lexact_%=:\n" \
"v_mov_b32 v239, 0xff800000\n" \
"s_and_b32 s70, s61, 1\n" \
"s_lshl_b32 s70, s70, 4\n" \
"v_add_u32 v186, s70, v238\n" \
"v_accvgpr_read_b32 v160, a128\n" \
"v_accvgpr_read_b32 v161, a129\n" \
"v_accvgpr_read_b32 v162, a130\n" \
"v_accvgpr_read_b32 v163, a131\n" \
"v_accvgpr_read_b32 v164, a132\n" \
"v_accvgpr_read_b32 v165, a133\n" \
"v_accvgpr_read_b32 v166, a134\n" \
"v_accvgpr_read_b32 v167, a135\n" \
"v_accvgpr_read_b32 v168, a136\n" \
"v_accvgpr_read_b32 v169, a137\n" \
"v_accvgpr_read_b32 v170, a138\n" \
"v_accvgpr_read_b32 v171, a139\n" \
"v_accvgpr_read_b32 v172, a140\n" \
"v_accvgpr_read_b32 v173, a141\n" \
"v_accvgpr_read_b32 v174, a142\n" \
"v_accvgpr_read_b32 v175, a143\n" \
"s_add_u32 s83, s85, 0\n" \
"s_add_u32 s84, s87, 0\n" \
"s_call_b64 s[64:65], Lex1_%=\n" \
"v_accvgpr_read_b32 v160, a144\n" \
"v_accvgpr_read_b32 v161, a145\n" \
"v_accvgpr_read_b32 v162, a146\n" \
"v_accvgpr_read_b32 v163, a147\n" \
"v_accvgpr_read_b32 v164, a148\n" \
"v_accvgpr_read_b32 v165, a149\n" \
"v_accvgpr_read_b32 v166, a150\n" \
"v_accvgpr_read_b32 v167, a151\n" \
"v_accvgpr_read_b32 v168, a152\n" \
"v_accvgpr_read_b32 v169, a153\n" \
"v_accvgpr_read_b32 v170, a154\n" \
"v_accvgpr_read_b32 v171, a155\n" \
"v_accvgpr_read_b32 v172, a156\n" \
"v_accvgpr_read_b32 v173, a157\n" \
"v_accvgpr_read_b32 v174, a158\n" \
"v_accvgpr_read_b32 v175, a159\n" \
"s_add_u32 s83, s85, 2\n" \
"s_add_u32 s84, s87, 532480\n" \
"s_call_b64 s[64:65], Lex1_%=\n" \
"v_accvgpr_read_b32 v160, a160\n" \
"v_accvgpr_read_b32 v161, a161\n" \
"v_accvgpr_read_b32 v162, a162\n" \
"v_accvgpr_read_b32 v163, a163\n" \
"v_accvgpr_read_b32 v164, a164\n" \
"v_accvgpr_read_b32 v165, a165\n" \
"v_accvgpr_read_b32 v166, a166\n" \
"v_accvgpr_read_b32 v167, a167\n" \
"v_accvgpr_read_b32 v168, a168\n" \
"v_accvgpr_read_b32 v169, a169\n" \
"v_accvgpr_read_b32 v170, a170\n" \
"v_accvgpr_read_b32 v171, a171\n" \
"v_accvgpr_read_b32 v172, a172\n" \
"v_accvgpr_read_b32 v173, a173\n" \
"v_accvgpr_read_b32 v174, a174\n" \
"v_accvgpr_read_b32 v175, a175\n" \
"s_add_u32 s83, s86, 0\n" \
"s_add_u32 s84, s88, 0\n" \
"s_call_b64 s[64:65], Lex1_%=\n" \
"v_accvgpr_read_b32 v160, a176\n" \
"v_accvgpr_read_b32 v161, a177\n" \
"v_accvgpr_read_b32 v162, a178\n" \
"v_accvgpr_read_b32 v163, a179\n" \
"v_accvgpr_read_b32 v164, a180\n" \
"v_accvgpr_read_b32 v165, a181\n" \
"v_accvgpr_read_b32 v166, a182\n" \
"v_accvgpr_read_b32 v167, a183\n" \
"v_accvgpr_read_b32 v168, a184\n" \
"v_accvgpr_read_b32 v169, a185\n" \
"v_accvgpr_read_b32 v170, a186\n" \
"v_accvgpr_read_b32 v171, a187\n" \
"v_accvgpr_read_b32 v172, a188\n" \
"v_accvgpr_read_b32 v173, a189\n" \
"v_accvgpr_read_b32 v174, a190\n" \
"v_accvgpr_read_b32 v175, a191\n" \
"s_add_u32 s83, s86, 2\n" \
"s_add_u32 s84, s88, 532480\n" \
"s_call_b64 s[64:65], Lex1_%=\n" \
"s_waitcnt vmcnt(0)\n" \
"s_branch Lexr_%=\n" \
"Lex1_%=:\n" \
"v_mov_b32 v128, 0\n" \
"v_mov_b32 v129, 0\n" \
"v_mov_b32 v130, 0\n" \
"v_mov_b32 v131, 0\n" \
"v_mov_b32 v132, 0\n" \
"v_mov_b32 v133, 0\n" \
"v_mov_b32 v134, 0\n" \
"v_mov_b32 v135, 0\n" \
"v_mov_b32 v136, 0\n" \
"v_mov_b32 v137, 0\n" \
"v_mov_b32 v138, 0\n" \
"v_mov_b32 v139, 0\n" \
"v_mov_b32 v140, 0\n" \
"v_mov_b32 v141, 0\n" \
"v_mov_b32 v142, 0\n" \
"v_mov_b32 v143, 0\n" \
"v_mov_b32 v144, 0\n" \
"v_mov_b32 v145, 0\n" \
"v_mov_b32 v146, 0\n" \
"v_mov_b32 v147, 0\n" \
"v_mov_b32 v148, 0\n" \
"v_mov_b32 v149, 0\n" \
"v_mov_b32 v150, 0\n" \
"v_mov_b32 v151, 0\n" \
"v_mov_b32 v152, 0\n" \
"v_mov_b32 v153, 0\n" \
"v_mov_b32 v154, 0\n" \
"v_mov_b32 v155, 0\n" \
"v_mov_b32 v156, 0\n" \
"v_mov_b32 v157, 0\n" \
"v_mov_b32 v158, 0\n" \
"v_mov_b32 v159, 0\n" \
"v_mov_b32 v184, 0xf149f2ca\n" \
"v_mov_b32 v185, 0\n" \
"s_mov_b32 s81, 0\n" \
"s_mul_i32 s82, s62, 557824\n" \
"Lex2_%=:\n" \
"s_add_u32 s70, s82, 4096\n" \
"buffer_load_dwordx4 a[64:67], v236, s[56:59], s82 offen offset:0 sc1\n" \
"buffer_load_dwordx4 a[68:71], v236, s[56:59], s82 offen offset:2048 sc1\n" \
"buffer_load_dwordx4 a[72:75], v236, s[56:59], s70 offen offset:0 sc1\n" \
"buffer_load_dwordx4 a[76:79], v236, s[56:59], s70 offen offset:2048 sc1\n" \
"buffer_load_dwordx4 a[80:83], v236, s[56:59], s82 offen offset:256 sc1\n" \
"buffer_load_dwordx4 a[84:87], v236, s[56:59], s82 offen offset:2304 sc1\n" \
"buffer_load_dwordx4 a[88:91], v236, s[56:59], s70 offen offset:256 sc1\n" \
"buffer_load_dwordx4 a[92:95], v236, s[56:59], s70 offen offset:2304 sc1\n" \
"s_add_u32 s70, s82, 17850368\n" \
"s_add_u32 s71, s70, 4096\n" \
"buffer_load_dwordx4 a[96:99], v234, s[56:59], s70 offen offset:0 sc1\n" \
"buffer_load_dwordx4 a[100:103], v234, s[56:59], s70 offen offset:1024 sc1\n" \
"buffer_load_dwordx4 a[104:107], v234, s[56:59], s70 offen offset:2048 sc1\n" \
"buffer_load_dwordx4 a[108:111], v234, s[56:59], s70 offen offset:3072 sc1\n" \
"buffer_load_dwordx4 a[112:115], v234, s[56:59], s71 offen offset:0 sc1\n" \
"buffer_load_dwordx4 a[116:119], v234, s[56:59], s71 offen offset:1024 sc1\n" \
"buffer_load_dwordx4 a[120:123], v234, s[56:59], s71 offen offset:2048 sc1\n" \
"buffer_load_dwordx4 a[124:127], v234, s[56:59], s71 offen offset:3072 sc1\n" \
"s_waitcnt vmcnt(0)\n" \
"v_mfma_f32_16x16x32_bf16 v[176:179], a[64:67], v[160:163], 0\n" \
"v_mfma_f32_16x16x32_bf16 v[176:179], a[68:71], v[164:167], v[176:179]\n" \
"v_mfma_f32_16x16x32_bf16 v[176:179], a[72:75], v[168:171], v[176:179]\n" \
"v_mfma_f32_16x16x32_bf16 v[176:179], a[76:79], v[172:175], v[176:179]\n" \
"v_mfma_f32_16x16x32_bf16 v[180:183], a[80:83], v[160:163], 0\n" \
"v_mfma_f32_16x16x32_bf16 v[180:183], a[84:87], v[164:167], v[180:183]\n" \
"v_mfma_f32_16x16x32_bf16 v[180:183], a[88:91], v[168:171], v[180:183]\n" \
"v_mfma_f32_16x16x32_bf16 v[180:183], a[92:95], v[172:175], v[180:183]\n" \
"s_nop 7\n" \
"s_nop 1\n" \
"s_cmp_eq_u32 s81, s83\n" \
"s_cbranch_scc0 Lex3_%=\n" \
"v_cmp_gt_i32 vcc, 0, v186\n" \
"s_nop 1\n" \
"v_cndmask_b32 v176, v176, v239, vcc\n" \
"v_cmp_gt_i32 vcc, 1, v186\n" \
"s_nop 1\n" \
"v_cndmask_b32 v177, v177, v239, vcc\n" \
"v_cmp_gt_i32 vcc, 2, v186\n" \
"s_nop 1\n" \
"v_cndmask_b32 v178, v178, v239, vcc\n" \
"v_cmp_gt_i32 vcc, 3, v186\n" \
"s_nop 1\n" \
"v_cndmask_b32 v179, v179, v239, vcc\n" \
"v_cmp_gt_i32 vcc, 16, v186\n" \
"s_nop 1\n" \
"v_cndmask_b32 v180, v180, v239, vcc\n" \
"v_cmp_gt_i32 vcc, 17, v186\n" \
"s_nop 1\n" \
"v_cndmask_b32 v181, v181, v239, vcc\n" \
"v_cmp_gt_i32 vcc, 18, v186\n" \
"s_nop 1\n" \
"v_cndmask_b32 v182, v182, v239, vcc\n" \
"v_cmp_gt_i32 vcc, 19, v186\n" \
"s_nop 1\n" \
"v_cndmask_b32 v183, v183, v239, vcc\n" \
"Lex3_%=:\n" \
"v_max3_f32 v187, v176, v177, v178\n" \
"v_max3_f32 v187, v187, v179, v180\n" \
"v_max3_f32 v187, v187, v181, v182\n" \
"v_max3_f32 v187, v187, v183, v184\n" \
"v_mov_b32 v188, v187\n" \
"s_nop 1\n" \
"v_permlane16_swap_b32 v188, v187\n" \
"v_max_f32 v187, v187, v188\n" \
"v_mov_b32 v188, v187\n" \
"s_nop 1\n" \
"v_permlane32_swap_b32 v188, v187\n" \
"v_max_f32 v187, v187, v188\n" \
"v_sub_f32 v189, v184, v187\n" \
"v_exp_f32 v189, v189\n" \
"v_mov_b32 v184, v187\n" \
"v_sub_f32 v176, v176, v187\n" \
"v_sub_f32 v177, v177, v187\n" \
"v_sub_f32 v178, v178, v187\n" \
"v_sub_f32 v179, v179, v187\n" \
"v_sub_f32 v180, v180, v187\n" \
"v_sub_f32 v181, v181, v187\n" \
"v_sub_f32 v182, v182, v187\n" \
"v_sub_f32 v183, v183, v187\n" \
"v_exp_f32 v176, v176\n" \
"v_exp_f32 v177, v177\n" \
"v_exp_f32 v178, v178\n" \
"v_exp_f32 v179, v179\n" \
"v_exp_f32 v180, v180\n" \
"v_exp_f32 v181, v181\n" \
"v_exp_f32 v182, v182\n" \
"v_exp_f32 v183, v183\n" \
"v_mul_f32 v185, v185, v189\n" \
"v_add_f32 v185, v185, v176\n" \
"v_add_f32 v185, v185, v177\n" \
"v_add_f32 v185, v185, v178\n" \
"v_add_f32 v185, v185, v179\n" \
"v_add_f32 v185, v185, v180\n" \
"v_add_f32 v185, v185, v181\n" \
"v_add_f32 v185, v185, v182\n" \
"v_add_f32 v185, v185, v183\n" \
"v_cvt_pk_bf16_f32 v112, v176, v177\n" \
"v_cvt_pk_bf16_f32 v113, v178, v179\n" \
"v_cvt_pk_bf16_f32 v114, v180, v181\n" \
"v_cvt_pk_bf16_f32 v115, v182, v183\n" \
"v_mul_f32 v128, v128, v189\n" \
"v_mul_f32 v129, v129, v189\n" \
"v_mul_f32 v130, v130, v189\n" \
"v_mul_f32 v131, v131, v189\n" \
"v_mul_f32 v132, v132, v189\n" \
"v_mul_f32 v133, v133, v189\n" \
"v_mul_f32 v134, v134, v189\n" \
"v_mul_f32 v135, v135, v189\n" \
"v_mul_f32 v136, v136, v189\n" \
"v_mul_f32 v137, v137, v189\n" \
"v_mul_f32 v138, v138, v189\n" \
"v_mul_f32 v139, v139, v189\n" \
"v_mul_f32 v140, v140, v189\n" \
"v_mul_f32 v141, v141, v189\n" \
"v_mul_f32 v142, v142, v189\n" \
"v_mul_f32 v143, v143, v189\n" \
"v_mul_f32 v144, v144, v189\n" \
"v_mul_f32 v145, v145, v189\n" \
"v_mul_f32 v146, v146, v189\n" \
"v_mul_f32 v147, v147, v189\n" \
"v_mul_f32 v148, v148, v189\n" \
"v_mul_f32 v149, v149, v189\n" \
"v_mul_f32 v150, v150, v189\n" \
"v_mul_f32 v151, v151, v189\n" \
"v_mul_f32 v152, v152, v189\n" \
"v_mul_f32 v153, v153, v189\n" \
"v_mul_f32 v154, v154, v189\n" \
"v_mul_f32 v155, v155, v189\n" \
"v_mul_f32 v156, v156, v189\n" \
"v_mul_f32 v157, v157, v189\n" \
"v_mul_f32 v158, v158, v189\n" \
"v_mul_f32 v159, v159, v189\n" \
"s_nop 1\n" \
"v_mfma_f32_16x16x32_bf16 v[128:131], a[96:99], v[112:115], v[128:131]\n" \
"v_mfma_f32_16x16x32_bf16 v[132:135], a[100:103], v[112:115], v[132:135]\n" \
"v_mfma_f32_16x16x32_bf16 v[136:139], a[104:107], v[112:115], v[136:139]\n" \
"v_mfma_f32_16x16x32_bf16 v[140:143], a[108:111], v[112:115], v[140:143]\n" \
"v_mfma_f32_16x16x32_bf16 v[144:147], a[112:115], v[112:115], v[144:147]\n" \
"v_mfma_f32_16x16x32_bf16 v[148:151], a[116:119], v[112:115], v[148:151]\n" \
"v_mfma_f32_16x16x32_bf16 v[152:155], a[120:123], v[112:115], v[152:155]\n" \
"v_mfma_f32_16x16x32_bf16 v[156:159], a[124:127], v[112:115], v[156:159]\n" \
"s_nop 7\n" \
"s_nop 1\n" \
"s_and_b32 s70, s81, 1\n" \
"s_mul_i32 s70, s70, 1024\n" \
"s_add_u32 s82, s82, s70\n" \
"s_add_u32 s82, s82, 8192\n" \
"s_add_u32 s81, s81, 1\n" \
"s_cmp_le_u32 s81, s83\n" \
"s_cbranch_scc1 Lex2_%=\n" \
"v_mov_b32 v187, v185\n" \
"s_nop 1\n" \
"v_permlane16_swap_b32 v187, v185\n" \
"v_add_f32 v185, v185, v187\n" \
"v_mov_b32 v187, v185\n" \
"s_nop 1\n" \
"v_permlane32_swap_b32 v187, v185\n" \
"v_add_f32 v185, v185, v187\n" \
"v_rcp_f32 v185, v185\n" \
"s_nop 0\n" \
"v_mul_f32 v128, v128, v185\n" \
"v_mul_f32 v129, v129, v185\n" \
"v_mul_f32 v130, v130, v185\n" \
"v_mul_f32 v131, v131, v185\n" \
"v_mul_f32 v132, v132, v185\n" \
"v_mul_f32 v133, v133, v185\n" \
"v_mul_f32 v134, v134, v185\n" \
"v_mul_f32 v135, v135, v185\n" \
"v_mul_f32 v136, v136, v185\n" \
"v_mul_f32 v137, v137, v185\n" \
"v_mul_f32 v138, v138, v185\n" \
"v_mul_f32 v139, v139, v185\n" \
"v_mul_f32 v140, v140, v185\n" \
"v_mul_f32 v141, v141, v185\n" \
"v_mul_f32 v142, v142, v185\n" \
"v_mul_f32 v143, v143, v185\n" \
"v_mul_f32 v144, v144, v185\n" \
"v_mul_f32 v145, v145, v185\n" \
"v_mul_f32 v146, v146, v185\n" \
"v_mul_f32 v147, v147, v185\n" \
"v_mul_f32 v148, v148, v185\n" \
"v_mul_f32 v149, v149, v185\n" \
"v_mul_f32 v150, v150, v185\n" \
"v_mul_f32 v151, v151, v185\n" \
"v_mul_f32 v152, v152, v185\n" \
"v_mul_f32 v153, v153, v185\n" \
"v_mul_f32 v154, v154, v185\n" \
"v_mul_f32 v155, v155, v185\n" \
"v_mul_f32 v156, v156, v185\n" \
"v_mul_f32 v157, v157, v185\n" \
"v_mul_f32 v158, v158, v185\n" \
"v_mul_f32 v159, v159, v185\n" \
"s_add_u32 s70, s84, 4096\n" \
"v_cvt_pk_bf16_f32 v112, v128, v129\n" \
"v_cvt_pk_bf16_f32 v113, v130, v131\n" \
"v_cvt_pk_bf16_f32 v114, v132, v133\n" \
"v_cvt_pk_bf16_f32 v115, v134, v135\n" \
"buffer_store_dwordx4 v[112:115], v236, s[56:59], s84 offen offset:0 sc1\n" \
"v_cvt_pk_bf16_f32 v116, v136, v137\n" \
"v_cvt_pk_bf16_f32 v117, v138, v139\n" \
"v_cvt_pk_bf16_f32 v118, v140, v141\n" \
"v_cvt_pk_bf16_f32 v119, v142, v143\n" \
"buffer_store_dwordx4 v[116:119], v236, s[56:59], s84 offen offset:2048 sc1\n" \
"v_cvt_pk_bf16_f32 v120, v144, v145\n" \
"v_cvt_pk_bf16_f32 v121, v146, v147\n" \
"v_cvt_pk_bf16_f32 v122, v148, v149\n" \
"v_cvt_pk_bf16_f32 v123, v150, v151\n" \
"buffer_store_dwordx4 v[120:123], v236, s[56:59], s70 offen offset:0 sc1\n" \
"v_cvt_pk_bf16_f32 v124, v152, v153\n" \
"v_cvt_pk_bf16_f32 v125, v154, v155\n" \
"v_cvt_pk_bf16_f32 v126, v156, v157\n" \
"v_cvt_pk_bf16_f32 v127, v158, v159\n" \
"buffer_store_dwordx4 v[124:127], v236, s[56:59], s70 offen offset:2048 sc1\n" \
"s_waitcnt vmcnt(0)\n" \
"s_setpc_b64 s[64:65]\n" \
"Lpl4_%=:\n" \
"s_mov_b32 s72, 0\n" \
"Lpk4_%=:\n" \
"s_sleep 2\n" \
"buffer_load_dwordx2 v[226:227], v228, s[56:59], s13 offen sc1\n" \
"s_waitcnt vmcnt(0)\n" \
"v_cmp_eq_u32 vcc, s60, v226\n" \
"s_nop 1\n" \
"s_andn2_b32 s70, 0xff, vcc_lo\n" \
"s_cbranch_scc0 Lpr4_%=\n" \
"s_add_u32 s72, s72, 1\n" \
"s_cmp_lt_u32 s72, 0x40000\n" \
"s_cbranch_scc1 Lpk4_%=\n" \
"s_branch Lpr4_%=\n" \
"Lpl5_%=:\n" \
"s_mov_b32 s72, 0\n" \
"Lpk5_%=:\n" \
"s_sleep 2\n" \
"buffer_load_dword v220, v221, s[56:59], s66 offen sc1\n" \
"s_waitcnt vmcnt(0)\n" \
"v_cmp_eq_u32 vcc, s60, v220\n" \
"s_nop 1\n" \
"s_andn2_b32 s70, s65, vcc_lo\n" \
"s_cbranch_scc0 Lpr5_%=\n" \
"s_add_u32 s72, s72, 1\n" \
"s_cmp_lt_u32 s72, 0x40000\n" \
"s_cbranch_scc1 Lpk5_%=\n" \
"s_branch Lpr5_%=\n" \
"Lpl6_%=:\n" \
"s_mov_b32 s72, 0\n" \
"Lpk6_%=:\n" \
"s_sleep 2\n" \
"buffer_load_dword v220, v221, s[56:59], s67 offen sc1\n" \
"s_waitcnt vmcnt(0)\n" \
"v_cmp_eq_u32 vcc, s60, v220\n" \
"s_nop 1\n" \
"s_andn2_b32 s70, s74, vcc_lo\n" \
"s_cbranch_scc0 Lpr6_%=\n" \
"s_add_u32 s72, s72, 1\n" \
"s_cmp_lt_u32 s72, 0x40000\n" \
"s_cbranch_scc1 Lpk6_%=\n" \
"s_branch Lpr6_%=\n" \
"Lpl7_%=:\n" \
"s_mov_b32 s72, 0\n" \
"Lpk7_%=:\n" \
"s_sleep 2\n" \
"buffer_load_dword v220, v241, s[56:59], s64 offen sc1\n" \
"s_waitcnt vmcnt(0)\n" \
"v_cmp_eq_u32 vcc, s60, v220\n" \
"s_nop 1\n" \
"s_andn2_b32 s70, s69, vcc_lo\n" \
"s_cbranch_scc0 Lpr7_%=\n" \
"s_add_u32 s72, s72, 1\n" \
"s_cmp_lt_u32 s72, 0x40000\n" \
"s_cbranch_scc1 Lpk7_%=\n" \
"s_branch Lpr7_%=\n" \
"Lcode_end_%=:\n" \
".p2align 12\n" \
""
#define MK_ASM_CLOBBERS "v1", "v2", "v3", "v4", "v5", "v6", "v7", "v8", "v9", "v10", "v11", "v12", "v13", "v14", "v15", "v16", "v17", "v18", "v19", "v20", "v21", "v22", "v23", "v24", "v25", "v26", "v27", "v28", "v29", "v30", "v31", "v32", "v33", "v34", "v35", "v36", "v37", "v38", "v39", "v40", "v41", "v42", "v43", "v44", "v45", "v46", "v47", "v48", "v49", "v50", "v51", "v52", "v53", "v54", "v55", "v56", "v57", "v58", "v59", "v60", "v61", "v62", "v63", "v64", "v65", "v66", "v67", "v68", "v69", "v70", "v71", "v72", "v73", "v74", "v75", "v76", "v77", "v78", "v79", "v80", "v81", "v82", "v83", "v84", "v85", "v86", "v87", "v88", "v89", "v90", "v91", "v92", "v93", "v94", "v95", "v96", "v97", "v98", "v99", "v100", "v101", "v102", "v103", "v104", "v105", "v106", "v107", "v108", "v109", "v110", "v111", "v112", "v113", "v114", "v115", "v116", "v117", "v118", "v119", "v120", "v121", "v122", "v123", "v124", "v125", "v126", "v127", "v128", "v129", "v130", "v131", "v132", "v133", "v134", "v135", "v136", "v137", "v138", "v139", "v140", "v141", "v142", "v143", "v144", "v145", "v146", "v147", "v148", "v149", "v150", "v151", "v152", "v153", "v154", "v155", "v156", "v157", "v158", "v159", "v160", "v161", "v162", "v163", "v164", "v165", "v166", "v167", "v168", "v169", "v170", "v171", "v172", "v173", "v174", "v175", "v176", "v177", "v178", "v179", "v180", "v181", "v182", "v183", "v184", "v185", "v186", "v187", "v188", "v189", "v190", "v191", "v192", "v193", "v194", "v195", "v196", "v197", "v198", "v199", "v200", "v201", "v202", "v203", "v204", "v205", "v206", "v207", "v208", "v209", "v210", "v211", "v212", "v213", "v214", "v215", "v216", "v217", "v218", "v219", "v220", "v221", "v222", "v223", "v224", "v225", "v226", "v227", "v228", "v229", "v230", "v231", "v232", "v233", "v234", "v235", "v236", "v237", "v238", "v239", "v240", "v241", "v242", "v243", "v244", "v245", "v246", "v247", "v248", "v249", "v250", "v251", "v252", "v253", "v254", "v255", "a0", "a1", "a2", "a3", "a4", "a5", "a6", "a7", "a8", "a9", "a10", "a11", "a12", "a13", "a14", "a15", "a16", "a17", "a18", "a19", "a20", "a21", "a22", "a23", "a24", "a25", "a26", "a27", "a28", "a29", "a30", "a31", "a32", "a33", "a34", "a35", "a36", "a37", "a38", "a39", "a40", "a41", "a42", "a43", "a44", "a45", "a46", "a47", "a48", "a49", "a50", "a51", "a52", "a53", "a54", "a55", "a56", "a57", "a58", "a59", "a60", "a61", "a62", "a63", "a64", "a65", "a66", "a67", "a68", "a69", "a70", "a71", "a72", "a73", "a74", "a75", "a76", "a77", "a78", "a79", "a80", "a81", "a82", "a83", "a84", "a85", "a86", "a87", "a88", "a89", "a90", "a91", "a92", "a93", "a94", "a95", "a96", "a97", "a98", "a99", "a100", "a101", "a102", "a103", "a104", "a105", "a106", "a107", "a108", "a109", "a110", "a111", "a112", "a113", "a114", "a115", "a116", "a117", "a118", "a119", "a120", "a121", "a122", "a123", "a124", "a125", "a126", "a127", "a128", "a129", "a130", "a131", "a132", "a133", "a134", "a135", "a136", "a137", "a138", "a139", "a140", "a141", "a142", "a143", "a144", "a145", "a146", "a147", "a148", "a149", "a150", "a151", "a152", "a153", "a154", "a155", "a156", "a157", "a158", "a159", "a160", "a161", "a162", "a163", "a164", "a165", "a166", "a167", "a168", "a169", "a170", "a171", "a172", "a173", "a174", "a175", "a176", "a177", "a178", "a179", "a180", "a181", "a182", "a183", "a184", "a185", "a186", "a187", "a188", "a189", "a190", "a191", "a192", "a193", "a194", "a195", "a196", "a197", "a198", "a199", "a200", "a201", "a202", "a203", "a204", "a205", "a206", "a207", "a208", "a209", "a210", "a211", "a212", "a213", "a214", "a215", "a216", "a217", "a218", "a219", "a220", "a221", "a222", "a223", "a224", "a225", "a226", "a227", "a228", "a229", "a230", "a231", "a232", "a233", "a234", "a235", "a236", "a237", "a238", "a239", "a240", "a241", "a242", "a243", "a244", "a245", "a246", "a247", "a248", "a249", "a250", "a251", "a252", "a253", "a254", "a255", "s8", "s9", "s10", "s11", "s12", "s13", "s14", "s15", "s16", "s17", "s18", "s19", "s20", "s21", "s22", "s23", "s24", "s25", "s26", "s27", "s28", "s29", "s30", "s31", "s36", "s37", "s38", "s39", "s40", "s41", "s42", "s43", "s44", "s45", "s46", "s47", "s48", "s49", "s50", "s51", "s52", "s53", "s54", "s55", "s56", "s57", "s58", "s59", "s60", "s61", "s62", "s63", "s64", "s65", "s66", "s67", "s68", "s69", "s70", "s71", "s72", "s73", "s74", "s75", "s76", "s77", "s78", "s79", "s80", "s81", "s82", "s83", "s84", "s85", "s86", "s87", "s88", "s89", "s90", "s91", "s92", "s93", "s94", "s95", "vcc", "scc", "memory"
namespace mk {
constexpr int LDS_BYTES = 163840;
struct Args { const void* in[9]; void* out; void* ws; };
__global__ __launch_bounds__(256, 1) void mega_k(const Args a) {
    extern __shared__ __attribute__((aligned(16))) unsigned char shm[];
    const unsigned ldsb = (unsigned)(size_t)(LAS unsigned char*)shm;
    asm volatile(MK_ASM_BODY
                 :
                 : "s"(__builtin_amdgcn_kernarg_segment_ptr()), "s"(blockIdx.x), "v"(threadIdx.x), "s"(ldsb)
                 : MK_ASM_CLOBBERS);
}
}
extern "C" void kernel_launch(void* const* d_in, const int* in_sizes, int n_in, void* d_out, int out_size, void* d_ws, size_t ws_size, hipStream_t stream) {
    (void)hipFuncSetAttribute((const void*)mk::mega_k, hipFuncAttributeMaxDynamicSharedMemorySize, mk::LDS_BYTES);
    mk::Args a;
    for (int i = 0; i < 9; ++i) a.in[i] = d_in[i];
    a.out = d_out; a.ws = (char*)d_ws + ((ws_size - WS_END) & ~(size_t)0x1fffff);
    mk::mega_k<<<256, 256, mk::LDS_BYTES, stream>>>(a);
}
```

```cpp
#include <hip/hip_runtime.h>
#include <stdint.h>
#define LAS __attribute__((address_space(3)))
constexpr size_t LAY_TS = 17408, LAY_HS = 557824, LAY_CRS = 266240, LAY_ORS = 69632, WS_K = 0, WS_V = 17850368, WS_CTX = 35700736, WS_OB = 52740096, WS_FL = 65585152, WS_END = 65634304, LAY_VS = 2;
#define MK_ASM_BODY \
"s_load_dwordx16 s[8:23], %0, 0x0\n" \
"s_load_dwordx4 s[24:27], %0, 0x40\n" \
"s_load_dwordx2 s[28:29], %0, 0x50\n" \
"v_readfirstlane_b32 s61, %2\n" \
"s_lshr_b32 s61, s61, 6\n" \
"v_mbcnt_lo_u32_b32 v78, -1, 0\n" \
"v_mbcnt_hi_u32_b32 v78, -1, v78\n" \
"v_lshlrev_b32 v234, 4, v78\n" \
"s_getpc_b64 s[64:65]\n" \
"Lpc_%=:\n" \
"s_lshr_b32 s70, %1, 3\n" \
"s_lshl_b32 s70, s70, 2\n" \
"s_add_u32 s70, s70, s61\n" \
"s_and_b32 s71, s70, 127\n" \
"s_lshl_b32 s71, s71, 10\n" \
"v_lshl_add_u32 v237, v234, 0, s71\n" \
"s_mov_b32 s71, Lcode_end_%=-Lpc_%=-4\n" \
"v_min_u32 v237, s71, v237\n" \
"global_load_dword v238, v237, s[64:65]\n" \
"s_and_b32 s62, %1, 7\n" \
"s_lshl_b32 s62, s62, 2\n" \
"s_lshr_b32 s70, %1, 6\n" \
"s_add_u32 s62, s62, s70\n" \
"s_bfe_u32 s63, %1, 0x30003\n" \
"s_lshl_b32 s74, s61, 13\n" \
"s_add_u32 s74, s74, %3\n" \
"v_bfe_u32 v79, v78, 4, 1\n" \
"v_bfe_u32 v80, v78, 2, 2\n" \
"v_lshl_add_u32 v80, v79, 3, v80\n" \
"s_lshl_b32 s71, s61, 4\n" \
"v_add_u32 v80, s71, v80\n" \
"v_lshlrev_b32 v80, 13, v80\n" \
"v_lshlrev_b32 v79, 1, v79\n" \
"v_and_b32 v81, 3, v78\n" \
"v_xor_b32 v79, v79, v81\n" \
"v_lshl_add_u32 v80, v79, 4, v80\n" \
"v_lshrrev_b32 v90, 5, v78\n" \
"v_lshl_add_u32 v84, v90, 6, v80\n" \
"v_add_u32 v68, 4096, v84\n" \
"v_add_u32 v69, 3072, v84\n" \
"v_add_u32 v70, 2176, v84\n" \
"v_add_u32 v71, 1152, v84\n" \
"s_lshl_b32 s83, s62, 8\n" \
"s_add_u32 s84, s83, 32768\n" \
"v_lshrrev_b32 v85, 3, v78\n" \
"v_and_b32 v86, 7, v78\n" \
"v_xor_b32 v86, v86, v85\n" \
"v_lshlrev_b32 v86, 4, v86\n" \
"v_lshl_add_u32 v64, v85, 10, v86\n" \
"v_add_u32 v65, 7168, v64\n" \
"v_add_u32 v66, 14336, v64\n" \
"v_add_u32 v67, 21504, v64\n" \
"s_bitcmp1_b32 s61, 0\n" \
"s_cselect_b32 s70, 0, 15\n" \
"s_xor_b32 s81, s63, s70\n" \
"s_lshl_b32 s81, s81, 17\n" \
"s_lshr_b32 s70, s61, 1\n" \
"s_lshl_b32 s71, s70, 16\n" \
"s_add_u32 s81, s81, s71\n" \
"s_add_u32 s82, s81, 32768\n" \
"s_mov_b32 s42, 4198400\n" \
"s_mov_b32 s43, 0x20000\n" \
"s_mov_b32 s46, 4198400\n" \
"s_mov_b32 s47, 0x20000\n" \
"s_mov_b32 s38, 2097152\n" \
"s_mov_b32 s39, 0x20000\n" \
"s_lshr_b32 s69, s61, 1\n" \
"s_xor_b32 s69, s69, s63\n" \
"s_xor_b32 s69, s69, s62\n" \
"s_and_b32 s69, s69, 1\n" \
"s_cbranch_scc1 Lech_%=\n" \
"Lecr_%=:\n" \
"s_waitcnt lgkmcnt(0)\n" \
"s_mov_b64 s[40:41], s[14:15]\n" \
"s_sub_u32 s40, s40, 4096\n" \
"s_subb_u32 s41, s41, 0\n" \
"s_and_b32 s41, s41, 0xffff\n" \
"s_mov_b64 s[44:45], s[16:17]\n" \
"s_sub_u32 s44, s44, 4096\n" \
"s_subb_u32 s45, s45, 0\n" \
"s_and_b32 s45, s45, 0xffff\n" \
"s_mov_b64 s[36:37], s[10:11]\n" \
"s_and_b32 s37, s37, 0xffff\n" \
"s_add_u32 m0, s74, 32768\n" \
"s_nop 0\n" \
"buffer_load_dwordx4 v68, s[40:43], s83 offen offset:0 lds\n" \
"buffer_load_dwordx4 v70, s[40:43], s83 offen offset:2048 lds\n" \
"s_add_u32 m0, s74, 36864\n" \
"s_nop 0\n" \
"buffer_load_dwordx4 v68, s[40:43], s84 offen offset:0 lds\n" \
"buffer_load_dwordx4 v70, s[40:43], s84 offen offset:2048 lds\n" \
"s_add_u32 m0, s74, 0\n" \
"s_nop 0\n" \
"buffer_load_dwordx4 v64, s[36:39], s81 offen offset:0 lds\n" \
"buffer_load_dwordx4 v65, s[36:39], s81 offen offset:1024 lds\n" \
"buffer_load_dwordx4 v66, s[36:39], s81 offen offset:2048 lds\n" \
"buffer_load_dwordx4 v67, s[36:39], s81 offen offset:3072 lds\n" \
"s_xor_b32 s81, s81, 0x80\n" \
"s_add_u32 m0, s74, 4096\n" \
"s_nop 0\n" \
"buffer_load_dwordx4 v64, s[36:39], s82 offen offset:0 lds\n" \
"buffer_load_dwordx4 v65, s[36:39], s82 offen offset:1024 lds\n" \
"buffer_load_dwordx4 v66, s[36:39], s82 offen offset:2048 lds\n" \
"buffer_load_dwordx4 v67, s[36:39], s82 offen offset:3072 lds\n" \
"s_xor_b32 s82, s82, 0x80\n" \
"s_add_u32 m0, s74, 32768\n" \
"s_nop 0\n" \
"buffer_load_dwordx4 v69, s[44:47], s83 offen offset:1024 lds\n" \
"buffer_load_dwordx4 v71, s[44:47], s83 offen offset:3072 lds\n" \
"s_xor_b32 s83, s83, 0x80000\n" \
"s_add_u32 m0, s74, 36864\n" \
"s_nop 0\n" \
"buffer_load_dwordx4 v69, s[44:47], s84 offen offset:1024 lds\n" \
"buffer_load_dwordx4 v71, s[44:47], s84 offen offset:3072 lds\n" \
"s_xor_b32 s84, s84, 0x80000\n" \
"v_lshrrev_b32 v87, 4, v78\n" \
"v_and_b32 v79, 15, v78\n" \
"v_and_b32 v80, 7, v78\n" \
"v_xor_b32 v80, v80, v87\n" \
"v_lshlrev_b32 v79, 7, v79\n" \
"s_lshl_b32 s70, s70, 14\n" \
"s_add_u32 s70, s70, %3\n" \
"v_lshl_add_u32 v88, v80, 4, v79\n" \
"v_add_u32 v72, s70, v88\n" \
"v_xor_b32 v73, 64, v72\n" \
"v_lshlrev_b32 v79, 3, v81\n" \
"v_bfe_u32 v80, v78, 2, 2\n" \
"v_lshl_add_u32 v79, v80, 6, v79\n" \
"v_bfe_u32 v80, v78, 4, 1\n" \
"v_lshl_add_u32 v79, v80, 8, v79\n" \
"v_lshl_add_u32 v89, v80, 5, v79\n" \
"s_and_b32 s70, s61, 1\n" \
"s_lshl_b32 s70, s70, 11\n" \
"s_add_u32 s70, s70, %3\n" \
"v_lshl_add_u32 v82, v90, 13, v89\n" \
"v_add_u32 v74, s70, v82\n" \
"v_xor_b32 v75, 32, v74\n" \
"s_mov_b64 s[56:57], s[28:29]\n" \
"s_and_b32 s57, s57, 0xffff\n" \
"s_mov_b32 s58, 65634304\n" \
"s_mov_b32 s59, 0x20000\n" \
"v_mov_b32 v241, 0\n" \
"v_lshl_add_u32 v110, v90, 9, v234\n" \
"s_lshl_b32 s66, %1, 2\n" \
"s_add_u32 s66, s66, s61\n" \
"s_lshl_b32 s66, s66, 10\n" \
"s_lshr_b32 s67, %1, 3\n" \
"s_lshl_b32 s67, s67, 2\n" \
"s_add_u32 s67, s67, s61\n" \
"s_lshl_b32 s67, s67, 13\n" \
"s_and_b32 s64, %1, 7\n" \
"s_lshl_b32 s64, s64, 10\n" \
"s_add_u32 s67, s67, s64\n" \
"s_cmp_eq_u32 s69, 0\n" \
"s_cbranch_scc0 Lecs_%=\n" \
"Lech_%=:\n" \
"s_mov_b64 exec, 0\n" \
"s_cmp_eq_u32 s61, 0\n" \
"s_cbranch_scc1 Lst0x0_%=\n" \
"s_cmp_eq_u32 s61, 1\n" \
"s_cbranch_scc1 Lst0x1_%=\n" \
"s_cmp_eq_u32 s61, 2\n" \
"s_cbranch_scc1 Lst0x2_%=\n" \
"s_branch Lst0x3_%=\n" \
"Lcb0_%=:\n" \
"s_mov_b64 exec, -1\n" \
"s_lshl_b32 s69, s69, 1\n" \
"s_cbranch_scc1 Lecr_%=\n" \
"Lecs_%=:\n" \
"s_add_u32 m0, s74, 131072\n" \
"s_nop 0\n" \
"buffer_load_dwordx4 v68, s[40:43], s83 offen offset:0 lds\n" \
"buffer_load_dwordx4 v69, s[44:47], s83 offen offset:1024 lds\n" \
"buffer_load_dwordx4 v70, s[40:43], s83 offen offset:2048 lds\n" \
"buffer_load_dwordx4 v71, s[44:47], s83 offen offset:3072 lds\n" \
"s_xor_b32 s83, s83, 0x180000\n" \
"s_add_u32 m0, s74, 135168\n" \
"s_nop 0\n" \
"buffer_load_dwordx4 v68, s[40:43], s84 offen offset:0 lds\n" \
"buffer_load_dwordx4 v69, s[44:47], s84 offen offset:1024 lds\n" \
"buffer_load_dwordx4 v70, s[40:43], s84 offen offset:2048 lds\n" \
"buffer_load_dwordx4 v71, s[44:47], s84 offen offset:3072 lds\n" \
"s_xor_b32 s84, s84, 0x180000\n" \
"s_waitcnt vmcnt(16)\n" \
"s_barrier\n" \
"ds_read_b64_tr_b16 a[64:65], v74 offset:32768\n" \
"ds_read_b64_tr_b16 a[66:67], v74 offset:36864\n" \
"ds_read_b64_tr_b16 a[68:69], v74 offset:49152\n" \
"ds_read_b64_tr_b16 a[70:71], v74 offset:53248\n" \
"ds_read_b128 a[0:3], v72 offset:0\n" \
"ds_read_b128 a[4:7], v73 offset:0\n" \
"ds_read_b64_tr_b16 a[72:73], v75 offset:32768\n" \
"ds_read_b64_tr_b16 a[74:75], v75 offset:36864\n" \
"ds_read_b64_tr_b16 a[76:77], v75 offset:49152\n" \
"ds_read_b64_tr_b16 a[78:79], v75 offset:53248\n" \
"ds_read_b64_tr_b16 a[80:81], v74 offset:33280\n" \
"ds_read_b64_tr_b16 a[82:83], v74 offset:37376\n" \
"ds_read_b64_tr_b16 a[84:85], v74 offset:49664\n" \
"ds_read_b64_tr_b16 a[86:87], v74 offset:53760\n" \
"ds_read_b64_tr_b16 a[88:89], v75 offset:33280\n" \
"ds_read_b64_tr_b16 a[90:91], v75 offset:37376\n" \
"ds_read_b64_tr_b16 a[92:93], v75 offset:49664\n" \
"ds_read_b64_tr_b16 a[94:95], v75 offset:53760\n" \
"ds_read_b128 a[8:11], v72 offset:2048\n" \
"ds_read_b128 a[12:15], v73 offset:2048\n" \
"s_waitcnt lgkmcnt(14)\n" \
"v_mfma_f32_16x16x32_bf16 v[0:3], a[64:67], a[0:3], 0\n" \
"s_mov_b32 s93, 65593344\n" \
"ds_read_b128 a[32:35], v72 offset:8192\n" \
"v_mfma_f32_16x16x32_bf16 v[0:3], a[68:71], a[4:7], v[0:3]\n" \
"s_waitcnt lgkmcnt(11)\n" \
"buffer_load_dword v240, v241, s[56:59], s93 offen sc1\n" \
"v_mfma_f32_16x16x32_bf16 v[4:7], a[72:75], a[0:3], 0\n" \
"s_mov_b64 s[64:65], s[10:11]\n" \
"s_lshl1_add_u32 s68, s66, 0\n" \
"s_add_u32 s64, s64, s68\n" \
"s_addc_u32 s65, s65, 0\n" \
"ds_read_b128 a[36:39], v73 offset:8192\n" \
"v_mfma_f32_16x16x32_bf16 v[4:7], a[76:79], a[4:7], v[4:7]\n" \
"s_waitcnt lgkmcnt(8)\n" \
"global_load_dword v238, v110, s[64:65]\n" \
"v_mfma_f32_16x16x32_bf16 v[8:11], a[80:83], a[0:3], 0\n" \
"s_mov_b64 s[64:65], s[14:15]\n" \
"s_add_u32 s68, s67, 0x100000\n" \
"s_add_u32 s64, s64, s68\n" \
"s_addc_u32 s65, s65, 0\n" \
"ds_read_b128 a[40:43], v72 offset:10240\n" \
"v_mfma_f32_16x16x32_bf16 v[8:11], a[84:87], a[4:7], v[8:11]\n" \
"s_waitcnt lgkmcnt(2)\n" \
"global_load_dword v238, v234, s[64:65]\n" \
"v_mfma_f32_16x16x32_bf16 v[12:15], a[88:91], a[0:3], 0\n" \
"s_mov_b64 s[64:65], s[16:17]\n" \
"s_add_u32 s68, s67, 0x100000\n" \
"s_add_u32 s64, s64, s68\n" \
"s_addc_u32 s65, s65, 0\n" \
"ds_read_b128 a[44:47], v73 offset:10240\n" \
"v_mfma_f32_16x16x32_bf16 v[12:15], a[92:95], a[4:7], v[12:15]\n" \
"global_load_dword v238, v234, s[64:65]\n" \
"v_mfma_f32_16x16x32_bf16 v[28:31], a[88:91], a[8:11], 0\n" \
"s_add_u32 m0, s74, 65536\n" \
"v_mfma_f32_16x16x32_bf16 v[28:31], a[92:95], a[12:15], v[28:31]\n" \
"buffer_load_dwordx4 v64, s[36:39], s81 offen offset:0 lds\n" \
"v_mfma_f32_16x16x32_bf16 v[24:27], a[80:83], a[8:11], 0\n" \
"v_mfma_f32_16x16x32_bf16 v[24:27], a[84:87], a[12:15], v[24:27]\n" \
"v_mfma_f32_16x16x32_bf16 v[20:23], a[72:75], a[8:11], 0\n" \
"v_mfma_f32_16x16x32_bf16 v[20:23], a[76:79], a[12:15], v[20:23]\n" \
"buffer_load_dwordx4 v65, s[36:39], s81 offen offset:1024 lds\n" \
"v_mfma_f32_16x16x32_bf16 v[16:19], a[64:67], a[8:11], 0\n" \
"v_mfma_f32_16x16x32_bf16 v[16:19], a[68:71], a[12:15], v[16:19]\n" \
"s_waitcnt lgkmcnt(0)\n" \
"v_mfma_f32_16x16x32_bf16 v[144:147], a[64:67], a[32:35], 0\n" \
"v_mfma_f32_16x16x32_bf16 v[144:147], a[68:71], a[36:39], v[144:147]\n" \
"buffer_load_dwordx4 v66, s[36:39], s81 offen offset:2048 lds\n" \
"v_mfma_f32_16x16x32_bf16 v[148:151], a[72:75], a[32:35], 0\n" \
"v_mfma_f32_16x16x32_bf16 v[148:151], a[76:79], a[36:39], v[148:151]\n" \
"v_mfma_f32_16x16x32_bf16 v[152:155], a[80:83], a[32:35], 0\n" \
"v_mfma_f32_16x16x32_bf16 v[152:155], a[84:87], a[36:39], v[152:155]\n" \
"buffer_load_dwordx4 v67, s[36:39], s81 offen offset:3072 lds\n" \
"v_mfma_f32_16x16x32_bf16 v[156:159], a[88:91], a[32:35], 0\n" \
"v_mfma_f32_16x16x32_bf16 v[156:159], a[92:95], a[36:39], v[156:159]\n" \
"s_xor_b32 s81, s81, 0x180\n" \
"s_waitcnt vmcnt(16) lgkmcnt(0)\n" \
"s_barrier\n" \
"v_mfma_f32_16x16x32_bf16 v[188:191], a[88:91], a[40:43], 0\n" \
"s_add_u32 m0, s74, 69632\n" \
"ds_read_b128 a[16:19], v72 offset:4096\n" \
"ds_read_b128 a[20:23], v73 offset:4096\n" \
"v_mfma_f32_16x16x32_bf16 v[188:191], a[92:95], a[44:47], v[188:191]\n" \
"buffer_load_dwordx4 v64, s[36:39], s82 offen offset:0 lds\n" \
"v_mfma_f32_16x16x32_bf16 v[184:187], a[80:83], a[40:43], 0\n" \
"ds_read_b128 a[24:27], v72 offset:6144\n" \
"ds_read_b128 a[28:31], v73 offset:6144\n" \
"v_mfma_f32_16x16x32_bf16 v[184:187], a[84:87], a[44:47], v[184:187]\n" \
"v_mfma_f32_16x16x32_bf16 v[180:183], a[72:75], a[40:43], 0\n" \
"ds_read_b128 a[48:51], v72 offset:12288\n" \
"ds_read_b128 a[52:55], v73 offset:12288\n" \
"v_mfma_f32_16x16x32_bf16 v[180:183], a[76:79], a[44:47], v[180:183]\n" \
"buffer_load_dwordx4 v65, s[36:39], s82 offen offset:1024 lds\n" \
"v_mfma_f32_16x16x32_bf16 v[176:179], a[64:67], a[40:43], 0\n" \
"ds_read_b128 a[56:59], v72 offset:14336\n" \
"ds_read_b128 a[60:63], v73 offset:14336\n" \
"v_mfma_f32_16x16x32_bf16 v[176:179], a[68:71], a[44:47], v[176:179]\n" \
"s_waitcnt lgkmcnt(4)\n" \
"v_mfma_f32_16x16x32_bf16 v[32:35], a[64:67], a[16:19], 0\n" \
"ds_read_b64_tr_b16 a[96:97], v74 offset:33792\n" \
"ds_read_b64_tr_b16 a[98:99], v74 offset:37888\n" \
"v_mfma_f32_16x16x32_bf16 v[32:35], a[68:71], a[20:23], v[32:35]\n" \
"buffer_load_dwordx4 v66, s[36:39], s82 offen offset:2048 lds\n" \
"v_mfma_f32_16x16x32_bf16 v[36:39], a[72:75], a[16:19], 0\n" \
"ds_read_b64_tr_b16 a[100:101], v74 offset:50176\n" \
"ds_read_b64_tr_b16 a[102:103], v74 offset:54272\n" \
"v_mfma_f32_16x16x32_bf16 v[36:39], a[76:79], a[20:23], v[36:39]\n" \
"v_mfma_f32_16x16x32_bf16 v[40:43], a[80:83], a[16:19], 0\n" \
"ds_read_b64_tr_b16 a[104:105], v75 offset:33792\n" \
"ds_read_b64_tr_b16 a[106:107], v75 offset:37888\n" \
"v_mfma_f32_16x16x32_bf16 v[40:43], a[84:87], a[20:23], v[40:43]\n" \
"buffer_load_dwordx4 v67, s[36:39], s82 offen offset:3072 lds\n" \
"v_mfma_f32_16x16x32_bf16 v[44:47], a[88:91], a[16:19], 0\n" \
"ds_read_b64_tr_b16 a[108:109], v75 offset:50176\n" \
"ds_read_b64_tr_b16 a[110:111], v75 offset:54272\n" \
"v_mfma_f32_16x16x32_bf16 v[44:47], a[92:95], a[20:23], v[44:47]\n" \
"v_mfma_f32_16x16x32_bf16 v[60:63], a[88:91], a[24:27], 0\n" \
"s_xor_b32 s82, s82, 0x180\n" \
"ds_read_b64_tr_b16 a[112:113], v74 offset:34304\n" \
"ds_read_b64_tr_b16 a[114:115], v74 offset:38400\n" \
"v_add_u32 v96, 0x10000, v72\n" \
"v_mfma_f32_16x16x32_bf16 v[60:63], a[92:95], a[28:31], v[60:63]\n" \
"v_mfma_f32_16x16x32_bf16 v[56:59], a[80:83], a[24:27], 0\n" \
"ds_read_b64_tr_b16 a[116:117], v74 offset:50688\n" \
"ds_read_b64_tr_b16 a[118:119], v74 offset:54784\n" \
"v_add_u32 v97, 0x10000, v73\n" \
"v_mfma_f32_16x16x32_bf16 v[56:59], a[84:87], a[28:31], v[56:59]\n" \
"v_mfma_f32_16x16x32_bf16 v[52:55], a[72:75], a[24:27], 0\n" \
"ds_read_b64_tr_b16 a[120:121], v75 offset:34304\n" \
"ds_read_b64_tr_b16 a[122:123], v75 offset:38400\n" \
"v_add_u32 v98, 0x10000, v74\n" \
"v_mfma_f32_16x16x32_bf16 v[52:55], a[76:79], a[28:31], v[52:55]\n" \
"v_mfma_f32_16x16x32_bf16 v[48:51], a[64:67], a[24:27], 0\n" \
"ds_read_b64_tr_b16 a[124:125], v75 offset:50688\n" \
"ds_read_b64_tr_b16 a[126:127], v75 offset:54784\n" \
"v_add_u32 v99, 0x10000, v75\n" \
"v_mfma_f32_16x16x32_bf16 v[48:51], a[68:71], a[28:31], v[48:51]\n" \
"s_waitcnt lgkmcnt(4)\n" \
"v_mfma_f32_16x16x32_bf16 v[192:195], a[64:67], a[48:51], 0\n" \
"v_add_u32 v100, 0x20000, v72\n" \
"v_mfma_f32_16x16x32_bf16 v[192:195], a[68:71], a[52:55], v[192:195]\n" \
"v_mfma_f32_16x16x32_bf16 v[196:199], a[72:75], a[48:51], 0\n" \
"v_add_u32 v101, 0x20000, v73\n" \
"v_mfma_f32_16x16x32_bf16 v[196:199], a[76:79], a[52:55], v[196:199]\n" \
"v_mfma_f32_16x16x32_bf16 v[200:203], a[80:83], a[48:51], 0\n" \
"s_add_u32 m0, s74, 98304\n" \
"v_add_u32 v102, 0x20000, v74\n" \
"v_mfma_f32_16x16x32_bf16 v[200:203], a[84:87], a[52:55], v[200:203]\n" \
"buffer_load_dwordx4 v68, s[40:43], s83 offen offset:0 lds\n" \
"v_mfma_f32_16x16x32_bf16 v[204:207], a[88:91], a[48:51], 0\n" \
"v_add_u32 v103, 0x20000, v75\n" \
"v_mfma_f32_16x16x32_bf16 v[204:207], a[92:95], a[52:55], v[204:207]\n" \
"v_mfma_f32_16x16x32_bf16 v[220:223], a[88:91], a[56:59], 0\n" \
"v_mfma_f32_16x16x32_bf16 v[220:223], a[92:95], a[60:63], v[220:223]\n" \
"buffer_load_dwordx4 v69, s[44:47], s83 offen offset:1024 lds\n" \
"v_mfma_f32_16x16x32_bf16 v[216:219], a[80:83], a[56:59], 0\n" \
"v_mfma_f32_16x16x32_bf16 v[216:219], a[84:87], a[60:63], v[216:219]\n" \
"v_mfma_f32_16x16x32_bf16 v[212:215], a[72:75], a[56:59], 0\n" \
"v_mfma_f32_16x16x32_bf16 v[212:215], a[76:79], a[60:63], v[212:215]\n" \
"buffer_load_dwordx4 v70, s[40:43], s83 offen offset:2048 lds\n" \
"v_mfma_f32_16x16x32_bf16 v[208:211], a[64:67], a[56:59], 0\n" \
"v_mfma_f32_16x16x32_bf16 v[208:211], a[68:71], a[60:63], v[208:211]\n" \
"s_waitcnt lgkmcnt(0)\n" \
"s_barrier\n" \
"v_mfma_f32_16x16x32_bf16 a[128:131], a[0:3], a[96:99], 0\n" \
"v_mfma_f32_16x16x32_bf16 a[128:131], a[4:7], a[100:103], a[128:131]\n" \
"buffer_load_dwordx4 v71, s[44:47], s83 offen offset:3072 lds\n" \
"v_mfma_f32_16x16x32_bf16 a[132:135], a[0:3], a[104:107], 0\n" \
"v_mfma_f32_16x16x32_bf16 a[132:135], a[4:7], a[108:111], a[132:135]\n" \
"s_xor_b32 s83, s83, 0x80000\n" \
"v_mfma_f32_16x16x32_bf16 a[136:139], a[0:3], a[112:115], 0\n" \
"s_add_u32 m0, s74, 102400\n" \
"v_mfma_f32_16x16x32_bf16 a[136:139], a[4:7], a[116:119], a[136:139]\n" \
"buffer_load_dwordx4 v68, s[40:43], s84 offen offset:0 lds\n" \
"v_mfma_f32_16x16x32_bf16 a[140:143], a[0:3], a[120:123], 0\n" \
"v_mfma_f32_16x16x32_bf16 a[140:143], a[4:7], a[124:127], a[140:143]\n" \
"v_mfma_f32_16x16x32_bf16 a[156:159], a[8:11], a[120:123], 0\n" \
"v_mfma_f32_16x16x32_bf16 a[156:159], a[12:15], a[124:127], a[156:159]\n" \
"buffer_load_dwordx4 v69, s[44:47], s84 offen offset:1024 lds\n" \
"v_mfma_f32_16x16x32_bf16 a[152:155], a[8:11], a[112:115], 0\n" \
"v_mfma_f32_16x16x32_bf16 a[152:155], a[12:15], a[116:119], a[152:155]\n" \
"v_mfma_f32_16x16x32_bf16 a[148:151], a[8:11], a[104:107], 0\n" \
"v_mfma_f32_16x16x32_bf16 a[148:151], a[12:15], a[108:111], a[148:151]\n" \
"buffer_load_dwordx4 v70, s[40:43], s84 offen offset:2048 lds\n" \
"v_mfma_f32_16x16x32_bf16 a[144:147], a[8:11], a[96:99], 0\n" \
"v_mfma_f32_16x16x32_bf16 a[144:147], a[12:15], a[100:103], a[144:147]\n" \
"v_mfma_f32_16x16x32_bf16 a[160:163], a[16:19], a[96:99], 0\n" \
"v_mfma_f32_16x16x32_bf16 a[160:163], a[20:23], a[100:103], a[160:163]\n" \
"buffer_load_dwordx4 v71, s[44:47], s84 offen offset:3072 lds\n" \
"v_mfma_f32_16x16x32_bf16 a[164:167], a[16:19], a[104:107], 0\n" \
"v_mfma_f32_16x16x32_bf16 a[164:167], a[20:23], a[108:111], a[164:167]\n" \
"v_mfma_f32_16x16x32_bf16 a[168:171], a[16:19], a[112:115], 0\n" \
"s_xor_b32 s84, s84, 0x80000\n" \
"s_add_u32 m0, s74, 32768\n" \
"v_mfma_f32_16x16x32_bf16 a[168:171], a[20:23], a[116:119], a[168:171]\n" \
"buffer_load_dwordx4 v64, s[36:39], s81 offen offset:0 lds\n" \
"v_mfma_f32_16x16x32_bf16 a[172:175], a[16:19], a[120:123], 0\n" \
"v_mfma_f32_16x16x32_bf16 a[172:175], a[20:23], a[124:127], a[172:175]\n" \
"v_mfma_f32_16x16x32_bf16 a[188:191], a[24:27], a[120:123], 0\n" \
"v_mfma_f32_16x16x32_bf16 a[188:191], a[28:31], a[124:127], a[188:191]\n" \
"buffer_load_dwordx4 v65, s[36:39], s81 offen offset:1024 lds\n" \
"v_mfma_f32_16x16x32_bf16 a[184:187], a[24:27], a[112:115], 0\n" \
"v_mfma_f32_16x16x32_bf16 a[184:187], a[28:31], a[116:119], a[184:187]\n" \
"v_mfma_f32_16x16x32_bf16 a[180:183], a[24:27], a[104:107], 0\n" \
"v_mfma_f32_16x16x32_bf16 a[180:183], a[28:31], a[108:111], a[180:183]\n" \
"buffer_load_dwordx4 v66, s[36:39], s81 offen offset:2048 lds\n" \
"v_mfma_f32_16x16x32_bf16 a[176:179], a[24:27], a[96:99], 0\n" \
"v_mfma_f32_16x16x32_bf16 a[176:179], a[28:31], a[100:103], a[176:179]\n" \
"v_mfma_f32_16x16x32_bf16 a[192:195], a[32:35], a[96:99], 0\n" \
"v_mfma_f32_16x16x32_bf16 a[192:195], a[36:39], a[100:103], a[192:195]\n" \
"buffer_load_dwordx4 v67, s[36:39], s81 offen offset:3072 lds\n" \
"v_mfma_f32_16x16x32_bf16 a[196:199], a[32:35], a[104:107], 0\n" \
"v_mfma_f32_16x16x32_bf16 a[196:199], a[36:39], a[108:111], a[196:199]\n" \
"s_xor_b32 s81, s81, 0x80\n" \
"v_mfma_f32_16x16x32_bf16 a[200:203], a[32:35], a[112:115], 0\n" \
"s_add_u32 m0, s74, 36864\n" \
"v_mfma_f32_16x16x32_bf16 a[200:203], a[36:39], a[116:119], a[200:203]\n" \
"buffer_load_dwordx4 v64, s[36:39], s82 offen offset:0 lds\n" \
"v_mfma_f32_16x16x32_bf16 a[204:207], a[32:35], a[120:123], 0\n" \
"v_mfma_f32_16x16x32_bf16 a[204:207], a[36:39], a[124:127], a[204:207]\n" \
"s_waitcnt vmcnt(13) lgkmcnt(0)\n" \
"s_barrier\n" \
"v_mfma_f32_16x16x32_bf16 a[220:223], a[40:43], a[120:123], 0\n" \
"ds_read_b64_tr_b16 a[64:65], v102 offset:0\n" \
"ds_read_b64_tr_b16 a[66:67], v102 offset:4096\n" \
"v_mfma_f32_16x16x32_bf16 a[220:223], a[44:47], a[124:127], a[220:223]\n" \
"buffer_load_dwordx4 v65, s[36:39], s82 offen offset:1024 lds\n" \
"v_mfma_f32_16x16x32_bf16 a[216:219], a[40:43], a[112:115], 0\n" \
"ds_read_b64_tr_b16 a[68:69], v102 offset:16384\n" \
"ds_read_b64_tr_b16 a[70:71], v102 offset:20480\n" \
"v_mfma_f32_16x16x32_bf16 a[216:219], a[44:47], a[116:119], a[216:219]\n" \
"ds_read_b128 a[0:3], v96 offset:0\n" \
"ds_read_b128 a[4:7], v97 offset:0\n" \
"v_mfma_f32_16x16x32_bf16 a[212:215], a[40:43], a[104:107], 0\n" \
"ds_read_b64_tr_b16 a[72:73], v103 offset:0\n" \
"ds_read_b64_tr_b16 a[74:75], v103 offset:4096\n" \
"v_mfma_f32_16x16x32_bf16 a[212:215], a[44:47], a[108:111], a[212:215]\n" \
"buffer_load_dwordx4 v66, s[36:39], s82 offen offset:2048 lds\n" \
"v_mfma_f32_16x16x32_bf16 a[208:211], a[40:43], a[96:99], 0\n" \
"ds_read_b64_tr_b16 a[76:77], v103 offset:16384\n" \
"ds_read_b64_tr_b16 a[78:79], v103 offset:20480\n" \
"v_mfma_f32_16x16x32_bf16 a[208:211], a[44:47], a[100:103], a[208:211]\n" \
"ds_read_b64_tr_b16 a[80:81], v102 offset:512\n" \
"ds_read_b64_tr_b16 a[82:83], v102 offset:4608\n" \
"v_mfma_f32_16x16x32_bf16 a[224:227], a[48:51], a[96:99], 0\n" \
"ds_read_b64_tr_b16 a[84:85], v102 offset:16896\n" \
"ds_read_b64_tr_b16 a[86:87], v102 offset:20992\n" \
"v_mfma_f32_16x16x32_bf16 a[224:227], a[52:55], a[100:103], a[224:227]\n" \
"buffer_load_dwordx4 v67, s[36:39], s82 offen offset:3072 lds\n" \
"v_mfma_f32_16x16x32_bf16 a[228:231], a[48:51], a[104:107], 0\n" \
"s_mov_b64 s[64:65], s[14:15]\n" \
"s_add_u32 s68, s67, 0x200000\n" \
"ds_read_b64_tr_b16 a[88:89], v103 offset:512\n" \
"ds_read_b64_tr_b16 a[90:91], v103 offset:4608\n" \
"v_mfma_f32_16x16x32_bf16 a[228:231], a[52:55], a[108:111], a[228:231]\n" \
"s_add_u32 s64, s64, s68\n" \
"s_addc_u32 s65, s65, 0\n" \
"ds_read_b64_tr_b16 a[92:93], v103 offset:16896\n" \
"ds_read_b64_tr_b16 a[94:95], v103 offset:20992\n" \
"v_mfma_f32_16x16x32_bf16 a[232:235], a[48:51], a[112:115], 0\n" \
"s_xor_b32 s82, s82, 0x80\n" \
"ds_read_b128 a[8:11], v96 offset:2048\n" \
"ds_read_b128 a[12:15], v97 offset:2048\n" \
"v_mfma_f32_16x16x32_bf16 a[232:235], a[52:55], a[116:119], a[232:235]\n" \
"global_load_dword v238, v234, s[64:65]\n" \
"v_mfma_f32_16x16x32_bf16 a[236:239], a[48:51], a[120:123], 0\n" \
"s_mov_b64 s[64:65], s[16:17]\n" \
"s_add_u32 s68, s67, 0x200000\n" \
"ds_read_b128 a[16:19], v96 offset:4096\n" \
"ds_read_b128 a[20:23], v97 offset:4096\n" \
"v_mfma_f32_16x16x32_bf16 a[236:239], a[52:55], a[124:127], a[236:239]\n" \
"s_add_u32 s64, s64, s68\n" \
"s_addc_u32 s65, s65, 0\n" \
"ds_read_b128 a[24:27], v96 offset:6144\n" \
"ds_read_b128 a[28:31], v97 offset:6144\n" \
"v_mfma_f32_16x16x32_bf16 a[252:255], a[56:59], a[120:123], 0\n" \
"ds_read_b128 a[32:35], v96 offset:8192\n" \
"ds_read_b128 a[36:39], v97 offset:8192\n" \
"v_mfma_f32_16x16x32_bf16 a[252:255], a[60:63], a[124:127], a[252:255]\n" \
"global_load_dword v238, v234, s[64:65]\n" \
"v_mfma_f32_16x16x32_bf16 a[248:251], a[56:59], a[112:115], 0\n" \
"ds_read_b128 a[40:43], v96 offset:10240\n" \
"ds_read_b128 a[44:47], v97 offset:10240\n" \
"v_mfma_f32_16x16x32_bf16 a[248:251], a[60:63], a[116:119], a[248:251]\n" \
"ds_read_b128 a[48:51], v96 offset:12288\n" \
"ds_read_b128 a[52:55], v97 offset:12288\n" \
"v_mfma_f32_16x16x32_bf16 a[244:247], a[56:59], a[104:107], 0\n" \
"v_mfma_f32_16x16x32_bf16 a[244:247], a[60:63], a[108:111], a[244:247]\n" \
"v_mfma_f32_16x16x32_bf16 a[240:243], a[56:59], a[96:99], 0\n" \
"v_mfma_f32_16x16x32_bf16 a[240:243], a[60:63], a[100:103], a[240:243]\n" \
"s_waitcnt lgkmcnt(2)\n" \
"v_mfma_f32_16x16x32_bf16 v[0:3], a[64:67], a[0:3], v[0:3]\n" \
"s_lshl_b32 s79, s61, 12\n" \
"ds_read_b128 a[56:59], v96 offset:14336\n" \
"v_mfma_f32_16x16x32_bf16 v[0:3], a[68:71], a[4:7], v[0:3]\n" \
"s_add_u32 s79, s79, %3\n" \
"v_add_u32 v228, 4096, v84\n" \
"v_add_u32 v229, 3200, v84\n" \
"v_mfma_f32_16x16x32_bf16 v[4:7], a[72:75], a[0:3], v[4:7]\n" \
"s_add_u32 m0, s74, 0\n" \
"ds_read_b128 a[60:63], v97 offset:14336\n" \
"v_mfma_f32_16x16x32_bf16 v[4:7], a[76:79], a[4:7], v[4:7]\n" \
"buffer_load_dwordx4 v68, s[40:43], s83 offen offset:0 lds\n" \
"v_mfma_f32_16x16x32_bf16 v[8:11], a[80:83], a[0:3], v[8:11]\n" \
"s_lshl_b32 s78, s62, 8\n" \
"ds_read_b64_tr_b16 a[96:97], v102 offset:1024\n" \
"v_mfma_f32_16x16x32_bf16 v[8:11], a[84:87], a[4:7], v[8:11]\n" \
"s_add_u32 s89, s78, 30720\n" \
"v_mfma_f32_16x16x32_bf16 v[12:15], a[88:91], a[0:3], v[12:15]\n" \
"s_mov_b64 s[48:49], s[8:9]\n" \
"ds_read_b64_tr_b16 a[98:99], v102 offset:5120\n" \
"v_mfma_f32_16x16x32_bf16 v[12:15], a[92:95], a[4:7], v[12:15]\n" \
"buffer_load_dwordx4 v69, s[44:47], s83 offen offset:1024 lds\n" \
"v_mfma_f32_16x16x32_bf16 v[28:31], a[88:91], a[8:11], v[28:31]\n" \
"s_and_b32 s49, s49, 0xffff\n" \
"ds_read_b64_tr_b16 a[100:101], v102 offset:17408\n" \
"v_mfma_f32_16x16x32_bf16 v[28:31], a[92:95], a[12:15], v[28:31]\n" \
"s_mov_b32 s50, 4194304\n" \
"v_mfma_f32_16x16x32_bf16 v[24:27], a[80:83], a[8:11], v[24:27]\n" \
"s_mov_b32 s51, 0x20000\n" \
"ds_read_b64_tr_b16 a[102:103], v102 offset:21504\n" \
"v_mfma_f32_16x16x32_bf16 v[24:27], a[84:87], a[12:15], v[24:27]\n" \
"buffer_load_dwordx4 v70, s[40:43], s83 offen offset:2048 lds\n" \
"v_mfma_f32_16x16x32_bf16 v[20:23], a[72:75], a[8:11], v[20:23]\n" \
"s_mov_b64 s[52:53], s[12:13]\n" \
"ds_read_b64_tr_b16 a[104:105], v103 offset:1024\n" \
"v_mfma_f32_16x16x32_bf16 v[20:23], a[76:79], a[12:15], v[20:23]\n" \
"s_sub_u32 s52, s52, 4096\n" \
"s_subb_u32 s53, s53, 0\n" \
"v_mfma_f32_16x16x32_bf16 v[16:19], a[64:67], a[8:11], v[16:19]\n" \
"s_and_b32 s53, s53, 0xffff\n" \
"ds_read_b64_tr_b16 a[106:107], v103 offset:5120\n" \
"v_mfma_f32_16x16x32_bf16 v[16:19], a[68:71], a[12:15], v[16:19]\n" \
"buffer_load_dwordx4 v71, s[44:47], s83 offen offset:3072 lds\n" \
"v_mfma_f32_16x16x32_bf16 v[32:35], a[64:67], a[16:19], v[32:35]\n" \
"s_mov_b32 s54, 8392704\n" \
"s_mov_b32 s55, 0x20000\n" \
"ds_read_b64_tr_b16 a[108:109], v103 offset:17408\n" \
"v_mfma_f32_16x16x32_bf16 v[32:35], a[68:71], a[20:23], v[32:35]\n" \
"s_xor_b32 s83, s83, 0x380000\n" \
"v_lshl_add_u32 v224, v85, 11, v86\n" \
"v_add_u32 v225, 15360, v224\n" \
"v_mfma_f32_16x16x32_bf16 v[36:39], a[72:75], a[16:19], v[36:39]\n" \
"s_add_u32 m0, s74, 4096\n" \
"ds_read_b64_tr_b16 a[110:111], v103 offset:21504\n" \
"v_add_u32 v226, 129024, v224\n" \
"v_mfma_f32_16x16x32_bf16 v[36:39], a[76:79], a[20:23], v[36:39]\n" \
"buffer_load_dwordx4 v68, s[40:43], s84 offen offset:0 lds\n" \
"v_mfma_f32_16x16x32_bf16 v[40:43], a[80:83], a[16:19], v[40:43]\n" \
"ds_read_b64_tr_b16 a[112:113], v102 offset:1536\n" \
"v_add_u32 v227, 144384, v224\n" \
"v_mfma_f32_16x16x32_bf16 v[40:43], a[84:87], a[20:23], v[40:43]\n" \
"s_lshl_b32 s70, s61, 15\n" \
"v_mfma_f32_16x16x32_bf16 v[44:47], a[88:91], a[16:19], v[44:47]\n" \
"s_lshl_b32 s75, s63, 18\n" \
"ds_read_b64_tr_b16 a[114:115], v102 offset:5632\n" \
"v_mfma_f32_16x16x32_bf16 v[44:47], a[92:95], a[20:23], v[44:47]\n" \
"buffer_load_dwordx4 v69, s[44:47], s84 offen offset:1024 lds\n" \
"v_mfma_f32_16x16x32_bf16 v[60:63], a[88:91], a[24:27], v[60:63]\n" \
"s_sub_u32 s77, 3932160, s75\n" \
"ds_read_b64_tr_b16 a[116:117], v102 offset:17920\n" \
"v_mfma_f32_16x16x32_bf16 v[60:63], a[92:95], a[28:31], v[60:63]\n" \
"s_lshr_b32 s71, %1, 6\n" \
"v_mfma_f32_16x16x32_bf16 v[56:59], a[80:83], a[24:27], v[56:59]\n" \
"s_xor_b32 s71, s71, s63\n" \
"ds_read_b64_tr_b16 a[118:119], v102 offset:22016\n" \
"v_mfma_f32_16x16x32_bf16 v[56:59], a[84:87], a[28:31], v[56:59]\n" \
"buffer_load_dwordx4 v70, s[40:43], s84 offen offset:2048 lds\n" \
"v_mfma_f32_16x16x32_bf16 v[52:55], a[72:75], a[24:27], v[52:55]\n" \
"s_and_b32 s71, s71, 0\n" \
"ds_read_b64_tr_b16 a[120:121], v103 offset:1536\n" \
"v_mfma_f32_16x16x32_bf16 v[52:55], a[76:79], a[28:31], v[52:55]\n" \
"s_lshl_b32 s71, s71, 7\n" \
"v_mfma_f32_16x16x32_bf16 v[48:51], a[64:67], a[24:27], v[48:51]\n" \
"s_add_u32 s70, s70, s71\n" \
"ds_read_b64_tr_b16 a[122:123], v103 offset:5632\n" \
"v_mfma_f32_16x16x32_bf16 v[48:51], a[68:71], a[28:31], v[48:51]\n" \
"buffer_load_dwordx4 v71, s[44:47], s84 offen offset:3072 lds\n" \
"v_mfma_f32_16x16x32_bf16 v[144:147], a[64:67], a[32:35], v[144:147]\n" \
"s_add_u32 s75, s75, s70\n" \
"ds_read_b64_tr_b16 a[124:125], v103 offset:17920\n" \
"v_mfma_f32_16x16x32_bf16 v[144:147], a[68:71], a[36:39], v[144:147]\n" \
"s_add_u32 s77, s77, s70\n" \
"v_mfma_f32_16x16x32_bf16 v[148:151], a[72:75], a[32:35], v[148:151]\n" \
"s_xor_b32 s84, s84, 0x380000\n" \
"ds_read_b64_tr_b16 a[126:127], v103 offset:22016\n" \
"v_mfma_f32_16x16x32_bf16 v[148:151], a[76:79], a[36:39], v[148:151]\n" \
"s_lshl_b32 s71, s71, 12\n" \
"v_mfma_f32_16x16x32_bf16 v[152:155], a[80:83], a[32:35], v[152:155]\n" \
"s_add_u32 s78, s78, s71\n" \
"v_mfma_f32_16x16x32_bf16 v[152:155], a[84:87], a[36:39], v[152:155]\n" \
"s_add_u32 s89, s89, s71\n" \
"v_lshlrev_b32 v242, 2, v78\n" \
"v_and_b32 v236, 0xf0, v234\n" \
"v_mfma_f32_16x16x32_bf16 v[156:159], a[88:91], a[32:35], v[156:159]\n" \
"v_lshl_add_u32 v236, v87, 9, v236\n" \
"v_mfma_f32_16x16x32_bf16 v[156:159], a[92:95], a[36:39], v[156:159]\n" \
"s_add_u32 s71, s74, 0x10000\n" \
"v_add_u32 v230, s71, v88\n" \
"v_xor_b32 v231, 64, v230\n" \
"v_mfma_f32_16x16x32_bf16 v[188:191], a[88:91], a[40:43], v[188:191]\n" \
"v_add_u32 v250, 0x10000, v230\n" \
"v_add_u32 v251, 0x10000, v231\n" \
"v_mfma_f32_16x16x32_bf16 v[188:191], a[92:95], a[44:47], v[188:191]\n" \
"v_lshl_add_u32 v82, v90, 12, v89\n" \
"v_add_u32 v232, %3, v82\n" \
"v_mfma_f32_16x16x32_bf16 v[184:187], a[80:83], a[40:43], v[184:187]\n" \
"Lpg592_%=:\n" \
"Lst0x0_%=:\n" \
"s_cbranch_execz Lpg2773_%=\n" \
"v_xor_b32 v233, 32, v232\n" \
"v_mfma_f32_16x16x32_bf16 v[184:187], a[84:87], a[44:47], v[184:187]\n" \
"s_mul_i32 s87, s62, 557824\n" \
"v_mfma_f32_16x16x32_bf16 v[180:183], a[72:75], a[40:43], v[180:183]\n" \
"s_lshr_b32 s70, s61, 1\n" \
"v_mfma_f32_16x16x32_bf16 v[180:183], a[76:79], a[44:47], v[180:183]\n" \
"s_mul_i32 s70, s70, 17408\n" \
"v_mfma_f32_16x16x32_bf16 v[176:179], a[64:67], a[40:43], v[176:179]\n" \
"s_add_u32 s87, s87, s70\n" \
"v_mfma_f32_16x16x32_bf16 v[176:179], a[68:71], a[44:47], v[176:179]\n" \
"s_waitcnt lgkmcnt(0)\n" \
"s_and_b32 s70, s61, 1\n" \
"v_mfma_f32_16x16x32_bf16 v[192:195], a[64:67], a[48:51], v[192:195]\n" \
"s_lshl_b32 s70, s70, 12\n" \
"v_mfma_f32_16x16x32_bf16 v[192:195], a[68:71], a[52:55], v[192:195]\n" \
"s_add_u32 s87, s87, s70\n" \
"v_mfma_f32_16x16x32_bf16 v[196:199], a[72:75], a[48:51], v[196:199]\n" \
"s_mul_i32 s70, s63, 34816\n" \
"v_mfma_f32_16x16x32_bf16 v[196:199], a[76:79], a[52:55], v[196:199]\n" \
"s_add_u32 s94, s87, 522240\n" \
"v_mfma_f32_16x16x32_bf16 v[200:203], a[80:83], a[48:51], v[200:203]\n" \
"s_sub_u32 s94, s94, s70\n" \
"v_mfma_f32_16x16x32_bf16 v[200:203], a[84:87], a[52:55], v[200:203]\n" \
"s_add_u32 s87, s87, s70\n" \
"v_mfma_f32_16x16x32_bf16 v[204:207], a[88:91], a[48:51], v[204:207]\n" \
"s_lshl_b32 s90, s62, 7\n" \
"v_mfma_f32_16x16x32_bf16 v[204:207], a[92:95], a[52:55], v[204:207]\n" \
"s_add_u32 s95, s90, 65597440\n" \
"v_mfma_f32_16x16x32_bf16 v[220:223], a[88:91], a[56:59], v[220:223]\n" \
"s_add_u32 s90, s90, 65585152\n" \
"v_mfma_f32_16x16x32_bf16 v[220:223], a[92:95], a[60:63], v[220:223]\n" \
"s_lshl_b32 s92, 0xff, s63\n" \
"v_mfma_f32_16x16x32_bf16 v[216:219], a[80:83], a[56:59], v[216:219]\n" \
"s_and_b32 s92, s92, 0xff\n" \
"v_mfma_f32_16x16x32_bf16 v[216:219], a[84:87], a[60:63], v[216:219]\n" \
"s_mov_b32 s76, 0x3e0293ee\n" \
"v_mfma_f32_16x16x32_bf16 v[212:215], a[72:75], a[56:59], v[212:215]\n" \
"s_mov_b32 s31, 0x3e800000\n" \
"v_mfma_f32_16x16x32_bf16 v[212:215], a[76:79], a[60:63], v[212:215]\n" \
"v_mfma_f32_16x16x32_bf16 v[208:211], a[64:67], a[56:59], v[208:211]\n" \
"v_mfma_f32_16x16x32_bf16 v[208:211], a[68:71], a[60:63], v[208:211]\n" \
"s_waitcnt vmcnt(10) lgkmcnt(0)\n" \
"s_barrier\n" \
"v_readfirstlane_b32 s60, v240\n" \
"s_add_u32 s60, s60, 0x9e3779b9\n" \
"v_mfma_f32_16x16x32_bf16 a[128:131], a[0:3], a[96:99], a[128:131]\n" \
"s_add_u32 m0, s74, 131072\n" \
"ds_read_b64_tr_b16 a[64:65], v98 offset:32768\n" \
"v_mfma_f32_16x16x32_bf16 a[128:131], a[4:7], a[100:103], a[128:131]\n" \
"buffer_load_dwordx4 v64, s[36:39], s81 offen offset:0 lds\n" \
"v_mfma_f32_16x16x32_bf16 a[132:135], a[0:3], a[104:107], a[132:135]\n" \
"ds_read_b64_tr_b16 a[66:67], v98 offset:36864\n" \
"v_mfma_f32_16x16x32_bf16 a[132:135], a[4:7], a[108:111], a[132:135]\n" \
"v_mfma_f32_16x16x32_bf16 a[136:139], a[0:3], a[112:115], a[136:139]\n" \
"ds_read_b64_tr_b16 a[68:69], v98 offset:49152\n" \
"v_mfma_f32_16x16x32_bf16 a[136:139], a[4:7], a[116:119], a[136:139]\n" \
"buffer_load_dwordx4 v65, s[36:39], s81 offen offset:1024 lds\n" \
"v_mfma_f32_16x16x32_bf16 a[140:143], a[0:3], a[120:123], a[140:143]\n" \
"ds_read_b64_tr_b16 a[70:71], v98 offset:53248\n" \
"v_mfma_f32_16x16x32_bf16 a[140:143], a[4:7], a[124:127], a[140:143]\n" \
"v_mfma_f32_16x16x32_bf16 a[156:159], a[8:11], a[120:123], a[156:159]\n" \
"ds_read_b128 a[0:3], v72 offset:32768\n" \
"v_mfma_f32_16x16x32_bf16 a[156:159], a[12:15], a[124:127], a[156:159]\n" \
"buffer_load_dwordx4 v66, s[36:39], s81 offen offset:2048 lds\n" \
"v_mfma_f32_16x16x32_bf16 a[152:155], a[8:11], a[112:115], a[152:155]\n" \
"ds_read_b64_tr_b16 a[72:73], v99 offset:32768\n" \
"v_mfma_f32_16x16x32_bf16 a[152:155], a[12:15], a[116:119], a[152:155]\n" \
"v_mfma_f32_16x16x32_bf16 a[148:151], a[8:11], a[104:107], a[148:151]\n" \
"ds_read_b128 a[4:7], v73 offset:32768\n" \
"v_mfma_f32_16x16x32_bf16 a[148:151], a[12:15], a[108:111], a[148:151]\n" \
"buffer_load_dwordx4 v67, s[36:39], s81 offen offset:3072 lds\n" \
"v_mfma_f32_16x16x32_bf16 a[144:147], a[8:11], a[96:99], a[144:147]\n" \
"ds_read_b64_tr_b16 a[74:75], v99 offset:36864\n" \
"v_mfma_f32_16x16x32_bf16 a[144:147], a[12:15], a[100:103], a[144:147]\n" \
"s_xor_b32 s81, s81, 0x380\n" \
"v_mfma_f32_16x16x32_bf16 a[160:163], a[16:19], a[96:99], a[160:163]\n" \
"s_add_u32 m0, s74, 135168\n" \
"ds_read_b128 a[8:11], v72 offset:34816\n" \
"v_mfma_f32_16x16x32_bf16 a[160:163], a[20:23], a[100:103], a[160:163]\n" \
"buffer_load_dwordx4 v64, s[36:39], s82 offen offset:0 lds\n" \
"v_mfma_f32_16x16x32_bf16 a[164:167], a[16:19], a[104:107], a[164:167]\n" \
"ds_read_b64_tr_b16 a[76:77], v99 offset:49152\n" \
"v_mfma_f32_16x16x32_bf16 a[164:167], a[20:23], a[108:111], a[164:167]\n" \
"v_mfma_f32_16x16x32_bf16 a[168:171], a[16:19], a[112:115], a[168:171]\n" \
"ds_read_b128 a[12:15], v73 offset:34816\n" \
"v_mfma_f32_16x16x32_bf16 a[168:171], a[20:23], a[116:119], a[168:171]\n" \
"buffer_load_dwordx4 v65, s[36:39], s82 offen offset:1024 lds\n" \
"v_mfma_f32_16x16x32_bf16 a[172:175], a[16:19], a[120:123], a[172:175]\n" \
"ds_read_b64_tr_b16 a[78:79], v99 offset:53248\n" \
"v_mfma_f32_16x16x32_bf16 a[172:175], a[20:23], a[124:127], a[172:175]\n" \
"v_mfma_f32_16x16x32_bf16 a[188:191], a[24:27], a[120:123], a[188:191]\n" \
"ds_read_b128 a[16:19], v72 offset:36864\n" \
"v_mfma_f32_16x16x32_bf16 a[188:191], a[28:31], a[124:127], a[188:191]\n" \
"buffer_load_dwordx4 v66, s[36:39], s82 offen offset:2048 lds\n" \
"v_mfma_f32_16x16x32_bf16 a[184:187], a[24:27], a[112:115], a[184:187]\n" \
"ds_read_b64_tr_b16 a[80:81], v98 offset:33280\n" \
"v_mfma_f32_16x16x32_bf16 a[184:187], a[28:31], a[116:119], a[184:187]\n" \
"v_mfma_f32_16x16x32_bf16 a[180:183], a[24:27], a[104:107], a[180:183]\n" \
"ds_read_b128 a[20:23], v73 offset:36864\n" \
"v_mfma_f32_16x16x32_bf16 a[180:183], a[28:31], a[108:111], a[180:183]\n" \
"buffer_load_dwordx4 v67, s[36:39], s82 offen offset:3072 lds\n" \
"v_mfma_f32_16x16x32_bf16 a[176:179], a[24:27], a[96:99], a[176:179]\n" \
"s_mov_b64 s[64:65], s[10:11]\n" \
"s_lshl1_add_u32 s68, s66, 512\n" \
"ds_read_b64_tr_b16 a[82:83], v98 offset:37376\n" \
"v_mfma_f32_16x16x32_bf16 a[176:179], a[28:31], a[100:103], a[176:179]\n" \
"s_add_u32 s64, s64, s68\n" \
"s_addc_u32 s65, s65, 0\n" \
"v_mfma_f32_16x16x32_bf16 a[192:195], a[32:35], a[96:99], a[192:195]\n" \
"s_xor_b32 s82, s82, 0x380\n" \
"ds_read_b128 a[24:27], v72 offset:38912\n" \
"v_mfma_f32_16x16x32_bf16 a[192:195], a[36:39], a[100:103], a[192:195]\n" \
"global_load_dword v238, v110, s[64:65]\n" \
"v_mfma_f32_16x16x32_bf16 a[196:199], a[32:35], a[104:107], a[196:199]\n" \
"s_mov_b64 s[64:65], s[14:15]\n" \
"s_add_u32 s68, s67, 0x300000\n" \
"ds_read_b64_tr_b16 a[84:85], v98 offset:49664\n" \
"v_mfma_f32_16x16x32_bf16 a[196:199], a[36:39], a[108:111], a[196:199]\n" \
"s_add_u32 s64, s64, s68\n" \
"s_addc_u32 s65, s65, 0\n" \
"v_mfma_f32_16x16x32_bf16 a[200:203], a[32:35], a[112:115], a[200:203]\n" \
"ds_read_b128 a[28:31], v73 offset:38912\n" \
"v_mfma_f32_16x16x32_bf16 a[200:203], a[36:39], a[116:119], a[200:203]\n" \
"global_load_dword v238, v234, s[64:65]\n" \
"v_mfma_f32_16x16x32_bf16 a[204:207], a[32:35], a[120:123], a[204:207]\n" \
"ds_read_b64_tr_b16 a[86:87], v98 offset:53760\n" \
"v_mfma_f32_16x16x32_bf16 a[204:207], a[36:39], a[124:127], a[204:207]\n" \
"v_mfma_f32_16x16x32_bf16 a[220:223], a[40:43], a[120:123], a[220:223]\n" \
"ds_read_b128 a[32:35], v72 offset:40960\n" \
"v_mfma_f32_16x16x32_bf16 a[220:223], a[44:47], a[124:127], a[220:223]\n" \
"v_mfma_f32_16x16x32_bf16 a[216:219], a[40:43], a[112:115], a[216:219]\n" \
"ds_read_b64_tr_b16 a[88:89], v99 offset:33280\n" \
"v_mfma_f32_16x16x32_bf16 a[216:219], a[44:47], a[116:119], a[216:219]\n" \
"v_mfma_f32_16x16x32_bf16 a[212:215], a[40:43], a[104:107], a[212:215]\n" \
"ds_read_b128 a[36:39], v73 offset:40960\n" \
"v_mfma_f32_16x16x32_bf16 a[212:215], a[44:47], a[108:111], a[212:215]\n" \
"v_mfma_f32_16x16x32_bf16 a[208:211], a[40:43], a[96:99], a[208:211]\n" \
"ds_read_b64_tr_b16 a[90:91], v99 offset:37376\n" \
"v_mfma_f32_16x16x32_bf16 a[208:211], a[44:47], a[100:103], a[208:211]\n" \
"v_mfma_f32_16x16x32_bf16 a[224:227], a[48:51], a[96:99], a[224:227]\n" \
"ds_read_b128 a[40:43], v72 offset:43008\n" \
"v_mfma_f32_16x16x32_bf16 a[224:227], a[52:55], a[100:103], a[224:227]\n" \
"v_mfma_f32_16x16x32_bf16 a[228:231], a[48:51], a[104:107], a[228:231]\n" \
"ds_read_b64_tr_b16 a[92:93], v99 offset:49664\n" \
"v_mfma_f32_16x16x32_bf16 a[228:231], a[52:55], a[108:111], a[228:231]\n" \
"v_mfma_f32_16x16x32_bf16 a[232:235], a[48:51], a[112:115], a[232:235]\n" \
"ds_read_b128 a[44:47], v73 offset:43008\n" \
"v_mfma_f32_16x16x32_bf16 a[232:235], a[52:55], a[116:119], a[232:235]\n" \
"v_mfma_f32_16x16x32_bf16 a[236:239], a[48:51], a[120:123], a[236:239]\n" \
"ds_read_b64_tr_b16 a[94:95], v99 offset:53760\n" \
"v_mfma_f32_16x16x32_bf16 a[236:239], a[52:55], a[124:127], a[236:239]\n" \
"v_mfma_f32_16x16x32_bf16 a[252:255], a[56:59], a[120:123], a[252:255]\n" \
"ds_read_b128 a[48:51], v72 offset:45056\n" \
"v_mfma_f32_16x16x32_bf16 a[252:255], a[60:63], a[124:127], a[252:255]\n" \
"v_mfma_f32_16x16x32_bf16 a[248:251], a[56:59], a[112:115], a[248:251]\n" \
"v_mfma_f32_16x16x32_bf16 a[248:251], a[60:63], a[116:119], a[248:251]\n" \
"v_mfma_f32_16x16x32_bf16 a[244:247], a[56:59], a[104:107], a[244:247]\n" \
"ds_read_b128 a[52:55], v73 offset:45056\n" \
"v_mfma_f32_16x16x32_bf16 a[244:247], a[60:63], a[108:111], a[244:247]\n" \
"v_mfma_f32_16x16x32_bf16 a[240:243], a[56:59], a[96:99], a[240:243]\n" \
"v_mfma_f32_16x16x32_bf16 a[240:243], a[60:63], a[100:103], a[240:243]\n" \
"s_waitcnt lgkmcnt(1)\n" \
"v_mfma_f32_16x16x32_bf16 v[0:3], a[64:67], a[0:3], v[0:3]\n" \
"ds_read_b128 a[56:59], v72 offset:47104\n" \
"v_lshrrev_b32 v104, 4, v234\n" \
"v_mfma_f32_16x16x32_bf16 v[0:3], a[68:71], a[4:7], v[0:3]\n" \
"v_bfe_u32 v105, v104, 4, 1\n" \
"v_bfe_u32 v106, v104, 2, 2\n" \
"v_mfma_f32_16x16x32_bf16 v[4:7], a[72:75], a[0:3], v[4:7]\n" \
"s_add_u32 m0, s74, 65536\n" \
"ds_read_b128 a[60:63], v73 offset:47104\n" \
"v_lshl_add_u32 v106, v105, 3, v106\n" \
"v_mfma_f32_16x16x32_bf16 v[4:7], a[76:79], a[4:7], v[4:7]\n" \
"buffer_load_dwordx4 v68, s[40:43], s83 offen offset:0 lds\n" \
"v_mfma_f32_16x16x32_bf16 v[8:11], a[80:83], a[0:3], v[8:11]\n" \
"s_lshl_b32 s71, s61, 4\n" \
"ds_read_b64_tr_b16 a[96:97], v98 offset:33792\n" \
"v_add_u32 v106, s71, v106\n" \
"v_mfma_f32_16x16x32_bf16 v[8:11], a[84:87], a[4:7], v[8:11]\n" \
"v_mul_u32_u24 v106, 6144, v106\n" \
"v_lshlrev_b32 v105, 1, v105\n" \
"v_mfma_f32_16x16x32_bf16 v[12:15], a[88:91], a[0:3], v[12:15]\n" \
"ds_read_b64_tr_b16 a[98:99], v98 offset:37888\n" \
"v_and_b32 v107, 3, v104\n" \
"v_mfma_f32_16x16x32_bf16 v[12:15], a[92:95], a[4:7], v[12:15]\n" \
"buffer_load_dwordx4 v69, s[44:47], s83 offen offset:1024 lds\n" \
"v_mfma_f32_16x16x32_bf16 v[28:31], a[88:91], a[8:11], v[28:31]\n" \
"ds_read_b64_tr_b16 a[100:101], v98 offset:50176\n" \
"v_xor_b32 v105, v105, v107\n" \
"v_mfma_f32_16x16x32_bf16 v[28:31], a[92:95], a[12:15], v[28:31]\n" \
"v_lshl_add_u32 v106, v105, 4, v106\n" \
"v_lshrrev_b32 v105, 5, v104\n" \
"v_mfma_f32_16x16x32_bf16 v[24:27], a[80:83], a[8:11], v[24:27]\n" \
"ds_read_b64_tr_b16 a[102:103], v98 offset:54272\n" \
"v_lshl_add_u32 v244, v105, 6, v106\n" \
"v_mfma_f32_16x16x32_bf16 v[24:27], a[84:87], a[12:15], v[24:27]\n" \
"buffer_load_dwordx4 v70, s[40:43], s83 offen offset:2048 lds\n" \
"v_mfma_f32_16x16x32_bf16 v[20:23], a[72:75], a[8:11], v[20:23]\n" \
"ds_read_b64_tr_b16 a[104:105], v99 offset:33792\n" \
"v_lshrrev_b32 v107, 3, v104\n" \
"v_mfma_f32_16x16x32_bf16 v[20:23], a[76:79], a[12:15], v[20:23]\n" \
"v_and_b32 v108, 7, v104\n" \
"v_xor_b32 v109, v108, v107\n" \
"v_mfma_f32_16x16x32_bf16 v[16:19], a[64:67], a[8:11], v[16:19]\n" \
"ds_read_b64_tr_b16 a[106:107], v99 offset:37888\n" \
"v_lshlrev_b32 v109, 4, v109\n" \
"v_mfma_f32_16x16x32_bf16 v[16:19], a[68:71], a[12:15], v[16:19]\n" \
"buffer_load_dwordx4 v71, s[44:47], s83 offen offset:3072 lds\n" \
"v_mfma_f32_16x16x32_bf16 v[32:35], a[64:67], a[16:19], v[32:35]\n" \
"ds_read_b64_tr_b16 a[108:109], v99 offset:50176\n" \
"v_lshl_add_u32 v245, v107, 12, v109\n" \
"v_mfma_f32_16x16x32_bf16 v[32:35], a[68:71], a[20:23], v[32:35]\n" \
"s_xor_b32 s83, s83, 0x80000\n" \
"v_mfma_f32_16x16x32_bf16 v[36:39], a[72:75], a[16:19], v[36:39]\n" \
"s_add_u32 m0, s74, 69632\n" \
"ds_read_b64_tr_b16 a[110:111], v99 offset:54272\n" \
"v_mfma_f32_16x16x32_bf16 v[36:39], a[76:79], a[20:23], v[36:39]\n" \
"buffer_load_dwordx4 v68, s[40:43], s84 offen offset:0 lds\n" \
"v_mfma_f32_16x16x32_bf16 v[40:43], a[80:83], a[16:19], v[40:43]\n" \
"s_lshr_b32 s71, s61, 1\n" \
"ds_read_b64_tr_b16 a[112:113], v98 offset:34304\n" \
"v_mfma_f32_16x16x32_bf16 v[40:43], a[84:87], a[20:23], v[40:43]\n" \
"s_lshl_b32 s70, s71, 11\n" \
"v_lshrrev_b32 v107, 4, v104\n" \
"v_xor_b32 v107, v107, v108\n" \
"v_mfma_f32_16x16x32_bf16 v[44:47], a[88:91], a[16:19], v[44:47]\n" \
"ds_read_b64_tr_b16 a[114:115], v98 offset:38400\n" \
"v_and_b32 v109, 15, v104\n" \
"v_mfma_f32_16x16x32_bf16 v[44:47], a[92:95], a[20:23], v[44:47]\n" \
"buffer_load_dwordx4 v69, s[44:47], s84 offen offset:1024 lds\n" \
"v_mfma_f32_16x16x32_bf16 v[60:63], a[88:91], a[24:27], v[60:63]\n" \
"ds_read_b64_tr_b16 a[116:117], v98 offset:50688\n" \
"v_lshlrev_b32 v106, 7, v109\n" \
"v_mfma_f32_16x16x32_bf16 v[60:63], a[92:95], a[28:31], v[60:63]\n" \
"v_lshl_add_u32 v106, v107, 4, v106\n" \
"v_mfma_f32_16x16x32_bf16 v[56:59], a[80:83], a[24:27], v[56:59]\n" \
"s_add_u32 s70, s70, %3\n" \
"ds_read_b64_tr_b16 a[118:119], v98 offset:54784\n" \
"v_add_u32 v247, s70, v106\n" \
"v_mfma_f32_16x16x32_bf16 v[56:59], a[84:87], a[28:31], v[56:59]\n" \
"buffer_load_dwordx4 v70, s[40:43], s84 offen offset:2048 lds\n" \
"v_mfma_f32_16x16x32_bf16 v[52:55], a[72:75], a[24:27], v[52:55]\n" \
"ds_read_b64_tr_b16 a[120:121], v99 offset:34304\n" \
"v_lshrrev_b32 v109, 3, v109\n" \
"v_mfma_f32_16x16x32_bf16 v[52:55], a[76:79], a[28:31], v[52:55]\n" \
"v_mul_u32_u24 v109, 7168, v109\n" \
"v_add_u32 v109, v109, v106\n" \
"v_mfma_f32_16x16x32_bf16 v[48:51], a[64:67], a[24:27], v[48:51]\n" \
"s_add_u32 s70, %3, 131072\n" \
"ds_read_b64_tr_b16 a[122:123], v99 offset:38400\n" \
"v_add_u32 v248, s70, v109\n" \
"v_mfma_f32_16x16x32_bf16 v[48:51], a[68:71], a[28:31], v[48:51]\n" \
"buffer_load_dwordx4 v71, s[44:47], s84 offen offset:3072 lds\n" \
"v_mfma_f32_16x16x32_bf16 v[144:147], a[64:67], a[32:35], v[144:147]\n" \
"ds_read_b64_tr_b16 a[124:125], v99 offset:50688\n" \
"v_and_b32 v106, 3, v104\n" \
"v_mfma_f32_16x16x32_bf16 v[144:147], a[68:71], a[36:39], v[144:147]\n" \
"v_lshlrev_b32 v106, 3, v106\n" \
"v_bfe_u32 v107, v104, 2, 2\n" \
"v_mfma_f32_16x16x32_bf16 v[148:151], a[72:75], a[32:35], v[148:151]\n" \
"s_xor_b32 s84, s84, 0x80000\n" \
"ds_read_b64_tr_b16 a[126:127], v99 offset:54784\n" \
"v_lshl_add_u32 v106, v107, 6, v106\n" \
"v_mfma_f32_16x16x32_bf16 v[148:151], a[76:79], a[36:39], v[148:151]\n" \
"v_bfe_u32 v107, v104, 4, 1\n" \
"v_mul_u32_u24 v107, 288, v107\n" \
"v_mfma_f32_16x16x32_bf16 v[152:155], a[80:83], a[32:35], v[152:155]\n" \
"v_mul_u32_u24 v108, 6144, v105\n" \
"v_add3_u32 v106, v106, v107, v108\n" \
"v_mfma_f32_16x16x32_bf16 v[152:155], a[84:87], a[36:39], v[152:155]\n" \
"s_and_b32 s70, s61, 1\n" \
"v_mfma_f32_16x16x32_bf16 v[156:159], a[88:91], a[32:35], v[156:159]\n" \
"s_lshl_b32 s70, s70, 9\n" \
"v_mfma_f32_16x16x32_bf16 v[156:159], a[92:95], a[36:39], v[156:159]\n" \
"s_add_u32 s70, s70, %3\n" \
"v_add_u32 v249, s70, v106\n" \
"v_mfma_f32_16x16x32_bf16 v[188:191], a[88:91], a[40:43], v[188:191]\n" \
"s_lshl_b32 s70, s71, 5\n" \
"v_xor_b32 v249, s70, v249\n" \
"v_mfma_f32_16x16x32_bf16 v[188:191], a[92:95], a[44:47], v[188:191]\n" \
"v_mfma_f32_16x16x32_bf16 v[184:187], a[80:83], a[40:43], v[184:187]\n" \
"v_mfma_f32_16x16x32_bf16 v[184:187], a[84:87], a[44:47], v[184:187]\n" \
"v_mfma_f32_16x16x32_bf16 v[180:183], a[72:75], a[40:43], v[180:183]\n" \
"v_mfma_f32_16x16x32_bf16 v[180:183], a[76:79], a[44:47], v[180:183]\n" \
"v_mfma_f32_16x16x32_bf16 v[176:179], a[64:67], a[40:43], v[176:179]\n" \
"v_mfma_f32_16x16x32_bf16 v[176:179], a[68:71], a[44:47], v[176:179]\n" \
"s_waitcnt lgkmcnt(0)\n" \
"v_mfma_f32_16x16x32_bf16 v[192:195], a[64:67], a[48:51], v[192:195]\n" \
"v_mfma_f32_16x16x32_bf16 v[192:195], a[68:71], a[52:55], v[192:195]\n" \
"v_mfma_f32_16x16x32_bf16 v[196:199], a[72:75], a[48:51], v[196:199]\n" \
"v_mfma_f32_16x16x32_bf16 v[196:199], a[76:79], a[52:55], v[196:199]\n" \
"v_mfma_f32_16x16x32_bf16 v[200:203], a[80:83], a[48:51], v[200:203]\n" \
"v_mfma_f32_16x16x32_bf16 v[200:203], a[84:87], a[52:55], v[200:203]\n" \
"v_mfma_f32_16x16x32_bf16 v[204:207], a[88:91], a[48:51], v[204:207]\n" \
"v_mfma_f32_16x16x32_bf16 v[204:207], a[92:95], a[52:55], v[204:207]\n" \
"v_mfma_f32_16x16x32_bf16 v[220:223], a[88:91], a[56:59], v[220:223]\n" \
"v_mfma_f32_16x16x32_bf16 v[220:223], a[92:95], a[60:63], v[220:223]\n" \
"v_mfma_f32_16x16x32_bf16 v[216:219], a[80:83], a[56:59], v[216:219]\n" \
"v_mfma_f32_16x16x32_bf16 v[216:219], a[84:87], a[60:63], v[216:219]\n" \
"v_mfma_f32_16x16x32_bf16 v[212:215], a[72:75], a[56:59], v[212:215]\n" \
"v_mfma_f32_16x16x32_bf16 v[212:215], a[76:79], a[60:63], v[212:215]\n" \
"v_mfma_f32_16x16x32_bf16 v[208:211], a[64:67], a[56:59], v[208:211]\n" \
"v_mfma_f32_16x16x32_bf16 v[208:211], a[68:71], a[60:63], v[208:211]\n" \
"s_waitcnt vmcnt(10) lgkmcnt(0)\n" \
"s_barrier\n" \
"v_mfma_f32_16x16x32_bf16 a[128:131], a[0:3], a[96:99], a[128:131]\n" \
"s_add_u32 m0, s74, 98304\n" \
"ds_read_b64_tr_b16 a[64:65], v74 offset:0\n" \
"v_mfma_f32_16x16x32_bf16 a[128:131], a[4:7], a[100:103], a[128:131]\n" \
"buffer_load_dwordx4 v64, s[36:39], s81 offen offset:0 lds\n" \
"v_mfma_f32_16x16x32_bf16 a[132:135], a[0:3], a[104:107], a[132:135]\n" \
"ds_read_b64_tr_b16 a[66:67], v74 offset:4096\n" \
"v_mfma_f32_16x16x32_bf16 a[132:135], a[4:7], a[108:111], a[132:135]\n" \
"v_mfma_f32_16x16x32_bf16 a[136:139], a[0:3], a[112:115], a[136:139]\n" \
"ds_read_b64_tr_b16 a[68:69], v74 offset:16384\n" \
"v_mfma_f32_16x16x32_bf16 a[136:139], a[4:7], a[116:119], a[136:139]\n" \
"buffer_load_dwordx4 v65, s[36:39], s81 offen offset:1024 lds\n" \
"v_mfma_f32_16x16x32_bf16 a[140:143], a[0:3], a[120:123], a[140:143]\n" \
"ds_read_b64_tr_b16 a[70:71], v74 offset:20480\n" \
"v_mfma_f32_16x16x32_bf16 a[140:143], a[4:7], a[124:127], a[140:143]\n" \
"v_mfma_f32_16x16x32_bf16 a[156:159], a[8:11], a[120:123], a[156:159]\n" \
"ds_read_b128 a[0:3], v100 offset:0\n" \
"v_mfma_f32_16x16x32_bf16 a[156:159], a[12:15], a[124:127], a[156:159]\n" \
"buffer_load_dwordx4 v66, s[36:39], s81 offen offset:2048 lds\n" \
"v_mfma_f32_16x16x32_bf16 a[152:155], a[8:11], a[112:115], a[152:155]\n" \
"ds_read_b64_tr_b16 a[72:73], v75 offset:0\n" \
"v_mfma_f32_16x16x32_bf16 a[152:155], a[12:15], a[116:119], a[152:155]\n" \
"v_mfma_f32_16x16x32_bf16 a[148:151], a[8:11], a[104:107], a[148:151]\n" \
"ds_read_b128 a[4:7], v101 offset:0\n" \
"v_mfma_f32_16x16x32_bf16 a[148:151], a[12:15], a[108:111], a[148:151]\n" \
"buffer_load_dwordx4 v67, s[36:39], s81 offen offset:3072 lds\n" \
"v_mfma_f32_16x16x32_bf16 a[144:147], a[8:11], a[96:99], a[144:147]\n" \
"ds_read_b64_tr_b16 a[74:75], v75 offset:4096\n" \
"v_mfma_f32_16x16x32_bf16 a[144:147], a[12:15], a[100:103], a[144:147]\n" \
"s_xor_b32 s81, s81, 0x80\n" \
"v_mfma_f32_16x16x32_bf16 a[160:163], a[16:19], a[96:99], a[160:163]\n" \
"s_add_u32 m0, s74, 102400\n" \
"ds_read_b128 a[8:11], v100 offset:2048\n" \
"v_mfma_f32_16x16x32_bf16 a[160:163], a[20:23], a[100:103], a[160:163]\n" \
"buffer_load_dwordx4 v64, s[36:39], s82 offen offset:0 lds\n" \
"v_mfma_f32_16x16x32_bf16 a[164:167], a[16:19], a[104:107], a[164:167]\n" \
"ds_read_b64_tr_b16 a[76:77], v75 offset:16384\n" \
"v_mfma_f32_16x16x32_bf16 a[164:167], a[20:23], a[108:111], a[164:167]\n" \
"v_mfma_f32_16x16x32_bf16 a[168:171], a[16:19], a[112:115], a[168:171]\n" \
"ds_read_b128 a[12:15], v101 offset:2048\n" \
"v_mfma_f32_16x16x32_bf16 a[168:171], a[20:23], a[116:119], a[168:171]\n" \
"buffer_load_dwordx4 v65, s[36:39], s82 offen offset:1024 lds\n" \
"v_mfma_f32_16x16x32_bf16 a[172:175], a[16:19], a[120:123], a[172:175]\n" \
"ds_read_b64_tr_b16 a[78:79], v75 offset:20480\n" \
"v_mfma_f32_16x16x32_bf16 a[172:175], a[20:23], a[124:127], a[172:175]\n" \
"v_mfma_f32_16x16x32_bf16 a[188:191], a[24:27], a[120:123], a[188:191]\n" \
"ds_read_b128 a[16:19], v100 offset:4096\n" \
"v_mfma_f32_16x16x32_bf16 a[188:191], a[28:31], a[124:127], a[188:191]\n" \
"buffer_load_dwordx4 v66, s[36:39], s82 offen offset:2048 lds\n" \
"v_mfma_f32_16x16x32_bf16 a[184:187], a[24:27], a[112:115], a[184:187]\n" \
"ds_read_b64_tr_b16 a[80:81], v74 offset:512\n" \
"v_mfma_f32_16x16x32_bf16 a[184:187], a[28:31], a[116:119], a[184:187]\n" \
"v_mfma_f32_16x16x32_bf16 a[180:183], a[24:27], a[104:107], a[180:183]\n" \
"ds_read_b128 a[20:23], v101 offset:4096\n" \
"v_mfma_f32_16x16x32_bf16 a[180:183], a[28:31], a[108:111], a[180:183]\n" \
"buffer_load_dwordx4 v67, s[36:39], s82 offen offset:3072 lds\n" \
"v_mfma_f32_16x16x32_bf16 a[176:179], a[24:27], a[96:99], a[176:179]\n" \
"s_mov_b64 s[64:65], s[16:17]\n" \
"s_add_u32 s68, s67, 0x300000\n" \
"ds_read_b64_tr_b16 a[82:83], v74 offset:4608\n" \
"v_mfma_f32_16x16x32_bf16 a[176:179], a[28:31], a[100:103], a[176:179]\n" \
"s_add_u32 s64, s64, s68\n" \
"s_addc_u32 s65, s65, 0\n" \
"v_mfma_f32_16x16x32_bf16 a[192:195], a[32:35], a[96:99], a[192:195]\n" \
"s_xor_b32 s82, s82, 0x80\n" \
"ds_read_b128 a[24:27], v100 offset:6144\n" \
"v_mfma_f32_16x16x32_bf16 a[192:195], a[36:39], a[100:103], a[192:195]\n" \
"global_load_dword v238, v234, s[64:65]\n" \
"v_mfma_f32_16x16x32_bf16 a[196:199], a[32:35], a[104:107], a[196:199]\n" \
"s_mov_b64 s[64:65], s[8:9]\n" \
"s_add_u32 s68, s66, 0x0\n" \
"ds_read_b64_tr_b16 a[84:85], v74 offset:16896\n" \
"v_mfma_f32_16x16x32_bf16 a[196:199], a[36:39], a[108:111], a[196:199]\n" \
"s_add_u32 s64, s64, s68\n" \
"s_addc_u32 s65, s65, 0\n" \
"v_mfma_f32_16x16x32_bf16 a[200:203], a[32:35], a[112:115], a[200:203]\n" \
"ds_read_b128 a[28:31], v101 offset:6144\n" \
"v_mfma_f32_16x16x32_bf16 a[200:203], a[36:39], a[116:119], a[200:203]\n" \
"global_load_dword v238, v234, s[64:65]\n" \
"v_mfma_f32_16x16x32_bf16 a[204:207], a[32:35], a[120:123], a[204:207]\n" \
"s_mov_b64 s[64:65], s[8:9]\n" \
"s_add_u32 s68, s66, 0x100000\n" \
"ds_read_b64_tr_b16 a[86:87], v74 offset:20992\n" \
"v_mfma_f32_16x16x32_bf16 a[204:207], a[36:39], a[124:127], a[204:207]\n" \
"s_add_u32 s64, s64, s68\n" \
"s_addc_u32 s65, s65, 0\n" \
"v_mfma_f32_16x16x32_bf16 a[220:223], a[40:43], a[120:123], a[220:223]\n" \
"ds_read_b128 a[32:35], v100 offset:8192\n" \
"v_mfma_f32_16x16x32_bf16 a[220:223], a[44:47], a[124:127], a[220:223]\n" \
"global_load_dword v238, v234, s[64:65]\n" \
"v_mfma_f32_16x16x32_bf16 a[216:219], a[40:43], a[112:115], a[216:219]\n" \
"ds_read_b64_tr_b16 a[88:89], v75 offset:512\n" \
"v_mfma_f32_16x16x32_bf16 a[216:219], a[44:47], a[116:119], a[216:219]\n" \
"v_mfma_f32_16x16x32_bf16 a[212:215], a[40:43], a[104:107], a[212:215]\n" \
"ds_read_b128 a[36:39], v101 offset:8192\n" \
"v_mfma_f32_16x16x32_bf16 a[212:215], a[44:47], a[108:111], a[212:215]\n" \
"v_mfma_f32_16x16x32_bf16 a[208:211], a[40:43], a[96:99], a[208:211]\n" \
"ds_read_b64_tr_b16 a[90:91], v75 offset:4608\n" \
"v_mfma_f32_16x16x32_bf16 a[208:211], a[44:47], a[100:103], a[208:211]\n" \
"v_mfma_f32_16x16x32_bf16 a[224:227], a[48:51], a[96:99], a[224:227]\n" \
"ds_read_b128 a[40:43], v100 offset:10240\n" \
"v_mfma_f32_16x16x32_bf16 a[224:227], a[52:55], a[100:103], a[224:227]\n" \
"v_mfma_f32_16x16x32_bf16 a[228:231], a[48:51], a[104:107], a[228:231]\n" \
"ds_read_b64_tr_b16 a[92:93], v75 offset:16896\n" \
"v_mfma_f32_16x16x32_bf16 a[228:231], a[52:55], a[108:111], a[228:231]\n" \
"v_mfma_f32_16x16x32_bf16 a[232:235], a[48:51], a[112:115], a[232:235]\n" \
"ds_read_b128 a[44:47], v101 offset:10240\n" \
"v_mfma_f32_16x16x32_bf16 a[232:235], a[52:55], a[116:119], a[232:235]\n" \
"v_mfma_f32_16x16x32_bf16 a[236:239], a[48:51], a[120:123], a[236:239]\n" \
"ds_read_b64_tr_b16 a[94:95], v75 offset:20992\n" \
"v_mfma_f32_16x16x32_bf16 a[236:239], a[52:55], a[124:127], a[236:239]\n" \
"v_mfma_f32_16x16x32_bf16 a[252:255], a[56:59], a[120:123], a[252:255]\n" \
"ds_read_b128 a[48:51], v100 offset:12288\n" \
"v_mfma_f32_16x16x32_bf16 a[252:255], a[60:63], a[124:127], a[252:255]\n" \
"v_mfma_f32_16x16x32_bf16 a[248:251], a[56:59], a[112:115], a[248:251]\n" \
"v_mfma_f32_16x16x32_bf16 a[248:251], a[60:63], a[116:119], a[248:251]\n" \
"v_mfma_f32_16x16x32_bf16 a[244:247], a[56:59], a[104:107], a[244:247]\n" \
"ds_read_b128 a[52:55], v101 offset:12288\n" \
"v_mfma_f32_16x16x32_bf16 a[244:247], a[60:63], a[108:111], a[244:247]\n" \
"v_mfma_f32_16x16x32_bf16 a[240:243], a[56:59], a[96:99], a[240:243]\n" \
"v_mfma_f32_16x16x32_bf16 a[240:243], a[60:63], a[100:103], a[240:243]\n" \
"s_waitcnt lgkmcnt(1)\n" \
"v_mfma_f32_16x16x32_bf16 v[0:3], a[64:67], a[0:3], v[0:3]\n" \
"ds_read_b128 a[56:59], v100 offset:14336\n" \
"v_mfma_f32_16x16x32_bf16 v[0:3], a[68:71], a[4:7], v[0:3]\n" \
"v_mfma_f32_16x16x32_bf16 v[4:7], a[72:75], a[0:3], v[4:7]\n" \
"s_add_u32 m0, s74, 32768\n" \
"ds_read_b128 a[60:63], v101 offset:14336\n" \
"v_mfma_f32_16x16x32_bf16 v[4:7], a[76:79], a[4:7], v[4:7]\n" \
"buffer_load_dwordx4 v68, s[40:43], s83 offen offset:0 lds\n" \
"v_mfma_f32_16x16x32_bf16 v[8:11], a[80:83], a[0:3], v[8:11]\n" \
"ds_read_b64_tr_b16 a[96:97], v74 offset:1024\n" \
"v_mfma_f32_16x16x32_bf16 v[8:11], a[84:87], a[4:7], v[8:11]\n" \
"v_mfma_f32_16x16x32_bf16 v[12:15], a[88:91], a[0:3], v[12:15]\n" \
"ds_read_b64_tr_b16 a[98:99], v74 offset:5120\n" \
"v_mfma_f32_16x16x32_bf16 v[12:15], a[92:95], a[4:7], v[12:15]\n" \
"buffer_load_dwordx4 v69, s[44:47], s83 offen offset:1024 lds\n" \
"v_mfma_f32_16x16x32_bf16 v[28:31], a[88:91], a[8:11], v[28:31]\n" \
"ds_read_b64_tr_b16 a[100:101], v74 offset:17408\n" \
"v_mfma_f32_16x16x32_bf16 v[28:31], a[92:95], a[12:15], v[28:31]\n" \
"v_mfma_f32_16x16x32_bf16 v[24:27], a[80:83], a[8:11], v[24:27]\n" \
"ds_read_b64_tr_b16 a[102:103], v74 offset:21504\n" \
"v_mfma_f32_16x16x32_bf16 v[24:27], a[84:87], a[12:15], v[24:27]\n" \
"buffer_load_dwordx4 v70, s[40:43], s83 offen offset:2048 lds\n" \
"v_mfma_f32_16x16x32_bf16 v[20:23], a[72:75], a[8:11], v[20:23]\n" \
"ds_read_b64_tr_b16 a[104:105], v75 offset:1024\n" \
"v_mfma_f32_16x16x32_bf16 v[20:23], a[76:79], a[12:15], v[20:23]\n" \
"v_mfma_f32_16x16x32_bf16 v[16:19], a[64:67], a[8:11], v[16:19]\n" \
"ds_read_b64_tr_b16 a[106:107], v75 offset:5120\n" \
"v_mfma_f32_16x16x32_bf16 v[16:19], a[68:71], a[12:15], v[16:19]\n" \
"buffer_load_dwordx4 v71, s[44:47], s83 offen offset:3072 lds\n" \
"v_mfma_f32_16x16x32_bf16 v[32:35], a[64:67], a[16:19], v[32:35]\n" \
"ds_read_b64_tr_b16 a[108:109], v75 offset:17408\n" \
"v_mfma_f32_16x16x32_bf16 v[32:35], a[68:71], a[20:23], v[32:35]\n" \
"s_xor_b32 s83, s83, 0x180000\n" \
"v_mfma_f32_16x16x32_bf16 v[36:39], a[72:75], a[16:19], v[36:39]\n" \
"s_add_u32 m0, s74, 36864\n" \
"ds_read_b64_tr_b16 a[110:111], v75 offset:21504\n" \
"v_mfma_f32_16x16x32_bf16 v[36:39], a[76:79], a[20:23], v[36:39]\n" \
"buffer_load_dwordx4 v68, s[40:43], s84 offen offset:0 lds\n" \
"v_mfma_f32_16x16x32_bf16 v[40:43], a[80:83], a[16:19], v[40:43]\n" \
"ds_read_b64_tr_b16 a[112:113], v74 offset:1536\n" \
"v_mfma_f32_16x16x32_bf16 v[40:43], a[84:87], a[20:23], v[40:43]\n" \
"v_mfma_f32_16x16x32_bf16 v[44:47], a[88:91], a[16:19], v[44:47]\n" \
"ds_read_b64_tr_b16 a[114:115], v74 offset:5632\n" \
"v_mfma_f32_16x16x32_bf16 v[44:47], a[92:95], a[20:23], v[44:47]\n" \
"buffer_load_dwordx4 v69, s[44:47], s84 offen offset:1024 lds\n" \
"v_mfma_f32_16x16x32_bf16 v[60:63], a[88:91], a[24:27], v[60:63]\n" \
"ds_read_b64_tr_b16 a[116:117], v74 offset:17920\n" \
"v_mfma_f32_16x16x32_bf16 v[60:63], a[92:95], a[28:31], v[60:63]\n" \
"v_mfma_f32_16x16x32_bf16 v[56:59], a[80:83], a[24:27], v[56:59]\n" \
"ds_read_b64_tr_b16 a[118:119], v74 offset:22016\n" \
"v_mfma_f32_16x16x32_bf16 v[56:59], a[84:87], a[28:31], v[56:59]\n" \
"buffer_load_dwordx4 v70, s[40:43], s84 offen offset:2048 lds\n" \
"v_mfma_f32_16x16x32_bf16 v[52:55], a[72:75], a[24:27], v[52:55]\n" \
"ds_read_b64_tr_b16 a[120:121], v75 offset:1536\n" \
"v_mfma_f32_16x16x32_bf16 v[52:55], a[76:79], a[28:31], v[52:55]\n" \
"v_mfma_f32_16x16x32_bf16 v[48:51], a[64:67], a[24:27], v[48:51]\n" \
"ds_read_b64_tr_b16 a[122:123], v75 offset:5632\n" \
"v_mfma_f32_16x16x32_bf16 v[48:51], a[68:71], a[28:31], v[48:51]\n" \
"buffer_load_dwordx4 v71, s[44:47], s84 offen offset:3072 lds\n" \
"v_mfma_f32_16x16x32_bf16 v[144:147], a[64:67], a[32:35], v[144:147]\n" \
"ds_read_b64_tr_b16 a[124:125], v75 offset:17920\n" \
"v_mfma_f32_16x16x32_bf16 v[144:147], a[68:71], a[36:39], v[144:147]\n" \
"v_mfma_f32_16x16x32_bf16 v[148:151], a[72:75], a[32:35], v[148:151]\n" \
"s_xor_b32 s84, s84, 0x180000\n" \
"ds_read_b64_tr_b16 a[126:127], v75 offset:22016\n" \
"v_mfma_f32_16x16x32_bf16 v[148:151], a[76:79], a[36:39], v[148:151]\n" \
"v_mfma_f32_16x16x32_bf16 v[152:155], a[80:83], a[32:35], v[152:155]\n" \
"v_mfma_f32_16x16x32_bf16 v[152:155], a[84:87], a[36:39], v[152:155]\n" \
"v_mfma_f32_16x16x32_bf16 v[156:159], a[88:91], a[32:35], v[156:159]\n" \
"v_mfma_f32_16x16x32_bf16 v[156:159], a[92:95], a[36:39], v[156:159]\n" \
"v_mfma_f32_16x16x32_bf16 v[188:191], a[88:91], a[40:43], v[188:191]\n" \
"v_mfma_f32_16x16x32_bf16 v[188:191], a[92:95], a[44:47], v[188:191]\n" \
"v_mfma_f32_16x16x32_bf16 v[184:187], a[80:83], a[40:43], v[184:187]\n" \
"v_mfma_f32_16x16x32_bf16 v[184:187], a[84:87], a[44:47], v[184:187]\n" \
"v_mfma_f32_16x16x32_bf16 v[180:183], a[72:75], a[40:43], v[180:183]\n" \
"v_mfma_f32_16x16x32_bf16 v[180:183], a[76:79], a[44:47], v[180:183]\n" \
"v_mfma_f32_16x16x32_bf16 v[176:179], a[64:67], a[40:43], v[176:179]\n" \
"v_mfma_f32_16x16x32_bf16 v[176:179], a[68:71], a[44:47], v[176:179]\n" \
"s_waitcnt lgkmcnt(0)\n" \
"v_mfma_f32_16x16x32_bf16 v[192:195], a[64:67], a[48:51], v[192:195]\n" \
"v_mfma_f32_16x16x32_bf16 v[192:195], a[68:71], a[52:55], v[192:195]\n" \
"v_mfma_f32_16x16x32_bf16 v[196:199], a[72:75], a[48:51], v[196:199]\n" \
"v_mfma_f32_16x16x32_bf16 v[196:199], a[76:79], a[52:55], v[196:199]\n" \
"v_mfma_f32_16x16x32_bf16 v[200:203], a[80:83], a[48:51], v[200:203]\n" \
"v_mfma_f32_16x16x32_bf16 v[200:203], a[84:87], a[52:55], v[200:203]\n" \
"v_mfma_f32_16x16x32_bf16 v[204:207], a[88:91], a[48:51], v[204:207]\n" \
"v_mfma_f32_16x16x32_bf16 v[204:207], a[92:95], a[52:55], v[204:207]\n" \
"v_mfma_f32_16x16x32_bf16 v[220:223], a[88:91], a[56:59], v[220:223]\n" \
"v_mfma_f32_16x16x32_bf16 v[220:223], a[92:95], a[60:63], v[220:223]\n" \
"v_mfma_f32_16x16x32_bf16 v[216:219], a[80:83], a[56:59], v[216:219]\n" \
"v_mfma_f32_16x16x32_bf16 v[216:219], a[84:87], a[60:63], v[216:219]\n" \
"v_mfma_f32_16x16x32_bf16 v[212:215], a[72:75], a[56:59], v[212:215]\n" \
"v_mfma_f32_16x16x32_bf16 v[212:215], a[76:79], a[60:63], v[212:215]\n" \
"v_mfma_f32_16x16x32_bf16 v[208:211], a[64:67], a[56:59], v[208:211]\n" \
"v_mfma_f32_16x16x32_bf16 v[208:211], a[68:71], a[60:63], v[208:211]\n" \
"s_waitcnt vmcnt(11) lgkmcnt(0)\n" \
"s_barrier\n" \
"v_mfma_f32_16x16x32_bf16 a[128:131], a[0:3], a[96:99], a[128:131]\n" \
"s_add_u32 m0, s74, 0\n" \
"ds_read_b64_tr_b16 a[64:65], v98 offset:0\n" \
"v_mfma_f32_16x16x32_bf16 a[128:131], a[4:7], a[100:103], a[128:131]\n" \
"buffer_load_dwordx4 v64, s[36:39], s81 offen offset:0 lds\n" \
"v_mfma_f32_16x16x32_bf16 a[132:135], a[0:3], a[104:107], a[132:135]\n" \
"ds_read_b64_tr_b16 a[66:67], v98 offset:4096\n" \
"v_mfma_f32_16x16x32_bf16 a[132:135], a[4:7], a[108:111], a[132:135]\n" \
"v_mfma_f32_16x16x32_bf16 a[136:139], a[0:3], a[112:115], a[136:139]\n" \
"ds_read_b64_tr_b16 a[68:69], v98 offset:16384\n" \
"v_mfma_f32_16x16x32_bf16 a[136:139], a[4:7], a[116:119], a[136:139]\n" \
"buffer_load_dwordx4 v65, s[36:39], s81 offen offset:1024 lds\n" \
"v_mfma_f32_16x16x32_bf16 a[140:143], a[0:3], a[120:123], a[140:143]\n" \
"ds_read_b64_tr_b16 a[70:71], v98 offset:20480\n" \
"v_mfma_f32_16x16x32_bf16 a[140:143], a[4:7], a[124:127], a[140:143]\n" \
"v_mfma_f32_16x16x32_bf16 a[156:159], a[8:11], a[120:123], a[156:159]\n" \
"ds_read_b128 a[0:3], v96 offset:32768\n" \
"v_mfma_f32_16x16x32_bf16 a[156:159], a[12:15], a[124:127], a[156:159]\n" \
"buffer_load_dwordx4 v66, s[36:39], s81 offen offset:2048 lds\n" \
"v_mfma_f32_16x16x32_bf16 a[152:155], a[8:11], a[112:115], a[152:155]\n" \
"ds_read_b64_tr_b16 a[72:73], v99 offset:0\n" \
"v_mfma_f32_16x16x32_bf16 a[152:155], a[12:15], a[116:119], a[152:155]\n" \
"Lpg1139_%=:\n" \
"Lst0x1_%=:\n" \
"s_cbranch_execz Lpg3348_%=\n" \
"v_mfma_f32_16x16x32_bf16 a[148:151], a[8:11], a[104:107], a[148:151]\n" \
"ds_read_b128 a[4:7], v97 offset:32768\n" \
"v_mfma_f32_16x16x32_bf16 a[148:151], a[12:15], a[108:111], a[148:151]\n" \
"buffer_load_dwordx4 v67, s[36:39], s81 offen offset:3072 lds\n" \
"v_mfma_f32_16x16x32_bf16 a[144:147], a[8:11], a[96:99], a[144:147]\n" \
"ds_read_b64_tr_b16 a[74:75], v99 offset:4096\n" \
"v_mfma_f32_16x16x32_bf16 a[144:147], a[12:15], a[100:103], a[144:147]\n" \
"s_xor_b32 s81, s81, 0x180\n" \
"v_mfma_f32_16x16x32_bf16 a[160:163], a[16:19], a[96:99], a[160:163]\n" \
"s_add_u32 m0, s74, 4096\n" \
"ds_read_b128 a[8:11], v96 offset:34816\n" \
"v_mfma_f32_16x16x32_bf16 a[160:163], a[20:23], a[100:103], a[160:163]\n" \
"buffer_load_dwordx4 v64, s[36:39], s82 offen offset:0 lds\n" \
"v_mfma_f32_16x16x32_bf16 a[164:167], a[16:19], a[104:107], a[164:167]\n" \
"ds_read_b64_tr_b16 a[76:77], v99 offset:16384\n" \
"v_mfma_f32_16x16x32_bf16 a[164:167], a[20:23], a[108:111], a[164:167]\n" \
"v_mfma_f32_16x16x32_bf16 a[168:171], a[16:19], a[112:115], a[168:171]\n" \
"ds_read_b128 a[12:15], v97 offset:34816\n" \
"v_mfma_f32_16x16x32_bf16 a[168:171], a[20:23], a[116:119], a[168:171]\n" \
"buffer_load_dwordx4 v65, s[36:39], s82 offen offset:1024 lds\n" \
"v_mfma_f32_16x16x32_bf16 a[172:175], a[16:19], a[120:123], a[172:175]\n" \
"ds_read_b64_tr_b16 a[78:79], v99 offset:20480\n" \
"v_mfma_f32_16x16x32_bf16 a[172:175], a[20:23], a[124:127], a[172:175]\n" \
"v_mfma_f32_16x16x32_bf16 a[188:191], a[24:27], a[120:123], a[188:191]\n" \
"ds_read_b128 a[16:19], v96 offset:36864\n" \
"v_mfma_f32_16x16x32_bf16 a[188:191], a[28:31], a[124:127], a[188:191]\n" \
"buffer_load_dwordx4 v66, s[36:39], s82 offen offset:2048 lds\n" \
"v_mfma_f32_16x16x32_bf16 a[184:187], a[24:27], a[112:115], a[184:187]\n" \
"ds_read_b64_tr_b16 a[80:81], v98 offset:512\n" \
"v_mfma_f32_16x16x32_bf16 a[184:187], a[28:31], a[116:119], a[184:187]\n" \
"v_mfma_f32_16x16x32_bf16 a[180:183], a[24:27], a[104:107], a[180:183]\n" \
"ds_read_b128 a[20:23], v97 offset:36864\n" \
"v_mfma_f32_16x16x32_bf16 a[180:183], a[28:31], a[108:111], a[180:183]\n" \
"buffer_load_dwordx4 v67, s[36:39], s82 offen offset:3072 lds\n" \
"v_mfma_f32_16x16x32_bf16 a[176:179], a[24:27], a[96:99], a[176:179]\n" \
"s_mov_b64 s[64:65], s[8:9]\n" \
"s_add_u32 s68, s66, 0x200000\n" \
"ds_read_b64_tr_b16 a[82:83], v98 offset:4608\n" \
"v_mfma_f32_16x16x32_bf16 a[176:179], a[28:31], a[100:103], a[176:179]\n" \
"s_add_u32 s64, s64, s68\n" \
"s_addc_u32 s65, s65, 0\n" \
"v_mfma_f32_16x16x32_bf16 a[192:195], a[32:35], a[96:99], a[192:195]\n" \
"s_xor_b32 s82, s82, 0x180\n" \
"ds_read_b128 a[24:27], v96 offset:38912\n" \
"v_mfma_f32_16x16x32_bf16 a[192:195], a[36:39], a[100:103], a[192:195]\n" \
"global_load_dword v238, v234, s[64:65]\n" \
"v_mfma_f32_16x16x32_bf16 a[196:199], a[32:35], a[104:107], a[196:199]\n" \
"s_mov_b64 s[64:65], s[8:9]\n" \
"s_add_u32 s68, s66, 0x300000\n" \
"ds_read_b64_tr_b16 a[84:85], v98 offset:16896\n" \
"v_mfma_f32_16x16x32_bf16 a[196:199], a[36:39], a[108:111], a[196:199]\n" \
"s_add_u32 s64, s64, s68\n" \
"s_addc_u32 s65, s65, 0\n" \
"v_mfma_f32_16x16x32_bf16 a[200:203], a[32:35], a[112:115], a[200:203]\n" \
"ds_read_b128 a[28:31], v97 offset:38912\n" \
"v_mfma_f32_16x16x32_bf16 a[200:203], a[36:39], a[116:119], a[200:203]\n" \
"global_load_dword v238, v234, s[64:65]\n" \
"v_mfma_f32_16x16x32_bf16 a[204:207], a[32:35], a[120:123], a[204:207]\n" \
"s_mov_b64 s[64:65], s[12:13]\n" \
"s_add_u32 s68, s67, 0x0\n" \
"ds_read_b64_tr_b16 a[86:87], v98 offset:20992\n" \
"v_mfma_f32_16x16x32_bf16 a[204:207], a[36:39], a[124:127], a[204:207]\n" \
"s_add_u32 s64, s64, s68\n" \
"s_addc_u32 s65, s65, 0\n" \
"v_mfma_f32_16x16x32_bf16 a[220:223], a[40:43], a[120:123], a[220:223]\n" \
"ds_read_b128 a[32:35], v96 offset:40960\n" \
"v_mfma_f32_16x16x32_bf16 a[220:223], a[44:47], a[124:127], a[220:223]\n" \
"global_load_dword v238, v234, s[64:65]\n" \
"v_mfma_f32_16x16x32_bf16 a[216:219], a[40:43], a[112:115], a[216:219]\n" \
"ds_read_b64_tr_b16 a[88:89], v99 offset:512\n" \
"v_mfma_f32_16x16x32_bf16 a[216:219], a[44:47], a[116:119], a[216:219]\n" \
"v_mfma_f32_16x16x32_bf16 a[212:215], a[40:43], a[104:107], a[212:215]\n" \
"ds_read_b128 a[36:39], v97 offset:40960\n" \
"v_mfma_f32_16x16x32_bf16 a[212:215], a[44:47], a[108:111], a[212:215]\n" \
"v_mfma_f32_16x16x32_bf16 a[208:211], a[40:43], a[96:99], a[208:211]\n" \
"ds_read_b64_tr_b16 a[90:91], v99 offset:4608\n" \
"v_mfma_f32_16x16x32_bf16 a[208:211], a[44:47], a[100:103], a[208:211]\n" \
"v_mfma_f32_16x16x32_bf16 a[224:227], a[48:51], a[96:99], a[224:227]\n" \
"ds_read_b128 a[40:43], v96 offset:43008\n" \
"v_mfma_f32_16x16x32_bf16 a[224:227], a[52:55], a[100:103], a[224:227]\n" \
"v_mfma_f32_16x16x32_bf16 a[228:231], a[48:51], a[104:107], a[228:231]\n" \
"ds_read_b64_tr_b16 a[92:93], v99 offset:16896\n" \
"v_mfma_f32_16x16x32_bf16 a[228:231], a[52:55], a[108:111], a[228:231]\n" \
"v_mfma_f32_16x16x32_bf16 a[232:235], a[48:51], a[112:115], a[232:235]\n" \
"ds_read_b128 a[44:47], v97 offset:43008\n" \
"v_mfma_f32_16x16x32_bf16 a[232:235], a[52:55], a[116:119], a[232:235]\n" \
"v_mfma_f32_16x16x32_bf16 a[236:239], a[48:51], a[120:123], a[236:239]\n" \
"ds_read_b64_tr_b16 a[94:95], v99 offset:20992\n" \
"v_mfma_f32_16x16x32_bf16 a[236:239], a[52:55], a[124:127], a[236:239]\n" \
"v_mfma_f32_16x16x32_bf16 a[252:255], a[56:59], a[120:123], a[252:255]\n" \
"ds_read_b128 a[48:51], v96 offset:45056\n" \
"v_mfma_f32_16x16x32_bf16 a[252:255], a[60:63], a[124:127], a[252:255]\n" \
"v_mfma_f32_16x16x32_bf16 a[248:251], a[56:59], a[112:115], a[248:251]\n" \
"v_mfma_f32_16x16x32_bf16 a[248:251], a[60:63], a[116:119], a[248:251]\n" \
"v_mfma_f32_16x16x32_bf16 a[244:247], a[56:59], a[104:107], a[244:247]\n" \
"ds_read_b128 a[52:55], v97 offset:45056\n" \
"v_mfma_f32_16x16x32_bf16 a[244:247], a[60:63], a[108:111], a[244:247]\n" \
"v_mfma_f32_16x16x32_bf16 a[240:243], a[56:59], a[96:99], a[240:243]\n" \
"v_mfma_f32_16x16x32_bf16 a[240:243], a[60:63], a[100:103], a[240:243]\n" \
"s_waitcnt lgkmcnt(1)\n" \
"v_mfma_f32_16x16x32_bf16 v[0:3], a[64:67], a[0:3], v[0:3]\n" \
"ds_read_b128 a[56:59], v96 offset:47104\n" \
"v_mfma_f32_16x16x32_bf16 v[0:3], a[68:71], a[4:7], v[0:3]\n" \
"v_mfma_f32_16x16x32_bf16 v[4:7], a[72:75], a[0:3], v[4:7]\n" \
"s_add_u32 m0, s74, 131072\n" \
"ds_read_b128 a[60:63], v97 offset:47104\n" \
"v_mfma_f32_16x16x32_bf16 v[4:7], a[76:79], a[4:7], v[4:7]\n" \
"buffer_load_dwordx4 v68, s[40:43], s83 offen offset:0 lds\n" \
"v_mfma_f32_16x16x32_bf16 v[8:11], a[80:83], a[0:3], v[8:11]\n" \
"ds_read_b64_tr_b16 a[96:97], v98 offset:1024\n" \
"v_mfma_f32_16x16x32_bf16 v[8:11], a[84:87], a[4:7], v[8:11]\n" \
"v_mfma_f32_16x16x32_bf16 v[12:15], a[88:91], a[0:3], v[12:15]\n" \
"ds_read_b64_tr_b16 a[98:99], v98 offset:5120\n" \
"v_mfma_f32_16x16x32_bf16 v[12:15], a[92:95], a[4:7], v[12:15]\n" \
"buffer_load_dwordx4 v69, s[44:47], s83 offen offset:1024 lds\n" \
"v_mfma_f32_16x16x32_bf16 v[28:31], a[88:91], a[8:11], v[28:31]\n" \
"ds_read_b64_tr_b16 a[100:101], v98 offset:17408\n" \
"v_mfma_f32_16x16x32_bf16 v[28:31], a[92:95], a[12:15], v[28:31]\n" \
"v_mfma_f32_16x16x32_bf16 v[24:27], a[80:83], a[8:11], v[24:27]\n" \
"ds_read_b64_tr_b16 a[102:103], v98 offset:21504\n" \
"v_mfma_f32_16x16x32_bf16 v[24:27], a[84:87], a[12:15], v[24:27]\n" \
"buffer_load_dwordx4 v70, s[40:43], s83 offen offset:2048 lds\n" \
"v_mfma_f32_16x16x32_bf16 v[20:23], a[72:75], a[8:11], v[20:23]\n" \
"ds_read_b64_tr_b16 a[104:105], v99 offset:1024\n" \
"v_mfma_f32_16x16x32_bf16 v[20:23], a[76:79], a[12:15], v[20:23]\n" \
"v_mfma_f32_16x16x32_bf16 v[16:19], a[64:67], a[8:11], v[16:19]\n" \
"ds_read_b64_tr_b16 a[106:107], v99 offset:5120\n" \
"v_mfma_f32_16x16x32_bf16 v[16:19], a[68:71], a[12:15], v[16:19]\n" \
"buffer_load_dwordx4 v71, s[44:47], s83 offen offset:3072 lds\n" \
"v_mfma_f32_16x16x32_bf16 v[32:35], a[64:67], a[16:19], v[32:35]\n" \
"ds_read_b64_tr_b16 a[108:109], v99 offset:17408\n" \
"v_mfma_f32_16x16x32_bf16 v[32:35], a[68:71], a[20:23], v[32:35]\n" \
"s_xor_b32 s83, s83, 0x80000\n" \
"v_mfma_f32_16x16x32_bf16 v[36:39], a[72:75], a[16:19], v[36:39]\n" \
"s_add_u32 m0, s74, 135168\n" \
"ds_read_b64_tr_b16 a[110:111], v99 offset:21504\n" \
"v_mfma_f32_16x16x32_bf16 v[36:39], a[76:79], a[20:23], v[36:39]\n" \
"buffer_load_dwordx4 v68, s[40:43], s84 offen offset:0 lds\n" \
"v_mfma_f32_16x16x32_bf16 v[40:43], a[80:83], a[16:19], v[40:43]\n" \
"ds_read_b64_tr_b16 a[112:113], v98 offset:1536\n" \
"v_mfma_f32_16x16x32_bf16 v[40:43], a[84:87], a[20:23], v[40:43]\n" \
"v_mfma_f32_16x16x32_bf16 v[44:47], a[88:91], a[16:19], v[44:47]\n" \
"ds_read_b64_tr_b16 a[114:115], v98 offset:5632\n" \
"v_mfma_f32_16x16x32_bf16 v[44:47], a[92:95], a[20:23], v[44:47]\n" \
"buffer_load_dwordx4 v69, s[44:47], s84 offen offset:1024 lds\n" \
"v_mfma_f32_16x16x32_bf16 v[60:63], a[88:91], a[24:27], v[60:63]\n" \
"ds_read_b64_tr_b16 a[116:117], v98 offset:17920\n" \
"v_mfma_f32_16x16x32_bf16 v[60:63], a[92:95], a[28:31], v[60:63]\n" \
"v_mfma_f32_16x16x32_bf16 v[56:59], a[80:83], a[24:27], v[56:59]\n" \
"ds_read_b64_tr_b16 a[118:119], v98 offset:22016\n" \
"v_mfma_f32_16x16x32_bf16 v[56:59], a[84:87], a[28:31], v[56:59]\n" \
"buffer_load_dwordx4 v70, s[40:43], s84 offen offset:2048 lds\n" \
"v_mfma_f32_16x16x32_bf16 v[52:55], a[72:75], a[24:27], v[52:55]\n" \
"ds_read_b64_tr_b16 a[120:121], v99 offset:1536\n" \
"v_mfma_f32_16x16x32_bf16 v[52:55], a[76:79], a[28:31], v[52:55]\n" \
"v_mfma_f32_16x16x32_bf16 v[48:51], a[64:67], a[24:27], v[48:51]\n" \
"ds_read_b64_tr_b16 a[122:123], v99 offset:5632\n" \
"v_mfma_f32_16x16x32_bf16 v[48:51], a[68:71], a[28:31], v[48:51]\n" \
"buffer_load_dwordx4 v71, s[44:47], s84 offen offset:3072 lds\n" \
"v_mfma_f32_16x16x32_bf16 v[144:147], a[64:67], a[32:35], v[144:147]\n" \
"ds_read_b64_tr_b16 a[124:125], v99 offset:17920\n" \
"v_mfma_f32_16x16x32_bf16 v[144:147], a[68:71], a[36:39], v[144:147]\n" \
"v_mfma_f32_16x16x32_bf16 v[148:151], a[72:75], a[32:35], v[148:151]\n" \
"s_xor_b32 s84, s84, 0x80000\n" \
"ds_read_b64_tr_b16 a[126:127], v99 offset:22016\n" \
"v_mfma_f32_16x16x32_bf16 v[148:151], a[76:79], a[36:39], v[148:151]\n" \
"v_mfma_f32_16x16x32_bf16 v[152:155], a[80:83], a[32:35], v[152:155]\n" \
"v_mfma_f32_16x16x32_bf16 v[152:155], a[84:87], a[36:39], v[152:155]\n" \
"v_mfma_f32_16x16x32_bf16 v[156:159], a[88:91], a[32:35], v[156:159]\n" \
"v_mfma_f32_16x16x32_bf16 v[156:159], a[92:95], a[36:39], v[156:159]\n" \
"v_mfma_f32_16x16x32_bf16 v[188:191], a[88:91], a[40:43], v[188:191]\n" \
"v_mfma_f32_16x16x32_bf16 v[188:191], a[92:95], a[44:47], v[188:191]\n" \
"v_mfma_f32_16x16x32_bf16 v[184:187], a[80:83], a[40:43], v[184:187]\n" \
"v_mfma_f32_16x16x32_bf16 v[184:187], a[84:87], a[44:47], v[184:187]\n" \
"v_mfma_f32_16x16x32_bf16 v[180:183], a[72:75], a[40:43], v[180:183]\n" \
"v_mfma_f32_16x16x32_bf16 v[180:183], a[76:79], a[44:47], v[180:183]\n" \
"v_mfma_f32_16x16x32_bf16 v[176:179], a[64:67], a[40:43], v[176:179]\n" \
"v_mfma_f32_16x16x32_bf16 v[176:179], a[68:71], a[44:47], v[176:179]\n" \
"s_waitcnt lgkmcnt(0)\n" \
"v_mfma_f32_16x16x32_bf16 v[192:195], a[64:67], a[48:51], v[192:195]\n" \
"v_mfma_f32_16x16x32_bf16 v[192:195], a[68:71], a[52:55], v[192:195]\n" \
"v_mfma_f32_16x16x32_bf16 v[196:199], a[72:75], a[48:51], v[196:199]\n" \
"v_mfma_f32_16x16x32_bf16 v[196:199], a[76:79], a[52:55], v[196:199]\n" \
"v_mfma_f32_16x16x32_bf16 v[200:203], a[80:83], a[48:51], v[200:203]\n" \
"v_mfma_f32_16x16x32_bf16 v[200:203], a[84:87], a[52:55], v[200:203]\n" \
"v_mfma_f32_16x16x32_bf16 v[204:207], a[88:91], a[48:51], v[204:207]\n" \
"v_mfma_f32_16x16x32_bf16 v[204:207], a[92:95], a[52:55], v[204:207]\n" \
"v_mfma_f32_16x16x32_bf16 v[220:223], a[88:91], a[56:59], v[220:223]\n" \
"v_mfma_f32_16x16x32_bf16 v[220:223], a[92:95], a[60:63], v[220:223]\n" \
"v_mfma_f32_16x16x32_bf16 v[216:219], a[80:83], a[56:59], v[216:219]\n" \
"v_mfma_f32_16x16x32_bf16 v[216:219], a[84:87], a[60:63], v[216:219]\n" \
"v_mfma_f32_16x16x32_bf16 v[212:215], a[72:75], a[56:59], v[212:215]\n" \
"v_mfma_f32_16x16x32_bf16 v[212:215], a[76:79], a[60:63], v[212:215]\n" \
"v_mfma_f32_16x16x32_bf16 v[208:211], a[64:67], a[56:59], v[208:211]\n" \
"v_mfma_f32_16x16x32_bf16 v[208:211], a[68:71], a[60:63], v[208:211]\n" \
"s_waitcnt vmcnt(11) lgkmcnt(0)\n" \
"s_barrier\n" \
"v_mfma_f32_16x16x32_bf16 a[128:131], a[0:3], a[96:99], a[128:131]\n" \
"s_add_u32 m0, s74, 65536\n" \
"ds_read_b64_tr_b16 a[64:65], v74 offset:32768\n" \
"v_mfma_f32_16x16x32_bf16 a[128:131], a[4:7], a[100:103], a[128:131]\n" \
"buffer_load_dwordx4 v64, s[36:39], s81 offen offset:0 lds\n" \
"v_mfma_f32_16x16x32_bf16 a[132:135], a[0:3], a[104:107], a[132:135]\n" \
"ds_read_b64_tr_b16 a[66:67], v74 offset:36864\n" \
"v_mfma_f32_16x16x32_bf16 a[132:135], a[4:7], a[108:111], a[132:135]\n" \
"v_mfma_f32_16x16x32_bf16 a[136:139], a[0:3], a[112:115], a[136:139]\n" \
"ds_read_b64_tr_b16 a[68:69], v74 offset:49152\n" \
"v_mfma_f32_16x16x32_bf16 a[136:139], a[4:7], a[116:119], a[136:139]\n" \
"buffer_load_dwordx4 v65, s[36:39], s81 offen offset:1024 lds\n" \
"v_mfma_f32_16x16x32_bf16 a[140:143], a[0:3], a[120:123], a[140:143]\n" \
"ds_read_b64_tr_b16 a[70:71], v74 offset:53248\n" \
"v_mfma_f32_16x16x32_bf16 a[140:143], a[4:7], a[124:127], a[140:143]\n" \
"v_mfma_f32_16x16x32_bf16 a[156:159], a[8:11], a[120:123], a[156:159]\n" \
"ds_read_b128 a[0:3], v72 offset:0\n" \
"v_mfma_f32_16x16x32_bf16 a[156:159], a[12:15], a[124:127], a[156:159]\n" \
"buffer_load_dwordx4 v66, s[36:39], s81 offen offset:2048 lds\n" \
"v_mfma_f32_16x16x32_bf16 a[152:155], a[8:11], a[112:115], a[152:155]\n" \
"ds_read_b64_tr_b16 a[72:73], v75 offset:32768\n" \
"v_mfma_f32_16x16x32_bf16 a[152:155], a[12:15], a[116:119], a[152:155]\n" \
"v_mfma_f32_16x16x32_bf16 a[148:151], a[8:11], a[104:107], a[148:151]\n" \
"ds_read_b128 a[4:7], v73 offset:0\n" \
"v_mfma_f32_16x16x32_bf16 a[148:151], a[12:15], a[108:111], a[148:151]\n" \
"buffer_load_dwordx4 v67, s[36:39], s81 offen offset:3072 lds\n" \
"v_mfma_f32_16x16x32_bf16 a[144:147], a[8:11], a[96:99], a[144:147]\n" \
"ds_read_b64_tr_b16 a[74:75], v75 offset:36864\n" \
"v_mfma_f32_16x16x32_bf16 a[144:147], a[12:15], a[100:103], a[144:147]\n" \
"s_xor_b32 s81, s81, 0x80\n" \
"v_mfma_f32_16x16x32_bf16 a[160:163], a[16:19], a[96:99], a[160:163]\n" \
"s_add_u32 m0, s74, 69632\n" \
"ds_read_b128 a[8:11], v72 offset:2048\n" \
"v_mfma_f32_16x16x32_bf16 a[160:163], a[20:23], a[100:103], a[160:163]\n" \
"buffer_load_dwordx4 v64, s[36:39], s82 offen offset:0 lds\n" \
"v_mfma_f32_16x16x32_bf16 a[164:167], a[16:19], a[104:107], a[164:167]\n" \
"ds_read_b64_tr_b16 a[76:77], v75 offset:49152\n" \
"v_mfma_f32_16x16x32_bf16 a[164:167], a[20:23], a[108:111], a[164:167]\n" \
"v_mfma_f32_16x16x32_bf16 a[168:171], a[16:19], a[112:115], a[168:171]\n" \
"ds_read_b128 a[12:15], v73 offset:2048\n" \
"v_mfma_f32_16x16x32_bf16 a[168:171], a[20:23], a[116:119], a[168:171]\n" \
"buffer_load_dwordx4 v65, s[36:39], s82 offen offset:1024 lds\n" \
"v_mfma_f32_16x16x32_bf16 a[172:175], a[16:19], a[120:123], a[172:175]\n" \
"ds_read_b64_tr_b16 a[78:79], v75 offset:53248\n" \
"v_mfma_f32_16x16x32_bf16 a[172:175], a[20:23], a[124:127], a[172:175]\n" \
"v_mfma_f32_16x16x32_bf16 a[188:191], a[24:27], a[120:123], a[188:191]\n" \
"ds_read_b128 a[16:19], v72 offset:4096\n" \
"v_mfma_f32_16x16x32_bf16 a[188:191], a[28:31], a[124:127], a[188:191]\n" \
"buffer_load_dwordx4 v66, s[36:39], s82 offen offset:2048 lds\n" \
"v_mfma_f32_16x16x32_bf16 a[184:187], a[24:27], a[112:115], a[184:187]\n" \
"ds_read_b64_tr_b16 a[80:81], v74 offset:33280\n" \
"v_mfma_f32_16x16x32_bf16 a[184:187], a[28:31], a[116:119], a[184:187]\n" \
"v_mfma_f32_16x16x32_bf16 a[180:183], a[24:27], a[104:107], a[180:183]\n" \
"ds_read_b128 a[20:23], v73 offset:4096\n" \
"v_mfma_f32_16x16x32_bf16 a[180:183], a[28:31], a[108:111], a[180:183]\n" \
"buffer_load_dwordx4 v67, s[36:39], s82 offen offset:3072 lds\n" \
"v_mfma_f32_16x16x32_bf16 a[176:179], a[24:27], a[96:99], a[176:179]\n" \
"s_mov_b64 s[64:65], s[12:13]\n" \
"s_add_u32 s68, s67, 0x100000\n" \
"ds_read_b64_tr_b16 a[82:83], v74 offset:37376\n" \
"v_mfma_f32_16x16x32_bf16 a[176:179], a[28:31], a[100:103], a[176:179]\n" \
"s_add_u32 s64, s64, s68\n" \
"s_addc_u32 s65, s65, 0\n" \
"v_mfma_f32_16x16x32_bf16 a[192:195], a[32:35], a[96:99], a[192:195]\n" \
"s_xor_b32 s82, s82, 0x80\n" \
"ds_read_b128 a[24:27], v72 offset:6144\n" \
"v_mfma_f32_16x16x32_bf16 a[192:195], a[36:39], a[100:103], a[192:195]\n" \
"global_load_dword v238, v234, s[64:65]\n" \
"v_mfma_f32_16x16x32_bf16 a[196:199], a[32:35], a[104:107], a[196:199]\n" \
"s_mov_b64 s[64:65], s[12:13]\n" \
"s_add_u32 s68, s67, 0x500000\n" \
"ds_read_b64_tr_b16 a[84:85], v74 offset:49664\n" \
"v_mfma_f32_16x16x32_bf16 a[196:199], a[36:39], a[108:111], a[196:199]\n" \
"s_add_u32 s64, s64, s68\n" \
"s_addc_u32 s65, s65, 0\n" \
"v_mfma_f32_16x16x32_bf16 a[200:203], a[32:35], a[112:115], a[200:203]\n" \
"ds_read_b128 a[28:31], v73 offset:6144\n" \
"v_mfma_f32_16x16x32_bf16 a[200:203], a[36:39], a[116:119], a[200:203]\n" \
"global_load_dword v238, v234, s[64:65]\n" \
"v_mfma_f32_16x16x32_bf16 a[204:207], a[32:35], a[120:123], a[204:207]\n" \
"s_mov_b64 s[64:65], s[12:13]\n" \
"s_add_u32 s68, s67, 0x200000\n" \
"ds_read_b64_tr_b16 a[86:87], v74 offset:53760\n" \
"v_mfma_f32_16x16x32_bf16 a[204:207], a[36:39], a[124:127], a[204:207]\n" \
"s_add_u32 s64, s64, s68\n" \
"s_addc_u32 s65, s65, 0\n" \
"v_mfma_f32_16x16x32_bf16 a[220:223], a[40:43], a[120:123], a[220:223]\n" \
"ds_read_b128 a[32:35], v72 offset:8192\n" \
"v_mfma_f32_16x16x32_bf16 a[220:223], a[44:47], a[124:127], a[220:223]\n" \
"global_load_dword v238, v234, s[64:65]\n" \
"v_mfma_f32_16x16x32_bf16 a[216:219], a[40:43], a[112:115], a[216:219]\n" \
"ds_read_b64_tr_b16 a[88:89], v75 offset:33280\n" \
"v_mfma_f32_16x16x32_bf16 a[216:219], a[44:47], a[116:119], a[216:219]\n" \
"v_mfma_f32_16x16x32_bf16 a[212:215], a[40:43], a[104:107], a[212:215]\n" \
"ds_read_b128 a[36:39], v73 offset:8192\n" \
"v_mfma_f32_16x16x32_bf16 a[212:215], a[44:47], a[108:111], a[212:215]\n" \
"v_mfma_f32_16x16x32_bf16 a[208:211], a[40:43], a[96:99], a[208:211]\n" \
"ds_read_b64_tr_b16 a[90:91], v75 offset:37376\n" \
"v_mfma_f32_16x16x32_bf16 a[208:211], a[44:47], a[100:103], a[208:211]\n" \
"v_mfma_f32_16x16x32_bf16 a[224:227], a[48:51], a[96:99], a[224:227]\n" \
"ds_read_b128 a[40:43], v72 offset:10240\n" \
"v_mfma_f32_16x16x32_bf16 a[224:227], a[52:55], a[100:103], a[224:227]\n" \
"v_mfma_f32_16x16x32_bf16 a[228:231], a[48:51], a[104:107], a[228:231]\n" \
"ds_read_b64_tr_b16 a[92:93], v75 offset:49664\n" \
"v_mfma_f32_16x16x32_bf16 a[228:231], a[52:55], a[108:111], a[228:231]\n" \
"v_mfma_f32_16x16x32_bf16 a[232:235], a[48:51], a[112:115], a[232:235]\n" \
"ds_read_b128 a[44:47], v73 offset:10240\n" \
"v_mfma_f32_16x16x32_bf16 a[232:235], a[52:55], a[116:119], a[232:235]\n" \
"v_mfma_f32_16x16x32_bf16 a[236:239], a[48:51], a[120:123], a[236:239]\n" \
"ds_read_b64_tr_b16 a[94:95], v75 offset:53760\n" \
"v_mfma_f32_16x16x32_bf16 a[236:239], a[52:55], a[124:127], a[236:239]\n" \
"v_mfma_f32_16x16x32_bf16 a[252:255], a[56:59], a[120:123], a[252:255]\n" \
"ds_read_b128 a[48:51], v72 offset:12288\n" \
"v_mfma_f32_16x16x32_bf16 a[252:255], a[60:63], a[124:127], a[252:255]\n" \
"v_mfma_f32_16x16x32_bf16 a[248:251], a[56:59], a[112:115], a[248:251]\n" \
"v_mfma_f32_16x16x32_bf16 a[248:251], a[60:63], a[116:119], a[248:251]\n" \
"v_mfma_f32_16x16x32_bf16 a[244:247], a[56:59], a[104:107], a[244:247]\n" \
"ds_read_b128 a[52:55], v73 offset:12288\n" \
"v_mfma_f32_16x16x32_bf16 a[244:247], a[60:63], a[108:111], a[244:247]\n" \
"v_mfma_f32_16x16x32_bf16 a[240:243], a[56:59], a[96:99], a[240:243]\n" \
"v_mfma_f32_16x16x32_bf16 a[240:243], a[60:63], a[100:103], a[240:243]\n" \
"s_waitcnt lgkmcnt(1)\n" \
"v_mfma_f32_16x16x32_bf16 v[0:3], a[64:67], a[0:3], v[0:3]\n" \
"ds_read_b128 a[56:59], v72 offset:14336\n" \
"v_mfma_f32_16x16x32_bf16 v[0:3], a[68:71], a[4:7], v[0:3]\n" \
"v_mfma_f32_16x16x32_bf16 v[4:7], a[72:75], a[0:3], v[4:7]\n" \
"s_add_u32 m0, s74, 98304\n" \
"ds_read_b128 a[60:63], v73 offset:14336\n" \
"v_mfma_f32_16x16x32_bf16 v[4:7], a[76:79], a[4:7], v[4:7]\n" \
"buffer_load_dwordx4 v68, s[40:43], s83 offen offset:0 lds\n" \
"v_mfma_f32_16x16x32_bf16 v[8:11], a[80:83], a[0:3], v[8:11]\n" \
"ds_read_b64_tr_b16 a[96:97], v74 offset:33792\n" \
"v_mfma_f32_16x16x32_bf16 v[8:11], a[84:87], a[4:7], v[8:11]\n" \
"v_mfma_f32_16x16x32_bf16 v[12:15], a[88:91], a[0:3], v[12:15]\n" \
"ds_read_b64_tr_b16 a[98:99], v74 offset:37888\n" \
"v_mfma_f32_16x16x32_bf16 v[12:15], a[92:95], a[4:7], v[12:15]\n" \
"buffer_load_dwordx4 v69, s[44:47], s83 offen offset:1024 lds\n" \
"v_mfma_f32_16x16x32_bf16 v[28:31], a[88:91], a[8:11], v[28:31]\n" \
"ds_read_b64_tr_b16 a[100:101], v74 offset:50176\n" \
"v_mfma_f32_16x16x32_bf16 v[28:31], a[92:95], a[12:15], v[28:31]\n" \
"v_mfma_f32_16x16x32_bf16 v[24:27], a[80:83], a[8:11], v[24:27]\n" \
"ds_read_b64_tr_b16 a[102:103], v74 offset:54272\n" \
"v_mfma_f32_16x16x32_bf16 v[24:27], a[84:87], a[12:15], v[24:27]\n" \
"buffer_load_dwordx4 v70, s[40:43], s83 offen offset:2048 lds\n" \
"v_mfma_f32_16x16x32_bf16 v[20:23], a[72:75], a[8:11], v[20:23]\n" \
"ds_read_b64_tr_b16 a[104:105], v75 offset:33792\n" \
"v_mfma_f32_16x16x32_bf16 v[20:23], a[76:79], a[12:15], v[20:23]\n" \
"v_mfma_f32_16x16x32_bf16 v[16:19], a[64:67], a[8:11], v[16:19]\n" \
"ds_read_b64_tr_b16 a[106:107], v75 offset:37888\n" \
"v_mfma_f32_16x16x32_bf16 v[16:19], a[68:71], a[12:15], v[16:19]\n" \
"buffer_load_dwordx4 v71, s[44:47], s83 offen offset:3072 lds\n" \
"v_mfma_f32_16x16x32_bf16 v[32:35], a[64:67], a[16:19], v[32:35]\n" \
"ds_read_b64_tr_b16 a[108:109], v75 offset:50176\n" \
"v_mfma_f32_16x16x32_bf16 v[32:35], a[68:71], a[20:23], v[32:35]\n" \
"v_mfma_f32_16x16x32_bf16 v[36:39], a[72:75], a[16:19], v[36:39]\n" \
"s_add_u32 m0, s74, 102400\n" \
"ds_read_b64_tr_b16 a[110:111], v75 offset:54272\n" \
"v_mfma_f32_16x16x32_bf16 v[36:39], a[76:79], a[20:23], v[36:39]\n" \
"buffer_load_dwordx4 v68, s[40:43], s84 offen offset:0 lds\n" \
"v_mfma_f32_16x16x32_bf16 v[40:43], a[80:83], a[16:19], v[40:43]\n" \
"ds_read_b64_tr_b16 a[112:113], v74 offset:34304\n" \
"v_mfma_f32_16x16x32_bf16 v[40:43], a[84:87], a[20:23], v[40:43]\n" \
"v_mfma_f32_16x16x32_bf16 v[44:47], a[88:91], a[16:19], v[44:47]\n" \
"ds_read_b64_tr_b16 a[114:115], v74 offset:38400\n" \
"v_mfma_f32_16x16x32_bf16 v[44:47], a[92:95], a[20:23], v[44:47]\n" \
"buffer_load_dwordx4 v69, s[44:47], s84 offen offset:1024 lds\n" \
"v_mfma_f32_16x16x32_bf16 v[60:63], a[88:91], a[24:27], v[60:63]\n" \
"ds_read_b64_tr_b16 a[116:117], v74 offset:50688\n" \
"v_mfma_f32_16x16x32_bf16 v[60:63], a[92:95], a[28:31], v[60:63]\n" \
"v_mfma_f32_16x16x32_bf16 v[56:59], a[80:83], a[24:27], v[56:59]\n" \
"ds_read_b64_tr_b16 a[118:119], v74 offset:54784\n" \
"v_mfma_f32_16x16x32_bf16 v[56:59], a[84:87], a[28:31], v[56:59]\n" \
"buffer_load_dwordx4 v70, s[40:43], s84 offen offset:2048 lds\n" \
"v_mfma_f32_16x16x32_bf16 v[52:55], a[72:75], a[24:27], v[52:55]\n" \
"ds_read_b64_tr_b16 a[120:121], v75 offset:34304\n" \
"v_mfma_f32_16x16x32_bf16 v[52:55], a[76:79], a[28:31], v[52:55]\n" \
"v_mfma_f32_16x16x32_bf16 v[48:51], a[64:67], a[24:27], v[48:51]\n" \
"ds_read_b64_tr_b16 a[122:123], v75 offset:38400\n" \
"v_mfma_f32_16x16x32_bf16 v[48:51], a[68:71], a[28:31], v[48:51]\n" \
"buffer_load_dwordx4 v71, s[44:47], s84 offen offset:3072 lds\n" \
"v_mfma_f32_16x16x32_bf16 v[144:147], a[64:67], a[32:35], v[144:147]\n" \
"ds_read_b64_tr_b16 a[124:125], v75 offset:50688\n" \
"v_mfma_f32_16x16x32_bf16 v[144:147], a[68:71], a[36:39], v[144:147]\n" \
"v_mfma_f32_16x16x32_bf16 v[148:151], a[72:75], a[32:35], v[148:151]\n" \
"ds_read_b64_tr_b16 a[126:127], v75 offset:54784\n" \
"v_mfma_f32_16x16x32_bf16 v[148:151], a[76:79], a[36:39], v[148:151]\n" \
"v_mfma_f32_16x16x32_bf16 v[152:155], a[80:83], a[32:35], v[152:155]\n" \
"v_mfma_f32_16x16x32_bf16 v[152:155], a[84:87], a[36:39], v[152:155]\n" \
"v_mfma_f32_16x16x32_bf16 v[156:159], a[88:91], a[32:35], v[156:159]\n" \
"v_mfma_f32_16x16x32_bf16 v[156:159], a[92:95], a[36:39], v[156:159]\n" \
"v_mfma_f32_16x16x32_bf16 v[188:191], a[88:91], a[40:43], v[188:191]\n" \
"v_mfma_f32_16x16x32_bf16 v[188:191], a[92:95], a[44:47], v[188:191]\n" \
"v_mfma_f32_16x16x32_bf16 v[184:187], a[80:83], a[40:43], v[184:187]\n" \
"v_mfma_f32_16x16x32_bf16 v[184:187], a[84:87], a[44:47], v[184:187]\n" \
"v_mfma_f32_16x16x32_bf16 v[180:183], a[72:75], a[40:43], v[180:183]\n" \
"v_mfma_f32_16x16x32_bf16 v[180:183], a[76:79], a[44:47], v[180:183]\n" \
"v_mfma_f32_16x16x32_bf16 v[176:179], a[64:67], a[40:43], v[176:179]\n" \
"v_mfma_f32_16x16x32_bf16 v[176:179], a[68:71], a[44:47], v[176:179]\n" \
"s_waitcnt lgkmcnt(0)\n" \
"v_mfma_f32_16x16x32_bf16 v[192:195], a[64:67], a[48:51], v[192:195]\n" \
"v_mfma_f32_16x16x32_bf16 v[192:195], a[68:71], a[52:55], v[192:195]\n" \
"v_mfma_f32_16x16x32_bf16 v[196:199], a[72:75], a[48:51], v[196:199]\n" \
"v_mfma_f32_16x16x32_bf16 v[196:199], a[76:79], a[52:55], v[196:199]\n" \
"v_mfma_f32_16x16x32_bf16 v[200:203], a[80:83], a[48:51], v[200:203]\n" \
"v_mfma_f32_16x16x32_bf16 v[200:203], a[84:87], a[52:55], v[200:203]\n" \
"v_mfma_f32_16x16x32_bf16 v[204:207], a[88:91], a[48:51], v[204:207]\n" \
"v_mfma_f32_16x16x32_bf16 v[204:207], a[92:95], a[52:55], v[204:207]\n" \
"v_mfma_f32_16x16x32_bf16 v[220:223], a[88:91], a[56:59], v[220:223]\n" \
"v_mfma_f32_16x16x32_bf16 v[220:223], a[92:95], a[60:63], v[220:223]\n" \
"v_mfma_f32_16x16x32_bf16 v[216:219], a[80:83], a[56:59], v[216:219]\n" \
"v_mfma_f32_16x16x32_bf16 v[216:219], a[84:87], a[60:63], v[216:219]\n" \
"v_mfma_f32_16x16x32_bf16 v[212:215], a[72:75], a[56:59], v[212:215]\n" \
"v_mfma_f32_16x16x32_bf16 v[212:215], a[76:79], a[60:63], v[212:215]\n" \
"v_mfma_f32_16x16x32_bf16 v[208:211], a[64:67], a[56:59], v[208:211]\n" \
"v_mfma_f32_16x16x32_bf16 v[208:211], a[68:71], a[60:63], v[208:211]\n" \
"s_waitcnt vmcnt(11) lgkmcnt(0)\n" \
"s_barrier\n" \
"v_mfma_f32_16x16x32_bf16 a[128:131], a[0:3], a[96:99], a[128:131]\n" \
"s_add_u32 m0, s74, 32768\n" \
"ds_read_b64_tr_b16 a[64:65], v102 offset:0\n" \
"v_mfma_f32_16x16x32_bf16 a[128:131], a[4:7], a[100:103], a[128:131]\n" \
"buffer_load_dwordx4 v64, s[36:39], s81 offen offset:0 lds\n" \
"v_mfma_f32_16x16x32_bf16 a[132:135], a[0:3], a[104:107], a[132:135]\n" \
"ds_read_b64_tr_b16 a[66:67], v102 offset:4096\n" \
"v_mfma_f32_16x16x32_bf16 a[132:135], a[4:7], a[108:111], a[132:135]\n" \
"v_mfma_f32_16x16x32_bf16 a[136:139], a[0:3], a[112:115], a[136:139]\n" \
"ds_read_b64_tr_b16 a[68:69], v102 offset:16384\n" \
"v_mfma_f32_16x16x32_bf16 a[136:139], a[4:7], a[116:119], a[136:139]\n" \
"buffer_load_dwordx4 v65, s[36:39], s81 offen offset:1024 lds\n" \
"v_mfma_f32_16x16x32_bf16 a[140:143], a[0:3], a[120:123], a[140:143]\n" \
"ds_read_b64_tr_b16 a[70:71], v102 offset:20480\n" \
"v_mfma_f32_16x16x32_bf16 a[140:143], a[4:7], a[124:127], a[140:143]\n" \
"v_mfma_f32_16x16x32_bf16 a[156:159], a[8:11], a[120:123], a[156:159]\n" \
"ds_read_b128 a[0:3], v96 offset:0\n" \
"v_mfma_f32_16x16x32_bf16 a[156:159], a[12:15], a[124:127], a[156:159]\n" \
"buffer_load_dwordx4 v66, s[36:39], s81 offen offset:2048 lds\n" \
"v_mfma_f32_16x16x32_bf16 a[152:155], a[8:11], a[112:115], a[152:155]\n" \
"ds_read_b64_tr_b16 a[72:73], v103 offset:0\n" \
"v_mfma_f32_16x16x32_bf16 a[152:155], a[12:15], a[116:119], a[152:155]\n" \
"v_mfma_f32_16x16x32_bf16 a[148:151], a[8:11], a[104:107], a[148:151]\n" \
"ds_read_b128 a[4:7], v97 offset:0\n" \
"v_mfma_f32_16x16x32_bf16 a[148:151], a[12:15], a[108:111], a[148:151]\n" \
"buffer_load_dwordx4 v67, s[36:39], s81 offen offset:3072 lds\n" \
"v_mfma_f32_16x16x32_bf16 a[144:147], a[8:11], a[96:99], a[144:147]\n" \
"ds_read_b64_tr_b16 a[74:75], v103 offset:4096\n" \
"v_mfma_f32_16x16x32_bf16 a[144:147], a[12:15], a[100:103], a[144:147]\n" \
"v_mfma_f32_16x16x32_bf16 a[160:163], a[16:19], a[96:99], a[160:163]\n" \
"s_add_u32 m0, s74, 36864\n" \
"ds_read_b128 a[8:11], v96 offset:2048\n" \
"v_mfma_f32_16x16x32_bf16 a[160:163], a[20:23], a[100:103], a[160:163]\n" \
"buffer_load_dwordx4 v64, s[36:39], s82 offen offset:0 lds\n" \
"v_mfma_f32_16x16x32_bf16 a[164:167], a[16:19], a[104:107], a[164:167]\n" \
"ds_read_b64_tr_b16 a[76:77], v103 offset:16384\n" \
"v_mfma_f32_16x16x32_bf16 a[164:167], a[20:23], a[108:111], a[164:167]\n" \
"v_mfma_f32_16x16x32_bf16 a[168:171], a[16:19], a[112:115], a[168:171]\n" \
"ds_read_b128 a[12:15], v97 offset:2048\n" \
"v_mfma_f32_16x16x32_bf16 a[168:171], a[20:23], a[116:119], a[168:171]\n" \
"buffer_load_dwordx4 v65, s[36:39], s82 offen offset:1024 lds\n" \
"v_mfma_f32_16x16x32_bf16 a[172:175], a[16:19], a[120:123], a[172:175]\n" \
"ds_read_b64_tr_b16 a[78:79], v103 offset:20480\n" \
"v_mfma_f32_16x16x32_bf16 a[172:175], a[20:23], a[124:127], a[172:175]\n" \
"v_mfma_f32_16x16x32_bf16 a[188:191], a[24:27], a[120:123], a[188:191]\n" \
"ds_read_b128 a[16:19], v96 offset:4096\n" \
"v_mfma_f32_16x16x32_bf16 a[188:191], a[28:31], a[124:127], a[188:191]\n" \
"buffer_load_dwordx4 v66, s[36:39], s82 offen offset:2048 lds\n" \
"v_mfma_f32_16x16x32_bf16 a[184:187], a[24:27], a[112:115], a[184:187]\n" \
"ds_read_b64_tr_b16 a[80:81], v102 offset:512\n" \
"v_mfma_f32_16x16x32_bf16 a[184:187], a[28:31], a[116:119], a[184:187]\n" \
"v_mfma_f32_16x16x32_bf16 a[180:183], a[24:27], a[104:107], a[180:183]\n" \
"ds_read_b128 a[20:23], v97 offset:4096\n" \
"v_mfma_f32_16x16x32_bf16 a[180:183], a[28:31], a[108:111], a[180:183]\n" \
"buffer_load_dwordx4 v67, s[36:39], s82 offen offset:3072 lds\n" \
"v_mfma_f32_16x16x32_bf16 a[176:179], a[24:27], a[96:99], a[176:179]\n" \
"s_mov_b64 s[64:65], s[12:13]\n" \
"s_add_u32 s68, s67, 0x300000\n" \
"ds_read_b64_tr_b16 a[82:83], v102 offset:4608\n" \
"v_mfma_f32_16x16x32_bf16 a[176:179], a[28:31], a[100:103], a[176:179]\n" \
"s_add_u32 s64, s64, s68\n" \
"s_addc_u32 s65, s65, 0\n" \
"v_mfma_f32_16x16x32_bf16 a[192:195], a[32:35], a[96:99], a[192:195]\n" \
"ds_read_b128 a[24:27], v96 offset:6144\n" \
"v_mfma_f32_16x16x32_bf16 a[192:195], a[36:39], a[100:103], a[192:195]\n" \
"global_load_dword v238, v234, s[64:65]\n" \
"v_mfma_f32_16x16x32_bf16 a[196:199], a[32:35], a[104:107], a[196:199]\n" \
"s_mov_b64 s[64:65], s[12:13]\n" \
"s_add_u32 s68, s67, 0x400000\n" \
"ds_read_b64_tr_b16 a[84:85], v102 offset:16896\n" \
"v_mfma_f32_16x16x32_bf16 a[196:199], a[36:39], a[108:111], a[196:199]\n" \
"s_add_u32 s64, s64, s68\n" \
"s_addc_u32 s65, s65, 0\n" \
"v_mfma_f32_16x16x32_bf16 a[200:203], a[32:35], a[112:115], a[200:203]\n" \
"ds_read_b128 a[28:31], v97 offset:6144\n" \
"v_mfma_f32_16x16x32_bf16 a[200:203], a[36:39], a[116:119], a[200:203]\n" \
"global_load_dword v238, v234, s[64:65]\n" \
"v_mfma_f32_16x16x32_bf16 a[204:207], a[32:35], a[120:123], a[204:207]\n" \
"s_mov_b64 s[64:65], s[12:13]\n" \
"s_add_u32 s68, s67, 0x600000\n" \
"ds_read_b64_tr_b16 a[86:87], v102 offset:20992\n" \
"v_mfma_f32_16x16x32_bf16 a[204:207], a[36:39], a[124:127], a[204:207]\n" \
"s_add_u32 s64, s64, s68\n" \
"s_addc_u32 s65, s65, 0\n" \
"v_mfma_f32_16x16x32_bf16 a[220:223], a[40:43], a[120:123], a[220:223]\n" \
"ds_read_b128 a[32:35], v96 offset:8192\n" \
"v_mfma_f32_16x16x32_bf16 a[220:223], a[44:47], a[124:127], a[220:223]\n" \
"global_load_dword v238, v234, s[64:65]\n" \
"v_mfma_f32_16x16x32_bf16 a[216:219], a[40:43], a[112:115], a[216:219]\n" \
"ds_read_b64_tr_b16 a[88:89], v103 offset:512\n" \
"v_mfma_f32_16x16x32_bf16 a[216:219], a[44:47], a[116:119], a[216:219]\n" \
"v_mfma_f32_16x16x32_bf16 a[212:215], a[40:43], a[104:107], a[212:215]\n" \
"ds_read_b128 a[36:39], v97 offset:8192\n" \
"v_mfma_f32_16x16x32_bf16 a[212:215], a[44:47], a[108:111], a[212:215]\n" \
"v_mfma_f32_16x16x32_bf16 a[208:211], a[40:43], a[96:99], a[208:211]\n" \
"ds_read_b64_tr_b16 a[90:91], v103 offset:4608\n" \
"v_mfma_f32_16x16x32_bf16 a[208:211], a[44:47], a[100:103], a[208:211]\n" \
"v_mfma_f32_16x16x32_bf16 a[224:227], a[48:51], a[96:99], a[224:227]\n" \
"ds_read_b128 a[40:43], v96 offset:10240\n" \
"v_mfma_f32_16x16x32_bf16 a[224:227], a[52:55], a[100:103], a[224:227]\n" \
"v_mfma_f32_16x16x32_bf16 a[228:231], a[48:51], a[104:107], a[228:231]\n" \
"ds_read_b64_tr_b16 a[92:93], v103 offset:16896\n" \
"v_mfma_f32_16x16x32_bf16 a[228:231], a[52:55], a[108:111], a[228:231]\n" \
"v_mfma_f32_16x16x32_bf16 a[232:235], a[48:51], a[112:115], a[232:235]\n" \
"ds_read_b128 a[44:47], v97 offset:10240\n" \
"v_mfma_f32_16x16x32_bf16 a[232:235], a[52:55], a[116:119], a[232:235]\n" \
"v_mfma_f32_16x16x32_bf16 a[236:239], a[48:51], a[120:123], a[236:239]\n" \
"ds_read_b64_tr_b16 a[94:95], v103 offset:20992\n" \
"v_mfma_f32_16x16x32_bf16 a[236:239], a[52:55], a[124:127], a[236:239]\n" \
"v_mfma_f32_16x16x32_bf16 a[252:255], a[56:59], a[120:123], a[252:255]\n" \
"ds_read_b128 a[48:51], v96 offset:12288\n" \
"v_mfma_f32_16x16x32_bf16 a[252:255], a[60:63], a[124:127], a[252:255]\n" \
"v_mfma_f32_16x16x32_bf16 a[248:251], a[56:59], a[112:115], a[248:251]\n" \
"v_mfma_f32_16x16x32_bf16 a[248:251], a[60:63], a[116:119], a[248:251]\n" \
"v_mfma_f32_16x16x32_bf16 a[244:247], a[56:59], a[104:107], a[244:247]\n" \
"Lpg1669_%=:\n" \
"Lst0x2_%=:\n" \
"s_cbranch_execz Lpg3904_%=\n" \
"ds_read_b128 a[52:55], v97 offset:12288\n" \
"v_mfma_f32_16x16x32_bf16 a[244:247], a[60:63], a[108:111], a[244:247]\n" \
"v_mfma_f32_16x16x32_bf16 a[240:243], a[56:59], a[96:99], a[240:243]\n" \
"v_mfma_f32_16x16x32_bf16 a[240:243], a[60:63], a[100:103], a[240:243]\n" \
"s_waitcnt lgkmcnt(1)\n" \
"v_mfma_f32_16x16x32_bf16 v[0:3], a[64:67], a[0:3], v[0:3]\n" \
"ds_read_b128 a[56:59], v96 offset:14336\n" \
"v_mfma_f32_16x16x32_bf16 v[0:3], a[68:71], a[4:7], v[0:3]\n" \
"v_mfma_f32_16x16x32_bf16 v[4:7], a[72:75], a[0:3], v[4:7]\n" \
"s_add_u32 m0, s79, 0\n" \
"ds_read_b128 a[60:63], v97 offset:14336\n" \
"v_mfma_f32_16x16x32_bf16 v[4:7], a[76:79], a[4:7], v[4:7]\n" \
"buffer_load_dwordx4 v228, s[52:55], s78 offen offset:0 lds\n" \
"v_mfma_f32_16x16x32_bf16 v[8:11], a[80:83], a[0:3], v[8:11]\n" \
"ds_read_b64_tr_b16 a[96:97], v102 offset:1024\n" \
"v_mfma_f32_16x16x32_bf16 v[8:11], a[84:87], a[4:7], v[8:11]\n" \
"v_mfma_f32_16x16x32_bf16 v[12:15], a[88:91], a[0:3], v[12:15]\n" \
"ds_read_b64_tr_b16 a[98:99], v102 offset:5120\n" \
"v_mfma_f32_16x16x32_bf16 v[12:15], a[92:95], a[4:7], v[12:15]\n" \
"buffer_load_dwordx4 v229, s[52:55], s78 offen offset:1024 lds\n" \
"v_mfma_f32_16x16x32_bf16 v[28:31], a[88:91], a[8:11], v[28:31]\n" \
"ds_read_b64_tr_b16 a[100:101], v102 offset:17408\n" \
"v_mfma_f32_16x16x32_bf16 v[28:31], a[92:95], a[12:15], v[28:31]\n" \
"v_mfma_f32_16x16x32_bf16 v[24:27], a[80:83], a[8:11], v[24:27]\n" \
"s_xor_b32 s78, s78, 0x180000\n" \
"ds_read_b64_tr_b16 a[102:103], v102 offset:21504\n" \
"v_mfma_f32_16x16x32_bf16 v[24:27], a[84:87], a[12:15], v[24:27]\n" \
"buffer_load_dwordx4 v228, s[52:55], s89 offen offset:2048 lds\n" \
"v_mfma_f32_16x16x32_bf16 v[20:23], a[72:75], a[8:11], v[20:23]\n" \
"ds_read_b64_tr_b16 a[104:105], v103 offset:1024\n" \
"v_mfma_f32_16x16x32_bf16 v[20:23], a[76:79], a[12:15], v[20:23]\n" \
"v_mfma_f32_16x16x32_bf16 v[16:19], a[64:67], a[8:11], v[16:19]\n" \
"ds_read_b64_tr_b16 a[106:107], v103 offset:5120\n" \
"v_mfma_f32_16x16x32_bf16 v[16:19], a[68:71], a[12:15], v[16:19]\n" \
"buffer_load_dwordx4 v229, s[52:55], s89 offen offset:3072 lds\n" \
"v_mfma_f32_16x16x32_bf16 v[32:35], a[64:67], a[16:19], v[32:35]\n" \
"ds_read_b64_tr_b16 a[108:109], v103 offset:17408\n" \
"v_mfma_f32_16x16x32_bf16 v[32:35], a[68:71], a[20:23], v[32:35]\n" \
"s_xor_b32 s89, s89, 0x180000\n" \
"v_mfma_f32_16x16x32_bf16 v[36:39], a[72:75], a[16:19], v[36:39]\n" \
"s_add_u32 m0, s79, 16384\n" \
"ds_read_b64_tr_b16 a[110:111], v103 offset:21504\n" \
"v_mfma_f32_16x16x32_bf16 v[36:39], a[76:79], a[20:23], v[36:39]\n" \
"buffer_load_dwordx4 v228, s[52:55], s78 offen offset:0 lds\n" \
"v_mfma_f32_16x16x32_bf16 v[40:43], a[80:83], a[16:19], v[40:43]\n" \
"ds_read_b64_tr_b16 a[112:113], v102 offset:1536\n" \
"v_mfma_f32_16x16x32_bf16 v[40:43], a[84:87], a[20:23], v[40:43]\n" \
"v_mfma_f32_16x16x32_bf16 v[44:47], a[88:91], a[16:19], v[44:47]\n" \
"ds_read_b64_tr_b16 a[114:115], v102 offset:5632\n" \
"v_mfma_f32_16x16x32_bf16 v[44:47], a[92:95], a[20:23], v[44:47]\n" \
"buffer_load_dwordx4 v229, s[52:55], s78 offen offset:1024 lds\n" \
"v_mfma_f32_16x16x32_bf16 v[60:63], a[88:91], a[24:27], v[60:63]\n" \
"ds_read_b64_tr_b16 a[116:117], v102 offset:17920\n" \
"v_mfma_f32_16x16x32_bf16 v[60:63], a[92:95], a[28:31], v[60:63]\n" \
"v_mfma_f32_16x16x32_bf16 v[56:59], a[80:83], a[24:27], v[56:59]\n" \
"s_xor_b32 s78, s78, 0x400000\n" \
"ds_read_b64_tr_b16 a[118:119], v102 offset:22016\n" \
"v_mfma_f32_16x16x32_bf16 v[56:59], a[84:87], a[28:31], v[56:59]\n" \
"buffer_load_dwordx4 v228, s[52:55], s89 offen offset:2048 lds\n" \
"v_mfma_f32_16x16x32_bf16 v[52:55], a[72:75], a[24:27], v[52:55]\n" \
"ds_read_b64_tr_b16 a[120:121], v103 offset:1536\n" \
"v_mfma_f32_16x16x32_bf16 v[52:55], a[76:79], a[28:31], v[52:55]\n" \
"v_mfma_f32_16x16x32_bf16 v[48:51], a[64:67], a[24:27], v[48:51]\n" \
"ds_read_b64_tr_b16 a[122:123], v103 offset:5632\n" \
"v_mfma_f32_16x16x32_bf16 v[48:51], a[68:71], a[28:31], v[48:51]\n" \
"buffer_load_dwordx4 v229, s[52:55], s89 offen offset:3072 lds\n" \
"v_mfma_f32_16x16x32_bf16 v[144:147], a[64:67], a[32:35], v[144:147]\n" \
"ds_read_b64_tr_b16 a[124:125], v103 offset:17920\n" \
"v_mfma_f32_16x16x32_bf16 v[144:147], a[68:71], a[36:39], v[144:147]\n" \
"v_mfma_f32_16x16x32_bf16 v[148:151], a[72:75], a[32:35], v[148:151]\n" \
"s_xor_b32 s89, s89, 0x400000\n" \
"ds_read_b64_tr_b16 a[126:127], v103 offset:22016\n" \
"v_mfma_f32_16x16x32_bf16 v[148:151], a[76:79], a[36:39], v[148:151]\n" \
"v_mfma_f32_16x16x32_bf16 v[152:155], a[80:83], a[32:35], v[152:155]\n" \
"v_mfma_f32_16x16x32_bf16 v[152:155], a[84:87], a[36:39], v[152:155]\n" \
"v_mfma_f32_16x16x32_bf16 v[156:159], a[88:91], a[32:35], v[156:159]\n" \
"v_mfma_f32_16x16x32_bf16 v[156:159], a[92:95], a[36:39], v[156:159]\n" \
"v_mfma_f32_16x16x32_bf16 v[188:191], a[88:91], a[40:43], v[188:191]\n" \
"v_mfma_f32_16x16x32_bf16 v[188:191], a[92:95], a[44:47], v[188:191]\n" \
"v_mfma_f32_16x16x32_bf16 v[184:187], a[80:83], a[40:43], v[184:187]\n" \
"v_mfma_f32_16x16x32_bf16 v[184:187], a[84:87], a[44:47], v[184:187]\n" \
"v_mfma_f32_16x16x32_bf16 v[180:183], a[72:75], a[40:43], v[180:183]\n" \
"v_mfma_f32_16x16x32_bf16 v[180:183], a[76:79], a[44:47], v[180:183]\n" \
"v_mfma_f32_16x16x32_bf16 v[176:179], a[64:67], a[40:43], v[176:179]\n" \
"v_mfma_f32_16x16x32_bf16 v[176:179], a[68:71], a[44:47], v[176:179]\n" \
"s_waitcnt lgkmcnt(0)\n" \
"v_mfma_f32_16x16x32_bf16 v[192:195], a[64:67], a[48:51], v[192:195]\n" \
"v_mfma_f32_16x16x32_bf16 v[192:195], a[68:71], a[52:55], v[192:195]\n" \
"v_mfma_f32_16x16x32_bf16 v[196:199], a[72:75], a[48:51], v[196:199]\n" \
"v_mfma_f32_16x16x32_bf16 v[196:199], a[76:79], a[52:55], v[196:199]\n" \
"v_mfma_f32_16x16x32_bf16 v[200:203], a[80:83], a[48:51], v[200:203]\n" \
"v_mfma_f32_16x16x32_bf16 v[200:203], a[84:87], a[52:55], v[200:203]\n" \
"v_mfma_f32_16x16x32_bf16 v[204:207], a[88:91], a[48:51], v[204:207]\n" \
"v_mfma_f32_16x16x32_bf16 v[204:207], a[92:95], a[52:55], v[204:207]\n" \
"v_mfma_f32_16x16x32_bf16 v[220:223], a[88:91], a[56:59], v[220:223]\n" \
"v_mfma_f32_16x16x32_bf16 v[220:223], a[92:95], a[60:63], v[220:223]\n" \
"v_mfma_f32_16x16x32_bf16 v[216:219], a[80:83], a[56:59], v[216:219]\n" \
"v_mfma_f32_16x16x32_bf16 v[216:219], a[84:87], a[60:63], v[216:219]\n" \
"v_mfma_f32_16x16x32_bf16 v[212:215], a[72:75], a[56:59], v[212:215]\n" \
"v_mfma_f32_16x16x32_bf16 v[212:215], a[76:79], a[60:63], v[212:215]\n" \
"v_mfma_f32_16x16x32_bf16 v[208:211], a[64:67], a[56:59], v[208:211]\n" \
"v_mfma_f32_16x16x32_bf16 v[208:211], a[68:71], a[60:63], v[208:211]\n" \
"s_waitcnt vmcnt(11) lgkmcnt(0)\n" \
"s_barrier\n" \
"v_mfma_f32_16x16x32_bf16 a[128:131], a[0:3], a[96:99], a[128:131]\n" \
"s_add_u32 m0, s74, 131072\n" \
"ds_read_b64_tr_b16 a[64:65], v98 offset:32768\n" \
"v_mfma_f32_16x16x32_bf16 a[128:131], a[4:7], a[100:103], a[128:131]\n" \
"buffer_load_dwordx4 v224, s[48:51], s75 offen offset:0 lds\n" \
"v_mfma_f32_16x16x32_bf16 a[132:135], a[0:3], a[104:107], a[132:135]\n" \
"ds_read_b64_tr_b16 a[66:67], v98 offset:36864\n" \
"v_mfma_f32_16x16x32_bf16 a[132:135], a[4:7], a[108:111], a[132:135]\n" \
"v_mfma_f32_16x16x32_bf16 a[136:139], a[0:3], a[112:115], a[136:139]\n" \
"ds_read_b64_tr_b16 a[68:69], v98 offset:49152\n" \
"v_mfma_f32_16x16x32_bf16 a[136:139], a[4:7], a[116:119], a[136:139]\n" \
"buffer_load_dwordx4 v225, s[48:51], s75 offen offset:1024 lds\n" \
"v_mfma_f32_16x16x32_bf16 a[140:143], a[0:3], a[120:123], a[140:143]\n" \
"ds_read_b64_tr_b16 a[70:71], v98 offset:53248\n" \
"v_mfma_f32_16x16x32_bf16 a[140:143], a[4:7], a[124:127], a[140:143]\n" \
"v_mfma_f32_16x16x32_bf16 a[156:159], a[8:11], a[120:123], a[156:159]\n" \
"ds_read_b128 a[0:3], v72 offset:32768\n" \
"v_mfma_f32_16x16x32_bf16 a[156:159], a[12:15], a[124:127], a[156:159]\n" \
"buffer_load_dwordx4 v226, s[48:51], s75 offen offset:2048 lds\n" \
"v_mfma_f32_16x16x32_bf16 a[152:155], a[8:11], a[112:115], a[152:155]\n" \
"ds_read_b64_tr_b16 a[72:73], v99 offset:32768\n" \
"v_mfma_f32_16x16x32_bf16 a[152:155], a[12:15], a[116:119], a[152:155]\n" \
"v_mfma_f32_16x16x32_bf16 a[148:151], a[8:11], a[104:107], a[148:151]\n" \
"ds_read_b128 a[4:7], v73 offset:32768\n" \
"v_mfma_f32_16x16x32_bf16 a[148:151], a[12:15], a[108:111], a[148:151]\n" \
"buffer_load_dwordx4 v227, s[48:51], s75 offen offset:3072 lds\n" \
"v_mfma_f32_16x16x32_bf16 a[144:147], a[8:11], a[96:99], a[144:147]\n" \
"ds_read_b64_tr_b16 a[74:75], v99 offset:36864\n" \
"v_mfma_f32_16x16x32_bf16 a[144:147], a[12:15], a[100:103], a[144:147]\n" \
"s_xor_b32 s75, s75, 0x180\n" \
"v_mfma_f32_16x16x32_bf16 a[160:163], a[16:19], a[96:99], a[160:163]\n" \
"s_add_u32 m0, s74, 135168\n" \
"ds_read_b128 a[8:11], v72 offset:34816\n" \
"v_mfma_f32_16x16x32_bf16 a[160:163], a[20:23], a[100:103], a[160:163]\n" \
"buffer_load_dwordx4 v224, s[48:51], s77 offen offset:0 lds\n" \
"v_mfma_f32_16x16x32_bf16 a[164:167], a[16:19], a[104:107], a[164:167]\n" \
"ds_read_b64_tr_b16 a[76:77], v99 offset:49152\n" \
"v_mfma_f32_16x16x32_bf16 a[164:167], a[20:23], a[108:111], a[164:167]\n" \
"v_mfma_f32_16x16x32_bf16 a[168:171], a[16:19], a[112:115], a[168:171]\n" \
"ds_read_b128 a[12:15], v73 offset:34816\n" \
"v_mfma_f32_16x16x32_bf16 a[168:171], a[20:23], a[116:119], a[168:171]\n" \
"buffer_load_dwordx4 v225, s[48:51], s77 offen offset:1024 lds\n" \
"v_mfma_f32_16x16x32_bf16 a[172:175], a[16:19], a[120:123], a[172:175]\n" \
"ds_read_b64_tr_b16 a[78:79], v99 offset:53248\n" \
"v_mfma_f32_16x16x32_bf16 a[172:175], a[20:23], a[124:127], a[172:175]\n" \
"v_mfma_f32_16x16x32_bf16 a[188:191], a[24:27], a[120:123], a[188:191]\n" \
"ds_read_b128 a[16:19], v72 offset:36864\n" \
"v_mfma_f32_16x16x32_bf16 a[188:191], a[28:31], a[124:127], a[188:191]\n" \
"buffer_load_dwordx4 v226, s[48:51], s77 offen offset:2048 lds\n" \
"v_mfma_f32_16x16x32_bf16 a[184:187], a[24:27], a[112:115], a[184:187]\n" \
"ds_read_b64_tr_b16 a[80:81], v98 offset:33280\n" \
"v_mfma_f32_16x16x32_bf16 a[184:187], a[28:31], a[116:119], a[184:187]\n" \
"v_mfma_f32_16x16x32_bf16 a[180:183], a[24:27], a[104:107], a[180:183]\n" \
"ds_read_b128 a[20:23], v73 offset:36864\n" \
"v_mfma_f32_16x16x32_bf16 a[180:183], a[28:31], a[108:111], a[180:183]\n" \
"buffer_load_dwordx4 v227, s[48:51], s77 offen offset:3072 lds\n" \
"v_mfma_f32_16x16x32_bf16 a[176:179], a[24:27], a[96:99], a[176:179]\n" \
"s_mov_b64 s[64:65], s[12:13]\n" \
"s_add_u32 s68, s67, 0x700000\n" \
"ds_read_b64_tr_b16 a[82:83], v98 offset:37376\n" \
"v_mfma_f32_16x16x32_bf16 a[176:179], a[28:31], a[100:103], a[176:179]\n" \
"s_add_u32 s64, s64, s68\n" \
"s_addc_u32 s65, s65, 0\n" \
"v_mfma_f32_16x16x32_bf16 a[192:195], a[32:35], a[96:99], a[192:195]\n" \
"s_xor_b32 s77, s77, 0x180\n" \
"ds_read_b128 a[24:27], v72 offset:38912\n" \
"v_mfma_f32_16x16x32_bf16 a[192:195], a[36:39], a[100:103], a[192:195]\n" \
"global_load_dword v238, v234, s[64:65]\n" \
"v_mfma_f32_16x16x32_bf16 a[196:199], a[32:35], a[104:107], a[196:199]\n" \
"ds_read_b64_tr_b16 a[84:85], v98 offset:49664\n" \
"v_mfma_f32_16x16x32_bf16 a[196:199], a[36:39], a[108:111], a[196:199]\n" \
"v_mfma_f32_16x16x32_bf16 a[200:203], a[32:35], a[112:115], a[200:203]\n" \
"s_lshr_b32 s68, s66, 2\n" \
"s_and_b32 s68, s68, 0xffff\n" \
"s_add_u32 s68, s68, 65585152\n" \
"ds_read_b128 a[28:31], v73 offset:38912\n" \
"v_mfma_f32_16x16x32_bf16 a[200:203], a[36:39], a[116:119], a[200:203]\n" \
"buffer_load_dword v238, v242, s[56:59], s68 offen sc1\n" \
"v_mfma_f32_16x16x32_bf16 a[204:207], a[32:35], a[120:123], a[204:207]\n" \
"ds_read_b64_tr_b16 a[86:87], v98 offset:53760\n" \
"v_mfma_f32_16x16x32_bf16 a[204:207], a[36:39], a[124:127], a[204:207]\n" \
"v_mfma_f32_16x16x32_bf16 a[220:223], a[40:43], a[120:123], a[220:223]\n" \
"ds_read_b128 a[32:35], v72 offset:40960\n" \
"v_mfma_f32_16x16x32_bf16 a[220:223], a[44:47], a[124:127], a[220:223]\n" \
"v_mfma_f32_16x16x32_bf16 a[216:219], a[40:43], a[112:115], a[216:219]\n" \
"ds_read_b64_tr_b16 a[88:89], v99 offset:33280\n" \
"v_mfma_f32_16x16x32_bf16 a[216:219], a[44:47], a[116:119], a[216:219]\n" \
"v_mfma_f32_16x16x32_bf16 a[212:215], a[40:43], a[104:107], a[212:215]\n" \
"ds_read_b128 a[36:39], v73 offset:40960\n" \
"v_mfma_f32_16x16x32_bf16 a[212:215], a[44:47], a[108:111], a[212:215]\n" \
"v_mfma_f32_16x16x32_bf16 a[208:211], a[40:43], a[96:99], a[208:211]\n" \
"ds_read_b64_tr_b16 a[90:91], v99 offset:37376\n" \
"v_mfma_f32_16x16x32_bf16 a[208:211], a[44:47], a[100:103], a[208:211]\n" \
"v_mfma_f32_16x16x32_bf16 a[224:227], a[48:51], a[96:99], a[224:227]\n" \
"ds_read_b128 a[40:43], v72 offset:43008\n" \
"v_mfma_f32_16x16x32_bf16 a[224:227], a[52:55], a[100:103], a[224:227]\n" \
"v_mfma_f32_16x16x32_bf16 a[228:231], a[48:51], a[104:107], a[228:231]\n" \
"ds_read_b64_tr_b16 a[92:93], v99 offset:49664\n" \
"v_mfma_f32_16x16x32_bf16 a[228:231], a[52:55], a[108:111], a[228:231]\n" \
"v_mfma_f32_16x16x32_bf16 a[232:235], a[48:51], a[112:115], a[232:235]\n" \
"ds_read_b128 a[44:47], v73 offset:43008\n" \
"v_mfma_f32_16x16x32_bf16 a[232:235], a[52:55], a[116:119], a[232:235]\n" \
"v_mfma_f32_16x16x32_bf16 a[236:239], a[48:51], a[120:123], a[236:239]\n" \
"ds_read_b64_tr_b16 a[94:95], v99 offset:53760\n" \
"v_mfma_f32_16x16x32_bf16 a[236:239], a[52:55], a[124:127], a[236:239]\n" \
"v_mfma_f32_16x16x32_bf16 a[252:255], a[56:59], a[120:123], a[252:255]\n" \
"ds_read_b128 a[48:51], v72 offset:45056\n" \
"v_mfma_f32_16x16x32_bf16 a[252:255], a[60:63], a[124:127], a[252:255]\n" \
"v_mfma_f32_16x16x32_bf16 a[248:251], a[56:59], a[112:115], a[248:251]\n" \
"v_mfma_f32_16x16x32_bf16 a[248:251], a[60:63], a[116:119], a[248:251]\n" \
"v_mfma_f32_16x16x32_bf16 a[244:247], a[56:59], a[104:107], a[244:247]\n" \
"ds_read_b128 a[52:55], v73 offset:45056\n" \
"v_mfma_f32_16x16x32_bf16 a[244:247], a[60:63], a[108:111], a[244:247]\n" \
"v_mfma_f32_16x16x32_bf16 a[240:243], a[56:59], a[96:99], a[240:243]\n" \
"v_mfma_f32_16x16x32_bf16 a[240:243], a[60:63], a[100:103], a[240:243]\n" \
"s_waitcnt lgkmcnt(1)\n" \
"v_mfma_f32_16x16x32_bf16 v[0:3], a[64:67], a[0:3], v[0:3]\n" \
"ds_read_b128 a[56:59], v72 offset:47104\n" \
"v_mfma_f32_16x16x32_bf16 v[0:3], a[68:71], a[4:7], v[0:3]\n" \
"v_mfma_f32_16x16x32_bf16 v[4:7], a[72:75], a[0:3], v[4:7]\n" \
"s_add_u32 m0, s74, 65536\n" \
"ds_read_b128 a[60:63], v73 offset:47104\n" \
"v_mfma_f32_16x16x32_bf16 v[4:7], a[76:79], a[4:7], v[4:7]\n" \
"buffer_load_dwordx4 v224, s[48:51], s75 offen offset:0 lds\n" \
"v_mfma_f32_16x16x32_bf16 v[8:11], a[80:83], a[0:3], v[8:11]\n" \
"ds_read_b64_tr_b16 a[96:97], v98 offset:33792\n" \
"v_mfma_f32_16x16x32_bf16 v[8:11], a[84:87], a[4:7], v[8:11]\n" \
"v_mfma_f32_16x16x32_bf16 v[12:15], a[88:91], a[0:3], v[12:15]\n" \
"ds_read_b64_tr_b16 a[98:99], v98 offset:37888\n" \
"v_mfma_f32_16x16x32_bf16 v[12:15], a[92:95], a[4:7], v[12:15]\n" \
"buffer_load_dwordx4 v225, s[48:51], s75 offen offset:1024 lds\n" \
"v_mfma_f32_16x16x32_bf16 v[28:31], a[88:91], a[8:11], v[28:31]\n" \
"ds_read_b64_tr_b16 a[100:101], v98 offset:50176\n" \
"v_mfma_f32_16x16x32_bf16 v[28:31], a[92:95], a[12:15], v[28:31]\n" \
"v_mfma_f32_16x16x32_bf16 v[24:27], a[80:83], a[8:11], v[24:27]\n" \
"ds_read_b64_tr_b16 a[102:103], v98 offset:54272\n" \
"v_mfma_f32_16x16x32_bf16 v[24:27], a[84:87], a[12:15], v[24:27]\n" \
"buffer_load_dwordx4 v226, s[48:51], s75 offen offset:2048 lds\n" \
"v_mfma_f32_16x16x32_bf16 v[20:23], a[72:75], a[8:11], v[20:23]\n" \
"ds_read_b64_tr_b16 a[104:105], v99 offset:33792\n" \
"v_mfma_f32_16x16x32_bf16 v[20:23], a[76:79], a[12:15], v[20:23]\n" \
"v_mfma_f32_16x16x32_bf16 v[16:19], a[64:67], a[8:11], v[16:19]\n" \
"ds_read_b64_tr_b16 a[106:107], v99 offset:37888\n" \
"v_mfma_f32_16x16x32_bf16 v[16:19], a[68:71], a[12:15], v[16:19]\n" \
"buffer_load_dwordx4 v227, s[48:51], s75 offen offset:3072 lds\n" \
"v_mfma_f32_16x16x32_bf16 v[32:35], a[64:67], a[16:19], v[32:35]\n" \
"ds_read_b64_tr_b16 a[108:109], v99 offset:50176\n" \
"v_mfma_f32_16x16x32_bf16 v[32:35], a[68:71], a[20:23], v[32:35]\n" \
"s_xor_b32 s75, s75, 0x400\n" \
"v_mfma_f32_16x16x32_bf16 v[36:39], a[72:75], a[16:19], v[36:39]\n" \
"s_add_u32 m0, s74, 69632\n" \
"ds_read_b64_tr_b16 a[110:111], v99 offset:54272\n" \
"v_mfma_f32_16x16x32_bf16 v[36:39], a[76:79], a[20:23], v[36:39]\n" \
"buffer_load_dwordx4 v224, s[48:51], s77 offen offset:0 lds\n" \
"v_mfma_f32_16x16x32_bf16 v[40:43], a[80:83], a[16:19], v[40:43]\n" \
"ds_read_b64_tr_b16 a[112:113], v98 offset:34304\n" \
"v_mfma_f32_16x16x32_bf16 v[40:43], a[84:87], a[20:23], v[40:43]\n" \
"v_mfma_f32_16x16x32_bf16 v[44:47], a[88:91], a[16:19], v[44:47]\n" \
"ds_read_b64_tr_b16 a[114:115], v98 offset:38400\n" \
"v_mfma_f32_16x16x32_bf16 v[44:47], a[92:95], a[20:23], v[44:47]\n" \
"buffer_load_dwordx4 v225, s[48:51], s77 offen offset:1024 lds\n" \
"v_mfma_f32_16x16x32_bf16 v[60:63], a[88:91], a[24:27], v[60:63]\n" \
"ds_read_b64_tr_b16 a[116:117], v98 offset:50688\n" \
"v_mfma_f32_16x16x32_bf16 v[60:63], a[92:95], a[28:31], v[60:63]\n" \
"v_mfma_f32_16x16x32_bf16 v[56:59], a[80:83], a[24:27], v[56:59]\n" \
"ds_read_b64_tr_b16 a[118:119], v98 offset:54784\n" \
"v_mfma_f32_16x16x32_bf16 v[56:59], a[84:87], a[28:31], v[56:59]\n" \
"buffer_load_dwordx4 v226, s[48:51], s77 offen offset:2048 lds\n" \
"v_mfma_f32_16x16x32_bf16 v[52:55], a[72:75], a[24:27], v[52:55]\n" \
"ds_read_b64_tr_b16 a[120:121], v99 offset:34304\n" \
"v_mfma_f32_16x16x32_bf16 v[52:55], a[76:79], a[28:31], v[52:55]\n" \
"v_mfma_f32_16x16x32_bf16 v[48:51], a[64:67], a[24:27], v[48:51]\n" \
"ds_read_b64_tr_b16 a[122:123], v99 offset:38400\n" \
"v_mfma_f32_16x16x32_bf16 v[48:51], a[68:71], a[28:31], v[48:51]\n" \
"buffer_load_dwordx4 v227, s[48:51], s77 offen offset:3072 lds\n" \
"v_mfma_f32_16x16x32_bf16 v[144:147], a[64:67], a[32:35], v[144:147]\n" \
"ds_read_b64_tr_b16 a[124:125], v99 offset:50688\n" \
"v_mfma_f32_16x16x32_bf16 v[144:147], a[68:71], a[36:39], v[144:147]\n" \
"v_mfma_f32_16x16x32_bf16 v[148:151], a[72:75], a[32:35], v[148:151]\n" \
"s_xor_b32 s77, s77, 0x400\n" \
"ds_read_b64_tr_b16 a[126:127], v99 offset:54784\n" \
"v_mfma_f32_16x16x32_bf16 v[148:151], a[76:79], a[36:39], v[148:151]\n" \
"v_mfma_f32_16x16x32_bf16 v[152:155], a[80:83], a[32:35], v[152:155]\n" \
"v_mfma_f32_16x16x32_bf16 v[152:155], a[84:87], a[36:39], v[152:155]\n" \
"v_mfma_f32_16x16x32_bf16 v[156:159], a[88:91], a[32:35], v[156:159]\n" \
"v_mfma_f32_16x16x32_bf16 v[156:159], a[92:95], a[36:39], v[156:159]\n" \
"v_mfma_f32_16x16x32_bf16 v[188:191], a[88:91], a[40:43], v[188:191]\n" \
"v_mfma_f32_16x16x32_bf16 v[188:191], a[92:95], a[44:47], v[188:191]\n" \
"v_mfma_f32_16x16x32_bf16 v[184:187], a[80:83], a[40:43], v[184:187]\n" \
"v_mfma_f32_16x16x32_bf16 v[184:187], a[84:87], a[44:47], v[184:187]\n" \
"v_mfma_f32_16x16x32_bf16 v[180:183], a[72:75], a[40:43], v[180:183]\n" \
"v_mfma_f32_16x16x32_bf16 v[180:183], a[76:79], a[44:47], v[180:183]\n" \
"v_mfma_f32_16x16x32_bf16 v[176:179], a[64:67], a[40:43], v[176:179]\n" \
"v_mfma_f32_16x16x32_bf16 v[176:179], a[68:71], a[44:47], v[176:179]\n" \
"s_waitcnt lgkmcnt(0)\n" \
"v_mfma_f32_16x16x32_bf16 v[192:195], a[64:67], a[48:51], v[192:195]\n" \
"v_mfma_f32_16x16x32_bf16 v[192:195], a[68:71], a[52:55], v[192:195]\n" \
"v_mfma_f32_16x16x32_bf16 v[196:199], a[72:75], a[48:51], v[196:199]\n" \
"v_mfma_f32_16x16x32_bf16 v[196:199], a[76:79], a[52:55], v[196:199]\n" \
"v_mfma_f32_16x16x32_bf16 v[200:203], a[80:83], a[48:51], v[200:203]\n" \
"v_mfma_f32_16x16x32_bf16 v[200:203], a[84:87], a[52:55], v[200:203]\n" \
"v_mfma_f32_16x16x32_bf16 v[204:207], a[88:91], a[48:51], v[204:207]\n" \
"v_mfma_f32_16x16x32_bf16 v[204:207], a[92:95], a[52:55], v[204:207]\n" \
"v_mfma_f32_16x16x32_bf16 v[220:223], a[88:91], a[56:59], v[220:223]\n" \
"v_mfma_f32_16x16x32_bf16 v[220:223], a[92:95], a[60:63], v[220:223]\n" \
"v_mfma_f32_16x16x32_bf16 v[216:219], a[80:83], a[56:59], v[216:219]\n" \
"v_mfma_f32_16x16x32_bf16 v[216:219], a[84:87], a[60:63], v[216:219]\n" \
"v_mfma_f32_16x16x32_bf16 v[212:215], a[72:75], a[56:59], v[212:215]\n" \
"v_mfma_f32_16x16x32_bf16 v[212:215], a[76:79], a[60:63], v[212:215]\n" \
"v_mfma_f32_16x16x32_bf16 v[208:211], a[64:67], a[56:59], v[208:211]\n" \
"v_mfma_f32_16x16x32_bf16 v[208:211], a[68:71], a[60:63], v[208:211]\n" \
"s_waitcnt vmcnt(10) lgkmcnt(0)\n" \
"s_barrier\n" \
"v_mfma_f32_16x16x32_bf16 a[128:131], a[0:3], a[96:99], a[128:131]\n" \
"s_add_u32 m0, s74, 98304\n" \
"v_mfma_f32_16x16x32_bf16 a[128:131], a[4:7], a[100:103], a[128:131]\n" \
"buffer_load_dwordx4 v224, s[48:51], s75 offen offset:0 lds\n" \
"v_mfma_f32_16x16x32_bf16 a[132:135], a[0:3], a[104:107], a[132:135]\n" \
"ds_read_b64_tr_b16 a[64:65], v232 offset:0\n" \
"ds_read_b64_tr_b16 a[66:67], v232 offset:2048\n" \
"v_mfma_f32_16x16x32_bf16 a[132:135], a[4:7], a[108:111], a[132:135]\n" \
"v_mfma_f32_16x16x32_bf16 a[136:139], a[0:3], a[112:115], a[136:139]\n" \
"v_mfma_f32_16x16x32_bf16 a[136:139], a[4:7], a[116:119], a[136:139]\n" \
"buffer_load_dwordx4 v225, s[48:51], s75 offen offset:1024 lds\n" \
"v_mfma_f32_16x16x32_bf16 a[140:143], a[0:3], a[120:123], a[140:143]\n" \
"ds_read_b64_tr_b16 a[68:69], v232 offset:8192\n" \
"ds_read_b64_tr_b16 a[70:71], v232 offset:10240\n" \
"v_mfma_f32_16x16x32_bf16 a[140:143], a[4:7], a[124:127], a[140:143]\n" \
"v_mfma_f32_16x16x32_bf16 a[156:159], a[8:11], a[120:123], a[156:159]\n" \
"v_mul_f32 v192, s76, v192\n" \
"v_mul_f32 v193, s76, v193\n" \
"v_mfma_f32_16x16x32_bf16 a[156:159], a[12:15], a[124:127], a[156:159]\n" \
"buffer_load_dwordx4 v226, s[48:51], s75 offen offset:2048 lds\n" \
"v_mfma_f32_16x16x32_bf16 a[152:155], a[8:11], a[112:115], a[152:155]\n" \
"ds_read_b64_tr_b16 a[72:73], v233 offset:0\n" \
"ds_read_b64_tr_b16 a[74:75], v233 offset:2048\n" \
"v_mfma_f32_16x16x32_bf16 a[152:155], a[12:15], a[116:119], a[152:155]\n" \
"v_mul_f32 v194, s76, v194\n" \
"v_mul_f32 v195, s76, v195\n" \
"v_mfma_f32_16x16x32_bf16 a[148:151], a[8:11], a[104:107], a[148:151]\n" \
"v_cvt_pk_bf16_f32 v192, v192, v193\n" \
"v_cvt_pk_bf16_f32 v193, v194, v195\n" \
"v_mfma_f32_16x16x32_bf16 a[148:151], a[12:15], a[108:111], a[148:151]\n" \
"buffer_load_dwordx4 v227, s[48:51], s75 offen offset:3072 lds\n" \
"v_mfma_f32_16x16x32_bf16 a[144:147], a[8:11], a[96:99], a[144:147]\n" \
"ds_read_b64_tr_b16 a[76:77], v233 offset:8192\n" \
"ds_read_b64_tr_b16 a[78:79], v233 offset:10240\n" \
"v_mfma_f32_16x16x32_bf16 a[144:147], a[12:15], a[100:103], a[144:147]\n" \
"s_xor_b32 s75, s75, 0x500\n" \
"v_mul_f32 v196, s76, v196\n" \
"v_mul_f32 v197, s76, v197\n" \
"v_mfma_f32_16x16x32_bf16 a[160:163], a[16:19], a[96:99], a[160:163]\n" \
"s_add_u32 m0, s74, 102400\n" \
"v_mul_f32 v198, s76, v198\n" \
"v_mul_f32 v199, s76, v199\n" \
"v_mfma_f32_16x16x32_bf16 a[160:163], a[20:23], a[100:103], a[160:163]\n" \
"buffer_load_dwordx4 v224, s[48:51], s77 offen offset:0 lds\n" \
"v_mfma_f32_16x16x32_bf16 a[164:167], a[16:19], a[104:107], a[164:167]\n" \
"ds_read_b64_tr_b16 a[80:81], v232 offset:512\n" \
"ds_read_b64_tr_b16 a[82:83], v232 offset:2560\n" \
"v_mfma_f32_16x16x32_bf16 a[164:167], a[20:23], a[108:111], a[164:167]\n" \
"v_cvt_pk_bf16_f32 v194, v196, v197\n" \
"v_cvt_pk_bf16_f32 v195, v198, v199\n" \
"v_mfma_f32_16x16x32_bf16 a[168:171], a[16:19], a[112:115], a[168:171]\n" \
"v_mul_f32 v208, s76, v208\n" \
"v_mul_f32 v209, s76, v209\n" \
"v_mfma_f32_16x16x32_bf16 a[168:171], a[20:23], a[116:119], a[168:171]\n" \
"buffer_load_dwordx4 v225, s[48:51], s77 offen offset:1024 lds\n" \
"v_mfma_f32_16x16x32_bf16 a[172:175], a[16:19], a[120:123], a[172:175]\n" \
"ds_read_b64_tr_b16 a[84:85], v232 offset:8704\n" \
"ds_read_b64_tr_b16 a[86:87], v232 offset:10752\n" \
"v_mfma_f32_16x16x32_bf16 a[172:175], a[20:23], a[124:127], a[172:175]\n" \
"v_mul_f32 v210, s76, v210\n" \
"v_mul_f32 v211, s76, v211\n" \
"v_mfma_f32_16x16x32_bf16 a[188:191], a[24:27], a[120:123], a[188:191]\n" \
"v_cvt_pk_bf16_f32 v208, v208, v209\n" \
"v_cvt_pk_bf16_f32 v209, v210, v211\n" \
"v_mfma_f32_16x16x32_bf16 a[188:191], a[28:31], a[124:127], a[188:191]\n" \
"buffer_load_dwordx4 v226, s[48:51], s77 offen offset:2048 lds\n" \
"v_mfma_f32_16x16x32_bf16 a[184:187], a[24:27], a[112:115], a[184:187]\n" \
"ds_read_b64_tr_b16 a[88:89], v233 offset:512\n" \
"ds_read_b64_tr_b16 a[90:91], v233 offset:2560\n" \
"v_mfma_f32_16x16x32_bf16 a[184:187], a[28:31], a[116:119], a[184:187]\n" \
"v_mul_f32 v212, s76, v212\n" \
"v_mul_f32 v213, s76, v213\n" \
"v_mfma_f32_16x16x32_bf16 a[180:183], a[24:27], a[104:107], a[180:183]\n" \
"v_mul_f32 v214, s76, v214\n" \
"v_mul_f32 v215, s76, v215\n" \
"v_mfma_f32_16x16x32_bf16 a[180:183], a[28:31], a[108:111], a[180:183]\n" \
"buffer_load_dwordx4 v227, s[48:51], s77 offen offset:3072 lds\n" \
"v_mfma_f32_16x16x32_bf16 a[176:179], a[24:27], a[96:99], a[176:179]\n" \
"ds_read_b64_tr_b16 a[92:93], v233 offset:8704\n" \
"ds_read_b64_tr_b16 a[94:95], v233 offset:10752\n" \
"v_mfma_f32_16x16x32_bf16 a[176:179], a[28:31], a[100:103], a[176:179]\n" \
"s_xor_b32 s77, s77, 0x500\n" \
"v_cvt_pk_bf16_f32 v210, v212, v213\n" \
"v_cvt_pk_bf16_f32 v211, v214, v215\n" \
"v_mfma_f32_16x16x32_bf16 a[192:195], a[32:35], a[96:99], a[192:195]\n" \
"s_add_u32 m0, s79, 32768\n" \
"v_mul_f32 v200, s76, v200\n" \
"v_mul_f32 v201, s76, v201\n" \
"v_mfma_f32_16x16x32_bf16 a[192:195], a[36:39], a[100:103], a[192:195]\n" \
"buffer_load_dwordx4 v228, s[52:55], s78 offen offset:0 lds\n" \
"v_mfma_f32_16x16x32_bf16 a[196:199], a[32:35], a[104:107], a[196:199]\n" \
"ds_read_b128 a[0:3], v250 offset:0\n" \
"ds_read_b128 a[4:7], v251 offset:0\n" \
"v_mfma_f32_16x16x32_bf16 a[196:199], a[36:39], a[108:111], a[196:199]\n" \
"v_mul_f32 v202, s76, v202\n" \
"v_mul_f32 v203, s76, v203\n" \
"v_mfma_f32_16x16x32_bf16 a[200:203], a[32:35], a[112:115], a[200:203]\n" \
"v_cvt_pk_bf16_f32 v200, v200, v201\n" \
"v_cvt_pk_bf16_f32 v201, v202, v203\n" \
"v_mfma_f32_16x16x32_bf16 a[200:203], a[36:39], a[116:119], a[200:203]\n" \
"buffer_load_dwordx4 v229, s[52:55], s78 offen offset:1024 lds\n" \
"v_mfma_f32_16x16x32_bf16 a[204:207], a[32:35], a[120:123], a[204:207]\n" \
"ds_read_b128 a[8:11], v250 offset:2048\n" \
"ds_read_b128 a[12:15], v251 offset:2048\n" \
"v_mfma_f32_16x16x32_bf16 a[204:207], a[36:39], a[124:127], a[204:207]\n" \
"v_mul_f32 v204, s76, v204\n" \
"v_mul_f32 v205, s76, v205\n" \
"v_mfma_f32_16x16x32_bf16 a[220:223], a[40:43], a[120:123], a[220:223]\n" \
"s_xor_b32 s78, s78, 0x500000\n" \
"v_mul_f32 v206, s76, v206\n" \
"v_mul_f32 v207, s76, v207\n" \
"v_mfma_f32_16x16x32_bf16 a[220:223], a[44:47], a[124:127], a[220:223]\n" \
"buffer_load_dwordx4 v228, s[52:55], s89 offen offset:2048 lds\n" \
"v_mfma_f32_16x16x32_bf16 a[216:219], a[40:43], a[112:115], a[216:219]\n" \
"ds_read_b128 a[16:19], v250 offset:4096\n" \
"ds_read_b128 a[20:23], v251 offset:4096\n" \
"v_mfma_f32_16x16x32_bf16 a[216:219], a[44:47], a[116:119], a[216:219]\n" \
"v_cvt_pk_bf16_f32 v202, v204, v205\n" \
"v_cvt_pk_bf16_f32 v203, v206, v207\n" \
"v_mfma_f32_16x16x32_bf16 a[212:215], a[40:43], a[104:107], a[212:215]\n" \
"v_mul_f32 v216, s76, v216\n" \
"v_mul_f32 v217, s76, v217\n" \
"v_mfma_f32_16x16x32_bf16 a[212:215], a[44:47], a[108:111], a[212:215]\n" \
"buffer_load_dwordx4 v229, s[52:55], s89 offen offset:3072 lds\n" \
"v_mfma_f32_16x16x32_bf16 a[208:211], a[40:43], a[96:99], a[208:211]\n" \
"v_mul_f32 v218, s76, v218\n" \
"v_mul_f32 v219, s76, v219\n" \
"v_mfma_f32_16x16x32_bf16 a[208:211], a[44:47], a[100:103], a[208:211]\n" \
"s_xor_b32 s89, s89, 0x500000\n" \
"v_cvt_pk_bf16_f32 v216, v216, v217\n" \
"v_cvt_pk_bf16_f32 v217, v218, v219\n" \
"v_mfma_f32_16x16x32_bf16 a[224:227], a[48:51], a[96:99], a[224:227]\n" \
"s_add_u32 m0, s79, 49152\n" \
"v_mul_f32 v220, s76, v220\n" \
"v_mul_f32 v221, s76, v221\n" \
"v_mfma_f32_16x16x32_bf16 a[224:227], a[52:55], a[100:103], a[224:227]\n" \
"buffer_load_dwordx4 v228, s[52:55], s78 offen offset:0 lds\n" \
"v_mfma_f32_16x16x32_bf16 a[228:231], a[48:51], a[104:107], a[228:231]\n" \
"v_mul_f32 v222, s76, v222\n" \
"v_mul_f32 v223, s76, v223\n" \
"v_mfma_f32_16x16x32_bf16 a[228:231], a[52:55], a[108:111], a[228:231]\n" \
"v_cvt_pk_bf16_f32 v218, v220, v221\n" \
"v_cvt_pk_bf16_f32 v219, v222, v223\n" \
"v_mfma_f32_16x16x32_bf16 a[232:235], a[48:51], a[112:115], a[232:235]\n" \
"v_mul_f32 v144, s76, v144\n" \
"v_mul_f32 v145, s76, v145\n" \
"v_mfma_f32_16x16x32_bf16 a[232:235], a[52:55], a[116:119], a[232:235]\n" \
"buffer_load_dwordx4 v229, s[52:55], s78 offen offset:1024 lds\n" \
"v_mfma_f32_16x16x32_bf16 a[236:239], a[48:51], a[120:123], a[236:239]\n" \
"v_mul_f32 v146, s76, v146\n" \
"v_mul_f32 v147, s76, v147\n" \
"v_mfma_f32_16x16x32_bf16 a[236:239], a[52:55], a[124:127], a[236:239]\n" \
"v_cvt_pk_bf16_f32 v196, v144, v145\n" \
"v_cvt_pk_bf16_f32 v197, v146, v147\n" \
"v_mfma_f32_16x16x32_bf16 a[252:255], a[56:59], a[120:123], a[252:255]\n" \
"s_xor_b32 s78, s78, 0x180000\n" \
"v_mul_f32 v148, s76, v148\n" \
"v_mul_f32 v149, s76, v149\n" \
"v_mfma_f32_16x16x32_bf16 a[252:255], a[60:63], a[124:127], a[252:255]\n" \
"buffer_load_dwordx4 v228, s[52:55], s89 offen offset:2048 lds\n" \
"v_mfma_f32_16x16x32_bf16 a[248:251], a[56:59], a[112:115], a[248:251]\n" \
"v_mul_f32 v150, s76, v150\n" \
"v_mul_f32 v151, s76, v151\n" \
"v_mfma_f32_16x16x32_bf16 a[248:251], a[60:63], a[116:119], a[248:251]\n" \
"v_cvt_pk_bf16_f32 v198, v148, v149\n" \
"v_cvt_pk_bf16_f32 v199, v150, v151\n" \
"v_mfma_f32_16x16x32_bf16 a[244:247], a[56:59], a[104:107], a[244:247]\n" \
"v_mul_f32 v176, s76, v176\n" \
"v_mul_f32 v177, s76, v177\n" \
"v_mfma_f32_16x16x32_bf16 a[244:247], a[60:63], a[108:111], a[244:247]\n" \
"buffer_load_dwordx4 v229, s[52:55], s89 offen offset:3072 lds\n" \
"v_mfma_f32_16x16x32_bf16 a[240:243], a[56:59], a[96:99], a[240:243]\n" \
"s_waitcnt lgkmcnt(0)\n" \
"v_mul_f32 v178, s76, v178\n" \
"v_mul_f32 v179, s76, v179\n" \
"v_mfma_f32_16x16x32_bf16 a[240:243], a[60:63], a[100:103], a[240:243]\n" \
"s_xor_b32 s89, s89, 0x180000\n" \
"v_cvt_pk_bf16_f32 v212, v176, v177\n" \
"v_cvt_pk_bf16_f32 v213, v178, v179\n" \
"v_mfma_f32_16x16x32_bf16 v[64:67], a[64:67], a[0:3], 0\n" \
"ds_read_b128 a[24:27], v250 offset:6144\n" \
"v_mul_f32 v180, s76, v180\n" \
"v_mfma_f32_16x16x32_bf16 v[64:67], a[68:71], a[4:7], v[64:67]\n" \
"s_add_u32 m0, s74, 131072\n" \
"ds_read_b128 a[28:31], v251 offset:6144\n" \
"v_mul_f32 v181, s76, v181\n" \
"v_mfma_f32_16x16x32_bf16 v[68:71], a[72:75], a[0:3], 0\n" \
"ds_read_b64_tr_b16 a[96:97], v232 offset:1024\n" \
"v_mul_f32 v182, s76, v182\n" \
"v_mfma_f32_16x16x32_bf16 v[68:71], a[76:79], a[4:7], v[68:71]\n" \
"buffer_load_dwordx4 v224, s[48:51], s75 offen offset:0 lds\n" \
"v_mfma_f32_16x16x32_bf16 v[72:75], a[80:83], a[0:3], 0\n" \
"ds_read_b64_tr_b16 a[98:99], v232 offset:3072\n" \
"v_mul_f32 v183, s76, v183\n" \
"v_mfma_f32_16x16x32_bf16 v[72:75], a[84:87], a[4:7], v[72:75]\n" \
"ds_read_b64_tr_b16 a[100:101], v232 offset:9216\n" \
"v_cvt_pk_bf16_f32 v214, v180, v181\n" \
"v_mfma_f32_16x16x32_bf16 v[76:79], a[88:91], a[0:3], 0\n" \
"ds_read_b64_tr_b16 a[102:103], v232 offset:11264\n" \
"v_cvt_pk_bf16_f32 v215, v182, v183\n" \
"v_mfma_f32_16x16x32_bf16 v[76:79], a[92:95], a[4:7], v[76:79]\n" \
"buffer_load_dwordx4 v225, s[48:51], s75 offen offset:1024 lds\n" \
"v_mfma_f32_16x16x32_bf16 v[108:111], a[88:91], a[8:11], 0\n" \
"ds_read_b64_tr_b16 a[104:105], v233 offset:1024\n" \
"v_mul_f32 v152, s76, v152\n" \
"v_mfma_f32_16x16x32_bf16 v[108:111], a[92:95], a[12:15], v[108:111]\n" \
"ds_read_b64_tr_b16 a[106:107], v233 offset:3072\n" \
"v_mul_f32 v153, s76, v153\n" \
"v_mfma_f32_16x16x32_bf16 v[104:107], a[80:83], a[8:11], 0\n" \
"ds_read_b64_tr_b16 a[108:109], v233 offset:9216\n" \
"v_mul_f32 v154, s76, v154\n" \
"v_mfma_f32_16x16x32_bf16 v[104:107], a[84:87], a[12:15], v[104:107]\n" \
"buffer_load_dwordx4 v226, s[48:51], s75 offen offset:2048 lds\n" \
"v_mfma_f32_16x16x32_bf16 v[100:103], a[72:75], a[8:11], 0\n" \
"ds_read_b64_tr_b16 a[110:111], v233 offset:11264\n" \
"v_mul_f32 v155, s76, v155\n" \
"v_mfma_f32_16x16x32_bf16 v[100:103], a[76:79], a[12:15], v[100:103]\n" \
"ds_read_b64_tr_b16 a[112:113], v232 offset:1536\n" \
"v_cvt_pk_bf16_f32 v204, v152, v153\n" \
"v_mfma_f32_16x16x32_bf16 v[96:99], a[64:67], a[8:11], 0\n" \
"ds_read_b64_tr_b16 a[114:115], v232 offset:3584\n" \
"v_cvt_pk_bf16_f32 v205, v154, v155\n" \
"v_mfma_f32_16x16x32_bf16 v[96:99], a[68:71], a[12:15], v[96:99]\n" \
"buffer_load_dwordx4 v227, s[48:51], s75 offen offset:3072 lds\n" \
"v_mfma_f32_16x16x32_bf16 v[128:131], a[64:67], a[16:19], 0\n" \
"s_xor_b32 s75, s75, 0x180\n" \
"ds_read_b64_tr_b16 a[116:117], v232 offset:9728\n" \
"v_mul_f32 v156, s76, v156\n" \
"Lpg2215_%=:\n" \
"Lst0x3_%=:\n" \
"s_cbranch_execz Lpg4453_%=\n" \
"v_mfma_f32_16x16x32_bf16 v[128:131], a[68:71], a[20:23], v[128:131]\n" \
"s_add_u32 m0, s74, 135168\n" \
"ds_read_b64_tr_b16 a[118:119], v232 offset:11776\n" \
"v_mul_f32 v157, s76, v157\n" \
"v_mfma_f32_16x16x32_bf16 v[132:135], a[72:75], a[16:19], 0\n" \
"ds_read_b64_tr_b16 a[120:121], v233 offset:1536\n" \
"v_mul_f32 v158, s76, v158\n" \
"v_mfma_f32_16x16x32_bf16 v[132:135], a[76:79], a[20:23], v[132:135]\n" \
"buffer_load_dwordx4 v224, s[48:51], s77 offen offset:0 lds\n" \
"v_mfma_f32_16x16x32_bf16 v[136:139], a[80:83], a[16:19], 0\n" \
"ds_read_b64_tr_b16 a[122:123], v233 offset:3584\n" \
"v_mul_f32 v159, s76, v159\n" \
"v_mfma_f32_16x16x32_bf16 v[136:139], a[84:87], a[20:23], v[136:139]\n" \
"ds_read_b64_tr_b16 a[124:125], v233 offset:9728\n" \
"v_cvt_pk_bf16_f32 v206, v156, v157\n" \
"v_mfma_f32_16x16x32_bf16 v[140:143], a[88:91], a[16:19], 0\n" \
"s_waitcnt lgkmcnt(3)\n" \
"ds_read_b64_tr_b16 a[126:127], v233 offset:11776\n" \
"v_cvt_pk_bf16_f32 v207, v158, v159\n" \
"v_mfma_f32_16x16x32_bf16 v[140:143], a[92:95], a[20:23], v[140:143]\n" \
"buffer_load_dwordx4 v225, s[48:51], s77 offen offset:1024 lds\n" \
"v_mfma_f32_16x16x32_bf16 v[172:175], a[88:91], a[24:27], 0\n" \
"s_add_u32 s93, s87, 8192\n" \
"v_mul_f32 v184, s76, v184\n" \
"v_mul_f32 v185, s76, v185\n" \
"v_mfma_f32_16x16x32_bf16 v[172:175], a[92:95], a[28:31], v[172:175]\n" \
"buffer_store_dwordx4 v[192:195], v236, s[56:59], s93 offen offset:0 sc1\n" \
"v_mfma_f32_16x16x32_bf16 v[168:171], a[80:83], a[24:27], 0\n" \
"v_mul_f32 v186, s76, v186\n" \
"v_mul_f32 v187, s76, v187\n" \
"v_mfma_f32_16x16x32_bf16 v[168:171], a[84:87], a[28:31], v[168:171]\n" \
"buffer_load_dwordx4 v226, s[48:51], s77 offen offset:2048 lds\n" \
"v_mfma_f32_16x16x32_bf16 v[164:167], a[72:75], a[24:27], 0\n" \
"v_cvt_pk_bf16_f32 v220, v184, v185\n" \
"v_cvt_pk_bf16_f32 v221, v186, v187\n" \
"v_mfma_f32_16x16x32_bf16 v[164:167], a[76:79], a[28:31], v[164:167]\n" \
"buffer_load_dwordx4 v227, s[48:51], s77 offen offset:3072 lds\n" \
"v_mfma_f32_16x16x32_bf16 v[160:163], a[64:67], a[24:27], 0\n" \
"v_mul_f32 v188, s76, v188\n" \
"v_mul_f32 v189, s76, v189\n" \
"v_mfma_f32_16x16x32_bf16 v[160:163], a[68:71], a[28:31], v[160:163]\n" \
"s_waitcnt vmcnt(43) lgkmcnt(0)\n" \
"s_barrier\n" \
"v_mfma_f32_16x16x32_bf16 v[80:83], a[96:99], a[0:3], 0\n" \
"s_xor_b32 s77, s77, 0x180\n" \
"ds_read_b64_tr_b16 a[64:65], v232 offset:16384\n" \
"v_mul_f32 v190, s76, v190\n" \
"v_mfma_f32_16x16x32_bf16 v[80:83], a[100:103], a[4:7], v[80:83]\n" \
"ds_read_b64_tr_b16 a[66:67], v232 offset:18432\n" \
"v_mul_f32 v191, s76, v191\n" \
"v_mfma_f32_16x16x32_bf16 v[84:87], a[104:107], a[0:3], 0\n" \
"s_add_u32 m0, s79, 0\n" \
"ds_read_b64_tr_b16 a[68:69], v232 offset:24576\n" \
"v_cvt_pk_bf16_f32 v222, v188, v189\n" \
"v_mfma_f32_16x16x32_bf16 v[84:87], a[108:111], a[4:7], v[84:87]\n" \
"buffer_load_dwordx4 v228, s[52:55], s78 offen offset:0 lds\n" \
"v_mfma_f32_16x16x32_bf16 v[88:91], a[112:115], a[0:3], 0\n" \
"ds_read_b64_tr_b16 a[70:71], v232 offset:26624\n" \
"v_cvt_pk_bf16_f32 v223, v190, v191\n" \
"v_mfma_f32_16x16x32_bf16 v[88:91], a[116:119], a[4:7], v[88:91]\n" \
"ds_read_b64_tr_b16 a[72:73], v233 offset:16384\n" \
"v_accvgpr_read_b32 v243, a224\n" \
"v_mfma_f32_16x16x32_bf16 v[92:95], a[120:123], a[0:3], 0\n" \
"ds_read_b64_tr_b16 a[74:75], v233 offset:18432\n" \
"v_accvgpr_read_b32 v252, a225\n" \
"v_mfma_f32_16x16x32_bf16 v[92:95], a[124:127], a[4:7], v[92:95]\n" \
"buffer_load_dwordx4 v229, s[52:55], s78 offen offset:1024 lds\n" \
"v_mfma_f32_16x16x32_bf16 v[124:127], a[120:123], a[8:11], 0\n" \
"s_waitcnt vmcnt(33)\n" \
"ds_read_b128 a[0:3], v230 offset:0\n" \
"v_accvgpr_read_b32 v253, a226\n" \
"v_mfma_f32_16x16x32_bf16 v[124:127], a[124:127], a[12:15], v[124:127]\n" \
"ds_read_b128 a[4:7], v231 offset:0\n" \
"v_accvgpr_read_b32 v254, a227\n" \
"v_mfma_f32_16x16x32_bf16 v[120:123], a[112:115], a[8:11], 0\n" \
"s_xor_b32 s78, s78, 0x300000\n" \
"ds_read_b64_tr_b16 a[76:77], v233 offset:24576\n" \
"v_mul_f32 v243, s31, v243\n" \
"v_mfma_f32_16x16x32_bf16 v[120:123], a[116:119], a[12:15], v[120:123]\n" \
"buffer_load_dwordx4 v228, s[52:55], s89 offen offset:2048 lds\n" \
"v_mfma_f32_16x16x32_bf16 v[116:119], a[104:107], a[8:11], 0\n" \
"ds_read_b64_tr_b16 a[78:79], v233 offset:26624\n" \
"v_mul_f32 v252, s31, v252\n" \
"v_mfma_f32_16x16x32_bf16 v[116:119], a[108:111], a[12:15], v[116:119]\n" \
"ds_read_b64_tr_b16 a[80:81], v232 offset:16896\n" \
"v_mul_f32 v253, s31, v253\n" \
"v_mfma_f32_16x16x32_bf16 v[112:115], a[96:99], a[8:11], 0\n" \
"ds_read_b64_tr_b16 a[82:83], v232 offset:18944\n" \
"v_mul_f32 v254, s31, v254\n" \
"v_mfma_f32_16x16x32_bf16 v[112:115], a[100:103], a[12:15], v[112:115]\n" \
"buffer_load_dwordx4 v229, s[52:55], s89 offen offset:3072 lds\n" \
"v_mfma_f32_16x16x32_bf16 v[144:147], a[96:99], a[16:19], 0\n" \
"s_waitcnt vmcnt(33)\n" \
"s_xor_b32 s89, s89, 0x300000\n" \
"ds_read_b128 a[8:11], v230 offset:2048\n" \
"v_mfma_f32_16x16x32_bf16 v[144:147], a[100:103], a[20:23], v[144:147]\n" \
"s_add_u32 m0, s74, 65536\n" \
"ds_read_b128 a[12:15], v231 offset:2048\n" \
"v_cvt_pk_bf16_f32 v243, v243, v252\n" \
"v_mfma_f32_16x16x32_bf16 v[148:151], a[104:107], a[16:19], 0\n" \
"s_waitcnt lgkmcnt(6)\n" \
"ds_read_b64_tr_b16 a[84:85], v232 offset:25088\n" \
"v_cvt_pk_bf16_f32 v253, v253, v254\n" \
"v_mfma_f32_16x16x32_bf16 v[148:151], a[108:111], a[20:23], v[148:151]\n" \
"buffer_load_dwordx4 v224, s[48:51], s75 offen offset:0 lds\n" \
"v_mfma_f32_16x16x32_bf16 v[152:155], a[112:115], a[16:19], 0\n" \
"ds_read_b64_tr_b16 a[86:87], v232 offset:27136\n" \
"v_accvgpr_write_b32 a224, v243\n" \
"v_mfma_f32_16x16x32_bf16 v[152:155], a[116:119], a[20:23], v[152:155]\n" \
"ds_read_b64_tr_b16 a[88:89], v233 offset:16896\n" \
"v_accvgpr_write_b32 a225, v253\n" \
"v_mfma_f32_16x16x32_bf16 v[156:159], a[120:123], a[16:19], 0\n" \
"ds_read_b64_tr_b16 a[90:91], v233 offset:18944\n" \
"v_accvgpr_read_b32 v243, a240\n" \
"v_mfma_f32_16x16x32_bf16 v[156:159], a[124:127], a[20:23], v[156:159]\n" \
"buffer_load_dwordx4 v225, s[48:51], s75 offen offset:1024 lds\n" \
"v_mfma_f32_16x16x32_bf16 v[188:191], a[120:123], a[24:27], 0\n" \
"s_waitcnt vmcnt(33)\n" \
"ds_read_b128 a[16:19], v230 offset:4096\n" \
"v_accvgpr_read_b32 v252, a241\n" \
"v_mfma_f32_16x16x32_bf16 v[188:191], a[124:127], a[28:31], v[188:191]\n" \
"ds_read_b128 a[20:23], v231 offset:4096\n" \
"v_accvgpr_read_b32 v253, a242\n" \
"v_mfma_f32_16x16x32_bf16 v[184:187], a[112:115], a[24:27], 0\n" \
"s_waitcnt lgkmcnt(6)\n" \
"ds_read_b64_tr_b16 a[92:93], v233 offset:25088\n" \
"v_accvgpr_read_b32 v254, a243\n" \
"v_mfma_f32_16x16x32_bf16 v[184:187], a[116:119], a[28:31], v[184:187]\n" \
"buffer_load_dwordx4 v226, s[48:51], s75 offen offset:2048 lds\n" \
"v_mfma_f32_16x16x32_bf16 v[180:183], a[104:107], a[24:27], 0\n" \
"ds_read_b64_tr_b16 a[94:95], v233 offset:27136\n" \
"v_mul_f32 v243, s31, v243\n" \
"v_mfma_f32_16x16x32_bf16 v[180:183], a[108:111], a[28:31], v[180:183]\n" \
"v_mul_f32 v252, s31, v252\n" \
"v_mul_f32 v253, s31, v253\n" \
"v_mfma_f32_16x16x32_bf16 v[176:179], a[96:99], a[24:27], 0\n" \
"v_mul_f32 v254, s31, v254\n" \
"v_cvt_pk_bf16_f32 v243, v243, v252\n" \
"v_mfma_f32_16x16x32_bf16 v[176:179], a[100:103], a[28:31], v[176:179]\n" \
"buffer_load_dwordx4 v227, s[48:51], s75 offen offset:3072 lds\n" \
"v_mfma_f32_16x16x32_bf16 v[64:67], a[64:67], a[0:3], v[64:67]\n" \
"s_waitcnt vmcnt(33)\n" \
"s_xor_b32 s75, s75, 0x300\n" \
"ds_read_b128 a[24:27], v230 offset:6144\n" \
"v_mfma_f32_16x16x32_bf16 v[64:67], a[68:71], a[4:7], v[64:67]\n" \
"s_add_u32 m0, s74, 69632\n" \
"ds_read_b128 a[28:31], v231 offset:6144\n" \
"v_cvt_pk_bf16_f32 v253, v253, v254\n" \
"v_mfma_f32_16x16x32_bf16 v[68:71], a[72:75], a[0:3], v[68:71]\n" \
"s_waitcnt lgkmcnt(4)\n" \
"ds_read_b64_tr_b16 a[96:97], v232 offset:17408\n" \
"v_accvgpr_write_b32 a226, v243\n" \
"v_mfma_f32_16x16x32_bf16 v[68:71], a[76:79], a[4:7], v[68:71]\n" \
"buffer_load_dwordx4 v224, s[48:51], s77 offen offset:0 lds\n" \
"v_mfma_f32_16x16x32_bf16 v[72:75], a[80:83], a[0:3], v[72:75]\n" \
"ds_read_b64_tr_b16 a[98:99], v232 offset:19456\n" \
"v_accvgpr_write_b32 a227, v253\n" \
"v_mfma_f32_16x16x32_bf16 v[72:75], a[84:87], a[4:7], v[72:75]\n" \
"s_waitcnt lgkmcnt(3)\n" \
"ds_read_b64_tr_b16 a[100:101], v232 offset:25600\n" \
"v_accvgpr_read_b32 v243, a228\n" \
"v_mfma_f32_16x16x32_bf16 v[76:79], a[88:91], a[0:3], v[76:79]\n" \
"ds_read_b64_tr_b16 a[102:103], v232 offset:27648\n" \
"v_accvgpr_read_b32 v252, a229\n" \
"v_mfma_f32_16x16x32_bf16 v[76:79], a[92:95], a[4:7], v[76:79]\n" \
"buffer_load_dwordx4 v225, s[48:51], s77 offen offset:1024 lds\n" \
"v_mfma_f32_16x16x32_bf16 v[108:111], a[88:91], a[8:11], v[108:111]\n" \
"ds_read_b64_tr_b16 a[104:105], v233 offset:17408\n" \
"v_accvgpr_read_b32 v253, a230\n" \
"v_mfma_f32_16x16x32_bf16 v[108:111], a[92:95], a[12:15], v[108:111]\n" \
"ds_read_b64_tr_b16 a[106:107], v233 offset:19456\n" \
"v_accvgpr_read_b32 v254, a231\n" \
"v_mfma_f32_16x16x32_bf16 v[104:107], a[80:83], a[8:11], v[104:107]\n" \
"s_waitcnt lgkmcnt(6)\n" \
"ds_read_b64_tr_b16 a[108:109], v233 offset:25600\n" \
"v_mul_f32 v243, s31, v243\n" \
"v_mfma_f32_16x16x32_bf16 v[104:107], a[84:87], a[12:15], v[104:107]\n" \
"buffer_load_dwordx4 v226, s[48:51], s77 offen offset:2048 lds\n" \
"v_mfma_f32_16x16x32_bf16 v[100:103], a[72:75], a[8:11], v[100:103]\n" \
"ds_read_b64_tr_b16 a[110:111], v233 offset:27648\n" \
"v_mul_f32 v252, s31, v252\n" \
"v_mfma_f32_16x16x32_bf16 v[100:103], a[76:79], a[12:15], v[100:103]\n" \
"ds_read_b64_tr_b16 a[112:113], v232 offset:17920\n" \
"v_mul_f32 v253, s31, v253\n" \
"v_mfma_f32_16x16x32_bf16 v[96:99], a[64:67], a[8:11], v[96:99]\n" \
"ds_read_b64_tr_b16 a[114:115], v232 offset:19968\n" \
"v_mul_f32 v254, s31, v254\n" \
"v_mfma_f32_16x16x32_bf16 v[96:99], a[68:71], a[12:15], v[96:99]\n" \
"buffer_load_dwordx4 v227, s[48:51], s77 offen offset:3072 lds\n" \
"v_mfma_f32_16x16x32_bf16 v[128:131], a[64:67], a[16:19], v[128:131]\n" \
"ds_read_b64_tr_b16 a[116:117], v232 offset:26112\n" \
"v_cvt_pk_bf16_f32 v243, v243, v252\n" \
"v_mfma_f32_16x16x32_bf16 v[128:131], a[68:71], a[20:23], v[128:131]\n" \
"ds_read_b64_tr_b16 a[118:119], v232 offset:28160\n" \
"v_cvt_pk_bf16_f32 v253, v253, v254\n" \
"v_mfma_f32_16x16x32_bf16 v[132:135], a[72:75], a[16:19], v[132:135]\n" \
"s_xor_b32 s77, s77, 0x300\n" \
"ds_read_b64_tr_b16 a[120:121], v233 offset:17920\n" \
"v_accvgpr_write_b32 a228, v243\n" \
"v_mfma_f32_16x16x32_bf16 v[132:135], a[76:79], a[20:23], v[132:135]\n" \
"v_accvgpr_write_b32 a229, v253\n" \
"v_accvgpr_read_b32 v243, a244\n" \
"v_mfma_f32_16x16x32_bf16 v[136:139], a[80:83], a[16:19], v[136:139]\n" \
"ds_read_b64_tr_b16 a[122:123], v233 offset:19968\n" \
"v_accvgpr_read_b32 v252, a245\n" \
"v_mfma_f32_16x16x32_bf16 v[136:139], a[84:87], a[20:23], v[136:139]\n" \
"ds_read_b64_tr_b16 a[124:125], v233 offset:26112\n" \
"v_accvgpr_read_b32 v253, a246\n" \
"v_mfma_f32_16x16x32_bf16 v[140:143], a[88:91], a[16:19], v[140:143]\n" \
"ds_read_b64_tr_b16 a[126:127], v233 offset:28160\n" \
"v_accvgpr_read_b32 v254, a247\n" \
"v_mfma_f32_16x16x32_bf16 v[140:143], a[92:95], a[20:23], v[140:143]\n" \
"buffer_store_dwordx4 v[208:211], v236, s[56:59], s93 offen offset:256 sc1\n" \
"v_mfma_f32_16x16x32_bf16 v[172:175], a[88:91], a[24:27], v[172:175]\n" \
"v_mul_f32 v243, s31, v243\n" \
"v_mul_f32 v252, s31, v252\n" \
"v_mfma_f32_16x16x32_bf16 v[172:175], a[92:95], a[28:31], v[172:175]\n" \
"v_mul_f32 v253, s31, v253\n" \
"v_mul_f32 v254, s31, v254\n" \
"v_mfma_f32_16x16x32_bf16 v[168:171], a[80:83], a[24:27], v[168:171]\n" \
"v_cvt_pk_bf16_f32 v243, v243, v252\n" \
"v_cvt_pk_bf16_f32 v253, v253, v254\n" \
"v_mfma_f32_16x16x32_bf16 v[168:171], a[84:87], a[28:31], v[168:171]\n" \
"v_accvgpr_write_b32 a230, v243\n" \
"v_accvgpr_write_b32 a231, v253\n" \
"v_mfma_f32_16x16x32_bf16 v[164:167], a[72:75], a[24:27], v[164:167]\n" \
"v_accvgpr_read_b32 v243, a232\n" \
"v_accvgpr_read_b32 v252, a233\n" \
"v_mfma_f32_16x16x32_bf16 v[164:167], a[76:79], a[28:31], v[164:167]\n" \
"v_accvgpr_read_b32 v253, a234\n" \
"v_accvgpr_read_b32 v254, a235\n" \
"v_mfma_f32_16x16x32_bf16 v[160:163], a[64:67], a[24:27], v[160:163]\n" \
"v_mul_f32 v243, s31, v243\n" \
"v_mul_f32 v252, s31, v252\n" \
"v_mfma_f32_16x16x32_bf16 v[160:163], a[68:71], a[28:31], v[160:163]\n" \
"s_waitcnt vmcnt(26) lgkmcnt(0)\n" \
"s_barrier\n" \
"v_mfma_f32_16x16x32_bf16 v[80:83], a[96:99], a[0:3], v[80:83]\n" \
"ds_read_b64_tr_b16 a[64:65], v232 offset:32768\n" \
"v_mul_f32 v253, s31, v253\n" \
"v_mfma_f32_16x16x32_bf16 v[80:83], a[100:103], a[4:7], v[80:83]\n" \
"ds_read_b64_tr_b16 a[66:67], v232 offset:34816\n" \
"v_mul_f32 v254, s31, v254\n" \
"v_mfma_f32_16x16x32_bf16 v[84:87], a[104:107], a[0:3], v[84:87]\n" \
"s_add_u32 m0, s79, 16384\n" \
"ds_read_b64_tr_b16 a[68:69], v232 offset:40960\n" \
"v_cvt_pk_bf16_f32 v243, v243, v252\n" \
"v_mfma_f32_16x16x32_bf16 v[84:87], a[108:111], a[4:7], v[84:87]\n" \
"buffer_load_dwordx4 v228, s[52:55], s78 offen offset:0 lds\n" \
"v_mfma_f32_16x16x32_bf16 v[88:91], a[112:115], a[0:3], v[88:91]\n" \
"ds_read_b64_tr_b16 a[70:71], v232 offset:43008\n" \
"v_cvt_pk_bf16_f32 v253, v253, v254\n" \
"v_mfma_f32_16x16x32_bf16 v[88:91], a[116:119], a[4:7], v[88:91]\n" \
"ds_read_b64_tr_b16 a[72:73], v233 offset:32768\n" \
"v_accvgpr_write_b32 a232, v243\n" \
"v_mfma_f32_16x16x32_bf16 v[92:95], a[120:123], a[0:3], v[92:95]\n" \
"ds_read_b64_tr_b16 a[74:75], v233 offset:34816\n" \
"v_accvgpr_write_b32 a233, v253\n" \
"v_mfma_f32_16x16x32_bf16 v[92:95], a[124:127], a[4:7], v[92:95]\n" \
"buffer_load_dwordx4 v229, s[52:55], s78 offen offset:1024 lds\n" \
"v_mfma_f32_16x16x32_bf16 v[124:127], a[120:123], a[8:11], v[124:127]\n" \
"ds_read_b128 a[0:3], v230 offset:32768\n" \
"v_accvgpr_read_b32 v243, a248\n" \
"v_mfma_f32_16x16x32_bf16 v[124:127], a[124:127], a[12:15], v[124:127]\n" \
"ds_read_b128 a[4:7], v231 offset:32768\n" \
"v_accvgpr_read_b32 v252, a249\n" \
"v_mfma_f32_16x16x32_bf16 v[120:123], a[112:115], a[8:11], v[120:123]\n" \
"s_xor_b32 s78, s78, 0x80000\n" \
"ds_read_b64_tr_b16 a[76:77], v233 offset:40960\n" \
"v_accvgpr_read_b32 v253, a250\n" \
"v_mfma_f32_16x16x32_bf16 v[120:123], a[116:119], a[12:15], v[120:123]\n" \
"buffer_load_dwordx4 v228, s[52:55], s89 offen offset:2048 lds\n" \
"v_mfma_f32_16x16x32_bf16 v[116:119], a[104:107], a[8:11], v[116:119]\n" \
"ds_read_b64_tr_b16 a[78:79], v233 offset:43008\n" \
"v_accvgpr_read_b32 v254, a251\n" \
"v_mfma_f32_16x16x32_bf16 v[116:119], a[108:111], a[12:15], v[116:119]\n" \
"ds_read_b64_tr_b16 a[80:81], v232 offset:33280\n" \
"v_mul_f32 v243, s31, v243\n" \
"v_mfma_f32_16x16x32_bf16 v[112:115], a[96:99], a[8:11], v[112:115]\n" \
"ds_read_b64_tr_b16 a[82:83], v232 offset:35328\n" \
"v_mul_f32 v252, s31, v252\n" \
"v_mfma_f32_16x16x32_bf16 v[112:115], a[100:103], a[12:15], v[112:115]\n" \
"buffer_load_dwordx4 v229, s[52:55], s89 offen offset:3072 lds\n" \
"v_mfma_f32_16x16x32_bf16 v[144:147], a[96:99], a[16:19], v[144:147]\n" \
"s_xor_b32 s89, s89, 0x80000\n" \
"ds_read_b128 a[8:11], v230 offset:34816\n" \
"v_mfma_f32_16x16x32_bf16 v[144:147], a[100:103], a[20:23], v[144:147]\n" \
"s_add_u32 m0, s74, 98304\n" \
"ds_read_b128 a[12:15], v231 offset:34816\n" \
"v_mul_f32 v253, s31, v253\n" \
"v_mfma_f32_16x16x32_bf16 v[148:151], a[104:107], a[16:19], v[148:151]\n" \
"s_waitcnt lgkmcnt(6)\n" \
"ds_read_b64_tr_b16 a[84:85], v232 offset:41472\n" \
"v_mul_f32 v254, s31, v254\n" \
"v_mfma_f32_16x16x32_bf16 v[148:151], a[108:111], a[20:23], v[148:151]\n" \
"buffer_load_dwordx4 v224, s[48:51], s75 offen offset:0 lds\n" \
"v_mfma_f32_16x16x32_bf16 v[152:155], a[112:115], a[16:19], v[152:155]\n" \
"ds_read_b64_tr_b16 a[86:87], v232 offset:43520\n" \
"v_cvt_pk_bf16_f32 v243, v243, v252\n" \
"v_mfma_f32_16x16x32_bf16 v[152:155], a[116:119], a[20:23], v[152:155]\n" \
"ds_read_b64_tr_b16 a[88:89], v233 offset:33280\n" \
"v_cvt_pk_bf16_f32 v253, v253, v254\n" \
"v_mfma_f32_16x16x32_bf16 v[156:159], a[120:123], a[16:19], v[156:159]\n" \
"ds_read_b64_tr_b16 a[90:91], v233 offset:35328\n" \
"v_accvgpr_write_b32 a234, v243\n" \
"v_mfma_f32_16x16x32_bf16 v[156:159], a[124:127], a[20:23], v[156:159]\n" \
"buffer_load_dwordx4 v225, s[48:51], s75 offen offset:1024 lds\n" \
"v_mfma_f32_16x16x32_bf16 v[188:191], a[120:123], a[24:27], v[188:191]\n" \
"ds_read_b128 a[16:19], v230 offset:36864\n" \
"v_accvgpr_write_b32 a235, v253\n" \
"v_mfma_f32_16x16x32_bf16 v[188:191], a[124:127], a[28:31], v[188:191]\n" \
"ds_read_b128 a[20:23], v231 offset:36864\n" \
"v_accvgpr_read_b32 v243, a236\n" \
"v_mfma_f32_16x16x32_bf16 v[184:187], a[112:115], a[24:27], v[184:187]\n" \
"s_waitcnt lgkmcnt(6)\n" \
"ds_read_b64_tr_b16 a[92:93], v233 offset:41472\n" \
"v_accvgpr_read_b32 v252, a237\n" \
"v_mfma_f32_16x16x32_bf16 v[184:187], a[116:119], a[28:31], v[184:187]\n" \
"buffer_load_dwordx4 v226, s[48:51], s75 offen offset:2048 lds\n" \
"v_mfma_f32_16x16x32_bf16 v[180:183], a[104:107], a[24:27], v[180:183]\n" \
"ds_read_b64_tr_b16 a[94:95], v233 offset:43520\n" \
"v_accvgpr_read_b32 v253, a238\n" \
"v_mfma_f32_16x16x32_bf16 v[180:183], a[108:111], a[28:31], v[180:183]\n" \
"v_accvgpr_read_b32 v254, a239\n" \
"v_mul_f32 v243, s31, v243\n" \
"v_mfma_f32_16x16x32_bf16 v[176:179], a[96:99], a[24:27], v[176:179]\n" \
"v_mul_f32 v252, s31, v252\n" \
"v_mul_f32 v253, s31, v253\n" \
"v_mfma_f32_16x16x32_bf16 v[176:179], a[100:103], a[28:31], v[176:179]\n" \
"buffer_load_dwordx4 v227, s[48:51], s75 offen offset:3072 lds\n" \
"v_mfma_f32_16x16x32_bf16 v[64:67], a[64:67], a[0:3], v[64:67]\n" \
"s_xor_b32 s75, s75, 0x80\n" \
"ds_read_b128 a[24:27], v230 offset:38912\n" \
"v_mfma_f32_16x16x32_bf16 v[64:67], a[68:71], a[4:7], v[64:67]\n" \
"s_add_u32 m0, s74, 102400\n" \
"ds_read_b128 a[28:31], v231 offset:38912\n" \
"v_mul_f32 v254, s31, v254\n" \
"v_mfma_f32_16x16x32_bf16 v[68:71], a[72:75], a[0:3], v[68:71]\n" \
"s_waitcnt lgkmcnt(4)\n" \
"ds_read_b64_tr_b16 a[96:97], v232 offset:33792\n" \
"v_cvt_pk_bf16_f32 v243, v243, v252\n" \
"v_mfma_f32_16x16x32_bf16 v[68:71], a[76:79], a[4:7], v[68:71]\n" \
"buffer_load_dwordx4 v224, s[48:51], s77 offen offset:0 lds\n" \
"v_mfma_f32_16x16x32_bf16 v[72:75], a[80:83], a[0:3], v[72:75]\n" \
"ds_read_b64_tr_b16 a[98:99], v232 offset:35840\n" \
"v_cvt_pk_bf16_f32 v253, v253, v254\n" \
"v_mfma_f32_16x16x32_bf16 v[72:75], a[84:87], a[4:7], v[72:75]\n" \
"s_waitcnt lgkmcnt(3)\n" \
"ds_read_b64_tr_b16 a[100:101], v232 offset:41984\n" \
"v_accvgpr_write_b32 a236, v243\n" \
"v_mfma_f32_16x16x32_bf16 v[76:79], a[88:91], a[0:3], v[76:79]\n" \
"ds_read_b64_tr_b16 a[102:103], v232 offset:44032\n" \
"v_accvgpr_write_b32 a237, v253\n" \
"v_mfma_f32_16x16x32_bf16 v[76:79], a[92:95], a[4:7], v[76:79]\n" \
"buffer_load_dwordx4 v225, s[48:51], s77 offen offset:1024 lds\n" \
"v_mfma_f32_16x16x32_bf16 v[108:111], a[88:91], a[8:11], v[108:111]\n" \
"ds_read_b64_tr_b16 a[104:105], v233 offset:33792\n" \
"v_accvgpr_read_b32 v243, a252\n" \
"v_mfma_f32_16x16x32_bf16 v[108:111], a[92:95], a[12:15], v[108:111]\n" \
"ds_read_b64_tr_b16 a[106:107], v233 offset:35840\n" \
"v_accvgpr_read_b32 v252, a253\n" \
"v_mfma_f32_16x16x32_bf16 v[104:107], a[80:83], a[8:11], v[104:107]\n" \
"s_waitcnt lgkmcnt(6)\n" \
"ds_read_b64_tr_b16 a[108:109], v233 offset:41984\n" \
"v_accvgpr_read_b32 v253, a254\n" \
"v_mfma_f32_16x16x32_bf16 v[104:107], a[84:87], a[12:15], v[104:107]\n" \
"buffer_load_dwordx4 v226, s[48:51], s77 offen offset:2048 lds\n" \
"v_mfma_f32_16x16x32_bf16 v[100:103], a[72:75], a[8:11], v[100:103]\n" \
"ds_read_b64_tr_b16 a[110:111], v233 offset:44032\n" \
"v_accvgpr_read_b32 v254, a255\n" \
"v_mfma_f32_16x16x32_bf16 v[100:103], a[76:79], a[12:15], v[100:103]\n" \
"ds_read_b64_tr_b16 a[112:113], v232 offset:34304\n" \
"v_mul_f32 v243, s31, v243\n" \
"v_mfma_f32_16x16x32_bf16 v[96:99], a[64:67], a[8:11], v[96:99]\n" \
"ds_read_b64_tr_b16 a[114:115], v232 offset:36352\n" \
"v_mul_f32 v252, s31, v252\n" \
"v_mfma_f32_16x16x32_bf16 v[96:99], a[68:71], a[12:15], v[96:99]\n" \
"buffer_load_dwordx4 v227, s[48:51], s77 offen offset:3072 lds\n" \
"v_mfma_f32_16x16x32_bf16 v[128:131], a[64:67], a[16:19], v[128:131]\n" \
"ds_read_b64_tr_b16 a[116:117], v232 offset:42496\n" \
"v_mul_f32 v253, s31, v253\n" \
"v_mfma_f32_16x16x32_bf16 v[128:131], a[68:71], a[20:23], v[128:131]\n" \
"ds_read_b64_tr_b16 a[118:119], v232 offset:44544\n" \
"v_mul_f32 v254, s31, v254\n" \
"v_mfma_f32_16x16x32_bf16 v[132:135], a[72:75], a[16:19], v[132:135]\n" \
"s_xor_b32 s77, s77, 0x80\n" \
"ds_read_b64_tr_b16 a[120:121], v233 offset:34304\n" \
"v_cvt_pk_bf16_f32 v243, v243, v252\n" \
"v_mfma_f32_16x16x32_bf16 v[132:135], a[76:79], a[20:23], v[132:135]\n" \
"v_cvt_pk_bf16_f32 v253, v253, v254\n" \
"v_accvgpr_write_b32 a238, v243\n" \
"v_mfma_f32_16x16x32_bf16 v[136:139], a[80:83], a[16:19], v[136:139]\n" \
"ds_read_b64_tr_b16 a[122:123], v233 offset:36352\n" \
"v_accvgpr_write_b32 a239, v253\n" \
"v_mfma_f32_16x16x32_bf16 v[136:139], a[84:87], a[20:23], v[136:139]\n" \
"ds_read_b64_tr_b16 a[124:125], v233 offset:42496\n" \
"v_accvgpr_read_b32 v243, a192\n" \
"v_mfma_f32_16x16x32_bf16 v[140:143], a[88:91], a[16:19], v[140:143]\n" \
"ds_read_b64_tr_b16 a[126:127], v233 offset:44544\n" \
"v_accvgpr_read_b32 v252, a193\n" \
"v_mfma_f32_16x16x32_bf16 v[140:143], a[92:95], a[20:23], v[140:143]\n" \
"buffer_store_dwordx4 v[200:203], v236, s[56:59], s93 offen offset:2048 sc1\n" \
"v_mfma_f32_16x16x32_bf16 v[172:175], a[88:91], a[24:27], v[172:175]\n" \
"v_accvgpr_read_b32 v253, a194\n" \
"v_accvgpr_read_b32 v254, a195\n" \
"v_mfma_f32_16x16x32_bf16 v[172:175], a[92:95], a[28:31], v[172:175]\n" \
"v_mul_f32 v243, s31, v243\n" \
"v_mul_f32 v252, s31, v252\n" \
"v_mfma_f32_16x16x32_bf16 v[168:171], a[80:83], a[24:27], v[168:171]\n" \
"v_mul_f32 v253, s31, v253\n" \
"v_mul_f32 v254, s31, v254\n" \
"v_mfma_f32_16x16x32_bf16 v[168:171], a[84:87], a[28:31], v[168:171]\n" \
"v_cvt_pk_bf16_f32 v243, v243, v252\n" \
"v_cvt_pk_bf16_f32 v253, v253, v254\n" \
"v_mfma_f32_16x16x32_bf16 v[164:167], a[72:75], a[24:27], v[164:167]\n" \
"v_accvgpr_write_b32 a192, v243\n" \
"v_accvgpr_write_b32 a193, v253\n" \
"v_mfma_f32_16x16x32_bf16 v[164:167], a[76:79], a[28:31], v[164:167]\n" \
"v_accvgpr_read_b32 v243, a208\n" \
"v_accvgpr_read_b32 v252, a209\n" \
"v_mfma_f32_16x16x32_bf16 v[160:163], a[64:67], a[24:27], v[160:163]\n" \
"v_accvgpr_read_b32 v253, a210\n" \
"v_accvgpr_read_b32 v254, a211\n" \
"v_mfma_f32_16x16x32_bf16 v[160:163], a[68:71], a[28:31], v[160:163]\n" \
"s_waitcnt vmcnt(35) lgkmcnt(0)\n" \
"s_barrier\n" \
"v_mfma_f32_16x16x32_bf16 v[80:83], a[96:99], a[0:3], v[80:83]\n" \
"ds_read_b64_tr_b16 a[64:65], v232 offset:49152\n" \
"v_mul_f32 v243, s31, v243\n" \
"v_mfma_f32_16x16x32_bf16 v[80:83], a[100:103], a[4:7], v[80:83]\n" \
"ds_read_b64_tr_b16 a[66:67], v232 offset:51200\n" \
"v_mul_f32 v252, s31, v252\n" \
"v_mfma_f32_16x16x32_bf16 v[84:87], a[104:107], a[0:3], v[84:87]\n" \
"s_add_u32 m0, s79, 32768\n" \
"ds_read_b64_tr_b16 a[68:69], v232 offset:57344\n" \
"v_mul_f32 v253, s31, v253\n" \
"v_mfma_f32_16x16x32_bf16 v[84:87], a[108:111], a[4:7], v[84:87]\n" \
"buffer_load_dwordx4 v228, s[52:55], s78 offen offset:0 lds\n" \
"v_mfma_f32_16x16x32_bf16 v[88:91], a[112:115], a[0:3], v[88:91]\n" \
"ds_read_b64_tr_b16 a[70:71], v232 offset:59392\n" \
"v_mul_f32 v254, s31, v254\n" \
"v_mfma_f32_16x16x32_bf16 v[88:91], a[116:119], a[4:7], v[88:91]\n" \
"ds_read_b64_tr_b16 a[72:73], v233 offset:49152\n" \
"v_cvt_pk_bf16_f32 v243, v243, v252\n" \
"v_mfma_f32_16x16x32_bf16 v[92:95], a[120:123], a[0:3], v[92:95]\n" \
"ds_read_b64_tr_b16 a[74:75], v233 offset:51200\n" \
"v_cvt_pk_bf16_f32 v253, v253, v254\n" \
"v_mfma_f32_16x16x32_bf16 v[92:95], a[124:127], a[4:7], v[92:95]\n" \
"buffer_load_dwordx4 v229, s[52:55], s78 offen offset:1024 lds\n" \
"v_mfma_f32_16x16x32_bf16 v[124:127], a[120:123], a[8:11], v[124:127]\n" \
"s_waitcnt vmcnt(35)\n" \
"ds_read_b128 a[0:3], v250 offset:0\n" \
"v_accvgpr_write_b32 a194, v243\n" \
"v_mfma_f32_16x16x32_bf16 v[124:127], a[124:127], a[12:15], v[124:127]\n" \
"ds_read_b128 a[4:7], v251 offset:0\n" \
"v_accvgpr_write_b32 a195, v253\n" \
"v_mfma_f32_16x16x32_bf16 v[120:123], a[112:115], a[8:11], v[120:123]\n" \
"s_xor_b32 s78, s78, 0x180000\n" \
"ds_read_b64_tr_b16 a[76:77], v233 offset:57344\n" \
"v_accvgpr_read_b32 v243, a196\n" \
"v_mfma_f32_16x16x32_bf16 v[120:123], a[116:119], a[12:15], v[120:123]\n" \
"buffer_load_dwordx4 v228, s[52:55], s89 offen offset:2048 lds\n" \
"v_mfma_f32_16x16x32_bf16 v[116:119], a[104:107], a[8:11], v[116:119]\n" \
"ds_read_b64_tr_b16 a[78:79], v233 offset:59392\n" \
"v_accvgpr_read_b32 v252, a197\n" \
"v_mfma_f32_16x16x32_bf16 v[116:119], a[108:111], a[12:15], v[116:119]\n" \
"ds_read_b64_tr_b16 a[80:81], v232 offset:49664\n" \
"v_accvgpr_read_b32 v253, a198\n" \
"v_mfma_f32_16x16x32_bf16 v[112:115], a[96:99], a[8:11], v[112:115]\n" \
"ds_read_b64_tr_b16 a[82:83], v232 offset:51712\n" \
"v_accvgpr_read_b32 v254, a199\n" \
"v_mfma_f32_16x16x32_bf16 v[112:115], a[100:103], a[12:15], v[112:115]\n" \
"buffer_load_dwordx4 v229, s[52:55], s89 offen offset:3072 lds\n" \
"v_mfma_f32_16x16x32_bf16 v[144:147], a[96:99], a[16:19], v[144:147]\n" \
"s_waitcnt vmcnt(35)\n" \
"s_xor_b32 s89, s89, 0x180000\n" \
"ds_read_b128 a[8:11], v250 offset:2048\n" \
"v_mfma_f32_16x16x32_bf16 v[144:147], a[100:103], a[20:23], v[144:147]\n" \
"s_add_u32 m0, s74, 131072\n" \
"ds_read_b128 a[12:15], v251 offset:2048\n" \
"v_mul_f32 v243, s31, v243\n" \
"v_mfma_f32_16x16x32_bf16 v[148:151], a[104:107], a[16:19], v[148:151]\n" \
"s_waitcnt lgkmcnt(6)\n" \
"ds_read_b64_tr_b16 a[84:85], v232 offset:57856\n" \
"v_mul_f32 v252, s31, v252\n" \
"v_mfma_f32_16x16x32_bf16 v[148:151], a[108:111], a[20:23], v[148:151]\n" \
"buffer_load_dwordx4 v224, s[48:51], s75 offen offset:0 lds\n" \
"v_mfma_f32_16x16x32_bf16 v[152:155], a[112:115], a[16:19], v[152:155]\n" \
"ds_read_b64_tr_b16 a[86:87], v232 offset:59904\n" \
"v_mul_f32 v253, s31, v253\n" \
"v_mfma_f32_16x16x32_bf16 v[152:155], a[116:119], a[20:23], v[152:155]\n" \
"ds_read_b64_tr_b16 a[88:89], v233 offset:49664\n" \
"v_mul_f32 v254, s31, v254\n" \
"v_mfma_f32_16x16x32_bf16 v[156:159], a[120:123], a[16:19], v[156:159]\n" \
"ds_read_b64_tr_b16 a[90:91], v233 offset:51712\n" \
"v_cvt_pk_bf16_f32 v243, v243, v252\n" \
"v_mfma_f32_16x16x32_bf16 v[156:159], a[124:127], a[20:23], v[156:159]\n" \
"buffer_load_dwordx4 v225, s[48:51], s75 offen offset:1024 lds\n" \
"v_mfma_f32_16x16x32_bf16 v[188:191], a[120:123], a[24:27], v[188:191]\n" \
"s_waitcnt vmcnt(35)\n" \
"ds_read_b128 a[16:19], v250 offset:4096\n" \
"v_cvt_pk_bf16_f32 v253, v253, v254\n" \
"v_mfma_f32_16x16x32_bf16 v[188:191], a[124:127], a[28:31], v[188:191]\n" \
"ds_read_b128 a[20:23], v251 offset:4096\n" \
"v_accvgpr_write_b32 a196, v243\n" \
"v_mfma_f32_16x16x32_bf16 v[184:187], a[112:115], a[24:27], v[184:187]\n" \
"s_waitcnt lgkmcnt(6)\n" \
"ds_read_b64_tr_b16 a[92:93], v233 offset:57856\n" \
"v_accvgpr_write_b32 a197, v253\n" \
"v_mfma_f32_16x16x32_bf16 v[184:187], a[116:119], a[28:31], v[184:187]\n" \
"buffer_load_dwordx4 v226, s[48:51], s75 offen offset:2048 lds\n" \
"v_mfma_f32_16x16x32_bf16 v[180:183], a[104:107], a[24:27], v[180:183]\n" \
"ds_read_b64_tr_b16 a[94:95], v233 offset:59904\n" \
"v_accvgpr_read_b32 v243, a212\n" \
"v_mfma_f32_16x16x32_bf16 v[180:183], a[108:111], a[28:31], v[180:183]\n" \
"v_accvgpr_read_b32 v252, a213\n" \
"v_accvgpr_read_b32 v253, a214\n" \
"v_mfma_f32_16x16x32_bf16 v[176:179], a[96:99], a[24:27], v[176:179]\n" \
"v_accvgpr_read_b32 v254, a215\n" \
"v_mul_f32 v243, s31, v243\n" \
"v_mfma_f32_16x16x32_bf16 v[176:179], a[100:103], a[28:31], v[176:179]\n" \
"buffer_load_dwordx4 v227, s[48:51], s75 offen offset:3072 lds\n" \
"v_mfma_f32_16x16x32_bf16 v[64:67], a[64:67], a[0:3], v[64:67]\n" \
"s_waitcnt vmcnt(34)\n" \
"s_xor_b32 s75, s75, 0x180\n" \
"ds_read_b128 a[24:27], v250 offset:6144\n" \
"v_mfma_f32_16x16x32_bf16 v[64:67], a[68:71], a[4:7], v[64:67]\n" \
"s_add_u32 m0, s74, 135168\n" \
"ds_read_b128 a[28:31], v251 offset:6144\n" \
"v_mul_f32 v252, s31, v252\n" \
"v_mfma_f32_16x16x32_bf16 v[68:71], a[72:75], a[0:3], v[68:71]\n" \
"s_waitcnt lgkmcnt(4)\n" \
"ds_read_b64_tr_b16 a[96:97], v232 offset:50176\n" \
"v_mul_f32 v253, s31, v253\n" \
"v_mfma_f32_16x16x32_bf16 v[68:71], a[76:79], a[4:7], v[68:71]\n" \
"buffer_load_dwordx4 v224, s[48:51], s77 offen offset:0 lds\n" \
"v_mfma_f32_16x16x32_bf16 v[72:75], a[80:83], a[0:3], v[72:75]\n" \
"ds_read_b64_tr_b16 a[98:99], v232 offset:52224\n" \
"v_mul_f32 v254, s31, v254\n" \
"v_mfma_f32_16x16x32_bf16 v[72:75], a[84:87], a[4:7], v[72:75]\n" \
"s_waitcnt lgkmcnt(3)\n" \
"ds_read_b64_tr_b16 a[100:101], v232 offset:58368\n" \
"v_cvt_pk_bf16_f32 v243, v243, v252\n" \
"v_mfma_f32_16x16x32_bf16 v[76:79], a[88:91], a[0:3], v[76:79]\n" \
"ds_read_b64_tr_b16 a[102:103], v232 offset:60416\n" \
"v_cvt_pk_bf16_f32 v253, v253, v254\n" \
"v_mfma_f32_16x16x32_bf16 v[76:79], a[92:95], a[4:7], v[76:79]\n" \
"buffer_load_dwordx4 v225, s[48:51], s77 offen offset:1024 lds\n" \
"v_mfma_f32_16x16x32_bf16 v[108:111], a[88:91], a[8:11], v[108:111]\n" \
"ds_read_b64_tr_b16 a[104:105], v233 offset:50176\n" \
"v_accvgpr_write_b32 a198, v243\n" \
"v_mfma_f32_16x16x32_bf16 v[108:111], a[92:95], a[12:15], v[108:111]\n" \
"ds_read_b64_tr_b16 a[106:107], v233 offset:52224\n" \
"v_accvgpr_write_b32 a199, v253\n" \
"v_mfma_f32_16x16x32_bf16 v[104:107], a[80:83], a[8:11], v[104:107]\n" \
"s_waitcnt lgkmcnt(6)\n" \
"ds_read_b64_tr_b16 a[108:109], v233 offset:58368\n" \
"Lpg2773_%=:\n" \
"s_cbranch_execz Lpg5019_%=\n" \
"v_accvgpr_read_b32 v243, a200\n" \
"v_mfma_f32_16x16x32_bf16 v[104:107], a[84:87], a[12:15], v[104:107]\n" \
"buffer_load_dwordx4 v226, s[48:51], s77 offen offset:2048 lds\n" \
"v_mfma_f32_16x16x32_bf16 v[100:103], a[72:75], a[8:11], v[100:103]\n" \
"ds_read_b64_tr_b16 a[110:111], v233 offset:60416\n" \
"v_accvgpr_read_b32 v252, a201\n" \
"v_mfma_f32_16x16x32_bf16 v[100:103], a[76:79], a[12:15], v[100:103]\n" \
"ds_read_b64_tr_b16 a[112:113], v232 offset:50688\n" \
"v_accvgpr_read_b32 v253, a202\n" \
"v_mfma_f32_16x16x32_bf16 v[96:99], a[64:67], a[8:11], v[96:99]\n" \
"ds_read_b64_tr_b16 a[114:115], v232 offset:52736\n" \
"v_accvgpr_read_b32 v254, a203\n" \
"v_mfma_f32_16x16x32_bf16 v[96:99], a[68:71], a[12:15], v[96:99]\n" \
"buffer_load_dwordx4 v227, s[48:51], s77 offen offset:3072 lds\n" \
"v_mfma_f32_16x16x32_bf16 v[128:131], a[64:67], a[16:19], v[128:131]\n" \
"ds_read_b64_tr_b16 a[116:117], v232 offset:58880\n" \
"v_mul_f32 v243, s31, v243\n" \
"v_mfma_f32_16x16x32_bf16 v[128:131], a[68:71], a[20:23], v[128:131]\n" \
"ds_read_b64_tr_b16 a[118:119], v232 offset:60928\n" \
"v_mul_f32 v252, s31, v252\n" \
"v_mfma_f32_16x16x32_bf16 v[132:135], a[72:75], a[16:19], v[132:135]\n" \
"s_xor_b32 s77, s77, 0x180\n" \
"ds_read_b64_tr_b16 a[120:121], v233 offset:50688\n" \
"v_mul_f32 v253, s31, v253\n" \
"v_mfma_f32_16x16x32_bf16 v[132:135], a[76:79], a[20:23], v[132:135]\n" \
"v_mul_f32 v254, s31, v254\n" \
"v_cvt_pk_bf16_f32 v243, v243, v252\n" \
"v_mfma_f32_16x16x32_bf16 v[136:139], a[80:83], a[16:19], v[136:139]\n" \
"ds_read_b64_tr_b16 a[122:123], v233 offset:52736\n" \
"v_cvt_pk_bf16_f32 v253, v253, v254\n" \
"v_mfma_f32_16x16x32_bf16 v[136:139], a[84:87], a[20:23], v[136:139]\n" \
"ds_read_b64_tr_b16 a[124:125], v233 offset:58880\n" \
"v_accvgpr_write_b32 a200, v243\n" \
"v_mfma_f32_16x16x32_bf16 v[140:143], a[88:91], a[16:19], v[140:143]\n" \
"ds_read_b64_tr_b16 a[126:127], v233 offset:60928\n" \
"v_accvgpr_write_b32 a201, v253\n" \
"v_mfma_f32_16x16x32_bf16 v[140:143], a[92:95], a[20:23], v[140:143]\n" \
"buffer_store_dwordx4 v[216:219], v236, s[56:59], s93 offen offset:2304 sc1\n" \
"v_mfma_f32_16x16x32_bf16 v[172:175], a[88:91], a[24:27], v[172:175]\n" \
"v_accvgpr_read_b32 v243, a216\n" \
"v_accvgpr_read_b32 v252, a217\n" \
"v_mfma_f32_16x16x32_bf16 v[172:175], a[92:95], a[28:31], v[172:175]\n" \
"v_accvgpr_read_b32 v253, a218\n" \
"v_accvgpr_read_b32 v254, a219\n" \
"v_mfma_f32_16x16x32_bf16 v[168:171], a[80:83], a[24:27], v[168:171]\n" \
"v_mul_f32 v243, s31, v243\n" \
"v_mul_f32 v252, s31, v252\n" \
"v_mfma_f32_16x16x32_bf16 v[168:171], a[84:87], a[28:31], v[168:171]\n" \
"v_mul_f32 v253, s31, v253\n" \
"v_mul_f32 v254, s31, v254\n" \
"v_mfma_f32_16x16x32_bf16 v[164:167], a[72:75], a[24:27], v[164:167]\n" \
"v_cvt_pk_bf16_f32 v243, v243, v252\n" \
"v_cvt_pk_bf16_f32 v253, v253, v254\n" \
"v_mfma_f32_16x16x32_bf16 v[164:167], a[76:79], a[28:31], v[164:167]\n" \
"v_accvgpr_write_b32 a202, v243\n" \
"v_accvgpr_write_b32 a203, v253\n" \
"v_mfma_f32_16x16x32_bf16 v[160:163], a[64:67], a[24:27], v[160:163]\n" \
"v_accvgpr_read_b32 v243, a204\n" \
"v_accvgpr_read_b32 v252, a205\n" \
"v_mfma_f32_16x16x32_bf16 v[160:163], a[68:71], a[28:31], v[160:163]\n" \
"s_waitcnt vmcnt(35) lgkmcnt(0)\n" \
"s_barrier\n" \
"v_mfma_f32_16x16x32_bf16 v[80:83], a[96:99], a[0:3], v[80:83]\n" \
"ds_read_b64_tr_b16 a[64:65], v232 offset:0\n" \
"v_accvgpr_read_b32 v253, a206\n" \
"v_mfma_f32_16x16x32_bf16 v[80:83], a[100:103], a[4:7], v[80:83]\n" \
"ds_read_b64_tr_b16 a[66:67], v232 offset:2048\n" \
"v_accvgpr_read_b32 v254, a207\n" \
"v_mfma_f32_16x16x32_bf16 v[84:87], a[104:107], a[0:3], v[84:87]\n" \
"s_add_u32 m0, s79, 49152\n" \
"ds_read_b64_tr_b16 a[68:69], v232 offset:8192\n" \
"v_mul_f32 v243, s31, v243\n" \
"v_mfma_f32_16x16x32_bf16 v[84:87], a[108:111], a[4:7], v[84:87]\n" \
"buffer_load_dwordx4 v228, s[52:55], s78 offen offset:0 lds\n" \
"v_mfma_f32_16x16x32_bf16 v[88:91], a[112:115], a[0:3], v[88:91]\n" \
"ds_read_b64_tr_b16 a[70:71], v232 offset:10240\n" \
"v_mul_f32 v252, s31, v252\n" \
"v_mfma_f32_16x16x32_bf16 v[88:91], a[116:119], a[4:7], v[88:91]\n" \
"ds_read_b64_tr_b16 a[72:73], v233 offset:0\n" \
"v_mul_f32 v253, s31, v253\n" \
"v_mfma_f32_16x16x32_bf16 v[92:95], a[120:123], a[0:3], v[92:95]\n" \
"ds_read_b64_tr_b16 a[74:75], v233 offset:2048\n" \
"v_mul_f32 v254, s31, v254\n" \
"v_mfma_f32_16x16x32_bf16 v[92:95], a[124:127], a[4:7], v[92:95]\n" \
"buffer_load_dwordx4 v229, s[52:55], s78 offen offset:1024 lds\n" \
"v_mfma_f32_16x16x32_bf16 v[124:127], a[120:123], a[8:11], v[124:127]\n" \
"s_waitcnt vmcnt(35)\n" \
"ds_read_b128 a[0:3], v230 offset:0\n" \
"v_cvt_pk_bf16_f32 v243, v243, v252\n" \
"v_mfma_f32_16x16x32_bf16 v[124:127], a[124:127], a[12:15], v[124:127]\n" \
"ds_read_b128 a[4:7], v231 offset:0\n" \
"v_cvt_pk_bf16_f32 v253, v253, v254\n" \
"v_mfma_f32_16x16x32_bf16 v[120:123], a[112:115], a[8:11], v[120:123]\n" \
"s_xor_b32 s78, s78, 0x80000\n" \
"ds_read_b64_tr_b16 a[76:77], v233 offset:8192\n" \
"v_accvgpr_write_b32 a204, v243\n" \
"v_mfma_f32_16x16x32_bf16 v[120:123], a[116:119], a[12:15], v[120:123]\n" \
"buffer_load_dwordx4 v228, s[52:55], s89 offen offset:2048 lds\n" \
"v_mfma_f32_16x16x32_bf16 v[116:119], a[104:107], a[8:11], v[116:119]\n" \
"ds_read_b64_tr_b16 a[78:79], v233 offset:10240\n" \
"v_accvgpr_write_b32 a205, v253\n" \
"v_mfma_f32_16x16x32_bf16 v[116:119], a[108:111], a[12:15], v[116:119]\n" \
"ds_read_b64_tr_b16 a[80:81], v232 offset:512\n" \
"v_accvgpr_read_b32 v243, a220\n" \
"v_mfma_f32_16x16x32_bf16 v[112:115], a[96:99], a[8:11], v[112:115]\n" \
"ds_read_b64_tr_b16 a[82:83], v232 offset:2560\n" \
"v_accvgpr_read_b32 v252, a221\n" \
"v_mfma_f32_16x16x32_bf16 v[112:115], a[100:103], a[12:15], v[112:115]\n" \
"buffer_load_dwordx4 v229, s[52:55], s89 offen offset:3072 lds\n" \
"v_mfma_f32_16x16x32_bf16 v[144:147], a[96:99], a[16:19], v[144:147]\n" \
"s_waitcnt vmcnt(35)\n" \
"s_xor_b32 s89, s89, 0x80000\n" \
"ds_read_b128 a[8:11], v230 offset:2048\n" \
"v_mfma_f32_16x16x32_bf16 v[144:147], a[100:103], a[20:23], v[144:147]\n" \
"s_add_u32 m0, s74, 65536\n" \
"ds_read_b128 a[12:15], v231 offset:2048\n" \
"v_accvgpr_read_b32 v253, a222\n" \
"v_mfma_f32_16x16x32_bf16 v[148:151], a[104:107], a[16:19], v[148:151]\n" \
"s_waitcnt lgkmcnt(6)\n" \
"ds_read_b64_tr_b16 a[84:85], v232 offset:8704\n" \
"v_accvgpr_read_b32 v254, a223\n" \
"v_mfma_f32_16x16x32_bf16 v[148:151], a[108:111], a[20:23], v[148:151]\n" \
"buffer_load_dwordx4 v224, s[48:51], s75 offen offset:0 lds\n" \
"v_mfma_f32_16x16x32_bf16 v[152:155], a[112:115], a[16:19], v[152:155]\n" \
"ds_read_b64_tr_b16 a[86:87], v232 offset:10752\n" \
"v_mul_f32 v243, s31, v243\n" \
"v_mfma_f32_16x16x32_bf16 v[152:155], a[116:119], a[20:23], v[152:155]\n" \
"ds_read_b64_tr_b16 a[88:89], v233 offset:512\n" \
"v_mul_f32 v252, s31, v252\n" \
"v_mfma_f32_16x16x32_bf16 v[156:159], a[120:123], a[16:19], v[156:159]\n" \
"ds_read_b64_tr_b16 a[90:91], v233 offset:2560\n" \
"v_mul_f32 v253, s31, v253\n" \
"v_mfma_f32_16x16x32_bf16 v[156:159], a[124:127], a[20:23], v[156:159]\n" \
"buffer_load_dwordx4 v225, s[48:51], s75 offen offset:1024 lds\n" \
"v_mfma_f32_16x16x32_bf16 v[188:191], a[120:123], a[24:27], v[188:191]\n" \
"s_waitcnt vmcnt(35)\n" \
"ds_read_b128 a[16:19], v230 offset:4096\n" \
"v_mul_f32 v254, s31, v254\n" \
"v_mfma_f32_16x16x32_bf16 v[188:191], a[124:127], a[28:31], v[188:191]\n" \
"ds_read_b128 a[20:23], v231 offset:4096\n" \
"v_cvt_pk_bf16_f32 v243, v243, v252\n" \
"v_mfma_f32_16x16x32_bf16 v[184:187], a[112:115], a[24:27], v[184:187]\n" \
"s_waitcnt lgkmcnt(6)\n" \
"ds_read_b64_tr_b16 a[92:93], v233 offset:8704\n" \
"v_cvt_pk_bf16_f32 v253, v253, v254\n" \
"v_mfma_f32_16x16x32_bf16 v[184:187], a[116:119], a[28:31], v[184:187]\n" \
"buffer_load_dwordx4 v226, s[48:51], s75 offen offset:2048 lds\n" \
"v_mfma_f32_16x16x32_bf16 v[180:183], a[104:107], a[24:27], v[180:183]\n" \
"ds_read_b64_tr_b16 a[94:95], v233 offset:10752\n" \
"v_accvgpr_write_b32 a206, v243\n" \
"v_mfma_f32_16x16x32_bf16 v[180:183], a[108:111], a[28:31], v[180:183]\n" \
"v_accvgpr_write_b32 a207, v253\n" \
"v_mul_f32 v32, s76, v32\n" \
"v_mfma_f32_16x16x32_bf16 v[176:179], a[96:99], a[24:27], v[176:179]\n" \
"v_mul_f32 v33, s76, v33\n" \
"v_mul_f32 v34, s76, v34\n" \
"v_mfma_f32_16x16x32_bf16 v[176:179], a[100:103], a[28:31], v[176:179]\n" \
"buffer_load_dwordx4 v227, s[48:51], s75 offen offset:3072 lds\n" \
"v_mfma_f32_16x16x32_bf16 v[64:67], a[64:67], a[0:3], v[64:67]\n" \
"s_waitcnt vmcnt(35)\n" \
"s_xor_b32 s75, s75, 0x80\n" \
"ds_read_b128 a[24:27], v230 offset:6144\n" \
"v_mfma_f32_16x16x32_bf16 v[64:67], a[68:71], a[4:7], v[64:67]\n" \
"s_add_u32 m0, s74, 69632\n" \
"ds_read_b128 a[28:31], v231 offset:6144\n" \
"v_mul_f32 v35, s76, v35\n" \
"v_mfma_f32_16x16x32_bf16 v[68:71], a[72:75], a[0:3], v[68:71]\n" \
"s_waitcnt lgkmcnt(4)\n" \
"ds_read_b64_tr_b16 a[96:97], v232 offset:1024\n" \
"v_cvt_pk_bf16_f32 v32, v32, v33\n" \
"v_mfma_f32_16x16x32_bf16 v[68:71], a[76:79], a[4:7], v[68:71]\n" \
"buffer_load_dwordx4 v224, s[48:51], s77 offen offset:0 lds\n" \
"v_mfma_f32_16x16x32_bf16 v[72:75], a[80:83], a[0:3], v[72:75]\n" \
"ds_read_b64_tr_b16 a[98:99], v232 offset:3072\n" \
"v_cvt_pk_bf16_f32 v33, v34, v35\n" \
"v_mfma_f32_16x16x32_bf16 v[72:75], a[84:87], a[4:7], v[72:75]\n" \
"s_waitcnt lgkmcnt(3)\n" \
"ds_read_b64_tr_b16 a[100:101], v232 offset:9216\n" \
"v_mul_f32 v36, s76, v36\n" \
"v_mfma_f32_16x16x32_bf16 v[76:79], a[88:91], a[0:3], v[76:79]\n" \
"ds_read_b64_tr_b16 a[102:103], v232 offset:11264\n" \
"v_mul_f32 v37, s76, v37\n" \
"v_mfma_f32_16x16x32_bf16 v[76:79], a[92:95], a[4:7], v[76:79]\n" \
"buffer_load_dwordx4 v225, s[48:51], s77 offen offset:1024 lds\n" \
"v_mfma_f32_16x16x32_bf16 v[108:111], a[88:91], a[8:11], v[108:111]\n" \
"ds_read_b64_tr_b16 a[104:105], v233 offset:1024\n" \
"v_mul_f32 v38, s76, v38\n" \
"v_mfma_f32_16x16x32_bf16 v[108:111], a[92:95], a[12:15], v[108:111]\n" \
"ds_read_b64_tr_b16 a[106:107], v233 offset:3072\n" \
"v_mul_f32 v39, s76, v39\n" \
"v_mfma_f32_16x16x32_bf16 v[104:107], a[80:83], a[8:11], v[104:107]\n" \
"s_waitcnt lgkmcnt(6)\n" \
"ds_read_b64_tr_b16 a[108:109], v233 offset:9216\n" \
"v_cvt_pk_bf16_f32 v34, v36, v37\n" \
"v_mfma_f32_16x16x32_bf16 v[104:107], a[84:87], a[12:15], v[104:107]\n" \
"buffer_load_dwordx4 v226, s[48:51], s77 offen offset:2048 lds\n" \
"v_mfma_f32_16x16x32_bf16 v[100:103], a[72:75], a[8:11], v[100:103]\n" \
"ds_read_b64_tr_b16 a[110:111], v233 offset:11264\n" \
"v_cvt_pk_bf16_f32 v35, v38, v39\n" \
"v_mfma_f32_16x16x32_bf16 v[100:103], a[76:79], a[12:15], v[100:103]\n" \
"ds_read_b64_tr_b16 a[112:113], v232 offset:1536\n" \
"v_mul_f32 v48, s76, v48\n" \
"v_mfma_f32_16x16x32_bf16 v[96:99], a[64:67], a[8:11], v[96:99]\n" \
"ds_read_b64_tr_b16 a[114:115], v232 offset:3584\n" \
"v_mul_f32 v49, s76, v49\n" \
"v_mfma_f32_16x16x32_bf16 v[96:99], a[68:71], a[12:15], v[96:99]\n" \
"buffer_load_dwordx4 v227, s[48:51], s77 offen offset:3072 lds\n" \
"v_mfma_f32_16x16x32_bf16 v[128:131], a[64:67], a[16:19], v[128:131]\n" \
"ds_read_b64_tr_b16 a[116:117], v232 offset:9728\n" \
"v_mul_f32 v50, s76, v50\n" \
"v_mfma_f32_16x16x32_bf16 v[128:131], a[68:71], a[20:23], v[128:131]\n" \
"ds_read_b64_tr_b16 a[118:119], v232 offset:11776\n" \
"v_mul_f32 v51, s76, v51\n" \
"v_mfma_f32_16x16x32_bf16 v[132:135], a[72:75], a[16:19], v[132:135]\n" \
"s_xor_b32 s77, s77, 0x80\n" \
"ds_read_b64_tr_b16 a[120:121], v233 offset:1536\n" \
"v_cvt_pk_bf16_f32 v48, v48, v49\n" \
"v_mfma_f32_16x16x32_bf16 v[132:135], a[76:79], a[20:23], v[132:135]\n" \
"v_cvt_pk_bf16_f32 v49, v50, v51\n" \
"v_mul_f32 v52, s76, v52\n" \
"v_mfma_f32_16x16x32_bf16 v[136:139], a[80:83], a[16:19], v[136:139]\n" \
"ds_read_b64_tr_b16 a[122:123], v233 offset:3584\n" \
"v_mul_f32 v53, s76, v53\n" \
"v_mfma_f32_16x16x32_bf16 v[136:139], a[84:87], a[20:23], v[136:139]\n" \
"ds_read_b64_tr_b16 a[124:125], v233 offset:9728\n" \
"v_mul_f32 v54, s76, v54\n" \
"v_mfma_f32_16x16x32_bf16 v[140:143], a[88:91], a[16:19], v[140:143]\n" \
"s_add_u32 s93, s87, 0\n" \
"ds_read_b64_tr_b16 a[126:127], v233 offset:11776\n" \
"v_mul_f32 v55, s76, v55\n" \
"v_mfma_f32_16x16x32_bf16 v[140:143], a[92:95], a[20:23], v[140:143]\n" \
"buffer_store_dwordx4 v[196:199], v236, s[56:59], s93 offen offset:0 sc1\n" \
"v_mfma_f32_16x16x32_bf16 v[172:175], a[88:91], a[24:27], v[172:175]\n" \
"v_cvt_pk_bf16_f32 v50, v52, v53\n" \
"v_cvt_pk_bf16_f32 v51, v54, v55\n" \
"v_mfma_f32_16x16x32_bf16 v[172:175], a[92:95], a[28:31], v[172:175]\n" \
"v_mul_f32 v40, s76, v40\n" \
"v_mul_f32 v41, s76, v41\n" \
"v_mfma_f32_16x16x32_bf16 v[168:171], a[80:83], a[24:27], v[168:171]\n" \
"v_mul_f32 v42, s76, v42\n" \
"v_mul_f32 v43, s76, v43\n" \
"v_mfma_f32_16x16x32_bf16 v[168:171], a[84:87], a[28:31], v[168:171]\n" \
"v_cvt_pk_bf16_f32 v40, v40, v41\n" \
"v_cvt_pk_bf16_f32 v41, v42, v43\n" \
"v_mfma_f32_16x16x32_bf16 v[164:167], a[72:75], a[24:27], v[164:167]\n" \
"v_mul_f32 v44, s76, v44\n" \
"v_mul_f32 v45, s76, v45\n" \
"v_mfma_f32_16x16x32_bf16 v[164:167], a[76:79], a[28:31], v[164:167]\n" \
"v_mul_f32 v46, s76, v46\n" \
"v_mul_f32 v47, s76, v47\n" \
"v_mfma_f32_16x16x32_bf16 v[160:163], a[64:67], a[24:27], v[160:163]\n" \
"v_cvt_pk_bf16_f32 v42, v44, v45\n" \
"v_cvt_pk_bf16_f32 v43, v46, v47\n" \
"v_mfma_f32_16x16x32_bf16 v[160:163], a[68:71], a[28:31], v[160:163]\n" \
"s_waitcnt vmcnt(35) lgkmcnt(0)\n" \
"s_barrier\n" \
"v_mfma_f32_16x16x32_bf16 v[80:83], a[96:99], a[0:3], v[80:83]\n" \
"ds_read_b64_tr_b16 a[64:65], v232 offset:16384\n" \
"v_mul_f32 v56, s76, v56\n" \
"v_mfma_f32_16x16x32_bf16 v[80:83], a[100:103], a[4:7], v[80:83]\n" \
"ds_read_b64_tr_b16 a[66:67], v232 offset:18432\n" \
"v_mul_f32 v57, s76, v57\n" \
"v_mfma_f32_16x16x32_bf16 v[84:87], a[104:107], a[0:3], v[84:87]\n" \
"s_add_u32 m0, s79, 0\n" \
"ds_read_b64_tr_b16 a[68:69], v232 offset:24576\n" \
"v_mul_f32 v58, s76, v58\n" \
"v_mfma_f32_16x16x32_bf16 v[84:87], a[108:111], a[4:7], v[84:87]\n" \
"buffer_load_dwordx4 v228, s[52:55], s78 offen offset:0 lds\n" \
"v_mfma_f32_16x16x32_bf16 v[88:91], a[112:115], a[0:3], v[88:91]\n" \
"ds_read_b64_tr_b16 a[70:71], v232 offset:26624\n" \
"v_mul_f32 v59, s76, v59\n" \
"v_mfma_f32_16x16x32_bf16 v[88:91], a[116:119], a[4:7], v[88:91]\n" \
"ds_read_b64_tr_b16 a[72:73], v233 offset:16384\n" \
"v_cvt_pk_bf16_f32 v56, v56, v57\n" \
"v_mfma_f32_16x16x32_bf16 v[92:95], a[120:123], a[0:3], v[92:95]\n" \
"ds_read_b64_tr_b16 a[74:75], v233 offset:18432\n" \
"v_cvt_pk_bf16_f32 v57, v58, v59\n" \
"v_mfma_f32_16x16x32_bf16 v[92:95], a[124:127], a[4:7], v[92:95]\n" \
"buffer_load_dwordx4 v229, s[52:55], s78 offen offset:1024 lds\n" \
"v_mfma_f32_16x16x32_bf16 v[124:127], a[120:123], a[8:11], v[124:127]\n" \
"s_waitcnt vmcnt(35)\n" \
"ds_read_b128 a[0:3], v230 offset:32768\n" \
"v_mul_f32 v60, s76, v60\n" \
"v_mfma_f32_16x16x32_bf16 v[124:127], a[124:127], a[12:15], v[124:127]\n" \
"ds_read_b128 a[4:7], v231 offset:32768\n" \
"v_mul_f32 v61, s76, v61\n" \
"v_mfma_f32_16x16x32_bf16 v[120:123], a[112:115], a[8:11], v[120:123]\n" \
"s_xor_b32 s78, s78, 0x780000\n" \
"ds_read_b64_tr_b16 a[76:77], v233 offset:24576\n" \
"v_mul_f32 v62, s76, v62\n" \
"v_mfma_f32_16x16x32_bf16 v[120:123], a[116:119], a[12:15], v[120:123]\n" \
"buffer_load_dwordx4 v228, s[52:55], s89 offen offset:2048 lds\n" \
"v_mfma_f32_16x16x32_bf16 v[116:119], a[104:107], a[8:11], v[116:119]\n" \
"ds_read_b64_tr_b16 a[78:79], v233 offset:26624\n" \
"v_mul_f32 v63, s76, v63\n" \
"v_mfma_f32_16x16x32_bf16 v[116:119], a[108:111], a[12:15], v[116:119]\n" \
"ds_read_b64_tr_b16 a[80:81], v232 offset:16896\n" \
"v_cvt_pk_bf16_f32 v58, v60, v61\n" \
"v_mfma_f32_16x16x32_bf16 v[112:115], a[96:99], a[8:11], v[112:115]\n" \
"ds_read_b64_tr_b16 a[82:83], v232 offset:18944\n" \
"v_cvt_pk_bf16_f32 v59, v62, v63\n" \
"v_mfma_f32_16x16x32_bf16 v[112:115], a[100:103], a[12:15], v[112:115]\n" \
"buffer_load_dwordx4 v229, s[52:55], s89 offen offset:3072 lds\n" \
"v_mfma_f32_16x16x32_bf16 v[144:147], a[96:99], a[16:19], v[144:147]\n" \
"s_waitcnt vmcnt(35)\n" \
"s_xor_b32 s89, s89, 0x780000\n" \
"ds_read_b128 a[8:11], v230 offset:34816\n" \
"v_mfma_f32_16x16x32_bf16 v[144:147], a[100:103], a[20:23], v[144:147]\n" \
"s_add_u32 m0, s74, 98304\n" \
"ds_read_b128 a[12:15], v231 offset:34816\n" \
"v_mul_f32 v0, s76, v0\n" \
"v_mfma_f32_16x16x32_bf16 v[148:151], a[104:107], a[16:19], v[148:151]\n" \
"s_waitcnt lgkmcnt(6)\n" \
"ds_read_b64_tr_b16 a[84:85], v232 offset:25088\n" \
"v_mul_f32 v1, s76, v1\n" \
"v_mfma_f32_16x16x32_bf16 v[148:151], a[108:111], a[20:23], v[148:151]\n" \
"buffer_load_dwordx4 v224, s[48:51], s75 offen offset:0 lds\n" \
"v_mfma_f32_16x16x32_bf16 v[152:155], a[112:115], a[16:19], v[152:155]\n" \
"ds_read_b64_tr_b16 a[86:87], v232 offset:27136\n" \
"v_mul_f32 v2, s76, v2\n" \
"v_mfma_f32_16x16x32_bf16 v[152:155], a[116:119], a[20:23], v[152:155]\n" \
"ds_read_b64_tr_b16 a[88:89], v233 offset:16896\n" \
"v_mul_f32 v3, s76, v3\n" \
"v_mfma_f32_16x16x32_bf16 v[156:159], a[120:123], a[16:19], v[156:159]\n" \
"ds_read_b64_tr_b16 a[90:91], v233 offset:18944\n" \
"v_cvt_pk_bf16_f32 v36, v0, v1\n" \
"v_mfma_f32_16x16x32_bf16 v[156:159], a[124:127], a[20:23], v[156:159]\n" \
"buffer_load_dwordx4 v225, s[48:51], s75 offen offset:1024 lds\n" \
"v_mfma_f32_16x16x32_bf16 v[188:191], a[120:123], a[24:27], v[188:191]\n" \
"s_waitcnt vmcnt(35)\n" \
"ds_read_b128 a[16:19], v230 offset:36864\n" \
"v_cvt_pk_bf16_f32 v37, v2, v3\n" \
"v_mfma_f32_16x16x32_bf16 v[188:191], a[124:127], a[28:31], v[188:191]\n" \
"ds_read_b128 a[20:23], v231 offset:36864\n" \
"v_mul_f32 v4, s76, v4\n" \
"v_mfma_f32_16x16x32_bf16 v[184:187], a[112:115], a[24:27], v[184:187]\n" \
"s_waitcnt lgkmcnt(6)\n" \
"ds_read_b64_tr_b16 a[92:93], v233 offset:25088\n" \
"v_mul_f32 v5, s76, v5\n" \
"v_mfma_f32_16x16x32_bf16 v[184:187], a[116:119], a[28:31], v[184:187]\n" \
"buffer_load_dwordx4 v226, s[48:51], s75 offen offset:2048 lds\n" \
"v_mfma_f32_16x16x32_bf16 v[180:183], a[104:107], a[24:27], v[180:183]\n" \
"ds_read_b64_tr_b16 a[94:95], v233 offset:27136\n" \
"v_mul_f32 v6, s76, v6\n" \
"v_mfma_f32_16x16x32_bf16 v[180:183], a[108:111], a[28:31], v[180:183]\n" \
"v_mul_f32 v7, s76, v7\n" \
"v_cvt_pk_bf16_f32 v38, v4, v5\n" \
"v_mfma_f32_16x16x32_bf16 v[176:179], a[96:99], a[24:27], v[176:179]\n" \
"v_cvt_pk_bf16_f32 v39, v6, v7\n" \
"v_mul_f32 v16, s76, v16\n" \
"v_mfma_f32_16x16x32_bf16 v[176:179], a[100:103], a[28:31], v[176:179]\n" \
"buffer_load_dwordx4 v227, s[48:51], s75 offen offset:3072 lds\n" \
"v_mfma_f32_16x16x32_bf16 v[64:67], a[64:67], a[0:3], v[64:67]\n" \
"s_waitcnt vmcnt(35)\n" \
"s_xor_b32 s75, s75, 0x780\n" \
"ds_read_b128 a[24:27], v230 offset:38912\n" \
"v_mfma_f32_16x16x32_bf16 v[64:67], a[68:71], a[4:7], v[64:67]\n" \
"s_add_u32 m0, s74, 102400\n" \
"ds_read_b128 a[28:31], v231 offset:38912\n" \
"v_mul_f32 v17, s76, v17\n" \
"v_mfma_f32_16x16x32_bf16 v[68:71], a[72:75], a[0:3], v[68:71]\n" \
"s_waitcnt lgkmcnt(4)\n" \
"ds_read_b64_tr_b16 a[96:97], v232 offset:17408\n" \
"v_mul_f32 v18, s76, v18\n" \
"v_mfma_f32_16x16x32_bf16 v[68:71], a[76:79], a[4:7], v[68:71]\n" \
"buffer_load_dwordx4 v224, s[48:51], s77 offen offset:0 lds\n" \
"v_mfma_f32_16x16x32_bf16 v[72:75], a[80:83], a[0:3], v[72:75]\n" \
"ds_read_b64_tr_b16 a[98:99], v232 offset:19456\n" \
"v_mul_f32 v19, s76, v19\n" \
"v_mfma_f32_16x16x32_bf16 v[72:75], a[84:87], a[4:7], v[72:75]\n" \
"s_waitcnt lgkmcnt(3)\n" \
"ds_read_b64_tr_b16 a[100:101], v232 offset:25600\n" \
"v_cvt_pk_bf16_f32 v16, v16, v17\n" \
"v_mfma_f32_16x16x32_bf16 v[76:79], a[88:91], a[0:3], v[76:79]\n" \
"ds_read_b64_tr_b16 a[102:103], v232 offset:27648\n" \
"v_cvt_pk_bf16_f32 v17, v18, v19\n" \
"v_mfma_f32_16x16x32_bf16 v[76:79], a[92:95], a[4:7], v[76:79]\n" \
"buffer_load_dwordx4 v225, s[48:51], s77 offen offset:1024 lds\n" \
"v_mfma_f32_16x16x32_bf16 v[108:111], a[88:91], a[8:11], v[108:111]\n" \
"ds_read_b64_tr_b16 a[104:105], v233 offset:17408\n" \
"v_mul_f32 v20, s76, v20\n" \
"v_mfma_f32_16x16x32_bf16 v[108:111], a[92:95], a[12:15], v[108:111]\n" \
"ds_read_b64_tr_b16 a[106:107], v233 offset:19456\n" \
"v_mul_f32 v21, s76, v21\n" \
"v_mfma_f32_16x16x32_bf16 v[104:107], a[80:83], a[8:11], v[104:107]\n" \
"s_waitcnt lgkmcnt(6)\n" \
"ds_read_b64_tr_b16 a[108:109], v233 offset:25600\n" \
"v_mul_f32 v22, s76, v22\n" \
"v_mfma_f32_16x16x32_bf16 v[104:107], a[84:87], a[12:15], v[104:107]\n" \
"buffer_load_dwordx4 v226, s[48:51], s77 offen offset:2048 lds\n" \
"v_mfma_f32_16x16x32_bf16 v[100:103], a[72:75], a[8:11], v[100:103]\n" \
"ds_read_b64_tr_b16 a[110:111], v233 offset:27648\n" \
"v_mul_f32 v23, s76, v23\n" \
"v_mfma_f32_16x16x32_bf16 v[100:103], a[76:79], a[12:15], v[100:103]\n" \
"ds_read_b64_tr_b16 a[112:113], v232 offset:17920\n" \
"v_cvt_pk_bf16_f32 v18, v20, v21\n" \
"v_mfma_f32_16x16x32_bf16 v[96:99], a[64:67], a[8:11], v[96:99]\n" \
"ds_read_b64_tr_b16 a[114:115], v232 offset:19968\n" \
"v_cvt_pk_bf16_f32 v19, v22, v23\n" \
"v_mfma_f32_16x16x32_bf16 v[96:99], a[68:71], a[12:15], v[96:99]\n" \
"buffer_load_dwordx4 v227, s[48:51], s77 offen offset:3072 lds\n" \
"v_mfma_f32_16x16x32_bf16 v[128:131], a[64:67], a[16:19], v[128:131]\n" \
"ds_read_b64_tr_b16 a[116:117], v232 offset:26112\n" \
"v_mul_f32 v8, s76, v8\n" \
"v_mfma_f32_16x16x32_bf16 v[128:131], a[68:71], a[20:23], v[128:131]\n" \
"ds_read_b64_tr_b16 a[118:119], v232 offset:28160\n" \
"v_mul_f32 v9, s76, v9\n" \
"v_mfma_f32_16x16x32_bf16 v[132:135], a[72:75], a[16:19], v[132:135]\n" \
"s_xor_b32 s77, s77, 0x780\n" \
"ds_read_b64_tr_b16 a[120:121], v233 offset:17920\n" \
"v_mul_f32 v10, s76, v10\n" \
"v_mfma_f32_16x16x32_bf16 v[132:135], a[76:79], a[20:23], v[132:135]\n" \
"v_mul_f32 v11, s76, v11\n" \
"v_cvt_pk_bf16_f32 v52, v8, v9\n" \
"v_mfma_f32_16x16x32_bf16 v[136:139], a[80:83], a[16:19], v[136:139]\n" \
"ds_read_b64_tr_b16 a[122:123], v233 offset:19968\n" \
"v_cvt_pk_bf16_f32 v53, v10, v11\n" \
"v_mfma_f32_16x16x32_bf16 v[136:139], a[84:87], a[20:23], v[136:139]\n" \
"ds_read_b64_tr_b16 a[124:125], v233 offset:26112\n" \
"v_mul_f32 v12, s76, v12\n" \
"v_mfma_f32_16x16x32_bf16 v[140:143], a[88:91], a[16:19], v[140:143]\n" \
"ds_read_b64_tr_b16 a[126:127], v233 offset:28160\n" \
"v_mul_f32 v13, s76, v13\n" \
"v_mfma_f32_16x16x32_bf16 v[140:143], a[92:95], a[20:23], v[140:143]\n" \
"buffer_store_dwordx4 v[212:215], v236, s[56:59], s93 offen offset:256 sc1\n" \
"v_mfma_f32_16x16x32_bf16 v[172:175], a[88:91], a[24:27], v[172:175]\n" \
"v_mul_f32 v14, s76, v14\n" \
"v_mul_f32 v15, s76, v15\n" \
"v_mfma_f32_16x16x32_bf16 v[172:175], a[92:95], a[28:31], v[172:175]\n" \
"v_cvt_pk_bf16_f32 v54, v12, v13\n" \
"v_cvt_pk_bf16_f32 v55, v14, v15\n" \
"v_mfma_f32_16x16x32_bf16 v[168:171], a[80:83], a[24:27], v[168:171]\n" \
"v_mul_f32 v24, s76, v24\n" \
"v_mul_f32 v25, s76, v25\n" \
"v_mfma_f32_16x16x32_bf16 v[168:171], a[84:87], a[28:31], v[168:171]\n" \
"v_mul_f32 v26, s76, v26\n" \
"v_mul_f32 v27, s76, v27\n" \
"v_mfma_f32_16x16x32_bf16 v[164:167], a[72:75], a[24:27], v[164:167]\n" \
"v_cvt_pk_bf16_f32 v24, v24, v25\n" \
"v_cvt_pk_bf16_f32 v25, v26, v27\n" \
"v_mfma_f32_16x16x32_bf16 v[164:167], a[76:79], a[28:31], v[164:167]\n" \
"v_mul_f32 v28, s76, v28\n" \
"v_mul_f32 v29, s76, v29\n" \
"v_mfma_f32_16x16x32_bf16 v[160:163], a[64:67], a[24:27], v[160:163]\n" \
"v_mul_f32 v30, s76, v30\n" \
"v_mul_f32 v31, s76, v31\n" \
"v_mfma_f32_16x16x32_bf16 v[160:163], a[68:71], a[28:31], v[160:163]\n" \
"s_waitcnt vmcnt(35) lgkmcnt(0)\n" \
"s_barrier\n" \
"v_mfma_f32_16x16x32_bf16 v[80:83], a[96:99], a[0:3], v[80:83]\n" \
"ds_read_b64_tr_b16 a[64:65], v232 offset:32768\n" \
"v_cvt_pk_bf16_f32 v26, v28, v29\n" \
"v_mfma_f32_16x16x32_bf16 v[80:83], a[100:103], a[4:7], v[80:83]\n" \
"ds_read_b64_tr_b16 a[66:67], v232 offset:34816\n" \
"v_cvt_pk_bf16_f32 v27, v30, v31\n" \
"v_mfma_f32_16x16x32_bf16 v[84:87], a[104:107], a[0:3], v[84:87]\n" \
"s_add_u32 m0, s79, 16384\n" \
"ds_read_b64_tr_b16 a[68:69], v232 offset:40960\n" \
"v_accvgpr_read_b32 v243, a160\n" \
"v_mfma_f32_16x16x32_bf16 v[84:87], a[108:111], a[4:7], v[84:87]\n" \
"buffer_load_dwordx4 v228, s[52:55], s78 offen offset:0 lds\n" \
"v_mfma_f32_16x16x32_bf16 v[88:91], a[112:115], a[0:3], v[88:91]\n" \
"ds_read_b64_tr_b16 a[70:71], v232 offset:43008\n" \
"v_accvgpr_read_b32 v252, a161\n" \
"v_mfma_f32_16x16x32_bf16 v[88:91], a[116:119], a[4:7], v[88:91]\n" \
"ds_read_b64_tr_b16 a[72:73], v233 offset:32768\n" \
"v_accvgpr_read_b32 v253, a162\n" \
"v_mfma_f32_16x16x32_bf16 v[92:95], a[120:123], a[0:3], v[92:95]\n" \
"ds_read_b64_tr_b16 a[74:75], v233 offset:34816\n" \
"v_accvgpr_read_b32 v254, a163\n" \
"v_mfma_f32_16x16x32_bf16 v[92:95], a[124:127], a[4:7], v[92:95]\n" \
"buffer_load_dwordx4 v229, s[52:55], s78 offen offset:1024 lds\n" \
"v_mfma_f32_16x16x32_bf16 v[124:127], a[120:123], a[8:11], v[124:127]\n" \
"s_waitcnt vmcnt(35)\n" \
"ds_read_b128 a[0:3], v250 offset:0\n" \
"v_mul_f32 v243, s31, v243\n" \
"v_mfma_f32_16x16x32_bf16 v[124:127], a[124:127], a[12:15], v[124:127]\n" \
"ds_read_b128 a[4:7], v251 offset:0\n" \
"v_mul_f32 v252, s31, v252\n" \
"v_mfma_f32_16x16x32_bf16 v[120:123], a[112:115], a[8:11], v[120:123]\n" \
"s_xor_b32 s78, s78, 0x80000\n" \
"ds_read_b64_tr_b16 a[76:77], v233 offset:40960\n" \
"v_mul_f32 v253, s31, v253\n" \
"v_mfma_f32_16x16x32_bf16 v[120:123], a[116:119], a[12:15], v[120:123]\n" \
"buffer_load_dwordx4 v228, s[52:55], s89 offen offset:2048 lds\n" \
"v_mfma_f32_16x16x32_bf16 v[116:119], a[104:107], a[8:11], v[116:119]\n" \
"ds_read_b64_tr_b16 a[78:79], v233 offset:43008\n" \
"v_mul_f32 v254, s31, v254\n" \
"v_mfma_f32_16x16x32_bf16 v[116:119], a[108:111], a[12:15], v[116:119]\n" \
"ds_read_b64_tr_b16 a[80:81], v232 offset:33280\n" \
"v_cvt_pk_bf16_f32 v44, v243, v252\n" \
"v_mfma_f32_16x16x32_bf16 v[112:115], a[96:99], a[8:11], v[112:115]\n" \
"ds_read_b64_tr_b16 a[82:83], v232 offset:35328\n" \
"v_cvt_pk_bf16_f32 v45, v253, v254\n" \
"v_mfma_f32_16x16x32_bf16 v[112:115], a[100:103], a[12:15], v[112:115]\n" \
"buffer_load_dwordx4 v229, s[52:55], s89 offen offset:3072 lds\n" \
"v_mfma_f32_16x16x32_bf16 v[144:147], a[96:99], a[16:19], v[144:147]\n" \
"s_waitcnt vmcnt(35)\n" \
"s_xor_b32 s89, s89, 0x80000\n" \
"ds_read_b128 a[8:11], v250 offset:2048\n" \
"v_mfma_f32_16x16x32_bf16 v[144:147], a[100:103], a[20:23], v[144:147]\n" \
"s_add_u32 m0, s74, 131072\n" \
"ds_read_b128 a[12:15], v251 offset:2048\n" \
"v_accvgpr_read_b32 v243, a176\n" \
"v_mfma_f32_16x16x32_bf16 v[148:151], a[104:107], a[16:19], v[148:151]\n" \
"s_waitcnt lgkmcnt(6)\n" \
"ds_read_b64_tr_b16 a[84:85], v232 offset:41472\n" \
"v_accvgpr_read_b32 v252, a177\n" \
"v_mfma_f32_16x16x32_bf16 v[148:151], a[108:111], a[20:23], v[148:151]\n" \
"buffer_load_dwordx4 v224, s[48:51], s75 offen offset:0 lds\n" \
"v_mfma_f32_16x16x32_bf16 v[152:155], a[112:115], a[16:19], v[152:155]\n" \
"ds_read_b64_tr_b16 a[86:87], v232 offset:43520\n" \
"v_accvgpr_read_b32 v253, a178\n" \
"v_mfma_f32_16x16x32_bf16 v[152:155], a[116:119], a[20:23], v[152:155]\n" \
"ds_read_b64_tr_b16 a[88:89], v233 offset:33280\n" \
"v_accvgpr_read_b32 v254, a179\n" \
"v_mfma_f32_16x16x32_bf16 v[156:159], a[120:123], a[16:19], v[156:159]\n" \
"ds_read_b64_tr_b16 a[90:91], v233 offset:35328\n" \
"v_mul_f32 v243, s31, v243\n" \
"v_mfma_f32_16x16x32_bf16 v[156:159], a[124:127], a[20:23], v[156:159]\n" \
"buffer_load_dwordx4 v225, s[48:51], s75 offen offset:1024 lds\n" \
"v_mfma_f32_16x16x32_bf16 v[188:191], a[120:123], a[24:27], v[188:191]\n" \
"s_waitcnt vmcnt(35)\n" \
"ds_read_b128 a[16:19], v250 offset:4096\n" \
"v_mul_f32 v252, s31, v252\n" \
"v_mfma_f32_16x16x32_bf16 v[188:191], a[124:127], a[28:31], v[188:191]\n" \
"ds_read_b128 a[20:23], v251 offset:4096\n" \
"v_mul_f32 v253, s31, v253\n" \
"v_mfma_f32_16x16x32_bf16 v[184:187], a[112:115], a[24:27], v[184:187]\n" \
"s_waitcnt lgkmcnt(6)\n" \
"ds_read_b64_tr_b16 a[92:93], v233 offset:41472\n" \
"v_mul_f32 v254, s31, v254\n" \
"v_mfma_f32_16x16x32_bf16 v[184:187], a[116:119], a[28:31], v[184:187]\n" \
"buffer_load_dwordx4 v226, s[48:51], s75 offen offset:2048 lds\n" \
"v_mfma_f32_16x16x32_bf16 v[180:183], a[104:107], a[24:27], v[180:183]\n" \
"ds_read_b64_tr_b16 a[94:95], v233 offset:43520\n" \
"v_cvt_pk_bf16_f32 v46, v243, v252\n" \
"v_mfma_f32_16x16x32_bf16 v[180:183], a[108:111], a[28:31], v[180:183]\n" \
"v_cvt_pk_bf16_f32 v47, v253, v254\n" \
"v_accvgpr_read_b32 v243, a164\n" \
"v_mfma_f32_16x16x32_bf16 v[176:179], a[96:99], a[24:27], v[176:179]\n" \
"v_accvgpr_read_b32 v252, a165\n" \
"v_accvgpr_read_b32 v253, a166\n" \
"v_mfma_f32_16x16x32_bf16 v[176:179], a[100:103], a[28:31], v[176:179]\n" \
"buffer_load_dwordx4 v227, s[48:51], s75 offen offset:3072 lds\n" \
"v_mfma_f32_16x16x32_bf16 v[64:67], a[64:67], a[0:3], v[64:67]\n" \
"s_waitcnt vmcnt(35)\n" \
"s_xor_b32 s75, s75, 0x80\n" \
"ds_read_b128 a[24:27], v250 offset:6144\n" \
"v_mfma_f32_16x16x32_bf16 v[64:67], a[68:71], a[4:7], v[64:67]\n" \
"s_add_u32 m0, s74, 135168\n" \
"ds_read_b128 a[28:31], v251 offset:6144\n" \
"v_accvgpr_read_b32 v254, a167\n" \
"v_mfma_f32_16x16x32_bf16 v[68:71], a[72:75], a[0:3], v[68:71]\n" \
"s_waitcnt lgkmcnt(4)\n" \
"ds_read_b64_tr_b16 a[96:97], v232 offset:33792\n" \
"v_mul_f32 v243, s31, v243\n" \
"v_mfma_f32_16x16x32_bf16 v[68:71], a[76:79], a[4:7], v[68:71]\n" \
"buffer_load_dwordx4 v224, s[48:51], s77 offen offset:0 lds\n" \
"v_mfma_f32_16x16x32_bf16 v[72:75], a[80:83], a[0:3], v[72:75]\n" \
"ds_read_b64_tr_b16 a[98:99], v232 offset:35840\n" \
"v_mul_f32 v252, s31, v252\n" \
"v_mfma_f32_16x16x32_bf16 v[72:75], a[84:87], a[4:7], v[72:75]\n" \
"s_waitcnt lgkmcnt(3)\n" \
"ds_read_b64_tr_b16 a[100:101], v232 offset:41984\n" \
"v_mul_f32 v253, s31, v253\n" \
"v_mfma_f32_16x16x32_bf16 v[76:79], a[88:91], a[0:3], v[76:79]\n" \
"ds_read_b64_tr_b16 a[102:103], v232 offset:44032\n" \
"v_mul_f32 v254, s31, v254\n" \
"v_mfma_f32_16x16x32_bf16 v[76:79], a[92:95], a[4:7], v[76:79]\n" \
"buffer_load_dwordx4 v225, s[48:51], s77 offen offset:1024 lds\n" \
"v_mfma_f32_16x16x32_bf16 v[108:111], a[88:91], a[8:11], v[108:111]\n" \
"ds_read_b64_tr_b16 a[104:105], v233 offset:33792\n" \
"v_cvt_pk_bf16_f32 v60, v243, v252\n" \
"Lpg3348_%=:\n" \
"s_cbranch_execz Lpg5605_%=\n" \
"v_mfma_f32_16x16x32_bf16 v[108:111], a[92:95], a[12:15], v[108:111]\n" \
"ds_read_b64_tr_b16 a[106:107], v233 offset:35840\n" \
"v_cvt_pk_bf16_f32 v61, v253, v254\n" \
"v_mfma_f32_16x16x32_bf16 v[104:107], a[80:83], a[8:11], v[104:107]\n" \
"s_waitcnt lgkmcnt(6)\n" \
"ds_read_b64_tr_b16 a[108:109], v233 offset:41984\n" \
"v_accvgpr_read_b32 v243, a180\n" \
"v_mfma_f32_16x16x32_bf16 v[104:107], a[84:87], a[12:15], v[104:107]\n" \
"buffer_load_dwordx4 v226, s[48:51], s77 offen offset:2048 lds\n" \
"v_mfma_f32_16x16x32_bf16 v[100:103], a[72:75], a[8:11], v[100:103]\n" \
"ds_read_b64_tr_b16 a[110:111], v233 offset:44032\n" \
"v_accvgpr_read_b32 v252, a181\n" \
"v_mfma_f32_16x16x32_bf16 v[100:103], a[76:79], a[12:15], v[100:103]\n" \
"ds_read_b64_tr_b16 a[112:113], v232 offset:34304\n" \
"v_accvgpr_read_b32 v253, a182\n" \
"v_mfma_f32_16x16x32_bf16 v[96:99], a[64:67], a[8:11], v[96:99]\n" \
"ds_read_b64_tr_b16 a[114:115], v232 offset:36352\n" \
"v_accvgpr_read_b32 v254, a183\n" \
"v_mfma_f32_16x16x32_bf16 v[96:99], a[68:71], a[12:15], v[96:99]\n" \
"buffer_load_dwordx4 v227, s[48:51], s77 offen offset:3072 lds\n" \
"v_mfma_f32_16x16x32_bf16 v[128:131], a[64:67], a[16:19], v[128:131]\n" \
"ds_read_b64_tr_b16 a[116:117], v232 offset:42496\n" \
"v_mul_f32 v243, s31, v243\n" \
"v_mfma_f32_16x16x32_bf16 v[128:131], a[68:71], a[20:23], v[128:131]\n" \
"ds_read_b64_tr_b16 a[118:119], v232 offset:44544\n" \
"v_mul_f32 v252, s31, v252\n" \
"v_mfma_f32_16x16x32_bf16 v[132:135], a[72:75], a[16:19], v[132:135]\n" \
"s_xor_b32 s77, s77, 0x80\n" \
"ds_read_b64_tr_b16 a[120:121], v233 offset:34304\n" \
"v_mul_f32 v253, s31, v253\n" \
"v_mfma_f32_16x16x32_bf16 v[132:135], a[76:79], a[20:23], v[132:135]\n" \
"v_mul_f32 v254, s31, v254\n" \
"v_cvt_pk_bf16_f32 v62, v243, v252\n" \
"v_mfma_f32_16x16x32_bf16 v[136:139], a[80:83], a[16:19], v[136:139]\n" \
"ds_read_b64_tr_b16 a[122:123], v233 offset:36352\n" \
"v_cvt_pk_bf16_f32 v63, v253, v254\n" \
"v_mfma_f32_16x16x32_bf16 v[136:139], a[84:87], a[20:23], v[136:139]\n" \
"ds_read_b64_tr_b16 a[124:125], v233 offset:42496\n" \
"v_accvgpr_read_b32 v243, a168\n" \
"v_mfma_f32_16x16x32_bf16 v[140:143], a[88:91], a[16:19], v[140:143]\n" \
"ds_read_b64_tr_b16 a[126:127], v233 offset:44544\n" \
"v_accvgpr_read_b32 v252, a169\n" \
"v_mfma_f32_16x16x32_bf16 v[140:143], a[92:95], a[20:23], v[140:143]\n" \
"buffer_store_dwordx4 v[204:207], v236, s[56:59], s93 offen offset:2048 sc1\n" \
"v_mfma_f32_16x16x32_bf16 v[172:175], a[88:91], a[24:27], v[172:175]\n" \
"v_accvgpr_read_b32 v253, a170\n" \
"v_accvgpr_read_b32 v254, a171\n" \
"v_mfma_f32_16x16x32_bf16 v[172:175], a[92:95], a[28:31], v[172:175]\n" \
"v_mul_f32 v243, s31, v243\n" \
"v_mul_f32 v252, s31, v252\n" \
"v_mfma_f32_16x16x32_bf16 v[168:171], a[80:83], a[24:27], v[168:171]\n" \
"v_mul_f32 v253, s31, v253\n" \
"v_mul_f32 v254, s31, v254\n" \
"v_mfma_f32_16x16x32_bf16 v[168:171], a[84:87], a[28:31], v[168:171]\n" \
"v_cvt_pk_bf16_f32 v20, v243, v252\n" \
"v_cvt_pk_bf16_f32 v21, v253, v254\n" \
"v_mfma_f32_16x16x32_bf16 v[164:167], a[72:75], a[24:27], v[164:167]\n" \
"v_accvgpr_read_b32 v243, a184\n" \
"v_accvgpr_read_b32 v252, a185\n" \
"v_mfma_f32_16x16x32_bf16 v[164:167], a[76:79], a[28:31], v[164:167]\n" \
"v_accvgpr_read_b32 v253, a186\n" \
"v_accvgpr_read_b32 v254, a187\n" \
"v_mfma_f32_16x16x32_bf16 v[160:163], a[64:67], a[24:27], v[160:163]\n" \
"v_mul_f32 v243, s31, v243\n" \
"v_mul_f32 v252, s31, v252\n" \
"v_mfma_f32_16x16x32_bf16 v[160:163], a[68:71], a[28:31], v[160:163]\n" \
"s_waitcnt vmcnt(35) lgkmcnt(0)\n" \
"s_barrier\n" \
"v_mfma_f32_16x16x32_bf16 v[80:83], a[96:99], a[0:3], v[80:83]\n" \
"ds_read_b64_tr_b16 a[64:65], v232 offset:49152\n" \
"v_mul_f32 v253, s31, v253\n" \
"v_mfma_f32_16x16x32_bf16 v[80:83], a[100:103], a[4:7], v[80:83]\n" \
"ds_read_b64_tr_b16 a[66:67], v232 offset:51200\n" \
"v_mul_f32 v254, s31, v254\n" \
"v_mfma_f32_16x16x32_bf16 v[84:87], a[104:107], a[0:3], v[84:87]\n" \
"s_add_u32 m0, s79, 32768\n" \
"ds_read_b64_tr_b16 a[68:69], v232 offset:57344\n" \
"v_cvt_pk_bf16_f32 v22, v243, v252\n" \
"v_mfma_f32_16x16x32_bf16 v[84:87], a[108:111], a[4:7], v[84:87]\n" \
"buffer_load_dwordx4 v228, s[52:55], s78 offen offset:0 lds\n" \
"v_mfma_f32_16x16x32_bf16 v[88:91], a[112:115], a[0:3], v[88:91]\n" \
"ds_read_b64_tr_b16 a[70:71], v232 offset:59392\n" \
"v_cvt_pk_bf16_f32 v23, v253, v254\n" \
"v_mfma_f32_16x16x32_bf16 v[88:91], a[116:119], a[4:7], v[88:91]\n" \
"ds_read_b64_tr_b16 a[72:73], v233 offset:49152\n" \
"v_accvgpr_read_b32 v243, a172\n" \
"v_mfma_f32_16x16x32_bf16 v[92:95], a[120:123], a[0:3], v[92:95]\n" \
"ds_read_b64_tr_b16 a[74:75], v233 offset:51200\n" \
"v_accvgpr_read_b32 v252, a173\n" \
"v_mfma_f32_16x16x32_bf16 v[92:95], a[124:127], a[4:7], v[92:95]\n" \
"buffer_load_dwordx4 v229, s[52:55], s78 offen offset:1024 lds\n" \
"v_mfma_f32_16x16x32_bf16 v[124:127], a[120:123], a[8:11], v[124:127]\n" \
"s_waitcnt vmcnt(35)\n" \
"ds_read_b128 a[0:3], v230 offset:0\n" \
"v_accvgpr_read_b32 v253, a174\n" \
"v_mfma_f32_16x16x32_bf16 v[124:127], a[124:127], a[12:15], v[124:127]\n" \
"ds_read_b128 a[4:7], v231 offset:0\n" \
"v_accvgpr_read_b32 v254, a175\n" \
"v_mfma_f32_16x16x32_bf16 v[120:123], a[112:115], a[8:11], v[120:123]\n" \
"s_xor_b32 s78, s78, 0x180000\n" \
"ds_read_b64_tr_b16 a[76:77], v233 offset:57344\n" \
"v_mul_f32 v243, s31, v243\n" \
"v_mfma_f32_16x16x32_bf16 v[120:123], a[116:119], a[12:15], v[120:123]\n" \
"buffer_load_dwordx4 v228, s[52:55], s89 offen offset:2048 lds\n" \
"v_mfma_f32_16x16x32_bf16 v[116:119], a[104:107], a[8:11], v[116:119]\n" \
"ds_read_b64_tr_b16 a[78:79], v233 offset:59392\n" \
"v_mul_f32 v252, s31, v252\n" \
"v_mfma_f32_16x16x32_bf16 v[116:119], a[108:111], a[12:15], v[116:119]\n" \
"ds_read_b64_tr_b16 a[80:81], v232 offset:49664\n" \
"v_mul_f32 v253, s31, v253\n" \
"v_mfma_f32_16x16x32_bf16 v[112:115], a[96:99], a[8:11], v[112:115]\n" \
"ds_read_b64_tr_b16 a[82:83], v232 offset:51712\n" \
"v_mul_f32 v254, s31, v254\n" \
"v_mfma_f32_16x16x32_bf16 v[112:115], a[100:103], a[12:15], v[112:115]\n" \
"buffer_load_dwordx4 v229, s[52:55], s89 offen offset:3072 lds\n" \
"v_mfma_f32_16x16x32_bf16 v[144:147], a[96:99], a[16:19], v[144:147]\n" \
"s_waitcnt vmcnt(35)\n" \
"s_xor_b32 s89, s89, 0x180000\n" \
"ds_read_b128 a[8:11], v230 offset:2048\n" \
"v_mfma_f32_16x16x32_bf16 v[144:147], a[100:103], a[20:23], v[144:147]\n" \
"s_add_u32 m0, s74, 65536\n" \
"ds_read_b128 a[12:15], v231 offset:2048\n" \
"v_cvt_pk_bf16_f32 v28, v243, v252\n" \
"v_mfma_f32_16x16x32_bf16 v[148:151], a[104:107], a[16:19], v[148:151]\n" \
"s_waitcnt lgkmcnt(6)\n" \
"ds_read_b64_tr_b16 a[84:85], v232 offset:57856\n" \
"v_cvt_pk_bf16_f32 v29, v253, v254\n" \
"v_mfma_f32_16x16x32_bf16 v[148:151], a[108:111], a[20:23], v[148:151]\n" \
"buffer_load_dwordx4 v224, s[48:51], s75 offen offset:0 lds\n" \
"v_mfma_f32_16x16x32_bf16 v[152:155], a[112:115], a[16:19], v[152:155]\n" \
"ds_read_b64_tr_b16 a[86:87], v232 offset:59904\n" \
"v_accvgpr_read_b32 v243, a188\n" \
"v_mfma_f32_16x16x32_bf16 v[152:155], a[116:119], a[20:23], v[152:155]\n" \
"ds_read_b64_tr_b16 a[88:89], v233 offset:49664\n" \
"v_accvgpr_read_b32 v252, a189\n" \
"v_mfma_f32_16x16x32_bf16 v[156:159], a[120:123], a[16:19], v[156:159]\n" \
"ds_read_b64_tr_b16 a[90:91], v233 offset:51712\n" \
"v_accvgpr_read_b32 v253, a190\n" \
"v_mfma_f32_16x16x32_bf16 v[156:159], a[124:127], a[20:23], v[156:159]\n" \
"buffer_load_dwordx4 v225, s[48:51], s75 offen offset:1024 lds\n" \
"v_mfma_f32_16x16x32_bf16 v[188:191], a[120:123], a[24:27], v[188:191]\n" \
"s_waitcnt vmcnt(35)\n" \
"ds_read_b128 a[16:19], v230 offset:4096\n" \
"v_accvgpr_read_b32 v254, a191\n" \
"v_mfma_f32_16x16x32_bf16 v[188:191], a[124:127], a[28:31], v[188:191]\n" \
"ds_read_b128 a[20:23], v231 offset:4096\n" \
"v_mul_f32 v243, s31, v243\n" \
"v_mfma_f32_16x16x32_bf16 v[184:187], a[112:115], a[24:27], v[184:187]\n" \
"s_waitcnt lgkmcnt(6)\n" \
"ds_read_b64_tr_b16 a[92:93], v233 offset:57856\n" \
"v_mul_f32 v252, s31, v252\n" \
"v_mfma_f32_16x16x32_bf16 v[184:187], a[116:119], a[28:31], v[184:187]\n" \
"buffer_load_dwordx4 v226, s[48:51], s75 offen offset:2048 lds\n" \
"v_mfma_f32_16x16x32_bf16 v[180:183], a[104:107], a[24:27], v[180:183]\n" \
"ds_read_b64_tr_b16 a[94:95], v233 offset:59904\n" \
"v_mul_f32 v253, s31, v253\n" \
"v_mfma_f32_16x16x32_bf16 v[180:183], a[108:111], a[28:31], v[180:183]\n" \
"v_mul_f32 v254, s31, v254\n" \
"v_cvt_pk_bf16_f32 v30, v243, v252\n" \
"v_mfma_f32_16x16x32_bf16 v[176:179], a[96:99], a[24:27], v[176:179]\n" \
"v_cvt_pk_bf16_f32 v31, v253, v254\n" \
"v_accvgpr_read_b32 v243, a128\n" \
"v_mfma_f32_16x16x32_bf16 v[176:179], a[100:103], a[28:31], v[176:179]\n" \
"buffer_load_dwordx4 v227, s[48:51], s75 offen offset:3072 lds\n" \
"v_mfma_f32_16x16x32_bf16 v[64:67], a[64:67], a[0:3], v[64:67]\n" \
"s_waitcnt vmcnt(35)\n" \
"s_xor_b32 s75, s75, 0x180\n" \
"ds_read_b128 a[24:27], v230 offset:6144\n" \
"v_mfma_f32_16x16x32_bf16 v[64:67], a[68:71], a[4:7], v[64:67]\n" \
"s_add_u32 m0, s74, 69632\n" \
"ds_read_b128 a[28:31], v231 offset:6144\n" \
"v_accvgpr_read_b32 v252, a129\n" \
"v_mfma_f32_16x16x32_bf16 v[68:71], a[72:75], a[0:3], v[68:71]\n" \
"s_waitcnt lgkmcnt(4)\n" \
"ds_read_b64_tr_b16 a[96:97], v232 offset:50176\n" \
"v_accvgpr_read_b32 v253, a130\n" \
"v_mfma_f32_16x16x32_bf16 v[68:71], a[76:79], a[4:7], v[68:71]\n" \
"buffer_load_dwordx4 v224, s[48:51], s77 offen offset:0 lds\n" \
"v_mfma_f32_16x16x32_bf16 v[72:75], a[80:83], a[0:3], v[72:75]\n" \
"ds_read_b64_tr_b16 a[98:99], v232 offset:52224\n" \
"v_accvgpr_read_b32 v254, a131\n" \
"v_mfma_f32_16x16x32_bf16 v[72:75], a[84:87], a[4:7], v[72:75]\n" \
"s_waitcnt lgkmcnt(3)\n" \
"ds_read_b64_tr_b16 a[100:101], v232 offset:58368\n" \
"v_mul_f32 v243, s31, v243\n" \
"v_mfma_f32_16x16x32_bf16 v[76:79], a[88:91], a[0:3], v[76:79]\n" \
"ds_read_b64_tr_b16 a[102:103], v232 offset:60416\n" \
"v_mul_f32 v252, s31, v252\n" \
"v_mfma_f32_16x16x32_bf16 v[76:79], a[92:95], a[4:7], v[76:79]\n" \
"buffer_load_dwordx4 v225, s[48:51], s77 offen offset:1024 lds\n" \
"v_mfma_f32_16x16x32_bf16 v[108:111], a[88:91], a[8:11], v[108:111]\n" \
"ds_read_b64_tr_b16 a[104:105], v233 offset:50176\n" \
"v_mul_f32 v253, s31, v253\n" \
"v_mfma_f32_16x16x32_bf16 v[108:111], a[92:95], a[12:15], v[108:111]\n" \
"ds_read_b64_tr_b16 a[106:107], v233 offset:52224\n" \
"v_mul_f32 v254, s31, v254\n" \
"v_mfma_f32_16x16x32_bf16 v[104:107], a[80:83], a[8:11], v[104:107]\n" \
"s_waitcnt lgkmcnt(6)\n" \
"ds_read_b64_tr_b16 a[108:109], v233 offset:58368\n" \
"v_cvt_pk_bf16_f32 v0, v243, v252\n" \
"v_mfma_f32_16x16x32_bf16 v[104:107], a[84:87], a[12:15], v[104:107]\n" \
"buffer_load_dwordx4 v226, s[48:51], s77 offen offset:2048 lds\n" \
"v_mfma_f32_16x16x32_bf16 v[100:103], a[72:75], a[8:11], v[100:103]\n" \
"ds_read_b64_tr_b16 a[110:111], v233 offset:60416\n" \
"v_cvt_pk_bf16_f32 v1, v253, v254\n" \
"v_mfma_f32_16x16x32_bf16 v[100:103], a[76:79], a[12:15], v[100:103]\n" \
"ds_read_b64_tr_b16 a[112:113], v232 offset:50688\n" \
"v_accvgpr_read_b32 v243, a144\n" \
"v_mfma_f32_16x16x32_bf16 v[96:99], a[64:67], a[8:11], v[96:99]\n" \
"ds_read_b64_tr_b16 a[114:115], v232 offset:52736\n" \
"v_accvgpr_read_b32 v252, a145\n" \
"v_mfma_f32_16x16x32_bf16 v[96:99], a[68:71], a[12:15], v[96:99]\n" \
"buffer_load_dwordx4 v227, s[48:51], s77 offen offset:3072 lds\n" \
"v_mfma_f32_16x16x32_bf16 v[128:131], a[64:67], a[16:19], v[128:131]\n" \
"ds_read_b64_tr_b16 a[116:117], v232 offset:58880\n" \
"v_accvgpr_read_b32 v253, a146\n" \
"v_mfma_f32_16x16x32_bf16 v[128:131], a[68:71], a[20:23], v[128:131]\n" \
"ds_read_b64_tr_b16 a[118:119], v232 offset:60928\n" \
"v_accvgpr_read_b32 v254, a147\n" \
"v_mfma_f32_16x16x32_bf16 v[132:135], a[72:75], a[16:19], v[132:135]\n" \
"s_xor_b32 s77, s77, 0x180\n" \
"ds_read_b64_tr_b16 a[120:121], v233 offset:50688\n" \
"v_mul_f32 v243, s31, v243\n" \
"v_mfma_f32_16x16x32_bf16 v[132:135], a[76:79], a[20:23], v[132:135]\n" \
"v_mul_f32 v252, s31, v252\n" \
"v_mul_f32 v253, s31, v253\n" \
"v_mfma_f32_16x16x32_bf16 v[136:139], a[80:83], a[16:19], v[136:139]\n" \
"ds_read_b64_tr_b16 a[122:123], v233 offset:52736\n" \
"v_mul_f32 v254, s31, v254\n" \
"v_mfma_f32_16x16x32_bf16 v[136:139], a[84:87], a[20:23], v[136:139]\n" \
"ds_read_b64_tr_b16 a[124:125], v233 offset:58880\n" \
"v_cvt_pk_bf16_f32 v2, v243, v252\n" \
"v_mfma_f32_16x16x32_bf16 v[140:143], a[88:91], a[16:19], v[140:143]\n" \
"ds_read_b64_tr_b16 a[126:127], v233 offset:60928\n" \
"v_cvt_pk_bf16_f32 v3, v253, v254\n" \
"v_mfma_f32_16x16x32_bf16 v[140:143], a[92:95], a[20:23], v[140:143]\n" \
"buffer_store_dwordx4 v[220:223], v236, s[56:59], s93 offen offset:2304 sc1\n" \
"v_mfma_f32_16x16x32_bf16 v[172:175], a[88:91], a[24:27], v[172:175]\n" \
"v_accvgpr_read_b32 v243, a132\n" \
"v_accvgpr_read_b32 v252, a133\n" \
"v_mfma_f32_16x16x32_bf16 v[172:175], a[92:95], a[28:31], v[172:175]\n" \
"v_accvgpr_read_b32 v253, a134\n" \
"v_accvgpr_read_b32 v254, a135\n" \
"v_mfma_f32_16x16x32_bf16 v[168:171], a[80:83], a[24:27], v[168:171]\n" \
"v_mul_f32 v243, s31, v243\n" \
"v_mul_f32 v252, s31, v252\n" \
"v_mfma_f32_16x16x32_bf16 v[168:171], a[84:87], a[28:31], v[168:171]\n" \
"v_mul_f32 v253, s31, v253\n" \
"v_mul_f32 v254, s31, v254\n" \
"v_mfma_f32_16x16x32_bf16 v[164:167], a[72:75], a[24:27], v[164:167]\n" \
"v_cvt_pk_bf16_f32 v4, v243, v252\n" \
"v_cvt_pk_bf16_f32 v5, v253, v254\n" \
"v_mfma_f32_16x16x32_bf16 v[164:167], a[76:79], a[28:31], v[164:167]\n" \
"v_accvgpr_read_b32 v243, a148\n" \
"v_accvgpr_read_b32 v252, a149\n" \
"v_mfma_f32_16x16x32_bf16 v[160:163], a[64:67], a[24:27], v[160:163]\n" \
"v_accvgpr_read_b32 v253, a150\n" \
"v_accvgpr_read_b32 v254, a151\n" \
"v_mfma_f32_16x16x32_bf16 v[160:163], a[68:71], a[28:31], v[160:163]\n" \
"s_waitcnt vmcnt(35) lgkmcnt(0)\n" \
"s_barrier\n" \
"v_mfma_f32_16x16x32_bf16 v[80:83], a[96:99], a[0:3], v[80:83]\n" \
"ds_read_b64_tr_b16 a[64:65], v232 offset:0\n" \
"v_mul_f32 v243, s31, v243\n" \
"v_mfma_f32_16x16x32_bf16 v[80:83], a[100:103], a[4:7], v[80:83]\n" \
"ds_read_b64_tr_b16 a[66:67], v232 offset:2048\n" \
"v_mul_f32 v252, s31, v252\n" \
"v_mfma_f32_16x16x32_bf16 v[84:87], a[104:107], a[0:3], v[84:87]\n" \
"s_add_u32 m0, s79, 49152\n" \
"ds_read_b64_tr_b16 a[68:69], v232 offset:8192\n" \
"v_mul_f32 v253, s31, v253\n" \
"v_mfma_f32_16x16x32_bf16 v[84:87], a[108:111], a[4:7], v[84:87]\n" \
"buffer_load_dwordx4 v228, s[52:55], s78 offen offset:0 lds\n" \
"v_mfma_f32_16x16x32_bf16 v[88:91], a[112:115], a[0:3], v[88:91]\n" \
"ds_read_b64_tr_b16 a[70:71], v232 offset:10240\n" \
"v_mul_f32 v254, s31, v254\n" \
"v_mfma_f32_16x16x32_bf16 v[88:91], a[116:119], a[4:7], v[88:91]\n" \
"ds_read_b64_tr_b16 a[72:73], v233 offset:0\n" \
"v_cvt_pk_bf16_f32 v6, v243, v252\n" \
"v_mfma_f32_16x16x32_bf16 v[92:95], a[120:123], a[0:3], v[92:95]\n" \
"ds_read_b64_tr_b16 a[74:75], v233 offset:2048\n" \
"v_cvt_pk_bf16_f32 v7, v253, v254\n" \
"v_mfma_f32_16x16x32_bf16 v[92:95], a[124:127], a[4:7], v[92:95]\n" \
"buffer_load_dwordx4 v229, s[52:55], s78 offen offset:1024 lds\n" \
"v_mfma_f32_16x16x32_bf16 v[124:127], a[120:123], a[8:11], v[124:127]\n" \
"s_waitcnt vmcnt(35)\n" \
"ds_read_b128 a[0:3], v230 offset:32768\n" \
"v_accvgpr_read_b32 v243, a136\n" \
"v_mfma_f32_16x16x32_bf16 v[124:127], a[124:127], a[12:15], v[124:127]\n" \
"ds_read_b128 a[4:7], v231 offset:32768\n" \
"v_accvgpr_read_b32 v252, a137\n" \
"v_mfma_f32_16x16x32_bf16 v[120:123], a[112:115], a[8:11], v[120:123]\n" \
"s_xor_b32 s78, s78, 0x300000\n" \
"ds_read_b64_tr_b16 a[76:77], v233 offset:8192\n" \
"v_accvgpr_read_b32 v253, a138\n" \
"v_mfma_f32_16x16x32_bf16 v[120:123], a[116:119], a[12:15], v[120:123]\n" \
"buffer_load_dwordx4 v228, s[52:55], s89 offen offset:2048 lds\n" \
"v_mfma_f32_16x16x32_bf16 v[116:119], a[104:107], a[8:11], v[116:119]\n" \
"ds_read_b64_tr_b16 a[78:79], v233 offset:10240\n" \
"v_accvgpr_read_b32 v254, a139\n" \
"v_mfma_f32_16x16x32_bf16 v[116:119], a[108:111], a[12:15], v[116:119]\n" \
"ds_read_b64_tr_b16 a[80:81], v232 offset:512\n" \
"v_mul_f32 v243, s31, v243\n" \
"v_mfma_f32_16x16x32_bf16 v[112:115], a[96:99], a[8:11], v[112:115]\n" \
"ds_read_b64_tr_b16 a[82:83], v232 offset:2560\n" \
"v_mul_f32 v252, s31, v252\n" \
"v_mfma_f32_16x16x32_bf16 v[112:115], a[100:103], a[12:15], v[112:115]\n" \
"buffer_load_dwordx4 v229, s[52:55], s89 offen offset:3072 lds\n" \
"v_mfma_f32_16x16x32_bf16 v[144:147], a[96:99], a[16:19], v[144:147]\n" \
"s_waitcnt vmcnt(35)\n" \
"s_xor_b32 s89, s89, 0x300000\n" \
"ds_read_b128 a[8:11], v230 offset:34816\n" \
"v_mfma_f32_16x16x32_bf16 v[144:147], a[100:103], a[20:23], v[144:147]\n" \
"s_add_u32 m0, s74, 98304\n" \
"ds_read_b128 a[12:15], v231 offset:34816\n" \
"v_mul_f32 v253, s31, v253\n" \
"v_mfma_f32_16x16x32_bf16 v[148:151], a[104:107], a[16:19], v[148:151]\n" \
"s_waitcnt lgkmcnt(6)\n" \
"ds_read_b64_tr_b16 a[84:85], v232 offset:8704\n" \
"v_mul_f32 v254, s31, v254\n" \
"v_mfma_f32_16x16x32_bf16 v[148:151], a[108:111], a[20:23], v[148:151]\n" \
"buffer_load_dwordx4 v224, s[48:51], s75 offen offset:0 lds\n" \
"v_mfma_f32_16x16x32_bf16 v[152:155], a[112:115], a[16:19], v[152:155]\n" \
"ds_read_b64_tr_b16 a[86:87], v232 offset:10752\n" \
"v_cvt_pk_bf16_f32 v8, v243, v252\n" \
"v_mfma_f32_16x16x32_bf16 v[152:155], a[116:119], a[20:23], v[152:155]\n" \
"ds_read_b64_tr_b16 a[88:89], v233 offset:512\n" \
"v_cvt_pk_bf16_f32 v9, v253, v254\n" \
"v_mfma_f32_16x16x32_bf16 v[156:159], a[120:123], a[16:19], v[156:159]\n" \
"ds_read_b64_tr_b16 a[90:91], v233 offset:2560\n" \
"v_accvgpr_read_b32 v243, a152\n" \
"v_mfma_f32_16x16x32_bf16 v[156:159], a[124:127], a[20:23], v[156:159]\n" \
"buffer_load_dwordx4 v225, s[48:51], s75 offen offset:1024 lds\n" \
"v_mfma_f32_16x16x32_bf16 v[188:191], a[120:123], a[24:27], v[188:191]\n" \
"s_waitcnt vmcnt(35)\n" \
"ds_read_b128 a[16:19], v230 offset:36864\n" \
"v_accvgpr_read_b32 v252, a153\n" \
"v_mfma_f32_16x16x32_bf16 v[188:191], a[124:127], a[28:31], v[188:191]\n" \
"ds_read_b128 a[20:23], v231 offset:36864\n" \
"v_accvgpr_read_b32 v253, a154\n" \
"v_mfma_f32_16x16x32_bf16 v[184:187], a[112:115], a[24:27], v[184:187]\n" \
"s_waitcnt lgkmcnt(6)\n" \
"ds_read_b64_tr_b16 a[92:93], v233 offset:8704\n" \
"v_accvgpr_read_b32 v254, a155\n" \
"v_mfma_f32_16x16x32_bf16 v[184:187], a[116:119], a[28:31], v[184:187]\n" \
"buffer_load_dwordx4 v226, s[48:51], s75 offen offset:2048 lds\n" \
"v_mfma_f32_16x16x32_bf16 v[180:183], a[104:107], a[24:27], v[180:183]\n" \
"ds_read_b64_tr_b16 a[94:95], v233 offset:10752\n" \
"v_mul_f32 v243, s31, v243\n" \
"v_mfma_f32_16x16x32_bf16 v[180:183], a[108:111], a[28:31], v[180:183]\n" \
"v_mul_f32 v252, s31, v252\n" \
"v_mul_f32 v253, s31, v253\n" \
"v_mfma_f32_16x16x32_bf16 v[176:179], a[96:99], a[24:27], v[176:179]\n" \
"v_mul_f32 v254, s31, v254\n" \
"v_cvt_pk_bf16_f32 v10, v243, v252\n" \
"v_mfma_f32_16x16x32_bf16 v[176:179], a[100:103], a[28:31], v[176:179]\n" \
"buffer_load_dwordx4 v227, s[48:51], s75 offen offset:3072 lds\n" \
"v_mfma_f32_16x16x32_bf16 v[64:67], a[64:67], a[0:3], v[64:67]\n" \
"s_waitcnt vmcnt(35)\n" \
"s_xor_b32 s75, s75, 0x300\n" \
"ds_read_b128 a[24:27], v230 offset:38912\n" \
"v_mfma_f32_16x16x32_bf16 v[64:67], a[68:71], a[4:7], v[64:67]\n" \
"s_add_u32 m0, s74, 102400\n" \
"ds_read_b128 a[28:31], v231 offset:38912\n" \
"v_cvt_pk_bf16_f32 v11, v253, v254\n" \
"v_mfma_f32_16x16x32_bf16 v[68:71], a[72:75], a[0:3], v[68:71]\n" \
"s_waitcnt lgkmcnt(4)\n" \
"ds_read_b64_tr_b16 a[96:97], v232 offset:1024\n" \
"v_accvgpr_read_b32 v243, a140\n" \
"v_mfma_f32_16x16x32_bf16 v[68:71], a[76:79], a[4:7], v[68:71]\n" \
"buffer_load_dwordx4 v224, s[48:51], s77 offen offset:0 lds\n" \
"v_mfma_f32_16x16x32_bf16 v[72:75], a[80:83], a[0:3], v[72:75]\n" \
"ds_read_b64_tr_b16 a[98:99], v232 offset:3072\n" \
"v_accvgpr_read_b32 v252, a141\n" \
"v_mfma_f32_16x16x32_bf16 v[72:75], a[84:87], a[4:7], v[72:75]\n" \
"s_waitcnt lgkmcnt(3)\n" \
"ds_read_b64_tr_b16 a[100:101], v232 offset:9216\n" \
"v_accvgpr_read_b32 v253, a142\n" \
"v_mfma_f32_16x16x32_bf16 v[76:79], a[88:91], a[0:3], v[76:79]\n" \
"ds_read_b64_tr_b16 a[102:103], v232 offset:11264\n" \
"v_accvgpr_read_b32 v254, a143\n" \
"v_mfma_f32_16x16x32_bf16 v[76:79], a[92:95], a[4:7], v[76:79]\n" \
"buffer_load_dwordx4 v225, s[48:51], s77 offen offset:1024 lds\n" \
"v_mfma_f32_16x16x32_bf16 v[108:111], a[88:91], a[8:11], v[108:111]\n" \
"ds_read_b64_tr_b16 a[104:105], v233 offset:1024\n" \
"v_mul_f32 v243, s31, v243\n" \
"v_mfma_f32_16x16x32_bf16 v[108:111], a[92:95], a[12:15], v[108:111]\n" \
"ds_read_b64_tr_b16 a[106:107], v233 offset:3072\n" \
"v_mul_f32 v252, s31, v252\n" \
"v_mfma_f32_16x16x32_bf16 v[104:107], a[80:83], a[8:11], v[104:107]\n" \
"s_waitcnt lgkmcnt(6)\n" \
"ds_read_b64_tr_b16 a[108:109], v233 offset:9216\n" \
"v_mul_f32 v253, s31, v253\n" \
"v_mfma_f32_16x16x32_bf16 v[104:107], a[84:87], a[12:15], v[104:107]\n" \
"buffer_load_dwordx4 v226, s[48:51], s77 offen offset:2048 lds\n" \
"v_mfma_f32_16x16x32_bf16 v[100:103], a[72:75], a[8:11], v[100:103]\n" \
"ds_read_b64_tr_b16 a[110:111], v233 offset:11264\n" \
"v_mul_f32 v254, s31, v254\n" \
"v_mfma_f32_16x16x32_bf16 v[100:103], a[76:79], a[12:15], v[100:103]\n" \
"ds_read_b64_tr_b16 a[112:113], v232 offset:1536\n" \
"v_cvt_pk_bf16_f32 v12, v243, v252\n" \
"v_mfma_f32_16x16x32_bf16 v[96:99], a[64:67], a[8:11], v[96:99]\n" \
"ds_read_b64_tr_b16 a[114:115], v232 offset:3584\n" \
"v_cvt_pk_bf16_f32 v13, v253, v254\n" \
"v_mfma_f32_16x16x32_bf16 v[96:99], a[68:71], a[12:15], v[96:99]\n" \
"buffer_load_dwordx4 v227, s[48:51], s77 offen offset:3072 lds\n" \
"v_mfma_f32_16x16x32_bf16 v[128:131], a[64:67], a[16:19], v[128:131]\n" \
"ds_read_b64_tr_b16 a[116:117], v232 offset:9728\n" \
"v_accvgpr_read_b32 v243, a156\n" \
"v_mfma_f32_16x16x32_bf16 v[128:131], a[68:71], a[20:23], v[128:131]\n" \
"ds_read_b64_tr_b16 a[118:119], v232 offset:11776\n" \
"v_accvgpr_read_b32 v252, a157\n" \
"v_mfma_f32_16x16x32_bf16 v[132:135], a[72:75], a[16:19], v[132:135]\n" \
"s_xor_b32 s77, s77, 0x300\n" \
"ds_read_b64_tr_b16 a[120:121], v233 offset:1536\n" \
"v_accvgpr_read_b32 v253, a158\n" \
"v_mfma_f32_16x16x32_bf16 v[132:135], a[76:79], a[20:23], v[132:135]\n" \
"v_accvgpr_read_b32 v254, a159\n" \
"v_mul_f32 v243, s31, v243\n" \
"v_mfma_f32_16x16x32_bf16 v[136:139], a[80:83], a[16:19], v[136:139]\n" \
"ds_read_b64_tr_b16 a[122:123], v233 offset:3584\n" \
"v_mul_f32 v252, s31, v252\n" \
"v_mfma_f32_16x16x32_bf16 v[136:139], a[84:87], a[20:23], v[136:139]\n" \
"ds_read_b64_tr_b16 a[124:125], v233 offset:9728\n" \
"v_mul_f32 v253, s31, v253\n" \
"v_mfma_f32_16x16x32_bf16 v[140:143], a[88:91], a[16:19], v[140:143]\n" \
"s_add_u32 s93, s87, 17858560\n" \
"ds_read_b64_tr_b16 a[126:127], v233 offset:11776\n" \
"v_mul_f32 v254, s31, v254\n" \
"v_mfma_f32_16x16x32_bf16 v[140:143], a[92:95], a[20:23], v[140:143]\n" \
"buffer_store_dwordx4 a[224:227], v234, s[56:59], s93 offen offset:0 sc1\n" \
"v_mfma_f32_16x16x32_bf16 v[172:175], a[88:91], a[24:27], v[172:175]\n" \
"v_cvt_pk_bf16_f32 v14, v243, v252\n" \
"v_cvt_pk_bf16_f32 v15, v253, v254\n" \
"v_mfma_f32_16x16x32_bf16 v[172:175], a[92:95], a[28:31], v[172:175]\n" \
"v_accvgpr_write_b32 a56, v244\n" \
"v_accvgpr_write_b32 a57, v245\n" \
"v_mfma_f32_16x16x32_bf16 v[168:171], a[80:83], a[24:27], v[168:171]\n" \
"v_accvgpr_write_b32 a59, v247\n" \
"v_accvgpr_write_b32 a60, v248\n" \
"v_mfma_f32_16x16x32_bf16 v[168:171], a[84:87], a[28:31], v[168:171]\n" \
"v_accvgpr_write_b32 a61, v249\n" \
"v_accvgpr_write_b32 a48, 0\n" \
"v_mfma_f32_16x16x32_bf16 v[164:167], a[72:75], a[24:27], v[164:167]\n" \
"v_accvgpr_write_b32 a49, 0\n" \
"v_accvgpr_write_b32 a50, 0\n" \
"v_mfma_f32_16x16x32_bf16 v[164:167], a[76:79], a[28:31], v[164:167]\n" \
"v_accvgpr_write_b32 a51, 0\n" \
"v_accvgpr_write_b32 a52, 0\n" \
"v_mfma_f32_16x16x32_bf16 v[160:163], a[64:67], a[24:27], v[160:163]\n" \
"v_accvgpr_write_b32 a53, 0\n" \
"v_mfma_f32_16x16x32_bf16 v[160:163], a[68:71], a[28:31], v[160:163]\n" \
"s_waitcnt vmcnt(35) lgkmcnt(0)\n" \
"s_barrier\n" \
"v_mfma_f32_16x16x32_bf16 v[80:83], a[96:99], a[0:3], v[80:83]\n" \
"ds_read_b64_tr_b16 a[64:65], v232 offset:16384\n" \
"v_mfma_f32_16x16x32_bf16 v[80:83], a[100:103], a[4:7], v[80:83]\n" \
"ds_read_b64_tr_b16 a[66:67], v232 offset:18432\n" \
"v_mfma_f32_16x16x32_bf16 v[84:87], a[104:107], a[0:3], v[84:87]\n" \
"s_add_u32 m0, s79, 0\n" \
"ds_read_b64_tr_b16 a[68:69], v232 offset:24576\n" \
"v_mfma_f32_16x16x32_bf16 v[84:87], a[108:111], a[4:7], v[84:87]\n" \
"buffer_load_dwordx4 v228, s[52:55], s78 offen offset:0 lds\n" \
"v_mfma_f32_16x16x32_bf16 v[88:91], a[112:115], a[0:3], v[88:91]\n" \
"ds_read_b64_tr_b16 a[70:71], v232 offset:26624\n" \
"v_mfma_f32_16x16x32_bf16 v[88:91], a[116:119], a[4:7], v[88:91]\n" \
"ds_read_b64_tr_b16 a[72:73], v233 offset:16384\n" \
"v_mfma_f32_16x16x32_bf16 v[92:95], a[120:123], a[0:3], v[92:95]\n" \
"ds_read_b64_tr_b16 a[74:75], v233 offset:18432\n" \
"v_mfma_f32_16x16x32_bf16 v[92:95], a[124:127], a[4:7], v[92:95]\n" \
"buffer_load_dwordx4 v229, s[52:55], s78 offen offset:1024 lds\n" \
"v_mfma_f32_16x16x32_bf16 v[124:127], a[120:123], a[8:11], v[124:127]\n" \
"s_waitcnt vmcnt(35)\n" \
"ds_read_b128 a[0:3], v250 offset:0\n" \
"v_mfma_f32_16x16x32_bf16 v[124:127], a[124:127], a[12:15], v[124:127]\n" \
"ds_read_b128 a[4:7], v251 offset:0\n" \
"v_mfma_f32_16x16x32_bf16 v[120:123], a[112:115], a[8:11], v[120:123]\n" \
"s_xor_b32 s78, s78, 0x80000\n" \
"ds_read_b64_tr_b16 a[76:77], v233 offset:24576\n" \
"v_mfma_f32_16x16x32_bf16 v[120:123], a[116:119], a[12:15], v[120:123]\n" \
"buffer_load_dwordx4 v228, s[52:55], s89 offen offset:2048 lds\n" \
"v_mfma_f32_16x16x32_bf16 v[116:119], a[104:107], a[8:11], v[116:119]\n" \
"ds_read_b64_tr_b16 a[78:79], v233 offset:26624\n" \
"v_mfma_f32_16x16x32_bf16 v[116:119], a[108:111], a[12:15], v[116:119]\n" \
"ds_read_b64_tr_b16 a[80:81], v232 offset:16896\n" \
"v_mfma_f32_16x16x32_bf16 v[112:115], a[96:99], a[8:11], v[112:115]\n" \
"ds_read_b64_tr_b16 a[82:83], v232 offset:18944\n" \
"v_mfma_f32_16x16x32_bf16 v[112:115], a[100:103], a[12:15], v[112:115]\n" \
"buffer_load_dwordx4 v229, s[52:55], s89 offen offset:3072 lds\n" \
"v_mfma_f32_16x16x32_bf16 v[144:147], a[96:99], a[16:19], v[144:147]\n" \
"s_waitcnt vmcnt(35)\n" \
"s_xor_b32 s89, s89, 0x80000\n" \
"ds_read_b128 a[8:11], v250 offset:2048\n" \
"v_mfma_f32_16x16x32_bf16 v[144:147], a[100:103], a[20:23], v[144:147]\n" \
"s_add_u32 m0, s74, 131072\n" \
"ds_read_b128 a[12:15], v251 offset:2048\n" \
"v_mfma_f32_16x16x32_bf16 v[148:151], a[104:107], a[16:19], v[148:151]\n" \
"s_waitcnt lgkmcnt(6)\n" \
"ds_read_b64_tr_b16 a[84:85], v232 offset:25088\n" \
"v_mfma_f32_16x16x32_bf16 v[148:151], a[108:111], a[20:23], v[148:151]\n" \
"buffer_load_dwordx4 v224, s[48:51], s75 offen offset:0 lds\n" \
"v_mfma_f32_16x16x32_bf16 v[152:155], a[112:115], a[16:19], v[152:155]\n" \
"ds_read_b64_tr_b16 a[86:87], v232 offset:27136\n" \
"v_mfma_f32_16x16x32_bf16 v[152:155], a[116:119], a[20:23], v[152:155]\n" \
"ds_read_b64_tr_b16 a[88:89], v233 offset:16896\n" \
"v_mfma_f32_16x16x32_bf16 v[156:159], a[120:123], a[16:19], v[156:159]\n" \
"ds_read_b64_tr_b16 a[90:91], v233 offset:18944\n" \
"v_mfma_f32_16x16x32_bf16 v[156:159], a[124:127], a[20:23], v[156:159]\n" \
"buffer_load_dwordx4 v225, s[48:51], s75 offen offset:1024 lds\n" \
"v_mfma_f32_16x16x32_bf16 v[188:191], a[120:123], a[24:27], v[188:191]\n" \
"s_waitcnt vmcnt(35)\n" \
"ds_read_b128 a[16:19], v250 offset:4096\n" \
"v_mfma_f32_16x16x32_bf16 v[188:191], a[124:127], a[28:31], v[188:191]\n" \
"ds_read_b128 a[20:23], v251 offset:4096\n" \
"v_mfma_f32_16x16x32_bf16 v[184:187], a[112:115], a[24:27], v[184:187]\n" \
"s_waitcnt lgkmcnt(6)\n" \
"ds_read_b64_tr_b16 a[92:93], v233 offset:25088\n" \
"v_mfma_f32_16x16x32_bf16 v[184:187], a[116:119], a[28:31], v[184:187]\n" \
"buffer_load_dwordx4 v226, s[48:51], s75 offen offset:2048 lds\n" \
"v_mfma_f32_16x16x32_bf16 v[180:183], a[104:107], a[24:27], v[180:183]\n" \
"ds_read_b64_tr_b16 a[94:95], v233 offset:27136\n" \
"v_mfma_f32_16x16x32_bf16 v[180:183], a[108:111], a[28:31], v[180:183]\n" \
"v_mfma_f32_16x16x32_bf16 v[176:179], a[96:99], a[24:27], v[176:179]\n" \
"v_mfma_f32_16x16x32_bf16 v[176:179], a[100:103], a[28:31], v[176:179]\n" \
"buffer_load_dwordx4 v227, s[48:51], s75 offen offset:3072 lds\n" \
"v_mfma_f32_16x16x32_bf16 v[64:67], a[64:67], a[0:3], v[64:67]\n" \
"s_waitcnt vmcnt(35)\n" \
"s_xor_b32 s75, s75, 0x80\n" \
"ds_read_b128 a[24:27], v250 offset:6144\n" \
"v_mfma_f32_16x16x32_bf16 v[64:67], a[68:71], a[4:7], v[64:67]\n" \
"s_add_u32 m0, s74, 135168\n" \
"ds_read_b128 a[28:31], v251 offset:6144\n" \
"v_mfma_f32_16x16x32_bf16 v[68:71], a[72:75], a[0:3], v[68:71]\n" \
"s_waitcnt lgkmcnt(2)\n" \
"ds_read_b64_tr_b16 a[96:97], v232 offset:17408\n" \
"v_mfma_f32_16x16x32_bf16 v[68:71], a[76:79], a[4:7], v[68:71]\n" \
"buffer_load_dwordx4 v224, s[48:51], s77 offen offset:0 lds\n" \
"v_mfma_f32_16x16x32_bf16 v[72:75], a[80:83], a[0:3], v[72:75]\n" \
"ds_read_b64_tr_b16 a[98:99], v232 offset:19456\n" \
"v_mfma_f32_16x16x32_bf16 v[72:75], a[84:87], a[4:7], v[72:75]\n" \
"ds_read_b64_tr_b16 a[100:101], v232 offset:25600\n" \
"v_mfma_f32_16x16x32_bf16 v[76:79], a[88:91], a[0:3], v[76:79]\n" \
"ds_read_b64_tr_b16 a[102:103], v232 offset:27648\n" \
"v_mfma_f32_16x16x32_bf16 v[76:79], a[92:95], a[4:7], v[76:79]\n" \
"buffer_load_dwordx4 v225, s[48:51], s77 offen offset:1024 lds\n" \
"v_mfma_f32_16x16x32_bf16 v[108:111], a[88:91], a[8:11], v[108:111]\n" \
"ds_read_b64_tr_b16 a[104:105], v233 offset:17408\n" \
"v_mfma_f32_16x16x32_bf16 v[108:111], a[92:95], a[12:15], v[108:111]\n" \
"ds_read_b64_tr_b16 a[106:107], v233 offset:19456\n" \
"v_mfma_f32_16x16x32_bf16 v[104:107], a[80:83], a[8:11], v[104:107]\n" \
"s_waitcnt lgkmcnt(6)\n" \
"ds_read_b64_tr_b16 a[108:109], v233 offset:25600\n" \
"v_mfma_f32_16x16x32_bf16 v[104:107], a[84:87], a[12:15], v[104:107]\n" \
"buffer_load_dwordx4 v226, s[48:51], s77 offen offset:2048 lds\n" \
"v_mfma_f32_16x16x32_bf16 v[100:103], a[72:75], a[8:11], v[100:103]\n" \
"Lpg3904_%=:\n" \
"s_cbranch_execz Lpg6064_%=\n" \
"ds_read_b64_tr_b16 a[110:111], v233 offset:27648\n" \
"v_mfma_f32_16x16x32_bf16 v[100:103], a[76:79], a[12:15], v[100:103]\n" \
"ds_read_b64_tr_b16 a[112:113], v232 offset:17920\n" \
"v_mfma_f32_16x16x32_bf16 v[96:99], a[64:67], a[8:11], v[96:99]\n" \
"ds_read_b64_tr_b16 a[114:115], v232 offset:19968\n" \
"v_mfma_f32_16x16x32_bf16 v[96:99], a[68:71], a[12:15], v[96:99]\n" \
"buffer_load_dwordx4 v227, s[48:51], s77 offen offset:3072 lds\n" \
"v_mfma_f32_16x16x32_bf16 v[128:131], a[64:67], a[16:19], v[128:131]\n" \
"ds_read_b64_tr_b16 a[116:117], v232 offset:26112\n" \
"v_mfma_f32_16x16x32_bf16 v[128:131], a[68:71], a[20:23], v[128:131]\n" \
"ds_read_b64_tr_b16 a[118:119], v232 offset:28160\n" \
"v_mfma_f32_16x16x32_bf16 v[132:135], a[72:75], a[16:19], v[132:135]\n" \
"s_xor_b32 s77, s77, 0x80\n" \
"ds_read_b64_tr_b16 a[120:121], v233 offset:17920\n" \
"v_mfma_f32_16x16x32_bf16 v[132:135], a[76:79], a[20:23], v[132:135]\n" \
"v_mfma_f32_16x16x32_bf16 v[136:139], a[80:83], a[16:19], v[136:139]\n" \
"ds_read_b64_tr_b16 a[122:123], v233 offset:19968\n" \
"v_mfma_f32_16x16x32_bf16 v[136:139], a[84:87], a[20:23], v[136:139]\n" \
"ds_read_b64_tr_b16 a[124:125], v233 offset:26112\n" \
"v_mfma_f32_16x16x32_bf16 v[140:143], a[88:91], a[16:19], v[140:143]\n" \
"ds_read_b64_tr_b16 a[126:127], v233 offset:28160\n" \
"v_mfma_f32_16x16x32_bf16 v[140:143], a[92:95], a[20:23], v[140:143]\n" \
"buffer_store_dwordx4 a[228:231], v234, s[56:59], s93 offen offset:1024 sc1\n" \
"v_mfma_f32_16x16x32_bf16 v[172:175], a[88:91], a[24:27], v[172:175]\n" \
"v_mfma_f32_16x16x32_bf16 v[172:175], a[92:95], a[28:31], v[172:175]\n" \
"v_mfma_f32_16x16x32_bf16 v[168:171], a[80:83], a[24:27], v[168:171]\n" \
"v_mfma_f32_16x16x32_bf16 v[168:171], a[84:87], a[28:31], v[168:171]\n" \
"v_mfma_f32_16x16x32_bf16 v[164:167], a[72:75], a[24:27], v[164:167]\n" \
"v_mfma_f32_16x16x32_bf16 v[164:167], a[76:79], a[28:31], v[164:167]\n" \
"v_mfma_f32_16x16x32_bf16 v[160:163], a[64:67], a[24:27], v[160:163]\n" \
"v_mfma_f32_16x16x32_bf16 v[160:163], a[68:71], a[28:31], v[160:163]\n" \
"s_waitcnt vmcnt(35) lgkmcnt(0)\n" \
"s_barrier\n" \
"v_mfma_f32_16x16x32_bf16 v[80:83], a[96:99], a[0:3], v[80:83]\n" \
"ds_read_b64_tr_b16 a[64:65], v232 offset:32768\n" \
"v_mfma_f32_16x16x32_bf16 v[80:83], a[100:103], a[4:7], v[80:83]\n" \
"ds_read_b64_tr_b16 a[66:67], v232 offset:34816\n" \
"v_mfma_f32_16x16x32_bf16 v[84:87], a[104:107], a[0:3], v[84:87]\n" \
"s_add_u32 m0, s79, 16384\n" \
"ds_read_b64_tr_b16 a[68:69], v232 offset:40960\n" \
"v_mfma_f32_16x16x32_bf16 v[84:87], a[108:111], a[4:7], v[84:87]\n" \
"buffer_load_dwordx4 v228, s[52:55], s78 offen offset:0 lds\n" \
"v_mfma_f32_16x16x32_bf16 v[88:91], a[112:115], a[0:3], v[88:91]\n" \
"ds_read_b64_tr_b16 a[70:71], v232 offset:43008\n" \
"v_mfma_f32_16x16x32_bf16 v[88:91], a[116:119], a[4:7], v[88:91]\n" \
"ds_read_b64_tr_b16 a[72:73], v233 offset:32768\n" \
"v_mfma_f32_16x16x32_bf16 v[92:95], a[120:123], a[0:3], v[92:95]\n" \
"ds_read_b64_tr_b16 a[74:75], v233 offset:34816\n" \
"v_mfma_f32_16x16x32_bf16 v[92:95], a[124:127], a[4:7], v[92:95]\n" \
"buffer_load_dwordx4 v229, s[52:55], s78 offen offset:1024 lds\n" \
"v_mfma_f32_16x16x32_bf16 v[124:127], a[120:123], a[8:11], v[124:127]\n" \
"s_waitcnt vmcnt(35)\n" \
"ds_read_b128 a[0:3], v230 offset:0\n" \
"v_mfma_f32_16x16x32_bf16 v[124:127], a[124:127], a[12:15], v[124:127]\n" \
"ds_read_b128 a[4:7], v231 offset:0\n" \
"v_mfma_f32_16x16x32_bf16 v[120:123], a[112:115], a[8:11], v[120:123]\n" \
"s_xor_b32 s78, s78, 0x180000\n" \
"ds_read_b64_tr_b16 a[76:77], v233 offset:40960\n" \
"v_mfma_f32_16x16x32_bf16 v[120:123], a[116:119], a[12:15], v[120:123]\n" \
"buffer_load_dwordx4 v228, s[52:55], s89 offen offset:2048 lds\n" \
"v_mfma_f32_16x16x32_bf16 v[116:119], a[104:107], a[8:11], v[116:119]\n" \
"ds_read_b64_tr_b16 a[78:79], v233 offset:43008\n" \
"v_mfma_f32_16x16x32_bf16 v[116:119], a[108:111], a[12:15], v[116:119]\n" \
"ds_read_b64_tr_b16 a[80:81], v232 offset:33280\n" \
"v_mfma_f32_16x16x32_bf16 v[112:115], a[96:99], a[8:11], v[112:115]\n" \
"ds_read_b64_tr_b16 a[82:83], v232 offset:35328\n" \
"v_mfma_f32_16x16x32_bf16 v[112:115], a[100:103], a[12:15], v[112:115]\n" \
"buffer_load_dwordx4 v229, s[52:55], s89 offen offset:3072 lds\n" \
"v_mfma_f32_16x16x32_bf16 v[144:147], a[96:99], a[16:19], v[144:147]\n" \
"s_waitcnt vmcnt(35)\n" \
"s_xor_b32 s89, s89, 0x180000\n" \
"ds_read_b128 a[8:11], v230 offset:2048\n" \
"v_mfma_f32_16x16x32_bf16 v[144:147], a[100:103], a[20:23], v[144:147]\n" \
"s_add_u32 m0, s74, 65536\n" \
"ds_read_b128 a[12:15], v231 offset:2048\n" \
"v_mfma_f32_16x16x32_bf16 v[148:151], a[104:107], a[16:19], v[148:151]\n" \
"s_waitcnt lgkmcnt(6)\n" \
"ds_read_b64_tr_b16 a[84:85], v232 offset:41472\n" \
"v_mfma_f32_16x16x32_bf16 v[148:151], a[108:111], a[20:23], v[148:151]\n" \
"buffer_load_dwordx4 v224, s[48:51], s75 offen offset:0 lds\n" \
"v_mfma_f32_16x16x32_bf16 v[152:155], a[112:115], a[16:19], v[152:155]\n" \
"ds_read_b64_tr_b16 a[86:87], v232 offset:43520\n" \
"v_mfma_f32_16x16x32_bf16 v[152:155], a[116:119], a[20:23], v[152:155]\n" \
"ds_read_b64_tr_b16 a[88:89], v233 offset:33280\n" \
"v_mfma_f32_16x16x32_bf16 v[156:159], a[120:123], a[16:19], v[156:159]\n" \
"ds_read_b64_tr_b16 a[90:91], v233 offset:35328\n" \
"v_mfma_f32_16x16x32_bf16 v[156:159], a[124:127], a[20:23], v[156:159]\n" \
"buffer_load_dwordx4 v225, s[48:51], s75 offen offset:1024 lds\n" \
"v_mfma_f32_16x16x32_bf16 v[188:191], a[120:123], a[24:27], v[188:191]\n" \
"s_waitcnt vmcnt(35)\n" \
"ds_read_b128 a[16:19], v230 offset:4096\n" \
"v_mfma_f32_16x16x32_bf16 v[188:191], a[124:127], a[28:31], v[188:191]\n" \
"ds_read_b128 a[20:23], v231 offset:4096\n" \
"v_mfma_f32_16x16x32_bf16 v[184:187], a[112:115], a[24:27], v[184:187]\n" \
"s_waitcnt lgkmcnt(6)\n" \
"ds_read_b64_tr_b16 a[92:93], v233 offset:41472\n" \
"v_mfma_f32_16x16x32_bf16 v[184:187], a[116:119], a[28:31], v[184:187]\n" \
"buffer_load_dwordx4 v226, s[48:51], s75 offen offset:2048 lds\n" \
"v_mfma_f32_16x16x32_bf16 v[180:183], a[104:107], a[24:27], v[180:183]\n" \
"ds_read_b64_tr_b16 a[94:95], v233 offset:43520\n" \
"v_mfma_f32_16x16x32_bf16 v[180:183], a[108:111], a[28:31], v[180:183]\n" \
"v_mfma_f32_16x16x32_bf16 v[176:179], a[96:99], a[24:27], v[176:179]\n" \
"v_mfma_f32_16x16x32_bf16 v[176:179], a[100:103], a[28:31], v[176:179]\n" \
"buffer_load_dwordx4 v227, s[48:51], s75 offen offset:3072 lds\n" \
"v_mfma_f32_16x16x32_bf16 v[64:67], a[64:67], a[0:3], v[64:67]\n" \
"s_waitcnt vmcnt(35)\n" \
"s_xor_b32 s75, s75, 0x180\n" \
"ds_read_b128 a[24:27], v230 offset:6144\n" \
"v_mfma_f32_16x16x32_bf16 v[64:67], a[68:71], a[4:7], v[64:67]\n" \
"s_add_u32 m0, s74, 69632\n" \
"ds_read_b128 a[28:31], v231 offset:6144\n" \
"v_mfma_f32_16x16x32_bf16 v[68:71], a[72:75], a[0:3], v[68:71]\n" \
"s_waitcnt lgkmcnt(2)\n" \
"ds_read_b64_tr_b16 a[96:97], v232 offset:33792\n" \
"v_mfma_f32_16x16x32_bf16 v[68:71], a[76:79], a[4:7], v[68:71]\n" \
"buffer_load_dwordx4 v224, s[48:51], s77 offen offset:0 lds\n" \
"v_mfma_f32_16x16x32_bf16 v[72:75], a[80:83], a[0:3], v[72:75]\n" \
"ds_read_b64_tr_b16 a[98:99], v232 offset:35840\n" \
"v_mfma_f32_16x16x32_bf16 v[72:75], a[84:87], a[4:7], v[72:75]\n" \
"ds_read_b64_tr_b16 a[100:101], v232 offset:41984\n" \
"v_mfma_f32_16x16x32_bf16 v[76:79], a[88:91], a[0:3], v[76:79]\n" \
"ds_read_b64_tr_b16 a[102:103], v232 offset:44032\n" \
"v_mfma_f32_16x16x32_bf16 v[76:79], a[92:95], a[4:7], v[76:79]\n" \
"buffer_load_dwordx4 v225, s[48:51], s77 offen offset:1024 lds\n" \
"v_mfma_f32_16x16x32_bf16 v[108:111], a[88:91], a[8:11], v[108:111]\n" \
"ds_read_b64_tr_b16 a[104:105], v233 offset:33792\n" \
"v_mfma_f32_16x16x32_bf16 v[108:111], a[92:95], a[12:15], v[108:111]\n" \
"ds_read_b64_tr_b16 a[106:107], v233 offset:35840\n" \
"v_mfma_f32_16x16x32_bf16 v[104:107], a[80:83], a[8:11], v[104:107]\n" \
"s_waitcnt lgkmcnt(6)\n" \
"ds_read_b64_tr_b16 a[108:109], v233 offset:41984\n" \
"v_mfma_f32_16x16x32_bf16 v[104:107], a[84:87], a[12:15], v[104:107]\n" \
"buffer_load_dwordx4 v226, s[48:51], s77 offen offset:2048 lds\n" \
"v_mfma_f32_16x16x32_bf16 v[100:103], a[72:75], a[8:11], v[100:103]\n" \
"ds_read_b64_tr_b16 a[110:111], v233 offset:44032\n" \
"v_mfma_f32_16x16x32_bf16 v[100:103], a[76:79], a[12:15], v[100:103]\n" \
"ds_read_b64_tr_b16 a[112:113], v232 offset:34304\n" \
"v_mfma_f32_16x16x32_bf16 v[96:99], a[64:67], a[8:11], v[96:99]\n" \
"ds_read_b64_tr_b16 a[114:115], v232 offset:36352\n" \
"v_mfma_f32_16x16x32_bf16 v[96:99], a[68:71], a[12:15], v[96:99]\n" \
"buffer_load_dwordx4 v227, s[48:51], s77 offen offset:3072 lds\n" \
"v_mfma_f32_16x16x32_bf16 v[128:131], a[64:67], a[16:19], v[128:131]\n" \
"ds_read_b64_tr_b16 a[116:117], v232 offset:42496\n" \
"v_mfma_f32_16x16x32_bf16 v[128:131], a[68:71], a[20:23], v[128:131]\n" \
"ds_read_b64_tr_b16 a[118:119], v232 offset:44544\n" \
"v_mfma_f32_16x16x32_bf16 v[132:135], a[72:75], a[16:19], v[132:135]\n" \
"s_xor_b32 s77, s77, 0x180\n" \
"ds_read_b64_tr_b16 a[120:121], v233 offset:34304\n" \
"v_mfma_f32_16x16x32_bf16 v[132:135], a[76:79], a[20:23], v[132:135]\n" \
"buffer_store_dwordx4 a[232:235], v234, s[56:59], s93 offen offset:2048 sc1\n" \
"v_mfma_f32_16x16x32_bf16 v[136:139], a[80:83], a[16:19], v[136:139]\n" \
"ds_read_b64_tr_b16 a[122:123], v233 offset:36352\n" \
"v_mfma_f32_16x16x32_bf16 v[136:139], a[84:87], a[20:23], v[136:139]\n" \
"ds_read_b64_tr_b16 a[124:125], v233 offset:42496\n" \
"v_mfma_f32_16x16x32_bf16 v[140:143], a[88:91], a[16:19], v[140:143]\n" \
"ds_read_b64_tr_b16 a[126:127], v233 offset:44544\n" \
"v_mfma_f32_16x16x32_bf16 v[140:143], a[92:95], a[20:23], v[140:143]\n" \
"v_mfma_f32_16x16x32_bf16 v[172:175], a[88:91], a[24:27], v[172:175]\n" \
"v_mfma_f32_16x16x32_bf16 v[172:175], a[92:95], a[28:31], v[172:175]\n" \
"v_mfma_f32_16x16x32_bf16 v[168:171], a[80:83], a[24:27], v[168:171]\n" \
"v_mfma_f32_16x16x32_bf16 v[168:171], a[84:87], a[28:31], v[168:171]\n" \
"buffer_store_dwordx4 a[236:239], v234, s[56:59], s93 offen offset:3072 sc1\n" \
"v_mfma_f32_16x16x32_bf16 v[164:167], a[72:75], a[24:27], v[164:167]\n" \
"v_mfma_f32_16x16x32_bf16 v[164:167], a[76:79], a[28:31], v[164:167]\n" \
"v_mfma_f32_16x16x32_bf16 v[160:163], a[64:67], a[24:27], v[160:163]\n" \
"v_mfma_f32_16x16x32_bf16 v[160:163], a[68:71], a[28:31], v[160:163]\n" \
"s_waitcnt vmcnt(36) lgkmcnt(0)\n" \
"s_barrier\n" \
"v_mfma_f32_16x16x32_bf16 v[80:83], a[96:99], a[0:3], v[80:83]\n" \
"ds_read_b64_tr_b16 a[64:65], v232 offset:49152\n" \
"v_mfma_f32_16x16x32_bf16 v[80:83], a[100:103], a[4:7], v[80:83]\n" \
"ds_read_b64_tr_b16 a[66:67], v232 offset:51200\n" \
"v_mfma_f32_16x16x32_bf16 v[84:87], a[104:107], a[0:3], v[84:87]\n" \
"s_add_u32 m0, s79, 32768\n" \
"ds_read_b64_tr_b16 a[68:69], v232 offset:57344\n" \
"v_mfma_f32_16x16x32_bf16 v[84:87], a[108:111], a[4:7], v[84:87]\n" \
"buffer_load_dwordx4 v228, s[52:55], s78 offen offset:0 lds\n" \
"v_mfma_f32_16x16x32_bf16 v[88:91], a[112:115], a[0:3], v[88:91]\n" \
"ds_read_b64_tr_b16 a[70:71], v232 offset:59392\n" \
"v_mfma_f32_16x16x32_bf16 v[88:91], a[116:119], a[4:7], v[88:91]\n" \
"ds_read_b64_tr_b16 a[72:73], v233 offset:49152\n" \
"v_mfma_f32_16x16x32_bf16 v[92:95], a[120:123], a[0:3], v[92:95]\n" \
"ds_read_b64_tr_b16 a[74:75], v233 offset:51200\n" \
"v_mfma_f32_16x16x32_bf16 v[92:95], a[124:127], a[4:7], v[92:95]\n" \
"buffer_load_dwordx4 v229, s[52:55], s78 offen offset:1024 lds\n" \
"v_mfma_f32_16x16x32_bf16 v[124:127], a[120:123], a[8:11], v[124:127]\n" \
"s_waitcnt vmcnt(36)\n" \
"ds_read_b128 a[0:3], v230 offset:32768\n" \
"v_mfma_f32_16x16x32_bf16 v[124:127], a[124:127], a[12:15], v[124:127]\n" \
"ds_read_b128 a[4:7], v231 offset:32768\n" \
"v_mfma_f32_16x16x32_bf16 v[120:123], a[112:115], a[8:11], v[120:123]\n" \
"s_xor_b32 s78, s78, 0x80000\n" \
"ds_read_b64_tr_b16 a[76:77], v233 offset:57344\n" \
"v_mfma_f32_16x16x32_bf16 v[120:123], a[116:119], a[12:15], v[120:123]\n" \
"buffer_load_dwordx4 v228, s[52:55], s89 offen offset:2048 lds\n" \
"v_mfma_f32_16x16x32_bf16 v[116:119], a[104:107], a[8:11], v[116:119]\n" \
"ds_read_b64_tr_b16 a[78:79], v233 offset:59392\n" \
"v_mfma_f32_16x16x32_bf16 v[116:119], a[108:111], a[12:15], v[116:119]\n" \
"ds_read_b64_tr_b16 a[80:81], v232 offset:49664\n" \
"v_mfma_f32_16x16x32_bf16 v[112:115], a[96:99], a[8:11], v[112:115]\n" \
"ds_read_b64_tr_b16 a[82:83], v232 offset:51712\n" \
"v_mfma_f32_16x16x32_bf16 v[112:115], a[100:103], a[12:15], v[112:115]\n" \
"buffer_load_dwordx4 v229, s[52:55], s89 offen offset:3072 lds\n" \
"v_mfma_f32_16x16x32_bf16 v[144:147], a[96:99], a[16:19], v[144:147]\n" \
"s_waitcnt vmcnt(36)\n" \
"s_xor_b32 s89, s89, 0x80000\n" \
"ds_read_b128 a[8:11], v230 offset:34816\n" \
"v_mfma_f32_16x16x32_bf16 v[144:147], a[100:103], a[20:23], v[144:147]\n" \
"s_add_u32 m0, s74, 98304\n" \
"ds_read_b128 a[12:15], v231 offset:34816\n" \
"v_mfma_f32_16x16x32_bf16 v[148:151], a[104:107], a[16:19], v[148:151]\n" \
"s_waitcnt lgkmcnt(6)\n" \
"ds_read_b64_tr_b16 a[84:85], v232 offset:57856\n" \
"v_mfma_f32_16x16x32_bf16 v[148:151], a[108:111], a[20:23], v[148:151]\n" \
"buffer_load_dwordx4 v224, s[48:51], s75 offen offset:0 lds\n" \
"v_mfma_f32_16x16x32_bf16 v[152:155], a[112:115], a[16:19], v[152:155]\n" \
"ds_read_b64_tr_b16 a[86:87], v232 offset:59904\n" \
"v_mfma_f32_16x16x32_bf16 v[152:155], a[116:119], a[20:23], v[152:155]\n" \
"ds_read_b64_tr_b16 a[88:89], v233 offset:49664\n" \
"v_mfma_f32_16x16x32_bf16 v[156:159], a[120:123], a[16:19], v[156:159]\n" \
"ds_read_b64_tr_b16 a[90:91], v233 offset:51712\n" \
"v_mfma_f32_16x16x32_bf16 v[156:159], a[124:127], a[20:23], v[156:159]\n" \
"buffer_load_dwordx4 v225, s[48:51], s75 offen offset:1024 lds\n" \
"v_mfma_f32_16x16x32_bf16 v[188:191], a[120:123], a[24:27], v[188:191]\n" \
"s_waitcnt vmcnt(36)\n" \
"ds_read_b128 a[16:19], v230 offset:36864\n" \
"v_mfma_f32_16x16x32_bf16 v[188:191], a[124:127], a[28:31], v[188:191]\n" \
"ds_read_b128 a[20:23], v231 offset:36864\n" \
"v_mfma_f32_16x16x32_bf16 v[184:187], a[112:115], a[24:27], v[184:187]\n" \
"s_waitcnt lgkmcnt(6)\n" \
"ds_read_b64_tr_b16 a[92:93], v233 offset:57856\n" \
"v_mfma_f32_16x16x32_bf16 v[184:187], a[116:119], a[28:31], v[184:187]\n" \
"buffer_load_dwordx4 v226, s[48:51], s75 offen offset:2048 lds\n" \
"v_mfma_f32_16x16x32_bf16 v[180:183], a[104:107], a[24:27], v[180:183]\n" \
"ds_read_b64_tr_b16 a[94:95], v233 offset:59904\n" \
"v_mfma_f32_16x16x32_bf16 v[180:183], a[108:111], a[28:31], v[180:183]\n" \
"v_mfma_f32_16x16x32_bf16 v[176:179], a[96:99], a[24:27], v[176:179]\n" \
"v_mfma_f32_16x16x32_bf16 v[176:179], a[100:103], a[28:31], v[176:179]\n" \
"buffer_load_dwordx4 v227, s[48:51], s75 offen offset:3072 lds\n" \
"v_mfma_f32_16x16x32_bf16 v[64:67], a[64:67], a[0:3], v[64:67]\n" \
"s_waitcnt vmcnt(36)\n" \
"s_xor_b32 s75, s75, 0x80\n" \
"ds_read_b128 a[24:27], v230 offset:38912\n" \
"v_mfma_f32_16x16x32_bf16 v[64:67], a[68:71], a[4:7], v[64:67]\n" \
"s_add_u32 m0, s74, 102400\n" \
"ds_read_b128 a[28:31], v231 offset:38912\n" \
"v_mfma_f32_16x16x32_bf16 v[68:71], a[72:75], a[0:3], v[68:71]\n" \
"s_waitcnt lgkmcnt(2)\n" \
"ds_read_b64_tr_b16 a[96:97], v232 offset:50176\n" \
"v_mfma_f32_16x16x32_bf16 v[68:71], a[76:79], a[4:7], v[68:71]\n" \
"buffer_load_dwordx4 v224, s[48:51], s77 offen offset:0 lds\n" \
"v_mfma_f32_16x16x32_bf16 v[72:75], a[80:83], a[0:3], v[72:75]\n" \
"ds_read_b64_tr_b16 a[98:99], v232 offset:52224\n" \
"v_mfma_f32_16x16x32_bf16 v[72:75], a[84:87], a[4:7], v[72:75]\n" \
"ds_read_b64_tr_b16 a[100:101], v232 offset:58368\n" \
"v_mfma_f32_16x16x32_bf16 v[76:79], a[88:91], a[0:3], v[76:79]\n" \
"ds_read_b64_tr_b16 a[102:103], v232 offset:60416\n" \
"v_mfma_f32_16x16x32_bf16 v[76:79], a[92:95], a[4:7], v[76:79]\n" \
"buffer_load_dwordx4 v225, s[48:51], s77 offen offset:1024 lds\n" \
"v_mfma_f32_16x16x32_bf16 v[108:111], a[88:91], a[8:11], v[108:111]\n" \
"ds_read_b64_tr_b16 a[104:105], v233 offset:50176\n" \
"v_mfma_f32_16x16x32_bf16 v[108:111], a[92:95], a[12:15], v[108:111]\n" \
"ds_read_b64_tr_b16 a[106:107], v233 offset:52224\n" \
"v_mfma_f32_16x16x32_bf16 v[104:107], a[80:83], a[8:11], v[104:107]\n" \
"s_waitcnt lgkmcnt(6)\n" \
"ds_read_b64_tr_b16 a[108:109], v233 offset:58368\n" \
"v_mfma_f32_16x16x32_bf16 v[104:107], a[84:87], a[12:15], v[104:107]\n" \
"buffer_load_dwordx4 v226, s[48:51], s77 offen offset:2048 lds\n" \
"v_mfma_f32_16x16x32_bf16 v[100:103], a[72:75], a[8:11], v[100:103]\n" \
"ds_read_b64_tr_b16 a[110:111], v233 offset:60416\n" \
"v_mfma_f32_16x16x32_bf16 v[100:103], a[76:79], a[12:15], v[100:103]\n" \
"ds_read_b64_tr_b16 a[112:113], v232 offset:50688\n" \
"v_mfma_f32_16x16x32_bf16 v[96:99], a[64:67], a[8:11], v[96:99]\n" \
"ds_read_b64_tr_b16 a[114:115], v232 offset:52736\n" \
"v_mfma_f32_16x16x32_bf16 v[96:99], a[68:71], a[12:15], v[96:99]\n" \
"buffer_load_dwordx4 v227, s[48:51], s77 offen offset:3072 lds\n" \
"v_mfma_f32_16x16x32_bf16 v[128:131], a[64:67], a[16:19], v[128:131]\n" \
"ds_read_b64_tr_b16 a[116:117], v232 offset:58880\n" \
"v_mfma_f32_16x16x32_bf16 v[128:131], a[68:71], a[20:23], v[128:131]\n" \
"ds_read_b64_tr_b16 a[118:119], v232 offset:60928\n" \
"v_mfma_f32_16x16x32_bf16 v[132:135], a[72:75], a[16:19], v[132:135]\n" \
"s_xor_b32 s77, s77, 0x80\n" \
"s_add_u32 s93, s87, 17850368\n" \
"ds_read_b64_tr_b16 a[120:121], v233 offset:50688\n" \
"v_mfma_f32_16x16x32_bf16 v[132:135], a[76:79], a[20:23], v[132:135]\n" \
"buffer_store_dwordx4 a[192:195], v234, s[56:59], s93 offen offset:0 sc1\n" \
"v_mfma_f32_16x16x32_bf16 v[136:139], a[80:83], a[16:19], v[136:139]\n" \
"ds_read_b64_tr_b16 a[122:123], v233 offset:52736\n" \
"v_mfma_f32_16x16x32_bf16 v[136:139], a[84:87], a[20:23], v[136:139]\n" \
"ds_read_b64_tr_b16 a[124:125], v233 offset:58880\n" \
"v_mfma_f32_16x16x32_bf16 v[140:143], a[88:91], a[16:19], v[140:143]\n" \
"ds_read_b64_tr_b16 a[126:127], v233 offset:60928\n" \
"v_mfma_f32_16x16x32_bf16 v[140:143], a[92:95], a[20:23], v[140:143]\n" \
"v_mfma_f32_16x16x32_bf16 v[172:175], a[88:91], a[24:27], v[172:175]\n" \
"v_mfma_f32_16x16x32_bf16 v[172:175], a[92:95], a[28:31], v[172:175]\n" \
"v_mfma_f32_16x16x32_bf16 v[168:171], a[80:83], a[24:27], v[168:171]\n" \
"v_mfma_f32_16x16x32_bf16 v[168:171], a[84:87], a[28:31], v[168:171]\n" \
"buffer_store_dwordx4 a[196:199], v234, s[56:59], s93 offen offset:1024 sc1\n" \
"v_mfma_f32_16x16x32_bf16 v[164:167], a[72:75], a[24:27], v[164:167]\n" \
"v_mfma_f32_16x16x32_bf16 v[164:167], a[76:79], a[28:31], v[164:167]\n" \
"v_mfma_f32_16x16x32_bf16 v[160:163], a[64:67], a[24:27], v[160:163]\n" \
"v_mfma_f32_16x16x32_bf16 v[160:163], a[68:71], a[28:31], v[160:163]\n" \
"s_waitcnt vmcnt(37) lgkmcnt(0)\n" \
"s_barrier\n" \
"v_mfma_f32_16x16x32_bf16 v[80:83], a[96:99], a[0:3], v[80:83]\n" \
"ds_read_b64_tr_b16 a[64:65], v232 offset:0\n" \
"v_mfma_f32_16x16x32_bf16 v[80:83], a[100:103], a[4:7], v[80:83]\n" \
"ds_read_b64_tr_b16 a[66:67], v232 offset:2048\n" \
"v_mfma_f32_16x16x32_bf16 v[84:87], a[104:107], a[0:3], v[84:87]\n" \
"s_add_u32 m0, s79, 49152\n" \
"ds_read_b64_tr_b16 a[68:69], v232 offset:8192\n" \
"v_mfma_f32_16x16x32_bf16 v[84:87], a[108:111], a[4:7], v[84:87]\n" \
"buffer_load_dwordx4 v228, s[52:55], s78 offen offset:0 lds\n" \
"v_mfma_f32_16x16x32_bf16 v[88:91], a[112:115], a[0:3], v[88:91]\n" \
"s_mul_i32 s73, s62, 557824\n" \
"s_add_u32 s85, s73, 17850368\n" \
"ds_read_b64_tr_b16 a[70:71], v232 offset:10240\n" \
"v_mfma_f32_16x16x32_bf16 v[88:91], a[116:119], a[4:7], v[88:91]\n" \
"s_mul_i32 s83, s63, 34816\n" \
"s_sub_u32 s86, 557056, s83\n" \
"ds_read_b64_tr_b16 a[72:73], v233 offset:0\n" \
"v_mfma_f32_16x16x32_bf16 v[92:95], a[120:123], a[0:3], v[92:95]\n" \
"ds_read_b64_tr_b16 a[74:75], v233 offset:2048\n" \
"v_add_u32 v237, %3, v236\n" \
"v_mfma_f32_16x16x32_bf16 v[92:95], a[124:127], a[4:7], v[92:95]\n" \
"buffer_load_dwordx4 v229, s[52:55], s78 offen offset:1024 lds\n" \
"v_mfma_f32_16x16x32_bf16 v[124:127], a[120:123], a[8:11], v[124:127]\n" \
"s_waitcnt vmcnt(37)\n" \
"ds_read_b128 a[0:3], v250 offset:0\n" \
"v_mfma_f32_16x16x32_bf16 v[124:127], a[124:127], a[12:15], v[124:127]\n" \
"s_add_u32 s36, s56, s73\n" \
"s_addc_u32 s37, s57, 0\n" \
"ds_read_b128 a[4:7], v251 offset:0\n" \
"v_mfma_f32_16x16x32_bf16 v[120:123], a[112:115], a[8:11], v[120:123]\n" \
"s_mov_b32 s38, s86\n" \
"s_mov_b32 s39, 0x20000\n" \
"ds_read_b64_tr_b16 a[76:77], v233 offset:8192\n" \
"v_mfma_f32_16x16x32_bf16 v[120:123], a[116:119], a[12:15], v[120:123]\n" \
"buffer_load_dwordx4 v228, s[52:55], s89 offen offset:2048 lds\n" \
"v_mfma_f32_16x16x32_bf16 v[116:119], a[104:107], a[8:11], v[116:119]\n" \
"ds_read_b64_tr_b16 a[78:79], v233 offset:10240\n" \
"v_add_u32 v235, %3, v234\n" \
"v_mfma_f32_16x16x32_bf16 v[116:119], a[108:111], a[12:15], v[116:119]\n" \
"s_add_u32 s40, s56, s85\n" \
"s_addc_u32 s41, s57, 0\n" \
"ds_read_b64_tr_b16 a[80:81], v232 offset:512\n" \
"v_mfma_f32_16x16x32_bf16 v[112:115], a[96:99], a[8:11], v[112:115]\n" \
"s_mov_b32 s42, s86\n" \
"s_mov_b32 s43, 0x20000\n" \
"ds_read_b64_tr_b16 a[82:83], v232 offset:2560\n" \
"v_mfma_f32_16x16x32_bf16 v[112:115], a[100:103], a[12:15], v[112:115]\n" \
"buffer_load_dwordx4 v229, s[52:55], s89 offen offset:3072 lds\n" \
"v_mfma_f32_16x16x32_bf16 v[144:147], a[96:99], a[16:19], v[144:147]\n" \
"s_waitcnt vmcnt(37)\n" \
"s_add_u32 s85, s74, 131072\n" \
"ds_read_b128 a[8:11], v250 offset:2048\n" \
"v_mfma_f32_16x16x32_bf16 v[144:147], a[100:103], a[20:23], v[144:147]\n" \
"s_add_u32 m0, s74, 131072\n" \
"ds_read_b128 a[12:15], v251 offset:2048\n" \
"v_add_u32 v239, s85, v234\n" \
"v_mfma_f32_16x16x32_bf16 v[148:151], a[104:107], a[16:19], v[148:151]\n" \
"s_waitcnt lgkmcnt(6)\n" \
"s_lshl_b32 s73, s61, 12\n" \
"ds_read_b64_tr_b16 a[84:85], v232 offset:8704\n" \
"v_mfma_f32_16x16x32_bf16 v[148:151], a[108:111], a[20:23], v[148:151]\n" \
"buffer_load_dwordx4 v224, s[48:51], s75 offen offset:0 lds\n" \
"v_mfma_f32_16x16x32_bf16 v[152:155], a[112:115], a[16:19], v[152:155]\n" \
"s_add_u32 s80, s79, 0x10000\n" \
"s_add_u32 s83, s83, s73\n" \
"ds_read_b64_tr_b16 a[86:87], v232 offset:10752\n" \
"v_mfma_f32_16x16x32_bf16 v[152:155], a[116:119], a[20:23], v[152:155]\n" \
"s_cmp_eq_u32 s63, 0\n" \
"s_cselect_b32 s85, 34816, 0\n" \
"ds_read_b64_tr_b16 a[88:89], v233 offset:512\n" \
"v_mfma_f32_16x16x32_bf16 v[156:159], a[120:123], a[16:19], v[156:159]\n" \
"s_add_u32 s73, s73, s85\n" \
"ds_read_b64_tr_b16 a[90:91], v233 offset:2560\n" \
"v_mfma_f32_16x16x32_bf16 v[156:159], a[124:127], a[20:23], v[156:159]\n" \
"buffer_load_dwordx4 v225, s[48:51], s75 offen offset:1024 lds\n" \
"v_mfma_f32_16x16x32_bf16 v[188:191], a[120:123], a[24:27], v[188:191]\n" \
"s_waitcnt vmcnt(37)\n" \
"ds_read_b128 a[16:19], v250 offset:4096\n" \
"v_mfma_f32_16x16x32_bf16 v[188:191], a[124:127], a[28:31], v[188:191]\n" \
"ds_read_b128 a[20:23], v251 offset:4096\n" \
"v_mfma_f32_16x16x32_bf16 v[184:187], a[112:115], a[24:27], v[184:187]\n" \
"s_waitcnt lgkmcnt(6)\n" \
"ds_read_b64_tr_b16 a[92:93], v233 offset:8704\n" \
"v_mfma_f32_16x16x32_bf16 v[184:187], a[116:119], a[28:31], v[184:187]\n" \
"buffer_load_dwordx4 v226, s[48:51], s75 offen offset:2048 lds\n" \
"v_mfma_f32_16x16x32_bf16 v[180:183], a[104:107], a[24:27], v[180:183]\n" \
"ds_read_b64_tr_b16 a[94:95], v233 offset:10752\n" \
"v_mfma_f32_16x16x32_bf16 v[180:183], a[108:111], a[28:31], v[180:183]\n" \
"v_mfma_f32_16x16x32_bf16 v[176:179], a[96:99], a[24:27], v[176:179]\n" \
"v_mfma_f32_16x16x32_bf16 v[176:179], a[100:103], a[28:31], v[176:179]\n" \
"buffer_load_dwordx4 v227, s[48:51], s75 offen offset:3072 lds\n" \
"v_mfma_f32_16x16x32_bf16 v[64:67], a[64:67], a[0:3], v[64:67]\n" \
"s_waitcnt vmcnt(37)\n" \
"ds_read_b128 a[24:27], v250 offset:6144\n" \
"v_mfma_f32_16x16x32_bf16 v[64:67], a[68:71], a[4:7], v[64:67]\n" \
"s_add_u32 m0, s74, 135168\n" \
"ds_read_b128 a[28:31], v251 offset:6144\n" \
"v_mfma_f32_16x16x32_bf16 v[68:71], a[72:75], a[0:3], v[68:71]\n" \
"s_waitcnt lgkmcnt(2)\n" \
"ds_read_b64_tr_b16 a[96:97], v232 offset:1024\n" \
"v_mfma_f32_16x16x32_bf16 v[68:71], a[76:79], a[4:7], v[68:71]\n" \
"buffer_load_dwordx4 v224, s[48:51], s77 offen offset:0 lds\n" \
"v_mfma_f32_16x16x32_bf16 v[72:75], a[80:83], a[0:3], v[72:75]\n" \
"ds_read_b64_tr_b16 a[98:99], v232 offset:3072\n" \
"v_mfma_f32_16x16x32_bf16 v[72:75], a[84:87], a[4:7], v[72:75]\n" \
"ds_read_b64_tr_b16 a[100:101], v232 offset:9216\n" \
"v_mfma_f32_16x16x32_bf16 v[76:79], a[88:91], a[0:3], v[76:79]\n" \
"ds_read_b64_tr_b16 a[102:103], v232 offset:11264\n" \
"v_mfma_f32_16x16x32_bf16 v[76:79], a[92:95], a[4:7], v[76:79]\n" \
"buffer_load_dwordx4 v225, s[48:51], s77 offen offset:1024 lds\n" \
"v_mfma_f32_16x16x32_bf16 v[108:111], a[88:91], a[8:11], v[108:111]\n" \
"ds_read_b64_tr_b16 a[104:105], v233 offset:1024\n" \
"v_mfma_f32_16x16x32_bf16 v[108:111], a[92:95], a[12:15], v[108:111]\n" \
"ds_read_b64_tr_b16 a[106:107], v233 offset:3072\n" \
"v_mfma_f32_16x16x32_bf16 v[104:107], a[80:83], a[8:11], v[104:107]\n" \
"s_waitcnt lgkmcnt(6)\n" \
"ds_read_b64_tr_b16 a[108:109], v233 offset:9216\n" \
"v_mfma_f32_16x16x32_bf16 v[104:107], a[84:87], a[12:15], v[104:107]\n" \
"buffer_load_dwordx4 v226, s[48:51], s77 offen offset:2048 lds\n" \
"v_mfma_f32_16x16x32_bf16 v[100:103], a[72:75], a[8:11], v[100:103]\n" \
"ds_read_b64_tr_b16 a[110:111], v233 offset:11264\n" \
"v_mfma_f32_16x16x32_bf16 v[100:103], a[76:79], a[12:15], v[100:103]\n" \
"ds_read_b64_tr_b16 a[112:113], v232 offset:1536\n" \
"v_mfma_f32_16x16x32_bf16 v[96:99], a[64:67], a[8:11], v[96:99]\n" \
"ds_read_b64_tr_b16 a[114:115], v232 offset:3584\n" \
"v_mfma_f32_16x16x32_bf16 v[96:99], a[68:71], a[12:15], v[96:99]\n" \
"buffer_load_dwordx4 v227, s[48:51], s77 offen offset:3072 lds\n" \
"v_mfma_f32_16x16x32_bf16 v[128:131], a[64:67], a[16:19], v[128:131]\n" \
"ds_read_b64_tr_b16 a[116:117], v232 offset:9728\n" \
"v_mfma_f32_16x16x32_bf16 v[128:131], a[68:71], a[20:23], v[128:131]\n" \
"ds_read_b64_tr_b16 a[118:119], v232 offset:11776\n" \
"v_mfma_f32_16x16x32_bf16 v[132:135], a[72:75], a[16:19], v[132:135]\n" \
"ds_read_b64_tr_b16 a[120:121], v233 offset:1536\n" \
"v_mfma_f32_16x16x32_bf16 v[132:135], a[76:79], a[20:23], v[132:135]\n" \
"buffer_store_dwordx4 a[200:203], v234, s[56:59], s93 offen offset:2048 sc1\n" \
"v_mfma_f32_16x16x32_bf16 v[136:139], a[80:83], a[16:19], v[136:139]\n" \
"ds_read_b64_tr_b16 a[122:123], v233 offset:3584\n" \
"v_mfma_f32_16x16x32_bf16 v[136:139], a[84:87], a[20:23], v[136:139]\n" \
"ds_read_b64_tr_b16 a[124:125], v233 offset:9728\n" \
"v_mfma_f32_16x16x32_bf16 v[140:143], a[88:91], a[16:19], v[140:143]\n" \
"ds_read_b64_tr_b16 a[126:127], v233 offset:11776\n" \
"v_mfma_f32_16x16x32_bf16 v[140:143], a[92:95], a[20:23], v[140:143]\n" \
"v_mfma_f32_16x16x32_bf16 v[172:175], a[88:91], a[24:27], v[172:175]\n" \
"v_mfma_f32_16x16x32_bf16 v[172:175], a[92:95], a[28:31], v[172:175]\n" \
"v_mfma_f32_16x16x32_bf16 v[168:171], a[80:83], a[24:27], v[168:171]\n" \
"v_mfma_f32_16x16x32_bf16 v[168:171], a[84:87], a[28:31], v[168:171]\n" \
"buffer_store_dwordx4 a[204:207], v234, s[56:59], s93 offen offset:3072 sc1\n" \
"v_mfma_f32_16x16x32_bf16 v[164:167], a[72:75], a[24:27], v[164:167]\n" \
"v_mfma_f32_16x16x32_bf16 v[164:167], a[76:79], a[28:31], v[164:167]\n" \
"v_mfma_f32_16x16x32_bf16 v[160:163], a[64:67], a[24:27], v[160:163]\n" \
"v_mfma_f32_16x16x32_bf16 v[160:163], a[68:71], a[28:31], v[160:163]\n" \
"s_waitcnt vmcnt(38) lgkmcnt(0)\n" \
"s_barrier\n" \
"v_mfma_f32_16x16x32_bf16 v[80:83], a[96:99], a[0:3], v[80:83]\n" \
"s_lshr_b32 s71, s61, 1\n" \
"s_lshl_b32 s71, s71, 14\n" \
"ds_read_b64_tr_b16 a[64:65], v232 offset:16384\n" \
"v_mfma_f32_16x16x32_bf16 v[80:83], a[100:103], a[4:7], v[80:83]\n" \
"s_xor_b32 s71, s71, 0x4000\n" \
"s_and_b32 s70, s61, 1\n" \
"ds_read_b64_tr_b16 a[66:67], v232 offset:18432\n" \
"v_mfma_f32_16x16x32_bf16 v[84:87], a[104:107], a[0:3], v[84:87]\n" \
"s_lshl_b32 s70, s70, 12\n" \
"s_add_u32 s71, s71, s70\n" \
"ds_read_b64_tr_b16 a[68:69], v232 offset:24576\n" \
"v_mfma_f32_16x16x32_bf16 v[84:87], a[108:111], a[4:7], v[84:87]\n" \
"v_add_u32 v224, s71, v237\n" \
"s_and_b32 s70, s63, 1\n" \
"v_mfma_f32_16x16x32_bf16 v[88:91], a[112:115], a[0:3], v[88:91]\n" \
"s_lshl_b32 s70, s70, 15\n" \
"s_xor_b32 s70, s70, 0x18000\n" \
"ds_read_b64_tr_b16 a[70:71], v232 offset:26624\n" \
"v_mfma_f32_16x16x32_bf16 v[88:91], a[116:119], a[4:7], v[88:91]\n" \
"s_add_u32 s71, s71, s70\n" \
"ds_read_b64_tr_b16 a[72:73], v233 offset:16384\n" \
"v_add_u32 v225, s71, v235\n" \
"v_mfma_f32_16x16x32_bf16 v[92:95], a[120:123], a[0:3], v[92:95]\n" \
"s_lshr_b32 s64, s22, 7\n" \
"s_sub_u32 s64, 3, s64\n" \
"ds_read_b64_tr_b16 a[74:75], v233 offset:18432\n" \
"v_mfma_f32_16x16x32_bf16 v[92:95], a[124:127], a[4:7], v[92:95]\n" \
"s_and_b32 s64, s64, 7\n" \
"s_and_b32 s65, %1, 1\n" \
"s_lshl_b32 s65, s65, 3\n" \
"v_mfma_f32_16x16x32_bf16 v[124:127], a[120:123], a[8:11], v[124:127]\n" \
"s_waitcnt vmcnt(36)\n" \
"s_add_u32 s64, s64, s65\n" \
"ds_read_b128 a[0:3], v230 offset:0\n" \
"v_mfma_f32_16x16x32_bf16 v[124:127], a[124:127], a[12:15], v[124:127]\n" \
"s_lshl_b32 s64, s64, 7\n" \
"s_lshr_b32 s65, %1, 3\n" \
"ds_read_b128 a[4:7], v231 offset:0\n" \
"v_mfma_f32_16x16x32_bf16 v[120:123], a[112:115], a[8:11], v[120:123]\n" \
"s_lshl_b32 s65, s65, 2\n" \
"s_add_u32 s65, s65, s61\n" \
"ds_read_b64_tr_b16 a[76:77], v233 offset:24576\n" \
"v_mfma_f32_16x16x32_bf16 v[120:123], a[116:119], a[12:15], v[120:123]\n" \
"s_mul_i32 s65, s65, 24576\n" \
"s_add_u32 s64, s64, s65\n" \
"v_bfe_u32 v254, v234, 7, 2\n" \
"v_mfma_f32_16x16x32_bf16 v[116:119], a[104:107], a[8:11], v[116:119]\n" \
"ds_read_b64_tr_b16 a[78:79], v233 offset:26624\n" \
"v_mul_u32_u24 v254, 3, v254\n" \
"v_mfma_f32_16x16x32_bf16 v[116:119], a[108:111], a[12:15], v[116:119]\n" \
"ds_read_b64_tr_b16 a[80:81], v232 offset:16896\n" \
"v_bfe_u32 v243, v234, 5, 2\n" \
"v_mfma_f32_16x16x32_bf16 v[112:115], a[96:99], a[8:11], v[112:115]\n" \
"s_add_u32 s93, s94, 8192\n" \
"ds_read_b64_tr_b16 a[82:83], v232 offset:18944\n" \
"v_min_u32 v243, 2, v243\n" \
"v_mfma_f32_16x16x32_bf16 v[112:115], a[100:103], a[12:15], v[112:115]\n" \
"buffer_store_dwordx4 v[32:35], v236, s[56:59], s93 offen offset:0 sc1\n" \
"v_mfma_f32_16x16x32_bf16 v[144:147], a[96:99], a[16:19], v[144:147]\n" \
"s_waitcnt vmcnt(35)\n" \
"ds_read_b128 a[8:11], v230 offset:2048\n" \
"v_add_u32 v254, v254, v243\n" \
"v_mfma_f32_16x16x32_bf16 v[144:147], a[100:103], a[20:23], v[144:147]\n" \
"ds_read_b128 a[12:15], v231 offset:2048\n" \
"v_and_b32 v243, 16, v234\n" \
"v_mfma_f32_16x16x32_bf16 v[148:151], a[104:107], a[16:19], v[148:151]\n" \
"ds_read_b64_tr_b16 a[84:85], v232 offset:25088\n" \
"v_lshl_add_u32 v243, v243, 2, s64\n" \
"v_mfma_f32_16x16x32_bf16 v[148:151], a[108:111], a[20:23], v[148:151]\n" \
"v_lshl_add_u32 v254, v254, 11, v243\n" \
"v_mfma_f32_16x16x32_bf16 v[152:155], a[112:115], a[16:19], v[152:155]\n" \
"ds_read_b64_tr_b16 a[86:87], v232 offset:27136\n" \
"v_mfma_f32_16x16x32_bf16 v[152:155], a[116:119], a[20:23], v[152:155]\n" \
"ds_read_b64_tr_b16 a[88:89], v233 offset:16896\n" \
"v_mfma_f32_16x16x32_bf16 v[156:159], a[120:123], a[16:19], v[156:159]\n" \
"ds_read_b64_tr_b16 a[90:91], v233 offset:18944\n" \
"v_mfma_f32_16x16x32_bf16 v[156:159], a[124:127], a[20:23], v[156:159]\n" \
"v_mfma_f32_16x16x32_bf16 v[188:191], a[120:123], a[24:27], v[188:191]\n" \
"s_waitcnt vmcnt(33)\n" \
"ds_read_b128 a[16:19], v230 offset:4096\n" \
"v_mfma_f32_16x16x32_bf16 v[188:191], a[124:127], a[28:31], v[188:191]\n" \
"ds_read_b128 a[20:23], v231 offset:4096\n" \
"v_mfma_f32_16x16x32_bf16 v[184:187], a[112:115], a[24:27], v[184:187]\n" \
"ds_read_b64_tr_b16 a[92:93], v233 offset:25088\n" \
"v_mfma_f32_16x16x32_bf16 v[184:187], a[116:119], a[28:31], v[184:187]\n" \
"v_mfma_f32_16x16x32_bf16 v[180:183], a[104:107], a[24:27], v[180:183]\n" \
"ds_read_b64_tr_b16 a[94:95], v233 offset:27136\n" \
"v_mfma_f32_16x16x32_bf16 v[180:183], a[108:111], a[28:31], v[180:183]\n" \
"v_mfma_f32_16x16x32_bf16 v[176:179], a[96:99], a[24:27], v[176:179]\n" \
"buffer_store_dwordx4 v[48:51], v236, s[56:59], s93 offen offset:256 sc1\n" \
"Lpg4453_%=:\n" \
"s_cbranch_execz Lpg6195_%=\n" \
"v_mfma_f32_16x16x32_bf16 v[176:179], a[100:103], a[28:31], v[176:179]\n" \
"s_waitcnt lgkmcnt(1)\n" \
"v_mfma_f32_16x16x32_bf16 v[64:67], a[64:67], a[0:3], v[64:67]\n" \
"s_waitcnt vmcnt(32)\n" \
"s_lshl_b32 s68, s63, 2\n" \
"ds_read_b128 a[24:27], v230 offset:6144\n" \
"v_mfma_f32_16x16x32_bf16 v[64:67], a[68:71], a[4:7], v[64:67]\n" \
"s_add_u32 s68, s68, s90\n" \
"ds_read_b128 a[28:31], v231 offset:6144\n" \
"v_mfma_f32_16x16x32_bf16 v[68:71], a[72:75], a[0:3], v[68:71]\n" \
"ds_read_b64_tr_b16 a[96:97], v232 offset:17408\n" \
"v_mfma_f32_16x16x32_bf16 v[68:71], a[76:79], a[4:7], v[68:71]\n" \
"v_mov_b32 v228, s60\n" \
"v_lshlrev_b32 v229, 28, s61\n" \
"v_mfma_f32_16x16x32_bf16 v[72:75], a[80:83], a[0:3], v[72:75]\n" \
"ds_read_b64_tr_b16 a[98:99], v232 offset:19456\n" \
"v_mfma_f32_16x16x32_bf16 v[72:75], a[84:87], a[4:7], v[72:75]\n" \
"s_waitcnt lgkmcnt(3)\n" \
"ds_read_b64_tr_b16 a[100:101], v232 offset:25600\n" \
"v_mfma_f32_16x16x32_bf16 v[76:79], a[88:91], a[0:3], v[76:79]\n" \
"ds_read_b64_tr_b16 a[102:103], v232 offset:27648\n" \
"v_mfma_f32_16x16x32_bf16 v[76:79], a[92:95], a[4:7], v[76:79]\n" \
"v_mfma_f32_16x16x32_bf16 v[108:111], a[88:91], a[8:11], v[108:111]\n" \
"ds_read_b64_tr_b16 a[104:105], v233 offset:17408\n" \
"v_mfma_f32_16x16x32_bf16 v[108:111], a[92:95], a[12:15], v[108:111]\n" \
"ds_read_b64_tr_b16 a[106:107], v233 offset:19456\n" \
"v_mfma_f32_16x16x32_bf16 v[104:107], a[80:83], a[8:11], v[104:107]\n" \
"ds_read_b64_tr_b16 a[108:109], v233 offset:25600\n" \
"v_mfma_f32_16x16x32_bf16 v[104:107], a[84:87], a[12:15], v[104:107]\n" \
"v_mfma_f32_16x16x32_bf16 v[100:103], a[72:75], a[8:11], v[100:103]\n" \
"ds_read_b64_tr_b16 a[110:111], v233 offset:27648\n" \
"v_mfma_f32_16x16x32_bf16 v[100:103], a[76:79], a[12:15], v[100:103]\n" \
"ds_read_b64_tr_b16 a[112:113], v232 offset:17920\n" \
"v_mfma_f32_16x16x32_bf16 v[96:99], a[64:67], a[8:11], v[96:99]\n" \
"ds_read_b64_tr_b16 a[114:115], v232 offset:19968\n" \
"v_mfma_f32_16x16x32_bf16 v[96:99], a[68:71], a[12:15], v[96:99]\n" \
"v_mfma_f32_16x16x32_bf16 v[128:131], a[64:67], a[16:19], v[128:131]\n" \
"ds_read_b64_tr_b16 a[116:117], v232 offset:26112\n" \
"v_mfma_f32_16x16x32_bf16 v[128:131], a[68:71], a[20:23], v[128:131]\n" \
"ds_read_b64_tr_b16 a[118:119], v232 offset:28160\n" \
"v_mfma_f32_16x16x32_bf16 v[132:135], a[72:75], a[16:19], v[132:135]\n" \
"ds_read_b64_tr_b16 a[120:121], v233 offset:17920\n" \
"v_mfma_f32_16x16x32_bf16 v[132:135], a[76:79], a[20:23], v[132:135]\n" \
"buffer_store_dwordx4 v[40:43], v236, s[56:59], s93 offen offset:2048 sc1\n" \
"v_mfma_f32_16x16x32_bf16 v[136:139], a[80:83], a[16:19], v[136:139]\n" \
"ds_read_b64_tr_b16 a[122:123], v233 offset:19968\n" \
"v_mfma_f32_16x16x32_bf16 v[136:139], a[84:87], a[20:23], v[136:139]\n" \
"ds_read_b64_tr_b16 a[124:125], v233 offset:26112\n" \
"v_mfma_f32_16x16x32_bf16 v[140:143], a[88:91], a[16:19], v[140:143]\n" \
"ds_read_b64_tr_b16 a[126:127], v233 offset:28160\n" \
"v_mfma_f32_16x16x32_bf16 v[140:143], a[92:95], a[20:23], v[140:143]\n" \
"s_waitcnt lgkmcnt(3)\n" \
"v_mfma_f32_16x16x32_bf16 v[172:175], a[88:91], a[24:27], v[172:175]\n" \
"v_mfma_f32_16x16x32_bf16 v[172:175], a[92:95], a[28:31], v[172:175]\n" \
"v_mfma_f32_16x16x32_bf16 v[168:171], a[80:83], a[24:27], v[168:171]\n" \
"v_mfma_f32_16x16x32_bf16 v[168:171], a[84:87], a[28:31], v[168:171]\n" \
"v_mfma_f32_16x16x32_bf16 v[164:167], a[72:75], a[24:27], v[164:167]\n" \
"v_mfma_f32_16x16x32_bf16 v[164:167], a[76:79], a[28:31], v[164:167]\n" \
"v_mfma_f32_16x16x32_bf16 v[160:163], a[64:67], a[24:27], v[160:163]\n" \
"v_mfma_f32_16x16x32_bf16 v[160:163], a[68:71], a[28:31], v[160:163]\n" \
"s_waitcnt vmcnt(3) lgkmcnt(0)\n" \
"s_barrier\n" \
"v_mfma_f32_16x16x32_bf16 v[80:83], a[96:99], a[0:3], v[80:83]\n" \
"ds_read_b64_tr_b16 a[64:65], v232 offset:32768\n" \
"v_mfma_f32_16x16x32_bf16 v[80:83], a[100:103], a[4:7], v[80:83]\n" \
"ds_read_b64_tr_b16 a[66:67], v232 offset:34816\n" \
"buffer_store_dword v228, v229, s[56:59], s68 offen sc1\n" \
"v_mfma_f32_16x16x32_bf16 v[84:87], a[104:107], a[0:3], v[84:87]\n" \
"ds_read_b64_tr_b16 a[68:69], v232 offset:40960\n" \
"v_lshrrev_b32 v238, 4, v234\n" \
"v_mfma_f32_16x16x32_bf16 v[84:87], a[108:111], a[4:7], v[84:87]\n" \
"buffer_store_dwordx4 v[56:59], v236, s[56:59], s93 offen offset:2304 sc1\n" \
"v_mfma_f32_16x16x32_bf16 v[88:91], a[112:115], a[0:3], v[88:91]\n" \
"ds_read_b64_tr_b16 a[70:71], v232 offset:43008\n" \
"v_lshrrev_b32 v240, 4, v238\n" \
"v_mfma_f32_16x16x32_bf16 v[88:91], a[116:119], a[4:7], v[88:91]\n" \
"ds_read_b64_tr_b16 a[72:73], v233 offset:32768\n" \
"v_lshlrev_b32 v240, 2, v240\n" \
"v_mfma_f32_16x16x32_bf16 v[92:95], a[120:123], a[0:3], v[92:95]\n" \
"ds_read_b64_tr_b16 a[74:75], v233 offset:34816\n" \
"v_and_b32 v238, 15, v238\n" \
"v_mfma_f32_16x16x32_bf16 v[92:95], a[124:127], a[4:7], v[92:95]\n" \
"ds_write_b128 v224, v[192:195] offset:8192\n" \
"v_mfma_f32_16x16x32_bf16 v[124:127], a[120:123], a[8:11], v[124:127]\n" \
"ds_read_b128 a[0:3], v230 offset:32768\n" \
"v_sub_u32 v238, v238, v240\n" \
"v_mfma_f32_16x16x32_bf16 v[124:127], a[124:127], a[12:15], v[124:127]\n" \
"ds_read_b128 a[4:7], v231 offset:32768\n" \
"v_subrev_u32 v248, 0, v238\n" \
"v_mfma_f32_16x16x32_bf16 v[120:123], a[112:115], a[8:11], v[120:123]\n" \
"ds_read_b64_tr_b16 a[76:77], v233 offset:40960\n" \
"v_subrev_u32 v249, 1, v238\n" \
"v_mfma_f32_16x16x32_bf16 v[120:123], a[116:119], a[12:15], v[120:123]\n" \
"ds_write_b128 v224, v[208:211] offset:8448\n" \
"v_mfma_f32_16x16x32_bf16 v[116:119], a[104:107], a[8:11], v[116:119]\n" \
"ds_read_b64_tr_b16 a[78:79], v233 offset:43008\n" \
"v_subrev_u32 v250, 2, v238\n" \
"v_mfma_f32_16x16x32_bf16 v[116:119], a[108:111], a[12:15], v[116:119]\n" \
"ds_read_b64_tr_b16 a[80:81], v232 offset:33280\n" \
"v_subrev_u32 v251, 3, v238\n" \
"v_mfma_f32_16x16x32_bf16 v[112:115], a[96:99], a[8:11], v[112:115]\n" \
"ds_read_b64_tr_b16 a[82:83], v232 offset:35328\n" \
"v_ashrrev_i32 v248, 31, v248\n" \
"v_mfma_f32_16x16x32_bf16 v[112:115], a[100:103], a[12:15], v[112:115]\n" \
"v_ashrrev_i32 v249, 31, v249\n" \
"v_ashrrev_i32 v250, 31, v250\n" \
"v_mfma_f32_16x16x32_bf16 v[144:147], a[96:99], a[16:19], v[144:147]\n" \
"ds_read_b128 a[8:11], v230 offset:34816\n" \
"v_ashrrev_i32 v251, 31, v251\n" \
"v_mfma_f32_16x16x32_bf16 v[144:147], a[100:103], a[20:23], v[144:147]\n" \
"ds_read_b128 a[12:15], v231 offset:34816\n" \
"v_lshlrev_b32 v248, 23, v248\n" \
"v_mfma_f32_16x16x32_bf16 v[148:151], a[104:107], a[16:19], v[148:151]\n" \
"ds_read_b64_tr_b16 a[84:85], v232 offset:41472\n" \
"v_lshlrev_b32 v249, 23, v249\n" \
"v_mfma_f32_16x16x32_bf16 v[148:151], a[108:111], a[20:23], v[148:151]\n" \
"ds_write_b128 v224, v[200:203] offset:10240\n" \
"v_mfma_f32_16x16x32_bf16 v[152:155], a[112:115], a[16:19], v[152:155]\n" \
"ds_read_b64_tr_b16 a[86:87], v232 offset:43520\n" \
"v_lshlrev_b32 v250, 23, v250\n" \
"v_mfma_f32_16x16x32_bf16 v[152:155], a[116:119], a[20:23], v[152:155]\n" \
"ds_read_b64_tr_b16 a[88:89], v233 offset:33280\n" \
"v_lshlrev_b32 v251, 23, v251\n" \
"v_mfma_f32_16x16x32_bf16 v[156:159], a[120:123], a[16:19], v[156:159]\n" \
"s_add_u32 s93, s94, 0\n" \
"ds_read_b64_tr_b16 a[90:91], v233 offset:35328\n" \
"v_mov_b32 v240, 0xff800000\n" \
"v_mfma_f32_16x16x32_bf16 v[156:159], a[124:127], a[20:23], v[156:159]\n" \
"buffer_store_dwordx4 v[36:39], v236, s[56:59], s93 offen offset:0 sc1\n" \
"v_mfma_f32_16x16x32_bf16 v[188:191], a[120:123], a[24:27], v[188:191]\n" \
"ds_read_b128 a[16:19], v230 offset:36864\n" \
"v_add_f32 v248, 0x42400000, v248\n" \
"v_mfma_f32_16x16x32_bf16 v[188:191], a[124:127], a[28:31], v[188:191]\n" \
"ds_read_b128 a[20:23], v231 offset:36864\n" \
"v_add_f32 v249, 0x42400000, v249\n" \
"v_mfma_f32_16x16x32_bf16 v[184:187], a[112:115], a[24:27], v[184:187]\n" \
"ds_read_b64_tr_b16 a[92:93], v233 offset:41472\n" \
"v_add_f32 v250, 0x42400000, v250\n" \
"v_mfma_f32_16x16x32_bf16 v[184:187], a[116:119], a[28:31], v[184:187]\n" \
"ds_write_b128 v224, v[216:219] offset:10496\n" \
"v_mfma_f32_16x16x32_bf16 v[180:183], a[104:107], a[24:27], v[180:183]\n" \
"ds_read_b64_tr_b16 a[94:95], v233 offset:43520\n" \
"v_add_f32 v251, 0x42400000, v251\n" \
"v_mfma_f32_16x16x32_bf16 v[180:183], a[108:111], a[28:31], v[180:183]\n" \
"s_mov_b32 s72, 0x42400000\n" \
"s_cmp_gt_u32 s61, 0\n" \
"s_cselect_b32 s70, s72, 0xff800000\n" \
"v_mfma_f32_16x16x32_bf16 v[176:179], a[96:99], a[24:27], v[176:179]\n" \
"ds_write_b128 v224, v[196:199] offset:0\n" \
"v_mfma_f32_16x16x32_bf16 v[176:179], a[100:103], a[28:31], v[176:179]\n" \
"s_waitcnt lgkmcnt(3)\n" \
"v_mov_b32 v252, s70\n" \
"v_mfma_f32_16x16x32_bf16 v[64:67], a[64:67], a[0:3], v[64:67]\n" \
"ds_read_b128 a[24:27], v230 offset:38912\n" \
"v_mfma_f32_16x16x32_bf16 v[64:67], a[68:71], a[4:7], v[64:67]\n" \
"s_cmp_gt_u32 s61, 1\n" \
"s_cselect_b32 s70, s72, 0xff800000\n" \
"ds_read_b128 a[28:31], v231 offset:38912\n" \
"v_mfma_f32_16x16x32_bf16 v[68:71], a[72:75], a[0:3], v[68:71]\n" \
"ds_read_b64_tr_b16 a[96:97], v232 offset:33792\n" \
"v_mov_b32 v253, s70\n" \
"v_mfma_f32_16x16x32_bf16 v[68:71], a[76:79], a[4:7], v[68:71]\n" \
"s_cmp_gt_u32 s61, 2\n" \
"s_cselect_b32 s70, s72, 0xff800000\n" \
"v_mov_b32 v226, s70\n" \
"v_mfma_f32_16x16x32_bf16 v[72:75], a[80:83], a[0:3], v[72:75]\n" \
"ds_read_b64_tr_b16 a[98:99], v232 offset:35840\n" \
"v_bfe_i32 v243, v234, 4, 1\n" \
"v_mfma_f32_16x16x32_bf16 v[72:75], a[84:87], a[4:7], v[72:75]\n" \
"s_waitcnt lgkmcnt(3)\n" \
"ds_read_b64_tr_b16 a[100:101], v232 offset:41984\n" \
"v_and_b32 v243, 0xaaaa, v243\n" \
"v_mfma_f32_16x16x32_bf16 v[76:79], a[88:91], a[0:3], v[76:79]\n" \
"ds_read_b64_tr_b16 a[102:103], v232 offset:44032\n" \
"v_or_b32 v243, 0x4444, v243\n" \
"v_mfma_f32_16x16x32_bf16 v[76:79], a[92:95], a[4:7], v[76:79]\n" \
"ds_write_b128 v224, v[212:215] offset:256\n" \
"v_mfma_f32_16x16x32_bf16 v[108:111], a[88:91], a[8:11], v[108:111]\n" \
"ds_read_b64_tr_b16 a[104:105], v233 offset:33792\n" \
"v_bfe_i32 v244, v234, 5, 1\n" \
"v_mfma_f32_16x16x32_bf16 v[108:111], a[92:95], a[12:15], v[108:111]\n" \
"ds_read_b64_tr_b16 a[106:107], v233 offset:35840\n" \
"v_not_b32 v244, v244\n" \
"v_mfma_f32_16x16x32_bf16 v[104:107], a[80:83], a[8:11], v[104:107]\n" \
"ds_read_b64_tr_b16 a[108:109], v233 offset:41984\n" \
"v_and_b32 v244, 0x3f803f80, v244\n" \
"v_mfma_f32_16x16x32_bf16 v[104:107], a[84:87], a[12:15], v[104:107]\n" \
"buffer_store_dwordx4 v[16:19], v236, s[56:59], s93 offen offset:256 sc1\n" \
"v_mfma_f32_16x16x32_bf16 v[100:103], a[72:75], a[8:11], v[100:103]\n" \
"ds_read_b64_tr_b16 a[110:111], v233 offset:44032\n" \
"v_mov_b32 v245, v244\n" \
"v_mfma_f32_16x16x32_bf16 v[100:103], a[76:79], a[12:15], v[100:103]\n" \
"ds_read_b64_tr_b16 a[112:113], v232 offset:34304\n" \
"v_mov_b32 v246, v244\n" \
"v_mfma_f32_16x16x32_bf16 v[96:99], a[64:67], a[8:11], v[96:99]\n" \
"ds_read_b64_tr_b16 a[114:115], v232 offset:36352\n" \
"v_mov_b32 v247, v244\n" \
"v_mfma_f32_16x16x32_bf16 v[96:99], a[68:71], a[12:15], v[96:99]\n" \
"ds_write_b128 v224, v[204:207] offset:2048\n" \
"v_mfma_f32_16x16x32_bf16 v[128:131], a[64:67], a[16:19], v[128:131]\n" \
"s_mov_b32 s76, s73\n" \
"s_and_b32 s77, s63, 1\n" \
"ds_read_b64_tr_b16 a[116:117], v232 offset:42496\n" \
"v_mfma_f32_16x16x32_bf16 v[128:131], a[68:71], a[20:23], v[128:131]\n" \
"s_lshl_b32 s77, s77, 15\n" \
"s_xor_b32 s89, s77, 0x8000\n" \
"ds_read_b64_tr_b16 a[118:119], v232 offset:44544\n" \
"v_mfma_f32_16x16x32_bf16 v[132:135], a[72:75], a[16:19], v[132:135]\n" \
"s_add_u32 s82, s89, 0x10000\n" \
"s_mov_b32 s74, 0\n" \
"ds_read_b64_tr_b16 a[120:121], v233 offset:34304\n" \
"v_mfma_f32_16x16x32_bf16 v[132:135], a[76:79], a[20:23], v[132:135]\n" \
"ds_write_b128 v224, v[220:223] offset:2304\n" \
"v_mfma_f32_16x16x32_bf16 v[136:139], a[80:83], a[16:19], v[136:139]\n" \
"s_waitcnt lgkmcnt(4)\n" \
"s_add_u32 s73, s73, 34816\n" \
"ds_read_b64_tr_b16 a[122:123], v233 offset:36352\n" \
"v_mfma_f32_16x16x32_bf16 v[136:139], a[84:87], a[20:23], v[136:139]\n" \
"s_cmp_eq_u32 s73, s83\n" \
"s_cselect_b32 s70, 34816, 0\n" \
"ds_read_b64_tr_b16 a[124:125], v233 offset:42496\n" \
"v_mfma_f32_16x16x32_bf16 v[140:143], a[88:91], a[16:19], v[140:143]\n" \
"s_add_u32 s73, s73, s70\n" \
"s_lshr_b32 s70, s61, 1\n" \
"ds_read_b64_tr_b16 a[126:127], v233 offset:44544\n" \
"v_mfma_f32_16x16x32_bf16 v[140:143], a[92:95], a[20:23], v[140:143]\n" \
"s_lshl_b32 s85, s63, 2\n" \
"s_sub_u32 s86, 60, s85\n" \
"s_add_u32 s85, s85, s70\n" \
"v_mfma_f32_16x16x32_bf16 v[172:175], a[88:91], a[24:27], v[172:175]\n" \
"s_add_u32 s86, s86, s70\n" \
"s_mul_i32 s87, s85, 266240\n" \
"v_add_u32 v230, 0x10000, v230\n" \
"v_mfma_f32_16x16x32_bf16 v[172:175], a[92:95], a[28:31], v[172:175]\n" \
"s_mul_i32 s88, s86, 266240\n" \
"s_and_b32 s75, s61, 1\n" \
"v_add_u32 v231, 0x10000, v231\n" \
"v_mfma_f32_16x16x32_bf16 v[168:171], a[80:83], a[24:27], v[168:171]\n" \
"s_lshl_b32 s70, s75, 8\n" \
"s_lshl_b32 s91, s62, 13\n" \
"s_add_u32 s91, s91, s70\n" \
"v_mfma_f32_16x16x32_bf16 v[168:171], a[84:87], a[28:31], v[168:171]\n" \
"buffer_store_dwordx4 v[52:55], v236, s[56:59], s93 offen offset:2048 sc1\n" \
"v_mfma_f32_16x16x32_bf16 v[164:167], a[72:75], a[24:27], v[164:167]\n" \
"s_add_u32 s91, s91, 35700736\n" \
"s_add_u32 s87, s87, s91\n" \
"s_add_u32 s88, s88, s91\n" \
"v_mfma_f32_16x16x32_bf16 v[164:167], a[76:79], a[28:31], v[164:167]\n" \
"s_lshl_b32 s75, s75, 14\n" \
"s_cmp_eq_u32 s63, 7\n" \
"s_cselect_b32 s75, 0, s75\n" \
"s_xor_b32 s78, s75, 0xc000\n" \
"s_mov_b32 s81, s78\n" \
"s_cmp_eq_u32 s63, 0\n" \
"s_cselect_b32 s91, s75, 0\n" \
"s_add_u32 s91, s91, 0x8000\n" \
"s_xor_b32 s75, s75, 0x8000\n" \
"s_sub_u32 s84, 7, s63\n" \
"v_mfma_f32_16x16x32_bf16 v[160:163], a[64:67], a[24:27], v[160:163]\n" \
"v_mfma_f32_16x16x32_bf16 v[160:163], a[68:71], a[28:31], v[160:163]\n" \
"s_waitcnt vmcnt(18) lgkmcnt(0)\n" \
"s_barrier\n" \
"v_mfma_f32_16x16x32_bf16 v[144:147], a[96:99], a[16:19], v[144:147]\n" \
"s_mov_b64 s[44:45], s[10:11]\n" \
"s_mov_b32 s46, 0\n" \
"ds_read_b64_tr_b16 a[64:65], v232 offset:49152\n" \
"v_mfma_f32_16x16x32_bf16 v[144:147], a[100:103], a[20:23], v[144:147]\n" \
"s_mov_b32 s47, 0\n" \
"s_cmp_eq_u32 s63, 7\n" \
"s_cselect_b64 s[44:45], s[18:19], s[44:45]\n" \
"s_cselect_b32 s46, 5, s46\n" \
"s_cmp_eq_u32 s63, 3\n" \
"s_cselect_b64 s[44:45], s[18:19], s[44:45]\n" \
"s_cselect_b32 s46, 3, s46\n" \
"ds_read_b64_tr_b16 a[66:67], v232 offset:51200\n" \
"v_mfma_f32_16x16x32_bf16 v[148:151], a[104:107], a[16:19], v[148:151]\n" \
"s_cmp_eq_u32 s63, 3\n" \
"s_cselect_b32 s47, 640, s47\n" \
"ds_read_b64_tr_b16 a[68:69], v232 offset:57344\n" \
"v_mfma_f32_16x16x32_bf16 v[148:151], a[108:111], a[20:23], v[148:151]\n" \
"ds_write_b128 v225, a[192:195] offset:0\n" \
"v_mfma_f32_16x16x32_bf16 v[152:155], a[112:115], a[16:19], v[152:155]\n" \
"ds_read_b64_tr_b16 a[70:71], v232 offset:59392\n" \
"v_mfma_f32_16x16x32_bf16 v[152:155], a[116:119], a[20:23], v[152:155]\n" \
"s_cmp_eq_u32 s63, 6\n" \
"s_cselect_b64 s[44:45], s[22:23], s[44:45]\n" \
"s_cselect_b32 s46, 6, s46\n" \
"ds_read_b64_tr_b16 a[72:73], v233 offset:49152\n" \
"v_mfma_f32_16x16x32_bf16 v[156:159], a[120:123], a[16:19], v[156:159]\n" \
"ds_read_b64_tr_b16 a[74:75], v233 offset:51200\n" \
"v_mfma_f32_16x16x32_bf16 v[156:159], a[124:127], a[20:23], v[156:159]\n" \
"s_cmp_eq_u32 s63, 4\n" \
"s_cselect_b64 s[44:45], s[20:21], s[44:45]\n" \
"s_cselect_b32 s46, 4, s46\n" \
"v_mfma_f32_16x16x32_bf16 v[188:191], a[120:123], a[24:27], v[188:191]\n" \
"ds_read_b128 a[16:19], v230 offset:0\n" \
"v_mfma_f32_16x16x32_bf16 v[188:191], a[124:127], a[28:31], v[188:191]\n" \
"s_cmp_eq_u32 s63, 2\n" \
"s_cselect_b64 s[44:45], s[24:25], s[44:45]\n" \
"s_cselect_b32 s46, 2, s46\n" \
"ds_read_b128 a[20:23], v231 offset:0\n" \
"v_mfma_f32_16x16x32_bf16 v[184:187], a[112:115], a[24:27], v[184:187]\n" \
"s_lshl_b32 s48, s62, 2\n" \
"s_add_u32 s48, s48, s61\n" \
"ds_read_b64_tr_b16 a[76:77], v233 offset:57344\n" \
"v_mfma_f32_16x16x32_bf16 v[184:187], a[116:119], a[28:31], v[184:187]\n" \
"ds_write_b128 v225, a[196:199] offset:1024\n" \
"v_mfma_f32_16x16x32_bf16 v[180:183], a[104:107], a[24:27], v[180:183]\n" \
"s_add_u32 s48, s48, s47\n" \
"s_lshl_b32 s48, s48, 13\n" \
"ds_read_b64_tr_b16 a[78:79], v233 offset:59392\n" \
"v_mfma_f32_16x16x32_bf16 v[180:183], a[108:111], a[28:31], v[180:183]\n" \
"s_add_u32 s44, s44, s48\n" \
"s_addc_u32 s45, s45, 0\n" \
"ds_read_b64_tr_b16 a[80:81], v232 offset:49664\n" \
"v_mfma_f32_16x16x32_bf16 v[176:179], a[96:99], a[24:27], v[176:179]\n" \
"s_cmp_eq_u32 s46, 0\n" \
"s_cselect_b32 s48, 28, 3\n" \
"ds_read_b64_tr_b16 a[82:83], v232 offset:51712\n" \
"v_mfma_f32_16x16x32_bf16 v[176:179], a[100:103], a[28:31], v[176:179]\n" \
"buffer_store_dwordx4 v[24:27], v236, s[56:59], s93 offen offset:2304 sc1\n" \
"v_mfma_f32_16x16x32_bf16 v[80:83], a[96:99], a[0:3], v[80:83]\n" \
"ds_read_b128 a[24:27], v230 offset:2048\n" \
"v_mfma_f32_16x16x32_bf16 v[80:83], a[100:103], a[4:7], v[80:83]\n" \
"ds_read_b128 a[28:31], v231 offset:2048\n" \
"v_mfma_f32_16x16x32_bf16 v[84:87], a[104:107], a[0:3], v[84:87]\n" \
"ds_read_b64_tr_b16 a[84:85], v232 offset:57856\n" \
"v_mfma_f32_16x16x32_bf16 v[84:87], a[108:111], a[4:7], v[84:87]\n" \
"ds_write_b128 v225, a[200:203] offset:2048\n" \
"v_mfma_f32_16x16x32_bf16 v[88:91], a[112:115], a[0:3], v[88:91]\n" \
"ds_read_b64_tr_b16 a[86:87], v232 offset:59904\n" \
"v_mfma_f32_16x16x32_bf16 v[88:91], a[116:119], a[4:7], v[88:91]\n" \
"ds_read_b64_tr_b16 a[88:89], v233 offset:49664\n" \
"v_mfma_f32_16x16x32_bf16 v[92:95], a[120:123], a[0:3], v[92:95]\n" \
"ds_read_b64_tr_b16 a[90:91], v233 offset:51712\n" \
"v_mfma_f32_16x16x32_bf16 v[92:95], a[124:127], a[4:7], v[92:95]\n" \
"ds_write_b128 v225, a[204:207] offset:3072\n" \
"v_mfma_f32_16x16x32_bf16 v[124:127], a[120:123], a[8:11], v[124:127]\n" \
"ds_read_b128 a[0:3], v230 offset:4096\n" \
"v_mfma_f32_16x16x32_bf16 v[124:127], a[124:127], a[12:15], v[124:127]\n" \
"ds_read_b128 a[4:7], v231 offset:4096\n" \
"v_mfma_f32_16x16x32_bf16 v[120:123], a[112:115], a[8:11], v[120:123]\n" \
"ds_read_b64_tr_b16 a[92:93], v233 offset:57856\n" \
"v_mfma_f32_16x16x32_bf16 v[120:123], a[116:119], a[12:15], v[120:123]\n" \
"v_mfma_f32_16x16x32_bf16 v[116:119], a[104:107], a[8:11], v[116:119]\n" \
"ds_read_b64_tr_b16 a[94:95], v233 offset:59904\n" \
"v_mfma_f32_16x16x32_bf16 v[116:119], a[108:111], a[12:15], v[116:119]\n" \
"v_mfma_f32_16x16x32_bf16 v[112:115], a[96:99], a[8:11], v[112:115]\n" \
"ds_write_b128 v225, a[224:227] offset:8192\n" \
"v_mfma_f32_16x16x32_bf16 v[112:115], a[100:103], a[12:15], v[112:115]\n" \
"s_waitcnt lgkmcnt(2)\n" \
"v_mfma_f32_16x16x32_bf16 v[64:67], a[64:67], a[16:19], v[64:67]\n" \
"ds_read_b128 a[8:11], v230 offset:6144\n" \
"ds_read_b128 a[12:15], v231 offset:6144\n" \
"v_mfma_f32_16x16x32_bf16 v[64:67], a[68:71], a[20:23], v[64:67]\n" \
"ds_read_b64_tr_b16 a[96:97], v232 offset:50176\n" \
"ds_read_b64_tr_b16 a[98:99], v232 offset:52224\n" \
"v_mfma_f32_16x16x32_bf16 v[68:71], a[72:75], a[16:19], v[68:71]\n" \
"s_waitcnt lgkmcnt(5)\n" \
"ds_read_b64_tr_b16 a[100:101], v232 offset:58368\n" \
"ds_read_b64_tr_b16 a[102:103], v232 offset:60416\n" \
"v_mfma_f32_16x16x32_bf16 v[68:71], a[76:79], a[20:23], v[68:71]\n" \
"s_getpc_b64 s[64:65]\n" \
"Lpd1_%=:\n" \
"s_lshl_b32 s66, s62, 2\n" \
"ds_read_b64_tr_b16 a[104:105], v233 offset:50176\n" \
"ds_read_b64_tr_b16 a[106:107], v233 offset:52224\n" \
"v_mfma_f32_16x16x32_bf16 v[72:75], a[80:83], a[16:19], v[72:75]\n" \
"s_add_u32 s66, s66, s61\n" \
"ds_read_b64_tr_b16 a[108:109], v233 offset:58368\n" \
"ds_read_b64_tr_b16 a[110:111], v233 offset:60416\n" \
"v_mfma_f32_16x16x32_bf16 v[72:75], a[84:87], a[20:23], v[72:75]\n" \
"s_and_b32 s66, s66, 15\n" \
"ds_read_b64_tr_b16 a[112:113], v232 offset:50688\n" \
"ds_read_b64_tr_b16 a[114:115], v232 offset:52736\n" \
"v_mfma_f32_16x16x32_bf16 v[76:79], a[88:91], a[16:19], v[76:79]\n" \
"s_lshl_b32 s66, s66, 12\n" \
"ds_read_b64_tr_b16 a[116:117], v232 offset:58880\n" \
"ds_read_b64_tr_b16 a[118:119], v232 offset:60928\n" \
"v_mfma_f32_16x16x32_bf16 v[76:79], a[92:95], a[20:23], v[76:79]\n" \
"s_add_u32 s64, s64, Lloop2_%=-Lpd1_%=\n" \
"s_addc_u32 s65, s65, 0\n" \
"s_mov_b32 s67, Lcode_end_%=-Lloop2_%=-4\n" \
"ds_read_b64_tr_b16 a[120:121], v233 offset:50688\n" \
"ds_read_b64_tr_b16 a[122:123], v233 offset:52736\n" \
"v_mfma_f32_16x16x32_bf16 v[96:99], a[64:67], a[24:27], v[96:99]\n" \
"s_add_u32 s93, s94, 17858560\n" \
"ds_read_b64_tr_b16 a[124:125], v233 offset:58880\n" \
"ds_read_b64_tr_b16 a[126:127], v233 offset:60928\n" \
"v_mfma_f32_16x16x32_bf16 v[96:99], a[68:71], a[28:31], v[96:99]\n" \
"buffer_store_dwordx4 v[44:47], v234, s[56:59], s93 offen offset:0 sc1\n" \
"v_mfma_f32_16x16x32_bf16 v[100:103], a[72:75], a[24:27], v[100:103]\n" \
"ds_write_b128 v225, a[228:231] offset:9216\n" \
"v_mfma_f32_16x16x32_bf16 v[100:103], a[76:79], a[28:31], v[100:103]\n" \
"v_lshl_add_u32 v230, v234, 2, s66\n" \
"v_min_u32 v230, s67, v230\n" \
"v_mfma_f32_16x16x32_bf16 v[104:107], a[80:83], a[24:27], v[104:107]\n" \
"buffer_load_dword v227, v242, s[56:59], s90 offen sc1\n" \
"v_mfma_f32_16x16x32_bf16 v[104:107], a[84:87], a[28:31], v[104:107]\n" \
"ds_write_b128 v225, a[232:235] offset:10240\n" \
"v_mfma_f32_16x16x32_bf16 v[108:111], a[88:91], a[24:27], v[108:111]\n" \
"global_load_dword v230, v230, s[64:65]\n" \
"v_mfma_f32_16x16x32_bf16 v[108:111], a[92:95], a[28:31], v[108:111]\n" \
"s_waitcnt lgkmcnt(1)\n" \
"v_cvt_pk_bf16_f32 v64, v64, v65\n" \
"v_cvt_pk_bf16_f32 v65, v66, v67\n" \
"v_mfma_f32_16x16x32_bf16 v[80:83], a[96:99], a[16:19], v[80:83]\n" \
"v_cvt_pk_bf16_f32 v66, v68, v69\n" \
"v_cvt_pk_bf16_f32 v67, v70, v71\n" \
"v_mfma_f32_16x16x32_bf16 v[80:83], a[100:103], a[20:23], v[80:83]\n" \
"v_accvgpr_write_b32 a128, v64\n" \
"v_accvgpr_write_b32 a129, v65\n" \
"v_accvgpr_write_b32 a130, v66\n" \
"v_mfma_f32_16x16x32_bf16 v[84:87], a[104:107], a[16:19], v[84:87]\n" \
"v_accvgpr_write_b32 a131, v67\n" \
"v_cvt_pk_bf16_f32 v72, v72, v73\n" \
"v_mfma_f32_16x16x32_bf16 v[84:87], a[108:111], a[20:23], v[84:87]\n" \
"buffer_store_dwordx4 v[60:63], v234, s[56:59], s93 offen offset:1024 sc1\n" \
"v_mfma_f32_16x16x32_bf16 v[88:91], a[112:115], a[16:19], v[88:91]\n" \
"ds_write_b128 v225, a[236:239] offset:11264\n" \
"v_mfma_f32_16x16x32_bf16 v[88:91], a[116:119], a[20:23], v[88:91]\n" \
"v_cvt_pk_bf16_f32 v73, v74, v75\n" \
"v_cvt_pk_bf16_f32 v74, v76, v77\n" \
"v_cvt_pk_bf16_f32 v75, v78, v79\n" \
"v_mfma_f32_16x16x32_bf16 v[92:95], a[120:123], a[16:19], v[92:95]\n" \
"v_accvgpr_write_b32 a132, v72\n" \
"v_accvgpr_write_b32 a133, v73\n" \
"v_mfma_f32_16x16x32_bf16 v[92:95], a[124:127], a[20:23], v[92:95]\n" \
"v_accvgpr_write_b32 a134, v74\n" \
"v_accvgpr_write_b32 a135, v75\n" \
"v_mfma_f32_16x16x32_bf16 v[112:115], a[96:99], a[24:27], v[112:115]\n" \
"v_cvt_pk_bf16_f32 v96, v96, v97\n" \
"v_cvt_pk_bf16_f32 v97, v98, v99\n" \
"v_mfma_f32_16x16x32_bf16 v[112:115], a[100:103], a[28:31], v[112:115]\n" \
"v_cvt_pk_bf16_f32 v98, v100, v101\n" \
"v_cvt_pk_bf16_f32 v99, v102, v103\n" \
"v_mfma_f32_16x16x32_bf16 v[116:119], a[104:107], a[24:27], v[116:119]\n" \
"v_cvt_pk_bf16_f32 v104, v104, v105\n" \
"v_cvt_pk_bf16_f32 v105, v106, v107\n" \
"v_mfma_f32_16x16x32_bf16 v[116:119], a[108:111], a[28:31], v[116:119]\n" \
"v_cvt_pk_bf16_f32 v106, v108, v109\n" \
"v_cvt_pk_bf16_f32 v107, v110, v111\n" \
"v_mfma_f32_16x16x32_bf16 v[120:123], a[112:115], a[24:27], v[120:123]\n" \
"v_mfma_f32_16x16x32_bf16 v[120:123], a[116:119], a[28:31], v[120:123]\n" \
"v_cvt_pk_bf16_f32 v80, v80, v81\n" \
"v_cvt_pk_bf16_f32 v81, v82, v83\n" \
"v_cvt_pk_bf16_f32 v82, v84, v85\n" \
"v_mfma_f32_16x16x32_bf16 v[124:127], a[120:123], a[24:27], v[124:127]\n" \
"v_cvt_pk_bf16_f32 v83, v86, v87\n" \
"v_accvgpr_write_b32 a136, v80\n" \
"v_mfma_f32_16x16x32_bf16 v[124:127], a[124:127], a[28:31], v[124:127]\n" \
"v_accvgpr_write_b32 a137, v81\n" \
"s_waitcnt lgkmcnt(0)\n" \
"s_barrier\n" \
"v_mfma_f32_16x16x32_bf16 v[128:131], a[64:67], a[0:3], v[128:131]\n" \
"v_accvgpr_write_b32 a138, v82\n" \
"v_accvgpr_write_b32 a139, v83\n" \
"v_mfma_f32_16x16x32_bf16 v[128:131], a[68:71], a[4:7], v[128:131]\n" \
"ds_read_b128 a[192:195], v237 offset:0\n" \
"v_cvt_pk_bf16_f32 v88, v88, v89\n" \
"v_mfma_f32_16x16x32_bf16 v[132:135], a[72:75], a[0:3], v[132:135]\n" \
"v_cvt_pk_bf16_f32 v89, v90, v91\n" \
"v_cvt_pk_bf16_f32 v90, v92, v93\n" \
"v_mfma_f32_16x16x32_bf16 v[132:135], a[76:79], a[4:7], v[132:135]\n" \
"ds_read_b128 a[196:199], v237 offset:2048\n" \
"v_cvt_pk_bf16_f32 v91, v94, v95\n" \
"v_mfma_f32_16x16x32_bf16 v[136:139], a[80:83], a[0:3], v[136:139]\n" \
"v_med3_f32 v208, v248, v252, s72\n" \
"v_med3_f32 v209, v249, v252, s72\n" \
"v_mfma_f32_16x16x32_bf16 v[136:139], a[84:87], a[4:7], v[136:139]\n" \
"ds_read_b128 a[200:203], v237 offset:4096\n" \
"v_med3_f32 v210, v250, v252, s72\n" \
"v_med3_f32 v211, v251, v252, s72\n" \
"v_mfma_f32_16x16x32_bf16 v[140:143], a[88:91], a[0:3], v[140:143]\n" \
"buffer_store_dwordx4 v[20:23], v234, s[56:59], s93 offen offset:2048 sc1\n" \
"v_mfma_f32_16x16x32_bf16 v[140:143], a[92:95], a[4:7], v[140:143]\n" \
"ds_read_b128 a[204:207], v237 offset:6144\n" \
"v_cvt_pk_bf16_f32 v112, v112, v113\n" \
"v_mfma_f32_16x16x32_bf16 v[144:147], a[96:99], a[0:3], v[144:147]\n" \
"v_cvt_pk_bf16_f32 v113, v114, v115\n" \
"v_cvt_pk_bf16_f32 v114, v116, v117\n" \
"v_mfma_f32_16x16x32_bf16 v[144:147], a[100:103], a[4:7], v[144:147]\n" \
"ds_read_b128 a[208:211], v237 offset:256\n" \
"v_cvt_pk_bf16_f32 v115, v118, v119\n" \
"v_cvt_pk_bf16_f32 v120, v120, v121\n" \
"v_mfma_f32_16x16x32_bf16 v[148:151], a[104:107], a[0:3], v[148:151]\n" \
"v_cvt_pk_bf16_f32 v121, v122, v123\n" \
"v_cvt_pk_bf16_f32 v122, v124, v125\n" \
"v_mfma_f32_16x16x32_bf16 v[148:151], a[108:111], a[4:7], v[148:151]\n" \
"ds_read_b128 a[212:215], v237 offset:2304\n" \
"v_cvt_pk_bf16_f32 v123, v126, v127\n" \
"v_mfma_f32_16x16x32_bf16 v[152:155], a[112:115], a[0:3], v[152:155]\n" \
"v_mfma_f32_16x16x32_bf16 v[152:155], a[116:119], a[4:7], v[152:155]\n" \
"ds_read_b128 a[216:219], v237 offset:4352\n" \
"v_cvt_pk_bf16_f32 v128, v128, v129\n" \
"v_mfma_f32_16x16x32_bf16 v[156:159], a[120:123], a[0:3], v[156:159]\n" \
"v_cvt_pk_bf16_f32 v129, v130, v131\n" \
"v_cvt_pk_bf16_f32 v130, v132, v133\n" \
"v_mfma_f32_16x16x32_bf16 v[156:159], a[124:127], a[4:7], v[156:159]\n" \
"ds_read_b128 a[220:223], v237 offset:6400\n" \
"v_cvt_pk_bf16_f32 v131, v134, v135\n" \
"v_cvt_pk_bf16_f32 v136, v136, v137\n" \
"v_mfma_f32_16x16x32_bf16 v[160:163], a[64:67], a[8:11], v[160:163]\n" \
"v_cvt_pk_bf16_f32 v137, v138, v139\n" \
"v_cvt_pk_bf16_f32 v138, v140, v141\n" \
"v_mfma_f32_16x16x32_bf16 v[160:163], a[68:71], a[12:15], v[160:163]\n" \
"ds_read_b128 a[224:227], v237 offset:8192\n" \
"v_cvt_pk_bf16_f32 v139, v142, v143\n" \
"v_mfma_f32_16x16x32_bf16 v[164:167], a[72:75], a[8:11], v[164:167]\n" \
"v_mfma_f32_16x16x32_bf16 v[164:167], a[76:79], a[12:15], v[164:167]\n" \
"ds_read_b128 a[228:231], v237 offset:10240\n" \
"v_mfma_f32_16x16x32_bf16 v[168:171], a[80:83], a[8:11], v[168:171]\n" \
"v_mfma_f32_16x16x32_bf16 v[168:171], a[84:87], a[12:15], v[168:171]\n" \
"ds_read_b128 a[232:235], v237 offset:12288\n" \
"v_cvt_pk_bf16_f32 v144, v144, v145\n" \
"v_cvt_pk_bf16_f32 v145, v146, v147\n" \
"v_mfma_f32_16x16x32_bf16 v[172:175], a[88:91], a[8:11], v[172:175]\n" \
"v_cvt_pk_bf16_f32 v146, v148, v149\n" \
"v_cvt_pk_bf16_f32 v147, v150, v151\n" \
"v_mfma_f32_16x16x32_bf16 v[172:175], a[92:95], a[12:15], v[172:175]\n" \
"ds_read_b128 a[236:239], v237 offset:14336\n" \
"v_cvt_pk_bf16_f32 v152, v152, v153\n" \
"v_mfma_f32_16x16x32_bf16 v[176:179], a[96:99], a[8:11], v[176:179]\n" \
"v_cvt_pk_bf16_f32 v153, v154, v155\n" \
"v_cvt_pk_bf16_f32 v154, v156, v157\n" \
"v_mfma_f32_16x16x32_bf16 v[176:179], a[100:103], a[12:15], v[176:179]\n" \
"ds_read_b128 a[240:243], v237 offset:8448\n" \
"v_cvt_pk_bf16_f32 v155, v158, v159\n" \
"v_mfma_f32_16x16x32_bf16 v[180:183], a[104:107], a[8:11], v[180:183]\n" \
"buffer_store_dwordx4 v[28:31], v234, s[56:59], s93 offen offset:3072 sc1\n" \
"v_mfma_f32_16x16x32_bf16 v[180:183], a[108:111], a[12:15], v[180:183]\n" \
"ds_read_b128 a[244:247], v237 offset:10496\n" \
"ds_write_b128 v239, v[128:131] offset:0\n" \
"v_mov_b32 v196, 0x42400000\n" \
"v_mfma_f32_16x16x32_bf16 v[184:187], a[112:115], a[8:11], v[184:187]\n" \
"v_mov_b32 v197, 0x42400000\n" \
"v_mov_b32 v198, 0x42400000\n" \
"v_mfma_f32_16x16x32_bf16 v[184:187], a[116:119], a[12:15], v[184:187]\n" \
"ds_read_b128 a[248:251], v237 offset:12544\n" \
"v_mov_b32 v199, 0x42400000\n" \
"v_cvt_pk_bf16_f32 v160, v160, v161\n" \
"v_mfma_f32_16x16x32_bf16 v[188:191], a[120:123], a[8:11], v[188:191]\n" \
"v_cvt_pk_bf16_f32 v161, v162, v163\n" \
"v_cvt_pk_bf16_f32 v162, v164, v165\n" \
"v_mfma_f32_16x16x32_bf16 v[188:191], a[124:127], a[12:15], v[188:191]\n" \
"s_waitcnt lgkmcnt(3)\n" \
"ds_read_b128 a[252:255], v237 offset:14592\n" \
"v_cvt_pk_bf16_f32 v163, v166, v167\n" \
"v_mfma_f32_16x16x32_bf16 v[16:19], a[192:195], v[96:99], v[208:211]\n" \
"v_cvt_pk_bf16_f32 v168, v168, v169\n" \
"v_cvt_pk_bf16_f32 v169, v170, v171\n" \
"v_mfma_f32_16x16x32_bf16 v[16:19], a[196:199], v[104:107], v[16:19]\n" \
"v_cvt_pk_bf16_f32 v170, v172, v173\n" \
"v_cvt_pk_bf16_f32 v171, v174, v175\n" \
"v_mfma_f32_16x16x32_bf16 v[16:19], a[200:203], v[112:115], v[16:19]\n" \
"v_cvt_pk_bf16_f32 v176, v176, v177\n" \
"v_cvt_pk_bf16_f32 v177, v178, v179\n" \
"v_mfma_f32_16x16x32_bf16 v[16:19], a[204:207], v[120:123], v[16:19]\n" \
"v_cvt_pk_bf16_f32 v178, v180, v181\n" \
"v_cvt_pk_bf16_f32 v179, v182, v183\n" \
"v_mfma_f32_16x16x32_bf16 v[32:35], a[204:207], v[152:155], v[196:199]\n" \
"v_cvt_pk_bf16_f32 v184, v184, v185\n" \
"v_cvt_pk_bf16_f32 v185, v186, v187\n" \
"v_mfma_f32_16x16x32_bf16 v[32:35], a[200:203], v[144:147], v[32:35]\n" \
"v_cvt_pk_bf16_f32 v186, v188, v189\n" \
"Lpg5019_%=:\n" \
"s_cbranch_execz Lpg6787_%=\n" \
"v_cvt_pk_bf16_f32 v187, v190, v191\n" \
"v_mfma_f32_16x16x32_bf16 v[32:35], a[196:199], v[136:139], v[32:35]\n" \
"s_add_u32 s93, s94, 17850368\n" \
"v_med3_f32 v212, v248, v253, v252\n" \
"v_med3_f32 v213, v249, v253, v252\n" \
"v_mfma_f32_16x16x32_bf16 v[32:35], a[192:195], v[128:131], v[32:35]\n" \
"buffer_store_dwordx4 v[0:3], v234, s[56:59], s93 offen offset:0 sc1\n" \
"v_mfma_f32_16x16x32_bf16 v[48:51], a[192:195], v[160:163], v[196:199]\n" \
"v_med3_f32 v214, v250, v253, v252\n" \
"v_med3_f32 v215, v251, v253, v252\n" \
"v_mfma_f32_16x16x32_bf16 v[48:51], a[196:199], v[168:171], v[48:51]\n" \
"ds_read_b128 a[192:195], v237 offset:16384\n" \
"v_med3_f32 v216, v248, v226, v253\n" \
"v_mfma_f32_16x16x32_bf16 v[48:51], a[200:203], v[176:179], v[48:51]\n" \
"ds_read_b128 a[196:199], v237 offset:18432\n" \
"v_med3_f32 v217, v249, v226, v253\n" \
"v_mfma_f32_16x16x32_bf16 v[48:51], a[204:207], v[184:187], v[48:51]\n" \
"ds_read_b128 a[200:203], v237 offset:20480\n" \
"ds_write_b128 v239, v[136:139] offset:1024\n" \
"v_mfma_f32_16x16x32_bf16 v[52:55], a[220:223], v[184:187], v[196:199]\n" \
"ds_read_b128 a[204:207], v237 offset:22528\n" \
"v_med3_f32 v218, v250, v226, v253\n" \
"v_mfma_f32_16x16x32_bf16 v[52:55], a[216:219], v[176:179], v[52:55]\n" \
"v_med3_f32 v219, v251, v226, v253\n" \
"v_med3_f32 v220, v248, v240, v226\n" \
"v_mfma_f32_16x16x32_bf16 v[52:55], a[212:215], v[168:171], v[52:55]\n" \
"ds_write_b128 v239, v[144:147] offset:2048\n" \
"v_mfma_f32_16x16x32_bf16 v[52:55], a[208:211], v[160:163], v[52:55]\n" \
"buffer_store_dwordx4 v[4:7], v234, s[56:59], s93 offen offset:1024 sc1\n" \
"v_mfma_f32_16x16x32_bf16 v[36:39], a[208:211], v[128:131], v[196:199]\n" \
"v_med3_f32 v221, v249, v240, v226\n" \
"v_med3_f32 v222, v250, v240, v226\n" \
"v_mfma_f32_16x16x32_bf16 v[36:39], a[212:215], v[136:139], v[36:39]\n" \
"v_med3_f32 v223, v251, v240, v226\n" \
"v_accvgpr_write_b32 a144, v96\n" \
"v_mfma_f32_16x16x32_bf16 v[36:39], a[216:219], v[144:147], v[36:39]\n" \
"v_accvgpr_write_b32 a145, v97\n" \
"v_accvgpr_write_b32 a146, v98\n" \
"v_mfma_f32_16x16x32_bf16 v[36:39], a[220:223], v[152:155], v[36:39]\n" \
"ds_write_b128 v239, v[152:155] offset:3072\n" \
"v_mfma_f32_16x16x32_bf16 v[20:23], a[220:223], v[120:123], v[212:215]\n" \
"v_accvgpr_write_b32 a147, v99\n" \
"v_accvgpr_write_b32 a148, v104\n" \
"v_mfma_f32_16x16x32_bf16 v[20:23], a[216:219], v[112:115], v[20:23]\n" \
"ds_read_b128 a[220:223], v237 offset:22784\n" \
"v_accvgpr_write_b32 a149, v105\n" \
"v_mfma_f32_16x16x32_bf16 v[20:23], a[212:215], v[104:107], v[20:23]\n" \
"ds_read_b128 a[216:219], v237 offset:20736\n" \
"v_accvgpr_write_b32 a150, v106\n" \
"v_mfma_f32_16x16x32_bf16 v[20:23], a[208:211], v[96:99], v[20:23]\n" \
"ds_read_b128 a[212:215], v237 offset:18688\n" \
"ds_write_b128 v239, v[160:163] offset:4096\n" \
"v_mfma_f32_16x16x32_bf16 v[24:27], a[224:227], v[96:99], v[216:219]\n" \
"ds_read_b128 a[208:211], v237 offset:16640\n" \
"v_accvgpr_write_b32 a151, v107\n" \
"v_mfma_f32_16x16x32_bf16 v[24:27], a[228:231], v[104:107], v[24:27]\n" \
"v_accvgpr_write_b32 a152, v112\n" \
"v_accvgpr_write_b32 a153, v113\n" \
"v_mfma_f32_16x16x32_bf16 v[24:27], a[232:235], v[112:115], v[24:27]\n" \
"ds_write_b128 v239, v[168:171] offset:5120\n" \
"v_mfma_f32_16x16x32_bf16 v[24:27], a[236:239], v[120:123], v[24:27]\n" \
"buffer_store_dwordx4 v[8:11], v234, s[56:59], s93 offen offset:2048 sc1\n" \
"v_mfma_f32_16x16x32_bf16 v[40:43], a[236:239], v[152:155], v[196:199]\n" \
"v_accvgpr_write_b32 a154, v114\n" \
"v_accvgpr_write_b32 a155, v115\n" \
"v_mfma_f32_16x16x32_bf16 v[40:43], a[232:235], v[144:147], v[40:43]\n" \
"v_accvgpr_write_b32 a156, v120\n" \
"v_accvgpr_write_b32 a157, v121\n" \
"v_mfma_f32_16x16x32_bf16 v[40:43], a[228:231], v[136:139], v[40:43]\n" \
"v_accvgpr_write_b32 a158, v122\n" \
"v_accvgpr_write_b32 a159, v123\n" \
"v_mfma_f32_16x16x32_bf16 v[40:43], a[224:227], v[128:131], v[40:43]\n" \
"ds_write_b128 v239, v[176:179] offset:6144\n" \
"v_mfma_f32_16x16x32_bf16 v[56:59], a[224:227], v[160:163], v[196:199]\n" \
"v_accvgpr_write_b32 a140, v88\n" \
"v_accvgpr_write_b32 a141, v89\n" \
"v_mfma_f32_16x16x32_bf16 v[56:59], a[228:231], v[168:171], v[56:59]\n" \
"ds_read_b128 a[224:227], v237 offset:24576\n" \
"v_accvgpr_write_b32 a142, v90\n" \
"v_mfma_f32_16x16x32_bf16 v[56:59], a[232:235], v[176:179], v[56:59]\n" \
"s_waitcnt lgkmcnt(2)\n" \
"ds_read_b128 a[228:231], v237 offset:26624\n" \
"v_accvgpr_write_b32 a143, v91\n" \
"v_mfma_f32_16x16x32_bf16 v[56:59], a[236:239], v[184:187], v[56:59]\n" \
"buffer_store_dwordx4 v[12:15], v234, s[56:59], s93 offen offset:3072 sc1\n" \
"ds_write_b128 v239, v[184:187] offset:7168\n" \
"v_mfma_f32_16x16x32_bf16 v[60:63], a[252:255], v[184:187], v[196:199]\n" \
"ds_read_b128 a[232:235], v237 offset:28672\n" \
"v_add_u32 v233, s82, v235\n" \
"v_mfma_f32_16x16x32_bf16 v[60:63], a[248:251], v[176:179], v[60:63]\n" \
"s_waitcnt vmcnt(8)\n" \
"ds_read_b128 a[236:239], v237 offset:30720\n" \
"v_cmp_eq_u32 vcc, s60, v227\n" \
"v_mfma_f32_16x16x32_bf16 v[60:63], a[244:247], v[168:171], v[60:63]\n" \
"v_exp_f32 v16, v16\n" \
"v_mfma_f32_16x16x32_bf16 v[60:63], a[240:243], v[160:163], v[60:63]\n" \
"s_andn2_b32 s70, 0xff, vcc_lo\n" \
"s_cbranch_scc1 Lpl1_%=\n" \
"Lpr1_%=:\n" \
"v_exp_f32 v17, v17\n" \
"v_exp_f32 v18, v18\n" \
"v_mfma_f32_16x16x32_bf16 v[44:47], a[240:243], v[128:131], v[196:199]\n" \
"v_exp_f32 v19, v19\n" \
"v_mfma_f32_16x16x32_bf16 v[44:47], a[244:247], v[136:139], v[44:47]\n" \
"s_add_u32 m0, s79, 32768\n" \
"v_exp_f32 v20, v20\n" \
"v_mfma_f32_16x16x32_bf16 v[44:47], a[248:251], v[144:147], v[44:47]\n" \
"s_add_u32 s71, s76, 0\n" \
"v_cvt_pk_bf16_f32 v16, v16, v17\n" \
"v_cvt_pk_bf16_f32 v17, v18, v19\n" \
"v_mfma_f32_16x16x32_bf16 v[44:47], a[252:255], v[152:155], v[44:47]\n" \
"buffer_load_dwordx4 v234, s[36:39], s71 offen offset:0 sc1 lds\n" \
"v_exp_f32 v21, v21\n" \
"v_exp_f32 v22, v22\n" \
"v_mfma_f32_16x16x32_bf16 v[28:31], a[252:255], v[120:123], v[220:223]\n" \
"v_exp_f32 v23, v23\n" \
"v_mfma_f32_16x16x32_bf16 v[28:31], a[248:251], v[112:115], v[28:31]\n" \
"ds_read_b128 a[252:255], v237 offset:30976\n" \
"v_cvt_pk_bf16_f32 v18, v20, v21\n" \
"v_mfma_f32_16x16x32_bf16 v[28:31], a[244:247], v[104:107], v[28:31]\n" \
"buffer_load_dwordx4 v234, s[36:39], s71 offen offset:1024 sc1 lds\n" \
"v_mfma_f32_16x16x32_bf16 v[28:31], a[240:243], v[96:99], v[28:31]\n" \
"ds_read_b128 a[244:247], v237 offset:26880\n" \
"ds_read_b128 a[248:251], v237 offset:28928\n" \
"v_exp_f32 v32, v32\n" \
"v_exp_f32 v33, v33\n" \
"v_mfma_f32_16x16x32_bf16 v[80:83], a[192:195], a[144:147], v[196:199]\n" \
"ds_read_b128 a[240:243], v237 offset:24832\n" \
"v_cvt_pk_bf16_f32 v19, v22, v23\n" \
"v_mfma_f32_16x16x32_bf16 v[80:83], a[196:199], a[148:151], v[80:83]\n" \
"buffer_load_dwordx4 v234, s[36:39], s71 offen offset:2048 sc1 lds\n" \
"v_mfma_f32_16x16x32_bf16 v[80:83], a[200:203], a[152:155], v[80:83]\n" \
"s_lshl_b32 s68, s63, 2\n" \
"s_add_u32 s68, s68, s95\n" \
"s_mov_b32 s69, 0\n" \
"v_mfma_f32_16x16x32_bf16 v[80:83], a[204:207], a[156:159], v[80:83]\n" \
"s_mov_b32 s89, s63\n" \
"ds_read_b128 a[44:47], v233 offset:15360\n" \
"v_mov_b32 v192, s60\n" \
"v_lshlrev_b32 v193, 28, s61\n" \
"v_exp_f32 v34, v34\n" \
"v_exp_f32 v35, v35\n" \
"v_mfma_f32_16x16x32_bf16 v[96:99], a[204:207], v[152:155], v[196:199]\n" \
"buffer_load_dwordx4 v234, s[36:39], s71 offen offset:3072 sc1 lds\n" \
"v_mfma_f32_16x16x32_bf16 v[96:99], a[200:203], v[144:147], v[96:99]\n" \
"s_add_u32 m0, s79, 49152\n" \
"s_mov_b32 s90, 0\n" \
"ds_read_b128 a[40:43], v233 offset:7168\n" \
"v_mfma_f32_16x16x32_bf16 v[96:99], a[196:199], v[136:139], v[96:99]\n" \
"s_add_u32 s71, s76, 17408\n" \
"v_exp_f32 v36, v36\n" \
"v_cvt_pk_bf16_f32 v32, v32, v33\n" \
"v_mfma_f32_16x16x32_bf16 v[96:99], a[192:195], v[128:131], v[96:99]\n" \
"buffer_load_dwordx4 v234, s[36:39], s71 offen offset:0 sc1 lds\n" \
"v_mfma_f32_16x16x32_bf16 v[112:115], a[192:195], v[160:163], v[196:199]\n" \
"ds_read_b128 a[36:39], v233 offset:14336\n" \
"v_cvt_pk_bf16_f32 v33, v34, v35\n" \
"v_mfma_f32_16x16x32_bf16 v[112:115], a[196:199], v[168:171], v[112:115]\n" \
"v_exp_f32 v37, v37\n" \
"v_mfma_f32_16x16x32_bf16 v[112:115], a[200:203], v[176:179], v[112:115]\n" \
"buffer_load_dwordx4 v234, s[36:39], s71 offen offset:1024 sc1 lds\n" \
"v_mfma_f32_16x16x32_bf16 v[112:115], a[204:207], v[184:187], v[112:115]\n" \
"ds_read_b128 a[32:35], v233 offset:6144\n" \
"v_exp_f32 v38, v38\n" \
"v_exp_f32 v39, v39\n" \
"v_mfma_f32_16x16x32_bf16 v[64:67], a[204:207], a[140:143], v[208:211]\n" \
"v_exp_f32 v48, v48\n" \
"v_mfma_f32_16x16x32_bf16 v[64:67], a[200:203], a[136:139], v[64:67]\n" \
"buffer_load_dwordx4 v234, s[36:39], s71 offen offset:2048 sc1 lds\n" \
"v_cvt_pk_bf16_f32 v34, v36, v37\n" \
"v_mfma_f32_16x16x32_bf16 v[64:67], a[196:199], a[132:135], v[64:67]\n" \
"ds_read_b128 a[204:207], v233 offset:9216\n" \
"v_cvt_pk_bf16_f32 v35, v38, v39\n" \
"v_mfma_f32_16x16x32_bf16 v[64:67], a[192:195], a[128:131], v[64:67]\n" \
"v_exp_f32 v49, v49\n" \
"v_exp_f32 v50, v50\n" \
"v_mfma_f32_16x16x32_bf16 v[68:71], a[208:211], a[128:131], v[212:215]\n" \
"buffer_load_dwordx4 v234, s[36:39], s71 offen offset:3072 sc1 lds\n" \
"v_mfma_f32_16x16x32_bf16 v[68:71], a[212:215], a[132:135], v[68:71]\n" \
"ds_read_b128 a[200:203], v233 offset:1024\n" \
"v_cvt_pk_bf16_f32 v48, v48, v49\n" \
"v_mfma_f32_16x16x32_bf16 v[68:71], a[216:219], a[136:139], v[68:71]\n" \
"s_add_u32 m0, s77, s80\n" \
"v_exp_f32 v51, v51\n" \
"v_mfma_f32_16x16x32_bf16 v[68:71], a[220:223], a[140:143], v[68:71]\n" \
"buffer_load_dwordx4 v234, s[40:43], s76 offen offset:0 sc1 lds\n" \
"v_exp_f32 v52, v52\n" \
"v_exp_f32 v53, v53\n" \
"v_mfma_f32_16x16x32_bf16 v[116:119], a[220:223], v[184:187], v[196:199]\n" \
"ds_read_b128 a[196:199], v233 offset:8192\n" \
"v_cvt_pk_bf16_f32 v49, v50, v51\n" \
"v_mfma_f32_16x16x32_bf16 v[116:119], a[216:219], v[176:179], v[116:119]\n" \
"ds_read_b128 a[160:163], v239 offset:0\n" \
"v_mfma_f32_16x16x32_bf16 v[116:119], a[212:215], v[168:171], v[116:119]\n" \
"s_add_u32 s71, s76, 17408\n" \
"ds_read_b128 a[192:195], v233 offset:0\n" \
"v_exp_f32 v54, v54\n" \
"v_mfma_f32_16x16x32_bf16 v[116:119], a[208:211], v[160:163], v[116:119]\n" \
"buffer_load_dwordx4 v234, s[40:43], s76 offen offset:1024 sc1 lds\n" \
"v_mfma_f32_16x16x32_bf16 v[100:103], a[208:211], v[128:131], v[196:199]\n" \
"ds_read_b128 a[164:167], v239 offset:1024\n" \
"v_cvt_pk_bf16_f32 v50, v52, v53\n" \
"v_mfma_f32_16x16x32_bf16 v[100:103], a[212:215], v[136:139], v[100:103]\n" \
"v_exp_f32 v55, v55\n" \
"v_mfma_f32_16x16x32_bf16 v[100:103], a[216:219], v[144:147], v[100:103]\n" \
"v_cvt_pk_bf16_f32 v51, v54, v55\n" \
"v_exp_f32 v24, v24\n" \
"v_mfma_f32_16x16x32_bf16 v[100:103], a[220:223], v[152:155], v[100:103]\n" \
"buffer_load_dwordx4 v234, s[40:43], s76 offen offset:2048 sc1 lds\n" \
"v_exp_f32 v25, v25\n" \
"v_mfma_f32_16x16x32_bf16 v[84:87], a[220:223], a[156:159], v[196:199]\n" \
"ds_read_b128 a[168:171], v239 offset:2048\n" \
"v_mfma_f32_16x16x32_bf16 v[84:87], a[216:219], a[152:155], v[84:87]\n" \
"ds_read_b128 a[220:223], v233 offset:11264\n" \
"v_mfma_f32_16x16x32_bf16 v[84:87], a[212:215], a[148:151], v[84:87]\n" \
"s_waitcnt lgkmcnt(2)\n" \
"v_exp_f32 v26, v26\n" \
"v_mfma_f32_16x16x32_bf16 v[84:87], a[208:211], a[144:147], v[84:87]\n" \
"buffer_load_dwordx4 v234, s[40:43], s76 offen offset:3072 sc1 lds\n" \
"v_exp_f32 v27, v27\n" \
"v_mfma_f32_16x16x32_bf16 v[88:91], a[224:227], a[144:147], v[196:199]\n" \
"ds_read_b128 a[216:219], v233 offset:3072\n" \
"v_cvt_pk_bf16_f32 v20, v24, v25\n" \
"v_mfma_f32_16x16x32_bf16 v[88:91], a[228:231], a[148:151], v[88:91]\n" \
"ds_read_b128 a[172:175], v239 offset:3072\n" \
"v_exp_f32 v28, v28\n" \
"v_mfma_f32_16x16x32_bf16 v[88:91], a[232:235], a[152:155], v[88:91]\n" \
"s_add_u32 m0, m0, 0x4000\n" \
"ds_read_b128 a[212:215], v233 offset:10240\n" \
"v_xor_b32 v248, 0x2000, v239\n" \
"v_mfma_f32_16x16x32_bf16 v[88:91], a[236:239], a[156:159], v[88:91]\n" \
"buffer_load_dwordx4 v234, s[40:43], s71 offen offset:0 sc1 lds\n" \
"v_exp_f32 v29, v29\n" \
"v_mfma_f32_16x16x32_bf16 v[104:107], a[236:239], v[152:155], v[196:199]\n" \
"ds_read_b128 a[208:211], v233 offset:2048\n" \
"v_lshlrev_b32 v252, s48, v234\n" \
"v_mfma_f32_16x16x32_bf16 v[104:107], a[232:235], v[144:147], v[104:107]\n" \
"ds_read_b128 a[176:179], v239 offset:4096\n" \
"v_mov_b32 v224, 0\n" \
"v_mfma_f32_16x16x32_bf16 v[104:107], a[228:231], v[136:139], v[104:107]\n" \
"v_mov_b32 v225, 0\n" \
"v_mov_b32 v228, 0\n" \
"v_mfma_f32_16x16x32_bf16 v[104:107], a[224:227], v[128:131], v[104:107]\n" \
"buffer_load_dwordx4 v234, s[40:43], s71 offen offset:1024 sc1 lds\n" \
"v_exp_f32 v30, v30\n" \
"v_cvt_pk_bf16_f32 v21, v26, v27\n" \
"v_mfma_f32_16x16x32_bf16 v[120:123], a[224:227], v[160:163], v[196:199]\n" \
"ds_read_b128 a[180:183], v239 offset:5120\n" \
"v_mov_b32 v229, 0\n" \
"v_mfma_f32_16x16x32_bf16 v[120:123], a[228:231], v[168:171], v[120:123]\n" \
"v_exp_f32 v31, v31\n" \
"v_mfma_f32_16x16x32_bf16 v[120:123], a[232:235], v[176:179], v[120:123]\n" \
"v_exp_f32 v40, v40\n" \
"v_cvt_pk_bf16_f32 v22, v28, v29\n" \
"v_mfma_f32_16x16x32_bf16 v[120:123], a[236:239], v[184:187], v[120:123]\n" \
"buffer_load_dwordx4 v234, s[40:43], s71 offen offset:2048 sc1 lds\n" \
"v_exp_f32 v41, v41\n" \
"v_mfma_f32_16x16x32_bf16 v[72:75], a[236:239], a[140:143], v[216:219]\n" \
"ds_read_b128 a[184:187], v239 offset:6144\n" \
"v_cvt_pk_bf16_f32 v23, v30, v31\n" \
"v_mfma_f32_16x16x32_bf16 v[72:75], a[232:235], a[136:139], v[72:75]\n" \
"ds_read_b128 a[236:239], v233 offset:13312\n" \
"v_mfma_f32_16x16x32_bf16 v[72:75], a[228:231], a[132:135], v[72:75]\n" \
"v_exp_f32 v42, v42\n" \
"v_mfma_f32_16x16x32_bf16 v[72:75], a[224:227], a[128:131], v[72:75]\n" \
"buffer_load_dwordx4 v234, s[40:43], s71 offen offset:3072 sc1 lds\n" \
"v_exp_f32 v43, v43\n" \
"v_exp_f32 v44, v44\n" \
"v_mfma_f32_16x16x32_bf16 v[76:79], a[240:243], a[128:131], v[220:223]\n" \
"ds_read_b128 a[232:235], v233 offset:5120\n" \
"v_cvt_pk_bf16_f32 v36, v40, v41\n" \
"v_mfma_f32_16x16x32_bf16 v[76:79], a[244:247], a[132:135], v[76:79]\n" \
"ds_read_b128 a[188:191], v239 offset:7168\n" \
"v_cvt_pk_bf16_f32 v37, v42, v43\n" \
"v_mfma_f32_16x16x32_bf16 v[76:79], a[248:251], a[136:139], v[76:79]\n" \
"ds_read_b128 a[228:231], v233 offset:12288\n" \
"v_mfma_f32_16x16x32_bf16 v[76:79], a[252:255], a[140:143], v[76:79]\n" \
"v_exp_f32 v45, v45\n" \
"v_exp_f32 v46, v46\n" \
"v_mfma_f32_16x16x32_bf16 v[124:127], a[252:255], v[184:187], v[196:199]\n" \
"ds_read_b128 a[224:227], v233 offset:4096\n" \
"v_cvt_pk_bf16_f32 v38, v44, v45\n" \
"v_mfma_f32_16x16x32_bf16 v[124:127], a[248:251], v[176:179], v[124:127]\n" \
"v_exp_f32 v47, v47\n" \
"v_mfma_f32_16x16x32_bf16 v[124:127], a[244:247], v[168:171], v[124:127]\n" \
"v_exp_f32 v56, v56\n" \
"v_exp_f32 v57, v57\n" \
"v_mfma_f32_16x16x32_bf16 v[124:127], a[240:243], v[160:163], v[124:127]\n" \
"v_exp_f32 v58, v58\n" \
"v_cvt_pk_bf16_f32 v39, v46, v47\n" \
"v_mfma_f32_16x16x32_bf16 v[108:111], a[240:243], v[128:131], v[196:199]\n" \
"v_exp_f32 v59, v59\n" \
"v_mfma_f32_16x16x32_bf16 v[108:111], a[244:247], v[136:139], v[108:111]\n" \
"v_exp_f32 v60, v60\n" \
"v_mfma_f32_16x16x32_bf16 v[108:111], a[248:251], v[144:147], v[108:111]\n" \
"v_cvt_pk_bf16_f32 v52, v56, v57\n" \
"v_cvt_pk_bf16_f32 v53, v58, v59\n" \
"v_mfma_f32_16x16x32_bf16 v[108:111], a[252:255], v[152:155], v[108:111]\n" \
"v_exp_f32 v61, v61\n" \
"v_exp_f32 v62, v62\n" \
"v_exp_f32 v63, v63\n" \
"v_mfma_f32_16x16x32_bf16 v[92:95], a[252:255], a[156:159], v[196:199]\n" \
"v_add_u32 v232, s91, v237\n" \
"v_cvt_pk_bf16_f32 v54, v60, v61\n" \
"v_mfma_f32_16x16x32_bf16 v[92:95], a[248:251], a[152:155], v[92:95]\n" \
"v_cvt_pk_bf16_f32 v55, v62, v63\n" \
"v_mfma_f32_16x16x32_bf16 v[92:95], a[244:247], a[148:151], v[92:95]\n" \
"s_waitcnt vmcnt(8) lgkmcnt(0)\n" \
"s_barrier\n" \
"v_mfma_f32_16x16x32_bf16 v[92:95], a[240:243], a[144:147], v[92:95]\n" \
"buffer_store_dword v192, v193, s[56:59], s68 offen sc1\n" \
"v_mfma_f32_16x16x32_bf16 v[160:163], a[192:195], v[16:19], 0\n" \
"s_add_u32 m0, s74, s79\n" \
"ds_read_b128 a[240:243], v232 offset:8448\n" \
"v_xor_b32 v233, 0x4000, v233\n" \
"v_mfma_f32_16x16x32_bf16 v[160:163], a[196:199], v[20:23], v[160:163]\n" \
"buffer_load_dwordx4 v234, s[36:39], s73 offen offset:0 sc1 lds\n" \
"v_exp_f32 v80, v80\n" \
"v_mfma_f32_16x16x32_bf16 a[64:67], a[192:195], v[32:35], 0\n" \
"ds_read_b128 a[244:247], v232 offset:10496\n" \
"v_mfma_f32_16x16x32_bf16 a[64:67], a[196:199], v[36:39], a[64:67]\n" \
"ds_read_b128 a[0:3], v248 offset:0\n" \
"v_exp_f32 v81, v81\n" \
"v_mfma_f32_16x16x32_bf16 a[96:99], a[192:195], v[48:51], 0\n" \
"v_exp_f32 v82, v82\n" \
"v_mfma_f32_16x16x32_bf16 a[96:99], a[196:199], v[52:55], a[96:99]\n" \
"s_add_u32 s71, s73, 17408\n" \
"ds_read_b128 a[192:195], v232 offset:0\n" \
"v_cvt_pk_bf16_f32 v80, v80, v81\n" \
"v_mfma_f32_16x16x32_bf16 a[100:103], a[200:203], v[48:51], 0\n" \
"buffer_load_dwordx4 v234, s[36:39], s73 offen offset:1024 sc1 lds\n" \
"v_mfma_f32_16x16x32_bf16 a[100:103], a[204:207], v[52:55], a[100:103]\n" \
"ds_read_b128 a[196:199], v232 offset:2048\n" \
"v_exp_f32 v83, v83\n" \
"v_mfma_f32_16x16x32_bf16 a[68:71], a[200:203], v[32:35], 0\n" \
"ds_read_b128 a[4:7], v248 offset:1024\n" \
"v_mfma_f32_16x16x32_bf16 a[68:71], a[204:207], v[36:39], a[68:71]\n" \
"v_exp_f32 v84, v84\n" \
"v_exp_f32 v85, v85\n" \
"v_mfma_f32_16x16x32_bf16 v[164:167], a[200:203], v[16:19], 0\n" \
"ds_read_b128 a[8:11], v248 offset:2048\n" \
"v_cvt_pk_bf16_f32 v81, v82, v83\n" \
"v_mfma_f32_16x16x32_bf16 v[164:167], a[204:207], v[20:23], v[164:167]\n" \
"buffer_load_dwordx4 v234, s[36:39], s73 offen offset:2048 sc1 lds\n" \
"v_mfma_f32_16x16x32_bf16 v[168:171], a[208:211], v[16:19], 0\n" \
"ds_read_b128 a[200:203], v232 offset:4096\n" \
"v_mfma_f32_16x16x32_bf16 v[168:171], a[212:215], v[20:23], v[168:171]\n" \
"ds_read_b128 a[204:207], v232 offset:6144\n" \
"v_exp_f32 v86, v86\n" \
"v_mfma_f32_16x16x32_bf16 a[72:75], a[208:211], v[32:35], 0\n" \
"ds_read_b128 a[12:15], v248 offset:3072\n" \
"v_cvt_pk_bf16_f32 v82, v84, v85\n" \
"v_mfma_f32_16x16x32_bf16 a[72:75], a[212:215], v[36:39], a[72:75]\n" \
"v_exp_f32 v87, v87\n" \
"v_exp_f32 v96, v96\n" \
"v_mfma_f32_16x16x32_bf16 a[104:107], a[208:211], v[48:51], 0\n" \
"buffer_load_dwordx4 v234, s[36:39], s73 offen offset:3072 sc1 lds\n" \
"v_mfma_f32_16x16x32_bf16 a[104:107], a[212:215], v[52:55], a[104:107]\n" \
"ds_read_b128 a[208:211], v232 offset:256\n" \
"v_cvt_pk_bf16_f32 v83, v86, v87\n" \
"v_smfmac_f32_16x16x64_bf16 a[52:55], v[244:247], v[16:23], v243\n" \
"ds_read_b128 a[212:215], v232 offset:2304\n" \
"v_exp_f32 v97, v97\n" \
"v_mfma_f32_16x16x32_bf16 a[108:111], a[216:219], v[48:51], 0\n" \
"ds_read_b128 a[16:19], v248 offset:4096\n" \
"v_mfma_f32_16x16x32_bf16 a[108:111], a[220:223], v[52:55], a[108:111]\n" \
"s_add_u32 m0, m0, 0x4000\n" \
"v_exp_f32 v98, v98\n" \
"v_mfma_f32_16x16x32_bf16 a[76:79], a[216:219], v[32:35], 0\n" \
"buffer_load_dwordx4 v234, s[36:39], s71 offen offset:0 sc1 lds\n" \
"v_mfma_f32_16x16x32_bf16 a[76:79], a[220:223], v[36:39], a[76:79]\n" \
"ds_read_b128 a[20:23], v248 offset:5120\n" \
"v_cvt_pk_bf16_f32 v96, v96, v97\n" \
"v_exp_f32 v99, v99\n" \
"v_mfma_f32_16x16x32_bf16 v[172:175], a[216:219], v[16:19], 0\n" \
"v_exp_f32 v100, v100\n" \
"v_mfma_f32_16x16x32_bf16 v[172:175], a[220:223], v[20:23], v[172:175]\n" \
"ds_read_b128 a[216:219], v232 offset:4352\n" \
"v_cvt_pk_bf16_f32 v97, v98, v99\n" \
"v_mfma_f32_32x32x16_bf16 v[128:143], a[48:51], a[48:51], 0\n" \
"ds_read_b128 a[220:223], v232 offset:6400\n" \
"v_exp_f32 v101, v101\n" \
"v_mfma_f32_16x16x32_bf16 v[176:179], a[224:227], v[16:19], 0\n" \
"buffer_load_dwordx4 v234, s[36:39], s71 offen offset:1024 sc1 lds\n" \
"v_mfma_f32_16x16x32_bf16 v[176:179], a[228:231], v[20:23], v[176:179]\n" \
"ds_read_b128 a[24:27], v248 offset:6144\n" \
"v_exp_f32 v102, v102\n" \
"v_mfma_f32_16x16x32_bf16 a[80:83], a[224:227], v[32:35], 0\n" \
"v_exp_f32 v103, v103\n" \
"v_mfma_f32_16x16x32_bf16 a[80:83], a[228:231], v[36:39], a[80:83]\n" \
"ds_read_b128 a[28:31], v248 offset:7168\n" \
"v_cvt_pk_bf16_f32 v98, v100, v101\n" \
"v_exp_f32 v112, v112\n" \
"v_mfma_f32_16x16x32_bf16 a[112:115], a[224:227], v[48:51], 0\n" \
"v_exp_f32 v113, v113\n" \
"v_mfma_f32_16x16x32_bf16 a[112:115], a[228:231], v[52:55], a[112:115]\n" \
"buffer_load_dwordx4 v234, s[36:39], s71 offen offset:2048 sc1 lds\n" \
"v_smfmac_f32_16x16x64_bf16 v[224:227], v[244:247], v[32:39], v243\n" \
"ds_read_b128 a[224:227], v232 offset:8192\n" \
"v_cvt_pk_bf16_f32 v99, v102, v103\n" \
"v_mfma_f32_16x16x32_bf16 a[116:119], a[232:235], v[48:51], 0\n" \
"ds_read_b128 a[228:231], v232 offset:10240\n" \
"v_mfma_f32_16x16x32_bf16 a[116:119], a[236:239], v[52:55], a[116:119]\n" \
"v_exp_f32 v114, v114\n" \
"v_exp_f32 v115, v115\n" \
"v_mfma_f32_16x16x32_bf16 a[84:87], a[232:235], v[32:35], 0\n" \
"v_exp_f32 v116, v116\n" \
"v_mfma_f32_16x16x32_bf16 a[84:87], a[236:239], v[36:39], a[84:87]\n" \
"buffer_load_dwordx4 v234, s[36:39], s71 offen offset:3072 sc1 lds\n" \
"v_cvt_pk_bf16_f32 v112, v112, v113\n" \
"v_mfma_f32_16x16x32_bf16 v[180:183], a[232:235], v[16:19], 0\n" \
"v_exp_f32 v117, v117\n" \
"v_mfma_f32_16x16x32_bf16 v[180:183], a[236:239], v[20:23], v[180:183]\n" \
"s_mov_b32 s76, s73\n" \
"s_add_u32 s73, s73, 34816\n" \
"ds_read_b128 a[232:235], v232 offset:12288\n" \
"v_mfma_f32_32x32x16_bf16 v[144:159], a[48:51], a[48:51], 0\n" \
"s_cmp_eq_u32 s73, s83\n" \
"s_cselect_b32 s70, 34816, 0\n" \
"ds_read_b128 a[236:239], v232 offset:14336\n" \
"v_exp_f32 v118, v118\n" \
"v_mfma_f32_16x16x32_bf16 v[184:187], a[32:35], v[16:19], 0\n" \
"s_add_u32 s73, s73, s70\n" \
"s_xor_b32 s74, s74, 0x8000\n" \
"ds_read_b128 a[248:251], v232 offset:12544\n" \
"v_mfma_f32_16x16x32_bf16 v[184:187], a[36:39], v[20:23], v[184:187]\n" \
"s_xor_b32 s77, s77, 0x8000\n" \
"ds_read_b128 a[252:255], v232 offset:14592\n" \
"v_cvt_pk_bf16_f32 v113, v114, v115\n" \
"v_mfma_f32_16x16x32_bf16 a[88:91], a[32:35], v[32:35], 0\n" \
"v_exp_f32 v119, v119\n" \
"v_mfma_f32_16x16x32_bf16 a[88:91], a[36:39], v[36:39], a[88:91]\n" \
"v_exp_f32 v64, v64\n" \
"v_cvt_pk_bf16_f32 v114, v116, v117\n" \
"v_mfma_f32_16x16x32_bf16 a[120:123], a[32:35], v[48:51], 0\n" \
"v_exp_f32 v65, v65\n" \
"v_mfma_f32_16x16x32_bf16 a[120:123], a[36:39], v[52:55], a[120:123]\n" \
"v_exp_f32 v66, v66\n" \
"v_cvt_pk_bf16_f32 v115, v118, v119\n" \
"v_smfmac_f32_16x16x64_bf16 v[228:231], v[244:247], v[48:55], v243\n" \
"v_cvt_pk_bf16_f32 v64, v64, v65\n" \
"v_mfma_f32_16x16x32_bf16 a[124:127], a[40:43], v[48:51], 0\n" \
"v_exp_f32 v67, v67\n" \
"v_mfma_f32_16x16x32_bf16 a[124:127], a[44:47], v[52:55], a[124:127]\n" \
"v_exp_f32 v68, v68\n" \
"v_exp_f32 v69, v69\n" \
"v_mfma_f32_16x16x32_bf16 a[92:95], a[40:43], v[32:35], 0\n" \
"v_exp_f32 v70, v70\n" \
"v_mfma_f32_16x16x32_bf16 a[92:95], a[44:47], v[36:39], a[92:95]\n" \
"v_cvt_pk_bf16_f32 v65, v66, v67\n" \
"v_cvt_pk_bf16_f32 v66, v68, v69\n" \
"v_mfma_f32_16x16x32_bf16 v[188:191], a[40:43], v[16:19], 0\n" \
"s_cmp_eq_u32 s63, 0\n" \
"v_exp_f32_e64 v71, v71\n" \
"v_mfma_f32_16x16x32_bf16 v[188:191], a[44:47], v[20:23], v[188:191]\n" \
"s_waitcnt lgkmcnt(0)\n" \
"v_cvt_pk_bf16_f32 v67, v70, v71\n" \
"s_cbranch_scc1 Lloop2_%=\n" \
"Lloop_%=:\n" \
"v_mfma_f32_16x16x32_bf16 v[0:3], a[192:195], a[128:131], v[196:199]\n" \
"v_exp_f32 v72, v72\n" \
"v_mfma_f32_16x16x32_bf16 v[0:3], a[196:199], a[132:135], v[0:3]\n" \
"v_mfma_f32_16x16x32_bf16 v[0:3], a[200:203], a[136:139], v[0:3]\n" \
"v_exp_f32 v73, v73\n" \
"v_mfma_f32_16x16x32_bf16 v[0:3], a[204:207], a[140:143], v[0:3]\n" \
"ds_read_b128 a[32:35], v233 offset:6144\n" \
"v_cvt_pk_bf16_f32 v68, v72, v73\n" \
"v_mfma_f32_16x16x32_bf16 v[16:19], a[204:207], a[156:159], v[196:199]\n" \
"v_exp_f32 v74, v74\n" \
"v_mfma_f32_16x16x32_bf16 v[16:19], a[200:203], a[152:155], v[16:19]\n" \
"global_load_dword v253, v252, s[44:45]\n" \
"v_mfma_f32_16x16x32_bf16 v[16:19], a[196:199], a[148:151], v[16:19]\n" \
"s_sub_u32 s46, s46, 1\n" \
"v_exp_f32 v75, v75\n" \
"v_mfma_f32_16x16x32_bf16 v[16:19], a[192:195], a[144:147], v[16:19]\n" \
"ds_read_b128 a[36:39], v233 offset:14336\n" \
"v_cvt_pk_bf16_f32 v69, v74, v75\n" \
"v_mfma_f32_16x16x32_bf16 v[32:35], a[192:195], a[160:163], v[196:199]\n" \
"v_exp_f32 v76, v76\n" \
"v_mfma_f32_16x16x32_bf16 v[32:35], a[196:199], a[164:167], v[32:35]\n" \
"s_cmp_gt_i32 s46, 0\n" \
"s_cselect_b32 s47, 0x100000, 0\n" \
"v_mfma_f32_16x16x32_bf16 v[32:35], a[200:203], a[168:171], v[32:35]\n" \
"v_exp_f32 v77, v77\n" \
"v_mfma_f32_16x16x32_bf16 v[32:35], a[204:207], a[172:175], v[32:35]\n" \
"s_add_u32 s44, s44, s47\n" \
"s_addc_u32 s45, s45, 0\n" \
"ds_read_b128 a[40:43], v233 offset:7168\n" \
"v_cvt_pk_bf16_f32 v70, v76, v77\n" \
"v_mfma_f32_16x16x32_bf16 v[48:51], a[204:207], a[188:191], v[196:199]\n" \
"v_exp_f32 v78, v78\n" \
"v_mfma_f32_16x16x32_bf16 v[48:51], a[200:203], a[184:187], v[48:51]\n" \
"v_xor_b32 v232, 0x4000, v232\n" \
"v_mfma_f32_16x16x32_bf16 v[48:51], a[196:199], a[180:183], v[48:51]\n" \
"v_exp_f32 v79, v79\n" \
"v_mfma_f32_16x16x32_bf16 v[48:51], a[192:195], a[176:179], v[48:51]\n" \
"ds_read_b128 a[204:207], v233 offset:9216\n" \
"v_cvt_pk_bf16_f32 v71, v78, v79\n" \
"v_mfma_f32_16x16x32_bf16 v[52:55], a[208:211], a[176:179], v[196:199]\n" \
"v_exp_f32 v88, v88\n" \
"v_mfma_f32_16x16x32_bf16 v[52:55], a[212:215], a[180:183], v[52:55]\n" \
"v_mfma_f32_16x16x32_bf16 v[52:55], a[216:219], a[184:187], v[52:55]\n" \
"v_exp_f32 v89, v89\n" \
"v_mfma_f32_16x16x32_bf16 v[52:55], a[220:223], a[188:191], v[52:55]\n" \
"ds_read_b128 a[200:203], v233 offset:1024\n" \
"v_cvt_pk_bf16_f32 v84, v88, v89\n" \
"v_mfma_f32_16x16x32_bf16 v[36:39], a[220:223], a[172:175], v[196:199]\n" \
"v_exp_f32 v90, v90\n" \
"v_mfma_f32_16x16x32_bf16 v[36:39], a[216:219], a[168:171], v[36:39]\n" \
"v_mfma_f32_16x16x32_bf16 v[36:39], a[212:215], a[164:167], v[36:39]\n" \
"v_exp_f32 v91, v91\n" \
"v_mfma_f32_16x16x32_bf16 v[36:39], a[208:211], a[160:163], v[36:39]\n" \
"ds_read_b128 a[196:199], v233 offset:8192\n" \
"v_cvt_pk_bf16_f32 v85, v90, v91\n" \
"v_mfma_f32_16x16x32_bf16 v[20:23], a[208:211], a[144:147], v[196:199]\n" \
"v_exp_f32 v92, v92\n" \
"v_mfma_f32_16x16x32_bf16 v[20:23], a[212:215], a[148:151], v[20:23]\n" \
"v_mfma_f32_16x16x32_bf16 v[20:23], a[216:219], a[152:155], v[20:23]\n" \
"v_exp_f32 v93, v93\n" \
"v_mfma_f32_16x16x32_bf16 v[20:23], a[220:223], a[156:159], v[20:23]\n" \
"ds_read_b128 a[192:195], v233 offset:0\n" \
"v_cvt_pk_bf16_f32 v86, v92, v93\n" \
"v_mfma_f32_16x16x32_bf16 v[4:7], a[220:223], a[140:143], v[196:199]\n" \
"v_exp_f32 v94, v94\n" \
"v_mfma_f32_16x16x32_bf16 v[4:7], a[216:219], a[136:139], v[4:7]\n" \
"v_mfma_f32_16x16x32_bf16 v[4:7], a[212:215], a[132:135], v[4:7]\n" \
"s_waitcnt lgkmcnt(7)\n" \
"v_exp_f32 v95, v95\n" \
"v_mfma_f32_16x16x32_bf16 v[4:7], a[208:211], a[128:131], v[4:7]\n" \
"ds_read_b128 a[220:223], v233 offset:11264\n" \
"v_cvt_pk_bf16_f32 v87, v94, v95\n" \
"v_mfma_f32_16x16x32_bf16 v[8:11], a[224:227], a[128:131], v[196:199]\n" \
"v_exp_f32 v104, v104\n" \
"v_mfma_f32_16x16x32_bf16 v[8:11], a[228:231], a[132:135], v[8:11]\n" \
"v_mfma_f32_16x16x32_bf16 v[8:11], a[232:235], a[136:139], v[8:11]\n" \
"v_exp_f32 v105, v105\n" \
"v_mfma_f32_16x16x32_bf16 v[8:11], a[236:239], a[140:143], v[8:11]\n" \
"ds_read_b128 a[216:219], v233 offset:3072\n" \
"v_cvt_pk_bf16_f32 v100, v104, v105\n" \
"v_mfma_f32_16x16x32_bf16 v[24:27], a[236:239], a[156:159], v[196:199]\n" \
"v_exp_f32 v106, v106\n" \
"v_mfma_f32_16x16x32_bf16 v[24:27], a[232:235], a[152:155], v[24:27]\n" \
"v_mfma_f32_16x16x32_bf16 v[24:27], a[228:231], a[148:151], v[24:27]\n" \
"v_exp_f32 v107, v107\n" \
"v_mfma_f32_16x16x32_bf16 v[24:27], a[224:227], a[144:147], v[24:27]\n" \
"ds_read_b128 a[212:215], v233 offset:10240\n" \
"v_cvt_pk_bf16_f32 v101, v106, v107\n" \
"v_mfma_f32_16x16x32_bf16 v[40:43], a[224:227], a[160:163], v[196:199]\n" \
"v_exp_f32 v108, v108\n" \
"v_mfma_f32_16x16x32_bf16 v[40:43], a[228:231], a[164:167], v[40:43]\n" \
"v_mfma_f32_16x16x32_bf16 v[40:43], a[232:235], a[168:171], v[40:43]\n" \
"v_exp_f32 v109, v109\n" \
"v_mfma_f32_16x16x32_bf16 v[40:43], a[236:239], a[172:175], v[40:43]\n" \
"ds_read_b128 a[208:211], v233 offset:2048\n" \
"v_cvt_pk_bf16_f32 v102, v108, v109\n" \
"v_mfma_f32_16x16x32_bf16 v[56:59], a[236:239], a[188:191], v[196:199]\n" \
"v_exp_f32 v110, v110\n" \
"v_mfma_f32_16x16x32_bf16 v[56:59], a[232:235], a[184:187], v[56:59]\n" \
"v_mfma_f32_16x16x32_bf16 v[56:59], a[228:231], a[180:183], v[56:59]\n" \
"v_exp_f32 v111, v111\n" \
"v_mfma_f32_16x16x32_bf16 v[56:59], a[224:227], a[176:179], v[56:59]\n" \
"ds_read_b128 a[236:239], v233 offset:13312\n" \
"v_cvt_pk_bf16_f32 v103, v110, v111\n" \
"v_mfma_f32_16x16x32_bf16 v[60:63], a[240:243], a[176:179], v[196:199]\n" \
"v_exp_f32 v120, v120\n" \
"v_mfma_f32_16x16x32_bf16 v[60:63], a[244:247], a[180:183], v[60:63]\n" \
"v_mfma_f32_16x16x32_bf16 v[60:63], a[248:251], a[184:187], v[60:63]\n" \
"v_exp_f32 v121, v121\n" \
"v_mfma_f32_16x16x32_bf16 v[60:63], a[252:255], a[188:191], v[60:63]\n" \
"ds_read_b128 a[232:235], v233 offset:5120\n" \
"v_cvt_pk_bf16_f32 v116, v120, v121\n" \
"v_mfma_f32_16x16x32_bf16 v[44:47], a[252:255], a[172:175], v[196:199]\n" \
"v_exp_f32 v122, v122\n" \
"v_mfma_f32_16x16x32_bf16 v[44:47], a[248:251], a[168:171], v[44:47]\n" \
"v_mfma_f32_16x16x32_bf16 v[44:47], a[244:247], a[164:167], v[44:47]\n" \
"v_exp_f32 v123, v123\n" \
"v_mfma_f32_16x16x32_bf16 v[44:47], a[240:243], a[160:163], v[44:47]\n" \
"ds_read_b128 a[228:231], v233 offset:12288\n" \
"v_cvt_pk_bf16_f32 v117, v122, v123\n" \
"v_mfma_f32_16x16x32_bf16 v[28:31], a[240:243], a[144:147], v[196:199]\n" \
"v_exp_f32 v124, v124\n" \
"v_mfma_f32_16x16x32_bf16 v[28:31], a[244:247], a[148:151], v[28:31]\n" \
"v_mfma_f32_16x16x32_bf16 v[28:31], a[248:251], a[152:155], v[28:31]\n" \
"v_exp_f32 v125, v125\n" \
"v_mfma_f32_16x16x32_bf16 v[28:31], a[252:255], a[156:159], v[28:31]\n" \
"Lpg5605_%=:\n" \
"s_cbranch_execz Lpg6801_%=\n" \
"ds_read_b128 a[224:227], v233 offset:4096\n" \
"v_cvt_pk_bf16_f32 v118, v124, v125\n" \
"v_mfma_f32_16x16x32_bf16 v[12:15], a[252:255], a[140:143], v[196:199]\n" \
"v_exp_f32 v126, v126\n" \
"v_mfma_f32_16x16x32_bf16 v[12:15], a[248:251], a[136:139], v[12:15]\n" \
"v_mfma_f32_16x16x32_bf16 v[12:15], a[244:247], a[132:135], v[12:15]\n" \
"s_waitcnt lgkmcnt(2)\n" \
"v_exp_f32 v127, v127\n" \
"v_mfma_f32_16x16x32_bf16 v[12:15], a[240:243], a[128:131], v[12:15]\n" \
"ds_read_b128 a[44:47], v233 offset:15360\n" \
"v_cvt_pk_bf16_f32 v119, v126, v127\n" \
"v_mfma_f32_16x16x32_bf16 v[128:131], a[192:195], v[64:67], v[128:131]\n" \
"v_exp_f32 v0, v0\n" \
"v_mfma_f32_16x16x32_bf16 v[128:131], a[196:199], v[68:71], v[128:131]\n" \
"ds_read_b128 a[244:247], v232 offset:10496\n" \
"v_mfma_f32_16x16x32_bf16 v[160:163], a[192:195], v[80:83], v[160:163]\n" \
"v_exp_f32 v1, v1\n" \
"v_mfma_f32_16x16x32_bf16 v[160:163], a[196:199], v[84:87], v[160:163]\n" \
"ds_read_b128 a[240:243], v232 offset:8448\n" \
"v_cvt_pk_bf16_f32 v0, v0, v1\n" \
"v_mfma_f32_16x16x32_bf16 a[64:67], a[192:195], v[96:99], a[64:67]\n" \
"v_exp_f32 v2, v2\n" \
"v_mfma_f32_16x16x32_bf16 a[64:67], a[196:199], v[100:103], a[64:67]\n" \
"ds_read_b128 a[248:251], v232 offset:12544\n" \
"v_xor_b32 v233, 0xc000, v233\n" \
"v_mfma_f32_16x16x32_bf16 a[96:99], a[192:195], v[112:115], a[96:99]\n" \
"v_exp_f32 v3, v3\n" \
"v_mfma_f32_16x16x32_bf16 a[96:99], a[196:199], v[116:119], a[96:99]\n" \
"ds_read_b128 a[192:195], v232 offset:0\n" \
"v_cvt_pk_bf16_f32 v1, v2, v3\n" \
"v_mfma_f32_16x16x32_bf16 a[100:103], a[200:203], v[112:115], a[100:103]\n" \
"v_exp_f32 v4, v4\n" \
"v_mfma_f32_16x16x32_bf16 a[100:103], a[204:207], v[116:119], a[100:103]\n" \
"ds_read_b128 a[252:255], v232 offset:14592\n" \
"v_mfma_f32_16x16x32_bf16 a[68:71], a[200:203], v[96:99], a[68:71]\n" \
"v_exp_f32 v5, v5\n" \
"v_mfma_f32_16x16x32_bf16 a[68:71], a[204:207], v[100:103], a[68:71]\n" \
"ds_read_b128 a[196:199], v232 offset:2048\n" \
"v_cvt_pk_bf16_f32 v2, v4, v5\n" \
"v_smfmac_f32_16x16x64_bf16 a[48:51], v[244:247], v[64:71], v243\n" \
"v_mfma_f32_16x16x32_bf16 v[164:167], a[200:203], v[80:83], v[164:167]\n" \
"v_exp_f32 v6, v6\n" \
"v_mfma_f32_16x16x32_bf16 v[164:167], a[204:207], v[84:87], v[164:167]\n" \
"v_mfma_f32_16x16x32_bf16 v[132:135], a[200:203], v[64:67], v[132:135]\n" \
"v_exp_f32 v7, v7\n" \
"v_mfma_f32_16x16x32_bf16 v[132:135], a[204:207], v[68:71], v[132:135]\n" \
"ds_read_b128 a[200:203], v232 offset:4096\n" \
"v_cvt_pk_bf16_f32 v3, v6, v7\n" \
"v_mfma_f32_16x16x32_bf16 v[136:139], a[208:211], v[64:67], v[136:139]\n" \
"v_exp_f32 v16, v16\n" \
"v_mfma_f32_16x16x32_bf16 v[136:139], a[212:215], v[68:71], v[136:139]\n" \
"v_mfma_f32_16x16x32_bf16 v[168:171], a[208:211], v[80:83], v[168:171]\n" \
"v_exp_f32 v17, v17\n" \
"v_mfma_f32_16x16x32_bf16 v[168:171], a[212:215], v[84:87], v[168:171]\n" \
"ds_read_b128 a[204:207], v232 offset:6144\n" \
"v_cvt_pk_bf16_f32 v16, v16, v17\n" \
"v_mfma_f32_16x16x32_bf16 a[72:75], a[208:211], v[96:99], a[72:75]\n" \
"v_exp_f32 v18, v18\n" \
"v_mfma_f32_16x16x32_bf16 a[72:75], a[212:215], v[100:103], a[72:75]\n" \
"v_mfma_f32_16x16x32_bf16 a[104:107], a[208:211], v[112:115], a[104:107]\n" \
"v_exp_f32 v19, v19\n" \
"v_mfma_f32_16x16x32_bf16 a[104:107], a[212:215], v[116:119], a[104:107]\n" \
"ds_read_b128 a[208:211], v232 offset:256\n" \
"v_cvt_pk_bf16_f32 v17, v18, v19\n" \
"v_mfma_f32_16x16x32_bf16 a[108:111], a[216:219], v[112:115], a[108:111]\n" \
"v_exp_f32 v20, v20\n" \
"v_mfma_f32_16x16x32_bf16 a[108:111], a[220:223], v[116:119], a[108:111]\n" \
"v_mfma_f32_16x16x32_bf16 a[76:79], a[216:219], v[96:99], a[76:79]\n" \
"v_exp_f32 v21, v21\n" \
"v_mfma_f32_16x16x32_bf16 a[76:79], a[220:223], v[100:103], a[76:79]\n" \
"ds_read_b128 a[212:215], v232 offset:2304\n" \
"v_cvt_pk_bf16_f32 v18, v20, v21\n" \
"v_smfmac_f32_16x16x64_bf16 a[52:55], v[244:247], v[80:87], v243\n" \
"v_mfma_f32_16x16x32_bf16 v[172:175], a[216:219], v[80:83], v[172:175]\n" \
"v_exp_f32 v22, v22\n" \
"v_mfma_f32_16x16x32_bf16 v[172:175], a[220:223], v[84:87], v[172:175]\n" \
"v_mfma_f32_16x16x32_bf16 v[140:143], a[216:219], v[64:67], v[140:143]\n" \
"s_waitcnt lgkmcnt(10)\n" \
"v_exp_f32 v23, v23\n" \
"v_mfma_f32_16x16x32_bf16 v[140:143], a[220:223], v[68:71], v[140:143]\n" \
"ds_read_b128 a[216:219], v232 offset:4352\n" \
"v_cvt_pk_bf16_f32 v19, v22, v23\n" \
"v_mfma_f32_16x16x32_bf16 v[144:147], a[224:227], v[64:67], v[144:147]\n" \
"v_exp_f32 v32, v32\n" \
"v_mfma_f32_16x16x32_bf16 v[144:147], a[228:231], v[68:71], v[144:147]\n" \
"v_mfma_f32_16x16x32_bf16 v[176:179], a[224:227], v[80:83], v[176:179]\n" \
"v_exp_f32 v33, v33\n" \
"v_mfma_f32_16x16x32_bf16 v[176:179], a[228:231], v[84:87], v[176:179]\n" \
"ds_read_b128 a[220:223], v232 offset:6400\n" \
"v_cvt_pk_bf16_f32 v32, v32, v33\n" \
"v_mfma_f32_16x16x32_bf16 a[80:83], a[224:227], v[96:99], a[80:83]\n" \
"v_exp_f32 v34, v34\n" \
"v_mfma_f32_16x16x32_bf16 a[80:83], a[228:231], v[100:103], a[80:83]\n" \
"v_mfma_f32_16x16x32_bf16 a[112:115], a[224:227], v[112:115], a[112:115]\n" \
"v_exp_f32 v35, v35\n" \
"v_mfma_f32_16x16x32_bf16 a[112:115], a[228:231], v[116:119], a[112:115]\n" \
"ds_read_b128 a[224:227], v232 offset:8192\n" \
"v_cvt_pk_bf16_f32 v33, v34, v35\n" \
"v_mfma_f32_16x16x32_bf16 a[116:119], a[232:235], v[112:115], a[116:119]\n" \
"v_exp_f32 v36, v36\n" \
"v_mfma_f32_16x16x32_bf16 a[116:119], a[236:239], v[116:119], a[116:119]\n" \
"v_mfma_f32_16x16x32_bf16 a[84:87], a[232:235], v[96:99], a[84:87]\n" \
"v_exp_f32 v37, v37\n" \
"v_mfma_f32_16x16x32_bf16 a[84:87], a[236:239], v[100:103], a[84:87]\n" \
"ds_read_b128 a[228:231], v232 offset:10240\n" \
"v_cvt_pk_bf16_f32 v34, v36, v37\n" \
"v_smfmac_f32_16x16x64_bf16 v[224:227], v[244:247], v[96:103], v243\n" \
"v_mfma_f32_16x16x32_bf16 v[180:183], a[232:235], v[80:83], v[180:183]\n" \
"v_exp_f32 v38, v38\n" \
"v_mfma_f32_16x16x32_bf16 v[180:183], a[236:239], v[84:87], v[180:183]\n" \
"v_mfma_f32_16x16x32_bf16 v[148:151], a[232:235], v[64:67], v[148:151]\n" \
"v_exp_f32 v39, v39\n" \
"v_mfma_f32_16x16x32_bf16 v[148:151], a[236:239], v[68:71], v[148:151]\n" \
"ds_read_b128 a[232:235], v232 offset:12288\n" \
"v_cvt_pk_bf16_f32 v35, v38, v39\n" \
"v_mfma_f32_16x16x32_bf16 v[152:155], a[32:35], v[64:67], v[152:155]\n" \
"v_exp_f32 v48, v48\n" \
"v_mfma_f32_16x16x32_bf16 v[152:155], a[36:39], v[68:71], v[152:155]\n" \
"v_mfma_f32_16x16x32_bf16 v[184:187], a[32:35], v[80:83], v[184:187]\n" \
"v_exp_f32 v49, v49\n" \
"v_mfma_f32_16x16x32_bf16 v[184:187], a[36:39], v[84:87], v[184:187]\n" \
"ds_read_b128 a[236:239], v232 offset:14336\n" \
"v_cvt_pk_bf16_f32 v48, v48, v49\n" \
"v_mfma_f32_16x16x32_bf16 a[88:91], a[32:35], v[96:99], a[88:91]\n" \
"v_exp_f32 v50, v50\n" \
"v_mfma_f32_16x16x32_bf16 a[88:91], a[36:39], v[100:103], a[88:91]\n" \
"v_mfma_f32_16x16x32_bf16 a[120:123], a[32:35], v[112:115], a[120:123]\n" \
"v_exp_f32 v51, v51\n" \
"v_mfma_f32_16x16x32_bf16 a[120:123], a[36:39], v[116:119], a[120:123]\n" \
"v_cvt_pk_bf16_f32 v49, v50, v51\n" \
"v_mfma_f32_16x16x32_bf16 a[124:127], a[40:43], v[112:115], a[124:127]\n" \
"v_exp_f32 v52, v52\n" \
"v_mfma_f32_16x16x32_bf16 a[124:127], a[44:47], v[116:119], a[124:127]\n" \
"v_mfma_f32_16x16x32_bf16 a[92:95], a[40:43], v[96:99], a[92:95]\n" \
"v_exp_f32 v53, v53\n" \
"v_mfma_f32_16x16x32_bf16 a[92:95], a[44:47], v[100:103], a[92:95]\n" \
"v_cvt_pk_bf16_f32 v50, v52, v53\n" \
"v_smfmac_f32_16x16x64_bf16 v[228:231], v[244:247], v[112:119], v243\n" \
"v_mfma_f32_16x16x32_bf16 v[188:191], a[40:43], v[80:83], v[188:191]\n" \
"v_exp_f32 v54, v54\n" \
"v_mfma_f32_16x16x32_bf16 v[188:191], a[44:47], v[84:87], v[188:191]\n" \
"v_mfma_f32_16x16x32_bf16 v[156:159], a[40:43], v[64:67], v[156:159]\n" \
"s_waitcnt vmcnt(1) lgkmcnt(0)\n" \
"v_exp_f32 v55, v55\n" \
"v_mfma_f32_16x16x32_bf16 v[156:159], a[44:47], v[68:71], v[156:159]\n" \
"s_barrier\n" \
"v_cvt_pk_bf16_f32 v51, v54, v55\n" \
"v_mfma_f32_16x16x32_bf16 v[64:67], a[192:195], a[128:131], v[196:199]\n" \
"v_exp_f32 v8, v8\n" \
"v_mfma_f32_16x16x32_bf16 v[64:67], a[196:199], a[132:135], v[64:67]\n" \
"v_cmp_eq_u32 vcc, s60, v239\n" \
"v_mfma_f32_16x16x32_bf16 v[64:67], a[200:203], a[136:139], v[64:67]\n" \
"v_exp_f32_e64 v9, v9\n" \
"v_mfma_f32_16x16x32_bf16 v[64:67], a[204:207], a[140:143], v[64:67]\n" \
"s_andn2_b32 s70, s69, vcc_lo\n" \
"s_cbranch_scc1 Lpl2_%=\n" \
"Lpr2_%=:\n" \
"ds_read_b128 a[32:35], v233 offset:6144\n" \
"v_cvt_pk_bf16_f32 v4, v8, v9\n" \
"v_mfma_f32_16x16x32_bf16 v[80:83], a[204:207], a[156:159], v[196:199]\n" \
"s_add_u32 m0, s77, s80\n" \
"v_exp_f32 v10, v10\n" \
"v_mfma_f32_16x16x32_bf16 v[80:83], a[200:203], a[152:155], v[80:83]\n" \
"buffer_load_dwordx4 v234, s[40:43], s76 offen offset:0 sc1 lds\n" \
"v_mfma_f32_16x16x32_bf16 v[80:83], a[196:199], a[148:151], v[80:83]\n" \
"s_sub_u32 s89, s89, 1\n" \
"v_exp_f32 v11, v11\n" \
"v_mfma_f32_16x16x32_bf16 v[80:83], a[192:195], a[144:147], v[80:83]\n" \
"s_cmp_eq_u32 s89, 0\n" \
"s_cselect_b32 s70, s78, 0xc000\n" \
"ds_read_b128 a[36:39], v233 offset:14336\n" \
"v_cvt_pk_bf16_f32 v5, v10, v11\n" \
"v_mfma_f32_16x16x32_bf16 v[96:99], a[192:195], a[160:163], v[196:199]\n" \
"s_add_u32 s71, s76, 17408\n" \
"v_exp_f32 v12, v12\n" \
"v_mfma_f32_16x16x32_bf16 v[96:99], a[196:199], a[164:167], v[96:99]\n" \
"buffer_load_dwordx4 v234, s[40:43], s76 offen offset:1024 sc1 lds\n" \
"v_mfma_f32_16x16x32_bf16 v[96:99], a[200:203], a[168:171], v[96:99]\n" \
"v_exp_f32 v13, v13\n" \
"v_mfma_f32_16x16x32_bf16 v[96:99], a[204:207], a[172:175], v[96:99]\n" \
"ds_read_b128 a[40:43], v233 offset:7168\n" \
"v_cvt_pk_bf16_f32 v6, v12, v13\n" \
"v_mfma_f32_16x16x32_bf16 v[112:115], a[204:207], a[188:191], v[196:199]\n" \
"v_exp_f32 v14, v14\n" \
"v_mfma_f32_16x16x32_bf16 v[112:115], a[200:203], a[184:187], v[112:115]\n" \
"v_xor_b32 v232, s70, v232\n" \
"v_mfma_f32_16x16x32_bf16 v[112:115], a[196:199], a[180:183], v[112:115]\n" \
"v_exp_f32 v15, v15\n" \
"v_mfma_f32_16x16x32_bf16 v[112:115], a[192:195], a[176:179], v[112:115]\n" \
"ds_read_b128 a[204:207], v233 offset:9216\n" \
"v_cvt_pk_bf16_f32 v7, v14, v15\n" \
"v_mfma_f32_16x16x32_bf16 v[116:119], a[208:211], a[176:179], v[196:199]\n" \
"v_exp_f32 v24, v24\n" \
"v_mfma_f32_16x16x32_bf16 v[116:119], a[212:215], a[180:183], v[116:119]\n" \
"buffer_load_dwordx4 v234, s[40:43], s76 offen offset:2048 sc1 lds\n" \
"v_mfma_f32_16x16x32_bf16 v[116:119], a[216:219], a[184:187], v[116:119]\n" \
"v_exp_f32 v25, v25\n" \
"v_mfma_f32_16x16x32_bf16 v[116:119], a[220:223], a[188:191], v[116:119]\n" \
"ds_read_b128 a[200:203], v233 offset:1024\n" \
"v_cvt_pk_bf16_f32 v20, v24, v25\n" \
"v_mfma_f32_16x16x32_bf16 v[100:103], a[220:223], a[172:175], v[196:199]\n" \
"v_exp_f32 v26, v26\n" \
"v_mfma_f32_16x16x32_bf16 v[100:103], a[216:219], a[168:171], v[100:103]\n" \
"buffer_load_dwordx4 v234, s[40:43], s76 offen offset:3072 sc1 lds\n" \
"v_mfma_f32_16x16x32_bf16 v[100:103], a[212:215], a[164:167], v[100:103]\n" \
"v_exp_f32 v27, v27\n" \
"v_mfma_f32_16x16x32_bf16 v[100:103], a[208:211], a[160:163], v[100:103]\n" \
"ds_read_b128 a[196:199], v233 offset:8192\n" \
"v_cvt_pk_bf16_f32 v21, v26, v27\n" \
"v_mfma_f32_16x16x32_bf16 v[84:87], a[208:211], a[144:147], v[196:199]\n" \
"v_exp_f32 v28, v28\n" \
"v_mfma_f32_16x16x32_bf16 v[84:87], a[212:215], a[148:151], v[84:87]\n" \
"v_mfma_f32_16x16x32_bf16 v[84:87], a[216:219], a[152:155], v[84:87]\n" \
"v_exp_f32 v29, v29\n" \
"v_mfma_f32_16x16x32_bf16 v[84:87], a[220:223], a[156:159], v[84:87]\n" \
"ds_read_b128 a[192:195], v233 offset:0\n" \
"v_cvt_pk_bf16_f32 v22, v28, v29\n" \
"v_mfma_f32_16x16x32_bf16 v[68:71], a[220:223], a[140:143], v[196:199]\n" \
"s_add_u32 m0, m0, 0x4000\n" \
"v_exp_f32 v30, v30\n" \
"v_mfma_f32_16x16x32_bf16 v[68:71], a[216:219], a[136:139], v[68:71]\n" \
"buffer_load_dwordx4 v234, s[40:43], s71 offen offset:0 sc1 lds\n" \
"v_mfma_f32_16x16x32_bf16 v[68:71], a[212:215], a[132:135], v[68:71]\n" \
"v_exp_f32 v31, v31\n" \
"v_mfma_f32_16x16x32_bf16 v[68:71], a[208:211], a[128:131], v[68:71]\n" \
"ds_read_b128 a[220:223], v233 offset:11264\n" \
"v_cvt_pk_bf16_f32 v23, v30, v31\n" \
"v_mfma_f32_16x16x32_bf16 v[72:75], a[224:227], a[128:131], v[196:199]\n" \
"v_exp_f32 v40, v40\n" \
"v_mfma_f32_16x16x32_bf16 v[72:75], a[228:231], a[132:135], v[72:75]\n" \
"buffer_load_dwordx4 v234, s[40:43], s71 offen offset:1024 sc1 lds\n" \
"v_mfma_f32_16x16x32_bf16 v[72:75], a[232:235], a[136:139], v[72:75]\n" \
"v_exp_f32 v41, v41\n" \
"v_mfma_f32_16x16x32_bf16 v[72:75], a[236:239], a[140:143], v[72:75]\n" \
"ds_read_b128 a[216:219], v233 offset:3072\n" \
"v_cvt_pk_bf16_f32 v36, v40, v41\n" \
"v_mfma_f32_16x16x32_bf16 v[88:91], a[236:239], a[156:159], v[196:199]\n" \
"v_exp_f32 v42, v42\n" \
"v_mfma_f32_16x16x32_bf16 v[88:91], a[232:235], a[152:155], v[88:91]\n" \
"v_mfma_f32_16x16x32_bf16 v[88:91], a[228:231], a[148:151], v[88:91]\n" \
"v_exp_f32 v43, v43\n" \
"v_mfma_f32_16x16x32_bf16 v[88:91], a[224:227], a[144:147], v[88:91]\n" \
"ds_read_b128 a[212:215], v233 offset:10240\n" \
"v_cvt_pk_bf16_f32 v37, v42, v43\n" \
"v_mfma_f32_16x16x32_bf16 v[104:107], a[224:227], a[160:163], v[196:199]\n" \
"v_exp_f32 v44, v44\n" \
"v_mfma_f32_16x16x32_bf16 v[104:107], a[228:231], a[164:167], v[104:107]\n" \
"buffer_load_dwordx4 v234, s[40:43], s71 offen offset:2048 sc1 lds\n" \
"v_mfma_f32_16x16x32_bf16 v[104:107], a[232:235], a[168:171], v[104:107]\n" \
"v_exp_f32 v45, v45\n" \
"v_mfma_f32_16x16x32_bf16 v[104:107], a[236:239], a[172:175], v[104:107]\n" \
"ds_read_b128 a[208:211], v233 offset:2048\n" \
"v_cvt_pk_bf16_f32 v38, v44, v45\n" \
"v_mfma_f32_16x16x32_bf16 v[120:123], a[236:239], a[188:191], v[196:199]\n" \
"v_exp_f32 v46, v46\n" \
"v_mfma_f32_16x16x32_bf16 v[120:123], a[232:235], a[184:187], v[120:123]\n" \
"buffer_load_dwordx4 v234, s[40:43], s71 offen offset:3072 sc1 lds\n" \
"v_mfma_f32_16x16x32_bf16 v[120:123], a[228:231], a[180:183], v[120:123]\n" \
"v_exp_f32 v47, v47\n" \
"v_mfma_f32_16x16x32_bf16 v[120:123], a[224:227], a[176:179], v[120:123]\n" \
"ds_read_b128 a[236:239], v233 offset:13312\n" \
"v_cvt_pk_bf16_f32 v39, v46, v47\n" \
"v_mfma_f32_16x16x32_bf16 v[124:127], a[240:243], a[176:179], v[196:199]\n" \
"v_exp_f32 v56, v56\n" \
"v_mfma_f32_16x16x32_bf16 v[124:127], a[244:247], a[180:183], v[124:127]\n" \
"v_mfma_f32_16x16x32_bf16 v[124:127], a[248:251], a[184:187], v[124:127]\n" \
"v_exp_f32 v57, v57\n" \
"v_mfma_f32_16x16x32_bf16 v[124:127], a[252:255], a[188:191], v[124:127]\n" \
"ds_read_b128 a[232:235], v233 offset:5120\n" \
"v_cvt_pk_bf16_f32 v52, v56, v57\n" \
"v_mfma_f32_16x16x32_bf16 v[108:111], a[252:255], a[172:175], v[196:199]\n" \
"v_exp_f32 v58, v58\n" \
"v_mfma_f32_16x16x32_bf16 v[108:111], a[248:251], a[168:171], v[108:111]\n" \
"v_mfma_f32_16x16x32_bf16 v[108:111], a[244:247], a[164:167], v[108:111]\n" \
"v_exp_f32 v59, v59\n" \
"v_mfma_f32_16x16x32_bf16 v[108:111], a[240:243], a[160:163], v[108:111]\n" \
"ds_read_b128 a[228:231], v233 offset:12288\n" \
"v_cvt_pk_bf16_f32 v53, v58, v59\n" \
"v_mfma_f32_16x16x32_bf16 v[92:95], a[240:243], a[144:147], v[196:199]\n" \
"v_exp_f32 v60, v60\n" \
"v_mfma_f32_16x16x32_bf16 v[92:95], a[244:247], a[148:151], v[92:95]\n" \
"v_mfma_f32_16x16x32_bf16 v[92:95], a[248:251], a[152:155], v[92:95]\n" \
"v_exp_f32 v61, v61\n" \
"v_mfma_f32_16x16x32_bf16 v[92:95], a[252:255], a[156:159], v[92:95]\n" \
"ds_read_b128 a[224:227], v233 offset:4096\n" \
"v_cvt_pk_bf16_f32 v54, v60, v61\n" \
"v_mfma_f32_16x16x32_bf16 v[76:79], a[252:255], a[140:143], v[196:199]\n" \
"v_exp_f32 v62, v62\n" \
"v_mfma_f32_16x16x32_bf16 v[76:79], a[248:251], a[136:139], v[76:79]\n" \
"v_mfma_f32_16x16x32_bf16 v[76:79], a[244:247], a[132:135], v[76:79]\n" \
"s_waitcnt lgkmcnt(2)\n" \
"v_exp_f32 v63, v63\n" \
"v_mfma_f32_16x16x32_bf16 v[76:79], a[240:243], a[128:131], v[76:79]\n" \
"ds_read_b128 a[44:47], v233 offset:15360\n" \
"v_cvt_pk_bf16_f32 v55, v62, v63\n" \
"v_mfma_f32_16x16x32_bf16 v[128:131], a[192:195], v[0:3], v[128:131]\n" \
"s_add_u32 m0, s74, s79\n" \
"v_exp_f32 v64, v64\n" \
"v_mfma_f32_16x16x32_bf16 v[128:131], a[196:199], v[4:7], v[128:131]\n" \
"buffer_load_dwordx4 v234, s[36:39], s73 offen offset:0 sc1 lds\n" \
"v_mfma_f32_16x16x32_bf16 v[160:163], a[192:195], v[16:19], v[160:163]\n" \
"v_exp_f32 v65, v65\n" \
"v_mfma_f32_16x16x32_bf16 v[160:163], a[196:199], v[20:23], v[160:163]\n" \
"ds_read_b128 a[240:243], v232 offset:8448\n" \
"v_cvt_pk_bf16_f32 v64, v64, v65\n" \
"v_mfma_f32_16x16x32_bf16 a[64:67], a[192:195], v[32:35], a[64:67]\n" \
"s_add_u32 s71, s73, 17408\n" \
"v_exp_f32 v66, v66\n" \
"v_mfma_f32_16x16x32_bf16 a[64:67], a[196:199], v[36:39], a[64:67]\n" \
"buffer_load_dwordx4 v234, s[36:39], s73 offen offset:1024 sc1 lds\n" \
"v_mfma_f32_16x16x32_bf16 a[96:99], a[192:195], v[48:51], a[96:99]\n" \
"v_exp_f32 v67, v67\n" \
"v_mfma_f32_16x16x32_bf16 a[96:99], a[196:199], v[52:55], a[96:99]\n" \
"ds_read_b128 a[192:195], v232 offset:0\n" \
"v_cvt_pk_bf16_f32 v65, v66, v67\n" \
"v_mfma_f32_16x16x32_bf16 a[100:103], a[200:203], v[48:51], a[100:103]\n" \
"v_exp_f32 v68, v68\n" \
"v_mfma_f32_16x16x32_bf16 a[100:103], a[204:207], v[52:55], a[100:103]\n" \
"v_xor_b32 v233, 0x4000, v233\n" \
"v_mfma_f32_16x16x32_bf16 a[68:71], a[200:203], v[32:35], a[68:71]\n" \
"v_exp_f32 v69, v69\n" \
"v_mfma_f32_16x16x32_bf16 a[68:71], a[204:207], v[36:39], a[68:71]\n" \
"ds_read_b128 a[196:199], v232 offset:2048\n" \
"v_cvt_pk_bf16_f32 v66, v68, v69\n" \
"v_smfmac_f32_16x16x64_bf16 a[48:51], v[244:247], v[0:7], v243\n" \
"buffer_load_dwordx4 v234, s[36:39], s73 offen offset:2048 sc1 lds\n" \
"v_mfma_f32_16x16x32_bf16 v[164:167], a[200:203], v[16:19], v[164:167]\n" \
"v_exp_f32 v70, v70\n" \
"v_mfma_f32_16x16x32_bf16 v[164:167], a[204:207], v[20:23], v[164:167]\n" \
"v_mfma_f32_16x16x32_bf16 v[132:135], a[200:203], v[0:3], v[132:135]\n" \
"v_exp_f32 v71, v71\n" \
"v_mfma_f32_16x16x32_bf16 v[132:135], a[204:207], v[4:7], v[132:135]\n" \
"ds_read_b128 a[200:203], v232 offset:4096\n" \
"v_cvt_pk_bf16_f32 v67, v70, v71\n" \
"v_mfma_f32_16x16x32_bf16 v[136:139], a[208:211], v[0:3], v[136:139]\n" \
"v_exp_f32 v80, v80\n" \
"v_mfma_f32_16x16x32_bf16 v[136:139], a[212:215], v[4:7], v[136:139]\n" \
"buffer_load_dwordx4 v234, s[36:39], s73 offen offset:3072 sc1 lds\n" \
"v_mfma_f32_16x16x32_bf16 v[168:171], a[208:211], v[16:19], v[168:171]\n" \
"v_exp_f32 v81, v81\n" \
"v_mfma_f32_16x16x32_bf16 v[168:171], a[212:215], v[20:23], v[168:171]\n" \
"ds_read_b128 a[204:207], v232 offset:6144\n" \
"v_cvt_pk_bf16_f32 v80, v80, v81\n" \
"v_mfma_f32_16x16x32_bf16 a[72:75], a[208:211], v[32:35], a[72:75]\n" \
"v_exp_f32 v82, v82\n" \
"v_mfma_f32_16x16x32_bf16 a[72:75], a[212:215], v[36:39], a[72:75]\n" \
"v_mfma_f32_16x16x32_bf16 a[104:107], a[208:211], v[48:51], a[104:107]\n" \
"v_exp_f32 v83, v83\n" \
"v_mfma_f32_16x16x32_bf16 a[104:107], a[212:215], v[52:55], a[104:107]\n" \
"ds_read_b128 a[208:211], v232 offset:256\n" \
"v_cvt_pk_bf16_f32 v81, v82, v83\n" \
"v_mfma_f32_16x16x32_bf16 a[108:111], a[216:219], v[48:51], a[108:111]\n" \
"s_add_u32 m0, m0, 0x4000\n" \
"v_exp_f32 v84, v84\n" \
"v_mfma_f32_16x16x32_bf16 a[108:111], a[220:223], v[52:55], a[108:111]\n" \
"buffer_load_dwordx4 v234, s[36:39], s71 offen offset:0 sc1 lds\n" \
"v_mfma_f32_16x16x32_bf16 a[76:79], a[216:219], v[32:35], a[76:79]\n" \
"v_exp_f32 v85, v85\n" \
"v_mfma_f32_16x16x32_bf16 a[76:79], a[220:223], v[36:39], a[76:79]\n" \
"ds_read_b128 a[212:215], v232 offset:2304\n" \
"v_cvt_pk_bf16_f32 v82, v84, v85\n" \
"v_smfmac_f32_16x16x64_bf16 a[52:55], v[244:247], v[16:23], v243\n" \
"v_mfma_f32_16x16x32_bf16 v[172:175], a[216:219], v[16:19], v[172:175]\n" \
"v_exp_f32 v86, v86\n" \
"v_mfma_f32_16x16x32_bf16 v[172:175], a[220:223], v[20:23], v[172:175]\n" \
"buffer_load_dwordx4 v234, s[36:39], s71 offen offset:1024 sc1 lds\n" \
"v_mfma_f32_16x16x32_bf16 v[140:143], a[216:219], v[0:3], v[140:143]\n" \
"s_waitcnt lgkmcnt(7)\n" \
"v_exp_f32 v87, v87\n" \
"v_mfma_f32_16x16x32_bf16 v[140:143], a[220:223], v[4:7], v[140:143]\n" \
"ds_read_b128 a[216:219], v232 offset:4352\n" \
"v_cvt_pk_bf16_f32 v83, v86, v87\n" \
"v_mfma_f32_16x16x32_bf16 v[144:147], a[224:227], v[0:3], v[144:147]\n" \
"v_exp_f32 v96, v96\n" \
"v_mfma_f32_16x16x32_bf16 v[144:147], a[228:231], v[4:7], v[144:147]\n" \
"buffer_load_dwordx4 v234, s[36:39], s71 offen offset:2048 sc1 lds\n" \
"v_mfma_f32_16x16x32_bf16 v[176:179], a[224:227], v[16:19], v[176:179]\n" \
"v_exp_f32 v97, v97\n" \
"v_mfma_f32_16x16x32_bf16 v[176:179], a[228:231], v[20:23], v[176:179]\n" \
"ds_read_b128 a[220:223], v232 offset:6400\n" \
"v_cvt_pk_bf16_f32 v96, v96, v97\n" \
"v_mfma_f32_16x16x32_bf16 a[80:83], a[224:227], v[32:35], a[80:83]\n" \
"v_exp_f32 v98, v98\n" \
"v_mfma_f32_16x16x32_bf16 a[80:83], a[228:231], v[36:39], a[80:83]\n" \
"v_mfma_f32_16x16x32_bf16 a[112:115], a[224:227], v[48:51], a[112:115]\n" \
"v_exp_f32 v99, v99\n" \
"v_mfma_f32_16x16x32_bf16 a[112:115], a[228:231], v[52:55], a[112:115]\n" \
"ds_read_b128 a[224:227], v232 offset:8192\n" \
"v_cvt_pk_bf16_f32 v97, v98, v99\n" \
"v_mfma_f32_16x16x32_bf16 a[116:119], a[232:235], v[48:51], a[116:119]\n" \
"v_exp_f32 v100, v100\n" \
"v_mfma_f32_16x16x32_bf16 a[116:119], a[236:239], v[52:55], a[116:119]\n" \
"buffer_load_dwordx4 v234, s[36:39], s71 offen offset:3072 sc1 lds\n" \
"v_mfma_f32_16x16x32_bf16 a[84:87], a[232:235], v[32:35], a[84:87]\n" \
"v_exp_f32 v101, v101\n" \
"v_mfma_f32_16x16x32_bf16 a[84:87], a[236:239], v[36:39], a[84:87]\n" \
"ds_read_b128 a[228:231], v232 offset:10240\n" \
"v_cvt_pk_bf16_f32 v98, v100, v101\n" \
"v_smfmac_f32_16x16x64_bf16 v[224:227], v[244:247], v[32:39], v243\n" \
"v_mfma_f32_16x16x32_bf16 v[180:183], a[232:235], v[16:19], v[180:183]\n" \
"v_exp_f32 v102, v102\n" \
"v_mfma_f32_16x16x32_bf16 v[180:183], a[236:239], v[20:23], v[180:183]\n" \
"buffer_load_dword v239, v242, s[56:59], s95 offen sc1\n" \
"v_mfma_f32_16x16x32_bf16 v[148:151], a[232:235], v[0:3], v[148:151]\n" \
"s_mov_b32 s69, s92\n" \
"v_exp_f32 v103, v103\n" \
"v_mfma_f32_16x16x32_bf16 v[148:151], a[236:239], v[4:7], v[148:151]\n" \
"s_mov_b32 s76, s73\n" \
"ds_read_b128 a[232:235], v232 offset:12288\n" \
"v_cvt_pk_bf16_f32 v99, v102, v103\n" \
"v_mfma_f32_16x16x32_bf16 v[152:155], a[32:35], v[0:3], v[152:155]\n" \
"s_add_u32 s73, s73, 34816\n" \
"v_exp_f32 v112, v112\n" \
"v_mfma_f32_16x16x32_bf16 v[152:155], a[36:39], v[4:7], v[152:155]\n" \
"s_cmp_eq_u32 s73, s83\n" \
"s_cselect_b32 s70, 34816, 0\n" \
"s_add_u32 s73, s73, s70\n" \
"v_mfma_f32_16x16x32_bf16 v[184:187], a[32:35], v[16:19], v[184:187]\n" \
"s_xor_b32 s74, s74, 0x8000\n" \
"v_exp_f32 v113, v113\n" \
"v_mfma_f32_16x16x32_bf16 v[184:187], a[36:39], v[20:23], v[184:187]\n" \
"s_xor_b32 s77, s77, 0x8000\n" \
"ds_read_b128 a[236:239], v232 offset:14336\n" \
"v_cvt_pk_bf16_f32 v112, v112, v113\n" \
"v_mfma_f32_16x16x32_bf16 a[88:91], a[32:35], v[32:35], a[88:91]\n" \
"v_exp_f32 v114, v114\n" \
"v_mfma_f32_16x16x32_bf16 a[88:91], a[36:39], v[36:39], a[88:91]\n" \
"v_mfma_f32_16x16x32_bf16 a[120:123], a[32:35], v[48:51], a[120:123]\n" \
"v_exp_f32 v115, v115\n" \
"v_mfma_f32_16x16x32_bf16 a[120:123], a[36:39], v[52:55], a[120:123]\n" \
"ds_read_b128 a[244:247], v232 offset:10496\n" \
"v_cvt_pk_bf16_f32 v113, v114, v115\n" \
"v_mfma_f32_16x16x32_bf16 a[124:127], a[40:43], v[48:51], a[124:127]\n" \
"v_exp_f32 v116, v116\n" \
"v_mfma_f32_16x16x32_bf16 a[124:127], a[44:47], v[52:55], a[124:127]\n" \
"v_mfma_f32_16x16x32_bf16 a[92:95], a[40:43], v[32:35], a[92:95]\n" \
"v_exp_f32 v117, v117\n" \
"v_mfma_f32_16x16x32_bf16 a[92:95], a[44:47], v[36:39], a[92:95]\n" \
"ds_read_b128 a[248:251], v232 offset:12544\n" \
"v_cvt_pk_bf16_f32 v114, v116, v117\n" \
"v_smfmac_f32_16x16x64_bf16 v[228:231], v[244:247], v[48:55], v243\n" \
"v_mfma_f32_16x16x32_bf16 v[188:191], a[40:43], v[16:19], v[188:191]\n" \
"v_exp_f32 v118, v118\n" \
"v_mfma_f32_16x16x32_bf16 v[188:191], a[44:47], v[20:23], v[188:191]\n" \
"s_cmp_lg_u32 s89, 0\n" \
"v_exp_f32 v119, v119\n" \
"v_mfma_f32_16x16x32_bf16 v[156:159], a[40:43], v[0:3], v[156:159]\n" \
"s_waitcnt lgkmcnt(2)\n" \
"ds_read_b128 a[252:255], v232 offset:14592\n" \
"v_cvt_pk_bf16_f32 v115, v118, v119\n" \
"v_mfma_f32_16x16x32_bf16 v[156:159], a[44:47], v[4:7], v[156:159]\n" \
"s_cbranch_scc1 Lloop_%=\n" \
"s_cmp_eq_u32 s63, 7\n" \
"s_cbranch_scc0 Lloop2_%=\n" \
"s_branch Lg_%=\n" \
"s_nop 0\n" \
"s_nop 0\n" \
"Lep_%=:\n" \
"Lpg6064_%=:\n" \
"s_cbranch_execz Lpg7381_%=\n" \
"s_nop 4\n" \
"v_accvgpr_read_b32 v92, a48\n" \
"v_accvgpr_read_b32 v93, a49\n" \
"v_accvgpr_read_b32 v94, a52\n" \
"v_accvgpr_read_b32 v95, a53\n" \
"v_accvgpr_write_b32 a48, 0\n" \
"v_accvgpr_write_b32 a49, 0\n" \
"v_accvgpr_write_b32 a52, 0\n" \
"v_accvgpr_write_b32 a53, 0\n" \
"v_add_f32 v86, v92, v93\n" \
"v_add_f32 v87, v94, v95\n" \
"v_rcp_f32 v84, v86\n" \
"v_rcp_f32 v85, v87\n" \
"v_add_u32 v240, 0x1000, v236\n" \
"v_add_f32 v80, v86, v87\n" \
"v_mul_f32 v80, 0x43800000, v80\n" \
"v_add_f32 v86, v84, v85\n" \
"v_fmac_f32 v80, 0x5f800000, v86\n" \
"v_mov_b32 v81, 0\n" \
"v_mov_b32 v82, 0\n" \
"v_mov_b32 v83, 0\n" \
"v_mov_b32 v88, 0\n" \
"v_mov_b32 v89, 0\n" \
"v_mov_b32 v90, 0\n" \
"v_mov_b32 v91, 0\n" \
"v_pk_mul_f32 v[128:129], v[128:129], v[84:85] op_sel:[0,0] op_sel_hi:[1,0]\n" \
"v_pk_mul_f32 v[130:131], v[130:131], v[84:85] op_sel:[0,0] op_sel_hi:[1,0]\n" \
"v_pk_mul_f32 v[132:133], v[132:133], v[84:85] op_sel:[0,0] op_sel_hi:[1,0]\n" \
"v_pk_mul_f32 v[134:135], v[134:135], v[84:85] op_sel:[0,0] op_sel_hi:[1,0]\n" \
"v_cvt_pk_bf16_f32 v64, v128, v129\n" \
"v_cvt_pk_bf16_f32 v65, v130, v131\n" \
"v_cvt_pk_bf16_f32 v66, v132, v133\n" \
"v_cvt_pk_bf16_f32 v67, v134, v135\n" \
"buffer_store_dwordx4 v[64:67], v236, s[56:59], s87 offen offset:0 sc1\n" \
"v_pk_mul_f32 v[136:137], v[136:137], v[84:85] op_sel:[0,0] op_sel_hi:[1,0]\n" \
"v_pk_mul_f32 v[138:139], v[138:139], v[84:85] op_sel:[0,0] op_sel_hi:[1,0]\n" \
"v_pk_mul_f32 v[140:141], v[140:141], v[84:85] op_sel:[0,0] op_sel_hi:[1,0]\n" \
"v_pk_mul_f32 v[142:143], v[142:143], v[84:85] op_sel:[0,0] op_sel_hi:[1,0]\n" \
"v_cvt_pk_bf16_f32 v68, v136, v137\n" \
"v_cvt_pk_bf16_f32 v69, v138, v139\n" \
"v_cvt_pk_bf16_f32 v70, v140, v141\n" \
"v_cvt_pk_bf16_f32 v71, v142, v143\n" \
"buffer_store_dwordx4 v[68:71], v236, s[56:59], s87 offen offset:2048 sc1\n" \
"v_pk_mul_f32 v[144:145], v[144:145], v[84:85] op_sel:[0,0] op_sel_hi:[1,0]\n" \
"v_pk_mul_f32 v[146:147], v[146:147], v[84:85] op_sel:[0,0] op_sel_hi:[1,0]\n" \
"v_pk_mul_f32 v[148:149], v[148:149], v[84:85] op_sel:[0,0] op_sel_hi:[1,0]\n" \
"v_pk_mul_f32 v[150:151], v[150:151], v[84:85] op_sel:[0,0] op_sel_hi:[1,0]\n" \
"v_cvt_pk_bf16_f32 v72, v144, v145\n" \
"v_cvt_pk_bf16_f32 v73, v146, v147\n" \
"v_cvt_pk_bf16_f32 v74, v148, v149\n" \
"v_cvt_pk_bf16_f32 v75, v150, v151\n" \
"buffer_store_dwordx4 v[72:75], v240, s[56:59], s87 offen offset:0 sc1\n" \
"v_pk_mul_f32 v[152:153], v[152:153], v[84:85] op_sel:[0,0] op_sel_hi:[1,0]\n" \
"v_pk_mul_f32 v[154:155], v[154:155], v[84:85] op_sel:[0,0] op_sel_hi:[1,0]\n" \
"v_pk_mul_f32 v[156:157], v[156:157], v[84:85] op_sel:[0,0] op_sel_hi:[1,0]\n" \
"v_pk_mul_f32 v[158:159], v[158:159], v[84:85] op_sel:[0,0] op_sel_hi:[1,0]\n" \
"v_cvt_pk_bf16_f32 v76, v152, v153\n" \
"v_cvt_pk_bf16_f32 v77, v154, v155\n" \
"v_cvt_pk_bf16_f32 v78, v156, v157\n" \
"v_cvt_pk_bf16_f32 v79, v158, v159\n" \
"buffer_store_dwordx4 v[76:79], v240, s[56:59], s87 offen offset:2048 sc1\n" \
"v_smfmac_f32_16x16x64_bf16 v[80:83], v[244:247], v[64:71], v243\n" \
"v_smfmac_f32_16x16x64_bf16 v[80:83], v[244:247], v[72:79], v243\n" \
"v_mfma_f32_32x32x16_bf16 v[128:143], v[88:91], v[88:91], 0\n" \
"v_mfma_f32_32x32x16_bf16 v[144:159], v[88:91], v[88:91], 0\n" \
"s_mov_b64 exec, 0\n" \
"s_cmp_eq_u32 s61, 0\n" \
"s_cbranch_scc1 Lst1x0_%=\n" \
"s_cmp_eq_u32 s61, 1\n" \
"s_cbranch_scc1 Lst1x1_%=\n" \
"s_cmp_eq_u32 s61, 2\n" \
"s_cbranch_scc1 Lst1x2_%=\n" \
"s_branch Lst1x3_%=\n" \
"Lcb1_%=:\n" \
"s_mov_b64 exec, -1\n" \
"s_add_u32 s72, s87, 532480\n" \
"v_pk_mul_f32 v[160:161], v[160:161], v[84:85] op_sel:[0,1] op_sel_hi:[1,1]\n" \
"v_pk_mul_f32 v[162:163], v[162:163], v[84:85] op_sel:[0,1] op_sel_hi:[1,1]\n" \
"v_pk_mul_f32 v[164:165], v[164:165], v[84:85] op_sel:[0,1] op_sel_hi:[1,1]\n" \
"v_pk_mul_f32 v[166:167], v[166:167], v[84:85] op_sel:[0,1] op_sel_hi:[1,1]\n" \
"v_cvt_pk_bf16_f32 v64, v160, v161\n" \
"v_cvt_pk_bf16_f32 v65, v162, v163\n" \
"v_cvt_pk_bf16_f32 v66, v164, v165\n" \
"v_cvt_pk_bf16_f32 v67, v166, v167\n" \
"buffer_store_dwordx4 v[64:67], v236, s[56:59], s72 offen offset:0 sc1\n" \
"v_pk_mul_f32 v[168:169], v[168:169], v[84:85] op_sel:[0,1] op_sel_hi:[1,1]\n" \
"v_pk_mul_f32 v[170:171], v[170:171], v[84:85] op_sel:[0,1] op_sel_hi:[1,1]\n" \
"v_pk_mul_f32 v[172:173], v[172:173], v[84:85] op_sel:[0,1] op_sel_hi:[1,1]\n" \
"v_pk_mul_f32 v[174:175], v[174:175], v[84:85] op_sel:[0,1] op_sel_hi:[1,1]\n" \
"v_cvt_pk_bf16_f32 v68, v168, v169\n" \
"v_cvt_pk_bf16_f32 v69, v170, v171\n" \
"v_cvt_pk_bf16_f32 v70, v172, v173\n" \
"v_cvt_pk_bf16_f32 v71, v174, v175\n" \
"buffer_store_dwordx4 v[68:71], v236, s[56:59], s72 offen offset:2048 sc1\n" \
"v_pk_mul_f32 v[176:177], v[176:177], v[84:85] op_sel:[0,1] op_sel_hi:[1,1]\n" \
"v_pk_mul_f32 v[178:179], v[178:179], v[84:85] op_sel:[0,1] op_sel_hi:[1,1]\n" \
"v_pk_mul_f32 v[180:181], v[180:181], v[84:85] op_sel:[0,1] op_sel_hi:[1,1]\n" \
"v_pk_mul_f32 v[182:183], v[182:183], v[84:85] op_sel:[0,1] op_sel_hi:[1,1]\n" \
"v_cvt_pk_bf16_f32 v72, v176, v177\n" \
"v_cvt_pk_bf16_f32 v73, v178, v179\n" \
"v_cvt_pk_bf16_f32 v74, v180, v181\n" \
"v_cvt_pk_bf16_f32 v75, v182, v183\n" \
"buffer_store_dwordx4 v[72:75], v240, s[56:59], s72 offen offset:0 sc1\n" \
"v_pk_mul_f32 v[184:185], v[184:185], v[84:85] op_sel:[0,1] op_sel_hi:[1,1]\n" \
"v_pk_mul_f32 v[186:187], v[186:187], v[84:85] op_sel:[0,1] op_sel_hi:[1,1]\n" \
"v_pk_mul_f32 v[188:189], v[188:189], v[84:85] op_sel:[0,1] op_sel_hi:[1,1]\n" \
"v_pk_mul_f32 v[190:191], v[190:191], v[84:85] op_sel:[0,1] op_sel_hi:[1,1]\n" \
"v_cvt_pk_bf16_f32 v76, v184, v185\n" \
"v_cvt_pk_bf16_f32 v77, v186, v187\n" \
"v_cvt_pk_bf16_f32 v78, v188, v189\n" \
"v_cvt_pk_bf16_f32 v79, v190, v191\n" \
"buffer_store_dwordx4 v[76:79], v240, s[56:59], s72 offen offset:2048 sc1\n" \
"v_smfmac_f32_16x16x64_bf16 v[80:83], v[244:247], v[64:71], v243\n" \
"v_smfmac_f32_16x16x64_bf16 v[80:83], v[244:247], v[72:79], v243\n" \
"v_mfma_f32_32x32x16_bf16 v[160:175], v[88:91], v[88:91], 0\n" \
"v_mfma_f32_32x32x16_bf16 v[176:191], v[88:91], v[88:91], 0\n" \
"s_nop 5\n" \
"v_add_f32 v80, v80, v81\n" \
"s_movk_i32 s64, 0x207\n" \
"v_cmp_class_f32 vcc, v80, s64\n" \
"s_nop 1\n" \
"s_cmp_lg_u64 vcc, 0\n" \
"s_addc_u32 s90, s90, 0\n" \
"s_waitcnt vmcnt(8)\n" \
"s_cmp_eq_u32 s63, 7\n" \
"s_cbranch_scc0 Lepr_%=\n" \
"s_branch Lepg_%=\n" \
".p2align 12\n" \
"Lloop2_%=:\n" \
"v_mfma_f32_16x16x32_bf16 v[0:3], a[192:195], a[0:3], v[196:199]\n" \
"Lpg6195_%=:\n" \
"s_cbranch_execz Lcb0_%=\n" \
"v_exp_f32 v72, v72\n" \
"v_mfma_f32_16x16x32_bf16 v[0:3], a[196:199], a[4:7], v[0:3]\n" \
"ds_read_b128 a[44:47], v233 offset:15360\n" \
"v_mfma_f32_16x16x32_bf16 v[0:3], a[200:203], a[8:11], v[0:3]\n" \
"v_exp_f32 v73, v73\n" \
"v_mfma_f32_16x16x32_bf16 v[0:3], a[204:207], a[12:15], v[0:3]\n" \
"ds_read_b128 a[32:35], v233 offset:6144\n" \
"v_cvt_pk_bf16_f32 v68, v72, v73\n" \
"v_mfma_f32_16x16x32_bf16 v[16:19], a[204:207], a[28:31], v[196:199]\n" \
"v_exp_f32 v74, v74\n" \
"v_mfma_f32_16x16x32_bf16 v[16:19], a[200:203], a[24:27], v[16:19]\n" \
"v_xor_b32 v232, 0x8000, v232\n" \
"v_mfma_f32_16x16x32_bf16 v[16:19], a[196:199], a[20:23], v[16:19]\n" \
"v_exp_f32 v75, v75\n" \
"v_mfma_f32_16x16x32_bf16 v[16:19], a[192:195], a[16:19], v[16:19]\n" \
"ds_read_b128 a[36:39], v233 offset:14336\n" \
"v_cvt_pk_bf16_f32 v69, v74, v75\n" \
"v_mfma_f32_16x16x32_bf16 v[32:35], a[192:195], a[160:163], v[196:199]\n" \
"v_exp_f32 v76, v76\n" \
"v_mfma_f32_16x16x32_bf16 v[32:35], a[196:199], a[164:167], v[32:35]\n" \
"v_mfma_f32_16x16x32_bf16 v[32:35], a[200:203], a[168:171], v[32:35]\n" \
"v_exp_f32 v77, v77\n" \
"v_mfma_f32_16x16x32_bf16 v[32:35], a[204:207], a[172:175], v[32:35]\n" \
"ds_read_b128 a[40:43], v233 offset:7168\n" \
"v_cvt_pk_bf16_f32 v70, v76, v77\n" \
"v_mfma_f32_16x16x32_bf16 v[48:51], a[204:207], a[188:191], v[196:199]\n" \
"v_exp_f32 v78, v78\n" \
"v_mfma_f32_16x16x32_bf16 v[48:51], a[200:203], a[184:187], v[48:51]\n" \
"v_mfma_f32_16x16x32_bf16 v[48:51], a[196:199], a[180:183], v[48:51]\n" \
"v_exp_f32 v79, v79\n" \
"v_mfma_f32_16x16x32_bf16 v[48:51], a[192:195], a[176:179], v[48:51]\n" \
"ds_read_b128 a[204:207], v233 offset:9216\n" \
"v_cvt_pk_bf16_f32 v71, v78, v79\n" \
"v_mfma_f32_16x16x32_bf16 v[52:55], a[208:211], a[176:179], v[196:199]\n" \
"v_exp_f32 v88, v88\n" \
"v_mfma_f32_16x16x32_bf16 v[52:55], a[212:215], a[180:183], v[52:55]\n" \
"v_mfma_f32_16x16x32_bf16 v[52:55], a[216:219], a[184:187], v[52:55]\n" \
"v_exp_f32 v89, v89\n" \
"v_mfma_f32_16x16x32_bf16 v[52:55], a[220:223], a[188:191], v[52:55]\n" \
"ds_read_b128 a[200:203], v233 offset:1024\n" \
"v_cvt_pk_bf16_f32 v84, v88, v89\n" \
"v_mfma_f32_16x16x32_bf16 v[36:39], a[220:223], a[172:175], v[196:199]\n" \
"v_exp_f32 v90, v90\n" \
"v_mfma_f32_16x16x32_bf16 v[36:39], a[216:219], a[168:171], v[36:39]\n" \
"v_mfma_f32_16x16x32_bf16 v[36:39], a[212:215], a[164:167], v[36:39]\n" \
"v_exp_f32 v91, v91\n" \
"v_mfma_f32_16x16x32_bf16 v[36:39], a[208:211], a[160:163], v[36:39]\n" \
"ds_read_b128 a[196:199], v233 offset:8192\n" \
"v_cvt_pk_bf16_f32 v85, v90, v91\n" \
"v_mfma_f32_16x16x32_bf16 v[20:23], a[208:211], a[16:19], v[196:199]\n" \
"v_exp_f32 v92, v92\n" \
"v_mfma_f32_16x16x32_bf16 v[20:23], a[212:215], a[20:23], v[20:23]\n" \
"v_mfma_f32_16x16x32_bf16 v[20:23], a[216:219], a[24:27], v[20:23]\n" \
"v_exp_f32 v93, v93\n" \
"v_mfma_f32_16x16x32_bf16 v[20:23], a[220:223], a[28:31], v[20:23]\n" \
"ds_read_b128 a[192:195], v233 offset:0\n" \
"v_cvt_pk_bf16_f32 v86, v92, v93\n" \
"v_mfma_f32_16x16x32_bf16 v[4:7], a[220:223], a[12:15], v[196:199]\n" \
"v_exp_f32 v94, v94\n" \
"v_mfma_f32_16x16x32_bf16 v[4:7], a[216:219], a[8:11], v[4:7]\n" \
"v_mfma_f32_16x16x32_bf16 v[4:7], a[212:215], a[4:7], v[4:7]\n" \
"s_waitcnt lgkmcnt(8)\n" \
"v_exp_f32 v95, v95\n" \
"v_mfma_f32_16x16x32_bf16 v[4:7], a[208:211], a[0:3], v[4:7]\n" \
"ds_read_b128 a[220:223], v233 offset:11264\n" \
"v_cvt_pk_bf16_f32 v87, v94, v95\n" \
"v_mfma_f32_16x16x32_bf16 v[8:11], a[224:227], a[0:3], v[196:199]\n" \
"v_exp_f32 v104, v104\n" \
"v_mfma_f32_16x16x32_bf16 v[8:11], a[228:231], a[4:7], v[8:11]\n" \
"v_mfma_f32_16x16x32_bf16 v[8:11], a[232:235], a[8:11], v[8:11]\n" \
"v_exp_f32 v105, v105\n" \
"v_mfma_f32_16x16x32_bf16 v[8:11], a[236:239], a[12:15], v[8:11]\n" \
"ds_read_b128 a[216:219], v233 offset:3072\n" \
"v_cvt_pk_bf16_f32 v100, v104, v105\n" \
"v_mfma_f32_16x16x32_bf16 v[24:27], a[236:239], a[28:31], v[196:199]\n" \
"v_exp_f32 v106, v106\n" \
"v_mfma_f32_16x16x32_bf16 v[24:27], a[232:235], a[24:27], v[24:27]\n" \
"v_mfma_f32_16x16x32_bf16 v[24:27], a[228:231], a[20:23], v[24:27]\n" \
"v_exp_f32 v107, v107\n" \
"v_mfma_f32_16x16x32_bf16 v[24:27], a[224:227], a[16:19], v[24:27]\n" \
"ds_read_b128 a[212:215], v233 offset:10240\n" \
"v_cvt_pk_bf16_f32 v101, v106, v107\n" \
"v_mfma_f32_16x16x32_bf16 v[40:43], a[224:227], a[160:163], v[196:199]\n" \
"v_exp_f32 v108, v108\n" \
"v_mfma_f32_16x16x32_bf16 v[40:43], a[228:231], a[164:167], v[40:43]\n" \
"v_mfma_f32_16x16x32_bf16 v[40:43], a[232:235], a[168:171], v[40:43]\n" \
"v_exp_f32 v109, v109\n" \
"v_mfma_f32_16x16x32_bf16 v[40:43], a[236:239], a[172:175], v[40:43]\n" \
"ds_read_b128 a[208:211], v233 offset:2048\n" \
"v_cvt_pk_bf16_f32 v102, v108, v109\n" \
"v_mfma_f32_16x16x32_bf16 v[56:59], a[236:239], a[188:191], v[196:199]\n" \
"v_exp_f32 v110, v110\n" \
"v_mfma_f32_16x16x32_bf16 v[56:59], a[232:235], a[184:187], v[56:59]\n" \
"v_mfma_f32_16x16x32_bf16 v[56:59], a[228:231], a[180:183], v[56:59]\n" \
"v_exp_f32 v111, v111\n" \
"v_mfma_f32_16x16x32_bf16 v[56:59], a[224:227], a[176:179], v[56:59]\n" \
"ds_read_b128 a[236:239], v233 offset:13312\n" \
"v_cvt_pk_bf16_f32 v103, v110, v111\n" \
"v_mfma_f32_16x16x32_bf16 v[60:63], a[240:243], a[176:179], v[196:199]\n" \
"v_exp_f32 v120, v120\n" \
"v_mfma_f32_16x16x32_bf16 v[60:63], a[244:247], a[180:183], v[60:63]\n" \
"ds_read_b128 a[232:235], v233 offset:5120\n" \
"v_mfma_f32_16x16x32_bf16 v[60:63], a[248:251], a[184:187], v[60:63]\n" \
"v_exp_f32 v121, v121\n" \
"v_mfma_f32_16x16x32_bf16 v[60:63], a[252:255], a[188:191], v[60:63]\n" \
"ds_read_b128 a[228:231], v233 offset:12288\n" \
"v_cvt_pk_bf16_f32 v116, v120, v121\n" \
"v_mfma_f32_16x16x32_bf16 v[44:47], a[252:255], a[172:175], v[196:199]\n" \
"v_exp_f32 v122, v122\n" \
"v_mfma_f32_16x16x32_bf16 v[44:47], a[248:251], a[168:171], v[44:47]\n" \
"ds_read_b128 a[224:227], v233 offset:4096\n" \
"v_mfma_f32_16x16x32_bf16 v[44:47], a[244:247], a[164:167], v[44:47]\n" \
"v_exp_f32 v123, v123\n" \
"v_mfma_f32_16x16x32_bf16 v[44:47], a[240:243], a[160:163], v[44:47]\n" \
"v_cvt_pk_bf16_f32 v117, v122, v123\n" \
"v_mfma_f32_16x16x32_bf16 v[28:31], a[240:243], a[16:19], v[196:199]\n" \
"v_exp_f32 v124, v124\n" \
"v_mfma_f32_16x16x32_bf16 v[28:31], a[244:247], a[20:23], v[28:31]\n" \
"v_mfma_f32_16x16x32_bf16 v[28:31], a[248:251], a[24:27], v[28:31]\n" \
"v_exp_f32 v125, v125\n" \
"v_mfma_f32_16x16x32_bf16 v[28:31], a[252:255], a[28:31], v[28:31]\n" \
"v_cvt_pk_bf16_f32 v118, v124, v125\n" \
"v_mfma_f32_16x16x32_bf16 v[12:15], a[252:255], a[12:15], v[196:199]\n" \
"v_exp_f32 v126, v126\n" \
"v_mfma_f32_16x16x32_bf16 v[12:15], a[248:251], a[8:11], v[12:15]\n" \
"v_mfma_f32_16x16x32_bf16 v[12:15], a[244:247], a[4:7], v[12:15]\n" \
"s_waitcnt vmcnt(0) lgkmcnt(0)\n" \
"v_exp_f32 v127, v127\n" \
"v_mfma_f32_16x16x32_bf16 v[12:15], a[240:243], a[0:3], v[12:15]\n" \
"s_barrier\n" \
"v_cvt_pk_bf16_f32 v119, v126, v127\n" \
"v_mfma_f32_16x16x32_bf16 v[128:131], a[192:195], v[64:67], v[128:131]\n" \
"v_exp_f32 v0, v0\n" \
"v_mfma_f32_16x16x32_bf16 v[128:131], a[196:199], v[68:71], v[128:131]\n" \
"v_cmp_eq_u32 vcc, s60, v239\n" \
"v_mfma_f32_16x16x32_bf16 v[160:163], a[192:195], v[80:83], v[160:163]\n" \
"v_exp_f32 v1, v1\n" \
"v_mfma_f32_16x16x32_bf16 v[160:163], a[196:199], v[84:87], v[160:163]\n" \
"s_andn2_b32 s70, s69, vcc_lo\n" \
"s_cbranch_scc1 Lpl3_%=\n" \
"Lpr3_%=:\n" \
"ds_read_b128 a[240:243], v232 offset:8448\n" \
"v_cvt_pk_bf16_f32 v0, v0, v1\n" \
"v_mfma_f32_16x16x32_bf16 a[64:67], a[192:195], v[96:99], a[64:67]\n" \
"s_add_u32 m0, s74, s79\n" \
"v_exp_f32 v2, v2\n" \
"v_mfma_f32_16x16x32_bf16 a[64:67], a[196:199], v[100:103], a[64:67]\n" \
"buffer_load_dwordx4 v234, s[36:39], s73 offen offset:0 sc1 lds\n" \
"v_mfma_f32_16x16x32_bf16 a[96:99], a[192:195], v[112:115], a[96:99]\n" \
"v_exp_f32 v3, v3\n" \
"v_mfma_f32_16x16x32_bf16 a[96:99], a[196:199], v[116:119], a[96:99]\n" \
"ds_read_b128 a[192:195], v232 offset:0\n" \
"v_cvt_pk_bf16_f32 v1, v2, v3\n" \
"v_mfma_f32_16x16x32_bf16 a[100:103], a[200:203], v[112:115], a[100:103]\n" \
"s_add_u32 s71, s73, 17408\n" \
"v_exp_f32 v4, v4\n" \
"v_mfma_f32_16x16x32_bf16 a[100:103], a[204:207], v[116:119], a[100:103]\n" \
"buffer_load_dwordx4 v234, s[36:39], s73 offen offset:1024 sc1 lds\n" \
"v_mfma_f32_16x16x32_bf16 a[68:71], a[200:203], v[96:99], a[68:71]\n" \
"v_exp_f32 v5, v5\n" \
"v_mfma_f32_16x16x32_bf16 a[68:71], a[204:207], v[100:103], a[68:71]\n" \
"ds_read_b128 a[196:199], v232 offset:2048\n" \
"v_cvt_pk_bf16_f32 v2, v4, v5\n" \
"v_smfmac_f32_16x16x64_bf16 a[48:51], v[244:247], v[64:71], v243\n" \
"buffer_load_dwordx4 v234, s[36:39], s73 offen offset:2048 sc1 lds\n" \
"v_mfma_f32_16x16x32_bf16 v[164:167], a[200:203], v[80:83], v[164:167]\n" \
"v_exp_f32 v6, v6\n" \
"v_mfma_f32_16x16x32_bf16 v[164:167], a[204:207], v[84:87], v[164:167]\n" \
"buffer_load_dwordx4 v234, s[36:39], s73 offen offset:3072 sc1 lds\n" \
"v_mfma_f32_16x16x32_bf16 v[132:135], a[200:203], v[64:67], v[132:135]\n" \
"v_exp_f32 v7, v7\n" \
"v_mfma_f32_16x16x32_bf16 v[132:135], a[204:207], v[68:71], v[132:135]\n" \
"ds_read_b128 a[200:203], v232 offset:4096\n" \
"v_cvt_pk_bf16_f32 v3, v6, v7\n" \
"v_mfma_f32_16x16x32_bf16 v[136:139], a[208:211], v[64:67], v[136:139]\n" \
"s_add_u32 m0, m0, 0x4000\n" \
"v_exp_f32 v16, v16\n" \
"v_mfma_f32_16x16x32_bf16 v[136:139], a[212:215], v[68:71], v[136:139]\n" \
"buffer_load_dwordx4 v234, s[36:39], s71 offen offset:0 sc1 lds\n" \
"v_mfma_f32_16x16x32_bf16 v[168:171], a[208:211], v[80:83], v[168:171]\n" \
"v_exp_f32 v17, v17\n" \
"v_mfma_f32_16x16x32_bf16 v[168:171], a[212:215], v[84:87], v[168:171]\n" \
"ds_read_b128 a[204:207], v232 offset:6144\n" \
"v_cvt_pk_bf16_f32 v16, v16, v17\n" \
"v_mfma_f32_16x16x32_bf16 a[72:75], a[208:211], v[96:99], a[72:75]\n" \
"v_exp_f32 v18, v18\n" \
"v_mfma_f32_16x16x32_bf16 a[72:75], a[212:215], v[100:103], a[72:75]\n" \
"buffer_load_dwordx4 v234, s[36:39], s71 offen offset:1024 sc1 lds\n" \
"v_mfma_f32_16x16x32_bf16 a[104:107], a[208:211], v[112:115], a[104:107]\n" \
"v_exp_f32 v19, v19\n" \
"v_mfma_f32_16x16x32_bf16 a[104:107], a[212:215], v[116:119], a[104:107]\n" \
"ds_read_b128 a[208:211], v232 offset:256\n" \
"v_cvt_pk_bf16_f32 v17, v18, v19\n" \
"v_mfma_f32_16x16x32_bf16 a[108:111], a[216:219], v[112:115], a[108:111]\n" \
"v_exp_f32 v20, v20\n" \
"v_mfma_f32_16x16x32_bf16 a[108:111], a[220:223], v[116:119], a[108:111]\n" \
"buffer_load_dwordx4 v234, s[36:39], s71 offen offset:2048 sc1 lds\n" \
"v_mfma_f32_16x16x32_bf16 a[76:79], a[216:219], v[96:99], a[76:79]\n" \
"v_exp_f32 v21, v21\n" \
"v_mfma_f32_16x16x32_bf16 a[76:79], a[220:223], v[100:103], a[76:79]\n" \
"ds_read_b128 a[212:215], v232 offset:2304\n" \
"v_cvt_pk_bf16_f32 v18, v20, v21\n" \
"v_smfmac_f32_16x16x64_bf16 a[52:55], v[244:247], v[80:87], v243\n" \
"buffer_load_dwordx4 v234, s[36:39], s71 offen offset:3072 sc1 lds\n" \
"v_mfma_f32_16x16x32_bf16 v[172:175], a[216:219], v[80:83], v[172:175]\n" \
"s_add_u32 m0, s77, s80\n" \
"v_exp_f32 v22, v22\n" \
"v_mfma_f32_16x16x32_bf16 v[172:175], a[220:223], v[84:87], v[172:175]\n" \
"buffer_load_dwordx4 v234, s[40:43], s76 offen offset:0 sc1 lds\n" \
"v_mfma_f32_16x16x32_bf16 v[140:143], a[216:219], v[64:67], v[140:143]\n" \
"v_exp_f32 v23, v23\n" \
"v_mfma_f32_16x16x32_bf16 v[140:143], a[220:223], v[68:71], v[140:143]\n" \
"ds_read_b128 a[216:219], v232 offset:4352\n" \
"v_cvt_pk_bf16_f32 v19, v22, v23\n" \
"v_mfma_f32_16x16x32_bf16 v[144:147], a[224:227], v[64:67], v[144:147]\n" \
"s_add_u32 s71, s76, 17408\n" \
"v_exp_f32 v32, v32\n" \
"v_mfma_f32_16x16x32_bf16 v[144:147], a[228:231], v[68:71], v[144:147]\n" \
"buffer_load_dwordx4 v234, s[40:43], s76 offen offset:1024 sc1 lds\n" \
"v_mfma_f32_16x16x32_bf16 v[176:179], a[224:227], v[80:83], v[176:179]\n" \
"v_exp_f32 v33, v33\n" \
"v_mfma_f32_16x16x32_bf16 v[176:179], a[228:231], v[84:87], v[176:179]\n" \
"ds_read_b128 a[220:223], v232 offset:6400\n" \
"v_cvt_pk_bf16_f32 v32, v32, v33\n" \
"v_mfma_f32_16x16x32_bf16 a[80:83], a[224:227], v[96:99], a[80:83]\n" \
"v_exp_f32 v34, v34\n" \
"v_mfma_f32_16x16x32_bf16 a[80:83], a[228:231], v[100:103], a[80:83]\n" \
"buffer_load_dwordx4 v234, s[40:43], s76 offen offset:2048 sc1 lds\n" \
"v_mfma_f32_16x16x32_bf16 a[112:115], a[224:227], v[112:115], a[112:115]\n" \
"v_exp_f32 v35, v35\n" \
"v_mfma_f32_16x16x32_bf16 a[112:115], a[228:231], v[116:119], a[112:115]\n" \
"ds_read_b128 a[224:227], v232 offset:8192\n" \
"v_cvt_pk_bf16_f32 v33, v34, v35\n" \
"v_mfma_f32_16x16x32_bf16 a[116:119], a[232:235], v[112:115], a[116:119]\n" \
"v_exp_f32 v36, v36\n" \
"v_mfma_f32_16x16x32_bf16 a[116:119], a[236:239], v[116:119], a[116:119]\n" \
"buffer_load_dwordx4 v234, s[40:43], s76 offen offset:3072 sc1 lds\n" \
"v_mfma_f32_16x16x32_bf16 a[84:87], a[232:235], v[96:99], a[84:87]\n" \
"v_exp_f32 v37, v37\n" \
"v_mfma_f32_16x16x32_bf16 a[84:87], a[236:239], v[100:103], a[84:87]\n" \
"s_add_u32 m0, m0, 0x4000\n" \
"ds_read_b128 a[228:231], v232 offset:10240\n" \
"v_cvt_pk_bf16_f32 v34, v36, v37\n" \
"v_smfmac_f32_16x16x64_bf16 v[224:227], v[244:247], v[96:103], v243\n" \
"buffer_load_dwordx4 v234, s[40:43], s71 offen offset:0 sc1 lds\n" \
"v_mfma_f32_16x16x32_bf16 v[180:183], a[232:235], v[80:83], v[180:183]\n" \
"v_exp_f32 v38, v38\n" \
"v_mfma_f32_16x16x32_bf16 v[180:183], a[236:239], v[84:87], v[180:183]\n" \
"buffer_load_dwordx4 v234, s[40:43], s71 offen offset:1024 sc1 lds\n" \
"v_mfma_f32_16x16x32_bf16 v[148:151], a[232:235], v[64:67], v[148:151]\n" \
"v_exp_f32 v39, v39\n" \
"v_mfma_f32_16x16x32_bf16 v[148:151], a[236:239], v[68:71], v[148:151]\n" \
"ds_read_b128 a[232:235], v232 offset:12288\n" \
"v_cvt_pk_bf16_f32 v35, v38, v39\n" \
"v_mfma_f32_16x16x32_bf16 v[152:155], a[32:35], v[64:67], v[152:155]\n" \
"v_exp_f32 v48, v48\n" \
"v_mfma_f32_16x16x32_bf16 v[152:155], a[36:39], v[68:71], v[152:155]\n" \
"buffer_load_dwordx4 v234, s[40:43], s71 offen offset:2048 sc1 lds\n" \
"v_mfma_f32_16x16x32_bf16 v[184:187], a[32:35], v[80:83], v[184:187]\n" \
"v_exp_f32 v49, v49\n" \
"v_mfma_f32_16x16x32_bf16 v[184:187], a[36:39], v[84:87], v[184:187]\n" \
"ds_read_b128 a[236:239], v232 offset:14336\n" \
"v_cvt_pk_bf16_f32 v48, v48, v49\n" \
"v_mfma_f32_16x16x32_bf16 a[88:91], a[32:35], v[96:99], a[88:91]\n" \
"v_exp_f32 v50, v50\n" \
"v_mfma_f32_16x16x32_bf16 a[88:91], a[36:39], v[100:103], a[88:91]\n" \
"buffer_load_dwordx4 v234, s[40:43], s71 offen offset:3072 sc1 lds\n" \
"v_mfma_f32_16x16x32_bf16 a[120:123], a[32:35], v[112:115], a[120:123]\n" \
"s_mov_b32 s76, s73\n" \
"v_exp_f32 v51, v51\n" \
"v_mfma_f32_16x16x32_bf16 a[120:123], a[36:39], v[116:119], a[120:123]\n" \
"s_add_u32 s73, s73, 34816\n" \
"ds_read_b128 a[244:247], v232 offset:10496\n" \
"v_cvt_pk_bf16_f32 v49, v50, v51\n" \
"v_mfma_f32_16x16x32_bf16 a[124:127], a[40:43], v[112:115], a[124:127]\n" \
"s_xor_b32 s74, s74, 0x8000\n" \
"v_exp_f32 v52, v52\n" \
"v_mfma_f32_16x16x32_bf16 a[124:127], a[44:47], v[116:119], a[124:127]\n" \
"s_xor_b32 s77, s77, 0x8000\n" \
"v_xor_b32 v233, s81, v233\n" \
"s_mov_b32 s81, 0x8000\n" \
"v_mfma_f32_16x16x32_bf16 a[92:95], a[40:43], v[96:99], a[92:95]\n" \
"v_exp_f32 v53, v53\n" \
"v_mfma_f32_16x16x32_bf16 a[92:95], a[44:47], v[100:103], a[92:95]\n" \
"ds_read_b128 a[248:251], v232 offset:12544\n" \
"v_cvt_pk_bf16_f32 v50, v52, v53\n" \
"v_smfmac_f32_16x16x64_bf16 v[228:231], v[244:247], v[112:119], v243\n" \
"v_mfma_f32_16x16x32_bf16 v[188:191], a[40:43], v[80:83], v[188:191]\n" \
"v_exp_f32 v54, v54\n" \
"v_mfma_f32_16x16x32_bf16 v[188:191], a[44:47], v[84:87], v[188:191]\n" \
"s_cmp_lg_u32 s82, 0\n" \
"v_exp_f32_e64 v55, v55\n" \
"v_mfma_f32_16x16x32_bf16 v[156:159], a[40:43], v[64:67], v[156:159]\n" \
"s_waitcnt lgkmcnt(2)\n" \
"ds_read_b128 a[252:255], v232 offset:14592\n" \
"v_cvt_pk_bf16_f32 v51, v54, v55\n" \
"v_mfma_f32_16x16x32_bf16 v[156:159], a[44:47], v[68:71], v[156:159]\n" \
"s_cbranch_scc1 Lep_%=\n" \
"Lepr_%=:\n" \
"v_mfma_f32_16x16x32_bf16 v[64:67], a[192:195], a[0:3], v[196:199]\n" \
"v_exp_f32 v8, v8\n" \
"v_mfma_f32_16x16x32_bf16 v[64:67], a[196:199], a[4:7], v[64:67]\n" \
"ds_read_b128 a[44:47], v233 offset:15360\n" \
"v_mfma_f32_16x16x32_bf16 v[64:67], a[200:203], a[8:11], v[64:67]\n" \
"v_exp_f32 v9, v9\n" \
"v_mfma_f32_16x16x32_bf16 v[64:67], a[204:207], a[12:15], v[64:67]\n" \
"ds_read_b128 a[32:35], v233 offset:6144\n" \
"v_cvt_pk_bf16_f32 v4, v8, v9\n" \
"v_mfma_f32_16x16x32_bf16 v[80:83], a[204:207], a[28:31], v[196:199]\n" \
"s_sub_u32 s84, s84, 1\n" \
"v_exp_f32 v10, v10\n" \
"v_mfma_f32_16x16x32_bf16 v[80:83], a[200:203], a[24:27], v[80:83]\n" \
"s_cmp_eq_u32 s84, 0\n" \
"s_cselect_b32 s70, s75, 0x8000\n" \
"v_xor_b32 v232, s70, v232\n" \
"v_mfma_f32_16x16x32_bf16 v[80:83], a[196:199], a[20:23], v[80:83]\n" \
"v_exp_f32 v11, v11\n" \
"v_mfma_f32_16x16x32_bf16 v[80:83], a[192:195], a[16:19], v[80:83]\n" \
"ds_read_b128 a[36:39], v233 offset:14336\n" \
"v_cvt_pk_bf16_f32 v5, v10, v11\n" \
"v_mfma_f32_16x16x32_bf16 v[96:99], a[192:195], a[160:163], v[196:199]\n" \
"v_exp_f32 v12, v12\n" \
"v_mfma_f32_16x16x32_bf16 v[96:99], a[196:199], a[164:167], v[96:99]\n" \
"v_mfma_f32_16x16x32_bf16 v[96:99], a[200:203], a[168:171], v[96:99]\n" \
"v_exp_f32 v13, v13\n" \
"v_mfma_f32_16x16x32_bf16 v[96:99], a[204:207], a[172:175], v[96:99]\n" \
"ds_read_b128 a[40:43], v233 offset:7168\n" \
"v_cvt_pk_bf16_f32 v6, v12, v13\n" \
"v_mfma_f32_16x16x32_bf16 v[112:115], a[204:207], a[188:191], v[196:199]\n" \
"v_exp_f32 v14, v14\n" \
"v_mfma_f32_16x16x32_bf16 v[112:115], a[200:203], a[184:187], v[112:115]\n" \
"v_mfma_f32_16x16x32_bf16 v[112:115], a[196:199], a[180:183], v[112:115]\n" \
"v_exp_f32 v15, v15\n" \
"v_mfma_f32_16x16x32_bf16 v[112:115], a[192:195], a[176:179], v[112:115]\n" \
"ds_read_b128 a[204:207], v233 offset:9216\n" \
"v_cvt_pk_bf16_f32 v7, v14, v15\n" \
"v_mfma_f32_16x16x32_bf16 v[116:119], a[208:211], a[176:179], v[196:199]\n" \
"v_exp_f32 v24, v24\n" \
"v_mfma_f32_16x16x32_bf16 v[116:119], a[212:215], a[180:183], v[116:119]\n" \
"v_mfma_f32_16x16x32_bf16 v[116:119], a[216:219], a[184:187], v[116:119]\n" \
"v_exp_f32 v25, v25\n" \
"v_mfma_f32_16x16x32_bf16 v[116:119], a[220:223], a[188:191], v[116:119]\n" \
"ds_read_b128 a[200:203], v233 offset:1024\n" \
"v_cvt_pk_bf16_f32 v20, v24, v25\n" \
"v_mfma_f32_16x16x32_bf16 v[100:103], a[220:223], a[172:175], v[196:199]\n" \
"v_exp_f32 v26, v26\n" \
"v_mfma_f32_16x16x32_bf16 v[100:103], a[216:219], a[168:171], v[100:103]\n" \
"v_mfma_f32_16x16x32_bf16 v[100:103], a[212:215], a[164:167], v[100:103]\n" \
"v_exp_f32 v27, v27\n" \
"v_mfma_f32_16x16x32_bf16 v[100:103], a[208:211], a[160:163], v[100:103]\n" \
"ds_read_b128 a[196:199], v233 offset:8192\n" \
"v_cvt_pk_bf16_f32 v21, v26, v27\n" \
"v_mfma_f32_16x16x32_bf16 v[84:87], a[208:211], a[16:19], v[196:199]\n" \
"v_exp_f32 v28, v28\n" \
"v_mfma_f32_16x16x32_bf16 v[84:87], a[212:215], a[20:23], v[84:87]\n" \
"v_mfma_f32_16x16x32_bf16 v[84:87], a[216:219], a[24:27], v[84:87]\n" \
"v_exp_f32 v29, v29\n" \
"v_mfma_f32_16x16x32_bf16 v[84:87], a[220:223], a[28:31], v[84:87]\n" \
"ds_read_b128 a[192:195], v233 offset:0\n" \
"v_cvt_pk_bf16_f32 v22, v28, v29\n" \
"v_mfma_f32_16x16x32_bf16 v[68:71], a[220:223], a[12:15], v[196:199]\n" \
"v_exp_f32 v30, v30\n" \
"v_mfma_f32_16x16x32_bf16 v[68:71], a[216:219], a[8:11], v[68:71]\n" \
"v_mfma_f32_16x16x32_bf16 v[68:71], a[212:215], a[4:7], v[68:71]\n" \
"s_waitcnt lgkmcnt(8)\n" \
"v_exp_f32 v31, v31\n" \
"v_mfma_f32_16x16x32_bf16 v[68:71], a[208:211], a[0:3], v[68:71]\n" \
"ds_read_b128 a[220:223], v233 offset:11264\n" \
"v_cvt_pk_bf16_f32 v23, v30, v31\n" \
"v_mfma_f32_16x16x32_bf16 v[72:75], a[224:227], a[0:3], v[196:199]\n" \
"v_exp_f32 v40, v40\n" \
"v_mfma_f32_16x16x32_bf16 v[72:75], a[228:231], a[4:7], v[72:75]\n" \
"v_mfma_f32_16x16x32_bf16 v[72:75], a[232:235], a[8:11], v[72:75]\n" \
"v_exp_f32 v41, v41\n" \
"v_mfma_f32_16x16x32_bf16 v[72:75], a[236:239], a[12:15], v[72:75]\n" \
"ds_read_b128 a[216:219], v233 offset:3072\n" \
"v_cvt_pk_bf16_f32 v36, v40, v41\n" \
"v_mfma_f32_16x16x32_bf16 v[88:91], a[236:239], a[28:31], v[196:199]\n" \
"v_exp_f32 v42, v42\n" \
"v_mfma_f32_16x16x32_bf16 v[88:91], a[232:235], a[24:27], v[88:91]\n" \
"v_mfma_f32_16x16x32_bf16 v[88:91], a[228:231], a[20:23], v[88:91]\n" \
"v_exp_f32 v43, v43\n" \
"v_mfma_f32_16x16x32_bf16 v[88:91], a[224:227], a[16:19], v[88:91]\n" \
"ds_read_b128 a[212:215], v233 offset:10240\n" \
"v_cvt_pk_bf16_f32 v37, v42, v43\n" \
"v_mfma_f32_16x16x32_bf16 v[104:107], a[224:227], a[160:163], v[196:199]\n" \
"v_exp_f32 v44, v44\n" \
"v_mfma_f32_16x16x32_bf16 v[104:107], a[228:231], a[164:167], v[104:107]\n" \
"v_mfma_f32_16x16x32_bf16 v[104:107], a[232:235], a[168:171], v[104:107]\n" \
"v_exp_f32 v45, v45\n" \
"v_mfma_f32_16x16x32_bf16 v[104:107], a[236:239], a[172:175], v[104:107]\n" \
"ds_read_b128 a[208:211], v233 offset:2048\n" \
"v_cvt_pk_bf16_f32 v38, v44, v45\n" \
"v_mfma_f32_16x16x32_bf16 v[120:123], a[236:239], a[188:191], v[196:199]\n" \
"v_exp_f32 v46, v46\n" \
"v_mfma_f32_16x16x32_bf16 v[120:123], a[232:235], a[184:187], v[120:123]\n" \
"v_mfma_f32_16x16x32_bf16 v[120:123], a[228:231], a[180:183], v[120:123]\n" \
"v_exp_f32 v47, v47\n" \
"v_mfma_f32_16x16x32_bf16 v[120:123], a[224:227], a[176:179], v[120:123]\n" \
"ds_read_b128 a[236:239], v233 offset:13312\n" \
"v_cvt_pk_bf16_f32 v39, v46, v47\n" \
"v_mfma_f32_16x16x32_bf16 v[124:127], a[240:243], a[176:179], v[196:199]\n" \
"v_exp_f32 v56, v56\n" \
"v_mfma_f32_16x16x32_bf16 v[124:127], a[244:247], a[180:183], v[124:127]\n" \
"ds_read_b128 a[232:235], v233 offset:5120\n" \
"v_mfma_f32_16x16x32_bf16 v[124:127], a[248:251], a[184:187], v[124:127]\n" \
"v_exp_f32 v57, v57\n" \
"v_mfma_f32_16x16x32_bf16 v[124:127], a[252:255], a[188:191], v[124:127]\n" \
"ds_read_b128 a[228:231], v233 offset:12288\n" \
"v_cvt_pk_bf16_f32 v52, v56, v57\n" \
"v_mfma_f32_16x16x32_bf16 v[108:111], a[252:255], a[172:175], v[196:199]\n" \
"v_exp_f32 v58, v58\n" \
"v_mfma_f32_16x16x32_bf16 v[108:111], a[248:251], a[168:171], v[108:111]\n" \
"ds_read_b128 a[224:227], v233 offset:4096\n" \
"v_mfma_f32_16x16x32_bf16 v[108:111], a[244:247], a[164:167], v[108:111]\n" \
"v_exp_f32 v59, v59\n" \
"v_mfma_f32_16x16x32_bf16 v[108:111], a[240:243], a[160:163], v[108:111]\n" \
"v_cvt_pk_bf16_f32 v53, v58, v59\n" \
"v_mfma_f32_16x16x32_bf16 v[92:95], a[240:243], a[16:19], v[196:199]\n" \
"v_exp_f32 v60, v60\n" \
"v_mfma_f32_16x16x32_bf16 v[92:95], a[244:247], a[20:23], v[92:95]\n" \
"v_mfma_f32_16x16x32_bf16 v[92:95], a[248:251], a[24:27], v[92:95]\n" \
"v_exp_f32 v61, v61\n" \
"v_mfma_f32_16x16x32_bf16 v[92:95], a[252:255], a[28:31], v[92:95]\n" \
"v_cvt_pk_bf16_f32 v54, v60, v61\n" \
"v_mfma_f32_16x16x32_bf16 v[76:79], a[252:255], a[12:15], v[196:199]\n" \
"v_exp_f32 v62, v62\n" \
"v_mfma_f32_16x16x32_bf16 v[76:79], a[248:251], a[8:11], v[76:79]\n" \
"s_cmp_lg_u32 s82, 0\n" \
"s_cbranch_scc1 Lnw_%=\n" \
"s_waitcnt vmcnt(0)\n" \
"Lnw_%=:\n" \
"v_mfma_f32_16x16x32_bf16 v[76:79], a[244:247], a[4:7], v[76:79]\n" \
"s_waitcnt lgkmcnt(0)\n" \
"v_exp_f32 v63, v63\n" \
"v_mfma_f32_16x16x32_bf16 v[76:79], a[240:243], a[0:3], v[76:79]\n" \
"s_barrier\n" \
"v_cvt_pk_bf16_f32 v55, v62, v63\n" \
"v_mfma_f32_16x16x32_bf16 v[128:131], a[192:195], v[0:3], v[128:131]\n" \
"s_add_u32 m0, s74, s79\n" \
"v_exp_f32 v64, v64\n" \
"v_mfma_f32_16x16x32_bf16 v[128:131], a[196:199], v[4:7], v[128:131]\n" \
"buffer_load_dwordx4 v234, s[36:39], s73 offen offset:0 sc1 lds\n" \
"v_mfma_f32_16x16x32_bf16 v[160:163], a[192:195], v[16:19], v[160:163]\n" \
"v_exp_f32 v65, v65\n" \
"v_mfma_f32_16x16x32_bf16 v[160:163], a[196:199], v[20:23], v[160:163]\n" \
"ds_read_b128 a[240:243], v232 offset:8448\n" \
"v_cvt_pk_bf16_f32 v64, v64, v65\n" \
"v_mfma_f32_16x16x32_bf16 a[64:67], a[192:195], v[32:35], a[64:67]\n" \
"s_add_u32 s71, s73, 17408\n" \
"v_exp_f32 v66, v66\n" \
"v_mfma_f32_16x16x32_bf16 a[64:67], a[196:199], v[36:39], a[64:67]\n" \
"buffer_load_dwordx4 v234, s[36:39], s73 offen offset:1024 sc1 lds\n" \
"v_mfma_f32_16x16x32_bf16 a[96:99], a[192:195], v[48:51], a[96:99]\n" \
"v_exp_f32 v67, v67\n" \
"v_mfma_f32_16x16x32_bf16 a[96:99], a[196:199], v[52:55], a[96:99]\n" \
"ds_read_b128 a[192:195], v232 offset:0\n" \
"v_cvt_pk_bf16_f32 v65, v66, v67\n" \
"v_mfma_f32_16x16x32_bf16 a[100:103], a[200:203], v[48:51], a[100:103]\n" \
"v_exp_f32 v68, v68\n" \
"v_mfma_f32_16x16x32_bf16 a[100:103], a[204:207], v[52:55], a[100:103]\n" \
"buffer_load_dwordx4 v234, s[36:39], s73 offen offset:2048 sc1 lds\n" \
"v_mfma_f32_16x16x32_bf16 a[68:71], a[200:203], v[32:35], a[68:71]\n" \
"v_exp_f32 v69, v69\n" \
"v_mfma_f32_16x16x32_bf16 a[68:71], a[204:207], v[36:39], a[68:71]\n" \
"ds_read_b128 a[196:199], v232 offset:2048\n" \
"v_cvt_pk_bf16_f32 v66, v68, v69\n" \
"v_smfmac_f32_16x16x64_bf16 a[48:51], v[244:247], v[0:7], v243\n" \
"buffer_load_dwordx4 v234, s[36:39], s73 offen offset:3072 sc1 lds\n" \
"v_mfma_f32_16x16x32_bf16 v[164:167], a[200:203], v[16:19], v[164:167]\n" \
"s_add_u32 m0, m0, 0x4000\n" \
"v_exp_f32 v70, v70\n" \
"v_mfma_f32_16x16x32_bf16 v[164:167], a[204:207], v[20:23], v[164:167]\n" \
"buffer_load_dwordx4 v234, s[36:39], s71 offen offset:0 sc1 lds\n" \
"v_mfma_f32_16x16x32_bf16 v[132:135], a[200:203], v[0:3], v[132:135]\n" \
"v_exp_f32 v71, v71\n" \
"v_mfma_f32_16x16x32_bf16 v[132:135], a[204:207], v[4:7], v[132:135]\n" \
"ds_read_b128 a[200:203], v232 offset:4096\n" \
"v_cvt_pk_bf16_f32 v67, v70, v71\n" \
"v_mfma_f32_16x16x32_bf16 v[136:139], a[208:211], v[0:3], v[136:139]\n" \
"v_exp_f32 v80, v80\n" \
"v_mfma_f32_16x16x32_bf16 v[136:139], a[212:215], v[4:7], v[136:139]\n" \
"buffer_load_dwordx4 v234, s[36:39], s71 offen offset:1024 sc1 lds\n" \
"v_mfma_f32_16x16x32_bf16 v[168:171], a[208:211], v[16:19], v[168:171]\n" \
"v_exp_f32 v81, v81\n" \
"v_mfma_f32_16x16x32_bf16 v[168:171], a[212:215], v[20:23], v[168:171]\n" \
"ds_read_b128 a[204:207], v232 offset:6144\n" \
"v_cvt_pk_bf16_f32 v80, v80, v81\n" \
"v_mfma_f32_16x16x32_bf16 a[72:75], a[208:211], v[32:35], a[72:75]\n" \
"v_exp_f32 v82, v82\n" \
"v_mfma_f32_16x16x32_bf16 a[72:75], a[212:215], v[36:39], a[72:75]\n" \
"buffer_load_dwordx4 v234, s[36:39], s71 offen offset:2048 sc1 lds\n" \
"v_mfma_f32_16x16x32_bf16 a[104:107], a[208:211], v[48:51], a[104:107]\n" \
"v_exp_f32 v83, v83\n" \
"v_mfma_f32_16x16x32_bf16 a[104:107], a[212:215], v[52:55], a[104:107]\n" \
"ds_read_b128 a[208:211], v232 offset:256\n" \
"v_cvt_pk_bf16_f32 v81, v82, v83\n" \
"v_mfma_f32_16x16x32_bf16 a[108:111], a[216:219], v[48:51], a[108:111]\n" \
"v_exp_f32 v84, v84\n" \
"v_mfma_f32_16x16x32_bf16 a[108:111], a[220:223], v[52:55], a[108:111]\n" \
"buffer_load_dwordx4 v234, s[36:39], s71 offen offset:3072 sc1 lds\n" \
"v_mfma_f32_16x16x32_bf16 a[76:79], a[216:219], v[32:35], a[76:79]\n" \
"v_exp_f32 v85, v85\n" \
"v_mfma_f32_16x16x32_bf16 a[76:79], a[220:223], v[36:39], a[76:79]\n" \
"s_add_u32 m0, s77, s80\n" \
"ds_read_b128 a[212:215], v232 offset:2304\n" \
"v_cvt_pk_bf16_f32 v82, v84, v85\n" \
"v_smfmac_f32_16x16x64_bf16 a[52:55], v[244:247], v[16:23], v243\n" \
"buffer_load_dwordx4 v234, s[40:43], s76 offen offset:0 sc1 lds\n" \
"v_mfma_f32_16x16x32_bf16 v[172:175], a[216:219], v[16:19], v[172:175]\n" \
"s_add_u32 s71, s76, 17408\n" \
"v_exp_f32 v86, v86\n" \
"v_mfma_f32_16x16x32_bf16 v[172:175], a[220:223], v[20:23], v[172:175]\n" \
"buffer_load_dwordx4 v234, s[40:43], s76 offen offset:1024 sc1 lds\n" \
"v_mfma_f32_16x16x32_bf16 v[140:143], a[216:219], v[0:3], v[140:143]\n" \
"v_exp_f32 v87, v87\n" \
"v_mfma_f32_16x16x32_bf16 v[140:143], a[220:223], v[4:7], v[140:143]\n" \
"ds_read_b128 a[216:219], v232 offset:4352\n" \
"v_cvt_pk_bf16_f32 v83, v86, v87\n" \
"v_mfma_f32_16x16x32_bf16 v[144:147], a[224:227], v[0:3], v[144:147]\n" \
"v_exp_f32 v96, v96\n" \
"v_mfma_f32_16x16x32_bf16 v[144:147], a[228:231], v[4:7], v[144:147]\n" \
"buffer_load_dwordx4 v234, s[40:43], s76 offen offset:2048 sc1 lds\n" \
"v_mfma_f32_16x16x32_bf16 v[176:179], a[224:227], v[16:19], v[176:179]\n" \
"v_exp_f32 v97, v97\n" \
"v_mfma_f32_16x16x32_bf16 v[176:179], a[228:231], v[20:23], v[176:179]\n" \
"ds_read_b128 a[220:223], v232 offset:6400\n" \
"v_cvt_pk_bf16_f32 v96, v96, v97\n" \
"v_mfma_f32_16x16x32_bf16 a[80:83], a[224:227], v[32:35], a[80:83]\n" \
"v_exp_f32 v98, v98\n" \
"v_mfma_f32_16x16x32_bf16 a[80:83], a[228:231], v[36:39], a[80:83]\n" \
"buffer_load_dwordx4 v234, s[40:43], s76 offen offset:3072 sc1 lds\n" \
"v_mfma_f32_16x16x32_bf16 a[112:115], a[224:227], v[48:51], a[112:115]\n" \
"v_exp_f32 v99, v99\n" \
"v_mfma_f32_16x16x32_bf16 a[112:115], a[228:231], v[52:55], a[112:115]\n" \
"ds_read_b128 a[224:227], v232 offset:8192\n" \
"v_cvt_pk_bf16_f32 v97, v98, v99\n" \
"v_mfma_f32_16x16x32_bf16 a[116:119], a[232:235], v[48:51], a[116:119]\n" \
"s_add_u32 m0, m0, 0x4000\n" \
"v_exp_f32 v100, v100\n" \
"v_mfma_f32_16x16x32_bf16 a[116:119], a[236:239], v[52:55], a[116:119]\n" \
"buffer_load_dwordx4 v234, s[40:43], s71 offen offset:0 sc1 lds\n" \
"v_mfma_f32_16x16x32_bf16 a[84:87], a[232:235], v[32:35], a[84:87]\n" \
"v_exp_f32 v101, v101\n" \
"v_mfma_f32_16x16x32_bf16 a[84:87], a[236:239], v[36:39], a[84:87]\n" \
"ds_read_b128 a[228:231], v232 offset:10240\n" \
"v_cvt_pk_bf16_f32 v98, v100, v101\n" \
"v_smfmac_f32_16x16x64_bf16 v[224:227], v[244:247], v[32:39], v243\n" \
"buffer_load_dwordx4 v234, s[40:43], s71 offen offset:1024 sc1 lds\n" \
"v_mfma_f32_16x16x32_bf16 v[180:183], a[232:235], v[16:19], v[180:183]\n" \
"v_exp_f32 v102, v102\n" \
"v_mfma_f32_16x16x32_bf16 v[180:183], a[236:239], v[20:23], v[180:183]\n" \
"buffer_load_dwordx4 v234, s[40:43], s71 offen offset:2048 sc1 lds\n" \
"v_mfma_f32_16x16x32_bf16 v[148:151], a[232:235], v[0:3], v[148:151]\n" \
"v_exp_f32 v103, v103\n" \
"v_mfma_f32_16x16x32_bf16 v[148:151], a[236:239], v[4:7], v[148:151]\n" \
"ds_read_b128 a[232:235], v232 offset:12288\n" \
"v_cvt_pk_bf16_f32 v99, v102, v103\n" \
"v_mfma_f32_16x16x32_bf16 v[152:155], a[32:35], v[0:3], v[152:155]\n" \
"v_exp_f32 v112, v112\n" \
"v_mfma_f32_16x16x32_bf16 v[152:155], a[36:39], v[4:7], v[152:155]\n" \
"buffer_load_dwordx4 v234, s[40:43], s71 offen offset:3072 sc1 lds\n" \
"v_mfma_f32_16x16x32_bf16 v[184:187], a[32:35], v[16:19], v[184:187]\n" \
"v_exp_f32 v113, v113\n" \
"v_mfma_f32_16x16x32_bf16 v[184:187], a[36:39], v[20:23], v[184:187]\n" \
"ds_read_b128 a[236:239], v232 offset:14336\n" \
"v_cvt_pk_bf16_f32 v112, v112, v113\n" \
"v_mfma_f32_16x16x32_bf16 a[88:91], a[32:35], v[32:35], a[88:91]\n" \
"v_exp_f32 v114, v114\n" \
"v_mfma_f32_16x16x32_bf16 a[88:91], a[36:39], v[36:39], a[88:91]\n" \
"buffer_load_dword v239, v242, s[56:59], s95 offen sc1\n" \
"v_mfma_f32_16x16x32_bf16 a[120:123], a[32:35], v[48:51], a[120:123]\n" \
"s_mov_b32 s69, s92\n" \
"v_exp_f32 v115, v115\n" \
"v_mfma_f32_16x16x32_bf16 a[120:123], a[36:39], v[52:55], a[120:123]\n" \
"s_mov_b32 s76, s73\n" \
"ds_read_b128 a[244:247], v232 offset:10496\n" \
"v_cvt_pk_bf16_f32 v113, v114, v115\n" \
"v_mfma_f32_16x16x32_bf16 a[124:127], a[40:43], v[48:51], a[124:127]\n" \
"s_add_u32 s73, s73, 34816\n" \
"v_exp_f32 v116, v116\n" \
"v_mfma_f32_16x16x32_bf16 a[124:127], a[44:47], v[52:55], a[124:127]\n" \
"s_xor_b32 s74, s74, 0x8000\n" \
"s_xor_b32 s77, s77, 0x8000\n" \
"v_xor_b32 v233, 0x8000, v233\n" \
"v_mfma_f32_16x16x32_bf16 a[92:95], a[40:43], v[32:35], a[92:95]\n" \
"s_mov_b32 s82, 0\n" \
"v_exp_f32 v117, v117\n" \
"v_mfma_f32_16x16x32_bf16 a[92:95], a[44:47], v[36:39], a[92:95]\n" \
"ds_read_b128 a[248:251], v232 offset:12544\n" \
"v_cvt_pk_bf16_f32 v114, v116, v117\n" \
"Lpg6787_%=:\n" \
"s_cbranch_execz Lcb0_%=\n" \
"v_smfmac_f32_16x16x64_bf16 v[228:231], v[244:247], v[48:55], v243\n" \
"v_mfma_f32_16x16x32_bf16 v[188:191], a[40:43], v[16:19], v[188:191]\n" \
"v_exp_f32_e64 v118, v118\n" \
"v_mfma_f32_16x16x32_bf16 v[188:191], a[44:47], v[20:23], v[188:191]\n" \
"s_cmp_lg_u32 s84, 0\n" \
"v_exp_f32_e64 v119, v119\n" \
"v_mfma_f32_16x16x32_bf16 v[156:159], a[40:43], v[0:3], v[156:159]\n" \
"s_waitcnt lgkmcnt(2)\n" \
"ds_read_b128 a[252:255], v232 offset:14592\n" \
"v_cvt_pk_bf16_f32 v115, v118, v119\n" \
"v_mfma_f32_16x16x32_bf16 v[156:159], a[44:47], v[4:7], v[156:159]\n" \
"s_cbranch_scc1 Lloop2_%=\n" \
"Lg_%=:\n" \
"Lpg6801_%=:\n" \
"s_cbranch_execz Lcb0_%=\n" \
"v_mfma_f32_16x16x32_bf16 v[32:35], a[192:195], a[160:163], v[208:211]\n" \
"s_mov_b64 s[44:45], s[20:21]\n" \
"s_memrealtime s[14:15]\n" \
"v_exp_f32 v72, v72\n" \
"v_mfma_f32_16x16x32_bf16 v[32:35], a[196:199], a[164:167], v[32:35]\n" \
"s_and_b32 s45, s45, 0xffff\n" \
"ds_read_b128 a[32:35], v233 offset:6144\n" \
"ds_read_b128 a[36:39], v233 offset:14336\n" \
"v_mfma_f32_16x16x32_bf16 v[32:35], a[200:203], a[168:171], v[32:35]\n" \
"s_mov_b32 s46, 4194304\n" \
"v_exp_f32 v73, v73\n" \
"v_mfma_f32_16x16x32_bf16 v[32:35], a[204:207], a[172:175], v[32:35]\n" \
"ds_read_b128 a[40:43], v233 offset:7168\n" \
"ds_read_b128 a[44:47], v233 offset:15360\n" \
"global_load_dword v253, v254, s[22:23]\n" \
"v_mfma_f32_16x16x32_bf16 v[48:51], a[204:207], a[188:191], v[196:199]\n" \
"s_mov_b32 s47, 0x20000\n" \
"v_exp_f32 v74, v74\n" \
"v_mfma_f32_16x16x32_bf16 v[48:51], a[200:203], a[184:187], v[48:51]\n" \
"s_mov_b64 s[48:49], s[22:23]\n" \
"v_exp_f32 v75, v75\n" \
"v_mfma_f32_16x16x32_bf16 v[48:51], a[196:199], a[180:183], v[48:51]\n" \
"s_and_b32 s49, s49, 0xffff\n" \
"v_exp_f32 v76, v76\n" \
"v_mfma_f32_16x16x32_bf16 v[48:51], a[192:195], a[176:179], v[48:51]\n" \
"s_mov_b32 s50, 6291456\n" \
"ds_read_b128 a[200:203], v233 offset:1024\n" \
"ds_read_b128 a[204:207], v233 offset:9216\n" \
"v_mfma_f32_16x16x32_bf16 v[52:55], a[208:211], a[176:179], v[196:199]\n" \
"s_mov_b32 s51, 0x20000\n" \
"v_cvt_pk_bf16_f32 v68, v72, v73\n" \
"v_cvt_pk_bf16_f32 v69, v74, v75\n" \
"v_mfma_f32_16x16x32_bf16 v[52:55], a[212:215], a[180:183], v[52:55]\n" \
"s_bfe_u32 s94, %1, 0x20001\n" \
"v_exp_f32 v77, v77\n" \
"v_mfma_f32_16x16x32_bf16 v[52:55], a[216:219], a[184:187], v[52:55]\n" \
"s_lshl_b32 s94, s94, 2\n" \
"v_exp_f32 v78, v78\n" \
"v_mfma_f32_16x16x32_bf16 v[52:55], a[220:223], a[188:191], v[52:55]\n" \
"s_lshr_b32 s70, %1, 6\n" \
"ds_read_b128 a[192:195], v233 offset:0\n" \
"ds_read_b128 a[196:199], v233 offset:8192\n" \
"v_mfma_f32_16x16x32_bf16 v[36:39], a[220:223], a[172:175], v[212:215]\n" \
"s_add_u32 s94, s94, s70\n" \
"v_exp_f32 v79, v79\n" \
"v_mfma_f32_16x16x32_bf16 v[36:39], a[216:219], a[168:171], v[36:39]\n" \
"s_lshl_b32 s73, s94, 18\n" \
"v_exp_f32 v88, v88\n" \
"v_mfma_f32_16x16x32_bf16 v[36:39], a[212:215], a[164:167], v[36:39]\n" \
"s_max_u32 s75, s73, 32768\n" \
"v_cvt_pk_bf16_f32 v70, v76, v77\n" \
"v_cvt_pk_bf16_f32 v71, v78, v79\n" \
"v_mfma_f32_16x16x32_bf16 v[36:39], a[208:211], a[160:163], v[36:39]\n" \
"s_sub_u32 s75, s75, 32768\n" \
"ds_read_b128 a[216:219], v233 offset:3072\n" \
"ds_read_b128 a[220:223], v233 offset:11264\n" \
"v_mfma_f32_16x16x32_bf16 v[40:43], a[224:227], a[160:163], v[216:219]\n" \
"s_and_b32 s70, s61, 1\n" \
"v_exp_f32 v89, v89\n" \
"v_mfma_f32_16x16x32_bf16 v[40:43], a[228:231], a[164:167], v[40:43]\n" \
"s_lshr_b32 s71, s61, 1\n" \
"v_exp_f32 v90, v90\n" \
"v_mfma_f32_16x16x32_bf16 v[40:43], a[232:235], a[168:171], v[40:43]\n" \
"s_lshl1_add_u32 s70, s70, s71\n" \
"v_exp_f32 v91, v91\n" \
"v_mfma_f32_16x16x32_bf16 v[40:43], a[236:239], a[172:175], v[40:43]\n" \
"s_lshl_b32 s70, s70, 10\n" \
"ds_read_b128 a[208:211], v233 offset:2048\n" \
"ds_read_b128 a[212:215], v233 offset:10240\n" \
"v_mfma_f32_16x16x32_bf16 v[56:59], a[236:239], a[188:191], v[196:199]\n" \
"s_add_u32 s75, s75, s70\n" \
"v_exp_f32 v92, v92\n" \
"v_mfma_f32_16x16x32_bf16 v[56:59], a[232:235], a[184:187], v[56:59]\n" \
"s_lshl_b32 s89, s61, 13\n" \
"v_cvt_pk_bf16_f32 v84, v88, v89\n" \
"v_cvt_pk_bf16_f32 v85, v90, v91\n" \
"v_mfma_f32_16x16x32_bf16 v[56:59], a[228:231], a[180:183], v[56:59]\n" \
"s_add_u32 s89, s89, %3\n" \
"v_exp_f32 v93, v93\n" \
"v_mfma_f32_16x16x32_bf16 v[56:59], a[224:227], a[176:179], v[56:59]\n" \
"s_lshr_b32 s71, s22, 7\n" \
"ds_read_b128 a[232:235], v233 offset:5120\n" \
"ds_read_b128 a[236:239], v233 offset:13312\n" \
"v_mfma_f32_16x16x32_bf16 v[60:63], a[240:243], a[176:179], v[196:199]\n" \
"s_waitcnt lgkmcnt(2)\n" \
"s_sub_u32 s71, s63, s71\n" \
"v_exp_f32 v94, v94\n" \
"v_mfma_f32_16x16x32_bf16 v[60:63], a[244:247], a[180:183], v[60:63]\n" \
"s_add_u32 s71, s71, 6\n" \
"v_exp_f32 v95, v95\n" \
"v_mfma_f32_16x16x32_bf16 v[60:63], a[248:251], a[184:187], v[60:63]\n" \
"s_and_b32 s71, s71, 7\n" \
"v_cvt_pk_bf16_f32 v86, v92, v93\n" \
"v_accvgpr_read_b32 v192, a57\n" \
"v_mfma_f32_16x16x32_bf16 v[60:63], a[252:255], a[188:191], v[60:63]\n" \
"s_and_b32 s70, %1, 1\n" \
"ds_read_b128 a[224:227], v233 offset:4096\n" \
"ds_read_b128 a[228:231], v233 offset:12288\n" \
"v_mfma_f32_16x16x32_bf16 v[44:47], a[252:255], a[172:175], v[220:223]\n" \
"s_lshl_b32 s70, s70, 3\n" \
"v_cvt_pk_bf16_f32 v87, v94, v95\n" \
"v_mfma_f32_16x16x32_bf16 v[44:47], a[248:251], a[168:171], v[44:47]\n" \
"s_add_u32 s70, s70, s71\n" \
"v_exp_f32 v104, v104\n" \
"v_mfma_f32_16x16x32_bf16 v[44:47], a[244:247], a[164:167], v[44:47]\n" \
"s_lshl_b32 s78, s70, 7\n" \
"v_accvgpr_read_b32 v197, a56\n" \
"v_add_u32 v198, 1024, v197\n" \
"v_mfma_f32_16x16x32_bf16 v[44:47], a[240:243], a[160:163], v[44:47]\n" \
"s_lshr_b32 s80, %1, 6\n" \
"v_add_u32 v199, 2048, v197\n" \
"v_add_u32 v193, 31744, v192\n" \
"v_mfma_f32_16x16x32_bf16 v[128:131], a[192:195], v[64:67], v[128:131]\n" \
"s_xor_b32 s80, s80, s63\n" \
"v_exp_f32 v105, v105\n" \
"v_mfma_f32_16x16x32_bf16 v[128:131], a[196:199], v[68:71], v[128:131]\n" \
"s_and_b32 s80, s80, 0\n" \
"ds_read_b128 a[0:3], v232 offset:16384\n" \
"ds_read_b128 a[4:7], v232 offset:18432\n" \
"v_mfma_f32_16x16x32_bf16 v[160:163], a[192:195], v[80:83], v[160:163]\n" \
"s_add_u32 m0, s89, 131072\n" \
"v_exp_f32 v106, v106\n" \
"v_mfma_f32_16x16x32_bf16 v[160:163], a[196:199], v[84:87], v[160:163]\n" \
"buffer_load_dwordx4 v192, s[44:47], s75 offen offset:0 lds\n" \
"v_exp_f32 v107, v107\n" \
"v_mfma_f32_16x16x32_bf16 v[164:167], a[200:203], v[80:83], v[164:167]\n" \
"s_lshl_b32 s93, s70, 12\n" \
"ds_read_b128 a[8:11], v232 offset:20480\n" \
"ds_read_b128 a[12:15], v232 offset:22528\n" \
"v_mfma_f32_16x16x32_bf16 v[164:167], a[204:207], v[84:87], v[164:167]\n" \
"s_add_u32 m0, s89, 131968\n" \
"v_exp_f32 v108, v108\n" \
"v_mfma_f32_16x16x32_bf16 v[132:135], a[200:203], v[64:67], v[132:135]\n" \
"buffer_load_dwordx4 v192, s[44:47], s75 offen offset:128 lds\n" \
"v_mfma_f32_16x16x32_bf16 v[132:135], a[204:207], v[68:71], v[132:135]\n" \
"s_mul_i32 s91, s61, 6144\n" \
"ds_read_b128 a[16:19], v232 offset:16640\n" \
"ds_read_b128 a[20:23], v232 offset:18688\n" \
"v_cvt_pk_bf16_f32 v100, v104, v105\n" \
"v_smfmac_f32_16x16x64_bf16 a[48:51], v[244:247], v[64:71], v243\n" \
"s_add_u32 m0, s89, 132864\n" \
"v_cvt_pk_bf16_f32 v101, v106, v107\n" \
"v_mfma_f32_16x16x32_bf16 v[136:139], a[208:211], v[64:67], v[136:139]\n" \
"buffer_load_dwordx4 v192, s[44:47], s75 offen offset:256 lds\n" \
"v_mfma_f32_16x16x32_bf16 v[136:139], a[212:215], v[68:71], v[136:139]\n" \
"s_add_u32 s91, s91, %3\n" \
"ds_read_b128 a[24:27], v232 offset:20736\n" \
"ds_read_b128 a[28:31], v232 offset:22784\n" \
"v_mfma_f32_16x16x32_bf16 v[168:171], a[208:211], v[80:83], v[168:171]\n" \
"s_add_u32 m0, s89, 133760\n" \
"v_exp_f32 v109, v109\n" \
"v_mfma_f32_16x16x32_bf16 v[168:171], a[212:215], v[84:87], v[168:171]\n" \
"buffer_load_dwordx4 v192, s[44:47], s75 offen offset:384 lds\n" \
"v_mfma_f32_16x16x32_bf16 v[172:175], a[216:219], v[80:83], v[172:175]\n" \
"s_lshl_b32 s71, s80, 7\n" \
"ds_read_b128 v[0:3], v232 offset:24576\n" \
"ds_read_b128 v[4:7], v232 offset:26624\n" \
"v_mfma_f32_16x16x32_bf16 v[172:175], a[220:223], v[84:87], v[172:175]\n" \
"s_add_u32 m0, s89, 134656\n" \
"v_exp_f32 v110, v110\n" \
"v_mfma_f32_16x16x32_bf16 v[140:143], a[216:219], v[64:67], v[140:143]\n" \
"buffer_load_dwordx4 v192, s[44:47], s75 offen offset:512 lds\n" \
"v_mfma_f32_16x16x32_bf16 v[140:143], a[220:223], v[68:71], v[140:143]\n" \
"s_lshl_b32 s70, s61, 16\n" \
"ds_read_b128 v[8:11], v232 offset:28672\n" \
"ds_read_b128 v[12:15], v232 offset:30720\n" \
"v_exp_f32 v111, v111\n" \
"v_smfmac_f32_16x16x64_bf16 a[52:55], v[244:247], v[80:87], v243\n" \
"s_waitcnt lgkmcnt(4)\n" \
"s_add_u32 m0, s89, 135552\n" \
"v_cvt_pk_bf16_f32 v102, v108, v109\n" \
"v_mfma_f32_16x16x32_bf16 v[144:147], a[224:227], v[64:67], v[144:147]\n" \
"buffer_load_dwordx4 v192, s[44:47], s75 offen offset:640 lds\n" \
"v_mfma_f32_16x16x32_bf16 v[144:147], a[228:231], v[68:71], v[144:147]\n" \
"s_add_u32 s73, s73, s70\n" \
"ds_read_b128 v[200:203], v232 offset:24832\n" \
"ds_read_b128 v[204:207], v232 offset:26880\n" \
"v_mfma_f32_16x16x32_bf16 v[176:179], a[224:227], v[80:83], v[176:179]\n" \
"s_add_u32 m0, s89, 136448\n" \
"v_exp_f32 v120, v120\n" \
"v_mfma_f32_16x16x32_bf16 v[176:179], a[228:231], v[84:87], v[176:179]\n" \
"buffer_load_dwordx4 v192, s[44:47], s75 offen offset:768 lds\n" \
"v_mfma_f32_16x16x32_bf16 v[180:183], a[232:235], v[80:83], v[180:183]\n" \
"s_add_u32 s73, s73, s71\n" \
"ds_read_b128 a[240:243], v232 offset:28928\n" \
"ds_read_b128 a[244:247], v232 offset:30976\n" \
"v_mfma_f32_16x16x32_bf16 v[180:183], a[236:239], v[84:87], v[180:183]\n" \
"s_add_u32 m0, s89, 137344\n" \
"v_exp_f32 v121, v121\n" \
"v_exp_f32 v122, v122\n" \
"v_mfma_f32_16x16x32_bf16 v[148:151], a[232:235], v[64:67], v[148:151]\n" \
"buffer_load_dwordx4 v192, s[44:47], s75 offen offset:896 lds\n" \
"v_mfma_f32_16x16x32_bf16 v[148:151], a[236:239], v[68:71], v[148:151]\n" \
"s_and_b32 s70, s61, 1\n" \
"v_cvt_pk_bf16_f32 v103, v110, v111\n" \
"v_cvt_pk_bf16_f32 v116, v120, v121\n" \
"v_mfma_f32_16x16x32_bf16 v[152:155], a[32:35], v[64:67], v[152:155]\n" \
"s_lshl_b32 s70, s70, 11\n" \
"v_add_u32 v233, 0x10000, v235\n" \
"s_lshl_b32 s72, s61, 13\n" \
"v_mfma_f32_16x16x32_bf16 v[152:155], a[36:39], v[68:71], v[152:155]\n" \
"s_add_u32 s93, s93, s70\n" \
"v_add_u32 v232, s72, v235\n" \
"v_exp_f32 v123, v123\n" \
"v_mfma_f32_16x16x32_bf16 v[184:187], a[32:35], v[80:83], v[184:187]\n" \
"s_add_u32 s93, s93, 52740096\n" \
"s_cmp_eq_u32 s94, 0\n" \
"s_cselect_b32 s92, -1, 0\n" \
"s_mul_i32 s70, s94, 278528\n" \
"s_add_u32 s93, s93, s70\n" \
"s_lshr_b32 s70, s61, 1\n" \
"s_lshl_b32 s70, s70, 8\n" \
"s_add_u32 s93, s93, s70\n" \
"v_mfma_f32_16x16x32_bf16 v[184:187], a[36:39], v[84:87], v[184:187]\n" \
"v_exp_f32 v124, v124\n" \
"v_mfma_f32_16x16x32_bf16 v[188:191], a[40:43], v[80:83], v[188:191]\n" \
"v_exp_f32 v125, v125\n" \
"v_mfma_f32_16x16x32_bf16 v[188:191], a[44:47], v[84:87], v[188:191]\n" \
"v_exp_f32 v126, v126\n" \
"v_cvt_pk_bf16_f32 v117, v122, v123\n" \
"v_mfma_f32_16x16x32_bf16 v[156:159], a[40:43], v[64:67], v[156:159]\n" \
"s_mul_i32 s76, s80, 393216\n" \
"v_exp_f32 v127, v127\n" \
"v_mfma_f32_16x16x32_bf16 v[156:159], a[44:47], v[68:71], v[156:159]\n" \
"s_add_u32 s76, s76, s78\n" \
"s_cmp_eq_u32 s63, 7\n" \
"s_waitcnt vmcnt(9) lgkmcnt(0)\n" \
"s_barrier\n" \
"s_cbranch_scc1 Lep_%=\n" \
"Lepg_%=:\n" \
"v_mfma_f32_16x16x32_bf16 v[16:19], a[0:3], a[176:179], v[208:211]\n" \
"s_add_u32 m0, s91, 106496\n" \
"ds_read_b128 v[64:67], v233 offset:16384\n" \
"ds_read_b128 v[68:71], v233 offset:24576\n" \
"v_mfma_f32_16x16x32_bf16 v[16:19], a[4:7], a[180:183], v[16:19]\n" \
"buffer_load_dwordx4 v197, s[48:51], s76 offen offset:0 lds\n" \
"v_mfma_f32_16x16x32_bf16 v[16:19], a[8:11], a[184:187], v[16:19]\n" \
"ds_read_b128 v[72:75], v233 offset:17408\n" \
"ds_read_b128 v[76:79], v233 offset:25600\n" \
"v_mfma_f32_16x16x32_bf16 v[16:19], a[12:15], a[188:191], v[16:19]\n" \
"ds_read_b128 v[80:83], v233 offset:18432\n" \
"ds_read_b128 v[84:87], v233 offset:26624\n" \
"v_cvt_pk_bf16_f32 v128, v128, v129\n" \
"v_cvt_pk_bf16_f32 v129, v130, v131\n" \
"v_cvt_pk_bf16_f32 v130, v132, v133\n" \
"v_cvt_pk_bf16_f32 v118, v124, v125\n" \
"v_mfma_f32_16x16x32_bf16 v[20:23], a[28:31], a[188:191], v[212:215]\n" \
"s_add_u32 s77, s76, 24576\n" \
"ds_read_b128 v[88:91], v233 offset:19456\n" \
"ds_read_b128 v[92:95], v233 offset:27648\n" \
"v_mfma_f32_16x16x32_bf16 v[20:23], a[24:27], a[184:187], v[20:23]\n" \
"buffer_load_dwordx4 v198, s[48:51], s76 offen offset:1024 lds\n" \
"v_cvt_pk_bf16_f32 v131, v134, v135\n" \
"v_cvt_pk_bf16_f32 v119, v126, v127\n" \
"v_exp_f32 v32, v32\n" \
"v_xor_b32 v248, 8192, v232\n" \
"v_mfma_f32_16x16x32_bf16 v[20:23], a[20:23], a[180:183], v[20:23]\n" \
"ds_write_b128 v232, v[128:131] offset:0\n" \
"v_exp_f32 v33, v33\n" \
"v_mfma_f32_16x16x32_bf16 v[20:23], a[16:19], a[176:179], v[20:23]\n" \
"ds_read_b128 a[248:251], v233 offset:7168\n" \
"ds_read_b128 a[252:255], v233 offset:15360\n" \
"v_exp_f32 v34, v34\n" \
"v_mfma_f32_16x16x32_bf16 v[24:27], v[0:3], a[176:179], v[216:219]\n" \
"s_xor_b32 s80, s80, 0x3\n" \
"v_cvt_pk_bf16_f32 v132, v136, v137\n" \
"v_cvt_pk_bf16_f32 v133, v138, v139\n" \
"v_mfma_f32_16x16x32_bf16 v[24:27], v[4:7], a[180:183], v[24:27]\n" \
"buffer_load_dwordx4 v199, s[48:51], s76 offen offset:2048 lds\n" \
"v_exp_f32 v35, v35\n" \
"v_lshrrev_b32 v249, 1, v234\n" \
"v_mfma_f32_16x16x32_bf16 v[24:27], v[8:11], a[184:187], v[24:27]\n" \
"s_lshl_b32 s72, s61, 9\n" \
"v_cvt_pk_bf16_f32 v134, v140, v141\n" \
"v_cvt_pk_bf16_f32 v135, v142, v143\n" \
"v_mfma_f32_16x16x32_bf16 v[24:27], v[12:15], a[188:191], v[24:27]\n" \
"s_add_u32 s72, s72, 57344\n" \
"s_add_u32 s72, s72, %3\n" \
"v_cvt_pk_bf16_f32 v136, v144, v145\n" \
"v_cvt_pk_bf16_f32 v137, v146, v147\n" \
"v_exp_f32 v36, v36\n" \
"v_cvt_pk_bf16_f32 v32, v32, v33\n" \
"v_mfma_f32_16x16x32_bf16 v[28:31], a[244:247], a[188:191], v[220:223]\n" \
"s_add_u32 m0, m0, 3072\n" \
"v_cvt_pk_bf16_f32 v138, v148, v149\n" \
"v_cvt_pk_bf16_f32 v139, v150, v151\n" \
"v_mfma_f32_16x16x32_bf16 v[28:31], a[240:243], a[184:187], v[28:31]\n" \
"buffer_load_dwordx4 v197, s[48:51], s77 offen offset:0 lds\n" \
"v_exp_f32 v37, v37\n" \
"v_mfma_f32_16x16x32_bf16 v[28:31], v[204:207], a[180:183], v[28:31]\n" \
"ds_write_b128 v232, v[132:135] offset:1024\n" \
"v_exp_f32 v38, v38\n" \
"v_cvt_pk_bf16_f32 v33, v34, v35\n" \
"v_add_u32 v249, s72, v249\n" \
"v_mfma_f32_16x16x32_bf16 v[28:31], v[200:203], a[176:179], v[28:31]\n" \
"v_cvt_pk_bf16_f32 v140, v152, v153\n" \
"v_cvt_pk_bf16_f32 v141, v154, v155\n" \
"v_exp_f32 v39, v39\n" \
"v_mfma_f32_16x16x32_bf16 a[64:67], a[192:195], v[96:99], a[64:67]\n" \
"v_cvt_pk_bf16_f32 v142, v156, v157\n" \
"v_cvt_pk_bf16_f32 v143, v158, v159\n" \
"v_mfma_f32_16x16x32_bf16 a[64:67], a[196:199], v[100:103], a[64:67]\n" \
"buffer_load_dwordx4 v198, s[48:51], s77 offen offset:1024 lds\n" \
"v_exp_f32 v48, v48\n" \
"v_cvt_pk_bf16_f32 v34, v36, v37\n" \
"v_mfma_f32_16x16x32_bf16 a[96:99], a[192:195], v[112:115], a[96:99]\n" \
"v_cvt_pk_bf16_f32 v144, v160, v161\n" \
"v_cvt_pk_bf16_f32 v145, v162, v163\n" \
"v_mfma_f32_16x16x32_bf16 a[96:99], a[196:199], v[116:119], a[96:99]\n" \
"ds_write_b128 v232, v[136:139] offset:2048\n" \
"v_exp_f32 v49, v49\n" \
"v_exp_f32 v50, v50\n" \
"v_cvt_pk_bf16_f32 v35, v38, v39\n" \
"v_accvgpr_read_b32 v250, a48\n" \
"v_mfma_f32_16x16x32_bf16 a[100:103], a[200:203], v[112:115], a[100:103]\n" \
"ds_read_b128 a[192:195], v233 offset:0\n" \
"ds_read_b128 a[196:199], v233 offset:8192\n" \
"v_mfma_f32_16x16x32_bf16 a[100:103], a[204:207], v[116:119], a[100:103]\n" \
"buffer_load_dwordx4 v199, s[48:51], s77 offen offset:2048 lds\n" \
"v_exp_f32 v51, v51\n" \
"v_exp_f32 v52, v52\n" \
"v_mfma_f32_16x16x32_bf16 a[68:71], a[200:203], v[96:99], a[68:71]\n" \
"v_cvt_pk_bf16_f32 v146, v164, v165\n" \
"v_cvt_pk_bf16_f32 v147, v166, v167\n" \
"v_mfma_f32_16x16x32_bf16 a[68:71], a[204:207], v[100:103], a[68:71]\n" \
"ds_read_b128 a[200:203], v233 offset:1024\n" \
"ds_read_b128 a[204:207], v233 offset:9216\n" \
"v_cvt_pk_bf16_f32 v48, v48, v49\n" \
"v_exp_f32 v53, v53\n" \
"v_smfmac_f32_16x16x64_bf16 v[224:227], v[244:247], v[96:103], v243\n" \
"s_add_u32 m0, s79, 40960\n" \
"v_cvt_pk_bf16_f32 v148, v168, v169\n" \
"v_exp_f32 v54, v54\n" \
"v_cvt_pk_bf16_f32 v49, v50, v51\n" \
"v_accvgpr_read_b32 v240, a49\n" \
"v_mfma_f32_16x16x32_bf16 a[72:75], a[208:211], v[96:99], a[72:75]\n" \
"buffer_load_dwordx4 v192, s[44:47], s73 offen offset:0 lds\n" \
"v_mfma_f32_16x16x32_bf16 a[72:75], a[212:215], v[100:103], a[72:75]\n" \
"ds_write_b128 v232, v[140:143] offset:3072\n" \
"v_exp_f32 v55, v55\n" \
"v_exp_f32 v40, v40\n" \
"v_mfma_f32_16x16x32_bf16 a[104:107], a[208:211], v[112:115], a[104:107]\n" \
"v_cvt_pk_bf16_f32 v149, v170, v171\n" \
"v_cvt_pk_bf16_f32 v150, v172, v173\n" \
"v_mfma_f32_16x16x32_bf16 a[104:107], a[212:215], v[116:119], a[104:107]\n" \
"ds_read_b128 a[208:211], v233 offset:2048\n" \
"ds_read_b128 a[212:215], v233 offset:10240\n" \
"v_cvt_pk_bf16_f32 v50, v52, v53\n" \
"v_exp_f32 v41, v41\n" \
"v_mfma_f32_16x16x32_bf16 a[108:111], a[216:219], v[112:115], a[108:111]\n" \
"buffer_load_dwordx4 v193, s[44:47], s73 offen offset:1024 lds\n" \
"v_mfma_f32_16x16x32_bf16 a[108:111], a[220:223], v[116:119], a[108:111]\n" \
"v_cvt_pk_bf16_f32 v151, v174, v175\n" \
"v_cvt_pk_bf16_f32 v152, v176, v177\n" \
"v_exp_f32 v42, v42\n" \
"v_cvt_pk_bf16_f32 v51, v54, v55\n" \
"v_exp_f32 v43, v43\n" \
"v_add_f32 v250, v250, v240\n" \
"v_mfma_f32_16x16x32_bf16 a[76:79], a[216:219], v[96:99], a[76:79]\n" \
"v_cvt_pk_bf16_f32 v153, v178, v179\n" \
"v_cvt_pk_bf16_f32 v154, v180, v181\n" \
"v_mfma_f32_16x16x32_bf16 a[76:79], a[220:223], v[100:103], a[76:79]\n" \
"ds_read_b128 a[216:219], v233 offset:3072\n" \
"ds_read_b128 a[220:223], v233 offset:11264\n" \
"v_exp_f32 v44, v44\n" \
"v_cvt_pk_bf16_f32 v36, v40, v41\n" \
"v_smfmac_f32_16x16x64_bf16 v[228:231], v[244:247], v[112:119], v243\n" \
"buffer_load_dwordx4 v192, s[44:47], s73 offen offset:2048 lds\n" \
"v_exp_f32 v45, v45\n" \
"v_mfma_f32_16x16x32_bf16 a[80:83], a[224:227], v[96:99], a[80:83]\n" \
"ds_write_b128 v232, v[144:147] offset:4096\n" \
"v_exp_f32 v46, v46\n" \
"v_cvt_pk_bf16_f32 v37, v42, v43\n" \
"v_accvgpr_read_b32 v251, a52\n" \
"v_mfma_f32_16x16x32_bf16 a[80:83], a[228:231], v[100:103], a[80:83]\n" \
"v_cvt_pk_bf16_f32 v155, v182, v183\n" \
"v_cvt_pk_bf16_f32 v156, v184, v185\n" \
"v_exp_f32 v47, v47\n" \
"v_mfma_f32_16x16x32_bf16 a[112:115], a[224:227], v[112:115], a[112:115]\n" \
"v_cvt_pk_bf16_f32 v157, v186, v187\n" \
"v_cvt_pk_bf16_f32 v158, v188, v189\n" \
"v_mfma_f32_16x16x32_bf16 a[112:115], a[228:231], v[116:119], a[112:115]\n" \
"buffer_load_dwordx4 v193, s[44:47], s73 offen offset:3072 lds\n" \
"v_exp_f32 v56, v56\n" \
"v_cvt_pk_bf16_f32 v38, v44, v45\n" \
"v_mfma_f32_16x16x32_bf16 a[116:119], a[232:235], v[112:115], a[116:119]\n" \
"ds_read_b128 a[224:227], v233 offset:4096\n" \
"ds_read_b128 a[228:231], v233 offset:12288\n" \
"v_mfma_f32_16x16x32_bf16 a[116:119], a[236:239], v[116:119], a[116:119]\n" \
"v_cvt_pk_bf16_f32 v159, v190, v191\n" \
"v_cvt_pk_bf16_f32 v39, v46, v47\n" \
"v_exp_f32 v57, v57\n" \
"v_exp_f32 v58, v58\n" \
"v_accvgpr_read_b32 v240, a53\n" \
"v_mfma_f32_16x16x32_bf16 a[84:87], a[232:235], v[96:99], a[84:87]\n" \
"s_xor_b32 s73, s73, 0x180\n" \
"v_exp_f32 v59, v59\n" \
"v_mfma_f32_16x16x32_bf16 a[84:87], a[236:239], v[100:103], a[84:87]\n" \
"ds_write_b128 v232, v[148:151] offset:5120\n" \
"v_exp_f32 v60, v60\n" \
"v_cvt_pk_bf16_f32 v52, v56, v57\n" \
"v_exp_f32 v61, v61\n" \
"v_mfma_f32_16x16x32_bf16 a[88:91], a[32:35], v[96:99], a[88:91]\n" \
"ds_read_b128 a[232:235], v233 offset:5120\n" \
"ds_read_b128 a[236:239], v233 offset:13312\n" \
"v_mfma_f32_16x16x32_bf16 a[88:91], a[36:39], v[100:103], a[88:91]\n" \
"ds_read_b128 a[240:243], v233 offset:6144\n" \
"ds_read_b128 a[244:247], v233 offset:14336\n" \
"v_exp_f32 v62, v62\n" \
"v_cvt_pk_bf16_f32 v53, v58, v59\n" \
"v_mfma_f32_16x16x32_bf16 a[120:123], a[32:35], v[112:115], a[120:123]\n" \
"ds_read_b128 v[0:3], v233 offset:20480\n" \
"ds_read_b128 v[4:7], v233 offset:28672\n" \
"v_mfma_f32_16x16x32_bf16 a[120:123], a[36:39], v[116:119], a[120:123]\n" \
"ds_read_b128 v[8:11], v233 offset:21504\n" \
"ds_read_b128 v[12:15], v233 offset:29696\n" \
"v_exp_f32 v63, v63\n" \
"v_cvt_pk_bf16_f32 v54, v60, v61\n" \
"v_add_f32 v251, v251, v240\n" \
"v_mfma_f32_16x16x32_bf16 a[124:127], a[40:43], v[112:115], a[124:127]\n" \
"v_cvt_pk_bf16_f32 v55, v62, v63\n" \
"v_mfma_f32_16x16x32_bf16 a[124:127], a[44:47], v[116:119], a[124:127]\n" \
"ds_write_b128 v232, v[152:155] offset:6144\n" \
"ds_write_b64 v249, v[250:251]\n" \
"v_mfma_f32_16x16x32_bf16 a[92:95], a[40:43], v[96:99], a[92:95]\n" \
"v_exp_f32 v16, v16\n" \
"v_mfma_f32_16x16x32_bf16 a[92:95], a[44:47], v[100:103], a[92:95]\n" \
"s_waitcnt lgkmcnt(4)\n" \
"ds_read_b128 v[96:99], v233 offset:22528\n" \
"ds_read_b128 v[100:103], v233 offset:30720\n" \
"v_exp_f32 v17, v17\n" \
"v_xor_b32 v249, 0x200, v249\n" \
"v_mfma_f32_16x16x32_bf16 a[64:67], a[192:195], v[32:35], a[64:67]\n" \
"ds_read_b128 v[104:107], v233 offset:23552\n" \
"ds_read_b128 v[108:111], v233 offset:31744\n" \
"v_mfma_f32_16x16x32_bf16 a[64:67], a[196:199], v[36:39], a[64:67]\n" \
"v_exp_f32 v18, v18\n" \
"v_exp_f32 v19, v19\n" \
"v_accvgpr_read_b32 v200, a59\n" \
"v_accvgpr_read_b32 v202, a60\n" \
"v_mfma_f32_16x16x32_bf16 a[96:99], a[192:195], v[48:51], a[96:99]\n" \
"v_exp_f32 v20, v20\n" \
"v_mfma_f32_16x16x32_bf16 a[96:99], a[196:199], v[52:55], a[96:99]\n" \
"v_cvt_pk_bf16_f32 v16, v16, v17\n" \
"v_cvt_pk_bf16_f32 v17, v18, v19\n" \
"v_accvgpr_read_b32 v204, a61\n" \
"v_xor_b32 v201, 64, v200\n" \
"v_mfma_f32_16x16x32_bf16 a[100:103], a[200:203], v[48:51], a[100:103]\n" \
"v_exp_f32 v21, v21\n" \
"v_mfma_f32_16x16x32_bf16 a[100:103], a[204:207], v[52:55], a[100:103]\n" \
"ds_write_b128 v232, v[156:159] offset:7168\n" \
"v_exp_f32 v22, v22\n" \
"v_xor_b32 v203, 64, v202\n" \
"v_xor_b32 v222, 32, v204\n" \
"v_mfma_f32_16x16x32_bf16 a[68:71], a[200:203], v[32:35], a[68:71]\n" \
"v_exp_f32 v23, v23\n" \
"v_mfma_f32_16x16x32_bf16 a[68:71], a[204:207], v[36:39], a[68:71]\n" \
"s_mul_i32 s76, s80, 393216\n" \
"s_add_u32 s76, s76, s78\n" \
"v_cvt_pk_bf16_f32 v18, v20, v21\n" \
"v_exp_f32 v24, v24\n" \
"v_add_u32 v209, 81920, v204\n" \
"v_add_u32 v210, 81920, v222\n" \
"v_smfmac_f32_16x16x64_bf16 v[224:227], v[244:247], v[32:39], v243\n" \
"s_waitcnt vmcnt(0) lgkmcnt(0)\n" \
"s_barrier\n" \
"v_mfma_f32_16x16x32_bf16 a[72:75], a[208:211], v[32:35], a[72:75]\n" \
"s_add_u32 m0, s91, 81920\n" \
"ds_read_b64_tr_b16 a[32:33], v209 offset:25600\n" \
"ds_read_b64_tr_b16 a[34:35], v209 offset:28672\n" \
"v_mfma_f32_16x16x32_bf16 a[72:75], a[212:215], v[36:39], a[72:75]\n" \
"buffer_load_dwordx4 v197, s[48:51], s76 offen offset:0 lds\n" \
"v_exp_f32 v25, v25\n" \
"v_mfma_f32_16x16x32_bf16 a[104:107], a[208:211], v[48:51], a[104:107]\n" \
"s_and_b32 s15, %1, 7\n" \
"ds_read_b64_tr_b16 a[36:37], v209 offset:37888\n" \
"ds_read_b64_tr_b16 a[38:39], v209 offset:40960\n" \
"v_mfma_f32_16x16x32_bf16 a[104:107], a[212:215], v[52:55], a[104:107]\n" \
"s_add_u32 s77, s76, 24576\n" \
"ds_read_b64_tr_b16 a[40:41], v209 offset:24576\n" \
"ds_read_b64_tr_b16 a[42:43], v209 offset:27648\n" \
"v_mfma_f32_16x16x32_bf16 a[108:111], a[216:219], v[48:51], a[108:111]\n" \
"buffer_load_dwordx4 v198, s[48:51], s76 offen offset:1024 lds\n" \
"v_mfma_f32_16x16x32_bf16 a[108:111], a[220:223], v[52:55], a[108:111]\n" \
"s_lshl3_add_u32 s14, s14, s15\n" \
"ds_read_b64_tr_b16 a[44:45], v209 offset:36864\n" \
"ds_read_b64_tr_b16 a[46:47], v209 offset:39936\n" \
"v_exp_f32 v26, v26\n" \
"v_cvt_pk_bf16_f32 v19, v22, v23\n" \
"v_mfma_f32_16x16x32_bf16 a[76:79], a[216:219], v[32:35], a[76:79]\n" \
"s_xor_b32 s80, s80, 0x8\n" \
"ds_read_b64_tr_b16 a[48:49], v210 offset:24576\n" \
"ds_read_b64_tr_b16 a[50:51], v210 offset:27648\n" \
"v_mfma_f32_16x16x32_bf16 a[76:79], a[220:223], v[36:39], a[76:79]\n" \
"buffer_load_dwordx4 v199, s[48:51], s76 offen offset:2048 lds\n" \
"v_smfmac_f32_16x16x64_bf16 v[228:231], v[244:247], v[48:55], v243\n" \
"ds_read_b64_tr_b16 a[52:53], v210 offset:36864\n" \
"ds_read_b64_tr_b16 a[54:55], v210 offset:39936\n" \
"v_exp_f32 v27, v27\n" \
"v_mfma_f32_16x16x32_bf16 a[80:83], a[224:227], v[32:35], a[80:83]\n" \
"s_add_u32 m0, m0, 3072\n" \
"ds_read_b128 a[0:3], v202 offset:0\n" \
"ds_read_b128 a[4:7], v203 offset:0\n" \
"v_mfma_f32_16x16x32_bf16 a[80:83], a[228:231], v[36:39], a[80:83]\n" \
"buffer_load_dwordx4 v197, s[48:51], s77 offen offset:0 lds\n" \
"v_mfma_f32_16x16x32_bf16 a[112:115], a[224:227], v[48:51], a[112:115]\n" \
"ds_read_b128 a[8:11], v200 offset:40960\n" \
"ds_read_b128 a[12:15], v201 offset:40960\n" \
"v_mfma_f32_16x16x32_bf16 a[112:115], a[228:231], v[52:55], a[112:115]\n" \
"ds_read_b128 a[16:19], v200 offset:45056\n" \
"ds_read_b128 a[20:23], v201 offset:45056\n" \
"v_exp_f32 v28, v28\n" \
"v_cvt_pk_bf16_f32 v20, v24, v25\n" \
"v_mfma_f32_16x16x32_bf16 a[116:119], a[232:235], v[48:51], a[116:119]\n" \
"buffer_load_dwordx4 v198, s[48:51], s77 offen offset:1024 lds\n" \
"v_mfma_f32_16x16x32_bf16 a[116:119], a[236:239], v[52:55], a[116:119]\n" \
"ds_read_b128 a[24:27], v200 offset:49152\n" \
"ds_read_b128 a[28:31], v201 offset:49152\n" \
"v_mfma_f32_16x16x32_bf16 a[84:87], a[232:235], v[32:35], a[84:87]\n" \
"ds_read_b64 v[250:251], v249\n" \
"ds_read_b128 v[128:131], v248 offset:0\n" \
"v_mfma_f32_16x16x32_bf16 a[84:87], a[236:239], v[36:39], a[84:87]\n" \
"buffer_load_dwordx4 v199, s[48:51], s77 offen offset:2048 lds\n" \
"v_exp_f32 v29, v29\n" \
"v_mfma_f32_16x16x32_bf16 a[88:91], a[240:243], v[32:35], a[88:91]\n" \
"ds_read_b128 v[132:135], v248 offset:1024\n" \
"ds_read_b128 v[136:139], v248 offset:2048\n" \
"v_mfma_f32_16x16x32_bf16 a[88:91], a[244:247], v[36:39], a[88:91]\n" \
"s_add_u32 m0, s79, 65536\n" \
"ds_read_b128 v[140:143], v248 offset:3072\n" \
"ds_read_b128 v[144:147], v248 offset:4096\n" \
"v_exp_f32 v30, v30\n" \
"v_mfma_f32_16x16x32_bf16 a[120:123], a[240:243], v[48:51], a[120:123]\n" \
"buffer_load_dwordx4 v192, s[44:47], s73 offen offset:0 lds\n" \
"v_mfma_f32_16x16x32_bf16 a[120:123], a[244:247], v[52:55], a[120:123]\n" \
"ds_read_b128 v[148:151], v248 offset:5120\n" \
"ds_read_b128 v[152:155], v248 offset:6144\n" \
"v_cvt_pk_bf16_f32 v21, v26, v27\n" \
"v_mfma_f32_16x16x32_bf16 a[124:127], a[248:251], v[48:51], a[124:127]\n" \
"ds_read_b128 v[156:159], v248 offset:7168\n" \
"v_cvt_pk_bf16_f32 v22, v28, v29\n" \
"v_mfma_f32_16x16x32_bf16 a[124:127], a[252:255], v[52:55], a[124:127]\n" \
"buffer_load_dwordx4 v193, s[44:47], s73 offen offset:1024 lds\n" \
"v_exp_f32 v31, v31\n" \
"v_mfma_f32_16x16x32_bf16 a[92:95], a[248:251], v[32:35], a[92:95]\n" \
"v_cvt_pk_bf16_f32 v23, v30, v31\n" \
"v_mov_b32 v218, s60\n" \
"v_mfma_f32_16x16x32_bf16 a[92:95], a[252:255], v[36:39], a[92:95]\n" \
"v_mov_b32 v219, s14\n" \
"v_lshlrev_b32 v220, 28, s61\n" \
"v_mfma_f32_16x16x32_bf16 a[96:99], v[64:67], v[16:19], a[96:99]\n" \
"buffer_load_dwordx4 v192, s[44:47], s73 offen offset:2048 lds\n" \
"v_mfma_f32_16x16x32_bf16 a[96:99], v[68:71], v[20:23], a[96:99]\n" \
"s_lshl3_add_u32 s15, s15, 65593856\n" \
"v_mov_b32 v170, 0x3f80\n" \
"v_mov_b32 v160, 0\n" \
"v_mfma_f32_16x16x32_bf16 a[100:103], v[72:75], v[16:19], a[100:103]\n" \
"s_lshr_b32 s14, %1, 3\n" \
"v_mov_b32 v161, 0\n" \
"v_mov_b32 v164, 0\n" \
"v_mfma_f32_16x16x32_bf16 a[100:103], v[76:79], v[20:23], a[100:103]\n" \
"buffer_load_dwordx4 v193, s[44:47], s73 offen offset:3072 lds\n" \
"v_smfmac_f32_16x16x64_bf16 v[228:231], v[244:247], v[16:23], v243\n" \
"s_cmp_eq_u32 s14, 3\n" \
"s_cselect_b32 s15, s15, 0x20000000\n" \
"v_mov_b32 v165, 0\n" \
"v_mfma_f32_16x16x32_bf16 a[104:107], v[80:83], v[16:19], a[104:107]\n" \
"buffer_store_dwordx2 v[218:219], v220, s[56:59], s15 offen sc1\n" \
"v_mfma_f32_16x16x32_bf16 a[104:107], v[84:87], v[20:23], a[104:107]\n" \
"s_xor_b32 s73, s73, 0x400\n" \
"v_lshrrev_b32 v168, 8, v234\n" \
"v_bfe_u32 v169, v234, 6, 2\n" \
"v_mfma_f32_16x16x32_bf16 a[108:111], v[88:91], v[16:19], a[108:111]\n" \
"v_cmp_eq_u32 s[16:17], v168, v169\n" \
"v_and_b32 v168, 16, v234\n" \
"v_mfma_f32_16x16x32_bf16 a[108:111], v[92:95], v[20:23], a[108:111]\n" \
"v_lshlrev_b32 v168, v168, v170\n" \
"v_cndmask_b32 v168, 0, v168, s[16:17]\n" \
"v_mfma_f32_16x16x32_bf16 a[112:115], v[0:3], v[16:19], a[112:115]\n" \
"Lpg7381_%=:\n" \
"s_cbranch_execz Lcb0_%=\n" \
"v_bfe_i32 v169, v234, 5, 1\n" \
"v_and_b32 v163, v168, v169\n" \
"v_mfma_f32_16x16x32_bf16 a[112:115], v[4:7], v[20:23], a[112:115]\n" \
"v_xor_b32 v162, v168, v163\n" \
"v_mfma_f32_16x16x32_bf16 a[116:119], v[8:11], v[16:19], a[116:119]\n" \
"v_mfma_f32_16x16x32_bf16 a[116:119], v[12:15], v[20:23], a[116:119]\n" \
"v_mfma_f32_16x16x32_bf16 a[120:123], v[96:99], v[16:19], a[120:123]\n" \
"v_mfma_f32_16x16x32_bf16 a[120:123], v[100:103], v[20:23], a[120:123]\n" \
"v_mfma_f32_16x16x32_bf16 a[124:127], v[104:107], v[16:19], a[124:127]\n" \
"v_mfma_f32_16x16x32_bf16 a[124:127], v[108:111], v[20:23], a[124:127]\n" \
"s_waitcnt lgkmcnt(0)\n" \
"s_barrier\n" \
"v_mfma_f32_16x16x32_bf16 v[32:35], a[32:35], a[0:3], 0\n" \
"s_add_u32 m0, s79, 0\n" \
"ds_read_b128 a[192:195], v200 offset:53248\n" \
"v_mfma_f32_16x16x32_bf16 v[32:35], a[36:39], a[4:7], v[32:35]\n" \
"buffer_load_dwordx4 v192, s[44:47], s73 offen offset:0 lds\n" \
"v_mfma_f32_16x16x32_bf16 v[40:43], a[32:35], a[8:11], 0\n" \
"ds_read_b128 a[196:199], v201 offset:53248\n" \
"v_add_u32 v205, 0x10000, v200\n" \
"v_mfma_f32_16x16x32_bf16 v[40:43], a[36:39], a[12:15], v[40:43]\n" \
"buffer_load_dwordx4 v193, s[44:47], s73 offen offset:1024 lds\n" \
"v_mfma_f32_16x16x32_bf16 a[64:67], v[162:165], v[128:131], a[64:67]\n" \
"v_mfma_f32_16x16x32_bf16 a[68:71], v[160:163], v[128:131], a[68:71]\n" \
"v_mfma_f32_16x16x32_bf16 a[72:75], v[162:165], v[132:135], a[72:75]\n" \
"v_mfma_f32_16x16x32_bf16 a[76:79], v[160:163], v[132:135], a[76:79]\n" \
"v_add_f32 v248, v224, v225\n" \
"v_add_f32 v249, v228, v229\n" \
"v_mfma_f32_16x16x32_bf16 v[0:3], a[40:43], a[8:11], 0\n" \
"ds_read_b64_tr_b16 a[200:201], v210 offset:25600\n" \
"ds_read_b64_tr_b16 a[202:203], v210 offset:28672\n" \
"v_mfma_f32_16x16x32_bf16 v[0:3], a[44:47], a[12:15], v[0:3]\n" \
"buffer_load_dwordx4 v192, s[44:47], s73 offen offset:2048 lds\n" \
"v_mfma_f32_16x16x32_bf16 v[4:7], a[48:51], a[8:11], 0\n" \
"ds_read_b64_tr_b16 a[204:205], v210 offset:37888\n" \
"ds_read_b64_tr_b16 a[206:207], v210 offset:40960\n" \
"v_mfma_f32_16x16x32_bf16 v[4:7], a[52:55], a[12:15], v[4:7]\n" \
"buffer_load_dwordx4 v193, s[44:47], s73 offen offset:3072 lds\n" \
"v_mfma_f32_16x16x32_bf16 v[12:15], a[48:51], a[16:19], 0\n" \
"s_xor_b32 s73, s73, 0x500\n" \
"s_mul_i32 s76, s80, 393216\n" \
"s_add_u32 s76, s76, s78\n" \
"s_add_u32 m0, s91, 16384\n" \
"ds_read_b64_tr_b16 a[208:209], v209 offset:26624\n" \
"ds_read_b64_tr_b16 a[210:211], v209 offset:29696\n" \
"v_add_f32 v248, v248, v250\n" \
"v_mfma_f32_16x16x32_bf16 v[12:15], a[52:55], a[20:23], v[12:15]\n" \
"buffer_load_dwordx4 v197, s[48:51], s76 offen offset:0 lds\n" \
"v_mfma_f32_16x16x32_bf16 v[8:11], a[40:43], a[16:19], 0\n" \
"s_add_u32 s77, s76, 24576\n" \
"ds_read_b64_tr_b16 a[212:213], v209 offset:38912\n" \
"ds_read_b64_tr_b16 a[214:215], v209 offset:41984\n" \
"v_mfma_f32_16x16x32_bf16 v[8:11], a[44:47], a[20:23], v[8:11]\n" \
"buffer_load_dwordx4 v198, s[48:51], s76 offen offset:1024 lds\n" \
"v_mfma_f32_16x16x32_bf16 v[48:51], a[32:35], a[16:19], 0\n" \
"s_xor_b32 s80, s80, 0xa\n" \
"ds_read_b64_tr_b16 a[216:217], v210 offset:26624\n" \
"v_add_u32 v206, 0x10000, v201\n" \
"v_mfma_f32_16x16x32_bf16 v[48:51], a[36:39], a[20:23], v[48:51]\n" \
"buffer_load_dwordx4 v199, s[48:51], s76 offen offset:2048 lds\n" \
"v_mfma_f32_16x16x32_bf16 v[56:59], a[32:35], a[24:27], 0\n" \
"s_add_u32 m0, m0, 3072\n" \
"ds_read_b64_tr_b16 a[218:219], v210 offset:29696\n" \
"v_add_u32 v207, 0x20000, v200\n" \
"v_mfma_f32_16x16x32_bf16 v[56:59], a[36:39], a[28:31], v[56:59]\n" \
"buffer_load_dwordx4 v197, s[48:51], s77 offen offset:0 lds\n" \
"v_add_f32 v249, v249, v251\n" \
"v_mfma_f32_16x16x32_bf16 v[16:19], a[40:43], a[24:27], 0\n" \
"ds_read_b64_tr_b16 a[220:221], v210 offset:38912\n" \
"v_add_u32 v208, 0x20000, v201\n" \
"v_mfma_f32_16x16x32_bf16 v[16:19], a[44:47], a[28:31], v[16:19]\n" \
"buffer_load_dwordx4 v198, s[48:51], s77 offen offset:1024 lds\n" \
"v_mfma_f32_16x16x32_bf16 v[20:23], a[48:51], a[24:27], 0\n" \
"s_waitcnt lgkmcnt(2)\n" \
"ds_read_b64_tr_b16 a[222:223], v210 offset:41984\n" \
"v_mfma_f32_16x16x32_bf16 v[20:23], a[52:55], a[28:31], v[20:23]\n" \
"buffer_load_dwordx4 v199, s[48:51], s77 offen offset:2048 lds\n" \
"v_mfma_f32_16x16x32_bf16 v[28:31], a[48:51], a[192:195], 0\n" \
"v_rcp_f32 v232, v248\n" \
"v_mfma_f32_16x16x32_bf16 v[28:31], a[52:55], a[196:199], v[28:31]\n" \
"v_rcp_f32 v233, v249\n" \
"v_add_u32 v240, 0x1000, v236\n" \
"v_mfma_f32_16x16x32_bf16 v[24:27], a[40:43], a[192:195], 0\n" \
"v_add_f32 v224, v248, v249\n" \
"v_mul_f32 v224, 0x43800000, v224\n" \
"v_mfma_f32_16x16x32_bf16 v[24:27], a[44:47], a[196:199], v[24:27]\n" \
"v_add_f32 v248, v232, v233\n" \
"v_fmac_f32 v224, 0x5f800000, v248\n" \
"v_mfma_f32_16x16x32_bf16 v[64:67], a[32:35], a[192:195], 0\n" \
"v_mov_b32 v225, 0\n" \
"v_mov_b32 v226, 0\n" \
"v_mfma_f32_16x16x32_bf16 v[64:67], a[36:39], a[196:199], v[64:67]\n" \
"s_waitcnt vmcnt(11) lgkmcnt(0)\n" \
"s_barrier\n" \
"v_mfma_f32_16x16x32_bf16 v[68:71], a[200:203], a[192:195], 0\n" \
"ds_read_b64_tr_b16 a[32:33], v209 offset:1024\n" \
"ds_read_b64_tr_b16 a[34:35], v209 offset:4096\n" \
"v_mfma_f32_16x16x32_bf16 v[68:71], a[204:207], a[196:199], v[68:71]\n" \
"ds_read_b64_tr_b16 a[36:37], v209 offset:13312\n" \
"ds_read_b64_tr_b16 a[38:39], v209 offset:16384\n" \
"v_mfma_f32_16x16x32_bf16 v[60:63], a[200:203], a[24:27], 0\n" \
"s_add_u32 m0, s79, 40960\n" \
"ds_read_b64_tr_b16 a[40:41], v209 offset:0\n" \
"v_mov_b32 v227, 0\n" \
"v_mfma_f32_16x16x32_bf16 v[60:63], a[204:207], a[28:31], v[60:63]\n" \
"buffer_load_dwordx4 v192, s[44:47], s73 offen offset:0 lds\n" \
"v_accvgpr_read_b32 v112, a64\n" \
"v_mfma_f32_16x16x32_bf16 v[52:55], a[200:203], a[16:19], 0\n" \
"ds_read_b64_tr_b16 a[42:43], v209 offset:3072\n" \
"v_accvgpr_read_b32 v113, a65\n" \
"v_mfma_f32_16x16x32_bf16 v[52:55], a[204:207], a[20:23], v[52:55]\n" \
"ds_read_b64_tr_b16 a[44:45], v209 offset:12288\n" \
"v_accvgpr_read_b32 v114, a66\n" \
"v_mfma_f32_16x16x32_bf16 v[44:47], a[200:203], a[8:11], 0\n" \
"ds_read_b64_tr_b16 a[46:47], v209 offset:15360\n" \
"v_accvgpr_read_b32 v115, a67\n" \
"v_mfma_f32_16x16x32_bf16 v[44:47], a[204:207], a[12:15], v[44:47]\n" \
"buffer_load_dwordx4 v193, s[44:47], s73 offen offset:1024 lds\n" \
"v_mfma_f32_16x16x32_bf16 v[72:75], a[0:3], a[208:211], 0\n" \
"ds_read_b64_tr_b16 a[48:49], v210 offset:0\n" \
"v_accvgpr_read_b32 v116, a68\n" \
"v_mfma_f32_16x16x32_bf16 v[72:75], a[4:7], a[212:215], v[72:75]\n" \
"ds_read_b128 a[0:3], v202 offset:3072\n" \
"v_accvgpr_read_b32 v117, a69\n" \
"v_accvgpr_read_b32 v118, a70\n" \
"v_mfma_f32_16x16x32_bf16 v[80:83], a[8:11], a[208:211], 0\n" \
"ds_read_b128 a[4:7], v203 offset:3072\n" \
"v_accvgpr_read_b32 v119, a71\n" \
"v_mfma_f32_16x16x32_bf16 v[80:83], a[12:15], a[212:215], v[80:83]\n" \
"buffer_load_dwordx4 v192, s[44:47], s73 offen offset:2048 lds\n" \
"v_mfma_f32_16x16x32_bf16 a[80:83], v[162:165], v[136:139], a[80:83]\n" \
"v_mul_f32 v112, v112, v232\n" \
"v_mul_f32 v113, v113, v232\n" \
"v_mfma_f32_16x16x32_bf16 v[84:87], a[8:11], a[216:219], 0\n" \
"ds_read_b64_tr_b16 a[50:51], v210 offset:3072\n" \
"v_mul_f32 v114, v114, v232\n" \
"v_mfma_f32_16x16x32_bf16 v[84:87], a[12:15], a[220:223], v[84:87]\n" \
"ds_read_b128 a[8:11], v205 offset:0\n" \
"v_mul_f32 v115, v115, v232\n" \
"v_mfma_f32_16x16x32_bf16 a[84:87], v[160:163], v[136:139], a[84:87]\n" \
"v_mul_f32 v116, v116, v232\n" \
"v_mul_f32 v117, v117, v232\n" \
"v_mfma_f32_16x16x32_bf16 v[92:95], a[16:19], a[216:219], 0\n" \
"ds_read_b128 a[12:15], v206 offset:0\n" \
"v_mul_f32 v118, v118, v232\n" \
"v_mfma_f32_16x16x32_bf16 v[92:95], a[20:23], a[220:223], v[92:95]\n" \
"buffer_load_dwordx4 v193, s[44:47], s73 offen offset:3072 lds\n" \
"v_mul_f32 v119, v119, v232\n" \
"v_mfma_f32_16x16x32_bf16 v[88:91], a[16:19], a[208:211], 0\n" \
"s_xor_b32 s73, s73, 0x180\n" \
"s_mul_i32 s76, s80, 393216\n" \
"ds_read_b64_tr_b16 a[52:53], v210 offset:12288\n" \
"v_mfma_f32_16x16x32_bf16 v[88:91], a[20:23], a[212:215], v[88:91]\n" \
"s_add_u32 s76, s76, s78\n" \
"ds_read_b128 a[16:19], v205 offset:4096\n" \
"v_cvt_pk_bf16_f32 v120, v112, v113\n" \
"v_mfma_f32_16x16x32_bf16 v[96:99], a[24:27], a[208:211], 0\n" \
"s_add_u32 m0, s91, 106496\n" \
"ds_read_b128 a[20:23], v206 offset:4096\n" \
"v_cvt_pk_bf16_f32 v121, v114, v115\n" \
"v_mfma_f32_16x16x32_bf16 v[96:99], a[28:31], a[212:215], v[96:99]\n" \
"buffer_load_dwordx4 v197, s[48:51], s76 offen offset:0 lds\n" \
"v_mfma_f32_16x16x32_bf16 v[100:103], a[24:27], a[216:219], 0\n" \
"ds_read_b64_tr_b16 a[54:55], v210 offset:15360\n" \
"v_cvt_pk_bf16_f32 v122, v116, v117\n" \
"v_mfma_f32_16x16x32_bf16 v[100:103], a[28:31], a[220:223], v[100:103]\n" \
"ds_read_b128 a[24:27], v205 offset:8192\n" \
"v_cvt_pk_bf16_f32 v123, v118, v119\n" \
"v_mfma_f32_16x16x32_bf16 v[108:111], a[192:195], a[216:219], 0\n" \
"s_add_u32 s77, s76, 24576\n" \
"ds_read_b128 a[28:31], v206 offset:8192\n" \
"v_mfma_f32_16x16x32_bf16 v[108:111], a[196:199], a[220:223], v[108:111]\n" \
"buffer_load_dwordx4 v198, s[48:51], s76 offen offset:1024 lds\n" \
"v_mfma_f32_16x16x32_bf16 v[104:107], a[192:195], a[208:211], 0\n" \
"s_waitcnt lgkmcnt(3)\n" \
"v_mfma_f32_16x16x32_bf16 v[104:107], a[196:199], a[212:215], v[104:107]\n" \
"buffer_store_dwordx4 v[120:123], v236, s[56:59], s88 offen offset:0 sc1\n" \
"v_mfma_f32_16x16x32_bf16 v[32:35], a[32:35], a[0:3], v[32:35]\n" \
"s_xor_b32 s80, s80, 0x3\n" \
"ds_read_b128 a[192:195], v205 offset:12288\n" \
"v_mfma_f32_16x16x32_bf16 v[32:35], a[36:39], a[4:7], v[32:35]\n" \
"buffer_load_dwordx4 v199, s[48:51], s76 offen offset:2048 lds\n" \
"v_mfma_f32_16x16x32_bf16 v[224:227], v[244:247], v[120:123], v[224:227]\n" \
"v_accvgpr_read_b32 v112, a72\n" \
"v_accvgpr_read_b32 v113, a73\n" \
"v_accvgpr_read_b32 v114, a74\n" \
"v_mfma_f32_16x16x32_bf16 v[40:43], a[32:35], a[8:11], v[40:43]\n" \
"ds_read_b128 a[196:199], v206 offset:12288\n" \
"v_accvgpr_read_b32 v115, a75\n" \
"v_mfma_f32_16x16x32_bf16 v[40:43], a[36:39], a[12:15], v[40:43]\n" \
"s_waitcnt lgkmcnt(2)\n" \
"ds_read_b64_tr_b16 a[200:201], v210 offset:1024\n" \
"v_accvgpr_read_b32 v116, a76\n" \
"v_accvgpr_read_b32 v117, a77\n" \
"v_mfma_f32_16x16x32_bf16 v[0:3], a[40:43], a[8:11], v[0:3]\n" \
"s_add_u32 m0, m0, 3072\n" \
"ds_read_b64_tr_b16 a[202:203], v210 offset:4096\n" \
"v_accvgpr_read_b32 v118, a78\n" \
"v_mfma_f32_16x16x32_bf16 v[0:3], a[44:47], a[12:15], v[0:3]\n" \
"buffer_load_dwordx4 v197, s[48:51], s77 offen offset:0 lds\n" \
"v_mfma_f32_16x16x32_bf16 v[4:7], a[48:51], a[8:11], v[4:7]\n" \
"ds_read_b64_tr_b16 a[204:205], v210 offset:13312\n" \
"v_accvgpr_read_b32 v119, a79\n" \
"v_mfma_f32_16x16x32_bf16 v[4:7], a[52:55], a[12:15], v[4:7]\n" \
"ds_read_b64_tr_b16 a[206:207], v210 offset:16384\n" \
"v_mfma_f32_16x16x32_bf16 a[88:91], v[162:165], v[140:143], a[88:91]\n" \
"v_mul_f32 v112, v112, v232\n" \
"v_mul_f32 v113, v113, v232\n" \
"v_mfma_f32_16x16x32_bf16 v[12:15], a[48:51], a[16:19], v[12:15]\n" \
"ds_read_b64_tr_b16 a[208:209], v209 offset:2048\n" \
"v_mul_f32 v114, v114, v232\n" \
"v_mfma_f32_16x16x32_bf16 v[12:15], a[52:55], a[20:23], v[12:15]\n" \
"buffer_load_dwordx4 v198, s[48:51], s77 offen offset:1024 lds\n" \
"v_mul_f32 v115, v115, v232\n" \
"v_mfma_f32_16x16x32_bf16 a[92:95], v[160:163], v[140:143], a[92:95]\n" \
"v_mul_f32 v116, v116, v232\n" \
"v_mul_f32 v117, v117, v232\n" \
"v_mfma_f32_16x16x32_bf16 v[8:11], a[40:43], a[16:19], v[8:11]\n" \
"ds_read_b64_tr_b16 a[210:211], v209 offset:5120\n" \
"v_mul_f32 v118, v118, v232\n" \
"v_mfma_f32_16x16x32_bf16 v[8:11], a[44:47], a[20:23], v[8:11]\n" \
"ds_read_b64_tr_b16 a[212:213], v209 offset:14336\n" \
"v_mul_f32 v119, v119, v232\n" \
"v_mfma_f32_16x16x32_bf16 v[48:51], a[32:35], a[16:19], v[48:51]\n" \
"ds_read_b64_tr_b16 a[214:215], v209 offset:17408\n" \
"v_cvt_pk_bf16_f32 v124, v112, v113\n" \
"v_mfma_f32_16x16x32_bf16 v[48:51], a[36:39], a[20:23], v[48:51]\n" \
"buffer_load_dwordx4 v199, s[48:51], s77 offen offset:2048 lds\n" \
"v_mfma_f32_16x16x32_bf16 v[56:59], a[32:35], a[24:27], v[56:59]\n" \
"ds_read_b64_tr_b16 a[216:217], v210 offset:2048\n" \
"v_cvt_pk_bf16_f32 v125, v114, v115\n" \
"v_mfma_f32_16x16x32_bf16 v[56:59], a[36:39], a[28:31], v[56:59]\n" \
"ds_read_b64_tr_b16 a[218:219], v210 offset:5120\n" \
"v_cvt_pk_bf16_f32 v126, v116, v117\n" \
"v_mfma_f32_16x16x32_bf16 v[16:19], a[40:43], a[24:27], v[16:19]\n" \
"ds_read_b64_tr_b16 a[220:221], v210 offset:14336\n" \
"v_cvt_pk_bf16_f32 v127, v118, v119\n" \
"v_mfma_f32_16x16x32_bf16 v[16:19], a[44:47], a[28:31], v[16:19]\n" \
"buffer_store_dwordx4 v[124:127], v236, s[56:59], s88 offen offset:2048 sc1\n" \
"v_mfma_f32_16x16x32_bf16 v[20:23], a[48:51], a[24:27], v[20:23]\n" \
"ds_read_b64_tr_b16 a[222:223], v210 offset:17408\n" \
"v_mfma_f32_16x16x32_bf16 v[20:23], a[52:55], a[28:31], v[20:23]\n" \
"s_waitcnt lgkmcnt(3)\n" \
"v_mfma_f32_16x16x32_bf16 v[224:227], v[244:247], v[124:127], v[224:227]\n" \
"v_accvgpr_read_b32 v112, a80\n" \
"v_accvgpr_read_b32 v113, a81\n" \
"v_accvgpr_read_b32 v114, a82\n" \
"v_mfma_f32_16x16x32_bf16 v[28:31], a[48:51], a[192:195], v[28:31]\n" \
"v_accvgpr_read_b32 v115, a83\n" \
"v_accvgpr_read_b32 v116, a84\n" \
"v_mfma_f32_16x16x32_bf16 v[28:31], a[52:55], a[196:199], v[28:31]\n" \
"v_accvgpr_read_b32 v117, a85\n" \
"v_accvgpr_read_b32 v118, a86\n" \
"v_mfma_f32_16x16x32_bf16 v[24:27], a[40:43], a[192:195], v[24:27]\n" \
"v_accvgpr_read_b32 v119, a87\n" \
"v_mfma_f32_16x16x32_bf16 v[24:27], a[44:47], a[196:199], v[24:27]\n" \
"v_mfma_f32_16x16x32_bf16 a[96:99], v[162:165], v[144:147], a[96:99]\n" \
"v_mul_f32 v112, v112, v232\n" \
"v_mul_f32 v113, v113, v232\n" \
"v_mul_f32 v114, v114, v232\n" \
"v_mfma_f32_16x16x32_bf16 v[64:67], a[32:35], a[192:195], v[64:67]\n" \
"v_mul_f32 v115, v115, v232\n" \
"v_mfma_f32_16x16x32_bf16 v[64:67], a[36:39], a[196:199], v[64:67]\n" \
"v_mfma_f32_16x16x32_bf16 a[100:103], v[160:163], v[144:147], a[100:103]\n" \
"s_waitcnt vmcnt(12) lgkmcnt(0)\n" \
"s_barrier\n" \
"v_mfma_f32_16x16x32_bf16 v[68:71], a[200:203], a[192:195], v[68:71]\n" \
"ds_read_b64_tr_b16 a[32:33], v204 offset:17408\n" \
"ds_read_b64_tr_b16 a[34:35], v204 offset:20480\n" \
"v_mfma_f32_16x16x32_bf16 v[68:71], a[204:207], a[196:199], v[68:71]\n" \
"ds_read_b64_tr_b16 a[36:37], v204 offset:29696\n" \
"ds_read_b64_tr_b16 a[38:39], v204 offset:32768\n" \
"v_mfma_f32_16x16x32_bf16 v[60:63], a[200:203], a[24:27], v[60:63]\n" \
"s_add_u32 m0, s79, 65536\n" \
"ds_read_b64_tr_b16 a[40:41], v204 offset:16384\n" \
"v_mul_f32 v116, v116, v232\n" \
"v_mfma_f32_16x16x32_bf16 v[60:63], a[204:207], a[28:31], v[60:63]\n" \
"buffer_load_dwordx4 v192, s[44:47], s73 offen offset:0 lds\n" \
"v_mul_f32 v117, v117, v232\n" \
"v_mfma_f32_16x16x32_bf16 v[52:55], a[200:203], a[16:19], v[52:55]\n" \
"ds_read_b64_tr_b16 a[42:43], v204 offset:19456\n" \
"v_mul_f32 v118, v118, v232\n" \
"v_mfma_f32_16x16x32_bf16 v[52:55], a[204:207], a[20:23], v[52:55]\n" \
"ds_read_b64_tr_b16 a[44:45], v204 offset:28672\n" \
"v_mul_f32 v119, v119, v232\n" \
"v_mfma_f32_16x16x32_bf16 v[44:47], a[200:203], a[8:11], v[44:47]\n" \
"ds_read_b64_tr_b16 a[46:47], v204 offset:31744\n" \
"v_cvt_pk_bf16_f32 v120, v112, v113\n" \
"v_mfma_f32_16x16x32_bf16 v[44:47], a[204:207], a[12:15], v[44:47]\n" \
"buffer_load_dwordx4 v193, s[44:47], s73 offen offset:1024 lds\n" \
"v_mfma_f32_16x16x32_bf16 v[72:75], a[0:3], a[208:211], v[72:75]\n" \
"ds_read_b64_tr_b16 a[48:49], v222 offset:16384\n" \
"v_cvt_pk_bf16_f32 v121, v114, v115\n" \
"v_mfma_f32_16x16x32_bf16 v[72:75], a[4:7], a[212:215], v[72:75]\n" \
"ds_read_b128 a[0:3], v202 offset:19456\n" \
"v_cvt_pk_bf16_f32 v122, v116, v117\n" \
"v_mfma_f32_16x16x32_bf16 v[80:83], a[8:11], a[208:211], v[80:83]\n" \
"ds_read_b128 a[4:7], v203 offset:19456\n" \
"v_cvt_pk_bf16_f32 v123, v118, v119\n" \
"v_mfma_f32_16x16x32_bf16 v[80:83], a[12:15], a[212:215], v[80:83]\n" \
"buffer_load_dwordx4 v192, s[44:47], s73 offen offset:2048 lds\n" \
"v_mfma_f32_16x16x32_bf16 v[84:87], a[8:11], a[216:219], v[84:87]\n" \
"ds_read_b64_tr_b16 a[50:51], v222 offset:19456\n" \
"v_mfma_f32_16x16x32_bf16 v[84:87], a[12:15], a[220:223], v[84:87]\n" \
"ds_read_b128 a[8:11], v200 offset:0\n" \
"buffer_store_dwordx4 v[120:123], v240, s[56:59], s88 offen offset:0 sc1\n" \
"v_mfma_f32_16x16x32_bf16 v[92:95], a[16:19], a[216:219], v[92:95]\n" \
"ds_read_b128 a[12:15], v201 offset:0\n" \
"v_mfma_f32_16x16x32_bf16 v[92:95], a[20:23], a[220:223], v[92:95]\n" \
"buffer_load_dwordx4 v193, s[44:47], s73 offen offset:3072 lds\n" \
"v_mfma_f32_16x16x32_bf16 v[224:227], v[244:247], v[120:123], v[224:227]\n" \
"v_accvgpr_read_b32 v112, a88\n" \
"v_accvgpr_read_b32 v113, a89\n" \
"v_accvgpr_read_b32 v114, a90\n" \
"v_mfma_f32_16x16x32_bf16 v[88:91], a[16:19], a[208:211], v[88:91]\n" \
"s_xor_b32 s73, s73, 0x300\n" \
"s_mul_i32 s76, s80, 393216\n" \
"ds_read_b64_tr_b16 a[52:53], v222 offset:28672\n" \
"v_mfma_f32_16x16x32_bf16 v[88:91], a[20:23], a[212:215], v[88:91]\n" \
"s_add_u32 s76, s76, s78\n" \
"ds_read_b128 a[16:19], v200 offset:4096\n" \
"v_accvgpr_read_b32 v115, a91\n" \
"v_accvgpr_read_b32 v116, a92\n" \
"v_mfma_f32_16x16x32_bf16 v[96:99], a[24:27], a[208:211], v[96:99]\n" \
"s_add_u32 m0, s91, 81920\n" \
"ds_read_b128 a[20:23], v201 offset:4096\n" \
"v_accvgpr_read_b32 v117, a93\n" \
"v_mfma_f32_16x16x32_bf16 v[96:99], a[28:31], a[212:215], v[96:99]\n" \
"buffer_load_dwordx4 v197, s[48:51], s76 offen offset:0 lds\n" \
"v_mfma_f32_16x16x32_bf16 v[100:103], a[24:27], a[216:219], v[100:103]\n" \
"ds_read_b64_tr_b16 a[54:55], v222 offset:31744\n" \
"v_accvgpr_read_b32 v118, a94\n" \
"v_mfma_f32_16x16x32_bf16 v[100:103], a[28:31], a[220:223], v[100:103]\n" \
"ds_read_b128 a[24:27], v200 offset:8192\n" \
"v_accvgpr_read_b32 v119, a95\n" \
"v_mfma_f32_16x16x32_bf16 a[104:107], v[162:165], v[148:151], a[104:107]\n" \
"v_mul_f32 v112, v112, v232\n" \
"v_mul_f32 v113, v113, v232\n" \
"v_mfma_f32_16x16x32_bf16 v[108:111], a[192:195], a[216:219], v[108:111]\n" \
"s_add_u32 s77, s76, 24576\n" \
"ds_read_b128 a[28:31], v201 offset:8192\n" \
"v_mul_f32 v114, v114, v232\n" \
"v_mfma_f32_16x16x32_bf16 v[108:111], a[196:199], a[220:223], v[108:111]\n" \
"buffer_load_dwordx4 v198, s[48:51], s76 offen offset:1024 lds\n" \
"v_mul_f32 v115, v115, v232\n" \
"v_mfma_f32_16x16x32_bf16 a[108:111], v[160:163], v[148:151], a[108:111]\n" \
"v_mul_f32 v116, v116, v232\n" \
"v_mul_f32 v117, v117, v232\n" \
"v_mfma_f32_16x16x32_bf16 v[104:107], a[192:195], a[208:211], v[104:107]\n" \
"v_mul_f32 v118, v118, v232\n" \
"v_mul_f32 v119, v119, v232\n" \
"v_mfma_f32_16x16x32_bf16 v[104:107], a[196:199], a[212:215], v[104:107]\n" \
"s_waitcnt lgkmcnt(2)\n" \
"v_cvt_pk_bf16_f32 v124, v112, v113\n" \
"v_cvt_pk_bf16_f32 v125, v114, v115\n" \
"v_mfma_f32_16x16x32_bf16 v[32:35], a[32:35], a[0:3], v[32:35]\n" \
"s_xor_b32 s80, s80, 0x6\n" \
"ds_read_b128 a[192:195], v200 offset:12288\n" \
"v_cvt_pk_bf16_f32 v126, v116, v117\n" \
"v_mfma_f32_16x16x32_bf16 v[32:35], a[36:39], a[4:7], v[32:35]\n" \
"buffer_load_dwordx4 v199, s[48:51], s76 offen offset:2048 lds\n" \
"v_mfma_f32_16x16x32_bf16 v[40:43], a[32:35], a[8:11], v[40:43]\n" \
"ds_read_b128 a[196:199], v201 offset:12288\n" \
"v_cvt_pk_bf16_f32 v127, v118, v119\n" \
"v_mfma_f32_16x16x32_bf16 v[40:43], a[36:39], a[12:15], v[40:43]\n" \
"ds_read_b64_tr_b16 a[200:201], v222 offset:17408\n" \
"buffer_store_dwordx4 v[124:127], v240, s[56:59], s88 offen offset:2048 sc1\n" \
"v_mfma_f32_16x16x32_bf16 v[0:3], a[40:43], a[8:11], v[0:3]\n" \
"s_add_u32 m0, m0, 3072\n" \
"ds_read_b64_tr_b16 a[202:203], v222 offset:20480\n" \
"v_mfma_f32_16x16x32_bf16 v[0:3], a[44:47], a[12:15], v[0:3]\n" \
"buffer_load_dwordx4 v197, s[48:51], s77 offen offset:0 lds\n" \
"v_mfma_f32_16x16x32_bf16 v[224:227], v[244:247], v[124:127], v[224:227]\n" \
"s_add_u32 s82, s88, 532480\n" \
"v_accvgpr_read_b32 v112, a96\n" \
"v_accvgpr_read_b32 v113, a97\n" \
"v_accvgpr_read_b32 v114, a98\n" \
"v_mfma_f32_16x16x32_bf16 v[4:7], a[48:51], a[8:11], v[4:7]\n" \
"ds_read_b64_tr_b16 a[204:205], v222 offset:29696\n" \
"v_accvgpr_read_b32 v115, a99\n" \
"v_mfma_f32_16x16x32_bf16 v[4:7], a[52:55], a[12:15], v[4:7]\n" \
"ds_read_b64_tr_b16 a[206:207], v222 offset:32768\n" \
"v_accvgpr_read_b32 v116, a100\n" \
"v_mfma_f32_16x16x32_bf16 v[12:15], a[48:51], a[16:19], v[12:15]\n" \
"ds_read_b64_tr_b16 a[208:209], v204 offset:18432\n" \
"v_accvgpr_read_b32 v117, a101\n" \
"v_mfma_f32_16x16x32_bf16 v[12:15], a[52:55], a[20:23], v[12:15]\n" \
"buffer_load_dwordx4 v198, s[48:51], s77 offen offset:1024 lds\n" \
"v_mfma_f32_16x16x32_bf16 v[8:11], a[40:43], a[16:19], v[8:11]\n" \
"ds_read_b64_tr_b16 a[210:211], v204 offset:21504\n" \
"v_accvgpr_read_b32 v118, a102\n" \
"v_mfma_f32_16x16x32_bf16 v[8:11], a[44:47], a[20:23], v[8:11]\n" \
"ds_read_b64_tr_b16 a[212:213], v204 offset:30720\n" \
"v_accvgpr_read_b32 v119, a103\n" \
"v_mfma_f32_16x16x32_bf16 a[112:115], v[162:165], v[152:155], a[112:115]\n" \
"v_mul_f32 v112, v112, v233\n" \
"v_mul_f32 v113, v113, v233\n" \
"v_mul_f32 v114, v114, v233\n" \
"v_mfma_f32_16x16x32_bf16 v[48:51], a[32:35], a[16:19], v[48:51]\n" \
"s_waitcnt lgkmcnt(3)\n" \
"ds_read_b64_tr_b16 a[214:215], v204 offset:33792\n" \
"v_mul_f32 v115, v115, v233\n" \
"v_mfma_f32_16x16x32_bf16 v[48:51], a[36:39], a[20:23], v[48:51]\n" \
"buffer_load_dwordx4 v199, s[48:51], s77 offen offset:2048 lds\n" \
"v_mfma_f32_16x16x32_bf16 a[116:119], v[160:163], v[152:155], a[116:119]\n" \
"v_mul_f32 v116, v116, v233\n" \
"v_mul_f32 v117, v117, v233\n" \
"v_mfma_f32_16x16x32_bf16 v[56:59], a[32:35], a[24:27], v[56:59]\n" \
"ds_read_b64_tr_b16 a[216:217], v222 offset:18432\n" \
"v_mul_f32 v118, v118, v233\n" \
"v_mfma_f32_16x16x32_bf16 v[56:59], a[36:39], a[28:31], v[56:59]\n" \
"ds_read_b64_tr_b16 a[218:219], v222 offset:21504\n" \
"v_mul_f32 v119, v119, v233\n" \
"v_mfma_f32_16x16x32_bf16 v[16:19], a[40:43], a[24:27], v[16:19]\n" \
"ds_read_b64_tr_b16 a[220:221], v222 offset:30720\n" \
"v_cvt_pk_bf16_f32 v120, v112, v113\n" \
"v_mfma_f32_16x16x32_bf16 v[16:19], a[44:47], a[28:31], v[16:19]\n" \
"v_cvt_pk_bf16_f32 v121, v114, v115\n" \
"v_cvt_pk_bf16_f32 v122, v116, v117\n" \
"v_cvt_pk_bf16_f32 v123, v118, v119\n" \
"v_mfma_f32_16x16x32_bf16 v[20:23], a[48:51], a[24:27], v[20:23]\n" \
"ds_read_b64_tr_b16 a[222:223], v222 offset:33792\n" \
"v_mfma_f32_16x16x32_bf16 v[20:23], a[52:55], a[28:31], v[20:23]\n" \
"buffer_store_dwordx4 v[120:123], v236, s[56:59], s82 offen offset:0 sc1\n" \
"v_mfma_f32_16x16x32_bf16 v[28:31], a[48:51], a[192:195], v[28:31]\n" \
"v_mfma_f32_16x16x32_bf16 v[28:31], a[52:55], a[196:199], v[28:31]\n" \
"v_mfma_f32_16x16x32_bf16 v[224:227], v[244:247], v[120:123], v[224:227]\n" \
"v_accvgpr_read_b32 v112, a104\n" \
"v_accvgpr_read_b32 v113, a105\n" \
"v_mfma_f32_16x16x32_bf16 v[24:27], a[40:43], a[192:195], v[24:27]\n" \
"v_accvgpr_read_b32 v114, a106\n" \
"v_accvgpr_read_b32 v115, a107\n" \
"v_mfma_f32_16x16x32_bf16 v[24:27], a[44:47], a[196:199], v[24:27]\n" \
"v_accvgpr_read_b32 v116, a108\n" \
"v_accvgpr_read_b32 v117, a109\n" \
"v_mfma_f32_16x16x32_bf16 v[64:67], a[32:35], a[192:195], v[64:67]\n" \
"v_accvgpr_read_b32 v118, a110\n" \
"v_accvgpr_read_b32 v119, a111\n" \
"v_mfma_f32_16x16x32_bf16 v[64:67], a[36:39], a[196:199], v[64:67]\n" \
"v_mfma_f32_16x16x32_bf16 a[120:123], v[162:165], v[156:159], a[120:123]\n" \
"v_mul_f32 v112, v112, v233\n" \
"s_waitcnt vmcnt(14) lgkmcnt(0)\n" \
"s_barrier\n" \
"v_mfma_f32_16x16x32_bf16 v[68:71], a[200:203], a[192:195], v[68:71]\n" \
"ds_read_b64_tr_b16 a[32:33], v209 offset:25600\n" \
"ds_read_b64_tr_b16 a[34:35], v209 offset:28672\n" \
"v_mfma_f32_16x16x32_bf16 v[68:71], a[204:207], a[196:199], v[68:71]\n" \
"ds_read_b64_tr_b16 a[36:37], v209 offset:37888\n" \
"ds_read_b64_tr_b16 a[38:39], v209 offset:40960\n" \
"v_mfma_f32_16x16x32_bf16 v[60:63], a[200:203], a[24:27], v[60:63]\n" \
"s_add_u32 m0, s79, 0\n" \
"ds_read_b64_tr_b16 a[40:41], v209 offset:24576\n" \
"v_mul_f32 v113, v113, v233\n" \
"v_mfma_f32_16x16x32_bf16 v[60:63], a[204:207], a[28:31], v[60:63]\n" \
"buffer_load_dwordx4 v192, s[44:47], s73 offen offset:0 lds\n" \
"v_mfma_f32_16x16x32_bf16 v[52:55], a[200:203], a[16:19], v[52:55]\n" \
"ds_read_b64_tr_b16 a[42:43], v209 offset:27648\n" \
"v_mul_f32 v114, v114, v233\n" \
"v_mfma_f32_16x16x32_bf16 v[52:55], a[204:207], a[20:23], v[52:55]\n" \
"ds_read_b64_tr_b16 a[44:45], v209 offset:36864\n" \
"v_mul_f32 v115, v115, v233\n" \
"v_mfma_f32_16x16x32_bf16 a[124:127], v[160:163], v[156:159], a[124:127]\n" \
"v_mul_f32 v116, v116, v233\n" \
"v_mul_f32 v117, v117, v233\n" \
"v_mul_f32 v118, v118, v233\n" \
"v_mfma_f32_16x16x32_bf16 v[44:47], a[200:203], a[8:11], v[44:47]\n" \
"ds_read_b64_tr_b16 a[46:47], v209 offset:39936\n" \
"v_mul_f32 v119, v119, v233\n" \
"v_mfma_f32_16x16x32_bf16 v[44:47], a[204:207], a[12:15], v[44:47]\n" \
"buffer_load_dwordx4 v193, s[44:47], s73 offen offset:1024 lds\n" \
"v_mfma_f32_16x16x32_bf16 v[72:75], a[0:3], a[208:211], v[72:75]\n" \
"ds_read_b64_tr_b16 a[48:49], v210 offset:24576\n" \
"v_cvt_pk_bf16_f32 v124, v112, v113\n" \
"v_mfma_f32_16x16x32_bf16 v[72:75], a[4:7], a[212:215], v[72:75]\n" \
"ds_read_b128 a[0:3], v202 offset:1024\n" \
"v_cvt_pk_bf16_f32 v125, v114, v115\n" \
"v_mfma_f32_16x16x32_bf16 v[80:83], a[8:11], a[208:211], v[80:83]\n" \
"ds_read_b128 a[4:7], v203 offset:1024\n" \
"v_cvt_pk_bf16_f32 v126, v116, v117\n" \
"v_mfma_f32_16x16x32_bf16 v[80:83], a[12:15], a[212:215], v[80:83]\n" \
"buffer_load_dwordx4 v192, s[44:47], s73 offen offset:2048 lds\n" \
"v_cvt_pk_bf16_f32 v127, v118, v119\n" \
"v_mfma_f32_16x16x32_bf16 v[84:87], a[8:11], a[216:219], v[84:87]\n" \
"ds_read_b64_tr_b16 a[50:51], v210 offset:27648\n" \
"v_mfma_f32_16x16x32_bf16 v[84:87], a[12:15], a[220:223], v[84:87]\n" \
"ds_read_b128 a[8:11], v200 offset:40960\n" \
"buffer_store_dwordx4 v[124:127], v236, s[56:59], s82 offen offset:2048 sc1\n" \
"v_mfma_f32_16x16x32_bf16 v[92:95], a[16:19], a[216:219], v[92:95]\n" \
"ds_read_b128 a[12:15], v201 offset:40960\n" \
"v_mfma_f32_16x16x32_bf16 v[92:95], a[20:23], a[220:223], v[92:95]\n" \
"buffer_load_dwordx4 v193, s[44:47], s73 offen offset:3072 lds\n" \
"v_mfma_f32_16x16x32_bf16 v[224:227], v[244:247], v[124:127], v[224:227]\n" \
"v_accvgpr_read_b32 v112, a112\n" \
"v_accvgpr_read_b32 v113, a113\n" \
"v_mfma_f32_16x16x32_bf16 v[88:91], a[16:19], a[208:211], v[88:91]\n" \
"s_xor_b32 s73, s73, 0x80\n" \
"s_mul_i32 s76, s80, 393216\n" \
"ds_read_b64_tr_b16 a[52:53], v210 offset:36864\n" \
"v_mfma_f32_16x16x32_bf16 v[88:91], a[20:23], a[212:215], v[88:91]\n" \
"s_add_u32 s76, s76, s78\n" \
"ds_read_b128 a[16:19], v200 offset:45056\n" \
"v_accvgpr_read_b32 v114, a114\n" \
"v_mfma_f32_16x16x32_bf16 v[96:99], a[24:27], a[208:211], v[96:99]\n" \
"s_add_u32 m0, s91, 16384\n" \
"ds_read_b128 a[20:23], v201 offset:45056\n" \
"v_accvgpr_read_b32 v115, a115\n" \
"v_mfma_f32_16x16x32_bf16 v[96:99], a[28:31], a[212:215], v[96:99]\n" \
"buffer_load_dwordx4 v197, s[48:51], s76 offen offset:0 lds\n" \
"v_accvgpr_read_b32 v116, a116\n" \
"v_mfma_f32_16x16x32_bf16 v[100:103], a[24:27], a[216:219], v[100:103]\n" \
"ds_read_b64_tr_b16 a[54:55], v210 offset:39936\n" \
"v_accvgpr_read_b32 v117, a117\n" \
"v_mfma_f32_16x16x32_bf16 v[100:103], a[28:31], a[220:223], v[100:103]\n" \
"ds_read_b128 a[24:27], v200 offset:49152\n" \
"v_accvgpr_read_b32 v118, a118\n" \
"v_mfma_f32_16x16x32_bf16 v[108:111], a[192:195], a[216:219], v[108:111]\n" \
"s_add_u32 s77, s76, 24576\n" \
"ds_read_b128 a[28:31], v201 offset:49152\n" \
"v_accvgpr_read_b32 v119, a119\n" \
"v_mfma_f32_16x16x32_bf16 v[108:111], a[196:199], a[220:223], v[108:111]\n" \
"buffer_load_dwordx4 v198, s[48:51], s76 offen offset:1024 lds\n" \
"v_mfma_f32_16x16x32_bf16 v[104:107], a[192:195], a[208:211], v[104:107]\n" \
"v_mul_f32 v112, v112, v233\n" \
"v_mul_f32 v113, v113, v233\n" \
"v_mfma_f32_16x16x32_bf16 v[104:107], a[196:199], a[212:215], v[104:107]\n" \
"s_waitcnt lgkmcnt(2)\n" \
"v_mul_f32 v114, v114, v233\n" \
"v_mul_f32 v115, v115, v233\n" \
"v_mul_f32 v116, v116, v233\n" \
"v_mfma_f32_16x16x32_bf16 v[32:35], a[32:35], a[0:3], v[32:35]\n" \
"s_xor_b32 s80, s80, 0x1\n" \
"ds_read_b128 a[192:195], v200 offset:53248\n" \
"v_mul_f32 v117, v117, v233\n" \
"v_mfma_f32_16x16x32_bf16 v[32:35], a[36:39], a[4:7], v[32:35]\n" \
"buffer_load_dwordx4 v199, s[48:51], s76 offen offset:2048 lds\n" \
"v_mfma_f32_16x16x32_bf16 v[40:43], a[32:35], a[8:11], v[40:43]\n" \
"ds_read_b128 a[196:199], v201 offset:53248\n" \
"v_mul_f32 v118, v118, v233\n" \
"v_mfma_f32_16x16x32_bf16 v[40:43], a[36:39], a[12:15], v[40:43]\n" \
"ds_read_b64_tr_b16 a[200:201], v210 offset:25600\n" \
"v_mul_f32 v119, v119, v233\n" \
"v_mfma_f32_16x16x32_bf16 v[0:3], a[40:43], a[8:11], v[0:3]\n" \
"s_add_u32 m0, m0, 3072\n" \
"ds_read_b64_tr_b16 a[202:203], v210 offset:28672\n" \
"v_cvt_pk_bf16_f32 v120, v112, v113\n" \
"v_mfma_f32_16x16x32_bf16 v[0:3], a[44:47], a[12:15], v[0:3]\n" \
"buffer_load_dwordx4 v197, s[48:51], s77 offen offset:0 lds\n" \
"v_cvt_pk_bf16_f32 v121, v114, v115\n" \
"v_mfma_f32_16x16x32_bf16 v[4:7], a[48:51], a[8:11], v[4:7]\n" \
"ds_read_b64_tr_b16 a[204:205], v210 offset:37888\n" \
"v_cvt_pk_bf16_f32 v122, v116, v117\n" \
"v_mfma_f32_16x16x32_bf16 v[4:7], a[52:55], a[12:15], v[4:7]\n" \
"ds_read_b64_tr_b16 a[206:207], v210 offset:40960\n" \
"v_cvt_pk_bf16_f32 v123, v118, v119\n" \
"v_mfma_f32_16x16x32_bf16 v[12:15], a[48:51], a[16:19], v[12:15]\n" \
"ds_read_b64_tr_b16 a[208:209], v209 offset:26624\n" \
"v_mfma_f32_16x16x32_bf16 v[12:15], a[52:55], a[20:23], v[12:15]\n" \
"buffer_load_dwordx4 v198, s[48:51], s77 offen offset:1024 lds\n" \
"v_mfma_f32_16x16x32_bf16 v[8:11], a[40:43], a[16:19], v[8:11]\n" \
"Lpg7941_%=:\n" \
"Lst1x0_%=:\n" \
"s_cbranch_execz Lpg10225_%=\n" \
"ds_read_b64_tr_b16 a[210:211], v209 offset:29696\n" \
"v_mfma_f32_16x16x32_bf16 v[8:11], a[44:47], a[20:23], v[8:11]\n" \
"ds_read_b64_tr_b16 a[212:213], v209 offset:38912\n" \
"buffer_store_dwordx4 v[120:123], v240, s[56:59], s82 offen offset:0 sc1\n" \
"v_mfma_f32_16x16x32_bf16 v[48:51], a[32:35], a[16:19], v[48:51]\n" \
"s_waitcnt lgkmcnt(3)\n" \
"ds_read_b64_tr_b16 a[214:215], v209 offset:41984\n" \
"v_mfma_f32_16x16x32_bf16 v[48:51], a[36:39], a[20:23], v[48:51]\n" \
"buffer_load_dwordx4 v199, s[48:51], s77 offen offset:2048 lds\n" \
"v_mfma_f32_16x16x32_bf16 v[224:227], v[244:247], v[120:123], v[224:227]\n" \
"v_accvgpr_read_b32 v112, a120\n" \
"v_accvgpr_read_b32 v113, a121\n" \
"v_accvgpr_read_b32 v114, a122\n" \
"v_mfma_f32_16x16x32_bf16 v[56:59], a[32:35], a[24:27], v[56:59]\n" \
"ds_read_b64_tr_b16 a[216:217], v210 offset:26624\n" \
"v_accvgpr_read_b32 v115, a123\n" \
"v_mfma_f32_16x16x32_bf16 v[56:59], a[36:39], a[28:31], v[56:59]\n" \
"ds_read_b64_tr_b16 a[218:219], v210 offset:29696\n" \
"v_accvgpr_read_b32 v116, a124\n" \
"v_mfma_f32_16x16x32_bf16 v[16:19], a[40:43], a[24:27], v[16:19]\n" \
"ds_read_b64_tr_b16 a[220:221], v210 offset:38912\n" \
"v_accvgpr_read_b32 v117, a125\n" \
"v_mfma_f32_16x16x32_bf16 v[16:19], a[44:47], a[28:31], v[16:19]\n" \
"v_accvgpr_read_b32 v118, a126\n" \
"v_accvgpr_read_b32 v119, a127\n" \
"v_mfma_f32_16x16x32_bf16 v[20:23], a[48:51], a[24:27], v[20:23]\n" \
"ds_read_b64_tr_b16 a[222:223], v210 offset:41984\n" \
"v_mul_f32 v112, v112, v233\n" \
"v_mfma_f32_16x16x32_bf16 v[20:23], a[52:55], a[28:31], v[20:23]\n" \
"v_mul_f32 v113, v113, v233\n" \
"v_mul_f32 v114, v114, v233\n" \
"v_mul_f32 v115, v115, v233\n" \
"v_mfma_f32_16x16x32_bf16 v[28:31], a[48:51], a[192:195], v[28:31]\n" \
"v_mul_f32 v116, v116, v233\n" \
"v_mul_f32 v117, v117, v233\n" \
"v_mfma_f32_16x16x32_bf16 v[28:31], a[52:55], a[196:199], v[28:31]\n" \
"v_mul_f32_e64 v118, v118, v233\n" \
"v_mul_f32_e64 v119, v119, v233\n" \
"v_mfma_f32_16x16x32_bf16 v[24:27], a[40:43], a[192:195], v[24:27]\n" \
"v_cvt_pk_bf16_f32 v124, v112, v113\n" \
"v_cvt_pk_bf16_f32 v125, v114, v115\n" \
"v_mfma_f32_16x16x32_bf16 v[24:27], a[44:47], a[196:199], v[24:27]\n" \
"v_cvt_pk_bf16_f32 v126, v116, v117\n" \
"v_cvt_pk_bf16_f32 v127, v118, v119\n" \
"v_mfma_f32_16x16x32_bf16 v[64:67], a[32:35], a[192:195], v[64:67]\n" \
"buffer_store_dwordx4 v[124:127], v240, s[56:59], s82 offen offset:2048 sc1\n" \
"v_mfma_f32_16x16x32_bf16 v[64:67], a[36:39], a[196:199], v[64:67]\n" \
"v_mfma_f32_16x16x32_bf16 v[224:227], v[244:247], v[124:127], v[224:227]\n" \
"s_waitcnt vmcnt(14) lgkmcnt(0)\n" \
"s_barrier\n" \
"v_mfma_f32_16x16x32_bf16 v[68:71], a[200:203], a[192:195], v[68:71]\n" \
"ds_read_b64_tr_b16 a[32:33], v209 offset:1024\n" \
"ds_read_b64_tr_b16 a[34:35], v209 offset:4096\n" \
"v_mfma_f32_16x16x32_bf16 v[68:71], a[204:207], a[196:199], v[68:71]\n" \
"ds_read_b64_tr_b16 a[36:37], v209 offset:13312\n" \
"ds_read_b64_tr_b16 a[38:39], v209 offset:16384\n" \
"v_mfma_f32_16x16x32_bf16 v[60:63], a[200:203], a[24:27], v[60:63]\n" \
"s_add_u32 m0, s79, 40960\n" \
"ds_read_b64_tr_b16 a[40:41], v209 offset:0\n" \
"v_mfma_f32_16x16x32_bf16 v[60:63], a[204:207], a[28:31], v[60:63]\n" \
"buffer_load_dwordx4 v192, s[44:47], s73 offen offset:0 lds\n" \
"v_mfma_f32_16x16x32_bf16 v[52:55], a[200:203], a[16:19], v[52:55]\n" \
"ds_read_b64_tr_b16 a[42:43], v209 offset:3072\n" \
"v_mfma_f32_16x16x32_bf16 v[52:55], a[204:207], a[20:23], v[52:55]\n" \
"ds_read_b64_tr_b16 a[44:45], v209 offset:12288\n" \
"v_mfma_f32_16x16x32_bf16 v[44:47], a[200:203], a[8:11], v[44:47]\n" \
"ds_read_b64_tr_b16 a[46:47], v209 offset:15360\n" \
"v_mfma_f32_16x16x32_bf16 v[44:47], a[204:207], a[12:15], v[44:47]\n" \
"buffer_load_dwordx4 v193, s[44:47], s73 offen offset:1024 lds\n" \
"v_mfma_f32_16x16x32_bf16 v[72:75], a[0:3], a[208:211], v[72:75]\n" \
"ds_read_b64_tr_b16 a[48:49], v210 offset:0\n" \
"v_mfma_f32_16x16x32_bf16 v[72:75], a[4:7], a[212:215], v[72:75]\n" \
"ds_read_b128 a[0:3], v202 offset:2048\n" \
"v_mfma_f32_16x16x32_bf16 v[80:83], a[8:11], a[208:211], v[80:83]\n" \
"ds_read_b128 a[4:7], v203 offset:2048\n" \
"v_mfma_f32_16x16x32_bf16 v[80:83], a[12:15], a[212:215], v[80:83]\n" \
"buffer_load_dwordx4 v192, s[44:47], s73 offen offset:2048 lds\n" \
"v_mfma_f32_16x16x32_bf16 v[84:87], a[8:11], a[216:219], v[84:87]\n" \
"ds_read_b64_tr_b16 a[50:51], v210 offset:3072\n" \
"v_mfma_f32_16x16x32_bf16 v[84:87], a[12:15], a[220:223], v[84:87]\n" \
"ds_read_b128 a[8:11], v205 offset:0\n" \
"v_mfma_f32_16x16x32_bf16 v[92:95], a[16:19], a[216:219], v[92:95]\n" \
"ds_read_b128 a[12:15], v206 offset:0\n" \
"v_mfma_f32_16x16x32_bf16 v[92:95], a[20:23], a[220:223], v[92:95]\n" \
"buffer_load_dwordx4 v193, s[44:47], s73 offen offset:3072 lds\n" \
"v_mfma_f32_16x16x32_bf16 v[88:91], a[16:19], a[208:211], v[88:91]\n" \
"s_xor_b32 s73, s73, 0x180\n" \
"s_mul_i32 s76, s80, 393216\n" \
"ds_read_b64_tr_b16 a[52:53], v210 offset:12288\n" \
"v_mfma_f32_16x16x32_bf16 v[88:91], a[20:23], a[212:215], v[88:91]\n" \
"s_add_u32 s76, s76, s78\n" \
"ds_read_b128 a[16:19], v205 offset:4096\n" \
"v_mfma_f32_16x16x32_bf16 v[96:99], a[24:27], a[208:211], v[96:99]\n" \
"s_add_u32 m0, s91, 106496\n" \
"ds_read_b128 a[20:23], v206 offset:4096\n" \
"v_mfma_f32_16x16x32_bf16 v[96:99], a[28:31], a[212:215], v[96:99]\n" \
"buffer_load_dwordx4 v197, s[48:51], s76 offen offset:0 lds\n" \
"v_mfma_f32_16x16x32_bf16 v[100:103], a[24:27], a[216:219], v[100:103]\n" \
"ds_read_b64_tr_b16 a[54:55], v210 offset:15360\n" \
"v_mfma_f32_16x16x32_bf16 v[100:103], a[28:31], a[220:223], v[100:103]\n" \
"ds_read_b128 a[24:27], v205 offset:8192\n" \
"v_mfma_f32_16x16x32_bf16 v[108:111], a[192:195], a[216:219], v[108:111]\n" \
"s_add_u32 s77, s76, 24576\n" \
"ds_read_b128 a[28:31], v206 offset:8192\n" \
"v_mfma_f32_16x16x32_bf16 v[108:111], a[196:199], a[220:223], v[108:111]\n" \
"buffer_load_dwordx4 v198, s[48:51], s76 offen offset:1024 lds\n" \
"v_mfma_f32_16x16x32_bf16 v[104:107], a[192:195], a[208:211], v[104:107]\n" \
"v_mfma_f32_16x16x32_bf16 v[104:107], a[196:199], a[212:215], v[104:107]\n" \
"s_waitcnt lgkmcnt(2)\n" \
"v_mfma_f32_16x16x32_bf16 v[32:35], a[32:35], a[0:3], v[32:35]\n" \
"s_xor_b32 s80, s80, 0x3\n" \
"ds_read_b128 a[192:195], v205 offset:12288\n" \
"v_mfma_f32_16x16x32_bf16 v[32:35], a[36:39], a[4:7], v[32:35]\n" \
"buffer_load_dwordx4 v199, s[48:51], s76 offen offset:2048 lds\n" \
"v_mfma_f32_16x16x32_bf16 v[40:43], a[32:35], a[8:11], v[40:43]\n" \
"ds_read_b128 a[196:199], v206 offset:12288\n" \
"v_mfma_f32_16x16x32_bf16 v[40:43], a[36:39], a[12:15], v[40:43]\n" \
"ds_read_b64_tr_b16 a[200:201], v210 offset:1024\n" \
"v_mfma_f32_16x16x32_bf16 v[0:3], a[40:43], a[8:11], v[0:3]\n" \
"s_add_u32 m0, m0, 3072\n" \
"ds_read_b64_tr_b16 a[202:203], v210 offset:4096\n" \
"v_mfma_f32_16x16x32_bf16 v[0:3], a[44:47], a[12:15], v[0:3]\n" \
"buffer_load_dwordx4 v197, s[48:51], s77 offen offset:0 lds\n" \
"v_mfma_f32_16x16x32_bf16 v[4:7], a[48:51], a[8:11], v[4:7]\n" \
"ds_read_b64_tr_b16 a[204:205], v210 offset:13312\n" \
"v_mfma_f32_16x16x32_bf16 v[4:7], a[52:55], a[12:15], v[4:7]\n" \
"ds_read_b64_tr_b16 a[206:207], v210 offset:16384\n" \
"v_mfma_f32_16x16x32_bf16 v[12:15], a[48:51], a[16:19], v[12:15]\n" \
"ds_read_b64_tr_b16 a[208:209], v209 offset:2048\n" \
"v_mfma_f32_16x16x32_bf16 v[12:15], a[52:55], a[20:23], v[12:15]\n" \
"buffer_load_dwordx4 v198, s[48:51], s77 offen offset:1024 lds\n" \
"v_mfma_f32_16x16x32_bf16 v[8:11], a[40:43], a[16:19], v[8:11]\n" \
"ds_read_b64_tr_b16 a[210:211], v209 offset:5120\n" \
"v_mfma_f32_16x16x32_bf16 v[8:11], a[44:47], a[20:23], v[8:11]\n" \
"ds_read_b64_tr_b16 a[212:213], v209 offset:14336\n" \
"v_mfma_f32_16x16x32_bf16 v[48:51], a[32:35], a[16:19], v[48:51]\n" \
"s_waitcnt lgkmcnt(3)\n" \
"ds_read_b64_tr_b16 a[214:215], v209 offset:17408\n" \
"v_mfma_f32_16x16x32_bf16 v[48:51], a[36:39], a[20:23], v[48:51]\n" \
"buffer_load_dwordx4 v199, s[48:51], s77 offen offset:2048 lds\n" \
"v_mfma_f32_16x16x32_bf16 v[56:59], a[32:35], a[24:27], v[56:59]\n" \
"ds_read_b64_tr_b16 a[216:217], v210 offset:2048\n" \
"v_mfma_f32_16x16x32_bf16 v[56:59], a[36:39], a[28:31], v[56:59]\n" \
"ds_read_b64_tr_b16 a[218:219], v210 offset:5120\n" \
"v_mfma_f32_16x16x32_bf16 v[16:19], a[40:43], a[24:27], v[16:19]\n" \
"ds_read_b64_tr_b16 a[220:221], v210 offset:14336\n" \
"v_mfma_f32_16x16x32_bf16 v[16:19], a[44:47], a[28:31], v[16:19]\n" \
"v_mfma_f32_16x16x32_bf16 v[20:23], a[48:51], a[24:27], v[20:23]\n" \
"ds_read_b64_tr_b16 a[222:223], v210 offset:17408\n" \
"v_mfma_f32_16x16x32_bf16 v[20:23], a[52:55], a[28:31], v[20:23]\n" \
"v_mfma_f32_16x16x32_bf16 v[28:31], a[48:51], a[192:195], v[28:31]\n" \
"v_mfma_f32_16x16x32_bf16 v[28:31], a[52:55], a[196:199], v[28:31]\n" \
"v_mfma_f32_16x16x32_bf16 v[24:27], a[40:43], a[192:195], v[24:27]\n" \
"v_mfma_f32_16x16x32_bf16 v[24:27], a[44:47], a[196:199], v[24:27]\n" \
"v_mfma_f32_16x16x32_bf16 v[64:67], a[32:35], a[192:195], v[64:67]\n" \
"v_mfma_f32_16x16x32_bf16 v[64:67], a[36:39], a[196:199], v[64:67]\n" \
"s_waitcnt vmcnt(11) lgkmcnt(0)\n" \
"s_barrier\n" \
"s_movk_i32 s64, 0x207\n" \
"v_cmp_class_f32 vcc, v224, s64\n" \
"s_nop 1\n" \
"s_cmp_lg_u64 vcc, 0\n" \
"s_addc_u32 s90, s90, 0\n" \
"s_cmp_lg_u32 s90, 0\n" \
"s_cbranch_scc1 Lexact_%=\n" \
"Lexr_%=:\n" \
"v_mfma_f32_16x16x32_bf16 v[68:71], a[200:203], a[192:195], v[68:71]\n" \
"s_lshl_b32 s15, s63, 7\n" \
"ds_read_b64_tr_b16 a[32:33], v204 offset:17408\n" \
"ds_read_b64_tr_b16 a[34:35], v204 offset:20480\n" \
"v_mfma_f32_16x16x32_bf16 v[68:71], a[204:207], a[196:199], v[68:71]\n" \
"s_lshl2_add_u32 s16, s62, 65589248\n" \
"ds_read_b64_tr_b16 a[36:37], v204 offset:29696\n" \
"ds_read_b64_tr_b16 a[38:39], v204 offset:32768\n" \
"v_mfma_f32_16x16x32_bf16 v[60:63], a[200:203], a[24:27], v[60:63]\n" \
"s_add_u32 m0, s79, 65536\n" \
"s_sub_u32 s17, s16, s15\n" \
"ds_read_b64_tr_b16 a[40:41], v204 offset:16384\n" \
"v_mfma_f32_16x16x32_bf16 v[60:63], a[204:207], a[28:31], v[60:63]\n" \
"buffer_load_dwordx4 v192, s[44:47], s73 offen offset:0 lds\n" \
"v_mfma_f32_16x16x32_bf16 v[52:55], a[200:203], a[16:19], v[52:55]\n" \
"s_add_u32 s15, s15, s16\n" \
"s_add_u32 s17, s17, 1920\n" \
"ds_read_b64_tr_b16 a[42:43], v204 offset:19456\n" \
"v_mfma_f32_16x16x32_bf16 v[52:55], a[204:207], a[20:23], v[52:55]\n" \
"ds_read_b64_tr_b16 a[44:45], v204 offset:28672\n" \
"v_mov_b32 v146, s60\n" \
"v_mfma_f32_16x16x32_bf16 v[44:47], a[200:203], a[8:11], v[44:47]\n" \
"ds_read_b64_tr_b16 a[46:47], v204 offset:31744\n" \
"v_lshlrev_b32 v147, 28, s61\n" \
"v_mfma_f32_16x16x32_bf16 v[44:47], a[204:207], a[12:15], v[44:47]\n" \
"buffer_load_dwordx4 v193, s[44:47], s73 offen offset:1024 lds\n" \
"v_mfma_f32_16x16x32_bf16 v[72:75], a[0:3], a[208:211], v[72:75]\n" \
"ds_read_b64_tr_b16 a[48:49], v222 offset:16384\n" \
"v_mfma_f32_16x16x32_bf16 v[72:75], a[4:7], a[212:215], v[72:75]\n" \
"ds_read_b128 a[0:3], v202 offset:4096\n" \
"v_mfma_f32_16x16x32_bf16 v[80:83], a[8:11], a[208:211], v[80:83]\n" \
"ds_read_b128 a[4:7], v203 offset:4096\n" \
"v_mfma_f32_16x16x32_bf16 v[80:83], a[12:15], a[212:215], v[80:83]\n" \
"buffer_load_dwordx4 v192, s[44:47], s73 offen offset:2048 lds\n" \
"v_mfma_f32_16x16x32_bf16 v[84:87], a[8:11], a[216:219], v[84:87]\n" \
"ds_read_b64_tr_b16 a[50:51], v222 offset:19456\n" \
"v_mfma_f32_16x16x32_bf16 v[84:87], a[12:15], a[220:223], v[84:87]\n" \
"ds_read_b128 a[8:11], v200 offset:0\n" \
"v_mfma_f32_16x16x32_bf16 v[92:95], a[16:19], a[216:219], v[92:95]\n" \
"ds_read_b128 a[12:15], v201 offset:0\n" \
"v_mfma_f32_16x16x32_bf16 v[92:95], a[20:23], a[220:223], v[92:95]\n" \
"buffer_load_dwordx4 v193, s[44:47], s73 offen offset:3072 lds\n" \
"v_mfma_f32_16x16x32_bf16 v[88:91], a[16:19], a[208:211], v[88:91]\n" \
"s_xor_b32 s73, s73, 0x80\n" \
"s_mul_i32 s76, s80, 393216\n" \
"ds_read_b64_tr_b16 a[52:53], v222 offset:28672\n" \
"v_mfma_f32_16x16x32_bf16 v[88:91], a[20:23], a[212:215], v[88:91]\n" \
"s_add_u32 s76, s76, s78\n" \
"ds_read_b128 a[16:19], v200 offset:4096\n" \
"v_mfma_f32_16x16x32_bf16 v[96:99], a[24:27], a[208:211], v[96:99]\n" \
"s_add_u32 m0, s91, 81920\n" \
"ds_read_b128 a[20:23], v201 offset:4096\n" \
"v_mfma_f32_16x16x32_bf16 v[96:99], a[28:31], a[212:215], v[96:99]\n" \
"buffer_load_dwordx4 v197, s[48:51], s76 offen offset:0 lds\n" \
"v_mfma_f32_16x16x32_bf16 v[100:103], a[24:27], a[216:219], v[100:103]\n" \
"ds_read_b64_tr_b16 a[54:55], v222 offset:31744\n" \
"v_mfma_f32_16x16x32_bf16 v[100:103], a[28:31], a[220:223], v[100:103]\n" \
"ds_read_b128 a[24:27], v200 offset:8192\n" \
"v_mfma_f32_16x16x32_bf16 v[108:111], a[192:195], a[216:219], v[108:111]\n" \
"s_add_u32 s77, s76, 24576\n" \
"ds_read_b128 a[28:31], v201 offset:8192\n" \
"v_mfma_f32_16x16x32_bf16 v[108:111], a[196:199], a[220:223], v[108:111]\n" \
"buffer_load_dwordx4 v198, s[48:51], s76 offen offset:1024 lds\n" \
"v_mfma_f32_16x16x32_bf16 v[104:107], a[192:195], a[208:211], v[104:107]\n" \
"v_mfma_f32_16x16x32_bf16 v[104:107], a[196:199], a[212:215], v[104:107]\n" \
"s_waitcnt lgkmcnt(2)\n" \
"v_mfma_f32_16x16x32_bf16 v[32:35], a[32:35], a[0:3], v[32:35]\n" \
"s_xor_b32 s80, s80, 0x1\n" \
"ds_read_b128 a[192:195], v200 offset:12288\n" \
"v_mfma_f32_16x16x32_bf16 v[32:35], a[36:39], a[4:7], v[32:35]\n" \
"buffer_load_dwordx4 v199, s[48:51], s76 offen offset:2048 lds\n" \
"v_mfma_f32_16x16x32_bf16 v[40:43], a[32:35], a[8:11], v[40:43]\n" \
"ds_read_b128 a[196:199], v201 offset:12288\n" \
"v_mfma_f32_16x16x32_bf16 v[40:43], a[36:39], a[12:15], v[40:43]\n" \
"ds_read_b64_tr_b16 a[200:201], v222 offset:17408\n" \
"v_mfma_f32_16x16x32_bf16 v[0:3], a[40:43], a[8:11], v[0:3]\n" \
"s_add_u32 m0, m0, 3072\n" \
"ds_read_b64_tr_b16 a[202:203], v222 offset:20480\n" \
"v_mfma_f32_16x16x32_bf16 v[0:3], a[44:47], a[12:15], v[0:3]\n" \
"buffer_load_dwordx4 v197, s[48:51], s77 offen offset:0 lds\n" \
"v_mfma_f32_16x16x32_bf16 v[4:7], a[48:51], a[8:11], v[4:7]\n" \
"ds_read_b64_tr_b16 a[204:205], v222 offset:29696\n" \
"v_mfma_f32_16x16x32_bf16 v[4:7], a[52:55], a[12:15], v[4:7]\n" \
"ds_read_b64_tr_b16 a[206:207], v222 offset:32768\n" \
"v_mfma_f32_16x16x32_bf16 v[12:15], a[48:51], a[16:19], v[12:15]\n" \
"ds_read_b64_tr_b16 a[208:209], v204 offset:18432\n" \
"v_mfma_f32_16x16x32_bf16 v[12:15], a[52:55], a[20:23], v[12:15]\n" \
"buffer_load_dwordx4 v198, s[48:51], s77 offen offset:1024 lds\n" \
"v_mfma_f32_16x16x32_bf16 v[8:11], a[40:43], a[16:19], v[8:11]\n" \
"ds_read_b64_tr_b16 a[210:211], v204 offset:21504\n" \
"v_mfma_f32_16x16x32_bf16 v[8:11], a[44:47], a[20:23], v[8:11]\n" \
"ds_read_b64_tr_b16 a[212:213], v204 offset:30720\n" \
"v_mfma_f32_16x16x32_bf16 v[48:51], a[32:35], a[16:19], v[48:51]\n" \
"s_waitcnt lgkmcnt(3)\n" \
"ds_read_b64_tr_b16 a[214:215], v204 offset:33792\n" \
"v_mfma_f32_16x16x32_bf16 v[48:51], a[36:39], a[20:23], v[48:51]\n" \
"buffer_load_dwordx4 v199, s[48:51], s77 offen offset:2048 lds\n" \
"v_mfma_f32_16x16x32_bf16 v[56:59], a[32:35], a[24:27], v[56:59]\n" \
"ds_read_b64_tr_b16 a[216:217], v222 offset:18432\n" \
"v_mfma_f32_16x16x32_bf16 v[56:59], a[36:39], a[28:31], v[56:59]\n" \
"ds_read_b64_tr_b16 a[218:219], v222 offset:21504\n" \
"v_mfma_f32_16x16x32_bf16 v[16:19], a[40:43], a[24:27], v[16:19]\n" \
"ds_read_b64_tr_b16 a[220:221], v222 offset:30720\n" \
"v_mfma_f32_16x16x32_bf16 v[16:19], a[44:47], a[28:31], v[16:19]\n" \
"v_mfma_f32_16x16x32_bf16 v[20:23], a[48:51], a[24:27], v[20:23]\n" \
"ds_read_b64_tr_b16 a[222:223], v222 offset:33792\n" \
"v_mfma_f32_16x16x32_bf16 v[20:23], a[52:55], a[28:31], v[20:23]\n" \
"v_mfma_f32_16x16x32_bf16 v[28:31], a[48:51], a[192:195], v[28:31]\n" \
"v_mfma_f32_16x16x32_bf16 v[28:31], a[52:55], a[196:199], v[28:31]\n" \
"v_mfma_f32_16x16x32_bf16 v[24:27], a[40:43], a[192:195], v[24:27]\n" \
"v_mfma_f32_16x16x32_bf16 v[24:27], a[44:47], a[196:199], v[24:27]\n" \
"v_mfma_f32_16x16x32_bf16 v[64:67], a[32:35], a[192:195], v[64:67]\n" \
"v_mfma_f32_16x16x32_bf16 v[64:67], a[36:39], a[196:199], v[64:67]\n" \
"s_waitcnt vmcnt(10) lgkmcnt(0)\n" \
"s_barrier\n" \
"v_mfma_f32_16x16x32_bf16 v[68:71], a[200:203], a[192:195], v[68:71]\n" \
"ds_read_b64_tr_b16 a[32:33], v209 offset:25600\n" \
"ds_read_b64_tr_b16 a[34:35], v209 offset:28672\n" \
"v_mfma_f32_16x16x32_bf16 v[68:71], a[204:207], a[196:199], v[68:71]\n" \
"ds_read_b64_tr_b16 a[36:37], v209 offset:37888\n" \
"ds_read_b64_tr_b16 a[38:39], v209 offset:40960\n" \
"v_mfma_f32_16x16x32_bf16 v[60:63], a[200:203], a[24:27], v[60:63]\n" \
"s_add_u32 m0, s79, 0\n" \
"ds_read_b64_tr_b16 a[40:41], v209 offset:24576\n" \
"buffer_store_dword v146, v147, s[56:59], s15 offen sc1\n" \
"v_mfma_f32_16x16x32_bf16 v[60:63], a[204:207], a[28:31], v[60:63]\n" \
"buffer_load_dwordx4 v192, s[44:47], s73 offen offset:0 lds\n" \
"v_mfma_f32_16x16x32_bf16 v[52:55], a[200:203], a[16:19], v[52:55]\n" \
"ds_read_b64_tr_b16 a[42:43], v209 offset:27648\n" \
"v_mfma_f32_16x16x32_bf16 v[52:55], a[204:207], a[20:23], v[52:55]\n" \
"ds_read_b64_tr_b16 a[44:45], v209 offset:36864\n" \
"buffer_store_dword v146, v147, s[56:59], s17 offen sc1\n" \
"v_mfma_f32_16x16x32_bf16 v[44:47], a[200:203], a[8:11], v[44:47]\n" \
"s_mov_b64 s[36:37], s[18:19]\n" \
"ds_read_b64_tr_b16 a[46:47], v209 offset:39936\n" \
"v_mfma_f32_16x16x32_bf16 v[44:47], a[204:207], a[12:15], v[44:47]\n" \
"buffer_load_dwordx4 v193, s[44:47], s73 offen offset:1024 lds\n" \
"v_mfma_f32_16x16x32_bf16 v[72:75], a[0:3], a[208:211], v[72:75]\n" \
"s_sub_u32 s36, s36, 4096\n" \
"s_subb_u32 s37, s37, 0\n" \
"ds_read_b64_tr_b16 a[48:49], v210 offset:24576\n" \
"v_mfma_f32_16x16x32_bf16 v[72:75], a[4:7], a[212:215], v[72:75]\n" \
"s_and_b32 s37, s37, 0xffff\n" \
"s_mov_b32 s38, 8392704\n" \
"ds_read_b128 a[0:3], v202 offset:5120\n" \
"v_mfma_f32_16x16x32_bf16 v[80:83], a[8:11], a[208:211], v[80:83]\n" \
"s_mov_b32 s39, 0x20000\n" \
"s_mov_b64 s[40:41], s[24:25]\n" \
"ds_read_b128 a[4:7], v203 offset:5120\n" \
"v_mfma_f32_16x16x32_bf16 v[80:83], a[12:15], a[212:215], v[80:83]\n" \
"buffer_load_dwordx4 v192, s[44:47], s73 offen offset:2048 lds\n" \
"v_mfma_f32_16x16x32_bf16 v[84:87], a[8:11], a[216:219], v[84:87]\n" \
"s_sub_u32 s40, s40, 4096\n" \
"s_subb_u32 s41, s41, 0\n" \
"ds_read_b64_tr_b16 a[50:51], v210 offset:27648\n" \
"v_mfma_f32_16x16x32_bf16 v[84:87], a[12:15], a[220:223], v[84:87]\n" \
"s_and_b32 s41, s41, 0xffff\n" \
"s_mov_b32 s42, 2101248\n" \
"ds_read_b128 a[8:11], v200 offset:40960\n" \
"v_mfma_f32_16x16x32_bf16 v[92:95], a[16:19], a[216:219], v[92:95]\n" \
"s_mov_b32 s43, 0x20000\n" \
"s_mov_b64 s[52:53], s[26:27]\n" \
"ds_read_b128 a[12:15], v201 offset:40960\n" \
"v_mfma_f32_16x16x32_bf16 v[92:95], a[20:23], a[220:223], v[92:95]\n" \
"buffer_load_dwordx4 v193, s[44:47], s73 offen offset:3072 lds\n" \
"v_mfma_f32_16x16x32_bf16 v[88:91], a[16:19], a[208:211], v[88:91]\n" \
"s_xor_b32 s73, s73, 0x780\n" \
"s_mul_i32 s76, s80, 393216\n" \
"ds_read_b64_tr_b16 a[52:53], v210 offset:36864\n" \
"v_mfma_f32_16x16x32_bf16 v[88:91], a[20:23], a[212:215], v[88:91]\n" \
"s_add_u32 s76, s76, s78\n" \
"s_and_b32 s53, s53, 0xffff\n" \
"ds_read_b128 a[16:19], v200 offset:45056\n" \
"v_mfma_f32_16x16x32_bf16 v[96:99], a[24:27], a[208:211], v[96:99]\n" \
"s_add_u32 m0, s91, 16384\n" \
"s_mov_b32 s54, 8388608\n" \
"ds_read_b128 a[20:23], v201 offset:45056\n" \
"v_mfma_f32_16x16x32_bf16 v[96:99], a[28:31], a[212:215], v[96:99]\n" \
"buffer_load_dwordx4 v197, s[48:51], s76 offen offset:0 lds\n" \
"v_mfma_f32_16x16x32_bf16 v[100:103], a[24:27], a[216:219], v[100:103]\n" \
"s_mov_b32 s55, 0x20000\n" \
"ds_read_b64_tr_b16 a[54:55], v210 offset:39936\n" \
"v_lshrrev_b32 v221, 2, v234\n" \
"v_mfma_f32_16x16x32_bf16 v[100:103], a[28:31], a[220:223], v[100:103]\n" \
"s_waitcnt lgkmcnt(3)\n" \
"ds_read_b128 a[24:27], v200 offset:49152\n" \
"v_lshrrev_b32 v244, 4, v234\n" \
"v_mfma_f32_16x16x32_bf16 v[108:111], a[192:195], a[216:219], v[108:111]\n" \
"s_add_u32 s77, s76, 24576\n" \
"s_lshl_b32 s12, s94, 7\n" \
"ds_read_b128 a[28:31], v201 offset:49152\n" \
"v_mfma_f32_16x16x32_bf16 v[108:111], a[196:199], a[220:223], v[108:111]\n" \
"buffer_load_dwordx4 v198, s[48:51], s76 offen offset:1024 lds\n" \
"v_mfma_f32_16x16x32_bf16 v[104:107], a[192:195], a[208:211], v[104:107]\n" \
"s_add_u32 s12, s12, 65591296\n" \
"s_and_b32 s70, %1, 1\n" \
"s_lshl3_add_u32 s70, s70, s63\n" \
"v_mfma_f32_16x16x32_bf16 v[104:107], a[196:199], a[212:215], v[104:107]\n" \
"s_lshl2_add_u32 s12, s70, s12\n" \
"s_lshr_b32 s68, s63, 1\n" \
"s_add_u32 s68, s68, 2\n" \
"v_mfma_f32_16x16x32_bf16 v[32:35], a[32:35], a[0:3], v[32:35]\n" \
"s_xor_b32 s80, s80, 0xf\n" \
"s_lshr_b32 s70, s18, 8\n" \
"ds_read_b128 a[192:195], v200 offset:53248\n" \
"v_mfma_f32_16x16x32_bf16 v[32:35], a[36:39], a[4:7], v[32:35]\n" \
"buffer_load_dwordx4 v199, s[48:51], s76 offen offset:2048 lds\n" \
"v_mfma_f32_16x16x32_bf16 v[40:43], a[32:35], a[8:11], v[40:43]\n" \
"s_sub_u32 s68, s68, s70\n" \
"s_and_b32 s68, s68, 3\n" \
"ds_read_b128 a[196:199], v201 offset:53248\n" \
"v_mfma_f32_16x16x32_bf16 v[40:43], a[36:39], a[12:15], v[40:43]\n" \
"s_and_b32 s70, s63, 1\n" \
"s_lshl_b32 s70, s70, 2\n" \
"ds_read_b64_tr_b16 a[200:201], v210 offset:25600\n" \
"v_mfma_f32_16x16x32_bf16 v[0:3], a[40:43], a[8:11], v[0:3]\n" \
"s_add_u32 m0, m0, 3072\n" \
"s_or_b32 s68, s68, s70\n" \
"ds_read_b64_tr_b16 a[202:203], v210 offset:28672\n" \
"v_mfma_f32_16x16x32_bf16 v[0:3], a[44:47], a[12:15], v[0:3]\n" \
"s_waitcnt lgkmcnt(3)\n" \
"buffer_load_dwordx4 v197, s[48:51], s77 offen offset:0 lds\n" \
"v_mfma_f32_16x16x32_bf16 v[4:7], a[48:51], a[8:11], v[4:7]\n" \
"s_lshr_b32 s84, s61, 1\n" \
"s_lshl_b32 s84, s84, 16\n" \
"ds_read_b64_tr_b16 a[204:205], v210 offset:37888\n" \
"v_mfma_f32_16x16x32_bf16 v[4:7], a[52:55], a[12:15], v[4:7]\n" \
"s_and_b32 s70, s61, 1\n" \
"s_lshl_b32 s70, s70, 14\n" \
"ds_read_b64_tr_b16 a[206:207], v210 offset:40960\n" \
"v_mfma_f32_16x16x32_bf16 v[12:15], a[48:51], a[16:19], v[12:15]\n" \
"s_add_u32 s84, s84, s70\n" \
"s_lshl_b32 s88, s68, 8\n" \
"ds_read_b64_tr_b16 a[208:209], v209 offset:26624\n" \
"v_mfma_f32_16x16x32_bf16 v[12:15], a[52:55], a[20:23], v[12:15]\n" \
"buffer_load_dwordx4 v198, s[48:51], s77 offen offset:1024 lds\n" \
"v_mfma_f32_16x16x32_bf16 v[8:11], a[40:43], a[16:19], v[8:11]\n" \
"s_add_u32 s84, s84, s88\n" \
"ds_read_b64_tr_b16 a[210:211], v209 offset:29696\n" \
"v_bfe_u32 v245, v244, 2, 3\n" \
"v_mfma_f32_16x16x32_bf16 v[8:11], a[44:47], a[20:23], v[8:11]\n" \
"ds_read_b64_tr_b16 a[212:213], v209 offset:38912\n" \
"v_lshlrev_b32 v245, 11, v245\n" \
"v_mfma_f32_16x16x32_bf16 v[48:51], a[32:35], a[16:19], v[48:51]\n" \
"ds_read_b64_tr_b16 a[214:215], v209 offset:41984\n" \
"v_lshrrev_b32 v246, 5, v244\n" \
"v_mfma_f32_16x16x32_bf16 v[48:51], a[36:39], a[20:23], v[48:51]\n" \
"buffer_load_dwordx4 v199, s[48:51], s77 offen offset:2048 lds\n" \
"v_mfma_f32_16x16x32_bf16 v[56:59], a[32:35], a[24:27], v[56:59]\n" \
"ds_read_b64_tr_b16 a[216:217], v210 offset:26624\n" \
"v_lshl_add_u32 v245, v246, 6, v245\n" \
"v_mfma_f32_16x16x32_bf16 v[56:59], a[36:39], a[28:31], v[56:59]\n" \
"ds_read_b64_tr_b16 a[218:219], v210 offset:29696\n" \
"v_and_b32 v246, 3, v244\n" \
"v_mfma_f32_16x16x32_bf16 v[16:19], a[40:43], a[24:27], v[16:19]\n" \
"ds_read_b64_tr_b16 a[220:221], v210 offset:38912\n" \
"v_add_u32 v254, 3145728, v254\n" \
"v_mfma_f32_16x16x32_bf16 v[16:19], a[44:47], a[28:31], v[16:19]\n" \
"global_load_dword v253, v254, s[22:23]\n" \
"v_mfma_f32_16x16x32_bf16 v[20:23], a[48:51], a[24:27], v[20:23]\n" \
"ds_read_b64_tr_b16 a[222:223], v210 offset:41984\n" \
"v_bfe_u32 v247, v244, 4, 1\n" \
"v_mfma_f32_16x16x32_bf16 v[20:23], a[52:55], a[28:31], v[20:23]\n" \
"s_waitcnt lgkmcnt(3)\n" \
"v_lshlrev_b32 v247, 1, v247\n" \
"v_xor_b32 v246, v246, v247\n" \
"v_mfma_f32_16x16x32_bf16 v[28:31], a[48:51], a[192:195], v[28:31]\n" \
"v_lshl_add_u32 v245, v246, 4, v245\n" \
"v_add_u32 v216, 4096, v245\n" \
"v_mfma_f32_16x16x32_bf16 v[28:31], a[52:55], a[196:199], v[28:31]\n" \
"v_add_u32 v217, 3200, v245\n" \
"v_add_u32 v218, 34816, v245\n" \
"v_mfma_f32_16x16x32_bf16 v[24:27], a[40:43], a[192:195], v[24:27]\n" \
"v_add_u32 v219, 33920, v245\n" \
"s_mov_b32 s86, 65536\n" \
"v_mfma_f32_16x16x32_bf16 v[24:27], a[44:47], a[196:199], v[24:27]\n" \
"s_lshr_b32 s70, s61, 1\n" \
"s_lshl_b32 s71, s70, 13\n" \
"s_add_u32 s71, s71, %3\n" \
"v_mfma_f32_16x16x32_bf16 v[64:67], a[32:35], a[192:195], v[64:67]\n" \
"v_add_u32 v211, s71, v236\n" \
"s_lshl_b32 s70, s70, 18\n" \
"v_mfma_f32_16x16x32_bf16 v[64:67], a[36:39], a[196:199], v[64:67]\n" \
"s_add_u32 s88, s88, s70\n" \
"s_waitcnt vmcnt(13) lgkmcnt(0)\n" \
"s_barrier\n" \
"v_mfma_f32_16x16x32_bf16 v[68:71], a[200:203], a[192:195], v[68:71]\n" \
"s_and_b32 s70, s61, 1\n" \
"ds_read_b64_tr_b16 a[32:33], v209 offset:1024\n" \
"ds_read_b64_tr_b16 a[34:35], v209 offset:4096\n" \
"v_mfma_f32_16x16x32_bf16 v[68:71], a[204:207], a[196:199], v[68:71]\n" \
"s_lshl_b32 s71, s70, 7\n" \
"ds_read_b64_tr_b16 a[36:37], v209 offset:13312\n" \
"ds_read_b64_tr_b16 a[38:39], v209 offset:16384\n" \
"v_mfma_f32_16x16x32_bf16 v[60:63], a[200:203], a[24:27], v[60:63]\n" \
"s_add_u32 m0, s79, 40960\n" \
"s_add_u32 s88, s88, s71\n" \
"ds_read_b64_tr_b16 a[40:41], v209 offset:0\n" \
"v_mfma_f32_16x16x32_bf16 v[60:63], a[204:207], a[28:31], v[60:63]\n" \
"buffer_load_dwordx4 v192, s[44:47], s73 offen offset:0 lds\n" \
"v_mfma_f32_16x16x32_bf16 v[52:55], a[200:203], a[16:19], v[52:55]\n" \
"ds_read_b64_tr_b16 a[42:43], v209 offset:3072\n" \
"v_and_b32 v245, 3, v244\n" \
"v_mfma_f32_16x16x32_bf16 v[52:55], a[204:207], a[20:23], v[52:55]\n" \
"ds_read_b64_tr_b16 a[44:45], v209 offset:12288\n" \
"v_lshlrev_b32 v245, 3, v245\n" \
"v_mfma_f32_16x16x32_bf16 v[44:47], a[200:203], a[8:11], v[44:47]\n" \
"ds_read_b64_tr_b16 a[46:47], v209 offset:15360\n" \
"v_bfe_u32 v246, v244, 2, 2\n" \
"v_mfma_f32_16x16x32_bf16 v[44:47], a[204:207], a[12:15], v[44:47]\n" \
"buffer_load_dwordx4 v193, s[44:47], s73 offen offset:1024 lds\n" \
"v_mfma_f32_16x16x32_bf16 v[72:75], a[0:3], a[208:211], v[72:75]\n" \
"ds_read_b64_tr_b16 a[48:49], v210 offset:0\n" \
"v_lshl_add_u32 v245, v246, 6, v245\n" \
"v_mfma_f32_16x16x32_bf16 v[72:75], a[4:7], a[212:215], v[72:75]\n" \
"ds_read_b128 a[0:3], v202 offset:6144\n" \
"v_bfe_u32 v246, v244, 4, 1\n" \
"v_mfma_f32_16x16x32_bf16 v[80:83], a[8:11], a[208:211], v[80:83]\n" \
"ds_read_b128 a[4:7], v203 offset:6144\n" \
"v_lshl_add_u32 v245, v246, 5, v245\n" \
"v_mfma_f32_16x16x32_bf16 v[80:83], a[12:15], a[212:215], v[80:83]\n" \
"buffer_load_dwordx4 v192, s[44:47], s73 offen offset:2048 lds\n" \
"v_mfma_f32_16x16x32_bf16 v[84:87], a[8:11], a[216:219], v[84:87]\n" \
"ds_read_b64_tr_b16 a[50:51], v210 offset:3072\n" \
"v_lshl_add_u32 v245, v246, 8, v245\n" \
"v_mfma_f32_16x16x32_bf16 v[84:87], a[12:15], a[220:223], v[84:87]\n" \
"ds_read_b128 a[8:11], v205 offset:0\n" \
"v_lshrrev_b32 v246, 5, v244\n" \
"v_mfma_f32_16x16x32_bf16 v[92:95], a[16:19], a[216:219], v[92:95]\n" \
"ds_read_b128 a[12:15], v206 offset:0\n" \
"v_lshl_add_u32 v245, v246, 12, v245\n" \
"v_mfma_f32_16x16x32_bf16 v[92:95], a[20:23], a[220:223], v[92:95]\n" \
"buffer_load_dwordx4 v193, s[44:47], s73 offen offset:3072 lds\n" \
"v_mfma_f32_16x16x32_bf16 v[88:91], a[16:19], a[208:211], v[88:91]\n" \
"s_xor_b32 s73, s73, 0x80\n" \
"s_mul_i32 s76, s80, 393216\n" \
"ds_read_b64_tr_b16 a[52:53], v210 offset:12288\n" \
"v_mfma_f32_16x16x32_bf16 v[88:91], a[20:23], a[212:215], v[88:91]\n" \
"s_add_u32 s76, s76, s78\n" \
"s_lshl_b32 s71, s70, 10\n" \
"ds_read_b128 a[16:19], v205 offset:4096\n" \
"v_mfma_f32_16x16x32_bf16 v[96:99], a[24:27], a[208:211], v[96:99]\n" \
"s_add_u32 m0, s91, 106496\n" \
"s_add_u32 s71, s71, %3\n" \
"ds_read_b128 a[20:23], v206 offset:4096\n" \
"v_mfma_f32_16x16x32_bf16 v[96:99], a[28:31], a[212:215], v[96:99]\n" \
"buffer_load_dwordx4 v197, s[48:51], s76 offen offset:0 lds\n" \
"v_mfma_f32_16x16x32_bf16 v[100:103], a[24:27], a[216:219], v[100:103]\n" \
"ds_read_b64_tr_b16 a[54:55], v210 offset:15360\n" \
"v_add_u32 v212, s71, v245\n" \
"v_mfma_f32_16x16x32_bf16 v[100:103], a[28:31], a[220:223], v[100:103]\n" \
"s_mov_b32 s69, 1\n" \
"s_mov_b32 s30, 0x0\n" \
"ds_read_b128 a[24:27], v205 offset:8192\n" \
"v_mfma_f32_16x16x32_bf16 v[108:111], a[192:195], a[216:219], v[108:111]\n" \
"s_add_u32 s77, s76, 24576\n" \
"s_mov_b32 s85, 98304\n" \
"ds_read_b128 a[28:31], v206 offset:8192\n" \
"v_mfma_f32_16x16x32_bf16 v[108:111], a[196:199], a[220:223], v[108:111]\n" \
"buffer_load_dwordx4 v198, s[48:51], s76 offen offset:1024 lds\n" \
"v_mfma_f32_16x16x32_bf16 v[104:107], a[192:195], a[208:211], v[104:107]\n" \
"s_sub_u32 s85, s85, 32768\n" \
"s_cselect_b32 s85, 131072, s85\n" \
"v_add_u32 v213, s85, v211\n" \
"v_mfma_f32_16x16x32_bf16 v[104:107], a[196:199], a[212:215], v[104:107]\n" \
"s_waitcnt lgkmcnt(2)\n" \
"v_add_u32 v214, s85, v212\n" \
"v_xor_b32 v215, 32, v214\n" \
"v_mfma_f32_16x16x32_bf16 v[32:35], a[32:35], a[0:3], v[32:35]\n" \
"s_xor_b32 s80, s80, 0x1\n" \
"s_mov_b32 s13, 65593856\n" \
"ds_read_b128 a[192:195], v205 offset:12288\n" \
"v_mfma_f32_16x16x32_bf16 v[32:35], a[36:39], a[4:7], v[32:35]\n" \
"buffer_load_dwordx4 v199, s[48:51], s76 offen offset:2048 lds\n" \
"v_mfma_f32_16x16x32_bf16 v[40:43], a[32:35], a[8:11], v[40:43]\n" \
"ds_read_b128 a[196:199], v206 offset:12288\n" \
"v_lshrrev_b32 v228, 1, v234\n" \
"v_mfma_f32_16x16x32_bf16 v[40:43], a[36:39], a[12:15], v[40:43]\n" \
"ds_read_b64_tr_b16 a[200:201], v210 offset:1024\n" \
"v_mov_b32 v230, s60\n" \
"v_mfma_f32_16x16x32_bf16 v[0:3], a[40:43], a[8:11], v[0:3]\n" \
"s_add_u32 m0, m0, 3072\n" \
"ds_read_b64_tr_b16 a[202:203], v210 offset:4096\n" \
"v_lshlrev_b32 v231, 28, s61\n" \
"v_mfma_f32_16x16x32_bf16 v[0:3], a[44:47], a[12:15], v[0:3]\n" \
"buffer_load_dwordx4 v197, s[48:51], s77 offen offset:0 lds\n" \
"v_mfma_f32_16x16x32_bf16 v[4:7], a[48:51], a[8:11], v[4:7]\n" \
"ds_read_b64_tr_b16 a[204:205], v210 offset:13312\n" \
"v_mfma_f32_16x16x32_bf16 v[4:7], a[52:55], a[12:15], v[4:7]\n" \
"ds_read_b64_tr_b16 a[206:207], v210 offset:16384\n" \
"v_mfma_f32_16x16x32_bf16 v[12:15], a[48:51], a[16:19], v[12:15]\n" \
"ds_read_b64_tr_b16 a[208:209], v209 offset:2048\n" \
"v_mfma_f32_16x16x32_bf16 v[12:15], a[52:55], a[20:23], v[12:15]\n" \
"buffer_load_dwordx4 v198, s[48:51], s77 offen offset:1024 lds\n" \
"Lpg8502_%=:\n" \
"Lst1x1_%=:\n" \
"s_cbranch_execz Lpg10821_%=\n" \
"v_mfma_f32_16x16x32_bf16 v[8:11], a[40:43], a[16:19], v[8:11]\n" \
"ds_read_b64_tr_b16 a[210:211], v209 offset:5120\n" \
"v_mfma_f32_16x16x32_bf16 v[8:11], a[44:47], a[20:23], v[8:11]\n" \
"ds_read_b64_tr_b16 a[212:213], v209 offset:14336\n" \
"v_mfma_f32_16x16x32_bf16 v[48:51], a[32:35], a[16:19], v[48:51]\n" \
"s_waitcnt lgkmcnt(3)\n" \
"ds_read_b64_tr_b16 a[214:215], v209 offset:17408\n" \
"v_mfma_f32_16x16x32_bf16 v[48:51], a[36:39], a[20:23], v[48:51]\n" \
"buffer_load_dwordx4 v199, s[48:51], s77 offen offset:2048 lds\n" \
"v_mfma_f32_16x16x32_bf16 v[56:59], a[32:35], a[24:27], v[56:59]\n" \
"ds_read_b64_tr_b16 a[216:217], v210 offset:2048\n" \
"v_mfma_f32_16x16x32_bf16 v[56:59], a[36:39], a[28:31], v[56:59]\n" \
"ds_read_b64_tr_b16 a[218:219], v210 offset:5120\n" \
"v_mfma_f32_16x16x32_bf16 v[16:19], a[40:43], a[24:27], v[16:19]\n" \
"ds_read_b64_tr_b16 a[220:221], v210 offset:14336\n" \
"v_mfma_f32_16x16x32_bf16 v[16:19], a[44:47], a[28:31], v[16:19]\n" \
"v_mfma_f32_16x16x32_bf16 v[20:23], a[48:51], a[24:27], v[20:23]\n" \
"ds_read_b64_tr_b16 a[222:223], v210 offset:17408\n" \
"v_mfma_f32_16x16x32_bf16 v[20:23], a[52:55], a[28:31], v[20:23]\n" \
"v_mfma_f32_16x16x32_bf16 v[28:31], a[48:51], a[192:195], v[28:31]\n" \
"v_mfma_f32_16x16x32_bf16 v[28:31], a[52:55], a[196:199], v[28:31]\n" \
"buffer_load_dwordx2 v[226:227], v228, s[56:59], s13 offen sc1\n" \
"v_mfma_f32_16x16x32_bf16 v[24:27], a[40:43], a[192:195], v[24:27]\n" \
"v_mfma_f32_16x16x32_bf16 v[24:27], a[44:47], a[196:199], v[24:27]\n" \
"v_mfma_f32_16x16x32_bf16 v[64:67], a[32:35], a[192:195], v[64:67]\n" \
"v_mfma_f32_16x16x32_bf16 v[64:67], a[36:39], a[196:199], v[64:67]\n" \
"s_waitcnt vmcnt(12) lgkmcnt(0)\n" \
"s_barrier\n" \
"v_mfma_f32_16x16x32_bf16 v[68:71], a[200:203], a[192:195], v[68:71]\n" \
"ds_read_b64_tr_b16 a[32:33], v204 offset:17408\n" \
"ds_read_b64_tr_b16 a[34:35], v204 offset:20480\n" \
"v_mfma_f32_16x16x32_bf16 v[68:71], a[204:207], a[196:199], v[68:71]\n" \
"ds_read_b64_tr_b16 a[36:37], v204 offset:29696\n" \
"ds_read_b64_tr_b16 a[38:39], v204 offset:32768\n" \
"v_mfma_f32_16x16x32_bf16 v[60:63], a[200:203], a[24:27], v[60:63]\n" \
"s_add_u32 m0, s79, 65536\n" \
"ds_read_b64_tr_b16 a[40:41], v204 offset:16384\n" \
"v_mfma_f32_16x16x32_bf16 v[60:63], a[204:207], a[28:31], v[60:63]\n" \
"buffer_load_dwordx4 v192, s[44:47], s73 offen offset:0 lds\n" \
"v_mfma_f32_16x16x32_bf16 v[52:55], a[200:203], a[16:19], v[52:55]\n" \
"ds_read_b64_tr_b16 a[42:43], v204 offset:19456\n" \
"v_mfma_f32_16x16x32_bf16 v[52:55], a[204:207], a[20:23], v[52:55]\n" \
"ds_read_b64_tr_b16 a[44:45], v204 offset:28672\n" \
"v_mfma_f32_16x16x32_bf16 v[44:47], a[200:203], a[8:11], v[44:47]\n" \
"ds_read_b64_tr_b16 a[46:47], v204 offset:31744\n" \
"v_mfma_f32_16x16x32_bf16 v[44:47], a[204:207], a[12:15], v[44:47]\n" \
"buffer_load_dwordx4 v193, s[44:47], s73 offen offset:1024 lds\n" \
"v_mfma_f32_16x16x32_bf16 v[72:75], a[0:3], a[208:211], v[72:75]\n" \
"ds_read_b64_tr_b16 a[48:49], v222 offset:16384\n" \
"v_mfma_f32_16x16x32_bf16 v[72:75], a[4:7], a[212:215], v[72:75]\n" \
"ds_read_b128 a[0:3], v202 offset:7168\n" \
"v_mfma_f32_16x16x32_bf16 v[80:83], a[8:11], a[208:211], v[80:83]\n" \
"ds_read_b128 a[4:7], v203 offset:7168\n" \
"v_mfma_f32_16x16x32_bf16 v[80:83], a[12:15], a[212:215], v[80:83]\n" \
"buffer_load_dwordx4 v192, s[44:47], s73 offen offset:2048 lds\n" \
"v_mfma_f32_16x16x32_bf16 v[84:87], a[8:11], a[216:219], v[84:87]\n" \
"ds_read_b64_tr_b16 a[50:51], v222 offset:19456\n" \
"v_mfma_f32_16x16x32_bf16 v[84:87], a[12:15], a[220:223], v[84:87]\n" \
"ds_read_b128 a[8:11], v200 offset:0\n" \
"v_mfma_f32_16x16x32_bf16 v[92:95], a[16:19], a[216:219], v[92:95]\n" \
"ds_read_b128 a[12:15], v201 offset:0\n" \
"v_mfma_f32_16x16x32_bf16 v[92:95], a[20:23], a[220:223], v[92:95]\n" \
"buffer_load_dwordx4 v193, s[44:47], s73 offen offset:3072 lds\n" \
"v_mfma_f32_16x16x32_bf16 v[88:91], a[16:19], a[208:211], v[88:91]\n" \
"s_xor_b32 s73, s73, 0x180\n" \
"s_mul_i32 s76, s80, 393216\n" \
"ds_read_b64_tr_b16 a[52:53], v222 offset:28672\n" \
"v_mfma_f32_16x16x32_bf16 v[88:91], a[20:23], a[212:215], v[88:91]\n" \
"s_add_u32 s76, s76, s78\n" \
"ds_read_b128 a[16:19], v200 offset:4096\n" \
"v_mfma_f32_16x16x32_bf16 v[96:99], a[24:27], a[208:211], v[96:99]\n" \
"s_add_u32 m0, s91, 81920\n" \
"ds_read_b128 a[20:23], v201 offset:4096\n" \
"v_mfma_f32_16x16x32_bf16 v[96:99], a[28:31], a[212:215], v[96:99]\n" \
"buffer_load_dwordx4 v197, s[48:51], s76 offen offset:0 lds\n" \
"v_mfma_f32_16x16x32_bf16 v[100:103], a[24:27], a[216:219], v[100:103]\n" \
"ds_read_b64_tr_b16 a[54:55], v222 offset:31744\n" \
"v_mfma_f32_16x16x32_bf16 v[100:103], a[28:31], a[220:223], v[100:103]\n" \
"ds_read_b128 a[24:27], v200 offset:8192\n" \
"v_mfma_f32_16x16x32_bf16 v[108:111], a[192:195], a[216:219], v[108:111]\n" \
"s_add_u32 s77, s76, 24576\n" \
"ds_read_b128 a[28:31], v201 offset:8192\n" \
"v_mfma_f32_16x16x32_bf16 v[108:111], a[196:199], a[220:223], v[108:111]\n" \
"buffer_load_dwordx4 v198, s[48:51], s76 offen offset:1024 lds\n" \
"v_mfma_f32_16x16x32_bf16 v[104:107], a[192:195], a[208:211], v[104:107]\n" \
"v_mfma_f32_16x16x32_bf16 v[104:107], a[196:199], a[212:215], v[104:107]\n" \
"s_waitcnt lgkmcnt(2)\n" \
"v_mfma_f32_16x16x32_bf16 v[32:35], a[32:35], a[0:3], v[32:35]\n" \
"s_xor_b32 s80, s80, 0x3\n" \
"ds_read_b128 a[192:195], v200 offset:12288\n" \
"v_mfma_f32_16x16x32_bf16 v[32:35], a[36:39], a[4:7], v[32:35]\n" \
"buffer_load_dwordx4 v199, s[48:51], s76 offen offset:2048 lds\n" \
"v_mfma_f32_16x16x32_bf16 v[40:43], a[32:35], a[8:11], v[40:43]\n" \
"ds_read_b128 a[196:199], v201 offset:12288\n" \
"v_mfma_f32_16x16x32_bf16 v[40:43], a[36:39], a[12:15], v[40:43]\n" \
"ds_read_b64_tr_b16 a[200:201], v222 offset:17408\n" \
"v_mfma_f32_16x16x32_bf16 v[0:3], a[40:43], a[8:11], v[0:3]\n" \
"s_add_u32 m0, m0, 3072\n" \
"ds_read_b64_tr_b16 a[202:203], v222 offset:20480\n" \
"v_mfma_f32_16x16x32_bf16 v[0:3], a[44:47], a[12:15], v[0:3]\n" \
"buffer_load_dwordx4 v197, s[48:51], s77 offen offset:0 lds\n" \
"v_mfma_f32_16x16x32_bf16 v[4:7], a[48:51], a[8:11], v[4:7]\n" \
"ds_read_b64_tr_b16 a[204:205], v222 offset:29696\n" \
"v_mfma_f32_16x16x32_bf16 v[4:7], a[52:55], a[12:15], v[4:7]\n" \
"ds_read_b64_tr_b16 a[206:207], v222 offset:32768\n" \
"v_mfma_f32_16x16x32_bf16 v[12:15], a[48:51], a[16:19], v[12:15]\n" \
"ds_read_b64_tr_b16 a[208:209], v204 offset:18432\n" \
"v_mfma_f32_16x16x32_bf16 v[12:15], a[52:55], a[20:23], v[12:15]\n" \
"buffer_load_dwordx4 v198, s[48:51], s77 offen offset:1024 lds\n" \
"v_mfma_f32_16x16x32_bf16 v[8:11], a[40:43], a[16:19], v[8:11]\n" \
"ds_read_b64_tr_b16 a[210:211], v204 offset:21504\n" \
"v_mfma_f32_16x16x32_bf16 v[8:11], a[44:47], a[20:23], v[8:11]\n" \
"ds_read_b64_tr_b16 a[212:213], v204 offset:30720\n" \
"v_mfma_f32_16x16x32_bf16 v[48:51], a[32:35], a[16:19], v[48:51]\n" \
"s_waitcnt lgkmcnt(3)\n" \
"ds_read_b64_tr_b16 a[214:215], v204 offset:33792\n" \
"v_mfma_f32_16x16x32_bf16 v[48:51], a[36:39], a[20:23], v[48:51]\n" \
"buffer_load_dwordx4 v199, s[48:51], s77 offen offset:2048 lds\n" \
"v_mfma_f32_16x16x32_bf16 v[56:59], a[32:35], a[24:27], v[56:59]\n" \
"ds_read_b64_tr_b16 a[216:217], v222 offset:18432\n" \
"v_mfma_f32_16x16x32_bf16 v[56:59], a[36:39], a[28:31], v[56:59]\n" \
"ds_read_b64_tr_b16 a[218:219], v222 offset:21504\n" \
"v_mfma_f32_16x16x32_bf16 v[16:19], a[40:43], a[24:27], v[16:19]\n" \
"ds_read_b64_tr_b16 a[220:221], v222 offset:30720\n" \
"v_mfma_f32_16x16x32_bf16 v[16:19], a[44:47], a[28:31], v[16:19]\n" \
"v_mfma_f32_16x16x32_bf16 v[20:23], a[48:51], a[24:27], v[20:23]\n" \
"ds_read_b64_tr_b16 a[222:223], v222 offset:33792\n" \
"v_mfma_f32_16x16x32_bf16 v[20:23], a[52:55], a[28:31], v[20:23]\n" \
"v_mfma_f32_16x16x32_bf16 v[28:31], a[48:51], a[192:195], v[28:31]\n" \
"v_mfma_f32_16x16x32_bf16 v[28:31], a[52:55], a[196:199], v[28:31]\n" \
"v_mfma_f32_16x16x32_bf16 v[24:27], a[40:43], a[192:195], v[24:27]\n" \
"v_mfma_f32_16x16x32_bf16 v[24:27], a[44:47], a[196:199], v[24:27]\n" \
"v_mfma_f32_16x16x32_bf16 v[64:67], a[32:35], a[192:195], v[64:67]\n" \
"v_mfma_f32_16x16x32_bf16 v[64:67], a[36:39], a[196:199], v[64:67]\n" \
"s_waitcnt vmcnt(11) lgkmcnt(0)\n" \
"s_barrier\n" \
"v_mfma_f32_16x16x32_bf16 v[68:71], a[200:203], a[192:195], v[68:71]\n" \
"ds_read_b64_tr_b16 a[32:33], v209 offset:25600\n" \
"ds_read_b64_tr_b16 a[34:35], v209 offset:28672\n" \
"v_mfma_f32_16x16x32_bf16 v[68:71], a[204:207], a[196:199], v[68:71]\n" \
"ds_read_b64_tr_b16 a[36:37], v209 offset:37888\n" \
"ds_read_b64_tr_b16 a[38:39], v209 offset:40960\n" \
"v_mfma_f32_16x16x32_bf16 v[60:63], a[200:203], a[24:27], v[60:63]\n" \
"s_add_u32 m0, s79, 0\n" \
"ds_read_b64_tr_b16 a[40:41], v209 offset:24576\n" \
"v_mfma_f32_16x16x32_bf16 v[60:63], a[204:207], a[28:31], v[60:63]\n" \
"buffer_load_dwordx4 v192, s[44:47], s73 offen offset:0 lds\n" \
"v_mfma_f32_16x16x32_bf16 v[52:55], a[200:203], a[16:19], v[52:55]\n" \
"ds_read_b64_tr_b16 a[42:43], v209 offset:27648\n" \
"v_mfma_f32_16x16x32_bf16 v[52:55], a[204:207], a[20:23], v[52:55]\n" \
"ds_read_b64_tr_b16 a[44:45], v209 offset:36864\n" \
"v_mfma_f32_16x16x32_bf16 v[44:47], a[200:203], a[8:11], v[44:47]\n" \
"ds_read_b64_tr_b16 a[46:47], v209 offset:39936\n" \
"v_mfma_f32_16x16x32_bf16 v[44:47], a[204:207], a[12:15], v[44:47]\n" \
"buffer_load_dwordx4 v193, s[44:47], s73 offen offset:1024 lds\n" \
"v_mfma_f32_16x16x32_bf16 v[72:75], a[0:3], a[208:211], v[72:75]\n" \
"ds_read_b64_tr_b16 a[48:49], v210 offset:24576\n" \
"v_mfma_f32_16x16x32_bf16 v[72:75], a[4:7], a[212:215], v[72:75]\n" \
"ds_read_b128 a[0:3], v202 offset:16384\n" \
"v_mfma_f32_16x16x32_bf16 v[80:83], a[8:11], a[208:211], v[80:83]\n" \
"ds_read_b128 a[4:7], v203 offset:16384\n" \
"v_mfma_f32_16x16x32_bf16 v[80:83], a[12:15], a[212:215], v[80:83]\n" \
"buffer_load_dwordx4 v192, s[44:47], s73 offen offset:2048 lds\n" \
"v_mfma_f32_16x16x32_bf16 v[84:87], a[8:11], a[216:219], v[84:87]\n" \
"ds_read_b64_tr_b16 a[50:51], v210 offset:27648\n" \
"v_mfma_f32_16x16x32_bf16 v[84:87], a[12:15], a[220:223], v[84:87]\n" \
"ds_read_b128 a[8:11], v200 offset:40960\n" \
"v_mfma_f32_16x16x32_bf16 v[92:95], a[16:19], a[216:219], v[92:95]\n" \
"ds_read_b128 a[12:15], v201 offset:40960\n" \
"v_mfma_f32_16x16x32_bf16 v[92:95], a[20:23], a[220:223], v[92:95]\n" \
"buffer_load_dwordx4 v193, s[44:47], s73 offen offset:3072 lds\n" \
"v_mfma_f32_16x16x32_bf16 v[88:91], a[16:19], a[208:211], v[88:91]\n" \
"s_xor_b32 s73, s73, 0x300\n" \
"s_mul_i32 s76, s80, 393216\n" \
"ds_read_b64_tr_b16 a[52:53], v210 offset:36864\n" \
"v_mfma_f32_16x16x32_bf16 v[88:91], a[20:23], a[212:215], v[88:91]\n" \
"s_add_u32 s76, s76, s78\n" \
"ds_read_b128 a[16:19], v200 offset:45056\n" \
"v_mfma_f32_16x16x32_bf16 v[96:99], a[24:27], a[208:211], v[96:99]\n" \
"s_add_u32 m0, s91, 16384\n" \
"ds_read_b128 a[20:23], v201 offset:45056\n" \
"v_mfma_f32_16x16x32_bf16 v[96:99], a[28:31], a[212:215], v[96:99]\n" \
"buffer_load_dwordx4 v197, s[48:51], s76 offen offset:0 lds\n" \
"v_mfma_f32_16x16x32_bf16 v[100:103], a[24:27], a[216:219], v[100:103]\n" \
"ds_read_b64_tr_b16 a[54:55], v210 offset:39936\n" \
"v_mfma_f32_16x16x32_bf16 v[100:103], a[28:31], a[220:223], v[100:103]\n" \
"ds_read_b128 a[24:27], v200 offset:49152\n" \
"v_mfma_f32_16x16x32_bf16 v[108:111], a[192:195], a[216:219], v[108:111]\n" \
"s_add_u32 s77, s76, 24576\n" \
"ds_read_b128 a[28:31], v201 offset:49152\n" \
"v_mfma_f32_16x16x32_bf16 v[108:111], a[196:199], a[220:223], v[108:111]\n" \
"buffer_load_dwordx4 v198, s[48:51], s76 offen offset:1024 lds\n" \
"v_mfma_f32_16x16x32_bf16 v[104:107], a[192:195], a[208:211], v[104:107]\n" \
"v_mfma_f32_16x16x32_bf16 v[104:107], a[196:199], a[212:215], v[104:107]\n" \
"s_waitcnt lgkmcnt(2)\n" \
"v_mfma_f32_16x16x32_bf16 v[32:35], a[32:35], a[0:3], v[32:35]\n" \
"s_xor_b32 s80, s80, 0x6\n" \
"ds_read_b128 a[192:195], v200 offset:53248\n" \
"v_mfma_f32_16x16x32_bf16 v[32:35], a[36:39], a[4:7], v[32:35]\n" \
"buffer_load_dwordx4 v199, s[48:51], s76 offen offset:2048 lds\n" \
"v_mfma_f32_16x16x32_bf16 v[40:43], a[32:35], a[8:11], v[40:43]\n" \
"ds_read_b128 a[196:199], v201 offset:53248\n" \
"v_mfma_f32_16x16x32_bf16 v[40:43], a[36:39], a[12:15], v[40:43]\n" \
"ds_read_b64_tr_b16 a[200:201], v210 offset:25600\n" \
"v_mfma_f32_16x16x32_bf16 v[0:3], a[40:43], a[8:11], v[0:3]\n" \
"s_add_u32 m0, m0, 3072\n" \
"ds_read_b64_tr_b16 a[202:203], v210 offset:28672\n" \
"v_mfma_f32_16x16x32_bf16 v[0:3], a[44:47], a[12:15], v[0:3]\n" \
"buffer_load_dwordx4 v197, s[48:51], s77 offen offset:0 lds\n" \
"v_mfma_f32_16x16x32_bf16 v[4:7], a[48:51], a[8:11], v[4:7]\n" \
"ds_read_b64_tr_b16 a[204:205], v210 offset:37888\n" \
"v_mfma_f32_16x16x32_bf16 v[4:7], a[52:55], a[12:15], v[4:7]\n" \
"ds_read_b64_tr_b16 a[206:207], v210 offset:40960\n" \
"v_mfma_f32_16x16x32_bf16 v[12:15], a[48:51], a[16:19], v[12:15]\n" \
"ds_read_b64_tr_b16 a[208:209], v209 offset:26624\n" \
"v_mfma_f32_16x16x32_bf16 v[12:15], a[52:55], a[20:23], v[12:15]\n" \
"buffer_load_dwordx4 v198, s[48:51], s77 offen offset:1024 lds\n" \
"v_mfma_f32_16x16x32_bf16 v[8:11], a[40:43], a[16:19], v[8:11]\n" \
"ds_read_b64_tr_b16 a[210:211], v209 offset:29696\n" \
"v_mfma_f32_16x16x32_bf16 v[8:11], a[44:47], a[20:23], v[8:11]\n" \
"ds_read_b64_tr_b16 a[212:213], v209 offset:38912\n" \
"v_mfma_f32_16x16x32_bf16 v[48:51], a[32:35], a[16:19], v[48:51]\n" \
"s_waitcnt lgkmcnt(3)\n" \
"ds_read_b64_tr_b16 a[214:215], v209 offset:41984\n" \
"v_mfma_f32_16x16x32_bf16 v[48:51], a[36:39], a[20:23], v[48:51]\n" \
"buffer_load_dwordx4 v199, s[48:51], s77 offen offset:2048 lds\n" \
"v_mfma_f32_16x16x32_bf16 v[56:59], a[32:35], a[24:27], v[56:59]\n" \
"ds_read_b64_tr_b16 a[216:217], v210 offset:26624\n" \
"v_mfma_f32_16x16x32_bf16 v[56:59], a[36:39], a[28:31], v[56:59]\n" \
"ds_read_b64_tr_b16 a[218:219], v210 offset:29696\n" \
"v_mfma_f32_16x16x32_bf16 v[16:19], a[40:43], a[24:27], v[16:19]\n" \
"ds_read_b64_tr_b16 a[220:221], v210 offset:38912\n" \
"v_mfma_f32_16x16x32_bf16 v[16:19], a[44:47], a[28:31], v[16:19]\n" \
"v_mfma_f32_16x16x32_bf16 v[20:23], a[48:51], a[24:27], v[20:23]\n" \
"ds_read_b64_tr_b16 a[222:223], v210 offset:41984\n" \
"v_mfma_f32_16x16x32_bf16 v[20:23], a[52:55], a[28:31], v[20:23]\n" \
"v_mfma_f32_16x16x32_bf16 v[28:31], a[48:51], a[192:195], v[28:31]\n" \
"v_mfma_f32_16x16x32_bf16 v[28:31], a[52:55], a[196:199], v[28:31]\n" \
"v_mfma_f32_16x16x32_bf16 v[24:27], a[40:43], a[192:195], v[24:27]\n" \
"v_mfma_f32_16x16x32_bf16 v[24:27], a[44:47], a[196:199], v[24:27]\n" \
"v_mfma_f32_16x16x32_bf16 v[64:67], a[32:35], a[192:195], v[64:67]\n" \
"v_mfma_f32_16x16x32_bf16 v[64:67], a[36:39], a[196:199], v[64:67]\n" \
"s_waitcnt vmcnt(10) lgkmcnt(0)\n" \
"s_barrier\n" \
"v_mfma_f32_16x16x32_bf16 v[68:71], a[200:203], a[192:195], v[68:71]\n" \
"ds_read_b64_tr_b16 a[32:33], v209 offset:1024\n" \
"ds_read_b64_tr_b16 a[34:35], v209 offset:4096\n" \
"v_mfma_f32_16x16x32_bf16 v[68:71], a[204:207], a[196:199], v[68:71]\n" \
"ds_read_b64_tr_b16 a[36:37], v209 offset:13312\n" \
"ds_read_b64_tr_b16 a[38:39], v209 offset:16384\n" \
"v_mfma_f32_16x16x32_bf16 v[60:63], a[200:203], a[24:27], v[60:63]\n" \
"s_add_u32 m0, s79, 40960\n" \
"ds_read_b64_tr_b16 a[40:41], v209 offset:0\n" \
"v_cmp_eq_u32 vcc, s60, v226\n" \
"v_mfma_f32_16x16x32_bf16 v[60:63], a[204:207], a[28:31], v[60:63]\n" \
"buffer_load_dwordx4 v192, s[44:47], s73 offen offset:0 lds\n" \
"v_mfma_f32_16x16x32_bf16 v[52:55], a[200:203], a[16:19], v[52:55]\n" \
"s_andn2_b32 s70, 0xff, vcc_lo\n" \
"s_nop 0\n" \
"s_cbranch_scc1 Lpl4_%=\n" \
"Lpr4_%=:\n" \
"ds_read_b64_tr_b16 a[42:43], v209 offset:3072\n" \
"v_mfma_f32_16x16x32_bf16 v[52:55], a[204:207], a[20:23], v[52:55]\n" \
"s_and_b32 s70, %1, 7\n" \
"ds_read_b64_tr_b16 a[44:45], v209 offset:12288\n" \
"v_readlane_b32 s70, v227, s70\n" \
"v_mfma_f32_16x16x32_bf16 v[44:47], a[200:203], a[8:11], v[44:47]\n" \
"ds_read_b64_tr_b16 a[46:47], v209 offset:15360\n" \
"v_mfma_f32_16x16x32_bf16 v[44:47], a[204:207], a[12:15], v[44:47]\n" \
"buffer_load_dwordx4 v193, s[44:47], s73 offen offset:1024 lds\n" \
"v_mfma_f32_16x16x32_bf16 v[72:75], a[0:3], a[208:211], v[72:75]\n" \
"ds_read_b64_tr_b16 a[48:49], v210 offset:0\n" \
"v_subrev_u32 v227, s70, v227\n" \
"v_mfma_f32_16x16x32_bf16 v[72:75], a[4:7], a[212:215], v[72:75]\n" \
"ds_read_b128 a[0:3], v202 offset:17408\n" \
"v_cmp_gt_i32 vcc, 0, v227\n" \
"v_mfma_f32_16x16x32_bf16 v[80:83], a[8:11], a[208:211], v[80:83]\n" \
"ds_read_b128 a[4:7], v203 offset:17408\n" \
"v_mfma_f32_16x16x32_bf16 v[80:83], a[12:15], a[212:215], v[80:83]\n" \
"buffer_load_dwordx4 v192, s[44:47], s73 offen offset:2048 lds\n" \
"v_mfma_f32_16x16x32_bf16 v[84:87], a[8:11], a[216:219], v[84:87]\n" \
"s_and_b32 s13, vcc_lo, 0xff\n" \
"s_bcnt1_i32_b32 s13, s13\n" \
"ds_read_b64_tr_b16 a[50:51], v210 offset:3072\n" \
"v_mfma_f32_16x16x32_bf16 v[84:87], a[12:15], a[220:223], v[84:87]\n" \
"s_lshr_b32 s90, s13, 2\n" \
"s_mul_i32 s70, s90, 7\n" \
"ds_read_b128 a[8:11], v205 offset:0\n" \
"v_mfma_f32_16x16x32_bf16 v[92:95], a[16:19], a[216:219], v[92:95]\n" \
"s_xor_b32 s13, s13, s70\n" \
"s_lshl_b32 s70, s13, 3\n" \
"ds_read_b128 a[12:15], v206 offset:0\n" \
"v_mfma_f32_16x16x32_bf16 v[92:95], a[20:23], a[220:223], v[92:95]\n" \
"buffer_load_dwordx4 v193, s[44:47], s73 offen offset:3072 lds\n" \
"v_mfma_f32_16x16x32_bf16 v[88:91], a[16:19], a[208:211], v[88:91]\n" \
"s_xor_b32 s73, s73, 0x80\n" \
"s_mul_i32 s76, s80, 393216\n" \
"ds_read_b64_tr_b16 a[52:53], v210 offset:12288\n" \
"v_mfma_f32_16x16x32_bf16 v[88:91], a[20:23], a[212:215], v[88:91]\n" \
"s_add_u32 s76, s76, s78\n" \
"s_lshr_b32 s9, 0x2a2b2d2f, s70\n" \
"ds_read_b128 a[16:19], v205 offset:4096\n" \
"v_mfma_f32_16x16x32_bf16 v[96:99], a[24:27], a[208:211], v[96:99]\n" \
"s_add_u32 m0, s91, 106496\n" \
"s_and_b32 s9, s9, 0xff\n" \
"ds_read_b128 a[20:23], v206 offset:4096\n" \
"v_mfma_f32_16x16x32_bf16 v[96:99], a[28:31], a[212:215], v[96:99]\n" \
"buffer_load_dwordx4 v197, s[48:51], s76 offen offset:0 lds\n" \
"v_mfma_f32_16x16x32_bf16 v[100:103], a[24:27], a[216:219], v[100:103]\n" \
"s_lshr_b32 s8, %1, 6\n" \
"s_lshl2_add_u32 s8, s13, s8\n" \
"ds_read_b64_tr_b16 a[54:55], v210 offset:15360\n" \
"v_mfma_f32_16x16x32_bf16 v[100:103], a[28:31], a[220:223], v[100:103]\n" \
"ds_read_b128 a[24:27], v205 offset:8192\n" \
"v_max_i32_dpp v226, v227, v227 quad_perm:[1,0,3,2] row_mask:0xf bank_mask:0xf\n" \
"v_mfma_f32_16x16x32_bf16 v[108:111], a[192:195], a[216:219], v[108:111]\n" \
"s_add_u32 s77, s76, 24576\n" \
"s_mov_b32 s14, 0\n" \
"ds_read_b128 a[28:31], v206 offset:8192\n" \
"v_mfma_f32_16x16x32_bf16 v[108:111], a[196:199], a[220:223], v[108:111]\n" \
"buffer_load_dwordx4 v198, s[48:51], s76 offen offset:1024 lds\n" \
"v_mfma_f32_16x16x32_bf16 v[104:107], a[192:195], a[208:211], v[104:107]\n" \
"v_readlane_b32 s15, v226, 2\n" \
"v_readlane_b32 s16, v226, 4\n" \
"v_mfma_f32_16x16x32_bf16 v[104:107], a[196:199], a[212:215], v[104:107]\n" \
"s_waitcnt lgkmcnt(2)\n" \
"v_readlane_b32 s17, v226, 6\n" \
"v_cmp_gt_i32 vcc, s15, v226\n" \
"v_mfma_f32_16x16x32_bf16 v[32:35], a[32:35], a[0:3], v[32:35]\n" \
"s_xor_b32 s80, s80, 0x1\n" \
"ds_read_b128 a[192:195], v205 offset:12288\n" \
"v_mfma_f32_16x16x32_bf16 v[32:35], a[36:39], a[4:7], v[32:35]\n" \
"buffer_load_dwordx4 v199, s[48:51], s76 offen offset:2048 lds\n" \
"v_mfma_f32_16x16x32_bf16 v[40:43], a[32:35], a[8:11], v[40:43]\n" \
"s_and_b32 s70, vcc_lo, 0x55\n" \
"s_bcnt1_i32_b32 s70, s70\n" \
"ds_read_b128 a[196:199], v206 offset:12288\n" \
"v_mfma_f32_16x16x32_bf16 v[40:43], a[36:39], a[12:15], v[40:43]\n" \
"s_cmp_eq_u32 s70, s13\n" \
"s_cselect_b32 s14, 4, s14\n" \
"ds_read_b64_tr_b16 a[200:201], v210 offset:1024\n" \
"v_mfma_f32_16x16x32_bf16 v[0:3], a[40:43], a[8:11], v[0:3]\n" \
"s_add_u32 m0, m0, 3072\n" \
"ds_read_b64_tr_b16 a[202:203], v210 offset:4096\n" \
"v_cmp_gt_i32 vcc, s16, v226\n" \
"v_mfma_f32_16x16x32_bf16 v[0:3], a[44:47], a[12:15], v[0:3]\n" \
"buffer_load_dwordx4 v197, s[48:51], s77 offen offset:0 lds\n" \
"v_mfma_f32_16x16x32_bf16 v[4:7], a[48:51], a[8:11], v[4:7]\n" \
"s_and_b32 s70, vcc_lo, 0x55\n" \
"s_bcnt1_i32_b32 s70, s70\n" \
"ds_read_b64_tr_b16 a[204:205], v210 offset:13312\n" \
"v_mfma_f32_16x16x32_bf16 v[4:7], a[52:55], a[12:15], v[4:7]\n" \
"s_cmp_eq_u32 s70, s13\n" \
"s_cselect_b32 s14, 8, s14\n" \
"ds_read_b64_tr_b16 a[206:207], v210 offset:16384\n" \
"v_mfma_f32_16x16x32_bf16 v[12:15], a[48:51], a[16:19], v[12:15]\n" \
"ds_read_b64_tr_b16 a[208:209], v209 offset:2048\n" \
"v_cmp_gt_i32 vcc, s17, v226\n" \
"v_mfma_f32_16x16x32_bf16 v[12:15], a[52:55], a[20:23], v[12:15]\n" \
"buffer_load_dwordx4 v198, s[48:51], s77 offen offset:1024 lds\n" \
"v_mfma_f32_16x16x32_bf16 v[8:11], a[40:43], a[16:19], v[8:11]\n" \
"s_and_b32 s70, vcc_lo, 0x55\n" \
"s_bcnt1_i32_b32 s70, s70\n" \
"ds_read_b64_tr_b16 a[210:211], v209 offset:5120\n" \
"v_mfma_f32_16x16x32_bf16 v[8:11], a[44:47], a[20:23], v[8:11]\n" \
"s_cmp_eq_u32 s70, s13\n" \
"s_cselect_b32 s14, 12, s14\n" \
"ds_read_b64_tr_b16 a[212:213], v209 offset:14336\n" \
"v_mfma_f32_16x16x32_bf16 v[48:51], a[32:35], a[16:19], v[48:51]\n" \
"s_lshr_b32 s70, %1, 6\n" \
"s_add_u32 s14, s14, s70\n" \
"ds_read_b64_tr_b16 a[214:215], v209 offset:17408\n" \
"v_mfma_f32_16x16x32_bf16 v[48:51], a[36:39], a[20:23], v[48:51]\n" \
"s_waitcnt lgkmcnt(3)\n" \
"buffer_load_dwordx4 v199, s[48:51], s77 offen offset:2048 lds\n" \
"v_mfma_f32_16x16x32_bf16 v[56:59], a[32:35], a[24:27], v[56:59]\n" \
"s_cmp_eq_u32 s90, 0\n" \
"s_cselect_b32 s14, s8, s14\n" \
"ds_read_b64_tr_b16 a[216:217], v210 offset:2048\n" \
"v_mfma_f32_16x16x32_bf16 v[56:59], a[36:39], a[28:31], v[56:59]\n" \
"s_sub_u32 s10, 49, s9\n" \
"s_lshl_b32 s11, s9, 1\n" \
"ds_read_b64_tr_b16 a[218:219], v210 offset:5120\n" \
"v_mfma_f32_16x16x32_bf16 v[16:19], a[40:43], a[24:27], v[16:19]\n" \
"s_sub_u32 s11, s11, 71\n" \
"s_lshl2_add_u32 s70, s8, s61\n" \
"ds_read_b64_tr_b16 a[220:221], v210 offset:14336\n" \
"v_mfma_f32_16x16x32_bf16 v[16:19], a[44:47], a[28:31], v[16:19]\n" \
"s_mul_i32 s81, s70, 266240\n" \
"s_mul_i32 s82, s90, s9\n" \
"s_lshl_b32 s71, s82, 12\n" \
"v_mfma_f32_16x16x32_bf16 v[20:23], a[48:51], a[24:27], v[20:23]\n" \
"s_add_u32 s81, s81, s71\n" \
"s_add_u32 s81, s81, 35700736\n" \
"ds_read_b64_tr_b16 a[222:223], v210 offset:17408\n" \
"v_mfma_f32_16x16x32_bf16 v[20:23], a[52:55], a[28:31], v[20:23]\n" \
"s_lshl2_add_u32 s70, s14, s61\n" \
"s_mul_i32 s83, s70, 69632\n" \
"s_add_u32 s83, s83, 52740096\n" \
"v_mfma_f32_16x16x32_bf16 v[28:31], a[48:51], a[192:195], v[28:31]\n" \
"s_lshl_b32 s82, s82, 17\n" \
"s_add_u32 s82, s82, s84\n" \
"s_lshl_b32 s70, s14, 19\n" \
"v_mfma_f32_16x16x32_bf16 v[28:31], a[52:55], a[196:199], v[28:31]\n" \
"s_add_u32 s88, s88, s70\n" \
"s_lshl_b32 s71, s90, 11\n" \
"s_add_u32 s88, s88, s71\n" \
"v_mfma_f32_16x16x32_bf16 v[24:27], a[40:43], a[192:195], v[24:27]\n" \
"s_lshl3_add_u32 s70, s8, s68\n" \
"s_lshl2_add_u32 s87, s70, s61\n" \
"s_lshl_b32 s87, s87, 14\n" \
"v_mfma_f32_16x16x32_bf16 v[24:27], a[44:47], a[196:199], v[24:27]\n" \
"s_add_u32 s87, s87, 57196544\n" \
"s_lshl_b32 s64, s70, 7\n" \
"s_add_u32 s64, s64, 65601536\n" \
"v_mfma_f32_16x16x32_bf16 v[64:67], a[32:35], a[192:195], v[64:67]\n" \
"s_lshl_b32 s66, s8, 7\n" \
"s_lshl_b32 s67, s14, 7\n" \
"s_add_u32 s67, s67, 65591296\n" \
"v_mfma_f32_16x16x32_bf16 v[64:67], a[36:39], a[196:199], v[64:67]\n" \
"s_add_u32 s66, s66, 65589248\n" \
"s_waitcnt vmcnt(10) lgkmcnt(0)\n" \
"s_barrier\n" \
"v_mfma_f32_16x16x32_bf16 v[68:71], a[200:203], a[192:195], v[68:71]\n" \
"s_add_u32 s70, s9, 1\n" \
"ds_read_b64_tr_b16 a[32:33], v204 offset:17408\n" \
"ds_read_b64_tr_b16 a[34:35], v204 offset:20480\n" \
"v_mfma_f32_16x16x32_bf16 v[68:71], a[204:207], a[196:199], v[68:71]\n" \
"s_sub_u32 s70, s70, s90\n" \
"ds_read_b64_tr_b16 a[36:37], v204 offset:29696\n" \
"ds_read_b64_tr_b16 a[38:39], v204 offset:32768\n" \
"v_mfma_f32_16x16x32_bf16 v[60:63], a[200:203], a[24:27], v[60:63]\n" \
"s_add_u32 m0, s79, 65536\n" \
"s_lshr_b32 s70, s70, 1\n" \
"ds_read_b64_tr_b16 a[40:41], v204 offset:16384\n" \
"v_mfma_f32_16x16x32_bf16 v[60:63], a[204:207], a[28:31], v[60:63]\n" \
"buffer_load_dwordx4 v192, s[44:47], s73 offen offset:0 lds\n" \
"v_mfma_f32_16x16x32_bf16 v[52:55], a[200:203], a[16:19], v[52:55]\n" \
"s_bfm_b32 s65, s70, 0\n" \
"s_sub_u32 s70, 0, s90\n" \
"ds_read_b64_tr_b16 a[42:43], v204 offset:19456\n" \
"v_mfma_f32_16x16x32_bf16 v[52:55], a[204:207], a[20:23], v[52:55]\n" \
"s_xor_b32 s65, s65, s70\n" \
"s_and_b32 s74, s70, 0xffff\n" \
"ds_read_b64_tr_b16 a[44:45], v204 offset:28672\n" \
"v_mfma_f32_16x16x32_bf16 v[44:47], a[200:203], a[8:11], v[44:47]\n" \
"s_xor_b32 s68, s68, s8\n" \
"s_and_b32 s68, s68, 3\n" \
"ds_read_b64_tr_b16 a[46:47], v204 offset:31744\n" \
"v_mfma_f32_16x16x32_bf16 v[44:47], a[204:207], a[12:15], v[44:47]\n" \
"buffer_load_dwordx4 v193, s[44:47], s73 offen offset:1024 lds\n" \
"v_mfma_f32_16x16x32_bf16 v[72:75], a[0:3], a[208:211], v[72:75]\n" \
"s_lshl_b32 s68, s68, 12\n" \
"ds_read_b64_tr_b16 a[48:49], v222 offset:16384\n" \
"v_mfma_f32_16x16x32_bf16 v[72:75], a[4:7], a[212:215], v[72:75]\n" \
"ds_read_b128 a[0:3], v202 offset:18432\n" \
"v_mfma_f32_16x16x32_bf16 v[80:83], a[8:11], a[208:211], v[80:83]\n" \
"ds_read_b128 a[4:7], v203 offset:18432\n" \
"v_mfma_f32_16x16x32_bf16 v[80:83], a[12:15], a[212:215], v[80:83]\n" \
"buffer_load_dwordx4 v192, s[44:47], s73 offen offset:2048 lds\n" \
"v_mfma_f32_16x16x32_bf16 v[84:87], a[8:11], a[216:219], v[84:87]\n" \
"ds_read_b64_tr_b16 a[50:51], v222 offset:19456\n" \
"v_mfma_f32_16x16x32_bf16 v[84:87], a[12:15], a[220:223], v[84:87]\n" \
"ds_read_b128 a[8:11], v200 offset:0\n" \
"v_mfma_f32_16x16x32_bf16 v[92:95], a[16:19], a[216:219], v[92:95]\n" \
"ds_read_b128 a[12:15], v201 offset:0\n" \
"v_mfma_f32_16x16x32_bf16 v[92:95], a[20:23], a[220:223], v[92:95]\n" \
"buffer_load_dwordx4 v193, s[44:47], s73 offen offset:3072 lds\n" \
"v_mfma_f32_16x16x32_bf16 v[88:91], a[16:19], a[208:211], v[88:91]\n" \
"s_xor_b32 s73, s73, 0x180\n" \
"s_mul_i32 s76, s80, 393216\n" \
"ds_read_b64_tr_b16 a[52:53], v222 offset:28672\n" \
"v_mfma_f32_16x16x32_bf16 v[88:91], a[20:23], a[212:215], v[88:91]\n" \
"s_add_u32 s76, s76, s78\n" \
"ds_read_b128 a[16:19], v200 offset:4096\n" \
"v_mfma_f32_16x16x32_bf16 v[96:99], a[24:27], a[208:211], v[96:99]\n" \
"s_add_u32 m0, s91, 81920\n" \
"ds_read_b128 a[20:23], v201 offset:4096\n" \
"v_mfma_f32_16x16x32_bf16 v[96:99], a[28:31], a[212:215], v[96:99]\n" \
"buffer_load_dwordx4 v197, s[48:51], s76 offen offset:0 lds\n" \
"v_mfma_f32_16x16x32_bf16 v[100:103], a[24:27], a[216:219], v[100:103]\n" \
"ds_read_b64_tr_b16 a[54:55], v222 offset:31744\n" \
"v_mfma_f32_16x16x32_bf16 v[100:103], a[28:31], a[220:223], v[100:103]\n" \
"ds_read_b128 a[24:27], v200 offset:8192\n" \
"v_mfma_f32_16x16x32_bf16 v[108:111], a[192:195], a[216:219], v[108:111]\n" \
"s_add_u32 s77, s76, 24576\n" \
"ds_read_b128 a[28:31], v201 offset:8192\n" \
"v_mfma_f32_16x16x32_bf16 v[108:111], a[196:199], a[220:223], v[108:111]\n" \
"buffer_load_dwordx4 v198, s[48:51], s76 offen offset:1024 lds\n" \
"v_mfma_f32_16x16x32_bf16 v[104:107], a[192:195], a[208:211], v[104:107]\n" \
"v_mfma_f32_16x16x32_bf16 v[104:107], a[196:199], a[212:215], v[104:107]\n" \
"s_waitcnt lgkmcnt(2)\n" \
"v_mfma_f32_16x16x32_bf16 v[32:35], a[32:35], a[0:3], v[32:35]\n" \
"s_xor_b32 s80, s80, 0x3\n" \
"ds_read_b128 a[192:195], v200 offset:12288\n" \
"v_mfma_f32_16x16x32_bf16 v[32:35], a[36:39], a[4:7], v[32:35]\n" \
"buffer_load_dwordx4 v199, s[48:51], s76 offen offset:2048 lds\n" \
"v_mfma_f32_16x16x32_bf16 v[40:43], a[32:35], a[8:11], v[40:43]\n" \
"ds_read_b128 a[196:199], v201 offset:12288\n" \
"v_mfma_f32_16x16x32_bf16 v[40:43], a[36:39], a[12:15], v[40:43]\n" \
"ds_read_b64_tr_b16 a[200:201], v222 offset:17408\n" \
"v_mfma_f32_16x16x32_bf16 v[0:3], a[40:43], a[8:11], v[0:3]\n" \
"s_add_u32 m0, m0, 3072\n" \
"ds_read_b64_tr_b16 a[202:203], v222 offset:20480\n" \
"v_mfma_f32_16x16x32_bf16 v[0:3], a[44:47], a[12:15], v[0:3]\n" \
"buffer_load_dwordx4 v197, s[48:51], s77 offen offset:0 lds\n" \
"v_mfma_f32_16x16x32_bf16 v[4:7], a[48:51], a[8:11], v[4:7]\n" \
"ds_read_b64_tr_b16 a[204:205], v222 offset:29696\n" \
"v_mfma_f32_16x16x32_bf16 v[4:7], a[52:55], a[12:15], v[4:7]\n" \
"ds_read_b64_tr_b16 a[206:207], v222 offset:32768\n" \
"v_mfma_f32_16x16x32_bf16 v[12:15], a[48:51], a[16:19], v[12:15]\n" \
"ds_read_b64_tr_b16 a[208:209], v204 offset:18432\n" \
"v_mfma_f32_16x16x32_bf16 v[12:15], a[52:55], a[20:23], v[12:15]\n" \
"buffer_load_dwordx4 v198, s[48:51], s77 offen offset:1024 lds\n" \
"v_mfma_f32_16x16x32_bf16 v[8:11], a[40:43], a[16:19], v[8:11]\n" \
"ds_read_b64_tr_b16 a[210:211], v204 offset:21504\n" \
"v_mfma_f32_16x16x32_bf16 v[8:11], a[44:47], a[20:23], v[8:11]\n" \
"ds_read_b64_tr_b16 a[212:213], v204 offset:30720\n" \
"v_mfma_f32_16x16x32_bf16 v[48:51], a[32:35], a[16:19], v[48:51]\n" \
"s_waitcnt lgkmcnt(3)\n" \
"ds_read_b64_tr_b16 a[214:215], v204 offset:33792\n" \
"v_mfma_f32_16x16x32_bf16 v[48:51], a[36:39], a[20:23], v[48:51]\n" \
"buffer_load_dwordx4 v199, s[48:51], s77 offen offset:2048 lds\n" \
"v_mfma_f32_16x16x32_bf16 v[56:59], a[32:35], a[24:27], v[56:59]\n" \
"ds_read_b64_tr_b16 a[216:217], v222 offset:18432\n" \
"v_mfma_f32_16x16x32_bf16 v[56:59], a[36:39], a[28:31], v[56:59]\n" \
"ds_read_b64_tr_b16 a[218:219], v222 offset:21504\n" \
"v_mfma_f32_16x16x32_bf16 v[16:19], a[40:43], a[24:27], v[16:19]\n" \
"ds_read_b64_tr_b16 a[220:221], v222 offset:30720\n" \
"v_mfma_f32_16x16x32_bf16 v[16:19], a[44:47], a[28:31], v[16:19]\n" \
"v_mfma_f32_16x16x32_bf16 v[20:23], a[48:51], a[24:27], v[20:23]\n" \
"ds_read_b64_tr_b16 a[222:223], v222 offset:33792\n" \
"v_mfma_f32_16x16x32_bf16 v[20:23], a[52:55], a[28:31], v[20:23]\n" \
"v_mfma_f32_16x16x32_bf16 v[28:31], a[48:51], a[192:195], v[28:31]\n" \
"v_mfma_f32_16x16x32_bf16 v[28:31], a[52:55], a[196:199], v[28:31]\n" \
"v_mfma_f32_16x16x32_bf16 v[24:27], a[40:43], a[192:195], v[24:27]\n" \
"v_mfma_f32_16x16x32_bf16 v[24:27], a[44:47], a[196:199], v[24:27]\n" \
"v_mfma_f32_16x16x32_bf16 v[64:67], a[32:35], a[192:195], v[64:67]\n" \
"v_mfma_f32_16x16x32_bf16 v[64:67], a[36:39], a[196:199], v[64:67]\n" \
"s_waitcnt vmcnt(10) lgkmcnt(0)\n" \
"s_barrier\n" \
"v_mfma_f32_16x16x32_bf16 v[68:71], a[200:203], a[192:195], v[68:71]\n" \
"ds_read_b64_tr_b16 a[32:33], v209 offset:25600\n" \
"ds_read_b64_tr_b16 a[34:35], v209 offset:28672\n" \
"v_mfma_f32_16x16x32_bf16 v[68:71], a[204:207], a[196:199], v[68:71]\n" \
"ds_read_b64_tr_b16 a[36:37], v209 offset:37888\n" \
"ds_read_b64_tr_b16 a[38:39], v209 offset:40960\n" \
"v_mfma_f32_16x16x32_bf16 v[60:63], a[200:203], a[24:27], v[60:63]\n" \
"s_add_u32 m0, s79, 0\n" \
"ds_read_b64_tr_b16 a[40:41], v209 offset:24576\n" \
"v_mfma_f32_16x16x32_bf16 v[60:63], a[204:207], a[28:31], v[60:63]\n" \
"buffer_load_dwordx4 v192, s[44:47], s73 offen offset:0 lds\n" \
"v_mfma_f32_16x16x32_bf16 v[52:55], a[200:203], a[16:19], v[52:55]\n" \
"ds_read_b64_tr_b16 a[42:43], v209 offset:27648\n" \
"v_mfma_f32_16x16x32_bf16 v[52:55], a[204:207], a[20:23], v[52:55]\n" \
"ds_read_b64_tr_b16 a[44:45], v209 offset:36864\n" \
"Lpg9059_%=:\n" \
"Lst1x2_%=:\n" \
"s_cbranch_execz Lpg10905_%=\n" \
"v_mfma_f32_16x16x32_bf16 v[44:47], a[200:203], a[8:11], v[44:47]\n" \
"ds_read_b64_tr_b16 a[46:47], v209 offset:39936\n" \
"v_mfma_f32_16x16x32_bf16 v[44:47], a[204:207], a[12:15], v[44:47]\n" \
"buffer_load_dwordx4 v193, s[44:47], s73 offen offset:1024 lds\n" \
"v_mfma_f32_16x16x32_bf16 v[72:75], a[0:3], a[208:211], v[72:75]\n" \
"ds_read_b64_tr_b16 a[48:49], v210 offset:24576\n" \
"v_mfma_f32_16x16x32_bf16 v[72:75], a[4:7], a[212:215], v[72:75]\n" \
"ds_read_b128 a[0:3], v202 offset:20480\n" \
"v_mfma_f32_16x16x32_bf16 v[80:83], a[8:11], a[208:211], v[80:83]\n" \
"ds_read_b128 a[4:7], v203 offset:20480\n" \
"v_mfma_f32_16x16x32_bf16 v[80:83], a[12:15], a[212:215], v[80:83]\n" \
"buffer_load_dwordx4 v192, s[44:47], s73 offen offset:2048 lds\n" \
"v_mfma_f32_16x16x32_bf16 v[84:87], a[8:11], a[216:219], v[84:87]\n" \
"ds_read_b64_tr_b16 a[50:51], v210 offset:27648\n" \
"v_mfma_f32_16x16x32_bf16 v[84:87], a[12:15], a[220:223], v[84:87]\n" \
"ds_read_b128 a[8:11], v200 offset:40960\n" \
"v_mfma_f32_16x16x32_bf16 v[92:95], a[16:19], a[216:219], v[92:95]\n" \
"ds_read_b128 a[12:15], v201 offset:40960\n" \
"v_mfma_f32_16x16x32_bf16 v[92:95], a[20:23], a[220:223], v[92:95]\n" \
"buffer_load_dwordx4 v193, s[44:47], s73 offen offset:3072 lds\n" \
"v_mfma_f32_16x16x32_bf16 v[88:91], a[16:19], a[208:211], v[88:91]\n" \
"s_xor_b32 s73, s73, 0x80\n" \
"s_mul_i32 s76, s80, 393216\n" \
"ds_read_b64_tr_b16 a[52:53], v210 offset:36864\n" \
"v_mfma_f32_16x16x32_bf16 v[88:91], a[20:23], a[212:215], v[88:91]\n" \
"s_add_u32 s76, s76, s78\n" \
"ds_read_b128 a[16:19], v200 offset:45056\n" \
"v_mfma_f32_16x16x32_bf16 v[96:99], a[24:27], a[208:211], v[96:99]\n" \
"s_add_u32 m0, s91, 16384\n" \
"ds_read_b128 a[20:23], v201 offset:45056\n" \
"v_mfma_f32_16x16x32_bf16 v[96:99], a[28:31], a[212:215], v[96:99]\n" \
"buffer_load_dwordx4 v197, s[48:51], s76 offen offset:0 lds\n" \
"v_mfma_f32_16x16x32_bf16 v[100:103], a[24:27], a[216:219], v[100:103]\n" \
"ds_read_b64_tr_b16 a[54:55], v210 offset:39936\n" \
"v_mfma_f32_16x16x32_bf16 v[100:103], a[28:31], a[220:223], v[100:103]\n" \
"ds_read_b128 a[24:27], v200 offset:49152\n" \
"v_mfma_f32_16x16x32_bf16 v[108:111], a[192:195], a[216:219], v[108:111]\n" \
"s_add_u32 s77, s76, 24576\n" \
"ds_read_b128 a[28:31], v201 offset:49152\n" \
"v_mfma_f32_16x16x32_bf16 v[108:111], a[196:199], a[220:223], v[108:111]\n" \
"buffer_load_dwordx4 v198, s[48:51], s76 offen offset:1024 lds\n" \
"v_mfma_f32_16x16x32_bf16 v[104:107], a[192:195], a[208:211], v[104:107]\n" \
"v_mfma_f32_16x16x32_bf16 v[104:107], a[196:199], a[212:215], v[104:107]\n" \
"s_waitcnt lgkmcnt(2)\n" \
"v_mfma_f32_16x16x32_bf16 v[32:35], a[32:35], a[0:3], v[32:35]\n" \
"s_xor_b32 s80, s80, 0x1\n" \
"ds_read_b128 a[192:195], v200 offset:53248\n" \
"v_mfma_f32_16x16x32_bf16 v[32:35], a[36:39], a[4:7], v[32:35]\n" \
"buffer_load_dwordx4 v199, s[48:51], s76 offen offset:2048 lds\n" \
"v_mfma_f32_16x16x32_bf16 v[40:43], a[32:35], a[8:11], v[40:43]\n" \
"ds_read_b128 a[196:199], v201 offset:53248\n" \
"v_mfma_f32_16x16x32_bf16 v[40:43], a[36:39], a[12:15], v[40:43]\n" \
"ds_read_b64_tr_b16 a[200:201], v210 offset:25600\n" \
"v_mfma_f32_16x16x32_bf16 v[0:3], a[40:43], a[8:11], v[0:3]\n" \
"s_add_u32 m0, m0, 3072\n" \
"ds_read_b64_tr_b16 a[202:203], v210 offset:28672\n" \
"v_mfma_f32_16x16x32_bf16 v[0:3], a[44:47], a[12:15], v[0:3]\n" \
"buffer_load_dwordx4 v197, s[48:51], s77 offen offset:0 lds\n" \
"v_mfma_f32_16x16x32_bf16 v[4:7], a[48:51], a[8:11], v[4:7]\n" \
"ds_read_b64_tr_b16 a[204:205], v210 offset:37888\n" \
"v_mfma_f32_16x16x32_bf16 v[4:7], a[52:55], a[12:15], v[4:7]\n" \
"ds_read_b64_tr_b16 a[206:207], v210 offset:40960\n" \
"v_mfma_f32_16x16x32_bf16 v[12:15], a[48:51], a[16:19], v[12:15]\n" \
"ds_read_b64_tr_b16 a[208:209], v209 offset:26624\n" \
"v_mfma_f32_16x16x32_bf16 v[12:15], a[52:55], a[20:23], v[12:15]\n" \
"buffer_load_dwordx4 v198, s[48:51], s77 offen offset:1024 lds\n" \
"v_mfma_f32_16x16x32_bf16 v[8:11], a[40:43], a[16:19], v[8:11]\n" \
"ds_read_b64_tr_b16 a[210:211], v209 offset:29696\n" \
"v_mfma_f32_16x16x32_bf16 v[8:11], a[44:47], a[20:23], v[8:11]\n" \
"ds_read_b64_tr_b16 a[212:213], v209 offset:38912\n" \
"v_mfma_f32_16x16x32_bf16 v[48:51], a[32:35], a[16:19], v[48:51]\n" \
"s_waitcnt lgkmcnt(3)\n" \
"ds_read_b64_tr_b16 a[214:215], v209 offset:41984\n" \
"v_mfma_f32_16x16x32_bf16 v[48:51], a[36:39], a[20:23], v[48:51]\n" \
"buffer_load_dwordx4 v199, s[48:51], s77 offen offset:2048 lds\n" \
"v_mfma_f32_16x16x32_bf16 v[56:59], a[32:35], a[24:27], v[56:59]\n" \
"ds_read_b64_tr_b16 a[216:217], v210 offset:26624\n" \
"v_mfma_f32_16x16x32_bf16 v[56:59], a[36:39], a[28:31], v[56:59]\n" \
"ds_read_b64_tr_b16 a[218:219], v210 offset:29696\n" \
"v_mfma_f32_16x16x32_bf16 v[16:19], a[40:43], a[24:27], v[16:19]\n" \
"ds_read_b64_tr_b16 a[220:221], v210 offset:38912\n" \
"v_mfma_f32_16x16x32_bf16 v[16:19], a[44:47], a[28:31], v[16:19]\n" \
"v_mfma_f32_16x16x32_bf16 v[20:23], a[48:51], a[24:27], v[20:23]\n" \
"ds_read_b64_tr_b16 a[222:223], v210 offset:41984\n" \
"v_mfma_f32_16x16x32_bf16 v[20:23], a[52:55], a[28:31], v[20:23]\n" \
"v_mfma_f32_16x16x32_bf16 v[28:31], a[48:51], a[192:195], v[28:31]\n" \
"v_mfma_f32_16x16x32_bf16 v[28:31], a[52:55], a[196:199], v[28:31]\n" \
"buffer_load_dword v220, v221, s[56:59], s66 offen sc1\n" \
"v_mfma_f32_16x16x32_bf16 v[24:27], a[40:43], a[192:195], v[24:27]\n" \
"v_mfma_f32_16x16x32_bf16 v[24:27], a[44:47], a[196:199], v[24:27]\n" \
"v_mfma_f32_16x16x32_bf16 v[64:67], a[32:35], a[192:195], v[64:67]\n" \
"v_mfma_f32_16x16x32_bf16 v[64:67], a[36:39], a[196:199], v[64:67]\n" \
"s_waitcnt vmcnt(11) lgkmcnt(0)\n" \
"s_barrier\n" \
"v_mfma_f32_16x16x32_bf16 v[68:71], a[200:203], a[192:195], v[68:71]\n" \
"ds_read_b64_tr_b16 a[32:33], v209 offset:1024\n" \
"ds_read_b64_tr_b16 a[34:35], v209 offset:4096\n" \
"v_mfma_f32_16x16x32_bf16 v[68:71], a[204:207], a[196:199], v[68:71]\n" \
"ds_read_b64_tr_b16 a[36:37], v209 offset:13312\n" \
"ds_read_b64_tr_b16 a[38:39], v209 offset:16384\n" \
"v_mfma_f32_16x16x32_bf16 v[60:63], a[200:203], a[24:27], v[60:63]\n" \
"s_add_u32 m0, s79, 131072\n" \
"ds_read_b64_tr_b16 a[40:41], v209 offset:0\n" \
"v_mfma_f32_16x16x32_bf16 v[60:63], a[204:207], a[28:31], v[60:63]\n" \
"buffer_load_dwordx4 v192, s[44:47], s73 offen offset:0 lds\n" \
"v_mfma_f32_16x16x32_bf16 v[52:55], a[200:203], a[16:19], v[52:55]\n" \
"ds_read_b64_tr_b16 a[42:43], v209 offset:3072\n" \
"v_mfma_f32_16x16x32_bf16 v[52:55], a[204:207], a[20:23], v[52:55]\n" \
"ds_read_b64_tr_b16 a[44:45], v209 offset:12288\n" \
"v_mfma_f32_16x16x32_bf16 v[44:47], a[200:203], a[8:11], v[44:47]\n" \
"ds_read_b64_tr_b16 a[46:47], v209 offset:15360\n" \
"v_mfma_f32_16x16x32_bf16 v[44:47], a[204:207], a[12:15], v[44:47]\n" \
"buffer_load_dwordx4 v193, s[44:47], s73 offen offset:1024 lds\n" \
"v_mfma_f32_16x16x32_bf16 v[72:75], a[0:3], a[208:211], v[72:75]\n" \
"ds_read_b64_tr_b16 a[48:49], v210 offset:0\n" \
"v_mfma_f32_16x16x32_bf16 v[72:75], a[4:7], a[212:215], v[72:75]\n" \
"ds_read_b128 a[0:3], v202 offset:21504\n" \
"v_mfma_f32_16x16x32_bf16 v[80:83], a[8:11], a[208:211], v[80:83]\n" \
"ds_read_b128 a[4:7], v203 offset:21504\n" \
"v_mfma_f32_16x16x32_bf16 v[80:83], a[12:15], a[212:215], v[80:83]\n" \
"buffer_load_dwordx4 v192, s[44:47], s73 offen offset:2048 lds\n" \
"v_mfma_f32_16x16x32_bf16 v[84:87], a[8:11], a[216:219], v[84:87]\n" \
"ds_read_b64_tr_b16 a[50:51], v210 offset:3072\n" \
"v_mfma_f32_16x16x32_bf16 v[84:87], a[12:15], a[220:223], v[84:87]\n" \
"ds_read_b128 a[8:11], v205 offset:0\n" \
"v_mfma_f32_16x16x32_bf16 v[92:95], a[16:19], a[216:219], v[92:95]\n" \
"ds_read_b128 a[12:15], v206 offset:0\n" \
"v_mfma_f32_16x16x32_bf16 v[92:95], a[20:23], a[220:223], v[92:95]\n" \
"buffer_load_dwordx4 v193, s[44:47], s73 offen offset:3072 lds\n" \
"v_mfma_f32_16x16x32_bf16 v[88:91], a[16:19], a[208:211], v[88:91]\n" \
"s_xor_b32 s73, s73, 0x80\n" \
"s_mul_i32 s76, s80, 393216\n" \
"ds_read_b64_tr_b16 a[52:53], v210 offset:12288\n" \
"v_mfma_f32_16x16x32_bf16 v[88:91], a[20:23], a[212:215], v[88:91]\n" \
"s_add_u32 s76, s76, s78\n" \
"ds_read_b128 a[16:19], v205 offset:4096\n" \
"v_mfma_f32_16x16x32_bf16 v[96:99], a[24:27], a[208:211], v[96:99]\n" \
"s_add_u32 m0, s91, 106496\n" \
"ds_read_b128 a[20:23], v206 offset:4096\n" \
"v_mfma_f32_16x16x32_bf16 v[96:99], a[28:31], a[212:215], v[96:99]\n" \
"buffer_load_dwordx4 v197, s[48:51], s76 offen offset:0 lds\n" \
"v_mfma_f32_16x16x32_bf16 v[100:103], a[24:27], a[216:219], v[100:103]\n" \
"ds_read_b64_tr_b16 a[54:55], v210 offset:15360\n" \
"v_mfma_f32_16x16x32_bf16 v[100:103], a[28:31], a[220:223], v[100:103]\n" \
"ds_read_b128 a[24:27], v205 offset:8192\n" \
"v_mfma_f32_16x16x32_bf16 v[108:111], a[192:195], a[216:219], v[108:111]\n" \
"s_add_u32 s77, s76, 24576\n" \
"ds_read_b128 a[28:31], v206 offset:8192\n" \
"v_mfma_f32_16x16x32_bf16 v[108:111], a[196:199], a[220:223], v[108:111]\n" \
"buffer_load_dwordx4 v198, s[48:51], s76 offen offset:1024 lds\n" \
"v_mfma_f32_16x16x32_bf16 v[104:107], a[192:195], a[208:211], v[104:107]\n" \
"v_mfma_f32_16x16x32_bf16 v[104:107], a[196:199], a[212:215], v[104:107]\n" \
"s_waitcnt lgkmcnt(2)\n" \
"v_mfma_f32_16x16x32_bf16 v[32:35], a[32:35], a[0:3], v[32:35]\n" \
"s_xor_b32 s80, s80, 0x1\n" \
"ds_read_b128 a[192:195], v205 offset:12288\n" \
"v_mfma_f32_16x16x32_bf16 v[32:35], a[36:39], a[4:7], v[32:35]\n" \
"buffer_load_dwordx4 v199, s[48:51], s76 offen offset:2048 lds\n" \
"v_mfma_f32_16x16x32_bf16 v[40:43], a[32:35], a[8:11], v[40:43]\n" \
"ds_read_b128 a[196:199], v206 offset:12288\n" \
"v_mfma_f32_16x16x32_bf16 v[40:43], a[36:39], a[12:15], v[40:43]\n" \
"ds_read_b64_tr_b16 a[200:201], v210 offset:1024\n" \
"v_mfma_f32_16x16x32_bf16 v[0:3], a[40:43], a[8:11], v[0:3]\n" \
"s_add_u32 m0, m0, 3072\n" \
"ds_read_b64_tr_b16 a[202:203], v210 offset:4096\n" \
"v_mfma_f32_16x16x32_bf16 v[0:3], a[44:47], a[12:15], v[0:3]\n" \
"buffer_load_dwordx4 v197, s[48:51], s77 offen offset:0 lds\n" \
"v_mfma_f32_16x16x32_bf16 v[4:7], a[48:51], a[8:11], v[4:7]\n" \
"ds_read_b64_tr_b16 a[204:205], v210 offset:13312\n" \
"v_mfma_f32_16x16x32_bf16 v[4:7], a[52:55], a[12:15], v[4:7]\n" \
"ds_read_b64_tr_b16 a[206:207], v210 offset:16384\n" \
"v_mfma_f32_16x16x32_bf16 v[12:15], a[48:51], a[16:19], v[12:15]\n" \
"ds_read_b64_tr_b16 a[208:209], v209 offset:2048\n" \
"v_mfma_f32_16x16x32_bf16 v[12:15], a[52:55], a[20:23], v[12:15]\n" \
"buffer_load_dwordx4 v198, s[48:51], s77 offen offset:1024 lds\n" \
"v_mfma_f32_16x16x32_bf16 v[8:11], a[40:43], a[16:19], v[8:11]\n" \
"ds_read_b64_tr_b16 a[210:211], v209 offset:5120\n" \
"v_mfma_f32_16x16x32_bf16 v[8:11], a[44:47], a[20:23], v[8:11]\n" \
"ds_read_b64_tr_b16 a[212:213], v209 offset:14336\n" \
"v_mfma_f32_16x16x32_bf16 v[48:51], a[32:35], a[16:19], v[48:51]\n" \
"s_waitcnt lgkmcnt(3)\n" \
"ds_read_b64_tr_b16 a[214:215], v209 offset:17408\n" \
"v_mfma_f32_16x16x32_bf16 v[48:51], a[36:39], a[20:23], v[48:51]\n" \
"buffer_load_dwordx4 v199, s[48:51], s77 offen offset:2048 lds\n" \
"v_mfma_f32_16x16x32_bf16 v[56:59], a[32:35], a[24:27], v[56:59]\n" \
"ds_read_b64_tr_b16 a[216:217], v210 offset:2048\n" \
"v_mfma_f32_16x16x32_bf16 v[56:59], a[36:39], a[28:31], v[56:59]\n" \
"ds_read_b64_tr_b16 a[218:219], v210 offset:5120\n" \
"v_mfma_f32_16x16x32_bf16 v[16:19], a[40:43], a[24:27], v[16:19]\n" \
"ds_read_b64_tr_b16 a[220:221], v210 offset:14336\n" \
"v_mfma_f32_16x16x32_bf16 v[16:19], a[44:47], a[28:31], v[16:19]\n" \
"v_mfma_f32_16x16x32_bf16 v[20:23], a[48:51], a[24:27], v[20:23]\n" \
"s_mov_b32 s46, s81\n" \
"s_mov_b32 s47, s82\n" \
"ds_read_b64_tr_b16 a[222:223], v210 offset:17408\n" \
"v_mfma_f32_16x16x32_bf16 v[20:23], a[52:55], a[28:31], v[20:23]\n" \
"s_mov_b32 s48, 0\n" \
"s_mov_b32 s49, s68\n" \
"s_sub_u32 s50, 64, s9\n" \
"v_mfma_f32_16x16x32_bf16 v[28:31], a[48:51], a[192:195], v[28:31]\n" \
"s_cmp_eq_u32 s90, 0\n" \
"s_cselect_b32 s50, s9, s50\n" \
"s_andn2_b32 s50, s50, 3\n" \
"v_mfma_f32_16x16x32_bf16 v[28:31], a[52:55], a[196:199], v[28:31]\n" \
"s_lshl_b32 s50, s50, 12\n" \
"s_waitcnt vmcnt(10)\n" \
"v_cmp_eq_u32 vcc, s60, v220\n" \
"v_mfma_f32_16x16x32_bf16 v[24:27], a[40:43], a[192:195], v[24:27]\n" \
"v_mfma_f32_16x16x32_bf16 v[24:27], a[44:47], a[196:199], v[24:27]\n" \
"s_andn2_b32 s70, s65, vcc_lo\n" \
"s_cbranch_scc1 Lpl5_%=\n" \
"Lpr5_%=:\n" \
"v_mfma_f32_16x16x32_bf16 v[64:67], a[32:35], a[192:195], v[64:67]\n" \
"v_mfma_f32_16x16x32_bf16 v[64:67], a[36:39], a[196:199], v[64:67]\n" \
"s_xor_b32 s82, s48, s49\n" \
"s_cmp_lt_u32 s48, s50\n" \
"s_cselect_b32 s82, s82, s48\n" \
"s_waitcnt vmcnt(11) lgkmcnt(0)\n" \
"s_barrier\n" \
"v_mfma_f32_16x16x32_bf16 v[68:71], a[200:203], a[192:195], v[68:71]\n" \
"ds_read_b64_tr_b16 a[32:33], v204 offset:17408\n" \
"ds_read_b64_tr_b16 a[34:35], v204 offset:20480\n" \
"v_mfma_f32_16x16x32_bf16 v[68:71], a[204:207], a[196:199], v[68:71]\n" \
"s_add_u32 s81, s82, s46\n" \
"s_lshr_b32 s78, s61, 1\n" \
"s_lshl_b32 s80, s78, 9\n" \
"s_add_u32 s80, s80, 511\n" \
"ds_read_b64_tr_b16 a[36:37], v204 offset:29696\n" \
"ds_read_b64_tr_b16 a[38:39], v204 offset:32768\n" \
"v_mfma_f32_16x16x32_bf16 v[60:63], a[200:203], a[24:27], v[60:63]\n" \
"s_add_u32 m0, s86, s79\n" \
"ds_read_b64_tr_b16 a[40:41], v204 offset:16384\n" \
"v_sub_u32 v145, s80, v234\n" \
"v_mfma_f32_16x16x32_bf16 v[60:63], a[204:207], a[28:31], v[60:63]\n" \
"buffer_load_dwordx4 v234, s[56:59], s81 offen offset:0 sc1 lds\n" \
"v_mfma_f32_16x16x32_bf16 v[52:55], a[200:203], a[16:19], v[52:55]\n" \
"s_lshl_b32 s80, s78, 3\n" \
"s_not_b32 s80, s80\n" \
"ds_read_b64_tr_b16 a[42:43], v204 offset:19456\n" \
"v_mfma_f32_16x16x32_bf16 v[52:55], a[204:207], a[20:23], v[52:55]\n" \
"s_lshl_b32 s82, s82, 5\n" \
"ds_read_b64_tr_b16 a[44:45], v204 offset:28672\n" \
"v_sub_u32 v146, s80, v238\n" \
"v_mfma_f32_16x16x32_bf16 v[44:47], a[200:203], a[8:11], v[44:47]\n" \
"s_add_u32 s82, s82, s47\n" \
"ds_read_b64_tr_b16 a[46:47], v204 offset:31744\n" \
"v_subrev_u32 v152, 0, v238\n" \
"v_mfma_f32_16x16x32_bf16 v[44:47], a[204:207], a[12:15], v[44:47]\n" \
"buffer_load_dwordx4 v234, s[56:59], s81 offen offset:1024 sc1 lds\n" \
"v_mfma_f32_16x16x32_bf16 v[72:75], a[0:3], a[208:211], v[72:75]\n" \
"ds_read_b64_tr_b16 a[48:49], v222 offset:16384\n" \
"v_sub_u32 v144, 7, v238\n" \
"v_mfma_f32_16x16x32_bf16 v[72:75], a[4:7], a[212:215], v[72:75]\n" \
"ds_read_b128 a[0:3], v202 offset:22528\n" \
"v_or_b32 v152, v152, v144\n" \
"v_mfma_f32_16x16x32_bf16 v[80:83], a[8:11], a[208:211], v[80:83]\n" \
"ds_read_b128 a[4:7], v203 offset:22528\n" \
"v_ashrrev_i32 v152, 31, v152\n" \
"v_mfma_f32_16x16x32_bf16 v[80:83], a[12:15], a[212:215], v[80:83]\n" \
"buffer_load_dwordx4 v234, s[56:59], s81 offen offset:2048 sc1 lds\n" \
"v_mfma_f32_16x16x32_bf16 v[84:87], a[8:11], a[216:219], v[84:87]\n" \
"ds_read_b64_tr_b16 a[50:51], v222 offset:19456\n" \
"v_and_b32 v152, 0xff800000, v152\n" \
"v_mfma_f32_16x16x32_bf16 v[84:87], a[12:15], a[220:223], v[84:87]\n" \
"ds_read_b128 a[8:11], v200 offset:0\n" \
"v_sub_u32 v156, -9, v238\n" \
"v_mfma_f32_16x16x32_bf16 v[92:95], a[16:19], a[216:219], v[92:95]\n" \
"ds_read_b128 a[12:15], v201 offset:0\n" \
"v_ashrrev_i32 v156, 31, v156\n" \
"v_mfma_f32_16x16x32_bf16 v[92:95], a[20:23], a[220:223], v[92:95]\n" \
"buffer_load_dwordx4 v234, s[56:59], s81 offen offset:3072 sc1 lds\n" \
"v_mfma_f32_16x16x32_bf16 v[88:91], a[16:19], a[208:211], v[88:91]\n" \
"ds_read_b64_tr_b16 a[52:53], v222 offset:28672\n" \
"v_and_b32 v156, 0xff800000, v156\n" \
"v_mfma_f32_16x16x32_bf16 v[88:91], a[20:23], a[212:215], v[88:91]\n" \
"ds_read_b128 a[16:19], v200 offset:4096\n" \
"v_add_u32 v148, 0, v146\n" \
"v_mfma_f32_16x16x32_bf16 v[96:99], a[24:27], a[208:211], v[96:99]\n" \
"s_add_u32 m0, m0, 0x4000\n" \
"ds_read_b128 a[20:23], v201 offset:4096\n" \
"v_or3_b32 v148, v148, v145, s92\n" \
"v_mfma_f32_16x16x32_bf16 v[96:99], a[28:31], a[212:215], v[96:99]\n" \
"buffer_load_dwordx4 v216, s[36:39], s82 offen offset:0 lds\n" \
"v_mfma_f32_16x16x32_bf16 v[100:103], a[24:27], a[216:219], v[100:103]\n" \
"ds_read_b64_tr_b16 a[54:55], v222 offset:31744\n" \
"v_ashrrev_i32 v148, 31, v148\n" \
"v_mfma_f32_16x16x32_bf16 v[100:103], a[28:31], a[220:223], v[100:103]\n" \
"ds_read_b128 a[24:27], v200 offset:8192\n" \
"v_and_b32 v148, 0xff800000, v148\n" \
"v_mfma_f32_16x16x32_bf16 v[108:111], a[192:195], a[216:219], v[108:111]\n" \
"s_add_u32 s48, s48, 0x1000\n" \
"ds_read_b128 a[28:31], v201 offset:8192\n" \
"v_subrev_u32 v153, 1, v238\n" \
"v_mfma_f32_16x16x32_bf16 v[108:111], a[196:199], a[220:223], v[108:111]\n" \
"buffer_load_dwordx4 v217, s[36:39], s82 offen offset:1024 lds\n" \
"v_mfma_f32_16x16x32_bf16 v[104:107], a[192:195], a[208:211], v[104:107]\n" \
"v_sub_u32 v144, 8, v238\n" \
"v_or_b32 v153, v153, v144\n" \
"v_mfma_f32_16x16x32_bf16 v[104:107], a[196:199], a[212:215], v[104:107]\n" \
"s_waitcnt lgkmcnt(2)\n" \
"v_ashrrev_i32 v153, 31, v153\n" \
"v_and_b32 v153, 0xff800000, v153\n" \
"v_mfma_f32_16x16x32_bf16 v[32:35], a[32:35], a[0:3], v[32:35]\n" \
"s_sub_u32 s86, s86, 32768\n" \
"s_cselect_b32 s86, 131072, s86\n" \
"ds_read_b128 a[192:195], v200 offset:12288\n" \
"v_mfma_f32_16x16x32_bf16 v[32:35], a[36:39], a[4:7], v[32:35]\n" \
"buffer_load_dwordx4 v218, s[36:39], s82 offen offset:2048 lds\n" \
"v_mfma_f32_16x16x32_bf16 v[40:43], a[32:35], a[8:11], v[40:43]\n" \
"ds_read_b128 a[196:199], v201 offset:12288\n" \
"v_sub_u32 v157, -8, v238\n" \
"v_mfma_f32_16x16x32_bf16 v[40:43], a[36:39], a[12:15], v[40:43]\n" \
"ds_read_b64_tr_b16 a[200:201], v222 offset:17408\n" \
"v_ashrrev_i32 v157, 31, v157\n" \
"v_mfma_f32_16x16x32_bf16 v[0:3], a[40:43], a[8:11], v[0:3]\n" \
"ds_read_b64_tr_b16 a[202:203], v222 offset:20480\n" \
"v_and_b32 v157, 0xff800000, v157\n" \
"v_mfma_f32_16x16x32_bf16 v[0:3], a[44:47], a[12:15], v[0:3]\n" \
"buffer_load_dwordx4 v219, s[36:39], s82 offen offset:3072 lds\n" \
"v_mfma_f32_16x16x32_bf16 v[4:7], a[48:51], a[8:11], v[4:7]\n" \
"ds_read_b64_tr_b16 a[204:205], v222 offset:29696\n" \
"v_add_u32 v149, 1, v146\n" \
"v_mfma_f32_16x16x32_bf16 v[4:7], a[52:55], a[12:15], v[4:7]\n" \
"ds_read_b64_tr_b16 a[206:207], v222 offset:32768\n" \
"v_or3_b32 v149, v149, v145, s92\n" \
"v_mfma_f32_16x16x32_bf16 v[12:15], a[48:51], a[16:19], v[12:15]\n" \
"ds_read_b64_tr_b16 a[208:209], v204 offset:18432\n" \
"v_ashrrev_i32 v149, 31, v149\n" \
"v_mfma_f32_16x16x32_bf16 v[12:15], a[52:55], a[20:23], v[12:15]\n" \
"v_and_b32 v149, 0xff800000, v149\n" \
"v_subrev_u32 v154, 2, v238\n" \
"v_mfma_f32_16x16x32_bf16 v[8:11], a[40:43], a[16:19], v[8:11]\n" \
"ds_read_b64_tr_b16 a[210:211], v204 offset:21504\n" \
"v_sub_u32 v144, 9, v238\n" \
"v_mfma_f32_16x16x32_bf16 v[8:11], a[44:47], a[20:23], v[8:11]\n" \
"ds_read_b64_tr_b16 a[212:213], v204 offset:30720\n" \
"v_or_b32 v154, v154, v144\n" \
"v_mfma_f32_16x16x32_bf16 v[48:51], a[32:35], a[16:19], v[48:51]\n" \
"ds_read_b64_tr_b16 a[214:215], v204 offset:33792\n" \
"v_ashrrev_i32 v154, 31, v154\n" \
"v_mfma_f32_16x16x32_bf16 v[48:51], a[36:39], a[20:23], v[48:51]\n" \
"s_waitcnt lgkmcnt(3)\n" \
"v_and_b32 v154, 0xff800000, v154\n" \
"v_sub_u32 v158, -7, v238\n" \
"v_mfma_f32_16x16x32_bf16 v[56:59], a[32:35], a[24:27], v[56:59]\n" \
"ds_read_b64_tr_b16 a[216:217], v222 offset:18432\n" \
"v_ashrrev_i32 v158, 31, v158\n" \
"v_mfma_f32_16x16x32_bf16 v[56:59], a[36:39], a[28:31], v[56:59]\n" \
"ds_read_b64_tr_b16 a[218:219], v222 offset:21504\n" \
"v_and_b32 v158, 0xff800000, v158\n" \
"v_mfma_f32_16x16x32_bf16 v[16:19], a[40:43], a[24:27], v[16:19]\n" \
"ds_read_b64_tr_b16 a[220:221], v222 offset:30720\n" \
"v_add_u32 v150, 2, v146\n" \
"v_mfma_f32_16x16x32_bf16 v[16:19], a[44:47], a[28:31], v[16:19]\n" \
"v_or3_b32 v150, v150, v145, s92\n" \
"v_ashrrev_i32 v150, 31, v150\n" \
"v_mfma_f32_16x16x32_bf16 v[20:23], a[48:51], a[24:27], v[20:23]\n" \
"ds_read_b64_tr_b16 a[222:223], v222 offset:33792\n" \
"v_and_b32 v150, 0xff800000, v150\n" \
"v_mfma_f32_16x16x32_bf16 v[20:23], a[52:55], a[28:31], v[20:23]\n" \
"v_subrev_u32 v155, 3, v238\n" \
"v_sub_u32 v144, 10, v238\n" \
"v_mfma_f32_16x16x32_bf16 v[28:31], a[48:51], a[192:195], v[28:31]\n" \
"v_or_b32 v155, v155, v144\n" \
"v_ashrrev_i32 v155, 31, v155\n" \
"v_mfma_f32_16x16x32_bf16 v[28:31], a[52:55], a[196:199], v[28:31]\n" \
"v_and_b32 v155, 0xff800000, v155\n" \
"v_sub_u32 v159, -6, v238\n" \
"v_mfma_f32_16x16x32_bf16 v[24:27], a[40:43], a[192:195], v[24:27]\n" \
"v_ashrrev_i32 v159, 31, v159\n" \
"v_and_b32 v159, 0xff800000, v159\n" \
"v_mfma_f32_16x16x32_bf16 v[24:27], a[44:47], a[196:199], v[24:27]\n" \
"v_add_u32 v151, 3, v146\n" \
"v_or3_b32 v151, v151, v145, s92\n" \
"v_mfma_f32_16x16x32_bf16 v[64:67], a[32:35], a[192:195], v[64:67]\n" \
"v_ashrrev_i32 v151, 31, v151\n" \
"v_and_b32 v151, 0xff800000, v151\n" \
"v_mfma_f32_16x16x32_bf16 v[64:67], a[36:39], a[196:199], v[64:67]\n" \
"s_xor_b32 s82, s48, s49\n" \
"s_cmp_lt_u32 s48, s50\n" \
"s_cselect_b32 s82, s82, s48\n" \
"s_waitcnt vmcnt(8) lgkmcnt(0)\n" \
"s_barrier\n" \
"v_mfma_f32_16x16x32_bf16 v[68:71], a[200:203], a[192:195], v[68:71]\n" \
"ds_read_b64_tr_b16 a[32:33], v209 offset:25600\n" \
"ds_read_b64_tr_b16 a[34:35], v209 offset:28672\n" \
"v_mfma_f32_16x16x32_bf16 v[68:71], a[204:207], a[196:199], v[68:71]\n" \
"s_add_u32 s81, s82, s46\n" \
"ds_read_b64_tr_b16 a[36:37], v209 offset:37888\n" \
"ds_read_b64_tr_b16 a[38:39], v209 offset:40960\n" \
"v_mfma_f32_16x16x32_bf16 v[60:63], a[200:203], a[24:27], v[60:63]\n" \
"s_add_u32 m0, s86, s79\n" \
"ds_read_b64_tr_b16 a[40:41], v209 offset:24576\n" \
"v_mov_b32 v186, 0xffff0000\n" \
"v_mfma_f32_16x16x32_bf16 v[60:63], a[204:207], a[28:31], v[60:63]\n" \
"buffer_load_dwordx4 v234, s[56:59], s81 offen offset:0 sc1 lds\n" \
"v_mfma_f32_16x16x32_bf16 v[52:55], a[200:203], a[16:19], v[52:55]\n" \
"ds_read_b64_tr_b16 a[42:43], v209 offset:27648\n" \
"v_subrev_u32 v144, 0, v238\n" \
"v_mfma_f32_16x16x32_bf16 v[52:55], a[204:207], a[20:23], v[52:55]\n" \
"s_lshl_b32 s82, s82, 5\n" \
"ds_read_b64_tr_b16 a[44:45], v209 offset:36864\n" \
"v_subrev_u32 v184, 1, v238\n" \
"v_mfma_f32_16x16x32_bf16 v[44:47], a[200:203], a[8:11], v[44:47]\n" \
"s_add_u32 s82, s82, s47\n" \
"ds_read_b64_tr_b16 a[46:47], v209 offset:39936\n" \
"v_lshrrev_b32 v144, 16, v144\n" \
"v_mfma_f32_16x16x32_bf16 v[44:47], a[204:207], a[12:15], v[44:47]\n" \
"buffer_load_dwordx4 v234, s[56:59], s81 offen offset:1024 sc1 lds\n" \
"v_mfma_f32_16x16x32_bf16 v[72:75], a[0:3], a[208:211], v[72:75]\n" \
"ds_read_b64_tr_b16 a[48:49], v210 offset:24576\n" \
"v_and_or_b32 v184, v184, v186, v144\n" \
"v_mfma_f32_16x16x32_bf16 v[72:75], a[4:7], a[212:215], v[72:75]\n" \
"ds_read_b128 a[0:3], v202 offset:23552\n" \
"v_subrev_u32 v144, 2, v238\n" \
"v_mfma_f32_16x16x32_bf16 v[80:83], a[8:11], a[208:211], v[80:83]\n" \
"ds_read_b128 a[4:7], v203 offset:23552\n" \
"v_subrev_u32 v185, 3, v238\n" \
"v_mfma_f32_16x16x32_bf16 v[80:83], a[12:15], a[212:215], v[80:83]\n" \
"buffer_load_dwordx4 v234, s[56:59], s81 offen offset:2048 sc1 lds\n" \
"v_mfma_f32_16x16x32_bf16 v[84:87], a[8:11], a[216:219], v[84:87]\n" \
"ds_read_b64_tr_b16 a[50:51], v210 offset:27648\n" \
"v_lshrrev_b32 v144, 16, v144\n" \
"v_mfma_f32_16x16x32_bf16 v[84:87], a[12:15], a[220:223], v[84:87]\n" \
"ds_read_b128 a[8:11], v207 offset:0\n" \
"v_and_or_b32 v185, v185, v186, v144\n" \
"v_mfma_f32_16x16x32_bf16 v[92:95], a[16:19], a[216:219], v[92:95]\n" \
"ds_read_b128 a[12:15], v208 offset:0\n" \
"v_mov_b32 v144, 0x3e8293ee\n" \
"v_mfma_f32_16x16x32_bf16 v[92:95], a[20:23], a[220:223], v[92:95]\n" \
"buffer_load_dwordx4 v234, s[56:59], s81 offen offset:3072 sc1 lds\n" \
"v_mfma_f32_16x16x32_bf16 v[88:91], a[16:19], a[208:211], v[88:91]\n" \
"s_add_u32 s80, s79, 117760\n" \
"ds_read_b64_tr_b16 a[52:53], v210 offset:36864\n" \
"v_add_u32 v223, s80, v234\n" \
"v_mfma_f32_16x16x32_bf16 v[88:91], a[20:23], a[212:215], v[88:91]\n" \
"s_cmp_lg_u32 s78, 0\n" \
"s_cselect_b64 s[76:77], -1, 0\n" \
"ds_read_b128 a[16:19], v207 offset:4096\n" \
"v_mfma_f32_16x16x32_bf16 v[96:99], a[24:27], a[208:211], v[96:99]\n" \
"s_add_u32 m0, m0, 0x4000\n" \
"ds_read_b128 a[20:23], v208 offset:4096\n" \
"v_mfma_f32_16x16x32_bf16 v[96:99], a[28:31], a[212:215], v[96:99]\n" \
"buffer_load_dwordx4 v216, s[36:39], s82 offen offset:0 lds\n" \
"v_mfma_f32_16x16x32_bf16 v[100:103], a[24:27], a[216:219], v[100:103]\n" \
"ds_read_b64_tr_b16 a[54:55], v210 offset:39936\n" \
"v_mfma_f32_16x16x32_bf16 v[100:103], a[28:31], a[220:223], v[100:103]\n" \
"ds_read_b128 a[24:27], v207 offset:8192\n" \
"v_mfma_f32_16x16x32_bf16 v[108:111], a[192:195], a[216:219], v[108:111]\n" \
"s_add_u32 s48, s48, 0x1000\n" \
"ds_read_b128 a[28:31], v208 offset:8192\n" \
"v_mfma_f32_16x16x32_bf16 v[108:111], a[196:199], a[220:223], v[108:111]\n" \
"buffer_load_dwordx4 v217, s[36:39], s82 offen offset:1024 lds\n" \
"v_mfma_f32_16x16x32_bf16 v[104:107], a[192:195], a[208:211], v[104:107]\n" \
"v_mfma_f32_16x16x32_bf16 v[104:107], a[196:199], a[212:215], v[104:107]\n" \
"s_waitcnt lgkmcnt(2)\n" \
"v_mfma_f32_16x16x32_bf16 v[32:35], a[32:35], a[0:3], v[32:35]\n" \
"s_sub_u32 s86, s86, 32768\n" \
"s_cselect_b32 s86, 131072, s86\n" \
"ds_read_b128 a[192:195], v207 offset:12288\n" \
"v_mfma_f32_16x16x32_bf16 v[32:35], a[36:39], a[4:7], v[32:35]\n" \
"buffer_load_dwordx4 v218, s[36:39], s82 offen offset:2048 lds\n" \
"v_mfma_f32_16x16x32_bf16 v[40:43], a[32:35], a[8:11], v[40:43]\n" \
"ds_read_b128 a[196:199], v208 offset:12288\n" \
"v_mfma_f32_16x16x32_bf16 v[40:43], a[36:39], a[12:15], v[40:43]\n" \
"ds_read_b64_tr_b16 a[200:201], v210 offset:25600\n" \
"v_mfma_f32_16x16x32_bf16 v[0:3], a[40:43], a[8:11], v[0:3]\n" \
"ds_read_b64_tr_b16 a[202:203], v210 offset:28672\n" \
"v_mfma_f32_16x16x32_bf16 v[0:3], a[44:47], a[12:15], v[0:3]\n" \
"buffer_load_dwordx4 v219, s[36:39], s82 offen offset:3072 lds\n" \
"v_mfma_f32_16x16x32_bf16 v[4:7], a[48:51], a[8:11], v[4:7]\n" \
"s_xor_b32 s82, s48, s49\n" \
"s_cmp_lt_u32 s48, s50\n" \
"s_cselect_b32 s82, s82, s48\n" \
"ds_read_b64_tr_b16 a[204:205], v210 offset:37888\n" \
"v_mfma_f32_16x16x32_bf16 v[4:7], a[52:55], a[12:15], v[4:7]\n" \
"s_add_u32 s81, s82, s46\n" \
"ds_read_b64_tr_b16 a[206:207], v210 offset:40960\n" \
"v_mfma_f32_16x16x32_bf16 v[12:15], a[48:51], a[16:19], v[12:15]\n" \
"s_add_u32 m0, s86, s79\n" \
"ds_read_b64_tr_b16 a[208:209], v209 offset:26624\n" \
"v_mfma_f32_16x16x32_bf16 v[12:15], a[52:55], a[20:23], v[12:15]\n" \
"buffer_load_dwordx4 v234, s[56:59], s81 offen offset:0 sc1 lds\n" \
"v_mfma_f32_16x16x32_bf16 v[8:11], a[40:43], a[16:19], v[8:11]\n" \
"ds_read_b64_tr_b16 a[210:211], v209 offset:29696\n" \
"v_mfma_f32_16x16x32_bf16 v[8:11], a[44:47], a[20:23], v[8:11]\n" \
"s_lshl_b32 s82, s82, 5\n" \
"ds_read_b64_tr_b16 a[212:213], v209 offset:38912\n" \
"v_mfma_f32_16x16x32_bf16 v[48:51], a[32:35], a[16:19], v[48:51]\n" \
"s_waitcnt lgkmcnt(3)\n" \
"s_add_u32 s82, s82, s47\n" \
"ds_read_b64_tr_b16 a[214:215], v209 offset:41984\n" \
"v_mfma_f32_16x16x32_bf16 v[48:51], a[36:39], a[20:23], v[48:51]\n" \
"buffer_load_dwordx4 v234, s[56:59], s81 offen offset:1024 sc1 lds\n" \
"v_mfma_f32_16x16x32_bf16 v[56:59], a[32:35], a[24:27], v[56:59]\n" \
"ds_read_b64_tr_b16 a[216:217], v210 offset:26624\n" \
"v_mfma_f32_16x16x32_bf16 v[56:59], a[36:39], a[28:31], v[56:59]\n" \
"ds_read_b64_tr_b16 a[218:219], v210 offset:29696\n" \
"v_mfma_f32_16x16x32_bf16 v[16:19], a[40:43], a[24:27], v[16:19]\n" \
"ds_read_b64_tr_b16 a[220:221], v210 offset:38912\n" \
"v_mfma_f32_16x16x32_bf16 v[16:19], a[44:47], a[28:31], v[16:19]\n" \
"buffer_load_dwordx4 v234, s[56:59], s81 offen offset:2048 sc1 lds\n" \
"v_mfma_f32_16x16x32_bf16 v[20:23], a[48:51], a[24:27], v[20:23]\n" \
"ds_read_b64_tr_b16 a[222:223], v210 offset:41984\n" \
"v_mfma_f32_16x16x32_bf16 v[20:23], a[52:55], a[28:31], v[20:23]\n" \
"v_mfma_f32_16x16x32_bf16 v[28:31], a[48:51], a[192:195], v[28:31]\n" \
"v_mfma_f32_16x16x32_bf16 v[28:31], a[52:55], a[196:199], v[28:31]\n" \
"buffer_load_dwordx4 v234, s[56:59], s81 offen offset:3072 sc1 lds\n" \
"v_mfma_f32_16x16x32_bf16 v[24:27], a[40:43], a[192:195], v[24:27]\n" \
"v_mfma_f32_16x16x32_bf16 v[24:27], a[44:47], a[196:199], v[24:27]\n" \
"v_mfma_f32_16x16x32_bf16 v[64:67], a[32:35], a[192:195], v[64:67]\n" \
"s_add_u32 m0, m0, 0x4000\n" \
"v_mfma_f32_16x16x32_bf16 v[64:67], a[36:39], a[196:199], v[64:67]\n" \
"buffer_load_dwordx4 v216, s[36:39], s82 offen offset:0 lds\n" \
"s_waitcnt vmcnt(13) lgkmcnt(0)\n" \
"s_barrier\n" \
"v_mfma_f32_16x16x32_bf16 v[44:47], a[200:203], a[8:11], v[44:47]\n" \
"ds_read_b64_tr_b16 a[96:97], v214 offset:16384\n" \
"v_mfma_f32_16x16x32_bf16 v[44:47], a[204:207], a[12:15], v[44:47]\n" \
"ds_read_b64_tr_b16 a[98:99], v214 offset:18432\n" \
"v_mfma_f32_16x16x32_bf16 v[52:55], a[200:203], a[16:19], v[52:55]\n" \
"s_add_u32 s48, s48, 0x1000\n" \
"ds_read_b64_tr_b16 a[100:101], v214 offset:24576\n" \
"v_mfma_f32_16x16x32_bf16 v[52:55], a[204:207], a[20:23], v[52:55]\n" \
"buffer_load_dwordx4 v217, s[36:39], s82 offen offset:1024 lds\n" \
"v_mfma_f32_16x16x32_bf16 v[60:63], a[200:203], a[24:27], v[60:63]\n" \
"ds_read_b64_tr_b16 a[102:103], v214 offset:26624\n" \
"v_cvt_pk_bf16_f32 v112, v0, v1\n" \
"v_mfma_f32_16x16x32_bf16 v[60:63], a[204:207], a[28:31], v[60:63]\n" \
"ds_read_b128 a[64:67], v213 offset:0\n" \
"v_cvt_pk_bf16_f32 v113, v2, v3\n" \
"v_mfma_f32_16x16x32_bf16 v[68:71], a[200:203], a[192:195], v[68:71]\n" \
"s_sub_u32 s86, s86, 32768\n" \
"s_cselect_b32 s86, 131072, s86\n" \
"ds_read_b128 a[68:71], v213 offset:2048\n" \
"v_mfma_f32_16x16x32_bf16 v[68:71], a[204:207], a[196:199], v[68:71]\n" \
"buffer_load_dwordx4 v218, s[36:39], s82 offen offset:2048 lds\n" \
"v_mfma_f32_16x16x32_bf16 v[72:75], a[0:3], a[208:211], v[72:75]\n" \
"ds_read_b64_tr_b16 a[104:105], v215 offset:16384\n" \
"v_cvt_pk_bf16_f32 v114, v4, v5\n" \
"v_mfma_f32_16x16x32_bf16 v[72:75], a[4:7], a[212:215], v[72:75]\n" \
"ds_read_b64_tr_b16 a[106:107], v215 offset:18432\n" \
"v_cvt_pk_bf16_f32 v115, v6, v7\n" \
"v_mfma_f32_16x16x32_bf16 v[80:83], a[8:11], a[208:211], v[80:83]\n" \
"ds_read_b64_tr_b16 a[108:109], v215 offset:24576\n" \
"v_cvt_pk_bf16_f32 v116, v8, v9\n" \
"v_mfma_f32_16x16x32_bf16 v[80:83], a[12:15], a[212:215], v[80:83]\n" \
"buffer_load_dwordx4 v219, s[36:39], s82 offen offset:3072 lds\n" \
"v_mfma_f32_16x16x32_bf16 v[84:87], a[8:11], a[216:219], v[84:87]\n" \
"s_xor_b32 s82, s48, s49\n" \
"s_cmp_lt_u32 s48, s50\n" \
"s_cselect_b32 s82, s82, s48\n" \
"ds_read_b64_tr_b16 a[110:111], v215 offset:26624\n" \
"v_mfma_f32_16x16x32_bf16 v[84:87], a[12:15], a[220:223], v[84:87]\n" \
"s_add_u32 s81, s82, s46\n" \
"ds_read_b128 a[72:75], v213 offset:256\n" \
"v_cvt_pk_bf16_f32 v117, v10, v11\n" \
"v_mfma_f32_16x16x32_bf16 v[92:95], a[16:19], a[216:219], v[92:95]\n" \
"s_add_u32 m0, s86, s79\n" \
"ds_read_b128 a[76:79], v213 offset:2304\n" \
"v_cvt_pk_bf16_f32 v118, v12, v13\n" \
"v_mfma_f32_16x16x32_bf16 v[92:95], a[20:23], a[220:223], v[92:95]\n" \
"buffer_load_dwordx4 v234, s[56:59], s81 offen offset:0 sc1 lds\n" \
"v_mfma_f32_16x16x32_bf16 v[88:91], a[16:19], a[208:211], v[88:91]\n" \
"ds_read_b128 a[80:83], v213 offset:4096\n" \
"Lpg9625_%=:\n" \
"Lst1x3_%=:\n" \
"s_cbranch_execz Lpg11466_%=\n" \
"v_cvt_pk_bf16_f32 v119, v14, v15\n" \
"v_mfma_f32_16x16x32_bf16 v[88:91], a[20:23], a[212:215], v[88:91]\n" \
"s_lshl_b32 s82, s82, 5\n" \
"ds_read_b128 a[84:87], v213 offset:6144\n" \
"v_cvt_pk_bf16_f32 v120, v16, v17\n" \
"v_mfma_f32_16x16x32_bf16 v[96:99], a[24:27], a[208:211], v[96:99]\n" \
"s_add_u32 s82, s82, s47\n" \
"v_cvt_pk_bf16_f32 v121, v18, v19\n" \
"v_cvt_pk_bf16_f32 v122, v20, v21\n" \
"v_mfma_f32_16x16x32_bf16 v[96:99], a[28:31], a[212:215], v[96:99]\n" \
"buffer_load_dwordx4 v234, s[56:59], s81 offen offset:1024 sc1 lds\n" \
"v_mfma_f32_16x16x32_bf16 v[100:103], a[24:27], a[216:219], v[100:103]\n" \
"v_cvt_pk_bf16_f32 v123, v22, v23\n" \
"v_cvt_pk_bf16_f32 v124, v24, v25\n" \
"v_mfma_f32_16x16x32_bf16 v[100:103], a[28:31], a[220:223], v[100:103]\n" \
"v_cvt_pk_bf16_f32 v125, v26, v27\n" \
"v_cvt_pk_bf16_f32 v126, v28, v29\n" \
"v_mfma_f32_16x16x32_bf16 v[108:111], a[192:195], a[216:219], v[108:111]\n" \
"v_cvt_pk_bf16_f32 v127, v30, v31\n" \
"v_cvt_pk_bf16_f32 v128, v32, v33\n" \
"v_mfma_f32_16x16x32_bf16 v[108:111], a[196:199], a[220:223], v[108:111]\n" \
"buffer_load_dwordx4 v234, s[56:59], s81 offen offset:2048 sc1 lds\n" \
"v_mfma_f32_16x16x32_bf16 v[104:107], a[192:195], a[208:211], v[104:107]\n" \
"v_cvt_pk_bf16_f32 v129, v34, v35\n" \
"v_xor_b32 v145, 0x2000, v223\n" \
"v_mfma_f32_16x16x32_bf16 v[104:107], a[196:199], a[212:215], v[104:107]\n" \
"s_waitcnt lgkmcnt(0)\n" \
"v_cvt_pk_bf16_f32 v192, v72, v73\n" \
"v_cvt_pk_bf16_f32 v193, v74, v75\n" \
"ds_write_b64 v145, v[128:129]\n" \
"s_nop 0\n" \
"ds_write_b64 v145, v[192:193] offset:8\n" \
"v_cvt_pk_bf16_f32 v132, v40, v41\n" \
"v_cvt_pk_bf16_f32 v133, v42, v43\n" \
"v_cvt_pk_bf16_f32 v134, v44, v45\n" \
"v_cvt_pk_bf16_f32 v135, v46, v47\n" \
"v_cvt_pk_bf16_f32 v136, v48, v49\n" \
"v_cvt_pk_bf16_f32 v137, v50, v51\n" \
"v_cvt_pk_bf16_f32 v138, v52, v53\n" \
"v_cvt_pk_bf16_f32 v139, v54, v55\n" \
"v_cvt_pk_bf16_f32 v140, v56, v57\n" \
"v_cvt_pk_bf16_f32 v141, v58, v59\n" \
"v_cvt_pk_bf16_f32 v142, v60, v61\n" \
"v_cvt_pk_bf16_f32 v143, v62, v63\n" \
"v_mfma_f32_16x16x32_bf16 v[0:3], a[96:99], a[64:67], 0\n" \
"ds_read_b128 a[88:91], v213 offset:4352\n" \
"v_mfma_f32_16x16x32_bf16 v[0:3], a[100:103], a[68:71], v[0:3]\n" \
"ds_read_b128 a[92:95], v213 offset:6400\n" \
"v_mfma_f32_16x16x32_bf16 v[4:7], a[104:107], a[64:67], 0\n" \
"ds_read_b64_tr_b16 a[112:113], v214 offset:16896\n" \
"v_mfma_f32_16x16x32_bf16 v[4:7], a[108:111], a[68:71], v[4:7]\n" \
"ds_read_b64_tr_b16 a[114:115], v214 offset:18944\n" \
"v_mfma_f32_16x16x32_bf16 v[20:23], a[104:107], a[72:75], 0\n" \
"ds_read_b64_tr_b16 a[116:117], v214 offset:25088\n" \
"v_cvt_pk_bf16_f32 v64, v64, v65\n" \
"v_mfma_f32_16x16x32_bf16 v[20:23], a[108:111], a[76:79], v[20:23]\n" \
"buffer_load_dwordx4 v234, s[56:59], s81 offen offset:3072 sc1 lds\n" \
"v_cvt_pk_bf16_f32 v65, v66, v67\n" \
"v_cvt_pk_bf16_f32 v66, v68, v69\n" \
"v_mfma_f32_16x16x32_bf16 v[16:19], a[96:99], a[72:75], 0\n" \
"s_sub_u32 s85, s85, 32768\n" \
"s_cselect_b32 s85, 131072, s85\n" \
"ds_read_b64_tr_b16 a[118:119], v214 offset:27136\n" \
"v_mfma_f32_16x16x32_bf16 v[16:19], a[100:103], a[76:79], v[16:19]\n" \
"s_waitcnt lgkmcnt(3)\n" \
"ds_read_b64_tr_b16 a[120:121], v215 offset:16896\n" \
"v_cvt_pk_bf16_f32 v67, v70, v71\n" \
"v_cvt_pk_bf16_f32 v194, v80, v81\n" \
"v_cvt_pk_bf16_f32 v195, v82, v83\n" \
"v_cvt_pk_bf16_f32 v74, v84, v85\n" \
"v_mfma_f32_16x16x32_bf16 v[32:35], a[96:99], a[80:83], 0\n" \
"s_add_u32 m0, m0, 0x4000\n" \
"ds_read_b64_tr_b16 a[122:123], v215 offset:18944\n" \
"v_cvt_pk_bf16_f32 v75, v86, v87\n" \
"v_mfma_f32_16x16x32_bf16 v[32:35], a[100:103], a[84:87], v[32:35]\n" \
"buffer_load_dwordx4 v216, s[36:39], s82 offen offset:0 lds\n" \
"v_cvt_pk_bf16_f32 v196, v88, v89\n" \
"v_cvt_pk_bf16_f32 v197, v90, v91\n" \
"v_mfma_f32_16x16x32_bf16 v[176:179], v[64:67], v[124:127], v[152:155]\n" \
"v_mfma_f32_16x16x32_bf16 v[172:175], v[140:143], v[124:127], v[156:159]\n" \
"v_mfma_f32_16x16x32_bf16 v[168:171], v[140:143], v[120:123], v[152:155]\n" \
"v_mfma_f32_16x16x32_bf16 v[164:167], v[136:139], v[120:123], v[156:159]\n" \
"v_mfma_f32_16x16x32_bf16 v[160:163], v[136:139], v[116:119], v[152:155]\n" \
"v_mfma_f32_16x16x32_bf16 v[156:159], v[132:135], v[116:119], v[156:159]\n" \
"v_mfma_f32_16x16x32_bf16 v[152:155], v[132:135], v[112:115], v[152:155]\n" \
"v_mfma_f32_16x16x32_bf16 v[36:39], a[104:107], a[80:83], 0\n" \
"s_add_u32 s48, s48, 0x1000\n" \
"ds_read_b64_tr_b16 a[124:125], v215 offset:25088\n" \
"v_add_u32 v213, s85, v211\n" \
"v_mfma_f32_16x16x32_bf16 v[36:39], a[108:111], a[84:87], v[36:39]\n" \
"buffer_load_dwordx4 v217, s[36:39], s82 offen offset:1024 lds\n" \
"v_mfma_f32_16x16x32_bf16 v[52:55], a[104:107], a[88:91], 0\n" \
"s_sub_u32 s86, s86, 32768\n" \
"s_cselect_b32 s86, 131072, s86\n" \
"ds_read_b64_tr_b16 a[126:127], v215 offset:27136\n" \
"v_mfma_f32_16x16x32_bf16 v[52:55], a[108:111], a[92:95], v[52:55]\n" \
"buffer_load_dwordx4 v218, s[36:39], s82 offen offset:2048 lds\n" \
"v_mfma_f32_16x16x32_bf16 v[48:51], a[96:99], a[88:91], 0\n" \
"v_add_u32 v214, s85, v212\n" \
"v_xor_b32 v215, 32, v214\n" \
"v_mfma_f32_16x16x32_bf16 v[48:51], a[100:103], a[92:95], v[48:51]\n" \
"s_waitcnt vmcnt(15) lgkmcnt(0)\n" \
"s_barrier\n" \
"v_mfma_f32_16x16x32_bf16 v[8:11], a[112:115], a[64:67], 0\n" \
"ds_read_b64 v[130:131], v223\n" \
"ds_read_b64_tr_b16 a[96:97], v214 offset:16384\n" \
"ds_read_b64_tr_b16 a[98:99], v214 offset:18432\n" \
"v_mfma_f32_16x16x32_bf16 v[8:11], a[116:119], a[68:71], v[8:11]\n" \
"ds_read_b64 v[72:73], v223 offset:8\n" \
"buffer_load_dwordx4 v219, s[36:39], s82 offen offset:3072 lds\n" \
"v_mfma_f32_16x16x32_bf16 v[12:15], a[120:123], a[64:67], 0\n" \
"s_xor_b32 s82, s48, s49\n" \
"s_cmp_lt_u32 s48, s50\n" \
"s_cselect_b32 s82, s82, s48\n" \
"ds_read_b64_tr_b16 a[100:101], v214 offset:24576\n" \
"ds_read_b64_tr_b16 a[102:103], v214 offset:26624\n" \
"v_mfma_f32_16x16x32_bf16 v[12:15], a[124:127], a[68:71], v[12:15]\n" \
"s_add_u32 s81, s82, s46\n" \
"ds_read_b128 a[64:67], v213 offset:0\n" \
"v_mfma_f32_16x16x32_bf16 v[28:31], a[120:123], a[72:75], 0\n" \
"s_add_u32 m0, s86, s79\n" \
"ds_read_b128 a[68:71], v213 offset:2048\n" \
"v_mfma_f32_16x16x32_bf16 v[28:31], a[124:127], a[76:79], v[28:31]\n" \
"buffer_load_dwordx4 v234, s[56:59], s81 offen offset:0 sc1 lds\n" \
"v_mfma_f32_16x16x32_bf16 v[24:27], a[112:115], a[72:75], 0\n" \
"ds_read_b64_tr_b16 a[104:105], v215 offset:16384\n" \
"ds_read_b64_tr_b16 a[106:107], v215 offset:18432\n" \
"v_mfma_f32_16x16x32_bf16 v[24:27], a[116:119], a[76:79], v[24:27]\n" \
"s_waitcnt lgkmcnt(4)\n" \
"s_lshl_b32 s82, s82, 5\n" \
"ds_read_b128 a[72:75], v213 offset:256\n" \
"v_mfma_f32_16x16x32_bf16 v[40:43], a[112:115], a[80:83], 0\n" \
"s_add_u32 s82, s82, s47\n" \
"ds_read_b128 a[76:79], v213 offset:2304\n" \
"v_mfma_f32_16x16x32_bf16 v[148:151], v[128:131], v[112:115], v[148:151]\n" \
"v_max_f32 v172, v172, v176\n" \
"v_max_f32 v173, v173, v177\n" \
"v_max_f32 v174, v174, v178\n" \
"v_max_f32 v175, v175, v179\n" \
"v_max_f32 v164, v164, v168\n" \
"v_max_f32 v165, v165, v169\n" \
"v_max_f32 v166, v166, v170\n" \
"v_max_f32 v167, v167, v171\n" \
"v_max_f32 v156, v156, v160\n" \
"v_max_f32 v157, v157, v161\n" \
"v_max_f32 v158, v158, v162\n" \
"v_max_f32 v159, v159, v163\n" \
"v_max_f32 v148, v148, v152\n" \
"v_max_f32 v149, v149, v153\n" \
"v_max_f32 v150, v150, v154\n" \
"v_max_f32 v151, v151, v155\n" \
"v_max3_f32 v183, v172, v173, v174\n" \
"v_max3_f32 v182, v164, v165, v166\n" \
"v_max3_f32 v181, v156, v157, v158\n" \
"v_mfma_f32_16x16x32_bf16 v[40:43], a[116:119], a[84:87], v[40:43]\n" \
"buffer_load_dwordx4 v234, s[56:59], s81 offen offset:1024 sc1 lds\n" \
"v_max3_f32 v180, v148, v149, v150\n" \
"v_mfma_f32_16x16x32_bf16 v[44:47], a[120:123], a[80:83], 0\n" \
"ds_read_b64_tr_b16 a[108:109], v215 offset:24576\n" \
"ds_read_b64_tr_b16 a[110:111], v215 offset:26624\n" \
"v_mfma_f32_16x16x32_bf16 v[44:47], a[124:127], a[84:87], v[44:47]\n" \
"buffer_load_dwordx4 v234, s[56:59], s81 offen offset:2048 sc1 lds\n" \
"v_max_f32 v183, v183, v175\n" \
"v_max_f32 v182, v182, v167\n" \
"v_max_f32 v181, v181, v159\n" \
"v_mfma_f32_16x16x32_bf16 v[60:63], a[120:123], a[88:91], 0\n" \
"ds_read_b128 a[80:83], v213 offset:4096\n" \
"s_waitcnt lgkmcnt(4)\n" \
"v_mfma_f32_16x16x32_bf16 v[60:63], a[124:127], a[92:95], v[60:63]\n" \
"ds_read_b128 a[84:87], v213 offset:6144\n" \
"v_max_f32 v180, v180, v151\n" \
"v_cvt_pk_bf16_f32 v76, v92, v93\n" \
"v_cvt_pk_bf16_f32 v77, v94, v95\n" \
"v_mfma_f32_16x16x32_bf16 v[56:59], a[112:115], a[88:91], 0\n" \
"buffer_load_dwordx4 v234, s[56:59], s81 offen offset:3072 sc1 lds\n" \
"v_mfma_f32_16x16x32_bf16 v[56:59], a[116:119], a[92:95], v[56:59]\n" \
"s_getpc_b64 s[44:45]\n" \
"Lop_%=:\n" \
"s_add_u32 s44, s44, Lor_%=-Lop_%=\n" \
"s_addc_u32 s45, s45, 0\n" \
"v_permlane32_swap_b32 v180, v181\n" \
"v_permlane32_swap_b32 v182, v183\n" \
"v_max_f32 v180, v180, v181\n" \
"v_mfma_f32_16x16x32_bf16 v[0:3], a[96:99], a[64:67], v[0:3]\n" \
"ds_read_b128 a[88:91], v213 offset:4352\n" \
"v_max_f32 v182, v182, v183\n" \
"v_mfma_f32_16x16x32_bf16 v[0:3], a[100:103], a[68:71], v[0:3]\n" \
"s_waitcnt lgkmcnt(2)\n" \
"ds_read_b128 a[92:95], v213 offset:6400\n" \
"v_cvt_pk_bf16_f32 v198, v96, v97\n" \
"v_cvt_pk_bf16_f32 v199, v98, v99\n" \
"v_permlane16_swap_b32 v180, v182\n" \
"v_mfma_f32_16x16x32_bf16 v[4:7], a[104:107], a[64:67], v[4:7]\n" \
"ds_read_b64_tr_b16 a[112:113], v214 offset:16896\n" \
"v_max_f32 v180, v180, v182\n" \
"v_mfma_f32_16x16x32_bf16 v[4:7], a[108:111], a[68:71], v[4:7]\n" \
"ds_read_b64_tr_b16 a[114:115], v214 offset:18944\n" \
"v_mul_f32 v180, v180, v144\n" \
"v_cvt_pk_bf16_f32 v78, v100, v101\n" \
"v_cvt_pk_bf16_f32 v79, v102, v103\n" \
"v_mov_b32 v182, v180\n" \
"v_mfma_f32_16x16x32_bf16 v[20:23], a[104:107], a[72:75], v[20:23]\n" \
"s_add_u32 m0, m0, 0x4000\n" \
"ds_read_b64_tr_b16 a[116:117], v214 offset:25088\n" \
"v_cvt_pk_bf16_f32 v200, v104, v105\n" \
"v_mfma_f32_16x16x32_bf16 v[20:23], a[108:111], a[76:79], v[20:23]\n" \
"buffer_load_dwordx4 v216, s[36:39], s82 offen offset:0 lds\n" \
"v_cvt_pk_bf16_f32 v201, v106, v107\n" \
"v_permlane16_swap_b32 v180, v182\n" \
"v_mfma_f32_16x16x32_bf16 v[16:19], a[96:99], a[72:75], v[16:19]\n" \
"s_sub_u32 s85, s85, 32768\n" \
"s_cselect_b32 s85, 131072, s85\n" \
"ds_read_b64_tr_b16 a[118:119], v214 offset:27136\n" \
"v_mfma_f32_16x16x32_bf16 v[16:19], a[100:103], a[76:79], v[16:19]\n" \
"s_waitcnt lgkmcnt(3)\n" \
"ds_read_b64_tr_b16 a[120:121], v215 offset:16896\n" \
"v_mov_b32 v181, v180\n" \
"v_mov_b32 v183, v182\n" \
"v_cvt_pk_bf16_f32 v80, v108, v109\n" \
"v_cvt_pk_bf16_f32 v81, v110, v111\n" \
"v_mfma_f32_16x16x32_bf16 v[32:35], a[96:99], a[80:83], v[32:35]\n" \
"s_add_u32 s48, s48, 0x1000\n" \
"ds_read_b64_tr_b16 a[122:123], v215 offset:18944\n" \
"v_permlane32_swap_b32 v180, v181\n" \
"v_mfma_f32_16x16x32_bf16 v[32:35], a[100:103], a[84:87], v[32:35]\n" \
"buffer_load_dwordx4 v217, s[36:39], s82 offen offset:1024 lds\n" \
"v_permlane32_swap_b32 v182, v183\n" \
"v_fma_f32 v172, v172, v144, -v183\n" \
"v_mfma_f32_16x16x32_bf16 v[36:39], a[104:107], a[80:83], v[36:39]\n" \
"s_sub_u32 s86, s86, 32768\n" \
"s_cselect_b32 s86, 131072, s86\n" \
"ds_read_b64_tr_b16 a[124:125], v215 offset:25088\n" \
"v_add_u32 v213, s85, v211\n" \
"v_fma_f32 v164, v164, v144, -v182\n" \
"v_mfma_f32_16x16x32_bf16 v[36:39], a[108:111], a[84:87], v[36:39]\n" \
"buffer_load_dwordx4 v218, s[36:39], s82 offen offset:2048 lds\n" \
"v_fma_f32 v156, v156, v144, -v181\n" \
"v_fma_f32 v148, v148, v144, -v180\n" \
"v_mfma_f32_16x16x32_bf16 v[52:55], a[104:107], a[88:91], v[52:55]\n" \
"ds_read_b64_tr_b16 a[126:127], v215 offset:27136\n" \
"v_fma_f32 v173, v173, v144, -v183\n" \
"v_mfma_f32_16x16x32_bf16 v[52:55], a[108:111], a[92:95], v[52:55]\n" \
"buffer_load_dwordx4 v219, s[36:39], s82 offen offset:3072 lds\n" \
"v_fma_f32 v165, v165, v144, -v182\n" \
"v_fma_f32 v157, v157, v144, -v181\n" \
"v_mfma_f32_16x16x32_bf16 v[48:51], a[96:99], a[88:91], v[48:51]\n" \
"s_xor_b32 s82, s48, s49\n" \
"s_cmp_lt_u32 s48, s50\n" \
"s_cselect_b32 s82, s82, s48\n" \
"v_add_u32 v214, s85, v212\n" \
"v_xor_b32 v215, 32, v214\n" \
"v_fma_f32 v149, v149, v144, -v180\n" \
"v_mfma_f32_16x16x32_bf16 v[48:51], a[100:103], a[92:95], v[48:51]\n" \
"s_add_u32 s81, s82, s46\n" \
"v_fma_f32 v174, v174, v144, -v183\n" \
"v_fma_f32 v166, v166, v144, -v182\n" \
"s_waitcnt vmcnt(16) lgkmcnt(0)\n" \
"s_barrier\n" \
"v_mfma_f32_16x16x32_bf16 v[8:11], a[112:115], a[64:67], v[8:11]\n" \
"s_add_u32 m0, s86, s79\n" \
"ds_read_b64_tr_b16 a[96:97], v214 offset:16384\n" \
"ds_read_b64_tr_b16 a[98:99], v214 offset:18432\n" \
"v_mfma_f32_16x16x32_bf16 v[8:11], a[116:119], a[68:71], v[8:11]\n" \
"buffer_load_dwordx4 v234, s[56:59], s81 offen offset:0 sc1 lds\n" \
"v_fma_f32 v158, v158, v144, -v181\n" \
"v_fma_f32 v150, v150, v144, -v180\n" \
"v_mfma_f32_16x16x32_bf16 v[12:15], a[120:123], a[64:67], v[12:15]\n" \
"ds_read_b64_tr_b16 a[100:101], v214 offset:24576\n" \
"ds_read_b64_tr_b16 a[102:103], v214 offset:26624\n" \
"v_mfma_f32_16x16x32_bf16 v[12:15], a[124:127], a[68:71], v[12:15]\n" \
"s_lshl_b32 s82, s82, 5\n" \
"ds_read_b128 a[64:67], v213 offset:0\n" \
"v_fma_f32 v175, v175, v144, -v183\n" \
"v_fma_f32 v167, v167, v144, -v182\n" \
"v_fma_f32 v159, v159, v144, -v181\n" \
"v_fma_f32 v151, v151, v144, -v180\n" \
"v_mfma_f32_16x16x32_bf16 v[28:31], a[120:123], a[72:75], v[28:31]\n" \
"s_add_u32 s82, s82, s47\n" \
"ds_read_b128 a[68:71], v213 offset:2048\n" \
"v_mfma_f32_16x16x32_bf16 v[28:31], a[124:127], a[76:79], v[28:31]\n" \
"buffer_load_dwordx4 v234, s[56:59], s81 offen offset:1024 sc1 lds\n" \
"v_exp_f32 v172, v172\n" \
"v_mfma_f32_16x16x32_bf16 v[24:27], a[112:115], a[72:75], v[24:27]\n" \
"ds_read_b64_tr_b16 a[104:105], v215 offset:16384\n" \
"ds_read_b64_tr_b16 a[106:107], v215 offset:18432\n" \
"v_mfma_f32_16x16x32_bf16 v[24:27], a[116:119], a[76:79], v[24:27]\n" \
"ds_read_b128 a[72:75], v213 offset:256\n" \
"v_exp_f32 v164, v164\n" \
"v_exp_f32 v156, v156\n" \
"v_mfma_f32_16x16x32_bf16 v[40:43], a[112:115], a[80:83], v[40:43]\n" \
"ds_read_b128 a[76:79], v213 offset:2304\n" \
"v_mfma_f32_16x16x32_bf16 v[40:43], a[116:119], a[84:87], v[40:43]\n" \
"buffer_load_dwordx4 v234, s[56:59], s81 offen offset:2048 sc1 lds\n" \
"v_exp_f32 v148, v148\n" \
"v_mfma_f32_16x16x32_bf16 v[44:47], a[120:123], a[80:83], v[44:47]\n" \
"ds_read_b64_tr_b16 a[108:109], v215 offset:24576\n" \
"ds_read_b64_tr_b16 a[110:111], v215 offset:26624\n" \
"v_mfma_f32_16x16x32_bf16 v[44:47], a[124:127], a[84:87], v[44:47]\n" \
"buffer_load_dwordx4 v234, s[56:59], s81 offen offset:3072 sc1 lds\n" \
"v_exp_f32 v173, v173\n" \
"v_mfma_f32_16x16x32_bf16 v[60:63], a[120:123], a[88:91], v[60:63]\n" \
"ds_read_b128 a[80:83], v213 offset:4096\n" \
"s_waitcnt lgkmcnt(4)\n" \
"v_mfma_f32_16x16x32_bf16 v[60:63], a[124:127], a[92:95], v[60:63]\n" \
"s_add_u32 m0, m0, 0x4000\n" \
"ds_read_b128 a[84:87], v213 offset:6144\n" \
"v_exp_f32 v165, v165\n" \
"v_exp_f32 v157, v157\n" \
"v_mfma_f32_16x16x32_bf16 v[56:59], a[112:115], a[88:91], v[56:59]\n" \
"buffer_load_dwordx4 v216, s[36:39], s82 offen offset:0 lds\n" \
"v_mfma_f32_16x16x32_bf16 v[56:59], a[116:119], a[92:95], v[56:59]\n" \
"v_exp_f32 v149, v149\n" \
"v_exp_f32 v174, v174\n" \
"v_mfma_f32_16x16x32_bf16 v[0:3], a[96:99], a[64:67], v[0:3]\n" \
"ds_read_b128 a[88:91], v213 offset:4352\n" \
"v_mfma_f32_16x16x32_bf16 v[0:3], a[100:103], a[68:71], v[0:3]\n" \
"s_waitcnt lgkmcnt(2)\n" \
"ds_read_b128 a[92:95], v213 offset:6400\n" \
"v_exp_f32 v166, v166\n" \
"v_exp_f32 v158, v158\n" \
"v_mfma_f32_16x16x32_bf16 v[4:7], a[104:107], a[64:67], v[4:7]\n" \
"ds_read_b64_tr_b16 a[112:113], v214 offset:16896\n" \
"v_mfma_f32_16x16x32_bf16 v[4:7], a[108:111], a[68:71], v[4:7]\n" \
"ds_read_b64_tr_b16 a[114:115], v214 offset:18944\n" \
"v_exp_f32 v150, v150\n" \
"v_exp_f32 v175, v175\n" \
"v_mfma_f32_16x16x32_bf16 v[20:23], a[104:107], a[72:75], v[20:23]\n" \
"s_add_u32 s48, s48, 0x1000\n" \
"ds_read_b64_tr_b16 a[116:117], v214 offset:25088\n" \
"v_mfma_f32_16x16x32_bf16 v[20:23], a[108:111], a[76:79], v[20:23]\n" \
"buffer_load_dwordx4 v217, s[36:39], s82 offen offset:1024 lds\n" \
"v_exp_f32 v167, v167\n" \
"v_mfma_f32_16x16x32_bf16 v[16:19], a[96:99], a[72:75], v[16:19]\n" \
"s_sub_u32 s85, s85, 32768\n" \
"s_cselect_b32 s85, 131072, s85\n" \
"ds_read_b64_tr_b16 a[118:119], v214 offset:27136\n" \
"v_mfma_f32_16x16x32_bf16 v[16:19], a[100:103], a[76:79], v[16:19]\n" \
"s_waitcnt lgkmcnt(3)\n" \
"ds_read_b64_tr_b16 a[120:121], v215 offset:16896\n" \
"v_exp_f32 v159, v159\n" \
"v_exp_f32 v151, v151\n" \
"v_mfma_f32_16x16x32_bf16 v[32:35], a[96:99], a[80:83], v[32:35]\n" \
"s_sub_u32 s86, s86, 32768\n" \
"s_cselect_b32 s86, 131072, s86\n" \
"ds_read_b64_tr_b16 a[122:123], v215 offset:18944\n" \
"v_mfma_f32_16x16x32_bf16 v[32:35], a[100:103], a[84:87], v[32:35]\n" \
"buffer_load_dwordx4 v218, s[36:39], s82 offen offset:2048 lds\n" \
"v_add_f32 v191, v172, v173\n" \
"v_add_f32 v190, v164, v165\n" \
"v_mfma_f32_16x16x32_bf16 v[36:39], a[104:107], a[80:83], v[36:39]\n" \
"ds_read_b64_tr_b16 a[124:125], v215 offset:25088\n" \
"v_add_u32 v213, s85, v211\n" \
"v_mfma_f32_16x16x32_bf16 v[36:39], a[108:111], a[84:87], v[36:39]\n" \
"buffer_load_dwordx4 v219, s[36:39], s82 offen offset:3072 lds\n" \
"v_add_f32 v189, v156, v157\n" \
"v_add_f32 v188, v148, v149\n" \
"v_add_f32 v191, v191, v174\n" \
"v_mfma_f32_16x16x32_bf16 v[52:55], a[104:107], a[88:91], v[52:55]\n" \
"s_xor_b32 s82, s48, s49\n" \
"s_cmp_lt_u32 s48, s50\n" \
"s_cselect_b32 s82, s82, s48\n" \
"s_add_u32 s81, s82, s46\n" \
"s_add_u32 m0, s86, s79\n" \
"ds_read_b64_tr_b16 a[126:127], v215 offset:27136\n" \
"v_add_f32 v190, v190, v166\n" \
"v_mfma_f32_16x16x32_bf16 v[52:55], a[108:111], a[92:95], v[52:55]\n" \
"buffer_load_dwordx4 v234, s[56:59], s81 offen offset:0 sc1 lds\n" \
"v_add_f32 v189, v189, v158\n" \
"v_mfma_f32_16x16x32_bf16 v[48:51], a[96:99], a[88:91], v[48:51]\n" \
"v_add_u32 v214, s85, v212\n" \
"v_xor_b32 v215, 32, v214\n" \
"v_mfma_f32_16x16x32_bf16 v[48:51], a[100:103], a[92:95], v[48:51]\n" \
"s_lshl_b32 s82, s82, 5\n" \
"v_add_f32 v188, v188, v150\n" \
"v_add_f32 v191, v191, v175\n" \
"v_add_f32 v190, v190, v167\n" \
"s_waitcnt vmcnt(17) lgkmcnt(0)\n" \
"s_barrier\n" \
"v_mfma_f32_16x16x32_bf16 v[8:11], a[112:115], a[64:67], v[8:11]\n" \
"s_add_u32 s82, s82, s47\n" \
"ds_read_b64_tr_b16 a[96:97], v214 offset:16384\n" \
"ds_read_b64_tr_b16 a[98:99], v214 offset:18432\n" \
"v_mfma_f32_16x16x32_bf16 v[8:11], a[116:119], a[68:71], v[8:11]\n" \
"buffer_load_dwordx4 v234, s[56:59], s81 offen offset:1024 sc1 lds\n" \
"v_add_f32 v189, v189, v159\n" \
"v_add_f32 v188, v188, v151\n" \
"v_mfma_f32_16x16x32_bf16 v[12:15], a[120:123], a[64:67], v[12:15]\n" \
"ds_read_b64_tr_b16 a[100:101], v214 offset:24576\n" \
"ds_read_b64_tr_b16 a[102:103], v214 offset:26624\n" \
"v_mfma_f32_16x16x32_bf16 v[12:15], a[124:127], a[68:71], v[12:15]\n" \
"ds_read_b128 a[64:67], v213 offset:0\n" \
"v_permlane32_swap_b32 v188, v189\n" \
"v_permlane32_swap_b32 v190, v191\n" \
"v_add_f32 v188, v188, v189\n" \
"v_add_f32 v190, v190, v191\n" \
"v_mfma_f32_16x16x32_bf16 v[28:31], a[120:123], a[72:75], v[28:31]\n" \
"ds_read_b128 a[68:71], v213 offset:2048\n" \
"v_mfma_f32_16x16x32_bf16 v[28:31], a[124:127], a[76:79], v[28:31]\n" \
"buffer_load_dwordx4 v234, s[56:59], s81 offen offset:2048 sc1 lds\n" \
"v_permlane16_swap_b32 v188, v190\n" \
"v_add_f32 v188, v188, v190\n" \
"v_mfma_f32_16x16x32_bf16 v[24:27], a[112:115], a[72:75], v[24:27]\n" \
"ds_read_b64_tr_b16 a[104:105], v215 offset:16384\n" \
"ds_read_b64_tr_b16 a[106:107], v215 offset:18432\n" \
"v_mfma_f32_16x16x32_bf16 v[24:27], a[116:119], a[76:79], v[24:27]\n" \
"ds_read_b128 a[72:75], v213 offset:256\n" \
"v_rcp_f32 v188, v188\n" \
"v_mfma_f32_16x16x32_bf16 v[40:43], a[112:115], a[80:83], v[40:43]\n" \
"ds_read_b128 a[76:79], v213 offset:2304\n" \
"v_mfma_f32_16x16x32_bf16 v[40:43], a[116:119], a[84:87], v[40:43]\n" \
"buffer_load_dwordx4 v234, s[56:59], s81 offen offset:3072 sc1 lds\n" \
"v_mov_b32 v190, v188\n" \
"v_mfma_f32_16x16x32_bf16 v[44:47], a[120:123], a[80:83], v[44:47]\n" \
"s_add_u32 m0, m0, 0x4000\n" \
"ds_read_b64_tr_b16 a[108:109], v215 offset:24576\n" \
"ds_read_b64_tr_b16 a[110:111], v215 offset:26624\n" \
"v_mfma_f32_16x16x32_bf16 v[44:47], a[124:127], a[84:87], v[44:47]\n" \
"buffer_load_dwordx4 v216, s[36:39], s82 offen offset:0 lds\n" \
"v_permlane16_swap_b32 v188, v190\n" \
"v_mov_b32 v189, v188\n" \
"v_mov_b32 v191, v190\n" \
"v_mfma_f32_16x16x32_bf16 v[60:63], a[120:123], a[88:91], v[60:63]\n" \
"ds_read_b128 a[80:83], v213 offset:4096\n" \
"s_waitcnt lgkmcnt(4)\n" \
"v_mfma_f32_16x16x32_bf16 v[60:63], a[124:127], a[92:95], v[60:63]\n" \
"s_add_u32 s48, s48, 0x1000\n" \
"ds_read_b128 a[84:87], v213 offset:6144\n" \
"v_permlane32_swap_b32 v188, v189\n" \
"v_permlane32_swap_b32 v190, v191\n" \
"v_mul_f32 v172, v172, v191\n" \
"v_mul_f32 v164, v164, v190\n" \
"v_mul_f32 v156, v156, v189\n" \
"v_mul_f32 v148, v148, v188\n" \
"v_mfma_f32_16x16x32_bf16 v[56:59], a[112:115], a[88:91], v[56:59]\n" \
"buffer_load_dwordx4 v217, s[36:39], s82 offen offset:1024 lds\n" \
"v_mfma_f32_16x16x32_bf16 v[56:59], a[116:119], a[92:95], v[56:59]\n" \
"v_mul_f32 v173, v173, v191\n" \
"v_mul_f32 v165, v165, v190\n" \
"v_mul_f32 v157, v157, v189\n" \
"v_mul_f32 v149, v149, v188\n" \
"v_mul_f32 v174, v174, v191\n" \
"v_mfma_f32_16x16x32_bf16 v[0:3], a[96:99], a[64:67], v[0:3]\n" \
"ds_read_b128 a[88:91], v213 offset:4352\n" \
"v_mul_f32 v166, v166, v190\n" \
"v_mfma_f32_16x16x32_bf16 v[0:3], a[100:103], a[68:71], v[0:3]\n" \
"s_waitcnt lgkmcnt(2)\n" \
"ds_read_b128 a[92:95], v213 offset:6400\n" \
"v_mul_f32 v158, v158, v189\n" \
"v_mul_f32 v150, v150, v188\n" \
"v_mul_f32 v175, v175, v191\n" \
"v_mfma_f32_16x16x32_bf16 v[4:7], a[104:107], a[64:67], v[4:7]\n" \
"ds_read_b64_tr_b16 a[112:113], v214 offset:16896\n" \
"v_mul_f32 v167, v167, v190\n" \
"v_mfma_f32_16x16x32_bf16 v[4:7], a[108:111], a[68:71], v[4:7]\n" \
"ds_read_b64_tr_b16 a[114:115], v214 offset:18944\n" \
"v_mul_f32 v159, v159, v189\n" \
"v_mul_f32 v151, v151, v188\n" \
"v_cvt_pk_bf16_f32 v178, v172, v173\n" \
"v_cvt_pk_bf16_f32 v179, v174, v175\n" \
"v_mfma_f32_16x16x32_bf16 v[20:23], a[104:107], a[72:75], v[20:23]\n" \
"s_sub_u32 s86, s86, 32768\n" \
"s_cselect_b32 s86, 131072, s86\n" \
"ds_read_b64_tr_b16 a[116:117], v214 offset:25088\n" \
"v_mfma_f32_16x16x32_bf16 v[20:23], a[108:111], a[76:79], v[20:23]\n" \
"buffer_load_dwordx4 v218, s[36:39], s82 offen offset:2048 lds\n" \
"v_cvt_pk_bf16_f32 v170, v164, v165\n" \
"v_cvt_pk_bf16_f32 v171, v166, v167\n" \
"v_mfma_f32_16x16x32_bf16 v[16:19], a[96:99], a[72:75], v[16:19]\n" \
"s_sub_u32 s85, s85, 32768\n" \
"s_cselect_b32 s85, 131072, s85\n" \
"ds_read_b64_tr_b16 a[118:119], v214 offset:27136\n" \
"v_mfma_f32_16x16x32_bf16 v[16:19], a[100:103], a[76:79], v[16:19]\n" \
"s_waitcnt lgkmcnt(3)\n" \
"ds_read_b64_tr_b16 a[120:121], v215 offset:16896\n" \
"v_cvt_pk_bf16_f32 v162, v156, v157\n" \
"v_cvt_pk_bf16_f32 v163, v158, v159\n" \
"v_cvt_pk_bf16_f32 v154, v148, v149\n" \
"v_cvt_pk_bf16_f32 v155, v150, v151\n" \
"v_mfma_f32_16x16x32_bf16 v[32:35], a[96:99], a[80:83], v[32:35]\n" \
"ds_read_b64_tr_b16 a[122:123], v215 offset:18944\n" \
"v_and_b32 v176, v178, v184\n" \
"v_mfma_f32_16x16x32_bf16 v[32:35], a[100:103], a[84:87], v[32:35]\n" \
"buffer_load_dwordx4 v219, s[36:39], s82 offen offset:3072 lds\n" \
"v_and_b32 v177, v179, v185\n" \
"v_xor_b32 v178, v178, v176\n" \
"v_mfma_f32_16x16x32_bf16 v[36:39], a[104:107], a[80:83], v[36:39]\n" \
"s_xor_b32 s82, s48, s49\n" \
"s_cmp_lt_u32 s48, s50\n" \
"s_cselect_b32 s82, s82, s48\n" \
"s_add_u32 s81, s82, s46\n" \
"s_add_u32 m0, s86, s79\n" \
"ds_read_b64_tr_b16 a[124:125], v215 offset:25088\n" \
"v_add_u32 v213, s85, v211\n" \
"v_xor_b32 v179, v179, v177\n" \
"v_mfma_f32_16x16x32_bf16 v[36:39], a[108:111], a[84:87], v[36:39]\n" \
"buffer_load_dwordx4 v234, s[56:59], s81 offen offset:0 sc1 lds\n" \
"v_and_b32 v168, v170, v184\n" \
"v_and_b32 v169, v171, v185\n" \
"v_mfma_f32_16x16x32_bf16 v[52:55], a[104:107], a[88:91], v[52:55]\n" \
"s_lshl_b32 s82, s82, 5\n" \
"s_add_u32 s82, s82, s47\n" \
"ds_read_b64_tr_b16 a[126:127], v215 offset:27136\n" \
"v_mfma_f32_16x16x32_bf16 v[52:55], a[108:111], a[92:95], v[52:55]\n" \
"buffer_load_dwordx4 v234, s[56:59], s81 offen offset:1024 sc1 lds\n" \
"v_xor_b32 v170, v170, v168\n" \
"v_xor_b32 v171, v171, v169\n" \
"v_mfma_f32_16x16x32_bf16 v[48:51], a[96:99], a[88:91], v[48:51]\n" \
"v_add_u32 v214, s85, v212\n" \
"v_xor_b32 v215, 32, v214\n" \
"v_mfma_f32_16x16x32_bf16 v[48:51], a[100:103], a[92:95], v[48:51]\n" \
"v_and_b32 v160, v162, v184\n" \
"v_and_b32 v161, v163, v185\n" \
"v_xor_b32 v162, v162, v160\n" \
"s_waitcnt vmcnt(18) lgkmcnt(0)\n" \
"s_barrier\n" \
"v_mfma_f32_16x16x32_bf16 v[8:11], a[112:115], a[64:67], v[8:11]\n" \
"ds_read_b64_tr_b16 a[96:97], v214 offset:16384\n" \
"ds_read_b64_tr_b16 a[98:99], v214 offset:18432\n" \
"v_mfma_f32_16x16x32_bf16 v[8:11], a[116:119], a[68:71], v[8:11]\n" \
"buffer_load_dwordx4 v234, s[56:59], s81 offen offset:2048 sc1 lds\n" \
"v_xor_b32 v163, v163, v161\n" \
"v_and_b32 v152, v154, v184\n" \
"v_mfma_f32_16x16x32_bf16 v[12:15], a[120:123], a[64:67], v[12:15]\n" \
"ds_read_b64_tr_b16 a[100:101], v214 offset:24576\n" \
"ds_read_b64_tr_b16 a[102:103], v214 offset:26624\n" \
"v_mfma_f32_16x16x32_bf16 v[12:15], a[124:127], a[68:71], v[12:15]\n" \
"ds_read_b128 a[64:67], v213 offset:0\n" \
"v_and_b32 v153, v155, v185\n" \
"v_xor_b32 v154, v154, v152\n" \
"v_xor_b32 v155, v155, v153\n" \
"v_mfma_f32_16x16x32_bf16 v[28:31], a[120:123], a[72:75], v[28:31]\n" \
"ds_read_b128 a[68:71], v213 offset:2048\n" \
"v_mfma_f32_16x16x32_bf16 v[136:139], v[198:201], v[176:179], 0\n" \
"v_mfma_f32_16x16x32_bf16 v[140:143], v[78:81], v[176:179], 0\n" \
"v_mfma_f32_16x16x32_bf16 v[128:131], v[196:199], v[168:171], 0\n" \
"v_mfma_f32_16x16x32_bf16 v[132:135], v[76:79], v[168:171], 0\n" \
"v_mfma_f32_16x16x32_bf16 v[120:123], v[194:197], v[160:163], 0\n" \
"v_mfma_f32_16x16x32_bf16 v[124:127], v[74:77], v[160:163], 0\n" \
"v_mfma_f32_16x16x32_bf16 v[112:115], v[192:195], v[152:155], 0\n" \
"v_mfma_f32_16x16x32_bf16 v[116:119], v[72:75], v[152:155], 0\n" \
"v_mfma_f32_16x16x32_bf16 v[28:31], a[124:127], a[76:79], v[28:31]\n" \
"buffer_load_dwordx4 v234, s[56:59], s81 offen offset:3072 sc1 lds\n" \
"v_mfma_f32_16x16x32_bf16 v[24:27], a[112:115], a[72:75], v[24:27]\n" \
"ds_read_b64_tr_b16 a[104:105], v215 offset:16384\n" \
"ds_read_b64_tr_b16 a[106:107], v215 offset:18432\n" \
"v_mfma_f32_16x16x32_bf16 v[24:27], a[116:119], a[76:79], v[24:27]\n" \
"ds_read_b128 a[72:75], v213 offset:256\n" \
"v_cvt_pk_bf16_f32 v136, v136, v137\n" \
"v_cvt_pk_bf16_f32 v137, v138, v139\n" \
"v_cvt_pk_bf16_f32 v138, v140, v141\n" \
"v_cvt_pk_bf16_f32 v139, v142, v143\n" \
"v_cndmask_b32 v140, v136, v138, s[76:77]\n" \
"v_mfma_f32_16x16x32_bf16 v[40:43], a[112:115], a[80:83], v[40:43]\n" \
"s_add_u32 m0, m0, 0x4000\n" \
"ds_read_b128 a[76:79], v213 offset:2304\n" \
"v_cndmask_b32 v141, v137, v139, s[76:77]\n" \
"v_mfma_f32_16x16x32_bf16 v[40:43], a[116:119], a[84:87], v[40:43]\n" \
"buffer_load_dwordx4 v216, s[36:39], s82 offen offset:0 lds\n" \
"v_cndmask_b32 v142, v138, v136, s[76:77]\n" \
"v_cndmask_b32 v143, v139, v137, s[76:77]\n" \
"v_mfma_f32_16x16x32_bf16 v[44:47], a[120:123], a[80:83], v[44:47]\n" \
"s_add_u32 s48, s48, 0x1000\n" \
"ds_read_b64_tr_b16 a[108:109], v215 offset:24576\n" \
"ds_read_b64_tr_b16 a[110:111], v215 offset:26624\n" \
"v_mfma_f32_16x16x32_bf16 v[44:47], a[124:127], a[84:87], v[44:47]\n" \
"buffer_load_dwordx4 v217, s[36:39], s82 offen offset:1024 lds\n" \
"v_cvt_pk_bf16_f32 v128, v128, v129\n" \
"v_cvt_pk_bf16_f32 v129, v130, v131\n" \
"v_cvt_pk_bf16_f32 v130, v132, v133\n" \
"v_mfma_f32_16x16x32_bf16 v[60:63], a[120:123], a[88:91], v[60:63]\n" \
"ds_read_b128 a[80:83], v213 offset:4096\n" \
"s_waitcnt lgkmcnt(4)\n" \
"v_mfma_f32_16x16x32_bf16 v[60:63], a[124:127], a[92:95], v[60:63]\n" \
"s_sub_u32 s86, s86, 32768\n" \
"s_cselect_b32 s86, 131072, s86\n" \
"ds_read_b128 a[84:87], v213 offset:6144\n" \
"v_cvt_pk_bf16_f32 v131, v134, v135\n" \
"v_cndmask_b32 v132, v128, v130, s[76:77]\n" \
"v_mfma_f32_16x16x32_bf16 v[56:59], a[112:115], a[88:91], v[56:59]\n" \
"buffer_load_dwordx4 v218, s[36:39], s82 offen offset:2048 lds\n" \
"v_mfma_f32_16x16x32_bf16 v[56:59], a[116:119], a[92:95], v[56:59]\n" \
"v_cndmask_b32 v133, v129, v131, s[76:77]\n" \
"v_cndmask_b32 v134, v130, v128, s[76:77]\n" \
"v_cndmask_b32 v135, v131, v129, s[76:77]\n" \
"v_cvt_pk_bf16_f32 v120, v120, v121\n" \
"v_cvt_pk_bf16_f32 v121, v122, v123\n" \
"v_mfma_f32_16x16x32_bf16 v[0:3], a[96:99], a[64:67], v[0:3]\n" \
"ds_read_b128 a[88:91], v213 offset:4352\n" \
"v_cvt_pk_bf16_f32 v122, v124, v125\n" \
"v_mfma_f32_16x16x32_bf16 v[0:3], a[100:103], a[68:71], v[0:3]\n" \
"s_waitcnt lgkmcnt(2)\n" \
"ds_read_b128 a[92:95], v213 offset:6400\n" \
"v_cvt_pk_bf16_f32 v123, v126, v127\n" \
"v_cndmask_b32 v124, v120, v122, s[76:77]\n" \
"v_cndmask_b32 v125, v121, v123, s[76:77]\n" \
"v_mfma_f32_16x16x32_bf16 v[4:7], a[104:107], a[64:67], v[4:7]\n" \
"ds_read_b64_tr_b16 a[112:113], v214 offset:16896\n" \
"v_cndmask_b32 v126, v122, v120, s[76:77]\n" \
"v_mfma_f32_16x16x32_bf16 v[4:7], a[108:111], a[68:71], v[4:7]\n" \
"Lpg10225_%=:\n" \
"s_cbranch_execz Lcb1_%=\n" \
"ds_read_b64_tr_b16 a[114:115], v214 offset:18944\n" \
"v_cndmask_b32 v127, v123, v121, s[76:77]\n" \
"v_cvt_pk_bf16_f32 v112, v112, v113\n" \
"v_cvt_pk_bf16_f32 v113, v114, v115\n" \
"v_cvt_pk_bf16_f32 v114, v116, v117\n" \
"v_mfma_f32_16x16x32_bf16 v[20:23], a[104:107], a[72:75], v[20:23]\n" \
"s_waitcnt lgkmcnt(4)\n" \
"ds_read_b64_tr_b16 a[116:117], v214 offset:25088\n" \
"v_cvt_pk_bf16_f32 v115, v118, v119\n" \
"v_mfma_f32_16x16x32_bf16 v[20:23], a[108:111], a[76:79], v[20:23]\n" \
"buffer_load_dwordx4 v219, s[36:39], s82 offen offset:3072 lds\n" \
"v_cndmask_b32 v116, v112, v114, s[76:77]\n" \
"v_cndmask_b32 v117, v113, v115, s[76:77]\n" \
"v_mfma_f32_16x16x32_bf16 v[16:19], a[96:99], a[72:75], v[16:19]\n" \
"s_sub_u32 s85, s85, 32768\n" \
"s_cselect_b32 s85, 131072, s85\n" \
"s_xor_b32 s82, s48, s49\n" \
"s_cmp_lt_u32 s48, s50\n" \
"s_cselect_b32 s82, s82, s48\n" \
"ds_read_b64_tr_b16 a[118:119], v214 offset:27136\n" \
"v_cndmask_b32 v118, v114, v112, s[76:77]\n" \
"v_mfma_f32_16x16x32_bf16 v[16:19], a[100:103], a[76:79], v[16:19]\n" \
"s_add_u32 s81, s82, s46\n" \
"ds_read_b64_tr_b16 a[120:121], v215 offset:16896\n" \
"v_cndmask_b32 v119, v115, v113, s[76:77]\n" \
"s_add_u32 s71, s93, 208896\n" \
"v_mfma_f32_16x16x32_bf16 v[32:35], a[96:99], a[80:83], v[32:35]\n" \
"s_add_u32 m0, s86, s79\n" \
"ds_read_b64_tr_b16 a[122:123], v215 offset:18944\n" \
"buffer_store_dwordx4 v[140:143], v236, s[56:59], s71 offen sc1\n" \
"s_add_u32 s71, s93, 139264\n" \
"v_mfma_f32_16x16x32_bf16 v[32:35], a[100:103], a[84:87], v[32:35]\n" \
"buffer_load_dwordx4 v234, s[56:59], s81 offen offset:0 sc1 lds\n" \
"v_mfma_f32_16x16x32_bf16 v[36:39], a[104:107], a[80:83], v[36:39]\n" \
"s_lshl_b32 s82, s82, 5\n" \
"s_add_u32 s82, s82, s47\n" \
"ds_read_b64_tr_b16 a[124:125], v215 offset:25088\n" \
"buffer_store_dwordx4 v[132:135], v236, s[56:59], s71 offen sc1\n" \
"s_add_u32 s71, s93, 69632\n" \
"v_add_u32_e64 v213, s85, v211\n" \
"v_mfma_f32_16x16x32_bf16 v[36:39], a[108:111], a[84:87], v[36:39]\n" \
"s_waitcnt lgkmcnt(3)\n" \
"buffer_load_dwordx4 v234, s[56:59], s81 offen offset:1024 sc1 lds\n" \
"v_mfma_f32_16x16x32_bf16 v[52:55], a[104:107], a[88:91], v[52:55]\n" \
"ds_read_b64_tr_b16 a[126:127], v215 offset:27136\n" \
"buffer_store_dwordx4 v[124:127], v236, s[56:59], s71 offen sc1\n" \
"s_add_u32 s71, s93, 0\n" \
"v_mfma_f32_16x16x32_bf16 v[52:55], a[108:111], a[92:95], v[52:55]\n" \
"buffer_load_dwordx4 v234, s[56:59], s81 offen offset:2048 sc1 lds\n" \
"v_mfma_f32_16x16x32_bf16 v[48:51], a[96:99], a[88:91], v[48:51]\n" \
"v_add_u32_e64 v214, s85, v212\n" \
"v_xor_b32_e64 v215, 32, v214\n" \
"v_mfma_f32_16x16x32_bf16 v[48:51], a[100:103], a[92:95], v[48:51]\n" \
"buffer_store_dwordx4 v[116:119], v236, s[56:59], s71 offen sc1\n" \
"s_waitcnt vmcnt(23) lgkmcnt(0)\n" \
"s_barrier\n" \
"v_mfma_f32_16x16x32_bf16 v[8:11], a[112:115], a[64:67], v[8:11]\n" \
"ds_read_b64_tr_b16 a[96:97], v214 offset:16384\n" \
"ds_read_b64_tr_b16 a[98:99], v214 offset:18432\n" \
"v_mfma_f32_16x16x32_bf16 v[8:11], a[116:119], a[68:71], v[8:11]\n" \
"buffer_load_dwordx4 v234, s[56:59], s81 offen offset:3072 sc1 lds\n" \
"v_mfma_f32_16x16x32_bf16 v[12:15], a[120:123], a[64:67], v[12:15]\n" \
"ds_read_b64_tr_b16 a[100:101], v214 offset:24576\n" \
"ds_read_b64_tr_b16 a[102:103], v214 offset:26624\n" \
"v_mfma_f32_16x16x32_bf16 v[12:15], a[124:127], a[68:71], v[12:15]\n" \
"ds_read_b128 a[64:67], v213 offset:0\n" \
"v_mfma_f32_16x16x32_bf16 v[28:31], a[120:123], a[72:75], v[28:31]\n" \
"s_add_u32 m0, m0, 0x4000\n" \
"ds_read_b128 a[68:71], v213 offset:2048\n" \
"v_mfma_f32_16x16x32_bf16 v[28:31], a[124:127], a[76:79], v[28:31]\n" \
"buffer_load_dwordx4 v216, s[36:39], s82 offen offset:0 lds\n" \
"v_mfma_f32_16x16x32_bf16 v[24:27], a[112:115], a[72:75], v[24:27]\n" \
"ds_read_b64_tr_b16 a[104:105], v215 offset:16384\n" \
"ds_read_b64_tr_b16 a[106:107], v215 offset:18432\n" \
"v_mfma_f32_16x16x32_bf16 v[24:27], a[116:119], a[76:79], v[24:27]\n" \
"ds_read_b128 a[72:75], v213 offset:256\n" \
"v_mfma_f32_16x16x32_bf16 v[40:43], a[112:115], a[80:83], v[40:43]\n" \
"s_add_u32 s48, s48, 0x1000\n" \
"ds_read_b128 a[76:79], v213 offset:2304\n" \
"v_mfma_f32_16x16x32_bf16 v[40:43], a[116:119], a[84:87], v[40:43]\n" \
"buffer_load_dwordx4 v217, s[36:39], s82 offen offset:1024 lds\n" \
"v_mfma_f32_16x16x32_bf16 v[44:47], a[120:123], a[80:83], v[44:47]\n" \
"s_sub_u32 s86, s86, 32768\n" \
"s_cselect_b32 s86, 131072, s86\n" \
"ds_read_b64_tr_b16 a[108:109], v215 offset:24576\n" \
"ds_read_b64_tr_b16 a[110:111], v215 offset:26624\n" \
"v_mfma_f32_16x16x32_bf16 v[44:47], a[124:127], a[84:87], v[44:47]\n" \
"buffer_load_dwordx4 v218, s[36:39], s82 offen offset:2048 lds\n" \
"v_mfma_f32_16x16x32_bf16 v[60:63], a[120:123], a[88:91], v[60:63]\n" \
"ds_read_b128 a[80:83], v213 offset:4096\n" \
"s_waitcnt lgkmcnt(4)\n" \
"v_mfma_f32_16x16x32_bf16 v[60:63], a[124:127], a[92:95], v[60:63]\n" \
"ds_read_b128 a[84:87], v213 offset:6144\n" \
"v_mfma_f32_16x16x32_bf16 v[56:59], a[112:115], a[88:91], v[56:59]\n" \
"buffer_load_dwordx4 v219, s[36:39], s82 offen offset:3072 lds\n" \
"v_mfma_f32_16x16x32_bf16 v[56:59], a[116:119], a[92:95], v[56:59]\n" \
"s_mov_b32 s72, 2\n" \
"s_nop 0\n" \
"Lo0_%=:\n" \
"v_mfma_f32_16x16x32_bf16 v[0:3], a[96:99], a[64:67], v[0:3]\n" \
"ds_read_b128 a[88:91], v213 offset:4352\n" \
"v_mfma_f32_16x16x32_bf16 v[0:3], a[100:103], a[68:71], v[0:3]\n" \
"s_waitcnt lgkmcnt(2)\n" \
"s_xor_b32 s82, s48, s49\n" \
"ds_read_b128 a[92:95], v213 offset:6400\n" \
"v_mfma_f32_16x16x32_bf16 v[4:7], a[104:107], a[64:67], v[4:7]\n" \
"s_cmp_lt_u32 s48, s50\n" \
"s_cselect_b32 s82, s82, s48\n" \
"ds_read_b64_tr_b16 a[112:113], v214 offset:16896\n" \
"v_mfma_f32_16x16x32_bf16 v[4:7], a[108:111], a[68:71], v[4:7]\n" \
"s_add_u32 s81, s82, s46\n" \
"ds_read_b64_tr_b16 a[114:115], v214 offset:18944\n" \
"v_mfma_f32_16x16x32_bf16 v[20:23], a[104:107], a[72:75], v[20:23]\n" \
"s_add_u32 m0, s86, s79\n" \
"ds_read_b64_tr_b16 a[116:117], v214 offset:25088\n" \
"v_mfma_f32_16x16x32_bf16 v[20:23], a[108:111], a[76:79], v[20:23]\n" \
"buffer_load_dwordx4 v234, s[56:59], s81 offen offset:0 sc1 lds\n" \
"v_mfma_f32_16x16x32_bf16 v[16:19], a[96:99], a[72:75], v[16:19]\n" \
"s_sub_u32 s85, s85, 32768\n" \
"s_cselect_b32 s85, 131072, s85\n" \
"ds_read_b64_tr_b16 a[118:119], v214 offset:27136\n" \
"v_mfma_f32_16x16x32_bf16 v[16:19], a[100:103], a[76:79], v[16:19]\n" \
"s_waitcnt lgkmcnt(3)\n" \
"s_lshl_b32 s82, s82, 5\n" \
"ds_read_b64_tr_b16 a[120:121], v215 offset:16896\n" \
"v_mfma_f32_16x16x32_bf16 v[32:35], a[96:99], a[80:83], v[32:35]\n" \
"s_add_u32 s82, s82, s47\n" \
"ds_read_b64_tr_b16 a[122:123], v215 offset:18944\n" \
"v_mfma_f32_16x16x32_bf16 v[32:35], a[100:103], a[84:87], v[32:35]\n" \
"buffer_load_dwordx4 v234, s[56:59], s81 offen offset:1024 sc1 lds\n" \
"v_mfma_f32_16x16x32_bf16 v[36:39], a[104:107], a[80:83], v[36:39]\n" \
"ds_read_b64_tr_b16 a[124:125], v215 offset:25088\n" \
"v_add_u32 v213, s85, v211\n" \
"v_mfma_f32_16x16x32_bf16 v[36:39], a[108:111], a[84:87], v[36:39]\n" \
"buffer_load_dwordx4 v234, s[56:59], s81 offen offset:2048 sc1 lds\n" \
"v_mfma_f32_16x16x32_bf16 v[52:55], a[104:107], a[88:91], v[52:55]\n" \
"ds_read_b64_tr_b16 a[126:127], v215 offset:27136\n" \
"v_mfma_f32_16x16x32_bf16 v[52:55], a[108:111], a[92:95], v[52:55]\n" \
"buffer_load_dwordx4 v234, s[56:59], s81 offen offset:3072 sc1 lds\n" \
"v_mfma_f32_16x16x32_bf16 v[48:51], a[96:99], a[88:91], v[48:51]\n" \
"v_add_u32 v214, s85, v212\n" \
"v_xor_b32 v215, 32, v214\n" \
"v_mfma_f32_16x16x32_bf16 v[48:51], a[100:103], a[92:95], v[48:51]\n" \
"s_waitcnt vmcnt(20) lgkmcnt(0)\n" \
"s_barrier\n" \
"v_mfma_f32_16x16x32_bf16 v[8:11], a[112:115], a[64:67], v[8:11]\n" \
"s_add_u32 m0, m0, 0x4000\n" \
"ds_read_b64_tr_b16 a[96:97], v214 offset:16384\n" \
"ds_read_b64_tr_b16 a[98:99], v214 offset:18432\n" \
"v_mfma_f32_16x16x32_bf16 v[8:11], a[116:119], a[68:71], v[8:11]\n" \
"buffer_load_dwordx4 v216, s[36:39], s82 offen offset:0 lds\n" \
"v_mfma_f32_16x16x32_bf16 v[12:15], a[120:123], a[64:67], v[12:15]\n" \
"ds_read_b64_tr_b16 a[100:101], v214 offset:24576\n" \
"ds_read_b64_tr_b16 a[102:103], v214 offset:26624\n" \
"v_mfma_f32_16x16x32_bf16 v[12:15], a[124:127], a[68:71], v[12:15]\n" \
"ds_read_b128 a[64:67], v213 offset:0\n" \
"v_mfma_f32_16x16x32_bf16 v[28:31], a[120:123], a[72:75], v[28:31]\n" \
"s_add_u32 s48, s48, 0x1000\n" \
"ds_read_b128 a[68:71], v213 offset:2048\n" \
"v_mfma_f32_16x16x32_bf16 v[28:31], a[124:127], a[76:79], v[28:31]\n" \
"buffer_load_dwordx4 v217, s[36:39], s82 offen offset:1024 lds\n" \
"v_mfma_f32_16x16x32_bf16 v[24:27], a[112:115], a[72:75], v[24:27]\n" \
"ds_read_b64_tr_b16 a[104:105], v215 offset:16384\n" \
"ds_read_b64_tr_b16 a[106:107], v215 offset:18432\n" \
"v_mfma_f32_16x16x32_bf16 v[24:27], a[116:119], a[76:79], v[24:27]\n" \
"ds_read_b128 a[72:75], v213 offset:256\n" \
"v_mfma_f32_16x16x32_bf16 v[40:43], a[112:115], a[80:83], v[40:43]\n" \
"s_sub_u32 s86, s86, 32768\n" \
"s_cselect_b32 s86, 131072, s86\n" \
"ds_read_b128 a[76:79], v213 offset:2304\n" \
"v_mfma_f32_16x16x32_bf16 v[40:43], a[116:119], a[84:87], v[40:43]\n" \
"buffer_load_dwordx4 v218, s[36:39], s82 offen offset:2048 lds\n" \
"v_mfma_f32_16x16x32_bf16 v[44:47], a[120:123], a[80:83], v[44:47]\n" \
"ds_read_b64_tr_b16 a[108:109], v215 offset:24576\n" \
"ds_read_b64_tr_b16 a[110:111], v215 offset:26624\n" \
"v_mfma_f32_16x16x32_bf16 v[44:47], a[124:127], a[84:87], v[44:47]\n" \
"buffer_load_dwordx4 v219, s[36:39], s82 offen offset:3072 lds\n" \
"v_mfma_f32_16x16x32_bf16 v[60:63], a[120:123], a[88:91], v[60:63]\n" \
"ds_read_b128 a[80:83], v213 offset:4096\n" \
"s_waitcnt lgkmcnt(4)\n" \
"v_mfma_f32_16x16x32_bf16 v[60:63], a[124:127], a[92:95], v[60:63]\n" \
"s_sub_u32 s72, s72, 1\n" \
"ds_read_b128 a[84:87], v213 offset:6144\n" \
"v_mfma_f32_16x16x32_bf16 v[56:59], a[112:115], a[88:91], v[56:59]\n" \
"v_mfma_f32_16x16x32_bf16 v[56:59], a[116:119], a[92:95], v[56:59]\n" \
"s_cbranch_scc0 Lo0_%=\n" \
"s_setpc_b64 s[44:45]\n" \
"Lor_%=:\n" \
"buffer_store_dword v230, v231, s[56:59], s12 offen sc1\n" \
"s_mov_b32 s72, s10\n" \
"s_call_b64 s[44:45], Lo0_%=\n" \
"buffer_load_dword v220, v221, s[56:59], s67 offen sc1\n" \
"s_mov_b32 s72, 1\n" \
"s_call_b64 s[44:45], Lo0_%=\n" \
"s_waitcnt vmcnt(16)\n" \
"v_cmp_eq_u32 vcc, s60, v220\n" \
"s_nop 1\n" \
"s_andn2_b32 s70, s74, vcc_lo\n" \
"s_cbranch_scc1 Lpl6_%=\n" \
"Lpr6_%=:\n" \
"s_cmp_eq_u32 s90, 0\n" \
"s_cselect_b32 s46, s46, s83\n" \
"s_cselect_b32 s47, s47, s84\n" \
"s_cselect_b32 s48, s48, 0\n" \
"s_cselect_b64 s[36:37], s[36:37], s[40:41]\n" \
"s_cselect_b32 s38, s38, s42\n" \
"s_mov_b32 s72, 3\n" \
"s_call_b64 s[44:45], Lo0_%=\n" \
"s_cmp_eq_u32 s90, 0\n" \
"s_cbranch_scc0 Lrole1_%=\n" \
"s_mov_b32 s72, s11\n" \
"s_call_b64 s[44:45], Lo0_%=\n" \
"buffer_load_dword v220, v241, s[56:59], s64 offen sc1\n" \
"s_mov_b32 s72, 1\n" \
"s_call_b64 s[44:45], Lo0_%=\n" \
"v_mfma_f32_16x16x32_bf16 v[0:3], a[96:99], a[64:67], v[0:3]\n" \
"s_waitcnt lgkmcnt(2)\n" \
"ds_read_b128 a[88:91], v213 offset:4352\n" \
"v_mov_b32 v74, 0x3f80\n" \
"v_mfma_f32_16x16x32_bf16 v[0:3], a[100:103], a[68:71], v[0:3]\n" \
"s_add_u32 s48, s86, 32768\n" \
"s_cmp_eq_u32 s48, 163840\n" \
"s_cselect_b32 s48, 0, s48\n" \
"ds_read_b128 a[92:95], v213 offset:6400\n" \
"v_mfma_f32_16x16x32_bf16 v[4:7], a[104:107], a[64:67], v[4:7]\n" \
"ds_read_b64_tr_b16 a[112:113], v214 offset:16896\n" \
"v_mov_b32 v64, 0\n" \
"v_mfma_f32_16x16x32_bf16 v[4:7], a[108:111], a[68:71], v[4:7]\n" \
"s_add_u32 s49, s48, 32768\n" \
"s_cmp_eq_u32 s49, 163840\n" \
"s_cselect_b32 s49, 0, s49\n" \
"ds_read_b64_tr_b16 a[114:115], v214 offset:18944\n" \
"v_mfma_f32_16x16x32_bf16 v[20:23], a[104:107], a[72:75], v[20:23]\n" \
"ds_read_b64_tr_b16 a[116:117], v214 offset:25088\n" \
"v_add_u32 v224, s49, v211\n" \
"v_mfma_f32_16x16x32_bf16 v[20:23], a[108:111], a[76:79], v[20:23]\n" \
"v_add_u32 v225, s49, v212\n" \
"v_xor_b32 v226, 32, v225\n" \
"v_mfma_f32_16x16x32_bf16 v[16:19], a[96:99], a[72:75], v[16:19]\n" \
"s_sub_u32 s85, s85, 32768\n" \
"s_cselect_b32 s85, 131072, s85\n" \
"ds_read_b64_tr_b16 a[118:119], v214 offset:27136\n" \
"v_mfma_f32_16x16x32_bf16 v[16:19], a[100:103], a[76:79], v[16:19]\n" \
"s_waitcnt lgkmcnt(3)\n" \
"ds_read_b64_tr_b16 a[120:121], v215 offset:16896\n" \
"v_add_u32 v227, s48, v211\n" \
"v_mfma_f32_16x16x32_bf16 v[32:35], a[96:99], a[80:83], v[32:35]\n" \
"ds_read_b64_tr_b16 a[122:123], v215 offset:18944\n" \
"v_add_u32 v228, s48, v212\n" \
"v_mfma_f32_16x16x32_bf16 v[32:35], a[100:103], a[84:87], v[32:35]\n" \
"v_xor_b32 v229, 32, v228\n" \
"v_mov_b32 v65, 0\n" \
"v_mfma_f32_16x16x32_bf16 v[36:39], a[104:107], a[80:83], v[36:39]\n" \
"ds_read_b64_tr_b16 a[124:125], v215 offset:25088\n" \
"v_add_u32 v213, s85, v211\n" \
"v_mfma_f32_16x16x32_bf16 v[36:39], a[108:111], a[84:87], v[36:39]\n" \
"v_mov_b32 v68, 0\n" \
"v_mov_b32 v69, 0\n" \
"v_mfma_f32_16x16x32_bf16 v[52:55], a[104:107], a[88:91], v[52:55]\n" \
"ds_read_b64_tr_b16 a[126:127], v215 offset:27136\n" \
"v_lshrrev_b32 v72, 8, v234\n" \
"v_mfma_f32_16x16x32_bf16 v[52:55], a[108:111], a[92:95], v[52:55]\n" \
"v_bfe_u32 v73, v234, 6, 2\n" \
"v_cmp_eq_u32 s[16:17], v72, v73\n" \
"v_mfma_f32_16x16x32_bf16 v[48:51], a[96:99], a[88:91], v[48:51]\n" \
"v_add_u32 v214, s85, v212\n" \
"v_xor_b32 v215, 32, v214\n" \
"v_mfma_f32_16x16x32_bf16 v[48:51], a[100:103], a[92:95], v[48:51]\n" \
"s_waitcnt vmcnt(8) lgkmcnt(0)\n" \
"s_barrier\n" \
"s_waitcnt vmcnt(16)\n" \
"v_cmp_eq_u32 vcc, s60, v220\n" \
"s_nop 1\n" \
"s_andn2_b32 s70, s69, vcc_lo\n" \
"s_cbranch_scc1 Lpl7_%=\n" \
"Lpr7_%=:\n" \
"v_mfma_f32_16x16x32_bf16 v[8:11], a[112:115], a[64:67], v[8:11]\n" \
"s_add_u32 s71, s87, 0\n" \
"ds_read_b128 a[0:3], v213 offset:0\n" \
"ds_read_b128 a[4:7], v213 offset:2048\n" \
"v_mfma_f32_16x16x32_bf16 v[8:11], a[116:119], a[68:71], v[8:11]\n" \
"buffer_load_dwordx4 v[176:179], v234, s[56:59], s71 offen offset:0 sc1\n" \
"v_mfma_f32_16x16x32_bf16 v[12:15], a[120:123], a[64:67], v[12:15]\n" \
"ds_read_b64_tr_b16 a[128:129], v214 offset:16384\n" \
"ds_read_b64_tr_b16 a[130:131], v214 offset:18432\n" \
"v_mfma_f32_16x16x32_bf16 v[12:15], a[124:127], a[68:71], v[12:15]\n" \
"buffer_load_dwordx4 v[180:183], v234, s[56:59], s71 offen offset:1024 sc1\n" \
"v_mfma_f32_16x16x32_bf16 v[28:31], a[120:123], a[72:75], v[28:31]\n" \
"ds_read_b64_tr_b16 a[132:133], v214 offset:24576\n" \
"ds_read_b64_tr_b16 a[134:135], v214 offset:26624\n" \
"v_mfma_f32_16x16x32_bf16 v[28:31], a[124:127], a[76:79], v[28:31]\n" \
"buffer_load_dwordx4 v[184:187], v234, s[56:59], s71 offen offset:2048 sc1\n" \
"v_mfma_f32_16x16x32_bf16 v[24:27], a[112:115], a[72:75], v[24:27]\n" \
"ds_read_b64_tr_b16 a[136:137], v215 offset:16384\n" \
"ds_read_b64_tr_b16 a[138:139], v215 offset:18432\n" \
"v_mfma_f32_16x16x32_bf16 v[24:27], a[116:119], a[76:79], v[24:27]\n" \
"buffer_load_dwordx4 v[188:191], v234, s[56:59], s71 offen offset:3072 sc1\n" \
"v_mfma_f32_16x16x32_bf16 v[40:43], a[112:115], a[80:83], v[40:43]\n" \
"ds_read_b64_tr_b16 a[140:141], v215 offset:24576\n" \
"ds_read_b64_tr_b16 a[142:143], v215 offset:26624\n" \
"v_mfma_f32_16x16x32_bf16 v[40:43], a[116:119], a[84:87], v[40:43]\n" \
"ds_read_b64_tr_b16 a[144:145], v214 offset:16896\n" \
"ds_read_b64_tr_b16 a[146:147], v214 offset:18944\n" \
"v_mfma_f32_16x16x32_bf16 v[44:47], a[120:123], a[80:83], v[44:47]\n" \
"ds_read_b64_tr_b16 a[148:149], v214 offset:25088\n" \
"ds_read_b64_tr_b16 a[150:151], v214 offset:27136\n" \
"v_mfma_f32_16x16x32_bf16 v[44:47], a[124:127], a[84:87], v[44:47]\n" \
"ds_read_b64_tr_b16 a[152:153], v215 offset:16896\n" \
"ds_read_b64_tr_b16 a[154:155], v215 offset:18944\n" \
"v_mfma_f32_16x16x32_bf16 v[60:63], a[120:123], a[88:91], v[60:63]\n" \
"ds_read_b64_tr_b16 a[156:157], v215 offset:25088\n" \
"ds_read_b64_tr_b16 a[158:159], v215 offset:27136\n" \
"v_mfma_f32_16x16x32_bf16 v[60:63], a[124:127], a[92:95], v[60:63]\n" \
"ds_read_b128 a[24:27], v213 offset:256\n" \
"ds_read_b128 a[28:31], v213 offset:2304\n" \
"v_mfma_f32_16x16x32_bf16 v[56:59], a[112:115], a[88:91], v[56:59]\n" \
"ds_read_b128 a[8:11], v224 offset:0\n" \
"ds_read_b128 a[12:15], v224 offset:2048\n" \
"v_mfma_f32_16x16x32_bf16 v[56:59], a[116:119], a[92:95], v[56:59]\n" \
"s_waitcnt lgkmcnt(8)\n" \
"ds_read_b64_tr_b16 a[160:161], v225 offset:16384\n" \
"ds_read_b64_tr_b16 a[162:163], v225 offset:18432\n" \
"v_mfma_f32_16x16x32_bf16 v[0:3], a[128:131], a[0:3], v[0:3]\n" \
"ds_read_b64_tr_b16 a[164:165], v225 offset:24576\n" \
"ds_read_b64_tr_b16 a[166:167], v225 offset:26624\n" \
"v_mfma_f32_16x16x32_bf16 v[0:3], a[132:135], a[4:7], v[0:3]\n" \
"ds_read_b64_tr_b16 a[168:169], v226 offset:16384\n" \
"ds_read_b64_tr_b16 a[170:171], v226 offset:18432\n" \
"v_mfma_f32_16x16x32_bf16 v[4:7], a[136:139], a[0:3], v[4:7]\n" \
"s_mov_b32 s70, 0x20000000\n" \
"ds_read_b64_tr_b16 a[172:173], v226 offset:24576\n" \
"ds_read_b64_tr_b16 a[174:175], v226 offset:26624\n" \
"v_mfma_f32_16x16x32_bf16 v[4:7], a[140:143], a[4:7], v[4:7]\n" \
"s_cmp_eq_u32 s64, 65616896\n" \
"s_cselect_b32 s70, 65593344, s70\n" \
"ds_read_b64_tr_b16 a[176:177], v225 offset:16896\n" \
"ds_read_b64_tr_b16 a[178:179], v225 offset:18944\n" \
"buffer_store_dword v230, v231, s[56:59], s70 offen sc1\n" \
"v_mfma_f32_16x16x32_bf16 v[8:11], a[144:147], a[0:3], v[8:11]\n" \
"s_waitcnt lgkmcnt(8)\n" \
"ds_read_b64_tr_b16 a[180:181], v225 offset:25088\n" \
"ds_read_b64_tr_b16 a[182:183], v225 offset:27136\n" \
"v_mfma_f32_16x16x32_bf16 v[8:11], a[148:151], a[4:7], v[8:11]\n" \
"s_sub_u32 s68, s86, 32768\n" \
"s_cselect_b32 s68, 131072, s68\n" \
"ds_read_b64_tr_b16 a[184:185], v226 offset:16896\n" \
"ds_read_b64_tr_b16 a[186:187], v226 offset:18944\n" \
"v_mfma_f32_16x16x32_bf16 v[12:15], a[152:155], a[0:3], v[12:15]\n" \
"ds_read_b64_tr_b16 a[188:189], v226 offset:25088\n" \
"ds_read_b64_tr_b16 a[190:191], v226 offset:27136\n" \
"v_mfma_f32_16x16x32_bf16 v[12:15], a[156:159], a[4:7], v[12:15]\n" \
"s_cmp_lt_u32 s61, 2\n" \
"s_cselect_b32 s68, s86, s68\n" \
"v_and_b32 v72, 16, v234\n" \
"v_lshlrev_b32 v72, v72, v74\n" \
"v_mfma_f32_16x16x32_bf16 v[28:31], a[152:155], a[24:27], v[28:31]\n" \
"s_and_b32 s50, s61, 1\n" \
"v_cndmask_b32 v72, 0, v72, s[16:17]\n" \
"v_bfe_i32 v73, v234, 5, 1\n" \
"v_mfma_f32_16x16x32_bf16 v[28:31], a[156:159], a[28:31], v[28:31]\n" \
"s_mul_i32 s50, s50, 9216\n" \
"v_and_b32 v67, v72, v73\n" \
"v_xor_b32 v66, v72, v67\n" \
"v_mfma_f32_16x16x32_bf16 v[24:27], a[144:147], a[24:27], v[24:27]\n" \
"s_add_u32 s68, s68, s50\n" \
"s_add_u32 s68, s68, %3\n" \
"v_lshrrev_b32 v132, 4, v234\n" \
"v_mfma_f32_16x16x32_bf16 v[24:27], a[148:151], a[28:31], v[24:27]\n" \
"v_and_b32 v133, 15, v132\n" \
"v_mul_u32_u24 v128, 144, v133\n" \
"v_lshrrev_b32 v133, 4, v132\n" \
"v_mfma_f32_16x16x32_bf16 v[20:23], a[136:139], a[24:27], v[20:23]\n" \
"s_waitcnt vmcnt(5)\n" \
"s_barrier\n" \
"v_mfma_f32_16x16x32_bf16 v[20:23], a[140:143], a[28:31], v[20:23]\n" \
"s_add_u32 s71, s87, 4096\n" \
"ds_read_b128 a[32:35], v224 offset:256\n" \
"ds_read_b128 a[36:39], v224 offset:2304\n" \
"v_mfma_f32_16x16x32_bf16 v[16:19], a[128:131], a[24:27], v[16:19]\n" \
"buffer_load_dwordx4 v[192:195], v234, s[56:59], s71 offen offset:0 sc1\n" \
"v_mfma_f32_16x16x32_bf16 v[16:19], a[132:135], a[28:31], v[16:19]\n" \
"s_waitcnt lgkmcnt(2)\n" \
"ds_read_b128 a[16:19], v227 offset:0\n" \
"ds_read_b128 a[20:23], v227 offset:2048\n" \
"v_mfma_f32_16x16x32_bf16 v[0:3], a[160:163], a[8:11], v[0:3]\n" \
"ds_read_b64_tr_b16 a[192:193], v228 offset:16384\n" \
"ds_read_b64_tr_b16 a[194:195], v228 offset:18432\n" \
"v_mfma_f32_16x16x32_bf16 v[0:3], a[164:167], a[12:15], v[0:3]\n" \
"buffer_load_dwordx4 v[196:199], v234, s[56:59], s71 offen offset:1024 sc1\n" \
"v_mfma_f32_16x16x32_bf16 v[4:7], a[168:171], a[8:11], v[4:7]\n" \
"ds_read_b64_tr_b16 a[196:197], v228 offset:24576\n" \
"ds_read_b64_tr_b16 a[198:199], v228 offset:26624\n" \
"v_mfma_f32_16x16x32_bf16 v[4:7], a[172:175], a[12:15], v[4:7]\n" \
"ds_read_b64_tr_b16 a[200:201], v229 offset:16384\n" \
"ds_read_b64_tr_b16 a[202:203], v229 offset:18432\n" \
"v_mfma_f32_16x16x32_bf16 v[8:11], a[176:179], a[8:11], v[8:11]\n" \
"buffer_load_dwordx4 v[200:203], v234, s[56:59], s71 offen offset:2048 sc1\n" \
"v_mfma_f32_16x16x32_bf16 v[8:11], a[180:183], a[12:15], v[8:11]\n" \
"ds_read_b64_tr_b16 a[204:205], v229 offset:24576\n" \
"ds_read_b64_tr_b16 a[206:207], v229 offset:26624\n" \
"v_lshl_add_u32 v128, v133, 3, v128\n" \
"v_mfma_f32_16x16x32_bf16 v[12:15], a[184:187], a[8:11], v[12:15]\n" \
"s_waitcnt lgkmcnt(6)\n" \
"ds_read_b64_tr_b16 a[208:209], v228 offset:16896\n" \
"ds_read_b64_tr_b16 a[210:211], v228 offset:18944\n" \
"v_mfma_f32_16x16x32_bf16 v[12:15], a[188:191], a[12:15], v[12:15]\n" \
"buffer_load_dwordx4 v[204:207], v234, s[56:59], s71 offen offset:3072 sc1\n" \
"v_mfma_f32_16x16x32_bf16 v[28:31], a[184:187], a[32:35], v[28:31]\n" \
"ds_read_b64_tr_b16 a[212:213], v228 offset:25088\n" \
"ds_read_b64_tr_b16 a[214:215], v228 offset:27136\n" \
"v_mfma_f32_16x16x32_bf16 v[28:31], a[188:191], a[36:39], v[28:31]\n" \
"ds_read_b64_tr_b16 a[216:217], v229 offset:16896\n" \
"ds_read_b64_tr_b16 a[218:219], v229 offset:18944\n" \
"v_mfma_f32_16x16x32_bf16 v[24:27], a[176:179], a[32:35], v[24:27]\n" \
"ds_read_b64_tr_b16 a[220:221], v229 offset:25088\n" \
"ds_read_b64_tr_b16 a[222:223], v229 offset:27136\n" \
"v_mfma_f32_16x16x32_bf16 v[24:27], a[180:183], a[36:39], v[24:27]\n" \
"ds_read_b128 a[40:43], v227 offset:256\n" \
"ds_read_b128 a[44:47], v227 offset:2304\n" \
"v_mfma_f32_16x16x32_bf16 v[20:23], a[168:171], a[32:35], v[20:23]\n" \
"ds_read_b128 a[248:251], v213 offset:4096\n" \
"ds_read_b128 a[252:255], v213 offset:6144\n" \
"v_mfma_f32_16x16x32_bf16 v[20:23], a[172:175], a[36:39], v[20:23]\n" \
"ds_read_b128 a[48:51], v224 offset:4096\n" \
"ds_read_b128 a[52:55], v224 offset:6144\n" \
"v_mfma_f32_16x16x32_bf16 v[16:19], a[160:163], a[32:35], v[16:19]\n" \
"ds_read_b128 a[56:59], v227 offset:4096\n" \
"ds_read_b128 a[60:63], v227 offset:6144\n" \
"v_mfma_f32_16x16x32_bf16 v[16:19], a[164:167], a[36:39], v[16:19]\n" \
"s_waitcnt lgkmcnt(8)\n" \
"ds_read_b128 a[224:227], v213 offset:4352\n" \
"ds_read_b128 a[228:231], v213 offset:6400\n" \
"v_add_u32 v128, s68, v128\n" \
"v_mfma_f32_16x16x32_bf16 v[0:3], a[192:195], a[16:19], v[0:3]\n" \
"s_waitcnt vmcnt(7)\n" \
"ds_read_b128 a[232:235], v224 offset:4352\n" \
"ds_read_b128 a[236:239], v224 offset:6400\n" \
"v_mfma_f32_16x16x32_bf16 v[0:3], a[196:199], a[20:23], v[0:3]\n" \
"ds_read_b128 a[240:243], v227 offset:4352\n" \
"ds_read_b128 a[244:247], v227 offset:6400\n" \
"v_mfma_f32_16x16x32_bf16 v[0:3], v[66:69], v[176:179], v[0:3]\n" \
"v_lshrrev_b32 v133, 3, v132\n" \
"v_and_b32 v134, 7, v132\n" \
"v_mfma_f32_16x16x32_bf16 v[4:7], a[200:203], a[16:19], v[4:7]\n" \
"v_mul_u32_u24 v129, 144, v133\n" \
"v_lshl_add_u32 v129, v134, 4, v129\n" \
"v_mfma_f32_16x16x32_bf16 v[4:7], a[204:207], a[20:23], v[4:7]\n" \
"v_add_u32 v129, s68, v129\n" \
"v_lshlrev_b32 v134, 4, v134\n" \
"v_mfma_f32_16x16x32_bf16 v[4:7], v[64:67], v[176:179], v[4:7]\n" \
"v_lshl_add_u32 v130, v133, 12, v134\n" \
"v_mfma_f32_16x16x32_bf16 v[8:11], a[208:211], a[16:19], v[8:11]\n" \
"v_mul_f32 v0, 4.0, v0\n" \
"v_mul_f32 v1, 4.0, v1\n" \
"v_mul_f32 v2, 4.0, v2\n" \
"v_mul_f32 v3, 4.0, v3\n" \
"v_cvt_pk_bf16_f32 v136, v0, v1\n" \
"v_cvt_pk_bf16_f32 v137, v2, v3\n" \
"v_mfma_f32_16x16x32_bf16 v[8:11], a[212:215], a[20:23], v[8:11]\n" \
"v_mfma_f32_16x16x32_bf16 v[8:11], v[66:69], v[180:183], v[8:11]\n" \
"ds_write_b64 v128, v[136:137] offset:0\n" \
"v_mfma_f32_16x16x32_bf16 v[12:15], a[216:219], a[16:19], v[12:15]\n" \
"v_mul_f32 v4, 4.0, v4\n" \
"v_mul_f32 v5, 4.0, v5\n" \
"v_mfma_f32_16x16x32_bf16 v[12:15], a[220:223], a[20:23], v[12:15]\n" \
"v_mul_f32 v6, 4.0, v6\n" \
"v_mul_f32 v7, 4.0, v7\n" \
"v_mfma_f32_16x16x32_bf16 v[12:15], v[64:67], v[180:183], v[12:15]\n" \
"s_waitcnt lgkmcnt(1)\n" \
"v_cvt_pk_bf16_f32 v138, v4, v5\n" \
"v_cvt_pk_bf16_f32 v139, v6, v7\n" \
"v_mfma_f32_16x16x32_bf16 v[16:19], a[192:195], a[40:43], v[16:19]\n" \
"s_waitcnt vmcnt(5)\n" \
"v_mfma_f32_16x16x32_bf16 v[16:19], a[196:199], a[44:47], v[16:19]\n" \
"v_mfma_f32_16x16x32_bf16 v[16:19], v[66:69], v[184:187], v[16:19]\n" \
"ds_write_b64 v128, v[138:139] offset:32\n" \
"v_mul_f32 v8, 4.0, v8\n" \
"v_mfma_f32_16x16x32_bf16 v[20:23], a[200:203], a[40:43], v[20:23]\n" \
"v_mul_f32 v9, 4.0, v9\n" \
"v_mul_f32 v10, 4.0, v10\n" \
"v_mfma_f32_16x16x32_bf16 v[20:23], a[204:207], a[44:47], v[20:23]\n" \
"v_mul_f32 v11, 4.0, v11\n" \
"v_cvt_pk_bf16_f32 v136, v8, v9\n" \
"v_mfma_f32_16x16x32_bf16 v[20:23], v[64:67], v[184:187], v[20:23]\n" \
"v_cvt_pk_bf16_f32 v137, v10, v11\n" \
"v_mfma_f32_16x16x32_bf16 v[24:27], a[208:211], a[40:43], v[24:27]\n" \
"v_mfma_f32_16x16x32_bf16 v[24:27], a[212:215], a[44:47], v[24:27]\n" \
"v_mfma_f32_16x16x32_bf16 v[24:27], v[66:69], v[188:191], v[24:27]\n" \
"ds_write_b64 v128, v[136:137] offset:64\n" \
"v_mul_f32 v12, 4.0, v12\n" \
"v_mfma_f32_16x16x32_bf16 v[28:31], a[216:219], a[40:43], v[28:31]\n" \
"v_mul_f32 v13, 4.0, v13\n" \
"v_mul_f32 v14, 4.0, v14\n" \
"v_mfma_f32_16x16x32_bf16 v[28:31], a[220:223], a[44:47], v[28:31]\n" \
"v_mul_f32 v15, 4.0, v15\n" \
"v_cvt_pk_bf16_f32 v138, v12, v13\n" \
"v_mfma_f32_16x16x32_bf16 v[28:31], v[64:67], v[188:191], v[28:31]\n" \
"v_cvt_pk_bf16_f32 v139, v14, v15\n" \
"v_mfma_f32_16x16x32_bf16 v[32:35], a[128:131], a[248:251], v[32:35]\n" \
"ds_write_b64 v128, v[138:139] offset:96\n" \
"v_mfma_f32_16x16x32_bf16 v[32:35], a[132:135], a[252:255], v[32:35]\n" \
"ds_read_b128 v[140:143], v129 offset:0\n" \
"ds_read_b128 v[144:147], v129 offset:1152\n" \
"v_mfma_f32_16x16x32_bf16 v[32:35], a[160:163], a[48:51], v[32:35]\n" \
"s_waitcnt vmcnt(2)\n" \
"v_mul_f32 v16, 4.0, v16\n" \
"v_mul_f32 v17, 4.0, v17\n" \
"v_mfma_f32_16x16x32_bf16 v[32:35], a[164:167], a[52:55], v[32:35]\n" \
"v_mul_f32 v18, 4.0, v18\n" \
"v_mul_f32 v19, 4.0, v19\n" \
"v_mfma_f32_16x16x32_bf16 v[32:35], v[66:69], v[192:195], v[32:35]\n" \
"s_add_u32 s70, s88, 0\n" \
"s_waitcnt lgkmcnt(1)\n" \
"v_cvt_pk_bf16_f32 v136, v16, v17\n" \
"v_mfma_f32_16x16x32_bf16 v[32:35], a[192:195], a[56:59], v[32:35]\n" \
"buffer_store_dwordx4 v[140:143], v130, s[52:55], s70 offen sc1\n" \
"v_cvt_pk_bf16_f32 v137, v18, v19\n" \
"v_mfma_f32_16x16x32_bf16 v[32:35], a[196:199], a[60:63], v[32:35]\n" \
"ds_write_b64 v128, v[136:137] offset:2304\n" \
"v_mfma_f32_16x16x32_bf16 v[36:39], a[204:207], a[60:63], v[36:39]\n" \
"s_add_u32 s70, s88, 32768\n" \
"s_waitcnt lgkmcnt(1)\n" \
"v_mul_f32 v20, 4.0, v20\n" \
"v_mfma_f32_16x16x32_bf16 v[36:39], a[200:203], a[56:59], v[36:39]\n" \
"buffer_store_dwordx4 v[144:147], v130, s[52:55], s70 offen sc1\n" \
"v_mfma_f32_16x16x32_bf16 v[36:39], a[172:175], a[52:55], v[36:39]\n" \
"v_mul_f32 v21, 4.0, v21\n" \
"v_mul_f32 v22, 4.0, v22\n" \
"v_mfma_f32_16x16x32_bf16 v[36:39], a[168:171], a[48:51], v[36:39]\n" \
"v_mul_f32 v23, 4.0, v23\n" \
"v_cvt_pk_bf16_f32 v138, v20, v21\n" \
"v_mfma_f32_16x16x32_bf16 v[36:39], v[64:67], v[192:195], v[36:39]\n" \
"v_cvt_pk_bf16_f32 v139, v22, v23\n" \
"v_mfma_f32_16x16x32_bf16 v[36:39], a[140:143], a[252:255], v[36:39]\n" \
"v_mfma_f32_16x16x32_bf16 v[36:39], a[136:139], a[248:251], v[36:39]\n" \
"ds_write_b64 v128, v[138:139] offset:2336\n" \
"v_mul_f32 v24, 4.0, v24\n" \
"v_mul_f32 v25, 4.0, v25\n" \
"v_mfma_f32_16x16x32_bf16 v[40:43], a[144:147], a[248:251], v[40:43]\n" \
"v_mul_f32 v26, 4.0, v26\n" \
"v_mul_f32 v27, 4.0, v27\n" \
"v_mfma_f32_16x16x32_bf16 v[40:43], a[148:151], a[252:255], v[40:43]\n" \
"v_cvt_pk_bf16_f32 v136, v24, v25\n" \
"v_cvt_pk_bf16_f32 v137, v26, v27\n" \
"v_mfma_f32_16x16x32_bf16 v[40:43], a[176:179], a[48:51], v[40:43]\n" \
"ds_write_b64 v128, v[136:137] offset:2368\n" \
"v_mfma_f32_16x16x32_bf16 v[40:43], a[180:183], a[52:55], v[40:43]\n" \
"v_mul_f32 v28, 4.0, v28\n" \
"v_mul_f32 v29, 4.0, v29\n" \
"v_mfma_f32_16x16x32_bf16 v[40:43], v[66:69], v[196:199], v[40:43]\n" \
"v_mul_f32 v30, 4.0, v30\n" \
"v_mul_f32 v31, 4.0, v31\n" \
"v_mfma_f32_16x16x32_bf16 v[40:43], a[208:211], a[56:59], v[40:43]\n" \
"v_cvt_pk_bf16_f32 v138, v28, v29\n" \
"v_cvt_pk_bf16_f32 v139, v30, v31\n" \
"v_mfma_f32_16x16x32_bf16 v[40:43], a[212:215], a[60:63], v[40:43]\n" \
"ds_write_b64 v128, v[138:139] offset:2400\n" \
"v_mfma_f32_16x16x32_bf16 v[44:47], a[220:223], a[60:63], v[44:47]\n" \
"ds_read_b128 v[148:151], v129 offset:2304\n" \
"ds_read_b128 v[152:155], v129 offset:3456\n" \
"v_mfma_f32_16x16x32_bf16 v[44:47], a[216:219], a[56:59], v[44:47]\n" \
"v_mul_f32 v32, 4.0, v32\n" \
"v_mul_f32 v33, 4.0, v33\n" \
"v_mfma_f32_16x16x32_bf16 v[44:47], a[188:191], a[52:55], v[44:47]\n" \
"v_mul_f32 v34, 4.0, v34\n" \
"v_mul_f32 v35, 4.0, v35\n" \
"v_mfma_f32_16x16x32_bf16 v[44:47], a[184:187], a[48:51], v[44:47]\n" \
"s_add_u32 s70, s88, 65536\n" \
"s_waitcnt lgkmcnt(1)\n" \
"v_cvt_pk_bf16_f32 v136, v32, v33\n" \
"v_mfma_f32_16x16x32_bf16 v[44:47], v[64:67], v[196:199], v[44:47]\n" \
"buffer_store_dwordx4 v[148:151], v130, s[52:55], s70 offen sc1\n" \
"v_cvt_pk_bf16_f32 v137, v34, v35\n" \
"v_mfma_f32_16x16x32_bf16 v[44:47], a[156:159], a[252:255], v[44:47]\n" \
"v_mfma_f32_16x16x32_bf16 v[44:47], a[152:155], a[248:251], v[44:47]\n" \
"s_add_u32 s70, s88, 98304\n" \
"s_waitcnt lgkmcnt(0)\n" \
"ds_write_b64 v128, v[136:137] offset:4608\n" \
"v_mul_f32 v36, 4.0, v36\n" \
"v_mfma_f32_16x16x32_bf16 v[48:51], a[128:131], a[224:227], v[48:51]\n" \
"buffer_store_dwordx4 v[152:155], v130, s[52:55], s70 offen sc1\n" \
"v_mul_f32 v37, 4.0, v37\n" \
"v_mfma_f32_16x16x32_bf16 v[48:51], a[132:135], a[228:231], v[48:51]\n" \
"v_mul_f32 v38, 4.0, v38\n" \
"v_mul_f32 v39, 4.0, v39\n" \
"v_mfma_f32_16x16x32_bf16 v[48:51], a[160:163], a[232:235], v[48:51]\n" \
"s_waitcnt vmcnt(4)\n" \
"v_cvt_pk_bf16_f32 v138, v36, v37\n" \
"v_cvt_pk_bf16_f32 v139, v38, v39\n" \
"v_mfma_f32_16x16x32_bf16 v[48:51], a[164:167], a[236:239], v[48:51]\n" \
"ds_write_b64 v128, v[138:139] offset:4640\n" \
"v_mul_f32 v40, 4.0, v40\n" \
"v_mfma_f32_16x16x32_bf16 v[48:51], v[66:69], v[200:203], v[48:51]\n" \
"v_mul_f32 v41, 4.0, v41\n" \
"v_mul_f32 v42, 4.0, v42\n" \
"Lpg10821_%=:\n" \
"s_cbranch_execz Lcb1_%=\n" \
"v_mfma_f32_16x16x32_bf16 v[48:51], a[192:195], a[240:243], v[48:51]\n" \
"v_mul_f32 v43, 4.0, v43\n" \
"v_cvt_pk_bf16_f32 v136, v40, v41\n" \
"v_mfma_f32_16x16x32_bf16 v[48:51], a[196:199], a[244:247], v[48:51]\n" \
"v_cvt_pk_bf16_f32 v137, v42, v43\n" \
"v_mfma_f32_16x16x32_bf16 v[52:55], a[204:207], a[244:247], v[52:55]\n" \
"ds_write_b64 v128, v[136:137] offset:4672\n" \
"v_mul_f32 v44, 4.0, v44\n" \
"v_mul_f32 v45, 4.0, v45\n" \
"v_mfma_f32_16x16x32_bf16 v[52:55], a[200:203], a[240:243], v[52:55]\n" \
"v_mul_f32 v46, 4.0, v46\n" \
"v_mul_f32 v47, 4.0, v47\n" \
"v_mfma_f32_16x16x32_bf16 v[52:55], a[172:175], a[236:239], v[52:55]\n" \
"v_cvt_pk_bf16_f32 v138, v44, v45\n" \
"v_cvt_pk_bf16_f32 v139, v46, v47\n" \
"v_mfma_f32_16x16x32_bf16 v[52:55], a[168:171], a[232:235], v[52:55]\n" \
"ds_write_b64 v128, v[138:139] offset:4704\n" \
"v_mfma_f32_16x16x32_bf16 v[52:55], v[64:67], v[200:203], v[52:55]\n" \
"ds_read_b128 v[156:159], v129 offset:4608\n" \
"ds_read_b128 v[160:163], v129 offset:5760\n" \
"v_mfma_f32_16x16x32_bf16 v[52:55], a[140:143], a[228:231], v[52:55]\n" \
"v_mul_f32 v48, 4.0, v48\n" \
"v_mul_f32 v49, 4.0, v49\n" \
"v_mfma_f32_16x16x32_bf16 v[52:55], a[136:139], a[224:227], v[52:55]\n" \
"v_mul_f32 v50, 4.0, v50\n" \
"v_mul_f32 v51, 4.0, v51\n" \
"v_mfma_f32_16x16x32_bf16 v[56:59], a[144:147], a[224:227], v[56:59]\n" \
"s_add_u32 s70, s88, 131072\n" \
"s_waitcnt lgkmcnt(1)\n" \
"v_cvt_pk_bf16_f32 v136, v48, v49\n" \
"v_mfma_f32_16x16x32_bf16 v[56:59], a[148:151], a[228:231], v[56:59]\n" \
"buffer_store_dwordx4 v[156:159], v130, s[52:55], s70 offen sc1\n" \
"v_cvt_pk_bf16_f32 v137, v50, v51\n" \
"v_mfma_f32_16x16x32_bf16 v[56:59], a[176:179], a[232:235], v[56:59]\n" \
"ds_write_b64 v128, v[136:137] offset:6912\n" \
"v_mfma_f32_16x16x32_bf16 v[56:59], a[180:183], a[236:239], v[56:59]\n" \
"s_add_u32 s70, s88, 163840\n" \
"s_waitcnt lgkmcnt(1)\n" \
"v_mul_f32 v52, 4.0, v52\n" \
"v_mfma_f32_16x16x32_bf16 v[56:59], v[66:69], v[204:207], v[56:59]\n" \
"buffer_store_dwordx4 v[160:163], v130, s[52:55], s70 offen sc1\n" \
"v_mfma_f32_16x16x32_bf16 v[56:59], a[208:211], a[240:243], v[56:59]\n" \
"v_mul_f32 v53, 4.0, v53\n" \
"v_mul_f32 v54, 4.0, v54\n" \
"v_mfma_f32_16x16x32_bf16 v[56:59], a[212:215], a[244:247], v[56:59]\n" \
"v_mul_f32 v55, 4.0, v55\n" \
"v_cvt_pk_bf16_f32 v138, v52, v53\n" \
"v_cvt_pk_bf16_f32 v139, v54, v55\n" \
"v_mfma_f32_16x16x32_bf16 v[60:63], a[220:223], a[244:247], v[60:63]\n" \
"ds_write_b64 v128, v[138:139] offset:6944\n" \
"v_mfma_f32_16x16x32_bf16 v[60:63], a[216:219], a[240:243], v[60:63]\n" \
"v_mfma_f32_16x16x32_bf16 v[60:63], a[188:191], a[236:239], v[60:63]\n" \
"v_mfma_f32_16x16x32_bf16 v[60:63], a[184:187], a[232:235], v[60:63]\n" \
"v_mul_f32 v56, 4.0, v56\n" \
"v_mul_f32 v57, 4.0, v57\n" \
"v_mfma_f32_16x16x32_bf16 v[60:63], v[64:67], v[204:207], v[60:63]\n" \
"v_mul_f32 v58, 4.0, v58\n" \
"v_mul_f32 v59, 4.0, v59\n" \
"v_mfma_f32_16x16x32_bf16 v[60:63], a[156:159], a[228:231], v[60:63]\n" \
"v_cvt_pk_bf16_f32 v136, v56, v57\n" \
"v_cvt_pk_bf16_f32 v137, v58, v59\n" \
"v_mfma_f32_16x16x32_bf16 v[60:63], a[152:155], a[224:227], v[60:63]\n" \
"ds_write_b64 v128, v[136:137] offset:6976\n" \
"s_nop 7\n" \
"v_mul_f32 v60, 4.0, v60\n" \
"v_mul_f32 v61, 4.0, v61\n" \
"v_mul_f32 v62, 4.0, v62\n" \
"v_mul_f32 v63, 4.0, v63\n" \
"v_cvt_pk_bf16_f32 v138, v60, v61\n" \
"v_cvt_pk_bf16_f32 v139, v62, v63\n" \
"ds_write_b64 v128, v[138:139] offset:7008\n" \
"ds_read_b128 v[164:167], v129 offset:6912\n" \
"ds_read_b128 v[168:171], v129 offset:8064\n" \
"s_add_u32 s70, s88, 196608\n" \
"s_waitcnt lgkmcnt(1)\n" \
"buffer_store_dwordx4 v[164:167], v130, s[52:55], s70 offen sc1\n" \
"s_add_u32 s70, s88, 229376\n" \
"s_waitcnt lgkmcnt(0)\n" \
"buffer_store_dwordx4 v[168:171], v130, s[52:55], s70 offen sc1\n" \
"s_endpgm\n" \
".p2align 12\n" \
"Lrole1_%=:\n" \
"v_mfma_f32_16x16x32_bf16 v[64:67], a[96:99], a[64:67], 0\n" \
"Lpg10905_%=:\n" \
"s_cbranch_execz Lcb1_%=\n" \
"ds_read_b128 a[88:91], v213 offset:4352\n" \
"v_mfma_f32_16x16x32_bf16 v[64:67], a[100:103], a[68:71], v[64:67]\n" \
"s_waitcnt lgkmcnt(2)\n" \
"s_xor_b32 s82, s48, s49\n" \
"ds_read_b128 a[92:95], v213 offset:6400\n" \
"v_mfma_f32_16x16x32_bf16 v[68:71], a[104:107], a[64:67], 0\n" \
"s_cmp_lt_u32 s48, s50\n" \
"s_cselect_b32 s82, s82, s48\n" \
"ds_read_b64_tr_b16 a[112:113], v214 offset:16896\n" \
"v_mfma_f32_16x16x32_bf16 v[68:71], a[108:111], a[68:71], v[68:71]\n" \
"s_add_u32 s81, s82, s46\n" \
"ds_read_b64_tr_b16 a[114:115], v214 offset:18944\n" \
"v_mfma_f32_16x16x32_bf16 v[84:87], a[104:107], a[72:75], 0\n" \
"s_waitcnt lgkmcnt(4)\n" \
"s_add_u32 m0, s86, s79\n" \
"ds_read_b64_tr_b16 a[116:117], v214 offset:25088\n" \
"v_mfma_f32_16x16x32_bf16 v[84:87], a[108:111], a[76:79], v[84:87]\n" \
"buffer_load_dwordx4 v234, s[56:59], s81 offen offset:0 sc1 lds\n" \
"v_mfma_f32_16x16x32_bf16 v[80:83], a[96:99], a[72:75], 0\n" \
"s_sub_u32 s85, s85, 32768\n" \
"s_cselect_b32 s85, 131072, s85\n" \
"ds_read_b64_tr_b16 a[118:119], v214 offset:27136\n" \
"v_mfma_f32_16x16x32_bf16 v[80:83], a[100:103], a[76:79], v[80:83]\n" \
"s_lshl_b32 s82, s82, 5\n" \
"ds_read_b64_tr_b16 a[120:121], v215 offset:16896\n" \
"v_cvt_pk_bf16_f32 v0, v0, v1\n" \
"v_mfma_f32_16x16x32_bf16 v[96:99], a[96:99], a[80:83], 0\n" \
"s_add_u32 s82, s82, s47\n" \
"ds_read_b64_tr_b16 a[122:123], v215 offset:18944\n" \
"v_cvt_pk_bf16_f32 v1, v2, v3\n" \
"v_mfma_f32_16x16x32_bf16 v[96:99], a[100:103], a[84:87], v[96:99]\n" \
"buffer_load_dwordx4 v234, s[56:59], s81 offen offset:1024 sc1 lds\n" \
"v_cvt_pk_bf16_f32 v2, v4, v5\n" \
"v_mfma_f32_16x16x32_bf16 v[100:103], a[104:107], a[80:83], 0\n" \
"s_waitcnt lgkmcnt(3)\n" \
"ds_read_b64_tr_b16 a[124:125], v215 offset:25088\n" \
"v_add_u32 v213, s85, v211\n" \
"v_mfma_f32_16x16x32_bf16 v[100:103], a[108:111], a[84:87], v[100:103]\n" \
"buffer_load_dwordx4 v234, s[56:59], s81 offen offset:2048 sc1 lds\n" \
"v_mfma_f32_16x16x32_bf16 v[116:119], a[104:107], a[88:91], 0\n" \
"ds_read_b64_tr_b16 a[126:127], v215 offset:27136\n" \
"v_cvt_pk_bf16_f32 v3, v6, v7\n" \
"v_mfma_f32_16x16x32_bf16 v[116:119], a[108:111], a[92:95], v[116:119]\n" \
"buffer_load_dwordx4 v234, s[56:59], s81 offen offset:3072 sc1 lds\n" \
"v_cvt_pk_bf16_f32 v4, v8, v9\n" \
"v_mfma_f32_16x16x32_bf16 v[112:115], a[96:99], a[88:91], 0\n" \
"v_add_u32 v214, s85, v212\n" \
"v_xor_b32 v215, 32, v214\n" \
"v_mfma_f32_16x16x32_bf16 v[112:115], a[100:103], a[92:95], v[112:115]\n" \
"v_cvt_pk_bf16_f32 v5, v10, v11\n" \
"s_waitcnt vmcnt(20) lgkmcnt(0)\n" \
"s_barrier\n" \
"v_mfma_f32_16x16x32_bf16 v[72:75], a[112:115], a[64:67], 0\n" \
"s_add_u32 m0, m0, 0x4000\n" \
"ds_read_b64_tr_b16 a[96:97], v214 offset:16384\n" \
"ds_read_b64_tr_b16 a[98:99], v214 offset:18432\n" \
"v_mfma_f32_16x16x32_bf16 v[72:75], a[116:119], a[68:71], v[72:75]\n" \
"buffer_load_dwordx4 v216, s[36:39], s82 offen offset:0 lds\n" \
"v_mfma_f32_16x16x32_bf16 v[76:79], a[120:123], a[64:67], 0\n" \
"ds_read_b64_tr_b16 a[100:101], v214 offset:24576\n" \
"ds_read_b64_tr_b16 a[102:103], v214 offset:26624\n" \
"v_mfma_f32_16x16x32_bf16 v[76:79], a[124:127], a[68:71], v[76:79]\n" \
"ds_read_b128 a[64:67], v213 offset:0\n" \
"v_cvt_pk_bf16_f32 v6, v12, v13\n" \
"v_cvt_pk_bf16_f32 v7, v14, v15\n" \
"v_mfma_f32_16x16x32_bf16 v[92:95], a[120:123], a[72:75], 0\n" \
"s_add_u32 s48, s48, 0x1000\n" \
"ds_read_b128 a[68:71], v213 offset:2048\n" \
"v_cvt_pk_bf16_f32 v8, v16, v17\n" \
"v_mfma_f32_16x16x32_bf16 v[92:95], a[124:127], a[76:79], v[92:95]\n" \
"buffer_load_dwordx4 v217, s[36:39], s82 offen offset:1024 lds\n" \
"v_mfma_f32_16x16x32_bf16 v[88:91], a[112:115], a[72:75], 0\n" \
"ds_read_b64_tr_b16 a[104:105], v215 offset:16384\n" \
"ds_read_b64_tr_b16 a[106:107], v215 offset:18432\n" \
"v_mfma_f32_16x16x32_bf16 v[88:91], a[116:119], a[76:79], v[88:91]\n" \
"ds_read_b128 a[72:75], v213 offset:256\n" \
"v_cvt_pk_bf16_f32 v9, v18, v19\n" \
"v_cvt_pk_bf16_f32 v10, v20, v21\n" \
"v_mfma_f32_16x16x32_bf16 v[104:107], a[112:115], a[80:83], 0\n" \
"s_sub_u32 s86, s86, 32768\n" \
"s_cselect_b32 s86, 131072, s86\n" \
"ds_read_b128 a[76:79], v213 offset:2304\n" \
"v_mfma_f32_16x16x32_bf16 v[104:107], a[116:119], a[84:87], v[104:107]\n" \
"buffer_load_dwordx4 v218, s[36:39], s82 offen offset:2048 lds\n" \
"v_cvt_pk_bf16_f32 v11, v22, v23\n" \
"v_mfma_f32_16x16x32_bf16 v[108:111], a[120:123], a[80:83], 0\n" \
"ds_read_b64_tr_b16 a[108:109], v215 offset:24576\n" \
"ds_read_b64_tr_b16 a[110:111], v215 offset:26624\n" \
"v_mfma_f32_16x16x32_bf16 v[108:111], a[124:127], a[84:87], v[108:111]\n" \
"buffer_load_dwordx4 v219, s[36:39], s82 offen offset:3072 lds\n" \
"v_mfma_f32_16x16x32_bf16 v[124:127], a[120:123], a[88:91], 0\n" \
"s_waitcnt lgkmcnt(3)\n" \
"ds_read_b128 a[80:83], v213 offset:4096\n" \
"v_mfma_f32_16x16x32_bf16 v[124:127], a[124:127], a[92:95], v[124:127]\n" \
"s_add_u32 s71, s87, 0\n" \
"ds_read_b128 a[84:87], v213 offset:6144\n" \
"v_cvt_pk_bf16_f32 v12, v24, v25\n" \
"v_mfma_f32_16x16x32_bf16 v[120:123], a[112:115], a[88:91], 0\n" \
"buffer_store_dwordx4 v[0:3], v234, s[56:59], s71 offen offset:0 sc1\n" \
"v_mfma_f32_16x16x32_bf16 v[120:123], a[116:119], a[92:95], v[120:123]\n" \
"buffer_store_dwordx4 v[4:7], v234, s[56:59], s71 offen offset:1024 sc1\n" \
"buffer_store_dwordx4 v[8:11], v234, s[56:59], s71 offen offset:2048 sc1\n" \
"v_mfma_f32_16x16x32_bf16 v[64:67], a[96:99], a[64:67], v[64:67]\n" \
"s_waitcnt lgkmcnt(2)\n" \
"ds_read_b128 a[88:91], v213 offset:4352\n" \
"v_cvt_pk_bf16_f32 v13, v26, v27\n" \
"v_mfma_f32_16x16x32_bf16 v[64:67], a[100:103], a[68:71], v[64:67]\n" \
"s_xor_b32 s82, s48, s49\n" \
"ds_read_b128 a[92:95], v213 offset:6400\n" \
"v_cvt_pk_bf16_f32 v14, v28, v29\n" \
"v_mfma_f32_16x16x32_bf16 v[68:71], a[104:107], a[64:67], v[68:71]\n" \
"s_cmp_lt_u32 s48, s50\n" \
"s_cselect_b32 s82, s82, s48\n" \
"ds_read_b64_tr_b16 a[112:113], v214 offset:16896\n" \
"v_mfma_f32_16x16x32_bf16 v[68:71], a[108:111], a[68:71], v[68:71]\n" \
"s_add_u32 s81, s82, s46\n" \
"ds_read_b64_tr_b16 a[114:115], v214 offset:18944\n" \
"v_cvt_pk_bf16_f32 v15, v30, v31\n" \
"v_mfma_f32_16x16x32_bf16 v[84:87], a[104:107], a[72:75], v[84:87]\n" \
"s_add_u32 m0, s86, s79\n" \
"ds_read_b64_tr_b16 a[116:117], v214 offset:25088\n" \
"v_cvt_pk_bf16_f32 v16, v32, v33\n" \
"v_mfma_f32_16x16x32_bf16 v[84:87], a[108:111], a[76:79], v[84:87]\n" \
"buffer_load_dwordx4 v234, s[56:59], s81 offen offset:0 sc1 lds\n" \
"v_mfma_f32_16x16x32_bf16 v[80:83], a[96:99], a[72:75], v[80:83]\n" \
"s_sub_u32 s85, s85, 32768\n" \
"s_cselect_b32 s85, 131072, s85\n" \
"ds_read_b64_tr_b16 a[118:119], v214 offset:27136\n" \
"v_mfma_f32_16x16x32_bf16 v[80:83], a[100:103], a[76:79], v[80:83]\n" \
"s_waitcnt lgkmcnt(3)\n" \
"s_lshl_b32 s82, s82, 5\n" \
"ds_read_b64_tr_b16 a[120:121], v215 offset:16896\n" \
"v_cvt_pk_bf16_f32 v17, v34, v35\n" \
"v_mfma_f32_16x16x32_bf16 v[96:99], a[96:99], a[80:83], v[96:99]\n" \
"s_add_u32 s82, s82, s47\n" \
"ds_read_b64_tr_b16 a[122:123], v215 offset:18944\n" \
"v_cvt_pk_bf16_f32 v18, v36, v37\n" \
"v_mfma_f32_16x16x32_bf16 v[96:99], a[100:103], a[84:87], v[96:99]\n" \
"buffer_load_dwordx4 v234, s[56:59], s81 offen offset:1024 sc1 lds\n" \
"v_mfma_f32_16x16x32_bf16 v[100:103], a[104:107], a[80:83], v[100:103]\n" \
"ds_read_b64_tr_b16 a[124:125], v215 offset:25088\n" \
"v_add_u32 v213, s85, v211\n" \
"v_mfma_f32_16x16x32_bf16 v[100:103], a[108:111], a[84:87], v[100:103]\n" \
"buffer_load_dwordx4 v234, s[56:59], s81 offen offset:2048 sc1 lds\n" \
"v_mfma_f32_16x16x32_bf16 v[116:119], a[104:107], a[88:91], v[116:119]\n" \
"ds_read_b64_tr_b16 a[126:127], v215 offset:27136\n" \
"v_cvt_pk_bf16_f32 v19, v38, v39\n" \
"v_mfma_f32_16x16x32_bf16 v[116:119], a[108:111], a[92:95], v[116:119]\n" \
"buffer_load_dwordx4 v234, s[56:59], s81 offen offset:3072 sc1 lds\n" \
"v_cvt_pk_bf16_f32 v20, v40, v41\n" \
"v_mfma_f32_16x16x32_bf16 v[112:115], a[96:99], a[88:91], v[112:115]\n" \
"v_add_u32 v214, s85, v212\n" \
"v_xor_b32 v215, 32, v214\n" \
"v_mfma_f32_16x16x32_bf16 v[112:115], a[100:103], a[92:95], v[112:115]\n" \
"s_waitcnt vmcnt(23) lgkmcnt(0)\n" \
"s_barrier\n" \
"v_mfma_f32_16x16x32_bf16 v[72:75], a[112:115], a[64:67], v[72:75]\n" \
"s_add_u32 m0, m0, 0x4000\n" \
"ds_read_b64_tr_b16 a[96:97], v214 offset:16384\n" \
"ds_read_b64_tr_b16 a[98:99], v214 offset:18432\n" \
"v_mfma_f32_16x16x32_bf16 v[72:75], a[116:119], a[68:71], v[72:75]\n" \
"buffer_load_dwordx4 v216, s[36:39], s82 offen offset:0 lds\n" \
"v_mfma_f32_16x16x32_bf16 v[76:79], a[120:123], a[64:67], v[76:79]\n" \
"ds_read_b64_tr_b16 a[100:101], v214 offset:24576\n" \
"ds_read_b64_tr_b16 a[102:103], v214 offset:26624\n" \
"v_mfma_f32_16x16x32_bf16 v[76:79], a[124:127], a[68:71], v[76:79]\n" \
"ds_read_b128 a[64:67], v213 offset:0\n" \
"v_cvt_pk_bf16_f32 v21, v42, v43\n" \
"v_mfma_f32_16x16x32_bf16 v[92:95], a[120:123], a[72:75], v[92:95]\n" \
"s_add_u32 s48, s48, 0x1000\n" \
"ds_read_b128 a[68:71], v213 offset:2048\n" \
"v_cvt_pk_bf16_f32 v22, v44, v45\n" \
"v_mfma_f32_16x16x32_bf16 v[92:95], a[124:127], a[76:79], v[92:95]\n" \
"buffer_load_dwordx4 v217, s[36:39], s82 offen offset:1024 lds\n" \
"v_mfma_f32_16x16x32_bf16 v[88:91], a[112:115], a[72:75], v[88:91]\n" \
"ds_read_b64_tr_b16 a[104:105], v215 offset:16384\n" \
"ds_read_b64_tr_b16 a[106:107], v215 offset:18432\n" \
"v_mfma_f32_16x16x32_bf16 v[88:91], a[116:119], a[76:79], v[88:91]\n" \
"ds_read_b128 a[72:75], v213 offset:256\n" \
"v_cvt_pk_bf16_f32 v23, v46, v47\n" \
"v_mfma_f32_16x16x32_bf16 v[104:107], a[112:115], a[80:83], v[104:107]\n" \
"s_sub_u32 s86, s86, 32768\n" \
"s_cselect_b32 s86, 131072, s86\n" \
"ds_read_b128 a[76:79], v213 offset:2304\n" \
"v_mfma_f32_16x16x32_bf16 v[104:107], a[116:119], a[84:87], v[104:107]\n" \
"buffer_load_dwordx4 v218, s[36:39], s82 offen offset:2048 lds\n" \
"v_mfma_f32_16x16x32_bf16 v[108:111], a[120:123], a[80:83], v[108:111]\n" \
"ds_read_b64_tr_b16 a[108:109], v215 offset:24576\n" \
"ds_read_b64_tr_b16 a[110:111], v215 offset:26624\n" \
"v_mfma_f32_16x16x32_bf16 v[108:111], a[124:127], a[84:87], v[108:111]\n" \
"buffer_load_dwordx4 v219, s[36:39], s82 offen offset:3072 lds\n" \
"v_mfma_f32_16x16x32_bf16 v[124:127], a[120:123], a[88:91], v[124:127]\n" \
"ds_read_b128 a[80:83], v213 offset:4096\n" \
"s_waitcnt lgkmcnt(4)\n" \
"v_mfma_f32_16x16x32_bf16 v[124:127], a[124:127], a[92:95], v[124:127]\n" \
"ds_read_b128 a[84:87], v213 offset:6144\n" \
"v_cvt_pk_bf16_f32 v24, v48, v49\n" \
"v_mfma_f32_16x16x32_bf16 v[120:123], a[112:115], a[88:91], v[120:123]\n" \
"buffer_store_dwordx4 v[12:15], v234, s[56:59], s71 offen offset:3072 sc1\n" \
"v_mfma_f32_16x16x32_bf16 v[120:123], a[116:119], a[92:95], v[120:123]\n" \
"s_add_u32 s71, s87, 4096\n" \
"buffer_store_dwordx4 v[16:19], v234, s[56:59], s71 offen offset:0 sc1\n" \
"buffer_store_dwordx4 v[20:23], v234, s[56:59], s71 offen offset:1024 sc1\n" \
"v_mfma_f32_16x16x32_bf16 v[64:67], a[96:99], a[64:67], v[64:67]\n" \
"s_waitcnt lgkmcnt(2)\n" \
"ds_read_b128 a[88:91], v213 offset:4352\n" \
"v_cvt_pk_bf16_f32 v25, v50, v51\n" \
"v_mfma_f32_16x16x32_bf16 v[64:67], a[100:103], a[68:71], v[64:67]\n" \
"s_xor_b32 s82, s48, s49\n" \
"ds_read_b128 a[92:95], v213 offset:6400\n" \
"v_cvt_pk_bf16_f32 v26, v52, v53\n" \
"v_mfma_f32_16x16x32_bf16 v[68:71], a[104:107], a[64:67], v[68:71]\n" \
"s_cmp_lt_u32 s48, s50\n" \
"s_cselect_b32 s82, s82, s48\n" \
"ds_read_b64_tr_b16 a[112:113], v214 offset:16896\n" \
"v_mfma_f32_16x16x32_bf16 v[68:71], a[108:111], a[68:71], v[68:71]\n" \
"s_add_u32 s81, s82, s46\n" \
"ds_read_b64_tr_b16 a[114:115], v214 offset:18944\n" \
"v_cvt_pk_bf16_f32 v27, v54, v55\n" \
"v_mfma_f32_16x16x32_bf16 v[84:87], a[104:107], a[72:75], v[84:87]\n" \
"s_add_u32 m0, s86, s79\n" \
"ds_read_b64_tr_b16 a[116:117], v214 offset:25088\n" \
"v_cvt_pk_bf16_f32 v28, v56, v57\n" \
"v_mfma_f32_16x16x32_bf16 v[84:87], a[108:111], a[76:79], v[84:87]\n" \
"buffer_load_dwordx4 v234, s[56:59], s81 offen offset:0 sc1 lds\n" \
"v_mfma_f32_16x16x32_bf16 v[80:83], a[96:99], a[72:75], v[80:83]\n" \
"s_sub_u32 s85, s85, 32768\n" \
"s_cselect_b32 s85, 131072, s85\n" \
"ds_read_b64_tr_b16 a[118:119], v214 offset:27136\n" \
"v_mfma_f32_16x16x32_bf16 v[80:83], a[100:103], a[76:79], v[80:83]\n" \
"s_waitcnt lgkmcnt(3)\n" \
"s_lshl_b32 s82, s82, 5\n" \
"ds_read_b64_tr_b16 a[120:121], v215 offset:16896\n" \
"v_cvt_pk_bf16_f32 v29, v58, v59\n" \
"v_mfma_f32_16x16x32_bf16 v[96:99], a[96:99], a[80:83], v[96:99]\n" \
"s_add_u32 s82, s82, s47\n" \
"ds_read_b64_tr_b16 a[122:123], v215 offset:18944\n" \
"v_cvt_pk_bf16_f32 v30, v60, v61\n" \
"v_mfma_f32_16x16x32_bf16 v[96:99], a[100:103], a[84:87], v[96:99]\n" \
"buffer_load_dwordx4 v234, s[56:59], s81 offen offset:1024 sc1 lds\n" \
"v_mfma_f32_16x16x32_bf16 v[100:103], a[104:107], a[80:83], v[100:103]\n" \
"ds_read_b64_tr_b16 a[124:125], v215 offset:25088\n" \
"v_add_u32 v213, s85, v211\n" \
"v_mfma_f32_16x16x32_bf16 v[100:103], a[108:111], a[84:87], v[100:103]\n" \
"buffer_load_dwordx4 v234, s[56:59], s81 offen offset:2048 sc1 lds\n" \
"v_mfma_f32_16x16x32_bf16 v[116:119], a[104:107], a[88:91], v[116:119]\n" \
"ds_read_b64_tr_b16 a[126:127], v215 offset:27136\n" \
"v_cvt_pk_bf16_f32 v31, v62, v63\n" \
"v_mfma_f32_16x16x32_bf16 v[116:119], a[108:111], a[92:95], v[116:119]\n" \
"buffer_load_dwordx4 v234, s[56:59], s81 offen offset:3072 sc1 lds\n" \
"v_mfma_f32_16x16x32_bf16 v[112:115], a[96:99], a[88:91], v[112:115]\n" \
"v_add_u32 v214, s85, v212\n" \
"v_xor_b32 v215, 32, v214\n" \
"v_mfma_f32_16x16x32_bf16 v[112:115], a[100:103], a[92:95], v[112:115]\n" \
"s_waitcnt vmcnt(26) lgkmcnt(0)\n" \
"s_barrier\n" \
"v_mfma_f32_16x16x32_bf16 v[72:75], a[112:115], a[64:67], v[72:75]\n" \
"s_add_u32 m0, m0, 0x4000\n" \
"ds_read_b64_tr_b16 a[96:97], v214 offset:16384\n" \
"ds_read_b64_tr_b16 a[98:99], v214 offset:18432\n" \
"v_mfma_f32_16x16x32_bf16 v[72:75], a[116:119], a[68:71], v[72:75]\n" \
"buffer_load_dwordx4 v216, s[36:39], s82 offen offset:0 lds\n" \
"v_mfma_f32_16x16x32_bf16 v[76:79], a[120:123], a[64:67], v[76:79]\n" \
"ds_read_b64_tr_b16 a[100:101], v214 offset:24576\n" \
"ds_read_b64_tr_b16 a[102:103], v214 offset:26624\n" \
"v_mfma_f32_16x16x32_bf16 v[76:79], a[124:127], a[68:71], v[76:79]\n" \
"ds_read_b128 a[64:67], v213 offset:0\n" \
"v_mfma_f32_16x16x32_bf16 v[92:95], a[120:123], a[72:75], v[92:95]\n" \
"s_add_u32 s48, s48, 0x1000\n" \
"ds_read_b128 a[68:71], v213 offset:2048\n" \
"v_mfma_f32_16x16x32_bf16 v[92:95], a[124:127], a[76:79], v[92:95]\n" \
"buffer_load_dwordx4 v217, s[36:39], s82 offen offset:1024 lds\n" \
"v_mfma_f32_16x16x32_bf16 v[88:91], a[112:115], a[72:75], v[88:91]\n" \
"ds_read_b64_tr_b16 a[104:105], v215 offset:16384\n" \
"ds_read_b64_tr_b16 a[106:107], v215 offset:18432\n" \
"v_mfma_f32_16x16x32_bf16 v[88:91], a[116:119], a[76:79], v[88:91]\n" \
"ds_read_b128 a[72:75], v213 offset:256\n" \
"v_mfma_f32_16x16x32_bf16 v[104:107], a[112:115], a[80:83], v[104:107]\n" \
"s_sub_u32 s86, s86, 32768\n" \
"s_cselect_b32 s86, 131072, s86\n" \
"ds_read_b128 a[76:79], v213 offset:2304\n" \
"v_mfma_f32_16x16x32_bf16 v[104:107], a[116:119], a[84:87], v[104:107]\n" \
"buffer_load_dwordx4 v218, s[36:39], s82 offen offset:2048 lds\n" \
"v_mfma_f32_16x16x32_bf16 v[108:111], a[120:123], a[80:83], v[108:111]\n" \
"ds_read_b64_tr_b16 a[108:109], v215 offset:24576\n" \
"ds_read_b64_tr_b16 a[110:111], v215 offset:26624\n" \
"v_mfma_f32_16x16x32_bf16 v[108:111], a[124:127], a[84:87], v[108:111]\n" \
"buffer_load_dwordx4 v219, s[36:39], s82 offen offset:3072 lds\n" \
"v_mfma_f32_16x16x32_bf16 v[124:127], a[120:123], a[88:91], v[124:127]\n" \
"ds_read_b128 a[80:83], v213 offset:4096\n" \
"s_waitcnt lgkmcnt(4)\n" \
"v_mfma_f32_16x16x32_bf16 v[124:127], a[124:127], a[92:95], v[124:127]\n" \
"ds_read_b128 a[84:87], v213 offset:6144\n" \
"v_mfma_f32_16x16x32_bf16 v[120:123], a[112:115], a[88:91], v[120:123]\n" \
"buffer_store_dwordx4 v[24:27], v234, s[56:59], s71 offen offset:2048 sc1\n" \
"v_mfma_f32_16x16x32_bf16 v[120:123], a[116:119], a[92:95], v[120:123]\n" \
"buffer_store_dwordx4 v[28:31], v234, s[56:59], s71 offen offset:3072 sc1\n" \
"v_mfma_f32_16x16x32_bf16 v[64:67], a[96:99], a[64:67], v[64:67]\n" \
"ds_read_b128 a[88:91], v213 offset:4352\n" \
"v_mfma_f32_16x16x32_bf16 v[64:67], a[100:103], a[68:71], v[64:67]\n" \
"s_waitcnt lgkmcnt(2)\n" \
"s_xor_b32 s82, s48, s49\n" \
"ds_read_b128 a[92:95], v213 offset:6400\n" \
"v_mfma_f32_16x16x32_bf16 v[68:71], a[104:107], a[64:67], v[68:71]\n" \
"s_cmp_lt_u32 s48, s50\n" \
"s_cselect_b32 s82, s82, s48\n" \
"ds_read_b64_tr_b16 a[112:113], v214 offset:16896\n" \
"v_mfma_f32_16x16x32_bf16 v[68:71], a[108:111], a[68:71], v[68:71]\n" \
"s_add_u32 s81, s82, s46\n" \
"ds_read_b64_tr_b16 a[114:115], v214 offset:18944\n" \
"v_mfma_f32_16x16x32_bf16 v[84:87], a[104:107], a[72:75], v[84:87]\n" \
"s_add_u32 m0, s86, s79\n" \
"ds_read_b64_tr_b16 a[116:117], v214 offset:25088\n" \
"v_mfma_f32_16x16x32_bf16 v[84:87], a[108:111], a[76:79], v[84:87]\n" \
"buffer_load_dwordx4 v234, s[56:59], s81 offen offset:0 sc1 lds\n" \
"v_mfma_f32_16x16x32_bf16 v[80:83], a[96:99], a[72:75], v[80:83]\n" \
"s_sub_u32 s85, s85, 32768\n" \
"s_cselect_b32 s85, 131072, s85\n" \
"ds_read_b64_tr_b16 a[118:119], v214 offset:27136\n" \
"v_mfma_f32_16x16x32_bf16 v[80:83], a[100:103], a[76:79], v[80:83]\n" \
"s_waitcnt lgkmcnt(3)\n" \
"s_lshl_b32 s82, s82, 5\n" \
"ds_read_b64_tr_b16 a[120:121], v215 offset:16896\n" \
"v_mfma_f32_16x16x32_bf16 v[96:99], a[96:99], a[80:83], v[96:99]\n" \
"s_add_u32 s82, s82, s47\n" \
"ds_read_b64_tr_b16 a[122:123], v215 offset:18944\n" \
"v_mfma_f32_16x16x32_bf16 v[96:99], a[100:103], a[84:87], v[96:99]\n" \
"buffer_load_dwordx4 v234, s[56:59], s81 offen offset:1024 sc1 lds\n" \
"v_mfma_f32_16x16x32_bf16 v[100:103], a[104:107], a[80:83], v[100:103]\n" \
"ds_read_b64_tr_b16 a[124:125], v215 offset:25088\n" \
"v_add_u32 v213, s85, v211\n" \
"v_mfma_f32_16x16x32_bf16 v[100:103], a[108:111], a[84:87], v[100:103]\n" \
"buffer_load_dwordx4 v234, s[56:59], s81 offen offset:2048 sc1 lds\n" \
"v_mfma_f32_16x16x32_bf16 v[116:119], a[104:107], a[88:91], v[116:119]\n" \
"ds_read_b64_tr_b16 a[126:127], v215 offset:27136\n" \
"v_mfma_f32_16x16x32_bf16 v[116:119], a[108:111], a[92:95], v[116:119]\n" \
"buffer_load_dwordx4 v234, s[56:59], s81 offen offset:3072 sc1 lds\n" \
"v_mfma_f32_16x16x32_bf16 v[112:115], a[96:99], a[88:91], v[112:115]\n" \
"v_add_u32 v214, s85, v212\n" \
"v_xor_b32 v215, 32, v214\n" \
"v_mfma_f32_16x16x32_bf16 v[112:115], a[100:103], a[92:95], v[112:115]\n" \
"s_waitcnt vmcnt(28) lgkmcnt(0)\n" \
"s_barrier\n" \
"v_mfma_f32_16x16x32_bf16 v[72:75], a[112:115], a[64:67], v[72:75]\n" \
"s_add_u32 m0, m0, 0x4000\n" \
"ds_read_b64_tr_b16 a[96:97], v214 offset:16384\n" \
"ds_read_b64_tr_b16 a[98:99], v214 offset:18432\n" \
"v_mfma_f32_16x16x32_bf16 v[72:75], a[116:119], a[68:71], v[72:75]\n" \
"buffer_load_dwordx4 v216, s[36:39], s82 offen offset:0 lds\n" \
"v_mfma_f32_16x16x32_bf16 v[76:79], a[120:123], a[64:67], v[76:79]\n" \
"ds_read_b64_tr_b16 a[100:101], v214 offset:24576\n" \
"ds_read_b64_tr_b16 a[102:103], v214 offset:26624\n" \
"v_mfma_f32_16x16x32_bf16 v[76:79], a[124:127], a[68:71], v[76:79]\n" \
"ds_read_b128 a[64:67], v213 offset:0\n" \
"v_mfma_f32_16x16x32_bf16 v[92:95], a[120:123], a[72:75], v[92:95]\n" \
"s_add_u32 s48, s48, 0x1000\n" \
"ds_read_b128 a[68:71], v213 offset:2048\n" \
"v_mfma_f32_16x16x32_bf16 v[92:95], a[124:127], a[76:79], v[92:95]\n" \
"buffer_load_dwordx4 v217, s[36:39], s82 offen offset:1024 lds\n" \
"v_mfma_f32_16x16x32_bf16 v[88:91], a[112:115], a[72:75], v[88:91]\n" \
"ds_read_b64_tr_b16 a[104:105], v215 offset:16384\n" \
"ds_read_b64_tr_b16 a[106:107], v215 offset:18432\n" \
"v_mfma_f32_16x16x32_bf16 v[88:91], a[116:119], a[76:79], v[88:91]\n" \
"ds_read_b128 a[72:75], v213 offset:256\n" \
"v_mfma_f32_16x16x32_bf16 v[104:107], a[112:115], a[80:83], v[104:107]\n" \
"s_sub_u32 s86, s86, 32768\n" \
"s_cselect_b32 s86, 131072, s86\n" \
"ds_read_b128 a[76:79], v213 offset:2304\n" \
"v_mfma_f32_16x16x32_bf16 v[104:107], a[116:119], a[84:87], v[104:107]\n" \
"buffer_load_dwordx4 v218, s[36:39], s82 offen offset:2048 lds\n" \
"v_mfma_f32_16x16x32_bf16 v[108:111], a[120:123], a[80:83], v[108:111]\n" \
"ds_read_b64_tr_b16 a[108:109], v215 offset:24576\n" \
"ds_read_b64_tr_b16 a[110:111], v215 offset:26624\n" \
"v_mfma_f32_16x16x32_bf16 v[108:111], a[124:127], a[84:87], v[108:111]\n" \
"buffer_load_dwordx4 v219, s[36:39], s82 offen offset:3072 lds\n" \
"v_mfma_f32_16x16x32_bf16 v[124:127], a[120:123], a[88:91], v[124:127]\n" \
"ds_read_b128 a[80:83], v213 offset:4096\n" \
"s_waitcnt lgkmcnt(4)\n" \
"v_mfma_f32_16x16x32_bf16 v[124:127], a[124:127], a[92:95], v[124:127]\n" \
"ds_read_b128 a[84:87], v213 offset:6144\n" \
"v_mfma_f32_16x16x32_bf16 v[120:123], a[112:115], a[88:91], v[120:123]\n" \
"v_mfma_f32_16x16x32_bf16 v[120:123], a[116:119], a[92:95], v[120:123]\n" \
"v_mfma_f32_16x16x32_bf16 v[64:67], a[96:99], a[64:67], v[64:67]\n" \
"ds_read_b128 a[88:91], v213 offset:4352\n" \
"v_mfma_f32_16x16x32_bf16 v[64:67], a[100:103], a[68:71], v[64:67]\n" \
"s_waitcnt lgkmcnt(2)\n" \
"s_xor_b32 s82, s48, s49\n" \
"ds_read_b128 a[92:95], v213 offset:6400\n" \
"v_mfma_f32_16x16x32_bf16 v[68:71], a[104:107], a[64:67], v[68:71]\n" \
"s_cmp_lt_u32 s48, s50\n" \
"s_cselect_b32 s82, s82, s48\n" \
"ds_read_b64_tr_b16 a[112:113], v214 offset:16896\n" \
"v_mfma_f32_16x16x32_bf16 v[68:71], a[108:111], a[68:71], v[68:71]\n" \
"s_add_u32 s81, s82, s46\n" \
"ds_read_b64_tr_b16 a[114:115], v214 offset:18944\n" \
"v_mfma_f32_16x16x32_bf16 v[84:87], a[104:107], a[72:75], v[84:87]\n" \
"s_add_u32 m0, s86, s79\n" \
"ds_read_b64_tr_b16 a[116:117], v214 offset:25088\n" \
"v_mfma_f32_16x16x32_bf16 v[84:87], a[108:111], a[76:79], v[84:87]\n" \
"buffer_load_dwordx4 v234, s[56:59], s81 offen offset:0 sc1 lds\n" \
"v_mfma_f32_16x16x32_bf16 v[80:83], a[96:99], a[72:75], v[80:83]\n" \
"s_sub_u32 s85, s85, 32768\n" \
"s_cselect_b32 s85, 131072, s85\n" \
"ds_read_b64_tr_b16 a[118:119], v214 offset:27136\n" \
"v_mfma_f32_16x16x32_bf16 v[80:83], a[100:103], a[76:79], v[80:83]\n" \
"s_waitcnt lgkmcnt(3)\n" \
"s_lshl_b32 s82, s82, 5\n" \
"ds_read_b64_tr_b16 a[120:121], v215 offset:16896\n" \
"v_mfma_f32_16x16x32_bf16 v[96:99], a[96:99], a[80:83], v[96:99]\n" \
"s_add_u32 s82, s82, s47\n" \
"ds_read_b64_tr_b16 a[122:123], v215 offset:18944\n" \
"v_mfma_f32_16x16x32_bf16 v[96:99], a[100:103], a[84:87], v[96:99]\n" \
"buffer_load_dwordx4 v234, s[56:59], s81 offen offset:1024 sc1 lds\n" \
"v_mfma_f32_16x16x32_bf16 v[100:103], a[104:107], a[80:83], v[100:103]\n" \
"ds_read_b64_tr_b16 a[124:125], v215 offset:25088\n" \
"v_add_u32 v213, s85, v211\n" \
"v_mfma_f32_16x16x32_bf16 v[100:103], a[108:111], a[84:87], v[100:103]\n" \
"buffer_load_dwordx4 v234, s[56:59], s81 offen offset:2048 sc1 lds\n" \
"v_mfma_f32_16x16x32_bf16 v[116:119], a[104:107], a[88:91], v[116:119]\n" \
"ds_read_b64_tr_b16 a[126:127], v215 offset:27136\n" \
"v_mfma_f32_16x16x32_bf16 v[116:119], a[108:111], a[92:95], v[116:119]\n" \
"buffer_load_dwordx4 v234, s[56:59], s81 offen offset:3072 sc1 lds\n" \
"v_mfma_f32_16x16x32_bf16 v[112:115], a[96:99], a[88:91], v[112:115]\n" \
"v_add_u32 v214, s85, v212\n" \
"v_xor_b32 v215, 32, v214\n" \
"v_mfma_f32_16x16x32_bf16 v[112:115], a[100:103], a[92:95], v[112:115]\n" \
"s_waitcnt vmcnt(25) lgkmcnt(0)\n" \
"s_barrier\n" \
"v_mfma_f32_16x16x32_bf16 v[72:75], a[112:115], a[64:67], v[72:75]\n" \
"s_add_u32 m0, m0, 0x4000\n" \
"ds_read_b64_tr_b16 a[96:97], v214 offset:16384\n" \
"ds_read_b64_tr_b16 a[98:99], v214 offset:18432\n" \
"v_mfma_f32_16x16x32_bf16 v[72:75], a[116:119], a[68:71], v[72:75]\n" \
"buffer_load_dwordx4 v216, s[36:39], s82 offen offset:0 lds\n" \
"v_mfma_f32_16x16x32_bf16 v[76:79], a[120:123], a[64:67], v[76:79]\n" \
"ds_read_b64_tr_b16 a[100:101], v214 offset:24576\n" \
"ds_read_b64_tr_b16 a[102:103], v214 offset:26624\n" \
"v_mfma_f32_16x16x32_bf16 v[76:79], a[124:127], a[68:71], v[76:79]\n" \
"ds_read_b128 a[64:67], v213 offset:0\n" \
"v_mfma_f32_16x16x32_bf16 v[92:95], a[120:123], a[72:75], v[92:95]\n" \
"s_add_u32 s48, s48, 0x1000\n" \
"ds_read_b128 a[68:71], v213 offset:2048\n" \
"v_mfma_f32_16x16x32_bf16 v[92:95], a[124:127], a[76:79], v[92:95]\n" \
"buffer_load_dwordx4 v217, s[36:39], s82 offen offset:1024 lds\n" \
"v_mfma_f32_16x16x32_bf16 v[88:91], a[112:115], a[72:75], v[88:91]\n" \
"ds_read_b64_tr_b16 a[104:105], v215 offset:16384\n" \
"ds_read_b64_tr_b16 a[106:107], v215 offset:18432\n" \
"v_mfma_f32_16x16x32_bf16 v[88:91], a[116:119], a[76:79], v[88:91]\n" \
"ds_read_b128 a[72:75], v213 offset:256\n" \
"v_mfma_f32_16x16x32_bf16 v[104:107], a[112:115], a[80:83], v[104:107]\n" \
"s_sub_u32 s86, s86, 32768\n" \
"s_cselect_b32 s86, 131072, s86\n" \
"ds_read_b128 a[76:79], v213 offset:2304\n" \
"v_mfma_f32_16x16x32_bf16 v[104:107], a[116:119], a[84:87], v[104:107]\n" \
"buffer_load_dwordx4 v218, s[36:39], s82 offen offset:2048 lds\n" \
"v_mfma_f32_16x16x32_bf16 v[108:111], a[120:123], a[80:83], v[108:111]\n" \
"ds_read_b64_tr_b16 a[108:109], v215 offset:24576\n" \
"ds_read_b64_tr_b16 a[110:111], v215 offset:26624\n" \
"v_mfma_f32_16x16x32_bf16 v[108:111], a[124:127], a[84:87], v[108:111]\n" \
"buffer_load_dwordx4 v219, s[36:39], s82 offen offset:3072 lds\n" \
"v_mfma_f32_16x16x32_bf16 v[124:127], a[120:123], a[88:91], v[124:127]\n" \
"ds_read_b128 a[80:83], v213 offset:4096\n" \
"s_waitcnt lgkmcnt(4)\n" \
"v_mfma_f32_16x16x32_bf16 v[124:127], a[124:127], a[92:95], v[124:127]\n" \
"ds_read_b128 a[84:87], v213 offset:6144\n" \
"v_mfma_f32_16x16x32_bf16 v[120:123], a[112:115], a[88:91], v[120:123]\n" \
"v_mfma_f32_16x16x32_bf16 v[120:123], a[116:119], a[92:95], v[120:123]\n" \
"v_mfma_f32_16x16x32_bf16 v[64:67], a[96:99], a[64:67], v[64:67]\n" \
"ds_read_b128 a[88:91], v213 offset:4352\n" \
"v_mfma_f32_16x16x32_bf16 v[64:67], a[100:103], a[68:71], v[64:67]\n" \
"s_waitcnt lgkmcnt(2)\n" \
"s_xor_b32 s82, s48, s49\n" \
"ds_read_b128 a[92:95], v213 offset:6400\n" \
"v_mfma_f32_16x16x32_bf16 v[68:71], a[104:107], a[64:67], v[68:71]\n" \
"s_cmp_lt_u32 s48, s50\n" \
"s_cselect_b32 s82, s82, s48\n" \
"ds_read_b64_tr_b16 a[112:113], v214 offset:16896\n" \
"v_mfma_f32_16x16x32_bf16 v[68:71], a[108:111], a[68:71], v[68:71]\n" \
"s_add_u32 s81, s82, s46\n" \
"ds_read_b64_tr_b16 a[114:115], v214 offset:18944\n" \
"v_mfma_f32_16x16x32_bf16 v[84:87], a[104:107], a[72:75], v[84:87]\n" \
"s_add_u32 m0, s86, s79\n" \
"ds_read_b64_tr_b16 a[116:117], v214 offset:25088\n" \
"v_mfma_f32_16x16x32_bf16 v[84:87], a[108:111], a[76:79], v[84:87]\n" \
"buffer_load_dwordx4 v234, s[56:59], s81 offen offset:0 sc1 lds\n" \
"v_mfma_f32_16x16x32_bf16 v[80:83], a[96:99], a[72:75], v[80:83]\n" \
"s_sub_u32 s85, s85, 32768\n" \
"s_cselect_b32 s85, 131072, s85\n" \
"ds_read_b64_tr_b16 a[118:119], v214 offset:27136\n" \
"v_mfma_f32_16x16x32_bf16 v[80:83], a[100:103], a[76:79], v[80:83]\n" \
"s_waitcnt lgkmcnt(3)\n" \
"s_lshl_b32 s82, s82, 5\n" \
"ds_read_b64_tr_b16 a[120:121], v215 offset:16896\n" \
"v_mfma_f32_16x16x32_bf16 v[96:99], a[96:99], a[80:83], v[96:99]\n" \
"s_add_u32 s82, s82, s47\n" \
"ds_read_b64_tr_b16 a[122:123], v215 offset:18944\n" \
"v_mfma_f32_16x16x32_bf16 v[96:99], a[100:103], a[84:87], v[96:99]\n" \
"buffer_load_dwordx4 v234, s[56:59], s81 offen offset:1024 sc1 lds\n" \
"v_mfma_f32_16x16x32_bf16 v[100:103], a[104:107], a[80:83], v[100:103]\n" \
"ds_read_b64_tr_b16 a[124:125], v215 offset:25088\n" \
"v_add_u32 v213, s85, v211\n" \
"v_mfma_f32_16x16x32_bf16 v[100:103], a[108:111], a[84:87], v[100:103]\n" \
"buffer_load_dwordx4 v234, s[56:59], s81 offen offset:2048 sc1 lds\n" \
"v_mfma_f32_16x16x32_bf16 v[116:119], a[104:107], a[88:91], v[116:119]\n" \
"ds_read_b64_tr_b16 a[126:127], v215 offset:27136\n" \
"v_mfma_f32_16x16x32_bf16 v[116:119], a[108:111], a[92:95], v[116:119]\n" \
"buffer_load_dwordx4 v234, s[56:59], s81 offen offset:3072 sc1 lds\n" \
"v_mfma_f32_16x16x32_bf16 v[112:115], a[96:99], a[88:91], v[112:115]\n" \
"v_add_u32 v214, s85, v212\n" \
"v_xor_b32 v215, 32, v214\n" \
"v_mfma_f32_16x16x32_bf16 v[112:115], a[100:103], a[92:95], v[112:115]\n" \
"s_waitcnt vmcnt(20) lgkmcnt(0)\n" \
"s_barrier\n" \
"buffer_store_dword v230, v231, s[56:59], s64 offen sc1\n" \
"v_mfma_f32_16x16x32_bf16 v[72:75], a[112:115], a[64:67], v[72:75]\n" \
"s_add_u32 m0, m0, 0x4000\n" \
"ds_read_b64_tr_b16 a[96:97], v214 offset:16384\n" \
"ds_read_b64_tr_b16 a[98:99], v214 offset:18432\n" \
"v_mfma_f32_16x16x32_bf16 v[72:75], a[116:119], a[68:71], v[72:75]\n" \
"buffer_load_dwordx4 v216, s[36:39], s82 offen offset:0 lds\n" \
"v_mfma_f32_16x16x32_bf16 v[76:79], a[120:123], a[64:67], v[76:79]\n" \
"ds_read_b64_tr_b16 a[100:101], v214 offset:24576\n" \
"ds_read_b64_tr_b16 a[102:103], v214 offset:26624\n" \
"v_mfma_f32_16x16x32_bf16 v[76:79], a[124:127], a[68:71], v[76:79]\n" \
"ds_read_b128 a[64:67], v213 offset:0\n" \
"v_mfma_f32_16x16x32_bf16 v[92:95], a[120:123], a[72:75], v[92:95]\n" \
"s_add_u32 s48, s48, 0x1000\n" \
"ds_read_b128 a[68:71], v213 offset:2048\n" \
"v_mfma_f32_16x16x32_bf16 v[92:95], a[124:127], a[76:79], v[92:95]\n" \
"buffer_load_dwordx4 v217, s[36:39], s82 offen offset:1024 lds\n" \
"v_mfma_f32_16x16x32_bf16 v[88:91], a[112:115], a[72:75], v[88:91]\n" \
"ds_read_b64_tr_b16 a[104:105], v215 offset:16384\n" \
"ds_read_b64_tr_b16 a[106:107], v215 offset:18432\n" \
"v_mfma_f32_16x16x32_bf16 v[88:91], a[116:119], a[76:79], v[88:91]\n" \
"ds_read_b128 a[72:75], v213 offset:256\n" \
"v_mfma_f32_16x16x32_bf16 v[104:107], a[112:115], a[80:83], v[104:107]\n" \
"s_sub_u32 s86, s86, 32768\n" \
"s_cselect_b32 s86, 131072, s86\n" \
"ds_read_b128 a[76:79], v213 offset:2304\n" \
"v_mfma_f32_16x16x32_bf16 v[104:107], a[116:119], a[84:87], v[104:107]\n" \
"buffer_load_dwordx4 v218, s[36:39], s82 offen offset:2048 lds\n" \
"v_mfma_f32_16x16x32_bf16 v[108:111], a[120:123], a[80:83], v[108:111]\n" \
"ds_read_b64_tr_b16 a[108:109], v215 offset:24576\n" \
"ds_read_b64_tr_b16 a[110:111], v215 offset:26624\n" \
"v_mfma_f32_16x16x32_bf16 v[108:111], a[124:127], a[84:87], v[108:111]\n" \
"buffer_load_dwordx4 v219, s[36:39], s82 offen offset:3072 lds\n" \
"v_mfma_f32_16x16x32_bf16 v[124:127], a[120:123], a[88:91], v[124:127]\n" \
"ds_read_b128 a[80:83], v213 offset:4096\n" \
"s_waitcnt lgkmcnt(4)\n" \
"v_mfma_f32_16x16x32_bf16 v[124:127], a[124:127], a[92:95], v[124:127]\n" \
"ds_read_b128 a[84:87], v213 offset:6144\n" \
"v_mfma_f32_16x16x32_bf16 v[120:123], a[112:115], a[88:91], v[120:123]\n" \
"v_mfma_f32_16x16x32_bf16 v[120:123], a[116:119], a[92:95], v[120:123]\n" \
"s_mov_b32 s72, 5\n" \
"s_nop 0\n" \
"s_nop 0\n" \
"Lo64_%=:\n" \
"v_mfma_f32_16x16x32_bf16 v[64:67], a[96:99], a[64:67], v[64:67]\n" \
"ds_read_b128 a[88:91], v213 offset:4352\n" \
"v_mfma_f32_16x16x32_bf16 v[64:67], a[100:103], a[68:71], v[64:67]\n" \
"Lpg11466_%=:\n" \
"s_cbranch_execz Lcb1_%=\n" \
"s_waitcnt lgkmcnt(2)\n" \
"s_xor_b32 s82, s48, s49\n" \
"ds_read_b128 a[92:95], v213 offset:6400\n" \
"v_mfma_f32_16x16x32_bf16 v[68:71], a[104:107], a[64:67], v[68:71]\n" \
"s_cmp_lt_u32 s48, s50\n" \
"s_cselect_b32 s82, s82, s48\n" \
"ds_read_b64_tr_b16 a[112:113], v214 offset:16896\n" \
"v_mfma_f32_16x16x32_bf16 v[68:71], a[108:111], a[68:71], v[68:71]\n" \
"s_add_u32 s81, s82, s46\n" \
"ds_read_b64_tr_b16 a[114:115], v214 offset:18944\n" \
"v_mfma_f32_16x16x32_bf16 v[84:87], a[104:107], a[72:75], v[84:87]\n" \
"s_add_u32 m0, s86, s79\n" \
"ds_read_b64_tr_b16 a[116:117], v214 offset:25088\n" \
"v_mfma_f32_16x16x32_bf16 v[84:87], a[108:111], a[76:79], v[84:87]\n" \
"buffer_load_dwordx4 v234, s[56:59], s81 offen offset:0 sc1 lds\n" \
"v_mfma_f32_16x16x32_bf16 v[80:83], a[96:99], a[72:75], v[80:83]\n" \
"s_sub_u32 s85, s85, 32768\n" \
"s_cselect_b32 s85, 131072, s85\n" \
"ds_read_b64_tr_b16 a[118:119], v214 offset:27136\n" \
"v_mfma_f32_16x16x32_bf16 v[80:83], a[100:103], a[76:79], v[80:83]\n" \
"s_waitcnt lgkmcnt(3)\n" \
"s_lshl_b32 s82, s82, 5\n" \
"ds_read_b64_tr_b16 a[120:121], v215 offset:16896\n" \
"v_mfma_f32_16x16x32_bf16 v[96:99], a[96:99], a[80:83], v[96:99]\n" \
"s_add_u32 s82, s82, s47\n" \
"ds_read_b64_tr_b16 a[122:123], v215 offset:18944\n" \
"v_mfma_f32_16x16x32_bf16 v[96:99], a[100:103], a[84:87], v[96:99]\n" \
"buffer_load_dwordx4 v234, s[56:59], s81 offen offset:1024 sc1 lds\n" \
"v_mfma_f32_16x16x32_bf16 v[100:103], a[104:107], a[80:83], v[100:103]\n" \
"ds_read_b64_tr_b16 a[124:125], v215 offset:25088\n" \
"v_add_u32 v213, s85, v211\n" \
"v_mfma_f32_16x16x32_bf16 v[100:103], a[108:111], a[84:87], v[100:103]\n" \
"buffer_load_dwordx4 v234, s[56:59], s81 offen offset:2048 sc1 lds\n" \
"v_mfma_f32_16x16x32_bf16 v[116:119], a[104:107], a[88:91], v[116:119]\n" \
"ds_read_b64_tr_b16 a[126:127], v215 offset:27136\n" \
"v_mfma_f32_16x16x32_bf16 v[116:119], a[108:111], a[92:95], v[116:119]\n" \
"buffer_load_dwordx4 v234, s[56:59], s81 offen offset:3072 sc1 lds\n" \
"v_mfma_f32_16x16x32_bf16 v[112:115], a[96:99], a[88:91], v[112:115]\n" \
"v_add_u32 v214, s85, v212\n" \
"v_xor_b32 v215, 32, v214\n" \
"v_mfma_f32_16x16x32_bf16 v[112:115], a[100:103], a[92:95], v[112:115]\n" \
"s_waitcnt vmcnt(20) lgkmcnt(0)\n" \
"s_barrier\n" \
"v_mfma_f32_16x16x32_bf16 v[72:75], a[112:115], a[64:67], v[72:75]\n" \
"s_add_u32 m0, m0, 0x4000\n" \
"ds_read_b64_tr_b16 a[96:97], v214 offset:16384\n" \
"ds_read_b64_tr_b16 a[98:99], v214 offset:18432\n" \
"v_mfma_f32_16x16x32_bf16 v[72:75], a[116:119], a[68:71], v[72:75]\n" \
"buffer_load_dwordx4 v216, s[36:39], s82 offen offset:0 lds\n" \
"v_mfma_f32_16x16x32_bf16 v[76:79], a[120:123], a[64:67], v[76:79]\n" \
"ds_read_b64_tr_b16 a[100:101], v214 offset:24576\n" \
"ds_read_b64_tr_b16 a[102:103], v214 offset:26624\n" \
"v_mfma_f32_16x16x32_bf16 v[76:79], a[124:127], a[68:71], v[76:79]\n" \
"ds_read_b128 a[64:67], v213 offset:0\n" \
"v_mfma_f32_16x16x32_bf16 v[92:95], a[120:123], a[72:75], v[92:95]\n" \
"s_add_u32 s48, s48, 0x1000\n" \
"ds_read_b128 a[68:71], v213 offset:2048\n" \
"v_mfma_f32_16x16x32_bf16 v[92:95], a[124:127], a[76:79], v[92:95]\n" \
"buffer_load_dwordx4 v217, s[36:39], s82 offen offset:1024 lds\n" \
"v_mfma_f32_16x16x32_bf16 v[88:91], a[112:115], a[72:75], v[88:91]\n" \
"ds_read_b64_tr_b16 a[104:105], v215 offset:16384\n" \
"ds_read_b64_tr_b16 a[106:107], v215 offset:18432\n" \
"v_mfma_f32_16x16x32_bf16 v[88:91], a[116:119], a[76:79], v[88:91]\n" \
"ds_read_b128 a[72:75], v213 offset:256\n" \
"v_mfma_f32_16x16x32_bf16 v[104:107], a[112:115], a[80:83], v[104:107]\n" \
"s_sub_u32 s86, s86, 32768\n" \
"s_cselect_b32 s86, 131072, s86\n" \
"ds_read_b128 a[76:79], v213 offset:2304\n" \
"v_mfma_f32_16x16x32_bf16 v[104:107], a[116:119], a[84:87], v[104:107]\n" \
"buffer_load_dwordx4 v218, s[36:39], s82 offen offset:2048 lds\n" \
"v_mfma_f32_16x16x32_bf16 v[108:111], a[120:123], a[80:83], v[108:111]\n" \
"ds_read_b64_tr_b16 a[108:109], v215 offset:24576\n" \
"ds_read_b64_tr_b16 a[110:111], v215 offset:26624\n" \
"v_mfma_f32_16x16x32_bf16 v[108:111], a[124:127], a[84:87], v[108:111]\n" \
"buffer_load_dwordx4 v219, s[36:39], s82 offen offset:3072 lds\n" \
"v_mfma_f32_16x16x32_bf16 v[124:127], a[120:123], a[88:91], v[124:127]\n" \
"ds_read_b128 a[80:83], v213 offset:4096\n" \
"s_waitcnt lgkmcnt(4)\n" \
"v_mfma_f32_16x16x32_bf16 v[124:127], a[124:127], a[92:95], v[124:127]\n" \
"s_sub_u32 s72, s72, 1\n" \
"ds_read_b128 a[84:87], v213 offset:6144\n" \
"v_mfma_f32_16x16x32_bf16 v[120:123], a[112:115], a[88:91], v[120:123]\n" \
"v_mfma_f32_16x16x32_bf16 v[120:123], a[116:119], a[92:95], v[120:123]\n" \
"s_cbranch_scc0 Lo64_%=\n" \
"v_mfma_f32_16x16x32_bf16 v[64:67], a[96:99], a[64:67], v[64:67]\n" \
"s_waitcnt lgkmcnt(2)\n" \
"ds_read_b128 a[88:91], v213 offset:4352\n" \
"v_mfma_f32_16x16x32_bf16 v[64:67], a[100:103], a[68:71], v[64:67]\n" \
"s_add_u32 s48, s86, 32768\n" \
"s_cmp_eq_u32 s48, 163840\n" \
"s_cselect_b32 s48, 0, s48\n" \
"ds_read_b128 a[92:95], v213 offset:6400\n" \
"v_mfma_f32_16x16x32_bf16 v[68:71], a[104:107], a[64:67], v[68:71]\n" \
"ds_read_b64_tr_b16 a[112:113], v214 offset:16896\n" \
"v_mfma_f32_16x16x32_bf16 v[68:71], a[108:111], a[68:71], v[68:71]\n" \
"s_add_u32 s49, s48, 32768\n" \
"s_cmp_eq_u32 s49, 163840\n" \
"s_cselect_b32 s49, 0, s49\n" \
"ds_read_b64_tr_b16 a[114:115], v214 offset:18944\n" \
"v_mfma_f32_16x16x32_bf16 v[84:87], a[104:107], a[72:75], v[84:87]\n" \
"ds_read_b64_tr_b16 a[116:117], v214 offset:25088\n" \
"v_add_u32 v224, s49, v211\n" \
"v_mfma_f32_16x16x32_bf16 v[84:87], a[108:111], a[76:79], v[84:87]\n" \
"v_add_u32 v225, s49, v212\n" \
"v_xor_b32 v226, 32, v225\n" \
"v_mfma_f32_16x16x32_bf16 v[80:83], a[96:99], a[72:75], v[80:83]\n" \
"s_sub_u32 s85, s85, 32768\n" \
"s_cselect_b32 s85, 131072, s85\n" \
"ds_read_b64_tr_b16 a[118:119], v214 offset:27136\n" \
"v_mfma_f32_16x16x32_bf16 v[80:83], a[100:103], a[76:79], v[80:83]\n" \
"s_waitcnt lgkmcnt(3)\n" \
"ds_read_b64_tr_b16 a[120:121], v215 offset:16896\n" \
"v_add_u32 v227, s48, v211\n" \
"v_mfma_f32_16x16x32_bf16 v[96:99], a[96:99], a[80:83], v[96:99]\n" \
"ds_read_b64_tr_b16 a[122:123], v215 offset:18944\n" \
"v_add_u32 v228, s48, v212\n" \
"v_mfma_f32_16x16x32_bf16 v[96:99], a[100:103], a[84:87], v[96:99]\n" \
"v_xor_b32 v229, 32, v228\n" \
"v_mfma_f32_16x16x32_bf16 v[100:103], a[104:107], a[80:83], v[100:103]\n" \
"ds_read_b64_tr_b16 a[124:125], v215 offset:25088\n" \
"v_add_u32 v213, s85, v211\n" \
"v_mfma_f32_16x16x32_bf16 v[100:103], a[108:111], a[84:87], v[100:103]\n" \
"v_mfma_f32_16x16x32_bf16 v[116:119], a[104:107], a[88:91], v[116:119]\n" \
"ds_read_b64_tr_b16 a[126:127], v215 offset:27136\n" \
"v_mfma_f32_16x16x32_bf16 v[116:119], a[108:111], a[92:95], v[116:119]\n" \
"v_mfma_f32_16x16x32_bf16 v[112:115], a[96:99], a[88:91], v[112:115]\n" \
"v_add_u32 v214, s85, v212\n" \
"v_xor_b32 v215, 32, v214\n" \
"v_mfma_f32_16x16x32_bf16 v[112:115], a[100:103], a[92:95], v[112:115]\n" \
"s_waitcnt vmcnt(8) lgkmcnt(0)\n" \
"s_barrier\n" \
"v_mfma_f32_16x16x32_bf16 v[72:75], a[112:115], a[64:67], v[72:75]\n" \
"ds_read_b128 a[0:3], v213 offset:0\n" \
"ds_read_b128 a[4:7], v213 offset:2048\n" \
"v_mfma_f32_16x16x32_bf16 v[72:75], a[116:119], a[68:71], v[72:75]\n" \
"s_sub_u32 s68, s86, 32768\n" \
"s_cselect_b32 s68, 131072, s68\n" \
"ds_read_b64_tr_b16 a[128:129], v214 offset:16384\n" \
"ds_read_b64_tr_b16 a[130:131], v214 offset:18432\n" \
"v_mfma_f32_16x16x32_bf16 v[76:79], a[120:123], a[64:67], v[76:79]\n" \
"ds_read_b64_tr_b16 a[132:133], v214 offset:24576\n" \
"ds_read_b64_tr_b16 a[134:135], v214 offset:26624\n" \
"v_mfma_f32_16x16x32_bf16 v[76:79], a[124:127], a[68:71], v[76:79]\n" \
"s_cmp_lt_u32 s61, 2\n" \
"s_cselect_b32 s68, s86, s68\n" \
"ds_read_b64_tr_b16 a[136:137], v215 offset:16384\n" \
"ds_read_b64_tr_b16 a[138:139], v215 offset:18432\n" \
"v_mfma_f32_16x16x32_bf16 v[92:95], a[120:123], a[72:75], v[92:95]\n" \
"s_and_b32 s50, s61, 1\n" \
"ds_read_b64_tr_b16 a[140:141], v215 offset:24576\n" \
"ds_read_b64_tr_b16 a[142:143], v215 offset:26624\n" \
"v_mfma_f32_16x16x32_bf16 v[92:95], a[124:127], a[76:79], v[92:95]\n" \
"s_mul_i32 s50, s50, 9216\n" \
"ds_read_b64_tr_b16 a[144:145], v214 offset:16896\n" \
"ds_read_b64_tr_b16 a[146:147], v214 offset:18944\n" \
"v_mfma_f32_16x16x32_bf16 v[88:91], a[112:115], a[72:75], v[88:91]\n" \
"s_add_u32 s68, s68, s50\n" \
"ds_read_b64_tr_b16 a[148:149], v214 offset:25088\n" \
"ds_read_b64_tr_b16 a[150:151], v214 offset:27136\n" \
"v_mfma_f32_16x16x32_bf16 v[88:91], a[116:119], a[76:79], v[88:91]\n" \
"s_add_u32 s68, s68, %3\n" \
"ds_read_b64_tr_b16 a[152:153], v215 offset:16896\n" \
"ds_read_b64_tr_b16 a[154:155], v215 offset:18944\n" \
"v_mfma_f32_16x16x32_bf16 v[104:107], a[112:115], a[80:83], v[104:107]\n" \
"ds_read_b64_tr_b16 a[156:157], v215 offset:25088\n" \
"ds_read_b64_tr_b16 a[158:159], v215 offset:27136\n" \
"v_mfma_f32_16x16x32_bf16 v[104:107], a[116:119], a[84:87], v[104:107]\n" \
"ds_read_b128 a[24:27], v213 offset:256\n" \
"ds_read_b128 a[28:31], v213 offset:2304\n" \
"v_mfma_f32_16x16x32_bf16 v[108:111], a[120:123], a[80:83], v[108:111]\n" \
"ds_read_b128 a[8:11], v224 offset:0\n" \
"ds_read_b128 a[12:15], v224 offset:2048\n" \
"v_mfma_f32_16x16x32_bf16 v[108:111], a[124:127], a[84:87], v[108:111]\n" \
"ds_read_b64_tr_b16 a[160:161], v225 offset:16384\n" \
"ds_read_b64_tr_b16 a[162:163], v225 offset:18432\n" \
"v_mfma_f32_16x16x32_bf16 v[124:127], a[120:123], a[88:91], v[124:127]\n" \
"ds_read_b64_tr_b16 a[164:165], v225 offset:24576\n" \
"ds_read_b64_tr_b16 a[166:167], v225 offset:26624\n" \
"v_mfma_f32_16x16x32_bf16 v[124:127], a[124:127], a[92:95], v[124:127]\n" \
"ds_read_b64_tr_b16 a[168:169], v226 offset:16384\n" \
"ds_read_b64_tr_b16 a[170:171], v226 offset:18432\n" \
"v_mfma_f32_16x16x32_bf16 v[120:123], a[112:115], a[88:91], v[120:123]\n" \
"ds_read_b64_tr_b16 a[172:173], v226 offset:24576\n" \
"ds_read_b64_tr_b16 a[174:175], v226 offset:26624\n" \
"v_mfma_f32_16x16x32_bf16 v[120:123], a[116:119], a[92:95], v[120:123]\n" \
"s_waitcnt lgkmcnt(8)\n" \
"ds_read_b64_tr_b16 a[176:177], v225 offset:16896\n" \
"ds_read_b64_tr_b16 a[178:179], v225 offset:18944\n" \
"v_mfma_f32_16x16x32_bf16 v[64:67], a[128:131], a[0:3], v[64:67]\n" \
"ds_read_b64_tr_b16 a[180:181], v225 offset:25088\n" \
"ds_read_b64_tr_b16 a[182:183], v225 offset:27136\n" \
"v_mfma_f32_16x16x32_bf16 v[64:67], a[132:135], a[4:7], v[64:67]\n" \
"ds_read_b64_tr_b16 a[184:185], v226 offset:16896\n" \
"ds_read_b64_tr_b16 a[186:187], v226 offset:18944\n" \
"v_mfma_f32_16x16x32_bf16 v[68:71], a[136:139], a[0:3], v[68:71]\n" \
"ds_read_b64_tr_b16 a[188:189], v226 offset:25088\n" \
"ds_read_b64_tr_b16 a[190:191], v226 offset:27136\n" \
"v_mfma_f32_16x16x32_bf16 v[68:71], a[140:143], a[4:7], v[68:71]\n" \
"v_lshrrev_b32 v132, 4, v234\n" \
"v_and_b32 v133, 15, v132\n" \
"v_mfma_f32_16x16x32_bf16 v[72:75], a[144:147], a[0:3], v[72:75]\n" \
"v_mul_u32_u24 v128, 144, v133\n" \
"v_lshrrev_b32 v133, 4, v132\n" \
"v_mfma_f32_16x16x32_bf16 v[72:75], a[148:151], a[4:7], v[72:75]\n" \
"v_lshl_add_u32 v128, v133, 3, v128\n" \
"v_add_u32 v128, s68, v128\n" \
"v_mfma_f32_16x16x32_bf16 v[76:79], a[152:155], a[0:3], v[76:79]\n" \
"v_lshrrev_b32 v133, 3, v132\n" \
"v_and_b32 v134, 7, v132\n" \
"v_mfma_f32_16x16x32_bf16 v[76:79], a[156:159], a[4:7], v[76:79]\n" \
"v_mul_u32_u24 v129, 144, v133\n" \
"v_lshl_add_u32 v129, v134, 4, v129\n" \
"v_mfma_f32_16x16x32_bf16 v[92:95], a[152:155], a[24:27], v[92:95]\n" \
"v_add_u32 v129, s68, v129\n" \
"v_lshlrev_b32 v134, 4, v134\n" \
"v_mfma_f32_16x16x32_bf16 v[92:95], a[156:159], a[28:31], v[92:95]\n" \
"v_lshl_add_u32 v130, v133, 13, v134\n" \
"v_mfma_f32_16x16x32_bf16 v[88:91], a[144:147], a[24:27], v[88:91]\n" \
"v_mfma_f32_16x16x32_bf16 v[88:91], a[148:151], a[28:31], v[88:91]\n" \
"v_mfma_f32_16x16x32_bf16 v[84:87], a[136:139], a[24:27], v[84:87]\n" \
"s_waitcnt vmcnt(0)\n" \
"s_barrier\n" \
"v_mfma_f32_16x16x32_bf16 v[84:87], a[140:143], a[28:31], v[84:87]\n" \
"ds_read_b128 a[32:35], v224 offset:256\n" \
"ds_read_b128 a[36:39], v224 offset:2304\n" \
"v_mfma_f32_16x16x32_bf16 v[80:83], a[128:131], a[24:27], v[80:83]\n" \
"ds_read_b128 a[16:19], v227 offset:0\n" \
"ds_read_b128 a[20:23], v227 offset:2048\n" \
"v_mfma_f32_16x16x32_bf16 v[80:83], a[132:135], a[28:31], v[80:83]\n" \
"s_waitcnt lgkmcnt(4)\n" \
"ds_read_b64_tr_b16 a[192:193], v228 offset:16384\n" \
"ds_read_b64_tr_b16 a[194:195], v228 offset:18432\n" \
"v_mfma_f32_16x16x32_bf16 v[64:67], a[160:163], a[8:11], v[64:67]\n" \
"ds_read_b64_tr_b16 a[196:197], v228 offset:24576\n" \
"ds_read_b64_tr_b16 a[198:199], v228 offset:26624\n" \
"v_mfma_f32_16x16x32_bf16 v[64:67], a[164:167], a[12:15], v[64:67]\n" \
"ds_read_b64_tr_b16 a[200:201], v229 offset:16384\n" \
"ds_read_b64_tr_b16 a[202:203], v229 offset:18432\n" \
"v_mfma_f32_16x16x32_bf16 v[68:71], a[168:171], a[8:11], v[68:71]\n" \
"ds_read_b64_tr_b16 a[204:205], v229 offset:24576\n" \
"ds_read_b64_tr_b16 a[206:207], v229 offset:26624\n" \
"v_mfma_f32_16x16x32_bf16 v[68:71], a[172:175], a[12:15], v[68:71]\n" \
"ds_read_b64_tr_b16 a[208:209], v228 offset:16896\n" \
"ds_read_b64_tr_b16 a[210:211], v228 offset:18944\n" \
"v_mfma_f32_16x16x32_bf16 v[72:75], a[176:179], a[8:11], v[72:75]\n" \
"ds_read_b64_tr_b16 a[212:213], v228 offset:25088\n" \
"ds_read_b64_tr_b16 a[214:215], v228 offset:27136\n" \
"v_mfma_f32_16x16x32_bf16 v[72:75], a[180:183], a[12:15], v[72:75]\n" \
"ds_read_b64_tr_b16 a[216:217], v229 offset:16896\n" \
"ds_read_b64_tr_b16 a[218:219], v229 offset:18944\n" \
"v_mfma_f32_16x16x32_bf16 v[76:79], a[184:187], a[8:11], v[76:79]\n" \
"ds_read_b64_tr_b16 a[220:221], v229 offset:25088\n" \
"ds_read_b64_tr_b16 a[222:223], v229 offset:27136\n" \
"v_mfma_f32_16x16x32_bf16 v[76:79], a[188:191], a[12:15], v[76:79]\n" \
"s_waitcnt lgkmcnt(8)\n" \
"ds_read_b128 a[40:43], v227 offset:256\n" \
"ds_read_b128 a[44:47], v227 offset:2304\n" \
"v_mfma_f32_16x16x32_bf16 v[92:95], a[184:187], a[32:35], v[92:95]\n" \
"ds_read_b128 a[248:251], v213 offset:4096\n" \
"ds_read_b128 a[252:255], v213 offset:6144\n" \
"v_mfma_f32_16x16x32_bf16 v[92:95], a[188:191], a[36:39], v[92:95]\n" \
"ds_read_b128 a[48:51], v224 offset:4096\n" \
"ds_read_b128 a[52:55], v224 offset:6144\n" \
"v_mfma_f32_16x16x32_bf16 v[88:91], a[176:179], a[32:35], v[88:91]\n" \
"ds_read_b128 a[56:59], v227 offset:4096\n" \
"ds_read_b128 a[60:63], v227 offset:6144\n" \
"v_mfma_f32_16x16x32_bf16 v[88:91], a[180:183], a[36:39], v[88:91]\n" \
"ds_read_b128 a[224:227], v213 offset:4352\n" \
"ds_read_b128 a[228:231], v213 offset:6400\n" \
"v_mfma_f32_16x16x32_bf16 v[84:87], a[168:171], a[32:35], v[84:87]\n" \
"ds_read_b128 a[232:235], v224 offset:4352\n" \
"ds_read_b128 a[236:239], v224 offset:6400\n" \
"v_mfma_f32_16x16x32_bf16 v[84:87], a[172:175], a[36:39], v[84:87]\n" \
"ds_read_b128 a[240:243], v227 offset:4352\n" \
"ds_read_b128 a[244:247], v227 offset:6400\n" \
"v_mfma_f32_16x16x32_bf16 v[80:83], a[160:163], a[32:35], v[80:83]\n" \
"v_mfma_f32_16x16x32_bf16 v[80:83], a[164:167], a[36:39], v[80:83]\n" \
"v_mfma_f32_16x16x32_bf16 v[64:67], a[192:195], a[16:19], v[64:67]\n" \
"v_mfma_f32_16x16x32_bf16 v[64:67], a[196:199], a[20:23], v[64:67]\n" \
"v_mfma_f32_16x16x32_bf16 v[68:71], a[200:203], a[16:19], v[68:71]\n" \
"v_mfma_f32_16x16x32_bf16 v[68:71], a[204:207], a[20:23], v[68:71]\n" \
"s_waitcnt lgkmcnt(0)\n" \
"v_mfma_f32_16x16x32_bf16 v[72:75], a[208:211], a[16:19], v[72:75]\n" \
"v_mfma_f32_16x16x32_bf16 v[72:75], a[212:215], a[20:23], v[72:75]\n" \
"v_cvt_pk_bf16_f32 v136, v64, v65\n" \
"v_cvt_pk_bf16_f32 v137, v66, v67\n" \
"v_mfma_f32_16x16x32_bf16 v[76:79], a[216:219], a[16:19], v[76:79]\n" \
"v_mfma_f32_16x16x32_bf16 v[76:79], a[220:223], a[20:23], v[76:79]\n" \
"ds_write_b64 v128, v[136:137] offset:0\n" \
"v_mfma_f32_16x16x32_bf16 v[80:83], a[192:195], a[40:43], v[80:83]\n" \
"v_cvt_pk_bf16_f32 v138, v68, v69\n" \
"v_cvt_pk_bf16_f32 v139, v70, v71\n" \
"v_mfma_f32_16x16x32_bf16 v[80:83], a[196:199], a[44:47], v[80:83]\n" \
"ds_write_b64 v128, v[138:139] offset:32\n" \
"v_mfma_f32_16x16x32_bf16 v[84:87], a[200:203], a[40:43], v[84:87]\n" \
"v_cvt_pk_bf16_f32 v136, v72, v73\n" \
"v_cvt_pk_bf16_f32 v137, v74, v75\n" \
"v_mfma_f32_16x16x32_bf16 v[84:87], a[204:207], a[44:47], v[84:87]\n" \
"ds_write_b64 v128, v[136:137] offset:64\n" \
"v_mfma_f32_16x16x32_bf16 v[88:91], a[208:211], a[40:43], v[88:91]\n" \
"v_cvt_pk_bf16_f32 v138, v76, v77\n" \
"v_cvt_pk_bf16_f32 v139, v78, v79\n" \
"v_mfma_f32_16x16x32_bf16 v[88:91], a[212:215], a[44:47], v[88:91]\n" \
"ds_write_b64 v128, v[138:139] offset:96\n" \
"v_mfma_f32_16x16x32_bf16 v[92:95], a[216:219], a[40:43], v[92:95]\n" \
"ds_read_b128 v[140:143], v129 offset:0\n" \
"ds_read_b128 v[144:147], v129 offset:1152\n" \
"v_mfma_f32_16x16x32_bf16 v[92:95], a[220:223], a[44:47], v[92:95]\n" \
"v_cvt_pk_bf16_f32 v136, v80, v81\n" \
"v_cvt_pk_bf16_f32 v137, v82, v83\n" \
"v_mfma_f32_16x16x32_bf16 v[96:99], a[128:131], a[248:251], v[96:99]\n" \
"ds_write_b64 v128, v[136:137] offset:2304\n" \
"v_mfma_f32_16x16x32_bf16 v[96:99], a[132:135], a[252:255], v[96:99]\n" \
"s_add_u32 s70, s88, 0\n" \
"s_waitcnt lgkmcnt(2)\n" \
"v_cvt_pk_bf16_f32 v138, v84, v85\n" \
"v_mfma_f32_16x16x32_bf16 v[96:99], a[160:163], a[48:51], v[96:99]\n" \
"buffer_store_dwordx4 v[140:143], v130, s[52:55], s70 offen sc1\n" \
"v_mfma_f32_16x16x32_bf16 v[96:99], a[164:167], a[52:55], v[96:99]\n" \
"v_cvt_pk_bf16_f32 v139, v86, v87\n" \
"v_mfma_f32_16x16x32_bf16 v[96:99], a[192:195], a[56:59], v[96:99]\n" \
"s_add_u32 s70, s88, 65536\n" \
"s_waitcnt lgkmcnt(1)\n" \
"v_mfma_f32_16x16x32_bf16 v[96:99], a[196:199], a[60:63], v[96:99]\n" \
"buffer_store_dwordx4 v[144:147], v130, s[52:55], s70 offen sc1\n" \
"ds_write_b64 v128, v[138:139] offset:2336\n" \
"v_mfma_f32_16x16x32_bf16 v[100:103], a[204:207], a[60:63], v[100:103]\n" \
"v_cvt_pk_bf16_f32 v136, v88, v89\n" \
"v_cvt_pk_bf16_f32 v137, v90, v91\n" \
"v_mfma_f32_16x16x32_bf16 v[100:103], a[200:203], a[56:59], v[100:103]\n" \
"ds_write_b64 v128, v[136:137] offset:2368\n" \
"v_mfma_f32_16x16x32_bf16 v[100:103], a[172:175], a[52:55], v[100:103]\n" \
"v_cvt_pk_bf16_f32 v138, v92, v93\n" \
"v_cvt_pk_bf16_f32 v139, v94, v95\n" \
"v_mfma_f32_16x16x32_bf16 v[100:103], a[168:171], a[48:51], v[100:103]\n" \
"v_mfma_f32_16x16x32_bf16 v[100:103], a[140:143], a[252:255], v[100:103]\n" \
"v_mfma_f32_16x16x32_bf16 v[100:103], a[136:139], a[248:251], v[100:103]\n" \
"ds_write_b64 v128, v[138:139] offset:2400\n" \
"v_mfma_f32_16x16x32_bf16 v[104:107], a[144:147], a[248:251], v[104:107]\n" \
"ds_read_b128 v[148:151], v129 offset:2304\n" \
"ds_read_b128 v[152:155], v129 offset:3456\n" \
"v_mfma_f32_16x16x32_bf16 v[104:107], a[148:151], a[252:255], v[104:107]\n" \
"v_cvt_pk_bf16_f32 v136, v96, v97\n" \
"v_cvt_pk_bf16_f32 v137, v98, v99\n" \
"v_mfma_f32_16x16x32_bf16 v[104:107], a[176:179], a[48:51], v[104:107]\n" \
"ds_write_b64 v128, v[136:137] offset:4608\n" \
"v_mfma_f32_16x16x32_bf16 v[104:107], a[180:183], a[52:55], v[104:107]\n" \
"v_cvt_pk_bf16_f32 v138, v100, v101\n" \
"v_cvt_pk_bf16_f32 v139, v102, v103\n" \
"v_mfma_f32_16x16x32_bf16 v[104:107], a[208:211], a[56:59], v[104:107]\n" \
"s_add_u32 s70, s88, 4096\n" \
"s_waitcnt lgkmcnt(2)\n" \
"v_mfma_f32_16x16x32_bf16 v[104:107], a[212:215], a[60:63], v[104:107]\n" \
"buffer_store_dwordx4 v[148:151], v130, s[52:55], s70 offen sc1\n" \
"ds_write_b64 v128, v[138:139] offset:4640\n" \
"v_mfma_f32_16x16x32_bf16 v[108:111], a[220:223], a[60:63], v[108:111]\n" \
"s_add_u32 s70, s88, 69632\n" \
"s_waitcnt lgkmcnt(2)\n" \
"v_mfma_f32_16x16x32_bf16 v[108:111], a[216:219], a[56:59], v[108:111]\n" \
"buffer_store_dwordx4 v[152:155], v130, s[52:55], s70 offen sc1\n" \
"v_mfma_f32_16x16x32_bf16 v[108:111], a[188:191], a[52:55], v[108:111]\n" \
"v_mfma_f32_16x16x32_bf16 v[108:111], a[184:187], a[48:51], v[108:111]\n" \
"v_cvt_pk_bf16_f32 v136, v104, v105\n" \
"v_cvt_pk_bf16_f32 v137, v106, v107\n" \
"v_mfma_f32_16x16x32_bf16 v[108:111], a[156:159], a[252:255], v[108:111]\n" \
"v_mfma_f32_16x16x32_bf16 v[108:111], a[152:155], a[248:251], v[108:111]\n" \
"ds_write_b64 v128, v[136:137] offset:4672\n" \
"v_mfma_f32_16x16x32_bf16 v[112:115], a[128:131], a[224:227], v[112:115]\n" \
"v_mfma_f32_16x16x32_bf16 v[112:115], a[132:135], a[228:231], v[112:115]\n" \
"v_mfma_f32_16x16x32_bf16 v[112:115], a[160:163], a[232:235], v[112:115]\n" \
"v_mfma_f32_16x16x32_bf16 v[112:115], a[164:167], a[236:239], v[112:115]\n" \
"v_cvt_pk_bf16_f32 v138, v108, v109\n" \
"v_cvt_pk_bf16_f32 v139, v110, v111\n" \
"v_mfma_f32_16x16x32_bf16 v[112:115], a[192:195], a[240:243], v[112:115]\n" \
"v_mfma_f32_16x16x32_bf16 v[112:115], a[196:199], a[244:247], v[112:115]\n" \
"ds_write_b64 v128, v[138:139] offset:4704\n" \
"v_mfma_f32_16x16x32_bf16 v[116:119], a[204:207], a[244:247], v[116:119]\n" \
"ds_read_b128 v[156:159], v129 offset:4608\n" \
"ds_read_b128 v[160:163], v129 offset:5760\n" \
"v_mfma_f32_16x16x32_bf16 v[116:119], a[200:203], a[240:243], v[116:119]\n" \
"v_mfma_f32_16x16x32_bf16 v[116:119], a[172:175], a[236:239], v[116:119]\n" \
"v_mfma_f32_16x16x32_bf16 v[116:119], a[168:171], a[232:235], v[116:119]\n" \
"v_cvt_pk_bf16_f32 v136, v112, v113\n" \
"v_cvt_pk_bf16_f32 v137, v114, v115\n" \
"v_mfma_f32_16x16x32_bf16 v[116:119], a[140:143], a[228:231], v[116:119]\n" \
"s_add_u32 s70, s88, 131072\n" \
"s_waitcnt lgkmcnt(1)\n" \
"v_mfma_f32_16x16x32_bf16 v[116:119], a[136:139], a[224:227], v[116:119]\n" \
"buffer_store_dwordx4 v[156:159], v130, s[52:55], s70 offen sc1\n" \
"ds_write_b64 v128, v[136:137] offset:6912\n" \
"v_mfma_f32_16x16x32_bf16 v[120:123], a[144:147], a[224:227], v[120:123]\n" \
"s_add_u32 s70, s88, 196608\n" \
"s_waitcnt lgkmcnt(1)\n" \
"v_mfma_f32_16x16x32_bf16 v[120:123], a[148:151], a[228:231], v[120:123]\n" \
"buffer_store_dwordx4 v[160:163], v130, s[52:55], s70 offen sc1\n" \
"v_mfma_f32_16x16x32_bf16 v[120:123], a[176:179], a[232:235], v[120:123]\n" \
"v_mfma_f32_16x16x32_bf16 v[120:123], a[180:183], a[236:239], v[120:123]\n" \
"v_cvt_pk_bf16_f32 v138, v116, v117\n" \
"v_cvt_pk_bf16_f32 v139, v118, v119\n" \
"v_mfma_f32_16x16x32_bf16 v[120:123], a[208:211], a[240:243], v[120:123]\n" \
"v_mfma_f32_16x16x32_bf16 v[120:123], a[212:215], a[244:247], v[120:123]\n" \
"ds_write_b64 v128, v[138:139] offset:6944\n" \
"v_mfma_f32_16x16x32_bf16 v[124:127], a[220:223], a[244:247], v[124:127]\n" \
"v_mfma_f32_16x16x32_bf16 v[124:127], a[216:219], a[240:243], v[124:127]\n" \
"v_mfma_f32_16x16x32_bf16 v[124:127], a[188:191], a[236:239], v[124:127]\n" \
"v_mfma_f32_16x16x32_bf16 v[124:127], a[184:187], a[232:235], v[124:127]\n" \
"v_cvt_pk_bf16_f32 v136, v120, v121\n" \
"v_cvt_pk_bf16_f32 v137, v122, v123\n" \
"v_mfma_f32_16x16x32_bf16 v[124:127], a[156:159], a[228:231], v[124:127]\n" \
"v_mfma_f32_16x16x32_bf16 v[124:127], a[152:155], a[224:227], v[124:127]\n" \
"ds_write_b64 v128, v[136:137] offset:6976\n" \
"s_nop 7\n" \
"v_cvt_pk_bf16_f32 v138, v124, v125\n" \
"v_cvt_pk_bf16_f32 v139, v126, v127\n" \
"ds_write_b64 v128, v[138:139] offset:7008\n" \
"ds_read_b128 v[164:167], v129 offset:6912\n" \
"ds_read_b128 v[168:171], v129 offset:8064\n" \
"s_add_u32 s70, s88, 135168\n" \
"s_waitcnt lgkmcnt(1)\n" \
"buffer_store_dwordx4 v[164:167], v130, s[52:55], s70 offen sc1\n" \
"s_add_u32 s70, s88, 200704\n" \
"s_waitcnt lgkmcnt(0)\n" \
"buffer_store_dwordx4 v[168:171], v130, s[52:55], s70 offen sc1\n" \
"s_endpgm\n" \
"Lhot_end_%=:\n" \
"Lpl1_%=:\n" \
"s_mov_b32 s72, 0\n" \
"Lpk1_%=:\n" \
"s_sleep 2\n" \
"buffer_load_dword v227, v242, s[56:59], s90 offen sc1\n" \
"s_waitcnt vmcnt(0)\n" \
"v_cmp_eq_u32 vcc, s60, v227\n" \
"s_nop 1\n" \
"s_andn2_b32 s70, 0xff, vcc_lo\n" \
"s_cbranch_scc0 Lpr1_%=\n" \
"s_add_u32 s72, s72, 1\n" \
"s_cmp_lt_u32 s72, 0x40000\n" \
"s_cbranch_scc1 Lpk1_%=\n" \
"s_branch Lpr1_%=\n" \
"Lpl2_%=:\n" \
"s_mov_b32 s72, 0\n" \
"Lpk2_%=:\n" \
"s_sleep 2\n" \
"buffer_load_dword v239, v242, s[56:59], s95 offen sc1\n" \
"s_waitcnt vmcnt(0)\n" \
"v_cmp_eq_u32 vcc, s60, v239\n" \
"s_nop 1\n" \
"s_andn2_b32 s70, s69, vcc_lo\n" \
"s_cbranch_scc0 Lpr2_%=\n" \
"s_add_u32 s72, s72, 1\n" \
"s_cmp_lt_u32 s72, 0x40000\n" \
"s_cbranch_scc1 Lpk2_%=\n" \
"s_branch Lpr2_%=\n" \
"Lpl3_%=:\n" \
"s_mov_b32 s72, 0\n" \
"Lpk3_%=:\n" \
"s_sleep 2\n" \
"buffer_load_dword v239, v242, s[56:59], s95 offen sc1\n" \
"s_waitcnt vmcnt(0)\n" \
"v_cmp_eq_u32 vcc, s60, v239\n" \
"s_nop 1\n" \
"s_andn2_b32 s70, s69, vcc_lo\n" \
"s_cbranch_scc0 Lpr3_%=\n" \
"s_add_u32 s72, s72, 1\n" \
"s_cmp_lt_u32 s72, 0x40000\n" \
"s_cbranch_scc1 Lpk3_%=\n" \
"s_branch Lpr3_%=\n" \
"Lexact_%=:\n" \
"v_mov_b32 v239, 0xff800000\n" \
"s_and_b32 s70, s61, 1\n" \
"s_lshl_b32 s70, s70, 4\n" \
"v_add_u32 v186, s70, v238\n" \
"v_accvgpr_read_b32 v160, a128\n" \
"v_accvgpr_read_b32 v161, a129\n" \
"v_accvgpr_read_b32 v162, a130\n" \
"v_accvgpr_read_b32 v163, a131\n" \
"v_accvgpr_read_b32 v164, a132\n" \
"v_accvgpr_read_b32 v165, a133\n" \
"v_accvgpr_read_b32 v166, a134\n" \
"v_accvgpr_read_b32 v167, a135\n" \
"v_accvgpr_read_b32 v168, a136\n" \
"v_accvgpr_read_b32 v169, a137\n" \
"v_accvgpr_read_b32 v170, a138\n" \
"v_accvgpr_read_b32 v171, a139\n" \
"v_accvgpr_read_b32 v172, a140\n" \
"v_accvgpr_read_b32 v173, a141\n" \
"v_accvgpr_read_b32 v174, a142\n" \
"v_accvgpr_read_b32 v175, a143\n" \
"s_add_u32 s83, s85, 0\n" \
"s_add_u32 s84, s87, 0\n" \
"s_call_b64 s[64:65], Lex1_%=\n" \
"v_accvgpr_read_b32 v160, a144\n" \
"v_accvgpr_read_b32 v161, a145\n" \
"v_accvgpr_read_b32 v162, a146\n" \
"v_accvgpr_read_b32 v163, a147\n" \
"v_accvgpr_read_b32 v164, a148\n" \
"v_accvgpr_read_b32 v165, a149\n" \
"v_accvgpr_read_b32 v166, a150\n" \
"v_accvgpr_read_b32 v167, a151\n" \
"v_accvgpr_read_b32 v168, a152\n" \
"v_accvgpr_read_b32 v169, a153\n" \
"v_accvgpr_read_b32 v170, a154\n" \
"v_accvgpr_read_b32 v171, a155\n" \
"v_accvgpr_read_b32 v172, a156\n" \
"v_accvgpr_read_b32 v173, a157\n" \
"v_accvgpr_read_b32 v174, a158\n" \
"v_accvgpr_read_b32 v175, a159\n" \
"s_add_u32 s83, s85, 2\n" \
"s_add_u32 s84, s87, 532480\n" \
"s_call_b64 s[64:65], Lex1_%=\n" \
"v_accvgpr_read_b32 v160, a160\n" \
"v_accvgpr_read_b32 v161, a161\n" \
"v_accvgpr_read_b32 v162, a162\n" \
"v_accvgpr_read_b32 v163, a163\n" \
"v_accvgpr_read_b32 v164, a164\n" \
"v_accvgpr_read_b32 v165, a165\n" \
"v_accvgpr_read_b32 v166, a166\n" \
"v_accvgpr_read_b32 v167, a167\n" \
"v_accvgpr_read_b32 v168, a168\n" \
"v_accvgpr_read_b32 v169, a169\n" \
"v_accvgpr_read_b32 v170, a170\n" \
"v_accvgpr_read_b32 v171, a171\n" \
"v_accvgpr_read_b32 v172, a172\n" \
"v_accvgpr_read_b32 v173, a173\n" \
"v_accvgpr_read_b32 v174, a174\n" \
"v_accvgpr_read_b32 v175, a175\n" \
"s_add_u32 s83, s86, 0\n" \
"s_add_u32 s84, s88, 0\n" \
"s_call_b64 s[64:65], Lex1_%=\n" \
"v_accvgpr_read_b32 v160, a176\n" \
"v_accvgpr_read_b32 v161, a177\n" \
"v_accvgpr_read_b32 v162, a178\n" \
"v_accvgpr_read_b32 v163, a179\n" \
"v_accvgpr_read_b32 v164, a180\n" \
"v_accvgpr_read_b32 v165, a181\n" \
"v_accvgpr_read_b32 v166, a182\n" \
"v_accvgpr_read_b32 v167, a183\n" \
"v_accvgpr_read_b32 v168, a184\n" \
"v_accvgpr_read_b32 v169, a185\n" \
"v_accvgpr_read_b32 v170, a186\n" \
"v_accvgpr_read_b32 v171, a187\n" \
"v_accvgpr_read_b32 v172, a188\n" \
"v_accvgpr_read_b32 v173, a189\n" \
"v_accvgpr_read_b32 v174, a190\n" \
"v_accvgpr_read_b32 v175, a191\n" \
"s_add_u32 s83, s86, 2\n" \
"s_add_u32 s84, s88, 532480\n" \
"s_call_b64 s[64:65], Lex1_%=\n" \
"s_waitcnt vmcnt(0)\n" \
"s_branch Lexr_%=\n" \
"Lex1_%=:\n" \
"v_mov_b32 v128, 0\n" \
"v_mov_b32 v129, 0\n" \
"v_mov_b32 v130, 0\n" \
"v_mov_b32 v131, 0\n" \
"v_mov_b32 v132, 0\n" \
"v_mov_b32 v133, 0\n" \
"v_mov_b32 v134, 0\n" \
"v_mov_b32 v135, 0\n" \
"v_mov_b32 v136, 0\n" \
"v_mov_b32 v137, 0\n" \
"v_mov_b32 v138, 0\n" \
"v_mov_b32 v139, 0\n" \
"v_mov_b32 v140, 0\n" \
"v_mov_b32 v141, 0\n" \
"v_mov_b32 v142, 0\n" \
"v_mov_b32 v143, 0\n" \
"v_mov_b32 v144, 0\n" \
"v_mov_b32 v145, 0\n" \
"v_mov_b32 v146, 0\n" \
"v_mov_b32 v147, 0\n" \
"v_mov_b32 v148, 0\n" \
"v_mov_b32 v149, 0\n" \
"v_mov_b32 v150, 0\n" \
"v_mov_b32 v151, 0\n" \
"v_mov_b32 v152, 0\n" \
"v_mov_b32 v153, 0\n" \
"v_mov_b32 v154, 0\n" \
"v_mov_b32 v155, 0\n" \
"v_mov_b32 v156, 0\n" \
"v_mov_b32 v157, 0\n" \
"v_mov_b32 v158, 0\n" \
"v_mov_b32 v159, 0\n" \
"v_mov_b32 v184, 0xf149f2ca\n" \
"v_mov_b32 v185, 0\n" \
"s_mov_b32 s81, 0\n" \
"s_mul_i32 s82, s62, 557824\n" \
"Lex2_%=:\n" \
"s_add_u32 s70, s82, 4096\n" \
"buffer_load_dwordx4 a[64:67], v236, s[56:59], s82 offen offset:0 sc1\n" \
"buffer_load_dwordx4 a[68:71], v236, s[56:59], s82 offen offset:2048 sc1\n" \
"buffer_load_dwordx4 a[72:75], v236, s[56:59], s70 offen offset:0 sc1\n" \
"buffer_load_dwordx4 a[76:79], v236, s[56:59], s70 offen offset:2048 sc1\n" \
"buffer_load_dwordx4 a[80:83], v236, s[56:59], s82 offen offset:256 sc1\n" \
"buffer_load_dwordx4 a[84:87], v236, s[56:59], s82 offen offset:2304 sc1\n" \
"buffer_load_dwordx4 a[88:91], v236, s[56:59], s70 offen offset:256 sc1\n" \
"buffer_load_dwordx4 a[92:95], v236, s[56:59], s70 offen offset:2304 sc1\n" \
"s_add_u32 s70, s82, 17850368\n" \
"s_add_u32 s71, s70, 4096\n" \
"buffer_load_dwordx4 a[96:99], v234, s[56:59], s70 offen offset:0 sc1\n" \
"buffer_load_dwordx4 a[100:103], v234, s[56:59], s70 offen offset:1024 sc1\n" \
"buffer_load_dwordx4 a[104:107], v234, s[56:59], s70 offen offset:2048 sc1\n" \
"buffer_load_dwordx4 a[108:111], v234, s[56:59], s70 offen offset:3072 sc1\n" \
"buffer_load_dwordx4 a[112:115], v234, s[56:59], s71 offen offset:0 sc1\n" \
"buffer_load_dwordx4 a[116:119], v234, s[56:59], s71 offen offset:1024 sc1\n" \
"buffer_load_dwordx4 a[120:123], v234, s[56:59], s71 offen offset:2048 sc1\n" \
"buffer_load_dwordx4 a[124:127], v234, s[56:59], s71 offen offset:3072 sc1\n" \
"s_waitcnt vmcnt(0)\n" \
"v_mfma_f32_16x16x32_bf16 v[176:179], a[64:67], v[160:163], 0\n" \
"v_mfma_f32_16x16x32_bf16 v[176:179], a[68:71], v[164:167], v[176:179]\n" \
"v_mfma_f32_16x16x32_bf16 v[176:179], a[72:75], v[168:171], v[176:179]\n" \
"v_mfma_f32_16x16x32_bf16 v[176:179], a[76:79], v[172:175], v[176:179]\n" \
"v_mfma_f32_16x16x32_bf16 v[180:183], a[80:83], v[160:163], 0\n" \
"v_mfma_f32_16x16x32_bf16 v[180:183], a[84:87], v[164:167], v[180:183]\n" \
"v_mfma_f32_16x16x32_bf16 v[180:183], a[88:91], v[168:171], v[180:183]\n" \
"v_mfma_f32_16x16x32_bf16 v[180:183], a[92:95], v[172:175], v[180:183]\n" \
"s_nop 7\n" \
"s_nop 1\n" \
"s_cmp_eq_u32 s81, s83\n" \
"s_cbranch_scc0 Lex3_%=\n" \
"v_cmp_gt_i32 vcc, 0, v186\n" \
"s_nop 1\n" \
"v_cndmask_b32 v176, v176, v239, vcc\n" \
"v_cmp_gt_i32 vcc, 1, v186\n" \
"s_nop 1\n" \
"v_cndmask_b32 v177, v177, v239, vcc\n" \
"v_cmp_gt_i32 vcc, 2, v186\n" \
"s_nop 1\n" \
"v_cndmask_b32 v178, v178, v239, vcc\n" \
"v_cmp_gt_i32 vcc, 3, v186\n" \
"s_nop 1\n" \
"v_cndmask_b32 v179, v179, v239, vcc\n" \
"v_cmp_gt_i32 vcc, 16, v186\n" \
"s_nop 1\n" \
"v_cndmask_b32 v180, v180, v239, vcc\n" \
"v_cmp_gt_i32 vcc, 17, v186\n" \
"s_nop 1\n" \
"v_cndmask_b32 v181, v181, v239, vcc\n" \
"v_cmp_gt_i32 vcc, 18, v186\n" \
"s_nop 1\n" \
"v_cndmask_b32 v182, v182, v239, vcc\n" \
"v_cmp_gt_i32 vcc, 19, v186\n" \
"s_nop 1\n" \
"v_cndmask_b32 v183, v183, v239, vcc\n" \
"Lex3_%=:\n" \
"v_max3_f32 v187, v176, v177, v178\n" \
"v_max3_f32 v187, v187, v179, v180\n" \
"v_max3_f32 v187, v187, v181, v182\n" \
"v_max3_f32 v187, v187, v183, v184\n" \
"v_mov_b32 v188, v187\n" \
"s_nop 1\n" \
"v_permlane16_swap_b32 v188, v187\n" \
"v_max_f32 v187, v187, v188\n" \
"v_mov_b32 v188, v187\n" \
"s_nop 1\n" \
"v_permlane32_swap_b32 v188, v187\n" \
"v_max_f32 v187, v187, v188\n" \
"v_sub_f32 v189, v184, v187\n" \
"v_exp_f32 v189, v189\n" \
"v_mov_b32 v184, v187\n" \
"v_sub_f32 v176, v176, v187\n" \
"v_sub_f32 v177, v177, v187\n" \
"v_sub_f32 v178, v178, v187\n" \
"v_sub_f32 v179, v179, v187\n" \
"v_sub_f32 v180, v180, v187\n" \
"v_sub_f32 v181, v181, v187\n" \
"v_sub_f32 v182, v182, v187\n" \
"v_sub_f32 v183, v183, v187\n" \
"v_exp_f32 v176, v176\n" \
"v_exp_f32 v177, v177\n" \
"v_exp_f32 v178, v178\n" \
"v_exp_f32 v179, v179\n" \
"v_exp_f32 v180, v180\n" \
"v_exp_f32 v181, v181\n" \
"v_exp_f32 v182, v182\n" \
"v_exp_f32 v183, v183\n" \
"v_mul_f32 v185, v185, v189\n" \
"v_add_f32 v185, v185, v176\n" \
"v_add_f32 v185, v185, v177\n" \
"v_add_f32 v185, v185, v178\n" \
"v_add_f32 v185, v185, v179\n" \
"v_add_f32 v185, v185, v180\n" \
"v_add_f32 v185, v185, v181\n" \
"v_add_f32 v185, v185, v182\n" \
"v_add_f32 v185, v185, v183\n" \
"v_cvt_pk_bf16_f32 v112, v176, v177\n" \
"v_cvt_pk_bf16_f32 v113, v178, v179\n" \
"v_cvt_pk_bf16_f32 v114, v180, v181\n" \
"v_cvt_pk_bf16_f32 v115, v182, v183\n" \
"v_mul_f32 v128, v128, v189\n" \
"v_mul_f32 v129, v129, v189\n" \
"v_mul_f32 v130, v130, v189\n" \
"v_mul_f32 v131, v131, v189\n" \
"v_mul_f32 v132, v132, v189\n" \
"v_mul_f32 v133, v133, v189\n" \
"v_mul_f32 v134, v134, v189\n" \
"v_mul_f32 v135, v135, v189\n" \
"v_mul_f32 v136, v136, v189\n" \
"v_mul_f32 v137, v137, v189\n" \
"v_mul_f32 v138, v138, v189\n" \
"v_mul_f32 v139, v139, v189\n" \
"v_mul_f32 v140, v140, v189\n" \
"v_mul_f32 v141, v141, v189\n" \
"v_mul_f32 v142, v142, v189\n" \
"v_mul_f32 v143, v143, v189\n" \
"v_mul_f32 v144, v144, v189\n" \
"v_mul_f32 v145, v145, v189\n" \
"v_mul_f32 v146, v146, v189\n" \
"v_mul_f32 v147, v147, v189\n" \
"v_mul_f32 v148, v148, v189\n" \
"v_mul_f32 v149, v149, v189\n" \
"v_mul_f32 v150, v150, v189\n" \
"v_mul_f32 v151, v151, v189\n" \
"v_mul_f32 v152, v152, v189\n" \
"v_mul_f32 v153, v153, v189\n" \
"v_mul_f32 v154, v154, v189\n" \
"v_mul_f32 v155, v155, v189\n" \
"v_mul_f32 v156, v156, v189\n" \
"v_mul_f32 v157, v157, v189\n" \
"v_mul_f32 v158, v158, v189\n" \
"v_mul_f32 v159, v159, v189\n" \
"s_nop 1\n" \
"v_mfma_f32_16x16x32_bf16 v[128:131], a[96:99], v[112:115], v[128:131]\n" \
"v_mfma_f32_16x16x32_bf16 v[132:135], a[100:103], v[112:115], v[132:135]\n" \
"v_mfma_f32_16x16x32_bf16 v[136:139], a[104:107], v[112:115], v[136:139]\n" \
"v_mfma_f32_16x16x32_bf16 v[140:143], a[108:111], v[112:115], v[140:143]\n" \
"v_mfma_f32_16x16x32_bf16 v[144:147], a[112:115], v[112:115], v[144:147]\n" \
"v_mfma_f32_16x16x32_bf16 v[148:151], a[116:119], v[112:115], v[148:151]\n" \
"v_mfma_f32_16x16x32_bf16 v[152:155], a[120:123], v[112:115], v[152:155]\n" \
"v_mfma_f32_16x16x32_bf16 v[156:159], a[124:127], v[112:115], v[156:159]\n" \
"s_nop 7\n" \
"s_nop 1\n" \
"s_and_b32 s70, s81, 1\n" \
"s_mul_i32 s70, s70, 1024\n" \
"s_add_u32 s82, s82, s70\n" \
"s_add_u32 s82, s82, 8192\n" \
"s_add_u32 s81, s81, 1\n" \
"s_cmp_le_u32 s81, s83\n" \
"s_cbranch_scc1 Lex2_%=\n" \
"v_mov_b32 v187, v185\n" \
"s_nop 1\n" \
"v_permlane16_swap_b32 v187, v185\n" \
"v_add_f32 v185, v185, v187\n" \
"v_mov_b32 v187, v185\n" \
"s_nop 1\n" \
"v_permlane32_swap_b32 v187, v185\n" \
"v_add_f32 v185, v185, v187\n" \
"v_rcp_f32 v185, v185\n" \
"s_nop 0\n" \
"v_mul_f32 v128, v128, v185\n" \
"v_mul_f32 v129, v129, v185\n" \
"v_mul_f32 v130, v130, v185\n" \
"v_mul_f32 v131, v131, v185\n" \
"v_mul_f32 v132, v132, v185\n" \
"v_mul_f32 v133, v133, v185\n" \
"v_mul_f32 v134, v134, v185\n" \
"v_mul_f32 v135, v135, v185\n" \
"v_mul_f32 v136, v136, v185\n" \
"v_mul_f32 v137, v137, v185\n" \
"v_mul_f32 v138, v138, v185\n" \
"v_mul_f32 v139, v139, v185\n" \
"v_mul_f32 v140, v140, v185\n" \
"v_mul_f32 v141, v141, v185\n" \
"v_mul_f32 v142, v142, v185\n" \
"v_mul_f32 v143, v143, v185\n" \
"v_mul_f32 v144, v144, v185\n" \
"v_mul_f32 v145, v145, v185\n" \
"v_mul_f32 v146, v146, v185\n" \
"v_mul_f32 v147, v147, v185\n" \
"v_mul_f32 v148, v148, v185\n" \
"v_mul_f32 v149, v149, v185\n" \
"v_mul_f32 v150, v150, v185\n" \
"v_mul_f32 v151, v151, v185\n" \
"v_mul_f32 v152, v152, v185\n" \
"v_mul_f32 v153, v153, v185\n" \
"v_mul_f32 v154, v154, v185\n" \
"v_mul_f32 v155, v155, v185\n" \
"v_mul_f32 v156, v156, v185\n" \
"v_mul_f32 v157, v157, v185\n" \
"v_mul_f32 v158, v158, v185\n" \
"v_mul_f32 v159, v159, v185\n" \
"s_add_u32 s70, s84, 4096\n" \
"v_cvt_pk_bf16_f32 v112, v128, v129\n" \
"v_cvt_pk_bf16_f32 v113, v130, v131\n" \
"v_cvt_pk_bf16_f32 v114, v132, v133\n" \
"v_cvt_pk_bf16_f32 v115, v134, v135\n" \
"buffer_store_dwordx4 v[112:115], v236, s[56:59], s84 offen offset:0 sc1\n" \
"v_cvt_pk_bf16_f32 v116, v136, v137\n" \
"v_cvt_pk_bf16_f32 v117, v138, v139\n" \
"v_cvt_pk_bf16_f32 v118, v140, v141\n" \
"v_cvt_pk_bf16_f32 v119, v142, v143\n" \
"buffer_store_dwordx4 v[116:119], v236, s[56:59], s84 offen offset:2048 sc1\n" \
"v_cvt_pk_bf16_f32 v120, v144, v145\n" \
"v_cvt_pk_bf16_f32 v121, v146, v147\n" \
"v_cvt_pk_bf16_f32 v122, v148, v149\n" \
"v_cvt_pk_bf16_f32 v123, v150, v151\n" \
"buffer_store_dwordx4 v[120:123], v236, s[56:59], s70 offen offset:0 sc1\n" \
"v_cvt_pk_bf16_f32 v124, v152, v153\n" \
"v_cvt_pk_bf16_f32 v125, v154, v155\n" \
"v_cvt_pk_bf16_f32 v126, v156, v157\n" \
"v_cvt_pk_bf16_f32 v127, v158, v159\n" \
"buffer_store_dwordx4 v[124:127], v236, s[56:59], s70 offen offset:2048 sc1\n" \
"s_waitcnt vmcnt(0)\n" \
"s_setpc_b64 s[64:65]\n" \
"Lpl4_%=:\n" \
"s_mov_b32 s72, 0\n" \
"Lpk4_%=:\n" \
"s_sleep 2\n" \
"buffer_load_dwordx2 v[226:227], v228, s[56:59], s13 offen sc1\n" \
"s_waitcnt vmcnt(0)\n" \
"v_cmp_eq_u32 vcc, s60, v226\n" \
"s_nop 1\n" \
"s_andn2_b32 s70, 0xff, vcc_lo\n" \
"s_cbranch_scc0 Lpr4_%=\n" \
"s_add_u32 s72, s72, 1\n" \
"s_cmp_lt_u32 s72, 0x40000\n" \
"s_cbranch_scc1 Lpk4_%=\n" \
"s_branch Lpr4_%=\n" \
"Lpl5_%=:\n" \
"s_mov_b32 s72, 0\n" \
"Lpk5_%=:\n" \
"s_sleep 2\n" \
"buffer_load_dword v220, v221, s[56:59], s66 offen sc1\n" \
"s_waitcnt vmcnt(0)\n" \
"v_cmp_eq_u32 vcc, s60, v220\n" \
"s_nop 1\n" \
"s_andn2_b32 s70, s65, vcc_lo\n" \
"s_cbranch_scc0 Lpr5_%=\n" \
"s_add_u32 s72, s72, 1\n" \
"s_cmp_lt_u32 s72, 0x40000\n" \
"s_cbranch_scc1 Lpk5_%=\n" \
"s_branch Lpr5_%=\n" \
"Lpl6_%=:\n" \
"s_mov_b32 s72, 0\n" \
"Lpk6_%=:\n" \
"s_sleep 2\n" \
"buffer_load_dword v220, v221, s[56:59], s67 offen sc1\n" \
"s_waitcnt vmcnt(0)\n" \
"v_cmp_eq_u32 vcc, s60, v220\n" \
"s_nop 1\n" \
"s_andn2_b32 s70, s74, vcc_lo\n" \
"s_cbranch_scc0 Lpr6_%=\n" \
"s_add_u32 s72, s72, 1\n" \
"s_cmp_lt_u32 s72, 0x40000\n" \
"s_cbranch_scc1 Lpk6_%=\n" \
"s_branch Lpr6_%=\n" \
"Lpl7_%=:\n" \
"s_mov_b32 s72, 0\n" \
"Lpk7_%=:\n" \
"s_sleep 2\n" \
"buffer_load_dword v220, v241, s[56:59], s64 offen sc1\n" \
"s_waitcnt vmcnt(0)\n" \
"v_cmp_eq_u32 vcc, s60, v220\n" \
"s_nop 1\n" \
"s_andn2_b32 s70, s69, vcc_lo\n" \
"s_cbranch_scc0 Lpr7_%=\n" \
"s_add_u32 s72, s72, 1\n" \
"s_cmp_lt_u32 s72, 0x40000\n" \
"s_cbranch_scc1 Lpk7_%=\n" \
"s_branch Lpr7_%=\n" \
"Lcode_end_%=:\n" \
".p2align 12\n" \
""
#define MK_ASM_CLOBBERS "v1", "v2", "v3", "v4", "v5", "v6", "v7", "v8", "v9", "v10", "v11", "v12", "v13", "v14", "v15", "v16", "v17", "v18", "v19", "v20", "v21", "v22", "v23", "v24", "v25", "v26", "v27", "v28", "v29", "v30", "v31", "v32", "v33", "v34", "v35", "v36", "v37", "v38", "v39", "v40", "v41", "v42", "v43", "v44", "v45", "v46", "v47", "v48", "v49", "v50", "v51", "v52", "v53", "v54", "v55", "v56", "v57", "v58", "v59", "v60", "v61", "v62", "v63", "v64", "v65", "v66", "v67", "v68", "v69", "v70", "v71", "v72", "v73", "v74", "v75", "v76", "v77", "v78", "v79", "v80", "v81", "v82", "v83", "v84", "v85", "v86", "v87", "v88", "v89", "v90", "v91", "v92", "v93", "v94", "v95", "v96", "v97", "v98", "v99", "v100", "v101", "v102", "v103", "v104", "v105", "v106", "v107", "v108", "v109", "v110", "v111", "v112", "v113", "v114", "v115", "v116", "v117", "v118", "v119", "v120", "v121", "v122", "v123", "v124", "v125", "v126", "v127", "v128", "v129", "v130", "v131", "v132", "v133", "v134", "v135", "v136", "v137", "v138", "v139", "v140", "v141", "v142", "v143", "v144", "v145", "v146", "v147", "v148", "v149", "v150", "v151", "v152", "v153", "v154", "v155", "v156", "v157", "v158", "v159", "v160", "v161", "v162", "v163", "v164", "v165", "v166", "v167", "v168", "v169", "v170", "v171", "v172", "v173", "v174", "v175", "v176", "v177", "v178", "v179", "v180", "v181", "v182", "v183", "v184", "v185", "v186", "v187", "v188", "v189", "v190", "v191", "v192", "v193", "v194", "v195", "v196", "v197", "v198", "v199", "v200", "v201", "v202", "v203", "v204", "v205", "v206", "v207", "v208", "v209", "v210", "v211", "v212", "v213", "v214", "v215", "v216", "v217", "v218", "v219", "v220", "v221", "v222", "v223", "v224", "v225", "v226", "v227", "v228", "v229", "v230", "v231", "v232", "v233", "v234", "v235", "v236", "v237", "v238", "v239", "v240", "v241", "v242", "v243", "v244", "v245", "v246", "v247", "v248", "v249", "v250", "v251", "v252", "v253", "v254", "v255", "a0", "a1", "a2", "a3", "a4", "a5", "a6", "a7", "a8", "a9", "a10", "a11", "a12", "a13", "a14", "a15", "a16", "a17", "a18", "a19", "a20", "a21", "a22", "a23", "a24", "a25", "a26", "a27", "a28", "a29", "a30", "a31", "a32", "a33", "a34", "a35", "a36", "a37", "a38", "a39", "a40", "a41", "a42", "a43", "a44", "a45", "a46", "a47", "a48", "a49", "a50", "a51", "a52", "a53", "a54", "a55", "a56", "a57", "a58", "a59", "a60", "a61", "a62", "a63", "a64", "a65", "a66", "a67", "a68", "a69", "a70", "a71", "a72", "a73", "a74", "a75", "a76", "a77", "a78", "a79", "a80", "a81", "a82", "a83", "a84", "a85", "a86", "a87", "a88", "a89", "a90", "a91", "a92", "a93", "a94", "a95", "a96", "a97", "a98", "a99", "a100", "a101", "a102", "a103", "a104", "a105", "a106", "a107", "a108", "a109", "a110", "a111", "a112", "a113", "a114", "a115", "a116", "a117", "a118", "a119", "a120", "a121", "a122", "a123", "a124", "a125", "a126", "a127", "a128", "a129", "a130", "a131", "a132", "a133", "a134", "a135", "a136", "a137", "a138", "a139", "a140", "a141", "a142", "a143", "a144", "a145", "a146", "a147", "a148", "a149", "a150", "a151", "a152", "a153", "a154", "a155", "a156", "a157", "a158", "a159", "a160", "a161", "a162", "a163", "a164", "a165", "a166", "a167", "a168", "a169", "a170", "a171", "a172", "a173", "a174", "a175", "a176", "a177", "a178", "a179", "a180", "a181", "a182", "a183", "a184", "a185", "a186", "a187", "a188", "a189", "a190", "a191", "a192", "a193", "a194", "a195", "a196", "a197", "a198", "a199", "a200", "a201", "a202", "a203", "a204", "a205", "a206", "a207", "a208", "a209", "a210", "a211", "a212", "a213", "a214", "a215", "a216", "a217", "a218", "a219", "a220", "a221", "a222", "a223", "a224", "a225", "a226", "a227", "a228", "a229", "a230", "a231", "a232", "a233", "a234", "a235", "a236", "a237", "a238", "a239", "a240", "a241", "a242", "a243", "a244", "a245", "a246", "a247", "a248", "a249", "a250", "a251", "a252", "a253", "a254", "a255", "s8", "s9", "s10", "s11", "s12", "s13", "s14", "s15", "s16", "s17", "s18", "s19", "s20", "s21", "s22", "s23", "s24", "s25", "s26", "s27", "s28", "s29", "s30", "s31", "s36", "s37", "s38", "s39", "s40", "s41", "s42", "s43", "s44", "s45", "s46", "s47", "s48", "s49", "s50", "s51", "s52", "s53", "s54", "s55", "s56", "s57", "s58", "s59", "s60", "s61", "s62", "s63", "s64", "s65", "s66", "s67", "s68", "s69", "s70", "s71", "s72", "s73", "s74", "s75", "s76", "s77", "s78", "s79", "s80", "s81", "s82", "s83", "s84", "s85", "s86", "s87", "s88", "s89", "s90", "s91", "s92", "s93", "s94", "s95", "vcc", "scc", "memory"
namespace mk {
constexpr int LDS_BYTES = 163840;
struct Args { const void* in[9]; void* out; void* ws; };
__global__ __launch_bounds__(256, 1) void mega_k(const Args a) {
    extern __shared__ __attribute__((aligned(16))) unsigned char shm[];
    const unsigned ldsb = (unsigned)(size_t)(LAS unsigned char*)shm;
    asm volatile(MK_ASM_BODY
                 :
                 : "s"(__builtin_amdgcn_kernarg_segment_ptr()), "s"(blockIdx.x), "v"(threadIdx.x), "s"(ldsb)
                 : MK_ASM_CLOBBERS);
}
}
extern "C" void kernel_launch(void* const* d_in, const int* in_sizes, int n_in, void* d_out, int out_size, void* d_ws, size_t ws_size, hipStream_t stream) {
    (void)hipFuncSetAttribute((const void*)mk::mega_k, hipFuncAttributeMaxDynamicSharedMemorySize, mk::LDS_BYTES);
    mk::Args a;
    for (int i = 0; i < 9; ++i) a.in[i] = d_in[i];
    a.out = d_out; a.ws = (char*)d_ws + ((ws_size - WS_END) & ~(size_t)0x1fffff);
    mk::mega_k<<<256, 256, mk::LDS_BYTES, stream>>>(a);
}
```
